```python
import torch

def _softmax(x, axis=-1):
    return torch.softmax(x, dim=axis)
def _gelu(x):
    return torch.nn.functional.gelu(x, approximate="tanh")
def _silu(x):
    return torch.nn.functional.silu(x)
def _sigmoid(x):
    return torch.sigmoid(x)
def _layernorm(x, axis=-1, eps=1e-5):
    m = x.mean(dim=axis, keepdim=True)
    v = x.var(dim=axis, keepdim=True, unbiased=False)
    return (x - m) / torch.sqrt(v + eps)
def _scatter_add(tgt, idx, src, axis=0):
    return tgt.clone().index_add_(axis, idx, src)
def _segment_sum(src, seg, n):
    out = torch.zeros((n,) + src.shape[1:], dtype=src.dtype)
    return out.index_add_(0, seg, src)
def _pad(x, pw):
    flat = tuple(v for pair in reversed(pw) for v in pair)
    return torch.nn.functional.pad(x, flat)

kernel_name = 'sequential_preLN__compressed_kv_cla__moe_arch_mod__prompt'

def setup_inputs():

    _w = (lambda a: (lambda t: t.to(torch.bfloat16) if t.is_floating_point() else t)(torch.as_tensor(a)))
    def _A_setup():
        B, H, Sq, Sk, D, M = 1, 32, 2048, 2048, 128, 1024
        Dc = 4 * D
        return {
            "x": _w((lambda *s: torch.randn(*s, dtype=torch.bfloat16) * 0.1)(B, Sq, M)),
            "c_kv": _w((lambda *s: torch.randn(*s, dtype=torch.bfloat16) * 1.6)(B, Sk, Dc)),
            "w_q": _w((lambda *s: torch.randn(*s, dtype=torch.bfloat16) * 0.8)(M, H, D) * (M ** -0.5)),
            "w_uk": _w((lambda *s: torch.randn(*s, dtype=torch.bfloat16) * 0.8)(Dc, H, D) * (Dc ** -0.5)),
            "w_uv": _w((lambda *s: torch.randn(*s, dtype=torch.bfloat16) * 1.6)(Dc, H, D) * (Dc ** -0.5)),
            "w_o": _w((lambda *s: torch.randn(*s, dtype=torch.bfloat16) * 1.6)(H, D, M) * ((H * D) ** -0.5)),
        }
    _A_in = _A_setup()

    def _B_setup():
        B, S, D, F, C = 1, 2048, 1024, 3584, 2
        return {
            "x": _w((lambda *s: torch.randn(*s, dtype=torch.bfloat16) * 0.1)(B, S, D)),
            "w_r": _w((lambda *s: torch.randn(*s, dtype=torch.bfloat16) * 0.2)(D)),
            "w_gate": _w((lambda *s: torch.randn(*s, dtype=torch.bfloat16) * 0.2)(D, F)),
            "w_up": _w((lambda *s: torch.randn(*s, dtype=torch.bfloat16) * 0.2)(D, F)),
            "w_down": _w((lambda *s: torch.randn(*s, dtype=torch.bfloat16) * 0.2)(F, D)),
        }
    _B_in = _B_setup()

    return {
        "A_x": _A_in["x"],
        "A_c_kv": _A_in["c_kv"],
        "A_w_q": _A_in["w_q"],
        "A_w_uk": _A_in["w_uk"],
        "A_w_uv": _A_in["w_uv"],
        "A_w_o": _A_in["w_o"],
        "B_x": _B_in["x"],
        "B_w_r": _B_in["w_r"],
        "B_w_gate": _B_in["w_gate"],
        "B_w_up": _B_in["w_up"],
        "B_w_down": _B_in["w_down"],
    }

def reference(A_x, A_c_kv, A_w_q, A_w_uk, A_w_uv, A_w_o, B_x, B_w_r, B_w_gate, B_w_up, B_w_down):
    def _A_ref(x, c_kv, w_q, w_uk, w_uv, w_o):
        B, Sq, M = x.shape[0], x.shape[1], x.shape[2]
        Sk = c_kv.shape[1]
        H, D = w_q.shape[1], w_q.shape[2]
        q = torch.einsum("bqm,mhd->bhqd", x, w_q)
        k = torch.einsum("bkc,chd->bhkd", c_kv, w_uk)
        v = torch.einsum("bkc,chd->bhkd", c_kv, w_uv)
        scores = torch.einsum("bhqd,bhkd->bhqk", q, k) * (D ** -0.5)
        off = Sk - Sq
        qi = torch.arange(Sq)[:, None]
        kj = torch.arange(Sk)[None, :]
        cmask = kj <= (qi + off)
        scores = torch.where(cmask[None, None], scores, -1e9)
        p = _softmax(scores, axis=-1)
        ctx = torch.einsum("bhqk,bhkd->bhqd", p, v)
        return torch.einsum("bhqd,hdm->bqm", ctx, w_o)

    def _B_ref(x, w_r, w_gate, w_up, w_down):
        B, S, D = x.shape
        C = 2
        r = x @ w_r
        idx = torch.argsort(-r, axis=-1)[:, :C]
        idx_e = (lambda x, n, axis: torch.repeat_interleave(x, n, dim=axis))(idx.reshape(B, C, 1), D, axis=2)
        x_sel = (lambda x, i, axis: torch.take_along_dim(x, i, dim=axis))(x, idx_e, axis=1)
        g = _silu(x_sel @ w_gate)
        u = x_sel @ w_up
        y = (g * u) @ w_down
        r_sel = _sigmoid((lambda x, i, axis: torch.take_along_dim(x, i, dim=axis))(r, idx, axis=1))
        y = y * r_sel.reshape(B, C, 1)
        b_off = (torch.arange(B) * S).reshape(B, 1)
        flat_idx = (idx + b_off).reshape(B * C)
        zero = (x * 0.0).reshape(B * S, D)
        upd = _scatter_add(zero, flat_idx, y.reshape(B * C, D), axis=0)
        return x + upd.reshape(B, S, D)

    x = A_x
    h = x + _A_ref(_layernorm(x, axis=-1), A_c_kv, A_w_q, A_w_uk, A_w_uv, A_w_o)
    h2 = h + _B_ref(_layernorm(h, axis=-1), B_w_r, B_w_gate, B_w_up, B_w_down)
    return h2
```

```cpp
#include <hip/hip_runtime.h>

extern "C" __global__ __launch_bounds__(256, 1) void fused_kernel(const char* __restrict__ x, const char* __restrict__ ckv, const char* __restrict__ wq,
                                                       const char* __restrict__ wuk, const char* __restrict__ wuv, char* ws,
                                                       const char* __restrict__ pwo, const char* __restrict__ pwg, const char* __restrict__ pwu, const char* __restrict__ pwd,
                                                       const char* __restrict__ pwr, char* pout) {
    unsigned tid = threadIdx.x, bid = blockIdx.x;
#if defined(__HIP_DEVICE_COMPILE__)
    asm volatile(
"s_load_dwordx2 s[16:17],s[0:1],8\n"
"s_load_dword s3,s[0:1],72\n"
"s_getpc_b64 s[8:9]\n"
".Lfk_pc:\n"
"s_lshr_b32 s82,s2,3\n"
"s_lshl_b32 s82,s82,12\n"
"v_lshlrev_b32 v1,4,v0\n"
"s_mov_b32 s10,.Lfk_cold-.Lfk_pc\n"
"s_mov_b32 s11,0x20000\n"
"buffer_load_dword v115,v1,s[8:11],s82 offen\n"
"v_readfirstlane_b32 s52,v0\n"
"s_load_dwordx2 s[32:33],s[0:1],0\n"
"s_load_dwordx2 s[24:25],s[0:1],40\n"
"s_load_dwordx2 s[44:45],s[0:1],8\n"
"s_load_dwordx2 s[28:29],s[0:1],16\n"
"s_load_dwordx2 s[40:41],s[0:1],16\n"
"s_load_dwordx2 s[4:5],s[0:1],48\n"
"s_and_b32 s82,s2,8\n"
"s_add_u32 s82,s82,24\n"
"s_load_dwordx2 s[20:21],s[0:1],s82\n"
"s_lshr_b32 s52,s52,6\n"
"s_lshr_b32 s83,s2,7\n"
"s_lshl_b32 s83,s83,3\n"
"s_add_u32 s83,s83,24\n"
"s_load_dwordx2 s[36:37],s[0:1],s83\n"
"s_mov_b32 s100,s2\n"
"s_lshl_b32 s92,s2,2\n"
"s_add_u32 s92,s92,s52\n"
"v_and_b32 v0,63,v0\n"
"v_lshlrev_b32 v112,4,v0\n"
"s_and_b32 s88,s2,7\n"
"s_lshr_b32 s89,s2,3\n"
"s_lshl_b32 s93,s89,2\n"
"s_add_u32 s93,s93,s52\n"
"v_lshrrev_b32 v2,3,v0\n"
"v_xor_b32 v3,v2,v0\n"
"v_and_b32 v3,7,v3\n"
"v_lshlrev_b32 v3,4,v3\n"
"s_lshl_b32 s82,s52,9\n"
"v_add_u32 v2,s82,v2\n"
"v_lshl_add_u32 v106,v2,10,v3\n"
"v_add_u32 v107,7168,v106\n"
"v_add_u32 v108,14336,v106\n"
"v_add_u32 v109,21504,v106\n"
"v_lshrrev_b32 v4,4,v0\n"
"v_and_b32 v5,15,v0\n"
"v_lshlrev_b32 v6,1,v4\n"
"v_xor_b32 v5,v5,v6\n"
"v_lshlrev_b32 v5,4,v5\n"
"v_xor_b32 v6,0x80,v5\n"
"s_and_b32 s82,s52,1\n"
"s_lshl_b32 s82,s82,17\n"
"s_lshr_b32 s83,s52,1\n"
"s_lshl_b32 s83,s83,8\n"
"s_add_u32 s82,s82,s83\n"
"v_lshl_add_u32 v7,v4,13,s82\n"
"v_add_u32 v110,v7,v5\n"
"v_add_u32 v111,31744,v110\n"
"v_add_u32 v113,v7,v6\n"
"v_add_u32 v113,63488,v113\n"
"v_add_u32 v114,31744,v113\n"
"s_lshl_b32 s78,s52,12\n"
"s_lshl_b32 s79,s52,13\n"
"s_lshl_b32 s85,s52,11\n"
"s_lshr_b32 s82,s2,5\n"
"s_lshl_b32 s82,s82,15\n"
"s_lshl_b32 s83,s88,10\n"
"s_bfe_u32 s97,s2,0x10004\n"
"s_lshl_b32 s97,s97,9\n"
"s_add_u32 s83,s83,s97\n"
"v_lshrrev_b32 v60,3,v0\n"
"v_lshlrev_b32 v60,19,v60\n"
"v_and_b32 v61,7,v0\n"
"v_lshl_add_u32 v60,v61,7,v60\n"
"s_and_b32 s12,s93,63\n"
"s_lshl3_add_u32 s12,s12,s88\n"
"s_lshl_b32 s12,s12,10\n"
"s_add_u32 s38,s12,1049600\n"
"s_mov_b32 s39,0x20000\n"
"s_and_b32 s14,s52,1\n"
"s_mul_i32 s13,s14,507904\n"
"v_subrev_u32 v62,s13,v106\n"
"v_subrev_u32 v63,s13,v107\n"
"s_mul_i32 s14,s14,6144\n"
"s_sub_u32 s13,s79,s14\n"
"s_waitcnt lgkmcnt(0)\n"
"s_add_u32 s16,s16,s82\n"
"s_addc_u32 s17,s17,0\n"
"s_sub_u32 s18,2097152,s82\n"
"s_mov_b32 s19,0x20000\n"
"s_add_u32 s20,s20,s83\n"
"s_addc_u32 s21,s21,0\n"
"s_sub_u32 s22,4194304,s83\n"
"s_mov_b32 s23,0x20000\n"
"s_mov_b32 s77,0\n"
"s_add_u32 m0,s78,147456\n"
"s_nop 0\n"
"buffer_load_dwordx4 v110,s[20:23],s77 offen offset:0 lds\n"
"buffer_load_dwordx4 v111,s[20:23],s77 offen offset:1024 lds\n"
"buffer_load_dwordx4 v113,s[20:23],s77 offen offset:2048 lds\n"
"buffer_load_dwordx4 v114,s[20:23],s77 offen offset:3072 lds\n"
"s_mov_b32 s76,786432\n"
"s_add_u32 m0,s13,12288\n"
"s_nop 0\n"
"buffer_load_dwordx4 v62,s[16:19],s76 offen offset:0 lds\n"
"buffer_load_dwordx4 v63,s[16:19],s76 offen offset:1024 lds\n"
"s_lshl_b32 s87,s92,11\n"
"s_add_u32 s44,s44,s87\n"
"s_addc_u32 s45,s45,0\n"
"s_mov_b32 s46,2048\n"
"s_mov_b32 s47,0x20000\n"
"v_lshlrev_b32 v1,5,v0\n"
"buffer_load_dword v115,v1,s[44:47],0 offen\n"
"s_mov_b32 s77,262144\n"
"s_add_u32 m0,s78,131072\n"
"s_nop 0\n"
"buffer_load_dwordx4 v110,s[20:23],s77 offen offset:0 lds\n"
"buffer_load_dwordx4 v111,s[20:23],s77 offen offset:1024 lds\n"
"buffer_load_dwordx4 v113,s[20:23],s77 offen offset:2048 lds\n"
"buffer_load_dwordx4 v114,s[20:23],s77 offen offset:3072 lds\n"
"s_mov_b32 s76,0\n"
"s_add_u32 m0,s13,0\n"
"s_nop 0\n"
"buffer_load_dwordx4 v62,s[16:19],s76 offen offset:0 lds\n"
"buffer_load_dwordx4 v63,s[16:19],s76 offen offset:1024 lds\n"
"buffer_load_dword v115,v60,s[36:39],s12 offen\n"
"s_mov_b32 s76,262144\n"
"s_add_u32 m0,s13,4096\n"
"s_nop 0\n"
"buffer_load_dwordx4 v62,s[16:19],s76 offen offset:0 lds\n"
"buffer_load_dwordx4 v63,s[16:19],s76 offen offset:1024 lds\n"
"s_mov_b32 s76,524288\n"
"s_add_u32 m0,s13,8192\n"
"s_nop 0\n"
"buffer_load_dwordx4 v62,s[16:19],s76 offen offset:0 lds\n"
"buffer_load_dwordx4 v63,s[16:19],s76 offen offset:1024 lds\n"
"s_mov_b32 s76,128\n"
"s_add_u32 m0,s79,32768\n"
"s_nop 0\n"
"buffer_load_dwordx4 v106,s[16:19],s76 offen offset:0 lds\n"
"buffer_load_dwordx4 v107,s[16:19],s76 offen offset:1024 lds\n"
"buffer_load_dwordx4 v108,s[16:19],s76 offen offset:2048 lds\n"
"buffer_load_dwordx4 v109,s[16:19],s76 offen offset:3072 lds\n"
"s_mov_b32 s76,262272\n"
"s_add_u32 m0,s79,36864\n"
"s_nop 0\n"
"buffer_load_dwordx4 v106,s[16:19],s76 offen offset:0 lds\n"
"buffer_load_dwordx4 v107,s[16:19],s76 offen offset:1024 lds\n"
"buffer_load_dwordx4 v108,s[16:19],s76 offen offset:2048 lds\n"
"buffer_load_dwordx4 v109,s[16:19],s76 offen offset:3072 lds\n"
"s_lshl_b32 s82,s92,12\n"
"s_add_u32 s32,s32,s82\n"
"s_addc_u32 s33,s33,0\n"
"s_mov_b32 s34,4096\n"
"s_mov_b32 s35,0x20000\n"
"s_mov_b32 s26,176160768\n"
"s_mov_b32 s27,0x20000\n"
"s_mov_b32 s75,148897792\n"
"s_lshr_b32 s54,s2,5\n"
"s_lshl_b32 s54,s54,19\n"
"s_and_b32 s82,s92,0x47\n"
"s_bfe_u32 s83,s92,0x10003\n"
"s_lshl_b32 s83,s83,5\n"
"s_or_b32 s82,s82,s83\n"
"s_bfe_u32 s83,s92,0x20004\n"
"s_lshl3_add_u32 s82,s83,s82\n"
"s_lshl_b32 s82,s82,7\n"
"s_add_u32 s54,s54,s82\n"
"v_lshrrev_b32 v1,3,v0\n"
"v_bfe_u32 v2,v0,2,1\n"
"v_and_b32 v3,3,v0\n"
"v_lshlrev_b32 v3,4,v3\n"
"v_lshl_add_u32 v70,v1,6,v3\n"
"v_lshl_add_u32 v70,v2,11,v70\n"
"s_bfe_u32 s82,s54,0x10009\n"
"s_lshl_b32 s82,s82,5\n"
"v_xor_b32 v3,s82,v3\n"
"v_lshl_add_u32 v71,v2,6,v3\n"
"v_lshl_add_u32 v71,v1,14,v71\n"
"buffer_load_dwordx2 v[78:79],off,s[24:27],s75 sc1\n"
"s_lshl_b32 s82,s52,16\n"
"v_lshl_add_u32 v7,v4,13,s82\n"
"s_bitcmp1_b32 s52,0\n"
"s_cselect_b32 s83,0x80,0\n"
"v_xor_b32 v5,s83,v5\n"
"v_add_u32 v72,v7,v5\n"
"v_add_u32 v73,32768,v72\n"
"s_mov_b32 s86,0\n"
"v_and_b32 v1,15,v0\n"
"v_lshrrev_b32 v2,4,v0\n"
"v_lshlrev_b32 v3,6,v1\n"
"v_lshl_add_u32 v3,v2,4,v3\n"
"v_lshrrev_b32 v4,4,v3\n"
"v_and_b32 v4,32,v4\n"
"v_xor_b32 v97,v3,v4\n"
"s_lshr_b32 s82,s52,1\n"
"s_lshl_b32 s82,s82,14\n"
"v_and_b32 v3,7,v1\n"
"v_xor_b32 v3,v3,v2\n"
"v_lshlrev_b32 v3,4,v3\n"
"v_lshl_add_u32 v3,v1,7,v3\n"
"v_add_u32 v80,s82,v3\n"
"v_xor_b32 v81,64,v80\n"
"v_add_u32 v82,65536,v80\n"
"v_add_u32 v83,65536,v81\n"
"v_bfe_u32 v3,v0,2,2\n"
"v_lshl_add_u32 v4,v2,3,v3\n"
"v_lshlrev_b32 v4,8,v4\n"
"v_and_b32 v5,1,v2\n"
"v_lshl_add_u32 v5,v5,2,v3\n"
"v_lshlrev_b32 v5,5,v5\n"
"v_and_b32 v6,3,v0\n"
"v_lshl_add_u32 v4,v6,3,v4\n"
"s_and_b32 s82,s52,1\n"
"s_lshl_b32 s82,s82,13\n"
"s_add_u32 s82,s82,98304\n"
"v_add_u32 v4,s82,v4\n"
"v_xor_b32 v6,0,v5\n"
"v_add_u32 v98,v4,v6\n"
"v_xor_b32 v6,32,v5\n"
"v_add_u32 v99,v4,v6\n"
"v_xor_b32 v6,64,v5\n"
"v_add_u32 v100,v4,v6\n"
"v_xor_b32 v6,96,v5\n"
"v_add_u32 v101,v4,v6\n"
"v_xor_b32 v6,128,v5\n"
"v_add_u32 v102,v4,v6\n"
"v_xor_b32 v6,160,v5\n"
"v_add_u32 v103,v4,v6\n"
"v_xor_b32 v6,192,v5\n"
"v_add_u32 v104,v4,v6\n"
"v_xor_b32 v6,224,v5\n"
"v_add_u32 v105,v4,v6\n"
"s_and_b32 s53,s89,1\n"
"s_lshl_b32 s83,s88,10\n"
"s_and_b32 s82,s93,63\n"
"s_add_u32 s82,s82,128\n"
"s_lshl_b32 s82,s82,13\n"
"s_add_u32 s82,s82,s83\n"
"s_add_u32 s36,s36,s82\n"
"s_addc_u32 s37,s37,0\n"
"s_mov_b32 s38,2622464\n"
"s_mov_b32 s39,0x20000\n"
"s_lshl_b32 s82,s93,13\n"
"s_add_u32 s82,s82,s83\n"
"s_add_u32 s40,s40,s82\n"
"s_addc_u32 s41,s41,0\n"
"s_mov_b32 s42,7341056\n"
"s_mov_b32 s43,0x20000\n"
"s_cmp_lg_u32 s53,0\n"
"s_cbranch_scc1 .Lfk_v\n"
"s_waitcnt vmcnt(21)\n"
"s_barrier\n"
"ds_read_b128 v[152:155],v80 offset:12288\n"
"ds_read_b128 v[156:159],v80 offset:14336\n"
"ds_read_b64_tr_b16 v[160:161],v98 offset:49152\n"
"ds_read_b64_tr_b16 v[162:163],v98 offset:50176\n"
"ds_read_b64_tr_b16 v[164:165],v99 offset:49152\n"
"ds_read_b64_tr_b16 v[166:167],v99 offset:50176\n"
"ds_read_b64_tr_b16 v[168:169],v100 offset:49152\n"
"ds_read_b64_tr_b16 v[170:171],v100 offset:50176\n"
"ds_read_b64_tr_b16 v[172:173],v101 offset:49152\n"
"ds_read_b64_tr_b16 v[174:175],v101 offset:50176\n"
"ds_read_b64_tr_b16 v[176:177],v102 offset:49152\n"
"ds_read_b64_tr_b16 v[178:179],v102 offset:50176\n"
"ds_read_b64_tr_b16 v[180:181],v103 offset:49152\n"
"ds_read_b64_tr_b16 v[182:183],v103 offset:50176\n"
"ds_read_b64_tr_b16 v[184:185],v104 offset:49152\n"
"ds_read_b64_tr_b16 v[186:187],v104 offset:50176\n"
"ds_read_b64_tr_b16 v[188:189],v105 offset:49152\n"
"ds_read_b64_tr_b16 v[190:191],v105 offset:50176\n"
"s_waitcnt lgkmcnt(12)\n"
"v_mfma_f32_16x16x32_bf16 a[192:195],v[160:163],v[152:155],0\n"
"s_and_b32 s91,s89,7\n"
"s_mov_b32 s77,524288\n"
"s_add_u32 m0,s78,98304\n"
"v_mfma_f32_16x16x32_bf16 a[224:227],v[160:163],v[156:159],0\n"
"buffer_load_dwordx4 v110,s[20:23],s77 offen offset:0 lds\n"
"v_mfma_f32_16x16x32_bf16 a[228:231],v[164:167],v[156:159],0\n"
"s_lshr_b32 s97,s89,3\n"
"s_lshl2_add_u32 s90,s88,s97\n"
"v_mfma_f32_16x16x32_bf16 a[196:199],v[164:167],v[152:155],0\n"
"buffer_load_dwordx4 v111,s[20:23],s77 offen offset:1024 lds\n"
"s_waitcnt lgkmcnt(8)\n"
"v_mfma_f32_16x16x32_bf16 a[200:203],v[168:171],v[152:155],0\n"
"s_lshl_b32 vcc_lo,s90,8\n"
"s_add_u32 s28,s28,vcc_lo\n"
"s_addc_u32 s29,s29,0\n"
"v_mfma_f32_16x16x32_bf16 a[232:235],v[168:171],v[156:159],0\n"
"buffer_load_dwordx4 v113,s[20:23],s77 offen offset:2048 lds\n"
"v_mfma_f32_16x16x32_bf16 a[236:239],v[172:175],v[156:159],0\n"
"s_sub_u32 s30,8388608,vcc_lo\n"
"s_mov_b32 s31,0x20000\n"
"buffer_load_dwordx4 v114,s[20:23],s77 offen offset:3072 lds\n"
"v_mfma_f32_16x16x32_bf16 a[204:207],v[172:175],v[152:155],0\n"
"s_waitcnt vmcnt(18)\n"
"s_barrier\n"
"s_waitcnt lgkmcnt(4)\n"
"v_mfma_f32_16x16x32_bf16 a[208:211],v[176:179],v[152:155],0\n"
"ds_read_b128 v[128:131],v80 offset:0\n"
"ds_read_b128 v[192:195],v81 offset:0\n"
"v_mfma_f32_16x16x32_bf16 a[240:243],v[176:179],v[156:159],0\n"
"ds_read_b64_tr_b16 v[224:225],v98 offset:32768\n"
"ds_read_b64_tr_b16 v[226:227],v98 offset:33792\n"
"v_mfma_f32_16x16x32_bf16 a[244:247],v[180:183],v[156:159],0\n"
"ds_read_b64_tr_b16 v[228:229],v99 offset:32768\n"
"ds_read_b64_tr_b16 v[230:231],v99 offset:33792\n"
"v_mfma_f32_16x16x32_bf16 a[212:215],v[180:183],v[152:155],0\n"
"ds_read_b64_tr_b16 v[232:233],v100 offset:32768\n"
"ds_read_b64_tr_b16 v[234:235],v100 offset:33792\n"
"s_waitcnt lgkmcnt(8)\n"
"v_mfma_f32_16x16x32_bf16 a[216:219],v[184:187],v[152:155],0\n"
"ds_read_b64_tr_b16 v[236:237],v101 offset:32768\n"
"ds_read_b64_tr_b16 v[238:239],v101 offset:33792\n"
"v_mfma_f32_16x16x32_bf16 a[248:251],v[184:187],v[156:159],0\n"
"ds_read_b64_tr_b16 v[240:241],v102 offset:32768\n"
"ds_read_b64_tr_b16 v[242:243],v102 offset:33792\n"
"v_mfma_f32_16x16x32_bf16 a[252:255],v[188:191],v[156:159],0\n"
"ds_read_b64_tr_b16 v[244:245],v103 offset:32768\n"
"ds_read_b64_tr_b16 v[246:247],v103 offset:33792\n"
"v_mfma_f32_16x16x32_bf16 a[220:223],v[188:191],v[152:155],0\n"
"ds_read_b64_tr_b16 v[248:249],v104 offset:32768\n"
"ds_read_b64_tr_b16 v[250:251],v104 offset:33792\n"
"s_waitcnt lgkmcnt(10)\n"
"v_mfma_f32_16x16x32_bf16 a[0:3],v[160:163],v[128:131],0\n"
"ds_read_b64_tr_b16 v[252:253],v105 offset:32768\n"
"ds_read_b64_tr_b16 v[254:255],v105 offset:33792\n"
"v_mfma_f32_16x16x32_bf16 a[0:3],v[224:227],v[192:195],a[0:3]\n"
"ds_read_b128 v[132:135],v80 offset:2048\n"
"ds_read_b128 v[196:199],v81 offset:2048\n"
"v_mfma_f32_16x16x32_bf16 a[4:7],v[228:231],v[192:195],0\n"
"ds_read_b128 v[216:219],v81 offset:12288\n"
"ds_read_b128 v[220:223],v81 offset:14336\n"
"v_mfma_f32_16x16x32_bf16 a[4:7],v[164:167],v[128:131],a[4:7]\n"
"s_waitcnt lgkmcnt(12)\n"
"s_mov_b32 s77,786432\n"
"s_add_u32 m0,s78,114688\n"
"v_mfma_f32_16x16x32_bf16 a[8:11],v[168:171],v[128:131],0\n"
"buffer_load_dwordx4 v110,s[20:23],s77 offen offset:0 lds\n"
"v_mfma_f32_16x16x32_bf16 a[8:11],v[232:235],v[192:195],a[8:11]\n"
"s_mov_b32 s60,s91\n"
"s_sub_u32 s94,15,s91\n"
"v_mfma_f32_16x16x32_bf16 a[12:15],v[236:239],v[192:195],0\n"
"buffer_load_dwordx4 v111,s[20:23],s77 offen offset:1024 lds\n"
"v_mfma_f32_16x16x32_bf16 a[12:15],v[172:175],v[128:131],a[12:15]\n"
"s_cmp_lt_u32 s52,2\n"
"s_cselect_b32 s95,s94,s91\n"
"s_waitcnt lgkmcnt(8)\n"
"v_mfma_f32_16x16x32_bf16 a[16:19],v[176:179],v[128:131],0\n"
"buffer_load_dwordx4 v113,s[20:23],s77 offen offset:2048 lds\n"
"v_mfma_f32_16x16x32_bf16 a[16:19],v[240:243],v[192:195],a[16:19]\n"
"s_lshr_b32 vcc_lo,s95,1\n"
"s_lshl_b32 s55,vcc_lo,19\n"
"v_mfma_f32_16x16x32_bf16 a[20:23],v[244:247],v[192:195],0\n"
"buffer_load_dwordx4 v114,s[20:23],s77 offen offset:3072 lds\n"
"v_mfma_f32_16x16x32_bf16 a[20:23],v[180:183],v[128:131],a[20:23]\n"
"s_waitcnt lgkmcnt(4)\n"
"s_mov_b32 s76,256\n"
"s_add_u32 m0,s79,65536\n"
"v_mfma_f32_16x16x32_bf16 a[24:27],v[184:187],v[128:131],0\n"
"buffer_load_dwordx4 v106,s[16:19],s76 offen offset:0 lds\n"
"v_mfma_f32_16x16x32_bf16 a[24:27],v[248:251],v[192:195],a[24:27]\n"
"s_lshl_b32 s58,vcc_lo,10\n"
"s_add_u32 s58,s58,148898816\n"
"v_mfma_f32_16x16x32_bf16 a[28:31],v[252:255],v[192:195],0\n"
"buffer_load_dwordx4 v107,s[16:19],s76 offen offset:1024 lds\n"
"v_mfma_f32_16x16x32_bf16 a[28:31],v[188:191],v[128:131],a[28:31]\n"
"s_and_b32 vcc_lo,s95,1\n"
"s_waitcnt lgkmcnt(2)\n"
"v_mfma_f32_16x16x32_bf16 a[32:35],v[160:163],v[132:135],0\n"
"buffer_load_dwordx4 v108,s[16:19],s76 offen offset:2048 lds\n"
"v_mfma_f32_16x16x32_bf16 a[32:35],v[224:227],v[196:199],a[32:35]\n"
"s_lshl_b32 vcc_lo,vcc_lo,13\n"
"s_and_b32 vcc_hi,s78,4096\n"
"v_mfma_f32_16x16x32_bf16 a[36:39],v[228:231],v[196:199],0\n"
"buffer_load_dwordx4 v109,s[16:19],s76 offen offset:3072 lds\n"
"v_mfma_f32_16x16x32_bf16 a[36:39],v[164:167],v[132:135],a[36:39]\n"
"s_waitcnt vmcnt(21)\n"
"s_barrier\n"
"v_mfma_f32_16x16x32_bf16 a[40:43],v[168:171],v[132:135],0\n"
"ds_read_b128 v[136:139],v80 offset:4096\n"
"ds_read_b128 v[200:203],v81 offset:4096\n"
"v_mfma_f32_16x16x32_bf16 a[40:43],v[232:235],v[196:199],a[40:43]\n"
"ds_read_b128 v[140:143],v80 offset:6144\n"
"ds_read_b128 v[204:207],v81 offset:6144\n"
"v_mfma_f32_16x16x32_bf16 a[44:47],v[236:239],v[196:199],0\n"
"ds_read_b128 v[144:147],v80 offset:8192\n"
"ds_read_b128 v[208:211],v81 offset:8192\n"
"v_mfma_f32_16x16x32_bf16 a[44:47],v[172:175],v[132:135],a[44:47]\n"
"ds_read_b128 v[148:151],v80 offset:10240\n"
"ds_read_b128 v[212:215],v81 offset:10240\n"
"v_mfma_f32_16x16x32_bf16 a[48:51],v[176:179],v[132:135],0\n"
"s_add_u32 s55,s55,vcc_lo\n"
"s_mov_b32 s76,262400\n"
"s_add_u32 m0,s79,69632\n"
"v_mfma_f32_16x16x32_bf16 a[48:51],v[240:243],v[196:199],a[48:51]\n"
"buffer_load_dwordx4 v106,s[16:19],s76 offen offset:0 lds\n"
"v_mfma_f32_16x16x32_bf16 a[52:55],v[244:247],v[196:199],0\n"
"buffer_load_dwordx4 v107,s[16:19],s76 offen offset:1024 lds\n"
"v_mfma_f32_16x16x32_bf16 a[52:55],v[180:183],v[132:135],a[52:55]\n"
"buffer_load_dwordx4 v108,s[16:19],s76 offen offset:2048 lds\n"
"v_mfma_f32_16x16x32_bf16 a[56:59],v[184:187],v[132:135],0\n"
"buffer_load_dwordx4 v109,s[16:19],s76 offen offset:3072 lds\n"
"v_mfma_f32_16x16x32_bf16 a[56:59],v[248:251],v[196:199],a[56:59]\n"
"s_add_u32 s55,s55,vcc_hi\n"
"s_waitcnt lgkmcnt(0)\n"
"s_barrier\n"
"v_mfma_f32_16x16x32_bf16 a[60:63],v[252:255],v[196:199],0\n"
"s_and_b32 vcc_lo,s89,2\n"
"s_mov_b32 s77,1048576\n"
"s_add_u32 m0,s78,147456\n"
"v_mfma_f32_16x16x32_bf16 a[60:63],v[188:191],v[132:135],a[60:63]\n"
"buffer_load_dwordx4 v110,s[20:23],s77 offen offset:0 lds\n"
"v_mfma_f32_16x16x32_bf16 a[64:67],v[160:163],v[136:139],0\n"
"s_lshl2_add_u32 vcc_lo,s88,vcc_lo\n"
"s_and_b32 vcc_hi,s52,1\n"
"v_mfma_f32_16x16x32_bf16 a[64:67],v[224:227],v[200:203],a[64:67]\n"
"s_add_u32 vcc_lo,vcc_lo,vcc_hi\n"
"s_lshr_b32 vcc_hi,s89,2\n"
"v_mfma_f32_16x16x32_bf16 a[68:71],v[228:231],v[200:203],0\n"
"s_lshr_b32 s84,s52,1\n"
"s_lshl1_add_u32 vcc_hi,vcc_hi,s84\n"
"v_mfma_f32_16x16x32_bf16 a[68:71],v[164:167],v[136:139],a[68:71]\n"
"buffer_load_dwordx4 v111,s[20:23],s77 offen offset:1024 lds\n"
"v_mfma_f32_16x16x32_bf16 a[72:75],v[168:171],v[136:139],0\n"
"s_mul_i32 s56,vcc_lo,1179648\n"
"s_lshl_b32 s84,s84,19\n"
"v_mfma_f32_16x16x32_bf16 a[72:75],v[232:235],v[200:203],a[72:75]\n"
"s_add_u32 s56,s56,s84\n"
"s_lshr_b32 s84,s89,2\n"
"v_mfma_f32_16x16x32_bf16 a[76:79],v[236:239],v[200:203],0\n"
"s_lshl_b32 s84,s84,14\n"
"s_add_u32 s56,s56,s84\n"
"v_mfma_f32_16x16x32_bf16 a[76:79],v[172:175],v[136:139],a[76:79]\n"
"buffer_load_dwordx4 v113,s[20:23],s77 offen offset:2048 lds\n"
"v_mfma_f32_16x16x32_bf16 a[80:83],v[176:179],v[136:139],0\n"
"s_lshl_b32 s84,s53,13\n"
"s_add_u32 s56,s56,s84\n"
"v_mfma_f32_16x16x32_bf16 a[80:83],v[240:243],v[200:203],a[80:83]\n"
"s_add_u32 s56,s56,4194304\n"
"s_lshl4_add_u32 vcc_hi,s53,vcc_hi\n"
"v_mfma_f32_16x16x32_bf16 a[84:87],v[244:247],v[200:203],0\n"
"s_lshl_b32 vcc_lo,vcc_lo,8\n"
"s_lshl3_add_u32 s59,vcc_hi,vcc_lo\n"
"v_mfma_f32_16x16x32_bf16 a[84:87],v[180:183],v[136:139],a[84:87]\n"
"buffer_load_dwordx4 v114,s[20:23],s77 offen offset:3072 lds\n"
"v_mfma_f32_16x16x32_bf16 a[88:91],v[184:187],v[136:139],0\n"
"s_add_u32 s59,s59,148914176\n"
"s_lshl_b32 s57,s92,3\n"
"v_mfma_f32_16x16x32_bf16 a[88:91],v[248:251],v[200:203],a[88:91]\n"
"s_add_u32 s57,s57,148898816\n"
"s_lshl_b32 s63,s90,8\n"
"v_mfma_f32_16x16x32_bf16 a[92:95],v[252:255],v[200:203],0\n"
"s_add_u32 s63,s63,148914176\n"
"s_mov_b32 s77,1310720\n"
"s_add_u32 m0,s78,131072\n"
"v_mfma_f32_16x16x32_bf16 a[92:95],v[188:191],v[136:139],a[92:95]\n"
"buffer_load_dwordx4 v110,s[20:23],s77 offen offset:0 lds\n"
"v_mfma_f32_16x16x32_bf16 a[96:99],v[160:163],v[140:143],0\n"
"s_lshl_b32 s74,s92,11\n"
"s_add_u32 s74,s74,171966464\n"
"v_mfma_f32_16x16x32_bf16 a[96:99],v[224:227],v[204:207],a[96:99]\n"
"s_mul_i32 s96,s90,1179648\n"
"s_add_u32 s96,s96,4194304\n"
"v_mfma_f32_16x16x32_bf16 a[100:103],v[228:231],v[204:207],0\n"
"buffer_load_dwordx4 v111,s[20:23],s77 offen offset:1024 lds\n"
"v_mfma_f32_16x16x32_bf16 a[100:103],v[164:167],v[140:143],a[100:103]\n"
"s_add_u32 s73,s96,s85\n"
"s_and_b32 vcc_hi,s78,4096\n"
"v_mfma_f32_16x16x32_bf16 a[104:107],v[168:171],v[140:143],0\n"
"s_lshl_b32 vcc_hi,vcc_hi,1\n"
"s_add_u32 vcc_lo,s96,vcc_hi\n"
"v_mfma_f32_16x16x32_bf16 a[104:107],v[232:235],v[204:207],a[104:107]\n"
"s_add_u32 s65,vcc_lo,524288\n"
"s_lshl_b32 s66,s91,16\n"
"v_mfma_f32_16x16x32_bf16 a[108:111],v[236:239],v[204:207],0\n"
"buffer_load_dwordx4 v113,s[20:23],s77 offen offset:2048 lds\n"
"v_mfma_f32_16x16x32_bf16 a[108:111],v[172:175],v[140:143],a[108:111]\n"
"s_sub_u32 s66,vcc_lo,s66\n"
"s_add_u32 s66,s66,999424\n"
"v_mfma_f32_16x16x32_bf16 a[112:115],v[176:179],v[140:143],0\n"
"s_add_u32 s67,vcc_hi,65536\n"
"s_mul_i32 vcc_lo,vcc_hi,5\n"
"v_mfma_f32_16x16x32_bf16 a[112:115],v[240:243],v[204:207],a[112:115]\n"
"s_sub_u32 vcc_lo,s96,vcc_lo\n"
"s_add_u32 s64,vcc_lo,81920\n"
"v_mfma_f32_16x16x32_bf16 a[116:119],v[244:247],v[204:207],0\n"
"buffer_load_dwordx4 v114,s[20:23],s77 offen offset:3072 lds\n"
"v_mfma_f32_16x16x32_bf16 a[116:119],v[180:183],v[140:143],a[116:119]\n"
"s_add_u32 s62,vcc_lo,135168\n"
"s_add_u32 s61,vcc_lo,524288\n"
"s_cmp_lt_u32 s52,2\n"
"v_mfma_f32_16x16x32_bf16 a[120:123],v[184:187],v[140:143],0\n"
"s_cselect_b32 s64,s64,2146435072\n"
"s_cselect_b32 s67,s67,163840\n"
"s_cselect_b32 s65,s65,1\n"
"s_cselect_b32 vcc_lo,4,0\n"
"s_and_b32 vcc_hi,s52,1\n"
"v_mfma_f32_16x16x32_bf16 a[120:123],v[248:251],v[204:207],a[120:123]\n"
"s_add_u32 vcc_lo,vcc_lo,vcc_hi\n"
"s_mov_b32 s76,384\n"
"s_add_u32 m0,s79,0\n"
"v_mfma_f32_16x16x32_bf16 a[124:127],v[252:255],v[204:207],0\n"
"buffer_load_dwordx4 v106,s[16:19],s76 offen offset:0 lds\n"
"v_mfma_f32_16x16x32_bf16 a[124:127],v[188:191],v[140:143],a[124:127]\n"
"s_lshl_b32 vcc_hi,s90,12\n"
"s_add_u32 vcc_hi,vcc_hi,54525952\n"
"v_mfma_f32_16x16x32_bf16 a[128:131],v[160:163],v[144:147],0\n"
"s_lshl3_add_u32 s70,s95,vcc_lo\n"
"s_lshl_b32 s70,s70,17\n"
"v_mfma_f32_16x16x32_bf16 a[128:131],v[224:227],v[208:211],a[128:131]\n"
"s_add_u32 s70,s70,vcc_hi\n"
"s_lshl3_add_u32 s68,s94,vcc_lo\n"
"v_mfma_f32_16x16x32_bf16 a[132:135],v[228:231],v[208:211],0\n"
"buffer_load_dwordx4 v107,s[16:19],s76 offen offset:1024 lds\n"
"v_mfma_f32_16x16x32_bf16 a[132:135],v[164:167],v[144:147],a[132:135]\n"
"s_lshl_b32 s68,s68,17\n"
"s_add_u32 s68,s68,vcc_hi\n"
"v_mfma_f32_16x16x32_bf16 a[136:139],v[168:171],v[144:147],0\n"
"s_lshr_b32 vcc_lo,s90,3\n"
"s_lshl2_add_u32 vcc_lo,vcc_lo,s52\n"
"v_mfma_f32_16x16x32_bf16 a[136:139],v[232:235],v[208:211],a[136:139]\n"
"s_lshl_b32 vcc_lo,vcc_lo,7\n"
"s_and_b32 vcc_hi,s90,7\n"
"v_mfma_f32_16x16x32_bf16 a[140:143],v[236:239],v[208:211],0\n"
"buffer_load_dwordx4 v108,s[16:19],s76 offen offset:2048 lds\n"
"v_mfma_f32_16x16x32_bf16 a[140:143],v[172:175],v[144:147],a[140:143]\n"
"s_add_u32 vcc_lo,vcc_lo,vcc_hi\n"
"s_lshl3_add_u32 s69,s91,vcc_lo\n"
"v_mfma_f32_16x16x32_bf16 a[144:147],v[176:179],v[144:147],0\n"
"s_lshl_b32 s69,s69,3\n"
"s_add_u32 s69,s69,148930560\n"
"v_mfma_f32_16x16x32_bf16 a[144:147],v[240:243],v[208:211],a[144:147]\n"
"s_lshl3_add_u32 s71,s94,vcc_lo\n"
"s_lshl_b32 s71,s71,3\n"
"v_mfma_f32_16x16x32_bf16 a[148:151],v[244:247],v[208:211],0\n"
"buffer_load_dwordx4 v109,s[16:19],s76 offen offset:3072 lds\n"
"v_mfma_f32_16x16x32_bf16 a[148:151],v[180:183],v[144:147],a[148:151]\n"
"s_add_u32 s71,s71,148930560\n"
"s_mov_b32 s6,8388608\n"
"v_mfma_f32_16x16x32_bf16 a[152:155],v[184:187],v[144:147],0\n"
"s_mov_b32 s7,0x20000\n"
"s_lshr_b32 s94,s88,1\n"
"v_mfma_f32_16x16x32_bf16 a[152:155],v[248:251],v[208:211],a[152:155]\n"
"s_and_b32 vcc_lo,s91,1\n"
"s_mov_b32 s76,262528\n"
"s_add_u32 m0,s79,4096\n"
"v_mfma_f32_16x16x32_bf16 a[156:159],v[252:255],v[208:211],0\n"
"buffer_load_dwordx4 v106,s[16:19],s76 offen offset:0 lds\n"
"v_mfma_f32_16x16x32_bf16 a[156:159],v[188:191],v[144:147],a[156:159]\n"
"s_lshl1_add_u32 s95,s97,vcc_lo\n"
"s_and_b32 vcc_lo,s88,1\n"
"v_mfma_f32_16x16x32_bf16 a[160:163],v[160:163],v[148:151],0\n"
"s_lshr_b32 vcc_hi,s91,1\n"
"s_lshl2_add_u32 s96,vcc_lo,vcc_hi\n"
"v_mfma_f32_16x16x32_bf16 a[160:163],v[224:227],v[212:215],a[160:163]\n"
"s_lshr_b32 vcc_lo,s88,2\n"
"s_lshl2_add_u32 s96,vcc_lo,vcc_hi\n"
"v_mfma_f32_16x16x32_bf16 a[164:167],v[228:231],v[212:215],0\n"
"buffer_load_dwordx4 v107,s[16:19],s76 offen offset:1024 lds\n"
"v_mfma_f32_16x16x32_bf16 a[164:167],v[164:167],v[148:151],a[164:167]\n"
"s_sub_u32 vcc_lo,7,s88\n"
"s_min_u32 s94,vcc_lo,s88\n"
"v_mfma_f32_16x16x32_bf16 a[168:171],v[168:171],v[148:151],0\n"
"s_lshl3_add_u32 vcc_lo,s94,s96\n"
"s_lshl3_add_u32 vcc_lo,vcc_lo,s52\n"
"v_mfma_f32_16x16x32_bf16 a[168:171],v[232:235],v[212:215],a[168:171]\n"
"s_lshl_b32 s13,vcc_lo,15\n"
"s_lshl_b32 vcc_lo,s95,8\n"
"v_mfma_f32_16x16x32_bf16 a[172:175],v[236:239],v[212:215],0\n"
"buffer_load_dwordx4 v108,s[16:19],s76 offen offset:2048 lds\n"
"v_mfma_f32_16x16x32_bf16 a[172:175],v[172:175],v[148:151],a[172:175]\n"
"s_add_u32 s13,s13,vcc_lo\n"
"s_lshl3_add_u32 vcc_hi,s94,s95\n"
"v_mfma_f32_16x16x32_bf16 a[176:179],v[176:179],v[148:151],0\n"
"s_lshl_b32 s72,vcc_hi,8\n"
"s_add_u32 s72,s72,149061632\n"
"v_mfma_f32_16x16x32_bf16 a[176:179],v[240:243],v[212:215],a[176:179]\n"
"s_or_b32 vcc_lo,s96,s52\n"
"s_cselect_b32 s72,2146435072,s72\n"
"s_lshl_b32 s3,vcc_hi,18\n"
"v_mfma_f32_16x16x32_bf16 a[180:183],v[244:247],v[212:215],0\n"
"buffer_load_dwordx4 v109,s[16:19],s76 offen offset:3072 lds\n"
"v_mfma_f32_16x16x32_bf16 a[180:183],v[180:183],v[148:151],a[180:183]\n"
"s_lshl_b32 vcc_lo,s96,15\n"
"s_sub_u32 s3,s3,vcc_lo\n"
"s_add_u32 s3,s3,s78\n"
"v_mfma_f32_16x16x32_bf16 a[184:187],v[184:187],v[148:151],0\n"
"buffer_load_dwordx4 v[8:11],v70,s[32:35],0 offen offset:0\n"
"v_mfma_f32_16x16x32_bf16 a[184:187],v[248:251],v[212:215],a[184:187]\n"
"buffer_load_dwordx4 v[12:15],v70,s[32:35],0 offen offset:512\n"
"s_add_u32 s3,s3,100909056\n"
"v_mfma_f32_16x16x32_bf16 a[188:191],v[252:255],v[212:215],0\n"
"buffer_load_dwordx4 v[16:19],v70,s[32:35],0 offen offset:1024\n"
"buffer_load_dwordx4 v[20:23],v70,s[32:35],0 offen offset:1536\n"
"s_lshl3_add_u32 vcc_hi,vcc_hi,s96\n"
"v_mfma_f32_16x16x32_bf16 a[188:191],v[188:191],v[148:151],a[188:191]\n"
"s_waitcnt vmcnt(28)\n"
"s_barrier\n"
"v_mfma_f32_16x16x32_bf16 a[192:195],v[224:227],v[216:219],a[192:195]\n"
"ds_read_b128 v[152:155],v80 offset:45056\n"
"s_mov_b32 s87,524288\n"
"v_mfma_f32_16x16x32_bf16 a[224:227],v[224:227],v[220:223],a[224:227]\n"
"ds_read_b128 v[156:159],v80 offset:47104\n"
"buffer_load_dword v115,v112,s[36:39],s87 offen\n"
"v_mfma_f32_16x16x32_bf16 a[228:231],v[228:231],v[220:223],a[228:231]\n"
"ds_read_b64_tr_b16 v[160:161],v98 offset:0\n"
"s_mov_b32 s87,1048576\n"
"v_mfma_f32_16x16x32_bf16 a[196:199],v[228:231],v[216:219],a[196:199]\n"
"ds_read_b64_tr_b16 v[162:163],v98 offset:1024\n"
"buffer_load_dword v115,v112,s[36:39],s87 offen\n"
"v_mfma_f32_16x16x32_bf16 a[200:203],v[232:235],v[216:219],a[200:203]\n"
"ds_read_b64_tr_b16 v[164:165],v99 offset:0\n"
"s_mov_b32 s87,1572864\n"
"v_mfma_f32_16x16x32_bf16 a[232:235],v[232:235],v[220:223],a[232:235]\n"
"ds_read_b64_tr_b16 v[166:167],v99 offset:1024\n"
"buffer_load_dword v115,v112,s[36:39],s87 offen\n"
"v_mfma_f32_16x16x32_bf16 a[236:239],v[236:239],v[220:223],a[236:239]\n"
"ds_read_b64_tr_b16 v[168:169],v100 offset:0\n"
"s_lshl2_add_u32 vcc_hi,vcc_hi,s52\n"
"v_mfma_f32_16x16x32_bf16 a[204:207],v[236:239],v[216:219],a[204:207]\n"
"ds_read_b64_tr_b16 v[170:171],v100 offset:1024\n"
"s_lshl_b32 s2,vcc_hi,4\n"
"v_mfma_f32_16x16x32_bf16 a[208:211],v[240:243],v[216:219],a[208:211]\n"
"ds_read_b64_tr_b16 v[172:173],v101 offset:0\n"
"s_add_u32 s2,s2,149028864\n"
"v_mfma_f32_16x16x32_bf16 a[240:243],v[240:243],v[220:223],a[240:243]\n"
"ds_read_b64_tr_b16 v[174:175],v101 offset:1024\n"
"s_lshl2_add_u32 vcc_lo,s95,s52\n"
"v_mfma_f32_16x16x32_bf16 a[244:247],v[244:247],v[220:223],a[244:247]\n"
"ds_read_b64_tr_b16 v[176:177],v102 offset:0\n"
"s_lshl_b32 vcc_lo,vcc_lo,10\n"
"v_mfma_f32_16x16x32_bf16 a[212:215],v[244:247],v[216:219],a[212:215]\n"
"ds_read_b64_tr_b16 v[178:179],v102 offset:1024\n"
"s_lshl_b32 vcc_hi,s94,22\n"
"v_mfma_f32_16x16x32_bf16 a[216:219],v[248:251],v[216:219],a[216:219]\n"
"ds_read_b64_tr_b16 v[180:181],v103 offset:0\n"
"s_add_u32 s12,vcc_lo,vcc_hi\n"
"v_mfma_f32_16x16x32_bf16 a[248:251],v[248:251],v[220:223],a[248:251]\n"
"ds_read_b64_tr_b16 v[182:183],v103 offset:1024\n"
"s_lshl_b32 vcc_lo,s96,19\n"
"v_mfma_f32_16x16x32_bf16 a[252:255],v[252:255],v[220:223],a[252:255]\n"
"ds_read_b64_tr_b16 v[184:185],v104 offset:0\n"
"s_add_u32 s10,s12,vcc_lo\n"
"v_mfma_f32_16x16x32_bf16 a[220:223],v[252:255],v[216:219],a[220:223]\n"
"s_add_u32 s10,s10,71303168\n"
"ds_read_b64_tr_b16 v[186:187],v104 offset:1024\n"
"s_waitcnt lgkmcnt(10)\n"
"v_mfma_f32_16x16x32_bf16 a[192:195],v[160:163],v[152:155],a[192:195]\n"
"ds_read_b64_tr_b16 v[188:189],v105 offset:0\n"
"s_mul_i32 vcc_lo,s52,31744\n"
"v_mfma_f32_16x16x32_bf16 a[224:227],v[160:163],v[156:159],a[224:227]\n"
"ds_read_b64_tr_b16 v[190:191],v105 offset:1024\n"
"s_add_u32 s10,s10,vcc_lo\n"
"v_mfma_f32_16x16x32_bf16 a[228:231],v[164:167],v[156:159],a[228:231]\n"
"ds_read_b128 v[128:131],v80 offset:32768\n"
"s_lshl2_add_u32 vcc_lo,s94,s52\n"
"v_mfma_f32_16x16x32_bf16 a[196:199],v[164:167],v[152:155],a[196:199]\n"
"ds_read_b128 v[192:195],v81 offset:32768\n"
"s_waitcnt lgkmcnt(10)\n"
"v_mfma_f32_16x16x32_bf16 a[200:203],v[168:171],v[152:155],a[200:203]\n"
"ds_read_b64_tr_b16 v[224:225],v98 offset:16384\n"
"s_lshl_b32 vcc_hi,vcc_lo,11\n"
"v_mfma_f32_16x16x32_bf16 a[232:235],v[168:171],v[156:159],a[232:235]\n"
"ds_read_b64_tr_b16 v[226:227],v98 offset:17408\n"
"s_sub_u32 s12,s12,vcc_hi\n"
"v_mfma_f32_16x16x32_bf16 a[236:239],v[172:175],v[156:159],a[236:239]\n"
"ds_read_b64_tr_b16 v[228:229],v99 offset:16384\n"
"s_add_u32 s12,s12,57671680\n"
"v_mfma_f32_16x16x32_bf16 a[204:207],v[172:175],v[152:155],a[204:207]\n"
"ds_read_b64_tr_b16 v[230:231],v99 offset:17408\n"
"s_waitcnt lgkmcnt(10)\n"
"v_mfma_f32_16x16x32_bf16 a[208:211],v[176:179],v[152:155],a[208:211]\n"
"ds_read_b64_tr_b16 v[232:233],v100 offset:16384\n"
"s_lshl_b32 s95,vcc_lo,13\n"
"v_mfma_f32_16x16x32_bf16 a[240:243],v[176:179],v[156:159],a[240:243]\n"
"ds_read_b64_tr_b16 v[234:235],v100 offset:17408\n"
"s_add_u32 s95,s95,54525952\n"
"v_mfma_f32_16x16x32_bf16 a[244:247],v[180:183],v[156:159],a[244:247]\n"
"ds_read_b64_tr_b16 v[236:237],v101 offset:16384\n"
"s_lshl_b32 s8,s96,17\n"
"v_mfma_f32_16x16x32_bf16 a[212:215],v[180:183],v[152:155],a[212:215]\n"
"ds_read_b64_tr_b16 v[238:239],v101 offset:17408\n"
"s_waitcnt lgkmcnt(10)\n"
"v_mfma_f32_16x16x32_bf16 a[216:219],v[184:187],v[152:155],a[216:219]\n"
"ds_read_b64_tr_b16 v[240:241],v102 offset:16384\n"
"s_add_u32 s8,s8,s95\n"
"v_mfma_f32_16x16x32_bf16 a[248:251],v[184:187],v[156:159],a[248:251]\n"
"ds_read_b64_tr_b16 v[242:243],v102 offset:17408\n"
"s_and_b32 vcc_lo,s96,1\n"
"v_mfma_f32_16x16x32_bf16 a[252:255],v[188:191],v[156:159],a[252:255]\n"
"ds_read_b64_tr_b16 v[244:245],v103 offset:16384\n"
"s_lshl2_add_u32 vcc_lo,s94,vcc_lo\n"
"v_mfma_f32_16x16x32_bf16 a[220:223],v[188:191],v[152:155],a[220:223]\n"
"ds_read_b64_tr_b16 v[246:247],v103 offset:17408\n"
"s_waitcnt lgkmcnt(8)\n"
"v_mfma_f32_16x16x32_bf16 a[0:3],v[160:163],v[128:131],a[0:3]\n"
"ds_read_b64_tr_b16 v[248:249],v104 offset:16384\n"
"s_lshl_b32 s11,vcc_lo,10\n"
"v_mfma_f32_16x16x32_bf16 a[0:3],v[224:227],v[192:195],a[0:3]\n"
"ds_read_b64_tr_b16 v[250:251],v104 offset:17408\n"
"s_add_u32 s11,s11,148932608\n"
"v_mfma_f32_16x16x32_bf16 a[4:7],v[228:231],v[192:195],a[4:7]\n"
"ds_read_b64_tr_b16 v[252:253],v105 offset:16384\n"
"s_lshl_b32 s9,s94,12\n"
"v_mfma_f32_16x16x32_bf16 a[4:7],v[164:167],v[128:131],a[4:7]\n"
"ds_read_b64_tr_b16 v[254:255],v105 offset:17408\n"
"s_waitcnt lgkmcnt(8)\n"
"v_mfma_f32_16x16x32_bf16 a[8:11],v[168:171],v[128:131],a[8:11]\n"
"ds_read_b128 v[132:135],v80 offset:34816\n"
"s_add_u32 s9,s9,148931072\n"
"v_mfma_f32_16x16x32_bf16 a[8:11],v[232:235],v[192:195],a[8:11]\n"
"s_cmp_eq_u32 s52,0\n"
"ds_read_b128 v[196:199],v81 offset:34816\n"
"s_cselect_b32 s71,s71,2146435072\n"
"v_mfma_f32_16x16x32_bf16 a[12:15],v[236:239],v[192:195],a[12:15]\n"
"ds_read_b128 v[136:139],v80 offset:36864\n"
"v_mfma_f32_16x16x32_bf16 a[12:15],v[172:175],v[128:131],a[12:15]\n"
"ds_read_b128 v[200:203],v81 offset:36864\n"
"s_waitcnt lgkmcnt(8)\n"
"v_mfma_f32_16x16x32_bf16 a[16:19],v[176:179],v[128:131],a[16:19]\n"
"ds_read_b128 v[140:143],v80 offset:38912\n"
"v_mfma_f32_16x16x32_bf16 a[16:19],v[240:243],v[192:195],a[16:19]\n"
"ds_read_b128 v[204:207],v81 offset:38912\n"
"v_mfma_f32_16x16x32_bf16 a[20:23],v[244:247],v[192:195],a[20:23]\n"
"ds_read_b128 v[144:147],v80 offset:40960\n"
"v_mfma_f32_16x16x32_bf16 a[20:23],v[180:183],v[128:131],a[20:23]\n"
"ds_read_b128 v[208:211],v81 offset:40960\n"
"s_waitcnt lgkmcnt(8)\n"
"v_mfma_f32_16x16x32_bf16 a[24:27],v[184:187],v[128:131],a[24:27]\n"
"ds_read_b128 v[148:151],v80 offset:43008\n"
"v_mfma_f32_16x16x32_bf16 a[24:27],v[248:251],v[192:195],a[24:27]\n"
"ds_read_b128 v[212:215],v81 offset:43008\n"
"v_mfma_f32_16x16x32_bf16 a[28:31],v[252:255],v[192:195],a[28:31]\n"
"ds_read_b128 v[216:219],v81 offset:45056\n"
"v_mfma_f32_16x16x32_bf16 a[28:31],v[188:191],v[128:131],a[28:31]\n"
"ds_read_b128 v[220:223],v81 offset:47104\n"
"s_waitcnt lgkmcnt(10)\n"
"v_mfma_f32_16x16x32_bf16 a[32:35],v[160:163],v[132:135],a[32:35]\n"
"v_mfma_f32_16x16x32_bf16 a[32:35],v[224:227],v[196:199],a[32:35]\n"
"v_mfma_f32_16x16x32_bf16 a[36:39],v[228:231],v[196:199],a[36:39]\n"
"v_mfma_f32_16x16x32_bf16 a[36:39],v[164:167],v[132:135],a[36:39]\n"
"v_mfma_f32_16x16x32_bf16 a[40:43],v[168:171],v[132:135],a[40:43]\n"
"s_waitcnt lgkmcnt(0)\n"
"s_barrier\n"
"v_mfma_f32_16x16x32_bf16 a[40:43],v[232:235],v[196:199],a[40:43]\n"
"s_mov_b32 s77,1572864\n"
"s_add_u32 m0,s78,98304\n"
"v_mfma_f32_16x16x32_bf16 a[44:47],v[236:239],v[196:199],a[44:47]\n"
"buffer_load_dwordx4 v110,s[20:23],s77 offen offset:0 lds\n"
"v_mfma_f32_16x16x32_bf16 a[44:47],v[172:175],v[132:135],a[44:47]\n"
"v_mfma_f32_16x16x32_bf16 a[48:51],v[176:179],v[132:135],a[48:51]\n"
"v_mfma_f32_16x16x32_bf16 a[48:51],v[240:243],v[196:199],a[48:51]\n"
"v_mfma_f32_16x16x32_bf16 a[52:55],v[244:247],v[196:199],a[52:55]\n"
"buffer_load_dwordx4 v111,s[20:23],s77 offen offset:1024 lds\n"
"v_mfma_f32_16x16x32_bf16 a[52:55],v[180:183],v[132:135],a[52:55]\n"
"v_mfma_f32_16x16x32_bf16 a[56:59],v[184:187],v[132:135],a[56:59]\n"
"v_mfma_f32_16x16x32_bf16 a[56:59],v[248:251],v[196:199],a[56:59]\n"
"v_mfma_f32_16x16x32_bf16 a[60:63],v[252:255],v[196:199],a[60:63]\n"
"buffer_load_dwordx4 v113,s[20:23],s77 offen offset:2048 lds\n"
"v_mfma_f32_16x16x32_bf16 a[60:63],v[188:191],v[132:135],a[60:63]\n"
"v_mfma_f32_16x16x32_bf16 a[64:67],v[160:163],v[136:139],a[64:67]\n"
"v_mfma_f32_16x16x32_bf16 a[64:67],v[224:227],v[200:203],a[64:67]\n"
"v_mfma_f32_16x16x32_bf16 a[68:71],v[228:231],v[200:203],a[68:71]\n"
"buffer_load_dwordx4 v114,s[20:23],s77 offen offset:3072 lds\n"
"v_mfma_f32_16x16x32_bf16 a[68:71],v[164:167],v[136:139],a[68:71]\n"
"v_mfma_f32_16x16x32_bf16 a[72:75],v[168:171],v[136:139],a[72:75]\n"
"v_mfma_f32_16x16x32_bf16 a[72:75],v[232:235],v[200:203],a[72:75]\n"
"s_mov_b32 s77,1835008\n"
"s_add_u32 m0,s78,114688\n"
"v_mfma_f32_16x16x32_bf16 a[76:79],v[236:239],v[200:203],a[76:79]\n"
"buffer_load_dwordx4 v110,s[20:23],s77 offen offset:0 lds\n"
"v_mfma_f32_16x16x32_bf16 a[76:79],v[172:175],v[136:139],a[76:79]\n"
"v_mfma_f32_16x16x32_bf16 a[80:83],v[176:179],v[136:139],a[80:83]\n"
"v_mfma_f32_16x16x32_bf16 a[80:83],v[240:243],v[200:203],a[80:83]\n"
"v_mfma_f32_16x16x32_bf16 a[84:87],v[244:247],v[200:203],a[84:87]\n"
"buffer_load_dwordx4 v111,s[20:23],s77 offen offset:1024 lds\n"
"v_mfma_f32_16x16x32_bf16 a[84:87],v[180:183],v[136:139],a[84:87]\n"
"v_mfma_f32_16x16x32_bf16 a[88:91],v[184:187],v[136:139],a[88:91]\n"
"v_mfma_f32_16x16x32_bf16 a[88:91],v[248:251],v[200:203],a[88:91]\n"
"v_mfma_f32_16x16x32_bf16 a[92:95],v[252:255],v[200:203],a[92:95]\n"
"buffer_load_dwordx4 v113,s[20:23],s77 offen offset:2048 lds\n"
"v_mfma_f32_16x16x32_bf16 a[92:95],v[188:191],v[136:139],a[92:95]\n"
"s_waitcnt vmcnt(10)\n"
"v_mfma_f32_16x16x32_bf16 a[96:99],v[160:163],v[140:143],a[96:99]\n"
"v_lshlrev_b32 v24,16,v8\n"
"v_and_b32 v25,0xffff0000,v8\n"
"v_mfma_f32_16x16x32_bf16 a[96:99],v[224:227],v[204:207],a[96:99]\n"
"v_lshlrev_b32 v26,16,v9\n"
"v_and_b32 v27,0xffff0000,v9\n"
"v_mfma_f32_16x16x32_bf16 a[100:103],v[228:231],v[204:207],a[100:103]\n"
"buffer_load_dwordx4 v114,s[20:23],s77 offen offset:3072 lds\n"
"v_mfma_f32_16x16x32_bf16 a[100:103],v[164:167],v[140:143],a[100:103]\n"
"v_lshlrev_b32 v28,16,v10\n"
"v_and_b32 v29,0xffff0000,v10\n"
"v_mfma_f32_16x16x32_bf16 a[104:107],v[168:171],v[140:143],a[104:107]\n"
"v_lshlrev_b32 v30,16,v11\n"
"v_and_b32 v31,0xffff0000,v11\n"
"v_mfma_f32_16x16x32_bf16 a[104:107],v[232:235],v[204:207],a[104:107]\n"
"v_lshlrev_b32 v32,16,v12\n"
"v_and_b32 v33,0xffff0000,v12\n"
"v_mfma_f32_16x16x32_bf16 a[108:111],v[236:239],v[204:207],a[108:111]\n"
"s_mov_b32 s76,512\n"
"v_lshlrev_b32 v34,16,v13\n"
"s_add_u32 m0,s79,32768\n"
"v_mfma_f32_16x16x32_bf16 a[108:111],v[172:175],v[140:143],a[108:111]\n"
"buffer_load_dwordx4 v106,s[16:19],s76 offen offset:0 lds\n"
"v_mfma_f32_16x16x32_bf16 a[112:115],v[176:179],v[140:143],a[112:115]\n"
"v_and_b32 v35,0xffff0000,v13\n"
"v_lshlrev_b32 v36,16,v14\n"
"v_mfma_f32_16x16x32_bf16 a[112:115],v[240:243],v[204:207],a[112:115]\n"
"v_and_b32 v37,0xffff0000,v14\n"
"v_lshlrev_b32 v38,16,v15\n"
"v_mfma_f32_16x16x32_bf16 a[116:119],v[244:247],v[204:207],a[116:119]\n"
"v_and_b32 v39,0xffff0000,v15\n"
"v_lshlrev_b32 v40,16,v16\n"
"v_mfma_f32_16x16x32_bf16 a[116:119],v[180:183],v[140:143],a[116:119]\n"
"v_and_b32 v41,0xffff0000,v16\n"
"v_lshlrev_b32 v42,16,v17\n"
"v_mfma_f32_16x16x32_bf16 a[120:123],v[184:187],v[140:143],a[120:123]\n"
"buffer_load_dwordx4 v107,s[16:19],s76 offen offset:1024 lds\n"
"v_mfma_f32_16x16x32_bf16 a[120:123],v[248:251],v[204:207],a[120:123]\n"
"v_and_b32 v43,0xffff0000,v17\n"
"v_lshlrev_b32 v44,16,v18\n"
"v_mfma_f32_16x16x32_bf16 a[124:127],v[252:255],v[204:207],a[124:127]\n"
"v_and_b32 v45,0xffff0000,v18\n"
"v_lshlrev_b32 v46,16,v19\n"
"v_mfma_f32_16x16x32_bf16 a[124:127],v[188:191],v[140:143],a[124:127]\n"
"v_and_b32 v47,0xffff0000,v19\n"
"v_lshlrev_b32 v48,16,v20\n"
"v_mfma_f32_16x16x32_bf16 a[128:131],v[160:163],v[144:147],a[128:131]\n"
"v_and_b32 v49,0xffff0000,v20\n"
"v_lshlrev_b32 v50,16,v21\n"
"v_mfma_f32_16x16x32_bf16 a[128:131],v[224:227],v[208:211],a[128:131]\n"
"buffer_load_dwordx4 v108,s[16:19],s76 offen offset:2048 lds\n"
"v_mfma_f32_16x16x32_bf16 a[132:135],v[228:231],v[208:211],a[132:135]\n"
"v_and_b32 v51,0xffff0000,v21\n"
"v_lshlrev_b32 v52,16,v22\n"
"v_mfma_f32_16x16x32_bf16 a[132:135],v[164:167],v[144:147],a[132:135]\n"
"v_and_b32 v53,0xffff0000,v22\n"
"v_lshlrev_b32 v54,16,v23\n"
"v_mfma_f32_16x16x32_bf16 a[136:139],v[168:171],v[144:147],a[136:139]\n"
"v_and_b32 v55,0xffff0000,v23\n"
"v_add_f32 v56,v24,v25\n"
"v_mfma_f32_16x16x32_bf16 a[136:139],v[232:235],v[208:211],a[136:139]\n"
"v_add_f32 v57,v26,v27\n"
"v_add_f32 v56,v56,v28\n"
"v_mfma_f32_16x16x32_bf16 a[140:143],v[236:239],v[208:211],a[140:143]\n"
"buffer_load_dwordx4 v109,s[16:19],s76 offen offset:3072 lds\n"
"v_mfma_f32_16x16x32_bf16 a[140:143],v[172:175],v[144:147],a[140:143]\n"
"v_add_f32 v57,v57,v29\n"
"v_add_f32 v56,v56,v30\n"
"v_mfma_f32_16x16x32_bf16 a[144:147],v[176:179],v[144:147],a[144:147]\n"
"v_add_f32 v57,v57,v31\n"
"v_add_f32 v56,v56,v32\n"
"v_mfma_f32_16x16x32_bf16 a[144:147],v[240:243],v[208:211],a[144:147]\n"
"v_readfirstlane_b32 s82,v78\n"
"v_readfirstlane_b32 s83,v79\n"
"v_mfma_f32_16x16x32_bf16 a[148:151],v[244:247],v[208:211],a[148:151]\n"
"s_mov_b32 s76,262656\n"
"v_add_f32 v57,v57,v33\n"
"s_add_u32 m0,s79,36864\n"
"v_mfma_f32_16x16x32_bf16 a[148:151],v[180:183],v[144:147],a[148:151]\n"
"buffer_load_dwordx4 v106,s[16:19],s76 offen offset:0 lds\n"
"s_mul_i32 s80,s82,0x9e3779b1\n"
"v_mfma_f32_16x16x32_bf16 a[152:155],v[184:187],v[144:147],a[152:155]\n"
"s_add_u32 s80,s80,0x7f4a7c15\n"
"v_add_f32 v56,v56,v34\n"
"v_add_f32 v57,v57,v35\n"
"v_mfma_f32_16x16x32_bf16 a[152:155],v[248:251],v[208:211],a[152:155]\n"
"s_add_u32 s81,s83,s80\n"
"s_add_u32 s81,s81,1\n"
"v_mfma_f32_16x16x32_bf16 a[156:159],v[252:255],v[208:211],a[156:159]\n"
"v_mov_b32 v124,s80\n"
"v_mov_b32 v125,s81\n"
"v_mfma_f32_16x16x32_bf16 a[156:159],v[188:191],v[144:147],a[156:159]\n"
"v_add_f32 v56,v56,v36\n"
"v_add_f32 v57,v57,v37\n"
"v_mfma_f32_16x16x32_bf16 a[160:163],v[160:163],v[148:151],a[160:163]\n"
"buffer_load_dwordx4 v107,s[16:19],s76 offen offset:1024 lds\n"
"v_mfma_f32_16x16x32_bf16 a[160:163],v[224:227],v[212:215],a[160:163]\n"
"v_add_f32 v56,v56,v38\n"
"v_add_f32 v57,v57,v39\n"
"v_mfma_f32_16x16x32_bf16 a[164:167],v[228:231],v[212:215],a[164:167]\n"
"v_add_f32 v56,v56,v40\n"
"v_add_f32 v57,v57,v41\n"
"v_mfma_f32_16x16x32_bf16 a[164:167],v[164:167],v[148:151],a[164:167]\n"
"v_add_f32 v56,v56,v42\n"
"v_add_f32 v57,v57,v43\n"
"v_mfma_f32_16x16x32_bf16 a[168:171],v[168:171],v[148:151],a[168:171]\n"
"v_add_f32 v56,v56,v44\n"
"v_add_f32 v57,v57,v45\n"
"v_mfma_f32_16x16x32_bf16 a[168:171],v[232:235],v[212:215],a[168:171]\n"
"buffer_load_dwordx4 v108,s[16:19],s76 offen offset:2048 lds\n"
"v_mfma_f32_16x16x32_bf16 a[172:175],v[236:239],v[212:215],a[172:175]\n"
"v_add_f32 v56,v56,v46\n"
"v_add_f32 v57,v57,v47\n"
"v_mfma_f32_16x16x32_bf16 a[172:175],v[172:175],v[148:151],a[172:175]\n"
"v_add_f32 v56,v56,v48\n"
"v_add_f32 v57,v57,v49\n"
"v_mfma_f32_16x16x32_bf16 a[176:179],v[176:179],v[148:151],a[176:179]\n"
"v_add_f32 v56,v56,v50\n"
"v_add_f32 v57,v57,v51\n"
"v_mfma_f32_16x16x32_bf16 a[176:179],v[240:243],v[212:215],a[176:179]\n"
"v_add_f32 v56,v56,v52\n"
"v_add_f32 v57,v57,v53\n"
"v_mfma_f32_16x16x32_bf16 a[180:183],v[244:247],v[212:215],a[180:183]\n"
"buffer_load_dwordx4 v109,s[16:19],s76 offen offset:3072 lds\n"
"v_mfma_f32_16x16x32_bf16 a[180:183],v[180:183],v[148:151],a[180:183]\n"
"s_mov_b32 s87,2097152\n"
"v_add_f32 v56,v56,v54\n"
"v_add_f32 v57,v57,v55\n"
"v_mfma_f32_16x16x32_bf16 a[184:187],v[184:187],v[148:151],a[184:187]\n"
"buffer_load_dword v115,v112,s[36:39],s87 offen\n"
"v_mfma_f32_16x16x32_bf16 a[184:187],v[248:251],v[212:215],a[184:187]\n"
"s_mov_b32 s87,2621440\n"
"v_add_f32 v56,v56,v57\n"
"v_mfma_f32_16x16x32_bf16 a[188:191],v[252:255],v[212:215],a[188:191]\n"
"buffer_load_dword v115,v112,s[36:39],s87 offen\n"
"v_add_f32_dpp v56,v56,v56 quad_perm:[1,0,3,2] row_mask:0xf bank_mask:0xf\n"
"v_mfma_f32_16x16x32_bf16 a[188:191],v[188:191],v[148:151],a[188:191]\n"
"s_barrier\n"
"v_mfma_f32_16x16x32_bf16 a[192:195],v[224:227],v[216:219],a[192:195]\n"
"ds_read_b128 v[152:155],v82 offset:12288\n"
"v_mfma_f32_16x16x32_bf16 a[224:227],v[224:227],v[220:223],a[224:227]\n"
"ds_read_b128 v[156:159],v82 offset:14336\n"
"v_add_f32_dpp v56,v56,v56 quad_perm:[2,3,0,1] row_mask:0xf bank_mask:0xf\n"
"v_mfma_f32_16x16x32_bf16 a[228:231],v[228:231],v[220:223],a[228:231]\n"
"ds_read_b64_tr_b16 v[160:161],v98 offset:49152\n"
"v_mfma_f32_16x16x32_bf16 a[196:199],v[228:231],v[216:219],a[196:199]\n"
"ds_read_b64_tr_b16 v[162:163],v98 offset:50176\n"
"v_add_f32_dpp v56,v56,v56 row_ror:8 row_mask:0xf bank_mask:0xf\n"
"v_mfma_f32_16x16x32_bf16 a[200:203],v[232:235],v[216:219],a[200:203]\n"
"ds_read_b64_tr_b16 v[164:165],v99 offset:49152\n"
"v_mov_b32 v57,v56\n"
"v_mfma_f32_16x16x32_bf16 a[232:235],v[232:235],v[220:223],a[232:235]\n"
"ds_read_b64_tr_b16 v[166:167],v99 offset:50176\n"
"v_mfma_f32_16x16x32_bf16 a[236:239],v[236:239],v[220:223],a[236:239]\n"
"ds_read_b64_tr_b16 v[168:169],v100 offset:49152\n"
"v_permlane16_swap_b32 v57,v56\n"
"v_mfma_f32_16x16x32_bf16 a[204:207],v[236:239],v[216:219],a[204:207]\n"
"ds_read_b64_tr_b16 v[170:171],v100 offset:50176\n"
"v_add_f32 v56,v56,v57\n"
"v_mfma_f32_16x16x32_bf16 a[208:211],v[240:243],v[216:219],a[208:211]\n"
"ds_read_b64_tr_b16 v[172:173],v101 offset:49152\n"
"v_mov_b32 v57,v56\n"
"v_mfma_f32_16x16x32_bf16 a[240:243],v[240:243],v[220:223],a[240:243]\n"
"ds_read_b64_tr_b16 v[174:175],v101 offset:50176\n"
"v_mfma_f32_16x16x32_bf16 a[244:247],v[244:247],v[220:223],a[244:247]\n"
"ds_read_b64_tr_b16 v[176:177],v102 offset:49152\n"
"v_permlane32_swap_b32 v57,v56\n"
"v_mfma_f32_16x16x32_bf16 a[212:215],v[244:247],v[216:219],a[212:215]\n"
"ds_read_b64_tr_b16 v[178:179],v102 offset:50176\n"
"v_add_f32 v56,v56,v57\n"
"v_mfma_f32_16x16x32_bf16 a[216:219],v[248:251],v[216:219],a[216:219]\n"
"ds_read_b64_tr_b16 v[180:181],v103 offset:49152\n"
"v_mul_f32 v58,0x3a800000,v56\n"
"v_mfma_f32_16x16x32_bf16 a[248:251],v[248:251],v[220:223],a[248:251]\n"
"ds_read_b64_tr_b16 v[182:183],v103 offset:50176\n"
"v_sub_f32 v62,v24,v58\n"
"v_mfma_f32_16x16x32_bf16 a[252:255],v[252:255],v[220:223],a[252:255]\n"
"ds_read_b64_tr_b16 v[184:185],v104 offset:49152\n"
"v_mul_f32 v59,v62,v62\n"
"v_mfma_f32_16x16x32_bf16 a[220:223],v[252:255],v[216:219],a[220:223]\n"
"ds_read_b64_tr_b16 v[186:187],v104 offset:50176\n"
"v_sub_f32 v63,v25,v58\n"
"s_waitcnt lgkmcnt(10)\n"
"v_mfma_f32_16x16x32_bf16 a[192:195],v[160:163],v[152:155],a[192:195]\n"
"ds_read_b64_tr_b16 v[188:189],v105 offset:49152\n"
"v_mul_f32 v60,v63,v63\n"
"v_mfma_f32_16x16x32_bf16 a[224:227],v[160:163],v[156:159],a[224:227]\n"
"ds_read_b64_tr_b16 v[190:191],v105 offset:50176\n"
"v_sub_f32 v64,v26,v58\n"
"v_mfma_f32_16x16x32_bf16 a[228:231],v[164:167],v[156:159],a[228:231]\n"
"ds_read_b128 v[128:131],v82 offset:0\n"
"v_fmac_f32 v59,v64,v64\n"
"v_mfma_f32_16x16x32_bf16 a[196:199],v[164:167],v[152:155],a[196:199]\n"
"ds_read_b128 v[192:195],v83 offset:0\n"
"s_waitcnt lgkmcnt(10)\n"
"v_mfma_f32_16x16x32_bf16 a[200:203],v[168:171],v[152:155],a[200:203]\n"
"ds_read_b64_tr_b16 v[224:225],v98 offset:32768\n"
"v_sub_f32 v65,v27,v58\n"
"v_mfma_f32_16x16x32_bf16 a[232:235],v[168:171],v[156:159],a[232:235]\n"
"ds_read_b64_tr_b16 v[226:227],v98 offset:33792\n"
"v_fmac_f32 v60,v65,v65\n"
"v_mfma_f32_16x16x32_bf16 a[236:239],v[172:175],v[156:159],a[236:239]\n"
"ds_read_b64_tr_b16 v[228:229],v99 offset:32768\n"
"v_sub_f32 v62,v28,v58\n"
"v_mfma_f32_16x16x32_bf16 a[204:207],v[172:175],v[152:155],a[204:207]\n"
"ds_read_b64_tr_b16 v[230:231],v99 offset:33792\n"
"s_waitcnt lgkmcnt(10)\n"
"v_mfma_f32_16x16x32_bf16 a[208:211],v[176:179],v[152:155],a[208:211]\n"
"ds_read_b64_tr_b16 v[232:233],v100 offset:32768\n"
"v_fmac_f32 v59,v62,v62\n"
"v_mfma_f32_16x16x32_bf16 a[240:243],v[176:179],v[156:159],a[240:243]\n"
"ds_read_b64_tr_b16 v[234:235],v100 offset:33792\n"
"v_sub_f32 v63,v29,v58\n"
"v_mfma_f32_16x16x32_bf16 a[244:247],v[180:183],v[156:159],a[244:247]\n"
"ds_read_b64_tr_b16 v[236:237],v101 offset:32768\n"
"v_fmac_f32 v60,v63,v63\n"
"v_mfma_f32_16x16x32_bf16 a[212:215],v[180:183],v[152:155],a[212:215]\n"
"ds_read_b64_tr_b16 v[238:239],v101 offset:33792\n"
"s_waitcnt lgkmcnt(10)\n"
"v_mfma_f32_16x16x32_bf16 a[216:219],v[184:187],v[152:155],a[216:219]\n"
"ds_read_b64_tr_b16 v[240:241],v102 offset:32768\n"
"v_sub_f32 v64,v30,v58\n"
"v_mfma_f32_16x16x32_bf16 a[248:251],v[184:187],v[156:159],a[248:251]\n"
"ds_read_b64_tr_b16 v[242:243],v102 offset:33792\n"
"v_fmac_f32 v59,v64,v64\n"
"v_mfma_f32_16x16x32_bf16 a[252:255],v[188:191],v[156:159],a[252:255]\n"
"ds_read_b64_tr_b16 v[244:245],v103 offset:32768\n"
"v_sub_f32 v65,v31,v58\n"
"v_mfma_f32_16x16x32_bf16 a[220:223],v[188:191],v[152:155],a[220:223]\n"
"ds_read_b64_tr_b16 v[246:247],v103 offset:33792\n"
"s_waitcnt lgkmcnt(8)\n"
"v_mfma_f32_16x16x32_bf16 a[0:3],v[160:163],v[128:131],a[0:3]\n"
"ds_read_b64_tr_b16 v[248:249],v104 offset:32768\n"
"v_fmac_f32 v60,v65,v65\n"
"v_mfma_f32_16x16x32_bf16 a[0:3],v[224:227],v[192:195],a[0:3]\n"
"ds_read_b64_tr_b16 v[250:251],v104 offset:33792\n"
"v_sub_f32 v62,v32,v58\n"
"v_mfma_f32_16x16x32_bf16 a[4:7],v[228:231],v[192:195],a[4:7]\n"
"ds_read_b64_tr_b16 v[252:253],v105 offset:32768\n"
"v_fmac_f32 v59,v62,v62\n"
"v_mfma_f32_16x16x32_bf16 a[4:7],v[164:167],v[128:131],a[4:7]\n"
"ds_read_b64_tr_b16 v[254:255],v105 offset:33792\n"
"s_waitcnt lgkmcnt(8)\n"
"v_mfma_f32_16x16x32_bf16 a[8:11],v[168:171],v[128:131],a[8:11]\n"
"ds_read_b128 v[132:135],v82 offset:2048\n"
"v_sub_f32 v63,v33,v58\n"
"v_mfma_f32_16x16x32_bf16 a[8:11],v[232:235],v[192:195],a[8:11]\n"
"ds_read_b128 v[196:199],v83 offset:2048\n"
"v_fmac_f32 v60,v63,v63\n"
"v_mfma_f32_16x16x32_bf16 a[12:15],v[236:239],v[192:195],a[12:15]\n"
"ds_read_b128 v[136:139],v82 offset:4096\n"
"v_sub_f32 v64,v34,v58\n"
"v_mfma_f32_16x16x32_bf16 a[12:15],v[172:175],v[128:131],a[12:15]\n"
"ds_read_b128 v[200:203],v83 offset:4096\n"
"s_waitcnt lgkmcnt(8)\n"
"v_mfma_f32_16x16x32_bf16 a[16:19],v[176:179],v[128:131],a[16:19]\n"
"ds_read_b128 v[140:143],v82 offset:6144\n"
"v_fmac_f32 v59,v64,v64\n"
"v_mfma_f32_16x16x32_bf16 a[16:19],v[240:243],v[192:195],a[16:19]\n"
"ds_read_b128 v[204:207],v83 offset:6144\n"
"v_sub_f32 v65,v35,v58\n"
"v_mfma_f32_16x16x32_bf16 a[20:23],v[244:247],v[192:195],a[20:23]\n"
"ds_read_b128 v[144:147],v82 offset:8192\n"
"v_fmac_f32 v60,v65,v65\n"
"v_mfma_f32_16x16x32_bf16 a[20:23],v[180:183],v[128:131],a[20:23]\n"
"ds_read_b128 v[208:211],v83 offset:8192\n"
"s_waitcnt lgkmcnt(8)\n"
"v_mfma_f32_16x16x32_bf16 a[24:27],v[184:187],v[128:131],a[24:27]\n"
"ds_read_b128 v[148:151],v82 offset:10240\n"
"v_sub_f32 v62,v36,v58\n"
"v_mfma_f32_16x16x32_bf16 a[24:27],v[248:251],v[192:195],a[24:27]\n"
"ds_read_b128 v[212:215],v83 offset:10240\n"
"v_fmac_f32 v59,v62,v62\n"
"v_mfma_f32_16x16x32_bf16 a[28:31],v[252:255],v[192:195],a[28:31]\n"
"ds_read_b128 v[216:219],v83 offset:12288\n"
"v_sub_f32 v63,v37,v58\n"
"v_mfma_f32_16x16x32_bf16 a[28:31],v[188:191],v[128:131],a[28:31]\n"
"ds_read_b128 v[220:223],v83 offset:14336\n"
"s_waitcnt lgkmcnt(10)\n"
"v_mfma_f32_16x16x32_bf16 a[32:35],v[160:163],v[132:135],a[32:35]\n"
"v_fmac_f32 v60,v63,v63\n"
"v_sub_f32 v64,v38,v58\n"
"v_mfma_f32_16x16x32_bf16 a[32:35],v[224:227],v[196:199],a[32:35]\n"
"v_fmac_f32 v59,v64,v64\n"
"v_sub_f32 v65,v39,v58\n"
"v_mfma_f32_16x16x32_bf16 a[36:39],v[228:231],v[196:199],a[36:39]\n"
"v_fmac_f32 v60,v65,v65\n"
"v_sub_f32 v62,v40,v58\n"
"v_mfma_f32_16x16x32_bf16 a[36:39],v[164:167],v[132:135],a[36:39]\n"
"v_fmac_f32 v59,v62,v62\n"
"v_sub_f32 v63,v41,v58\n"
"v_mfma_f32_16x16x32_bf16 a[40:43],v[168:171],v[132:135],a[40:43]\n"
"s_waitcnt lgkmcnt(0)\n"
"s_barrier\n"
"v_mfma_f32_16x16x32_bf16 a[40:43],v[232:235],v[196:199],a[40:43]\n"
"s_mov_b32 s77,2097152\n"
"v_fmac_f32 v60,v63,v63\n"
"s_add_u32 m0,s78,147456\n"
"v_mfma_f32_16x16x32_bf16 a[44:47],v[236:239],v[196:199],a[44:47]\n"
"buffer_load_dwordx4 v110,s[20:23],s77 offen offset:0 lds\n"
"v_mfma_f32_16x16x32_bf16 a[44:47],v[172:175],v[132:135],a[44:47]\n"
"v_sub_f32 v64,v42,v58\n"
"v_fmac_f32 v59,v64,v64\n"
"v_mfma_f32_16x16x32_bf16 a[48:51],v[176:179],v[132:135],a[48:51]\n"
"v_sub_f32 v65,v43,v58\n"
"v_fmac_f32 v60,v65,v65\n"
"v_mfma_f32_16x16x32_bf16 a[48:51],v[240:243],v[196:199],a[48:51]\n"
"v_sub_f32 v62,v44,v58\n"
"v_fmac_f32 v59,v62,v62\n"
"v_mfma_f32_16x16x32_bf16 a[52:55],v[244:247],v[196:199],a[52:55]\n"
"buffer_load_dwordx4 v111,s[20:23],s77 offen offset:1024 lds\n"
"v_mfma_f32_16x16x32_bf16 a[52:55],v[180:183],v[132:135],a[52:55]\n"
"v_sub_f32 v63,v45,v58\n"
"v_fmac_f32 v60,v63,v63\n"
"v_mfma_f32_16x16x32_bf16 a[56:59],v[184:187],v[132:135],a[56:59]\n"
"v_sub_f32 v64,v46,v58\n"
"v_fmac_f32 v59,v64,v64\n"
"v_mfma_f32_16x16x32_bf16 a[56:59],v[248:251],v[196:199],a[56:59]\n"
"v_sub_f32 v65,v47,v58\n"
"v_fmac_f32 v60,v65,v65\n"
"v_mfma_f32_16x16x32_bf16 a[60:63],v[252:255],v[196:199],a[60:63]\n"
"buffer_load_dwordx4 v113,s[20:23],s77 offen offset:2048 lds\n"
"v_mfma_f32_16x16x32_bf16 a[60:63],v[188:191],v[132:135],a[60:63]\n"
"v_sub_f32 v62,v48,v58\n"
"v_fmac_f32 v59,v62,v62\n"
"v_mfma_f32_16x16x32_bf16 a[64:67],v[160:163],v[136:139],a[64:67]\n"
"v_sub_f32 v63,v49,v58\n"
"v_fmac_f32 v60,v63,v63\n"
"v_mfma_f32_16x16x32_bf16 a[64:67],v[224:227],v[200:203],a[64:67]\n"
"v_sub_f32 v64,v50,v58\n"
"v_fmac_f32 v59,v64,v64\n"
"v_mfma_f32_16x16x32_bf16 a[68:71],v[228:231],v[200:203],a[68:71]\n"
"buffer_load_dwordx4 v114,s[20:23],s77 offen offset:3072 lds\n"
"v_mfma_f32_16x16x32_bf16 a[68:71],v[164:167],v[136:139],a[68:71]\n"
"v_sub_f32 v65,v51,v58\n"
"v_fmac_f32 v60,v65,v65\n"
"v_mfma_f32_16x16x32_bf16 a[72:75],v[168:171],v[136:139],a[72:75]\n"
"v_sub_f32 v62,v52,v58\n"
"v_fmac_f32 v59,v62,v62\n"
"v_mfma_f32_16x16x32_bf16 a[72:75],v[232:235],v[200:203],a[72:75]\n"
"s_mov_b32 s77,2359296\n"
"v_sub_f32 v63,v53,v58\n"
"s_add_u32 m0,s78,131072\n"
"v_mfma_f32_16x16x32_bf16 a[76:79],v[236:239],v[200:203],a[76:79]\n"
"buffer_load_dwordx4 v110,s[20:23],s77 offen offset:0 lds\n"
"v_mfma_f32_16x16x32_bf16 a[76:79],v[172:175],v[136:139],a[76:79]\n"
"v_fmac_f32 v60,v63,v63\n"
"v_sub_f32 v64,v54,v58\n"
"v_mfma_f32_16x16x32_bf16 a[80:83],v[176:179],v[136:139],a[80:83]\n"
"v_fmac_f32 v59,v64,v64\n"
"v_sub_f32 v65,v55,v58\n"
"v_mfma_f32_16x16x32_bf16 a[80:83],v[240:243],v[200:203],a[80:83]\n"
"v_fmac_f32 v60,v65,v65\n"
"v_add_f32 v59,v59,v60\n"
"v_mfma_f32_16x16x32_bf16 a[84:87],v[244:247],v[200:203],a[84:87]\n"
"buffer_load_dwordx4 v111,s[20:23],s77 offen offset:1024 lds\n"
"v_mfma_f32_16x16x32_bf16 a[84:87],v[180:183],v[136:139],a[84:87]\n"
"v_mfma_f32_16x16x32_bf16 a[88:91],v[184:187],v[136:139],a[88:91]\n"
"v_add_f32_dpp v59,v59,v59 quad_perm:[1,0,3,2] row_mask:0xf bank_mask:0xf\n"
"v_mfma_f32_16x16x32_bf16 a[88:91],v[248:251],v[200:203],a[88:91]\n"
"s_nop 0\n"
"v_add_f32_dpp v59,v59,v59 quad_perm:[2,3,0,1] row_mask:0xf bank_mask:0xf\n"
"v_mfma_f32_16x16x32_bf16 a[92:95],v[252:255],v[200:203],a[92:95]\n"
"buffer_load_dwordx4 v113,s[20:23],s77 offen offset:2048 lds\n"
"v_mfma_f32_16x16x32_bf16 a[92:95],v[188:191],v[136:139],a[92:95]\n"
"v_add_f32_dpp v59,v59,v59 row_ror:8 row_mask:0xf bank_mask:0xf\n"
"v_mov_b32 v57,v59\n"
"v_mfma_f32_16x16x32_bf16 a[96:99],v[160:163],v[140:143],a[96:99]\n"
"s_nop 0\n"
"v_permlane16_swap_b32 v57,v59\n"
"v_mfma_f32_16x16x32_bf16 a[96:99],v[224:227],v[204:207],a[96:99]\n"
"v_add_f32 v59,v59,v57\n"
"v_mov_b32 v57,v59\n"
"v_mfma_f32_16x16x32_bf16 a[100:103],v[228:231],v[204:207],a[100:103]\n"
"buffer_load_dwordx4 v114,s[20:23],s77 offen offset:3072 lds\n"
"v_mfma_f32_16x16x32_bf16 a[100:103],v[164:167],v[140:143],a[100:103]\n"
"v_permlane32_swap_b32 v57,v59\n"
"v_add_f32 v59,v59,v57\n"
"v_mul_f32 v59,0x3a800000,v59\n"
"v_mfma_f32_16x16x32_bf16 a[104:107],v[168:171],v[140:143],a[104:107]\n"
"v_cvt_pk_bf16_f32 v59,v59,v59\n"
"v_lshlrev_b32 v59,16,v59\n"
"v_add_f32 v59,0x3727c5ac,v59\n"
"v_mfma_f32_16x16x32_bf16 a[104:107],v[232:235],v[204:207],a[104:107]\n"
"v_cvt_pk_bf16_f32 v59,v59,v59\n"
"v_lshlrev_b32 v59,16,v59\n"
"v_sqrt_f32 v59,v59\n"
"v_mfma_f32_16x16x32_bf16 a[108:111],v[236:239],v[204:207],a[108:111]\n"
"s_mov_b32 s76,640\n"
"v_cvt_pk_bf16_f32 v58,v58,v58\n"
"v_lshlrev_b32 v58,16,v58\n"
"s_add_u32 m0,s79,65536\n"
"v_mfma_f32_16x16x32_bf16 a[108:111],v[172:175],v[140:143],a[108:111]\n"
"buffer_load_dwordx4 v106,s[16:19],s76 offen offset:0 lds\n"
"v_cvt_pk_bf16_f32 v59,v59,v59\n"
"v_mfma_f32_16x16x32_bf16 a[112:115],v[176:179],v[140:143],a[112:115]\n"
"v_lshlrev_b32 v59,16,v59\n"
"v_rcp_f32 v66,v59\n"
"v_mfma_f32_16x16x32_bf16 a[112:115],v[240:243],v[204:207],a[112:115]\n"
"v_mul_f32 v66,0x3e0293ee,v66\n"
"v_mul_f32 v58,v58,v66\n"
"v_fma_f32 v24,v24,v66,-v58\n"
"v_mfma_f32_16x16x32_bf16 a[116:119],v[244:247],v[204:207],a[116:119]\n"
"v_fma_f32 v25,v25,v66,-v58\n"
"v_cvt_pk_bf16_f32 v8,v24,v25\n"
"v_fma_f32 v26,v26,v66,-v58\n"
"v_mfma_f32_16x16x32_bf16 a[116:119],v[180:183],v[140:143],a[116:119]\n"
"v_fma_f32 v27,v27,v66,-v58\n"
"v_cvt_pk_bf16_f32 v9,v26,v27\n"
"v_fma_f32 v28,v28,v66,-v58\n"
"v_mfma_f32_16x16x32_bf16 a[120:123],v[184:187],v[140:143],a[120:123]\n"
"buffer_load_dwordx4 v107,s[16:19],s76 offen offset:1024 lds\n"
"v_fma_f32 v29,v29,v66,-v58\n"
"v_mfma_f32_16x16x32_bf16 a[120:123],v[248:251],v[204:207],a[120:123]\n"
"v_cvt_pk_bf16_f32 v10,v28,v29\n"
"v_fma_f32 v30,v30,v66,-v58\n"
"v_fma_f32 v31,v31,v66,-v58\n"
"v_cvt_pk_bf16_f32 v11,v30,v31\n"
"v_mfma_f32_16x16x32_bf16 a[124:127],v[252:255],v[204:207],a[124:127]\n"
"buffer_store_dwordx4 v[8:11],v71,s[24:27],s54 offen sc1\n"
"v_mfma_f32_16x16x32_bf16 a[124:127],v[188:191],v[140:143],a[124:127]\n"
"s_add_u32 s54,s54,131072\n"
"v_fma_f32 v32,v32,v66,-v58\n"
"v_fma_f32 v33,v33,v66,-v58\n"
"v_cvt_pk_bf16_f32 v12,v32,v33\n"
"v_mfma_f32_16x16x32_bf16 a[128:131],v[160:163],v[144:147],a[128:131]\n"
"v_fma_f32 v34,v34,v66,-v58\n"
"v_fma_f32 v35,v35,v66,-v58\n"
"v_cvt_pk_bf16_f32 v13,v34,v35\n"
"v_mfma_f32_16x16x32_bf16 a[128:131],v[224:227],v[208:211],a[128:131]\n"
"buffer_load_dwordx4 v108,s[16:19],s76 offen offset:2048 lds\n"
"v_fma_f32 v36,v36,v66,-v58\n"
"v_mfma_f32_16x16x32_bf16 a[132:135],v[228:231],v[208:211],a[132:135]\n"
"v_fma_f32 v37,v37,v66,-v58\n"
"v_cvt_pk_bf16_f32 v14,v36,v37\n"
"v_fma_f32 v38,v38,v66,-v58\n"
"v_mfma_f32_16x16x32_bf16 a[132:135],v[164:167],v[144:147],a[132:135]\n"
"v_fma_f32 v39,v39,v66,-v58\n"
"v_cvt_pk_bf16_f32 v15,v38,v39\n"
"v_mfma_f32_16x16x32_bf16 a[136:139],v[168:171],v[144:147],a[136:139]\n"
"buffer_store_dwordx4 v[12:15],v71,s[24:27],s54 offen sc1\n"
"v_fma_f32 v40,v40,v66,-v58\n"
"v_fma_f32 v41,v41,v66,-v58\n"
"v_mfma_f32_16x16x32_bf16 a[136:139],v[232:235],v[208:211],a[136:139]\n"
"s_add_u32 s54,s54,131072\n"
"v_cvt_pk_bf16_f32 v16,v40,v41\n"
"v_fma_f32 v42,v42,v66,-v58\n"
"v_fma_f32 v43,v43,v66,-v58\n"
"v_mfma_f32_16x16x32_bf16 a[140:143],v[236:239],v[208:211],a[140:143]\n"
"buffer_load_dwordx4 v109,s[16:19],s76 offen offset:3072 lds\n"
"v_cvt_pk_bf16_f32 v17,v42,v43\n"
"v_mfma_f32_16x16x32_bf16 a[140:143],v[172:175],v[144:147],a[140:143]\n"
"v_fma_f32 v44,v44,v66,-v58\n"
"v_fma_f32 v45,v45,v66,-v58\n"
"v_cvt_pk_bf16_f32 v18,v44,v45\n"
"v_mfma_f32_16x16x32_bf16 a[144:147],v[176:179],v[144:147],a[144:147]\n"
"v_fma_f32 v46,v46,v66,-v58\n"
"v_fma_f32 v47,v47,v66,-v58\n"
"v_cvt_pk_bf16_f32 v19,v46,v47\n"
"v_mfma_f32_16x16x32_bf16 a[144:147],v[240:243],v[208:211],a[144:147]\n"
"buffer_store_dwordx4 v[16:19],v71,s[24:27],s54 offen sc1\n"
"v_fma_f32 v48,v48,v66,-v58\n"
"v_mfma_f32_16x16x32_bf16 a[148:151],v[244:247],v[208:211],a[148:151]\n"
"s_add_u32 s54,s54,131072\n"
"v_fma_f32 v49,v49,v66,-v58\n"
"v_cvt_pk_bf16_f32 v20,v48,v49\n"
"s_mov_b32 s76,262784\n"
"s_add_u32 m0,s79,69632\n"
"v_mfma_f32_16x16x32_bf16 a[148:151],v[180:183],v[144:147],a[148:151]\n"
"buffer_load_dwordx4 v106,s[16:19],s76 offen offset:0 lds\n"
"v_fma_f32 v50,v50,v66,-v58\n"
"v_mfma_f32_16x16x32_bf16 a[152:155],v[184:187],v[144:147],a[152:155]\n"
"v_fma_f32 v51,v51,v66,-v58\n"
"v_cvt_pk_bf16_f32 v21,v50,v51\n"
"v_fma_f32 v52,v52,v66,-v58\n"
"v_mfma_f32_16x16x32_bf16 a[152:155],v[248:251],v[208:211],a[152:155]\n"
"v_fma_f32 v53,v53,v66,-v58\n"
"v_cvt_pk_bf16_f32 v22,v52,v53\n"
"v_fma_f32 v54,v54,v66,-v58\n"
"v_mfma_f32_16x16x32_bf16 a[156:159],v[252:255],v[208:211],a[156:159]\n"
"v_fma_f32 v55,v55,v66,-v58\n"
"v_cvt_pk_bf16_f32 v23,v54,v55\n"
"v_mfma_f32_16x16x32_bf16 a[156:159],v[188:191],v[144:147],a[156:159]\n"
"buffer_store_dwordx4 v[20:23],v71,s[24:27],s54 offen sc1\n"
"v_mfma_f32_16x16x32_bf16 a[160:163],v[160:163],v[148:151],a[160:163]\n"
"buffer_load_dwordx4 v107,s[16:19],s76 offen offset:1024 lds\n"
"v_mfma_f32_16x16x32_bf16 a[160:163],v[224:227],v[212:215],a[160:163]\n"
"v_mfma_f32_16x16x32_bf16 a[164:167],v[228:231],v[212:215],a[164:167]\n"
"v_mfma_f32_16x16x32_bf16 a[164:167],v[164:167],v[148:151],a[164:167]\n"
"v_mfma_f32_16x16x32_bf16 a[168:171],v[168:171],v[148:151],a[168:171]\n"
"v_mfma_f32_16x16x32_bf16 a[168:171],v[232:235],v[212:215],a[168:171]\n"
"buffer_load_dwordx4 v108,s[16:19],s76 offen offset:2048 lds\n"
"v_mfma_f32_16x16x32_bf16 a[172:175],v[236:239],v[212:215],a[172:175]\n"
"v_mfma_f32_16x16x32_bf16 a[172:175],v[172:175],v[148:151],a[172:175]\n"
"v_mfma_f32_16x16x32_bf16 a[176:179],v[176:179],v[148:151],a[176:179]\n"
"v_mfma_f32_16x16x32_bf16 a[176:179],v[240:243],v[212:215],a[176:179]\n"
"v_mfma_f32_16x16x32_bf16 a[180:183],v[244:247],v[212:215],a[180:183]\n"
"buffer_load_dwordx4 v109,s[16:19],s76 offen offset:3072 lds\n"
"v_mfma_f32_16x16x32_bf16 a[180:183],v[180:183],v[148:151],a[180:183]\n"
"s_mov_b32 s87,0\n"
"v_mfma_f32_16x16x32_bf16 a[184:187],v[184:187],v[148:151],a[184:187]\n"
"buffer_load_dword v115,v112,s[40:43],s87 offen\n"
"v_mfma_f32_16x16x32_bf16 a[184:187],v[248:251],v[212:215],a[184:187]\n"
"s_mov_b32 s87,1048576\n"
"v_mfma_f32_16x16x32_bf16 a[188:191],v[252:255],v[212:215],a[188:191]\n"
"buffer_load_dword v115,v112,s[40:43],s87 offen\n"
"v_mfma_f32_16x16x32_bf16 a[188:191],v[188:191],v[148:151],a[188:191]\n"
"s_mov_b32 s87,2097152\n"
"s_waitcnt vmcnt(32)\n"
"s_barrier\n"
"v_mfma_f32_16x16x32_bf16 a[192:195],v[224:227],v[216:219],a[192:195]\n"
"ds_read_b128 v[152:155],v80 offset:12288\n"
"buffer_load_dword v115,v112,s[40:43],s87 offen\n"
"v_mfma_f32_16x16x32_bf16 a[224:227],v[224:227],v[220:223],a[224:227]\n"
"ds_read_b128 v[156:159],v80 offset:14336\n"
"v_mfma_f32_16x16x32_bf16 a[228:231],v[228:231],v[220:223],a[228:231]\n"
"ds_read_b64_tr_b16 v[160:161],v98 offset:0\n"
"v_mfma_f32_16x16x32_bf16 a[196:199],v[228:231],v[216:219],a[196:199]\n"
"ds_read_b64_tr_b16 v[162:163],v98 offset:1024\n"
"v_mfma_f32_16x16x32_bf16 a[200:203],v[232:235],v[216:219],a[200:203]\n"
"ds_read_b64_tr_b16 v[164:165],v99 offset:0\n"
"v_mfma_f32_16x16x32_bf16 a[232:235],v[232:235],v[220:223],a[232:235]\n"
"ds_read_b64_tr_b16 v[166:167],v99 offset:1024\n"
"v_mfma_f32_16x16x32_bf16 a[236:239],v[236:239],v[220:223],a[236:239]\n"
"ds_read_b64_tr_b16 v[168:169],v100 offset:0\n"
"v_mfma_f32_16x16x32_bf16 a[204:207],v[236:239],v[216:219],a[204:207]\n"
"ds_read_b64_tr_b16 v[170:171],v100 offset:1024\n"
"v_mfma_f32_16x16x32_bf16 a[208:211],v[240:243],v[216:219],a[208:211]\n"
"ds_read_b64_tr_b16 v[172:173],v101 offset:0\n"
"v_mfma_f32_16x16x32_bf16 a[240:243],v[240:243],v[220:223],a[240:243]\n"
"ds_read_b64_tr_b16 v[174:175],v101 offset:1024\n"
"v_mfma_f32_16x16x32_bf16 a[244:247],v[244:247],v[220:223],a[244:247]\n"
"ds_read_b64_tr_b16 v[176:177],v102 offset:0\n"
"v_mfma_f32_16x16x32_bf16 a[212:215],v[244:247],v[216:219],a[212:215]\n"
"ds_read_b64_tr_b16 v[178:179],v102 offset:1024\n"
"v_mfma_f32_16x16x32_bf16 a[216:219],v[248:251],v[216:219],a[216:219]\n"
"ds_read_b64_tr_b16 v[180:181],v103 offset:0\n"
"v_mfma_f32_16x16x32_bf16 a[248:251],v[248:251],v[220:223],a[248:251]\n"
"ds_read_b64_tr_b16 v[182:183],v103 offset:1024\n"
"v_mfma_f32_16x16x32_bf16 a[252:255],v[252:255],v[220:223],a[252:255]\n"
"ds_read_b64_tr_b16 v[184:185],v104 offset:0\n"
"v_mfma_f32_16x16x32_bf16 a[220:223],v[252:255],v[216:219],a[220:223]\n"
"ds_read_b64_tr_b16 v[186:187],v104 offset:1024\n"
"s_waitcnt lgkmcnt(10)\n"
"v_mfma_f32_16x16x32_bf16 a[192:195],v[160:163],v[152:155],a[192:195]\n"
"ds_read_b64_tr_b16 v[188:189],v105 offset:0\n"
"v_mfma_f32_16x16x32_bf16 a[224:227],v[160:163],v[156:159],a[224:227]\n"
"ds_read_b64_tr_b16 v[190:191],v105 offset:1024\n"
"v_mfma_f32_16x16x32_bf16 a[228:231],v[164:167],v[156:159],a[228:231]\n"
"ds_read_b128 v[128:131],v80 offset:0\n"
"v_mfma_f32_16x16x32_bf16 a[196:199],v[164:167],v[152:155],a[196:199]\n"
"ds_read_b128 v[192:195],v81 offset:0\n"
"s_waitcnt lgkmcnt(10)\n"
"v_mfma_f32_16x16x32_bf16 a[200:203],v[168:171],v[152:155],a[200:203]\n"
"ds_read_b64_tr_b16 v[224:225],v98 offset:16384\n"
"v_mfma_f32_16x16x32_bf16 a[232:235],v[168:171],v[156:159],a[232:235]\n"
"ds_read_b64_tr_b16 v[226:227],v98 offset:17408\n"
"v_mfma_f32_16x16x32_bf16 a[236:239],v[172:175],v[156:159],a[236:239]\n"
"ds_read_b64_tr_b16 v[228:229],v99 offset:16384\n"
"v_mfma_f32_16x16x32_bf16 a[204:207],v[172:175],v[152:155],a[204:207]\n"
"ds_read_b64_tr_b16 v[230:231],v99 offset:17408\n"
"s_waitcnt lgkmcnt(10)\n"
"v_mfma_f32_16x16x32_bf16 a[208:211],v[176:179],v[152:155],a[208:211]\n"
"ds_read_b64_tr_b16 v[232:233],v100 offset:16384\n"
"v_mfma_f32_16x16x32_bf16 a[240:243],v[176:179],v[156:159],a[240:243]\n"
"ds_read_b64_tr_b16 v[234:235],v100 offset:17408\n"
"v_mfma_f32_16x16x32_bf16 a[244:247],v[180:183],v[156:159],a[244:247]\n"
"ds_read_b64_tr_b16 v[236:237],v101 offset:16384\n"
"v_mfma_f32_16x16x32_bf16 a[212:215],v[180:183],v[152:155],a[212:215]\n"
"ds_read_b64_tr_b16 v[238:239],v101 offset:17408\n"
"s_waitcnt lgkmcnt(10)\n"
"v_mfma_f32_16x16x32_bf16 a[216:219],v[184:187],v[152:155],a[216:219]\n"
"ds_read_b64_tr_b16 v[240:241],v102 offset:16384\n"
"v_mfma_f32_16x16x32_bf16 a[248:251],v[184:187],v[156:159],a[248:251]\n"
"ds_read_b64_tr_b16 v[242:243],v102 offset:17408\n"
"v_mfma_f32_16x16x32_bf16 a[252:255],v[188:191],v[156:159],a[252:255]\n"
"ds_read_b64_tr_b16 v[244:245],v103 offset:16384\n"
"v_mfma_f32_16x16x32_bf16 a[220:223],v[188:191],v[152:155],a[220:223]\n"
"ds_read_b64_tr_b16 v[246:247],v103 offset:17408\n"
"s_waitcnt lgkmcnt(8)\n"
"v_mfma_f32_16x16x32_bf16 a[0:3],v[160:163],v[128:131],a[0:3]\n"
"ds_read_b64_tr_b16 v[248:249],v104 offset:16384\n"
"v_mfma_f32_16x16x32_bf16 a[0:3],v[224:227],v[192:195],a[0:3]\n"
"ds_read_b64_tr_b16 v[250:251],v104 offset:17408\n"
"v_mfma_f32_16x16x32_bf16 a[4:7],v[228:231],v[192:195],a[4:7]\n"
"ds_read_b64_tr_b16 v[252:253],v105 offset:16384\n"
"v_mfma_f32_16x16x32_bf16 a[4:7],v[164:167],v[128:131],a[4:7]\n"
"ds_read_b64_tr_b16 v[254:255],v105 offset:17408\n"
"s_waitcnt lgkmcnt(8)\n"
"v_mfma_f32_16x16x32_bf16 a[8:11],v[168:171],v[128:131],a[8:11]\n"
"ds_read_b128 v[132:135],v80 offset:2048\n"
"v_mfma_f32_16x16x32_bf16 a[8:11],v[232:235],v[192:195],a[8:11]\n"
"ds_read_b128 v[196:199],v81 offset:2048\n"
"v_mfma_f32_16x16x32_bf16 a[12:15],v[236:239],v[192:195],a[12:15]\n"
"ds_read_b128 v[136:139],v80 offset:4096\n"
"v_mfma_f32_16x16x32_bf16 a[12:15],v[172:175],v[128:131],a[12:15]\n"
"ds_read_b128 v[200:203],v81 offset:4096\n"
"s_waitcnt lgkmcnt(8)\n"
"v_mfma_f32_16x16x32_bf16 a[16:19],v[176:179],v[128:131],a[16:19]\n"
"ds_read_b128 v[140:143],v80 offset:6144\n"
"v_mfma_f32_16x16x32_bf16 a[16:19],v[240:243],v[192:195],a[16:19]\n"
"ds_read_b128 v[204:207],v81 offset:6144\n"
"v_mfma_f32_16x16x32_bf16 a[20:23],v[244:247],v[192:195],a[20:23]\n"
"ds_read_b128 v[144:147],v80 offset:8192\n"
"v_mfma_f32_16x16x32_bf16 a[20:23],v[180:183],v[128:131],a[20:23]\n"
"ds_read_b128 v[208:211],v81 offset:8192\n"
"s_waitcnt lgkmcnt(8)\n"
"v_mfma_f32_16x16x32_bf16 a[24:27],v[184:187],v[128:131],a[24:27]\n"
"ds_read_b128 v[148:151],v80 offset:10240\n"
"v_mfma_f32_16x16x32_bf16 a[24:27],v[248:251],v[192:195],a[24:27]\n"
"ds_read_b128 v[212:215],v81 offset:10240\n"
"v_mfma_f32_16x16x32_bf16 a[28:31],v[252:255],v[192:195],a[28:31]\n"
"ds_read_b128 v[216:219],v81 offset:12288\n"
"v_mfma_f32_16x16x32_bf16 a[28:31],v[188:191],v[128:131],a[28:31]\n"
"ds_read_b128 v[220:223],v81 offset:14336\n"
"s_waitcnt lgkmcnt(10)\n"
"v_mfma_f32_16x16x32_bf16 a[32:35],v[160:163],v[132:135],a[32:35]\n"
"v_mfma_f32_16x16x32_bf16 a[32:35],v[224:227],v[196:199],a[32:35]\n"
"v_mfma_f32_16x16x32_bf16 a[36:39],v[228:231],v[196:199],a[36:39]\n"
"v_mfma_f32_16x16x32_bf16 a[36:39],v[164:167],v[132:135],a[36:39]\n"
"v_mfma_f32_16x16x32_bf16 a[40:43],v[168:171],v[132:135],a[40:43]\n"
"s_waitcnt lgkmcnt(0)\n"
"s_barrier\n"
"v_mfma_f32_16x16x32_bf16 a[40:43],v[232:235],v[196:199],a[40:43]\n"
"s_mov_b32 s77,2621440\n"
"s_add_u32 m0,s78,98304\n"
"v_mfma_f32_16x16x32_bf16 a[44:47],v[236:239],v[196:199],a[44:47]\n"
"buffer_load_dwordx4 v110,s[20:23],s77 offen offset:0 lds\n"
"v_mfma_f32_16x16x32_bf16 a[44:47],v[172:175],v[132:135],a[44:47]\n"
"v_mfma_f32_16x16x32_bf16 a[48:51],v[176:179],v[132:135],a[48:51]\n"
"v_mfma_f32_16x16x32_bf16 a[48:51],v[240:243],v[196:199],a[48:51]\n"
"v_mfma_f32_16x16x32_bf16 a[52:55],v[244:247],v[196:199],a[52:55]\n"
"buffer_load_dwordx4 v111,s[20:23],s77 offen offset:1024 lds\n"
"v_mfma_f32_16x16x32_bf16 a[52:55],v[180:183],v[132:135],a[52:55]\n"
"v_mfma_f32_16x16x32_bf16 a[56:59],v[184:187],v[132:135],a[56:59]\n"
"v_mfma_f32_16x16x32_bf16 a[56:59],v[248:251],v[196:199],a[56:59]\n"
"v_mfma_f32_16x16x32_bf16 a[60:63],v[252:255],v[196:199],a[60:63]\n"
"buffer_load_dwordx4 v113,s[20:23],s77 offen offset:2048 lds\n"
"v_mfma_f32_16x16x32_bf16 a[60:63],v[188:191],v[132:135],a[60:63]\n"
"v_mfma_f32_16x16x32_bf16 a[64:67],v[160:163],v[136:139],a[64:67]\n"
"v_mfma_f32_16x16x32_bf16 a[64:67],v[224:227],v[200:203],a[64:67]\n"
"v_mfma_f32_16x16x32_bf16 a[68:71],v[228:231],v[200:203],a[68:71]\n"
"buffer_load_dwordx4 v114,s[20:23],s77 offen offset:3072 lds\n"
"v_mfma_f32_16x16x32_bf16 a[68:71],v[164:167],v[136:139],a[68:71]\n"
"v_mfma_f32_16x16x32_bf16 a[72:75],v[168:171],v[136:139],a[72:75]\n"
"v_mfma_f32_16x16x32_bf16 a[72:75],v[232:235],v[200:203],a[72:75]\n"
"s_mov_b32 s77,2883584\n"
"s_add_u32 m0,s78,114688\n"
"v_mfma_f32_16x16x32_bf16 a[76:79],v[236:239],v[200:203],a[76:79]\n"
"buffer_load_dwordx4 v110,s[20:23],s77 offen offset:0 lds\n"
"v_mfma_f32_16x16x32_bf16 a[76:79],v[172:175],v[136:139],a[76:79]\n"
"v_mfma_f32_16x16x32_bf16 a[80:83],v[176:179],v[136:139],a[80:83]\n"
"v_mfma_f32_16x16x32_bf16 a[80:83],v[240:243],v[200:203],a[80:83]\n"
"v_mfma_f32_16x16x32_bf16 a[84:87],v[244:247],v[200:203],a[84:87]\n"
"buffer_load_dwordx4 v111,s[20:23],s77 offen offset:1024 lds\n"
"v_mfma_f32_16x16x32_bf16 a[84:87],v[180:183],v[136:139],a[84:87]\n"
"v_mfma_f32_16x16x32_bf16 a[88:91],v[184:187],v[136:139],a[88:91]\n"
"v_mfma_f32_16x16x32_bf16 a[88:91],v[248:251],v[200:203],a[88:91]\n"
"v_mfma_f32_16x16x32_bf16 a[92:95],v[252:255],v[200:203],a[92:95]\n"
"buffer_load_dwordx4 v113,s[20:23],s77 offen offset:2048 lds\n"
"v_mfma_f32_16x16x32_bf16 a[92:95],v[188:191],v[136:139],a[92:95]\n"
"v_mfma_f32_16x16x32_bf16 a[96:99],v[160:163],v[140:143],a[96:99]\n"
"v_mfma_f32_16x16x32_bf16 a[96:99],v[224:227],v[204:207],a[96:99]\n"
"v_mfma_f32_16x16x32_bf16 a[100:103],v[228:231],v[204:207],a[100:103]\n"
"buffer_load_dwordx4 v114,s[20:23],s77 offen offset:3072 lds\n"
"v_mfma_f32_16x16x32_bf16 a[100:103],v[164:167],v[140:143],a[100:103]\n"
"v_mfma_f32_16x16x32_bf16 a[104:107],v[168:171],v[140:143],a[104:107]\n"
"v_mfma_f32_16x16x32_bf16 a[104:107],v[232:235],v[204:207],a[104:107]\n"
"v_mfma_f32_16x16x32_bf16 a[108:111],v[236:239],v[204:207],a[108:111]\n"
"s_mov_b32 s76,768\n"
"s_add_u32 m0,s79,0\n"
"v_mfma_f32_16x16x32_bf16 a[108:111],v[172:175],v[140:143],a[108:111]\n"
"buffer_load_dwordx4 v106,s[16:19],s76 offen offset:0 lds\n"
"v_mfma_f32_16x16x32_bf16 a[112:115],v[176:179],v[140:143],a[112:115]\n"
"v_mfma_f32_16x16x32_bf16 a[112:115],v[240:243],v[204:207],a[112:115]\n"
"v_mfma_f32_16x16x32_bf16 a[116:119],v[244:247],v[204:207],a[116:119]\n"
"v_mfma_f32_16x16x32_bf16 a[116:119],v[180:183],v[140:143],a[116:119]\n"
"v_mfma_f32_16x16x32_bf16 a[120:123],v[184:187],v[140:143],a[120:123]\n"
"buffer_load_dwordx4 v107,s[16:19],s76 offen offset:1024 lds\n"
"v_mfma_f32_16x16x32_bf16 a[120:123],v[248:251],v[204:207],a[120:123]\n"
"v_mfma_f32_16x16x32_bf16 a[124:127],v[252:255],v[204:207],a[124:127]\n"
"v_mfma_f32_16x16x32_bf16 a[124:127],v[188:191],v[140:143],a[124:127]\n"
"v_mfma_f32_16x16x32_bf16 a[128:131],v[160:163],v[144:147],a[128:131]\n"
"v_mfma_f32_16x16x32_bf16 a[128:131],v[224:227],v[208:211],a[128:131]\n"
"buffer_load_dwordx4 v108,s[16:19],s76 offen offset:2048 lds\n"
"v_mfma_f32_16x16x32_bf16 a[132:135],v[228:231],v[208:211],a[132:135]\n"
"v_mfma_f32_16x16x32_bf16 a[132:135],v[164:167],v[144:147],a[132:135]\n"
"v_mfma_f32_16x16x32_bf16 a[136:139],v[168:171],v[144:147],a[136:139]\n"
"v_mfma_f32_16x16x32_bf16 a[136:139],v[232:235],v[208:211],a[136:139]\n"
"v_mfma_f32_16x16x32_bf16 a[140:143],v[236:239],v[208:211],a[140:143]\n"
"buffer_load_dwordx4 v109,s[16:19],s76 offen offset:3072 lds\n"
"v_mfma_f32_16x16x32_bf16 a[140:143],v[172:175],v[144:147],a[140:143]\n"
"v_mfma_f32_16x16x32_bf16 a[144:147],v[176:179],v[144:147],a[144:147]\n"
"v_mfma_f32_16x16x32_bf16 a[144:147],v[240:243],v[208:211],a[144:147]\n"
"v_mfma_f32_16x16x32_bf16 a[148:151],v[244:247],v[208:211],a[148:151]\n"
"s_mov_b32 s76,262912\n"
"s_add_u32 m0,s79,4096\n"
"v_mfma_f32_16x16x32_bf16 a[148:151],v[180:183],v[144:147],a[148:151]\n"
"buffer_load_dwordx4 v106,s[16:19],s76 offen offset:0 lds\n"
"v_mfma_f32_16x16x32_bf16 a[152:155],v[184:187],v[144:147],a[152:155]\n"
"v_mfma_f32_16x16x32_bf16 a[152:155],v[248:251],v[208:211],a[152:155]\n"
"v_mfma_f32_16x16x32_bf16 a[156:159],v[252:255],v[208:211],a[156:159]\n"
"v_mfma_f32_16x16x32_bf16 a[156:159],v[188:191],v[144:147],a[156:159]\n"
"v_mfma_f32_16x16x32_bf16 a[160:163],v[160:163],v[148:151],a[160:163]\n"
"buffer_load_dwordx4 v107,s[16:19],s76 offen offset:1024 lds\n"
"v_mfma_f32_16x16x32_bf16 a[160:163],v[224:227],v[212:215],a[160:163]\n"
"v_mfma_f32_16x16x32_bf16 a[164:167],v[228:231],v[212:215],a[164:167]\n"
"v_mfma_f32_16x16x32_bf16 a[164:167],v[164:167],v[148:151],a[164:167]\n"
"v_mfma_f32_16x16x32_bf16 a[168:171],v[168:171],v[148:151],a[168:171]\n"
"v_mfma_f32_16x16x32_bf16 a[168:171],v[232:235],v[212:215],a[168:171]\n"
"buffer_load_dwordx4 v108,s[16:19],s76 offen offset:2048 lds\n"
"v_mfma_f32_16x16x32_bf16 a[172:175],v[236:239],v[212:215],a[172:175]\n"
"v_mfma_f32_16x16x32_bf16 a[172:175],v[172:175],v[148:151],a[172:175]\n"
"v_mfma_f32_16x16x32_bf16 a[176:179],v[176:179],v[148:151],a[176:179]\n"
"v_mfma_f32_16x16x32_bf16 a[176:179],v[240:243],v[212:215],a[176:179]\n"
"v_mfma_f32_16x16x32_bf16 a[180:183],v[244:247],v[212:215],a[180:183]\n"
"buffer_load_dwordx4 v109,s[16:19],s76 offen offset:3072 lds\n"
"v_mfma_f32_16x16x32_bf16 a[180:183],v[180:183],v[148:151],a[180:183]\n"
"s_mov_b32 s87,3145728\n"
"v_mfma_f32_16x16x32_bf16 a[184:187],v[184:187],v[148:151],a[184:187]\n"
"buffer_load_dword v115,v112,s[40:43],s87 offen\n"
"v_mfma_f32_16x16x32_bf16 a[184:187],v[248:251],v[212:215],a[184:187]\n"
"s_mov_b32 s87,4194304\n"
"v_mfma_f32_16x16x32_bf16 a[188:191],v[252:255],v[212:215],a[188:191]\n"
"buffer_load_dword v115,v112,s[40:43],s87 offen\n"
"v_mfma_f32_16x16x32_bf16 a[188:191],v[188:191],v[148:151],a[188:191]\n"
"s_mov_b32 s87,5242880\n"
"s_waitcnt vmcnt(33)\n"
"s_barrier\n"
"v_mfma_f32_16x16x32_bf16 a[192:195],v[224:227],v[216:219],a[192:195]\n"
"ds_read_b128 v[152:155],v80 offset:45056\n"
"buffer_load_dword v115,v112,s[40:43],s87 offen\n"
"v_mfma_f32_16x16x32_bf16 a[224:227],v[224:227],v[220:223],a[224:227]\n"
"ds_read_b128 v[156:159],v80 offset:47104\n"
"v_mfma_f32_16x16x32_bf16 a[228:231],v[228:231],v[220:223],a[228:231]\n"
"ds_read_b64_tr_b16 v[160:161],v98 offset:49152\n"
"v_mfma_f32_16x16x32_bf16 a[196:199],v[228:231],v[216:219],a[196:199]\n"
"ds_read_b64_tr_b16 v[162:163],v98 offset:50176\n"
"v_mfma_f32_16x16x32_bf16 a[200:203],v[232:235],v[216:219],a[200:203]\n"
"ds_read_b64_tr_b16 v[164:165],v99 offset:49152\n"
"v_mfma_f32_16x16x32_bf16 a[232:235],v[232:235],v[220:223],a[232:235]\n"
"ds_read_b64_tr_b16 v[166:167],v99 offset:50176\n"
"v_mfma_f32_16x16x32_bf16 a[236:239],v[236:239],v[220:223],a[236:239]\n"
"ds_read_b64_tr_b16 v[168:169],v100 offset:49152\n"
"v_mfma_f32_16x16x32_bf16 a[204:207],v[236:239],v[216:219],a[204:207]\n"
"ds_read_b64_tr_b16 v[170:171],v100 offset:50176\n"
"v_mfma_f32_16x16x32_bf16 a[208:211],v[240:243],v[216:219],a[208:211]\n"
"ds_read_b64_tr_b16 v[172:173],v101 offset:49152\n"
"v_mfma_f32_16x16x32_bf16 a[240:243],v[240:243],v[220:223],a[240:243]\n"
"ds_read_b64_tr_b16 v[174:175],v101 offset:50176\n"
"v_mfma_f32_16x16x32_bf16 a[244:247],v[244:247],v[220:223],a[244:247]\n"
"ds_read_b64_tr_b16 v[176:177],v102 offset:49152\n"
"v_mfma_f32_16x16x32_bf16 a[212:215],v[244:247],v[216:219],a[212:215]\n"
"ds_read_b64_tr_b16 v[178:179],v102 offset:50176\n"
"v_mfma_f32_16x16x32_bf16 a[216:219],v[248:251],v[216:219],a[216:219]\n"
"ds_read_b64_tr_b16 v[180:181],v103 offset:49152\n"
"v_mfma_f32_16x16x32_bf16 a[248:251],v[248:251],v[220:223],a[248:251]\n"
"ds_read_b64_tr_b16 v[182:183],v103 offset:50176\n"
"v_mfma_f32_16x16x32_bf16 a[252:255],v[252:255],v[220:223],a[252:255]\n"
"ds_read_b64_tr_b16 v[184:185],v104 offset:49152\n"
"ds_read_b64_tr_b16 v[186:187],v104 offset:50176\n"
"v_mfma_f32_16x16x32_bf16 a[220:223],v[252:255],v[216:219],a[220:223]\n"
"s_waitcnt vmcnt(25)\n"
"s_waitcnt lgkmcnt(10)\n"
"v_mfma_f32_16x16x32_bf16 a[192:195],v[160:163],v[152:155],a[192:195]\n"
"ds_read_b64_tr_b16 v[188:189],v105 offset:49152\n"
"ds_read_b64_tr_b16 v[190:191],v105 offset:50176\n"
"v_mfma_f32_16x16x32_bf16 a[224:227],v[160:163],v[156:159],a[224:227]\n"
"buffer_store_dwordx2 v[124:125],off,s[24:27],s57 sc1\n"
"v_mfma_f32_16x16x32_bf16 a[228:231],v[164:167],v[156:159],a[228:231]\n"
"ds_read_b128 v[128:131],v80 offset:32768\n"
"v_mfma_f32_16x16x32_bf16 a[196:199],v[164:167],v[152:155],a[196:199]\n"
"ds_read_b128 v[192:195],v81 offset:32768\n"
"s_waitcnt lgkmcnt(10)\n"
"v_mfma_f32_16x16x32_bf16 a[200:203],v[168:171],v[152:155],a[200:203]\n"
"ds_read_b64_tr_b16 v[224:225],v98 offset:32768\n"
"v_mfma_f32_16x16x32_bf16 a[232:235],v[168:171],v[156:159],a[232:235]\n"
"ds_read_b64_tr_b16 v[226:227],v98 offset:33792\n"
"v_mfma_f32_16x16x32_bf16 a[236:239],v[172:175],v[156:159],a[236:239]\n"
"ds_read_b64_tr_b16 v[228:229],v99 offset:32768\n"
"v_mfma_f32_16x16x32_bf16 a[204:207],v[172:175],v[152:155],a[204:207]\n"
"ds_read_b64_tr_b16 v[230:231],v99 offset:33792\n"
"s_waitcnt lgkmcnt(10)\n"
"v_mfma_f32_16x16x32_bf16 a[208:211],v[176:179],v[152:155],a[208:211]\n"
"ds_read_b64_tr_b16 v[232:233],v100 offset:32768\n"
"v_mfma_f32_16x16x32_bf16 a[240:243],v[176:179],v[156:159],a[240:243]\n"
"ds_read_b64_tr_b16 v[234:235],v100 offset:33792\n"
"v_mfma_f32_16x16x32_bf16 a[244:247],v[180:183],v[156:159],a[244:247]\n"
"ds_read_b64_tr_b16 v[236:237],v101 offset:32768\n"
"v_mfma_f32_16x16x32_bf16 a[212:215],v[180:183],v[152:155],a[212:215]\n"
"ds_read_b64_tr_b16 v[238:239],v101 offset:33792\n"
"s_waitcnt lgkmcnt(10)\n"
"v_mfma_f32_16x16x32_bf16 a[216:219],v[184:187],v[152:155],a[216:219]\n"
"ds_read_b64_tr_b16 v[240:241],v102 offset:32768\n"
"v_mfma_f32_16x16x32_bf16 a[248:251],v[184:187],v[156:159],a[248:251]\n"
"ds_read_b64_tr_b16 v[242:243],v102 offset:33792\n"
"v_mfma_f32_16x16x32_bf16 a[252:255],v[188:191],v[156:159],a[252:255]\n"
"ds_read_b64_tr_b16 v[244:245],v103 offset:32768\n"
"v_mfma_f32_16x16x32_bf16 a[220:223],v[188:191],v[152:155],a[220:223]\n"
"ds_read_b64_tr_b16 v[246:247],v103 offset:33792\n"
"s_waitcnt lgkmcnt(8)\n"
"v_mfma_f32_16x16x32_bf16 a[0:3],v[160:163],v[128:131],a[0:3]\n"
"ds_read_b64_tr_b16 v[248:249],v104 offset:32768\n"
"v_mfma_f32_16x16x32_bf16 a[0:3],v[224:227],v[192:195],a[0:3]\n"
"ds_read_b64_tr_b16 v[250:251],v104 offset:33792\n"
"v_mfma_f32_16x16x32_bf16 a[4:7],v[228:231],v[192:195],a[4:7]\n"
"ds_read_b64_tr_b16 v[252:253],v105 offset:32768\n"
"v_mfma_f32_16x16x32_bf16 a[4:7],v[164:167],v[128:131],a[4:7]\n"
"ds_read_b64_tr_b16 v[254:255],v105 offset:33792\n"
"s_waitcnt lgkmcnt(8)\n"
"v_mfma_f32_16x16x32_bf16 a[8:11],v[168:171],v[128:131],a[8:11]\n"
"ds_read_b128 v[132:135],v80 offset:34816\n"
"v_mfma_f32_16x16x32_bf16 a[8:11],v[232:235],v[192:195],a[8:11]\n"
"ds_read_b128 v[196:199],v81 offset:34816\n"
"v_mfma_f32_16x16x32_bf16 a[12:15],v[236:239],v[192:195],a[12:15]\n"
"ds_read_b128 v[136:139],v80 offset:36864\n"
"v_mfma_f32_16x16x32_bf16 a[12:15],v[172:175],v[128:131],a[12:15]\n"
"ds_read_b128 v[200:203],v81 offset:36864\n"
"s_waitcnt lgkmcnt(8)\n"
"v_mfma_f32_16x16x32_bf16 a[16:19],v[176:179],v[128:131],a[16:19]\n"
"ds_read_b128 v[140:143],v80 offset:38912\n"
"v_mfma_f32_16x16x32_bf16 a[16:19],v[240:243],v[192:195],a[16:19]\n"
"ds_read_b128 v[204:207],v81 offset:38912\n"
"v_mfma_f32_16x16x32_bf16 a[20:23],v[244:247],v[192:195],a[20:23]\n"
"ds_read_b128 v[144:147],v80 offset:40960\n"
"v_mfma_f32_16x16x32_bf16 a[20:23],v[180:183],v[128:131],a[20:23]\n"
"ds_read_b128 v[208:211],v81 offset:40960\n"
"s_waitcnt lgkmcnt(8)\n"
"v_mfma_f32_16x16x32_bf16 a[24:27],v[184:187],v[128:131],a[24:27]\n"
"ds_read_b128 v[148:151],v80 offset:43008\n"
"v_mfma_f32_16x16x32_bf16 a[24:27],v[248:251],v[192:195],a[24:27]\n"
"ds_read_b128 v[212:215],v81 offset:43008\n"
"v_mfma_f32_16x16x32_bf16 a[28:31],v[252:255],v[192:195],a[28:31]\n"
"ds_read_b128 v[216:219],v81 offset:45056\n"
"v_mfma_f32_16x16x32_bf16 a[28:31],v[188:191],v[128:131],a[28:31]\n"
"ds_read_b128 v[220:223],v81 offset:47104\n"
"s_waitcnt lgkmcnt(10)\n"
"v_mfma_f32_16x16x32_bf16 a[32:35],v[160:163],v[132:135],a[32:35]\n"
"v_mfma_f32_16x16x32_bf16 a[32:35],v[224:227],v[196:199],a[32:35]\n"
"v_mfma_f32_16x16x32_bf16 a[36:39],v[228:231],v[196:199],a[36:39]\n"
"v_mfma_f32_16x16x32_bf16 a[36:39],v[164:167],v[132:135],a[36:39]\n"
"v_mfma_f32_16x16x32_bf16 a[40:43],v[168:171],v[132:135],a[40:43]\n"
"s_waitcnt lgkmcnt(0)\n"
"s_barrier\n"
"v_mfma_f32_16x16x32_bf16 a[40:43],v[232:235],v[196:199],a[40:43]\n"
"s_mov_b32 s77,3145728\n"
"s_add_u32 m0,s78,147456\n"
"v_mfma_f32_16x16x32_bf16 a[44:47],v[236:239],v[196:199],a[44:47]\n"
"buffer_load_dwordx4 v110,s[20:23],s77 offen offset:0 lds\n"
"v_mfma_f32_16x16x32_bf16 a[44:47],v[172:175],v[132:135],a[44:47]\n"
"v_mfma_f32_16x16x32_bf16 a[48:51],v[176:179],v[132:135],a[48:51]\n"
"v_mfma_f32_16x16x32_bf16 a[48:51],v[240:243],v[196:199],a[48:51]\n"
"v_mfma_f32_16x16x32_bf16 a[52:55],v[244:247],v[196:199],a[52:55]\n"
"buffer_load_dwordx4 v111,s[20:23],s77 offen offset:1024 lds\n"
"v_mfma_f32_16x16x32_bf16 a[52:55],v[180:183],v[132:135],a[52:55]\n"
"v_mfma_f32_16x16x32_bf16 a[56:59],v[184:187],v[132:135],a[56:59]\n"
"v_mfma_f32_16x16x32_bf16 a[56:59],v[248:251],v[196:199],a[56:59]\n"
"v_mfma_f32_16x16x32_bf16 a[60:63],v[252:255],v[196:199],a[60:63]\n"
"buffer_load_dwordx4 v113,s[20:23],s77 offen offset:2048 lds\n"
"v_mfma_f32_16x16x32_bf16 a[60:63],v[188:191],v[132:135],a[60:63]\n"
"v_mfma_f32_16x16x32_bf16 a[64:67],v[160:163],v[136:139],a[64:67]\n"
"v_mfma_f32_16x16x32_bf16 a[64:67],v[224:227],v[200:203],a[64:67]\n"
"v_mfma_f32_16x16x32_bf16 a[68:71],v[228:231],v[200:203],a[68:71]\n"
"buffer_load_dwordx4 v114,s[20:23],s77 offen offset:3072 lds\n"
"v_mfma_f32_16x16x32_bf16 a[68:71],v[164:167],v[136:139],a[68:71]\n"
"v_mfma_f32_16x16x32_bf16 a[72:75],v[168:171],v[136:139],a[72:75]\n"
"v_mfma_f32_16x16x32_bf16 a[72:75],v[232:235],v[200:203],a[72:75]\n"
"s_mov_b32 s77,3407872\n"
"s_add_u32 m0,s78,131072\n"
"v_mfma_f32_16x16x32_bf16 a[76:79],v[236:239],v[200:203],a[76:79]\n"
"buffer_load_dwordx4 v110,s[20:23],s77 offen offset:0 lds\n"
"v_mfma_f32_16x16x32_bf16 a[76:79],v[172:175],v[136:139],a[76:79]\n"
"v_mfma_f32_16x16x32_bf16 a[80:83],v[176:179],v[136:139],a[80:83]\n"
"v_mfma_f32_16x16x32_bf16 a[80:83],v[240:243],v[200:203],a[80:83]\n"
"v_mfma_f32_16x16x32_bf16 a[84:87],v[244:247],v[200:203],a[84:87]\n"
"buffer_load_dwordx4 v111,s[20:23],s77 offen offset:1024 lds\n"
"v_mfma_f32_16x16x32_bf16 a[84:87],v[180:183],v[136:139],a[84:87]\n"
"v_mfma_f32_16x16x32_bf16 a[88:91],v[184:187],v[136:139],a[88:91]\n"
"v_mfma_f32_16x16x32_bf16 a[88:91],v[248:251],v[200:203],a[88:91]\n"
"v_mfma_f32_16x16x32_bf16 a[92:95],v[252:255],v[200:203],a[92:95]\n"
"buffer_load_dwordx4 v113,s[20:23],s77 offen offset:2048 lds\n"
"v_mfma_f32_16x16x32_bf16 a[92:95],v[188:191],v[136:139],a[92:95]\n"
"v_mfma_f32_16x16x32_bf16 a[96:99],v[160:163],v[140:143],a[96:99]\n"
"v_mfma_f32_16x16x32_bf16 a[96:99],v[224:227],v[204:207],a[96:99]\n"
"v_mfma_f32_16x16x32_bf16 a[100:103],v[228:231],v[204:207],a[100:103]\n"
"buffer_load_dwordx4 v114,s[20:23],s77 offen offset:3072 lds\n"
"v_mfma_f32_16x16x32_bf16 a[100:103],v[164:167],v[140:143],a[100:103]\n"
"v_mfma_f32_16x16x32_bf16 a[104:107],v[168:171],v[140:143],a[104:107]\n"
"v_mfma_f32_16x16x32_bf16 a[104:107],v[232:235],v[204:207],a[104:107]\n"
"v_mfma_f32_16x16x32_bf16 a[108:111],v[236:239],v[204:207],a[108:111]\n"
"s_mov_b32 s76,896\n"
"s_add_u32 m0,s79,32768\n"
"v_mfma_f32_16x16x32_bf16 a[108:111],v[172:175],v[140:143],a[108:111]\n"
"buffer_load_dwordx4 v106,s[16:19],s76 offen offset:0 lds\n"
"v_mfma_f32_16x16x32_bf16 a[112:115],v[176:179],v[140:143],a[112:115]\n"
"v_mfma_f32_16x16x32_bf16 a[112:115],v[240:243],v[204:207],a[112:115]\n"
"v_mfma_f32_16x16x32_bf16 a[116:119],v[244:247],v[204:207],a[116:119]\n"
"v_mfma_f32_16x16x32_bf16 a[116:119],v[180:183],v[140:143],a[116:119]\n"
"v_mfma_f32_16x16x32_bf16 a[120:123],v[184:187],v[140:143],a[120:123]\n"
"buffer_load_dwordx4 v107,s[16:19],s76 offen offset:1024 lds\n"
"v_mfma_f32_16x16x32_bf16 a[120:123],v[248:251],v[204:207],a[120:123]\n"
"v_mfma_f32_16x16x32_bf16 a[124:127],v[252:255],v[204:207],a[124:127]\n"
"v_mfma_f32_16x16x32_bf16 a[124:127],v[188:191],v[140:143],a[124:127]\n"
"v_mfma_f32_16x16x32_bf16 a[128:131],v[160:163],v[144:147],a[128:131]\n"
"v_mfma_f32_16x16x32_bf16 a[128:131],v[224:227],v[208:211],a[128:131]\n"
"buffer_load_dwordx4 v108,s[16:19],s76 offen offset:2048 lds\n"
"v_mfma_f32_16x16x32_bf16 a[132:135],v[228:231],v[208:211],a[132:135]\n"
"v_mfma_f32_16x16x32_bf16 a[132:135],v[164:167],v[144:147],a[132:135]\n"
"v_mfma_f32_16x16x32_bf16 a[136:139],v[168:171],v[144:147],a[136:139]\n"
"v_mfma_f32_16x16x32_bf16 a[136:139],v[232:235],v[208:211],a[136:139]\n"
"v_mfma_f32_16x16x32_bf16 a[140:143],v[236:239],v[208:211],a[140:143]\n"
"buffer_load_dwordx4 v109,s[16:19],s76 offen offset:3072 lds\n"
"v_mfma_f32_16x16x32_bf16 a[140:143],v[172:175],v[144:147],a[140:143]\n"
"v_mfma_f32_16x16x32_bf16 a[144:147],v[176:179],v[144:147],a[144:147]\n"
"v_mfma_f32_16x16x32_bf16 a[144:147],v[240:243],v[208:211],a[144:147]\n"
"v_mfma_f32_16x16x32_bf16 a[148:151],v[244:247],v[208:211],a[148:151]\n"
"s_mov_b32 s76,263040\n"
"s_add_u32 m0,s79,36864\n"
"v_mfma_f32_16x16x32_bf16 a[148:151],v[180:183],v[144:147],a[148:151]\n"
"buffer_load_dwordx4 v106,s[16:19],s76 offen offset:0 lds\n"
"v_mfma_f32_16x16x32_bf16 a[152:155],v[184:187],v[144:147],a[152:155]\n"
"v_mfma_f32_16x16x32_bf16 a[152:155],v[248:251],v[208:211],a[152:155]\n"
"v_mfma_f32_16x16x32_bf16 a[156:159],v[252:255],v[208:211],a[156:159]\n"
"v_mfma_f32_16x16x32_bf16 a[156:159],v[188:191],v[144:147],a[156:159]\n"
"v_mfma_f32_16x16x32_bf16 a[160:163],v[160:163],v[148:151],a[160:163]\n"
"buffer_load_dwordx4 v107,s[16:19],s76 offen offset:1024 lds\n"
"v_mfma_f32_16x16x32_bf16 a[160:163],v[224:227],v[212:215],a[160:163]\n"
"v_mfma_f32_16x16x32_bf16 a[164:167],v[228:231],v[212:215],a[164:167]\n"
"v_mfma_f32_16x16x32_bf16 a[164:167],v[164:167],v[148:151],a[164:167]\n"
"v_mfma_f32_16x16x32_bf16 a[168:171],v[168:171],v[148:151],a[168:171]\n"
"v_mfma_f32_16x16x32_bf16 a[168:171],v[232:235],v[212:215],a[168:171]\n"
"buffer_load_dwordx4 v108,s[16:19],s76 offen offset:2048 lds\n"
"v_mfma_f32_16x16x32_bf16 a[172:175],v[236:239],v[212:215],a[172:175]\n"
"v_mfma_f32_16x16x32_bf16 a[172:175],v[172:175],v[148:151],a[172:175]\n"
"v_mfma_f32_16x16x32_bf16 a[176:179],v[176:179],v[148:151],a[176:179]\n"
"v_mfma_f32_16x16x32_bf16 a[176:179],v[240:243],v[212:215],a[176:179]\n"
"v_mfma_f32_16x16x32_bf16 a[180:183],v[244:247],v[212:215],a[180:183]\n"
"buffer_load_dwordx4 v109,s[16:19],s76 offen offset:3072 lds\n"
"v_mfma_f32_16x16x32_bf16 a[180:183],v[180:183],v[148:151],a[180:183]\n"
"s_mov_b32 s87,6291456\n"
"v_mfma_f32_16x16x32_bf16 a[184:187],v[184:187],v[148:151],a[184:187]\n"
"buffer_load_dword v115,v112,s[40:43],s87 offen\n"
"v_mfma_f32_16x16x32_bf16 a[184:187],v[248:251],v[212:215],a[184:187]\n"
"s_mov_b32 s87,7340032\n"
"v_mfma_f32_16x16x32_bf16 a[188:191],v[252:255],v[212:215],a[188:191]\n"
"buffer_load_dword v115,v112,s[40:43],s87 offen\n"
"v_mfma_f32_16x16x32_bf16 a[188:191],v[188:191],v[148:151],a[188:191]\n"
"s_waitcnt vmcnt(30)\n"
"s_barrier\n"
"v_mfma_f32_16x16x32_bf16 a[192:195],v[224:227],v[216:219],a[192:195]\n"
"ds_read_b128 v[152:155],v82 offset:12288\n"
"v_mfma_f32_16x16x32_bf16 a[224:227],v[224:227],v[220:223],a[224:227]\n"
"ds_read_b128 v[156:159],v82 offset:14336\n"
"v_mfma_f32_16x16x32_bf16 a[228:231],v[228:231],v[220:223],a[228:231]\n"
"ds_read_b64_tr_b16 v[160:161],v98 offset:0\n"
"v_mfma_f32_16x16x32_bf16 a[196:199],v[228:231],v[216:219],a[196:199]\n"
"ds_read_b64_tr_b16 v[162:163],v98 offset:1024\n"
"v_mfma_f32_16x16x32_bf16 a[200:203],v[232:235],v[216:219],a[200:203]\n"
"ds_read_b64_tr_b16 v[164:165],v99 offset:0\n"
"v_mfma_f32_16x16x32_bf16 a[232:235],v[232:235],v[220:223],a[232:235]\n"
"ds_read_b64_tr_b16 v[166:167],v99 offset:1024\n"
"v_mfma_f32_16x16x32_bf16 a[236:239],v[236:239],v[220:223],a[236:239]\n"
"ds_read_b64_tr_b16 v[168:169],v100 offset:0\n"
"v_mfma_f32_16x16x32_bf16 a[204:207],v[236:239],v[216:219],a[204:207]\n"
"ds_read_b64_tr_b16 v[170:171],v100 offset:1024\n"
"v_mfma_f32_16x16x32_bf16 a[208:211],v[240:243],v[216:219],a[208:211]\n"
"ds_read_b64_tr_b16 v[172:173],v101 offset:0\n"
"v_mfma_f32_16x16x32_bf16 a[240:243],v[240:243],v[220:223],a[240:243]\n"
"ds_read_b64_tr_b16 v[174:175],v101 offset:1024\n"
"v_mfma_f32_16x16x32_bf16 a[244:247],v[244:247],v[220:223],a[244:247]\n"
"ds_read_b64_tr_b16 v[176:177],v102 offset:0\n"
"v_mfma_f32_16x16x32_bf16 a[212:215],v[244:247],v[216:219],a[212:215]\n"
"ds_read_b64_tr_b16 v[178:179],v102 offset:1024\n"
"v_mfma_f32_16x16x32_bf16 a[216:219],v[248:251],v[216:219],a[216:219]\n"
"ds_read_b64_tr_b16 v[180:181],v103 offset:0\n"
"v_mfma_f32_16x16x32_bf16 a[248:251],v[248:251],v[220:223],a[248:251]\n"
"ds_read_b64_tr_b16 v[182:183],v103 offset:1024\n"
"v_mfma_f32_16x16x32_bf16 a[252:255],v[252:255],v[220:223],a[252:255]\n"
"ds_read_b64_tr_b16 v[184:185],v104 offset:0\n"
"v_mfma_f32_16x16x32_bf16 a[220:223],v[252:255],v[216:219],a[220:223]\n"
"ds_read_b64_tr_b16 v[186:187],v104 offset:1024\n"
"s_waitcnt lgkmcnt(10)\n"
"v_mfma_f32_16x16x32_bf16 a[192:195],v[160:163],v[152:155],a[192:195]\n"
"ds_read_b64_tr_b16 v[188:189],v105 offset:0\n"
"v_mfma_f32_16x16x32_bf16 a[224:227],v[160:163],v[156:159],a[224:227]\n"
"ds_read_b64_tr_b16 v[190:191],v105 offset:1024\n"
"v_mfma_f32_16x16x32_bf16 a[228:231],v[164:167],v[156:159],a[228:231]\n"
"ds_read_b128 v[128:131],v82 offset:0\n"
"v_mfma_f32_16x16x32_bf16 a[196:199],v[164:167],v[152:155],a[196:199]\n"
"ds_read_b128 v[192:195],v83 offset:0\n"
"s_waitcnt lgkmcnt(10)\n"
"v_mfma_f32_16x16x32_bf16 a[200:203],v[168:171],v[152:155],a[200:203]\n"
"ds_read_b64_tr_b16 v[224:225],v98 offset:16384\n"
"v_mfma_f32_16x16x32_bf16 a[232:235],v[168:171],v[156:159],a[232:235]\n"
"ds_read_b64_tr_b16 v[226:227],v98 offset:17408\n"
"v_mfma_f32_16x16x32_bf16 a[236:239],v[172:175],v[156:159],a[236:239]\n"
"ds_read_b64_tr_b16 v[228:229],v99 offset:16384\n"
"v_mfma_f32_16x16x32_bf16 a[204:207],v[172:175],v[152:155],a[204:207]\n"
"ds_read_b64_tr_b16 v[230:231],v99 offset:17408\n"
"s_waitcnt lgkmcnt(10)\n"
"v_mfma_f32_16x16x32_bf16 a[208:211],v[176:179],v[152:155],a[208:211]\n"
"ds_read_b64_tr_b16 v[232:233],v100 offset:16384\n"
"v_mfma_f32_16x16x32_bf16 a[240:243],v[176:179],v[156:159],a[240:243]\n"
"ds_read_b64_tr_b16 v[234:235],v100 offset:17408\n"
"v_mfma_f32_16x16x32_bf16 a[244:247],v[180:183],v[156:159],a[244:247]\n"
"ds_read_b64_tr_b16 v[236:237],v101 offset:16384\n"
"v_mfma_f32_16x16x32_bf16 a[212:215],v[180:183],v[152:155],a[212:215]\n"
"ds_read_b64_tr_b16 v[238:239],v101 offset:17408\n"
"s_waitcnt lgkmcnt(10)\n"
"v_mfma_f32_16x16x32_bf16 a[216:219],v[184:187],v[152:155],a[216:219]\n"
"ds_read_b64_tr_b16 v[240:241],v102 offset:16384\n"
"v_mfma_f32_16x16x32_bf16 a[248:251],v[184:187],v[156:159],a[248:251]\n"
"ds_read_b64_tr_b16 v[242:243],v102 offset:17408\n"
"v_mfma_f32_16x16x32_bf16 a[252:255],v[188:191],v[156:159],a[252:255]\n"
"ds_read_b64_tr_b16 v[244:245],v103 offset:16384\n"
"v_mfma_f32_16x16x32_bf16 a[220:223],v[188:191],v[152:155],a[220:223]\n"
"ds_read_b64_tr_b16 v[246:247],v103 offset:17408\n"
"s_waitcnt lgkmcnt(8)\n"
"v_mfma_f32_16x16x32_bf16 a[0:3],v[160:163],v[128:131],a[0:3]\n"
"ds_read_b64_tr_b16 v[248:249],v104 offset:16384\n"
"v_mfma_f32_16x16x32_bf16 a[0:3],v[224:227],v[192:195],a[0:3]\n"
"ds_read_b64_tr_b16 v[250:251],v104 offset:17408\n"
"v_mfma_f32_16x16x32_bf16 a[4:7],v[228:231],v[192:195],a[4:7]\n"
"ds_read_b64_tr_b16 v[252:253],v105 offset:16384\n"
"v_mfma_f32_16x16x32_bf16 a[4:7],v[164:167],v[128:131],a[4:7]\n"
"ds_read_b64_tr_b16 v[254:255],v105 offset:17408\n"
"s_waitcnt lgkmcnt(8)\n"
"v_mfma_f32_16x16x32_bf16 a[8:11],v[168:171],v[128:131],a[8:11]\n"
"ds_read_b128 v[132:135],v82 offset:2048\n"
"v_mfma_f32_16x16x32_bf16 a[8:11],v[232:235],v[192:195],a[8:11]\n"
"ds_read_b128 v[196:199],v83 offset:2048\n"
"v_mfma_f32_16x16x32_bf16 a[12:15],v[236:239],v[192:195],a[12:15]\n"
"ds_read_b128 v[136:139],v82 offset:4096\n"
"v_mfma_f32_16x16x32_bf16 a[12:15],v[172:175],v[128:131],a[12:15]\n"
"ds_read_b128 v[200:203],v83 offset:4096\n"
"s_waitcnt lgkmcnt(8)\n"
"v_mfma_f32_16x16x32_bf16 a[16:19],v[176:179],v[128:131],a[16:19]\n"
"ds_read_b128 v[140:143],v82 offset:6144\n"
"v_mfma_f32_16x16x32_bf16 a[16:19],v[240:243],v[192:195],a[16:19]\n"
"ds_read_b128 v[204:207],v83 offset:6144\n"
"v_mfma_f32_16x16x32_bf16 a[20:23],v[244:247],v[192:195],a[20:23]\n"
"ds_read_b128 v[144:147],v82 offset:8192\n"
"v_mfma_f32_16x16x32_bf16 a[20:23],v[180:183],v[128:131],a[20:23]\n"
"ds_read_b128 v[208:211],v83 offset:8192\n"
"s_waitcnt lgkmcnt(8)\n"
"v_mfma_f32_16x16x32_bf16 a[24:27],v[184:187],v[128:131],a[24:27]\n"
"ds_read_b128 v[148:151],v82 offset:10240\n"
"v_mfma_f32_16x16x32_bf16 a[24:27],v[248:251],v[192:195],a[24:27]\n"
"ds_read_b128 v[212:215],v83 offset:10240\n"
"v_mfma_f32_16x16x32_bf16 a[28:31],v[252:255],v[192:195],a[28:31]\n"
"ds_read_b128 v[216:219],v83 offset:12288\n"
"v_mfma_f32_16x16x32_bf16 a[28:31],v[188:191],v[128:131],a[28:31]\n"
"ds_read_b128 v[220:223],v83 offset:14336\n"
"s_waitcnt lgkmcnt(10)\n"
"v_mfma_f32_16x16x32_bf16 a[32:35],v[160:163],v[132:135],a[32:35]\n"
"v_mfma_f32_16x16x32_bf16 a[32:35],v[224:227],v[196:199],a[32:35]\n"
"buffer_load_dwordx4 v[74:77],v112,s[24:27],s58 offen sc1\n"
"v_mfma_f32_16x16x32_bf16 a[36:39],v[228:231],v[196:199],a[36:39]\n"
"v_mfma_f32_16x16x32_bf16 a[36:39],v[164:167],v[132:135],a[36:39]\n"
"v_mfma_f32_16x16x32_bf16 a[40:43],v[168:171],v[132:135],a[40:43]\n"
"s_waitcnt lgkmcnt(0)\n"
"s_barrier\n"
"v_mfma_f32_16x16x32_bf16 a[40:43],v[232:235],v[196:199],a[40:43]\n"
"s_mov_b32 s77,3670016\n"
"s_add_u32 m0,s78,98304\n"
"v_mfma_f32_16x16x32_bf16 a[44:47],v[236:239],v[196:199],a[44:47]\n"
"buffer_load_dwordx4 v110,s[20:23],s77 offen offset:0 lds\n"
"v_mfma_f32_16x16x32_bf16 a[44:47],v[172:175],v[132:135],a[44:47]\n"
"v_mfma_f32_16x16x32_bf16 a[48:51],v[176:179],v[132:135],a[48:51]\n"
"s_load_dwordx2 s[48:49],s[0:1],56\n"
"v_mfma_f32_16x16x32_bf16 a[48:51],v[240:243],v[196:199],a[48:51]\n"
"v_mfma_f32_16x16x32_bf16 a[52:55],v[244:247],v[196:199],a[52:55]\n"
"buffer_load_dwordx4 v111,s[20:23],s77 offen offset:1024 lds\n"
"v_mfma_f32_16x16x32_bf16 a[52:55],v[180:183],v[132:135],a[52:55]\n"
"v_mfma_f32_16x16x32_bf16 a[56:59],v[184:187],v[132:135],a[56:59]\n"
"v_mfma_f32_16x16x32_bf16 a[56:59],v[248:251],v[196:199],a[56:59]\n"
"v_mfma_f32_16x16x32_bf16 a[60:63],v[252:255],v[196:199],a[60:63]\n"
"buffer_load_dwordx4 v113,s[20:23],s77 offen offset:2048 lds\n"
"v_mfma_f32_16x16x32_bf16 a[60:63],v[188:191],v[132:135],a[60:63]\n"
"v_mfma_f32_16x16x32_bf16 a[64:67],v[160:163],v[136:139],a[64:67]\n"
"v_mfma_f32_16x16x32_bf16 a[64:67],v[224:227],v[200:203],a[64:67]\n"
"v_mfma_f32_16x16x32_bf16 a[68:71],v[228:231],v[200:203],a[68:71]\n"
"buffer_load_dwordx4 v114,s[20:23],s77 offen offset:3072 lds\n"
"v_mfma_f32_16x16x32_bf16 a[68:71],v[164:167],v[136:139],a[68:71]\n"
"v_mfma_f32_16x16x32_bf16 a[72:75],v[168:171],v[136:139],a[72:75]\n"
"v_mfma_f32_16x16x32_bf16 a[72:75],v[232:235],v[200:203],a[72:75]\n"
"s_mov_b32 s77,3932160\n"
"s_add_u32 m0,s78,114688\n"
"v_mfma_f32_16x16x32_bf16 a[76:79],v[236:239],v[200:203],a[76:79]\n"
"buffer_load_dwordx4 v110,s[20:23],s77 offen offset:0 lds\n"
"v_mfma_f32_16x16x32_bf16 a[76:79],v[172:175],v[136:139],a[76:79]\n"
"v_mfma_f32_16x16x32_bf16 a[80:83],v[176:179],v[136:139],a[80:83]\n"
"v_mfma_f32_16x16x32_bf16 a[80:83],v[240:243],v[200:203],a[80:83]\n"
"v_mfma_f32_16x16x32_bf16 a[84:87],v[244:247],v[200:203],a[84:87]\n"
"buffer_load_dwordx4 v111,s[20:23],s77 offen offset:1024 lds\n"
"v_mfma_f32_16x16x32_bf16 a[84:87],v[180:183],v[136:139],a[84:87]\n"
"v_mfma_f32_16x16x32_bf16 a[88:91],v[184:187],v[136:139],a[88:91]\n"
"v_mfma_f32_16x16x32_bf16 a[88:91],v[248:251],v[200:203],a[88:91]\n"
"s_mov_b32 s76,s55\n"
"s_mov_b32 s84,0x40000\n"
"v_mfma_f32_16x16x32_bf16 a[92:95],v[252:255],v[200:203],a[92:95]\n"
"buffer_load_dwordx4 v113,s[20:23],s77 offen offset:2048 lds\n"
"v_mfma_f32_16x16x32_bf16 a[92:95],v[188:191],v[136:139],a[92:95]\n"
"s_waitcnt vmcnt(7)\n"
".Lfk_xn0_chk:\n"
"v_xor_b32 v116,s80,v74\n"
"v_xor_b32 v117,s81,v75\n"
"v_or_b32 v116,v116,v117\n"
"v_xor_b32 v117,s80,v76\n"
"v_or_b32 v116,v116,v117\n"
"v_xor_b32 v117,s81,v77\n"
"v_or_b32 v116,v116,v117\n"
"v_cmp_ne_u32 vcc,0,v116\n"
"s_nop 0\n"
"s_cbranch_vccz .Lfk_xn0_ok\n"
"s_sub_u32 s84,s84,1\n"
"s_cbranch_scc1 .Lfk_xn0_ok\n"
"s_sleep 4\n"
"buffer_load_dwordx4 v[74:77],v112,s[24:27],s58 offen sc1\n"
"s_waitcnt vmcnt(0)\n"
"s_branch .Lfk_xn0_chk\n"
".Lfk_xn0_ok:\n"
"v_mfma_f32_16x16x32_bf16 a[96:99],v[160:163],v[140:143],a[96:99]\n"
"v_mfma_f32_16x16x32_bf16 a[96:99],v[224:227],v[204:207],a[96:99]\n"
"v_mfma_f32_16x16x32_bf16 a[100:103],v[228:231],v[204:207],a[100:103]\n"
"buffer_load_dwordx4 v114,s[20:23],s77 offen offset:3072 lds\n"
"v_mfma_f32_16x16x32_bf16 a[100:103],v[164:167],v[140:143],a[100:103]\n"
"v_mfma_f32_16x16x32_bf16 a[104:107],v[168:171],v[140:143],a[104:107]\n"
"s_waitcnt lgkmcnt(0)\n"
"s_lshl2_add_u32 s88,s100,s52\n"
"v_mfma_f32_16x16x32_bf16 a[104:107],v[232:235],v[204:207],a[104:107]\n"
"s_mul_i32 s88,s88,7168\n"
"s_add_u32 s88,s88,0\n"
"v_mfma_f32_16x16x32_bf16 a[108:111],v[236:239],v[204:207],a[108:111]\n"
"s_add_u32 s48,s48,s88\n"
"s_addc_u32 s49,s49,0\n"
"s_add_u32 m0,s78,65536\n"
"v_mfma_f32_16x16x32_bf16 a[108:111],v[172:175],v[140:143],a[108:111]\n"
"buffer_load_dwordx4 v112,s[24:27],s76 offen offset:0 sc1 lds\n"
"v_mfma_f32_16x16x32_bf16 a[112:115],v[176:179],v[140:143],a[112:115]\n"
"s_mov_b32 s50,3584\n"
"s_mov_b32 s51,0x20000\n"
"v_mfma_f32_16x16x32_bf16 a[112:115],v[240:243],v[204:207],a[112:115]\n"
"v_lshlrev_b32 v8,3,v112\n"
"v_mfma_f32_16x16x32_bf16 a[116:119],v[244:247],v[204:207],a[116:119]\n"
"v_mfma_f32_16x16x32_bf16 a[116:119],v[180:183],v[140:143],a[116:119]\n"
"v_mfma_f32_16x16x32_bf16 a[120:123],v[184:187],v[140:143],a[120:123]\n"
"buffer_load_dwordx4 v112,s[24:27],s76 offen offset:1024 sc1 lds\n"
"v_mfma_f32_16x16x32_bf16 a[120:123],v[248:251],v[204:207],a[120:123]\n"
"v_mfma_f32_16x16x32_bf16 a[124:127],v[252:255],v[204:207],a[124:127]\n"
"v_mfma_f32_16x16x32_bf16 a[124:127],v[188:191],v[140:143],a[124:127]\n"
"v_mfma_f32_16x16x32_bf16 a[128:131],v[160:163],v[144:147],a[128:131]\n"
"v_mfma_f32_16x16x32_bf16 a[128:131],v[224:227],v[208:211],a[128:131]\n"
"buffer_load_dwordx4 v112,s[24:27],s76 offen offset:2048 sc1 lds\n"
"v_mfma_f32_16x16x32_bf16 a[132:135],v[228:231],v[208:211],a[132:135]\n"
"v_mfma_f32_16x16x32_bf16 a[132:135],v[164:167],v[144:147],a[132:135]\n"
"v_mfma_f32_16x16x32_bf16 a[136:139],v[168:171],v[144:147],a[136:139]\n"
"v_mfma_f32_16x16x32_bf16 a[136:139],v[232:235],v[208:211],a[136:139]\n"
"buffer_load_dwordx4 v112,s[24:27],s76 offen offset:3072 sc1 lds\n"
"v_mfma_f32_16x16x32_bf16 a[140:143],v[236:239],v[208:211],a[140:143]\n"
"s_xor_b32 s76,s76,16384\n"
"v_mfma_f32_16x16x32_bf16 a[140:143],v[172:175],v[144:147],a[140:143]\n"
"v_mfma_f32_16x16x32_bf16 a[144:147],v[176:179],v[144:147],a[144:147]\n"
"v_mfma_f32_16x16x32_bf16 a[144:147],v[240:243],v[208:211],a[144:147]\n"
"v_mfma_f32_16x16x32_bf16 a[148:151],v[244:247],v[208:211],a[148:151]\n"
"s_add_u32 m0,s78,81920\n"
"v_mfma_f32_16x16x32_bf16 a[148:151],v[180:183],v[144:147],a[148:151]\n"
"buffer_load_dwordx4 v112,s[24:27],s76 offen offset:0 sc1 lds\n"
"v_mfma_f32_16x16x32_bf16 a[152:155],v[184:187],v[144:147],a[152:155]\n"
"v_mfma_f32_16x16x32_bf16 a[152:155],v[248:251],v[208:211],a[152:155]\n"
"v_mfma_f32_16x16x32_bf16 a[156:159],v[252:255],v[208:211],a[156:159]\n"
"v_mfma_f32_16x16x32_bf16 a[156:159],v[188:191],v[144:147],a[156:159]\n"
"v_mfma_f32_16x16x32_bf16 a[160:163],v[160:163],v[148:151],a[160:163]\n"
"buffer_load_dwordx4 v112,s[24:27],s76 offen offset:1024 sc1 lds\n"
"v_mfma_f32_16x16x32_bf16 a[160:163],v[224:227],v[212:215],a[160:163]\n"
"v_mfma_f32_16x16x32_bf16 a[164:167],v[228:231],v[212:215],a[164:167]\n"
"v_mfma_f32_16x16x32_bf16 a[164:167],v[164:167],v[148:151],a[164:167]\n"
"v_mfma_f32_16x16x32_bf16 a[168:171],v[168:171],v[148:151],a[168:171]\n"
"v_mfma_f32_16x16x32_bf16 a[168:171],v[232:235],v[212:215],a[168:171]\n"
"buffer_load_dwordx4 v112,s[24:27],s76 offen offset:2048 sc1 lds\n"
"v_mfma_f32_16x16x32_bf16 a[172:175],v[236:239],v[212:215],a[172:175]\n"
"v_mfma_f32_16x16x32_bf16 a[172:175],v[172:175],v[148:151],a[172:175]\n"
"v_mfma_f32_16x16x32_bf16 a[176:179],v[176:179],v[148:151],a[176:179]\n"
"v_mfma_f32_16x16x32_bf16 a[176:179],v[240:243],v[212:215],a[176:179]\n"
"buffer_load_dwordx4 v112,s[24:27],s76 offen offset:3072 sc1 lds\n"
"v_mfma_f32_16x16x32_bf16 a[180:183],v[244:247],v[212:215],a[180:183]\n"
"s_xor_b32 s76,s76,49152\n"
"v_mfma_f32_16x16x32_bf16 a[180:183],v[180:183],v[148:151],a[180:183]\n"
"v_mfma_f32_16x16x32_bf16 a[184:187],v[184:187],v[148:151],a[184:187]\n"
"buffer_load_dword v115,v8,s[48:51],0 offen\n"
"v_mfma_f32_16x16x32_bf16 a[184:187],v[248:251],v[212:215],a[184:187]\n"
"v_mfma_f32_16x16x32_bf16 a[188:191],v[252:255],v[212:215],a[188:191]\n"
"v_mfma_f32_16x16x32_bf16 a[188:191],v[188:191],v[148:151],a[188:191]\n"
"s_barrier\n"
"v_mfma_f32_16x16x32_bf16 a[192:195],v[224:227],v[216:219],a[192:195]\n"
"ds_read_b128 v[152:155],v80 offset:12288\n"
"v_mfma_f32_16x16x32_bf16 a[224:227],v[224:227],v[220:223],a[224:227]\n"
"ds_read_b128 v[156:159],v80 offset:14336\n"
"v_mfma_f32_16x16x32_bf16 a[228:231],v[228:231],v[220:223],a[228:231]\n"
"ds_read_b64_tr_b16 v[160:161],v98 offset:49152\n"
"v_mfma_f32_16x16x32_bf16 a[196:199],v[228:231],v[216:219],a[196:199]\n"
"ds_read_b64_tr_b16 v[162:163],v98 offset:50176\n"
"v_mfma_f32_16x16x32_bf16 a[200:203],v[232:235],v[216:219],a[200:203]\n"
"ds_read_b64_tr_b16 v[164:165],v99 offset:49152\n"
"v_mfma_f32_16x16x32_bf16 a[232:235],v[232:235],v[220:223],a[232:235]\n"
"ds_read_b64_tr_b16 v[166:167],v99 offset:50176\n"
"v_mfma_f32_16x16x32_bf16 a[236:239],v[236:239],v[220:223],a[236:239]\n"
"ds_read_b64_tr_b16 v[168:169],v100 offset:49152\n"
"v_mfma_f32_16x16x32_bf16 a[204:207],v[236:239],v[216:219],a[204:207]\n"
"ds_read_b64_tr_b16 v[170:171],v100 offset:50176\n"
"v_mfma_f32_16x16x32_bf16 a[208:211],v[240:243],v[216:219],a[208:211]\n"
"ds_read_b64_tr_b16 v[172:173],v101 offset:49152\n"
"v_mfma_f32_16x16x32_bf16 a[240:243],v[240:243],v[220:223],a[240:243]\n"
"ds_read_b64_tr_b16 v[174:175],v101 offset:50176\n"
"v_mfma_f32_16x16x32_bf16 a[244:247],v[244:247],v[220:223],a[244:247]\n"
"ds_read_b64_tr_b16 v[176:177],v102 offset:49152\n"
"v_mfma_f32_16x16x32_bf16 a[212:215],v[244:247],v[216:219],a[212:215]\n"
"ds_read_b64_tr_b16 v[178:179],v102 offset:50176\n"
"v_mfma_f32_16x16x32_bf16 a[216:219],v[248:251],v[216:219],a[216:219]\n"
"ds_read_b64_tr_b16 v[180:181],v103 offset:49152\n"
"v_mfma_f32_16x16x32_bf16 a[248:251],v[248:251],v[220:223],a[248:251]\n"
"ds_read_b64_tr_b16 v[182:183],v103 offset:50176\n"
"ds_read_b64_tr_b16 v[184:185],v104 offset:49152\n"
"v_mfma_f32_16x16x32_bf16 a[252:255],v[252:255],v[220:223],a[252:255]\n"
"v_mov_b32 v110,v72\n"
"v_mov_b32 v111,v73\n"
"v_mfma_f32_16x16x32_bf16 a[220:223],v[252:255],v[216:219],a[220:223]\n"
"ds_read_b64_tr_b16 v[186:187],v104 offset:50176\n"
"s_waitcnt lgkmcnt(10)\n"
"v_mfma_f32_16x16x32_bf16 a[192:195],v[160:163],v[152:155],a[192:195]\n"
"ds_read_b64_tr_b16 v[188:189],v105 offset:49152\n"
"v_mfma_f32_16x16x32_bf16 a[224:227],v[160:163],v[156:159],a[224:227]\n"
"ds_read_b64_tr_b16 v[190:191],v105 offset:50176\n"
"v_mfma_f32_16x16x32_bf16 a[228:231],v[164:167],v[156:159],a[228:231]\n"
"ds_read_b128 v[128:131],v80 offset:0\n"
"v_mfma_f32_16x16x32_bf16 a[196:199],v[164:167],v[152:155],a[196:199]\n"
"ds_read_b128 v[192:195],v81 offset:0\n"
"s_waitcnt lgkmcnt(10)\n"
"v_mfma_f32_16x16x32_bf16 a[200:203],v[168:171],v[152:155],a[200:203]\n"
"ds_read_b64_tr_b16 v[224:225],v98 offset:32768\n"
"v_mfma_f32_16x16x32_bf16 a[232:235],v[168:171],v[156:159],a[232:235]\n"
"ds_read_b64_tr_b16 v[226:227],v98 offset:33792\n"
"v_mfma_f32_16x16x32_bf16 a[236:239],v[172:175],v[156:159],a[236:239]\n"
"ds_read_b64_tr_b16 v[228:229],v99 offset:32768\n"
"v_mfma_f32_16x16x32_bf16 a[204:207],v[172:175],v[152:155],a[204:207]\n"
"ds_read_b64_tr_b16 v[230:231],v99 offset:33792\n"
"s_waitcnt lgkmcnt(10)\n"
"v_mfma_f32_16x16x32_bf16 a[208:211],v[176:179],v[152:155],a[208:211]\n"
"ds_read_b64_tr_b16 v[232:233],v100 offset:32768\n"
"v_mfma_f32_16x16x32_bf16 a[240:243],v[176:179],v[156:159],a[240:243]\n"
"ds_read_b64_tr_b16 v[234:235],v100 offset:33792\n"
"v_mfma_f32_16x16x32_bf16 a[244:247],v[180:183],v[156:159],a[244:247]\n"
"ds_read_b64_tr_b16 v[236:237],v101 offset:32768\n"
"v_mfma_f32_16x16x32_bf16 a[212:215],v[180:183],v[152:155],a[212:215]\n"
"ds_read_b64_tr_b16 v[238:239],v101 offset:33792\n"
"s_waitcnt lgkmcnt(10)\n"
"v_mfma_f32_16x16x32_bf16 a[216:219],v[184:187],v[152:155],a[216:219]\n"
"ds_read_b64_tr_b16 v[240:241],v102 offset:32768\n"
"v_mfma_f32_16x16x32_bf16 a[248:251],v[184:187],v[156:159],a[248:251]\n"
"ds_read_b64_tr_b16 v[242:243],v102 offset:33792\n"
"v_mfma_f32_16x16x32_bf16 a[252:255],v[188:191],v[156:159],a[252:255]\n"
"ds_read_b64_tr_b16 v[244:245],v103 offset:32768\n"
"v_mfma_f32_16x16x32_bf16 a[220:223],v[188:191],v[152:155],a[220:223]\n"
"ds_read_b64_tr_b16 v[246:247],v103 offset:33792\n"
"s_waitcnt lgkmcnt(8)\n"
"v_mfma_f32_16x16x32_bf16 a[0:3],v[160:163],v[128:131],a[0:3]\n"
"ds_read_b64_tr_b16 v[248:249],v104 offset:32768\n"
"v_mfma_f32_16x16x32_bf16 a[0:3],v[224:227],v[192:195],a[0:3]\n"
"ds_read_b64_tr_b16 v[250:251],v104 offset:33792\n"
"v_mfma_f32_16x16x32_bf16 a[4:7],v[228:231],v[192:195],a[4:7]\n"
"ds_read_b64_tr_b16 v[252:253],v105 offset:32768\n"
"v_mfma_f32_16x16x32_bf16 a[4:7],v[164:167],v[128:131],a[4:7]\n"
"ds_read_b64_tr_b16 v[254:255],v105 offset:33792\n"
"s_waitcnt lgkmcnt(8)\n"
"v_mfma_f32_16x16x32_bf16 a[8:11],v[168:171],v[128:131],a[8:11]\n"
"ds_read_b128 v[132:135],v80 offset:2048\n"
"v_mfma_f32_16x16x32_bf16 a[8:11],v[232:235],v[192:195],a[8:11]\n"
"ds_read_b128 v[196:199],v81 offset:2048\n"
"v_mfma_f32_16x16x32_bf16 a[12:15],v[236:239],v[192:195],a[12:15]\n"
"ds_read_b128 v[136:139],v80 offset:4096\n"
"v_mfma_f32_16x16x32_bf16 a[12:15],v[172:175],v[128:131],a[12:15]\n"
"ds_read_b128 v[200:203],v81 offset:4096\n"
"s_waitcnt lgkmcnt(8)\n"
"v_mfma_f32_16x16x32_bf16 a[16:19],v[176:179],v[128:131],a[16:19]\n"
"ds_read_b128 v[140:143],v80 offset:6144\n"
"v_mfma_f32_16x16x32_bf16 a[16:19],v[240:243],v[192:195],a[16:19]\n"
"ds_read_b128 v[204:207],v81 offset:6144\n"
"v_mfma_f32_16x16x32_bf16 a[20:23],v[244:247],v[192:195],a[20:23]\n"
"ds_read_b128 v[144:147],v80 offset:8192\n"
"v_mfma_f32_16x16x32_bf16 a[20:23],v[180:183],v[128:131],a[20:23]\n"
"ds_read_b128 v[208:211],v81 offset:8192\n"
"s_waitcnt lgkmcnt(8)\n"
"v_mfma_f32_16x16x32_bf16 a[24:27],v[184:187],v[128:131],a[24:27]\n"
"ds_read_b128 v[148:151],v80 offset:10240\n"
"v_mfma_f32_16x16x32_bf16 a[24:27],v[248:251],v[192:195],a[24:27]\n"
"ds_read_b128 v[212:215],v81 offset:10240\n"
"v_mfma_f32_16x16x32_bf16 a[28:31],v[252:255],v[192:195],a[28:31]\n"
"ds_read_b128 v[216:219],v81 offset:12288\n"
"v_mfma_f32_16x16x32_bf16 a[28:31],v[188:191],v[128:131],a[28:31]\n"
"ds_read_b128 v[220:223],v81 offset:14336\n"
"s_waitcnt lgkmcnt(10)\n"
"v_mfma_f32_16x16x32_bf16 a[32:35],v[160:163],v[132:135],a[32:35]\n"
"v_mfma_f32_16x16x32_bf16 a[32:35],v[224:227],v[196:199],a[32:35]\n"
"v_mfma_f32_16x16x32_bf16 a[36:39],v[228:231],v[196:199],a[36:39]\n"
"v_mfma_f32_16x16x32_bf16 a[36:39],v[164:167],v[132:135],a[36:39]\n"
"v_mfma_f32_16x16x32_bf16 a[40:43],v[168:171],v[132:135],a[40:43]\n"
"s_waitcnt lgkmcnt(0)\n"
"s_barrier\n"
"v_mfma_f32_16x16x32_bf16 a[40:43],v[232:235],v[196:199],a[40:43]\n"
"s_add_u32 m0,s85,147456\n"
"v_mfma_f32_16x16x32_bf16 a[44:47],v[236:239],v[196:199],a[44:47]\n"
"buffer_load_dwordx4 v110,s[28:31],s86 offen lds\n"
"v_mfma_f32_16x16x32_bf16 a[44:47],v[172:175],v[132:135],a[44:47]\n"
"v_mfma_f32_16x16x32_bf16 a[48:51],v[176:179],v[132:135],a[48:51]\n"
"s_load_dwordx2 s[48:49],s[0:1],56\n"
"v_mfma_f32_16x16x32_bf16 a[48:51],v[240:243],v[196:199],a[48:51]\n"
"s_add_u32 m0,s85,148480\n"
"v_mfma_f32_16x16x32_bf16 a[52:55],v[244:247],v[196:199],a[52:55]\n"
"buffer_load_dwordx4 v111,s[28:31],s86 offen lds\n"
"v_mfma_f32_16x16x32_bf16 a[52:55],v[180:183],v[132:135],a[52:55]\n"
"s_xor_b32 s86,s86,262144\n"
"v_mfma_f32_16x16x32_bf16 a[56:59],v[184:187],v[132:135],a[56:59]\n"
"v_mfma_f32_16x16x32_bf16 a[56:59],v[248:251],v[196:199],a[56:59]\n"
"s_add_u32 m0,s85,155648\n"
"v_mfma_f32_16x16x32_bf16 a[60:63],v[252:255],v[196:199],a[60:63]\n"
"buffer_load_dwordx4 v110,s[28:31],s86 offen lds\n"
"v_mfma_f32_16x16x32_bf16 a[60:63],v[188:191],v[132:135],a[60:63]\n"
"v_mfma_f32_16x16x32_bf16 a[64:67],v[160:163],v[136:139],a[64:67]\n"
"v_mfma_f32_16x16x32_bf16 a[64:67],v[224:227],v[200:203],a[64:67]\n"
"s_add_u32 m0,s85,156672\n"
"v_mfma_f32_16x16x32_bf16 a[68:71],v[228:231],v[200:203],a[68:71]\n"
"buffer_load_dwordx4 v111,s[28:31],s86 offen lds\n"
"v_mfma_f32_16x16x32_bf16 a[68:71],v[164:167],v[136:139],a[68:71]\n"
"s_xor_b32 s86,s86,786432\n"
"v_mfma_f32_16x16x32_bf16 a[72:75],v[168:171],v[136:139],a[72:75]\n"
"v_mfma_f32_16x16x32_bf16 a[72:75],v[232:235],v[200:203],a[72:75]\n"
"s_add_u32 m0,s85,131072\n"
"v_mfma_f32_16x16x32_bf16 a[76:79],v[236:239],v[200:203],a[76:79]\n"
"buffer_load_dwordx4 v110,s[28:31],s86 offen lds\n"
"v_mfma_f32_16x16x32_bf16 a[76:79],v[172:175],v[136:139],a[76:79]\n"
"v_mfma_f32_16x16x32_bf16 a[80:83],v[176:179],v[136:139],a[80:83]\n"
"v_mfma_f32_16x16x32_bf16 a[80:83],v[240:243],v[200:203],a[80:83]\n"
"s_add_u32 m0,s85,132096\n"
"v_mfma_f32_16x16x32_bf16 a[84:87],v[244:247],v[200:203],a[84:87]\n"
"buffer_load_dwordx4 v111,s[28:31],s86 offen lds\n"
"v_mfma_f32_16x16x32_bf16 a[84:87],v[180:183],v[136:139],a[84:87]\n"
"s_xor_b32 s86,s86,262144\n"
"v_mfma_f32_16x16x32_bf16 a[88:91],v[184:187],v[136:139],a[88:91]\n"
"v_mfma_f32_16x16x32_bf16 a[88:91],v[248:251],v[200:203],a[88:91]\n"
"v_mfma_f32_16x16x32_bf16 a[92:95],v[252:255],v[200:203],a[92:95]\n"
"v_mfma_f32_16x16x32_bf16 a[92:95],v[188:191],v[136:139],a[92:95]\n"
"v_mfma_f32_16x16x32_bf16 a[96:99],v[160:163],v[140:143],a[96:99]\n"
"v_mfma_f32_16x16x32_bf16 a[96:99],v[224:227],v[204:207],a[96:99]\n"
"s_add_u32 m0,s85,139264\n"
"v_mfma_f32_16x16x32_bf16 a[100:103],v[228:231],v[204:207],a[100:103]\n"
"buffer_load_dwordx4 v110,s[28:31],s86 offen lds\n"
"v_mfma_f32_16x16x32_bf16 a[100:103],v[164:167],v[140:143],a[100:103]\n"
"v_mfma_f32_16x16x32_bf16 a[104:107],v[168:171],v[140:143],a[104:107]\n"
"s_waitcnt lgkmcnt(0)\n"
"s_lshl2_add_u32 s88,s100,s52\n"
"v_mfma_f32_16x16x32_bf16 a[104:107],v[232:235],v[204:207],a[104:107]\n"
"s_mul_i32 s88,s88,7168\n"
"s_add_u32 m0,s85,140288\n"
"v_mfma_f32_16x16x32_bf16 a[108:111],v[236:239],v[204:207],a[108:111]\n"
"buffer_load_dwordx4 v111,s[28:31],s86 offen lds\n"
"s_xor_b32 s86,s86,1835008\n"
"v_mfma_f32_16x16x32_bf16 a[108:111],v[172:175],v[140:143],a[108:111]\n"
"s_add_u32 s88,s88,3584\n"
"s_add_u32 s48,s48,s88\n"
"s_addc_u32 s49,s49,0\n"
"v_mfma_f32_16x16x32_bf16 a[112:115],v[176:179],v[140:143],a[112:115]\n"
"s_mov_b32 s50,3584\n"
"s_mov_b32 s51,0x20000\n"
"v_mfma_f32_16x16x32_bf16 a[112:115],v[240:243],v[204:207],a[112:115]\n"
"v_lshlrev_b32 v8,3,v112\n"
"s_add_u32 m0,s78,0\n"
"v_mfma_f32_16x16x32_bf16 a[116:119],v[244:247],v[204:207],a[116:119]\n"
"buffer_load_dwordx4 v112,s[24:27],s76 offen offset:0 sc1 lds\n"
"v_mfma_f32_16x16x32_bf16 a[116:119],v[180:183],v[140:143],a[116:119]\n"
"v_mfma_f32_16x16x32_bf16 a[120:123],v[184:187],v[140:143],a[120:123]\n"
"v_mfma_f32_16x16x32_bf16 a[120:123],v[248:251],v[204:207],a[120:123]\n"
"v_mfma_f32_16x16x32_bf16 a[124:127],v[252:255],v[204:207],a[124:127]\n"
"buffer_load_dwordx4 v112,s[24:27],s76 offen offset:1024 sc1 lds\n"
"v_mfma_f32_16x16x32_bf16 a[124:127],v[188:191],v[140:143],a[124:127]\n"
"v_mfma_f32_16x16x32_bf16 a[128:131],v[160:163],v[144:147],a[128:131]\n"
"v_mfma_f32_16x16x32_bf16 a[128:131],v[224:227],v[208:211],a[128:131]\n"
"v_mfma_f32_16x16x32_bf16 a[132:135],v[228:231],v[208:211],a[132:135]\n"
"buffer_load_dwordx4 v112,s[24:27],s76 offen offset:2048 sc1 lds\n"
"v_mfma_f32_16x16x32_bf16 a[132:135],v[164:167],v[144:147],a[132:135]\n"
"v_mfma_f32_16x16x32_bf16 a[136:139],v[168:171],v[144:147],a[136:139]\n"
"v_mfma_f32_16x16x32_bf16 a[136:139],v[232:235],v[208:211],a[136:139]\n"
"buffer_load_dwordx4 v112,s[24:27],s76 offen offset:3072 sc1 lds\n"
"v_mfma_f32_16x16x32_bf16 a[140:143],v[236:239],v[208:211],a[140:143]\n"
"s_xor_b32 s76,s76,16384\n"
"v_mfma_f32_16x16x32_bf16 a[140:143],v[172:175],v[144:147],a[140:143]\n"
"v_mfma_f32_16x16x32_bf16 a[144:147],v[176:179],v[144:147],a[144:147]\n"
"v_mfma_f32_16x16x32_bf16 a[144:147],v[240:243],v[208:211],a[144:147]\n"
"s_add_u32 m0,s78,16384\n"
"v_mfma_f32_16x16x32_bf16 a[148:151],v[244:247],v[208:211],a[148:151]\n"
"buffer_load_dwordx4 v112,s[24:27],s76 offen offset:0 sc1 lds\n"
"v_mfma_f32_16x16x32_bf16 a[148:151],v[180:183],v[144:147],a[148:151]\n"
"v_mfma_f32_16x16x32_bf16 a[152:155],v[184:187],v[144:147],a[152:155]\n"
"v_mfma_f32_16x16x32_bf16 a[152:155],v[248:251],v[208:211],a[152:155]\n"
"v_mfma_f32_16x16x32_bf16 a[156:159],v[252:255],v[208:211],a[156:159]\n"
"buffer_load_dwordx4 v112,s[24:27],s76 offen offset:1024 sc1 lds\n"
"v_mfma_f32_16x16x32_bf16 a[156:159],v[188:191],v[144:147],a[156:159]\n"
"v_mfma_f32_16x16x32_bf16 a[160:163],v[160:163],v[148:151],a[160:163]\n"
"v_mfma_f32_16x16x32_bf16 a[160:163],v[224:227],v[212:215],a[160:163]\n"
"v_mfma_f32_16x16x32_bf16 a[164:167],v[228:231],v[212:215],a[164:167]\n"
"buffer_load_dwordx4 v112,s[24:27],s76 offen offset:2048 sc1 lds\n"
"v_mfma_f32_16x16x32_bf16 a[164:167],v[164:167],v[148:151],a[164:167]\n"
"v_mfma_f32_16x16x32_bf16 a[168:171],v[168:171],v[148:151],a[168:171]\n"
"v_mfma_f32_16x16x32_bf16 a[168:171],v[232:235],v[212:215],a[168:171]\n"
"buffer_load_dwordx4 v112,s[24:27],s76 offen offset:3072 sc1 lds\n"
"v_mfma_f32_16x16x32_bf16 a[172:175],v[236:239],v[212:215],a[172:175]\n"
"s_xor_b32 s76,s76,114688\n"
"v_mfma_f32_16x16x32_bf16 a[172:175],v[172:175],v[148:151],a[172:175]\n"
"v_mfma_f32_16x16x32_bf16 a[176:179],v[176:179],v[148:151],a[176:179]\n"
"buffer_load_dword v115,v8,s[48:51],0 offen\n"
"v_mfma_f32_16x16x32_bf16 a[176:179],v[240:243],v[212:215],a[176:179]\n"
"v_mfma_f32_16x16x32_bf16 a[180:183],v[244:247],v[212:215],a[180:183]\n"
"v_mfma_f32_16x16x32_bf16 a[180:183],v[180:183],v[148:151],a[180:183]\n"
"v_mfma_f32_16x16x32_bf16 a[184:187],v[184:187],v[148:151],a[184:187]\n"
"v_mfma_f32_16x16x32_bf16 a[184:187],v[248:251],v[212:215],a[184:187]\n"
"v_mfma_f32_16x16x32_bf16 a[188:191],v[252:255],v[212:215],a[188:191]\n"
"v_mfma_f32_16x16x32_bf16 a[188:191],v[188:191],v[148:151],a[188:191]\n"
"s_waitcnt vmcnt(26)\n"
"s_barrier\n"
"v_mfma_f32_16x16x32_bf16 a[192:195],v[224:227],v[216:219],a[192:195]\n"
"ds_read_b128 v[152:155],v80 offset:45056\n"
"v_mfma_f32_16x16x32_bf16 a[224:227],v[224:227],v[220:223],a[224:227]\n"
"ds_read_b128 v[156:159],v80 offset:47104\n"
"v_mfma_f32_16x16x32_bf16 a[228:231],v[228:231],v[220:223],a[228:231]\n"
"ds_read_b64_tr_b16 v[160:161],v98 offset:0\n"
"v_mfma_f32_16x16x32_bf16 a[196:199],v[228:231],v[216:219],a[196:199]\n"
"ds_read_b64_tr_b16 v[162:163],v98 offset:1024\n"
"v_mfma_f32_16x16x32_bf16 a[200:203],v[232:235],v[216:219],a[200:203]\n"
"ds_read_b64_tr_b16 v[164:165],v99 offset:0\n"
"v_mfma_f32_16x16x32_bf16 a[232:235],v[232:235],v[220:223],a[232:235]\n"
"ds_read_b64_tr_b16 v[166:167],v99 offset:1024\n"
"v_mfma_f32_16x16x32_bf16 a[236:239],v[236:239],v[220:223],a[236:239]\n"
"ds_read_b64_tr_b16 v[168:169],v100 offset:0\n"
"v_mfma_f32_16x16x32_bf16 a[204:207],v[236:239],v[216:219],a[204:207]\n"
"ds_read_b64_tr_b16 v[170:171],v100 offset:1024\n"
"v_mfma_f32_16x16x32_bf16 a[208:211],v[240:243],v[216:219],a[208:211]\n"
"ds_read_b64_tr_b16 v[172:173],v101 offset:0\n"
"v_mfma_f32_16x16x32_bf16 a[240:243],v[240:243],v[220:223],a[240:243]\n"
"ds_read_b64_tr_b16 v[174:175],v101 offset:1024\n"
"v_mfma_f32_16x16x32_bf16 a[244:247],v[244:247],v[220:223],a[244:247]\n"
"ds_read_b64_tr_b16 v[176:177],v102 offset:0\n"
"v_mfma_f32_16x16x32_bf16 a[212:215],v[244:247],v[216:219],a[212:215]\n"
"ds_read_b64_tr_b16 v[178:179],v102 offset:1024\n"
"v_mfma_f32_16x16x32_bf16 a[216:219],v[248:251],v[216:219],a[216:219]\n"
"ds_read_b64_tr_b16 v[180:181],v103 offset:0\n"
"v_mfma_f32_16x16x32_bf16 a[248:251],v[248:251],v[220:223],a[248:251]\n"
"ds_read_b64_tr_b16 v[182:183],v103 offset:1024\n"
"v_mfma_f32_16x16x32_bf16 a[252:255],v[252:255],v[220:223],a[252:255]\n"
"ds_read_b64_tr_b16 v[184:185],v104 offset:0\n"
"v_mfma_f32_16x16x32_bf16 a[220:223],v[252:255],v[216:219],a[220:223]\n"
"ds_read_b64_tr_b16 v[186:187],v104 offset:1024\n"
"s_waitcnt lgkmcnt(10)\n"
"v_mfma_f32_16x16x32_bf16 a[192:195],v[160:163],v[152:155],a[192:195]\n"
"ds_read_b64_tr_b16 v[188:189],v105 offset:0\n"
"v_mfma_f32_16x16x32_bf16 a[224:227],v[160:163],v[156:159],a[224:227]\n"
"ds_read_b64_tr_b16 v[190:191],v105 offset:1024\n"
"v_mfma_f32_16x16x32_bf16 a[228:231],v[164:167],v[156:159],a[228:231]\n"
"ds_read_b128 v[128:131],v80 offset:32768\n"
"v_mfma_f32_16x16x32_bf16 a[196:199],v[164:167],v[152:155],a[196:199]\n"
"ds_read_b128 v[192:195],v81 offset:32768\n"
"s_waitcnt lgkmcnt(10)\n"
"v_mfma_f32_16x16x32_bf16 a[200:203],v[168:171],v[152:155],a[200:203]\n"
"ds_read_b64_tr_b16 v[224:225],v98 offset:16384\n"
"v_mfma_f32_16x16x32_bf16 a[232:235],v[168:171],v[156:159],a[232:235]\n"
"ds_read_b64_tr_b16 v[226:227],v98 offset:17408\n"
"v_mfma_f32_16x16x32_bf16 a[236:239],v[172:175],v[156:159],a[236:239]\n"
"ds_read_b64_tr_b16 v[228:229],v99 offset:16384\n"
"v_mfma_f32_16x16x32_bf16 a[204:207],v[172:175],v[152:155],a[204:207]\n"
"ds_read_b64_tr_b16 v[230:231],v99 offset:17408\n"
"s_waitcnt lgkmcnt(10)\n"
"v_mfma_f32_16x16x32_bf16 a[208:211],v[176:179],v[152:155],a[208:211]\n"
"ds_read_b64_tr_b16 v[232:233],v100 offset:16384\n"
"v_mfma_f32_16x16x32_bf16 a[240:243],v[176:179],v[156:159],a[240:243]\n"
"ds_read_b64_tr_b16 v[234:235],v100 offset:17408\n"
"v_mfma_f32_16x16x32_bf16 a[244:247],v[180:183],v[156:159],a[244:247]\n"
"ds_read_b64_tr_b16 v[236:237],v101 offset:16384\n"
"v_mfma_f32_16x16x32_bf16 a[212:215],v[180:183],v[152:155],a[212:215]\n"
"ds_read_b64_tr_b16 v[238:239],v101 offset:17408\n"
"s_waitcnt lgkmcnt(10)\n"
"v_mfma_f32_16x16x32_bf16 a[216:219],v[184:187],v[152:155],a[216:219]\n"
"ds_read_b64_tr_b16 v[240:241],v102 offset:16384\n"
"v_mfma_f32_16x16x32_bf16 a[248:251],v[184:187],v[156:159],a[248:251]\n"
"ds_read_b64_tr_b16 v[242:243],v102 offset:17408\n"
"v_mfma_f32_16x16x32_bf16 a[252:255],v[188:191],v[156:159],a[252:255]\n"
"ds_read_b64_tr_b16 v[244:245],v103 offset:16384\n"
"v_mfma_f32_16x16x32_bf16 a[220:223],v[188:191],v[152:155],a[220:223]\n"
"ds_read_b64_tr_b16 v[246:247],v103 offset:17408\n"
"s_waitcnt lgkmcnt(8)\n"
"v_mfma_f32_16x16x32_bf16 a[0:3],v[160:163],v[128:131],a[0:3]\n"
"ds_read_b64_tr_b16 v[248:249],v104 offset:16384\n"
"v_mfma_f32_16x16x32_bf16 a[0:3],v[224:227],v[192:195],a[0:3]\n"
"ds_read_b64_tr_b16 v[250:251],v104 offset:17408\n"
"v_mfma_f32_16x16x32_bf16 a[4:7],v[228:231],v[192:195],a[4:7]\n"
"ds_read_b64_tr_b16 v[252:253],v105 offset:16384\n"
"v_mfma_f32_16x16x32_bf16 a[4:7],v[164:167],v[128:131],a[4:7]\n"
"ds_read_b64_tr_b16 v[254:255],v105 offset:17408\n"
"s_waitcnt lgkmcnt(8)\n"
"v_mfma_f32_16x16x32_bf16 a[8:11],v[168:171],v[128:131],a[8:11]\n"
"ds_read_b128 v[132:135],v80 offset:34816\n"
"v_mfma_f32_16x16x32_bf16 a[8:11],v[232:235],v[192:195],a[8:11]\n"
"ds_read_b128 v[196:199],v81 offset:34816\n"
"v_mfma_f32_16x16x32_bf16 a[12:15],v[236:239],v[192:195],a[12:15]\n"
"ds_read_b128 v[136:139],v80 offset:36864\n"
"v_mfma_f32_16x16x32_bf16 a[12:15],v[172:175],v[128:131],a[12:15]\n"
"ds_read_b128 v[200:203],v81 offset:36864\n"
"s_waitcnt lgkmcnt(8)\n"
"v_mfma_f32_16x16x32_bf16 a[16:19],v[176:179],v[128:131],a[16:19]\n"
"s_and_b32 s82,s52,1\n"
"ds_read_b128 v[140:143],v80 offset:38912\n"
"s_lshl_b32 s82,s82,13\n"
"v_mfma_f32_16x16x32_bf16 a[16:19],v[240:243],v[192:195],a[16:19]\n"
"ds_read_b128 v[204:207],v81 offset:38912\n"
"v_mfma_f32_16x16x32_bf16 a[20:23],v[244:247],v[192:195],a[20:23]\n"
"ds_read_b128 v[144:147],v80 offset:40960\n"
"v_mfma_f32_16x16x32_bf16 a[20:23],v[180:183],v[128:131],a[20:23]\n"
"ds_read_b128 v[208:211],v81 offset:40960\n"
"s_waitcnt lgkmcnt(8)\n"
"v_mfma_f32_16x16x32_bf16 a[24:27],v[184:187],v[128:131],a[24:27]\n"
"ds_read_b128 v[148:151],v80 offset:43008\n"
"v_mfma_f32_16x16x32_bf16 a[24:27],v[248:251],v[192:195],a[24:27]\n"
"ds_read_b128 v[212:215],v81 offset:43008\n"
"v_subrev_u32 v98,s82,v98\n"
"v_mfma_f32_16x16x32_bf16 a[28:31],v[252:255],v[192:195],a[28:31]\n"
"ds_read_b128 v[216:219],v81 offset:45056\n"
"v_add_u32 v96,s78,v97\n"
"v_mfma_f32_16x16x32_bf16 a[28:31],v[188:191],v[128:131],a[28:31]\n"
"ds_read_b128 v[220:223],v81 offset:47104\n"
"s_waitcnt lgkmcnt(10)\n"
"v_mfma_f32_16x16x32_bf16 a[32:35],v[160:163],v[132:135],a[32:35]\n"
"v_add_u32 v97,65536,v96\n"
"v_mfma_f32_16x16x32_bf16 a[32:35],v[224:227],v[196:199],a[32:35]\n"
"v_subrev_u32 v99,s82,v99\n"
"v_mfma_f32_16x16x32_bf16 a[36:39],v[228:231],v[196:199],a[36:39]\n"
"v_mfma_f32_16x16x32_bf16 a[36:39],v[164:167],v[132:135],a[36:39]\n"
"v_mfma_f32_16x16x32_bf16 a[40:43],v[168:171],v[132:135],a[40:43]\n"
"s_waitcnt lgkmcnt(0)\n"
"s_barrier\n"
"v_mfma_f32_16x16x32_bf16 a[40:43],v[232:235],v[196:199],a[40:43]\n"
"v_subrev_u32 v100,s82,v100\n"
"s_add_u32 m0,s85,98304\n"
"v_mfma_f32_16x16x32_bf16 a[44:47],v[236:239],v[196:199],a[44:47]\n"
"buffer_load_dwordx4 v110,s[28:31],s86 offen lds\n"
"v_mfma_f32_16x16x32_bf16 a[44:47],v[172:175],v[132:135],a[44:47]\n"
"v_mfma_f32_16x16x32_bf16 a[48:51],v[176:179],v[132:135],a[48:51]\n"
"v_mfma_f32_16x16x32_bf16 a[48:51],v[240:243],v[196:199],a[48:51]\n"
"v_subrev_u32 v101,s82,v101\n"
"s_add_u32 m0,s85,99328\n"
"v_mfma_f32_16x16x32_bf16 a[52:55],v[244:247],v[196:199],a[52:55]\n"
"buffer_load_dwordx4 v111,s[28:31],s86 offen lds\n"
"v_mfma_f32_16x16x32_bf16 a[52:55],v[180:183],v[132:135],a[52:55]\n"
"s_xor_b32 s86,s86,262144\n"
"v_mfma_f32_16x16x32_bf16 a[56:59],v[184:187],v[132:135],a[56:59]\n"
"v_mfma_f32_16x16x32_bf16 a[56:59],v[248:251],v[196:199],a[56:59]\n"
"v_subrev_u32 v102,s82,v102\n"
"s_add_u32 m0,s78,32768\n"
"v_mfma_f32_16x16x32_bf16 a[60:63],v[252:255],v[196:199],a[60:63]\n"
"buffer_load_dwordx4 v112,s[24:27],s76 offen offset:0 sc1 lds\n"
"v_mfma_f32_16x16x32_bf16 a[60:63],v[188:191],v[132:135],a[60:63]\n"
"v_mfma_f32_16x16x32_bf16 a[64:67],v[160:163],v[136:139],a[64:67]\n"
"v_mfma_f32_16x16x32_bf16 a[64:67],v[224:227],v[200:203],a[64:67]\n"
"v_subrev_u32 v103,s82,v103\n"
"v_mfma_f32_16x16x32_bf16 a[68:71],v[228:231],v[200:203],a[68:71]\n"
"buffer_load_dwordx4 v112,s[24:27],s76 offen offset:1024 sc1 lds\n"
"v_mfma_f32_16x16x32_bf16 a[68:71],v[164:167],v[136:139],a[68:71]\n"
"v_mfma_f32_16x16x32_bf16 a[72:75],v[168:171],v[136:139],a[72:75]\n"
"v_accvgpr_read_b32 v116,a0\n"
"v_accvgpr_read_b32 v117,a1\n"
"v_mfma_f32_16x16x32_bf16 a[72:75],v[232:235],v[200:203],a[72:75]\n"
"v_subrev_u32 v104,s82,v104\n"
"v_accvgpr_read_b32 v118,a2\n"
"v_mfma_f32_16x16x32_bf16 a[76:79],v[236:239],v[200:203],a[76:79]\n"
"buffer_load_dwordx4 v112,s[24:27],s76 offen offset:2048 sc1 lds\n"
"v_mfma_f32_16x16x32_bf16 a[76:79],v[172:175],v[136:139],a[76:79]\n"
"v_accvgpr_read_b32 v119,a3\n"
"v_accvgpr_read_b32 v120,a4\n"
"v_mfma_f32_16x16x32_bf16 a[80:83],v[176:179],v[136:139],a[80:83]\n"
"v_accvgpr_read_b32 v121,a5\n"
"v_accvgpr_read_b32 v122,a6\n"
"v_mfma_f32_16x16x32_bf16 a[80:83],v[240:243],v[200:203],a[80:83]\n"
"v_subrev_u32 v105,s82,v105\n"
"v_accvgpr_read_b32 v123,a7\n"
"v_mfma_f32_16x16x32_bf16 a[84:87],v[244:247],v[200:203],a[84:87]\n"
"buffer_load_dwordx4 v112,s[24:27],s76 offen offset:3072 sc1 lds\n"
"v_mfma_f32_16x16x32_bf16 a[84:87],v[180:183],v[136:139],a[84:87]\n"
"s_xor_b32 s76,s76,16384\n"
"v_cvt_pk_bf16_f32 v116,v116,v117\n"
"v_cvt_pk_bf16_f32 v117,v118,v119\n"
"v_mfma_f32_16x16x32_bf16 a[88:91],v[184:187],v[136:139],a[88:91]\n"
"v_cvt_pk_bf16_f32 v118,v120,v121\n"
"v_cvt_pk_bf16_f32 v119,v122,v123\n"
"v_mfma_f32_16x16x32_bf16 a[88:91],v[248:251],v[200:203],a[88:91]\n"
"buffer_store_dwordx4 v[116:119],v112,s[24:27],s56 offen offset:0 sc1\n"
"v_mfma_f32_16x16x32_bf16 a[92:95],v[252:255],v[200:203],a[92:95]\n"
"s_nop 0\n"
"v_accvgpr_read_b32 v116,a8\n"
"v_accvgpr_read_b32 v117,a9\n"
"v_accvgpr_read_b32 v118,a10\n"
"v_mfma_f32_16x16x32_bf16 a[92:95],v[188:191],v[136:139],a[92:95]\n"
"s_waitcnt vmcnt(20)\n"
"v_accvgpr_read_b32 v119,a11\n"
"v_accvgpr_read_b32 v120,a12\n"
"s_barrier\n"
"v_mfma_f32_16x16x32_bf16 a[96:99],v[160:163],v[140:143],a[96:99]\n"
"ds_read_b128 v[0:3],v97 offset:0\n"
"v_accvgpr_read_b32 v121,a13\n"
"v_mfma_f32_16x16x32_bf16 a[96:99],v[224:227],v[204:207],a[96:99]\n"
"ds_read_b128 v[8:11],v97 offset:1024\n"
"v_accvgpr_read_b32 v122,a14\n"
"v_mfma_f32_16x16x32_bf16 a[100:103],v[228:231],v[204:207],a[100:103]\n"
"ds_read_b128 v[16:19],v97 offset:2048\n"
"v_accvgpr_read_b32 v123,a15\n"
"v_mfma_f32_16x16x32_bf16 a[100:103],v[164:167],v[140:143],a[100:103]\n"
"ds_read_b128 v[24:27],v97 offset:3072\n"
"v_cvt_pk_bf16_f32 v116,v116,v117\n"
"v_mfma_f32_16x16x32_bf16 a[104:107],v[168:171],v[140:143],a[104:107]\n"
"ds_read_b128 v[4:7],v97 offset:16384\n"
"v_cvt_pk_bf16_f32 v117,v118,v119\n"
"v_mfma_f32_16x16x32_bf16 a[104:107],v[232:235],v[204:207],a[104:107]\n"
"ds_read_b128 v[12:15],v97 offset:17408\n"
"v_cvt_pk_bf16_f32 v118,v120,v121\n"
"v_mfma_f32_16x16x32_bf16 a[108:111],v[236:239],v[204:207],a[108:111]\n"
"ds_read_b128 v[20:23],v97 offset:18432\n"
"v_cvt_pk_bf16_f32 v119,v122,v123\n"
"v_mfma_f32_16x16x32_bf16 a[108:111],v[172:175],v[140:143],a[108:111]\n"
"ds_read_b128 v[28:31],v97 offset:19456\n"
"buffer_store_dwordx4 v[116:119],v112,s[24:27],s56 offen offset:1024 sc1\n"
"v_mfma_f32_16x16x32_bf16 a[112:115],v[176:179],v[140:143],a[112:115]\n"
"ds_read_b64_tr_b16 v[32:33],v98 offset:49152\n"
"v_accvgpr_read_b32 v116,a16\n"
"v_mfma_f32_16x16x32_bf16 a[112:115],v[240:243],v[204:207],a[112:115]\n"
"ds_read_b64_tr_b16 v[34:35],v98 offset:50176\n"
"v_accvgpr_read_b32 v117,a17\n"
"v_mfma_f32_16x16x32_bf16 a[116:119],v[244:247],v[204:207],a[116:119]\n"
"ds_read_b64_tr_b16 v[36:37],v98 offset:57344\n"
"v_accvgpr_read_b32 v118,a18\n"
"v_mfma_f32_16x16x32_bf16 a[116:119],v[180:183],v[140:143],a[116:119]\n"
"ds_read_b64_tr_b16 v[38:39],v98 offset:58368\n"
"v_accvgpr_read_b32 v119,a19\n"
"v_mfma_f32_16x16x32_bf16 a[120:123],v[184:187],v[140:143],a[120:123]\n"
"ds_read_b64_tr_b16 v[40:41],v99 offset:49152\n"
"v_accvgpr_read_b32 v120,a20\n"
"v_mfma_f32_16x16x32_bf16 a[120:123],v[248:251],v[204:207],a[120:123]\n"
"ds_read_b64_tr_b16 v[42:43],v99 offset:50176\n"
"v_accvgpr_read_b32 v121,a21\n"
"v_mfma_f32_16x16x32_bf16 a[124:127],v[252:255],v[204:207],a[124:127]\n"
"ds_read_b64_tr_b16 v[44:45],v99 offset:57344\n"
"v_accvgpr_read_b32 v122,a22\n"
"v_mfma_f32_16x16x32_bf16 a[124:127],v[188:191],v[140:143],a[124:127]\n"
"ds_read_b64_tr_b16 v[46:47],v99 offset:58368\n"
"v_accvgpr_read_b32 v123,a23\n"
"v_mfma_f32_16x16x32_bf16 a[128:131],v[160:163],v[144:147],a[128:131]\n"
"ds_read_b64_tr_b16 v[48:49],v100 offset:49152\n"
"v_cvt_pk_bf16_f32 v116,v116,v117\n"
"v_mfma_f32_16x16x32_bf16 a[128:131],v[224:227],v[208:211],a[128:131]\n"
"ds_read_b64_tr_b16 v[50:51],v100 offset:50176\n"
"v_cvt_pk_bf16_f32 v117,v118,v119\n"
"v_mfma_f32_16x16x32_bf16 a[132:135],v[228:231],v[208:211],a[132:135]\n"
"ds_read_b64_tr_b16 v[52:53],v100 offset:57344\n"
"v_cvt_pk_bf16_f32 v118,v120,v121\n"
"v_mfma_f32_16x16x32_bf16 a[132:135],v[164:167],v[144:147],a[132:135]\n"
"ds_read_b64_tr_b16 v[54:55],v100 offset:58368\n"
"v_cvt_pk_bf16_f32 v119,v122,v123\n"
"v_mfma_f32_16x16x32_bf16 a[136:139],v[168:171],v[144:147],a[136:139]\n"
"ds_read_b64_tr_b16 v[56:57],v101 offset:49152\n"
"buffer_store_dwordx4 v[116:119],v112,s[24:27],s56 offen offset:2048 sc1\n"
"v_mfma_f32_16x16x32_bf16 a[136:139],v[232:235],v[208:211],a[136:139]\n"
"ds_read_b64_tr_b16 v[58:59],v101 offset:50176\n"
"v_accvgpr_read_b32 v116,a24\n"
"v_mfma_f32_16x16x32_bf16 a[140:143],v[236:239],v[208:211],a[140:143]\n"
"ds_read_b64_tr_b16 v[60:61],v101 offset:57344\n"
"v_accvgpr_read_b32 v117,a25\n"
"v_mfma_f32_16x16x32_bf16 a[140:143],v[172:175],v[144:147],a[140:143]\n"
"ds_read_b64_tr_b16 v[62:63],v101 offset:58368\n"
"v_accvgpr_read_b32 v118,a26\n"
"v_mfma_f32_16x16x32_bf16 a[144:147],v[176:179],v[144:147],a[144:147]\n"
"ds_read_b64_tr_b16 v[64:65],v102 offset:49152\n"
"v_accvgpr_read_b32 v119,a27\n"
"v_mfma_f32_16x16x32_bf16 a[144:147],v[240:243],v[208:211],a[144:147]\n"
"ds_read_b64_tr_b16 v[66:67],v102 offset:50176\n"
"v_accvgpr_read_b32 v120,a28\n"
"v_mfma_f32_16x16x32_bf16 a[148:151],v[244:247],v[208:211],a[148:151]\n"
"ds_read_b64_tr_b16 v[68:69],v102 offset:57344\n"
"v_accvgpr_read_b32 v121,a29\n"
"v_mfma_f32_16x16x32_bf16 a[148:151],v[180:183],v[144:147],a[148:151]\n"
"ds_read_b64_tr_b16 v[70:71],v102 offset:58368\n"
"v_accvgpr_read_b32 v122,a30\n"
"v_mfma_f32_16x16x32_bf16 a[152:155],v[184:187],v[144:147],a[152:155]\n"
"ds_read_b64_tr_b16 v[72:73],v103 offset:49152\n"
"v_accvgpr_read_b32 v123,a31\n"
"v_mfma_f32_16x16x32_bf16 a[152:155],v[248:251],v[208:211],a[152:155]\n"
"ds_read_b64_tr_b16 v[74:75],v103 offset:50176\n"
"v_cvt_pk_bf16_f32 v116,v116,v117\n"
"v_mfma_f32_16x16x32_bf16 a[156:159],v[252:255],v[208:211],a[156:159]\n"
"ds_read_b64_tr_b16 v[76:77],v103 offset:57344\n"
"v_cvt_pk_bf16_f32 v117,v118,v119\n"
"v_mfma_f32_16x16x32_bf16 a[156:159],v[188:191],v[144:147],a[156:159]\n"
"ds_read_b64_tr_b16 v[78:79],v103 offset:58368\n"
"v_cvt_pk_bf16_f32 v118,v120,v121\n"
"v_mfma_f32_16x16x32_bf16 a[160:163],v[160:163],v[148:151],a[160:163]\n"
"ds_read_b64_tr_b16 v[80:81],v104 offset:49152\n"
"v_cvt_pk_bf16_f32 v119,v122,v123\n"
"v_mfma_f32_16x16x32_bf16 a[160:163],v[224:227],v[212:215],a[160:163]\n"
"ds_read_b64_tr_b16 v[82:83],v104 offset:50176\n"
"buffer_store_dwordx4 v[116:119],v112,s[24:27],s56 offen offset:3072 sc1\n"
"v_mfma_f32_16x16x32_bf16 a[164:167],v[228:231],v[212:215],a[164:167]\n"
"ds_read_b64_tr_b16 v[84:85],v104 offset:57344\n"
"v_mfma_f32_16x16x32_bf16 a[164:167],v[164:167],v[148:151],a[164:167]\n"
"ds_read_b64_tr_b16 v[86:87],v104 offset:58368\n"
"v_mfma_f32_16x16x32_bf16 a[168:171],v[168:171],v[148:151],a[168:171]\n"
"ds_read_b64_tr_b16 v[88:89],v105 offset:49152\n"
"v_mfma_f32_16x16x32_bf16 a[168:171],v[232:235],v[212:215],a[168:171]\n"
"ds_read_b64_tr_b16 v[90:91],v105 offset:50176\n"
"v_mfma_f32_16x16x32_bf16 a[172:175],v[236:239],v[212:215],a[172:175]\n"
"ds_read_b64_tr_b16 v[92:93],v105 offset:57344\n"
"v_mfma_f32_16x16x32_bf16 a[172:175],v[172:175],v[148:151],a[172:175]\n"
"ds_read_b64_tr_b16 v[94:95],v105 offset:58368\n"
"v_mfma_f32_16x16x32_bf16 a[176:179],v[176:179],v[148:151],a[176:179]\n"
"s_add_u32 m0,s85,106496\n"
"v_mfma_f32_16x16x32_bf16 a[176:179],v[240:243],v[212:215],a[176:179]\n"
"buffer_load_dwordx4 v110,s[28:31],s86 offen lds\n"
"v_mfma_f32_16x16x32_bf16 a[180:183],v[244:247],v[212:215],a[180:183]\n"
"v_mfma_f32_16x16x32_bf16 a[180:183],v[180:183],v[148:151],a[180:183]\n"
"s_add_u32 m0,s85,107520\n"
"v_mfma_f32_16x16x32_bf16 a[184:187],v[184:187],v[148:151],a[184:187]\n"
"buffer_load_dwordx4 v111,s[28:31],s86 offen lds\n"
"v_mfma_f32_16x16x32_bf16 a[184:187],v[248:251],v[212:215],a[184:187]\n"
"s_xor_b32 s86,s86,786432\n"
"v_mfma_f32_16x16x32_bf16 a[188:191],v[252:255],v[212:215],a[188:191]\n"
"s_add_u32 m0,s78,49152\n"
"v_mfma_f32_16x16x32_bf16 a[188:191],v[188:191],v[148:151],a[188:191]\n"
"buffer_load_dwordx4 v112,s[24:27],s76 offen offset:0 sc1 lds\n"
"v_mfma_f32_16x16x32_bf16 a[192:195],v[224:227],v[216:219],a[192:195]\n"
"v_mfma_f32_16x16x32_bf16 a[224:227],v[224:227],v[220:223],a[224:227]\n"
"v_mfma_f32_16x16x32_bf16 a[228:231],v[228:231],v[220:223],a[228:231]\n"
"buffer_load_dwordx4 v112,s[24:27],s76 offen offset:1024 sc1 lds\n"
"v_mfma_f32_16x16x32_bf16 a[196:199],v[228:231],v[216:219],a[196:199]\n"
"v_mfma_f32_16x16x32_bf16 a[200:203],v[232:235],v[216:219],a[200:203]\n"
"v_mfma_f32_16x16x32_bf16 a[232:235],v[232:235],v[220:223],a[232:235]\n"
"buffer_load_dwordx4 v112,s[24:27],s76 offen offset:2048 sc1 lds\n"
"v_mfma_f32_16x16x32_bf16 a[236:239],v[236:239],v[220:223],a[236:239]\n"
"v_mfma_f32_16x16x32_bf16 a[204:207],v[236:239],v[216:219],a[204:207]\n"
"buffer_load_dwordx4 v112,s[24:27],s76 offen offset:3072 sc1 lds\n"
"v_mfma_f32_16x16x32_bf16 a[208:211],v[240:243],v[216:219],a[208:211]\n"
"s_xor_b32 s76,s76,49152\n"
"v_mfma_f32_16x16x32_bf16 a[240:243],v[240:243],v[220:223],a[240:243]\n"
"v_mfma_f32_16x16x32_bf16 a[244:247],v[244:247],v[220:223],a[244:247]\n"
"v_mfma_f32_16x16x32_bf16 a[212:215],v[244:247],v[216:219],a[212:215]\n"
"v_mfma_f32_16x16x32_bf16 a[216:219],v[248:251],v[216:219],a[216:219]\n"
"v_mfma_f32_16x16x32_bf16 a[248:251],v[248:251],v[220:223],a[248:251]\n"
"v_mfma_f32_16x16x32_bf16 a[252:255],v[252:255],v[220:223],a[252:255]\n"
"v_mfma_f32_16x16x32_bf16 a[220:223],v[252:255],v[216:219],a[220:223]\n"
"s_branch .Lfk_ph2\n"
".Lfk_v:\n"
"s_waitcnt vmcnt(21)\n"
"s_barrier\n"
"ds_read_b128 v[152:155],v80 offset:12288\n"
"ds_read_b128 v[156:159],v80 offset:14336\n"
"ds_read_b64_tr_b16 v[160:161],v98 offset:49152\n"
"ds_read_b64_tr_b16 v[162:163],v98 offset:50176\n"
"ds_read_b64_tr_b16 v[164:165],v99 offset:49152\n"
"ds_read_b64_tr_b16 v[166:167],v99 offset:50176\n"
"ds_read_b64_tr_b16 v[168:169],v100 offset:49152\n"
"ds_read_b64_tr_b16 v[170:171],v100 offset:50176\n"
"ds_read_b64_tr_b16 v[172:173],v101 offset:49152\n"
"ds_read_b64_tr_b16 v[174:175],v101 offset:50176\n"
"ds_read_b64_tr_b16 v[176:177],v102 offset:49152\n"
"ds_read_b64_tr_b16 v[178:179],v102 offset:50176\n"
"ds_read_b64_tr_b16 v[180:181],v103 offset:49152\n"
"ds_read_b64_tr_b16 v[182:183],v103 offset:50176\n"
"ds_read_b64_tr_b16 v[184:185],v104 offset:49152\n"
"ds_read_b64_tr_b16 v[186:187],v104 offset:50176\n"
"ds_read_b64_tr_b16 v[188:189],v105 offset:49152\n"
"ds_read_b64_tr_b16 v[190:191],v105 offset:50176\n"
"s_waitcnt lgkmcnt(12)\n"
"v_mfma_f32_16x16x32_bf16 a[192:195],v[152:155],v[160:163],0\n"
"s_and_b32 s91,s89,7\n"
"s_mov_b32 s77,524288\n"
"s_add_u32 m0,s78,98304\n"
"v_mfma_f32_16x16x32_bf16 a[196:199],v[156:159],v[160:163],0\n"
"buffer_load_dwordx4 v110,s[20:23],s77 offen offset:0 lds\n"
"v_mfma_f32_16x16x32_bf16 a[204:207],v[156:159],v[164:167],0\n"
"s_lshr_b32 s97,s89,3\n"
"s_lshl2_add_u32 s90,s88,s97\n"
"v_mfma_f32_16x16x32_bf16 a[200:203],v[152:155],v[164:167],0\n"
"buffer_load_dwordx4 v111,s[20:23],s77 offen offset:1024 lds\n"
"s_waitcnt lgkmcnt(8)\n"
"v_mfma_f32_16x16x32_bf16 a[208:211],v[152:155],v[168:171],0\n"
"s_lshl_b32 vcc_lo,s90,8\n"
"s_add_u32 s28,s28,vcc_lo\n"
"s_addc_u32 s29,s29,0\n"
"v_mfma_f32_16x16x32_bf16 a[212:215],v[156:159],v[168:171],0\n"
"buffer_load_dwordx4 v113,s[20:23],s77 offen offset:2048 lds\n"
"v_mfma_f32_16x16x32_bf16 a[220:223],v[156:159],v[172:175],0\n"
"s_sub_u32 s30,8388608,vcc_lo\n"
"s_mov_b32 s31,0x20000\n"
"buffer_load_dwordx4 v114,s[20:23],s77 offen offset:3072 lds\n"
"v_mfma_f32_16x16x32_bf16 a[216:219],v[152:155],v[172:175],0\n"
"s_waitcnt vmcnt(18)\n"
"s_barrier\n"
"s_waitcnt lgkmcnt(4)\n"
"v_mfma_f32_16x16x32_bf16 a[224:227],v[152:155],v[176:179],0\n"
"ds_read_b128 v[128:131],v80 offset:0\n"
"ds_read_b128 v[192:195],v81 offset:0\n"
"v_mfma_f32_16x16x32_bf16 a[228:231],v[156:159],v[176:179],0\n"
"ds_read_b64_tr_b16 v[224:225],v98 offset:32768\n"
"ds_read_b64_tr_b16 v[226:227],v98 offset:33792\n"
"v_mfma_f32_16x16x32_bf16 a[236:239],v[156:159],v[180:183],0\n"
"ds_read_b64_tr_b16 v[228:229],v99 offset:32768\n"
"ds_read_b64_tr_b16 v[230:231],v99 offset:33792\n"
"v_mfma_f32_16x16x32_bf16 a[232:235],v[152:155],v[180:183],0\n"
"ds_read_b64_tr_b16 v[232:233],v100 offset:32768\n"
"ds_read_b64_tr_b16 v[234:235],v100 offset:33792\n"
"s_waitcnt lgkmcnt(8)\n"
"v_mfma_f32_16x16x32_bf16 a[240:243],v[152:155],v[184:187],0\n"
"ds_read_b64_tr_b16 v[236:237],v101 offset:32768\n"
"ds_read_b64_tr_b16 v[238:239],v101 offset:33792\n"
"v_mfma_f32_16x16x32_bf16 a[244:247],v[156:159],v[184:187],0\n"
"ds_read_b64_tr_b16 v[240:241],v102 offset:32768\n"
"ds_read_b64_tr_b16 v[242:243],v102 offset:33792\n"
"v_mfma_f32_16x16x32_bf16 a[252:255],v[156:159],v[188:191],0\n"
"ds_read_b64_tr_b16 v[244:245],v103 offset:32768\n"
"ds_read_b64_tr_b16 v[246:247],v103 offset:33792\n"
"v_mfma_f32_16x16x32_bf16 a[248:251],v[152:155],v[188:191],0\n"
"ds_read_b64_tr_b16 v[248:249],v104 offset:32768\n"
"ds_read_b64_tr_b16 v[250:251],v104 offset:33792\n"
"s_waitcnt lgkmcnt(10)\n"
"v_mfma_f32_16x16x32_bf16 a[0:3],v[128:131],v[160:163],0\n"
"ds_read_b64_tr_b16 v[252:253],v105 offset:32768\n"
"ds_read_b64_tr_b16 v[254:255],v105 offset:33792\n"
"v_mfma_f32_16x16x32_bf16 a[0:3],v[192:195],v[224:227],a[0:3]\n"
"ds_read_b128 v[132:135],v80 offset:2048\n"
"ds_read_b128 v[196:199],v81 offset:2048\n"
"v_mfma_f32_16x16x32_bf16 a[8:11],v[192:195],v[228:231],0\n"
"ds_read_b128 v[216:219],v81 offset:12288\n"
"ds_read_b128 v[220:223],v81 offset:14336\n"
"v_mfma_f32_16x16x32_bf16 a[8:11],v[128:131],v[164:167],a[8:11]\n"
"s_waitcnt lgkmcnt(12)\n"
"s_mov_b32 s77,786432\n"
"s_add_u32 m0,s78,114688\n"
"v_mfma_f32_16x16x32_bf16 a[16:19],v[128:131],v[168:171],0\n"
"buffer_load_dwordx4 v110,s[20:23],s77 offen offset:0 lds\n"
"v_mfma_f32_16x16x32_bf16 a[16:19],v[192:195],v[232:235],a[16:19]\n"
"s_mov_b32 s60,s91\n"
"s_sub_u32 s94,15,s91\n"
"v_mfma_f32_16x16x32_bf16 a[24:27],v[192:195],v[236:239],0\n"
"buffer_load_dwordx4 v111,s[20:23],s77 offen offset:1024 lds\n"
"v_mfma_f32_16x16x32_bf16 a[24:27],v[128:131],v[172:175],a[24:27]\n"
"s_cmp_lt_u32 s52,2\n"
"s_cselect_b32 s95,s94,s91\n"
"s_waitcnt lgkmcnt(8)\n"
"v_mfma_f32_16x16x32_bf16 a[32:35],v[128:131],v[176:179],0\n"
"buffer_load_dwordx4 v113,s[20:23],s77 offen offset:2048 lds\n"
"v_mfma_f32_16x16x32_bf16 a[32:35],v[192:195],v[240:243],a[32:35]\n"
"s_lshr_b32 vcc_lo,s95,1\n"
"s_lshl_b32 s55,vcc_lo,19\n"
"v_mfma_f32_16x16x32_bf16 a[40:43],v[192:195],v[244:247],0\n"
"buffer_load_dwordx4 v114,s[20:23],s77 offen offset:3072 lds\n"
"v_mfma_f32_16x16x32_bf16 a[40:43],v[128:131],v[180:183],a[40:43]\n"
"s_waitcnt lgkmcnt(4)\n"
"s_mov_b32 s76,256\n"
"s_add_u32 m0,s79,65536\n"
"v_mfma_f32_16x16x32_bf16 a[48:51],v[128:131],v[184:187],0\n"
"buffer_load_dwordx4 v106,s[16:19],s76 offen offset:0 lds\n"
"v_mfma_f32_16x16x32_bf16 a[48:51],v[192:195],v[248:251],a[48:51]\n"
"s_lshl_b32 s58,vcc_lo,10\n"
"s_add_u32 s58,s58,148898816\n"
"v_mfma_f32_16x16x32_bf16 a[56:59],v[192:195],v[252:255],0\n"
"buffer_load_dwordx4 v107,s[16:19],s76 offen offset:1024 lds\n"
"v_mfma_f32_16x16x32_bf16 a[56:59],v[128:131],v[188:191],a[56:59]\n"
"s_and_b32 vcc_lo,s95,1\n"
"s_waitcnt lgkmcnt(2)\n"
"v_mfma_f32_16x16x32_bf16 a[4:7],v[132:135],v[160:163],0\n"
"buffer_load_dwordx4 v108,s[16:19],s76 offen offset:2048 lds\n"
"v_mfma_f32_16x16x32_bf16 a[4:7],v[196:199],v[224:227],a[4:7]\n"
"s_lshl_b32 vcc_lo,vcc_lo,13\n"
"s_and_b32 vcc_hi,s78,4096\n"
"v_mfma_f32_16x16x32_bf16 a[12:15],v[196:199],v[228:231],0\n"
"buffer_load_dwordx4 v109,s[16:19],s76 offen offset:3072 lds\n"
"v_mfma_f32_16x16x32_bf16 a[12:15],v[132:135],v[164:167],a[12:15]\n"
"s_waitcnt vmcnt(21)\n"
"s_barrier\n"
"v_mfma_f32_16x16x32_bf16 a[20:23],v[132:135],v[168:171],0\n"
"ds_read_b128 v[136:139],v80 offset:4096\n"
"ds_read_b128 v[200:203],v81 offset:4096\n"
"v_mfma_f32_16x16x32_bf16 a[20:23],v[196:199],v[232:235],a[20:23]\n"
"ds_read_b128 v[140:143],v80 offset:6144\n"
"ds_read_b128 v[204:207],v81 offset:6144\n"
"v_mfma_f32_16x16x32_bf16 a[28:31],v[196:199],v[236:239],0\n"
"ds_read_b128 v[144:147],v80 offset:8192\n"
"ds_read_b128 v[208:211],v81 offset:8192\n"
"v_mfma_f32_16x16x32_bf16 a[28:31],v[132:135],v[172:175],a[28:31]\n"
"ds_read_b128 v[148:151],v80 offset:10240\n"
"ds_read_b128 v[212:215],v81 offset:10240\n"
"v_mfma_f32_16x16x32_bf16 a[36:39],v[132:135],v[176:179],0\n"
"s_add_u32 s55,s55,vcc_lo\n"
"s_mov_b32 s76,262400\n"
"s_add_u32 m0,s79,69632\n"
"v_mfma_f32_16x16x32_bf16 a[36:39],v[196:199],v[240:243],a[36:39]\n"
"buffer_load_dwordx4 v106,s[16:19],s76 offen offset:0 lds\n"
"v_mfma_f32_16x16x32_bf16 a[44:47],v[196:199],v[244:247],0\n"
"buffer_load_dwordx4 v107,s[16:19],s76 offen offset:1024 lds\n"
"v_mfma_f32_16x16x32_bf16 a[44:47],v[132:135],v[180:183],a[44:47]\n"
"buffer_load_dwordx4 v108,s[16:19],s76 offen offset:2048 lds\n"
"v_mfma_f32_16x16x32_bf16 a[52:55],v[132:135],v[184:187],0\n"
"buffer_load_dwordx4 v109,s[16:19],s76 offen offset:3072 lds\n"
"v_mfma_f32_16x16x32_bf16 a[52:55],v[196:199],v[248:251],a[52:55]\n"
"s_add_u32 s55,s55,vcc_hi\n"
"s_waitcnt lgkmcnt(0)\n"
"s_barrier\n"
"v_mfma_f32_16x16x32_bf16 a[60:63],v[196:199],v[252:255],0\n"
"s_and_b32 vcc_lo,s89,2\n"
"s_mov_b32 s77,1048576\n"
"s_add_u32 m0,s78,147456\n"
"v_mfma_f32_16x16x32_bf16 a[60:63],v[132:135],v[188:191],a[60:63]\n"
"buffer_load_dwordx4 v110,s[20:23],s77 offen offset:0 lds\n"
"v_mfma_f32_16x16x32_bf16 a[64:67],v[136:139],v[160:163],0\n"
"s_lshl2_add_u32 vcc_lo,s88,vcc_lo\n"
"s_and_b32 vcc_hi,s52,1\n"
"v_mfma_f32_16x16x32_bf16 a[64:67],v[200:203],v[224:227],a[64:67]\n"
"s_add_u32 vcc_lo,vcc_lo,vcc_hi\n"
"s_lshr_b32 vcc_hi,s89,2\n"
"v_mfma_f32_16x16x32_bf16 a[72:75],v[200:203],v[228:231],0\n"
"s_lshr_b32 s84,s52,1\n"
"s_lshl1_add_u32 vcc_hi,vcc_hi,s84\n"
"v_mfma_f32_16x16x32_bf16 a[72:75],v[136:139],v[164:167],a[72:75]\n"
"buffer_load_dwordx4 v111,s[20:23],s77 offen offset:1024 lds\n"
"v_mfma_f32_16x16x32_bf16 a[80:83],v[136:139],v[168:171],0\n"
"s_mul_i32 s56,vcc_lo,1179648\n"
"s_lshl_b32 s84,s84,19\n"
"v_mfma_f32_16x16x32_bf16 a[80:83],v[200:203],v[232:235],a[80:83]\n"
"s_add_u32 s56,s56,s84\n"
"s_lshr_b32 s84,s89,2\n"
"v_mfma_f32_16x16x32_bf16 a[88:91],v[200:203],v[236:239],0\n"
"s_lshl_b32 s84,s84,14\n"
"s_add_u32 s56,s56,s84\n"
"v_mfma_f32_16x16x32_bf16 a[88:91],v[136:139],v[172:175],a[88:91]\n"
"buffer_load_dwordx4 v113,s[20:23],s77 offen offset:2048 lds\n"
"v_mfma_f32_16x16x32_bf16 a[96:99],v[136:139],v[176:179],0\n"
"s_lshl_b32 s84,s53,13\n"
"s_add_u32 s56,s56,s84\n"
"v_mfma_f32_16x16x32_bf16 a[96:99],v[200:203],v[240:243],a[96:99]\n"
"s_add_u32 s56,s56,4194304\n"
"s_lshl4_add_u32 vcc_hi,s53,vcc_hi\n"
"v_mfma_f32_16x16x32_bf16 a[104:107],v[200:203],v[244:247],0\n"
"s_lshl_b32 vcc_lo,vcc_lo,8\n"
"s_lshl3_add_u32 s59,vcc_hi,vcc_lo\n"
"v_mfma_f32_16x16x32_bf16 a[104:107],v[136:139],v[180:183],a[104:107]\n"
"buffer_load_dwordx4 v114,s[20:23],s77 offen offset:3072 lds\n"
"v_mfma_f32_16x16x32_bf16 a[112:115],v[136:139],v[184:187],0\n"
"s_add_u32 s59,s59,148914176\n"
"s_lshl_b32 s57,s92,3\n"
"v_mfma_f32_16x16x32_bf16 a[112:115],v[200:203],v[248:251],a[112:115]\n"
"s_add_u32 s57,s57,148898816\n"
"s_lshl_b32 s63,s90,8\n"
"v_mfma_f32_16x16x32_bf16 a[120:123],v[200:203],v[252:255],0\n"
"s_add_u32 s63,s63,148914176\n"
"s_mov_b32 s77,1310720\n"
"s_add_u32 m0,s78,131072\n"
"v_mfma_f32_16x16x32_bf16 a[120:123],v[136:139],v[188:191],a[120:123]\n"
"buffer_load_dwordx4 v110,s[20:23],s77 offen offset:0 lds\n"
"v_mfma_f32_16x16x32_bf16 a[68:71],v[140:143],v[160:163],0\n"
"s_lshl_b32 s74,s92,11\n"
"s_add_u32 s74,s74,171966464\n"
"v_mfma_f32_16x16x32_bf16 a[68:71],v[204:207],v[224:227],a[68:71]\n"
"s_mul_i32 s96,s90,1179648\n"
"s_add_u32 s96,s96,4194304\n"
"v_mfma_f32_16x16x32_bf16 a[76:79],v[204:207],v[228:231],0\n"
"buffer_load_dwordx4 v111,s[20:23],s77 offen offset:1024 lds\n"
"v_mfma_f32_16x16x32_bf16 a[76:79],v[140:143],v[164:167],a[76:79]\n"
"s_add_u32 s73,s96,s85\n"
"s_and_b32 vcc_hi,s78,4096\n"
"v_mfma_f32_16x16x32_bf16 a[84:87],v[140:143],v[168:171],0\n"
"s_lshl_b32 vcc_hi,vcc_hi,1\n"
"s_add_u32 vcc_lo,s96,vcc_hi\n"
"v_mfma_f32_16x16x32_bf16 a[84:87],v[204:207],v[232:235],a[84:87]\n"
"s_add_u32 s65,vcc_lo,524288\n"
"s_lshl_b32 s66,s91,16\n"
"v_mfma_f32_16x16x32_bf16 a[92:95],v[204:207],v[236:239],0\n"
"buffer_load_dwordx4 v113,s[20:23],s77 offen offset:2048 lds\n"
"v_mfma_f32_16x16x32_bf16 a[92:95],v[140:143],v[172:175],a[92:95]\n"
"s_sub_u32 s66,vcc_lo,s66\n"
"s_add_u32 s66,s66,999424\n"
"v_mfma_f32_16x16x32_bf16 a[100:103],v[140:143],v[176:179],0\n"
"s_add_u32 s67,vcc_hi,65536\n"
"s_mul_i32 vcc_lo,vcc_hi,5\n"
"v_mfma_f32_16x16x32_bf16 a[100:103],v[204:207],v[240:243],a[100:103]\n"
"s_sub_u32 vcc_lo,s96,vcc_lo\n"
"s_add_u32 s64,vcc_lo,81920\n"
"v_mfma_f32_16x16x32_bf16 a[108:111],v[204:207],v[244:247],0\n"
"buffer_load_dwordx4 v114,s[20:23],s77 offen offset:3072 lds\n"
"v_mfma_f32_16x16x32_bf16 a[108:111],v[140:143],v[180:183],a[108:111]\n"
"s_add_u32 s62,vcc_lo,135168\n"
"s_add_u32 s61,vcc_lo,524288\n"
"s_cmp_lt_u32 s52,2\n"
"v_mfma_f32_16x16x32_bf16 a[116:119],v[140:143],v[184:187],0\n"
"s_cselect_b32 s64,s64,2146435072\n"
"s_cselect_b32 s67,s67,163840\n"
"s_cselect_b32 s65,s65,1\n"
"s_cselect_b32 vcc_lo,4,0\n"
"s_and_b32 vcc_hi,s52,1\n"
"v_mfma_f32_16x16x32_bf16 a[116:119],v[204:207],v[248:251],a[116:119]\n"
"s_add_u32 vcc_lo,vcc_lo,vcc_hi\n"
"s_mov_b32 s76,384\n"
"s_add_u32 m0,s79,0\n"
"v_mfma_f32_16x16x32_bf16 a[124:127],v[204:207],v[252:255],0\n"
"buffer_load_dwordx4 v106,s[16:19],s76 offen offset:0 lds\n"
"v_mfma_f32_16x16x32_bf16 a[124:127],v[140:143],v[188:191],a[124:127]\n"
"s_lshl_b32 vcc_hi,s90,12\n"
"s_add_u32 vcc_hi,vcc_hi,54525952\n"
"v_mfma_f32_16x16x32_bf16 a[128:131],v[144:147],v[160:163],0\n"
"s_lshl3_add_u32 s70,s95,vcc_lo\n"
"s_lshl_b32 s70,s70,17\n"
"v_mfma_f32_16x16x32_bf16 a[128:131],v[208:211],v[224:227],a[128:131]\n"
"s_add_u32 s70,s70,vcc_hi\n"
"s_lshl3_add_u32 s68,s94,vcc_lo\n"
"v_mfma_f32_16x16x32_bf16 a[136:139],v[208:211],v[228:231],0\n"
"buffer_load_dwordx4 v107,s[16:19],s76 offen offset:1024 lds\n"
"v_mfma_f32_16x16x32_bf16 a[136:139],v[144:147],v[164:167],a[136:139]\n"
"s_lshl_b32 s68,s68,17\n"
"s_add_u32 s68,s68,vcc_hi\n"
"v_mfma_f32_16x16x32_bf16 a[144:147],v[144:147],v[168:171],0\n"
"s_lshr_b32 vcc_lo,s90,3\n"
"s_lshl2_add_u32 vcc_lo,vcc_lo,s52\n"
"v_mfma_f32_16x16x32_bf16 a[144:147],v[208:211],v[232:235],a[144:147]\n"
"s_lshl_b32 vcc_lo,vcc_lo,7\n"
"s_and_b32 vcc_hi,s90,7\n"
"v_mfma_f32_16x16x32_bf16 a[152:155],v[208:211],v[236:239],0\n"
"buffer_load_dwordx4 v108,s[16:19],s76 offen offset:2048 lds\n"
"v_mfma_f32_16x16x32_bf16 a[152:155],v[144:147],v[172:175],a[152:155]\n"
"s_add_u32 vcc_lo,vcc_lo,vcc_hi\n"
"s_lshl3_add_u32 s69,s91,vcc_lo\n"
"v_mfma_f32_16x16x32_bf16 a[160:163],v[144:147],v[176:179],0\n"
"s_lshl_b32 s69,s69,3\n"
"s_add_u32 s69,s69,148930560\n"
"v_mfma_f32_16x16x32_bf16 a[160:163],v[208:211],v[240:243],a[160:163]\n"
"s_lshl3_add_u32 s71,s94,vcc_lo\n"
"s_lshl_b32 s71,s71,3\n"
"v_mfma_f32_16x16x32_bf16 a[168:171],v[208:211],v[244:247],0\n"
"buffer_load_dwordx4 v109,s[16:19],s76 offen offset:3072 lds\n"
"v_mfma_f32_16x16x32_bf16 a[168:171],v[144:147],v[180:183],a[168:171]\n"
"s_add_u32 s71,s71,148930560\n"
"s_mov_b32 s6,8388608\n"
"v_mfma_f32_16x16x32_bf16 a[176:179],v[144:147],v[184:187],0\n"
"s_mov_b32 s7,0x20000\n"
"s_lshr_b32 s94,s88,1\n"
"v_mfma_f32_16x16x32_bf16 a[176:179],v[208:211],v[248:251],a[176:179]\n"
"s_and_b32 vcc_lo,s91,1\n"
"s_mov_b32 s76,262528\n"
"s_add_u32 m0,s79,4096\n"
"v_mfma_f32_16x16x32_bf16 a[184:187],v[208:211],v[252:255],0\n"
"buffer_load_dwordx4 v106,s[16:19],s76 offen offset:0 lds\n"
"v_mfma_f32_16x16x32_bf16 a[184:187],v[144:147],v[188:191],a[184:187]\n"
"s_lshl1_add_u32 s95,s97,vcc_lo\n"
"s_and_b32 vcc_lo,s88,1\n"
"v_mfma_f32_16x16x32_bf16 a[132:135],v[148:151],v[160:163],0\n"
"s_lshr_b32 vcc_hi,s91,1\n"
"s_lshl2_add_u32 s96,vcc_lo,vcc_hi\n"
"v_mfma_f32_16x16x32_bf16 a[132:135],v[212:215],v[224:227],a[132:135]\n"
"s_lshr_b32 vcc_lo,s88,2\n"
"s_lshl2_add_u32 s96,vcc_lo,vcc_hi\n"
"v_mfma_f32_16x16x32_bf16 a[140:143],v[212:215],v[228:231],0\n"
"buffer_load_dwordx4 v107,s[16:19],s76 offen offset:1024 lds\n"
"v_mfma_f32_16x16x32_bf16 a[140:143],v[148:151],v[164:167],a[140:143]\n"
"s_sub_u32 vcc_lo,7,s88\n"
"s_min_u32 s94,vcc_lo,s88\n"
"v_mfma_f32_16x16x32_bf16 a[148:151],v[148:151],v[168:171],0\n"
"s_lshl3_add_u32 vcc_lo,s94,s96\n"
"s_lshl3_add_u32 vcc_lo,vcc_lo,s52\n"
"v_mfma_f32_16x16x32_bf16 a[148:151],v[212:215],v[232:235],a[148:151]\n"
"s_lshl_b32 s13,vcc_lo,15\n"
"s_lshl_b32 vcc_lo,s95,8\n"
"v_mfma_f32_16x16x32_bf16 a[156:159],v[212:215],v[236:239],0\n"
"buffer_load_dwordx4 v108,s[16:19],s76 offen offset:2048 lds\n"
"v_mfma_f32_16x16x32_bf16 a[156:159],v[148:151],v[172:175],a[156:159]\n"
"s_add_u32 s13,s13,vcc_lo\n"
"s_lshl3_add_u32 vcc_hi,s94,s95\n"
"v_mfma_f32_16x16x32_bf16 a[164:167],v[148:151],v[176:179],0\n"
"s_lshl_b32 s72,vcc_hi,8\n"
"s_add_u32 s72,s72,149061632\n"
"v_mfma_f32_16x16x32_bf16 a[164:167],v[212:215],v[240:243],a[164:167]\n"
"s_or_b32 vcc_lo,s96,s52\n"
"s_cselect_b32 s72,2146435072,s72\n"
"s_lshl_b32 s3,vcc_hi,18\n"
"v_mfma_f32_16x16x32_bf16 a[172:175],v[212:215],v[244:247],0\n"
"buffer_load_dwordx4 v109,s[16:19],s76 offen offset:3072 lds\n"
"v_mfma_f32_16x16x32_bf16 a[172:175],v[148:151],v[180:183],a[172:175]\n"
"s_lshl_b32 vcc_lo,s96,15\n"
"s_sub_u32 s3,s3,vcc_lo\n"
"s_add_u32 s3,s3,s78\n"
"v_mfma_f32_16x16x32_bf16 a[180:183],v[148:151],v[184:187],0\n"
"buffer_load_dwordx4 v[8:11],v70,s[32:35],0 offen offset:0\n"
"v_mfma_f32_16x16x32_bf16 a[180:183],v[212:215],v[248:251],a[180:183]\n"
"buffer_load_dwordx4 v[12:15],v70,s[32:35],0 offen offset:512\n"
"s_add_u32 s3,s3,100909056\n"
"v_mfma_f32_16x16x32_bf16 a[188:191],v[212:215],v[252:255],0\n"
"buffer_load_dwordx4 v[16:19],v70,s[32:35],0 offen offset:1024\n"
"buffer_load_dwordx4 v[20:23],v70,s[32:35],0 offen offset:1536\n"
"s_lshl3_add_u32 vcc_hi,vcc_hi,s96\n"
"v_mfma_f32_16x16x32_bf16 a[188:191],v[148:151],v[188:191],a[188:191]\n"
"s_waitcnt vmcnt(28)\n"
"s_barrier\n"
"v_mfma_f32_16x16x32_bf16 a[192:195],v[216:219],v[224:227],a[192:195]\n"
"ds_read_b128 v[152:155],v80 offset:45056\n"
"s_mov_b32 s87,524288\n"
"v_mfma_f32_16x16x32_bf16 a[196:199],v[220:223],v[224:227],a[196:199]\n"
"ds_read_b128 v[156:159],v80 offset:47104\n"
"buffer_load_dword v115,v112,s[36:39],s87 offen\n"
"v_mfma_f32_16x16x32_bf16 a[204:207],v[220:223],v[228:231],a[204:207]\n"
"ds_read_b64_tr_b16 v[160:161],v98 offset:0\n"
"s_mov_b32 s87,1048576\n"
"v_mfma_f32_16x16x32_bf16 a[200:203],v[216:219],v[228:231],a[200:203]\n"
"ds_read_b64_tr_b16 v[162:163],v98 offset:1024\n"
"buffer_load_dword v115,v112,s[36:39],s87 offen\n"
"v_mfma_f32_16x16x32_bf16 a[208:211],v[216:219],v[232:235],a[208:211]\n"
"ds_read_b64_tr_b16 v[164:165],v99 offset:0\n"
"s_mov_b32 s87,1572864\n"
"v_mfma_f32_16x16x32_bf16 a[212:215],v[220:223],v[232:235],a[212:215]\n"
"ds_read_b64_tr_b16 v[166:167],v99 offset:1024\n"
"buffer_load_dword v115,v112,s[36:39],s87 offen\n"
"v_mfma_f32_16x16x32_bf16 a[220:223],v[220:223],v[236:239],a[220:223]\n"
"ds_read_b64_tr_b16 v[168:169],v100 offset:0\n"
"s_lshl2_add_u32 vcc_hi,vcc_hi,s52\n"
"v_mfma_f32_16x16x32_bf16 a[216:219],v[216:219],v[236:239],a[216:219]\n"
"ds_read_b64_tr_b16 v[170:171],v100 offset:1024\n"
"s_lshl_b32 s2,vcc_hi,4\n"
"v_mfma_f32_16x16x32_bf16 a[224:227],v[216:219],v[240:243],a[224:227]\n"
"ds_read_b64_tr_b16 v[172:173],v101 offset:0\n"
"s_add_u32 s2,s2,149028864\n"
"v_mfma_f32_16x16x32_bf16 a[228:231],v[220:223],v[240:243],a[228:231]\n"
"ds_read_b64_tr_b16 v[174:175],v101 offset:1024\n"
"s_lshl2_add_u32 vcc_lo,s95,s52\n"
"v_mfma_f32_16x16x32_bf16 a[236:239],v[220:223],v[244:247],a[236:239]\n"
"ds_read_b64_tr_b16 v[176:177],v102 offset:0\n"
"s_lshl_b32 vcc_lo,vcc_lo,10\n"
"v_mfma_f32_16x16x32_bf16 a[232:235],v[216:219],v[244:247],a[232:235]\n"
"ds_read_b64_tr_b16 v[178:179],v102 offset:1024\n"
"s_lshl_b32 vcc_hi,s94,22\n"
"v_mfma_f32_16x16x32_bf16 a[240:243],v[216:219],v[248:251],a[240:243]\n"
"ds_read_b64_tr_b16 v[180:181],v103 offset:0\n"
"s_add_u32 s12,vcc_lo,vcc_hi\n"
"v_mfma_f32_16x16x32_bf16 a[244:247],v[220:223],v[248:251],a[244:247]\n"
"ds_read_b64_tr_b16 v[182:183],v103 offset:1024\n"
"s_lshl_b32 vcc_lo,s96,19\n"
"v_mfma_f32_16x16x32_bf16 a[252:255],v[220:223],v[252:255],a[252:255]\n"
"ds_read_b64_tr_b16 v[184:185],v104 offset:0\n"
"s_add_u32 s10,s12,vcc_lo\n"
"v_mfma_f32_16x16x32_bf16 a[248:251],v[216:219],v[252:255],a[248:251]\n"
"s_add_u32 s10,s10,71303168\n"
"ds_read_b64_tr_b16 v[186:187],v104 offset:1024\n"
"s_waitcnt lgkmcnt(10)\n"
"v_mfma_f32_16x16x32_bf16 a[192:195],v[152:155],v[160:163],a[192:195]\n"
"ds_read_b64_tr_b16 v[188:189],v105 offset:0\n"
"s_mul_i32 vcc_lo,s52,31744\n"
"v_mfma_f32_16x16x32_bf16 a[196:199],v[156:159],v[160:163],a[196:199]\n"
"ds_read_b64_tr_b16 v[190:191],v105 offset:1024\n"
"s_add_u32 s10,s10,vcc_lo\n"
"v_mfma_f32_16x16x32_bf16 a[204:207],v[156:159],v[164:167],a[204:207]\n"
"ds_read_b128 v[128:131],v80 offset:32768\n"
"s_lshl2_add_u32 vcc_lo,s94,s52\n"
"v_mfma_f32_16x16x32_bf16 a[200:203],v[152:155],v[164:167],a[200:203]\n"
"ds_read_b128 v[192:195],v81 offset:32768\n"
"s_waitcnt lgkmcnt(10)\n"
"v_mfma_f32_16x16x32_bf16 a[208:211],v[152:155],v[168:171],a[208:211]\n"
"ds_read_b64_tr_b16 v[224:225],v98 offset:16384\n"
"s_lshl_b32 vcc_hi,vcc_lo,11\n"
"v_mfma_f32_16x16x32_bf16 a[212:215],v[156:159],v[168:171],a[212:215]\n"
"ds_read_b64_tr_b16 v[226:227],v98 offset:17408\n"
"s_sub_u32 s12,s12,vcc_hi\n"
"v_mfma_f32_16x16x32_bf16 a[220:223],v[156:159],v[172:175],a[220:223]\n"
"ds_read_b64_tr_b16 v[228:229],v99 offset:16384\n"
"s_add_u32 s12,s12,57671680\n"
"v_mfma_f32_16x16x32_bf16 a[216:219],v[152:155],v[172:175],a[216:219]\n"
"ds_read_b64_tr_b16 v[230:231],v99 offset:17408\n"
"s_waitcnt lgkmcnt(10)\n"
"v_mfma_f32_16x16x32_bf16 a[224:227],v[152:155],v[176:179],a[224:227]\n"
"ds_read_b64_tr_b16 v[232:233],v100 offset:16384\n"
"s_lshl_b32 s95,vcc_lo,13\n"
"v_mfma_f32_16x16x32_bf16 a[228:231],v[156:159],v[176:179],a[228:231]\n"
"ds_read_b64_tr_b16 v[234:235],v100 offset:17408\n"
"s_add_u32 s95,s95,54525952\n"
"v_mfma_f32_16x16x32_bf16 a[236:239],v[156:159],v[180:183],a[236:239]\n"
"ds_read_b64_tr_b16 v[236:237],v101 offset:16384\n"
"s_lshl_b32 s8,s96,17\n"
"v_mfma_f32_16x16x32_bf16 a[232:235],v[152:155],v[180:183],a[232:235]\n"
"ds_read_b64_tr_b16 v[238:239],v101 offset:17408\n"
"s_waitcnt lgkmcnt(10)\n"
"v_mfma_f32_16x16x32_bf16 a[240:243],v[152:155],v[184:187],a[240:243]\n"
"ds_read_b64_tr_b16 v[240:241],v102 offset:16384\n"
"s_add_u32 s8,s8,s95\n"
"v_mfma_f32_16x16x32_bf16 a[244:247],v[156:159],v[184:187],a[244:247]\n"
"ds_read_b64_tr_b16 v[242:243],v102 offset:17408\n"
"s_and_b32 vcc_lo,s96,1\n"
"v_mfma_f32_16x16x32_bf16 a[252:255],v[156:159],v[188:191],a[252:255]\n"
"ds_read_b64_tr_b16 v[244:245],v103 offset:16384\n"
"s_lshl2_add_u32 vcc_lo,s94,vcc_lo\n"
"v_mfma_f32_16x16x32_bf16 a[248:251],v[152:155],v[188:191],a[248:251]\n"
"ds_read_b64_tr_b16 v[246:247],v103 offset:17408\n"
"s_waitcnt lgkmcnt(8)\n"
"v_mfma_f32_16x16x32_bf16 a[0:3],v[128:131],v[160:163],a[0:3]\n"
"ds_read_b64_tr_b16 v[248:249],v104 offset:16384\n"
"s_lshl_b32 s11,vcc_lo,10\n"
"v_mfma_f32_16x16x32_bf16 a[0:3],v[192:195],v[224:227],a[0:3]\n"
"ds_read_b64_tr_b16 v[250:251],v104 offset:17408\n"
"s_add_u32 s11,s11,148932608\n"
"v_mfma_f32_16x16x32_bf16 a[8:11],v[192:195],v[228:231],a[8:11]\n"
"ds_read_b64_tr_b16 v[252:253],v105 offset:16384\n"
"s_lshl_b32 s9,s94,12\n"
"v_mfma_f32_16x16x32_bf16 a[8:11],v[128:131],v[164:167],a[8:11]\n"
"ds_read_b64_tr_b16 v[254:255],v105 offset:17408\n"
"s_waitcnt lgkmcnt(8)\n"
"v_mfma_f32_16x16x32_bf16 a[16:19],v[128:131],v[168:171],a[16:19]\n"
"ds_read_b128 v[132:135],v80 offset:34816\n"
"s_add_u32 s9,s9,148931072\n"
"v_mfma_f32_16x16x32_bf16 a[16:19],v[192:195],v[232:235],a[16:19]\n"
"s_cmp_eq_u32 s52,0\n"
"ds_read_b128 v[196:199],v81 offset:34816\n"
"s_cselect_b32 s71,s71,2146435072\n"
"v_mfma_f32_16x16x32_bf16 a[24:27],v[192:195],v[236:239],a[24:27]\n"
"ds_read_b128 v[136:139],v80 offset:36864\n"
"v_mfma_f32_16x16x32_bf16 a[24:27],v[128:131],v[172:175],a[24:27]\n"
"ds_read_b128 v[200:203],v81 offset:36864\n"
"s_waitcnt lgkmcnt(8)\n"
"v_mfma_f32_16x16x32_bf16 a[32:35],v[128:131],v[176:179],a[32:35]\n"
"ds_read_b128 v[140:143],v80 offset:38912\n"
"v_mfma_f32_16x16x32_bf16 a[32:35],v[192:195],v[240:243],a[32:35]\n"
"ds_read_b128 v[204:207],v81 offset:38912\n"
"v_mfma_f32_16x16x32_bf16 a[40:43],v[192:195],v[244:247],a[40:43]\n"
"ds_read_b128 v[144:147],v80 offset:40960\n"
"v_mfma_f32_16x16x32_bf16 a[40:43],v[128:131],v[180:183],a[40:43]\n"
"ds_read_b128 v[208:211],v81 offset:40960\n"
"s_waitcnt lgkmcnt(8)\n"
"v_mfma_f32_16x16x32_bf16 a[48:51],v[128:131],v[184:187],a[48:51]\n"
"ds_read_b128 v[148:151],v80 offset:43008\n"
"v_mfma_f32_16x16x32_bf16 a[48:51],v[192:195],v[248:251],a[48:51]\n"
"ds_read_b128 v[212:215],v81 offset:43008\n"
"v_mfma_f32_16x16x32_bf16 a[56:59],v[192:195],v[252:255],a[56:59]\n"
"ds_read_b128 v[216:219],v81 offset:45056\n"
"v_mfma_f32_16x16x32_bf16 a[56:59],v[128:131],v[188:191],a[56:59]\n"
"ds_read_b128 v[220:223],v81 offset:47104\n"
"s_waitcnt lgkmcnt(10)\n"
"v_mfma_f32_16x16x32_bf16 a[4:7],v[132:135],v[160:163],a[4:7]\n"
"v_mfma_f32_16x16x32_bf16 a[4:7],v[196:199],v[224:227],a[4:7]\n"
"v_mfma_f32_16x16x32_bf16 a[12:15],v[196:199],v[228:231],a[12:15]\n"
"v_mfma_f32_16x16x32_bf16 a[12:15],v[132:135],v[164:167],a[12:15]\n"
"v_mfma_f32_16x16x32_bf16 a[20:23],v[132:135],v[168:171],a[20:23]\n"
"s_waitcnt lgkmcnt(0)\n"
"s_barrier\n"
"v_mfma_f32_16x16x32_bf16 a[20:23],v[196:199],v[232:235],a[20:23]\n"
"s_mov_b32 s77,1572864\n"
"s_add_u32 m0,s78,98304\n"
"v_mfma_f32_16x16x32_bf16 a[28:31],v[196:199],v[236:239],a[28:31]\n"
"buffer_load_dwordx4 v110,s[20:23],s77 offen offset:0 lds\n"
"v_mfma_f32_16x16x32_bf16 a[28:31],v[132:135],v[172:175],a[28:31]\n"
"v_mfma_f32_16x16x32_bf16 a[36:39],v[132:135],v[176:179],a[36:39]\n"
"v_mfma_f32_16x16x32_bf16 a[36:39],v[196:199],v[240:243],a[36:39]\n"
"v_mfma_f32_16x16x32_bf16 a[44:47],v[196:199],v[244:247],a[44:47]\n"
"buffer_load_dwordx4 v111,s[20:23],s77 offen offset:1024 lds\n"
"v_mfma_f32_16x16x32_bf16 a[44:47],v[132:135],v[180:183],a[44:47]\n"
"v_mfma_f32_16x16x32_bf16 a[52:55],v[132:135],v[184:187],a[52:55]\n"
"v_mfma_f32_16x16x32_bf16 a[52:55],v[196:199],v[248:251],a[52:55]\n"
"v_mfma_f32_16x16x32_bf16 a[60:63],v[196:199],v[252:255],a[60:63]\n"
"buffer_load_dwordx4 v113,s[20:23],s77 offen offset:2048 lds\n"
"v_mfma_f32_16x16x32_bf16 a[60:63],v[132:135],v[188:191],a[60:63]\n"
"v_mfma_f32_16x16x32_bf16 a[64:67],v[136:139],v[160:163],a[64:67]\n"
"v_mfma_f32_16x16x32_bf16 a[64:67],v[200:203],v[224:227],a[64:67]\n"
"v_mfma_f32_16x16x32_bf16 a[72:75],v[200:203],v[228:231],a[72:75]\n"
"buffer_load_dwordx4 v114,s[20:23],s77 offen offset:3072 lds\n"
"v_mfma_f32_16x16x32_bf16 a[72:75],v[136:139],v[164:167],a[72:75]\n"
"v_mfma_f32_16x16x32_bf16 a[80:83],v[136:139],v[168:171],a[80:83]\n"
"v_mfma_f32_16x16x32_bf16 a[80:83],v[200:203],v[232:235],a[80:83]\n"
"s_mov_b32 s77,1835008\n"
"s_add_u32 m0,s78,114688\n"
"v_mfma_f32_16x16x32_bf16 a[88:91],v[200:203],v[236:239],a[88:91]\n"
"buffer_load_dwordx4 v110,s[20:23],s77 offen offset:0 lds\n"
"v_mfma_f32_16x16x32_bf16 a[88:91],v[136:139],v[172:175],a[88:91]\n"
"v_mfma_f32_16x16x32_bf16 a[96:99],v[136:139],v[176:179],a[96:99]\n"
"v_mfma_f32_16x16x32_bf16 a[96:99],v[200:203],v[240:243],a[96:99]\n"
"v_mfma_f32_16x16x32_bf16 a[104:107],v[200:203],v[244:247],a[104:107]\n"
"buffer_load_dwordx4 v111,s[20:23],s77 offen offset:1024 lds\n"
"v_mfma_f32_16x16x32_bf16 a[104:107],v[136:139],v[180:183],a[104:107]\n"
"v_mfma_f32_16x16x32_bf16 a[112:115],v[136:139],v[184:187],a[112:115]\n"
"v_mfma_f32_16x16x32_bf16 a[112:115],v[200:203],v[248:251],a[112:115]\n"
"v_mfma_f32_16x16x32_bf16 a[120:123],v[200:203],v[252:255],a[120:123]\n"
"buffer_load_dwordx4 v113,s[20:23],s77 offen offset:2048 lds\n"
"v_mfma_f32_16x16x32_bf16 a[120:123],v[136:139],v[188:191],a[120:123]\n"
"s_waitcnt vmcnt(10)\n"
"v_mfma_f32_16x16x32_bf16 a[68:71],v[140:143],v[160:163],a[68:71]\n"
"v_lshlrev_b32 v24,16,v8\n"
"v_and_b32 v25,0xffff0000,v8\n"
"v_mfma_f32_16x16x32_bf16 a[68:71],v[204:207],v[224:227],a[68:71]\n"
"v_lshlrev_b32 v26,16,v9\n"
"v_and_b32 v27,0xffff0000,v9\n"
"v_mfma_f32_16x16x32_bf16 a[76:79],v[204:207],v[228:231],a[76:79]\n"
"buffer_load_dwordx4 v114,s[20:23],s77 offen offset:3072 lds\n"
"v_mfma_f32_16x16x32_bf16 a[76:79],v[140:143],v[164:167],a[76:79]\n"
"v_lshlrev_b32 v28,16,v10\n"
"v_and_b32 v29,0xffff0000,v10\n"
"v_mfma_f32_16x16x32_bf16 a[84:87],v[140:143],v[168:171],a[84:87]\n"
"v_lshlrev_b32 v30,16,v11\n"
"v_and_b32 v31,0xffff0000,v11\n"
"v_mfma_f32_16x16x32_bf16 a[84:87],v[204:207],v[232:235],a[84:87]\n"
"v_lshlrev_b32 v32,16,v12\n"
"v_and_b32 v33,0xffff0000,v12\n"
"v_mfma_f32_16x16x32_bf16 a[92:95],v[204:207],v[236:239],a[92:95]\n"
"s_mov_b32 s76,512\n"
"v_lshlrev_b32 v34,16,v13\n"
"s_add_u32 m0,s79,32768\n"
"v_mfma_f32_16x16x32_bf16 a[92:95],v[140:143],v[172:175],a[92:95]\n"
"buffer_load_dwordx4 v106,s[16:19],s76 offen offset:0 lds\n"
"v_mfma_f32_16x16x32_bf16 a[100:103],v[140:143],v[176:179],a[100:103]\n"
"v_and_b32 v35,0xffff0000,v13\n"
"v_lshlrev_b32 v36,16,v14\n"
"v_mfma_f32_16x16x32_bf16 a[100:103],v[204:207],v[240:243],a[100:103]\n"
"v_and_b32 v37,0xffff0000,v14\n"
"v_lshlrev_b32 v38,16,v15\n"
"v_mfma_f32_16x16x32_bf16 a[108:111],v[204:207],v[244:247],a[108:111]\n"
"v_and_b32 v39,0xffff0000,v15\n"
"v_lshlrev_b32 v40,16,v16\n"
"v_mfma_f32_16x16x32_bf16 a[108:111],v[140:143],v[180:183],a[108:111]\n"
"v_and_b32 v41,0xffff0000,v16\n"
"v_lshlrev_b32 v42,16,v17\n"
"v_mfma_f32_16x16x32_bf16 a[116:119],v[140:143],v[184:187],a[116:119]\n"
"buffer_load_dwordx4 v107,s[16:19],s76 offen offset:1024 lds\n"
"v_mfma_f32_16x16x32_bf16 a[116:119],v[204:207],v[248:251],a[116:119]\n"
"v_and_b32 v43,0xffff0000,v17\n"
"v_lshlrev_b32 v44,16,v18\n"
"v_mfma_f32_16x16x32_bf16 a[124:127],v[204:207],v[252:255],a[124:127]\n"
"v_and_b32 v45,0xffff0000,v18\n"
"v_lshlrev_b32 v46,16,v19\n"
"v_mfma_f32_16x16x32_bf16 a[124:127],v[140:143],v[188:191],a[124:127]\n"
"v_and_b32 v47,0xffff0000,v19\n"
"v_lshlrev_b32 v48,16,v20\n"
"v_mfma_f32_16x16x32_bf16 a[128:131],v[144:147],v[160:163],a[128:131]\n"
"v_and_b32 v49,0xffff0000,v20\n"
"v_lshlrev_b32 v50,16,v21\n"
"v_mfma_f32_16x16x32_bf16 a[128:131],v[208:211],v[224:227],a[128:131]\n"
"buffer_load_dwordx4 v108,s[16:19],s76 offen offset:2048 lds\n"
"v_mfma_f32_16x16x32_bf16 a[136:139],v[208:211],v[228:231],a[136:139]\n"
"v_and_b32 v51,0xffff0000,v21\n"
"v_lshlrev_b32 v52,16,v22\n"
"v_mfma_f32_16x16x32_bf16 a[136:139],v[144:147],v[164:167],a[136:139]\n"
"v_and_b32 v53,0xffff0000,v22\n"
"v_lshlrev_b32 v54,16,v23\n"
"v_mfma_f32_16x16x32_bf16 a[144:147],v[144:147],v[168:171],a[144:147]\n"
"v_and_b32 v55,0xffff0000,v23\n"
"v_add_f32 v56,v24,v25\n"
"v_mfma_f32_16x16x32_bf16 a[144:147],v[208:211],v[232:235],a[144:147]\n"
"v_add_f32 v57,v26,v27\n"
"v_add_f32 v56,v56,v28\n"
"v_mfma_f32_16x16x32_bf16 a[152:155],v[208:211],v[236:239],a[152:155]\n"
"buffer_load_dwordx4 v109,s[16:19],s76 offen offset:3072 lds\n"
"v_mfma_f32_16x16x32_bf16 a[152:155],v[144:147],v[172:175],a[152:155]\n"
"v_add_f32 v57,v57,v29\n"
"v_add_f32 v56,v56,v30\n"
"v_mfma_f32_16x16x32_bf16 a[160:163],v[144:147],v[176:179],a[160:163]\n"
"v_add_f32 v57,v57,v31\n"
"v_add_f32 v56,v56,v32\n"
"v_mfma_f32_16x16x32_bf16 a[160:163],v[208:211],v[240:243],a[160:163]\n"
"v_readfirstlane_b32 s82,v78\n"
"v_readfirstlane_b32 s83,v79\n"
"v_mfma_f32_16x16x32_bf16 a[168:171],v[208:211],v[244:247],a[168:171]\n"
"s_mov_b32 s76,262656\n"
"v_add_f32 v57,v57,v33\n"
"s_add_u32 m0,s79,36864\n"
"v_mfma_f32_16x16x32_bf16 a[168:171],v[144:147],v[180:183],a[168:171]\n"
"buffer_load_dwordx4 v106,s[16:19],s76 offen offset:0 lds\n"
"s_mul_i32 s80,s82,0x9e3779b1\n"
"v_mfma_f32_16x16x32_bf16 a[176:179],v[144:147],v[184:187],a[176:179]\n"
"s_add_u32 s80,s80,0x7f4a7c15\n"
"v_add_f32 v56,v56,v34\n"
"v_add_f32 v57,v57,v35\n"
"v_mfma_f32_16x16x32_bf16 a[176:179],v[208:211],v[248:251],a[176:179]\n"
"s_add_u32 s81,s83,s80\n"
"s_add_u32 s81,s81,1\n"
"v_mfma_f32_16x16x32_bf16 a[184:187],v[208:211],v[252:255],a[184:187]\n"
"v_mov_b32 v124,s80\n"
"v_mov_b32 v125,s81\n"
"v_mfma_f32_16x16x32_bf16 a[184:187],v[144:147],v[188:191],a[184:187]\n"
"v_add_f32 v56,v56,v36\n"
"v_add_f32 v57,v57,v37\n"
"v_mfma_f32_16x16x32_bf16 a[132:135],v[148:151],v[160:163],a[132:135]\n"
"buffer_load_dwordx4 v107,s[16:19],s76 offen offset:1024 lds\n"
"v_mfma_f32_16x16x32_bf16 a[132:135],v[212:215],v[224:227],a[132:135]\n"
"v_add_f32 v56,v56,v38\n"
"v_add_f32 v57,v57,v39\n"
"v_mfma_f32_16x16x32_bf16 a[140:143],v[212:215],v[228:231],a[140:143]\n"
"v_add_f32 v56,v56,v40\n"
"v_add_f32 v57,v57,v41\n"
"v_mfma_f32_16x16x32_bf16 a[140:143],v[148:151],v[164:167],a[140:143]\n"
"v_add_f32 v56,v56,v42\n"
"v_add_f32 v57,v57,v43\n"
"v_mfma_f32_16x16x32_bf16 a[148:151],v[148:151],v[168:171],a[148:151]\n"
"v_add_f32 v56,v56,v44\n"
"v_add_f32 v57,v57,v45\n"
"v_mfma_f32_16x16x32_bf16 a[148:151],v[212:215],v[232:235],a[148:151]\n"
"buffer_load_dwordx4 v108,s[16:19],s76 offen offset:2048 lds\n"
"v_mfma_f32_16x16x32_bf16 a[156:159],v[212:215],v[236:239],a[156:159]\n"
"v_add_f32 v56,v56,v46\n"
"v_add_f32 v57,v57,v47\n"
"v_mfma_f32_16x16x32_bf16 a[156:159],v[148:151],v[172:175],a[156:159]\n"
"v_add_f32 v56,v56,v48\n"
"v_add_f32 v57,v57,v49\n"
"v_mfma_f32_16x16x32_bf16 a[164:167],v[148:151],v[176:179],a[164:167]\n"
"v_add_f32 v56,v56,v50\n"
"v_add_f32 v57,v57,v51\n"
"v_mfma_f32_16x16x32_bf16 a[164:167],v[212:215],v[240:243],a[164:167]\n"
"v_add_f32 v56,v56,v52\n"
"v_add_f32 v57,v57,v53\n"
"v_mfma_f32_16x16x32_bf16 a[172:175],v[212:215],v[244:247],a[172:175]\n"
"buffer_load_dwordx4 v109,s[16:19],s76 offen offset:3072 lds\n"
"v_mfma_f32_16x16x32_bf16 a[172:175],v[148:151],v[180:183],a[172:175]\n"
"s_mov_b32 s87,2097152\n"
"v_add_f32 v56,v56,v54\n"
"v_add_f32 v57,v57,v55\n"
"v_mfma_f32_16x16x32_bf16 a[180:183],v[148:151],v[184:187],a[180:183]\n"
"buffer_load_dword v115,v112,s[36:39],s87 offen\n"
"v_mfma_f32_16x16x32_bf16 a[180:183],v[212:215],v[248:251],a[180:183]\n"
"s_mov_b32 s87,2621440\n"
"v_add_f32 v56,v56,v57\n"
"v_mfma_f32_16x16x32_bf16 a[188:191],v[212:215],v[252:255],a[188:191]\n"
"buffer_load_dword v115,v112,s[36:39],s87 offen\n"
"v_add_f32_dpp v56,v56,v56 quad_perm:[1,0,3,2] row_mask:0xf bank_mask:0xf\n"
"v_mfma_f32_16x16x32_bf16 a[188:191],v[148:151],v[188:191],a[188:191]\n"
"s_barrier\n"
"v_mfma_f32_16x16x32_bf16 a[192:195],v[216:219],v[224:227],a[192:195]\n"
"ds_read_b128 v[152:155],v82 offset:12288\n"
"v_mfma_f32_16x16x32_bf16 a[196:199],v[220:223],v[224:227],a[196:199]\n"
"ds_read_b128 v[156:159],v82 offset:14336\n"
"v_add_f32_dpp v56,v56,v56 quad_perm:[2,3,0,1] row_mask:0xf bank_mask:0xf\n"
"v_mfma_f32_16x16x32_bf16 a[204:207],v[220:223],v[228:231],a[204:207]\n"
"ds_read_b64_tr_b16 v[160:161],v98 offset:49152\n"
"v_mfma_f32_16x16x32_bf16 a[200:203],v[216:219],v[228:231],a[200:203]\n"
"ds_read_b64_tr_b16 v[162:163],v98 offset:50176\n"
"v_add_f32_dpp v56,v56,v56 row_ror:8 row_mask:0xf bank_mask:0xf\n"
"v_mfma_f32_16x16x32_bf16 a[208:211],v[216:219],v[232:235],a[208:211]\n"
"ds_read_b64_tr_b16 v[164:165],v99 offset:49152\n"
"v_mov_b32 v57,v56\n"
"v_mfma_f32_16x16x32_bf16 a[212:215],v[220:223],v[232:235],a[212:215]\n"
"ds_read_b64_tr_b16 v[166:167],v99 offset:50176\n"
"v_mfma_f32_16x16x32_bf16 a[220:223],v[220:223],v[236:239],a[220:223]\n"
"ds_read_b64_tr_b16 v[168:169],v100 offset:49152\n"
"v_permlane16_swap_b32 v57,v56\n"
"v_mfma_f32_16x16x32_bf16 a[216:219],v[216:219],v[236:239],a[216:219]\n"
"ds_read_b64_tr_b16 v[170:171],v100 offset:50176\n"
"v_add_f32 v56,v56,v57\n"
"v_mfma_f32_16x16x32_bf16 a[224:227],v[216:219],v[240:243],a[224:227]\n"
"ds_read_b64_tr_b16 v[172:173],v101 offset:49152\n"
"v_mov_b32 v57,v56\n"
"v_mfma_f32_16x16x32_bf16 a[228:231],v[220:223],v[240:243],a[228:231]\n"
"ds_read_b64_tr_b16 v[174:175],v101 offset:50176\n"
"v_mfma_f32_16x16x32_bf16 a[236:239],v[220:223],v[244:247],a[236:239]\n"
"ds_read_b64_tr_b16 v[176:177],v102 offset:49152\n"
"v_permlane32_swap_b32 v57,v56\n"
"v_mfma_f32_16x16x32_bf16 a[232:235],v[216:219],v[244:247],a[232:235]\n"
"ds_read_b64_tr_b16 v[178:179],v102 offset:50176\n"
"v_add_f32 v56,v56,v57\n"
"v_mfma_f32_16x16x32_bf16 a[240:243],v[216:219],v[248:251],a[240:243]\n"
"ds_read_b64_tr_b16 v[180:181],v103 offset:49152\n"
"v_mul_f32 v58,0x3a800000,v56\n"
"v_mfma_f32_16x16x32_bf16 a[244:247],v[220:223],v[248:251],a[244:247]\n"
"ds_read_b64_tr_b16 v[182:183],v103 offset:50176\n"
"v_sub_f32 v62,v24,v58\n"
"v_mfma_f32_16x16x32_bf16 a[252:255],v[220:223],v[252:255],a[252:255]\n"
"ds_read_b64_tr_b16 v[184:185],v104 offset:49152\n"
"v_mul_f32 v59,v62,v62\n"
"v_mfma_f32_16x16x32_bf16 a[248:251],v[216:219],v[252:255],a[248:251]\n"
"ds_read_b64_tr_b16 v[186:187],v104 offset:50176\n"
"v_sub_f32 v63,v25,v58\n"
"s_waitcnt lgkmcnt(10)\n"
"v_mfma_f32_16x16x32_bf16 a[192:195],v[152:155],v[160:163],a[192:195]\n"
"ds_read_b64_tr_b16 v[188:189],v105 offset:49152\n"
"v_mul_f32 v60,v63,v63\n"
"v_mfma_f32_16x16x32_bf16 a[196:199],v[156:159],v[160:163],a[196:199]\n"
"ds_read_b64_tr_b16 v[190:191],v105 offset:50176\n"
"v_sub_f32 v64,v26,v58\n"
"v_mfma_f32_16x16x32_bf16 a[204:207],v[156:159],v[164:167],a[204:207]\n"
"ds_read_b128 v[128:131],v82 offset:0\n"
"v_fmac_f32 v59,v64,v64\n"
"v_mfma_f32_16x16x32_bf16 a[200:203],v[152:155],v[164:167],a[200:203]\n"
"ds_read_b128 v[192:195],v83 offset:0\n"
"s_waitcnt lgkmcnt(10)\n"
"v_mfma_f32_16x16x32_bf16 a[208:211],v[152:155],v[168:171],a[208:211]\n"
"ds_read_b64_tr_b16 v[224:225],v98 offset:32768\n"
"v_sub_f32 v65,v27,v58\n"
"v_mfma_f32_16x16x32_bf16 a[212:215],v[156:159],v[168:171],a[212:215]\n"
"ds_read_b64_tr_b16 v[226:227],v98 offset:33792\n"
"v_fmac_f32 v60,v65,v65\n"
"v_mfma_f32_16x16x32_bf16 a[220:223],v[156:159],v[172:175],a[220:223]\n"
"ds_read_b64_tr_b16 v[228:229],v99 offset:32768\n"
"v_sub_f32 v62,v28,v58\n"
"v_mfma_f32_16x16x32_bf16 a[216:219],v[152:155],v[172:175],a[216:219]\n"
"ds_read_b64_tr_b16 v[230:231],v99 offset:33792\n"
"s_waitcnt lgkmcnt(10)\n"
"v_mfma_f32_16x16x32_bf16 a[224:227],v[152:155],v[176:179],a[224:227]\n"
"ds_read_b64_tr_b16 v[232:233],v100 offset:32768\n"
"v_fmac_f32 v59,v62,v62\n"
"v_mfma_f32_16x16x32_bf16 a[228:231],v[156:159],v[176:179],a[228:231]\n"
"ds_read_b64_tr_b16 v[234:235],v100 offset:33792\n"
"v_sub_f32 v63,v29,v58\n"
"v_mfma_f32_16x16x32_bf16 a[236:239],v[156:159],v[180:183],a[236:239]\n"
"ds_read_b64_tr_b16 v[236:237],v101 offset:32768\n"
"v_fmac_f32 v60,v63,v63\n"
"v_mfma_f32_16x16x32_bf16 a[232:235],v[152:155],v[180:183],a[232:235]\n"
"ds_read_b64_tr_b16 v[238:239],v101 offset:33792\n"
"s_waitcnt lgkmcnt(10)\n"
"v_mfma_f32_16x16x32_bf16 a[240:243],v[152:155],v[184:187],a[240:243]\n"
"ds_read_b64_tr_b16 v[240:241],v102 offset:32768\n"
"v_sub_f32 v64,v30,v58\n"
"v_mfma_f32_16x16x32_bf16 a[244:247],v[156:159],v[184:187],a[244:247]\n"
"ds_read_b64_tr_b16 v[242:243],v102 offset:33792\n"
"v_fmac_f32 v59,v64,v64\n"
"v_mfma_f32_16x16x32_bf16 a[252:255],v[156:159],v[188:191],a[252:255]\n"
"ds_read_b64_tr_b16 v[244:245],v103 offset:32768\n"
"v_sub_f32 v65,v31,v58\n"
"v_mfma_f32_16x16x32_bf16 a[248:251],v[152:155],v[188:191],a[248:251]\n"
"ds_read_b64_tr_b16 v[246:247],v103 offset:33792\n"
"s_waitcnt lgkmcnt(8)\n"
"v_mfma_f32_16x16x32_bf16 a[0:3],v[128:131],v[160:163],a[0:3]\n"
"ds_read_b64_tr_b16 v[248:249],v104 offset:32768\n"
"v_fmac_f32 v60,v65,v65\n"
"v_mfma_f32_16x16x32_bf16 a[0:3],v[192:195],v[224:227],a[0:3]\n"
"ds_read_b64_tr_b16 v[250:251],v104 offset:33792\n"
"v_sub_f32 v62,v32,v58\n"
"v_mfma_f32_16x16x32_bf16 a[8:11],v[192:195],v[228:231],a[8:11]\n"
"ds_read_b64_tr_b16 v[252:253],v105 offset:32768\n"
"v_fmac_f32 v59,v62,v62\n"
"v_mfma_f32_16x16x32_bf16 a[8:11],v[128:131],v[164:167],a[8:11]\n"
"ds_read_b64_tr_b16 v[254:255],v105 offset:33792\n"
"s_waitcnt lgkmcnt(8)\n"
"v_mfma_f32_16x16x32_bf16 a[16:19],v[128:131],v[168:171],a[16:19]\n"
"ds_read_b128 v[132:135],v82 offset:2048\n"
"v_sub_f32 v63,v33,v58\n"
"v_mfma_f32_16x16x32_bf16 a[16:19],v[192:195],v[232:235],a[16:19]\n"
"ds_read_b128 v[196:199],v83 offset:2048\n"
"v_fmac_f32 v60,v63,v63\n"
"v_mfma_f32_16x16x32_bf16 a[24:27],v[192:195],v[236:239],a[24:27]\n"
"ds_read_b128 v[136:139],v82 offset:4096\n"
"v_sub_f32 v64,v34,v58\n"
"v_mfma_f32_16x16x32_bf16 a[24:27],v[128:131],v[172:175],a[24:27]\n"
"ds_read_b128 v[200:203],v83 offset:4096\n"
"s_waitcnt lgkmcnt(8)\n"
"v_mfma_f32_16x16x32_bf16 a[32:35],v[128:131],v[176:179],a[32:35]\n"
"ds_read_b128 v[140:143],v82 offset:6144\n"
"v_fmac_f32 v59,v64,v64\n"
"v_mfma_f32_16x16x32_bf16 a[32:35],v[192:195],v[240:243],a[32:35]\n"
"ds_read_b128 v[204:207],v83 offset:6144\n"
"v_sub_f32 v65,v35,v58\n"
"v_mfma_f32_16x16x32_bf16 a[40:43],v[192:195],v[244:247],a[40:43]\n"
"ds_read_b128 v[144:147],v82 offset:8192\n"
"v_fmac_f32 v60,v65,v65\n"
"v_mfma_f32_16x16x32_bf16 a[40:43],v[128:131],v[180:183],a[40:43]\n"
"ds_read_b128 v[208:211],v83 offset:8192\n"
"s_waitcnt lgkmcnt(8)\n"
"v_mfma_f32_16x16x32_bf16 a[48:51],v[128:131],v[184:187],a[48:51]\n"
"ds_read_b128 v[148:151],v82 offset:10240\n"
"v_sub_f32 v62,v36,v58\n"
"v_mfma_f32_16x16x32_bf16 a[48:51],v[192:195],v[248:251],a[48:51]\n"
"ds_read_b128 v[212:215],v83 offset:10240\n"
"v_fmac_f32 v59,v62,v62\n"
"v_mfma_f32_16x16x32_bf16 a[56:59],v[192:195],v[252:255],a[56:59]\n"
"ds_read_b128 v[216:219],v83 offset:12288\n"
"v_sub_f32 v63,v37,v58\n"
"v_mfma_f32_16x16x32_bf16 a[56:59],v[128:131],v[188:191],a[56:59]\n"
"ds_read_b128 v[220:223],v83 offset:14336\n"
"s_waitcnt lgkmcnt(10)\n"
"v_mfma_f32_16x16x32_bf16 a[4:7],v[132:135],v[160:163],a[4:7]\n"
"v_fmac_f32 v60,v63,v63\n"
"v_sub_f32 v64,v38,v58\n"
"v_mfma_f32_16x16x32_bf16 a[4:7],v[196:199],v[224:227],a[4:7]\n"
"v_fmac_f32 v59,v64,v64\n"
"v_sub_f32 v65,v39,v58\n"
"v_mfma_f32_16x16x32_bf16 a[12:15],v[196:199],v[228:231],a[12:15]\n"
"v_fmac_f32 v60,v65,v65\n"
"v_sub_f32 v62,v40,v58\n"
"v_mfma_f32_16x16x32_bf16 a[12:15],v[132:135],v[164:167],a[12:15]\n"
"v_fmac_f32 v59,v62,v62\n"
"v_sub_f32 v63,v41,v58\n"
"v_mfma_f32_16x16x32_bf16 a[20:23],v[132:135],v[168:171],a[20:23]\n"
"s_waitcnt lgkmcnt(0)\n"
"s_barrier\n"
"v_mfma_f32_16x16x32_bf16 a[20:23],v[196:199],v[232:235],a[20:23]\n"
"s_mov_b32 s77,2097152\n"
"v_fmac_f32 v60,v63,v63\n"
"s_add_u32 m0,s78,147456\n"
"v_mfma_f32_16x16x32_bf16 a[28:31],v[196:199],v[236:239],a[28:31]\n"
"buffer_load_dwordx4 v110,s[20:23],s77 offen offset:0 lds\n"
"v_mfma_f32_16x16x32_bf16 a[28:31],v[132:135],v[172:175],a[28:31]\n"
"v_sub_f32 v64,v42,v58\n"
"v_fmac_f32 v59,v64,v64\n"
"v_mfma_f32_16x16x32_bf16 a[36:39],v[132:135],v[176:179],a[36:39]\n"
"v_sub_f32 v65,v43,v58\n"
"v_fmac_f32 v60,v65,v65\n"
"v_mfma_f32_16x16x32_bf16 a[36:39],v[196:199],v[240:243],a[36:39]\n"
"v_sub_f32 v62,v44,v58\n"
"v_fmac_f32 v59,v62,v62\n"
"v_mfma_f32_16x16x32_bf16 a[44:47],v[196:199],v[244:247],a[44:47]\n"
"buffer_load_dwordx4 v111,s[20:23],s77 offen offset:1024 lds\n"
"v_mfma_f32_16x16x32_bf16 a[44:47],v[132:135],v[180:183],a[44:47]\n"
"v_sub_f32 v63,v45,v58\n"
"v_fmac_f32 v60,v63,v63\n"
"v_mfma_f32_16x16x32_bf16 a[52:55],v[132:135],v[184:187],a[52:55]\n"
"v_sub_f32 v64,v46,v58\n"
"v_fmac_f32 v59,v64,v64\n"
"v_mfma_f32_16x16x32_bf16 a[52:55],v[196:199],v[248:251],a[52:55]\n"
"v_sub_f32 v65,v47,v58\n"
"v_fmac_f32 v60,v65,v65\n"
"v_mfma_f32_16x16x32_bf16 a[60:63],v[196:199],v[252:255],a[60:63]\n"
"buffer_load_dwordx4 v113,s[20:23],s77 offen offset:2048 lds\n"
"v_mfma_f32_16x16x32_bf16 a[60:63],v[132:135],v[188:191],a[60:63]\n"
"v_sub_f32 v62,v48,v58\n"
"v_fmac_f32 v59,v62,v62\n"
"v_mfma_f32_16x16x32_bf16 a[64:67],v[136:139],v[160:163],a[64:67]\n"
"v_sub_f32 v63,v49,v58\n"
"v_fmac_f32 v60,v63,v63\n"
"v_mfma_f32_16x16x32_bf16 a[64:67],v[200:203],v[224:227],a[64:67]\n"
"v_sub_f32 v64,v50,v58\n"
"v_fmac_f32 v59,v64,v64\n"
"v_mfma_f32_16x16x32_bf16 a[72:75],v[200:203],v[228:231],a[72:75]\n"
"buffer_load_dwordx4 v114,s[20:23],s77 offen offset:3072 lds\n"
"v_mfma_f32_16x16x32_bf16 a[72:75],v[136:139],v[164:167],a[72:75]\n"
"v_sub_f32 v65,v51,v58\n"
"v_fmac_f32 v60,v65,v65\n"
"v_mfma_f32_16x16x32_bf16 a[80:83],v[136:139],v[168:171],a[80:83]\n"
"v_sub_f32 v62,v52,v58\n"
"v_fmac_f32 v59,v62,v62\n"
"v_mfma_f32_16x16x32_bf16 a[80:83],v[200:203],v[232:235],a[80:83]\n"
"s_mov_b32 s77,2359296\n"
"v_sub_f32 v63,v53,v58\n"
"s_add_u32 m0,s78,131072\n"
"v_mfma_f32_16x16x32_bf16 a[88:91],v[200:203],v[236:239],a[88:91]\n"
"buffer_load_dwordx4 v110,s[20:23],s77 offen offset:0 lds\n"
"v_mfma_f32_16x16x32_bf16 a[88:91],v[136:139],v[172:175],a[88:91]\n"
"v_fmac_f32 v60,v63,v63\n"
"v_sub_f32 v64,v54,v58\n"
"v_mfma_f32_16x16x32_bf16 a[96:99],v[136:139],v[176:179],a[96:99]\n"
"v_fmac_f32 v59,v64,v64\n"
"v_sub_f32 v65,v55,v58\n"
"v_mfma_f32_16x16x32_bf16 a[96:99],v[200:203],v[240:243],a[96:99]\n"
"v_fmac_f32 v60,v65,v65\n"
"v_add_f32 v59,v59,v60\n"
"v_mfma_f32_16x16x32_bf16 a[104:107],v[200:203],v[244:247],a[104:107]\n"
"buffer_load_dwordx4 v111,s[20:23],s77 offen offset:1024 lds\n"
"v_mfma_f32_16x16x32_bf16 a[104:107],v[136:139],v[180:183],a[104:107]\n"
"v_mfma_f32_16x16x32_bf16 a[112:115],v[136:139],v[184:187],a[112:115]\n"
"v_add_f32_dpp v59,v59,v59 quad_perm:[1,0,3,2] row_mask:0xf bank_mask:0xf\n"
"v_mfma_f32_16x16x32_bf16 a[112:115],v[200:203],v[248:251],a[112:115]\n"
"s_nop 0\n"
"v_add_f32_dpp v59,v59,v59 quad_perm:[2,3,0,1] row_mask:0xf bank_mask:0xf\n"
"v_mfma_f32_16x16x32_bf16 a[120:123],v[200:203],v[252:255],a[120:123]\n"
"buffer_load_dwordx4 v113,s[20:23],s77 offen offset:2048 lds\n"
"v_mfma_f32_16x16x32_bf16 a[120:123],v[136:139],v[188:191],a[120:123]\n"
"v_add_f32_dpp v59,v59,v59 row_ror:8 row_mask:0xf bank_mask:0xf\n"
"v_mov_b32 v57,v59\n"
"v_mfma_f32_16x16x32_bf16 a[68:71],v[140:143],v[160:163],a[68:71]\n"
"s_nop 0\n"
"v_permlane16_swap_b32 v57,v59\n"
"v_mfma_f32_16x16x32_bf16 a[68:71],v[204:207],v[224:227],a[68:71]\n"
"v_add_f32 v59,v59,v57\n"
"v_mov_b32 v57,v59\n"
"v_mfma_f32_16x16x32_bf16 a[76:79],v[204:207],v[228:231],a[76:79]\n"
"buffer_load_dwordx4 v114,s[20:23],s77 offen offset:3072 lds\n"
"v_mfma_f32_16x16x32_bf16 a[76:79],v[140:143],v[164:167],a[76:79]\n"
"v_permlane32_swap_b32 v57,v59\n"
"v_add_f32 v59,v59,v57\n"
"v_mul_f32 v59,0x3a800000,v59\n"
"v_mfma_f32_16x16x32_bf16 a[84:87],v[140:143],v[168:171],a[84:87]\n"
"v_cvt_pk_bf16_f32 v59,v59,v59\n"
"v_lshlrev_b32 v59,16,v59\n"
"v_add_f32 v59,0x3727c5ac,v59\n"
"v_mfma_f32_16x16x32_bf16 a[84:87],v[204:207],v[232:235],a[84:87]\n"
"v_cvt_pk_bf16_f32 v59,v59,v59\n"
"v_lshlrev_b32 v59,16,v59\n"
"v_sqrt_f32 v59,v59\n"
"v_mfma_f32_16x16x32_bf16 a[92:95],v[204:207],v[236:239],a[92:95]\n"
"s_mov_b32 s76,640\n"
"v_cvt_pk_bf16_f32 v58,v58,v58\n"
"v_lshlrev_b32 v58,16,v58\n"
"s_add_u32 m0,s79,65536\n"
"v_mfma_f32_16x16x32_bf16 a[92:95],v[140:143],v[172:175],a[92:95]\n"
"buffer_load_dwordx4 v106,s[16:19],s76 offen offset:0 lds\n"
"v_cvt_pk_bf16_f32 v59,v59,v59\n"
"v_mfma_f32_16x16x32_bf16 a[100:103],v[140:143],v[176:179],a[100:103]\n"
"v_lshlrev_b32 v59,16,v59\n"
"v_rcp_f32 v66,v59\n"
"v_mfma_f32_16x16x32_bf16 a[100:103],v[204:207],v[240:243],a[100:103]\n"
"v_mul_f32 v66,0x3e0293ee,v66\n"
"v_mul_f32 v58,v58,v66\n"
"v_fma_f32 v24,v24,v66,-v58\n"
"v_mfma_f32_16x16x32_bf16 a[108:111],v[204:207],v[244:247],a[108:111]\n"
"v_fma_f32 v25,v25,v66,-v58\n"
"v_cvt_pk_bf16_f32 v8,v24,v25\n"
"v_fma_f32 v26,v26,v66,-v58\n"
"v_mfma_f32_16x16x32_bf16 a[108:111],v[140:143],v[180:183],a[108:111]\n"
"v_fma_f32 v27,v27,v66,-v58\n"
"v_cvt_pk_bf16_f32 v9,v26,v27\n"
"v_fma_f32 v28,v28,v66,-v58\n"
"v_mfma_f32_16x16x32_bf16 a[116:119],v[140:143],v[184:187],a[116:119]\n"
"buffer_load_dwordx4 v107,s[16:19],s76 offen offset:1024 lds\n"
"v_fma_f32 v29,v29,v66,-v58\n"
"v_mfma_f32_16x16x32_bf16 a[116:119],v[204:207],v[248:251],a[116:119]\n"
"v_cvt_pk_bf16_f32 v10,v28,v29\n"
"v_fma_f32 v30,v30,v66,-v58\n"
"v_fma_f32 v31,v31,v66,-v58\n"
"v_cvt_pk_bf16_f32 v11,v30,v31\n"
"v_mfma_f32_16x16x32_bf16 a[124:127],v[204:207],v[252:255],a[124:127]\n"
"buffer_store_dwordx4 v[8:11],v71,s[24:27],s54 offen sc1\n"
"v_mfma_f32_16x16x32_bf16 a[124:127],v[140:143],v[188:191],a[124:127]\n"
"s_add_u32 s54,s54,131072\n"
"v_fma_f32 v32,v32,v66,-v58\n"
"v_fma_f32 v33,v33,v66,-v58\n"
"v_cvt_pk_bf16_f32 v12,v32,v33\n"
"v_mfma_f32_16x16x32_bf16 a[128:131],v[144:147],v[160:163],a[128:131]\n"
"v_fma_f32 v34,v34,v66,-v58\n"
"v_fma_f32 v35,v35,v66,-v58\n"
"v_cvt_pk_bf16_f32 v13,v34,v35\n"
"v_mfma_f32_16x16x32_bf16 a[128:131],v[208:211],v[224:227],a[128:131]\n"
"buffer_load_dwordx4 v108,s[16:19],s76 offen offset:2048 lds\n"
"v_fma_f32 v36,v36,v66,-v58\n"
"v_mfma_f32_16x16x32_bf16 a[136:139],v[208:211],v[228:231],a[136:139]\n"
"v_fma_f32 v37,v37,v66,-v58\n"
"v_cvt_pk_bf16_f32 v14,v36,v37\n"
"v_fma_f32 v38,v38,v66,-v58\n"
"v_mfma_f32_16x16x32_bf16 a[136:139],v[144:147],v[164:167],a[136:139]\n"
"v_fma_f32 v39,v39,v66,-v58\n"
"v_cvt_pk_bf16_f32 v15,v38,v39\n"
"v_mfma_f32_16x16x32_bf16 a[144:147],v[144:147],v[168:171],a[144:147]\n"
"buffer_store_dwordx4 v[12:15],v71,s[24:27],s54 offen sc1\n"
"v_fma_f32 v40,v40,v66,-v58\n"
"v_fma_f32 v41,v41,v66,-v58\n"
"v_mfma_f32_16x16x32_bf16 a[144:147],v[208:211],v[232:235],a[144:147]\n"
"s_add_u32 s54,s54,131072\n"
"v_cvt_pk_bf16_f32 v16,v40,v41\n"
"v_fma_f32 v42,v42,v66,-v58\n"
"v_fma_f32 v43,v43,v66,-v58\n"
"v_mfma_f32_16x16x32_bf16 a[152:155],v[208:211],v[236:239],a[152:155]\n"
"buffer_load_dwordx4 v109,s[16:19],s76 offen offset:3072 lds\n"
"v_cvt_pk_bf16_f32 v17,v42,v43\n"
"v_mfma_f32_16x16x32_bf16 a[152:155],v[144:147],v[172:175],a[152:155]\n"
"v_fma_f32 v44,v44,v66,-v58\n"
"v_fma_f32 v45,v45,v66,-v58\n"
"v_cvt_pk_bf16_f32 v18,v44,v45\n"
"v_mfma_f32_16x16x32_bf16 a[160:163],v[144:147],v[176:179],a[160:163]\n"
"v_fma_f32 v46,v46,v66,-v58\n"
"v_fma_f32 v47,v47,v66,-v58\n"
"v_cvt_pk_bf16_f32 v19,v46,v47\n"
"v_mfma_f32_16x16x32_bf16 a[160:163],v[208:211],v[240:243],a[160:163]\n"
"buffer_store_dwordx4 v[16:19],v71,s[24:27],s54 offen sc1\n"
"v_fma_f32 v48,v48,v66,-v58\n"
"v_mfma_f32_16x16x32_bf16 a[168:171],v[208:211],v[244:247],a[168:171]\n"
"s_add_u32 s54,s54,131072\n"
"v_fma_f32 v49,v49,v66,-v58\n"
"v_cvt_pk_bf16_f32 v20,v48,v49\n"
"s_mov_b32 s76,262784\n"
"s_add_u32 m0,s79,69632\n"
"v_mfma_f32_16x16x32_bf16 a[168:171],v[144:147],v[180:183],a[168:171]\n"
"buffer_load_dwordx4 v106,s[16:19],s76 offen offset:0 lds\n"
"v_fma_f32 v50,v50,v66,-v58\n"
"v_mfma_f32_16x16x32_bf16 a[176:179],v[144:147],v[184:187],a[176:179]\n"
"v_fma_f32 v51,v51,v66,-v58\n"
"v_cvt_pk_bf16_f32 v21,v50,v51\n"
"v_fma_f32 v52,v52,v66,-v58\n"
"v_mfma_f32_16x16x32_bf16 a[176:179],v[208:211],v[248:251],a[176:179]\n"
"v_fma_f32 v53,v53,v66,-v58\n"
"v_cvt_pk_bf16_f32 v22,v52,v53\n"
"v_fma_f32 v54,v54,v66,-v58\n"
"v_mfma_f32_16x16x32_bf16 a[184:187],v[208:211],v[252:255],a[184:187]\n"
"v_fma_f32 v55,v55,v66,-v58\n"
"v_cvt_pk_bf16_f32 v23,v54,v55\n"
"v_mfma_f32_16x16x32_bf16 a[184:187],v[144:147],v[188:191],a[184:187]\n"
"buffer_store_dwordx4 v[20:23],v71,s[24:27],s54 offen sc1\n"
"v_mfma_f32_16x16x32_bf16 a[132:135],v[148:151],v[160:163],a[132:135]\n"
"buffer_load_dwordx4 v107,s[16:19],s76 offen offset:1024 lds\n"
"v_mfma_f32_16x16x32_bf16 a[132:135],v[212:215],v[224:227],a[132:135]\n"
"v_mfma_f32_16x16x32_bf16 a[140:143],v[212:215],v[228:231],a[140:143]\n"
"v_mfma_f32_16x16x32_bf16 a[140:143],v[148:151],v[164:167],a[140:143]\n"
"v_mfma_f32_16x16x32_bf16 a[148:151],v[148:151],v[168:171],a[148:151]\n"
"v_mfma_f32_16x16x32_bf16 a[148:151],v[212:215],v[232:235],a[148:151]\n"
"buffer_load_dwordx4 v108,s[16:19],s76 offen offset:2048 lds\n"
"v_mfma_f32_16x16x32_bf16 a[156:159],v[212:215],v[236:239],a[156:159]\n"
"v_mfma_f32_16x16x32_bf16 a[156:159],v[148:151],v[172:175],a[156:159]\n"
"v_mfma_f32_16x16x32_bf16 a[164:167],v[148:151],v[176:179],a[164:167]\n"
"v_mfma_f32_16x16x32_bf16 a[164:167],v[212:215],v[240:243],a[164:167]\n"
"v_mfma_f32_16x16x32_bf16 a[172:175],v[212:215],v[244:247],a[172:175]\n"
"buffer_load_dwordx4 v109,s[16:19],s76 offen offset:3072 lds\n"
"v_mfma_f32_16x16x32_bf16 a[172:175],v[148:151],v[180:183],a[172:175]\n"
"s_mov_b32 s87,0\n"
"v_mfma_f32_16x16x32_bf16 a[180:183],v[148:151],v[184:187],a[180:183]\n"
"buffer_load_dword v115,v112,s[40:43],s87 offen\n"
"v_mfma_f32_16x16x32_bf16 a[180:183],v[212:215],v[248:251],a[180:183]\n"
"s_mov_b32 s87,1048576\n"
"v_mfma_f32_16x16x32_bf16 a[188:191],v[212:215],v[252:255],a[188:191]\n"
"buffer_load_dword v115,v112,s[40:43],s87 offen\n"
"v_mfma_f32_16x16x32_bf16 a[188:191],v[148:151],v[188:191],a[188:191]\n"
"s_mov_b32 s87,2097152\n"
"s_waitcnt vmcnt(32)\n"
"s_barrier\n"
"v_mfma_f32_16x16x32_bf16 a[192:195],v[216:219],v[224:227],a[192:195]\n"
"ds_read_b128 v[152:155],v80 offset:12288\n"
"buffer_load_dword v115,v112,s[40:43],s87 offen\n"
"v_mfma_f32_16x16x32_bf16 a[196:199],v[220:223],v[224:227],a[196:199]\n"
"ds_read_b128 v[156:159],v80 offset:14336\n"
"v_mfma_f32_16x16x32_bf16 a[204:207],v[220:223],v[228:231],a[204:207]\n"
"ds_read_b64_tr_b16 v[160:161],v98 offset:0\n"
"v_mfma_f32_16x16x32_bf16 a[200:203],v[216:219],v[228:231],a[200:203]\n"
"ds_read_b64_tr_b16 v[162:163],v98 offset:1024\n"
"v_mfma_f32_16x16x32_bf16 a[208:211],v[216:219],v[232:235],a[208:211]\n"
"ds_read_b64_tr_b16 v[164:165],v99 offset:0\n"
"v_mfma_f32_16x16x32_bf16 a[212:215],v[220:223],v[232:235],a[212:215]\n"
"ds_read_b64_tr_b16 v[166:167],v99 offset:1024\n"
"v_mfma_f32_16x16x32_bf16 a[220:223],v[220:223],v[236:239],a[220:223]\n"
"ds_read_b64_tr_b16 v[168:169],v100 offset:0\n"
"v_mfma_f32_16x16x32_bf16 a[216:219],v[216:219],v[236:239],a[216:219]\n"
"ds_read_b64_tr_b16 v[170:171],v100 offset:1024\n"
"v_mfma_f32_16x16x32_bf16 a[224:227],v[216:219],v[240:243],a[224:227]\n"
"ds_read_b64_tr_b16 v[172:173],v101 offset:0\n"
"v_mfma_f32_16x16x32_bf16 a[228:231],v[220:223],v[240:243],a[228:231]\n"
"ds_read_b64_tr_b16 v[174:175],v101 offset:1024\n"
"v_mfma_f32_16x16x32_bf16 a[236:239],v[220:223],v[244:247],a[236:239]\n"
"ds_read_b64_tr_b16 v[176:177],v102 offset:0\n"
"v_mfma_f32_16x16x32_bf16 a[232:235],v[216:219],v[244:247],a[232:235]\n"
"ds_read_b64_tr_b16 v[178:179],v102 offset:1024\n"
"v_mfma_f32_16x16x32_bf16 a[240:243],v[216:219],v[248:251],a[240:243]\n"
"ds_read_b64_tr_b16 v[180:181],v103 offset:0\n"
"v_mfma_f32_16x16x32_bf16 a[244:247],v[220:223],v[248:251],a[244:247]\n"
"ds_read_b64_tr_b16 v[182:183],v103 offset:1024\n"
"v_mfma_f32_16x16x32_bf16 a[252:255],v[220:223],v[252:255],a[252:255]\n"
"ds_read_b64_tr_b16 v[184:185],v104 offset:0\n"
"v_mfma_f32_16x16x32_bf16 a[248:251],v[216:219],v[252:255],a[248:251]\n"
"ds_read_b64_tr_b16 v[186:187],v104 offset:1024\n"
"s_waitcnt lgkmcnt(10)\n"
"v_mfma_f32_16x16x32_bf16 a[192:195],v[152:155],v[160:163],a[192:195]\n"
"ds_read_b64_tr_b16 v[188:189],v105 offset:0\n"
"v_mfma_f32_16x16x32_bf16 a[196:199],v[156:159],v[160:163],a[196:199]\n"
"ds_read_b64_tr_b16 v[190:191],v105 offset:1024\n"
"v_mfma_f32_16x16x32_bf16 a[204:207],v[156:159],v[164:167],a[204:207]\n"
"ds_read_b128 v[128:131],v80 offset:0\n"
"v_mfma_f32_16x16x32_bf16 a[200:203],v[152:155],v[164:167],a[200:203]\n"
"ds_read_b128 v[192:195],v81 offset:0\n"
"s_waitcnt lgkmcnt(10)\n"
"v_mfma_f32_16x16x32_bf16 a[208:211],v[152:155],v[168:171],a[208:211]\n"
"ds_read_b64_tr_b16 v[224:225],v98 offset:16384\n"
"v_mfma_f32_16x16x32_bf16 a[212:215],v[156:159],v[168:171],a[212:215]\n"
"ds_read_b64_tr_b16 v[226:227],v98 offset:17408\n"
"v_mfma_f32_16x16x32_bf16 a[220:223],v[156:159],v[172:175],a[220:223]\n"
"ds_read_b64_tr_b16 v[228:229],v99 offset:16384\n"
"v_mfma_f32_16x16x32_bf16 a[216:219],v[152:155],v[172:175],a[216:219]\n"
"ds_read_b64_tr_b16 v[230:231],v99 offset:17408\n"
"s_waitcnt lgkmcnt(10)\n"
"v_mfma_f32_16x16x32_bf16 a[224:227],v[152:155],v[176:179],a[224:227]\n"
"ds_read_b64_tr_b16 v[232:233],v100 offset:16384\n"
"v_mfma_f32_16x16x32_bf16 a[228:231],v[156:159],v[176:179],a[228:231]\n"
"ds_read_b64_tr_b16 v[234:235],v100 offset:17408\n"
"v_mfma_f32_16x16x32_bf16 a[236:239],v[156:159],v[180:183],a[236:239]\n"
"ds_read_b64_tr_b16 v[236:237],v101 offset:16384\n"
"v_mfma_f32_16x16x32_bf16 a[232:235],v[152:155],v[180:183],a[232:235]\n"
"ds_read_b64_tr_b16 v[238:239],v101 offset:17408\n"
"s_waitcnt lgkmcnt(10)\n"
"v_mfma_f32_16x16x32_bf16 a[240:243],v[152:155],v[184:187],a[240:243]\n"
"ds_read_b64_tr_b16 v[240:241],v102 offset:16384\n"
"v_mfma_f32_16x16x32_bf16 a[244:247],v[156:159],v[184:187],a[244:247]\n"
"ds_read_b64_tr_b16 v[242:243],v102 offset:17408\n"
"v_mfma_f32_16x16x32_bf16 a[252:255],v[156:159],v[188:191],a[252:255]\n"
"ds_read_b64_tr_b16 v[244:245],v103 offset:16384\n"
"v_mfma_f32_16x16x32_bf16 a[248:251],v[152:155],v[188:191],a[248:251]\n"
"ds_read_b64_tr_b16 v[246:247],v103 offset:17408\n"
"s_waitcnt lgkmcnt(8)\n"
"v_mfma_f32_16x16x32_bf16 a[0:3],v[128:131],v[160:163],a[0:3]\n"
"ds_read_b64_tr_b16 v[248:249],v104 offset:16384\n"
"v_mfma_f32_16x16x32_bf16 a[0:3],v[192:195],v[224:227],a[0:3]\n"
"ds_read_b64_tr_b16 v[250:251],v104 offset:17408\n"
"v_mfma_f32_16x16x32_bf16 a[8:11],v[192:195],v[228:231],a[8:11]\n"
"ds_read_b64_tr_b16 v[252:253],v105 offset:16384\n"
"v_mfma_f32_16x16x32_bf16 a[8:11],v[128:131],v[164:167],a[8:11]\n"
"ds_read_b64_tr_b16 v[254:255],v105 offset:17408\n"
"s_waitcnt lgkmcnt(8)\n"
"v_mfma_f32_16x16x32_bf16 a[16:19],v[128:131],v[168:171],a[16:19]\n"
"ds_read_b128 v[132:135],v80 offset:2048\n"
"v_mfma_f32_16x16x32_bf16 a[16:19],v[192:195],v[232:235],a[16:19]\n"
"ds_read_b128 v[196:199],v81 offset:2048\n"
"v_mfma_f32_16x16x32_bf16 a[24:27],v[192:195],v[236:239],a[24:27]\n"
"ds_read_b128 v[136:139],v80 offset:4096\n"
"v_mfma_f32_16x16x32_bf16 a[24:27],v[128:131],v[172:175],a[24:27]\n"
"ds_read_b128 v[200:203],v81 offset:4096\n"
"s_waitcnt lgkmcnt(8)\n"
"v_mfma_f32_16x16x32_bf16 a[32:35],v[128:131],v[176:179],a[32:35]\n"
"ds_read_b128 v[140:143],v80 offset:6144\n"
"v_mfma_f32_16x16x32_bf16 a[32:35],v[192:195],v[240:243],a[32:35]\n"
"ds_read_b128 v[204:207],v81 offset:6144\n"
"v_mfma_f32_16x16x32_bf16 a[40:43],v[192:195],v[244:247],a[40:43]\n"
"ds_read_b128 v[144:147],v80 offset:8192\n"
"v_mfma_f32_16x16x32_bf16 a[40:43],v[128:131],v[180:183],a[40:43]\n"
"ds_read_b128 v[208:211],v81 offset:8192\n"
"s_waitcnt lgkmcnt(8)\n"
"v_mfma_f32_16x16x32_bf16 a[48:51],v[128:131],v[184:187],a[48:51]\n"
"ds_read_b128 v[148:151],v80 offset:10240\n"
"v_mfma_f32_16x16x32_bf16 a[48:51],v[192:195],v[248:251],a[48:51]\n"
"ds_read_b128 v[212:215],v81 offset:10240\n"
"v_mfma_f32_16x16x32_bf16 a[56:59],v[192:195],v[252:255],a[56:59]\n"
"ds_read_b128 v[216:219],v81 offset:12288\n"
"v_mfma_f32_16x16x32_bf16 a[56:59],v[128:131],v[188:191],a[56:59]\n"
"ds_read_b128 v[220:223],v81 offset:14336\n"
"s_waitcnt lgkmcnt(10)\n"
"v_mfma_f32_16x16x32_bf16 a[4:7],v[132:135],v[160:163],a[4:7]\n"
"v_mfma_f32_16x16x32_bf16 a[4:7],v[196:199],v[224:227],a[4:7]\n"
"v_mfma_f32_16x16x32_bf16 a[12:15],v[196:199],v[228:231],a[12:15]\n"
"v_mfma_f32_16x16x32_bf16 a[12:15],v[132:135],v[164:167],a[12:15]\n"
"v_mfma_f32_16x16x32_bf16 a[20:23],v[132:135],v[168:171],a[20:23]\n"
"s_waitcnt lgkmcnt(0)\n"
"s_barrier\n"
"v_mfma_f32_16x16x32_bf16 a[20:23],v[196:199],v[232:235],a[20:23]\n"
"s_mov_b32 s77,2621440\n"
"s_add_u32 m0,s78,98304\n"
"v_mfma_f32_16x16x32_bf16 a[28:31],v[196:199],v[236:239],a[28:31]\n"
"buffer_load_dwordx4 v110,s[20:23],s77 offen offset:0 lds\n"
"v_mfma_f32_16x16x32_bf16 a[28:31],v[132:135],v[172:175],a[28:31]\n"
"v_mfma_f32_16x16x32_bf16 a[36:39],v[132:135],v[176:179],a[36:39]\n"
"v_mfma_f32_16x16x32_bf16 a[36:39],v[196:199],v[240:243],a[36:39]\n"
"v_mfma_f32_16x16x32_bf16 a[44:47],v[196:199],v[244:247],a[44:47]\n"
"buffer_load_dwordx4 v111,s[20:23],s77 offen offset:1024 lds\n"
"v_mfma_f32_16x16x32_bf16 a[44:47],v[132:135],v[180:183],a[44:47]\n"
"v_mfma_f32_16x16x32_bf16 a[52:55],v[132:135],v[184:187],a[52:55]\n"
"v_mfma_f32_16x16x32_bf16 a[52:55],v[196:199],v[248:251],a[52:55]\n"
"v_mfma_f32_16x16x32_bf16 a[60:63],v[196:199],v[252:255],a[60:63]\n"
"buffer_load_dwordx4 v113,s[20:23],s77 offen offset:2048 lds\n"
"v_mfma_f32_16x16x32_bf16 a[60:63],v[132:135],v[188:191],a[60:63]\n"
"v_mfma_f32_16x16x32_bf16 a[64:67],v[136:139],v[160:163],a[64:67]\n"
"v_mfma_f32_16x16x32_bf16 a[64:67],v[200:203],v[224:227],a[64:67]\n"
"v_mfma_f32_16x16x32_bf16 a[72:75],v[200:203],v[228:231],a[72:75]\n"
"buffer_load_dwordx4 v114,s[20:23],s77 offen offset:3072 lds\n"
"v_mfma_f32_16x16x32_bf16 a[72:75],v[136:139],v[164:167],a[72:75]\n"
"v_mfma_f32_16x16x32_bf16 a[80:83],v[136:139],v[168:171],a[80:83]\n"
"v_mfma_f32_16x16x32_bf16 a[80:83],v[200:203],v[232:235],a[80:83]\n"
"s_mov_b32 s77,2883584\n"
"s_add_u32 m0,s78,114688\n"
"v_mfma_f32_16x16x32_bf16 a[88:91],v[200:203],v[236:239],a[88:91]\n"
"buffer_load_dwordx4 v110,s[20:23],s77 offen offset:0 lds\n"
"v_mfma_f32_16x16x32_bf16 a[88:91],v[136:139],v[172:175],a[88:91]\n"
"v_mfma_f32_16x16x32_bf16 a[96:99],v[136:139],v[176:179],a[96:99]\n"
"v_mfma_f32_16x16x32_bf16 a[96:99],v[200:203],v[240:243],a[96:99]\n"
"v_mfma_f32_16x16x32_bf16 a[104:107],v[200:203],v[244:247],a[104:107]\n"
"buffer_load_dwordx4 v111,s[20:23],s77 offen offset:1024 lds\n"
"v_mfma_f32_16x16x32_bf16 a[104:107],v[136:139],v[180:183],a[104:107]\n"
"v_mfma_f32_16x16x32_bf16 a[112:115],v[136:139],v[184:187],a[112:115]\n"
"v_mfma_f32_16x16x32_bf16 a[112:115],v[200:203],v[248:251],a[112:115]\n"
"v_mfma_f32_16x16x32_bf16 a[120:123],v[200:203],v[252:255],a[120:123]\n"
"buffer_load_dwordx4 v113,s[20:23],s77 offen offset:2048 lds\n"
"v_mfma_f32_16x16x32_bf16 a[120:123],v[136:139],v[188:191],a[120:123]\n"
"v_mfma_f32_16x16x32_bf16 a[68:71],v[140:143],v[160:163],a[68:71]\n"
"v_mfma_f32_16x16x32_bf16 a[68:71],v[204:207],v[224:227],a[68:71]\n"
"v_mfma_f32_16x16x32_bf16 a[76:79],v[204:207],v[228:231],a[76:79]\n"
"buffer_load_dwordx4 v114,s[20:23],s77 offen offset:3072 lds\n"
"v_mfma_f32_16x16x32_bf16 a[76:79],v[140:143],v[164:167],a[76:79]\n"
"v_mfma_f32_16x16x32_bf16 a[84:87],v[140:143],v[168:171],a[84:87]\n"
"v_mfma_f32_16x16x32_bf16 a[84:87],v[204:207],v[232:235],a[84:87]\n"
"v_mfma_f32_16x16x32_bf16 a[92:95],v[204:207],v[236:239],a[92:95]\n"
"s_mov_b32 s76,768\n"
"s_add_u32 m0,s79,0\n"
"v_mfma_f32_16x16x32_bf16 a[92:95],v[140:143],v[172:175],a[92:95]\n"
"buffer_load_dwordx4 v106,s[16:19],s76 offen offset:0 lds\n"
"v_mfma_f32_16x16x32_bf16 a[100:103],v[140:143],v[176:179],a[100:103]\n"
"v_mfma_f32_16x16x32_bf16 a[100:103],v[204:207],v[240:243],a[100:103]\n"
"v_mfma_f32_16x16x32_bf16 a[108:111],v[204:207],v[244:247],a[108:111]\n"
"v_mfma_f32_16x16x32_bf16 a[108:111],v[140:143],v[180:183],a[108:111]\n"
"v_mfma_f32_16x16x32_bf16 a[116:119],v[140:143],v[184:187],a[116:119]\n"
"buffer_load_dwordx4 v107,s[16:19],s76 offen offset:1024 lds\n"
"v_mfma_f32_16x16x32_bf16 a[116:119],v[204:207],v[248:251],a[116:119]\n"
"v_mfma_f32_16x16x32_bf16 a[124:127],v[204:207],v[252:255],a[124:127]\n"
"v_mfma_f32_16x16x32_bf16 a[124:127],v[140:143],v[188:191],a[124:127]\n"
"v_mfma_f32_16x16x32_bf16 a[128:131],v[144:147],v[160:163],a[128:131]\n"
"v_mfma_f32_16x16x32_bf16 a[128:131],v[208:211],v[224:227],a[128:131]\n"
"buffer_load_dwordx4 v108,s[16:19],s76 offen offset:2048 lds\n"
"v_mfma_f32_16x16x32_bf16 a[136:139],v[208:211],v[228:231],a[136:139]\n"
"v_mfma_f32_16x16x32_bf16 a[136:139],v[144:147],v[164:167],a[136:139]\n"
"v_mfma_f32_16x16x32_bf16 a[144:147],v[144:147],v[168:171],a[144:147]\n"
"v_mfma_f32_16x16x32_bf16 a[144:147],v[208:211],v[232:235],a[144:147]\n"
"v_mfma_f32_16x16x32_bf16 a[152:155],v[208:211],v[236:239],a[152:155]\n"
"buffer_load_dwordx4 v109,s[16:19],s76 offen offset:3072 lds\n"
"v_mfma_f32_16x16x32_bf16 a[152:155],v[144:147],v[172:175],a[152:155]\n"
"v_mfma_f32_16x16x32_bf16 a[160:163],v[144:147],v[176:179],a[160:163]\n"
"v_mfma_f32_16x16x32_bf16 a[160:163],v[208:211],v[240:243],a[160:163]\n"
"v_mfma_f32_16x16x32_bf16 a[168:171],v[208:211],v[244:247],a[168:171]\n"
"s_mov_b32 s76,262912\n"
"s_add_u32 m0,s79,4096\n"
"v_mfma_f32_16x16x32_bf16 a[168:171],v[144:147],v[180:183],a[168:171]\n"
"buffer_load_dwordx4 v106,s[16:19],s76 offen offset:0 lds\n"
"v_mfma_f32_16x16x32_bf16 a[176:179],v[144:147],v[184:187],a[176:179]\n"
"v_mfma_f32_16x16x32_bf16 a[176:179],v[208:211],v[248:251],a[176:179]\n"
"v_mfma_f32_16x16x32_bf16 a[184:187],v[208:211],v[252:255],a[184:187]\n"
"v_mfma_f32_16x16x32_bf16 a[184:187],v[144:147],v[188:191],a[184:187]\n"
"v_mfma_f32_16x16x32_bf16 a[132:135],v[148:151],v[160:163],a[132:135]\n"
"buffer_load_dwordx4 v107,s[16:19],s76 offen offset:1024 lds\n"
"v_mfma_f32_16x16x32_bf16 a[132:135],v[212:215],v[224:227],a[132:135]\n"
"v_mfma_f32_16x16x32_bf16 a[140:143],v[212:215],v[228:231],a[140:143]\n"
"v_mfma_f32_16x16x32_bf16 a[140:143],v[148:151],v[164:167],a[140:143]\n"
"v_mfma_f32_16x16x32_bf16 a[148:151],v[148:151],v[168:171],a[148:151]\n"
"v_mfma_f32_16x16x32_bf16 a[148:151],v[212:215],v[232:235],a[148:151]\n"
"buffer_load_dwordx4 v108,s[16:19],s76 offen offset:2048 lds\n"
"v_mfma_f32_16x16x32_bf16 a[156:159],v[212:215],v[236:239],a[156:159]\n"
"v_mfma_f32_16x16x32_bf16 a[156:159],v[148:151],v[172:175],a[156:159]\n"
"v_mfma_f32_16x16x32_bf16 a[164:167],v[148:151],v[176:179],a[164:167]\n"
"v_mfma_f32_16x16x32_bf16 a[164:167],v[212:215],v[240:243],a[164:167]\n"
"v_mfma_f32_16x16x32_bf16 a[172:175],v[212:215],v[244:247],a[172:175]\n"
"buffer_load_dwordx4 v109,s[16:19],s76 offen offset:3072 lds\n"
"v_mfma_f32_16x16x32_bf16 a[172:175],v[148:151],v[180:183],a[172:175]\n"
"s_mov_b32 s87,3145728\n"
"v_mfma_f32_16x16x32_bf16 a[180:183],v[148:151],v[184:187],a[180:183]\n"
"buffer_load_dword v115,v112,s[40:43],s87 offen\n"
"v_mfma_f32_16x16x32_bf16 a[180:183],v[212:215],v[248:251],a[180:183]\n"
"s_mov_b32 s87,4194304\n"
"v_mfma_f32_16x16x32_bf16 a[188:191],v[212:215],v[252:255],a[188:191]\n"
"buffer_load_dword v115,v112,s[40:43],s87 offen\n"
"v_mfma_f32_16x16x32_bf16 a[188:191],v[148:151],v[188:191],a[188:191]\n"
"s_mov_b32 s87,5242880\n"
"s_waitcnt vmcnt(33)\n"
"s_barrier\n"
"v_mfma_f32_16x16x32_bf16 a[192:195],v[216:219],v[224:227],a[192:195]\n"
"ds_read_b128 v[152:155],v80 offset:45056\n"
"buffer_load_dword v115,v112,s[40:43],s87 offen\n"
"v_mfma_f32_16x16x32_bf16 a[196:199],v[220:223],v[224:227],a[196:199]\n"
"ds_read_b128 v[156:159],v80 offset:47104\n"
"v_mfma_f32_16x16x32_bf16 a[204:207],v[220:223],v[228:231],a[204:207]\n"
"ds_read_b64_tr_b16 v[160:161],v98 offset:49152\n"
"v_mfma_f32_16x16x32_bf16 a[200:203],v[216:219],v[228:231],a[200:203]\n"
"ds_read_b64_tr_b16 v[162:163],v98 offset:50176\n"
"v_mfma_f32_16x16x32_bf16 a[208:211],v[216:219],v[232:235],a[208:211]\n"
"ds_read_b64_tr_b16 v[164:165],v99 offset:49152\n"
"v_mfma_f32_16x16x32_bf16 a[212:215],v[220:223],v[232:235],a[212:215]\n"
"ds_read_b64_tr_b16 v[166:167],v99 offset:50176\n"
"v_mfma_f32_16x16x32_bf16 a[220:223],v[220:223],v[236:239],a[220:223]\n"
"ds_read_b64_tr_b16 v[168:169],v100 offset:49152\n"
"v_mfma_f32_16x16x32_bf16 a[216:219],v[216:219],v[236:239],a[216:219]\n"
"ds_read_b64_tr_b16 v[170:171],v100 offset:50176\n"
"v_mfma_f32_16x16x32_bf16 a[224:227],v[216:219],v[240:243],a[224:227]\n"
"ds_read_b64_tr_b16 v[172:173],v101 offset:49152\n"
"v_mfma_f32_16x16x32_bf16 a[228:231],v[220:223],v[240:243],a[228:231]\n"
"ds_read_b64_tr_b16 v[174:175],v101 offset:50176\n"
"v_mfma_f32_16x16x32_bf16 a[236:239],v[220:223],v[244:247],a[236:239]\n"
"ds_read_b64_tr_b16 v[176:177],v102 offset:49152\n"
"v_mfma_f32_16x16x32_bf16 a[232:235],v[216:219],v[244:247],a[232:235]\n"
"ds_read_b64_tr_b16 v[178:179],v102 offset:50176\n"
"v_mfma_f32_16x16x32_bf16 a[240:243],v[216:219],v[248:251],a[240:243]\n"
"ds_read_b64_tr_b16 v[180:181],v103 offset:49152\n"
"v_mfma_f32_16x16x32_bf16 a[244:247],v[220:223],v[248:251],a[244:247]\n"
"ds_read_b64_tr_b16 v[182:183],v103 offset:50176\n"
"v_mfma_f32_16x16x32_bf16 a[252:255],v[220:223],v[252:255],a[252:255]\n"
"ds_read_b64_tr_b16 v[184:185],v104 offset:49152\n"
"ds_read_b64_tr_b16 v[186:187],v104 offset:50176\n"
"v_mfma_f32_16x16x32_bf16 a[248:251],v[216:219],v[252:255],a[248:251]\n"
"s_waitcnt vmcnt(25)\n"
"s_waitcnt lgkmcnt(10)\n"
"v_mfma_f32_16x16x32_bf16 a[192:195],v[152:155],v[160:163],a[192:195]\n"
"ds_read_b64_tr_b16 v[188:189],v105 offset:49152\n"
"ds_read_b64_tr_b16 v[190:191],v105 offset:50176\n"
"v_mfma_f32_16x16x32_bf16 a[196:199],v[156:159],v[160:163],a[196:199]\n"
"buffer_store_dwordx2 v[124:125],off,s[24:27],s57 sc1\n"
"v_mfma_f32_16x16x32_bf16 a[204:207],v[156:159],v[164:167],a[204:207]\n"
"ds_read_b128 v[128:131],v80 offset:32768\n"
"v_mfma_f32_16x16x32_bf16 a[200:203],v[152:155],v[164:167],a[200:203]\n"
"ds_read_b128 v[192:195],v81 offset:32768\n"
"s_waitcnt lgkmcnt(10)\n"
"v_mfma_f32_16x16x32_bf16 a[208:211],v[152:155],v[168:171],a[208:211]\n"
"ds_read_b64_tr_b16 v[224:225],v98 offset:32768\n"
"v_mfma_f32_16x16x32_bf16 a[212:215],v[156:159],v[168:171],a[212:215]\n"
"ds_read_b64_tr_b16 v[226:227],v98 offset:33792\n"
"v_mfma_f32_16x16x32_bf16 a[220:223],v[156:159],v[172:175],a[220:223]\n"
"ds_read_b64_tr_b16 v[228:229],v99 offset:32768\n"
"v_mfma_f32_16x16x32_bf16 a[216:219],v[152:155],v[172:175],a[216:219]\n"
"ds_read_b64_tr_b16 v[230:231],v99 offset:33792\n"
"s_waitcnt lgkmcnt(10)\n"
"v_mfma_f32_16x16x32_bf16 a[224:227],v[152:155],v[176:179],a[224:227]\n"
"ds_read_b64_tr_b16 v[232:233],v100 offset:32768\n"
"v_mfma_f32_16x16x32_bf16 a[228:231],v[156:159],v[176:179],a[228:231]\n"
"ds_read_b64_tr_b16 v[234:235],v100 offset:33792\n"
"v_mfma_f32_16x16x32_bf16 a[236:239],v[156:159],v[180:183],a[236:239]\n"
"ds_read_b64_tr_b16 v[236:237],v101 offset:32768\n"
"v_mfma_f32_16x16x32_bf16 a[232:235],v[152:155],v[180:183],a[232:235]\n"
"ds_read_b64_tr_b16 v[238:239],v101 offset:33792\n"
"s_waitcnt lgkmcnt(10)\n"
"v_mfma_f32_16x16x32_bf16 a[240:243],v[152:155],v[184:187],a[240:243]\n"
"ds_read_b64_tr_b16 v[240:241],v102 offset:32768\n"
"v_mfma_f32_16x16x32_bf16 a[244:247],v[156:159],v[184:187],a[244:247]\n"
"ds_read_b64_tr_b16 v[242:243],v102 offset:33792\n"
"v_mfma_f32_16x16x32_bf16 a[252:255],v[156:159],v[188:191],a[252:255]\n"
"ds_read_b64_tr_b16 v[244:245],v103 offset:32768\n"
"v_mfma_f32_16x16x32_bf16 a[248:251],v[152:155],v[188:191],a[248:251]\n"
"ds_read_b64_tr_b16 v[246:247],v103 offset:33792\n"
"s_waitcnt lgkmcnt(8)\n"
"v_mfma_f32_16x16x32_bf16 a[0:3],v[128:131],v[160:163],a[0:3]\n"
"ds_read_b64_tr_b16 v[248:249],v104 offset:32768\n"
"v_mfma_f32_16x16x32_bf16 a[0:3],v[192:195],v[224:227],a[0:3]\n"
"ds_read_b64_tr_b16 v[250:251],v104 offset:33792\n"
"v_mfma_f32_16x16x32_bf16 a[8:11],v[192:195],v[228:231],a[8:11]\n"
"ds_read_b64_tr_b16 v[252:253],v105 offset:32768\n"
"v_mfma_f32_16x16x32_bf16 a[8:11],v[128:131],v[164:167],a[8:11]\n"
"ds_read_b64_tr_b16 v[254:255],v105 offset:33792\n"
"s_waitcnt lgkmcnt(8)\n"
"v_mfma_f32_16x16x32_bf16 a[16:19],v[128:131],v[168:171],a[16:19]\n"
"ds_read_b128 v[132:135],v80 offset:34816\n"
"v_mfma_f32_16x16x32_bf16 a[16:19],v[192:195],v[232:235],a[16:19]\n"
"ds_read_b128 v[196:199],v81 offset:34816\n"
"v_mfma_f32_16x16x32_bf16 a[24:27],v[192:195],v[236:239],a[24:27]\n"
"ds_read_b128 v[136:139],v80 offset:36864\n"
"v_mfma_f32_16x16x32_bf16 a[24:27],v[128:131],v[172:175],a[24:27]\n"
"ds_read_b128 v[200:203],v81 offset:36864\n"
"s_waitcnt lgkmcnt(8)\n"
"v_mfma_f32_16x16x32_bf16 a[32:35],v[128:131],v[176:179],a[32:35]\n"
"ds_read_b128 v[140:143],v80 offset:38912\n"
"v_mfma_f32_16x16x32_bf16 a[32:35],v[192:195],v[240:243],a[32:35]\n"
"ds_read_b128 v[204:207],v81 offset:38912\n"
"v_mfma_f32_16x16x32_bf16 a[40:43],v[192:195],v[244:247],a[40:43]\n"
"ds_read_b128 v[144:147],v80 offset:40960\n"
"v_mfma_f32_16x16x32_bf16 a[40:43],v[128:131],v[180:183],a[40:43]\n"
"ds_read_b128 v[208:211],v81 offset:40960\n"
"s_waitcnt lgkmcnt(8)\n"
"v_mfma_f32_16x16x32_bf16 a[48:51],v[128:131],v[184:187],a[48:51]\n"
"ds_read_b128 v[148:151],v80 offset:43008\n"
"v_mfma_f32_16x16x32_bf16 a[48:51],v[192:195],v[248:251],a[48:51]\n"
"ds_read_b128 v[212:215],v81 offset:43008\n"
"v_mfma_f32_16x16x32_bf16 a[56:59],v[192:195],v[252:255],a[56:59]\n"
"ds_read_b128 v[216:219],v81 offset:45056\n"
"v_mfma_f32_16x16x32_bf16 a[56:59],v[128:131],v[188:191],a[56:59]\n"
"ds_read_b128 v[220:223],v81 offset:47104\n"
"s_waitcnt lgkmcnt(10)\n"
"v_mfma_f32_16x16x32_bf16 a[4:7],v[132:135],v[160:163],a[4:7]\n"
"v_mfma_f32_16x16x32_bf16 a[4:7],v[196:199],v[224:227],a[4:7]\n"
"v_mfma_f32_16x16x32_bf16 a[12:15],v[196:199],v[228:231],a[12:15]\n"
"v_mfma_f32_16x16x32_bf16 a[12:15],v[132:135],v[164:167],a[12:15]\n"
"v_mfma_f32_16x16x32_bf16 a[20:23],v[132:135],v[168:171],a[20:23]\n"
"s_waitcnt lgkmcnt(0)\n"
"s_barrier\n"
"v_mfma_f32_16x16x32_bf16 a[20:23],v[196:199],v[232:235],a[20:23]\n"
"s_mov_b32 s77,3145728\n"
"s_add_u32 m0,s78,147456\n"
"v_mfma_f32_16x16x32_bf16 a[28:31],v[196:199],v[236:239],a[28:31]\n"
"buffer_load_dwordx4 v110,s[20:23],s77 offen offset:0 lds\n"
"v_mfma_f32_16x16x32_bf16 a[28:31],v[132:135],v[172:175],a[28:31]\n"
"v_mfma_f32_16x16x32_bf16 a[36:39],v[132:135],v[176:179],a[36:39]\n"
"v_mfma_f32_16x16x32_bf16 a[36:39],v[196:199],v[240:243],a[36:39]\n"
"v_mfma_f32_16x16x32_bf16 a[44:47],v[196:199],v[244:247],a[44:47]\n"
"buffer_load_dwordx4 v111,s[20:23],s77 offen offset:1024 lds\n"
"v_mfma_f32_16x16x32_bf16 a[44:47],v[132:135],v[180:183],a[44:47]\n"
"v_mfma_f32_16x16x32_bf16 a[52:55],v[132:135],v[184:187],a[52:55]\n"
"v_mfma_f32_16x16x32_bf16 a[52:55],v[196:199],v[248:251],a[52:55]\n"
"v_mfma_f32_16x16x32_bf16 a[60:63],v[196:199],v[252:255],a[60:63]\n"
"buffer_load_dwordx4 v113,s[20:23],s77 offen offset:2048 lds\n"
"v_mfma_f32_16x16x32_bf16 a[60:63],v[132:135],v[188:191],a[60:63]\n"
"v_mfma_f32_16x16x32_bf16 a[64:67],v[136:139],v[160:163],a[64:67]\n"
"v_mfma_f32_16x16x32_bf16 a[64:67],v[200:203],v[224:227],a[64:67]\n"
"v_mfma_f32_16x16x32_bf16 a[72:75],v[200:203],v[228:231],a[72:75]\n"
"buffer_load_dwordx4 v114,s[20:23],s77 offen offset:3072 lds\n"
"v_mfma_f32_16x16x32_bf16 a[72:75],v[136:139],v[164:167],a[72:75]\n"
"v_mfma_f32_16x16x32_bf16 a[80:83],v[136:139],v[168:171],a[80:83]\n"
"v_mfma_f32_16x16x32_bf16 a[80:83],v[200:203],v[232:235],a[80:83]\n"
"s_mov_b32 s77,3407872\n"
"s_add_u32 m0,s78,131072\n"
"v_mfma_f32_16x16x32_bf16 a[88:91],v[200:203],v[236:239],a[88:91]\n"
"buffer_load_dwordx4 v110,s[20:23],s77 offen offset:0 lds\n"
"v_mfma_f32_16x16x32_bf16 a[88:91],v[136:139],v[172:175],a[88:91]\n"
"v_mfma_f32_16x16x32_bf16 a[96:99],v[136:139],v[176:179],a[96:99]\n"
"v_mfma_f32_16x16x32_bf16 a[96:99],v[200:203],v[240:243],a[96:99]\n"
"v_mfma_f32_16x16x32_bf16 a[104:107],v[200:203],v[244:247],a[104:107]\n"
"buffer_load_dwordx4 v111,s[20:23],s77 offen offset:1024 lds\n"
"v_mfma_f32_16x16x32_bf16 a[104:107],v[136:139],v[180:183],a[104:107]\n"
"v_mfma_f32_16x16x32_bf16 a[112:115],v[136:139],v[184:187],a[112:115]\n"
"v_mfma_f32_16x16x32_bf16 a[112:115],v[200:203],v[248:251],a[112:115]\n"
"v_mfma_f32_16x16x32_bf16 a[120:123],v[200:203],v[252:255],a[120:123]\n"
"buffer_load_dwordx4 v113,s[20:23],s77 offen offset:2048 lds\n"
"v_mfma_f32_16x16x32_bf16 a[120:123],v[136:139],v[188:191],a[120:123]\n"
"v_mfma_f32_16x16x32_bf16 a[68:71],v[140:143],v[160:163],a[68:71]\n"
"v_mfma_f32_16x16x32_bf16 a[68:71],v[204:207],v[224:227],a[68:71]\n"
"v_mfma_f32_16x16x32_bf16 a[76:79],v[204:207],v[228:231],a[76:79]\n"
"buffer_load_dwordx4 v114,s[20:23],s77 offen offset:3072 lds\n"
"v_mfma_f32_16x16x32_bf16 a[76:79],v[140:143],v[164:167],a[76:79]\n"
"v_mfma_f32_16x16x32_bf16 a[84:87],v[140:143],v[168:171],a[84:87]\n"
"v_mfma_f32_16x16x32_bf16 a[84:87],v[204:207],v[232:235],a[84:87]\n"
"v_mfma_f32_16x16x32_bf16 a[92:95],v[204:207],v[236:239],a[92:95]\n"
"s_mov_b32 s76,896\n"
"s_add_u32 m0,s79,32768\n"
"v_mfma_f32_16x16x32_bf16 a[92:95],v[140:143],v[172:175],a[92:95]\n"
"buffer_load_dwordx4 v106,s[16:19],s76 offen offset:0 lds\n"
"v_mfma_f32_16x16x32_bf16 a[100:103],v[140:143],v[176:179],a[100:103]\n"
"v_mfma_f32_16x16x32_bf16 a[100:103],v[204:207],v[240:243],a[100:103]\n"
"v_mfma_f32_16x16x32_bf16 a[108:111],v[204:207],v[244:247],a[108:111]\n"
"v_mfma_f32_16x16x32_bf16 a[108:111],v[140:143],v[180:183],a[108:111]\n"
"v_mfma_f32_16x16x32_bf16 a[116:119],v[140:143],v[184:187],a[116:119]\n"
"buffer_load_dwordx4 v107,s[16:19],s76 offen offset:1024 lds\n"
"v_mfma_f32_16x16x32_bf16 a[116:119],v[204:207],v[248:251],a[116:119]\n"
"v_mfma_f32_16x16x32_bf16 a[124:127],v[204:207],v[252:255],a[124:127]\n"
"v_mfma_f32_16x16x32_bf16 a[124:127],v[140:143],v[188:191],a[124:127]\n"
"v_mfma_f32_16x16x32_bf16 a[128:131],v[144:147],v[160:163],a[128:131]\n"
"v_mfma_f32_16x16x32_bf16 a[128:131],v[208:211],v[224:227],a[128:131]\n"
"buffer_load_dwordx4 v108,s[16:19],s76 offen offset:2048 lds\n"
"v_mfma_f32_16x16x32_bf16 a[136:139],v[208:211],v[228:231],a[136:139]\n"
"v_mfma_f32_16x16x32_bf16 a[136:139],v[144:147],v[164:167],a[136:139]\n"
"v_mfma_f32_16x16x32_bf16 a[144:147],v[144:147],v[168:171],a[144:147]\n"
"v_mfma_f32_16x16x32_bf16 a[144:147],v[208:211],v[232:235],a[144:147]\n"
"v_mfma_f32_16x16x32_bf16 a[152:155],v[208:211],v[236:239],a[152:155]\n"
"buffer_load_dwordx4 v109,s[16:19],s76 offen offset:3072 lds\n"
"v_mfma_f32_16x16x32_bf16 a[152:155],v[144:147],v[172:175],a[152:155]\n"
"v_mfma_f32_16x16x32_bf16 a[160:163],v[144:147],v[176:179],a[160:163]\n"
"v_mfma_f32_16x16x32_bf16 a[160:163],v[208:211],v[240:243],a[160:163]\n"
"v_mfma_f32_16x16x32_bf16 a[168:171],v[208:211],v[244:247],a[168:171]\n"
"s_mov_b32 s76,263040\n"
"s_add_u32 m0,s79,36864\n"
"v_mfma_f32_16x16x32_bf16 a[168:171],v[144:147],v[180:183],a[168:171]\n"
"buffer_load_dwordx4 v106,s[16:19],s76 offen offset:0 lds\n"
"v_mfma_f32_16x16x32_bf16 a[176:179],v[144:147],v[184:187],a[176:179]\n"
"v_mfma_f32_16x16x32_bf16 a[176:179],v[208:211],v[248:251],a[176:179]\n"
"v_mfma_f32_16x16x32_bf16 a[184:187],v[208:211],v[252:255],a[184:187]\n"
"v_mfma_f32_16x16x32_bf16 a[184:187],v[144:147],v[188:191],a[184:187]\n"
"v_mfma_f32_16x16x32_bf16 a[132:135],v[148:151],v[160:163],a[132:135]\n"
"buffer_load_dwordx4 v107,s[16:19],s76 offen offset:1024 lds\n"
"v_mfma_f32_16x16x32_bf16 a[132:135],v[212:215],v[224:227],a[132:135]\n"
"v_mfma_f32_16x16x32_bf16 a[140:143],v[212:215],v[228:231],a[140:143]\n"
"v_mfma_f32_16x16x32_bf16 a[140:143],v[148:151],v[164:167],a[140:143]\n"
"v_mfma_f32_16x16x32_bf16 a[148:151],v[148:151],v[168:171],a[148:151]\n"
"v_mfma_f32_16x16x32_bf16 a[148:151],v[212:215],v[232:235],a[148:151]\n"
"buffer_load_dwordx4 v108,s[16:19],s76 offen offset:2048 lds\n"
"v_mfma_f32_16x16x32_bf16 a[156:159],v[212:215],v[236:239],a[156:159]\n"
"v_mfma_f32_16x16x32_bf16 a[156:159],v[148:151],v[172:175],a[156:159]\n"
"v_mfma_f32_16x16x32_bf16 a[164:167],v[148:151],v[176:179],a[164:167]\n"
"v_mfma_f32_16x16x32_bf16 a[164:167],v[212:215],v[240:243],a[164:167]\n"
"v_mfma_f32_16x16x32_bf16 a[172:175],v[212:215],v[244:247],a[172:175]\n"
"buffer_load_dwordx4 v109,s[16:19],s76 offen offset:3072 lds\n"
"v_mfma_f32_16x16x32_bf16 a[172:175],v[148:151],v[180:183],a[172:175]\n"
"s_mov_b32 s87,6291456\n"
"v_mfma_f32_16x16x32_bf16 a[180:183],v[148:151],v[184:187],a[180:183]\n"
"buffer_load_dword v115,v112,s[40:43],s87 offen\n"
"v_mfma_f32_16x16x32_bf16 a[180:183],v[212:215],v[248:251],a[180:183]\n"
"s_mov_b32 s87,7340032\n"
"v_mfma_f32_16x16x32_bf16 a[188:191],v[212:215],v[252:255],a[188:191]\n"
"buffer_load_dword v115,v112,s[40:43],s87 offen\n"
"v_mfma_f32_16x16x32_bf16 a[188:191],v[148:151],v[188:191],a[188:191]\n"
"s_waitcnt vmcnt(30)\n"
"s_barrier\n"
"v_mfma_f32_16x16x32_bf16 a[192:195],v[216:219],v[224:227],a[192:195]\n"
"ds_read_b128 v[152:155],v82 offset:12288\n"
"v_mfma_f32_16x16x32_bf16 a[196:199],v[220:223],v[224:227],a[196:199]\n"
"ds_read_b128 v[156:159],v82 offset:14336\n"
"v_mfma_f32_16x16x32_bf16 a[204:207],v[220:223],v[228:231],a[204:207]\n"
"ds_read_b64_tr_b16 v[160:161],v98 offset:0\n"
"v_mfma_f32_16x16x32_bf16 a[200:203],v[216:219],v[228:231],a[200:203]\n"
"ds_read_b64_tr_b16 v[162:163],v98 offset:1024\n"
"v_mfma_f32_16x16x32_bf16 a[208:211],v[216:219],v[232:235],a[208:211]\n"
"ds_read_b64_tr_b16 v[164:165],v99 offset:0\n"
"v_mfma_f32_16x16x32_bf16 a[212:215],v[220:223],v[232:235],a[212:215]\n"
"ds_read_b64_tr_b16 v[166:167],v99 offset:1024\n"
"v_mfma_f32_16x16x32_bf16 a[220:223],v[220:223],v[236:239],a[220:223]\n"
"ds_read_b64_tr_b16 v[168:169],v100 offset:0\n"
"v_mfma_f32_16x16x32_bf16 a[216:219],v[216:219],v[236:239],a[216:219]\n"
"ds_read_b64_tr_b16 v[170:171],v100 offset:1024\n"
"v_mfma_f32_16x16x32_bf16 a[224:227],v[216:219],v[240:243],a[224:227]\n"
"ds_read_b64_tr_b16 v[172:173],v101 offset:0\n"
"v_mfma_f32_16x16x32_bf16 a[228:231],v[220:223],v[240:243],a[228:231]\n"
"ds_read_b64_tr_b16 v[174:175],v101 offset:1024\n"
"v_mfma_f32_16x16x32_bf16 a[236:239],v[220:223],v[244:247],a[236:239]\n"
"ds_read_b64_tr_b16 v[176:177],v102 offset:0\n"
"v_mfma_f32_16x16x32_bf16 a[232:235],v[216:219],v[244:247],a[232:235]\n"
"ds_read_b64_tr_b16 v[178:179],v102 offset:1024\n"
"v_mfma_f32_16x16x32_bf16 a[240:243],v[216:219],v[248:251],a[240:243]\n"
"ds_read_b64_tr_b16 v[180:181],v103 offset:0\n"
"v_mfma_f32_16x16x32_bf16 a[244:247],v[220:223],v[248:251],a[244:247]\n"
"ds_read_b64_tr_b16 v[182:183],v103 offset:1024\n"
"v_mfma_f32_16x16x32_bf16 a[252:255],v[220:223],v[252:255],a[252:255]\n"
"ds_read_b64_tr_b16 v[184:185],v104 offset:0\n"
"v_mfma_f32_16x16x32_bf16 a[248:251],v[216:219],v[252:255],a[248:251]\n"
"ds_read_b64_tr_b16 v[186:187],v104 offset:1024\n"
"s_waitcnt lgkmcnt(10)\n"
"v_mfma_f32_16x16x32_bf16 a[192:195],v[152:155],v[160:163],a[192:195]\n"
"ds_read_b64_tr_b16 v[188:189],v105 offset:0\n"
"v_mfma_f32_16x16x32_bf16 a[196:199],v[156:159],v[160:163],a[196:199]\n"
"ds_read_b64_tr_b16 v[190:191],v105 offset:1024\n"
"v_mfma_f32_16x16x32_bf16 a[204:207],v[156:159],v[164:167],a[204:207]\n"
"ds_read_b128 v[128:131],v82 offset:0\n"
"v_mfma_f32_16x16x32_bf16 a[200:203],v[152:155],v[164:167],a[200:203]\n"
"ds_read_b128 v[192:195],v83 offset:0\n"
"s_waitcnt lgkmcnt(10)\n"
"v_mfma_f32_16x16x32_bf16 a[208:211],v[152:155],v[168:171],a[208:211]\n"
"ds_read_b64_tr_b16 v[224:225],v98 offset:16384\n"
"v_mfma_f32_16x16x32_bf16 a[212:215],v[156:159],v[168:171],a[212:215]\n"
"ds_read_b64_tr_b16 v[226:227],v98 offset:17408\n"
"v_mfma_f32_16x16x32_bf16 a[220:223],v[156:159],v[172:175],a[220:223]\n"
"ds_read_b64_tr_b16 v[228:229],v99 offset:16384\n"
"v_mfma_f32_16x16x32_bf16 a[216:219],v[152:155],v[172:175],a[216:219]\n"
"ds_read_b64_tr_b16 v[230:231],v99 offset:17408\n"
"s_waitcnt lgkmcnt(10)\n"
"v_mfma_f32_16x16x32_bf16 a[224:227],v[152:155],v[176:179],a[224:227]\n"
"ds_read_b64_tr_b16 v[232:233],v100 offset:16384\n"
"v_mfma_f32_16x16x32_bf16 a[228:231],v[156:159],v[176:179],a[228:231]\n"
"ds_read_b64_tr_b16 v[234:235],v100 offset:17408\n"
"v_mfma_f32_16x16x32_bf16 a[236:239],v[156:159],v[180:183],a[236:239]\n"
"ds_read_b64_tr_b16 v[236:237],v101 offset:16384\n"
"v_mfma_f32_16x16x32_bf16 a[232:235],v[152:155],v[180:183],a[232:235]\n"
"ds_read_b64_tr_b16 v[238:239],v101 offset:17408\n"
"s_waitcnt lgkmcnt(10)\n"
"v_mfma_f32_16x16x32_bf16 a[240:243],v[152:155],v[184:187],a[240:243]\n"
"ds_read_b64_tr_b16 v[240:241],v102 offset:16384\n"
"v_mfma_f32_16x16x32_bf16 a[244:247],v[156:159],v[184:187],a[244:247]\n"
"ds_read_b64_tr_b16 v[242:243],v102 offset:17408\n"
"v_mfma_f32_16x16x32_bf16 a[252:255],v[156:159],v[188:191],a[252:255]\n"
"ds_read_b64_tr_b16 v[244:245],v103 offset:16384\n"
"v_mfma_f32_16x16x32_bf16 a[248:251],v[152:155],v[188:191],a[248:251]\n"
"ds_read_b64_tr_b16 v[246:247],v103 offset:17408\n"
"s_waitcnt lgkmcnt(8)\n"
"v_mfma_f32_16x16x32_bf16 a[0:3],v[128:131],v[160:163],a[0:3]\n"
"ds_read_b64_tr_b16 v[248:249],v104 offset:16384\n"
"v_mfma_f32_16x16x32_bf16 a[0:3],v[192:195],v[224:227],a[0:3]\n"
"ds_read_b64_tr_b16 v[250:251],v104 offset:17408\n"
"v_mfma_f32_16x16x32_bf16 a[8:11],v[192:195],v[228:231],a[8:11]\n"
"ds_read_b64_tr_b16 v[252:253],v105 offset:16384\n"
"v_mfma_f32_16x16x32_bf16 a[8:11],v[128:131],v[164:167],a[8:11]\n"
"ds_read_b64_tr_b16 v[254:255],v105 offset:17408\n"
"s_waitcnt lgkmcnt(8)\n"
"v_mfma_f32_16x16x32_bf16 a[16:19],v[128:131],v[168:171],a[16:19]\n"
"ds_read_b128 v[132:135],v82 offset:2048\n"
"v_mfma_f32_16x16x32_bf16 a[16:19],v[192:195],v[232:235],a[16:19]\n"
"ds_read_b128 v[196:199],v83 offset:2048\n"
"v_mfma_f32_16x16x32_bf16 a[24:27],v[192:195],v[236:239],a[24:27]\n"
"ds_read_b128 v[136:139],v82 offset:4096\n"
"v_mfma_f32_16x16x32_bf16 a[24:27],v[128:131],v[172:175],a[24:27]\n"
"ds_read_b128 v[200:203],v83 offset:4096\n"
"s_waitcnt lgkmcnt(8)\n"
"v_mfma_f32_16x16x32_bf16 a[32:35],v[128:131],v[176:179],a[32:35]\n"
"ds_read_b128 v[140:143],v82 offset:6144\n"
"v_mfma_f32_16x16x32_bf16 a[32:35],v[192:195],v[240:243],a[32:35]\n"
"ds_read_b128 v[204:207],v83 offset:6144\n"
"v_mfma_f32_16x16x32_bf16 a[40:43],v[192:195],v[244:247],a[40:43]\n"
"ds_read_b128 v[144:147],v82 offset:8192\n"
"v_mfma_f32_16x16x32_bf16 a[40:43],v[128:131],v[180:183],a[40:43]\n"
"ds_read_b128 v[208:211],v83 offset:8192\n"
"s_waitcnt lgkmcnt(8)\n"
"v_mfma_f32_16x16x32_bf16 a[48:51],v[128:131],v[184:187],a[48:51]\n"
"ds_read_b128 v[148:151],v82 offset:10240\n"
"v_mfma_f32_16x16x32_bf16 a[48:51],v[192:195],v[248:251],a[48:51]\n"
"ds_read_b128 v[212:215],v83 offset:10240\n"
"v_mfma_f32_16x16x32_bf16 a[56:59],v[192:195],v[252:255],a[56:59]\n"
"ds_read_b128 v[216:219],v83 offset:12288\n"
"v_mfma_f32_16x16x32_bf16 a[56:59],v[128:131],v[188:191],a[56:59]\n"
"ds_read_b128 v[220:223],v83 offset:14336\n"
"s_waitcnt lgkmcnt(10)\n"
"v_mfma_f32_16x16x32_bf16 a[4:7],v[132:135],v[160:163],a[4:7]\n"
"v_mfma_f32_16x16x32_bf16 a[4:7],v[196:199],v[224:227],a[4:7]\n"
"buffer_load_dwordx4 v[74:77],v112,s[24:27],s58 offen sc1\n"
"v_mfma_f32_16x16x32_bf16 a[12:15],v[196:199],v[228:231],a[12:15]\n"
"v_mfma_f32_16x16x32_bf16 a[12:15],v[132:135],v[164:167],a[12:15]\n"
"v_mfma_f32_16x16x32_bf16 a[20:23],v[132:135],v[168:171],a[20:23]\n"
"s_waitcnt lgkmcnt(0)\n"
"s_barrier\n"
"v_mfma_f32_16x16x32_bf16 a[20:23],v[196:199],v[232:235],a[20:23]\n"
"s_mov_b32 s77,3670016\n"
"s_add_u32 m0,s78,98304\n"
"v_mfma_f32_16x16x32_bf16 a[28:31],v[196:199],v[236:239],a[28:31]\n"
"buffer_load_dwordx4 v110,s[20:23],s77 offen offset:0 lds\n"
"v_mfma_f32_16x16x32_bf16 a[28:31],v[132:135],v[172:175],a[28:31]\n"
"v_mfma_f32_16x16x32_bf16 a[36:39],v[132:135],v[176:179],a[36:39]\n"
"s_load_dwordx2 s[48:49],s[0:1],56\n"
"v_mfma_f32_16x16x32_bf16 a[36:39],v[196:199],v[240:243],a[36:39]\n"
"v_mfma_f32_16x16x32_bf16 a[44:47],v[196:199],v[244:247],a[44:47]\n"
"buffer_load_dwordx4 v111,s[20:23],s77 offen offset:1024 lds\n"
"v_mfma_f32_16x16x32_bf16 a[44:47],v[132:135],v[180:183],a[44:47]\n"
"v_mfma_f32_16x16x32_bf16 a[52:55],v[132:135],v[184:187],a[52:55]\n"
"v_mfma_f32_16x16x32_bf16 a[52:55],v[196:199],v[248:251],a[52:55]\n"
"v_mfma_f32_16x16x32_bf16 a[60:63],v[196:199],v[252:255],a[60:63]\n"
"buffer_load_dwordx4 v113,s[20:23],s77 offen offset:2048 lds\n"
"v_mfma_f32_16x16x32_bf16 a[60:63],v[132:135],v[188:191],a[60:63]\n"
"v_mfma_f32_16x16x32_bf16 a[64:67],v[136:139],v[160:163],a[64:67]\n"
"v_mfma_f32_16x16x32_bf16 a[64:67],v[200:203],v[224:227],a[64:67]\n"
"v_mfma_f32_16x16x32_bf16 a[72:75],v[200:203],v[228:231],a[72:75]\n"
"buffer_load_dwordx4 v114,s[20:23],s77 offen offset:3072 lds\n"
"v_mfma_f32_16x16x32_bf16 a[72:75],v[136:139],v[164:167],a[72:75]\n"
"v_mfma_f32_16x16x32_bf16 a[80:83],v[136:139],v[168:171],a[80:83]\n"
"v_mfma_f32_16x16x32_bf16 a[80:83],v[200:203],v[232:235],a[80:83]\n"
"s_mov_b32 s77,3932160\n"
"s_add_u32 m0,s78,114688\n"
"v_mfma_f32_16x16x32_bf16 a[88:91],v[200:203],v[236:239],a[88:91]\n"
"buffer_load_dwordx4 v110,s[20:23],s77 offen offset:0 lds\n"
"v_mfma_f32_16x16x32_bf16 a[88:91],v[136:139],v[172:175],a[88:91]\n"
"v_mfma_f32_16x16x32_bf16 a[96:99],v[136:139],v[176:179],a[96:99]\n"
"v_mfma_f32_16x16x32_bf16 a[96:99],v[200:203],v[240:243],a[96:99]\n"
"v_mfma_f32_16x16x32_bf16 a[104:107],v[200:203],v[244:247],a[104:107]\n"
"buffer_load_dwordx4 v111,s[20:23],s77 offen offset:1024 lds\n"
"v_mfma_f32_16x16x32_bf16 a[104:107],v[136:139],v[180:183],a[104:107]\n"
"v_mfma_f32_16x16x32_bf16 a[112:115],v[136:139],v[184:187],a[112:115]\n"
"v_mfma_f32_16x16x32_bf16 a[112:115],v[200:203],v[248:251],a[112:115]\n"
"s_mov_b32 s76,s55\n"
"s_mov_b32 s84,0x40000\n"
"v_mfma_f32_16x16x32_bf16 a[120:123],v[200:203],v[252:255],a[120:123]\n"
"buffer_load_dwordx4 v113,s[20:23],s77 offen offset:2048 lds\n"
"v_mfma_f32_16x16x32_bf16 a[120:123],v[136:139],v[188:191],a[120:123]\n"
"s_waitcnt vmcnt(7)\n"
".Lfk_xn1_chk:\n"
"v_xor_b32 v116,s80,v74\n"
"v_xor_b32 v117,s81,v75\n"
"v_or_b32 v116,v116,v117\n"
"v_xor_b32 v117,s80,v76\n"
"v_or_b32 v116,v116,v117\n"
"v_xor_b32 v117,s81,v77\n"
"v_or_b32 v116,v116,v117\n"
"v_cmp_ne_u32 vcc,0,v116\n"
"s_nop 0\n"
"s_cbranch_vccz .Lfk_xn1_ok\n"
"s_sub_u32 s84,s84,1\n"
"s_cbranch_scc1 .Lfk_xn1_ok\n"
"s_sleep 4\n"
"buffer_load_dwordx4 v[74:77],v112,s[24:27],s58 offen sc1\n"
"s_waitcnt vmcnt(0)\n"
"s_branch .Lfk_xn1_chk\n"
".Lfk_xn1_ok:\n"
"v_mfma_f32_16x16x32_bf16 a[68:71],v[140:143],v[160:163],a[68:71]\n"
"v_mfma_f32_16x16x32_bf16 a[68:71],v[204:207],v[224:227],a[68:71]\n"
"v_mfma_f32_16x16x32_bf16 a[76:79],v[204:207],v[228:231],a[76:79]\n"
"buffer_load_dwordx4 v114,s[20:23],s77 offen offset:3072 lds\n"
"v_mfma_f32_16x16x32_bf16 a[76:79],v[140:143],v[164:167],a[76:79]\n"
"v_mfma_f32_16x16x32_bf16 a[84:87],v[140:143],v[168:171],a[84:87]\n"
"s_waitcnt lgkmcnt(0)\n"
"s_lshl2_add_u32 s88,s100,s52\n"
"v_mfma_f32_16x16x32_bf16 a[84:87],v[204:207],v[232:235],a[84:87]\n"
"s_mul_i32 s88,s88,7168\n"
"s_add_u32 s88,s88,0\n"
"v_mfma_f32_16x16x32_bf16 a[92:95],v[204:207],v[236:239],a[92:95]\n"
"s_add_u32 s48,s48,s88\n"
"s_addc_u32 s49,s49,0\n"
"s_add_u32 m0,s78,65536\n"
"v_mfma_f32_16x16x32_bf16 a[92:95],v[140:143],v[172:175],a[92:95]\n"
"buffer_load_dwordx4 v112,s[24:27],s76 offen offset:0 sc1 lds\n"
"v_mfma_f32_16x16x32_bf16 a[100:103],v[140:143],v[176:179],a[100:103]\n"
"s_mov_b32 s50,3584\n"
"s_mov_b32 s51,0x20000\n"
"v_mfma_f32_16x16x32_bf16 a[100:103],v[204:207],v[240:243],a[100:103]\n"
"v_lshlrev_b32 v8,3,v112\n"
"v_mfma_f32_16x16x32_bf16 a[108:111],v[204:207],v[244:247],a[108:111]\n"
"v_mfma_f32_16x16x32_bf16 a[108:111],v[140:143],v[180:183],a[108:111]\n"
"v_mfma_f32_16x16x32_bf16 a[116:119],v[140:143],v[184:187],a[116:119]\n"
"buffer_load_dwordx4 v112,s[24:27],s76 offen offset:1024 sc1 lds\n"
"v_mfma_f32_16x16x32_bf16 a[116:119],v[204:207],v[248:251],a[116:119]\n"
"v_mfma_f32_16x16x32_bf16 a[124:127],v[204:207],v[252:255],a[124:127]\n"
"v_mfma_f32_16x16x32_bf16 a[124:127],v[140:143],v[188:191],a[124:127]\n"
"v_mfma_f32_16x16x32_bf16 a[128:131],v[144:147],v[160:163],a[128:131]\n"
"v_mfma_f32_16x16x32_bf16 a[128:131],v[208:211],v[224:227],a[128:131]\n"
"buffer_load_dwordx4 v112,s[24:27],s76 offen offset:2048 sc1 lds\n"
"v_mfma_f32_16x16x32_bf16 a[136:139],v[208:211],v[228:231],a[136:139]\n"
"v_mfma_f32_16x16x32_bf16 a[136:139],v[144:147],v[164:167],a[136:139]\n"
"v_mfma_f32_16x16x32_bf16 a[144:147],v[144:147],v[168:171],a[144:147]\n"
"v_mfma_f32_16x16x32_bf16 a[144:147],v[208:211],v[232:235],a[144:147]\n"
"buffer_load_dwordx4 v112,s[24:27],s76 offen offset:3072 sc1 lds\n"
"v_mfma_f32_16x16x32_bf16 a[152:155],v[208:211],v[236:239],a[152:155]\n"
"s_xor_b32 s76,s76,16384\n"
"v_mfma_f32_16x16x32_bf16 a[152:155],v[144:147],v[172:175],a[152:155]\n"
"v_mfma_f32_16x16x32_bf16 a[160:163],v[144:147],v[176:179],a[160:163]\n"
"v_mfma_f32_16x16x32_bf16 a[160:163],v[208:211],v[240:243],a[160:163]\n"
"v_mfma_f32_16x16x32_bf16 a[168:171],v[208:211],v[244:247],a[168:171]\n"
"s_add_u32 m0,s78,81920\n"
"v_mfma_f32_16x16x32_bf16 a[168:171],v[144:147],v[180:183],a[168:171]\n"
"buffer_load_dwordx4 v112,s[24:27],s76 offen offset:0 sc1 lds\n"
"v_mfma_f32_16x16x32_bf16 a[176:179],v[144:147],v[184:187],a[176:179]\n"
"v_mfma_f32_16x16x32_bf16 a[176:179],v[208:211],v[248:251],a[176:179]\n"
"v_mfma_f32_16x16x32_bf16 a[184:187],v[208:211],v[252:255],a[184:187]\n"
"v_mfma_f32_16x16x32_bf16 a[184:187],v[144:147],v[188:191],a[184:187]\n"
"v_mfma_f32_16x16x32_bf16 a[132:135],v[148:151],v[160:163],a[132:135]\n"
"buffer_load_dwordx4 v112,s[24:27],s76 offen offset:1024 sc1 lds\n"
"v_mfma_f32_16x16x32_bf16 a[132:135],v[212:215],v[224:227],a[132:135]\n"
"v_mfma_f32_16x16x32_bf16 a[140:143],v[212:215],v[228:231],a[140:143]\n"
"v_mfma_f32_16x16x32_bf16 a[140:143],v[148:151],v[164:167],a[140:143]\n"
"v_mfma_f32_16x16x32_bf16 a[148:151],v[148:151],v[168:171],a[148:151]\n"
"v_mfma_f32_16x16x32_bf16 a[148:151],v[212:215],v[232:235],a[148:151]\n"
"buffer_load_dwordx4 v112,s[24:27],s76 offen offset:2048 sc1 lds\n"
"v_mfma_f32_16x16x32_bf16 a[156:159],v[212:215],v[236:239],a[156:159]\n"
"v_mfma_f32_16x16x32_bf16 a[156:159],v[148:151],v[172:175],a[156:159]\n"
"v_mfma_f32_16x16x32_bf16 a[164:167],v[148:151],v[176:179],a[164:167]\n"
"v_mfma_f32_16x16x32_bf16 a[164:167],v[212:215],v[240:243],a[164:167]\n"
"buffer_load_dwordx4 v112,s[24:27],s76 offen offset:3072 sc1 lds\n"
"v_mfma_f32_16x16x32_bf16 a[172:175],v[212:215],v[244:247],a[172:175]\n"
"s_xor_b32 s76,s76,49152\n"
"v_mfma_f32_16x16x32_bf16 a[172:175],v[148:151],v[180:183],a[172:175]\n"
"v_mfma_f32_16x16x32_bf16 a[180:183],v[148:151],v[184:187],a[180:183]\n"
"buffer_load_dword v115,v8,s[48:51],0 offen\n"
"v_mfma_f32_16x16x32_bf16 a[180:183],v[212:215],v[248:251],a[180:183]\n"
"v_mfma_f32_16x16x32_bf16 a[188:191],v[212:215],v[252:255],a[188:191]\n"
"v_mfma_f32_16x16x32_bf16 a[188:191],v[148:151],v[188:191],a[188:191]\n"
"s_barrier\n"
"v_mfma_f32_16x16x32_bf16 a[192:195],v[216:219],v[224:227],a[192:195]\n"
"ds_read_b128 v[152:155],v80 offset:12288\n"
"v_mfma_f32_16x16x32_bf16 a[196:199],v[220:223],v[224:227],a[196:199]\n"
"ds_read_b128 v[156:159],v80 offset:14336\n"
"v_mfma_f32_16x16x32_bf16 a[204:207],v[220:223],v[228:231],a[204:207]\n"
"ds_read_b64_tr_b16 v[160:161],v98 offset:49152\n"
"v_mfma_f32_16x16x32_bf16 a[200:203],v[216:219],v[228:231],a[200:203]\n"
"ds_read_b64_tr_b16 v[162:163],v98 offset:50176\n"
"v_mfma_f32_16x16x32_bf16 a[208:211],v[216:219],v[232:235],a[208:211]\n"
"ds_read_b64_tr_b16 v[164:165],v99 offset:49152\n"
"v_mfma_f32_16x16x32_bf16 a[212:215],v[220:223],v[232:235],a[212:215]\n"
"ds_read_b64_tr_b16 v[166:167],v99 offset:50176\n"
"v_mfma_f32_16x16x32_bf16 a[220:223],v[220:223],v[236:239],a[220:223]\n"
"ds_read_b64_tr_b16 v[168:169],v100 offset:49152\n"
"v_mfma_f32_16x16x32_bf16 a[216:219],v[216:219],v[236:239],a[216:219]\n"
"ds_read_b64_tr_b16 v[170:171],v100 offset:50176\n"
"v_mfma_f32_16x16x32_bf16 a[224:227],v[216:219],v[240:243],a[224:227]\n"
"ds_read_b64_tr_b16 v[172:173],v101 offset:49152\n"
"v_mfma_f32_16x16x32_bf16 a[228:231],v[220:223],v[240:243],a[228:231]\n"
"ds_read_b64_tr_b16 v[174:175],v101 offset:50176\n"
"v_mfma_f32_16x16x32_bf16 a[236:239],v[220:223],v[244:247],a[236:239]\n"
"ds_read_b64_tr_b16 v[176:177],v102 offset:49152\n"
"v_mfma_f32_16x16x32_bf16 a[232:235],v[216:219],v[244:247],a[232:235]\n"
"ds_read_b64_tr_b16 v[178:179],v102 offset:50176\n"
"v_mfma_f32_16x16x32_bf16 a[240:243],v[216:219],v[248:251],a[240:243]\n"
"ds_read_b64_tr_b16 v[180:181],v103 offset:49152\n"
"v_mfma_f32_16x16x32_bf16 a[244:247],v[220:223],v[248:251],a[244:247]\n"
"ds_read_b64_tr_b16 v[182:183],v103 offset:50176\n"
"ds_read_b64_tr_b16 v[184:185],v104 offset:49152\n"
"v_mfma_f32_16x16x32_bf16 a[252:255],v[220:223],v[252:255],a[252:255]\n"
"v_mov_b32 v110,v72\n"
"v_mov_b32 v111,v73\n"
"v_mfma_f32_16x16x32_bf16 a[248:251],v[216:219],v[252:255],a[248:251]\n"
"ds_read_b64_tr_b16 v[186:187],v104 offset:50176\n"
"s_waitcnt lgkmcnt(10)\n"
"v_mfma_f32_16x16x32_bf16 a[192:195],v[152:155],v[160:163],a[192:195]\n"
"ds_read_b64_tr_b16 v[188:189],v105 offset:49152\n"
"v_mfma_f32_16x16x32_bf16 a[196:199],v[156:159],v[160:163],a[196:199]\n"
"ds_read_b64_tr_b16 v[190:191],v105 offset:50176\n"
"v_mfma_f32_16x16x32_bf16 a[204:207],v[156:159],v[164:167],a[204:207]\n"
"ds_read_b128 v[128:131],v80 offset:0\n"
"v_mfma_f32_16x16x32_bf16 a[200:203],v[152:155],v[164:167],a[200:203]\n"
"ds_read_b128 v[192:195],v81 offset:0\n"
"s_waitcnt lgkmcnt(10)\n"
"v_mfma_f32_16x16x32_bf16 a[208:211],v[152:155],v[168:171],a[208:211]\n"
"ds_read_b64_tr_b16 v[224:225],v98 offset:32768\n"
"v_mfma_f32_16x16x32_bf16 a[212:215],v[156:159],v[168:171],a[212:215]\n"
"ds_read_b64_tr_b16 v[226:227],v98 offset:33792\n"
"v_mfma_f32_16x16x32_bf16 a[220:223],v[156:159],v[172:175],a[220:223]\n"
"ds_read_b64_tr_b16 v[228:229],v99 offset:32768\n"
"v_mfma_f32_16x16x32_bf16 a[216:219],v[152:155],v[172:175],a[216:219]\n"
"ds_read_b64_tr_b16 v[230:231],v99 offset:33792\n"
"s_waitcnt lgkmcnt(10)\n"
"v_mfma_f32_16x16x32_bf16 a[224:227],v[152:155],v[176:179],a[224:227]\n"
"ds_read_b64_tr_b16 v[232:233],v100 offset:32768\n"
"v_mfma_f32_16x16x32_bf16 a[228:231],v[156:159],v[176:179],a[228:231]\n"
"ds_read_b64_tr_b16 v[234:235],v100 offset:33792\n"
"v_mfma_f32_16x16x32_bf16 a[236:239],v[156:159],v[180:183],a[236:239]\n"
"ds_read_b64_tr_b16 v[236:237],v101 offset:32768\n"
"v_mfma_f32_16x16x32_bf16 a[232:235],v[152:155],v[180:183],a[232:235]\n"
"ds_read_b64_tr_b16 v[238:239],v101 offset:33792\n"
"s_waitcnt lgkmcnt(10)\n"
"v_mfma_f32_16x16x32_bf16 a[240:243],v[152:155],v[184:187],a[240:243]\n"
"ds_read_b64_tr_b16 v[240:241],v102 offset:32768\n"
"v_mfma_f32_16x16x32_bf16 a[244:247],v[156:159],v[184:187],a[244:247]\n"
"ds_read_b64_tr_b16 v[242:243],v102 offset:33792\n"
"v_mfma_f32_16x16x32_bf16 a[252:255],v[156:159],v[188:191],a[252:255]\n"
"ds_read_b64_tr_b16 v[244:245],v103 offset:32768\n"
"v_mfma_f32_16x16x32_bf16 a[248:251],v[152:155],v[188:191],a[248:251]\n"
"ds_read_b64_tr_b16 v[246:247],v103 offset:33792\n"
"s_waitcnt lgkmcnt(8)\n"
"v_mfma_f32_16x16x32_bf16 a[0:3],v[128:131],v[160:163],a[0:3]\n"
"ds_read_b64_tr_b16 v[248:249],v104 offset:32768\n"
"v_mfma_f32_16x16x32_bf16 a[0:3],v[192:195],v[224:227],a[0:3]\n"
"ds_read_b64_tr_b16 v[250:251],v104 offset:33792\n"
"v_mfma_f32_16x16x32_bf16 a[8:11],v[192:195],v[228:231],a[8:11]\n"
"ds_read_b64_tr_b16 v[252:253],v105 offset:32768\n"
"v_mfma_f32_16x16x32_bf16 a[8:11],v[128:131],v[164:167],a[8:11]\n"
"ds_read_b64_tr_b16 v[254:255],v105 offset:33792\n"
"s_waitcnt lgkmcnt(8)\n"
"v_mfma_f32_16x16x32_bf16 a[16:19],v[128:131],v[168:171],a[16:19]\n"
"ds_read_b128 v[132:135],v80 offset:2048\n"
"v_mfma_f32_16x16x32_bf16 a[16:19],v[192:195],v[232:235],a[16:19]\n"
"ds_read_b128 v[196:199],v81 offset:2048\n"
"v_mfma_f32_16x16x32_bf16 a[24:27],v[192:195],v[236:239],a[24:27]\n"
"ds_read_b128 v[136:139],v80 offset:4096\n"
"v_mfma_f32_16x16x32_bf16 a[24:27],v[128:131],v[172:175],a[24:27]\n"
"ds_read_b128 v[200:203],v81 offset:4096\n"
"s_waitcnt lgkmcnt(8)\n"
"v_mfma_f32_16x16x32_bf16 a[32:35],v[128:131],v[176:179],a[32:35]\n"
"ds_read_b128 v[140:143],v80 offset:6144\n"
"v_mfma_f32_16x16x32_bf16 a[32:35],v[192:195],v[240:243],a[32:35]\n"
"ds_read_b128 v[204:207],v81 offset:6144\n"
"v_mfma_f32_16x16x32_bf16 a[40:43],v[192:195],v[244:247],a[40:43]\n"
"ds_read_b128 v[144:147],v80 offset:8192\n"
"v_mfma_f32_16x16x32_bf16 a[40:43],v[128:131],v[180:183],a[40:43]\n"
"ds_read_b128 v[208:211],v81 offset:8192\n"
"s_waitcnt lgkmcnt(8)\n"
"v_mfma_f32_16x16x32_bf16 a[48:51],v[128:131],v[184:187],a[48:51]\n"
"ds_read_b128 v[148:151],v80 offset:10240\n"
"v_mfma_f32_16x16x32_bf16 a[48:51],v[192:195],v[248:251],a[48:51]\n"
"ds_read_b128 v[212:215],v81 offset:10240\n"
"v_mfma_f32_16x16x32_bf16 a[56:59],v[192:195],v[252:255],a[56:59]\n"
"ds_read_b128 v[216:219],v81 offset:12288\n"
"v_mfma_f32_16x16x32_bf16 a[56:59],v[128:131],v[188:191],a[56:59]\n"
"ds_read_b128 v[220:223],v81 offset:14336\n"
"s_waitcnt lgkmcnt(10)\n"
"v_mfma_f32_16x16x32_bf16 a[4:7],v[132:135],v[160:163],a[4:7]\n"
"v_mfma_f32_16x16x32_bf16 a[4:7],v[196:199],v[224:227],a[4:7]\n"
"v_mfma_f32_16x16x32_bf16 a[12:15],v[196:199],v[228:231],a[12:15]\n"
"v_mfma_f32_16x16x32_bf16 a[12:15],v[132:135],v[164:167],a[12:15]\n"
"v_mfma_f32_16x16x32_bf16 a[20:23],v[132:135],v[168:171],a[20:23]\n"
"s_waitcnt lgkmcnt(0)\n"
"s_barrier\n"
"v_mfma_f32_16x16x32_bf16 a[20:23],v[196:199],v[232:235],a[20:23]\n"
"s_add_u32 m0,s85,147456\n"
"v_mfma_f32_16x16x32_bf16 a[28:31],v[196:199],v[236:239],a[28:31]\n"
"buffer_load_dwordx4 v110,s[28:31],s86 offen lds\n"
"v_mfma_f32_16x16x32_bf16 a[28:31],v[132:135],v[172:175],a[28:31]\n"
"v_mfma_f32_16x16x32_bf16 a[36:39],v[132:135],v[176:179],a[36:39]\n"
"s_load_dwordx2 s[48:49],s[0:1],56\n"
"v_mfma_f32_16x16x32_bf16 a[36:39],v[196:199],v[240:243],a[36:39]\n"
"s_add_u32 m0,s85,148480\n"
"v_mfma_f32_16x16x32_bf16 a[44:47],v[196:199],v[244:247],a[44:47]\n"
"buffer_load_dwordx4 v111,s[28:31],s86 offen lds\n"
"v_mfma_f32_16x16x32_bf16 a[44:47],v[132:135],v[180:183],a[44:47]\n"
"s_xor_b32 s86,s86,262144\n"
"v_mfma_f32_16x16x32_bf16 a[52:55],v[132:135],v[184:187],a[52:55]\n"
"v_mfma_f32_16x16x32_bf16 a[52:55],v[196:199],v[248:251],a[52:55]\n"
"s_add_u32 m0,s85,155648\n"
"v_mfma_f32_16x16x32_bf16 a[60:63],v[196:199],v[252:255],a[60:63]\n"
"buffer_load_dwordx4 v110,s[28:31],s86 offen lds\n"
"v_mfma_f32_16x16x32_bf16 a[60:63],v[132:135],v[188:191],a[60:63]\n"
"v_mfma_f32_16x16x32_bf16 a[64:67],v[136:139],v[160:163],a[64:67]\n"
"v_mfma_f32_16x16x32_bf16 a[64:67],v[200:203],v[224:227],a[64:67]\n"
"s_add_u32 m0,s85,156672\n"
"v_mfma_f32_16x16x32_bf16 a[72:75],v[200:203],v[228:231],a[72:75]\n"
"buffer_load_dwordx4 v111,s[28:31],s86 offen lds\n"
"v_mfma_f32_16x16x32_bf16 a[72:75],v[136:139],v[164:167],a[72:75]\n"
"s_xor_b32 s86,s86,786432\n"
"v_mfma_f32_16x16x32_bf16 a[80:83],v[136:139],v[168:171],a[80:83]\n"
"v_mfma_f32_16x16x32_bf16 a[80:83],v[200:203],v[232:235],a[80:83]\n"
"s_add_u32 m0,s85,131072\n"
"v_mfma_f32_16x16x32_bf16 a[88:91],v[200:203],v[236:239],a[88:91]\n"
"buffer_load_dwordx4 v110,s[28:31],s86 offen lds\n"
"v_mfma_f32_16x16x32_bf16 a[88:91],v[136:139],v[172:175],a[88:91]\n"
"v_mfma_f32_16x16x32_bf16 a[96:99],v[136:139],v[176:179],a[96:99]\n"
"v_mfma_f32_16x16x32_bf16 a[96:99],v[200:203],v[240:243],a[96:99]\n"
"s_add_u32 m0,s85,132096\n"
"v_mfma_f32_16x16x32_bf16 a[104:107],v[200:203],v[244:247],a[104:107]\n"
"buffer_load_dwordx4 v111,s[28:31],s86 offen lds\n"
"v_mfma_f32_16x16x32_bf16 a[104:107],v[136:139],v[180:183],a[104:107]\n"
"s_xor_b32 s86,s86,262144\n"
"v_mfma_f32_16x16x32_bf16 a[112:115],v[136:139],v[184:187],a[112:115]\n"
"v_mfma_f32_16x16x32_bf16 a[112:115],v[200:203],v[248:251],a[112:115]\n"
"v_mfma_f32_16x16x32_bf16 a[120:123],v[200:203],v[252:255],a[120:123]\n"
"v_mfma_f32_16x16x32_bf16 a[120:123],v[136:139],v[188:191],a[120:123]\n"
"v_mfma_f32_16x16x32_bf16 a[68:71],v[140:143],v[160:163],a[68:71]\n"
"v_mfma_f32_16x16x32_bf16 a[68:71],v[204:207],v[224:227],a[68:71]\n"
"s_add_u32 m0,s85,139264\n"
"v_mfma_f32_16x16x32_bf16 a[76:79],v[204:207],v[228:231],a[76:79]\n"
"buffer_load_dwordx4 v110,s[28:31],s86 offen lds\n"
"v_mfma_f32_16x16x32_bf16 a[76:79],v[140:143],v[164:167],a[76:79]\n"
"v_mfma_f32_16x16x32_bf16 a[84:87],v[140:143],v[168:171],a[84:87]\n"
"s_waitcnt lgkmcnt(0)\n"
"s_lshl2_add_u32 s88,s100,s52\n"
"v_mfma_f32_16x16x32_bf16 a[84:87],v[204:207],v[232:235],a[84:87]\n"
"s_mul_i32 s88,s88,7168\n"
"s_add_u32 m0,s85,140288\n"
"v_mfma_f32_16x16x32_bf16 a[92:95],v[204:207],v[236:239],a[92:95]\n"
"buffer_load_dwordx4 v111,s[28:31],s86 offen lds\n"
"s_xor_b32 s86,s86,1835008\n"
"v_mfma_f32_16x16x32_bf16 a[92:95],v[140:143],v[172:175],a[92:95]\n"
"s_add_u32 s88,s88,3584\n"
"s_add_u32 s48,s48,s88\n"
"s_addc_u32 s49,s49,0\n"
"v_mfma_f32_16x16x32_bf16 a[100:103],v[140:143],v[176:179],a[100:103]\n"
"s_mov_b32 s50,3584\n"
"s_mov_b32 s51,0x20000\n"
"v_mfma_f32_16x16x32_bf16 a[100:103],v[204:207],v[240:243],a[100:103]\n"
"v_lshlrev_b32 v8,3,v112\n"
"s_add_u32 m0,s78,0\n"
"v_mfma_f32_16x16x32_bf16 a[108:111],v[204:207],v[244:247],a[108:111]\n"
"buffer_load_dwordx4 v112,s[24:27],s76 offen offset:0 sc1 lds\n"
"v_mfma_f32_16x16x32_bf16 a[108:111],v[140:143],v[180:183],a[108:111]\n"
"v_mfma_f32_16x16x32_bf16 a[116:119],v[140:143],v[184:187],a[116:119]\n"
"v_mfma_f32_16x16x32_bf16 a[116:119],v[204:207],v[248:251],a[116:119]\n"
"v_mfma_f32_16x16x32_bf16 a[124:127],v[204:207],v[252:255],a[124:127]\n"
"buffer_load_dwordx4 v112,s[24:27],s76 offen offset:1024 sc1 lds\n"
"v_mfma_f32_16x16x32_bf16 a[124:127],v[140:143],v[188:191],a[124:127]\n"
"v_mfma_f32_16x16x32_bf16 a[128:131],v[144:147],v[160:163],a[128:131]\n"
"v_mfma_f32_16x16x32_bf16 a[128:131],v[208:211],v[224:227],a[128:131]\n"
"v_mfma_f32_16x16x32_bf16 a[136:139],v[208:211],v[228:231],a[136:139]\n"
"buffer_load_dwordx4 v112,s[24:27],s76 offen offset:2048 sc1 lds\n"
"v_mfma_f32_16x16x32_bf16 a[136:139],v[144:147],v[164:167],a[136:139]\n"
"v_mfma_f32_16x16x32_bf16 a[144:147],v[144:147],v[168:171],a[144:147]\n"
"v_mfma_f32_16x16x32_bf16 a[144:147],v[208:211],v[232:235],a[144:147]\n"
"buffer_load_dwordx4 v112,s[24:27],s76 offen offset:3072 sc1 lds\n"
"v_mfma_f32_16x16x32_bf16 a[152:155],v[208:211],v[236:239],a[152:155]\n"
"s_xor_b32 s76,s76,16384\n"
"v_mfma_f32_16x16x32_bf16 a[152:155],v[144:147],v[172:175],a[152:155]\n"
"v_mfma_f32_16x16x32_bf16 a[160:163],v[144:147],v[176:179],a[160:163]\n"
"v_mfma_f32_16x16x32_bf16 a[160:163],v[208:211],v[240:243],a[160:163]\n"
"s_add_u32 m0,s78,16384\n"
"v_mfma_f32_16x16x32_bf16 a[168:171],v[208:211],v[244:247],a[168:171]\n"
"buffer_load_dwordx4 v112,s[24:27],s76 offen offset:0 sc1 lds\n"
"v_mfma_f32_16x16x32_bf16 a[168:171],v[144:147],v[180:183],a[168:171]\n"
"v_mfma_f32_16x16x32_bf16 a[176:179],v[144:147],v[184:187],a[176:179]\n"
"v_mfma_f32_16x16x32_bf16 a[176:179],v[208:211],v[248:251],a[176:179]\n"
"v_mfma_f32_16x16x32_bf16 a[184:187],v[208:211],v[252:255],a[184:187]\n"
"buffer_load_dwordx4 v112,s[24:27],s76 offen offset:1024 sc1 lds\n"
"v_mfma_f32_16x16x32_bf16 a[184:187],v[144:147],v[188:191],a[184:187]\n"
"v_mfma_f32_16x16x32_bf16 a[132:135],v[148:151],v[160:163],a[132:135]\n"
"v_mfma_f32_16x16x32_bf16 a[132:135],v[212:215],v[224:227],a[132:135]\n"
"v_mfma_f32_16x16x32_bf16 a[140:143],v[212:215],v[228:231],a[140:143]\n"
"buffer_load_dwordx4 v112,s[24:27],s76 offen offset:2048 sc1 lds\n"
"v_mfma_f32_16x16x32_bf16 a[140:143],v[148:151],v[164:167],a[140:143]\n"
"v_mfma_f32_16x16x32_bf16 a[148:151],v[148:151],v[168:171],a[148:151]\n"
"v_mfma_f32_16x16x32_bf16 a[148:151],v[212:215],v[232:235],a[148:151]\n"
"buffer_load_dwordx4 v112,s[24:27],s76 offen offset:3072 sc1 lds\n"
"v_mfma_f32_16x16x32_bf16 a[156:159],v[212:215],v[236:239],a[156:159]\n"
"s_xor_b32 s76,s76,114688\n"
"v_mfma_f32_16x16x32_bf16 a[156:159],v[148:151],v[172:175],a[156:159]\n"
"v_mfma_f32_16x16x32_bf16 a[164:167],v[148:151],v[176:179],a[164:167]\n"
"buffer_load_dword v115,v8,s[48:51],0 offen\n"
"v_mfma_f32_16x16x32_bf16 a[164:167],v[212:215],v[240:243],a[164:167]\n"
"v_mfma_f32_16x16x32_bf16 a[172:175],v[212:215],v[244:247],a[172:175]\n"
"v_mfma_f32_16x16x32_bf16 a[172:175],v[148:151],v[180:183],a[172:175]\n"
"v_mfma_f32_16x16x32_bf16 a[180:183],v[148:151],v[184:187],a[180:183]\n"
"v_mfma_f32_16x16x32_bf16 a[180:183],v[212:215],v[248:251],a[180:183]\n"
"v_mfma_f32_16x16x32_bf16 a[188:191],v[212:215],v[252:255],a[188:191]\n"
"v_mfma_f32_16x16x32_bf16 a[188:191],v[148:151],v[188:191],a[188:191]\n"
"s_waitcnt vmcnt(26)\n"
"s_barrier\n"
"v_mfma_f32_16x16x32_bf16 a[192:195],v[216:219],v[224:227],a[192:195]\n"
"ds_read_b128 v[152:155],v80 offset:45056\n"
"v_mfma_f32_16x16x32_bf16 a[196:199],v[220:223],v[224:227],a[196:199]\n"
"ds_read_b128 v[156:159],v80 offset:47104\n"
"v_mfma_f32_16x16x32_bf16 a[204:207],v[220:223],v[228:231],a[204:207]\n"
"ds_read_b64_tr_b16 v[160:161],v98 offset:0\n"
"v_mfma_f32_16x16x32_bf16 a[200:203],v[216:219],v[228:231],a[200:203]\n"
"ds_read_b64_tr_b16 v[162:163],v98 offset:1024\n"
"v_mfma_f32_16x16x32_bf16 a[208:211],v[216:219],v[232:235],a[208:211]\n"
"ds_read_b64_tr_b16 v[164:165],v99 offset:0\n"
"v_mfma_f32_16x16x32_bf16 a[212:215],v[220:223],v[232:235],a[212:215]\n"
"ds_read_b64_tr_b16 v[166:167],v99 offset:1024\n"
"v_mfma_f32_16x16x32_bf16 a[220:223],v[220:223],v[236:239],a[220:223]\n"
"ds_read_b64_tr_b16 v[168:169],v100 offset:0\n"
"v_mfma_f32_16x16x32_bf16 a[216:219],v[216:219],v[236:239],a[216:219]\n"
"ds_read_b64_tr_b16 v[170:171],v100 offset:1024\n"
"v_mfma_f32_16x16x32_bf16 a[224:227],v[216:219],v[240:243],a[224:227]\n"
"ds_read_b64_tr_b16 v[172:173],v101 offset:0\n"
"v_mfma_f32_16x16x32_bf16 a[228:231],v[220:223],v[240:243],a[228:231]\n"
"ds_read_b64_tr_b16 v[174:175],v101 offset:1024\n"
"v_mfma_f32_16x16x32_bf16 a[236:239],v[220:223],v[244:247],a[236:239]\n"
"ds_read_b64_tr_b16 v[176:177],v102 offset:0\n"
"v_mfma_f32_16x16x32_bf16 a[232:235],v[216:219],v[244:247],a[232:235]\n"
"ds_read_b64_tr_b16 v[178:179],v102 offset:1024\n"
"v_mfma_f32_16x16x32_bf16 a[240:243],v[216:219],v[248:251],a[240:243]\n"
"ds_read_b64_tr_b16 v[180:181],v103 offset:0\n"
"v_mfma_f32_16x16x32_bf16 a[244:247],v[220:223],v[248:251],a[244:247]\n"
"ds_read_b64_tr_b16 v[182:183],v103 offset:1024\n"
"v_mfma_f32_16x16x32_bf16 a[252:255],v[220:223],v[252:255],a[252:255]\n"
"ds_read_b64_tr_b16 v[184:185],v104 offset:0\n"
"v_mfma_f32_16x16x32_bf16 a[248:251],v[216:219],v[252:255],a[248:251]\n"
"ds_read_b64_tr_b16 v[186:187],v104 offset:1024\n"
"s_waitcnt lgkmcnt(10)\n"
"v_mfma_f32_16x16x32_bf16 a[192:195],v[152:155],v[160:163],a[192:195]\n"
"ds_read_b64_tr_b16 v[188:189],v105 offset:0\n"
"v_mfma_f32_16x16x32_bf16 a[196:199],v[156:159],v[160:163],a[196:199]\n"
"ds_read_b64_tr_b16 v[190:191],v105 offset:1024\n"
"v_mfma_f32_16x16x32_bf16 a[204:207],v[156:159],v[164:167],a[204:207]\n"
"ds_read_b128 v[128:131],v80 offset:32768\n"
"v_mfma_f32_16x16x32_bf16 a[200:203],v[152:155],v[164:167],a[200:203]\n"
"ds_read_b128 v[192:195],v81 offset:32768\n"
"s_waitcnt lgkmcnt(10)\n"
"v_mfma_f32_16x16x32_bf16 a[208:211],v[152:155],v[168:171],a[208:211]\n"
"ds_read_b64_tr_b16 v[224:225],v98 offset:16384\n"
"v_mfma_f32_16x16x32_bf16 a[212:215],v[156:159],v[168:171],a[212:215]\n"
"ds_read_b64_tr_b16 v[226:227],v98 offset:17408\n"
"v_mfma_f32_16x16x32_bf16 a[220:223],v[156:159],v[172:175],a[220:223]\n"
"ds_read_b64_tr_b16 v[228:229],v99 offset:16384\n"
"v_mfma_f32_16x16x32_bf16 a[216:219],v[152:155],v[172:175],a[216:219]\n"
"ds_read_b64_tr_b16 v[230:231],v99 offset:17408\n"
"s_waitcnt lgkmcnt(10)\n"
"v_mfma_f32_16x16x32_bf16 a[224:227],v[152:155],v[176:179],a[224:227]\n"
"ds_read_b64_tr_b16 v[232:233],v100 offset:16384\n"
"v_mfma_f32_16x16x32_bf16 a[228:231],v[156:159],v[176:179],a[228:231]\n"
"ds_read_b64_tr_b16 v[234:235],v100 offset:17408\n"
"v_mfma_f32_16x16x32_bf16 a[236:239],v[156:159],v[180:183],a[236:239]\n"
"ds_read_b64_tr_b16 v[236:237],v101 offset:16384\n"
"v_mfma_f32_16x16x32_bf16 a[232:235],v[152:155],v[180:183],a[232:235]\n"
"ds_read_b64_tr_b16 v[238:239],v101 offset:17408\n"
"s_waitcnt lgkmcnt(10)\n"
"v_mfma_f32_16x16x32_bf16 a[240:243],v[152:155],v[184:187],a[240:243]\n"
"ds_read_b64_tr_b16 v[240:241],v102 offset:16384\n"
"v_mfma_f32_16x16x32_bf16 a[244:247],v[156:159],v[184:187],a[244:247]\n"
"ds_read_b64_tr_b16 v[242:243],v102 offset:17408\n"
"v_mfma_f32_16x16x32_bf16 a[252:255],v[156:159],v[188:191],a[252:255]\n"
"ds_read_b64_tr_b16 v[244:245],v103 offset:16384\n"
"v_mfma_f32_16x16x32_bf16 a[248:251],v[152:155],v[188:191],a[248:251]\n"
"ds_read_b64_tr_b16 v[246:247],v103 offset:17408\n"
"s_waitcnt lgkmcnt(8)\n"
"v_mfma_f32_16x16x32_bf16 a[0:3],v[128:131],v[160:163],a[0:3]\n"
"ds_read_b64_tr_b16 v[248:249],v104 offset:16384\n"
"v_mfma_f32_16x16x32_bf16 a[0:3],v[192:195],v[224:227],a[0:3]\n"
"ds_read_b64_tr_b16 v[250:251],v104 offset:17408\n"
"v_mfma_f32_16x16x32_bf16 a[8:11],v[192:195],v[228:231],a[8:11]\n"
"ds_read_b64_tr_b16 v[252:253],v105 offset:16384\n"
"v_mfma_f32_16x16x32_bf16 a[8:11],v[128:131],v[164:167],a[8:11]\n"
"ds_read_b64_tr_b16 v[254:255],v105 offset:17408\n"
"s_waitcnt lgkmcnt(8)\n"
"v_mfma_f32_16x16x32_bf16 a[16:19],v[128:131],v[168:171],a[16:19]\n"
"ds_read_b128 v[132:135],v80 offset:34816\n"
"v_mfma_f32_16x16x32_bf16 a[16:19],v[192:195],v[232:235],a[16:19]\n"
"ds_read_b128 v[196:199],v81 offset:34816\n"
"v_mfma_f32_16x16x32_bf16 a[24:27],v[192:195],v[236:239],a[24:27]\n"
"ds_read_b128 v[136:139],v80 offset:36864\n"
"v_mfma_f32_16x16x32_bf16 a[24:27],v[128:131],v[172:175],a[24:27]\n"
"ds_read_b128 v[200:203],v81 offset:36864\n"
"s_waitcnt lgkmcnt(8)\n"
"v_mfma_f32_16x16x32_bf16 a[32:35],v[128:131],v[176:179],a[32:35]\n"
"s_and_b32 s82,s52,1\n"
"ds_read_b128 v[140:143],v80 offset:38912\n"
"s_lshl_b32 s82,s82,13\n"
"v_mfma_f32_16x16x32_bf16 a[32:35],v[192:195],v[240:243],a[32:35]\n"
"ds_read_b128 v[204:207],v81 offset:38912\n"
"v_mfma_f32_16x16x32_bf16 a[40:43],v[192:195],v[244:247],a[40:43]\n"
"ds_read_b128 v[144:147],v80 offset:40960\n"
"v_mfma_f32_16x16x32_bf16 a[40:43],v[128:131],v[180:183],a[40:43]\n"
"ds_read_b128 v[208:211],v81 offset:40960\n"
"s_waitcnt lgkmcnt(8)\n"
"v_mfma_f32_16x16x32_bf16 a[48:51],v[128:131],v[184:187],a[48:51]\n"
"ds_read_b128 v[148:151],v80 offset:43008\n"
"v_mfma_f32_16x16x32_bf16 a[48:51],v[192:195],v[248:251],a[48:51]\n"
"ds_read_b128 v[212:215],v81 offset:43008\n"
"v_subrev_u32 v98,s82,v98\n"
"v_mfma_f32_16x16x32_bf16 a[56:59],v[192:195],v[252:255],a[56:59]\n"
"ds_read_b128 v[216:219],v81 offset:45056\n"
"v_add_u32 v96,s78,v97\n"
"v_mfma_f32_16x16x32_bf16 a[56:59],v[128:131],v[188:191],a[56:59]\n"
"ds_read_b128 v[220:223],v81 offset:47104\n"
"s_waitcnt lgkmcnt(10)\n"
"v_mfma_f32_16x16x32_bf16 a[4:7],v[132:135],v[160:163],a[4:7]\n"
"v_add_u32 v97,65536,v96\n"
"v_mfma_f32_16x16x32_bf16 a[4:7],v[196:199],v[224:227],a[4:7]\n"
"v_subrev_u32 v99,s82,v99\n"
"v_mfma_f32_16x16x32_bf16 a[12:15],v[196:199],v[228:231],a[12:15]\n"
"v_mfma_f32_16x16x32_bf16 a[12:15],v[132:135],v[164:167],a[12:15]\n"
"v_mfma_f32_16x16x32_bf16 a[20:23],v[132:135],v[168:171],a[20:23]\n"
"s_waitcnt lgkmcnt(0)\n"
"s_barrier\n"
"v_mfma_f32_16x16x32_bf16 a[20:23],v[196:199],v[232:235],a[20:23]\n"
"v_subrev_u32 v100,s82,v100\n"
"s_add_u32 m0,s85,98304\n"
"v_mfma_f32_16x16x32_bf16 a[28:31],v[196:199],v[236:239],a[28:31]\n"
"buffer_load_dwordx4 v110,s[28:31],s86 offen lds\n"
"v_mfma_f32_16x16x32_bf16 a[28:31],v[132:135],v[172:175],a[28:31]\n"
"v_mfma_f32_16x16x32_bf16 a[36:39],v[132:135],v[176:179],a[36:39]\n"
"v_mfma_f32_16x16x32_bf16 a[36:39],v[196:199],v[240:243],a[36:39]\n"
"v_subrev_u32 v101,s82,v101\n"
"s_add_u32 m0,s85,99328\n"
"v_mfma_f32_16x16x32_bf16 a[44:47],v[196:199],v[244:247],a[44:47]\n"
"buffer_load_dwordx4 v111,s[28:31],s86 offen lds\n"
"v_mfma_f32_16x16x32_bf16 a[44:47],v[132:135],v[180:183],a[44:47]\n"
"s_xor_b32 s86,s86,262144\n"
"v_mfma_f32_16x16x32_bf16 a[52:55],v[132:135],v[184:187],a[52:55]\n"
"v_mfma_f32_16x16x32_bf16 a[52:55],v[196:199],v[248:251],a[52:55]\n"
"v_subrev_u32 v102,s82,v102\n"
"s_add_u32 m0,s78,32768\n"
"v_mfma_f32_16x16x32_bf16 a[60:63],v[196:199],v[252:255],a[60:63]\n"
"buffer_load_dwordx4 v112,s[24:27],s76 offen offset:0 sc1 lds\n"
"v_mfma_f32_16x16x32_bf16 a[60:63],v[132:135],v[188:191],a[60:63]\n"
"v_mfma_f32_16x16x32_bf16 a[64:67],v[136:139],v[160:163],a[64:67]\n"
"v_mfma_f32_16x16x32_bf16 a[64:67],v[200:203],v[224:227],a[64:67]\n"
"v_subrev_u32 v103,s82,v103\n"
"v_mfma_f32_16x16x32_bf16 a[72:75],v[200:203],v[228:231],a[72:75]\n"
"buffer_load_dwordx4 v112,s[24:27],s76 offen offset:1024 sc1 lds\n"
"v_mfma_f32_16x16x32_bf16 a[72:75],v[136:139],v[164:167],a[72:75]\n"
"v_mfma_f32_16x16x32_bf16 a[80:83],v[136:139],v[168:171],a[80:83]\n"
"v_accvgpr_read_b32 v116,a0\n"
"v_accvgpr_read_b32 v117,a1\n"
"v_mfma_f32_16x16x32_bf16 a[80:83],v[200:203],v[232:235],a[80:83]\n"
"v_subrev_u32 v104,s82,v104\n"
"v_accvgpr_read_b32 v118,a2\n"
"v_mfma_f32_16x16x32_bf16 a[88:91],v[200:203],v[236:239],a[88:91]\n"
"buffer_load_dwordx4 v112,s[24:27],s76 offen offset:2048 sc1 lds\n"
"v_mfma_f32_16x16x32_bf16 a[88:91],v[136:139],v[172:175],a[88:91]\n"
"v_accvgpr_read_b32 v119,a3\n"
"v_accvgpr_read_b32 v120,a4\n"
"v_mfma_f32_16x16x32_bf16 a[96:99],v[136:139],v[176:179],a[96:99]\n"
"v_accvgpr_read_b32 v121,a5\n"
"v_accvgpr_read_b32 v122,a6\n"
"v_mfma_f32_16x16x32_bf16 a[96:99],v[200:203],v[240:243],a[96:99]\n"
"v_subrev_u32 v105,s82,v105\n"
"v_accvgpr_read_b32 v123,a7\n"
"v_mfma_f32_16x16x32_bf16 a[104:107],v[200:203],v[244:247],a[104:107]\n"
"buffer_load_dwordx4 v112,s[24:27],s76 offen offset:3072 sc1 lds\n"
"v_mfma_f32_16x16x32_bf16 a[104:107],v[136:139],v[180:183],a[104:107]\n"
"s_xor_b32 s76,s76,16384\n"
"v_cvt_pk_bf16_f32 v116,v116,v117\n"
"v_cvt_pk_bf16_f32 v117,v118,v119\n"
"v_mfma_f32_16x16x32_bf16 a[112:115],v[136:139],v[184:187],a[112:115]\n"
"v_cvt_pk_bf16_f32 v118,v120,v121\n"
"v_cvt_pk_bf16_f32 v119,v122,v123\n"
"v_mfma_f32_16x16x32_bf16 a[112:115],v[200:203],v[248:251],a[112:115]\n"
"buffer_store_dwordx4 v[116:119],v112,s[24:27],s56 offen offset:0 sc1\n"
"v_mfma_f32_16x16x32_bf16 a[120:123],v[200:203],v[252:255],a[120:123]\n"
"s_nop 0\n"
"v_accvgpr_read_b32 v116,a8\n"
"v_accvgpr_read_b32 v117,a9\n"
"v_accvgpr_read_b32 v118,a10\n"
"v_mfma_f32_16x16x32_bf16 a[120:123],v[136:139],v[188:191],a[120:123]\n"
"s_waitcnt vmcnt(20)\n"
"v_accvgpr_read_b32 v119,a11\n"
"v_accvgpr_read_b32 v120,a12\n"
"s_barrier\n"
"v_mfma_f32_16x16x32_bf16 a[68:71],v[140:143],v[160:163],a[68:71]\n"
"ds_read_b128 v[0:3],v97 offset:0\n"
"v_accvgpr_read_b32 v121,a13\n"
"v_mfma_f32_16x16x32_bf16 a[68:71],v[204:207],v[224:227],a[68:71]\n"
"ds_read_b128 v[8:11],v97 offset:1024\n"
"v_accvgpr_read_b32 v122,a14\n"
"v_mfma_f32_16x16x32_bf16 a[76:79],v[204:207],v[228:231],a[76:79]\n"
"ds_read_b128 v[16:19],v97 offset:2048\n"
"v_accvgpr_read_b32 v123,a15\n"
"v_mfma_f32_16x16x32_bf16 a[76:79],v[140:143],v[164:167],a[76:79]\n"
"ds_read_b128 v[24:27],v97 offset:3072\n"
"v_cvt_pk_bf16_f32 v116,v116,v117\n"
"v_mfma_f32_16x16x32_bf16 a[84:87],v[140:143],v[168:171],a[84:87]\n"
"ds_read_b128 v[4:7],v97 offset:16384\n"
"v_cvt_pk_bf16_f32 v117,v118,v119\n"
"v_mfma_f32_16x16x32_bf16 a[84:87],v[204:207],v[232:235],a[84:87]\n"
"ds_read_b128 v[12:15],v97 offset:17408\n"
"v_cvt_pk_bf16_f32 v118,v120,v121\n"
"v_mfma_f32_16x16x32_bf16 a[92:95],v[204:207],v[236:239],a[92:95]\n"
"ds_read_b128 v[20:23],v97 offset:18432\n"
"v_cvt_pk_bf16_f32 v119,v122,v123\n"
"v_mfma_f32_16x16x32_bf16 a[92:95],v[140:143],v[172:175],a[92:95]\n"
"ds_read_b128 v[28:31],v97 offset:19456\n"
"buffer_store_dwordx4 v[116:119],v112,s[24:27],s56 offen offset:1024 sc1\n"
"v_mfma_f32_16x16x32_bf16 a[100:103],v[140:143],v[176:179],a[100:103]\n"
"ds_read_b64_tr_b16 v[32:33],v98 offset:49152\n"
"v_accvgpr_read_b32 v116,a16\n"
"v_mfma_f32_16x16x32_bf16 a[100:103],v[204:207],v[240:243],a[100:103]\n"
"ds_read_b64_tr_b16 v[34:35],v98 offset:50176\n"
"v_accvgpr_read_b32 v117,a17\n"
"v_mfma_f32_16x16x32_bf16 a[108:111],v[204:207],v[244:247],a[108:111]\n"
"ds_read_b64_tr_b16 v[36:37],v98 offset:57344\n"
"v_accvgpr_read_b32 v118,a18\n"
"v_mfma_f32_16x16x32_bf16 a[108:111],v[140:143],v[180:183],a[108:111]\n"
"ds_read_b64_tr_b16 v[38:39],v98 offset:58368\n"
"v_accvgpr_read_b32 v119,a19\n"
"v_mfma_f32_16x16x32_bf16 a[116:119],v[140:143],v[184:187],a[116:119]\n"
"ds_read_b64_tr_b16 v[40:41],v99 offset:49152\n"
"v_accvgpr_read_b32 v120,a20\n"
"v_mfma_f32_16x16x32_bf16 a[116:119],v[204:207],v[248:251],a[116:119]\n"
"ds_read_b64_tr_b16 v[42:43],v99 offset:50176\n"
"v_accvgpr_read_b32 v121,a21\n"
"v_mfma_f32_16x16x32_bf16 a[124:127],v[204:207],v[252:255],a[124:127]\n"
"ds_read_b64_tr_b16 v[44:45],v99 offset:57344\n"
"v_accvgpr_read_b32 v122,a22\n"
"v_mfma_f32_16x16x32_bf16 a[124:127],v[140:143],v[188:191],a[124:127]\n"
"ds_read_b64_tr_b16 v[46:47],v99 offset:58368\n"
"v_accvgpr_read_b32 v123,a23\n"
"v_mfma_f32_16x16x32_bf16 a[128:131],v[144:147],v[160:163],a[128:131]\n"
"ds_read_b64_tr_b16 v[48:49],v100 offset:49152\n"
"v_cvt_pk_bf16_f32 v116,v116,v117\n"
"v_mfma_f32_16x16x32_bf16 a[128:131],v[208:211],v[224:227],a[128:131]\n"
"ds_read_b64_tr_b16 v[50:51],v100 offset:50176\n"
"v_cvt_pk_bf16_f32 v117,v118,v119\n"
"v_mfma_f32_16x16x32_bf16 a[136:139],v[208:211],v[228:231],a[136:139]\n"
"ds_read_b64_tr_b16 v[52:53],v100 offset:57344\n"
"v_cvt_pk_bf16_f32 v118,v120,v121\n"
"v_mfma_f32_16x16x32_bf16 a[136:139],v[144:147],v[164:167],a[136:139]\n"
"ds_read_b64_tr_b16 v[54:55],v100 offset:58368\n"
"v_cvt_pk_bf16_f32 v119,v122,v123\n"
"v_mfma_f32_16x16x32_bf16 a[144:147],v[144:147],v[168:171],a[144:147]\n"
"ds_read_b64_tr_b16 v[56:57],v101 offset:49152\n"
"buffer_store_dwordx4 v[116:119],v112,s[24:27],s56 offen offset:2048 sc1\n"
"v_mfma_f32_16x16x32_bf16 a[144:147],v[208:211],v[232:235],a[144:147]\n"
"ds_read_b64_tr_b16 v[58:59],v101 offset:50176\n"
"v_accvgpr_read_b32 v116,a24\n"
"v_mfma_f32_16x16x32_bf16 a[152:155],v[208:211],v[236:239],a[152:155]\n"
"ds_read_b64_tr_b16 v[60:61],v101 offset:57344\n"
"v_accvgpr_read_b32 v117,a25\n"
"v_mfma_f32_16x16x32_bf16 a[152:155],v[144:147],v[172:175],a[152:155]\n"
"ds_read_b64_tr_b16 v[62:63],v101 offset:58368\n"
"v_accvgpr_read_b32 v118,a26\n"
"v_mfma_f32_16x16x32_bf16 a[160:163],v[144:147],v[176:179],a[160:163]\n"
"ds_read_b64_tr_b16 v[64:65],v102 offset:49152\n"
"v_accvgpr_read_b32 v119,a27\n"
"v_mfma_f32_16x16x32_bf16 a[160:163],v[208:211],v[240:243],a[160:163]\n"
"ds_read_b64_tr_b16 v[66:67],v102 offset:50176\n"
"v_accvgpr_read_b32 v120,a28\n"
"v_mfma_f32_16x16x32_bf16 a[168:171],v[208:211],v[244:247],a[168:171]\n"
"ds_read_b64_tr_b16 v[68:69],v102 offset:57344\n"
"v_accvgpr_read_b32 v121,a29\n"
"v_mfma_f32_16x16x32_bf16 a[168:171],v[144:147],v[180:183],a[168:171]\n"
"ds_read_b64_tr_b16 v[70:71],v102 offset:58368\n"
"v_accvgpr_read_b32 v122,a30\n"
"v_mfma_f32_16x16x32_bf16 a[176:179],v[144:147],v[184:187],a[176:179]\n"
"ds_read_b64_tr_b16 v[72:73],v103 offset:49152\n"
"v_accvgpr_read_b32 v123,a31\n"
"v_mfma_f32_16x16x32_bf16 a[176:179],v[208:211],v[248:251],a[176:179]\n"
"ds_read_b64_tr_b16 v[74:75],v103 offset:50176\n"
"v_cvt_pk_bf16_f32 v116,v116,v117\n"
"v_mfma_f32_16x16x32_bf16 a[184:187],v[208:211],v[252:255],a[184:187]\n"
"ds_read_b64_tr_b16 v[76:77],v103 offset:57344\n"
"v_cvt_pk_bf16_f32 v117,v118,v119\n"
"v_mfma_f32_16x16x32_bf16 a[184:187],v[144:147],v[188:191],a[184:187]\n"
"ds_read_b64_tr_b16 v[78:79],v103 offset:58368\n"
"v_cvt_pk_bf16_f32 v118,v120,v121\n"
"v_mfma_f32_16x16x32_bf16 a[132:135],v[148:151],v[160:163],a[132:135]\n"
"ds_read_b64_tr_b16 v[80:81],v104 offset:49152\n"
"v_cvt_pk_bf16_f32 v119,v122,v123\n"
"v_mfma_f32_16x16x32_bf16 a[132:135],v[212:215],v[224:227],a[132:135]\n"
"ds_read_b64_tr_b16 v[82:83],v104 offset:50176\n"
"buffer_store_dwordx4 v[116:119],v112,s[24:27],s56 offen offset:3072 sc1\n"
"v_mfma_f32_16x16x32_bf16 a[140:143],v[212:215],v[228:231],a[140:143]\n"
"ds_read_b64_tr_b16 v[84:85],v104 offset:57344\n"
"v_mfma_f32_16x16x32_bf16 a[140:143],v[148:151],v[164:167],a[140:143]\n"
"ds_read_b64_tr_b16 v[86:87],v104 offset:58368\n"
"v_mfma_f32_16x16x32_bf16 a[148:151],v[148:151],v[168:171],a[148:151]\n"
"ds_read_b64_tr_b16 v[88:89],v105 offset:49152\n"
"v_mfma_f32_16x16x32_bf16 a[148:151],v[212:215],v[232:235],a[148:151]\n"
"ds_read_b64_tr_b16 v[90:91],v105 offset:50176\n"
"v_mfma_f32_16x16x32_bf16 a[156:159],v[212:215],v[236:239],a[156:159]\n"
"ds_read_b64_tr_b16 v[92:93],v105 offset:57344\n"
"v_mfma_f32_16x16x32_bf16 a[156:159],v[148:151],v[172:175],a[156:159]\n"
"ds_read_b64_tr_b16 v[94:95],v105 offset:58368\n"
"v_mfma_f32_16x16x32_bf16 a[164:167],v[148:151],v[176:179],a[164:167]\n"
"s_add_u32 m0,s85,106496\n"
"v_mfma_f32_16x16x32_bf16 a[164:167],v[212:215],v[240:243],a[164:167]\n"
"buffer_load_dwordx4 v110,s[28:31],s86 offen lds\n"
"v_mfma_f32_16x16x32_bf16 a[172:175],v[212:215],v[244:247],a[172:175]\n"
"v_mfma_f32_16x16x32_bf16 a[172:175],v[148:151],v[180:183],a[172:175]\n"
"s_add_u32 m0,s85,107520\n"
"v_mfma_f32_16x16x32_bf16 a[180:183],v[148:151],v[184:187],a[180:183]\n"
"buffer_load_dwordx4 v111,s[28:31],s86 offen lds\n"
"v_mfma_f32_16x16x32_bf16 a[180:183],v[212:215],v[248:251],a[180:183]\n"
"s_xor_b32 s86,s86,786432\n"
"v_mfma_f32_16x16x32_bf16 a[188:191],v[212:215],v[252:255],a[188:191]\n"
"s_add_u32 m0,s78,49152\n"
"v_mfma_f32_16x16x32_bf16 a[188:191],v[148:151],v[188:191],a[188:191]\n"
"buffer_load_dwordx4 v112,s[24:27],s76 offen offset:0 sc1 lds\n"
"v_mfma_f32_16x16x32_bf16 a[192:195],v[216:219],v[224:227],a[192:195]\n"
"v_mfma_f32_16x16x32_bf16 a[196:199],v[220:223],v[224:227],a[196:199]\n"
"v_mfma_f32_16x16x32_bf16 a[204:207],v[220:223],v[228:231],a[204:207]\n"
"buffer_load_dwordx4 v112,s[24:27],s76 offen offset:1024 sc1 lds\n"
"v_mfma_f32_16x16x32_bf16 a[200:203],v[216:219],v[228:231],a[200:203]\n"
"v_mfma_f32_16x16x32_bf16 a[208:211],v[216:219],v[232:235],a[208:211]\n"
"v_mfma_f32_16x16x32_bf16 a[212:215],v[220:223],v[232:235],a[212:215]\n"
"buffer_load_dwordx4 v112,s[24:27],s76 offen offset:2048 sc1 lds\n"
"v_mfma_f32_16x16x32_bf16 a[220:223],v[220:223],v[236:239],a[220:223]\n"
"v_mfma_f32_16x16x32_bf16 a[216:219],v[216:219],v[236:239],a[216:219]\n"
"buffer_load_dwordx4 v112,s[24:27],s76 offen offset:3072 sc1 lds\n"
"v_mfma_f32_16x16x32_bf16 a[224:227],v[216:219],v[240:243],a[224:227]\n"
"s_xor_b32 s76,s76,49152\n"
"v_mfma_f32_16x16x32_bf16 a[228:231],v[220:223],v[240:243],a[228:231]\n"
"v_mfma_f32_16x16x32_bf16 a[236:239],v[220:223],v[244:247],a[236:239]\n"
"v_mfma_f32_16x16x32_bf16 a[232:235],v[216:219],v[244:247],a[232:235]\n"
"v_mfma_f32_16x16x32_bf16 a[240:243],v[216:219],v[248:251],a[240:243]\n"
"v_mfma_f32_16x16x32_bf16 a[244:247],v[220:223],v[248:251],a[244:247]\n"
"v_mfma_f32_16x16x32_bf16 a[252:255],v[220:223],v[252:255],a[252:255]\n"
"v_mfma_f32_16x16x32_bf16 a[248:251],v[216:219],v[252:255],a[248:251]\n"
".Lfk_ph2:\n"
"s_waitcnt vmcnt(25) lgkmcnt(0)\n"
"s_barrier\n"
"v_mfma_f32_16x16x32_bf16 v[128:131],v[32:35],v[0:3],0\n"
"v_accvgpr_read_b32 v116,a32\n"
"v_accvgpr_read_b32 v117,a33\n"
"v_mfma_f32_16x16x32_bf16 v[128:131],v[36:39],v[4:7],v[128:131]\n"
"v_accvgpr_read_b32 v118,a34\n"
"v_accvgpr_read_b32 v119,a35\n"
"v_mfma_f32_16x16x32_bf16 v[160:163],v[36:39],v[12:15],0\n"
"v_accvgpr_read_b32 v120,a36\n"
"v_accvgpr_read_b32 v121,a37\n"
"v_mfma_f32_16x16x32_bf16 v[160:163],v[32:35],v[8:11],v[160:163]\n"
"s_add_u32 m0,s85,147456\n"
"v_accvgpr_read_b32 v122,a38\n"
"v_mfma_f32_16x16x32_bf16 v[192:195],v[32:35],v[16:19],0\n"
"buffer_load_dwordx4 v110,s[28:31],s86 offen lds\n"
"v_mfma_f32_16x16x32_bf16 v[192:195],v[36:39],v[20:23],v[192:195]\n"
"v_accvgpr_read_b32 v123,a39\n"
"v_cvt_pk_bf16_f32 v116,v116,v117\n"
"v_mfma_f32_16x16x32_bf16 v[224:227],v[36:39],v[28:31],0\n"
"v_cvt_pk_bf16_f32 v117,v118,v119\n"
"v_cvt_pk_bf16_f32 v118,v120,v121\n"
"v_mfma_f32_16x16x32_bf16 v[224:227],v[32:35],v[24:27],v[224:227]\n"
"v_cvt_pk_bf16_f32 v119,v122,v123\n"
"s_add_u32 s56,s56,4096\n"
"v_mfma_f32_16x16x32_bf16 v[228:231],v[40:43],v[24:27],0\n"
"ds_read_b64_tr_b16 v[32:33],v100 offset:32768\n"
"buffer_store_dwordx4 v[116:119],v112,s[24:27],s56 offen offset:0 sc1\n"
"v_mfma_f32_16x16x32_bf16 v[228:231],v[44:47],v[28:31],v[228:231]\n"
"ds_read_b64_tr_b16 v[34:35],v100 offset:33792\n"
"v_accvgpr_read_b32 v116,a40\n"
"v_mfma_f32_16x16x32_bf16 v[196:199],v[44:47],v[20:23],0\n"
"ds_read_b64_tr_b16 v[36:37],v100 offset:40960\n"
"v_accvgpr_read_b32 v117,a41\n"
"v_mfma_f32_16x16x32_bf16 v[196:199],v[40:43],v[16:19],v[196:199]\n"
"s_add_u32 m0,s85,148480\n"
"ds_read_b64_tr_b16 v[38:39],v100 offset:41984\n"
"v_mfma_f32_16x16x32_bf16 v[164:167],v[40:43],v[8:11],0\n"
"buffer_load_dwordx4 v111,s[28:31],s86 offen lds\n"
"v_mfma_f32_16x16x32_bf16 v[164:167],v[44:47],v[12:15],v[164:167]\n"
"s_add_u32 m0,s85,155648\n"
"s_xor_b32 s86,s86,262144\n"
"v_mfma_f32_16x16x32_bf16 v[132:135],v[44:47],v[4:7],0\n"
"buffer_load_dwordx4 v110,s[28:31],s86 offen lds\n"
"v_mfma_f32_16x16x32_bf16 v[132:135],v[40:43],v[0:3],v[132:135]\n"
"v_accvgpr_read_b32 v118,a42\n"
"v_accvgpr_read_b32 v119,a43\n"
"v_mfma_f32_16x16x32_bf16 v[136:139],v[48:51],v[0:3],0\n"
"ds_read_b64_tr_b16 v[40:41],v101 offset:32768\n"
"v_accvgpr_read_b32 v120,a44\n"
"v_mfma_f32_16x16x32_bf16 v[136:139],v[52:55],v[4:7],v[136:139]\n"
"ds_read_b64_tr_b16 v[42:43],v101 offset:33792\n"
"v_accvgpr_read_b32 v121,a45\n"
"v_mfma_f32_16x16x32_bf16 v[168:171],v[52:55],v[12:15],0\n"
"ds_read_b64_tr_b16 v[44:45],v101 offset:40960\n"
"v_accvgpr_read_b32 v122,a46\n"
"v_mfma_f32_16x16x32_bf16 v[168:171],v[48:51],v[8:11],v[168:171]\n"
"s_add_u32 m0,s85,156672\n"
"ds_read_b64_tr_b16 v[46:47],v101 offset:41984\n"
"v_mfma_f32_16x16x32_bf16 v[200:203],v[48:51],v[16:19],0\n"
"buffer_load_dwordx4 v111,s[28:31],s86 offen lds\n"
"v_mfma_f32_16x16x32_bf16 v[200:203],v[52:55],v[20:23],v[200:203]\n"
"s_xor_b32 s86,s86,3932160\n"
"v_accvgpr_read_b32 v123,a47\n"
"v_mfma_f32_16x16x32_bf16 v[232:235],v[52:55],v[28:31],0\n"
"v_cvt_pk_bf16_f32 v116,v116,v117\n"
"v_cvt_pk_bf16_f32 v117,v118,v119\n"
"v_mfma_f32_16x16x32_bf16 v[232:235],v[48:51],v[24:27],v[232:235]\n"
"v_cvt_pk_bf16_f32 v118,v120,v121\n"
"v_cvt_pk_bf16_f32 v119,v122,v123\n"
"v_mfma_f32_16x16x32_bf16 v[236:239],v[56:59],v[24:27],0\n"
"ds_read_b64_tr_b16 v[48:49],v102 offset:32768\n"
"buffer_store_dwordx4 v[116:119],v112,s[24:27],s56 offen offset:1024 sc1\n"
"v_mfma_f32_16x16x32_bf16 v[236:239],v[60:63],v[28:31],v[236:239]\n"
"ds_read_b64_tr_b16 v[50:51],v102 offset:33792\n"
"v_accvgpr_read_b32 v116,a48\n"
"v_mfma_f32_16x16x32_bf16 v[204:207],v[60:63],v[20:23],0\n"
"ds_read_b64_tr_b16 v[52:53],v102 offset:40960\n"
"v_accvgpr_read_b32 v117,a49\n"
"v_mfma_f32_16x16x32_bf16 v[204:207],v[56:59],v[16:19],v[204:207]\n"
"s_add_u32 m0,s78,65536\n"
"ds_read_b64_tr_b16 v[54:55],v102 offset:41984\n"
"v_mfma_f32_16x16x32_bf16 v[172:175],v[56:59],v[8:11],0\n"
"buffer_load_dwordx4 v112,s[24:27],s76 offen offset:0 sc1 lds\n"
"v_mfma_f32_16x16x32_bf16 v[172:175],v[60:63],v[12:15],v[172:175]\n"
"v_accvgpr_read_b32 v118,a50\n"
"v_mfma_f32_16x16x32_bf16 v[140:143],v[60:63],v[4:7],0\n"
"buffer_load_dwordx4 v112,s[24:27],s76 offen offset:1024 sc1 lds\n"
"v_mfma_f32_16x16x32_bf16 v[140:143],v[56:59],v[0:3],v[140:143]\n"
"s_waitcnt vmcnt(25)\n"
"v_mfma_f32_16x16x32_bf16 v[144:147],v[64:67],v[0:3],0\n"
"ds_read_b64_tr_b16 a[32:33],v98 offset:32768\n"
"ds_read_b64_tr_b16 v[56:57],v103 offset:32768\n"
"v_mfma_f32_16x16x32_bf16 v[144:147],v[68:71],v[4:7],v[144:147]\n"
"ds_read_b64_tr_b16 a[34:35],v98 offset:33792\n"
"ds_read_b64_tr_b16 v[58:59],v103 offset:33792\n"
"v_mfma_f32_16x16x32_bf16 v[176:179],v[68:71],v[12:15],0\n"
"ds_read_b64_tr_b16 a[36:37],v98 offset:40960\n"
"ds_read_b64_tr_b16 v[60:61],v103 offset:40960\n"
"v_mfma_f32_16x16x32_bf16 v[176:179],v[64:67],v[8:11],v[176:179]\n"
"ds_read_b64_tr_b16 a[38:39],v98 offset:41984\n"
"ds_read_b64_tr_b16 v[62:63],v103 offset:41984\n"
"v_mfma_f32_16x16x32_bf16 v[208:211],v[64:67],v[16:19],0\n"
"buffer_load_dwordx4 v112,s[24:27],s76 offen offset:2048 sc1 lds\n"
"v_mfma_f32_16x16x32_bf16 v[208:211],v[68:71],v[20:23],v[208:211]\n"
"ds_read_b128 a[0:3],v96 offset:0\n"
"v_accvgpr_read_b32 v119,a51\n"
"v_mfma_f32_16x16x32_bf16 v[240:243],v[68:71],v[28:31],0\n"
"v_accvgpr_read_b32 v120,a52\n"
"v_accvgpr_read_b32 v121,a53\n"
"v_mfma_f32_16x16x32_bf16 v[240:243],v[64:67],v[24:27],v[240:243]\n"
"ds_read_b128 a[8:11],v96 offset:1024\n"
"v_accvgpr_read_b32 v122,a54\n"
"v_mfma_f32_16x16x32_bf16 v[244:247],v[72:75],v[24:27],0\n"
"ds_read_b64_tr_b16 a[40:41],v99 offset:32768\n"
"ds_read_b64_tr_b16 v[64:65],v104 offset:32768\n"
"v_mfma_f32_16x16x32_bf16 v[244:247],v[76:79],v[28:31],v[244:247]\n"
"ds_read_b64_tr_b16 a[42:43],v99 offset:33792\n"
"ds_read_b64_tr_b16 v[66:67],v104 offset:33792\n"
"v_mfma_f32_16x16x32_bf16 v[212:215],v[76:79],v[20:23],0\n"
"ds_read_b64_tr_b16 a[44:45],v99 offset:40960\n"
"ds_read_b64_tr_b16 v[68:69],v104 offset:40960\n"
"v_mfma_f32_16x16x32_bf16 v[212:215],v[72:75],v[16:19],v[212:215]\n"
"ds_read_b64_tr_b16 a[46:47],v99 offset:41984\n"
"ds_read_b64_tr_b16 v[70:71],v104 offset:41984\n"
"v_mfma_f32_16x16x32_bf16 v[180:183],v[72:75],v[8:11],0\n"
"buffer_load_dwordx4 v112,s[24:27],s76 offen offset:3072 sc1 lds\n"
"v_mfma_f32_16x16x32_bf16 v[180:183],v[76:79],v[12:15],v[180:183]\n"
"s_add_u32 m0,s78,81920\n"
"s_xor_b32 s76,s76,16384\n"
"ds_read_b128 a[16:19],v96 offset:2048\n"
"v_mfma_f32_16x16x32_bf16 v[148:151],v[76:79],v[4:7],0\n"
"buffer_load_dwordx4 v112,s[24:27],s76 offen offset:0 sc1 lds\n"
"v_mfma_f32_16x16x32_bf16 v[148:151],v[72:75],v[0:3],v[148:151]\n"
"ds_read_b128 a[24:27],v96 offset:3072\n"
"v_accvgpr_read_b32 v123,a55\n"
"v_mfma_f32_16x16x32_bf16 v[152:155],v[80:83],v[0:3],0\n"
"ds_read_b64_tr_b16 v[72:73],v105 offset:32768\n"
"v_cvt_pk_bf16_f32 v116,v116,v117\n"
"v_mfma_f32_16x16x32_bf16 v[152:155],v[84:87],v[4:7],v[152:155]\n"
"ds_read_b64_tr_b16 v[74:75],v105 offset:33792\n"
"v_cvt_pk_bf16_f32 v117,v118,v119\n"
"v_mfma_f32_16x16x32_bf16 v[184:187],v[84:87],v[12:15],0\n"
"ds_read_b64_tr_b16 v[76:77],v105 offset:40960\n"
"v_cvt_pk_bf16_f32 v118,v120,v121\n"
"v_mfma_f32_16x16x32_bf16 v[184:187],v[80:83],v[8:11],v[184:187]\n"
"ds_read_b64_tr_b16 v[78:79],v105 offset:41984\n"
"v_cvt_pk_bf16_f32 v119,v122,v123\n"
"v_mfma_f32_16x16x32_bf16 v[216:219],v[80:83],v[16:19],0\n"
"ds_read_b128 a[4:7],v96 offset:16384\n"
"buffer_store_dwordx4 v[116:119],v112,s[24:27],s56 offen offset:2048 sc1\n"
"v_mfma_f32_16x16x32_bf16 v[216:219],v[84:87],v[20:23],v[216:219]\n"
"ds_read_b128 a[12:15],v96 offset:17408\n"
"v_accvgpr_read_b32 v116,a56\n"
"v_mfma_f32_16x16x32_bf16 v[248:251],v[84:87],v[28:31],0\n"
"ds_read_b128 a[20:23],v96 offset:18432\n"
"v_accvgpr_read_b32 v117,a57\n"
"v_mfma_f32_16x16x32_bf16 v[248:251],v[80:83],v[24:27],v[248:251]\n"
"ds_read_b128 a[28:31],v96 offset:19456\n"
"v_accvgpr_read_b32 v118,a58\n"
"v_mfma_f32_16x16x32_bf16 v[252:255],v[88:91],v[24:27],0\n"
"buffer_load_dwordx4 v112,s[24:27],s76 offen offset:1024 sc1 lds\n"
"v_mfma_f32_16x16x32_bf16 v[252:255],v[92:95],v[28:31],v[252:255]\n"
"v_accvgpr_read_b32 v119,a59\n"
"v_accvgpr_read_b32 v120,a60\n"
"v_mfma_f32_16x16x32_bf16 v[220:223],v[92:95],v[20:23],0\n"
"v_accvgpr_read_b32 v121,a61\n"
"v_accvgpr_read_b32 v122,a62\n"
"v_mfma_f32_16x16x32_bf16 v[220:223],v[88:91],v[16:19],v[220:223]\n"
"buffer_load_dwordx4 v112,s[24:27],s76 offen offset:2048 sc1 lds\n"
"v_mfma_f32_16x16x32_bf16 v[188:191],v[88:91],v[8:11],0\n"
"v_accvgpr_read_b32 v123,a63\n"
"v_cvt_pk_bf16_f32 v116,v116,v117\n"
"v_mfma_f32_16x16x32_bf16 v[188:191],v[92:95],v[12:15],v[188:191]\n"
"v_cvt_pk_bf16_f32 v117,v118,v119\n"
"v_cvt_pk_bf16_f32 v118,v120,v121\n"
"v_mfma_f32_16x16x32_bf16 v[156:159],v[92:95],v[4:7],0\n"
"buffer_load_dwordx4 v112,s[24:27],s76 offen offset:3072 sc1 lds\n"
"v_mfma_f32_16x16x32_bf16 v[156:159],v[88:91],v[0:3],v[156:159]\n"
"s_xor_b32 s76,s76,245760\n"
"v_cvt_pk_bf16_f32 v119,v122,v123\n"
"s_waitcnt vmcnt(19) lgkmcnt(0)\n"
"s_barrier\n"
"v_mfma_f32_16x16x32_bf16 v[128:131],a[32:35],a[0:3],v[128:131]\n"
"ds_read_b64_tr_b16 v[80:81],v98 offset:0\n"
"ds_read_b64_tr_b16 v[88:89],v99 offset:0\n"
"v_mfma_f32_16x16x32_bf16 v[128:131],a[36:39],a[4:7],v[128:131]\n"
"ds_read_b64_tr_b16 v[82:83],v98 offset:1024\n"
"ds_read_b64_tr_b16 v[90:91],v99 offset:1024\n"
"v_mfma_f32_16x16x32_bf16 v[160:163],a[36:39],a[12:15],v[160:163]\n"
"ds_read_b64_tr_b16 v[84:85],v98 offset:8192\n"
"ds_read_b64_tr_b16 v[92:93],v99 offset:8192\n"
"v_mfma_f32_16x16x32_bf16 v[160:163],a[32:35],a[8:11],v[160:163]\n"
"s_add_u32 m0,s85,131072\n"
"ds_read_b64_tr_b16 v[86:87],v98 offset:9216\n"
"ds_read_b64_tr_b16 v[94:95],v99 offset:9216\n"
"v_mfma_f32_16x16x32_bf16 v[192:195],a[32:35],a[16:19],v[192:195]\n"
"buffer_load_dwordx4 v110,s[28:31],s86 offen lds\n"
"buffer_store_dwordx4 v[116:119],v112,s[24:27],s56 offen offset:3072 sc1\n"
"v_mfma_f32_16x16x32_bf16 v[192:195],a[36:39],a[20:23],v[192:195]\n"
"s_nop 0\n"
"v_accvgpr_read_b32 v116,a64\n"
"v_mfma_f32_16x16x32_bf16 v[224:227],a[36:39],a[28:31],v[224:227]\n"
"v_accvgpr_read_b32 v117,a65\n"
"v_accvgpr_read_b32 v118,a66\n"
"v_mfma_f32_16x16x32_bf16 v[224:227],a[32:35],a[24:27],v[224:227]\n"
"v_accvgpr_read_b32 v119,a67\n"
"v_accvgpr_read_b32 v120,a68\n"
"v_mfma_f32_16x16x32_bf16 v[228:231],a[40:43],a[24:27],v[228:231]\n"
"ds_read_b64_tr_b16 a[32:33],v100 offset:0\n"
"v_accvgpr_read_b32 v121,a69\n"
"v_mfma_f32_16x16x32_bf16 v[228:231],a[44:47],a[28:31],v[228:231]\n"
"ds_read_b64_tr_b16 a[34:35],v100 offset:1024\n"
"v_accvgpr_read_b32 v122,a70\n"
"v_mfma_f32_16x16x32_bf16 v[196:199],a[44:47],a[20:23],v[196:199]\n"
"ds_read_b64_tr_b16 a[36:37],v100 offset:8192\n"
"v_accvgpr_read_b32 v123,a71\n"
"v_mfma_f32_16x16x32_bf16 v[196:199],a[40:43],a[16:19],v[196:199]\n"
"s_add_u32 m0,s85,132096\n"
"ds_read_b64_tr_b16 a[38:39],v100 offset:9216\n"
"v_mfma_f32_16x16x32_bf16 v[164:167],a[40:43],a[8:11],v[164:167]\n"
"buffer_load_dwordx4 v111,s[28:31],s86 offen lds\n"
"v_mfma_f32_16x16x32_bf16 v[164:167],a[44:47],a[12:15],v[164:167]\n"
"s_add_u32 m0,s85,139264\n"
"s_xor_b32 s86,s86,262144\n"
"v_mfma_f32_16x16x32_bf16 v[132:135],a[44:47],a[4:7],v[132:135]\n"
"buffer_load_dwordx4 v110,s[28:31],s86 offen lds\n"
"v_mfma_f32_16x16x32_bf16 v[132:135],a[40:43],a[0:3],v[132:135]\n"
"v_cvt_pk_bf16_f32 v116,v116,v117\n"
"v_cvt_pk_bf16_f32 v117,v118,v119\n"
"v_mfma_f32_16x16x32_bf16 v[136:139],v[32:35],a[0:3],v[136:139]\n"
"ds_read_b64_tr_b16 a[40:41],v101 offset:0\n"
"v_cvt_pk_bf16_f32 v118,v120,v121\n"
"v_mfma_f32_16x16x32_bf16 v[136:139],v[36:39],a[4:7],v[136:139]\n"
"ds_read_b64_tr_b16 a[42:43],v101 offset:1024\n"
"v_cvt_pk_bf16_f32 v119,v122,v123\n"
"v_mfma_f32_16x16x32_bf16 v[168:171],v[36:39],a[12:15],v[168:171]\n"
"ds_read_b64_tr_b16 a[44:45],v101 offset:8192\n"
"s_add_u32 s56,s56,126976\n"
"v_mfma_f32_16x16x32_bf16 v[168:171],v[32:35],a[8:11],v[168:171]\n"
"s_add_u32 m0,s85,140288\n"
"ds_read_b64_tr_b16 a[46:47],v101 offset:9216\n"
"v_mfma_f32_16x16x32_bf16 v[200:203],v[32:35],a[16:19],v[200:203]\n"
"buffer_load_dwordx4 v111,s[28:31],s86 offen lds\n"
"v_mfma_f32_16x16x32_bf16 v[200:203],v[36:39],a[20:23],v[200:203]\n"
"buffer_store_dwordx4 v[116:119],v112,s[24:27],s56 offen offset:0 sc1\n"
"v_mfma_f32_16x16x32_bf16 v[232:235],v[36:39],a[28:31],v[232:235]\n"
"s_xor_b32 s86,s86,786432\n"
"v_accvgpr_read_b32 v116,a72\n"
"v_accvgpr_read_b32 v117,a73\n"
"v_mfma_f32_16x16x32_bf16 v[232:235],v[32:35],a[24:27],v[232:235]\n"
"v_accvgpr_read_b32 v118,a74\n"
"v_accvgpr_read_b32 v119,a75\n"
"v_mfma_f32_16x16x32_bf16 v[236:239],v[40:43],a[24:27],v[236:239]\n"
"ds_read_b64_tr_b16 v[32:33],v102 offset:0\n"
"v_accvgpr_read_b32 v120,a76\n"
"v_mfma_f32_16x16x32_bf16 v[236:239],v[44:47],a[28:31],v[236:239]\n"
"ds_read_b64_tr_b16 v[34:35],v102 offset:1024\n"
"v_accvgpr_read_b32 v121,a77\n"
"v_mfma_f32_16x16x32_bf16 v[204:207],v[44:47],a[20:23],v[204:207]\n"
"ds_read_b64_tr_b16 v[36:37],v102 offset:8192\n"
"v_accvgpr_read_b32 v122,a78\n"
"v_mfma_f32_16x16x32_bf16 v[204:207],v[40:43],a[16:19],v[204:207]\n"
"s_add_u32 m0,s78,0\n"
"ds_read_b64_tr_b16 v[38:39],v102 offset:9216\n"
"v_mfma_f32_16x16x32_bf16 v[172:175],v[40:43],a[8:11],v[172:175]\n"
"buffer_load_dwordx4 v112,s[24:27],s76 offen offset:0 sc1 lds\n"
"v_mfma_f32_16x16x32_bf16 v[172:175],v[44:47],a[12:15],v[172:175]\n"
"v_accvgpr_read_b32 v123,a79\n"
"v_cvt_pk_bf16_f32 v116,v116,v117\n"
"v_mfma_f32_16x16x32_bf16 v[140:143],v[44:47],a[4:7],v[140:143]\n"
"buffer_load_dwordx4 v112,s[24:27],s76 offen offset:1024 sc1 lds\n"
"v_mfma_f32_16x16x32_bf16 v[140:143],v[40:43],a[0:3],v[140:143]\n"
"s_waitcnt vmcnt(23)\n"
"v_cvt_pk_bf16_f32 v117,v118,v119\n"
"v_cvt_pk_bf16_f32 v118,v120,v121\n"
"v_mfma_f32_16x16x32_bf16 v[144:147],v[48:51],a[0:3],v[144:147]\n"
"ds_read_b64_tr_b16 v[40:41],v103 offset:0\n"
"v_cvt_pk_bf16_f32 v119,v122,v123\n"
"v_mfma_f32_16x16x32_bf16 v[144:147],v[52:55],a[4:7],v[144:147]\n"
"ds_read_b64_tr_b16 v[42:43],v103 offset:1024\n"
"buffer_store_dwordx4 v[116:119],v112,s[24:27],s56 offen offset:1024 sc1\n"
"v_mfma_f32_16x16x32_bf16 v[176:179],v[52:55],a[12:15],v[176:179]\n"
"ds_read_b64_tr_b16 v[44:45],v103 offset:8192\n"
"v_accvgpr_read_b32 v116,a80\n"
"v_mfma_f32_16x16x32_bf16 v[176:179],v[48:51],a[8:11],v[176:179]\n"
"ds_read_b64_tr_b16 v[46:47],v103 offset:9216\n"
"v_accvgpr_read_b32 v117,a81\n"
"v_mfma_f32_16x16x32_bf16 v[208:211],v[48:51],a[16:19],v[208:211]\n"
"buffer_load_dwordx4 v112,s[24:27],s76 offen offset:2048 sc1 lds\n"
"v_mfma_f32_16x16x32_bf16 v[208:211],v[52:55],a[20:23],v[208:211]\n"
"ds_read_b128 v[0:3],v96 offset:32768\n"
"v_accvgpr_read_b32 v118,a82\n"
"v_mfma_f32_16x16x32_bf16 v[240:243],v[52:55],a[28:31],v[240:243]\n"
"v_accvgpr_read_b32 v119,a83\n"
"v_accvgpr_read_b32 v120,a84\n"
"v_mfma_f32_16x16x32_bf16 v[240:243],v[48:51],a[24:27],v[240:243]\n"
"ds_read_b128 v[8:11],v96 offset:33792\n"
"v_accvgpr_read_b32 v121,a85\n"
"v_mfma_f32_16x16x32_bf16 v[244:247],v[56:59],a[24:27],v[244:247]\n"
"ds_read_b64_tr_b16 v[48:49],v104 offset:0\n"
"v_accvgpr_read_b32 v122,a86\n"
"v_mfma_f32_16x16x32_bf16 v[244:247],v[60:63],a[28:31],v[244:247]\n"
"ds_read_b64_tr_b16 v[50:51],v104 offset:1024\n"
"v_accvgpr_read_b32 v123,a87\n"
"v_mfma_f32_16x16x32_bf16 v[212:215],v[60:63],a[20:23],v[212:215]\n"
"ds_read_b64_tr_b16 v[52:53],v104 offset:8192\n"
"v_cvt_pk_bf16_f32 v116,v116,v117\n"
"v_mfma_f32_16x16x32_bf16 v[212:215],v[56:59],a[16:19],v[212:215]\n"
"ds_read_b64_tr_b16 v[54:55],v104 offset:9216\n"
"v_cvt_pk_bf16_f32 v117,v118,v119\n"
"v_mfma_f32_16x16x32_bf16 v[180:183],v[56:59],a[8:11],v[180:183]\n"
"buffer_load_dwordx4 v112,s[24:27],s76 offen offset:3072 sc1 lds\n"
"v_mfma_f32_16x16x32_bf16 v[180:183],v[60:63],a[12:15],v[180:183]\n"
"s_add_u32 m0,s78,16384\n"
"s_xor_b32 s76,s76,16384\n"
"ds_read_b128 v[16:19],v96 offset:34816\n"
"v_mfma_f32_16x16x32_bf16 v[148:151],v[60:63],a[4:7],v[148:151]\n"
"buffer_load_dwordx4 v112,s[24:27],s76 offen offset:0 sc1 lds\n"
"v_mfma_f32_16x16x32_bf16 v[148:151],v[56:59],a[0:3],v[148:151]\n"
"ds_read_b128 v[24:27],v96 offset:35840\n"
"v_cvt_pk_bf16_f32 v118,v120,v121\n"
"v_mfma_f32_16x16x32_bf16 v[152:155],v[64:67],a[0:3],v[152:155]\n"
"ds_read_b64_tr_b16 v[56:57],v105 offset:0\n"
"v_cvt_pk_bf16_f32 v119,v122,v123\n"
"v_mfma_f32_16x16x32_bf16 v[152:155],v[68:71],a[4:7],v[152:155]\n"
"ds_read_b64_tr_b16 v[58:59],v105 offset:1024\n"
"buffer_store_dwordx4 v[116:119],v112,s[24:27],s56 offen offset:2048 sc1\n"
"v_mfma_f32_16x16x32_bf16 v[184:187],v[68:71],a[12:15],v[184:187]\n"
"ds_read_b64_tr_b16 v[60:61],v105 offset:8192\n"
"v_accvgpr_read_b32 v116,a88\n"
"v_mfma_f32_16x16x32_bf16 v[184:187],v[64:67],a[8:11],v[184:187]\n"
"ds_read_b64_tr_b16 v[62:63],v105 offset:9216\n"
"v_accvgpr_read_b32 v117,a89\n"
"v_mfma_f32_16x16x32_bf16 v[216:219],v[64:67],a[16:19],v[216:219]\n"
"ds_read_b128 v[4:7],v96 offset:49152\n"
"v_accvgpr_read_b32 v118,a90\n"
"v_mfma_f32_16x16x32_bf16 v[216:219],v[68:71],a[20:23],v[216:219]\n"
"ds_read_b128 v[12:15],v96 offset:50176\n"
"v_accvgpr_read_b32 v119,a91\n"
"v_mfma_f32_16x16x32_bf16 v[248:251],v[68:71],a[28:31],v[248:251]\n"
"ds_read_b128 v[20:23],v96 offset:51200\n"
"v_accvgpr_read_b32 v120,a92\n"
"v_mfma_f32_16x16x32_bf16 v[248:251],v[64:67],a[24:27],v[248:251]\n"
"ds_read_b128 v[28:31],v96 offset:52224\n"
"v_accvgpr_read_b32 v121,a93\n"
"v_mfma_f32_16x16x32_bf16 v[252:255],v[72:75],a[24:27],v[252:255]\n"
"buffer_load_dwordx4 v112,s[24:27],s76 offen offset:1024 sc1 lds\n"
"v_mfma_f32_16x16x32_bf16 v[252:255],v[76:79],a[28:31],v[252:255]\n"
"v_accvgpr_read_b32 v122,a94\n"
"v_accvgpr_read_b32 v123,a95\n"
"v_mfma_f32_16x16x32_bf16 v[220:223],v[76:79],a[20:23],v[220:223]\n"
"v_cvt_pk_bf16_f32 v116,v116,v117\n"
"v_cvt_pk_bf16_f32 v117,v118,v119\n"
"v_mfma_f32_16x16x32_bf16 v[220:223],v[72:75],a[16:19],v[220:223]\n"
"buffer_load_dwordx4 v112,s[24:27],s76 offen offset:2048 sc1 lds\n"
"v_mfma_f32_16x16x32_bf16 v[188:191],v[72:75],a[8:11],v[188:191]\n"
"v_cvt_pk_bf16_f32 v118,v120,v121\n"
"v_cvt_pk_bf16_f32 v119,v122,v123\n"
"v_mfma_f32_16x16x32_bf16 v[188:191],v[76:79],a[12:15],v[188:191]\n"
"buffer_store_dwordx4 v[116:119],v112,s[24:27],s56 offen offset:3072 sc1\n"
"v_mfma_f32_16x16x32_bf16 v[156:159],v[76:79],a[4:7],v[156:159]\n"
"buffer_load_dwordx4 v112,s[24:27],s76 offen offset:3072 sc1 lds\n"
"v_accvgpr_read_b32 v116,a96\n"
"v_mfma_f32_16x16x32_bf16 v[156:159],v[72:75],a[0:3],v[156:159]\n"
"s_xor_b32 s76,s76,49152\n"
"v_accvgpr_read_b32 v117,a97\n"
"s_waitcnt vmcnt(27) lgkmcnt(0)\n"
"s_barrier\n"
"v_mfma_f32_16x16x32_bf16 v[128:131],v[80:83],v[0:3],v[128:131]\n"
"ds_read_b64_tr_b16 v[64:65],v98 offset:49152\n"
"ds_read_b64_tr_b16 v[72:73],v99 offset:49152\n"
"v_mfma_f32_16x16x32_bf16 v[128:131],v[84:87],v[4:7],v[128:131]\n"
"ds_read_b64_tr_b16 v[66:67],v98 offset:50176\n"
"ds_read_b64_tr_b16 v[74:75],v99 offset:50176\n"
"v_mfma_f32_16x16x32_bf16 v[160:163],v[84:87],v[12:15],v[160:163]\n"
"ds_read_b64_tr_b16 v[68:69],v98 offset:57344\n"
"ds_read_b64_tr_b16 v[76:77],v99 offset:57344\n"
"v_mfma_f32_16x16x32_bf16 v[160:163],v[80:83],v[8:11],v[160:163]\n"
"s_add_u32 m0,s85,98304\n"
"ds_read_b64_tr_b16 v[70:71],v98 offset:58368\n"
"ds_read_b64_tr_b16 v[78:79],v99 offset:58368\n"
"v_mfma_f32_16x16x32_bf16 v[192:195],v[80:83],v[16:19],v[192:195]\n"
"buffer_load_dwordx4 v110,s[28:31],s86 offen lds\n"
"v_mfma_f32_16x16x32_bf16 v[192:195],v[84:87],v[20:23],v[192:195]\n"
"v_accvgpr_read_b32 v118,a98\n"
"v_accvgpr_read_b32 v119,a99\n"
"v_mfma_f32_16x16x32_bf16 v[224:227],v[84:87],v[28:31],v[224:227]\n"
"v_accvgpr_read_b32 v120,a100\n"
"v_accvgpr_read_b32 v121,a101\n"
"v_mfma_f32_16x16x32_bf16 v[224:227],v[80:83],v[24:27],v[224:227]\n"
"v_accvgpr_read_b32 v122,a102\n"
"v_accvgpr_read_b32 v123,a103\n"
"v_mfma_f32_16x16x32_bf16 v[228:231],v[88:91],v[24:27],v[228:231]\n"
"ds_read_b64_tr_b16 v[80:81],v100 offset:49152\n"
"v_cvt_pk_bf16_f32 v116,v116,v117\n"
"v_mfma_f32_16x16x32_bf16 v[228:231],v[92:95],v[28:31],v[228:231]\n"
"ds_read_b64_tr_b16 v[82:83],v100 offset:50176\n"
"v_cvt_pk_bf16_f32 v117,v118,v119\n"
"v_mfma_f32_16x16x32_bf16 v[196:199],v[92:95],v[20:23],v[196:199]\n"
"ds_read_b64_tr_b16 v[84:85],v100 offset:57344\n"
"v_cvt_pk_bf16_f32 v118,v120,v121\n"
"v_mfma_f32_16x16x32_bf16 v[196:199],v[88:91],v[16:19],v[196:199]\n"
"s_add_u32 m0,s85,99328\n"
"ds_read_b64_tr_b16 v[86:87],v100 offset:58368\n"
"v_mfma_f32_16x16x32_bf16 v[164:167],v[88:91],v[8:11],v[164:167]\n"
"buffer_load_dwordx4 v111,s[28:31],s86 offen lds\n"
"v_mfma_f32_16x16x32_bf16 v[164:167],v[92:95],v[12:15],v[164:167]\n"
"s_add_u32 m0,s85,106496\n"
"s_xor_b32 s86,s86,262144\n"
"v_mfma_f32_16x16x32_bf16 v[132:135],v[92:95],v[4:7],v[132:135]\n"
"buffer_load_dwordx4 v110,s[28:31],s86 offen lds\n"
"v_mfma_f32_16x16x32_bf16 v[132:135],v[88:91],v[0:3],v[132:135]\n"
"v_cvt_pk_bf16_f32 v119,v122,v123\n"
"s_add_u32 s56,s56,4096\n"
"v_mfma_f32_16x16x32_bf16 v[136:139],a[32:35],v[0:3],v[136:139]\n"
"ds_read_b64_tr_b16 v[88:89],v101 offset:49152\n"
"buffer_store_dwordx4 v[116:119],v112,s[24:27],s56 offen offset:0 sc1\n"
"v_mfma_f32_16x16x32_bf16 v[136:139],a[36:39],v[4:7],v[136:139]\n"
"ds_read_b64_tr_b16 v[90:91],v101 offset:50176\n"
"v_accvgpr_read_b32 v116,a104\n"
"v_mfma_f32_16x16x32_bf16 v[168:171],a[36:39],v[12:15],v[168:171]\n"
"ds_read_b64_tr_b16 v[92:93],v101 offset:57344\n"
"v_accvgpr_read_b32 v117,a105\n"
"v_mfma_f32_16x16x32_bf16 v[168:171],a[32:35],v[8:11],v[168:171]\n"
"s_add_u32 m0,s85,107520\n"
"ds_read_b64_tr_b16 v[94:95],v101 offset:58368\n"
"v_mfma_f32_16x16x32_bf16 v[200:203],a[32:35],v[16:19],v[200:203]\n"
"buffer_load_dwordx4 v111,s[28:31],s86 offen lds\n"
"v_mfma_f32_16x16x32_bf16 v[200:203],a[36:39],v[20:23],v[200:203]\n"
"s_xor_b32 s86,s86,1835008\n"
"v_accvgpr_read_b32 v118,a106\n"
"v_mfma_f32_16x16x32_bf16 v[232:235],a[36:39],v[28:31],v[232:235]\n"
"v_accvgpr_read_b32 v119,a107\n"
"v_accvgpr_read_b32 v120,a108\n"
"v_mfma_f32_16x16x32_bf16 v[232:235],a[32:35],v[24:27],v[232:235]\n"
"v_accvgpr_read_b32 v121,a109\n"
"v_accvgpr_read_b32 v122,a110\n"
"v_mfma_f32_16x16x32_bf16 v[236:239],a[40:43],v[24:27],v[236:239]\n"
"ds_read_b64_tr_b16 a[32:33],v102 offset:49152\n"
"v_accvgpr_read_b32 v123,a111\n"
"v_mfma_f32_16x16x32_bf16 v[236:239],a[44:47],v[28:31],v[236:239]\n"
"ds_read_b64_tr_b16 a[34:35],v102 offset:50176\n"
"v_cvt_pk_bf16_f32 v116,v116,v117\n"
"v_mfma_f32_16x16x32_bf16 v[204:207],a[44:47],v[20:23],v[204:207]\n"
"ds_read_b64_tr_b16 a[36:37],v102 offset:57344\n"
"v_cvt_pk_bf16_f32 v117,v118,v119\n"
"v_mfma_f32_16x16x32_bf16 v[204:207],a[40:43],v[16:19],v[204:207]\n"
"s_add_u32 m0,s78,32768\n"
"ds_read_b64_tr_b16 a[38:39],v102 offset:58368\n"
"v_mfma_f32_16x16x32_bf16 v[172:175],a[40:43],v[8:11],v[172:175]\n"
"buffer_load_dwordx4 v112,s[24:27],s76 offen offset:0 sc1 lds\n"
"v_mfma_f32_16x16x32_bf16 v[172:175],a[44:47],v[12:15],v[172:175]\n"
"v_cvt_pk_bf16_f32 v118,v120,v121\n"
"v_cvt_pk_bf16_f32 v119,v122,v123\n"
"v_mfma_f32_16x16x32_bf16 v[140:143],a[44:47],v[4:7],v[140:143]\n"
"buffer_load_dwordx4 v112,s[24:27],s76 offen offset:1024 sc1 lds\n"
"buffer_store_dwordx4 v[116:119],v112,s[24:27],s56 offen offset:1024 sc1\n"
"v_mfma_f32_16x16x32_bf16 v[140:143],a[40:43],v[0:3],v[140:143]\n"
"s_nop 0\n"
"v_accvgpr_read_b32 v116,a112\n"
"s_waitcnt vmcnt(25)\n"
"v_mfma_f32_16x16x32_bf16 v[144:147],v[32:35],v[0:3],v[144:147]\n"
"ds_read_b64_tr_b16 a[40:41],v103 offset:49152\n"
"v_accvgpr_read_b32 v117,a113\n"
"v_mfma_f32_16x16x32_bf16 v[144:147],v[36:39],v[4:7],v[144:147]\n"
"ds_read_b64_tr_b16 a[42:43],v103 offset:50176\n"
"v_accvgpr_read_b32 v118,a114\n"
"v_mfma_f32_16x16x32_bf16 v[176:179],v[36:39],v[12:15],v[176:179]\n"
"ds_read_b64_tr_b16 a[44:45],v103 offset:57344\n"
"v_accvgpr_read_b32 v119,a115\n"
"v_mfma_f32_16x16x32_bf16 v[176:179],v[32:35],v[8:11],v[176:179]\n"
"ds_read_b64_tr_b16 a[46:47],v103 offset:58368\n"
"v_accvgpr_read_b32 v120,a116\n"
"v_mfma_f32_16x16x32_bf16 v[208:211],v[32:35],v[16:19],v[208:211]\n"
"buffer_load_dwordx4 v112,s[24:27],s76 offen offset:2048 sc1 lds\n"
"v_mfma_f32_16x16x32_bf16 v[208:211],v[36:39],v[20:23],v[208:211]\n"
"ds_read_b128 a[0:3],v97 offset:0\n"
"v_accvgpr_read_b32 v121,a117\n"
"v_mfma_f32_16x16x32_bf16 v[240:243],v[36:39],v[28:31],v[240:243]\n"
"v_accvgpr_read_b32 v122,a118\n"
"v_accvgpr_read_b32 v123,a119\n"
"v_mfma_f32_16x16x32_bf16 v[240:243],v[32:35],v[24:27],v[240:243]\n"
"ds_read_b128 a[8:11],v97 offset:1024\n"
"v_cvt_pk_bf16_f32 v116,v116,v117\n"
"v_mfma_f32_16x16x32_bf16 v[244:247],v[40:43],v[24:27],v[244:247]\n"
"ds_read_b64_tr_b16 v[32:33],v104 offset:49152\n"
"v_cvt_pk_bf16_f32 v117,v118,v119\n"
"v_mfma_f32_16x16x32_bf16 v[244:247],v[44:47],v[28:31],v[244:247]\n"
"ds_read_b64_tr_b16 v[34:35],v104 offset:50176\n"
"v_cvt_pk_bf16_f32 v118,v120,v121\n"
"v_mfma_f32_16x16x32_bf16 v[212:215],v[44:47],v[20:23],v[212:215]\n"
"ds_read_b64_tr_b16 v[36:37],v104 offset:57344\n"
"v_cvt_pk_bf16_f32 v119,v122,v123\n"
"v_mfma_f32_16x16x32_bf16 v[212:215],v[40:43],v[16:19],v[212:215]\n"
"ds_read_b64_tr_b16 v[38:39],v104 offset:58368\n"
"buffer_store_dwordx4 v[116:119],v112,s[24:27],s56 offen offset:2048 sc1\n"
"v_mfma_f32_16x16x32_bf16 v[180:183],v[40:43],v[8:11],v[180:183]\n"
"buffer_load_dwordx4 v112,s[24:27],s76 offen offset:3072 sc1 lds\n"
"v_mfma_f32_16x16x32_bf16 v[180:183],v[44:47],v[12:15],v[180:183]\n"
"s_add_u32 m0,s78,49152\n"
"s_xor_b32 s76,s76,16384\n"
"ds_read_b128 a[16:19],v97 offset:2048\n"
"v_mfma_f32_16x16x32_bf16 v[148:151],v[44:47],v[4:7],v[148:151]\n"
"buffer_load_dwordx4 v112,s[24:27],s76 offen offset:0 sc1 lds\n"
"v_mfma_f32_16x16x32_bf16 v[148:151],v[40:43],v[0:3],v[148:151]\n"
"ds_read_b128 a[24:27],v97 offset:3072\n"
"v_accvgpr_read_b32 v116,a120\n"
"v_mfma_f32_16x16x32_bf16 v[152:155],v[48:51],v[0:3],v[152:155]\n"
"ds_read_b64_tr_b16 v[40:41],v105 offset:49152\n"
"v_accvgpr_read_b32 v117,a121\n"
"v_mfma_f32_16x16x32_bf16 v[152:155],v[52:55],v[4:7],v[152:155]\n"
"ds_read_b64_tr_b16 v[42:43],v105 offset:50176\n"
"v_accvgpr_read_b32 v118,a122\n"
"v_mfma_f32_16x16x32_bf16 v[184:187],v[52:55],v[12:15],v[184:187]\n"
"ds_read_b64_tr_b16 v[44:45],v105 offset:57344\n"
"v_accvgpr_read_b32 v119,a123\n"
"v_mfma_f32_16x16x32_bf16 v[184:187],v[48:51],v[8:11],v[184:187]\n"
"ds_read_b64_tr_b16 v[46:47],v105 offset:58368\n"
"v_accvgpr_read_b32 v120,a124\n"
"v_mfma_f32_16x16x32_bf16 v[216:219],v[48:51],v[16:19],v[216:219]\n"
"ds_read_b128 a[4:7],v97 offset:16384\n"
"v_accvgpr_read_b32 v121,a125\n"
"v_mfma_f32_16x16x32_bf16 v[216:219],v[52:55],v[20:23],v[216:219]\n"
"ds_read_b128 a[12:15],v97 offset:17408\n"
"v_accvgpr_read_b32 v122,a126\n"
"v_mfma_f32_16x16x32_bf16 v[248:251],v[52:55],v[28:31],v[248:251]\n"
"ds_read_b128 a[20:23],v97 offset:18432\n"
"v_accvgpr_read_b32 v123,a127\n"
"v_mfma_f32_16x16x32_bf16 v[248:251],v[48:51],v[24:27],v[248:251]\n"
"ds_read_b128 a[28:31],v97 offset:19456\n"
"v_cvt_pk_bf16_f32 v116,v116,v117\n"
"v_mfma_f32_16x16x32_bf16 v[252:255],v[56:59],v[24:27],v[252:255]\n"
"buffer_load_dwordx4 v112,s[24:27],s76 offen offset:1024 sc1 lds\n"
"v_mfma_f32_16x16x32_bf16 v[252:255],v[60:63],v[28:31],v[252:255]\n"
"v_cvt_pk_bf16_f32 v117,v118,v119\n"
"v_cvt_pk_bf16_f32 v118,v120,v121\n"
"v_cvt_pk_bf16_f32 v119,v122,v123\n"
"v_mfma_f32_16x16x32_bf16 v[220:223],v[60:63],v[20:23],v[220:223]\n"
"buffer_store_dwordx4 v[116:119],v112,s[24:27],s56 offen offset:3072 sc1\n"
"v_mfma_f32_16x16x32_bf16 v[220:223],v[56:59],v[16:19],v[220:223]\n"
"buffer_load_dwordx4 v112,s[24:27],s76 offen offset:2048 sc1 lds\n"
"v_mfma_f32_16x16x32_bf16 v[188:191],v[56:59],v[8:11],v[188:191]\n"
"v_accvgpr_read_b32 v116,a128\n"
"v_accvgpr_read_b32 v117,a129\n"
"v_mfma_f32_16x16x32_bf16 v[188:191],v[60:63],v[12:15],v[188:191]\n"
"v_accvgpr_read_b32 v118,a130\n"
"v_accvgpr_read_b32 v119,a131\n"
"v_mfma_f32_16x16x32_bf16 v[156:159],v[60:63],v[4:7],v[156:159]\n"
"buffer_load_dwordx4 v112,s[24:27],s76 offen offset:3072 sc1 lds\n"
"v_mfma_f32_16x16x32_bf16 v[156:159],v[56:59],v[0:3],v[156:159]\n"
"s_xor_b32 s76,s76,114688\n"
"v_accvgpr_read_b32 v120,a132\n"
"s_waitcnt vmcnt(28) lgkmcnt(0)\n"
"s_barrier\n"
"v_mfma_f32_16x16x32_bf16 v[128:131],v[64:67],a[0:3],v[128:131]\n"
"ds_read_b64_tr_b16 v[48:49],v98 offset:32768\n"
"ds_read_b64_tr_b16 v[56:57],v99 offset:32768\n"
"v_mfma_f32_16x16x32_bf16 v[128:131],v[68:71],a[4:7],v[128:131]\n"
"ds_read_b64_tr_b16 v[50:51],v98 offset:33792\n"
"ds_read_b64_tr_b16 v[58:59],v99 offset:33792\n"
"v_mfma_f32_16x16x32_bf16 v[160:163],v[68:71],a[12:15],v[160:163]\n"
"ds_read_b64_tr_b16 v[52:53],v98 offset:40960\n"
"ds_read_b64_tr_b16 v[60:61],v99 offset:40960\n"
"v_mfma_f32_16x16x32_bf16 v[160:163],v[64:67],a[8:11],v[160:163]\n"
"s_add_u32 m0,s85,147456\n"
"ds_read_b64_tr_b16 v[54:55],v98 offset:41984\n"
"ds_read_b64_tr_b16 v[62:63],v99 offset:41984\n"
"v_mfma_f32_16x16x32_bf16 v[192:195],v[64:67],a[16:19],v[192:195]\n"
"buffer_load_dwordx4 v110,s[28:31],s86 offen lds\n"
"v_mfma_f32_16x16x32_bf16 v[192:195],v[68:71],a[20:23],v[192:195]\n"
"v_accvgpr_read_b32 v121,a133\n"
"v_accvgpr_read_b32 v122,a134\n"
"v_mfma_f32_16x16x32_bf16 v[224:227],v[68:71],a[28:31],v[224:227]\n"
"v_accvgpr_read_b32 v123,a135\n"
"v_cvt_pk_bf16_f32 v116,v116,v117\n"
"v_mfma_f32_16x16x32_bf16 v[224:227],v[64:67],a[24:27],v[224:227]\n"
"v_cvt_pk_bf16_f32 v117,v118,v119\n"
"v_cvt_pk_bf16_f32 v118,v120,v121\n"
"v_mfma_f32_16x16x32_bf16 v[228:231],v[72:75],a[24:27],v[228:231]\n"
"ds_read_b64_tr_b16 v[64:65],v100 offset:32768\n"
"v_cvt_pk_bf16_f32 v119,v122,v123\n"
"v_mfma_f32_16x16x32_bf16 v[228:231],v[76:79],a[28:31],v[228:231]\n"
"ds_read_b64_tr_b16 v[66:67],v100 offset:33792\n"
"s_add_u32 s56,s56,126976\n"
"v_mfma_f32_16x16x32_bf16 v[196:199],v[76:79],a[20:23],v[196:199]\n"
"ds_read_b64_tr_b16 v[68:69],v100 offset:40960\n"
"buffer_store_dwordx4 v[116:119],v112,s[24:27],s56 offen offset:0 sc1\n"
"v_mfma_f32_16x16x32_bf16 v[196:199],v[72:75],a[16:19],v[196:199]\n"
"s_add_u32 m0,s85,148480\n"
"ds_read_b64_tr_b16 v[70:71],v100 offset:41984\n"
"v_mfma_f32_16x16x32_bf16 v[164:167],v[72:75],a[8:11],v[164:167]\n"
"buffer_load_dwordx4 v111,s[28:31],s86 offen lds\n"
"v_mfma_f32_16x16x32_bf16 v[164:167],v[76:79],a[12:15],v[164:167]\n"
"s_add_u32 m0,s85,155648\n"
"s_xor_b32 s86,s86,262144\n"
"v_mfma_f32_16x16x32_bf16 v[132:135],v[76:79],a[4:7],v[132:135]\n"
"buffer_load_dwordx4 v110,s[28:31],s86 offen lds\n"
"v_mfma_f32_16x16x32_bf16 v[132:135],v[72:75],a[0:3],v[132:135]\n"
"v_accvgpr_read_b32 v116,a136\n"
"v_accvgpr_read_b32 v117,a137\n"
"v_mfma_f32_16x16x32_bf16 v[136:139],v[80:83],a[0:3],v[136:139]\n"
"ds_read_b64_tr_b16 v[72:73],v101 offset:32768\n"
"v_accvgpr_read_b32 v118,a138\n"
"v_mfma_f32_16x16x32_bf16 v[136:139],v[84:87],a[4:7],v[136:139]\n"
"ds_read_b64_tr_b16 v[74:75],v101 offset:33792\n"
"v_accvgpr_read_b32 v119,a139\n"
"v_mfma_f32_16x16x32_bf16 v[168:171],v[84:87],a[12:15],v[168:171]\n"
"ds_read_b64_tr_b16 v[76:77],v101 offset:40960\n"
"v_accvgpr_read_b32 v120,a140\n"
"v_mfma_f32_16x16x32_bf16 v[168:171],v[80:83],a[8:11],v[168:171]\n"
"s_add_u32 m0,s85,156672\n"
"ds_read_b64_tr_b16 v[78:79],v101 offset:41984\n"
"v_mfma_f32_16x16x32_bf16 v[200:203],v[80:83],a[16:19],v[200:203]\n"
"buffer_load_dwordx4 v111,s[28:31],s86 offen lds\n"
"v_mfma_f32_16x16x32_bf16 v[200:203],v[84:87],a[20:23],v[200:203]\n"
"s_xor_b32 s86,s86,786432\n"
"v_accvgpr_read_b32 v121,a141\n"
"v_mfma_f32_16x16x32_bf16 v[232:235],v[84:87],a[28:31],v[232:235]\n"
"v_accvgpr_read_b32 v122,a142\n"
"v_accvgpr_read_b32 v123,a143\n"
"v_mfma_f32_16x16x32_bf16 v[232:235],v[80:83],a[24:27],v[232:235]\n"
"v_cvt_pk_bf16_f32 v116,v116,v117\n"
"v_cvt_pk_bf16_f32 v117,v118,v119\n"
"v_mfma_f32_16x16x32_bf16 v[236:239],v[88:91],a[24:27],v[236:239]\n"
"ds_read_b64_tr_b16 v[80:81],v102 offset:32768\n"
"v_cvt_pk_bf16_f32 v118,v120,v121\n"
"v_mfma_f32_16x16x32_bf16 v[236:239],v[92:95],a[28:31],v[236:239]\n"
"ds_read_b64_tr_b16 v[82:83],v102 offset:33792\n"
"v_cvt_pk_bf16_f32 v119,v122,v123\n"
"v_mfma_f32_16x16x32_bf16 v[204:207],v[92:95],a[20:23],v[204:207]\n"
"ds_read_b64_tr_b16 v[84:85],v102 offset:40960\n"
"buffer_store_dwordx4 v[116:119],v112,s[24:27],s56 offen offset:1024 sc1\n"
"v_mfma_f32_16x16x32_bf16 v[204:207],v[88:91],a[16:19],v[204:207]\n"
"s_add_u32 m0,s78,65536\n"
"ds_read_b64_tr_b16 v[86:87],v102 offset:41984\n"
"v_accvgpr_read_b32 v118,a146\n"
"v_mfma_f32_16x16x32_bf16 v[172:175],v[88:91],a[8:11],v[172:175]\n"
"buffer_load_dwordx4 v112,s[24:27],s76 offen offset:0 sc1 lds\n"
"v_mfma_f32_16x16x32_bf16 v[172:175],v[92:95],a[12:15],v[172:175]\n"
"v_accvgpr_read_b32 v116,a144\n"
"v_accvgpr_read_b32 v117,a145\n"
"v_mfma_f32_16x16x32_bf16 v[140:143],v[92:95],a[4:7],v[140:143]\n"
"buffer_load_dwordx4 v112,s[24:27],s76 offen offset:1024 sc1 lds\n"
"v_mfma_f32_16x16x32_bf16 v[140:143],v[88:91],a[0:3],v[140:143]\n"
"v_accvgpr_read_b32 v119,a147\n"
"s_waitcnt vmcnt(24)\n"
"v_mfma_f32_16x16x32_bf16 v[144:147],a[32:35],a[0:3],v[144:147]\n"
"ds_read_b64_tr_b16 v[88:89],v103 offset:32768\n"
"v_accvgpr_read_b32 v120,a148\n"
"v_mfma_f32_16x16x32_bf16 v[144:147],a[36:39],a[4:7],v[144:147]\n"
"ds_read_b64_tr_b16 v[90:91],v103 offset:33792\n"
"v_accvgpr_read_b32 v121,a149\n"
"v_mfma_f32_16x16x32_bf16 v[176:179],a[36:39],a[12:15],v[176:179]\n"
"ds_read_b64_tr_b16 v[92:93],v103 offset:40960\n"
"v_accvgpr_read_b32 v122,a150\n"
"v_mfma_f32_16x16x32_bf16 v[176:179],a[32:35],a[8:11],v[176:179]\n"
"ds_read_b64_tr_b16 v[94:95],v103 offset:41984\n"
"v_accvgpr_read_b32 v123,a151\n"
"v_mfma_f32_16x16x32_bf16 v[208:211],a[32:35],a[16:19],v[208:211]\n"
"buffer_load_dwordx4 v112,s[24:27],s76 offen offset:2048 sc1 lds\n"
"v_mfma_f32_16x16x32_bf16 v[208:211],a[36:39],a[20:23],v[208:211]\n"
"ds_read_b128 v[0:3],v96 offset:0\n"
"v_cvt_pk_bf16_f32 v116,v116,v117\n"
"v_mfma_f32_16x16x32_bf16 v[240:243],a[36:39],a[28:31],v[240:243]\n"
"v_cvt_pk_bf16_f32 v117,v118,v119\n"
"v_cvt_pk_bf16_f32 v118,v120,v121\n"
"v_mfma_f32_16x16x32_bf16 v[240:243],a[32:35],a[24:27],v[240:243]\n"
"ds_read_b128 v[8:11],v96 offset:1024\n"
"v_cvt_pk_bf16_f32 v119,v122,v123\n"
"v_mfma_f32_16x16x32_bf16 v[244:247],a[40:43],a[24:27],v[244:247]\n"
"ds_read_b64_tr_b16 a[32:33],v104 offset:32768\n"
"buffer_store_dwordx4 v[116:119],v112,s[24:27],s56 offen offset:2048 sc1\n"
"v_mfma_f32_16x16x32_bf16 v[244:247],a[44:47],a[28:31],v[244:247]\n"
"ds_read_b64_tr_b16 a[34:35],v104 offset:33792\n"
"v_accvgpr_read_b32 v116,a152\n"
"v_mfma_f32_16x16x32_bf16 v[212:215],a[44:47],a[20:23],v[212:215]\n"
"ds_read_b64_tr_b16 a[36:37],v104 offset:40960\n"
"v_accvgpr_read_b32 v117,a153\n"
"v_mfma_f32_16x16x32_bf16 v[212:215],a[40:43],a[16:19],v[212:215]\n"
"ds_read_b64_tr_b16 a[38:39],v104 offset:41984\n"
"v_accvgpr_read_b32 v118,a154\n"
"v_mfma_f32_16x16x32_bf16 v[180:183],a[40:43],a[8:11],v[180:183]\n"
"buffer_load_dwordx4 v112,s[24:27],s76 offen offset:3072 sc1 lds\n"
"v_mfma_f32_16x16x32_bf16 v[180:183],a[44:47],a[12:15],v[180:183]\n"
"s_add_u32 m0,s78,81920\n"
"s_xor_b32 s76,s76,16384\n"
"ds_read_b128 v[16:19],v96 offset:2048\n"
"v_mfma_f32_16x16x32_bf16 v[148:151],a[44:47],a[4:7],v[148:151]\n"
"buffer_load_dwordx4 v112,s[24:27],s76 offen offset:0 sc1 lds\n"
"v_mfma_f32_16x16x32_bf16 v[148:151],a[40:43],a[0:3],v[148:151]\n"
"ds_read_b128 v[24:27],v96 offset:3072\n"
"v_accvgpr_read_b32 v119,a155\n"
"v_mfma_f32_16x16x32_bf16 v[152:155],v[32:35],a[0:3],v[152:155]\n"
"ds_read_b64_tr_b16 a[40:41],v105 offset:32768\n"
"v_accvgpr_read_b32 v120,a156\n"
"v_mfma_f32_16x16x32_bf16 v[152:155],v[36:39],a[4:7],v[152:155]\n"
"ds_read_b64_tr_b16 a[42:43],v105 offset:33792\n"
"v_accvgpr_read_b32 v121,a157\n"
"v_mfma_f32_16x16x32_bf16 v[184:187],v[36:39],a[12:15],v[184:187]\n"
"ds_read_b64_tr_b16 a[44:45],v105 offset:40960\n"
"v_accvgpr_read_b32 v122,a158\n"
"v_mfma_f32_16x16x32_bf16 v[184:187],v[32:35],a[8:11],v[184:187]\n"
"ds_read_b64_tr_b16 a[46:47],v105 offset:41984\n"
"v_accvgpr_read_b32 v123,a159\n"
"v_mfma_f32_16x16x32_bf16 v[216:219],v[32:35],a[16:19],v[216:219]\n"
"ds_read_b128 v[4:7],v96 offset:16384\n"
"v_cvt_pk_bf16_f32 v116,v116,v117\n"
"v_mfma_f32_16x16x32_bf16 v[216:219],v[36:39],a[20:23],v[216:219]\n"
"ds_read_b128 v[12:15],v96 offset:17408\n"
"v_cvt_pk_bf16_f32 v117,v118,v119\n"
"v_mfma_f32_16x16x32_bf16 v[248:251],v[36:39],a[28:31],v[248:251]\n"
"ds_read_b128 v[20:23],v96 offset:18432\n"
"v_cvt_pk_bf16_f32 v118,v120,v121\n"
"v_mfma_f32_16x16x32_bf16 v[248:251],v[32:35],a[24:27],v[248:251]\n"
"ds_read_b128 v[28:31],v96 offset:19456\n"
"v_cvt_pk_bf16_f32 v119,v122,v123\n"
"v_mfma_f32_16x16x32_bf16 v[252:255],v[40:43],a[24:27],v[252:255]\n"
"buffer_load_dwordx4 v112,s[24:27],s76 offen offset:1024 sc1 lds\n"
"buffer_store_dwordx4 v[116:119],v112,s[24:27],s56 offen offset:3072 sc1\n"
"v_mfma_f32_16x16x32_bf16 v[252:255],v[44:47],a[28:31],v[252:255]\n"
"s_nop 0\n"
"v_accvgpr_read_b32 v116,a160\n"
"v_mfma_f32_16x16x32_bf16 v[220:223],v[44:47],a[20:23],v[220:223]\n"
"v_accvgpr_read_b32 v117,a161\n"
"v_accvgpr_read_b32 v118,a162\n"
"v_mfma_f32_16x16x32_bf16 v[220:223],v[40:43],a[16:19],v[220:223]\n"
"buffer_load_dwordx4 v112,s[24:27],s76 offen offset:2048 sc1 lds\n"
"v_mfma_f32_16x16x32_bf16 v[188:191],v[40:43],a[8:11],v[188:191]\n"
"v_accvgpr_read_b32 v119,a163\n"
"v_accvgpr_read_b32 v120,a164\n"
"v_mfma_f32_16x16x32_bf16 v[188:191],v[44:47],a[12:15],v[188:191]\n"
"v_accvgpr_read_b32 v121,a165\n"
"v_accvgpr_read_b32 v122,a166\n"
"v_mfma_f32_16x16x32_bf16 v[156:159],v[44:47],a[4:7],v[156:159]\n"
"buffer_load_dwordx4 v112,s[24:27],s76 offen offset:3072 sc1 lds\n"
"v_mfma_f32_16x16x32_bf16 v[156:159],v[40:43],a[0:3],v[156:159]\n"
"s_xor_b32 s76,s76,49152\n"
"v_accvgpr_read_b32 v123,a167\n"
"s_waitcnt vmcnt(27) lgkmcnt(0)\n"
"s_barrier\n"
"v_mfma_f32_16x16x32_bf16 v[128:131],v[48:51],v[0:3],v[128:131]\n"
"ds_read_b64_tr_b16 v[32:33],v98 offset:0\n"
"ds_read_b64_tr_b16 v[40:41],v99 offset:0\n"
"v_mfma_f32_16x16x32_bf16 v[128:131],v[52:55],v[4:7],v[128:131]\n"
"ds_read_b64_tr_b16 v[34:35],v98 offset:1024\n"
"ds_read_b64_tr_b16 v[42:43],v99 offset:1024\n"
"v_mfma_f32_16x16x32_bf16 v[160:163],v[52:55],v[12:15],v[160:163]\n"
"ds_read_b64_tr_b16 v[36:37],v98 offset:8192\n"
"ds_read_b64_tr_b16 v[44:45],v99 offset:8192\n"
"v_mfma_f32_16x16x32_bf16 v[160:163],v[48:51],v[8:11],v[160:163]\n"
"s_add_u32 m0,s85,131072\n"
"ds_read_b64_tr_b16 v[38:39],v98 offset:9216\n"
"ds_read_b64_tr_b16 v[46:47],v99 offset:9216\n"
"v_mfma_f32_16x16x32_bf16 v[192:195],v[48:51],v[16:19],v[192:195]\n"
"buffer_load_dwordx4 v110,s[28:31],s86 offen lds\n"
"v_mfma_f32_16x16x32_bf16 v[192:195],v[52:55],v[20:23],v[192:195]\n"
"v_cvt_pk_bf16_f32 v116,v116,v117\n"
"v_cvt_pk_bf16_f32 v117,v118,v119\n"
"v_mfma_f32_16x16x32_bf16 v[224:227],v[52:55],v[28:31],v[224:227]\n"
"s_add_u32 s56,s56,4096\n"
"v_cvt_pk_bf16_f32 v118,v120,v121\n"
"v_cvt_pk_bf16_f32 v119,v122,v123\n"
"v_mfma_f32_16x16x32_bf16 v[224:227],v[48:51],v[24:27],v[224:227]\n"
"buffer_store_dwordx4 v[116:119],v112,s[24:27],s56 offen offset:0 sc1\n"
"v_mfma_f32_16x16x32_bf16 v[228:231],v[56:59],v[24:27],v[228:231]\n"
"ds_read_b64_tr_b16 v[48:49],v100 offset:0\n"
"v_accvgpr_read_b32 v116,a168\n"
"v_mfma_f32_16x16x32_bf16 v[228:231],v[60:63],v[28:31],v[228:231]\n"
"ds_read_b64_tr_b16 v[50:51],v100 offset:1024\n"
"v_accvgpr_read_b32 v117,a169\n"
"v_mfma_f32_16x16x32_bf16 v[196:199],v[60:63],v[20:23],v[196:199]\n"
"ds_read_b64_tr_b16 v[52:53],v100 offset:8192\n"
"v_accvgpr_read_b32 v118,a170\n"
"v_mfma_f32_16x16x32_bf16 v[196:199],v[56:59],v[16:19],v[196:199]\n"
"s_add_u32 m0,s85,132096\n"
"ds_read_b64_tr_b16 v[54:55],v100 offset:9216\n"
"v_mfma_f32_16x16x32_bf16 v[164:167],v[56:59],v[8:11],v[164:167]\n"
"buffer_load_dwordx4 v111,s[28:31],s86 offen lds\n"
"v_mfma_f32_16x16x32_bf16 v[164:167],v[60:63],v[12:15],v[164:167]\n"
"s_add_u32 m0,s85,139264\n"
"s_xor_b32 s86,s86,262144\n"
"v_mfma_f32_16x16x32_bf16 v[132:135],v[60:63],v[4:7],v[132:135]\n"
"buffer_load_dwordx4 v110,s[28:31],s86 offen lds\n"
"v_mfma_f32_16x16x32_bf16 v[132:135],v[56:59],v[0:3],v[132:135]\n"
"v_accvgpr_read_b32 v119,a171\n"
"v_accvgpr_read_b32 v120,a172\n"
"v_mfma_f32_16x16x32_bf16 v[136:139],v[64:67],v[0:3],v[136:139]\n"
"ds_read_b64_tr_b16 v[56:57],v101 offset:0\n"
"v_accvgpr_read_b32 v121,a173\n"
"v_mfma_f32_16x16x32_bf16 v[136:139],v[68:71],v[4:7],v[136:139]\n"
"ds_read_b64_tr_b16 v[58:59],v101 offset:1024\n"
"v_accvgpr_read_b32 v122,a174\n"
"v_mfma_f32_16x16x32_bf16 v[168:171],v[68:71],v[12:15],v[168:171]\n"
"ds_read_b64_tr_b16 v[60:61],v101 offset:8192\n"
"v_accvgpr_read_b32 v123,a175\n"
"v_mfma_f32_16x16x32_bf16 v[168:171],v[64:67],v[8:11],v[168:171]\n"
"s_add_u32 m0,s85,140288\n"
"ds_read_b64_tr_b16 v[62:63],v101 offset:9216\n"
"v_mfma_f32_16x16x32_bf16 v[200:203],v[64:67],v[16:19],v[200:203]\n"
"buffer_load_dwordx4 v111,s[28:31],s86 offen lds\n"
"v_mfma_f32_16x16x32_bf16 v[200:203],v[68:71],v[20:23],v[200:203]\n"
"s_xor_b32 s86,s86,8126464\n"
"v_cvt_pk_bf16_f32 v116,v116,v117\n"
"v_cvt_pk_bf16_f32 v117,v118,v119\n"
"v_mfma_f32_16x16x32_bf16 v[232:235],v[68:71],v[28:31],v[232:235]\n"
"v_cvt_pk_bf16_f32 v118,v120,v121\n"
"v_cvt_pk_bf16_f32 v119,v122,v123\n"
"v_mfma_f32_16x16x32_bf16 v[232:235],v[64:67],v[24:27],v[232:235]\n"
"buffer_store_dwordx4 v[116:119],v112,s[24:27],s56 offen offset:1024 sc1\n"
"v_mfma_f32_16x16x32_bf16 v[236:239],v[72:75],v[24:27],v[236:239]\n"
"ds_read_b64_tr_b16 v[64:65],v102 offset:0\n"
"v_accvgpr_read_b32 v116,a176\n"
"v_mfma_f32_16x16x32_bf16 v[236:239],v[76:79],v[28:31],v[236:239]\n"
"ds_read_b64_tr_b16 v[66:67],v102 offset:1024\n"
"v_accvgpr_read_b32 v117,a177\n"
"v_mfma_f32_16x16x32_bf16 v[204:207],v[76:79],v[20:23],v[204:207]\n"
"ds_read_b64_tr_b16 v[68:69],v102 offset:8192\n"
"v_accvgpr_read_b32 v118,a178\n"
"v_mfma_f32_16x16x32_bf16 v[204:207],v[72:75],v[16:19],v[204:207]\n"
"s_add_u32 m0,s78,0\n"
"ds_read_b64_tr_b16 v[70:71],v102 offset:9216\n"
"v_accvgpr_read_b32 v121,a181\n"
"v_mfma_f32_16x16x32_bf16 v[172:175],v[72:75],v[8:11],v[172:175]\n"
"buffer_load_dwordx4 v112,s[24:27],s76 offen offset:0 sc1 lds\n"
"v_mfma_f32_16x16x32_bf16 v[172:175],v[76:79],v[12:15],v[172:175]\n"
"v_accvgpr_read_b32 v119,a179\n"
"v_accvgpr_read_b32 v120,a180\n"
"v_mfma_f32_16x16x32_bf16 v[140:143],v[76:79],v[4:7],v[140:143]\n"
"buffer_load_dwordx4 v112,s[24:27],s76 offen offset:1024 sc1 lds\n"
"v_mfma_f32_16x16x32_bf16 v[140:143],v[72:75],v[0:3],v[140:143]\n"
"v_accvgpr_read_b32 v122,a182\n"
"s_waitcnt vmcnt(24)\n"
"v_mfma_f32_16x16x32_bf16 v[144:147],v[80:83],v[0:3],v[144:147]\n"
"ds_read_b64_tr_b16 v[72:73],v103 offset:0\n"
"v_accvgpr_read_b32 v123,a183\n"
"v_mfma_f32_16x16x32_bf16 v[144:147],v[84:87],v[4:7],v[144:147]\n"
"ds_read_b64_tr_b16 v[74:75],v103 offset:1024\n"
"v_cvt_pk_bf16_f32 v116,v116,v117\n"
"v_mfma_f32_16x16x32_bf16 v[176:179],v[84:87],v[12:15],v[176:179]\n"
"ds_read_b64_tr_b16 v[76:77],v103 offset:8192\n"
"v_cvt_pk_bf16_f32 v117,v118,v119\n"
"v_mfma_f32_16x16x32_bf16 v[176:179],v[80:83],v[8:11],v[176:179]\n"
"ds_read_b64_tr_b16 v[78:79],v103 offset:9216\n"
"v_cvt_pk_bf16_f32 v118,v120,v121\n"
"v_mfma_f32_16x16x32_bf16 v[208:211],v[80:83],v[16:19],v[208:211]\n"
"buffer_load_dwordx4 v112,s[24:27],s76 offen offset:2048 sc1 lds\n"
"v_mfma_f32_16x16x32_bf16 v[208:211],v[84:87],v[20:23],v[208:211]\n"
"ds_read_b128 a[0:3],v96 offset:32768\n"
"v_cvt_pk_bf16_f32 v119,v122,v123\n"
"v_mfma_f32_16x16x32_bf16 v[240:243],v[84:87],v[28:31],v[240:243]\n"
"buffer_store_dwordx4 v[116:119],v112,s[24:27],s56 offen offset:2048 sc1\n"
"v_mfma_f32_16x16x32_bf16 v[240:243],v[80:83],v[24:27],v[240:243]\n"
"ds_read_b128 a[8:11],v96 offset:33792\n"
"v_accvgpr_read_b32 v116,a184\n"
"v_accvgpr_read_b32 v117,a185\n"
"v_mfma_f32_16x16x32_bf16 v[244:247],v[88:91],v[24:27],v[244:247]\n"
"ds_read_b64_tr_b16 v[80:81],v104 offset:0\n"
"v_accvgpr_read_b32 v118,a186\n"
"v_mfma_f32_16x16x32_bf16 v[244:247],v[92:95],v[28:31],v[244:247]\n"
"ds_read_b64_tr_b16 v[82:83],v104 offset:1024\n"
"v_accvgpr_read_b32 v119,a187\n"
"v_mfma_f32_16x16x32_bf16 v[212:215],v[92:95],v[20:23],v[212:215]\n"
"ds_read_b64_tr_b16 v[84:85],v104 offset:8192\n"
"v_accvgpr_read_b32 v120,a188\n"
"v_mfma_f32_16x16x32_bf16 v[212:215],v[88:91],v[16:19],v[212:215]\n"
"ds_read_b64_tr_b16 v[86:87],v104 offset:9216\n"
"v_accvgpr_read_b32 v121,a189\n"
"v_mfma_f32_16x16x32_bf16 v[180:183],v[88:91],v[8:11],v[180:183]\n"
"buffer_load_dwordx4 v112,s[24:27],s76 offen offset:3072 sc1 lds\n"
"v_mfma_f32_16x16x32_bf16 v[180:183],v[92:95],v[12:15],v[180:183]\n"
"s_add_u32 m0,s78,16384\n"
"s_xor_b32 s76,s76,16384\n"
"ds_read_b128 a[16:19],v96 offset:34816\n"
"v_mfma_f32_16x16x32_bf16 v[148:151],v[92:95],v[4:7],v[148:151]\n"
"buffer_load_dwordx4 v112,s[24:27],s76 offen offset:0 sc1 lds\n"
"v_mfma_f32_16x16x32_bf16 v[148:151],v[88:91],v[0:3],v[148:151]\n"
"ds_read_b128 a[24:27],v96 offset:35840\n"
"v_accvgpr_read_b32 v122,a190\n"
"v_mfma_f32_16x16x32_bf16 v[152:155],a[32:35],v[0:3],v[152:155]\n"
"ds_read_b64_tr_b16 v[88:89],v105 offset:0\n"
"v_accvgpr_read_b32 v123,a191\n"
"v_mfma_f32_16x16x32_bf16 v[152:155],a[36:39],v[4:7],v[152:155]\n"
"ds_read_b64_tr_b16 v[90:91],v105 offset:1024\n"
"v_cvt_pk_bf16_f32 v116,v116,v117\n"
"v_mfma_f32_16x16x32_bf16 v[184:187],a[36:39],v[12:15],v[184:187]\n"
"ds_read_b64_tr_b16 v[92:93],v105 offset:8192\n"
"v_cvt_pk_bf16_f32 v117,v118,v119\n"
"v_mfma_f32_16x16x32_bf16 v[184:187],a[32:35],v[8:11],v[184:187]\n"
"ds_read_b64_tr_b16 v[94:95],v105 offset:9216\n"
"v_cvt_pk_bf16_f32 v118,v120,v121\n"
"v_mfma_f32_16x16x32_bf16 v[216:219],a[32:35],v[16:19],v[216:219]\n"
"ds_read_b128 a[4:7],v96 offset:49152\n"
"v_cvt_pk_bf16_f32 v119,v122,v123\n"
"v_mfma_f32_16x16x32_bf16 v[216:219],a[36:39],v[20:23],v[216:219]\n"
"ds_read_b128 a[12:15],v96 offset:50176\n"
"buffer_store_dwordx4 v[116:119],v112,s[24:27],s56 offen offset:3072 sc1\n"
"v_mfma_f32_16x16x32_bf16 v[248:251],a[36:39],v[28:31],v[248:251]\n"
"ds_read_b128 a[20:23],v96 offset:51200\n"
"v_accvgpr_read_b32 v116,a248\n"
"v_mfma_f32_16x16x32_bf16 v[248:251],a[32:35],v[24:27],v[248:251]\n"
"ds_read_b128 a[28:31],v96 offset:52224\n"
"v_accvgpr_read_b32 v117,a249\n"
"v_mfma_f32_16x16x32_bf16 v[252:255],a[40:43],v[24:27],v[252:255]\n"
"buffer_load_dwordx4 v112,s[24:27],s76 offen offset:1024 sc1 lds\n"
"v_mfma_f32_16x16x32_bf16 v[252:255],a[44:47],v[28:31],v[252:255]\n"
"v_accvgpr_read_b32 v118,a250\n"
"v_accvgpr_read_b32 v119,a251\n"
"v_mfma_f32_16x16x32_bf16 v[220:223],a[44:47],v[20:23],v[220:223]\n"
"v_accvgpr_read_b32 v120,a252\n"
"v_accvgpr_read_b32 v121,a253\n"
"v_mfma_f32_16x16x32_bf16 v[220:223],a[40:43],v[16:19],v[220:223]\n"
"buffer_load_dwordx4 v112,s[24:27],s76 offen offset:2048 sc1 lds\n"
"v_mfma_f32_16x16x32_bf16 v[188:191],a[40:43],v[8:11],v[188:191]\n"
"v_accvgpr_read_b32 v122,a254\n"
"v_accvgpr_read_b32 v123,a255\n"
"v_mfma_f32_16x16x32_bf16 v[188:191],a[44:47],v[12:15],v[188:191]\n"
"v_cvt_pk_bf16_f32 v116,v116,v117\n"
"v_cvt_pk_bf16_f32 v117,v118,v119\n"
"v_mfma_f32_16x16x32_bf16 v[156:159],a[44:47],v[4:7],v[156:159]\n"
"buffer_load_dwordx4 v112,s[24:27],s76 offen offset:3072 sc1 lds\n"
"v_mfma_f32_16x16x32_bf16 v[156:159],a[40:43],v[0:3],v[156:159]\n"
"s_xor_b32 s76,s76,507904\n"
"v_cvt_pk_bf16_f32 v118,v120,v121\n"
"s_waitcnt vmcnt(27) lgkmcnt(0)\n"
"s_barrier\n"
"v_mfma_f32_16x16x32_bf16 v[128:131],v[32:35],a[0:3],v[128:131]\n"
"ds_read_b64_tr_b16 a[32:33],v98 offset:49152\n"
"ds_read_b64_tr_b16 a[40:41],v99 offset:49152\n"
"v_mfma_f32_16x16x32_bf16 v[128:131],v[36:39],a[4:7],v[128:131]\n"
"ds_read_b64_tr_b16 a[34:35],v98 offset:50176\n"
"ds_read_b64_tr_b16 a[42:43],v99 offset:50176\n"
"v_mfma_f32_16x16x32_bf16 v[160:163],v[36:39],a[12:15],v[160:163]\n"
"ds_read_b64_tr_b16 a[36:37],v98 offset:57344\n"
"ds_read_b64_tr_b16 a[44:45],v99 offset:57344\n"
"v_mfma_f32_16x16x32_bf16 v[160:163],v[32:35],a[8:11],v[160:163]\n"
"s_add_u32 m0,s85,98304\n"
"ds_read_b64_tr_b16 a[38:39],v98 offset:58368\n"
"ds_read_b64_tr_b16 a[46:47],v99 offset:58368\n"
"v_mfma_f32_16x16x32_bf16 v[192:195],v[32:35],a[16:19],v[192:195]\n"
"buffer_load_dwordx4 v110,s[28:31],s86 offen lds\n"
"v_mfma_f32_16x16x32_bf16 v[192:195],v[36:39],a[20:23],v[192:195]\n"
"v_cvt_pk_bf16_f32 v119,v122,v123\n"
"v_accvgpr_write_b32 a252,v116\n"
"v_mfma_f32_16x16x32_bf16 v[224:227],v[36:39],a[28:31],v[224:227]\n"
"v_accvgpr_write_b32 a253,v117\n"
"v_accvgpr_write_b32 a254,v118\n"
"v_mfma_f32_16x16x32_bf16 v[224:227],v[32:35],a[24:27],v[224:227]\n"
"v_accvgpr_write_b32 a255,v119\n"
"v_accvgpr_read_b32 v116,a240\n"
"v_mfma_f32_16x16x32_bf16 v[228:231],v[40:43],a[24:27],v[228:231]\n"
"ds_read_b64_tr_b16 v[32:33],v100 offset:49152\n"
"v_accvgpr_read_b32 v117,a241\n"
"v_mfma_f32_16x16x32_bf16 v[228:231],v[44:47],a[28:31],v[228:231]\n"
"ds_read_b64_tr_b16 v[34:35],v100 offset:50176\n"
"v_accvgpr_read_b32 v118,a242\n"
"v_mfma_f32_16x16x32_bf16 v[196:199],v[44:47],a[20:23],v[196:199]\n"
"ds_read_b64_tr_b16 v[36:37],v100 offset:57344\n"
"v_accvgpr_read_b32 v119,a243\n"
"v_mfma_f32_16x16x32_bf16 v[196:199],v[40:43],a[16:19],v[196:199]\n"
"s_add_u32 m0,s85,99328\n"
"ds_read_b64_tr_b16 v[38:39],v100 offset:58368\n"
"v_mfma_f32_16x16x32_bf16 v[164:167],v[40:43],a[8:11],v[164:167]\n"
"buffer_load_dwordx4 v111,s[28:31],s86 offen lds\n"
"v_mfma_f32_16x16x32_bf16 v[164:167],v[44:47],a[12:15],v[164:167]\n"
"s_add_u32 m0,s85,106496\n"
"s_xor_b32 s86,s86,262144\n"
"v_mfma_f32_16x16x32_bf16 v[132:135],v[44:47],a[4:7],v[132:135]\n"
"buffer_load_dwordx4 v110,s[28:31],s86 offen lds\n"
"v_mfma_f32_16x16x32_bf16 v[132:135],v[40:43],a[0:3],v[132:135]\n"
"v_accvgpr_read_b32 v120,a244\n"
"v_accvgpr_read_b32 v121,a245\n"
"v_mfma_f32_16x16x32_bf16 v[136:139],v[48:51],a[0:3],v[136:139]\n"
"ds_read_b64_tr_b16 v[40:41],v101 offset:49152\n"
"v_accvgpr_read_b32 v122,a246\n"
"v_mfma_f32_16x16x32_bf16 v[136:139],v[52:55],a[4:7],v[136:139]\n"
"ds_read_b64_tr_b16 v[42:43],v101 offset:50176\n"
"v_accvgpr_read_b32 v123,a247\n"
"v_mfma_f32_16x16x32_bf16 v[168:171],v[52:55],a[12:15],v[168:171]\n"
"ds_read_b64_tr_b16 v[44:45],v101 offset:57344\n"
"v_cvt_pk_bf16_f32 v116,v116,v117\n"
"v_mfma_f32_16x16x32_bf16 v[168:171],v[48:51],a[8:11],v[168:171]\n"
"s_add_u32 m0,s85,107520\n"
"ds_read_b64_tr_b16 v[46:47],v101 offset:58368\n"
"v_mfma_f32_16x16x32_bf16 v[200:203],v[48:51],a[16:19],v[200:203]\n"
"buffer_load_dwordx4 v111,s[28:31],s86 offen lds\n"
"v_mfma_f32_16x16x32_bf16 v[200:203],v[52:55],a[20:23],v[200:203]\n"
"s_xor_b32 s86,s86,786432\n"
"v_cvt_pk_bf16_f32 v117,v118,v119\n"
"v_mfma_f32_16x16x32_bf16 v[232:235],v[52:55],a[28:31],v[232:235]\n"
"v_cvt_pk_bf16_f32 v118,v120,v121\n"
"v_cvt_pk_bf16_f32 v119,v122,v123\n"
"v_mfma_f32_16x16x32_bf16 v[232:235],v[48:51],a[24:27],v[232:235]\n"
"v_accvgpr_write_b32 a248,v116\n"
"v_accvgpr_write_b32 a249,v117\n"
"v_mfma_f32_16x16x32_bf16 v[236:239],v[56:59],a[24:27],v[236:239]\n"
"ds_read_b64_tr_b16 v[48:49],v102 offset:49152\n"
"v_accvgpr_write_b32 a250,v118\n"
"v_mfma_f32_16x16x32_bf16 v[236:239],v[60:63],a[28:31],v[236:239]\n"
"ds_read_b64_tr_b16 v[50:51],v102 offset:50176\n"
"v_accvgpr_write_b32 a251,v119\n"
"v_mfma_f32_16x16x32_bf16 v[204:207],v[60:63],a[20:23],v[204:207]\n"
"ds_read_b64_tr_b16 v[52:53],v102 offset:57344\n"
"v_accvgpr_read_b32 v116,a232\n"
"v_mfma_f32_16x16x32_bf16 v[204:207],v[56:59],a[16:19],v[204:207]\n"
"s_add_u32 m0,s78,32768\n"
"ds_read_b64_tr_b16 v[54:55],v102 offset:58368\n"
"v_accvgpr_read_b32 v119,a235\n"
"v_mfma_f32_16x16x32_bf16 v[172:175],v[56:59],a[8:11],v[172:175]\n"
"buffer_load_dwordx4 v112,s[24:27],s76 offen offset:0 sc1 lds\n"
"v_mfma_f32_16x16x32_bf16 v[172:175],v[60:63],a[12:15],v[172:175]\n"
"v_accvgpr_read_b32 v117,a233\n"
"v_accvgpr_read_b32 v118,a234\n"
"v_mfma_f32_16x16x32_bf16 v[140:143],v[60:63],a[4:7],v[140:143]\n"
"buffer_load_dwordx4 v112,s[24:27],s76 offen offset:1024 sc1 lds\n"
"v_mfma_f32_16x16x32_bf16 v[140:143],v[56:59],a[0:3],v[140:143]\n"
"v_accvgpr_read_b32 v120,a236\n"
"s_waitcnt vmcnt(22)\n"
"v_mfma_f32_16x16x32_bf16 v[144:147],v[64:67],a[0:3],v[144:147]\n"
"ds_read_b64_tr_b16 v[56:57],v103 offset:49152\n"
"v_accvgpr_read_b32 v121,a237\n"
"v_mfma_f32_16x16x32_bf16 v[144:147],v[68:71],a[4:7],v[144:147]\n"
"ds_read_b64_tr_b16 v[58:59],v103 offset:50176\n"
"v_accvgpr_read_b32 v122,a238\n"
"v_mfma_f32_16x16x32_bf16 v[176:179],v[68:71],a[12:15],v[176:179]\n"
"ds_read_b64_tr_b16 v[60:61],v103 offset:57344\n"
"v_accvgpr_read_b32 v123,a239\n"
"v_mfma_f32_16x16x32_bf16 v[176:179],v[64:67],a[8:11],v[176:179]\n"
"ds_read_b64_tr_b16 v[62:63],v103 offset:58368\n"
"v_cvt_pk_bf16_f32 v116,v116,v117\n"
"v_mfma_f32_16x16x32_bf16 v[208:211],v[64:67],a[16:19],v[208:211]\n"
"buffer_load_dwordx4 v112,s[24:27],s76 offen offset:2048 sc1 lds\n"
"v_mfma_f32_16x16x32_bf16 v[208:211],v[68:71],a[20:23],v[208:211]\n"
"ds_read_b128 v[0:3],v97 offset:0\n"
"v_cvt_pk_bf16_f32 v117,v118,v119\n"
"v_mfma_f32_16x16x32_bf16 v[240:243],v[68:71],a[28:31],v[240:243]\n"
"v_cvt_pk_bf16_f32 v118,v120,v121\n"
"v_cvt_pk_bf16_f32 v119,v122,v123\n"
"v_mfma_f32_16x16x32_bf16 v[240:243],v[64:67],a[24:27],v[240:243]\n"
"ds_read_b128 v[8:11],v97 offset:1024\n"
"v_accvgpr_write_b32 a244,v116\n"
"v_mfma_f32_16x16x32_bf16 v[244:247],v[72:75],a[24:27],v[244:247]\n"
"ds_read_b64_tr_b16 v[64:65],v104 offset:49152\n"
"v_accvgpr_write_b32 a245,v117\n"
"v_mfma_f32_16x16x32_bf16 v[244:247],v[76:79],a[28:31],v[244:247]\n"
"ds_read_b64_tr_b16 v[66:67],v104 offset:50176\n"
"v_accvgpr_write_b32 a246,v118\n"
"v_mfma_f32_16x16x32_bf16 v[212:215],v[76:79],a[20:23],v[212:215]\n"
"ds_read_b64_tr_b16 v[68:69],v104 offset:57344\n"
"v_accvgpr_write_b32 a247,v119\n"
"v_mfma_f32_16x16x32_bf16 v[212:215],v[72:75],a[16:19],v[212:215]\n"
"ds_read_b64_tr_b16 v[70:71],v104 offset:58368\n"
"v_accvgpr_read_b32 v116,a224\n"
"v_mfma_f32_16x16x32_bf16 v[180:183],v[72:75],a[8:11],v[180:183]\n"
"buffer_load_dwordx4 v112,s[24:27],s76 offen offset:3072 sc1 lds\n"
"v_mfma_f32_16x16x32_bf16 v[180:183],v[76:79],a[12:15],v[180:183]\n"
"s_add_u32 m0,s78,49152\n"
"s_xor_b32 s76,s76,16384\n"
"ds_read_b128 v[16:19],v97 offset:2048\n"
"v_mfma_f32_16x16x32_bf16 v[148:151],v[76:79],a[4:7],v[148:151]\n"
"buffer_load_dwordx4 v112,s[24:27],s76 offen offset:0 sc1 lds\n"
"v_mfma_f32_16x16x32_bf16 v[148:151],v[72:75],a[0:3],v[148:151]\n"
"ds_read_b128 v[24:27],v97 offset:3072\n"
"v_accvgpr_read_b32 v117,a225\n"
"v_mfma_f32_16x16x32_bf16 v[152:155],v[80:83],a[0:3],v[152:155]\n"
"ds_read_b64_tr_b16 v[72:73],v105 offset:49152\n"
"v_accvgpr_read_b32 v118,a226\n"
"v_mfma_f32_16x16x32_bf16 v[152:155],v[84:87],a[4:7],v[152:155]\n"
"ds_read_b64_tr_b16 v[74:75],v105 offset:50176\n"
"v_accvgpr_read_b32 v119,a227\n"
"v_mfma_f32_16x16x32_bf16 v[184:187],v[84:87],a[12:15],v[184:187]\n"
"ds_read_b64_tr_b16 v[76:77],v105 offset:57344\n"
"v_accvgpr_read_b32 v120,a228\n"
"v_mfma_f32_16x16x32_bf16 v[184:187],v[80:83],a[8:11],v[184:187]\n"
"ds_read_b64_tr_b16 v[78:79],v105 offset:58368\n"
"v_accvgpr_read_b32 v121,a229\n"
"v_mfma_f32_16x16x32_bf16 v[216:219],v[80:83],a[16:19],v[216:219]\n"
"ds_read_b128 v[4:7],v97 offset:16384\n"
"v_accvgpr_read_b32 v122,a230\n"
"v_mfma_f32_16x16x32_bf16 v[216:219],v[84:87],a[20:23],v[216:219]\n"
"ds_read_b128 v[12:15],v97 offset:17408\n"
"v_accvgpr_read_b32 v123,a231\n"
"v_mfma_f32_16x16x32_bf16 v[248:251],v[84:87],a[28:31],v[248:251]\n"
"ds_read_b128 v[20:23],v97 offset:18432\n"
"v_cvt_pk_bf16_f32 v116,v116,v117\n"
"v_mfma_f32_16x16x32_bf16 v[248:251],v[80:83],a[24:27],v[248:251]\n"
"ds_read_b128 v[28:31],v97 offset:19456\n"
"v_cvt_pk_bf16_f32 v117,v118,v119\n"
"v_mfma_f32_16x16x32_bf16 v[252:255],v[88:91],a[24:27],v[252:255]\n"
"buffer_load_dwordx4 v112,s[24:27],s76 offen offset:1024 sc1 lds\n"
"v_mfma_f32_16x16x32_bf16 v[252:255],v[92:95],a[28:31],v[252:255]\n"
"v_cvt_pk_bf16_f32 v118,v120,v121\n"
"v_cvt_pk_bf16_f32 v119,v122,v123\n"
"v_mfma_f32_16x16x32_bf16 v[220:223],v[92:95],a[20:23],v[220:223]\n"
"v_accvgpr_write_b32 a240,v116\n"
"v_accvgpr_write_b32 a241,v117\n"
"v_mfma_f32_16x16x32_bf16 v[220:223],v[88:91],a[16:19],v[220:223]\n"
"buffer_load_dwordx4 v112,s[24:27],s76 offen offset:2048 sc1 lds\n"
"v_mfma_f32_16x16x32_bf16 v[188:191],v[88:91],a[8:11],v[188:191]\n"
"v_accvgpr_write_b32 a242,v118\n"
"v_accvgpr_write_b32 a243,v119\n"
"v_mfma_f32_16x16x32_bf16 v[188:191],v[92:95],a[12:15],v[188:191]\n"
"v_accvgpr_read_b32 v116,a216\n"
"v_accvgpr_read_b32 v117,a217\n"
"v_mfma_f32_16x16x32_bf16 v[156:159],v[92:95],a[4:7],v[156:159]\n"
"buffer_load_dwordx4 v112,s[24:27],s76 offen offset:3072 sc1 lds\n"
"v_mfma_f32_16x16x32_bf16 v[156:159],v[88:91],a[0:3],v[156:159]\n"
"s_xor_b32 s76,s76,49152\n"
"v_accvgpr_read_b32 v118,a218\n"
"s_waitcnt vmcnt(23) lgkmcnt(0)\n"
"s_barrier\n"
"v_mfma_f32_16x16x32_bf16 v[128:131],a[32:35],v[0:3],v[128:131]\n"
"ds_read_b64_tr_b16 v[80:81],v98 offset:32768\n"
"ds_read_b64_tr_b16 v[88:89],v99 offset:32768\n"
"v_mfma_f32_16x16x32_bf16 v[128:131],a[36:39],v[4:7],v[128:131]\n"
"ds_read_b64_tr_b16 v[82:83],v98 offset:33792\n"
"ds_read_b64_tr_b16 v[90:91],v99 offset:33792\n"
"v_mfma_f32_16x16x32_bf16 v[160:163],a[36:39],v[12:15],v[160:163]\n"
"ds_read_b64_tr_b16 v[84:85],v98 offset:40960\n"
"ds_read_b64_tr_b16 v[92:93],v99 offset:40960\n"
"v_mfma_f32_16x16x32_bf16 v[160:163],a[32:35],v[8:11],v[160:163]\n"
"s_add_u32 m0,s85,147456\n"
"ds_read_b64_tr_b16 v[86:87],v98 offset:41984\n"
"ds_read_b64_tr_b16 v[94:95],v99 offset:41984\n"
"v_mfma_f32_16x16x32_bf16 v[192:195],a[32:35],v[16:19],v[192:195]\n"
"buffer_load_dwordx4 v110,s[28:31],s86 offen lds\n"
"v_mfma_f32_16x16x32_bf16 v[192:195],a[36:39],v[20:23],v[192:195]\n"
"v_accvgpr_read_b32 v119,a219\n"
"v_accvgpr_read_b32 v120,a220\n"
"v_mfma_f32_16x16x32_bf16 v[224:227],a[36:39],v[28:31],v[224:227]\n"
"v_accvgpr_read_b32 v121,a221\n"
"v_accvgpr_read_b32 v122,a222\n"
"v_mfma_f32_16x16x32_bf16 v[224:227],a[32:35],v[24:27],v[224:227]\n"
"v_accvgpr_read_b32 v123,a223\n"
"v_cvt_pk_bf16_f32 v116,v116,v117\n"
"v_mfma_f32_16x16x32_bf16 v[228:231],a[40:43],v[24:27],v[228:231]\n"
"ds_read_b64_tr_b16 a[32:33],v100 offset:32768\n"
"v_cvt_pk_bf16_f32 v117,v118,v119\n"
"v_mfma_f32_16x16x32_bf16 v[228:231],a[44:47],v[28:31],v[228:231]\n"
"ds_read_b64_tr_b16 a[34:35],v100 offset:33792\n"
"v_cvt_pk_bf16_f32 v118,v120,v121\n"
"v_mfma_f32_16x16x32_bf16 v[196:199],a[44:47],v[20:23],v[196:199]\n"
"ds_read_b64_tr_b16 a[36:37],v100 offset:40960\n"
"v_cvt_pk_bf16_f32 v119,v122,v123\n"
"v_mfma_f32_16x16x32_bf16 v[196:199],a[40:43],v[16:19],v[196:199]\n"
"s_add_u32 m0,s85,148480\n"
"ds_read_b64_tr_b16 a[38:39],v100 offset:41984\n"
"v_mfma_f32_16x16x32_bf16 v[164:167],a[40:43],v[8:11],v[164:167]\n"
"buffer_load_dwordx4 v111,s[28:31],s86 offen lds\n"
"v_mfma_f32_16x16x32_bf16 v[164:167],a[44:47],v[12:15],v[164:167]\n"
"s_add_u32 m0,s85,155648\n"
"s_xor_b32 s86,s86,262144\n"
"v_mfma_f32_16x16x32_bf16 v[132:135],a[44:47],v[4:7],v[132:135]\n"
"buffer_load_dwordx4 v110,s[28:31],s86 offen lds\n"
"v_mfma_f32_16x16x32_bf16 v[132:135],a[40:43],v[0:3],v[132:135]\n"
"v_accvgpr_write_b32 a236,v116\n"
"v_accvgpr_write_b32 a237,v117\n"
"v_mfma_f32_16x16x32_bf16 v[136:139],v[32:35],v[0:3],v[136:139]\n"
"ds_read_b64_tr_b16 a[40:41],v101 offset:32768\n"
"v_accvgpr_write_b32 a238,v118\n"
"v_mfma_f32_16x16x32_bf16 v[136:139],v[36:39],v[4:7],v[136:139]\n"
"ds_read_b64_tr_b16 a[42:43],v101 offset:33792\n"
"v_accvgpr_write_b32 a239,v119\n"
"v_mfma_f32_16x16x32_bf16 v[168:171],v[36:39],v[12:15],v[168:171]\n"
"ds_read_b64_tr_b16 a[44:45],v101 offset:40960\n"
"v_accvgpr_read_b32 v116,a208\n"
"v_mfma_f32_16x16x32_bf16 v[168:171],v[32:35],v[8:11],v[168:171]\n"
"s_add_u32 m0,s85,156672\n"
"ds_read_b64_tr_b16 a[46:47],v101 offset:41984\n"
"v_mfma_f32_16x16x32_bf16 v[200:203],v[32:35],v[16:19],v[200:203]\n"
"buffer_load_dwordx4 v111,s[28:31],s86 offen lds\n"
"v_mfma_f32_16x16x32_bf16 v[200:203],v[36:39],v[20:23],v[200:203]\n"
"s_xor_b32 s86,s86,1835008\n"
"v_accvgpr_read_b32 v117,a209\n"
"v_mfma_f32_16x16x32_bf16 v[232:235],v[36:39],v[28:31],v[232:235]\n"
"v_accvgpr_read_b32 v118,a210\n"
"v_accvgpr_read_b32 v119,a211\n"
"v_mfma_f32_16x16x32_bf16 v[232:235],v[32:35],v[24:27],v[232:235]\n"
"v_accvgpr_read_b32 v120,a212\n"
"v_accvgpr_read_b32 v121,a213\n"
"v_mfma_f32_16x16x32_bf16 v[236:239],v[40:43],v[24:27],v[236:239]\n"
"ds_read_b64_tr_b16 v[32:33],v102 offset:32768\n"
"v_accvgpr_read_b32 v122,a214\n"
"v_mfma_f32_16x16x32_bf16 v[236:239],v[44:47],v[28:31],v[236:239]\n"
"ds_read_b64_tr_b16 v[34:35],v102 offset:33792\n"
"v_accvgpr_read_b32 v123,a215\n"
"v_mfma_f32_16x16x32_bf16 v[204:207],v[44:47],v[20:23],v[204:207]\n"
"ds_read_b64_tr_b16 v[36:37],v102 offset:40960\n"
"v_cvt_pk_bf16_f32 v116,v116,v117\n"
"v_mfma_f32_16x16x32_bf16 v[204:207],v[40:43],v[16:19],v[204:207]\n"
"s_add_u32 m0,s78,65536\n"
"ds_read_b64_tr_b16 v[38:39],v102 offset:41984\n"
"v_accvgpr_write_b32 a232,v116\n"
"v_mfma_f32_16x16x32_bf16 v[172:175],v[40:43],v[8:11],v[172:175]\n"
"buffer_load_dwordx4 v112,s[24:27],s76 offen offset:0 sc1 lds\n"
"v_mfma_f32_16x16x32_bf16 v[172:175],v[44:47],v[12:15],v[172:175]\n"
"v_cvt_pk_bf16_f32 v117,v118,v119\n"
"v_cvt_pk_bf16_f32 v118,v120,v121\n"
"v_mfma_f32_16x16x32_bf16 v[140:143],v[44:47],v[4:7],v[140:143]\n"
"buffer_load_dwordx4 v112,s[24:27],s76 offen offset:1024 sc1 lds\n"
"v_mfma_f32_16x16x32_bf16 v[140:143],v[40:43],v[0:3],v[140:143]\n"
"v_cvt_pk_bf16_f32 v119,v122,v123\n"
"s_waitcnt vmcnt(18)\n"
"v_mfma_f32_16x16x32_bf16 v[144:147],v[48:51],v[0:3],v[144:147]\n"
"ds_read_b64_tr_b16 v[40:41],v103 offset:32768\n"
"v_accvgpr_write_b32 a233,v117\n"
"v_lshrrev_b32 v106,8,v112\n"
"v_mfma_f32_16x16x32_bf16 v[144:147],v[52:55],v[4:7],v[144:147]\n"
"ds_read_b64_tr_b16 v[42:43],v103 offset:33792\n"
"v_accvgpr_write_b32 a234,v118\n"
"v_lshlrev_b32 v107,6,v106\n"
"v_mfma_f32_16x16x32_bf16 v[176:179],v[52:55],v[12:15],v[176:179]\n"
"ds_read_b64_tr_b16 v[44:45],v103 offset:40960\n"
"v_accvgpr_write_b32 a235,v119\n"
"v_and_b32 v108,0xf0,v112\n"
"v_mfma_f32_16x16x32_bf16 v[176:179],v[48:51],v[8:11],v[176:179]\n"
"ds_read_b64_tr_b16 v[46:47],v103 offset:41984\n"
"v_accvgpr_read_b32 v116,a200\n"
"v_xor_b32 v107,v107,v108\n"
"v_mfma_f32_16x16x32_bf16 v[208:211],v[48:51],v[16:19],v[208:211]\n"
"buffer_load_dwordx4 v112,s[24:27],s76 offen offset:2048 sc1 lds\n"
"v_lshl_add_u32 v106,v106,11,v107\n"
"v_mfma_f32_16x16x32_bf16 v[208:211],v[52:55],v[20:23],v[208:211]\n"
"ds_read_b128 a[0:3],v96 offset:0\n"
"v_accvgpr_read_b32 v117,a201\n"
"v_lshrrev_b32 v107,6,v112\n"
"v_mfma_f32_16x16x32_bf16 v[240:243],v[52:55],v[28:31],v[240:243]\n"
"v_accvgpr_read_b32 v118,a202\n"
"v_accvgpr_read_b32 v119,a203\n"
"v_and_b32 v108,3,v107\n"
"v_mfma_f32_16x16x32_bf16 v[240:243],v[48:51],v[24:27],v[240:243]\n"
"ds_read_b128 a[8:11],v96 offset:1024\n"
"v_accvgpr_read_b32 v120,a204\n"
"v_lshlrev_b32 v108,6,v108\n"
"v_mfma_f32_16x16x32_bf16 v[244:247],v[56:59],v[24:27],v[244:247]\n"
"ds_read_b64_tr_b16 v[48:49],v104 offset:32768\n"
"v_accvgpr_read_b32 v121,a205\n"
"v_mfma_f32_16x16x32_bf16 v[244:247],v[60:63],v[28:31],v[244:247]\n"
"s_lshl_b32 s82,s52,6\n"
"ds_read_b64_tr_b16 v[50:51],v104 offset:33792\n"
"v_accvgpr_read_b32 v122,a206\n"
"v_xor_b32 v108,s82,v108\n"
"v_mfma_f32_16x16x32_bf16 v[212:215],v[60:63],v[20:23],v[212:215]\n"
"ds_read_b64_tr_b16 v[52:53],v104 offset:40960\n"
"v_accvgpr_read_b32 v123,a207\n"
"v_lshl_add_u32 v107,v107,8,v108\n"
"v_mfma_f32_16x16x32_bf16 v[212:215],v[56:59],v[16:19],v[212:215]\n"
"ds_read_b64_tr_b16 v[54:55],v104 offset:41984\n"
"v_cvt_pk_bf16_f32 v116,v116,v117\n"
"v_and_b32 v108,0x30,v112\n"
"v_mfma_f32_16x16x32_bf16 v[180:183],v[56:59],v[8:11],v[180:183]\n"
"buffer_load_dwordx4 v112,s[24:27],s76 offen offset:3072 sc1 lds\n"
"v_add_u32 v107,v107,v108\n"
"v_mfma_f32_16x16x32_bf16 v[180:183],v[60:63],v[12:15],v[180:183]\n"
"s_add_u32 m0,s78,81920\n"
"s_xor_b32 s76,s76,16384\n"
"ds_read_b128 a[16:19],v96 offset:2048\n"
"v_mfma_f32_16x16x32_bf16 v[148:151],v[60:63],v[4:7],v[148:151]\n"
"buffer_load_dwordx4 v112,s[24:27],s76 offen offset:0 sc1 lds\n"
"v_add_u32 v107,114688,v107\n"
"v_mfma_f32_16x16x32_bf16 v[148:151],v[56:59],v[0:3],v[148:151]\n"
"ds_read_b128 a[24:27],v96 offset:3072\n"
"v_cvt_pk_bf16_f32 v117,v118,v119\n"
"v_mfma_f32_16x16x32_bf16 v[152:155],v[64:67],v[0:3],v[152:155]\n"
"ds_read_b64_tr_b16 v[56:57],v105 offset:32768\n"
"v_cvt_pk_bf16_f32 v118,v120,v121\n"
"v_mfma_f32_16x16x32_bf16 v[152:155],v[68:71],v[4:7],v[152:155]\n"
"ds_read_b64_tr_b16 v[58:59],v105 offset:33792\n"
"v_cvt_pk_bf16_f32 v119,v122,v123\n"
"v_mfma_f32_16x16x32_bf16 v[184:187],v[68:71],v[12:15],v[184:187]\n"
"ds_read_b64_tr_b16 v[60:61],v105 offset:40960\n"
"v_accvgpr_write_b32 a228,v116\n"
"v_mfma_f32_16x16x32_bf16 v[184:187],v[64:67],v[8:11],v[184:187]\n"
"ds_read_b64_tr_b16 v[62:63],v105 offset:41984\n"
"v_accvgpr_write_b32 a229,v117\n"
"v_mfma_f32_16x16x32_bf16 v[216:219],v[64:67],v[16:19],v[216:219]\n"
"ds_read_b128 a[4:7],v96 offset:16384\n"
"v_accvgpr_write_b32 a230,v118\n"
"v_mfma_f32_16x16x32_bf16 v[216:219],v[68:71],v[20:23],v[216:219]\n"
"ds_read_b128 a[12:15],v96 offset:17408\n"
"v_accvgpr_write_b32 a231,v119\n"
"v_mfma_f32_16x16x32_bf16 v[248:251],v[68:71],v[28:31],v[248:251]\n"
"ds_read_b128 a[20:23],v96 offset:18432\n"
"v_accvgpr_read_b32 v116,a192\n"
"v_mfma_f32_16x16x32_bf16 v[248:251],v[64:67],v[24:27],v[248:251]\n"
"ds_read_b128 a[28:31],v96 offset:19456\n"
"v_accvgpr_read_b32 v117,a193\n"
"v_mfma_f32_16x16x32_bf16 v[252:255],v[72:75],v[24:27],v[252:255]\n"
"buffer_load_dwordx4 v112,s[24:27],s76 offen offset:1024 sc1 lds\n"
"v_mfma_f32_16x16x32_bf16 v[252:255],v[76:79],v[28:31],v[252:255]\n"
"v_accvgpr_read_b32 v118,a194\n"
"v_accvgpr_read_b32 v119,a195\n"
"v_mfma_f32_16x16x32_bf16 v[220:223],v[76:79],v[20:23],v[220:223]\n"
"v_accvgpr_read_b32 v120,a196\n"
"v_accvgpr_read_b32 v121,a197\n"
"v_mfma_f32_16x16x32_bf16 v[220:223],v[72:75],v[16:19],v[220:223]\n"
"buffer_load_dwordx4 v112,s[24:27],s76 offen offset:2048 sc1 lds\n"
"v_mfma_f32_16x16x32_bf16 v[188:191],v[72:75],v[8:11],v[188:191]\n"
"v_accvgpr_read_b32 v122,a198\n"
"v_accvgpr_read_b32 v123,a199\n"
"v_mfma_f32_16x16x32_bf16 v[188:191],v[76:79],v[12:15],v[188:191]\n"
"v_cvt_pk_bf16_f32 v116,v116,v117\n"
"v_cvt_pk_bf16_f32 v117,v118,v119\n"
"v_mfma_f32_16x16x32_bf16 v[156:159],v[76:79],v[4:7],v[156:159]\n"
"buffer_load_dwordx4 v112,s[24:27],s76 offen offset:3072 sc1 lds\n"
"v_mfma_f32_16x16x32_bf16 v[156:159],v[72:75],v[0:3],v[156:159]\n"
"s_xor_b32 s76,s76,114688\n"
"v_cvt_pk_bf16_f32 v118,v120,v121\n"
"s_waitcnt vmcnt(20) lgkmcnt(0)\n"
"s_barrier\n"
"v_mfma_f32_16x16x32_bf16 v[128:131],v[80:83],a[0:3],v[128:131]\n"
"ds_read_b64_tr_b16 v[64:65],v98 offset:0\n"
"ds_read_b64_tr_b16 v[72:73],v99 offset:0\n"
"v_mfma_f32_16x16x32_bf16 v[128:131],v[84:87],a[4:7],v[128:131]\n"
"ds_read_b64_tr_b16 v[66:67],v98 offset:1024\n"
"ds_read_b64_tr_b16 v[74:75],v99 offset:1024\n"
"v_mfma_f32_16x16x32_bf16 v[160:163],v[84:87],a[12:15],v[160:163]\n"
"ds_read_b64_tr_b16 v[68:69],v98 offset:8192\n"
"ds_read_b64_tr_b16 v[76:77],v99 offset:8192\n"
"v_mfma_f32_16x16x32_bf16 v[160:163],v[80:83],a[8:11],v[160:163]\n"
"s_add_u32 m0,s85,131072\n"
"ds_read_b64_tr_b16 v[70:71],v98 offset:9216\n"
"ds_read_b64_tr_b16 v[78:79],v99 offset:9216\n"
"v_mfma_f32_16x16x32_bf16 v[192:195],v[80:83],a[16:19],v[192:195]\n"
"buffer_load_dwordx4 v110,s[28:31],s86 offen lds\n"
"v_mfma_f32_16x16x32_bf16 v[192:195],v[84:87],a[20:23],v[192:195]\n"
"s_add_u32 m0,s85,132096\n"
"v_cvt_pk_bf16_f32 v119,v122,v123\n"
"v_mfma_f32_16x16x32_bf16 v[224:227],v[84:87],a[28:31],v[224:227]\n"
"buffer_load_dwordx4 v111,s[28:31],s86 offen lds\n"
"v_mfma_f32_16x16x32_bf16 v[224:227],v[80:83],a[24:27],v[224:227]\n"
"s_xor_b32 s86,s86,262144\n"
"v_accvgpr_write_b32 a224,v116\n"
"v_mfma_f32_16x16x32_bf16 v[228:231],v[88:91],a[24:27],v[228:231]\n"
"ds_read_b64_tr_b16 v[80:81],v100 offset:0\n"
"v_accvgpr_write_b32 a225,v117\n"
"v_mfma_f32_16x16x32_bf16 v[228:231],v[92:95],a[28:31],v[228:231]\n"
"ds_read_b64_tr_b16 v[82:83],v100 offset:1024\n"
"v_accvgpr_write_b32 a226,v118\n"
"v_mfma_f32_16x16x32_bf16 v[196:199],v[92:95],a[20:23],v[196:199]\n"
"ds_read_b64_tr_b16 v[84:85],v100 offset:8192\n"
"v_accvgpr_write_b32 a227,v119\n"
"v_mfma_f32_16x16x32_bf16 v[196:199],v[88:91],a[16:19],v[196:199]\n"
"s_add_u32 m0,s85,139264\n"
"ds_read_b64_tr_b16 v[86:87],v100 offset:9216\n"
"v_mfma_f32_16x16x32_bf16 v[164:167],v[88:91],a[8:11],v[164:167]\n"
"buffer_load_dwordx4 v110,s[28:31],s86 offen lds\n"
"v_mfma_f32_16x16x32_bf16 v[164:167],v[92:95],a[12:15],v[164:167]\n"
"s_add_u32 m0,s85,140288\n"
"s_add_u32 s56,s56,126976\n"
"v_mfma_f32_16x16x32_bf16 v[132:135],v[92:95],a[4:7],v[132:135]\n"
"buffer_load_dwordx4 v111,s[28:31],s86 offen lds\n"
"v_mfma_f32_16x16x32_bf16 v[132:135],v[88:91],a[0:3],v[132:135]\n"
"s_xor_b32 s86,s86,786432\n"
"v_mfma_f32_16x16x32_bf16 v[136:139],a[32:35],a[0:3],v[136:139]\n"
"ds_read_b64_tr_b16 v[88:89],v101 offset:0\n"
"v_mfma_f32_16x16x32_bf16 v[136:139],a[36:39],a[4:7],v[136:139]\n"
"ds_read_b64_tr_b16 v[90:91],v101 offset:1024\n"
"v_mfma_f32_16x16x32_bf16 v[168:171],a[36:39],a[12:15],v[168:171]\n"
"ds_read_b64_tr_b16 v[92:93],v101 offset:8192\n"
"v_mfma_f32_16x16x32_bf16 v[168:171],a[32:35],a[8:11],v[168:171]\n"
"s_add_u32 m0,s78,114688\n"
"ds_read_b64_tr_b16 v[94:95],v101 offset:9216\n"
"v_mfma_f32_16x16x32_bf16 v[200:203],a[32:35],a[16:19],v[200:203]\n"
"buffer_load_dwordx4 v106,s[4:7],s13 offen offset:0 nt lds\n"
"v_mfma_f32_16x16x32_bf16 v[200:203],a[36:39],a[20:23],v[200:203]\n"
"s_add_u32 s13,s13,7168\n"
"v_mfma_f32_16x16x32_bf16 v[232:235],a[36:39],a[28:31],v[232:235]\n"
"buffer_load_dwordx4 v106,s[4:7],s13 offen offset:1024 nt lds\n"
"v_mfma_f32_16x16x32_bf16 v[232:235],a[32:35],a[24:27],v[232:235]\n"
"s_add_u32 s13,s13,7168\n"
"v_mfma_f32_16x16x32_bf16 v[236:239],a[40:43],a[24:27],v[236:239]\n"
"ds_read_b64_tr_b16 a[32:33],v102 offset:0\n"
"v_mfma_f32_16x16x32_bf16 v[236:239],a[44:47],a[28:31],v[236:239]\n"
"ds_read_b64_tr_b16 a[34:35],v102 offset:1024\n"
"v_mfma_f32_16x16x32_bf16 v[204:207],a[44:47],a[20:23],v[204:207]\n"
"ds_read_b64_tr_b16 a[36:37],v102 offset:8192\n"
"v_mfma_f32_16x16x32_bf16 v[204:207],a[40:43],a[16:19],v[204:207]\n"
"ds_read_b64_tr_b16 a[38:39],v102 offset:9216\n"
"v_mfma_f32_16x16x32_bf16 v[172:175],a[40:43],a[8:11],v[172:175]\n"
"buffer_load_dwordx4 v106,s[4:7],s13 offen offset:2048 nt lds\n"
"v_mfma_f32_16x16x32_bf16 v[172:175],a[44:47],a[12:15],v[172:175]\n"
"s_add_u32 s13,s13,7168\n"
"v_mfma_f32_16x16x32_bf16 v[140:143],a[44:47],a[4:7],v[140:143]\n"
"buffer_load_dwordx4 v106,s[4:7],s13 offen offset:3072 nt lds\n"
"v_mfma_f32_16x16x32_bf16 v[140:143],a[40:43],a[0:3],v[140:143]\n"
"s_add_u32 s13,s13,109568\n"
"s_waitcnt vmcnt(20)\n"
"v_mfma_f32_16x16x32_bf16 v[144:147],v[32:35],a[0:3],v[144:147]\n"
"ds_read_b64_tr_b16 a[40:41],v103 offset:0\n"
"v_mfma_f32_16x16x32_bf16 v[144:147],v[36:39],a[4:7],v[144:147]\n"
"ds_read_b64_tr_b16 a[42:43],v103 offset:1024\n"
"v_mfma_f32_16x16x32_bf16 v[176:179],v[36:39],a[12:15],v[176:179]\n"
"ds_read_b64_tr_b16 a[44:45],v103 offset:8192\n"
"v_mfma_f32_16x16x32_bf16 v[176:179],v[32:35],a[8:11],v[176:179]\n"
"s_add_u32 m0,s78,0\n"
"ds_read_b64_tr_b16 a[46:47],v103 offset:9216\n"
"v_mfma_f32_16x16x32_bf16 v[208:211],v[32:35],a[16:19],v[208:211]\n"
"buffer_load_dwordx4 v112,s[24:27],s76 offen offset:0 sc1 lds\n"
"v_mfma_f32_16x16x32_bf16 v[208:211],v[36:39],a[20:23],v[208:211]\n"
"ds_read_b128 v[0:3],v96 offset:32768\n"
"v_mfma_f32_16x16x32_bf16 v[240:243],v[36:39],a[28:31],v[240:243]\n"
"buffer_load_dwordx4 v112,s[24:27],s76 offen offset:1024 sc1 lds\n"
"v_mfma_f32_16x16x32_bf16 v[240:243],v[32:35],a[24:27],v[240:243]\n"
"ds_read_b128 v[8:11],v96 offset:33792\n"
"v_mfma_f32_16x16x32_bf16 v[244:247],v[40:43],a[24:27],v[244:247]\n"
"ds_read_b64_tr_b16 v[32:33],v104 offset:0\n"
"v_mfma_f32_16x16x32_bf16 v[244:247],v[44:47],a[28:31],v[244:247]\n"
"ds_read_b64_tr_b16 v[34:35],v104 offset:1024\n"
"v_mfma_f32_16x16x32_bf16 v[212:215],v[44:47],a[20:23],v[212:215]\n"
"ds_read_b64_tr_b16 v[36:37],v104 offset:8192\n"
"v_mfma_f32_16x16x32_bf16 v[212:215],v[40:43],a[16:19],v[212:215]\n"
"ds_read_b64_tr_b16 v[38:39],v104 offset:9216\n"
"v_mfma_f32_16x16x32_bf16 v[180:183],v[40:43],a[8:11],v[180:183]\n"
"buffer_load_dwordx4 v112,s[24:27],s76 offen offset:2048 sc1 lds\n"
"v_mfma_f32_16x16x32_bf16 v[180:183],v[44:47],a[12:15],v[180:183]\n"
"ds_read_b128 v[16:19],v96 offset:34816\n"
"v_mfma_f32_16x16x32_bf16 v[148:151],v[44:47],a[4:7],v[148:151]\n"
"buffer_load_dwordx4 v112,s[24:27],s76 offen offset:3072 sc1 lds\n"
"v_mfma_f32_16x16x32_bf16 v[148:151],v[40:43],a[0:3],v[148:151]\n"
"s_xor_b32 s76,s76,16384\n"
"ds_read_b128 v[24:27],v96 offset:35840\n"
"v_mfma_f32_16x16x32_bf16 v[152:155],v[48:51],a[0:3],v[152:155]\n"
"ds_read_b64_tr_b16 v[40:41],v105 offset:0\n"
"v_mfma_f32_16x16x32_bf16 v[152:155],v[52:55],a[4:7],v[152:155]\n"
"ds_read_b64_tr_b16 v[42:43],v105 offset:1024\n"
"v_mfma_f32_16x16x32_bf16 v[184:187],v[52:55],a[12:15],v[184:187]\n"
"ds_read_b64_tr_b16 v[44:45],v105 offset:8192\n"
"v_mfma_f32_16x16x32_bf16 v[184:187],v[48:51],a[8:11],v[184:187]\n"
"ds_read_b64_tr_b16 v[46:47],v105 offset:9216\n"
"v_mfma_f32_16x16x32_bf16 v[216:219],v[48:51],a[16:19],v[216:219]\n"
"ds_read_b128 v[4:7],v96 offset:49152\n"
"v_mfma_f32_16x16x32_bf16 v[216:219],v[52:55],a[20:23],v[216:219]\n"
"ds_read_b128 v[12:15],v96 offset:50176\n"
"ds_read_b128 v[20:23],v96 offset:51200\n"
"v_mfma_f32_16x16x32_bf16 v[248:251],v[52:55],a[28:31],v[248:251]\n"
"buffer_store_dwordx2 v[124:125],off,s[24:27],s59 sc1\n"
"v_mfma_f32_16x16x32_bf16 v[248:251],v[48:51],a[24:27],v[248:251]\n"
"s_add_u32 m0,s78,16384\n"
"ds_read_b128 v[28:31],v96 offset:52224\n"
"v_mfma_f32_16x16x32_bf16 v[252:255],v[56:59],a[24:27],v[252:255]\n"
"buffer_load_dwordx4 v112,s[24:27],s76 offen offset:0 sc1 lds\n"
"v_mfma_f32_16x16x32_bf16 v[252:255],v[60:63],a[28:31],v[252:255]\n"
"v_mfma_f32_16x16x32_bf16 v[220:223],v[60:63],a[20:23],v[220:223]\n"
"buffer_load_dwordx4 v112,s[24:27],s76 offen offset:1024 sc1 lds\n"
"v_mfma_f32_16x16x32_bf16 v[220:223],v[56:59],a[16:19],v[220:223]\n"
"v_mfma_f32_16x16x32_bf16 v[188:191],v[56:59],a[8:11],v[188:191]\n"
"buffer_load_dwordx4 v112,s[24:27],s76 offen offset:2048 sc1 lds\n"
"v_mfma_f32_16x16x32_bf16 v[188:191],v[60:63],a[12:15],v[188:191]\n"
"v_mfma_f32_16x16x32_bf16 v[156:159],v[60:63],a[4:7],v[156:159]\n"
"buffer_load_dwordx4 v112,s[24:27],s76 offen offset:3072 sc1 lds\n"
"v_mfma_f32_16x16x32_bf16 v[156:159],v[56:59],a[0:3],v[156:159]\n"
"s_xor_b32 s76,s76,49152\n"
"s_waitcnt vmcnt(25) lgkmcnt(0)\n"
"s_barrier\n"
"v_mfma_f32_16x16x32_bf16 v[128:131],v[64:67],v[0:3],v[128:131]\n"
"ds_read_b64_tr_b16 v[48:49],v98 offset:49152\n"
"ds_read_b64_tr_b16 v[56:57],v99 offset:49152\n"
"v_mfma_f32_16x16x32_bf16 v[128:131],v[68:71],v[4:7],v[128:131]\n"
"ds_read_b64_tr_b16 v[50:51],v98 offset:50176\n"
"ds_read_b64_tr_b16 v[58:59],v99 offset:50176\n"
"v_mfma_f32_16x16x32_bf16 v[160:163],v[68:71],v[12:15],v[160:163]\n"
"ds_read_b64_tr_b16 v[52:53],v98 offset:57344\n"
"ds_read_b64_tr_b16 v[60:61],v99 offset:57344\n"
"v_mfma_f32_16x16x32_bf16 v[160:163],v[64:67],v[8:11],v[160:163]\n"
"s_add_u32 m0,s85,98304\n"
"ds_read_b64_tr_b16 v[54:55],v98 offset:58368\n"
"ds_read_b64_tr_b16 v[62:63],v99 offset:58368\n"
"v_mfma_f32_16x16x32_bf16 v[192:195],v[64:67],v[16:19],v[192:195]\n"
"buffer_load_dwordx4 v110,s[28:31],s86 offen lds\n"
"v_mfma_f32_16x16x32_bf16 v[192:195],v[68:71],v[20:23],v[192:195]\n"
"v_mfma_f32_16x16x32_bf16 v[224:227],v[68:71],v[28:31],v[224:227]\n"
"v_mfma_f32_16x16x32_bf16 v[224:227],v[64:67],v[24:27],v[224:227]\n"
"v_mfma_f32_16x16x32_bf16 v[228:231],v[72:75],v[24:27],v[228:231]\n"
"ds_read_b64_tr_b16 v[64:65],v100 offset:49152\n"
"v_mfma_f32_16x16x32_bf16 v[228:231],v[76:79],v[28:31],v[228:231]\n"
"ds_read_b64_tr_b16 v[66:67],v100 offset:50176\n"
"v_mfma_f32_16x16x32_bf16 v[196:199],v[76:79],v[20:23],v[196:199]\n"
"ds_read_b64_tr_b16 v[68:69],v100 offset:57344\n"
"v_mfma_f32_16x16x32_bf16 v[196:199],v[72:75],v[16:19],v[196:199]\n"
"s_add_u32 m0,s85,99328\n"
"ds_read_b64_tr_b16 v[70:71],v100 offset:58368\n"
"v_mfma_f32_16x16x32_bf16 v[164:167],v[72:75],v[8:11],v[164:167]\n"
"buffer_load_dwordx4 v111,s[28:31],s86 offen lds\n"
"v_mfma_f32_16x16x32_bf16 v[164:167],v[76:79],v[12:15],v[164:167]\n"
"s_add_u32 m0,s85,106496\n"
"s_xor_b32 s86,s86,262144\n"
"v_mfma_f32_16x16x32_bf16 v[132:135],v[76:79],v[4:7],v[132:135]\n"
"buffer_load_dwordx4 v110,s[28:31],s86 offen lds\n"
"v_mfma_f32_16x16x32_bf16 v[132:135],v[72:75],v[0:3],v[132:135]\n"
"v_mfma_f32_16x16x32_bf16 v[136:139],v[80:83],v[0:3],v[136:139]\n"
"ds_read_b64_tr_b16 v[72:73],v101 offset:49152\n"
"v_mfma_f32_16x16x32_bf16 v[136:139],v[84:87],v[4:7],v[136:139]\n"
"ds_read_b64_tr_b16 v[74:75],v101 offset:50176\n"
"v_mfma_f32_16x16x32_bf16 v[168:171],v[84:87],v[12:15],v[168:171]\n"
"ds_read_b64_tr_b16 v[76:77],v101 offset:57344\n"
"v_mfma_f32_16x16x32_bf16 v[168:171],v[80:83],v[8:11],v[168:171]\n"
"s_add_u32 m0,s85,107520\n"
"ds_read_b64_tr_b16 v[78:79],v101 offset:58368\n"
"v_mfma_f32_16x16x32_bf16 v[200:203],v[80:83],v[16:19],v[200:203]\n"
"buffer_load_dwordx4 v111,s[28:31],s86 offen lds\n"
"v_mfma_f32_16x16x32_bf16 v[200:203],v[84:87],v[20:23],v[200:203]\n"
"s_xor_b32 s86,s86,3932160\n"
"v_mfma_f32_16x16x32_bf16 v[232:235],v[84:87],v[28:31],v[232:235]\n"
"v_mfma_f32_16x16x32_bf16 v[232:235],v[80:83],v[24:27],v[232:235]\n"
"v_mfma_f32_16x16x32_bf16 v[236:239],v[88:91],v[24:27],v[236:239]\n"
"ds_read_b64_tr_b16 v[80:81],v102 offset:49152\n"
"v_mfma_f32_16x16x32_bf16 v[236:239],v[92:95],v[28:31],v[236:239]\n"
"ds_read_b64_tr_b16 v[82:83],v102 offset:50176\n"
"v_mfma_f32_16x16x32_bf16 v[204:207],v[92:95],v[20:23],v[204:207]\n"
"ds_read_b64_tr_b16 v[84:85],v102 offset:57344\n"
"v_mfma_f32_16x16x32_bf16 v[204:207],v[88:91],v[16:19],v[204:207]\n"
"s_add_u32 m0,s78,32768\n"
"ds_read_b64_tr_b16 v[86:87],v102 offset:58368\n"
"v_mfma_f32_16x16x32_bf16 v[172:175],v[88:91],v[8:11],v[172:175]\n"
"buffer_load_dwordx4 v112,s[24:27],s76 offen offset:0 sc1 lds\n"
"v_mfma_f32_16x16x32_bf16 v[172:175],v[92:95],v[12:15],v[172:175]\n"
"v_mfma_f32_16x16x32_bf16 v[140:143],v[92:95],v[4:7],v[140:143]\n"
"buffer_load_dwordx4 v112,s[24:27],s76 offen offset:1024 sc1 lds\n"
"v_mfma_f32_16x16x32_bf16 v[140:143],v[88:91],v[0:3],v[140:143]\n"
"s_waitcnt vmcnt(23)\n"
"ds_read_b64_tr_b16 v[88:89],v103 offset:49152\n"
"ds_read_b64_tr_b16 v[90:91],v103 offset:50176\n"
"v_mfma_f32_16x16x32_bf16 v[144:147],a[32:35],v[0:3],v[144:147]\n"
"v_lshrrev_b32 v120,4,v112\n"
"v_and_b32 v121,15,v120\n"
"v_mfma_f32_16x16x32_bf16 v[144:147],a[36:39],v[4:7],v[144:147]\n"
"v_lshrrev_b32 v122,4,v120\n"
"v_lshlrev_b32 v122,2,v122\n"
"v_mfma_f32_16x16x32_bf16 v[176:179],a[36:39],v[12:15],v[176:179]\n"
"ds_read_b64_tr_b16 v[92:93],v103 offset:57344\n"
"v_sub_u32 v121,v121,v122\n"
"v_mfma_f32_16x16x32_bf16 v[176:179],a[32:35],v[8:11],v[176:179]\n"
"s_and_b32 s82,s52,1\n"
"ds_read_b64_tr_b16 v[94:95],v103 offset:58368\n"
"s_lshl_b32 s82,s82,4\n"
"v_add_u32 v121,s82,v121\n"
"v_mfma_f32_16x16x32_bf16 v[208:211],a[32:35],v[16:19],v[208:211]\n"
"buffer_load_dwordx4 v112,s[24:27],s76 offen offset:2048 sc1 lds\n"
"v_cvt_f32_i32 v121,v121\n"
"v_mfma_f32_16x16x32_bf16 v[208:211],a[36:39],v[20:23],v[208:211]\n"
"ds_read_b128 a[96:99],v97 offset:0\n"
"v_add_f32 v121,0.5,v121\n"
"v_mul_f32 v126,0x7149f2ca,v121\n"
"v_mfma_f32_16x16x32_bf16 v[240:243],a[36:39],v[28:31],v[240:243]\n"
"v_and_b32 v120,31,v120\n"
"v_mov_b32 v121,0x3f803f80\n"
"v_mfma_f32_16x16x32_bf16 v[240:243],a[32:35],v[24:27],v[240:243]\n"
"s_mov_b32 s93,0x3f800000\n"
"ds_read_b128 a[104:107],v97 offset:1024\n"
"v_accvgpr_write_b32 a144,v121\n"
"v_mfma_f32_16x16x32_bf16 v[244:247],a[40:43],v[24:27],v[244:247]\n"
"ds_read_b64_tr_b16 a[128:129],v104 offset:49152\n"
"v_accvgpr_write_b32 a146,v121\n"
"v_mfma_f32_16x16x32_bf16 v[244:247],a[44:47],v[28:31],v[244:247]\n"
"ds_read_b64_tr_b16 a[130:131],v104 offset:50176\n"
"v_accvgpr_write_b32 a147,v121\n"
"v_mfma_f32_16x16x32_bf16 v[212:215],a[44:47],v[20:23],v[212:215]\n"
"ds_read_b64_tr_b16 a[132:133],v104 offset:57344\n"
"v_lshlrev_b32 v120,3,v120\n"
"v_mfma_f32_16x16x32_bf16 v[212:215],a[40:43],v[16:19],v[212:215]\n"
"ds_read_b64_tr_b16 a[134:135],v104 offset:58368\n"
"v_accvgpr_write_b32 a145,v121\n"
"v_mfma_f32_16x16x32_bf16 v[180:183],a[40:43],v[8:11],v[180:183]\n"
"buffer_load_dwordx4 v112,s[24:27],s76 offen offset:3072 sc1 lds\n"
"v_mfma_f32_16x16x32_bf16 v[180:183],a[44:47],v[12:15],v[180:183]\n"
"s_add_u32 m0,s78,49152\n"
"s_xor_b32 s76,s76,16384\n"
"ds_read_b128 a[112:115],v97 offset:2048\n"
"v_mfma_f32_16x16x32_bf16 v[148:151],a[44:47],v[4:7],v[148:151]\n"
"buffer_load_dwordx4 v112,s[24:27],s76 offen offset:0 sc1 lds\n"
"v_mfma_f32_16x16x32_bf16 v[148:151],a[40:43],v[0:3],v[148:151]\n"
"ds_read_b128 a[120:123],v97 offset:3072\n"
"v_mfma_f32_16x16x32_bf16 v[152:155],v[32:35],v[0:3],v[152:155]\n"
"ds_read_b64_tr_b16 a[136:137],v105 offset:49152\n"
"v_mfma_f32_16x16x32_bf16 v[152:155],v[36:39],v[4:7],v[152:155]\n"
"ds_read_b64_tr_b16 a[138:139],v105 offset:50176\n"
"v_mfma_f32_16x16x32_bf16 v[184:187],v[36:39],v[12:15],v[184:187]\n"
"ds_read_b64_tr_b16 a[140:141],v105 offset:57344\n"
"v_mfma_f32_16x16x32_bf16 v[184:187],v[32:35],v[8:11],v[184:187]\n"
"ds_read_b64_tr_b16 a[142:143],v105 offset:58368\n"
"v_mfma_f32_16x16x32_bf16 v[216:219],v[32:35],v[16:19],v[216:219]\n"
"ds_read_b128 a[100:103],v97 offset:16384\n"
"v_mfma_f32_16x16x32_bf16 v[216:219],v[36:39],v[20:23],v[216:219]\n"
"ds_read_b128 a[108:111],v97 offset:17408\n"
"v_mfma_f32_16x16x32_bf16 v[248:251],v[36:39],v[28:31],v[248:251]\n"
"ds_read_b128 a[116:119],v97 offset:18432\n"
"v_mfma_f32_16x16x32_bf16 v[248:251],v[32:35],v[24:27],v[248:251]\n"
"ds_read_b128 a[124:127],v97 offset:19456\n"
"v_mfma_f32_16x16x32_bf16 v[252:255],v[40:43],v[24:27],v[252:255]\n"
"buffer_load_dwordx4 v112,s[24:27],s76 offen offset:1024 sc1 lds\n"
"v_mfma_f32_16x16x32_bf16 v[252:255],v[44:47],v[28:31],v[252:255]\n"
"v_mfma_f32_16x16x32_bf16 v[220:223],v[44:47],v[20:23],v[220:223]\n"
"v_mfma_f32_16x16x32_bf16 v[220:223],v[40:43],v[16:19],v[220:223]\n"
"buffer_load_dwordx4 v112,s[24:27],s76 offen offset:2048 sc1 lds\n"
"v_mfma_f32_16x16x32_bf16 v[188:191],v[40:43],v[8:11],v[188:191]\n"
"v_mfma_f32_16x16x32_bf16 v[188:191],v[44:47],v[12:15],v[188:191]\n"
"v_mfma_f32_16x16x32_bf16 v[156:159],v[44:47],v[4:7],v[156:159]\n"
"buffer_load_dwordx4 v112,s[24:27],s76 offen offset:3072 sc1 lds\n"
"v_mfma_f32_16x16x32_bf16 v[156:159],v[40:43],v[0:3],v[156:159]\n"
"s_xor_b32 s76,s76,245760\n"
"s_waitcnt vmcnt(21) lgkmcnt(0)\n"
"s_barrier\n"
"v_mfma_f32_16x16x32_bf16 v[128:131],v[48:51],a[96:99],v[128:131]\n"
"ds_read_b64_tr_b16 v[32:33],v98 offset:32768\n"
"ds_read_b64_tr_b16 v[40:41],v99 offset:32768\n"
"ds_read_b64_tr_b16 a[160:161],v107 offset:0\n"
"v_mfma_f32_16x16x32_bf16 v[128:131],v[52:55],a[100:103],v[128:131]\n"
"ds_read_b64_tr_b16 v[34:35],v98 offset:33792\n"
"ds_read_b64_tr_b16 v[42:43],v99 offset:33792\n"
"ds_read_b64_tr_b16 a[162:163],v107 offset:4096\n"
"v_mfma_f32_16x16x32_bf16 v[160:163],v[52:55],a[108:111],v[160:163]\n"
"ds_read_b64_tr_b16 v[36:37],v98 offset:40960\n"
"ds_read_b64_tr_b16 v[44:45],v99 offset:40960\n"
"ds_read_b64_tr_b16 a[164:165],v107 offset:8\n"
"v_mfma_f32_16x16x32_bf16 v[160:163],v[48:51],a[104:107],v[160:163]\n"
"s_add_u32 m0,s85,147456\n"
"ds_read_b64_tr_b16 v[38:39],v98 offset:41984\n"
"ds_read_b64_tr_b16 v[46:47],v99 offset:41984\n"
"v_mfma_f32_16x16x32_bf16 v[192:195],v[48:51],a[112:115],v[192:195]\n"
"buffer_load_dwordx4 v110,s[28:31],s86 offen lds\n"
"ds_read_b64_tr_b16 a[166:167],v107 offset:4104\n"
"v_mfma_f32_16x16x32_bf16 v[192:195],v[52:55],a[116:119],v[192:195]\n"
"ds_read_b64_tr_b16 a[168:169],v107 offset:8192\n"
"ds_read_b64_tr_b16 a[170:171],v107 offset:12288\n"
"v_mfma_f32_16x16x32_bf16 v[224:227],v[52:55],a[124:127],v[224:227]\n"
"ds_read_b64_tr_b16 a[172:173],v107 offset:8200\n"
"ds_read_b64_tr_b16 a[174:175],v107 offset:12296\n"
"v_mfma_f32_16x16x32_bf16 v[224:227],v[48:51],a[120:123],v[224:227]\n"
"v_mfma_f32_16x16x32_bf16 v[228:231],v[56:59],a[120:123],v[228:231]\n"
"ds_read_b64_tr_b16 v[48:49],v100 offset:32768\n"
"v_mfma_f32_16x16x32_bf16 v[228:231],v[60:63],a[124:127],v[228:231]\n"
"ds_read_b64_tr_b16 v[50:51],v100 offset:33792\n"
"v_mfma_f32_16x16x32_bf16 v[196:199],v[60:63],a[116:119],v[196:199]\n"
"ds_read_b64_tr_b16 v[52:53],v100 offset:40960\n"
"v_mfma_f32_16x16x32_bf16 v[196:199],v[56:59],a[112:115],v[196:199]\n"
"s_add_u32 m0,s85,148480\n"
"ds_read_b64_tr_b16 v[54:55],v100 offset:41984\n"
"v_mfma_f32_16x16x32_bf16 v[164:167],v[56:59],a[104:107],v[164:167]\n"
"buffer_load_dwordx4 v111,s[28:31],s86 offen lds\n"
"v_mfma_f32_16x16x32_bf16 v[164:167],v[60:63],a[108:111],v[164:167]\n"
"s_add_u32 m0,s85,155648\n"
"s_xor_b32 s86,s86,262144\n"
"v_mfma_f32_16x16x32_bf16 v[132:135],v[60:63],a[100:103],v[132:135]\n"
"buffer_load_dwordx4 v110,s[28:31],s86 offen lds\n"
"v_mfma_f32_16x16x32_bf16 v[132:135],v[56:59],a[96:99],v[132:135]\n"
"v_mfma_f32_16x16x32_bf16 v[136:139],v[64:67],a[96:99],v[136:139]\n"
"ds_read_b64_tr_b16 v[56:57],v101 offset:32768\n"
"v_mfma_f32_16x16x32_bf16 v[136:139],v[68:71],a[100:103],v[136:139]\n"
"ds_read_b64_tr_b16 v[58:59],v101 offset:33792\n"
"v_mfma_f32_16x16x32_bf16 v[168:171],v[68:71],a[108:111],v[168:171]\n"
"ds_read_b64_tr_b16 v[60:61],v101 offset:40960\n"
"v_mfma_f32_16x16x32_bf16 v[168:171],v[64:67],a[104:107],v[168:171]\n"
"s_add_u32 m0,s85,156672\n"
"ds_read_b64_tr_b16 v[62:63],v101 offset:41984\n"
"v_mfma_f32_16x16x32_bf16 v[200:203],v[64:67],a[112:115],v[200:203]\n"
"buffer_load_dwordx4 v111,s[28:31],s86 offen lds\n"
"v_mfma_f32_16x16x32_bf16 v[200:203],v[68:71],a[116:119],v[200:203]\n"
"s_xor_b32 s86,s86,786432\n"
"v_mfma_f32_16x16x32_bf16 v[232:235],v[68:71],a[124:127],v[232:235]\n"
"v_mfma_f32_16x16x32_bf16 v[232:235],v[64:67],a[120:123],v[232:235]\n"
"v_mfma_f32_16x16x32_bf16 v[236:239],v[72:75],a[120:123],v[236:239]\n"
"ds_read_b64_tr_b16 v[64:65],v102 offset:32768\n"
"v_mfma_f32_16x16x32_bf16 v[236:239],v[76:79],a[124:127],v[236:239]\n"
"ds_read_b64_tr_b16 v[66:67],v102 offset:33792\n"
"v_mfma_f32_16x16x32_bf16 v[204:207],v[76:79],a[116:119],v[204:207]\n"
"ds_read_b64_tr_b16 v[68:69],v102 offset:40960\n"
"v_mfma_f32_16x16x32_bf16 v[204:207],v[72:75],a[112:115],v[204:207]\n"
"s_add_u32 m0,s78,65536\n"
"ds_read_b64_tr_b16 v[70:71],v102 offset:41984\n"
"v_mfma_f32_16x16x32_bf16 v[172:175],v[72:75],a[104:107],v[172:175]\n"
"buffer_load_dwordx4 v112,s[24:27],s76 offen offset:0 sc1 lds\n"
"v_mfma_f32_16x16x32_bf16 v[172:175],v[76:79],a[108:111],v[172:175]\n"
"v_mfma_f32_16x16x32_bf16 v[140:143],v[76:79],a[100:103],v[140:143]\n"
"buffer_load_dwordx4 v112,s[24:27],s76 offen offset:1024 sc1 lds\n"
"v_mfma_f32_16x16x32_bf16 v[140:143],v[72:75],a[96:99],v[140:143]\n"
"s_waitcnt vmcnt(18)\n"
"v_mfma_f32_16x16x32_bf16 v[144:147],v[80:83],a[96:99],v[144:147]\n"
"ds_read_b64_tr_b16 v[72:73],v103 offset:32768\n"
"v_mfma_f32_16x16x32_bf16 v[144:147],v[84:87],a[100:103],v[144:147]\n"
"ds_read_b64_tr_b16 v[74:75],v103 offset:33792\n"
"v_mfma_f32_16x16x32_bf16 v[176:179],v[84:87],a[108:111],v[176:179]\n"
"ds_read_b64_tr_b16 v[76:77],v103 offset:40960\n"
"v_mfma_f32_16x16x32_bf16 v[176:179],v[80:83],a[104:107],v[176:179]\n"
"ds_read_b64_tr_b16 v[78:79],v103 offset:41984\n"
"v_mfma_f32_16x16x32_bf16 v[208:211],v[80:83],a[112:115],v[208:211]\n"
"buffer_load_dwordx4 v112,s[24:27],s76 offen offset:2048 sc1 lds\n"
"v_mfma_f32_16x16x32_bf16 v[208:211],v[84:87],a[116:119],v[208:211]\n"
"ds_read_b128 v[0:3],v96 offset:0\n"
"v_mfma_f32_16x16x32_bf16 v[240:243],v[84:87],a[124:127],v[240:243]\n"
"v_mfma_f32_16x16x32_bf16 v[240:243],v[80:83],a[120:123],v[240:243]\n"
"ds_read_b128 v[8:11],v96 offset:1024\n"
"v_mfma_f32_16x16x32_bf16 v[244:247],v[88:91],a[120:123],v[244:247]\n"
"ds_read_b64_tr_b16 v[80:81],v104 offset:32768\n"
"v_mfma_f32_16x16x32_bf16 v[244:247],v[92:95],a[124:127],v[244:247]\n"
"ds_read_b64_tr_b16 v[82:83],v104 offset:33792\n"
"v_mfma_f32_16x16x32_bf16 v[212:215],v[92:95],a[116:119],v[212:215]\n"
"ds_read_b64_tr_b16 v[84:85],v104 offset:40960\n"
"v_mfma_f32_16x16x32_bf16 v[212:215],v[88:91],a[112:115],v[212:215]\n"
"ds_read_b64_tr_b16 v[86:87],v104 offset:41984\n"
"v_mfma_f32_16x16x32_bf16 v[180:183],v[88:91],a[104:107],v[180:183]\n"
"buffer_load_dwordx4 v112,s[24:27],s76 offen offset:3072 sc1 lds\n"
"v_mfma_f32_16x16x32_bf16 v[180:183],v[92:95],a[108:111],v[180:183]\n"
"s_add_u32 m0,s78,81920\n"
"s_xor_b32 s76,s76,16384\n"
"ds_read_b128 v[16:19],v96 offset:2048\n"
"v_mfma_f32_16x16x32_bf16 v[148:151],v[92:95],a[100:103],v[148:151]\n"
"buffer_load_dwordx4 v112,s[24:27],s76 offen offset:0 sc1 lds\n"
"v_mfma_f32_16x16x32_bf16 v[148:151],v[88:91],a[96:99],v[148:151]\n"
"ds_read_b128 v[24:27],v96 offset:3072\n"
"v_mfma_f32_16x16x32_bf16 v[152:155],a[128:131],a[96:99],v[152:155]\n"
"ds_read_b64_tr_b16 v[88:89],v105 offset:32768\n"
"v_mfma_f32_16x16x32_bf16 v[152:155],a[132:135],a[100:103],v[152:155]\n"
"ds_read_b64_tr_b16 v[90:91],v105 offset:33792\n"
"v_mfma_f32_16x16x32_bf16 v[184:187],a[132:135],a[108:111],v[184:187]\n"
"ds_read_b64_tr_b16 v[92:93],v105 offset:40960\n"
"v_mfma_f32_16x16x32_bf16 v[184:187],a[128:131],a[104:107],v[184:187]\n"
"ds_read_b64_tr_b16 v[94:95],v105 offset:41984\n"
"v_mfma_f32_16x16x32_bf16 v[216:219],a[128:131],a[112:115],v[216:219]\n"
"ds_read_b128 v[4:7],v96 offset:16384\n"
"v_mfma_f32_16x16x32_bf16 v[216:219],a[132:135],a[116:119],v[216:219]\n"
"ds_read_b128 v[12:15],v96 offset:17408\n"
"v_mfma_f32_16x16x32_bf16 v[248:251],a[132:135],a[124:127],v[248:251]\n"
"ds_read_b128 v[20:23],v96 offset:18432\n"
"v_mfma_f32_16x16x32_bf16 v[248:251],a[128:131],a[120:123],v[248:251]\n"
"ds_read_b128 v[28:31],v96 offset:19456\n"
"v_mfma_f32_16x16x32_bf16 v[252:255],a[136:139],a[120:123],v[252:255]\n"
"buffer_load_dwordx4 v112,s[24:27],s76 offen offset:1024 sc1 lds\n"
"v_mfma_f32_16x16x32_bf16 v[252:255],a[140:143],a[124:127],v[252:255]\n"
"v_mfma_f32_16x16x32_bf16 v[220:223],a[140:143],a[116:119],v[220:223]\n"
"v_mfma_f32_16x16x32_bf16 v[220:223],a[136:139],a[112:115],v[220:223]\n"
"buffer_load_dwordx4 v112,s[24:27],s76 offen offset:2048 sc1 lds\n"
"v_mfma_f32_16x16x32_bf16 v[188:191],a[136:139],a[104:107],v[188:191]\n"
"v_mfma_f32_16x16x32_bf16 v[188:191],a[140:143],a[108:111],v[188:191]\n"
"v_mfma_f32_16x16x32_bf16 v[156:159],a[140:143],a[100:103],v[156:159]\n"
"buffer_load_dwordx4 v112,s[24:27],s76 offen offset:3072 sc1 lds\n"
"v_mfma_f32_16x16x32_bf16 v[156:159],a[136:139],a[96:99],v[156:159]\n"
"s_xor_b32 s76,s76,49152\n"
"s_waitcnt vmcnt(20) lgkmcnt(0)\n"
"s_barrier\n"
"v_mfma_f32_16x16x32_bf16 v[128:131],v[32:35],v[0:3],v[128:131]\n"
"ds_read_b64_tr_b16 a[128:129],v98 offset:0\n"
"ds_read_b64_tr_b16 a[136:137],v99 offset:0\n"
"v_mfma_f32_16x16x32_bf16 v[128:131],v[36:39],v[4:7],v[128:131]\n"
"ds_read_b64_tr_b16 a[130:131],v98 offset:1024\n"
"ds_read_b64_tr_b16 a[138:139],v99 offset:1024\n"
"v_mfma_f32_16x16x32_bf16 v[160:163],v[36:39],v[12:15],v[160:163]\n"
"ds_read_b64_tr_b16 a[132:133],v98 offset:8192\n"
"ds_read_b64_tr_b16 a[140:141],v99 offset:8192\n"
"v_mfma_f32_16x16x32_bf16 v[160:163],v[32:35],v[8:11],v[160:163]\n"
"s_add_u32 m0,s85,131072\n"
"ds_read_b64_tr_b16 a[134:135],v98 offset:9216\n"
"ds_read_b64_tr_b16 a[142:143],v99 offset:9216\n"
"v_mfma_f32_16x16x32_bf16 v[192:195],v[32:35],v[16:19],v[192:195]\n"
"buffer_load_dwordx4 v110,s[28:31],s86 offen lds\n"
"v_mfma_f32_16x16x32_bf16 v[192:195],v[36:39],v[20:23],v[192:195]\n"
"s_add_u32 m0,s85,132096\n"
"v_mfma_f32_16x16x32_bf16 v[224:227],v[36:39],v[28:31],v[224:227]\n"
"buffer_load_dwordx4 v111,s[28:31],s86 offen lds\n"
"v_mfma_f32_16x16x32_bf16 v[224:227],v[32:35],v[24:27],v[224:227]\n"
"s_xor_b32 s86,s86,262144\n"
"v_mfma_f32_16x16x32_bf16 v[228:231],v[40:43],v[24:27],v[228:231]\n"
"ds_read_b64_tr_b16 v[32:33],v100 offset:0\n"
"v_mfma_f32_16x16x32_bf16 v[228:231],v[44:47],v[28:31],v[228:231]\n"
"ds_read_b64_tr_b16 v[34:35],v100 offset:1024\n"
"v_mfma_f32_16x16x32_bf16 v[196:199],v[44:47],v[20:23],v[196:199]\n"
"ds_read_b64_tr_b16 v[36:37],v100 offset:8192\n"
"v_mfma_f32_16x16x32_bf16 v[196:199],v[40:43],v[16:19],v[196:199]\n"
"s_add_u32 m0,s85,139264\n"
"ds_read_b64_tr_b16 v[38:39],v100 offset:9216\n"
"v_mfma_f32_16x16x32_bf16 v[164:167],v[40:43],v[8:11],v[164:167]\n"
"buffer_load_dwordx4 v110,s[28:31],s86 offen lds\n"
"v_mfma_f32_16x16x32_bf16 v[164:167],v[44:47],v[12:15],v[164:167]\n"
"s_add_u32 m0,s85,140288\n"
"v_mfma_f32_16x16x32_bf16 v[132:135],v[44:47],v[4:7],v[132:135]\n"
"buffer_load_dwordx4 v111,s[28:31],s86 offen lds\n"
"v_mfma_f32_16x16x32_bf16 v[132:135],v[40:43],v[0:3],v[132:135]\n"
"s_xor_b32 s86,s86,1835008\n"
"v_mfma_f32_16x16x32_bf16 v[136:139],v[48:51],v[0:3],v[136:139]\n"
"ds_read_b64_tr_b16 v[40:41],v101 offset:0\n"
"v_mfma_f32_16x16x32_bf16 v[136:139],v[52:55],v[4:7],v[136:139]\n"
"ds_read_b64_tr_b16 v[42:43],v101 offset:1024\n"
"v_mfma_f32_16x16x32_bf16 v[168:171],v[52:55],v[12:15],v[168:171]\n"
"ds_read_b64_tr_b16 v[44:45],v101 offset:8192\n"
"v_mfma_f32_16x16x32_bf16 v[168:171],v[48:51],v[8:11],v[168:171]\n"
"s_add_u32 m0,s78,114688\n"
"ds_read_b64_tr_b16 v[46:47],v101 offset:9216\n"
"v_mfma_f32_16x16x32_bf16 v[200:203],v[48:51],v[16:19],v[200:203]\n"
"buffer_load_dwordx4 v106,s[4:7],s13 offen offset:0 nt lds\n"
"v_mfma_f32_16x16x32_bf16 v[200:203],v[52:55],v[20:23],v[200:203]\n"
"s_add_u32 s13,s13,7168\n"
"v_mfma_f32_16x16x32_bf16 v[232:235],v[52:55],v[28:31],v[232:235]\n"
"buffer_load_dwordx4 v106,s[4:7],s13 offen offset:1024 nt lds\n"
"v_mfma_f32_16x16x32_bf16 v[232:235],v[48:51],v[24:27],v[232:235]\n"
"s_add_u32 s13,s13,7168\n"
"v_mfma_f32_16x16x32_bf16 v[236:239],v[56:59],v[24:27],v[236:239]\n"
"ds_read_b64_tr_b16 v[48:49],v102 offset:0\n"
"v_mfma_f32_16x16x32_bf16 v[236:239],v[60:63],v[28:31],v[236:239]\n"
"ds_read_b64_tr_b16 v[50:51],v102 offset:1024\n"
"v_mfma_f32_16x16x32_bf16 v[204:207],v[60:63],v[20:23],v[204:207]\n"
"ds_read_b64_tr_b16 v[52:53],v102 offset:8192\n"
"v_mfma_f32_16x16x32_bf16 v[204:207],v[56:59],v[16:19],v[204:207]\n"
"ds_read_b64_tr_b16 v[54:55],v102 offset:9216\n"
"v_mfma_f32_16x16x32_bf16 v[172:175],v[56:59],v[8:11],v[172:175]\n"
"buffer_load_dwordx4 v106,s[4:7],s13 offen offset:2048 nt lds\n"
"v_mfma_f32_16x16x32_bf16 v[172:175],v[60:63],v[12:15],v[172:175]\n"
"s_add_u32 s13,s13,7168\n"
"v_mfma_f32_16x16x32_bf16 v[140:143],v[60:63],v[4:7],v[140:143]\n"
"buffer_load_dwordx4 v106,s[4:7],s13 offen offset:3072 nt lds\n"
"v_mfma_f32_16x16x32_bf16 v[140:143],v[56:59],v[0:3],v[140:143]\n"
"s_add_u32 s13,s13,109568\n"
"s_waitcnt vmcnt(20)\n"
"v_mfma_f32_16x16x32_bf16 v[144:147],v[64:67],v[0:3],v[144:147]\n"
"ds_read_b64_tr_b16 v[56:57],v103 offset:0\n"
"v_mfma_f32_16x16x32_bf16 v[144:147],v[68:71],v[4:7],v[144:147]\n"
"ds_read_b64_tr_b16 v[58:59],v103 offset:1024\n"
"v_mfma_f32_16x16x32_bf16 v[176:179],v[68:71],v[12:15],v[176:179]\n"
"ds_read_b64_tr_b16 v[60:61],v103 offset:8192\n"
"v_mfma_f32_16x16x32_bf16 v[176:179],v[64:67],v[8:11],v[176:179]\n"
"s_add_u32 m0,s78,0\n"
"ds_read_b64_tr_b16 v[62:63],v103 offset:9216\n"
"v_mfma_f32_16x16x32_bf16 v[208:211],v[64:67],v[16:19],v[208:211]\n"
"buffer_load_dwordx4 v112,s[24:27],s76 offen offset:0 sc1 lds\n"
"v_mfma_f32_16x16x32_bf16 v[208:211],v[68:71],v[20:23],v[208:211]\n"
"ds_read_b128 a[96:99],v96 offset:32768\n"
"v_mfma_f32_16x16x32_bf16 v[240:243],v[68:71],v[28:31],v[240:243]\n"
"buffer_load_dwordx4 v112,s[24:27],s76 offen offset:1024 sc1 lds\n"
"v_mfma_f32_16x16x32_bf16 v[240:243],v[64:67],v[24:27],v[240:243]\n"
"ds_read_b128 a[104:107],v96 offset:33792\n"
"v_mfma_f32_16x16x32_bf16 v[244:247],v[72:75],v[24:27],v[244:247]\n"
"ds_read_b64_tr_b16 v[64:65],v104 offset:0\n"
"v_mfma_f32_16x16x32_bf16 v[244:247],v[76:79],v[28:31],v[244:247]\n"
"ds_read_b64_tr_b16 v[66:67],v104 offset:1024\n"
"v_mfma_f32_16x16x32_bf16 v[212:215],v[76:79],v[20:23],v[212:215]\n"
"ds_read_b64_tr_b16 v[68:69],v104 offset:8192\n"
"v_mfma_f32_16x16x32_bf16 v[212:215],v[72:75],v[16:19],v[212:215]\n"
"ds_read_b64_tr_b16 v[70:71],v104 offset:9216\n"
"v_mfma_f32_16x16x32_bf16 v[180:183],v[72:75],v[8:11],v[180:183]\n"
"buffer_load_dwordx4 v112,s[24:27],s76 offen offset:2048 sc1 lds\n"
"v_mfma_f32_16x16x32_bf16 v[180:183],v[76:79],v[12:15],v[180:183]\n"
"ds_read_b128 a[112:115],v96 offset:34816\n"
"v_mfma_f32_16x16x32_bf16 v[148:151],v[76:79],v[4:7],v[148:151]\n"
"buffer_load_dwordx4 v112,s[24:27],s76 offen offset:3072 sc1 lds\n"
"v_mfma_f32_16x16x32_bf16 v[148:151],v[72:75],v[0:3],v[148:151]\n"
"s_xor_b32 s76,s76,16384\n"
"ds_read_b128 a[120:123],v96 offset:35840\n"
"v_mfma_f32_16x16x32_bf16 v[152:155],v[80:83],v[0:3],v[152:155]\n"
"ds_read_b64_tr_b16 v[72:73],v105 offset:0\n"
"v_mfma_f32_16x16x32_bf16 v[152:155],v[84:87],v[4:7],v[152:155]\n"
"ds_read_b64_tr_b16 v[74:75],v105 offset:1024\n"
"v_mfma_f32_16x16x32_bf16 v[184:187],v[84:87],v[12:15],v[184:187]\n"
"ds_read_b64_tr_b16 v[76:77],v105 offset:8192\n"
"v_mfma_f32_16x16x32_bf16 v[184:187],v[80:83],v[8:11],v[184:187]\n"
"ds_read_b64_tr_b16 v[78:79],v105 offset:9216\n"
"v_mfma_f32_16x16x32_bf16 v[216:219],v[80:83],v[16:19],v[216:219]\n"
"ds_read_b128 a[100:103],v96 offset:49152\n"
"v_mfma_f32_16x16x32_bf16 v[216:219],v[84:87],v[20:23],v[216:219]\n"
"ds_read_b128 a[108:111],v96 offset:50176\n"
"ds_read_b128 a[116:119],v96 offset:51200\n"
"v_mfma_f32_16x16x32_bf16 v[248:251],v[84:87],v[28:31],v[248:251]\n"
"s_load_dwordx2 s[32:33],s[0:1],56\n"
"s_load_dwordx2 s[36:37],s[0:1],64\n"
"s_load_dwordx2 s[40:41],s[0:1],72\n"
"v_mfma_f32_16x16x32_bf16 v[248:251],v[80:83],v[24:27],v[248:251]\n"
"s_add_u32 m0,s78,16384\n"
"ds_read_b128 a[124:127],v96 offset:52224\n"
"v_mfma_f32_16x16x32_bf16 v[252:255],v[88:91],v[24:27],v[252:255]\n"
"buffer_load_dwordx4 v112,s[24:27],s76 offen offset:0 sc1 lds\n"
"v_mfma_f32_16x16x32_bf16 v[252:255],v[92:95],v[28:31],v[252:255]\n"
"v_mfma_f32_16x16x32_bf16 v[220:223],v[92:95],v[20:23],v[220:223]\n"
"buffer_load_dwordx4 v112,s[24:27],s76 offen offset:1024 sc1 lds\n"
"v_mfma_f32_16x16x32_bf16 v[220:223],v[88:91],v[16:19],v[220:223]\n"
"v_mfma_f32_16x16x32_bf16 v[188:191],v[88:91],v[8:11],v[188:191]\n"
"buffer_load_dwordx4 v112,s[24:27],s76 offen offset:2048 sc1 lds\n"
"v_mfma_f32_16x16x32_bf16 v[188:191],v[92:95],v[12:15],v[188:191]\n"
"v_mfma_f32_16x16x32_bf16 v[156:159],v[92:95],v[4:7],v[156:159]\n"
"buffer_load_dwordx4 v112,s[24:27],s76 offen offset:3072 sc1 lds\n"
"v_mfma_f32_16x16x32_bf16 v[156:159],v[88:91],v[0:3],v[156:159]\n"
"s_xor_b32 s76,s76,114688\n"
"s_waitcnt vmcnt(24) lgkmcnt(0)\n"
"s_barrier\n"
"v_mfma_f32_16x16x32_bf16 v[128:131],a[128:131],a[96:99],v[128:131]\n"
"ds_read_b64_tr_b16 v[80:81],v98 offset:49152\n"
"ds_read_b64_tr_b16 v[88:89],v99 offset:49152\n"
"v_mfma_f32_16x16x32_bf16 v[128:131],a[132:135],a[100:103],v[128:131]\n"
"ds_read_b64_tr_b16 v[82:83],v98 offset:50176\n"
"ds_read_b64_tr_b16 v[90:91],v99 offset:50176\n"
"v_mfma_f32_16x16x32_bf16 v[160:163],a[132:135],a[108:111],v[160:163]\n"
"ds_read_b64_tr_b16 v[84:85],v98 offset:57344\n"
"ds_read_b64_tr_b16 v[92:93],v99 offset:57344\n"
"v_mfma_f32_16x16x32_bf16 v[160:163],a[128:131],a[104:107],v[160:163]\n"
"s_add_u32 m0,s85,98304\n"
"ds_read_b64_tr_b16 v[86:87],v98 offset:58368\n"
"ds_read_b64_tr_b16 v[94:95],v99 offset:58368\n"
"v_mfma_f32_16x16x32_bf16 v[192:195],a[128:131],a[112:115],v[192:195]\n"
"buffer_load_dwordx4 v110,s[28:31],s86 offen lds\n"
"v_mfma_f32_16x16x32_bf16 v[192:195],a[132:135],a[116:119],v[192:195]\n"
"v_accvgpr_write_b32 a154,v124\n"
"v_accvgpr_write_b32 a155,v125\n"
"v_mfma_f32_16x16x32_bf16 v[224:227],a[132:135],a[124:127],v[224:227]\n"
"v_accvgpr_write_b32 a152,1\n"
"v_lshrrev_b32 v124,2,v112\n"
"v_mfma_f32_16x16x32_bf16 v[224:227],a[128:131],a[120:123],v[224:227]\n"
"v_add_u32 v124,147456,v124\n"
"v_mov_b32 v125,0\n"
"v_mfma_f32_16x16x32_bf16 v[228:231],a[136:139],a[120:123],v[228:231]\n"
"ds_read_b64_tr_b16 a[128:129],v100 offset:49152\n"
"s_mov_b32 s101,0\n"
"v_mfma_f32_16x16x32_bf16 v[228:231],a[140:143],a[124:127],v[228:231]\n"
"ds_read_b64_tr_b16 a[130:131],v100 offset:50176\n"
"v_mfma_f32_16x16x32_bf16 v[196:199],a[140:143],a[116:119],v[196:199]\n"
"ds_read_b64_tr_b16 a[132:133],v100 offset:57344\n"
"v_mfma_f32_16x16x32_bf16 v[196:199],a[136:139],a[112:115],v[196:199]\n"
"s_add_u32 m0,s85,99328\n"
"ds_read_b64_tr_b16 a[134:135],v100 offset:58368\n"
"v_mfma_f32_16x16x32_bf16 v[164:167],a[136:139],a[104:107],v[164:167]\n"
"buffer_load_dwordx4 v111,s[28:31],s86 offen lds\n"
"v_mfma_f32_16x16x32_bf16 v[164:167],a[140:143],a[108:111],v[164:167]\n"
"s_add_u32 m0,s85,106496\n"
"s_xor_b32 s86,s86,262144\n"
"v_mfma_f32_16x16x32_bf16 v[132:135],a[140:143],a[100:103],v[132:135]\n"
"buffer_load_dwordx4 v110,s[28:31],s86 offen lds\n"
"v_mfma_f32_16x16x32_bf16 v[132:135],a[136:139],a[96:99],v[132:135]\n"
"v_mfma_f32_16x16x32_bf16 v[136:139],v[32:35],a[96:99],v[136:139]\n"
"ds_read_b64_tr_b16 a[136:137],v101 offset:49152\n"
"v_mfma_f32_16x16x32_bf16 v[136:139],v[36:39],a[100:103],v[136:139]\n"
"ds_read_b64_tr_b16 a[138:139],v101 offset:50176\n"
"v_mfma_f32_16x16x32_bf16 v[168:171],v[36:39],a[108:111],v[168:171]\n"
"ds_read_b64_tr_b16 a[140:141],v101 offset:57344\n"
"v_mfma_f32_16x16x32_bf16 v[168:171],v[32:35],a[104:107],v[168:171]\n"
"s_add_u32 m0,s85,107520\n"
"ds_read_b64_tr_b16 a[142:143],v101 offset:58368\n"
"v_mfma_f32_16x16x32_bf16 v[200:203],v[32:35],a[112:115],v[200:203]\n"
"buffer_load_dwordx4 v111,s[28:31],s86 offen lds\n"
"v_mfma_f32_16x16x32_bf16 v[200:203],v[36:39],a[116:119],v[200:203]\n"
"s_xor_b32 s86,s86,786432\n"
"v_mfma_f32_16x16x32_bf16 v[232:235],v[36:39],a[124:127],v[232:235]\n"
"v_mfma_f32_16x16x32_bf16 v[232:235],v[32:35],a[120:123],v[232:235]\n"
"v_mfma_f32_16x16x32_bf16 v[236:239],v[40:43],a[120:123],v[236:239]\n"
"ds_read_b64_tr_b16 v[32:33],v102 offset:49152\n"
"v_mfma_f32_16x16x32_bf16 v[236:239],v[44:47],a[124:127],v[236:239]\n"
"ds_read_b64_tr_b16 v[34:35],v102 offset:50176\n"
"v_mfma_f32_16x16x32_bf16 v[204:207],v[44:47],a[116:119],v[204:207]\n"
"ds_read_b64_tr_b16 v[36:37],v102 offset:57344\n"
"v_mfma_f32_16x16x32_bf16 v[204:207],v[40:43],a[112:115],v[204:207]\n"
"s_add_u32 m0,s78,32768\n"
"ds_read_b64_tr_b16 v[38:39],v102 offset:58368\n"
"buffer_load_dwordx4 v112,s[24:27],s76 offen offset:0 sc1 lds\n"
"v_mfma_f32_16x16x32_bf16 v[172:175],v[40:43],a[104:107],v[172:175]\n"
"buffer_load_dwordx2 v[118:119],v120,s[24:27],s63 offen sc1\n"
"v_mfma_f32_16x16x32_bf16 v[172:175],v[44:47],a[108:111],v[172:175]\n"
"v_mfma_f32_16x16x32_bf16 v[140:143],v[44:47],a[100:103],v[140:143]\n"
"buffer_load_dwordx4 v112,s[24:27],s76 offen offset:1024 sc1 lds\n"
"v_mfma_f32_16x16x32_bf16 v[140:143],v[40:43],a[96:99],v[140:143]\n"
"s_waitcnt vmcnt(23)\n"
"v_mfma_f32_16x16x32_bf16 v[144:147],v[48:51],a[96:99],v[144:147]\n"
"ds_read_b64_tr_b16 v[40:41],v103 offset:49152\n"
"s_and_b32 s82,s100,7\n"
"v_mfma_f32_16x16x32_bf16 v[144:147],v[52:55],a[100:103],v[144:147]\n"
"ds_read_b64_tr_b16 v[42:43],v103 offset:50176\n"
"s_mul_i32 s82,s82,28\n"
"v_mfma_f32_16x16x32_bf16 v[176:179],v[52:55],a[108:111],v[176:179]\n"
"ds_read_b64_tr_b16 v[44:45],v103 offset:57344\n"
"s_lshr_b32 s83,s100,3\n"
"v_mfma_f32_16x16x32_bf16 v[176:179],v[48:51],a[104:107],v[176:179]\n"
"ds_read_b64_tr_b16 v[46:47],v103 offset:58368\n"
"s_add_u32 s23,s82,s83\n"
"v_mfma_f32_16x16x32_bf16 v[208:211],v[48:51],a[112:115],v[208:211]\n"
"buffer_load_dwordx4 v112,s[24:27],s76 offen offset:2048 sc1 lds\n"
"v_mfma_f32_16x16x32_bf16 v[208:211],v[52:55],a[116:119],v[208:211]\n"
"s_cmp_lt_u32 s83,28\n"
"s_cselect_b32 s34,1827872,0\n"
"s_cselect_b32 s42,8192,0\n"
"s_cselect_b32 s23,s23,0\n"
"v_mfma_f32_16x16x32_bf16 v[240:243],v[52:55],a[124:127],v[240:243]\n"
"s_mov_b32 s35,0x20000\n"
"ds_read_b128 v[0:3],v97 offset:0\n"
"s_mov_b32 s43,0x20000\n"
"v_mfma_f32_16x16x32_bf16 v[240:243],v[48:51],a[120:123],v[240:243]\n"
"ds_read_b128 v[8:11],v97 offset:1024\n"
"s_lshl2_add_u32 s82,s83,s52\n"
"v_mfma_f32_16x16x32_bf16 v[244:247],v[56:59],a[120:123],v[244:247]\n"
"ds_read_b64_tr_b16 v[48:49],v104 offset:49152\n"
"s_mul_i32 s82,s82,57344\n"
"v_mfma_f32_16x16x32_bf16 v[244:247],v[60:63],a[124:127],v[244:247]\n"
"ds_read_b64_tr_b16 v[50:51],v104 offset:50176\n"
"s_and_b32 s83,s100,7\n"
"v_mfma_f32_16x16x32_bf16 v[212:215],v[60:63],a[116:119],v[212:215]\n"
"ds_read_b64_tr_b16 v[52:53],v104 offset:57344\n"
"s_mul_i32 s83,s83,896\n"
"v_mfma_f32_16x16x32_bf16 v[212:215],v[56:59],a[112:115],v[212:215]\n"
"ds_read_b64_tr_b16 v[54:55],v104 offset:58368\n"
"s_add_u32 s82,s82,s83\n"
"v_mfma_f32_16x16x32_bf16 v[180:183],v[56:59],a[104:107],v[180:183]\n"
"buffer_load_dwordx4 v112,s[24:27],s76 offen offset:3072 sc1 lds\n"
"v_mfma_f32_16x16x32_bf16 v[180:183],v[60:63],a[108:111],v[180:183]\n"
"s_add_u32 m0,s78,49152\n"
"s_xor_b32 s76,s76,16384\n"
"ds_read_b128 v[16:19],v97 offset:2048\n"
"v_mfma_f32_16x16x32_bf16 v[148:151],v[60:63],a[100:103],v[148:151]\n"
"buffer_load_dwordx4 v112,s[24:27],s76 offen offset:0 sc1 lds\n"
"v_mfma_f32_16x16x32_bf16 v[148:151],v[56:59],a[96:99],v[148:151]\n"
"s_add_u32 s44,s32,s82\n"
"ds_read_b128 v[24:27],v97 offset:3072\n"
"s_addc_u32 s45,s33,0\n"
"v_mfma_f32_16x16x32_bf16 v[152:155],v[64:67],a[96:99],v[152:155]\n"
"s_add_u32 s48,s36,s82\n"
"ds_read_b64_tr_b16 v[56:57],v105 offset:49152\n"
"s_addc_u32 s49,s37,0\n"
"v_mfma_f32_16x16x32_bf16 v[152:155],v[68:71],a[100:103],v[152:155]\n"
"ds_read_b64_tr_b16 v[58:59],v105 offset:50176\n"
"s_mov_b32 s46,51072\n"
"v_mfma_f32_16x16x32_bf16 v[184:187],v[68:71],a[108:111],v[184:187]\n"
"ds_read_b64_tr_b16 v[60:61],v105 offset:57344\n"
"s_mov_b32 s50,s46\n"
"v_mfma_f32_16x16x32_bf16 v[184:187],v[64:67],a[104:107],v[184:187]\n"
"ds_read_b64_tr_b16 v[62:63],v105 offset:58368\n"
"s_mov_b32 s47,0x20000\n"
"v_mfma_f32_16x16x32_bf16 v[216:219],v[64:67],a[112:115],v[216:219]\n"
"ds_read_b128 v[4:7],v97 offset:16384\n"
"s_mov_b32 s51,0x20000\n"
"v_mfma_f32_16x16x32_bf16 v[216:219],v[68:71],a[116:119],v[216:219]\n"
"ds_read_b128 v[12:15],v97 offset:17408\n"
"s_mul_i32 s82,s52,1835008\n"
"v_mfma_f32_16x16x32_bf16 v[248:251],v[68:71],a[124:127],v[248:251]\n"
"ds_read_b128 v[20:23],v97 offset:18432\n"
"s_lshl_b32 s83,s23,5\n"
"v_mfma_f32_16x16x32_bf16 v[248:251],v[64:67],a[120:123],v[248:251]\n"
"ds_read_b128 v[28:31],v97 offset:19456\n"
"s_add_u32 s82,s82,s83\n"
"v_mfma_f32_16x16x32_bf16 v[252:255],v[72:75],a[120:123],v[252:255]\n"
"buffer_load_dwordx4 v112,s[24:27],s76 offen offset:1024 sc1 lds\n"
"v_mfma_f32_16x16x32_bf16 v[252:255],v[76:79],a[124:127],v[252:255]\n"
"s_add_u32 s32,s32,s82\n"
"s_addc_u32 s33,s33,0\n"
"s_add_u32 s36,s36,s82\n"
"v_mfma_f32_16x16x32_bf16 v[220:223],v[76:79],a[116:119],v[220:223]\n"
"s_addc_u32 s37,s37,0\n"
"s_lshl2_add_u32 s82,s23,s52\n"
"s_lshl_b32 s82,s82,13\n"
"v_mfma_f32_16x16x32_bf16 v[220:223],v[72:75],a[112:115],v[220:223]\n"
"buffer_load_dwordx4 v112,s[24:27],s76 offen offset:2048 sc1 lds\n"
"v_mfma_f32_16x16x32_bf16 v[188:191],v[72:75],a[104:107],v[188:191]\n"
"s_add_u32 s40,s40,s82\n"
"s_addc_u32 s41,s41,0\n"
"v_lshrrev_b32 v121,5,v112\n"
"v_mfma_f32_16x16x32_bf16 v[188:191],v[76:79],a[108:111],v[188:191]\n"
"v_mul_u32_u24 v121,7168,v121\n"
"v_and_b32 v122,16,v112\n"
"v_mfma_f32_16x16x32_bf16 v[156:159],v[76:79],a[100:103],v[156:159]\n"
"buffer_load_dwordx4 v112,s[24:27],s76 offen offset:3072 sc1 lds\n"
"v_mfma_f32_16x16x32_bf16 v[156:159],v[72:75],a[96:99],v[156:159]\n"
"s_xor_b32 s76,s76,49152\n"
"v_add_u32 v115,v121,v122\n"
"s_waitcnt vmcnt(7) lgkmcnt(0)\n"
"s_mov_b32 s84,0x40000\n"
".Lfk_kv_chk:\n"
"v_xor_b32 v116,s80,v118\n"
"v_xor_b32 v117,s81,v119\n"
"v_or_b32 v116,v116,v117\n"
"v_cmp_ne_u32 vcc,0,v116\n"
"s_nop 0\n"
"s_cbranch_vccz .Lfk_kv_ok\n"
"s_sub_u32 s84,s84,1\n"
"s_cbranch_scc1 .Lfk_kv_ok\n"
"s_sleep 4\n"
"buffer_load_dwordx2 v[118:119],v120,s[24:27],s63 offen sc1\n"
"s_waitcnt vmcnt(0)\n"
"s_branch .Lfk_kv_chk\n"
".Lfk_kv_ok:\n"
"s_barrier\n"
"v_mfma_f32_16x16x32_bf16 v[128:131],v[80:83],v[0:3],v[128:131]\n"
"ds_read_b64_tr_b16 v[64:65],v98 offset:32768\n"
"ds_read_b64_tr_b16 v[72:73],v99 offset:32768\n"
"ds_read_b64_tr_b16 a[176:177],v107 offset:0\n"
"v_mfma_f32_16x16x32_bf16 v[128:131],v[84:87],v[4:7],v[128:131]\n"
"ds_read_b64_tr_b16 v[66:67],v98 offset:33792\n"
"ds_read_b64_tr_b16 v[74:75],v99 offset:33792\n"
"ds_read_b64_tr_b16 a[178:179],v107 offset:4096\n"
"v_mfma_f32_16x16x32_bf16 v[160:163],v[84:87],v[12:15],v[160:163]\n"
"ds_read_b64_tr_b16 v[68:69],v98 offset:40960\n"
"ds_read_b64_tr_b16 v[76:77],v99 offset:40960\n"
"ds_read_b64_tr_b16 a[180:181],v107 offset:8\n"
"v_mfma_f32_16x16x32_bf16 v[160:163],v[80:83],v[8:11],v[160:163]\n"
"s_add_u32 m0,s85,147456\n"
"ds_read_b64_tr_b16 v[70:71],v98 offset:41984\n"
"ds_read_b64_tr_b16 v[78:79],v99 offset:41984\n"
"v_mfma_f32_16x16x32_bf16 v[192:195],v[80:83],v[16:19],v[192:195]\n"
"buffer_load_dwordx4 v110,s[28:31],s86 offen lds\n"
"ds_read_b64_tr_b16 a[182:183],v107 offset:4104\n"
"v_mfma_f32_16x16x32_bf16 v[192:195],v[84:87],v[20:23],v[192:195]\n"
"s_add_u32 m0,s85,148480\n"
"ds_read_b64_tr_b16 a[184:185],v107 offset:8192\n"
"ds_read_b64_tr_b16 a[186:187],v107 offset:12288\n"
"v_mfma_f32_16x16x32_bf16 v[224:227],v[84:87],v[28:31],v[224:227]\n"
"buffer_load_dwordx4 v111,s[28:31],s86 offen lds\n"
"v_mfma_f32_16x16x32_bf16 v[224:227],v[80:83],v[24:27],v[224:227]\n"
"s_xor_b32 s86,s86,262144\n"
"ds_read_b64_tr_b16 a[188:189],v107 offset:8200\n"
"ds_read_b64_tr_b16 a[190:191],v107 offset:12296\n"
"v_mfma_f32_16x16x32_bf16 v[228:231],v[88:91],v[24:27],v[228:231]\n"
"ds_read_b64_tr_b16 v[80:81],v100 offset:32768\n"
"s_mov_b32 s54,-229376\n"
"v_mfma_f32_16x16x32_bf16 v[228:231],v[92:95],v[28:31],v[228:231]\n"
"ds_read_b64_tr_b16 v[82:83],v100 offset:33792\n"
"s_lshl_b32 s55,s52,10\n"
"v_mfma_f32_16x16x32_bf16 v[196:199],v[92:95],v[20:23],v[196:199]\n"
"ds_read_b64_tr_b16 v[84:85],v100 offset:40960\n"
"s_add_u32 s55,s55,131072\n"
"v_mfma_f32_16x16x32_bf16 v[196:199],v[88:91],v[16:19],v[196:199]\n"
"s_add_u32 m0,s85,155648\n"
"ds_read_b64_tr_b16 v[86:87],v100 offset:41984\n"
"v_mfma_f32_16x16x32_bf16 v[164:167],v[88:91],v[8:11],v[164:167]\n"
"buffer_load_dwordx4 v110,s[28:31],s86 offen lds\n"
"v_mfma_f32_16x16x32_bf16 v[164:167],v[92:95],v[12:15],v[164:167]\n"
"s_add_u32 m0,s85,156672\n"
"s_mov_b32 s22,229376\n"
"v_mfma_f32_16x16x32_bf16 v[132:135],v[92:95],v[4:7],v[132:135]\n"
"buffer_load_dwordx4 v111,s[28:31],s86 offen lds\n"
"v_mfma_f32_16x16x32_bf16 v[132:135],v[88:91],v[0:3],v[132:135]\n"
"s_xor_b32 s86,s86,16515072\n"
"v_lshrrev_b32 v121,7,v112\n"
"v_mfma_f32_16x16x32_bf16 v[136:139],a[128:131],v[0:3],v[136:139]\n"
"ds_read_b64_tr_b16 v[88:89],v101 offset:32768\n"
"v_mul_u32_u24 v121,7168,v121\n"
"v_mfma_f32_16x16x32_bf16 v[136:139],a[132:135],v[4:7],v[136:139]\n"
"ds_read_b64_tr_b16 v[90:91],v101 offset:33792\n"
"v_lshlrev_b32 v123,3,v112\n"
"v_mfma_f32_16x16x32_bf16 v[168:171],a[132:135],v[12:15],v[168:171]\n"
"ds_read_b64_tr_b16 v[92:93],v101 offset:40960\n"
"v_and_b32 v122,0x380,v123\n"
"v_mfma_f32_16x16x32_bf16 v[168:171],a[128:131],v[8:11],v[168:171]\n"
"s_add_u32 m0,s85,81920\n"
"s_add_u32 s77,s73,16384\n"
"ds_read_b64_tr_b16 v[94:95],v101 offset:41984\n"
"v_mfma_f32_16x16x32_bf16 v[200:203],a[128:131],v[16:19],v[200:203]\n"
"buffer_load_dwordx4 v112,s[24:27],s77 offen offset:0 sc1 lds\n"
"v_mfma_f32_16x16x32_bf16 v[200:203],a[132:135],v[20:23],v[200:203]\n"
"v_min_u32 v122,0x300,v122\n"
"v_add_u32 v122,v121,v122\n"
"v_mfma_f32_16x16x32_bf16 v[232:235],a[132:135],v[28:31],v[232:235]\n"
"buffer_load_dwordx4 v112,s[24:27],s77 offen offset:1024 sc1 lds\n"
"v_mfma_f32_16x16x32_bf16 v[232:235],a[128:131],v[24:27],v[232:235]\n"
"v_mfma_f32_16x16x32_bf16 v[236:239],a[136:139],v[24:27],v[236:239]\n"
"ds_read_b64_tr_b16 a[128:129],v102 offset:32768\n"
"v_mfma_f32_16x16x32_bf16 v[236:239],a[140:143],v[28:31],v[236:239]\n"
"ds_read_b64_tr_b16 a[130:131],v102 offset:33792\n"
"v_mfma_f32_16x16x32_bf16 v[204:207],a[140:143],v[20:23],v[204:207]\n"
"ds_read_b64_tr_b16 a[132:133],v102 offset:40960\n"
"v_mfma_f32_16x16x32_bf16 v[204:207],a[136:139],v[16:19],v[204:207]\n"
"s_add_u32 m0,s85,65536\n"
"s_add_u32 s77,s73,65536\n"
"ds_read_b64_tr_b16 a[134:135],v102 offset:41984\n"
"v_mfma_f32_16x16x32_bf16 v[172:175],a[136:139],v[8:11],v[172:175]\n"
"buffer_load_dwordx4 v112,s[24:27],s77 offen offset:0 sc1 lds\n"
"v_mfma_f32_16x16x32_bf16 v[172:175],a[140:143],v[12:15],v[172:175]\n"
"v_mfma_f32_16x16x32_bf16 v[140:143],a[140:143],v[4:7],v[140:143]\n"
"buffer_load_dwordx4 v112,s[24:27],s77 offen offset:1024 sc1 lds\n"
"v_mfma_f32_16x16x32_bf16 v[140:143],a[136:139],v[0:3],v[140:143]\n"
"v_mfma_f32_16x16x32_bf16 v[144:147],v[32:35],v[0:3],v[144:147]\n"
"ds_read_b64_tr_b16 a[136:137],v103 offset:32768\n"
"v_mfma_f32_16x16x32_bf16 v[144:147],v[36:39],v[4:7],v[144:147]\n"
"ds_read_b64_tr_b16 a[138:139],v103 offset:33792\n"
"v_mfma_f32_16x16x32_bf16 v[176:179],v[36:39],v[12:15],v[176:179]\n"
"ds_read_b64_tr_b16 a[140:141],v103 offset:40960\n"
"v_mfma_f32_16x16x32_bf16 v[176:179],v[32:35],v[8:11],v[176:179]\n"
"s_add_u32 m0,s85,73728\n"
"s_add_u32 s77,s73,24576\n"
"ds_read_b64_tr_b16 a[142:143],v103 offset:41984\n"
"v_mfma_f32_16x16x32_bf16 v[208:211],v[32:35],v[16:19],v[208:211]\n"
"buffer_load_dwordx4 v112,s[24:27],s77 offen offset:0 sc1 lds\n"
"v_mfma_f32_16x16x32_bf16 v[208:211],v[36:39],v[20:23],v[208:211]\n"
"ds_read_b128 a[96:99],v96 offset:0\n"
"v_mfma_f32_16x16x32_bf16 v[240:243],v[36:39],v[28:31],v[240:243]\n"
"buffer_load_dwordx4 v112,s[24:27],s77 offen offset:1024 sc1 lds\n"
"v_mfma_f32_16x16x32_bf16 v[240:243],v[32:35],v[24:27],v[240:243]\n"
"ds_read_b128 a[104:107],v96 offset:1024\n"
"v_mfma_f32_16x16x32_bf16 v[244:247],v[40:43],v[24:27],v[244:247]\n"
"ds_read_b64_tr_b16 v[32:33],v104 offset:32768\n"
"v_mfma_f32_16x16x32_bf16 v[244:247],v[44:47],v[28:31],v[244:247]\n"
"ds_read_b64_tr_b16 v[34:35],v104 offset:33792\n"
"v_mfma_f32_16x16x32_bf16 v[212:215],v[44:47],v[20:23],v[212:215]\n"
"ds_read_b64_tr_b16 v[36:37],v104 offset:40960\n"
"v_mfma_f32_16x16x32_bf16 v[212:215],v[40:43],v[16:19],v[212:215]\n"
"ds_read_b64_tr_b16 v[38:39],v104 offset:41984\n"
"v_mfma_f32_16x16x32_bf16 v[180:183],v[40:43],v[8:11],v[180:183]\n"
"v_mfma_f32_16x16x32_bf16 v[180:183],v[44:47],v[12:15],v[180:183]\n"
"ds_read_b128 a[112:115],v96 offset:2048\n"
"v_mfma_f32_16x16x32_bf16 v[148:151],v[44:47],v[4:7],v[148:151]\n"
"v_mfma_f32_16x16x32_bf16 v[148:151],v[40:43],v[0:3],v[148:151]\n"
"ds_read_b128 a[120:123],v96 offset:3072\n"
"v_mfma_f32_16x16x32_bf16 v[152:155],v[48:51],v[0:3],v[152:155]\n"
"ds_read_b64_tr_b16 v[40:41],v105 offset:32768\n"
"v_mfma_f32_16x16x32_bf16 v[152:155],v[52:55],v[4:7],v[152:155]\n"
"ds_read_b64_tr_b16 v[42:43],v105 offset:33792\n"
"v_mfma_f32_16x16x32_bf16 v[184:187],v[52:55],v[12:15],v[184:187]\n"
"ds_read_b64_tr_b16 v[44:45],v105 offset:40960\n"
"v_mfma_f32_16x16x32_bf16 v[184:187],v[48:51],v[8:11],v[184:187]\n"
"ds_read_b64_tr_b16 v[46:47],v105 offset:41984\n"
"v_mfma_f32_16x16x32_bf16 v[216:219],v[48:51],v[16:19],v[216:219]\n"
"ds_read_b128 a[100:103],v96 offset:16384\n"
"v_mfma_f32_16x16x32_bf16 v[216:219],v[52:55],v[20:23],v[216:219]\n"
"ds_read_b128 a[108:111],v96 offset:17408\n"
"v_mfma_f32_16x16x32_bf16 v[248:251],v[52:55],v[28:31],v[248:251]\n"
"ds_read_b128 a[116:119],v96 offset:18432\n"
"v_mfma_f32_16x16x32_bf16 v[248:251],v[48:51],v[24:27],v[248:251]\n"
"ds_read_b128 a[124:127],v96 offset:19456\n"
"v_mfma_f32_16x16x32_bf16 v[252:255],v[56:59],v[24:27],v[252:255]\n"
"v_mfma_f32_16x16x32_bf16 v[252:255],v[60:63],v[28:31],v[252:255]\n"
"v_mfma_f32_16x16x32_bf16 v[220:223],v[60:63],v[20:23],v[220:223]\n"
"v_mfma_f32_16x16x32_bf16 v[220:223],v[56:59],v[16:19],v[220:223]\n"
"v_mfma_f32_16x16x32_bf16 v[188:191],v[56:59],v[8:11],v[188:191]\n"
"v_mfma_f32_16x16x32_bf16 v[188:191],v[60:63],v[12:15],v[188:191]\n"
"v_mfma_f32_16x16x32_bf16 v[156:159],v[60:63],v[4:7],v[156:159]\n"
"v_mfma_f32_16x16x32_bf16 v[156:159],v[56:59],v[0:3],v[156:159]\n"
"s_waitcnt lgkmcnt(0)\n"
"s_barrier\n"
"v_mfma_f32_16x16x32_bf16 v[128:131],v[64:67],a[96:99],v[128:131]\n"
"ds_read_b64_tr_b16 v[48:49],v98 offset:0\n"
"ds_read_b64_tr_b16 v[56:57],v99 offset:0\n"
"v_mfma_f32_16x16x32_bf16 v[128:131],v[68:71],a[100:103],v[128:131]\n"
"ds_read_b64_tr_b16 v[50:51],v98 offset:1024\n"
"ds_read_b64_tr_b16 v[58:59],v99 offset:1024\n"
"v_mfma_f32_16x16x32_bf16 v[160:163],v[68:71],a[108:111],v[160:163]\n"
"ds_read_b64_tr_b16 v[52:53],v98 offset:8192\n"
"ds_read_b64_tr_b16 v[60:61],v99 offset:8192\n"
"v_mfma_f32_16x16x32_bf16 v[160:163],v[64:67],a[104:107],v[160:163]\n"
"s_add_u32 m0,s78,0\n"
"ds_read_b64_tr_b16 v[54:55],v98 offset:9216\n"
"ds_read_b64_tr_b16 v[62:63],v99 offset:9216\n"
"v_mfma_f32_16x16x32_bf16 v[192:195],v[64:67],a[112:115],v[192:195]\n"
"buffer_load_dwordx4 v112,s[24:27],s76 offen offset:0 sc1 lds\n"
"v_mfma_f32_16x16x32_bf16 v[192:195],v[68:71],a[116:119],v[192:195]\n"
"v_mfma_f32_16x16x32_bf16 v[224:227],v[68:71],a[124:127],v[224:227]\n"
"v_mfma_f32_16x16x32_bf16 v[224:227],v[64:67],a[120:123],v[224:227]\n"
"v_mfma_f32_16x16x32_bf16 v[228:231],v[72:75],a[120:123],v[228:231]\n"
"ds_read_b64_tr_b16 v[64:65],v100 offset:0\n"
"v_mfma_f32_16x16x32_bf16 v[228:231],v[76:79],a[124:127],v[228:231]\n"
"ds_read_b64_tr_b16 v[66:67],v100 offset:1024\n"
"v_mfma_f32_16x16x32_bf16 v[196:199],v[76:79],a[116:119],v[196:199]\n"
"ds_read_b64_tr_b16 v[68:69],v100 offset:8192\n"
"v_mfma_f32_16x16x32_bf16 v[196:199],v[72:75],a[112:115],v[196:199]\n"
"ds_read_b64_tr_b16 v[70:71],v100 offset:9216\n"
"v_mfma_f32_16x16x32_bf16 v[164:167],v[72:75],a[104:107],v[164:167]\n"
"buffer_load_dwordx4 v112,s[24:27],s76 offen offset:1024 sc1 lds\n"
"v_mfma_f32_16x16x32_bf16 v[164:167],v[76:79],a[108:111],v[164:167]\n"
"v_mfma_f32_16x16x32_bf16 v[132:135],v[76:79],a[100:103],v[132:135]\n"
"buffer_load_dwordx4 v112,s[24:27],s76 offen offset:2048 sc1 lds\n"
"v_mfma_f32_16x16x32_bf16 v[132:135],v[72:75],a[96:99],v[132:135]\n"
"v_mfma_f32_16x16x32_bf16 v[136:139],v[80:83],a[96:99],v[136:139]\n"
"ds_read_b64_tr_b16 v[72:73],v101 offset:0\n"
"v_mfma_f32_16x16x32_bf16 v[136:139],v[84:87],a[100:103],v[136:139]\n"
"ds_read_b64_tr_b16 v[74:75],v101 offset:1024\n"
"v_mfma_f32_16x16x32_bf16 v[168:171],v[84:87],a[108:111],v[168:171]\n"
"ds_read_b64_tr_b16 v[76:77],v101 offset:8192\n"
"v_mfma_f32_16x16x32_bf16 v[168:171],v[80:83],a[104:107],v[168:171]\n"
"ds_read_b64_tr_b16 v[78:79],v101 offset:9216\n"
"v_mfma_f32_16x16x32_bf16 v[200:203],v[80:83],a[112:115],v[200:203]\n"
"buffer_load_dwordx4 v112,s[24:27],s76 offen offset:3072 sc1 lds\n"
"v_mfma_f32_16x16x32_bf16 v[200:203],v[84:87],a[116:119],v[200:203]\n"
"s_xor_b32 s76,s76,16384\n"
"v_mfma_f32_16x16x32_bf16 v[232:235],v[84:87],a[124:127],v[232:235]\n"
"v_mfma_f32_16x16x32_bf16 v[232:235],v[80:83],a[120:123],v[232:235]\n"
"v_mfma_f32_16x16x32_bf16 v[236:239],v[88:91],a[120:123],v[236:239]\n"
"ds_read_b64_tr_b16 v[80:81],v102 offset:0\n"
"v_mfma_f32_16x16x32_bf16 v[236:239],v[92:95],a[124:127],v[236:239]\n"
"ds_read_b64_tr_b16 v[82:83],v102 offset:1024\n"
"v_mfma_f32_16x16x32_bf16 v[204:207],v[92:95],a[116:119],v[204:207]\n"
"ds_read_b64_tr_b16 v[84:85],v102 offset:8192\n"
"v_mfma_f32_16x16x32_bf16 v[204:207],v[88:91],a[112:115],v[204:207]\n"
"s_add_u32 m0,s78,16384\n"
"ds_read_b64_tr_b16 v[86:87],v102 offset:9216\n"
"v_mfma_f32_16x16x32_bf16 v[172:175],v[88:91],a[104:107],v[172:175]\n"
"buffer_load_dwordx4 v112,s[24:27],s76 offen offset:0 sc1 lds\n"
"v_mfma_f32_16x16x32_bf16 v[172:175],v[92:95],a[108:111],v[172:175]\n"
"v_mfma_f32_16x16x32_bf16 v[140:143],v[92:95],a[100:103],v[140:143]\n"
"buffer_load_dwordx4 v112,s[24:27],s76 offen offset:1024 sc1 lds\n"
"v_mfma_f32_16x16x32_bf16 v[140:143],v[88:91],a[96:99],v[140:143]\n"
".Lfk_q27:\n"
"s_waitcnt vmcnt(16)\n"
"v_mfma_f32_16x16x32_bf16 v[144:147],a[128:131],a[96:99],v[144:147]\n"
"ds_read_b64_tr_b16 v[88:89],v103 offset:0\n"
"v_mfma_f32_16x16x32_bf16 v[144:147],a[132:135],a[100:103],v[144:147]\n"
"ds_read_b64_tr_b16 v[90:91],v103 offset:1024\n"
"v_mfma_f32_16x16x32_bf16 v[176:179],a[132:135],a[108:111],v[176:179]\n"
"ds_read_b64_tr_b16 v[92:93],v103 offset:8192\n"
"v_mfma_f32_16x16x32_bf16 v[176:179],a[128:131],a[104:107],v[176:179]\n"
"ds_read_b64_tr_b16 v[94:95],v103 offset:9216\n"
"v_mfma_f32_16x16x32_bf16 v[208:211],a[128:131],a[112:115],v[208:211]\n"
"buffer_load_dwordx4 v112,s[24:27],s76 offen offset:2048 sc1 lds\n"
"v_mfma_f32_16x16x32_bf16 v[208:211],a[132:135],a[116:119],v[208:211]\n"
"ds_read_b128 v[0:3],v96 offset:32768\n"
"v_mfma_f32_16x16x32_bf16 v[240:243],a[132:135],a[124:127],v[240:243]\n"
"v_mfma_f32_16x16x32_bf16 v[240:243],a[128:131],a[120:123],v[240:243]\n"
"ds_read_b128 v[8:11],v96 offset:33792\n"
"v_mfma_f32_16x16x32_bf16 v[244:247],a[136:139],a[120:123],v[244:247]\n"
"ds_read_b64_tr_b16 a[128:129],v104 offset:0\n"
"v_mfma_f32_16x16x32_bf16 v[244:247],a[140:143],a[124:127],v[244:247]\n"
"ds_read_b64_tr_b16 a[130:131],v104 offset:1024\n"
"v_mfma_f32_16x16x32_bf16 v[212:215],a[140:143],a[116:119],v[212:215]\n"
"ds_read_b64_tr_b16 a[132:133],v104 offset:8192\n"
"v_mfma_f32_16x16x32_bf16 v[212:215],a[136:139],a[112:115],v[212:215]\n"
"ds_read_b64_tr_b16 a[134:135],v104 offset:9216\n"
"v_mfma_f32_16x16x32_bf16 v[180:183],a[136:139],a[104:107],v[180:183]\n"
"buffer_load_dwordx4 v112,s[24:27],s76 offen offset:3072 sc1 lds\n"
"v_mfma_f32_16x16x32_bf16 v[180:183],a[140:143],a[108:111],v[180:183]\n"
"s_add_u32 m0,s85,122880\n"
"s_add_u32 s77,s73,0\n"
"ds_read_b128 v[16:19],v96 offset:34816\n"
"v_mfma_f32_16x16x32_bf16 v[148:151],a[140:143],a[100:103],v[148:151]\n"
"buffer_load_dwordx4 v112,s[24:27],s77 offen offset:0 sc1 lds\n"
"v_mfma_f32_16x16x32_bf16 v[148:151],a[136:139],a[96:99],v[148:151]\n"
"s_xor_b32 s76,s76,1032192\n"
"ds_read_b128 v[24:27],v96 offset:35840\n"
"v_mfma_f32_16x16x32_bf16 v[152:155],v[32:35],a[96:99],v[152:155]\n"
"ds_read_b64_tr_b16 a[136:137],v105 offset:0\n"
"v_mfma_f32_16x16x32_bf16 v[152:155],v[36:39],a[100:103],v[152:155]\n"
"ds_read_b64_tr_b16 a[138:139],v105 offset:1024\n"
"v_mfma_f32_16x16x32_bf16 v[184:187],v[36:39],a[108:111],v[184:187]\n"
"ds_read_b64_tr_b16 a[140:141],v105 offset:8192\n"
"v_mfma_f32_16x16x32_bf16 v[184:187],v[32:35],a[104:107],v[184:187]\n"
"ds_read_b64_tr_b16 a[142:143],v105 offset:9216\n"
"v_mfma_f32_16x16x32_bf16 v[216:219],v[32:35],a[112:115],v[216:219]\n"
"ds_read_b128 v[4:7],v96 offset:49152\n"
"v_mfma_f32_16x16x32_bf16 v[216:219],v[36:39],a[116:119],v[216:219]\n"
"ds_read_b128 v[12:15],v96 offset:50176\n"
"v_mfma_f32_16x16x32_bf16 v[248:251],v[36:39],a[124:127],v[248:251]\n"
"ds_read_b128 v[20:23],v96 offset:51200\n"
"v_mfma_f32_16x16x32_bf16 v[248:251],v[32:35],a[120:123],v[248:251]\n"
"ds_read_b128 v[28:31],v96 offset:52224\n"
"v_mfma_f32_16x16x32_bf16 v[252:255],v[40:43],a[120:123],v[252:255]\n"
"buffer_load_dwordx4 v112,s[24:27],s77 offen offset:1024 sc1 lds\n"
"v_mfma_f32_16x16x32_bf16 v[252:255],v[44:47],a[124:127],v[252:255]\n"
"v_mfma_f32_16x16x32_bf16 v[220:223],v[44:47],a[116:119],v[220:223]\n"
"s_add_u32 m0,s85,114688\n"
"s_add_u32 s77,s73,32768\n"
"v_mfma_f32_16x16x32_bf16 v[220:223],v[40:43],a[112:115],v[220:223]\n"
"buffer_load_dwordx4 v112,s[24:27],s77 offen offset:0 sc1 lds\n"
"v_mfma_f32_16x16x32_bf16 v[188:191],v[40:43],a[104:107],v[188:191]\n"
"v_mfma_f32_16x16x32_bf16 v[188:191],v[44:47],a[108:111],v[188:191]\n"
"v_mfma_f32_16x16x32_bf16 v[156:159],v[44:47],a[100:103],v[156:159]\n"
"buffer_load_dwordx4 v112,s[24:27],s77 offen offset:1024 sc1 lds\n"
"v_mfma_f32_16x16x32_bf16 v[156:159],v[40:43],a[96:99],v[156:159]\n"
"s_waitcnt vmcnt(18) lgkmcnt(0)\n"
"s_barrier\n"
"v_mfma_f32_16x16x32_bf16 v[128:131],v[48:51],v[0:3],v[128:131]\n"
"ds_read_b64_tr_b16 v[32:33],v98 offset:49152\n"
"ds_read_b64_tr_b16 v[40:41],v99 offset:49152\n"
"v_mfma_f32_16x16x32_bf16 v[128:131],v[52:55],v[4:7],v[128:131]\n"
"ds_read_b64_tr_b16 v[34:35],v98 offset:50176\n"
"ds_read_b64_tr_b16 v[42:43],v99 offset:50176\n"
"v_mfma_f32_16x16x32_bf16 v[160:163],v[52:55],v[12:15],v[160:163]\n"
"s_add_u32 m0,s85,98304\n"
"ds_read_b64_tr_b16 v[36:37],v98 offset:57344\n"
"ds_read_b64_tr_b16 v[44:45],v99 offset:57344\n"
"v_mfma_f32_16x16x32_bf16 v[160:163],v[48:51],v[8:11],v[160:163]\n"
"s_add_u32 s77,s73,49152\n"
"ds_read_b64_tr_b16 v[38:39],v98 offset:58368\n"
"ds_read_b64_tr_b16 v[46:47],v99 offset:58368\n"
"v_mfma_f32_16x16x32_bf16 v[192:195],v[48:51],v[16:19],v[192:195]\n"
"buffer_load_dwordx4 v112,s[24:27],s77 offen offset:0 sc1 lds\n"
"v_mfma_f32_16x16x32_bf16 v[192:195],v[52:55],v[20:23],v[192:195]\n"
"v_mfma_f32_16x16x32_bf16 v[224:227],v[52:55],v[28:31],v[224:227]\n"
"buffer_load_dwordx4 v112,s[24:27],s77 offen offset:1024 sc1 lds\n"
"v_mfma_f32_16x16x32_bf16 v[224:227],v[48:51],v[24:27],v[224:227]\n"
"v_mfma_f32_16x16x32_bf16 v[228:231],v[56:59],v[24:27],v[228:231]\n"
"ds_read_b64_tr_b16 v[48:49],v100 offset:49152\n"
"v_mfma_f32_16x16x32_bf16 v[228:231],v[60:63],v[28:31],v[228:231]\n"
"ds_read_b64_tr_b16 v[50:51],v100 offset:50176\n"
"v_mfma_f32_16x16x32_bf16 v[196:199],v[60:63],v[20:23],v[196:199]\n"
"ds_read_b64_tr_b16 v[52:53],v100 offset:57344\n"
"v_mfma_f32_16x16x32_bf16 v[196:199],v[56:59],v[16:19],v[196:199]\n"
"s_add_u32 m0,s85,106496\n"
"s_add_u32 s77,s73,8192\n"
"ds_read_b64_tr_b16 v[54:55],v100 offset:58368\n"
"v_mfma_f32_16x16x32_bf16 v[164:167],v[56:59],v[8:11],v[164:167]\n"
"buffer_load_dwordx4 v112,s[24:27],s77 offen offset:0 sc1 lds\n"
"v_mfma_f32_16x16x32_bf16 v[164:167],v[60:63],v[12:15],v[164:167]\n"
"v_mfma_f32_16x16x32_bf16 v[132:135],v[60:63],v[4:7],v[132:135]\n"
"buffer_load_dwordx4 v112,s[24:27],s77 offen offset:1024 sc1 lds\n"
"v_mfma_f32_16x16x32_bf16 v[132:135],v[56:59],v[0:3],v[132:135]\n"
"v_mfma_f32_16x16x32_bf16 v[136:139],v[64:67],v[0:3],v[136:139]\n"
"ds_read_b64_tr_b16 v[56:57],v101 offset:49152\n"
"v_mfma_f32_16x16x32_bf16 v[136:139],v[68:71],v[4:7],v[136:139]\n"
"ds_read_b64_tr_b16 v[58:59],v101 offset:50176\n"
"v_mfma_f32_16x16x32_bf16 v[168:171],v[68:71],v[12:15],v[168:171]\n"
"ds_read_b64_tr_b16 v[60:61],v101 offset:57344\n"
"v_mfma_f32_16x16x32_bf16 v[168:171],v[64:67],v[8:11],v[168:171]\n"
"ds_read_b64_tr_b16 v[62:63],v101 offset:58368\n"
"v_mfma_f32_16x16x32_bf16 v[200:203],v[64:67],v[16:19],v[200:203]\n"
"buffer_load_dword a148,v122,s[48:51],0 offen\n"
"v_mfma_f32_16x16x32_bf16 v[200:203],v[68:71],v[20:23],v[200:203]\n"
"v_mfma_f32_16x16x32_bf16 v[232:235],v[68:71],v[28:31],v[232:235]\n"
"v_mfma_f32_16x16x32_bf16 v[232:235],v[64:67],v[24:27],v[232:235]\n"
"v_mfma_f32_16x16x32_bf16 v[236:239],v[72:75],v[24:27],v[236:239]\n"
"ds_read_b64_tr_b16 v[64:65],v102 offset:49152\n"
"v_mfma_f32_16x16x32_bf16 v[236:239],v[76:79],v[28:31],v[236:239]\n"
"ds_read_b64_tr_b16 v[66:67],v102 offset:50176\n"
"v_mfma_f32_16x16x32_bf16 v[204:207],v[76:79],v[20:23],v[204:207]\n"
"ds_read_b64_tr_b16 v[68:69],v102 offset:57344\n"
"v_mfma_f32_16x16x32_bf16 v[204:207],v[72:75],v[16:19],v[204:207]\n"
"ds_read_b64_tr_b16 v[70:71],v102 offset:58368\n"
"v_mfma_f32_16x16x32_bf16 v[172:175],v[72:75],v[8:11],v[172:175]\n"
"v_mfma_f32_16x16x32_bf16 v[172:175],v[76:79],v[12:15],v[172:175]\n"
"v_mfma_f32_16x16x32_bf16 v[140:143],v[76:79],v[4:7],v[140:143]\n"
"v_mfma_f32_16x16x32_bf16 v[140:143],v[72:75],v[0:3],v[140:143]\n"
"s_waitcnt vmcnt(9)\n"
"v_mfma_f32_16x16x32_bf16 v[144:147],v[80:83],v[0:3],v[144:147]\n"
"ds_read_b64_tr_b16 v[72:73],v103 offset:49152\n"
"s_mov_b32 s90,0\n"
"v_mfma_f32_16x16x32_bf16 v[144:147],v[84:87],v[4:7],v[144:147]\n"
"ds_read_b64_tr_b16 v[74:75],v103 offset:50176\n"
"s_mov_b32 s16,0xc21c0000\n"
"v_mfma_f32_16x16x32_bf16 v[176:179],v[84:87],v[12:15],v[176:179]\n"
"ds_read_b64_tr_b16 v[76:77],v103 offset:57344\n"
"s_mov_b32 s17,0x41a00000\n"
"v_mfma_f32_16x16x32_bf16 v[176:179],v[80:83],v[8:11],v[176:179]\n"
"ds_read_b64_tr_b16 v[78:79],v103 offset:58368\n"
"s_mov_b32 s89,0\n"
"v_mfma_f32_16x16x32_bf16 v[208:211],v[80:83],v[16:19],v[208:211]\n"
"s_xor_b32 s82,s78,8192\n"
"v_add_u32 v116,s82,v112\n"
"v_mfma_f32_16x16x32_bf16 v[208:211],v[84:87],v[20:23],v[208:211]\n"
"ds_read_b128 a[96:99],v96 offset:0\n"
"v_add_u32 v117,4096,v112\n"
"v_mfma_f32_16x16x32_bf16 v[240:243],v[84:87],v[28:31],v[240:243]\n"
"s_getpc_b64 s[20:21]\n"
".Lfk_evp:\n"
"s_add_u32 s20,s20,.Lfk_ev-.Lfk_evp\n"
"s_addc_u32 s21,s21,0\n"
"v_mfma_f32_16x16x32_bf16 v[240:243],v[80:83],v[24:27],v[240:243]\n"
"ds_read_b128 a[104:107],v96 offset:1024\n"
"v_mfma_f32_16x16x32_bf16 v[244:247],v[88:91],v[24:27],v[244:247]\n"
"ds_read_b64_tr_b16 v[80:81],v104 offset:49152\n"
"v_mfma_f32_16x16x32_bf16 v[244:247],v[92:95],v[28:31],v[244:247]\n"
"ds_read_b64_tr_b16 v[82:83],v104 offset:50176\n"
"v_mfma_f32_16x16x32_bf16 v[212:215],v[92:95],v[20:23],v[212:215]\n"
"ds_read_b64_tr_b16 v[84:85],v104 offset:57344\n"
"v_mfma_f32_16x16x32_bf16 v[212:215],v[88:91],v[16:19],v[212:215]\n"
"ds_read_b64_tr_b16 v[86:87],v104 offset:58368\n"
"v_mfma_f32_16x16x32_bf16 v[180:183],v[88:91],v[8:11],v[180:183]\n"
"v_mfma_f32_16x16x32_bf16 v[180:183],v[92:95],v[12:15],v[180:183]\n"
"ds_read_b128 a[112:115],v96 offset:2048\n"
"v_mfma_f32_16x16x32_bf16 v[148:151],v[92:95],v[4:7],v[148:151]\n"
"v_mfma_f32_16x16x32_bf16 v[148:151],v[88:91],v[0:3],v[148:151]\n"
"ds_read_b128 a[120:123],v96 offset:3072\n"
"v_mfma_f32_16x16x32_bf16 v[152:155],a[128:131],v[0:3],v[152:155]\n"
"ds_read_b64_tr_b16 v[88:89],v105 offset:49152\n"
"v_mfma_f32_16x16x32_bf16 v[152:155],a[132:135],v[4:7],v[152:155]\n"
"ds_read_b64_tr_b16 v[90:91],v105 offset:50176\n"
"v_mfma_f32_16x16x32_bf16 v[184:187],a[132:135],v[12:15],v[184:187]\n"
"ds_read_b64_tr_b16 v[92:93],v105 offset:57344\n"
"v_mfma_f32_16x16x32_bf16 v[184:187],a[128:131],v[8:11],v[184:187]\n"
"ds_read_b64_tr_b16 v[94:95],v105 offset:58368\n"
"v_mfma_f32_16x16x32_bf16 v[216:219],a[128:131],v[16:19],v[216:219]\n"
"ds_read_b128 a[100:103],v96 offset:16384\n"
"v_mfma_f32_16x16x32_bf16 v[216:219],a[132:135],v[20:23],v[216:219]\n"
"ds_read_b128 a[108:111],v96 offset:17408\n"
"v_add_u32 v113,65536,v112\n"
"v_mfma_f32_16x16x32_bf16 v[248:251],a[132:135],v[28:31],v[248:251]\n"
"ds_read_b128 a[116:119],v96 offset:18432\n"
"v_add_u32 v114,65536,v112\n"
"v_mfma_f32_16x16x32_bf16 v[248:251],a[128:131],v[24:27],v[248:251]\n"
"ds_read_b128 a[124:127],v96 offset:19456\n"
"v_mfma_f32_16x16x32_bf16 v[252:255],a[136:139],v[24:27],v[252:255]\n"
"v_mfma_f32_16x16x32_bf16 v[252:255],a[140:143],v[28:31],v[252:255]\n"
"v_mfma_f32_16x16x32_bf16 v[220:223],a[140:143],v[20:23],v[220:223]\n"
"v_mfma_f32_16x16x32_bf16 v[220:223],a[136:139],v[16:19],v[220:223]\n"
"v_mfma_f32_16x16x32_bf16 v[188:191],a[136:139],v[8:11],v[188:191]\n"
"v_mfma_f32_16x16x32_bf16 v[188:191],a[140:143],v[12:15],v[188:191]\n"
"v_mfma_f32_16x16x32_bf16 v[156:159],a[140:143],v[4:7],v[156:159]\n"
"v_mfma_f32_16x16x32_bf16 v[156:159],a[136:139],v[0:3],v[156:159]\n"
"s_waitcnt vmcnt(7) lgkmcnt(0)\n"
"s_barrier\n"
"v_mfma_f32_16x16x32_bf16 v[224:227],v[32:35],a[120:123],v[224:227]\n"
"ds_read_b128 a[64:67],v113 offset:57344\n"
"v_mov_b32 v96,0xc1980000\n"
"v_mfma_f32_16x16x32_bf16 v[224:227],v[36:39],a[124:127],v[224:227]\n"
"ds_read_b128 a[68:71],v113 offset:58368\n"
"v_mov_b32 v97,0xc1980000\n"
"v_mfma_f32_16x16x32_bf16 v[228:231],v[44:47],a[124:127],v[228:231]\n"
"ds_read_b128 a[72:75],v113 offset:59392\n"
"v_mov_b32 v98,0xc1980000\n"
"v_mfma_f32_16x16x32_bf16 v[228:231],v[40:43],a[120:123],v[228:231]\n"
"ds_read_b128 a[76:79],v113 offset:60416\n"
"v_mov_b32 v99,0xc1980000\n"
"v_mfma_f32_16x16x32_bf16 v[232:235],v[48:51],a[120:123],v[232:235]\n"
"ds_read_b128 a[80:83],v113 offset:61440\n"
"v_mov_b32 v100,0xc1980000\n"
"v_mfma_f32_16x16x32_bf16 v[232:235],v[52:55],a[124:127],v[232:235]\n"
"ds_read_b128 a[84:87],v113 offset:62464\n"
"v_mov_b32 v101,0xc1980000\n"
"v_mfma_f32_16x16x32_bf16 v[236:239],v[60:63],a[124:127],v[236:239]\n"
"ds_read_b128 a[88:91],v113 offset:63488\n"
"v_mov_b32 v102,0xc1980000\n"
"v_mfma_f32_16x16x32_bf16 v[236:239],v[56:59],a[120:123],v[236:239]\n"
"ds_read_b128 a[92:95],v113 offset:64512\n"
"v_mov_b32 v103,0xc1980000\n"
"v_mfma_f32_16x16x32_bf16 v[240:243],v[64:67],a[120:123],v[240:243]\n"
"v_mov_b32 v104,0xc1980000\n"
"v_mov_b32 v105,0xc1980000\n"
"v_mfma_f32_16x16x32_bf16 v[240:243],v[68:71],a[124:127],v[240:243]\n"
"ds_write_b32 v124,v125\n"
"v_mfma_f32_16x16x32_bf16 v[244:247],v[76:79],a[124:127],v[244:247]\n"
"v_mov_b32 v107,0xc1980000\n"
"v_mov_b32 v108,0xc1980000\n"
"v_mfma_f32_16x16x32_bf16 v[244:247],v[72:75],a[120:123],v[244:247]\n"
"v_mov_b32 v109,0xc1980000\n"
"v_mov_b32 v110,0xc1980000\n"
"v_mfma_f32_16x16x32_bf16 v[248:251],v[80:83],a[120:123],v[248:251]\n"
"v_mov_b32 v106,0xc1980000\n"
"v_mov_b32 v111,0xc1980000\n"
"v_mfma_f32_16x16x32_bf16 v[248:251],v[84:87],a[124:127],v[248:251]\n"
"v_mfma_f32_16x16x32_bf16 v[252:255],v[92:95],a[124:127],v[252:255]\n"
"v_mfma_f32_16x16x32_bf16 v[252:255],v[88:91],a[120:123],v[252:255]\n"
"v_mfma_f32_16x16x32_bf16 v[128:131],v[32:35],a[96:99],v[128:131]\n"
"v_mfma_f32_16x16x32_bf16 v[128:131],v[36:39],a[100:103],v[128:131]\n"
"v_cvt_pk_bf16_f32 v232,v232,v233\n"
"v_cvt_pk_bf16_f32 v233,v234,v235\n"
"v_mfma_f32_16x16x32_bf16 v[132:135],v[44:47],a[100:103],v[132:135]\n"
"v_cvt_pk_bf16_f32 v224,v224,v225\n"
"v_cvt_pk_bf16_f32 v225,v226,v227\n"
"v_mfma_f32_16x16x32_bf16 v[132:135],v[40:43],a[96:99],v[132:135]\n"
"v_cvt_pk_bf16_f32 v226,v228,v229\n"
"v_cvt_pk_bf16_f32 v227,v230,v231\n"
"v_mfma_f32_16x16x32_bf16 v[136:139],v[48:51],a[96:99],v[136:139]\n"
"ds_write_b128 v116,v[224:227] offset:0\n"
"v_mfma_f32_16x16x32_bf16 v[136:139],v[52:55],a[100:103],v[136:139]\n"
"v_cvt_pk_bf16_f32 v234,v236,v237\n"
"v_cvt_pk_bf16_f32 v235,v238,v239\n"
"v_mfma_f32_16x16x32_bf16 v[140:143],v[60:63],a[100:103],v[140:143]\n"
"ds_write_b128 v116,v[232:235] offset:1024\n"
"v_mfma_f32_16x16x32_bf16 v[140:143],v[56:59],a[96:99],v[140:143]\n"
"v_cvt_pk_bf16_f32 v240,v240,v241\n"
"v_cvt_pk_bf16_f32 v241,v242,v243\n"
"v_mfma_f32_16x16x32_bf16 v[144:147],v[64:67],a[96:99],v[144:147]\n"
"v_cvt_pk_bf16_f32 v242,v244,v245\n"
"v_cvt_pk_bf16_f32 v243,v246,v247\n"
"v_mfma_f32_16x16x32_bf16 v[144:147],v[68:71],a[100:103],v[144:147]\n"
"ds_write_b128 v116,v[240:243] offset:2048\n"
"v_cvt_pk_bf16_f32 v248,v248,v249\n"
"v_mfma_f32_16x16x32_bf16 v[148:151],v[76:79],a[100:103],v[148:151]\n"
"v_cvt_pk_bf16_f32 v249,v250,v251\n"
"v_cvt_pk_bf16_f32 v250,v252,v253\n"
"v_cvt_pk_bf16_f32 v251,v254,v255\n"
"v_mfma_f32_16x16x32_bf16 v[148:151],v[72:75],a[96:99],v[148:151]\n"
"ds_write_b128 v116,v[248:251] offset:3072\n"
"v_mfma_f32_16x16x32_bf16 v[152:155],v[80:83],a[96:99],v[152:155]\n"
"ds_read_b128 a[48:51],v116 offset:0\n"
"ds_read_b128 a[52:55],v116 offset:1024\n"
"v_mfma_f32_16x16x32_bf16 v[152:155],v[84:87],a[100:103],v[152:155]\n"
"ds_read_b128 a[56:59],v116 offset:2048\n"
"ds_read_b128 a[60:63],v116 offset:3072\n"
"v_mfma_f32_16x16x32_bf16 v[156:159],v[92:95],a[100:103],v[156:159]\n"
"v_mfma_f32_16x16x32_bf16 v[156:159],v[88:91],a[96:99],v[156:159]\n"
"v_mfma_f32_16x16x32_bf16 v[160:163],v[32:35],a[104:107],v[160:163]\n"
"v_mfma_f32_16x16x32_bf16 v[160:163],v[36:39],a[108:111],v[160:163]\n"
"v_cvt_pk_bf16_f32 v136,v136,v137\n"
"v_cvt_pk_bf16_f32 v137,v138,v139\n"
"v_mfma_f32_16x16x32_bf16 v[164:167],v[44:47],a[108:111],v[164:167]\n"
"v_cvt_pk_bf16_f32 v128,v128,v129\n"
"v_cvt_pk_bf16_f32 v129,v130,v131\n"
"v_mfma_f32_16x16x32_bf16 v[164:167],v[40:43],a[104:107],v[164:167]\n"
"v_cvt_pk_bf16_f32 v130,v132,v133\n"
"v_cvt_pk_bf16_f32 v131,v134,v135\n"
"v_mfma_f32_16x16x32_bf16 v[168:171],v[48:51],a[104:107],v[168:171]\n"
"ds_write_b128 v116,v[128:131] offset:16384\n"
"v_mfma_f32_16x16x32_bf16 v[168:171],v[52:55],a[108:111],v[168:171]\n"
"v_cvt_pk_bf16_f32 v138,v140,v141\n"
"v_cvt_pk_bf16_f32 v139,v142,v143\n"
"v_mfma_f32_16x16x32_bf16 v[172:175],v[60:63],a[108:111],v[172:175]\n"
"ds_write_b128 v116,v[136:139] offset:17408\n"
"v_mfma_f32_16x16x32_bf16 v[172:175],v[56:59],a[104:107],v[172:175]\n"
"v_cvt_pk_bf16_f32 v144,v144,v145\n"
"v_cvt_pk_bf16_f32 v145,v146,v147\n"
"v_mfma_f32_16x16x32_bf16 v[176:179],v[64:67],a[104:107],v[176:179]\n"
"v_cvt_pk_bf16_f32 v146,v148,v149\n"
"v_cvt_pk_bf16_f32 v147,v150,v151\n"
"v_mfma_f32_16x16x32_bf16 v[176:179],v[68:71],a[108:111],v[176:179]\n"
"ds_write_b128 v116,v[144:147] offset:18432\n"
"v_cvt_pk_bf16_f32 v152,v152,v153\n"
"v_mfma_f32_16x16x32_bf16 v[180:183],v[76:79],a[108:111],v[180:183]\n"
"v_cvt_pk_bf16_f32 v153,v154,v155\n"
"v_cvt_pk_bf16_f32 v154,v156,v157\n"
"v_cvt_pk_bf16_f32 v155,v158,v159\n"
"v_mfma_f32_16x16x32_bf16 v[180:183],v[72:75],a[104:107],v[180:183]\n"
"ds_write_b128 v116,v[152:155] offset:19456\n"
"v_mfma_f32_16x16x32_bf16 v[184:187],v[80:83],a[104:107],v[184:187]\n"
"ds_read_b128 a[0:3],v116 offset:16384\n"
"ds_read_b128 a[4:7],v116 offset:17408\n"
"v_mfma_f32_16x16x32_bf16 v[184:187],v[84:87],a[108:111],v[184:187]\n"
"ds_read_b128 a[8:11],v116 offset:18432\n"
"ds_read_b128 a[12:15],v116 offset:19456\n"
"v_mfma_f32_16x16x32_bf16 v[188:191],v[92:95],a[108:111],v[188:191]\n"
"v_mfma_f32_16x16x32_bf16 v[188:191],v[88:91],a[104:107],v[188:191]\n"
"v_mfma_f32_16x16x32_bf16 v[192:195],v[32:35],a[112:115],v[192:195]\n"
"v_mfma_f32_16x16x32_bf16 v[192:195],v[36:39],a[116:119],v[192:195]\n"
"v_cvt_pk_bf16_f32 v168,v168,v169\n"
"v_cvt_pk_bf16_f32 v169,v170,v171\n"
"v_mfma_f32_16x16x32_bf16 v[196:199],v[44:47],a[116:119],v[196:199]\n"
"v_cvt_pk_bf16_f32 v160,v160,v161\n"
"v_cvt_pk_bf16_f32 v161,v162,v163\n"
"v_mfma_f32_16x16x32_bf16 v[196:199],v[40:43],a[112:115],v[196:199]\n"
"v_cvt_pk_bf16_f32 v162,v164,v165\n"
"v_cvt_pk_bf16_f32 v163,v166,v167\n"
"v_mfma_f32_16x16x32_bf16 v[200:203],v[48:51],a[112:115],v[200:203]\n"
"ds_write_b128 v116,v[160:163] offset:49152\n"
"v_mfma_f32_16x16x32_bf16 v[200:203],v[52:55],a[116:119],v[200:203]\n"
"v_cvt_pk_bf16_f32 v170,v172,v173\n"
"v_cvt_pk_bf16_f32 v171,v174,v175\n"
"v_mfma_f32_16x16x32_bf16 v[204:207],v[60:63],a[116:119],v[204:207]\n"
"ds_write_b128 v116,v[168:171] offset:50176\n"
"v_mfma_f32_16x16x32_bf16 v[204:207],v[56:59],a[112:115],v[204:207]\n"
"v_cvt_pk_bf16_f32 v176,v176,v177\n"
"v_cvt_pk_bf16_f32 v177,v178,v179\n"
"v_mfma_f32_16x16x32_bf16 v[208:211],v[64:67],a[112:115],v[208:211]\n"
"v_cvt_pk_bf16_f32 v178,v180,v181\n"
"v_cvt_pk_bf16_f32 v179,v182,v183\n"
"v_mfma_f32_16x16x32_bf16 v[208:211],v[68:71],a[116:119],v[208:211]\n"
"ds_write_b128 v116,v[176:179] offset:51200\n"
"v_cvt_pk_bf16_f32 v184,v184,v185\n"
"v_mfma_f32_16x16x32_bf16 v[212:215],v[76:79],a[116:119],v[212:215]\n"
"v_cvt_pk_bf16_f32 v185,v186,v187\n"
"v_cvt_pk_bf16_f32 v186,v188,v189\n"
"v_cvt_pk_bf16_f32 v187,v190,v191\n"
"v_mfma_f32_16x16x32_bf16 v[212:215],v[72:75],a[112:115],v[212:215]\n"
"ds_write_b128 v116,v[184:187] offset:52224\n"
"v_mfma_f32_16x16x32_bf16 v[216:219],v[80:83],a[112:115],v[216:219]\n"
"ds_read_b128 a[16:19],v116 offset:49152\n"
"ds_read_b128 a[20:23],v116 offset:50176\n"
"v_mfma_f32_16x16x32_bf16 v[216:219],v[84:87],a[116:119],v[216:219]\n"
"ds_read_b128 a[24:27],v116 offset:51200\n"
"ds_read_b128 a[28:31],v116 offset:52224\n"
"v_mfma_f32_16x16x32_bf16 v[220:223],v[92:95],a[116:119],v[220:223]\n"
"s_lshl2_add_u32 s82,s60,s52\n"
"s_lshr_b32 s82,s82,1\n"
"s_cmp_eq_u32 s82,1\n"
"v_mfma_f32_16x16x32_bf16 v[220:223],v[88:91],a[112:115],v[220:223]\n"
"s_cbranch_scc1 .Lfk_head0\n"
"s_waitcnt lgkmcnt(0)\n"
"s_barrier\n"
"v_mfma_f32_16x16x32_bf16 v[24:27],a[64:67],a[48:51],v[108:111]\n"
"s_andn2_b32 m0,s55,16384\n"
"s_add_u32 s54,s54,s22\n"
"buffer_load_dwordx4 v115,s[32:35],s54 offen sc1 lds\n"
"v_mfma_f32_16x16x32_bf16 v[24:27],a[68:71],a[52:55],v[24:27]\n"
"v_cvt_pk_bf16_f32 v192,v192,v193\n"
"v_cvt_pk_bf16_f32 v193,v194,v195\n"
"v_cvt_pk_bf16_f32 v194,v196,v197\n"
"v_cvt_pk_bf16_f32 v195,v198,v199\n"
"v_mfma_f32_16x16x32_bf16 v[24:27],a[72:75],a[56:59],v[24:27]\n"
"ds_write_b128 v116,v[192:195] offset:32768\n"
"v_cvt_pk_bf16_f32 v200,v200,v201\n"
"v_cvt_pk_bf16_f32 v201,v202,v203\n"
"v_cvt_pk_bf16_f32 v202,v204,v205\n"
"v_cvt_pk_bf16_f32 v203,v206,v207\n"
"v_mfma_f32_16x16x32_bf16 v[24:27],a[76:79],a[60:63],v[24:27]\n"
"ds_write_b128 v116,v[200:203] offset:33792\n"
"v_cvt_pk_bf16_f32 v208,v208,v209\n"
"v_cvt_pk_bf16_f32 v209,v210,v211\n"
"v_mfma_f32_16x16x32_bf16 v[28:31],a[92:95],a[60:63],v[108:111]\n"
"v_cvt_pk_bf16_f32 v210,v212,v213\n"
"v_cvt_pk_bf16_f32 v211,v214,v215\n"
"v_cvt_pk_bf16_f32 v216,v216,v217\n"
"v_mfma_f32_16x16x32_bf16 v[28:31],a[88:91],a[56:59],v[28:31]\n"
"ds_write_b128 v116,v[208:211] offset:34816\n"
"buffer_store_dwordx4 a[160:163],v112,s[24:27],s3 offen offset:0 sc1\n"
"v_cvt_pk_bf16_f32 v217,v218,v219\n"
"v_cvt_pk_bf16_f32 v218,v220,v221\n"
"v_cvt_pk_bf16_f32 v219,v222,v223\n"
"v_mfma_f32_16x16x32_bf16 v[28:31],a[84:87],a[52:55],v[28:31]\n"
"ds_write_b128 v116,v[216:219] offset:35840\n"
"ds_read_b128 a[32:35],v116 offset:32768\n"
"ds_read_b128 a[36:39],v116 offset:33792\n"
"v_mfma_f32_16x16x32_bf16 v[28:31],a[80:83],a[48:51],v[28:31]\n"
"ds_read_b128 a[40:43],v116 offset:34816\n"
"ds_read_b128 a[44:47],v116 offset:35840\n"
"v_mfma_f32_16x16x32_bf16 v[4:7],a[80:83],a[0:3],v[96:99]\n"
"ds_read_b128 a[96:99],v113 offset:16384\n"
"ds_read_b128 a[100:103],v113 offset:17408\n"
"v_mfma_f32_16x16x32_bf16 v[4:7],a[84:87],a[4:7],v[4:7]\n"
"ds_read_b128 a[104:107],v113 offset:18432\n"
"ds_read_b128 a[108:111],v113 offset:19456\n"
"v_mfma_f32_16x16x32_bf16 v[4:7],a[88:91],a[8:11],v[4:7]\n"
"ds_read_b128 a[112:115],v113 offset:20480\n"
"ds_read_b128 a[116:119],v113 offset:21504\n"
"ds_read_b128 a[120:123],v113 offset:22528\n"
"ds_read_b128 a[124:127],v113 offset:23552\n"
"v_mfma_f32_16x16x32_bf16 v[4:7],a[92:95],a[12:15],v[4:7]\n"
"v_max3_f32 v118,v24,v25,v26\n"
"v_max3_f32 v118,v118,v27,v28\n"
"v_max3_f32 v118,v118,v29,v30\n"
"v_mfma_f32_16x16x32_bf16 v[0:3],a[76:79],a[12:15],v[96:99]\n"
"v_max_f32 v118,v118,v31\n"
"v_mov_b32 v120,v118\n"
"v_mfma_f32_16x16x32_bf16 v[0:3],a[72:75],a[8:11],v[0:3]\n"
"v_mfma_f32_16x16x32_bf16 v[0:3],a[68:71],a[4:7],v[0:3]\n"
"v_mfma_f32_16x16x32_bf16 v[0:3],a[64:67],a[0:3],v[0:3]\n"
"v_mfma_f32_16x16x32_bf16 v[8:11],a[64:67],a[16:19],v[100:103]\n"
"buffer_store_dwordx4 a[164:167],v112,s[24:27],s3 offen offset:1024 sc1\n"
"v_mfma_f32_16x16x32_bf16 v[8:11],a[68:71],a[20:23],v[8:11]\n"
"v_mfma_f32_16x16x32_bf16 v[8:11],a[72:75],a[24:27],v[8:11]\n"
"s_nop 3\n"
"v_max3_f32 v119,v0,v1,v2\n"
"v_mfma_f32_16x16x32_bf16 v[8:11],a[76:79],a[28:31],v[8:11]\n"
"v_max3_f32 v119,v119,v3,v4\n"
"v_max3_f32 v119,v119,v5,v6\n"
"v_max_f32 v119,v119,v7\n"
"v_mfma_f32_16x16x32_bf16 v[12:15],a[92:95],a[28:31],v[100:103]\n"
"v_min_f32 v120,v120,v119\n"
"v_max_f32 v118,v118,v119\n"
"v_mfma_f32_16x16x32_bf16 v[12:15],a[88:91],a[24:27],v[12:15]\n"
"v_mfma_f32_16x16x32_bf16 v[12:15],a[84:87],a[20:23],v[12:15]\n"
"v_mfma_f32_16x16x32_bf16 v[12:15],a[80:83],a[16:19],v[12:15]\n"
"s_waitcnt lgkmcnt(8)\n"
"v_mfma_f32_16x16x32_bf16 v[20:23],a[80:83],a[32:35],v[104:107]\n"
"v_mfma_f32_16x16x32_bf16 v[20:23],a[84:87],a[36:39],v[20:23]\n"
"v_max3_f32 v119,v8,v9,v10\n"
"v_mfma_f32_16x16x32_bf16 v[20:23],a[88:91],a[40:43],v[20:23]\n"
"buffer_store_dwordx4 a[224:227],v112,s[24:27],s56 offen offset:0 sc1\n"
"v_mfma_f32_16x16x32_bf16 v[20:23],a[92:95],a[44:47],v[20:23]\n"
"s_nop 0\n"
"v_max3_f32 v119,v119,v11,v12\n"
"v_max3_f32 v119,v119,v13,v14\n"
"v_mfma_f32_16x16x32_bf16 v[16:19],a[76:79],a[44:47],v[104:107]\n"
"v_max_f32 v119,v119,v15\n"
"v_max_f32 v118,v118,v119\n"
"v_mfma_f32_16x16x32_bf16 v[16:19],a[72:75],a[40:43],v[16:19]\n"
"v_min_f32 v120,v120,v119\n"
"v_mfma_f32_16x16x32_bf16 v[16:19],a[68:71],a[36:39],v[16:19]\n"
"v_mfma_f32_16x16x32_bf16 v[16:19],a[64:67],a[32:35],v[16:19]\n"
"s_nop 7\n"
"v_max3_f32 v119,v16,v17,v18\n"
"v_max3_f32 v119,v119,v19,v20\n"
"v_max3_f32 v119,v119,v21,v22\n"
"v_max_f32 v119,v119,v23\n"
"v_min_f32 v120,v120,v119\n"
"v_max_f32 v118,v118,v119\n"
"v_cmp_gt_f32 s[96:97],s16,v120\n"
"v_cmp_lt_f32 vcc,s93,v118\n"
"s_nop 0\n"
"s_or_b64 s[96:97],s[96:97],vcc\n"
"s_cbranch_scc1 .Lfk_cs_h3\n"
".Lfk_cr_h3:\n"
"s_cmp_ge_u32 s52,2\n"
"s_cbranch_scc1 .Lfk_g0\n"
"s_waitcnt vmcnt(9) lgkmcnt(0)\n"
"s_barrier\n"
"v_mfma_f32_16x16x32_bf16 v[32:35],a[96:99],a[0:3],v[96:99]\n"
"s_add_u32 m0,s55,4096\n"
"v_exp_f32 v0,v0\n"
"v_mfma_f32_16x16x32_bf16 v[32:35],a[100:103],a[4:7],v[32:35]\n"
"s_add_u32 s54,s54,s22\n"
"v_exp_f32 v1,v1\n"
"v_mfma_f32_16x16x32_bf16 v[32:35],a[104:107],a[8:11],v[32:35]\n"
"buffer_load_dwordx4 v115,s[32:35],s54 offen sc1 lds\n"
"v_exp_f32 v2,v2\n"
"v_cvt_pk_bf16_f32 v64,v0,v1\n"
"v_mfma_f32_16x16x32_bf16 v[32:35],a[108:111],a[12:15],v[32:35]\n"
"s_add_u32 m0,s67,16384\n"
"v_exp_f32 v3,v3\n"
"v_mfma_f32_16x16x32_bf16 v[36:39],a[124:127],a[12:15],v[96:99]\n"
"v_exp_f32 v4,v4\n"
"v_cvt_pk_bf16_f32 v65,v2,v3\n"
"v_exp_f32 v5,v5\n"
"v_mfma_f32_16x16x32_bf16 v[36:39],a[120:123],a[8:11],v[36:39]\n"
"buffer_load_dwordx4 v112,s[24:27],s64 offen offset:0 sc1 lds\n"
"v_mfma_f32_16x16x32_bf16 v[36:39],a[116:119],a[4:7],v[36:39]\n"
"v_exp_f32 v6,v6\n"
"v_cvt_pk_bf16_f32 v66,v4,v5\n"
"v_exp_f32 v7,v7\n"
"v_mfma_f32_16x16x32_bf16 v[36:39],a[112:115],a[0:3],v[36:39]\n"
"buffer_load_dwordx4 v112,s[24:27],s64 offen offset:1024 sc1 lds\n"
"v_mfma_f32_16x16x32_bf16 v[44:47],a[112:115],a[16:19],v[100:103]\n"
"v_exp_f32 v8,v8\n"
"v_cvt_pk_bf16_f32 v67,v6,v7\n"
"v_exp_f32 v9,v9\n"
"v_mfma_f32_16x16x32_bf16 v[44:47],a[116:119],a[20:23],v[44:47]\n"
"ds_read_b128 a[64:67],v113 offset:49152\n"
"ds_read_b128 a[68:71],v113 offset:50176\n"
"v_exp_f32 v10,v10\n"
"v_mfma_f32_16x16x32_bf16 v[44:47],a[120:123],a[24:27],v[44:47]\n"
"ds_read_b128 a[72:75],v113 offset:51200\n"
"ds_read_b128 a[76:79],v113 offset:52224\n"
"v_exp_f32 v11,v11\n"
"v_mfma_f32_16x16x32_bf16 v[44:47],a[124:127],a[28:31],v[44:47]\n"
"ds_read_b128 a[80:83],v113 offset:53248\n"
"ds_read_b128 a[84:87],v113 offset:54272\n"
"v_exp_f32 v12,v12\n"
"v_mfma_f32_16x16x32_bf16 v[40:43],a[108:111],a[28:31],v[100:103]\n"
"ds_read_b128 a[88:91],v113 offset:55296\n"
"ds_read_b128 a[92:95],v113 offset:56320\n"
"v_exp_f32 v13,v13\n"
"v_mfma_f32_16x16x32_bf16 v[40:43],a[104:107],a[24:27],v[40:43]\n"
"buffer_load_dwordx4 v112,s[24:27],s64 offen offset:2048 sc1 lds\n"
"v_exp_f32 v14,v14\n"
"v_mfma_f32_16x16x32_bf16 v[40:43],a[100:103],a[20:23],v[40:43]\n"
"buffer_load_dwordx4 v112,s[24:27],s64 offen offset:3072 sc1 lds\n"
"v_mfma_f32_16x16x32_bf16 v[40:43],a[96:99],a[16:19],v[40:43]\n"
"s_add_u32 m0,s67,20480\n"
"v_exp_f32 v15,v15\n"
"v_mfma_f32_16x16x32_bf16 v[48:51],a[96:99],a[32:35],v[104:107]\n"
"buffer_load_dwordx4 v117,s[24:27],s64 offen offset:0 sc1 lds\n"
"buffer_load_dwordx4 v117,s[24:27],s64 offen offset:1024 sc1 lds\n"
"v_exp_f32 v16,v16\n"
"v_exp_f32 v17,v17\n"
"v_mfma_f32_16x16x32_bf16 v[48:51],a[100:103],a[36:39],v[48:51]\n"
"buffer_load_dwordx4 v117,s[24:27],s64 offen offset:2048 sc1 lds\n"
"v_mfma_f32_16x16x32_bf16 v[48:51],a[104:107],a[40:43],v[48:51]\n"
"v_exp_f32 v18,v18\n"
"v_cvt_pk_bf16_f32 v68,v8,v9\n"
"v_cvt_pk_bf16_f32 v69,v10,v11\n"
"v_exp_f32 v19,v19\n"
"v_mfma_f32_16x16x32_bf16 v[48:51],a[108:111],a[44:47],v[48:51]\n"
"buffer_load_dwordx4 v117,s[24:27],s64 offen offset:3072 sc1 lds\n"
"v_mfma_f32_16x16x32_bf16 v[52:55],a[124:127],a[44:47],v[104:107]\n"
"s_add_u32 s64,s64,16384\n"
"v_exp_f32 v20,v20\n"
"v_cvt_pk_bf16_f32 v70,v12,v13\n"
"v_cvt_pk_bf16_f32 v71,v14,v15\n"
"v_mfma_f32_16x16x32_bf16 v[52:55],a[120:123],a[40:43],v[52:55]\n"
"v_exp_f32 v21,v21\n"
"v_cvt_pk_bf16_f32 v72,v16,v17\n"
"v_cvt_pk_bf16_f32 v73,v18,v19\n"
"v_mfma_f32_16x16x32_bf16 v[52:55],a[116:119],a[36:39],v[52:55]\n"
"v_exp_f32 v22,v22\n"
"v_cvt_pk_bf16_f32 v74,v20,v21\n"
"v_mfma_f32_16x16x32_bf16 v[52:55],a[112:115],a[32:35],v[52:55]\n"
"v_exp_f32 v23,v23\n"
"v_mfma_f32_16x16x32_bf16 v[60:63],a[112:115],a[48:51],v[108:111]\n"
"v_exp_f32 v24,v24\n"
"v_cvt_pk_bf16_f32 v75,v22,v23\n"
"v_mfma_f32_16x16x32_bf16 v[60:63],a[116:119],a[52:55],v[60:63]\n"
"v_exp_f32 v25,v25\n"
"v_mfma_f32_16x16x32_bf16 v[60:63],a[120:123],a[56:59],v[60:63]\n"
"v_exp_f32 v26,v26\n"
"v_exp_f32 v27,v27\n"
"v_mfma_f32_16x16x32_bf16 v[60:63],a[124:127],a[60:63],v[60:63]\n"
"buffer_store_dwordx4 a[228:231],v112,s[24:27],s56 offen offset:1024 sc1\n"
"v_mfma_f32_16x16x32_bf16 v[56:59],a[108:111],a[60:63],v[108:111]\n"
"v_exp_f32 v28,v28\n"
"v_cvt_pk_bf16_f32 v77,v26,v27\n"
"v_mfma_f32_16x16x32_bf16 v[56:59],a[104:107],a[56:59],v[56:59]\n"
"v_exp_f32 v29,v29\n"
"v_mfma_f32_16x16x32_bf16 v[56:59],a[100:103],a[52:55],v[56:59]\n"
"v_exp_f32 v30,v30\n"
"v_cvt_pk_bf16_f32 v78,v28,v29\n"
"v_mfma_f32_16x16x32_bf16 v[56:59],a[96:99],a[48:51],v[56:59]\n"
"s_waitcnt vmcnt(15) lgkmcnt(0)\n"
"v_exp_f32 v31,v31\n"
"v_cvt_pk_bf16_f32 v76,v24,v25\n"
"v_cvt_pk_bf16_f32 v79,v30,v31\n"
"s_barrier\n"
"v_mfma_f32_16x16x32_bf16 v[0:3],a[64:67],a[0:3],v[96:99]\n"
"s_add_u32 m0,s55,8192\n"
"v_exp_f32 v122,v32\n"
"v_mfma_f32_16x16x32_bf16 v[0:3],a[68:71],a[4:7],v[0:3]\n"
"s_add_u32 s54,s54,s22\n"
"v_exp_f32 v123,v33\n"
"v_mfma_f32_16x16x32_bf16 v[0:3],a[72:75],a[8:11],v[0:3]\n"
"ds_read_b128 a[96:99],v114 offset:40960\n"
"ds_read_b128 a[100:103],v114 offset:41984\n"
"v_mfma_f32_16x16x32_bf16 v[0:3],a[76:79],a[12:15],v[0:3]\n"
"v_exp_f32 v119,v34\n"
"v_mfma_f32_16x16x32_bf16 v[4:7],a[92:95],a[12:15],v[96:99]\n"
"ds_read_b128 a[104:107],v114 offset:43008\n"
"v_cvt_pk_bf16_f32 v80,v122,v123\n"
"v_mfma_f32_16x16x32_bf16 v[4:7],a[88:91],a[8:11],v[4:7]\n"
"v_exp_f32 v120,v35\n"
"v_mfma_f32_16x16x32_bf16 v[4:7],a[84:87],a[4:7],v[4:7]\n"
"ds_read_b128 a[108:111],v114 offset:44032\n"
"ds_read_b128 a[112:115],v114 offset:45056\n"
"v_mfma_f32_16x16x32_bf16 v[4:7],a[80:83],a[0:3],v[4:7]\n"
"v_exp_f32 v122,v36\n"
"v_mfma_f32_16x16x32_bf16 v[12:15],a[80:83],a[16:19],v[100:103]\n"
"ds_read_b128 a[116:119],v114 offset:46080\n"
"v_cvt_pk_bf16_f32 v81,v119,v120\n"
"v_mfma_f32_16x16x32_bf16 v[12:15],a[84:87],a[20:23],v[12:15]\n"
"v_exp_f32 v123,v37\n"
"v_mfma_f32_16x16x32_bf16 v[12:15],a[88:91],a[24:27],v[12:15]\n"
"ds_read_b128 a[120:123],v114 offset:47104\n"
"ds_read_b128 a[124:127],v114 offset:48128\n"
"v_mfma_f32_16x16x32_bf16 v[12:15],a[92:95],a[28:31],v[12:15]\n"
"v_exp_f32 v119,v38\n"
"v_cvt_pk_bf16_f32 v82,v122,v123\n"
"v_mfma_f32_16x16x32_bf16 v[8:11],a[76:79],a[28:31],v[100:103]\n"
"buffer_store_dwordx4 a[168:171],v112,s[24:27],s3 offen offset:2048 sc1\n"
"v_mfma_f32_16x16x32_bf16 v[8:11],a[72:75],a[24:27],v[8:11]\n"
"v_exp_f32 v120,v39\n"
"v_mfma_f32_16x16x32_bf16 v[8:11],a[68:71],a[20:23],v[8:11]\n"
"buffer_load_dwordx4 v115,s[32:35],s54 offen sc1 lds\n"
"v_mfma_f32_16x16x32_bf16 v[8:11],a[64:67],a[16:19],v[8:11]\n"
"s_add_u32 m0,s67,49152\n"
"v_exp_f32 v122,v40\n"
"v_mfma_f32_16x16x32_bf16 v[16:19],a[64:67],a[32:35],v[104:107]\n"
"v_cvt_pk_bf16_f32 v83,v119,v120\n"
"v_or3_b32 v118,v80,v81,v82\n"
"v_mfma_f32_16x16x32_bf16 v[16:19],a[68:71],a[36:39],v[16:19]\n"
"v_exp_f32 v123,v41\n"
"v_mfma_f32_16x16x32_bf16 v[16:19],a[72:75],a[40:43],v[16:19]\n"
"buffer_load_dwordx4 v112,s[24:27],s64 offen offset:0 sc1 lds\n"
"v_mfma_f32_16x16x32_bf16 v[16:19],a[76:79],a[44:47],v[16:19]\n"
"v_exp_f32 v119,v42\n"
"v_mfma_f32_16x16x32_bf16 v[20:23],a[92:95],a[44:47],v[104:107]\n"
"v_cvt_pk_bf16_f32 v84,v122,v123\n"
"v_or3_b32 v118,v118,v83,v84\n"
"v_mfma_f32_16x16x32_bf16 v[20:23],a[88:91],a[40:43],v[20:23]\n"
"v_exp_f32 v120,v43\n"
"v_mfma_f32_16x16x32_bf16 v[20:23],a[84:87],a[36:39],v[20:23]\n"
"buffer_load_dwordx4 v112,s[24:27],s64 offen offset:1024 sc1 lds\n"
"v_mfma_f32_16x16x32_bf16 v[20:23],a[80:83],a[32:35],v[20:23]\n"
"v_exp_f32 v122,v44\n"
"v_mfma_f32_16x16x32_bf16 v[28:31],a[80:83],a[48:51],v[108:111]\n"
"v_cvt_pk_bf16_f32 v85,v119,v120\n"
"v_mfma_f32_16x16x32_bf16 v[28:31],a[84:87],a[52:55],v[28:31]\n"
"v_exp_f32 v123,v45\n"
"v_mfma_f32_16x16x32_bf16 v[28:31],a[88:91],a[56:59],v[28:31]\n"
"buffer_load_dwordx4 v112,s[24:27],s64 offen offset:2048 sc1 lds\n"
"v_mfma_f32_16x16x32_bf16 v[28:31],a[92:95],a[60:63],v[28:31]\n"
"v_exp_f32 v119,v46\n"
"v_cvt_pk_bf16_f32 v86,v122,v123\n"
"v_mfma_f32_16x16x32_bf16 v[24:27],a[76:79],a[60:63],v[108:111]\n"
"buffer_store_dwordx4 a[172:175],v112,s[24:27],s3 offen offset:3072 sc1\n"
"v_mfma_f32_16x16x32_bf16 v[24:27],a[72:75],a[56:59],v[24:27]\n"
"v_exp_f32 v120,v47\n"
"v_mfma_f32_16x16x32_bf16 v[24:27],a[68:71],a[52:55],v[24:27]\n"
"buffer_load_dwordx4 v112,s[24:27],s64 offen offset:3072 sc1 lds\n"
"v_mfma_f32_16x16x32_bf16 v[24:27],a[64:67],a[48:51],v[24:27]\n"
"s_waitcnt lgkmcnt(0)\n"
"v_exp_f32 v122,v48\n"
"v_mfma_f32_16x16x32_bf16 v[128:131],a[96:99],v[64:67],0\n"
"v_exp_f32 v123,v49\n"
"v_mfma_f32_16x16x32_bf16 v[160:163],a[96:99],v[68:71],0\n"
"ds_read_b128 a[64:67],v113 offset:32768\n"
"v_cvt_pk_bf16_f32 v87,v119,v120\n"
"v_mfma_f32_16x16x32_bf16 v[192:195],a[96:99],v[72:75],0\n"
"s_add_u32 m0,s67,53248\n"
"v_exp_f32 v119,v50\n"
"v_mfma_f32_16x16x32_bf16 v[224:227],a[96:99],v[76:79],0\n"
"ds_read_b128 a[68:71],v113 offset:33792\n"
"v_cvt_pk_bf16_f32 v88,v122,v123\n"
"v_mfma_f32_16x16x32_bf16 a[140:143],a[144:147],v[76:79],0\n"
"v_exp_f32 v120,v51\n"
"v_mfma_f32_16x16x32_bf16 v[228:231],a[100:103],v[76:79],0\n"
"ds_read_b128 a[72:75],v113 offset:34816\n"
"ds_read_b128 a[76:79],v113 offset:35840\n"
"v_mfma_f32_16x16x32_bf16 v[132:135],a[100:103],v[64:67],0\n"
"v_exp_f32 v122,v52\n"
"v_mfma_f32_16x16x32_bf16 v[164:167],a[100:103],v[68:71],0\n"
"ds_read_b128 a[80:83],v113 offset:36864\n"
"v_cvt_pk_bf16_f32 v89,v119,v120\n"
"v_mfma_f32_16x16x32_bf16 v[196:199],a[100:103],v[72:75],0\n"
"v_exp_f32 v123,v53\n"
"v_mfma_f32_16x16x32_bf16 a[136:139],a[144:147],v[72:75],0\n"
"ds_read_b128 a[84:87],v113 offset:37888\n"
"ds_read_b128 a[88:91],v113 offset:38912\n"
"v_mfma_f32_16x16x32_bf16 v[200:203],a[104:107],v[72:75],0\n"
"v_exp_f32 v119,v54\n"
"v_mfma_f32_16x16x32_bf16 v[232:235],a[104:107],v[76:79],0\n"
"ds_read_b128 a[92:95],v113 offset:39936\n"
"v_cvt_pk_bf16_f32 v90,v122,v123\n"
"v_mfma_f32_16x16x32_bf16 v[136:139],a[104:107],v[64:67],0\n"
"v_exp_f32 v120,v55\n"
"v_mfma_f32_16x16x32_bf16 v[168:171],a[104:107],v[68:71],0\n"
"buffer_load_dwordx4 v117,s[24:27],s64 offen offset:0 sc1 lds\n"
"v_mfma_f32_16x16x32_bf16 a[132:135],a[144:147],v[68:71],0\n"
"v_exp_f32 v122,v56\n"
"v_mfma_f32_16x16x32_bf16 v[172:175],a[108:111],v[68:71],0\n"
"v_cvt_pk_bf16_f32 v91,v119,v120\n"
"s_sub_u32 s3,s3,16384\n"
"v_mfma_f32_16x16x32_bf16 v[204:207],a[108:111],v[72:75],0\n"
"v_exp_f32 v123,v57\n"
"v_mfma_f32_16x16x32_bf16 v[236:239],a[108:111],v[76:79],0\n"
"buffer_load_dwordx4 v117,s[24:27],s64 offen offset:1024 sc1 lds\n"
"v_mfma_f32_16x16x32_bf16 v[140:143],a[108:111],v[64:67],0\n"
"v_exp_f32 v119,v58\n"
"v_cvt_pk_bf16_f32 v92,v122,v123\n"
"v_mfma_f32_16x16x32_bf16 a[128:131],a[144:147],v[64:67],0\n"
"buffer_store_dwordx4 a[176:179],v112,s[24:27],s3 offen offset:0 sc1\n"
"v_mfma_f32_16x16x32_bf16 v[144:147],a[112:115],v[64:67],0\n"
"v_exp_f32 v120,v59\n"
"v_mfma_f32_16x16x32_bf16 v[176:179],a[112:115],v[68:71],0\n"
"buffer_load_dwordx4 v117,s[24:27],s64 offen offset:2048 sc1 lds\n"
"v_mfma_f32_16x16x32_bf16 v[208:211],a[112:115],v[72:75],0\n"
"v_exp_f32 v122,v60\n"
"v_mfma_f32_16x16x32_bf16 v[240:243],a[112:115],v[76:79],0\n"
"v_cvt_pk_bf16_f32 v93,v119,v120\n"
"v_or3_b32 v118,v118,v85,v86\n"
"v_mfma_f32_16x16x32_bf16 v[244:247],a[116:119],v[76:79],0\n"
"v_exp_f32 v123,v61\n"
"v_mfma_f32_16x16x32_bf16 v[148:151],a[116:119],v[64:67],0\n"
"buffer_load_dwordx4 v117,s[24:27],s64 offen offset:3072 sc1 lds\n"
"v_mfma_f32_16x16x32_bf16 v[180:183],a[116:119],v[68:71],0\n"
"s_add_u32 s64,s64,16384\n"
"v_exp_f32 v119,v62\n"
"v_cvt_pk_bf16_f32 v94,v122,v123\n"
"v_mfma_f32_16x16x32_bf16 v[212:215],a[116:119],v[72:75],0\n"
"buffer_store_dwordx4 a[232:235],v112,s[24:27],s56 offen offset:2048 sc1\n"
"v_mfma_f32_16x16x32_bf16 v[216:219],a[120:123],v[72:75],0\n"
"v_exp_f32 v120,v63\n"
"v_mfma_f32_16x16x32_bf16 v[248:251],a[120:123],v[76:79],0\n"
"v_cvt_pk_bf16_f32 v95,v119,v120\n"
"v_or3_b32 v118,v118,v87,v88\n"
"v_mfma_f32_16x16x32_bf16 v[152:155],a[120:123],v[64:67],0\n"
"v_or3_b32 v118,v118,v89,v90\n"
"v_or3_b32 v118,v118,v91,v92\n"
"v_mfma_f32_16x16x32_bf16 v[184:187],a[120:123],v[68:71],0\n"
"v_or3_b32 v118,v118,v93,v94\n"
"v_or_b32 v118,v118,v95\n"
"v_mfma_f32_16x16x32_bf16 v[188:191],a[124:127],v[68:71],0\n"
"v_and_b32 v118,0x40004000,v118\n"
"v_cmp_ne_u32 vcc,0,v118\n"
"v_mfma_f32_16x16x32_bf16 v[220:223],a[124:127],v[72:75],0\n"
"v_mfma_f32_16x16x32_bf16 v[252:255],a[124:127],v[76:79],0\n"
"v_mfma_f32_16x16x32_bf16 v[156:159],a[124:127],v[64:67],0\n"
"s_cbranch_vccnz .Lfk_cs_h1\n"
".Lfk_cr_h1:\n"
"s_waitcnt lgkmcnt(0)\n"
"s_barrier\n"
"v_mfma_f32_16x16x32_bf16 v[32:35],a[64:67],a[0:3],v[96:99]\n"
"s_add_u32 m0,s55,12288\n"
"v_exp_f32 v122,v0\n"
"v_mfma_f32_16x16x32_bf16 v[32:35],a[68:71],a[4:7],v[32:35]\n"
"s_add_u32 s54,s54,s22\n"
"v_exp_f32 v123,v1\n"
"v_mfma_f32_16x16x32_bf16 v[32:35],a[72:75],a[8:11],v[32:35]\n"
"ds_read_b128 a[96:99],v114 offset:8192\n"
"ds_read_b128 a[100:103],v114 offset:9216\n"
"v_mfma_f32_16x16x32_bf16 v[32:35],a[76:79],a[12:15],v[32:35]\n"
"v_exp_f32 v119,v2\n"
"v_mfma_f32_16x16x32_bf16 v[36:39],a[92:95],a[12:15],v[96:99]\n"
"ds_read_b128 a[104:107],v114 offset:10240\n"
"v_cvt_pk_bf16_f32 v64,v122,v123\n"
"v_mfma_f32_16x16x32_bf16 v[36:39],a[88:91],a[8:11],v[36:39]\n"
"v_exp_f32 v120,v3\n"
"v_mfma_f32_16x16x32_bf16 v[36:39],a[84:87],a[4:7],v[36:39]\n"
"ds_read_b128 a[108:111],v114 offset:11264\n"
"ds_read_b128 a[112:115],v114 offset:12288\n"
"v_mfma_f32_16x16x32_bf16 v[36:39],a[80:83],a[0:3],v[36:39]\n"
"v_exp_f32 v122,v4\n"
"v_mfma_f32_16x16x32_bf16 v[44:47],a[80:83],a[16:19],v[100:103]\n"
"ds_read_b128 a[116:119],v114 offset:13312\n"
"v_cvt_pk_bf16_f32 v65,v119,v120\n"
"v_mfma_f32_16x16x32_bf16 v[44:47],a[84:87],a[20:23],v[44:47]\n"
"v_exp_f32 v123,v5\n"
"v_mfma_f32_16x16x32_bf16 v[44:47],a[88:91],a[24:27],v[44:47]\n"
"ds_read_b128 a[120:123],v114 offset:14336\n"
"ds_read_b128 a[124:127],v114 offset:15360\n"
"v_mfma_f32_16x16x32_bf16 v[44:47],a[92:95],a[28:31],v[44:47]\n"
"v_exp_f32 v119,v6\n"
"v_cvt_pk_bf16_f32 v66,v122,v123\n"
"v_mfma_f32_16x16x32_bf16 v[40:43],a[76:79],a[28:31],v[100:103]\n"
"buffer_store_dwordx4 a[180:183],v112,s[24:27],s3 offen offset:1024 sc1\n"
"v_mfma_f32_16x16x32_bf16 v[40:43],a[72:75],a[24:27],v[40:43]\n"
"v_exp_f32 v120,v7\n"
"v_mfma_f32_16x16x32_bf16 v[40:43],a[68:71],a[20:23],v[40:43]\n"
"buffer_load_dwordx4 v115,s[32:35],s54 offen sc1 lds\n"
"v_mfma_f32_16x16x32_bf16 v[40:43],a[64:67],a[16:19],v[40:43]\n"
"s_add_u32 m0,s67,32768\n"
"v_exp_f32 v122,v8\n"
"v_mfma_f32_16x16x32_bf16 v[48:51],a[64:67],a[32:35],v[104:107]\n"
"v_cvt_pk_bf16_f32 v67,v119,v120\n"
"v_or3_b32 v118,v64,v65,v66\n"
"v_mfma_f32_16x16x32_bf16 v[48:51],a[68:71],a[36:39],v[48:51]\n"
"v_exp_f32 v123,v9\n"
"v_mfma_f32_16x16x32_bf16 v[48:51],a[72:75],a[40:43],v[48:51]\n"
"buffer_load_dwordx4 v112,s[24:27],s64 offen offset:0 sc1 lds\n"
"v_mfma_f32_16x16x32_bf16 v[48:51],a[76:79],a[44:47],v[48:51]\n"
"v_exp_f32 v119,v10\n"
"v_mfma_f32_16x16x32_bf16 v[52:55],a[92:95],a[44:47],v[104:107]\n"
"v_cvt_pk_bf16_f32 v68,v122,v123\n"
"v_or3_b32 v118,v118,v67,v68\n"
"v_mfma_f32_16x16x32_bf16 v[52:55],a[88:91],a[40:43],v[52:55]\n"
"v_exp_f32 v120,v11\n"
"v_mfma_f32_16x16x32_bf16 v[52:55],a[84:87],a[36:39],v[52:55]\n"
"buffer_load_dwordx4 v112,s[24:27],s64 offen offset:1024 sc1 lds\n"
"v_mfma_f32_16x16x32_bf16 v[52:55],a[80:83],a[32:35],v[52:55]\n"
"v_exp_f32 v122,v12\n"
"v_mfma_f32_16x16x32_bf16 v[60:63],a[80:83],a[48:51],v[108:111]\n"
"v_cvt_pk_bf16_f32 v69,v119,v120\n"
"v_mfma_f32_16x16x32_bf16 v[60:63],a[84:87],a[52:55],v[60:63]\n"
"v_exp_f32 v123,v13\n"
"v_mfma_f32_16x16x32_bf16 v[60:63],a[88:91],a[56:59],v[60:63]\n"
"buffer_load_dwordx4 v112,s[24:27],s64 offen offset:2048 sc1 lds\n"
"v_mfma_f32_16x16x32_bf16 v[60:63],a[92:95],a[60:63],v[60:63]\n"
"v_exp_f32 v119,v14\n"
"v_cvt_pk_bf16_f32 v70,v122,v123\n"
"v_mfma_f32_16x16x32_bf16 v[56:59],a[76:79],a[60:63],v[108:111]\n"
"buffer_store_dwordx4 a[184:187],v112,s[24:27],s3 offen offset:2048 sc1\n"
"v_mfma_f32_16x16x32_bf16 v[56:59],a[72:75],a[56:59],v[56:59]\n"
"v_exp_f32 v120,v15\n"
"v_mfma_f32_16x16x32_bf16 v[56:59],a[68:71],a[52:55],v[56:59]\n"
"buffer_load_dwordx4 v112,s[24:27],s64 offen offset:3072 sc1 lds\n"
"v_mfma_f32_16x16x32_bf16 v[56:59],a[64:67],a[48:51],v[56:59]\n"
"s_waitcnt lgkmcnt(0)\n"
"v_exp_f32 v122,v16\n"
"v_mfma_f32_16x16x32_bf16 v[128:131],a[96:99],v[80:83],v[128:131]\n"
"v_exp_f32 v123,v17\n"
"v_mfma_f32_16x16x32_bf16 v[160:163],a[96:99],v[84:87],v[160:163]\n"
"ds_read_b128 a[64:67],v113 offset:0\n"
"v_cvt_pk_bf16_f32 v71,v119,v120\n"
"v_mfma_f32_16x16x32_bf16 v[192:195],a[96:99],v[88:91],v[192:195]\n"
"s_add_u32 m0,s67,36864\n"
"v_exp_f32 v119,v18\n"
"v_mfma_f32_16x16x32_bf16 v[224:227],a[96:99],v[92:95],v[224:227]\n"
"ds_read_b128 a[68:71],v113 offset:1024\n"
"v_cvt_pk_bf16_f32 v72,v122,v123\n"
"v_mfma_f32_16x16x32_bf16 a[140:143],a[144:147],v[92:95],a[140:143]\n"
"v_exp_f32 v120,v19\n"
"v_mfma_f32_16x16x32_bf16 v[228:231],a[100:103],v[92:95],v[228:231]\n"
"ds_read_b128 a[72:75],v113 offset:2048\n"
"ds_read_b128 a[76:79],v113 offset:3072\n"
"v_mfma_f32_16x16x32_bf16 v[132:135],a[100:103],v[80:83],v[132:135]\n"
"v_exp_f32 v122,v20\n"
"v_mfma_f32_16x16x32_bf16 v[164:167],a[100:103],v[84:87],v[164:167]\n"
"ds_read_b128 a[80:83],v113 offset:4096\n"
"v_cvt_pk_bf16_f32 v73,v119,v120\n"
"v_mfma_f32_16x16x32_bf16 v[196:199],a[100:103],v[88:91],v[196:199]\n"
"v_exp_f32 v123,v21\n"
"v_mfma_f32_16x16x32_bf16 a[136:139],a[144:147],v[88:91],a[136:139]\n"
"ds_read_b128 a[84:87],v113 offset:5120\n"
"ds_read_b128 a[88:91],v113 offset:6144\n"
"v_mfma_f32_16x16x32_bf16 v[200:203],a[104:107],v[88:91],v[200:203]\n"
"v_exp_f32 v119,v22\n"
"v_mfma_f32_16x16x32_bf16 v[232:235],a[104:107],v[92:95],v[232:235]\n"
"ds_read_b128 a[92:95],v113 offset:7168\n"
"v_cvt_pk_bf16_f32 v74,v122,v123\n"
"v_mfma_f32_16x16x32_bf16 v[136:139],a[104:107],v[80:83],v[136:139]\n"
"v_exp_f32 v120,v23\n"
"v_mfma_f32_16x16x32_bf16 v[168:171],a[104:107],v[84:87],v[168:171]\n"
"buffer_load_dwordx4 v117,s[24:27],s64 offen offset:0 sc1 lds\n"
"v_mfma_f32_16x16x32_bf16 a[132:135],a[144:147],v[84:87],a[132:135]\n"
"v_exp_f32 v122,v24\n"
"v_cvt_pk_bf16_f32 v75,v119,v120\n"
"v_mfma_f32_16x16x32_bf16 v[172:175],a[108:111],v[84:87],v[172:175]\n"
"buffer_store_dwordx4 a[188:191],v112,s[24:27],s3 offen offset:3072 sc1\n"
"v_mfma_f32_16x16x32_bf16 v[204:207],a[108:111],v[88:91],v[204:207]\n"
"v_exp_f32 v123,v25\n"
"v_mfma_f32_16x16x32_bf16 v[236:239],a[108:111],v[92:95],v[236:239]\n"
"buffer_load_dwordx4 v117,s[24:27],s64 offen offset:1024 sc1 lds\n"
"v_mfma_f32_16x16x32_bf16 v[140:143],a[108:111],v[80:83],v[140:143]\n"
"v_exp_f32 v119,v26\n"
"v_mfma_f32_16x16x32_bf16 a[128:131],a[144:147],v[80:83],a[128:131]\n"
"v_cvt_pk_bf16_f32 v76,v122,v123\n"
"s_or_b32 s3,s3,0x3c000\n"
"v_mfma_f32_16x16x32_bf16 v[144:147],a[112:115],v[80:83],v[144:147]\n"
"v_exp_f32 v120,v27\n"
"v_mfma_f32_16x16x32_bf16 v[176:179],a[112:115],v[84:87],v[176:179]\n"
"buffer_load_dwordx4 v117,s[24:27],s64 offen offset:2048 sc1 lds\n"
"v_mfma_f32_16x16x32_bf16 v[208:211],a[112:115],v[88:91],v[208:211]\n"
"v_exp_f32 v122,v28\n"
"v_mfma_f32_16x16x32_bf16 v[240:243],a[112:115],v[92:95],v[240:243]\n"
"v_cvt_pk_bf16_f32 v77,v119,v120\n"
"v_or3_b32 v118,v118,v69,v70\n"
"v_mfma_f32_16x16x32_bf16 v[244:247],a[116:119],v[92:95],v[244:247]\n"
"v_exp_f32 v123,v29\n"
"v_mfma_f32_16x16x32_bf16 v[148:151],a[116:119],v[80:83],v[148:151]\n"
"buffer_load_dwordx4 v117,s[24:27],s64 offen offset:3072 sc1 lds\n"
"v_mfma_f32_16x16x32_bf16 v[180:183],a[116:119],v[84:87],v[180:183]\n"
"s_add_u32 s64,s64,16384\n"
"v_exp_f32 v119,v30\n"
"v_cvt_pk_bf16_f32 v78,v122,v123\n"
"v_mfma_f32_16x16x32_bf16 v[212:215],a[116:119],v[88:91],v[212:215]\n"
"buffer_store_dwordx4 a[236:239],v112,s[24:27],s56 offen offset:3072 sc1\n"
"v_mfma_f32_16x16x32_bf16 v[216:219],a[120:123],v[88:91],v[216:219]\n"
"v_exp_f32 v120,v31\n"
"v_mfma_f32_16x16x32_bf16 v[248:251],a[120:123],v[92:95],v[248:251]\n"
"v_cvt_pk_bf16_f32 v79,v119,v120\n"
"v_or3_b32 v118,v118,v71,v72\n"
"v_mfma_f32_16x16x32_bf16 v[152:155],a[120:123],v[80:83],v[152:155]\n"
"v_or3_b32 v118,v118,v73,v74\n"
"v_or3_b32 v118,v118,v75,v76\n"
"v_mfma_f32_16x16x32_bf16 v[184:187],a[120:123],v[84:87],v[184:187]\n"
"v_or3_b32 v118,v118,v77,v78\n"
"v_or_b32 v118,v118,v79\n"
"v_mfma_f32_16x16x32_bf16 v[188:191],a[124:127],v[84:87],v[188:191]\n"
"v_and_b32 v118,0x40004000,v118\n"
"v_cmp_ne_u32 vcc,0,v118\n"
"v_mfma_f32_16x16x32_bf16 v[220:223],a[124:127],v[88:91],v[220:223]\n"
"v_mfma_f32_16x16x32_bf16 v[252:255],a[124:127],v[92:95],v[252:255]\n"
"v_mfma_f32_16x16x32_bf16 v[156:159],a[124:127],v[80:83],v[156:159]\n"
"s_cbranch_vccnz .Lfk_cs_h2\n"
".Lfk_cr_h2:\n"
"s_waitcnt vmcnt(27) lgkmcnt(0)\n"
"s_barrier\n"
".Lfk_g1:\n"
"s_and_b32 s82,s78,4096\n"
"s_lshl1_add_u32 s82,s82,69632\n"
"s_cmp_lt_u32 s52,2\n"
"s_cselect_b32 s64,s64,s62\n"
"s_cselect_b32 s67,s67,s82\n"
".Lfk_F:\n"
"s_add_u32 s89,s89,1\n"
"s_cmp_ge_u32 s89,s60\n"
"s_cbranch_scc1 .Lfk_Fx\n"
"v_mfma_f32_16x16x32_bf16 v[0:3],a[64:67],a[0:3],v[96:99]\n"
"s_swappc_b64 s[94:95],s[20:21]\n"
"v_exp_f32 v122,v32\n"
"v_mfma_f32_16x16x32_bf16 v[0:3],a[68:71],a[4:7],v[0:3]\n"
"v_exp_f32 v123,v33\n"
"v_mfma_f32_16x16x32_bf16 v[0:3],a[72:75],a[8:11],v[0:3]\n"
"ds_read_b128 a[96:99],v114 offset:24576\n"
"ds_read_b128 a[100:103],v114 offset:25600\n"
"v_mfma_f32_16x16x32_bf16 v[0:3],a[76:79],a[12:15],v[0:3]\n"
"v_exp_f32 v119,v34\n"
"v_mfma_f32_16x16x32_bf16 v[4:7],a[92:95],a[12:15],v[96:99]\n"
"s_add_u32 s101,s101,4\n"
"ds_read_b128 a[104:107],v114 offset:26624\n"
"v_cvt_pk_bf16_f32 v80,v122,v123\n"
"v_mfma_f32_16x16x32_bf16 v[4:7],a[88:91],a[8:11],v[4:7]\n"
"v_exp_f32 v120,v35\n"
"v_mfma_f32_16x16x32_bf16 v[4:7],a[84:87],a[4:7],v[4:7]\n"
"ds_read_b128 a[108:111],v114 offset:27648\n"
"ds_read_b128 a[112:115],v114 offset:28672\n"
"v_mfma_f32_16x16x32_bf16 v[4:7],a[80:83],a[0:3],v[4:7]\n"
"v_exp_f32 v122,v36\n"
"v_mfma_f32_16x16x32_bf16 v[12:15],a[80:83],a[16:19],v[100:103]\n"
"ds_read_b128 a[116:119],v114 offset:29696\n"
"v_cvt_pk_bf16_f32 v81,v119,v120\n"
"v_mfma_f32_16x16x32_bf16 v[12:15],a[84:87],a[20:23],v[12:15]\n"
"v_exp_f32 v123,v37\n"
"v_mfma_f32_16x16x32_bf16 v[12:15],a[88:91],a[24:27],v[12:15]\n"
"ds_read_b128 a[120:123],v114 offset:30720\n"
"ds_read_b128 a[124:127],v114 offset:31744\n"
"v_mfma_f32_16x16x32_bf16 v[12:15],a[92:95],a[28:31],v[12:15]\n"
"s_add_u32 m0,s67,0\n"
"v_exp_f32 v119,v38\n"
"v_mfma_f32_16x16x32_bf16 v[8:11],a[76:79],a[28:31],v[100:103]\n"
"v_cvt_pk_bf16_f32 v82,v122,v123\n"
"v_or3_b32 v118,v80,v81,v82\n"
"v_mfma_f32_16x16x32_bf16 v[8:11],a[72:75],a[24:27],v[8:11]\n"
"v_exp_f32 v120,v39\n"
"v_mfma_f32_16x16x32_bf16 v[8:11],a[68:71],a[20:23],v[8:11]\n"
"buffer_load_dwordx4 v112,s[24:27],s64 offen offset:0 sc1 lds\n"
"v_mfma_f32_16x16x32_bf16 v[8:11],a[64:67],a[16:19],v[8:11]\n"
"v_exp_f32 v122,v40\n"
"v_mfma_f32_16x16x32_bf16 v[16:19],a[64:67],a[32:35],v[104:107]\n"
"v_cvt_pk_bf16_f32 v83,v119,v120\n"
"v_mfma_f32_16x16x32_bf16 v[16:19],a[68:71],a[36:39],v[16:19]\n"
"v_exp_f32 v123,v41\n"
"v_mfma_f32_16x16x32_bf16 v[16:19],a[72:75],a[40:43],v[16:19]\n"
"buffer_load_dwordx4 v112,s[24:27],s64 offen offset:1024 sc1 lds\n"
"v_mfma_f32_16x16x32_bf16 v[16:19],a[76:79],a[44:47],v[16:19]\n"
"v_exp_f32 v119,v42\n"
"v_mfma_f32_16x16x32_bf16 v[20:23],a[92:95],a[44:47],v[104:107]\n"
"v_cvt_pk_bf16_f32 v84,v122,v123\n"
"v_or3_b32 v118,v118,v83,v84\n"
"v_mfma_f32_16x16x32_bf16 v[20:23],a[88:91],a[40:43],v[20:23]\n"
"v_exp_f32 v120,v43\n"
"v_mfma_f32_16x16x32_bf16 v[20:23],a[84:87],a[36:39],v[20:23]\n"
"v_cvt_pk_bf16_f32 v85,v119,v120\n"
"v_mfma_f32_16x16x32_bf16 v[20:23],a[80:83],a[32:35],v[20:23]\n"
"v_exp_f32 v122,v44\n"
"v_mfma_f32_16x16x32_bf16 v[28:31],a[80:83],a[48:51],v[108:111]\n"
"buffer_load_dwordx4 v112,s[24:27],s64 offen offset:2048 sc1 lds\n"
"v_mfma_f32_16x16x32_bf16 v[28:31],a[84:87],a[52:55],v[28:31]\n"
"v_exp_f32 v123,v45\n"
"v_mfma_f32_16x16x32_bf16 v[28:31],a[88:91],a[56:59],v[28:31]\n"
"v_cvt_pk_bf16_f32 v86,v122,v123\n"
"v_or3_b32 v118,v118,v85,v86\n"
"v_mfma_f32_16x16x32_bf16 v[28:31],a[92:95],a[60:63],v[28:31]\n"
"v_exp_f32 v119,v46\n"
"v_mfma_f32_16x16x32_bf16 v[24:27],a[76:79],a[60:63],v[108:111]\n"
"v_exp_f32 v122,v48\n"
"v_mfma_f32_16x16x32_bf16 v[24:27],a[72:75],a[56:59],v[24:27]\n"
"v_exp_f32 v120,v47\n"
"v_mfma_f32_16x16x32_bf16 v[24:27],a[68:71],a[52:55],v[24:27]\n"
"buffer_load_dwordx4 v112,s[24:27],s64 offen offset:3072 sc1 lds\n"
"v_mfma_f32_16x16x32_bf16 v[24:27],a[64:67],a[48:51],v[24:27]\n"
"s_add_u32 s64,s64,16384\n"
"s_waitcnt lgkmcnt(0)\n"
"v_mfma_f32_16x16x32_bf16 v[128:131],a[96:99],v[64:67],v[128:131]\n"
"s_andn2_b32 m0,s55,16384\n"
"v_exp_f32 v123,v49\n"
"v_mfma_f32_16x16x32_bf16 v[160:163],a[96:99],v[68:71],v[160:163]\n"
"ds_read_b128 a[64:67],v113 offset:16384\n"
"v_cvt_pk_bf16_f32 v87,v119,v120\n"
"v_mfma_f32_16x16x32_bf16 v[192:195],a[96:99],v[72:75],v[192:195]\n"
"s_add_u32 s54,s54,s22\n"
"v_exp_f32 v119,v50\n"
"v_mfma_f32_16x16x32_bf16 v[224:227],a[96:99],v[76:79],v[224:227]\n"
"ds_read_b128 a[68:71],v113 offset:17408\n"
"v_cvt_pk_bf16_f32 v88,v122,v123\n"
"v_mfma_f32_16x16x32_bf16 a[140:143],a[144:147],v[76:79],a[140:143]\n"
"v_exp_f32 v120,v51\n"
"v_mfma_f32_16x16x32_bf16 v[228:231],a[100:103],v[76:79],v[228:231]\n"
"ds_read_b128 a[72:75],v113 offset:18432\n"
"ds_read_b128 a[76:79],v113 offset:19456\n"
"v_mfma_f32_16x16x32_bf16 v[132:135],a[100:103],v[64:67],v[132:135]\n"
"v_exp_f32 v122,v52\n"
"v_mfma_f32_16x16x32_bf16 v[164:167],a[100:103],v[68:71],v[164:167]\n"
"ds_read_b128 a[80:83],v113 offset:20480\n"
"v_cvt_pk_bf16_f32 v89,v119,v120\n"
"v_mfma_f32_16x16x32_bf16 v[196:199],a[100:103],v[72:75],v[196:199]\n"
"v_exp_f32 v123,v53\n"
"v_mfma_f32_16x16x32_bf16 a[136:139],a[144:147],v[72:75],a[136:139]\n"
"ds_read_b128 a[84:87],v113 offset:21504\n"
"ds_read_b128 a[88:91],v113 offset:22528\n"
"v_mfma_f32_16x16x32_bf16 v[200:203],a[104:107],v[72:75],v[200:203]\n"
"v_exp_f32 v119,v54\n"
"v_mfma_f32_16x16x32_bf16 v[232:235],a[104:107],v[76:79],v[232:235]\n"
"ds_read_b128 a[92:95],v113 offset:23552\n"
"v_cvt_pk_bf16_f32 v90,v122,v123\n"
"v_mfma_f32_16x16x32_bf16 v[136:139],a[104:107],v[64:67],v[136:139]\n"
"v_exp_f32 v120,v55\n"
"v_mfma_f32_16x16x32_bf16 v[168:171],a[104:107],v[68:71],v[168:171]\n"
"s_waitcnt vmcnt(11)\n"
"ds_add_u32 v124,a152\n"
"v_mfma_f32_16x16x32_bf16 a[132:135],a[144:147],v[68:71],a[132:135]\n"
"v_exp_f32 v122,v56\n"
"v_mfma_f32_16x16x32_bf16 v[172:175],a[108:111],v[68:71],v[172:175]\n"
"v_cvt_pk_bf16_f32 v91,v119,v120\n"
"v_or3_b32 v118,v118,v87,v88\n"
"v_mfma_f32_16x16x32_bf16 v[204:207],a[108:111],v[72:75],v[204:207]\n"
"v_exp_f32 v123,v57\n"
"v_mfma_f32_16x16x32_bf16 v[236:239],a[108:111],v[76:79],v[236:239]\n"
"buffer_load_dwordx4 v115,s[32:35],s54 offen sc1 lds\n"
"v_mfma_f32_16x16x32_bf16 v[140:143],a[108:111],v[64:67],v[140:143]\n"
"v_exp_f32 v119,v58\n"
"v_mfma_f32_16x16x32_bf16 a[128:131],a[144:147],v[64:67],a[128:131]\n"
"v_cvt_pk_bf16_f32 v92,v122,v123\n"
"v_or3_b32 v118,v118,v89,v90\n"
"v_mfma_f32_16x16x32_bf16 v[144:147],a[112:115],v[64:67],v[144:147]\n"
"v_exp_f32 v120,v59\n"
"v_mfma_f32_16x16x32_bf16 v[176:179],a[112:115],v[68:71],v[176:179]\n"
"v_cvt_pk_bf16_f32 v93,v119,v120\n"
"v_or3_b32 v118,v118,v91,v92\n"
"v_mfma_f32_16x16x32_bf16 v[208:211],a[112:115],v[72:75],v[208:211]\n"
"v_exp_f32 v122,v60\n"
"v_mfma_f32_16x16x32_bf16 v[240:243],a[112:115],v[76:79],v[240:243]\n"
"v_mfma_f32_16x16x32_bf16 v[244:247],a[116:119],v[76:79],v[244:247]\n"
"v_exp_f32 v123,v61\n"
"v_mfma_f32_16x16x32_bf16 v[148:151],a[116:119],v[64:67],v[148:151]\n"
"v_cvt_pk_bf16_f32 v94,v122,v123\n"
"v_or3_b32 v118,v118,v93,v94\n"
"v_mfma_f32_16x16x32_bf16 v[180:183],a[116:119],v[68:71],v[180:183]\n"
"v_exp_f32 v119,v62\n"
"v_mfma_f32_16x16x32_bf16 v[212:215],a[116:119],v[72:75],v[212:215]\n"
"ds_read_b32 v125,v124\n"
"buffer_store_dwordx4 a[240:243],v112,s[24:27],s56 offen offset:0 sc1\n"
"v_mfma_f32_16x16x32_bf16 v[216:219],a[120:123],v[72:75],v[216:219]\n"
"v_exp_f32 v120,v63\n"
"v_mfma_f32_16x16x32_bf16 v[248:251],a[120:123],v[76:79],v[248:251]\n"
"v_cvt_pk_bf16_f32 v95,v119,v120\n"
"v_mfma_f32_16x16x32_bf16 v[152:155],a[120:123],v[64:67],v[152:155]\n"
"v_mfma_f32_16x16x32_bf16 v[184:187],a[120:123],v[68:71],v[184:187]\n"
"v_mfma_f32_16x16x32_bf16 v[188:191],a[124:127],v[68:71],v[188:191]\n"
"s_waitcnt lgkmcnt(0)\n"
"v_subrev_u32 v125,s101,v125\n"
"v_or3_b32 v125,v118,v95,v125\n"
"v_mfma_f32_16x16x32_bf16 v[220:223],a[124:127],v[72:75],v[220:223]\n"
"v_and_b32 v125,0x40004000,v125\n"
"v_cmp_ne_u32 vcc,0,v125\n"
"v_mfma_f32_16x16x32_bf16 v[252:255],a[124:127],v[76:79],v[252:255]\n"
"v_mfma_f32_16x16x32_bf16 v[156:159],a[124:127],v[64:67],v[156:159]\n"
"s_cbranch_vccnz .Lfk_cs_f3\n"
".Lfk_cr_f3:\n"
"v_mfma_f32_16x16x32_bf16 v[32:35],a[64:67],a[0:3],v[96:99]\n"
"s_add_u32 s101,s101,4\n"
"v_exp_f32 v122,v0\n"
"v_mfma_f32_16x16x32_bf16 v[32:35],a[68:71],a[4:7],v[32:35]\n"
"s_add_u32 m0,s55,4096\n"
"v_exp_f32 v123,v1\n"
"v_mfma_f32_16x16x32_bf16 v[32:35],a[72:75],a[8:11],v[32:35]\n"
"ds_read_b128 a[96:99],v114 offset:57344\n"
"ds_read_b128 a[100:103],v114 offset:58368\n"
"v_mfma_f32_16x16x32_bf16 v[32:35],a[76:79],a[12:15],v[32:35]\n"
"s_add_u32 s54,s54,s22\n"
"v_exp_f32 v119,v2\n"
"v_mfma_f32_16x16x32_bf16 v[36:39],a[92:95],a[12:15],v[96:99]\n"
"ds_read_b128 a[104:107],v114 offset:59392\n"
"v_cvt_pk_bf16_f32 v64,v122,v123\n"
"v_mfma_f32_16x16x32_bf16 v[36:39],a[88:91],a[8:11],v[36:39]\n"
"v_exp_f32 v120,v3\n"
"v_mfma_f32_16x16x32_bf16 v[36:39],a[84:87],a[4:7],v[36:39]\n"
"ds_read_b128 a[108:111],v114 offset:60416\n"
"ds_read_b128 a[112:115],v114 offset:61440\n"
"v_mfma_f32_16x16x32_bf16 v[36:39],a[80:83],a[0:3],v[36:39]\n"
"v_exp_f32 v122,v4\n"
"v_mfma_f32_16x16x32_bf16 v[44:47],a[80:83],a[16:19],v[100:103]\n"
"ds_read_b128 a[116:119],v114 offset:62464\n"
"v_cvt_pk_bf16_f32 v65,v119,v120\n"
"v_mfma_f32_16x16x32_bf16 v[44:47],a[84:87],a[20:23],v[44:47]\n"
"v_exp_f32 v123,v5\n"
"v_mfma_f32_16x16x32_bf16 v[44:47],a[88:91],a[24:27],v[44:47]\n"
"ds_read_b128 a[120:123],v114 offset:63488\n"
"ds_read_b128 a[124:127],v114 offset:64512\n"
"v_mfma_f32_16x16x32_bf16 v[44:47],a[92:95],a[28:31],v[44:47]\n"
"v_exp_f32 v119,v6\n"
"v_mfma_f32_16x16x32_bf16 v[40:43],a[76:79],a[28:31],v[100:103]\n"
"v_cvt_pk_bf16_f32 v66,v122,v123\n"
"v_or3_b32 v118,v64,v65,v66\n"
"v_mfma_f32_16x16x32_bf16 v[40:43],a[72:75],a[24:27],v[40:43]\n"
"v_exp_f32 v120,v7\n"
"v_mfma_f32_16x16x32_bf16 v[40:43],a[68:71],a[20:23],v[40:43]\n"
"buffer_load_dwordx4 v115,s[32:35],s54 offen sc1 lds\n"
"v_mfma_f32_16x16x32_bf16 v[40:43],a[64:67],a[16:19],v[40:43]\n"
"s_add_u32 m0,s67,16384\n"
"v_exp_f32 v122,v8\n"
"v_mfma_f32_16x16x32_bf16 v[48:51],a[64:67],a[32:35],v[104:107]\n"
"v_cvt_pk_bf16_f32 v67,v119,v120\n"
"v_mfma_f32_16x16x32_bf16 v[48:51],a[68:71],a[36:39],v[48:51]\n"
"v_exp_f32 v123,v9\n"
"v_mfma_f32_16x16x32_bf16 v[48:51],a[72:75],a[40:43],v[48:51]\n"
"buffer_load_dwordx4 v112,s[24:27],s64 offen offset:0 sc1 lds\n"
"v_mfma_f32_16x16x32_bf16 v[48:51],a[76:79],a[44:47],v[48:51]\n"
"v_exp_f32 v119,v10\n"
"v_mfma_f32_16x16x32_bf16 v[52:55],a[92:95],a[44:47],v[104:107]\n"
"v_cvt_pk_bf16_f32 v68,v122,v123\n"
"v_or3_b32 v118,v118,v67,v68\n"
"v_mfma_f32_16x16x32_bf16 v[52:55],a[88:91],a[40:43],v[52:55]\n"
"v_exp_f32 v120,v11\n"
"v_mfma_f32_16x16x32_bf16 v[52:55],a[84:87],a[36:39],v[52:55]\n"
"buffer_load_dwordx4 v112,s[24:27],s64 offen offset:1024 sc1 lds\n"
"v_mfma_f32_16x16x32_bf16 v[52:55],a[80:83],a[32:35],v[52:55]\n"
"v_exp_f32 v122,v12\n"
"v_mfma_f32_16x16x32_bf16 v[60:63],a[80:83],a[48:51],v[108:111]\n"
"v_cvt_pk_bf16_f32 v69,v119,v120\n"
"v_mfma_f32_16x16x32_bf16 v[60:63],a[84:87],a[52:55],v[60:63]\n"
"v_exp_f32 v123,v13\n"
"v_mfma_f32_16x16x32_bf16 v[60:63],a[88:91],a[56:59],v[60:63]\n"
"buffer_load_dwordx4 v112,s[24:27],s64 offen offset:2048 sc1 lds\n"
"v_mfma_f32_16x16x32_bf16 v[60:63],a[92:95],a[60:63],v[60:63]\n"
"v_exp_f32 v119,v14\n"
"v_mfma_f32_16x16x32_bf16 v[56:59],a[76:79],a[60:63],v[108:111]\n"
"v_cvt_pk_bf16_f32 v70,v122,v123\n"
"v_or3_b32 v118,v118,v69,v70\n"
"v_mfma_f32_16x16x32_bf16 v[56:59],a[72:75],a[56:59],v[56:59]\n"
"v_exp_f32 v120,v15\n"
"v_mfma_f32_16x16x32_bf16 v[56:59],a[68:71],a[52:55],v[56:59]\n"
"buffer_load_dwordx4 v112,s[24:27],s64 offen offset:3072 sc1 lds\n"
"v_mfma_f32_16x16x32_bf16 v[56:59],a[64:67],a[48:51],v[56:59]\n"
"s_waitcnt lgkmcnt(0)\n"
"v_exp_f32 v122,v16\n"
"v_mfma_f32_16x16x32_bf16 v[128:131],a[96:99],v[80:83],v[128:131]\n"
"v_exp_f32 v123,v17\n"
"v_mfma_f32_16x16x32_bf16 v[160:163],a[96:99],v[84:87],v[160:163]\n"
"s_add_u32 s64,s64,16384\n"
"ds_read_b128 a[64:67],v113 offset:49152\n"
"v_cvt_pk_bf16_f32 v71,v119,v120\n"
"v_mfma_f32_16x16x32_bf16 v[192:195],a[96:99],v[88:91],v[192:195]\n"
"v_exp_f32 v119,v18\n"
"v_mfma_f32_16x16x32_bf16 v[224:227],a[96:99],v[92:95],v[224:227]\n"
"ds_read_b128 a[68:71],v113 offset:50176\n"
"v_cvt_pk_bf16_f32 v72,v122,v123\n"
"v_mfma_f32_16x16x32_bf16 a[140:143],a[144:147],v[92:95],a[140:143]\n"
"v_exp_f32 v120,v19\n"
"v_mfma_f32_16x16x32_bf16 v[228:231],a[100:103],v[92:95],v[228:231]\n"
"ds_read_b128 a[72:75],v113 offset:51200\n"
"ds_read_b128 a[76:79],v113 offset:52224\n"
"v_mfma_f32_16x16x32_bf16 v[132:135],a[100:103],v[80:83],v[132:135]\n"
"v_exp_f32 v122,v20\n"
"v_mfma_f32_16x16x32_bf16 v[164:167],a[100:103],v[84:87],v[164:167]\n"
"ds_read_b128 a[80:83],v113 offset:53248\n"
"v_cvt_pk_bf16_f32 v73,v119,v120\n"
"v_mfma_f32_16x16x32_bf16 v[196:199],a[100:103],v[88:91],v[196:199]\n"
"v_exp_f32 v123,v21\n"
"v_mfma_f32_16x16x32_bf16 a[136:139],a[144:147],v[88:91],a[136:139]\n"
"ds_read_b128 a[84:87],v113 offset:54272\n"
"ds_read_b128 a[88:91],v113 offset:55296\n"
"v_mfma_f32_16x16x32_bf16 v[200:203],a[104:107],v[88:91],v[200:203]\n"
"v_exp_f32 v119,v22\n"
"v_mfma_f32_16x16x32_bf16 v[232:235],a[104:107],v[92:95],v[232:235]\n"
"ds_read_b128 a[92:95],v113 offset:56320\n"
"v_cvt_pk_bf16_f32 v74,v122,v123\n"
"v_mfma_f32_16x16x32_bf16 v[136:139],a[104:107],v[80:83],v[136:139]\n"
"v_exp_f32 v120,v23\n"
"v_mfma_f32_16x16x32_bf16 v[168:171],a[104:107],v[84:87],v[168:171]\n"
"s_waitcnt vmcnt(12)\n"
"ds_add_u32 v124,a152\n"
"v_mfma_f32_16x16x32_bf16 a[132:135],a[144:147],v[84:87],a[132:135]\n"
"v_exp_f32 v122,v24\n"
"v_mfma_f32_16x16x32_bf16 v[172:175],a[108:111],v[84:87],v[172:175]\n"
"v_cvt_pk_bf16_f32 v75,v119,v120\n"
"v_or3_b32 v118,v118,v71,v72\n"
"v_mfma_f32_16x16x32_bf16 v[204:207],a[108:111],v[88:91],v[204:207]\n"
"v_exp_f32 v123,v25\n"
"v_mfma_f32_16x16x32_bf16 v[236:239],a[108:111],v[92:95],v[236:239]\n"
"v_cvt_pk_bf16_f32 v76,v122,v123\n"
"v_or3_b32 v118,v118,v73,v74\n"
"v_mfma_f32_16x16x32_bf16 v[140:143],a[108:111],v[80:83],v[140:143]\n"
"v_exp_f32 v119,v26\n"
"v_mfma_f32_16x16x32_bf16 a[128:131],a[144:147],v[80:83],a[128:131]\n"
"v_or3_b32 v118,v118,v75,v76\n"
"v_mfma_f32_16x16x32_bf16 v[144:147],a[112:115],v[80:83],v[144:147]\n"
"v_exp_f32 v120,v27\n"
"v_mfma_f32_16x16x32_bf16 v[176:179],a[112:115],v[84:87],v[176:179]\n"
"v_cvt_pk_bf16_f32 v77,v119,v120\n"
"v_mfma_f32_16x16x32_bf16 v[208:211],a[112:115],v[88:91],v[208:211]\n"
"v_exp_f32 v122,v28\n"
"v_mfma_f32_16x16x32_bf16 v[240:243],a[112:115],v[92:95],v[240:243]\n"
"v_mfma_f32_16x16x32_bf16 v[244:247],a[116:119],v[92:95],v[244:247]\n"
"v_exp_f32 v123,v29\n"
"v_mfma_f32_16x16x32_bf16 v[148:151],a[116:119],v[80:83],v[148:151]\n"
"v_cvt_pk_bf16_f32 v78,v122,v123\n"
"v_or3_b32 v118,v118,v77,v78\n"
"v_mfma_f32_16x16x32_bf16 v[180:183],a[116:119],v[84:87],v[180:183]\n"
"v_exp_f32 v119,v30\n"
"v_mfma_f32_16x16x32_bf16 v[212:215],a[116:119],v[88:91],v[212:215]\n"
"ds_read_b32 v125,v124\n"
"buffer_store_dwordx4 a[244:247],v112,s[24:27],s56 offen offset:1024 sc1\n"
"v_mfma_f32_16x16x32_bf16 v[216:219],a[120:123],v[88:91],v[216:219]\n"
"v_exp_f32 v120,v31\n"
"v_mfma_f32_16x16x32_bf16 v[248:251],a[120:123],v[92:95],v[248:251]\n"
"v_cvt_pk_bf16_f32 v79,v119,v120\n"
"v_mfma_f32_16x16x32_bf16 v[152:155],a[120:123],v[80:83],v[152:155]\n"
"v_mfma_f32_16x16x32_bf16 v[184:187],a[120:123],v[84:87],v[184:187]\n"
"v_mfma_f32_16x16x32_bf16 v[188:191],a[124:127],v[84:87],v[188:191]\n"
"s_waitcnt lgkmcnt(0)\n"
"v_subrev_u32 v125,s101,v125\n"
"v_or3_b32 v125,v118,v79,v125\n"
"v_mfma_f32_16x16x32_bf16 v[220:223],a[124:127],v[88:91],v[220:223]\n"
"v_and_b32 v125,0x40004000,v125\n"
"v_cmp_ne_u32 vcc,0,v125\n"
"v_mfma_f32_16x16x32_bf16 v[252:255],a[124:127],v[92:95],v[252:255]\n"
"v_mfma_f32_16x16x32_bf16 v[156:159],a[124:127],v[80:83],v[156:159]\n"
"s_cbranch_vccnz .Lfk_cs_f0\n"
".Lfk_cr_f0:\n"
"v_mfma_f32_16x16x32_bf16 v[0:3],a[64:67],a[0:3],v[96:99]\n"
"s_add_u32 s101,s101,4\n"
"v_exp_f32 v122,v32\n"
"v_mfma_f32_16x16x32_bf16 v[0:3],a[68:71],a[4:7],v[0:3]\n"
"s_add_u32 m0,s55,8192\n"
"v_exp_f32 v123,v33\n"
"v_mfma_f32_16x16x32_bf16 v[0:3],a[72:75],a[8:11],v[0:3]\n"
"ds_read_b128 a[96:99],v114 offset:40960\n"
"ds_read_b128 a[100:103],v114 offset:41984\n"
"v_mfma_f32_16x16x32_bf16 v[0:3],a[76:79],a[12:15],v[0:3]\n"
"s_add_u32 s54,s54,s22\n"
"v_exp_f32 v119,v34\n"
"v_mfma_f32_16x16x32_bf16 v[4:7],a[92:95],a[12:15],v[96:99]\n"
"ds_read_b128 a[104:107],v114 offset:43008\n"
"v_cvt_pk_bf16_f32 v80,v122,v123\n"
"v_mfma_f32_16x16x32_bf16 v[4:7],a[88:91],a[8:11],v[4:7]\n"
"v_exp_f32 v120,v35\n"
"v_mfma_f32_16x16x32_bf16 v[4:7],a[84:87],a[4:7],v[4:7]\n"
"ds_read_b128 a[108:111],v114 offset:44032\n"
"ds_read_b128 a[112:115],v114 offset:45056\n"
"v_mfma_f32_16x16x32_bf16 v[4:7],a[80:83],a[0:3],v[4:7]\n"
"v_exp_f32 v122,v36\n"
"v_mfma_f32_16x16x32_bf16 v[12:15],a[80:83],a[16:19],v[100:103]\n"
"ds_read_b128 a[116:119],v114 offset:46080\n"
"v_cvt_pk_bf16_f32 v81,v119,v120\n"
"v_mfma_f32_16x16x32_bf16 v[12:15],a[84:87],a[20:23],v[12:15]\n"
"v_exp_f32 v123,v37\n"
"v_mfma_f32_16x16x32_bf16 v[12:15],a[88:91],a[24:27],v[12:15]\n"
"ds_read_b128 a[120:123],v114 offset:47104\n"
"ds_read_b128 a[124:127],v114 offset:48128\n"
"v_mfma_f32_16x16x32_bf16 v[12:15],a[92:95],a[28:31],v[12:15]\n"
"v_exp_f32 v119,v38\n"
"v_mfma_f32_16x16x32_bf16 v[8:11],a[76:79],a[28:31],v[100:103]\n"
"v_cvt_pk_bf16_f32 v82,v122,v123\n"
"v_or3_b32 v118,v80,v81,v82\n"
"v_mfma_f32_16x16x32_bf16 v[8:11],a[72:75],a[24:27],v[8:11]\n"
"v_exp_f32 v120,v39\n"
"v_mfma_f32_16x16x32_bf16 v[8:11],a[68:71],a[20:23],v[8:11]\n"
"buffer_load_dwordx4 v115,s[32:35],s54 offen sc1 lds\n"
"v_mfma_f32_16x16x32_bf16 v[8:11],a[64:67],a[16:19],v[8:11]\n"
"s_add_u32 m0,s67,49152\n"
"v_exp_f32 v122,v40\n"
"v_mfma_f32_16x16x32_bf16 v[16:19],a[64:67],a[32:35],v[104:107]\n"
"v_cvt_pk_bf16_f32 v83,v119,v120\n"
"v_mfma_f32_16x16x32_bf16 v[16:19],a[68:71],a[36:39],v[16:19]\n"
"v_exp_f32 v123,v41\n"
"v_mfma_f32_16x16x32_bf16 v[16:19],a[72:75],a[40:43],v[16:19]\n"
"buffer_load_dwordx4 v112,s[24:27],s64 offen offset:0 sc1 lds\n"
"v_mfma_f32_16x16x32_bf16 v[16:19],a[76:79],a[44:47],v[16:19]\n"
"v_exp_f32 v119,v42\n"
"v_mfma_f32_16x16x32_bf16 v[20:23],a[92:95],a[44:47],v[104:107]\n"
"v_cvt_pk_bf16_f32 v84,v122,v123\n"
"v_or3_b32 v118,v118,v83,v84\n"
"v_mfma_f32_16x16x32_bf16 v[20:23],a[88:91],a[40:43],v[20:23]\n"
"v_exp_f32 v120,v43\n"
"v_mfma_f32_16x16x32_bf16 v[20:23],a[84:87],a[36:39],v[20:23]\n"
"buffer_load_dwordx4 v112,s[24:27],s64 offen offset:1024 sc1 lds\n"
"v_mfma_f32_16x16x32_bf16 v[20:23],a[80:83],a[32:35],v[20:23]\n"
"v_exp_f32 v122,v44\n"
"v_mfma_f32_16x16x32_bf16 v[28:31],a[80:83],a[48:51],v[108:111]\n"
"v_cvt_pk_bf16_f32 v85,v119,v120\n"
"v_mfma_f32_16x16x32_bf16 v[28:31],a[84:87],a[52:55],v[28:31]\n"
"v_exp_f32 v123,v45\n"
"v_mfma_f32_16x16x32_bf16 v[28:31],a[88:91],a[56:59],v[28:31]\n"
"buffer_load_dwordx4 v112,s[24:27],s64 offen offset:2048 sc1 lds\n"
"v_mfma_f32_16x16x32_bf16 v[28:31],a[92:95],a[60:63],v[28:31]\n"
"v_exp_f32 v119,v46\n"
"v_mfma_f32_16x16x32_bf16 v[24:27],a[76:79],a[60:63],v[108:111]\n"
"v_cvt_pk_bf16_f32 v86,v122,v123\n"
"v_or3_b32 v118,v118,v85,v86\n"
"v_mfma_f32_16x16x32_bf16 v[24:27],a[72:75],a[56:59],v[24:27]\n"
"v_exp_f32 v120,v47\n"
"v_mfma_f32_16x16x32_bf16 v[24:27],a[68:71],a[52:55],v[24:27]\n"
"buffer_load_dwordx4 v112,s[24:27],s64 offen offset:3072 sc1 lds\n"
"v_mfma_f32_16x16x32_bf16 v[24:27],a[64:67],a[48:51],v[24:27]\n"
"s_waitcnt lgkmcnt(0)\n"
"v_exp_f32 v122,v48\n"
"v_mfma_f32_16x16x32_bf16 v[128:131],a[96:99],v[64:67],v[128:131]\n"
"v_exp_f32 v123,v49\n"
"v_mfma_f32_16x16x32_bf16 v[160:163],a[96:99],v[68:71],v[160:163]\n"
"s_add_u32 s64,s64,16384\n"
"ds_read_b128 a[64:67],v113 offset:32768\n"
"v_cvt_pk_bf16_f32 v87,v119,v120\n"
"v_mfma_f32_16x16x32_bf16 v[192:195],a[96:99],v[72:75],v[192:195]\n"
"v_exp_f32 v119,v50\n"
"v_mfma_f32_16x16x32_bf16 v[224:227],a[96:99],v[76:79],v[224:227]\n"
"ds_read_b128 a[68:71],v113 offset:33792\n"
"v_cvt_pk_bf16_f32 v88,v122,v123\n"
"v_mfma_f32_16x16x32_bf16 a[140:143],a[144:147],v[76:79],a[140:143]\n"
"v_exp_f32 v120,v51\n"
"v_mfma_f32_16x16x32_bf16 v[228:231],a[100:103],v[76:79],v[228:231]\n"
"ds_read_b128 a[72:75],v113 offset:34816\n"
"ds_read_b128 a[76:79],v113 offset:35840\n"
"v_mfma_f32_16x16x32_bf16 v[132:135],a[100:103],v[64:67],v[132:135]\n"
"v_exp_f32 v122,v52\n"
"v_mfma_f32_16x16x32_bf16 v[164:167],a[100:103],v[68:71],v[164:167]\n"
"ds_read_b128 a[80:83],v113 offset:36864\n"
"v_cvt_pk_bf16_f32 v89,v119,v120\n"
"v_mfma_f32_16x16x32_bf16 v[196:199],a[100:103],v[72:75],v[196:199]\n"
"v_exp_f32 v123,v53\n"
"v_mfma_f32_16x16x32_bf16 a[136:139],a[144:147],v[72:75],a[136:139]\n"
"ds_read_b128 a[84:87],v113 offset:37888\n"
"ds_read_b128 a[88:91],v113 offset:38912\n"
"v_mfma_f32_16x16x32_bf16 v[200:203],a[104:107],v[72:75],v[200:203]\n"
"v_exp_f32 v119,v54\n"
"v_mfma_f32_16x16x32_bf16 v[232:235],a[104:107],v[76:79],v[232:235]\n"
"ds_read_b128 a[92:95],v113 offset:39936\n"
"v_cvt_pk_bf16_f32 v90,v122,v123\n"
"v_mfma_f32_16x16x32_bf16 v[136:139],a[104:107],v[64:67],v[136:139]\n"
"v_exp_f32 v120,v55\n"
"v_mfma_f32_16x16x32_bf16 v[168:171],a[104:107],v[68:71],v[168:171]\n"
"s_waitcnt vmcnt(12)\n"
"ds_add_u32 v124,a152\n"
"v_mfma_f32_16x16x32_bf16 a[132:135],a[144:147],v[68:71],a[132:135]\n"
"v_exp_f32 v122,v56\n"
"v_mfma_f32_16x16x32_bf16 v[172:175],a[108:111],v[68:71],v[172:175]\n"
"v_cvt_pk_bf16_f32 v91,v119,v120\n"
"v_or3_b32 v118,v118,v87,v88\n"
"v_mfma_f32_16x16x32_bf16 v[204:207],a[108:111],v[72:75],v[204:207]\n"
"v_exp_f32 v123,v57\n"
"v_mfma_f32_16x16x32_bf16 v[236:239],a[108:111],v[76:79],v[236:239]\n"
"v_cvt_pk_bf16_f32 v92,v122,v123\n"
"v_or3_b32 v118,v118,v89,v90\n"
"v_mfma_f32_16x16x32_bf16 v[140:143],a[108:111],v[64:67],v[140:143]\n"
"v_exp_f32 v119,v58\n"
"v_mfma_f32_16x16x32_bf16 a[128:131],a[144:147],v[64:67],a[128:131]\n"
"v_or3_b32 v118,v118,v91,v92\n"
"v_mfma_f32_16x16x32_bf16 v[144:147],a[112:115],v[64:67],v[144:147]\n"
"v_exp_f32 v120,v59\n"
"v_mfma_f32_16x16x32_bf16 v[176:179],a[112:115],v[68:71],v[176:179]\n"
"v_cvt_pk_bf16_f32 v93,v119,v120\n"
"v_mfma_f32_16x16x32_bf16 v[208:211],a[112:115],v[72:75],v[208:211]\n"
"v_exp_f32 v122,v60\n"
"v_mfma_f32_16x16x32_bf16 v[240:243],a[112:115],v[76:79],v[240:243]\n"
"v_mfma_f32_16x16x32_bf16 v[244:247],a[116:119],v[76:79],v[244:247]\n"
"v_exp_f32 v123,v61\n"
"v_mfma_f32_16x16x32_bf16 v[148:151],a[116:119],v[64:67],v[148:151]\n"
"v_cvt_pk_bf16_f32 v94,v122,v123\n"
"v_or3_b32 v118,v118,v93,v94\n"
"v_mfma_f32_16x16x32_bf16 v[180:183],a[116:119],v[68:71],v[180:183]\n"
"v_exp_f32 v119,v62\n"
"v_mfma_f32_16x16x32_bf16 v[212:215],a[116:119],v[72:75],v[212:215]\n"
"ds_read_b32 v125,v124\n"
"buffer_store_dwordx4 a[248:251],v112,s[24:27],s56 offen offset:2048 sc1\n"
"v_mfma_f32_16x16x32_bf16 v[216:219],a[120:123],v[72:75],v[216:219]\n"
"v_exp_f32 v120,v63\n"
"v_mfma_f32_16x16x32_bf16 v[248:251],a[120:123],v[76:79],v[248:251]\n"
"v_cvt_pk_bf16_f32 v95,v119,v120\n"
"v_mfma_f32_16x16x32_bf16 v[152:155],a[120:123],v[64:67],v[152:155]\n"
"v_mfma_f32_16x16x32_bf16 v[184:187],a[120:123],v[68:71],v[184:187]\n"
"v_mfma_f32_16x16x32_bf16 v[188:191],a[124:127],v[68:71],v[188:191]\n"
"s_waitcnt lgkmcnt(0)\n"
"v_subrev_u32 v125,s101,v125\n"
"v_or3_b32 v125,v118,v95,v125\n"
"v_mfma_f32_16x16x32_bf16 v[220:223],a[124:127],v[72:75],v[220:223]\n"
"v_and_b32 v125,0x40004000,v125\n"
"v_cmp_ne_u32 vcc,0,v125\n"
"v_mfma_f32_16x16x32_bf16 v[252:255],a[124:127],v[76:79],v[252:255]\n"
"v_mfma_f32_16x16x32_bf16 v[156:159],a[124:127],v[64:67],v[156:159]\n"
"s_cbranch_vccnz .Lfk_cs_f1\n"
".Lfk_cr_f1:\n"
"v_mfma_f32_16x16x32_bf16 v[32:35],a[64:67],a[0:3],v[96:99]\n"
"s_add_u32 s101,s101,4\n"
"v_exp_f32 v122,v0\n"
"v_mfma_f32_16x16x32_bf16 v[32:35],a[68:71],a[4:7],v[32:35]\n"
"s_add_u32 m0,s55,12288\n"
"v_exp_f32 v123,v1\n"
"v_mfma_f32_16x16x32_bf16 v[32:35],a[72:75],a[8:11],v[32:35]\n"
"ds_read_b128 a[96:99],v114 offset:8192\n"
"ds_read_b128 a[100:103],v114 offset:9216\n"
"v_mfma_f32_16x16x32_bf16 v[32:35],a[76:79],a[12:15],v[32:35]\n"
"s_add_u32 s54,s54,s22\n"
"v_exp_f32 v119,v2\n"
"v_mfma_f32_16x16x32_bf16 v[36:39],a[92:95],a[12:15],v[96:99]\n"
"ds_read_b128 a[104:107],v114 offset:10240\n"
"v_cvt_pk_bf16_f32 v64,v122,v123\n"
"v_mfma_f32_16x16x32_bf16 v[36:39],a[88:91],a[8:11],v[36:39]\n"
"v_exp_f32 v120,v3\n"
"v_mfma_f32_16x16x32_bf16 v[36:39],a[84:87],a[4:7],v[36:39]\n"
"ds_read_b128 a[108:111],v114 offset:11264\n"
"ds_read_b128 a[112:115],v114 offset:12288\n"
"v_mfma_f32_16x16x32_bf16 v[36:39],a[80:83],a[0:3],v[36:39]\n"
"v_exp_f32 v122,v4\n"
"v_mfma_f32_16x16x32_bf16 v[44:47],a[80:83],a[16:19],v[100:103]\n"
"ds_read_b128 a[116:119],v114 offset:13312\n"
"v_cvt_pk_bf16_f32 v65,v119,v120\n"
"v_mfma_f32_16x16x32_bf16 v[44:47],a[84:87],a[20:23],v[44:47]\n"
"v_exp_f32 v123,v5\n"
"v_mfma_f32_16x16x32_bf16 v[44:47],a[88:91],a[24:27],v[44:47]\n"
"ds_read_b128 a[120:123],v114 offset:14336\n"
"ds_read_b128 a[124:127],v114 offset:15360\n"
"v_mfma_f32_16x16x32_bf16 v[44:47],a[92:95],a[28:31],v[44:47]\n"
"v_exp_f32 v119,v6\n"
"v_mfma_f32_16x16x32_bf16 v[40:43],a[76:79],a[28:31],v[100:103]\n"
"v_cvt_pk_bf16_f32 v66,v122,v123\n"
"v_or3_b32 v118,v64,v65,v66\n"
"v_mfma_f32_16x16x32_bf16 v[40:43],a[72:75],a[24:27],v[40:43]\n"
"v_exp_f32 v120,v7\n"
"v_mfma_f32_16x16x32_bf16 v[40:43],a[68:71],a[20:23],v[40:43]\n"
"buffer_load_dwordx4 v115,s[32:35],s54 offen sc1 lds\n"
"v_mfma_f32_16x16x32_bf16 v[40:43],a[64:67],a[16:19],v[40:43]\n"
"s_add_u32 m0,s67,32768\n"
"v_exp_f32 v122,v8\n"
"v_mfma_f32_16x16x32_bf16 v[48:51],a[64:67],a[32:35],v[104:107]\n"
"v_cvt_pk_bf16_f32 v67,v119,v120\n"
"v_mfma_f32_16x16x32_bf16 v[48:51],a[68:71],a[36:39],v[48:51]\n"
"v_exp_f32 v123,v9\n"
"v_mfma_f32_16x16x32_bf16 v[48:51],a[72:75],a[40:43],v[48:51]\n"
"buffer_load_dwordx4 v112,s[24:27],s64 offen offset:0 sc1 lds\n"
"v_mfma_f32_16x16x32_bf16 v[48:51],a[76:79],a[44:47],v[48:51]\n"
"v_exp_f32 v119,v10\n"
"v_mfma_f32_16x16x32_bf16 v[52:55],a[92:95],a[44:47],v[104:107]\n"
"v_cvt_pk_bf16_f32 v68,v122,v123\n"
"v_or3_b32 v118,v118,v67,v68\n"
"v_mfma_f32_16x16x32_bf16 v[52:55],a[88:91],a[40:43],v[52:55]\n"
"v_exp_f32 v120,v11\n"
"v_mfma_f32_16x16x32_bf16 v[52:55],a[84:87],a[36:39],v[52:55]\n"
"buffer_load_dwordx4 v112,s[24:27],s64 offen offset:1024 sc1 lds\n"
"v_mfma_f32_16x16x32_bf16 v[52:55],a[80:83],a[32:35],v[52:55]\n"
"v_exp_f32 v122,v12\n"
"v_mfma_f32_16x16x32_bf16 v[60:63],a[80:83],a[48:51],v[108:111]\n"
"v_cvt_pk_bf16_f32 v69,v119,v120\n"
"v_mfma_f32_16x16x32_bf16 v[60:63],a[84:87],a[52:55],v[60:63]\n"
"v_exp_f32 v123,v13\n"
"v_mfma_f32_16x16x32_bf16 v[60:63],a[88:91],a[56:59],v[60:63]\n"
"buffer_load_dwordx4 v112,s[24:27],s64 offen offset:2048 sc1 lds\n"
"v_mfma_f32_16x16x32_bf16 v[60:63],a[92:95],a[60:63],v[60:63]\n"
"v_exp_f32 v119,v14\n"
"v_mfma_f32_16x16x32_bf16 v[56:59],a[76:79],a[60:63],v[108:111]\n"
"v_cvt_pk_bf16_f32 v70,v122,v123\n"
"v_or3_b32 v118,v118,v69,v70\n"
"v_mfma_f32_16x16x32_bf16 v[56:59],a[72:75],a[56:59],v[56:59]\n"
"v_exp_f32 v120,v15\n"
"v_mfma_f32_16x16x32_bf16 v[56:59],a[68:71],a[52:55],v[56:59]\n"
"buffer_load_dwordx4 v112,s[24:27],s64 offen offset:3072 sc1 lds\n"
"v_mfma_f32_16x16x32_bf16 v[56:59],a[64:67],a[48:51],v[56:59]\n"
"s_waitcnt lgkmcnt(0)\n"
"v_exp_f32 v122,v16\n"
"v_mfma_f32_16x16x32_bf16 v[128:131],a[96:99],v[80:83],v[128:131]\n"
"v_exp_f32 v123,v17\n"
"v_mfma_f32_16x16x32_bf16 v[160:163],a[96:99],v[84:87],v[160:163]\n"
"s_add_u32 s64,s64,16384\n"
"ds_read_b128 a[64:67],v113 offset:0\n"
"v_cvt_pk_bf16_f32 v71,v119,v120\n"
"v_mfma_f32_16x16x32_bf16 v[192:195],a[96:99],v[88:91],v[192:195]\n"
"v_exp_f32 v119,v18\n"
"v_mfma_f32_16x16x32_bf16 v[224:227],a[96:99],v[92:95],v[224:227]\n"
"ds_read_b128 a[68:71],v113 offset:1024\n"
"v_cvt_pk_bf16_f32 v72,v122,v123\n"
"v_mfma_f32_16x16x32_bf16 a[140:143],a[144:147],v[92:95],a[140:143]\n"
"v_exp_f32 v120,v19\n"
"v_mfma_f32_16x16x32_bf16 v[228:231],a[100:103],v[92:95],v[228:231]\n"
"ds_read_b128 a[72:75],v113 offset:2048\n"
"ds_read_b128 a[76:79],v113 offset:3072\n"
"v_mfma_f32_16x16x32_bf16 v[132:135],a[100:103],v[80:83],v[132:135]\n"
"v_exp_f32 v122,v20\n"
"v_mfma_f32_16x16x32_bf16 v[164:167],a[100:103],v[84:87],v[164:167]\n"
"ds_read_b128 a[80:83],v113 offset:4096\n"
"v_cvt_pk_bf16_f32 v73,v119,v120\n"
"v_mfma_f32_16x16x32_bf16 v[196:199],a[100:103],v[88:91],v[196:199]\n"
"v_exp_f32 v123,v21\n"
"v_mfma_f32_16x16x32_bf16 a[136:139],a[144:147],v[88:91],a[136:139]\n"
"ds_read_b128 a[84:87],v113 offset:5120\n"
"ds_read_b128 a[88:91],v113 offset:6144\n"
"v_mfma_f32_16x16x32_bf16 v[200:203],a[104:107],v[88:91],v[200:203]\n"
"v_exp_f32 v119,v22\n"
"v_mfma_f32_16x16x32_bf16 v[232:235],a[104:107],v[92:95],v[232:235]\n"
"ds_read_b128 a[92:95],v113 offset:7168\n"
"v_cvt_pk_bf16_f32 v74,v122,v123\n"
"v_mfma_f32_16x16x32_bf16 v[136:139],a[104:107],v[80:83],v[136:139]\n"
"v_exp_f32 v120,v23\n"
"v_mfma_f32_16x16x32_bf16 v[168:171],a[104:107],v[84:87],v[168:171]\n"
"s_waitcnt vmcnt(12)\n"
"ds_add_u32 v124,a152\n"
"v_mfma_f32_16x16x32_bf16 a[132:135],a[144:147],v[84:87],a[132:135]\n"
"v_exp_f32 v122,v24\n"
"v_mfma_f32_16x16x32_bf16 v[172:175],a[108:111],v[84:87],v[172:175]\n"
"v_cvt_pk_bf16_f32 v75,v119,v120\n"
"v_or3_b32 v118,v118,v71,v72\n"
"v_mfma_f32_16x16x32_bf16 v[204:207],a[108:111],v[88:91],v[204:207]\n"
"v_exp_f32 v123,v25\n"
"v_mfma_f32_16x16x32_bf16 v[236:239],a[108:111],v[92:95],v[236:239]\n"
"v_cvt_pk_bf16_f32 v76,v122,v123\n"
"v_or3_b32 v118,v118,v73,v74\n"
"v_mfma_f32_16x16x32_bf16 v[140:143],a[108:111],v[80:83],v[140:143]\n"
"v_exp_f32 v119,v26\n"
"v_mfma_f32_16x16x32_bf16 a[128:131],a[144:147],v[80:83],a[128:131]\n"
"v_or3_b32 v118,v118,v75,v76\n"
"v_mfma_f32_16x16x32_bf16 v[144:147],a[112:115],v[80:83],v[144:147]\n"
"v_exp_f32 v120,v27\n"
"v_mfma_f32_16x16x32_bf16 v[176:179],a[112:115],v[84:87],v[176:179]\n"
"v_cvt_pk_bf16_f32 v77,v119,v120\n"
"v_mfma_f32_16x16x32_bf16 v[208:211],a[112:115],v[88:91],v[208:211]\n"
"v_exp_f32 v122,v28\n"
"v_mfma_f32_16x16x32_bf16 v[240:243],a[112:115],v[92:95],v[240:243]\n"
"v_mfma_f32_16x16x32_bf16 v[244:247],a[116:119],v[92:95],v[244:247]\n"
"v_exp_f32 v123,v29\n"
"v_mfma_f32_16x16x32_bf16 v[148:151],a[116:119],v[80:83],v[148:151]\n"
"v_cvt_pk_bf16_f32 v78,v122,v123\n"
"v_or3_b32 v118,v118,v77,v78\n"
"v_mfma_f32_16x16x32_bf16 v[180:183],a[116:119],v[84:87],v[180:183]\n"
"v_exp_f32 v119,v30\n"
"v_mfma_f32_16x16x32_bf16 v[212:215],a[116:119],v[88:91],v[212:215]\n"
"ds_read_b32 v125,v124\n"
"buffer_store_dwordx4 a[252:255],v112,s[24:27],s56 offen offset:3072 sc1\n"
"v_mfma_f32_16x16x32_bf16 v[216:219],a[120:123],v[88:91],v[216:219]\n"
"v_exp_f32 v120,v31\n"
"v_mfma_f32_16x16x32_bf16 v[248:251],a[120:123],v[92:95],v[248:251]\n"
"v_cvt_pk_bf16_f32 v79,v119,v120\n"
"v_mfma_f32_16x16x32_bf16 v[152:155],a[120:123],v[80:83],v[152:155]\n"
"v_mfma_f32_16x16x32_bf16 v[184:187],a[120:123],v[84:87],v[184:187]\n"
"v_mfma_f32_16x16x32_bf16 v[188:191],a[124:127],v[84:87],v[188:191]\n"
"s_waitcnt lgkmcnt(0)\n"
"v_subrev_u32 v125,s101,v125\n"
"v_or3_b32 v125,v118,v79,v125\n"
"v_mfma_f32_16x16x32_bf16 v[220:223],a[124:127],v[88:91],v[220:223]\n"
"v_and_b32 v125,0x40004000,v125\n"
"v_cmp_ne_u32 vcc,0,v125\n"
"v_mfma_f32_16x16x32_bf16 v[252:255],a[124:127],v[92:95],v[252:255]\n"
"v_mfma_f32_16x16x32_bf16 v[156:159],a[124:127],v[80:83],v[156:159]\n"
"s_cbranch_vccnz .Lfk_cs_f2\n"
".Lfk_cr_f2:\n"
"s_branch .Lfk_F\n"
".Lfk_cs_f3:\n"
"s_call_b64 s[94:95],.Lfk_meet3\n"
"v_or_b32 v118,v118,v95\n"
"v_and_b32 v118,0x40004000,v118\n"
"v_cmp_ne_u32 vcc,0,v118\n"
"s_nop 0\n"
"s_cbranch_vccz .Lfk_cr_f3\n"
"s_mov_b32 s91,15\n"
"s_call_b64 s[94:95],.Lfk_rare3\n"
"s_branch .Lfk_cr_f3\n"
".Lfk_cs_f0:\n"
"s_call_b64 s[94:95],.Lfk_meet3\n"
"v_or_b32 v118,v118,v79\n"
"v_and_b32 v118,0x40004000,v118\n"
"v_cmp_ne_u32 vcc,0,v118\n"
"s_nop 0\n"
"s_cbranch_vccz .Lfk_cr_f0\n"
"s_mov_b32 s91,15\n"
"s_call_b64 s[94:95],.Lfk_rare2\n"
"s_branch .Lfk_cr_f0\n"
".Lfk_cs_f1:\n"
"s_call_b64 s[94:95],.Lfk_meet3\n"
"v_or_b32 v118,v118,v95\n"
"v_and_b32 v118,0x40004000,v118\n"
"v_cmp_ne_u32 vcc,0,v118\n"
"s_nop 0\n"
"s_cbranch_vccz .Lfk_cr_f1\n"
"s_mov_b32 s91,15\n"
"s_call_b64 s[94:95],.Lfk_rare3\n"
"s_branch .Lfk_cr_f1\n"
".Lfk_cs_f2:\n"
"s_call_b64 s[94:95],.Lfk_meet3\n"
"v_or_b32 v118,v118,v79\n"
"v_and_b32 v118,0x40004000,v118\n"
"v_cmp_ne_u32 vcc,0,v118\n"
"s_nop 0\n"
"s_cbranch_vccz .Lfk_cr_f2\n"
"s_mov_b32 s91,15\n"
"s_call_b64 s[94:95],.Lfk_rare2\n"
"s_branch .Lfk_cr_f2\n"
".Lfk_cs_g3:\n"
"s_call_b64 s[94:95],.Lfk_meet3\n"
"v_or_b32 v118,v118,v95\n"
"v_and_b32 v118,0x40004000,v118\n"
"v_cmp_ne_u32 vcc,0,v118\n"
"s_nop 0\n"
"s_cbranch_vccz .Lfk_cr_g3\n"
"s_mov_b32 s91,15\n"
"s_call_b64 s[94:95],.Lfk_rare3\n"
"s_branch .Lfk_cr_g3\n"
".Lfk_cs_g0:\n"
"s_call_b64 s[94:95],.Lfk_meet3\n"
"v_or_b32 v118,v118,v79\n"
"v_and_b32 v118,0x40004000,v118\n"
"v_cmp_ne_u32 vcc,0,v118\n"
"s_nop 0\n"
"s_cbranch_vccz .Lfk_cr_g0\n"
"s_mov_b32 s91,15\n"
"s_call_b64 s[94:95],.Lfk_rare2\n"
"s_branch .Lfk_cr_g0\n"
".Lfk_cs_g1:\n"
"s_call_b64 s[94:95],.Lfk_meet3\n"
"v_or_b32 v118,v118,v95\n"
"v_and_b32 v118,0x40004000,v118\n"
"v_cmp_ne_u32 vcc,0,v118\n"
"s_nop 0\n"
"s_cbranch_vccz .Lfk_cr_g1\n"
"s_mov_b32 s91,15\n"
"s_call_b64 s[94:95],.Lfk_rare3\n"
"s_branch .Lfk_cr_g1\n"
".Lfk_cs_g2:\n"
"s_call_b64 s[94:95],.Lfk_meet3\n"
"v_or_b32 v118,v118,v79\n"
"v_and_b32 v118,0x40004000,v118\n"
"v_cmp_ne_u32 vcc,0,v118\n"
"s_nop 0\n"
"s_cbranch_vccz .Lfk_cr_g2\n"
"s_mov_b32 s91,15\n"
"s_call_b64 s[94:95],.Lfk_rare2\n"
"s_branch .Lfk_cr_g2\n"
".Lfk_meet3:\n"
"s_sub_u32 s82,s101,0\n"
"s_mov_b32 s84,0x1000000\n"
".Lfk_meetl3:\n"
"ds_read_b32 v125,v124\n"
"s_sub_u32 s84,s84,1\n"
"s_cbranch_scc1 .Lfk_meetx3\n"
"s_waitcnt lgkmcnt(0)\n"
"v_cmp_gt_u32 vcc,s82,v125\n"
"s_nop 0\n"
"s_cbranch_vccnz .Lfk_meetl3\n"
".Lfk_meetx3:\n"
"s_setpc_b64 s[94:95]\n"
".Lfk_Fx:\n"
"s_mov_b32 s82,2146435072\n"
"buffer_load_dword a148,off,s[24:27],s82\n"
"buffer_load_dword a148,off,s[24:27],s82\n"
"buffer_load_dword a148,off,s[24:27],s82\n"
"buffer_load_dword a148,off,s[24:27],s82\n"
"s_cmp_ge_u32 s52,2\n"
"s_cbranch_scc1 .Lfk_winA\n"
".Lfk_H:\n"
"v_mfma_f32_16x16x32_bf16 v[0:3],a[64:67],a[0:3],v[96:99]\n"
"s_swappc_b64 s[94:95],s[20:21]\n"
"s_add_u32 s101,s101,4\n"
"v_exp_f32 v122,v32\n"
"v_mfma_f32_16x16x32_bf16 v[0:3],a[68:71],a[4:7],v[0:3]\n"
"s_cmp_eq_u32 s64,s65\n"
"v_exp_f32 v123,v33\n"
"v_mfma_f32_16x16x32_bf16 v[0:3],a[72:75],a[8:11],v[0:3]\n"
"ds_read_b128 a[96:99],v114 offset:24576\n"
"ds_read_b128 a[100:103],v114 offset:25600\n"
"v_mfma_f32_16x16x32_bf16 v[0:3],a[76:79],a[12:15],v[0:3]\n"
"s_cselect_b32 s64,s66,s64\n"
"v_exp_f32 v119,v34\n"
"v_mfma_f32_16x16x32_bf16 v[4:7],a[92:95],a[12:15],v[96:99]\n"
"ds_read_b128 a[104:107],v114 offset:26624\n"
"v_cvt_pk_bf16_f32 v80,v122,v123\n"
"v_mfma_f32_16x16x32_bf16 v[4:7],a[88:91],a[8:11],v[4:7]\n"
"v_exp_f32 v120,v35\n"
"v_mfma_f32_16x16x32_bf16 v[4:7],a[84:87],a[4:7],v[4:7]\n"
"ds_read_b128 a[108:111],v114 offset:27648\n"
"ds_read_b128 a[112:115],v114 offset:28672\n"
"v_mfma_f32_16x16x32_bf16 v[4:7],a[80:83],a[0:3],v[4:7]\n"
"v_exp_f32 v122,v36\n"
"v_mfma_f32_16x16x32_bf16 v[12:15],a[80:83],a[16:19],v[100:103]\n"
"ds_read_b128 a[116:119],v114 offset:29696\n"
"v_cvt_pk_bf16_f32 v81,v119,v120\n"
"v_mfma_f32_16x16x32_bf16 v[12:15],a[84:87],a[20:23],v[12:15]\n"
"v_exp_f32 v123,v37\n"
"v_mfma_f32_16x16x32_bf16 v[12:15],a[88:91],a[24:27],v[12:15]\n"
"ds_read_b128 a[120:123],v114 offset:30720\n"
"ds_read_b128 a[124:127],v114 offset:31744\n"
"v_mfma_f32_16x16x32_bf16 v[12:15],a[92:95],a[28:31],v[12:15]\n"
"s_add_u32 m0,s67,0\n"
"v_exp_f32 v119,v38\n"
"v_mfma_f32_16x16x32_bf16 v[8:11],a[76:79],a[28:31],v[100:103]\n"
"v_cvt_pk_bf16_f32 v82,v122,v123\n"
"v_or3_b32 v118,v80,v81,v82\n"
"v_mfma_f32_16x16x32_bf16 v[8:11],a[72:75],a[24:27],v[8:11]\n"
"v_exp_f32 v120,v39\n"
"v_mfma_f32_16x16x32_bf16 v[8:11],a[68:71],a[20:23],v[8:11]\n"
"buffer_load_dwordx4 v112,s[24:27],s64 offen offset:0 sc1 lds\n"
"v_mfma_f32_16x16x32_bf16 v[8:11],a[64:67],a[16:19],v[8:11]\n"
"v_exp_f32 v122,v40\n"
"v_mfma_f32_16x16x32_bf16 v[16:19],a[64:67],a[32:35],v[104:107]\n"
"v_cvt_pk_bf16_f32 v83,v119,v120\n"
"v_mfma_f32_16x16x32_bf16 v[16:19],a[68:71],a[36:39],v[16:19]\n"
"v_exp_f32 v123,v41\n"
"v_mfma_f32_16x16x32_bf16 v[16:19],a[72:75],a[40:43],v[16:19]\n"
"buffer_load_dwordx4 v112,s[24:27],s64 offen offset:1024 sc1 lds\n"
"v_mfma_f32_16x16x32_bf16 v[16:19],a[76:79],a[44:47],v[16:19]\n"
"v_exp_f32 v119,v42\n"
"v_mfma_f32_16x16x32_bf16 v[20:23],a[92:95],a[44:47],v[104:107]\n"
"v_cvt_pk_bf16_f32 v84,v122,v123\n"
"v_or3_b32 v118,v118,v83,v84\n"
"v_mfma_f32_16x16x32_bf16 v[20:23],a[88:91],a[40:43],v[20:23]\n"
"v_exp_f32 v120,v43\n"
"v_mfma_f32_16x16x32_bf16 v[20:23],a[84:87],a[36:39],v[20:23]\n"
"v_cvt_pk_bf16_f32 v85,v119,v120\n"
"v_mfma_f32_16x16x32_bf16 v[20:23],a[80:83],a[32:35],v[20:23]\n"
"v_exp_f32 v122,v44\n"
"v_mfma_f32_16x16x32_bf16 v[28:31],a[80:83],a[48:51],v[108:111]\n"
"buffer_load_dwordx4 v112,s[24:27],s64 offen offset:2048 sc1 lds\n"
"v_mfma_f32_16x16x32_bf16 v[28:31],a[84:87],a[52:55],v[28:31]\n"
"v_exp_f32 v123,v45\n"
"v_mfma_f32_16x16x32_bf16 v[28:31],a[88:91],a[56:59],v[28:31]\n"
"v_cvt_pk_bf16_f32 v86,v122,v123\n"
"v_or3_b32 v118,v118,v85,v86\n"
"v_mfma_f32_16x16x32_bf16 v[28:31],a[92:95],a[60:63],v[28:31]\n"
"v_exp_f32 v119,v46\n"
"v_mfma_f32_16x16x32_bf16 v[24:27],a[76:79],a[60:63],v[108:111]\n"
"v_mfma_f32_16x16x32_bf16 v[24:27],a[72:75],a[56:59],v[24:27]\n"
"v_exp_f32 v120,v47\n"
"v_mfma_f32_16x16x32_bf16 v[24:27],a[68:71],a[52:55],v[24:27]\n"
"buffer_load_dwordx4 v112,s[24:27],s64 offen offset:3072 sc1 lds\n"
"v_mfma_f32_16x16x32_bf16 v[24:27],a[64:67],a[48:51],v[24:27]\n"
"s_waitcnt lgkmcnt(0)\n"
"v_exp_f32 v122,v48\n"
"v_mfma_f32_16x16x32_bf16 v[128:131],a[96:99],v[64:67],v[128:131]\n"
"v_exp_f32 v123,v49\n"
"v_mfma_f32_16x16x32_bf16 v[160:163],a[96:99],v[68:71],v[160:163]\n"
"ds_read_b128 a[64:67],v113 offset:16384\n"
"v_cvt_pk_bf16_f32 v87,v119,v120\n"
"v_mfma_f32_16x16x32_bf16 v[192:195],a[96:99],v[72:75],v[192:195]\n"
"s_add_u32 m0,s67,4096\n"
"v_exp_f32 v119,v50\n"
"v_mfma_f32_16x16x32_bf16 v[224:227],a[96:99],v[76:79],v[224:227]\n"
"ds_read_b128 a[68:71],v113 offset:17408\n"
"v_cvt_pk_bf16_f32 v88,v122,v123\n"
"v_mfma_f32_16x16x32_bf16 a[140:143],a[144:147],v[76:79],a[140:143]\n"
"v_exp_f32 v120,v51\n"
"v_mfma_f32_16x16x32_bf16 v[228:231],a[100:103],v[76:79],v[228:231]\n"
"ds_read_b128 a[72:75],v113 offset:18432\n"
"ds_read_b128 a[76:79],v113 offset:19456\n"
"v_mfma_f32_16x16x32_bf16 v[132:135],a[100:103],v[64:67],v[132:135]\n"
"v_exp_f32 v122,v52\n"
"v_mfma_f32_16x16x32_bf16 v[164:167],a[100:103],v[68:71],v[164:167]\n"
"ds_read_b128 a[80:83],v113 offset:20480\n"
"v_cvt_pk_bf16_f32 v89,v119,v120\n"
"v_mfma_f32_16x16x32_bf16 v[196:199],a[100:103],v[72:75],v[196:199]\n"
"v_exp_f32 v123,v53\n"
"v_mfma_f32_16x16x32_bf16 a[136:139],a[144:147],v[72:75],a[136:139]\n"
"ds_read_b128 a[84:87],v113 offset:21504\n"
"ds_read_b128 a[88:91],v113 offset:22528\n"
"v_mfma_f32_16x16x32_bf16 v[200:203],a[104:107],v[72:75],v[200:203]\n"
"v_exp_f32 v119,v54\n"
"v_mfma_f32_16x16x32_bf16 v[232:235],a[104:107],v[76:79],v[232:235]\n"
"ds_read_b128 a[92:95],v113 offset:23552\n"
"v_cvt_pk_bf16_f32 v90,v122,v123\n"
"v_mfma_f32_16x16x32_bf16 v[136:139],a[104:107],v[64:67],v[136:139]\n"
"v_exp_f32 v120,v55\n"
"v_mfma_f32_16x16x32_bf16 v[168:171],a[104:107],v[68:71],v[168:171]\n"
"s_waitcnt vmcnt(15)\n"
"ds_add_u32 v124,a152\n"
"v_mfma_f32_16x16x32_bf16 a[132:135],a[144:147],v[68:71],a[132:135]\n"
"v_exp_f32 v122,v56\n"
"v_mfma_f32_16x16x32_bf16 v[172:175],a[108:111],v[68:71],v[172:175]\n"
"v_cvt_pk_bf16_f32 v91,v119,v120\n"
"v_or3_b32 v118,v118,v87,v88\n"
"v_mfma_f32_16x16x32_bf16 v[204:207],a[108:111],v[72:75],v[204:207]\n"
"v_exp_f32 v123,v57\n"
"v_mfma_f32_16x16x32_bf16 v[236:239],a[108:111],v[76:79],v[236:239]\n"
"buffer_load_dwordx4 v117,s[24:27],s64 offen offset:0 sc1 lds\n"
"v_mfma_f32_16x16x32_bf16 v[140:143],a[108:111],v[64:67],v[140:143]\n"
"v_exp_f32 v119,v58\n"
"v_mfma_f32_16x16x32_bf16 a[128:131],a[144:147],v[64:67],a[128:131]\n"
"v_cvt_pk_bf16_f32 v92,v122,v123\n"
"v_or3_b32 v118,v118,v89,v90\n"
"v_mfma_f32_16x16x32_bf16 v[144:147],a[112:115],v[64:67],v[144:147]\n"
"v_exp_f32 v120,v59\n"
"v_mfma_f32_16x16x32_bf16 v[176:179],a[112:115],v[68:71],v[176:179]\n"
"buffer_load_dwordx4 v117,s[24:27],s64 offen offset:1024 sc1 lds\n"
"v_mfma_f32_16x16x32_bf16 v[208:211],a[112:115],v[72:75],v[208:211]\n"
"v_exp_f32 v122,v60\n"
"v_mfma_f32_16x16x32_bf16 v[240:243],a[112:115],v[76:79],v[240:243]\n"
"v_cvt_pk_bf16_f32 v93,v119,v120\n"
"v_or3_b32 v118,v118,v91,v92\n"
"v_mfma_f32_16x16x32_bf16 v[244:247],a[116:119],v[76:79],v[244:247]\n"
"v_exp_f32 v123,v61\n"
"v_mfma_f32_16x16x32_bf16 v[148:151],a[116:119],v[64:67],v[148:151]\n"
"buffer_load_dwordx4 v117,s[24:27],s64 offen offset:2048 sc1 lds\n"
"v_mfma_f32_16x16x32_bf16 v[180:183],a[116:119],v[68:71],v[180:183]\n"
"v_exp_f32 v119,v62\n"
"v_mfma_f32_16x16x32_bf16 v[212:215],a[116:119],v[72:75],v[212:215]\n"
"ds_read_b32 v125,v124\n"
"v_cvt_pk_bf16_f32 v94,v122,v123\n"
"v_mfma_f32_16x16x32_bf16 v[216:219],a[120:123],v[72:75],v[216:219]\n"
"v_exp_f32 v120,v63\n"
"v_mfma_f32_16x16x32_bf16 v[248:251],a[120:123],v[76:79],v[248:251]\n"
"buffer_load_dwordx4 v117,s[24:27],s64 offen offset:3072 sc1 lds\n"
"v_mfma_f32_16x16x32_bf16 v[152:155],a[120:123],v[64:67],v[152:155]\n"
"s_add_u32 s64,s64,16384\n"
"s_andn2_b32 m0,s55,16384\n"
"v_mfma_f32_16x16x32_bf16 v[184:187],a[120:123],v[68:71],v[184:187]\n"
"s_add_u32 s54,s54,s22\n"
"v_cvt_pk_bf16_f32 v95,v119,v120\n"
"v_mfma_f32_16x16x32_bf16 v[188:191],a[124:127],v[68:71],v[188:191]\n"
"s_waitcnt lgkmcnt(0)\n"
"v_subrev_u32 v125,s101,v125\n"
"v_or3_b32 v118,v118,v93,v94\n"
"v_mfma_f32_16x16x32_bf16 v[220:223],a[124:127],v[72:75],v[220:223]\n"
"v_or3_b32 v125,v118,v95,v125\n"
"v_and_b32 v125,0x40004000,v125\n"
"v_mfma_f32_16x16x32_bf16 v[252:255],a[124:127],v[76:79],v[252:255]\n"
"buffer_load_dwordx4 v115,s[32:35],s54 offen sc1 lds\n"
"buffer_store_dwordx4 a[240:243],v112,s[24:27],s56 offen offset:0 sc1\n"
"v_cmp_ne_u32 vcc,0,v125\n"
"v_mfma_f32_16x16x32_bf16 v[156:159],a[124:127],v[64:67],v[156:159]\n"
"s_cbranch_vccnz .Lfk_cs_g3\n"
".Lfk_cr_g3:\n"
"v_mfma_f32_16x16x32_bf16 v[32:35],a[64:67],a[0:3],v[96:99]\n"
"s_add_u32 s101,s101,4\n"
"v_exp_f32 v122,v0\n"
"v_mfma_f32_16x16x32_bf16 v[32:35],a[68:71],a[4:7],v[32:35]\n"
"s_add_u32 m0,s55,4096\n"
"v_exp_f32 v123,v1\n"
"v_mfma_f32_16x16x32_bf16 v[32:35],a[72:75],a[8:11],v[32:35]\n"
"ds_read_b128 a[96:99],v114 offset:57344\n"
"ds_read_b128 a[100:103],v114 offset:58368\n"
"v_mfma_f32_16x16x32_bf16 v[32:35],a[76:79],a[12:15],v[32:35]\n"
"s_add_u32 s54,s54,s22\n"
"v_exp_f32 v119,v2\n"
"v_mfma_f32_16x16x32_bf16 v[36:39],a[92:95],a[12:15],v[96:99]\n"
"ds_read_b128 a[104:107],v114 offset:59392\n"
"v_cvt_pk_bf16_f32 v64,v122,v123\n"
"v_mfma_f32_16x16x32_bf16 v[36:39],a[88:91],a[8:11],v[36:39]\n"
"v_exp_f32 v120,v3\n"
"v_mfma_f32_16x16x32_bf16 v[36:39],a[84:87],a[4:7],v[36:39]\n"
"ds_read_b128 a[108:111],v114 offset:60416\n"
"ds_read_b128 a[112:115],v114 offset:61440\n"
"v_mfma_f32_16x16x32_bf16 v[36:39],a[80:83],a[0:3],v[36:39]\n"
"v_exp_f32 v122,v4\n"
"v_mfma_f32_16x16x32_bf16 v[44:47],a[80:83],a[16:19],v[100:103]\n"
"ds_read_b128 a[116:119],v114 offset:62464\n"
"v_cvt_pk_bf16_f32 v65,v119,v120\n"
"v_mfma_f32_16x16x32_bf16 v[44:47],a[84:87],a[20:23],v[44:47]\n"
"v_exp_f32 v123,v5\n"
"v_mfma_f32_16x16x32_bf16 v[44:47],a[88:91],a[24:27],v[44:47]\n"
"ds_read_b128 a[120:123],v114 offset:63488\n"
"ds_read_b128 a[124:127],v114 offset:64512\n"
"v_mfma_f32_16x16x32_bf16 v[44:47],a[92:95],a[28:31],v[44:47]\n"
"v_exp_f32 v119,v6\n"
"v_mfma_f32_16x16x32_bf16 v[40:43],a[76:79],a[28:31],v[100:103]\n"
"v_cvt_pk_bf16_f32 v66,v122,v123\n"
"v_or3_b32 v118,v64,v65,v66\n"
"v_mfma_f32_16x16x32_bf16 v[40:43],a[72:75],a[24:27],v[40:43]\n"
"v_exp_f32 v120,v7\n"
"v_mfma_f32_16x16x32_bf16 v[40:43],a[68:71],a[20:23],v[40:43]\n"
"buffer_load_dwordx4 v115,s[32:35],s54 offen sc1 lds\n"
"v_mfma_f32_16x16x32_bf16 v[40:43],a[64:67],a[16:19],v[40:43]\n"
"s_add_u32 m0,s67,16384\n"
"v_exp_f32 v122,v8\n"
"v_mfma_f32_16x16x32_bf16 v[48:51],a[64:67],a[32:35],v[104:107]\n"
"v_cvt_pk_bf16_f32 v67,v119,v120\n"
"v_mfma_f32_16x16x32_bf16 v[48:51],a[68:71],a[36:39],v[48:51]\n"
"v_exp_f32 v123,v9\n"
"v_mfma_f32_16x16x32_bf16 v[48:51],a[72:75],a[40:43],v[48:51]\n"
"buffer_load_dwordx4 v112,s[24:27],s64 offen offset:0 sc1 lds\n"
"v_mfma_f32_16x16x32_bf16 v[48:51],a[76:79],a[44:47],v[48:51]\n"
"v_exp_f32 v119,v10\n"
"v_mfma_f32_16x16x32_bf16 v[52:55],a[92:95],a[44:47],v[104:107]\n"
"v_cvt_pk_bf16_f32 v68,v122,v123\n"
"v_or3_b32 v118,v118,v67,v68\n"
"v_mfma_f32_16x16x32_bf16 v[52:55],a[88:91],a[40:43],v[52:55]\n"
"v_exp_f32 v120,v11\n"
"v_mfma_f32_16x16x32_bf16 v[52:55],a[84:87],a[36:39],v[52:55]\n"
"buffer_load_dwordx4 v112,s[24:27],s64 offen offset:1024 sc1 lds\n"
"v_mfma_f32_16x16x32_bf16 v[52:55],a[80:83],a[32:35],v[52:55]\n"
"v_exp_f32 v122,v12\n"
"v_mfma_f32_16x16x32_bf16 v[60:63],a[80:83],a[48:51],v[108:111]\n"
"v_cvt_pk_bf16_f32 v69,v119,v120\n"
"v_mfma_f32_16x16x32_bf16 v[60:63],a[84:87],a[52:55],v[60:63]\n"
"v_exp_f32 v123,v13\n"
"v_mfma_f32_16x16x32_bf16 v[60:63],a[88:91],a[56:59],v[60:63]\n"
"buffer_load_dwordx4 v112,s[24:27],s64 offen offset:2048 sc1 lds\n"
"v_mfma_f32_16x16x32_bf16 v[60:63],a[92:95],a[60:63],v[60:63]\n"
"v_exp_f32 v119,v14\n"
"v_mfma_f32_16x16x32_bf16 v[56:59],a[76:79],a[60:63],v[108:111]\n"
"v_cvt_pk_bf16_f32 v70,v122,v123\n"
"v_or3_b32 v118,v118,v69,v70\n"
"v_mfma_f32_16x16x32_bf16 v[56:59],a[72:75],a[56:59],v[56:59]\n"
"v_exp_f32 v120,v15\n"
"v_mfma_f32_16x16x32_bf16 v[56:59],a[68:71],a[52:55],v[56:59]\n"
"buffer_load_dwordx4 v112,s[24:27],s64 offen offset:3072 sc1 lds\n"
"v_mfma_f32_16x16x32_bf16 v[56:59],a[64:67],a[48:51],v[56:59]\n"
"s_waitcnt lgkmcnt(0)\n"
"v_exp_f32 v122,v16\n"
"v_mfma_f32_16x16x32_bf16 v[128:131],a[96:99],v[80:83],v[128:131]\n"
"v_exp_f32 v123,v17\n"
"v_mfma_f32_16x16x32_bf16 v[160:163],a[96:99],v[84:87],v[160:163]\n"
"ds_read_b128 a[64:67],v113 offset:49152\n"
"v_cvt_pk_bf16_f32 v71,v119,v120\n"
"v_mfma_f32_16x16x32_bf16 v[192:195],a[96:99],v[88:91],v[192:195]\n"
"s_add_u32 m0,s67,20480\n"
"v_exp_f32 v119,v18\n"
"v_mfma_f32_16x16x32_bf16 v[224:227],a[96:99],v[92:95],v[224:227]\n"
"ds_read_b128 a[68:71],v113 offset:50176\n"
"v_cvt_pk_bf16_f32 v72,v122,v123\n"
"v_mfma_f32_16x16x32_bf16 a[140:143],a[144:147],v[92:95],a[140:143]\n"
"v_exp_f32 v120,v19\n"
"v_mfma_f32_16x16x32_bf16 v[228:231],a[100:103],v[92:95],v[228:231]\n"
"ds_read_b128 a[72:75],v113 offset:51200\n"
"ds_read_b128 a[76:79],v113 offset:52224\n"
"v_mfma_f32_16x16x32_bf16 v[132:135],a[100:103],v[80:83],v[132:135]\n"
"v_exp_f32 v122,v20\n"
"v_mfma_f32_16x16x32_bf16 v[164:167],a[100:103],v[84:87],v[164:167]\n"
"ds_read_b128 a[80:83],v113 offset:53248\n"
"v_cvt_pk_bf16_f32 v73,v119,v120\n"
"v_mfma_f32_16x16x32_bf16 v[196:199],a[100:103],v[88:91],v[196:199]\n"
"v_exp_f32 v123,v21\n"
"v_mfma_f32_16x16x32_bf16 a[136:139],a[144:147],v[88:91],a[136:139]\n"
"ds_read_b128 a[84:87],v113 offset:54272\n"
"ds_read_b128 a[88:91],v113 offset:55296\n"
"v_mfma_f32_16x16x32_bf16 v[200:203],a[104:107],v[88:91],v[200:203]\n"
"v_exp_f32 v119,v22\n"
"v_mfma_f32_16x16x32_bf16 v[232:235],a[104:107],v[92:95],v[232:235]\n"
"ds_read_b128 a[92:95],v113 offset:56320\n"
"v_cvt_pk_bf16_f32 v74,v122,v123\n"
"v_mfma_f32_16x16x32_bf16 v[136:139],a[104:107],v[80:83],v[136:139]\n"
"v_exp_f32 v120,v23\n"
"v_mfma_f32_16x16x32_bf16 v[168:171],a[104:107],v[84:87],v[168:171]\n"
"s_waitcnt vmcnt(16)\n"
"ds_add_u32 v124,a152\n"
"v_mfma_f32_16x16x32_bf16 a[132:135],a[144:147],v[84:87],a[132:135]\n"
"v_exp_f32 v122,v24\n"
"v_mfma_f32_16x16x32_bf16 v[172:175],a[108:111],v[84:87],v[172:175]\n"
"v_cvt_pk_bf16_f32 v75,v119,v120\n"
"v_or3_b32 v118,v118,v71,v72\n"
"v_mfma_f32_16x16x32_bf16 v[204:207],a[108:111],v[88:91],v[204:207]\n"
"v_exp_f32 v123,v25\n"
"v_mfma_f32_16x16x32_bf16 v[236:239],a[108:111],v[92:95],v[236:239]\n"
"buffer_load_dwordx4 v117,s[24:27],s64 offen offset:0 sc1 lds\n"
"v_mfma_f32_16x16x32_bf16 v[140:143],a[108:111],v[80:83],v[140:143]\n"
"v_exp_f32 v119,v26\n"
"v_mfma_f32_16x16x32_bf16 a[128:131],a[144:147],v[80:83],a[128:131]\n"
"v_cvt_pk_bf16_f32 v76,v122,v123\n"
"v_or3_b32 v118,v118,v73,v74\n"
"v_mfma_f32_16x16x32_bf16 v[144:147],a[112:115],v[80:83],v[144:147]\n"
"v_exp_f32 v120,v27\n"
"v_mfma_f32_16x16x32_bf16 v[176:179],a[112:115],v[84:87],v[176:179]\n"
"buffer_load_dwordx4 v117,s[24:27],s64 offen offset:1024 sc1 lds\n"
"v_mfma_f32_16x16x32_bf16 v[208:211],a[112:115],v[88:91],v[208:211]\n"
"v_exp_f32 v122,v28\n"
"v_mfma_f32_16x16x32_bf16 v[240:243],a[112:115],v[92:95],v[240:243]\n"
"v_cvt_pk_bf16_f32 v77,v119,v120\n"
"v_or3_b32 v118,v118,v75,v76\n"
"v_mfma_f32_16x16x32_bf16 v[244:247],a[116:119],v[92:95],v[244:247]\n"
"v_exp_f32 v123,v29\n"
"v_mfma_f32_16x16x32_bf16 v[148:151],a[116:119],v[80:83],v[148:151]\n"
"buffer_load_dwordx4 v117,s[24:27],s64 offen offset:2048 sc1 lds\n"
"v_mfma_f32_16x16x32_bf16 v[180:183],a[116:119],v[84:87],v[180:183]\n"
"v_exp_f32 v119,v30\n"
"v_mfma_f32_16x16x32_bf16 v[212:215],a[116:119],v[88:91],v[212:215]\n"
"ds_read_b32 v125,v124\n"
"v_cvt_pk_bf16_f32 v78,v122,v123\n"
"v_mfma_f32_16x16x32_bf16 v[216:219],a[120:123],v[88:91],v[216:219]\n"
"v_exp_f32 v120,v31\n"
"v_mfma_f32_16x16x32_bf16 v[248:251],a[120:123],v[92:95],v[248:251]\n"
"buffer_load_dwordx4 v117,s[24:27],s64 offen offset:3072 sc1 lds\n"
"v_mfma_f32_16x16x32_bf16 v[152:155],a[120:123],v[80:83],v[152:155]\n"
"buffer_store_dwordx4 a[244:247],v112,s[24:27],s56 offen offset:1024 sc1\n"
"v_mfma_f32_16x16x32_bf16 v[184:187],a[120:123],v[84:87],v[184:187]\n"
"s_add_u32 s64,s64,16384\n"
"v_cvt_pk_bf16_f32 v79,v119,v120\n"
"v_or3_b32 v118,v118,v77,v78\n"
"v_mfma_f32_16x16x32_bf16 v[188:191],a[124:127],v[84:87],v[188:191]\n"
"s_waitcnt lgkmcnt(0)\n"
"v_subrev_u32 v125,s101,v125\n"
"v_or3_b32 v125,v118,v79,v125\n"
"v_mfma_f32_16x16x32_bf16 v[220:223],a[124:127],v[88:91],v[220:223]\n"
"v_and_b32 v125,0x40004000,v125\n"
"v_cmp_ne_u32 vcc,0,v125\n"
"v_mfma_f32_16x16x32_bf16 v[252:255],a[124:127],v[92:95],v[252:255]\n"
"v_mfma_f32_16x16x32_bf16 v[156:159],a[124:127],v[80:83],v[156:159]\n"
"s_cbranch_vccnz .Lfk_cs_g0\n"
".Lfk_cr_g0:\n"
"v_mfma_f32_16x16x32_bf16 v[0:3],a[64:67],a[0:3],v[96:99]\n"
"s_add_u32 s101,s101,4\n"
"v_exp_f32 v122,v32\n"
"v_mfma_f32_16x16x32_bf16 v[0:3],a[68:71],a[4:7],v[0:3]\n"
"s_add_u32 m0,s55,8192\n"
"v_exp_f32 v123,v33\n"
"v_mfma_f32_16x16x32_bf16 v[0:3],a[72:75],a[8:11],v[0:3]\n"
"ds_read_b128 a[96:99],v114 offset:40960\n"
"ds_read_b128 a[100:103],v114 offset:41984\n"
"v_mfma_f32_16x16x32_bf16 v[0:3],a[76:79],a[12:15],v[0:3]\n"
"s_add_u32 s54,s54,s22\n"
"v_exp_f32 v119,v34\n"
"v_mfma_f32_16x16x32_bf16 v[4:7],a[92:95],a[12:15],v[96:99]\n"
"ds_read_b128 a[104:107],v114 offset:43008\n"
"v_cvt_pk_bf16_f32 v80,v122,v123\n"
"v_mfma_f32_16x16x32_bf16 v[4:7],a[88:91],a[8:11],v[4:7]\n"
"v_exp_f32 v120,v35\n"
"v_mfma_f32_16x16x32_bf16 v[4:7],a[84:87],a[4:7],v[4:7]\n"
"ds_read_b128 a[108:111],v114 offset:44032\n"
"ds_read_b128 a[112:115],v114 offset:45056\n"
"v_mfma_f32_16x16x32_bf16 v[4:7],a[80:83],a[0:3],v[4:7]\n"
"v_exp_f32 v122,v36\n"
"v_mfma_f32_16x16x32_bf16 v[12:15],a[80:83],a[16:19],v[100:103]\n"
"ds_read_b128 a[116:119],v114 offset:46080\n"
"v_cvt_pk_bf16_f32 v81,v119,v120\n"
"v_mfma_f32_16x16x32_bf16 v[12:15],a[84:87],a[20:23],v[12:15]\n"
"v_exp_f32 v123,v37\n"
"v_mfma_f32_16x16x32_bf16 v[12:15],a[88:91],a[24:27],v[12:15]\n"
"ds_read_b128 a[120:123],v114 offset:47104\n"
"ds_read_b128 a[124:127],v114 offset:48128\n"
"v_mfma_f32_16x16x32_bf16 v[12:15],a[92:95],a[28:31],v[12:15]\n"
"v_exp_f32 v119,v38\n"
"v_mfma_f32_16x16x32_bf16 v[8:11],a[76:79],a[28:31],v[100:103]\n"
"v_cvt_pk_bf16_f32 v82,v122,v123\n"
"v_or3_b32 v118,v80,v81,v82\n"
"v_mfma_f32_16x16x32_bf16 v[8:11],a[72:75],a[24:27],v[8:11]\n"
"v_exp_f32 v120,v39\n"
"v_mfma_f32_16x16x32_bf16 v[8:11],a[68:71],a[20:23],v[8:11]\n"
"buffer_load_dwordx4 v115,s[32:35],s54 offen sc1 lds\n"
"v_mfma_f32_16x16x32_bf16 v[8:11],a[64:67],a[16:19],v[8:11]\n"
"s_add_u32 m0,s67,49152\n"
"v_exp_f32 v122,v40\n"
"v_mfma_f32_16x16x32_bf16 v[16:19],a[64:67],a[32:35],v[104:107]\n"
"v_cvt_pk_bf16_f32 v83,v119,v120\n"
"v_mfma_f32_16x16x32_bf16 v[16:19],a[68:71],a[36:39],v[16:19]\n"
"v_exp_f32 v123,v41\n"
"v_mfma_f32_16x16x32_bf16 v[16:19],a[72:75],a[40:43],v[16:19]\n"
"buffer_load_dwordx4 v112,s[24:27],s64 offen offset:0 sc1 lds\n"
"v_mfma_f32_16x16x32_bf16 v[16:19],a[76:79],a[44:47],v[16:19]\n"
"v_exp_f32 v119,v42\n"
"v_mfma_f32_16x16x32_bf16 v[20:23],a[92:95],a[44:47],v[104:107]\n"
"v_cvt_pk_bf16_f32 v84,v122,v123\n"
"v_or3_b32 v118,v118,v83,v84\n"
"v_mfma_f32_16x16x32_bf16 v[20:23],a[88:91],a[40:43],v[20:23]\n"
"v_exp_f32 v120,v43\n"
"v_mfma_f32_16x16x32_bf16 v[20:23],a[84:87],a[36:39],v[20:23]\n"
"buffer_load_dwordx4 v112,s[24:27],s64 offen offset:1024 sc1 lds\n"
"v_mfma_f32_16x16x32_bf16 v[20:23],a[80:83],a[32:35],v[20:23]\n"
"v_exp_f32 v122,v44\n"
"v_mfma_f32_16x16x32_bf16 v[28:31],a[80:83],a[48:51],v[108:111]\n"
"v_cvt_pk_bf16_f32 v85,v119,v120\n"
"v_mfma_f32_16x16x32_bf16 v[28:31],a[84:87],a[52:55],v[28:31]\n"
"v_exp_f32 v123,v45\n"
"v_mfma_f32_16x16x32_bf16 v[28:31],a[88:91],a[56:59],v[28:31]\n"
"buffer_load_dwordx4 v112,s[24:27],s64 offen offset:2048 sc1 lds\n"
"v_mfma_f32_16x16x32_bf16 v[28:31],a[92:95],a[60:63],v[28:31]\n"
"v_exp_f32 v119,v46\n"
"v_mfma_f32_16x16x32_bf16 v[24:27],a[76:79],a[60:63],v[108:111]\n"
"v_cvt_pk_bf16_f32 v86,v122,v123\n"
"v_or3_b32 v118,v118,v85,v86\n"
"v_mfma_f32_16x16x32_bf16 v[24:27],a[72:75],a[56:59],v[24:27]\n"
"v_exp_f32 v120,v47\n"
"v_mfma_f32_16x16x32_bf16 v[24:27],a[68:71],a[52:55],v[24:27]\n"
"buffer_load_dwordx4 v112,s[24:27],s64 offen offset:3072 sc1 lds\n"
"v_mfma_f32_16x16x32_bf16 v[24:27],a[64:67],a[48:51],v[24:27]\n"
"s_waitcnt lgkmcnt(0)\n"
"v_exp_f32 v122,v48\n"
"v_mfma_f32_16x16x32_bf16 v[128:131],a[96:99],v[64:67],v[128:131]\n"
"v_exp_f32 v123,v49\n"
"v_mfma_f32_16x16x32_bf16 v[160:163],a[96:99],v[68:71],v[160:163]\n"
"ds_read_b128 a[64:67],v113 offset:32768\n"
"v_cvt_pk_bf16_f32 v87,v119,v120\n"
"v_mfma_f32_16x16x32_bf16 v[192:195],a[96:99],v[72:75],v[192:195]\n"
"s_add_u32 m0,s67,53248\n"
"v_exp_f32 v119,v50\n"
"v_mfma_f32_16x16x32_bf16 v[224:227],a[96:99],v[76:79],v[224:227]\n"
"ds_read_b128 a[68:71],v113 offset:33792\n"
"v_cvt_pk_bf16_f32 v88,v122,v123\n"
"v_mfma_f32_16x16x32_bf16 a[140:143],a[144:147],v[76:79],a[140:143]\n"
"v_exp_f32 v120,v51\n"
"v_mfma_f32_16x16x32_bf16 v[228:231],a[100:103],v[76:79],v[228:231]\n"
"ds_read_b128 a[72:75],v113 offset:34816\n"
"ds_read_b128 a[76:79],v113 offset:35840\n"
"v_mfma_f32_16x16x32_bf16 v[132:135],a[100:103],v[64:67],v[132:135]\n"
"v_exp_f32 v122,v52\n"
"v_mfma_f32_16x16x32_bf16 v[164:167],a[100:103],v[68:71],v[164:167]\n"
"ds_read_b128 a[80:83],v113 offset:36864\n"
"v_cvt_pk_bf16_f32 v89,v119,v120\n"
"v_mfma_f32_16x16x32_bf16 v[196:199],a[100:103],v[72:75],v[196:199]\n"
"v_exp_f32 v123,v53\n"
"v_mfma_f32_16x16x32_bf16 a[136:139],a[144:147],v[72:75],a[136:139]\n"
"ds_read_b128 a[84:87],v113 offset:37888\n"
"ds_read_b128 a[88:91],v113 offset:38912\n"
"v_mfma_f32_16x16x32_bf16 v[200:203],a[104:107],v[72:75],v[200:203]\n"
"v_exp_f32 v119,v54\n"
"v_mfma_f32_16x16x32_bf16 v[232:235],a[104:107],v[76:79],v[232:235]\n"
"ds_read_b128 a[92:95],v113 offset:39936\n"
"v_cvt_pk_bf16_f32 v90,v122,v123\n"
"v_mfma_f32_16x16x32_bf16 v[136:139],a[104:107],v[64:67],v[136:139]\n"
"v_exp_f32 v120,v55\n"
"v_mfma_f32_16x16x32_bf16 v[168:171],a[104:107],v[68:71],v[168:171]\n"
"s_waitcnt vmcnt(16)\n"
"ds_add_u32 v124,a152\n"
"v_mfma_f32_16x16x32_bf16 a[132:135],a[144:147],v[68:71],a[132:135]\n"
"v_exp_f32 v122,v56\n"
"v_mfma_f32_16x16x32_bf16 v[172:175],a[108:111],v[68:71],v[172:175]\n"
"v_cvt_pk_bf16_f32 v91,v119,v120\n"
"v_or3_b32 v118,v118,v87,v88\n"
"v_mfma_f32_16x16x32_bf16 v[204:207],a[108:111],v[72:75],v[204:207]\n"
"v_exp_f32 v123,v57\n"
"v_mfma_f32_16x16x32_bf16 v[236:239],a[108:111],v[76:79],v[236:239]\n"
"buffer_load_dwordx4 v117,s[24:27],s64 offen offset:0 sc1 lds\n"
"v_mfma_f32_16x16x32_bf16 v[140:143],a[108:111],v[64:67],v[140:143]\n"
"v_exp_f32 v119,v58\n"
"v_mfma_f32_16x16x32_bf16 a[128:131],a[144:147],v[64:67],a[128:131]\n"
"v_cvt_pk_bf16_f32 v92,v122,v123\n"
"v_or3_b32 v118,v118,v89,v90\n"
"v_mfma_f32_16x16x32_bf16 v[144:147],a[112:115],v[64:67],v[144:147]\n"
"v_exp_f32 v120,v59\n"
"v_mfma_f32_16x16x32_bf16 v[176:179],a[112:115],v[68:71],v[176:179]\n"
"buffer_load_dwordx4 v117,s[24:27],s64 offen offset:1024 sc1 lds\n"
"v_mfma_f32_16x16x32_bf16 v[208:211],a[112:115],v[72:75],v[208:211]\n"
"v_exp_f32 v122,v60\n"
"v_mfma_f32_16x16x32_bf16 v[240:243],a[112:115],v[76:79],v[240:243]\n"
"v_cvt_pk_bf16_f32 v93,v119,v120\n"
"v_or3_b32 v118,v118,v91,v92\n"
"v_mfma_f32_16x16x32_bf16 v[244:247],a[116:119],v[76:79],v[244:247]\n"
"v_exp_f32 v123,v61\n"
"v_mfma_f32_16x16x32_bf16 v[148:151],a[116:119],v[64:67],v[148:151]\n"
"buffer_load_dwordx4 v117,s[24:27],s64 offen offset:2048 sc1 lds\n"
"v_mfma_f32_16x16x32_bf16 v[180:183],a[116:119],v[68:71],v[180:183]\n"
"v_exp_f32 v119,v62\n"
"v_mfma_f32_16x16x32_bf16 v[212:215],a[116:119],v[72:75],v[212:215]\n"
"ds_read_b32 v125,v124\n"
"v_cvt_pk_bf16_f32 v94,v122,v123\n"
"v_mfma_f32_16x16x32_bf16 v[216:219],a[120:123],v[72:75],v[216:219]\n"
"v_exp_f32 v120,v63\n"
"v_mfma_f32_16x16x32_bf16 v[248:251],a[120:123],v[76:79],v[248:251]\n"
"buffer_load_dwordx4 v117,s[24:27],s64 offen offset:3072 sc1 lds\n"
"v_mfma_f32_16x16x32_bf16 v[152:155],a[120:123],v[64:67],v[152:155]\n"
"buffer_store_dwordx4 a[248:251],v112,s[24:27],s56 offen offset:2048 sc1\n"
"v_mfma_f32_16x16x32_bf16 v[184:187],a[120:123],v[68:71],v[184:187]\n"
"s_add_u32 s64,s64,16384\n"
"v_cvt_pk_bf16_f32 v95,v119,v120\n"
"v_or3_b32 v118,v118,v93,v94\n"
"v_mfma_f32_16x16x32_bf16 v[188:191],a[124:127],v[68:71],v[188:191]\n"
"s_waitcnt lgkmcnt(0)\n"
"v_subrev_u32 v125,s101,v125\n"
"v_or3_b32 v125,v118,v95,v125\n"
"v_mfma_f32_16x16x32_bf16 v[220:223],a[124:127],v[72:75],v[220:223]\n"
"v_and_b32 v125,0x40004000,v125\n"
"v_cmp_ne_u32 vcc,0,v125\n"
"v_mfma_f32_16x16x32_bf16 v[252:255],a[124:127],v[76:79],v[252:255]\n"
"v_mfma_f32_16x16x32_bf16 v[156:159],a[124:127],v[64:67],v[156:159]\n"
"s_cbranch_vccnz .Lfk_cs_g1\n"
".Lfk_cr_g1:\n"
"v_mfma_f32_16x16x32_bf16 v[32:35],a[64:67],a[0:3],v[96:99]\n"
"s_add_u32 s101,s101,4\n"
"v_exp_f32 v122,v0\n"
"v_mfma_f32_16x16x32_bf16 v[32:35],a[68:71],a[4:7],v[32:35]\n"
"s_add_u32 m0,s55,12288\n"
"v_exp_f32 v123,v1\n"
"v_mfma_f32_16x16x32_bf16 v[32:35],a[72:75],a[8:11],v[32:35]\n"
"ds_read_b128 a[96:99],v114 offset:8192\n"
"ds_read_b128 a[100:103],v114 offset:9216\n"
"v_mfma_f32_16x16x32_bf16 v[32:35],a[76:79],a[12:15],v[32:35]\n"
"s_add_u32 s54,s54,s22\n"
"v_exp_f32 v119,v2\n"
"v_mfma_f32_16x16x32_bf16 v[36:39],a[92:95],a[12:15],v[96:99]\n"
"ds_read_b128 a[104:107],v114 offset:10240\n"
"v_cvt_pk_bf16_f32 v64,v122,v123\n"
"v_mfma_f32_16x16x32_bf16 v[36:39],a[88:91],a[8:11],v[36:39]\n"
"v_exp_f32 v120,v3\n"
"v_mfma_f32_16x16x32_bf16 v[36:39],a[84:87],a[4:7],v[36:39]\n"
"ds_read_b128 a[108:111],v114 offset:11264\n"
"ds_read_b128 a[112:115],v114 offset:12288\n"
"v_mfma_f32_16x16x32_bf16 v[36:39],a[80:83],a[0:3],v[36:39]\n"
"v_exp_f32 v122,v4\n"
"v_mfma_f32_16x16x32_bf16 v[44:47],a[80:83],a[16:19],v[100:103]\n"
"ds_read_b128 a[116:119],v114 offset:13312\n"
"v_cvt_pk_bf16_f32 v65,v119,v120\n"
"v_mfma_f32_16x16x32_bf16 v[44:47],a[84:87],a[20:23],v[44:47]\n"
"v_exp_f32 v123,v5\n"
"v_mfma_f32_16x16x32_bf16 v[44:47],a[88:91],a[24:27],v[44:47]\n"
"ds_read_b128 a[120:123],v114 offset:14336\n"
"ds_read_b128 a[124:127],v114 offset:15360\n"
"v_mfma_f32_16x16x32_bf16 v[44:47],a[92:95],a[28:31],v[44:47]\n"
"v_exp_f32 v119,v6\n"
"v_mfma_f32_16x16x32_bf16 v[40:43],a[76:79],a[28:31],v[100:103]\n"
"v_cvt_pk_bf16_f32 v66,v122,v123\n"
"v_or3_b32 v118,v64,v65,v66\n"
"v_mfma_f32_16x16x32_bf16 v[40:43],a[72:75],a[24:27],v[40:43]\n"
"v_exp_f32 v120,v7\n"
"v_mfma_f32_16x16x32_bf16 v[40:43],a[68:71],a[20:23],v[40:43]\n"
"buffer_load_dwordx4 v115,s[32:35],s54 offen sc1 lds\n"
"v_mfma_f32_16x16x32_bf16 v[40:43],a[64:67],a[16:19],v[40:43]\n"
"s_cmp_eq_u32 s64,s65\n"
"v_exp_f32 v122,v8\n"
"v_mfma_f32_16x16x32_bf16 v[48:51],a[64:67],a[32:35],v[104:107]\n"
"v_cvt_pk_bf16_f32 v67,v119,v120\n"
"s_cselect_b32 s64,s66,s64\n"
"v_mfma_f32_16x16x32_bf16 v[48:51],a[68:71],a[36:39],v[48:51]\n"
"s_add_u32 m0,s67,32768\n"
"v_exp_f32 v123,v9\n"
"v_mfma_f32_16x16x32_bf16 v[48:51],a[72:75],a[40:43],v[48:51]\n"
"buffer_load_dwordx4 v112,s[24:27],s64 offen offset:0 sc1 lds\n"
"v_mfma_f32_16x16x32_bf16 v[48:51],a[76:79],a[44:47],v[48:51]\n"
"v_exp_f32 v119,v10\n"
"v_mfma_f32_16x16x32_bf16 v[52:55],a[92:95],a[44:47],v[104:107]\n"
"v_cvt_pk_bf16_f32 v68,v122,v123\n"
"v_or3_b32 v118,v118,v67,v68\n"
"v_mfma_f32_16x16x32_bf16 v[52:55],a[88:91],a[40:43],v[52:55]\n"
"v_exp_f32 v120,v11\n"
"v_mfma_f32_16x16x32_bf16 v[52:55],a[84:87],a[36:39],v[52:55]\n"
"buffer_load_dwordx4 v112,s[24:27],s64 offen offset:1024 sc1 lds\n"
"v_mfma_f32_16x16x32_bf16 v[52:55],a[80:83],a[32:35],v[52:55]\n"
"v_exp_f32 v122,v12\n"
"v_mfma_f32_16x16x32_bf16 v[60:63],a[80:83],a[48:51],v[108:111]\n"
"v_cvt_pk_bf16_f32 v69,v119,v120\n"
"v_mfma_f32_16x16x32_bf16 v[60:63],a[84:87],a[52:55],v[60:63]\n"
"v_exp_f32 v123,v13\n"
"v_mfma_f32_16x16x32_bf16 v[60:63],a[88:91],a[56:59],v[60:63]\n"
"buffer_load_dwordx4 v112,s[24:27],s64 offen offset:2048 sc1 lds\n"
"v_mfma_f32_16x16x32_bf16 v[60:63],a[92:95],a[60:63],v[60:63]\n"
"v_exp_f32 v119,v14\n"
"v_mfma_f32_16x16x32_bf16 v[56:59],a[76:79],a[60:63],v[108:111]\n"
"v_cvt_pk_bf16_f32 v70,v122,v123\n"
"v_or3_b32 v118,v118,v69,v70\n"
"v_mfma_f32_16x16x32_bf16 v[56:59],a[72:75],a[56:59],v[56:59]\n"
"v_exp_f32 v120,v15\n"
"v_mfma_f32_16x16x32_bf16 v[56:59],a[68:71],a[52:55],v[56:59]\n"
"buffer_load_dwordx4 v112,s[24:27],s64 offen offset:3072 sc1 lds\n"
"v_mfma_f32_16x16x32_bf16 v[56:59],a[64:67],a[48:51],v[56:59]\n"
"s_waitcnt lgkmcnt(0)\n"
"v_exp_f32 v122,v16\n"
"v_mfma_f32_16x16x32_bf16 v[128:131],a[96:99],v[80:83],v[128:131]\n"
"v_exp_f32 v123,v17\n"
"v_mfma_f32_16x16x32_bf16 v[160:163],a[96:99],v[84:87],v[160:163]\n"
"ds_read_b128 a[64:67],v113 offset:0\n"
"v_cvt_pk_bf16_f32 v71,v119,v120\n"
"v_mfma_f32_16x16x32_bf16 v[192:195],a[96:99],v[88:91],v[192:195]\n"
"s_add_u32 m0,s67,36864\n"
"v_exp_f32 v119,v18\n"
"v_mfma_f32_16x16x32_bf16 v[224:227],a[96:99],v[92:95],v[224:227]\n"
"ds_read_b128 a[68:71],v113 offset:1024\n"
"v_cvt_pk_bf16_f32 v72,v122,v123\n"
"v_mfma_f32_16x16x32_bf16 a[140:143],a[144:147],v[92:95],a[140:143]\n"
"v_exp_f32 v120,v19\n"
"v_mfma_f32_16x16x32_bf16 v[228:231],a[100:103],v[92:95],v[228:231]\n"
"ds_read_b128 a[72:75],v113 offset:2048\n"
"ds_read_b128 a[76:79],v113 offset:3072\n"
"v_mfma_f32_16x16x32_bf16 v[132:135],a[100:103],v[80:83],v[132:135]\n"
"v_exp_f32 v122,v20\n"
"v_mfma_f32_16x16x32_bf16 v[164:167],a[100:103],v[84:87],v[164:167]\n"
"ds_read_b128 a[80:83],v113 offset:4096\n"
"v_cvt_pk_bf16_f32 v73,v119,v120\n"
"v_mfma_f32_16x16x32_bf16 v[196:199],a[100:103],v[88:91],v[196:199]\n"
"v_exp_f32 v123,v21\n"
"v_mfma_f32_16x16x32_bf16 a[136:139],a[144:147],v[88:91],a[136:139]\n"
"ds_read_b128 a[84:87],v113 offset:5120\n"
"ds_read_b128 a[88:91],v113 offset:6144\n"
"v_mfma_f32_16x16x32_bf16 v[200:203],a[104:107],v[88:91],v[200:203]\n"
"v_exp_f32 v119,v22\n"
"v_mfma_f32_16x16x32_bf16 v[232:235],a[104:107],v[92:95],v[232:235]\n"
"ds_read_b128 a[92:95],v113 offset:7168\n"
"v_cvt_pk_bf16_f32 v74,v122,v123\n"
"v_mfma_f32_16x16x32_bf16 v[136:139],a[104:107],v[80:83],v[136:139]\n"
"v_exp_f32 v120,v23\n"
"v_mfma_f32_16x16x32_bf16 v[168:171],a[104:107],v[84:87],v[168:171]\n"
"s_waitcnt vmcnt(16)\n"
"ds_add_u32 v124,a152\n"
"v_mfma_f32_16x16x32_bf16 a[132:135],a[144:147],v[84:87],a[132:135]\n"
"v_exp_f32 v122,v24\n"
"v_mfma_f32_16x16x32_bf16 v[172:175],a[108:111],v[84:87],v[172:175]\n"
"v_cvt_pk_bf16_f32 v75,v119,v120\n"
"v_or3_b32 v118,v118,v71,v72\n"
"v_mfma_f32_16x16x32_bf16 v[204:207],a[108:111],v[88:91],v[204:207]\n"
"v_exp_f32 v123,v25\n"
"v_mfma_f32_16x16x32_bf16 v[236:239],a[108:111],v[92:95],v[236:239]\n"
"buffer_load_dwordx4 v117,s[24:27],s64 offen offset:0 sc1 lds\n"
"v_mfma_f32_16x16x32_bf16 v[140:143],a[108:111],v[80:83],v[140:143]\n"
"v_exp_f32 v119,v26\n"
"v_mfma_f32_16x16x32_bf16 a[128:131],a[144:147],v[80:83],a[128:131]\n"
"v_cvt_pk_bf16_f32 v76,v122,v123\n"
"v_or3_b32 v118,v118,v73,v74\n"
"v_mfma_f32_16x16x32_bf16 v[144:147],a[112:115],v[80:83],v[144:147]\n"
"v_exp_f32 v120,v27\n"
"v_mfma_f32_16x16x32_bf16 v[176:179],a[112:115],v[84:87],v[176:179]\n"
"buffer_load_dwordx4 v117,s[24:27],s64 offen offset:1024 sc1 lds\n"
"v_mfma_f32_16x16x32_bf16 v[208:211],a[112:115],v[88:91],v[208:211]\n"
"v_exp_f32 v122,v28\n"
"v_mfma_f32_16x16x32_bf16 v[240:243],a[112:115],v[92:95],v[240:243]\n"
"v_cvt_pk_bf16_f32 v77,v119,v120\n"
"v_or3_b32 v118,v118,v75,v76\n"
"v_mfma_f32_16x16x32_bf16 v[244:247],a[116:119],v[92:95],v[244:247]\n"
"v_exp_f32 v123,v29\n"
"v_mfma_f32_16x16x32_bf16 v[148:151],a[116:119],v[80:83],v[148:151]\n"
"buffer_load_dwordx4 v117,s[24:27],s64 offen offset:2048 sc1 lds\n"
"v_mfma_f32_16x16x32_bf16 v[180:183],a[116:119],v[84:87],v[180:183]\n"
"v_exp_f32 v119,v30\n"
"v_mfma_f32_16x16x32_bf16 v[212:215],a[116:119],v[88:91],v[212:215]\n"
"ds_read_b32 v125,v124\n"
"v_cvt_pk_bf16_f32 v78,v122,v123\n"
"v_mfma_f32_16x16x32_bf16 v[216:219],a[120:123],v[88:91],v[216:219]\n"
"v_exp_f32 v120,v31\n"
"v_mfma_f32_16x16x32_bf16 v[248:251],a[120:123],v[92:95],v[248:251]\n"
"buffer_load_dwordx4 v117,s[24:27],s64 offen offset:3072 sc1 lds\n"
"v_mfma_f32_16x16x32_bf16 v[152:155],a[120:123],v[80:83],v[152:155]\n"
"buffer_store_dwordx4 a[252:255],v112,s[24:27],s56 offen offset:3072 sc1\n"
"v_mfma_f32_16x16x32_bf16 v[184:187],a[120:123],v[84:87],v[184:187]\n"
"s_add_u32 s64,s64,16384\n"
"v_cvt_pk_bf16_f32 v79,v119,v120\n"
"v_or3_b32 v118,v118,v77,v78\n"
"v_mfma_f32_16x16x32_bf16 v[188:191],a[124:127],v[84:87],v[188:191]\n"
"s_waitcnt lgkmcnt(0)\n"
"v_subrev_u32 v125,s101,v125\n"
"v_or3_b32 v125,v118,v79,v125\n"
"v_mfma_f32_16x16x32_bf16 v[220:223],a[124:127],v[88:91],v[220:223]\n"
"v_and_b32 v125,0x40004000,v125\n"
"v_cmp_ne_u32 vcc,0,v125\n"
"v_mfma_f32_16x16x32_bf16 v[252:255],a[124:127],v[92:95],v[252:255]\n"
"v_mfma_f32_16x16x32_bf16 v[156:159],a[124:127],v[80:83],v[156:159]\n"
"s_cbranch_vccnz .Lfk_cs_g2\n"
".Lfk_cr_g2:\n"
".Lfk_Hx:\n"
"s_add_u32 s89,s89,1\n"
"s_cmp_lt_u32 s89,8\n"
"s_cbranch_scc1 .Lfk_H\n"
"v_max3_f32 v118,v32,v33,v34\n"
"v_max3_f32 v119,v35,v36,v37\n"
"v_max3_f32 v119,v119,v38,v39\n"
"v_max3_f32 v118,v118,v40,v41\n"
"v_max3_f32 v119,v119,v42,v43\n"
"v_max3_f32 v118,v118,v44,v45\n"
"v_max3_f32 v119,v119,v46,v47\n"
"v_max3_f32 v118,v118,v48,v49\n"
"v_max3_f32 v119,v119,v50,v51\n"
"v_max3_f32 v118,v118,v52,v53\n"
"v_max3_f32 v119,v119,v54,v55\n"
"v_max3_f32 v118,v118,v56,v57\n"
"v_max3_f32 v119,v119,v58,v59\n"
"v_max3_f32 v118,v118,v60,v61\n"
"v_max3_f32 v119,v119,v62,v63\n"
"v_max_f32 v118,v118,v119\n"
"v_cmp_lt_f32 vcc,s93,v118\n"
"s_nop 0\n"
"s_cbranch_vccnz .Lfk_cs_x1\n"
".Lfk_cr_x1:\n"
"s_cmp_ge_u32 s52,2\n"
"s_cbranch_scc1 .Lfk_tailB\n"
".Lfk_tailA:\n"
"s_add_u32 s82,s66,28672\n"
"s_cmp_eq_u32 s52,0\n"
"s_cselect_b32 s67,77824,s67\n"
"s_cselect_b32 s64,s82,s64\n"
"s_waitcnt vmcnt(11) lgkmcnt(0)\n"
"s_barrier\n"
"v_mfma_f32_16x16x32_bf16 v[16:19],a[64:67],a[32:35],v[104:107]\n"
"ds_read_b128 a[96:99],v114 offset:24576\n"
"ds_read_b128 a[100:103],v114 offset:25600\n"
"ds_read_b128 a[104:107],v114 offset:26624\n"
"v_mfma_f32_16x16x32_bf16 v[16:19],a[68:71],a[36:39],v[16:19]\n"
"ds_read_b128 a[108:111],v114 offset:27648\n"
"ds_read_b128 a[112:115],v114 offset:28672\n"
"ds_read_b128 a[116:119],v114 offset:29696\n"
"v_mfma_f32_16x16x32_bf16 v[16:19],a[72:75],a[40:43],v[16:19]\n"
"ds_read_b128 a[120:123],v114 offset:30720\n"
"ds_read_b128 a[124:127],v114 offset:31744\n"
"v_add_f32 v8,0x80000000,v126\n"
"v_mfma_f32_16x16x32_bf16 v[16:19],a[76:79],a[44:47],v[16:19]\n"
"v_min_f32 v8,v8,v100\n"
"v_add_f32 v9,0xf149f2ca,v126\n"
"v_min_f32 v9,v9,v101\n"
"v_mfma_f32_16x16x32_bf16 v[20:23],a[92:95],a[44:47],v[104:107]\n"
"v_add_f32 v10,0xf1c9f2ca,v126\n"
"v_min_f32 v10,v10,v102\n"
"v_add_f32 v11,0xf2177617,v126\n"
"v_mfma_f32_16x16x32_bf16 v[20:23],a[88:91],a[40:43],v[20:23]\n"
"v_min_f32 v11,v11,v103\n"
"v_add_f32 v12,0xf349f2ca,v126\n"
"v_min_f32 v12,v12,v100\n"
"v_mfma_f32_16x16x32_bf16 v[20:23],a[84:87],a[36:39],v[20:23]\n"
"v_add_f32 v13,0xf35691f6,v126\n"
"v_min_f32 v13,v13,v101\n"
"v_add_f32 v14,0xf3633123,v126\n"
"v_mfma_f32_16x16x32_bf16 v[20:23],a[80:83],a[32:35],v[20:23]\n"
"v_min_f32 v14,v14,v102\n"
"v_add_f32 v15,0xf36fd050,v126\n"
"v_min_f32 v15,v15,v103\n"
"v_mfma_f32_16x16x32_bf16 v[28:31],a[80:83],a[48:51],v[108:111]\n"
"s_add_u32 m0,s67,0\n"
"v_exp_f32 v32,v32\n"
"v_mfma_f32_16x16x32_bf16 v[28:31],a[84:87],a[52:55],v[28:31]\n"
"buffer_load_dwordx4 v112,s[24:27],s64 offen offset:0 sc1 lds\n"
"buffer_load_dwordx4 v112,s[24:27],s64 offen offset:1024 sc1 lds\n"
"v_exp_f32 v33,v33\n"
"v_mfma_f32_16x16x32_bf16 v[28:31],a[88:91],a[56:59],v[28:31]\n"
"v_exp_f32 v34,v34\n"
"v_cvt_pk_bf16_f32 v80,v32,v33\n"
"v_mfma_f32_16x16x32_bf16 v[28:31],a[92:95],a[60:63],v[28:31]\n"
"v_exp_f32 v35,v35\n"
"v_max3_f32 v118,v16,v17,v18\n"
"v_exp_f32 v36,v36\n"
"v_mfma_f32_16x16x32_bf16 v[24:27],a[76:79],a[60:63],v[108:111]\n"
"buffer_load_dwordx4 v112,s[24:27],s64 offen offset:2048 sc1 lds\n"
"v_mfma_f32_16x16x32_bf16 v[24:27],a[72:75],a[56:59],v[24:27]\n"
"v_exp_f32 v37,v37\n"
"v_cvt_pk_bf16_f32 v81,v34,v35\n"
"v_mfma_f32_16x16x32_bf16 v[24:27],a[68:71],a[52:55],v[24:27]\n"
"v_exp_f32 v38,v38\n"
"v_cvt_pk_bf16_f32 v82,v36,v37\n"
"v_exp_f32 v39,v39\n"
"v_mfma_f32_16x16x32_bf16 v[24:27],a[64:67],a[48:51],v[24:27]\n"
"buffer_load_dwordx4 v112,s[24:27],s64 offen offset:3072 sc1 lds\n"
"v_mfma_f32_16x16x32_bf16 v[8:11],a[64:67],a[16:19],v[8:11]\n"
"s_add_u32 s64,s64,16384\n"
"v_exp_f32 v40,v40\n"
"v_accvgpr_read_b32 v119,a150\n"
"v_mfma_f32_16x16x32_bf16 v[8:11],a[68:71],a[20:23],v[8:11]\n"
"v_exp_f32 v41,v41\n"
"v_accvgpr_read_b32 v120,a151\n"
"v_mfma_f32_16x16x32_bf16 v[8:11],a[72:75],a[24:27],v[8:11]\n"
"v_exp_f32 v42,v42\n"
"v_xor_b32 v119,s80,v119\n"
"v_mfma_f32_16x16x32_bf16 v[8:11],a[76:79],a[28:31],v[8:11]\n"
"v_exp_f32 v43,v43\n"
"v_xor_b32 v120,s81,v120\n"
"v_mfma_f32_16x16x32_bf16 v[12:15],a[92:95],a[28:31],v[12:15]\n"
"v_exp_f32 v44,v44\n"
"v_or_b32 v119,v119,v120\n"
"v_mfma_f32_16x16x32_bf16 v[12:15],a[88:91],a[24:27],v[12:15]\n"
"s_add_u32 s18,s8,0\n"
"v_exp_f32 v45,v45\n"
"v_cmp_ne_u32 s[14:15],0,v119\n"
"v_mfma_f32_16x16x32_bf16 v[12:15],a[84:87],a[20:23],v[12:15]\n"
"s_cmp_lg_u32 s14,0\n"
"s_cselect_b32 s18,2146435072,s18\n"
"v_exp_f32 v46,v46\n"
"v_mfma_f32_16x16x32_bf16 v[12:15],a[80:83],a[16:19],v[12:15]\n"
"s_add_u32 m0,s30,0\n"
"ds_read_b128 a[64:67],v113 offset:16384\n"
"s_waitcnt lgkmcnt(1)\n"
"v_mfma_f32_16x16x32_bf16 v[128:131],a[96:99],v[64:67],v[128:131]\n"
"ds_read_b128 a[68:71],v113 offset:17408\n"
"ds_read_b128 a[72:75],v113 offset:18432\n"
"ds_read_b128 a[76:79],v113 offset:19456\n"
"v_mfma_f32_16x16x32_bf16 v[160:163],a[96:99],v[68:71],v[160:163]\n"
"ds_read_b128 a[80:83],v113 offset:20480\n"
"v_exp_f32 v47,v47\n"
"v_mfma_f32_16x16x32_bf16 v[192:195],a[96:99],v[72:75],v[192:195]\n"
"ds_read_b128 a[84:87],v113 offset:21504\n"
"ds_read_b128 a[88:91],v113 offset:22528\n"
"ds_read_b128 a[92:95],v113 offset:23552\n"
"v_exp_f32 v48,v48\n"
"v_mfma_f32_16x16x32_bf16 v[224:227],a[96:99],v[76:79],v[224:227]\n"
"buffer_load_dwordx4 v112,s[24:27],s18 offen offset:0 sc1 lds\n"
"v_exp_f32 v49,v49\n"
"v_mfma_f32_16x16x32_bf16 a[140:143],a[144:147],v[76:79],a[140:143]\n"
"buffer_load_dwordx4 v112,s[24:27],s18 offen offset:1024 sc1 lds\n"
"v_mfma_f32_16x16x32_bf16 v[228:231],a[100:103],v[76:79],v[228:231]\n"
"buffer_load_dwordx4 v112,s[24:27],s18 offen offset:2048 sc1 lds\n"
"buffer_load_dwordx4 v112,s[24:27],s18 offen offset:3072 sc1 lds\n"
"v_mfma_f32_16x16x32_bf16 v[132:135],a[100:103],v[64:67],v[132:135]\n"
"s_add_u32 m0,m0,4096\n"
"v_exp_f32 v50,v50\n"
"v_cvt_pk_bf16_f32 v83,v38,v39\n"
"v_mfma_f32_16x16x32_bf16 v[164:167],a[100:103],v[68:71],v[164:167]\n"
"buffer_load_dwordx4 v117,s[24:27],s18 offen offset:0 sc1 lds\n"
"v_cvt_pk_bf16_f32 v84,v40,v41\n"
"v_cvt_pk_bf16_f32 v85,v42,v43\n"
"v_mfma_f32_16x16x32_bf16 v[196:199],a[100:103],v[72:75],v[196:199]\n"
"v_exp_f32 v51,v51\n"
"v_cvt_pk_bf16_f32 v86,v44,v45\n"
"v_mfma_f32_16x16x32_bf16 a[136:139],a[144:147],v[72:75],a[136:139]\n"
"v_exp_f32 v52,v52\n"
"v_cvt_pk_bf16_f32 v87,v46,v47\n"
"v_mfma_f32_16x16x32_bf16 v[200:203],a[104:107],v[72:75],v[200:203]\n"
"buffer_load_dwordx4 v117,s[24:27],s18 offen offset:1024 sc1 lds\n"
"v_cvt_pk_bf16_f32 v88,v48,v49\n"
"v_cvt_pk_bf16_f32 v89,v50,v51\n"
"v_mfma_f32_16x16x32_bf16 v[232:235],a[104:107],v[76:79],v[232:235]\n"
"v_exp_f32 v53,v53\n"
"v_max3_f32 v118,v118,v19,v20\n"
"v_mfma_f32_16x16x32_bf16 v[136:139],a[104:107],v[64:67],v[136:139]\n"
"v_cvt_pk_bf16_f32 v90,v52,v53\n"
"v_max3_f32 v118,v118,v21,v22\n"
"v_max3_f32 v118,v118,v23,v28\n"
"v_exp_f32 v54,v54\n"
"v_mfma_f32_16x16x32_bf16 v[168:171],a[104:107],v[68:71],v[168:171]\n"
"buffer_load_dwordx4 v117,s[24:27],s18 offen offset:2048 sc1 lds\n"
"v_mfma_f32_16x16x32_bf16 a[132:135],a[144:147],v[68:71],a[132:135]\n"
"v_exp_f32 v55,v55\n"
"v_max3_f32 v118,v118,v29,v30\n"
"v_mfma_f32_16x16x32_bf16 v[172:175],a[108:111],v[68:71],v[172:175]\n"
"v_cvt_pk_bf16_f32 v91,v54,v55\n"
"v_max3_f32 v118,v118,v31,v24\n"
"v_max3_f32 v118,v118,v25,v26\n"
"v_exp_f32 v56,v56\n"
"v_mfma_f32_16x16x32_bf16 v[204:207],a[108:111],v[72:75],v[204:207]\n"
"buffer_load_dwordx4 v117,s[24:27],s18 offen offset:3072 sc1 lds\n"
"v_mfma_f32_16x16x32_bf16 v[236:239],a[108:111],v[76:79],v[236:239]\n"
"s_sub_u32 s82,s3,32768\n"
"v_max3_f32 v118,v118,v27,v8\n"
"v_max3_f32 v118,v118,v9,v10\n"
"v_mfma_f32_16x16x32_bf16 v[140:143],a[108:111],v[64:67],v[140:143]\n"
"v_exp_f32 v57,v57\n"
"v_max3_f32 v118,v118,v11,v12\n"
"v_exp_f32 v58,v58\n"
"v_mfma_f32_16x16x32_bf16 a[128:131],a[144:147],v[64:67],a[128:131]\n"
"buffer_load_dwordx4 a[0:3],v112,s[24:27],s82 offen offset:0 sc1\n"
"v_mfma_f32_16x16x32_bf16 v[144:147],a[112:115],v[64:67],v[144:147]\n"
"v_max3_f32 v118,v118,v13,v14\n"
"v_max_f32 v118,v118,v15\n"
"v_mfma_f32_16x16x32_bf16 v[176:179],a[112:115],v[68:71],v[176:179]\n"
"v_exp_f32 v59,v59\n"
"v_mfma_f32_16x16x32_bf16 v[208:211],a[112:115],v[72:75],v[208:211]\n"
"v_cvt_pk_bf16_f32 v92,v56,v57\n"
"v_cvt_pk_bf16_f32 v93,v58,v59\n"
"v_exp_f32 v60,v60\n"
"v_mfma_f32_16x16x32_bf16 v[240:243],a[112:115],v[76:79],v[240:243]\n"
"buffer_load_dwordx4 a[4:7],v112,s[24:27],s82 offen offset:1024 sc1\n"
"v_mfma_f32_16x16x32_bf16 v[244:247],a[116:119],v[76:79],v[244:247]\n"
"v_exp_f32 v61,v61\n"
"v_mfma_f32_16x16x32_bf16 v[148:151],a[116:119],v[64:67],v[148:151]\n"
"v_cmp_lt_f32 vcc,s93,v118\n"
"v_cvt_pk_bf16_f32 v94,v60,v61\n"
"v_mfma_f32_16x16x32_bf16 v[180:183],a[116:119],v[68:71],v[180:183]\n"
"buffer_load_dwordx4 a[8:11],v112,s[24:27],s82 offen offset:2048 sc1\n"
"v_mfma_f32_16x16x32_bf16 v[212:215],a[116:119],v[72:75],v[212:215]\n"
"v_exp_f32 v62,v62\n"
"v_mfma_f32_16x16x32_bf16 v[216:219],a[120:123],v[72:75],v[216:219]\n"
"v_exp_f32 v63,v63\n"
"v_mfma_f32_16x16x32_bf16 v[248:251],a[120:123],v[76:79],v[248:251]\n"
"v_cvt_pk_bf16_f32 v95,v62,v63\n"
"v_mfma_f32_16x16x32_bf16 v[152:155],a[120:123],v[64:67],v[152:155]\n"
"buffer_load_dwordx4 a[12:15],v112,s[24:27],s82 offen offset:3072 sc1\n"
"v_mfma_f32_16x16x32_bf16 v[184:187],a[120:123],v[68:71],v[184:187]\n"
"v_mfma_f32_16x16x32_bf16 v[188:191],a[124:127],v[68:71],v[188:191]\n"
"v_mfma_f32_16x16x32_bf16 v[220:223],a[124:127],v[72:75],v[220:223]\n"
"v_mfma_f32_16x16x32_bf16 v[252:255],a[124:127],v[76:79],v[252:255]\n"
"v_mfma_f32_16x16x32_bf16 v[156:159],a[124:127],v[64:67],v[156:159]\n"
"s_cbranch_vccnz .Lfk_cs_a3\n"
".Lfk_cr_a3:\n"
"s_waitcnt vmcnt(17) lgkmcnt(0)\n"
"s_barrier\n"
"v_mfma_f32_16x16x32_bf16 v[56:59],a[64:67],a[48:51],v[108:111]\n"
"ds_read_b128 a[96:99],v114 offset:57344\n"
"ds_read_b128 a[100:103],v114 offset:58368\n"
"ds_read_b128 a[104:107],v114 offset:59392\n"
"v_mfma_f32_16x16x32_bf16 v[56:59],a[68:71],a[52:55],v[56:59]\n"
"ds_read_b128 a[108:111],v114 offset:60416\n"
"ds_read_b128 a[112:115],v114 offset:61440\n"
"ds_read_b128 a[116:119],v114 offset:62464\n"
"v_mfma_f32_16x16x32_bf16 v[56:59],a[72:75],a[56:59],v[56:59]\n"
"ds_read_b128 a[120:123],v114 offset:63488\n"
"ds_read_b128 a[124:127],v114 offset:64512\n"
"v_add_f32 v48,0x80000000,v126\n"
"v_mfma_f32_16x16x32_bf16 v[56:59],a[76:79],a[60:63],v[56:59]\n"
"v_min_f32 v48,v48,v104\n"
"v_add_f32 v49,0xf149f2ca,v126\n"
"v_min_f32 v49,v49,v105\n"
"v_mfma_f32_16x16x32_bf16 v[60:63],a[92:95],a[60:63],v[108:111]\n"
"v_add_f32 v50,0xf1c9f2ca,v126\n"
"v_add_f32 v51,0xf2177617,v126\n"
"v_mfma_f32_16x16x32_bf16 v[60:63],a[88:91],a[56:59],v[60:63]\n"
"v_add_f32 v52,0xf349f2ca,v126\n"
"v_min_f32 v52,v52,v104\n"
"v_mfma_f32_16x16x32_bf16 v[60:63],a[84:87],a[52:55],v[60:63]\n"
"v_add_f32 v53,0xf35691f6,v126\n"
"v_min_f32 v53,v53,v105\n"
"v_add_f32 v54,0xf3633123,v126\n"
"v_mfma_f32_16x16x32_bf16 v[60:63],a[80:83],a[48:51],v[60:63]\n"
"v_min_f32 v54,v54,v106\n"
"v_add_f32 v55,0xf36fd050,v126\n"
"v_min_f32 v55,v55,v107\n"
"v_min_f32 v51,v51,v107\n"
"v_min_f32 v50,v50,v106\n"
"v_mfma_f32_16x16x32_bf16 v[52:55],a[80:83],a[32:35],v[52:55]\n"
"s_add_u32 m0,s67,16384\n"
"v_exp_f32 v8,v8\n"
"v_mfma_f32_16x16x32_bf16 v[52:55],a[84:87],a[36:39],v[52:55]\n"
"buffer_load_dwordx4 v112,s[24:27],s64 offen offset:0 sc1 lds\n"
"buffer_load_dwordx4 v112,s[24:27],s64 offen offset:1024 sc1 lds\n"
"v_exp_f32 v9,v9\n"
"v_mfma_f32_16x16x32_bf16 v[52:55],a[88:91],a[40:43],v[52:55]\n"
"v_exp_f32 v10,v10\n"
"v_cvt_pk_bf16_f32 v68,v8,v9\n"
"v_mfma_f32_16x16x32_bf16 v[52:55],a[92:95],a[44:47],v[52:55]\n"
"v_exp_f32 v11,v11\n"
"v_max3_f32 v118,v56,v57,v58\n"
"v_exp_f32 v12,v12\n"
"v_mfma_f32_16x16x32_bf16 v[48:51],a[76:79],a[44:47],v[48:51]\n"
"buffer_load_dwordx4 v112,s[24:27],s64 offen offset:2048 sc1 lds\n"
"v_mfma_f32_16x16x32_bf16 v[48:51],a[72:75],a[40:43],v[48:51]\n"
"v_exp_f32 v13,v13\n"
"v_cvt_pk_bf16_f32 v69,v10,v11\n"
"v_mfma_f32_16x16x32_bf16 v[48:51],a[68:71],a[36:39],v[48:51]\n"
"v_exp_f32 v14,v14\n"
"v_cvt_pk_bf16_f32 v70,v12,v13\n"
"v_exp_f32 v15,v15\n"
"v_mfma_f32_16x16x32_bf16 v[48:51],a[64:67],a[32:35],v[48:51]\n"
"ds_read_b128 a[64:67],v113 offset:49152\n"
"s_waitcnt lgkmcnt(1)\n"
"v_mfma_f32_16x16x32_bf16 v[128:131],a[96:99],v[80:83],v[128:131]\n"
"ds_read_b128 a[68:71],v113 offset:50176\n"
"v_exp_f32 v16,v16\n"
"v_mfma_f32_16x16x32_bf16 v[160:163],a[96:99],v[84:87],v[160:163]\n"
"ds_read_b128 a[72:75],v113 offset:51200\n"
"v_exp_f32 v17,v17\n"
"v_mfma_f32_16x16x32_bf16 v[192:195],a[96:99],v[88:91],v[192:195]\n"
"ds_read_b128 a[76:79],v113 offset:52224\n"
"ds_read_b128 a[80:83],v113 offset:53248\n"
"ds_read_b128 a[84:87],v113 offset:54272\n"
"v_mfma_f32_16x16x32_bf16 v[224:227],a[96:99],v[92:95],v[224:227]\n"
"ds_read_b128 a[88:91],v113 offset:55296\n"
"v_exp_f32 v18,v18\n"
"v_mfma_f32_16x16x32_bf16 a[140:143],a[144:147],v[92:95],a[140:143]\n"
"ds_read_b128 a[92:95],v113 offset:56320\n"
"buffer_load_dwordx4 v112,s[24:27],s64 offen offset:3072 sc1 lds\n"
"s_add_u32 s64,s64,16384\n"
"v_mfma_f32_16x16x32_bf16 v[228:231],a[100:103],v[92:95],v[228:231]\n"
"s_sub_u32 s82,s3,49152\n"
"v_exp_f32 v19,v19\n"
"v_mfma_f32_16x16x32_bf16 v[132:135],a[100:103],v[80:83],v[132:135]\n"
"buffer_load_dwordx4 a[16:19],v112,s[24:27],s82 offen offset:0 sc1\n"
"v_cvt_pk_bf16_f32 v71,v14,v15\n"
"v_cvt_pk_bf16_f32 v72,v16,v17\n"
"v_cvt_pk_bf16_f32 v73,v18,v19\n"
"v_exp_f32 v20,v20\n"
"v_mfma_f32_16x16x32_bf16 v[164:167],a[100:103],v[84:87],v[164:167]\n"
"buffer_load_dwordx4 a[20:23],v112,s[24:27],s82 offen offset:1024 sc1\n"
"v_mfma_f32_16x16x32_bf16 v[196:199],a[100:103],v[88:91],v[196:199]\n"
"v_exp_f32 v21,v21\n"
"v_max3_f32 v118,v118,v59,v60\n"
"v_mfma_f32_16x16x32_bf16 a[136:139],a[144:147],v[88:91],a[136:139]\n"
"v_cvt_pk_bf16_f32 v74,v20,v21\n"
"v_max3_f32 v118,v118,v61,v62\n"
"v_max3_f32 v118,v118,v63,v52\n"
"v_mfma_f32_16x16x32_bf16 v[200:203],a[104:107],v[88:91],v[200:203]\n"
"v_exp_f32 v22,v22\n"
"v_max3_f32 v118,v118,v53,v54\n"
"v_mfma_f32_16x16x32_bf16 v[232:235],a[104:107],v[92:95],v[232:235]\n"
"buffer_load_dwordx4 a[24:27],v112,s[24:27],s82 offen offset:2048 sc1\n"
"v_max3_f32 v118,v118,v55,v48\n"
"v_max3_f32 v118,v118,v49,v50\n"
"v_mfma_f32_16x16x32_bf16 v[136:139],a[104:107],v[80:83],v[136:139]\n"
"v_exp_f32 v23,v23\n"
"v_max_f32 v118,v118,v51\n"
"v_mfma_f32_16x16x32_bf16 v[168:171],a[104:107],v[84:87],v[168:171]\n"
"v_cvt_pk_bf16_f32 v75,v22,v23\n"
"v_cmp_lt_f32 vcc,s93,v118\n"
"v_mfma_f32_16x16x32_bf16 a[132:135],a[144:147],v[84:87],a[132:135]\n"
"buffer_load_dwordx4 a[28:31],v112,s[24:27],s82 offen offset:3072 sc1\n"
"v_mfma_f32_16x16x32_bf16 v[172:175],a[108:111],v[84:87],v[172:175]\n"
"v_exp_f32 v24,v24\n"
"v_mfma_f32_16x16x32_bf16 v[204:207],a[108:111],v[88:91],v[204:207]\n"
"s_sub_u32 s82,s3,65536\n"
"v_exp_f32 v25,v25\n"
"v_mfma_f32_16x16x32_bf16 v[236:239],a[108:111],v[92:95],v[236:239]\n"
"v_cvt_pk_bf16_f32 v76,v24,v25\n"
"v_mfma_f32_16x16x32_bf16 v[140:143],a[108:111],v[80:83],v[140:143]\n"
"buffer_load_dwordx4 v[32:35],v112,s[24:27],s82 offen offset:0 sc1\n"
"v_mfma_f32_16x16x32_bf16 a[128:131],a[144:147],v[80:83],a[128:131]\n"
"v_exp_f32 v26,v26\n"
"v_mfma_f32_16x16x32_bf16 v[144:147],a[112:115],v[80:83],v[144:147]\n"
"v_exp_f32 v27,v27\n"
"v_mfma_f32_16x16x32_bf16 v[176:179],a[112:115],v[84:87],v[176:179]\n"
"buffer_load_dwordx4 v[36:39],v112,s[24:27],s82 offen offset:1024 sc1\n"
"v_mfma_f32_16x16x32_bf16 v[208:211],a[112:115],v[88:91],v[208:211]\n"
"v_exp_f32 v28,v28\n"
"v_mfma_f32_16x16x32_bf16 v[240:243],a[112:115],v[92:95],v[240:243]\n"
"v_cvt_pk_bf16_f32 v77,v26,v27\n"
"v_mfma_f32_16x16x32_bf16 v[244:247],a[116:119],v[92:95],v[244:247]\n"
"v_exp_f32 v29,v29\n"
"v_mfma_f32_16x16x32_bf16 v[148:151],a[116:119],v[80:83],v[148:151]\n"
"buffer_load_dwordx4 v[40:43],v112,s[24:27],s82 offen offset:2048 sc1\n"
"v_mfma_f32_16x16x32_bf16 v[180:183],a[116:119],v[84:87],v[180:183]\n"
"v_exp_f32 v30,v30\n"
"v_mfma_f32_16x16x32_bf16 v[212:215],a[116:119],v[88:91],v[212:215]\n"
"v_cvt_pk_bf16_f32 v78,v28,v29\n"
"v_mfma_f32_16x16x32_bf16 v[216:219],a[120:123],v[88:91],v[216:219]\n"
"v_exp_f32 v31,v31\n"
"v_mfma_f32_16x16x32_bf16 v[248:251],a[120:123],v[92:95],v[248:251]\n"
"v_cvt_pk_bf16_f32 v79,v30,v31\n"
"v_mfma_f32_16x16x32_bf16 v[152:155],a[120:123],v[80:83],v[152:155]\n"
"buffer_load_dwordx4 v[44:47],v112,s[24:27],s82 offen offset:3072 sc1\n"
"v_mfma_f32_16x16x32_bf16 v[184:187],a[120:123],v[84:87],v[184:187]\n"
"v_mfma_f32_16x16x32_bf16 v[188:191],a[124:127],v[84:87],v[188:191]\n"
"v_mfma_f32_16x16x32_bf16 v[220:223],a[124:127],v[88:91],v[220:223]\n"
"v_mfma_f32_16x16x32_bf16 v[252:255],a[124:127],v[92:95],v[252:255]\n"
"v_mfma_f32_16x16x32_bf16 v[156:159],a[124:127],v[80:83],v[156:159]\n"
"s_cbranch_vccnz .Lfk_cs_a0\n"
".Lfk_cr_a0:\n"
"s_load_dwordx2 s[36:37],s[0:1],0\n"
"s_load_dwordx2 s[44:45],s[0:1],80\n"
"s_load_dwordx2 s[48:49],s[0:1],88\n"
"s_lshl_b32 s82,s78,2\n"
"s_add_u32 s82,s82,65536\n"
"v_add_u32 v113,s82,v112\n"
"s_lshr_b32 s82,s78,2\n"
"s_add_u32 s82,s82,122880\n"
"v_lshrrev_b32 v115,1,v112\n"
"v_add_u32 v116,s82,v115\n"
"s_waitcnt lgkmcnt(0)\n"
"s_barrier\n"
"v_add_f32 v24,0x80000000,v126\n"
"v_min_f32 v24,v24,v108\n"
"v_add_f32 v25,0xf149f2ca,v126\n"
"v_min_f32 v25,v25,v109\n"
"v_add_f32 v26,0xf1c9f2ca,v126\n"
"v_min_f32 v26,v26,v110\n"
"v_add_f32 v27,0xf2177617,v126\n"
"v_min_f32 v27,v27,v111\n"
"v_add_f32 v28,0xf349f2ca,v126\n"
"v_min_f32 v28,v28,v108\n"
"v_add_f32 v29,0xf35691f6,v126\n"
"v_min_f32 v29,v29,v109\n"
"v_add_f32 v30,0xf3633123,v126\n"
"v_min_f32 v30,v30,v110\n"
"v_add_f32 v31,0xf36fd050,v126\n"
"v_min_f32 v31,v31,v111\n"
"v_exp_f32 v48,v48\n"
"v_mfma_f32_16x16x32_bf16 v[24:27],a[64:67],a[48:51],v[24:27]\n"
"s_add_u32 s83,s18,2097152\n"
"ds_read_b128 a[96:99],v114 offset:40960\n"
"ds_read_b128 a[100:103],v114 offset:41984\n"
"v_exp_f32 v49,v49\n"
"v_mfma_f32_16x16x32_bf16 v[24:27],a[68:71],a[52:55],v[24:27]\n"
"s_add_u32 m0,s30,32768\n"
"ds_read_b128 a[104:107],v114 offset:43008\n"
"ds_read_b128 a[108:111],v114 offset:44032\n"
"v_mfma_f32_16x16x32_bf16 v[24:27],a[72:75],a[56:59],v[24:27]\n"
"ds_read_b128 a[112:115],v114 offset:45056\n"
"ds_read_b128 a[116:119],v114 offset:46080\n"
"ds_read_b128 a[120:123],v114 offset:47104\n"
"ds_read_b128 a[124:127],v114 offset:48128\n"
"v_exp_f32 v50,v50\n"
"v_mfma_f32_16x16x32_bf16 v[24:27],a[76:79],a[60:63],v[24:27]\n"
"buffer_load_dwordx4 v112,s[24:27],s83 offen offset:0 sc1 lds\n"
"v_cvt_pk_bf16_f32 v88,v48,v49\n"
"v_mfma_f32_16x16x32_bf16 v[28:31],a[92:95],a[60:63],v[28:31]\n"
"buffer_load_dwordx4 v112,s[24:27],s83 offen offset:1024 sc1 lds\n"
"v_accvgpr_read_b32 v97,a128\n"
"v_cvt_pk_bf16_f32 v128,v128,v129\n"
"v_cvt_pk_bf16_f32 v129,v130,v131\n"
"v_mfma_f32_16x16x32_bf16 v[28:31],a[88:91],a[56:59],v[28:31]\n"
"v_exp_f32 v51,v51\n"
"v_cvt_pk_bf16_f32 v130,v132,v133\n"
"v_cvt_pk_bf16_f32 v131,v134,v135\n"
"v_cvt_pk_bf16_f32 v89,v50,v51\n"
"v_mfma_f32_16x16x32_bf16 v[28:31],a[84:87],a[52:55],v[28:31]\n"
"ds_write_b128 v113,v[128:131] offset:0\n"
"v_cvt_pk_bf16_f32 v136,v136,v137\n"
"v_cvt_pk_bf16_f32 v137,v138,v139\n"
"v_exp_f32 v52,v52\n"
"v_mfma_f32_16x16x32_bf16 v[28:31],a[80:83],a[48:51],v[28:31]\n"
"buffer_load_dwordx4 v112,s[24:27],s83 offen offset:2048 sc1 lds\n"
"v_max3_f32 v118,v24,v25,v26\n"
"s_waitcnt lgkmcnt(1)\n"
"v_mfma_f32_16x16x32_bf16 v[160:163],a[96:99],v[68:71],v[160:163]\n"
"v_cvt_pk_bf16_f32 v138,v140,v141\n"
"v_cvt_pk_bf16_f32 v139,v142,v143\n"
"v_cvt_pk_bf16_f32 v144,v144,v145\n"
"v_mfma_f32_16x16x32_bf16 a[132:135],a[144:147],v[68:71],a[132:135]\n"
"ds_write_b128 v113,v[136:139] offset:1024\n"
"buffer_load_dwordx4 v112,s[24:27],s83 offen offset:3072 sc1 lds\n"
"v_exp_f32 v53,v53\n"
"v_cvt_pk_bf16_f32 v145,v146,v147\n"
"v_mfma_f32_16x16x32_bf16 v[164:167],a[100:103],v[68:71],v[164:167]\n"
"s_add_u32 m0,m0,4096\n"
"v_exp_f32 v54,v54\n"
"v_cvt_pk_bf16_f32 v90,v52,v53\n"
"v_cvt_pk_bf16_f32 v146,v148,v149\n"
"v_mfma_f32_16x16x32_bf16 v[168:171],a[104:107],v[68:71],v[168:171]\n"
"v_max3_f32 v118,v118,v27,v28\n"
"v_max3_f32 v118,v118,v29,v30\n"
"v_max_f32 v118,v118,v31\n"
"v_cmp_lt_f32 vcc,s93,v118\n"
"v_exp_f32 v55,v55\n"
"v_mfma_f32_16x16x32_bf16 v[172:175],a[108:111],v[68:71],v[172:175]\n"
"buffer_load_dwordx4 v117,s[24:27],s83 offen offset:0 sc1 lds\n"
"v_cvt_pk_bf16_f32 v147,v150,v151\n"
"v_cvt_pk_bf16_f32 v91,v54,v55\n"
"v_mfma_f32_16x16x32_bf16 v[176:179],a[112:115],v[68:71],v[176:179]\n"
"ds_write_b128 v113,v[144:147] offset:2048\n"
"v_cvt_pk_bf16_f32 v152,v152,v153\n"
"v_cvt_pk_bf16_f32 v153,v154,v155\n"
"v_exp_f32 v56,v56\n"
"v_mfma_f32_16x16x32_bf16 v[180:183],a[116:119],v[68:71],v[180:183]\n"
"buffer_load_dwordx4 v117,s[24:27],s83 offen offset:1024 sc1 lds\n"
"v_cvt_pk_bf16_f32 v154,v156,v157\n"
"v_cvt_pk_bf16_f32 v155,v158,v159\n"
"v_mfma_f32_16x16x32_bf16 v[184:187],a[120:123],v[68:71],v[184:187]\n"
"ds_write_b128 v113,v[152:155] offset:3072\n"
"ds_write_b64 v116,v[96:97] offset:0\n"
"v_mfma_f32_16x16x32_bf16 v[188:191],a[124:127],v[68:71],v[188:191]\n"
"v_exp_f32 v57,v57\n"
"v_mfma_f32_16x16x32_bf16 v[192:195],a[96:99],v[72:75],v[192:195]\n"
"buffer_load_dwordx4 v117,s[24:27],s83 offen offset:2048 sc1 lds\n"
"v_cvt_pk_bf16_f32 v92,v56,v57\n"
"v_mfma_f32_16x16x32_bf16 a[136:139],a[144:147],v[72:75],a[136:139]\n"
"v_exp_f32 v58,v58\n"
"v_exp_f32 v59,v59\n"
"v_mfma_f32_16x16x32_bf16 v[196:199],a[100:103],v[72:75],v[196:199]\n"
"buffer_load_dwordx4 v117,s[24:27],s83 offen offset:3072 sc1 lds\n"
"v_accvgpr_read_b32 v101,a132\n"
"v_mfma_f32_16x16x32_bf16 v[200:203],a[104:107],v[72:75],v[200:203]\n"
"s_sub_u32 s82,s3,81920\n"
"v_cvt_pk_bf16_f32 v93,v58,v59\n"
"v_cvt_pk_bf16_f32 v160,v160,v161\n"
"v_cvt_pk_bf16_f32 v161,v162,v163\n"
"v_mfma_f32_16x16x32_bf16 v[204:207],a[108:111],v[72:75],v[204:207]\n"
"v_exp_f32 v60,v60\n"
"v_cvt_pk_bf16_f32 v162,v164,v165\n"
"v_cvt_pk_bf16_f32 v163,v166,v167\n"
"v_mfma_f32_16x16x32_bf16 v[208:211],a[112:115],v[72:75],v[208:211]\n"
"buffer_load_dwordx4 a[32:35],v112,s[24:27],s82 offen offset:0 sc1\n"
"ds_write_b128 v113,v[160:163] offset:4096\n"
"v_cvt_pk_bf16_f32 v168,v168,v169\n"
"v_cvt_pk_bf16_f32 v169,v170,v171\n"
"v_mfma_f32_16x16x32_bf16 v[212:215],a[116:119],v[72:75],v[212:215]\n"
"v_exp_f32 v61,v61\n"
"v_cvt_pk_bf16_f32 v170,v172,v173\n"
"v_cvt_pk_bf16_f32 v171,v174,v175\n"
"v_cvt_pk_bf16_f32 v94,v60,v61\n"
"v_mfma_f32_16x16x32_bf16 v[216:219],a[120:123],v[72:75],v[216:219]\n"
"ds_write_b128 v113,v[168:171] offset:5120\n"
"v_cvt_pk_bf16_f32 v176,v176,v177\n"
"v_cvt_pk_bf16_f32 v177,v178,v179\n"
"v_exp_f32 v62,v62\n"
"v_mfma_f32_16x16x32_bf16 v[220:223],a[124:127],v[72:75],v[220:223]\n"
"buffer_load_dwordx4 a[36:39],v112,s[24:27],s82 offen offset:1024 sc1\n"
"v_cvt_pk_bf16_f32 v178,v180,v181\n"
"v_cvt_pk_bf16_f32 v179,v182,v183\n"
"v_mfma_f32_16x16x32_bf16 v[224:227],a[96:99],v[76:79],v[224:227]\n"
"ds_write_b128 v113,v[176:179] offset:6144\n"
"v_cvt_pk_bf16_f32 v184,v184,v185\n"
"v_cvt_pk_bf16_f32 v185,v186,v187\n"
"v_mfma_f32_16x16x32_bf16 a[140:143],a[144:147],v[76:79],a[140:143]\n"
"v_exp_f32 v63,v63\n"
"v_cvt_pk_bf16_f32 v186,v188,v189\n"
"v_cvt_pk_bf16_f32 v187,v190,v191\n"
"v_mfma_f32_16x16x32_bf16 v[228:231],a[100:103],v[76:79],v[228:231]\n"
"buffer_load_dwordx4 a[40:43],v112,s[24:27],s82 offen offset:2048 sc1\n"
"ds_write_b128 v113,v[184:187] offset:7168\n"
"v_mfma_f32_16x16x32_bf16 v[232:235],a[104:107],v[76:79],v[232:235]\n"
"ds_write_b64 v116,v[100:101] offset:512\n"
"v_cvt_pk_bf16_f32 v95,v62,v63\n"
"v_mfma_f32_16x16x32_bf16 v[236:239],a[108:111],v[76:79],v[236:239]\n"
"buffer_load_dwordx4 a[44:47],v112,s[24:27],s82 offen offset:3072 sc1\n"
"v_mfma_f32_16x16x32_bf16 v[240:243],a[112:115],v[76:79],v[240:243]\n"
"s_sub_u32 s82,s3,131072\n"
"buffer_load_dwordx4 v[128:131],v112,s[24:27],s82 offen offset:0 sc1\n"
"buffer_load_dwordx4 v[132:135],v112,s[24:27],s82 offen offset:1024 sc1\n"
"buffer_load_dwordx4 v[136:139],v112,s[24:27],s82 offen offset:2048 sc1\n"
"buffer_load_dwordx4 v[140:143],v112,s[24:27],s82 offen offset:3072 sc1\n"
"v_mfma_f32_16x16x32_bf16 v[244:247],a[116:119],v[76:79],v[244:247]\n"
"s_sub_u32 s82,s3,147456\n"
"buffer_load_dwordx4 v[144:147],v112,s[24:27],s82 offen offset:0 sc1\n"
"buffer_load_dwordx4 v[148:151],v112,s[24:27],s82 offen offset:1024 sc1\n"
"v_mfma_f32_16x16x32_bf16 v[248:251],a[120:123],v[76:79],v[248:251]\n"
"buffer_load_dwordx4 v[152:155],v112,s[24:27],s82 offen offset:2048 sc1\n"
"buffer_load_dwordx4 v[156:159],v112,s[24:27],s82 offen offset:3072 sc1\n"
"v_mfma_f32_16x16x32_bf16 v[252:255],a[124:127],v[76:79],v[252:255]\n"
"s_cbranch_vccnz .Lfk_cs_a1\n"
".Lfk_cr_a1:\n"
"s_waitcnt vmcnt(28) lgkmcnt(0)\n"
"s_barrier\n"
"ds_read_b128 a[96:99],v114 offset:8192\n"
"ds_read_b128 a[100:103],v114 offset:9216\n"
"ds_read_b128 a[104:107],v114 offset:10240\n"
"ds_read_b128 a[108:111],v114 offset:11264\n"
"ds_read_b128 a[112:115],v114 offset:12288\n"
"ds_read_b128 a[116:119],v114 offset:13312\n"
"ds_read_b128 a[120:123],v114 offset:14336\n"
"ds_read_b128 a[124:127],v114 offset:15360\n"
"v_xor_b32 v113,32768,v113\n"
"v_xor_b32 v116,2048,v116\n"
"ds_read_b64 v[98:99],v116 offset:0\n"
"ds_read_b128 a[64:67],v113 offset:0\n"
"ds_read_b128 a[68:71],v113 offset:1024\n"
"ds_read_b128 a[72:75],v113 offset:2048\n"
"ds_read_b128 a[76:79],v113 offset:3072\n"
"ds_read_b64 v[102:103],v116 offset:512\n"
"ds_read_b128 a[80:83],v113 offset:4096\n"
"ds_read_b128 a[84:87],v113 offset:5120\n"
"ds_read_b128 a[88:91],v113 offset:6144\n"
"ds_read_b128 a[92:95],v113 offset:7168\n"
"v_exp_f32 v24,v24\n"
"v_exp_f32 v25,v25\n"
"v_exp_f32 v26,v26\n"
"v_exp_f32 v27,v27\n"
"v_exp_f32 v28,v28\n"
"v_exp_f32 v29,v29\n"
"v_exp_f32 v30,v30\n"
"v_exp_f32 v31,v31\n"
"v_cvt_pk_bf16_f32 v76,v24,v25\n"
"v_cvt_pk_bf16_f32 v77,v26,v27\n"
"v_cvt_pk_bf16_f32 v78,v28,v29\n"
"v_cvt_pk_bf16_f32 v79,v30,v31\n"
"s_waitcnt lgkmcnt(10)\n"
"v_mfma_f32_16x16x32_bf16 a[136:139],a[144:147],v[88:91],a[136:139]\n"
"s_sub_u32 s82,s3,163840\n"
"v_lshrrev_b32 v70,4,v112\n"
"v_and_b32 v71,15,v70\n"
"v_mfma_f32_16x16x32_bf16 a[140:143],a[144:147],v[92:95],a[140:143]\n"
"buffer_load_dwordx4 v[0:3],v112,s[24:27],s82 offen offset:0 sc1\n"
"v_lshrrev_b32 v70,4,v70\n"
"v_lshlrev_b32 v70,2,v70\n"
"v_mfma_f32_16x16x32_bf16 v[192:195],a[96:99],v[88:91],v[192:195]\n"
"buffer_load_dwordx4 v[4:7],v112,s[24:27],s82 offen offset:1024 sc1\n"
"v_sub_u32 v70,v71,v70\n"
"v_mov_b32 v64,0\n"
"v_mfma_f32_16x16x32_bf16 v[224:227],a[96:99],v[92:95],v[224:227]\n"
"ds_read_b128 a[96:99],v114 offset:24576\n"
"buffer_load_dwordx4 v[8:11],v112,s[24:27],s82 offen offset:2048 sc1\n"
"v_mfma_f32_16x16x32_bf16 v[196:199],a[100:103],v[88:91],v[196:199]\n"
"buffer_load_dwordx4 v[12:15],v112,s[24:27],s82 offen offset:3072 sc1\n"
"v_mov_b32 v69,0\n"
"v_mfma_f32_16x16x32_bf16 v[228:231],a[100:103],v[92:95],v[228:231]\n"
"s_sub_u32 s82,s3,180224\n"
"ds_read_b128 a[100:103],v114 offset:25600\n"
"v_mov_b32 v71,0x3f80\n"
"v_mfma_f32_16x16x32_bf16 v[200:203],a[104:107],v[88:91],v[200:203]\n"
"buffer_load_dwordx4 v[160:163],v112,s[24:27],s82 offen offset:0 sc1\n"
"v_cmp_eq_u32 vcc,0,v70\n"
"v_mov_b32 v68,0\n"
"v_mov_b32 v65,0\n"
"v_cndmask_b32 v66,0,v71,vcc\n"
"v_cmp_eq_u32 vcc,1,v70\n"
"v_mfma_f32_16x16x32_bf16 v[232:235],a[104:107],v[92:95],v[232:235]\n"
"ds_read_b128 a[104:107],v114 offset:26624\n"
"buffer_load_dwordx4 v[164:167],v112,s[24:27],s82 offen offset:1024 sc1\n"
"v_mov_b32 v72,0x3f800000\n"
"v_cndmask_b32 v66,v66,v72,vcc\n"
"v_cmp_eq_u32 vcc,2,v70\n"
"v_mfma_f32_16x16x32_bf16 v[204:207],a[108:111],v[88:91],v[204:207]\n"
"buffer_load_dwordx4 v[168:171],v112,s[24:27],s82 offen offset:2048 sc1\n"
"v_cndmask_b32 v67,0,v71,vcc\n"
"v_cmp_eq_u32 vcc,3,v70\n"
"v_mfma_f32_16x16x32_bf16 v[236:239],a[108:111],v[92:95],v[236:239]\n"
"ds_read_b128 a[108:111],v114 offset:27648\n"
"buffer_load_dwordx4 v[172:175],v112,s[24:27],s82 offen offset:3072 sc1\n"
"v_mfma_f32_16x16x32_bf16 v[208:211],a[112:115],v[88:91],v[208:211]\n"
"v_cndmask_b32 v67,v67,v72,vcc\n"
"s_sub_u32 s82,s3,196608\n"
"v_mfma_f32_16x16x32_bf16 v[240:243],a[112:115],v[92:95],v[240:243]\n"
"ds_read_b128 a[112:115],v114 offset:28672\n"
"buffer_load_dwordx4 v[176:179],v112,s[24:27],s82 offen offset:0 sc1\n"
"v_mfma_f32_16x16x32_bf16 v[212:215],a[116:119],v[88:91],v[212:215]\n"
"buffer_load_dwordx4 v[180:183],v112,s[24:27],s82 offen offset:1024 sc1\n"
"v_mfma_f32_16x16x32_bf16 v[244:247],a[116:119],v[92:95],v[244:247]\n"
"ds_read_b128 a[116:119],v114 offset:29696\n"
"buffer_load_dwordx4 v[184:187],v112,s[24:27],s82 offen offset:2048 sc1\n"
"v_mfma_f32_16x16x32_bf16 v[216:219],a[120:123],v[88:91],v[216:219]\n"
"buffer_load_dwordx4 v[188:191],v112,s[24:27],s82 offen offset:3072 sc1\n"
"v_mfma_f32_16x16x32_bf16 v[248:251],a[120:123],v[92:95],v[248:251]\n"
"ds_read_b128 a[120:123],v114 offset:30720\n"
"v_mfma_f32_16x16x32_bf16 v[220:223],a[124:127],v[88:91],v[220:223]\n"
"v_mfma_f32_16x16x32_bf16 v[252:255],a[124:127],v[92:95],v[252:255]\n"
"ds_read_b128 a[124:127],v114 offset:31744\n"
"s_waitcnt lgkmcnt(13)\n"
"v_cmp_neq_f32 vcc,v98,v104\n"
"s_nop 0\n"
"s_cbranch_vccnz .Lfk_ms_a0\n"
".Lfk_mr_a0:\n"
"v_mfma_f32_16x16x32_bf16 v[192:195],v[66:69],a[64:67],v[192:195]\n"
"v_mfma_f32_16x16x32_bf16 v[196:199],v[64:67],a[64:67],v[196:199]\n"
"v_mfma_f32_16x16x32_bf16 v[200:203],v[66:69],a[68:71],v[200:203]\n"
"v_mfma_f32_16x16x32_bf16 v[204:207],v[64:67],a[68:71],v[204:207]\n"
"v_mfma_f32_16x16x32_bf16 v[208:211],v[66:69],a[72:75],v[208:211]\n"
"v_mfma_f32_16x16x32_bf16 v[212:215],v[64:67],a[72:75],v[212:215]\n"
"v_mfma_f32_16x16x32_bf16 v[216:219],v[66:69],a[76:79],v[216:219]\n"
"v_mfma_f32_16x16x32_bf16 v[220:223],v[64:67],a[76:79],v[220:223]\n"
"s_waitcnt lgkmcnt(0)\n"
"v_mfma_f32_16x16x32_bf16 a[140:143],a[144:147],v[76:79],a[140:143]\n"
"v_mfma_f32_16x16x32_bf16 v[224:227],a[96:99],v[76:79],v[224:227]\n"
"v_mfma_f32_16x16x32_bf16 v[228:231],a[100:103],v[76:79],v[228:231]\n"
"v_accvgpr_read_b32 v122,a136\n"
"v_add_f32 v122,v122,v99\n"
"v_mfma_f32_16x16x32_bf16 v[232:235],a[104:107],v[76:79],v[232:235]\n"
"v_rcp_f32 v122,v122\n"
"v_mfma_f32_16x16x32_bf16 v[236:239],a[108:111],v[76:79],v[236:239]\n"
"v_pk_mul_f32 v[192:193],v[192:193],v[122:123] op_sel_hi:[1,0]\n"
"v_pk_mul_f32 v[194:195],v[194:195],v[122:123] op_sel_hi:[1,0]\n"
"v_pk_mul_f32 v[196:197],v[196:197],v[122:123] op_sel_hi:[1,0]\n"
"v_pk_mul_f32 v[198:199],v[198:199],v[122:123] op_sel_hi:[1,0]\n"
"v_mfma_f32_16x16x32_bf16 v[240:243],a[112:115],v[76:79],v[240:243]\n"
"v_cvt_pk_bf16_f32 v192,v192,v193\n"
"v_cvt_pk_bf16_f32 v193,v194,v195\n"
"v_mfma_f32_16x16x32_bf16 v[244:247],a[116:119],v[76:79],v[244:247]\n"
"v_cvt_pk_bf16_f32 v194,v196,v197\n"
"v_cvt_pk_bf16_f32 v195,v198,v199\n"
"v_mfma_f32_16x16x32_bf16 v[248:251],a[120:123],v[76:79],v[248:251]\n"
"buffer_store_dwordx4 v[192:195],v112,s[24:27],s70 offen offset:0 sc1\n"
"v_pk_mul_f32 v[200:201],v[200:201],v[122:123] op_sel_hi:[1,0]\n"
"v_pk_mul_f32 v[202:203],v[202:203],v[122:123] op_sel_hi:[1,0]\n"
"v_pk_mul_f32 v[204:205],v[204:205],v[122:123] op_sel_hi:[1,0]\n"
"v_pk_mul_f32 v[206:207],v[206:207],v[122:123] op_sel_hi:[1,0]\n"
"v_mfma_f32_16x16x32_bf16 v[252:255],a[124:127],v[76:79],v[252:255]\n"
"v_cvt_pk_bf16_f32 v200,v200,v201\n"
"v_cvt_pk_bf16_f32 v201,v202,v203\n"
"v_cmp_neq_f32 vcc,v102,v108\n"
"s_nop 0\n"
"s_cbranch_vccnz .Lfk_ms_a1\n"
".Lfk_mr_a1:\n"
"v_mfma_f32_16x16x32_bf16 v[224:227],v[66:69],a[80:83],v[224:227]\n"
"v_cvt_pk_bf16_f32 v202,v204,v205\n"
"v_cvt_pk_bf16_f32 v203,v206,v207\n"
"v_mfma_f32_16x16x32_bf16 v[228:231],v[64:67],a[80:83],v[228:231]\n"
"buffer_store_dwordx4 v[200:203],v112,s[24:27],s70 offen offset:1024 sc1\n"
"v_pk_mul_f32 v[208:209],v[208:209],v[122:123] op_sel_hi:[1,0]\n"
"v_pk_mul_f32 v[210:211],v[210:211],v[122:123] op_sel_hi:[1,0]\n"
"v_pk_mul_f32 v[212:213],v[212:213],v[122:123] op_sel_hi:[1,0]\n"
"v_pk_mul_f32 v[214:215],v[214:215],v[122:123] op_sel_hi:[1,0]\n"
"v_mfma_f32_16x16x32_bf16 v[232:235],v[66:69],a[84:87],v[232:235]\n"
"v_cvt_pk_bf16_f32 v208,v208,v209\n"
"v_cvt_pk_bf16_f32 v209,v210,v211\n"
"v_mfma_f32_16x16x32_bf16 v[236:239],v[64:67],a[84:87],v[236:239]\n"
"v_cvt_pk_bf16_f32 v210,v212,v213\n"
"v_cvt_pk_bf16_f32 v211,v214,v215\n"
"v_mfma_f32_16x16x32_bf16 v[240:243],v[66:69],a[88:91],v[240:243]\n"
"buffer_store_dwordx4 v[208:211],v112,s[24:27],s70 offen offset:2048 sc1\n"
"v_pk_mul_f32 v[216:217],v[216:217],v[122:123] op_sel_hi:[1,0]\n"
"v_pk_mul_f32 v[218:219],v[218:219],v[122:123] op_sel_hi:[1,0]\n"
"v_pk_mul_f32 v[220:221],v[220:221],v[122:123] op_sel_hi:[1,0]\n"
"v_pk_mul_f32 v[222:223],v[222:223],v[122:123] op_sel_hi:[1,0]\n"
"v_mfma_f32_16x16x32_bf16 v[244:247],v[64:67],a[88:91],v[244:247]\n"
"v_cvt_pk_bf16_f32 v216,v216,v217\n"
"v_cvt_pk_bf16_f32 v217,v218,v219\n"
"v_mfma_f32_16x16x32_bf16 v[248:251],v[66:69],a[92:95],v[248:251]\n"
"v_cvt_pk_bf16_f32 v218,v220,v221\n"
"v_cvt_pk_bf16_f32 v219,v222,v223\n"
"v_mfma_f32_16x16x32_bf16 v[252:255],v[64:67],a[92:95],v[252:255]\n"
"buffer_store_dwordx4 v[216:219],v112,s[24:27],s70 offen offset:3072 sc1\n"
"s_add_u32 s70,s70,262144\n"
"s_branch .Lfk_p1\n"
".Lfk_look:\n"
"s_lshr_b64 s[28:29],s[14:15],s21\n"
"s_and_b32 s83,s28,s19\n"
"s_cbranch_scc0 .Lfk_lk1\n"
"s_mov_b32 s84,0x40000\n"
"s_waitcnt vmcnt(10)\n"
".Lfk_lk0:\n"
"v_xor_b32 v119,s80,v122\n"
"v_xor_b32 v120,s81,v123\n"
"v_or_b32 v119,v119,v120\n"
"v_cmp_ne_u32 s[14:15],0,v119\n"
"s_nop 0\n"
"s_lshr_b64 s[28:29],s[14:15],s21\n"
"s_and_b32 s83,s28,s19\n"
"s_cbranch_scc0 .Lfk_lk1\n"
"s_sleep 2\n"
"buffer_load_dwordx2 v[122:123],v115,s[24:27],s11 offen sc1\n"
"s_waitcnt vmcnt(0)\n"
"s_sub_u32 s84,s84,1\n"
"s_cbranch_scc0 .Lfk_lk0\n"
".Lfk_lk1:\n"
"s_andn2_b32 s28,s28,s19\n"
"s_lshl_b64 s[14:15],s[28:29],s21\n"
"s_lshl_b32 s83,s19,8\n"
"s_and_b32 s83,s28,s83\n"
"s_cbranch_scc0 .Lfk_lk2\n"
"buffer_load_dwordx2 v[122:123],v115,s[24:27],s11 offen sc1\n"
".Lfk_lk2:\n"
"s_nop 1\n"
"s_setpc_b64 s[94:95]\n"
".Lfk_meet4:\n"
"s_mov_b32 s84,0x1000000\n"
".Lfk_meetl4:\n"
"ds_read_b32 v125,v114\n"
"s_sub_u32 s84,s84,1\n"
"s_cbranch_scc1 .Lfk_meetx4\n"
"s_waitcnt lgkmcnt(0)\n"
"v_cmp_gt_u32 vcc,s16,v125\n"
"s_nop 0\n"
"s_cbranch_vccnz .Lfk_meetl4\n"
".Lfk_meetx4:\n"
"s_setpc_b64 s[94:95]\n"
".Lfk_es_lkone:\n"
"s_call_b64 s[94:95],.Lfk_look\n"
"s_branch .Lfk_er_lkone\n"
".Lfk_z0:\n"
"s_call_b64 s[94:95],.Lfk_meet4\n"
"v_xor_b32 v119,s80,v104\n"
"v_xor_b32 v120,s81,v105\n"
"v_or_b32 v119,v119,v120\n"
"v_xor_b32 v120,s80,v106\n"
"v_or_b32 v119,v119,v120\n"
"v_xor_b32 v120,s81,v107\n"
"v_or_b32 v119,v119,v120\n"
"v_xor_b32 v120,s80,v108\n"
"v_or_b32 v119,v119,v120\n"
"v_xor_b32 v120,s81,v109\n"
"v_or_b32 v119,v119,v120\n"
"v_xor_b32 v120,s80,v110\n"
"v_or_b32 v119,v119,v120\n"
"v_xor_b32 v120,s81,v111\n"
"v_or_b32 v119,v119,v120\n"
"v_cmp_ne_u32 vcc,0,v119\n"
"s_nop 0\n"
"s_cbranch_vccz .Lfk_z1\n"
"s_mov_b32 s84,0x40000\n"
"s_mov_b32 s83,148946944\n"
"buffer_load_dwordx4 v[104:107],v112,s[24:27],s83 offen offset:0 sc1\n"
"buffer_load_dwordx4 v[108:111],v112,s[24:27],s83 offen offset:1024 sc1\n"
".Lfk_w0:\n"
"s_waitcnt vmcnt(0)\n"
"v_xor_b32 v119,s80,v104\n"
"v_xor_b32 v120,s81,v105\n"
"v_or_b32 v119,v119,v120\n"
"v_xor_b32 v120,s80,v106\n"
"v_or_b32 v119,v119,v120\n"
"v_xor_b32 v120,s81,v107\n"
"v_or_b32 v119,v119,v120\n"
"v_xor_b32 v120,s80,v108\n"
"v_or_b32 v119,v119,v120\n"
"v_xor_b32 v120,s81,v109\n"
"v_or_b32 v119,v119,v120\n"
"v_xor_b32 v120,s80,v110\n"
"v_or_b32 v119,v119,v120\n"
"v_xor_b32 v120,s81,v111\n"
"v_or_b32 v119,v119,v120\n"
"v_cmp_ne_u32 vcc,0,v119\n"
"s_nop 0\n"
"s_cbranch_vccz .Lfk_w0_x\n"
"s_sleep 8\n"
"s_mov_b64 exec,vcc\n"
"s_mov_b32 s83,148946944\n"
"buffer_load_dwordx4 v[104:107],v112,s[24:27],s83 offen offset:0 sc1\n"
"buffer_load_dwordx4 v[108:111],v112,s[24:27],s83 offen offset:1024 sc1\n"
"s_mov_b64 exec,-1\n"
"s_sub_u32 s84,s84,1\n"
"s_cbranch_scc0 .Lfk_w0\n"
".Lfk_w0_x:\n"
"s_nop 1\n"
"s_branch .Lfk_z1\n"
".Lfk_es_lkonem:\n"
"s_call_b64 s[94:95],.Lfk_look\n"
"s_branch .Lfk_er_lkonem\n"
".Lfk_es_mone:\n"
"s_call_b64 s[94:95],.Lfk_meet4\n"
"s_branch .Lfk_er_mone\n"
".Lfk_es_lktwom:\n"
"s_call_b64 s[94:95],.Lfk_look\n"
"s_branch .Lfk_er_lktwom\n"
".Lfk_es_mtwo:\n"
"s_call_b64 s[94:95],.Lfk_meet4\n"
"s_branch .Lfk_er_mtwo\n"
".Lfk_es_lkloopm:\n"
"s_call_b64 s[94:95],.Lfk_look\n"
"s_branch .Lfk_er_lkloopm\n"
".Lfk_es_me3:\n"
"s_call_b64 s[94:95],.Lfk_meet4\n"
"s_branch .Lfk_er_me3\n"
".Lfk_es_me2:\n"
"s_call_b64 s[94:95],.Lfk_meet4\n"
"s_branch .Lfk_er_me2\n"
".Lfk_p1:\n"
"s_cmp_eq_u32 s18,2146435072\n"
"s_cbranch_scc1 .Lfk_p2\n"
".Lfk_p3:\n"
"s_waitcnt vmcnt(24) lgkmcnt(0)\n"
"s_barrier\n"
"ds_read_b128 v[192:195],v112 offset:0\n"
"ds_read_b128 v[196:199],v112 offset:1024\n"
"ds_read_b128 v[200:203],v112 offset:2048\n"
"ds_read_b128 v[204:207],v112 offset:3072\n"
"ds_read_b128 v[208:211],v112 offset:4096\n"
"ds_read_b128 v[212:215],v112 offset:5120\n"
"ds_read_b128 v[216:219],v112 offset:6144\n"
"ds_read_b128 v[220:223],v112 offset:7168\n"
"s_lshr_b32 s22,s8,2\n"
"s_and_b32 s31,s8,0xfff1ffff\n"
"s_add_u32 s18,s18,16384\n"
"s_lshr_b32 s91,s78,2\n"
"s_add_u32 s91,s91,131072\n"
"v_add_u32 v116,s91,v112\n"
"ds_read_b128 a[68:71],v116 offset:4096\n"
"ds_read_b128 a[72:75],v116 offset:8192\n"
"ds_read_b128 a[76:79],v116 offset:12288\n"
"ds_read_b128 a[80:83],v116 offset:0\n"
"ds_read_b128 a[84:87],v116 offset:20480\n"
"ds_read_b128 a[88:91],v116 offset:24576\n"
"ds_read_b128 a[92:95],v116 offset:28672\n"
"v_accvgpr_read_b32 v122,a140\n"
"v_add_f32 v122,v122,v103\n"
"v_rcp_f32 v122,v122\n"
"s_nop 0\n"
"v_pk_mul_f32 v[224:225],v[224:225],v[122:123] op_sel_hi:[1,0]\n"
"v_pk_mul_f32 v[226:227],v[226:227],v[122:123] op_sel_hi:[1,0]\n"
"v_pk_mul_f32 v[228:229],v[228:229],v[122:123] op_sel_hi:[1,0]\n"
"v_pk_mul_f32 v[230:231],v[230:231],v[122:123] op_sel_hi:[1,0]\n"
"v_cvt_pk_bf16_f32 v224,v224,v225\n"
"v_cvt_pk_bf16_f32 v225,v226,v227\n"
"v_cvt_pk_bf16_f32 v226,v228,v229\n"
"v_cvt_pk_bf16_f32 v227,v230,v231\n"
"buffer_store_dwordx4 v[224:227],v112,s[24:27],s70 offen offset:0 sc1\n"
"s_waitcnt lgkmcnt(11)\n"
"v_mfma_f32_16x16x32_bf16 v[80:83],a[224:227],v[192:195],0\n"
"ds_read_b128 v[224:227],v112 offset:8192\n"
"ds_read_b128 v[228:231],v112 offset:9216\n"
"v_mfma_f32_16x16x32_bf16 v[80:83],a[232:235],v[196:199],v[80:83]\n"
"v_pk_mul_f32 v[232:233],v[232:233],v[122:123] op_sel_hi:[1,0]\n"
"v_pk_mul_f32 v[234:235],v[234:235],v[122:123] op_sel_hi:[1,0]\n"
"v_pk_mul_f32 v[236:237],v[236:237],v[122:123] op_sel_hi:[1,0]\n"
"v_pk_mul_f32 v[238:239],v[238:239],v[122:123] op_sel_hi:[1,0]\n"
"v_mfma_f32_16x16x32_bf16 v[80:83],a[240:243],v[200:203],v[80:83]\n"
"v_cvt_pk_bf16_f32 v232,v232,v233\n"
"v_cvt_pk_bf16_f32 v233,v234,v235\n"
"v_cvt_pk_bf16_f32 v234,v236,v237\n"
"v_cvt_pk_bf16_f32 v235,v238,v239\n"
"v_mfma_f32_16x16x32_bf16 v[80:83],a[248:251],v[204:207],v[80:83]\n"
"buffer_store_dwordx4 v[232:235],v112,s[24:27],s70 offen offset:1024 sc1\n"
"v_mfma_f32_16x16x32_bf16 v[84:87],a[252:255],v[204:207],0\n"
"s_nop 0\n"
"ds_read_b128 v[232:235],v112 offset:10240\n"
"ds_read_b128 v[236:239],v112 offset:11264\n"
"v_pk_mul_f32 v[240:241],v[240:241],v[122:123] op_sel_hi:[1,0]\n"
"v_pk_mul_f32 v[242:243],v[242:243],v[122:123] op_sel_hi:[1,0]\n"
"v_mfma_f32_16x16x32_bf16 v[84:87],a[244:247],v[200:203],v[84:87]\n"
"v_cvt_pk_bf16_f32 v240,v240,v241\n"
"v_cvt_pk_bf16_f32 v241,v242,v243\n"
"v_pk_mul_f32 v[244:245],v[244:245],v[122:123] op_sel_hi:[1,0]\n"
"v_pk_mul_f32 v[246:247],v[246:247],v[122:123] op_sel_hi:[1,0]\n"
"v_mfma_f32_16x16x32_bf16 v[84:87],a[236:239],v[196:199],v[84:87]\n"
"v_cvt_pk_bf16_f32 v242,v244,v245\n"
"v_cvt_pk_bf16_f32 v243,v246,v247\n"
"v_mfma_f32_16x16x32_bf16 v[84:87],a[228:231],v[192:195],v[84:87]\n"
"buffer_store_dwordx4 v[240:243],v112,s[24:27],s70 offen offset:2048 sc1\n"
"s_nop 1\n"
"ds_read_b128 v[240:243],v112 offset:12288\n"
"ds_read_b128 v[244:247],v112 offset:13312\n"
"s_waitcnt lgkmcnt(13)\n"
"v_mfma_f32_16x16x32_bf16 v[84:87],a[4:7],v[208:211],v[84:87]\n"
"v_pk_mul_f32 v[248:249],v[248:249],v[122:123] op_sel_hi:[1,0]\n"
"v_pk_mul_f32 v[250:251],v[250:251],v[122:123] op_sel_hi:[1,0]\n"
"v_pk_mul_f32 v[252:253],v[252:253],v[122:123] op_sel_hi:[1,0]\n"
"v_pk_mul_f32 v[254:255],v[254:255],v[122:123] op_sel_hi:[1,0]\n"
"v_mfma_f32_16x16x32_bf16 v[84:87],a[12:15],v[212:215],v[84:87]\n"
"v_cvt_pk_bf16_f32 v248,v248,v249\n"
"v_cvt_pk_bf16_f32 v249,v250,v251\n"
"v_mfma_f32_16x16x32_bf16 v[84:87],a[20:23],v[216:219],v[84:87]\n"
"v_cvt_pk_bf16_f32 v250,v252,v253\n"
"v_cvt_pk_bf16_f32 v251,v254,v255\n"
"buffer_store_dwordx4 v[248:251],v112,s[24:27],s70 offen offset:3072 sc1\n"
"v_mfma_f32_16x16x32_bf16 v[84:87],a[28:31],v[220:223],v[84:87]\n"
"s_nop 0\n"
"ds_read_b128 v[248:251],v112 offset:14336\n"
"s_add_u32 s70,s70,262144\n"
"v_mfma_f32_16x16x32_bf16 v[80:83],a[24:27],v[220:223],v[80:83]\n"
"ds_read_b128 v[252:255],v112 offset:15360\n"
"v_mfma_f32_16x16x32_bf16 v[80:83],a[16:19],v[216:219],v[80:83]\n"
"ds_read_b128 v[204:207],v112 offset:19456\n"
"s_mov_b32 s83,148946944\n"
"v_mfma_f32_16x16x32_bf16 v[80:83],a[8:11],v[212:215],v[80:83]\n"
"buffer_load_dwordx4 v[104:107],v112,s[24:27],s83 offen offset:0 sc1\n"
"buffer_load_dwordx4 v[108:111],v112,s[24:27],s83 offen offset:1024 sc1\n"
"v_mfma_f32_16x16x32_bf16 v[80:83],a[0:3],v[208:211],v[80:83]\n"
"ds_read_b128 v[200:203],v112 offset:18432\n"
"s_waitcnt lgkmcnt(6)\n"
"v_mfma_f32_16x16x32_bf16 v[80:83],v[32:35],v[224:227],v[80:83]\n"
"s_add_u32 m0,s30,65536\n"
"ds_read_b128 v[196:199],v112 offset:17408\n"
"buffer_load_dwordx4 v112,s[24:27],s18 offen offset:0 sc1 lds\n"
"v_mfma_f32_16x16x32_bf16 v[80:83],v[40:43],v[228:231],v[80:83]\n"
"ds_read_b128 v[192:195],v112 offset:16384\n"
"v_mfma_f32_16x16x32_bf16 v[80:83],a[32:35],v[232:235],v[80:83]\n"
"ds_read_b128 v[220:223],v112 offset:23552\n"
"ds_read_b128 v[216:219],v112 offset:22528\n"
"v_mfma_f32_16x16x32_bf16 v[80:83],a[40:43],v[236:239],v[80:83]\n"
"buffer_load_dwordx4 v112,s[24:27],s18 offen offset:1024 sc1 lds\n"
"v_mfma_f32_16x16x32_bf16 v[84:87],a[44:47],v[236:239],v[84:87]\n"
"ds_read_b128 v[212:215],v112 offset:21504\n"
"v_mfma_f32_16x16x32_bf16 v[84:87],a[36:39],v[232:235],v[84:87]\n"
"ds_read_b128 v[208:211],v112 offset:20480\n"
"ds_read_b128 v[236:239],v112 offset:27648\n"
"v_mfma_f32_16x16x32_bf16 v[84:87],v[44:47],v[228:231],v[84:87]\n"
"buffer_load_dwordx4 v112,s[24:27],s18 offen offset:2048 sc1 lds\n"
"v_mfma_f32_16x16x32_bf16 v[84:87],v[36:39],v[224:227],v[84:87]\n"
"ds_read_b128 v[232:235],v112 offset:26624\n"
"s_waitcnt lgkmcnt(10)\n"
"v_mfma_f32_16x16x32_bf16 v[84:87],a[68:71],v[240:243],v[84:87]\n"
"ds_read_b128 v[228:231],v112 offset:25600\n"
"ds_read_b128 v[224:227],v112 offset:24576\n"
"v_mfma_f32_16x16x32_bf16 v[84:87],a[76:79],v[244:247],v[84:87]\n"
"buffer_load_dwordx4 v112,s[24:27],s18 offen offset:3072 sc1 lds\n"
"v_mfma_f32_16x16x32_bf16 v[84:87],a[84:87],v[248:251],v[84:87]\n"
"s_lshl_b32 s91,s78,3\n"
"s_add_u32 s91,s91,131072\n"
"v_mfma_f32_16x16x32_bf16 v[84:87],a[92:95],v[252:255],v[84:87]\n"
"v_lshrrev_b32 v101,2,v112\n"
"v_add_u32 v101,s91,v101\n"
"v_mfma_f32_16x16x32_bf16 v[80:83],a[88:91],v[252:255],v[80:83]\n"
"s_add_u32 m0,m0,4096\n"
"ds_read_b128 v[252:255],v112 offset:31744\n"
"buffer_load_dwordx4 v117,s[24:27],s18 offen offset:0 sc1 lds\n"
"v_mfma_f32_16x16x32_bf16 v[80:83],a[80:83],v[248:251],v[80:83]\n"
"ds_read_b128 v[248:251],v112 offset:30720\n"
"v_lshrrev_b32 v114,2,v112\n"
"v_mfma_f32_16x16x32_bf16 v[80:83],a[72:75],v[244:247],v[80:83]\n"
"ds_read_b128 v[244:247],v112 offset:29696\n"
"v_add_u32 v114,131328,v114\n"
"v_mfma_f32_16x16x32_bf16 v[80:83],a[156:159],v[240:243],v[80:83]\n"
"ds_read_b128 v[240:243],v112 offset:28672\n"
"buffer_load_dwordx4 v117,s[24:27],s18 offen offset:1024 sc1 lds\n"
"s_waitcnt lgkmcnt(12)\n"
"v_mov_b32 v116,0\n"
"v_mfma_f32_16x16x32_bf16 v[64:67],a[224:227],v[192:195],0\n"
"ds_write_b32 v101,v116 offset:256\n"
"v_mfma_f32_16x16x32_bf16 v[64:67],a[232:235],v[196:199],v[64:67]\n"
"v_mov_b32 v116,1\n"
"s_mov_b32 s16,4\n"
"v_mfma_f32_16x16x32_bf16 v[64:67],a[240:243],v[200:203],v[64:67]\n"
"buffer_load_dwordx4 v117,s[24:27],s18 offen offset:2048 sc1 lds\n"
"v_mfma_f32_16x16x32_bf16 v[64:67],a[248:251],v[204:207],v[64:67]\n"
"v_add_u32 v113,65536,v112\n"
"s_add_u32 s31,s31,8388608\n"
"v_mfma_f32_16x16x32_bf16 v[68:71],a[252:255],v[204:207],0\n"
"ds_read_b128 v[204:207],v112 offset:35840\n"
"s_add_u32 s53,s22,786432\n"
"v_mfma_f32_16x16x32_bf16 v[68:71],a[244:247],v[200:203],v[68:71]\n"
"ds_read_b128 v[200:203],v112 offset:34816\n"
"buffer_load_dwordx4 v117,s[24:27],s18 offen offset:3072 sc1 lds\n"
"v_mfma_f32_16x16x32_bf16 v[68:71],a[236:239],v[196:199],v[68:71]\n"
"ds_read_b128 v[196:199],v112 offset:33792\n"
"s_add_u32 s56,s22,1048576\n"
"v_mfma_f32_16x16x32_bf16 v[68:71],a[228:231],v[192:195],v[68:71]\n"
"ds_read_b128 v[192:195],v112 offset:32768\n"
"s_waitcnt lgkmcnt(13)\n"
"v_mfma_f32_16x16x32_bf16 v[68:71],a[4:7],v[208:211],v[68:71]\n"
"s_sub_u32 s82,s3,212992\n"
"buffer_load_dwordx4 a[48:51],v112,s[24:27],s82 offen offset:0 sc1\n"
"v_mfma_f32_16x16x32_bf16 v[68:71],a[12:15],v[212:215],v[68:71]\n"
"s_add_u32 s58,s22,1310720\n"
"s_add_u32 s59,s22,1572864\n"
"v_mfma_f32_16x16x32_bf16 v[68:71],a[20:23],v[216:219],v[68:71]\n"
"s_bfe_u32 s10,s3,0x50012\n"
"s_lshl_b32 s10,s10,8\n"
"v_mfma_f32_16x16x32_bf16 v[68:71],a[28:31],v[220:223],v[68:71]\n"
"buffer_load_dwordx4 a[52:55],v112,s[24:27],s82 offen offset:1024 sc1\n"
"v_mfma_f32_16x16x32_bf16 v[64:67],a[24:27],v[220:223],v[64:67]\n"
"s_add_u32 s10,s10,149061632\n"
"ds_read_b128 v[220:223],v112 offset:39936\n"
"s_mov_b32 s14,0\n"
"v_mfma_f32_16x16x32_bf16 v[64:67],a[16:19],v[216:219],v[64:67]\n"
"ds_read_b128 v[216:219],v112 offset:38912\n"
"v_not_b32 v122,s80\n"
"v_mfma_f32_16x16x32_bf16 v[64:67],a[8:11],v[212:215],v[64:67]\n"
"ds_read_b128 v[212:215],v112 offset:37888\n"
"buffer_load_dwordx4 a[56:59],v112,s[24:27],s82 offen offset:2048 sc1\n"
"v_mfma_f32_16x16x32_bf16 v[64:67],a[0:3],v[208:211],v[64:67]\n"
"s_cmp_eq_u32 s52,0\n"
"ds_read_b128 v[208:211],v112 offset:36864\n"
"s_waitcnt lgkmcnt(13)\n"
"v_mfma_f32_16x16x32_bf16 v[64:67],v[32:35],v[224:227],v[64:67]\n"
"s_cselect_b64 s[14:15],s[14:15],0\n"
"s_cselect_b32 s10,s10,2146435072\n"
"s_cmp_lg_u32 s15,0\n"
"s_cselect_b32 s91,s11,2146435072\n"
"v_mfma_f32_16x16x32_bf16 v[64:67],v[40:43],v[228:231],v[64:67]\n"
"buffer_load_dwordx4 a[60:63],v112,s[24:27],s82 offen offset:3072 sc1\n"
"buffer_load_dwordx2 v[122:123],v115,s[24:27],s91 offen sc1\n"
"v_mfma_f32_16x16x32_bf16 v[64:67],a[32:35],v[232:235],v[64:67]\n"
"s_mov_b32 s89,0\n"
"s_mov_b32 s20,32768\n"
"v_mfma_f32_16x16x32_bf16 v[64:67],a[40:43],v[236:239],v[64:67]\n"
"s_mov_b32 s19,0xffff\n"
"s_mov_b32 s21,32\n"
"v_mfma_f32_16x16x32_bf16 v[68:71],a[44:47],v[236:239],v[68:71]\n"
"s_sub_u32 s82,s3,229376\n"
"ds_read_b128 v[236:239],v112 offset:44032\n"
"buffer_load_dwordx4 v[48:51],v112,s[24:27],s82 offen offset:0 sc1\n"
"v_mfma_f32_16x16x32_bf16 v[68:71],a[36:39],v[232:235],v[68:71]\n"
"ds_read_b128 v[232:235],v112 offset:43008\n"
"v_mfma_f32_16x16x32_bf16 v[68:71],v[44:47],v[228:231],v[68:71]\n"
"ds_read_b128 v[228:231],v112 offset:41984\n"
"v_mfma_f32_16x16x32_bf16 v[68:71],v[36:39],v[224:227],v[68:71]\n"
"ds_read_b128 v[224:227],v112 offset:40960\n"
"buffer_load_dwordx4 v[52:55],v112,s[24:27],s82 offen offset:1024 sc1\n"
"s_waitcnt lgkmcnt(13)\n"
"s_waitcnt vmcnt(17) lgkmcnt(12)\n"
"s_barrier\n"
"v_mfma_f32_16x16x32_bf16 v[68:71],a[68:71],v[240:243],v[68:71]\n"
"buffer_store_dwordx2 a[154:155],off,s[24:27],s71 sc1\n"
"v_mfma_f32_16x16x32_bf16 v[68:71],a[76:79],v[244:247],v[68:71]\n"
"v_mfma_f32_16x16x32_bf16 v[68:71],a[84:87],v[248:251],v[68:71]\n"
"buffer_load_dwordx4 v[56:59],v112,s[24:27],s82 offen offset:2048 sc1\n"
"v_mfma_f32_16x16x32_bf16 v[68:71],a[92:95],v[252:255],v[68:71]\n"
"v_mfma_f32_16x16x32_bf16 v[64:67],a[88:91],v[252:255],v[64:67]\n"
"ds_read_b128 v[252:255],v112 offset:48128\n"
"v_mfma_f32_16x16x32_bf16 v[64:67],a[80:83],v[248:251],v[64:67]\n"
"ds_read_b128 v[248:251],v112 offset:47104\n"
"buffer_load_dwordx4 v[60:63],v112,s[24:27],s82 offen offset:3072 sc1\n"
"v_mfma_f32_16x16x32_bf16 v[64:67],a[72:75],v[244:247],v[64:67]\n"
"ds_read_b128 v[244:247],v112 offset:46080\n"
"v_mfma_f32_16x16x32_bf16 v[64:67],a[156:159],v[240:243],v[64:67]\n"
"ds_read_b128 v[240:243],v112 offset:45056\n"
"s_waitcnt lgkmcnt(12)\n"
"v_mfma_f32_16x16x32_bf16 v[72:75],a[224:227],v[192:195],0\n"
"s_sub_u32 s82,s3,245760\n"
"v_mfma_f32_16x16x32_bf16 v[72:75],a[232:235],v[196:199],v[72:75]\n"
"buffer_load_dwordx4 v[16:19],v112,s[24:27],s82 offen offset:0 sc1\n"
"v_mfma_f32_16x16x32_bf16 v[72:75],a[240:243],v[200:203],v[72:75]\n"
"v_mfma_f32_16x16x32_bf16 v[72:75],a[248:251],v[204:207],v[72:75]\n"
"buffer_load_dwordx4 v[20:23],v112,s[24:27],s82 offen offset:1024 sc1\n"
"v_mfma_f32_16x16x32_bf16 v[76:79],a[252:255],v[204:207],0\n"
"ds_read_b128 v[204:207],v112 offset:52224\n"
"v_mfma_f32_16x16x32_bf16 v[76:79],a[244:247],v[200:203],v[76:79]\n"
"ds_read_b128 v[200:203],v112 offset:51200\n"
"v_mfma_f32_16x16x32_bf16 v[76:79],a[236:239],v[196:199],v[76:79]\n"
"ds_read_b128 v[196:199],v112 offset:50176\n"
"buffer_load_dwordx4 v[24:27],v112,s[24:27],s82 offen offset:2048 sc1\n"
"v_mfma_f32_16x16x32_bf16 v[76:79],a[228:231],v[192:195],v[76:79]\n"
"ds_read_b128 v[192:195],v112 offset:49152\n"
"s_waitcnt lgkmcnt(12)\n"
"v_mfma_f32_16x16x32_bf16 v[76:79],a[4:7],v[208:211],v[76:79]\n"
"v_mfma_f32_16x16x32_bf16 v[76:79],a[12:15],v[212:215],v[76:79]\n"
"buffer_load_dwordx4 v[28:31],v112,s[24:27],s82 offen offset:3072 sc1\n"
"v_mfma_f32_16x16x32_bf16 v[76:79],a[20:23],v[216:219],v[76:79]\n"
"v_mfma_f32_16x16x32_bf16 v[76:79],a[28:31],v[220:223],v[76:79]\n"
"s_waitcnt vmcnt(22)\n"
"v_xor_b32 v120,s81,v105\n"
"s_add_u32 s18,s18,2097152\n"
"v_mfma_f32_16x16x32_bf16 v[72:75],a[24:27],v[220:223],v[72:75]\n"
"s_add_u32 m0,s30,98304\n"
"ds_read_b128 v[220:223],v112 offset:56320\n"
"buffer_load_dwordx4 v112,s[24:27],s18 offen offset:0 sc1 lds\n"
"v_mfma_f32_16x16x32_bf16 v[72:75],a[16:19],v[216:219],v[72:75]\n"
"ds_read_b128 v[216:219],v112 offset:55296\n"
"v_xor_b32 v119,s80,v104\n"
"v_mfma_f32_16x16x32_bf16 v[72:75],a[8:11],v[212:215],v[72:75]\n"
"ds_read_b128 v[212:215],v112 offset:54272\n"
"v_or_b32 v119,v119,v120\n"
"v_mfma_f32_16x16x32_bf16 v[72:75],a[0:3],v[208:211],v[72:75]\n"
"ds_read_b128 v[208:211],v112 offset:53248\n"
"buffer_load_dwordx4 v112,s[24:27],s18 offen offset:1024 sc1 lds\n"
"s_waitcnt lgkmcnt(12)\n"
"v_mfma_f32_16x16x32_bf16 v[72:75],v[32:35],v[224:227],v[72:75]\n"
"v_xor_b32 v120,s80,v106\n"
"v_or_b32 v119,v119,v120\n"
"v_xor_b32 v120,s81,v107\n"
"v_or_b32 v119,v119,v120\n"
"v_mfma_f32_16x16x32_bf16 v[72:75],v[40:43],v[228:231],v[72:75]\n"
"v_xor_b32 v120,s80,v108\n"
"v_or_b32 v119,v119,v120\n"
"v_xor_b32 v120,s81,v109\n"
"v_or_b32 v119,v119,v120\n"
"v_mfma_f32_16x16x32_bf16 v[72:75],a[32:35],v[232:235],v[72:75]\n"
"buffer_load_dwordx4 v112,s[24:27],s18 offen offset:2048 sc1 lds\n"
"v_mfma_f32_16x16x32_bf16 v[72:75],a[40:43],v[236:239],v[72:75]\n"
"v_xor_b32 v120,s80,v110\n"
"v_or_b32 v119,v119,v120\n"
"v_xor_b32 v120,s81,v111\n"
"v_or_b32 v119,v119,v120\n"
"v_mfma_f32_16x16x32_bf16 v[76:79],a[44:47],v[236:239],v[76:79]\n"
"ds_read_b128 v[236:239],v112 offset:60416\n"
"v_cmp_ne_u32 vcc,0,v119\n"
"v_mfma_f32_16x16x32_bf16 v[76:79],a[36:39],v[232:235],v[76:79]\n"
"ds_read_b128 v[232:235],v112 offset:59392\n"
"buffer_load_dwordx4 v112,s[24:27],s18 offen offset:3072 sc1 lds\n"
"v_mfma_f32_16x16x32_bf16 v[76:79],v[44:47],v[228:231],v[76:79]\n"
"ds_read_b128 v[228:231],v112 offset:58368\n"
"v_xor_b32 v119,s80,v122\n"
"v_mfma_f32_16x16x32_bf16 v[76:79],v[36:39],v[224:227],v[76:79]\n"
"s_add_u32 m0,m0,4096\n"
"ds_read_b128 v[224:227],v112 offset:57344\n"
"s_waitcnt lgkmcnt(12)\n"
"v_mfma_f32_16x16x32_bf16 v[76:79],a[68:71],v[240:243],v[76:79]\n"
"buffer_load_dwordx4 v117,s[24:27],s18 offen offset:0 sc1 lds\n"
"v_mfma_f32_16x16x32_bf16 v[76:79],a[76:79],v[244:247],v[76:79]\n"
"v_xor_b32 v120,s81,v123\n"
"v_or_b32 v119,v119,v120\n"
"v_cmp_ne_u32 s[28:29],0,v119\n"
"v_mfma_f32_16x16x32_bf16 v[76:79],a[84:87],v[248:251],v[76:79]\n"
"s_and_b64 s[14:15],s[14:15],s[28:29]\n"
"s_cselect_b32 s91,s11,2146435072\n"
"v_mfma_f32_16x16x32_bf16 v[76:79],a[92:95],v[252:255],v[76:79]\n"
"buffer_load_dwordx4 v117,s[24:27],s18 offen offset:1024 sc1 lds\n"
"v_mfma_f32_16x16x32_bf16 v[72:75],a[88:91],v[252:255],v[72:75]\n"
"ds_read_b128 v[252:255],v112 offset:64512\n"
"buffer_load_dwordx2 v[122:123],v115,s[24:27],s91 offen sc1\n"
"v_mfma_f32_16x16x32_bf16 v[72:75],a[80:83],v[248:251],v[72:75]\n"
"ds_read_b128 v[248:251],v112 offset:63488\n"
"v_mfma_f32_16x16x32_bf16 v[72:75],a[72:75],v[244:247],v[72:75]\n"
"ds_read_b128 v[244:247],v112 offset:62464\n"
"buffer_load_dwordx4 v117,s[24:27],s18 offen offset:2048 sc1 lds\n"
"v_mfma_f32_16x16x32_bf16 v[72:75],a[156:159],v[240:243],v[72:75]\n"
"s_and_b32 s83,s15,0xffff\n"
"ds_read_b128 v[240:243],v112 offset:61440\n"
"s_waitcnt lgkmcnt(12)\n"
"v_mfma_f32_16x16x32_bf16 v[88:91],a[224:227],v[192:195],0\n"
"buffer_load_dwordx4 v117,s[24:27],s18 offen offset:3072 sc1 lds\n"
"v_mfma_f32_16x16x32_bf16 v[88:91],a[232:235],v[196:199],v[88:91]\n"
"s_cbranch_scc1 .Lfk_es_lkone\n"
".Lfk_er_lkone:\n"
"v_mfma_f32_16x16x32_bf16 v[88:91],a[240:243],v[200:203],v[88:91]\n"
"s_waitcnt vmcnt(23)\n"
"ds_add_u32 v114,v116\n"
"v_mfma_f32_16x16x32_bf16 v[88:91],a[248:251],v[204:207],v[88:91]\n"
"v_and_b32 v118,0xf0,v112\n"
"v_lshrrev_b32 v120,8,v112\n"
"v_lshlrev_b32 v120,4,v120\n"
"v_mfma_f32_16x16x32_bf16 v[92:95],a[252:255],v[204:207],0\n"
"v_lshl_add_u32 v118,v118,2,v120\n"
"v_mov_b32 v100,1\n"
"v_mfma_f32_16x16x32_bf16 v[92:95],a[244:247],v[200:203],v[92:95]\n"
"v_add_u32 v118,s12,v118\n"
"v_mov_b32 v102,131076\n"
"v_mfma_f32_16x16x32_bf16 v[92:95],a[236:239],v[196:199],v[92:95]\n"
"v_mfma_f32_16x16x32_bf16 v[92:95],a[228:231],v[192:195],v[92:95]\n"
"s_waitcnt lgkmcnt(9)\n"
"v_mfma_f32_16x16x32_bf16 v[92:95],a[4:7],v[208:211],v[92:95]\n"
"v_mfma_f32_16x16x32_bf16 v[92:95],a[12:15],v[212:215],v[92:95]\n"
"ds_read_b32 v125,v114\n"
"v_mfma_f32_16x16x32_bf16 v[92:95],a[20:23],v[216:219],v[92:95]\n"
"v_mfma_f32_16x16x32_bf16 v[92:95],a[28:31],v[220:223],v[92:95]\n"
"v_mfma_f32_16x16x32_bf16 v[88:91],a[24:27],v[220:223],v[88:91]\n"
"v_mfma_f32_16x16x32_bf16 v[88:91],a[16:19],v[216:219],v[88:91]\n"
"s_waitcnt lgkmcnt(0)\n"
"v_cmp_gt_u32 s[28:29],s16,v125\n"
"v_mfma_f32_16x16x32_bf16 v[88:91],a[8:11],v[212:215],v[88:91]\n"
"s_add_u32 s18,s8,4194304\n"
"s_add_u32 s8,s8,5242880\n"
"s_or_b64 vcc,vcc,s[28:29]\n"
"v_mfma_f32_16x16x32_bf16 v[88:91],a[0:3],v[208:211],v[88:91]\n"
"s_cbranch_vccnz .Lfk_z0\n"
".Lfk_z1:\n"
"ds_read_b128 v[192:195],v113 offset:0\n"
"v_mfma_f32_16x16x32_bf16 v[88:91],v[32:35],v[224:227],v[88:91]\n"
"s_mov_b32 s89,1\n"
"s_mov_b32 s19,0xff\n"
"s_lshl_b64 exec,1,s89\n"
"buffer_atomic_add v100,off,s[24:27],s10 sc0\n"
"s_mov_b64 exec,-1\n"
"v_mfma_f32_16x16x32_bf16 v[88:91],v[40:43],v[228:231],v[88:91]\n"
"ds_read_b128 v[196:199],v113 offset:1024\n"
"s_add_u32 m0,s30,0\n"
"v_mfma_f32_16x16x32_bf16 v[88:91],a[32:35],v[232:235],v[88:91]\n"
"ds_read_b128 v[200:203],v113 offset:2048\n"
"buffer_load_dwordx4 v112,s[24:27],s18 offen offset:0 sc1 lds\n"
"v_mfma_f32_16x16x32_bf16 v[88:91],a[40:43],v[236:239],v[88:91]\n"
"s_add_u32 s89,s89,1\n"
"ds_read_b128 v[204:207],v113 offset:3072\n"
"s_add_u32 s21,s21,16\n"
"v_mfma_f32_16x16x32_bf16 v[92:95],a[44:47],v[236:239],v[92:95]\n"
"ds_read_b128 v[208:211],v113 offset:16384\n"
"v_mfma_f32_16x16x32_bf16 v[92:95],a[36:39],v[232:235],v[92:95]\n"
"ds_read_b128 v[212:215],v113 offset:17408\n"
"s_add_u32 s16,s16,4\n"
"v_mfma_f32_16x16x32_bf16 v[92:95],v[44:47],v[228:231],v[92:95]\n"
"ds_read_b128 v[216:219],v113 offset:18432\n"
"buffer_load_dwordx4 v112,s[24:27],s18 offen offset:1024 sc1 lds\n"
"v_mfma_f32_16x16x32_bf16 v[92:95],v[36:39],v[224:227],v[92:95]\n"
"ds_read_b128 v[220:223],v113 offset:19456\n"
"v_mfma_f32_16x16x32_bf16 v[92:95],a[68:71],v[240:243],v[92:95]\n"
"ds_read_b128 v[236:239],v113 offset:7168\n"
"v_mfma_f32_16x16x32_bf16 v[92:95],a[76:79],v[244:247],v[92:95]\n"
"ds_read_b128 v[232:235],v113 offset:6144\n"
"ds_read_b128 v[228:231],v113 offset:5120\n"
"v_mfma_f32_16x16x32_bf16 v[92:95],a[84:87],v[248:251],v[92:95]\n"
"buffer_load_dwordx4 v112,s[24:27],s18 offen offset:2048 sc1 lds\n"
"v_mfma_f32_16x16x32_bf16 v[92:95],a[92:95],v[252:255],v[92:95]\n"
"ds_read_b128 v[224:227],v113 offset:4096\n"
"v_mfma_f32_16x16x32_bf16 v[88:91],a[88:91],v[252:255],v[88:91]\n"
"ds_read_b128 v[252:255],v113 offset:23552\n"
"v_mfma_f32_16x16x32_bf16 v[88:91],a[80:83],v[248:251],v[88:91]\n"
"ds_read_b128 v[248:251],v113 offset:22528\n"
"v_mfma_f32_16x16x32_bf16 v[88:91],a[72:75],v[244:247],v[88:91]\n"
"ds_read_b128 v[244:247],v113 offset:21504\n"
"v_mfma_f32_16x16x32_bf16 v[88:91],a[156:159],v[240:243],v[88:91]\n"
"ds_read_b128 v[240:243],v113 offset:20480\n"
"s_waitcnt lgkmcnt(12)\n"
"v_mfma_f32_16x16x32_bf16 v[80:83],v[128:131],v[192:195],v[80:83]\n"
"buffer_load_dwordx4 v112,s[24:27],s18 offen offset:3072 sc1 lds\n"
"v_mfma_f32_16x16x32_bf16 v[80:83],v[136:139],v[196:199],v[80:83]\n"
"v_mfma_f32_16x16x32_bf16 v[80:83],v[144:147],v[200:203],v[80:83]\n"
"s_add_u32 m0,m0,4096\n"
"v_mfma_f32_16x16x32_bf16 v[80:83],v[152:155],v[204:207],v[80:83]\n"
"buffer_load_dwordx4 v117,s[24:27],s18 offen offset:0 sc1 lds\n"
"v_mfma_f32_16x16x32_bf16 v[84:87],v[156:159],v[204:207],v[84:87]\n"
"ds_read_b128 v[204:207],v113 offset:11264\n"
"s_add_u32 s20,s20,32768\n"
"v_mfma_f32_16x16x32_bf16 v[84:87],v[148:151],v[200:203],v[84:87]\n"
"ds_read_b128 v[200:203],v113 offset:10240\n"
"s_and_b32 s20,s20,0x1ffff\n"
"v_mfma_f32_16x16x32_bf16 v[84:87],v[140:143],v[196:199],v[84:87]\n"
"ds_read_b128 v[196:199],v113 offset:9216\n"
"v_mfma_f32_16x16x32_bf16 v[84:87],v[132:135],v[192:195],v[84:87]\n"
"ds_read_b128 v[192:195],v113 offset:8192\n"
"s_waitcnt lgkmcnt(12)\n"
"v_mfma_f32_16x16x32_bf16 v[68:71],v[132:135],v[208:211],v[68:71]\n"
"buffer_load_dwordx4 v117,s[24:27],s18 offen offset:1024 sc1 lds\n"
"v_mfma_f32_16x16x32_bf16 v[68:71],v[140:143],v[212:215],v[68:71]\n"
"v_mfma_f32_16x16x32_bf16 v[68:71],v[148:151],v[216:219],v[68:71]\n"
"v_mfma_f32_16x16x32_bf16 v[68:71],v[156:159],v[220:223],v[68:71]\n"
"buffer_load_dwordx4 v117,s[24:27],s18 offen offset:2048 sc1 lds\n"
"v_mfma_f32_16x16x32_bf16 v[64:67],v[152:155],v[220:223],v[64:67]\n"
"ds_read_b128 v[220:223],v113 offset:27648\n"
"v_xor_b32 v119,s80,v122\n"
"v_mfma_f32_16x16x32_bf16 v[64:67],v[144:147],v[216:219],v[64:67]\n"
"ds_read_b128 v[216:219],v113 offset:26624\n"
"v_xor_b32 v120,s81,v123\n"
"v_mfma_f32_16x16x32_bf16 v[64:67],v[136:139],v[212:215],v[64:67]\n"
"ds_read_b128 v[212:215],v113 offset:25600\n"
"v_or_b32 v119,v119,v120\n"
"v_mfma_f32_16x16x32_bf16 v[64:67],v[128:131],v[208:211],v[64:67]\n"
"ds_read_b128 v[208:211],v113 offset:24576\n"
"s_waitcnt lgkmcnt(12)\n"
"v_mfma_f32_16x16x32_bf16 v[80:83],v[0:3],v[224:227],v[80:83]\n"
"buffer_load_dwordx4 v117,s[24:27],s18 offen offset:3072 sc1 lds\n"
"v_cmp_ne_u32 s[28:29],0,v119\n"
"v_mfma_f32_16x16x32_bf16 v[80:83],v[8:11],v[228:231],v[80:83]\n"
"s_and_b64 s[14:15],s[14:15],s[28:29]\n"
"s_cselect_b32 s91,s11,2146435072\n"
"s_add_u32 m0,s30,32768\n"
"v_mfma_f32_16x16x32_bf16 v[80:83],v[160:163],v[232:235],v[80:83]\n"
"buffer_load_dwordx2 v[122:123],v115,s[24:27],s91 offen sc1\n"
"v_mfma_f32_16x16x32_bf16 v[80:83],v[168:171],v[236:239],v[80:83]\n"
"buffer_load_dwordx4 v112,s[24:27],s8 offen offset:0 sc1 lds\n"
"v_mfma_f32_16x16x32_bf16 v[84:87],v[172:175],v[236:239],v[84:87]\n"
"ds_read_b128 v[236:239],v113 offset:15360\n"
"v_mfma_f32_16x16x32_bf16 v[84:87],v[164:167],v[232:235],v[84:87]\n"
"ds_read_b128 v[232:235],v113 offset:14336\n"
"v_mfma_f32_16x16x32_bf16 v[84:87],v[12:15],v[228:231],v[84:87]\n"
"ds_read_b128 v[228:231],v113 offset:13312\n"
"v_mfma_f32_16x16x32_bf16 v[84:87],v[4:7],v[224:227],v[84:87]\n"
"s_and_b32 s83,s15,0xff0000\n"
"ds_read_b128 v[224:227],v113 offset:12288\n"
"s_waitcnt lgkmcnt(12)\n"
"v_mfma_f32_16x16x32_bf16 v[68:71],v[4:7],v[240:243],v[68:71]\n"
"buffer_load_dwordx4 v112,s[24:27],s8 offen offset:1024 sc1 lds\n"
"v_mfma_f32_16x16x32_bf16 v[68:71],v[12:15],v[244:247],v[68:71]\n"
"s_cbranch_scc1 .Lfk_es_lkonem\n"
".Lfk_er_lkonem:\n"
"v_mfma_f32_16x16x32_bf16 v[68:71],v[164:167],v[248:251],v[68:71]\n"
"v_mfma_f32_16x16x32_bf16 v[68:71],v[172:175],v[252:255],v[68:71]\n"
"buffer_load_dwordx4 v112,s[24:27],s8 offen offset:2048 sc1 lds\n"
"v_mfma_f32_16x16x32_bf16 v[64:67],v[168:171],v[252:255],v[64:67]\n"
"ds_read_b128 v[252:255],v113 offset:31744\n"
"v_mfma_f32_16x16x32_bf16 v[64:67],v[160:163],v[248:251],v[64:67]\n"
"ds_read_b128 v[248:251],v113 offset:30720\n"
"v_mfma_f32_16x16x32_bf16 v[64:67],v[8:11],v[244:247],v[64:67]\n"
"ds_read_b128 v[244:247],v113 offset:29696\n"
"v_mfma_f32_16x16x32_bf16 v[64:67],v[0:3],v[240:243],v[64:67]\n"
"ds_read_b128 v[240:243],v113 offset:28672\n"
"s_waitcnt lgkmcnt(12)\n"
"v_mfma_f32_16x16x32_bf16 v[80:83],v[176:179],v[192:195],v[80:83]\n"
"s_waitcnt vmcnt(13)\n"
"ds_add_u32 v114,v116\n"
"v_mfma_f32_16x16x32_bf16 v[80:83],v[184:187],v[196:199],v[80:83]\n"
"buffer_load_dwordx4 v112,s[24:27],s8 offen offset:3072 sc1 lds\n"
"v_mfma_f32_16x16x32_bf16 v[80:83],a[48:51],v[200:203],v[80:83]\n"
"s_add_u32 m0,m0,4096\n"
"v_mfma_f32_16x16x32_bf16 v[80:83],a[56:59],v[204:207],v[80:83]\n"
"s_add_u32 s83,s20,32768\n"
"s_and_b32 s83,s83,0x1ffff\n"
"v_add_u32 v113,s83,v112\n"
"v_mfma_f32_16x16x32_bf16 v[84:87],a[60:63],v[204:207],v[84:87]\n"
"buffer_load_dwordx4 v117,s[24:27],s8 offen offset:0 sc1 lds\n"
"v_mfma_f32_16x16x32_bf16 v[84:87],a[52:55],v[200:203],v[84:87]\n"
"s_cmp_eq_u32 s52,0\n"
"v_not_b32 v104,s80\n"
"s_cselect_b32 s9,s9,2146435072\n"
"v_mfma_f32_16x16x32_bf16 v[84:87],v[188:191],v[196:199],v[84:87]\n"
"buffer_load_dwordx2 v[104:105],v115,s[24:27],s9 offen sc1\n"
"v_mfma_f32_16x16x32_bf16 v[84:87],v[180:183],v[192:195],v[84:87]\n"
"buffer_load_dwordx4 v117,s[24:27],s8 offen offset:1024 sc1 lds\n"
"s_waitcnt lgkmcnt(9)\n"
"v_mfma_f32_16x16x32_bf16 v[68:71],v[180:183],v[208:211],v[68:71]\n"
"v_mfma_f32_16x16x32_bf16 v[68:71],v[188:191],v[212:215],v[68:71]\n"
"ds_read_b32 v125,v114\n"
"v_mfma_f32_16x16x32_bf16 v[68:71],a[52:55],v[216:219],v[68:71]\n"
"buffer_load_dwordx4 v117,s[24:27],s8 offen offset:2048 sc1 lds\n"
"v_mfma_f32_16x16x32_bf16 v[68:71],a[60:63],v[220:223],v[68:71]\n"
"v_mfma_f32_16x16x32_bf16 v[64:67],a[56:59],v[220:223],v[64:67]\n"
"v_mfma_f32_16x16x32_bf16 v[64:67],a[48:51],v[216:219],v[64:67]\n"
"s_waitcnt lgkmcnt(0)\n"
"v_cmp_gt_u32 vcc,s16,v125\n"
"v_mfma_f32_16x16x32_bf16 v[64:67],v[184:187],v[212:215],v[64:67]\n"
"buffer_load_dwordx4 v117,s[24:27],s8 offen offset:3072 sc1 lds\n"
"v_mfma_f32_16x16x32_bf16 v[64:67],v[176:179],v[208:211],v[64:67]\n"
"s_lshl_b64 exec,1,s89\n"
"buffer_atomic_add v100,off,s[24:27],s10 sc0\n"
"s_mov_b64 exec,-1\n"
"s_add_u32 s8,s8,1048576\n"
"s_cbranch_vccnz .Lfk_es_mone\n"
".Lfk_er_mone:\n"
"v_mfma_f32_16x16x32_bf16 v[80:83],v[48:51],v[224:227],v[80:83]\n"
"ds_read_b128 v[192:195],v113 offset:0\n"
"v_mfma_f32_16x16x32_bf16 v[80:83],v[56:59],v[228:231],v[80:83]\n"
"ds_read_b128 v[196:199],v113 offset:1024\n"
"v_mfma_f32_16x16x32_bf16 v[80:83],v[16:19],v[232:235],v[80:83]\n"
"ds_read_b128 v[200:203],v113 offset:2048\n"
"s_add_u32 s89,s89,1\n"
"v_mfma_f32_16x16x32_bf16 v[80:83],v[24:27],v[236:239],v[80:83]\n"
"ds_read_b128 v[204:207],v113 offset:3072\n"
"s_add_u32 s21,s21,8\n"
"v_mfma_f32_16x16x32_bf16 v[84:87],v[28:31],v[236:239],v[84:87]\n"
"ds_read_b128 v[208:211],v113 offset:4096\n"
"v_mfma_f32_16x16x32_bf16 v[84:87],v[20:23],v[232:235],v[84:87]\n"
"ds_read_b128 v[212:215],v113 offset:5120\n"
"s_add_u32 s16,s16,4\n"
"v_mfma_f32_16x16x32_bf16 v[84:87],v[60:63],v[228:231],v[84:87]\n"
"ds_read_b128 v[216:219],v113 offset:6144\n"
"v_mfma_f32_16x16x32_bf16 v[84:87],v[52:55],v[224:227],v[84:87]\n"
"ds_read_b128 v[220:223],v113 offset:7168\n"
"v_mfma_f32_16x16x32_bf16 v[68:71],v[52:55],v[240:243],v[68:71]\n"
"ds_read_b128 v[236:239],v113 offset:11264\n"
"v_mfma_f32_16x16x32_bf16 v[68:71],v[60:63],v[244:247],v[68:71]\n"
"ds_read_b128 v[232:235],v113 offset:10240\n"
"v_mfma_f32_16x16x32_bf16 v[68:71],v[20:23],v[248:251],v[68:71]\n"
"ds_read_b128 v[228:231],v113 offset:9216\n"
"ds_read_b128 v[224:227],v113 offset:8192\n"
"v_mfma_f32_16x16x32_bf16 v[68:71],v[28:31],v[252:255],v[68:71]\n"
"v_cvt_pk_bf16_f32 v80,v80,v81\n"
"v_cvt_pk_bf16_f32 v81,v82,v83\n"
"v_mfma_f32_16x16x32_bf16 v[64:67],v[24:27],v[252:255],v[64:67]\n"
"ds_read_b128 v[252:255],v113 offset:15360\n"
"v_cvt_pk_bf16_f32 v82,v84,v85\n"
"v_cvt_pk_bf16_f32 v83,v86,v87\n"
"v_mfma_f32_16x16x32_bf16 v[64:67],v[16:19],v[248:251],v[64:67]\n"
"ds_read_b128 v[248:251],v113 offset:14336\n"
"v_max_u32 v80,1,v80\n"
"v_max_u32 v81,1,v81\n"
"v_mfma_f32_16x16x32_bf16 v[64:67],v[56:59],v[244:247],v[64:67]\n"
"ds_read_b128 v[244:247],v113 offset:13312\n"
"v_max_u32 v82,1,v82\n"
"v_max_u32 v83,1,v83\n"
"v_mfma_f32_16x16x32_bf16 v[64:67],v[48:51],v[240:243],v[64:67]\n"
"ds_read_b128 v[240:243],v113 offset:12288\n"
"buffer_store_dwordx4 v[80:83],v118,s[24:27],s22 offen sc1\n"
"s_waitcnt lgkmcnt(12)\n"
"v_mfma_f32_16x16x32_bf16 v[72:75],v[128:131],v[192:195],v[72:75]\n"
"s_add_u32 m0,s30,s20\n"
"s_add_u32 s22,s22,262144\n"
"v_mfma_f32_16x16x32_bf16 v[72:75],v[136:139],v[196:199],v[72:75]\n"
"buffer_load_dwordx4 v112,s[24:27],s8 offen offset:0 sc1 lds\n"
"v_mfma_f32_16x16x32_bf16 v[72:75],v[144:147],v[200:203],v[72:75]\n"
"v_mfma_f32_16x16x32_bf16 v[72:75],v[152:155],v[204:207],v[72:75]\n"
"v_cvt_pk_bf16_f32 v64,v64,v65\n"
"v_mfma_f32_16x16x32_bf16 v[76:79],v[156:159],v[204:207],v[76:79]\n"
"ds_read_b128 v[204:207],v113 offset:19456\n"
"s_add_u32 s20,s20,32768\n"
"v_mfma_f32_16x16x32_bf16 v[76:79],v[148:151],v[200:203],v[76:79]\n"
"ds_read_b128 v[200:203],v113 offset:18432\n"
"s_and_b32 s20,s20,0x1ffff\n"
"v_mfma_f32_16x16x32_bf16 v[76:79],v[140:143],v[196:199],v[76:79]\n"
"ds_read_b128 v[196:199],v113 offset:17408\n"
"v_cvt_pk_bf16_f32 v65,v66,v67\n"
"v_mfma_f32_16x16x32_bf16 v[76:79],v[132:135],v[192:195],v[76:79]\n"
"ds_read_b128 v[192:195],v113 offset:16384\n"
"v_cvt_pk_bf16_f32 v66,v68,v69\n"
"s_waitcnt lgkmcnt(12)\n"
"v_mfma_f32_16x16x32_bf16 v[76:79],v[4:7],v[208:211],v[76:79]\n"
"buffer_load_dwordx4 v112,s[24:27],s8 offen offset:1024 sc1 lds\n"
"v_cvt_pk_bf16_f32 v67,v70,v71\n"
"v_mfma_f32_16x16x32_bf16 v[76:79],v[12:15],v[212:215],v[76:79]\n"
"v_max_u32 v64,1,v64\n"
"v_max_u32 v65,1,v65\n"
"v_max_u32 v66,1,v66\n"
"v_max_u32 v67,1,v67\n"
"v_mfma_f32_16x16x32_bf16 v[76:79],v[164:167],v[216:219],v[76:79]\n"
"buffer_store_dwordx4 v[64:67],v118,s[24:27],s22 offen sc1\n"
"v_mfma_f32_16x16x32_bf16 v[76:79],v[172:175],v[220:223],v[76:79]\n"
"buffer_load_dwordx4 v112,s[24:27],s8 offen offset:2048 sc1 lds\n"
"v_mfma_f32_16x16x32_bf16 v[72:75],v[168:171],v[220:223],v[72:75]\n"
"s_add_u32 s22,s22,262144\n"
"ds_read_b128 v[220:223],v113 offset:23552\n"
"v_mfma_f32_16x16x32_bf16 v[72:75],v[160:163],v[216:219],v[72:75]\n"
"ds_read_b128 v[216:219],v113 offset:22528\n"
"v_mfma_f32_16x16x32_bf16 v[72:75],v[8:11],v[212:215],v[72:75]\n"
"ds_read_b128 v[212:215],v113 offset:21504\n"
"v_mfma_f32_16x16x32_bf16 v[72:75],v[0:3],v[208:211],v[72:75]\n"
"ds_read_b128 v[208:211],v113 offset:20480\n"
"s_waitcnt lgkmcnt(12)\n"
"v_mfma_f32_16x16x32_bf16 v[72:75],v[176:179],v[224:227],v[72:75]\n"
"v_mfma_f32_16x16x32_bf16 v[72:75],v[184:187],v[228:231],v[72:75]\n"
"buffer_load_dwordx4 v112,s[24:27],s8 offen offset:3072 sc1 lds\n"
"v_mfma_f32_16x16x32_bf16 v[72:75],a[48:51],v[232:235],v[72:75]\n"
"v_xor_b32 v119,s80,v122\n"
"v_xor_b32 v120,s81,v123\n"
"v_mfma_f32_16x16x32_bf16 v[72:75],a[56:59],v[236:239],v[72:75]\n"
"v_or_b32 v119,v119,v120\n"
"v_cmp_ne_u32 s[28:29],0,v119\n"
"v_mfma_f32_16x16x32_bf16 v[76:79],a[60:63],v[236:239],v[76:79]\n"
"ds_read_b128 v[236:239],v113 offset:27648\n"
"s_and_b64 s[14:15],s[14:15],s[28:29]\n"
"v_mfma_f32_16x16x32_bf16 v[76:79],a[52:55],v[232:235],v[76:79]\n"
"ds_read_b128 v[232:235],v113 offset:26624\n"
"v_mfma_f32_16x16x32_bf16 v[76:79],v[188:191],v[228:231],v[76:79]\n"
"ds_read_b128 v[228:231],v113 offset:25600\n"
"v_mfma_f32_16x16x32_bf16 v[76:79],v[180:183],v[224:227],v[76:79]\n"
"s_add_u32 m0,m0,4096\n"
"ds_read_b128 v[224:227],v113 offset:24576\n"
"s_waitcnt lgkmcnt(12)\n"
"v_mfma_f32_16x16x32_bf16 v[76:79],v[52:55],v[240:243],v[76:79]\n"
"buffer_load_dwordx4 v117,s[24:27],s8 offen offset:0 sc1 lds\n"
"v_mfma_f32_16x16x32_bf16 v[76:79],v[60:63],v[244:247],v[76:79]\n"
"s_and_b32 s83,s15,0xff000000\n"
"s_cbranch_scc1 .Lfk_es_lktwom\n"
".Lfk_er_lktwom:\n"
"v_mfma_f32_16x16x32_bf16 v[76:79],v[20:23],v[248:251],v[76:79]\n"
"v_xor_b32 v119,s80,v104\n"
"v_xor_b32 v120,s81,v105\n"
"v_or_b32 v119,v119,v120\n"
"v_mfma_f32_16x16x32_bf16 v[76:79],v[28:31],v[252:255],v[76:79]\n"
"buffer_load_dwordx4 v117,s[24:27],s8 offen offset:1024 sc1 lds\n"
"v_cmp_ne_u32 s[28:29],0,v119\n"
"v_mfma_f32_16x16x32_bf16 v[72:75],v[24:27],v[252:255],v[72:75]\n"
"s_cmp_eq_u32 s52,0\n"
"ds_read_b128 v[252:255],v113 offset:31744\n"
"s_cselect_b64 s[14:15],-1,0\n"
"v_mfma_f32_16x16x32_bf16 v[72:75],v[16:19],v[248:251],v[72:75]\n"
"s_mov_b32 s11,s9\n"
"ds_read_b128 v[248:251],v113 offset:30720\n"
"s_waitcnt vmcnt(18)\n"
"v_not_b32 v122,s80\n"
"v_mfma_f32_16x16x32_bf16 v[72:75],v[56:59],v[244:247],v[72:75]\n"
"s_and_b64 s[14:15],s[14:15],s[28:29]\n"
"s_cselect_b32 s91,s11,2146435072\n"
"ds_read_b128 v[244:247],v113 offset:29696\n"
"buffer_load_dwordx2 v[122:123],v115,s[24:27],s91 offen sc1\n"
"v_mfma_f32_16x16x32_bf16 v[72:75],v[48:51],v[240:243],v[72:75]\n"
"ds_read_b128 v[240:243],v113 offset:28672\n"
"s_waitcnt lgkmcnt(12)\n"
"v_mfma_f32_16x16x32_bf16 v[88:91],v[128:131],v[192:195],v[88:91]\n"
"ds_write_b32 v101,v100\n"
"v_mfma_f32_16x16x32_bf16 v[88:91],v[136:139],v[196:199],v[88:91]\n"
"ds_add_u32 v114,v116\n"
"v_mfma_f32_16x16x32_bf16 v[88:91],v[144:147],v[200:203],v[88:91]\n"
"buffer_load_dwordx4 v117,s[24:27],s8 offen offset:2048 sc1 lds\n"
"v_mfma_f32_16x16x32_bf16 v[88:91],v[152:155],v[204:207],v[88:91]\n"
"s_add_u32 s83,s20,32768\n"
"s_and_b32 s83,s83,0x1ffff\n"
"v_add_u32 v113,s83,v112\n"
"v_mfma_f32_16x16x32_bf16 v[92:95],v[156:159],v[204:207],v[92:95]\n"
"v_cvt_pk_bf16_f32 v72,v72,v73\n"
"v_cvt_pk_bf16_f32 v73,v74,v75\n"
"v_cvt_pk_bf16_f32 v74,v76,v77\n"
"v_mfma_f32_16x16x32_bf16 v[92:95],v[148:151],v[200:203],v[92:95]\n"
"v_cvt_pk_bf16_f32 v75,v78,v79\n"
"v_max_u32 v72,1,v72\n"
"v_max_u32 v73,1,v73\n"
"v_mfma_f32_16x16x32_bf16 v[92:95],v[140:143],v[196:199],v[92:95]\n"
"v_max_u32 v74,1,v74\n"
"v_max_u32 v75,1,v75\n"
"v_mfma_f32_16x16x32_bf16 v[92:95],v[132:135],v[192:195],v[92:95]\n"
"buffer_store_dwordx4 v[72:75],v118,s[24:27],s22 offen sc1\n"
"s_waitcnt lgkmcnt(10)\n"
"v_mfma_f32_16x16x32_bf16 v[92:95],v[4:7],v[208:211],v[92:95]\n"
"buffer_load_dwordx4 v117,s[24:27],s8 offen offset:3072 sc1 lds\n"
"v_mfma_f32_16x16x32_bf16 v[92:95],v[12:15],v[212:215],v[92:95]\n"
"s_add_u32 s22,s22,262144\n"
"ds_read_b32 v125,v114\n"
"s_add_u32 s8,s8,1048576\n"
"v_mfma_f32_16x16x32_bf16 v[92:95],v[164:167],v[216:219],v[92:95]\n"
"v_mfma_f32_16x16x32_bf16 v[92:95],v[172:175],v[220:223],v[92:95]\n"
"v_mfma_f32_16x16x32_bf16 v[88:91],v[168:171],v[220:223],v[88:91]\n"
"s_lshl_b64 exec,1,s89\n"
"buffer_atomic_add v100,off,s[24:27],s10 sc0\n"
"s_mov_b64 exec,-1\n"
"v_mfma_f32_16x16x32_bf16 v[88:91],v[160:163],v[216:219],v[88:91]\n"
"s_waitcnt lgkmcnt(0)\n"
"v_cmp_gt_u32 vcc,s16,v125\n"
"v_mfma_f32_16x16x32_bf16 v[88:91],v[8:11],v[212:215],v[88:91]\n"
"v_mfma_f32_16x16x32_bf16 v[88:91],v[0:3],v[208:211],v[88:91]\n"
"s_cbranch_vccnz .Lfk_es_mtwo\n"
".Lfk_er_mtwo:\n"
".Lfk_l4:\n"
"v_mfma_f32_16x16x32_bf16 v[88:91],v[176:179],v[224:227],v[88:91]\n"
"ds_read_b128 v[192:195],v113 offset:0\n"
"v_mfma_f32_16x16x32_bf16 v[88:91],v[184:187],v[228:231],v[88:91]\n"
"ds_read_b128 v[196:199],v113 offset:1024\n"
"v_mfma_f32_16x16x32_bf16 v[88:91],a[48:51],v[232:235],v[88:91]\n"
"ds_read_b128 v[200:203],v113 offset:2048\n"
"s_add_u32 s89,s89,1\n"
"v_mfma_f32_16x16x32_bf16 v[88:91],a[56:59],v[236:239],v[88:91]\n"
"ds_read_b128 v[204:207],v113 offset:3072\n"
"v_mfma_f32_16x16x32_bf16 v[92:95],a[60:63],v[236:239],v[92:95]\n"
"ds_read_b128 v[208:211],v113 offset:4096\n"
"v_mfma_f32_16x16x32_bf16 v[92:95],a[52:55],v[232:235],v[92:95]\n"
"ds_read_b128 v[212:215],v113 offset:5120\n"
"s_add_u32 s16,s16,4\n"
"v_mfma_f32_16x16x32_bf16 v[92:95],v[188:191],v[228:231],v[92:95]\n"
"ds_read_b128 v[216:219],v113 offset:6144\n"
"v_mfma_f32_16x16x32_bf16 v[92:95],v[180:183],v[224:227],v[92:95]\n"
"ds_read_b128 v[220:223],v113 offset:7168\n"
"v_mfma_f32_16x16x32_bf16 v[92:95],v[52:55],v[240:243],v[92:95]\n"
"ds_read_b128 v[236:239],v113 offset:11264\n"
"v_mfma_f32_16x16x32_bf16 v[92:95],v[60:63],v[244:247],v[92:95]\n"
"ds_read_b128 v[232:235],v113 offset:10240\n"
"v_mfma_f32_16x16x32_bf16 v[92:95],v[20:23],v[248:251],v[92:95]\n"
"ds_read_b128 v[228:231],v113 offset:9216\n"
"v_mfma_f32_16x16x32_bf16 v[92:95],v[28:31],v[252:255],v[92:95]\n"
"ds_read_b128 v[224:227],v113 offset:8192\n"
"v_mfma_f32_16x16x32_bf16 v[88:91],v[24:27],v[252:255],v[88:91]\n"
"ds_read_b128 v[252:255],v113 offset:15360\n"
"v_mfma_f32_16x16x32_bf16 v[88:91],v[16:19],v[248:251],v[88:91]\n"
"ds_read_b128 v[248:251],v113 offset:14336\n"
"v_mfma_f32_16x16x32_bf16 v[88:91],v[56:59],v[244:247],v[88:91]\n"
"ds_read_b128 v[244:247],v113 offset:13312\n"
"v_mfma_f32_16x16x32_bf16 v[88:91],v[48:51],v[240:243],v[88:91]\n"
"ds_read_b128 v[240:243],v113 offset:12288\n"
"s_waitcnt lgkmcnt(12)\n"
"v_mfma_f32_16x16x32_bf16 v[80:83],a[224:227],v[192:195],0\n"
"s_add_u32 m0,s30,s20\n"
"ds_read_b32 v103,v102\n"
"v_mfma_f32_16x16x32_bf16 v[80:83],a[232:235],v[196:199],v[80:83]\n"
"buffer_load_dwordx4 v112,s[24:27],s8 offen offset:0 sc1 lds\n"
"v_mfma_f32_16x16x32_bf16 v[80:83],a[240:243],v[200:203],v[80:83]\n"
"v_add_u32 v102,4,v102\n"
"v_and_b32 v102,0x200ff,v102\n"
"v_mfma_f32_16x16x32_bf16 v[80:83],a[248:251],v[204:207],v[80:83]\n"
"v_cvt_pk_bf16_f32 v88,v88,v89\n"
"v_cvt_pk_bf16_f32 v89,v90,v91\n"
"v_mfma_f32_16x16x32_bf16 v[84:87],a[252:255],v[204:207],0\n"
"ds_read_b128 v[204:207],v113 offset:19456\n"
"s_add_u32 s20,s20,32768\n"
"v_mfma_f32_16x16x32_bf16 v[84:87],a[244:247],v[200:203],v[84:87]\n"
"ds_read_b128 v[200:203],v113 offset:18432\n"
"s_and_b32 s20,s20,0x1ffff\n"
"v_mfma_f32_16x16x32_bf16 v[84:87],a[236:239],v[196:199],v[84:87]\n"
"ds_read_b128 v[196:199],v113 offset:17408\n"
"v_cvt_pk_bf16_f32 v90,v92,v93\n"
"v_mfma_f32_16x16x32_bf16 v[84:87],a[228:231],v[192:195],v[84:87]\n"
"ds_read_b128 v[192:195],v113 offset:16384\n"
"s_waitcnt lgkmcnt(13)\n"
"v_mfma_f32_16x16x32_bf16 v[84:87],a[4:7],v[208:211],v[84:87]\n"
"buffer_load_dwordx4 v112,s[24:27],s8 offen offset:1024 sc1 lds\n"
"v_mfma_f32_16x16x32_bf16 v[84:87],a[12:15],v[212:215],v[84:87]\n"
"v_cvt_pk_bf16_f32 v91,v94,v95\n"
"v_max_u32 v88,1,v88\n"
"v_mfma_f32_16x16x32_bf16 v[84:87],a[20:23],v[216:219],v[84:87]\n"
"v_max_u32 v89,1,v89\n"
"v_max_u32 v90,1,v90\n"
"v_mfma_f32_16x16x32_bf16 v[84:87],a[28:31],v[220:223],v[84:87]\n"
"buffer_load_dwordx4 v112,s[24:27],s8 offen offset:2048 sc1 lds\n"
"v_mfma_f32_16x16x32_bf16 v[80:83],a[24:27],v[220:223],v[80:83]\n"
"ds_read_b128 v[220:223],v113 offset:23552\n"
"v_max_u32 v91,1,v91\n"
"v_mfma_f32_16x16x32_bf16 v[80:83],a[16:19],v[216:219],v[80:83]\n"
"ds_read_b128 v[216:219],v113 offset:22528\n"
"buffer_store_dwordx4 v[88:91],v118,s[24:27],s53 offen sc1\n"
"v_mfma_f32_16x16x32_bf16 v[80:83],a[8:11],v[212:215],v[80:83]\n"
"ds_read_b128 v[212:215],v113 offset:21504\n"
"v_mfma_f32_16x16x32_bf16 v[80:83],a[0:3],v[208:211],v[80:83]\n"
"ds_read_b128 v[208:211],v113 offset:20480\n"
"s_waitcnt lgkmcnt(13)\n"
"v_mfma_f32_16x16x32_bf16 v[80:83],v[32:35],v[224:227],v[80:83]\n"
"v_mfma_f32_16x16x32_bf16 v[80:83],v[40:43],v[228:231],v[80:83]\n"
"buffer_load_dwordx4 v112,s[24:27],s8 offen offset:3072 sc1 lds\n"
"v_mfma_f32_16x16x32_bf16 v[80:83],a[32:35],v[232:235],v[80:83]\n"
"s_waitcnt lgkmcnt(8)\n"
"v_readfirstlane_b32 s22,v103\n"
"v_mfma_f32_16x16x32_bf16 v[80:83],a[40:43],v[236:239],v[80:83]\n"
"v_mfma_f32_16x16x32_bf16 v[84:87],a[44:47],v[236:239],v[84:87]\n"
"ds_read_b128 v[236:239],v113 offset:27648\n"
"s_and_b32 s21,s22,0x38\n"
"v_mfma_f32_16x16x32_bf16 v[84:87],a[36:39],v[232:235],v[84:87]\n"
"ds_read_b128 v[232:235],v113 offset:26624\n"
"v_mfma_f32_16x16x32_bf16 v[84:87],v[44:47],v[228:231],v[84:87]\n"
"ds_read_b128 v[228:231],v113 offset:25600\n"
"v_mfma_f32_16x16x32_bf16 v[84:87],v[36:39],v[224:227],v[84:87]\n"
"ds_read_b128 v[224:227],v113 offset:24576\n"
"s_add_u32 m0,m0,4096\n"
"v_mfma_f32_16x16x32_bf16 v[84:87],a[68:71],v[240:243],v[84:87]\n"
"buffer_load_dwordx4 v117,s[24:27],s8 offen offset:0 sc1 lds\n"
"v_mfma_f32_16x16x32_bf16 v[84:87],a[76:79],v[244:247],v[84:87]\n"
"s_cmp_lg_u64 s[14:15],0\n"
"s_cbranch_scc1 .Lfk_es_lkloopm\n"
".Lfk_er_lkloopm:\n"
"v_mfma_f32_16x16x32_bf16 v[84:87],a[84:87],v[248:251],v[84:87]\n"
"v_mfma_f32_16x16x32_bf16 v[84:87],a[92:95],v[252:255],v[84:87]\n"
"buffer_load_dwordx4 v117,s[24:27],s8 offen offset:1024 sc1 lds\n"
"v_mfma_f32_16x16x32_bf16 v[80:83],a[88:91],v[252:255],v[80:83]\n"
"ds_read_b128 v[252:255],v113 offset:31744\n"
"s_mov_b32 s53,s56\n"
"v_mfma_f32_16x16x32_bf16 v[80:83],a[80:83],v[248:251],v[80:83]\n"
"ds_read_b128 v[248:251],v113 offset:30720\n"
"s_mov_b32 s56,s58\n"
"v_mfma_f32_16x16x32_bf16 v[80:83],a[72:75],v[244:247],v[80:83]\n"
"ds_read_b128 v[244:247],v113 offset:29696\n"
"s_mov_b32 s58,s59\n"
"v_mfma_f32_16x16x32_bf16 v[80:83],a[156:159],v[240:243],v[80:83]\n"
"ds_read_b128 v[240:243],v113 offset:28672\n"
"s_waitcnt lgkmcnt(12)\n"
"v_mfma_f32_16x16x32_bf16 v[88:91],v[128:131],v[192:195],v[80:83]\n"
"s_waitcnt vmcnt(17)\n"
"ds_write_b32 v101,v100\n"
"v_mfma_f32_16x16x32_bf16 v[88:91],v[136:139],v[196:199],v[88:91]\n"
"ds_add_u32 v114,v116\n"
"v_mfma_f32_16x16x32_bf16 v[88:91],v[144:147],v[200:203],v[88:91]\n"
"buffer_load_dwordx4 v117,s[24:27],s8 offen offset:2048 sc1 lds\n"
"v_mfma_f32_16x16x32_bf16 v[88:91],v[152:155],v[204:207],v[88:91]\n"
"s_add_u32 s83,s20,32768\n"
"s_and_b32 s83,s83,0x1ffff\n"
"v_add_u32 v113,s83,v112\n"
"v_mfma_f32_16x16x32_bf16 v[92:95],v[156:159],v[204:207],v[84:87]\n"
"s_cmp_lt_u32 s22,64\n"
"s_cselect_b32 s17,s16,-1\n"
"v_mfma_f32_16x16x32_bf16 v[92:95],v[148:151],v[200:203],v[92:95]\n"
"v_mfma_f32_16x16x32_bf16 v[92:95],v[140:143],v[196:199],v[92:95]\n"
"v_mfma_f32_16x16x32_bf16 v[92:95],v[132:135],v[192:195],v[92:95]\n"
"s_waitcnt lgkmcnt(10)\n"
"v_mfma_f32_16x16x32_bf16 v[92:95],v[4:7],v[208:211],v[92:95]\n"
"buffer_load_dwordx4 v117,s[24:27],s8 offen offset:3072 sc1 lds\n"
"v_mfma_f32_16x16x32_bf16 v[92:95],v[12:15],v[212:215],v[92:95]\n"
"s_lshr_b32 s59,s8,2\n"
"ds_read_b32 v125,v114\n"
"s_lshl_b32 s8,s22,17\n"
"v_mfma_f32_16x16x32_bf16 v[92:95],v[164:167],v[216:219],v[92:95]\n"
"s_add_u32 s8,s8,s31\n"
"v_mfma_f32_16x16x32_bf16 v[92:95],v[172:175],v[220:223],v[92:95]\n"
"v_mfma_f32_16x16x32_bf16 v[88:91],v[168:171],v[220:223],v[88:91]\n"
"s_lshl_b64 exec,1,s89\n"
"buffer_atomic_add v100,off,s[24:27],s10 sc0\n"
"s_mov_b64 exec,-1\n"
"v_mfma_f32_16x16x32_bf16 v[88:91],v[160:163],v[216:219],v[88:91]\n"
"s_waitcnt lgkmcnt(0)\n"
"v_cmp_gt_u32 vcc,s17,v125\n"
"v_mfma_f32_16x16x32_bf16 v[88:91],v[8:11],v[212:215],v[88:91]\n"
"v_mfma_f32_16x16x32_bf16 v[88:91],v[0:3],v[208:211],v[88:91]\n"
"s_cbranch_vccz .Lfk_l4\n"
"s_call_b64 s[94:95],.Lfk_meet4\n"
"s_cmp_lt_u32 s22,64\n"
"s_cbranch_scc1 .Lfk_l4\n"
"ds_read_b128 v[192:195],v113 offset:0\n"
"v_mfma_f32_16x16x32_bf16 v[88:91],v[176:179],v[224:227],v[88:91]\n"
"s_getpc_b64 s[96:97]\n"
".Lfk_rt2:\n"
"s_add_u32 s96,s96,.Lfk_tail-.Lfk_rt2\n"
"s_addc_u32 s97,s97,0\n"
"s_mov_b32 s98,.Lfk_cold-.Lfk_tail\n"
"s_mov_b32 s99,0x20000\n"
"s_lshr_b32 s82,s100,3\n"
"s_lshl2_add_u32 s82,s82,s52\n"
"v_lshrrev_b32 v121,2,v112\n"
"s_lshl_b32 s82,s82,8\n"
"v_mfma_f32_16x16x32_bf16 v[88:91],v[184:187],v[228:231],v[88:91]\n"
"buffer_load_dword v120,v121,s[96:99],s82 offen\n"
"ds_read_b128 v[196:199],v113 offset:1024\n"
"v_mfma_f32_16x16x32_bf16 v[88:91],a[48:51],v[232:235],v[88:91]\n"
"s_mov_b32 s77,0\n"
"ds_read_b128 v[200:203],v113 offset:2048\n"
"s_and_b32 s91,s52,1\n"
"v_mfma_f32_16x16x32_bf16 v[88:91],a[56:59],v[236:239],v[88:91]\n"
"ds_read_b128 v[204:207],v113 offset:3072\n"
"s_lshl_b32 s91,s91,7\n"
"v_mfma_f32_16x16x32_bf16 v[92:95],a[60:63],v[236:239],v[92:95]\n"
"ds_read_b128 v[208:211],v113 offset:4096\n"
"v_xor_b32 v96,s91,v112\n"
"v_mfma_f32_16x16x32_bf16 v[92:95],a[52:55],v[232:235],v[92:95]\n"
"ds_read_b128 v[212:215],v113 offset:5120\n"
"s_add_u32 s16,s16,4\n"
"v_mfma_f32_16x16x32_bf16 v[92:95],v[188:191],v[228:231],v[92:95]\n"
"ds_read_b128 v[216:219],v113 offset:6144\n"
"v_xor_b32 v97,32,v96\n"
"v_mfma_f32_16x16x32_bf16 v[92:95],v[180:183],v[224:227],v[92:95]\n"
"ds_read_b128 v[220:223],v113 offset:7168\n"
"v_xor_b32 v98,64,v96\n"
"v_mfma_f32_16x16x32_bf16 v[92:95],v[52:55],v[240:243],v[92:95]\n"
"ds_read_b128 v[236:239],v113 offset:11264\n"
"v_xor_b32 v99,96,v96\n"
"v_mfma_f32_16x16x32_bf16 v[92:95],v[60:63],v[244:247],v[92:95]\n"
"ds_read_b128 v[232:235],v113 offset:10240\n"
"s_mov_b32 s38,4194304\n"
"v_mfma_f32_16x16x32_bf16 v[92:95],v[20:23],v[248:251],v[92:95]\n"
"ds_read_b128 v[228:231],v113 offset:9216\n"
"s_mov_b32 s39,0x20000\n"
"v_mfma_f32_16x16x32_bf16 v[92:95],v[28:31],v[252:255],v[92:95]\n"
"ds_read_b128 v[224:227],v113 offset:8192\n"
"s_mov_b32 s46,2048\n"
"v_mfma_f32_16x16x32_bf16 v[88:91],v[24:27],v[252:255],v[88:91]\n"
"ds_read_b128 v[252:255],v113 offset:15360\n"
"s_mov_b32 s47,0x20000\n"
"v_mfma_f32_16x16x32_bf16 v[88:91],v[16:19],v[248:251],v[88:91]\n"
"ds_read_b128 v[248:251],v113 offset:14336\n"
"s_mov_b32 s50,4194304\n"
"v_mfma_f32_16x16x32_bf16 v[88:91],v[56:59],v[244:247],v[88:91]\n"
"ds_read_b128 v[244:247],v113 offset:13312\n"
"s_mov_b32 s51,0x20000\n"
"v_mfma_f32_16x16x32_bf16 v[88:91],v[48:51],v[240:243],v[88:91]\n"
"ds_read_b128 v[240:243],v113 offset:12288\n"
"s_waitcnt lgkmcnt(12)\n"
"v_mfma_f32_16x16x32_bf16 v[80:83],a[224:227],v[192:195],0\n"
"s_and_b32 s60,s100,7\n"
"s_lshl_b32 s60,s60,5\n"
"v_mfma_f32_16x16x32_bf16 v[80:83],a[232:235],v[196:199],v[80:83]\n"
"s_lshr_b32 s73,s100,3\n"
"s_add_u32 s73,s73,s60\n"
"v_mfma_f32_16x16x32_bf16 v[80:83],a[240:243],v[200:203],v[80:83]\n"
"s_lshl2_add_u32 s60,s100,s52\n"
"s_lshl_b32 s61,s60,11\n"
"v_mfma_f32_16x16x32_bf16 v[80:83],a[248:251],v[204:207],v[80:83]\n"
"v_cvt_pk_bf16_f32 v88,v88,v89\n"
"v_cvt_pk_bf16_f32 v89,v90,v91\n"
"v_mfma_f32_16x16x32_bf16 v[84:87],a[252:255],v[204:207],0\n"
"ds_read_b128 v[204:207],v113 offset:19456\n"
"s_add_u32 s20,s20,32768\n"
"v_mfma_f32_16x16x32_bf16 v[84:87],a[244:247],v[200:203],v[84:87]\n"
"ds_read_b128 v[200:203],v113 offset:18432\n"
"s_and_b32 s20,s20,0x1ffff\n"
"v_mfma_f32_16x16x32_bf16 v[84:87],a[236:239],v[196:199],v[84:87]\n"
"ds_read_b128 v[196:199],v113 offset:17408\n"
"v_cvt_pk_bf16_f32 v90,v92,v93\n"
"v_mfma_f32_16x16x32_bf16 v[84:87],a[228:231],v[192:195],v[84:87]\n"
"ds_read_b128 v[192:195],v113 offset:16384\n"
"s_waitcnt lgkmcnt(12)\n"
"v_mfma_f32_16x16x32_bf16 v[84:87],a[4:7],v[208:211],v[84:87]\n"
"v_cvt_pk_bf16_f32 v91,v94,v95\n"
"v_max_u32 v88,1,v88\n"
"v_mfma_f32_16x16x32_bf16 v[84:87],a[12:15],v[212:215],v[84:87]\n"
"v_max_u32 v89,1,v89\n"
"v_max_u32 v90,1,v90\n"
"v_max_u32 v91,1,v91\n"
"v_mfma_f32_16x16x32_bf16 v[84:87],a[20:23],v[216:219],v[84:87]\n"
"buffer_store_dwordx4 v[88:91],v118,s[24:27],s53 offen sc1\n"
"v_mfma_f32_16x16x32_bf16 v[84:87],a[28:31],v[220:223],v[84:87]\n"
"v_mfma_f32_16x16x32_bf16 v[80:83],a[24:27],v[220:223],v[80:83]\n"
"ds_read_b128 v[220:223],v113 offset:23552\n"
"v_mfma_f32_16x16x32_bf16 v[80:83],a[16:19],v[216:219],v[80:83]\n"
"ds_read_b128 v[216:219],v113 offset:22528\n"
"v_mfma_f32_16x16x32_bf16 v[80:83],a[8:11],v[212:215],v[80:83]\n"
"ds_read_b128 v[212:215],v113 offset:21504\n"
"v_mfma_f32_16x16x32_bf16 v[80:83],a[0:3],v[208:211],v[80:83]\n"
"ds_read_b128 v[208:211],v113 offset:20480\n"
"s_waitcnt lgkmcnt(12)\n"
"v_mfma_f32_16x16x32_bf16 v[80:83],v[32:35],v[224:227],v[80:83]\n"
"v_mfma_f32_16x16x32_bf16 v[80:83],v[40:43],v[228:231],v[80:83]\n"
"v_mfma_f32_16x16x32_bf16 v[80:83],a[32:35],v[232:235],v[80:83]\n"
"v_mfma_f32_16x16x32_bf16 v[80:83],a[40:43],v[236:239],v[80:83]\n"
"v_mfma_f32_16x16x32_bf16 v[84:87],a[44:47],v[236:239],v[84:87]\n"
"ds_read_b128 v[236:239],v113 offset:27648\n"
"v_mfma_f32_16x16x32_bf16 v[84:87],a[36:39],v[232:235],v[84:87]\n"
"ds_read_b128 v[232:235],v113 offset:26624\n"
"v_mfma_f32_16x16x32_bf16 v[84:87],v[44:47],v[228:231],v[84:87]\n"
"ds_read_b128 v[228:231],v113 offset:25600\n"
"v_mfma_f32_16x16x32_bf16 v[84:87],v[36:39],v[224:227],v[84:87]\n"
"ds_read_b128 v[224:227],v113 offset:24576\n"
"s_waitcnt lgkmcnt(12)\n"
"v_mfma_f32_16x16x32_bf16 v[84:87],a[68:71],v[240:243],v[84:87]\n"
"s_lshr_b32 s62,s60,4\n"
"s_lshl_b32 s62,s62,15\n"
"s_and_b32 s91,s60,15\n"
"v_mfma_f32_16x16x32_bf16 v[84:87],a[76:79],v[244:247],v[84:87]\n"
"s_lshl_b32 s91,s91,6\n"
"s_add_u32 s62,s62,s91\n"
"s_add_u32 s62,s62,71303168\n"
"v_mfma_f32_16x16x32_bf16 v[84:87],a[84:87],v[248:251],v[84:87]\n"
"s_cmp_eq_u32 s42,0\n"
"s_cselect_b32 s72,0x3ff80,s23\n"
"v_lshrrev_b32 v126,6,v112\n"
"v_mfma_f32_16x16x32_bf16 v[84:87],a[92:95],v[252:255],v[84:87]\n"
"s_add_u32 s91,s61,2097152\n"
"v_and_b32 v103,0x30,v112\n"
"v_lshl_add_u32 v126,v126,10,v103\n"
"v_add_u32 v117,16384,v126\n"
"v_mfma_f32_16x16x32_bf16 v[80:83],a[88:91],v[252:255],v[80:83]\n"
"ds_read_b128 v[252:255],v113 offset:31744\n"
"s_mov_b32 s53,s56\n"
"v_mfma_f32_16x16x32_bf16 v[80:83],a[80:83],v[248:251],v[80:83]\n"
"ds_read_b128 v[248:251],v113 offset:30720\n"
"s_mov_b32 s56,s58\n"
"v_mfma_f32_16x16x32_bf16 v[80:83],a[72:75],v[244:247],v[80:83]\n"
"ds_read_b128 v[244:247],v113 offset:29696\n"
"s_mov_b32 s58,s59\n"
"v_mfma_f32_16x16x32_bf16 v[80:83],a[156:159],v[240:243],v[80:83]\n"
"ds_read_b128 v[240:243],v113 offset:28672\n"
"s_waitcnt lgkmcnt(12)\n"
"v_mfma_f32_16x16x32_bf16 v[88:91],v[128:131],v[192:195],v[80:83]\n"
"s_waitcnt vmcnt(13)\n"
"ds_add_u32 v114,v116\n"
"v_mfma_f32_16x16x32_bf16 v[88:91],v[136:139],v[196:199],v[88:91]\n"
"v_mfma_f32_16x16x32_bf16 v[88:91],v[144:147],v[200:203],v[88:91]\n"
"v_mfma_f32_16x16x32_bf16 v[88:91],v[152:155],v[204:207],v[88:91]\n"
"s_add_u32 s83,s20,32768\n"
"s_and_b32 s83,s83,0x1ffff\n"
"v_add_u32 v113,s83,v112\n"
"v_mfma_f32_16x16x32_bf16 v[92:95],v[156:159],v[204:207],v[84:87]\n"
"v_mfma_f32_16x16x32_bf16 v[92:95],v[148:151],v[200:203],v[92:95]\n"
"v_mfma_f32_16x16x32_bf16 v[92:95],v[140:143],v[196:199],v[92:95]\n"
"v_mfma_f32_16x16x32_bf16 v[92:95],v[132:135],v[192:195],v[92:95]\n"
"s_waitcnt lgkmcnt(9)\n"
"v_mfma_f32_16x16x32_bf16 v[92:95],v[4:7],v[208:211],v[92:95]\n"
"v_mfma_f32_16x16x32_bf16 v[92:95],v[12:15],v[212:215],v[92:95]\n"
"ds_read_b32 v125,v114\n"
"v_mfma_f32_16x16x32_bf16 v[92:95],v[164:167],v[216:219],v[92:95]\n"
"v_mfma_f32_16x16x32_bf16 v[92:95],v[172:175],v[220:223],v[92:95]\n"
"v_mfma_f32_16x16x32_bf16 v[88:91],v[168:171],v[220:223],v[88:91]\n"
"v_mfma_f32_16x16x32_bf16 v[88:91],v[160:163],v[216:219],v[88:91]\n"
"s_waitcnt lgkmcnt(0)\n"
"v_cmp_gt_u32 vcc,s16,v125\n"
"v_mfma_f32_16x16x32_bf16 v[88:91],v[8:11],v[212:215],v[88:91]\n"
"v_mfma_f32_16x16x32_bf16 v[88:91],v[0:3],v[208:211],v[88:91]\n"
"s_cbranch_vccnz .Lfk_es_me3\n"
".Lfk_er_me3:\n"
"ds_read_b128 v[192:195],v113 offset:0\n"
"v_mfma_f32_16x16x32_bf16 v[88:91],v[176:179],v[224:227],v[88:91]\n"
"buffer_load_dwordx4 a[96:99],v112,s[36:39],s61 offen offset:0\n"
"v_mfma_f32_16x16x32_bf16 v[88:91],v[184:187],v[228:231],v[88:91]\n"
"ds_read_b128 v[196:199],v113 offset:1024\n"
"v_lshrrev_b32 v101,4,v112\n"
"v_mfma_f32_16x16x32_bf16 v[88:91],a[48:51],v[232:235],v[88:91]\n"
"ds_read_b128 v[200:203],v113 offset:2048\n"
"v_and_b32 v102,15,v101\n"
"v_mfma_f32_16x16x32_bf16 v[88:91],a[56:59],v[236:239],v[88:91]\n"
"ds_read_b128 v[204:207],v113 offset:3072\n"
"buffer_load_dwordx4 a[100:103],v112,s[36:39],s61 offen offset:1024\n"
"v_mfma_f32_16x16x32_bf16 v[92:95],a[60:63],v[236:239],v[92:95]\n"
"ds_read_b128 v[208:211],v113 offset:4096\n"
"v_lshrrev_b32 v101,4,v101\n"
"v_mfma_f32_16x16x32_bf16 v[92:95],a[52:55],v[232:235],v[92:95]\n"
"ds_read_b128 v[212:215],v113 offset:5120\n"
"s_add_u32 s16,s16,4\n"
"v_mfma_f32_16x16x32_bf16 v[92:95],v[188:191],v[228:231],v[92:95]\n"
"ds_read_b128 v[216:219],v113 offset:6144\n"
"buffer_load_dwordx4 a[104:107],v112,s[36:39],s91 offen offset:0\n"
"v_mfma_f32_16x16x32_bf16 v[92:95],v[180:183],v[224:227],v[92:95]\n"
"ds_read_b128 v[220:223],v113 offset:7168\n"
"v_lshlrev_b32 v101,2,v101\n"
"v_mfma_f32_16x16x32_bf16 v[92:95],v[52:55],v[240:243],v[92:95]\n"
"ds_read_b128 v[236:239],v113 offset:11264\n"
"v_sub_u32 v101,v102,v101\n"
"v_mfma_f32_16x16x32_bf16 v[92:95],v[60:63],v[244:247],v[92:95]\n"
"ds_read_b128 v[232:235],v113 offset:10240\n"
"buffer_load_dwordx4 a[108:111],v112,s[36:39],s91 offen offset:1024\n"
"v_mfma_f32_16x16x32_bf16 v[92:95],v[20:23],v[248:251],v[92:95]\n"
"ds_read_b128 v[228:231],v113 offset:9216\n"
"v_mov_b32 v64,0\n"
"v_mfma_f32_16x16x32_bf16 v[92:95],v[28:31],v[252:255],v[92:95]\n"
"ds_read_b128 v[224:227],v113 offset:8192\n"
"v_mov_b32 v65,0\n"
"v_mfma_f32_16x16x32_bf16 v[88:91],v[24:27],v[252:255],v[88:91]\n"
"ds_read_b128 v[252:255],v113 offset:15360\n"
"buffer_load_dwordx4 v[72:75],v112,s[44:47],0 offen offset:0\n"
"v_mfma_f32_16x16x32_bf16 v[88:91],v[16:19],v[248:251],v[88:91]\n"
"ds_read_b128 v[248:251],v113 offset:14336\n"
"v_mov_b32 v68,0\n"
"v_mfma_f32_16x16x32_bf16 v[88:91],v[56:59],v[244:247],v[88:91]\n"
"ds_read_b128 v[244:247],v113 offset:13312\n"
"v_mov_b32 v69,0\n"
"v_mfma_f32_16x16x32_bf16 v[88:91],v[48:51],v[240:243],v[88:91]\n"
"ds_read_b128 v[240:243],v113 offset:12288\n"
"buffer_load_dwordx4 v[76:79],v112,s[44:47],0 offen offset:1024\n"
"s_waitcnt lgkmcnt(12)\n"
"v_mfma_f32_16x16x32_bf16 v[80:83],a[224:227],v[192:195],0\n"
"v_mov_b32 v102,0x3f80\n"
"v_mov_b32 v103,0x3f800000\n"
"v_cmp_eq_u32 vcc,0,v101\n"
"v_mfma_f32_16x16x32_bf16 v[80:83],a[232:235],v[196:199],v[80:83]\n"
"s_nop 0\n"
"v_cndmask_b32 v66,0,v102,vcc\n"
"v_cmp_eq_u32 vcc,1,v101\n"
"v_mfma_f32_16x16x32_bf16 v[80:83],a[240:243],v[200:203],v[80:83]\n"
"s_mov_b32 s91,s62\n"
"v_cndmask_b32 v66,v66,v103,vcc\n"
"v_cmp_eq_u32 vcc,2,v101\n"
"v_mfma_f32_16x16x32_bf16 v[80:83],a[248:251],v[204:207],v[80:83]\n"
"s_nop 0\n"
"v_cndmask_b32 v67,0,v102,vcc\n"
"v_cvt_pk_bf16_f32 v88,v88,v89\n"
"v_cvt_pk_bf16_f32 v89,v90,v91\n"
"v_mfma_f32_16x16x32_bf16 v[84:87],a[252:255],v[204:207],0\n"
"ds_read_b128 v[204:207],v113 offset:19456\n"
"s_add_u32 s20,s20,32768\n"
"v_mfma_f32_16x16x32_bf16 v[84:87],a[244:247],v[200:203],v[84:87]\n"
"s_and_b32 s20,s20,0x1ffff\n"
"ds_read_b128 v[200:203],v113 offset:18432\n"
"buffer_load_dwordx4 a[112:115],v126,s[24:27],s91 offen sc1\n"
"v_mfma_f32_16x16x32_bf16 v[84:87],a[236:239],v[196:199],v[84:87]\n"
"ds_read_b128 v[196:199],v113 offset:17408\n"
"v_cvt_pk_bf16_f32 v90,v92,v93\n"
"v_mfma_f32_16x16x32_bf16 v[84:87],a[228:231],v[192:195],v[84:87]\n"
"ds_read_b128 v[192:195],v113 offset:16384\n"
"s_waitcnt lgkmcnt(12)\n"
"v_mfma_f32_16x16x32_bf16 v[84:87],a[4:7],v[208:211],v[84:87]\n"
"v_cvt_pk_bf16_f32 v91,v94,v95\n"
"v_max_u32 v88,1,v88\n"
"v_mfma_f32_16x16x32_bf16 v[84:87],a[12:15],v[212:215],v[84:87]\n"
"buffer_load_dwordx4 a[116:119],v117,s[24:27],s91 offen sc1\n"
"v_max_u32 v89,1,v89\n"
"v_max_u32 v90,1,v90\n"
"v_max_u32 v91,1,v91\n"
"v_mfma_f32_16x16x32_bf16 v[84:87],a[20:23],v[216:219],v[84:87]\n"
"buffer_store_dwordx4 v[88:91],v118,s[24:27],s53 offen sc1\n"
"v_cmp_eq_u32 vcc,3,v101\n"
"v_mfma_f32_16x16x32_bf16 v[84:87],a[28:31],v[220:223],v[84:87]\n"
"s_add_u32 s91,s91,4194304\n"
"v_cndmask_b32 v67,v67,v103,vcc\n"
"v_mfma_f32_16x16x32_bf16 v[80:83],a[24:27],v[220:223],v[80:83]\n"
"ds_read_b128 v[220:223],v113 offset:23552\n"
"v_mfma_f32_16x16x32_bf16 v[80:83],a[16:19],v[216:219],v[80:83]\n"
"ds_read_b128 v[216:219],v113 offset:22528\n"
"v_mfma_f32_16x16x32_bf16 v[80:83],a[8:11],v[212:215],v[80:83]\n"
"ds_read_b128 v[212:215],v113 offset:21504\n"
"buffer_load_dwordx4 a[120:123],v126,s[24:27],s91 offen sc1\n"
"v_mfma_f32_16x16x32_bf16 v[80:83],a[0:3],v[208:211],v[80:83]\n"
"ds_read_b128 v[208:211],v113 offset:20480\n"
"s_waitcnt lgkmcnt(12)\n"
"v_mfma_f32_16x16x32_bf16 v[80:83],v[32:35],v[224:227],v[80:83]\n"
"v_mfma_f32_16x16x32_bf16 v[80:83],v[40:43],v[228:231],v[80:83]\n"
"buffer_load_dwordx4 a[124:127],v117,s[24:27],s91 offen sc1\n"
"v_mfma_f32_16x16x32_bf16 v[80:83],a[32:35],v[232:235],v[80:83]\n"
"v_mfma_f32_16x16x32_bf16 v[80:83],a[40:43],v[236:239],v[80:83]\n"
"v_mfma_f32_16x16x32_bf16 v[84:87],a[44:47],v[236:239],v[84:87]\n"
"ds_read_b128 v[236:239],v113 offset:27648\n"
"s_add_u32 s91,s91,4194304\n"
"v_mfma_f32_16x16x32_bf16 v[84:87],a[36:39],v[232:235],v[84:87]\n"
"ds_read_b128 v[232:235],v113 offset:26624\n"
"v_mfma_f32_16x16x32_bf16 v[84:87],v[44:47],v[228:231],v[84:87]\n"
"ds_read_b128 v[228:231],v113 offset:25600\n"
"v_mfma_f32_16x16x32_bf16 v[84:87],v[36:39],v[224:227],v[84:87]\n"
"ds_read_b128 v[224:227],v113 offset:24576\n"
"buffer_load_dwordx4 a[128:131],v126,s[24:27],s91 offen sc1\n"
"s_waitcnt lgkmcnt(12)\n"
"v_mfma_f32_16x16x32_bf16 v[84:87],a[68:71],v[240:243],v[84:87]\n"
"v_mfma_f32_16x16x32_bf16 v[84:87],a[76:79],v[244:247],v[84:87]\n"
"v_mfma_f32_16x16x32_bf16 v[84:87],a[84:87],v[248:251],v[84:87]\n"
"buffer_load_dwordx4 a[132:135],v117,s[24:27],s91 offen sc1\n"
"v_mfma_f32_16x16x32_bf16 v[84:87],a[92:95],v[252:255],v[84:87]\n"
"v_mfma_f32_16x16x32_bf16 v[80:83],a[88:91],v[252:255],v[80:83]\n"
"ds_read_b128 v[252:255],v113 offset:31744\n"
"s_mov_b32 s53,s56\n"
"v_mfma_f32_16x16x32_bf16 v[80:83],a[80:83],v[248:251],v[80:83]\n"
"s_mov_b32 s56,s58\n"
"ds_read_b128 v[248:251],v113 offset:30720\n"
"s_add_u32 s91,s91,4194304\n"
"v_mfma_f32_16x16x32_bf16 v[80:83],a[72:75],v[244:247],v[80:83]\n"
"ds_read_b128 v[244:247],v113 offset:29696\n"
"s_mov_b32 s58,s59\n"
"v_mfma_f32_16x16x32_bf16 v[80:83],a[156:159],v[240:243],v[80:83]\n"
"ds_read_b128 v[240:243],v113 offset:28672\n"
"s_waitcnt lgkmcnt(12)\n"
"v_mfma_f32_16x16x32_bf16 v[88:91],v[128:131],v[192:195],v[80:83]\n"
"s_waitcnt vmcnt(16)\n"
"ds_add_u32 v114,v116\n"
"v_mfma_f32_16x16x32_bf16 v[88:91],v[136:139],v[196:199],v[88:91]\n"
"buffer_load_dwordx4 a[136:139],v126,s[24:27],s91 offen sc1\n"
"v_mfma_f32_16x16x32_bf16 v[88:91],v[144:147],v[200:203],v[88:91]\n"
"s_add_u32 s83,s20,32768\n"
"s_and_b32 s83,s83,0x1ffff\n"
"v_mfma_f32_16x16x32_bf16 v[88:91],v[152:155],v[204:207],v[88:91]\n"
"buffer_load_dwordx4 a[140:143],v117,s[24:27],s91 offen sc1\n"
"v_mfma_f32_16x16x32_bf16 v[92:95],v[156:159],v[204:207],v[84:87]\n"
"v_add_u32 v113,s83,v112\n"
"v_mfma_f32_16x16x32_bf16 v[92:95],v[148:151],v[200:203],v[92:95]\n"
"v_mfma_f32_16x16x32_bf16 v[92:95],v[140:143],v[196:199],v[92:95]\n"
"v_mfma_f32_16x16x32_bf16 v[92:95],v[132:135],v[192:195],v[92:95]\n"
"s_waitcnt lgkmcnt(9)\n"
"v_mfma_f32_16x16x32_bf16 v[92:95],v[4:7],v[208:211],v[92:95]\n"
"v_mfma_f32_16x16x32_bf16 v[92:95],v[12:15],v[212:215],v[92:95]\n"
"ds_read_b32 v125,v114\n"
"v_mfma_f32_16x16x32_bf16 v[92:95],v[164:167],v[216:219],v[92:95]\n"
"v_mfma_f32_16x16x32_bf16 v[92:95],v[172:175],v[220:223],v[92:95]\n"
"v_mfma_f32_16x16x32_bf16 v[88:91],v[168:171],v[220:223],v[88:91]\n"
"v_mfma_f32_16x16x32_bf16 v[88:91],v[160:163],v[216:219],v[88:91]\n"
"s_waitcnt lgkmcnt(0)\n"
"v_cmp_gt_u32 vcc,s16,v125\n"
"v_mfma_f32_16x16x32_bf16 v[88:91],v[8:11],v[212:215],v[88:91]\n"
"v_mfma_f32_16x16x32_bf16 v[88:91],v[0:3],v[208:211],v[88:91]\n"
"s_cbranch_vccnz .Lfk_es_me2\n"
".Lfk_er_me2:\n"
"v_mfma_f32_16x16x32_bf16 v[88:91],v[176:179],v[224:227],v[88:91]\n"
"ds_read_b128 v[192:195],v113 offset:0\n"
"v_mfma_f32_16x16x32_bf16 v[88:91],v[184:187],v[228:231],v[88:91]\n"
"ds_read_b128 v[196:199],v113 offset:1024\n"
"v_mfma_f32_16x16x32_bf16 v[88:91],a[48:51],v[232:235],v[88:91]\n"
"ds_read_b128 v[200:203],v113 offset:2048\n"
"v_mfma_f32_16x16x32_bf16 v[88:91],a[56:59],v[236:239],v[88:91]\n"
"ds_read_b128 v[204:207],v113 offset:3072\n"
"v_mfma_f32_16x16x32_bf16 v[92:95],a[60:63],v[236:239],v[92:95]\n"
"ds_read_b128 v[208:211],v113 offset:4096\n"
"v_mfma_f32_16x16x32_bf16 v[92:95],a[52:55],v[232:235],v[92:95]\n"
"ds_read_b128 v[212:215],v113 offset:5120\n"
"v_mfma_f32_16x16x32_bf16 v[92:95],v[188:191],v[228:231],v[92:95]\n"
"ds_read_b128 v[216:219],v113 offset:6144\n"
"v_mfma_f32_16x16x32_bf16 v[92:95],v[180:183],v[224:227],v[92:95]\n"
"ds_read_b128 v[220:223],v113 offset:7168\n"
"v_mfma_f32_16x16x32_bf16 v[92:95],v[52:55],v[240:243],v[92:95]\n"
"ds_read_b128 v[236:239],v113 offset:11264\n"
"v_mfma_f32_16x16x32_bf16 v[92:95],v[60:63],v[244:247],v[92:95]\n"
"ds_read_b128 v[232:235],v113 offset:10240\n"
"v_mfma_f32_16x16x32_bf16 v[92:95],v[20:23],v[248:251],v[92:95]\n"
"ds_read_b128 v[228:231],v113 offset:9216\n"
"v_mfma_f32_16x16x32_bf16 v[92:95],v[28:31],v[252:255],v[92:95]\n"
"ds_read_b128 v[224:227],v113 offset:8192\n"
"v_mfma_f32_16x16x32_bf16 v[88:91],v[24:27],v[252:255],v[88:91]\n"
"ds_read_b128 v[252:255],v113 offset:15360\n"
"v_mfma_f32_16x16x32_bf16 v[88:91],v[16:19],v[248:251],v[88:91]\n"
"ds_read_b128 v[248:251],v113 offset:14336\n"
"v_mfma_f32_16x16x32_bf16 v[88:91],v[56:59],v[244:247],v[88:91]\n"
"ds_read_b128 v[244:247],v113 offset:13312\n"
"v_mfma_f32_16x16x32_bf16 v[88:91],v[48:51],v[240:243],v[88:91]\n"
"ds_read_b128 v[240:243],v113 offset:12288\n"
"s_waitcnt lgkmcnt(12)\n"
"v_mfma_f32_16x16x32_bf16 v[80:83],a[224:227],v[192:195],0\n"
"v_mfma_f32_16x16x32_bf16 v[80:83],a[232:235],v[196:199],v[80:83]\n"
"v_mfma_f32_16x16x32_bf16 v[80:83],a[240:243],v[200:203],v[80:83]\n"
"s_nop 2\n"
"v_cvt_pk_bf16_f32 v88,v88,v89\n"
"v_mfma_f32_16x16x32_bf16 v[80:83],a[248:251],v[204:207],v[80:83]\n"
"v_cvt_pk_bf16_f32 v89,v90,v91\n"
"v_mfma_f32_16x16x32_bf16 v[84:87],a[252:255],v[204:207],0\n"
"ds_read_b128 v[204:207],v113 offset:19456\n"
"v_cvt_pk_bf16_f32 v90,v92,v93\n"
"v_mfma_f32_16x16x32_bf16 v[84:87],a[244:247],v[200:203],v[84:87]\n"
"ds_read_b128 v[200:203],v113 offset:18432\n"
"v_cvt_pk_bf16_f32 v91,v94,v95\n"
"v_mfma_f32_16x16x32_bf16 v[84:87],a[236:239],v[196:199],v[84:87]\n"
"ds_read_b128 v[196:199],v113 offset:17408\n"
"v_max_u32 v88,1,v88\n"
"v_mfma_f32_16x16x32_bf16 v[84:87],a[228:231],v[192:195],v[84:87]\n"
"ds_read_b128 v[192:195],v113 offset:16384\n"
"s_waitcnt lgkmcnt(12)\n"
"v_mfma_f32_16x16x32_bf16 v[84:87],a[4:7],v[208:211],v[84:87]\n"
"v_max_u32 v89,1,v89\n"
"v_max_u32 v90,1,v90\n"
"v_max_u32 v91,1,v91\n"
"v_mfma_f32_16x16x32_bf16 v[84:87],a[12:15],v[212:215],v[84:87]\n"
"buffer_store_dwordx4 v[88:91],v118,s[24:27],s53 offen sc1\n"
"v_mfma_f32_16x16x32_bf16 v[84:87],a[20:23],v[216:219],v[84:87]\n"
"v_mfma_f32_16x16x32_bf16 v[84:87],a[28:31],v[220:223],v[84:87]\n"
"v_mfma_f32_16x16x32_bf16 v[80:83],a[24:27],v[220:223],v[80:83]\n"
"ds_read_b128 v[220:223],v113 offset:23552\n"
"v_mfma_f32_16x16x32_bf16 v[80:83],a[16:19],v[216:219],v[80:83]\n"
"ds_read_b128 v[216:219],v113 offset:22528\n"
"v_mfma_f32_16x16x32_bf16 v[80:83],a[8:11],v[212:215],v[80:83]\n"
"ds_read_b128 v[212:215],v113 offset:21504\n"
"v_mfma_f32_16x16x32_bf16 v[80:83],a[0:3],v[208:211],v[80:83]\n"
"ds_read_b128 v[208:211],v113 offset:20480\n"
"s_waitcnt lgkmcnt(12)\n"
"v_mfma_f32_16x16x32_bf16 v[80:83],v[32:35],v[224:227],v[80:83]\n"
"v_mfma_f32_16x16x32_bf16 v[80:83],v[40:43],v[228:231],v[80:83]\n"
"v_mfma_f32_16x16x32_bf16 v[80:83],a[32:35],v[232:235],v[80:83]\n"
"v_mfma_f32_16x16x32_bf16 v[80:83],a[40:43],v[236:239],v[80:83]\n"
"v_mfma_f32_16x16x32_bf16 v[84:87],a[44:47],v[236:239],v[84:87]\n"
"ds_read_b128 v[236:239],v113 offset:27648\n"
"v_mfma_f32_16x16x32_bf16 v[84:87],a[36:39],v[232:235],v[84:87]\n"
"ds_read_b128 v[232:235],v113 offset:26624\n"
"v_mfma_f32_16x16x32_bf16 v[84:87],v[44:47],v[228:231],v[84:87]\n"
"ds_read_b128 v[228:231],v113 offset:25600\n"
"v_mfma_f32_16x16x32_bf16 v[84:87],v[36:39],v[224:227],v[84:87]\n"
"ds_read_b128 v[224:227],v113 offset:24576\n"
"s_waitcnt lgkmcnt(12)\n"
"v_mfma_f32_16x16x32_bf16 v[84:87],a[68:71],v[240:243],v[84:87]\n"
"v_mfma_f32_16x16x32_bf16 v[84:87],a[76:79],v[244:247],v[84:87]\n"
"v_mfma_f32_16x16x32_bf16 v[84:87],a[84:87],v[248:251],v[84:87]\n"
"v_mfma_f32_16x16x32_bf16 v[84:87],a[92:95],v[252:255],v[84:87]\n"
"v_mfma_f32_16x16x32_bf16 v[80:83],a[88:91],v[252:255],v[80:83]\n"
"ds_read_b128 v[252:255],v113 offset:31744\n"
"s_mov_b32 s53,s56\n"
"v_mfma_f32_16x16x32_bf16 v[80:83],a[80:83],v[248:251],v[80:83]\n"
"ds_read_b128 v[248:251],v113 offset:30720\n"
"s_mov_b32 s56,s58\n"
"v_mfma_f32_16x16x32_bf16 v[80:83],a[72:75],v[244:247],v[80:83]\n"
"ds_read_b128 v[244:247],v113 offset:29696\n"
"s_mov_b32 s58,s59\n"
"v_mfma_f32_16x16x32_bf16 v[80:83],a[156:159],v[240:243],v[80:83]\n"
"ds_read_b128 v[240:243],v113 offset:28672\n"
"s_waitcnt lgkmcnt(12)\n"
"v_mfma_f32_16x16x32_bf16 v[88:91],v[128:131],v[192:195],v[80:83]\n"
"v_mfma_f32_16x16x32_bf16 v[88:91],v[136:139],v[196:199],v[88:91]\n"
"v_mfma_f32_16x16x32_bf16 v[88:91],v[144:147],v[200:203],v[88:91]\n"
"v_mfma_f32_16x16x32_bf16 v[88:91],v[152:155],v[204:207],v[88:91]\n"
"v_mfma_f32_16x16x32_bf16 v[92:95],v[156:159],v[204:207],v[84:87]\n"
"v_mfma_f32_16x16x32_bf16 v[92:95],v[148:151],v[200:203],v[92:95]\n"
"v_mfma_f32_16x16x32_bf16 v[92:95],v[140:143],v[196:199],v[92:95]\n"
"v_mfma_f32_16x16x32_bf16 v[92:95],v[132:135],v[192:195],v[92:95]\n"
"s_waitcnt lgkmcnt(8)\n"
"v_mfma_f32_16x16x32_bf16 v[92:95],v[4:7],v[208:211],v[92:95]\n"
"v_mfma_f32_16x16x32_bf16 v[92:95],v[12:15],v[212:215],v[92:95]\n"
"v_mfma_f32_16x16x32_bf16 v[92:95],v[164:167],v[216:219],v[92:95]\n"
"v_mfma_f32_16x16x32_bf16 v[92:95],v[172:175],v[220:223],v[92:95]\n"
"v_mfma_f32_16x16x32_bf16 v[88:91],v[168:171],v[220:223],v[88:91]\n"
"s_mov_b32 s91,s62\n"
"v_mfma_f32_16x16x32_bf16 v[88:91],v[160:163],v[216:219],v[88:91]\n"
"v_mfma_f32_16x16x32_bf16 v[88:91],v[8:11],v[212:215],v[88:91]\n"
"v_mfma_f32_16x16x32_bf16 v[88:91],v[0:3],v[208:211],v[88:91]\n"
"s_waitcnt lgkmcnt(0)\n"
"s_waitcnt vmcnt(1)\n"
"v_mfma_f32_16x16x32_bf16 v[88:91],v[176:179],v[224:227],v[88:91]\n"
"v_accvgpr_read_b32 v128,a112\n"
"v_accvgpr_read_b32 v129,a113\n"
"v_mfma_f32_16x16x32_bf16 v[88:91],v[184:187],v[228:231],v[88:91]\n"
"v_accvgpr_read_b32 v130,a114\n"
"v_accvgpr_read_b32 v131,a115\n"
"v_mfma_f32_16x16x32_bf16 v[88:91],a[48:51],v[232:235],v[88:91]\n"
"v_accvgpr_read_b32 v132,a116\n"
"v_accvgpr_read_b32 v133,a117\n"
"v_mfma_f32_16x16x32_bf16 v[88:91],a[56:59],v[236:239],v[88:91]\n"
"v_accvgpr_read_b32 v134,a118\n"
"v_accvgpr_read_b32 v135,a119\n"
"v_mfma_f32_16x16x32_bf16 v[92:95],a[60:63],v[236:239],v[92:95]\n"
"v_accvgpr_read_b32 v136,a120\n"
"v_accvgpr_read_b32 v137,a121\n"
"v_mfma_f32_16x16x32_bf16 v[92:95],a[52:55],v[232:235],v[92:95]\n"
"v_accvgpr_read_b32 v138,a122\n"
"v_accvgpr_read_b32 v139,a123\n"
"v_mfma_f32_16x16x32_bf16 v[92:95],v[188:191],v[228:231],v[92:95]\n"
"v_accvgpr_read_b32 v140,a124\n"
"v_accvgpr_read_b32 v141,a125\n"
"v_mfma_f32_16x16x32_bf16 v[92:95],v[180:183],v[224:227],v[92:95]\n"
"v_accvgpr_read_b32 v142,a126\n"
"v_accvgpr_read_b32 v143,a127\n"
"v_mfma_f32_16x16x32_bf16 v[92:95],v[52:55],v[240:243],v[92:95]\n"
"v_accvgpr_read_b32 v144,a128\n"
"v_accvgpr_read_b32 v145,a129\n"
"v_mfma_f32_16x16x32_bf16 v[92:95],v[60:63],v[244:247],v[92:95]\n"
"v_accvgpr_read_b32 v146,a130\n"
"v_accvgpr_read_b32 v147,a131\n"
"v_mfma_f32_16x16x32_bf16 v[92:95],v[20:23],v[248:251],v[92:95]\n"
"v_accvgpr_read_b32 v148,a132\n"
"v_accvgpr_read_b32 v149,a133\n"
"v_mfma_f32_16x16x32_bf16 v[92:95],v[28:31],v[252:255],v[92:95]\n"
"v_accvgpr_read_b32 v150,a134\n"
"v_accvgpr_read_b32 v151,a135\n"
"v_mfma_f32_16x16x32_bf16 v[88:91],v[24:27],v[252:255],v[88:91]\n"
"v_accvgpr_read_b32 v152,a136\n"
"v_accvgpr_read_b32 v153,a137\n"
"v_mfma_f32_16x16x32_bf16 v[88:91],v[16:19],v[248:251],v[88:91]\n"
"v_accvgpr_read_b32 v154,a138\n"
"v_accvgpr_read_b32 v155,a139\n"
"v_mfma_f32_16x16x32_bf16 v[88:91],v[56:59],v[244:247],v[88:91]\n"
"v_accvgpr_read_b32 v156,a140\n"
"v_accvgpr_read_b32 v157,a141\n"
"v_mfma_f32_16x16x32_bf16 v[88:91],v[48:51],v[240:243],v[88:91]\n"
"v_accvgpr_read_b32 v158,a142\n"
"v_accvgpr_read_b32 v159,a143\n"
".Lfk_tail:\n"
"v_lshlrev_b32 v224,16,v72\n"
"v_and_b32 v225,0xffff0000,v72\n"
"v_lshlrev_b32 v226,16,v73\n"
"v_and_b32 v227,0xffff0000,v73\n"
"v_lshlrev_b32 v228,16,v74\n"
"s_nop 0\n"
"v_cvt_pk_bf16_f32 v88,v88,v89\n"
"v_cvt_pk_bf16_f32 v89,v90,v91\n"
"v_cvt_pk_bf16_f32 v90,v92,v93\n"
"v_cvt_pk_bf16_f32 v91,v94,v95\n"
"v_max_u32 v88,1,v88\n"
"v_max_u32 v89,1,v89\n"
"v_max_u32 v90,1,v90\n"
"v_max_u32 v91,1,v91\n"
"v_mfma_f32_16x16x32_bf16 v[0:3],v[66:69],a[96:99],0\n"
"buffer_store_dwordx4 v[88:91],v118,s[24:27],s53 offen sc1\n"
"v_mfma_f32_16x16x32_bf16 v[4:7],v[64:67],a[96:99],0\n"
"v_and_b32 v231,0xffff0000,v75\n"
"v_lshlrev_b32 v232,16,v76\n"
"v_mfma_f32_16x16x32_bf16 v[8:11],v[66:69],a[100:103],0\n"
"v_and_b32 v229,0xffff0000,v74\n"
"v_lshlrev_b32 v230,16,v75\n"
"v_mfma_f32_16x16x32_bf16 v[12:15],v[64:67],a[100:103],0\n"
"s_mov_b32 s67,0x3a800000\n"
"v_and_b32 v233,0xffff0000,v76\n"
"v_lshlrev_b32 v234,16,v77\n"
"v_and_b32 v235,0xffff0000,v77\n"
"v_lshlrev_b32 v236,16,v78\n"
"v_and_b32 v237,0xffff0000,v78\n"
"v_lshlrev_b32 v238,16,v79\n"
"v_and_b32 v239,0xffff0000,v79\n"
"v_pk_add_f32 v[70:71],v[224:225],v[226:227]\n"
"v_pk_add_f32 v[70:71],v[70:71],v[228:229]\n"
"v_pk_add_f32 v[70:71],v[70:71],v[230:231]\n"
"v_pk_add_f32 v[70:71],v[70:71],v[232:233]\n"
"v_pk_add_f32 v[70:71],v[70:71],v[234:235]\n"
"v_pk_add_f32 v[70:71],v[70:71],v[236:237]\n"
"v_pk_add_f32 v[70:71],v[70:71],v[238:239]\n"
"v_add_f32 v70,v70,v71\n"
"s_nop 1\n"
"v_add_f32_dpp v70,v70,v70 quad_perm:[1,0,3,2] row_mask:0xf bank_mask:0xf\n"
"s_nop 1\n"
"v_add_f32_dpp v70,v70,v70 quad_perm:[2,3,0,1] row_mask:0xf bank_mask:0xf\n"
"s_nop 1\n"
"v_add_f32_dpp v70,v70,v70 row_half_mirror row_mask:0xf bank_mask:0xf\n"
"s_nop 1\n"
"v_add_f32_dpp v70,v70,v70 row_mirror row_mask:0xf bank_mask:0xf\n"
"v_mov_b32 v71,v70\n"
"s_nop 1\n"
"v_permlane16_swap_b32 v71,v70\n"
"v_add_f32 v70,v70,v71\n"
"v_mov_b32 v71,v70\n"
"s_nop 1\n"
"v_permlane32_swap_b32 v71,v70\n"
"v_add_f32 v70,v70,v71\n"
"v_min3_u32 v36,v128,v129,v130\n"
"v_min3_u32 v37,v131,v132,v133\n"
"v_min3_u32 v36,v36,v134,v135\n"
"v_min3_u32 v37,v37,v136,v137\n"
"v_min3_u32 v36,v36,v138,v139\n"
"v_min3_u32 v37,v37,v140,v141\n"
"v_min3_u32 v36,v36,v142,v143\n"
"v_min3_u32 v37,v37,v144,v145\n"
"v_min3_u32 v36,v36,v146,v147\n"
"v_min3_u32 v37,v37,v148,v149\n"
"v_min3_u32 v36,v36,v150,v151\n"
"v_min3_u32 v37,v37,v152,v153\n"
"v_min3_u32 v36,v36,v154,v155\n"
"v_min3_u32 v37,v37,v156,v157\n"
"v_min3_u32 v36,v36,v158,v159\n"
"v_min_u32 v36,v36,v37\n"
"v_cmp_eq_u32 vcc,0,v36\n"
"s_nop 0\n"
"s_cbranch_vccz .Lfk_t0\n"
"s_mov_b32 s84,0x40000\n"
"s_mov_b32 s91,s62\n"
"buffer_load_dwordx4 v[128:131],v126,s[24:27],s91 offen sc1\n"
"buffer_load_dwordx4 v[132:135],v117,s[24:27],s91 offen sc1\n"
"s_add_u32 s91,s91,4194304\n"
"buffer_load_dwordx4 v[136:139],v126,s[24:27],s91 offen sc1\n"
"buffer_load_dwordx4 v[140:143],v117,s[24:27],s91 offen sc1\n"
"s_add_u32 s91,s91,4194304\n"
"buffer_load_dwordx4 v[144:147],v126,s[24:27],s91 offen sc1\n"
"buffer_load_dwordx4 v[148:151],v117,s[24:27],s91 offen sc1\n"
"s_add_u32 s91,s91,4194304\n"
"buffer_load_dwordx4 v[152:155],v126,s[24:27],s91 offen sc1\n"
"buffer_load_dwordx4 v[156:159],v117,s[24:27],s91 offen sc1\n"
".Lfk_wa:\n"
"s_waitcnt vmcnt(0)\n"
"v_min3_u32 v36,v128,v129,v130\n"
"v_min3_u32 v37,v131,v132,v133\n"
"v_min3_u32 v36,v36,v134,v135\n"
"v_min3_u32 v37,v37,v136,v137\n"
"v_min3_u32 v36,v36,v138,v139\n"
"v_min3_u32 v37,v37,v140,v141\n"
"v_min3_u32 v36,v36,v142,v143\n"
"v_min3_u32 v37,v37,v144,v145\n"
"v_min3_u32 v36,v36,v146,v147\n"
"v_min3_u32 v37,v37,v148,v149\n"
"v_min3_u32 v36,v36,v150,v151\n"
"v_min3_u32 v37,v37,v152,v153\n"
"v_min3_u32 v36,v36,v154,v155\n"
"v_min3_u32 v37,v37,v156,v157\n"
"v_min3_u32 v36,v36,v158,v159\n"
"v_min_u32 v36,v36,v37\n"
"v_cmp_eq_u32 vcc,0,v36\n"
"s_nop 0\n"
"s_cbranch_vccz .Lfk_wa_x\n"
"s_sleep 2\n"
"s_mov_b64 exec,vcc\n"
"s_mov_b32 s91,s62\n"
"buffer_load_dwordx4 v[128:131],v126,s[24:27],s91 offen sc1\n"
"buffer_load_dwordx4 v[132:135],v117,s[24:27],s91 offen sc1\n"
"s_add_u32 s91,s91,4194304\n"
"buffer_load_dwordx4 v[136:139],v126,s[24:27],s91 offen sc1\n"
"buffer_load_dwordx4 v[140:143],v117,s[24:27],s91 offen sc1\n"
"s_add_u32 s91,s91,4194304\n"
"buffer_load_dwordx4 v[144:147],v126,s[24:27],s91 offen sc1\n"
"buffer_load_dwordx4 v[148:151],v117,s[24:27],s91 offen sc1\n"
"s_add_u32 s91,s91,4194304\n"
"buffer_load_dwordx4 v[152:155],v126,s[24:27],s91 offen sc1\n"
"buffer_load_dwordx4 v[156:159],v117,s[24:27],s91 offen sc1\n"
"s_mov_b64 exec,-1\n"
"s_sub_u32 s84,s84,1\n"
"s_cbranch_scc0 .Lfk_wa\n"
".Lfk_wa_x:\n"
".Lfk_t0:\n"
"s_getpc_b64 s[94:95]\n"
".Lfk_rm0:\n"
"s_add_u32 s94,s94,.Lfk_rm1-.Lfk_rm0\n"
"v_mfma_f32_16x16x32_bf16 v[0:3],v[66:69],v[128:131],v[0:3]\n"
"s_addc_u32 s95,s95,0\n"
"v_mfma_f32_16x16x32_bf16 v[4:7],v[64:67],v[128:131],v[4:7]\n"
"v_mfma_f32_16x16x32_bf16 v[8:11],v[66:69],v[132:135],v[8:11]\n"
"v_mfma_f32_16x16x32_bf16 v[12:15],v[64:67],v[132:135],v[12:15]\n"
"v_mfma_f32_16x16x32_bf16 v[0:3],v[66:69],v[136:139],v[0:3]\n"
"v_mfma_f32_16x16x32_bf16 v[4:7],v[64:67],v[136:139],v[4:7]\n"
"v_mfma_f32_16x16x32_bf16 v[8:11],v[66:69],v[140:143],v[8:11]\n"
"v_mfma_f32_16x16x32_bf16 v[12:15],v[64:67],v[140:143],v[12:15]\n"
"v_mfma_f32_16x16x32_bf16 v[0:3],v[66:69],v[144:147],v[0:3]\n"
"v_mfma_f32_16x16x32_bf16 v[4:7],v[64:67],v[144:147],v[4:7]\n"
"v_mfma_f32_16x16x32_bf16 v[8:11],v[66:69],v[148:151],v[8:11]\n"
"v_mfma_f32_16x16x32_bf16 v[12:15],v[64:67],v[148:151],v[12:15]\n"
"v_mfma_f32_16x16x32_bf16 v[0:3],v[66:69],v[152:155],v[0:3]\n"
"v_mfma_f32_16x16x32_bf16 v[4:7],v[64:67],v[152:155],v[4:7]\n"
"v_mfma_f32_16x16x32_bf16 v[8:11],v[66:69],v[156:159],v[8:11]\n"
"v_mfma_f32_16x16x32_bf16 v[12:15],v[64:67],v[156:159],v[12:15]\n"
"s_nop 7\n"
".Lfk_rm:\n"
"v_readfirstlane_b32 s64,v0\n"
"s_nop 1\n"
"v_mov_b32 v38,s64\n"
"v_pk_add_f32 v[16:17],v[0:1],v[38:39] op_sel_hi:[1,0] neg_lo:[0,1] neg_hi:[0,1]\n"
"v_pk_add_f32 v[18:19],v[2:3],v[38:39] op_sel_hi:[1,0] neg_lo:[0,1] neg_hi:[0,1]\n"
"v_pk_add_f32 v[20:21],v[4:5],v[38:39] op_sel_hi:[1,0] neg_lo:[0,1] neg_hi:[0,1]\n"
"v_pk_add_f32 v[22:23],v[6:7],v[38:39] op_sel_hi:[1,0] neg_lo:[0,1] neg_hi:[0,1]\n"
"v_pk_add_f32 v[24:25],v[8:9],v[38:39] op_sel_hi:[1,0] neg_lo:[0,1] neg_hi:[0,1]\n"
"v_pk_add_f32 v[26:27],v[10:11],v[38:39] op_sel_hi:[1,0] neg_lo:[0,1] neg_hi:[0,1]\n"
"v_pk_add_f32 v[28:29],v[12:13],v[38:39] op_sel_hi:[1,0] neg_lo:[0,1] neg_hi:[0,1]\n"
"v_pk_add_f32 v[30:31],v[14:15],v[38:39] op_sel_hi:[1,0] neg_lo:[0,1] neg_hi:[0,1]\n"
"v_pk_add_f32 v[32:33],v[16:17],v[18:19]\n"
"v_pk_mul_f32 v[34:35],v[16:17],v[16:17]\n"
"v_pk_mul_f32 v[36:37],v[16:17],v[224:225]\n"
"v_pk_fma_f32 v[34:35],v[18:19],v[18:19],v[34:35]\n"
"v_pk_fma_f32 v[36:37],v[18:19],v[226:227],v[36:37]\n"
"v_pk_add_f32 v[32:33],v[32:33],v[20:21]\n"
"v_pk_fma_f32 v[34:35],v[20:21],v[20:21],v[34:35]\n"
"v_pk_fma_f32 v[36:37],v[20:21],v[228:229],v[36:37]\n"
"v_pk_add_f32 v[32:33],v[32:33],v[22:23]\n"
"v_pk_fma_f32 v[34:35],v[22:23],v[22:23],v[34:35]\n"
"v_pk_fma_f32 v[36:37],v[22:23],v[230:231],v[36:37]\n"
"v_pk_add_f32 v[32:33],v[32:33],v[24:25]\n"
"v_pk_fma_f32 v[34:35],v[24:25],v[24:25],v[34:35]\n"
"v_pk_fma_f32 v[36:37],v[24:25],v[232:233],v[36:37]\n"
"v_pk_add_f32 v[32:33],v[32:33],v[26:27]\n"
"v_pk_fma_f32 v[34:35],v[26:27],v[26:27],v[34:35]\n"
"v_pk_fma_f32 v[36:37],v[26:27],v[234:235],v[36:37]\n"
"v_pk_add_f32 v[32:33],v[32:33],v[28:29]\n"
"v_pk_fma_f32 v[34:35],v[28:29],v[28:29],v[34:35]\n"
"v_pk_fma_f32 v[36:37],v[28:29],v[236:237],v[36:37]\n"
"v_pk_add_f32 v[32:33],v[32:33],v[30:31]\n"
"v_pk_fma_f32 v[34:35],v[30:31],v[30:31],v[34:35]\n"
"v_pk_fma_f32 v[36:37],v[30:31],v[238:239],v[36:37]\n"
"v_add_f32 v32,v32,v33\n"
"v_add_f32 v33,v34,v35\n"
"v_add_f32 v34,v36,v37\n"
"v_add_f32_dpp v32,v32,v32 quad_perm:[1,0,3,2] row_mask:0xf bank_mask:0xf\n"
"v_add_f32_dpp v33,v33,v33 quad_perm:[1,0,3,2] row_mask:0xf bank_mask:0xf\n"
"v_add_f32_dpp v34,v34,v34 quad_perm:[1,0,3,2] row_mask:0xf bank_mask:0xf\n"
"v_add_f32_dpp v32,v32,v32 quad_perm:[2,3,0,1] row_mask:0xf bank_mask:0xf\n"
"v_add_f32_dpp v33,v33,v33 quad_perm:[2,3,0,1] row_mask:0xf bank_mask:0xf\n"
"v_add_f32_dpp v34,v34,v34 quad_perm:[2,3,0,1] row_mask:0xf bank_mask:0xf\n"
"v_add_f32_dpp v32,v32,v32 row_half_mirror row_mask:0xf bank_mask:0xf\n"
"v_add_f32_dpp v33,v33,v33 row_half_mirror row_mask:0xf bank_mask:0xf\n"
"v_add_f32_dpp v34,v34,v34 row_half_mirror row_mask:0xf bank_mask:0xf\n"
"v_add_f32_dpp v32,v32,v32 row_mirror row_mask:0xf bank_mask:0xf\n"
"v_add_f32_dpp v33,v33,v33 row_mirror row_mask:0xf bank_mask:0xf\n"
"v_add_f32_dpp v34,v34,v34 row_mirror row_mask:0xf bank_mask:0xf\n"
"v_add_f32_dpp v32,v32,v32 row_bcast:15 row_mask:0xa bank_mask:0xf\n"
"v_add_f32_dpp v33,v33,v33 row_bcast:15 row_mask:0xa bank_mask:0xf\n"
"v_add_f32_dpp v34,v34,v34 row_bcast:15 row_mask:0xa bank_mask:0xf\n"
"v_add_f32_dpp v32,v32,v32 row_bcast:31 row_mask:0xc bank_mask:0xf\n"
"v_add_f32_dpp v33,v33,v33 row_bcast:31 row_mask:0xc bank_mask:0xf\n"
"v_add_f32_dpp v34,v34,v34 row_bcast:31 row_mask:0xc bank_mask:0xf\n"
"v_mul_f32 v32,s67,v32\n"
"v_add_f32 v35,v32,v38\n"
"v_mul_f32 v32,v32,v32\n"
"v_cvt_pk_bf16_f32 v35,v35,v35\n"
"v_lshlrev_b32 v35,16,v35\n"
"v_fma_f32 v33,v33,s67,-v32\n"
"v_sub_f32 v36,v38,v35\n"
"v_max_f32 v33,0,v33\n"
"v_fmac_f32 v34,v36,v70\n"
"v_cvt_pk_bf16_f32 v33,v33,v33\n"
"v_lshlrev_b32 v33,16,v33\n"
"v_add_f32 v33,0x3727c5ac,v33\n"
"v_cvt_pk_bf16_f32 v33,v33,v33\n"
"v_lshlrev_b32 v33,16,v33\n"
"v_sqrt_f32 v33,v33\n"
"s_nop 0\n"
"v_cvt_pk_bf16_f32 v33,v33,v33\n"
"v_lshlrev_b32 v33,16,v33\n"
"v_rcp_f32 v36,v33\n"
"v_lshrrev_b32 v33,16,v33\n"
"v_mul_f32 v37,v34,v36\n"
"v_or_b32 v33,v33,v35\n"
"v_cvt_pk_bf16_f32 v37,v37,v37\n"
"v_ashrrev_i32 v36,31,v37\n"
"v_or_b32 v36,0x80000000,v36\n"
"v_xor_b32 v37,v37,v36\n"
"v_and_b32 v37,0xffff0000,v37\n"
"v_readlane_b32 s64,v33,63\n"
"v_readlane_b32 s63,v37,63\n"
"s_nop 1\n"
"s_setpc_b64 s[94:95]\n"
".Lfk_rm1:\n"
"s_add_u32 s91,s62,2097152\n"
"buffer_load_dwordx4 v[160:163],v126,s[24:27],s91 offen sc1\n"
"buffer_load_dwordx4 v[164:167],v117,s[24:27],s91 offen sc1\n"
"s_add_u32 s91,s91,4194304\n"
"buffer_load_dwordx4 v[168:171],v126,s[24:27],s91 offen sc1\n"
"buffer_load_dwordx4 v[172:175],v117,s[24:27],s91 offen sc1\n"
"s_add_u32 s91,s91,4194304\n"
"buffer_load_dwordx4 v[176:179],v126,s[24:27],s91 offen sc1\n"
"buffer_load_dwordx4 v[180:183],v117,s[24:27],s91 offen sc1\n"
"s_add_u32 s91,s91,4194304\n"
"buffer_load_dwordx4 v[184:187],v126,s[24:27],s91 offen sc1\n"
"buffer_load_dwordx4 v[188:191],v117,s[24:27],s91 offen sc1\n"
"s_sub_u32 s65,0xffff,s60\n"
"s_or_b32 s65,s65,s63\n"
"s_mov_b32 s66,s64\n"
"v_lshlrev_b32 v34,16,s64\n"
"v_mov_b32 v35,s64\n"
"v_rcp_f32 v34,v34\n"
"v_and_b32 v35,0xffff0000,v35\n"
"v_cvt_pk_bf16_f32 v240,v0,v1\n"
"v_cvt_pk_bf16_f32 v241,v2,v3\n"
"v_cvt_pk_bf16_f32 v242,v4,v5\n"
"v_cvt_pk_bf16_f32 v243,v6,v7\n"
"v_cvt_pk_bf16_f32 v244,v8,v9\n"
"v_cvt_pk_bf16_f32 v245,v10,v11\n"
"v_cvt_pk_bf16_f32 v246,v12,v13\n"
"v_cvt_pk_bf16_f32 v247,v14,v15\n"
"v_lshlrev_b32 v0,16,v240\n"
"v_and_b32 v1,0xffff0000,v240\n"
"v_lshlrev_b32 v2,16,v241\n"
"v_and_b32 v3,0xffff0000,v241\n"
"v_lshlrev_b32 v4,16,v242\n"
"v_and_b32 v5,0xffff0000,v242\n"
"v_lshlrev_b32 v6,16,v243\n"
"v_and_b32 v7,0xffff0000,v243\n"
"v_lshlrev_b32 v8,16,v244\n"
"v_and_b32 v9,0xffff0000,v244\n"
"v_lshlrev_b32 v10,16,v245\n"
"v_and_b32 v11,0xffff0000,v245\n"
"v_lshlrev_b32 v12,16,v246\n"
"v_and_b32 v13,0xffff0000,v246\n"
"v_lshlrev_b32 v14,16,v247\n"
"v_and_b32 v15,0xffff0000,v247\n"
"v_pk_add_f32 v[16:17],v[0:1],v[34:35] op_sel:[0,1] neg_lo:[0,1] neg_hi:[0,1]\n"
"v_pk_add_f32 v[18:19],v[2:3],v[34:35] op_sel:[0,1] neg_lo:[0,1] neg_hi:[0,1]\n"
"v_pk_add_f32 v[20:21],v[4:5],v[34:35] op_sel:[0,1] neg_lo:[0,1] neg_hi:[0,1]\n"
"v_pk_add_f32 v[22:23],v[6:7],v[34:35] op_sel:[0,1] neg_lo:[0,1] neg_hi:[0,1]\n"
"v_pk_add_f32 v[24:25],v[8:9],v[34:35] op_sel:[0,1] neg_lo:[0,1] neg_hi:[0,1]\n"
"v_pk_add_f32 v[26:27],v[10:11],v[34:35] op_sel:[0,1] neg_lo:[0,1] neg_hi:[0,1]\n"
"v_pk_add_f32 v[28:29],v[12:13],v[34:35] op_sel:[0,1] neg_lo:[0,1] neg_hi:[0,1]\n"
"v_pk_add_f32 v[30:31],v[14:15],v[34:35] op_sel:[0,1] neg_lo:[0,1] neg_hi:[0,1]\n"
"v_cvt_pk_bf16_f32 v240,v16,v17\n"
"v_cvt_pk_bf16_f32 v241,v18,v19\n"
"v_cvt_pk_bf16_f32 v242,v20,v21\n"
"v_cvt_pk_bf16_f32 v243,v22,v23\n"
"v_cvt_pk_bf16_f32 v244,v24,v25\n"
"v_cvt_pk_bf16_f32 v245,v26,v27\n"
"v_cvt_pk_bf16_f32 v246,v28,v29\n"
"v_cvt_pk_bf16_f32 v247,v30,v31\n"
"v_lshlrev_b32 v16,16,v240\n"
"v_and_b32 v17,0xffff0000,v240\n"
"v_lshlrev_b32 v18,16,v241\n"
"v_and_b32 v19,0xffff0000,v241\n"
"v_lshlrev_b32 v20,16,v242\n"
"v_and_b32 v21,0xffff0000,v242\n"
"v_lshlrev_b32 v22,16,v243\n"
"v_and_b32 v23,0xffff0000,v243\n"
"v_lshlrev_b32 v24,16,v244\n"
"v_and_b32 v25,0xffff0000,v244\n"
"v_lshlrev_b32 v26,16,v245\n"
"v_and_b32 v27,0xffff0000,v245\n"
"v_lshlrev_b32 v28,16,v246\n"
"v_and_b32 v29,0xffff0000,v246\n"
"v_lshlrev_b32 v30,16,v247\n"
"v_and_b32 v31,0xffff0000,v247\n"
"v_pk_mul_f32 v[16:17],v[16:17],v[34:35] op_sel_hi:[1,0]\n"
"v_pk_mul_f32 v[18:19],v[18:19],v[34:35] op_sel_hi:[1,0]\n"
"v_pk_mul_f32 v[20:21],v[20:21],v[34:35] op_sel_hi:[1,0]\n"
"v_pk_mul_f32 v[22:23],v[22:23],v[34:35] op_sel_hi:[1,0]\n"
"v_pk_mul_f32 v[24:25],v[24:25],v[34:35] op_sel_hi:[1,0]\n"
"v_pk_mul_f32 v[26:27],v[26:27],v[34:35] op_sel_hi:[1,0]\n"
"v_pk_mul_f32 v[28:29],v[28:29],v[34:35] op_sel_hi:[1,0]\n"
"v_pk_mul_f32 v[30:31],v[30:31],v[34:35] op_sel_hi:[1,0]\n"
"v_cvt_pk_bf16_f32 v240,v16,v17\n"
"v_cvt_pk_bf16_f32 v241,v18,v19\n"
"v_cvt_pk_bf16_f32 v242,v20,v21\n"
"v_cvt_pk_bf16_f32 v243,v22,v23\n"
"v_cvt_pk_bf16_f32 v244,v24,v25\n"
"v_cvt_pk_bf16_f32 v245,v26,v27\n"
"v_cvt_pk_bf16_f32 v246,v28,v29\n"
"v_cvt_pk_bf16_f32 v247,v30,v31\n"
"v_lshlrev_b32 v16,16,v240\n"
"v_and_b32 v17,0xffff0000,v240\n"
"v_lshlrev_b32 v18,16,v241\n"
"v_and_b32 v19,0xffff0000,v241\n"
"v_lshlrev_b32 v20,16,v242\n"
"v_and_b32 v21,0xffff0000,v242\n"
"v_lshlrev_b32 v22,16,v243\n"
"v_and_b32 v23,0xffff0000,v243\n"
"v_lshlrev_b32 v24,16,v244\n"
"v_and_b32 v25,0xffff0000,v244\n"
"v_lshlrev_b32 v26,16,v245\n"
"v_and_b32 v27,0xffff0000,v245\n"
"v_lshlrev_b32 v28,16,v246\n"
"v_and_b32 v29,0xffff0000,v246\n"
"v_lshlrev_b32 v30,16,v247\n"
"v_and_b32 v31,0xffff0000,v247\n"
"v_pk_add_f32 v[16:17],v[16:17],v[0:1]\n"
"v_pk_add_f32 v[18:19],v[18:19],v[2:3]\n"
"v_pk_add_f32 v[20:21],v[20:21],v[4:5]\n"
"v_pk_add_f32 v[22:23],v[22:23],v[6:7]\n"
"v_pk_add_f32 v[24:25],v[24:25],v[8:9]\n"
"v_pk_add_f32 v[26:27],v[26:27],v[10:11]\n"
"v_pk_add_f32 v[28:29],v[28:29],v[12:13]\n"
"v_pk_add_f32 v[30:31],v[30:31],v[14:15]\n"
"v_cvt_pk_bf16_f32 v240,v16,v17\n"
"v_cvt_pk_bf16_f32 v241,v18,v19\n"
"v_cvt_pk_bf16_f32 v242,v20,v21\n"
"v_cvt_pk_bf16_f32 v243,v22,v23\n"
"v_cvt_pk_bf16_f32 v244,v24,v25\n"
"v_cvt_pk_bf16_f32 v245,v26,v27\n"
"v_cvt_pk_bf16_f32 v246,v28,v29\n"
"v_cvt_pk_bf16_f32 v247,v30,v31\n"
"buffer_store_dwordx4 v[240:243],v112,s[48:51],s61 offen offset:0 sc1\n"
"buffer_store_dwordx4 v[244:247],v112,s[48:51],s61 offen offset:1024 sc1\n"
"s_barrier\n"
"s_lshl_b32 s82,s52,13\n"
"s_add_u32 s82,s82,0\n"
"v_bfe_u32 v41,v112,5,2\n"
"v_lshlrev_b32 v41,13,v41\n"
"v_bfe_u32 v42,v112,4,1\n"
"v_lshl_add_u32 v41,v42,3,v41\n"
"s_lshl_b32 s83,s100,4\n"
"v_mov_b32 v49,s80\n"
"v_mfma_f32_16x16x32_bf16 v[200:203],v[66:69],a[104:107],0\n"
"s_add_u32 s83,s83,149946368\n"
"v_mov_b32 v45,s66\n"
"v_add_u32 v41,0,v41\n"
"v_mfma_f32_16x16x32_bf16 v[204:207],v[64:67],a[104:107],0\n"
"v_mov_b32 v40,s82\n"
"v_mov_b32 v51,s80\n"
"v_mfma_f32_16x16x32_bf16 v[208:211],v[66:69],a[108:111],0\n"
"v_lshlrev_b32 v43,8,v112\n"
"v_mov_b32 v50,0\n"
"v_mfma_f32_16x16x32_bf16 v[212:215],v[64:67],a[108:111],0\n"
"s_mov_b32 s84,0x40000\n"
"v_mov_b32 v44,s65\n"
".Lfk_w1:\n"
"s_waitcnt vmcnt(0)\n"
"v_mfma_f32_16x16x32_bf16 v[0:3],v[66:69],v[160:163],v[200:203]\n"
"v_min3_u32 v36,v160,v161,v162\n"
"v_min3_u32 v37,v163,v164,v165\n"
"v_mfma_f32_16x16x32_bf16 v[4:7],v[64:67],v[160:163],v[204:207]\n"
"v_min3_u32 v36,v36,v166,v167\n"
"v_min3_u32 v37,v37,v168,v169\n"
"v_mfma_f32_16x16x32_bf16 v[8:11],v[66:69],v[164:167],v[208:211]\n"
"v_min3_u32 v36,v36,v170,v171\n"
"v_min3_u32 v37,v37,v172,v173\n"
"v_mfma_f32_16x16x32_bf16 v[12:15],v[64:67],v[164:167],v[212:215]\n"
"v_min3_u32 v36,v36,v174,v175\n"
"v_min3_u32 v37,v37,v176,v177\n"
"v_mfma_f32_16x16x32_bf16 v[0:3],v[66:69],v[168:171],v[0:3]\n"
"v_min3_u32 v36,v36,v178,v179\n"
"v_min3_u32 v37,v37,v180,v181\n"
"v_mfma_f32_16x16x32_bf16 v[4:7],v[64:67],v[168:171],v[4:7]\n"
"v_min3_u32 v36,v36,v182,v183\n"
"v_min3_u32 v37,v37,v184,v185\n"
"v_mfma_f32_16x16x32_bf16 v[8:11],v[66:69],v[172:175],v[8:11]\n"
"v_min3_u32 v36,v36,v186,v187\n"
"v_min3_u32 v37,v37,v188,v189\n"
"v_mfma_f32_16x16x32_bf16 v[12:15],v[64:67],v[172:175],v[12:15]\n"
"v_min3_u32 v36,v36,v190,v191\n"
"v_min_u32 v36,v36,v37\n"
"v_cmp_eq_u32 vcc,0,v36\n"
"v_mfma_f32_16x16x32_bf16 v[0:3],v[66:69],v[176:179],v[0:3]\n"
"s_cbranch_vccz .Lfk_w1_x\n"
"s_mov_b64 exec,vcc\n"
"s_add_u32 s91,s62,2097152\n"
"buffer_load_dwordx4 v[160:163],v126,s[24:27],s91 offen sc1\n"
"buffer_load_dwordx4 v[164:167],v117,s[24:27],s91 offen sc1\n"
"s_add_u32 s91,s91,4194304\n"
"buffer_load_dwordx4 v[168:171],v126,s[24:27],s91 offen sc1\n"
"buffer_load_dwordx4 v[172:175],v117,s[24:27],s91 offen sc1\n"
"s_add_u32 s91,s91,4194304\n"
"buffer_load_dwordx4 v[176:179],v126,s[24:27],s91 offen sc1\n"
"buffer_load_dwordx4 v[180:183],v117,s[24:27],s91 offen sc1\n"
"s_add_u32 s91,s91,4194304\n"
"buffer_load_dwordx4 v[184:187],v126,s[24:27],s91 offen sc1\n"
"buffer_load_dwordx4 v[188:191],v117,s[24:27],s91 offen sc1\n"
"s_mov_b64 exec,-1\n"
"s_sub_u32 s84,s84,1\n"
"s_cbranch_scc0 .Lfk_w1\n"
".Lfk_w1_x:\n"
"v_mfma_f32_16x16x32_bf16 v[4:7],v[64:67],v[176:179],v[4:7]\n"
"v_mfma_f32_16x16x32_bf16 v[8:11],v[66:69],v[180:183],v[8:11]\n"
"v_mfma_f32_16x16x32_bf16 v[12:15],v[64:67],v[180:183],v[12:15]\n"
"v_mfma_f32_16x16x32_bf16 v[0:3],v[66:69],v[184:187],v[0:3]\n"
"v_mfma_f32_16x16x32_bf16 v[4:7],v[64:67],v[184:187],v[4:7]\n"
"v_mfma_f32_16x16x32_bf16 v[8:11],v[66:69],v[188:191],v[8:11]\n"
"v_mfma_f32_16x16x32_bf16 v[12:15],v[64:67],v[188:191],v[12:15]\n"
"s_nop 6\n"
"s_call_b64 s[94:95],.Lfk_rm\n"
"s_sub_u32 s82,64511,s60\n"
"s_or_b32 s63,s63,s82\n"
"v_mov_b32 v46,s63\n"
"v_mov_b32 v47,s64\n"
"ds_write_b128 v40,v[44:47]\n"
"s_waitcnt lgkmcnt(0)\n"
"s_barrier\n"
"ds_read_b64 v[52:53],v41\n"
"s_waitcnt lgkmcnt(0)\n"
"v_mov_b32 v48,v52\n"
"s_nop 1\n"
"v_min_u32_dpp v54,v48,v48 quad_perm:[1,0,3,2] row_mask:0xf bank_mask:0xf\n"
"v_max_u32_dpp v48,v48,v48 quad_perm:[1,0,3,2] row_mask:0xf bank_mask:0xf\n"
"v_max_u32_dpp v50,v50,v50 quad_perm:[1,0,3,2] row_mask:0xf bank_mask:0xf\n"
"v_max_u32 v50,v50,v54\n"
"v_min_u32_dpp v54,v48,v48 quad_perm:[2,3,0,1] row_mask:0xf bank_mask:0xf\n"
"v_max_u32_dpp v48,v48,v48 quad_perm:[2,3,0,1] row_mask:0xf bank_mask:0xf\n"
"v_max_u32_dpp v50,v50,v50 quad_perm:[2,3,0,1] row_mask:0xf bank_mask:0xf\n"
"v_max_u32 v50,v50,v54\n"
"v_min_u32_dpp v54,v48,v48 row_half_mirror row_mask:0xf bank_mask:0xf\n"
"v_max_u32_dpp v48,v48,v48 row_half_mirror row_mask:0xf bank_mask:0xf\n"
"v_max_u32_dpp v50,v50,v50 row_half_mirror row_mask:0xf bank_mask:0xf\n"
"v_max_u32 v50,v50,v54\n"
"s_cmp_lg_u32 s52,0\n"
"s_cbranch_scc1 .Lfk_t1\n"
"s_mov_b64 exec,0xff\n"
"buffer_store_dwordx4 v[48:51],v43,s[24:27],s83 offen sc1\n"
"s_mov_b64 exec,-1\n"
"v_cmp_eq_u32 vcc,v48,v52\n"
"s_nop 0\n"
"s_ff1_i32_b64 s82,vcc\n"
"v_readlane_b32 s90,v53,s82\n"
"s_nop 1\n"
"v_mov_b32 v44,s90\n"
"v_mov_b32 v45,s80\n"
"v_cmp_eq_u32 vcc,v50,v52\n"
"s_nop 0\n"
"s_ff1_i32_b64 s82,vcc\n"
"v_readlane_b32 s90,v53,s82\n"
"s_nop 1\n"
"v_mov_b32 v46,s90\n"
"v_mov_b32 v47,s80\n"
"s_mov_b64 exec,0x100\n"
"buffer_store_dwordx4 v[44:47],v43,s[24:27],s83 offen sc1\n"
"s_mov_b64 exec,-1\n"
".Lfk_t1:\n"
"v_readfirstlane_b32 s89,v50\n"
"s_add_u32 m0,s30,131072\n"
"s_nop 0\n"
"buffer_load_dwordx4 v96,s[40:43],s77 offen offset:0 nt lds\n"
"buffer_load_dwordx4 v96,s[40:43],s77 offen offset:1024 nt lds\n"
"buffer_load_dwordx4 v97,s[40:43],s77 offen offset:2048 nt lds\n"
"buffer_load_dwordx4 v97,s[40:43],s77 offen offset:3072 nt lds\n"
"s_add_u32 s77,s77,4096\n"
"s_add_u32 m0,s30,135168\n"
"s_nop 0\n"
"buffer_load_dwordx4 v98,s[40:43],s77 offen offset:0 nt lds\n"
"buffer_load_dwordx4 v98,s[40:43],s77 offen offset:1024 nt lds\n"
"buffer_load_dwordx4 v99,s[40:43],s77 offen offset:2048 nt lds\n"
"buffer_load_dwordx4 v99,s[40:43],s77 offen offset:3072 nt lds\n"
"v_lshlrev_b32 v34,16,s64\n"
"v_mov_b32 v35,s64\n"
"v_rcp_f32 v34,v34\n"
"v_and_b32 v35,0xffff0000,v35\n"
"v_cvt_pk_bf16_f32 v248,v0,v1\n"
"v_cvt_pk_bf16_f32 v249,v2,v3\n"
"v_cvt_pk_bf16_f32 v250,v4,v5\n"
"v_cvt_pk_bf16_f32 v251,v6,v7\n"
"v_cvt_pk_bf16_f32 v252,v8,v9\n"
"v_cvt_pk_bf16_f32 v253,v10,v11\n"
"v_cvt_pk_bf16_f32 v254,v12,v13\n"
"v_cvt_pk_bf16_f32 v255,v14,v15\n"
"v_lshlrev_b32 v0,16,v248\n"
"v_and_b32 v1,0xffff0000,v248\n"
"v_lshlrev_b32 v2,16,v249\n"
"v_and_b32 v3,0xffff0000,v249\n"
"v_lshlrev_b32 v4,16,v250\n"
"v_and_b32 v5,0xffff0000,v250\n"
"v_lshlrev_b32 v6,16,v251\n"
"v_and_b32 v7,0xffff0000,v251\n"
"v_lshlrev_b32 v8,16,v252\n"
"v_and_b32 v9,0xffff0000,v252\n"
"v_lshlrev_b32 v10,16,v253\n"
"v_and_b32 v11,0xffff0000,v253\n"
"v_lshlrev_b32 v12,16,v254\n"
"v_and_b32 v13,0xffff0000,v254\n"
"v_lshlrev_b32 v14,16,v255\n"
"v_and_b32 v15,0xffff0000,v255\n"
"v_pk_add_f32 v[16:17],v[0:1],v[34:35] op_sel:[0,1] neg_lo:[0,1] neg_hi:[0,1]\n"
"v_pk_add_f32 v[18:19],v[2:3],v[34:35] op_sel:[0,1] neg_lo:[0,1] neg_hi:[0,1]\n"
"v_pk_add_f32 v[20:21],v[4:5],v[34:35] op_sel:[0,1] neg_lo:[0,1] neg_hi:[0,1]\n"
"v_pk_add_f32 v[22:23],v[6:7],v[34:35] op_sel:[0,1] neg_lo:[0,1] neg_hi:[0,1]\n"
"v_pk_add_f32 v[24:25],v[8:9],v[34:35] op_sel:[0,1] neg_lo:[0,1] neg_hi:[0,1]\n"
"v_pk_add_f32 v[26:27],v[10:11],v[34:35] op_sel:[0,1] neg_lo:[0,1] neg_hi:[0,1]\n"
"v_pk_add_f32 v[28:29],v[12:13],v[34:35] op_sel:[0,1] neg_lo:[0,1] neg_hi:[0,1]\n"
"v_pk_add_f32 v[30:31],v[14:15],v[34:35] op_sel:[0,1] neg_lo:[0,1] neg_hi:[0,1]\n"
"v_cvt_pk_bf16_f32 v248,v16,v17\n"
"v_cvt_pk_bf16_f32 v249,v18,v19\n"
"v_cvt_pk_bf16_f32 v250,v20,v21\n"
"v_cvt_pk_bf16_f32 v251,v22,v23\n"
"v_cvt_pk_bf16_f32 v252,v24,v25\n"
"v_cvt_pk_bf16_f32 v253,v26,v27\n"
"v_cvt_pk_bf16_f32 v254,v28,v29\n"
"v_cvt_pk_bf16_f32 v255,v30,v31\n"
"v_lshlrev_b32 v16,16,v248\n"
"v_and_b32 v17,0xffff0000,v248\n"
"v_lshlrev_b32 v18,16,v249\n"
"v_and_b32 v19,0xffff0000,v249\n"
"v_lshlrev_b32 v20,16,v250\n"
"v_and_b32 v21,0xffff0000,v250\n"
"v_lshlrev_b32 v22,16,v251\n"
"v_and_b32 v23,0xffff0000,v251\n"
"v_lshlrev_b32 v24,16,v252\n"
"v_and_b32 v25,0xffff0000,v252\n"
"v_lshlrev_b32 v26,16,v253\n"
"v_and_b32 v27,0xffff0000,v253\n"
"v_lshlrev_b32 v28,16,v254\n"
"v_and_b32 v29,0xffff0000,v254\n"
"v_lshlrev_b32 v30,16,v255\n"
"v_and_b32 v31,0xffff0000,v255\n"
"v_pk_mul_f32 v[16:17],v[16:17],v[34:35] op_sel_hi:[1,0]\n"
"v_pk_mul_f32 v[18:19],v[18:19],v[34:35] op_sel_hi:[1,0]\n"
"v_pk_mul_f32 v[20:21],v[20:21],v[34:35] op_sel_hi:[1,0]\n"
"v_pk_mul_f32 v[22:23],v[22:23],v[34:35] op_sel_hi:[1,0]\n"
"v_pk_mul_f32 v[24:25],v[24:25],v[34:35] op_sel_hi:[1,0]\n"
"v_pk_mul_f32 v[26:27],v[26:27],v[34:35] op_sel_hi:[1,0]\n"
"v_pk_mul_f32 v[28:29],v[28:29],v[34:35] op_sel_hi:[1,0]\n"
"v_pk_mul_f32 v[30:31],v[30:31],v[34:35] op_sel_hi:[1,0]\n"
"v_cvt_pk_bf16_f32 v248,v16,v17\n"
"v_cvt_pk_bf16_f32 v249,v18,v19\n"
"v_cvt_pk_bf16_f32 v250,v20,v21\n"
"v_cvt_pk_bf16_f32 v251,v22,v23\n"
"v_cvt_pk_bf16_f32 v252,v24,v25\n"
"v_cvt_pk_bf16_f32 v253,v26,v27\n"
"v_cvt_pk_bf16_f32 v254,v28,v29\n"
"v_cvt_pk_bf16_f32 v255,v30,v31\n"
"v_lshlrev_b32 v16,16,v248\n"
"v_and_b32 v17,0xffff0000,v248\n"
"v_lshlrev_b32 v18,16,v249\n"
"v_and_b32 v19,0xffff0000,v249\n"
"v_lshlrev_b32 v20,16,v250\n"
"v_and_b32 v21,0xffff0000,v250\n"
"v_lshlrev_b32 v22,16,v251\n"
"v_and_b32 v23,0xffff0000,v251\n"
"v_lshlrev_b32 v24,16,v252\n"
"v_and_b32 v25,0xffff0000,v252\n"
"v_lshlrev_b32 v26,16,v253\n"
"v_and_b32 v27,0xffff0000,v253\n"
"v_lshlrev_b32 v28,16,v254\n"
"v_and_b32 v29,0xffff0000,v254\n"
"v_lshlrev_b32 v30,16,v255\n"
"v_and_b32 v31,0xffff0000,v255\n"
"v_pk_add_f32 v[16:17],v[16:17],v[0:1]\n"
"v_pk_add_f32 v[18:19],v[18:19],v[2:3]\n"
"v_pk_add_f32 v[20:21],v[20:21],v[4:5]\n"
"v_pk_add_f32 v[22:23],v[22:23],v[6:7]\n"
"v_pk_add_f32 v[24:25],v[24:25],v[8:9]\n"
"v_pk_add_f32 v[26:27],v[26:27],v[10:11]\n"
"v_pk_add_f32 v[28:29],v[28:29],v[12:13]\n"
"v_pk_add_f32 v[30:31],v[30:31],v[14:15]\n"
"v_cvt_pk_bf16_f32 v248,v16,v17\n"
"v_cvt_pk_bf16_f32 v249,v18,v19\n"
"v_cvt_pk_bf16_f32 v250,v20,v21\n"
"v_cvt_pk_bf16_f32 v251,v22,v23\n"
"v_cvt_pk_bf16_f32 v252,v24,v25\n"
"v_cvt_pk_bf16_f32 v253,v26,v27\n"
"v_cvt_pk_bf16_f32 v254,v28,v29\n"
"v_cvt_pk_bf16_f32 v255,v30,v31\n"
"s_add_u32 s68,s61,2097152\n"
"s_cmp_lt_u32 s63,s89\n"
"s_cselect_b32 s83,s68,2146435072\n"
"s_cselect_b32 s68,2146435072,s68\n"
"buffer_store_dwordx4 v[248:251],v112,s[48:51],s83 offen offset:0 sc1\n"
"buffer_store_dwordx4 v[252:255],v112,s[48:51],s83 offen offset:1024 sc1\n"
"v_lshrrev_b32 v40,6,v112\n"
"v_and_b32 v41,7,v40\n"
"v_lshlrev_b32 v41,5,v41\n"
"v_bfe_u32 v42,v112,4,2\n"
"v_lshlrev_b32 v42,3,v42\n"
"v_lshl_add_u32 v40,v40,11,v42\n"
"s_lshl_b32 s82,s52,9\n"
"v_add_u32 v40,s82,v40\n"
"v_add_u32 v40,131072,v40\n"
"v_xor_b32 v42,0,v41\n"
"v_add_u32 v192,v40,v42\n"
"v_xor_b32 v42,32,v41\n"
"v_add_u32 v193,v40,v42\n"
"v_xor_b32 v42,64,v41\n"
"v_add_u32 v194,v40,v42\n"
"v_xor_b32 v42,96,v41\n"
"v_add_u32 v195,v40,v42\n"
"v_xor_b32 v42,128,v41\n"
"v_add_u32 v196,v40,v42\n"
"v_xor_b32 v42,160,v41\n"
"v_add_u32 v197,v40,v42\n"
"v_xor_b32 v42,192,v41\n"
"v_add_u32 v198,v40,v42\n"
"v_xor_b32 v42,224,v41\n"
"v_add_u32 v199,v40,v42\n"
"v_bfe_u32 v56,v112,4,1\n"
"v_lshrrev_b32 v57,1,v112\n"
"v_and_b32 v57,0x1f0,v57\n"
"s_lshl_b32 s83,s52,13\n"
"s_add_u32 s83,s83,1024\n"
"v_lshl_add_u32 v58,v56,9,s83\n"
"v_lshrrev_b32 v59,8,v112\n"
"v_lshl_add_u32 v59,v59,3,v58\n"
"v_add_u32 v58,v58,v57\n"
"v_add_u32 v57,s82,v57\n"
"v_and_b32 v55,0x7c0,v57\n"
"v_and_b32 v54,0x30,v57\n"
"v_lshl_add_u32 v55,v55,4,v54\n"
"v_add_u32 v60,s83,v112\n"
"v_add_u32 v61,1024,v112\n"
"v_bfe_u32 v62,v112,5,3\n"
"v_cmp_eq_u32 s[8:9],0,v62\n"
"v_cmp_eq_u32 s[10:11],1,v62\n"
"v_cmp_eq_u32 s[12:13],2,v62\n"
"v_cmp_eq_u32 s[14:15],3,v62\n"
"v_cmp_eq_u32 s[16:17],4,v62\n"
"v_cmp_eq_u32 s[18:19],5,v62\n"
"v_cmp_eq_u32 s[20:21],6,v62\n"
"v_cmp_eq_u32 s[22:23],7,v62\n"
"v_lshlrev_b32 v62,6,v62\n"
"v_lshl_add_u32 v62,v56,12,v62\n"
"v_lshrrev_b32 v63,8,v112\n"
"v_lshl_add_u32 v62,v63,4,v62\n"
"s_lshl_b32 s69,s72,13\n"
"s_lshl_b32 s82,s52,10\n"
"s_add_u32 s69,s69,s82\n"
"s_add_u32 s69,s69,171966464\n"
"v_lshrrev_b32 v63,5,v112\n"
"v_min_u32 v63,27,v63\n"
"v_lshlrev_b32 v63,13,v63\n"
"v_lshl_add_u32 v63,v56,4,v63\n"
"s_mul_i32 s70,s52,458752\n"
"s_lshl_b32 s82,s73,5\n"
"s_add_u32 s70,s70,s82\n"
"s_add_u32 s70,s70,171966464\n"
"s_add_u32 s71,s70,229376\n"
"s_and_b32 s83,s100,7\n"
"s_lshl_b32 s83,s83,12\n"
"s_add_u32 s83,s83,149946368\n"
"v_add_u32 v20,0,v112\n"
"v_lshlrev_b32 v20,0,v20\n"
"v_add_u32 v21,1024,v112\n"
"v_lshlrev_b32 v21,0,v21\n"
"v_add_u32 v22,2048,v112\n"
"v_lshlrev_b32 v22,0,v22\n"
"v_add_u32 v23,3072,v112\n"
"v_lshlrev_b32 v23,0,v23\n"
"s_sleep 16\n"
"s_mov_b32 s84,0x40000\n"
"buffer_load_dwordx4 v[0:3],v20,s[24:27],s83 offen sc1\n"
"buffer_load_dwordx4 v[4:7],v21,s[24:27],s83 offen sc1\n"
"buffer_load_dwordx4 v[8:11],v22,s[24:27],s83 offen sc1\n"
"buffer_load_dwordx4 v[12:15],v23,s[24:27],s83 offen sc1\n"
".Lfk_wc:\n"
"s_waitcnt vmcnt(0)\n"
"v_xor_b32 v24,s80,v1\n"
"v_xor_b32 v25,s80,v3\n"
"v_xor_b32 v26,s80,v5\n"
"v_xor_b32 v27,s80,v7\n"
"v_xor_b32 v28,s80,v9\n"
"v_xor_b32 v29,s80,v11\n"
"v_xor_b32 v30,s80,v13\n"
"v_xor_b32 v31,s80,v15\n"
"v_or3_b32 v16,v24,v25,v26\n"
"v_or3_b32 v17,v27,v28,v29\n"
"v_or3_b32 v16,v16,v30,v31\n"
"v_or_b32 v16,v16,v17\n"
"v_cmp_ne_u32 vcc,0,v16\n"
"s_nop 0\n"
"s_cbranch_vccz .Lfk_wc_x\n"
"s_sleep 2\n"
"s_mov_b64 exec,vcc\n"
"buffer_load_dwordx4 v[0:3],v20,s[24:27],s83 offen sc1\n"
"buffer_load_dwordx4 v[4:7],v21,s[24:27],s83 offen sc1\n"
"buffer_load_dwordx4 v[8:11],v22,s[24:27],s83 offen sc1\n"
"buffer_load_dwordx4 v[12:15],v23,s[24:27],s83 offen sc1\n"
"s_mov_b64 exec,-1\n"
"s_sub_u32 s84,s84,1\n"
"s_cbranch_scc0 .Lfk_wc\n"
".Lfk_wc_x:\n"
"v_min_u32 v16,v0,v4\n"
"v_max_u32 v0,v0,v4\n"
"v_max3_u32 v2,v2,v6,v16\n"
"v_min_u32 v16,v8,v12\n"
"v_max_u32 v8,v8,v12\n"
"v_max3_u32 v10,v10,v14,v16\n"
"v_min_u32 v16,v0,v8\n"
"v_max_u32 v0,v0,v8\n"
"v_max3_u32 v2,v2,v10,v16\n"
"v_cmp_eq_u32 vcc,0,v56\n"
"v_min_u32_dpp v16,v0,v0 quad_perm:[1,0,3,2] row_mask:0xf bank_mask:0xf\n"
"v_max_u32_dpp v0,v0,v0 quad_perm:[1,0,3,2] row_mask:0xf bank_mask:0xf\n"
"v_max_u32_dpp v2,v2,v2 quad_perm:[1,0,3,2] row_mask:0xf bank_mask:0xf\n"
"v_max_u32 v2,v2,v16\n"
"v_min_u32_dpp v16,v0,v0 quad_perm:[2,3,0,1] row_mask:0xf bank_mask:0xf\n"
"v_max_u32_dpp v0,v0,v0 quad_perm:[2,3,0,1] row_mask:0xf bank_mask:0xf\n"
"v_max_u32_dpp v2,v2,v2 quad_perm:[2,3,0,1] row_mask:0xf bank_mask:0xf\n"
"v_max_u32 v2,v2,v16\n"
"v_min_u32_dpp v16,v0,v0 row_half_mirror row_mask:0xf bank_mask:0xf\n"
"v_max_u32_dpp v0,v0,v0 row_half_mirror row_mask:0xf bank_mask:0xf\n"
"v_max_u32_dpp v2,v2,v2 row_half_mirror row_mask:0xf bank_mask:0xf\n"
"v_max_u32 v2,v2,v16\n"
"v_min_u32_dpp v16,v0,v0 row_mirror row_mask:0xf bank_mask:0xf\n"
"v_max_u32_dpp v0,v0,v0 row_mirror row_mask:0xf bank_mask:0xf\n"
"v_max_u32_dpp v2,v2,v2 row_mirror row_mask:0xf bank_mask:0xf\n"
"v_max_u32 v2,v2,v16\n"
"v_min_u32_dpp v16,v0,v0 row_bcast:15 row_mask:0xa bank_mask:0xf\n"
"v_max_u32_dpp v0,v0,v0 row_bcast:15 row_mask:0xa bank_mask:0xf\n"
"v_max_u32_dpp v2,v2,v2 row_bcast:15 row_mask:0xa bank_mask:0xf\n"
"v_max_u32 v2,v2,v16\n"
"v_min_u32_dpp v16,v0,v0 row_bcast:31 row_mask:0xc bank_mask:0xf\n"
"v_max_u32_dpp v0,v0,v0 row_bcast:31 row_mask:0xc bank_mask:0xf\n"
"v_max_u32_dpp v2,v2,v2 row_bcast:31 row_mask:0xc bank_mask:0xf\n"
"v_max_u32 v2,v2,v16\n"
"v_readlane_b32 s90,v0,63\n"
"v_readlane_b32 s92,v2,63\n"
"s_nop 0\n"
"v_mov_b32 v4,s90\n"
"v_mov_b32 v5,s92\n"
"v_cndmask_b32 v6,v5,v4,vcc\n"
"v_not_b32 v7,v6\n"
"v_and_b32 v7,0x7ff,v7\n"
"v_lshl_add_u32 v8,v7,11,v57\n"
"v_and_b32 v9,15,v7\n"
"v_lshl_add_u32 v9,v9,6,v55\n"
"v_and_b32 v10,0x7f0,v7\n"
"v_lshl_add_u32 v10,v10,11,v9\n"
"s_mov_b32 s82,71303168\n"
"buffer_load_dwordx4 a[0:3],v10,s[24:27],s82 offen sc1\n"
"s_add_u32 s82,s82,4194304\n"
"buffer_load_dwordx4 a[4:7],v10,s[24:27],s82 offen sc1\n"
"s_add_u32 s82,s82,4194304\n"
"buffer_load_dwordx4 a[8:11],v10,s[24:27],s82 offen sc1\n"
"s_add_u32 s82,s82,4194304\n"
"buffer_load_dwordx4 a[12:15],v10,s[24:27],s82 offen sc1\n"
"buffer_load_dwordx4 a[16:19],v8,s[36:39],0 offen\n"
"v_bfe_u32 v9,v7,2,8\n"
"v_lshlrev_b32 v9,4,v9\n"
"buffer_load_dwordx4 v[12:15],v9,s[24:27],s83 offen sc1\n"
"s_mov_b32 s82,149979136\n"
"buffer_load_dwordx4 v[16:19],v9,s[24:27],s82 offen sc1\n"
"s_barrier\n"
"ds_read_b64_tr_b16 a[224:225],v192 offset:0\n"
"ds_read_b64_tr_b16 a[240:241],v192 offset:256\n"
"ds_read_b64_tr_b16 a[226:227],v193 offset:0\n"
"ds_read_b64_tr_b16 a[242:243],v193 offset:256\n"
"ds_read_b64_tr_b16 a[228:229],v194 offset:0\n"
"ds_read_b64_tr_b16 a[244:245],v194 offset:256\n"
"ds_read_b64_tr_b16 a[230:231],v195 offset:0\n"
"ds_read_b64_tr_b16 a[246:247],v195 offset:256\n"
"ds_read_b64_tr_b16 a[232:233],v196 offset:0\n"
"ds_read_b64_tr_b16 a[248:249],v196 offset:256\n"
"ds_read_b64_tr_b16 a[234:235],v197 offset:0\n"
"ds_read_b64_tr_b16 a[250:251],v197 offset:256\n"
"ds_read_b64_tr_b16 a[236:237],v198 offset:0\n"
"ds_read_b64_tr_b16 a[252:253],v198 offset:256\n"
"ds_read_b64_tr_b16 a[238:239],v199 offset:0\n"
"ds_read_b64_tr_b16 a[254:255],v199 offset:256\n"
"s_mov_b32 s84,0x40000\n"
".Lfk_ws:\n"
"s_waitcnt vmcnt(0)\n"
"v_xor_b32 v20,s80,v17\n"
"v_xor_b32 v21,s80,v19\n"
"v_or_b32 v20,v20,v21\n"
"v_cmp_ne_u32 vcc,0,v20\n"
"s_nop 0\n"
"s_cbranch_vccz .Lfk_ws_x\n"
"s_mov_b64 exec,vcc\n"
"s_mov_b32 s82,149979136\n"
"buffer_load_dwordx4 v[12:15],v9,s[24:27],s83 offen sc1\n"
"buffer_load_dwordx4 v[16:19],v9,s[24:27],s82 offen sc1\n"
"s_mov_b64 exec,-1\n"
"s_sub_u32 s84,s84,1\n"
"s_cbranch_scc0 .Lfk_ws\n"
".Lfk_ws_x:\n"
"v_mfma_f32_16x16x32_bf16 v[24:27],v[66:69],a[0:3],0\n"
"v_mfma_f32_16x16x32_bf16 v[28:31],v[64:67],a[0:3],0\n"
"v_mfma_f32_16x16x32_bf16 v[24:27],v[66:69],a[4:7],v[24:27]\n"
"v_mfma_f32_16x16x32_bf16 v[28:31],v[64:67],a[4:7],v[28:31]\n"
"v_mfma_f32_16x16x32_bf16 v[24:27],v[66:69],a[8:11],v[24:27]\n"
"v_mfma_f32_16x16x32_bf16 v[28:31],v[64:67],a[8:11],v[28:31]\n"
"v_mfma_f32_16x16x32_bf16 v[24:27],v[66:69],a[12:15],v[24:27]\n"
"v_mfma_f32_16x16x32_bf16 v[28:31],v[64:67],a[12:15],v[28:31]\n"
"v_mfma_f32_16x16x32_bf16 v[24:27],v[66:69],a[16:19],v[24:27]\n"
"v_cmp_eq_u32 vcc,v12,v6\n"
"v_mfma_f32_16x16x32_bf16 v[28:31],v[64:67],a[16:19],v[28:31]\n"
"s_nop 0\n"
"v_cndmask_b32 v10,v18,v16,vcc\n"
"v_lshlrev_b32 v11,16,v10\n"
"v_rcp_f32 v11,v11\n"
"v_and_b32 v10,0xffff0000,v10\n"
"s_nop 0\n"
"v_cvt_pk_bf16_f32 v20,v24,v25\n"
"v_cvt_pk_bf16_f32 v21,v26,v27\n"
"v_lshlrev_b32 v24,16,v20\n"
"v_and_b32 v25,0xffff0000,v20\n"
"v_cvt_pk_bf16_f32 v23,v30,v31\n"
"v_cvt_pk_bf16_f32 v22,v28,v29\n"
"v_lshlrev_b32 v26,16,v21\n"
"v_and_b32 v27,0xffff0000,v21\n"
"v_lshlrev_b32 v28,16,v22\n"
"v_and_b32 v29,0xffff0000,v22\n"
"v_lshlrev_b32 v30,16,v23\n"
"v_and_b32 v31,0xffff0000,v23\n"
"v_pk_add_f32 v[32:33],v[24:25],v[10:11] op_sel_hi:[1,0] neg_lo:[0,1] neg_hi:[0,1]\n"
"v_pk_add_f32 v[34:35],v[26:27],v[10:11] op_sel_hi:[1,0] neg_lo:[0,1] neg_hi:[0,1]\n"
"v_pk_add_f32 v[36:37],v[28:29],v[10:11] op_sel_hi:[1,0] neg_lo:[0,1] neg_hi:[0,1]\n"
"v_pk_add_f32 v[38:39],v[30:31],v[10:11] op_sel_hi:[1,0] neg_lo:[0,1] neg_hi:[0,1]\n"
"v_cvt_pk_bf16_f32 v20,v32,v33\n"
"v_cvt_pk_bf16_f32 v21,v34,v35\n"
"v_cvt_pk_bf16_f32 v22,v36,v37\n"
"v_cvt_pk_bf16_f32 v23,v38,v39\n"
"v_lshlrev_b32 v32,16,v20\n"
"v_and_b32 v33,0xffff0000,v20\n"
"v_lshlrev_b32 v34,16,v21\n"
"v_and_b32 v35,0xffff0000,v21\n"
"v_lshlrev_b32 v36,16,v22\n"
"v_and_b32 v37,0xffff0000,v22\n"
"v_lshlrev_b32 v38,16,v23\n"
"v_and_b32 v39,0xffff0000,v23\n"
"v_pk_mul_f32 v[32:33],v[32:33],v[10:11] op_sel:[0,1]\n"
"v_pk_mul_f32 v[34:35],v[34:35],v[10:11] op_sel:[0,1]\n"
"v_pk_mul_f32 v[36:37],v[36:37],v[10:11] op_sel:[0,1]\n"
"v_pk_mul_f32 v[38:39],v[38:39],v[10:11] op_sel:[0,1]\n"
"v_cvt_pk_bf16_f32 v20,v32,v33\n"
"v_cvt_pk_bf16_f32 v21,v34,v35\n"
"v_cvt_pk_bf16_f32 v22,v36,v37\n"
"v_cvt_pk_bf16_f32 v23,v38,v39\n"
"ds_write_b128 v58,v[20:23]\n"
"ds_read2_b64 v[128:131],v59 offset0:0 offset1:4\n"
"ds_read2_b64 v[132:135],v59 offset0:8 offset1:12\n"
"ds_read2_b64 v[136:139],v59 offset0:16 offset1:20\n"
"ds_read2_b64 v[140:143],v59 offset0:24 offset1:28\n"
"ds_read2_b64 v[144:147],v59 offset0:32 offset1:36\n"
"ds_read2_b64 v[148:151],v59 offset0:40 offset1:44\n"
"ds_read2_b64 v[152:155],v59 offset0:48 offset1:52\n"
"ds_read2_b64 v[156:159],v59 offset0:56 offset1:60\n"
"ds_write_b128 v60,v[24:27] offset:3072\n"
"ds_write_b128 v60,v[28:31] offset:4096\n"
"s_waitcnt lgkmcnt(9)\n"
"v_mfma_f32_16x16x32_bf16 v[40:43],a[160:163],v[128:131],0\n"
"v_mfma_f32_16x16x32_bf16 v[44:47],a[192:195],v[128:131],0\n"
"s_waitcnt lgkmcnt(8)\n"
"v_mfma_f32_16x16x32_bf16 v[40:43],a[164:167],v[132:135],v[40:43]\n"
"v_mfma_f32_16x16x32_bf16 v[44:47],a[196:199],v[132:135],v[44:47]\n"
"s_waitcnt lgkmcnt(7)\n"
"v_mfma_f32_16x16x32_bf16 v[40:43],a[168:171],v[136:139],v[40:43]\n"
"v_mfma_f32_16x16x32_bf16 v[44:47],a[200:203],v[136:139],v[44:47]\n"
"s_waitcnt lgkmcnt(6)\n"
"v_mfma_f32_16x16x32_bf16 v[40:43],a[172:175],v[140:143],v[40:43]\n"
"v_mfma_f32_16x16x32_bf16 v[44:47],a[204:207],v[140:143],v[44:47]\n"
"s_waitcnt lgkmcnt(5)\n"
"v_mfma_f32_16x16x32_bf16 v[40:43],a[176:179],v[144:147],v[40:43]\n"
"v_mfma_f32_16x16x32_bf16 v[44:47],a[208:211],v[144:147],v[44:47]\n"
"s_waitcnt lgkmcnt(4)\n"
"v_mfma_f32_16x16x32_bf16 v[40:43],a[180:183],v[148:151],v[40:43]\n"
"v_mfma_f32_16x16x32_bf16 v[44:47],a[212:215],v[148:151],v[44:47]\n"
"s_waitcnt lgkmcnt(3)\n"
"v_mfma_f32_16x16x32_bf16 v[40:43],a[184:187],v[152:155],v[40:43]\n"
"v_mfma_f32_16x16x32_bf16 v[44:47],a[216:219],v[152:155],v[44:47]\n"
"s_waitcnt lgkmcnt(2)\n"
"v_mfma_f32_16x16x32_bf16 v[40:43],a[188:191],v[156:159],v[40:43]\n"
"v_mfma_f32_16x16x32_bf16 v[44:47],a[220:223],v[156:159],v[44:47]\n"
"s_nop 6\n"
"ds_write_b128 v60,v[40:43] offset:1024\n"
"ds_write_b128 v60,v[44:47] offset:2048\n"
"s_waitcnt lgkmcnt(0)\n"
"s_barrier\n"
"ds_read_b128 v[128:131],v61 offset:1024\n"
"ds_read_b128 v[144:147],v61 offset:2048\n"
"ds_read_b128 v[132:135],v61 offset:9216\n"
"ds_read_b128 v[148:151],v61 offset:10240\n"
"ds_read_b128 v[136:139],v61 offset:17408\n"
"ds_read_b128 v[152:155],v61 offset:18432\n"
"ds_read_b128 v[140:143],v61 offset:25600\n"
"ds_read_b128 v[156:159],v61 offset:26624\n"
"s_waitcnt lgkmcnt(0)\n"
"v_pk_add_f32 v[128:129],v[128:129],v[132:133]\n"
"v_pk_add_f32 v[136:137],v[136:137],v[140:141]\n"
"v_pk_add_f32 v[130:131],v[130:131],v[134:135]\n"
"v_pk_add_f32 v[138:139],v[138:139],v[142:143]\n"
"v_pk_add_f32 v[128:129],v[128:129],v[136:137]\n"
"v_pk_add_f32 v[130:131],v[130:131],v[138:139]\n"
"v_pk_add_f32 v[144:145],v[144:145],v[148:149]\n"
"v_pk_add_f32 v[152:153],v[152:153],v[156:157]\n"
"v_pk_add_f32 v[146:147],v[146:147],v[150:151]\n"
"v_pk_add_f32 v[154:155],v[154:155],v[158:159]\n"
"v_pk_add_f32 v[144:145],v[144:145],v[152:153]\n"
"v_pk_add_f32 v[146:147],v[146:147],v[154:155]\n"
"v_mul_f32 v132,0xbfb8aa3b,v128\n"
"v_mul_f32 v133,0xbfb8aa3b,v129\n"
"v_mul_f32 v134,0xbfb8aa3b,v130\n"
"v_mul_f32 v135,0xbfb8aa3b,v131\n"
"v_exp_f32 v132,v132\n"
"v_exp_f32 v133,v133\n"
"v_exp_f32 v134,v134\n"
"v_exp_f32 v135,v135\n"
"v_add_f32 v132,1.0,v132\n"
"v_add_f32 v133,1.0,v133\n"
"v_add_f32 v134,1.0,v134\n"
"v_add_f32 v135,1.0,v135\n"
"v_rcp_f32 v132,v132\n"
"v_rcp_f32 v133,v133\n"
"v_rcp_f32 v134,v134\n"
"v_rcp_f32 v135,v135\n"
"v_pk_mul_f32 v[128:129],v[128:129],v[132:133]\n"
"v_pk_mul_f32 v[130:131],v[130:131],v[134:135]\n"
"v_pk_mul_f32 v[128:129],v[128:129],v[144:145]\n"
"v_pk_mul_f32 v[130:131],v[130:131],v[146:147]\n"
"v_cvt_pk_bf16_f32 v132,v128,v129\n"
"v_cvt_pk_bf16_f32 v133,v130,v131\n"
"v_cndmask_b32 v136,0,v132,s[8:9]\n"
"v_cndmask_b32 v137,0,v133,s[8:9]\n"
"v_cndmask_b32 v138,0,v132,s[10:11]\n"
"v_cndmask_b32 v139,0,v133,s[10:11]\n"
"v_cndmask_b32 v141,0,v133,s[12:13]\n"
"v_cndmask_b32 v143,0,v133,s[14:15]\n"
"v_mfma_f32_16x16x32_bf16 v[40:43],a[224:227],v[136:139],0\n"
"v_cndmask_b32 v142,0,v132,s[14:15]\n"
"v_cndmask_b32 v140,0,v132,s[12:13]\n"
"v_mfma_f32_16x16x32_bf16 v[44:47],a[240:243],v[136:139],0\n"
"v_cndmask_b32 v145,0,v133,s[16:17]\n"
"v_cndmask_b32 v147,0,v133,s[18:19]\n"
"v_mfma_f32_16x16x32_bf16 v[40:43],a[228:231],v[140:143],v[40:43]\n"
"v_cndmask_b32 v146,0,v132,s[18:19]\n"
"v_cndmask_b32 v144,0,v132,s[16:17]\n"
"v_mfma_f32_16x16x32_bf16 v[44:47],a[244:247],v[140:143],v[44:47]\n"
"v_cndmask_b32 v149,0,v133,s[20:21]\n"
"v_cndmask_b32 v151,0,v133,s[22:23]\n"
"v_mfma_f32_16x16x32_bf16 v[40:43],a[232:235],v[144:147],v[40:43]\n"
"v_cndmask_b32 v150,0,v132,s[22:23]\n"
"v_cndmask_b32 v148,0,v132,s[20:21]\n"
"v_mfma_f32_16x16x32_bf16 v[44:47],a[248:251],v[144:147],v[44:47]\n"
"s_nop 0\n"
"v_mfma_f32_16x16x32_bf16 v[40:43],a[236:239],v[148:151],v[40:43]\n"
"v_mfma_f32_16x16x32_bf16 v[44:47],a[252:255],v[148:151],v[44:47]\n"
"s_cmp_ge_u32 s63,s92\n"
"s_cselect_b32 s68,2146435072,s68\n"
"s_nop 4\n"
"v_or_b32 v40,1,v40\n"
"v_or_b32 v41,1,v41\n"
"v_or_b32 v42,1,v42\n"
"v_or_b32 v43,1,v43\n"
"v_or_b32 v44,1,v44\n"
"v_or_b32 v45,1,v45\n"
"v_or_b32 v46,1,v46\n"
"v_or_b32 v47,1,v47\n"
"buffer_store_dwordx4 v[40:43],v62,s[24:27],s69 offen offset:0 sc1\n"
"buffer_store_dwordx4 v[44:47],v62,s[24:27],s69 offen offset:512 sc1\n"
"buffer_store_dwordx4 v[248:251],v112,s[48:51],s68 offen offset:0 sc1\n"
"buffer_store_dwordx4 v[252:255],v112,s[48:51],s68 offen offset:1024 sc1\n"
"s_cmp_eq_u32 s60,0\n"
"s_cselect_b32 s82,s75,2146435072\n"
"buffer_store_dwordx2 a[154:155],off,s[24:27],s82 sc1\n"
"s_bfe_u32 s82,s73,0x20005\n"
"s_lshl_b32 s82,s82,13\n"
"s_and_b32 s83,s73,31\n"
"s_lshr_b32 s91,s73,7\n"
"s_lshl_b32 s89,s91,9\n"
"s_lshl4_add_u32 s89,s83,s89\n"
"s_add_u32 s89,s89,s82\n"
"s_add_u32 s89,s89,1024\n"
"s_lshl1_add_u32 s83,s83,s91\n"
"s_lshl4_add_u32 s82,s83,s82\n"
"s_add_u32 s82,s82,4096\n"
"v_bfe_u32 v0,v112,4,3\n"
"v_lshl_add_u32 v1,v0,1,s89\n"
"ds_read_u16 v2,v1\n"
"v_and_b32 v3,3,v0\n"
"v_lshl_add_u32 v3,v3,2,s82\n"
"v_lshrrev_b32 v4,2,v0\n"
"v_lshl_add_u32 v3,v4,10,v3\n"
"ds_read_b32 v3,v3\n"
"s_cmp_eq_u32 s91,0\n"
"s_cselect_b32 s82,s90,s92\n"
"s_not_b32 s83,s82\n"
"s_and_b32 s89,s83,0x7ff\n"
"s_lshl_b32 s89,s89,11\n"
"s_and_b32 s91,s73,127\n"
"s_lshl4_add_u32 s89,s91,s89\n"
"s_ashr_i32 s83,s83,31\n"
"s_or_b32 s83,s83,0x80000000\n"
"s_xor_b32 s82,s82,s83\n"
"s_and_b32 s82,s82,0xffff0000\n"
"v_mov_b32 v5,0xbfb8aa3b\n"
"v_mul_f32 v5,s82,v5\n"
"v_exp_f32 v5,v5\n"
"s_nop 0\n"
"v_add_f32 v5,1.0,v5\n"
"v_rcp_f32 v5,v5\n"
"s_nop 0\n"
"v_cvt_pk_bf16_f32 v5,v5,v5\n"
"v_lshlrev_b32 v5,16,v5\n"
"s_lshl_b32 s82,s52,7\n"
"s_add_u32 s82,s82,64\n"
"v_lshrrev_b32 v6,3,v112\n"
"v_and_b32 v6,0x60,v6\n"
"v_and_b32 v7,16,v112\n"
"v_add3_u32 v6,v6,v7,s82\n"
"v_lshrrev_b32 v7,2,v112\n"
"v_add_u32 v7,64,v7\n"
"v_lshlrev_b32 v8,1,v0\n"
"s_waitcnt lgkmcnt(0)\n"
"v_lshlrev_b32 v2,16,v2\n"
"s_sleep 6\n"
"s_mov_b32 s84,0x40000\n"
"buffer_load_dwordx4 v[16:19],v63,s[24:27],s70 offen sc1\n"
"buffer_load_dwordx4 v[20:23],v63,s[24:27],s71 offen sc1\n"
".Lfk_wy:\n"
"s_waitcnt vmcnt(0)\n"
"v_min3_u32 v24,v16,v17,v18\n"
"v_min3_u32 v25,v19,v20,v21\n"
"v_min3_u32 v24,v24,v22,v23\n"
"v_min_u32 v24,v24,v25\n"
"v_cmp_eq_u32 vcc,0,v24\n"
"s_nop 0\n"
"s_cbranch_vccz .Lfk_wy_x\n"
"s_sleep 1\n"
"s_mov_b64 exec,vcc\n"
"buffer_load_dwordx4 v[16:19],v63,s[24:27],s70 offen sc1\n"
"buffer_load_dwordx4 v[20:23],v63,s[24:27],s71 offen sc1\n"
"s_mov_b64 exec,-1\n"
"s_sub_u32 s84,s84,1\n"
"s_cbranch_scc0 .Lfk_wy\n"
".Lfk_wy_x:\n"
"s_mov_b32 vcc_lo,-1\n"
"s_mov_b32 vcc_hi,0x00ffffff\n"
"v_add_f32 v16,v16,v20\n"
"v_add_f32 v17,v17,v21\n"
"v_add_f32 v18,v18,v22\n"
"v_add_f32 v19,v19,v23\n"
"v_cndmask_b32 v16,0,v16,vcc\n"
"v_cndmask_b32 v17,0,v17,vcc\n"
"v_cndmask_b32 v18,0,v18,vcc\n"
"v_cndmask_b32 v19,0,v19,vcc\n"
"v_add_f32_dpp v16,v16,v16 quad_perm:[2,3,0,1] row_mask:0xf bank_mask:0xf\n"
"v_add_f32_dpp v17,v17,v17 quad_perm:[2,3,0,1] row_mask:0xf bank_mask:0xf\n"
"v_add_f32_dpp v18,v18,v18 quad_perm:[2,3,0,1] row_mask:0xf bank_mask:0xf\n"
"v_add_f32_dpp v19,v19,v19 quad_perm:[2,3,0,1] row_mask:0xf bank_mask:0xf\n"
"v_add_f32_dpp v16,v16,v16 row_ror:4 row_mask:0xf bank_mask:0xf\n"
"v_add_f32_dpp v17,v17,v17 row_ror:4 row_mask:0xf bank_mask:0xf\n"
"v_add_f32_dpp v18,v18,v18 row_ror:4 row_mask:0xf bank_mask:0xf\n"
"v_add_f32_dpp v19,v19,v19 row_ror:4 row_mask:0xf bank_mask:0xf\n"
"v_add_f32_dpp v16,v16,v16 row_ror:8 row_mask:0xf bank_mask:0xf\n"
"v_add_f32_dpp v17,v17,v17 row_ror:8 row_mask:0xf bank_mask:0xf\n"
"v_add_f32_dpp v18,v18,v18 row_ror:8 row_mask:0xf bank_mask:0xf\n"
"v_add_f32_dpp v19,v19,v19 row_ror:8 row_mask:0xf bank_mask:0xf\n"
"s_mov_b32 exec_lo,0x30003\n"
"s_mov_b32 exec_hi,0x30003\n"
"ds_write_b128 v6,v[16:19]\n"
"s_mov_b64 exec,-1\n"
"s_waitcnt lgkmcnt(0)\n"
"s_barrier\n"
"s_cmp_lg_u32 s52,0\n"
"s_cbranch_scc1 .Lfk_t2\n"
"ds_read2_b32 v[16:17],v7 offset0:0 offset1:64\n"
"s_waitcnt lgkmcnt(0)\n"
"v_add_f32 v16,v16,v17\n"
"s_nop 1\n"
"v_add_f32_dpp v16,v16,v16 row_ror:8 row_mask:0xf bank_mask:0xf\n"
"v_mov_b32 v17,v16\n"
"s_nop 1\n"
"v_permlane16_swap_b32 v17,v16\n"
"v_add_f32 v16,v16,v17\n"
"v_mov_b32 v17,v16\n"
"s_nop 1\n"
"v_permlane32_swap_b32 v17,v16\n"
"v_add_f32 v16,v16,v17\n"
"v_cvt_pk_bf16_f32 v16,v16,v16\n"
"v_lshlrev_b32 v16,16,v16\n"
"v_mul_f32 v16,v16,v5\n"
"v_cvt_pk_bf16_f32 v16,v16,v16\n"
"v_lshlrev_b32 v16,16,v16\n"
"v_add_f32 v16,v16,v2\n"
"v_cvt_pk_bf16_f32 v16,v16,v16\n"
"v_lshlrev_b32 v16,16,v16\n"
"v_add_f32 v16,v16,v3\n"
"s_nop 1\n"
"v_mov_b32_dpp v17,v16 quad_perm:[1,0,3,2] row_mask:0xf bank_mask:0xf\n"
"v_cvt_pk_bf16_f32 v16,v16,v17\n"
"s_mov_b64 exec,0x55\n"
"buffer_store_dword v16,v8,s[48:51],s89 offen sc1\n"
"s_mov_b64 exec,-1\n"
".Lfk_t2:\n"
"s_branch .Lfk_end\n"
".Lfk_g0:\n"
"s_waitcnt vmcnt(9) lgkmcnt(0)\n"
"s_barrier\n"
"v_mfma_f32_16x16x32_bf16 v[32:35],a[96:99],a[0:3],v[96:99]\n"
"s_add_u32 m0,s55,4096\n"
"v_exp_f32 v0,v0\n"
"v_mfma_f32_16x16x32_bf16 v[32:35],a[100:103],a[4:7],v[32:35]\n"
"s_add_u32 s54,s54,s22\n"
"v_exp_f32 v1,v1\n"
"v_mfma_f32_16x16x32_bf16 v[32:35],a[104:107],a[8:11],v[32:35]\n"
"buffer_load_dwordx4 v115,s[32:35],s54 offen sc1 lds\n"
"v_exp_f32 v2,v2\n"
"v_cvt_pk_bf16_f32 v64,v0,v1\n"
"v_mfma_f32_16x16x32_bf16 v[32:35],a[108:111],a[12:15],v[32:35]\n"
"v_exp_f32 v3,v3\n"
"v_mfma_f32_16x16x32_bf16 v[36:39],a[124:127],a[12:15],v[96:99]\n"
"v_exp_f32 v4,v4\n"
"v_cvt_pk_bf16_f32 v65,v2,v3\n"
"v_mfma_f32_16x16x32_bf16 v[36:39],a[120:123],a[8:11],v[36:39]\n"
"v_exp_f32 v5,v5\n"
"v_mfma_f32_16x16x32_bf16 v[36:39],a[116:119],a[4:7],v[36:39]\n"
"v_exp_f32 v6,v6\n"
"v_cvt_pk_bf16_f32 v66,v4,v5\n"
"v_mfma_f32_16x16x32_bf16 v[36:39],a[112:115],a[0:3],v[36:39]\n"
"v_exp_f32 v7,v7\n"
"v_mfma_f32_16x16x32_bf16 v[44:47],a[112:115],a[16:19],v[100:103]\n"
"v_exp_f32 v8,v8\n"
"v_cvt_pk_bf16_f32 v67,v6,v7\n"
"v_exp_f32 v9,v9\n"
"v_mfma_f32_16x16x32_bf16 v[44:47],a[116:119],a[20:23],v[44:47]\n"
"ds_read_b128 a[64:67],v113 offset:49152\n"
"ds_read_b128 a[68:71],v113 offset:50176\n"
"v_exp_f32 v10,v10\n"
"v_mfma_f32_16x16x32_bf16 v[44:47],a[120:123],a[24:27],v[44:47]\n"
"ds_read_b128 a[72:75],v113 offset:51200\n"
"ds_read_b128 a[76:79],v113 offset:52224\n"
"v_exp_f32 v11,v11\n"
"v_mfma_f32_16x16x32_bf16 v[44:47],a[124:127],a[28:31],v[44:47]\n"
"ds_read_b128 a[80:83],v113 offset:53248\n"
"ds_read_b128 a[84:87],v113 offset:54272\n"
"v_exp_f32 v12,v12\n"
"v_mfma_f32_16x16x32_bf16 v[40:43],a[108:111],a[28:31],v[100:103]\n"
"ds_read_b128 a[88:91],v113 offset:55296\n"
"ds_read_b128 a[92:95],v113 offset:56320\n"
"v_mfma_f32_16x16x32_bf16 v[40:43],a[104:107],a[24:27],v[40:43]\n"
"v_exp_f32 v13,v13\n"
"v_cvt_pk_bf16_f32 v68,v8,v9\n"
"v_cvt_pk_bf16_f32 v69,v10,v11\n"
"v_mfma_f32_16x16x32_bf16 v[40:43],a[100:103],a[20:23],v[40:43]\n"
"v_exp_f32 v14,v14\n"
"v_cvt_pk_bf16_f32 v70,v12,v13\n"
"v_mfma_f32_16x16x32_bf16 v[40:43],a[96:99],a[16:19],v[40:43]\n"
"v_exp_f32 v15,v15\n"
"v_mfma_f32_16x16x32_bf16 v[48:51],a[96:99],a[32:35],v[104:107]\n"
"v_exp_f32 v16,v16\n"
"v_cvt_pk_bf16_f32 v71,v14,v15\n"
"v_mfma_f32_16x16x32_bf16 v[48:51],a[100:103],a[36:39],v[48:51]\n"
"v_exp_f32 v17,v17\n"
"v_mfma_f32_16x16x32_bf16 v[48:51],a[104:107],a[40:43],v[48:51]\n"
"v_exp_f32 v18,v18\n"
"v_cvt_pk_bf16_f32 v72,v16,v17\n"
"v_mfma_f32_16x16x32_bf16 v[48:51],a[108:111],a[44:47],v[48:51]\n"
"v_exp_f32 v19,v19\n"
"v_mfma_f32_16x16x32_bf16 v[52:55],a[124:127],a[44:47],v[104:107]\n"
"v_exp_f32 v20,v20\n"
"v_cvt_pk_bf16_f32 v73,v18,v19\n"
"v_mfma_f32_16x16x32_bf16 v[52:55],a[120:123],a[40:43],v[52:55]\n"
"v_exp_f32 v21,v21\n"
"v_mfma_f32_16x16x32_bf16 v[52:55],a[116:119],a[36:39],v[52:55]\n"
"v_exp_f32 v22,v22\n"
"v_cvt_pk_bf16_f32 v74,v20,v21\n"
"v_mfma_f32_16x16x32_bf16 v[52:55],a[112:115],a[32:35],v[52:55]\n"
"v_exp_f32 v23,v23\n"
"v_mfma_f32_16x16x32_bf16 v[60:63],a[112:115],a[48:51],v[108:111]\n"
"v_exp_f32 v24,v24\n"
"v_cvt_pk_bf16_f32 v75,v22,v23\n"
"v_mfma_f32_16x16x32_bf16 v[60:63],a[116:119],a[52:55],v[60:63]\n"
"v_exp_f32 v25,v25\n"
"v_mfma_f32_16x16x32_bf16 v[60:63],a[120:123],a[56:59],v[60:63]\n"
"v_exp_f32 v26,v26\n"
"v_exp_f32 v27,v27\n"
"v_mfma_f32_16x16x32_bf16 v[60:63],a[124:127],a[60:63],v[60:63]\n"
"buffer_store_dwordx4 a[228:231],v112,s[24:27],s56 offen offset:1024 sc1\n"
"v_mfma_f32_16x16x32_bf16 v[56:59],a[108:111],a[60:63],v[108:111]\n"
"v_exp_f32 v28,v28\n"
"v_cvt_pk_bf16_f32 v77,v26,v27\n"
"v_mfma_f32_16x16x32_bf16 v[56:59],a[104:107],a[56:59],v[56:59]\n"
"v_exp_f32 v29,v29\n"
"v_mfma_f32_16x16x32_bf16 v[56:59],a[100:103],a[52:55],v[56:59]\n"
"v_exp_f32 v30,v30\n"
"v_cvt_pk_bf16_f32 v78,v28,v29\n"
"v_mfma_f32_16x16x32_bf16 v[56:59],a[96:99],a[48:51],v[56:59]\n"
"s_waitcnt vmcnt(7) lgkmcnt(0)\n"
"v_exp_f32 v31,v31\n"
"v_cvt_pk_bf16_f32 v76,v24,v25\n"
"v_cvt_pk_bf16_f32 v79,v30,v31\n"
"s_barrier\n"
"v_mfma_f32_16x16x32_bf16 v[0:3],a[64:67],a[0:3],v[96:99]\n"
"s_add_u32 m0,s55,8192\n"
"v_exp_f32 v122,v32\n"
"v_mfma_f32_16x16x32_bf16 v[0:3],a[68:71],a[4:7],v[0:3]\n"
"s_add_u32 s54,s54,s22\n"
"v_exp_f32 v123,v33\n"
"v_mfma_f32_16x16x32_bf16 v[0:3],a[72:75],a[8:11],v[0:3]\n"
"ds_read_b128 a[96:99],v114 offset:40960\n"
"ds_read_b128 a[100:103],v114 offset:41984\n"
"v_mfma_f32_16x16x32_bf16 v[0:3],a[76:79],a[12:15],v[0:3]\n"
"v_exp_f32 v119,v34\n"
"v_mfma_f32_16x16x32_bf16 v[4:7],a[92:95],a[12:15],v[96:99]\n"
"ds_read_b128 a[104:107],v114 offset:43008\n"
"v_cvt_pk_bf16_f32 v80,v122,v123\n"
"v_mfma_f32_16x16x32_bf16 v[4:7],a[88:91],a[8:11],v[4:7]\n"
"v_exp_f32 v120,v35\n"
"v_mfma_f32_16x16x32_bf16 v[4:7],a[84:87],a[4:7],v[4:7]\n"
"ds_read_b128 a[108:111],v114 offset:44032\n"
"ds_read_b128 a[112:115],v114 offset:45056\n"
"v_mfma_f32_16x16x32_bf16 v[4:7],a[80:83],a[0:3],v[4:7]\n"
"v_exp_f32 v122,v36\n"
"v_mfma_f32_16x16x32_bf16 v[12:15],a[80:83],a[16:19],v[100:103]\n"
"ds_read_b128 a[116:119],v114 offset:46080\n"
"v_cvt_pk_bf16_f32 v81,v119,v120\n"
"v_mfma_f32_16x16x32_bf16 v[12:15],a[84:87],a[20:23],v[12:15]\n"
"v_exp_f32 v123,v37\n"
"v_mfma_f32_16x16x32_bf16 v[12:15],a[88:91],a[24:27],v[12:15]\n"
"ds_read_b128 a[120:123],v114 offset:47104\n"
"ds_read_b128 a[124:127],v114 offset:48128\n"
"v_mfma_f32_16x16x32_bf16 v[12:15],a[92:95],a[28:31],v[12:15]\n"
"v_exp_f32 v119,v38\n"
"v_cvt_pk_bf16_f32 v82,v122,v123\n"
"v_mfma_f32_16x16x32_bf16 v[8:11],a[76:79],a[28:31],v[100:103]\n"
"buffer_store_dwordx4 a[168:171],v112,s[24:27],s3 offen offset:2048 sc1\n"
"v_mfma_f32_16x16x32_bf16 v[8:11],a[72:75],a[24:27],v[8:11]\n"
"v_exp_f32 v120,v39\n"
"v_mfma_f32_16x16x32_bf16 v[8:11],a[68:71],a[20:23],v[8:11]\n"
"buffer_load_dwordx4 v115,s[32:35],s54 offen sc1 lds\n"
"v_mfma_f32_16x16x32_bf16 v[8:11],a[64:67],a[16:19],v[8:11]\n"
"v_exp_f32 v122,v40\n"
"v_mfma_f32_16x16x32_bf16 v[16:19],a[64:67],a[32:35],v[104:107]\n"
"v_cvt_pk_bf16_f32 v83,v119,v120\n"
"v_or3_b32 v118,v80,v81,v82\n"
"v_mfma_f32_16x16x32_bf16 v[16:19],a[68:71],a[36:39],v[16:19]\n"
"v_exp_f32 v123,v41\n"
"v_mfma_f32_16x16x32_bf16 v[16:19],a[72:75],a[40:43],v[16:19]\n"
"v_cvt_pk_bf16_f32 v84,v122,v123\n"
"v_or3_b32 v118,v118,v83,v84\n"
"v_mfma_f32_16x16x32_bf16 v[16:19],a[76:79],a[44:47],v[16:19]\n"
"v_exp_f32 v119,v42\n"
"v_mfma_f32_16x16x32_bf16 v[20:23],a[92:95],a[44:47],v[104:107]\n"
"v_mfma_f32_16x16x32_bf16 v[20:23],a[88:91],a[40:43],v[20:23]\n"
"v_exp_f32 v120,v43\n"
"v_mfma_f32_16x16x32_bf16 v[20:23],a[84:87],a[36:39],v[20:23]\n"
"v_cvt_pk_bf16_f32 v85,v119,v120\n"
"v_mfma_f32_16x16x32_bf16 v[20:23],a[80:83],a[32:35],v[20:23]\n"
"v_exp_f32 v122,v44\n"
"v_mfma_f32_16x16x32_bf16 v[28:31],a[80:83],a[48:51],v[108:111]\n"
"buffer_store_dwordx4 a[172:175],v112,s[24:27],s3 offen offset:3072 sc1\n"
"v_mfma_f32_16x16x32_bf16 v[28:31],a[84:87],a[52:55],v[28:31]\n"
"v_exp_f32 v123,v45\n"
"v_mfma_f32_16x16x32_bf16 v[28:31],a[88:91],a[56:59],v[28:31]\n"
"s_sub_u32 s3,s3,16384\n"
"v_mfma_f32_16x16x32_bf16 v[28:31],a[92:95],a[60:63],v[28:31]\n"
"v_exp_f32 v119,v46\n"
"v_mfma_f32_16x16x32_bf16 v[24:27],a[76:79],a[60:63],v[108:111]\n"
"v_cvt_pk_bf16_f32 v86,v122,v123\n"
"v_or3_b32 v118,v118,v85,v86\n"
"v_mfma_f32_16x16x32_bf16 v[24:27],a[72:75],a[56:59],v[24:27]\n"
"v_exp_f32 v120,v47\n"
"v_mfma_f32_16x16x32_bf16 v[24:27],a[68:71],a[52:55],v[24:27]\n"
"v_cvt_pk_bf16_f32 v87,v119,v120\n"
"v_mfma_f32_16x16x32_bf16 v[24:27],a[64:67],a[48:51],v[24:27]\n"
"s_waitcnt lgkmcnt(0)\n"
"v_exp_f32 v122,v48\n"
"v_mfma_f32_16x16x32_bf16 v[128:131],a[96:99],v[64:67],0\n"
"v_exp_f32 v123,v49\n"
"v_mfma_f32_16x16x32_bf16 v[160:163],a[96:99],v[68:71],0\n"
"ds_read_b128 a[64:67],v113 offset:32768\n"
"ds_read_b128 a[68:71],v113 offset:33792\n"
"v_mfma_f32_16x16x32_bf16 v[192:195],a[96:99],v[72:75],0\n"
"v_exp_f32 v119,v50\n"
"v_mfma_f32_16x16x32_bf16 v[224:227],a[96:99],v[76:79],0\n"
"ds_read_b128 a[72:75],v113 offset:34816\n"
"v_cvt_pk_bf16_f32 v88,v122,v123\n"
"v_mfma_f32_16x16x32_bf16 a[140:143],a[144:147],v[76:79],0\n"
"v_exp_f32 v120,v51\n"
"v_mfma_f32_16x16x32_bf16 v[228:231],a[100:103],v[76:79],0\n"
"ds_read_b128 a[76:79],v113 offset:35840\n"
"ds_read_b128 a[80:83],v113 offset:36864\n"
"v_mfma_f32_16x16x32_bf16 v[132:135],a[100:103],v[64:67],0\n"
"v_exp_f32 v122,v52\n"
"v_mfma_f32_16x16x32_bf16 v[164:167],a[100:103],v[68:71],0\n"
"ds_read_b128 a[84:87],v113 offset:37888\n"
"v_cvt_pk_bf16_f32 v89,v119,v120\n"
"v_mfma_f32_16x16x32_bf16 v[196:199],a[100:103],v[72:75],0\n"
"v_exp_f32 v123,v53\n"
"v_mfma_f32_16x16x32_bf16 a[136:139],a[144:147],v[72:75],0\n"
"ds_read_b128 a[88:91],v113 offset:38912\n"
"ds_read_b128 a[92:95],v113 offset:39936\n"
"v_mfma_f32_16x16x32_bf16 v[200:203],a[104:107],v[72:75],0\n"
"v_exp_f32 v119,v54\n"
"v_cvt_pk_bf16_f32 v90,v122,v123\n"
"v_mfma_f32_16x16x32_bf16 v[232:235],a[104:107],v[76:79],0\n"
"buffer_store_dwordx4 a[176:179],v112,s[24:27],s3 offen offset:0 sc1\n"
"v_mfma_f32_16x16x32_bf16 v[136:139],a[104:107],v[64:67],0\n"
"v_exp_f32 v120,v55\n"
"v_mfma_f32_16x16x32_bf16 v[168:171],a[104:107],v[68:71],0\n"
"v_cvt_pk_bf16_f32 v91,v119,v120\n"
"v_or3_b32 v118,v118,v87,v88\n"
"v_mfma_f32_16x16x32_bf16 a[132:135],a[144:147],v[68:71],0\n"
"v_exp_f32 v122,v56\n"
"v_mfma_f32_16x16x32_bf16 v[172:175],a[108:111],v[68:71],0\n"
"v_or3_b32 v118,v118,v89,v90\n"
"v_mfma_f32_16x16x32_bf16 v[204:207],a[108:111],v[72:75],0\n"
"v_exp_f32 v123,v57\n"
"v_mfma_f32_16x16x32_bf16 v[236:239],a[108:111],v[76:79],0\n"
"v_cvt_pk_bf16_f32 v92,v122,v123\n"
"v_or3_b32 v118,v118,v91,v92\n"
"v_mfma_f32_16x16x32_bf16 v[140:143],a[108:111],v[64:67],0\n"
"v_exp_f32 v119,v58\n"
"v_mfma_f32_16x16x32_bf16 a[128:131],a[144:147],v[64:67],0\n"
"v_mfma_f32_16x16x32_bf16 v[144:147],a[112:115],v[64:67],0\n"
"v_exp_f32 v120,v59\n"
"v_mfma_f32_16x16x32_bf16 v[176:179],a[112:115],v[68:71],0\n"
"v_cvt_pk_bf16_f32 v93,v119,v120\n"
"v_mfma_f32_16x16x32_bf16 v[208:211],a[112:115],v[72:75],0\n"
"v_exp_f32 v122,v60\n"
"v_mfma_f32_16x16x32_bf16 v[240:243],a[112:115],v[76:79],0\n"
"v_mfma_f32_16x16x32_bf16 v[244:247],a[116:119],v[76:79],0\n"
"v_exp_f32 v123,v61\n"
"v_mfma_f32_16x16x32_bf16 v[148:151],a[116:119],v[64:67],0\n"
"v_cvt_pk_bf16_f32 v94,v122,v123\n"
"v_or3_b32 v118,v118,v93,v94\n"
"v_mfma_f32_16x16x32_bf16 v[180:183],a[116:119],v[68:71],0\n"
"v_exp_f32 v119,v62\n"
"v_mfma_f32_16x16x32_bf16 v[212:215],a[116:119],v[72:75],0\n"
"buffer_store_dwordx4 a[232:235],v112,s[24:27],s56 offen offset:2048 sc1\n"
"v_mfma_f32_16x16x32_bf16 v[216:219],a[120:123],v[72:75],0\n"
"v_exp_f32 v120,v63\n"
"v_mfma_f32_16x16x32_bf16 v[248:251],a[120:123],v[76:79],0\n"
"v_cvt_pk_bf16_f32 v95,v119,v120\n"
"v_or_b32 v118,v118,v95\n"
"v_mfma_f32_16x16x32_bf16 v[152:155],a[120:123],v[64:67],0\n"
"v_and_b32 v118,0x40004000,v118\n"
"v_cmp_ne_u32 vcc,0,v118\n"
"v_mfma_f32_16x16x32_bf16 v[184:187],a[120:123],v[68:71],0\n"
"v_mfma_f32_16x16x32_bf16 v[188:191],a[124:127],v[68:71],0\n"
"v_mfma_f32_16x16x32_bf16 v[220:223],a[124:127],v[72:75],0\n"
"v_mfma_f32_16x16x32_bf16 v[252:255],a[124:127],v[76:79],0\n"
"v_mfma_f32_16x16x32_bf16 v[156:159],a[124:127],v[64:67],0\n"
"s_cbranch_vccnz .Lfk_cs_k1\n"
".Lfk_cr_k1:\n"
"s_waitcnt lgkmcnt(0)\n"
"s_barrier\n"
"v_mfma_f32_16x16x32_bf16 v[32:35],a[64:67],a[0:3],v[96:99]\n"
"s_add_u32 m0,s55,12288\n"
"v_exp_f32 v122,v0\n"
"v_mfma_f32_16x16x32_bf16 v[32:35],a[68:71],a[4:7],v[32:35]\n"
"s_add_u32 s54,s54,s22\n"
"v_exp_f32 v123,v1\n"
"v_mfma_f32_16x16x32_bf16 v[32:35],a[72:75],a[8:11],v[32:35]\n"
"ds_read_b128 a[96:99],v114 offset:8192\n"
"ds_read_b128 a[100:103],v114 offset:9216\n"
"v_mfma_f32_16x16x32_bf16 v[32:35],a[76:79],a[12:15],v[32:35]\n"
"v_exp_f32 v119,v2\n"
"v_mfma_f32_16x16x32_bf16 v[36:39],a[92:95],a[12:15],v[96:99]\n"
"ds_read_b128 a[104:107],v114 offset:10240\n"
"v_cvt_pk_bf16_f32 v64,v122,v123\n"
"v_mfma_f32_16x16x32_bf16 v[36:39],a[88:91],a[8:11],v[36:39]\n"
"v_exp_f32 v120,v3\n"
"v_mfma_f32_16x16x32_bf16 v[36:39],a[84:87],a[4:7],v[36:39]\n"
"ds_read_b128 a[108:111],v114 offset:11264\n"
"ds_read_b128 a[112:115],v114 offset:12288\n"
"v_mfma_f32_16x16x32_bf16 v[36:39],a[80:83],a[0:3],v[36:39]\n"
"v_exp_f32 v122,v4\n"
"v_mfma_f32_16x16x32_bf16 v[44:47],a[80:83],a[16:19],v[100:103]\n"
"ds_read_b128 a[116:119],v114 offset:13312\n"
"v_cvt_pk_bf16_f32 v65,v119,v120\n"
"v_mfma_f32_16x16x32_bf16 v[44:47],a[84:87],a[20:23],v[44:47]\n"
"v_exp_f32 v123,v5\n"
"v_mfma_f32_16x16x32_bf16 v[44:47],a[88:91],a[24:27],v[44:47]\n"
"ds_read_b128 a[120:123],v114 offset:14336\n"
"ds_read_b128 a[124:127],v114 offset:15360\n"
"v_mfma_f32_16x16x32_bf16 v[44:47],a[92:95],a[28:31],v[44:47]\n"
"v_exp_f32 v119,v6\n"
"v_cvt_pk_bf16_f32 v66,v122,v123\n"
"v_mfma_f32_16x16x32_bf16 v[40:43],a[76:79],a[28:31],v[100:103]\n"
"buffer_store_dwordx4 a[180:183],v112,s[24:27],s3 offen offset:1024 sc1\n"
"v_mfma_f32_16x16x32_bf16 v[40:43],a[72:75],a[24:27],v[40:43]\n"
"v_exp_f32 v120,v7\n"
"v_mfma_f32_16x16x32_bf16 v[40:43],a[68:71],a[20:23],v[40:43]\n"
"buffer_load_dwordx4 v115,s[32:35],s54 offen sc1 lds\n"
"v_mfma_f32_16x16x32_bf16 v[40:43],a[64:67],a[16:19],v[40:43]\n"
"v_exp_f32 v122,v8\n"
"v_mfma_f32_16x16x32_bf16 v[48:51],a[64:67],a[32:35],v[104:107]\n"
"v_cvt_pk_bf16_f32 v67,v119,v120\n"
"v_or3_b32 v118,v64,v65,v66\n"
"v_mfma_f32_16x16x32_bf16 v[48:51],a[68:71],a[36:39],v[48:51]\n"
"v_exp_f32 v123,v9\n"
"v_mfma_f32_16x16x32_bf16 v[48:51],a[72:75],a[40:43],v[48:51]\n"
"v_cvt_pk_bf16_f32 v68,v122,v123\n"
"v_or3_b32 v118,v118,v67,v68\n"
"v_mfma_f32_16x16x32_bf16 v[48:51],a[76:79],a[44:47],v[48:51]\n"
"v_exp_f32 v119,v10\n"
"v_mfma_f32_16x16x32_bf16 v[52:55],a[92:95],a[44:47],v[104:107]\n"
"v_mfma_f32_16x16x32_bf16 v[52:55],a[88:91],a[40:43],v[52:55]\n"
"v_exp_f32 v120,v11\n"
"v_mfma_f32_16x16x32_bf16 v[52:55],a[84:87],a[36:39],v[52:55]\n"
"v_cvt_pk_bf16_f32 v69,v119,v120\n"
"v_mfma_f32_16x16x32_bf16 v[52:55],a[80:83],a[32:35],v[52:55]\n"
"v_exp_f32 v122,v12\n"
"v_mfma_f32_16x16x32_bf16 v[60:63],a[80:83],a[48:51],v[108:111]\n"
"buffer_store_dwordx4 a[184:187],v112,s[24:27],s3 offen offset:2048 sc1\n"
"v_mfma_f32_16x16x32_bf16 v[60:63],a[84:87],a[52:55],v[60:63]\n"
"v_exp_f32 v123,v13\n"
"v_mfma_f32_16x16x32_bf16 v[60:63],a[88:91],a[56:59],v[60:63]\n"
"v_cvt_pk_bf16_f32 v70,v122,v123\n"
"v_mfma_f32_16x16x32_bf16 v[60:63],a[92:95],a[60:63],v[60:63]\n"
"v_exp_f32 v119,v14\n"
"v_mfma_f32_16x16x32_bf16 v[56:59],a[76:79],a[60:63],v[108:111]\n"
"v_or3_b32 v118,v118,v69,v70\n"
"v_mfma_f32_16x16x32_bf16 v[56:59],a[72:75],a[56:59],v[56:59]\n"
"v_exp_f32 v120,v15\n"
"v_mfma_f32_16x16x32_bf16 v[56:59],a[68:71],a[52:55],v[56:59]\n"
"v_cvt_pk_bf16_f32 v71,v119,v120\n"
"v_mfma_f32_16x16x32_bf16 v[56:59],a[64:67],a[48:51],v[56:59]\n"
"s_waitcnt lgkmcnt(0)\n"
"v_exp_f32 v122,v16\n"
"v_mfma_f32_16x16x32_bf16 v[128:131],a[96:99],v[80:83],v[128:131]\n"
"v_exp_f32 v123,v17\n"
"v_mfma_f32_16x16x32_bf16 v[160:163],a[96:99],v[84:87],v[160:163]\n"
"ds_read_b128 a[64:67],v113 offset:0\n"
"ds_read_b128 a[68:71],v113 offset:1024\n"
"v_mfma_f32_16x16x32_bf16 v[192:195],a[96:99],v[88:91],v[192:195]\n"
"v_exp_f32 v119,v18\n"
"v_mfma_f32_16x16x32_bf16 v[224:227],a[96:99],v[92:95],v[224:227]\n"
"ds_read_b128 a[72:75],v113 offset:2048\n"
"v_cvt_pk_bf16_f32 v72,v122,v123\n"
"v_mfma_f32_16x16x32_bf16 a[140:143],a[144:147],v[92:95],a[140:143]\n"
"v_exp_f32 v120,v19\n"
"v_mfma_f32_16x16x32_bf16 v[228:231],a[100:103],v[92:95],v[228:231]\n"
"ds_read_b128 a[76:79],v113 offset:3072\n"
"ds_read_b128 a[80:83],v113 offset:4096\n"
"v_mfma_f32_16x16x32_bf16 v[132:135],a[100:103],v[80:83],v[132:135]\n"
"v_exp_f32 v122,v20\n"
"v_mfma_f32_16x16x32_bf16 v[164:167],a[100:103],v[84:87],v[164:167]\n"
"ds_read_b128 a[84:87],v113 offset:5120\n"
"v_cvt_pk_bf16_f32 v73,v119,v120\n"
"v_mfma_f32_16x16x32_bf16 v[196:199],a[100:103],v[88:91],v[196:199]\n"
"v_exp_f32 v123,v21\n"
"v_mfma_f32_16x16x32_bf16 a[136:139],a[144:147],v[88:91],a[136:139]\n"
"ds_read_b128 a[88:91],v113 offset:6144\n"
"ds_read_b128 a[92:95],v113 offset:7168\n"
"v_mfma_f32_16x16x32_bf16 v[200:203],a[104:107],v[88:91],v[200:203]\n"
"v_exp_f32 v119,v22\n"
"v_mfma_f32_16x16x32_bf16 v[232:235],a[104:107],v[92:95],v[232:235]\n"
"buffer_store_dwordx4 a[188:191],v112,s[24:27],s3 offen offset:3072 sc1\n"
"v_mfma_f32_16x16x32_bf16 v[136:139],a[104:107],v[80:83],v[136:139]\n"
"v_exp_f32 v120,v23\n"
"v_mfma_f32_16x16x32_bf16 v[168:171],a[104:107],v[84:87],v[168:171]\n"
"s_or_b32 s3,s3,0x3c000\n"
"v_cvt_pk_bf16_f32 v74,v122,v123\n"
"v_cvt_pk_bf16_f32 v75,v119,v120\n"
"v_mfma_f32_16x16x32_bf16 a[132:135],a[144:147],v[84:87],a[132:135]\n"
"v_exp_f32 v122,v24\n"
"v_mfma_f32_16x16x32_bf16 v[172:175],a[108:111],v[84:87],v[172:175]\n"
"v_or3_b32 v118,v118,v71,v72\n"
"v_or3_b32 v118,v118,v73,v74\n"
"v_mfma_f32_16x16x32_bf16 v[204:207],a[108:111],v[88:91],v[204:207]\n"
"v_exp_f32 v123,v25\n"
"v_mfma_f32_16x16x32_bf16 v[236:239],a[108:111],v[92:95],v[236:239]\n"
"v_cvt_pk_bf16_f32 v76,v122,v123\n"
"v_or3_b32 v118,v118,v75,v76\n"
"v_mfma_f32_16x16x32_bf16 v[140:143],a[108:111],v[80:83],v[140:143]\n"
"v_exp_f32 v119,v26\n"
"v_mfma_f32_16x16x32_bf16 a[128:131],a[144:147],v[80:83],a[128:131]\n"
"v_mfma_f32_16x16x32_bf16 v[144:147],a[112:115],v[80:83],v[144:147]\n"
"v_exp_f32 v120,v27\n"
"v_mfma_f32_16x16x32_bf16 v[176:179],a[112:115],v[84:87],v[176:179]\n"
"v_cvt_pk_bf16_f32 v77,v119,v120\n"
"v_mfma_f32_16x16x32_bf16 v[208:211],a[112:115],v[88:91],v[208:211]\n"
"v_exp_f32 v122,v28\n"
"v_mfma_f32_16x16x32_bf16 v[240:243],a[112:115],v[92:95],v[240:243]\n"
"v_mfma_f32_16x16x32_bf16 v[244:247],a[116:119],v[92:95],v[244:247]\n"
"v_exp_f32 v123,v29\n"
"v_mfma_f32_16x16x32_bf16 v[148:151],a[116:119],v[80:83],v[148:151]\n"
"v_cvt_pk_bf16_f32 v78,v122,v123\n"
"v_or3_b32 v118,v118,v77,v78\n"
"v_mfma_f32_16x16x32_bf16 v[180:183],a[116:119],v[84:87],v[180:183]\n"
"v_exp_f32 v119,v30\n"
"v_mfma_f32_16x16x32_bf16 v[212:215],a[116:119],v[88:91],v[212:215]\n"
"buffer_store_dwordx4 a[236:239],v112,s[24:27],s56 offen offset:3072 sc1\n"
"v_mfma_f32_16x16x32_bf16 v[216:219],a[120:123],v[88:91],v[216:219]\n"
"v_exp_f32 v120,v31\n"
"v_mfma_f32_16x16x32_bf16 v[248:251],a[120:123],v[92:95],v[248:251]\n"
"v_cvt_pk_bf16_f32 v79,v119,v120\n"
"v_or_b32 v118,v118,v79\n"
"v_mfma_f32_16x16x32_bf16 v[152:155],a[120:123],v[80:83],v[152:155]\n"
"v_and_b32 v118,0x40004000,v118\n"
"v_cmp_ne_u32 vcc,0,v118\n"
"v_mfma_f32_16x16x32_bf16 v[184:187],a[120:123],v[84:87],v[184:187]\n"
"v_mfma_f32_16x16x32_bf16 v[188:191],a[124:127],v[84:87],v[188:191]\n"
"v_mfma_f32_16x16x32_bf16 v[220:223],a[124:127],v[88:91],v[220:223]\n"
"v_mfma_f32_16x16x32_bf16 v[252:255],a[124:127],v[92:95],v[252:255]\n"
"v_mfma_f32_16x16x32_bf16 v[156:159],a[124:127],v[80:83],v[156:159]\n"
"s_cbranch_vccnz .Lfk_cs_k2\n"
".Lfk_cr_k2:\n"
"s_waitcnt vmcnt(15) lgkmcnt(0)\n"
"s_barrier\n"
"s_branch .Lfk_g1\n"
".Lfk_winA:\n"
"v_max3_f32 v118,v32,v33,v34\n"
"v_max3_f32 v119,v35,v36,v37\n"
"v_max3_f32 v119,v119,v38,v39\n"
"v_max3_f32 v118,v118,v40,v41\n"
"v_max3_f32 v119,v119,v42,v43\n"
"v_max3_f32 v118,v118,v44,v45\n"
"v_max3_f32 v119,v119,v46,v47\n"
"v_max3_f32 v118,v118,v48,v49\n"
"v_max3_f32 v119,v119,v50,v51\n"
"v_max3_f32 v118,v118,v52,v53\n"
"v_max3_f32 v119,v119,v54,v55\n"
"v_max3_f32 v118,v118,v56,v57\n"
"v_max3_f32 v119,v119,v58,v59\n"
"v_max3_f32 v118,v118,v60,v61\n"
"v_max3_f32 v119,v119,v62,v63\n"
"v_max_f32 v118,v118,v119\n"
"v_cmp_lt_f32 vcc,s93,v118\n"
"s_nop 0\n"
"s_cbranch_vccnz .Lfk_cs_x2\n"
".Lfk_cr_x2:\n"
"s_mov_b32 s64,s61\n"
"s_cmp_eq_u32 s52,3\n"
"s_cselect_b32 s67,172032,0\n"
"v_mfma_f32_16x16x32_bf16 v[8:11],a[64:67],a[16:19],v[100:103]\n"
"s_swappc_b64 s[94:95],s[20:21]\n"
"ds_read_b128 a[96:99],v114 offset:24576\n"
"ds_read_b128 a[100:103],v114 offset:25600\n"
"v_mfma_f32_16x16x32_bf16 v[8:11],a[68:71],a[20:23],v[8:11]\n"
"ds_read_b128 a[104:107],v114 offset:26624\n"
"ds_read_b128 a[108:111],v114 offset:27648\n"
"v_mfma_f32_16x16x32_bf16 v[8:11],a[72:75],a[24:27],v[8:11]\n"
"ds_read_b128 a[112:115],v114 offset:28672\n"
"ds_read_b128 a[116:119],v114 offset:29696\n"
"v_mfma_f32_16x16x32_bf16 v[8:11],a[76:79],a[28:31],v[8:11]\n"
"ds_read_b128 a[120:123],v114 offset:30720\n"
"ds_read_b128 a[124:127],v114 offset:31744\n"
"v_mfma_f32_16x16x32_bf16 v[12:15],a[92:95],a[28:31],v[100:103]\n"
"v_add_f32 v0,0x80000000,v126\n"
"v_min_f32 v0,v0,v96\n"
"v_mfma_f32_16x16x32_bf16 v[12:15],a[88:91],a[24:27],v[12:15]\n"
"v_add_f32 v1,0xf149f2ca,v126\n"
"v_min_f32 v1,v1,v97\n"
"v_mfma_f32_16x16x32_bf16 v[12:15],a[84:87],a[20:23],v[12:15]\n"
"v_add_f32 v2,0xf1c9f2ca,v126\n"
"v_min_f32 v2,v2,v98\n"
"v_mfma_f32_16x16x32_bf16 v[12:15],a[80:83],a[16:19],v[12:15]\n"
"v_add_f32 v3,0xf2177617,v126\n"
"v_min_f32 v3,v3,v99\n"
"v_mfma_f32_16x16x32_bf16 v[20:23],a[80:83],a[32:35],v[104:107]\n"
"v_add_f32 v4,0xf349f2ca,v126\n"
"v_min_f32 v4,v4,v96\n"
"v_mfma_f32_16x16x32_bf16 v[20:23],a[84:87],a[36:39],v[20:23]\n"
"v_add_f32 v5,0xf35691f6,v126\n"
"v_min_f32 v5,v5,v97\n"
"v_mfma_f32_16x16x32_bf16 v[20:23],a[88:91],a[40:43],v[20:23]\n"
"v_add_f32 v6,0xf3633123,v126\n"
"v_min_f32 v6,v6,v98\n"
"v_mfma_f32_16x16x32_bf16 v[20:23],a[92:95],a[44:47],v[20:23]\n"
"v_add_f32 v7,0xf36fd050,v126\n"
"v_min_f32 v7,v7,v99\n"
"v_mfma_f32_16x16x32_bf16 v[16:19],a[76:79],a[44:47],v[104:107]\n"
"s_add_u32 s101,s101,4\n"
"v_exp_f32 v32,v32\n"
"v_mfma_f32_16x16x32_bf16 v[16:19],a[72:75],a[40:43],v[16:19]\n"
"s_add_u32 m0,s67,0\n"
"v_exp_f32 v33,v33\n"
"v_mfma_f32_16x16x32_bf16 v[16:19],a[68:71],a[36:39],v[16:19]\n"
"v_exp_f32 v34,v34\n"
"v_mfma_f32_16x16x32_bf16 v[16:19],a[64:67],a[32:35],v[16:19]\n"
"v_exp_f32 v35,v35\n"
"v_mfma_f32_16x16x32_bf16 v[24:27],a[64:67],a[48:51],v[108:111]\n"
"v_exp_f32 v36,v36\n"
"v_mfma_f32_16x16x32_bf16 v[24:27],a[68:71],a[52:55],v[24:27]\n"
"v_exp_f32 v37,v37\n"
"v_mfma_f32_16x16x32_bf16 v[24:27],a[72:75],a[56:59],v[24:27]\n"
"v_exp_f32 v38,v38\n"
"v_mfma_f32_16x16x32_bf16 v[24:27],a[76:79],a[60:63],v[24:27]\n"
"v_exp_f32 v39,v39\n"
"v_mfma_f32_16x16x32_bf16 v[28:31],a[92:95],a[60:63],v[108:111]\n"
"v_exp_f32 v40,v40\n"
"v_mfma_f32_16x16x32_bf16 v[28:31],a[88:91],a[56:59],v[28:31]\n"
"v_exp_f32 v41,v41\n"
"v_mfma_f32_16x16x32_bf16 v[28:31],a[84:87],a[52:55],v[28:31]\n"
"v_exp_f32 v42,v42\n"
"v_mfma_f32_16x16x32_bf16 v[28:31],a[80:83],a[48:51],v[28:31]\n"
"v_exp_f32 v43,v43\n"
"v_mfma_f32_16x16x32_bf16 v[4:7],a[80:83],a[0:3],v[4:7]\n"
"v_exp_f32 v44,v44\n"
"v_mfma_f32_16x16x32_bf16 v[4:7],a[84:87],a[4:7],v[4:7]\n"
"v_exp_f32 v45,v45\n"
"v_mfma_f32_16x16x32_bf16 v[4:7],a[88:91],a[8:11],v[4:7]\n"
"buffer_load_dwordx4 v112,s[24:27],s64 offen offset:0 sc1 lds\n"
"v_mfma_f32_16x16x32_bf16 v[4:7],a[92:95],a[12:15],v[4:7]\n"
"v_exp_f32 v46,v46\n"
"v_mfma_f32_16x16x32_bf16 v[0:3],a[76:79],a[12:15],v[0:3]\n"
"buffer_load_dwordx4 v112,s[24:27],s64 offen offset:1024 sc1 lds\n"
"v_mfma_f32_16x16x32_bf16 v[0:3],a[72:75],a[8:11],v[0:3]\n"
"v_exp_f32 v47,v47\n"
"v_mfma_f32_16x16x32_bf16 v[0:3],a[68:71],a[4:7],v[0:3]\n"
"buffer_load_dwordx4 v112,s[24:27],s64 offen offset:2048 sc1 lds\n"
"v_mfma_f32_16x16x32_bf16 v[0:3],a[64:67],a[0:3],v[0:3]\n"
"s_waitcnt lgkmcnt(0)\n"
"v_exp_f32 v48,v48\n"
"v_mfma_f32_16x16x32_bf16 v[128:131],a[96:99],v[64:67],v[128:131]\n"
"v_exp_f32 v49,v49\n"
"v_mfma_f32_16x16x32_bf16 v[160:163],a[96:99],v[68:71],v[160:163]\n"
"ds_read_b128 a[64:67],v113 offset:16384\n"
"ds_read_b128 a[68:71],v113 offset:17408\n"
"v_mfma_f32_16x16x32_bf16 v[192:195],a[96:99],v[72:75],v[192:195]\n"
"v_exp_f32 v50,v50\n"
"v_mfma_f32_16x16x32_bf16 v[224:227],a[96:99],v[76:79],v[224:227]\n"
"ds_read_b128 a[72:75],v113 offset:18432\n"
"ds_read_b128 a[76:79],v113 offset:19456\n"
"v_mfma_f32_16x16x32_bf16 a[140:143],a[144:147],v[76:79],a[140:143]\n"
"v_exp_f32 v51,v51\n"
"v_mfma_f32_16x16x32_bf16 v[228:231],a[100:103],v[76:79],v[228:231]\n"
"ds_read_b128 a[80:83],v113 offset:20480\n"
"ds_read_b128 a[84:87],v113 offset:21504\n"
"v_mfma_f32_16x16x32_bf16 v[132:135],a[100:103],v[64:67],v[132:135]\n"
"v_exp_f32 v52,v52\n"
"v_mfma_f32_16x16x32_bf16 v[164:167],a[100:103],v[68:71],v[164:167]\n"
"ds_read_b128 a[88:91],v113 offset:22528\n"
"ds_read_b128 a[92:95],v113 offset:23552\n"
"v_mfma_f32_16x16x32_bf16 v[196:199],a[100:103],v[72:75],v[196:199]\n"
"v_exp_f32 v53,v53\n"
"v_mfma_f32_16x16x32_bf16 a[136:139],a[144:147],v[72:75],a[136:139]\n"
"s_waitcnt vmcnt(14)\n"
"ds_add_u32 v124,a152\n"
"v_mfma_f32_16x16x32_bf16 v[200:203],a[104:107],v[72:75],v[200:203]\n"
"v_exp_f32 v54,v54\n"
"v_mfma_f32_16x16x32_bf16 v[232:235],a[104:107],v[76:79],v[232:235]\n"
"buffer_load_dwordx4 v112,s[24:27],s64 offen offset:3072 sc1 lds\n"
"v_mfma_f32_16x16x32_bf16 v[136:139],a[104:107],v[64:67],v[136:139]\n"
"s_add_u32 m0,s67,4096\n"
"v_exp_f32 v55,v55\n"
"v_mfma_f32_16x16x32_bf16 v[168:171],a[104:107],v[68:71],v[168:171]\n"
"buffer_load_dwordx4 v117,s[24:27],s64 offen offset:0 sc1 lds\n"
"v_mfma_f32_16x16x32_bf16 a[132:135],a[144:147],v[68:71],a[132:135]\n"
"v_exp_f32 v56,v56\n"
"v_mfma_f32_16x16x32_bf16 v[172:175],a[108:111],v[68:71],v[172:175]\n"
"buffer_load_dwordx4 v117,s[24:27],s64 offen offset:1024 sc1 lds\n"
"v_mfma_f32_16x16x32_bf16 v[204:207],a[108:111],v[72:75],v[204:207]\n"
"v_exp_f32 v57,v57\n"
"v_mfma_f32_16x16x32_bf16 v[236:239],a[108:111],v[76:79],v[236:239]\n"
"buffer_load_dwordx4 v117,s[24:27],s64 offen offset:2048 sc1 lds\n"
"v_mfma_f32_16x16x32_bf16 v[140:143],a[108:111],v[64:67],v[140:143]\n"
"v_exp_f32 v58,v58\n"
"v_mfma_f32_16x16x32_bf16 a[128:131],a[144:147],v[64:67],a[128:131]\n"
"v_cvt_pk_bf16_f32 v80,v32,v33\n"
"v_cvt_pk_bf16_f32 v81,v34,v35\n"
"v_mfma_f32_16x16x32_bf16 v[144:147],a[112:115],v[64:67],v[144:147]\n"
"v_exp_f32 v59,v59\n"
"v_mfma_f32_16x16x32_bf16 v[176:179],a[112:115],v[68:71],v[176:179]\n"
"v_cvt_pk_bf16_f32 v82,v36,v37\n"
"v_cvt_pk_bf16_f32 v83,v38,v39\n"
"v_mfma_f32_16x16x32_bf16 v[208:211],a[112:115],v[72:75],v[208:211]\n"
"v_exp_f32 v60,v60\n"
"v_mfma_f32_16x16x32_bf16 v[240:243],a[112:115],v[76:79],v[240:243]\n"
"buffer_load_dwordx4 v117,s[24:27],s64 offen offset:3072 sc1 lds\n"
"v_mfma_f32_16x16x32_bf16 v[244:247],a[116:119],v[76:79],v[244:247]\n"
"s_add_u32 s64,s64,16384\n"
"v_exp_f32 v61,v61\n"
"v_mfma_f32_16x16x32_bf16 v[148:151],a[116:119],v[64:67],v[148:151]\n"
"s_andn2_b32 m0,s55,16384\n"
"s_add_u32 s54,s54,s22\n"
"v_cvt_pk_bf16_f32 v85,v42,v43\n"
"v_mfma_f32_16x16x32_bf16 v[180:183],a[116:119],v[68:71],v[180:183]\n"
"v_exp_f32 v62,v62\n"
"v_mfma_f32_16x16x32_bf16 v[212:215],a[116:119],v[72:75],v[212:215]\n"
"ds_read_b32 v125,v124\n"
"v_cvt_pk_bf16_f32 v84,v40,v41\n"
"v_mfma_f32_16x16x32_bf16 v[216:219],a[120:123],v[72:75],v[216:219]\n"
"v_exp_f32 v63,v63\n"
"v_mfma_f32_16x16x32_bf16 v[248:251],a[120:123],v[76:79],v[248:251]\n"
"buffer_load_dwordx4 v115,s[32:35],s54 offen sc1 lds\n"
"v_mfma_f32_16x16x32_bf16 v[152:155],a[120:123],v[64:67],v[152:155]\n"
"buffer_store_dwordx4 a[240:243],v112,s[24:27],s56 offen offset:0 sc1\n"
"v_mfma_f32_16x16x32_bf16 v[184:187],a[120:123],v[68:71],v[184:187]\n"
"v_cvt_pk_bf16_f32 v86,v44,v45\n"
"v_cvt_pk_bf16_f32 v87,v46,v47\n"
"v_mfma_f32_16x16x32_bf16 v[188:191],a[124:127],v[68:71],v[188:191]\n"
"s_waitcnt lgkmcnt(0)\n"
"v_cvt_pk_bf16_f32 v88,v48,v49\n"
"v_cvt_pk_bf16_f32 v89,v50,v51\n"
"v_mfma_f32_16x16x32_bf16 v[220:223],a[124:127],v[72:75],v[220:223]\n"
"v_cvt_pk_bf16_f32 v90,v52,v53\n"
"v_cvt_pk_bf16_f32 v91,v54,v55\n"
"v_mfma_f32_16x16x32_bf16 v[252:255],a[124:127],v[76:79],v[252:255]\n"
"v_cvt_pk_bf16_f32 v92,v56,v57\n"
"v_cvt_pk_bf16_f32 v93,v58,v59\n"
"v_mfma_f32_16x16x32_bf16 v[156:159],a[124:127],v[64:67],v[156:159]\n"
"v_cvt_pk_bf16_f32 v94,v60,v61\n"
"v_cvt_pk_bf16_f32 v95,v62,v63\n"
"v_max3_f32 v118,v8,v9,v10\n"
"v_max3_f32 v118,v118,v11,v12\n"
"v_max3_f32 v118,v118,v13,v14\n"
"v_max3_f32 v118,v118,v15,v20\n"
"v_max3_f32 v118,v118,v21,v22\n"
"v_max3_f32 v118,v118,v23,v16\n"
"v_max3_f32 v118,v118,v17,v18\n"
"v_max3_f32 v118,v118,v19,v24\n"
"v_max3_f32 v118,v118,v25,v26\n"
"v_max3_f32 v118,v118,v27,v28\n"
"v_max3_f32 v118,v118,v29,v30\n"
"v_max3_f32 v118,v118,v31,v4\n"
"v_max3_f32 v118,v118,v5,v6\n"
"v_max3_f32 v118,v118,v7,v0\n"
"v_max3_f32 v118,v118,v1,v2\n"
"v_max_f32 v118,v118,v3\n"
"v_cmp_lt_f32 vcc,s93,v118\n"
"s_nop 0\n"
"s_cbranch_vccnz .Lfk_cs_u3\n"
".Lfk_cr_u3:\n"
"v_cmp_gt_u32 vcc,s101,v125\n"
"s_nop 0\n"
"s_cbranch_vccnz .Lfk_cs_mu3\n"
".Lfk_cr_mu3:\n"
"v_mfma_f32_16x16x32_bf16 v[48:51],a[64:67],a[32:35],v[104:107]\n"
"ds_read_b128 a[96:99],v114 offset:57344\n"
"ds_read_b128 a[100:103],v114 offset:58368\n"
"v_mfma_f32_16x16x32_bf16 v[48:51],a[68:71],a[36:39],v[48:51]\n"
"ds_read_b128 a[104:107],v114 offset:59392\n"
"ds_read_b128 a[108:111],v114 offset:60416\n"
"v_mfma_f32_16x16x32_bf16 v[48:51],a[72:75],a[40:43],v[48:51]\n"
"ds_read_b128 a[112:115],v114 offset:61440\n"
"ds_read_b128 a[116:119],v114 offset:62464\n"
"v_mfma_f32_16x16x32_bf16 v[48:51],a[76:79],a[44:47],v[48:51]\n"
"ds_read_b128 a[120:123],v114 offset:63488\n"
"ds_read_b128 a[124:127],v114 offset:64512\n"
"v_mfma_f32_16x16x32_bf16 v[52:55],a[92:95],a[44:47],v[104:107]\n"
"ds_read_b128 a[0:3],v116 offset:40960\n"
"ds_read_b128 a[4:7],v116 offset:41984\n"
"v_mfma_f32_16x16x32_bf16 v[52:55],a[88:91],a[40:43],v[52:55]\n"
"ds_read_b128 a[8:11],v116 offset:43008\n"
"ds_read_b128 a[12:15],v116 offset:44032\n"
"v_mfma_f32_16x16x32_bf16 v[52:55],a[84:87],a[36:39],v[52:55]\n"
"v_add_f32 v40,0x80000000,v126\n"
"v_min_f32 v40,v40,v100\n"
"v_mfma_f32_16x16x32_bf16 v[52:55],a[80:83],a[32:35],v[52:55]\n"
"v_add_f32 v41,0xf149f2ca,v126\n"
"v_min_f32 v41,v41,v101\n"
"v_mfma_f32_16x16x32_bf16 v[60:63],a[80:83],a[48:51],v[108:111]\n"
"v_add_f32 v42,0xf1c9f2ca,v126\n"
"v_min_f32 v42,v42,v102\n"
"v_mfma_f32_16x16x32_bf16 v[60:63],a[84:87],a[52:55],v[60:63]\n"
"v_add_f32 v43,0xf2177617,v126\n"
"v_min_f32 v43,v43,v103\n"
"v_mfma_f32_16x16x32_bf16 v[60:63],a[88:91],a[56:59],v[60:63]\n"
"v_add_f32 v44,0xf349f2ca,v126\n"
"v_min_f32 v44,v44,v100\n"
"v_mfma_f32_16x16x32_bf16 v[60:63],a[92:95],a[60:63],v[60:63]\n"
"v_add_f32 v45,0xf35691f6,v126\n"
"v_min_f32 v45,v45,v101\n"
"v_mfma_f32_16x16x32_bf16 v[56:59],a[76:79],a[60:63],v[108:111]\n"
"v_add_f32 v46,0xf3633123,v126\n"
"v_min_f32 v46,v46,v102\n"
"v_mfma_f32_16x16x32_bf16 v[56:59],a[72:75],a[56:59],v[56:59]\n"
"v_add_f32 v47,0xf36fd050,v126\n"
"v_min_f32 v47,v47,v103\n"
"v_mfma_f32_16x16x32_bf16 v[56:59],a[68:71],a[52:55],v[56:59]\n"
"s_add_u32 s101,s101,4\n"
"v_exp_f32 v0,v0\n"
"v_mfma_f32_16x16x32_bf16 v[56:59],a[64:67],a[48:51],v[56:59]\n"
"s_add_u32 m0,s55,4096\n"
"v_exp_f32 v1,v1\n"
"v_mfma_f32_16x16x32_bf16 v[40:43],a[64:67],a[16:19],v[40:43]\n"
"s_add_u32 s54,s54,s22\n"
"v_exp_f32 v2,v2\n"
"v_mfma_f32_16x16x32_bf16 v[40:43],a[68:71],a[20:23],v[40:43]\n"
"v_exp_f32 v3,v3\n"
"v_mfma_f32_16x16x32_bf16 v[40:43],a[72:75],a[24:27],v[40:43]\n"
"v_exp_f32 v4,v4\n"
"v_mfma_f32_16x16x32_bf16 v[40:43],a[76:79],a[28:31],v[40:43]\n"
"v_exp_f32 v5,v5\n"
"v_mfma_f32_16x16x32_bf16 v[44:47],a[92:95],a[28:31],v[44:47]\n"
"v_exp_f32 v6,v6\n"
"v_mfma_f32_16x16x32_bf16 v[44:47],a[88:91],a[24:27],v[44:47]\n"
"v_exp_f32 v7,v7\n"
"v_mfma_f32_16x16x32_bf16 v[44:47],a[84:87],a[20:23],v[44:47]\n"
"v_exp_f32 v8,v8\n"
"v_mfma_f32_16x16x32_bf16 v[44:47],a[80:83],a[16:19],v[44:47]\n"
"s_waitcnt lgkmcnt(4)\n"
"v_exp_f32 v9,v9\n"
"v_mfma_f32_16x16x32_bf16 v[128:131],a[96:99],v[80:83],v[128:131]\n"
"v_exp_f32 v10,v10\n"
"v_mfma_f32_16x16x32_bf16 v[160:163],a[96:99],v[84:87],v[160:163]\n"
"v_exp_f32 v11,v11\n"
"v_mfma_f32_16x16x32_bf16 v[192:195],a[96:99],v[88:91],v[192:195]\n"
"v_exp_f32 v12,v12\n"
"v_mfma_f32_16x16x32_bf16 v[224:227],a[96:99],v[92:95],v[224:227]\n"
"v_exp_f32 v13,v13\n"
"v_mfma_f32_16x16x32_bf16 a[140:143],a[144:147],v[92:95],a[140:143]\n"
"v_exp_f32 v14,v14\n"
"v_mfma_f32_16x16x32_bf16 v[228:231],a[100:103],v[92:95],v[228:231]\n"
"v_exp_f32 v15,v15\n"
"v_mfma_f32_16x16x32_bf16 v[132:135],a[100:103],v[80:83],v[132:135]\n"
"v_exp_f32 v16,v16\n"
"v_mfma_f32_16x16x32_bf16 v[164:167],a[100:103],v[84:87],v[164:167]\n"
"v_exp_f32 v17,v17\n"
"v_mfma_f32_16x16x32_bf16 v[196:199],a[100:103],v[88:91],v[196:199]\n"
"v_exp_f32 v18,v18\n"
"v_mfma_f32_16x16x32_bf16 a[136:139],a[144:147],v[88:91],a[136:139]\n"
"v_exp_f32 v19,v19\n"
"v_mfma_f32_16x16x32_bf16 v[200:203],a[104:107],v[88:91],v[200:203]\n"
"v_exp_f32 v20,v20\n"
"v_mfma_f32_16x16x32_bf16 v[232:235],a[104:107],v[92:95],v[232:235]\n"
"v_exp_f32 v21,v21\n"
"v_mfma_f32_16x16x32_bf16 v[136:139],a[104:107],v[80:83],v[136:139]\n"
"ds_read_b128 a[64:67],v113 offset:49152\n"
"ds_read_b128 a[68:71],v113 offset:50176\n"
"v_mfma_f32_16x16x32_bf16 v[168:171],a[104:107],v[84:87],v[168:171]\n"
"v_exp_f32 v22,v22\n"
"v_mfma_f32_16x16x32_bf16 a[132:135],a[144:147],v[84:87],a[132:135]\n"
"v_exp_f32 v23,v23\n"
"v_mfma_f32_16x16x32_bf16 v[172:175],a[108:111],v[84:87],v[172:175]\n"
"ds_read_b128 a[72:75],v113 offset:51200\n"
"ds_read_b128 a[76:79],v113 offset:52224\n"
"v_mfma_f32_16x16x32_bf16 v[204:207],a[108:111],v[88:91],v[204:207]\n"
"v_exp_f32 v24,v24\n"
"v_mfma_f32_16x16x32_bf16 v[236:239],a[108:111],v[92:95],v[236:239]\n"
"ds_read_b128 a[80:83],v113 offset:53248\n"
"ds_read_b128 a[84:87],v113 offset:54272\n"
"v_mfma_f32_16x16x32_bf16 v[140:143],a[108:111],v[80:83],v[140:143]\n"
"v_exp_f32 v25,v25\n"
"v_mfma_f32_16x16x32_bf16 a[128:131],a[144:147],v[80:83],a[128:131]\n"
"v_exp_f32 v26,v26\n"
"v_mfma_f32_16x16x32_bf16 v[144:147],a[112:115],v[80:83],v[144:147]\n"
"ds_read_b128 a[88:91],v113 offset:55296\n"
"ds_read_b128 a[92:95],v113 offset:56320\n"
"v_mfma_f32_16x16x32_bf16 v[176:179],a[112:115],v[84:87],v[176:179]\n"
"v_exp_f32 v27,v27\n"
"v_mfma_f32_16x16x32_bf16 v[208:211],a[112:115],v[88:91],v[208:211]\n"
"s_waitcnt vmcnt(11)\n"
"ds_add_u32 v124,a152\n"
"v_mfma_f32_16x16x32_bf16 v[240:243],a[112:115],v[92:95],v[240:243]\n"
"v_exp_f32 v28,v28\n"
"v_mfma_f32_16x16x32_bf16 v[244:247],a[116:119],v[92:95],v[244:247]\n"
"v_exp_f32 v29,v29\n"
"v_mfma_f32_16x16x32_bf16 v[148:151],a[116:119],v[80:83],v[148:151]\n"
"buffer_load_dwordx4 v115,s[32:35],s54 offen sc1 lds\n"
"v_mfma_f32_16x16x32_bf16 v[180:183],a[116:119],v[84:87],v[180:183]\n"
"s_add_u32 m0,s67,16384\n"
"v_exp_f32 v30,v30\n"
"v_mfma_f32_16x16x32_bf16 v[212:215],a[116:119],v[88:91],v[212:215]\n"
"ds_read_b32 v125,v124\n"
"v_cvt_pk_bf16_f32 v64,v0,v1\n"
"v_mfma_f32_16x16x32_bf16 v[216:219],a[120:123],v[88:91],v[216:219]\n"
"v_exp_f32 v31,v31\n"
"v_mfma_f32_16x16x32_bf16 v[248:251],a[120:123],v[92:95],v[248:251]\n"
"buffer_load_dwordx4 v112,s[24:27],s64 offen offset:0 sc1 lds\n"
"v_mfma_f32_16x16x32_bf16 v[152:155],a[120:123],v[80:83],v[152:155]\n"
"buffer_load_dwordx4 v112,s[24:27],s64 offen offset:1024 sc1 lds\n"
"v_mfma_f32_16x16x32_bf16 v[184:187],a[120:123],v[84:87],v[184:187]\n"
"buffer_load_dwordx4 v112,s[24:27],s64 offen offset:2048 sc1 lds\n"
"v_mfma_f32_16x16x32_bf16 v[188:191],a[124:127],v[84:87],v[188:191]\n"
"s_waitcnt lgkmcnt(0)\n"
"buffer_load_dwordx4 v112,s[24:27],s64 offen offset:3072 sc1 lds\n"
"v_mfma_f32_16x16x32_bf16 v[220:223],a[124:127],v[88:91],v[220:223]\n"
"s_add_u32 m0,s67,20480\n"
"v_cvt_pk_bf16_f32 v65,v2,v3\n"
"v_cvt_pk_bf16_f32 v66,v4,v5\n"
"v_mfma_f32_16x16x32_bf16 v[252:255],a[124:127],v[92:95],v[252:255]\n"
"buffer_load_dwordx4 v117,s[24:27],s64 offen offset:0 sc1 lds\n"
"v_mfma_f32_16x16x32_bf16 v[156:159],a[124:127],v[80:83],v[156:159]\n"
"buffer_load_dwordx4 v117,s[24:27],s64 offen offset:1024 sc1 lds\n"
"buffer_load_dwordx4 v117,s[24:27],s64 offen offset:2048 sc1 lds\n"
"buffer_load_dwordx4 v117,s[24:27],s64 offen offset:3072 sc1 lds\n"
"buffer_store_dwordx4 a[244:247],v112,s[24:27],s56 offen offset:1024 sc1\n"
"s_add_u32 s64,s64,16384\n"
"v_cvt_pk_bf16_f32 v67,v6,v7\n"
"v_cvt_pk_bf16_f32 v68,v8,v9\n"
"v_cvt_pk_bf16_f32 v69,v10,v11\n"
"v_cvt_pk_bf16_f32 v70,v12,v13\n"
"v_cvt_pk_bf16_f32 v71,v14,v15\n"
"v_cvt_pk_bf16_f32 v72,v16,v17\n"
"v_cvt_pk_bf16_f32 v73,v18,v19\n"
"v_cvt_pk_bf16_f32 v74,v20,v21\n"
"v_cvt_pk_bf16_f32 v75,v22,v23\n"
"v_cvt_pk_bf16_f32 v76,v24,v25\n"
"v_cvt_pk_bf16_f32 v77,v26,v27\n"
"v_cvt_pk_bf16_f32 v78,v28,v29\n"
"v_cvt_pk_bf16_f32 v79,v30,v31\n"
"v_max3_f32 v118,v48,v49,v50\n"
"v_max3_f32 v118,v118,v51,v52\n"
"v_max3_f32 v118,v118,v53,v54\n"
"v_max3_f32 v118,v118,v55,v60\n"
"v_max3_f32 v118,v118,v61,v62\n"
"v_max3_f32 v118,v118,v63,v56\n"
"v_max3_f32 v118,v118,v57,v58\n"
"v_max3_f32 v118,v118,v59,v40\n"
"v_max3_f32 v118,v118,v41,v42\n"
"v_max3_f32 v118,v118,v43,v44\n"
"v_max3_f32 v118,v118,v45,v46\n"
"v_max_f32 v118,v118,v47\n"
"v_cmp_lt_f32 vcc,s93,v118\n"
"s_nop 0\n"
"s_cbranch_vccnz .Lfk_cs_u0\n"
".Lfk_cr_u0:\n"
"v_cmp_gt_u32 vcc,s101,v125\n"
"s_nop 0\n"
"s_cbranch_vccnz .Lfk_cs_mu0\n"
".Lfk_cr_mu0:\n"
"v_mfma_f32_16x16x32_bf16 v[24:27],a[64:67],a[48:51],v[108:111]\n"
"ds_read_b128 a[96:99],v114 offset:40960\n"
"ds_read_b128 a[100:103],v114 offset:41984\n"
"v_mfma_f32_16x16x32_bf16 v[24:27],a[68:71],a[52:55],v[24:27]\n"
"ds_read_b128 a[104:107],v114 offset:43008\n"
"ds_read_b128 a[108:111],v114 offset:44032\n"
"v_mfma_f32_16x16x32_bf16 v[24:27],a[72:75],a[56:59],v[24:27]\n"
"ds_read_b128 a[112:115],v114 offset:45056\n"
"ds_read_b128 a[116:119],v114 offset:46080\n"
"v_mfma_f32_16x16x32_bf16 v[24:27],a[76:79],a[60:63],v[24:27]\n"
"ds_read_b128 a[120:123],v114 offset:47104\n"
"ds_read_b128 a[124:127],v114 offset:48128\n"
"v_mfma_f32_16x16x32_bf16 v[28:31],a[92:95],a[60:63],v[108:111]\n"
"ds_read_b128 a[16:19],v116 offset:8192\n"
"ds_read_b128 a[20:23],v116 offset:9216\n"
"v_mfma_f32_16x16x32_bf16 v[28:31],a[88:91],a[56:59],v[28:31]\n"
"ds_read_b128 a[24:27],v116 offset:10240\n"
"ds_read_b128 a[28:31],v116 offset:11264\n"
"v_mfma_f32_16x16x32_bf16 v[28:31],a[84:87],a[52:55],v[28:31]\n"
"v_add_f32 v16,0x80000000,v126\n"
"v_min_f32 v16,v16,v104\n"
"v_mfma_f32_16x16x32_bf16 v[28:31],a[80:83],a[48:51],v[28:31]\n"
"v_add_f32 v17,0xf149f2ca,v126\n"
"v_add_f32 v18,0xf1c9f2ca,v126\n"
"v_add_f32 v19,0xf2177617,v126\n"
"v_min_f32 v19,v19,v107\n"
"v_add_f32 v20,0xf349f2ca,v126\n"
"v_min_f32 v20,v20,v104\n"
"v_add_f32 v21,0xf35691f6,v126\n"
"v_min_f32 v21,v21,v105\n"
"v_add_f32 v22,0xf3633123,v126\n"
"v_min_f32 v22,v22,v106\n"
"v_add_f32 v23,0xf36fd050,v126\n"
"v_min_f32 v23,v23,v107\n"
"v_min_f32 v18,v18,v106\n"
"v_min_f32 v17,v17,v105\n"
"v_mfma_f32_16x16x32_bf16 v[20:23],a[80:83],a[32:35],v[20:23]\n"
"s_add_u32 s101,s101,4\n"
"v_exp_f32 v40,v40\n"
"v_mfma_f32_16x16x32_bf16 v[20:23],a[84:87],a[36:39],v[20:23]\n"
"s_add_u32 m0,s55,8192\n"
"v_exp_f32 v41,v41\n"
"v_mfma_f32_16x16x32_bf16 v[20:23],a[88:91],a[40:43],v[20:23]\n"
"s_add_u32 s54,s54,s22\n"
"v_exp_f32 v42,v42\n"
"v_mfma_f32_16x16x32_bf16 v[20:23],a[92:95],a[44:47],v[20:23]\n"
"v_exp_f32 v43,v43\n"
"v_mfma_f32_16x16x32_bf16 v[16:19],a[76:79],a[44:47],v[16:19]\n"
"v_exp_f32 v44,v44\n"
"v_mfma_f32_16x16x32_bf16 v[16:19],a[72:75],a[40:43],v[16:19]\n"
"v_exp_f32 v45,v45\n"
"v_mfma_f32_16x16x32_bf16 v[16:19],a[68:71],a[36:39],v[16:19]\n"
"v_exp_f32 v46,v46\n"
"v_mfma_f32_16x16x32_bf16 v[16:19],a[64:67],a[32:35],v[16:19]\n"
"s_waitcnt lgkmcnt(4)\n"
"v_exp_f32 v47,v47\n"
"v_mfma_f32_16x16x32_bf16 v[128:131],a[96:99],v[64:67],v[128:131]\n"
"v_exp_f32 v48,v48\n"
"v_mfma_f32_16x16x32_bf16 v[160:163],a[96:99],v[68:71],v[160:163]\n"
"v_exp_f32 v49,v49\n"
"v_mfma_f32_16x16x32_bf16 v[192:195],a[96:99],v[72:75],v[192:195]\n"
"ds_read_b128 a[64:67],v113 offset:32768\n"
"ds_read_b128 a[68:71],v113 offset:33792\n"
"v_mfma_f32_16x16x32_bf16 v[224:227],a[96:99],v[76:79],v[224:227]\n"
"v_exp_f32 v50,v50\n"
"v_mfma_f32_16x16x32_bf16 a[140:143],a[144:147],v[76:79],a[140:143]\n"
"ds_read_b128 a[72:75],v113 offset:34816\n"
"ds_read_b128 a[76:79],v113 offset:35840\n"
"v_mfma_f32_16x16x32_bf16 v[228:231],a[100:103],v[76:79],v[228:231]\n"
"v_exp_f32 v51,v51\n"
"v_mfma_f32_16x16x32_bf16 v[132:135],a[100:103],v[64:67],v[132:135]\n"
"ds_read_b128 a[80:83],v113 offset:36864\n"
"ds_read_b128 a[84:87],v113 offset:37888\n"
"v_mfma_f32_16x16x32_bf16 v[164:167],a[100:103],v[68:71],v[164:167]\n"
"v_exp_f32 v52,v52\n"
"v_mfma_f32_16x16x32_bf16 v[196:199],a[100:103],v[72:75],v[196:199]\n"
"v_exp_f32 v53,v53\n"
"v_mfma_f32_16x16x32_bf16 a[136:139],a[144:147],v[72:75],a[136:139]\n"
"ds_read_b128 a[88:91],v113 offset:38912\n"
"ds_read_b128 a[92:95],v113 offset:39936\n"
"v_mfma_f32_16x16x32_bf16 v[200:203],a[104:107],v[72:75],v[200:203]\n"
"v_exp_f32 v54,v54\n"
"v_mfma_f32_16x16x32_bf16 v[232:235],a[104:107],v[76:79],v[232:235]\n"
"s_waitcnt vmcnt(11)\n"
"ds_add_u32 v124,a152\n"
"v_mfma_f32_16x16x32_bf16 v[136:139],a[104:107],v[64:67],v[136:139]\n"
"v_exp_f32 v55,v55\n"
"v_mfma_f32_16x16x32_bf16 v[168:171],a[104:107],v[68:71],v[168:171]\n"
"buffer_load_dwordx4 v115,s[32:35],s54 offen sc1 lds\n"
"v_mfma_f32_16x16x32_bf16 a[132:135],a[144:147],v[68:71],a[132:135]\n"
"s_add_u32 m0,s67,49152\n"
"v_exp_f32 v56,v56\n"
"v_mfma_f32_16x16x32_bf16 v[172:175],a[108:111],v[68:71],v[172:175]\n"
"buffer_load_dwordx4 v112,s[24:27],s64 offen offset:0 sc1 lds\n"
"v_mfma_f32_16x16x32_bf16 v[204:207],a[108:111],v[72:75],v[204:207]\n"
"v_exp_f32 v57,v57\n"
"v_mfma_f32_16x16x32_bf16 v[236:239],a[108:111],v[76:79],v[236:239]\n"
"buffer_load_dwordx4 v112,s[24:27],s64 offen offset:1024 sc1 lds\n"
"v_mfma_f32_16x16x32_bf16 v[140:143],a[108:111],v[64:67],v[140:143]\n"
"v_exp_f32 v58,v58\n"
"v_mfma_f32_16x16x32_bf16 a[128:131],a[144:147],v[64:67],a[128:131]\n"
"buffer_load_dwordx4 v112,s[24:27],s64 offen offset:2048 sc1 lds\n"
"v_mfma_f32_16x16x32_bf16 v[144:147],a[112:115],v[64:67],v[144:147]\n"
"v_exp_f32 v59,v59\n"
"v_mfma_f32_16x16x32_bf16 v[176:179],a[112:115],v[68:71],v[176:179]\n"
"buffer_load_dwordx4 v112,s[24:27],s64 offen offset:3072 sc1 lds\n"
"v_mfma_f32_16x16x32_bf16 v[208:211],a[112:115],v[72:75],v[208:211]\n"
"s_add_u32 m0,s67,53248\n"
"v_exp_f32 v60,v60\n"
"v_mfma_f32_16x16x32_bf16 v[240:243],a[112:115],v[76:79],v[240:243]\n"
"buffer_load_dwordx4 v117,s[24:27],s64 offen offset:0 sc1 lds\n"
"v_mfma_f32_16x16x32_bf16 v[244:247],a[116:119],v[76:79],v[244:247]\n"
"v_exp_f32 v61,v61\n"
"v_mfma_f32_16x16x32_bf16 v[148:151],a[116:119],v[64:67],v[148:151]\n"
"buffer_load_dwordx4 v117,s[24:27],s64 offen offset:1024 sc1 lds\n"
"v_mfma_f32_16x16x32_bf16 v[180:183],a[116:119],v[68:71],v[180:183]\n"
"v_exp_f32 v62,v62\n"
"v_mfma_f32_16x16x32_bf16 v[212:215],a[116:119],v[72:75],v[212:215]\n"
"ds_read_b32 v125,v124\n"
"v_cvt_pk_bf16_f32 v84,v40,v41\n"
"v_mfma_f32_16x16x32_bf16 v[216:219],a[120:123],v[72:75],v[216:219]\n"
"v_exp_f32 v63,v63\n"
"v_mfma_f32_16x16x32_bf16 v[248:251],a[120:123],v[76:79],v[248:251]\n"
"buffer_load_dwordx4 v117,s[24:27],s64 offen offset:2048 sc1 lds\n"
"v_mfma_f32_16x16x32_bf16 v[152:155],a[120:123],v[64:67],v[152:155]\n"
"buffer_load_dwordx4 v117,s[24:27],s64 offen offset:3072 sc1 lds\n"
"v_mfma_f32_16x16x32_bf16 v[184:187],a[120:123],v[68:71],v[184:187]\n"
"s_add_u32 s64,s64,16384\n"
"buffer_store_dwordx4 a[248:251],v112,s[24:27],s56 offen offset:2048 sc1\n"
"v_mfma_f32_16x16x32_bf16 v[188:191],a[124:127],v[68:71],v[188:191]\n"
"s_waitcnt lgkmcnt(0)\n"
"s_and_b32 s67,s67,8192\n"
"v_cvt_pk_bf16_f32 v85,v42,v43\n"
"v_mfma_f32_16x16x32_bf16 v[220:223],a[124:127],v[72:75],v[220:223]\n"
"v_cvt_pk_bf16_f32 v86,v44,v45\n"
"v_cvt_pk_bf16_f32 v87,v46,v47\n"
"v_mfma_f32_16x16x32_bf16 v[252:255],a[124:127],v[76:79],v[252:255]\n"
"v_cvt_pk_bf16_f32 v88,v48,v49\n"
"v_cvt_pk_bf16_f32 v89,v50,v51\n"
"v_mfma_f32_16x16x32_bf16 v[156:159],a[124:127],v[64:67],v[156:159]\n"
"v_cvt_pk_bf16_f32 v90,v52,v53\n"
"v_cvt_pk_bf16_f32 v91,v54,v55\n"
"v_cvt_pk_bf16_f32 v92,v56,v57\n"
"v_cvt_pk_bf16_f32 v93,v58,v59\n"
"v_cvt_pk_bf16_f32 v94,v60,v61\n"
"v_cvt_pk_bf16_f32 v95,v62,v63\n"
"v_max3_f32 v118,v24,v25,v26\n"
"v_max3_f32 v118,v118,v27,v28\n"
"v_max3_f32 v118,v118,v29,v30\n"
"v_max3_f32 v118,v118,v31,v20\n"
"v_max3_f32 v118,v118,v21,v22\n"
"v_max3_f32 v118,v118,v23,v16\n"
"v_max3_f32 v118,v118,v17,v18\n"
"v_max_f32 v118,v118,v19\n"
"v_cmp_lt_f32 vcc,s93,v118\n"
"s_nop 0\n"
"s_cbranch_vccnz .Lfk_cs_u1\n"
".Lfk_cr_u1:\n"
"v_cmp_gt_u32 vcc,s101,v125\n"
"s_nop 0\n"
"s_cbranch_vccnz .Lfk_cs_mu1\n"
".Lfk_cr_mu1:\n"
"v_mov_b32 v113,v112\n"
"v_add_f32 v56,0x80000000,v126\n"
"v_min_f32 v56,v56,v108\n"
"v_add_f32 v57,0xf149f2ca,v126\n"
"v_min_f32 v57,v57,v109\n"
"v_add_f32 v58,0xf1c9f2ca,v126\n"
"v_min_f32 v58,v58,v110\n"
"v_add_f32 v59,0xf2177617,v126\n"
"v_min_f32 v59,v59,v111\n"
"v_add_f32 v60,0xf349f2ca,v126\n"
"v_min_f32 v60,v60,v108\n"
"v_add_f32 v61,0xf35691f6,v126\n"
"v_min_f32 v61,v61,v109\n"
"v_add_f32 v62,0xf3633123,v126\n"
"v_min_f32 v62,v62,v110\n"
"v_add_f32 v63,0xf36fd050,v126\n"
"v_min_f32 v63,v63,v111\n"
"v_exp_f32 v16,v16\n"
"v_mfma_f32_16x16x32_bf16 v[56:59],a[64:67],a[48:51],v[56:59]\n"
"s_add_u32 s101,s101,4\n"
"ds_read_b128 a[96:99],v114 offset:8192\n"
"ds_read_b128 a[100:103],v114 offset:9216\n"
"v_exp_f32 v17,v17\n"
"v_mfma_f32_16x16x32_bf16 v[56:59],a[68:71],a[52:55],v[56:59]\n"
"s_add_u32 m0,s55,12288\n"
"ds_read_b128 a[104:107],v114 offset:10240\n"
"ds_read_b128 a[108:111],v114 offset:11264\n"
"v_mfma_f32_16x16x32_bf16 v[56:59],a[72:75],a[56:59],v[56:59]\n"
"ds_read_b128 a[112:115],v114 offset:12288\n"
"ds_read_b128 a[116:119],v114 offset:13312\n"
"ds_read_b128 a[120:123],v114 offset:14336\n"
"ds_read_b128 a[124:127],v114 offset:15360\n"
"v_exp_f32 v18,v18\n"
"v_mfma_f32_16x16x32_bf16 v[56:59],a[76:79],a[60:63],v[56:59]\n"
"s_add_u32 s54,s54,s22\n"
"ds_read_b128 a[32:35],v116 offset:24576\n"
"ds_read_b128 a[36:39],v116 offset:25600\n"
"v_mfma_f32_16x16x32_bf16 v[60:63],a[92:95],a[60:63],v[60:63]\n"
"ds_read_b128 a[40:43],v116 offset:26624\n"
"ds_read_b128 a[44:47],v116 offset:27648\n"
"buffer_load_dwordx4 v115,s[32:35],s54 offen sc1 lds\n"
"v_mfma_f32_16x16x32_bf16 v[60:63],a[88:91],a[56:59],v[60:63]\n"
"s_add_u32 m0,s67,32768\n"
"v_exp_f32 v19,v19\n"
"v_cvt_pk_bf16_f32 v72,v16,v17\n"
"v_mfma_f32_16x16x32_bf16 v[60:63],a[84:87],a[52:55],v[60:63]\n"
"buffer_load_dwordx4 v112,s[24:27],s64 offen offset:0 sc1 lds\n"
"v_cvt_pk_bf16_f32 v73,v18,v19\n"
"v_exp_f32 v20,v20\n"
"v_mfma_f32_16x16x32_bf16 v[60:63],a[80:83],a[48:51],v[60:63]\n"
"ds_read_b128 a[64:67],v113 offset:0\n"
"ds_read_b128 a[68:71],v113 offset:1024\n"
"s_waitcnt lgkmcnt(6)\n"
"v_mfma_f32_16x16x32_bf16 v[160:163],a[96:99],v[84:87],v[160:163]\n"
"ds_read_b128 a[72:75],v113 offset:2048\n"
"ds_read_b128 a[76:79],v113 offset:3072\n"
"ds_read_b128 a[80:83],v113 offset:4096\n"
"ds_read_b128 a[84:87],v113 offset:5120\n"
"v_exp_f32 v21,v21\n"
"v_mfma_f32_16x16x32_bf16 a[132:135],a[144:147],v[84:87],a[132:135]\n"
"ds_read_b128 a[88:91],v113 offset:6144\n"
"ds_read_b128 a[92:95],v113 offset:7168\n"
"v_exp_f32 v22,v22\n"
"v_mfma_f32_16x16x32_bf16 v[164:167],a[100:103],v[84:87],v[164:167]\n"
"buffer_load_dwordx4 v112,s[24:27],s64 offen offset:1024 sc1 lds\n"
"v_mfma_f32_16x16x32_bf16 v[168:171],a[104:107],v[84:87],v[168:171]\n"
"v_cvt_pk_bf16_f32 v74,v20,v21\n"
"v_max3_f32 v118,v56,v57,v58\n"
"v_max3_f32 v118,v118,v59,v60\n"
"v_max3_f32 v118,v118,v61,v62\n"
"v_exp_f32 v23,v23\n"
"v_mfma_f32_16x16x32_bf16 v[172:175],a[108:111],v[84:87],v[172:175]\n"
"buffer_load_dwordx4 v112,s[24:27],s64 offen offset:2048 sc1 lds\n"
"v_mfma_f32_16x16x32_bf16 v[176:179],a[112:115],v[84:87],v[176:179]\n"
"v_cvt_pk_bf16_f32 v75,v22,v23\n"
"v_max_f32 v118,v118,v63\n"
"v_cmp_lt_f32 vcc,s93,v118\n"
"v_exp_f32 v24,v24\n"
"v_mfma_f32_16x16x32_bf16 v[180:183],a[116:119],v[84:87],v[180:183]\n"
"ds_read_b128 a[48:51],v116 offset:57344\n"
"ds_read_b128 a[52:55],v116 offset:58368\n"
"ds_read_b128 a[56:59],v116 offset:59392\n"
"v_mfma_f32_16x16x32_bf16 v[184:187],a[120:123],v[84:87],v[184:187]\n"
"buffer_load_dwordx4 v112,s[24:27],s64 offen offset:3072 sc1 lds\n"
"ds_read_b128 a[60:63],v116 offset:60416\n"
"v_mfma_f32_16x16x32_bf16 v[188:191],a[124:127],v[84:87],v[188:191]\n"
"s_add_u32 m0,s67,36864\n"
"v_exp_f32 v25,v25\n"
"s_waitcnt vmcnt(16)\n"
"ds_add_u32 v124,a152\n"
"v_mfma_f32_16x16x32_bf16 v[192:195],a[96:99],v[88:91],v[192:195]\n"
"v_accvgpr_read_b32 v122,a128\n"
"v_cvt_pk_bf16_f32 v76,v24,v25\n"
"v_rcp_f32 v122,v122\n"
"s_nop 0\n"
"v_pk_mul_f32 v[128:129],v[128:129],v[122:123] op_sel_hi:[1,0]\n"
"v_pk_mul_f32 v[130:131],v[130:131],v[122:123] op_sel_hi:[1,0]\n"
"v_mfma_f32_16x16x32_bf16 a[136:139],a[144:147],v[88:91],a[136:139]\n"
"buffer_load_dwordx4 v117,s[24:27],s64 offen offset:0 sc1 lds\n"
"v_mfma_f32_16x16x32_bf16 v[196:199],a[100:103],v[88:91],v[196:199]\n"
"v_exp_f32 v26,v26\n"
"v_exp_f32 v27,v27\n"
"v_cvt_pk_bf16_f32 v128,v128,v129\n"
"v_pk_mul_f32 v[132:133],v[132:133],v[122:123] op_sel_hi:[1,0]\n"
"v_pk_mul_f32 v[134:135],v[134:135],v[122:123] op_sel_hi:[1,0]\n"
"v_mfma_f32_16x16x32_bf16 v[200:203],a[104:107],v[88:91],v[200:203]\n"
"v_cvt_pk_bf16_f32 v129,v130,v131\n"
"v_cvt_pk_bf16_f32 v130,v132,v133\n"
"v_mfma_f32_16x16x32_bf16 v[204:207],a[108:111],v[88:91],v[204:207]\n"
"buffer_load_dwordx4 v117,s[24:27],s64 offen offset:1024 sc1 lds\n"
"v_cvt_pk_bf16_f32 v131,v134,v135\n"
"v_mfma_f32_16x16x32_bf16 v[208:211],a[112:115],v[88:91],v[208:211]\n"
"buffer_store_dwordx4 v[128:131],v112,s[24:27],s70 offen offset:0 sc1\n"
"v_pk_mul_f32 v[136:137],v[136:137],v[122:123] op_sel_hi:[1,0]\n"
"v_pk_mul_f32 v[138:139],v[138:139],v[122:123] op_sel_hi:[1,0]\n"
"v_mfma_f32_16x16x32_bf16 v[212:215],a[116:119],v[88:91],v[212:215]\n"
"v_exp_f32 v28,v28\n"
"v_cvt_pk_bf16_f32 v77,v26,v27\n"
"v_exp_f32 v29,v29\n"
"v_pk_mul_f32 v[140:141],v[140:141],v[122:123] op_sel_hi:[1,0]\n"
"v_pk_mul_f32 v[142:143],v[142:143],v[122:123] op_sel_hi:[1,0]\n"
"v_mfma_f32_16x16x32_bf16 v[216:219],a[120:123],v[88:91],v[216:219]\n"
"buffer_load_dwordx4 v117,s[24:27],s64 offen offset:2048 sc1 lds\n"
"v_cvt_pk_bf16_f32 v136,v136,v137\n"
"v_mfma_f32_16x16x32_bf16 v[220:223],a[124:127],v[88:91],v[220:223]\n"
"v_cvt_pk_bf16_f32 v137,v138,v139\n"
"v_cvt_pk_bf16_f32 v138,v140,v141\n"
"v_cvt_pk_bf16_f32 v139,v142,v143\n"
"v_mfma_f32_16x16x32_bf16 v[224:227],a[96:99],v[92:95],v[224:227]\n"
"ds_read_b32 v125,v124\n"
"buffer_store_dwordx4 v[136:139],v112,s[24:27],s70 offen offset:1024 sc1\n"
"v_pk_mul_f32 v[144:145],v[144:145],v[122:123] op_sel_hi:[1,0]\n"
"v_exp_f32 v31,v31\n"
"v_mfma_f32_16x16x32_bf16 a[140:143],a[144:147],v[92:95],a[140:143]\n"
"buffer_load_dwordx4 v117,s[24:27],s64 offen offset:3072 sc1 lds\n"
"v_mfma_f32_16x16x32_bf16 v[228:231],a[100:103],v[92:95],v[228:231]\n"
"v_exp_f32 v30,v30\n"
"v_cvt_pk_bf16_f32 v78,v28,v29\n"
"s_add_u32 s64,s64,16384\n"
"v_cvt_pk_bf16_f32 v79,v30,v31\n"
"v_pk_mul_f32 v[146:147],v[146:147],v[122:123] op_sel_hi:[1,0]\n"
"v_pk_mul_f32 v[148:149],v[148:149],v[122:123] op_sel_hi:[1,0]\n"
"v_mfma_f32_16x16x32_bf16 v[232:235],a[104:107],v[92:95],v[232:235]\n"
"buffer_store_dwordx4 a[252:255],v112,s[24:27],s56 offen offset:3072 sc1\n"
"v_pk_mul_f32 v[150:151],v[150:151],v[122:123] op_sel_hi:[1,0]\n"
"v_cvt_pk_bf16_f32 v144,v144,v145\n"
"v_mfma_f32_16x16x32_bf16 v[236:239],a[108:111],v[92:95],v[236:239]\n"
"v_cvt_pk_bf16_f32 v145,v146,v147\n"
"v_cvt_pk_bf16_f32 v146,v148,v149\n"
"v_cvt_pk_bf16_f32 v147,v150,v151\n"
"v_mfma_f32_16x16x32_bf16 v[240:243],a[112:115],v[92:95],v[240:243]\n"
"buffer_store_dwordx4 v[144:147],v112,s[24:27],s70 offen offset:2048 sc1\n"
"v_pk_mul_f32 v[152:153],v[152:153],v[122:123] op_sel_hi:[1,0]\n"
"s_waitcnt lgkmcnt(0)\n"
"v_pk_mul_f32 v[154:155],v[154:155],v[122:123] op_sel_hi:[1,0]\n"
"v_pk_mul_f32 v[156:157],v[156:157],v[122:123] op_sel_hi:[1,0]\n"
"v_pk_mul_f32 v[158:159],v[158:159],v[122:123] op_sel_hi:[1,0]\n"
"v_cvt_pk_bf16_f32 v152,v152,v153\n"
"v_mfma_f32_16x16x32_bf16 v[244:247],a[116:119],v[92:95],v[244:247]\n"
"v_cvt_pk_bf16_f32 v153,v154,v155\n"
"v_cvt_pk_bf16_f32 v154,v156,v157\n"
"v_cvt_pk_bf16_f32 v155,v158,v159\n"
"v_mfma_f32_16x16x32_bf16 v[248:251],a[120:123],v[92:95],v[248:251]\n"
"buffer_store_dwordx4 v[152:155],v112,s[24:27],s70 offen offset:3072 sc1\n"
"v_accvgpr_read_b32 v122,a132\n"
"v_rcp_f32 v122,v122\n"
"s_add_u32 s70,s70,262144\n"
"v_pk_mul_f32 v[160:161],v[160:161],v[122:123] op_sel_hi:[1,0]\n"
"v_mfma_f32_16x16x32_bf16 v[252:255],a[124:127],v[92:95],v[252:255]\n"
"v_cvt_pk_bf16_f32 v160,v160,v161\n"
"v_pk_mul_f32 v[162:163],v[162:163],v[122:123] op_sel_hi:[1,0]\n"
"v_pk_mul_f32 v[164:165],v[164:165],v[122:123] op_sel_hi:[1,0]\n"
"v_pk_mul_f32 v[166:167],v[166:167],v[122:123] op_sel_hi:[1,0]\n"
"v_cvt_pk_bf16_f32 v161,v162,v163\n"
"v_cvt_pk_bf16_f32 v162,v164,v165\n"
"v_cvt_pk_bf16_f32 v163,v166,v167\n"
"v_pk_mul_f32 v[168:169],v[168:169],v[122:123] op_sel_hi:[1,0]\n"
"v_pk_mul_f32 v[170:171],v[170:171],v[122:123] op_sel_hi:[1,0]\n"
"v_pk_mul_f32 v[172:173],v[172:173],v[122:123] op_sel_hi:[1,0]\n"
"v_pk_mul_f32 v[174:175],v[174:175],v[122:123] op_sel_hi:[1,0]\n"
"v_cvt_pk_bf16_f32 v168,v168,v169\n"
"v_cvt_pk_bf16_f32 v169,v170,v171\n"
"v_cvt_pk_bf16_f32 v170,v172,v173\n"
"v_cvt_pk_bf16_f32 v171,v174,v175\n"
"buffer_store_dwordx4 v[160:163],v112,s[24:27],s70 offen offset:0 sc1\n"
"buffer_store_dwordx4 v[168:171],v112,s[24:27],s70 offen offset:1024 sc1\n"
"v_pk_mul_f32 v[176:177],v[176:177],v[122:123] op_sel_hi:[1,0]\n"
"v_pk_mul_f32 v[178:179],v[178:179],v[122:123] op_sel_hi:[1,0]\n"
"v_pk_mul_f32 v[180:181],v[180:181],v[122:123] op_sel_hi:[1,0]\n"
"v_pk_mul_f32 v[182:183],v[182:183],v[122:123] op_sel_hi:[1,0]\n"
"v_cvt_pk_bf16_f32 v176,v176,v177\n"
"v_cvt_pk_bf16_f32 v177,v178,v179\n"
"v_cvt_pk_bf16_f32 v178,v180,v181\n"
"v_cvt_pk_bf16_f32 v179,v182,v183\n"
"buffer_store_dwordx4 v[176:179],v112,s[24:27],s70 offen offset:2048 sc1\n"
"v_pk_mul_f32 v[184:185],v[184:185],v[122:123] op_sel_hi:[1,0]\n"
"v_pk_mul_f32 v[186:187],v[186:187],v[122:123] op_sel_hi:[1,0]\n"
"v_pk_mul_f32 v[188:189],v[188:189],v[122:123] op_sel_hi:[1,0]\n"
"v_pk_mul_f32 v[190:191],v[190:191],v[122:123] op_sel_hi:[1,0]\n"
"v_cvt_pk_bf16_f32 v184,v184,v185\n"
"v_cvt_pk_bf16_f32 v185,v186,v187\n"
"v_cvt_pk_bf16_f32 v186,v188,v189\n"
"v_cvt_pk_bf16_f32 v187,v190,v191\n"
"buffer_store_dwordx4 v[184:187],v112,s[24:27],s70 offen offset:3072 sc1\n"
"s_add_u32 s70,s70,262144\n"
"s_cbranch_vccnz .Lfk_cs_u2\n"
".Lfk_cr_u2:\n"
"v_cmp_gt_u32 vcc,s101,v125\n"
"s_nop 0\n"
"s_cbranch_vccnz .Lfk_cs_mu2\n"
".Lfk_cr_mu2:\n"
"s_add_u32 s89,s89,1\n"
"s_cmp_eq_u32 s89,8\n"
"s_cbranch_scc1 .Lfk_tailB7\n"
".Lfk_winB:\n"
"v_mfma_f32_16x16x32_bf16 v[0:3],a[64:67],a[0:3],v[96:99]\n"
"s_swappc_b64 s[94:95],s[20:21]\n"
"ds_read_b128 a[96:99],v114 offset:24576\n"
"v_mfma_f32_16x16x32_bf16 v[0:3],a[68:71],a[4:7],v[0:3]\n"
"s_add_u32 s101,s101,4\n"
"ds_read_b128 a[100:103],v114 offset:25600\n"
"ds_read_b128 a[104:107],v114 offset:26624\n"
"ds_read_b128 a[108:111],v114 offset:27648\n"
"v_mfma_f32_16x16x32_bf16 v[0:3],a[72:75],a[8:11],v[0:3]\n"
"ds_read_b128 a[112:115],v114 offset:28672\n"
"ds_read_b128 a[116:119],v114 offset:29696\n"
"v_mfma_f32_16x16x32_bf16 v[0:3],a[76:79],a[12:15],v[0:3]\n"
"ds_read_b128 a[120:123],v114 offset:30720\n"
"ds_read_b128 a[124:127],v114 offset:31744\n"
"v_mfma_f32_16x16x32_bf16 v[4:7],a[92:95],a[12:15],v[96:99]\n"
"v_exp_f32 v56,v56\n"
"v_mfma_f32_16x16x32_bf16 v[4:7],a[88:91],a[8:11],v[4:7]\n"
"v_exp_f32 v57,v57\n"
"v_mfma_f32_16x16x32_bf16 v[4:7],a[84:87],a[4:7],v[4:7]\n"
"v_cvt_pk_bf16_f32 v92,v56,v57\n"
"v_mfma_f32_16x16x32_bf16 v[4:7],a[80:83],a[0:3],v[4:7]\n"
"s_add_u32 m0,s67,0\n"
"s_waitcnt lgkmcnt(0)\n"
"v_mfma_f32_16x16x32_bf16 v[192:195],a[96:99],v[72:75],v[192:195]\n"
"buffer_load_dwordx4 v112,s[24:27],s64 offen offset:0 sc1 lds\n"
"v_mfma_f32_16x16x32_bf16 a[136:139],a[144:147],v[72:75],a[136:139]\n"
"buffer_load_dwordx4 v112,s[24:27],s64 offen offset:1024 sc1 lds\n"
"v_mfma_f32_16x16x32_bf16 v[196:199],a[100:103],v[72:75],v[196:199]\n"
"v_exp_f32 v58,v58\n"
"v_mfma_f32_16x16x32_bf16 v[200:203],a[104:107],v[72:75],v[200:203]\n"
"v_max3_f32 v118,v0,v1,v2\n"
"v_mfma_f32_16x16x32_bf16 v[204:207],a[108:111],v[72:75],v[204:207]\n"
"v_max3_f32 v118,v118,v3,v4\n"
"v_max3_f32 v118,v118,v5,v6\n"
"v_mfma_f32_16x16x32_bf16 v[208:211],a[112:115],v[72:75],v[208:211]\n"
"v_max_f32 v118,v118,v7\n"
"v_mov_b32 v120,v118\n"
"v_mfma_f32_16x16x32_bf16 v[212:215],a[116:119],v[72:75],v[212:215]\n"
"v_mfma_f32_16x16x32_bf16 v[216:219],a[120:123],v[72:75],v[216:219]\n"
"v_mfma_f32_16x16x32_bf16 v[220:223],a[124:127],v[72:75],v[220:223]\n"
"v_exp_f32 v59,v59\n"
"v_mfma_f32_16x16x32_bf16 v[224:227],a[96:99],v[76:79],v[224:227]\n"
"v_cvt_pk_bf16_f32 v93,v58,v59\n"
"v_accvgpr_read_b32 v122,a136\n"
"v_mfma_f32_16x16x32_bf16 a[140:143],a[144:147],v[76:79],a[140:143]\n"
"buffer_load_dwordx4 v112,s[24:27],s64 offen offset:2048 sc1 lds\n"
"v_mfma_f32_16x16x32_bf16 v[228:231],a[100:103],v[76:79],v[228:231]\n"
"v_rcp_f32 v122,v122\n"
"v_mfma_f32_16x16x32_bf16 v[232:235],a[104:107],v[76:79],v[232:235]\n"
"buffer_load_dwordx4 v112,s[24:27],s64 offen offset:3072 sc1 lds\n"
"v_pk_mul_f32 v[192:193],v[192:193],v[122:123] op_sel_hi:[1,0]\n"
"v_pk_mul_f32 v[194:195],v[194:195],v[122:123] op_sel_hi:[1,0]\n"
"v_mfma_f32_16x16x32_bf16 v[236:239],a[108:111],v[76:79],v[236:239]\n"
"v_cvt_pk_bf16_f32 v192,v192,v193\n"
"v_exp_f32 v60,v60\n"
"v_cvt_pk_bf16_f32 v193,v194,v195\n"
"v_pk_mul_f32 v[196:197],v[196:197],v[122:123] op_sel_hi:[1,0]\n"
"v_pk_mul_f32 v[198:199],v[198:199],v[122:123] op_sel_hi:[1,0]\n"
"v_mfma_f32_16x16x32_bf16 v[240:243],a[112:115],v[76:79],v[240:243]\n"
"s_add_u32 m0,s67,4096\n"
"v_cvt_pk_bf16_f32 v194,v196,v197\n"
"v_cvt_pk_bf16_f32 v195,v198,v199\n"
"v_mfma_f32_16x16x32_bf16 v[244:247],a[116:119],v[76:79],v[244:247]\n"
"v_mfma_f32_16x16x32_bf16 v[248:251],a[120:123],v[76:79],v[248:251]\n"
"buffer_store_dwordx4 v[192:195],v112,s[24:27],s70 offen offset:0 sc1\n"
"v_pk_mul_f32 v[200:201],v[200:201],v[122:123] op_sel_hi:[1,0]\n"
"v_mfma_f32_16x16x32_bf16 v[252:255],a[124:127],v[76:79],v[252:255]\n"
"buffer_load_dwordx4 v117,s[24:27],s64 offen offset:0 sc1 lds\n"
"v_cvt_pk_bf16_f32 v200,v200,v201\n"
"v_exp_f32 v61,v61\n"
"v_pk_mul_f32 v[202:203],v[202:203],v[122:123] op_sel_hi:[1,0]\n"
"v_pk_mul_f32 v[204:205],v[204:205],v[122:123] op_sel_hi:[1,0]\n"
"v_pk_mul_f32 v[206:207],v[206:207],v[122:123] op_sel_hi:[1,0]\n"
"v_mfma_f32_16x16x32_bf16 v[12:15],a[80:83],a[16:19],v[100:103]\n"
"v_cvt_pk_bf16_f32 v201,v202,v203\n"
"v_cvt_pk_bf16_f32 v202,v204,v205\n"
"v_mfma_f32_16x16x32_bf16 v[12:15],a[84:87],a[20:23],v[12:15]\n"
"v_cvt_pk_bf16_f32 v203,v206,v207\n"
"v_cvt_pk_bf16_f32 v94,v60,v61\n"
"v_mfma_f32_16x16x32_bf16 v[12:15],a[88:91],a[24:27],v[12:15]\n"
"buffer_load_dwordx4 v117,s[24:27],s64 offen offset:1024 sc1 lds\n"
"v_mfma_f32_16x16x32_bf16 v[12:15],a[92:95],a[28:31],v[12:15]\n"
"buffer_store_dwordx4 v[200:203],v112,s[24:27],s70 offen offset:1024 sc1\n"
"v_pk_mul_f32 v[208:209],v[208:209],v[122:123] op_sel_hi:[1,0]\n"
"v_pk_mul_f32 v[210:211],v[210:211],v[122:123] op_sel_hi:[1,0]\n"
"v_mfma_f32_16x16x32_bf16 v[8:11],a[76:79],a[28:31],v[100:103]\n"
"buffer_load_dwordx4 v117,s[24:27],s64 offen offset:2048 sc1 lds\n"
"v_cvt_pk_bf16_f32 v208,v208,v209\n"
"v_cvt_pk_bf16_f32 v209,v210,v211\n"
"v_pk_mul_f32 v[212:213],v[212:213],v[122:123] op_sel_hi:[1,0]\n"
"v_pk_mul_f32 v[214:215],v[214:215],v[122:123] op_sel_hi:[1,0]\n"
"v_mfma_f32_16x16x32_bf16 v[8:11],a[72:75],a[24:27],v[8:11]\n"
"v_cvt_pk_bf16_f32 v210,v212,v213\n"
"v_cvt_pk_bf16_f32 v211,v214,v215\n"
"v_mfma_f32_16x16x32_bf16 v[8:11],a[68:71],a[20:23],v[8:11]\n"
"v_exp_f32 v62,v62\n"
"v_mfma_f32_16x16x32_bf16 v[8:11],a[64:67],a[16:19],v[8:11]\n"
"buffer_store_dwordx4 v[208:211],v112,s[24:27],s70 offen offset:2048 sc1\n"
"v_pk_mul_f32 v[216:217],v[216:217],v[122:123] op_sel_hi:[1,0]\n"
"v_mfma_f32_16x16x32_bf16 v[16:19],a[64:67],a[32:35],v[104:107]\n"
"v_cvt_pk_bf16_f32 v216,v216,v217\n"
"buffer_load_dwordx4 v117,s[24:27],s64 offen offset:3072 sc1 lds\n"
"v_pk_mul_f32 v[218:219],v[218:219],v[122:123] op_sel_hi:[1,0]\n"
"v_pk_mul_f32 v[220:221],v[220:221],v[122:123] op_sel_hi:[1,0]\n"
"v_pk_mul_f32 v[222:223],v[222:223],v[122:123] op_sel_hi:[1,0]\n"
"v_mfma_f32_16x16x32_bf16 v[16:19],a[68:71],a[36:39],v[16:19]\n"
"v_cvt_pk_bf16_f32 v217,v218,v219\n"
"v_cvt_pk_bf16_f32 v218,v220,v221\n"
"v_mfma_f32_16x16x32_bf16 v[16:19],a[72:75],a[40:43],v[16:19]\n"
"s_add_u32 s64,s64,16384\n"
"v_exp_f32 v63,v63\n"
"v_mfma_f32_16x16x32_bf16 v[16:19],a[76:79],a[44:47],v[16:19]\n"
"s_andn2_b32 m0,s55,16384\n"
"s_add_u32 s54,s54,s22\n"
"v_max3_f32 v119,v8,v9,v10\n"
"v_mfma_f32_16x16x32_bf16 v[20:23],a[92:95],a[44:47],v[104:107]\n"
"v_max3_f32 v119,v119,v11,v12\n"
"v_cvt_pk_bf16_f32 v95,v62,v63\n"
"v_mfma_f32_16x16x32_bf16 v[20:23],a[88:91],a[40:43],v[20:23]\n"
"buffer_load_dwordx4 v115,s[32:35],s54 offen sc1 lds\n"
"v_mfma_f32_16x16x32_bf16 v[20:23],a[84:87],a[36:39],v[20:23]\n"
"v_max3_f32 v119,v119,v13,v14\n"
"v_max_f32 v119,v119,v15\n"
"v_mfma_f32_16x16x32_bf16 v[20:23],a[80:83],a[32:35],v[20:23]\n"
"v_cvt_pk_bf16_f32 v219,v222,v223\n"
"v_mfma_f32_16x16x32_bf16 v[28:31],a[80:83],a[48:51],v[108:111]\n"
"buffer_store_dwordx4 v[216:219],v112,s[24:27],s70 offen offset:3072 sc1\n"
"buffer_store_dwordx4 a[240:243],v112,s[24:27],s56 offen offset:0 sc1\n"
"v_mfma_f32_16x16x32_bf16 v[28:31],a[84:87],a[52:55],v[28:31]\n"
"s_add_u32 s70,s70,262144\n"
"v_max_f32 v118,v118,v119\n"
"v_min_f32 v120,v120,v119\n"
"v_mfma_f32_16x16x32_bf16 v[28:31],a[88:91],a[56:59],v[28:31]\n"
"v_max3_f32 v119,v16,v17,v18\n"
"v_mfma_f32_16x16x32_bf16 v[28:31],a[92:95],a[60:63],v[28:31]\n"
"v_max3_f32 v119,v119,v19,v20\n"
"v_max3_f32 v119,v119,v21,v22\n"
"v_mfma_f32_16x16x32_bf16 v[24:27],a[76:79],a[60:63],v[108:111]\n"
"v_max_f32 v119,v119,v23\n"
"v_max_f32 v118,v118,v119\n"
"v_mfma_f32_16x16x32_bf16 v[24:27],a[72:75],a[56:59],v[24:27]\n"
"v_min_f32 v120,v120,v119\n"
"v_mfma_f32_16x16x32_bf16 v[24:27],a[68:71],a[52:55],v[24:27]\n"
"v_mfma_f32_16x16x32_bf16 v[24:27],a[64:67],a[48:51],v[24:27]\n"
"ds_read_b128 a[64:67],v113 offset:16384\n"
"ds_read_b128 a[68:71],v113 offset:17408\n"
"ds_read_b128 a[72:75],v113 offset:18432\n"
"ds_read_b128 a[76:79],v113 offset:19456\n"
"ds_read_b128 a[80:83],v113 offset:20480\n"
"ds_read_b128 a[84:87],v113 offset:21504\n"
"ds_read_b128 a[88:91],v113 offset:22528\n"
"ds_read_b128 a[92:95],v113 offset:23552\n"
"v_max3_f32 v119,v24,v25,v26\n"
"v_max3_f32 v119,v119,v27,v28\n"
"v_max3_f32 v119,v119,v29,v30\n"
"v_max_f32 v119,v119,v31\n"
"v_min_f32 v120,v120,v119\n"
"v_max_f32 v118,v118,v119\n"
"v_cmp_gt_f32 s[96:97],s16,v120\n"
"v_cmp_lt_f32 vcc,s93,v118\n"
"s_waitcnt vmcnt(25)\n"
"ds_add_u32 v124,a152\n"
"ds_read_b32 v125,v124\n"
"s_waitcnt lgkmcnt(0)\n"
"s_or_b64 s[96:97],s[96:97],vcc\n"
"s_cbranch_scc1 .Lfk_cs_v3\n"
".Lfk_cr_v3:\n"
"v_cmp_gt_u32 vcc,s101,v125\n"
"s_nop 0\n"
"s_cbranch_vccnz .Lfk_cs_mv3\n"
".Lfk_cr_mv3:\n"
"v_exp_f32 v0,v0\n"
"v_mfma_f32_16x16x32_bf16 v[32:35],a[64:67],a[0:3],v[96:99]\n"
"s_add_u32 s101,s101,4\n"
"ds_read_b128 a[96:99],v114 offset:57344\n"
"ds_read_b128 a[100:103],v114 offset:58368\n"
"v_exp_f32 v1,v1\n"
"v_mfma_f32_16x16x32_bf16 v[32:35],a[68:71],a[4:7],v[32:35]\n"
"s_add_u32 m0,s55,4096\n"
"ds_read_b128 a[104:107],v114 offset:59392\n"
"ds_read_b128 a[108:111],v114 offset:60416\n"
"v_exp_f32 v2,v2\n"
"v_mfma_f32_16x16x32_bf16 v[32:35],a[72:75],a[8:11],v[32:35]\n"
"s_add_u32 s54,s54,s22\n"
"ds_read_b128 a[112:115],v114 offset:61440\n"
"ds_read_b128 a[116:119],v114 offset:62464\n"
"v_exp_f32 v3,v3\n"
"v_mfma_f32_16x16x32_bf16 v[32:35],a[76:79],a[12:15],v[32:35]\n"
"ds_read_b128 a[120:123],v114 offset:63488\n"
"ds_read_b128 a[124:127],v114 offset:64512\n"
"v_exp_f32 v4,v4\n"
"v_mfma_f32_16x16x32_bf16 v[36:39],a[92:95],a[12:15],v[96:99]\n"
"buffer_load_dwordx4 v115,s[32:35],s54 offen sc1 lds\n"
"v_mfma_f32_16x16x32_bf16 v[36:39],a[88:91],a[8:11],v[36:39]\n"
"s_add_u32 m0,s67,16384\n"
"v_exp_f32 v5,v5\n"
"v_cvt_pk_bf16_f32 v64,v0,v1\n"
"v_cvt_pk_bf16_f32 v65,v2,v3\n"
"v_mfma_f32_16x16x32_bf16 v[36:39],a[84:87],a[4:7],v[36:39]\n"
"v_exp_f32 v6,v6\n"
"v_exp_f32 v7,v7\n"
"v_mfma_f32_16x16x32_bf16 v[36:39],a[80:83],a[0:3],v[36:39]\n"
"buffer_load_dwordx4 v112,s[24:27],s64 offen offset:0 sc1 lds\n"
"s_waitcnt lgkmcnt(0)\n"
"v_mfma_f32_16x16x32_bf16 v[224:227],a[96:99],v[92:95],v[224:227]\n"
"v_exp_f32 v8,v8\n"
"v_cvt_pk_bf16_f32 v67,v6,v7\n"
"v_mfma_f32_16x16x32_bf16 a[140:143],a[144:147],v[92:95],a[140:143]\n"
"v_exp_f32 v9,v9\n"
"v_exp_f32 v10,v10\n"
"v_mfma_f32_16x16x32_bf16 v[228:231],a[100:103],v[92:95],v[228:231]\n"
"buffer_load_dwordx4 v112,s[24:27],s64 offen offset:1024 sc1 lds\n"
"v_mfma_f32_16x16x32_bf16 v[232:235],a[104:107],v[92:95],v[232:235]\n"
"v_cvt_pk_bf16_f32 v66,v4,v5\n"
"v_cvt_pk_bf16_f32 v68,v8,v9\n"
"v_mfma_f32_16x16x32_bf16 v[236:239],a[108:111],v[92:95],v[236:239]\n"
"v_exp_f32 v11,v11\n"
"v_exp_f32 v12,v12\n"
"v_mfma_f32_16x16x32_bf16 v[240:243],a[112:115],v[92:95],v[240:243]\n"
"buffer_load_dwordx4 v112,s[24:27],s64 offen offset:2048 sc1 lds\n"
"v_mfma_f32_16x16x32_bf16 v[244:247],a[116:119],v[92:95],v[244:247]\n"
"v_exp_f32 v13,v13\n"
"v_cvt_pk_bf16_f32 v69,v10,v11\n"
"v_mfma_f32_16x16x32_bf16 v[248:251],a[120:123],v[92:95],v[248:251]\n"
"v_exp_f32 v14,v14\n"
"v_cvt_pk_bf16_f32 v70,v12,v13\n"
"v_exp_f32 v15,v15\n"
"v_mfma_f32_16x16x32_bf16 v[252:255],a[124:127],v[92:95],v[252:255]\n"
"buffer_load_dwordx4 v112,s[24:27],s64 offen offset:3072 sc1 lds\n"
"v_mfma_f32_16x16x32_bf16 v[44:47],a[80:83],a[16:19],v[100:103]\n"
"s_add_u32 m0,s67,20480\n"
"v_exp_f32 v16,v16\n"
"v_cvt_pk_bf16_f32 v71,v14,v15\n"
"v_mfma_f32_16x16x32_bf16 v[44:47],a[84:87],a[20:23],v[44:47]\n"
"v_exp_f32 v17,v17\n"
"v_exp_f32 v18,v18\n"
"v_mfma_f32_16x16x32_bf16 v[44:47],a[88:91],a[24:27],v[44:47]\n"
"buffer_load_dwordx4 v117,s[24:27],s64 offen offset:0 sc1 lds\n"
"v_mfma_f32_16x16x32_bf16 v[44:47],a[92:95],a[28:31],v[44:47]\n"
"v_exp_f32 v19,v19\n"
"v_accvgpr_read_b32 v122,a140\n"
"v_mfma_f32_16x16x32_bf16 v[40:43],a[76:79],a[28:31],v[100:103]\n"
"v_cvt_pk_bf16_f32 v73,v18,v19\n"
"v_rcp_f32 v122,v122\n"
"v_mfma_f32_16x16x32_bf16 v[40:43],a[72:75],a[24:27],v[40:43]\n"
"buffer_load_dwordx4 v117,s[24:27],s64 offen offset:1024 sc1 lds\n"
"v_mfma_f32_16x16x32_bf16 v[40:43],a[68:71],a[20:23],v[40:43]\n"
"v_exp_f32 v21,v21\n"
"v_exp_f32 v20,v20\n"
"v_cvt_pk_bf16_f32 v72,v16,v17\n"
"v_pk_mul_f32 v[224:225],v[224:225],v[122:123] op_sel_hi:[1,0]\n"
"v_pk_mul_f32 v[226:227],v[226:227],v[122:123] op_sel_hi:[1,0]\n"
"v_pk_mul_f32 v[228:229],v[228:229],v[122:123] op_sel_hi:[1,0]\n"
"v_pk_mul_f32 v[230:231],v[230:231],v[122:123] op_sel_hi:[1,0]\n"
"v_mfma_f32_16x16x32_bf16 v[40:43],a[64:67],a[16:19],v[40:43]\n"
"v_cvt_pk_bf16_f32 v74,v20,v21\n"
"v_exp_f32 v22,v22\n"
"v_cvt_pk_bf16_f32 v224,v224,v225\n"
"v_exp_f32 v23,v23\n"
"v_mfma_f32_16x16x32_bf16 v[48:51],a[64:67],a[32:35],v[104:107]\n"
"buffer_load_dwordx4 v117,s[24:27],s64 offen offset:2048 sc1 lds\n"
"v_mfma_f32_16x16x32_bf16 v[48:51],a[68:71],a[36:39],v[48:51]\n"
"v_exp_f32 v24,v24\n"
"v_cvt_pk_bf16_f32 v225,v226,v227\n"
"v_mfma_f32_16x16x32_bf16 v[48:51],a[72:75],a[40:43],v[48:51]\n"
"v_exp_f32 v25,v25\n"
"v_cvt_pk_bf16_f32 v226,v228,v229\n"
"v_cvt_pk_bf16_f32 v227,v230,v231\n"
"v_mfma_f32_16x16x32_bf16 v[48:51],a[76:79],a[44:47],v[48:51]\n"
"buffer_load_dwordx4 v117,s[24:27],s64 offen offset:3072 sc1 lds\n"
"v_mfma_f32_16x16x32_bf16 v[52:55],a[92:95],a[44:47],v[104:107]\n"
"buffer_store_dwordx4 v[224:227],v112,s[24:27],s70 offen offset:0 sc1\n"
"v_exp_f32 v27,v27\n"
"v_mfma_f32_16x16x32_bf16 v[52:55],a[88:91],a[40:43],v[52:55]\n"
"s_add_u32 s64,s64,16384\n"
"v_exp_f32 v26,v26\n"
"v_exp_f32 v28,v28\n"
"v_cvt_pk_bf16_f32 v77,v26,v27\n"
"v_pk_mul_f32 v[232:233],v[232:233],v[122:123] op_sel_hi:[1,0]\n"
"v_mfma_f32_16x16x32_bf16 v[52:55],a[84:87],a[36:39],v[52:55]\n"
"v_cvt_pk_bf16_f32 v75,v22,v23\n"
"v_cvt_pk_bf16_f32 v76,v24,v25\n"
"v_exp_f32 v29,v29\n"
"v_exp_f32 v31,v31\n"
"v_pk_mul_f32 v[234:235],v[234:235],v[122:123] op_sel_hi:[1,0]\n"
"v_pk_mul_f32 v[236:237],v[236:237],v[122:123] op_sel_hi:[1,0]\n"
"v_pk_mul_f32 v[238:239],v[238:239],v[122:123] op_sel_hi:[1,0]\n"
"v_mfma_f32_16x16x32_bf16 v[52:55],a[80:83],a[32:35],v[52:55]\n"
"v_exp_f32 v30,v30\n"
"v_cvt_pk_bf16_f32 v78,v28,v29\n"
"v_mfma_f32_16x16x32_bf16 v[60:63],a[80:83],a[48:51],v[108:111]\n"
"v_cvt_pk_bf16_f32 v232,v232,v233\n"
"v_cvt_pk_bf16_f32 v233,v234,v235\n"
"v_mfma_f32_16x16x32_bf16 v[60:63],a[84:87],a[52:55],v[60:63]\n"
"buffer_store_dwordx4 a[244:247],v112,s[24:27],s56 offen offset:1024 sc1\n"
"v_cvt_pk_bf16_f32 v234,v236,v237\n"
"v_cvt_pk_bf16_f32 v235,v238,v239\n"
"v_mfma_f32_16x16x32_bf16 v[60:63],a[88:91],a[56:59],v[60:63]\n"
"buffer_store_dwordx4 v[232:235],v112,s[24:27],s70 offen offset:1024 sc1\n"
"v_pk_mul_f32 v[240:241],v[240:241],v[122:123] op_sel_hi:[1,0]\n"
"v_pk_mul_f32 v[242:243],v[242:243],v[122:123] op_sel_hi:[1,0]\n"
"v_pk_mul_f32 v[244:245],v[244:245],v[122:123] op_sel_hi:[1,0]\n"
"v_pk_mul_f32 v[246:247],v[246:247],v[122:123] op_sel_hi:[1,0]\n"
"v_mfma_f32_16x16x32_bf16 v[60:63],a[92:95],a[60:63],v[60:63]\n"
"v_cvt_pk_bf16_f32 v240,v240,v241\n"
"v_cvt_pk_bf16_f32 v241,v242,v243\n"
"v_cvt_pk_bf16_f32 v242,v244,v245\n"
"v_cvt_pk_bf16_f32 v243,v246,v247\n"
"v_mfma_f32_16x16x32_bf16 v[56:59],a[76:79],a[60:63],v[108:111]\n"
"buffer_store_dwordx4 v[240:243],v112,s[24:27],s70 offen offset:2048 sc1\n"
"v_pk_mul_f32 v[248:249],v[248:249],v[122:123] op_sel_hi:[1,0]\n"
"v_pk_mul_f32 v[250:251],v[250:251],v[122:123] op_sel_hi:[1,0]\n"
"v_mfma_f32_16x16x32_bf16 v[56:59],a[72:75],a[56:59],v[56:59]\n"
"v_cvt_pk_bf16_f32 v79,v30,v31\n"
"v_cvt_pk_bf16_f32 v248,v248,v249\n"
"v_cvt_pk_bf16_f32 v249,v250,v251\n"
"v_pk_mul_f32 v[252:253],v[252:253],v[122:123] op_sel_hi:[1,0]\n"
"v_pk_mul_f32 v[254:255],v[254:255],v[122:123] op_sel_hi:[1,0]\n"
"v_mfma_f32_16x16x32_bf16 v[56:59],a[68:71],a[52:55],v[56:59]\n"
"v_cvt_pk_bf16_f32 v250,v252,v253\n"
"v_cvt_pk_bf16_f32 v251,v254,v255\n"
"v_mfma_f32_16x16x32_bf16 v[56:59],a[64:67],a[48:51],v[56:59]\n"
"buffer_store_dwordx4 v[248:251],v112,s[24:27],s70 offen offset:3072 sc1\n"
"s_add_u32 s70,s70,262144\n"
"ds_read_b128 a[64:67],v113 offset:49152\n"
"ds_read_b128 a[68:71],v113 offset:50176\n"
"ds_read_b128 a[72:75],v113 offset:51200\n"
"ds_read_b128 a[76:79],v113 offset:52224\n"
"ds_read_b128 a[80:83],v113 offset:53248\n"
"ds_read_b128 a[84:87],v113 offset:54272\n"
"ds_read_b128 a[88:91],v113 offset:55296\n"
"ds_read_b128 a[92:95],v113 offset:56320\n"
"v_mov_b32 v114,v112\n"
"s_mov_b32 s70,s68\n"
"s_waitcnt vmcnt(25)\n"
"ds_add_u32 v124,a152\n"
"ds_read_b32 v125,v124\n"
"s_waitcnt lgkmcnt(0)\n"
"v_cmp_gt_u32 vcc,s101,v125\n"
"s_nop 0\n"
"s_cbranch_vccnz .Lfk_cs_mv0\n"
".Lfk_cr_mv0:\n"
"v_mfma_f32_16x16x32_bf16 v[0:3],a[64:67],a[0:3],v[96:99]\n"
"s_add_u32 s101,s101,4\n"
"v_exp_f32 v122,v32\n"
"v_mfma_f32_16x16x32_bf16 v[0:3],a[68:71],a[4:7],v[0:3]\n"
"s_add_u32 m0,s55,8192\n"
"v_exp_f32 v123,v33\n"
"v_mfma_f32_16x16x32_bf16 v[0:3],a[72:75],a[8:11],v[0:3]\n"
"ds_read_b128 a[96:99],v114 offset:40960\n"
"ds_read_b128 a[100:103],v114 offset:41984\n"
"v_mfma_f32_16x16x32_bf16 v[0:3],a[76:79],a[12:15],v[0:3]\n"
"s_add_u32 s54,s54,s22\n"
"v_exp_f32 v119,v34\n"
"v_mfma_f32_16x16x32_bf16 v[4:7],a[92:95],a[12:15],v[96:99]\n"
"ds_read_b128 a[104:107],v114 offset:43008\n"
"v_cvt_pk_bf16_f32 v80,v122,v123\n"
"v_mfma_f32_16x16x32_bf16 v[4:7],a[88:91],a[8:11],v[4:7]\n"
"v_exp_f32 v120,v35\n"
"v_mfma_f32_16x16x32_bf16 v[4:7],a[84:87],a[4:7],v[4:7]\n"
"ds_read_b128 a[108:111],v114 offset:44032\n"
"ds_read_b128 a[112:115],v114 offset:45056\n"
"v_mfma_f32_16x16x32_bf16 v[4:7],a[80:83],a[0:3],v[4:7]\n"
"v_exp_f32 v122,v36\n"
"v_mfma_f32_16x16x32_bf16 v[12:15],a[80:83],a[16:19],v[100:103]\n"
"ds_read_b128 a[116:119],v114 offset:46080\n"
"v_cvt_pk_bf16_f32 v81,v119,v120\n"
"v_mfma_f32_16x16x32_bf16 v[12:15],a[84:87],a[20:23],v[12:15]\n"
"v_exp_f32 v123,v37\n"
"v_mfma_f32_16x16x32_bf16 v[12:15],a[88:91],a[24:27],v[12:15]\n"
"ds_read_b128 a[120:123],v114 offset:47104\n"
"ds_read_b128 a[124:127],v114 offset:48128\n"
"v_mfma_f32_16x16x32_bf16 v[12:15],a[92:95],a[28:31],v[12:15]\n"
"v_exp_f32 v119,v38\n"
"v_mfma_f32_16x16x32_bf16 v[8:11],a[76:79],a[28:31],v[100:103]\n"
"v_cvt_pk_bf16_f32 v82,v122,v123\n"
"v_or3_b32 v118,v80,v81,v82\n"
"v_mfma_f32_16x16x32_bf16 v[8:11],a[72:75],a[24:27],v[8:11]\n"
"v_exp_f32 v120,v39\n"
"v_mfma_f32_16x16x32_bf16 v[8:11],a[68:71],a[20:23],v[8:11]\n"
"buffer_load_dwordx4 v115,s[32:35],s54 offen sc1 lds\n"
"v_mfma_f32_16x16x32_bf16 v[8:11],a[64:67],a[16:19],v[8:11]\n"
"s_add_u32 m0,s67,49152\n"
"v_exp_f32 v122,v40\n"
"v_mfma_f32_16x16x32_bf16 v[16:19],a[64:67],a[32:35],v[104:107]\n"
"v_cvt_pk_bf16_f32 v83,v119,v120\n"
"v_mfma_f32_16x16x32_bf16 v[16:19],a[68:71],a[36:39],v[16:19]\n"
"v_exp_f32 v123,v41\n"
"v_mfma_f32_16x16x32_bf16 v[16:19],a[72:75],a[40:43],v[16:19]\n"
"buffer_load_dwordx4 v112,s[24:27],s64 offen offset:0 sc1 lds\n"
"v_mfma_f32_16x16x32_bf16 v[16:19],a[76:79],a[44:47],v[16:19]\n"
"v_exp_f32 v119,v42\n"
"v_mfma_f32_16x16x32_bf16 v[20:23],a[92:95],a[44:47],v[104:107]\n"
"v_cvt_pk_bf16_f32 v84,v122,v123\n"
"v_or3_b32 v118,v118,v83,v84\n"
"v_mfma_f32_16x16x32_bf16 v[20:23],a[88:91],a[40:43],v[20:23]\n"
"v_exp_f32 v120,v43\n"
"v_mfma_f32_16x16x32_bf16 v[20:23],a[84:87],a[36:39],v[20:23]\n"
"buffer_load_dwordx4 v112,s[24:27],s64 offen offset:1024 sc1 lds\n"
"v_mfma_f32_16x16x32_bf16 v[20:23],a[80:83],a[32:35],v[20:23]\n"
"v_exp_f32 v122,v44\n"
"v_mfma_f32_16x16x32_bf16 v[28:31],a[80:83],a[48:51],v[108:111]\n"
"v_cvt_pk_bf16_f32 v85,v119,v120\n"
"v_mfma_f32_16x16x32_bf16 v[28:31],a[84:87],a[52:55],v[28:31]\n"
"v_exp_f32 v123,v45\n"
"v_mfma_f32_16x16x32_bf16 v[28:31],a[88:91],a[56:59],v[28:31]\n"
"buffer_load_dwordx4 v112,s[24:27],s64 offen offset:2048 sc1 lds\n"
"v_mfma_f32_16x16x32_bf16 v[28:31],a[92:95],a[60:63],v[28:31]\n"
"v_exp_f32 v119,v46\n"
"v_mfma_f32_16x16x32_bf16 v[24:27],a[76:79],a[60:63],v[108:111]\n"
"v_cvt_pk_bf16_f32 v86,v122,v123\n"
"v_or3_b32 v118,v118,v85,v86\n"
"v_mfma_f32_16x16x32_bf16 v[24:27],a[72:75],a[56:59],v[24:27]\n"
"v_exp_f32 v120,v47\n"
"v_mfma_f32_16x16x32_bf16 v[24:27],a[68:71],a[52:55],v[24:27]\n"
"buffer_load_dwordx4 v112,s[24:27],s64 offen offset:3072 sc1 lds\n"
"v_mfma_f32_16x16x32_bf16 v[24:27],a[64:67],a[48:51],v[24:27]\n"
"s_waitcnt lgkmcnt(0)\n"
"v_exp_f32 v122,v48\n"
"v_mfma_f32_16x16x32_bf16 v[128:131],a[96:99],v[64:67],0\n"
"v_exp_f32 v123,v49\n"
"v_mfma_f32_16x16x32_bf16 v[160:163],a[96:99],v[68:71],0\n"
"ds_read_b128 a[64:67],v113 offset:32768\n"
"v_cvt_pk_bf16_f32 v87,v119,v120\n"
"v_mfma_f32_16x16x32_bf16 v[192:195],a[96:99],v[72:75],0\n"
"s_add_u32 m0,s67,53248\n"
"v_exp_f32 v119,v50\n"
"v_mfma_f32_16x16x32_bf16 v[224:227],a[96:99],v[76:79],0\n"
"ds_read_b128 a[68:71],v113 offset:33792\n"
"v_cvt_pk_bf16_f32 v88,v122,v123\n"
"v_mfma_f32_16x16x32_bf16 a[140:143],a[144:147],v[76:79],0\n"
"v_exp_f32 v120,v51\n"
"v_mfma_f32_16x16x32_bf16 v[228:231],a[100:103],v[76:79],0\n"
"ds_read_b128 a[72:75],v113 offset:34816\n"
"ds_read_b128 a[76:79],v113 offset:35840\n"
"v_mfma_f32_16x16x32_bf16 v[132:135],a[100:103],v[64:67],0\n"
"v_exp_f32 v122,v52\n"
"v_mfma_f32_16x16x32_bf16 v[164:167],a[100:103],v[68:71],0\n"
"ds_read_b128 a[80:83],v113 offset:36864\n"
"v_cvt_pk_bf16_f32 v89,v119,v120\n"
"v_mfma_f32_16x16x32_bf16 v[196:199],a[100:103],v[72:75],0\n"
"v_exp_f32 v123,v53\n"
"v_mfma_f32_16x16x32_bf16 a[136:139],a[144:147],v[72:75],0\n"
"ds_read_b128 a[84:87],v113 offset:37888\n"
"ds_read_b128 a[88:91],v113 offset:38912\n"
"v_mfma_f32_16x16x32_bf16 v[200:203],a[104:107],v[72:75],0\n"
"v_exp_f32 v119,v54\n"
"v_mfma_f32_16x16x32_bf16 v[232:235],a[104:107],v[76:79],0\n"
"ds_read_b128 a[92:95],v113 offset:39936\n"
"v_cvt_pk_bf16_f32 v90,v122,v123\n"
"v_mfma_f32_16x16x32_bf16 v[136:139],a[104:107],v[64:67],0\n"
"v_exp_f32 v120,v55\n"
"v_mfma_f32_16x16x32_bf16 v[168:171],a[104:107],v[68:71],0\n"
"s_waitcnt vmcnt(16)\n"
"ds_add_u32 v124,a152\n"
"v_mfma_f32_16x16x32_bf16 a[132:135],a[144:147],v[68:71],0\n"
"v_exp_f32 v122,v56\n"
"v_mfma_f32_16x16x32_bf16 v[172:175],a[108:111],v[68:71],0\n"
"v_cvt_pk_bf16_f32 v91,v119,v120\n"
"v_or3_b32 v118,v118,v87,v88\n"
"v_mfma_f32_16x16x32_bf16 v[204:207],a[108:111],v[72:75],0\n"
"v_exp_f32 v123,v57\n"
"v_mfma_f32_16x16x32_bf16 v[236:239],a[108:111],v[76:79],0\n"
"buffer_load_dwordx4 v117,s[24:27],s64 offen offset:0 sc1 lds\n"
"v_mfma_f32_16x16x32_bf16 v[140:143],a[108:111],v[64:67],0\n"
"v_exp_f32 v119,v58\n"
"v_mfma_f32_16x16x32_bf16 a[128:131],a[144:147],v[64:67],0\n"
"v_cvt_pk_bf16_f32 v92,v122,v123\n"
"v_or3_b32 v118,v118,v89,v90\n"
"v_mfma_f32_16x16x32_bf16 v[144:147],a[112:115],v[64:67],0\n"
"v_exp_f32 v120,v59\n"
"v_mfma_f32_16x16x32_bf16 v[176:179],a[112:115],v[68:71],0\n"
"buffer_load_dwordx4 v117,s[24:27],s64 offen offset:1024 sc1 lds\n"
"v_mfma_f32_16x16x32_bf16 v[208:211],a[112:115],v[72:75],0\n"
"v_exp_f32 v122,v60\n"
"v_mfma_f32_16x16x32_bf16 v[240:243],a[112:115],v[76:79],0\n"
"v_cvt_pk_bf16_f32 v93,v119,v120\n"
"v_or3_b32 v118,v118,v91,v92\n"
"v_mfma_f32_16x16x32_bf16 v[244:247],a[116:119],v[76:79],0\n"
"v_exp_f32 v123,v61\n"
"v_mfma_f32_16x16x32_bf16 v[148:151],a[116:119],v[64:67],0\n"
"buffer_load_dwordx4 v117,s[24:27],s64 offen offset:2048 sc1 lds\n"
"v_mfma_f32_16x16x32_bf16 v[180:183],a[116:119],v[68:71],0\n"
"v_exp_f32 v119,v62\n"
"v_mfma_f32_16x16x32_bf16 v[212:215],a[116:119],v[72:75],0\n"
"ds_read_b32 v125,v124\n"
"v_cvt_pk_bf16_f32 v94,v122,v123\n"
"v_mfma_f32_16x16x32_bf16 v[216:219],a[120:123],v[72:75],0\n"
"v_exp_f32 v120,v63\n"
"v_mfma_f32_16x16x32_bf16 v[248:251],a[120:123],v[76:79],0\n"
"buffer_load_dwordx4 v117,s[24:27],s64 offen offset:3072 sc1 lds\n"
"v_mfma_f32_16x16x32_bf16 v[152:155],a[120:123],v[64:67],0\n"
"buffer_store_dwordx4 a[248:251],v112,s[24:27],s56 offen offset:2048 sc1\n"
"v_mfma_f32_16x16x32_bf16 v[184:187],a[120:123],v[68:71],0\n"
"s_add_u32 s64,s64,16384\n"
"v_cvt_pk_bf16_f32 v95,v119,v120\n"
"v_or3_b32 v118,v118,v93,v94\n"
"v_mfma_f32_16x16x32_bf16 v[188:191],a[124:127],v[68:71],0\n"
"s_waitcnt lgkmcnt(0)\n"
"v_subrev_u32 v125,s101,v125\n"
"v_or3_b32 v125,v118,v95,v125\n"
"v_mfma_f32_16x16x32_bf16 v[220:223],a[124:127],v[72:75],0\n"
"v_and_b32 v125,0x40004000,v125\n"
"v_cmp_ne_u32 vcc,0,v125\n"
"v_mfma_f32_16x16x32_bf16 v[252:255],a[124:127],v[76:79],0\n"
"v_mfma_f32_16x16x32_bf16 v[156:159],a[124:127],v[64:67],0\n"
"s_cbranch_vccnz .Lfk_cs_v1\n"
".Lfk_cr_v1:\n"
"v_mfma_f32_16x16x32_bf16 v[32:35],a[64:67],a[0:3],v[96:99]\n"
"s_add_u32 s101,s101,4\n"
"v_exp_f32 v122,v0\n"
"v_mfma_f32_16x16x32_bf16 v[32:35],a[68:71],a[4:7],v[32:35]\n"
"s_add_u32 m0,s55,12288\n"
"v_exp_f32 v123,v1\n"
"v_mfma_f32_16x16x32_bf16 v[32:35],a[72:75],a[8:11],v[32:35]\n"
"ds_read_b128 a[96:99],v114 offset:8192\n"
"ds_read_b128 a[100:103],v114 offset:9216\n"
"v_mfma_f32_16x16x32_bf16 v[32:35],a[76:79],a[12:15],v[32:35]\n"
"s_add_u32 s54,s54,s22\n"
"v_exp_f32 v119,v2\n"
"v_mfma_f32_16x16x32_bf16 v[36:39],a[92:95],a[12:15],v[96:99]\n"
"ds_read_b128 a[104:107],v114 offset:10240\n"
"v_cvt_pk_bf16_f32 v64,v122,v123\n"
"v_mfma_f32_16x16x32_bf16 v[36:39],a[88:91],a[8:11],v[36:39]\n"
"v_exp_f32 v120,v3\n"
"v_mfma_f32_16x16x32_bf16 v[36:39],a[84:87],a[4:7],v[36:39]\n"
"ds_read_b128 a[108:111],v114 offset:11264\n"
"ds_read_b128 a[112:115],v114 offset:12288\n"
"v_mfma_f32_16x16x32_bf16 v[36:39],a[80:83],a[0:3],v[36:39]\n"
"v_exp_f32 v122,v4\n"
"v_mfma_f32_16x16x32_bf16 v[44:47],a[80:83],a[16:19],v[100:103]\n"
"ds_read_b128 a[116:119],v114 offset:13312\n"
"v_cvt_pk_bf16_f32 v65,v119,v120\n"
"v_mfma_f32_16x16x32_bf16 v[44:47],a[84:87],a[20:23],v[44:47]\n"
"v_exp_f32 v123,v5\n"
"v_mfma_f32_16x16x32_bf16 v[44:47],a[88:91],a[24:27],v[44:47]\n"
"ds_read_b128 a[120:123],v114 offset:14336\n"
"ds_read_b128 a[124:127],v114 offset:15360\n"
"v_mfma_f32_16x16x32_bf16 v[44:47],a[92:95],a[28:31],v[44:47]\n"
"v_exp_f32 v119,v6\n"
"v_mfma_f32_16x16x32_bf16 v[40:43],a[76:79],a[28:31],v[100:103]\n"
"v_cvt_pk_bf16_f32 v66,v122,v123\n"
"v_or3_b32 v118,v64,v65,v66\n"
"v_mfma_f32_16x16x32_bf16 v[40:43],a[72:75],a[24:27],v[40:43]\n"
"v_exp_f32 v120,v7\n"
"v_mfma_f32_16x16x32_bf16 v[40:43],a[68:71],a[20:23],v[40:43]\n"
"buffer_load_dwordx4 v115,s[32:35],s54 offen sc1 lds\n"
"v_mfma_f32_16x16x32_bf16 v[40:43],a[64:67],a[16:19],v[40:43]\n"
"s_add_u32 m0,s67,32768\n"
"v_exp_f32 v122,v8\n"
"v_mfma_f32_16x16x32_bf16 v[48:51],a[64:67],a[32:35],v[104:107]\n"
"v_cvt_pk_bf16_f32 v67,v119,v120\n"
"v_mfma_f32_16x16x32_bf16 v[48:51],a[68:71],a[36:39],v[48:51]\n"
"v_exp_f32 v123,v9\n"
"v_mfma_f32_16x16x32_bf16 v[48:51],a[72:75],a[40:43],v[48:51]\n"
"buffer_load_dwordx4 v112,s[24:27],s64 offen offset:0 sc1 lds\n"
"v_mfma_f32_16x16x32_bf16 v[48:51],a[76:79],a[44:47],v[48:51]\n"
"v_exp_f32 v119,v10\n"
"v_mfma_f32_16x16x32_bf16 v[52:55],a[92:95],a[44:47],v[104:107]\n"
"v_cvt_pk_bf16_f32 v68,v122,v123\n"
"v_or3_b32 v118,v118,v67,v68\n"
"v_mfma_f32_16x16x32_bf16 v[52:55],a[88:91],a[40:43],v[52:55]\n"
"v_exp_f32 v120,v11\n"
"v_mfma_f32_16x16x32_bf16 v[52:55],a[84:87],a[36:39],v[52:55]\n"
"buffer_load_dwordx4 v112,s[24:27],s64 offen offset:1024 sc1 lds\n"
"v_mfma_f32_16x16x32_bf16 v[52:55],a[80:83],a[32:35],v[52:55]\n"
"v_exp_f32 v122,v12\n"
"v_mfma_f32_16x16x32_bf16 v[60:63],a[80:83],a[48:51],v[108:111]\n"
"v_cvt_pk_bf16_f32 v69,v119,v120\n"
"v_mfma_f32_16x16x32_bf16 v[60:63],a[84:87],a[52:55],v[60:63]\n"
"v_exp_f32 v123,v13\n"
"v_mfma_f32_16x16x32_bf16 v[60:63],a[88:91],a[56:59],v[60:63]\n"
"buffer_load_dwordx4 v112,s[24:27],s64 offen offset:2048 sc1 lds\n"
"v_mfma_f32_16x16x32_bf16 v[60:63],a[92:95],a[60:63],v[60:63]\n"
"v_exp_f32 v119,v14\n"
"v_mfma_f32_16x16x32_bf16 v[56:59],a[76:79],a[60:63],v[108:111]\n"
"v_cvt_pk_bf16_f32 v70,v122,v123\n"
"v_or3_b32 v118,v118,v69,v70\n"
"v_mfma_f32_16x16x32_bf16 v[56:59],a[72:75],a[56:59],v[56:59]\n"
"v_exp_f32 v120,v15\n"
"v_mfma_f32_16x16x32_bf16 v[56:59],a[68:71],a[52:55],v[56:59]\n"
"buffer_load_dwordx4 v112,s[24:27],s64 offen offset:3072 sc1 lds\n"
"v_mfma_f32_16x16x32_bf16 v[56:59],a[64:67],a[48:51],v[56:59]\n"
"s_waitcnt lgkmcnt(0)\n"
"v_exp_f32 v122,v16\n"
"v_mfma_f32_16x16x32_bf16 v[128:131],a[96:99],v[80:83],v[128:131]\n"
"v_exp_f32 v123,v17\n"
"v_mfma_f32_16x16x32_bf16 v[160:163],a[96:99],v[84:87],v[160:163]\n"
"ds_read_b128 a[64:67],v113 offset:0\n"
"v_cvt_pk_bf16_f32 v71,v119,v120\n"
"v_mfma_f32_16x16x32_bf16 v[192:195],a[96:99],v[88:91],v[192:195]\n"
"s_add_u32 m0,s67,36864\n"
"v_exp_f32 v119,v18\n"
"v_mfma_f32_16x16x32_bf16 v[224:227],a[96:99],v[92:95],v[224:227]\n"
"ds_read_b128 a[68:71],v113 offset:1024\n"
"v_cvt_pk_bf16_f32 v72,v122,v123\n"
"v_mfma_f32_16x16x32_bf16 a[140:143],a[144:147],v[92:95],a[140:143]\n"
"v_exp_f32 v120,v19\n"
"v_mfma_f32_16x16x32_bf16 v[228:231],a[100:103],v[92:95],v[228:231]\n"
"ds_read_b128 a[72:75],v113 offset:2048\n"
"ds_read_b128 a[76:79],v113 offset:3072\n"
"v_mfma_f32_16x16x32_bf16 v[132:135],a[100:103],v[80:83],v[132:135]\n"
"v_exp_f32 v122,v20\n"
"v_mfma_f32_16x16x32_bf16 v[164:167],a[100:103],v[84:87],v[164:167]\n"
"ds_read_b128 a[80:83],v113 offset:4096\n"
"v_cvt_pk_bf16_f32 v73,v119,v120\n"
"v_mfma_f32_16x16x32_bf16 v[196:199],a[100:103],v[88:91],v[196:199]\n"
"v_exp_f32 v123,v21\n"
"v_mfma_f32_16x16x32_bf16 a[136:139],a[144:147],v[88:91],a[136:139]\n"
"ds_read_b128 a[84:87],v113 offset:5120\n"
"ds_read_b128 a[88:91],v113 offset:6144\n"
"v_mfma_f32_16x16x32_bf16 v[200:203],a[104:107],v[88:91],v[200:203]\n"
"v_exp_f32 v119,v22\n"
"v_mfma_f32_16x16x32_bf16 v[232:235],a[104:107],v[92:95],v[232:235]\n"
"ds_read_b128 a[92:95],v113 offset:7168\n"
"v_cvt_pk_bf16_f32 v74,v122,v123\n"
"v_mfma_f32_16x16x32_bf16 v[136:139],a[104:107],v[80:83],v[136:139]\n"
"v_exp_f32 v120,v23\n"
"v_mfma_f32_16x16x32_bf16 v[168:171],a[104:107],v[84:87],v[168:171]\n"
"s_waitcnt vmcnt(16)\n"
"ds_add_u32 v124,a152\n"
"v_mfma_f32_16x16x32_bf16 a[132:135],a[144:147],v[84:87],a[132:135]\n"
"v_exp_f32 v122,v24\n"
"v_mfma_f32_16x16x32_bf16 v[172:175],a[108:111],v[84:87],v[172:175]\n"
"v_cvt_pk_bf16_f32 v75,v119,v120\n"
"v_or3_b32 v118,v118,v71,v72\n"
"v_mfma_f32_16x16x32_bf16 v[204:207],a[108:111],v[88:91],v[204:207]\n"
"v_exp_f32 v123,v25\n"
"v_mfma_f32_16x16x32_bf16 v[236:239],a[108:111],v[92:95],v[236:239]\n"
"buffer_load_dwordx4 v117,s[24:27],s64 offen offset:0 sc1 lds\n"
"v_mfma_f32_16x16x32_bf16 v[140:143],a[108:111],v[80:83],v[140:143]\n"
"v_exp_f32 v119,v26\n"
"v_mfma_f32_16x16x32_bf16 a[128:131],a[144:147],v[80:83],a[128:131]\n"
"v_cvt_pk_bf16_f32 v76,v122,v123\n"
"v_or3_b32 v118,v118,v73,v74\n"
"v_mfma_f32_16x16x32_bf16 v[144:147],a[112:115],v[80:83],v[144:147]\n"
"v_exp_f32 v120,v27\n"
"v_mfma_f32_16x16x32_bf16 v[176:179],a[112:115],v[84:87],v[176:179]\n"
"buffer_load_dwordx4 v117,s[24:27],s64 offen offset:1024 sc1 lds\n"
"v_mfma_f32_16x16x32_bf16 v[208:211],a[112:115],v[88:91],v[208:211]\n"
"v_exp_f32 v122,v28\n"
"v_mfma_f32_16x16x32_bf16 v[240:243],a[112:115],v[92:95],v[240:243]\n"
"v_cvt_pk_bf16_f32 v77,v119,v120\n"
"v_or3_b32 v118,v118,v75,v76\n"
"v_mfma_f32_16x16x32_bf16 v[244:247],a[116:119],v[92:95],v[244:247]\n"
"v_exp_f32 v123,v29\n"
"v_mfma_f32_16x16x32_bf16 v[148:151],a[116:119],v[80:83],v[148:151]\n"
"buffer_load_dwordx4 v117,s[24:27],s64 offen offset:2048 sc1 lds\n"
"v_mfma_f32_16x16x32_bf16 v[180:183],a[116:119],v[84:87],v[180:183]\n"
"v_exp_f32 v119,v30\n"
"v_mfma_f32_16x16x32_bf16 v[212:215],a[116:119],v[88:91],v[212:215]\n"
"ds_read_b32 v125,v124\n"
"v_cvt_pk_bf16_f32 v78,v122,v123\n"
"v_mfma_f32_16x16x32_bf16 v[216:219],a[120:123],v[88:91],v[216:219]\n"
"v_exp_f32 v120,v31\n"
"v_mfma_f32_16x16x32_bf16 v[248:251],a[120:123],v[92:95],v[248:251]\n"
"buffer_load_dwordx4 v117,s[24:27],s64 offen offset:3072 sc1 lds\n"
"v_mfma_f32_16x16x32_bf16 v[152:155],a[120:123],v[80:83],v[152:155]\n"
"buffer_store_dwordx4 a[252:255],v112,s[24:27],s56 offen offset:3072 sc1\n"
"v_mfma_f32_16x16x32_bf16 v[184:187],a[120:123],v[84:87],v[184:187]\n"
"s_add_u32 s64,s64,16384\n"
"v_cvt_pk_bf16_f32 v79,v119,v120\n"
"v_or3_b32 v118,v118,v77,v78\n"
"v_mfma_f32_16x16x32_bf16 v[188:191],a[124:127],v[84:87],v[188:191]\n"
"s_waitcnt lgkmcnt(0)\n"
"v_subrev_u32 v125,s101,v125\n"
"v_or3_b32 v125,v118,v79,v125\n"
"v_mfma_f32_16x16x32_bf16 v[220:223],a[124:127],v[88:91],v[220:223]\n"
"v_and_b32 v125,0x40004000,v125\n"
"v_cmp_ne_u32 vcc,0,v125\n"
"v_mfma_f32_16x16x32_bf16 v[252:255],a[124:127],v[92:95],v[252:255]\n"
"v_mfma_f32_16x16x32_bf16 v[156:159],a[124:127],v[80:83],v[156:159]\n"
"s_cbranch_vccnz .Lfk_cs_v2\n"
".Lfk_cr_v2:\n"
"s_waitcnt vmcnt(20)\n"
"buffer_store_dwordx2 a[154:155],off,s[24:27],s69 sc1\n"
"s_branch .Lfk_Hx\n"
".Lfk_tailB:\n"
"s_waitcnt vmcnt(11) lgkmcnt(0)\n"
"s_barrier\n"
"v_mfma_f32_16x16x32_bf16 v[0:3],a[64:67],a[0:3],v[96:99]\n"
"ds_read_b128 a[96:99],v114 offset:24576\n"
"ds_read_b128 a[100:103],v114 offset:25600\n"
"ds_read_b128 a[104:107],v114 offset:26624\n"
"v_mfma_f32_16x16x32_bf16 v[0:3],a[68:71],a[4:7],v[0:3]\n"
"ds_read_b128 a[108:111],v114 offset:27648\n"
"ds_read_b128 a[112:115],v114 offset:28672\n"
"ds_read_b128 a[116:119],v114 offset:29696\n"
"v_mfma_f32_16x16x32_bf16 v[0:3],a[72:75],a[8:11],v[0:3]\n"
"ds_read_b128 a[120:123],v114 offset:30720\n"
"ds_read_b128 a[124:127],v114 offset:31744\n"
"v_add_f32 v16,0x80000000,v126\n"
"v_mfma_f32_16x16x32_bf16 v[0:3],a[76:79],a[12:15],v[0:3]\n"
"v_min_f32 v16,v16,v104\n"
"v_add_f32 v17,0xf149f2ca,v126\n"
"v_min_f32 v17,v17,v105\n"
"v_mfma_f32_16x16x32_bf16 v[4:7],a[92:95],a[12:15],v[96:99]\n"
"v_add_f32 v18,0xf1c9f2ca,v126\n"
"v_min_f32 v18,v18,v106\n"
"v_add_f32 v19,0xf2177617,v126\n"
"v_mfma_f32_16x16x32_bf16 v[4:7],a[88:91],a[8:11],v[4:7]\n"
"v_min_f32 v19,v19,v107\n"
"v_add_f32 v20,0xf349f2ca,v126\n"
"v_min_f32 v20,v20,v104\n"
"v_mfma_f32_16x16x32_bf16 v[4:7],a[84:87],a[4:7],v[4:7]\n"
"v_add_f32 v21,0xf35691f6,v126\n"
"v_min_f32 v21,v21,v105\n"
"v_add_f32 v22,0xf3633123,v126\n"
"v_mfma_f32_16x16x32_bf16 v[4:7],a[80:83],a[0:3],v[4:7]\n"
"v_min_f32 v22,v22,v106\n"
"v_add_f32 v23,0xf36fd050,v126\n"
"v_min_f32 v23,v23,v107\n"
"v_mfma_f32_16x16x32_bf16 v[12:15],a[80:83],a[16:19],v[100:103]\n"
"v_exp_f32 v32,v32\n"
"v_max3_f32 v118,v0,v1,v2\n"
"v_mfma_f32_16x16x32_bf16 v[12:15],a[84:87],a[20:23],v[12:15]\n"
"v_exp_f32 v33,v33\n"
"v_mfma_f32_16x16x32_bf16 v[12:15],a[88:91],a[24:27],v[12:15]\n"
"v_exp_f32 v34,v34\n"
"v_cvt_pk_bf16_f32 v80,v32,v33\n"
"v_mfma_f32_16x16x32_bf16 v[12:15],a[92:95],a[28:31],v[12:15]\n"
"v_exp_f32 v35,v35\n"
"v_max3_f32 v118,v118,v3,v4\n"
"v_mfma_f32_16x16x32_bf16 v[8:11],a[76:79],a[28:31],v[100:103]\n"
"v_exp_f32 v36,v36\n"
"v_cvt_pk_bf16_f32 v81,v34,v35\n"
"v_mfma_f32_16x16x32_bf16 v[8:11],a[72:75],a[24:27],v[8:11]\n"
"v_exp_f32 v37,v37\n"
"v_max3_f32 v118,v118,v5,v6\n"
"v_mfma_f32_16x16x32_bf16 v[8:11],a[68:71],a[20:23],v[8:11]\n"
"v_exp_f32 v38,v38\n"
"v_mfma_f32_16x16x32_bf16 v[8:11],a[64:67],a[16:19],v[8:11]\n"
"v_exp_f32 v39,v39\n"
"v_max3_f32 v118,v118,v7,v12\n"
"v_mfma_f32_16x16x32_bf16 v[24:27],a[64:67],a[48:51],v[108:111]\n"
"v_exp_f32 v40,v40\n"
"v_mfma_f32_16x16x32_bf16 v[24:27],a[68:71],a[52:55],v[24:27]\n"
"v_exp_f32 v41,v41\n"
"v_max3_f32 v118,v118,v13,v14\n"
"v_mfma_f32_16x16x32_bf16 v[24:27],a[72:75],a[56:59],v[24:27]\n"
"v_cvt_pk_bf16_f32 v82,v36,v37\n"
"v_cvt_pk_bf16_f32 v84,v40,v41\n"
"v_mfma_f32_16x16x32_bf16 v[24:27],a[76:79],a[60:63],v[24:27]\n"
"v_exp_f32 v42,v42\n"
"v_max3_f32 v118,v118,v15,v8\n"
"v_mfma_f32_16x16x32_bf16 v[28:31],a[92:95],a[60:63],v[108:111]\n"
"v_cvt_pk_bf16_f32 v83,v38,v39\n"
"v_max3_f32 v118,v118,v9,v10\n"
"v_mfma_f32_16x16x32_bf16 v[28:31],a[88:91],a[56:59],v[28:31]\n"
"v_exp_f32 v43,v43\n"
"v_mfma_f32_16x16x32_bf16 v[28:31],a[84:87],a[52:55],v[28:31]\n"
"v_cvt_pk_bf16_f32 v85,v42,v43\n"
"v_mfma_f32_16x16x32_bf16 v[28:31],a[80:83],a[48:51],v[28:31]\n"
"v_exp_f32 v44,v44\n"
"v_mfma_f32_16x16x32_bf16 v[20:23],a[80:83],a[32:35],v[20:23]\n"
"v_max3_f32 v118,v118,v11,v24\n"
"v_max3_f32 v118,v118,v25,v26\n"
"v_mfma_f32_16x16x32_bf16 v[20:23],a[84:87],a[36:39],v[20:23]\n"
"v_exp_f32 v45,v45\n"
"v_mfma_f32_16x16x32_bf16 v[20:23],a[88:91],a[40:43],v[20:23]\n"
"v_cvt_pk_bf16_f32 v86,v44,v45\n"
"v_mfma_f32_16x16x32_bf16 v[20:23],a[92:95],a[44:47],v[20:23]\n"
"v_exp_f32 v46,v46\n"
"v_mfma_f32_16x16x32_bf16 v[16:19],a[76:79],a[44:47],v[16:19]\n"
"v_max3_f32 v118,v118,v27,v28\n"
"v_max3_f32 v118,v118,v29,v30\n"
"v_mfma_f32_16x16x32_bf16 v[16:19],a[72:75],a[40:43],v[16:19]\n"
"v_exp_f32 v47,v47\n"
"v_mfma_f32_16x16x32_bf16 v[16:19],a[68:71],a[36:39],v[16:19]\n"
"v_cvt_pk_bf16_f32 v87,v46,v47\n"
"v_exp_f32 v48,v48\n"
"v_mfma_f32_16x16x32_bf16 v[16:19],a[64:67],a[32:35],v[16:19]\n"
"v_max3_f32 v118,v118,v31,v20\n"
"s_waitcnt lgkmcnt(0)\n"
"v_mfma_f32_16x16x32_bf16 v[128:131],a[96:99],v[64:67],v[128:131]\n"
"v_exp_f32 v49,v49\n"
"v_max3_f32 v118,v118,v21,v22\n"
"v_cvt_pk_bf16_f32 v88,v48,v49\n"
"v_mfma_f32_16x16x32_bf16 v[160:163],a[96:99],v[68:71],v[160:163]\n"
"ds_read_b128 a[64:67],v114 offset:57344\n"
"ds_read_b128 a[68:71],v114 offset:58368\n"
"v_mfma_f32_16x16x32_bf16 v[192:195],a[96:99],v[72:75],v[192:195]\n"
"ds_read_b128 a[72:75],v114 offset:59392\n"
"v_exp_f32 v50,v50\n"
"v_mfma_f32_16x16x32_bf16 v[224:227],a[96:99],v[76:79],v[224:227]\n"
"v_max3_f32 v118,v118,v23,v16\n"
"v_max3_f32 v118,v118,v17,v18\n"
"v_max_f32 v118,v118,v19\n"
"v_mfma_f32_16x16x32_bf16 a[140:143],a[144:147],v[76:79],a[140:143]\n"
"v_exp_f32 v51,v51\n"
"v_cmp_lt_f32 vcc,s93,v118\n"
"v_cvt_pk_bf16_f32 v89,v50,v51\n"
"v_mfma_f32_16x16x32_bf16 v[228:231],a[100:103],v[76:79],v[228:231]\n"
"ds_read_b128 a[76:79],v114 offset:60416\n"
"ds_read_b128 a[80:83],v114 offset:61440\n"
"v_mfma_f32_16x16x32_bf16 v[132:135],a[100:103],v[64:67],v[132:135]\n"
"ds_read_b128 a[84:87],v114 offset:62464\n"
"v_exp_f32 v52,v52\n"
"v_mfma_f32_16x16x32_bf16 v[164:167],a[100:103],v[68:71],v[164:167]\n"
"s_sub_u32 s82,s3,32768\n"
"ds_read_b128 a[88:91],v114 offset:63488\n"
"ds_read_b128 a[92:95],v114 offset:64512\n"
"v_mfma_f32_16x16x32_bf16 v[196:199],a[100:103],v[72:75],v[196:199]\n"
"buffer_load_dwordx4 a[0:3],v112,s[24:27],s82 offen offset:0 sc1\n"
"v_exp_f32 v53,v53\n"
"v_mfma_f32_16x16x32_bf16 a[136:139],a[144:147],v[72:75],a[136:139]\n"
"buffer_load_dwordx4 a[4:7],v112,s[24:27],s82 offen offset:1024 sc1\n"
"buffer_load_dwordx4 a[8:11],v112,s[24:27],s82 offen offset:2048 sc1\n"
"v_cvt_pk_bf16_f32 v90,v52,v53\n"
"v_exp_f32 v54,v54\n"
"v_mfma_f32_16x16x32_bf16 v[200:203],a[104:107],v[72:75],v[200:203]\n"
"buffer_load_dwordx4 a[12:15],v112,s[24:27],s82 offen offset:3072 sc1\n"
"v_mfma_f32_16x16x32_bf16 v[232:235],a[104:107],v[76:79],v[232:235]\n"
"s_sub_u32 s82,s3,49152\n"
"buffer_load_dwordx4 a[16:19],v112,s[24:27],s82 offen offset:0 sc1\n"
"buffer_load_dwordx4 a[20:23],v112,s[24:27],s82 offen offset:1024 sc1\n"
"v_exp_f32 v55,v55\n"
"v_mfma_f32_16x16x32_bf16 v[136:139],a[104:107],v[64:67],v[136:139]\n"
"buffer_load_dwordx4 a[24:27],v112,s[24:27],s82 offen offset:2048 sc1\n"
"v_mfma_f32_16x16x32_bf16 v[168:171],a[104:107],v[68:71],v[168:171]\n"
"buffer_load_dwordx4 a[28:31],v112,s[24:27],s82 offen offset:3072 sc1\n"
"v_mfma_f32_16x16x32_bf16 a[132:135],a[144:147],v[68:71],a[132:135]\n"
"v_exp_f32 v56,v56\n"
"v_mfma_f32_16x16x32_bf16 v[172:175],a[108:111],v[68:71],v[172:175]\n"
"v_cvt_pk_bf16_f32 v91,v54,v55\n"
"v_mfma_f32_16x16x32_bf16 v[204:207],a[108:111],v[72:75],v[204:207]\n"
"v_exp_f32 v57,v57\n"
"v_mfma_f32_16x16x32_bf16 v[236:239],a[108:111],v[76:79],v[236:239]\n"
"v_cvt_pk_bf16_f32 v92,v56,v57\n"
"v_mfma_f32_16x16x32_bf16 v[140:143],a[108:111],v[64:67],v[140:143]\n"
"v_exp_f32 v58,v58\n"
"v_mfma_f32_16x16x32_bf16 a[128:131],a[144:147],v[64:67],a[128:131]\n"
"v_mfma_f32_16x16x32_bf16 v[144:147],a[112:115],v[64:67],v[144:147]\n"
"v_exp_f32 v59,v59\n"
"v_mfma_f32_16x16x32_bf16 v[176:179],a[112:115],v[68:71],v[176:179]\n"
"v_cvt_pk_bf16_f32 v93,v58,v59\n"
"v_mfma_f32_16x16x32_bf16 v[208:211],a[112:115],v[72:75],v[208:211]\n"
"v_exp_f32 v60,v60\n"
"v_mfma_f32_16x16x32_bf16 v[240:243],a[112:115],v[76:79],v[240:243]\n"
"v_mfma_f32_16x16x32_bf16 v[244:247],a[116:119],v[76:79],v[244:247]\n"
"v_exp_f32 v61,v61\n"
"v_mfma_f32_16x16x32_bf16 v[148:151],a[116:119],v[64:67],v[148:151]\n"
"v_cvt_pk_bf16_f32 v94,v60,v61\n"
"v_mfma_f32_16x16x32_bf16 v[180:183],a[116:119],v[68:71],v[180:183]\n"
"v_exp_f32 v62,v62\n"
"v_mfma_f32_16x16x32_bf16 v[212:215],a[116:119],v[72:75],v[212:215]\n"
"v_mfma_f32_16x16x32_bf16 v[216:219],a[120:123],v[72:75],v[216:219]\n"
"v_exp_f32 v63,v63\n"
"v_mfma_f32_16x16x32_bf16 v[248:251],a[120:123],v[76:79],v[248:251]\n"
"v_cvt_pk_bf16_f32 v95,v62,v63\n"
"v_mfma_f32_16x16x32_bf16 v[152:155],a[120:123],v[64:67],v[152:155]\n"
"v_mfma_f32_16x16x32_bf16 v[184:187],a[120:123],v[68:71],v[184:187]\n"
"v_mfma_f32_16x16x32_bf16 v[188:191],a[124:127],v[68:71],v[188:191]\n"
"v_mfma_f32_16x16x32_bf16 v[220:223],a[124:127],v[72:75],v[220:223]\n"
"v_mfma_f32_16x16x32_bf16 v[252:255],a[124:127],v[76:79],v[252:255]\n"
"v_mfma_f32_16x16x32_bf16 v[156:159],a[124:127],v[64:67],v[156:159]\n"
"s_cbranch_vccnz .Lfk_cs_b3\n"
".Lfk_cr_b3:\n"
"s_waitcnt vmcnt(9) lgkmcnt(0)\n"
"s_barrier\n"
"v_mfma_f32_16x16x32_bf16 v[128:131],a[64:67],v[80:83],v[128:131]\n"
"v_exp_f32 v0,v0\n"
"v_mfma_f32_16x16x32_bf16 v[160:163],a[64:67],v[84:87],v[160:163]\n"
"v_exp_f32 v1,v1\n"
"v_mfma_f32_16x16x32_bf16 v[192:195],a[64:67],v[88:91],v[192:195]\n"
"v_exp_f32 v2,v2\n"
"v_cvt_pk_bf16_f32 v64,v0,v1\n"
"v_mfma_f32_16x16x32_bf16 v[224:227],a[64:67],v[92:95],v[224:227]\n"
"v_exp_f32 v3,v3\n"
"v_mfma_f32_16x16x32_bf16 a[140:143],a[144:147],v[92:95],a[140:143]\n"
"v_exp_f32 v4,v4\n"
"v_cvt_pk_bf16_f32 v65,v2,v3\n"
"v_mfma_f32_16x16x32_bf16 v[228:231],a[68:71],v[92:95],v[228:231]\n"
"v_exp_f32 v5,v5\n"
"v_mfma_f32_16x16x32_bf16 v[132:135],a[68:71],v[80:83],v[132:135]\n"
"v_exp_f32 v6,v6\n"
"v_cvt_pk_bf16_f32 v66,v4,v5\n"
"v_exp_f32 v7,v7\n"
"v_mfma_f32_16x16x32_bf16 v[164:167],a[68:71],v[84:87],v[164:167]\n"
"ds_read_b128 a[96:99],v114 offset:40960\n"
"ds_read_b128 a[100:103],v114 offset:41984\n"
"v_exp_f32 v8,v8\n"
"v_mfma_f32_16x16x32_bf16 v[196:199],a[68:71],v[88:91],v[196:199]\n"
"ds_read_b128 a[104:107],v114 offset:43008\n"
"ds_read_b128 a[108:111],v114 offset:44032\n"
"v_exp_f32 v9,v9\n"
"v_mfma_f32_16x16x32_bf16 a[136:139],a[144:147],v[88:91],a[136:139]\n"
"ds_read_b128 a[112:115],v114 offset:45056\n"
"ds_read_b128 a[116:119],v114 offset:46080\n"
"v_exp_f32 v10,v10\n"
"v_mfma_f32_16x16x32_bf16 v[200:203],a[72:75],v[88:91],v[200:203]\n"
"ds_read_b128 a[120:123],v114 offset:47104\n"
"ds_read_b128 a[124:127],v114 offset:48128\n"
"v_mfma_f32_16x16x32_bf16 v[232:235],a[72:75],v[92:95],v[232:235]\n"
"v_exp_f32 v11,v11\n"
"v_accvgpr_read_b32 v119,a150\n"
"v_accvgpr_read_b32 v120,a151\n"
"v_mfma_f32_16x16x32_bf16 v[136:139],a[72:75],v[80:83],v[136:139]\n"
"v_exp_f32 v12,v12\n"
"v_xor_b32 v119,s80,v119\n"
"v_xor_b32 v120,s81,v120\n"
"v_mfma_f32_16x16x32_bf16 v[168:171],a[72:75],v[84:87],v[168:171]\n"
"v_exp_f32 v13,v13\n"
"v_or_b32 v119,v119,v120\n"
"v_cmp_ne_u32 s[14:15],0,v119\n"
"v_mfma_f32_16x16x32_bf16 a[132:135],a[144:147],v[84:87],a[132:135]\n"
"s_add_u32 s18,s8,1032192\n"
"v_exp_f32 v14,v14\n"
"s_cmp_lg_u32 s14,0\n"
"s_cselect_b32 s18,2146435072,s18\n"
"v_mfma_f32_16x16x32_bf16 v[172:175],a[76:79],v[84:87],v[172:175]\n"
"s_add_u32 m0,s30,0\n"
"v_exp_f32 v15,v15\n"
"buffer_load_dwordx4 v112,s[24:27],s18 offen offset:0 sc1 lds\n"
"buffer_load_dwordx4 v112,s[24:27],s18 offen offset:1024 sc1 lds\n"
"v_exp_f32 v16,v16\n"
"v_mfma_f32_16x16x32_bf16 v[204:207],a[76:79],v[88:91],v[204:207]\n"
"buffer_load_dwordx4 v112,s[24:27],s18 offen offset:2048 sc1 lds\n"
"buffer_load_dwordx4 v112,s[24:27],s18 offen offset:3072 sc1 lds\n"
"v_mfma_f32_16x16x32_bf16 v[236:239],a[76:79],v[92:95],v[236:239]\n"
"s_add_u32 m0,m0,4096\n"
"v_exp_f32 v17,v17\n"
"v_cvt_pk_bf16_f32 v67,v6,v7\n"
"v_mfma_f32_16x16x32_bf16 v[140:143],a[76:79],v[80:83],v[140:143]\n"
"buffer_load_dwordx4 v117,s[24:27],s18 offen offset:0 sc1 lds\n"
"v_exp_f32 v18,v18\n"
"v_cvt_pk_bf16_f32 v68,v8,v9\n"
"v_cvt_pk_bf16_f32 v69,v10,v11\n"
"v_exp_f32 v19,v19\n"
"v_mfma_f32_16x16x32_bf16 a[128:131],a[144:147],v[80:83],a[128:131]\n"
"buffer_load_dwordx4 v117,s[24:27],s18 offen offset:1024 sc1 lds\n"
"v_cvt_pk_bf16_f32 v70,v12,v13\n"
"v_mfma_f32_16x16x32_bf16 v[144:147],a[80:83],v[80:83],v[144:147]\n"
"v_exp_f32 v20,v20\n"
"v_cvt_pk_bf16_f32 v71,v14,v15\n"
"v_cvt_pk_bf16_f32 v72,v16,v17\n"
"v_exp_f32 v21,v21\n"
"v_mfma_f32_16x16x32_bf16 v[176:179],a[80:83],v[84:87],v[176:179]\n"
"buffer_load_dwordx4 v117,s[24:27],s18 offen offset:2048 sc1 lds\n"
"v_cvt_pk_bf16_f32 v73,v18,v19\n"
"v_mfma_f32_16x16x32_bf16 v[208:211],a[80:83],v[88:91],v[208:211]\n"
"v_exp_f32 v22,v22\n"
"v_cvt_pk_bf16_f32 v74,v20,v21\n"
"v_exp_f32 v23,v23\n"
"v_mfma_f32_16x16x32_bf16 v[240:243],a[80:83],v[92:95],v[240:243]\n"
"buffer_load_dwordx4 v117,s[24:27],s18 offen offset:3072 sc1 lds\n"
"v_mfma_f32_16x16x32_bf16 v[244:247],a[84:87],v[92:95],v[244:247]\n"
"s_sub_u32 s82,s3,65536\n"
"v_exp_f32 v24,v24\n"
"v_cvt_pk_bf16_f32 v75,v22,v23\n"
"v_exp_f32 v25,v25\n"
"v_mfma_f32_16x16x32_bf16 v[148:151],a[84:87],v[80:83],v[148:151]\n"
"buffer_load_dwordx4 v[32:35],v112,s[24:27],s82 offen offset:0 sc1\n"
"v_mfma_f32_16x16x32_bf16 v[180:183],a[84:87],v[84:87],v[180:183]\n"
"v_exp_f32 v26,v26\n"
"v_cvt_pk_bf16_f32 v76,v24,v25\n"
"v_exp_f32 v27,v27\n"
"v_mfma_f32_16x16x32_bf16 v[212:215],a[84:87],v[88:91],v[212:215]\n"
"buffer_load_dwordx4 v[36:39],v112,s[24:27],s82 offen offset:1024 sc1\n"
"v_mfma_f32_16x16x32_bf16 v[216:219],a[88:91],v[88:91],v[216:219]\n"
"v_exp_f32 v28,v28\n"
"v_exp_f32 v29,v29\n"
"v_mfma_f32_16x16x32_bf16 v[248:251],a[88:91],v[92:95],v[248:251]\n"
"buffer_load_dwordx4 v[40:43],v112,s[24:27],s82 offen offset:2048 sc1\n"
"v_mfma_f32_16x16x32_bf16 v[152:155],a[88:91],v[80:83],v[152:155]\n"
"v_exp_f32 v30,v30\n"
"v_mfma_f32_16x16x32_bf16 v[184:187],a[88:91],v[84:87],v[184:187]\n"
"v_exp_f32 v31,v31\n"
"v_mfma_f32_16x16x32_bf16 v[188:191],a[92:95],v[84:87],v[188:191]\n"
"buffer_load_dwordx4 v[44:47],v112,s[24:27],s82 offen offset:3072 sc1\n"
"v_mfma_f32_16x16x32_bf16 v[220:223],a[92:95],v[88:91],v[220:223]\n"
"v_cvt_pk_bf16_f32 v78,v28,v29\n"
"v_cvt_pk_bf16_f32 v77,v26,v27\n"
"v_mfma_f32_16x16x32_bf16 v[252:255],a[92:95],v[92:95],v[252:255]\n"
"v_cvt_pk_bf16_f32 v79,v30,v31\n"
"v_mfma_f32_16x16x32_bf16 v[156:159],a[92:95],v[80:83],v[156:159]\n"
"s_load_dwordx2 s[36:37],s[0:1],0\n"
"s_load_dwordx2 s[44:45],s[0:1],80\n"
"s_load_dwordx2 s[48:49],s[0:1],88\n"
"s_lshl_b32 s82,s78,2\n"
"s_add_u32 s82,s82,65536\n"
"v_add_u32 v113,s82,v112\n"
"s_lshr_b32 s82,s78,2\n"
"s_add_u32 s82,s82,122880\n"
"v_lshrrev_b32 v115,1,v112\n"
"v_add_u32 v116,s82,v115\n"
"s_waitcnt lgkmcnt(0)\n"
"s_barrier\n"
"v_mfma_f32_16x16x32_bf16 v[128:131],a[96:99],v[64:67],v[128:131]\n"
"s_add_u32 s83,s18,2097152\n"
"s_add_u32 m0,s30,32768\n"
"v_mfma_f32_16x16x32_bf16 a[128:131],a[144:147],v[64:67],a[128:131]\n"
"buffer_load_dwordx4 v112,s[24:27],s83 offen offset:0 sc1 lds\n"
"v_mfma_f32_16x16x32_bf16 v[132:135],a[100:103],v[64:67],v[132:135]\n"
"buffer_load_dwordx4 v112,s[24:27],s83 offen offset:1024 sc1 lds\n"
"v_mfma_f32_16x16x32_bf16 v[136:139],a[104:107],v[64:67],v[136:139]\n"
"buffer_load_dwordx4 v112,s[24:27],s83 offen offset:2048 sc1 lds\n"
"v_mfma_f32_16x16x32_bf16 v[140:143],a[108:111],v[64:67],v[140:143]\n"
"v_mfma_f32_16x16x32_bf16 v[144:147],a[112:115],v[64:67],v[144:147]\n"
"buffer_load_dwordx4 v112,s[24:27],s83 offen offset:3072 sc1 lds\n"
"v_mfma_f32_16x16x32_bf16 v[148:151],a[116:119],v[64:67],v[148:151]\n"
"s_add_u32 m0,m0,4096\n"
"v_mfma_f32_16x16x32_bf16 v[152:155],a[120:123],v[64:67],v[152:155]\n"
"v_mfma_f32_16x16x32_bf16 v[156:159],a[124:127],v[64:67],v[156:159]\n"
"buffer_load_dwordx4 v117,s[24:27],s83 offen offset:0 sc1 lds\n"
"v_mfma_f32_16x16x32_bf16 v[160:163],a[96:99],v[68:71],v[160:163]\n"
"v_accvgpr_read_b32 v97,a128\n"
"v_cvt_pk_bf16_f32 v136,v136,v137\n"
"v_mfma_f32_16x16x32_bf16 a[132:135],a[144:147],v[68:71],a[132:135]\n"
"buffer_load_dwordx4 v117,s[24:27],s83 offen offset:1024 sc1 lds\n"
"v_mfma_f32_16x16x32_bf16 v[164:167],a[100:103],v[68:71],v[164:167]\n"
"v_cvt_pk_bf16_f32 v128,v128,v129\n"
"v_cvt_pk_bf16_f32 v129,v130,v131\n"
"v_mfma_f32_16x16x32_bf16 v[168:171],a[104:107],v[68:71],v[168:171]\n"
"buffer_load_dwordx4 v117,s[24:27],s83 offen offset:2048 sc1 lds\n"
"v_cvt_pk_bf16_f32 v130,v132,v133\n"
"v_cvt_pk_bf16_f32 v131,v134,v135\n"
"v_mfma_f32_16x16x32_bf16 v[172:175],a[108:111],v[68:71],v[172:175]\n"
"ds_write_b128 v113,v[128:131] offset:0\n"
"v_cvt_pk_bf16_f32 v137,v138,v139\n"
"v_mfma_f32_16x16x32_bf16 v[176:179],a[112:115],v[68:71],v[176:179]\n"
"buffer_load_dwordx4 v117,s[24:27],s83 offen offset:3072 sc1 lds\n"
"v_cvt_pk_bf16_f32 v138,v140,v141\n"
"v_cvt_pk_bf16_f32 v139,v142,v143\n"
"v_mfma_f32_16x16x32_bf16 v[180:183],a[116:119],v[68:71],v[180:183]\n"
"s_sub_u32 s82,s3,81920\n"
"ds_write_b128 v113,v[136:139] offset:1024\n"
"v_cvt_pk_bf16_f32 v144,v144,v145\n"
"v_mfma_f32_16x16x32_bf16 v[184:187],a[120:123],v[68:71],v[184:187]\n"
"buffer_load_dwordx4 a[32:35],v112,s[24:27],s82 offen offset:0 sc1\n"
"v_cvt_pk_bf16_f32 v145,v146,v147\n"
"v_cvt_pk_bf16_f32 v146,v148,v149\n"
"v_cvt_pk_bf16_f32 v147,v150,v151\n"
"v_mfma_f32_16x16x32_bf16 v[188:191],a[124:127],v[68:71],v[188:191]\n"
"ds_write_b128 v113,v[144:147] offset:2048\n"
"v_cvt_pk_bf16_f32 v152,v152,v153\n"
"v_mfma_f32_16x16x32_bf16 v[192:195],a[96:99],v[72:75],v[192:195]\n"
"buffer_load_dwordx4 a[36:39],v112,s[24:27],s82 offen offset:1024 sc1\n"
"v_cvt_pk_bf16_f32 v153,v154,v155\n"
"v_cvt_pk_bf16_f32 v154,v156,v157\n"
"v_cvt_pk_bf16_f32 v155,v158,v159\n"
"v_mfma_f32_16x16x32_bf16 a[136:139],a[144:147],v[72:75],a[136:139]\n"
"ds_write_b128 v113,v[152:155] offset:3072\n"
"ds_write_b64 v116,v[96:97] offset:0\n"
"v_mfma_f32_16x16x32_bf16 v[196:199],a[100:103],v[72:75],v[196:199]\n"
"buffer_load_dwordx4 a[40:43],v112,s[24:27],s82 offen offset:2048 sc1\n"
"v_accvgpr_read_b32 v101,a132\n"
"v_cvt_pk_bf16_f32 v160,v160,v161\n"
"v_mfma_f32_16x16x32_bf16 v[200:203],a[104:107],v[72:75],v[200:203]\n"
"v_cvt_pk_bf16_f32 v161,v162,v163\n"
"v_cvt_pk_bf16_f32 v162,v164,v165\n"
"v_cvt_pk_bf16_f32 v163,v166,v167\n"
"v_mfma_f32_16x16x32_bf16 v[204:207],a[108:111],v[72:75],v[204:207]\n"
"ds_write_b128 v113,v[160:163] offset:4096\n"
"v_cvt_pk_bf16_f32 v168,v168,v169\n"
"v_cvt_pk_bf16_f32 v169,v170,v171\n"
"v_mfma_f32_16x16x32_bf16 v[208:211],a[112:115],v[72:75],v[208:211]\n"
"buffer_load_dwordx4 a[44:47],v112,s[24:27],s82 offen offset:3072 sc1\n"
"v_cvt_pk_bf16_f32 v170,v172,v173\n"
"v_cvt_pk_bf16_f32 v171,v174,v175\n"
"v_mfma_f32_16x16x32_bf16 v[212:215],a[116:119],v[72:75],v[212:215]\n"
"ds_write_b128 v113,v[168:171] offset:5120\n"
"v_cvt_pk_bf16_f32 v176,v176,v177\n"
"v_cvt_pk_bf16_f32 v177,v178,v179\n"
"v_mfma_f32_16x16x32_bf16 v[216:219],a[120:123],v[72:75],v[216:219]\n"
"v_cvt_pk_bf16_f32 v178,v180,v181\n"
"v_cvt_pk_bf16_f32 v179,v182,v183\n"
"v_mfma_f32_16x16x32_bf16 v[220:223],a[124:127],v[72:75],v[220:223]\n"
"ds_write_b128 v113,v[176:179] offset:6144\n"
"v_cvt_pk_bf16_f32 v184,v184,v185\n"
"v_cvt_pk_bf16_f32 v185,v186,v187\n"
"v_cvt_pk_bf16_f32 v186,v188,v189\n"
"v_cvt_pk_bf16_f32 v187,v190,v191\n"
"v_mfma_f32_16x16x32_bf16 v[224:227],a[96:99],v[76:79],v[224:227]\n"
"ds_write_b128 v113,v[184:187] offset:7168\n"
"v_mfma_f32_16x16x32_bf16 a[140:143],a[144:147],v[76:79],a[140:143]\n"
"s_sub_u32 s82,s3,131072\n"
"ds_write_b64 v116,v[100:101] offset:512\n"
"buffer_load_dwordx4 v[128:131],v112,s[24:27],s82 offen offset:0 sc1\n"
"buffer_load_dwordx4 v[132:135],v112,s[24:27],s82 offen offset:1024 sc1\n"
"v_mfma_f32_16x16x32_bf16 v[228:231],a[100:103],v[76:79],v[228:231]\n"
"buffer_load_dwordx4 v[136:139],v112,s[24:27],s82 offen offset:2048 sc1\n"
"buffer_load_dwordx4 v[140:143],v112,s[24:27],s82 offen offset:3072 sc1\n"
"s_sub_u32 s82,s3,147456\n"
"v_mfma_f32_16x16x32_bf16 v[232:235],a[104:107],v[76:79],v[232:235]\n"
"buffer_load_dwordx4 v[144:147],v112,s[24:27],s82 offen offset:0 sc1\n"
"buffer_load_dwordx4 v[148:151],v112,s[24:27],s82 offen offset:1024 sc1\n"
"buffer_load_dwordx4 v[152:155],v112,s[24:27],s82 offen offset:2048 sc1\n"
"v_mfma_f32_16x16x32_bf16 v[236:239],a[108:111],v[76:79],v[236:239]\n"
"buffer_load_dwordx4 v[156:159],v112,s[24:27],s82 offen offset:3072 sc1\n"
"v_mfma_f32_16x16x32_bf16 v[240:243],a[112:115],v[76:79],v[240:243]\n"
"v_mfma_f32_16x16x32_bf16 v[244:247],a[116:119],v[76:79],v[244:247]\n"
"v_mfma_f32_16x16x32_bf16 v[248:251],a[120:123],v[76:79],v[248:251]\n"
"v_mfma_f32_16x16x32_bf16 v[252:255],a[124:127],v[76:79],v[252:255]\n"
".Lfk_endB:\n"
"v_lshrrev_b32 v70,4,v112\n"
"v_and_b32 v71,15,v70\n"
"v_lshrrev_b32 v70,4,v70\n"
"v_lshlrev_b32 v70,2,v70\n"
"v_sub_u32 v70,v71,v70\n"
"v_mov_b32 v71,0x3f80\n"
"v_cmp_eq_u32 vcc,0,v70\n"
"v_mov_b32 v68,0\n"
"v_mov_b32 v64,0\n"
"v_cndmask_b32 v66,0,v71,vcc\n"
"v_cmp_eq_u32 vcc,1,v70\n"
"v_mov_b32 v65,0\n"
"v_mov_b32 v72,0x3f800000\n"
"v_cndmask_b32 v66,v66,v72,vcc\n"
"v_cmp_eq_u32 vcc,2,v70\n"
"v_mov_b32 v69,0\n"
"s_nop 0\n"
"v_cndmask_b32 v67,0,v71,vcc\n"
"v_cmp_eq_u32 vcc,3,v70\n"
"s_nop 1\n"
"v_cndmask_b32 v67,v67,v72,vcc\n"
"s_sub_u32 s82,s3,163840\n"
"buffer_load_dwordx4 v[0:3],v112,s[24:27],s82 offen offset:0 sc1\n"
"buffer_load_dwordx4 v[4:7],v112,s[24:27],s82 offen offset:1024 sc1\n"
"buffer_load_dwordx4 v[8:11],v112,s[24:27],s82 offen offset:2048 sc1\n"
"buffer_load_dwordx4 v[12:15],v112,s[24:27],s82 offen offset:3072 sc1\n"
"s_sub_u32 s82,s3,180224\n"
"buffer_load_dwordx4 v[160:163],v112,s[24:27],s82 offen offset:0 sc1\n"
"buffer_load_dwordx4 v[164:167],v112,s[24:27],s82 offen offset:1024 sc1\n"
"buffer_load_dwordx4 v[168:171],v112,s[24:27],s82 offen offset:2048 sc1\n"
"buffer_load_dwordx4 v[172:175],v112,s[24:27],s82 offen offset:3072 sc1\n"
"s_sub_u32 s82,s3,196608\n"
"buffer_load_dwordx4 v[176:179],v112,s[24:27],s82 offen offset:0 sc1\n"
"buffer_load_dwordx4 v[180:183],v112,s[24:27],s82 offen offset:1024 sc1\n"
"buffer_load_dwordx4 v[184:187],v112,s[24:27],s82 offen offset:2048 sc1\n"
"buffer_load_dwordx4 v[188:191],v112,s[24:27],s82 offen offset:3072 sc1\n"
"s_waitcnt lgkmcnt(0)\n"
"s_barrier\n"
"v_xor_b32 v113,32768,v113\n"
"v_xor_b32 v116,2048,v116\n"
"ds_read_b64 v[98:99],v116 offset:0\n"
"ds_read_b128 a[64:67],v113 offset:0\n"
"ds_read_b128 a[68:71],v113 offset:1024\n"
"ds_read_b128 a[72:75],v113 offset:2048\n"
"ds_read_b128 a[76:79],v113 offset:3072\n"
"ds_read_b64 v[102:103],v116 offset:512\n"
"ds_read_b128 a[80:83],v113 offset:4096\n"
"ds_read_b128 a[84:87],v113 offset:5120\n"
"ds_read_b128 a[88:91],v113 offset:6144\n"
"ds_read_b128 a[92:95],v113 offset:7168\n"
"s_waitcnt lgkmcnt(5)\n"
"v_cmp_neq_f32 vcc,v98,v104\n"
"s_nop 0\n"
"s_cbranch_vccnz .Lfk_ms_b0\n"
".Lfk_mr_b0:\n"
"v_mfma_f32_16x16x32_bf16 v[192:195],v[66:69],a[64:67],v[192:195]\n"
"v_mfma_f32_16x16x32_bf16 v[196:199],v[64:67],a[64:67],v[196:199]\n"
"v_mfma_f32_16x16x32_bf16 v[200:203],v[66:69],a[68:71],v[200:203]\n"
"v_mfma_f32_16x16x32_bf16 v[204:207],v[64:67],a[68:71],v[204:207]\n"
"v_mfma_f32_16x16x32_bf16 v[208:211],v[66:69],a[72:75],v[208:211]\n"
"v_mfma_f32_16x16x32_bf16 v[212:215],v[64:67],a[72:75],v[212:215]\n"
"v_mfma_f32_16x16x32_bf16 v[216:219],v[66:69],a[76:79],v[216:219]\n"
"v_mfma_f32_16x16x32_bf16 v[220:223],v[64:67],a[76:79],v[220:223]\n"
"s_waitcnt lgkmcnt(0)\n"
"v_cmp_neq_f32 vcc,v102,v108\n"
"s_nop 0\n"
"s_cbranch_vccnz .Lfk_ms_b1\n"
".Lfk_mr_b1:\n"
"v_mfma_f32_16x16x32_bf16 v[224:227],v[66:69],a[80:83],v[224:227]\n"
"v_mfma_f32_16x16x32_bf16 v[228:231],v[64:67],a[80:83],v[228:231]\n"
"v_accvgpr_read_b32 v122,a136\n"
"v_add_f32 v122,v122,v99\n"
"v_mfma_f32_16x16x32_bf16 v[232:235],v[66:69],a[84:87],v[232:235]\n"
"v_rcp_f32 v122,v122\n"
"v_mfma_f32_16x16x32_bf16 v[236:239],v[64:67],a[84:87],v[236:239]\n"
"v_pk_mul_f32 v[192:193],v[192:193],v[122:123] op_sel_hi:[1,0]\n"
"v_pk_mul_f32 v[194:195],v[194:195],v[122:123] op_sel_hi:[1,0]\n"
"v_pk_mul_f32 v[196:197],v[196:197],v[122:123] op_sel_hi:[1,0]\n"
"v_pk_mul_f32 v[198:199],v[198:199],v[122:123] op_sel_hi:[1,0]\n"
"v_mfma_f32_16x16x32_bf16 v[240:243],v[66:69],a[88:91],v[240:243]\n"
"v_cvt_pk_bf16_f32 v192,v192,v193\n"
"v_cvt_pk_bf16_f32 v193,v194,v195\n"
"v_mfma_f32_16x16x32_bf16 v[244:247],v[64:67],a[88:91],v[244:247]\n"
"v_cvt_pk_bf16_f32 v194,v196,v197\n"
"v_cvt_pk_bf16_f32 v195,v198,v199\n"
"v_mfma_f32_16x16x32_bf16 v[248:251],v[66:69],a[92:95],v[248:251]\n"
"buffer_store_dwordx4 v[192:195],v112,s[24:27],s70 offen offset:0 sc1\n"
"v_pk_mul_f32 v[200:201],v[200:201],v[122:123] op_sel_hi:[1,0]\n"
"v_pk_mul_f32 v[202:203],v[202:203],v[122:123] op_sel_hi:[1,0]\n"
"v_pk_mul_f32 v[204:205],v[204:205],v[122:123] op_sel_hi:[1,0]\n"
"v_pk_mul_f32 v[206:207],v[206:207],v[122:123] op_sel_hi:[1,0]\n"
"v_mfma_f32_16x16x32_bf16 v[252:255],v[64:67],a[92:95],v[252:255]\n"
"v_cvt_pk_bf16_f32 v200,v200,v201\n"
"v_cvt_pk_bf16_f32 v201,v202,v203\n"
"v_cvt_pk_bf16_f32 v202,v204,v205\n"
"v_cvt_pk_bf16_f32 v203,v206,v207\n"
"v_pk_mul_f32 v[208:209],v[208:209],v[122:123] op_sel_hi:[1,0]\n"
"v_pk_mul_f32 v[210:211],v[210:211],v[122:123] op_sel_hi:[1,0]\n"
"v_pk_mul_f32 v[212:213],v[212:213],v[122:123] op_sel_hi:[1,0]\n"
"v_pk_mul_f32 v[214:215],v[214:215],v[122:123] op_sel_hi:[1,0]\n"
"v_cvt_pk_bf16_f32 v208,v208,v209\n"
"v_cvt_pk_bf16_f32 v209,v210,v211\n"
"v_cvt_pk_bf16_f32 v210,v212,v213\n"
"v_cvt_pk_bf16_f32 v211,v214,v215\n"
"buffer_store_dwordx4 v[200:203],v112,s[24:27],s70 offen offset:1024 sc1\n"
"buffer_store_dwordx4 v[208:211],v112,s[24:27],s70 offen offset:2048 sc1\n"
"v_pk_mul_f32 v[216:217],v[216:217],v[122:123] op_sel_hi:[1,0]\n"
"v_pk_mul_f32 v[218:219],v[218:219],v[122:123] op_sel_hi:[1,0]\n"
"v_pk_mul_f32 v[220:221],v[220:221],v[122:123] op_sel_hi:[1,0]\n"
"v_pk_mul_f32 v[222:223],v[222:223],v[122:123] op_sel_hi:[1,0]\n"
"v_cvt_pk_bf16_f32 v216,v216,v217\n"
"v_cvt_pk_bf16_f32 v217,v218,v219\n"
"v_cvt_pk_bf16_f32 v218,v220,v221\n"
"v_cvt_pk_bf16_f32 v219,v222,v223\n"
"buffer_store_dwordx4 v[216:219],v112,s[24:27],s70 offen offset:3072 sc1\n"
"s_add_u32 s70,s70,262144\n"
"s_branch .Lfk_p1\n"
".Lfk_tailB7:\n"
"s_waitcnt vmcnt(11) lgkmcnt(0)\n"
"s_barrier\n"
"v_mfma_f32_16x16x32_bf16 v[0:3],a[64:67],a[0:3],v[96:99]\n"
"ds_read_b128 a[96:99],v114 offset:24576\n"
"ds_read_b128 a[100:103],v114 offset:25600\n"
"ds_read_b128 a[104:107],v114 offset:26624\n"
"v_mfma_f32_16x16x32_bf16 v[0:3],a[68:71],a[4:7],v[0:3]\n"
"ds_read_b128 a[108:111],v114 offset:27648\n"
"ds_read_b128 a[112:115],v114 offset:28672\n"
"ds_read_b128 a[116:119],v114 offset:29696\n"
"v_mfma_f32_16x16x32_bf16 v[0:3],a[72:75],a[8:11],v[0:3]\n"
"ds_read_b128 a[120:123],v114 offset:30720\n"
"ds_read_b128 a[124:127],v114 offset:31744\n"
"v_add_f32 v16,0x80000000,v126\n"
"v_mfma_f32_16x16x32_bf16 v[0:3],a[76:79],a[12:15],v[0:3]\n"
"v_min_f32 v16,v16,v104\n"
"v_add_f32 v17,0xf149f2ca,v126\n"
"v_min_f32 v17,v17,v105\n"
"v_mfma_f32_16x16x32_bf16 v[4:7],a[92:95],a[12:15],v[96:99]\n"
"v_add_f32 v18,0xf1c9f2ca,v126\n"
"v_min_f32 v18,v18,v106\n"
"v_add_f32 v19,0xf2177617,v126\n"
"v_mfma_f32_16x16x32_bf16 v[4:7],a[88:91],a[8:11],v[4:7]\n"
"v_min_f32 v19,v19,v107\n"
"v_add_f32 v20,0xf349f2ca,v126\n"
"v_mfma_f32_16x16x32_bf16 v[4:7],a[84:87],a[4:7],v[4:7]\n"
"v_add_f32 v21,0xf35691f6,v126\n"
"v_min_f32 v21,v21,v105\n"
"v_add_f32 v22,0xf3633123,v126\n"
"v_mfma_f32_16x16x32_bf16 v[4:7],a[80:83],a[0:3],v[4:7]\n"
"s_waitcnt lgkmcnt(0)\n"
"v_min_f32 v22,v22,v106\n"
"v_add_f32 v23,0xf36fd050,v126\n"
"v_mfma_f32_16x16x32_bf16 v[192:195],a[96:99],v[72:75],v[192:195]\n"
"v_exp_f32 v56,v56\n"
"v_mfma_f32_16x16x32_bf16 a[136:139],a[144:147],v[72:75],a[136:139]\n"
"v_exp_f32 v57,v57\n"
"v_mfma_f32_16x16x32_bf16 v[196:199],a[100:103],v[72:75],v[196:199]\n"
"v_min_f32 v20,v20,v104\n"
"v_cvt_pk_bf16_f32 v92,v56,v57\n"
"v_mfma_f32_16x16x32_bf16 v[200:203],a[104:107],v[72:75],v[200:203]\n"
"v_max3_f32 v118,v0,v1,v2\n"
"v_max3_f32 v118,v118,v3,v4\n"
"v_mfma_f32_16x16x32_bf16 v[204:207],a[108:111],v[72:75],v[204:207]\n"
"v_max3_f32 v118,v118,v5,v6\n"
"v_max_f32 v118,v118,v7\n"
"v_mfma_f32_16x16x32_bf16 v[208:211],a[112:115],v[72:75],v[208:211]\n"
"v_min_f32 v23,v23,v107\n"
"v_mov_b32 v120,v118\n"
"v_mfma_f32_16x16x32_bf16 v[212:215],a[116:119],v[72:75],v[212:215]\n"
"v_exp_f32 v58,v58\n"
"v_mfma_f32_16x16x32_bf16 v[216:219],a[120:123],v[72:75],v[216:219]\n"
"v_mfma_f32_16x16x32_bf16 v[220:223],a[124:127],v[72:75],v[220:223]\n"
"v_exp_f32 v59,v59\n"
"v_mfma_f32_16x16x32_bf16 v[224:227],a[96:99],v[76:79],v[224:227]\n"
"v_cvt_pk_bf16_f32 v93,v58,v59\n"
"v_mfma_f32_16x16x32_bf16 a[140:143],a[144:147],v[76:79],a[140:143]\n"
"v_accvgpr_read_b32 v122,a136\n"
"v_mfma_f32_16x16x32_bf16 v[228:231],a[100:103],v[76:79],v[228:231]\n"
"v_rcp_f32 v122,v122\n"
"v_mfma_f32_16x16x32_bf16 v[232:235],a[104:107],v[76:79],v[232:235]\n"
"v_pk_mul_f32 v[192:193],v[192:193],v[122:123] op_sel_hi:[1,0]\n"
"v_pk_mul_f32 v[194:195],v[194:195],v[122:123] op_sel_hi:[1,0]\n"
"v_pk_mul_f32 v[196:197],v[196:197],v[122:123] op_sel_hi:[1,0]\n"
"v_pk_mul_f32 v[198:199],v[198:199],v[122:123] op_sel_hi:[1,0]\n"
"v_mfma_f32_16x16x32_bf16 v[236:239],a[108:111],v[76:79],v[236:239]\n"
"v_cvt_pk_bf16_f32 v192,v192,v193\n"
"v_cvt_pk_bf16_f32 v193,v194,v195\n"
"v_mfma_f32_16x16x32_bf16 v[240:243],a[112:115],v[76:79],v[240:243]\n"
"v_cvt_pk_bf16_f32 v194,v196,v197\n"
"v_cvt_pk_bf16_f32 v195,v198,v199\n"
"v_mfma_f32_16x16x32_bf16 v[244:247],a[116:119],v[76:79],v[244:247]\n"
"buffer_store_dwordx4 v[192:195],v112,s[24:27],s70 offen offset:0 sc1\n"
"v_pk_mul_f32 v[200:201],v[200:201],v[122:123] op_sel_hi:[1,0]\n"
"v_pk_mul_f32 v[202:203],v[202:203],v[122:123] op_sel_hi:[1,0]\n"
"v_mfma_f32_16x16x32_bf16 v[248:251],a[120:123],v[76:79],v[248:251]\n"
"v_exp_f32 v60,v60\n"
"v_cvt_pk_bf16_f32 v200,v200,v201\n"
"v_cvt_pk_bf16_f32 v201,v202,v203\n"
"v_pk_mul_f32 v[204:205],v[204:205],v[122:123] op_sel_hi:[1,0]\n"
"v_pk_mul_f32 v[206:207],v[206:207],v[122:123] op_sel_hi:[1,0]\n"
"v_mfma_f32_16x16x32_bf16 v[252:255],a[124:127],v[76:79],v[252:255]\n"
"v_cvt_pk_bf16_f32 v202,v204,v205\n"
"v_cvt_pk_bf16_f32 v203,v206,v207\n"
"v_mfma_f32_16x16x32_bf16 v[12:15],a[80:83],a[16:19],v[100:103]\n"
"buffer_store_dwordx4 v[200:203],v112,s[24:27],s70 offen offset:1024 sc1\n"
"v_pk_mul_f32 v[208:209],v[208:209],v[122:123] op_sel_hi:[1,0]\n"
"v_pk_mul_f32 v[210:211],v[210:211],v[122:123] op_sel_hi:[1,0]\n"
"v_pk_mul_f32 v[212:213],v[212:213],v[122:123] op_sel_hi:[1,0]\n"
"v_pk_mul_f32 v[214:215],v[214:215],v[122:123] op_sel_hi:[1,0]\n"
"v_mfma_f32_16x16x32_bf16 v[12:15],a[84:87],a[20:23],v[12:15]\n"
"v_exp_f32 v61,v61\n"
"v_mfma_f32_16x16x32_bf16 v[12:15],a[88:91],a[24:27],v[12:15]\n"
"v_cvt_pk_bf16_f32 v94,v60,v61\n"
"v_cvt_pk_bf16_f32 v208,v208,v209\n"
"v_mfma_f32_16x16x32_bf16 v[12:15],a[92:95],a[28:31],v[12:15]\n"
"v_cvt_pk_bf16_f32 v209,v210,v211\n"
"v_cvt_pk_bf16_f32 v210,v212,v213\n"
"v_cvt_pk_bf16_f32 v211,v214,v215\n"
"v_mfma_f32_16x16x32_bf16 v[8:11],a[76:79],a[28:31],v[100:103]\n"
"buffer_store_dwordx4 v[208:211],v112,s[24:27],s70 offen offset:2048 sc1\n"
"v_pk_mul_f32 v[216:217],v[216:217],v[122:123] op_sel_hi:[1,0]\n"
"v_pk_mul_f32 v[218:219],v[218:219],v[122:123] op_sel_hi:[1,0]\n"
"v_mfma_f32_16x16x32_bf16 v[8:11],a[72:75],a[24:27],v[8:11]\n"
"v_cvt_pk_bf16_f32 v216,v216,v217\n"
"v_cvt_pk_bf16_f32 v217,v218,v219\n"
"v_pk_mul_f32 v[220:221],v[220:221],v[122:123] op_sel_hi:[1,0]\n"
"v_pk_mul_f32 v[222:223],v[222:223],v[122:123] op_sel_hi:[1,0]\n"
"v_mfma_f32_16x16x32_bf16 v[8:11],a[68:71],a[20:23],v[8:11]\n"
"v_cvt_pk_bf16_f32 v218,v220,v221\n"
"v_cvt_pk_bf16_f32 v219,v222,v223\n"
"v_mfma_f32_16x16x32_bf16 v[8:11],a[64:67],a[16:19],v[8:11]\n"
"buffer_store_dwordx4 v[216:219],v112,s[24:27],s70 offen offset:3072 sc1\n"
"v_mfma_f32_16x16x32_bf16 v[24:27],a[64:67],a[48:51],v[108:111]\n"
"s_add_u32 s70,s70,262144\n"
"v_exp_f32 v62,v62\n"
"v_mfma_f32_16x16x32_bf16 v[24:27],a[68:71],a[52:55],v[24:27]\n"
"v_exp_f32 v63,v63\n"
"v_mfma_f32_16x16x32_bf16 v[24:27],a[72:75],a[56:59],v[24:27]\n"
"s_nop 0\n"
"v_max3_f32 v119,v8,v9,v10\n"
"v_mfma_f32_16x16x32_bf16 v[24:27],a[76:79],a[60:63],v[24:27]\n"
"v_max3_f32 v119,v119,v11,v12\n"
"v_max3_f32 v119,v119,v13,v14\n"
"v_mfma_f32_16x16x32_bf16 v[28:31],a[92:95],a[60:63],v[108:111]\n"
"v_max_f32 v119,v119,v15\n"
"v_mfma_f32_16x16x32_bf16 v[28:31],a[88:91],a[56:59],v[28:31]\n"
"v_min_f32 v120,v120,v119\n"
"v_max_f32 v118,v118,v119\n"
"v_mfma_f32_16x16x32_bf16 v[28:31],a[84:87],a[52:55],v[28:31]\n"
"v_mfma_f32_16x16x32_bf16 v[28:31],a[80:83],a[48:51],v[28:31]\n"
"v_cvt_pk_bf16_f32 v95,v62,v63\n"
"v_mfma_f32_16x16x32_bf16 v[20:23],a[80:83],a[32:35],v[20:23]\n"
"v_mfma_f32_16x16x32_bf16 v[20:23],a[84:87],a[36:39],v[20:23]\n"
"v_mfma_f32_16x16x32_bf16 v[20:23],a[88:91],a[40:43],v[20:23]\n"
"v_max3_f32 v119,v24,v25,v26\n"
"v_mfma_f32_16x16x32_bf16 v[20:23],a[92:95],a[44:47],v[20:23]\n"
"s_nop 1\n"
"v_max3_f32 v119,v119,v27,v28\n"
"v_mfma_f32_16x16x32_bf16 v[16:19],a[76:79],a[44:47],v[16:19]\n"
"v_max3_f32 v119,v119,v29,v30\n"
"v_max_f32 v119,v119,v31\n"
"v_mfma_f32_16x16x32_bf16 v[16:19],a[72:75],a[40:43],v[16:19]\n"
"v_mfma_f32_16x16x32_bf16 v[16:19],a[68:71],a[36:39],v[16:19]\n"
"v_mfma_f32_16x16x32_bf16 v[16:19],a[64:67],a[32:35],v[16:19]\n"
"ds_read_b128 a[64:67],v114 offset:57344\n"
"ds_read_b128 a[68:71],v114 offset:58368\n"
"ds_read_b128 a[72:75],v114 offset:59392\n"
"v_max_f32 v118,v118,v119\n"
"v_min_f32 v120,v120,v119\n"
"v_cmp_gt_f32 s[96:97],s16,v120\n"
"s_nop 1\n"
"v_max3_f32 v119,v16,v17,v18\n"
"v_max3_f32 v119,v119,v19,v20\n"
"v_max3_f32 v119,v119,v21,v22\n"
"v_max_f32 v119,v119,v23\n"
"v_cmp_gt_f32 s[28:29],s16,v119\n"
"v_max_f32 v118,v118,v119\n"
"v_cmp_lt_f32 vcc,s93,v118\n"
"ds_read_b128 a[76:79],v114 offset:60416\n"
"ds_read_b128 a[80:83],v114 offset:61440\n"
"ds_read_b128 a[84:87],v114 offset:62464\n"
"ds_read_b128 a[88:91],v114 offset:63488\n"
"ds_read_b128 a[92:95],v114 offset:64512\n"
"v_mov_b32 v114,v112\n"
"s_sub_u32 s82,s3,32768\n"
"buffer_load_dwordx4 a[0:3],v112,s[24:27],s82 offen offset:0 sc1\n"
"buffer_load_dwordx4 a[4:7],v112,s[24:27],s82 offen offset:1024 sc1\n"
"buffer_load_dwordx4 a[8:11],v112,s[24:27],s82 offen offset:2048 sc1\n"
"buffer_load_dwordx4 a[12:15],v112,s[24:27],s82 offen offset:3072 sc1\n"
"s_sub_u32 s82,s3,49152\n"
"buffer_load_dwordx4 a[16:19],v112,s[24:27],s82 offen offset:0 sc1\n"
"buffer_load_dwordx4 a[20:23],v112,s[24:27],s82 offen offset:1024 sc1\n"
"buffer_load_dwordx4 a[24:27],v112,s[24:27],s82 offen offset:2048 sc1\n"
"buffer_load_dwordx4 a[28:31],v112,s[24:27],s82 offen offset:3072 sc1\n"
"s_and_b32 s28,s28,0xffff\n"
"s_or_b32 s96,s96,s28\n"
"s_or_b64 s[96:97],s[96:97],vcc\n"
"s_cbranch_scc1 .Lfk_cs_c3\n"
".Lfk_cr_c3:\n"
"s_waitcnt vmcnt(13) lgkmcnt(0)\n"
"s_barrier\n"
"v_mfma_f32_16x16x32_bf16 v[224:227],a[64:67],v[92:95],v[224:227]\n"
"ds_read_b128 a[96:99],v114 offset:40960\n"
"v_exp_f32 v0,v0\n"
"v_mfma_f32_16x16x32_bf16 a[140:143],a[144:147],v[92:95],a[140:143]\n"
"ds_read_b128 a[100:103],v114 offset:41984\n"
"v_exp_f32 v1,v1\n"
"v_mfma_f32_16x16x32_bf16 v[228:231],a[68:71],v[92:95],v[228:231]\n"
"ds_read_b128 a[104:107],v114 offset:43008\n"
"v_exp_f32 v2,v2\n"
"v_mfma_f32_16x16x32_bf16 v[232:235],a[72:75],v[92:95],v[232:235]\n"
"ds_read_b128 a[108:111],v114 offset:44032\n"
"v_exp_f32 v3,v3\n"
"v_mfma_f32_16x16x32_bf16 v[236:239],a[76:79],v[92:95],v[236:239]\n"
"ds_read_b128 a[112:115],v114 offset:45056\n"
"v_exp_f32 v4,v4\n"
"v_mfma_f32_16x16x32_bf16 v[240:243],a[80:83],v[92:95],v[240:243]\n"
"ds_read_b128 a[116:119],v114 offset:46080\n"
"v_exp_f32 v5,v5\n"
"v_mfma_f32_16x16x32_bf16 v[244:247],a[84:87],v[92:95],v[244:247]\n"
"ds_read_b128 a[120:123],v114 offset:47104\n"
"v_exp_f32 v6,v6\n"
"v_mfma_f32_16x16x32_bf16 v[248:251],a[88:91],v[92:95],v[248:251]\n"
"v_exp_f32 v7,v7\n"
"v_mfma_f32_16x16x32_bf16 v[252:255],a[92:95],v[92:95],v[252:255]\n"
"v_exp_f32 v8,v8\n"
"v_accvgpr_read_b32 v119,a150\n"
"v_accvgpr_read_b32 v120,a151\n"
"v_xor_b32 v119,s80,v119\n"
"v_xor_b32 v120,s81,v120\n"
"v_or_b32 v119,v119,v120\n"
"v_cmp_ne_u32 s[14:15],0,v119\n"
"s_add_u32 s18,s8,1032192\n"
"s_cmp_lg_u32 s14,0\n"
"s_cselect_b32 s18,2146435072,s18\n"
"s_add_u32 m0,s30,0\n"
"ds_read_b128 a[124:127],v114 offset:48128\n"
"buffer_load_dwordx4 v112,s[24:27],s18 offen offset:0 sc1 lds\n"
"buffer_load_dwordx4 v112,s[24:27],s18 offen offset:1024 sc1 lds\n"
"buffer_load_dwordx4 v112,s[24:27],s18 offen offset:2048 sc1 lds\n"
"buffer_load_dwordx4 v112,s[24:27],s18 offen offset:3072 sc1 lds\n"
"s_add_u32 m0,m0,4096\n"
"s_nop 0\n"
"buffer_load_dwordx4 v117,s[24:27],s18 offen offset:0 sc1 lds\n"
"buffer_load_dwordx4 v117,s[24:27],s18 offen offset:1024 sc1 lds\n"
"buffer_load_dwordx4 v117,s[24:27],s18 offen offset:2048 sc1 lds\n"
"buffer_load_dwordx4 v117,s[24:27],s18 offen offset:3072 sc1 lds\n"
"s_sub_u32 s82,s3,65536\n"
"buffer_load_dwordx4 v[32:35],v112,s[24:27],s82 offen offset:0 sc1\n"
"buffer_load_dwordx4 v[36:39],v112,s[24:27],s82 offen offset:1024 sc1\n"
"buffer_load_dwordx4 v[40:43],v112,s[24:27],s82 offen offset:2048 sc1\n"
"buffer_load_dwordx4 v[44:47],v112,s[24:27],s82 offen offset:3072 sc1\n"
"v_exp_f32 v9,v9\n"
"v_cvt_pk_bf16_f32 v64,v0,v1\n"
"v_exp_f32 v10,v10\n"
"v_cvt_pk_bf16_f32 v65,v2,v3\n"
"v_exp_f32 v11,v11\n"
"v_cvt_pk_bf16_f32 v66,v4,v5\n"
"v_exp_f32 v12,v12\n"
"v_cvt_pk_bf16_f32 v67,v6,v7\n"
"v_exp_f32 v13,v13\n"
"v_cvt_pk_bf16_f32 v68,v8,v9\n"
"v_exp_f32 v14,v14\n"
"v_cvt_pk_bf16_f32 v69,v10,v11\n"
"v_exp_f32 v15,v15\n"
"v_cvt_pk_bf16_f32 v70,v12,v13\n"
"v_exp_f32 v16,v16\n"
"v_cvt_pk_bf16_f32 v71,v14,v15\n"
"v_exp_f32 v17,v17\n"
"v_exp_f32 v18,v18\n"
"v_cvt_pk_bf16_f32 v72,v16,v17\n"
"v_exp_f32 v19,v19\n"
"v_exp_f32 v20,v20\n"
"v_cvt_pk_bf16_f32 v73,v18,v19\n"
"v_exp_f32 v21,v21\n"
"v_exp_f32 v22,v22\n"
"v_cvt_pk_bf16_f32 v74,v20,v21\n"
"v_exp_f32 v23,v23\n"
"v_exp_f32 v24,v24\n"
"v_cvt_pk_bf16_f32 v75,v22,v23\n"
"v_exp_f32 v25,v25\n"
"v_exp_f32 v26,v26\n"
"v_cvt_pk_bf16_f32 v76,v24,v25\n"
"v_exp_f32 v27,v27\n"
"v_exp_f32 v28,v28\n"
"v_cvt_pk_bf16_f32 v77,v26,v27\n"
"v_exp_f32 v29,v29\n"
"v_exp_f32 v30,v30\n"
"v_cvt_pk_bf16_f32 v78,v28,v29\n"
"v_exp_f32 v31,v31\n"
"s_nop 0\n"
"v_cvt_pk_bf16_f32 v79,v30,v31\n"
"v_accvgpr_read_b32 v122,a140\n"
"v_rcp_f32 v122,v122\n"
"s_nop 0\n"
"v_pk_mul_f32 v[224:225],v[224:225],v[122:123] op_sel_hi:[1,0]\n"
"v_pk_mul_f32 v[226:227],v[226:227],v[122:123] op_sel_hi:[1,0]\n"
"v_pk_mul_f32 v[228:229],v[228:229],v[122:123] op_sel_hi:[1,0]\n"
"v_pk_mul_f32 v[230:231],v[230:231],v[122:123] op_sel_hi:[1,0]\n"
"v_cvt_pk_bf16_f32 v224,v224,v225\n"
"v_cvt_pk_bf16_f32 v225,v226,v227\n"
"v_cvt_pk_bf16_f32 v226,v228,v229\n"
"v_cvt_pk_bf16_f32 v227,v230,v231\n"
"buffer_store_dwordx4 v[224:227],v112,s[24:27],s70 offen offset:0 sc1\n"
"v_pk_mul_f32 v[232:233],v[232:233],v[122:123] op_sel_hi:[1,0]\n"
"v_pk_mul_f32 v[234:235],v[234:235],v[122:123] op_sel_hi:[1,0]\n"
"v_pk_mul_f32 v[236:237],v[236:237],v[122:123] op_sel_hi:[1,0]\n"
"v_pk_mul_f32 v[238:239],v[238:239],v[122:123] op_sel_hi:[1,0]\n"
"v_cvt_pk_bf16_f32 v232,v232,v233\n"
"v_cvt_pk_bf16_f32 v233,v234,v235\n"
"v_cvt_pk_bf16_f32 v234,v236,v237\n"
"v_cvt_pk_bf16_f32 v235,v238,v239\n"
"buffer_store_dwordx4 v[232:235],v112,s[24:27],s70 offen offset:1024 sc1\n"
"v_pk_mul_f32 v[240:241],v[240:241],v[122:123] op_sel_hi:[1,0]\n"
"v_pk_mul_f32 v[242:243],v[242:243],v[122:123] op_sel_hi:[1,0]\n"
"v_pk_mul_f32 v[244:245],v[244:245],v[122:123] op_sel_hi:[1,0]\n"
"v_pk_mul_f32 v[246:247],v[246:247],v[122:123] op_sel_hi:[1,0]\n"
"v_cvt_pk_bf16_f32 v240,v240,v241\n"
"v_cvt_pk_bf16_f32 v241,v242,v243\n"
"v_cvt_pk_bf16_f32 v242,v244,v245\n"
"v_cvt_pk_bf16_f32 v243,v246,v247\n"
"buffer_store_dwordx4 v[240:243],v112,s[24:27],s70 offen offset:2048 sc1\n"
"v_pk_mul_f32 v[248:249],v[248:249],v[122:123] op_sel_hi:[1,0]\n"
"v_pk_mul_f32 v[250:251],v[250:251],v[122:123] op_sel_hi:[1,0]\n"
"v_pk_mul_f32 v[252:253],v[252:253],v[122:123] op_sel_hi:[1,0]\n"
"v_pk_mul_f32 v[254:255],v[254:255],v[122:123] op_sel_hi:[1,0]\n"
"v_cvt_pk_bf16_f32 v248,v248,v249\n"
"v_cvt_pk_bf16_f32 v249,v250,v251\n"
"v_cvt_pk_bf16_f32 v250,v252,v253\n"
"v_cvt_pk_bf16_f32 v251,v254,v255\n"
"buffer_store_dwordx4 v[248:251],v112,s[24:27],s70 offen offset:3072 sc1\n"
"s_add_u32 s70,s70,262144\n"
"s_mov_b32 s70,s68\n"
"s_load_dwordx2 s[36:37],s[0:1],0\n"
"s_load_dwordx2 s[44:45],s[0:1],80\n"
"s_load_dwordx2 s[48:49],s[0:1],88\n"
"s_lshl_b32 s82,s78,2\n"
"s_add_u32 s82,s82,65536\n"
"v_add_u32 v113,s82,v112\n"
"s_lshr_b32 s82,s78,2\n"
"s_add_u32 s82,s82,122880\n"
"v_lshrrev_b32 v115,1,v112\n"
"v_add_u32 v116,s82,v115\n"
"s_waitcnt lgkmcnt(0)\n"
"s_barrier\n"
"v_mfma_f32_16x16x32_bf16 v[128:131],a[96:99],v[64:67],0\n"
"s_add_u32 s83,s18,2097152\n"
"s_add_u32 m0,s30,32768\n"
"v_mfma_f32_16x16x32_bf16 a[128:131],a[144:147],v[64:67],0\n"
"buffer_load_dwordx4 v112,s[24:27],s83 offen offset:0 sc1 lds\n"
"v_mfma_f32_16x16x32_bf16 v[132:135],a[100:103],v[64:67],0\n"
"buffer_load_dwordx4 v112,s[24:27],s83 offen offset:1024 sc1 lds\n"
"v_mfma_f32_16x16x32_bf16 v[136:139],a[104:107],v[64:67],0\n"
"buffer_load_dwordx4 v112,s[24:27],s83 offen offset:2048 sc1 lds\n"
"v_mfma_f32_16x16x32_bf16 v[140:143],a[108:111],v[64:67],0\n"
"v_mfma_f32_16x16x32_bf16 v[144:147],a[112:115],v[64:67],0\n"
"buffer_load_dwordx4 v112,s[24:27],s83 offen offset:3072 sc1 lds\n"
"v_mfma_f32_16x16x32_bf16 v[148:151],a[116:119],v[64:67],0\n"
"s_add_u32 m0,m0,4096\n"
"v_mfma_f32_16x16x32_bf16 v[152:155],a[120:123],v[64:67],0\n"
"v_mfma_f32_16x16x32_bf16 v[156:159],a[124:127],v[64:67],0\n"
"buffer_load_dwordx4 v117,s[24:27],s83 offen offset:0 sc1 lds\n"
"v_mfma_f32_16x16x32_bf16 v[160:163],a[96:99],v[68:71],0\n"
"v_accvgpr_read_b32 v97,a128\n"
"v_cvt_pk_bf16_f32 v136,v136,v137\n"
"v_mfma_f32_16x16x32_bf16 a[132:135],a[144:147],v[68:71],0\n"
"buffer_load_dwordx4 v117,s[24:27],s83 offen offset:1024 sc1 lds\n"
"v_mfma_f32_16x16x32_bf16 v[164:167],a[100:103],v[68:71],0\n"
"v_cvt_pk_bf16_f32 v128,v128,v129\n"
"v_cvt_pk_bf16_f32 v129,v130,v131\n"
"v_mfma_f32_16x16x32_bf16 v[168:171],a[104:107],v[68:71],0\n"
"buffer_load_dwordx4 v117,s[24:27],s83 offen offset:2048 sc1 lds\n"
"v_cvt_pk_bf16_f32 v130,v132,v133\n"
"v_cvt_pk_bf16_f32 v131,v134,v135\n"
"v_mfma_f32_16x16x32_bf16 v[172:175],a[108:111],v[68:71],0\n"
"ds_write_b128 v113,v[128:131] offset:0\n"
"v_cvt_pk_bf16_f32 v137,v138,v139\n"
"v_mfma_f32_16x16x32_bf16 v[176:179],a[112:115],v[68:71],0\n"
"buffer_load_dwordx4 v117,s[24:27],s83 offen offset:3072 sc1 lds\n"
"v_cvt_pk_bf16_f32 v138,v140,v141\n"
"v_cvt_pk_bf16_f32 v139,v142,v143\n"
"v_mfma_f32_16x16x32_bf16 v[180:183],a[116:119],v[68:71],0\n"
"s_sub_u32 s82,s3,81920\n"
"ds_write_b128 v113,v[136:139] offset:1024\n"
"v_cvt_pk_bf16_f32 v144,v144,v145\n"
"v_mfma_f32_16x16x32_bf16 v[184:187],a[120:123],v[68:71],0\n"
"buffer_load_dwordx4 a[32:35],v112,s[24:27],s82 offen offset:0 sc1\n"
"v_cvt_pk_bf16_f32 v145,v146,v147\n"
"v_cvt_pk_bf16_f32 v146,v148,v149\n"
"v_cvt_pk_bf16_f32 v147,v150,v151\n"
"v_mfma_f32_16x16x32_bf16 v[188:191],a[124:127],v[68:71],0\n"
"ds_write_b128 v113,v[144:147] offset:2048\n"
"v_cvt_pk_bf16_f32 v152,v152,v153\n"
"v_mfma_f32_16x16x32_bf16 v[192:195],a[96:99],v[72:75],0\n"
"buffer_load_dwordx4 a[36:39],v112,s[24:27],s82 offen offset:1024 sc1\n"
"v_cvt_pk_bf16_f32 v153,v154,v155\n"
"v_cvt_pk_bf16_f32 v154,v156,v157\n"
"v_cvt_pk_bf16_f32 v155,v158,v159\n"
"v_mfma_f32_16x16x32_bf16 a[136:139],a[144:147],v[72:75],0\n"
"ds_write_b128 v113,v[152:155] offset:3072\n"
"ds_write_b64 v116,v[96:97] offset:0\n"
"v_mfma_f32_16x16x32_bf16 v[196:199],a[100:103],v[72:75],0\n"
"buffer_load_dwordx4 a[40:43],v112,s[24:27],s82 offen offset:2048 sc1\n"
"v_accvgpr_read_b32 v101,a132\n"
"v_cvt_pk_bf16_f32 v160,v160,v161\n"
"v_mfma_f32_16x16x32_bf16 v[200:203],a[104:107],v[72:75],0\n"
"v_cvt_pk_bf16_f32 v161,v162,v163\n"
"v_cvt_pk_bf16_f32 v162,v164,v165\n"
"v_cvt_pk_bf16_f32 v163,v166,v167\n"
"v_mfma_f32_16x16x32_bf16 v[204:207],a[108:111],v[72:75],0\n"
"ds_write_b128 v113,v[160:163] offset:4096\n"
"v_cvt_pk_bf16_f32 v168,v168,v169\n"
"v_cvt_pk_bf16_f32 v169,v170,v171\n"
"v_mfma_f32_16x16x32_bf16 v[208:211],a[112:115],v[72:75],0\n"
"buffer_load_dwordx4 a[44:47],v112,s[24:27],s82 offen offset:3072 sc1\n"
"v_cvt_pk_bf16_f32 v170,v172,v173\n"
"v_cvt_pk_bf16_f32 v171,v174,v175\n"
"v_mfma_f32_16x16x32_bf16 v[212:215],a[116:119],v[72:75],0\n"
"ds_write_b128 v113,v[168:171] offset:5120\n"
"v_cvt_pk_bf16_f32 v176,v176,v177\n"
"v_cvt_pk_bf16_f32 v177,v178,v179\n"
"v_mfma_f32_16x16x32_bf16 v[216:219],a[120:123],v[72:75],0\n"
"v_cvt_pk_bf16_f32 v178,v180,v181\n"
"v_cvt_pk_bf16_f32 v179,v182,v183\n"
"v_mfma_f32_16x16x32_bf16 v[220:223],a[124:127],v[72:75],0\n"
"ds_write_b128 v113,v[176:179] offset:6144\n"
"v_cvt_pk_bf16_f32 v184,v184,v185\n"
"v_cvt_pk_bf16_f32 v185,v186,v187\n"
"v_cvt_pk_bf16_f32 v186,v188,v189\n"
"v_cvt_pk_bf16_f32 v187,v190,v191\n"
"v_mfma_f32_16x16x32_bf16 v[224:227],a[96:99],v[76:79],0\n"
"ds_write_b128 v113,v[184:187] offset:7168\n"
"v_mfma_f32_16x16x32_bf16 a[140:143],a[144:147],v[76:79],0\n"
"s_sub_u32 s82,s3,131072\n"
"ds_write_b64 v116,v[100:101] offset:512\n"
"buffer_load_dwordx4 v[128:131],v112,s[24:27],s82 offen offset:0 sc1\n"
"buffer_load_dwordx4 v[132:135],v112,s[24:27],s82 offen offset:1024 sc1\n"
"v_mfma_f32_16x16x32_bf16 v[228:231],a[100:103],v[76:79],0\n"
"buffer_load_dwordx4 v[136:139],v112,s[24:27],s82 offen offset:2048 sc1\n"
"buffer_load_dwordx4 v[140:143],v112,s[24:27],s82 offen offset:3072 sc1\n"
"s_sub_u32 s82,s3,147456\n"
"v_mfma_f32_16x16x32_bf16 v[232:235],a[104:107],v[76:79],0\n"
"buffer_load_dwordx4 v[144:147],v112,s[24:27],s82 offen offset:0 sc1\n"
"buffer_load_dwordx4 v[148:151],v112,s[24:27],s82 offen offset:1024 sc1\n"
"buffer_load_dwordx4 v[152:155],v112,s[24:27],s82 offen offset:2048 sc1\n"
"v_mfma_f32_16x16x32_bf16 v[236:239],a[108:111],v[76:79],0\n"
"buffer_load_dwordx4 v[156:159],v112,s[24:27],s82 offen offset:3072 sc1\n"
"v_mfma_f32_16x16x32_bf16 v[240:243],a[112:115],v[76:79],0\n"
"v_mfma_f32_16x16x32_bf16 v[244:247],a[116:119],v[76:79],0\n"
"v_mfma_f32_16x16x32_bf16 v[248:251],a[120:123],v[76:79],0\n"
"v_mfma_f32_16x16x32_bf16 v[252:255],a[124:127],v[76:79],0\n"
"s_waitcnt vmcnt(20)\n"
"buffer_store_dwordx2 a[154:155],off,s[24:27],s69 sc1\n"
"s_branch .Lfk_endB\n"
".Lfk_head0:\n"
"s_mov_b32 s64,s61\n"
"s_cmp_eq_u32 s52,3\n"
"s_cselect_b32 s67,172032,0\n"
"s_waitcnt lgkmcnt(0)\n"
"s_barrier\n"
"v_mfma_f32_16x16x32_bf16 v[24:27],a[64:67],a[48:51],v[108:111]\n"
"v_add_f32 v0,0x80000000,v126\n"
"v_min_f32 v0,v0,v96\n"
"v_add_f32 v1,0xf149f2ca,v126\n"
"v_min_f32 v1,v1,v97\n"
"v_mfma_f32_16x16x32_bf16 v[24:27],a[68:71],a[52:55],v[24:27]\n"
"v_add_f32 v2,0xf1c9f2ca,v126\n"
"v_min_f32 v2,v2,v98\n"
"v_add_f32 v3,0xf2177617,v126\n"
"v_min_f32 v3,v3,v99\n"
"v_mfma_f32_16x16x32_bf16 v[24:27],a[72:75],a[56:59],v[24:27]\n"
"v_add_f32 v4,0xf349f2ca,v126\n"
"v_min_f32 v4,v4,v96\n"
"v_add_f32 v5,0xf35691f6,v126\n"
"v_min_f32 v5,v5,v97\n"
"v_mfma_f32_16x16x32_bf16 v[24:27],a[76:79],a[60:63],v[24:27]\n"
"s_andn2_b32 m0,s55,16384\n"
"v_add_f32 v6,0xf3633123,v126\n"
"v_min_f32 v6,v6,v98\n"
"v_add_f32 v7,0xf36fd050,v126\n"
"v_min_f32 v7,v7,v99\n"
"v_mfma_f32_16x16x32_bf16 v[28:31],a[92:95],a[60:63],v[108:111]\n"
"s_add_u32 s54,s54,s22\n"
"buffer_load_dwordx4 v115,s[32:35],s54 offen sc1 lds\n"
"s_add_u32 m0,s67,0\n"
"v_mfma_f32_16x16x32_bf16 v[28:31],a[88:91],a[56:59],v[28:31]\n"
"buffer_load_dwordx4 v112,s[24:27],s64 offen offset:0 sc1 lds\n"
"v_cvt_pk_bf16_f32 v192,v192,v193\n"
"v_mfma_f32_16x16x32_bf16 v[28:31],a[84:87],a[52:55],v[28:31]\n"
"v_cvt_pk_bf16_f32 v193,v194,v195\n"
"v_cvt_pk_bf16_f32 v194,v196,v197\n"
"v_cvt_pk_bf16_f32 v195,v198,v199\n"
"v_mfma_f32_16x16x32_bf16 v[28:31],a[80:83],a[48:51],v[28:31]\n"
"ds_write_b128 v116,v[192:195] offset:32768\n"
"buffer_load_dwordx4 v112,s[24:27],s64 offen offset:1024 sc1 lds\n"
"v_cvt_pk_bf16_f32 v200,v200,v201\n"
"v_cvt_pk_bf16_f32 v201,v202,v203\n"
"v_mfma_f32_16x16x32_bf16 v[4:7],a[80:83],a[0:3],v[4:7]\n"
"v_cvt_pk_bf16_f32 v202,v204,v205\n"
"v_cvt_pk_bf16_f32 v203,v206,v207\n"
"v_cvt_pk_bf16_f32 v208,v208,v209\n"
"v_mfma_f32_16x16x32_bf16 v[4:7],a[84:87],a[4:7],v[4:7]\n"
"ds_write_b128 v116,v[200:203] offset:33792\n"
"ds_read_b128 a[96:99],v113 offset:16384\n"
"ds_read_b128 a[100:103],v113 offset:17408\n"
"ds_read_b128 a[104:107],v113 offset:18432\n"
"ds_read_b128 a[108:111],v113 offset:19456\n"
"v_mfma_f32_16x16x32_bf16 v[4:7],a[88:91],a[8:11],v[4:7]\n"
"ds_read_b128 a[112:115],v113 offset:20480\n"
"ds_read_b128 a[116:119],v113 offset:21504\n"
"ds_read_b128 a[120:123],v113 offset:22528\n"
"ds_read_b128 a[124:127],v113 offset:23552\n"
"v_mfma_f32_16x16x32_bf16 v[4:7],a[92:95],a[12:15],v[4:7]\n"
"buffer_load_dwordx4 v112,s[24:27],s64 offen offset:2048 sc1 lds\n"
"buffer_load_dwordx4 v112,s[24:27],s64 offen offset:3072 sc1 lds\n"
"v_mfma_f32_16x16x32_bf16 v[0:3],a[76:79],a[12:15],v[0:3]\n"
"s_add_u32 m0,s67,4096\n"
"v_max3_f32 v118,v24,v25,v26\n"
"v_max3_f32 v118,v118,v27,v28\n"
"v_max3_f32 v118,v118,v29,v30\n"
"v_mfma_f32_16x16x32_bf16 v[0:3],a[72:75],a[8:11],v[0:3]\n"
"buffer_load_dwordx4 v117,s[24:27],s64 offen offset:0 sc1 lds\n"
"v_max_f32 v118,v118,v31\n"
"v_mov_b32 v120,v118\n"
"v_mfma_f32_16x16x32_bf16 v[0:3],a[68:71],a[4:7],v[0:3]\n"
"v_cvt_pk_bf16_f32 v209,v210,v211\n"
"v_cvt_pk_bf16_f32 v210,v212,v213\n"
"v_cvt_pk_bf16_f32 v211,v214,v215\n"
"v_mfma_f32_16x16x32_bf16 v[0:3],a[64:67],a[0:3],v[0:3]\n"
"ds_write_b128 v116,v[208:211] offset:34816\n"
"buffer_load_dwordx4 v117,s[24:27],s64 offen offset:1024 sc1 lds\n"
"v_cvt_pk_bf16_f32 v216,v216,v217\n"
"v_cvt_pk_bf16_f32 v217,v218,v219\n"
"v_mfma_f32_16x16x32_bf16 v[8:11],a[64:67],a[16:19],v[100:103]\n"
"v_cvt_pk_bf16_f32 v218,v220,v221\n"
"v_cvt_pk_bf16_f32 v219,v222,v223\n"
"ds_write_b128 v116,v[216:219] offset:35840\n"
"ds_read_b128 a[32:35],v116 offset:32768\n"
"v_mfma_f32_16x16x32_bf16 v[8:11],a[68:71],a[20:23],v[8:11]\n"
"buffer_load_dwordx4 v117,s[24:27],s64 offen offset:2048 sc1 lds\n"
"ds_read_b128 a[36:39],v116 offset:33792\n"
"v_mfma_f32_16x16x32_bf16 v[8:11],a[72:75],a[24:27],v[8:11]\n"
"ds_read_b128 a[40:43],v116 offset:34816\n"
"ds_read_b128 a[44:47],v116 offset:35840\n"
"v_mfma_f32_16x16x32_bf16 v[8:11],a[76:79],a[28:31],v[8:11]\n"
"buffer_load_dwordx4 v117,s[24:27],s64 offen offset:3072 sc1 lds\n"
"v_max3_f32 v119,v0,v1,v2\n"
"v_mfma_f32_16x16x32_bf16 v[12:15],a[92:95],a[28:31],v[100:103]\n"
"v_max3_f32 v119,v119,v3,v4\n"
"v_max3_f32 v119,v119,v5,v6\n"
"v_max_f32 v119,v119,v7\n"
"v_mfma_f32_16x16x32_bf16 v[12:15],a[88:91],a[24:27],v[12:15]\n"
"v_cmp_gt_f32 s[28:29],s16,v119\n"
"v_max_f32 v118,v118,v119\n"
"v_mfma_f32_16x16x32_bf16 v[12:15],a[84:87],a[20:23],v[12:15]\n"
"v_mfma_f32_16x16x32_bf16 v[12:15],a[80:83],a[16:19],v[12:15]\n"
"s_add_u32 s64,s64,16384\n"
"s_waitcnt lgkmcnt(0)\n"
"v_mfma_f32_16x16x32_bf16 v[20:23],a[80:83],a[32:35],v[104:107]\n"
"v_mfma_f32_16x16x32_bf16 v[20:23],a[84:87],a[36:39],v[20:23]\n"
"v_max3_f32 v119,v8,v9,v10\n"
"v_mfma_f32_16x16x32_bf16 v[20:23],a[88:91],a[40:43],v[20:23]\n"
"buffer_store_dwordx4 a[224:227],v112,s[24:27],s56 offen offset:0 sc1\n"
"v_mfma_f32_16x16x32_bf16 v[20:23],a[92:95],a[44:47],v[20:23]\n"
"v_max3_f32 v119,v119,v11,v12\n"
"v_max3_f32 v119,v119,v13,v14\n"
"v_mfma_f32_16x16x32_bf16 v[16:19],a[76:79],a[44:47],v[104:107]\n"
"v_max_f32 v119,v119,v15\n"
"v_max_f32 v118,v118,v119\n"
"v_mfma_f32_16x16x32_bf16 v[16:19],a[72:75],a[40:43],v[16:19]\n"
"v_min_f32 v120,v120,v119\n"
"v_mfma_f32_16x16x32_bf16 v[16:19],a[68:71],a[36:39],v[16:19]\n"
"v_mfma_f32_16x16x32_bf16 v[16:19],a[64:67],a[32:35],v[16:19]\n"
"s_and_b32 s28,s28,0xffff\n"
"s_nop 6\n"
"v_max3_f32 v119,v16,v17,v18\n"
"v_max3_f32 v119,v119,v19,v20\n"
"v_max3_f32 v119,v119,v21,v22\n"
"v_max_f32 v119,v119,v23\n"
"v_min_f32 v120,v120,v119\n"
"v_max_f32 v118,v118,v119\n"
"v_cmp_gt_f32 s[96:97],s16,v120\n"
"v_cmp_lt_f32 vcc,s93,v118\n"
"s_or_b32 s96,s96,s28\n"
"s_or_b64 s[96:97],s[96:97],vcc\n"
"s_cbranch_scc1 .Lfk_cs_z3\n"
".Lfk_cr_z3:\n"
"s_waitcnt vmcnt(15) lgkmcnt(0)\n"
"s_barrier\n"
"v_mfma_f32_16x16x32_bf16 v[48:51],a[96:99],a[32:35],v[104:107]\n"
"v_add_f32 v40,0x80000000,v126\n"
"v_min_f32 v40,v40,v100\n"
"v_add_f32 v41,0xf149f2ca,v126\n"
"v_min_f32 v41,v41,v101\n"
"v_mfma_f32_16x16x32_bf16 v[48:51],a[100:103],a[36:39],v[48:51]\n"
"v_add_f32 v42,0xf1c9f2ca,v126\n"
"v_min_f32 v42,v42,v102\n"
"v_add_f32 v43,0xf2177617,v126\n"
"v_min_f32 v43,v43,v103\n"
"v_mfma_f32_16x16x32_bf16 v[48:51],a[104:107],a[40:43],v[48:51]\n"
"v_add_f32 v44,0xf349f2ca,v126\n"
"v_min_f32 v44,v44,v100\n"
"v_add_f32 v45,0xf35691f6,v126\n"
"v_min_f32 v45,v45,v101\n"
"v_mfma_f32_16x16x32_bf16 v[48:51],a[108:111],a[44:47],v[48:51]\n"
"v_add_f32 v46,0xf3633123,v126\n"
"v_min_f32 v46,v46,v102\n"
"v_add_f32 v47,0xf36fd050,v126\n"
"v_min_f32 v47,v47,v103\n"
"v_mfma_f32_16x16x32_bf16 v[52:55],a[124:127],a[44:47],v[104:107]\n"
"s_add_u32 m0,s55,4096\n"
"v_exp_f32 v0,v0\n"
"s_add_u32 s54,s54,s22\n"
"v_mfma_f32_16x16x32_bf16 v[52:55],a[120:123],a[40:43],v[52:55]\n"
"buffer_store_dwordx4 a[160:163],v112,s[24:27],s3 offen offset:0 sc1\n"
"buffer_load_dwordx4 v115,s[32:35],s54 offen sc1 lds\n"
"v_exp_f32 v1,v1\n"
"v_mfma_f32_16x16x32_bf16 v[52:55],a[116:119],a[36:39],v[52:55]\n"
"s_add_u32 m0,s67,16384\n"
"v_exp_f32 v2,v2\n"
"v_mfma_f32_16x16x32_bf16 v[52:55],a[112:115],a[32:35],v[52:55]\n"
"buffer_load_dwordx4 v112,s[24:27],s64 offen offset:0 sc1 lds\n"
"buffer_load_dwordx4 v112,s[24:27],s64 offen offset:1024 sc1 lds\n"
"v_exp_f32 v3,v3\n"
"v_exp_f32 v4,v4\n"
"v_mfma_f32_16x16x32_bf16 v[60:63],a[112:115],a[48:51],v[108:111]\n"
"buffer_load_dwordx4 v112,s[24:27],s64 offen offset:2048 sc1 lds\n"
"v_exp_f32 v5,v5\n"
"v_mfma_f32_16x16x32_bf16 v[60:63],a[116:119],a[52:55],v[60:63]\n"
"ds_read_b128 a[64:67],v113 offset:49152\n"
"ds_read_b128 a[68:71],v113 offset:50176\n"
"v_exp_f32 v6,v6\n"
"v_mfma_f32_16x16x32_bf16 v[60:63],a[120:123],a[56:59],v[60:63]\n"
"ds_read_b128 a[72:75],v113 offset:51200\n"
"ds_read_b128 a[76:79],v113 offset:52224\n"
"v_exp_f32 v7,v7\n"
"v_mfma_f32_16x16x32_bf16 v[60:63],a[124:127],a[60:63],v[60:63]\n"
"ds_read_b128 a[80:83],v113 offset:53248\n"
"ds_read_b128 a[84:87],v113 offset:54272\n"
"v_exp_f32 v8,v8\n"
"v_mfma_f32_16x16x32_bf16 v[56:59],a[108:111],a[60:63],v[108:111]\n"
"ds_read_b128 a[88:91],v113 offset:55296\n"
"ds_read_b128 a[92:95],v113 offset:56320\n"
"v_exp_f32 v9,v9\n"
"v_mfma_f32_16x16x32_bf16 v[56:59],a[104:107],a[56:59],v[56:59]\n"
"buffer_load_dwordx4 v112,s[24:27],s64 offen offset:3072 sc1 lds\n"
"v_mfma_f32_16x16x32_bf16 v[56:59],a[100:103],a[52:55],v[56:59]\n"
"s_add_u32 m0,s67,20480\n"
"v_exp_f32 v10,v10\n"
"v_mfma_f32_16x16x32_bf16 v[56:59],a[96:99],a[48:51],v[56:59]\n"
"buffer_load_dwordx4 v117,s[24:27],s64 offen offset:0 sc1 lds\n"
"buffer_load_dwordx4 v117,s[24:27],s64 offen offset:1024 sc1 lds\n"
"v_exp_f32 v11,v11\n"
"v_exp_f32 v12,v12\n"
"v_mfma_f32_16x16x32_bf16 v[40:43],a[96:99],a[16:19],v[40:43]\n"
"buffer_load_dwordx4 v117,s[24:27],s64 offen offset:2048 sc1 lds\n"
"v_exp_f32 v13,v13\n"
"v_mfma_f32_16x16x32_bf16 v[40:43],a[100:103],a[20:23],v[40:43]\n"
"buffer_store_dwordx4 a[164:167],v112,s[24:27],s3 offen offset:1024 sc1\n"
"v_cvt_pk_bf16_f32 v64,v0,v1\n"
"v_exp_f32 v14,v14\n"
"v_mfma_f32_16x16x32_bf16 v[40:43],a[104:107],a[24:27],v[40:43]\n"
"buffer_load_dwordx4 v117,s[24:27],s64 offen offset:3072 sc1 lds\n"
"v_mfma_f32_16x16x32_bf16 v[40:43],a[108:111],a[28:31],v[40:43]\n"
"s_add_u32 s64,s64,16384\n"
"v_exp_f32 v15,v15\n"
"v_cvt_pk_bf16_f32 v65,v2,v3\n"
"v_cvt_pk_bf16_f32 v66,v4,v5\n"
"v_exp_f32 v16,v16\n"
"v_mfma_f32_16x16x32_bf16 v[44:47],a[124:127],a[28:31],v[44:47]\n"
"buffer_store_dwordx4 a[228:231],v112,s[24:27],s56 offen offset:1024 sc1\n"
"v_cvt_pk_bf16_f32 v67,v6,v7\n"
"v_mfma_f32_16x16x32_bf16 v[44:47],a[120:123],a[24:27],v[44:47]\n"
"v_exp_f32 v17,v17\n"
"v_cvt_pk_bf16_f32 v68,v8,v9\n"
"v_cvt_pk_bf16_f32 v69,v10,v11\n"
"v_mfma_f32_16x16x32_bf16 v[44:47],a[116:119],a[20:23],v[44:47]\n"
"v_exp_f32 v18,v18\n"
"v_cvt_pk_bf16_f32 v70,v12,v13\n"
"v_cvt_pk_bf16_f32 v71,v14,v15\n"
"v_mfma_f32_16x16x32_bf16 v[44:47],a[112:115],a[16:19],v[44:47]\n"
"v_exp_f32 v19,v19\n"
"v_cvt_pk_bf16_f32 v72,v16,v17\n"
"v_max3_f32 v118,v48,v49,v50\n"
"v_exp_f32 v20,v20\n"
"v_cvt_pk_bf16_f32 v73,v18,v19\n"
"v_exp_f32 v21,v21\n"
"ds_read_b128 a[0:3],v116 offset:40960\n"
"ds_read_b128 a[4:7],v116 offset:41984\n"
"ds_read_b128 a[8:11],v116 offset:43008\n"
"ds_read_b128 a[12:15],v116 offset:44032\n"
"v_exp_f32 v22,v22\n"
"v_exp_f32 v23,v23\n"
"v_exp_f32 v24,v24\n"
"v_cvt_pk_bf16_f32 v75,v22,v23\n"
"v_exp_f32 v25,v25\n"
"v_exp_f32 v26,v26\n"
"v_cvt_pk_bf16_f32 v76,v24,v25\n"
"v_exp_f32 v27,v27\n"
"v_exp_f32 v28,v28\n"
"v_cvt_pk_bf16_f32 v77,v26,v27\n"
"v_exp_f32 v29,v29\n"
"v_exp_f32 v30,v30\n"
"v_cvt_pk_bf16_f32 v78,v28,v29\n"
"v_exp_f32 v31,v31\n"
"v_cvt_pk_bf16_f32 v74,v20,v21\n"
"v_cvt_pk_bf16_f32 v79,v30,v31\n"
"v_max3_f32 v118,v118,v51,v52\n"
"v_max3_f32 v118,v118,v53,v54\n"
"v_max3_f32 v118,v118,v55,v60\n"
"v_max3_f32 v118,v118,v61,v62\n"
"v_max3_f32 v118,v118,v63,v56\n"
"v_max3_f32 v118,v118,v57,v58\n"
"v_max3_f32 v118,v118,v59,v40\n"
"v_max3_f32 v118,v118,v41,v42\n"
"v_max3_f32 v118,v118,v43,v44\n"
"v_max3_f32 v118,v118,v45,v46\n"
"v_max_f32 v118,v118,v47\n"
"v_cmp_lt_f32 vcc,s93,v118\n"
"s_nop 0\n"
"s_cbranch_vccnz .Lfk_cs_z0\n"
".Lfk_cr_z0:\n"
"s_waitcnt vmcnt(23) lgkmcnt(0)\n"
"s_barrier\n"
"v_mfma_f32_16x16x32_bf16 v[24:27],a[64:67],a[48:51],v[108:111]\n"
"ds_read_b128 a[96:99],v114 offset:40960\n"
"ds_read_b128 a[100:103],v114 offset:41984\n"
"v_mfma_f32_16x16x32_bf16 v[24:27],a[68:71],a[52:55],v[24:27]\n"
"ds_read_b128 a[104:107],v114 offset:43008\n"
"ds_read_b128 a[108:111],v114 offset:44032\n"
"v_mfma_f32_16x16x32_bf16 v[24:27],a[72:75],a[56:59],v[24:27]\n"
"ds_read_b128 a[112:115],v114 offset:45056\n"
"ds_read_b128 a[116:119],v114 offset:46080\n"
"v_mfma_f32_16x16x32_bf16 v[24:27],a[76:79],a[60:63],v[24:27]\n"
"ds_read_b128 a[120:123],v114 offset:47104\n"
"ds_read_b128 a[124:127],v114 offset:48128\n"
"v_mfma_f32_16x16x32_bf16 v[28:31],a[92:95],a[60:63],v[108:111]\n"
"v_add_f32 v16,0x80000000,v126\n"
"v_min_f32 v16,v16,v104\n"
"v_mfma_f32_16x16x32_bf16 v[28:31],a[88:91],a[56:59],v[28:31]\n"
"v_add_f32 v17,0xf149f2ca,v126\n"
"v_min_f32 v17,v17,v105\n"
"v_mfma_f32_16x16x32_bf16 v[28:31],a[84:87],a[52:55],v[28:31]\n"
"v_add_f32 v18,0xf1c9f2ca,v126\n"
"v_min_f32 v18,v18,v106\n"
"v_mfma_f32_16x16x32_bf16 v[28:31],a[80:83],a[48:51],v[28:31]\n"
"v_add_f32 v20,0xf349f2ca,v126\n"
"v_min_f32 v20,v20,v104\n"
"v_add_f32 v21,0xf35691f6,v126\n"
"v_min_f32 v21,v21,v105\n"
"v_add_f32 v22,0xf3633123,v126\n"
"v_min_f32 v22,v22,v106\n"
"v_add_f32 v23,0xf36fd050,v126\n"
"v_min_f32 v23,v23,v107\n"
"v_add_f32 v19,0xf2177617,v126\n"
"v_min_f32 v19,v19,v107\n"
"v_mfma_f32_16x16x32_bf16 v[20:23],a[80:83],a[32:35],v[20:23]\n"
"s_add_u32 m0,s55,8192\n"
"v_exp_f32 v40,v40\n"
"v_mfma_f32_16x16x32_bf16 v[20:23],a[84:87],a[36:39],v[20:23]\n"
"s_add_u32 s54,s54,s22\n"
"v_exp_f32 v41,v41\n"
"v_mfma_f32_16x16x32_bf16 v[20:23],a[88:91],a[40:43],v[20:23]\n"
"v_exp_f32 v42,v42\n"
"v_mfma_f32_16x16x32_bf16 v[20:23],a[92:95],a[44:47],v[20:23]\n"
"v_exp_f32 v43,v43\n"
"v_mfma_f32_16x16x32_bf16 v[16:19],a[76:79],a[44:47],v[16:19]\n"
"v_exp_f32 v44,v44\n"
"v_mfma_f32_16x16x32_bf16 v[16:19],a[72:75],a[40:43],v[16:19]\n"
"v_exp_f32 v45,v45\n"
"v_mfma_f32_16x16x32_bf16 v[16:19],a[68:71],a[36:39],v[16:19]\n"
"v_exp_f32 v46,v46\n"
"v_mfma_f32_16x16x32_bf16 v[16:19],a[64:67],a[32:35],v[16:19]\n"
"s_waitcnt lgkmcnt(0)\n"
"v_exp_f32 v47,v47\n"
"v_mfma_f32_16x16x32_bf16 v[128:131],a[96:99],v[64:67],0\n"
"v_exp_f32 v48,v48\n"
"v_mfma_f32_16x16x32_bf16 v[160:163],a[96:99],v[68:71],0\n"
"v_exp_f32 v49,v49\n"
"v_mfma_f32_16x16x32_bf16 v[192:195],a[96:99],v[72:75],0\n"
"ds_read_b128 a[64:67],v113 offset:32768\n"
"ds_read_b128 a[68:71],v113 offset:33792\n"
"v_mfma_f32_16x16x32_bf16 v[224:227],a[96:99],v[76:79],0\n"
"v_exp_f32 v50,v50\n"
"v_mfma_f32_16x16x32_bf16 a[140:143],a[144:147],v[76:79],0\n"
"ds_read_b128 a[72:75],v113 offset:34816\n"
"ds_read_b128 a[76:79],v113 offset:35840\n"
"v_mfma_f32_16x16x32_bf16 v[228:231],a[100:103],v[76:79],0\n"
"v_exp_f32 v51,v51\n"
"v_mfma_f32_16x16x32_bf16 v[132:135],a[100:103],v[64:67],0\n"
"ds_read_b128 a[80:83],v113 offset:36864\n"
"ds_read_b128 a[84:87],v113 offset:37888\n"
"v_mfma_f32_16x16x32_bf16 v[164:167],a[100:103],v[68:71],0\n"
"v_exp_f32 v52,v52\n"
"v_mfma_f32_16x16x32_bf16 v[196:199],a[100:103],v[72:75],0\n"
"v_exp_f32 v53,v53\n"
"v_mfma_f32_16x16x32_bf16 a[136:139],a[144:147],v[72:75],0\n"
"ds_read_b128 a[88:91],v113 offset:38912\n"
"ds_read_b128 a[92:95],v113 offset:39936\n"
"v_mfma_f32_16x16x32_bf16 v[200:203],a[104:107],v[72:75],0\n"
"v_exp_f32 v54,v54\n"
"v_mfma_f32_16x16x32_bf16 v[232:235],a[104:107],v[76:79],0\n"
"buffer_load_dwordx4 v115,s[32:35],s54 offen sc1 lds\n"
"v_mfma_f32_16x16x32_bf16 v[136:139],a[104:107],v[64:67],0\n"
"s_add_u32 m0,s67,49152\n"
"v_exp_f32 v55,v55\n"
"v_mfma_f32_16x16x32_bf16 v[168:171],a[104:107],v[68:71],0\n"
"buffer_load_dwordx4 v112,s[24:27],s64 offen offset:0 sc1 lds\n"
"v_mfma_f32_16x16x32_bf16 a[132:135],a[144:147],v[68:71],0\n"
"v_exp_f32 v56,v56\n"
"v_mfma_f32_16x16x32_bf16 v[172:175],a[108:111],v[68:71],0\n"
"buffer_load_dwordx4 v112,s[24:27],s64 offen offset:1024 sc1 lds\n"
"v_mfma_f32_16x16x32_bf16 v[204:207],a[108:111],v[72:75],0\n"
"v_exp_f32 v57,v57\n"
"v_mfma_f32_16x16x32_bf16 v[236:239],a[108:111],v[76:79],0\n"
"buffer_load_dwordx4 v112,s[24:27],s64 offen offset:2048 sc1 lds\n"
"v_mfma_f32_16x16x32_bf16 v[140:143],a[108:111],v[64:67],0\n"
"v_exp_f32 v58,v58\n"
"v_mfma_f32_16x16x32_bf16 a[128:131],a[144:147],v[64:67],0\n"
"buffer_load_dwordx4 v112,s[24:27],s64 offen offset:3072 sc1 lds\n"
"v_mfma_f32_16x16x32_bf16 v[144:147],a[112:115],v[64:67],0\n"
"s_add_u32 m0,s67,53248\n"
"v_exp_f32 v59,v59\n"
"v_mfma_f32_16x16x32_bf16 v[176:179],a[112:115],v[68:71],0\n"
"buffer_load_dwordx4 v117,s[24:27],s64 offen offset:0 sc1 lds\n"
"v_mfma_f32_16x16x32_bf16 v[208:211],a[112:115],v[72:75],0\n"
"v_exp_f32 v60,v60\n"
"v_mfma_f32_16x16x32_bf16 v[240:243],a[112:115],v[76:79],0\n"
"buffer_load_dwordx4 v117,s[24:27],s64 offen offset:1024 sc1 lds\n"
"v_mfma_f32_16x16x32_bf16 v[244:247],a[116:119],v[76:79],0\n"
"v_exp_f32 v61,v61\n"
"v_mfma_f32_16x16x32_bf16 v[148:151],a[116:119],v[64:67],0\n"
"buffer_load_dwordx4 v117,s[24:27],s64 offen offset:2048 sc1 lds\n"
"v_mfma_f32_16x16x32_bf16 v[180:183],a[116:119],v[68:71],0\n"
"v_exp_f32 v62,v62\n"
"v_mfma_f32_16x16x32_bf16 v[212:215],a[116:119],v[72:75],0\n"
"buffer_load_dwordx4 v117,s[24:27],s64 offen offset:3072 sc1 lds\n"
"v_mfma_f32_16x16x32_bf16 v[216:219],a[120:123],v[72:75],0\n"
"s_add_u32 s64,s64,16384\n"
"v_exp_f32 v63,v63\n"
"v_mfma_f32_16x16x32_bf16 v[248:251],a[120:123],v[76:79],0\n"
"buffer_store_dwordx4 a[232:235],v112,s[24:27],s56 offen offset:2048 sc1\n"
"s_and_b32 s67,s67,8192\n"
"v_mfma_f32_16x16x32_bf16 v[152:155],a[120:123],v[64:67],0\n"
"buffer_store_dwordx4 a[168:171],v112,s[24:27],s3 offen offset:2048 sc1\n"
"buffer_store_dwordx4 a[172:175],v112,s[24:27],s3 offen offset:3072 sc1\n"
"v_mfma_f32_16x16x32_bf16 v[184:187],a[120:123],v[68:71],0\n"
"s_sub_u32 s3,s3,16384\n"
"buffer_store_dwordx4 a[176:179],v112,s[24:27],s3 offen offset:0 sc1\n"
"v_mfma_f32_16x16x32_bf16 v[188:191],a[124:127],v[68:71],0\n"
"v_cvt_pk_bf16_f32 v84,v40,v41\n"
"v_cvt_pk_bf16_f32 v85,v42,v43\n"
"v_mfma_f32_16x16x32_bf16 v[220:223],a[124:127],v[72:75],0\n"
"v_cvt_pk_bf16_f32 v86,v44,v45\n"
"v_cvt_pk_bf16_f32 v87,v46,v47\n"
"v_mfma_f32_16x16x32_bf16 v[252:255],a[124:127],v[76:79],0\n"
"v_cvt_pk_bf16_f32 v88,v48,v49\n"
"v_cvt_pk_bf16_f32 v89,v50,v51\n"
"v_mfma_f32_16x16x32_bf16 v[156:159],a[124:127],v[64:67],0\n"
"v_cvt_pk_bf16_f32 v90,v52,v53\n"
"v_cvt_pk_bf16_f32 v91,v54,v55\n"
"v_cvt_pk_bf16_f32 v92,v56,v57\n"
"ds_read_b128 a[16:19],v116 offset:8192\n"
"ds_read_b128 a[20:23],v116 offset:9216\n"
"ds_read_b128 a[24:27],v116 offset:10240\n"
"ds_read_b128 a[28:31],v116 offset:11264\n"
"v_cvt_pk_bf16_f32 v93,v58,v59\n"
"v_cvt_pk_bf16_f32 v94,v60,v61\n"
"v_cvt_pk_bf16_f32 v95,v62,v63\n"
"v_max3_f32 v118,v24,v25,v26\n"
"v_max3_f32 v118,v118,v27,v28\n"
"v_max3_f32 v118,v118,v29,v30\n"
"v_max3_f32 v118,v118,v31,v20\n"
"v_max3_f32 v118,v118,v21,v22\n"
"v_max3_f32 v118,v118,v23,v16\n"
"v_max3_f32 v118,v118,v17,v18\n"
"v_max_f32 v118,v118,v19\n"
"v_cmp_lt_f32 vcc,s93,v118\n"
"s_nop 0\n"
"s_cbranch_vccnz .Lfk_cs_z1\n"
".Lfk_cr_z1:\n"
"v_mov_b32 v113,v112\n"
"s_waitcnt vmcnt(21) lgkmcnt(0)\n"
"s_barrier\n"
"v_add_f32 v56,0x80000000,v126\n"
"v_min_f32 v56,v56,v108\n"
"v_add_f32 v57,0xf149f2ca,v126\n"
"v_min_f32 v57,v57,v109\n"
"v_add_f32 v58,0xf1c9f2ca,v126\n"
"v_min_f32 v58,v58,v110\n"
"v_add_f32 v59,0xf2177617,v126\n"
"v_min_f32 v59,v59,v111\n"
"v_add_f32 v60,0xf349f2ca,v126\n"
"v_min_f32 v60,v60,v108\n"
"v_add_f32 v61,0xf35691f6,v126\n"
"v_min_f32 v61,v61,v109\n"
"v_add_f32 v62,0xf3633123,v126\n"
"v_min_f32 v62,v62,v110\n"
"v_add_f32 v63,0xf36fd050,v126\n"
"v_min_f32 v63,v63,v111\n"
"v_exp_f32 v16,v16\n"
"v_mfma_f32_16x16x32_bf16 v[56:59],a[64:67],a[48:51],v[56:59]\n"
"s_add_u32 m0,s55,12288\n"
"ds_read_b128 a[96:99],v114 offset:8192\n"
"ds_read_b128 a[100:103],v114 offset:9216\n"
"v_exp_f32 v17,v17\n"
"v_mfma_f32_16x16x32_bf16 v[56:59],a[68:71],a[52:55],v[56:59]\n"
"s_add_u32 s54,s54,s22\n"
"ds_read_b128 a[104:107],v114 offset:10240\n"
"ds_read_b128 a[108:111],v114 offset:11264\n"
"v_mfma_f32_16x16x32_bf16 v[56:59],a[72:75],a[56:59],v[56:59]\n"
"ds_read_b128 a[112:115],v114 offset:12288\n"
"ds_read_b128 a[116:119],v114 offset:13312\n"
"ds_read_b128 a[120:123],v114 offset:14336\n"
"ds_read_b128 a[124:127],v114 offset:15360\n"
"v_exp_f32 v18,v18\n"
"v_mfma_f32_16x16x32_bf16 v[56:59],a[76:79],a[60:63],v[56:59]\n"
"buffer_load_dwordx4 v115,s[32:35],s54 offen sc1 lds\n"
"v_cvt_pk_bf16_f32 v72,v16,v17\n"
"v_mfma_f32_16x16x32_bf16 v[60:63],a[92:95],a[60:63],v[60:63]\n"
"buffer_store_dwordx4 a[180:183],v112,s[24:27],s3 offen offset:1024 sc1\n"
"ds_read_b128 a[32:35],v116 offset:24576\n"
"ds_read_b128 a[36:39],v116 offset:25600\n"
"v_exp_f32 v19,v19\n"
"v_mfma_f32_16x16x32_bf16 v[60:63],a[88:91],a[56:59],v[60:63]\n"
"s_add_u32 m0,s67,32768\n"
"ds_read_b128 a[40:43],v116 offset:26624\n"
"ds_read_b128 a[44:47],v116 offset:27648\n"
"v_mfma_f32_16x16x32_bf16 v[60:63],a[84:87],a[52:55],v[60:63]\n"
"buffer_load_dwordx4 v112,s[24:27],s64 offen offset:0 sc1 lds\n"
"v_cvt_pk_bf16_f32 v73,v18,v19\n"
"v_exp_f32 v20,v20\n"
"v_mfma_f32_16x16x32_bf16 v[60:63],a[80:83],a[48:51],v[60:63]\n"
"ds_read_b128 a[64:67],v113 offset:0\n"
"ds_read_b128 a[68:71],v113 offset:1024\n"
"s_waitcnt lgkmcnt(6)\n"
"v_mfma_f32_16x16x32_bf16 v[160:163],a[96:99],v[84:87],v[160:163]\n"
"ds_read_b128 a[72:75],v113 offset:2048\n"
"ds_read_b128 a[76:79],v113 offset:3072\n"
"ds_read_b128 a[80:83],v113 offset:4096\n"
"ds_read_b128 a[84:87],v113 offset:5120\n"
"v_exp_f32 v21,v21\n"
"v_mfma_f32_16x16x32_bf16 a[132:135],a[144:147],v[84:87],a[132:135]\n"
"ds_read_b128 a[88:91],v113 offset:6144\n"
"ds_read_b128 a[92:95],v113 offset:7168\n"
"v_exp_f32 v22,v22\n"
"v_mfma_f32_16x16x32_bf16 v[164:167],a[100:103],v[84:87],v[164:167]\n"
"buffer_load_dwordx4 v112,s[24:27],s64 offen offset:1024 sc1 lds\n"
"v_mfma_f32_16x16x32_bf16 v[168:171],a[104:107],v[84:87],v[168:171]\n"
"v_cvt_pk_bf16_f32 v74,v20,v21\n"
"v_max3_f32 v118,v56,v57,v58\n"
"v_max3_f32 v118,v118,v59,v60\n"
"v_max3_f32 v118,v118,v61,v62\n"
"v_exp_f32 v23,v23\n"
"v_mfma_f32_16x16x32_bf16 v[172:175],a[108:111],v[84:87],v[172:175]\n"
"buffer_load_dwordx4 v112,s[24:27],s64 offen offset:2048 sc1 lds\n"
"v_mfma_f32_16x16x32_bf16 v[176:179],a[112:115],v[84:87],v[176:179]\n"
"buffer_store_dwordx4 a[184:187],v112,s[24:27],s3 offen offset:2048 sc1\n"
"v_cvt_pk_bf16_f32 v75,v22,v23\n"
"v_max_f32 v118,v118,v63\n"
"v_mfma_f32_16x16x32_bf16 v[180:183],a[116:119],v[84:87],v[180:183]\n"
"ds_read_b128 a[48:51],v116 offset:57344\n"
"ds_read_b128 a[52:55],v116 offset:58368\n"
"v_mfma_f32_16x16x32_bf16 v[184:187],a[120:123],v[84:87],v[184:187]\n"
"buffer_load_dwordx4 v112,s[24:27],s64 offen offset:3072 sc1 lds\n"
"ds_read_b128 a[56:59],v116 offset:59392\n"
"ds_read_b128 a[60:63],v116 offset:60416\n"
"v_mfma_f32_16x16x32_bf16 v[188:191],a[124:127],v[84:87],v[188:191]\n"
"s_add_u32 m0,s67,36864\n"
"v_accvgpr_read_b32 v122,a128\n"
"v_rcp_f32 v122,v122\n"
"v_mfma_f32_16x16x32_bf16 v[192:195],a[96:99],v[88:91],v[192:195]\n"
"v_exp_f32 v25,v25\n"
"v_exp_f32 v24,v24\n"
"v_cmp_lt_f32 vcc,s93,v118\n"
"v_pk_mul_f32 v[128:129],v[128:129],v[122:123] op_sel_hi:[1,0]\n"
"v_pk_mul_f32 v[130:131],v[130:131],v[122:123] op_sel_hi:[1,0]\n"
"v_pk_mul_f32 v[132:133],v[132:133],v[122:123] op_sel_hi:[1,0]\n"
"v_mfma_f32_16x16x32_bf16 a[136:139],a[144:147],v[88:91],a[136:139]\n"
"buffer_load_dwordx4 v117,s[24:27],s64 offen offset:0 sc1 lds\n"
"v_mfma_f32_16x16x32_bf16 v[196:199],a[100:103],v[88:91],v[196:199]\n"
"v_exp_f32 v26,v26\n"
"v_cvt_pk_bf16_f32 v76,v24,v25\n"
"v_exp_f32 v27,v27\n"
"v_pk_mul_f32 v[134:135],v[134:135],v[122:123] op_sel_hi:[1,0]\n"
"v_cvt_pk_bf16_f32 v128,v128,v129\n"
"v_mfma_f32_16x16x32_bf16 v[200:203],a[104:107],v[88:91],v[200:203]\n"
"v_cvt_pk_bf16_f32 v129,v130,v131\n"
"v_cvt_pk_bf16_f32 v130,v132,v133\n"
"v_cvt_pk_bf16_f32 v131,v134,v135\n"
"v_mfma_f32_16x16x32_bf16 v[204:207],a[108:111],v[88:91],v[204:207]\n"
"buffer_load_dwordx4 v117,s[24:27],s64 offen offset:1024 sc1 lds\n"
"buffer_store_dwordx4 a[188:191],v112,s[24:27],s3 offen offset:3072 sc1\n"
"v_mfma_f32_16x16x32_bf16 v[208:211],a[112:115],v[88:91],v[208:211]\n"
"buffer_store_dwordx4 v[128:131],v112,s[24:27],s70 offen offset:0 sc1\n"
"v_pk_mul_f32 v[136:137],v[136:137],v[122:123] op_sel_hi:[1,0]\n"
"v_mfma_f32_16x16x32_bf16 v[212:215],a[116:119],v[88:91],v[212:215]\n"
"s_or_b32 s3,s3,0x3c000\n"
"v_exp_f32 v28,v28\n"
"v_cvt_pk_bf16_f32 v77,v26,v27\n"
"v_exp_f32 v29,v29\n"
"v_pk_mul_f32 v[138:139],v[138:139],v[122:123] op_sel_hi:[1,0]\n"
"v_pk_mul_f32 v[140:141],v[140:141],v[122:123] op_sel_hi:[1,0]\n"
"v_pk_mul_f32 v[142:143],v[142:143],v[122:123] op_sel_hi:[1,0]\n"
"v_mfma_f32_16x16x32_bf16 v[216:219],a[120:123],v[88:91],v[216:219]\n"
"buffer_load_dwordx4 v117,s[24:27],s64 offen offset:2048 sc1 lds\n"
"v_cvt_pk_bf16_f32 v78,v28,v29\n"
"v_mfma_f32_16x16x32_bf16 v[220:223],a[124:127],v[88:91],v[220:223]\n"
"v_cvt_pk_bf16_f32 v136,v136,v137\n"
"v_cvt_pk_bf16_f32 v137,v138,v139\n"
"v_mfma_f32_16x16x32_bf16 v[224:227],a[96:99],v[92:95],v[224:227]\n"
"v_cvt_pk_bf16_f32 v138,v140,v141\n"
"v_cvt_pk_bf16_f32 v139,v142,v143\n"
"buffer_store_dwordx4 v[136:139],v112,s[24:27],s70 offen offset:1024 sc1\n"
"v_pk_mul_f32 v[144:145],v[144:145],v[122:123] op_sel_hi:[1,0]\n"
"v_mfma_f32_16x16x32_bf16 a[140:143],a[144:147],v[92:95],a[140:143]\n"
"buffer_load_dwordx4 v117,s[24:27],s64 offen offset:3072 sc1 lds\n"
"v_mfma_f32_16x16x32_bf16 v[228:231],a[100:103],v[92:95],v[228:231]\n"
"v_exp_f32 v31,v31\n"
"v_exp_f32 v30,v30\n"
"s_add_u32 s64,s64,16384\n"
"v_pk_mul_f32 v[146:147],v[146:147],v[122:123] op_sel_hi:[1,0]\n"
"v_pk_mul_f32 v[148:149],v[148:149],v[122:123] op_sel_hi:[1,0]\n"
"v_mfma_f32_16x16x32_bf16 v[232:235],a[104:107],v[92:95],v[232:235]\n"
"buffer_store_dwordx4 a[236:239],v112,s[24:27],s56 offen offset:3072 sc1\n"
"v_pk_mul_f32 v[150:151],v[150:151],v[122:123] op_sel_hi:[1,0]\n"
"v_cvt_pk_bf16_f32 v144,v144,v145\n"
"v_mfma_f32_16x16x32_bf16 v[236:239],a[108:111],v[92:95],v[236:239]\n"
"v_cvt_pk_bf16_f32 v145,v146,v147\n"
"v_cvt_pk_bf16_f32 v146,v148,v149\n"
"v_cvt_pk_bf16_f32 v147,v150,v151\n"
"v_cvt_pk_bf16_f32 v79,v30,v31\n"
"v_mfma_f32_16x16x32_bf16 v[240:243],a[112:115],v[92:95],v[240:243]\n"
"buffer_store_dwordx4 v[144:147],v112,s[24:27],s70 offen offset:2048 sc1\n"
"v_pk_mul_f32 v[152:153],v[152:153],v[122:123] op_sel_hi:[1,0]\n"
"v_pk_mul_f32 v[154:155],v[154:155],v[122:123] op_sel_hi:[1,0]\n"
"v_pk_mul_f32 v[156:157],v[156:157],v[122:123] op_sel_hi:[1,0]\n"
"v_pk_mul_f32 v[158:159],v[158:159],v[122:123] op_sel_hi:[1,0]\n"
"v_cvt_pk_bf16_f32 v152,v152,v153\n"
"v_mfma_f32_16x16x32_bf16 v[244:247],a[116:119],v[92:95],v[244:247]\n"
"v_cvt_pk_bf16_f32 v153,v154,v155\n"
"v_cvt_pk_bf16_f32 v154,v156,v157\n"
"v_cvt_pk_bf16_f32 v155,v158,v159\n"
"v_mfma_f32_16x16x32_bf16 v[248:251],a[120:123],v[92:95],v[248:251]\n"
"buffer_store_dwordx4 v[152:155],v112,s[24:27],s70 offen offset:3072 sc1\n"
"v_accvgpr_read_b32 v122,a132\n"
"v_rcp_f32 v122,v122\n"
"s_add_u32 s70,s70,262144\n"
"v_pk_mul_f32 v[160:161],v[160:161],v[122:123] op_sel_hi:[1,0]\n"
"v_mfma_f32_16x16x32_bf16 v[252:255],a[124:127],v[92:95],v[252:255]\n"
"v_cvt_pk_bf16_f32 v160,v160,v161\n"
"v_pk_mul_f32 v[162:163],v[162:163],v[122:123] op_sel_hi:[1,0]\n"
"v_pk_mul_f32 v[164:165],v[164:165],v[122:123] op_sel_hi:[1,0]\n"
"v_pk_mul_f32 v[166:167],v[166:167],v[122:123] op_sel_hi:[1,0]\n"
"v_cvt_pk_bf16_f32 v161,v162,v163\n"
"v_cvt_pk_bf16_f32 v162,v164,v165\n"
"v_cvt_pk_bf16_f32 v163,v166,v167\n"
"v_pk_mul_f32 v[168:169],v[168:169],v[122:123] op_sel_hi:[1,0]\n"
"v_pk_mul_f32 v[170:171],v[170:171],v[122:123] op_sel_hi:[1,0]\n"
"v_pk_mul_f32 v[172:173],v[172:173],v[122:123] op_sel_hi:[1,0]\n"
"v_pk_mul_f32 v[174:175],v[174:175],v[122:123] op_sel_hi:[1,0]\n"
"v_cvt_pk_bf16_f32 v168,v168,v169\n"
"v_cvt_pk_bf16_f32 v169,v170,v171\n"
"v_cvt_pk_bf16_f32 v170,v172,v173\n"
"v_cvt_pk_bf16_f32 v171,v174,v175\n"
"buffer_store_dwordx4 v[160:163],v112,s[24:27],s70 offen offset:0 sc1\n"
"buffer_store_dwordx4 v[168:171],v112,s[24:27],s70 offen offset:1024 sc1\n"
"v_pk_mul_f32 v[176:177],v[176:177],v[122:123] op_sel_hi:[1,0]\n"
"v_pk_mul_f32 v[178:179],v[178:179],v[122:123] op_sel_hi:[1,0]\n"
"v_pk_mul_f32 v[180:181],v[180:181],v[122:123] op_sel_hi:[1,0]\n"
"v_pk_mul_f32 v[182:183],v[182:183],v[122:123] op_sel_hi:[1,0]\n"
"v_cvt_pk_bf16_f32 v176,v176,v177\n"
"v_cvt_pk_bf16_f32 v177,v178,v179\n"
"v_cvt_pk_bf16_f32 v178,v180,v181\n"
"v_cvt_pk_bf16_f32 v179,v182,v183\n"
"buffer_store_dwordx4 v[176:179],v112,s[24:27],s70 offen offset:2048 sc1\n"
"v_pk_mul_f32 v[184:185],v[184:185],v[122:123] op_sel_hi:[1,0]\n"
"v_pk_mul_f32 v[186:187],v[186:187],v[122:123] op_sel_hi:[1,0]\n"
"v_pk_mul_f32 v[188:189],v[188:189],v[122:123] op_sel_hi:[1,0]\n"
"v_pk_mul_f32 v[190:191],v[190:191],v[122:123] op_sel_hi:[1,0]\n"
"v_cvt_pk_bf16_f32 v184,v184,v185\n"
"v_cvt_pk_bf16_f32 v185,v186,v187\n"
"v_cvt_pk_bf16_f32 v186,v188,v189\n"
"v_cvt_pk_bf16_f32 v187,v190,v191\n"
"buffer_store_dwordx4 v[184:187],v112,s[24:27],s70 offen offset:3072 sc1\n"
"s_add_u32 s70,s70,262144\n"
"s_cbranch_vccnz .Lfk_cs_z2\n"
".Lfk_cr_z2:\n"
"s_waitcnt vmcnt(35) lgkmcnt(0)\n"
"s_barrier\n"
"s_add_u32 s89,s89,1\n"
"s_branch .Lfk_winB\n"
".Lfk_ev:\n"
"s_xor_b32 s55,s55,16384\n"
"s_lshr_b32 s91,s78,2\n"
"s_add_u32 s91,s91,131072\n"
"v_lshrrev_b32 v121,1,v112\n"
"v_add_u32 v121,s91,v121\n"
"ds_read_b64_tr_b16 a[160:161],v121 offset:0\n"
"ds_read_b64_tr_b16 a[162:163],v121 offset:512\n"
"s_add_u32 s56,s56,4096\n"
"s_nop 0\n"
"s_swappc_b64 s[20:21],s[94:95]\n"
"s_xor_b32 s55,s55,16384\n"
"s_lshr_b32 s91,s78,2\n"
"s_add_u32 s91,s91,131072\n"
"v_lshrrev_b32 v121,1,v112\n"
"v_add_u32 v121,s91,v121\n"
"ds_read_b64_tr_b16 a[164:165],v121 offset:4096\n"
"ds_read_b64_tr_b16 a[166:167],v121 offset:4608\n"
"ds_read_b64_tr_b16 a[168:169],v121 offset:8192\n"
"ds_read_b64_tr_b16 a[170:171],v121 offset:8704\n"
"ds_read_b64_tr_b16 a[172:173],v121 offset:12288\n"
"ds_read_b64_tr_b16 a[174:175],v121 offset:12800\n"
"ds_read_b64_tr_b16 a[176:177],v121 offset:0\n"
"ds_read_b64_tr_b16 a[178:179],v121 offset:512\n"
"buffer_store_dwordx2 a[154:155],off,s[24:27],s2 sc1\n"
"s_and_b32 s2,s2,0xfffffc00\n"
"s_mov_b32 s56,s10\n"
"v_accvgpr_write_b32 a240,0\n"
"v_accvgpr_write_b32 a241,0\n"
"v_accvgpr_write_b32 a242,0\n"
"v_accvgpr_write_b32 a243,0\n"
"v_accvgpr_write_b32 a244,0\n"
"v_accvgpr_write_b32 a245,0\n"
"v_accvgpr_write_b32 a246,0\n"
"v_accvgpr_write_b32 a247,0\n"
"v_accvgpr_write_b32 a248,0\n"
"v_accvgpr_write_b32 a249,0\n"
"v_accvgpr_write_b32 a250,0\n"
"v_accvgpr_write_b32 a251,0\n"
"v_accvgpr_write_b32 a252,0\n"
"v_accvgpr_write_b32 a253,0\n"
"v_accvgpr_write_b32 a254,0\n"
"v_accvgpr_write_b32 a255,0\n"
"s_mov_b64 s[32:33],s[36:37]\n"
"s_mov_b32 s54,-229376\n"
"s_nop 0\n"
"s_swappc_b64 s[20:21],s[94:95]\n"
"s_xor_b32 s55,s55,16384\n"
"s_add_u32 s56,s56,131072\n"
"s_lshr_b32 s91,s78,2\n"
"s_add_u32 s91,s91,131072\n"
"v_lshrrev_b32 v121,1,v112\n"
"v_add_u32 v121,s91,v121\n"
"ds_read_b64_tr_b16 a[180:181],v121 offset:20480\n"
"ds_read_b64_tr_b16 a[182:183],v121 offset:20992\n"
"ds_read_b64_tr_b16 a[184:185],v121 offset:24576\n"
"ds_read_b64_tr_b16 a[186:187],v121 offset:25088\n"
"ds_read_b64_tr_b16 a[188:189],v121 offset:28672\n"
"ds_read_b64_tr_b16 a[190:191],v121 offset:29184\n"
"ds_read_b64_tr_b16 a[192:193],v121 offset:0\n"
"ds_read_b64_tr_b16 a[194:195],v121 offset:512\n"
"buffer_load_dwordx2 a[150:151],v112,s[24:27],s2 offen sc1\n"
"s_add_u32 s82,s59,8192\n"
"buffer_store_dwordx2 a[154:155],off,s[24:27],s82 sc1\n"
"s_nop 0\n"
"s_swappc_b64 s[20:21],s[94:95]\n"
"s_xor_b32 s55,s55,16384\n"
"s_add_u32 s56,s56,131072\n"
"s_lshr_b32 s91,s78,2\n"
"s_add_u32 s91,s91,131072\n"
"v_lshrrev_b32 v121,1,v112\n"
"v_add_u32 v121,s91,v121\n"
"ds_read_b64_tr_b16 a[196:197],v121 offset:4096\n"
"ds_read_b64_tr_b16 a[198:199],v121 offset:4608\n"
"ds_read_b64_tr_b16 a[200:201],v121 offset:8192\n"
"ds_read_b64_tr_b16 a[202:203],v121 offset:8704\n"
"ds_read_b64_tr_b16 a[204:205],v121 offset:12288\n"
"ds_read_b64_tr_b16 a[206:207],v121 offset:12800\n"
"ds_read_b64_tr_b16 a[208:209],v121 offset:0\n"
"ds_read_b64_tr_b16 a[210:211],v121 offset:512\n"
"s_mov_b32 s84,0x40000\n"
".Lfk_w_chk:\n"
"v_accvgpr_read_b32 v119,a150\n"
"v_accvgpr_read_b32 v120,a151\n"
"v_xor_b32 v119,s80,v119\n"
"v_xor_b32 v120,s81,v120\n"
"v_or_b32 v119,v119,v120\n"
"v_cmp_ne_u32 vcc,0,v119\n"
"s_nop 0\n"
"s_cbranch_vccz .Lfk_w_ok\n"
"s_sub_u32 s84,s84,1\n"
"s_cbranch_scc1 .Lfk_w_ok\n"
"s_sleep 4\n"
"buffer_load_dwordx2 a[150:151],v112,s[24:27],s2 offen sc1\n"
"s_waitcnt vmcnt(0)\n"
"s_branch .Lfk_w_chk\n"
".Lfk_w_ok:\n"
"v_lshrrev_b32 v121,1,v112\n"
"v_and_b32 v121,0xf8,v121\n"
"s_add_u32 s82,s63,8192\n"
"buffer_load_dwordx2 a[150:151],v121,s[24:27],s82 offen sc1\n"
"s_mov_b64 s[32:33],s[24:25]\n"
"s_mov_b32 s34,s26\n"
"s_mov_b32 s22,1024\n"
"v_mov_b32 v115,v112\n"
"s_sub_u32 s54,s3,1024\n"
"buffer_store_dwordx4 a[240:243],v112,s[24:27],s74 offen offset:0 sc1\n"
"buffer_store_dwordx4 a[240:243],v112,s[24:27],s74 offen offset:1024 sc1\n"
"buffer_store_dword a240,off,s[24:27],s72 sc1\n"
"s_nop 0\n"
"s_swappc_b64 s[20:21],s[94:95]\n"
"s_xor_b32 s55,s55,16384\n"
"s_add_u32 s56,s56,131072\n"
"s_lshr_b32 s91,s78,2\n"
"s_add_u32 s91,s91,131072\n"
"v_lshrrev_b32 v121,1,v112\n"
"v_add_u32 v121,s91,v121\n"
"ds_read_b64_tr_b16 a[212:213],v121 offset:20480\n"
"ds_read_b64_tr_b16 a[214:215],v121 offset:20992\n"
"ds_read_b64_tr_b16 a[216:217],v121 offset:24576\n"
"ds_read_b64_tr_b16 a[218:219],v121 offset:25088\n"
"ds_read_b64_tr_b16 a[220:221],v121 offset:28672\n"
"ds_read_b64_tr_b16 a[222:223],v121 offset:29184\n"
"v_add_u32 v121,s91,v112\n"
"ds_read_b128 a[224:227],v121 offset:0\n"
"s_sub_u32 s54,s3,17408\n"
"s_mov_b32 s84,0x40000\n"
".Lfk_k_chk:\n"
"v_accvgpr_read_b32 v119,a150\n"
"v_accvgpr_read_b32 v120,a151\n"
"v_xor_b32 v119,s80,v119\n"
"v_xor_b32 v120,s81,v120\n"
"v_or_b32 v119,v119,v120\n"
"v_cmp_ne_u32 vcc,0,v119\n"
"s_nop 0\n"
"s_cbranch_vccz .Lfk_k_ok\n"
"s_sub_u32 s84,s84,1\n"
"s_cbranch_scc1 .Lfk_k_ok\n"
"s_sleep 4\n"
"v_lshrrev_b32 v121,1,v112\n"
"v_and_b32 v121,0xf8,v121\n"
"s_add_u32 s82,s63,8192\n"
"buffer_load_dwordx2 a[150:151],v121,s[24:27],s82 offen sc1\n"
"s_waitcnt vmcnt(0)\n"
"s_branch .Lfk_k_chk\n"
".Lfk_k_ok:\n"
"s_nop 0\n"
"s_swappc_b64 s[20:21],s[94:95]\n"
"s_xor_b32 s55,s55,16384\n"
"s_lshr_b32 s91,s78,2\n"
"s_add_u32 s91,s91,131072\n"
"v_add_u32 v121,s91,v112\n"
"ds_read_b128 a[228:231],v121 offset:4096\n"
"ds_read_b128 a[232:235],v121 offset:8192\n"
"ds_read_b128 a[236:239],v121 offset:12288\n"
"ds_read_b128 a[240:243],v121 offset:0\n"
"s_sub_u32 s54,s3,99328\n"
"s_mov_b32 s56,2146435072\n"
"s_getpc_b64 s[96:97]\n"
".Lfk_rt1:\n"
"s_sub_u32 s96,s96,.Lfk_rt1-.Lfk_tailA\n"
"s_subb_u32 s97,s97,0\n"
"s_mov_b32 s98,.Lfk_cold-.Lfk_tailA\n"
"s_mov_b32 s99,0x20000\n"
"s_lshr_b32 s82,s100,3\n"
"s_lshl2_add_u32 s82,s82,s52\n"
"s_lshl_b32 s82,s82,9\n"
"v_lshrrev_b32 v121,1,v112\n"
"buffer_load_dword a148,v121,s[96:99],s82 offen\n"
"s_nop 0\n"
"s_swappc_b64 s[20:21],s[94:95]\n"
"s_xor_b32 s55,s55,16384\n"
"s_lshr_b32 s91,s78,2\n"
"s_add_u32 s91,s91,131072\n"
"v_add_u32 v121,s91,v112\n"
"ds_read_b128 a[244:247],v121 offset:20480\n"
"ds_read_b128 a[248:251],v121 offset:24576\n"
"ds_read_b128 a[252:255],v121 offset:28672\n"
"ds_read_b128 a[156:159],v121 offset:0\n"
"s_sub_u32 s54,s3,115712\n"
"s_lshl_b32 s82,s100,3\n"
"s_add_u32 s82,s82,148946944\n"
"s_cmp_eq_u32 s52,0\n"
"s_cselect_b32 s82,s82,2146435072\n"
"buffer_store_dwordx2 a[154:155],off,s[24:27],s82 sc1\n"
"v_lshrrev_b32 v120,1,v112\n"
"buffer_load_dwordx2 a[150:151],v120,s[24:27],s11 offen sc1\n"
"s_lshl_b32 s30,s78,1\n"
"s_nop 0\n"
"s_swappc_b64 s[20:21],s[94:95]\n"
"s_nop 1\n"
".Lfk_cs_h3:\n"
"s_mov_b32 s91,15\n"
"s_mov_b32 s90,15\n"
"s_call_b64 s[94:95],.Lfk_rare0\n"
"s_branch .Lfk_cr_h3\n"
".Lfk_cs_h1:\n"
"s_mov_b32 s91,15\n"
"s_call_b64 s[94:95],.Lfk_rare3\n"
"s_branch .Lfk_cr_h1\n"
".Lfk_cs_h2:\n"
"s_mov_b32 s91,15\n"
"s_call_b64 s[94:95],.Lfk_rare2\n"
"s_branch .Lfk_cr_h2\n"
".Lfk_cs_x1:\n"
"s_mov_b32 s91,15\n"
"s_call_b64 s[94:95],.Lfk_rare1\n"
"s_branch .Lfk_cr_x1\n"
".Lfk_cs_a3:\n"
"s_mov_b32 s91,14\n"
"s_call_b64 s[94:95],.Lfk_rare0\n"
"s_branch .Lfk_cr_a3\n"
".Lfk_cs_a0:\n"
"s_mov_b32 s91,12\n"
"s_call_b64 s[94:95],.Lfk_rare1\n"
"s_branch .Lfk_cr_a0\n"
".Lfk_cs_a1:\n"
"s_mov_b32 s91,8\n"
"s_call_b64 s[94:95],.Lfk_rare0\n"
"s_branch .Lfk_cr_a1\n"
".Lfk_ms_a0:\n"
"s_call_b64 s[94:95],.Lfk_mslow0\n"
"s_branch .Lfk_mr_a0\n"
".Lfk_ms_a1:\n"
"s_call_b64 s[94:95],.Lfk_mslow1\n"
"s_branch .Lfk_mr_a1\n"
".Lfk_p2:\n"
"s_nop 7\n"
"s_mov_b32 s84,0x40000\n"
"buffer_load_dwordx2 v[122:123],v115,s[24:27],s11 offen sc1\n"
".Lfk_pj:\n"
"s_waitcnt vmcnt(0)\n"
"v_xor_b32 v119,s80,v122\n"
"v_xor_b32 v120,s81,v123\n"
"v_or_b32 v119,v119,v120\n"
"v_cmp_ne_u32 s[14:15],0,v119\n"
"s_nop 0\n"
"s_mov_b32 vcc_lo,s14\n"
"s_mov_b32 vcc_hi,0\n"
"s_cbranch_vccz .Lfk_pj_x\n"
"s_sleep 4\n"
"s_mov_b64 exec,vcc\n"
"buffer_load_dwordx2 v[122:123],v115,s[24:27],s11 offen sc1\n"
"s_mov_b64 exec,-1\n"
"s_sub_u32 s84,s84,1\n"
"s_cbranch_scc0 .Lfk_pj\n"
".Lfk_pj_x:\n"
"s_cmp_ge_u32 s52,2\n"
"s_cselect_b32 s18,1032192,0\n"
"s_add_u32 s18,s18,s8\n"
"s_add_u32 m0,s30,0\n"
"s_nop 0\n"
"buffer_load_dwordx4 v112,s[24:27],s18 offen offset:0 sc1 lds\n"
"buffer_load_dwordx4 v112,s[24:27],s18 offen offset:1024 sc1 lds\n"
"buffer_load_dwordx4 v112,s[24:27],s18 offen offset:2048 sc1 lds\n"
"buffer_load_dwordx4 v112,s[24:27],s18 offen offset:3072 sc1 lds\n"
"s_add_u32 m0,m0,4096\n"
"s_nop 0\n"
"buffer_load_dwordx4 v117,s[24:27],s18 offen offset:0 sc1 lds\n"
"buffer_load_dwordx4 v117,s[24:27],s18 offen offset:1024 sc1 lds\n"
"buffer_load_dwordx4 v117,s[24:27],s18 offen offset:2048 sc1 lds\n"
"buffer_load_dwordx4 v117,s[24:27],s18 offen offset:3072 sc1 lds\n"
"s_add_u32 s83,s18,2097152\n"
"s_add_u32 m0,s30,32768\n"
"s_nop 0\n"
"buffer_load_dwordx4 v112,s[24:27],s83 offen offset:0 sc1 lds\n"
"buffer_load_dwordx4 v112,s[24:27],s83 offen offset:1024 sc1 lds\n"
"buffer_load_dwordx4 v112,s[24:27],s83 offen offset:2048 sc1 lds\n"
"buffer_load_dwordx4 v112,s[24:27],s83 offen offset:3072 sc1 lds\n"
"s_add_u32 m0,m0,4096\n"
"s_nop 0\n"
"buffer_load_dwordx4 v117,s[24:27],s83 offen offset:0 sc1 lds\n"
"buffer_load_dwordx4 v117,s[24:27],s83 offen offset:1024 sc1 lds\n"
"buffer_load_dwordx4 v117,s[24:27],s83 offen offset:2048 sc1 lds\n"
"buffer_load_dwordx4 v117,s[24:27],s83 offen offset:3072 sc1 lds\n"
"s_waitcnt vmcnt(0)\n"
"s_nop 1\n"
"s_branch .Lfk_p3\n"
".Lfk_cs_k1:\n"
"s_mov_b32 s91,15\n"
"s_call_b64 s[94:95],.Lfk_rare3\n"
"s_branch .Lfk_cr_k1\n"
".Lfk_cs_k2:\n"
"s_mov_b32 s91,15\n"
"s_call_b64 s[94:95],.Lfk_rare2\n"
"s_branch .Lfk_cr_k2\n"
".Lfk_cs_x2:\n"
"s_mov_b32 s91,15\n"
"s_call_b64 s[94:95],.Lfk_rare1\n"
"s_branch .Lfk_cr_x2\n"
".Lfk_cs_u3:\n"
"s_mov_b32 s91,15\n"
"s_call_b64 s[94:95],.Lfk_rare0\n"
"s_branch .Lfk_cr_u3\n"
".Lfk_cs_mu3:\n"
"s_call_b64 s[94:95],.Lfk_meet3\n"
"s_branch .Lfk_cr_mu3\n"
".Lfk_cs_u0:\n"
"s_mov_b32 s91,14\n"
"s_call_b64 s[94:95],.Lfk_rare1\n"
"s_branch .Lfk_cr_u0\n"
".Lfk_cs_mu0:\n"
"s_call_b64 s[94:95],.Lfk_meet3\n"
"s_branch .Lfk_cr_mu0\n"
".Lfk_cs_u1:\n"
"s_mov_b32 s91,12\n"
"s_call_b64 s[94:95],.Lfk_rare0\n"
"s_branch .Lfk_cr_u1\n"
".Lfk_cs_mu1:\n"
"s_call_b64 s[94:95],.Lfk_meet3\n"
"s_branch .Lfk_cr_mu1\n"
".Lfk_cs_u2:\n"
"s_mov_b32 s91,8\n"
"s_call_b64 s[94:95],.Lfk_rare1\n"
"s_branch .Lfk_cr_u2\n"
".Lfk_cs_mu2:\n"
"s_call_b64 s[94:95],.Lfk_meet3\n"
"s_branch .Lfk_cr_mu2\n"
".Lfk_cs_v3:\n"
"s_mov_b32 s91,15\n"
"s_mov_b32 s90,15\n"
"s_call_b64 s[94:95],.Lfk_rare0\n"
"s_branch .Lfk_cr_v3\n"
".Lfk_cs_mv3:\n"
"s_call_b64 s[94:95],.Lfk_meet3\n"
"s_branch .Lfk_cr_mv3\n"
".Lfk_cs_mv0:\n"
"s_call_b64 s[94:95],.Lfk_meet3\n"
"s_branch .Lfk_cr_mv0\n"
".Lfk_cs_v1:\n"
"s_call_b64 s[94:95],.Lfk_meet3\n"
"v_or_b32 v118,v118,v95\n"
"v_and_b32 v118,0x40004000,v118\n"
"v_cmp_ne_u32 vcc,0,v118\n"
"s_nop 0\n"
"s_cbranch_vccz .Lfk_cr_v1\n"
"s_mov_b32 s91,15\n"
"s_call_b64 s[94:95],.Lfk_rare3\n"
"s_branch .Lfk_cr_v1\n"
".Lfk_cs_v2:\n"
"s_call_b64 s[94:95],.Lfk_meet3\n"
"v_or_b32 v118,v118,v79\n"
"v_and_b32 v118,0x40004000,v118\n"
"v_cmp_ne_u32 vcc,0,v118\n"
"s_nop 0\n"
"s_cbranch_vccz .Lfk_cr_v2\n"
"s_mov_b32 s91,15\n"
"s_call_b64 s[94:95],.Lfk_rare2\n"
"s_branch .Lfk_cr_v2\n"
".Lfk_cs_b3:\n"
"s_mov_b32 s91,15\n"
"s_call_b64 s[94:95],.Lfk_rare0\n"
"s_branch .Lfk_cr_b3\n"
".Lfk_ms_b0:\n"
"s_call_b64 s[94:95],.Lfk_mslow0\n"
"s_branch .Lfk_mr_b0\n"
".Lfk_ms_b1:\n"
"s_call_b64 s[94:95],.Lfk_mslow1\n"
"s_branch .Lfk_mr_b1\n"
".Lfk_cs_c3:\n"
"s_mov_b32 s91,15\n"
"s_mov_b32 s90,15\n"
"s_call_b64 s[94:95],.Lfk_rare0\n"
"s_branch .Lfk_cr_c3\n"
".Lfk_cs_z3:\n"
"s_mov_b32 s91,15\n"
"s_mov_b32 s90,15\n"
"s_call_b64 s[94:95],.Lfk_rare0\n"
"s_branch .Lfk_cr_z3\n"
".Lfk_cs_z0:\n"
"s_mov_b32 s91,14\n"
"s_call_b64 s[94:95],.Lfk_rare1\n"
"s_branch .Lfk_cr_z0\n"
".Lfk_cs_z1:\n"
"s_mov_b32 s91,12\n"
"s_call_b64 s[94:95],.Lfk_rare0\n"
"s_branch .Lfk_cr_z1\n"
".Lfk_cs_z2:\n"
"s_mov_b32 s91,8\n"
"s_call_b64 s[94:95],.Lfk_rare1\n"
"s_branch .Lfk_cr_z2\n"
".Lfk_cold:\n"
".Lfk_rare0:\n"
"s_nop 7\n"
"s_bitcmp1_b32 s91,0\n"
"s_cbranch_scc0 .Lfk_rare0_0\n"
"v_max3_f32 v119,v0,v1,v2\n"
"v_max3_f32 v120,v3,v4,v5\n"
"v_max3_f32 v119,v119,v6,v7\n"
"v_max_f32 v119,v119,v120\n"
"v_mov_b32 v120,v119\n"
"s_nop 1\n"
"v_permlane16_swap_b32 v120,v119\n"
"v_max_f32 v119,v119,v120\n"
"v_mov_b32 v120,v119\n"
"s_nop 1\n"
"v_permlane32_swap_b32 v120,v119\n"
"v_max_f32 v119,v119,v120\n"
"s_bitcmp1_b32 s90,0\n"
"s_cselect_b32 s82,0xff800000,0\n"
"v_add_f32 v119,0x41980000,v119\n"
"v_max_f32 v119,s82,v119\n"
"v_cmp_lt_f32 vcc,s17,|v119|\n"
"s_nop 0\n"
"s_cbranch_vccz .Lfk_rare0_0\n"
"v_ceil_f32 v119,v119\n"
"v_sub_f32 v0,v0,v119\n"
"v_sub_f32 v1,v1,v119\n"
"v_sub_f32 v2,v2,v119\n"
"v_sub_f32 v3,v3,v119\n"
"v_sub_f32 v4,v4,v119\n"
"v_sub_f32 v5,v5,v119\n"
"v_sub_f32 v6,v6,v119\n"
"v_sub_f32 v7,v7,v119\n"
"v_sub_f32 v96,v96,v119\n"
"v_sub_f32 v97,v97,v119\n"
"v_sub_f32 v98,v98,v119\n"
"v_sub_f32 v99,v99,v119\n"
"s_cbranch_scc1 .Lfk_rare0_0\n"
"v_exp_f32 v119,-v119\n"
"s_nop 0\n"
"v_mul_f32 v128,v128,v119\n"
"v_mul_f32 v129,v129,v119\n"
"v_mul_f32 v130,v130,v119\n"
"v_mul_f32 v131,v131,v119\n"
"v_mul_f32 v132,v132,v119\n"
"v_mul_f32 v133,v133,v119\n"
"v_mul_f32 v134,v134,v119\n"
"v_mul_f32 v135,v135,v119\n"
"v_mul_f32 v136,v136,v119\n"
"v_mul_f32 v137,v137,v119\n"
"v_mul_f32 v138,v138,v119\n"
"v_mul_f32 v139,v139,v119\n"
"v_mul_f32 v140,v140,v119\n"
"v_mul_f32 v141,v141,v119\n"
"v_mul_f32 v142,v142,v119\n"
"v_mul_f32 v143,v143,v119\n"
"v_mul_f32 v144,v144,v119\n"
"v_mul_f32 v145,v145,v119\n"
"v_mul_f32 v146,v146,v119\n"
"v_mul_f32 v147,v147,v119\n"
"v_mul_f32 v148,v148,v119\n"
"v_mul_f32 v149,v149,v119\n"
"v_mul_f32 v150,v150,v119\n"
"v_mul_f32 v151,v151,v119\n"
"v_mul_f32 v152,v152,v119\n"
"v_mul_f32 v153,v153,v119\n"
"v_mul_f32 v154,v154,v119\n"
"v_mul_f32 v155,v155,v119\n"
"v_mul_f32 v156,v156,v119\n"
"v_mul_f32 v157,v157,v119\n"
"v_mul_f32 v158,v158,v119\n"
"v_mul_f32 v159,v159,v119\n"
"v_accvgpr_read_b32 v120,a128\n"
"v_mul_f32 v120,v120,v119\n"
"v_accvgpr_write_b32 a128,v120\n"
"v_lshlrev_b32 v120,16,v80\n"
"v_and_b32 v121,0xffff0000,v80\n"
"v_mul_f32 v120,v120,v119\n"
"v_mul_f32 v121,v121,v119\n"
"v_cvt_pk_bf16_f32 v80,v120,v121\n"
"v_accvgpr_read_b32 v120,a129\n"
"v_mul_f32 v120,v120,v119\n"
"v_accvgpr_write_b32 a129,v120\n"
"v_lshlrev_b32 v120,16,v81\n"
"v_and_b32 v121,0xffff0000,v81\n"
"v_mul_f32 v120,v120,v119\n"
"v_mul_f32 v121,v121,v119\n"
"v_cvt_pk_bf16_f32 v81,v120,v121\n"
"v_accvgpr_read_b32 v120,a130\n"
"v_mul_f32 v120,v120,v119\n"
"v_accvgpr_write_b32 a130,v120\n"
"v_lshlrev_b32 v120,16,v82\n"
"v_and_b32 v121,0xffff0000,v82\n"
"v_mul_f32 v120,v120,v119\n"
"v_mul_f32 v121,v121,v119\n"
"v_cvt_pk_bf16_f32 v82,v120,v121\n"
"v_accvgpr_read_b32 v120,a131\n"
"v_mul_f32 v120,v120,v119\n"
"v_accvgpr_write_b32 a131,v120\n"
"v_lshlrev_b32 v120,16,v83\n"
"v_and_b32 v121,0xffff0000,v83\n"
"v_mul_f32 v120,v120,v119\n"
"v_mul_f32 v121,v121,v119\n"
"v_cvt_pk_bf16_f32 v83,v120,v121\n"
".Lfk_rare0_0:\n"
"s_bitcmp1_b32 s91,1\n"
"s_cbranch_scc0 .Lfk_rare0_1\n"
"v_max3_f32 v119,v8,v9,v10\n"
"v_max3_f32 v120,v11,v12,v13\n"
"v_max3_f32 v119,v119,v14,v15\n"
"v_max_f32 v119,v119,v120\n"
"v_mov_b32 v120,v119\n"
"s_nop 1\n"
"v_permlane16_swap_b32 v120,v119\n"
"v_max_f32 v119,v119,v120\n"
"v_mov_b32 v120,v119\n"
"s_nop 1\n"
"v_permlane32_swap_b32 v120,v119\n"
"v_max_f32 v119,v119,v120\n"
"s_bitcmp1_b32 s90,1\n"
"s_cselect_b32 s82,0xff800000,0\n"
"v_add_f32 v119,0x41980000,v119\n"
"v_max_f32 v119,s82,v119\n"
"v_cmp_lt_f32 vcc,s17,|v119|\n"
"s_nop 0\n"
"s_cbranch_vccz .Lfk_rare0_1\n"
"v_ceil_f32 v119,v119\n"
"v_sub_f32 v8,v8,v119\n"
"v_sub_f32 v9,v9,v119\n"
"v_sub_f32 v10,v10,v119\n"
"v_sub_f32 v11,v11,v119\n"
"v_sub_f32 v12,v12,v119\n"
"v_sub_f32 v13,v13,v119\n"
"v_sub_f32 v14,v14,v119\n"
"v_sub_f32 v15,v15,v119\n"
"v_sub_f32 v100,v100,v119\n"
"v_sub_f32 v101,v101,v119\n"
"v_sub_f32 v102,v102,v119\n"
"v_sub_f32 v103,v103,v119\n"
"s_cbranch_scc1 .Lfk_rare0_1\n"
"v_exp_f32 v119,-v119\n"
"s_nop 0\n"
"v_mul_f32 v160,v160,v119\n"
"v_mul_f32 v161,v161,v119\n"
"v_mul_f32 v162,v162,v119\n"
"v_mul_f32 v163,v163,v119\n"
"v_mul_f32 v164,v164,v119\n"
"v_mul_f32 v165,v165,v119\n"
"v_mul_f32 v166,v166,v119\n"
"v_mul_f32 v167,v167,v119\n"
"v_mul_f32 v168,v168,v119\n"
"v_mul_f32 v169,v169,v119\n"
"v_mul_f32 v170,v170,v119\n"
"v_mul_f32 v171,v171,v119\n"
"v_mul_f32 v172,v172,v119\n"
"v_mul_f32 v173,v173,v119\n"
"v_mul_f32 v174,v174,v119\n"
"v_mul_f32 v175,v175,v119\n"
"v_mul_f32 v176,v176,v119\n"
"v_mul_f32 v177,v177,v119\n"
"v_mul_f32 v178,v178,v119\n"
"v_mul_f32 v179,v179,v119\n"
"v_mul_f32 v180,v180,v119\n"
"v_mul_f32 v181,v181,v119\n"
"v_mul_f32 v182,v182,v119\n"
"v_mul_f32 v183,v183,v119\n"
"v_mul_f32 v184,v184,v119\n"
"v_mul_f32 v185,v185,v119\n"
"v_mul_f32 v186,v186,v119\n"
"v_mul_f32 v187,v187,v119\n"
"v_mul_f32 v188,v188,v119\n"
"v_mul_f32 v189,v189,v119\n"
"v_mul_f32 v190,v190,v119\n"
"v_mul_f32 v191,v191,v119\n"
"v_accvgpr_read_b32 v120,a132\n"
"v_mul_f32 v120,v120,v119\n"
"v_accvgpr_write_b32 a132,v120\n"
"v_lshlrev_b32 v120,16,v84\n"
"v_and_b32 v121,0xffff0000,v84\n"
"v_mul_f32 v120,v120,v119\n"
"v_mul_f32 v121,v121,v119\n"
"v_cvt_pk_bf16_f32 v84,v120,v121\n"
"v_accvgpr_read_b32 v120,a133\n"
"v_mul_f32 v120,v120,v119\n"
"v_accvgpr_write_b32 a133,v120\n"
"v_lshlrev_b32 v120,16,v85\n"
"v_and_b32 v121,0xffff0000,v85\n"
"v_mul_f32 v120,v120,v119\n"
"v_mul_f32 v121,v121,v119\n"
"v_cvt_pk_bf16_f32 v85,v120,v121\n"
"v_accvgpr_read_b32 v120,a134\n"
"v_mul_f32 v120,v120,v119\n"
"v_accvgpr_write_b32 a134,v120\n"
"v_lshlrev_b32 v120,16,v86\n"
"v_and_b32 v121,0xffff0000,v86\n"
"v_mul_f32 v120,v120,v119\n"
"v_mul_f32 v121,v121,v119\n"
"v_cvt_pk_bf16_f32 v86,v120,v121\n"
"v_accvgpr_read_b32 v120,a135\n"
"v_mul_f32 v120,v120,v119\n"
"v_accvgpr_write_b32 a135,v120\n"
"v_lshlrev_b32 v120,16,v87\n"
"v_and_b32 v121,0xffff0000,v87\n"
"v_mul_f32 v120,v120,v119\n"
"v_mul_f32 v121,v121,v119\n"
"v_cvt_pk_bf16_f32 v87,v120,v121\n"
".Lfk_rare0_1:\n"
"s_bitcmp1_b32 s91,2\n"
"s_cbranch_scc0 .Lfk_rare0_2\n"
"v_max3_f32 v119,v16,v17,v18\n"
"v_max3_f32 v120,v19,v20,v21\n"
"v_max3_f32 v119,v119,v22,v23\n"
"v_max_f32 v119,v119,v120\n"
"v_mov_b32 v120,v119\n"
"s_nop 1\n"
"v_permlane16_swap_b32 v120,v119\n"
"v_max_f32 v119,v119,v120\n"
"v_mov_b32 v120,v119\n"
"s_nop 1\n"
"v_permlane32_swap_b32 v120,v119\n"
"v_max_f32 v119,v119,v120\n"
"s_bitcmp1_b32 s90,2\n"
"s_cselect_b32 s82,0xff800000,0\n"
"v_add_f32 v119,0x41980000,v119\n"
"v_max_f32 v119,s82,v119\n"
"v_cmp_lt_f32 vcc,s17,|v119|\n"
"s_nop 0\n"
"s_cbranch_vccz .Lfk_rare0_2\n"
"v_ceil_f32 v119,v119\n"
"v_sub_f32 v16,v16,v119\n"
"v_sub_f32 v17,v17,v119\n"
"v_sub_f32 v18,v18,v119\n"
"v_sub_f32 v19,v19,v119\n"
"v_sub_f32 v20,v20,v119\n"
"v_sub_f32 v21,v21,v119\n"
"v_sub_f32 v22,v22,v119\n"
"v_sub_f32 v23,v23,v119\n"
"v_sub_f32 v104,v104,v119\n"
"v_sub_f32 v105,v105,v119\n"
"v_sub_f32 v106,v106,v119\n"
"v_sub_f32 v107,v107,v119\n"
"s_cbranch_scc1 .Lfk_rare0_2\n"
"v_exp_f32 v119,-v119\n"
"s_nop 0\n"
"v_mul_f32 v192,v192,v119\n"
"v_mul_f32 v193,v193,v119\n"
"v_mul_f32 v194,v194,v119\n"
"v_mul_f32 v195,v195,v119\n"
"v_mul_f32 v196,v196,v119\n"
"v_mul_f32 v197,v197,v119\n"
"v_mul_f32 v198,v198,v119\n"
"v_mul_f32 v199,v199,v119\n"
"v_mul_f32 v200,v200,v119\n"
"v_mul_f32 v201,v201,v119\n"
"v_mul_f32 v202,v202,v119\n"
"v_mul_f32 v203,v203,v119\n"
"v_mul_f32 v204,v204,v119\n"
"v_mul_f32 v205,v205,v119\n"
"v_mul_f32 v206,v206,v119\n"
"v_mul_f32 v207,v207,v119\n"
"v_mul_f32 v208,v208,v119\n"
"v_mul_f32 v209,v209,v119\n"
"v_mul_f32 v210,v210,v119\n"
"v_mul_f32 v211,v211,v119\n"
"v_mul_f32 v212,v212,v119\n"
"v_mul_f32 v213,v213,v119\n"
"v_mul_f32 v214,v214,v119\n"
"v_mul_f32 v215,v215,v119\n"
"v_mul_f32 v216,v216,v119\n"
"v_mul_f32 v217,v217,v119\n"
"v_mul_f32 v218,v218,v119\n"
"v_mul_f32 v219,v219,v119\n"
"v_mul_f32 v220,v220,v119\n"
"v_mul_f32 v221,v221,v119\n"
"v_mul_f32 v222,v222,v119\n"
"v_mul_f32 v223,v223,v119\n"
"v_accvgpr_read_b32 v120,a136\n"
"v_mul_f32 v120,v120,v119\n"
"v_accvgpr_write_b32 a136,v120\n"
"v_lshlrev_b32 v120,16,v88\n"
"v_and_b32 v121,0xffff0000,v88\n"
"v_mul_f32 v120,v120,v119\n"
"v_mul_f32 v121,v121,v119\n"
"v_cvt_pk_bf16_f32 v88,v120,v121\n"
"v_accvgpr_read_b32 v120,a137\n"
"v_mul_f32 v120,v120,v119\n"
"v_accvgpr_write_b32 a137,v120\n"
"v_lshlrev_b32 v120,16,v89\n"
"v_and_b32 v121,0xffff0000,v89\n"
"v_mul_f32 v120,v120,v119\n"
"v_mul_f32 v121,v121,v119\n"
"v_cvt_pk_bf16_f32 v89,v120,v121\n"
"v_accvgpr_read_b32 v120,a138\n"
"v_mul_f32 v120,v120,v119\n"
"v_accvgpr_write_b32 a138,v120\n"
"v_lshlrev_b32 v120,16,v90\n"
"v_and_b32 v121,0xffff0000,v90\n"
"v_mul_f32 v120,v120,v119\n"
"v_mul_f32 v121,v121,v119\n"
"v_cvt_pk_bf16_f32 v90,v120,v121\n"
"v_accvgpr_read_b32 v120,a139\n"
"v_mul_f32 v120,v120,v119\n"
"v_accvgpr_write_b32 a139,v120\n"
"v_lshlrev_b32 v120,16,v91\n"
"v_and_b32 v121,0xffff0000,v91\n"
"v_mul_f32 v120,v120,v119\n"
"v_mul_f32 v121,v121,v119\n"
"v_cvt_pk_bf16_f32 v91,v120,v121\n"
".Lfk_rare0_2:\n"
"s_bitcmp1_b32 s91,3\n"
"s_cbranch_scc0 .Lfk_rare0_3\n"
"v_max3_f32 v119,v24,v25,v26\n"
"v_max3_f32 v120,v27,v28,v29\n"
"v_max3_f32 v119,v119,v30,v31\n"
"v_max_f32 v119,v119,v120\n"
"v_mov_b32 v120,v119\n"
"s_nop 1\n"
"v_permlane16_swap_b32 v120,v119\n"
"v_max_f32 v119,v119,v120\n"
"v_mov_b32 v120,v119\n"
"s_nop 1\n"
"v_permlane32_swap_b32 v120,v119\n"
"v_max_f32 v119,v119,v120\n"
"s_bitcmp1_b32 s90,3\n"
"s_cselect_b32 s82,0xff800000,0\n"
"v_add_f32 v119,0x41980000,v119\n"
"v_max_f32 v119,s82,v119\n"
"v_cmp_lt_f32 vcc,s17,|v119|\n"
"s_nop 0\n"
"s_cbranch_vccz .Lfk_rare0_3\n"
"v_ceil_f32 v119,v119\n"
"v_sub_f32 v24,v24,v119\n"
"v_sub_f32 v25,v25,v119\n"
"v_sub_f32 v26,v26,v119\n"
"v_sub_f32 v27,v27,v119\n"
"v_sub_f32 v28,v28,v119\n"
"v_sub_f32 v29,v29,v119\n"
"v_sub_f32 v30,v30,v119\n"
"v_sub_f32 v31,v31,v119\n"
"v_sub_f32 v108,v108,v119\n"
"v_sub_f32 v109,v109,v119\n"
"v_sub_f32 v110,v110,v119\n"
"v_sub_f32 v111,v111,v119\n"
"s_cbranch_scc1 .Lfk_rare0_3\n"
"v_exp_f32 v119,-v119\n"
"s_nop 0\n"
"v_mul_f32 v224,v224,v119\n"
"v_mul_f32 v225,v225,v119\n"
"v_mul_f32 v226,v226,v119\n"
"v_mul_f32 v227,v227,v119\n"
"v_mul_f32 v228,v228,v119\n"
"v_mul_f32 v229,v229,v119\n"
"v_mul_f32 v230,v230,v119\n"
"v_mul_f32 v231,v231,v119\n"
"v_mul_f32 v232,v232,v119\n"
"v_mul_f32 v233,v233,v119\n"
"v_mul_f32 v234,v234,v119\n"
"v_mul_f32 v235,v235,v119\n"
"v_mul_f32 v236,v236,v119\n"
"v_mul_f32 v237,v237,v119\n"
"v_mul_f32 v238,v238,v119\n"
"v_mul_f32 v239,v239,v119\n"
"v_mul_f32 v240,v240,v119\n"
"v_mul_f32 v241,v241,v119\n"
"v_mul_f32 v242,v242,v119\n"
"v_mul_f32 v243,v243,v119\n"
"v_mul_f32 v244,v244,v119\n"
"v_mul_f32 v245,v245,v119\n"
"v_mul_f32 v246,v246,v119\n"
"v_mul_f32 v247,v247,v119\n"
"v_mul_f32 v248,v248,v119\n"
"v_mul_f32 v249,v249,v119\n"
"v_mul_f32 v250,v250,v119\n"
"v_mul_f32 v251,v251,v119\n"
"v_mul_f32 v252,v252,v119\n"
"v_mul_f32 v253,v253,v119\n"
"v_mul_f32 v254,v254,v119\n"
"v_mul_f32 v255,v255,v119\n"
"v_accvgpr_read_b32 v120,a140\n"
"v_mul_f32 v120,v120,v119\n"
"v_accvgpr_write_b32 a140,v120\n"
"v_lshlrev_b32 v120,16,v92\n"
"v_and_b32 v121,0xffff0000,v92\n"
"v_mul_f32 v120,v120,v119\n"
"v_mul_f32 v121,v121,v119\n"
"v_cvt_pk_bf16_f32 v92,v120,v121\n"
"v_accvgpr_read_b32 v120,a141\n"
"v_mul_f32 v120,v120,v119\n"
"v_accvgpr_write_b32 a141,v120\n"
"v_lshlrev_b32 v120,16,v93\n"
"v_and_b32 v121,0xffff0000,v93\n"
"v_mul_f32 v120,v120,v119\n"
"v_mul_f32 v121,v121,v119\n"
"v_cvt_pk_bf16_f32 v93,v120,v121\n"
"v_accvgpr_read_b32 v120,a142\n"
"v_mul_f32 v120,v120,v119\n"
"v_accvgpr_write_b32 a142,v120\n"
"v_lshlrev_b32 v120,16,v94\n"
"v_and_b32 v121,0xffff0000,v94\n"
"v_mul_f32 v120,v120,v119\n"
"v_mul_f32 v121,v121,v119\n"
"v_cvt_pk_bf16_f32 v94,v120,v121\n"
"v_accvgpr_read_b32 v120,a143\n"
"v_mul_f32 v120,v120,v119\n"
"v_accvgpr_write_b32 a143,v120\n"
"v_lshlrev_b32 v120,16,v95\n"
"v_and_b32 v121,0xffff0000,v95\n"
"v_mul_f32 v120,v120,v119\n"
"v_mul_f32 v121,v121,v119\n"
"v_cvt_pk_bf16_f32 v95,v120,v121\n"
".Lfk_rare0_3:\n"
"s_mov_b32 s90,0\n"
"s_nop 1\n"
"s_setpc_b64 s[94:95]\n"
".Lfk_rare2:\n"
"s_nop 7\n"
"v_max3_f32 v119,v0,v1,v2\n"
"v_max3_f32 v120,v3,v4,v5\n"
"v_max3_f32 v119,v119,v6,v7\n"
"v_max_f32 v119,v119,v120\n"
"v_mov_b32 v120,v119\n"
"s_nop 1\n"
"v_permlane16_swap_b32 v120,v119\n"
"v_max_f32 v119,v119,v120\n"
"v_mov_b32 v120,v119\n"
"s_nop 1\n"
"v_permlane32_swap_b32 v120,v119\n"
"v_max_f32 v119,v119,v120\n"
"s_bitcmp1_b32 s90,0\n"
"s_cselect_b32 s82,0xff800000,0\n"
"v_add_f32 v119,0x41980000,v119\n"
"v_max_f32 v119,s82,v119\n"
"v_cmp_lt_f32 vcc,s17,|v119|\n"
"s_nop 0\n"
"s_cbranch_vccz .Lfk_rare2_0\n"
"v_ceil_f32 v119,v119\n"
"v_sub_f32 v0,v0,v119\n"
"v_sub_f32 v1,v1,v119\n"
"v_sub_f32 v2,v2,v119\n"
"v_sub_f32 v3,v3,v119\n"
"v_sub_f32 v4,v4,v119\n"
"v_sub_f32 v5,v5,v119\n"
"v_sub_f32 v6,v6,v119\n"
"v_sub_f32 v7,v7,v119\n"
"v_sub_f32 v96,v96,v119\n"
"v_sub_f32 v97,v97,v119\n"
"v_sub_f32 v98,v98,v119\n"
"v_sub_f32 v99,v99,v119\n"
"v_sub_f32 v32,v32,v119\n"
"v_sub_f32 v33,v33,v119\n"
"v_sub_f32 v34,v34,v119\n"
"v_sub_f32 v35,v35,v119\n"
"v_sub_f32 v36,v36,v119\n"
"v_sub_f32 v37,v37,v119\n"
"v_sub_f32 v38,v38,v119\n"
"v_sub_f32 v39,v39,v119\n"
"v_exp_f32 v0,v0\n"
"v_exp_f32 v1,v1\n"
"v_exp_f32 v2,v2\n"
"v_exp_f32 v3,v3\n"
"v_exp_f32 v4,v4\n"
"v_exp_f32 v5,v5\n"
"v_exp_f32 v6,v6\n"
"v_exp_f32 v7,v7\n"
"v_cvt_pk_bf16_f32 v64,v0,v1\n"
"v_cvt_pk_bf16_f32 v65,v2,v3\n"
"v_cvt_pk_bf16_f32 v66,v4,v5\n"
"v_cvt_pk_bf16_f32 v67,v6,v7\n"
"s_cbranch_scc1 .Lfk_rare2_0\n"
"v_exp_f32 v119,-v119\n"
"s_nop 0\n"
"v_mul_f32 v128,v128,v119\n"
"v_mul_f32 v129,v129,v119\n"
"v_mul_f32 v130,v130,v119\n"
"v_mul_f32 v131,v131,v119\n"
"v_mul_f32 v132,v132,v119\n"
"v_mul_f32 v133,v133,v119\n"
"v_mul_f32 v134,v134,v119\n"
"v_mul_f32 v135,v135,v119\n"
"v_mul_f32 v136,v136,v119\n"
"v_mul_f32 v137,v137,v119\n"
"v_mul_f32 v138,v138,v119\n"
"v_mul_f32 v139,v139,v119\n"
"v_mul_f32 v140,v140,v119\n"
"v_mul_f32 v141,v141,v119\n"
"v_mul_f32 v142,v142,v119\n"
"v_mul_f32 v143,v143,v119\n"
"v_mul_f32 v144,v144,v119\n"
"v_mul_f32 v145,v145,v119\n"
"v_mul_f32 v146,v146,v119\n"
"v_mul_f32 v147,v147,v119\n"
"v_mul_f32 v148,v148,v119\n"
"v_mul_f32 v149,v149,v119\n"
"v_mul_f32 v150,v150,v119\n"
"v_mul_f32 v151,v151,v119\n"
"v_mul_f32 v152,v152,v119\n"
"v_mul_f32 v153,v153,v119\n"
"v_mul_f32 v154,v154,v119\n"
"v_mul_f32 v155,v155,v119\n"
"v_mul_f32 v156,v156,v119\n"
"v_mul_f32 v157,v157,v119\n"
"v_mul_f32 v158,v158,v119\n"
"v_mul_f32 v159,v159,v119\n"
"v_accvgpr_read_b32 v120,a128\n"
"v_mul_f32 v120,v120,v119\n"
"v_accvgpr_write_b32 a128,v120\n"
"v_accvgpr_read_b32 v120,a129\n"
"v_mul_f32 v120,v120,v119\n"
"v_accvgpr_write_b32 a129,v120\n"
"v_accvgpr_read_b32 v120,a130\n"
"v_mul_f32 v120,v120,v119\n"
"v_accvgpr_write_b32 a130,v120\n"
"v_accvgpr_read_b32 v120,a131\n"
"v_mul_f32 v120,v120,v119\n"
"v_accvgpr_write_b32 a131,v120\n"
".Lfk_rare2_0:\n"
"v_max3_f32 v119,v8,v9,v10\n"
"v_max3_f32 v120,v11,v12,v13\n"
"v_max3_f32 v119,v119,v14,v15\n"
"v_max_f32 v119,v119,v120\n"
"v_mov_b32 v120,v119\n"
"s_nop 1\n"
"v_permlane16_swap_b32 v120,v119\n"
"v_max_f32 v119,v119,v120\n"
"v_mov_b32 v120,v119\n"
"s_nop 1\n"
"v_permlane32_swap_b32 v120,v119\n"
"v_max_f32 v119,v119,v120\n"
"s_bitcmp1_b32 s90,1\n"
"s_cselect_b32 s82,0xff800000,0\n"
"v_add_f32 v119,0x41980000,v119\n"
"v_max_f32 v119,s82,v119\n"
"v_cmp_lt_f32 vcc,s17,|v119|\n"
"s_nop 0\n"
"s_cbranch_vccz .Lfk_rare2_1\n"
"v_ceil_f32 v119,v119\n"
"v_sub_f32 v8,v8,v119\n"
"v_sub_f32 v9,v9,v119\n"
"v_sub_f32 v10,v10,v119\n"
"v_sub_f32 v11,v11,v119\n"
"v_sub_f32 v12,v12,v119\n"
"v_sub_f32 v13,v13,v119\n"
"v_sub_f32 v14,v14,v119\n"
"v_sub_f32 v15,v15,v119\n"
"v_sub_f32 v100,v100,v119\n"
"v_sub_f32 v101,v101,v119\n"
"v_sub_f32 v102,v102,v119\n"
"v_sub_f32 v103,v103,v119\n"
"v_sub_f32 v40,v40,v119\n"
"v_sub_f32 v41,v41,v119\n"
"v_sub_f32 v42,v42,v119\n"
"v_sub_f32 v43,v43,v119\n"
"v_sub_f32 v44,v44,v119\n"
"v_sub_f32 v45,v45,v119\n"
"v_sub_f32 v46,v46,v119\n"
"v_sub_f32 v47,v47,v119\n"
"v_exp_f32 v8,v8\n"
"v_exp_f32 v9,v9\n"
"v_exp_f32 v10,v10\n"
"v_exp_f32 v11,v11\n"
"v_exp_f32 v12,v12\n"
"v_exp_f32 v13,v13\n"
"v_exp_f32 v14,v14\n"
"v_exp_f32 v15,v15\n"
"v_cvt_pk_bf16_f32 v68,v8,v9\n"
"v_cvt_pk_bf16_f32 v69,v10,v11\n"
"v_cvt_pk_bf16_f32 v70,v12,v13\n"
"v_cvt_pk_bf16_f32 v71,v14,v15\n"
"s_cbranch_scc1 .Lfk_rare2_1\n"
"v_exp_f32 v119,-v119\n"
"s_nop 0\n"
"v_mul_f32 v160,v160,v119\n"
"v_mul_f32 v161,v161,v119\n"
"v_mul_f32 v162,v162,v119\n"
"v_mul_f32 v163,v163,v119\n"
"v_mul_f32 v164,v164,v119\n"
"v_mul_f32 v165,v165,v119\n"
"v_mul_f32 v166,v166,v119\n"
"v_mul_f32 v167,v167,v119\n"
"v_mul_f32 v168,v168,v119\n"
"v_mul_f32 v169,v169,v119\n"
"v_mul_f32 v170,v170,v119\n"
"v_mul_f32 v171,v171,v119\n"
"v_mul_f32 v172,v172,v119\n"
"v_mul_f32 v173,v173,v119\n"
"v_mul_f32 v174,v174,v119\n"
"v_mul_f32 v175,v175,v119\n"
"v_mul_f32 v176,v176,v119\n"
"v_mul_f32 v177,v177,v119\n"
"v_mul_f32 v178,v178,v119\n"
"v_mul_f32 v179,v179,v119\n"
"v_mul_f32 v180,v180,v119\n"
"v_mul_f32 v181,v181,v119\n"
"v_mul_f32 v182,v182,v119\n"
"v_mul_f32 v183,v183,v119\n"
"v_mul_f32 v184,v184,v119\n"
"v_mul_f32 v185,v185,v119\n"
"v_mul_f32 v186,v186,v119\n"
"v_mul_f32 v187,v187,v119\n"
"v_mul_f32 v188,v188,v119\n"
"v_mul_f32 v189,v189,v119\n"
"v_mul_f32 v190,v190,v119\n"
"v_mul_f32 v191,v191,v119\n"
"v_accvgpr_read_b32 v120,a132\n"
"v_mul_f32 v120,v120,v119\n"
"v_accvgpr_write_b32 a132,v120\n"
"v_accvgpr_read_b32 v120,a133\n"
"v_mul_f32 v120,v120,v119\n"
"v_accvgpr_write_b32 a133,v120\n"
"v_accvgpr_read_b32 v120,a134\n"
"v_mul_f32 v120,v120,v119\n"
"v_accvgpr_write_b32 a134,v120\n"
"v_accvgpr_read_b32 v120,a135\n"
"v_mul_f32 v120,v120,v119\n"
"v_accvgpr_write_b32 a135,v120\n"
".Lfk_rare2_1:\n"
"v_max3_f32 v119,v16,v17,v18\n"
"v_max3_f32 v120,v19,v20,v21\n"
"v_max3_f32 v119,v119,v22,v23\n"
"v_max_f32 v119,v119,v120\n"
"v_mov_b32 v120,v119\n"
"s_nop 1\n"
"v_permlane16_swap_b32 v120,v119\n"
"v_max_f32 v119,v119,v120\n"
"v_mov_b32 v120,v119\n"
"s_nop 1\n"
"v_permlane32_swap_b32 v120,v119\n"
"v_max_f32 v119,v119,v120\n"
"s_bitcmp1_b32 s90,2\n"
"s_cselect_b32 s82,0xff800000,0\n"
"v_add_f32 v119,0x41980000,v119\n"
"v_max_f32 v119,s82,v119\n"
"v_cmp_lt_f32 vcc,s17,|v119|\n"
"s_nop 0\n"
"s_cbranch_vccz .Lfk_rare2_2\n"
"v_ceil_f32 v119,v119\n"
"v_sub_f32 v16,v16,v119\n"
"v_sub_f32 v17,v17,v119\n"
"v_sub_f32 v18,v18,v119\n"
"v_sub_f32 v19,v19,v119\n"
"v_sub_f32 v20,v20,v119\n"
"v_sub_f32 v21,v21,v119\n"
"v_sub_f32 v22,v22,v119\n"
"v_sub_f32 v23,v23,v119\n"
"v_sub_f32 v104,v104,v119\n"
"v_sub_f32 v105,v105,v119\n"
"v_sub_f32 v106,v106,v119\n"
"v_sub_f32 v107,v107,v119\n"
"v_sub_f32 v48,v48,v119\n"
"v_sub_f32 v49,v49,v119\n"
"v_sub_f32 v50,v50,v119\n"
"v_sub_f32 v51,v51,v119\n"
"v_sub_f32 v52,v52,v119\n"
"v_sub_f32 v53,v53,v119\n"
"v_sub_f32 v54,v54,v119\n"
"v_sub_f32 v55,v55,v119\n"
"v_exp_f32 v16,v16\n"
"v_exp_f32 v17,v17\n"
"v_exp_f32 v18,v18\n"
"v_exp_f32 v19,v19\n"
"v_exp_f32 v20,v20\n"
"v_exp_f32 v21,v21\n"
"v_exp_f32 v22,v22\n"
"v_exp_f32 v23,v23\n"
"v_cvt_pk_bf16_f32 v72,v16,v17\n"
"v_cvt_pk_bf16_f32 v73,v18,v19\n"
"v_cvt_pk_bf16_f32 v74,v20,v21\n"
"v_cvt_pk_bf16_f32 v75,v22,v23\n"
"s_cbranch_scc1 .Lfk_rare2_2\n"
"v_exp_f32 v119,-v119\n"
"s_nop 0\n"
"v_mul_f32 v192,v192,v119\n"
"v_mul_f32 v193,v193,v119\n"
"v_mul_f32 v194,v194,v119\n"
"v_mul_f32 v195,v195,v119\n"
"v_mul_f32 v196,v196,v119\n"
"v_mul_f32 v197,v197,v119\n"
"v_mul_f32 v198,v198,v119\n"
"v_mul_f32 v199,v199,v119\n"
"v_mul_f32 v200,v200,v119\n"
"v_mul_f32 v201,v201,v119\n"
"v_mul_f32 v202,v202,v119\n"
"v_mul_f32 v203,v203,v119\n"
"v_mul_f32 v204,v204,v119\n"
"v_mul_f32 v205,v205,v119\n"
"v_mul_f32 v206,v206,v119\n"
"v_mul_f32 v207,v207,v119\n"
"v_mul_f32 v208,v208,v119\n"
"v_mul_f32 v209,v209,v119\n"
"v_mul_f32 v210,v210,v119\n"
"v_mul_f32 v211,v211,v119\n"
"v_mul_f32 v212,v212,v119\n"
"v_mul_f32 v213,v213,v119\n"
"v_mul_f32 v214,v214,v119\n"
"v_mul_f32 v215,v215,v119\n"
"v_mul_f32 v216,v216,v119\n"
"v_mul_f32 v217,v217,v119\n"
"v_mul_f32 v218,v218,v119\n"
"v_mul_f32 v219,v219,v119\n"
"v_mul_f32 v220,v220,v119\n"
"v_mul_f32 v221,v221,v119\n"
"v_mul_f32 v222,v222,v119\n"
"v_mul_f32 v223,v223,v119\n"
"v_accvgpr_read_b32 v120,a136\n"
"v_mul_f32 v120,v120,v119\n"
"v_accvgpr_write_b32 a136,v120\n"
"v_accvgpr_read_b32 v120,a137\n"
"v_mul_f32 v120,v120,v119\n"
"v_accvgpr_write_b32 a137,v120\n"
"v_accvgpr_read_b32 v120,a138\n"
"v_mul_f32 v120,v120,v119\n"
"v_accvgpr_write_b32 a138,v120\n"
"v_accvgpr_read_b32 v120,a139\n"
"v_mul_f32 v120,v120,v119\n"
"v_accvgpr_write_b32 a139,v120\n"
".Lfk_rare2_2:\n"
"v_max3_f32 v119,v24,v25,v26\n"
"v_max3_f32 v120,v27,v28,v29\n"
"v_max3_f32 v119,v119,v30,v31\n"
"v_max_f32 v119,v119,v120\n"
"v_mov_b32 v120,v119\n"
"s_nop 1\n"
"v_permlane16_swap_b32 v120,v119\n"
"v_max_f32 v119,v119,v120\n"
"v_mov_b32 v120,v119\n"
"s_nop 1\n"
"v_permlane32_swap_b32 v120,v119\n"
"v_max_f32 v119,v119,v120\n"
"s_bitcmp1_b32 s90,3\n"
"s_cselect_b32 s82,0xff800000,0\n"
"v_add_f32 v119,0x41980000,v119\n"
"v_max_f32 v119,s82,v119\n"
"v_cmp_lt_f32 vcc,s17,|v119|\n"
"s_nop 0\n"
"s_cbranch_vccz .Lfk_rare2_3\n"
"v_ceil_f32 v119,v119\n"
"v_sub_f32 v24,v24,v119\n"
"v_sub_f32 v25,v25,v119\n"
"v_sub_f32 v26,v26,v119\n"
"v_sub_f32 v27,v27,v119\n"
"v_sub_f32 v28,v28,v119\n"
"v_sub_f32 v29,v29,v119\n"
"v_sub_f32 v30,v30,v119\n"
"v_sub_f32 v31,v31,v119\n"
"v_sub_f32 v108,v108,v119\n"
"v_sub_f32 v109,v109,v119\n"
"v_sub_f32 v110,v110,v119\n"
"v_sub_f32 v111,v111,v119\n"
"v_sub_f32 v56,v56,v119\n"
"v_sub_f32 v57,v57,v119\n"
"v_sub_f32 v58,v58,v119\n"
"v_sub_f32 v59,v59,v119\n"
"v_sub_f32 v60,v60,v119\n"
"v_sub_f32 v61,v61,v119\n"
"v_sub_f32 v62,v62,v119\n"
"v_sub_f32 v63,v63,v119\n"
"v_exp_f32 v24,v24\n"
"v_exp_f32 v25,v25\n"
"v_exp_f32 v26,v26\n"
"v_exp_f32 v27,v27\n"
"v_exp_f32 v28,v28\n"
"v_exp_f32 v29,v29\n"
"v_exp_f32 v30,v30\n"
"v_exp_f32 v31,v31\n"
"v_cvt_pk_bf16_f32 v76,v24,v25\n"
"v_cvt_pk_bf16_f32 v77,v26,v27\n"
"v_cvt_pk_bf16_f32 v78,v28,v29\n"
"v_cvt_pk_bf16_f32 v79,v30,v31\n"
"s_cbranch_scc1 .Lfk_rare2_3\n"
"v_exp_f32 v119,-v119\n"
"s_nop 0\n"
"v_mul_f32 v224,v224,v119\n"
"v_mul_f32 v225,v225,v119\n"
"v_mul_f32 v226,v226,v119\n"
"v_mul_f32 v227,v227,v119\n"
"v_mul_f32 v228,v228,v119\n"
"v_mul_f32 v229,v229,v119\n"
"v_mul_f32 v230,v230,v119\n"
"v_mul_f32 v231,v231,v119\n"
"v_mul_f32 v232,v232,v119\n"
"v_mul_f32 v233,v233,v119\n"
"v_mul_f32 v234,v234,v119\n"
"v_mul_f32 v235,v235,v119\n"
"v_mul_f32 v236,v236,v119\n"
"v_mul_f32 v237,v237,v119\n"
"v_mul_f32 v238,v238,v119\n"
"v_mul_f32 v239,v239,v119\n"
"v_mul_f32 v240,v240,v119\n"
"v_mul_f32 v241,v241,v119\n"
"v_mul_f32 v242,v242,v119\n"
"v_mul_f32 v243,v243,v119\n"
"v_mul_f32 v244,v244,v119\n"
"v_mul_f32 v245,v245,v119\n"
"v_mul_f32 v246,v246,v119\n"
"v_mul_f32 v247,v247,v119\n"
"v_mul_f32 v248,v248,v119\n"
"v_mul_f32 v249,v249,v119\n"
"v_mul_f32 v250,v250,v119\n"
"v_mul_f32 v251,v251,v119\n"
"v_mul_f32 v252,v252,v119\n"
"v_mul_f32 v253,v253,v119\n"
"v_mul_f32 v254,v254,v119\n"
"v_mul_f32 v255,v255,v119\n"
"v_accvgpr_read_b32 v120,a140\n"
"v_mul_f32 v120,v120,v119\n"
"v_accvgpr_write_b32 a140,v120\n"
"v_accvgpr_read_b32 v120,a141\n"
"v_mul_f32 v120,v120,v119\n"
"v_accvgpr_write_b32 a141,v120\n"
"v_accvgpr_read_b32 v120,a142\n"
"v_mul_f32 v120,v120,v119\n"
"v_accvgpr_write_b32 a142,v120\n"
"v_accvgpr_read_b32 v120,a143\n"
"v_mul_f32 v120,v120,v119\n"
"v_accvgpr_write_b32 a143,v120\n"
".Lfk_rare2_3:\n"
"s_mov_b32 s90,0\n"
"s_nop 1\n"
"s_setpc_b64 s[94:95]\n"
".Lfk_mslow0:\n"
"s_nop 7\n"
"v_min_f32 v119,v98,v104\n"
"v_sub_f32 v120,v119,v104\n"
"v_sub_f32 v119,v119,v98\n"
"v_exp_f32 v120,v120\n"
"v_exp_f32 v119,v119\n"
"v_mul_f32 v192,v192,v120\n"
"v_mul_f32 v193,v193,v120\n"
"v_mul_f32 v194,v194,v120\n"
"v_mul_f32 v195,v195,v120\n"
"v_mul_f32 v196,v196,v120\n"
"v_mul_f32 v197,v197,v120\n"
"v_mul_f32 v198,v198,v120\n"
"v_mul_f32 v199,v199,v120\n"
"v_mul_f32 v200,v200,v120\n"
"v_mul_f32 v201,v201,v120\n"
"v_mul_f32 v202,v202,v120\n"
"v_mul_f32 v203,v203,v120\n"
"v_mul_f32 v204,v204,v120\n"
"v_mul_f32 v205,v205,v120\n"
"v_mul_f32 v206,v206,v120\n"
"v_mul_f32 v207,v207,v120\n"
"v_mul_f32 v208,v208,v120\n"
"v_mul_f32 v209,v209,v120\n"
"v_mul_f32 v210,v210,v120\n"
"v_mul_f32 v211,v211,v120\n"
"v_mul_f32 v212,v212,v120\n"
"v_mul_f32 v213,v213,v120\n"
"v_mul_f32 v214,v214,v120\n"
"v_mul_f32 v215,v215,v120\n"
"v_mul_f32 v216,v216,v120\n"
"v_mul_f32 v217,v217,v120\n"
"v_mul_f32 v218,v218,v120\n"
"v_mul_f32 v219,v219,v120\n"
"v_mul_f32 v220,v220,v120\n"
"v_mul_f32 v221,v221,v120\n"
"v_mul_f32 v222,v222,v120\n"
"v_mul_f32 v223,v223,v120\n"
"v_accvgpr_read_b32 v121,a136\n"
"v_mul_f32 v121,v121,v120\n"
"v_accvgpr_write_b32 a136,v121\n"
"v_mul_f32 v99,v99,v119\n"
"v_accvgpr_read_b32 v121,a64\n"
"v_lshlrev_b32 v120,16,v121\n"
"v_and_b32 v121,0xffff0000,v121\n"
"v_mul_f32 v120,v120,v119\n"
"v_mul_f32 v121,v121,v119\n"
"v_cvt_pk_bf16_f32 v121,v120,v121\n"
"v_accvgpr_write_b32 a64,v121\n"
"v_accvgpr_read_b32 v121,a65\n"
"v_lshlrev_b32 v120,16,v121\n"
"v_and_b32 v121,0xffff0000,v121\n"
"v_mul_f32 v120,v120,v119\n"
"v_mul_f32 v121,v121,v119\n"
"v_cvt_pk_bf16_f32 v121,v120,v121\n"
"v_accvgpr_write_b32 a65,v121\n"
"v_accvgpr_read_b32 v121,a66\n"
"v_lshlrev_b32 v120,16,v121\n"
"v_and_b32 v121,0xffff0000,v121\n"
"v_mul_f32 v120,v120,v119\n"
"v_mul_f32 v121,v121,v119\n"
"v_cvt_pk_bf16_f32 v121,v120,v121\n"
"v_accvgpr_write_b32 a66,v121\n"
"v_accvgpr_read_b32 v121,a67\n"
"v_lshlrev_b32 v120,16,v121\n"
"v_and_b32 v121,0xffff0000,v121\n"
"v_mul_f32 v120,v120,v119\n"
"v_mul_f32 v121,v121,v119\n"
"v_cvt_pk_bf16_f32 v121,v120,v121\n"
"v_accvgpr_write_b32 a67,v121\n"
"v_accvgpr_read_b32 v121,a68\n"
"v_lshlrev_b32 v120,16,v121\n"
"v_and_b32 v121,0xffff0000,v121\n"
"v_mul_f32 v120,v120,v119\n"
"v_mul_f32 v121,v121,v119\n"
"v_cvt_pk_bf16_f32 v121,v120,v121\n"
"v_accvgpr_write_b32 a68,v121\n"
"v_accvgpr_read_b32 v121,a69\n"
"v_lshlrev_b32 v120,16,v121\n"
"v_and_b32 v121,0xffff0000,v121\n"
"v_mul_f32 v120,v120,v119\n"
"v_mul_f32 v121,v121,v119\n"
"v_cvt_pk_bf16_f32 v121,v120,v121\n"
"v_accvgpr_write_b32 a69,v121\n"
"v_accvgpr_read_b32 v121,a70\n"
"v_lshlrev_b32 v120,16,v121\n"
"v_and_b32 v121,0xffff0000,v121\n"
"v_mul_f32 v120,v120,v119\n"
"v_mul_f32 v121,v121,v119\n"
"v_cvt_pk_bf16_f32 v121,v120,v121\n"
"v_accvgpr_write_b32 a70,v121\n"
"v_accvgpr_read_b32 v121,a71\n"
"v_lshlrev_b32 v120,16,v121\n"
"v_and_b32 v121,0xffff0000,v121\n"
"v_mul_f32 v120,v120,v119\n"
"v_mul_f32 v121,v121,v119\n"
"v_cvt_pk_bf16_f32 v121,v120,v121\n"
"v_accvgpr_write_b32 a71,v121\n"
"v_accvgpr_read_b32 v121,a72\n"
"v_lshlrev_b32 v120,16,v121\n"
"v_and_b32 v121,0xffff0000,v121\n"
"v_mul_f32 v120,v120,v119\n"
"v_mul_f32 v121,v121,v119\n"
"v_cvt_pk_bf16_f32 v121,v120,v121\n"
"v_accvgpr_write_b32 a72,v121\n"
"v_accvgpr_read_b32 v121,a73\n"
"v_lshlrev_b32 v120,16,v121\n"
"v_and_b32 v121,0xffff0000,v121\n"
"v_mul_f32 v120,v120,v119\n"
"v_mul_f32 v121,v121,v119\n"
"v_cvt_pk_bf16_f32 v121,v120,v121\n"
"v_accvgpr_write_b32 a73,v121\n"
"v_accvgpr_read_b32 v121,a74\n"
"v_lshlrev_b32 v120,16,v121\n"
"v_and_b32 v121,0xffff0000,v121\n"
"v_mul_f32 v120,v120,v119\n"
"v_mul_f32 v121,v121,v119\n"
"v_cvt_pk_bf16_f32 v121,v120,v121\n"
"v_accvgpr_write_b32 a74,v121\n"
"v_accvgpr_read_b32 v121,a75\n"
"v_lshlrev_b32 v120,16,v121\n"
"v_and_b32 v121,0xffff0000,v121\n"
"v_mul_f32 v120,v120,v119\n"
"v_mul_f32 v121,v121,v119\n"
"v_cvt_pk_bf16_f32 v121,v120,v121\n"
"v_accvgpr_write_b32 a75,v121\n"
"v_accvgpr_read_b32 v121,a76\n"
"v_lshlrev_b32 v120,16,v121\n"
"v_and_b32 v121,0xffff0000,v121\n"
"v_mul_f32 v120,v120,v119\n"
"v_mul_f32 v121,v121,v119\n"
"v_cvt_pk_bf16_f32 v121,v120,v121\n"
"v_accvgpr_write_b32 a76,v121\n"
"v_accvgpr_read_b32 v121,a77\n"
"v_lshlrev_b32 v120,16,v121\n"
"v_and_b32 v121,0xffff0000,v121\n"
"v_mul_f32 v120,v120,v119\n"
"v_mul_f32 v121,v121,v119\n"
"v_cvt_pk_bf16_f32 v121,v120,v121\n"
"v_accvgpr_write_b32 a77,v121\n"
"v_accvgpr_read_b32 v121,a78\n"
"v_lshlrev_b32 v120,16,v121\n"
"v_and_b32 v121,0xffff0000,v121\n"
"v_mul_f32 v120,v120,v119\n"
"v_mul_f32 v121,v121,v119\n"
"v_cvt_pk_bf16_f32 v121,v120,v121\n"
"v_accvgpr_write_b32 a78,v121\n"
"v_accvgpr_read_b32 v121,a79\n"
"v_lshlrev_b32 v120,16,v121\n"
"v_and_b32 v121,0xffff0000,v121\n"
"v_mul_f32 v120,v120,v119\n"
"v_mul_f32 v121,v121,v119\n"
"v_cvt_pk_bf16_f32 v121,v120,v121\n"
"v_accvgpr_write_b32 a79,v121\n"
"s_nop 1\n"
"s_setpc_b64 s[94:95]\n"
".Lfk_rare1:\n"
"s_nop 7\n"
"s_bitcmp1_b32 s91,0\n"
"s_cbranch_scc0 .Lfk_rare1_0\n"
"v_max3_f32 v119,v32,v33,v34\n"
"v_max3_f32 v120,v35,v36,v37\n"
"v_max3_f32 v119,v119,v38,v39\n"
"v_max_f32 v119,v119,v120\n"
"v_mov_b32 v120,v119\n"
"s_nop 1\n"
"v_permlane16_swap_b32 v120,v119\n"
"v_max_f32 v119,v119,v120\n"
"v_mov_b32 v120,v119\n"
"s_nop 1\n"
"v_permlane32_swap_b32 v120,v119\n"
"v_max_f32 v119,v119,v120\n"
"s_bitcmp1_b32 s90,0\n"
"s_cselect_b32 s82,0xff800000,0\n"
"v_add_f32 v119,0x41980000,v119\n"
"v_max_f32 v119,s82,v119\n"
"v_cmp_lt_f32 vcc,s17,|v119|\n"
"s_nop 0\n"
"s_cbranch_vccz .Lfk_rare1_0\n"
"v_ceil_f32 v119,v119\n"
"v_sub_f32 v32,v32,v119\n"
"v_sub_f32 v33,v33,v119\n"
"v_sub_f32 v34,v34,v119\n"
"v_sub_f32 v35,v35,v119\n"
"v_sub_f32 v36,v36,v119\n"
"v_sub_f32 v37,v37,v119\n"
"v_sub_f32 v38,v38,v119\n"
"v_sub_f32 v39,v39,v119\n"
"v_sub_f32 v96,v96,v119\n"
"v_sub_f32 v97,v97,v119\n"
"v_sub_f32 v98,v98,v119\n"
"v_sub_f32 v99,v99,v119\n"
"s_cbranch_scc1 .Lfk_rare1_0\n"
"v_exp_f32 v119,-v119\n"
"s_nop 0\n"
"v_mul_f32 v128,v128,v119\n"
"v_mul_f32 v129,v129,v119\n"
"v_mul_f32 v130,v130,v119\n"
"v_mul_f32 v131,v131,v119\n"
"v_mul_f32 v132,v132,v119\n"
"v_mul_f32 v133,v133,v119\n"
"v_mul_f32 v134,v134,v119\n"
"v_mul_f32 v135,v135,v119\n"
"v_mul_f32 v136,v136,v119\n"
"v_mul_f32 v137,v137,v119\n"
"v_mul_f32 v138,v138,v119\n"
"v_mul_f32 v139,v139,v119\n"
"v_mul_f32 v140,v140,v119\n"
"v_mul_f32 v141,v141,v119\n"
"v_mul_f32 v142,v142,v119\n"
"v_mul_f32 v143,v143,v119\n"
"v_mul_f32 v144,v144,v119\n"
"v_mul_f32 v145,v145,v119\n"
"v_mul_f32 v146,v146,v119\n"
"v_mul_f32 v147,v147,v119\n"
"v_mul_f32 v148,v148,v119\n"
"v_mul_f32 v149,v149,v119\n"
"v_mul_f32 v150,v150,v119\n"
"v_mul_f32 v151,v151,v119\n"
"v_mul_f32 v152,v152,v119\n"
"v_mul_f32 v153,v153,v119\n"
"v_mul_f32 v154,v154,v119\n"
"v_mul_f32 v155,v155,v119\n"
"v_mul_f32 v156,v156,v119\n"
"v_mul_f32 v157,v157,v119\n"
"v_mul_f32 v158,v158,v119\n"
"v_mul_f32 v159,v159,v119\n"
"v_accvgpr_read_b32 v120,a128\n"
"v_mul_f32 v120,v120,v119\n"
"v_accvgpr_write_b32 a128,v120\n"
"v_lshlrev_b32 v120,16,v64\n"
"v_and_b32 v121,0xffff0000,v64\n"
"v_mul_f32 v120,v120,v119\n"
"v_mul_f32 v121,v121,v119\n"
"v_cvt_pk_bf16_f32 v64,v120,v121\n"
"v_accvgpr_read_b32 v120,a129\n"
"v_mul_f32 v120,v120,v119\n"
"v_accvgpr_write_b32 a129,v120\n"
"v_lshlrev_b32 v120,16,v65\n"
"v_and_b32 v121,0xffff0000,v65\n"
"v_mul_f32 v120,v120,v119\n"
"v_mul_f32 v121,v121,v119\n"
"v_cvt_pk_bf16_f32 v65,v120,v121\n"
"v_accvgpr_read_b32 v120,a130\n"
"v_mul_f32 v120,v120,v119\n"
"v_accvgpr_write_b32 a130,v120\n"
"v_lshlrev_b32 v120,16,v66\n"
"v_and_b32 v121,0xffff0000,v66\n"
"v_mul_f32 v120,v120,v119\n"
"v_mul_f32 v121,v121,v119\n"
"v_cvt_pk_bf16_f32 v66,v120,v121\n"
"v_accvgpr_read_b32 v120,a131\n"
"v_mul_f32 v120,v120,v119\n"
"v_accvgpr_write_b32 a131,v120\n"
"v_lshlrev_b32 v120,16,v67\n"
"v_and_b32 v121,0xffff0000,v67\n"
"v_mul_f32 v120,v120,v119\n"
"v_mul_f32 v121,v121,v119\n"
"v_cvt_pk_bf16_f32 v67,v120,v121\n"
".Lfk_rare1_0:\n"
"s_bitcmp1_b32 s91,1\n"
"s_cbranch_scc0 .Lfk_rare1_1\n"
"v_max3_f32 v119,v40,v41,v42\n"
"v_max3_f32 v120,v43,v44,v45\n"
"v_max3_f32 v119,v119,v46,v47\n"
"v_max_f32 v119,v119,v120\n"
"v_mov_b32 v120,v119\n"
"s_nop 1\n"
"v_permlane16_swap_b32 v120,v119\n"
"v_max_f32 v119,v119,v120\n"
"v_mov_b32 v120,v119\n"
"s_nop 1\n"
"v_permlane32_swap_b32 v120,v119\n"
"v_max_f32 v119,v119,v120\n"
"s_bitcmp1_b32 s90,1\n"
"s_cselect_b32 s82,0xff800000,0\n"
"v_add_f32 v119,0x41980000,v119\n"
"v_max_f32 v119,s82,v119\n"
"v_cmp_lt_f32 vcc,s17,|v119|\n"
"s_nop 0\n"
"s_cbranch_vccz .Lfk_rare1_1\n"
"v_ceil_f32 v119,v119\n"
"v_sub_f32 v40,v40,v119\n"
"v_sub_f32 v41,v41,v119\n"
"v_sub_f32 v42,v42,v119\n"
"v_sub_f32 v43,v43,v119\n"
"v_sub_f32 v44,v44,v119\n"
"v_sub_f32 v45,v45,v119\n"
"v_sub_f32 v46,v46,v119\n"
"v_sub_f32 v47,v47,v119\n"
"v_sub_f32 v100,v100,v119\n"
"v_sub_f32 v101,v101,v119\n"
"v_sub_f32 v102,v102,v119\n"
"v_sub_f32 v103,v103,v119\n"
"s_cbranch_scc1 .Lfk_rare1_1\n"
"v_exp_f32 v119,-v119\n"
"s_nop 0\n"
"v_mul_f32 v160,v160,v119\n"
"v_mul_f32 v161,v161,v119\n"
"v_mul_f32 v162,v162,v119\n"
"v_mul_f32 v163,v163,v119\n"
"v_mul_f32 v164,v164,v119\n"
"v_mul_f32 v165,v165,v119\n"
"v_mul_f32 v166,v166,v119\n"
"v_mul_f32 v167,v167,v119\n"
"v_mul_f32 v168,v168,v119\n"
"v_mul_f32 v169,v169,v119\n"
"v_mul_f32 v170,v170,v119\n"
"v_mul_f32 v171,v171,v119\n"
"v_mul_f32 v172,v172,v119\n"
"v_mul_f32 v173,v173,v119\n"
"v_mul_f32 v174,v174,v119\n"
"v_mul_f32 v175,v175,v119\n"
"v_mul_f32 v176,v176,v119\n"
"v_mul_f32 v177,v177,v119\n"
"v_mul_f32 v178,v178,v119\n"
"v_mul_f32 v179,v179,v119\n"
"v_mul_f32 v180,v180,v119\n"
"v_mul_f32 v181,v181,v119\n"
"v_mul_f32 v182,v182,v119\n"
"v_mul_f32 v183,v183,v119\n"
"v_mul_f32 v184,v184,v119\n"
"v_mul_f32 v185,v185,v119\n"
"v_mul_f32 v186,v186,v119\n"
"v_mul_f32 v187,v187,v119\n"
"v_mul_f32 v188,v188,v119\n"
"v_mul_f32 v189,v189,v119\n"
"v_mul_f32 v190,v190,v119\n"
"v_mul_f32 v191,v191,v119\n"
"v_accvgpr_read_b32 v120,a132\n"
"v_mul_f32 v120,v120,v119\n"
"v_accvgpr_write_b32 a132,v120\n"
"v_lshlrev_b32 v120,16,v68\n"
"v_and_b32 v121,0xffff0000,v68\n"
"v_mul_f32 v120,v120,v119\n"
"v_mul_f32 v121,v121,v119\n"
"v_cvt_pk_bf16_f32 v68,v120,v121\n"
"v_accvgpr_read_b32 v120,a133\n"
"v_mul_f32 v120,v120,v119\n"
"v_accvgpr_write_b32 a133,v120\n"
"v_lshlrev_b32 v120,16,v69\n"
"v_and_b32 v121,0xffff0000,v69\n"
"v_mul_f32 v120,v120,v119\n"
"v_mul_f32 v121,v121,v119\n"
"v_cvt_pk_bf16_f32 v69,v120,v121\n"
"v_accvgpr_read_b32 v120,a134\n"
"v_mul_f32 v120,v120,v119\n"
"v_accvgpr_write_b32 a134,v120\n"
"v_lshlrev_b32 v120,16,v70\n"
"v_and_b32 v121,0xffff0000,v70\n"
"v_mul_f32 v120,v120,v119\n"
"v_mul_f32 v121,v121,v119\n"
"v_cvt_pk_bf16_f32 v70,v120,v121\n"
"v_accvgpr_read_b32 v120,a135\n"
"v_mul_f32 v120,v120,v119\n"
"v_accvgpr_write_b32 a135,v120\n"
"v_lshlrev_b32 v120,16,v71\n"
"v_and_b32 v121,0xffff0000,v71\n"
"v_mul_f32 v120,v120,v119\n"
"v_mul_f32 v121,v121,v119\n"
"v_cvt_pk_bf16_f32 v71,v120,v121\n"
".Lfk_rare1_1:\n"
"s_bitcmp1_b32 s91,2\n"
"s_cbranch_scc0 .Lfk_rare1_2\n"
"v_max3_f32 v119,v48,v49,v50\n"
"v_max3_f32 v120,v51,v52,v53\n"
"v_max3_f32 v119,v119,v54,v55\n"
"v_max_f32 v119,v119,v120\n"
"v_mov_b32 v120,v119\n"
"s_nop 1\n"
"v_permlane16_swap_b32 v120,v119\n"
"v_max_f32 v119,v119,v120\n"
"v_mov_b32 v120,v119\n"
"s_nop 1\n"
"v_permlane32_swap_b32 v120,v119\n"
"v_max_f32 v119,v119,v120\n"
"s_bitcmp1_b32 s90,2\n"
"s_cselect_b32 s82,0xff800000,0\n"
"v_add_f32 v119,0x41980000,v119\n"
"v_max_f32 v119,s82,v119\n"
"v_cmp_lt_f32 vcc,s17,|v119|\n"
"s_nop 0\n"
"s_cbranch_vccz .Lfk_rare1_2\n"
"v_ceil_f32 v119,v119\n"
"v_sub_f32 v48,v48,v119\n"
"v_sub_f32 v49,v49,v119\n"
"v_sub_f32 v50,v50,v119\n"
"v_sub_f32 v51,v51,v119\n"
"v_sub_f32 v52,v52,v119\n"
"v_sub_f32 v53,v53,v119\n"
"v_sub_f32 v54,v54,v119\n"
"v_sub_f32 v55,v55,v119\n"
"v_sub_f32 v104,v104,v119\n"
"v_sub_f32 v105,v105,v119\n"
"v_sub_f32 v106,v106,v119\n"
"v_sub_f32 v107,v107,v119\n"
"s_cbranch_scc1 .Lfk_rare1_2\n"
"v_exp_f32 v119,-v119\n"
"s_nop 0\n"
"v_mul_f32 v192,v192,v119\n"
"v_mul_f32 v193,v193,v119\n"
"v_mul_f32 v194,v194,v119\n"
"v_mul_f32 v195,v195,v119\n"
"v_mul_f32 v196,v196,v119\n"
"v_mul_f32 v197,v197,v119\n"
"v_mul_f32 v198,v198,v119\n"
"v_mul_f32 v199,v199,v119\n"
"v_mul_f32 v200,v200,v119\n"
"v_mul_f32 v201,v201,v119\n"
"v_mul_f32 v202,v202,v119\n"
"v_mul_f32 v203,v203,v119\n"
"v_mul_f32 v204,v204,v119\n"
"v_mul_f32 v205,v205,v119\n"
"v_mul_f32 v206,v206,v119\n"
"v_mul_f32 v207,v207,v119\n"
"v_mul_f32 v208,v208,v119\n"
"v_mul_f32 v209,v209,v119\n"
"v_mul_f32 v210,v210,v119\n"
"v_mul_f32 v211,v211,v119\n"
"v_mul_f32 v212,v212,v119\n"
"v_mul_f32 v213,v213,v119\n"
"v_mul_f32 v214,v214,v119\n"
"v_mul_f32 v215,v215,v119\n"
"v_mul_f32 v216,v216,v119\n"
"v_mul_f32 v217,v217,v119\n"
"v_mul_f32 v218,v218,v119\n"
"v_mul_f32 v219,v219,v119\n"
"v_mul_f32 v220,v220,v119\n"
"v_mul_f32 v221,v221,v119\n"
"v_mul_f32 v222,v222,v119\n"
"v_mul_f32 v223,v223,v119\n"
"v_accvgpr_read_b32 v120,a136\n"
"v_mul_f32 v120,v120,v119\n"
"v_accvgpr_write_b32 a136,v120\n"
"v_lshlrev_b32 v120,16,v72\n"
"v_and_b32 v121,0xffff0000,v72\n"
"v_mul_f32 v120,v120,v119\n"
"v_mul_f32 v121,v121,v119\n"
"v_cvt_pk_bf16_f32 v72,v120,v121\n"
"v_accvgpr_read_b32 v120,a137\n"
"v_mul_f32 v120,v120,v119\n"
"v_accvgpr_write_b32 a137,v120\n"
"v_lshlrev_b32 v120,16,v73\n"
"v_and_b32 v121,0xffff0000,v73\n"
"v_mul_f32 v120,v120,v119\n"
"v_mul_f32 v121,v121,v119\n"
"v_cvt_pk_bf16_f32 v73,v120,v121\n"
"v_accvgpr_read_b32 v120,a138\n"
"v_mul_f32 v120,v120,v119\n"
"v_accvgpr_write_b32 a138,v120\n"
"v_lshlrev_b32 v120,16,v74\n"
"v_and_b32 v121,0xffff0000,v74\n"
"v_mul_f32 v120,v120,v119\n"
"v_mul_f32 v121,v121,v119\n"
"v_cvt_pk_bf16_f32 v74,v120,v121\n"
"v_accvgpr_read_b32 v120,a139\n"
"v_mul_f32 v120,v120,v119\n"
"v_accvgpr_write_b32 a139,v120\n"
"v_lshlrev_b32 v120,16,v75\n"
"v_and_b32 v121,0xffff0000,v75\n"
"v_mul_f32 v120,v120,v119\n"
"v_mul_f32 v121,v121,v119\n"
"v_cvt_pk_bf16_f32 v75,v120,v121\n"
".Lfk_rare1_2:\n"
"s_bitcmp1_b32 s91,3\n"
"s_cbranch_scc0 .Lfk_rare1_3\n"
"v_max3_f32 v119,v56,v57,v58\n"
"v_max3_f32 v120,v59,v60,v61\n"
"v_max3_f32 v119,v119,v62,v63\n"
"v_max_f32 v119,v119,v120\n"
"v_mov_b32 v120,v119\n"
"s_nop 1\n"
"v_permlane16_swap_b32 v120,v119\n"
"v_max_f32 v119,v119,v120\n"
"v_mov_b32 v120,v119\n"
"s_nop 1\n"
"v_permlane32_swap_b32 v120,v119\n"
"v_max_f32 v119,v119,v120\n"
"s_bitcmp1_b32 s90,3\n"
"s_cselect_b32 s82,0xff800000,0\n"
"v_add_f32 v119,0x41980000,v119\n"
"v_max_f32 v119,s82,v119\n"
"v_cmp_lt_f32 vcc,s17,|v119|\n"
"s_nop 0\n"
"s_cbranch_vccz .Lfk_rare1_3\n"
"v_ceil_f32 v119,v119\n"
"v_sub_f32 v56,v56,v119\n"
"v_sub_f32 v57,v57,v119\n"
"v_sub_f32 v58,v58,v119\n"
"v_sub_f32 v59,v59,v119\n"
"v_sub_f32 v60,v60,v119\n"
"v_sub_f32 v61,v61,v119\n"
"v_sub_f32 v62,v62,v119\n"
"v_sub_f32 v63,v63,v119\n"
"v_sub_f32 v108,v108,v119\n"
"v_sub_f32 v109,v109,v119\n"
"v_sub_f32 v110,v110,v119\n"
"v_sub_f32 v111,v111,v119\n"
"s_cbranch_scc1 .Lfk_rare1_3\n"
"v_exp_f32 v119,-v119\n"
"s_nop 0\n"
"v_mul_f32 v224,v224,v119\n"
"v_mul_f32 v225,v225,v119\n"
"v_mul_f32 v226,v226,v119\n"
"v_mul_f32 v227,v227,v119\n"
"v_mul_f32 v228,v228,v119\n"
"v_mul_f32 v229,v229,v119\n"
"v_mul_f32 v230,v230,v119\n"
"v_mul_f32 v231,v231,v119\n"
"v_mul_f32 v232,v232,v119\n"
"v_mul_f32 v233,v233,v119\n"
"v_mul_f32 v234,v234,v119\n"
"v_mul_f32 v235,v235,v119\n"
"v_mul_f32 v236,v236,v119\n"
"v_mul_f32 v237,v237,v119\n"
"v_mul_f32 v238,v238,v119\n"
"v_mul_f32 v239,v239,v119\n"
"v_mul_f32 v240,v240,v119\n"
"v_mul_f32 v241,v241,v119\n"
"v_mul_f32 v242,v242,v119\n"
"v_mul_f32 v243,v243,v119\n"
"v_mul_f32 v244,v244,v119\n"
"v_mul_f32 v245,v245,v119\n"
"v_mul_f32 v246,v246,v119\n"
"v_mul_f32 v247,v247,v119\n"
"v_mul_f32 v248,v248,v119\n"
"v_mul_f32 v249,v249,v119\n"
"v_mul_f32 v250,v250,v119\n"
"v_mul_f32 v251,v251,v119\n"
"v_mul_f32 v252,v252,v119\n"
"v_mul_f32 v253,v253,v119\n"
"v_mul_f32 v254,v254,v119\n"
"v_mul_f32 v255,v255,v119\n"
"v_accvgpr_read_b32 v120,a140\n"
"v_mul_f32 v120,v120,v119\n"
"v_accvgpr_write_b32 a140,v120\n"
"v_lshlrev_b32 v120,16,v76\n"
"v_and_b32 v121,0xffff0000,v76\n"
"v_mul_f32 v120,v120,v119\n"
"v_mul_f32 v121,v121,v119\n"
"v_cvt_pk_bf16_f32 v76,v120,v121\n"
"v_accvgpr_read_b32 v120,a141\n"
"v_mul_f32 v120,v120,v119\n"
"v_accvgpr_write_b32 a141,v120\n"
"v_lshlrev_b32 v120,16,v77\n"
"v_and_b32 v121,0xffff0000,v77\n"
"v_mul_f32 v120,v120,v119\n"
"v_mul_f32 v121,v121,v119\n"
"v_cvt_pk_bf16_f32 v77,v120,v121\n"
"v_accvgpr_read_b32 v120,a142\n"
"v_mul_f32 v120,v120,v119\n"
"v_accvgpr_write_b32 a142,v120\n"
"v_lshlrev_b32 v120,16,v78\n"
"v_and_b32 v121,0xffff0000,v78\n"
"v_mul_f32 v120,v120,v119\n"
"v_mul_f32 v121,v121,v119\n"
"v_cvt_pk_bf16_f32 v78,v120,v121\n"
"v_accvgpr_read_b32 v120,a143\n"
"v_mul_f32 v120,v120,v119\n"
"v_accvgpr_write_b32 a143,v120\n"
"v_lshlrev_b32 v120,16,v79\n"
"v_and_b32 v121,0xffff0000,v79\n"
"v_mul_f32 v120,v120,v119\n"
"v_mul_f32 v121,v121,v119\n"
"v_cvt_pk_bf16_f32 v79,v120,v121\n"
".Lfk_rare1_3:\n"
"s_mov_b32 s90,0\n"
"s_nop 1\n"
"s_setpc_b64 s[94:95]\n"
".Lfk_rare3:\n"
"s_nop 7\n"
"v_max3_f32 v119,v32,v33,v34\n"
"v_max3_f32 v120,v35,v36,v37\n"
"v_max3_f32 v119,v119,v38,v39\n"
"v_max_f32 v119,v119,v120\n"
"v_mov_b32 v120,v119\n"
"s_nop 1\n"
"v_permlane16_swap_b32 v120,v119\n"
"v_max_f32 v119,v119,v120\n"
"v_mov_b32 v120,v119\n"
"s_nop 1\n"
"v_permlane32_swap_b32 v120,v119\n"
"v_max_f32 v119,v119,v120\n"
"s_bitcmp1_b32 s90,0\n"
"s_cselect_b32 s82,0xff800000,0\n"
"v_add_f32 v119,0x41980000,v119\n"
"v_max_f32 v119,s82,v119\n"
"v_cmp_lt_f32 vcc,s17,|v119|\n"
"s_nop 0\n"
"s_cbranch_vccz .Lfk_rare3_0\n"
"v_ceil_f32 v119,v119\n"
"v_sub_f32 v32,v32,v119\n"
"v_sub_f32 v33,v33,v119\n"
"v_sub_f32 v34,v34,v119\n"
"v_sub_f32 v35,v35,v119\n"
"v_sub_f32 v36,v36,v119\n"
"v_sub_f32 v37,v37,v119\n"
"v_sub_f32 v38,v38,v119\n"
"v_sub_f32 v39,v39,v119\n"
"v_sub_f32 v96,v96,v119\n"
"v_sub_f32 v97,v97,v119\n"
"v_sub_f32 v98,v98,v119\n"
"v_sub_f32 v99,v99,v119\n"
"v_sub_f32 v0,v0,v119\n"
"v_sub_f32 v1,v1,v119\n"
"v_sub_f32 v2,v2,v119\n"
"v_sub_f32 v3,v3,v119\n"
"v_sub_f32 v4,v4,v119\n"
"v_sub_f32 v5,v5,v119\n"
"v_sub_f32 v6,v6,v119\n"
"v_sub_f32 v7,v7,v119\n"
"v_exp_f32 v32,v32\n"
"v_exp_f32 v33,v33\n"
"v_exp_f32 v34,v34\n"
"v_exp_f32 v35,v35\n"
"v_exp_f32 v36,v36\n"
"v_exp_f32 v37,v37\n"
"v_exp_f32 v38,v38\n"
"v_exp_f32 v39,v39\n"
"v_cvt_pk_bf16_f32 v80,v32,v33\n"
"v_cvt_pk_bf16_f32 v81,v34,v35\n"
"v_cvt_pk_bf16_f32 v82,v36,v37\n"
"v_cvt_pk_bf16_f32 v83,v38,v39\n"
"s_cbranch_scc1 .Lfk_rare3_0\n"
"v_exp_f32 v119,-v119\n"
"s_nop 0\n"
"v_mul_f32 v128,v128,v119\n"
"v_mul_f32 v129,v129,v119\n"
"v_mul_f32 v130,v130,v119\n"
"v_mul_f32 v131,v131,v119\n"
"v_mul_f32 v132,v132,v119\n"
"v_mul_f32 v133,v133,v119\n"
"v_mul_f32 v134,v134,v119\n"
"v_mul_f32 v135,v135,v119\n"
"v_mul_f32 v136,v136,v119\n"
"v_mul_f32 v137,v137,v119\n"
"v_mul_f32 v138,v138,v119\n"
"v_mul_f32 v139,v139,v119\n"
"v_mul_f32 v140,v140,v119\n"
"v_mul_f32 v141,v141,v119\n"
"v_mul_f32 v142,v142,v119\n"
"v_mul_f32 v143,v143,v119\n"
"v_mul_f32 v144,v144,v119\n"
"v_mul_f32 v145,v145,v119\n"
"v_mul_f32 v146,v146,v119\n"
"v_mul_f32 v147,v147,v119\n"
"v_mul_f32 v148,v148,v119\n"
"v_mul_f32 v149,v149,v119\n"
"v_mul_f32 v150,v150,v119\n"
"v_mul_f32 v151,v151,v119\n"
"v_mul_f32 v152,v152,v119\n"
"v_mul_f32 v153,v153,v119\n"
"v_mul_f32 v154,v154,v119\n"
"v_mul_f32 v155,v155,v119\n"
"v_mul_f32 v156,v156,v119\n"
"v_mul_f32 v157,v157,v119\n"
"v_mul_f32 v158,v158,v119\n"
"v_mul_f32 v159,v159,v119\n"
"v_accvgpr_read_b32 v120,a128\n"
"v_mul_f32 v120,v120,v119\n"
"v_accvgpr_write_b32 a128,v120\n"
"v_accvgpr_read_b32 v120,a129\n"
"v_mul_f32 v120,v120,v119\n"
"v_accvgpr_write_b32 a129,v120\n"
"v_accvgpr_read_b32 v120,a130\n"
"v_mul_f32 v120,v120,v119\n"
"v_accvgpr_write_b32 a130,v120\n"
"v_accvgpr_read_b32 v120,a131\n"
"v_mul_f32 v120,v120,v119\n"
"v_accvgpr_write_b32 a131,v120\n"
".Lfk_rare3_0:\n"
"v_max3_f32 v119,v40,v41,v42\n"
"v_max3_f32 v120,v43,v44,v45\n"
"v_max3_f32 v119,v119,v46,v47\n"
"v_max_f32 v119,v119,v120\n"
"v_mov_b32 v120,v119\n"
"s_nop 1\n"
"v_permlane16_swap_b32 v120,v119\n"
"v_max_f32 v119,v119,v120\n"
"v_mov_b32 v120,v119\n"
"s_nop 1\n"
"v_permlane32_swap_b32 v120,v119\n"
"v_max_f32 v119,v119,v120\n"
"s_bitcmp1_b32 s90,1\n"
"s_cselect_b32 s82,0xff800000,0\n"
"v_add_f32 v119,0x41980000,v119\n"
"v_max_f32 v119,s82,v119\n"
"v_cmp_lt_f32 vcc,s17,|v119|\n"
"s_nop 0\n"
"s_cbranch_vccz .Lfk_rare3_1\n"
"v_ceil_f32 v119,v119\n"
"v_sub_f32 v40,v40,v119\n"
"v_sub_f32 v41,v41,v119\n"
"v_sub_f32 v42,v42,v119\n"
"v_sub_f32 v43,v43,v119\n"
"v_sub_f32 v44,v44,v119\n"
"v_sub_f32 v45,v45,v119\n"
"v_sub_f32 v46,v46,v119\n"
"v_sub_f32 v47,v47,v119\n"
"v_sub_f32 v100,v100,v119\n"
"v_sub_f32 v101,v101,v119\n"
"v_sub_f32 v102,v102,v119\n"
"v_sub_f32 v103,v103,v119\n"
"v_sub_f32 v8,v8,v119\n"
"v_sub_f32 v9,v9,v119\n"
"v_sub_f32 v10,v10,v119\n"
"v_sub_f32 v11,v11,v119\n"
"v_sub_f32 v12,v12,v119\n"
"v_sub_f32 v13,v13,v119\n"
"v_sub_f32 v14,v14,v119\n"
"v_sub_f32 v15,v15,v119\n"
"v_exp_f32 v40,v40\n"
"v_exp_f32 v41,v41\n"
"v_exp_f32 v42,v42\n"
"v_exp_f32 v43,v43\n"
"v_exp_f32 v44,v44\n"
"v_exp_f32 v45,v45\n"
"v_exp_f32 v46,v46\n"
"v_exp_f32 v47,v47\n"
"v_cvt_pk_bf16_f32 v84,v40,v41\n"
"v_cvt_pk_bf16_f32 v85,v42,v43\n"
"v_cvt_pk_bf16_f32 v86,v44,v45\n"
"v_cvt_pk_bf16_f32 v87,v46,v47\n"
"s_cbranch_scc1 .Lfk_rare3_1\n"
"v_exp_f32 v119,-v119\n"
"s_nop 0\n"
"v_mul_f32 v160,v160,v119\n"
"v_mul_f32 v161,v161,v119\n"
"v_mul_f32 v162,v162,v119\n"
"v_mul_f32 v163,v163,v119\n"
"v_mul_f32 v164,v164,v119\n"
"v_mul_f32 v165,v165,v119\n"
"v_mul_f32 v166,v166,v119\n"
"v_mul_f32 v167,v167,v119\n"
"v_mul_f32 v168,v168,v119\n"
"v_mul_f32 v169,v169,v119\n"
"v_mul_f32 v170,v170,v119\n"
"v_mul_f32 v171,v171,v119\n"
"v_mul_f32 v172,v172,v119\n"
"v_mul_f32 v173,v173,v119\n"
"v_mul_f32 v174,v174,v119\n"
"v_mul_f32 v175,v175,v119\n"
"v_mul_f32 v176,v176,v119\n"
"v_mul_f32 v177,v177,v119\n"
"v_mul_f32 v178,v178,v119\n"
"v_mul_f32 v179,v179,v119\n"
"v_mul_f32 v180,v180,v119\n"
"v_mul_f32 v181,v181,v119\n"
"v_mul_f32 v182,v182,v119\n"
"v_mul_f32 v183,v183,v119\n"
"v_mul_f32 v184,v184,v119\n"
"v_mul_f32 v185,v185,v119\n"
"v_mul_f32 v186,v186,v119\n"
"v_mul_f32 v187,v187,v119\n"
"v_mul_f32 v188,v188,v119\n"
"v_mul_f32 v189,v189,v119\n"
"v_mul_f32 v190,v190,v119\n"
"v_mul_f32 v191,v191,v119\n"
"v_accvgpr_read_b32 v120,a132\n"
"v_mul_f32 v120,v120,v119\n"
"v_accvgpr_write_b32 a132,v120\n"
"v_accvgpr_read_b32 v120,a133\n"
"v_mul_f32 v120,v120,v119\n"
"v_accvgpr_write_b32 a133,v120\n"
"v_accvgpr_read_b32 v120,a134\n"
"v_mul_f32 v120,v120,v119\n"
"v_accvgpr_write_b32 a134,v120\n"
"v_accvgpr_read_b32 v120,a135\n"
"v_mul_f32 v120,v120,v119\n"
"v_accvgpr_write_b32 a135,v120\n"
".Lfk_rare3_1:\n"
"v_max3_f32 v119,v48,v49,v50\n"
"v_max3_f32 v120,v51,v52,v53\n"
"v_max3_f32 v119,v119,v54,v55\n"
"v_max_f32 v119,v119,v120\n"
"v_mov_b32 v120,v119\n"
"s_nop 1\n"
"v_permlane16_swap_b32 v120,v119\n"
"v_max_f32 v119,v119,v120\n"
"v_mov_b32 v120,v119\n"
"s_nop 1\n"
"v_permlane32_swap_b32 v120,v119\n"
"v_max_f32 v119,v119,v120\n"
"s_bitcmp1_b32 s90,2\n"
"s_cselect_b32 s82,0xff800000,0\n"
"v_add_f32 v119,0x41980000,v119\n"
"v_max_f32 v119,s82,v119\n"
"v_cmp_lt_f32 vcc,s17,|v119|\n"
"s_nop 0\n"
"s_cbranch_vccz .Lfk_rare3_2\n"
"v_ceil_f32 v119,v119\n"
"v_sub_f32 v48,v48,v119\n"
"v_sub_f32 v49,v49,v119\n"
"v_sub_f32 v50,v50,v119\n"
"v_sub_f32 v51,v51,v119\n"
"v_sub_f32 v52,v52,v119\n"
"v_sub_f32 v53,v53,v119\n"
"v_sub_f32 v54,v54,v119\n"
"v_sub_f32 v55,v55,v119\n"
"v_sub_f32 v104,v104,v119\n"
"v_sub_f32 v105,v105,v119\n"
"v_sub_f32 v106,v106,v119\n"
"v_sub_f32 v107,v107,v119\n"
"v_sub_f32 v16,v16,v119\n"
"v_sub_f32 v17,v17,v119\n"
"v_sub_f32 v18,v18,v119\n"
"v_sub_f32 v19,v19,v119\n"
"v_sub_f32 v20,v20,v119\n"
"v_sub_f32 v21,v21,v119\n"
"v_sub_f32 v22,v22,v119\n"
"v_sub_f32 v23,v23,v119\n"
"v_exp_f32 v48,v48\n"
"v_exp_f32 v49,v49\n"
"v_exp_f32 v50,v50\n"
"v_exp_f32 v51,v51\n"
"v_exp_f32 v52,v52\n"
"v_exp_f32 v53,v53\n"
"v_exp_f32 v54,v54\n"
"v_exp_f32 v55,v55\n"
"v_cvt_pk_bf16_f32 v88,v48,v49\n"
"v_cvt_pk_bf16_f32 v89,v50,v51\n"
"v_cvt_pk_bf16_f32 v90,v52,v53\n"
"v_cvt_pk_bf16_f32 v91,v54,v55\n"
"s_cbranch_scc1 .Lfk_rare3_2\n"
"v_exp_f32 v119,-v119\n"
"s_nop 0\n"
"v_mul_f32 v192,v192,v119\n"
"v_mul_f32 v193,v193,v119\n"
"v_mul_f32 v194,v194,v119\n"
"v_mul_f32 v195,v195,v119\n"
"v_mul_f32 v196,v196,v119\n"
"v_mul_f32 v197,v197,v119\n"
"v_mul_f32 v198,v198,v119\n"
"v_mul_f32 v199,v199,v119\n"
"v_mul_f32 v200,v200,v119\n"
"v_mul_f32 v201,v201,v119\n"
"v_mul_f32 v202,v202,v119\n"
"v_mul_f32 v203,v203,v119\n"
"v_mul_f32 v204,v204,v119\n"
"v_mul_f32 v205,v205,v119\n"
"v_mul_f32 v206,v206,v119\n"
"v_mul_f32 v207,v207,v119\n"
"v_mul_f32 v208,v208,v119\n"
"v_mul_f32 v209,v209,v119\n"
"v_mul_f32 v210,v210,v119\n"
"v_mul_f32 v211,v211,v119\n"
"v_mul_f32 v212,v212,v119\n"
"v_mul_f32 v213,v213,v119\n"
"v_mul_f32 v214,v214,v119\n"
"v_mul_f32 v215,v215,v119\n"
"v_mul_f32 v216,v216,v119\n"
"v_mul_f32 v217,v217,v119\n"
"v_mul_f32 v218,v218,v119\n"
"v_mul_f32 v219,v219,v119\n"
"v_mul_f32 v220,v220,v119\n"
"v_mul_f32 v221,v221,v119\n"
"v_mul_f32 v222,v222,v119\n"
"v_mul_f32 v223,v223,v119\n"
"v_accvgpr_read_b32 v120,a136\n"
"v_mul_f32 v120,v120,v119\n"
"v_accvgpr_write_b32 a136,v120\n"
"v_accvgpr_read_b32 v120,a137\n"
"v_mul_f32 v120,v120,v119\n"
"v_accvgpr_write_b32 a137,v120\n"
"v_accvgpr_read_b32 v120,a138\n"
"v_mul_f32 v120,v120,v119\n"
"v_accvgpr_write_b32 a138,v120\n"
"v_accvgpr_read_b32 v120,a139\n"
"v_mul_f32 v120,v120,v119\n"
"v_accvgpr_write_b32 a139,v120\n"
".Lfk_rare3_2:\n"
"v_max3_f32 v119,v56,v57,v58\n"
"v_max3_f32 v120,v59,v60,v61\n"
"v_max3_f32 v119,v119,v62,v63\n"
"v_max_f32 v119,v119,v120\n"
"v_mov_b32 v120,v119\n"
"s_nop 1\n"
"v_permlane16_swap_b32 v120,v119\n"
"v_max_f32 v119,v119,v120\n"
"v_mov_b32 v120,v119\n"
"s_nop 1\n"
"v_permlane32_swap_b32 v120,v119\n"
"v_max_f32 v119,v119,v120\n"
"s_bitcmp1_b32 s90,3\n"
"s_cselect_b32 s82,0xff800000,0\n"
"v_add_f32 v119,0x41980000,v119\n"
"v_max_f32 v119,s82,v119\n"
"v_cmp_lt_f32 vcc,s17,|v119|\n"
"s_nop 0\n"
"s_cbranch_vccz .Lfk_rare3_3\n"
"v_ceil_f32 v119,v119\n"
"v_sub_f32 v56,v56,v119\n"
"v_sub_f32 v57,v57,v119\n"
"v_sub_f32 v58,v58,v119\n"
"v_sub_f32 v59,v59,v119\n"
"v_sub_f32 v60,v60,v119\n"
"v_sub_f32 v61,v61,v119\n"
"v_sub_f32 v62,v62,v119\n"
"v_sub_f32 v63,v63,v119\n"
"v_sub_f32 v108,v108,v119\n"
"v_sub_f32 v109,v109,v119\n"
"v_sub_f32 v110,v110,v119\n"
"v_sub_f32 v111,v111,v119\n"
"v_sub_f32 v24,v24,v119\n"
"v_sub_f32 v25,v25,v119\n"
"v_sub_f32 v26,v26,v119\n"
"v_sub_f32 v27,v27,v119\n"
"v_sub_f32 v28,v28,v119\n"
"v_sub_f32 v29,v29,v119\n"
"v_sub_f32 v30,v30,v119\n"
"v_sub_f32 v31,v31,v119\n"
"v_exp_f32 v56,v56\n"
"v_exp_f32 v57,v57\n"
"v_exp_f32 v58,v58\n"
"v_exp_f32 v59,v59\n"
"v_exp_f32 v60,v60\n"
"v_exp_f32 v61,v61\n"
"v_exp_f32 v62,v62\n"
"v_exp_f32 v63,v63\n"
"v_cvt_pk_bf16_f32 v92,v56,v57\n"
"v_cvt_pk_bf16_f32 v93,v58,v59\n"
"v_cvt_pk_bf16_f32 v94,v60,v61\n"
"v_cvt_pk_bf16_f32 v95,v62,v63\n"
"s_cbranch_scc1 .Lfk_rare3_3\n"
"v_exp_f32 v119,-v119\n"
"s_nop 0\n"
"v_mul_f32 v224,v224,v119\n"
"v_mul_f32 v225,v225,v119\n"
"v_mul_f32 v226,v226,v119\n"
"v_mul_f32 v227,v227,v119\n"
"v_mul_f32 v228,v228,v119\n"
"v_mul_f32 v229,v229,v119\n"
"v_mul_f32 v230,v230,v119\n"
"v_mul_f32 v231,v231,v119\n"
"v_mul_f32 v232,v232,v119\n"
"v_mul_f32 v233,v233,v119\n"
"v_mul_f32 v234,v234,v119\n"
"v_mul_f32 v235,v235,v119\n"
"v_mul_f32 v236,v236,v119\n"
"v_mul_f32 v237,v237,v119\n"
"v_mul_f32 v238,v238,v119\n"
"v_mul_f32 v239,v239,v119\n"
"v_mul_f32 v240,v240,v119\n"
"v_mul_f32 v241,v241,v119\n"
"v_mul_f32 v242,v242,v119\n"
"v_mul_f32 v243,v243,v119\n"
"v_mul_f32 v244,v244,v119\n"
"v_mul_f32 v245,v245,v119\n"
"v_mul_f32 v246,v246,v119\n"
"v_mul_f32 v247,v247,v119\n"
"v_mul_f32 v248,v248,v119\n"
"v_mul_f32 v249,v249,v119\n"
"v_mul_f32 v250,v250,v119\n"
"v_mul_f32 v251,v251,v119\n"
"v_mul_f32 v252,v252,v119\n"
"v_mul_f32 v253,v253,v119\n"
"v_mul_f32 v254,v254,v119\n"
"v_mul_f32 v255,v255,v119\n"
"v_accvgpr_read_b32 v120,a140\n"
"v_mul_f32 v120,v120,v119\n"
"v_accvgpr_write_b32 a140,v120\n"
"v_accvgpr_read_b32 v120,a141\n"
"v_mul_f32 v120,v120,v119\n"
"v_accvgpr_write_b32 a141,v120\n"
"v_accvgpr_read_b32 v120,a142\n"
"v_mul_f32 v120,v120,v119\n"
"v_accvgpr_write_b32 a142,v120\n"
"v_accvgpr_read_b32 v120,a143\n"
"v_mul_f32 v120,v120,v119\n"
"v_accvgpr_write_b32 a143,v120\n"
".Lfk_rare3_3:\n"
"s_mov_b32 s90,0\n"
"s_nop 1\n"
"s_setpc_b64 s[94:95]\n"
".Lfk_mslow1:\n"
"s_nop 7\n"
"v_min_f32 v119,v102,v108\n"
"v_sub_f32 v120,v119,v108\n"
"v_sub_f32 v119,v119,v102\n"
"v_exp_f32 v120,v120\n"
"v_exp_f32 v119,v119\n"
"v_mul_f32 v224,v224,v120\n"
"v_mul_f32 v225,v225,v120\n"
"v_mul_f32 v226,v226,v120\n"
"v_mul_f32 v227,v227,v120\n"
"v_mul_f32 v228,v228,v120\n"
"v_mul_f32 v229,v229,v120\n"
"v_mul_f32 v230,v230,v120\n"
"v_mul_f32 v231,v231,v120\n"
"v_mul_f32 v232,v232,v120\n"
"v_mul_f32 v233,v233,v120\n"
"v_mul_f32 v234,v234,v120\n"
"v_mul_f32 v235,v235,v120\n"
"v_mul_f32 v236,v236,v120\n"
"v_mul_f32 v237,v237,v120\n"
"v_mul_f32 v238,v238,v120\n"
"v_mul_f32 v239,v239,v120\n"
"v_mul_f32 v240,v240,v120\n"
"v_mul_f32 v241,v241,v120\n"
"v_mul_f32 v242,v242,v120\n"
"v_mul_f32 v243,v243,v120\n"
"v_mul_f32 v244,v244,v120\n"
"v_mul_f32 v245,v245,v120\n"
"v_mul_f32 v246,v246,v120\n"
"v_mul_f32 v247,v247,v120\n"
"v_mul_f32 v248,v248,v120\n"
"v_mul_f32 v249,v249,v120\n"
"v_mul_f32 v250,v250,v120\n"
"v_mul_f32 v251,v251,v120\n"
"v_mul_f32 v252,v252,v120\n"
"v_mul_f32 v253,v253,v120\n"
"v_mul_f32 v254,v254,v120\n"
"v_mul_f32 v255,v255,v120\n"
"v_accvgpr_read_b32 v121,a140\n"
"v_mul_f32 v121,v121,v120\n"
"v_accvgpr_write_b32 a140,v121\n"
"v_mul_f32 v103,v103,v119\n"
"v_accvgpr_read_b32 v121,a80\n"
"v_lshlrev_b32 v120,16,v121\n"
"v_and_b32 v121,0xffff0000,v121\n"
"v_mul_f32 v120,v120,v119\n"
"v_mul_f32 v121,v121,v119\n"
"v_cvt_pk_bf16_f32 v121,v120,v121\n"
"v_accvgpr_write_b32 a80,v121\n"
"v_accvgpr_read_b32 v121,a81\n"
"v_lshlrev_b32 v120,16,v121\n"
"v_and_b32 v121,0xffff0000,v121\n"
"v_mul_f32 v120,v120,v119\n"
"v_mul_f32 v121,v121,v119\n"
"v_cvt_pk_bf16_f32 v121,v120,v121\n"
"v_accvgpr_write_b32 a81,v121\n"
"v_accvgpr_read_b32 v121,a82\n"
"v_lshlrev_b32 v120,16,v121\n"
"v_and_b32 v121,0xffff0000,v121\n"
"v_mul_f32 v120,v120,v119\n"
"v_mul_f32 v121,v121,v119\n"
"v_cvt_pk_bf16_f32 v121,v120,v121\n"
"v_accvgpr_write_b32 a82,v121\n"
"v_accvgpr_read_b32 v121,a83\n"
"v_lshlrev_b32 v120,16,v121\n"
"v_and_b32 v121,0xffff0000,v121\n"
"v_mul_f32 v120,v120,v119\n"
"v_mul_f32 v121,v121,v119\n"
"v_cvt_pk_bf16_f32 v121,v120,v121\n"
"v_accvgpr_write_b32 a83,v121\n"
"v_accvgpr_read_b32 v121,a84\n"
"v_lshlrev_b32 v120,16,v121\n"
"v_and_b32 v121,0xffff0000,v121\n"
"v_mul_f32 v120,v120,v119\n"
"v_mul_f32 v121,v121,v119\n"
"v_cvt_pk_bf16_f32 v121,v120,v121\n"
"v_accvgpr_write_b32 a84,v121\n"
"v_accvgpr_read_b32 v121,a85\n"
"v_lshlrev_b32 v120,16,v121\n"
"v_and_b32 v121,0xffff0000,v121\n"
"v_mul_f32 v120,v120,v119\n"
"v_mul_f32 v121,v121,v119\n"
"v_cvt_pk_bf16_f32 v121,v120,v121\n"
"v_accvgpr_write_b32 a85,v121\n"
"v_accvgpr_read_b32 v121,a86\n"
"v_lshlrev_b32 v120,16,v121\n"
"v_and_b32 v121,0xffff0000,v121\n"
"v_mul_f32 v120,v120,v119\n"
"v_mul_f32 v121,v121,v119\n"
"v_cvt_pk_bf16_f32 v121,v120,v121\n"
"v_accvgpr_write_b32 a86,v121\n"
"v_accvgpr_read_b32 v121,a87\n"
"v_lshlrev_b32 v120,16,v121\n"
"v_and_b32 v121,0xffff0000,v121\n"
"v_mul_f32 v120,v120,v119\n"
"v_mul_f32 v121,v121,v119\n"
"v_cvt_pk_bf16_f32 v121,v120,v121\n"
"v_accvgpr_write_b32 a87,v121\n"
"v_accvgpr_read_b32 v121,a88\n"
"v_lshlrev_b32 v120,16,v121\n"
"v_and_b32 v121,0xffff0000,v121\n"
"v_mul_f32 v120,v120,v119\n"
"v_mul_f32 v121,v121,v119\n"
"v_cvt_pk_bf16_f32 v121,v120,v121\n"
"v_accvgpr_write_b32 a88,v121\n"
"v_accvgpr_read_b32 v121,a89\n"
"v_lshlrev_b32 v120,16,v121\n"
"v_and_b32 v121,0xffff0000,v121\n"
"v_mul_f32 v120,v120,v119\n"
"v_mul_f32 v121,v121,v119\n"
"v_cvt_pk_bf16_f32 v121,v120,v121\n"
"v_accvgpr_write_b32 a89,v121\n"
"v_accvgpr_read_b32 v121,a90\n"
"v_lshlrev_b32 v120,16,v121\n"
"v_and_b32 v121,0xffff0000,v121\n"
"v_mul_f32 v120,v120,v119\n"
"v_mul_f32 v121,v121,v119\n"
"v_cvt_pk_bf16_f32 v121,v120,v121\n"
"v_accvgpr_write_b32 a90,v121\n"
"v_accvgpr_read_b32 v121,a91\n"
"v_lshlrev_b32 v120,16,v121\n"
"v_and_b32 v121,0xffff0000,v121\n"
"v_mul_f32 v120,v120,v119\n"
"v_mul_f32 v121,v121,v119\n"
"v_cvt_pk_bf16_f32 v121,v120,v121\n"
"v_accvgpr_write_b32 a91,v121\n"
"v_accvgpr_read_b32 v121,a92\n"
"v_lshlrev_b32 v120,16,v121\n"
"v_and_b32 v121,0xffff0000,v121\n"
"v_mul_f32 v120,v120,v119\n"
"v_mul_f32 v121,v121,v119\n"
"v_cvt_pk_bf16_f32 v121,v120,v121\n"
"v_accvgpr_write_b32 a92,v121\n"
"v_accvgpr_read_b32 v121,a93\n"
"v_lshlrev_b32 v120,16,v121\n"
"v_and_b32 v121,0xffff0000,v121\n"
"v_mul_f32 v120,v120,v119\n"
"v_mul_f32 v121,v121,v119\n"
"v_cvt_pk_bf16_f32 v121,v120,v121\n"
"v_accvgpr_write_b32 a93,v121\n"
"v_accvgpr_read_b32 v121,a94\n"
"v_lshlrev_b32 v120,16,v121\n"
"v_and_b32 v121,0xffff0000,v121\n"
"v_mul_f32 v120,v120,v119\n"
"v_mul_f32 v121,v121,v119\n"
"v_cvt_pk_bf16_f32 v121,v120,v121\n"
"v_accvgpr_write_b32 a94,v121\n"
"v_accvgpr_read_b32 v121,a95\n"
"v_lshlrev_b32 v120,16,v121\n"
"v_and_b32 v121,0xffff0000,v121\n"
"v_mul_f32 v120,v120,v119\n"
"v_mul_f32 v121,v121,v119\n"
"v_cvt_pk_bf16_f32 v121,v120,v121\n"
"v_accvgpr_write_b32 a95,v121\n"
"s_nop 1\n"
"s_setpc_b64 s[94:95]\n"
".Lfk_end:\n"
        : "+{v0}"(tid), "+{s2}"(bid)
        : "{s[0:1]}"(__builtin_amdgcn_kernarg_segment_ptr())
        : "v1","v2","v3","v4","v5","v6","v7","v8","v9","v10","v11","v12","v13","v14","v15","v16","v17","v18","v19","v20","v21","v22","v23","v24","v25","v26","v27","v28","v29","v30","v31","v32","v33","v34","v35","v36","v37","v38","v39","v40","v41","v42","v43","v44","v45","v46","v47","v48","v49","v50","v51","v52","v53","v54","v55","v56","v57","v58","v59","v60","v61","v62","v63","v64","v65","v66","v67","v68","v69","v70","v71","v72","v73","v74","v75","v76","v77","v78","v79","v80","v81","v82","v83","v84","v85","v86","v87","v88","v89","v90","v91","v92","v93","v94","v95","v96","v97","v98","v99","v100","v101","v102","v103","v104","v105","v106","v107","v108","v109","v110","v111","v112","v113","v114","v115","v116","v117","v118","v119","v120","v121","v122","v123","v124","v125","v126","v127","v128","v129","v130","v131","v132","v133","v134","v135","v136","v137","v138","v139","v140","v141","v142","v143","v144","v145","v146","v147","v148","v149","v150","v151","v152","v153","v154","v155","v156","v157","v158","v159","v160","v161","v162","v163","v164","v165","v166","v167","v168","v169","v170","v171","v172","v173","v174","v175","v176","v177","v178","v179","v180","v181","v182","v183","v184","v185","v186","v187","v188","v189","v190","v191","v192","v193","v194","v195","v196","v197","v198","v199","v200","v201","v202","v203","v204","v205","v206","v207","v208","v209","v210","v211","v212","v213","v214","v215","v216","v217","v218","v219","v220","v221","v222","v223","v224","v225","v226","v227","v228","v229","v230","v231","v232","v233","v234","v235","v236","v237","v238","v239","v240","v241","v242","v243","v244","v245","v246","v247","v248","v249","v250","v251","v252","v253","v254","v255",
          "a0","a1","a2","a3","a4","a5","a6","a7","a8","a9","a10","a11","a12","a13","a14","a15","a16","a17","a18","a19","a20","a21","a22","a23","a24","a25","a26","a27","a28","a29","a30","a31","a32","a33","a34","a35","a36","a37","a38","a39","a40","a41","a42","a43","a44","a45","a46","a47","a48","a49","a50","a51","a52","a53","a54","a55","a56","a57","a58","a59","a60","a61","a62","a63","a64","a65","a66","a67","a68","a69","a70","a71","a72","a73","a74","a75","a76","a77","a78","a79","a80","a81","a82","a83","a84","a85","a86","a87","a88","a89","a90","a91","a92","a93","a94","a95","a96","a97","a98","a99","a100","a101","a102","a103","a104","a105","a106","a107","a108","a109","a110","a111","a112","a113","a114","a115","a116","a117","a118","a119","a120","a121","a122","a123","a124","a125","a126","a127","a128","a129","a130","a131","a132","a133","a134","a135","a136","a137","a138","a139","a140","a141","a142","a143","a144","a145","a146","a147","a148","a149","a150","a151","a152","a153","a154","a155","a156","a157","a158","a159","a160","a161","a162","a163","a164","a165","a166","a167","a168","a169","a170","a171","a172","a173","a174","a175","a176","a177","a178","a179","a180","a181","a182","a183","a184","a185","a186","a187","a188","a189","a190","a191","a192","a193","a194","a195","a196","a197","a198","a199","a200","a201","a202","a203","a204","a205","a206","a207","a208","a209","a210","a211","a212","a213","a214","a215","a216","a217","a218","a219","a220","a221","a222","a223","a224","a225","a226","a227","a228","a229","a230","a231","a232","a233","a234","a235","a236","a237","a238","a239","a240","a241","a242","a243","a244","a245","a246","a247","a248","a249","a250","a251","a252","a253","a254","a255",
          "s3","s4","s5","s6","s7","s8","s9","s10","s11","s12","s13","s14","s15","s16","s17","s18","s19","s20","s21","s22","s23","s24","s25","s26","s27","s28","s29","s30","s31","s32","s33","s34","s35","s36","s37","s38","s39","s40","s41","s42","s43","s44","s45","s46","s47","s48","s49","s50","s51","s52","s53","s54","s55","s56","s57","s58","s59","s60","s61","s62","s63","s64","s65","s66","s67","s68","s69","s70","s71","s72","s73","s74","s75","s76","s77","s78","s79","s80","s81","s82","s83","s84","s85","s86","s87","s88","s89","s90","s91","s92","s93","s94","s95","s96","s97","s98","s99","s100","s101","vcc","scc","m0","memory");
#endif
}

extern "C" void kernel_launch(void* const* d_in, const int* in_sizes, int n_in, void* d_out, int out_size, void* d_ws, size_t ws_size,
                              hipStream_t stream) {
    (void)hipFuncSetAttribute((const void*)fused_kernel, hipFuncAttributeMaxDynamicSharedMemorySize, 163840);
    fused_kernel<<<256, 256, 163840, stream>>>((const char*)d_in[0], (const char*)d_in[1], (const char*)d_in[2], (const char*)d_in[3], (const char*)d_in[4], (char*)d_ws,
                                               (const char*)d_in[5], (const char*)d_in[8], (const char*)d_in[9], (const char*)d_in[10], (const char*)d_in[7], (char*)d_out);
}
```

```cpp
#include <hip/hip_runtime.h>

extern "C" __global__ __launch_bounds__(256, 1) void fused_kernel(const char* __restrict__ x, const char* __restrict__ ckv, const char* __restrict__ wq,
                                                       const char* __restrict__ wuk, const char* __restrict__ wuv, char* ws,
                                                       const char* __restrict__ pwo, const char* __restrict__ pwg, const char* __restrict__ pwu, const char* __restrict__ pwd,
                                                       const char* __restrict__ pwr, char* pout) {
    unsigned tid = threadIdx.x, bid = blockIdx.x;
#if defined(__HIP_DEVICE_COMPILE__)
    asm volatile(
"s_load_dwordx2 s[16:17],s[0:1],8\n"
"s_load_dword s3,s[0:1],72\n"
"s_getpc_b64 s[8:9]\n"
".Lfk_pc:\n"
"s_lshr_b32 s82,s2,3\n"
"s_lshl_b32 s82,s82,12\n"
"v_lshlrev_b32 v1,4,v0\n"
"s_mov_b32 s10,.Lfk_cold-.Lfk_pc\n"
"s_mov_b32 s11,0x20000\n"
"buffer_load_dword v115,v1,s[8:11],s82 offen\n"
"v_readfirstlane_b32 s52,v0\n"
"s_load_dwordx2 s[32:33],s[0:1],0\n"
"s_load_dwordx2 s[24:25],s[0:1],40\n"
"s_load_dwordx2 s[44:45],s[0:1],8\n"
"s_load_dwordx2 s[28:29],s[0:1],16\n"
"s_load_dwordx2 s[40:41],s[0:1],16\n"
"s_load_dwordx2 s[4:5],s[0:1],48\n"
"s_and_b32 s82,s2,8\n"
"s_add_u32 s82,s82,24\n"
"s_load_dwordx2 s[20:21],s[0:1],s82\n"
"s_lshr_b32 s52,s52,6\n"
"s_lshr_b32 s83,s2,7\n"
"s_lshl_b32 s83,s83,3\n"
"s_add_u32 s83,s83,24\n"
"s_load_dwordx2 s[36:37],s[0:1],s83\n"
"s_mov_b32 s100,s2\n"
"s_lshl_b32 s92,s2,2\n"
"s_add_u32 s92,s92,s52\n"
"v_and_b32 v0,63,v0\n"
"v_lshlrev_b32 v112,4,v0\n"
"s_and_b32 s88,s2,7\n"
"s_lshr_b32 s89,s2,3\n"
"s_lshl_b32 s93,s89,2\n"
"s_add_u32 s93,s93,s52\n"
"v_lshrrev_b32 v2,3,v0\n"
"v_xor_b32 v3,v2,v0\n"
"v_and_b32 v3,7,v3\n"
"v_lshlrev_b32 v3,4,v3\n"
"s_lshl_b32 s82,s52,9\n"
"v_add_u32 v2,s82,v2\n"
"v_lshl_add_u32 v106,v2,10,v3\n"
"v_add_u32 v107,7168,v106\n"
"v_add_u32 v108,14336,v106\n"
"v_add_u32 v109,21504,v106\n"
"v_lshrrev_b32 v4,4,v0\n"
"v_and_b32 v5,15,v0\n"
"v_lshlrev_b32 v6,1,v4\n"
"v_xor_b32 v5,v5,v6\n"
"v_lshlrev_b32 v5,4,v5\n"
"v_xor_b32 v6,0x80,v5\n"
"s_and_b32 s82,s52,1\n"
"s_lshl_b32 s82,s82,17\n"
"s_lshr_b32 s83,s52,1\n"
"s_lshl_b32 s83,s83,8\n"
"s_add_u32 s82,s82,s83\n"
"v_lshl_add_u32 v7,v4,13,s82\n"
"v_add_u32 v110,v7,v5\n"
"v_add_u32 v111,31744,v110\n"
"v_add_u32 v113,v7,v6\n"
"v_add_u32 v113,63488,v113\n"
"v_add_u32 v114,31744,v113\n"
"s_lshl_b32 s78,s52,12\n"
"s_lshl_b32 s79,s52,13\n"
"s_lshl_b32 s85,s52,11\n"
"s_lshr_b32 s82,s2,5\n"
"s_lshl_b32 s82,s82,15\n"
"s_lshl_b32 s83,s88,10\n"
"s_bfe_u32 s97,s2,0x10004\n"
"s_lshl_b32 s97,s97,9\n"
"s_add_u32 s83,s83,s97\n"
"v_lshrrev_b32 v60,3,v0\n"
"v_lshlrev_b32 v60,19,v60\n"
"v_and_b32 v61,7,v0\n"
"v_lshl_add_u32 v60,v61,7,v60\n"
"s_and_b32 s12,s93,63\n"
"s_lshl3_add_u32 s12,s12,s88\n"
"s_lshl_b32 s12,s12,10\n"
"s_add_u32 s38,s12,1049600\n"
"s_mov_b32 s39,0x20000\n"
"s_and_b32 s14,s52,1\n"
"s_mul_i32 s13,s14,507904\n"
"v_subrev_u32 v62,s13,v106\n"
"v_subrev_u32 v63,s13,v107\n"
"s_mul_i32 s14,s14,6144\n"
"s_sub_u32 s13,s79,s14\n"
"s_waitcnt lgkmcnt(0)\n"
"buffer_load_dword v115,v60,s[36:39],s12 offen\n"
"s_lshl_b32 s87,s92,11\n"
"s_add_u32 s44,s44,s87\n"
"s_addc_u32 s45,s45,0\n"
"s_mov_b32 s46,2048\n"
"s_mov_b32 s47,0x20000\n"
"v_lshlrev_b32 v1,5,v0\n"
"buffer_load_dword v115,v1,s[44:47],0 offen\n"
"s_add_u32 s16,s16,s82\n"
"s_addc_u32 s17,s17,0\n"
"s_sub_u32 s18,2097152,s82\n"
"s_mov_b32 s19,0x20000\n"
"s_add_u32 s20,s20,s83\n"
"s_addc_u32 s21,s21,0\n"
"s_sub_u32 s22,4194304,s83\n"
"s_mov_b32 s23,0x20000\n"
"s_mov_b32 s77,0\n"
"s_add_u32 m0,s78,147456\n"
"s_nop 0\n"
"buffer_load_dwordx4 v110,s[20:23],s77 offen offset:0 lds\n"
"buffer_load_dwordx4 v111,s[20:23],s77 offen offset:1024 lds\n"
"buffer_load_dwordx4 v113,s[20:23],s77 offen offset:2048 lds\n"
"buffer_load_dwordx4 v114,s[20:23],s77 offen offset:3072 lds\n"
"s_mov_b32 s76,786432\n"
"s_add_u32 m0,s13,12288\n"
"s_nop 0\n"
"buffer_load_dwordx4 v62,s[16:19],s76 offen offset:0 lds\n"
"buffer_load_dwordx4 v63,s[16:19],s76 offen offset:1024 lds\n"
"s_mov_b32 s77,262144\n"
"s_add_u32 m0,s78,131072\n"
"s_nop 0\n"
"buffer_load_dwordx4 v110,s[20:23],s77 offen offset:0 lds\n"
"buffer_load_dwordx4 v111,s[20:23],s77 offen offset:1024 lds\n"
"buffer_load_dwordx4 v113,s[20:23],s77 offen offset:2048 lds\n"
"buffer_load_dwordx4 v114,s[20:23],s77 offen offset:3072 lds\n"
"s_mov_b32 s76,0\n"
"s_add_u32 m0,s13,0\n"
"s_nop 0\n"
"buffer_load_dwordx4 v62,s[16:19],s76 offen offset:0 lds\n"
"buffer_load_dwordx4 v63,s[16:19],s76 offen offset:1024 lds\n"
"s_mov_b32 s76,262144\n"
"s_add_u32 m0,s13,4096\n"
"s_nop 0\n"
"buffer_load_dwordx4 v62,s[16:19],s76 offen offset:0 lds\n"
"buffer_load_dwordx4 v63,s[16:19],s76 offen offset:1024 lds\n"
"s_mov_b32 s76,524288\n"
"s_add_u32 m0,s13,8192\n"
"s_nop 0\n"
"buffer_load_dwordx4 v62,s[16:19],s76 offen offset:0 lds\n"
"buffer_load_dwordx4 v63,s[16:19],s76 offen offset:1024 lds\n"
"s_mov_b32 s76,128\n"
"s_add_u32 m0,s79,32768\n"
"s_nop 0\n"
"buffer_load_dwordx4 v106,s[16:19],s76 offen offset:0 lds\n"
"buffer_load_dwordx4 v107,s[16:19],s76 offen offset:1024 lds\n"
"buffer_load_dwordx4 v108,s[16:19],s76 offen offset:2048 lds\n"
"buffer_load_dwordx4 v109,s[16:19],s76 offen offset:3072 lds\n"
"s_mov_b32 s76,262272\n"
"s_add_u32 m0,s79,36864\n"
"s_nop 0\n"
"buffer_load_dwordx4 v106,s[16:19],s76 offen offset:0 lds\n"
"buffer_load_dwordx4 v107,s[16:19],s76 offen offset:1024 lds\n"
"buffer_load_dwordx4 v108,s[16:19],s76 offen offset:2048 lds\n"
"buffer_load_dwordx4 v109,s[16:19],s76 offen offset:3072 lds\n"
"s_lshl_b32 s82,s92,12\n"
"s_add_u32 s32,s32,s82\n"
"s_addc_u32 s33,s33,0\n"
"s_mov_b32 s34,4096\n"
"s_mov_b32 s35,0x20000\n"
"s_mov_b32 s26,176160768\n"
"s_mov_b32 s27,0x20000\n"
"s_mov_b32 s75,148897792\n"
"s_lshr_b32 s54,s2,5\n"
"s_lshl_b32 s54,s54,19\n"
"s_and_b32 s82,s92,0x47\n"
"s_bfe_u32 s83,s92,0x10003\n"
"s_lshl_b32 s83,s83,5\n"
"s_or_b32 s82,s82,s83\n"
"s_bfe_u32 s83,s92,0x20004\n"
"s_lshl3_add_u32 s82,s83,s82\n"
"s_lshl_b32 s82,s82,7\n"
"s_add_u32 s54,s54,s82\n"
"v_lshrrev_b32 v1,3,v0\n"
"v_bfe_u32 v2,v0,2,1\n"
"v_and_b32 v3,3,v0\n"
"v_lshlrev_b32 v3,4,v3\n"
"v_lshl_add_u32 v70,v1,6,v3\n"
"v_lshl_add_u32 v70,v2,11,v70\n"
"buffer_load_dwordx4 v[8:11],v70,s[32:35],0 offen offset:0\n"
"buffer_load_dwordx4 v[12:15],v70,s[32:35],0 offen offset:512\n"
"buffer_load_dwordx4 v[16:19],v70,s[32:35],0 offen offset:1024\n"
"buffer_load_dwordx4 v[20:23],v70,s[32:35],0 offen offset:1536\n"
"s_bfe_u32 s82,s54,0x10009\n"
"s_lshl_b32 s82,s82,5\n"
"v_xor_b32 v3,s82,v3\n"
"v_lshl_add_u32 v71,v2,6,v3\n"
"v_lshl_add_u32 v71,v1,14,v71\n"
"buffer_load_dwordx2 v[78:79],off,s[24:27],s75 sc1\n"
"s_lshl_b32 s82,s52,16\n"
"v_lshl_add_u32 v7,v4,13,s82\n"
"s_bitcmp1_b32 s52,0\n"
"s_cselect_b32 s83,0x80,0\n"
"v_xor_b32 v5,s83,v5\n"
"v_add_u32 v72,v7,v5\n"
"v_add_u32 v73,32768,v72\n"
"s_mov_b32 s86,0\n"
"v_and_b32 v1,15,v0\n"
"v_lshrrev_b32 v2,4,v0\n"
"v_lshlrev_b32 v3,6,v1\n"
"v_lshl_add_u32 v3,v2,4,v3\n"
"v_lshrrev_b32 v4,4,v3\n"
"v_and_b32 v4,32,v4\n"
"v_xor_b32 v97,v3,v4\n"
"s_lshr_b32 s82,s52,1\n"
"s_lshl_b32 s82,s82,14\n"
"v_and_b32 v3,7,v1\n"
"v_xor_b32 v3,v3,v2\n"
"v_lshlrev_b32 v3,4,v3\n"
"v_lshl_add_u32 v3,v1,7,v3\n"
"v_add_u32 v80,s82,v3\n"
"v_xor_b32 v81,64,v80\n"
"v_add_u32 v82,65536,v80\n"
"v_add_u32 v83,65536,v81\n"
"v_bfe_u32 v3,v0,2,2\n"
"v_lshl_add_u32 v4,v2,3,v3\n"
"v_lshlrev_b32 v4,8,v4\n"
"v_and_b32 v5,1,v2\n"
"v_lshl_add_u32 v5,v5,2,v3\n"
"v_lshlrev_b32 v5,5,v5\n"
"v_and_b32 v6,3,v0\n"
"v_lshl_add_u32 v4,v6,3,v4\n"
"s_and_b32 s82,s52,1\n"
"s_lshl_b32 s82,s82,13\n"
"s_add_u32 s82,s82,98304\n"
"v_add_u32 v4,s82,v4\n"
"v_xor_b32 v6,0,v5\n"
"v_add_u32 v98,v4,v6\n"
"v_xor_b32 v6,32,v5\n"
"v_add_u32 v99,v4,v6\n"
"v_xor_b32 v6,64,v5\n"
"v_add_u32 v100,v4,v6\n"
"v_xor_b32 v6,96,v5\n"
"v_add_u32 v101,v4,v6\n"
"v_xor_b32 v6,128,v5\n"
"v_add_u32 v102,v4,v6\n"
"v_xor_b32 v6,160,v5\n"
"v_add_u32 v103,v4,v6\n"
"v_xor_b32 v6,192,v5\n"
"v_add_u32 v104,v4,v6\n"
"v_xor_b32 v6,224,v5\n"
"v_add_u32 v105,v4,v6\n"
"s_and_b32 s53,s89,1\n"
"s_lshl_b32 s83,s88,10\n"
"s_and_b32 s82,s93,63\n"
"s_add_u32 s82,s82,128\n"
"s_lshl_b32 s82,s82,13\n"
"s_add_u32 s82,s82,s83\n"
"s_add_u32 s36,s36,s82\n"
"s_addc_u32 s37,s37,0\n"
"s_mov_b32 s38,2622464\n"
"s_mov_b32 s39,0x20000\n"
"s_lshl_b32 s82,s93,13\n"
"s_add_u32 s82,s82,s83\n"
"s_add_u32 s40,s40,s82\n"
"s_addc_u32 s41,s41,0\n"
"s_mov_b32 s42,7341056\n"
"s_mov_b32 s43,0x20000\n"
"s_cmp_lg_u32 s53,0\n"
"s_cbranch_scc1 .Lfk_v\n"
"s_waitcnt vmcnt(23)\n"
"s_barrier\n"
"ds_read_b128 v[152:155],v80 offset:12288\n"
"ds_read_b128 v[156:159],v80 offset:14336\n"
"ds_read_b64_tr_b16 v[160:161],v98 offset:49152\n"
"ds_read_b64_tr_b16 v[162:163],v98 offset:50176\n"
"ds_read_b64_tr_b16 v[164:165],v99 offset:49152\n"
"ds_read_b64_tr_b16 v[166:167],v99 offset:50176\n"
"ds_read_b64_tr_b16 v[168:169],v100 offset:49152\n"
"ds_read_b64_tr_b16 v[170:171],v100 offset:50176\n"
"ds_read_b64_tr_b16 v[172:173],v101 offset:49152\n"
"ds_read_b64_tr_b16 v[174:175],v101 offset:50176\n"
"ds_read_b64_tr_b16 v[176:177],v102 offset:49152\n"
"ds_read_b64_tr_b16 v[178:179],v102 offset:50176\n"
"ds_read_b64_tr_b16 v[180:181],v103 offset:49152\n"
"ds_read_b64_tr_b16 v[182:183],v103 offset:50176\n"
"ds_read_b64_tr_b16 v[184:185],v104 offset:49152\n"
"ds_read_b64_tr_b16 v[186:187],v104 offset:50176\n"
"ds_read_b64_tr_b16 v[188:189],v105 offset:49152\n"
"ds_read_b64_tr_b16 v[190:191],v105 offset:50176\n"
"s_waitcnt lgkmcnt(12)\n"
"v_mfma_f32_16x16x32_bf16 a[192:195],v[160:163],v[152:155],0\n"
"s_mov_b32 s77,524288\n"
"s_add_u32 m0,s78,98304\n"
"v_mfma_f32_16x16x32_bf16 a[224:227],v[160:163],v[156:159],0\n"
"buffer_load_dwordx4 v110,s[20:23],s77 offen offset:0 lds\n"
"v_mfma_f32_16x16x32_bf16 a[228:231],v[164:167],v[156:159],0\n"
"buffer_load_dwordx4 v111,s[20:23],s77 offen offset:1024 lds\n"
"v_mfma_f32_16x16x32_bf16 a[196:199],v[164:167],v[152:155],0\n"
"s_waitcnt lgkmcnt(8)\n"
"v_mfma_f32_16x16x32_bf16 a[200:203],v[168:171],v[152:155],0\n"
"v_mfma_f32_16x16x32_bf16 a[232:235],v[168:171],v[156:159],0\n"
"buffer_load_dwordx4 v113,s[20:23],s77 offen offset:2048 lds\n"
"v_mfma_f32_16x16x32_bf16 a[236:239],v[172:175],v[156:159],0\n"
"buffer_load_dwordx4 v114,s[20:23],s77 offen offset:3072 lds\n"
"v_mfma_f32_16x16x32_bf16 a[204:207],v[172:175],v[152:155],0\n"
"s_waitcnt vmcnt(21)\n"
"s_barrier\n"
"s_waitcnt lgkmcnt(4)\n"
"v_mfma_f32_16x16x32_bf16 a[208:211],v[176:179],v[152:155],0\n"
"ds_read_b128 v[128:131],v80 offset:0\n"
"ds_read_b128 v[192:195],v81 offset:0\n"
"v_mfma_f32_16x16x32_bf16 a[240:243],v[176:179],v[156:159],0\n"
"ds_read_b64_tr_b16 v[224:225],v98 offset:32768\n"
"ds_read_b64_tr_b16 v[226:227],v98 offset:33792\n"
"v_mfma_f32_16x16x32_bf16 a[244:247],v[180:183],v[156:159],0\n"
"ds_read_b64_tr_b16 v[228:229],v99 offset:32768\n"
"ds_read_b64_tr_b16 v[230:231],v99 offset:33792\n"
"v_mfma_f32_16x16x32_bf16 a[212:215],v[180:183],v[152:155],0\n"
"ds_read_b64_tr_b16 v[232:233],v100 offset:32768\n"
"ds_read_b64_tr_b16 v[234:235],v100 offset:33792\n"
"s_waitcnt lgkmcnt(8)\n"
"v_mfma_f32_16x16x32_bf16 a[216:219],v[184:187],v[152:155],0\n"
"ds_read_b64_tr_b16 v[236:237],v101 offset:32768\n"
"ds_read_b64_tr_b16 v[238:239],v101 offset:33792\n"
"v_mfma_f32_16x16x32_bf16 a[248:251],v[184:187],v[156:159],0\n"
"ds_read_b64_tr_b16 v[240:241],v102 offset:32768\n"
"ds_read_b64_tr_b16 v[242:243],v102 offset:33792\n"
"v_mfma_f32_16x16x32_bf16 a[252:255],v[188:191],v[156:159],0\n"
"ds_read_b64_tr_b16 v[244:245],v103 offset:32768\n"
"ds_read_b64_tr_b16 v[246:247],v103 offset:33792\n"
"v_mfma_f32_16x16x32_bf16 a[220:223],v[188:191],v[152:155],0\n"
"ds_read_b64_tr_b16 v[248:249],v104 offset:32768\n"
"ds_read_b64_tr_b16 v[250:251],v104 offset:33792\n"
"s_waitcnt lgkmcnt(10)\n"
"v_mfma_f32_16x16x32_bf16 a[0:3],v[160:163],v[128:131],0\n"
"ds_read_b64_tr_b16 v[252:253],v105 offset:32768\n"
"ds_read_b64_tr_b16 v[254:255],v105 offset:33792\n"
"v_mfma_f32_16x16x32_bf16 a[0:3],v[224:227],v[192:195],a[0:3]\n"
"ds_read_b128 v[132:135],v80 offset:2048\n"
"ds_read_b128 v[196:199],v81 offset:2048\n"
"v_mfma_f32_16x16x32_bf16 a[4:7],v[228:231],v[192:195],0\n"
"ds_read_b128 v[216:219],v81 offset:12288\n"
"ds_read_b128 v[220:223],v81 offset:14336\n"
"v_mfma_f32_16x16x32_bf16 a[4:7],v[164:167],v[128:131],a[4:7]\n"
"s_waitcnt lgkmcnt(12)\n"
"s_mov_b32 s77,786432\n"
"s_add_u32 m0,s78,114688\n"
"v_mfma_f32_16x16x32_bf16 a[8:11],v[168:171],v[128:131],0\n"
"buffer_load_dwordx4 v110,s[20:23],s77 offen offset:0 lds\n"
"v_mfma_f32_16x16x32_bf16 a[8:11],v[232:235],v[192:195],a[8:11]\n"
"v_mfma_f32_16x16x32_bf16 a[12:15],v[236:239],v[192:195],0\n"
"buffer_load_dwordx4 v111,s[20:23],s77 offen offset:1024 lds\n"
"v_mfma_f32_16x16x32_bf16 a[12:15],v[172:175],v[128:131],a[12:15]\n"
"s_waitcnt lgkmcnt(8)\n"
"v_mfma_f32_16x16x32_bf16 a[16:19],v[176:179],v[128:131],0\n"
"buffer_load_dwordx4 v113,s[20:23],s77 offen offset:2048 lds\n"
"v_mfma_f32_16x16x32_bf16 a[16:19],v[240:243],v[192:195],a[16:19]\n"
"v_mfma_f32_16x16x32_bf16 a[20:23],v[244:247],v[192:195],0\n"
"buffer_load_dwordx4 v114,s[20:23],s77 offen offset:3072 lds\n"
"v_mfma_f32_16x16x32_bf16 a[20:23],v[180:183],v[128:131],a[20:23]\n"
"s_waitcnt lgkmcnt(4)\n"
"s_mov_b32 s76,256\n"
"s_add_u32 m0,s79,65536\n"
"v_mfma_f32_16x16x32_bf16 a[24:27],v[184:187],v[128:131],0\n"
"buffer_load_dwordx4 v106,s[16:19],s76 offen offset:0 lds\n"
"v_mfma_f32_16x16x32_bf16 a[24:27],v[248:251],v[192:195],a[24:27]\n"
"v_mfma_f32_16x16x32_bf16 a[28:31],v[252:255],v[192:195],0\n"
"buffer_load_dwordx4 v107,s[16:19],s76 offen offset:1024 lds\n"
"v_mfma_f32_16x16x32_bf16 a[28:31],v[188:191],v[128:131],a[28:31]\n"
"s_waitcnt lgkmcnt(2)\n"
"v_mfma_f32_16x16x32_bf16 a[32:35],v[160:163],v[132:135],0\n"
"buffer_load_dwordx4 v108,s[16:19],s76 offen offset:2048 lds\n"
"v_mfma_f32_16x16x32_bf16 a[32:35],v[224:227],v[196:199],a[32:35]\n"
"v_mfma_f32_16x16x32_bf16 a[36:39],v[228:231],v[196:199],0\n"
"buffer_load_dwordx4 v109,s[16:19],s76 offen offset:3072 lds\n"
"v_mfma_f32_16x16x32_bf16 a[36:39],v[164:167],v[132:135],a[36:39]\n"
"s_waitcnt vmcnt(25)\n"
"s_barrier\n"
"v_mfma_f32_16x16x32_bf16 a[40:43],v[168:171],v[132:135],0\n"
"ds_read_b128 v[136:139],v80 offset:4096\n"
"ds_read_b128 v[200:203],v81 offset:4096\n"
"v_mfma_f32_16x16x32_bf16 a[40:43],v[232:235],v[196:199],a[40:43]\n"
"ds_read_b128 v[140:143],v80 offset:6144\n"
"ds_read_b128 v[204:207],v81 offset:6144\n"
"v_mfma_f32_16x16x32_bf16 a[44:47],v[236:239],v[196:199],0\n"
"ds_read_b128 v[144:147],v80 offset:8192\n"
"ds_read_b128 v[208:211],v81 offset:8192\n"
"v_mfma_f32_16x16x32_bf16 a[44:47],v[172:175],v[132:135],a[44:47]\n"
"ds_read_b128 v[148:151],v80 offset:10240\n"
"ds_read_b128 v[212:215],v81 offset:10240\n"
"v_mfma_f32_16x16x32_bf16 a[48:51],v[176:179],v[132:135],0\n"
"s_mov_b32 s76,262400\n"
"s_add_u32 m0,s79,69632\n"
"v_mfma_f32_16x16x32_bf16 a[48:51],v[240:243],v[196:199],a[48:51]\n"
"buffer_load_dwordx4 v106,s[16:19],s76 offen offset:0 lds\n"
"v_mfma_f32_16x16x32_bf16 a[52:55],v[244:247],v[196:199],0\n"
"buffer_load_dwordx4 v107,s[16:19],s76 offen offset:1024 lds\n"
"v_mfma_f32_16x16x32_bf16 a[52:55],v[180:183],v[132:135],a[52:55]\n"
"buffer_load_dwordx4 v108,s[16:19],s76 offen offset:2048 lds\n"
"v_mfma_f32_16x16x32_bf16 a[56:59],v[184:187],v[132:135],0\n"
"buffer_load_dwordx4 v109,s[16:19],s76 offen offset:3072 lds\n"
"v_mfma_f32_16x16x32_bf16 a[56:59],v[248:251],v[196:199],a[56:59]\n"
"s_waitcnt lgkmcnt(0)\n"
"s_barrier\n"
"v_mfma_f32_16x16x32_bf16 a[60:63],v[252:255],v[196:199],0\n"
"s_mov_b32 s77,1048576\n"
"s_add_u32 m0,s78,147456\n"
"v_mfma_f32_16x16x32_bf16 a[60:63],v[188:191],v[132:135],a[60:63]\n"
"buffer_load_dwordx4 v110,s[20:23],s77 offen offset:0 lds\n"
"v_mfma_f32_16x16x32_bf16 a[64:67],v[160:163],v[136:139],0\n"
"v_mfma_f32_16x16x32_bf16 a[64:67],v[224:227],v[200:203],a[64:67]\n"
"v_mfma_f32_16x16x32_bf16 a[68:71],v[228:231],v[200:203],0\n"
"v_mfma_f32_16x16x32_bf16 a[68:71],v[164:167],v[136:139],a[68:71]\n"
"buffer_load_dwordx4 v111,s[20:23],s77 offen offset:1024 lds\n"
"v_mfma_f32_16x16x32_bf16 a[72:75],v[168:171],v[136:139],0\n"
"v_mfma_f32_16x16x32_bf16 a[72:75],v[232:235],v[200:203],a[72:75]\n"
"v_mfma_f32_16x16x32_bf16 a[76:79],v[236:239],v[200:203],0\n"
"v_mfma_f32_16x16x32_bf16 a[76:79],v[172:175],v[136:139],a[76:79]\n"
"buffer_load_dwordx4 v113,s[20:23],s77 offen offset:2048 lds\n"
"v_mfma_f32_16x16x32_bf16 a[80:83],v[176:179],v[136:139],0\n"
"v_mfma_f32_16x16x32_bf16 a[80:83],v[240:243],v[200:203],a[80:83]\n"
"v_mfma_f32_16x16x32_bf16 a[84:87],v[244:247],v[200:203],0\n"
"v_mfma_f32_16x16x32_bf16 a[84:87],v[180:183],v[136:139],a[84:87]\n"
"buffer_load_dwordx4 v114,s[20:23],s77 offen offset:3072 lds\n"
"v_mfma_f32_16x16x32_bf16 a[88:91],v[184:187],v[136:139],0\n"
"v_mfma_f32_16x16x32_bf16 a[88:91],v[248:251],v[200:203],a[88:91]\n"
"v_mfma_f32_16x16x32_bf16 a[92:95],v[252:255],v[200:203],0\n"
"s_mov_b32 s77,1310720\n"
"s_add_u32 m0,s78,131072\n"
"v_mfma_f32_16x16x32_bf16 a[92:95],v[188:191],v[136:139],a[92:95]\n"
"buffer_load_dwordx4 v110,s[20:23],s77 offen offset:0 lds\n"
"s_waitcnt vmcnt(21)\n"
"v_mfma_f32_16x16x32_bf16 a[96:99],v[160:163],v[140:143],0\n"
"v_lshlrev_b32 v24,16,v8\n"
"v_and_b32 v25,0xffff0000,v8\n"
"v_mfma_f32_16x16x32_bf16 a[96:99],v[224:227],v[204:207],a[96:99]\n"
"v_lshlrev_b32 v26,16,v9\n"
"v_and_b32 v27,0xffff0000,v9\n"
"v_mfma_f32_16x16x32_bf16 a[100:103],v[228:231],v[204:207],0\n"
"buffer_load_dwordx4 v111,s[20:23],s77 offen offset:1024 lds\n"
"v_mfma_f32_16x16x32_bf16 a[100:103],v[164:167],v[140:143],a[100:103]\n"
"v_lshlrev_b32 v28,16,v10\n"
"v_and_b32 v29,0xffff0000,v10\n"
"v_mfma_f32_16x16x32_bf16 a[104:107],v[168:171],v[140:143],0\n"
"v_lshlrev_b32 v30,16,v11\n"
"v_and_b32 v31,0xffff0000,v11\n"
"v_mfma_f32_16x16x32_bf16 a[104:107],v[232:235],v[204:207],a[104:107]\n"
"v_lshlrev_b32 v32,16,v12\n"
"v_and_b32 v33,0xffff0000,v12\n"
"v_mfma_f32_16x16x32_bf16 a[108:111],v[236:239],v[204:207],0\n"
"buffer_load_dwordx4 v113,s[20:23],s77 offen offset:2048 lds\n"
"v_mfma_f32_16x16x32_bf16 a[108:111],v[172:175],v[140:143],a[108:111]\n"
"v_lshlrev_b32 v34,16,v13\n"
"v_and_b32 v35,0xffff0000,v13\n"
"v_mfma_f32_16x16x32_bf16 a[112:115],v[176:179],v[140:143],0\n"
"v_lshlrev_b32 v36,16,v14\n"
"v_and_b32 v37,0xffff0000,v14\n"
"v_mfma_f32_16x16x32_bf16 a[112:115],v[240:243],v[204:207],a[112:115]\n"
"v_lshlrev_b32 v38,16,v15\n"
"v_and_b32 v39,0xffff0000,v15\n"
"v_mfma_f32_16x16x32_bf16 a[116:119],v[244:247],v[204:207],0\n"
"buffer_load_dwordx4 v114,s[20:23],s77 offen offset:3072 lds\n"
"v_mfma_f32_16x16x32_bf16 a[116:119],v[180:183],v[140:143],a[116:119]\n"
"v_lshlrev_b32 v40,16,v16\n"
"v_and_b32 v41,0xffff0000,v16\n"
"v_mfma_f32_16x16x32_bf16 a[120:123],v[184:187],v[140:143],0\n"
"v_lshlrev_b32 v42,16,v17\n"
"v_and_b32 v43,0xffff0000,v17\n"
"v_mfma_f32_16x16x32_bf16 a[120:123],v[248:251],v[204:207],a[120:123]\n"
"s_mov_b32 s76,384\n"
"v_lshlrev_b32 v44,16,v18\n"
"s_add_u32 m0,s79,0\n"
"v_mfma_f32_16x16x32_bf16 a[124:127],v[252:255],v[204:207],0\n"
"buffer_load_dwordx4 v106,s[16:19],s76 offen offset:0 lds\n"
"v_mfma_f32_16x16x32_bf16 a[124:127],v[188:191],v[140:143],a[124:127]\n"
"v_and_b32 v45,0xffff0000,v18\n"
"v_lshlrev_b32 v46,16,v19\n"
"v_mfma_f32_16x16x32_bf16 a[128:131],v[160:163],v[144:147],0\n"
"v_and_b32 v47,0xffff0000,v19\n"
"v_lshlrev_b32 v48,16,v20\n"
"v_mfma_f32_16x16x32_bf16 a[128:131],v[224:227],v[208:211],a[128:131]\n"
"v_and_b32 v49,0xffff0000,v20\n"
"v_lshlrev_b32 v50,16,v21\n"
"v_mfma_f32_16x16x32_bf16 a[132:135],v[228:231],v[208:211],0\n"
"buffer_load_dwordx4 v107,s[16:19],s76 offen offset:1024 lds\n"
"v_mfma_f32_16x16x32_bf16 a[132:135],v[164:167],v[144:147],a[132:135]\n"
"v_and_b32 v51,0xffff0000,v21\n"
"v_lshlrev_b32 v52,16,v22\n"
"v_mfma_f32_16x16x32_bf16 a[136:139],v[168:171],v[144:147],0\n"
"v_and_b32 v53,0xffff0000,v22\n"
"v_lshlrev_b32 v54,16,v23\n"
"v_mfma_f32_16x16x32_bf16 a[136:139],v[232:235],v[208:211],a[136:139]\n"
"v_and_b32 v55,0xffff0000,v23\n"
"v_add_f32 v56,v24,v25\n"
"v_mfma_f32_16x16x32_bf16 a[140:143],v[236:239],v[208:211],0\n"
"buffer_load_dwordx4 v108,s[16:19],s76 offen offset:2048 lds\n"
"v_mfma_f32_16x16x32_bf16 a[140:143],v[172:175],v[144:147],a[140:143]\n"
"v_add_f32 v57,v26,v27\n"
"v_add_f32 v56,v56,v28\n"
"v_mfma_f32_16x16x32_bf16 a[144:147],v[176:179],v[144:147],0\n"
"v_add_f32 v57,v57,v29\n"
"v_add_f32 v56,v56,v30\n"
"v_mfma_f32_16x16x32_bf16 a[144:147],v[240:243],v[208:211],a[144:147]\n"
"v_readfirstlane_b32 s82,v78\n"
"v_readfirstlane_b32 s83,v79\n"
"v_mfma_f32_16x16x32_bf16 a[148:151],v[244:247],v[208:211],0\n"
"buffer_load_dwordx4 v109,s[16:19],s76 offen offset:3072 lds\n"
"v_mfma_f32_16x16x32_bf16 a[148:151],v[180:183],v[144:147],a[148:151]\n"
"s_mul_i32 s80,s82,0x9e3779b1\n"
"s_add_u32 s80,s80,0x7f4a7c15\n"
"v_mov_b32 v124,s80\n"
"v_mfma_f32_16x16x32_bf16 a[152:155],v[184:187],v[144:147],0\n"
"s_add_u32 s81,s83,s80\n"
"v_add_f32 v57,v57,v31\n"
"v_add_f32 v56,v56,v32\n"
"v_mfma_f32_16x16x32_bf16 a[152:155],v[248:251],v[208:211],a[152:155]\n"
"s_add_u32 s81,s81,1\n"
"s_mov_b32 s76,262528\n"
"s_add_u32 m0,s79,4096\n"
"v_mfma_f32_16x16x32_bf16 a[156:159],v[252:255],v[208:211],0\n"
"buffer_load_dwordx4 v106,s[16:19],s76 offen offset:0 lds\n"
"v_mov_b32 v125,s81\n"
"v_mfma_f32_16x16x32_bf16 a[156:159],v[188:191],v[144:147],a[156:159]\n"
"v_add_f32 v57,v57,v33\n"
"v_add_f32 v56,v56,v34\n"
"v_mfma_f32_16x16x32_bf16 a[160:163],v[160:163],v[148:151],0\n"
"v_add_f32 v57,v57,v35\n"
"v_add_f32 v56,v56,v36\n"
"v_mfma_f32_16x16x32_bf16 a[160:163],v[224:227],v[212:215],a[160:163]\n"
"v_add_f32 v57,v57,v37\n"
"v_add_f32 v56,v56,v38\n"
"v_mfma_f32_16x16x32_bf16 a[164:167],v[228:231],v[212:215],0\n"
"buffer_load_dwordx4 v107,s[16:19],s76 offen offset:1024 lds\n"
"v_mfma_f32_16x16x32_bf16 a[164:167],v[164:167],v[148:151],a[164:167]\n"
"v_add_f32 v57,v57,v39\n"
"v_add_f32 v56,v56,v40\n"
"v_mfma_f32_16x16x32_bf16 a[168:171],v[168:171],v[148:151],0\n"
"v_add_f32 v57,v57,v41\n"
"v_add_f32 v56,v56,v42\n"
"v_mfma_f32_16x16x32_bf16 a[168:171],v[232:235],v[212:215],a[168:171]\n"
"v_add_f32 v57,v57,v43\n"
"v_add_f32 v56,v56,v44\n"
"v_mfma_f32_16x16x32_bf16 a[172:175],v[236:239],v[212:215],0\n"
"buffer_load_dwordx4 v108,s[16:19],s76 offen offset:2048 lds\n"
"v_mfma_f32_16x16x32_bf16 a[172:175],v[172:175],v[148:151],a[172:175]\n"
"v_add_f32 v57,v57,v45\n"
"v_add_f32 v56,v56,v46\n"
"v_mfma_f32_16x16x32_bf16 a[176:179],v[176:179],v[148:151],0\n"
"v_add_f32 v57,v57,v47\n"
"v_add_f32 v56,v56,v48\n"
"v_mfma_f32_16x16x32_bf16 a[176:179],v[240:243],v[212:215],a[176:179]\n"
"v_add_f32 v57,v57,v49\n"
"v_add_f32 v56,v56,v50\n"
"v_mfma_f32_16x16x32_bf16 a[180:183],v[244:247],v[212:215],0\n"
"buffer_load_dwordx4 v109,s[16:19],s76 offen offset:3072 lds\n"
"v_mfma_f32_16x16x32_bf16 a[180:183],v[180:183],v[148:151],a[180:183]\n"
"s_mov_b32 s87,524288\n"
"v_add_f32 v57,v57,v51\n"
"v_add_f32 v56,v56,v52\n"
"v_mfma_f32_16x16x32_bf16 a[184:187],v[184:187],v[148:151],0\n"
"buffer_load_dword v115,v112,s[36:39],s87 offen\n"
"v_mfma_f32_16x16x32_bf16 a[184:187],v[248:251],v[212:215],a[184:187]\n"
"s_mov_b32 s87,1048576\n"
"v_add_f32 v57,v57,v53\n"
"v_add_f32 v56,v56,v54\n"
"v_mfma_f32_16x16x32_bf16 a[188:191],v[252:255],v[212:215],0\n"
"buffer_load_dword v115,v112,s[36:39],s87 offen\n"
"v_mfma_f32_16x16x32_bf16 a[188:191],v[188:191],v[148:151],a[188:191]\n"
"s_mov_b32 s87,1572864\n"
"s_waitcnt vmcnt(26)\n"
"s_barrier\n"
"v_mfma_f32_16x16x32_bf16 a[192:195],v[224:227],v[216:219],a[192:195]\n"
"ds_read_b128 v[152:155],v80 offset:45056\n"
"buffer_load_dword v115,v112,s[36:39],s87 offen\n"
"v_mfma_f32_16x16x32_bf16 a[224:227],v[224:227],v[220:223],a[224:227]\n"
"ds_read_b128 v[156:159],v80 offset:47104\n"
"v_add_f32 v57,v57,v55\n"
"v_mfma_f32_16x16x32_bf16 a[228:231],v[228:231],v[220:223],a[228:231]\n"
"ds_read_b64_tr_b16 v[160:161],v98 offset:0\n"
"v_add_f32 v56,v56,v57\n"
"v_mfma_f32_16x16x32_bf16 a[196:199],v[228:231],v[216:219],a[196:199]\n"
"ds_read_b64_tr_b16 v[162:163],v98 offset:1024\n"
"v_mfma_f32_16x16x32_bf16 a[200:203],v[232:235],v[216:219],a[200:203]\n"
"ds_read_b64_tr_b16 v[164:165],v99 offset:0\n"
"v_add_f32_dpp v56,v56,v56 quad_perm:[1,0,3,2] row_mask:0xf bank_mask:0xf\n"
"v_mfma_f32_16x16x32_bf16 a[232:235],v[232:235],v[220:223],a[232:235]\n"
"ds_read_b64_tr_b16 v[166:167],v99 offset:1024\n"
"v_mfma_f32_16x16x32_bf16 a[236:239],v[236:239],v[220:223],a[236:239]\n"
"ds_read_b64_tr_b16 v[168:169],v100 offset:0\n"
"v_add_f32_dpp v56,v56,v56 quad_perm:[2,3,0,1] row_mask:0xf bank_mask:0xf\n"
"v_mfma_f32_16x16x32_bf16 a[204:207],v[236:239],v[216:219],a[204:207]\n"
"ds_read_b64_tr_b16 v[170:171],v100 offset:1024\n"
"v_mfma_f32_16x16x32_bf16 a[208:211],v[240:243],v[216:219],a[208:211]\n"
"ds_read_b64_tr_b16 v[172:173],v101 offset:0\n"
"v_add_f32_dpp v56,v56,v56 row_ror:8 row_mask:0xf bank_mask:0xf\n"
"v_mfma_f32_16x16x32_bf16 a[240:243],v[240:243],v[220:223],a[240:243]\n"
"ds_read_b64_tr_b16 v[174:175],v101 offset:1024\n"
"v_mov_b32 v57,v56\n"
"v_mfma_f32_16x16x32_bf16 a[244:247],v[244:247],v[220:223],a[244:247]\n"
"ds_read_b64_tr_b16 v[176:177],v102 offset:0\n"
"v_mfma_f32_16x16x32_bf16 a[212:215],v[244:247],v[216:219],a[212:215]\n"
"ds_read_b64_tr_b16 v[178:179],v102 offset:1024\n"
"v_permlane16_swap_b32 v57,v56\n"
"v_mfma_f32_16x16x32_bf16 a[216:219],v[248:251],v[216:219],a[216:219]\n"
"ds_read_b64_tr_b16 v[180:181],v103 offset:0\n"
"v_add_f32 v56,v56,v57\n"
"v_mfma_f32_16x16x32_bf16 a[248:251],v[248:251],v[220:223],a[248:251]\n"
"ds_read_b64_tr_b16 v[182:183],v103 offset:1024\n"
"v_mov_b32 v57,v56\n"
"v_mfma_f32_16x16x32_bf16 a[252:255],v[252:255],v[220:223],a[252:255]\n"
"ds_read_b64_tr_b16 v[184:185],v104 offset:0\n"
"ds_read_b64_tr_b16 v[186:187],v104 offset:1024\n"
"v_mfma_f32_16x16x32_bf16 a[220:223],v[252:255],v[216:219],a[220:223]\n"
"v_permlane32_swap_b32 v57,v56\n"
"s_waitcnt lgkmcnt(10)\n"
"v_mfma_f32_16x16x32_bf16 a[192:195],v[160:163],v[152:155],a[192:195]\n"
"ds_read_b64_tr_b16 v[188:189],v105 offset:0\n"
"v_add_f32 v56,v56,v57\n"
"v_mfma_f32_16x16x32_bf16 a[224:227],v[160:163],v[156:159],a[224:227]\n"
"ds_read_b64_tr_b16 v[190:191],v105 offset:1024\n"
"v_mul_f32 v58,0x3a800000,v56\n"
"v_mfma_f32_16x16x32_bf16 a[228:231],v[164:167],v[156:159],a[228:231]\n"
"ds_read_b128 v[128:131],v80 offset:32768\n"
"v_sub_f32 v62,v24,v58\n"
"v_mfma_f32_16x16x32_bf16 a[196:199],v[164:167],v[152:155],a[196:199]\n"
"ds_read_b128 v[192:195],v81 offset:32768\n"
"s_waitcnt lgkmcnt(10)\n"
"v_mfma_f32_16x16x32_bf16 a[200:203],v[168:171],v[152:155],a[200:203]\n"
"ds_read_b64_tr_b16 v[224:225],v98 offset:16384\n"
"v_mul_f32 v59,v62,v62\n"
"v_mfma_f32_16x16x32_bf16 a[232:235],v[168:171],v[156:159],a[232:235]\n"
"ds_read_b64_tr_b16 v[226:227],v98 offset:17408\n"
"v_sub_f32 v63,v25,v58\n"
"v_mfma_f32_16x16x32_bf16 a[236:239],v[172:175],v[156:159],a[236:239]\n"
"ds_read_b64_tr_b16 v[228:229],v99 offset:16384\n"
"v_mul_f32 v60,v63,v63\n"
"v_mfma_f32_16x16x32_bf16 a[204:207],v[172:175],v[152:155],a[204:207]\n"
"ds_read_b64_tr_b16 v[230:231],v99 offset:17408\n"
"s_waitcnt lgkmcnt(10)\n"
"v_mfma_f32_16x16x32_bf16 a[208:211],v[176:179],v[152:155],a[208:211]\n"
"ds_read_b64_tr_b16 v[232:233],v100 offset:16384\n"
"v_sub_f32 v64,v26,v58\n"
"v_mfma_f32_16x16x32_bf16 a[240:243],v[176:179],v[156:159],a[240:243]\n"
"ds_read_b64_tr_b16 v[234:235],v100 offset:17408\n"
"v_fmac_f32 v59,v64,v64\n"
"v_mfma_f32_16x16x32_bf16 a[244:247],v[180:183],v[156:159],a[244:247]\n"
"ds_read_b64_tr_b16 v[236:237],v101 offset:16384\n"
"v_sub_f32 v65,v27,v58\n"
"v_mfma_f32_16x16x32_bf16 a[212:215],v[180:183],v[152:155],a[212:215]\n"
"ds_read_b64_tr_b16 v[238:239],v101 offset:17408\n"
"s_waitcnt lgkmcnt(10)\n"
"v_mfma_f32_16x16x32_bf16 a[216:219],v[184:187],v[152:155],a[216:219]\n"
"ds_read_b64_tr_b16 v[240:241],v102 offset:16384\n"
"v_fmac_f32 v60,v65,v65\n"
"v_mfma_f32_16x16x32_bf16 a[248:251],v[184:187],v[156:159],a[248:251]\n"
"ds_read_b64_tr_b16 v[242:243],v102 offset:17408\n"
"v_sub_f32 v62,v28,v58\n"
"v_mfma_f32_16x16x32_bf16 a[252:255],v[188:191],v[156:159],a[252:255]\n"
"ds_read_b64_tr_b16 v[244:245],v103 offset:16384\n"
"v_fmac_f32 v59,v62,v62\n"
"v_mfma_f32_16x16x32_bf16 a[220:223],v[188:191],v[152:155],a[220:223]\n"
"ds_read_b64_tr_b16 v[246:247],v103 offset:17408\n"
"s_waitcnt lgkmcnt(8)\n"
"v_mfma_f32_16x16x32_bf16 a[0:3],v[160:163],v[128:131],a[0:3]\n"
"ds_read_b64_tr_b16 v[248:249],v104 offset:16384\n"
"v_sub_f32 v63,v29,v58\n"
"v_mfma_f32_16x16x32_bf16 a[0:3],v[224:227],v[192:195],a[0:3]\n"
"ds_read_b64_tr_b16 v[250:251],v104 offset:17408\n"
"v_fmac_f32 v60,v63,v63\n"
"v_mfma_f32_16x16x32_bf16 a[4:7],v[228:231],v[192:195],a[4:7]\n"
"ds_read_b64_tr_b16 v[252:253],v105 offset:16384\n"
"v_sub_f32 v64,v30,v58\n"
"v_mfma_f32_16x16x32_bf16 a[4:7],v[164:167],v[128:131],a[4:7]\n"
"ds_read_b64_tr_b16 v[254:255],v105 offset:17408\n"
"s_waitcnt lgkmcnt(8)\n"
"v_mfma_f32_16x16x32_bf16 a[8:11],v[168:171],v[128:131],a[8:11]\n"
"ds_read_b128 v[132:135],v80 offset:34816\n"
"v_fmac_f32 v59,v64,v64\n"
"v_mfma_f32_16x16x32_bf16 a[8:11],v[232:235],v[192:195],a[8:11]\n"
"ds_read_b128 v[196:199],v81 offset:34816\n"
"v_sub_f32 v65,v31,v58\n"
"v_mfma_f32_16x16x32_bf16 a[12:15],v[236:239],v[192:195],a[12:15]\n"
"ds_read_b128 v[136:139],v80 offset:36864\n"
"v_fmac_f32 v60,v65,v65\n"
"v_mfma_f32_16x16x32_bf16 a[12:15],v[172:175],v[128:131],a[12:15]\n"
"ds_read_b128 v[200:203],v81 offset:36864\n"
"s_waitcnt lgkmcnt(8)\n"
"v_mfma_f32_16x16x32_bf16 a[16:19],v[176:179],v[128:131],a[16:19]\n"
"ds_read_b128 v[140:143],v80 offset:38912\n"
"v_sub_f32 v62,v32,v58\n"
"v_mfma_f32_16x16x32_bf16 a[16:19],v[240:243],v[192:195],a[16:19]\n"
"ds_read_b128 v[204:207],v81 offset:38912\n"
"v_fmac_f32 v59,v62,v62\n"
"v_mfma_f32_16x16x32_bf16 a[20:23],v[244:247],v[192:195],a[20:23]\n"
"ds_read_b128 v[144:147],v80 offset:40960\n"
"v_sub_f32 v63,v33,v58\n"
"v_mfma_f32_16x16x32_bf16 a[20:23],v[180:183],v[128:131],a[20:23]\n"
"ds_read_b128 v[208:211],v81 offset:40960\n"
"s_waitcnt lgkmcnt(8)\n"
"v_mfma_f32_16x16x32_bf16 a[24:27],v[184:187],v[128:131],a[24:27]\n"
"ds_read_b128 v[148:151],v80 offset:43008\n"
"v_fmac_f32 v60,v63,v63\n"
"v_mfma_f32_16x16x32_bf16 a[24:27],v[248:251],v[192:195],a[24:27]\n"
"ds_read_b128 v[212:215],v81 offset:43008\n"
"v_sub_f32 v64,v34,v58\n"
"v_mfma_f32_16x16x32_bf16 a[28:31],v[252:255],v[192:195],a[28:31]\n"
"ds_read_b128 v[216:219],v81 offset:45056\n"
"v_fmac_f32 v59,v64,v64\n"
"v_mfma_f32_16x16x32_bf16 a[28:31],v[188:191],v[128:131],a[28:31]\n"
"ds_read_b128 v[220:223],v81 offset:47104\n"
"s_waitcnt lgkmcnt(10)\n"
"v_mfma_f32_16x16x32_bf16 a[32:35],v[160:163],v[132:135],a[32:35]\n"
"v_sub_f32 v65,v35,v58\n"
"v_fmac_f32 v60,v65,v65\n"
"v_mfma_f32_16x16x32_bf16 a[32:35],v[224:227],v[196:199],a[32:35]\n"
"v_sub_f32 v62,v36,v58\n"
"v_fmac_f32 v59,v62,v62\n"
"v_mfma_f32_16x16x32_bf16 a[36:39],v[228:231],v[196:199],a[36:39]\n"
"v_sub_f32 v63,v37,v58\n"
"v_fmac_f32 v60,v63,v63\n"
"v_mfma_f32_16x16x32_bf16 a[36:39],v[164:167],v[132:135],a[36:39]\n"
"v_sub_f32 v64,v38,v58\n"
"v_fmac_f32 v59,v64,v64\n"
"v_mfma_f32_16x16x32_bf16 a[40:43],v[168:171],v[132:135],a[40:43]\n"
"s_waitcnt lgkmcnt(0)\n"
"s_barrier\n"
"v_mfma_f32_16x16x32_bf16 a[40:43],v[232:235],v[196:199],a[40:43]\n"
"s_mov_b32 s77,1572864\n"
"v_sub_f32 v65,v39,v58\n"
"s_add_u32 m0,s78,98304\n"
"v_mfma_f32_16x16x32_bf16 a[44:47],v[236:239],v[196:199],a[44:47]\n"
"buffer_load_dwordx4 v110,s[20:23],s77 offen offset:0 lds\n"
"v_mfma_f32_16x16x32_bf16 a[44:47],v[172:175],v[132:135],a[44:47]\n"
"v_fmac_f32 v60,v65,v65\n"
"v_sub_f32 v62,v40,v58\n"
"v_mfma_f32_16x16x32_bf16 a[48:51],v[176:179],v[132:135],a[48:51]\n"
"v_fmac_f32 v59,v62,v62\n"
"v_sub_f32 v63,v41,v58\n"
"v_mfma_f32_16x16x32_bf16 a[48:51],v[240:243],v[196:199],a[48:51]\n"
"v_fmac_f32 v60,v63,v63\n"
"v_sub_f32 v64,v42,v58\n"
"v_mfma_f32_16x16x32_bf16 a[52:55],v[244:247],v[196:199],a[52:55]\n"
"buffer_load_dwordx4 v111,s[20:23],s77 offen offset:1024 lds\n"
"v_mfma_f32_16x16x32_bf16 a[52:55],v[180:183],v[132:135],a[52:55]\n"
"v_fmac_f32 v59,v64,v64\n"
"v_sub_f32 v65,v43,v58\n"
"v_mfma_f32_16x16x32_bf16 a[56:59],v[184:187],v[132:135],a[56:59]\n"
"v_fmac_f32 v60,v65,v65\n"
"v_sub_f32 v62,v44,v58\n"
"v_mfma_f32_16x16x32_bf16 a[56:59],v[248:251],v[196:199],a[56:59]\n"
"v_fmac_f32 v59,v62,v62\n"
"v_sub_f32 v63,v45,v58\n"
"v_mfma_f32_16x16x32_bf16 a[60:63],v[252:255],v[196:199],a[60:63]\n"
"buffer_load_dwordx4 v113,s[20:23],s77 offen offset:2048 lds\n"
"v_mfma_f32_16x16x32_bf16 a[60:63],v[188:191],v[132:135],a[60:63]\n"
"v_fmac_f32 v60,v63,v63\n"
"v_sub_f32 v64,v46,v58\n"
"v_mfma_f32_16x16x32_bf16 a[64:67],v[160:163],v[136:139],a[64:67]\n"
"v_fmac_f32 v59,v64,v64\n"
"v_sub_f32 v65,v47,v58\n"
"v_mfma_f32_16x16x32_bf16 a[64:67],v[224:227],v[200:203],a[64:67]\n"
"v_fmac_f32 v60,v65,v65\n"
"v_sub_f32 v62,v48,v58\n"
"v_mfma_f32_16x16x32_bf16 a[68:71],v[228:231],v[200:203],a[68:71]\n"
"buffer_load_dwordx4 v114,s[20:23],s77 offen offset:3072 lds\n"
"v_mfma_f32_16x16x32_bf16 a[68:71],v[164:167],v[136:139],a[68:71]\n"
"v_fmac_f32 v59,v62,v62\n"
"v_sub_f32 v63,v49,v58\n"
"v_mfma_f32_16x16x32_bf16 a[72:75],v[168:171],v[136:139],a[72:75]\n"
"v_fmac_f32 v60,v63,v63\n"
"v_sub_f32 v64,v50,v58\n"
"v_mfma_f32_16x16x32_bf16 a[72:75],v[232:235],v[200:203],a[72:75]\n"
"s_mov_b32 s77,1835008\n"
"v_fmac_f32 v59,v64,v64\n"
"s_add_u32 m0,s78,114688\n"
"v_mfma_f32_16x16x32_bf16 a[76:79],v[236:239],v[200:203],a[76:79]\n"
"buffer_load_dwordx4 v110,s[20:23],s77 offen offset:0 lds\n"
"v_mfma_f32_16x16x32_bf16 a[76:79],v[172:175],v[136:139],a[76:79]\n"
"v_sub_f32 v65,v51,v58\n"
"v_fmac_f32 v60,v65,v65\n"
"v_mfma_f32_16x16x32_bf16 a[80:83],v[176:179],v[136:139],a[80:83]\n"
"v_sub_f32 v62,v52,v58\n"
"v_fmac_f32 v59,v62,v62\n"
"v_mfma_f32_16x16x32_bf16 a[80:83],v[240:243],v[200:203],a[80:83]\n"
"v_sub_f32 v63,v53,v58\n"
"v_fmac_f32 v60,v63,v63\n"
"v_mfma_f32_16x16x32_bf16 a[84:87],v[244:247],v[200:203],a[84:87]\n"
"buffer_load_dwordx4 v111,s[20:23],s77 offen offset:1024 lds\n"
"v_mfma_f32_16x16x32_bf16 a[84:87],v[180:183],v[136:139],a[84:87]\n"
"v_sub_f32 v64,v54,v58\n"
"v_fmac_f32 v59,v64,v64\n"
"v_mfma_f32_16x16x32_bf16 a[88:91],v[184:187],v[136:139],a[88:91]\n"
"v_sub_f32 v65,v55,v58\n"
"v_fmac_f32 v60,v65,v65\n"
"v_mfma_f32_16x16x32_bf16 a[88:91],v[248:251],v[200:203],a[88:91]\n"
"v_add_f32 v59,v59,v60\n"
"v_mfma_f32_16x16x32_bf16 a[92:95],v[252:255],v[200:203],a[92:95]\n"
"buffer_load_dwordx4 v113,s[20:23],s77 offen offset:2048 lds\n"
"v_mfma_f32_16x16x32_bf16 a[92:95],v[188:191],v[136:139],a[92:95]\n"
"v_add_f32_dpp v59,v59,v59 quad_perm:[1,0,3,2] row_mask:0xf bank_mask:0xf\n"
"v_mfma_f32_16x16x32_bf16 a[96:99],v[160:163],v[140:143],a[96:99]\n"
"s_nop 0\n"
"v_add_f32_dpp v59,v59,v59 quad_perm:[2,3,0,1] row_mask:0xf bank_mask:0xf\n"
"v_mfma_f32_16x16x32_bf16 a[96:99],v[224:227],v[204:207],a[96:99]\n"
"s_nop 0\n"
"v_add_f32_dpp v59,v59,v59 row_ror:8 row_mask:0xf bank_mask:0xf\n"
"v_mov_b32 v57,v59\n"
"v_mfma_f32_16x16x32_bf16 a[100:103],v[228:231],v[204:207],a[100:103]\n"
"buffer_load_dwordx4 v114,s[20:23],s77 offen offset:3072 lds\n"
"v_mfma_f32_16x16x32_bf16 a[100:103],v[164:167],v[140:143],a[100:103]\n"
"v_mfma_f32_16x16x32_bf16 a[104:107],v[168:171],v[140:143],a[104:107]\n"
"v_permlane16_swap_b32 v57,v59\n"
"v_add_f32 v59,v59,v57\n"
"v_mfma_f32_16x16x32_bf16 a[104:107],v[232:235],v[204:207],a[104:107]\n"
"v_mov_b32 v57,v59\n"
"v_mfma_f32_16x16x32_bf16 a[108:111],v[236:239],v[204:207],a[108:111]\n"
"s_mov_b32 s76,512\n"
"v_permlane32_swap_b32 v57,v59\n"
"s_add_u32 m0,s79,32768\n"
"v_mfma_f32_16x16x32_bf16 a[108:111],v[172:175],v[140:143],a[108:111]\n"
"buffer_load_dwordx4 v106,s[16:19],s76 offen offset:0 lds\n"
"v_mfma_f32_16x16x32_bf16 a[112:115],v[176:179],v[140:143],a[112:115]\n"
"v_add_f32 v59,v59,v57\n"
"v_mul_f32 v59,0x3a800000,v59\n"
"v_mfma_f32_16x16x32_bf16 a[112:115],v[240:243],v[204:207],a[112:115]\n"
"v_cvt_pk_bf16_f32 v59,v59,v59\n"
"v_lshlrev_b32 v59,16,v59\n"
"v_mfma_f32_16x16x32_bf16 a[116:119],v[244:247],v[204:207],a[116:119]\n"
"v_add_f32 v59,0x3727c5ac,v59\n"
"v_cvt_pk_bf16_f32 v59,v59,v59\n"
"v_mfma_f32_16x16x32_bf16 a[116:119],v[180:183],v[140:143],a[116:119]\n"
"v_lshlrev_b32 v59,16,v59\n"
"v_sqrt_f32 v59,v59\n"
"v_mfma_f32_16x16x32_bf16 a[120:123],v[184:187],v[140:143],a[120:123]\n"
"buffer_load_dwordx4 v107,s[16:19],s76 offen offset:1024 lds\n"
"v_mfma_f32_16x16x32_bf16 a[120:123],v[248:251],v[204:207],a[120:123]\n"
"v_cvt_pk_bf16_f32 v58,v58,v58\n"
"v_lshlrev_b32 v58,16,v58\n"
"v_mfma_f32_16x16x32_bf16 a[124:127],v[252:255],v[204:207],a[124:127]\n"
"v_cvt_pk_bf16_f32 v59,v59,v59\n"
"v_lshlrev_b32 v59,16,v59\n"
"v_mfma_f32_16x16x32_bf16 a[124:127],v[188:191],v[140:143],a[124:127]\n"
"v_rcp_f32 v66,v59\n"
"v_mfma_f32_16x16x32_bf16 a[128:131],v[160:163],v[144:147],a[128:131]\n"
"v_mul_f32 v66,0x3e0293ee,v66\n"
"v_mul_f32 v58,v58,v66\n"
"v_mfma_f32_16x16x32_bf16 a[128:131],v[224:227],v[208:211],a[128:131]\n"
"buffer_load_dwordx4 v108,s[16:19],s76 offen offset:2048 lds\n"
"v_mfma_f32_16x16x32_bf16 a[132:135],v[228:231],v[208:211],a[132:135]\n"
"v_fma_f32 v24,v24,v66,-v58\n"
"v_fma_f32 v25,v25,v66,-v58\n"
"v_mfma_f32_16x16x32_bf16 a[132:135],v[164:167],v[144:147],a[132:135]\n"
"v_cvt_pk_bf16_f32 v8,v24,v25\n"
"v_fma_f32 v26,v26,v66,-v58\n"
"v_mfma_f32_16x16x32_bf16 a[136:139],v[168:171],v[144:147],a[136:139]\n"
"v_fma_f32 v27,v27,v66,-v58\n"
"v_cvt_pk_bf16_f32 v9,v26,v27\n"
"v_mfma_f32_16x16x32_bf16 a[136:139],v[232:235],v[208:211],a[136:139]\n"
"v_fma_f32 v28,v28,v66,-v58\n"
"v_fma_f32 v29,v29,v66,-v58\n"
"v_mfma_f32_16x16x32_bf16 a[140:143],v[236:239],v[208:211],a[140:143]\n"
"buffer_load_dwordx4 v109,s[16:19],s76 offen offset:3072 lds\n"
"v_mfma_f32_16x16x32_bf16 a[140:143],v[172:175],v[144:147],a[140:143]\n"
"v_cvt_pk_bf16_f32 v10,v28,v29\n"
"v_fma_f32 v30,v30,v66,-v58\n"
"v_mfma_f32_16x16x32_bf16 a[144:147],v[176:179],v[144:147],a[144:147]\n"
"v_fma_f32 v31,v31,v66,-v58\n"
"v_cvt_pk_bf16_f32 v11,v30,v31\n"
"v_mfma_f32_16x16x32_bf16 a[144:147],v[240:243],v[208:211],a[144:147]\n"
"buffer_store_dwordx4 v[8:11],v71,s[24:27],s54 offen sc1\n"
"v_mfma_f32_16x16x32_bf16 a[148:151],v[244:247],v[208:211],a[148:151]\n"
"s_add_u32 s54,s54,131072\n"
"v_fma_f32 v32,v32,v66,-v58\n"
"s_mov_b32 s76,262656\n"
"s_add_u32 m0,s79,36864\n"
"v_mfma_f32_16x16x32_bf16 a[148:151],v[180:183],v[144:147],a[148:151]\n"
"buffer_load_dwordx4 v106,s[16:19],s76 offen offset:0 lds\n"
"v_mfma_f32_16x16x32_bf16 a[152:155],v[184:187],v[144:147],a[152:155]\n"
"v_fma_f32 v33,v33,v66,-v58\n"
"v_cvt_pk_bf16_f32 v12,v32,v33\n"
"v_mfma_f32_16x16x32_bf16 a[152:155],v[248:251],v[208:211],a[152:155]\n"
"v_fma_f32 v34,v34,v66,-v58\n"
"v_fma_f32 v35,v35,v66,-v58\n"
"v_mfma_f32_16x16x32_bf16 a[156:159],v[252:255],v[208:211],a[156:159]\n"
"v_cvt_pk_bf16_f32 v13,v34,v35\n"
"v_fma_f32 v36,v36,v66,-v58\n"
"v_mfma_f32_16x16x32_bf16 a[156:159],v[188:191],v[144:147],a[156:159]\n"
"v_fma_f32 v37,v37,v66,-v58\n"
"v_cvt_pk_bf16_f32 v14,v36,v37\n"
"v_mfma_f32_16x16x32_bf16 a[160:163],v[160:163],v[148:151],a[160:163]\n"
"buffer_load_dwordx4 v107,s[16:19],s76 offen offset:1024 lds\n"
"v_mfma_f32_16x16x32_bf16 a[160:163],v[224:227],v[212:215],a[160:163]\n"
"v_fma_f32 v38,v38,v66,-v58\n"
"v_fma_f32 v39,v39,v66,-v58\n"
"v_cvt_pk_bf16_f32 v15,v38,v39\n"
"v_mfma_f32_16x16x32_bf16 a[164:167],v[228:231],v[212:215],a[164:167]\n"
"buffer_store_dwordx4 v[12:15],v71,s[24:27],s54 offen sc1\n"
"v_mfma_f32_16x16x32_bf16 a[164:167],v[164:167],v[148:151],a[164:167]\n"
"s_add_u32 s54,s54,131072\n"
"v_fma_f32 v40,v40,v66,-v58\n"
"v_mfma_f32_16x16x32_bf16 a[168:171],v[168:171],v[148:151],a[168:171]\n"
"v_fma_f32 v41,v41,v66,-v58\n"
"v_cvt_pk_bf16_f32 v16,v40,v41\n"
"v_mfma_f32_16x16x32_bf16 a[168:171],v[232:235],v[212:215],a[168:171]\n"
"buffer_load_dwordx4 v108,s[16:19],s76 offen offset:2048 lds\n"
"v_mfma_f32_16x16x32_bf16 a[172:175],v[236:239],v[212:215],a[172:175]\n"
"v_fma_f32 v42,v42,v66,-v58\n"
"v_fma_f32 v43,v43,v66,-v58\n"
"v_mfma_f32_16x16x32_bf16 a[172:175],v[172:175],v[148:151],a[172:175]\n"
"v_cvt_pk_bf16_f32 v17,v42,v43\n"
"v_fma_f32 v44,v44,v66,-v58\n"
"v_mfma_f32_16x16x32_bf16 a[176:179],v[176:179],v[148:151],a[176:179]\n"
"v_fma_f32 v45,v45,v66,-v58\n"
"v_cvt_pk_bf16_f32 v18,v44,v45\n"
"v_mfma_f32_16x16x32_bf16 a[176:179],v[240:243],v[212:215],a[176:179]\n"
"v_fma_f32 v46,v46,v66,-v58\n"
"v_fma_f32 v47,v47,v66,-v58\n"
"v_mfma_f32_16x16x32_bf16 a[180:183],v[244:247],v[212:215],a[180:183]\n"
"buffer_load_dwordx4 v109,s[16:19],s76 offen offset:3072 lds\n"
"v_mfma_f32_16x16x32_bf16 a[180:183],v[180:183],v[148:151],a[180:183]\n"
"s_mov_b32 s87,2097152\n"
"v_cvt_pk_bf16_f32 v19,v46,v47\n"
"v_fma_f32 v49,v49,v66,-v58\n"
"v_mfma_f32_16x16x32_bf16 a[184:187],v[184:187],v[148:151],a[184:187]\n"
"buffer_load_dword v115,v112,s[36:39],s87 offen\n"
"buffer_store_dwordx4 v[16:19],v71,s[24:27],s54 offen sc1\n"
"v_mfma_f32_16x16x32_bf16 a[184:187],v[248:251],v[212:215],a[184:187]\n"
"s_mov_b32 s87,2621440\n"
"s_add_u32 s54,s54,131072\n"
"v_fma_f32 v48,v48,v66,-v58\n"
"v_mfma_f32_16x16x32_bf16 a[188:191],v[252:255],v[212:215],a[188:191]\n"
"buffer_load_dword v115,v112,s[36:39],s87 offen\n"
"v_mfma_f32_16x16x32_bf16 a[188:191],v[188:191],v[148:151],a[188:191]\n"
"s_waitcnt vmcnt(32)\n"
"s_barrier\n"
"v_mfma_f32_16x16x32_bf16 a[192:195],v[224:227],v[216:219],a[192:195]\n"
"ds_read_b128 v[152:155],v82 offset:12288\n"
"v_cvt_pk_bf16_f32 v20,v48,v49\n"
"v_mfma_f32_16x16x32_bf16 a[224:227],v[224:227],v[220:223],a[224:227]\n"
"ds_read_b128 v[156:159],v82 offset:14336\n"
"v_fma_f32 v50,v50,v66,-v58\n"
"v_mfma_f32_16x16x32_bf16 a[228:231],v[228:231],v[220:223],a[228:231]\n"
"ds_read_b64_tr_b16 v[160:161],v98 offset:49152\n"
"v_fma_f32 v51,v51,v66,-v58\n"
"v_mfma_f32_16x16x32_bf16 a[196:199],v[228:231],v[216:219],a[196:199]\n"
"ds_read_b64_tr_b16 v[162:163],v98 offset:50176\n"
"v_cvt_pk_bf16_f32 v21,v50,v51\n"
"v_mfma_f32_16x16x32_bf16 a[200:203],v[232:235],v[216:219],a[200:203]\n"
"ds_read_b64_tr_b16 v[164:165],v99 offset:49152\n"
"v_fma_f32 v52,v52,v66,-v58\n"
"v_mfma_f32_16x16x32_bf16 a[232:235],v[232:235],v[220:223],a[232:235]\n"
"ds_read_b64_tr_b16 v[166:167],v99 offset:50176\n"
"v_fma_f32 v53,v53,v66,-v58\n"
"v_mfma_f32_16x16x32_bf16 a[236:239],v[236:239],v[220:223],a[236:239]\n"
"ds_read_b64_tr_b16 v[168:169],v100 offset:49152\n"
"v_cvt_pk_bf16_f32 v22,v52,v53\n"
"v_mfma_f32_16x16x32_bf16 a[204:207],v[236:239],v[216:219],a[204:207]\n"
"ds_read_b64_tr_b16 v[170:171],v100 offset:50176\n"
"v_fma_f32 v54,v54,v66,-v58\n"
"v_mfma_f32_16x16x32_bf16 a[208:211],v[240:243],v[216:219],a[208:211]\n"
"ds_read_b64_tr_b16 v[172:173],v101 offset:49152\n"
"v_fma_f32 v55,v55,v66,-v58\n"
"v_mfma_f32_16x16x32_bf16 a[240:243],v[240:243],v[220:223],a[240:243]\n"
"ds_read_b64_tr_b16 v[174:175],v101 offset:50176\n"
"v_cvt_pk_bf16_f32 v23,v54,v55\n"
"v_mfma_f32_16x16x32_bf16 a[244:247],v[244:247],v[220:223],a[244:247]\n"
"ds_read_b64_tr_b16 v[176:177],v102 offset:49152\n"
"buffer_store_dwordx4 v[20:23],v71,s[24:27],s54 offen sc1\n"
"v_mfma_f32_16x16x32_bf16 a[212:215],v[244:247],v[216:219],a[212:215]\n"
"ds_read_b64_tr_b16 v[178:179],v102 offset:50176\n"
"v_mfma_f32_16x16x32_bf16 a[216:219],v[248:251],v[216:219],a[216:219]\n"
"ds_read_b64_tr_b16 v[180:181],v103 offset:49152\n"
"v_mfma_f32_16x16x32_bf16 a[248:251],v[248:251],v[220:223],a[248:251]\n"
"ds_read_b64_tr_b16 v[182:183],v103 offset:50176\n"
"v_mfma_f32_16x16x32_bf16 a[252:255],v[252:255],v[220:223],a[252:255]\n"
"ds_read_b64_tr_b16 v[184:185],v104 offset:49152\n"
"v_mfma_f32_16x16x32_bf16 a[220:223],v[252:255],v[216:219],a[220:223]\n"
"ds_read_b64_tr_b16 v[186:187],v104 offset:50176\n"
"s_waitcnt lgkmcnt(10)\n"
"v_mfma_f32_16x16x32_bf16 a[192:195],v[160:163],v[152:155],a[192:195]\n"
"ds_read_b64_tr_b16 v[188:189],v105 offset:49152\n"
"s_and_b32 s91,s89,7\n"
"v_mfma_f32_16x16x32_bf16 a[224:227],v[160:163],v[156:159],a[224:227]\n"
"ds_read_b64_tr_b16 v[190:191],v105 offset:50176\n"
"s_lshr_b32 s97,s89,3\n"
"v_mfma_f32_16x16x32_bf16 a[228:231],v[164:167],v[156:159],a[228:231]\n"
"ds_read_b128 v[128:131],v82 offset:0\n"
"s_lshl2_add_u32 s90,s88,s97\n"
"v_mfma_f32_16x16x32_bf16 a[196:199],v[164:167],v[152:155],a[196:199]\n"
"ds_read_b128 v[192:195],v83 offset:0\n"
"s_waitcnt lgkmcnt(10)\n"
"v_mfma_f32_16x16x32_bf16 a[200:203],v[168:171],v[152:155],a[200:203]\n"
"ds_read_b64_tr_b16 v[224:225],v98 offset:32768\n"
"s_lshl_b32 vcc_lo,s90,8\n"
"v_mfma_f32_16x16x32_bf16 a[232:235],v[168:171],v[156:159],a[232:235]\n"
"s_add_u32 s28,s28,vcc_lo\n"
"ds_read_b64_tr_b16 v[226:227],v98 offset:33792\n"
"s_addc_u32 s29,s29,0\n"
"v_mfma_f32_16x16x32_bf16 a[236:239],v[172:175],v[156:159],a[236:239]\n"
"ds_read_b64_tr_b16 v[228:229],v99 offset:32768\n"
"s_sub_u32 s30,8388608,vcc_lo\n"
"v_mfma_f32_16x16x32_bf16 a[204:207],v[172:175],v[152:155],a[204:207]\n"
"ds_read_b64_tr_b16 v[230:231],v99 offset:33792\n"
"s_waitcnt lgkmcnt(10)\n"
"v_mfma_f32_16x16x32_bf16 a[208:211],v[176:179],v[152:155],a[208:211]\n"
"ds_read_b64_tr_b16 v[232:233],v100 offset:32768\n"
"s_mov_b32 s31,0x20000\n"
"v_mfma_f32_16x16x32_bf16 a[240:243],v[176:179],v[156:159],a[240:243]\n"
"ds_read_b64_tr_b16 v[234:235],v100 offset:33792\n"
"s_mov_b32 s60,s91\n"
"v_mfma_f32_16x16x32_bf16 a[244:247],v[180:183],v[156:159],a[244:247]\n"
"ds_read_b64_tr_b16 v[236:237],v101 offset:32768\n"
"s_sub_u32 s94,15,s91\n"
"v_mfma_f32_16x16x32_bf16 a[212:215],v[180:183],v[152:155],a[212:215]\n"
"ds_read_b64_tr_b16 v[238:239],v101 offset:33792\n"
"s_waitcnt lgkmcnt(10)\n"
"v_mfma_f32_16x16x32_bf16 a[216:219],v[184:187],v[152:155],a[216:219]\n"
"s_cmp_lt_u32 s52,2\n"
"ds_read_b64_tr_b16 v[240:241],v102 offset:32768\n"
"s_cselect_b32 s95,s94,s91\n"
"v_mfma_f32_16x16x32_bf16 a[248:251],v[184:187],v[156:159],a[248:251]\n"
"ds_read_b64_tr_b16 v[242:243],v102 offset:33792\n"
"s_lshr_b32 vcc_lo,s95,1\n"
"v_mfma_f32_16x16x32_bf16 a[252:255],v[188:191],v[156:159],a[252:255]\n"
"ds_read_b64_tr_b16 v[244:245],v103 offset:32768\n"
"s_lshl_b32 s55,vcc_lo,19\n"
"v_mfma_f32_16x16x32_bf16 a[220:223],v[188:191],v[152:155],a[220:223]\n"
"ds_read_b64_tr_b16 v[246:247],v103 offset:33792\n"
"s_waitcnt lgkmcnt(8)\n"
"v_mfma_f32_16x16x32_bf16 a[0:3],v[160:163],v[128:131],a[0:3]\n"
"ds_read_b64_tr_b16 v[248:249],v104 offset:32768\n"
"s_lshl_b32 s58,vcc_lo,10\n"
"v_mfma_f32_16x16x32_bf16 a[0:3],v[224:227],v[192:195],a[0:3]\n"
"ds_read_b64_tr_b16 v[250:251],v104 offset:33792\n"
"s_add_u32 s58,s58,148898816\n"
"v_mfma_f32_16x16x32_bf16 a[4:7],v[228:231],v[192:195],a[4:7]\n"
"ds_read_b64_tr_b16 v[252:253],v105 offset:32768\n"
"s_and_b32 vcc_lo,s95,1\n"
"v_mfma_f32_16x16x32_bf16 a[4:7],v[164:167],v[128:131],a[4:7]\n"
"ds_read_b64_tr_b16 v[254:255],v105 offset:33792\n"
"s_waitcnt lgkmcnt(8)\n"
"v_mfma_f32_16x16x32_bf16 a[8:11],v[168:171],v[128:131],a[8:11]\n"
"ds_read_b128 v[132:135],v82 offset:2048\n"
"s_lshl_b32 vcc_lo,vcc_lo,13\n"
"v_mfma_f32_16x16x32_bf16 a[8:11],v[232:235],v[192:195],a[8:11]\n"
"ds_read_b128 v[196:199],v83 offset:2048\n"
"s_and_b32 vcc_hi,s78,4096\n"
"v_mfma_f32_16x16x32_bf16 a[12:15],v[236:239],v[192:195],a[12:15]\n"
"ds_read_b128 v[136:139],v82 offset:4096\n"
"s_add_u32 s55,s55,vcc_lo\n"
"v_mfma_f32_16x16x32_bf16 a[12:15],v[172:175],v[128:131],a[12:15]\n"
"ds_read_b128 v[200:203],v83 offset:4096\n"
"s_waitcnt lgkmcnt(8)\n"
"v_mfma_f32_16x16x32_bf16 a[16:19],v[176:179],v[128:131],a[16:19]\n"
"ds_read_b128 v[140:143],v82 offset:6144\n"
"s_add_u32 s55,s55,vcc_hi\n"
"v_mfma_f32_16x16x32_bf16 a[16:19],v[240:243],v[192:195],a[16:19]\n"
"ds_read_b128 v[204:207],v83 offset:6144\n"
"s_and_b32 vcc_lo,s89,2\n"
"v_mfma_f32_16x16x32_bf16 a[20:23],v[244:247],v[192:195],a[20:23]\n"
"ds_read_b128 v[144:147],v82 offset:8192\n"
"s_lshl2_add_u32 vcc_lo,s88,vcc_lo\n"
"v_mfma_f32_16x16x32_bf16 a[20:23],v[180:183],v[128:131],a[20:23]\n"
"ds_read_b128 v[208:211],v83 offset:8192\n"
"s_waitcnt lgkmcnt(8)\n"
"v_mfma_f32_16x16x32_bf16 a[24:27],v[184:187],v[128:131],a[24:27]\n"
"ds_read_b128 v[148:151],v82 offset:10240\n"
"s_and_b32 vcc_hi,s52,1\n"
"v_mfma_f32_16x16x32_bf16 a[24:27],v[248:251],v[192:195],a[24:27]\n"
"ds_read_b128 v[212:215],v83 offset:10240\n"
"s_add_u32 vcc_lo,vcc_lo,vcc_hi\n"
"v_mfma_f32_16x16x32_bf16 a[28:31],v[252:255],v[192:195],a[28:31]\n"
"ds_read_b128 v[216:219],v83 offset:12288\n"
"s_lshr_b32 vcc_hi,s89,2\n"
"v_mfma_f32_16x16x32_bf16 a[28:31],v[188:191],v[128:131],a[28:31]\n"
"ds_read_b128 v[220:223],v83 offset:14336\n"
"s_waitcnt lgkmcnt(10)\n"
"v_mfma_f32_16x16x32_bf16 a[32:35],v[160:163],v[132:135],a[32:35]\n"
"s_lshr_b32 s84,s52,1\n"
"s_lshl1_add_u32 vcc_hi,vcc_hi,s84\n"
"v_mfma_f32_16x16x32_bf16 a[32:35],v[224:227],v[196:199],a[32:35]\n"
"s_mul_i32 s56,vcc_lo,1179648\n"
"s_lshl_b32 s84,s84,19\n"
"v_mfma_f32_16x16x32_bf16 a[36:39],v[228:231],v[196:199],a[36:39]\n"
"s_add_u32 s56,s56,s84\n"
"s_lshr_b32 s84,s89,2\n"
"v_mfma_f32_16x16x32_bf16 a[36:39],v[164:167],v[132:135],a[36:39]\n"
"s_lshl_b32 s84,s84,14\n"
"s_add_u32 s56,s56,s84\n"
"v_mfma_f32_16x16x32_bf16 a[40:43],v[168:171],v[132:135],a[40:43]\n"
"s_waitcnt lgkmcnt(0)\n"
"s_barrier\n"
"v_mfma_f32_16x16x32_bf16 a[40:43],v[232:235],v[196:199],a[40:43]\n"
"s_lshl_b32 s84,s53,13\n"
"s_mov_b32 s77,2097152\n"
"s_add_u32 m0,s78,147456\n"
"v_mfma_f32_16x16x32_bf16 a[44:47],v[236:239],v[196:199],a[44:47]\n"
"buffer_load_dwordx4 v110,s[20:23],s77 offen offset:0 lds\n"
"v_mfma_f32_16x16x32_bf16 a[44:47],v[172:175],v[132:135],a[44:47]\n"
"s_add_u32 s56,s56,s84\n"
"s_add_u32 s56,s56,4194304\n"
"v_mfma_f32_16x16x32_bf16 a[48:51],v[176:179],v[132:135],a[48:51]\n"
"s_lshl4_add_u32 vcc_hi,s53,vcc_hi\n"
"s_lshl_b32 vcc_lo,vcc_lo,8\n"
"v_mfma_f32_16x16x32_bf16 a[48:51],v[240:243],v[196:199],a[48:51]\n"
"s_lshl3_add_u32 s59,vcc_hi,vcc_lo\n"
"s_add_u32 s59,s59,148914176\n"
"v_mfma_f32_16x16x32_bf16 a[52:55],v[244:247],v[196:199],a[52:55]\n"
"buffer_load_dwordx4 v111,s[20:23],s77 offen offset:1024 lds\n"
"v_mfma_f32_16x16x32_bf16 a[52:55],v[180:183],v[132:135],a[52:55]\n"
"s_lshl_b32 s57,s92,3\n"
"s_add_u32 s57,s57,148898816\n"
"v_mfma_f32_16x16x32_bf16 a[56:59],v[184:187],v[132:135],a[56:59]\n"
"s_lshl_b32 s63,s90,8\n"
"s_add_u32 s63,s63,148914176\n"
"v_mfma_f32_16x16x32_bf16 a[56:59],v[248:251],v[196:199],a[56:59]\n"
"s_lshl_b32 s74,s92,11\n"
"s_add_u32 s74,s74,171966464\n"
"v_mfma_f32_16x16x32_bf16 a[60:63],v[252:255],v[196:199],a[60:63]\n"
"buffer_load_dwordx4 v113,s[20:23],s77 offen offset:2048 lds\n"
"v_mfma_f32_16x16x32_bf16 a[60:63],v[188:191],v[132:135],a[60:63]\n"
"s_mul_i32 s96,s90,1179648\n"
"s_add_u32 s96,s96,4194304\n"
"v_mfma_f32_16x16x32_bf16 a[64:67],v[160:163],v[136:139],a[64:67]\n"
"s_add_u32 s73,s96,s85\n"
"s_and_b32 vcc_hi,s78,4096\n"
"v_mfma_f32_16x16x32_bf16 a[64:67],v[224:227],v[200:203],a[64:67]\n"
"s_lshl_b32 vcc_hi,vcc_hi,1\n"
"s_add_u32 vcc_lo,s96,vcc_hi\n"
"v_mfma_f32_16x16x32_bf16 a[68:71],v[228:231],v[200:203],a[68:71]\n"
"buffer_load_dwordx4 v114,s[20:23],s77 offen offset:3072 lds\n"
"v_mfma_f32_16x16x32_bf16 a[68:71],v[164:167],v[136:139],a[68:71]\n"
"s_add_u32 s65,vcc_lo,524288\n"
"s_lshl_b32 s66,s91,16\n"
"v_mfma_f32_16x16x32_bf16 a[72:75],v[168:171],v[136:139],a[72:75]\n"
"s_sub_u32 s66,vcc_lo,s66\n"
"s_add_u32 s66,s66,999424\n"
"v_mfma_f32_16x16x32_bf16 a[72:75],v[232:235],v[200:203],a[72:75]\n"
"s_add_u32 s67,vcc_hi,65536\n"
"s_mov_b32 s77,2359296\n"
"s_add_u32 m0,s78,131072\n"
"v_mfma_f32_16x16x32_bf16 a[76:79],v[236:239],v[200:203],a[76:79]\n"
"buffer_load_dwordx4 v110,s[20:23],s77 offen offset:0 lds\n"
"v_mfma_f32_16x16x32_bf16 a[76:79],v[172:175],v[136:139],a[76:79]\n"
"s_mul_i32 vcc_lo,vcc_hi,5\n"
"s_sub_u32 vcc_lo,s96,vcc_lo\n"
"v_mfma_f32_16x16x32_bf16 a[80:83],v[176:179],v[136:139],a[80:83]\n"
"s_add_u32 s64,vcc_lo,81920\n"
"s_add_u32 s62,vcc_lo,135168\n"
"s_add_u32 s61,vcc_lo,524288\n"
"v_mfma_f32_16x16x32_bf16 a[80:83],v[240:243],v[200:203],a[80:83]\n"
"s_cmp_lt_u32 s52,2\n"
"s_cselect_b32 s64,s64,2146435072\n"
"s_cselect_b32 s67,s67,163840\n"
"s_cselect_b32 s65,s65,1\n"
"v_mfma_f32_16x16x32_bf16 a[84:87],v[244:247],v[200:203],a[84:87]\n"
"buffer_load_dwordx4 v111,s[20:23],s77 offen offset:1024 lds\n"
"v_mfma_f32_16x16x32_bf16 a[84:87],v[180:183],v[136:139],a[84:87]\n"
"s_cselect_b32 vcc_lo,4,0\n"
"s_and_b32 vcc_hi,s52,1\n"
"s_add_u32 vcc_lo,vcc_lo,vcc_hi\n"
"v_mfma_f32_16x16x32_bf16 a[88:91],v[184:187],v[136:139],a[88:91]\n"
"s_lshl_b32 vcc_hi,s90,12\n"
"s_add_u32 vcc_hi,vcc_hi,54525952\n"
"v_mfma_f32_16x16x32_bf16 a[88:91],v[248:251],v[200:203],a[88:91]\n"
"s_lshl3_add_u32 s70,s95,vcc_lo\n"
"s_lshl_b32 s70,s70,17\n"
"v_mfma_f32_16x16x32_bf16 a[92:95],v[252:255],v[200:203],a[92:95]\n"
"buffer_load_dwordx4 v113,s[20:23],s77 offen offset:2048 lds\n"
"v_mfma_f32_16x16x32_bf16 a[92:95],v[188:191],v[136:139],a[92:95]\n"
"s_add_u32 s70,s70,vcc_hi\n"
"s_lshl3_add_u32 s68,s94,vcc_lo\n"
"v_mfma_f32_16x16x32_bf16 a[96:99],v[160:163],v[140:143],a[96:99]\n"
"s_lshl_b32 s68,s68,17\n"
"s_add_u32 s68,s68,vcc_hi\n"
"v_mfma_f32_16x16x32_bf16 a[96:99],v[224:227],v[204:207],a[96:99]\n"
"s_lshr_b32 vcc_lo,s90,3\n"
"s_lshl2_add_u32 vcc_lo,vcc_lo,s52\n"
"v_mfma_f32_16x16x32_bf16 a[100:103],v[228:231],v[204:207],a[100:103]\n"
"buffer_load_dwordx4 v114,s[20:23],s77 offen offset:3072 lds\n"
"v_mfma_f32_16x16x32_bf16 a[100:103],v[164:167],v[140:143],a[100:103]\n"
"s_lshl_b32 vcc_lo,vcc_lo,7\n"
"s_and_b32 vcc_hi,s90,7\n"
"v_mfma_f32_16x16x32_bf16 a[104:107],v[168:171],v[140:143],a[104:107]\n"
"s_add_u32 vcc_lo,vcc_lo,vcc_hi\n"
"s_lshl3_add_u32 s69,s91,vcc_lo\n"
"v_mfma_f32_16x16x32_bf16 a[104:107],v[232:235],v[204:207],a[104:107]\n"
"s_lshl_b32 s69,s69,3\n"
"s_add_u32 s69,s69,148930560\n"
"v_mfma_f32_16x16x32_bf16 a[108:111],v[236:239],v[204:207],a[108:111]\n"
"s_lshl3_add_u32 s71,s94,vcc_lo\n"
"s_mov_b32 s76,640\n"
"s_add_u32 m0,s79,65536\n"
"v_mfma_f32_16x16x32_bf16 a[108:111],v[172:175],v[140:143],a[108:111]\n"
"buffer_load_dwordx4 v106,s[16:19],s76 offen offset:0 lds\n"
"v_mfma_f32_16x16x32_bf16 a[112:115],v[176:179],v[140:143],a[112:115]\n"
"s_lshl_b32 s71,s71,3\n"
"s_add_u32 s71,s71,148930560\n"
"v_mfma_f32_16x16x32_bf16 a[112:115],v[240:243],v[204:207],a[112:115]\n"
"s_mov_b32 s6,8388608\n"
"s_mov_b32 s7,0x20000\n"
"v_mfma_f32_16x16x32_bf16 a[116:119],v[244:247],v[204:207],a[116:119]\n"
"s_lshr_b32 s94,s88,1\n"
"s_and_b32 vcc_lo,s91,1\n"
"v_mfma_f32_16x16x32_bf16 a[116:119],v[180:183],v[140:143],a[116:119]\n"
"s_lshl1_add_u32 s95,s97,vcc_lo\n"
"s_and_b32 vcc_lo,s88,1\n"
"v_mfma_f32_16x16x32_bf16 a[120:123],v[184:187],v[140:143],a[120:123]\n"
"buffer_load_dwordx4 v107,s[16:19],s76 offen offset:1024 lds\n"
"v_mfma_f32_16x16x32_bf16 a[120:123],v[248:251],v[204:207],a[120:123]\n"
"s_lshr_b32 vcc_hi,s91,1\n"
"s_lshl2_add_u32 s96,vcc_lo,vcc_hi\n"
"v_mfma_f32_16x16x32_bf16 a[124:127],v[252:255],v[204:207],a[124:127]\n"
"s_lshr_b32 vcc_lo,s88,2\n"
"s_lshl2_add_u32 s96,vcc_lo,vcc_hi\n"
"v_mfma_f32_16x16x32_bf16 a[124:127],v[188:191],v[140:143],a[124:127]\n"
"s_sub_u32 vcc_lo,7,s88\n"
"s_min_u32 s94,vcc_lo,s88\n"
"v_mfma_f32_16x16x32_bf16 a[128:131],v[160:163],v[144:147],a[128:131]\n"
"s_and_b32 s96,s88,4\n"
"s_add_u32 s96,s96,s97\n"
"v_mfma_f32_16x16x32_bf16 a[128:131],v[224:227],v[208:211],a[128:131]\n"
"buffer_load_dwordx4 v108,s[16:19],s76 offen offset:2048 lds\n"
"v_mfma_f32_16x16x32_bf16 a[132:135],v[228:231],v[208:211],a[132:135]\n"
"s_add_u32 s95,s91,s96\n"
"s_and_b32 s95,s95,7\n"
"v_mfma_f32_16x16x32_bf16 a[132:135],v[164:167],v[144:147],a[132:135]\n"
"s_lshl3_add_u32 vcc_lo,s94,s96\n"
"s_lshl3_add_u32 vcc_lo,vcc_lo,s52\n"
"v_mfma_f32_16x16x32_bf16 a[136:139],v[168:171],v[144:147],a[136:139]\n"
"s_lshl_b32 s13,vcc_lo,15\n"
"s_lshl_b32 vcc_lo,s95,8\n"
"v_mfma_f32_16x16x32_bf16 a[136:139],v[232:235],v[208:211],a[136:139]\n"
"s_add_u32 s13,s13,vcc_lo\n"
"s_lshl3_add_u32 vcc_hi,s94,s95\n"
"v_mfma_f32_16x16x32_bf16 a[140:143],v[236:239],v[208:211],a[140:143]\n"
"buffer_load_dwordx4 v109,s[16:19],s76 offen offset:3072 lds\n"
"v_mfma_f32_16x16x32_bf16 a[140:143],v[172:175],v[144:147],a[140:143]\n"
"s_lshl_b32 s72,vcc_hi,8\n"
"s_add_u32 s72,s72,149061632\n"
"v_mfma_f32_16x16x32_bf16 a[144:147],v[176:179],v[144:147],a[144:147]\n"
"s_or_b32 vcc_lo,s96,s52\n"
"s_cselect_b32 s72,2146435072,s72\n"
"s_lshl_b32 s3,vcc_hi,18\n"
"v_mfma_f32_16x16x32_bf16 a[144:147],v[240:243],v[208:211],a[144:147]\n"
"s_lshl_b32 vcc_lo,s96,15\n"
"s_sub_u32 s3,s3,vcc_lo\n"
"v_mfma_f32_16x16x32_bf16 a[148:151],v[244:247],v[208:211],a[148:151]\n"
"s_add_u32 s3,s3,s78\n"
"s_mov_b32 s76,262784\n"
"s_add_u32 m0,s79,69632\n"
"v_mfma_f32_16x16x32_bf16 a[148:151],v[180:183],v[144:147],a[148:151]\n"
"buffer_load_dwordx4 v106,s[16:19],s76 offen offset:0 lds\n"
"v_mfma_f32_16x16x32_bf16 a[152:155],v[184:187],v[144:147],a[152:155]\n"
"s_add_u32 s3,s3,100909056\n"
"s_lshl3_add_u32 vcc_hi,vcc_hi,s96\n"
"v_mfma_f32_16x16x32_bf16 a[152:155],v[248:251],v[208:211],a[152:155]\n"
"s_lshl2_add_u32 vcc_hi,vcc_hi,s52\n"
"s_lshl_b32 s2,vcc_hi,4\n"
"v_mfma_f32_16x16x32_bf16 a[156:159],v[252:255],v[208:211],a[156:159]\n"
"s_add_u32 s2,s2,149028864\n"
"s_lshl2_add_u32 vcc_lo,s95,s52\n"
"v_mfma_f32_16x16x32_bf16 a[156:159],v[188:191],v[144:147],a[156:159]\n"
"s_lshl_b32 vcc_lo,vcc_lo,10\n"
"s_lshl_b32 vcc_hi,s94,22\n"
"v_mfma_f32_16x16x32_bf16 a[160:163],v[160:163],v[148:151],a[160:163]\n"
"buffer_load_dwordx4 v107,s[16:19],s76 offen offset:1024 lds\n"
"v_mfma_f32_16x16x32_bf16 a[160:163],v[224:227],v[212:215],a[160:163]\n"
"s_add_u32 s12,vcc_lo,vcc_hi\n"
"s_lshl_b32 vcc_lo,s96,19\n"
"v_mfma_f32_16x16x32_bf16 a[164:167],v[228:231],v[212:215],a[164:167]\n"
"s_add_u32 s10,s12,vcc_lo\n"
"s_add_u32 s10,s10,71303168\n"
"v_mfma_f32_16x16x32_bf16 a[164:167],v[164:167],v[148:151],a[164:167]\n"
"s_mul_i32 vcc_lo,s52,31744\n"
"s_add_u32 s10,s10,vcc_lo\n"
"v_mfma_f32_16x16x32_bf16 a[168:171],v[168:171],v[148:151],a[168:171]\n"
"s_lshl_b32 vcc_lo,s52,10\n"
"s_sub_u32 s12,57671680,vcc_lo\n"
"v_mfma_f32_16x16x32_bf16 a[168:171],v[232:235],v[212:215],a[168:171]\n"
"buffer_load_dwordx4 v108,s[16:19],s76 offen offset:2048 lds\n"
"v_mfma_f32_16x16x32_bf16 a[172:175],v[236:239],v[212:215],a[172:175]\n"
"s_add_u32 s8,s79,54525952\n"
"s_and_b32 vcc_lo,s96,1\n"
"v_mfma_f32_16x16x32_bf16 a[172:175],v[172:175],v[148:151],a[172:175]\n"
"s_lshl_b32 s11,vcc_lo,10\n"
"s_add_u32 s11,s11,148932608\n"
"v_mfma_f32_16x16x32_bf16 a[176:179],v[176:179],v[148:151],a[176:179]\n"
"s_mov_b32 s9,148931072\n"
"s_cmp_eq_u32 s52,0\n"
"s_cselect_b32 s71,s71,2146435072\n"
"v_mfma_f32_16x16x32_bf16 a[176:179],v[240:243],v[212:215],a[176:179]\n"
"v_mfma_f32_16x16x32_bf16 a[180:183],v[244:247],v[212:215],a[180:183]\n"
"buffer_load_dwordx4 v109,s[16:19],s76 offen offset:3072 lds\n"
"v_mfma_f32_16x16x32_bf16 a[180:183],v[180:183],v[148:151],a[180:183]\n"
"s_mov_b32 s87,0\n"
"v_mfma_f32_16x16x32_bf16 a[184:187],v[184:187],v[148:151],a[184:187]\n"
"buffer_load_dword v115,v112,s[40:43],s87 offen\n"
"v_mfma_f32_16x16x32_bf16 a[184:187],v[248:251],v[212:215],a[184:187]\n"
"s_mov_b32 s87,1048576\n"
"v_mfma_f32_16x16x32_bf16 a[188:191],v[252:255],v[212:215],a[188:191]\n"
"buffer_load_dword v115,v112,s[40:43],s87 offen\n"
"v_mfma_f32_16x16x32_bf16 a[188:191],v[188:191],v[148:151],a[188:191]\n"
"s_mov_b32 s87,2097152\n"
"s_waitcnt vmcnt(32)\n"
"s_barrier\n"
"v_mfma_f32_16x16x32_bf16 a[192:195],v[224:227],v[216:219],a[192:195]\n"
"ds_read_b128 v[152:155],v80 offset:12288\n"
"buffer_load_dword v115,v112,s[40:43],s87 offen\n"
"v_mfma_f32_16x16x32_bf16 a[224:227],v[224:227],v[220:223],a[224:227]\n"
"ds_read_b128 v[156:159],v80 offset:14336\n"
"v_mfma_f32_16x16x32_bf16 a[228:231],v[228:231],v[220:223],a[228:231]\n"
"ds_read_b64_tr_b16 v[160:161],v98 offset:0\n"
"v_mfma_f32_16x16x32_bf16 a[196:199],v[228:231],v[216:219],a[196:199]\n"
"ds_read_b64_tr_b16 v[162:163],v98 offset:1024\n"
"v_mfma_f32_16x16x32_bf16 a[200:203],v[232:235],v[216:219],a[200:203]\n"
"ds_read_b64_tr_b16 v[164:165],v99 offset:0\n"
"v_mfma_f32_16x16x32_bf16 a[232:235],v[232:235],v[220:223],a[232:235]\n"
"ds_read_b64_tr_b16 v[166:167],v99 offset:1024\n"
"v_mfma_f32_16x16x32_bf16 a[236:239],v[236:239],v[220:223],a[236:239]\n"
"ds_read_b64_tr_b16 v[168:169],v100 offset:0\n"
"v_mfma_f32_16x16x32_bf16 a[204:207],v[236:239],v[216:219],a[204:207]\n"
"ds_read_b64_tr_b16 v[170:171],v100 offset:1024\n"
"v_mfma_f32_16x16x32_bf16 a[208:211],v[240:243],v[216:219],a[208:211]\n"
"ds_read_b64_tr_b16 v[172:173],v101 offset:0\n"
"v_mfma_f32_16x16x32_bf16 a[240:243],v[240:243],v[220:223],a[240:243]\n"
"ds_read_b64_tr_b16 v[174:175],v101 offset:1024\n"
"v_mfma_f32_16x16x32_bf16 a[244:247],v[244:247],v[220:223],a[244:247]\n"
"ds_read_b64_tr_b16 v[176:177],v102 offset:0\n"
"v_mfma_f32_16x16x32_bf16 a[212:215],v[244:247],v[216:219],a[212:215]\n"
"ds_read_b64_tr_b16 v[178:179],v102 offset:1024\n"
"v_mfma_f32_16x16x32_bf16 a[216:219],v[248:251],v[216:219],a[216:219]\n"
"ds_read_b64_tr_b16 v[180:181],v103 offset:0\n"
"v_mfma_f32_16x16x32_bf16 a[248:251],v[248:251],v[220:223],a[248:251]\n"
"ds_read_b64_tr_b16 v[182:183],v103 offset:1024\n"
"v_mfma_f32_16x16x32_bf16 a[252:255],v[252:255],v[220:223],a[252:255]\n"
"ds_read_b64_tr_b16 v[184:185],v104 offset:0\n"
"v_mfma_f32_16x16x32_bf16 a[220:223],v[252:255],v[216:219],a[220:223]\n"
"ds_read_b64_tr_b16 v[186:187],v104 offset:1024\n"
"s_waitcnt lgkmcnt(10)\n"
"v_mfma_f32_16x16x32_bf16 a[192:195],v[160:163],v[152:155],a[192:195]\n"
"ds_read_b64_tr_b16 v[188:189],v105 offset:0\n"
"v_mfma_f32_16x16x32_bf16 a[224:227],v[160:163],v[156:159],a[224:227]\n"
"ds_read_b64_tr_b16 v[190:191],v105 offset:1024\n"
"v_mfma_f32_16x16x32_bf16 a[228:231],v[164:167],v[156:159],a[228:231]\n"
"ds_read_b128 v[128:131],v80 offset:0\n"
"v_mfma_f32_16x16x32_bf16 a[196:199],v[164:167],v[152:155],a[196:199]\n"
"ds_read_b128 v[192:195],v81 offset:0\n"
"s_waitcnt lgkmcnt(10)\n"
"v_mfma_f32_16x16x32_bf16 a[200:203],v[168:171],v[152:155],a[200:203]\n"
"ds_read_b64_tr_b16 v[224:225],v98 offset:16384\n"
"v_mfma_f32_16x16x32_bf16 a[232:235],v[168:171],v[156:159],a[232:235]\n"
"ds_read_b64_tr_b16 v[226:227],v98 offset:17408\n"
"v_mfma_f32_16x16x32_bf16 a[236:239],v[172:175],v[156:159],a[236:239]\n"
"ds_read_b64_tr_b16 v[228:229],v99 offset:16384\n"
"v_mfma_f32_16x16x32_bf16 a[204:207],v[172:175],v[152:155],a[204:207]\n"
"ds_read_b64_tr_b16 v[230:231],v99 offset:17408\n"
"s_waitcnt lgkmcnt(10)\n"
"v_mfma_f32_16x16x32_bf16 a[208:211],v[176:179],v[152:155],a[208:211]\n"
"ds_read_b64_tr_b16 v[232:233],v100 offset:16384\n"
"v_mfma_f32_16x16x32_bf16 a[240:243],v[176:179],v[156:159],a[240:243]\n"
"ds_read_b64_tr_b16 v[234:235],v100 offset:17408\n"
"v_mfma_f32_16x16x32_bf16 a[244:247],v[180:183],v[156:159],a[244:247]\n"
"ds_read_b64_tr_b16 v[236:237],v101 offset:16384\n"
"v_mfma_f32_16x16x32_bf16 a[212:215],v[180:183],v[152:155],a[212:215]\n"
"ds_read_b64_tr_b16 v[238:239],v101 offset:17408\n"
"s_waitcnt lgkmcnt(10)\n"
"v_mfma_f32_16x16x32_bf16 a[216:219],v[184:187],v[152:155],a[216:219]\n"
"ds_read_b64_tr_b16 v[240:241],v102 offset:16384\n"
"v_mfma_f32_16x16x32_bf16 a[248:251],v[184:187],v[156:159],a[248:251]\n"
"ds_read_b64_tr_b16 v[242:243],v102 offset:17408\n"
"v_mfma_f32_16x16x32_bf16 a[252:255],v[188:191],v[156:159],a[252:255]\n"
"ds_read_b64_tr_b16 v[244:245],v103 offset:16384\n"
"v_mfma_f32_16x16x32_bf16 a[220:223],v[188:191],v[152:155],a[220:223]\n"
"ds_read_b64_tr_b16 v[246:247],v103 offset:17408\n"
"s_waitcnt lgkmcnt(8)\n"
"v_mfma_f32_16x16x32_bf16 a[0:3],v[160:163],v[128:131],a[0:3]\n"
"ds_read_b64_tr_b16 v[248:249],v104 offset:16384\n"
"v_mfma_f32_16x16x32_bf16 a[0:3],v[224:227],v[192:195],a[0:3]\n"
"ds_read_b64_tr_b16 v[250:251],v104 offset:17408\n"
"v_mfma_f32_16x16x32_bf16 a[4:7],v[228:231],v[192:195],a[4:7]\n"
"ds_read_b64_tr_b16 v[252:253],v105 offset:16384\n"
"v_mfma_f32_16x16x32_bf16 a[4:7],v[164:167],v[128:131],a[4:7]\n"
"ds_read_b64_tr_b16 v[254:255],v105 offset:17408\n"
"s_waitcnt lgkmcnt(8)\n"
"v_mfma_f32_16x16x32_bf16 a[8:11],v[168:171],v[128:131],a[8:11]\n"
"ds_read_b128 v[132:135],v80 offset:2048\n"
"v_mfma_f32_16x16x32_bf16 a[8:11],v[232:235],v[192:195],a[8:11]\n"
"ds_read_b128 v[196:199],v81 offset:2048\n"
"v_mfma_f32_16x16x32_bf16 a[12:15],v[236:239],v[192:195],a[12:15]\n"
"ds_read_b128 v[136:139],v80 offset:4096\n"
"v_mfma_f32_16x16x32_bf16 a[12:15],v[172:175],v[128:131],a[12:15]\n"
"ds_read_b128 v[200:203],v81 offset:4096\n"
"s_waitcnt lgkmcnt(8)\n"
"v_mfma_f32_16x16x32_bf16 a[16:19],v[176:179],v[128:131],a[16:19]\n"
"ds_read_b128 v[140:143],v80 offset:6144\n"
"v_mfma_f32_16x16x32_bf16 a[16:19],v[240:243],v[192:195],a[16:19]\n"
"ds_read_b128 v[204:207],v81 offset:6144\n"
"v_mfma_f32_16x16x32_bf16 a[20:23],v[244:247],v[192:195],a[20:23]\n"
"ds_read_b128 v[144:147],v80 offset:8192\n"
"v_mfma_f32_16x16x32_bf16 a[20:23],v[180:183],v[128:131],a[20:23]\n"
"ds_read_b128 v[208:211],v81 offset:8192\n"
"s_waitcnt lgkmcnt(8)\n"
"v_mfma_f32_16x16x32_bf16 a[24:27],v[184:187],v[128:131],a[24:27]\n"
"ds_read_b128 v[148:151],v80 offset:10240\n"
"v_mfma_f32_16x16x32_bf16 a[24:27],v[248:251],v[192:195],a[24:27]\n"
"ds_read_b128 v[212:215],v81 offset:10240\n"
"v_mfma_f32_16x16x32_bf16 a[28:31],v[252:255],v[192:195],a[28:31]\n"
"ds_read_b128 v[216:219],v81 offset:12288\n"
"v_mfma_f32_16x16x32_bf16 a[28:31],v[188:191],v[128:131],a[28:31]\n"
"ds_read_b128 v[220:223],v81 offset:14336\n"
"s_waitcnt lgkmcnt(10)\n"
"v_mfma_f32_16x16x32_bf16 a[32:35],v[160:163],v[132:135],a[32:35]\n"
"v_mfma_f32_16x16x32_bf16 a[32:35],v[224:227],v[196:199],a[32:35]\n"
"v_mfma_f32_16x16x32_bf16 a[36:39],v[228:231],v[196:199],a[36:39]\n"
"v_mfma_f32_16x16x32_bf16 a[36:39],v[164:167],v[132:135],a[36:39]\n"
"v_mfma_f32_16x16x32_bf16 a[40:43],v[168:171],v[132:135],a[40:43]\n"
"s_waitcnt lgkmcnt(0)\n"
"s_barrier\n"
"v_mfma_f32_16x16x32_bf16 a[40:43],v[232:235],v[196:199],a[40:43]\n"
"s_mov_b32 s77,2621440\n"
"s_add_u32 m0,s78,98304\n"
"v_mfma_f32_16x16x32_bf16 a[44:47],v[236:239],v[196:199],a[44:47]\n"
"buffer_load_dwordx4 v110,s[20:23],s77 offen offset:0 lds\n"
"v_mfma_f32_16x16x32_bf16 a[44:47],v[172:175],v[132:135],a[44:47]\n"
"v_mfma_f32_16x16x32_bf16 a[48:51],v[176:179],v[132:135],a[48:51]\n"
"v_mfma_f32_16x16x32_bf16 a[48:51],v[240:243],v[196:199],a[48:51]\n"
"v_mfma_f32_16x16x32_bf16 a[52:55],v[244:247],v[196:199],a[52:55]\n"
"buffer_load_dwordx4 v111,s[20:23],s77 offen offset:1024 lds\n"
"v_mfma_f32_16x16x32_bf16 a[52:55],v[180:183],v[132:135],a[52:55]\n"
"v_mfma_f32_16x16x32_bf16 a[56:59],v[184:187],v[132:135],a[56:59]\n"
"v_mfma_f32_16x16x32_bf16 a[56:59],v[248:251],v[196:199],a[56:59]\n"
"v_mfma_f32_16x16x32_bf16 a[60:63],v[252:255],v[196:199],a[60:63]\n"
"buffer_load_dwordx4 v113,s[20:23],s77 offen offset:2048 lds\n"
"v_mfma_f32_16x16x32_bf16 a[60:63],v[188:191],v[132:135],a[60:63]\n"
"v_mfma_f32_16x16x32_bf16 a[64:67],v[160:163],v[136:139],a[64:67]\n"
"v_mfma_f32_16x16x32_bf16 a[64:67],v[224:227],v[200:203],a[64:67]\n"
"v_mfma_f32_16x16x32_bf16 a[68:71],v[228:231],v[200:203],a[68:71]\n"
"buffer_load_dwordx4 v114,s[20:23],s77 offen offset:3072 lds\n"
"v_mfma_f32_16x16x32_bf16 a[68:71],v[164:167],v[136:139],a[68:71]\n"
"v_mfma_f32_16x16x32_bf16 a[72:75],v[168:171],v[136:139],a[72:75]\n"
"v_mfma_f32_16x16x32_bf16 a[72:75],v[232:235],v[200:203],a[72:75]\n"
"s_mov_b32 s77,2883584\n"
"s_add_u32 m0,s78,114688\n"
"v_mfma_f32_16x16x32_bf16 a[76:79],v[236:239],v[200:203],a[76:79]\n"
"buffer_load_dwordx4 v110,s[20:23],s77 offen offset:0 lds\n"
"v_mfma_f32_16x16x32_bf16 a[76:79],v[172:175],v[136:139],a[76:79]\n"
"v_mfma_f32_16x16x32_bf16 a[80:83],v[176:179],v[136:139],a[80:83]\n"
"v_mfma_f32_16x16x32_bf16 a[80:83],v[240:243],v[200:203],a[80:83]\n"
"v_mfma_f32_16x16x32_bf16 a[84:87],v[244:247],v[200:203],a[84:87]\n"
"buffer_load_dwordx4 v111,s[20:23],s77 offen offset:1024 lds\n"
"v_mfma_f32_16x16x32_bf16 a[84:87],v[180:183],v[136:139],a[84:87]\n"
"v_mfma_f32_16x16x32_bf16 a[88:91],v[184:187],v[136:139],a[88:91]\n"
"v_mfma_f32_16x16x32_bf16 a[88:91],v[248:251],v[200:203],a[88:91]\n"
"v_mfma_f32_16x16x32_bf16 a[92:95],v[252:255],v[200:203],a[92:95]\n"
"buffer_load_dwordx4 v113,s[20:23],s77 offen offset:2048 lds\n"
"v_mfma_f32_16x16x32_bf16 a[92:95],v[188:191],v[136:139],a[92:95]\n"
"s_waitcnt vmcnt(26)\n"
"v_mfma_f32_16x16x32_bf16 a[96:99],v[160:163],v[140:143],a[96:99]\n"
"v_mfma_f32_16x16x32_bf16 a[96:99],v[224:227],v[204:207],a[96:99]\n"
"v_mfma_f32_16x16x32_bf16 a[100:103],v[228:231],v[204:207],a[100:103]\n"
"buffer_load_dwordx4 v114,s[20:23],s77 offen offset:3072 lds\n"
"v_mfma_f32_16x16x32_bf16 a[100:103],v[164:167],v[140:143],a[100:103]\n"
"v_mfma_f32_16x16x32_bf16 a[104:107],v[168:171],v[140:143],a[104:107]\n"
"v_mfma_f32_16x16x32_bf16 a[104:107],v[232:235],v[204:207],a[104:107]\n"
"v_mfma_f32_16x16x32_bf16 a[108:111],v[236:239],v[204:207],a[108:111]\n"
"s_mov_b32 s76,768\n"
"s_add_u32 m0,s79,0\n"
"v_mfma_f32_16x16x32_bf16 a[108:111],v[172:175],v[140:143],a[108:111]\n"
"buffer_load_dwordx4 v106,s[16:19],s76 offen offset:0 lds\n"
"v_mfma_f32_16x16x32_bf16 a[112:115],v[176:179],v[140:143],a[112:115]\n"
"v_mfma_f32_16x16x32_bf16 a[112:115],v[240:243],v[204:207],a[112:115]\n"
"v_mfma_f32_16x16x32_bf16 a[116:119],v[244:247],v[204:207],a[116:119]\n"
"v_mfma_f32_16x16x32_bf16 a[116:119],v[180:183],v[140:143],a[116:119]\n"
"v_mfma_f32_16x16x32_bf16 a[120:123],v[184:187],v[140:143],a[120:123]\n"
"buffer_load_dwordx4 v107,s[16:19],s76 offen offset:1024 lds\n"
"v_mfma_f32_16x16x32_bf16 a[120:123],v[248:251],v[204:207],a[120:123]\n"
"v_mfma_f32_16x16x32_bf16 a[124:127],v[252:255],v[204:207],a[124:127]\n"
"v_mfma_f32_16x16x32_bf16 a[124:127],v[188:191],v[140:143],a[124:127]\n"
"v_mfma_f32_16x16x32_bf16 a[128:131],v[160:163],v[144:147],a[128:131]\n"
"v_mfma_f32_16x16x32_bf16 a[128:131],v[224:227],v[208:211],a[128:131]\n"
"buffer_load_dwordx4 v108,s[16:19],s76 offen offset:2048 lds\n"
"v_mfma_f32_16x16x32_bf16 a[132:135],v[228:231],v[208:211],a[132:135]\n"
"v_mfma_f32_16x16x32_bf16 a[132:135],v[164:167],v[144:147],a[132:135]\n"
"v_mfma_f32_16x16x32_bf16 a[136:139],v[168:171],v[144:147],a[136:139]\n"
"v_mfma_f32_16x16x32_bf16 a[136:139],v[232:235],v[208:211],a[136:139]\n"
"v_mfma_f32_16x16x32_bf16 a[140:143],v[236:239],v[208:211],a[140:143]\n"
"buffer_load_dwordx4 v109,s[16:19],s76 offen offset:3072 lds\n"
"v_mfma_f32_16x16x32_bf16 a[140:143],v[172:175],v[144:147],a[140:143]\n"
"v_mfma_f32_16x16x32_bf16 a[144:147],v[176:179],v[144:147],a[144:147]\n"
"v_mfma_f32_16x16x32_bf16 a[144:147],v[240:243],v[208:211],a[144:147]\n"
"buffer_store_dwordx2 v[124:125],off,s[24:27],s57 sc1\n"
"v_mfma_f32_16x16x32_bf16 a[148:151],v[244:247],v[208:211],a[148:151]\n"
"s_mov_b32 s76,262912\n"
"s_add_u32 m0,s79,4096\n"
"v_mfma_f32_16x16x32_bf16 a[148:151],v[180:183],v[144:147],a[148:151]\n"
"buffer_load_dwordx4 v106,s[16:19],s76 offen offset:0 lds\n"
"v_mfma_f32_16x16x32_bf16 a[152:155],v[184:187],v[144:147],a[152:155]\n"
"v_mfma_f32_16x16x32_bf16 a[152:155],v[248:251],v[208:211],a[152:155]\n"
"v_mfma_f32_16x16x32_bf16 a[156:159],v[252:255],v[208:211],a[156:159]\n"
"v_mfma_f32_16x16x32_bf16 a[156:159],v[188:191],v[144:147],a[156:159]\n"
"v_mfma_f32_16x16x32_bf16 a[160:163],v[160:163],v[148:151],a[160:163]\n"
"buffer_load_dwordx4 v107,s[16:19],s76 offen offset:1024 lds\n"
"v_mfma_f32_16x16x32_bf16 a[160:163],v[224:227],v[212:215],a[160:163]\n"
"v_mfma_f32_16x16x32_bf16 a[164:167],v[228:231],v[212:215],a[164:167]\n"
"v_mfma_f32_16x16x32_bf16 a[164:167],v[164:167],v[148:151],a[164:167]\n"
"v_mfma_f32_16x16x32_bf16 a[168:171],v[168:171],v[148:151],a[168:171]\n"
"v_mfma_f32_16x16x32_bf16 a[168:171],v[232:235],v[212:215],a[168:171]\n"
"buffer_load_dwordx4 v108,s[16:19],s76 offen offset:2048 lds\n"
"v_mfma_f32_16x16x32_bf16 a[172:175],v[236:239],v[212:215],a[172:175]\n"
"v_mfma_f32_16x16x32_bf16 a[172:175],v[172:175],v[148:151],a[172:175]\n"
"v_mfma_f32_16x16x32_bf16 a[176:179],v[176:179],v[148:151],a[176:179]\n"
"v_mfma_f32_16x16x32_bf16 a[176:179],v[240:243],v[212:215],a[176:179]\n"
"v_mfma_f32_16x16x32_bf16 a[180:183],v[244:247],v[212:215],a[180:183]\n"
"buffer_load_dwordx4 v109,s[16:19],s76 offen offset:3072 lds\n"
"v_mfma_f32_16x16x32_bf16 a[180:183],v[180:183],v[148:151],a[180:183]\n"
"s_mov_b32 s87,3145728\n"
"v_mfma_f32_16x16x32_bf16 a[184:187],v[184:187],v[148:151],a[184:187]\n"
"buffer_load_dword v115,v112,s[40:43],s87 offen\n"
"v_mfma_f32_16x16x32_bf16 a[184:187],v[248:251],v[212:215],a[184:187]\n"
"s_mov_b32 s87,4194304\n"
"v_mfma_f32_16x16x32_bf16 a[188:191],v[252:255],v[212:215],a[188:191]\n"
"buffer_load_dword v115,v112,s[40:43],s87 offen\n"
"v_mfma_f32_16x16x32_bf16 a[188:191],v[188:191],v[148:151],a[188:191]\n"
"s_mov_b32 s87,5242880\n"
"s_waitcnt vmcnt(30)\n"
"s_barrier\n"
"v_mfma_f32_16x16x32_bf16 a[192:195],v[224:227],v[216:219],a[192:195]\n"
"ds_read_b128 v[152:155],v80 offset:45056\n"
"buffer_load_dword v115,v112,s[40:43],s87 offen\n"
"v_mfma_f32_16x16x32_bf16 a[224:227],v[224:227],v[220:223],a[224:227]\n"
"ds_read_b128 v[156:159],v80 offset:47104\n"
"v_mfma_f32_16x16x32_bf16 a[228:231],v[228:231],v[220:223],a[228:231]\n"
"ds_read_b64_tr_b16 v[160:161],v98 offset:49152\n"
"v_mfma_f32_16x16x32_bf16 a[196:199],v[228:231],v[216:219],a[196:199]\n"
"ds_read_b64_tr_b16 v[162:163],v98 offset:50176\n"
"v_mfma_f32_16x16x32_bf16 a[200:203],v[232:235],v[216:219],a[200:203]\n"
"ds_read_b64_tr_b16 v[164:165],v99 offset:49152\n"
"v_mfma_f32_16x16x32_bf16 a[232:235],v[232:235],v[220:223],a[232:235]\n"
"ds_read_b64_tr_b16 v[166:167],v99 offset:50176\n"
"v_mfma_f32_16x16x32_bf16 a[236:239],v[236:239],v[220:223],a[236:239]\n"
"ds_read_b64_tr_b16 v[168:169],v100 offset:49152\n"
"v_mfma_f32_16x16x32_bf16 a[204:207],v[236:239],v[216:219],a[204:207]\n"
"ds_read_b64_tr_b16 v[170:171],v100 offset:50176\n"
"v_mfma_f32_16x16x32_bf16 a[208:211],v[240:243],v[216:219],a[208:211]\n"
"ds_read_b64_tr_b16 v[172:173],v101 offset:49152\n"
"v_mfma_f32_16x16x32_bf16 a[240:243],v[240:243],v[220:223],a[240:243]\n"
"ds_read_b64_tr_b16 v[174:175],v101 offset:50176\n"
"v_mfma_f32_16x16x32_bf16 a[244:247],v[244:247],v[220:223],a[244:247]\n"
"ds_read_b64_tr_b16 v[176:177],v102 offset:49152\n"
"v_mfma_f32_16x16x32_bf16 a[212:215],v[244:247],v[216:219],a[212:215]\n"
"ds_read_b64_tr_b16 v[178:179],v102 offset:50176\n"
"v_mfma_f32_16x16x32_bf16 a[216:219],v[248:251],v[216:219],a[216:219]\n"
"ds_read_b64_tr_b16 v[180:181],v103 offset:49152\n"
"v_mfma_f32_16x16x32_bf16 a[248:251],v[248:251],v[220:223],a[248:251]\n"
"ds_read_b64_tr_b16 v[182:183],v103 offset:50176\n"
"v_mfma_f32_16x16x32_bf16 a[252:255],v[252:255],v[220:223],a[252:255]\n"
"ds_read_b64_tr_b16 v[184:185],v104 offset:49152\n"
"v_mfma_f32_16x16x32_bf16 a[220:223],v[252:255],v[216:219],a[220:223]\n"
"ds_read_b64_tr_b16 v[186:187],v104 offset:50176\n"
"s_waitcnt lgkmcnt(10)\n"
"v_mfma_f32_16x16x32_bf16 a[192:195],v[160:163],v[152:155],a[192:195]\n"
"ds_read_b64_tr_b16 v[188:189],v105 offset:49152\n"
"v_mfma_f32_16x16x32_bf16 a[224:227],v[160:163],v[156:159],a[224:227]\n"
"ds_read_b64_tr_b16 v[190:191],v105 offset:50176\n"
"v_mfma_f32_16x16x32_bf16 a[228:231],v[164:167],v[156:159],a[228:231]\n"
"ds_read_b128 v[128:131],v80 offset:32768\n"
"v_mfma_f32_16x16x32_bf16 a[196:199],v[164:167],v[152:155],a[196:199]\n"
"ds_read_b128 v[192:195],v81 offset:32768\n"
"s_waitcnt lgkmcnt(10)\n"
"v_mfma_f32_16x16x32_bf16 a[200:203],v[168:171],v[152:155],a[200:203]\n"
"ds_read_b64_tr_b16 v[224:225],v98 offset:32768\n"
"v_mfma_f32_16x16x32_bf16 a[232:235],v[168:171],v[156:159],a[232:235]\n"
"ds_read_b64_tr_b16 v[226:227],v98 offset:33792\n"
"v_mfma_f32_16x16x32_bf16 a[236:239],v[172:175],v[156:159],a[236:239]\n"
"ds_read_b64_tr_b16 v[228:229],v99 offset:32768\n"
"v_mfma_f32_16x16x32_bf16 a[204:207],v[172:175],v[152:155],a[204:207]\n"
"ds_read_b64_tr_b16 v[230:231],v99 offset:33792\n"
"s_waitcnt lgkmcnt(10)\n"
"v_mfma_f32_16x16x32_bf16 a[208:211],v[176:179],v[152:155],a[208:211]\n"
"ds_read_b64_tr_b16 v[232:233],v100 offset:32768\n"
"v_mfma_f32_16x16x32_bf16 a[240:243],v[176:179],v[156:159],a[240:243]\n"
"ds_read_b64_tr_b16 v[234:235],v100 offset:33792\n"
"v_mfma_f32_16x16x32_bf16 a[244:247],v[180:183],v[156:159],a[244:247]\n"
"ds_read_b64_tr_b16 v[236:237],v101 offset:32768\n"
"v_mfma_f32_16x16x32_bf16 a[212:215],v[180:183],v[152:155],a[212:215]\n"
"ds_read_b64_tr_b16 v[238:239],v101 offset:33792\n"
"s_waitcnt lgkmcnt(10)\n"
"v_mfma_f32_16x16x32_bf16 a[216:219],v[184:187],v[152:155],a[216:219]\n"
"ds_read_b64_tr_b16 v[240:241],v102 offset:32768\n"
"v_mfma_f32_16x16x32_bf16 a[248:251],v[184:187],v[156:159],a[248:251]\n"
"ds_read_b64_tr_b16 v[242:243],v102 offset:33792\n"
"v_mfma_f32_16x16x32_bf16 a[252:255],v[188:191],v[156:159],a[252:255]\n"
"ds_read_b64_tr_b16 v[244:245],v103 offset:32768\n"
"v_mfma_f32_16x16x32_bf16 a[220:223],v[188:191],v[152:155],a[220:223]\n"
"ds_read_b64_tr_b16 v[246:247],v103 offset:33792\n"
"s_waitcnt lgkmcnt(8)\n"
"v_mfma_f32_16x16x32_bf16 a[0:3],v[160:163],v[128:131],a[0:3]\n"
"ds_read_b64_tr_b16 v[248:249],v104 offset:32768\n"
"v_mfma_f32_16x16x32_bf16 a[0:3],v[224:227],v[192:195],a[0:3]\n"
"ds_read_b64_tr_b16 v[250:251],v104 offset:33792\n"
"v_mfma_f32_16x16x32_bf16 a[4:7],v[228:231],v[192:195],a[4:7]\n"
"ds_read_b64_tr_b16 v[252:253],v105 offset:32768\n"
"v_mfma_f32_16x16x32_bf16 a[4:7],v[164:167],v[128:131],a[4:7]\n"
"ds_read_b64_tr_b16 v[254:255],v105 offset:33792\n"
"s_waitcnt lgkmcnt(8)\n"
"v_mfma_f32_16x16x32_bf16 a[8:11],v[168:171],v[128:131],a[8:11]\n"
"ds_read_b128 v[132:135],v80 offset:34816\n"
"v_mfma_f32_16x16x32_bf16 a[8:11],v[232:235],v[192:195],a[8:11]\n"
"ds_read_b128 v[196:199],v81 offset:34816\n"
"v_mfma_f32_16x16x32_bf16 a[12:15],v[236:239],v[192:195],a[12:15]\n"
"ds_read_b128 v[136:139],v80 offset:36864\n"
"v_mfma_f32_16x16x32_bf16 a[12:15],v[172:175],v[128:131],a[12:15]\n"
"ds_read_b128 v[200:203],v81 offset:36864\n"
"s_waitcnt lgkmcnt(8)\n"
"v_mfma_f32_16x16x32_bf16 a[16:19],v[176:179],v[128:131],a[16:19]\n"
"ds_read_b128 v[140:143],v80 offset:38912\n"
"v_mfma_f32_16x16x32_bf16 a[16:19],v[240:243],v[192:195],a[16:19]\n"
"ds_read_b128 v[204:207],v81 offset:38912\n"
"v_mfma_f32_16x16x32_bf16 a[20:23],v[244:247],v[192:195],a[20:23]\n"
"ds_read_b128 v[144:147],v80 offset:40960\n"
"v_mfma_f32_16x16x32_bf16 a[20:23],v[180:183],v[128:131],a[20:23]\n"
"ds_read_b128 v[208:211],v81 offset:40960\n"
"s_waitcnt lgkmcnt(8)\n"
"v_mfma_f32_16x16x32_bf16 a[24:27],v[184:187],v[128:131],a[24:27]\n"
"ds_read_b128 v[148:151],v80 offset:43008\n"
"v_mfma_f32_16x16x32_bf16 a[24:27],v[248:251],v[192:195],a[24:27]\n"
"ds_read_b128 v[212:215],v81 offset:43008\n"
"v_mfma_f32_16x16x32_bf16 a[28:31],v[252:255],v[192:195],a[28:31]\n"
"ds_read_b128 v[216:219],v81 offset:45056\n"
"v_mfma_f32_16x16x32_bf16 a[28:31],v[188:191],v[128:131],a[28:31]\n"
"ds_read_b128 v[220:223],v81 offset:47104\n"
"s_waitcnt lgkmcnt(10)\n"
"v_mfma_f32_16x16x32_bf16 a[32:35],v[160:163],v[132:135],a[32:35]\n"
"v_mfma_f32_16x16x32_bf16 a[32:35],v[224:227],v[196:199],a[32:35]\n"
"v_mfma_f32_16x16x32_bf16 a[36:39],v[228:231],v[196:199],a[36:39]\n"
"v_mfma_f32_16x16x32_bf16 a[36:39],v[164:167],v[132:135],a[36:39]\n"
"v_mfma_f32_16x16x32_bf16 a[40:43],v[168:171],v[132:135],a[40:43]\n"
"s_waitcnt lgkmcnt(0)\n"
"s_barrier\n"
"v_mfma_f32_16x16x32_bf16 a[40:43],v[232:235],v[196:199],a[40:43]\n"
"s_mov_b32 s77,3145728\n"
"s_add_u32 m0,s78,147456\n"
"v_mfma_f32_16x16x32_bf16 a[44:47],v[236:239],v[196:199],a[44:47]\n"
"buffer_load_dwordx4 v110,s[20:23],s77 offen offset:0 lds\n"
"v_mfma_f32_16x16x32_bf16 a[44:47],v[172:175],v[132:135],a[44:47]\n"
"v_mfma_f32_16x16x32_bf16 a[48:51],v[176:179],v[132:135],a[48:51]\n"
"v_mfma_f32_16x16x32_bf16 a[48:51],v[240:243],v[196:199],a[48:51]\n"
"v_mfma_f32_16x16x32_bf16 a[52:55],v[244:247],v[196:199],a[52:55]\n"
"buffer_load_dwordx4 v111,s[20:23],s77 offen offset:1024 lds\n"
"v_mfma_f32_16x16x32_bf16 a[52:55],v[180:183],v[132:135],a[52:55]\n"
"v_mfma_f32_16x16x32_bf16 a[56:59],v[184:187],v[132:135],a[56:59]\n"
"v_mfma_f32_16x16x32_bf16 a[56:59],v[248:251],v[196:199],a[56:59]\n"
"v_mfma_f32_16x16x32_bf16 a[60:63],v[252:255],v[196:199],a[60:63]\n"
"buffer_load_dwordx4 v113,s[20:23],s77 offen offset:2048 lds\n"
"v_mfma_f32_16x16x32_bf16 a[60:63],v[188:191],v[132:135],a[60:63]\n"
"v_mfma_f32_16x16x32_bf16 a[64:67],v[160:163],v[136:139],a[64:67]\n"
"v_mfma_f32_16x16x32_bf16 a[64:67],v[224:227],v[200:203],a[64:67]\n"
"v_mfma_f32_16x16x32_bf16 a[68:71],v[228:231],v[200:203],a[68:71]\n"
"buffer_load_dwordx4 v114,s[20:23],s77 offen offset:3072 lds\n"
"v_mfma_f32_16x16x32_bf16 a[68:71],v[164:167],v[136:139],a[68:71]\n"
"v_mfma_f32_16x16x32_bf16 a[72:75],v[168:171],v[136:139],a[72:75]\n"
"v_mfma_f32_16x16x32_bf16 a[72:75],v[232:235],v[200:203],a[72:75]\n"
"s_mov_b32 s77,3407872\n"
"s_add_u32 m0,s78,131072\n"
"v_mfma_f32_16x16x32_bf16 a[76:79],v[236:239],v[200:203],a[76:79]\n"
"buffer_load_dwordx4 v110,s[20:23],s77 offen offset:0 lds\n"
"v_mfma_f32_16x16x32_bf16 a[76:79],v[172:175],v[136:139],a[76:79]\n"
"v_mfma_f32_16x16x32_bf16 a[80:83],v[176:179],v[136:139],a[80:83]\n"
"v_mfma_f32_16x16x32_bf16 a[80:83],v[240:243],v[200:203],a[80:83]\n"
"v_mfma_f32_16x16x32_bf16 a[84:87],v[244:247],v[200:203],a[84:87]\n"
"buffer_load_dwordx4 v111,s[20:23],s77 offen offset:1024 lds\n"
"v_mfma_f32_16x16x32_bf16 a[84:87],v[180:183],v[136:139],a[84:87]\n"
"v_mfma_f32_16x16x32_bf16 a[88:91],v[184:187],v[136:139],a[88:91]\n"
"v_mfma_f32_16x16x32_bf16 a[88:91],v[248:251],v[200:203],a[88:91]\n"
"v_mfma_f32_16x16x32_bf16 a[92:95],v[252:255],v[200:203],a[92:95]\n"
"buffer_load_dwordx4 v113,s[20:23],s77 offen offset:2048 lds\n"
"v_mfma_f32_16x16x32_bf16 a[92:95],v[188:191],v[136:139],a[92:95]\n"
"v_mfma_f32_16x16x32_bf16 a[96:99],v[160:163],v[140:143],a[96:99]\n"
"v_mfma_f32_16x16x32_bf16 a[96:99],v[224:227],v[204:207],a[96:99]\n"
"v_mfma_f32_16x16x32_bf16 a[100:103],v[228:231],v[204:207],a[100:103]\n"
"buffer_load_dwordx4 v114,s[20:23],s77 offen offset:3072 lds\n"
"v_mfma_f32_16x16x32_bf16 a[100:103],v[164:167],v[140:143],a[100:103]\n"
"v_mfma_f32_16x16x32_bf16 a[104:107],v[168:171],v[140:143],a[104:107]\n"
"v_mfma_f32_16x16x32_bf16 a[104:107],v[232:235],v[204:207],a[104:107]\n"
"v_mfma_f32_16x16x32_bf16 a[108:111],v[236:239],v[204:207],a[108:111]\n"
"s_mov_b32 s76,896\n"
"s_add_u32 m0,s79,32768\n"
"v_mfma_f32_16x16x32_bf16 a[108:111],v[172:175],v[140:143],a[108:111]\n"
"buffer_load_dwordx4 v106,s[16:19],s76 offen offset:0 lds\n"
"v_mfma_f32_16x16x32_bf16 a[112:115],v[176:179],v[140:143],a[112:115]\n"
"v_mfma_f32_16x16x32_bf16 a[112:115],v[240:243],v[204:207],a[112:115]\n"
"v_mfma_f32_16x16x32_bf16 a[116:119],v[244:247],v[204:207],a[116:119]\n"
"v_mfma_f32_16x16x32_bf16 a[116:119],v[180:183],v[140:143],a[116:119]\n"
"v_mfma_f32_16x16x32_bf16 a[120:123],v[184:187],v[140:143],a[120:123]\n"
"buffer_load_dwordx4 v107,s[16:19],s76 offen offset:1024 lds\n"
"v_mfma_f32_16x16x32_bf16 a[120:123],v[248:251],v[204:207],a[120:123]\n"
"v_mfma_f32_16x16x32_bf16 a[124:127],v[252:255],v[204:207],a[124:127]\n"
"v_mfma_f32_16x16x32_bf16 a[124:127],v[188:191],v[140:143],a[124:127]\n"
"v_mfma_f32_16x16x32_bf16 a[128:131],v[160:163],v[144:147],a[128:131]\n"
"v_mfma_f32_16x16x32_bf16 a[128:131],v[224:227],v[208:211],a[128:131]\n"
"buffer_load_dwordx4 v108,s[16:19],s76 offen offset:2048 lds\n"
"v_mfma_f32_16x16x32_bf16 a[132:135],v[228:231],v[208:211],a[132:135]\n"
"v_mfma_f32_16x16x32_bf16 a[132:135],v[164:167],v[144:147],a[132:135]\n"
"v_mfma_f32_16x16x32_bf16 a[136:139],v[168:171],v[144:147],a[136:139]\n"
"v_mfma_f32_16x16x32_bf16 a[136:139],v[232:235],v[208:211],a[136:139]\n"
"v_mfma_f32_16x16x32_bf16 a[140:143],v[236:239],v[208:211],a[140:143]\n"
"buffer_load_dwordx4 v109,s[16:19],s76 offen offset:3072 lds\n"
"v_mfma_f32_16x16x32_bf16 a[140:143],v[172:175],v[144:147],a[140:143]\n"
"v_mfma_f32_16x16x32_bf16 a[144:147],v[176:179],v[144:147],a[144:147]\n"
"v_mfma_f32_16x16x32_bf16 a[144:147],v[240:243],v[208:211],a[144:147]\n"
"v_mfma_f32_16x16x32_bf16 a[148:151],v[244:247],v[208:211],a[148:151]\n"
"s_mov_b32 s76,263040\n"
"s_add_u32 m0,s79,36864\n"
"v_mfma_f32_16x16x32_bf16 a[148:151],v[180:183],v[144:147],a[148:151]\n"
"buffer_load_dwordx4 v106,s[16:19],s76 offen offset:0 lds\n"
"v_mfma_f32_16x16x32_bf16 a[152:155],v[184:187],v[144:147],a[152:155]\n"
"v_mfma_f32_16x16x32_bf16 a[152:155],v[248:251],v[208:211],a[152:155]\n"
"v_mfma_f32_16x16x32_bf16 a[156:159],v[252:255],v[208:211],a[156:159]\n"
"v_mfma_f32_16x16x32_bf16 a[156:159],v[188:191],v[144:147],a[156:159]\n"
"v_mfma_f32_16x16x32_bf16 a[160:163],v[160:163],v[148:151],a[160:163]\n"
"buffer_load_dwordx4 v107,s[16:19],s76 offen offset:1024 lds\n"
"v_mfma_f32_16x16x32_bf16 a[160:163],v[224:227],v[212:215],a[160:163]\n"
"v_mfma_f32_16x16x32_bf16 a[164:167],v[228:231],v[212:215],a[164:167]\n"
"v_mfma_f32_16x16x32_bf16 a[164:167],v[164:167],v[148:151],a[164:167]\n"
"v_mfma_f32_16x16x32_bf16 a[168:171],v[168:171],v[148:151],a[168:171]\n"
"v_mfma_f32_16x16x32_bf16 a[168:171],v[232:235],v[212:215],a[168:171]\n"
"buffer_load_dwordx4 v108,s[16:19],s76 offen offset:2048 lds\n"
"v_mfma_f32_16x16x32_bf16 a[172:175],v[236:239],v[212:215],a[172:175]\n"
"v_mfma_f32_16x16x32_bf16 a[172:175],v[172:175],v[148:151],a[172:175]\n"
"v_mfma_f32_16x16x32_bf16 a[176:179],v[176:179],v[148:151],a[176:179]\n"
"v_mfma_f32_16x16x32_bf16 a[176:179],v[240:243],v[212:215],a[176:179]\n"
"v_mfma_f32_16x16x32_bf16 a[180:183],v[244:247],v[212:215],a[180:183]\n"
"buffer_load_dwordx4 v109,s[16:19],s76 offen offset:3072 lds\n"
"v_mfma_f32_16x16x32_bf16 a[180:183],v[180:183],v[148:151],a[180:183]\n"
"s_mov_b32 s87,6291456\n"
"v_mfma_f32_16x16x32_bf16 a[184:187],v[184:187],v[148:151],a[184:187]\n"
"buffer_load_dword v115,v112,s[40:43],s87 offen\n"
"v_mfma_f32_16x16x32_bf16 a[184:187],v[248:251],v[212:215],a[184:187]\n"
"s_mov_b32 s87,7340032\n"
"v_mfma_f32_16x16x32_bf16 a[188:191],v[252:255],v[212:215],a[188:191]\n"
"buffer_load_dword v115,v112,s[40:43],s87 offen\n"
"v_mfma_f32_16x16x32_bf16 a[188:191],v[188:191],v[148:151],a[188:191]\n"
"s_waitcnt vmcnt(30)\n"
"s_barrier\n"
"v_mfma_f32_16x16x32_bf16 a[192:195],v[224:227],v[216:219],a[192:195]\n"
"ds_read_b128 v[152:155],v82 offset:12288\n"
"v_mfma_f32_16x16x32_bf16 a[224:227],v[224:227],v[220:223],a[224:227]\n"
"ds_read_b128 v[156:159],v82 offset:14336\n"
"v_mfma_f32_16x16x32_bf16 a[228:231],v[228:231],v[220:223],a[228:231]\n"
"ds_read_b64_tr_b16 v[160:161],v98 offset:0\n"
"v_mfma_f32_16x16x32_bf16 a[196:199],v[228:231],v[216:219],a[196:199]\n"
"ds_read_b64_tr_b16 v[162:163],v98 offset:1024\n"
"v_mfma_f32_16x16x32_bf16 a[200:203],v[232:235],v[216:219],a[200:203]\n"
"ds_read_b64_tr_b16 v[164:165],v99 offset:0\n"
"v_mfma_f32_16x16x32_bf16 a[232:235],v[232:235],v[220:223],a[232:235]\n"
"ds_read_b64_tr_b16 v[166:167],v99 offset:1024\n"
"v_mfma_f32_16x16x32_bf16 a[236:239],v[236:239],v[220:223],a[236:239]\n"
"ds_read_b64_tr_b16 v[168:169],v100 offset:0\n"
"v_mfma_f32_16x16x32_bf16 a[204:207],v[236:239],v[216:219],a[204:207]\n"
"ds_read_b64_tr_b16 v[170:171],v100 offset:1024\n"
"v_mfma_f32_16x16x32_bf16 a[208:211],v[240:243],v[216:219],a[208:211]\n"
"ds_read_b64_tr_b16 v[172:173],v101 offset:0\n"
"v_mfma_f32_16x16x32_bf16 a[240:243],v[240:243],v[220:223],a[240:243]\n"
"ds_read_b64_tr_b16 v[174:175],v101 offset:1024\n"
"v_mfma_f32_16x16x32_bf16 a[244:247],v[244:247],v[220:223],a[244:247]\n"
"ds_read_b64_tr_b16 v[176:177],v102 offset:0\n"
"v_mfma_f32_16x16x32_bf16 a[212:215],v[244:247],v[216:219],a[212:215]\n"
"ds_read_b64_tr_b16 v[178:179],v102 offset:1024\n"
"ds_read_b64_tr_b16 v[180:181],v103 offset:0\n"
"v_mfma_f32_16x16x32_bf16 a[216:219],v[248:251],v[216:219],a[216:219]\n"
"buffer_load_dwordx4 v[74:77],v112,s[24:27],s58 offen sc1\n"
"v_mfma_f32_16x16x32_bf16 a[248:251],v[248:251],v[220:223],a[248:251]\n"
"ds_read_b64_tr_b16 v[182:183],v103 offset:1024\n"
"v_mfma_f32_16x16x32_bf16 a[252:255],v[252:255],v[220:223],a[252:255]\n"
"ds_read_b64_tr_b16 v[184:185],v104 offset:0\n"
"v_mfma_f32_16x16x32_bf16 a[220:223],v[252:255],v[216:219],a[220:223]\n"
"ds_read_b64_tr_b16 v[186:187],v104 offset:1024\n"
"s_waitcnt lgkmcnt(10)\n"
"v_mfma_f32_16x16x32_bf16 a[192:195],v[160:163],v[152:155],a[192:195]\n"
"ds_read_b64_tr_b16 v[188:189],v105 offset:0\n"
"v_mfma_f32_16x16x32_bf16 a[224:227],v[160:163],v[156:159],a[224:227]\n"
"ds_read_b64_tr_b16 v[190:191],v105 offset:1024\n"
"v_mfma_f32_16x16x32_bf16 a[228:231],v[164:167],v[156:159],a[228:231]\n"
"ds_read_b128 v[128:131],v82 offset:0\n"
"v_mfma_f32_16x16x32_bf16 a[196:199],v[164:167],v[152:155],a[196:199]\n"
"ds_read_b128 v[192:195],v83 offset:0\n"
"s_waitcnt lgkmcnt(10)\n"
"v_mfma_f32_16x16x32_bf16 a[200:203],v[168:171],v[152:155],a[200:203]\n"
"ds_read_b64_tr_b16 v[224:225],v98 offset:16384\n"
"v_mfma_f32_16x16x32_bf16 a[232:235],v[168:171],v[156:159],a[232:235]\n"
"ds_read_b64_tr_b16 v[226:227],v98 offset:17408\n"
"v_mfma_f32_16x16x32_bf16 a[236:239],v[172:175],v[156:159],a[236:239]\n"
"ds_read_b64_tr_b16 v[228:229],v99 offset:16384\n"
"v_mfma_f32_16x16x32_bf16 a[204:207],v[172:175],v[152:155],a[204:207]\n"
"ds_read_b64_tr_b16 v[230:231],v99 offset:17408\n"
"s_waitcnt lgkmcnt(10)\n"
"v_mfma_f32_16x16x32_bf16 a[208:211],v[176:179],v[152:155],a[208:211]\n"
"ds_read_b64_tr_b16 v[232:233],v100 offset:16384\n"
"v_mfma_f32_16x16x32_bf16 a[240:243],v[176:179],v[156:159],a[240:243]\n"
"ds_read_b64_tr_b16 v[234:235],v100 offset:17408\n"
"v_mfma_f32_16x16x32_bf16 a[244:247],v[180:183],v[156:159],a[244:247]\n"
"ds_read_b64_tr_b16 v[236:237],v101 offset:16384\n"
"v_mfma_f32_16x16x32_bf16 a[212:215],v[180:183],v[152:155],a[212:215]\n"
"ds_read_b64_tr_b16 v[238:239],v101 offset:17408\n"
"s_waitcnt lgkmcnt(10)\n"
"v_mfma_f32_16x16x32_bf16 a[216:219],v[184:187],v[152:155],a[216:219]\n"
"ds_read_b64_tr_b16 v[240:241],v102 offset:16384\n"
"v_mfma_f32_16x16x32_bf16 a[248:251],v[184:187],v[156:159],a[248:251]\n"
"ds_read_b64_tr_b16 v[242:243],v102 offset:17408\n"
"v_mfma_f32_16x16x32_bf16 a[252:255],v[188:191],v[156:159],a[252:255]\n"
"ds_read_b64_tr_b16 v[244:245],v103 offset:16384\n"
"v_mfma_f32_16x16x32_bf16 a[220:223],v[188:191],v[152:155],a[220:223]\n"
"ds_read_b64_tr_b16 v[246:247],v103 offset:17408\n"
"s_waitcnt lgkmcnt(8)\n"
"v_mfma_f32_16x16x32_bf16 a[0:3],v[160:163],v[128:131],a[0:3]\n"
"ds_read_b64_tr_b16 v[248:249],v104 offset:16384\n"
"v_mfma_f32_16x16x32_bf16 a[0:3],v[224:227],v[192:195],a[0:3]\n"
"ds_read_b64_tr_b16 v[250:251],v104 offset:17408\n"
"v_mfma_f32_16x16x32_bf16 a[4:7],v[228:231],v[192:195],a[4:7]\n"
"ds_read_b64_tr_b16 v[252:253],v105 offset:16384\n"
"v_mfma_f32_16x16x32_bf16 a[4:7],v[164:167],v[128:131],a[4:7]\n"
"ds_read_b64_tr_b16 v[254:255],v105 offset:17408\n"
"s_waitcnt lgkmcnt(8)\n"
"v_mfma_f32_16x16x32_bf16 a[8:11],v[168:171],v[128:131],a[8:11]\n"
"ds_read_b128 v[132:135],v82 offset:2048\n"
"v_mfma_f32_16x16x32_bf16 a[8:11],v[232:235],v[192:195],a[8:11]\n"
"ds_read_b128 v[196:199],v83 offset:2048\n"
"v_mfma_f32_16x16x32_bf16 a[12:15],v[236:239],v[192:195],a[12:15]\n"
"ds_read_b128 v[136:139],v82 offset:4096\n"
"v_mfma_f32_16x16x32_bf16 a[12:15],v[172:175],v[128:131],a[12:15]\n"
"ds_read_b128 v[200:203],v83 offset:4096\n"
"s_waitcnt lgkmcnt(8)\n"
"v_mfma_f32_16x16x32_bf16 a[16:19],v[176:179],v[128:131],a[16:19]\n"
"ds_read_b128 v[140:143],v82 offset:6144\n"
"v_mfma_f32_16x16x32_bf16 a[16:19],v[240:243],v[192:195],a[16:19]\n"
"ds_read_b128 v[204:207],v83 offset:6144\n"
"v_mfma_f32_16x16x32_bf16 a[20:23],v[244:247],v[192:195],a[20:23]\n"
"ds_read_b128 v[144:147],v82 offset:8192\n"
"v_mfma_f32_16x16x32_bf16 a[20:23],v[180:183],v[128:131],a[20:23]\n"
"ds_read_b128 v[208:211],v83 offset:8192\n"
"s_waitcnt lgkmcnt(8)\n"
"v_mfma_f32_16x16x32_bf16 a[24:27],v[184:187],v[128:131],a[24:27]\n"
"ds_read_b128 v[148:151],v82 offset:10240\n"
"v_mfma_f32_16x16x32_bf16 a[24:27],v[248:251],v[192:195],a[24:27]\n"
"ds_read_b128 v[212:215],v83 offset:10240\n"
"v_mfma_f32_16x16x32_bf16 a[28:31],v[252:255],v[192:195],a[28:31]\n"
"ds_read_b128 v[216:219],v83 offset:12288\n"
"v_mfma_f32_16x16x32_bf16 a[28:31],v[188:191],v[128:131],a[28:31]\n"
"ds_read_b128 v[220:223],v83 offset:14336\n"
"s_waitcnt lgkmcnt(10)\n"
"v_mfma_f32_16x16x32_bf16 a[32:35],v[160:163],v[132:135],a[32:35]\n"
"v_mfma_f32_16x16x32_bf16 a[32:35],v[224:227],v[196:199],a[32:35]\n"
"v_mfma_f32_16x16x32_bf16 a[36:39],v[228:231],v[196:199],a[36:39]\n"
"v_mfma_f32_16x16x32_bf16 a[36:39],v[164:167],v[132:135],a[36:39]\n"
"v_mfma_f32_16x16x32_bf16 a[40:43],v[168:171],v[132:135],a[40:43]\n"
"s_waitcnt lgkmcnt(0)\n"
"s_barrier\n"
"v_mfma_f32_16x16x32_bf16 a[40:43],v[232:235],v[196:199],a[40:43]\n"
"s_mov_b32 s77,3670016\n"
"s_add_u32 m0,s78,98304\n"
"v_mfma_f32_16x16x32_bf16 a[44:47],v[236:239],v[196:199],a[44:47]\n"
"buffer_load_dwordx4 v110,s[20:23],s77 offen offset:0 lds\n"
"v_mfma_f32_16x16x32_bf16 a[44:47],v[172:175],v[132:135],a[44:47]\n"
"v_mfma_f32_16x16x32_bf16 a[48:51],v[176:179],v[132:135],a[48:51]\n"
"s_load_dwordx2 s[48:49],s[0:1],56\n"
"v_mfma_f32_16x16x32_bf16 a[48:51],v[240:243],v[196:199],a[48:51]\n"
"v_mfma_f32_16x16x32_bf16 a[52:55],v[244:247],v[196:199],a[52:55]\n"
"buffer_load_dwordx4 v111,s[20:23],s77 offen offset:1024 lds\n"
"v_mfma_f32_16x16x32_bf16 a[52:55],v[180:183],v[132:135],a[52:55]\n"
"v_mfma_f32_16x16x32_bf16 a[56:59],v[184:187],v[132:135],a[56:59]\n"
"v_mfma_f32_16x16x32_bf16 a[56:59],v[248:251],v[196:199],a[56:59]\n"
"v_mfma_f32_16x16x32_bf16 a[60:63],v[252:255],v[196:199],a[60:63]\n"
"buffer_load_dwordx4 v113,s[20:23],s77 offen offset:2048 lds\n"
"v_mfma_f32_16x16x32_bf16 a[60:63],v[188:191],v[132:135],a[60:63]\n"
"v_mfma_f32_16x16x32_bf16 a[64:67],v[160:163],v[136:139],a[64:67]\n"
"v_mfma_f32_16x16x32_bf16 a[64:67],v[224:227],v[200:203],a[64:67]\n"
"v_mfma_f32_16x16x32_bf16 a[68:71],v[228:231],v[200:203],a[68:71]\n"
"buffer_load_dwordx4 v114,s[20:23],s77 offen offset:3072 lds\n"
"v_mfma_f32_16x16x32_bf16 a[68:71],v[164:167],v[136:139],a[68:71]\n"
"v_mfma_f32_16x16x32_bf16 a[72:75],v[168:171],v[136:139],a[72:75]\n"
"v_mfma_f32_16x16x32_bf16 a[72:75],v[232:235],v[200:203],a[72:75]\n"
"s_mov_b32 s77,3932160\n"
"s_add_u32 m0,s78,114688\n"
"v_mfma_f32_16x16x32_bf16 a[76:79],v[236:239],v[200:203],a[76:79]\n"
"buffer_load_dwordx4 v110,s[20:23],s77 offen offset:0 lds\n"
"v_mfma_f32_16x16x32_bf16 a[76:79],v[172:175],v[136:139],a[76:79]\n"
"v_mfma_f32_16x16x32_bf16 a[80:83],v[176:179],v[136:139],a[80:83]\n"
"v_mfma_f32_16x16x32_bf16 a[80:83],v[240:243],v[200:203],a[80:83]\n"
"v_mfma_f32_16x16x32_bf16 a[84:87],v[244:247],v[200:203],a[84:87]\n"
"buffer_load_dwordx4 v111,s[20:23],s77 offen offset:1024 lds\n"
"v_mfma_f32_16x16x32_bf16 a[84:87],v[180:183],v[136:139],a[84:87]\n"
"v_mfma_f32_16x16x32_bf16 a[88:91],v[184:187],v[136:139],a[88:91]\n"
"v_mfma_f32_16x16x32_bf16 a[88:91],v[248:251],v[200:203],a[88:91]\n"
"v_mfma_f32_16x16x32_bf16 a[92:95],v[252:255],v[200:203],a[92:95]\n"
"buffer_load_dwordx4 v113,s[20:23],s77 offen offset:2048 lds\n"
"v_mfma_f32_16x16x32_bf16 a[92:95],v[188:191],v[136:139],a[92:95]\n"
"s_mov_b32 s76,s55\n"
"s_waitcnt vmcnt(7)\n"
"v_mfma_f32_16x16x32_bf16 a[96:99],v[160:163],v[140:143],a[96:99]\n"
"v_xor_b32 v116,s80,v74\n"
"v_xor_b32 v117,s81,v75\n"
"v_mfma_f32_16x16x32_bf16 a[96:99],v[224:227],v[204:207],a[96:99]\n"
"v_or_b32 v116,v116,v117\n"
"v_xor_b32 v117,s80,v76\n"
"v_mfma_f32_16x16x32_bf16 a[100:103],v[228:231],v[204:207],a[100:103]\n"
"buffer_load_dwordx4 v114,s[20:23],s77 offen offset:3072 lds\n"
"v_mfma_f32_16x16x32_bf16 a[100:103],v[164:167],v[140:143],a[100:103]\n"
"v_or_b32 v116,v116,v117\n"
"v_xor_b32 v117,s81,v77\n"
"v_mfma_f32_16x16x32_bf16 a[104:107],v[168:171],v[140:143],a[104:107]\n"
"v_or_b32 v116,v116,v117\n"
"v_cmp_ne_u32 vcc,0,v116\n"
"v_mfma_f32_16x16x32_bf16 a[104:107],v[232:235],v[204:207],a[104:107]\n"
"s_cbranch_vccnz .Lfk_xn0_s\n"
".Lfk_xn0_ok:\n"
"v_mfma_f32_16x16x32_bf16 a[108:111],v[236:239],v[204:207],a[108:111]\n"
"s_add_u32 m0,s78,65536\n"
"v_mfma_f32_16x16x32_bf16 a[108:111],v[172:175],v[140:143],a[108:111]\n"
"buffer_load_dwordx4 v112,s[24:27],s76 offen offset:0 sc1 lds\n"
"v_mfma_f32_16x16x32_bf16 a[112:115],v[176:179],v[140:143],a[112:115]\n"
"s_waitcnt lgkmcnt(0)\n"
"s_lshl2_add_u32 s88,s100,s52\n"
"v_mfma_f32_16x16x32_bf16 a[112:115],v[240:243],v[204:207],a[112:115]\n"
"s_mul_i32 s88,s88,7168\n"
"s_add_u32 s88,s88,0\n"
"v_mfma_f32_16x16x32_bf16 a[116:119],v[244:247],v[204:207],a[116:119]\n"
"s_add_u32 s48,s48,s88\n"
"s_addc_u32 s49,s49,0\n"
"s_mov_b32 s50,3584\n"
"v_mfma_f32_16x16x32_bf16 a[116:119],v[180:183],v[140:143],a[116:119]\n"
"s_mov_b32 s51,0x20000\n"
"v_lshlrev_b32 v8,3,v112\n"
"v_mfma_f32_16x16x32_bf16 a[120:123],v[184:187],v[140:143],a[120:123]\n"
"buffer_load_dwordx4 v112,s[24:27],s76 offen offset:1024 sc1 lds\n"
"v_mfma_f32_16x16x32_bf16 a[120:123],v[248:251],v[204:207],a[120:123]\n"
"v_mfma_f32_16x16x32_bf16 a[124:127],v[252:255],v[204:207],a[124:127]\n"
"v_mfma_f32_16x16x32_bf16 a[124:127],v[188:191],v[140:143],a[124:127]\n"
"v_mfma_f32_16x16x32_bf16 a[128:131],v[160:163],v[144:147],a[128:131]\n"
"v_mfma_f32_16x16x32_bf16 a[128:131],v[224:227],v[208:211],a[128:131]\n"
"buffer_load_dwordx4 v112,s[24:27],s76 offen offset:2048 sc1 lds\n"
"v_mfma_f32_16x16x32_bf16 a[132:135],v[228:231],v[208:211],a[132:135]\n"
"v_mfma_f32_16x16x32_bf16 a[132:135],v[164:167],v[144:147],a[132:135]\n"
"v_mfma_f32_16x16x32_bf16 a[136:139],v[168:171],v[144:147],a[136:139]\n"
"v_mfma_f32_16x16x32_bf16 a[136:139],v[232:235],v[208:211],a[136:139]\n"
"buffer_load_dwordx4 v112,s[24:27],s76 offen offset:3072 sc1 lds\n"
"v_mfma_f32_16x16x32_bf16 a[140:143],v[236:239],v[208:211],a[140:143]\n"
"s_xor_b32 s76,s76,16384\n"
"v_mfma_f32_16x16x32_bf16 a[140:143],v[172:175],v[144:147],a[140:143]\n"
"v_mfma_f32_16x16x32_bf16 a[144:147],v[176:179],v[144:147],a[144:147]\n"
"v_mfma_f32_16x16x32_bf16 a[144:147],v[240:243],v[208:211],a[144:147]\n"
"v_mfma_f32_16x16x32_bf16 a[148:151],v[244:247],v[208:211],a[148:151]\n"
"s_add_u32 m0,s78,81920\n"
"v_mfma_f32_16x16x32_bf16 a[148:151],v[180:183],v[144:147],a[148:151]\n"
"buffer_load_dwordx4 v112,s[24:27],s76 offen offset:0 sc1 lds\n"
"v_mfma_f32_16x16x32_bf16 a[152:155],v[184:187],v[144:147],a[152:155]\n"
"v_mfma_f32_16x16x32_bf16 a[152:155],v[248:251],v[208:211],a[152:155]\n"
"v_mfma_f32_16x16x32_bf16 a[156:159],v[252:255],v[208:211],a[156:159]\n"
"v_mfma_f32_16x16x32_bf16 a[156:159],v[188:191],v[144:147],a[156:159]\n"
"v_mfma_f32_16x16x32_bf16 a[160:163],v[160:163],v[148:151],a[160:163]\n"
"buffer_load_dwordx4 v112,s[24:27],s76 offen offset:1024 sc1 lds\n"
"v_mfma_f32_16x16x32_bf16 a[160:163],v[224:227],v[212:215],a[160:163]\n"
"v_mfma_f32_16x16x32_bf16 a[164:167],v[228:231],v[212:215],a[164:167]\n"
"v_mfma_f32_16x16x32_bf16 a[164:167],v[164:167],v[148:151],a[164:167]\n"
"v_mfma_f32_16x16x32_bf16 a[168:171],v[168:171],v[148:151],a[168:171]\n"
"v_mfma_f32_16x16x32_bf16 a[168:171],v[232:235],v[212:215],a[168:171]\n"
"buffer_load_dwordx4 v112,s[24:27],s76 offen offset:2048 sc1 lds\n"
"v_mfma_f32_16x16x32_bf16 a[172:175],v[236:239],v[212:215],a[172:175]\n"
"v_mfma_f32_16x16x32_bf16 a[172:175],v[172:175],v[148:151],a[172:175]\n"
"v_mfma_f32_16x16x32_bf16 a[176:179],v[176:179],v[148:151],a[176:179]\n"
"v_mfma_f32_16x16x32_bf16 a[176:179],v[240:243],v[212:215],a[176:179]\n"
"buffer_load_dwordx4 v112,s[24:27],s76 offen offset:3072 sc1 lds\n"
"v_mfma_f32_16x16x32_bf16 a[180:183],v[244:247],v[212:215],a[180:183]\n"
"s_xor_b32 s76,s76,49152\n"
"v_mfma_f32_16x16x32_bf16 a[180:183],v[180:183],v[148:151],a[180:183]\n"
"v_mfma_f32_16x16x32_bf16 a[184:187],v[184:187],v[148:151],a[184:187]\n"
"buffer_load_dword v115,v8,s[48:51],0 offen\n"
"v_mfma_f32_16x16x32_bf16 a[184:187],v[248:251],v[212:215],a[184:187]\n"
"v_mfma_f32_16x16x32_bf16 a[188:191],v[252:255],v[212:215],a[188:191]\n"
"v_mfma_f32_16x16x32_bf16 a[188:191],v[188:191],v[148:151],a[188:191]\n"
"s_barrier\n"
"v_mfma_f32_16x16x32_bf16 a[192:195],v[224:227],v[216:219],a[192:195]\n"
"ds_read_b128 v[152:155],v80 offset:12288\n"
"v_mfma_f32_16x16x32_bf16 a[224:227],v[224:227],v[220:223],a[224:227]\n"
"ds_read_b128 v[156:159],v80 offset:14336\n"
"v_mfma_f32_16x16x32_bf16 a[228:231],v[228:231],v[220:223],a[228:231]\n"
"ds_read_b64_tr_b16 v[160:161],v98 offset:49152\n"
"v_mfma_f32_16x16x32_bf16 a[196:199],v[228:231],v[216:219],a[196:199]\n"
"ds_read_b64_tr_b16 v[162:163],v98 offset:50176\n"
"v_mfma_f32_16x16x32_bf16 a[200:203],v[232:235],v[216:219],a[200:203]\n"
"ds_read_b64_tr_b16 v[164:165],v99 offset:49152\n"
"v_mfma_f32_16x16x32_bf16 a[232:235],v[232:235],v[220:223],a[232:235]\n"
"ds_read_b64_tr_b16 v[166:167],v99 offset:50176\n"
"v_mfma_f32_16x16x32_bf16 a[236:239],v[236:239],v[220:223],a[236:239]\n"
"ds_read_b64_tr_b16 v[168:169],v100 offset:49152\n"
"v_mfma_f32_16x16x32_bf16 a[204:207],v[236:239],v[216:219],a[204:207]\n"
"ds_read_b64_tr_b16 v[170:171],v100 offset:50176\n"
"v_mfma_f32_16x16x32_bf16 a[208:211],v[240:243],v[216:219],a[208:211]\n"
"ds_read_b64_tr_b16 v[172:173],v101 offset:49152\n"
"v_mfma_f32_16x16x32_bf16 a[240:243],v[240:243],v[220:223],a[240:243]\n"
"ds_read_b64_tr_b16 v[174:175],v101 offset:50176\n"
"v_mfma_f32_16x16x32_bf16 a[244:247],v[244:247],v[220:223],a[244:247]\n"
"ds_read_b64_tr_b16 v[176:177],v102 offset:49152\n"
"v_mfma_f32_16x16x32_bf16 a[212:215],v[244:247],v[216:219],a[212:215]\n"
"ds_read_b64_tr_b16 v[178:179],v102 offset:50176\n"
"v_mfma_f32_16x16x32_bf16 a[216:219],v[248:251],v[216:219],a[216:219]\n"
"ds_read_b64_tr_b16 v[180:181],v103 offset:49152\n"
"v_mfma_f32_16x16x32_bf16 a[248:251],v[248:251],v[220:223],a[248:251]\n"
"ds_read_b64_tr_b16 v[182:183],v103 offset:50176\n"
"ds_read_b64_tr_b16 v[184:185],v104 offset:49152\n"
"v_mfma_f32_16x16x32_bf16 a[252:255],v[252:255],v[220:223],a[252:255]\n"
"v_mov_b32 v110,v72\n"
"v_mov_b32 v111,v73\n"
"v_mfma_f32_16x16x32_bf16 a[220:223],v[252:255],v[216:219],a[220:223]\n"
"ds_read_b64_tr_b16 v[186:187],v104 offset:50176\n"
"s_waitcnt lgkmcnt(10)\n"
"v_mfma_f32_16x16x32_bf16 a[192:195],v[160:163],v[152:155],a[192:195]\n"
"ds_read_b64_tr_b16 v[188:189],v105 offset:49152\n"
"v_mfma_f32_16x16x32_bf16 a[224:227],v[160:163],v[156:159],a[224:227]\n"
"ds_read_b64_tr_b16 v[190:191],v105 offset:50176\n"
"v_mfma_f32_16x16x32_bf16 a[228:231],v[164:167],v[156:159],a[228:231]\n"
"ds_read_b128 v[128:131],v80 offset:0\n"
"v_mfma_f32_16x16x32_bf16 a[196:199],v[164:167],v[152:155],a[196:199]\n"
"ds_read_b128 v[192:195],v81 offset:0\n"
"s_waitcnt lgkmcnt(10)\n"
"v_mfma_f32_16x16x32_bf16 a[200:203],v[168:171],v[152:155],a[200:203]\n"
"ds_read_b64_tr_b16 v[224:225],v98 offset:32768\n"
"v_mfma_f32_16x16x32_bf16 a[232:235],v[168:171],v[156:159],a[232:235]\n"
"ds_read_b64_tr_b16 v[226:227],v98 offset:33792\n"
"v_mfma_f32_16x16x32_bf16 a[236:239],v[172:175],v[156:159],a[236:239]\n"
"ds_read_b64_tr_b16 v[228:229],v99 offset:32768\n"
"v_mfma_f32_16x16x32_bf16 a[204:207],v[172:175],v[152:155],a[204:207]\n"
"ds_read_b64_tr_b16 v[230:231],v99 offset:33792\n"
"s_waitcnt lgkmcnt(10)\n"
"v_mfma_f32_16x16x32_bf16 a[208:211],v[176:179],v[152:155],a[208:211]\n"
"ds_read_b64_tr_b16 v[232:233],v100 offset:32768\n"
"v_mfma_f32_16x16x32_bf16 a[240:243],v[176:179],v[156:159],a[240:243]\n"
"ds_read_b64_tr_b16 v[234:235],v100 offset:33792\n"
"v_mfma_f32_16x16x32_bf16 a[244:247],v[180:183],v[156:159],a[244:247]\n"
"ds_read_b64_tr_b16 v[236:237],v101 offset:32768\n"
"v_mfma_f32_16x16x32_bf16 a[212:215],v[180:183],v[152:155],a[212:215]\n"
"ds_read_b64_tr_b16 v[238:239],v101 offset:33792\n"
"s_waitcnt lgkmcnt(10)\n"
"v_mfma_f32_16x16x32_bf16 a[216:219],v[184:187],v[152:155],a[216:219]\n"
"ds_read_b64_tr_b16 v[240:241],v102 offset:32768\n"
"v_mfma_f32_16x16x32_bf16 a[248:251],v[184:187],v[156:159],a[248:251]\n"
"ds_read_b64_tr_b16 v[242:243],v102 offset:33792\n"
"v_mfma_f32_16x16x32_bf16 a[252:255],v[188:191],v[156:159],a[252:255]\n"
"ds_read_b64_tr_b16 v[244:245],v103 offset:32768\n"
"v_mfma_f32_16x16x32_bf16 a[220:223],v[188:191],v[152:155],a[220:223]\n"
"ds_read_b64_tr_b16 v[246:247],v103 offset:33792\n"
"s_waitcnt lgkmcnt(8)\n"
"v_mfma_f32_16x16x32_bf16 a[0:3],v[160:163],v[128:131],a[0:3]\n"
"ds_read_b64_tr_b16 v[248:249],v104 offset:32768\n"
"v_mfma_f32_16x16x32_bf16 a[0:3],v[224:227],v[192:195],a[0:3]\n"
"ds_read_b64_tr_b16 v[250:251],v104 offset:33792\n"
"v_mfma_f32_16x16x32_bf16 a[4:7],v[228:231],v[192:195],a[4:7]\n"
"ds_read_b64_tr_b16 v[252:253],v105 offset:32768\n"
"v_mfma_f32_16x16x32_bf16 a[4:7],v[164:167],v[128:131],a[4:7]\n"
"ds_read_b64_tr_b16 v[254:255],v105 offset:33792\n"
"s_waitcnt lgkmcnt(8)\n"
"v_mfma_f32_16x16x32_bf16 a[8:11],v[168:171],v[128:131],a[8:11]\n"
"ds_read_b128 v[132:135],v80 offset:2048\n"
"v_mfma_f32_16x16x32_bf16 a[8:11],v[232:235],v[192:195],a[8:11]\n"
"ds_read_b128 v[196:199],v81 offset:2048\n"
"v_mfma_f32_16x16x32_bf16 a[12:15],v[236:239],v[192:195],a[12:15]\n"
"ds_read_b128 v[136:139],v80 offset:4096\n"
"v_mfma_f32_16x16x32_bf16 a[12:15],v[172:175],v[128:131],a[12:15]\n"
"ds_read_b128 v[200:203],v81 offset:4096\n"
"s_waitcnt lgkmcnt(8)\n"
"v_mfma_f32_16x16x32_bf16 a[16:19],v[176:179],v[128:131],a[16:19]\n"
"ds_read_b128 v[140:143],v80 offset:6144\n"
"v_mfma_f32_16x16x32_bf16 a[16:19],v[240:243],v[192:195],a[16:19]\n"
"ds_read_b128 v[204:207],v81 offset:6144\n"
"v_mfma_f32_16x16x32_bf16 a[20:23],v[244:247],v[192:195],a[20:23]\n"
"ds_read_b128 v[144:147],v80 offset:8192\n"
"v_mfma_f32_16x16x32_bf16 a[20:23],v[180:183],v[128:131],a[20:23]\n"
"ds_read_b128 v[208:211],v81 offset:8192\n"
"s_waitcnt lgkmcnt(8)\n"
"v_mfma_f32_16x16x32_bf16 a[24:27],v[184:187],v[128:131],a[24:27]\n"
"ds_read_b128 v[148:151],v80 offset:10240\n"
"v_mfma_f32_16x16x32_bf16 a[24:27],v[248:251],v[192:195],a[24:27]\n"
"ds_read_b128 v[212:215],v81 offset:10240\n"
"v_mfma_f32_16x16x32_bf16 a[28:31],v[252:255],v[192:195],a[28:31]\n"
"ds_read_b128 v[216:219],v81 offset:12288\n"
"v_mfma_f32_16x16x32_bf16 a[28:31],v[188:191],v[128:131],a[28:31]\n"
"ds_read_b128 v[220:223],v81 offset:14336\n"
"s_waitcnt lgkmcnt(10)\n"
"v_mfma_f32_16x16x32_bf16 a[32:35],v[160:163],v[132:135],a[32:35]\n"
"v_mfma_f32_16x16x32_bf16 a[32:35],v[224:227],v[196:199],a[32:35]\n"
"v_mfma_f32_16x16x32_bf16 a[36:39],v[228:231],v[196:199],a[36:39]\n"
"v_mfma_f32_16x16x32_bf16 a[36:39],v[164:167],v[132:135],a[36:39]\n"
"v_mfma_f32_16x16x32_bf16 a[40:43],v[168:171],v[132:135],a[40:43]\n"
"s_waitcnt lgkmcnt(0)\n"
"s_barrier\n"
"v_mfma_f32_16x16x32_bf16 a[40:43],v[232:235],v[196:199],a[40:43]\n"
"s_add_u32 m0,s85,147456\n"
"v_mfma_f32_16x16x32_bf16 a[44:47],v[236:239],v[196:199],a[44:47]\n"
"buffer_load_dwordx4 v110,s[28:31],s86 offen lds\n"
"v_mfma_f32_16x16x32_bf16 a[44:47],v[172:175],v[132:135],a[44:47]\n"
"v_mfma_f32_16x16x32_bf16 a[48:51],v[176:179],v[132:135],a[48:51]\n"
"s_load_dwordx2 s[48:49],s[0:1],56\n"
"v_mfma_f32_16x16x32_bf16 a[48:51],v[240:243],v[196:199],a[48:51]\n"
"s_add_u32 m0,s85,148480\n"
"v_mfma_f32_16x16x32_bf16 a[52:55],v[244:247],v[196:199],a[52:55]\n"
"buffer_load_dwordx4 v111,s[28:31],s86 offen lds\n"
"v_mfma_f32_16x16x32_bf16 a[52:55],v[180:183],v[132:135],a[52:55]\n"
"s_xor_b32 s86,s86,262144\n"
"v_mfma_f32_16x16x32_bf16 a[56:59],v[184:187],v[132:135],a[56:59]\n"
"v_mfma_f32_16x16x32_bf16 a[56:59],v[248:251],v[196:199],a[56:59]\n"
"s_add_u32 m0,s85,155648\n"
"v_mfma_f32_16x16x32_bf16 a[60:63],v[252:255],v[196:199],a[60:63]\n"
"buffer_load_dwordx4 v110,s[28:31],s86 offen lds\n"
"v_mfma_f32_16x16x32_bf16 a[60:63],v[188:191],v[132:135],a[60:63]\n"
"v_mfma_f32_16x16x32_bf16 a[64:67],v[160:163],v[136:139],a[64:67]\n"
"v_mfma_f32_16x16x32_bf16 a[64:67],v[224:227],v[200:203],a[64:67]\n"
"s_add_u32 m0,s85,156672\n"
"v_mfma_f32_16x16x32_bf16 a[68:71],v[228:231],v[200:203],a[68:71]\n"
"buffer_load_dwordx4 v111,s[28:31],s86 offen lds\n"
"v_mfma_f32_16x16x32_bf16 a[68:71],v[164:167],v[136:139],a[68:71]\n"
"s_xor_b32 s86,s86,786432\n"
"v_mfma_f32_16x16x32_bf16 a[72:75],v[168:171],v[136:139],a[72:75]\n"
"v_mfma_f32_16x16x32_bf16 a[72:75],v[232:235],v[200:203],a[72:75]\n"
"s_add_u32 m0,s85,131072\n"
"v_mfma_f32_16x16x32_bf16 a[76:79],v[236:239],v[200:203],a[76:79]\n"
"buffer_load_dwordx4 v110,s[28:31],s86 offen lds\n"
"v_mfma_f32_16x16x32_bf16 a[76:79],v[172:175],v[136:139],a[76:79]\n"
"v_mfma_f32_16x16x32_bf16 a[80:83],v[176:179],v[136:139],a[80:83]\n"
"v_mfma_f32_16x16x32_bf16 a[80:83],v[240:243],v[200:203],a[80:83]\n"
"s_add_u32 m0,s85,132096\n"
"v_mfma_f32_16x16x32_bf16 a[84:87],v[244:247],v[200:203],a[84:87]\n"
"buffer_load_dwordx4 v111,s[28:31],s86 offen lds\n"
"v_mfma_f32_16x16x32_bf16 a[84:87],v[180:183],v[136:139],a[84:87]\n"
"s_xor_b32 s86,s86,262144\n"
"v_mfma_f32_16x16x32_bf16 a[88:91],v[184:187],v[136:139],a[88:91]\n"
"v_mfma_f32_16x16x32_bf16 a[88:91],v[248:251],v[200:203],a[88:91]\n"
"v_mfma_f32_16x16x32_bf16 a[92:95],v[252:255],v[200:203],a[92:95]\n"
"v_mfma_f32_16x16x32_bf16 a[92:95],v[188:191],v[136:139],a[92:95]\n"
"v_mfma_f32_16x16x32_bf16 a[96:99],v[160:163],v[140:143],a[96:99]\n"
"v_mfma_f32_16x16x32_bf16 a[96:99],v[224:227],v[204:207],a[96:99]\n"
"s_add_u32 m0,s85,139264\n"
"v_mfma_f32_16x16x32_bf16 a[100:103],v[228:231],v[204:207],a[100:103]\n"
"buffer_load_dwordx4 v110,s[28:31],s86 offen lds\n"
"v_mfma_f32_16x16x32_bf16 a[100:103],v[164:167],v[140:143],a[100:103]\n"
"v_mfma_f32_16x16x32_bf16 a[104:107],v[168:171],v[140:143],a[104:107]\n"
"v_mfma_f32_16x16x32_bf16 a[104:107],v[232:235],v[204:207],a[104:107]\n"
"s_add_u32 m0,s85,140288\n"
"v_mfma_f32_16x16x32_bf16 a[108:111],v[236:239],v[204:207],a[108:111]\n"
"buffer_load_dwordx4 v111,s[28:31],s86 offen lds\n"
"v_mfma_f32_16x16x32_bf16 a[108:111],v[172:175],v[140:143],a[108:111]\n"
"s_xor_b32 s86,s86,1835008\n"
"v_mfma_f32_16x16x32_bf16 a[112:115],v[176:179],v[140:143],a[112:115]\n"
"s_waitcnt lgkmcnt(0)\n"
"s_lshl2_add_u32 s88,s100,s52\n"
"v_mfma_f32_16x16x32_bf16 a[112:115],v[240:243],v[204:207],a[112:115]\n"
"s_mul_i32 s88,s88,7168\n"
"s_add_u32 m0,s78,0\n"
"v_mfma_f32_16x16x32_bf16 a[116:119],v[244:247],v[204:207],a[116:119]\n"
"buffer_load_dwordx4 v112,s[24:27],s76 offen offset:0 sc1 lds\n"
"v_mfma_f32_16x16x32_bf16 a[116:119],v[180:183],v[140:143],a[116:119]\n"
"s_add_u32 s88,s88,3584\n"
"s_add_u32 s48,s48,s88\n"
"s_addc_u32 s49,s49,0\n"
"v_mfma_f32_16x16x32_bf16 a[120:123],v[184:187],v[140:143],a[120:123]\n"
"s_mov_b32 s50,3584\n"
"s_mov_b32 s51,0x20000\n"
"v_mfma_f32_16x16x32_bf16 a[120:123],v[248:251],v[204:207],a[120:123]\n"
"v_lshlrev_b32 v8,3,v112\n"
"v_mfma_f32_16x16x32_bf16 a[124:127],v[252:255],v[204:207],a[124:127]\n"
"buffer_load_dwordx4 v112,s[24:27],s76 offen offset:1024 sc1 lds\n"
"v_mfma_f32_16x16x32_bf16 a[124:127],v[188:191],v[140:143],a[124:127]\n"
"v_mfma_f32_16x16x32_bf16 a[128:131],v[160:163],v[144:147],a[128:131]\n"
"v_mfma_f32_16x16x32_bf16 a[128:131],v[224:227],v[208:211],a[128:131]\n"
"v_mfma_f32_16x16x32_bf16 a[132:135],v[228:231],v[208:211],a[132:135]\n"
"buffer_load_dwordx4 v112,s[24:27],s76 offen offset:2048 sc1 lds\n"
"v_mfma_f32_16x16x32_bf16 a[132:135],v[164:167],v[144:147],a[132:135]\n"
"v_mfma_f32_16x16x32_bf16 a[136:139],v[168:171],v[144:147],a[136:139]\n"
"v_mfma_f32_16x16x32_bf16 a[136:139],v[232:235],v[208:211],a[136:139]\n"
"buffer_load_dwordx4 v112,s[24:27],s76 offen offset:3072 sc1 lds\n"
"v_mfma_f32_16x16x32_bf16 a[140:143],v[236:239],v[208:211],a[140:143]\n"
"s_xor_b32 s76,s76,16384\n"
"v_mfma_f32_16x16x32_bf16 a[140:143],v[172:175],v[144:147],a[140:143]\n"
"v_mfma_f32_16x16x32_bf16 a[144:147],v[176:179],v[144:147],a[144:147]\n"
"v_mfma_f32_16x16x32_bf16 a[144:147],v[240:243],v[208:211],a[144:147]\n"
"s_add_u32 m0,s78,16384\n"
"v_mfma_f32_16x16x32_bf16 a[148:151],v[244:247],v[208:211],a[148:151]\n"
"buffer_load_dwordx4 v112,s[24:27],s76 offen offset:0 sc1 lds\n"
"v_mfma_f32_16x16x32_bf16 a[148:151],v[180:183],v[144:147],a[148:151]\n"
"v_mfma_f32_16x16x32_bf16 a[152:155],v[184:187],v[144:147],a[152:155]\n"
"v_mfma_f32_16x16x32_bf16 a[152:155],v[248:251],v[208:211],a[152:155]\n"
"v_mfma_f32_16x16x32_bf16 a[156:159],v[252:255],v[208:211],a[156:159]\n"
"buffer_load_dwordx4 v112,s[24:27],s76 offen offset:1024 sc1 lds\n"
"v_mfma_f32_16x16x32_bf16 a[156:159],v[188:191],v[144:147],a[156:159]\n"
"v_mfma_f32_16x16x32_bf16 a[160:163],v[160:163],v[148:151],a[160:163]\n"
"v_mfma_f32_16x16x32_bf16 a[160:163],v[224:227],v[212:215],a[160:163]\n"
"v_mfma_f32_16x16x32_bf16 a[164:167],v[228:231],v[212:215],a[164:167]\n"
"buffer_load_dwordx4 v112,s[24:27],s76 offen offset:2048 sc1 lds\n"
"v_mfma_f32_16x16x32_bf16 a[164:167],v[164:167],v[148:151],a[164:167]\n"
"v_mfma_f32_16x16x32_bf16 a[168:171],v[168:171],v[148:151],a[168:171]\n"
"v_mfma_f32_16x16x32_bf16 a[168:171],v[232:235],v[212:215],a[168:171]\n"
"buffer_load_dwordx4 v112,s[24:27],s76 offen offset:3072 sc1 lds\n"
"v_mfma_f32_16x16x32_bf16 a[172:175],v[236:239],v[212:215],a[172:175]\n"
"s_xor_b32 s76,s76,114688\n"
"v_mfma_f32_16x16x32_bf16 a[172:175],v[172:175],v[148:151],a[172:175]\n"
"v_mfma_f32_16x16x32_bf16 a[176:179],v[176:179],v[148:151],a[176:179]\n"
"buffer_load_dword v115,v8,s[48:51],0 offen\n"
"v_mfma_f32_16x16x32_bf16 a[176:179],v[240:243],v[212:215],a[176:179]\n"
"v_mfma_f32_16x16x32_bf16 a[180:183],v[244:247],v[212:215],a[180:183]\n"
"v_mfma_f32_16x16x32_bf16 a[180:183],v[180:183],v[148:151],a[180:183]\n"
"v_mfma_f32_16x16x32_bf16 a[184:187],v[184:187],v[148:151],a[184:187]\n"
"v_mfma_f32_16x16x32_bf16 a[184:187],v[248:251],v[212:215],a[184:187]\n"
"v_mfma_f32_16x16x32_bf16 a[188:191],v[252:255],v[212:215],a[188:191]\n"
"v_mfma_f32_16x16x32_bf16 a[188:191],v[188:191],v[148:151],a[188:191]\n"
"s_waitcnt vmcnt(26)\n"
"s_barrier\n"
"v_mfma_f32_16x16x32_bf16 a[192:195],v[224:227],v[216:219],a[192:195]\n"
"ds_read_b128 v[152:155],v80 offset:45056\n"
"v_mfma_f32_16x16x32_bf16 a[224:227],v[224:227],v[220:223],a[224:227]\n"
"ds_read_b128 v[156:159],v80 offset:47104\n"
"v_mfma_f32_16x16x32_bf16 a[228:231],v[228:231],v[220:223],a[228:231]\n"
"ds_read_b64_tr_b16 v[160:161],v98 offset:0\n"
"v_mfma_f32_16x16x32_bf16 a[196:199],v[228:231],v[216:219],a[196:199]\n"
"ds_read_b64_tr_b16 v[162:163],v98 offset:1024\n"
"v_mfma_f32_16x16x32_bf16 a[200:203],v[232:235],v[216:219],a[200:203]\n"
"ds_read_b64_tr_b16 v[164:165],v99 offset:0\n"
"v_mfma_f32_16x16x32_bf16 a[232:235],v[232:235],v[220:223],a[232:235]\n"
"ds_read_b64_tr_b16 v[166:167],v99 offset:1024\n"
"v_mfma_f32_16x16x32_bf16 a[236:239],v[236:239],v[220:223],a[236:239]\n"
"ds_read_b64_tr_b16 v[168:169],v100 offset:0\n"
"v_mfma_f32_16x16x32_bf16 a[204:207],v[236:239],v[216:219],a[204:207]\n"
"ds_read_b64_tr_b16 v[170:171],v100 offset:1024\n"
"v_mfma_f32_16x16x32_bf16 a[208:211],v[240:243],v[216:219],a[208:211]\n"
"ds_read_b64_tr_b16 v[172:173],v101 offset:0\n"
"v_mfma_f32_16x16x32_bf16 a[240:243],v[240:243],v[220:223],a[240:243]\n"
"ds_read_b64_tr_b16 v[174:175],v101 offset:1024\n"
"v_mfma_f32_16x16x32_bf16 a[244:247],v[244:247],v[220:223],a[244:247]\n"
"ds_read_b64_tr_b16 v[176:177],v102 offset:0\n"
"v_mfma_f32_16x16x32_bf16 a[212:215],v[244:247],v[216:219],a[212:215]\n"
"ds_read_b64_tr_b16 v[178:179],v102 offset:1024\n"
"v_mfma_f32_16x16x32_bf16 a[216:219],v[248:251],v[216:219],a[216:219]\n"
"ds_read_b64_tr_b16 v[180:181],v103 offset:0\n"
"v_mfma_f32_16x16x32_bf16 a[248:251],v[248:251],v[220:223],a[248:251]\n"
"ds_read_b64_tr_b16 v[182:183],v103 offset:1024\n"
"v_mfma_f32_16x16x32_bf16 a[252:255],v[252:255],v[220:223],a[252:255]\n"
"ds_read_b64_tr_b16 v[184:185],v104 offset:0\n"
"v_mfma_f32_16x16x32_bf16 a[220:223],v[252:255],v[216:219],a[220:223]\n"
"ds_read_b64_tr_b16 v[186:187],v104 offset:1024\n"
"s_waitcnt lgkmcnt(10)\n"
"v_mfma_f32_16x16x32_bf16 a[192:195],v[160:163],v[152:155],a[192:195]\n"
"ds_read_b64_tr_b16 v[188:189],v105 offset:0\n"
"v_mfma_f32_16x16x32_bf16 a[224:227],v[160:163],v[156:159],a[224:227]\n"
"ds_read_b64_tr_b16 v[190:191],v105 offset:1024\n"
"v_mfma_f32_16x16x32_bf16 a[228:231],v[164:167],v[156:159],a[228:231]\n"
"ds_read_b128 v[128:131],v80 offset:32768\n"
"v_mfma_f32_16x16x32_bf16 a[196:199],v[164:167],v[152:155],a[196:199]\n"
"ds_read_b128 v[192:195],v81 offset:32768\n"
"s_waitcnt lgkmcnt(10)\n"
"v_mfma_f32_16x16x32_bf16 a[200:203],v[168:171],v[152:155],a[200:203]\n"
"ds_read_b64_tr_b16 v[224:225],v98 offset:16384\n"
"v_mfma_f32_16x16x32_bf16 a[232:235],v[168:171],v[156:159],a[232:235]\n"
"ds_read_b64_tr_b16 v[226:227],v98 offset:17408\n"
"v_mfma_f32_16x16x32_bf16 a[236:239],v[172:175],v[156:159],a[236:239]\n"
"ds_read_b64_tr_b16 v[228:229],v99 offset:16384\n"
"v_mfma_f32_16x16x32_bf16 a[204:207],v[172:175],v[152:155],a[204:207]\n"
"ds_read_b64_tr_b16 v[230:231],v99 offset:17408\n"
"s_waitcnt lgkmcnt(10)\n"
"v_mfma_f32_16x16x32_bf16 a[208:211],v[176:179],v[152:155],a[208:211]\n"
"ds_read_b64_tr_b16 v[232:233],v100 offset:16384\n"
"v_mfma_f32_16x16x32_bf16 a[240:243],v[176:179],v[156:159],a[240:243]\n"
"ds_read_b64_tr_b16 v[234:235],v100 offset:17408\n"
"v_mfma_f32_16x16x32_bf16 a[244:247],v[180:183],v[156:159],a[244:247]\n"
"ds_read_b64_tr_b16 v[236:237],v101 offset:16384\n"
"v_mfma_f32_16x16x32_bf16 a[212:215],v[180:183],v[152:155],a[212:215]\n"
"ds_read_b64_tr_b16 v[238:239],v101 offset:17408\n"
"s_waitcnt lgkmcnt(10)\n"
"v_mfma_f32_16x16x32_bf16 a[216:219],v[184:187],v[152:155],a[216:219]\n"
"ds_read_b64_tr_b16 v[240:241],v102 offset:16384\n"
"v_mfma_f32_16x16x32_bf16 a[248:251],v[184:187],v[156:159],a[248:251]\n"
"ds_read_b64_tr_b16 v[242:243],v102 offset:17408\n"
"v_mfma_f32_16x16x32_bf16 a[252:255],v[188:191],v[156:159],a[252:255]\n"
"ds_read_b64_tr_b16 v[244:245],v103 offset:16384\n"
"v_mfma_f32_16x16x32_bf16 a[220:223],v[188:191],v[152:155],a[220:223]\n"
"ds_read_b64_tr_b16 v[246:247],v103 offset:17408\n"
"s_waitcnt lgkmcnt(8)\n"
"v_mfma_f32_16x16x32_bf16 a[0:3],v[160:163],v[128:131],a[0:3]\n"
"ds_read_b64_tr_b16 v[248:249],v104 offset:16384\n"
"v_mfma_f32_16x16x32_bf16 a[0:3],v[224:227],v[192:195],a[0:3]\n"
"ds_read_b64_tr_b16 v[250:251],v104 offset:17408\n"
"v_mfma_f32_16x16x32_bf16 a[4:7],v[228:231],v[192:195],a[4:7]\n"
"ds_read_b64_tr_b16 v[252:253],v105 offset:16384\n"
"v_mfma_f32_16x16x32_bf16 a[4:7],v[164:167],v[128:131],a[4:7]\n"
"ds_read_b64_tr_b16 v[254:255],v105 offset:17408\n"
"s_waitcnt lgkmcnt(8)\n"
"v_mfma_f32_16x16x32_bf16 a[8:11],v[168:171],v[128:131],a[8:11]\n"
"ds_read_b128 v[132:135],v80 offset:34816\n"
"v_mfma_f32_16x16x32_bf16 a[8:11],v[232:235],v[192:195],a[8:11]\n"
"ds_read_b128 v[196:199],v81 offset:34816\n"
"v_mfma_f32_16x16x32_bf16 a[12:15],v[236:239],v[192:195],a[12:15]\n"
"ds_read_b128 v[136:139],v80 offset:36864\n"
"v_mfma_f32_16x16x32_bf16 a[12:15],v[172:175],v[128:131],a[12:15]\n"
"ds_read_b128 v[200:203],v81 offset:36864\n"
"s_waitcnt lgkmcnt(8)\n"
"v_mfma_f32_16x16x32_bf16 a[16:19],v[176:179],v[128:131],a[16:19]\n"
"s_and_b32 s82,s52,1\n"
"ds_read_b128 v[140:143],v80 offset:38912\n"
"s_lshl_b32 s82,s82,13\n"
"v_mfma_f32_16x16x32_bf16 a[16:19],v[240:243],v[192:195],a[16:19]\n"
"ds_read_b128 v[204:207],v81 offset:38912\n"
"v_mfma_f32_16x16x32_bf16 a[20:23],v[244:247],v[192:195],a[20:23]\n"
"ds_read_b128 v[144:147],v80 offset:40960\n"
"v_mfma_f32_16x16x32_bf16 a[20:23],v[180:183],v[128:131],a[20:23]\n"
"ds_read_b128 v[208:211],v81 offset:40960\n"
"s_waitcnt lgkmcnt(8)\n"
"v_mfma_f32_16x16x32_bf16 a[24:27],v[184:187],v[128:131],a[24:27]\n"
"ds_read_b128 v[148:151],v80 offset:43008\n"
"v_mfma_f32_16x16x32_bf16 a[24:27],v[248:251],v[192:195],a[24:27]\n"
"ds_read_b128 v[212:215],v81 offset:43008\n"
"v_subrev_u32 v98,s82,v98\n"
"v_mfma_f32_16x16x32_bf16 a[28:31],v[252:255],v[192:195],a[28:31]\n"
"ds_read_b128 v[216:219],v81 offset:45056\n"
"v_add_u32 v96,s78,v97\n"
"v_mfma_f32_16x16x32_bf16 a[28:31],v[188:191],v[128:131],a[28:31]\n"
"ds_read_b128 v[220:223],v81 offset:47104\n"
"s_waitcnt lgkmcnt(10)\n"
"v_mfma_f32_16x16x32_bf16 a[32:35],v[160:163],v[132:135],a[32:35]\n"
"v_add_u32 v97,65536,v96\n"
"v_mfma_f32_16x16x32_bf16 a[32:35],v[224:227],v[196:199],a[32:35]\n"
"v_subrev_u32 v99,s82,v99\n"
"v_mfma_f32_16x16x32_bf16 a[36:39],v[228:231],v[196:199],a[36:39]\n"
"v_mfma_f32_16x16x32_bf16 a[36:39],v[164:167],v[132:135],a[36:39]\n"
"v_mfma_f32_16x16x32_bf16 a[40:43],v[168:171],v[132:135],a[40:43]\n"
"s_waitcnt lgkmcnt(0)\n"
"s_barrier\n"
"v_mfma_f32_16x16x32_bf16 a[40:43],v[232:235],v[196:199],a[40:43]\n"
"v_subrev_u32 v100,s82,v100\n"
"s_add_u32 m0,s85,98304\n"
"v_mfma_f32_16x16x32_bf16 a[44:47],v[236:239],v[196:199],a[44:47]\n"
"buffer_load_dwordx4 v110,s[28:31],s86 offen lds\n"
"v_mfma_f32_16x16x32_bf16 a[44:47],v[172:175],v[132:135],a[44:47]\n"
"v_mfma_f32_16x16x32_bf16 a[48:51],v[176:179],v[132:135],a[48:51]\n"
"v_mfma_f32_16x16x32_bf16 a[48:51],v[240:243],v[196:199],a[48:51]\n"
"v_subrev_u32 v101,s82,v101\n"
"s_add_u32 m0,s85,99328\n"
"v_mfma_f32_16x16x32_bf16 a[52:55],v[244:247],v[196:199],a[52:55]\n"
"buffer_load_dwordx4 v111,s[28:31],s86 offen lds\n"
"v_mfma_f32_16x16x32_bf16 a[52:55],v[180:183],v[132:135],a[52:55]\n"
"s_xor_b32 s86,s86,262144\n"
"v_mfma_f32_16x16x32_bf16 a[56:59],v[184:187],v[132:135],a[56:59]\n"
"v_mfma_f32_16x16x32_bf16 a[56:59],v[248:251],v[196:199],a[56:59]\n"
"v_subrev_u32 v102,s82,v102\n"
"s_add_u32 m0,s78,32768\n"
"v_mfma_f32_16x16x32_bf16 a[60:63],v[252:255],v[196:199],a[60:63]\n"
"buffer_load_dwordx4 v112,s[24:27],s76 offen offset:0 sc1 lds\n"
"v_mfma_f32_16x16x32_bf16 a[60:63],v[188:191],v[132:135],a[60:63]\n"
"v_mfma_f32_16x16x32_bf16 a[64:67],v[160:163],v[136:139],a[64:67]\n"
"v_mfma_f32_16x16x32_bf16 a[64:67],v[224:227],v[200:203],a[64:67]\n"
"v_subrev_u32 v103,s82,v103\n"
"v_mfma_f32_16x16x32_bf16 a[68:71],v[228:231],v[200:203],a[68:71]\n"
"buffer_load_dwordx4 v112,s[24:27],s76 offen offset:1024 sc1 lds\n"
"v_mfma_f32_16x16x32_bf16 a[68:71],v[164:167],v[136:139],a[68:71]\n"
"v_mfma_f32_16x16x32_bf16 a[72:75],v[168:171],v[136:139],a[72:75]\n"
"v_accvgpr_read_b32 v116,a0\n"
"v_accvgpr_read_b32 v117,a1\n"
"v_mfma_f32_16x16x32_bf16 a[72:75],v[232:235],v[200:203],a[72:75]\n"
"v_subrev_u32 v104,s82,v104\n"
"v_accvgpr_read_b32 v118,a2\n"
"v_mfma_f32_16x16x32_bf16 a[76:79],v[236:239],v[200:203],a[76:79]\n"
"buffer_load_dwordx4 v112,s[24:27],s76 offen offset:2048 sc1 lds\n"
"v_mfma_f32_16x16x32_bf16 a[76:79],v[172:175],v[136:139],a[76:79]\n"
"v_accvgpr_read_b32 v119,a3\n"
"v_accvgpr_read_b32 v120,a4\n"
"v_mfma_f32_16x16x32_bf16 a[80:83],v[176:179],v[136:139],a[80:83]\n"
"v_accvgpr_read_b32 v121,a5\n"
"v_accvgpr_read_b32 v122,a6\n"
"v_mfma_f32_16x16x32_bf16 a[80:83],v[240:243],v[200:203],a[80:83]\n"
"v_subrev_u32 v105,s82,v105\n"
"v_accvgpr_read_b32 v123,a7\n"
"v_mfma_f32_16x16x32_bf16 a[84:87],v[244:247],v[200:203],a[84:87]\n"
"buffer_load_dwordx4 v112,s[24:27],s76 offen offset:3072 sc1 lds\n"
"v_mfma_f32_16x16x32_bf16 a[84:87],v[180:183],v[136:139],a[84:87]\n"
"s_xor_b32 s76,s76,16384\n"
"v_cvt_pk_bf16_f32 v116,v116,v117\n"
"v_cvt_pk_bf16_f32 v117,v118,v119\n"
"v_mfma_f32_16x16x32_bf16 a[88:91],v[184:187],v[136:139],a[88:91]\n"
"v_cvt_pk_bf16_f32 v118,v120,v121\n"
"v_cvt_pk_bf16_f32 v119,v122,v123\n"
"v_mfma_f32_16x16x32_bf16 a[88:91],v[248:251],v[200:203],a[88:91]\n"
"buffer_store_dwordx4 v[116:119],v112,s[24:27],s56 offen offset:0 sc1\n"
"v_mfma_f32_16x16x32_bf16 a[92:95],v[252:255],v[200:203],a[92:95]\n"
"s_nop 0\n"
"v_accvgpr_read_b32 v116,a8\n"
"v_accvgpr_read_b32 v117,a9\n"
"v_accvgpr_read_b32 v118,a10\n"
"v_mfma_f32_16x16x32_bf16 a[92:95],v[188:191],v[136:139],a[92:95]\n"
"s_waitcnt vmcnt(20)\n"
"v_accvgpr_read_b32 v119,a11\n"
"v_accvgpr_read_b32 v120,a12\n"
"s_barrier\n"
"v_mfma_f32_16x16x32_bf16 a[96:99],v[160:163],v[140:143],a[96:99]\n"
"ds_read_b128 v[0:3],v97 offset:0\n"
"v_accvgpr_read_b32 v121,a13\n"
"v_mfma_f32_16x16x32_bf16 a[96:99],v[224:227],v[204:207],a[96:99]\n"
"ds_read_b128 v[8:11],v97 offset:1024\n"
"v_accvgpr_read_b32 v122,a14\n"
"v_mfma_f32_16x16x32_bf16 a[100:103],v[228:231],v[204:207],a[100:103]\n"
"ds_read_b128 v[16:19],v97 offset:2048\n"
"v_accvgpr_read_b32 v123,a15\n"
"v_mfma_f32_16x16x32_bf16 a[100:103],v[164:167],v[140:143],a[100:103]\n"
"ds_read_b128 v[24:27],v97 offset:3072\n"
"v_cvt_pk_bf16_f32 v116,v116,v117\n"
"v_mfma_f32_16x16x32_bf16 a[104:107],v[168:171],v[140:143],a[104:107]\n"
"ds_read_b128 v[4:7],v97 offset:16384\n"
"v_cvt_pk_bf16_f32 v117,v118,v119\n"
"v_mfma_f32_16x16x32_bf16 a[104:107],v[232:235],v[204:207],a[104:107]\n"
"ds_read_b128 v[12:15],v97 offset:17408\n"
"v_cvt_pk_bf16_f32 v118,v120,v121\n"
"v_mfma_f32_16x16x32_bf16 a[108:111],v[236:239],v[204:207],a[108:111]\n"
"ds_read_b128 v[20:23],v97 offset:18432\n"
"v_cvt_pk_bf16_f32 v119,v122,v123\n"
"v_mfma_f32_16x16x32_bf16 a[108:111],v[172:175],v[140:143],a[108:111]\n"
"ds_read_b128 v[28:31],v97 offset:19456\n"
"buffer_store_dwordx4 v[116:119],v112,s[24:27],s56 offen offset:1024 sc1\n"
"v_mfma_f32_16x16x32_bf16 a[112:115],v[176:179],v[140:143],a[112:115]\n"
"ds_read_b64_tr_b16 v[32:33],v98 offset:49152\n"
"v_accvgpr_read_b32 v116,a16\n"
"v_mfma_f32_16x16x32_bf16 a[112:115],v[240:243],v[204:207],a[112:115]\n"
"ds_read_b64_tr_b16 v[34:35],v98 offset:50176\n"
"v_accvgpr_read_b32 v117,a17\n"
"v_mfma_f32_16x16x32_bf16 a[116:119],v[244:247],v[204:207],a[116:119]\n"
"ds_read_b64_tr_b16 v[36:37],v98 offset:57344\n"
"v_accvgpr_read_b32 v118,a18\n"
"v_mfma_f32_16x16x32_bf16 a[116:119],v[180:183],v[140:143],a[116:119]\n"
"ds_read_b64_tr_b16 v[38:39],v98 offset:58368\n"
"v_accvgpr_read_b32 v119,a19\n"
"v_mfma_f32_16x16x32_bf16 a[120:123],v[184:187],v[140:143],a[120:123]\n"
"ds_read_b64_tr_b16 v[40:41],v99 offset:49152\n"
"v_accvgpr_read_b32 v120,a20\n"
"v_mfma_f32_16x16x32_bf16 a[120:123],v[248:251],v[204:207],a[120:123]\n"
"ds_read_b64_tr_b16 v[42:43],v99 offset:50176\n"
"v_accvgpr_read_b32 v121,a21\n"
"v_mfma_f32_16x16x32_bf16 a[124:127],v[252:255],v[204:207],a[124:127]\n"
"ds_read_b64_tr_b16 v[44:45],v99 offset:57344\n"
"v_accvgpr_read_b32 v122,a22\n"
"v_mfma_f32_16x16x32_bf16 a[124:127],v[188:191],v[140:143],a[124:127]\n"
"ds_read_b64_tr_b16 v[46:47],v99 offset:58368\n"
"v_accvgpr_read_b32 v123,a23\n"
"v_mfma_f32_16x16x32_bf16 a[128:131],v[160:163],v[144:147],a[128:131]\n"
"ds_read_b64_tr_b16 v[48:49],v100 offset:49152\n"
"v_cvt_pk_bf16_f32 v116,v116,v117\n"
"v_mfma_f32_16x16x32_bf16 a[128:131],v[224:227],v[208:211],a[128:131]\n"
"ds_read_b64_tr_b16 v[50:51],v100 offset:50176\n"
"v_cvt_pk_bf16_f32 v117,v118,v119\n"
"v_mfma_f32_16x16x32_bf16 a[132:135],v[228:231],v[208:211],a[132:135]\n"
"ds_read_b64_tr_b16 v[52:53],v100 offset:57344\n"
"v_cvt_pk_bf16_f32 v118,v120,v121\n"
"v_mfma_f32_16x16x32_bf16 a[132:135],v[164:167],v[144:147],a[132:135]\n"
"ds_read_b64_tr_b16 v[54:55],v100 offset:58368\n"
"v_cvt_pk_bf16_f32 v119,v122,v123\n"
"v_mfma_f32_16x16x32_bf16 a[136:139],v[168:171],v[144:147],a[136:139]\n"
"ds_read_b64_tr_b16 v[56:57],v101 offset:49152\n"
"buffer_store_dwordx4 v[116:119],v112,s[24:27],s56 offen offset:2048 sc1\n"
"v_mfma_f32_16x16x32_bf16 a[136:139],v[232:235],v[208:211],a[136:139]\n"
"ds_read_b64_tr_b16 v[58:59],v101 offset:50176\n"
"v_accvgpr_read_b32 v116,a24\n"
"v_mfma_f32_16x16x32_bf16 a[140:143],v[236:239],v[208:211],a[140:143]\n"
"ds_read_b64_tr_b16 v[60:61],v101 offset:57344\n"
"v_accvgpr_read_b32 v117,a25\n"
"v_mfma_f32_16x16x32_bf16 a[140:143],v[172:175],v[144:147],a[140:143]\n"
"ds_read_b64_tr_b16 v[62:63],v101 offset:58368\n"
"v_accvgpr_read_b32 v118,a26\n"
"v_mfma_f32_16x16x32_bf16 a[144:147],v[176:179],v[144:147],a[144:147]\n"
"ds_read_b64_tr_b16 v[64:65],v102 offset:49152\n"
"v_accvgpr_read_b32 v119,a27\n"
"v_mfma_f32_16x16x32_bf16 a[144:147],v[240:243],v[208:211],a[144:147]\n"
"ds_read_b64_tr_b16 v[66:67],v102 offset:50176\n"
"v_accvgpr_read_b32 v120,a28\n"
"v_mfma_f32_16x16x32_bf16 a[148:151],v[244:247],v[208:211],a[148:151]\n"
"ds_read_b64_tr_b16 v[68:69],v102 offset:57344\n"
"v_accvgpr_read_b32 v121,a29\n"
"v_mfma_f32_16x16x32_bf16 a[148:151],v[180:183],v[144:147],a[148:151]\n"
"ds_read_b64_tr_b16 v[70:71],v102 offset:58368\n"
"v_accvgpr_read_b32 v122,a30\n"
"v_mfma_f32_16x16x32_bf16 a[152:155],v[184:187],v[144:147],a[152:155]\n"
"ds_read_b64_tr_b16 v[72:73],v103 offset:49152\n"
"v_accvgpr_read_b32 v123,a31\n"
"v_mfma_f32_16x16x32_bf16 a[152:155],v[248:251],v[208:211],a[152:155]\n"
"ds_read_b64_tr_b16 v[74:75],v103 offset:50176\n"
"v_cvt_pk_bf16_f32 v116,v116,v117\n"
"v_mfma_f32_16x16x32_bf16 a[156:159],v[252:255],v[208:211],a[156:159]\n"
"ds_read_b64_tr_b16 v[76:77],v103 offset:57344\n"
"v_cvt_pk_bf16_f32 v117,v118,v119\n"
"v_mfma_f32_16x16x32_bf16 a[156:159],v[188:191],v[144:147],a[156:159]\n"
"ds_read_b64_tr_b16 v[78:79],v103 offset:58368\n"
"v_cvt_pk_bf16_f32 v118,v120,v121\n"
"v_mfma_f32_16x16x32_bf16 a[160:163],v[160:163],v[148:151],a[160:163]\n"
"ds_read_b64_tr_b16 v[80:81],v104 offset:49152\n"
"v_cvt_pk_bf16_f32 v119,v122,v123\n"
"v_mfma_f32_16x16x32_bf16 a[160:163],v[224:227],v[212:215],a[160:163]\n"
"ds_read_b64_tr_b16 v[82:83],v104 offset:50176\n"
"buffer_store_dwordx4 v[116:119],v112,s[24:27],s56 offen offset:3072 sc1\n"
"v_mfma_f32_16x16x32_bf16 a[164:167],v[228:231],v[212:215],a[164:167]\n"
"ds_read_b64_tr_b16 v[84:85],v104 offset:57344\n"
"v_mfma_f32_16x16x32_bf16 a[164:167],v[164:167],v[148:151],a[164:167]\n"
"ds_read_b64_tr_b16 v[86:87],v104 offset:58368\n"
"v_mfma_f32_16x16x32_bf16 a[168:171],v[168:171],v[148:151],a[168:171]\n"
"ds_read_b64_tr_b16 v[88:89],v105 offset:49152\n"
"v_mfma_f32_16x16x32_bf16 a[168:171],v[232:235],v[212:215],a[168:171]\n"
"ds_read_b64_tr_b16 v[90:91],v105 offset:50176\n"
"v_mfma_f32_16x16x32_bf16 a[172:175],v[236:239],v[212:215],a[172:175]\n"
"ds_read_b64_tr_b16 v[92:93],v105 offset:57344\n"
"v_mfma_f32_16x16x32_bf16 a[172:175],v[172:175],v[148:151],a[172:175]\n"
"ds_read_b64_tr_b16 v[94:95],v105 offset:58368\n"
"v_mfma_f32_16x16x32_bf16 a[176:179],v[176:179],v[148:151],a[176:179]\n"
"s_add_u32 m0,s85,106496\n"
"v_mfma_f32_16x16x32_bf16 a[176:179],v[240:243],v[212:215],a[176:179]\n"
"buffer_load_dwordx4 v110,s[28:31],s86 offen lds\n"
"v_mfma_f32_16x16x32_bf16 a[180:183],v[244:247],v[212:215],a[180:183]\n"
"v_mfma_f32_16x16x32_bf16 a[180:183],v[180:183],v[148:151],a[180:183]\n"
"s_add_u32 m0,s85,107520\n"
"v_mfma_f32_16x16x32_bf16 a[184:187],v[184:187],v[148:151],a[184:187]\n"
"buffer_load_dwordx4 v111,s[28:31],s86 offen lds\n"
"v_mfma_f32_16x16x32_bf16 a[184:187],v[248:251],v[212:215],a[184:187]\n"
"s_xor_b32 s86,s86,786432\n"
"v_mfma_f32_16x16x32_bf16 a[188:191],v[252:255],v[212:215],a[188:191]\n"
"s_add_u32 m0,s78,49152\n"
"v_mfma_f32_16x16x32_bf16 a[188:191],v[188:191],v[148:151],a[188:191]\n"
"buffer_load_dwordx4 v112,s[24:27],s76 offen offset:0 sc1 lds\n"
"v_mfma_f32_16x16x32_bf16 a[192:195],v[224:227],v[216:219],a[192:195]\n"
"v_mfma_f32_16x16x32_bf16 a[224:227],v[224:227],v[220:223],a[224:227]\n"
"v_mfma_f32_16x16x32_bf16 a[228:231],v[228:231],v[220:223],a[228:231]\n"
"buffer_load_dwordx4 v112,s[24:27],s76 offen offset:1024 sc1 lds\n"
"v_mfma_f32_16x16x32_bf16 a[196:199],v[228:231],v[216:219],a[196:199]\n"
"v_mfma_f32_16x16x32_bf16 a[200:203],v[232:235],v[216:219],a[200:203]\n"
"v_mfma_f32_16x16x32_bf16 a[232:235],v[232:235],v[220:223],a[232:235]\n"
"buffer_load_dwordx4 v112,s[24:27],s76 offen offset:2048 sc1 lds\n"
"v_mfma_f32_16x16x32_bf16 a[236:239],v[236:239],v[220:223],a[236:239]\n"
"v_mfma_f32_16x16x32_bf16 a[204:207],v[236:239],v[216:219],a[204:207]\n"
"buffer_load_dwordx4 v112,s[24:27],s76 offen offset:3072 sc1 lds\n"
"v_mfma_f32_16x16x32_bf16 a[208:211],v[240:243],v[216:219],a[208:211]\n"
"s_xor_b32 s76,s76,49152\n"
"v_mfma_f32_16x16x32_bf16 a[240:243],v[240:243],v[220:223],a[240:243]\n"
"v_mfma_f32_16x16x32_bf16 a[244:247],v[244:247],v[220:223],a[244:247]\n"
"v_mfma_f32_16x16x32_bf16 a[212:215],v[244:247],v[216:219],a[212:215]\n"
"v_mfma_f32_16x16x32_bf16 a[216:219],v[248:251],v[216:219],a[216:219]\n"
"v_mfma_f32_16x16x32_bf16 a[248:251],v[248:251],v[220:223],a[248:251]\n"
"v_mfma_f32_16x16x32_bf16 a[252:255],v[252:255],v[220:223],a[252:255]\n"
"v_mfma_f32_16x16x32_bf16 a[220:223],v[252:255],v[216:219],a[220:223]\n"
"s_branch .Lfk_ph2\n"
".Lfk_v:\n"
"s_waitcnt vmcnt(23)\n"
"s_barrier\n"
"ds_read_b128 v[152:155],v80 offset:12288\n"
"ds_read_b128 v[156:159],v80 offset:14336\n"
"ds_read_b64_tr_b16 v[160:161],v98 offset:49152\n"
"ds_read_b64_tr_b16 v[162:163],v98 offset:50176\n"
"ds_read_b64_tr_b16 v[164:165],v99 offset:49152\n"
"ds_read_b64_tr_b16 v[166:167],v99 offset:50176\n"
"ds_read_b64_tr_b16 v[168:169],v100 offset:49152\n"
"ds_read_b64_tr_b16 v[170:171],v100 offset:50176\n"
"ds_read_b64_tr_b16 v[172:173],v101 offset:49152\n"
"ds_read_b64_tr_b16 v[174:175],v101 offset:50176\n"
"ds_read_b64_tr_b16 v[176:177],v102 offset:49152\n"
"ds_read_b64_tr_b16 v[178:179],v102 offset:50176\n"
"ds_read_b64_tr_b16 v[180:181],v103 offset:49152\n"
"ds_read_b64_tr_b16 v[182:183],v103 offset:50176\n"
"ds_read_b64_tr_b16 v[184:185],v104 offset:49152\n"
"ds_read_b64_tr_b16 v[186:187],v104 offset:50176\n"
"ds_read_b64_tr_b16 v[188:189],v105 offset:49152\n"
"ds_read_b64_tr_b16 v[190:191],v105 offset:50176\n"
"s_waitcnt lgkmcnt(12)\n"
"v_mfma_f32_16x16x32_bf16 a[192:195],v[152:155],v[160:163],0\n"
"s_mov_b32 s77,524288\n"
"s_add_u32 m0,s78,98304\n"
"v_mfma_f32_16x16x32_bf16 a[196:199],v[156:159],v[160:163],0\n"
"buffer_load_dwordx4 v110,s[20:23],s77 offen offset:0 lds\n"
"v_mfma_f32_16x16x32_bf16 a[204:207],v[156:159],v[164:167],0\n"
"buffer_load_dwordx4 v111,s[20:23],s77 offen offset:1024 lds\n"
"v_mfma_f32_16x16x32_bf16 a[200:203],v[152:155],v[164:167],0\n"
"s_waitcnt lgkmcnt(8)\n"
"v_mfma_f32_16x16x32_bf16 a[208:211],v[152:155],v[168:171],0\n"
"v_mfma_f32_16x16x32_bf16 a[212:215],v[156:159],v[168:171],0\n"
"buffer_load_dwordx4 v113,s[20:23],s77 offen offset:2048 lds\n"
"v_mfma_f32_16x16x32_bf16 a[220:223],v[156:159],v[172:175],0\n"
"buffer_load_dwordx4 v114,s[20:23],s77 offen offset:3072 lds\n"
"v_mfma_f32_16x16x32_bf16 a[216:219],v[152:155],v[172:175],0\n"
"s_waitcnt vmcnt(21)\n"
"s_barrier\n"
"s_waitcnt lgkmcnt(4)\n"
"v_mfma_f32_16x16x32_bf16 a[224:227],v[152:155],v[176:179],0\n"
"ds_read_b128 v[128:131],v80 offset:0\n"
"ds_read_b128 v[192:195],v81 offset:0\n"
"v_mfma_f32_16x16x32_bf16 a[228:231],v[156:159],v[176:179],0\n"
"ds_read_b64_tr_b16 v[224:225],v98 offset:32768\n"
"ds_read_b64_tr_b16 v[226:227],v98 offset:33792\n"
"v_mfma_f32_16x16x32_bf16 a[236:239],v[156:159],v[180:183],0\n"
"ds_read_b64_tr_b16 v[228:229],v99 offset:32768\n"
"ds_read_b64_tr_b16 v[230:231],v99 offset:33792\n"
"v_mfma_f32_16x16x32_bf16 a[232:235],v[152:155],v[180:183],0\n"
"ds_read_b64_tr_b16 v[232:233],v100 offset:32768\n"
"ds_read_b64_tr_b16 v[234:235],v100 offset:33792\n"
"s_waitcnt lgkmcnt(8)\n"
"v_mfma_f32_16x16x32_bf16 a[240:243],v[152:155],v[184:187],0\n"
"ds_read_b64_tr_b16 v[236:237],v101 offset:32768\n"
"ds_read_b64_tr_b16 v[238:239],v101 offset:33792\n"
"v_mfma_f32_16x16x32_bf16 a[244:247],v[156:159],v[184:187],0\n"
"ds_read_b64_tr_b16 v[240:241],v102 offset:32768\n"
"ds_read_b64_tr_b16 v[242:243],v102 offset:33792\n"
"v_mfma_f32_16x16x32_bf16 a[252:255],v[156:159],v[188:191],0\n"
"ds_read_b64_tr_b16 v[244:245],v103 offset:32768\n"
"ds_read_b64_tr_b16 v[246:247],v103 offset:33792\n"
"v_mfma_f32_16x16x32_bf16 a[248:251],v[152:155],v[188:191],0\n"
"ds_read_b64_tr_b16 v[248:249],v104 offset:32768\n"
"ds_read_b64_tr_b16 v[250:251],v104 offset:33792\n"
"s_waitcnt lgkmcnt(10)\n"
"v_mfma_f32_16x16x32_bf16 a[0:3],v[128:131],v[160:163],0\n"
"ds_read_b64_tr_b16 v[252:253],v105 offset:32768\n"
"ds_read_b64_tr_b16 v[254:255],v105 offset:33792\n"
"v_mfma_f32_16x16x32_bf16 a[0:3],v[192:195],v[224:227],a[0:3]\n"
"ds_read_b128 v[132:135],v80 offset:2048\n"
"ds_read_b128 v[196:199],v81 offset:2048\n"
"v_mfma_f32_16x16x32_bf16 a[8:11],v[192:195],v[228:231],0\n"
"ds_read_b128 v[216:219],v81 offset:12288\n"
"ds_read_b128 v[220:223],v81 offset:14336\n"
"v_mfma_f32_16x16x32_bf16 a[8:11],v[128:131],v[164:167],a[8:11]\n"
"s_waitcnt lgkmcnt(12)\n"
"s_mov_b32 s77,786432\n"
"s_add_u32 m0,s78,114688\n"
"v_mfma_f32_16x16x32_bf16 a[16:19],v[128:131],v[168:171],0\n"
"buffer_load_dwordx4 v110,s[20:23],s77 offen offset:0 lds\n"
"v_mfma_f32_16x16x32_bf16 a[16:19],v[192:195],v[232:235],a[16:19]\n"
"v_mfma_f32_16x16x32_bf16 a[24:27],v[192:195],v[236:239],0\n"
"buffer_load_dwordx4 v111,s[20:23],s77 offen offset:1024 lds\n"
"v_mfma_f32_16x16x32_bf16 a[24:27],v[128:131],v[172:175],a[24:27]\n"
"s_waitcnt lgkmcnt(8)\n"
"v_mfma_f32_16x16x32_bf16 a[32:35],v[128:131],v[176:179],0\n"
"buffer_load_dwordx4 v113,s[20:23],s77 offen offset:2048 lds\n"
"v_mfma_f32_16x16x32_bf16 a[32:35],v[192:195],v[240:243],a[32:35]\n"
"v_mfma_f32_16x16x32_bf16 a[40:43],v[192:195],v[244:247],0\n"
"buffer_load_dwordx4 v114,s[20:23],s77 offen offset:3072 lds\n"
"v_mfma_f32_16x16x32_bf16 a[40:43],v[128:131],v[180:183],a[40:43]\n"
"s_waitcnt lgkmcnt(4)\n"
"s_mov_b32 s76,256\n"
"s_add_u32 m0,s79,65536\n"
"v_mfma_f32_16x16x32_bf16 a[48:51],v[128:131],v[184:187],0\n"
"buffer_load_dwordx4 v106,s[16:19],s76 offen offset:0 lds\n"
"v_mfma_f32_16x16x32_bf16 a[48:51],v[192:195],v[248:251],a[48:51]\n"
"v_mfma_f32_16x16x32_bf16 a[56:59],v[192:195],v[252:255],0\n"
"buffer_load_dwordx4 v107,s[16:19],s76 offen offset:1024 lds\n"
"v_mfma_f32_16x16x32_bf16 a[56:59],v[128:131],v[188:191],a[56:59]\n"
"s_waitcnt lgkmcnt(2)\n"
"v_mfma_f32_16x16x32_bf16 a[4:7],v[132:135],v[160:163],0\n"
"buffer_load_dwordx4 v108,s[16:19],s76 offen offset:2048 lds\n"
"v_mfma_f32_16x16x32_bf16 a[4:7],v[196:199],v[224:227],a[4:7]\n"
"v_mfma_f32_16x16x32_bf16 a[12:15],v[196:199],v[228:231],0\n"
"buffer_load_dwordx4 v109,s[16:19],s76 offen offset:3072 lds\n"
"v_mfma_f32_16x16x32_bf16 a[12:15],v[132:135],v[164:167],a[12:15]\n"
"s_waitcnt vmcnt(25)\n"
"s_barrier\n"
"v_mfma_f32_16x16x32_bf16 a[20:23],v[132:135],v[168:171],0\n"
"ds_read_b128 v[136:139],v80 offset:4096\n"
"ds_read_b128 v[200:203],v81 offset:4096\n"
"v_mfma_f32_16x16x32_bf16 a[20:23],v[196:199],v[232:235],a[20:23]\n"
"ds_read_b128 v[140:143],v80 offset:6144\n"
"ds_read_b128 v[204:207],v81 offset:6144\n"
"v_mfma_f32_16x16x32_bf16 a[28:31],v[196:199],v[236:239],0\n"
"ds_read_b128 v[144:147],v80 offset:8192\n"
"ds_read_b128 v[208:211],v81 offset:8192\n"
"v_mfma_f32_16x16x32_bf16 a[28:31],v[132:135],v[172:175],a[28:31]\n"
"ds_read_b128 v[148:151],v80 offset:10240\n"
"ds_read_b128 v[212:215],v81 offset:10240\n"
"v_mfma_f32_16x16x32_bf16 a[36:39],v[132:135],v[176:179],0\n"
"s_mov_b32 s76,262400\n"
"s_add_u32 m0,s79,69632\n"
"v_mfma_f32_16x16x32_bf16 a[36:39],v[196:199],v[240:243],a[36:39]\n"
"buffer_load_dwordx4 v106,s[16:19],s76 offen offset:0 lds\n"
"v_mfma_f32_16x16x32_bf16 a[44:47],v[196:199],v[244:247],0\n"
"buffer_load_dwordx4 v107,s[16:19],s76 offen offset:1024 lds\n"
"v_mfma_f32_16x16x32_bf16 a[44:47],v[132:135],v[180:183],a[44:47]\n"
"buffer_load_dwordx4 v108,s[16:19],s76 offen offset:2048 lds\n"
"v_mfma_f32_16x16x32_bf16 a[52:55],v[132:135],v[184:187],0\n"
"buffer_load_dwordx4 v109,s[16:19],s76 offen offset:3072 lds\n"
"v_mfma_f32_16x16x32_bf16 a[52:55],v[196:199],v[248:251],a[52:55]\n"
"s_waitcnt lgkmcnt(0)\n"
"s_barrier\n"
"v_mfma_f32_16x16x32_bf16 a[60:63],v[196:199],v[252:255],0\n"
"s_mov_b32 s77,1048576\n"
"s_add_u32 m0,s78,147456\n"
"v_mfma_f32_16x16x32_bf16 a[60:63],v[132:135],v[188:191],a[60:63]\n"
"buffer_load_dwordx4 v110,s[20:23],s77 offen offset:0 lds\n"
"v_mfma_f32_16x16x32_bf16 a[64:67],v[136:139],v[160:163],0\n"
"v_mfma_f32_16x16x32_bf16 a[64:67],v[200:203],v[224:227],a[64:67]\n"
"v_mfma_f32_16x16x32_bf16 a[72:75],v[200:203],v[228:231],0\n"
"v_mfma_f32_16x16x32_bf16 a[72:75],v[136:139],v[164:167],a[72:75]\n"
"buffer_load_dwordx4 v111,s[20:23],s77 offen offset:1024 lds\n"
"v_mfma_f32_16x16x32_bf16 a[80:83],v[136:139],v[168:171],0\n"
"v_mfma_f32_16x16x32_bf16 a[80:83],v[200:203],v[232:235],a[80:83]\n"
"v_mfma_f32_16x16x32_bf16 a[88:91],v[200:203],v[236:239],0\n"
"v_mfma_f32_16x16x32_bf16 a[88:91],v[136:139],v[172:175],a[88:91]\n"
"buffer_load_dwordx4 v113,s[20:23],s77 offen offset:2048 lds\n"
"v_mfma_f32_16x16x32_bf16 a[96:99],v[136:139],v[176:179],0\n"
"v_mfma_f32_16x16x32_bf16 a[96:99],v[200:203],v[240:243],a[96:99]\n"
"v_mfma_f32_16x16x32_bf16 a[104:107],v[200:203],v[244:247],0\n"
"v_mfma_f32_16x16x32_bf16 a[104:107],v[136:139],v[180:183],a[104:107]\n"
"buffer_load_dwordx4 v114,s[20:23],s77 offen offset:3072 lds\n"
"v_mfma_f32_16x16x32_bf16 a[112:115],v[136:139],v[184:187],0\n"
"v_mfma_f32_16x16x32_bf16 a[112:115],v[200:203],v[248:251],a[112:115]\n"
"v_mfma_f32_16x16x32_bf16 a[120:123],v[200:203],v[252:255],0\n"
"s_mov_b32 s77,1310720\n"
"s_add_u32 m0,s78,131072\n"
"v_mfma_f32_16x16x32_bf16 a[120:123],v[136:139],v[188:191],a[120:123]\n"
"buffer_load_dwordx4 v110,s[20:23],s77 offen offset:0 lds\n"
"s_waitcnt vmcnt(21)\n"
"v_mfma_f32_16x16x32_bf16 a[68:71],v[140:143],v[160:163],0\n"
"v_lshlrev_b32 v24,16,v8\n"
"v_and_b32 v25,0xffff0000,v8\n"
"v_mfma_f32_16x16x32_bf16 a[68:71],v[204:207],v[224:227],a[68:71]\n"
"v_lshlrev_b32 v26,16,v9\n"
"v_and_b32 v27,0xffff0000,v9\n"
"v_mfma_f32_16x16x32_bf16 a[76:79],v[204:207],v[228:231],0\n"
"buffer_load_dwordx4 v111,s[20:23],s77 offen offset:1024 lds\n"
"v_mfma_f32_16x16x32_bf16 a[76:79],v[140:143],v[164:167],a[76:79]\n"
"v_lshlrev_b32 v28,16,v10\n"
"v_and_b32 v29,0xffff0000,v10\n"
"v_mfma_f32_16x16x32_bf16 a[84:87],v[140:143],v[168:171],0\n"
"v_lshlrev_b32 v30,16,v11\n"
"v_and_b32 v31,0xffff0000,v11\n"
"v_mfma_f32_16x16x32_bf16 a[84:87],v[204:207],v[232:235],a[84:87]\n"
"v_lshlrev_b32 v32,16,v12\n"
"v_and_b32 v33,0xffff0000,v12\n"
"v_mfma_f32_16x16x32_bf16 a[92:95],v[204:207],v[236:239],0\n"
"buffer_load_dwordx4 v113,s[20:23],s77 offen offset:2048 lds\n"
"v_mfma_f32_16x16x32_bf16 a[92:95],v[140:143],v[172:175],a[92:95]\n"
"v_lshlrev_b32 v34,16,v13\n"
"v_and_b32 v35,0xffff0000,v13\n"
"v_mfma_f32_16x16x32_bf16 a[100:103],v[140:143],v[176:179],0\n"
"v_lshlrev_b32 v36,16,v14\n"
"v_and_b32 v37,0xffff0000,v14\n"
"v_mfma_f32_16x16x32_bf16 a[100:103],v[204:207],v[240:243],a[100:103]\n"
"v_lshlrev_b32 v38,16,v15\n"
"v_and_b32 v39,0xffff0000,v15\n"
"v_mfma_f32_16x16x32_bf16 a[108:111],v[204:207],v[244:247],0\n"
"buffer_load_dwordx4 v114,s[20:23],s77 offen offset:3072 lds\n"
"v_mfma_f32_16x16x32_bf16 a[108:111],v[140:143],v[180:183],a[108:111]\n"
"v_lshlrev_b32 v40,16,v16\n"
"v_and_b32 v41,0xffff0000,v16\n"
"v_mfma_f32_16x16x32_bf16 a[116:119],v[140:143],v[184:187],0\n"
"v_lshlrev_b32 v42,16,v17\n"
"v_and_b32 v43,0xffff0000,v17\n"
"v_mfma_f32_16x16x32_bf16 a[116:119],v[204:207],v[248:251],a[116:119]\n"
"s_mov_b32 s76,384\n"
"v_lshlrev_b32 v44,16,v18\n"
"s_add_u32 m0,s79,0\n"
"v_mfma_f32_16x16x32_bf16 a[124:127],v[204:207],v[252:255],0\n"
"buffer_load_dwordx4 v106,s[16:19],s76 offen offset:0 lds\n"
"v_mfma_f32_16x16x32_bf16 a[124:127],v[140:143],v[188:191],a[124:127]\n"
"v_and_b32 v45,0xffff0000,v18\n"
"v_lshlrev_b32 v46,16,v19\n"
"v_mfma_f32_16x16x32_bf16 a[128:131],v[144:147],v[160:163],0\n"
"v_and_b32 v47,0xffff0000,v19\n"
"v_lshlrev_b32 v48,16,v20\n"
"v_mfma_f32_16x16x32_bf16 a[128:131],v[208:211],v[224:227],a[128:131]\n"
"v_and_b32 v49,0xffff0000,v20\n"
"v_lshlrev_b32 v50,16,v21\n"
"v_mfma_f32_16x16x32_bf16 a[136:139],v[208:211],v[228:231],0\n"
"buffer_load_dwordx4 v107,s[16:19],s76 offen offset:1024 lds\n"
"v_mfma_f32_16x16x32_bf16 a[136:139],v[144:147],v[164:167],a[136:139]\n"
"v_and_b32 v51,0xffff0000,v21\n"
"v_lshlrev_b32 v52,16,v22\n"
"v_mfma_f32_16x16x32_bf16 a[144:147],v[144:147],v[168:171],0\n"
"v_and_b32 v53,0xffff0000,v22\n"
"v_lshlrev_b32 v54,16,v23\n"
"v_mfma_f32_16x16x32_bf16 a[144:147],v[208:211],v[232:235],a[144:147]\n"
"v_and_b32 v55,0xffff0000,v23\n"
"v_add_f32 v56,v24,v25\n"
"v_mfma_f32_16x16x32_bf16 a[152:155],v[208:211],v[236:239],0\n"
"buffer_load_dwordx4 v108,s[16:19],s76 offen offset:2048 lds\n"
"v_mfma_f32_16x16x32_bf16 a[152:155],v[144:147],v[172:175],a[152:155]\n"
"v_add_f32 v57,v26,v27\n"
"v_add_f32 v56,v56,v28\n"
"v_mfma_f32_16x16x32_bf16 a[160:163],v[144:147],v[176:179],0\n"
"v_add_f32 v57,v57,v29\n"
"v_add_f32 v56,v56,v30\n"
"v_mfma_f32_16x16x32_bf16 a[160:163],v[208:211],v[240:243],a[160:163]\n"
"v_readfirstlane_b32 s82,v78\n"
"v_readfirstlane_b32 s83,v79\n"
"v_mfma_f32_16x16x32_bf16 a[168:171],v[208:211],v[244:247],0\n"
"buffer_load_dwordx4 v109,s[16:19],s76 offen offset:3072 lds\n"
"v_mfma_f32_16x16x32_bf16 a[168:171],v[144:147],v[180:183],a[168:171]\n"
"s_mul_i32 s80,s82,0x9e3779b1\n"
"s_add_u32 s80,s80,0x7f4a7c15\n"
"v_mov_b32 v124,s80\n"
"v_mfma_f32_16x16x32_bf16 a[176:179],v[144:147],v[184:187],0\n"
"s_add_u32 s81,s83,s80\n"
"v_add_f32 v57,v57,v31\n"
"v_add_f32 v56,v56,v32\n"
"v_mfma_f32_16x16x32_bf16 a[176:179],v[208:211],v[248:251],a[176:179]\n"
"s_add_u32 s81,s81,1\n"
"s_mov_b32 s76,262528\n"
"s_add_u32 m0,s79,4096\n"
"v_mfma_f32_16x16x32_bf16 a[184:187],v[208:211],v[252:255],0\n"
"buffer_load_dwordx4 v106,s[16:19],s76 offen offset:0 lds\n"
"v_mov_b32 v125,s81\n"
"v_mfma_f32_16x16x32_bf16 a[184:187],v[144:147],v[188:191],a[184:187]\n"
"v_add_f32 v57,v57,v33\n"
"v_add_f32 v56,v56,v34\n"
"v_mfma_f32_16x16x32_bf16 a[132:135],v[148:151],v[160:163],0\n"
"v_add_f32 v57,v57,v35\n"
"v_add_f32 v56,v56,v36\n"
"v_mfma_f32_16x16x32_bf16 a[132:135],v[212:215],v[224:227],a[132:135]\n"
"v_add_f32 v57,v57,v37\n"
"v_add_f32 v56,v56,v38\n"
"v_mfma_f32_16x16x32_bf16 a[140:143],v[212:215],v[228:231],0\n"
"buffer_load_dwordx4 v107,s[16:19],s76 offen offset:1024 lds\n"
"v_mfma_f32_16x16x32_bf16 a[140:143],v[148:151],v[164:167],a[140:143]\n"
"v_add_f32 v57,v57,v39\n"
"v_add_f32 v56,v56,v40\n"
"v_mfma_f32_16x16x32_bf16 a[148:151],v[148:151],v[168:171],0\n"
"v_add_f32 v57,v57,v41\n"
"v_add_f32 v56,v56,v42\n"
"v_mfma_f32_16x16x32_bf16 a[148:151],v[212:215],v[232:235],a[148:151]\n"
"v_add_f32 v57,v57,v43\n"
"v_add_f32 v56,v56,v44\n"
"v_mfma_f32_16x16x32_bf16 a[156:159],v[212:215],v[236:239],0\n"
"buffer_load_dwordx4 v108,s[16:19],s76 offen offset:2048 lds\n"
"v_mfma_f32_16x16x32_bf16 a[156:159],v[148:151],v[172:175],a[156:159]\n"
"v_add_f32 v57,v57,v45\n"
"v_add_f32 v56,v56,v46\n"
"v_mfma_f32_16x16x32_bf16 a[164:167],v[148:151],v[176:179],0\n"
"v_add_f32 v57,v57,v47\n"
"v_add_f32 v56,v56,v48\n"
"v_mfma_f32_16x16x32_bf16 a[164:167],v[212:215],v[240:243],a[164:167]\n"
"v_add_f32 v57,v57,v49\n"
"v_add_f32 v56,v56,v50\n"
"v_mfma_f32_16x16x32_bf16 a[172:175],v[212:215],v[244:247],0\n"
"buffer_load_dwordx4 v109,s[16:19],s76 offen offset:3072 lds\n"
"v_mfma_f32_16x16x32_bf16 a[172:175],v[148:151],v[180:183],a[172:175]\n"
"s_mov_b32 s87,524288\n"
"v_add_f32 v57,v57,v51\n"
"v_add_f32 v56,v56,v52\n"
"v_mfma_f32_16x16x32_bf16 a[180:183],v[148:151],v[184:187],0\n"
"buffer_load_dword v115,v112,s[36:39],s87 offen\n"
"v_mfma_f32_16x16x32_bf16 a[180:183],v[212:215],v[248:251],a[180:183]\n"
"s_mov_b32 s87,1048576\n"
"v_add_f32 v57,v57,v53\n"
"v_add_f32 v56,v56,v54\n"
"v_mfma_f32_16x16x32_bf16 a[188:191],v[212:215],v[252:255],0\n"
"buffer_load_dword v115,v112,s[36:39],s87 offen\n"
"v_mfma_f32_16x16x32_bf16 a[188:191],v[148:151],v[188:191],a[188:191]\n"
"s_mov_b32 s87,1572864\n"
"s_waitcnt vmcnt(26)\n"
"s_barrier\n"
"v_mfma_f32_16x16x32_bf16 a[192:195],v[216:219],v[224:227],a[192:195]\n"
"ds_read_b128 v[152:155],v80 offset:45056\n"
"buffer_load_dword v115,v112,s[36:39],s87 offen\n"
"v_mfma_f32_16x16x32_bf16 a[196:199],v[220:223],v[224:227],a[196:199]\n"
"ds_read_b128 v[156:159],v80 offset:47104\n"
"v_add_f32 v57,v57,v55\n"
"v_mfma_f32_16x16x32_bf16 a[204:207],v[220:223],v[228:231],a[204:207]\n"
"ds_read_b64_tr_b16 v[160:161],v98 offset:0\n"
"v_add_f32 v56,v56,v57\n"
"v_mfma_f32_16x16x32_bf16 a[200:203],v[216:219],v[228:231],a[200:203]\n"
"ds_read_b64_tr_b16 v[162:163],v98 offset:1024\n"
"v_mfma_f32_16x16x32_bf16 a[208:211],v[216:219],v[232:235],a[208:211]\n"
"ds_read_b64_tr_b16 v[164:165],v99 offset:0\n"
"v_add_f32_dpp v56,v56,v56 quad_perm:[1,0,3,2] row_mask:0xf bank_mask:0xf\n"
"v_mfma_f32_16x16x32_bf16 a[212:215],v[220:223],v[232:235],a[212:215]\n"
"ds_read_b64_tr_b16 v[166:167],v99 offset:1024\n"
"v_mfma_f32_16x16x32_bf16 a[220:223],v[220:223],v[236:239],a[220:223]\n"
"ds_read_b64_tr_b16 v[168:169],v100 offset:0\n"
"v_add_f32_dpp v56,v56,v56 quad_perm:[2,3,0,1] row_mask:0xf bank_mask:0xf\n"
"v_mfma_f32_16x16x32_bf16 a[216:219],v[216:219],v[236:239],a[216:219]\n"
"ds_read_b64_tr_b16 v[170:171],v100 offset:1024\n"
"v_mfma_f32_16x16x32_bf16 a[224:227],v[216:219],v[240:243],a[224:227]\n"
"ds_read_b64_tr_b16 v[172:173],v101 offset:0\n"
"v_add_f32_dpp v56,v56,v56 row_ror:8 row_mask:0xf bank_mask:0xf\n"
"v_mfma_f32_16x16x32_bf16 a[228:231],v[220:223],v[240:243],a[228:231]\n"
"ds_read_b64_tr_b16 v[174:175],v101 offset:1024\n"
"v_mov_b32 v57,v56\n"
"v_mfma_f32_16x16x32_bf16 a[236:239],v[220:223],v[244:247],a[236:239]\n"
"ds_read_b64_tr_b16 v[176:177],v102 offset:0\n"
"v_mfma_f32_16x16x32_bf16 a[232:235],v[216:219],v[244:247],a[232:235]\n"
"ds_read_b64_tr_b16 v[178:179],v102 offset:1024\n"
"v_permlane16_swap_b32 v57,v56\n"
"v_mfma_f32_16x16x32_bf16 a[240:243],v[216:219],v[248:251],a[240:243]\n"
"ds_read_b64_tr_b16 v[180:181],v103 offset:0\n"
"v_add_f32 v56,v56,v57\n"
"v_mfma_f32_16x16x32_bf16 a[244:247],v[220:223],v[248:251],a[244:247]\n"
"ds_read_b64_tr_b16 v[182:183],v103 offset:1024\n"
"v_mov_b32 v57,v56\n"
"v_mfma_f32_16x16x32_bf16 a[252:255],v[220:223],v[252:255],a[252:255]\n"
"ds_read_b64_tr_b16 v[184:185],v104 offset:0\n"
"ds_read_b64_tr_b16 v[186:187],v104 offset:1024\n"
"v_mfma_f32_16x16x32_bf16 a[248:251],v[216:219],v[252:255],a[248:251]\n"
"v_permlane32_swap_b32 v57,v56\n"
"s_waitcnt lgkmcnt(10)\n"
"v_mfma_f32_16x16x32_bf16 a[192:195],v[152:155],v[160:163],a[192:195]\n"
"ds_read_b64_tr_b16 v[188:189],v105 offset:0\n"
"v_add_f32 v56,v56,v57\n"
"v_mfma_f32_16x16x32_bf16 a[196:199],v[156:159],v[160:163],a[196:199]\n"
"ds_read_b64_tr_b16 v[190:191],v105 offset:1024\n"
"v_mul_f32 v58,0x3a800000,v56\n"
"v_mfma_f32_16x16x32_bf16 a[204:207],v[156:159],v[164:167],a[204:207]\n"
"ds_read_b128 v[128:131],v80 offset:32768\n"
"v_sub_f32 v62,v24,v58\n"
"v_mfma_f32_16x16x32_bf16 a[200:203],v[152:155],v[164:167],a[200:203]\n"
"ds_read_b128 v[192:195],v81 offset:32768\n"
"s_waitcnt lgkmcnt(10)\n"
"v_mfma_f32_16x16x32_bf16 a[208:211],v[152:155],v[168:171],a[208:211]\n"
"ds_read_b64_tr_b16 v[224:225],v98 offset:16384\n"
"v_mul_f32 v59,v62,v62\n"
"v_mfma_f32_16x16x32_bf16 a[212:215],v[156:159],v[168:171],a[212:215]\n"
"ds_read_b64_tr_b16 v[226:227],v98 offset:17408\n"
"v_sub_f32 v63,v25,v58\n"
"v_mfma_f32_16x16x32_bf16 a[220:223],v[156:159],v[172:175],a[220:223]\n"
"ds_read_b64_tr_b16 v[228:229],v99 offset:16384\n"
"v_mul_f32 v60,v63,v63\n"
"v_mfma_f32_16x16x32_bf16 a[216:219],v[152:155],v[172:175],a[216:219]\n"
"ds_read_b64_tr_b16 v[230:231],v99 offset:17408\n"
"s_waitcnt lgkmcnt(10)\n"
"v_mfma_f32_16x16x32_bf16 a[224:227],v[152:155],v[176:179],a[224:227]\n"
"ds_read_b64_tr_b16 v[232:233],v100 offset:16384\n"
"v_sub_f32 v64,v26,v58\n"
"v_mfma_f32_16x16x32_bf16 a[228:231],v[156:159],v[176:179],a[228:231]\n"
"ds_read_b64_tr_b16 v[234:235],v100 offset:17408\n"
"v_fmac_f32 v59,v64,v64\n"
"v_mfma_f32_16x16x32_bf16 a[236:239],v[156:159],v[180:183],a[236:239]\n"
"ds_read_b64_tr_b16 v[236:237],v101 offset:16384\n"
"v_sub_f32 v65,v27,v58\n"
"v_mfma_f32_16x16x32_bf16 a[232:235],v[152:155],v[180:183],a[232:235]\n"
"ds_read_b64_tr_b16 v[238:239],v101 offset:17408\n"
"s_waitcnt lgkmcnt(10)\n"
"v_mfma_f32_16x16x32_bf16 a[240:243],v[152:155],v[184:187],a[240:243]\n"
"ds_read_b64_tr_b16 v[240:241],v102 offset:16384\n"
"v_fmac_f32 v60,v65,v65\n"
"v_mfma_f32_16x16x32_bf16 a[244:247],v[156:159],v[184:187],a[244:247]\n"
"ds_read_b64_tr_b16 v[242:243],v102 offset:17408\n"
"v_sub_f32 v62,v28,v58\n"
"v_mfma_f32_16x16x32_bf16 a[252:255],v[156:159],v[188:191],a[252:255]\n"
"ds_read_b64_tr_b16 v[244:245],v103 offset:16384\n"
"v_fmac_f32 v59,v62,v62\n"
"v_mfma_f32_16x16x32_bf16 a[248:251],v[152:155],v[188:191],a[248:251]\n"
"ds_read_b64_tr_b16 v[246:247],v103 offset:17408\n"
"s_waitcnt lgkmcnt(8)\n"
"v_mfma_f32_16x16x32_bf16 a[0:3],v[128:131],v[160:163],a[0:3]\n"
"ds_read_b64_tr_b16 v[248:249],v104 offset:16384\n"
"v_sub_f32 v63,v29,v58\n"
"v_mfma_f32_16x16x32_bf16 a[0:3],v[192:195],v[224:227],a[0:3]\n"
"ds_read_b64_tr_b16 v[250:251],v104 offset:17408\n"
"v_fmac_f32 v60,v63,v63\n"
"v_mfma_f32_16x16x32_bf16 a[8:11],v[192:195],v[228:231],a[8:11]\n"
"ds_read_b64_tr_b16 v[252:253],v105 offset:16384\n"
"v_sub_f32 v64,v30,v58\n"
"v_mfma_f32_16x16x32_bf16 a[8:11],v[128:131],v[164:167],a[8:11]\n"
"ds_read_b64_tr_b16 v[254:255],v105 offset:17408\n"
"s_waitcnt lgkmcnt(8)\n"
"v_mfma_f32_16x16x32_bf16 a[16:19],v[128:131],v[168:171],a[16:19]\n"
"ds_read_b128 v[132:135],v80 offset:34816\n"
"v_fmac_f32 v59,v64,v64\n"
"v_mfma_f32_16x16x32_bf16 a[16:19],v[192:195],v[232:235],a[16:19]\n"
"ds_read_b128 v[196:199],v81 offset:34816\n"
"v_sub_f32 v65,v31,v58\n"
"v_mfma_f32_16x16x32_bf16 a[24:27],v[192:195],v[236:239],a[24:27]\n"
"ds_read_b128 v[136:139],v80 offset:36864\n"
"v_fmac_f32 v60,v65,v65\n"
"v_mfma_f32_16x16x32_bf16 a[24:27],v[128:131],v[172:175],a[24:27]\n"
"ds_read_b128 v[200:203],v81 offset:36864\n"
"s_waitcnt lgkmcnt(8)\n"
"v_mfma_f32_16x16x32_bf16 a[32:35],v[128:131],v[176:179],a[32:35]\n"
"ds_read_b128 v[140:143],v80 offset:38912\n"
"v_sub_f32 v62,v32,v58\n"
"v_mfma_f32_16x16x32_bf16 a[32:35],v[192:195],v[240:243],a[32:35]\n"
"ds_read_b128 v[204:207],v81 offset:38912\n"
"v_fmac_f32 v59,v62,v62\n"
"v_mfma_f32_16x16x32_bf16 a[40:43],v[192:195],v[244:247],a[40:43]\n"
"ds_read_b128 v[144:147],v80 offset:40960\n"
"v_sub_f32 v63,v33,v58\n"
"v_mfma_f32_16x16x32_bf16 a[40:43],v[128:131],v[180:183],a[40:43]\n"
"ds_read_b128 v[208:211],v81 offset:40960\n"
"s_waitcnt lgkmcnt(8)\n"
"v_mfma_f32_16x16x32_bf16 a[48:51],v[128:131],v[184:187],a[48:51]\n"
"ds_read_b128 v[148:151],v80 offset:43008\n"
"v_fmac_f32 v60,v63,v63\n"
"v_mfma_f32_16x16x32_bf16 a[48:51],v[192:195],v[248:251],a[48:51]\n"
"ds_read_b128 v[212:215],v81 offset:43008\n"
"v_sub_f32 v64,v34,v58\n"
"v_mfma_f32_16x16x32_bf16 a[56:59],v[192:195],v[252:255],a[56:59]\n"
"ds_read_b128 v[216:219],v81 offset:45056\n"
"v_fmac_f32 v59,v64,v64\n"
"v_mfma_f32_16x16x32_bf16 a[56:59],v[128:131],v[188:191],a[56:59]\n"
"ds_read_b128 v[220:223],v81 offset:47104\n"
"s_waitcnt lgkmcnt(10)\n"
"v_mfma_f32_16x16x32_bf16 a[4:7],v[132:135],v[160:163],a[4:7]\n"
"v_sub_f32 v65,v35,v58\n"
"v_fmac_f32 v60,v65,v65\n"
"v_mfma_f32_16x16x32_bf16 a[4:7],v[196:199],v[224:227],a[4:7]\n"
"v_sub_f32 v62,v36,v58\n"
"v_fmac_f32 v59,v62,v62\n"
"v_mfma_f32_16x16x32_bf16 a[12:15],v[196:199],v[228:231],a[12:15]\n"
"v_sub_f32 v63,v37,v58\n"
"v_fmac_f32 v60,v63,v63\n"
"v_mfma_f32_16x16x32_bf16 a[12:15],v[132:135],v[164:167],a[12:15]\n"
"v_sub_f32 v64,v38,v58\n"
"v_fmac_f32 v59,v64,v64\n"
"v_mfma_f32_16x16x32_bf16 a[20:23],v[132:135],v[168:171],a[20:23]\n"
"s_waitcnt lgkmcnt(0)\n"
"s_barrier\n"
"v_mfma_f32_16x16x32_bf16 a[20:23],v[196:199],v[232:235],a[20:23]\n"
"s_mov_b32 s77,1572864\n"
"v_sub_f32 v65,v39,v58\n"
"s_add_u32 m0,s78,98304\n"
"v_mfma_f32_16x16x32_bf16 a[28:31],v[196:199],v[236:239],a[28:31]\n"
"buffer_load_dwordx4 v110,s[20:23],s77 offen offset:0 lds\n"
"v_mfma_f32_16x16x32_bf16 a[28:31],v[132:135],v[172:175],a[28:31]\n"
"v_fmac_f32 v60,v65,v65\n"
"v_sub_f32 v62,v40,v58\n"
"v_mfma_f32_16x16x32_bf16 a[36:39],v[132:135],v[176:179],a[36:39]\n"
"v_fmac_f32 v59,v62,v62\n"
"v_sub_f32 v63,v41,v58\n"
"v_mfma_f32_16x16x32_bf16 a[36:39],v[196:199],v[240:243],a[36:39]\n"
"v_fmac_f32 v60,v63,v63\n"
"v_sub_f32 v64,v42,v58\n"
"v_mfma_f32_16x16x32_bf16 a[44:47],v[196:199],v[244:247],a[44:47]\n"
"buffer_load_dwordx4 v111,s[20:23],s77 offen offset:1024 lds\n"
"v_mfma_f32_16x16x32_bf16 a[44:47],v[132:135],v[180:183],a[44:47]\n"
"v_fmac_f32 v59,v64,v64\n"
"v_sub_f32 v65,v43,v58\n"
"v_mfma_f32_16x16x32_bf16 a[52:55],v[132:135],v[184:187],a[52:55]\n"
"v_fmac_f32 v60,v65,v65\n"
"v_sub_f32 v62,v44,v58\n"
"v_mfma_f32_16x16x32_bf16 a[52:55],v[196:199],v[248:251],a[52:55]\n"
"v_fmac_f32 v59,v62,v62\n"
"v_sub_f32 v63,v45,v58\n"
"v_mfma_f32_16x16x32_bf16 a[60:63],v[196:199],v[252:255],a[60:63]\n"
"buffer_load_dwordx4 v113,s[20:23],s77 offen offset:2048 lds\n"
"v_mfma_f32_16x16x32_bf16 a[60:63],v[132:135],v[188:191],a[60:63]\n"
"v_fmac_f32 v60,v63,v63\n"
"v_sub_f32 v64,v46,v58\n"
"v_mfma_f32_16x16x32_bf16 a[64:67],v[136:139],v[160:163],a[64:67]\n"
"v_fmac_f32 v59,v64,v64\n"
"v_sub_f32 v65,v47,v58\n"
"v_mfma_f32_16x16x32_bf16 a[64:67],v[200:203],v[224:227],a[64:67]\n"
"v_fmac_f32 v60,v65,v65\n"
"v_sub_f32 v62,v48,v58\n"
"v_mfma_f32_16x16x32_bf16 a[72:75],v[200:203],v[228:231],a[72:75]\n"
"buffer_load_dwordx4 v114,s[20:23],s77 offen offset:3072 lds\n"
"v_mfma_f32_16x16x32_bf16 a[72:75],v[136:139],v[164:167],a[72:75]\n"
"v_fmac_f32 v59,v62,v62\n"
"v_sub_f32 v63,v49,v58\n"
"v_mfma_f32_16x16x32_bf16 a[80:83],v[136:139],v[168:171],a[80:83]\n"
"v_fmac_f32 v60,v63,v63\n"
"v_sub_f32 v64,v50,v58\n"
"v_mfma_f32_16x16x32_bf16 a[80:83],v[200:203],v[232:235],a[80:83]\n"
"s_mov_b32 s77,1835008\n"
"v_fmac_f32 v59,v64,v64\n"
"s_add_u32 m0,s78,114688\n"
"v_mfma_f32_16x16x32_bf16 a[88:91],v[200:203],v[236:239],a[88:91]\n"
"buffer_load_dwordx4 v110,s[20:23],s77 offen offset:0 lds\n"
"v_mfma_f32_16x16x32_bf16 a[88:91],v[136:139],v[172:175],a[88:91]\n"
"v_sub_f32 v65,v51,v58\n"
"v_fmac_f32 v60,v65,v65\n"
"v_mfma_f32_16x16x32_bf16 a[96:99],v[136:139],v[176:179],a[96:99]\n"
"v_sub_f32 v62,v52,v58\n"
"v_fmac_f32 v59,v62,v62\n"
"v_mfma_f32_16x16x32_bf16 a[96:99],v[200:203],v[240:243],a[96:99]\n"
"v_sub_f32 v63,v53,v58\n"
"v_fmac_f32 v60,v63,v63\n"
"v_mfma_f32_16x16x32_bf16 a[104:107],v[200:203],v[244:247],a[104:107]\n"
"buffer_load_dwordx4 v111,s[20:23],s77 offen offset:1024 lds\n"
"v_mfma_f32_16x16x32_bf16 a[104:107],v[136:139],v[180:183],a[104:107]\n"
"v_sub_f32 v64,v54,v58\n"
"v_fmac_f32 v59,v64,v64\n"
"v_mfma_f32_16x16x32_bf16 a[112:115],v[136:139],v[184:187],a[112:115]\n"
"v_sub_f32 v65,v55,v58\n"
"v_fmac_f32 v60,v65,v65\n"
"v_mfma_f32_16x16x32_bf16 a[112:115],v[200:203],v[248:251],a[112:115]\n"
"v_add_f32 v59,v59,v60\n"
"v_mfma_f32_16x16x32_bf16 a[120:123],v[200:203],v[252:255],a[120:123]\n"
"buffer_load_dwordx4 v113,s[20:23],s77 offen offset:2048 lds\n"
"v_mfma_f32_16x16x32_bf16 a[120:123],v[136:139],v[188:191],a[120:123]\n"
"v_add_f32_dpp v59,v59,v59 quad_perm:[1,0,3,2] row_mask:0xf bank_mask:0xf\n"
"v_mfma_f32_16x16x32_bf16 a[68:71],v[140:143],v[160:163],a[68:71]\n"
"s_nop 0\n"
"v_add_f32_dpp v59,v59,v59 quad_perm:[2,3,0,1] row_mask:0xf bank_mask:0xf\n"
"v_mfma_f32_16x16x32_bf16 a[68:71],v[204:207],v[224:227],a[68:71]\n"
"s_nop 0\n"
"v_add_f32_dpp v59,v59,v59 row_ror:8 row_mask:0xf bank_mask:0xf\n"
"v_mov_b32 v57,v59\n"
"v_mfma_f32_16x16x32_bf16 a[76:79],v[204:207],v[228:231],a[76:79]\n"
"buffer_load_dwordx4 v114,s[20:23],s77 offen offset:3072 lds\n"
"v_mfma_f32_16x16x32_bf16 a[76:79],v[140:143],v[164:167],a[76:79]\n"
"v_mfma_f32_16x16x32_bf16 a[84:87],v[140:143],v[168:171],a[84:87]\n"
"v_permlane16_swap_b32 v57,v59\n"
"v_add_f32 v59,v59,v57\n"
"v_mfma_f32_16x16x32_bf16 a[84:87],v[204:207],v[232:235],a[84:87]\n"
"v_mov_b32 v57,v59\n"
"v_mfma_f32_16x16x32_bf16 a[92:95],v[204:207],v[236:239],a[92:95]\n"
"s_mov_b32 s76,512\n"
"v_permlane32_swap_b32 v57,v59\n"
"s_add_u32 m0,s79,32768\n"
"v_mfma_f32_16x16x32_bf16 a[92:95],v[140:143],v[172:175],a[92:95]\n"
"buffer_load_dwordx4 v106,s[16:19],s76 offen offset:0 lds\n"
"v_mfma_f32_16x16x32_bf16 a[100:103],v[140:143],v[176:179],a[100:103]\n"
"v_add_f32 v59,v59,v57\n"
"v_mul_f32 v59,0x3a800000,v59\n"
"v_mfma_f32_16x16x32_bf16 a[100:103],v[204:207],v[240:243],a[100:103]\n"
"v_cvt_pk_bf16_f32 v59,v59,v59\n"
"v_lshlrev_b32 v59,16,v59\n"
"v_mfma_f32_16x16x32_bf16 a[108:111],v[204:207],v[244:247],a[108:111]\n"
"v_add_f32 v59,0x3727c5ac,v59\n"
"v_cvt_pk_bf16_f32 v59,v59,v59\n"
"v_mfma_f32_16x16x32_bf16 a[108:111],v[140:143],v[180:183],a[108:111]\n"
"v_lshlrev_b32 v59,16,v59\n"
"v_sqrt_f32 v59,v59\n"
"v_mfma_f32_16x16x32_bf16 a[116:119],v[140:143],v[184:187],a[116:119]\n"
"buffer_load_dwordx4 v107,s[16:19],s76 offen offset:1024 lds\n"
"v_mfma_f32_16x16x32_bf16 a[116:119],v[204:207],v[248:251],a[116:119]\n"
"v_cvt_pk_bf16_f32 v58,v58,v58\n"
"v_lshlrev_b32 v58,16,v58\n"
"v_mfma_f32_16x16x32_bf16 a[124:127],v[204:207],v[252:255],a[124:127]\n"
"v_cvt_pk_bf16_f32 v59,v59,v59\n"
"v_lshlrev_b32 v59,16,v59\n"
"v_mfma_f32_16x16x32_bf16 a[124:127],v[140:143],v[188:191],a[124:127]\n"
"v_rcp_f32 v66,v59\n"
"v_mfma_f32_16x16x32_bf16 a[128:131],v[144:147],v[160:163],a[128:131]\n"
"v_mul_f32 v66,0x3e0293ee,v66\n"
"v_mul_f32 v58,v58,v66\n"
"v_mfma_f32_16x16x32_bf16 a[128:131],v[208:211],v[224:227],a[128:131]\n"
"buffer_load_dwordx4 v108,s[16:19],s76 offen offset:2048 lds\n"
"v_mfma_f32_16x16x32_bf16 a[136:139],v[208:211],v[228:231],a[136:139]\n"
"v_fma_f32 v24,v24,v66,-v58\n"
"v_fma_f32 v25,v25,v66,-v58\n"
"v_mfma_f32_16x16x32_bf16 a[136:139],v[144:147],v[164:167],a[136:139]\n"
"v_cvt_pk_bf16_f32 v8,v24,v25\n"
"v_fma_f32 v26,v26,v66,-v58\n"
"v_mfma_f32_16x16x32_bf16 a[144:147],v[144:147],v[168:171],a[144:147]\n"
"v_fma_f32 v27,v27,v66,-v58\n"
"v_cvt_pk_bf16_f32 v9,v26,v27\n"
"v_mfma_f32_16x16x32_bf16 a[144:147],v[208:211],v[232:235],a[144:147]\n"
"v_fma_f32 v28,v28,v66,-v58\n"
"v_fma_f32 v29,v29,v66,-v58\n"
"v_mfma_f32_16x16x32_bf16 a[152:155],v[208:211],v[236:239],a[152:155]\n"
"buffer_load_dwordx4 v109,s[16:19],s76 offen offset:3072 lds\n"
"v_mfma_f32_16x16x32_bf16 a[152:155],v[144:147],v[172:175],a[152:155]\n"
"v_cvt_pk_bf16_f32 v10,v28,v29\n"
"v_fma_f32 v30,v30,v66,-v58\n"
"v_mfma_f32_16x16x32_bf16 a[160:163],v[144:147],v[176:179],a[160:163]\n"
"v_fma_f32 v31,v31,v66,-v58\n"
"v_cvt_pk_bf16_f32 v11,v30,v31\n"
"v_mfma_f32_16x16x32_bf16 a[160:163],v[208:211],v[240:243],a[160:163]\n"
"buffer_store_dwordx4 v[8:11],v71,s[24:27],s54 offen sc1\n"
"v_mfma_f32_16x16x32_bf16 a[168:171],v[208:211],v[244:247],a[168:171]\n"
"s_add_u32 s54,s54,131072\n"
"v_fma_f32 v32,v32,v66,-v58\n"
"s_mov_b32 s76,262656\n"
"s_add_u32 m0,s79,36864\n"
"v_mfma_f32_16x16x32_bf16 a[168:171],v[144:147],v[180:183],a[168:171]\n"
"buffer_load_dwordx4 v106,s[16:19],s76 offen offset:0 lds\n"
"v_mfma_f32_16x16x32_bf16 a[176:179],v[144:147],v[184:187],a[176:179]\n"
"v_fma_f32 v33,v33,v66,-v58\n"
"v_cvt_pk_bf16_f32 v12,v32,v33\n"
"v_mfma_f32_16x16x32_bf16 a[176:179],v[208:211],v[248:251],a[176:179]\n"
"v_fma_f32 v34,v34,v66,-v58\n"
"v_fma_f32 v35,v35,v66,-v58\n"
"v_mfma_f32_16x16x32_bf16 a[184:187],v[208:211],v[252:255],a[184:187]\n"
"v_cvt_pk_bf16_f32 v13,v34,v35\n"
"v_fma_f32 v36,v36,v66,-v58\n"
"v_mfma_f32_16x16x32_bf16 a[184:187],v[144:147],v[188:191],a[184:187]\n"
"v_fma_f32 v37,v37,v66,-v58\n"
"v_cvt_pk_bf16_f32 v14,v36,v37\n"
"v_mfma_f32_16x16x32_bf16 a[132:135],v[148:151],v[160:163],a[132:135]\n"
"buffer_load_dwordx4 v107,s[16:19],s76 offen offset:1024 lds\n"
"v_mfma_f32_16x16x32_bf16 a[132:135],v[212:215],v[224:227],a[132:135]\n"
"v_fma_f32 v38,v38,v66,-v58\n"
"v_fma_f32 v39,v39,v66,-v58\n"
"v_cvt_pk_bf16_f32 v15,v38,v39\n"
"v_mfma_f32_16x16x32_bf16 a[140:143],v[212:215],v[228:231],a[140:143]\n"
"buffer_store_dwordx4 v[12:15],v71,s[24:27],s54 offen sc1\n"
"v_mfma_f32_16x16x32_bf16 a[140:143],v[148:151],v[164:167],a[140:143]\n"
"s_add_u32 s54,s54,131072\n"
"v_fma_f32 v40,v40,v66,-v58\n"
"v_mfma_f32_16x16x32_bf16 a[148:151],v[148:151],v[168:171],a[148:151]\n"
"v_fma_f32 v41,v41,v66,-v58\n"
"v_cvt_pk_bf16_f32 v16,v40,v41\n"
"v_mfma_f32_16x16x32_bf16 a[148:151],v[212:215],v[232:235],a[148:151]\n"
"buffer_load_dwordx4 v108,s[16:19],s76 offen offset:2048 lds\n"
"v_mfma_f32_16x16x32_bf16 a[156:159],v[212:215],v[236:239],a[156:159]\n"
"v_fma_f32 v42,v42,v66,-v58\n"
"v_fma_f32 v43,v43,v66,-v58\n"
"v_mfma_f32_16x16x32_bf16 a[156:159],v[148:151],v[172:175],a[156:159]\n"
"v_cvt_pk_bf16_f32 v17,v42,v43\n"
"v_fma_f32 v44,v44,v66,-v58\n"
"v_mfma_f32_16x16x32_bf16 a[164:167],v[148:151],v[176:179],a[164:167]\n"
"v_fma_f32 v45,v45,v66,-v58\n"
"v_cvt_pk_bf16_f32 v18,v44,v45\n"
"v_mfma_f32_16x16x32_bf16 a[164:167],v[212:215],v[240:243],a[164:167]\n"
"v_fma_f32 v46,v46,v66,-v58\n"
"v_fma_f32 v47,v47,v66,-v58\n"
"v_mfma_f32_16x16x32_bf16 a[172:175],v[212:215],v[244:247],a[172:175]\n"
"buffer_load_dwordx4 v109,s[16:19],s76 offen offset:3072 lds\n"
"v_mfma_f32_16x16x32_bf16 a[172:175],v[148:151],v[180:183],a[172:175]\n"
"s_mov_b32 s87,2097152\n"
"v_cvt_pk_bf16_f32 v19,v46,v47\n"
"v_fma_f32 v49,v49,v66,-v58\n"
"v_mfma_f32_16x16x32_bf16 a[180:183],v[148:151],v[184:187],a[180:183]\n"
"buffer_load_dword v115,v112,s[36:39],s87 offen\n"
"buffer_store_dwordx4 v[16:19],v71,s[24:27],s54 offen sc1\n"
"v_mfma_f32_16x16x32_bf16 a[180:183],v[212:215],v[248:251],a[180:183]\n"
"s_mov_b32 s87,2621440\n"
"s_add_u32 s54,s54,131072\n"
"v_fma_f32 v48,v48,v66,-v58\n"
"v_mfma_f32_16x16x32_bf16 a[188:191],v[212:215],v[252:255],a[188:191]\n"
"buffer_load_dword v115,v112,s[36:39],s87 offen\n"
"v_mfma_f32_16x16x32_bf16 a[188:191],v[148:151],v[188:191],a[188:191]\n"
"s_waitcnt vmcnt(32)\n"
"s_barrier\n"
"v_mfma_f32_16x16x32_bf16 a[192:195],v[216:219],v[224:227],a[192:195]\n"
"ds_read_b128 v[152:155],v82 offset:12288\n"
"v_cvt_pk_bf16_f32 v20,v48,v49\n"
"v_mfma_f32_16x16x32_bf16 a[196:199],v[220:223],v[224:227],a[196:199]\n"
"ds_read_b128 v[156:159],v82 offset:14336\n"
"v_fma_f32 v50,v50,v66,-v58\n"
"v_mfma_f32_16x16x32_bf16 a[204:207],v[220:223],v[228:231],a[204:207]\n"
"ds_read_b64_tr_b16 v[160:161],v98 offset:49152\n"
"v_fma_f32 v51,v51,v66,-v58\n"
"v_mfma_f32_16x16x32_bf16 a[200:203],v[216:219],v[228:231],a[200:203]\n"
"ds_read_b64_tr_b16 v[162:163],v98 offset:50176\n"
"v_cvt_pk_bf16_f32 v21,v50,v51\n"
"v_mfma_f32_16x16x32_bf16 a[208:211],v[216:219],v[232:235],a[208:211]\n"
"ds_read_b64_tr_b16 v[164:165],v99 offset:49152\n"
"v_fma_f32 v52,v52,v66,-v58\n"
"v_mfma_f32_16x16x32_bf16 a[212:215],v[220:223],v[232:235],a[212:215]\n"
"ds_read_b64_tr_b16 v[166:167],v99 offset:50176\n"
"v_fma_f32 v53,v53,v66,-v58\n"
"v_mfma_f32_16x16x32_bf16 a[220:223],v[220:223],v[236:239],a[220:223]\n"
"ds_read_b64_tr_b16 v[168:169],v100 offset:49152\n"
"v_cvt_pk_bf16_f32 v22,v52,v53\n"
"v_mfma_f32_16x16x32_bf16 a[216:219],v[216:219],v[236:239],a[216:219]\n"
"ds_read_b64_tr_b16 v[170:171],v100 offset:50176\n"
"v_fma_f32 v54,v54,v66,-v58\n"
"v_mfma_f32_16x16x32_bf16 a[224:227],v[216:219],v[240:243],a[224:227]\n"
"ds_read_b64_tr_b16 v[172:173],v101 offset:49152\n"
"v_fma_f32 v55,v55,v66,-v58\n"
"v_mfma_f32_16x16x32_bf16 a[228:231],v[220:223],v[240:243],a[228:231]\n"
"ds_read_b64_tr_b16 v[174:175],v101 offset:50176\n"
"v_cvt_pk_bf16_f32 v23,v54,v55\n"
"v_mfma_f32_16x16x32_bf16 a[236:239],v[220:223],v[244:247],a[236:239]\n"
"ds_read_b64_tr_b16 v[176:177],v102 offset:49152\n"
"buffer_store_dwordx4 v[20:23],v71,s[24:27],s54 offen sc1\n"
"v_mfma_f32_16x16x32_bf16 a[232:235],v[216:219],v[244:247],a[232:235]\n"
"ds_read_b64_tr_b16 v[178:179],v102 offset:50176\n"
"v_mfma_f32_16x16x32_bf16 a[240:243],v[216:219],v[248:251],a[240:243]\n"
"ds_read_b64_tr_b16 v[180:181],v103 offset:49152\n"
"v_mfma_f32_16x16x32_bf16 a[244:247],v[220:223],v[248:251],a[244:247]\n"
"ds_read_b64_tr_b16 v[182:183],v103 offset:50176\n"
"v_mfma_f32_16x16x32_bf16 a[252:255],v[220:223],v[252:255],a[252:255]\n"
"ds_read_b64_tr_b16 v[184:185],v104 offset:49152\n"
"v_mfma_f32_16x16x32_bf16 a[248:251],v[216:219],v[252:255],a[248:251]\n"
"ds_read_b64_tr_b16 v[186:187],v104 offset:50176\n"
"s_waitcnt lgkmcnt(10)\n"
"v_mfma_f32_16x16x32_bf16 a[192:195],v[152:155],v[160:163],a[192:195]\n"
"ds_read_b64_tr_b16 v[188:189],v105 offset:49152\n"
"s_and_b32 s91,s89,7\n"
"v_mfma_f32_16x16x32_bf16 a[196:199],v[156:159],v[160:163],a[196:199]\n"
"ds_read_b64_tr_b16 v[190:191],v105 offset:50176\n"
"s_lshr_b32 s97,s89,3\n"
"v_mfma_f32_16x16x32_bf16 a[204:207],v[156:159],v[164:167],a[204:207]\n"
"ds_read_b128 v[128:131],v82 offset:0\n"
"s_lshl2_add_u32 s90,s88,s97\n"
"v_mfma_f32_16x16x32_bf16 a[200:203],v[152:155],v[164:167],a[200:203]\n"
"ds_read_b128 v[192:195],v83 offset:0\n"
"s_waitcnt lgkmcnt(10)\n"
"v_mfma_f32_16x16x32_bf16 a[208:211],v[152:155],v[168:171],a[208:211]\n"
"ds_read_b64_tr_b16 v[224:225],v98 offset:32768\n"
"s_lshl_b32 vcc_lo,s90,8\n"
"v_mfma_f32_16x16x32_bf16 a[212:215],v[156:159],v[168:171],a[212:215]\n"
"s_add_u32 s28,s28,vcc_lo\n"
"ds_read_b64_tr_b16 v[226:227],v98 offset:33792\n"
"s_addc_u32 s29,s29,0\n"
"v_mfma_f32_16x16x32_bf16 a[220:223],v[156:159],v[172:175],a[220:223]\n"
"ds_read_b64_tr_b16 v[228:229],v99 offset:32768\n"
"s_sub_u32 s30,8388608,vcc_lo\n"
"v_mfma_f32_16x16x32_bf16 a[216:219],v[152:155],v[172:175],a[216:219]\n"
"ds_read_b64_tr_b16 v[230:231],v99 offset:33792\n"
"s_waitcnt lgkmcnt(10)\n"
"v_mfma_f32_16x16x32_bf16 a[224:227],v[152:155],v[176:179],a[224:227]\n"
"ds_read_b64_tr_b16 v[232:233],v100 offset:32768\n"
"s_mov_b32 s31,0x20000\n"
"v_mfma_f32_16x16x32_bf16 a[228:231],v[156:159],v[176:179],a[228:231]\n"
"ds_read_b64_tr_b16 v[234:235],v100 offset:33792\n"
"s_mov_b32 s60,s91\n"
"v_mfma_f32_16x16x32_bf16 a[236:239],v[156:159],v[180:183],a[236:239]\n"
"ds_read_b64_tr_b16 v[236:237],v101 offset:32768\n"
"s_sub_u32 s94,15,s91\n"
"v_mfma_f32_16x16x32_bf16 a[232:235],v[152:155],v[180:183],a[232:235]\n"
"ds_read_b64_tr_b16 v[238:239],v101 offset:33792\n"
"s_waitcnt lgkmcnt(10)\n"
"v_mfma_f32_16x16x32_bf16 a[240:243],v[152:155],v[184:187],a[240:243]\n"
"s_cmp_lt_u32 s52,2\n"
"ds_read_b64_tr_b16 v[240:241],v102 offset:32768\n"
"s_cselect_b32 s95,s94,s91\n"
"v_mfma_f32_16x16x32_bf16 a[244:247],v[156:159],v[184:187],a[244:247]\n"
"ds_read_b64_tr_b16 v[242:243],v102 offset:33792\n"
"s_lshr_b32 vcc_lo,s95,1\n"
"v_mfma_f32_16x16x32_bf16 a[252:255],v[156:159],v[188:191],a[252:255]\n"
"ds_read_b64_tr_b16 v[244:245],v103 offset:32768\n"
"s_lshl_b32 s55,vcc_lo,19\n"
"v_mfma_f32_16x16x32_bf16 a[248:251],v[152:155],v[188:191],a[248:251]\n"
"ds_read_b64_tr_b16 v[246:247],v103 offset:33792\n"
"s_waitcnt lgkmcnt(8)\n"
"v_mfma_f32_16x16x32_bf16 a[0:3],v[128:131],v[160:163],a[0:3]\n"
"ds_read_b64_tr_b16 v[248:249],v104 offset:32768\n"
"s_lshl_b32 s58,vcc_lo,10\n"
"v_mfma_f32_16x16x32_bf16 a[0:3],v[192:195],v[224:227],a[0:3]\n"
"ds_read_b64_tr_b16 v[250:251],v104 offset:33792\n"
"s_add_u32 s58,s58,148898816\n"
"v_mfma_f32_16x16x32_bf16 a[8:11],v[192:195],v[228:231],a[8:11]\n"
"ds_read_b64_tr_b16 v[252:253],v105 offset:32768\n"
"s_and_b32 vcc_lo,s95,1\n"
"v_mfma_f32_16x16x32_bf16 a[8:11],v[128:131],v[164:167],a[8:11]\n"
"ds_read_b64_tr_b16 v[254:255],v105 offset:33792\n"
"s_waitcnt lgkmcnt(8)\n"
"v_mfma_f32_16x16x32_bf16 a[16:19],v[128:131],v[168:171],a[16:19]\n"
"ds_read_b128 v[132:135],v82 offset:2048\n"
"s_lshl_b32 vcc_lo,vcc_lo,13\n"
"v_mfma_f32_16x16x32_bf16 a[16:19],v[192:195],v[232:235],a[16:19]\n"
"ds_read_b128 v[196:199],v83 offset:2048\n"
"s_and_b32 vcc_hi,s78,4096\n"
"v_mfma_f32_16x16x32_bf16 a[24:27],v[192:195],v[236:239],a[24:27]\n"
"ds_read_b128 v[136:139],v82 offset:4096\n"
"s_add_u32 s55,s55,vcc_lo\n"
"v_mfma_f32_16x16x32_bf16 a[24:27],v[128:131],v[172:175],a[24:27]\n"
"ds_read_b128 v[200:203],v83 offset:4096\n"
"s_waitcnt lgkmcnt(8)\n"
"v_mfma_f32_16x16x32_bf16 a[32:35],v[128:131],v[176:179],a[32:35]\n"
"ds_read_b128 v[140:143],v82 offset:6144\n"
"s_add_u32 s55,s55,vcc_hi\n"
"v_mfma_f32_16x16x32_bf16 a[32:35],v[192:195],v[240:243],a[32:35]\n"
"ds_read_b128 v[204:207],v83 offset:6144\n"
"s_and_b32 vcc_lo,s89,2\n"
"v_mfma_f32_16x16x32_bf16 a[40:43],v[192:195],v[244:247],a[40:43]\n"
"ds_read_b128 v[144:147],v82 offset:8192\n"
"s_lshl2_add_u32 vcc_lo,s88,vcc_lo\n"
"v_mfma_f32_16x16x32_bf16 a[40:43],v[128:131],v[180:183],a[40:43]\n"
"ds_read_b128 v[208:211],v83 offset:8192\n"
"s_waitcnt lgkmcnt(8)\n"
"v_mfma_f32_16x16x32_bf16 a[48:51],v[128:131],v[184:187],a[48:51]\n"
"ds_read_b128 v[148:151],v82 offset:10240\n"
"s_and_b32 vcc_hi,s52,1\n"
"v_mfma_f32_16x16x32_bf16 a[48:51],v[192:195],v[248:251],a[48:51]\n"
"ds_read_b128 v[212:215],v83 offset:10240\n"
"s_add_u32 vcc_lo,vcc_lo,vcc_hi\n"
"v_mfma_f32_16x16x32_bf16 a[56:59],v[192:195],v[252:255],a[56:59]\n"
"ds_read_b128 v[216:219],v83 offset:12288\n"
"s_lshr_b32 vcc_hi,s89,2\n"
"v_mfma_f32_16x16x32_bf16 a[56:59],v[128:131],v[188:191],a[56:59]\n"
"ds_read_b128 v[220:223],v83 offset:14336\n"
"s_waitcnt lgkmcnt(10)\n"
"v_mfma_f32_16x16x32_bf16 a[4:7],v[132:135],v[160:163],a[4:7]\n"
"s_lshr_b32 s84,s52,1\n"
"s_lshl1_add_u32 vcc_hi,vcc_hi,s84\n"
"v_mfma_f32_16x16x32_bf16 a[4:7],v[196:199],v[224:227],a[4:7]\n"
"s_mul_i32 s56,vcc_lo,1179648\n"
"s_lshl_b32 s84,s84,19\n"
"v_mfma_f32_16x16x32_bf16 a[12:15],v[196:199],v[228:231],a[12:15]\n"
"s_add_u32 s56,s56,s84\n"
"s_lshr_b32 s84,s89,2\n"
"v_mfma_f32_16x16x32_bf16 a[12:15],v[132:135],v[164:167],a[12:15]\n"
"s_lshl_b32 s84,s84,14\n"
"s_add_u32 s56,s56,s84\n"
"v_mfma_f32_16x16x32_bf16 a[20:23],v[132:135],v[168:171],a[20:23]\n"
"s_waitcnt lgkmcnt(0)\n"
"s_barrier\n"
"v_mfma_f32_16x16x32_bf16 a[20:23],v[196:199],v[232:235],a[20:23]\n"
"s_lshl_b32 s84,s53,13\n"
"s_mov_b32 s77,2097152\n"
"s_add_u32 m0,s78,147456\n"
"v_mfma_f32_16x16x32_bf16 a[28:31],v[196:199],v[236:239],a[28:31]\n"
"buffer_load_dwordx4 v110,s[20:23],s77 offen offset:0 lds\n"
"v_mfma_f32_16x16x32_bf16 a[28:31],v[132:135],v[172:175],a[28:31]\n"
"s_add_u32 s56,s56,s84\n"
"s_add_u32 s56,s56,4194304\n"
"v_mfma_f32_16x16x32_bf16 a[36:39],v[132:135],v[176:179],a[36:39]\n"
"s_lshl4_add_u32 vcc_hi,s53,vcc_hi\n"
"s_lshl_b32 vcc_lo,vcc_lo,8\n"
"v_mfma_f32_16x16x32_bf16 a[36:39],v[196:199],v[240:243],a[36:39]\n"
"s_lshl3_add_u32 s59,vcc_hi,vcc_lo\n"
"s_add_u32 s59,s59,148914176\n"
"v_mfma_f32_16x16x32_bf16 a[44:47],v[196:199],v[244:247],a[44:47]\n"
"buffer_load_dwordx4 v111,s[20:23],s77 offen offset:1024 lds\n"
"v_mfma_f32_16x16x32_bf16 a[44:47],v[132:135],v[180:183],a[44:47]\n"
"s_lshl_b32 s57,s92,3\n"
"s_add_u32 s57,s57,148898816\n"
"v_mfma_f32_16x16x32_bf16 a[52:55],v[132:135],v[184:187],a[52:55]\n"
"s_lshl_b32 s63,s90,8\n"
"s_add_u32 s63,s63,148914176\n"
"v_mfma_f32_16x16x32_bf16 a[52:55],v[196:199],v[248:251],a[52:55]\n"
"s_lshl_b32 s74,s92,11\n"
"s_add_u32 s74,s74,171966464\n"
"v_mfma_f32_16x16x32_bf16 a[60:63],v[196:199],v[252:255],a[60:63]\n"
"buffer_load_dwordx4 v113,s[20:23],s77 offen offset:2048 lds\n"
"v_mfma_f32_16x16x32_bf16 a[60:63],v[132:135],v[188:191],a[60:63]\n"
"s_mul_i32 s96,s90,1179648\n"
"s_add_u32 s96,s96,4194304\n"
"v_mfma_f32_16x16x32_bf16 a[64:67],v[136:139],v[160:163],a[64:67]\n"
"s_add_u32 s73,s96,s85\n"
"s_and_b32 vcc_hi,s78,4096\n"
"v_mfma_f32_16x16x32_bf16 a[64:67],v[200:203],v[224:227],a[64:67]\n"
"s_lshl_b32 vcc_hi,vcc_hi,1\n"
"s_add_u32 vcc_lo,s96,vcc_hi\n"
"v_mfma_f32_16x16x32_bf16 a[72:75],v[200:203],v[228:231],a[72:75]\n"
"buffer_load_dwordx4 v114,s[20:23],s77 offen offset:3072 lds\n"
"v_mfma_f32_16x16x32_bf16 a[72:75],v[136:139],v[164:167],a[72:75]\n"
"s_add_u32 s65,vcc_lo,524288\n"
"s_lshl_b32 s66,s91,16\n"
"v_mfma_f32_16x16x32_bf16 a[80:83],v[136:139],v[168:171],a[80:83]\n"
"s_sub_u32 s66,vcc_lo,s66\n"
"s_add_u32 s66,s66,999424\n"
"v_mfma_f32_16x16x32_bf16 a[80:83],v[200:203],v[232:235],a[80:83]\n"
"s_add_u32 s67,vcc_hi,65536\n"
"s_mov_b32 s77,2359296\n"
"s_add_u32 m0,s78,131072\n"
"v_mfma_f32_16x16x32_bf16 a[88:91],v[200:203],v[236:239],a[88:91]\n"
"buffer_load_dwordx4 v110,s[20:23],s77 offen offset:0 lds\n"
"v_mfma_f32_16x16x32_bf16 a[88:91],v[136:139],v[172:175],a[88:91]\n"
"s_mul_i32 vcc_lo,vcc_hi,5\n"
"s_sub_u32 vcc_lo,s96,vcc_lo\n"
"v_mfma_f32_16x16x32_bf16 a[96:99],v[136:139],v[176:179],a[96:99]\n"
"s_add_u32 s64,vcc_lo,81920\n"
"s_add_u32 s62,vcc_lo,135168\n"
"s_add_u32 s61,vcc_lo,524288\n"
"v_mfma_f32_16x16x32_bf16 a[96:99],v[200:203],v[240:243],a[96:99]\n"
"s_cmp_lt_u32 s52,2\n"
"s_cselect_b32 s64,s64,2146435072\n"
"s_cselect_b32 s67,s67,163840\n"
"s_cselect_b32 s65,s65,1\n"
"v_mfma_f32_16x16x32_bf16 a[104:107],v[200:203],v[244:247],a[104:107]\n"
"buffer_load_dwordx4 v111,s[20:23],s77 offen offset:1024 lds\n"
"v_mfma_f32_16x16x32_bf16 a[104:107],v[136:139],v[180:183],a[104:107]\n"
"s_cselect_b32 vcc_lo,4,0\n"
"s_and_b32 vcc_hi,s52,1\n"
"s_add_u32 vcc_lo,vcc_lo,vcc_hi\n"
"v_mfma_f32_16x16x32_bf16 a[112:115],v[136:139],v[184:187],a[112:115]\n"
"s_lshl_b32 vcc_hi,s90,12\n"
"s_add_u32 vcc_hi,vcc_hi,54525952\n"
"v_mfma_f32_16x16x32_bf16 a[112:115],v[200:203],v[248:251],a[112:115]\n"
"s_lshl3_add_u32 s70,s95,vcc_lo\n"
"s_lshl_b32 s70,s70,17\n"
"v_mfma_f32_16x16x32_bf16 a[120:123],v[200:203],v[252:255],a[120:123]\n"
"buffer_load_dwordx4 v113,s[20:23],s77 offen offset:2048 lds\n"
"v_mfma_f32_16x16x32_bf16 a[120:123],v[136:139],v[188:191],a[120:123]\n"
"s_add_u32 s70,s70,vcc_hi\n"
"s_lshl3_add_u32 s68,s94,vcc_lo\n"
"v_mfma_f32_16x16x32_bf16 a[68:71],v[140:143],v[160:163],a[68:71]\n"
"s_lshl_b32 s68,s68,17\n"
"s_add_u32 s68,s68,vcc_hi\n"
"v_mfma_f32_16x16x32_bf16 a[68:71],v[204:207],v[224:227],a[68:71]\n"
"s_lshr_b32 vcc_lo,s90,3\n"
"s_lshl2_add_u32 vcc_lo,vcc_lo,s52\n"
"v_mfma_f32_16x16x32_bf16 a[76:79],v[204:207],v[228:231],a[76:79]\n"
"buffer_load_dwordx4 v114,s[20:23],s77 offen offset:3072 lds\n"
"v_mfma_f32_16x16x32_bf16 a[76:79],v[140:143],v[164:167],a[76:79]\n"
"s_lshl_b32 vcc_lo,vcc_lo,7\n"
"s_and_b32 vcc_hi,s90,7\n"
"v_mfma_f32_16x16x32_bf16 a[84:87],v[140:143],v[168:171],a[84:87]\n"
"s_add_u32 vcc_lo,vcc_lo,vcc_hi\n"
"s_lshl3_add_u32 s69,s91,vcc_lo\n"
"v_mfma_f32_16x16x32_bf16 a[84:87],v[204:207],v[232:235],a[84:87]\n"
"s_lshl_b32 s69,s69,3\n"
"s_add_u32 s69,s69,148930560\n"
"v_mfma_f32_16x16x32_bf16 a[92:95],v[204:207],v[236:239],a[92:95]\n"
"s_lshl3_add_u32 s71,s94,vcc_lo\n"
"s_mov_b32 s76,640\n"
"s_add_u32 m0,s79,65536\n"
"v_mfma_f32_16x16x32_bf16 a[92:95],v[140:143],v[172:175],a[92:95]\n"
"buffer_load_dwordx4 v106,s[16:19],s76 offen offset:0 lds\n"
"v_mfma_f32_16x16x32_bf16 a[100:103],v[140:143],v[176:179],a[100:103]\n"
"s_lshl_b32 s71,s71,3\n"
"s_add_u32 s71,s71,148930560\n"
"v_mfma_f32_16x16x32_bf16 a[100:103],v[204:207],v[240:243],a[100:103]\n"
"s_mov_b32 s6,8388608\n"
"s_mov_b32 s7,0x20000\n"
"v_mfma_f32_16x16x32_bf16 a[108:111],v[204:207],v[244:247],a[108:111]\n"
"s_lshr_b32 s94,s88,1\n"
"s_and_b32 vcc_lo,s91,1\n"
"v_mfma_f32_16x16x32_bf16 a[108:111],v[140:143],v[180:183],a[108:111]\n"
"s_lshl1_add_u32 s95,s97,vcc_lo\n"
"s_and_b32 vcc_lo,s88,1\n"
"v_mfma_f32_16x16x32_bf16 a[116:119],v[140:143],v[184:187],a[116:119]\n"
"buffer_load_dwordx4 v107,s[16:19],s76 offen offset:1024 lds\n"
"v_mfma_f32_16x16x32_bf16 a[116:119],v[204:207],v[248:251],a[116:119]\n"
"s_lshr_b32 vcc_hi,s91,1\n"
"s_lshl2_add_u32 s96,vcc_lo,vcc_hi\n"
"v_mfma_f32_16x16x32_bf16 a[124:127],v[204:207],v[252:255],a[124:127]\n"
"s_lshr_b32 vcc_lo,s88,2\n"
"s_lshl2_add_u32 s96,vcc_lo,vcc_hi\n"
"v_mfma_f32_16x16x32_bf16 a[124:127],v[140:143],v[188:191],a[124:127]\n"
"s_sub_u32 vcc_lo,7,s88\n"
"s_min_u32 s94,vcc_lo,s88\n"
"v_mfma_f32_16x16x32_bf16 a[128:131],v[144:147],v[160:163],a[128:131]\n"
"s_and_b32 s96,s88,4\n"
"s_add_u32 s96,s96,s97\n"
"v_mfma_f32_16x16x32_bf16 a[128:131],v[208:211],v[224:227],a[128:131]\n"
"buffer_load_dwordx4 v108,s[16:19],s76 offen offset:2048 lds\n"
"v_mfma_f32_16x16x32_bf16 a[136:139],v[208:211],v[228:231],a[136:139]\n"
"s_add_u32 s95,s91,s96\n"
"s_and_b32 s95,s95,7\n"
"v_mfma_f32_16x16x32_bf16 a[136:139],v[144:147],v[164:167],a[136:139]\n"
"s_lshl3_add_u32 vcc_lo,s94,s96\n"
"s_lshl3_add_u32 vcc_lo,vcc_lo,s52\n"
"v_mfma_f32_16x16x32_bf16 a[144:147],v[144:147],v[168:171],a[144:147]\n"
"s_lshl_b32 s13,vcc_lo,15\n"
"s_lshl_b32 vcc_lo,s95,8\n"
"v_mfma_f32_16x16x32_bf16 a[144:147],v[208:211],v[232:235],a[144:147]\n"
"s_add_u32 s13,s13,vcc_lo\n"
"s_lshl3_add_u32 vcc_hi,s94,s95\n"
"v_mfma_f32_16x16x32_bf16 a[152:155],v[208:211],v[236:239],a[152:155]\n"
"buffer_load_dwordx4 v109,s[16:19],s76 offen offset:3072 lds\n"
"v_mfma_f32_16x16x32_bf16 a[152:155],v[144:147],v[172:175],a[152:155]\n"
"s_lshl_b32 s72,vcc_hi,8\n"
"s_add_u32 s72,s72,149061632\n"
"v_mfma_f32_16x16x32_bf16 a[160:163],v[144:147],v[176:179],a[160:163]\n"
"s_or_b32 vcc_lo,s96,s52\n"
"s_cselect_b32 s72,2146435072,s72\n"
"s_lshl_b32 s3,vcc_hi,18\n"
"v_mfma_f32_16x16x32_bf16 a[160:163],v[208:211],v[240:243],a[160:163]\n"
"s_lshl_b32 vcc_lo,s96,15\n"
"s_sub_u32 s3,s3,vcc_lo\n"
"v_mfma_f32_16x16x32_bf16 a[168:171],v[208:211],v[244:247],a[168:171]\n"
"s_add_u32 s3,s3,s78\n"
"s_mov_b32 s76,262784\n"
"s_add_u32 m0,s79,69632\n"
"v_mfma_f32_16x16x32_bf16 a[168:171],v[144:147],v[180:183],a[168:171]\n"
"buffer_load_dwordx4 v106,s[16:19],s76 offen offset:0 lds\n"
"v_mfma_f32_16x16x32_bf16 a[176:179],v[144:147],v[184:187],a[176:179]\n"
"s_add_u32 s3,s3,100909056\n"
"s_lshl3_add_u32 vcc_hi,vcc_hi,s96\n"
"v_mfma_f32_16x16x32_bf16 a[176:179],v[208:211],v[248:251],a[176:179]\n"
"s_lshl2_add_u32 vcc_hi,vcc_hi,s52\n"
"s_lshl_b32 s2,vcc_hi,4\n"
"v_mfma_f32_16x16x32_bf16 a[184:187],v[208:211],v[252:255],a[184:187]\n"
"s_add_u32 s2,s2,149028864\n"
"s_lshl2_add_u32 vcc_lo,s95,s52\n"
"v_mfma_f32_16x16x32_bf16 a[184:187],v[144:147],v[188:191],a[184:187]\n"
"s_lshl_b32 vcc_lo,vcc_lo,10\n"
"s_lshl_b32 vcc_hi,s94,22\n"
"v_mfma_f32_16x16x32_bf16 a[132:135],v[148:151],v[160:163],a[132:135]\n"
"buffer_load_dwordx4 v107,s[16:19],s76 offen offset:1024 lds\n"
"v_mfma_f32_16x16x32_bf16 a[132:135],v[212:215],v[224:227],a[132:135]\n"
"s_add_u32 s12,vcc_lo,vcc_hi\n"
"s_lshl_b32 vcc_lo,s96,19\n"
"v_mfma_f32_16x16x32_bf16 a[140:143],v[212:215],v[228:231],a[140:143]\n"
"s_add_u32 s10,s12,vcc_lo\n"
"s_add_u32 s10,s10,71303168\n"
"v_mfma_f32_16x16x32_bf16 a[140:143],v[148:151],v[164:167],a[140:143]\n"
"s_mul_i32 vcc_lo,s52,31744\n"
"s_add_u32 s10,s10,vcc_lo\n"
"v_mfma_f32_16x16x32_bf16 a[148:151],v[148:151],v[168:171],a[148:151]\n"
"s_lshl_b32 vcc_lo,s52,10\n"
"s_sub_u32 s12,57671680,vcc_lo\n"
"v_mfma_f32_16x16x32_bf16 a[148:151],v[212:215],v[232:235],a[148:151]\n"
"buffer_load_dwordx4 v108,s[16:19],s76 offen offset:2048 lds\n"
"v_mfma_f32_16x16x32_bf16 a[156:159],v[212:215],v[236:239],a[156:159]\n"
"s_add_u32 s8,s79,54525952\n"
"s_and_b32 vcc_lo,s96,1\n"
"v_mfma_f32_16x16x32_bf16 a[156:159],v[148:151],v[172:175],a[156:159]\n"
"s_lshl_b32 s11,vcc_lo,10\n"
"s_add_u32 s11,s11,148932608\n"
"v_mfma_f32_16x16x32_bf16 a[164:167],v[148:151],v[176:179],a[164:167]\n"
"s_mov_b32 s9,148931072\n"
"s_cmp_eq_u32 s52,0\n"
"s_cselect_b32 s71,s71,2146435072\n"
"v_mfma_f32_16x16x32_bf16 a[164:167],v[212:215],v[240:243],a[164:167]\n"
"v_mfma_f32_16x16x32_bf16 a[172:175],v[212:215],v[244:247],a[172:175]\n"
"buffer_load_dwordx4 v109,s[16:19],s76 offen offset:3072 lds\n"
"v_mfma_f32_16x16x32_bf16 a[172:175],v[148:151],v[180:183],a[172:175]\n"
"s_mov_b32 s87,0\n"
"v_mfma_f32_16x16x32_bf16 a[180:183],v[148:151],v[184:187],a[180:183]\n"
"buffer_load_dword v115,v112,s[40:43],s87 offen\n"
"v_mfma_f32_16x16x32_bf16 a[180:183],v[212:215],v[248:251],a[180:183]\n"
"s_mov_b32 s87,1048576\n"
"v_mfma_f32_16x16x32_bf16 a[188:191],v[212:215],v[252:255],a[188:191]\n"
"buffer_load_dword v115,v112,s[40:43],s87 offen\n"
"v_mfma_f32_16x16x32_bf16 a[188:191],v[148:151],v[188:191],a[188:191]\n"
"s_mov_b32 s87,2097152\n"
"s_waitcnt vmcnt(32)\n"
"s_barrier\n"
"v_mfma_f32_16x16x32_bf16 a[192:195],v[216:219],v[224:227],a[192:195]\n"
"ds_read_b128 v[152:155],v80 offset:12288\n"
"buffer_load_dword v115,v112,s[40:43],s87 offen\n"
"v_mfma_f32_16x16x32_bf16 a[196:199],v[220:223],v[224:227],a[196:199]\n"
"ds_read_b128 v[156:159],v80 offset:14336\n"
"v_mfma_f32_16x16x32_bf16 a[204:207],v[220:223],v[228:231],a[204:207]\n"
"ds_read_b64_tr_b16 v[160:161],v98 offset:0\n"
"v_mfma_f32_16x16x32_bf16 a[200:203],v[216:219],v[228:231],a[200:203]\n"
"ds_read_b64_tr_b16 v[162:163],v98 offset:1024\n"
"v_mfma_f32_16x16x32_bf16 a[208:211],v[216:219],v[232:235],a[208:211]\n"
"ds_read_b64_tr_b16 v[164:165],v99 offset:0\n"
"v_mfma_f32_16x16x32_bf16 a[212:215],v[220:223],v[232:235],a[212:215]\n"
"ds_read_b64_tr_b16 v[166:167],v99 offset:1024\n"
"v_mfma_f32_16x16x32_bf16 a[220:223],v[220:223],v[236:239],a[220:223]\n"
"ds_read_b64_tr_b16 v[168:169],v100 offset:0\n"
"v_mfma_f32_16x16x32_bf16 a[216:219],v[216:219],v[236:239],a[216:219]\n"
"ds_read_b64_tr_b16 v[170:171],v100 offset:1024\n"
"v_mfma_f32_16x16x32_bf16 a[224:227],v[216:219],v[240:243],a[224:227]\n"
"ds_read_b64_tr_b16 v[172:173],v101 offset:0\n"
"v_mfma_f32_16x16x32_bf16 a[228:231],v[220:223],v[240:243],a[228:231]\n"
"ds_read_b64_tr_b16 v[174:175],v101 offset:1024\n"
"v_mfma_f32_16x16x32_bf16 a[236:239],v[220:223],v[244:247],a[236:239]\n"
"ds_read_b64_tr_b16 v[176:177],v102 offset:0\n"
"v_mfma_f32_16x16x32_bf16 a[232:235],v[216:219],v[244:247],a[232:235]\n"
"ds_read_b64_tr_b16 v[178:179],v102 offset:1024\n"
"v_mfma_f32_16x16x32_bf16 a[240:243],v[216:219],v[248:251],a[240:243]\n"
"ds_read_b64_tr_b16 v[180:181],v103 offset:0\n"
"v_mfma_f32_16x16x32_bf16 a[244:247],v[220:223],v[248:251],a[244:247]\n"
"ds_read_b64_tr_b16 v[182:183],v103 offset:1024\n"
"v_mfma_f32_16x16x32_bf16 a[252:255],v[220:223],v[252:255],a[252:255]\n"
"ds_read_b64_tr_b16 v[184:185],v104 offset:0\n"
"v_mfma_f32_16x16x32_bf16 a[248:251],v[216:219],v[252:255],a[248:251]\n"
"ds_read_b64_tr_b16 v[186:187],v104 offset:1024\n"
"s_waitcnt lgkmcnt(10)\n"
"v_mfma_f32_16x16x32_bf16 a[192:195],v[152:155],v[160:163],a[192:195]\n"
"ds_read_b64_tr_b16 v[188:189],v105 offset:0\n"
"v_mfma_f32_16x16x32_bf16 a[196:199],v[156:159],v[160:163],a[196:199]\n"
"ds_read_b64_tr_b16 v[190:191],v105 offset:1024\n"
"v_mfma_f32_16x16x32_bf16 a[204:207],v[156:159],v[164:167],a[204:207]\n"
"ds_read_b128 v[128:131],v80 offset:0\n"
"v_mfma_f32_16x16x32_bf16 a[200:203],v[152:155],v[164:167],a[200:203]\n"
"ds_read_b128 v[192:195],v81 offset:0\n"
"s_waitcnt lgkmcnt(10)\n"
"v_mfma_f32_16x16x32_bf16 a[208:211],v[152:155],v[168:171],a[208:211]\n"
"ds_read_b64_tr_b16 v[224:225],v98 offset:16384\n"
"v_mfma_f32_16x16x32_bf16 a[212:215],v[156:159],v[168:171],a[212:215]\n"
"ds_read_b64_tr_b16 v[226:227],v98 offset:17408\n"
"v_mfma_f32_16x16x32_bf16 a[220:223],v[156:159],v[172:175],a[220:223]\n"
"ds_read_b64_tr_b16 v[228:229],v99 offset:16384\n"
"v_mfma_f32_16x16x32_bf16 a[216:219],v[152:155],v[172:175],a[216:219]\n"
"ds_read_b64_tr_b16 v[230:231],v99 offset:17408\n"
"s_waitcnt lgkmcnt(10)\n"
"v_mfma_f32_16x16x32_bf16 a[224:227],v[152:155],v[176:179],a[224:227]\n"
"ds_read_b64_tr_b16 v[232:233],v100 offset:16384\n"
"v_mfma_f32_16x16x32_bf16 a[228:231],v[156:159],v[176:179],a[228:231]\n"
"ds_read_b64_tr_b16 v[234:235],v100 offset:17408\n"
"v_mfma_f32_16x16x32_bf16 a[236:239],v[156:159],v[180:183],a[236:239]\n"
"ds_read_b64_tr_b16 v[236:237],v101 offset:16384\n"
"v_mfma_f32_16x16x32_bf16 a[232:235],v[152:155],v[180:183],a[232:235]\n"
"ds_read_b64_tr_b16 v[238:239],v101 offset:17408\n"
"s_waitcnt lgkmcnt(10)\n"
"v_mfma_f32_16x16x32_bf16 a[240:243],v[152:155],v[184:187],a[240:243]\n"
"ds_read_b64_tr_b16 v[240:241],v102 offset:16384\n"
"v_mfma_f32_16x16x32_bf16 a[244:247],v[156:159],v[184:187],a[244:247]\n"
"ds_read_b64_tr_b16 v[242:243],v102 offset:17408\n"
"v_mfma_f32_16x16x32_bf16 a[252:255],v[156:159],v[188:191],a[252:255]\n"
"ds_read_b64_tr_b16 v[244:245],v103 offset:16384\n"
"v_mfma_f32_16x16x32_bf16 a[248:251],v[152:155],v[188:191],a[248:251]\n"
"ds_read_b64_tr_b16 v[246:247],v103 offset:17408\n"
"s_waitcnt lgkmcnt(8)\n"
"v_mfma_f32_16x16x32_bf16 a[0:3],v[128:131],v[160:163],a[0:3]\n"
"ds_read_b64_tr_b16 v[248:249],v104 offset:16384\n"
"v_mfma_f32_16x16x32_bf16 a[0:3],v[192:195],v[224:227],a[0:3]\n"
"ds_read_b64_tr_b16 v[250:251],v104 offset:17408\n"
"v_mfma_f32_16x16x32_bf16 a[8:11],v[192:195],v[228:231],a[8:11]\n"
"ds_read_b64_tr_b16 v[252:253],v105 offset:16384\n"
"v_mfma_f32_16x16x32_bf16 a[8:11],v[128:131],v[164:167],a[8:11]\n"
"ds_read_b64_tr_b16 v[254:255],v105 offset:17408\n"
"s_waitcnt lgkmcnt(8)\n"
"v_mfma_f32_16x16x32_bf16 a[16:19],v[128:131],v[168:171],a[16:19]\n"
"ds_read_b128 v[132:135],v80 offset:2048\n"
"v_mfma_f32_16x16x32_bf16 a[16:19],v[192:195],v[232:235],a[16:19]\n"
"ds_read_b128 v[196:199],v81 offset:2048\n"
"v_mfma_f32_16x16x32_bf16 a[24:27],v[192:195],v[236:239],a[24:27]\n"
"ds_read_b128 v[136:139],v80 offset:4096\n"
"v_mfma_f32_16x16x32_bf16 a[24:27],v[128:131],v[172:175],a[24:27]\n"
"ds_read_b128 v[200:203],v81 offset:4096\n"
"s_waitcnt lgkmcnt(8)\n"
"v_mfma_f32_16x16x32_bf16 a[32:35],v[128:131],v[176:179],a[32:35]\n"
"ds_read_b128 v[140:143],v80 offset:6144\n"
"v_mfma_f32_16x16x32_bf16 a[32:35],v[192:195],v[240:243],a[32:35]\n"
"ds_read_b128 v[204:207],v81 offset:6144\n"
"v_mfma_f32_16x16x32_bf16 a[40:43],v[192:195],v[244:247],a[40:43]\n"
"ds_read_b128 v[144:147],v80 offset:8192\n"
"v_mfma_f32_16x16x32_bf16 a[40:43],v[128:131],v[180:183],a[40:43]\n"
"ds_read_b128 v[208:211],v81 offset:8192\n"
"s_waitcnt lgkmcnt(8)\n"
"v_mfma_f32_16x16x32_bf16 a[48:51],v[128:131],v[184:187],a[48:51]\n"
"ds_read_b128 v[148:151],v80 offset:10240\n"
"v_mfma_f32_16x16x32_bf16 a[48:51],v[192:195],v[248:251],a[48:51]\n"
"ds_read_b128 v[212:215],v81 offset:10240\n"
"v_mfma_f32_16x16x32_bf16 a[56:59],v[192:195],v[252:255],a[56:59]\n"
"ds_read_b128 v[216:219],v81 offset:12288\n"
"v_mfma_f32_16x16x32_bf16 a[56:59],v[128:131],v[188:191],a[56:59]\n"
"ds_read_b128 v[220:223],v81 offset:14336\n"
"s_waitcnt lgkmcnt(10)\n"
"v_mfma_f32_16x16x32_bf16 a[4:7],v[132:135],v[160:163],a[4:7]\n"
"v_mfma_f32_16x16x32_bf16 a[4:7],v[196:199],v[224:227],a[4:7]\n"
"v_mfma_f32_16x16x32_bf16 a[12:15],v[196:199],v[228:231],a[12:15]\n"
"v_mfma_f32_16x16x32_bf16 a[12:15],v[132:135],v[164:167],a[12:15]\n"
"v_mfma_f32_16x16x32_bf16 a[20:23],v[132:135],v[168:171],a[20:23]\n"
"s_waitcnt lgkmcnt(0)\n"
"s_barrier\n"
"v_mfma_f32_16x16x32_bf16 a[20:23],v[196:199],v[232:235],a[20:23]\n"
"s_mov_b32 s77,2621440\n"
"s_add_u32 m0,s78,98304\n"
"v_mfma_f32_16x16x32_bf16 a[28:31],v[196:199],v[236:239],a[28:31]\n"
"buffer_load_dwordx4 v110,s[20:23],s77 offen offset:0 lds\n"
"v_mfma_f32_16x16x32_bf16 a[28:31],v[132:135],v[172:175],a[28:31]\n"
"v_mfma_f32_16x16x32_bf16 a[36:39],v[132:135],v[176:179],a[36:39]\n"
"v_mfma_f32_16x16x32_bf16 a[36:39],v[196:199],v[240:243],a[36:39]\n"
"v_mfma_f32_16x16x32_bf16 a[44:47],v[196:199],v[244:247],a[44:47]\n"
"buffer_load_dwordx4 v111,s[20:23],s77 offen offset:1024 lds\n"
"v_mfma_f32_16x16x32_bf16 a[44:47],v[132:135],v[180:183],a[44:47]\n"
"v_mfma_f32_16x16x32_bf16 a[52:55],v[132:135],v[184:187],a[52:55]\n"
"v_mfma_f32_16x16x32_bf16 a[52:55],v[196:199],v[248:251],a[52:55]\n"
"v_mfma_f32_16x16x32_bf16 a[60:63],v[196:199],v[252:255],a[60:63]\n"
"buffer_load_dwordx4 v113,s[20:23],s77 offen offset:2048 lds\n"
"v_mfma_f32_16x16x32_bf16 a[60:63],v[132:135],v[188:191],a[60:63]\n"
"v_mfma_f32_16x16x32_bf16 a[64:67],v[136:139],v[160:163],a[64:67]\n"
"v_mfma_f32_16x16x32_bf16 a[64:67],v[200:203],v[224:227],a[64:67]\n"
"v_mfma_f32_16x16x32_bf16 a[72:75],v[200:203],v[228:231],a[72:75]\n"
"buffer_load_dwordx4 v114,s[20:23],s77 offen offset:3072 lds\n"
"v_mfma_f32_16x16x32_bf16 a[72:75],v[136:139],v[164:167],a[72:75]\n"
"v_mfma_f32_16x16x32_bf16 a[80:83],v[136:139],v[168:171],a[80:83]\n"
"v_mfma_f32_16x16x32_bf16 a[80:83],v[200:203],v[232:235],a[80:83]\n"
"s_mov_b32 s77,2883584\n"
"s_add_u32 m0,s78,114688\n"
"v_mfma_f32_16x16x32_bf16 a[88:91],v[200:203],v[236:239],a[88:91]\n"
"buffer_load_dwordx4 v110,s[20:23],s77 offen offset:0 lds\n"
"v_mfma_f32_16x16x32_bf16 a[88:91],v[136:139],v[172:175],a[88:91]\n"
"v_mfma_f32_16x16x32_bf16 a[96:99],v[136:139],v[176:179],a[96:99]\n"
"v_mfma_f32_16x16x32_bf16 a[96:99],v[200:203],v[240:243],a[96:99]\n"
"v_mfma_f32_16x16x32_bf16 a[104:107],v[200:203],v[244:247],a[104:107]\n"
"buffer_load_dwordx4 v111,s[20:23],s77 offen offset:1024 lds\n"
"v_mfma_f32_16x16x32_bf16 a[104:107],v[136:139],v[180:183],a[104:107]\n"
"v_mfma_f32_16x16x32_bf16 a[112:115],v[136:139],v[184:187],a[112:115]\n"
"v_mfma_f32_16x16x32_bf16 a[112:115],v[200:203],v[248:251],a[112:115]\n"
"v_mfma_f32_16x16x32_bf16 a[120:123],v[200:203],v[252:255],a[120:123]\n"
"buffer_load_dwordx4 v113,s[20:23],s77 offen offset:2048 lds\n"
"v_mfma_f32_16x16x32_bf16 a[120:123],v[136:139],v[188:191],a[120:123]\n"
"s_waitcnt vmcnt(26)\n"
"v_mfma_f32_16x16x32_bf16 a[68:71],v[140:143],v[160:163],a[68:71]\n"
"v_mfma_f32_16x16x32_bf16 a[68:71],v[204:207],v[224:227],a[68:71]\n"
"v_mfma_f32_16x16x32_bf16 a[76:79],v[204:207],v[228:231],a[76:79]\n"
"buffer_load_dwordx4 v114,s[20:23],s77 offen offset:3072 lds\n"
"v_mfma_f32_16x16x32_bf16 a[76:79],v[140:143],v[164:167],a[76:79]\n"
"v_mfma_f32_16x16x32_bf16 a[84:87],v[140:143],v[168:171],a[84:87]\n"
"v_mfma_f32_16x16x32_bf16 a[84:87],v[204:207],v[232:235],a[84:87]\n"
"v_mfma_f32_16x16x32_bf16 a[92:95],v[204:207],v[236:239],a[92:95]\n"
"s_mov_b32 s76,768\n"
"s_add_u32 m0,s79,0\n"
"v_mfma_f32_16x16x32_bf16 a[92:95],v[140:143],v[172:175],a[92:95]\n"
"buffer_load_dwordx4 v106,s[16:19],s76 offen offset:0 lds\n"
"v_mfma_f32_16x16x32_bf16 a[100:103],v[140:143],v[176:179],a[100:103]\n"
"v_mfma_f32_16x16x32_bf16 a[100:103],v[204:207],v[240:243],a[100:103]\n"
"v_mfma_f32_16x16x32_bf16 a[108:111],v[204:207],v[244:247],a[108:111]\n"
"v_mfma_f32_16x16x32_bf16 a[108:111],v[140:143],v[180:183],a[108:111]\n"
"v_mfma_f32_16x16x32_bf16 a[116:119],v[140:143],v[184:187],a[116:119]\n"
"buffer_load_dwordx4 v107,s[16:19],s76 offen offset:1024 lds\n"
"v_mfma_f32_16x16x32_bf16 a[116:119],v[204:207],v[248:251],a[116:119]\n"
"v_mfma_f32_16x16x32_bf16 a[124:127],v[204:207],v[252:255],a[124:127]\n"
"v_mfma_f32_16x16x32_bf16 a[124:127],v[140:143],v[188:191],a[124:127]\n"
"v_mfma_f32_16x16x32_bf16 a[128:131],v[144:147],v[160:163],a[128:131]\n"
"v_mfma_f32_16x16x32_bf16 a[128:131],v[208:211],v[224:227],a[128:131]\n"
"buffer_load_dwordx4 v108,s[16:19],s76 offen offset:2048 lds\n"
"v_mfma_f32_16x16x32_bf16 a[136:139],v[208:211],v[228:231],a[136:139]\n"
"v_mfma_f32_16x16x32_bf16 a[136:139],v[144:147],v[164:167],a[136:139]\n"
"v_mfma_f32_16x16x32_bf16 a[144:147],v[144:147],v[168:171],a[144:147]\n"
"v_mfma_f32_16x16x32_bf16 a[144:147],v[208:211],v[232:235],a[144:147]\n"
"v_mfma_f32_16x16x32_bf16 a[152:155],v[208:211],v[236:239],a[152:155]\n"
"buffer_load_dwordx4 v109,s[16:19],s76 offen offset:3072 lds\n"
"v_mfma_f32_16x16x32_bf16 a[152:155],v[144:147],v[172:175],a[152:155]\n"
"v_mfma_f32_16x16x32_bf16 a[160:163],v[144:147],v[176:179],a[160:163]\n"
"v_mfma_f32_16x16x32_bf16 a[160:163],v[208:211],v[240:243],a[160:163]\n"
"buffer_store_dwordx2 v[124:125],off,s[24:27],s57 sc1\n"
"v_mfma_f32_16x16x32_bf16 a[168:171],v[208:211],v[244:247],a[168:171]\n"
"s_mov_b32 s76,262912\n"
"s_add_u32 m0,s79,4096\n"
"v_mfma_f32_16x16x32_bf16 a[168:171],v[144:147],v[180:183],a[168:171]\n"
"buffer_load_dwordx4 v106,s[16:19],s76 offen offset:0 lds\n"
"v_mfma_f32_16x16x32_bf16 a[176:179],v[144:147],v[184:187],a[176:179]\n"
"v_mfma_f32_16x16x32_bf16 a[176:179],v[208:211],v[248:251],a[176:179]\n"
"v_mfma_f32_16x16x32_bf16 a[184:187],v[208:211],v[252:255],a[184:187]\n"
"v_mfma_f32_16x16x32_bf16 a[184:187],v[144:147],v[188:191],a[184:187]\n"
"v_mfma_f32_16x16x32_bf16 a[132:135],v[148:151],v[160:163],a[132:135]\n"
"buffer_load_dwordx4 v107,s[16:19],s76 offen offset:1024 lds\n"
"v_mfma_f32_16x16x32_bf16 a[132:135],v[212:215],v[224:227],a[132:135]\n"
"v_mfma_f32_16x16x32_bf16 a[140:143],v[212:215],v[228:231],a[140:143]\n"
"v_mfma_f32_16x16x32_bf16 a[140:143],v[148:151],v[164:167],a[140:143]\n"
"v_mfma_f32_16x16x32_bf16 a[148:151],v[148:151],v[168:171],a[148:151]\n"
"v_mfma_f32_16x16x32_bf16 a[148:151],v[212:215],v[232:235],a[148:151]\n"
"buffer_load_dwordx4 v108,s[16:19],s76 offen offset:2048 lds\n"
"v_mfma_f32_16x16x32_bf16 a[156:159],v[212:215],v[236:239],a[156:159]\n"
"v_mfma_f32_16x16x32_bf16 a[156:159],v[148:151],v[172:175],a[156:159]\n"
"v_mfma_f32_16x16x32_bf16 a[164:167],v[148:151],v[176:179],a[164:167]\n"
"v_mfma_f32_16x16x32_bf16 a[164:167],v[212:215],v[240:243],a[164:167]\n"
"v_mfma_f32_16x16x32_bf16 a[172:175],v[212:215],v[244:247],a[172:175]\n"
"buffer_load_dwordx4 v109,s[16:19],s76 offen offset:3072 lds\n"
"v_mfma_f32_16x16x32_bf16 a[172:175],v[148:151],v[180:183],a[172:175]\n"
"s_mov_b32 s87,3145728\n"
"v_mfma_f32_16x16x32_bf16 a[180:183],v[148:151],v[184:187],a[180:183]\n"
"buffer_load_dword v115,v112,s[40:43],s87 offen\n"
"v_mfma_f32_16x16x32_bf16 a[180:183],v[212:215],v[248:251],a[180:183]\n"
"s_mov_b32 s87,4194304\n"
"v_mfma_f32_16x16x32_bf16 a[188:191],v[212:215],v[252:255],a[188:191]\n"
"buffer_load_dword v115,v112,s[40:43],s87 offen\n"
"v_mfma_f32_16x16x32_bf16 a[188:191],v[148:151],v[188:191],a[188:191]\n"
"s_mov_b32 s87,5242880\n"
"s_waitcnt vmcnt(30)\n"
"s_barrier\n"
"v_mfma_f32_16x16x32_bf16 a[192:195],v[216:219],v[224:227],a[192:195]\n"
"ds_read_b128 v[152:155],v80 offset:45056\n"
"buffer_load_dword v115,v112,s[40:43],s87 offen\n"
"v_mfma_f32_16x16x32_bf16 a[196:199],v[220:223],v[224:227],a[196:199]\n"
"ds_read_b128 v[156:159],v80 offset:47104\n"
"v_mfma_f32_16x16x32_bf16 a[204:207],v[220:223],v[228:231],a[204:207]\n"
"ds_read_b64_tr_b16 v[160:161],v98 offset:49152\n"
"v_mfma_f32_16x16x32_bf16 a[200:203],v[216:219],v[228:231],a[200:203]\n"
"ds_read_b64_tr_b16 v[162:163],v98 offset:50176\n"
"v_mfma_f32_16x16x32_bf16 a[208:211],v[216:219],v[232:235],a[208:211]\n"
"ds_read_b64_tr_b16 v[164:165],v99 offset:49152\n"
"v_mfma_f32_16x16x32_bf16 a[212:215],v[220:223],v[232:235],a[212:215]\n"
"ds_read_b64_tr_b16 v[166:167],v99 offset:50176\n"
"v_mfma_f32_16x16x32_bf16 a[220:223],v[220:223],v[236:239],a[220:223]\n"
"ds_read_b64_tr_b16 v[168:169],v100 offset:49152\n"
"v_mfma_f32_16x16x32_bf16 a[216:219],v[216:219],v[236:239],a[216:219]\n"
"ds_read_b64_tr_b16 v[170:171],v100 offset:50176\n"
"v_mfma_f32_16x16x32_bf16 a[224:227],v[216:219],v[240:243],a[224:227]\n"
"ds_read_b64_tr_b16 v[172:173],v101 offset:49152\n"
"v_mfma_f32_16x16x32_bf16 a[228:231],v[220:223],v[240:243],a[228:231]\n"
"ds_read_b64_tr_b16 v[174:175],v101 offset:50176\n"
"v_mfma_f32_16x16x32_bf16 a[236:239],v[220:223],v[244:247],a[236:239]\n"
"ds_read_b64_tr_b16 v[176:177],v102 offset:49152\n"
"v_mfma_f32_16x16x32_bf16 a[232:235],v[216:219],v[244:247],a[232:235]\n"
"ds_read_b64_tr_b16 v[178:179],v102 offset:50176\n"
"v_mfma_f32_16x16x32_bf16 a[240:243],v[216:219],v[248:251],a[240:243]\n"
"ds_read_b64_tr_b16 v[180:181],v103 offset:49152\n"
"v_mfma_f32_16x16x32_bf16 a[244:247],v[220:223],v[248:251],a[244:247]\n"
"ds_read_b64_tr_b16 v[182:183],v103 offset:50176\n"
"v_mfma_f32_16x16x32_bf16 a[252:255],v[220:223],v[252:255],a[252:255]\n"
"ds_read_b64_tr_b16 v[184:185],v104 offset:49152\n"
"v_mfma_f32_16x16x32_bf16 a[248:251],v[216:219],v[252:255],a[248:251]\n"
"ds_read_b64_tr_b16 v[186:187],v104 offset:50176\n"
"s_waitcnt lgkmcnt(10)\n"
"v_mfma_f32_16x16x32_bf16 a[192:195],v[152:155],v[160:163],a[192:195]\n"
"ds_read_b64_tr_b16 v[188:189],v105 offset:49152\n"
"v_mfma_f32_16x16x32_bf16 a[196:199],v[156:159],v[160:163],a[196:199]\n"
"ds_read_b64_tr_b16 v[190:191],v105 offset:50176\n"
"v_mfma_f32_16x16x32_bf16 a[204:207],v[156:159],v[164:167],a[204:207]\n"
"ds_read_b128 v[128:131],v80 offset:32768\n"
"v_mfma_f32_16x16x32_bf16 a[200:203],v[152:155],v[164:167],a[200:203]\n"
"ds_read_b128 v[192:195],v81 offset:32768\n"
"s_waitcnt lgkmcnt(10)\n"
"v_mfma_f32_16x16x32_bf16 a[208:211],v[152:155],v[168:171],a[208:211]\n"
"ds_read_b64_tr_b16 v[224:225],v98 offset:32768\n"
"v_mfma_f32_16x16x32_bf16 a[212:215],v[156:159],v[168:171],a[212:215]\n"
"ds_read_b64_tr_b16 v[226:227],v98 offset:33792\n"
"v_mfma_f32_16x16x32_bf16 a[220:223],v[156:159],v[172:175],a[220:223]\n"
"ds_read_b64_tr_b16 v[228:229],v99 offset:32768\n"
"v_mfma_f32_16x16x32_bf16 a[216:219],v[152:155],v[172:175],a[216:219]\n"
"ds_read_b64_tr_b16 v[230:231],v99 offset:33792\n"
"s_waitcnt lgkmcnt(10)\n"
"v_mfma_f32_16x16x32_bf16 a[224:227],v[152:155],v[176:179],a[224:227]\n"
"ds_read_b64_tr_b16 v[232:233],v100 offset:32768\n"
"v_mfma_f32_16x16x32_bf16 a[228:231],v[156:159],v[176:179],a[228:231]\n"
"ds_read_b64_tr_b16 v[234:235],v100 offset:33792\n"
"v_mfma_f32_16x16x32_bf16 a[236:239],v[156:159],v[180:183],a[236:239]\n"
"ds_read_b64_tr_b16 v[236:237],v101 offset:32768\n"
"v_mfma_f32_16x16x32_bf16 a[232:235],v[152:155],v[180:183],a[232:235]\n"
"ds_read_b64_tr_b16 v[238:239],v101 offset:33792\n"
"s_waitcnt lgkmcnt(10)\n"
"v_mfma_f32_16x16x32_bf16 a[240:243],v[152:155],v[184:187],a[240:243]\n"
"ds_read_b64_tr_b16 v[240:241],v102 offset:32768\n"
"v_mfma_f32_16x16x32_bf16 a[244:247],v[156:159],v[184:187],a[244:247]\n"
"ds_read_b64_tr_b16 v[242:243],v102 offset:33792\n"
"v_mfma_f32_16x16x32_bf16 a[252:255],v[156:159],v[188:191],a[252:255]\n"
"ds_read_b64_tr_b16 v[244:245],v103 offset:32768\n"
"v_mfma_f32_16x16x32_bf16 a[248:251],v[152:155],v[188:191],a[248:251]\n"
"ds_read_b64_tr_b16 v[246:247],v103 offset:33792\n"
"s_waitcnt lgkmcnt(8)\n"
"v_mfma_f32_16x16x32_bf16 a[0:3],v[128:131],v[160:163],a[0:3]\n"
"ds_read_b64_tr_b16 v[248:249],v104 offset:32768\n"
"v_mfma_f32_16x16x32_bf16 a[0:3],v[192:195],v[224:227],a[0:3]\n"
"ds_read_b64_tr_b16 v[250:251],v104 offset:33792\n"
"v_mfma_f32_16x16x32_bf16 a[8:11],v[192:195],v[228:231],a[8:11]\n"
"ds_read_b64_tr_b16 v[252:253],v105 offset:32768\n"
"v_mfma_f32_16x16x32_bf16 a[8:11],v[128:131],v[164:167],a[8:11]\n"
"ds_read_b64_tr_b16 v[254:255],v105 offset:33792\n"
"s_waitcnt lgkmcnt(8)\n"
"v_mfma_f32_16x16x32_bf16 a[16:19],v[128:131],v[168:171],a[16:19]\n"
"ds_read_b128 v[132:135],v80 offset:34816\n"
"v_mfma_f32_16x16x32_bf16 a[16:19],v[192:195],v[232:235],a[16:19]\n"
"ds_read_b128 v[196:199],v81 offset:34816\n"
"v_mfma_f32_16x16x32_bf16 a[24:27],v[192:195],v[236:239],a[24:27]\n"
"ds_read_b128 v[136:139],v80 offset:36864\n"
"v_mfma_f32_16x16x32_bf16 a[24:27],v[128:131],v[172:175],a[24:27]\n"
"ds_read_b128 v[200:203],v81 offset:36864\n"
"s_waitcnt lgkmcnt(8)\n"
"v_mfma_f32_16x16x32_bf16 a[32:35],v[128:131],v[176:179],a[32:35]\n"
"ds_read_b128 v[140:143],v80 offset:38912\n"
"v_mfma_f32_16x16x32_bf16 a[32:35],v[192:195],v[240:243],a[32:35]\n"
"ds_read_b128 v[204:207],v81 offset:38912\n"
"v_mfma_f32_16x16x32_bf16 a[40:43],v[192:195],v[244:247],a[40:43]\n"
"ds_read_b128 v[144:147],v80 offset:40960\n"
"v_mfma_f32_16x16x32_bf16 a[40:43],v[128:131],v[180:183],a[40:43]\n"
"ds_read_b128 v[208:211],v81 offset:40960\n"
"s_waitcnt lgkmcnt(8)\n"
"v_mfma_f32_16x16x32_bf16 a[48:51],v[128:131],v[184:187],a[48:51]\n"
"ds_read_b128 v[148:151],v80 offset:43008\n"
"v_mfma_f32_16x16x32_bf16 a[48:51],v[192:195],v[248:251],a[48:51]\n"
"ds_read_b128 v[212:215],v81 offset:43008\n"
"v_mfma_f32_16x16x32_bf16 a[56:59],v[192:195],v[252:255],a[56:59]\n"
"ds_read_b128 v[216:219],v81 offset:45056\n"
"v_mfma_f32_16x16x32_bf16 a[56:59],v[128:131],v[188:191],a[56:59]\n"
"ds_read_b128 v[220:223],v81 offset:47104\n"
"s_waitcnt lgkmcnt(10)\n"
"v_mfma_f32_16x16x32_bf16 a[4:7],v[132:135],v[160:163],a[4:7]\n"
"v_mfma_f32_16x16x32_bf16 a[4:7],v[196:199],v[224:227],a[4:7]\n"
"v_mfma_f32_16x16x32_bf16 a[12:15],v[196:199],v[228:231],a[12:15]\n"
"v_mfma_f32_16x16x32_bf16 a[12:15],v[132:135],v[164:167],a[12:15]\n"
"v_mfma_f32_16x16x32_bf16 a[20:23],v[132:135],v[168:171],a[20:23]\n"
"s_waitcnt lgkmcnt(0)\n"
"s_barrier\n"
"v_mfma_f32_16x16x32_bf16 a[20:23],v[196:199],v[232:235],a[20:23]\n"
"s_mov_b32 s77,3145728\n"
"s_add_u32 m0,s78,147456\n"
"v_mfma_f32_16x16x32_bf16 a[28:31],v[196:199],v[236:239],a[28:31]\n"
"buffer_load_dwordx4 v110,s[20:23],s77 offen offset:0 lds\n"
"v_mfma_f32_16x16x32_bf16 a[28:31],v[132:135],v[172:175],a[28:31]\n"
"v_mfma_f32_16x16x32_bf16 a[36:39],v[132:135],v[176:179],a[36:39]\n"
"v_mfma_f32_16x16x32_bf16 a[36:39],v[196:199],v[240:243],a[36:39]\n"
"v_mfma_f32_16x16x32_bf16 a[44:47],v[196:199],v[244:247],a[44:47]\n"
"buffer_load_dwordx4 v111,s[20:23],s77 offen offset:1024 lds\n"
"v_mfma_f32_16x16x32_bf16 a[44:47],v[132:135],v[180:183],a[44:47]\n"
"v_mfma_f32_16x16x32_bf16 a[52:55],v[132:135],v[184:187],a[52:55]\n"
"v_mfma_f32_16x16x32_bf16 a[52:55],v[196:199],v[248:251],a[52:55]\n"
"v_mfma_f32_16x16x32_bf16 a[60:63],v[196:199],v[252:255],a[60:63]\n"
"buffer_load_dwordx4 v113,s[20:23],s77 offen offset:2048 lds\n"
"v_mfma_f32_16x16x32_bf16 a[60:63],v[132:135],v[188:191],a[60:63]\n"
"v_mfma_f32_16x16x32_bf16 a[64:67],v[136:139],v[160:163],a[64:67]\n"
"v_mfma_f32_16x16x32_bf16 a[64:67],v[200:203],v[224:227],a[64:67]\n"
"v_mfma_f32_16x16x32_bf16 a[72:75],v[200:203],v[228:231],a[72:75]\n"
"buffer_load_dwordx4 v114,s[20:23],s77 offen offset:3072 lds\n"
"v_mfma_f32_16x16x32_bf16 a[72:75],v[136:139],v[164:167],a[72:75]\n"
"v_mfma_f32_16x16x32_bf16 a[80:83],v[136:139],v[168:171],a[80:83]\n"
"v_mfma_f32_16x16x32_bf16 a[80:83],v[200:203],v[232:235],a[80:83]\n"
"s_mov_b32 s77,3407872\n"
"s_add_u32 m0,s78,131072\n"
"v_mfma_f32_16x16x32_bf16 a[88:91],v[200:203],v[236:239],a[88:91]\n"
"buffer_load_dwordx4 v110,s[20:23],s77 offen offset:0 lds\n"
"v_mfma_f32_16x16x32_bf16 a[88:91],v[136:139],v[172:175],a[88:91]\n"
"v_mfma_f32_16x16x32_bf16 a[96:99],v[136:139],v[176:179],a[96:99]\n"
"v_mfma_f32_16x16x32_bf16 a[96:99],v[200:203],v[240:243],a[96:99]\n"
"v_mfma_f32_16x16x32_bf16 a[104:107],v[200:203],v[244:247],a[104:107]\n"
"buffer_load_dwordx4 v111,s[20:23],s77 offen offset:1024 lds\n"
"v_mfma_f32_16x16x32_bf16 a[104:107],v[136:139],v[180:183],a[104:107]\n"
"v_mfma_f32_16x16x32_bf16 a[112:115],v[136:139],v[184:187],a[112:115]\n"
"v_mfma_f32_16x16x32_bf16 a[112:115],v[200:203],v[248:251],a[112:115]\n"
"v_mfma_f32_16x16x32_bf16 a[120:123],v[200:203],v[252:255],a[120:123]\n"
"buffer_load_dwordx4 v113,s[20:23],s77 offen offset:2048 lds\n"
"v_mfma_f32_16x16x32_bf16 a[120:123],v[136:139],v[188:191],a[120:123]\n"
"v_mfma_f32_16x16x32_bf16 a[68:71],v[140:143],v[160:163],a[68:71]\n"
"v_mfma_f32_16x16x32_bf16 a[68:71],v[204:207],v[224:227],a[68:71]\n"
"v_mfma_f32_16x16x32_bf16 a[76:79],v[204:207],v[228:231],a[76:79]\n"
"buffer_load_dwordx4 v114,s[20:23],s77 offen offset:3072 lds\n"
"v_mfma_f32_16x16x32_bf16 a[76:79],v[140:143],v[164:167],a[76:79]\n"
"v_mfma_f32_16x16x32_bf16 a[84:87],v[140:143],v[168:171],a[84:87]\n"
"v_mfma_f32_16x16x32_bf16 a[84:87],v[204:207],v[232:235],a[84:87]\n"
"v_mfma_f32_16x16x32_bf16 a[92:95],v[204:207],v[236:239],a[92:95]\n"
"s_mov_b32 s76,896\n"
"s_add_u32 m0,s79,32768\n"
"v_mfma_f32_16x16x32_bf16 a[92:95],v[140:143],v[172:175],a[92:95]\n"
"buffer_load_dwordx4 v106,s[16:19],s76 offen offset:0 lds\n"
"v_mfma_f32_16x16x32_bf16 a[100:103],v[140:143],v[176:179],a[100:103]\n"
"v_mfma_f32_16x16x32_bf16 a[100:103],v[204:207],v[240:243],a[100:103]\n"
"v_mfma_f32_16x16x32_bf16 a[108:111],v[204:207],v[244:247],a[108:111]\n"
"v_mfma_f32_16x16x32_bf16 a[108:111],v[140:143],v[180:183],a[108:111]\n"
"v_mfma_f32_16x16x32_bf16 a[116:119],v[140:143],v[184:187],a[116:119]\n"
"buffer_load_dwordx4 v107,s[16:19],s76 offen offset:1024 lds\n"
"v_mfma_f32_16x16x32_bf16 a[116:119],v[204:207],v[248:251],a[116:119]\n"
"v_mfma_f32_16x16x32_bf16 a[124:127],v[204:207],v[252:255],a[124:127]\n"
"v_mfma_f32_16x16x32_bf16 a[124:127],v[140:143],v[188:191],a[124:127]\n"
"v_mfma_f32_16x16x32_bf16 a[128:131],v[144:147],v[160:163],a[128:131]\n"
"v_mfma_f32_16x16x32_bf16 a[128:131],v[208:211],v[224:227],a[128:131]\n"
"buffer_load_dwordx4 v108,s[16:19],s76 offen offset:2048 lds\n"
"v_mfma_f32_16x16x32_bf16 a[136:139],v[208:211],v[228:231],a[136:139]\n"
"v_mfma_f32_16x16x32_bf16 a[136:139],v[144:147],v[164:167],a[136:139]\n"
"v_mfma_f32_16x16x32_bf16 a[144:147],v[144:147],v[168:171],a[144:147]\n"
"v_mfma_f32_16x16x32_bf16 a[144:147],v[208:211],v[232:235],a[144:147]\n"
"v_mfma_f32_16x16x32_bf16 a[152:155],v[208:211],v[236:239],a[152:155]\n"
"buffer_load_dwordx4 v109,s[16:19],s76 offen offset:3072 lds\n"
"v_mfma_f32_16x16x32_bf16 a[152:155],v[144:147],v[172:175],a[152:155]\n"
"v_mfma_f32_16x16x32_bf16 a[160:163],v[144:147],v[176:179],a[160:163]\n"
"v_mfma_f32_16x16x32_bf16 a[160:163],v[208:211],v[240:243],a[160:163]\n"
"v_mfma_f32_16x16x32_bf16 a[168:171],v[208:211],v[244:247],a[168:171]\n"
"s_mov_b32 s76,263040\n"
"s_add_u32 m0,s79,36864\n"
"v_mfma_f32_16x16x32_bf16 a[168:171],v[144:147],v[180:183],a[168:171]\n"
"buffer_load_dwordx4 v106,s[16:19],s76 offen offset:0 lds\n"
"v_mfma_f32_16x16x32_bf16 a[176:179],v[144:147],v[184:187],a[176:179]\n"
"v_mfma_f32_16x16x32_bf16 a[176:179],v[208:211],v[248:251],a[176:179]\n"
"v_mfma_f32_16x16x32_bf16 a[184:187],v[208:211],v[252:255],a[184:187]\n"
"v_mfma_f32_16x16x32_bf16 a[184:187],v[144:147],v[188:191],a[184:187]\n"
"v_mfma_f32_16x16x32_bf16 a[132:135],v[148:151],v[160:163],a[132:135]\n"
"buffer_load_dwordx4 v107,s[16:19],s76 offen offset:1024 lds\n"
"v_mfma_f32_16x16x32_bf16 a[132:135],v[212:215],v[224:227],a[132:135]\n"
"v_mfma_f32_16x16x32_bf16 a[140:143],v[212:215],v[228:231],a[140:143]\n"
"v_mfma_f32_16x16x32_bf16 a[140:143],v[148:151],v[164:167],a[140:143]\n"
"v_mfma_f32_16x16x32_bf16 a[148:151],v[148:151],v[168:171],a[148:151]\n"
"v_mfma_f32_16x16x32_bf16 a[148:151],v[212:215],v[232:235],a[148:151]\n"
"buffer_load_dwordx4 v108,s[16:19],s76 offen offset:2048 lds\n"
"v_mfma_f32_16x16x32_bf16 a[156:159],v[212:215],v[236:239],a[156:159]\n"
"v_mfma_f32_16x16x32_bf16 a[156:159],v[148:151],v[172:175],a[156:159]\n"
"v_mfma_f32_16x16x32_bf16 a[164:167],v[148:151],v[176:179],a[164:167]\n"
"v_mfma_f32_16x16x32_bf16 a[164:167],v[212:215],v[240:243],a[164:167]\n"
"v_mfma_f32_16x16x32_bf16 a[172:175],v[212:215],v[244:247],a[172:175]\n"
"buffer_load_dwordx4 v109,s[16:19],s76 offen offset:3072 lds\n"
"v_mfma_f32_16x16x32_bf16 a[172:175],v[148:151],v[180:183],a[172:175]\n"
"s_mov_b32 s87,6291456\n"
"v_mfma_f32_16x16x32_bf16 a[180:183],v[148:151],v[184:187],a[180:183]\n"
"buffer_load_dword v115,v112,s[40:43],s87 offen\n"
"v_mfma_f32_16x16x32_bf16 a[180:183],v[212:215],v[248:251],a[180:183]\n"
"s_mov_b32 s87,7340032\n"
"v_mfma_f32_16x16x32_bf16 a[188:191],v[212:215],v[252:255],a[188:191]\n"
"buffer_load_dword v115,v112,s[40:43],s87 offen\n"
"v_mfma_f32_16x16x32_bf16 a[188:191],v[148:151],v[188:191],a[188:191]\n"
"s_waitcnt vmcnt(30)\n"
"s_barrier\n"
"v_mfma_f32_16x16x32_bf16 a[192:195],v[216:219],v[224:227],a[192:195]\n"
"ds_read_b128 v[152:155],v82 offset:12288\n"
"v_mfma_f32_16x16x32_bf16 a[196:199],v[220:223],v[224:227],a[196:199]\n"
"ds_read_b128 v[156:159],v82 offset:14336\n"
"v_mfma_f32_16x16x32_bf16 a[204:207],v[220:223],v[228:231],a[204:207]\n"
"ds_read_b64_tr_b16 v[160:161],v98 offset:0\n"
"v_mfma_f32_16x16x32_bf16 a[200:203],v[216:219],v[228:231],a[200:203]\n"
"ds_read_b64_tr_b16 v[162:163],v98 offset:1024\n"
"v_mfma_f32_16x16x32_bf16 a[208:211],v[216:219],v[232:235],a[208:211]\n"
"ds_read_b64_tr_b16 v[164:165],v99 offset:0\n"
"v_mfma_f32_16x16x32_bf16 a[212:215],v[220:223],v[232:235],a[212:215]\n"
"ds_read_b64_tr_b16 v[166:167],v99 offset:1024\n"
"v_mfma_f32_16x16x32_bf16 a[220:223],v[220:223],v[236:239],a[220:223]\n"
"ds_read_b64_tr_b16 v[168:169],v100 offset:0\n"
"v_mfma_f32_16x16x32_bf16 a[216:219],v[216:219],v[236:239],a[216:219]\n"
"ds_read_b64_tr_b16 v[170:171],v100 offset:1024\n"
"v_mfma_f32_16x16x32_bf16 a[224:227],v[216:219],v[240:243],a[224:227]\n"
"ds_read_b64_tr_b16 v[172:173],v101 offset:0\n"
"v_mfma_f32_16x16x32_bf16 a[228:231],v[220:223],v[240:243],a[228:231]\n"
"ds_read_b64_tr_b16 v[174:175],v101 offset:1024\n"
"v_mfma_f32_16x16x32_bf16 a[236:239],v[220:223],v[244:247],a[236:239]\n"
"ds_read_b64_tr_b16 v[176:177],v102 offset:0\n"
"v_mfma_f32_16x16x32_bf16 a[232:235],v[216:219],v[244:247],a[232:235]\n"
"ds_read_b64_tr_b16 v[178:179],v102 offset:1024\n"
"ds_read_b64_tr_b16 v[180:181],v103 offset:0\n"
"v_mfma_f32_16x16x32_bf16 a[240:243],v[216:219],v[248:251],a[240:243]\n"
"buffer_load_dwordx4 v[74:77],v112,s[24:27],s58 offen sc1\n"
"v_mfma_f32_16x16x32_bf16 a[244:247],v[220:223],v[248:251],a[244:247]\n"
"ds_read_b64_tr_b16 v[182:183],v103 offset:1024\n"
"v_mfma_f32_16x16x32_bf16 a[252:255],v[220:223],v[252:255],a[252:255]\n"
"ds_read_b64_tr_b16 v[184:185],v104 offset:0\n"
"v_mfma_f32_16x16x32_bf16 a[248:251],v[216:219],v[252:255],a[248:251]\n"
"ds_read_b64_tr_b16 v[186:187],v104 offset:1024\n"
"s_waitcnt lgkmcnt(10)\n"
"v_mfma_f32_16x16x32_bf16 a[192:195],v[152:155],v[160:163],a[192:195]\n"
"ds_read_b64_tr_b16 v[188:189],v105 offset:0\n"
"v_mfma_f32_16x16x32_bf16 a[196:199],v[156:159],v[160:163],a[196:199]\n"
"ds_read_b64_tr_b16 v[190:191],v105 offset:1024\n"
"v_mfma_f32_16x16x32_bf16 a[204:207],v[156:159],v[164:167],a[204:207]\n"
"ds_read_b128 v[128:131],v82 offset:0\n"
"v_mfma_f32_16x16x32_bf16 a[200:203],v[152:155],v[164:167],a[200:203]\n"
"ds_read_b128 v[192:195],v83 offset:0\n"
"s_waitcnt lgkmcnt(10)\n"
"v_mfma_f32_16x16x32_bf16 a[208:211],v[152:155],v[168:171],a[208:211]\n"
"ds_read_b64_tr_b16 v[224:225],v98 offset:16384\n"
"v_mfma_f32_16x16x32_bf16 a[212:215],v[156:159],v[168:171],a[212:215]\n"
"ds_read_b64_tr_b16 v[226:227],v98 offset:17408\n"
"v_mfma_f32_16x16x32_bf16 a[220:223],v[156:159],v[172:175],a[220:223]\n"
"ds_read_b64_tr_b16 v[228:229],v99 offset:16384\n"
"v_mfma_f32_16x16x32_bf16 a[216:219],v[152:155],v[172:175],a[216:219]\n"
"ds_read_b64_tr_b16 v[230:231],v99 offset:17408\n"
"s_waitcnt lgkmcnt(10)\n"
"v_mfma_f32_16x16x32_bf16 a[224:227],v[152:155],v[176:179],a[224:227]\n"
"ds_read_b64_tr_b16 v[232:233],v100 offset:16384\n"
"v_mfma_f32_16x16x32_bf16 a[228:231],v[156:159],v[176:179],a[228:231]\n"
"ds_read_b64_tr_b16 v[234:235],v100 offset:17408\n"
"v_mfma_f32_16x16x32_bf16 a[236:239],v[156:159],v[180:183],a[236:239]\n"
"ds_read_b64_tr_b16 v[236:237],v101 offset:16384\n"
"v_mfma_f32_16x16x32_bf16 a[232:235],v[152:155],v[180:183],a[232:235]\n"
"ds_read_b64_tr_b16 v[238:239],v101 offset:17408\n"
"s_waitcnt lgkmcnt(10)\n"
"v_mfma_f32_16x16x32_bf16 a[240:243],v[152:155],v[184:187],a[240:243]\n"
"ds_read_b64_tr_b16 v[240:241],v102 offset:16384\n"
"v_mfma_f32_16x16x32_bf16 a[244:247],v[156:159],v[184:187],a[244:247]\n"
"ds_read_b64_tr_b16 v[242:243],v102 offset:17408\n"
"v_mfma_f32_16x16x32_bf16 a[252:255],v[156:159],v[188:191],a[252:255]\n"
"ds_read_b64_tr_b16 v[244:245],v103 offset:16384\n"
"v_mfma_f32_16x16x32_bf16 a[248:251],v[152:155],v[188:191],a[248:251]\n"
"ds_read_b64_tr_b16 v[246:247],v103 offset:17408\n"
"s_waitcnt lgkmcnt(8)\n"
"v_mfma_f32_16x16x32_bf16 a[0:3],v[128:131],v[160:163],a[0:3]\n"
"ds_read_b64_tr_b16 v[248:249],v104 offset:16384\n"
"v_mfma_f32_16x16x32_bf16 a[0:3],v[192:195],v[224:227],a[0:3]\n"
"ds_read_b64_tr_b16 v[250:251],v104 offset:17408\n"
"v_mfma_f32_16x16x32_bf16 a[8:11],v[192:195],v[228:231],a[8:11]\n"
"ds_read_b64_tr_b16 v[252:253],v105 offset:16384\n"
"v_mfma_f32_16x16x32_bf16 a[8:11],v[128:131],v[164:167],a[8:11]\n"
"ds_read_b64_tr_b16 v[254:255],v105 offset:17408\n"
"s_waitcnt lgkmcnt(8)\n"
"v_mfma_f32_16x16x32_bf16 a[16:19],v[128:131],v[168:171],a[16:19]\n"
"ds_read_b128 v[132:135],v82 offset:2048\n"
"v_mfma_f32_16x16x32_bf16 a[16:19],v[192:195],v[232:235],a[16:19]\n"
"ds_read_b128 v[196:199],v83 offset:2048\n"
"v_mfma_f32_16x16x32_bf16 a[24:27],v[192:195],v[236:239],a[24:27]\n"
"ds_read_b128 v[136:139],v82 offset:4096\n"
"v_mfma_f32_16x16x32_bf16 a[24:27],v[128:131],v[172:175],a[24:27]\n"
"ds_read_b128 v[200:203],v83 offset:4096\n"
"s_waitcnt lgkmcnt(8)\n"
"v_mfma_f32_16x16x32_bf16 a[32:35],v[128:131],v[176:179],a[32:35]\n"
"ds_read_b128 v[140:143],v82 offset:6144\n"
"v_mfma_f32_16x16x32_bf16 a[32:35],v[192:195],v[240:243],a[32:35]\n"
"ds_read_b128 v[204:207],v83 offset:6144\n"
"v_mfma_f32_16x16x32_bf16 a[40:43],v[192:195],v[244:247],a[40:43]\n"
"ds_read_b128 v[144:147],v82 offset:8192\n"
"v_mfma_f32_16x16x32_bf16 a[40:43],v[128:131],v[180:183],a[40:43]\n"
"ds_read_b128 v[208:211],v83 offset:8192\n"
"s_waitcnt lgkmcnt(8)\n"
"v_mfma_f32_16x16x32_bf16 a[48:51],v[128:131],v[184:187],a[48:51]\n"
"ds_read_b128 v[148:151],v82 offset:10240\n"
"v_mfma_f32_16x16x32_bf16 a[48:51],v[192:195],v[248:251],a[48:51]\n"
"ds_read_b128 v[212:215],v83 offset:10240\n"
"v_mfma_f32_16x16x32_bf16 a[56:59],v[192:195],v[252:255],a[56:59]\n"
"ds_read_b128 v[216:219],v83 offset:12288\n"
"v_mfma_f32_16x16x32_bf16 a[56:59],v[128:131],v[188:191],a[56:59]\n"
"ds_read_b128 v[220:223],v83 offset:14336\n"
"s_waitcnt lgkmcnt(10)\n"
"v_mfma_f32_16x16x32_bf16 a[4:7],v[132:135],v[160:163],a[4:7]\n"
"v_mfma_f32_16x16x32_bf16 a[4:7],v[196:199],v[224:227],a[4:7]\n"
"v_mfma_f32_16x16x32_bf16 a[12:15],v[196:199],v[228:231],a[12:15]\n"
"v_mfma_f32_16x16x32_bf16 a[12:15],v[132:135],v[164:167],a[12:15]\n"
"v_mfma_f32_16x16x32_bf16 a[20:23],v[132:135],v[168:171],a[20:23]\n"
"s_waitcnt lgkmcnt(0)\n"
"s_barrier\n"
"v_mfma_f32_16x16x32_bf16 a[20:23],v[196:199],v[232:235],a[20:23]\n"
"s_mov_b32 s77,3670016\n"
"s_add_u32 m0,s78,98304\n"
"v_mfma_f32_16x16x32_bf16 a[28:31],v[196:199],v[236:239],a[28:31]\n"
"buffer_load_dwordx4 v110,s[20:23],s77 offen offset:0 lds\n"
"v_mfma_f32_16x16x32_bf16 a[28:31],v[132:135],v[172:175],a[28:31]\n"
"v_mfma_f32_16x16x32_bf16 a[36:39],v[132:135],v[176:179],a[36:39]\n"
"s_load_dwordx2 s[48:49],s[0:1],56\n"
"v_mfma_f32_16x16x32_bf16 a[36:39],v[196:199],v[240:243],a[36:39]\n"
"v_mfma_f32_16x16x32_bf16 a[44:47],v[196:199],v[244:247],a[44:47]\n"
"buffer_load_dwordx4 v111,s[20:23],s77 offen offset:1024 lds\n"
"v_mfma_f32_16x16x32_bf16 a[44:47],v[132:135],v[180:183],a[44:47]\n"
"v_mfma_f32_16x16x32_bf16 a[52:55],v[132:135],v[184:187],a[52:55]\n"
"v_mfma_f32_16x16x32_bf16 a[52:55],v[196:199],v[248:251],a[52:55]\n"
"v_mfma_f32_16x16x32_bf16 a[60:63],v[196:199],v[252:255],a[60:63]\n"
"buffer_load_dwordx4 v113,s[20:23],s77 offen offset:2048 lds\n"
"v_mfma_f32_16x16x32_bf16 a[60:63],v[132:135],v[188:191],a[60:63]\n"
"v_mfma_f32_16x16x32_bf16 a[64:67],v[136:139],v[160:163],a[64:67]\n"
"v_mfma_f32_16x16x32_bf16 a[64:67],v[200:203],v[224:227],a[64:67]\n"
"v_mfma_f32_16x16x32_bf16 a[72:75],v[200:203],v[228:231],a[72:75]\n"
"buffer_load_dwordx4 v114,s[20:23],s77 offen offset:3072 lds\n"
"v_mfma_f32_16x16x32_bf16 a[72:75],v[136:139],v[164:167],a[72:75]\n"
"v_mfma_f32_16x16x32_bf16 a[80:83],v[136:139],v[168:171],a[80:83]\n"
"v_mfma_f32_16x16x32_bf16 a[80:83],v[200:203],v[232:235],a[80:83]\n"
"s_mov_b32 s77,3932160\n"
"s_add_u32 m0,s78,114688\n"
"v_mfma_f32_16x16x32_bf16 a[88:91],v[200:203],v[236:239],a[88:91]\n"
"buffer_load_dwordx4 v110,s[20:23],s77 offen offset:0 lds\n"
"v_mfma_f32_16x16x32_bf16 a[88:91],v[136:139],v[172:175],a[88:91]\n"
"v_mfma_f32_16x16x32_bf16 a[96:99],v[136:139],v[176:179],a[96:99]\n"
"v_mfma_f32_16x16x32_bf16 a[96:99],v[200:203],v[240:243],a[96:99]\n"
"v_mfma_f32_16x16x32_bf16 a[104:107],v[200:203],v[244:247],a[104:107]\n"
"buffer_load_dwordx4 v111,s[20:23],s77 offen offset:1024 lds\n"
"v_mfma_f32_16x16x32_bf16 a[104:107],v[136:139],v[180:183],a[104:107]\n"
"v_mfma_f32_16x16x32_bf16 a[112:115],v[136:139],v[184:187],a[112:115]\n"
"v_mfma_f32_16x16x32_bf16 a[112:115],v[200:203],v[248:251],a[112:115]\n"
"v_mfma_f32_16x16x32_bf16 a[120:123],v[200:203],v[252:255],a[120:123]\n"
"buffer_load_dwordx4 v113,s[20:23],s77 offen offset:2048 lds\n"
"v_mfma_f32_16x16x32_bf16 a[120:123],v[136:139],v[188:191],a[120:123]\n"
"s_mov_b32 s76,s55\n"
"s_waitcnt vmcnt(7)\n"
"v_mfma_f32_16x16x32_bf16 a[68:71],v[140:143],v[160:163],a[68:71]\n"
"v_xor_b32 v116,s80,v74\n"
"v_xor_b32 v117,s81,v75\n"
"v_mfma_f32_16x16x32_bf16 a[68:71],v[204:207],v[224:227],a[68:71]\n"
"v_or_b32 v116,v116,v117\n"
"v_xor_b32 v117,s80,v76\n"
"v_mfma_f32_16x16x32_bf16 a[76:79],v[204:207],v[228:231],a[76:79]\n"
"buffer_load_dwordx4 v114,s[20:23],s77 offen offset:3072 lds\n"
"v_mfma_f32_16x16x32_bf16 a[76:79],v[140:143],v[164:167],a[76:79]\n"
"v_or_b32 v116,v116,v117\n"
"v_xor_b32 v117,s81,v77\n"
"v_mfma_f32_16x16x32_bf16 a[84:87],v[140:143],v[168:171],a[84:87]\n"
"v_or_b32 v116,v116,v117\n"
"v_cmp_ne_u32 vcc,0,v116\n"
"v_mfma_f32_16x16x32_bf16 a[84:87],v[204:207],v[232:235],a[84:87]\n"
"s_cbranch_vccnz .Lfk_xn1_s\n"
".Lfk_xn1_ok:\n"
"v_mfma_f32_16x16x32_bf16 a[92:95],v[204:207],v[236:239],a[92:95]\n"
"s_add_u32 m0,s78,65536\n"
"v_mfma_f32_16x16x32_bf16 a[92:95],v[140:143],v[172:175],a[92:95]\n"
"buffer_load_dwordx4 v112,s[24:27],s76 offen offset:0 sc1 lds\n"
"v_mfma_f32_16x16x32_bf16 a[100:103],v[140:143],v[176:179],a[100:103]\n"
"s_waitcnt lgkmcnt(0)\n"
"s_lshl2_add_u32 s88,s100,s52\n"
"v_mfma_f32_16x16x32_bf16 a[100:103],v[204:207],v[240:243],a[100:103]\n"
"s_mul_i32 s88,s88,7168\n"
"s_add_u32 s88,s88,0\n"
"v_mfma_f32_16x16x32_bf16 a[108:111],v[204:207],v[244:247],a[108:111]\n"
"s_add_u32 s48,s48,s88\n"
"s_addc_u32 s49,s49,0\n"
"s_mov_b32 s50,3584\n"
"v_mfma_f32_16x16x32_bf16 a[108:111],v[140:143],v[180:183],a[108:111]\n"
"s_mov_b32 s51,0x20000\n"
"v_lshlrev_b32 v8,3,v112\n"
"v_mfma_f32_16x16x32_bf16 a[116:119],v[140:143],v[184:187],a[116:119]\n"
"buffer_load_dwordx4 v112,s[24:27],s76 offen offset:1024 sc1 lds\n"
"v_mfma_f32_16x16x32_bf16 a[116:119],v[204:207],v[248:251],a[116:119]\n"
"v_mfma_f32_16x16x32_bf16 a[124:127],v[204:207],v[252:255],a[124:127]\n"
"v_mfma_f32_16x16x32_bf16 a[124:127],v[140:143],v[188:191],a[124:127]\n"
"v_mfma_f32_16x16x32_bf16 a[128:131],v[144:147],v[160:163],a[128:131]\n"
"v_mfma_f32_16x16x32_bf16 a[128:131],v[208:211],v[224:227],a[128:131]\n"
"buffer_load_dwordx4 v112,s[24:27],s76 offen offset:2048 sc1 lds\n"
"v_mfma_f32_16x16x32_bf16 a[136:139],v[208:211],v[228:231],a[136:139]\n"
"v_mfma_f32_16x16x32_bf16 a[136:139],v[144:147],v[164:167],a[136:139]\n"
"v_mfma_f32_16x16x32_bf16 a[144:147],v[144:147],v[168:171],a[144:147]\n"
"v_mfma_f32_16x16x32_bf16 a[144:147],v[208:211],v[232:235],a[144:147]\n"
"buffer_load_dwordx4 v112,s[24:27],s76 offen offset:3072 sc1 lds\n"
"v_mfma_f32_16x16x32_bf16 a[152:155],v[208:211],v[236:239],a[152:155]\n"
"s_xor_b32 s76,s76,16384\n"
"v_mfma_f32_16x16x32_bf16 a[152:155],v[144:147],v[172:175],a[152:155]\n"
"v_mfma_f32_16x16x32_bf16 a[160:163],v[144:147],v[176:179],a[160:163]\n"
"v_mfma_f32_16x16x32_bf16 a[160:163],v[208:211],v[240:243],a[160:163]\n"
"v_mfma_f32_16x16x32_bf16 a[168:171],v[208:211],v[244:247],a[168:171]\n"
"s_add_u32 m0,s78,81920\n"
"v_mfma_f32_16x16x32_bf16 a[168:171],v[144:147],v[180:183],a[168:171]\n"
"buffer_load_dwordx4 v112,s[24:27],s76 offen offset:0 sc1 lds\n"
"v_mfma_f32_16x16x32_bf16 a[176:179],v[144:147],v[184:187],a[176:179]\n"
"v_mfma_f32_16x16x32_bf16 a[176:179],v[208:211],v[248:251],a[176:179]\n"
"v_mfma_f32_16x16x32_bf16 a[184:187],v[208:211],v[252:255],a[184:187]\n"
"v_mfma_f32_16x16x32_bf16 a[184:187],v[144:147],v[188:191],a[184:187]\n"
"v_mfma_f32_16x16x32_bf16 a[132:135],v[148:151],v[160:163],a[132:135]\n"
"buffer_load_dwordx4 v112,s[24:27],s76 offen offset:1024 sc1 lds\n"
"v_mfma_f32_16x16x32_bf16 a[132:135],v[212:215],v[224:227],a[132:135]\n"
"v_mfma_f32_16x16x32_bf16 a[140:143],v[212:215],v[228:231],a[140:143]\n"
"v_mfma_f32_16x16x32_bf16 a[140:143],v[148:151],v[164:167],a[140:143]\n"
"v_mfma_f32_16x16x32_bf16 a[148:151],v[148:151],v[168:171],a[148:151]\n"
"v_mfma_f32_16x16x32_bf16 a[148:151],v[212:215],v[232:235],a[148:151]\n"
"buffer_load_dwordx4 v112,s[24:27],s76 offen offset:2048 sc1 lds\n"
"v_mfma_f32_16x16x32_bf16 a[156:159],v[212:215],v[236:239],a[156:159]\n"
"v_mfma_f32_16x16x32_bf16 a[156:159],v[148:151],v[172:175],a[156:159]\n"
"v_mfma_f32_16x16x32_bf16 a[164:167],v[148:151],v[176:179],a[164:167]\n"
"v_mfma_f32_16x16x32_bf16 a[164:167],v[212:215],v[240:243],a[164:167]\n"
"buffer_load_dwordx4 v112,s[24:27],s76 offen offset:3072 sc1 lds\n"
"v_mfma_f32_16x16x32_bf16 a[172:175],v[212:215],v[244:247],a[172:175]\n"
"s_xor_b32 s76,s76,49152\n"
"v_mfma_f32_16x16x32_bf16 a[172:175],v[148:151],v[180:183],a[172:175]\n"
"v_mfma_f32_16x16x32_bf16 a[180:183],v[148:151],v[184:187],a[180:183]\n"
"buffer_load_dword v115,v8,s[48:51],0 offen\n"
"v_mfma_f32_16x16x32_bf16 a[180:183],v[212:215],v[248:251],a[180:183]\n"
"v_mfma_f32_16x16x32_bf16 a[188:191],v[212:215],v[252:255],a[188:191]\n"
"v_mfma_f32_16x16x32_bf16 a[188:191],v[148:151],v[188:191],a[188:191]\n"
"s_barrier\n"
"v_mfma_f32_16x16x32_bf16 a[192:195],v[216:219],v[224:227],a[192:195]\n"
"ds_read_b128 v[152:155],v80 offset:12288\n"
"v_mfma_f32_16x16x32_bf16 a[196:199],v[220:223],v[224:227],a[196:199]\n"
"ds_read_b128 v[156:159],v80 offset:14336\n"
"v_mfma_f32_16x16x32_bf16 a[204:207],v[220:223],v[228:231],a[204:207]\n"
"ds_read_b64_tr_b16 v[160:161],v98 offset:49152\n"
"v_mfma_f32_16x16x32_bf16 a[200:203],v[216:219],v[228:231],a[200:203]\n"
"ds_read_b64_tr_b16 v[162:163],v98 offset:50176\n"
"v_mfma_f32_16x16x32_bf16 a[208:211],v[216:219],v[232:235],a[208:211]\n"
"ds_read_b64_tr_b16 v[164:165],v99 offset:49152\n"
"v_mfma_f32_16x16x32_bf16 a[212:215],v[220:223],v[232:235],a[212:215]\n"
"ds_read_b64_tr_b16 v[166:167],v99 offset:50176\n"
"v_mfma_f32_16x16x32_bf16 a[220:223],v[220:223],v[236:239],a[220:223]\n"
"ds_read_b64_tr_b16 v[168:169],v100 offset:49152\n"
"v_mfma_f32_16x16x32_bf16 a[216:219],v[216:219],v[236:239],a[216:219]\n"
"ds_read_b64_tr_b16 v[170:171],v100 offset:50176\n"
"v_mfma_f32_16x16x32_bf16 a[224:227],v[216:219],v[240:243],a[224:227]\n"
"ds_read_b64_tr_b16 v[172:173],v101 offset:49152\n"
"v_mfma_f32_16x16x32_bf16 a[228:231],v[220:223],v[240:243],a[228:231]\n"
"ds_read_b64_tr_b16 v[174:175],v101 offset:50176\n"
"v_mfma_f32_16x16x32_bf16 a[236:239],v[220:223],v[244:247],a[236:239]\n"
"ds_read_b64_tr_b16 v[176:177],v102 offset:49152\n"
"v_mfma_f32_16x16x32_bf16 a[232:235],v[216:219],v[244:247],a[232:235]\n"
"ds_read_b64_tr_b16 v[178:179],v102 offset:50176\n"
"v_mfma_f32_16x16x32_bf16 a[240:243],v[216:219],v[248:251],a[240:243]\n"
"ds_read_b64_tr_b16 v[180:181],v103 offset:49152\n"
"v_mfma_f32_16x16x32_bf16 a[244:247],v[220:223],v[248:251],a[244:247]\n"
"ds_read_b64_tr_b16 v[182:183],v103 offset:50176\n"
"ds_read_b64_tr_b16 v[184:185],v104 offset:49152\n"
"v_mfma_f32_16x16x32_bf16 a[252:255],v[220:223],v[252:255],a[252:255]\n"
"v_mov_b32 v110,v72\n"
"v_mov_b32 v111,v73\n"
"v_mfma_f32_16x16x32_bf16 a[248:251],v[216:219],v[252:255],a[248:251]\n"
"ds_read_b64_tr_b16 v[186:187],v104 offset:50176\n"
"s_waitcnt lgkmcnt(10)\n"
"v_mfma_f32_16x16x32_bf16 a[192:195],v[152:155],v[160:163],a[192:195]\n"
"ds_read_b64_tr_b16 v[188:189],v105 offset:49152\n"
"v_mfma_f32_16x16x32_bf16 a[196:199],v[156:159],v[160:163],a[196:199]\n"
"ds_read_b64_tr_b16 v[190:191],v105 offset:50176\n"
"v_mfma_f32_16x16x32_bf16 a[204:207],v[156:159],v[164:167],a[204:207]\n"
"ds_read_b128 v[128:131],v80 offset:0\n"
"v_mfma_f32_16x16x32_bf16 a[200:203],v[152:155],v[164:167],a[200:203]\n"
"ds_read_b128 v[192:195],v81 offset:0\n"
"s_waitcnt lgkmcnt(10)\n"
"v_mfma_f32_16x16x32_bf16 a[208:211],v[152:155],v[168:171],a[208:211]\n"
"ds_read_b64_tr_b16 v[224:225],v98 offset:32768\n"
"v_mfma_f32_16x16x32_bf16 a[212:215],v[156:159],v[168:171],a[212:215]\n"
"ds_read_b64_tr_b16 v[226:227],v98 offset:33792\n"
"v_mfma_f32_16x16x32_bf16 a[220:223],v[156:159],v[172:175],a[220:223]\n"
"ds_read_b64_tr_b16 v[228:229],v99 offset:32768\n"
"v_mfma_f32_16x16x32_bf16 a[216:219],v[152:155],v[172:175],a[216:219]\n"
"ds_read_b64_tr_b16 v[230:231],v99 offset:33792\n"
"s_waitcnt lgkmcnt(10)\n"
"v_mfma_f32_16x16x32_bf16 a[224:227],v[152:155],v[176:179],a[224:227]\n"
"ds_read_b64_tr_b16 v[232:233],v100 offset:32768\n"
"v_mfma_f32_16x16x32_bf16 a[228:231],v[156:159],v[176:179],a[228:231]\n"
"ds_read_b64_tr_b16 v[234:235],v100 offset:33792\n"
"v_mfma_f32_16x16x32_bf16 a[236:239],v[156:159],v[180:183],a[236:239]\n"
"ds_read_b64_tr_b16 v[236:237],v101 offset:32768\n"
"v_mfma_f32_16x16x32_bf16 a[232:235],v[152:155],v[180:183],a[232:235]\n"
"ds_read_b64_tr_b16 v[238:239],v101 offset:33792\n"
"s_waitcnt lgkmcnt(10)\n"
"v_mfma_f32_16x16x32_bf16 a[240:243],v[152:155],v[184:187],a[240:243]\n"
"ds_read_b64_tr_b16 v[240:241],v102 offset:32768\n"
"v_mfma_f32_16x16x32_bf16 a[244:247],v[156:159],v[184:187],a[244:247]\n"
"ds_read_b64_tr_b16 v[242:243],v102 offset:33792\n"
"v_mfma_f32_16x16x32_bf16 a[252:255],v[156:159],v[188:191],a[252:255]\n"
"ds_read_b64_tr_b16 v[244:245],v103 offset:32768\n"
"v_mfma_f32_16x16x32_bf16 a[248:251],v[152:155],v[188:191],a[248:251]\n"
"ds_read_b64_tr_b16 v[246:247],v103 offset:33792\n"
"s_waitcnt lgkmcnt(8)\n"
"v_mfma_f32_16x16x32_bf16 a[0:3],v[128:131],v[160:163],a[0:3]\n"
"ds_read_b64_tr_b16 v[248:249],v104 offset:32768\n"
"v_mfma_f32_16x16x32_bf16 a[0:3],v[192:195],v[224:227],a[0:3]\n"
"ds_read_b64_tr_b16 v[250:251],v104 offset:33792\n"
"v_mfma_f32_16x16x32_bf16 a[8:11],v[192:195],v[228:231],a[8:11]\n"
"ds_read_b64_tr_b16 v[252:253],v105 offset:32768\n"
"v_mfma_f32_16x16x32_bf16 a[8:11],v[128:131],v[164:167],a[8:11]\n"
"ds_read_b64_tr_b16 v[254:255],v105 offset:33792\n"
"s_waitcnt lgkmcnt(8)\n"
"v_mfma_f32_16x16x32_bf16 a[16:19],v[128:131],v[168:171],a[16:19]\n"
"ds_read_b128 v[132:135],v80 offset:2048\n"
"v_mfma_f32_16x16x32_bf16 a[16:19],v[192:195],v[232:235],a[16:19]\n"
"ds_read_b128 v[196:199],v81 offset:2048\n"
"v_mfma_f32_16x16x32_bf16 a[24:27],v[192:195],v[236:239],a[24:27]\n"
"ds_read_b128 v[136:139],v80 offset:4096\n"
"v_mfma_f32_16x16x32_bf16 a[24:27],v[128:131],v[172:175],a[24:27]\n"
"ds_read_b128 v[200:203],v81 offset:4096\n"
"s_waitcnt lgkmcnt(8)\n"
"v_mfma_f32_16x16x32_bf16 a[32:35],v[128:131],v[176:179],a[32:35]\n"
"ds_read_b128 v[140:143],v80 offset:6144\n"
"v_mfma_f32_16x16x32_bf16 a[32:35],v[192:195],v[240:243],a[32:35]\n"
"ds_read_b128 v[204:207],v81 offset:6144\n"
"v_mfma_f32_16x16x32_bf16 a[40:43],v[192:195],v[244:247],a[40:43]\n"
"ds_read_b128 v[144:147],v80 offset:8192\n"
"v_mfma_f32_16x16x32_bf16 a[40:43],v[128:131],v[180:183],a[40:43]\n"
"ds_read_b128 v[208:211],v81 offset:8192\n"
"s_waitcnt lgkmcnt(8)\n"
"v_mfma_f32_16x16x32_bf16 a[48:51],v[128:131],v[184:187],a[48:51]\n"
"ds_read_b128 v[148:151],v80 offset:10240\n"
"v_mfma_f32_16x16x32_bf16 a[48:51],v[192:195],v[248:251],a[48:51]\n"
"ds_read_b128 v[212:215],v81 offset:10240\n"
"v_mfma_f32_16x16x32_bf16 a[56:59],v[192:195],v[252:255],a[56:59]\n"
"ds_read_b128 v[216:219],v81 offset:12288\n"
"v_mfma_f32_16x16x32_bf16 a[56:59],v[128:131],v[188:191],a[56:59]\n"
"ds_read_b128 v[220:223],v81 offset:14336\n"
"s_waitcnt lgkmcnt(10)\n"
"v_mfma_f32_16x16x32_bf16 a[4:7],v[132:135],v[160:163],a[4:7]\n"
"v_mfma_f32_16x16x32_bf16 a[4:7],v[196:199],v[224:227],a[4:7]\n"
"v_mfma_f32_16x16x32_bf16 a[12:15],v[196:199],v[228:231],a[12:15]\n"
"v_mfma_f32_16x16x32_bf16 a[12:15],v[132:135],v[164:167],a[12:15]\n"
"v_mfma_f32_16x16x32_bf16 a[20:23],v[132:135],v[168:171],a[20:23]\n"
"s_waitcnt lgkmcnt(0)\n"
"s_barrier\n"
"v_mfma_f32_16x16x32_bf16 a[20:23],v[196:199],v[232:235],a[20:23]\n"
"s_add_u32 m0,s85,147456\n"
"v_mfma_f32_16x16x32_bf16 a[28:31],v[196:199],v[236:239],a[28:31]\n"
"buffer_load_dwordx4 v110,s[28:31],s86 offen lds\n"
"v_mfma_f32_16x16x32_bf16 a[28:31],v[132:135],v[172:175],a[28:31]\n"
"v_mfma_f32_16x16x32_bf16 a[36:39],v[132:135],v[176:179],a[36:39]\n"
"s_load_dwordx2 s[48:49],s[0:1],56\n"
"v_mfma_f32_16x16x32_bf16 a[36:39],v[196:199],v[240:243],a[36:39]\n"
"s_add_u32 m0,s85,148480\n"
"v_mfma_f32_16x16x32_bf16 a[44:47],v[196:199],v[244:247],a[44:47]\n"
"buffer_load_dwordx4 v111,s[28:31],s86 offen lds\n"
"v_mfma_f32_16x16x32_bf16 a[44:47],v[132:135],v[180:183],a[44:47]\n"
"s_xor_b32 s86,s86,262144\n"
"v_mfma_f32_16x16x32_bf16 a[52:55],v[132:135],v[184:187],a[52:55]\n"
"v_mfma_f32_16x16x32_bf16 a[52:55],v[196:199],v[248:251],a[52:55]\n"
"s_add_u32 m0,s85,155648\n"
"v_mfma_f32_16x16x32_bf16 a[60:63],v[196:199],v[252:255],a[60:63]\n"
"buffer_load_dwordx4 v110,s[28:31],s86 offen lds\n"
"v_mfma_f32_16x16x32_bf16 a[60:63],v[132:135],v[188:191],a[60:63]\n"
"v_mfma_f32_16x16x32_bf16 a[64:67],v[136:139],v[160:163],a[64:67]\n"
"v_mfma_f32_16x16x32_bf16 a[64:67],v[200:203],v[224:227],a[64:67]\n"
"s_add_u32 m0,s85,156672\n"
"v_mfma_f32_16x16x32_bf16 a[72:75],v[200:203],v[228:231],a[72:75]\n"
"buffer_load_dwordx4 v111,s[28:31],s86 offen lds\n"
"v_mfma_f32_16x16x32_bf16 a[72:75],v[136:139],v[164:167],a[72:75]\n"
"s_xor_b32 s86,s86,786432\n"
"v_mfma_f32_16x16x32_bf16 a[80:83],v[136:139],v[168:171],a[80:83]\n"
"v_mfma_f32_16x16x32_bf16 a[80:83],v[200:203],v[232:235],a[80:83]\n"
"s_add_u32 m0,s85,131072\n"
"v_mfma_f32_16x16x32_bf16 a[88:91],v[200:203],v[236:239],a[88:91]\n"
"buffer_load_dwordx4 v110,s[28:31],s86 offen lds\n"
"v_mfma_f32_16x16x32_bf16 a[88:91],v[136:139],v[172:175],a[88:91]\n"
"v_mfma_f32_16x16x32_bf16 a[96:99],v[136:139],v[176:179],a[96:99]\n"
"v_mfma_f32_16x16x32_bf16 a[96:99],v[200:203],v[240:243],a[96:99]\n"
"s_add_u32 m0,s85,132096\n"
"v_mfma_f32_16x16x32_bf16 a[104:107],v[200:203],v[244:247],a[104:107]\n"
"buffer_load_dwordx4 v111,s[28:31],s86 offen lds\n"
"v_mfma_f32_16x16x32_bf16 a[104:107],v[136:139],v[180:183],a[104:107]\n"
"s_xor_b32 s86,s86,262144\n"
"v_mfma_f32_16x16x32_bf16 a[112:115],v[136:139],v[184:187],a[112:115]\n"
"v_mfma_f32_16x16x32_bf16 a[112:115],v[200:203],v[248:251],a[112:115]\n"
"v_mfma_f32_16x16x32_bf16 a[120:123],v[200:203],v[252:255],a[120:123]\n"
"v_mfma_f32_16x16x32_bf16 a[120:123],v[136:139],v[188:191],a[120:123]\n"
"v_mfma_f32_16x16x32_bf16 a[68:71],v[140:143],v[160:163],a[68:71]\n"
"v_mfma_f32_16x16x32_bf16 a[68:71],v[204:207],v[224:227],a[68:71]\n"
"s_add_u32 m0,s85,139264\n"
"v_mfma_f32_16x16x32_bf16 a[76:79],v[204:207],v[228:231],a[76:79]\n"
"buffer_load_dwordx4 v110,s[28:31],s86 offen lds\n"
"v_mfma_f32_16x16x32_bf16 a[76:79],v[140:143],v[164:167],a[76:79]\n"
"v_mfma_f32_16x16x32_bf16 a[84:87],v[140:143],v[168:171],a[84:87]\n"
"v_mfma_f32_16x16x32_bf16 a[84:87],v[204:207],v[232:235],a[84:87]\n"
"s_add_u32 m0,s85,140288\n"
"v_mfma_f32_16x16x32_bf16 a[92:95],v[204:207],v[236:239],a[92:95]\n"
"buffer_load_dwordx4 v111,s[28:31],s86 offen lds\n"
"v_mfma_f32_16x16x32_bf16 a[92:95],v[140:143],v[172:175],a[92:95]\n"
"s_xor_b32 s86,s86,1835008\n"
"v_mfma_f32_16x16x32_bf16 a[100:103],v[140:143],v[176:179],a[100:103]\n"
"s_waitcnt lgkmcnt(0)\n"
"s_lshl2_add_u32 s88,s100,s52\n"
"v_mfma_f32_16x16x32_bf16 a[100:103],v[204:207],v[240:243],a[100:103]\n"
"s_mul_i32 s88,s88,7168\n"
"s_add_u32 m0,s78,0\n"
"v_mfma_f32_16x16x32_bf16 a[108:111],v[204:207],v[244:247],a[108:111]\n"
"buffer_load_dwordx4 v112,s[24:27],s76 offen offset:0 sc1 lds\n"
"v_mfma_f32_16x16x32_bf16 a[108:111],v[140:143],v[180:183],a[108:111]\n"
"s_add_u32 s88,s88,3584\n"
"s_add_u32 s48,s48,s88\n"
"s_addc_u32 s49,s49,0\n"
"v_mfma_f32_16x16x32_bf16 a[116:119],v[140:143],v[184:187],a[116:119]\n"
"s_mov_b32 s50,3584\n"
"s_mov_b32 s51,0x20000\n"
"v_mfma_f32_16x16x32_bf16 a[116:119],v[204:207],v[248:251],a[116:119]\n"
"v_lshlrev_b32 v8,3,v112\n"
"v_mfma_f32_16x16x32_bf16 a[124:127],v[204:207],v[252:255],a[124:127]\n"
"buffer_load_dwordx4 v112,s[24:27],s76 offen offset:1024 sc1 lds\n"
"v_mfma_f32_16x16x32_bf16 a[124:127],v[140:143],v[188:191],a[124:127]\n"
"v_mfma_f32_16x16x32_bf16 a[128:131],v[144:147],v[160:163],a[128:131]\n"
"v_mfma_f32_16x16x32_bf16 a[128:131],v[208:211],v[224:227],a[128:131]\n"
"v_mfma_f32_16x16x32_bf16 a[136:139],v[208:211],v[228:231],a[136:139]\n"
"buffer_load_dwordx4 v112,s[24:27],s76 offen offset:2048 sc1 lds\n"
"v_mfma_f32_16x16x32_bf16 a[136:139],v[144:147],v[164:167],a[136:139]\n"
"v_mfma_f32_16x16x32_bf16 a[144:147],v[144:147],v[168:171],a[144:147]\n"
"v_mfma_f32_16x16x32_bf16 a[144:147],v[208:211],v[232:235],a[144:147]\n"
"buffer_load_dwordx4 v112,s[24:27],s76 offen offset:3072 sc1 lds\n"
"v_mfma_f32_16x16x32_bf16 a[152:155],v[208:211],v[236:239],a[152:155]\n"
"s_xor_b32 s76,s76,16384\n"
"v_mfma_f32_16x16x32_bf16 a[152:155],v[144:147],v[172:175],a[152:155]\n"
"v_mfma_f32_16x16x32_bf16 a[160:163],v[144:147],v[176:179],a[160:163]\n"
"v_mfma_f32_16x16x32_bf16 a[160:163],v[208:211],v[240:243],a[160:163]\n"
"s_add_u32 m0,s78,16384\n"
"v_mfma_f32_16x16x32_bf16 a[168:171],v[208:211],v[244:247],a[168:171]\n"
"buffer_load_dwordx4 v112,s[24:27],s76 offen offset:0 sc1 lds\n"
"v_mfma_f32_16x16x32_bf16 a[168:171],v[144:147],v[180:183],a[168:171]\n"
"v_mfma_f32_16x16x32_bf16 a[176:179],v[144:147],v[184:187],a[176:179]\n"
"v_mfma_f32_16x16x32_bf16 a[176:179],v[208:211],v[248:251],a[176:179]\n"
"v_mfma_f32_16x16x32_bf16 a[184:187],v[208:211],v[252:255],a[184:187]\n"
"buffer_load_dwordx4 v112,s[24:27],s76 offen offset:1024 sc1 lds\n"
"v_mfma_f32_16x16x32_bf16 a[184:187],v[144:147],v[188:191],a[184:187]\n"
"v_mfma_f32_16x16x32_bf16 a[132:135],v[148:151],v[160:163],a[132:135]\n"
"v_mfma_f32_16x16x32_bf16 a[132:135],v[212:215],v[224:227],a[132:135]\n"
"v_mfma_f32_16x16x32_bf16 a[140:143],v[212:215],v[228:231],a[140:143]\n"
"buffer_load_dwordx4 v112,s[24:27],s76 offen offset:2048 sc1 lds\n"
"v_mfma_f32_16x16x32_bf16 a[140:143],v[148:151],v[164:167],a[140:143]\n"
"v_mfma_f32_16x16x32_bf16 a[148:151],v[148:151],v[168:171],a[148:151]\n"
"v_mfma_f32_16x16x32_bf16 a[148:151],v[212:215],v[232:235],a[148:151]\n"
"buffer_load_dwordx4 v112,s[24:27],s76 offen offset:3072 sc1 lds\n"
"v_mfma_f32_16x16x32_bf16 a[156:159],v[212:215],v[236:239],a[156:159]\n"
"s_xor_b32 s76,s76,114688\n"
"v_mfma_f32_16x16x32_bf16 a[156:159],v[148:151],v[172:175],a[156:159]\n"
"v_mfma_f32_16x16x32_bf16 a[164:167],v[148:151],v[176:179],a[164:167]\n"
"buffer_load_dword v115,v8,s[48:51],0 offen\n"
"v_mfma_f32_16x16x32_bf16 a[164:167],v[212:215],v[240:243],a[164:167]\n"
"v_mfma_f32_16x16x32_bf16 a[172:175],v[212:215],v[244:247],a[172:175]\n"
"v_mfma_f32_16x16x32_bf16 a[172:175],v[148:151],v[180:183],a[172:175]\n"
"v_mfma_f32_16x16x32_bf16 a[180:183],v[148:151],v[184:187],a[180:183]\n"
"v_mfma_f32_16x16x32_bf16 a[180:183],v[212:215],v[248:251],a[180:183]\n"
"v_mfma_f32_16x16x32_bf16 a[188:191],v[212:215],v[252:255],a[188:191]\n"
"v_mfma_f32_16x16x32_bf16 a[188:191],v[148:151],v[188:191],a[188:191]\n"
"s_waitcnt vmcnt(26)\n"
"s_barrier\n"
"v_mfma_f32_16x16x32_bf16 a[192:195],v[216:219],v[224:227],a[192:195]\n"
"ds_read_b128 v[152:155],v80 offset:45056\n"
"v_mfma_f32_16x16x32_bf16 a[196:199],v[220:223],v[224:227],a[196:199]\n"
"ds_read_b128 v[156:159],v80 offset:47104\n"
"v_mfma_f32_16x16x32_bf16 a[204:207],v[220:223],v[228:231],a[204:207]\n"
"ds_read_b64_tr_b16 v[160:161],v98 offset:0\n"
"v_mfma_f32_16x16x32_bf16 a[200:203],v[216:219],v[228:231],a[200:203]\n"
"ds_read_b64_tr_b16 v[162:163],v98 offset:1024\n"
"v_mfma_f32_16x16x32_bf16 a[208:211],v[216:219],v[232:235],a[208:211]\n"
"ds_read_b64_tr_b16 v[164:165],v99 offset:0\n"
"v_mfma_f32_16x16x32_bf16 a[212:215],v[220:223],v[232:235],a[212:215]\n"
"ds_read_b64_tr_b16 v[166:167],v99 offset:1024\n"
"v_mfma_f32_16x16x32_bf16 a[220:223],v[220:223],v[236:239],a[220:223]\n"
"ds_read_b64_tr_b16 v[168:169],v100 offset:0\n"
"v_mfma_f32_16x16x32_bf16 a[216:219],v[216:219],v[236:239],a[216:219]\n"
"ds_read_b64_tr_b16 v[170:171],v100 offset:1024\n"
"v_mfma_f32_16x16x32_bf16 a[224:227],v[216:219],v[240:243],a[224:227]\n"
"ds_read_b64_tr_b16 v[172:173],v101 offset:0\n"
"v_mfma_f32_16x16x32_bf16 a[228:231],v[220:223],v[240:243],a[228:231]\n"
"ds_read_b64_tr_b16 v[174:175],v101 offset:1024\n"
"v_mfma_f32_16x16x32_bf16 a[236:239],v[220:223],v[244:247],a[236:239]\n"
"ds_read_b64_tr_b16 v[176:177],v102 offset:0\n"
"v_mfma_f32_16x16x32_bf16 a[232:235],v[216:219],v[244:247],a[232:235]\n"
"ds_read_b64_tr_b16 v[178:179],v102 offset:1024\n"
"v_mfma_f32_16x16x32_bf16 a[240:243],v[216:219],v[248:251],a[240:243]\n"
"ds_read_b64_tr_b16 v[180:181],v103 offset:0\n"
"v_mfma_f32_16x16x32_bf16 a[244:247],v[220:223],v[248:251],a[244:247]\n"
"ds_read_b64_tr_b16 v[182:183],v103 offset:1024\n"
"v_mfma_f32_16x16x32_bf16 a[252:255],v[220:223],v[252:255],a[252:255]\n"
"ds_read_b64_tr_b16 v[184:185],v104 offset:0\n"
"v_mfma_f32_16x16x32_bf16 a[248:251],v[216:219],v[252:255],a[248:251]\n"
"ds_read_b64_tr_b16 v[186:187],v104 offset:1024\n"
"s_waitcnt lgkmcnt(10)\n"
"v_mfma_f32_16x16x32_bf16 a[192:195],v[152:155],v[160:163],a[192:195]\n"
"ds_read_b64_tr_b16 v[188:189],v105 offset:0\n"
"v_mfma_f32_16x16x32_bf16 a[196:199],v[156:159],v[160:163],a[196:199]\n"
"ds_read_b64_tr_b16 v[190:191],v105 offset:1024\n"
"v_mfma_f32_16x16x32_bf16 a[204:207],v[156:159],v[164:167],a[204:207]\n"
"ds_read_b128 v[128:131],v80 offset:32768\n"
"v_mfma_f32_16x16x32_bf16 a[200:203],v[152:155],v[164:167],a[200:203]\n"
"ds_read_b128 v[192:195],v81 offset:32768\n"
"s_waitcnt lgkmcnt(10)\n"
"v_mfma_f32_16x16x32_bf16 a[208:211],v[152:155],v[168:171],a[208:211]\n"
"ds_read_b64_tr_b16 v[224:225],v98 offset:16384\n"
"v_mfma_f32_16x16x32_bf16 a[212:215],v[156:159],v[168:171],a[212:215]\n"
"ds_read_b64_tr_b16 v[226:227],v98 offset:17408\n"
"v_mfma_f32_16x16x32_bf16 a[220:223],v[156:159],v[172:175],a[220:223]\n"
"ds_read_b64_tr_b16 v[228:229],v99 offset:16384\n"
"v_mfma_f32_16x16x32_bf16 a[216:219],v[152:155],v[172:175],a[216:219]\n"
"ds_read_b64_tr_b16 v[230:231],v99 offset:17408\n"
"s_waitcnt lgkmcnt(10)\n"
"v_mfma_f32_16x16x32_bf16 a[224:227],v[152:155],v[176:179],a[224:227]\n"
"ds_read_b64_tr_b16 v[232:233],v100 offset:16384\n"
"v_mfma_f32_16x16x32_bf16 a[228:231],v[156:159],v[176:179],a[228:231]\n"
"ds_read_b64_tr_b16 v[234:235],v100 offset:17408\n"
"v_mfma_f32_16x16x32_bf16 a[236:239],v[156:159],v[180:183],a[236:239]\n"
"ds_read_b64_tr_b16 v[236:237],v101 offset:16384\n"
"v_mfma_f32_16x16x32_bf16 a[232:235],v[152:155],v[180:183],a[232:235]\n"
"ds_read_b64_tr_b16 v[238:239],v101 offset:17408\n"
"s_waitcnt lgkmcnt(10)\n"
"v_mfma_f32_16x16x32_bf16 a[240:243],v[152:155],v[184:187],a[240:243]\n"
"ds_read_b64_tr_b16 v[240:241],v102 offset:16384\n"
"v_mfma_f32_16x16x32_bf16 a[244:247],v[156:159],v[184:187],a[244:247]\n"
"ds_read_b64_tr_b16 v[242:243],v102 offset:17408\n"
"v_mfma_f32_16x16x32_bf16 a[252:255],v[156:159],v[188:191],a[252:255]\n"
"ds_read_b64_tr_b16 v[244:245],v103 offset:16384\n"
"v_mfma_f32_16x16x32_bf16 a[248:251],v[152:155],v[188:191],a[248:251]\n"
"ds_read_b64_tr_b16 v[246:247],v103 offset:17408\n"
"s_waitcnt lgkmcnt(8)\n"
"v_mfma_f32_16x16x32_bf16 a[0:3],v[128:131],v[160:163],a[0:3]\n"
"ds_read_b64_tr_b16 v[248:249],v104 offset:16384\n"
"v_mfma_f32_16x16x32_bf16 a[0:3],v[192:195],v[224:227],a[0:3]\n"
"ds_read_b64_tr_b16 v[250:251],v104 offset:17408\n"
"v_mfma_f32_16x16x32_bf16 a[8:11],v[192:195],v[228:231],a[8:11]\n"
"ds_read_b64_tr_b16 v[252:253],v105 offset:16384\n"
"v_mfma_f32_16x16x32_bf16 a[8:11],v[128:131],v[164:167],a[8:11]\n"
"ds_read_b64_tr_b16 v[254:255],v105 offset:17408\n"
"s_waitcnt lgkmcnt(8)\n"
"v_mfma_f32_16x16x32_bf16 a[16:19],v[128:131],v[168:171],a[16:19]\n"
"ds_read_b128 v[132:135],v80 offset:34816\n"
"v_mfma_f32_16x16x32_bf16 a[16:19],v[192:195],v[232:235],a[16:19]\n"
"ds_read_b128 v[196:199],v81 offset:34816\n"
"v_mfma_f32_16x16x32_bf16 a[24:27],v[192:195],v[236:239],a[24:27]\n"
"ds_read_b128 v[136:139],v80 offset:36864\n"
"v_mfma_f32_16x16x32_bf16 a[24:27],v[128:131],v[172:175],a[24:27]\n"
"ds_read_b128 v[200:203],v81 offset:36864\n"
"s_waitcnt lgkmcnt(8)\n"
"v_mfma_f32_16x16x32_bf16 a[32:35],v[128:131],v[176:179],a[32:35]\n"
"s_and_b32 s82,s52,1\n"
"ds_read_b128 v[140:143],v80 offset:38912\n"
"s_lshl_b32 s82,s82,13\n"
"v_mfma_f32_16x16x32_bf16 a[32:35],v[192:195],v[240:243],a[32:35]\n"
"ds_read_b128 v[204:207],v81 offset:38912\n"
"v_mfma_f32_16x16x32_bf16 a[40:43],v[192:195],v[244:247],a[40:43]\n"
"ds_read_b128 v[144:147],v80 offset:40960\n"
"v_mfma_f32_16x16x32_bf16 a[40:43],v[128:131],v[180:183],a[40:43]\n"
"ds_read_b128 v[208:211],v81 offset:40960\n"
"s_waitcnt lgkmcnt(8)\n"
"v_mfma_f32_16x16x32_bf16 a[48:51],v[128:131],v[184:187],a[48:51]\n"
"ds_read_b128 v[148:151],v80 offset:43008\n"
"v_mfma_f32_16x16x32_bf16 a[48:51],v[192:195],v[248:251],a[48:51]\n"
"ds_read_b128 v[212:215],v81 offset:43008\n"
"v_subrev_u32 v98,s82,v98\n"
"v_mfma_f32_16x16x32_bf16 a[56:59],v[192:195],v[252:255],a[56:59]\n"
"ds_read_b128 v[216:219],v81 offset:45056\n"
"v_add_u32 v96,s78,v97\n"
"v_mfma_f32_16x16x32_bf16 a[56:59],v[128:131],v[188:191],a[56:59]\n"
"ds_read_b128 v[220:223],v81 offset:47104\n"
"s_waitcnt lgkmcnt(10)\n"
"v_mfma_f32_16x16x32_bf16 a[4:7],v[132:135],v[160:163],a[4:7]\n"
"v_add_u32 v97,65536,v96\n"
"v_mfma_f32_16x16x32_bf16 a[4:7],v[196:199],v[224:227],a[4:7]\n"
"v_subrev_u32 v99,s82,v99\n"
"v_mfma_f32_16x16x32_bf16 a[12:15],v[196:199],v[228:231],a[12:15]\n"
"v_mfma_f32_16x16x32_bf16 a[12:15],v[132:135],v[164:167],a[12:15]\n"
"v_mfma_f32_16x16x32_bf16 a[20:23],v[132:135],v[168:171],a[20:23]\n"
"s_waitcnt lgkmcnt(0)\n"
"s_barrier\n"
"v_mfma_f32_16x16x32_bf16 a[20:23],v[196:199],v[232:235],a[20:23]\n"
"v_subrev_u32 v100,s82,v100\n"
"s_add_u32 m0,s85,98304\n"
"v_mfma_f32_16x16x32_bf16 a[28:31],v[196:199],v[236:239],a[28:31]\n"
"buffer_load_dwordx4 v110,s[28:31],s86 offen lds\n"
"v_mfma_f32_16x16x32_bf16 a[28:31],v[132:135],v[172:175],a[28:31]\n"
"v_mfma_f32_16x16x32_bf16 a[36:39],v[132:135],v[176:179],a[36:39]\n"
"v_mfma_f32_16x16x32_bf16 a[36:39],v[196:199],v[240:243],a[36:39]\n"
"v_subrev_u32 v101,s82,v101\n"
"s_add_u32 m0,s85,99328\n"
"v_mfma_f32_16x16x32_bf16 a[44:47],v[196:199],v[244:247],a[44:47]\n"
"buffer_load_dwordx4 v111,s[28:31],s86 offen lds\n"
"v_mfma_f32_16x16x32_bf16 a[44:47],v[132:135],v[180:183],a[44:47]\n"
"s_xor_b32 s86,s86,262144\n"
"v_mfma_f32_16x16x32_bf16 a[52:55],v[132:135],v[184:187],a[52:55]\n"
"v_mfma_f32_16x16x32_bf16 a[52:55],v[196:199],v[248:251],a[52:55]\n"
"v_subrev_u32 v102,s82,v102\n"
"s_add_u32 m0,s78,32768\n"
"v_mfma_f32_16x16x32_bf16 a[60:63],v[196:199],v[252:255],a[60:63]\n"
"buffer_load_dwordx4 v112,s[24:27],s76 offen offset:0 sc1 lds\n"
"v_mfma_f32_16x16x32_bf16 a[60:63],v[132:135],v[188:191],a[60:63]\n"
"v_mfma_f32_16x16x32_bf16 a[64:67],v[136:139],v[160:163],a[64:67]\n"
"v_mfma_f32_16x16x32_bf16 a[64:67],v[200:203],v[224:227],a[64:67]\n"
"v_subrev_u32 v103,s82,v103\n"
"v_mfma_f32_16x16x32_bf16 a[72:75],v[200:203],v[228:231],a[72:75]\n"
"buffer_load_dwordx4 v112,s[24:27],s76 offen offset:1024 sc1 lds\n"
"v_mfma_f32_16x16x32_bf16 a[72:75],v[136:139],v[164:167],a[72:75]\n"
"v_mfma_f32_16x16x32_bf16 a[80:83],v[136:139],v[168:171],a[80:83]\n"
"v_accvgpr_read_b32 v116,a0\n"
"v_accvgpr_read_b32 v117,a1\n"
"v_mfma_f32_16x16x32_bf16 a[80:83],v[200:203],v[232:235],a[80:83]\n"
"v_subrev_u32 v104,s82,v104\n"
"v_accvgpr_read_b32 v118,a2\n"
"v_mfma_f32_16x16x32_bf16 a[88:91],v[200:203],v[236:239],a[88:91]\n"
"buffer_load_dwordx4 v112,s[24:27],s76 offen offset:2048 sc1 lds\n"
"v_mfma_f32_16x16x32_bf16 a[88:91],v[136:139],v[172:175],a[88:91]\n"
"v_accvgpr_read_b32 v119,a3\n"
"v_accvgpr_read_b32 v120,a4\n"
"v_mfma_f32_16x16x32_bf16 a[96:99],v[136:139],v[176:179],a[96:99]\n"
"v_accvgpr_read_b32 v121,a5\n"
"v_accvgpr_read_b32 v122,a6\n"
"v_mfma_f32_16x16x32_bf16 a[96:99],v[200:203],v[240:243],a[96:99]\n"
"v_subrev_u32 v105,s82,v105\n"
"v_accvgpr_read_b32 v123,a7\n"
"v_mfma_f32_16x16x32_bf16 a[104:107],v[200:203],v[244:247],a[104:107]\n"
"buffer_load_dwordx4 v112,s[24:27],s76 offen offset:3072 sc1 lds\n"
"v_mfma_f32_16x16x32_bf16 a[104:107],v[136:139],v[180:183],a[104:107]\n"
"s_xor_b32 s76,s76,16384\n"
"v_cvt_pk_bf16_f32 v116,v116,v117\n"
"v_cvt_pk_bf16_f32 v117,v118,v119\n"
"v_mfma_f32_16x16x32_bf16 a[112:115],v[136:139],v[184:187],a[112:115]\n"
"v_cvt_pk_bf16_f32 v118,v120,v121\n"
"v_cvt_pk_bf16_f32 v119,v122,v123\n"
"v_mfma_f32_16x16x32_bf16 a[112:115],v[200:203],v[248:251],a[112:115]\n"
"buffer_store_dwordx4 v[116:119],v112,s[24:27],s56 offen offset:0 sc1\n"
"v_mfma_f32_16x16x32_bf16 a[120:123],v[200:203],v[252:255],a[120:123]\n"
"s_nop 0\n"
"v_accvgpr_read_b32 v116,a8\n"
"v_accvgpr_read_b32 v117,a9\n"
"v_accvgpr_read_b32 v118,a10\n"
"v_mfma_f32_16x16x32_bf16 a[120:123],v[136:139],v[188:191],a[120:123]\n"
"s_waitcnt vmcnt(20)\n"
"v_accvgpr_read_b32 v119,a11\n"
"v_accvgpr_read_b32 v120,a12\n"
"s_barrier\n"
"v_mfma_f32_16x16x32_bf16 a[68:71],v[140:143],v[160:163],a[68:71]\n"
"ds_read_b128 v[0:3],v97 offset:0\n"
"v_accvgpr_read_b32 v121,a13\n"
"v_mfma_f32_16x16x32_bf16 a[68:71],v[204:207],v[224:227],a[68:71]\n"
"ds_read_b128 v[8:11],v97 offset:1024\n"
"v_accvgpr_read_b32 v122,a14\n"
"v_mfma_f32_16x16x32_bf16 a[76:79],v[204:207],v[228:231],a[76:79]\n"
"ds_read_b128 v[16:19],v97 offset:2048\n"
"v_accvgpr_read_b32 v123,a15\n"
"v_mfma_f32_16x16x32_bf16 a[76:79],v[140:143],v[164:167],a[76:79]\n"
"ds_read_b128 v[24:27],v97 offset:3072\n"
"v_cvt_pk_bf16_f32 v116,v116,v117\n"
"v_mfma_f32_16x16x32_bf16 a[84:87],v[140:143],v[168:171],a[84:87]\n"
"ds_read_b128 v[4:7],v97 offset:16384\n"
"v_cvt_pk_bf16_f32 v117,v118,v119\n"
"v_mfma_f32_16x16x32_bf16 a[84:87],v[204:207],v[232:235],a[84:87]\n"
"ds_read_b128 v[12:15],v97 offset:17408\n"
"v_cvt_pk_bf16_f32 v118,v120,v121\n"
"v_mfma_f32_16x16x32_bf16 a[92:95],v[204:207],v[236:239],a[92:95]\n"
"ds_read_b128 v[20:23],v97 offset:18432\n"
"v_cvt_pk_bf16_f32 v119,v122,v123\n"
"v_mfma_f32_16x16x32_bf16 a[92:95],v[140:143],v[172:175],a[92:95]\n"
"ds_read_b128 v[28:31],v97 offset:19456\n"
"buffer_store_dwordx4 v[116:119],v112,s[24:27],s56 offen offset:1024 sc1\n"
"v_mfma_f32_16x16x32_bf16 a[100:103],v[140:143],v[176:179],a[100:103]\n"
"ds_read_b64_tr_b16 v[32:33],v98 offset:49152\n"
"v_accvgpr_read_b32 v116,a16\n"
"v_mfma_f32_16x16x32_bf16 a[100:103],v[204:207],v[240:243],a[100:103]\n"
"ds_read_b64_tr_b16 v[34:35],v98 offset:50176\n"
"v_accvgpr_read_b32 v117,a17\n"
"v_mfma_f32_16x16x32_bf16 a[108:111],v[204:207],v[244:247],a[108:111]\n"
"ds_read_b64_tr_b16 v[36:37],v98 offset:57344\n"
"v_accvgpr_read_b32 v118,a18\n"
"v_mfma_f32_16x16x32_bf16 a[108:111],v[140:143],v[180:183],a[108:111]\n"
"ds_read_b64_tr_b16 v[38:39],v98 offset:58368\n"
"v_accvgpr_read_b32 v119,a19\n"
"v_mfma_f32_16x16x32_bf16 a[116:119],v[140:143],v[184:187],a[116:119]\n"
"ds_read_b64_tr_b16 v[40:41],v99 offset:49152\n"
"v_accvgpr_read_b32 v120,a20\n"
"v_mfma_f32_16x16x32_bf16 a[116:119],v[204:207],v[248:251],a[116:119]\n"
"ds_read_b64_tr_b16 v[42:43],v99 offset:50176\n"
"v_accvgpr_read_b32 v121,a21\n"
"v_mfma_f32_16x16x32_bf16 a[124:127],v[204:207],v[252:255],a[124:127]\n"
"ds_read_b64_tr_b16 v[44:45],v99 offset:57344\n"
"v_accvgpr_read_b32 v122,a22\n"
"v_mfma_f32_16x16x32_bf16 a[124:127],v[140:143],v[188:191],a[124:127]\n"
"ds_read_b64_tr_b16 v[46:47],v99 offset:58368\n"
"v_accvgpr_read_b32 v123,a23\n"
"v_mfma_f32_16x16x32_bf16 a[128:131],v[144:147],v[160:163],a[128:131]\n"
"ds_read_b64_tr_b16 v[48:49],v100 offset:49152\n"
"v_cvt_pk_bf16_f32 v116,v116,v117\n"
"v_mfma_f32_16x16x32_bf16 a[128:131],v[208:211],v[224:227],a[128:131]\n"
"ds_read_b64_tr_b16 v[50:51],v100 offset:50176\n"
"v_cvt_pk_bf16_f32 v117,v118,v119\n"
"v_mfma_f32_16x16x32_bf16 a[136:139],v[208:211],v[228:231],a[136:139]\n"
"ds_read_b64_tr_b16 v[52:53],v100 offset:57344\n"
"v_cvt_pk_bf16_f32 v118,v120,v121\n"
"v_mfma_f32_16x16x32_bf16 a[136:139],v[144:147],v[164:167],a[136:139]\n"
"ds_read_b64_tr_b16 v[54:55],v100 offset:58368\n"
"v_cvt_pk_bf16_f32 v119,v122,v123\n"
"v_mfma_f32_16x16x32_bf16 a[144:147],v[144:147],v[168:171],a[144:147]\n"
"ds_read_b64_tr_b16 v[56:57],v101 offset:49152\n"
"buffer_store_dwordx4 v[116:119],v112,s[24:27],s56 offen offset:2048 sc1\n"
"v_mfma_f32_16x16x32_bf16 a[144:147],v[208:211],v[232:235],a[144:147]\n"
"ds_read_b64_tr_b16 v[58:59],v101 offset:50176\n"
"v_accvgpr_read_b32 v116,a24\n"
"v_mfma_f32_16x16x32_bf16 a[152:155],v[208:211],v[236:239],a[152:155]\n"
"ds_read_b64_tr_b16 v[60:61],v101 offset:57344\n"
"v_accvgpr_read_b32 v117,a25\n"
"v_mfma_f32_16x16x32_bf16 a[152:155],v[144:147],v[172:175],a[152:155]\n"
"ds_read_b64_tr_b16 v[62:63],v101 offset:58368\n"
"v_accvgpr_read_b32 v118,a26\n"
"v_mfma_f32_16x16x32_bf16 a[160:163],v[144:147],v[176:179],a[160:163]\n"
"ds_read_b64_tr_b16 v[64:65],v102 offset:49152\n"
"v_accvgpr_read_b32 v119,a27\n"
"v_mfma_f32_16x16x32_bf16 a[160:163],v[208:211],v[240:243],a[160:163]\n"
"ds_read_b64_tr_b16 v[66:67],v102 offset:50176\n"
"v_accvgpr_read_b32 v120,a28\n"
"v_mfma_f32_16x16x32_bf16 a[168:171],v[208:211],v[244:247],a[168:171]\n"
"ds_read_b64_tr_b16 v[68:69],v102 offset:57344\n"
"v_accvgpr_read_b32 v121,a29\n"
"v_mfma_f32_16x16x32_bf16 a[168:171],v[144:147],v[180:183],a[168:171]\n"
"ds_read_b64_tr_b16 v[70:71],v102 offset:58368\n"
"v_accvgpr_read_b32 v122,a30\n"
"v_mfma_f32_16x16x32_bf16 a[176:179],v[144:147],v[184:187],a[176:179]\n"
"ds_read_b64_tr_b16 v[72:73],v103 offset:49152\n"
"v_accvgpr_read_b32 v123,a31\n"
"v_mfma_f32_16x16x32_bf16 a[176:179],v[208:211],v[248:251],a[176:179]\n"
"ds_read_b64_tr_b16 v[74:75],v103 offset:50176\n"
"v_cvt_pk_bf16_f32 v116,v116,v117\n"
"v_mfma_f32_16x16x32_bf16 a[184:187],v[208:211],v[252:255],a[184:187]\n"
"ds_read_b64_tr_b16 v[76:77],v103 offset:57344\n"
"v_cvt_pk_bf16_f32 v117,v118,v119\n"
"v_mfma_f32_16x16x32_bf16 a[184:187],v[144:147],v[188:191],a[184:187]\n"
"ds_read_b64_tr_b16 v[78:79],v103 offset:58368\n"
"v_cvt_pk_bf16_f32 v118,v120,v121\n"
"v_mfma_f32_16x16x32_bf16 a[132:135],v[148:151],v[160:163],a[132:135]\n"
"ds_read_b64_tr_b16 v[80:81],v104 offset:49152\n"
"v_cvt_pk_bf16_f32 v119,v122,v123\n"
"v_mfma_f32_16x16x32_bf16 a[132:135],v[212:215],v[224:227],a[132:135]\n"
"ds_read_b64_tr_b16 v[82:83],v104 offset:50176\n"
"buffer_store_dwordx4 v[116:119],v112,s[24:27],s56 offen offset:3072 sc1\n"
"v_mfma_f32_16x16x32_bf16 a[140:143],v[212:215],v[228:231],a[140:143]\n"
"ds_read_b64_tr_b16 v[84:85],v104 offset:57344\n"
"v_mfma_f32_16x16x32_bf16 a[140:143],v[148:151],v[164:167],a[140:143]\n"
"ds_read_b64_tr_b16 v[86:87],v104 offset:58368\n"
"v_mfma_f32_16x16x32_bf16 a[148:151],v[148:151],v[168:171],a[148:151]\n"
"ds_read_b64_tr_b16 v[88:89],v105 offset:49152\n"
"v_mfma_f32_16x16x32_bf16 a[148:151],v[212:215],v[232:235],a[148:151]\n"
"ds_read_b64_tr_b16 v[90:91],v105 offset:50176\n"
"v_mfma_f32_16x16x32_bf16 a[156:159],v[212:215],v[236:239],a[156:159]\n"
"ds_read_b64_tr_b16 v[92:93],v105 offset:57344\n"
"v_mfma_f32_16x16x32_bf16 a[156:159],v[148:151],v[172:175],a[156:159]\n"
"ds_read_b64_tr_b16 v[94:95],v105 offset:58368\n"
"v_mfma_f32_16x16x32_bf16 a[164:167],v[148:151],v[176:179],a[164:167]\n"
"s_add_u32 m0,s85,106496\n"
"v_mfma_f32_16x16x32_bf16 a[164:167],v[212:215],v[240:243],a[164:167]\n"
"buffer_load_dwordx4 v110,s[28:31],s86 offen lds\n"
"v_mfma_f32_16x16x32_bf16 a[172:175],v[212:215],v[244:247],a[172:175]\n"
"v_mfma_f32_16x16x32_bf16 a[172:175],v[148:151],v[180:183],a[172:175]\n"
"s_add_u32 m0,s85,107520\n"
"v_mfma_f32_16x16x32_bf16 a[180:183],v[148:151],v[184:187],a[180:183]\n"
"buffer_load_dwordx4 v111,s[28:31],s86 offen lds\n"
"v_mfma_f32_16x16x32_bf16 a[180:183],v[212:215],v[248:251],a[180:183]\n"
"s_xor_b32 s86,s86,786432\n"
"v_mfma_f32_16x16x32_bf16 a[188:191],v[212:215],v[252:255],a[188:191]\n"
"s_add_u32 m0,s78,49152\n"
"v_mfma_f32_16x16x32_bf16 a[188:191],v[148:151],v[188:191],a[188:191]\n"
"buffer_load_dwordx4 v112,s[24:27],s76 offen offset:0 sc1 lds\n"
"v_mfma_f32_16x16x32_bf16 a[192:195],v[216:219],v[224:227],a[192:195]\n"
"v_mfma_f32_16x16x32_bf16 a[196:199],v[220:223],v[224:227],a[196:199]\n"
"v_mfma_f32_16x16x32_bf16 a[204:207],v[220:223],v[228:231],a[204:207]\n"
"buffer_load_dwordx4 v112,s[24:27],s76 offen offset:1024 sc1 lds\n"
"v_mfma_f32_16x16x32_bf16 a[200:203],v[216:219],v[228:231],a[200:203]\n"
"v_mfma_f32_16x16x32_bf16 a[208:211],v[216:219],v[232:235],a[208:211]\n"
"v_mfma_f32_16x16x32_bf16 a[212:215],v[220:223],v[232:235],a[212:215]\n"
"buffer_load_dwordx4 v112,s[24:27],s76 offen offset:2048 sc1 lds\n"
"v_mfma_f32_16x16x32_bf16 a[220:223],v[220:223],v[236:239],a[220:223]\n"
"v_mfma_f32_16x16x32_bf16 a[216:219],v[216:219],v[236:239],a[216:219]\n"
"buffer_load_dwordx4 v112,s[24:27],s76 offen offset:3072 sc1 lds\n"
"v_mfma_f32_16x16x32_bf16 a[224:227],v[216:219],v[240:243],a[224:227]\n"
"s_xor_b32 s76,s76,49152\n"
"v_mfma_f32_16x16x32_bf16 a[228:231],v[220:223],v[240:243],a[228:231]\n"
"v_mfma_f32_16x16x32_bf16 a[236:239],v[220:223],v[244:247],a[236:239]\n"
"v_mfma_f32_16x16x32_bf16 a[232:235],v[216:219],v[244:247],a[232:235]\n"
"v_mfma_f32_16x16x32_bf16 a[240:243],v[216:219],v[248:251],a[240:243]\n"
"v_mfma_f32_16x16x32_bf16 a[244:247],v[220:223],v[248:251],a[244:247]\n"
"v_mfma_f32_16x16x32_bf16 a[252:255],v[220:223],v[252:255],a[252:255]\n"
"v_mfma_f32_16x16x32_bf16 a[248:251],v[216:219],v[252:255],a[248:251]\n"
".Lfk_ph2:\n"
"s_waitcnt vmcnt(25) lgkmcnt(0)\n"
"s_barrier\n"
"v_mfma_f32_16x16x32_bf16 v[128:131],v[32:35],v[0:3],0\n"
"v_accvgpr_read_b32 v116,a32\n"
"v_accvgpr_read_b32 v117,a33\n"
"v_mfma_f32_16x16x32_bf16 v[128:131],v[36:39],v[4:7],v[128:131]\n"
"v_accvgpr_read_b32 v118,a34\n"
"v_accvgpr_read_b32 v119,a35\n"
"v_mfma_f32_16x16x32_bf16 v[160:163],v[36:39],v[12:15],0\n"
"v_accvgpr_read_b32 v120,a36\n"
"v_accvgpr_read_b32 v121,a37\n"
"v_mfma_f32_16x16x32_bf16 v[160:163],v[32:35],v[8:11],v[160:163]\n"
"s_add_u32 m0,s85,147456\n"
"v_accvgpr_read_b32 v122,a38\n"
"v_mfma_f32_16x16x32_bf16 v[192:195],v[32:35],v[16:19],0\n"
"buffer_load_dwordx4 v110,s[28:31],s86 offen lds\n"
"v_mfma_f32_16x16x32_bf16 v[192:195],v[36:39],v[20:23],v[192:195]\n"
"v_accvgpr_read_b32 v123,a39\n"
"v_cvt_pk_bf16_f32 v116,v116,v117\n"
"v_mfma_f32_16x16x32_bf16 v[224:227],v[36:39],v[28:31],0\n"
"v_cvt_pk_bf16_f32 v117,v118,v119\n"
"v_cvt_pk_bf16_f32 v118,v120,v121\n"
"v_mfma_f32_16x16x32_bf16 v[224:227],v[32:35],v[24:27],v[224:227]\n"
"v_cvt_pk_bf16_f32 v119,v122,v123\n"
"s_add_u32 s56,s56,4096\n"
"v_mfma_f32_16x16x32_bf16 v[228:231],v[40:43],v[24:27],0\n"
"ds_read_b64_tr_b16 v[32:33],v100 offset:32768\n"
"buffer_store_dwordx4 v[116:119],v112,s[24:27],s56 offen offset:0 sc1\n"
"v_mfma_f32_16x16x32_bf16 v[228:231],v[44:47],v[28:31],v[228:231]\n"
"ds_read_b64_tr_b16 v[34:35],v100 offset:33792\n"
"v_accvgpr_read_b32 v116,a40\n"
"v_mfma_f32_16x16x32_bf16 v[196:199],v[44:47],v[20:23],0\n"
"ds_read_b64_tr_b16 v[36:37],v100 offset:40960\n"
"v_accvgpr_read_b32 v117,a41\n"
"v_mfma_f32_16x16x32_bf16 v[196:199],v[40:43],v[16:19],v[196:199]\n"
"s_add_u32 m0,s85,148480\n"
"ds_read_b64_tr_b16 v[38:39],v100 offset:41984\n"
"v_mfma_f32_16x16x32_bf16 v[164:167],v[40:43],v[8:11],0\n"
"buffer_load_dwordx4 v111,s[28:31],s86 offen lds\n"
"v_mfma_f32_16x16x32_bf16 v[164:167],v[44:47],v[12:15],v[164:167]\n"
"s_add_u32 m0,s85,155648\n"
"s_xor_b32 s86,s86,262144\n"
"v_mfma_f32_16x16x32_bf16 v[132:135],v[44:47],v[4:7],0\n"
"buffer_load_dwordx4 v110,s[28:31],s86 offen lds\n"
"v_mfma_f32_16x16x32_bf16 v[132:135],v[40:43],v[0:3],v[132:135]\n"
"v_accvgpr_read_b32 v118,a42\n"
"v_accvgpr_read_b32 v119,a43\n"
"v_mfma_f32_16x16x32_bf16 v[136:139],v[48:51],v[0:3],0\n"
"ds_read_b64_tr_b16 v[40:41],v101 offset:32768\n"
"v_accvgpr_read_b32 v120,a44\n"
"v_mfma_f32_16x16x32_bf16 v[136:139],v[52:55],v[4:7],v[136:139]\n"
"ds_read_b64_tr_b16 v[42:43],v101 offset:33792\n"
"v_accvgpr_read_b32 v121,a45\n"
"v_mfma_f32_16x16x32_bf16 v[168:171],v[52:55],v[12:15],0\n"
"ds_read_b64_tr_b16 v[44:45],v101 offset:40960\n"
"v_accvgpr_read_b32 v122,a46\n"
"v_mfma_f32_16x16x32_bf16 v[168:171],v[48:51],v[8:11],v[168:171]\n"
"s_add_u32 m0,s85,156672\n"
"ds_read_b64_tr_b16 v[46:47],v101 offset:41984\n"
"v_mfma_f32_16x16x32_bf16 v[200:203],v[48:51],v[16:19],0\n"
"buffer_load_dwordx4 v111,s[28:31],s86 offen lds\n"
"v_mfma_f32_16x16x32_bf16 v[200:203],v[52:55],v[20:23],v[200:203]\n"
"s_xor_b32 s86,s86,3932160\n"
"v_accvgpr_read_b32 v123,a47\n"
"v_mfma_f32_16x16x32_bf16 v[232:235],v[52:55],v[28:31],0\n"
"v_cvt_pk_bf16_f32 v116,v116,v117\n"
"v_cvt_pk_bf16_f32 v117,v118,v119\n"
"v_mfma_f32_16x16x32_bf16 v[232:235],v[48:51],v[24:27],v[232:235]\n"
"v_cvt_pk_bf16_f32 v118,v120,v121\n"
"v_cvt_pk_bf16_f32 v119,v122,v123\n"
"v_mfma_f32_16x16x32_bf16 v[236:239],v[56:59],v[24:27],0\n"
"ds_read_b64_tr_b16 v[48:49],v102 offset:32768\n"
"buffer_store_dwordx4 v[116:119],v112,s[24:27],s56 offen offset:1024 sc1\n"
"v_mfma_f32_16x16x32_bf16 v[236:239],v[60:63],v[28:31],v[236:239]\n"
"ds_read_b64_tr_b16 v[50:51],v102 offset:33792\n"
"v_accvgpr_read_b32 v116,a48\n"
"v_mfma_f32_16x16x32_bf16 v[204:207],v[60:63],v[20:23],0\n"
"ds_read_b64_tr_b16 v[52:53],v102 offset:40960\n"
"v_accvgpr_read_b32 v117,a49\n"
"v_mfma_f32_16x16x32_bf16 v[204:207],v[56:59],v[16:19],v[204:207]\n"
"s_add_u32 m0,s78,65536\n"
"ds_read_b64_tr_b16 v[54:55],v102 offset:41984\n"
"v_mfma_f32_16x16x32_bf16 v[172:175],v[56:59],v[8:11],0\n"
"buffer_load_dwordx4 v112,s[24:27],s76 offen offset:0 sc1 lds\n"
"v_mfma_f32_16x16x32_bf16 v[172:175],v[60:63],v[12:15],v[172:175]\n"
"v_accvgpr_read_b32 v118,a50\n"
"v_mfma_f32_16x16x32_bf16 v[140:143],v[60:63],v[4:7],0\n"
"buffer_load_dwordx4 v112,s[24:27],s76 offen offset:1024 sc1 lds\n"
"v_mfma_f32_16x16x32_bf16 v[140:143],v[56:59],v[0:3],v[140:143]\n"
"s_waitcnt vmcnt(25)\n"
"v_mfma_f32_16x16x32_bf16 v[144:147],v[64:67],v[0:3],0\n"
"ds_read_b64_tr_b16 a[32:33],v98 offset:32768\n"
"ds_read_b64_tr_b16 v[56:57],v103 offset:32768\n"
"v_mfma_f32_16x16x32_bf16 v[144:147],v[68:71],v[4:7],v[144:147]\n"
"ds_read_b64_tr_b16 a[34:35],v98 offset:33792\n"
"ds_read_b64_tr_b16 v[58:59],v103 offset:33792\n"
"v_mfma_f32_16x16x32_bf16 v[176:179],v[68:71],v[12:15],0\n"
"ds_read_b64_tr_b16 a[36:37],v98 offset:40960\n"
"ds_read_b64_tr_b16 v[60:61],v103 offset:40960\n"
"v_mfma_f32_16x16x32_bf16 v[176:179],v[64:67],v[8:11],v[176:179]\n"
"ds_read_b64_tr_b16 a[38:39],v98 offset:41984\n"
"ds_read_b64_tr_b16 v[62:63],v103 offset:41984\n"
"v_mfma_f32_16x16x32_bf16 v[208:211],v[64:67],v[16:19],0\n"
"buffer_load_dwordx4 v112,s[24:27],s76 offen offset:2048 sc1 lds\n"
"v_mfma_f32_16x16x32_bf16 v[208:211],v[68:71],v[20:23],v[208:211]\n"
"ds_read_b128 a[0:3],v96 offset:0\n"
"v_accvgpr_read_b32 v119,a51\n"
"v_mfma_f32_16x16x32_bf16 v[240:243],v[68:71],v[28:31],0\n"
"v_accvgpr_read_b32 v120,a52\n"
"v_accvgpr_read_b32 v121,a53\n"
"v_mfma_f32_16x16x32_bf16 v[240:243],v[64:67],v[24:27],v[240:243]\n"
"ds_read_b128 a[8:11],v96 offset:1024\n"
"v_accvgpr_read_b32 v122,a54\n"
"v_mfma_f32_16x16x32_bf16 v[244:247],v[72:75],v[24:27],0\n"
"ds_read_b64_tr_b16 a[40:41],v99 offset:32768\n"
"ds_read_b64_tr_b16 v[64:65],v104 offset:32768\n"
"v_mfma_f32_16x16x32_bf16 v[244:247],v[76:79],v[28:31],v[244:247]\n"
"ds_read_b64_tr_b16 a[42:43],v99 offset:33792\n"
"ds_read_b64_tr_b16 v[66:67],v104 offset:33792\n"
"v_mfma_f32_16x16x32_bf16 v[212:215],v[76:79],v[20:23],0\n"
"ds_read_b64_tr_b16 a[44:45],v99 offset:40960\n"
"ds_read_b64_tr_b16 v[68:69],v104 offset:40960\n"
"v_mfma_f32_16x16x32_bf16 v[212:215],v[72:75],v[16:19],v[212:215]\n"
"ds_read_b64_tr_b16 a[46:47],v99 offset:41984\n"
"ds_read_b64_tr_b16 v[70:71],v104 offset:41984\n"
"v_mfma_f32_16x16x32_bf16 v[180:183],v[72:75],v[8:11],0\n"
"buffer_load_dwordx4 v112,s[24:27],s76 offen offset:3072 sc1 lds\n"
"v_mfma_f32_16x16x32_bf16 v[180:183],v[76:79],v[12:15],v[180:183]\n"
"s_add_u32 m0,s78,81920\n"
"s_xor_b32 s76,s76,16384\n"
"ds_read_b128 a[16:19],v96 offset:2048\n"
"v_mfma_f32_16x16x32_bf16 v[148:151],v[76:79],v[4:7],0\n"
"buffer_load_dwordx4 v112,s[24:27],s76 offen offset:0 sc1 lds\n"
"v_mfma_f32_16x16x32_bf16 v[148:151],v[72:75],v[0:3],v[148:151]\n"
"ds_read_b128 a[24:27],v96 offset:3072\n"
"v_accvgpr_read_b32 v123,a55\n"
"v_mfma_f32_16x16x32_bf16 v[152:155],v[80:83],v[0:3],0\n"
"ds_read_b64_tr_b16 v[72:73],v105 offset:32768\n"
"v_cvt_pk_bf16_f32 v116,v116,v117\n"
"v_mfma_f32_16x16x32_bf16 v[152:155],v[84:87],v[4:7],v[152:155]\n"
"ds_read_b64_tr_b16 v[74:75],v105 offset:33792\n"
"v_cvt_pk_bf16_f32 v117,v118,v119\n"
"v_mfma_f32_16x16x32_bf16 v[184:187],v[84:87],v[12:15],0\n"
"ds_read_b64_tr_b16 v[76:77],v105 offset:40960\n"
"v_cvt_pk_bf16_f32 v118,v120,v121\n"
"v_mfma_f32_16x16x32_bf16 v[184:187],v[80:83],v[8:11],v[184:187]\n"
"ds_read_b64_tr_b16 v[78:79],v105 offset:41984\n"
"v_cvt_pk_bf16_f32 v119,v122,v123\n"
"v_mfma_f32_16x16x32_bf16 v[216:219],v[80:83],v[16:19],0\n"
"ds_read_b128 a[4:7],v96 offset:16384\n"
"buffer_store_dwordx4 v[116:119],v112,s[24:27],s56 offen offset:2048 sc1\n"
"v_mfma_f32_16x16x32_bf16 v[216:219],v[84:87],v[20:23],v[216:219]\n"
"ds_read_b128 a[12:15],v96 offset:17408\n"
"v_accvgpr_read_b32 v116,a56\n"
"v_mfma_f32_16x16x32_bf16 v[248:251],v[84:87],v[28:31],0\n"
"ds_read_b128 a[20:23],v96 offset:18432\n"
"v_accvgpr_read_b32 v117,a57\n"
"v_mfma_f32_16x16x32_bf16 v[248:251],v[80:83],v[24:27],v[248:251]\n"
"ds_read_b128 a[28:31],v96 offset:19456\n"
"v_accvgpr_read_b32 v118,a58\n"
"v_mfma_f32_16x16x32_bf16 v[252:255],v[88:91],v[24:27],0\n"
"buffer_load_dwordx4 v112,s[24:27],s76 offen offset:1024 sc1 lds\n"
"v_mfma_f32_16x16x32_bf16 v[252:255],v[92:95],v[28:31],v[252:255]\n"
"v_accvgpr_read_b32 v119,a59\n"
"v_accvgpr_read_b32 v120,a60\n"
"v_mfma_f32_16x16x32_bf16 v[220:223],v[92:95],v[20:23],0\n"
"v_accvgpr_read_b32 v121,a61\n"
"v_accvgpr_read_b32 v122,a62\n"
"v_mfma_f32_16x16x32_bf16 v[220:223],v[88:91],v[16:19],v[220:223]\n"
"buffer_load_dwordx4 v112,s[24:27],s76 offen offset:2048 sc1 lds\n"
"v_mfma_f32_16x16x32_bf16 v[188:191],v[88:91],v[8:11],0\n"
"v_accvgpr_read_b32 v123,a63\n"
"v_cvt_pk_bf16_f32 v116,v116,v117\n"
"v_mfma_f32_16x16x32_bf16 v[188:191],v[92:95],v[12:15],v[188:191]\n"
"v_cvt_pk_bf16_f32 v117,v118,v119\n"
"v_cvt_pk_bf16_f32 v118,v120,v121\n"
"v_mfma_f32_16x16x32_bf16 v[156:159],v[92:95],v[4:7],0\n"
"buffer_load_dwordx4 v112,s[24:27],s76 offen offset:3072 sc1 lds\n"
"v_mfma_f32_16x16x32_bf16 v[156:159],v[88:91],v[0:3],v[156:159]\n"
"s_xor_b32 s76,s76,245760\n"
"v_cvt_pk_bf16_f32 v119,v122,v123\n"
"s_waitcnt vmcnt(19) lgkmcnt(0)\n"
"s_barrier\n"
"v_mfma_f32_16x16x32_bf16 v[128:131],a[32:35],a[0:3],v[128:131]\n"
"ds_read_b64_tr_b16 v[80:81],v98 offset:0\n"
"ds_read_b64_tr_b16 v[88:89],v99 offset:0\n"
"v_mfma_f32_16x16x32_bf16 v[128:131],a[36:39],a[4:7],v[128:131]\n"
"ds_read_b64_tr_b16 v[82:83],v98 offset:1024\n"
"ds_read_b64_tr_b16 v[90:91],v99 offset:1024\n"
"v_mfma_f32_16x16x32_bf16 v[160:163],a[36:39],a[12:15],v[160:163]\n"
"ds_read_b64_tr_b16 v[84:85],v98 offset:8192\n"
"ds_read_b64_tr_b16 v[92:93],v99 offset:8192\n"
"v_mfma_f32_16x16x32_bf16 v[160:163],a[32:35],a[8:11],v[160:163]\n"
"s_add_u32 m0,s85,131072\n"
"ds_read_b64_tr_b16 v[86:87],v98 offset:9216\n"
"ds_read_b64_tr_b16 v[94:95],v99 offset:9216\n"
"v_mfma_f32_16x16x32_bf16 v[192:195],a[32:35],a[16:19],v[192:195]\n"
"buffer_load_dwordx4 v110,s[28:31],s86 offen lds\n"
"buffer_store_dwordx4 v[116:119],v112,s[24:27],s56 offen offset:3072 sc1\n"
"v_mfma_f32_16x16x32_bf16 v[192:195],a[36:39],a[20:23],v[192:195]\n"
"s_nop 0\n"
"v_accvgpr_read_b32 v116,a64\n"
"v_mfma_f32_16x16x32_bf16 v[224:227],a[36:39],a[28:31],v[224:227]\n"
"v_accvgpr_read_b32 v117,a65\n"
"v_accvgpr_read_b32 v118,a66\n"
"v_mfma_f32_16x16x32_bf16 v[224:227],a[32:35],a[24:27],v[224:227]\n"
"v_accvgpr_read_b32 v119,a67\n"
"v_accvgpr_read_b32 v120,a68\n"
"v_mfma_f32_16x16x32_bf16 v[228:231],a[40:43],a[24:27],v[228:231]\n"
"ds_read_b64_tr_b16 a[32:33],v100 offset:0\n"
"v_accvgpr_read_b32 v121,a69\n"
"v_mfma_f32_16x16x32_bf16 v[228:231],a[44:47],a[28:31],v[228:231]\n"
"ds_read_b64_tr_b16 a[34:35],v100 offset:1024\n"
"v_accvgpr_read_b32 v122,a70\n"
"v_mfma_f32_16x16x32_bf16 v[196:199],a[44:47],a[20:23],v[196:199]\n"
"ds_read_b64_tr_b16 a[36:37],v100 offset:8192\n"
"v_accvgpr_read_b32 v123,a71\n"
"v_mfma_f32_16x16x32_bf16 v[196:199],a[40:43],a[16:19],v[196:199]\n"
"s_add_u32 m0,s85,132096\n"
"ds_read_b64_tr_b16 a[38:39],v100 offset:9216\n"
"v_mfma_f32_16x16x32_bf16 v[164:167],a[40:43],a[8:11],v[164:167]\n"
"buffer_load_dwordx4 v111,s[28:31],s86 offen lds\n"
"v_mfma_f32_16x16x32_bf16 v[164:167],a[44:47],a[12:15],v[164:167]\n"
"s_add_u32 m0,s85,139264\n"
"s_xor_b32 s86,s86,262144\n"
"v_mfma_f32_16x16x32_bf16 v[132:135],a[44:47],a[4:7],v[132:135]\n"
"buffer_load_dwordx4 v110,s[28:31],s86 offen lds\n"
"v_mfma_f32_16x16x32_bf16 v[132:135],a[40:43],a[0:3],v[132:135]\n"
"v_cvt_pk_bf16_f32 v116,v116,v117\n"
"v_cvt_pk_bf16_f32 v117,v118,v119\n"
"v_mfma_f32_16x16x32_bf16 v[136:139],v[32:35],a[0:3],v[136:139]\n"
"ds_read_b64_tr_b16 a[40:41],v101 offset:0\n"
"v_cvt_pk_bf16_f32 v118,v120,v121\n"
"v_mfma_f32_16x16x32_bf16 v[136:139],v[36:39],a[4:7],v[136:139]\n"
"ds_read_b64_tr_b16 a[42:43],v101 offset:1024\n"
"v_cvt_pk_bf16_f32 v119,v122,v123\n"
"v_mfma_f32_16x16x32_bf16 v[168:171],v[36:39],a[12:15],v[168:171]\n"
"ds_read_b64_tr_b16 a[44:45],v101 offset:8192\n"
"s_add_u32 s56,s56,126976\n"
"v_mfma_f32_16x16x32_bf16 v[168:171],v[32:35],a[8:11],v[168:171]\n"
"s_add_u32 m0,s85,140288\n"
"ds_read_b64_tr_b16 a[46:47],v101 offset:9216\n"
"v_mfma_f32_16x16x32_bf16 v[200:203],v[32:35],a[16:19],v[200:203]\n"
"buffer_load_dwordx4 v111,s[28:31],s86 offen lds\n"
"v_mfma_f32_16x16x32_bf16 v[200:203],v[36:39],a[20:23],v[200:203]\n"
"buffer_store_dwordx4 v[116:119],v112,s[24:27],s56 offen offset:0 sc1\n"
"v_mfma_f32_16x16x32_bf16 v[232:235],v[36:39],a[28:31],v[232:235]\n"
"s_xor_b32 s86,s86,786432\n"
"v_accvgpr_read_b32 v116,a72\n"
"v_accvgpr_read_b32 v117,a73\n"
"v_mfma_f32_16x16x32_bf16 v[232:235],v[32:35],a[24:27],v[232:235]\n"
"v_accvgpr_read_b32 v118,a74\n"
"v_accvgpr_read_b32 v119,a75\n"
"v_mfma_f32_16x16x32_bf16 v[236:239],v[40:43],a[24:27],v[236:239]\n"
"ds_read_b64_tr_b16 v[32:33],v102 offset:0\n"
"v_accvgpr_read_b32 v120,a76\n"
"v_mfma_f32_16x16x32_bf16 v[236:239],v[44:47],a[28:31],v[236:239]\n"
"ds_read_b64_tr_b16 v[34:35],v102 offset:1024\n"
"v_accvgpr_read_b32 v121,a77\n"
"v_mfma_f32_16x16x32_bf16 v[204:207],v[44:47],a[20:23],v[204:207]\n"
"ds_read_b64_tr_b16 v[36:37],v102 offset:8192\n"
"v_accvgpr_read_b32 v122,a78\n"
"v_mfma_f32_16x16x32_bf16 v[204:207],v[40:43],a[16:19],v[204:207]\n"
"s_add_u32 m0,s78,0\n"
"ds_read_b64_tr_b16 v[38:39],v102 offset:9216\n"
"v_mfma_f32_16x16x32_bf16 v[172:175],v[40:43],a[8:11],v[172:175]\n"
"buffer_load_dwordx4 v112,s[24:27],s76 offen offset:0 sc1 lds\n"
"v_mfma_f32_16x16x32_bf16 v[172:175],v[44:47],a[12:15],v[172:175]\n"
"v_accvgpr_read_b32 v123,a79\n"
"v_cvt_pk_bf16_f32 v116,v116,v117\n"
"v_mfma_f32_16x16x32_bf16 v[140:143],v[44:47],a[4:7],v[140:143]\n"
"buffer_load_dwordx4 v112,s[24:27],s76 offen offset:1024 sc1 lds\n"
"v_mfma_f32_16x16x32_bf16 v[140:143],v[40:43],a[0:3],v[140:143]\n"
"s_waitcnt vmcnt(23)\n"
"v_cvt_pk_bf16_f32 v117,v118,v119\n"
"v_cvt_pk_bf16_f32 v118,v120,v121\n"
"v_mfma_f32_16x16x32_bf16 v[144:147],v[48:51],a[0:3],v[144:147]\n"
"ds_read_b64_tr_b16 v[40:41],v103 offset:0\n"
"v_cvt_pk_bf16_f32 v119,v122,v123\n"
"v_mfma_f32_16x16x32_bf16 v[144:147],v[52:55],a[4:7],v[144:147]\n"
"ds_read_b64_tr_b16 v[42:43],v103 offset:1024\n"
"buffer_store_dwordx4 v[116:119],v112,s[24:27],s56 offen offset:1024 sc1\n"
"v_mfma_f32_16x16x32_bf16 v[176:179],v[52:55],a[12:15],v[176:179]\n"
"ds_read_b64_tr_b16 v[44:45],v103 offset:8192\n"
"v_accvgpr_read_b32 v116,a80\n"
"v_mfma_f32_16x16x32_bf16 v[176:179],v[48:51],a[8:11],v[176:179]\n"
"ds_read_b64_tr_b16 v[46:47],v103 offset:9216\n"
"v_accvgpr_read_b32 v117,a81\n"
"v_mfma_f32_16x16x32_bf16 v[208:211],v[48:51],a[16:19],v[208:211]\n"
"buffer_load_dwordx4 v112,s[24:27],s76 offen offset:2048 sc1 lds\n"
"v_mfma_f32_16x16x32_bf16 v[208:211],v[52:55],a[20:23],v[208:211]\n"
"ds_read_b128 v[0:3],v96 offset:32768\n"
"v_accvgpr_read_b32 v118,a82\n"
"v_mfma_f32_16x16x32_bf16 v[240:243],v[52:55],a[28:31],v[240:243]\n"
"v_accvgpr_read_b32 v119,a83\n"
"v_accvgpr_read_b32 v120,a84\n"
"v_mfma_f32_16x16x32_bf16 v[240:243],v[48:51],a[24:27],v[240:243]\n"
"ds_read_b128 v[8:11],v96 offset:33792\n"
"v_accvgpr_read_b32 v121,a85\n"
"v_mfma_f32_16x16x32_bf16 v[244:247],v[56:59],a[24:27],v[244:247]\n"
"ds_read_b64_tr_b16 v[48:49],v104 offset:0\n"
"v_accvgpr_read_b32 v122,a86\n"
"v_mfma_f32_16x16x32_bf16 v[244:247],v[60:63],a[28:31],v[244:247]\n"
"ds_read_b64_tr_b16 v[50:51],v104 offset:1024\n"
"v_accvgpr_read_b32 v123,a87\n"
"v_mfma_f32_16x16x32_bf16 v[212:215],v[60:63],a[20:23],v[212:215]\n"
"ds_read_b64_tr_b16 v[52:53],v104 offset:8192\n"
"v_cvt_pk_bf16_f32 v116,v116,v117\n"
"v_mfma_f32_16x16x32_bf16 v[212:215],v[56:59],a[16:19],v[212:215]\n"
"ds_read_b64_tr_b16 v[54:55],v104 offset:9216\n"
"v_cvt_pk_bf16_f32 v117,v118,v119\n"
"v_mfma_f32_16x16x32_bf16 v[180:183],v[56:59],a[8:11],v[180:183]\n"
"buffer_load_dwordx4 v112,s[24:27],s76 offen offset:3072 sc1 lds\n"
"v_mfma_f32_16x16x32_bf16 v[180:183],v[60:63],a[12:15],v[180:183]\n"
"s_add_u32 m0,s78,16384\n"
"s_xor_b32 s76,s76,16384\n"
"ds_read_b128 v[16:19],v96 offset:34816\n"
"v_mfma_f32_16x16x32_bf16 v[148:151],v[60:63],a[4:7],v[148:151]\n"
"buffer_load_dwordx4 v112,s[24:27],s76 offen offset:0 sc1 lds\n"
"v_mfma_f32_16x16x32_bf16 v[148:151],v[56:59],a[0:3],v[148:151]\n"
"ds_read_b128 v[24:27],v96 offset:35840\n"
"v_cvt_pk_bf16_f32 v118,v120,v121\n"
"v_mfma_f32_16x16x32_bf16 v[152:155],v[64:67],a[0:3],v[152:155]\n"
"ds_read_b64_tr_b16 v[56:57],v105 offset:0\n"
"v_cvt_pk_bf16_f32 v119,v122,v123\n"
"v_mfma_f32_16x16x32_bf16 v[152:155],v[68:71],a[4:7],v[152:155]\n"
"ds_read_b64_tr_b16 v[58:59],v105 offset:1024\n"
"buffer_store_dwordx4 v[116:119],v112,s[24:27],s56 offen offset:2048 sc1\n"
"v_mfma_f32_16x16x32_bf16 v[184:187],v[68:71],a[12:15],v[184:187]\n"
"ds_read_b64_tr_b16 v[60:61],v105 offset:8192\n"
"v_accvgpr_read_b32 v116,a88\n"
"v_mfma_f32_16x16x32_bf16 v[184:187],v[64:67],a[8:11],v[184:187]\n"
"ds_read_b64_tr_b16 v[62:63],v105 offset:9216\n"
"v_accvgpr_read_b32 v117,a89\n"
"v_mfma_f32_16x16x32_bf16 v[216:219],v[64:67],a[16:19],v[216:219]\n"
"ds_read_b128 v[4:7],v96 offset:49152\n"
"v_accvgpr_read_b32 v118,a90\n"
"v_mfma_f32_16x16x32_bf16 v[216:219],v[68:71],a[20:23],v[216:219]\n"
"ds_read_b128 v[12:15],v96 offset:50176\n"
"v_accvgpr_read_b32 v119,a91\n"
"v_mfma_f32_16x16x32_bf16 v[248:251],v[68:71],a[28:31],v[248:251]\n"
"ds_read_b128 v[20:23],v96 offset:51200\n"
"v_accvgpr_read_b32 v120,a92\n"
"v_mfma_f32_16x16x32_bf16 v[248:251],v[64:67],a[24:27],v[248:251]\n"
"ds_read_b128 v[28:31],v96 offset:52224\n"
"v_accvgpr_read_b32 v121,a93\n"
"v_mfma_f32_16x16x32_bf16 v[252:255],v[72:75],a[24:27],v[252:255]\n"
"buffer_load_dwordx4 v112,s[24:27],s76 offen offset:1024 sc1 lds\n"
"v_mfma_f32_16x16x32_bf16 v[252:255],v[76:79],a[28:31],v[252:255]\n"
"v_accvgpr_read_b32 v122,a94\n"
"v_accvgpr_read_b32 v123,a95\n"
"v_mfma_f32_16x16x32_bf16 v[220:223],v[76:79],a[20:23],v[220:223]\n"
"v_cvt_pk_bf16_f32 v116,v116,v117\n"
"v_cvt_pk_bf16_f32 v117,v118,v119\n"
"v_mfma_f32_16x16x32_bf16 v[220:223],v[72:75],a[16:19],v[220:223]\n"
"buffer_load_dwordx4 v112,s[24:27],s76 offen offset:2048 sc1 lds\n"
"v_mfma_f32_16x16x32_bf16 v[188:191],v[72:75],a[8:11],v[188:191]\n"
"v_cvt_pk_bf16_f32 v118,v120,v121\n"
"v_cvt_pk_bf16_f32 v119,v122,v123\n"
"v_mfma_f32_16x16x32_bf16 v[188:191],v[76:79],a[12:15],v[188:191]\n"
"buffer_store_dwordx4 v[116:119],v112,s[24:27],s56 offen offset:3072 sc1\n"
"v_mfma_f32_16x16x32_bf16 v[156:159],v[76:79],a[4:7],v[156:159]\n"
"buffer_load_dwordx4 v112,s[24:27],s76 offen offset:3072 sc1 lds\n"
"v_accvgpr_read_b32 v116,a96\n"
"v_mfma_f32_16x16x32_bf16 v[156:159],v[72:75],a[0:3],v[156:159]\n"
"s_xor_b32 s76,s76,49152\n"
"v_accvgpr_read_b32 v117,a97\n"
"s_waitcnt vmcnt(27) lgkmcnt(0)\n"
"s_barrier\n"
"v_mfma_f32_16x16x32_bf16 v[128:131],v[80:83],v[0:3],v[128:131]\n"
"ds_read_b64_tr_b16 v[64:65],v98 offset:49152\n"
"ds_read_b64_tr_b16 v[72:73],v99 offset:49152\n"
"v_mfma_f32_16x16x32_bf16 v[128:131],v[84:87],v[4:7],v[128:131]\n"
"ds_read_b64_tr_b16 v[66:67],v98 offset:50176\n"
"ds_read_b64_tr_b16 v[74:75],v99 offset:50176\n"
"v_mfma_f32_16x16x32_bf16 v[160:163],v[84:87],v[12:15],v[160:163]\n"
"ds_read_b64_tr_b16 v[68:69],v98 offset:57344\n"
"ds_read_b64_tr_b16 v[76:77],v99 offset:57344\n"
"v_mfma_f32_16x16x32_bf16 v[160:163],v[80:83],v[8:11],v[160:163]\n"
"s_add_u32 m0,s85,98304\n"
"ds_read_b64_tr_b16 v[70:71],v98 offset:58368\n"
"ds_read_b64_tr_b16 v[78:79],v99 offset:58368\n"
"v_mfma_f32_16x16x32_bf16 v[192:195],v[80:83],v[16:19],v[192:195]\n"
"buffer_load_dwordx4 v110,s[28:31],s86 offen lds\n"
"v_mfma_f32_16x16x32_bf16 v[192:195],v[84:87],v[20:23],v[192:195]\n"
"v_accvgpr_read_b32 v118,a98\n"
"v_accvgpr_read_b32 v119,a99\n"
"v_mfma_f32_16x16x32_bf16 v[224:227],v[84:87],v[28:31],v[224:227]\n"
"v_accvgpr_read_b32 v120,a100\n"
"v_accvgpr_read_b32 v121,a101\n"
"v_mfma_f32_16x16x32_bf16 v[224:227],v[80:83],v[24:27],v[224:227]\n"
"v_accvgpr_read_b32 v122,a102\n"
"v_accvgpr_read_b32 v123,a103\n"
"v_mfma_f32_16x16x32_bf16 v[228:231],v[88:91],v[24:27],v[228:231]\n"
"ds_read_b64_tr_b16 v[80:81],v100 offset:49152\n"
"v_cvt_pk_bf16_f32 v116,v116,v117\n"
"v_mfma_f32_16x16x32_bf16 v[228:231],v[92:95],v[28:31],v[228:231]\n"
"ds_read_b64_tr_b16 v[82:83],v100 offset:50176\n"
"v_cvt_pk_bf16_f32 v117,v118,v119\n"
"v_mfma_f32_16x16x32_bf16 v[196:199],v[92:95],v[20:23],v[196:199]\n"
"ds_read_b64_tr_b16 v[84:85],v100 offset:57344\n"
"v_cvt_pk_bf16_f32 v118,v120,v121\n"
"v_mfma_f32_16x16x32_bf16 v[196:199],v[88:91],v[16:19],v[196:199]\n"
"s_add_u32 m0,s85,99328\n"
"ds_read_b64_tr_b16 v[86:87],v100 offset:58368\n"
"v_mfma_f32_16x16x32_bf16 v[164:167],v[88:91],v[8:11],v[164:167]\n"
"buffer_load_dwordx4 v111,s[28:31],s86 offen lds\n"
"v_mfma_f32_16x16x32_bf16 v[164:167],v[92:95],v[12:15],v[164:167]\n"
"s_add_u32 m0,s85,106496\n"
"s_xor_b32 s86,s86,262144\n"
"v_mfma_f32_16x16x32_bf16 v[132:135],v[92:95],v[4:7],v[132:135]\n"
"buffer_load_dwordx4 v110,s[28:31],s86 offen lds\n"
"v_mfma_f32_16x16x32_bf16 v[132:135],v[88:91],v[0:3],v[132:135]\n"
"v_cvt_pk_bf16_f32 v119,v122,v123\n"
"s_add_u32 s56,s56,4096\n"
"v_mfma_f32_16x16x32_bf16 v[136:139],a[32:35],v[0:3],v[136:139]\n"
"ds_read_b64_tr_b16 v[88:89],v101 offset:49152\n"
"buffer_store_dwordx4 v[116:119],v112,s[24:27],s56 offen offset:0 sc1\n"
"v_mfma_f32_16x16x32_bf16 v[136:139],a[36:39],v[4:7],v[136:139]\n"
"ds_read_b64_tr_b16 v[90:91],v101 offset:50176\n"
"v_accvgpr_read_b32 v116,a104\n"
"v_mfma_f32_16x16x32_bf16 v[168:171],a[36:39],v[12:15],v[168:171]\n"
"ds_read_b64_tr_b16 v[92:93],v101 offset:57344\n"
"v_accvgpr_read_b32 v117,a105\n"
"v_mfma_f32_16x16x32_bf16 v[168:171],a[32:35],v[8:11],v[168:171]\n"
"s_add_u32 m0,s85,107520\n"
"ds_read_b64_tr_b16 v[94:95],v101 offset:58368\n"
"v_mfma_f32_16x16x32_bf16 v[200:203],a[32:35],v[16:19],v[200:203]\n"
"buffer_load_dwordx4 v111,s[28:31],s86 offen lds\n"
"v_mfma_f32_16x16x32_bf16 v[200:203],a[36:39],v[20:23],v[200:203]\n"
"s_xor_b32 s86,s86,1835008\n"
"v_accvgpr_read_b32 v118,a106\n"
"v_mfma_f32_16x16x32_bf16 v[232:235],a[36:39],v[28:31],v[232:235]\n"
"v_accvgpr_read_b32 v119,a107\n"
"v_accvgpr_read_b32 v120,a108\n"
"v_mfma_f32_16x16x32_bf16 v[232:235],a[32:35],v[24:27],v[232:235]\n"
"v_accvgpr_read_b32 v121,a109\n"
"v_accvgpr_read_b32 v122,a110\n"
"v_mfma_f32_16x16x32_bf16 v[236:239],a[40:43],v[24:27],v[236:239]\n"
"ds_read_b64_tr_b16 a[32:33],v102 offset:49152\n"
"v_accvgpr_read_b32 v123,a111\n"
"v_mfma_f32_16x16x32_bf16 v[236:239],a[44:47],v[28:31],v[236:239]\n"
"ds_read_b64_tr_b16 a[34:35],v102 offset:50176\n"
"v_cvt_pk_bf16_f32 v116,v116,v117\n"
"v_mfma_f32_16x16x32_bf16 v[204:207],a[44:47],v[20:23],v[204:207]\n"
"ds_read_b64_tr_b16 a[36:37],v102 offset:57344\n"
"v_cvt_pk_bf16_f32 v117,v118,v119\n"
"v_mfma_f32_16x16x32_bf16 v[204:207],a[40:43],v[16:19],v[204:207]\n"
"s_add_u32 m0,s78,32768\n"
"ds_read_b64_tr_b16 a[38:39],v102 offset:58368\n"
"v_mfma_f32_16x16x32_bf16 v[172:175],a[40:43],v[8:11],v[172:175]\n"
"buffer_load_dwordx4 v112,s[24:27],s76 offen offset:0 sc1 lds\n"
"v_mfma_f32_16x16x32_bf16 v[172:175],a[44:47],v[12:15],v[172:175]\n"
"v_cvt_pk_bf16_f32 v118,v120,v121\n"
"v_cvt_pk_bf16_f32 v119,v122,v123\n"
"v_mfma_f32_16x16x32_bf16 v[140:143],a[44:47],v[4:7],v[140:143]\n"
"buffer_load_dwordx4 v112,s[24:27],s76 offen offset:1024 sc1 lds\n"
"buffer_store_dwordx4 v[116:119],v112,s[24:27],s56 offen offset:1024 sc1\n"
"v_mfma_f32_16x16x32_bf16 v[140:143],a[40:43],v[0:3],v[140:143]\n"
"s_nop 0\n"
"v_accvgpr_read_b32 v116,a112\n"
"s_waitcnt vmcnt(25)\n"
"v_mfma_f32_16x16x32_bf16 v[144:147],v[32:35],v[0:3],v[144:147]\n"
"ds_read_b64_tr_b16 a[40:41],v103 offset:49152\n"
"v_accvgpr_read_b32 v117,a113\n"
"v_mfma_f32_16x16x32_bf16 v[144:147],v[36:39],v[4:7],v[144:147]\n"
"ds_read_b64_tr_b16 a[42:43],v103 offset:50176\n"
"v_accvgpr_read_b32 v118,a114\n"
"v_mfma_f32_16x16x32_bf16 v[176:179],v[36:39],v[12:15],v[176:179]\n"
"ds_read_b64_tr_b16 a[44:45],v103 offset:57344\n"
"v_accvgpr_read_b32 v119,a115\n"
"v_mfma_f32_16x16x32_bf16 v[176:179],v[32:35],v[8:11],v[176:179]\n"
"ds_read_b64_tr_b16 a[46:47],v103 offset:58368\n"
"v_accvgpr_read_b32 v120,a116\n"
"v_mfma_f32_16x16x32_bf16 v[208:211],v[32:35],v[16:19],v[208:211]\n"
"buffer_load_dwordx4 v112,s[24:27],s76 offen offset:2048 sc1 lds\n"
"v_mfma_f32_16x16x32_bf16 v[208:211],v[36:39],v[20:23],v[208:211]\n"
"ds_read_b128 a[0:3],v97 offset:0\n"
"v_accvgpr_read_b32 v121,a117\n"
"v_mfma_f32_16x16x32_bf16 v[240:243],v[36:39],v[28:31],v[240:243]\n"
"v_accvgpr_read_b32 v122,a118\n"
"v_accvgpr_read_b32 v123,a119\n"
"v_mfma_f32_16x16x32_bf16 v[240:243],v[32:35],v[24:27],v[240:243]\n"
"ds_read_b128 a[8:11],v97 offset:1024\n"
"v_cvt_pk_bf16_f32 v116,v116,v117\n"
"v_mfma_f32_16x16x32_bf16 v[244:247],v[40:43],v[24:27],v[244:247]\n"
"ds_read_b64_tr_b16 v[32:33],v104 offset:49152\n"
"v_cvt_pk_bf16_f32 v117,v118,v119\n"
"v_mfma_f32_16x16x32_bf16 v[244:247],v[44:47],v[28:31],v[244:247]\n"
"ds_read_b64_tr_b16 v[34:35],v104 offset:50176\n"
"v_cvt_pk_bf16_f32 v118,v120,v121\n"
"v_mfma_f32_16x16x32_bf16 v[212:215],v[44:47],v[20:23],v[212:215]\n"
"ds_read_b64_tr_b16 v[36:37],v104 offset:57344\n"
"v_cvt_pk_bf16_f32 v119,v122,v123\n"
"v_mfma_f32_16x16x32_bf16 v[212:215],v[40:43],v[16:19],v[212:215]\n"
"ds_read_b64_tr_b16 v[38:39],v104 offset:58368\n"
"buffer_store_dwordx4 v[116:119],v112,s[24:27],s56 offen offset:2048 sc1\n"
"v_mfma_f32_16x16x32_bf16 v[180:183],v[40:43],v[8:11],v[180:183]\n"
"buffer_load_dwordx4 v112,s[24:27],s76 offen offset:3072 sc1 lds\n"
"v_mfma_f32_16x16x32_bf16 v[180:183],v[44:47],v[12:15],v[180:183]\n"
"s_add_u32 m0,s78,49152\n"
"s_xor_b32 s76,s76,16384\n"
"ds_read_b128 a[16:19],v97 offset:2048\n"
"v_mfma_f32_16x16x32_bf16 v[148:151],v[44:47],v[4:7],v[148:151]\n"
"buffer_load_dwordx4 v112,s[24:27],s76 offen offset:0 sc1 lds\n"
"v_mfma_f32_16x16x32_bf16 v[148:151],v[40:43],v[0:3],v[148:151]\n"
"ds_read_b128 a[24:27],v97 offset:3072\n"
"v_accvgpr_read_b32 v116,a120\n"
"v_mfma_f32_16x16x32_bf16 v[152:155],v[48:51],v[0:3],v[152:155]\n"
"ds_read_b64_tr_b16 v[40:41],v105 offset:49152\n"
"v_accvgpr_read_b32 v117,a121\n"
"v_mfma_f32_16x16x32_bf16 v[152:155],v[52:55],v[4:7],v[152:155]\n"
"ds_read_b64_tr_b16 v[42:43],v105 offset:50176\n"
"v_accvgpr_read_b32 v118,a122\n"
"v_mfma_f32_16x16x32_bf16 v[184:187],v[52:55],v[12:15],v[184:187]\n"
"ds_read_b64_tr_b16 v[44:45],v105 offset:57344\n"
"v_accvgpr_read_b32 v119,a123\n"
"v_mfma_f32_16x16x32_bf16 v[184:187],v[48:51],v[8:11],v[184:187]\n"
"ds_read_b64_tr_b16 v[46:47],v105 offset:58368\n"
"v_accvgpr_read_b32 v120,a124\n"
"v_mfma_f32_16x16x32_bf16 v[216:219],v[48:51],v[16:19],v[216:219]\n"
"ds_read_b128 a[4:7],v97 offset:16384\n"
"v_accvgpr_read_b32 v121,a125\n"
"v_mfma_f32_16x16x32_bf16 v[216:219],v[52:55],v[20:23],v[216:219]\n"
"ds_read_b128 a[12:15],v97 offset:17408\n"
"v_accvgpr_read_b32 v122,a126\n"
"v_mfma_f32_16x16x32_bf16 v[248:251],v[52:55],v[28:31],v[248:251]\n"
"ds_read_b128 a[20:23],v97 offset:18432\n"
"v_accvgpr_read_b32 v123,a127\n"
"v_mfma_f32_16x16x32_bf16 v[248:251],v[48:51],v[24:27],v[248:251]\n"
"ds_read_b128 a[28:31],v97 offset:19456\n"
"v_cvt_pk_bf16_f32 v116,v116,v117\n"
"v_mfma_f32_16x16x32_bf16 v[252:255],v[56:59],v[24:27],v[252:255]\n"
"buffer_load_dwordx4 v112,s[24:27],s76 offen offset:1024 sc1 lds\n"
"v_mfma_f32_16x16x32_bf16 v[252:255],v[60:63],v[28:31],v[252:255]\n"
"v_cvt_pk_bf16_f32 v117,v118,v119\n"
"v_cvt_pk_bf16_f32 v118,v120,v121\n"
"v_cvt_pk_bf16_f32 v119,v122,v123\n"
"v_mfma_f32_16x16x32_bf16 v[220:223],v[60:63],v[20:23],v[220:223]\n"
"buffer_store_dwordx4 v[116:119],v112,s[24:27],s56 offen offset:3072 sc1\n"
"v_mfma_f32_16x16x32_bf16 v[220:223],v[56:59],v[16:19],v[220:223]\n"
"buffer_load_dwordx4 v112,s[24:27],s76 offen offset:2048 sc1 lds\n"
"v_mfma_f32_16x16x32_bf16 v[188:191],v[56:59],v[8:11],v[188:191]\n"
"v_accvgpr_read_b32 v116,a128\n"
"v_accvgpr_read_b32 v117,a129\n"
"v_mfma_f32_16x16x32_bf16 v[188:191],v[60:63],v[12:15],v[188:191]\n"
"v_accvgpr_read_b32 v118,a130\n"
"v_accvgpr_read_b32 v119,a131\n"
"v_mfma_f32_16x16x32_bf16 v[156:159],v[60:63],v[4:7],v[156:159]\n"
"buffer_load_dwordx4 v112,s[24:27],s76 offen offset:3072 sc1 lds\n"
"v_mfma_f32_16x16x32_bf16 v[156:159],v[56:59],v[0:3],v[156:159]\n"
"s_xor_b32 s76,s76,114688\n"
"v_accvgpr_read_b32 v120,a132\n"
"s_waitcnt vmcnt(28) lgkmcnt(0)\n"
"s_barrier\n"
"v_mfma_f32_16x16x32_bf16 v[128:131],v[64:67],a[0:3],v[128:131]\n"
"ds_read_b64_tr_b16 v[48:49],v98 offset:32768\n"
"ds_read_b64_tr_b16 v[56:57],v99 offset:32768\n"
"v_mfma_f32_16x16x32_bf16 v[128:131],v[68:71],a[4:7],v[128:131]\n"
"ds_read_b64_tr_b16 v[50:51],v98 offset:33792\n"
"ds_read_b64_tr_b16 v[58:59],v99 offset:33792\n"
"v_mfma_f32_16x16x32_bf16 v[160:163],v[68:71],a[12:15],v[160:163]\n"
"ds_read_b64_tr_b16 v[52:53],v98 offset:40960\n"
"ds_read_b64_tr_b16 v[60:61],v99 offset:40960\n"
"v_mfma_f32_16x16x32_bf16 v[160:163],v[64:67],a[8:11],v[160:163]\n"
"s_add_u32 m0,s85,147456\n"
"ds_read_b64_tr_b16 v[54:55],v98 offset:41984\n"
"ds_read_b64_tr_b16 v[62:63],v99 offset:41984\n"
"v_mfma_f32_16x16x32_bf16 v[192:195],v[64:67],a[16:19],v[192:195]\n"
"buffer_load_dwordx4 v110,s[28:31],s86 offen lds\n"
"v_mfma_f32_16x16x32_bf16 v[192:195],v[68:71],a[20:23],v[192:195]\n"
"v_accvgpr_read_b32 v121,a133\n"
"v_accvgpr_read_b32 v122,a134\n"
"v_mfma_f32_16x16x32_bf16 v[224:227],v[68:71],a[28:31],v[224:227]\n"
"v_accvgpr_read_b32 v123,a135\n"
"v_cvt_pk_bf16_f32 v116,v116,v117\n"
"v_mfma_f32_16x16x32_bf16 v[224:227],v[64:67],a[24:27],v[224:227]\n"
"v_cvt_pk_bf16_f32 v117,v118,v119\n"
"v_cvt_pk_bf16_f32 v118,v120,v121\n"
"v_mfma_f32_16x16x32_bf16 v[228:231],v[72:75],a[24:27],v[228:231]\n"
"ds_read_b64_tr_b16 v[64:65],v100 offset:32768\n"
"v_cvt_pk_bf16_f32 v119,v122,v123\n"
"v_mfma_f32_16x16x32_bf16 v[228:231],v[76:79],a[28:31],v[228:231]\n"
"ds_read_b64_tr_b16 v[66:67],v100 offset:33792\n"
"s_add_u32 s56,s56,126976\n"
"v_mfma_f32_16x16x32_bf16 v[196:199],v[76:79],a[20:23],v[196:199]\n"
"ds_read_b64_tr_b16 v[68:69],v100 offset:40960\n"
"buffer_store_dwordx4 v[116:119],v112,s[24:27],s56 offen offset:0 sc1\n"
"v_mfma_f32_16x16x32_bf16 v[196:199],v[72:75],a[16:19],v[196:199]\n"
"s_add_u32 m0,s85,148480\n"
"ds_read_b64_tr_b16 v[70:71],v100 offset:41984\n"
"v_mfma_f32_16x16x32_bf16 v[164:167],v[72:75],a[8:11],v[164:167]\n"
"buffer_load_dwordx4 v111,s[28:31],s86 offen lds\n"
"v_mfma_f32_16x16x32_bf16 v[164:167],v[76:79],a[12:15],v[164:167]\n"
"s_add_u32 m0,s85,155648\n"
"s_xor_b32 s86,s86,262144\n"
"v_mfma_f32_16x16x32_bf16 v[132:135],v[76:79],a[4:7],v[132:135]\n"
"buffer_load_dwordx4 v110,s[28:31],s86 offen lds\n"
"v_mfma_f32_16x16x32_bf16 v[132:135],v[72:75],a[0:3],v[132:135]\n"
"v_accvgpr_read_b32 v116,a136\n"
"v_accvgpr_read_b32 v117,a137\n"
"v_mfma_f32_16x16x32_bf16 v[136:139],v[80:83],a[0:3],v[136:139]\n"
"ds_read_b64_tr_b16 v[72:73],v101 offset:32768\n"
"v_accvgpr_read_b32 v118,a138\n"
"v_mfma_f32_16x16x32_bf16 v[136:139],v[84:87],a[4:7],v[136:139]\n"
"ds_read_b64_tr_b16 v[74:75],v101 offset:33792\n"
"v_accvgpr_read_b32 v119,a139\n"
"v_mfma_f32_16x16x32_bf16 v[168:171],v[84:87],a[12:15],v[168:171]\n"
"ds_read_b64_tr_b16 v[76:77],v101 offset:40960\n"
"v_accvgpr_read_b32 v120,a140\n"
"v_mfma_f32_16x16x32_bf16 v[168:171],v[80:83],a[8:11],v[168:171]\n"
"s_add_u32 m0,s85,156672\n"
"ds_read_b64_tr_b16 v[78:79],v101 offset:41984\n"
"v_mfma_f32_16x16x32_bf16 v[200:203],v[80:83],a[16:19],v[200:203]\n"
"buffer_load_dwordx4 v111,s[28:31],s86 offen lds\n"
"v_mfma_f32_16x16x32_bf16 v[200:203],v[84:87],a[20:23],v[200:203]\n"
"s_xor_b32 s86,s86,786432\n"
"v_accvgpr_read_b32 v121,a141\n"
"v_mfma_f32_16x16x32_bf16 v[232:235],v[84:87],a[28:31],v[232:235]\n"
"v_accvgpr_read_b32 v122,a142\n"
"v_accvgpr_read_b32 v123,a143\n"
"v_mfma_f32_16x16x32_bf16 v[232:235],v[80:83],a[24:27],v[232:235]\n"
"v_cvt_pk_bf16_f32 v116,v116,v117\n"
"v_cvt_pk_bf16_f32 v117,v118,v119\n"
"v_mfma_f32_16x16x32_bf16 v[236:239],v[88:91],a[24:27],v[236:239]\n"
"ds_read_b64_tr_b16 v[80:81],v102 offset:32768\n"
"v_cvt_pk_bf16_f32 v118,v120,v121\n"
"v_mfma_f32_16x16x32_bf16 v[236:239],v[92:95],a[28:31],v[236:239]\n"
"ds_read_b64_tr_b16 v[82:83],v102 offset:33792\n"
"v_cvt_pk_bf16_f32 v119,v122,v123\n"
"v_mfma_f32_16x16x32_bf16 v[204:207],v[92:95],a[20:23],v[204:207]\n"
"ds_read_b64_tr_b16 v[84:85],v102 offset:40960\n"
"buffer_store_dwordx4 v[116:119],v112,s[24:27],s56 offen offset:1024 sc1\n"
"v_mfma_f32_16x16x32_bf16 v[204:207],v[88:91],a[16:19],v[204:207]\n"
"s_add_u32 m0,s78,65536\n"
"ds_read_b64_tr_b16 v[86:87],v102 offset:41984\n"
"v_accvgpr_read_b32 v118,a146\n"
"v_mfma_f32_16x16x32_bf16 v[172:175],v[88:91],a[8:11],v[172:175]\n"
"buffer_load_dwordx4 v112,s[24:27],s76 offen offset:0 sc1 lds\n"
"v_mfma_f32_16x16x32_bf16 v[172:175],v[92:95],a[12:15],v[172:175]\n"
"v_accvgpr_read_b32 v116,a144\n"
"v_accvgpr_read_b32 v117,a145\n"
"v_mfma_f32_16x16x32_bf16 v[140:143],v[92:95],a[4:7],v[140:143]\n"
"buffer_load_dwordx4 v112,s[24:27],s76 offen offset:1024 sc1 lds\n"
"v_mfma_f32_16x16x32_bf16 v[140:143],v[88:91],a[0:3],v[140:143]\n"
"v_accvgpr_read_b32 v119,a147\n"
"s_waitcnt vmcnt(24)\n"
"v_mfma_f32_16x16x32_bf16 v[144:147],a[32:35],a[0:3],v[144:147]\n"
"ds_read_b64_tr_b16 v[88:89],v103 offset:32768\n"
"v_accvgpr_read_b32 v120,a148\n"
"v_mfma_f32_16x16x32_bf16 v[144:147],a[36:39],a[4:7],v[144:147]\n"
"ds_read_b64_tr_b16 v[90:91],v103 offset:33792\n"
"v_accvgpr_read_b32 v121,a149\n"
"v_mfma_f32_16x16x32_bf16 v[176:179],a[36:39],a[12:15],v[176:179]\n"
"ds_read_b64_tr_b16 v[92:93],v103 offset:40960\n"
"v_accvgpr_read_b32 v122,a150\n"
"v_mfma_f32_16x16x32_bf16 v[176:179],a[32:35],a[8:11],v[176:179]\n"
"ds_read_b64_tr_b16 v[94:95],v103 offset:41984\n"
"v_accvgpr_read_b32 v123,a151\n"
"v_mfma_f32_16x16x32_bf16 v[208:211],a[32:35],a[16:19],v[208:211]\n"
"buffer_load_dwordx4 v112,s[24:27],s76 offen offset:2048 sc1 lds\n"
"v_mfma_f32_16x16x32_bf16 v[208:211],a[36:39],a[20:23],v[208:211]\n"
"ds_read_b128 v[0:3],v96 offset:0\n"
"v_cvt_pk_bf16_f32 v116,v116,v117\n"
"v_mfma_f32_16x16x32_bf16 v[240:243],a[36:39],a[28:31],v[240:243]\n"
"v_cvt_pk_bf16_f32 v117,v118,v119\n"
"v_cvt_pk_bf16_f32 v118,v120,v121\n"
"v_mfma_f32_16x16x32_bf16 v[240:243],a[32:35],a[24:27],v[240:243]\n"
"ds_read_b128 v[8:11],v96 offset:1024\n"
"v_cvt_pk_bf16_f32 v119,v122,v123\n"
"v_mfma_f32_16x16x32_bf16 v[244:247],a[40:43],a[24:27],v[244:247]\n"
"ds_read_b64_tr_b16 a[32:33],v104 offset:32768\n"
"buffer_store_dwordx4 v[116:119],v112,s[24:27],s56 offen offset:2048 sc1\n"
"v_mfma_f32_16x16x32_bf16 v[244:247],a[44:47],a[28:31],v[244:247]\n"
"ds_read_b64_tr_b16 a[34:35],v104 offset:33792\n"
"v_accvgpr_read_b32 v116,a152\n"
"v_mfma_f32_16x16x32_bf16 v[212:215],a[44:47],a[20:23],v[212:215]\n"
"ds_read_b64_tr_b16 a[36:37],v104 offset:40960\n"
"v_accvgpr_read_b32 v117,a153\n"
"v_mfma_f32_16x16x32_bf16 v[212:215],a[40:43],a[16:19],v[212:215]\n"
"ds_read_b64_tr_b16 a[38:39],v104 offset:41984\n"
"v_accvgpr_read_b32 v118,a154\n"
"v_mfma_f32_16x16x32_bf16 v[180:183],a[40:43],a[8:11],v[180:183]\n"
"buffer_load_dwordx4 v112,s[24:27],s76 offen offset:3072 sc1 lds\n"
"v_mfma_f32_16x16x32_bf16 v[180:183],a[44:47],a[12:15],v[180:183]\n"
"s_add_u32 m0,s78,81920\n"
"s_xor_b32 s76,s76,16384\n"
"ds_read_b128 v[16:19],v96 offset:2048\n"
"v_mfma_f32_16x16x32_bf16 v[148:151],a[44:47],a[4:7],v[148:151]\n"
"buffer_load_dwordx4 v112,s[24:27],s76 offen offset:0 sc1 lds\n"
"v_mfma_f32_16x16x32_bf16 v[148:151],a[40:43],a[0:3],v[148:151]\n"
"ds_read_b128 v[24:27],v96 offset:3072\n"
"v_accvgpr_read_b32 v119,a155\n"
"v_mfma_f32_16x16x32_bf16 v[152:155],v[32:35],a[0:3],v[152:155]\n"
"ds_read_b64_tr_b16 a[40:41],v105 offset:32768\n"
"v_accvgpr_read_b32 v120,a156\n"
"v_mfma_f32_16x16x32_bf16 v[152:155],v[36:39],a[4:7],v[152:155]\n"
"ds_read_b64_tr_b16 a[42:43],v105 offset:33792\n"
"v_accvgpr_read_b32 v121,a157\n"
"v_mfma_f32_16x16x32_bf16 v[184:187],v[36:39],a[12:15],v[184:187]\n"
"ds_read_b64_tr_b16 a[44:45],v105 offset:40960\n"
"v_accvgpr_read_b32 v122,a158\n"
"v_mfma_f32_16x16x32_bf16 v[184:187],v[32:35],a[8:11],v[184:187]\n"
"ds_read_b64_tr_b16 a[46:47],v105 offset:41984\n"
"v_accvgpr_read_b32 v123,a159\n"
"v_mfma_f32_16x16x32_bf16 v[216:219],v[32:35],a[16:19],v[216:219]\n"
"ds_read_b128 v[4:7],v96 offset:16384\n"
"v_cvt_pk_bf16_f32 v116,v116,v117\n"
"v_mfma_f32_16x16x32_bf16 v[216:219],v[36:39],a[20:23],v[216:219]\n"
"ds_read_b128 v[12:15],v96 offset:17408\n"
"v_cvt_pk_bf16_f32 v117,v118,v119\n"
"v_mfma_f32_16x16x32_bf16 v[248:251],v[36:39],a[28:31],v[248:251]\n"
"ds_read_b128 v[20:23],v96 offset:18432\n"
"v_cvt_pk_bf16_f32 v118,v120,v121\n"
"v_mfma_f32_16x16x32_bf16 v[248:251],v[32:35],a[24:27],v[248:251]\n"
"ds_read_b128 v[28:31],v96 offset:19456\n"
"v_cvt_pk_bf16_f32 v119,v122,v123\n"
"v_mfma_f32_16x16x32_bf16 v[252:255],v[40:43],a[24:27],v[252:255]\n"
"buffer_load_dwordx4 v112,s[24:27],s76 offen offset:1024 sc1 lds\n"
"buffer_store_dwordx4 v[116:119],v112,s[24:27],s56 offen offset:3072 sc1\n"
"v_mfma_f32_16x16x32_bf16 v[252:255],v[44:47],a[28:31],v[252:255]\n"
"s_nop 0\n"
"v_accvgpr_read_b32 v116,a160\n"
"v_mfma_f32_16x16x32_bf16 v[220:223],v[44:47],a[20:23],v[220:223]\n"
"v_accvgpr_read_b32 v117,a161\n"
"v_accvgpr_read_b32 v118,a162\n"
"v_mfma_f32_16x16x32_bf16 v[220:223],v[40:43],a[16:19],v[220:223]\n"
"buffer_load_dwordx4 v112,s[24:27],s76 offen offset:2048 sc1 lds\n"
"v_mfma_f32_16x16x32_bf16 v[188:191],v[40:43],a[8:11],v[188:191]\n"
"v_accvgpr_read_b32 v119,a163\n"
"v_accvgpr_read_b32 v120,a164\n"
"v_mfma_f32_16x16x32_bf16 v[188:191],v[44:47],a[12:15],v[188:191]\n"
"v_accvgpr_read_b32 v121,a165\n"
"v_accvgpr_read_b32 v122,a166\n"
"v_mfma_f32_16x16x32_bf16 v[156:159],v[44:47],a[4:7],v[156:159]\n"
"buffer_load_dwordx4 v112,s[24:27],s76 offen offset:3072 sc1 lds\n"
"v_mfma_f32_16x16x32_bf16 v[156:159],v[40:43],a[0:3],v[156:159]\n"
"s_xor_b32 s76,s76,49152\n"
"v_accvgpr_read_b32 v123,a167\n"
"s_waitcnt vmcnt(27) lgkmcnt(0)\n"
"s_barrier\n"
"v_mfma_f32_16x16x32_bf16 v[128:131],v[48:51],v[0:3],v[128:131]\n"
"ds_read_b64_tr_b16 v[32:33],v98 offset:0\n"
"ds_read_b64_tr_b16 v[40:41],v99 offset:0\n"
"v_mfma_f32_16x16x32_bf16 v[128:131],v[52:55],v[4:7],v[128:131]\n"
"ds_read_b64_tr_b16 v[34:35],v98 offset:1024\n"
"ds_read_b64_tr_b16 v[42:43],v99 offset:1024\n"
"v_mfma_f32_16x16x32_bf16 v[160:163],v[52:55],v[12:15],v[160:163]\n"
"ds_read_b64_tr_b16 v[36:37],v98 offset:8192\n"
"ds_read_b64_tr_b16 v[44:45],v99 offset:8192\n"
"v_mfma_f32_16x16x32_bf16 v[160:163],v[48:51],v[8:11],v[160:163]\n"
"s_add_u32 m0,s85,131072\n"
"ds_read_b64_tr_b16 v[38:39],v98 offset:9216\n"
"ds_read_b64_tr_b16 v[46:47],v99 offset:9216\n"
"v_mfma_f32_16x16x32_bf16 v[192:195],v[48:51],v[16:19],v[192:195]\n"
"buffer_load_dwordx4 v110,s[28:31],s86 offen lds\n"
"v_mfma_f32_16x16x32_bf16 v[192:195],v[52:55],v[20:23],v[192:195]\n"
"v_cvt_pk_bf16_f32 v116,v116,v117\n"
"v_cvt_pk_bf16_f32 v117,v118,v119\n"
"v_mfma_f32_16x16x32_bf16 v[224:227],v[52:55],v[28:31],v[224:227]\n"
"s_add_u32 s56,s56,4096\n"
"v_cvt_pk_bf16_f32 v118,v120,v121\n"
"v_cvt_pk_bf16_f32 v119,v122,v123\n"
"v_mfma_f32_16x16x32_bf16 v[224:227],v[48:51],v[24:27],v[224:227]\n"
"buffer_store_dwordx4 v[116:119],v112,s[24:27],s56 offen offset:0 sc1\n"
"v_mfma_f32_16x16x32_bf16 v[228:231],v[56:59],v[24:27],v[228:231]\n"
"ds_read_b64_tr_b16 v[48:49],v100 offset:0\n"
"v_accvgpr_read_b32 v116,a168\n"
"v_mfma_f32_16x16x32_bf16 v[228:231],v[60:63],v[28:31],v[228:231]\n"
"ds_read_b64_tr_b16 v[50:51],v100 offset:1024\n"
"v_accvgpr_read_b32 v117,a169\n"
"v_mfma_f32_16x16x32_bf16 v[196:199],v[60:63],v[20:23],v[196:199]\n"
"ds_read_b64_tr_b16 v[52:53],v100 offset:8192\n"
"v_accvgpr_read_b32 v118,a170\n"
"v_mfma_f32_16x16x32_bf16 v[196:199],v[56:59],v[16:19],v[196:199]\n"
"s_add_u32 m0,s85,132096\n"
"ds_read_b64_tr_b16 v[54:55],v100 offset:9216\n"
"v_mfma_f32_16x16x32_bf16 v[164:167],v[56:59],v[8:11],v[164:167]\n"
"buffer_load_dwordx4 v111,s[28:31],s86 offen lds\n"
"v_mfma_f32_16x16x32_bf16 v[164:167],v[60:63],v[12:15],v[164:167]\n"
"s_add_u32 m0,s85,139264\n"
"s_xor_b32 s86,s86,262144\n"
"v_mfma_f32_16x16x32_bf16 v[132:135],v[60:63],v[4:7],v[132:135]\n"
"buffer_load_dwordx4 v110,s[28:31],s86 offen lds\n"
"v_mfma_f32_16x16x32_bf16 v[132:135],v[56:59],v[0:3],v[132:135]\n"
"v_accvgpr_read_b32 v119,a171\n"
"v_accvgpr_read_b32 v120,a172\n"
"v_mfma_f32_16x16x32_bf16 v[136:139],v[64:67],v[0:3],v[136:139]\n"
"ds_read_b64_tr_b16 v[56:57],v101 offset:0\n"
"v_accvgpr_read_b32 v121,a173\n"
"v_mfma_f32_16x16x32_bf16 v[136:139],v[68:71],v[4:7],v[136:139]\n"
"ds_read_b64_tr_b16 v[58:59],v101 offset:1024\n"
"v_accvgpr_read_b32 v122,a174\n"
"v_mfma_f32_16x16x32_bf16 v[168:171],v[68:71],v[12:15],v[168:171]\n"
"ds_read_b64_tr_b16 v[60:61],v101 offset:8192\n"
"v_accvgpr_read_b32 v123,a175\n"
"v_mfma_f32_16x16x32_bf16 v[168:171],v[64:67],v[8:11],v[168:171]\n"
"s_add_u32 m0,s85,140288\n"
"ds_read_b64_tr_b16 v[62:63],v101 offset:9216\n"
"v_mfma_f32_16x16x32_bf16 v[200:203],v[64:67],v[16:19],v[200:203]\n"
"buffer_load_dwordx4 v111,s[28:31],s86 offen lds\n"
"v_mfma_f32_16x16x32_bf16 v[200:203],v[68:71],v[20:23],v[200:203]\n"
"s_xor_b32 s86,s86,8126464\n"
"v_cvt_pk_bf16_f32 v116,v116,v117\n"
"v_cvt_pk_bf16_f32 v117,v118,v119\n"
"v_mfma_f32_16x16x32_bf16 v[232:235],v[68:71],v[28:31],v[232:235]\n"
"v_cvt_pk_bf16_f32 v118,v120,v121\n"
"v_cvt_pk_bf16_f32 v119,v122,v123\n"
"v_mfma_f32_16x16x32_bf16 v[232:235],v[64:67],v[24:27],v[232:235]\n"
"buffer_store_dwordx4 v[116:119],v112,s[24:27],s56 offen offset:1024 sc1\n"
"v_mfma_f32_16x16x32_bf16 v[236:239],v[72:75],v[24:27],v[236:239]\n"
"ds_read_b64_tr_b16 v[64:65],v102 offset:0\n"
"v_accvgpr_read_b32 v116,a176\n"
"v_mfma_f32_16x16x32_bf16 v[236:239],v[76:79],v[28:31],v[236:239]\n"
"ds_read_b64_tr_b16 v[66:67],v102 offset:1024\n"
"v_accvgpr_read_b32 v117,a177\n"
"v_mfma_f32_16x16x32_bf16 v[204:207],v[76:79],v[20:23],v[204:207]\n"
"ds_read_b64_tr_b16 v[68:69],v102 offset:8192\n"
"v_accvgpr_read_b32 v118,a178\n"
"v_mfma_f32_16x16x32_bf16 v[204:207],v[72:75],v[16:19],v[204:207]\n"
"s_add_u32 m0,s78,0\n"
"ds_read_b64_tr_b16 v[70:71],v102 offset:9216\n"
"v_accvgpr_read_b32 v121,a181\n"
"v_mfma_f32_16x16x32_bf16 v[172:175],v[72:75],v[8:11],v[172:175]\n"
"buffer_load_dwordx4 v112,s[24:27],s76 offen offset:0 sc1 lds\n"
"v_mfma_f32_16x16x32_bf16 v[172:175],v[76:79],v[12:15],v[172:175]\n"
"v_accvgpr_read_b32 v119,a179\n"
"v_accvgpr_read_b32 v120,a180\n"
"v_mfma_f32_16x16x32_bf16 v[140:143],v[76:79],v[4:7],v[140:143]\n"
"buffer_load_dwordx4 v112,s[24:27],s76 offen offset:1024 sc1 lds\n"
"v_mfma_f32_16x16x32_bf16 v[140:143],v[72:75],v[0:3],v[140:143]\n"
"v_accvgpr_read_b32 v122,a182\n"
"s_waitcnt vmcnt(24)\n"
"v_mfma_f32_16x16x32_bf16 v[144:147],v[80:83],v[0:3],v[144:147]\n"
"ds_read_b64_tr_b16 v[72:73],v103 offset:0\n"
"v_accvgpr_read_b32 v123,a183\n"
"v_mfma_f32_16x16x32_bf16 v[144:147],v[84:87],v[4:7],v[144:147]\n"
"ds_read_b64_tr_b16 v[74:75],v103 offset:1024\n"
"v_cvt_pk_bf16_f32 v116,v116,v117\n"
"v_mfma_f32_16x16x32_bf16 v[176:179],v[84:87],v[12:15],v[176:179]\n"
"ds_read_b64_tr_b16 v[76:77],v103 offset:8192\n"
"v_cvt_pk_bf16_f32 v117,v118,v119\n"
"v_mfma_f32_16x16x32_bf16 v[176:179],v[80:83],v[8:11],v[176:179]\n"
"ds_read_b64_tr_b16 v[78:79],v103 offset:9216\n"
"v_cvt_pk_bf16_f32 v118,v120,v121\n"
"v_mfma_f32_16x16x32_bf16 v[208:211],v[80:83],v[16:19],v[208:211]\n"
"buffer_load_dwordx4 v112,s[24:27],s76 offen offset:2048 sc1 lds\n"
"v_mfma_f32_16x16x32_bf16 v[208:211],v[84:87],v[20:23],v[208:211]\n"
"ds_read_b128 a[0:3],v96 offset:32768\n"
"v_cvt_pk_bf16_f32 v119,v122,v123\n"
"v_mfma_f32_16x16x32_bf16 v[240:243],v[84:87],v[28:31],v[240:243]\n"
"buffer_store_dwordx4 v[116:119],v112,s[24:27],s56 offen offset:2048 sc1\n"
"v_mfma_f32_16x16x32_bf16 v[240:243],v[80:83],v[24:27],v[240:243]\n"
"ds_read_b128 a[8:11],v96 offset:33792\n"
"v_accvgpr_read_b32 v116,a184\n"
"v_accvgpr_read_b32 v117,a185\n"
"v_mfma_f32_16x16x32_bf16 v[244:247],v[88:91],v[24:27],v[244:247]\n"
"ds_read_b64_tr_b16 v[80:81],v104 offset:0\n"
"v_accvgpr_read_b32 v118,a186\n"
"v_mfma_f32_16x16x32_bf16 v[244:247],v[92:95],v[28:31],v[244:247]\n"
"ds_read_b64_tr_b16 v[82:83],v104 offset:1024\n"
"v_accvgpr_read_b32 v119,a187\n"
"v_mfma_f32_16x16x32_bf16 v[212:215],v[92:95],v[20:23],v[212:215]\n"
"ds_read_b64_tr_b16 v[84:85],v104 offset:8192\n"
"v_accvgpr_read_b32 v120,a188\n"
"v_mfma_f32_16x16x32_bf16 v[212:215],v[88:91],v[16:19],v[212:215]\n"
"ds_read_b64_tr_b16 v[86:87],v104 offset:9216\n"
"v_accvgpr_read_b32 v121,a189\n"
"v_mfma_f32_16x16x32_bf16 v[180:183],v[88:91],v[8:11],v[180:183]\n"
"buffer_load_dwordx4 v112,s[24:27],s76 offen offset:3072 sc1 lds\n"
"v_mfma_f32_16x16x32_bf16 v[180:183],v[92:95],v[12:15],v[180:183]\n"
"s_add_u32 m0,s78,16384\n"
"s_xor_b32 s76,s76,16384\n"
"ds_read_b128 a[16:19],v96 offset:34816\n"
"v_mfma_f32_16x16x32_bf16 v[148:151],v[92:95],v[4:7],v[148:151]\n"
"buffer_load_dwordx4 v112,s[24:27],s76 offen offset:0 sc1 lds\n"
"v_mfma_f32_16x16x32_bf16 v[148:151],v[88:91],v[0:3],v[148:151]\n"
"ds_read_b128 a[24:27],v96 offset:35840\n"
"v_accvgpr_read_b32 v122,a190\n"
"v_mfma_f32_16x16x32_bf16 v[152:155],a[32:35],v[0:3],v[152:155]\n"
"ds_read_b64_tr_b16 v[88:89],v105 offset:0\n"
"v_accvgpr_read_b32 v123,a191\n"
"v_mfma_f32_16x16x32_bf16 v[152:155],a[36:39],v[4:7],v[152:155]\n"
"ds_read_b64_tr_b16 v[90:91],v105 offset:1024\n"
"v_cvt_pk_bf16_f32 v116,v116,v117\n"
"v_mfma_f32_16x16x32_bf16 v[184:187],a[36:39],v[12:15],v[184:187]\n"
"ds_read_b64_tr_b16 v[92:93],v105 offset:8192\n"
"v_cvt_pk_bf16_f32 v117,v118,v119\n"
"v_mfma_f32_16x16x32_bf16 v[184:187],a[32:35],v[8:11],v[184:187]\n"
"ds_read_b64_tr_b16 v[94:95],v105 offset:9216\n"
"v_cvt_pk_bf16_f32 v118,v120,v121\n"
"v_mfma_f32_16x16x32_bf16 v[216:219],a[32:35],v[16:19],v[216:219]\n"
"ds_read_b128 a[4:7],v96 offset:49152\n"
"v_cvt_pk_bf16_f32 v119,v122,v123\n"
"v_mfma_f32_16x16x32_bf16 v[216:219],a[36:39],v[20:23],v[216:219]\n"
"ds_read_b128 a[12:15],v96 offset:50176\n"
"buffer_store_dwordx4 v[116:119],v112,s[24:27],s56 offen offset:3072 sc1\n"
"v_mfma_f32_16x16x32_bf16 v[248:251],a[36:39],v[28:31],v[248:251]\n"
"ds_read_b128 a[20:23],v96 offset:51200\n"
"v_accvgpr_read_b32 v116,a248\n"
"v_mfma_f32_16x16x32_bf16 v[248:251],a[32:35],v[24:27],v[248:251]\n"
"ds_read_b128 a[28:31],v96 offset:52224\n"
"v_accvgpr_read_b32 v117,a249\n"
"v_mfma_f32_16x16x32_bf16 v[252:255],a[40:43],v[24:27],v[252:255]\n"
"buffer_load_dwordx4 v112,s[24:27],s76 offen offset:1024 sc1 lds\n"
"v_mfma_f32_16x16x32_bf16 v[252:255],a[44:47],v[28:31],v[252:255]\n"
"v_accvgpr_read_b32 v118,a250\n"
"v_accvgpr_read_b32 v119,a251\n"
"v_mfma_f32_16x16x32_bf16 v[220:223],a[44:47],v[20:23],v[220:223]\n"
"v_accvgpr_read_b32 v120,a252\n"
"v_accvgpr_read_b32 v121,a253\n"
"v_mfma_f32_16x16x32_bf16 v[220:223],a[40:43],v[16:19],v[220:223]\n"
"buffer_load_dwordx4 v112,s[24:27],s76 offen offset:2048 sc1 lds\n"
"v_mfma_f32_16x16x32_bf16 v[188:191],a[40:43],v[8:11],v[188:191]\n"
"v_accvgpr_read_b32 v122,a254\n"
"v_accvgpr_read_b32 v123,a255\n"
"v_mfma_f32_16x16x32_bf16 v[188:191],a[44:47],v[12:15],v[188:191]\n"
"v_cvt_pk_bf16_f32 v116,v116,v117\n"
"v_cvt_pk_bf16_f32 v117,v118,v119\n"
"v_mfma_f32_16x16x32_bf16 v[156:159],a[44:47],v[4:7],v[156:159]\n"
"buffer_load_dwordx4 v112,s[24:27],s76 offen offset:3072 sc1 lds\n"
"v_mfma_f32_16x16x32_bf16 v[156:159],a[40:43],v[0:3],v[156:159]\n"
"s_xor_b32 s76,s76,507904\n"
"v_cvt_pk_bf16_f32 v118,v120,v121\n"
"s_waitcnt vmcnt(27) lgkmcnt(0)\n"
"s_barrier\n"
"v_mfma_f32_16x16x32_bf16 v[128:131],v[32:35],a[0:3],v[128:131]\n"
"ds_read_b64_tr_b16 a[32:33],v98 offset:49152\n"
"ds_read_b64_tr_b16 a[40:41],v99 offset:49152\n"
"v_mfma_f32_16x16x32_bf16 v[128:131],v[36:39],a[4:7],v[128:131]\n"
"ds_read_b64_tr_b16 a[34:35],v98 offset:50176\n"
"ds_read_b64_tr_b16 a[42:43],v99 offset:50176\n"
"v_mfma_f32_16x16x32_bf16 v[160:163],v[36:39],a[12:15],v[160:163]\n"
"ds_read_b64_tr_b16 a[36:37],v98 offset:57344\n"
"ds_read_b64_tr_b16 a[44:45],v99 offset:57344\n"
"v_mfma_f32_16x16x32_bf16 v[160:163],v[32:35],a[8:11],v[160:163]\n"
"s_add_u32 m0,s85,98304\n"
"ds_read_b64_tr_b16 a[38:39],v98 offset:58368\n"
"ds_read_b64_tr_b16 a[46:47],v99 offset:58368\n"
"v_mfma_f32_16x16x32_bf16 v[192:195],v[32:35],a[16:19],v[192:195]\n"
"buffer_load_dwordx4 v110,s[28:31],s86 offen lds\n"
"v_mfma_f32_16x16x32_bf16 v[192:195],v[36:39],a[20:23],v[192:195]\n"
"v_cvt_pk_bf16_f32 v119,v122,v123\n"
"v_accvgpr_write_b32 a252,v116\n"
"v_mfma_f32_16x16x32_bf16 v[224:227],v[36:39],a[28:31],v[224:227]\n"
"v_accvgpr_write_b32 a253,v117\n"
"v_accvgpr_write_b32 a254,v118\n"
"v_mfma_f32_16x16x32_bf16 v[224:227],v[32:35],a[24:27],v[224:227]\n"
"v_accvgpr_write_b32 a255,v119\n"
"v_accvgpr_read_b32 v116,a240\n"
"v_mfma_f32_16x16x32_bf16 v[228:231],v[40:43],a[24:27],v[228:231]\n"
"ds_read_b64_tr_b16 v[32:33],v100 offset:49152\n"
"v_accvgpr_read_b32 v117,a241\n"
"v_mfma_f32_16x16x32_bf16 v[228:231],v[44:47],a[28:31],v[228:231]\n"
"ds_read_b64_tr_b16 v[34:35],v100 offset:50176\n"
"v_accvgpr_read_b32 v118,a242\n"
"v_mfma_f32_16x16x32_bf16 v[196:199],v[44:47],a[20:23],v[196:199]\n"
"ds_read_b64_tr_b16 v[36:37],v100 offset:57344\n"
"v_accvgpr_read_b32 v119,a243\n"
"v_mfma_f32_16x16x32_bf16 v[196:199],v[40:43],a[16:19],v[196:199]\n"
"s_add_u32 m0,s85,99328\n"
"ds_read_b64_tr_b16 v[38:39],v100 offset:58368\n"
"v_mfma_f32_16x16x32_bf16 v[164:167],v[40:43],a[8:11],v[164:167]\n"
"buffer_load_dwordx4 v111,s[28:31],s86 offen lds\n"
"v_mfma_f32_16x16x32_bf16 v[164:167],v[44:47],a[12:15],v[164:167]\n"
"s_add_u32 m0,s85,106496\n"
"s_xor_b32 s86,s86,262144\n"
"v_mfma_f32_16x16x32_bf16 v[132:135],v[44:47],a[4:7],v[132:135]\n"
"buffer_load_dwordx4 v110,s[28:31],s86 offen lds\n"
"v_mfma_f32_16x16x32_bf16 v[132:135],v[40:43],a[0:3],v[132:135]\n"
"v_accvgpr_read_b32 v120,a244\n"
"v_accvgpr_read_b32 v121,a245\n"
"v_mfma_f32_16x16x32_bf16 v[136:139],v[48:51],a[0:3],v[136:139]\n"
"ds_read_b64_tr_b16 v[40:41],v101 offset:49152\n"
"v_accvgpr_read_b32 v122,a246\n"
"v_mfma_f32_16x16x32_bf16 v[136:139],v[52:55],a[4:7],v[136:139]\n"
"ds_read_b64_tr_b16 v[42:43],v101 offset:50176\n"
"v_accvgpr_read_b32 v123,a247\n"
"v_mfma_f32_16x16x32_bf16 v[168:171],v[52:55],a[12:15],v[168:171]\n"
"ds_read_b64_tr_b16 v[44:45],v101 offset:57344\n"
"v_cvt_pk_bf16_f32 v116,v116,v117\n"
"v_mfma_f32_16x16x32_bf16 v[168:171],v[48:51],a[8:11],v[168:171]\n"
"s_add_u32 m0,s85,107520\n"
"ds_read_b64_tr_b16 v[46:47],v101 offset:58368\n"
"v_mfma_f32_16x16x32_bf16 v[200:203],v[48:51],a[16:19],v[200:203]\n"
"buffer_load_dwordx4 v111,s[28:31],s86 offen lds\n"
"v_mfma_f32_16x16x32_bf16 v[200:203],v[52:55],a[20:23],v[200:203]\n"
"s_xor_b32 s86,s86,786432\n"
"v_cvt_pk_bf16_f32 v117,v118,v119\n"
"v_mfma_f32_16x16x32_bf16 v[232:235],v[52:55],a[28:31],v[232:235]\n"
"v_cvt_pk_bf16_f32 v118,v120,v121\n"
"v_cvt_pk_bf16_f32 v119,v122,v123\n"
"v_mfma_f32_16x16x32_bf16 v[232:235],v[48:51],a[24:27],v[232:235]\n"
"v_accvgpr_write_b32 a248,v116\n"
"v_accvgpr_write_b32 a249,v117\n"
"v_mfma_f32_16x16x32_bf16 v[236:239],v[56:59],a[24:27],v[236:239]\n"
"ds_read_b64_tr_b16 v[48:49],v102 offset:49152\n"
"v_accvgpr_write_b32 a250,v118\n"
"v_mfma_f32_16x16x32_bf16 v[236:239],v[60:63],a[28:31],v[236:239]\n"
"ds_read_b64_tr_b16 v[50:51],v102 offset:50176\n"
"v_accvgpr_write_b32 a251,v119\n"
"v_mfma_f32_16x16x32_bf16 v[204:207],v[60:63],a[20:23],v[204:207]\n"
"ds_read_b64_tr_b16 v[52:53],v102 offset:57344\n"
"v_accvgpr_read_b32 v116,a232\n"
"v_mfma_f32_16x16x32_bf16 v[204:207],v[56:59],a[16:19],v[204:207]\n"
"s_add_u32 m0,s78,32768\n"
"ds_read_b64_tr_b16 v[54:55],v102 offset:58368\n"
"v_accvgpr_read_b32 v119,a235\n"
"v_mfma_f32_16x16x32_bf16 v[172:175],v[56:59],a[8:11],v[172:175]\n"
"buffer_load_dwordx4 v112,s[24:27],s76 offen offset:0 sc1 lds\n"
"v_mfma_f32_16x16x32_bf16 v[172:175],v[60:63],a[12:15],v[172:175]\n"
"v_accvgpr_read_b32 v117,a233\n"
"v_accvgpr_read_b32 v118,a234\n"
"v_mfma_f32_16x16x32_bf16 v[140:143],v[60:63],a[4:7],v[140:143]\n"
"buffer_load_dwordx4 v112,s[24:27],s76 offen offset:1024 sc1 lds\n"
"v_mfma_f32_16x16x32_bf16 v[140:143],v[56:59],a[0:3],v[140:143]\n"
"v_accvgpr_read_b32 v120,a236\n"
"s_waitcnt vmcnt(22)\n"
"v_mfma_f32_16x16x32_bf16 v[144:147],v[64:67],a[0:3],v[144:147]\n"
"ds_read_b64_tr_b16 v[56:57],v103 offset:49152\n"
"v_accvgpr_read_b32 v121,a237\n"
"v_mfma_f32_16x16x32_bf16 v[144:147],v[68:71],a[4:7],v[144:147]\n"
"ds_read_b64_tr_b16 v[58:59],v103 offset:50176\n"
"v_accvgpr_read_b32 v122,a238\n"
"v_mfma_f32_16x16x32_bf16 v[176:179],v[68:71],a[12:15],v[176:179]\n"
"ds_read_b64_tr_b16 v[60:61],v103 offset:57344\n"
"v_accvgpr_read_b32 v123,a239\n"
"v_mfma_f32_16x16x32_bf16 v[176:179],v[64:67],a[8:11],v[176:179]\n"
"ds_read_b64_tr_b16 v[62:63],v103 offset:58368\n"
"v_cvt_pk_bf16_f32 v116,v116,v117\n"
"v_mfma_f32_16x16x32_bf16 v[208:211],v[64:67],a[16:19],v[208:211]\n"
"buffer_load_dwordx4 v112,s[24:27],s76 offen offset:2048 sc1 lds\n"
"v_mfma_f32_16x16x32_bf16 v[208:211],v[68:71],a[20:23],v[208:211]\n"
"ds_read_b128 v[0:3],v97 offset:0\n"
"v_cvt_pk_bf16_f32 v117,v118,v119\n"
"v_mfma_f32_16x16x32_bf16 v[240:243],v[68:71],a[28:31],v[240:243]\n"
"v_cvt_pk_bf16_f32 v118,v120,v121\n"
"v_cvt_pk_bf16_f32 v119,v122,v123\n"
"v_mfma_f32_16x16x32_bf16 v[240:243],v[64:67],a[24:27],v[240:243]\n"
"ds_read_b128 v[8:11],v97 offset:1024\n"
"v_accvgpr_write_b32 a244,v116\n"
"v_mfma_f32_16x16x32_bf16 v[244:247],v[72:75],a[24:27],v[244:247]\n"
"ds_read_b64_tr_b16 v[64:65],v104 offset:49152\n"
"v_accvgpr_write_b32 a245,v117\n"
"v_mfma_f32_16x16x32_bf16 v[244:247],v[76:79],a[28:31],v[244:247]\n"
"ds_read_b64_tr_b16 v[66:67],v104 offset:50176\n"
"v_accvgpr_write_b32 a246,v118\n"
"v_mfma_f32_16x16x32_bf16 v[212:215],v[76:79],a[20:23],v[212:215]\n"
"ds_read_b64_tr_b16 v[68:69],v104 offset:57344\n"
"v_accvgpr_write_b32 a247,v119\n"
"v_mfma_f32_16x16x32_bf16 v[212:215],v[72:75],a[16:19],v[212:215]\n"
"ds_read_b64_tr_b16 v[70:71],v104 offset:58368\n"
"v_accvgpr_read_b32 v116,a224\n"
"v_mfma_f32_16x16x32_bf16 v[180:183],v[72:75],a[8:11],v[180:183]\n"
"buffer_load_dwordx4 v112,s[24:27],s76 offen offset:3072 sc1 lds\n"
"v_mfma_f32_16x16x32_bf16 v[180:183],v[76:79],a[12:15],v[180:183]\n"
"s_add_u32 m0,s78,49152\n"
"s_xor_b32 s76,s76,16384\n"
"ds_read_b128 v[16:19],v97 offset:2048\n"
"v_mfma_f32_16x16x32_bf16 v[148:151],v[76:79],a[4:7],v[148:151]\n"
"buffer_load_dwordx4 v112,s[24:27],s76 offen offset:0 sc1 lds\n"
"v_mfma_f32_16x16x32_bf16 v[148:151],v[72:75],a[0:3],v[148:151]\n"
"ds_read_b128 v[24:27],v97 offset:3072\n"
"v_accvgpr_read_b32 v117,a225\n"
"v_mfma_f32_16x16x32_bf16 v[152:155],v[80:83],a[0:3],v[152:155]\n"
"ds_read_b64_tr_b16 v[72:73],v105 offset:49152\n"
"v_accvgpr_read_b32 v118,a226\n"
"v_mfma_f32_16x16x32_bf16 v[152:155],v[84:87],a[4:7],v[152:155]\n"
"ds_read_b64_tr_b16 v[74:75],v105 offset:50176\n"
"v_accvgpr_read_b32 v119,a227\n"
"v_mfma_f32_16x16x32_bf16 v[184:187],v[84:87],a[12:15],v[184:187]\n"
"ds_read_b64_tr_b16 v[76:77],v105 offset:57344\n"
"v_accvgpr_read_b32 v120,a228\n"
"v_mfma_f32_16x16x32_bf16 v[184:187],v[80:83],a[8:11],v[184:187]\n"
"ds_read_b64_tr_b16 v[78:79],v105 offset:58368\n"
"v_accvgpr_read_b32 v121,a229\n"
"v_mfma_f32_16x16x32_bf16 v[216:219],v[80:83],a[16:19],v[216:219]\n"
"ds_read_b128 v[4:7],v97 offset:16384\n"
"v_accvgpr_read_b32 v122,a230\n"
"v_mfma_f32_16x16x32_bf16 v[216:219],v[84:87],a[20:23],v[216:219]\n"
"ds_read_b128 v[12:15],v97 offset:17408\n"
"v_accvgpr_read_b32 v123,a231\n"
"v_mfma_f32_16x16x32_bf16 v[248:251],v[84:87],a[28:31],v[248:251]\n"
"ds_read_b128 v[20:23],v97 offset:18432\n"
"v_cvt_pk_bf16_f32 v116,v116,v117\n"
"v_mfma_f32_16x16x32_bf16 v[248:251],v[80:83],a[24:27],v[248:251]\n"
"ds_read_b128 v[28:31],v97 offset:19456\n"
"v_cvt_pk_bf16_f32 v117,v118,v119\n"
"v_mfma_f32_16x16x32_bf16 v[252:255],v[88:91],a[24:27],v[252:255]\n"
"buffer_load_dwordx4 v112,s[24:27],s76 offen offset:1024 sc1 lds\n"
"v_mfma_f32_16x16x32_bf16 v[252:255],v[92:95],a[28:31],v[252:255]\n"
"v_cvt_pk_bf16_f32 v118,v120,v121\n"
"v_cvt_pk_bf16_f32 v119,v122,v123\n"
"v_mfma_f32_16x16x32_bf16 v[220:223],v[92:95],a[20:23],v[220:223]\n"
"v_accvgpr_write_b32 a240,v116\n"
"v_accvgpr_write_b32 a241,v117\n"
"v_mfma_f32_16x16x32_bf16 v[220:223],v[88:91],a[16:19],v[220:223]\n"
"buffer_load_dwordx4 v112,s[24:27],s76 offen offset:2048 sc1 lds\n"
"v_mfma_f32_16x16x32_bf16 v[188:191],v[88:91],a[8:11],v[188:191]\n"
"v_accvgpr_write_b32 a242,v118\n"
"v_accvgpr_write_b32 a243,v119\n"
"v_mfma_f32_16x16x32_bf16 v[188:191],v[92:95],a[12:15],v[188:191]\n"
"v_accvgpr_read_b32 v116,a216\n"
"v_accvgpr_read_b32 v117,a217\n"
"v_mfma_f32_16x16x32_bf16 v[156:159],v[92:95],a[4:7],v[156:159]\n"
"buffer_load_dwordx4 v112,s[24:27],s76 offen offset:3072 sc1 lds\n"
"v_mfma_f32_16x16x32_bf16 v[156:159],v[88:91],a[0:3],v[156:159]\n"
"s_xor_b32 s76,s76,49152\n"
"v_accvgpr_read_b32 v118,a218\n"
"s_waitcnt vmcnt(23) lgkmcnt(0)\n"
"s_barrier\n"
"v_mfma_f32_16x16x32_bf16 v[128:131],a[32:35],v[0:3],v[128:131]\n"
"ds_read_b64_tr_b16 v[80:81],v98 offset:32768\n"
"ds_read_b64_tr_b16 v[88:89],v99 offset:32768\n"
"v_mfma_f32_16x16x32_bf16 v[128:131],a[36:39],v[4:7],v[128:131]\n"
"ds_read_b64_tr_b16 v[82:83],v98 offset:33792\n"
"ds_read_b64_tr_b16 v[90:91],v99 offset:33792\n"
"v_mfma_f32_16x16x32_bf16 v[160:163],a[36:39],v[12:15],v[160:163]\n"
"ds_read_b64_tr_b16 v[84:85],v98 offset:40960\n"
"ds_read_b64_tr_b16 v[92:93],v99 offset:40960\n"
"v_mfma_f32_16x16x32_bf16 v[160:163],a[32:35],v[8:11],v[160:163]\n"
"s_add_u32 m0,s85,147456\n"
"ds_read_b64_tr_b16 v[86:87],v98 offset:41984\n"
"ds_read_b64_tr_b16 v[94:95],v99 offset:41984\n"
"v_mfma_f32_16x16x32_bf16 v[192:195],a[32:35],v[16:19],v[192:195]\n"
"buffer_load_dwordx4 v110,s[28:31],s86 offen lds\n"
"v_mfma_f32_16x16x32_bf16 v[192:195],a[36:39],v[20:23],v[192:195]\n"
"v_accvgpr_read_b32 v119,a219\n"
"v_accvgpr_read_b32 v120,a220\n"
"v_mfma_f32_16x16x32_bf16 v[224:227],a[36:39],v[28:31],v[224:227]\n"
"v_accvgpr_read_b32 v121,a221\n"
"v_accvgpr_read_b32 v122,a222\n"
"v_mfma_f32_16x16x32_bf16 v[224:227],a[32:35],v[24:27],v[224:227]\n"
"v_accvgpr_read_b32 v123,a223\n"
"v_cvt_pk_bf16_f32 v116,v116,v117\n"
"v_mfma_f32_16x16x32_bf16 v[228:231],a[40:43],v[24:27],v[228:231]\n"
"ds_read_b64_tr_b16 a[32:33],v100 offset:32768\n"
"v_cvt_pk_bf16_f32 v117,v118,v119\n"
"v_mfma_f32_16x16x32_bf16 v[228:231],a[44:47],v[28:31],v[228:231]\n"
"ds_read_b64_tr_b16 a[34:35],v100 offset:33792\n"
"v_cvt_pk_bf16_f32 v118,v120,v121\n"
"v_mfma_f32_16x16x32_bf16 v[196:199],a[44:47],v[20:23],v[196:199]\n"
"ds_read_b64_tr_b16 a[36:37],v100 offset:40960\n"
"v_cvt_pk_bf16_f32 v119,v122,v123\n"
"v_mfma_f32_16x16x32_bf16 v[196:199],a[40:43],v[16:19],v[196:199]\n"
"s_add_u32 m0,s85,148480\n"
"ds_read_b64_tr_b16 a[38:39],v100 offset:41984\n"
"v_mfma_f32_16x16x32_bf16 v[164:167],a[40:43],v[8:11],v[164:167]\n"
"buffer_load_dwordx4 v111,s[28:31],s86 offen lds\n"
"v_mfma_f32_16x16x32_bf16 v[164:167],a[44:47],v[12:15],v[164:167]\n"
"s_add_u32 m0,s85,155648\n"
"s_xor_b32 s86,s86,262144\n"
"v_mfma_f32_16x16x32_bf16 v[132:135],a[44:47],v[4:7],v[132:135]\n"
"buffer_load_dwordx4 v110,s[28:31],s86 offen lds\n"
"v_mfma_f32_16x16x32_bf16 v[132:135],a[40:43],v[0:3],v[132:135]\n"
"v_accvgpr_write_b32 a236,v116\n"
"v_accvgpr_write_b32 a237,v117\n"
"v_mfma_f32_16x16x32_bf16 v[136:139],v[32:35],v[0:3],v[136:139]\n"
"ds_read_b64_tr_b16 a[40:41],v101 offset:32768\n"
"v_accvgpr_write_b32 a238,v118\n"
"v_mfma_f32_16x16x32_bf16 v[136:139],v[36:39],v[4:7],v[136:139]\n"
"ds_read_b64_tr_b16 a[42:43],v101 offset:33792\n"
"v_accvgpr_write_b32 a239,v119\n"
"v_mfma_f32_16x16x32_bf16 v[168:171],v[36:39],v[12:15],v[168:171]\n"
"ds_read_b64_tr_b16 a[44:45],v101 offset:40960\n"
"v_accvgpr_read_b32 v116,a208\n"
"v_mfma_f32_16x16x32_bf16 v[168:171],v[32:35],v[8:11],v[168:171]\n"
"s_add_u32 m0,s85,156672\n"
"ds_read_b64_tr_b16 a[46:47],v101 offset:41984\n"
"v_mfma_f32_16x16x32_bf16 v[200:203],v[32:35],v[16:19],v[200:203]\n"
"buffer_load_dwordx4 v111,s[28:31],s86 offen lds\n"
"v_mfma_f32_16x16x32_bf16 v[200:203],v[36:39],v[20:23],v[200:203]\n"
"s_xor_b32 s86,s86,1835008\n"
"v_accvgpr_read_b32 v117,a209\n"
"v_mfma_f32_16x16x32_bf16 v[232:235],v[36:39],v[28:31],v[232:235]\n"
"v_accvgpr_read_b32 v118,a210\n"
"v_accvgpr_read_b32 v119,a211\n"
"v_mfma_f32_16x16x32_bf16 v[232:235],v[32:35],v[24:27],v[232:235]\n"
"v_accvgpr_read_b32 v120,a212\n"
"v_accvgpr_read_b32 v121,a213\n"
"v_mfma_f32_16x16x32_bf16 v[236:239],v[40:43],v[24:27],v[236:239]\n"
"ds_read_b64_tr_b16 v[32:33],v102 offset:32768\n"
"v_accvgpr_read_b32 v122,a214\n"
"v_mfma_f32_16x16x32_bf16 v[236:239],v[44:47],v[28:31],v[236:239]\n"
"ds_read_b64_tr_b16 v[34:35],v102 offset:33792\n"
"v_accvgpr_read_b32 v123,a215\n"
"v_mfma_f32_16x16x32_bf16 v[204:207],v[44:47],v[20:23],v[204:207]\n"
"ds_read_b64_tr_b16 v[36:37],v102 offset:40960\n"
"v_cvt_pk_bf16_f32 v116,v116,v117\n"
"v_mfma_f32_16x16x32_bf16 v[204:207],v[40:43],v[16:19],v[204:207]\n"
"s_add_u32 m0,s78,65536\n"
"ds_read_b64_tr_b16 v[38:39],v102 offset:41984\n"
"v_accvgpr_write_b32 a232,v116\n"
"v_mfma_f32_16x16x32_bf16 v[172:175],v[40:43],v[8:11],v[172:175]\n"
"buffer_load_dwordx4 v112,s[24:27],s76 offen offset:0 sc1 lds\n"
"v_mfma_f32_16x16x32_bf16 v[172:175],v[44:47],v[12:15],v[172:175]\n"
"v_cvt_pk_bf16_f32 v117,v118,v119\n"
"v_cvt_pk_bf16_f32 v118,v120,v121\n"
"v_mfma_f32_16x16x32_bf16 v[140:143],v[44:47],v[4:7],v[140:143]\n"
"buffer_load_dwordx4 v112,s[24:27],s76 offen offset:1024 sc1 lds\n"
"v_mfma_f32_16x16x32_bf16 v[140:143],v[40:43],v[0:3],v[140:143]\n"
"v_cvt_pk_bf16_f32 v119,v122,v123\n"
"s_waitcnt vmcnt(18)\n"
"v_mfma_f32_16x16x32_bf16 v[144:147],v[48:51],v[0:3],v[144:147]\n"
"ds_read_b64_tr_b16 v[40:41],v103 offset:32768\n"
"v_accvgpr_write_b32 a233,v117\n"
"v_lshrrev_b32 v106,8,v112\n"
"v_mfma_f32_16x16x32_bf16 v[144:147],v[52:55],v[4:7],v[144:147]\n"
"ds_read_b64_tr_b16 v[42:43],v103 offset:33792\n"
"v_accvgpr_write_b32 a234,v118\n"
"v_lshlrev_b32 v107,6,v106\n"
"v_mfma_f32_16x16x32_bf16 v[176:179],v[52:55],v[12:15],v[176:179]\n"
"ds_read_b64_tr_b16 v[44:45],v103 offset:40960\n"
"v_accvgpr_write_b32 a235,v119\n"
"v_and_b32 v108,0xf0,v112\n"
"v_mfma_f32_16x16x32_bf16 v[176:179],v[48:51],v[8:11],v[176:179]\n"
"ds_read_b64_tr_b16 v[46:47],v103 offset:41984\n"
"v_accvgpr_read_b32 v116,a200\n"
"v_xor_b32 v107,v107,v108\n"
"v_mfma_f32_16x16x32_bf16 v[208:211],v[48:51],v[16:19],v[208:211]\n"
"buffer_load_dwordx4 v112,s[24:27],s76 offen offset:2048 sc1 lds\n"
"v_lshl_add_u32 v106,v106,11,v107\n"
"v_mfma_f32_16x16x32_bf16 v[208:211],v[52:55],v[20:23],v[208:211]\n"
"ds_read_b128 a[0:3],v96 offset:0\n"
"v_accvgpr_read_b32 v117,a201\n"
"v_lshrrev_b32 v107,6,v112\n"
"v_mfma_f32_16x16x32_bf16 v[240:243],v[52:55],v[28:31],v[240:243]\n"
"v_accvgpr_read_b32 v118,a202\n"
"v_accvgpr_read_b32 v119,a203\n"
"v_and_b32 v108,3,v107\n"
"v_mfma_f32_16x16x32_bf16 v[240:243],v[48:51],v[24:27],v[240:243]\n"
"ds_read_b128 a[8:11],v96 offset:1024\n"
"v_accvgpr_read_b32 v120,a204\n"
"v_lshlrev_b32 v108,6,v108\n"
"v_mfma_f32_16x16x32_bf16 v[244:247],v[56:59],v[24:27],v[244:247]\n"
"ds_read_b64_tr_b16 v[48:49],v104 offset:32768\n"
"v_accvgpr_read_b32 v121,a205\n"
"v_mfma_f32_16x16x32_bf16 v[244:247],v[60:63],v[28:31],v[244:247]\n"
"s_lshl_b32 s82,s52,6\n"
"ds_read_b64_tr_b16 v[50:51],v104 offset:33792\n"
"v_accvgpr_read_b32 v122,a206\n"
"v_xor_b32 v108,s82,v108\n"
"v_mfma_f32_16x16x32_bf16 v[212:215],v[60:63],v[20:23],v[212:215]\n"
"ds_read_b64_tr_b16 v[52:53],v104 offset:40960\n"
"v_accvgpr_read_b32 v123,a207\n"
"v_lshl_add_u32 v107,v107,8,v108\n"
"v_mfma_f32_16x16x32_bf16 v[212:215],v[56:59],v[16:19],v[212:215]\n"
"ds_read_b64_tr_b16 v[54:55],v104 offset:41984\n"
"v_cvt_pk_bf16_f32 v116,v116,v117\n"
"v_and_b32 v108,0x30,v112\n"
"v_mfma_f32_16x16x32_bf16 v[180:183],v[56:59],v[8:11],v[180:183]\n"
"buffer_load_dwordx4 v112,s[24:27],s76 offen offset:3072 sc1 lds\n"
"v_add_u32 v107,v107,v108\n"
"v_mfma_f32_16x16x32_bf16 v[180:183],v[60:63],v[12:15],v[180:183]\n"
"s_add_u32 m0,s78,81920\n"
"s_xor_b32 s76,s76,16384\n"
"ds_read_b128 a[16:19],v96 offset:2048\n"
"v_mfma_f32_16x16x32_bf16 v[148:151],v[60:63],v[4:7],v[148:151]\n"
"buffer_load_dwordx4 v112,s[24:27],s76 offen offset:0 sc1 lds\n"
"v_add_u32 v107,114688,v107\n"
"v_mfma_f32_16x16x32_bf16 v[148:151],v[56:59],v[0:3],v[148:151]\n"
"ds_read_b128 a[24:27],v96 offset:3072\n"
"v_cvt_pk_bf16_f32 v117,v118,v119\n"
"v_mfma_f32_16x16x32_bf16 v[152:155],v[64:67],v[0:3],v[152:155]\n"
"ds_read_b64_tr_b16 v[56:57],v105 offset:32768\n"
"v_cvt_pk_bf16_f32 v118,v120,v121\n"
"v_mfma_f32_16x16x32_bf16 v[152:155],v[68:71],v[4:7],v[152:155]\n"
"ds_read_b64_tr_b16 v[58:59],v105 offset:33792\n"
"v_cvt_pk_bf16_f32 v119,v122,v123\n"
"v_mfma_f32_16x16x32_bf16 v[184:187],v[68:71],v[12:15],v[184:187]\n"
"ds_read_b64_tr_b16 v[60:61],v105 offset:40960\n"
"v_accvgpr_write_b32 a228,v116\n"
"v_mfma_f32_16x16x32_bf16 v[184:187],v[64:67],v[8:11],v[184:187]\n"
"ds_read_b64_tr_b16 v[62:63],v105 offset:41984\n"
"v_accvgpr_write_b32 a229,v117\n"
"v_mfma_f32_16x16x32_bf16 v[216:219],v[64:67],v[16:19],v[216:219]\n"
"ds_read_b128 a[4:7],v96 offset:16384\n"
"v_accvgpr_write_b32 a230,v118\n"
"v_mfma_f32_16x16x32_bf16 v[216:219],v[68:71],v[20:23],v[216:219]\n"
"ds_read_b128 a[12:15],v96 offset:17408\n"
"v_accvgpr_write_b32 a231,v119\n"
"v_mfma_f32_16x16x32_bf16 v[248:251],v[68:71],v[28:31],v[248:251]\n"
"ds_read_b128 a[20:23],v96 offset:18432\n"
"v_accvgpr_read_b32 v116,a192\n"
"v_mfma_f32_16x16x32_bf16 v[248:251],v[64:67],v[24:27],v[248:251]\n"
"ds_read_b128 a[28:31],v96 offset:19456\n"
"v_accvgpr_read_b32 v117,a193\n"
"v_mfma_f32_16x16x32_bf16 v[252:255],v[72:75],v[24:27],v[252:255]\n"
"buffer_load_dwordx4 v112,s[24:27],s76 offen offset:1024 sc1 lds\n"
"v_mfma_f32_16x16x32_bf16 v[252:255],v[76:79],v[28:31],v[252:255]\n"
"v_accvgpr_read_b32 v118,a194\n"
"v_accvgpr_read_b32 v119,a195\n"
"v_mfma_f32_16x16x32_bf16 v[220:223],v[76:79],v[20:23],v[220:223]\n"
"v_accvgpr_read_b32 v120,a196\n"
"v_accvgpr_read_b32 v121,a197\n"
"v_mfma_f32_16x16x32_bf16 v[220:223],v[72:75],v[16:19],v[220:223]\n"
"buffer_load_dwordx4 v112,s[24:27],s76 offen offset:2048 sc1 lds\n"
"v_mfma_f32_16x16x32_bf16 v[188:191],v[72:75],v[8:11],v[188:191]\n"
"v_accvgpr_read_b32 v122,a198\n"
"v_accvgpr_read_b32 v123,a199\n"
"v_mfma_f32_16x16x32_bf16 v[188:191],v[76:79],v[12:15],v[188:191]\n"
"v_cvt_pk_bf16_f32 v116,v116,v117\n"
"v_cvt_pk_bf16_f32 v117,v118,v119\n"
"v_mfma_f32_16x16x32_bf16 v[156:159],v[76:79],v[4:7],v[156:159]\n"
"buffer_load_dwordx4 v112,s[24:27],s76 offen offset:3072 sc1 lds\n"
"v_mfma_f32_16x16x32_bf16 v[156:159],v[72:75],v[0:3],v[156:159]\n"
"s_xor_b32 s76,s76,114688\n"
"v_cvt_pk_bf16_f32 v118,v120,v121\n"
"s_waitcnt vmcnt(20) lgkmcnt(0)\n"
"s_barrier\n"
"v_mfma_f32_16x16x32_bf16 v[128:131],v[80:83],a[0:3],v[128:131]\n"
"ds_read_b64_tr_b16 v[64:65],v98 offset:0\n"
"ds_read_b64_tr_b16 v[72:73],v99 offset:0\n"
"v_mfma_f32_16x16x32_bf16 v[128:131],v[84:87],a[4:7],v[128:131]\n"
"ds_read_b64_tr_b16 v[66:67],v98 offset:1024\n"
"ds_read_b64_tr_b16 v[74:75],v99 offset:1024\n"
"v_mfma_f32_16x16x32_bf16 v[160:163],v[84:87],a[12:15],v[160:163]\n"
"ds_read_b64_tr_b16 v[68:69],v98 offset:8192\n"
"ds_read_b64_tr_b16 v[76:77],v99 offset:8192\n"
"v_mfma_f32_16x16x32_bf16 v[160:163],v[80:83],a[8:11],v[160:163]\n"
"s_add_u32 m0,s85,131072\n"
"ds_read_b64_tr_b16 v[70:71],v98 offset:9216\n"
"ds_read_b64_tr_b16 v[78:79],v99 offset:9216\n"
"v_mfma_f32_16x16x32_bf16 v[192:195],v[80:83],a[16:19],v[192:195]\n"
"buffer_load_dwordx4 v110,s[28:31],s86 offen lds\n"
"v_mfma_f32_16x16x32_bf16 v[192:195],v[84:87],a[20:23],v[192:195]\n"
"s_add_u32 m0,s85,132096\n"
"v_cvt_pk_bf16_f32 v119,v122,v123\n"
"v_mfma_f32_16x16x32_bf16 v[224:227],v[84:87],a[28:31],v[224:227]\n"
"buffer_load_dwordx4 v111,s[28:31],s86 offen lds\n"
"v_mfma_f32_16x16x32_bf16 v[224:227],v[80:83],a[24:27],v[224:227]\n"
"s_xor_b32 s86,s86,262144\n"
"v_accvgpr_write_b32 a224,v116\n"
"v_mfma_f32_16x16x32_bf16 v[228:231],v[88:91],a[24:27],v[228:231]\n"
"ds_read_b64_tr_b16 v[80:81],v100 offset:0\n"
"v_accvgpr_write_b32 a225,v117\n"
"v_mfma_f32_16x16x32_bf16 v[228:231],v[92:95],a[28:31],v[228:231]\n"
"ds_read_b64_tr_b16 v[82:83],v100 offset:1024\n"
"v_accvgpr_write_b32 a226,v118\n"
"v_mfma_f32_16x16x32_bf16 v[196:199],v[92:95],a[20:23],v[196:199]\n"
"ds_read_b64_tr_b16 v[84:85],v100 offset:8192\n"
"v_accvgpr_write_b32 a227,v119\n"
"v_mfma_f32_16x16x32_bf16 v[196:199],v[88:91],a[16:19],v[196:199]\n"
"s_add_u32 m0,s85,139264\n"
"ds_read_b64_tr_b16 v[86:87],v100 offset:9216\n"
"v_mfma_f32_16x16x32_bf16 v[164:167],v[88:91],a[8:11],v[164:167]\n"
"buffer_load_dwordx4 v110,s[28:31],s86 offen lds\n"
"v_mfma_f32_16x16x32_bf16 v[164:167],v[92:95],a[12:15],v[164:167]\n"
"s_add_u32 m0,s85,140288\n"
"s_add_u32 s56,s56,126976\n"
"v_mfma_f32_16x16x32_bf16 v[132:135],v[92:95],a[4:7],v[132:135]\n"
"buffer_load_dwordx4 v111,s[28:31],s86 offen lds\n"
"v_mfma_f32_16x16x32_bf16 v[132:135],v[88:91],a[0:3],v[132:135]\n"
"s_xor_b32 s86,s86,786432\n"
"v_mfma_f32_16x16x32_bf16 v[136:139],a[32:35],a[0:3],v[136:139]\n"
"ds_read_b64_tr_b16 v[88:89],v101 offset:0\n"
"v_mfma_f32_16x16x32_bf16 v[136:139],a[36:39],a[4:7],v[136:139]\n"
"ds_read_b64_tr_b16 v[90:91],v101 offset:1024\n"
"v_mfma_f32_16x16x32_bf16 v[168:171],a[36:39],a[12:15],v[168:171]\n"
"ds_read_b64_tr_b16 v[92:93],v101 offset:8192\n"
"v_mfma_f32_16x16x32_bf16 v[168:171],a[32:35],a[8:11],v[168:171]\n"
"s_add_u32 m0,s78,114688\n"
"ds_read_b64_tr_b16 v[94:95],v101 offset:9216\n"
"v_mfma_f32_16x16x32_bf16 v[200:203],a[32:35],a[16:19],v[200:203]\n"
"buffer_load_dwordx4 v106,s[4:7],s13 offen offset:0 nt lds\n"
"v_mfma_f32_16x16x32_bf16 v[200:203],a[36:39],a[20:23],v[200:203]\n"
"s_add_u32 s13,s13,7168\n"
"v_mfma_f32_16x16x32_bf16 v[232:235],a[36:39],a[28:31],v[232:235]\n"
"buffer_load_dwordx4 v106,s[4:7],s13 offen offset:1024 nt lds\n"
"v_mfma_f32_16x16x32_bf16 v[232:235],a[32:35],a[24:27],v[232:235]\n"
"s_add_u32 s13,s13,7168\n"
"v_mfma_f32_16x16x32_bf16 v[236:239],a[40:43],a[24:27],v[236:239]\n"
"ds_read_b64_tr_b16 a[32:33],v102 offset:0\n"
"v_mfma_f32_16x16x32_bf16 v[236:239],a[44:47],a[28:31],v[236:239]\n"
"ds_read_b64_tr_b16 a[34:35],v102 offset:1024\n"
"v_mfma_f32_16x16x32_bf16 v[204:207],a[44:47],a[20:23],v[204:207]\n"
"ds_read_b64_tr_b16 a[36:37],v102 offset:8192\n"
"v_mfma_f32_16x16x32_bf16 v[204:207],a[40:43],a[16:19],v[204:207]\n"
"ds_read_b64_tr_b16 a[38:39],v102 offset:9216\n"
"v_mfma_f32_16x16x32_bf16 v[172:175],a[40:43],a[8:11],v[172:175]\n"
"buffer_load_dwordx4 v106,s[4:7],s13 offen offset:2048 nt lds\n"
"v_mfma_f32_16x16x32_bf16 v[172:175],a[44:47],a[12:15],v[172:175]\n"
"s_add_u32 s13,s13,7168\n"
"v_mfma_f32_16x16x32_bf16 v[140:143],a[44:47],a[4:7],v[140:143]\n"
"buffer_load_dwordx4 v106,s[4:7],s13 offen offset:3072 nt lds\n"
"v_mfma_f32_16x16x32_bf16 v[140:143],a[40:43],a[0:3],v[140:143]\n"
"s_add_u32 s13,s13,109568\n"
"s_waitcnt vmcnt(20)\n"
"v_mfma_f32_16x16x32_bf16 v[144:147],v[32:35],a[0:3],v[144:147]\n"
"ds_read_b64_tr_b16 a[40:41],v103 offset:0\n"
"v_mfma_f32_16x16x32_bf16 v[144:147],v[36:39],a[4:7],v[144:147]\n"
"ds_read_b64_tr_b16 a[42:43],v103 offset:1024\n"
"v_mfma_f32_16x16x32_bf16 v[176:179],v[36:39],a[12:15],v[176:179]\n"
"ds_read_b64_tr_b16 a[44:45],v103 offset:8192\n"
"v_mfma_f32_16x16x32_bf16 v[176:179],v[32:35],a[8:11],v[176:179]\n"
"s_add_u32 m0,s78,0\n"
"ds_read_b64_tr_b16 a[46:47],v103 offset:9216\n"
"v_mfma_f32_16x16x32_bf16 v[208:211],v[32:35],a[16:19],v[208:211]\n"
"buffer_load_dwordx4 v112,s[24:27],s76 offen offset:0 sc1 lds\n"
"v_mfma_f32_16x16x32_bf16 v[208:211],v[36:39],a[20:23],v[208:211]\n"
"ds_read_b128 v[0:3],v96 offset:32768\n"
"v_mfma_f32_16x16x32_bf16 v[240:243],v[36:39],a[28:31],v[240:243]\n"
"buffer_load_dwordx4 v112,s[24:27],s76 offen offset:1024 sc1 lds\n"
"v_mfma_f32_16x16x32_bf16 v[240:243],v[32:35],a[24:27],v[240:243]\n"
"ds_read_b128 v[8:11],v96 offset:33792\n"
"v_mfma_f32_16x16x32_bf16 v[244:247],v[40:43],a[24:27],v[244:247]\n"
"ds_read_b64_tr_b16 v[32:33],v104 offset:0\n"
"v_mfma_f32_16x16x32_bf16 v[244:247],v[44:47],a[28:31],v[244:247]\n"
"ds_read_b64_tr_b16 v[34:35],v104 offset:1024\n"
"v_mfma_f32_16x16x32_bf16 v[212:215],v[44:47],a[20:23],v[212:215]\n"
"ds_read_b64_tr_b16 v[36:37],v104 offset:8192\n"
"v_mfma_f32_16x16x32_bf16 v[212:215],v[40:43],a[16:19],v[212:215]\n"
"ds_read_b64_tr_b16 v[38:39],v104 offset:9216\n"
"v_mfma_f32_16x16x32_bf16 v[180:183],v[40:43],a[8:11],v[180:183]\n"
"buffer_load_dwordx4 v112,s[24:27],s76 offen offset:2048 sc1 lds\n"
"v_mfma_f32_16x16x32_bf16 v[180:183],v[44:47],a[12:15],v[180:183]\n"
"ds_read_b128 v[16:19],v96 offset:34816\n"
"v_mfma_f32_16x16x32_bf16 v[148:151],v[44:47],a[4:7],v[148:151]\n"
"buffer_load_dwordx4 v112,s[24:27],s76 offen offset:3072 sc1 lds\n"
"v_mfma_f32_16x16x32_bf16 v[148:151],v[40:43],a[0:3],v[148:151]\n"
"s_xor_b32 s76,s76,16384\n"
"ds_read_b128 v[24:27],v96 offset:35840\n"
"v_mfma_f32_16x16x32_bf16 v[152:155],v[48:51],a[0:3],v[152:155]\n"
"ds_read_b64_tr_b16 v[40:41],v105 offset:0\n"
"v_mfma_f32_16x16x32_bf16 v[152:155],v[52:55],a[4:7],v[152:155]\n"
"ds_read_b64_tr_b16 v[42:43],v105 offset:1024\n"
"v_mfma_f32_16x16x32_bf16 v[184:187],v[52:55],a[12:15],v[184:187]\n"
"ds_read_b64_tr_b16 v[44:45],v105 offset:8192\n"
"v_mfma_f32_16x16x32_bf16 v[184:187],v[48:51],a[8:11],v[184:187]\n"
"ds_read_b64_tr_b16 v[46:47],v105 offset:9216\n"
"v_mfma_f32_16x16x32_bf16 v[216:219],v[48:51],a[16:19],v[216:219]\n"
"ds_read_b128 v[4:7],v96 offset:49152\n"
"v_mfma_f32_16x16x32_bf16 v[216:219],v[52:55],a[20:23],v[216:219]\n"
"ds_read_b128 v[12:15],v96 offset:50176\n"
"ds_read_b128 v[20:23],v96 offset:51200\n"
"v_mfma_f32_16x16x32_bf16 v[248:251],v[52:55],a[28:31],v[248:251]\n"
"buffer_store_dwordx2 v[124:125],off,s[24:27],s59 sc1\n"
"v_mfma_f32_16x16x32_bf16 v[248:251],v[48:51],a[24:27],v[248:251]\n"
"s_add_u32 m0,s78,16384\n"
"ds_read_b128 v[28:31],v96 offset:52224\n"
"v_mfma_f32_16x16x32_bf16 v[252:255],v[56:59],a[24:27],v[252:255]\n"
"buffer_load_dwordx4 v112,s[24:27],s76 offen offset:0 sc1 lds\n"
"v_mfma_f32_16x16x32_bf16 v[252:255],v[60:63],a[28:31],v[252:255]\n"
"v_mfma_f32_16x16x32_bf16 v[220:223],v[60:63],a[20:23],v[220:223]\n"
"buffer_load_dwordx4 v112,s[24:27],s76 offen offset:1024 sc1 lds\n"
"v_mfma_f32_16x16x32_bf16 v[220:223],v[56:59],a[16:19],v[220:223]\n"
"v_mfma_f32_16x16x32_bf16 v[188:191],v[56:59],a[8:11],v[188:191]\n"
"buffer_load_dwordx4 v112,s[24:27],s76 offen offset:2048 sc1 lds\n"
"v_mfma_f32_16x16x32_bf16 v[188:191],v[60:63],a[12:15],v[188:191]\n"
"v_mfma_f32_16x16x32_bf16 v[156:159],v[60:63],a[4:7],v[156:159]\n"
"buffer_load_dwordx4 v112,s[24:27],s76 offen offset:3072 sc1 lds\n"
"v_mfma_f32_16x16x32_bf16 v[156:159],v[56:59],a[0:3],v[156:159]\n"
"s_xor_b32 s76,s76,49152\n"
"s_waitcnt vmcnt(25) lgkmcnt(0)\n"
"s_barrier\n"
"v_mfma_f32_16x16x32_bf16 v[128:131],v[64:67],v[0:3],v[128:131]\n"
"ds_read_b64_tr_b16 v[48:49],v98 offset:49152\n"
"ds_read_b64_tr_b16 v[56:57],v99 offset:49152\n"
"v_mfma_f32_16x16x32_bf16 v[128:131],v[68:71],v[4:7],v[128:131]\n"
"ds_read_b64_tr_b16 v[50:51],v98 offset:50176\n"
"ds_read_b64_tr_b16 v[58:59],v99 offset:50176\n"
"v_mfma_f32_16x16x32_bf16 v[160:163],v[68:71],v[12:15],v[160:163]\n"
"ds_read_b64_tr_b16 v[52:53],v98 offset:57344\n"
"ds_read_b64_tr_b16 v[60:61],v99 offset:57344\n"
"v_mfma_f32_16x16x32_bf16 v[160:163],v[64:67],v[8:11],v[160:163]\n"
"s_add_u32 m0,s85,98304\n"
"ds_read_b64_tr_b16 v[54:55],v98 offset:58368\n"
"ds_read_b64_tr_b16 v[62:63],v99 offset:58368\n"
"v_mfma_f32_16x16x32_bf16 v[192:195],v[64:67],v[16:19],v[192:195]\n"
"buffer_load_dwordx4 v110,s[28:31],s86 offen lds\n"
"v_mfma_f32_16x16x32_bf16 v[192:195],v[68:71],v[20:23],v[192:195]\n"
"v_mfma_f32_16x16x32_bf16 v[224:227],v[68:71],v[28:31],v[224:227]\n"
"v_mfma_f32_16x16x32_bf16 v[224:227],v[64:67],v[24:27],v[224:227]\n"
"v_mfma_f32_16x16x32_bf16 v[228:231],v[72:75],v[24:27],v[228:231]\n"
"ds_read_b64_tr_b16 v[64:65],v100 offset:49152\n"
"v_mfma_f32_16x16x32_bf16 v[228:231],v[76:79],v[28:31],v[228:231]\n"
"ds_read_b64_tr_b16 v[66:67],v100 offset:50176\n"
"v_mfma_f32_16x16x32_bf16 v[196:199],v[76:79],v[20:23],v[196:199]\n"
"ds_read_b64_tr_b16 v[68:69],v100 offset:57344\n"
"v_mfma_f32_16x16x32_bf16 v[196:199],v[72:75],v[16:19],v[196:199]\n"
"s_add_u32 m0,s85,99328\n"
"ds_read_b64_tr_b16 v[70:71],v100 offset:58368\n"
"v_mfma_f32_16x16x32_bf16 v[164:167],v[72:75],v[8:11],v[164:167]\n"
"buffer_load_dwordx4 v111,s[28:31],s86 offen lds\n"
"v_mfma_f32_16x16x32_bf16 v[164:167],v[76:79],v[12:15],v[164:167]\n"
"s_add_u32 m0,s85,106496\n"
"s_xor_b32 s86,s86,262144\n"
"v_mfma_f32_16x16x32_bf16 v[132:135],v[76:79],v[4:7],v[132:135]\n"
"buffer_load_dwordx4 v110,s[28:31],s86 offen lds\n"
"v_mfma_f32_16x16x32_bf16 v[132:135],v[72:75],v[0:3],v[132:135]\n"
"v_mfma_f32_16x16x32_bf16 v[136:139],v[80:83],v[0:3],v[136:139]\n"
"ds_read_b64_tr_b16 v[72:73],v101 offset:49152\n"
"v_mfma_f32_16x16x32_bf16 v[136:139],v[84:87],v[4:7],v[136:139]\n"
"ds_read_b64_tr_b16 v[74:75],v101 offset:50176\n"
"v_mfma_f32_16x16x32_bf16 v[168:171],v[84:87],v[12:15],v[168:171]\n"
"ds_read_b64_tr_b16 v[76:77],v101 offset:57344\n"
"v_mfma_f32_16x16x32_bf16 v[168:171],v[80:83],v[8:11],v[168:171]\n"
"s_add_u32 m0,s85,107520\n"
"ds_read_b64_tr_b16 v[78:79],v101 offset:58368\n"
"v_mfma_f32_16x16x32_bf16 v[200:203],v[80:83],v[16:19],v[200:203]\n"
"buffer_load_dwordx4 v111,s[28:31],s86 offen lds\n"
"v_mfma_f32_16x16x32_bf16 v[200:203],v[84:87],v[20:23],v[200:203]\n"
"s_xor_b32 s86,s86,3932160\n"
"v_mfma_f32_16x16x32_bf16 v[232:235],v[84:87],v[28:31],v[232:235]\n"
"v_mfma_f32_16x16x32_bf16 v[232:235],v[80:83],v[24:27],v[232:235]\n"
"v_mfma_f32_16x16x32_bf16 v[236:239],v[88:91],v[24:27],v[236:239]\n"
"ds_read_b64_tr_b16 v[80:81],v102 offset:49152\n"
"v_mfma_f32_16x16x32_bf16 v[236:239],v[92:95],v[28:31],v[236:239]\n"
"ds_read_b64_tr_b16 v[82:83],v102 offset:50176\n"
"v_mfma_f32_16x16x32_bf16 v[204:207],v[92:95],v[20:23],v[204:207]\n"
"ds_read_b64_tr_b16 v[84:85],v102 offset:57344\n"
"v_mfma_f32_16x16x32_bf16 v[204:207],v[88:91],v[16:19],v[204:207]\n"
"s_add_u32 m0,s78,32768\n"
"ds_read_b64_tr_b16 v[86:87],v102 offset:58368\n"
"v_mfma_f32_16x16x32_bf16 v[172:175],v[88:91],v[8:11],v[172:175]\n"
"buffer_load_dwordx4 v112,s[24:27],s76 offen offset:0 sc1 lds\n"
"v_mfma_f32_16x16x32_bf16 v[172:175],v[92:95],v[12:15],v[172:175]\n"
"v_mfma_f32_16x16x32_bf16 v[140:143],v[92:95],v[4:7],v[140:143]\n"
"buffer_load_dwordx4 v112,s[24:27],s76 offen offset:1024 sc1 lds\n"
"v_mfma_f32_16x16x32_bf16 v[140:143],v[88:91],v[0:3],v[140:143]\n"
"s_waitcnt vmcnt(23)\n"
"ds_read_b64_tr_b16 v[88:89],v103 offset:49152\n"
"ds_read_b64_tr_b16 v[90:91],v103 offset:50176\n"
"v_mfma_f32_16x16x32_bf16 v[144:147],a[32:35],v[0:3],v[144:147]\n"
"v_lshrrev_b32 v120,4,v112\n"
"v_and_b32 v121,15,v120\n"
"v_mfma_f32_16x16x32_bf16 v[144:147],a[36:39],v[4:7],v[144:147]\n"
"v_lshrrev_b32 v122,4,v120\n"
"v_lshlrev_b32 v122,2,v122\n"
"v_mfma_f32_16x16x32_bf16 v[176:179],a[36:39],v[12:15],v[176:179]\n"
"ds_read_b64_tr_b16 v[92:93],v103 offset:57344\n"
"v_sub_u32 v121,v121,v122\n"
"v_mfma_f32_16x16x32_bf16 v[176:179],a[32:35],v[8:11],v[176:179]\n"
"s_and_b32 s82,s52,1\n"
"ds_read_b64_tr_b16 v[94:95],v103 offset:58368\n"
"s_lshl_b32 s82,s82,4\n"
"v_add_u32 v121,s82,v121\n"
"v_mfma_f32_16x16x32_bf16 v[208:211],a[32:35],v[16:19],v[208:211]\n"
"buffer_load_dwordx4 v112,s[24:27],s76 offen offset:2048 sc1 lds\n"
"v_cvt_f32_i32 v121,v121\n"
"v_mfma_f32_16x16x32_bf16 v[208:211],a[36:39],v[20:23],v[208:211]\n"
"ds_read_b128 a[96:99],v97 offset:0\n"
"v_add_f32 v121,0.5,v121\n"
"v_mul_f32 v126,0x7149f2ca,v121\n"
"v_mfma_f32_16x16x32_bf16 v[240:243],a[36:39],v[28:31],v[240:243]\n"
"v_and_b32 v120,31,v120\n"
"v_mov_b32 v121,0x3f803f80\n"
"v_mfma_f32_16x16x32_bf16 v[240:243],a[32:35],v[24:27],v[240:243]\n"
"s_mov_b32 s93,0x3f800000\n"
"ds_read_b128 a[104:107],v97 offset:1024\n"
"v_accvgpr_write_b32 a144,v121\n"
"v_mfma_f32_16x16x32_bf16 v[244:247],a[40:43],v[24:27],v[244:247]\n"
"ds_read_b64_tr_b16 a[128:129],v104 offset:49152\n"
"v_accvgpr_write_b32 a146,v121\n"
"v_mfma_f32_16x16x32_bf16 v[244:247],a[44:47],v[28:31],v[244:247]\n"
"ds_read_b64_tr_b16 a[130:131],v104 offset:50176\n"
"v_accvgpr_write_b32 a147,v121\n"
"v_mfma_f32_16x16x32_bf16 v[212:215],a[44:47],v[20:23],v[212:215]\n"
"ds_read_b64_tr_b16 a[132:133],v104 offset:57344\n"
"v_lshlrev_b32 v120,3,v120\n"
"v_mfma_f32_16x16x32_bf16 v[212:215],a[40:43],v[16:19],v[212:215]\n"
"ds_read_b64_tr_b16 a[134:135],v104 offset:58368\n"
"v_accvgpr_write_b32 a145,v121\n"
"v_mfma_f32_16x16x32_bf16 v[180:183],a[40:43],v[8:11],v[180:183]\n"
"buffer_load_dwordx4 v112,s[24:27],s76 offen offset:3072 sc1 lds\n"
"v_mfma_f32_16x16x32_bf16 v[180:183],a[44:47],v[12:15],v[180:183]\n"
"s_add_u32 m0,s78,49152\n"
"s_xor_b32 s76,s76,16384\n"
"ds_read_b128 a[112:115],v97 offset:2048\n"
"v_mfma_f32_16x16x32_bf16 v[148:151],a[44:47],v[4:7],v[148:151]\n"
"buffer_load_dwordx4 v112,s[24:27],s76 offen offset:0 sc1 lds\n"
"v_mfma_f32_16x16x32_bf16 v[148:151],a[40:43],v[0:3],v[148:151]\n"
"ds_read_b128 a[120:123],v97 offset:3072\n"
"v_mfma_f32_16x16x32_bf16 v[152:155],v[32:35],v[0:3],v[152:155]\n"
"ds_read_b64_tr_b16 a[136:137],v105 offset:49152\n"
"v_mfma_f32_16x16x32_bf16 v[152:155],v[36:39],v[4:7],v[152:155]\n"
"ds_read_b64_tr_b16 a[138:139],v105 offset:50176\n"
"v_mfma_f32_16x16x32_bf16 v[184:187],v[36:39],v[12:15],v[184:187]\n"
"ds_read_b64_tr_b16 a[140:141],v105 offset:57344\n"
"v_mfma_f32_16x16x32_bf16 v[184:187],v[32:35],v[8:11],v[184:187]\n"
"ds_read_b64_tr_b16 a[142:143],v105 offset:58368\n"
"v_mfma_f32_16x16x32_bf16 v[216:219],v[32:35],v[16:19],v[216:219]\n"
"ds_read_b128 a[100:103],v97 offset:16384\n"
"v_mfma_f32_16x16x32_bf16 v[216:219],v[36:39],v[20:23],v[216:219]\n"
"ds_read_b128 a[108:111],v97 offset:17408\n"
"v_mfma_f32_16x16x32_bf16 v[248:251],v[36:39],v[28:31],v[248:251]\n"
"ds_read_b128 a[116:119],v97 offset:18432\n"
"v_mfma_f32_16x16x32_bf16 v[248:251],v[32:35],v[24:27],v[248:251]\n"
"ds_read_b128 a[124:127],v97 offset:19456\n"
"v_mfma_f32_16x16x32_bf16 v[252:255],v[40:43],v[24:27],v[252:255]\n"
"buffer_load_dwordx4 v112,s[24:27],s76 offen offset:1024 sc1 lds\n"
"v_mfma_f32_16x16x32_bf16 v[252:255],v[44:47],v[28:31],v[252:255]\n"
"v_mfma_f32_16x16x32_bf16 v[220:223],v[44:47],v[20:23],v[220:223]\n"
"v_mfma_f32_16x16x32_bf16 v[220:223],v[40:43],v[16:19],v[220:223]\n"
"buffer_load_dwordx4 v112,s[24:27],s76 offen offset:2048 sc1 lds\n"
"v_mfma_f32_16x16x32_bf16 v[188:191],v[40:43],v[8:11],v[188:191]\n"
"v_mfma_f32_16x16x32_bf16 v[188:191],v[44:47],v[12:15],v[188:191]\n"
"v_mfma_f32_16x16x32_bf16 v[156:159],v[44:47],v[4:7],v[156:159]\n"
"buffer_load_dwordx4 v112,s[24:27],s76 offen offset:3072 sc1 lds\n"
"v_mfma_f32_16x16x32_bf16 v[156:159],v[40:43],v[0:3],v[156:159]\n"
"s_xor_b32 s76,s76,245760\n"
"s_waitcnt vmcnt(21) lgkmcnt(0)\n"
"s_barrier\n"
"v_mfma_f32_16x16x32_bf16 v[128:131],v[48:51],a[96:99],v[128:131]\n"
"ds_read_b64_tr_b16 v[32:33],v98 offset:32768\n"
"ds_read_b64_tr_b16 v[40:41],v99 offset:32768\n"
"ds_read_b64_tr_b16 a[160:161],v107 offset:0\n"
"v_mfma_f32_16x16x32_bf16 v[128:131],v[52:55],a[100:103],v[128:131]\n"
"ds_read_b64_tr_b16 v[34:35],v98 offset:33792\n"
"ds_read_b64_tr_b16 v[42:43],v99 offset:33792\n"
"ds_read_b64_tr_b16 a[162:163],v107 offset:4096\n"
"v_mfma_f32_16x16x32_bf16 v[160:163],v[52:55],a[108:111],v[160:163]\n"
"ds_read_b64_tr_b16 v[36:37],v98 offset:40960\n"
"ds_read_b64_tr_b16 v[44:45],v99 offset:40960\n"
"ds_read_b64_tr_b16 a[164:165],v107 offset:8\n"
"v_mfma_f32_16x16x32_bf16 v[160:163],v[48:51],a[104:107],v[160:163]\n"
"s_add_u32 m0,s85,147456\n"
"ds_read_b64_tr_b16 v[38:39],v98 offset:41984\n"
"ds_read_b64_tr_b16 v[46:47],v99 offset:41984\n"
"v_mfma_f32_16x16x32_bf16 v[192:195],v[48:51],a[112:115],v[192:195]\n"
"buffer_load_dwordx4 v110,s[28:31],s86 offen lds\n"
"ds_read_b64_tr_b16 a[166:167],v107 offset:4104\n"
"v_mfma_f32_16x16x32_bf16 v[192:195],v[52:55],a[116:119],v[192:195]\n"
"ds_read_b64_tr_b16 a[168:169],v107 offset:8192\n"
"ds_read_b64_tr_b16 a[170:171],v107 offset:12288\n"
"v_mfma_f32_16x16x32_bf16 v[224:227],v[52:55],a[124:127],v[224:227]\n"
"ds_read_b64_tr_b16 a[172:173],v107 offset:8200\n"
"ds_read_b64_tr_b16 a[174:175],v107 offset:12296\n"
"v_mfma_f32_16x16x32_bf16 v[224:227],v[48:51],a[120:123],v[224:227]\n"
"v_mfma_f32_16x16x32_bf16 v[228:231],v[56:59],a[120:123],v[228:231]\n"
"ds_read_b64_tr_b16 v[48:49],v100 offset:32768\n"
"v_mfma_f32_16x16x32_bf16 v[228:231],v[60:63],a[124:127],v[228:231]\n"
"ds_read_b64_tr_b16 v[50:51],v100 offset:33792\n"
"v_mfma_f32_16x16x32_bf16 v[196:199],v[60:63],a[116:119],v[196:199]\n"
"ds_read_b64_tr_b16 v[52:53],v100 offset:40960\n"
"v_mfma_f32_16x16x32_bf16 v[196:199],v[56:59],a[112:115],v[196:199]\n"
"s_add_u32 m0,s85,148480\n"
"ds_read_b64_tr_b16 v[54:55],v100 offset:41984\n"
"v_mfma_f32_16x16x32_bf16 v[164:167],v[56:59],a[104:107],v[164:167]\n"
"buffer_load_dwordx4 v111,s[28:31],s86 offen lds\n"
"v_mfma_f32_16x16x32_bf16 v[164:167],v[60:63],a[108:111],v[164:167]\n"
"s_add_u32 m0,s85,155648\n"
"s_xor_b32 s86,s86,262144\n"
"v_mfma_f32_16x16x32_bf16 v[132:135],v[60:63],a[100:103],v[132:135]\n"
"buffer_load_dwordx4 v110,s[28:31],s86 offen lds\n"
"v_mfma_f32_16x16x32_bf16 v[132:135],v[56:59],a[96:99],v[132:135]\n"
"v_mfma_f32_16x16x32_bf16 v[136:139],v[64:67],a[96:99],v[136:139]\n"
"ds_read_b64_tr_b16 v[56:57],v101 offset:32768\n"
"v_mfma_f32_16x16x32_bf16 v[136:139],v[68:71],a[100:103],v[136:139]\n"
"ds_read_b64_tr_b16 v[58:59],v101 offset:33792\n"
"v_mfma_f32_16x16x32_bf16 v[168:171],v[68:71],a[108:111],v[168:171]\n"
"ds_read_b64_tr_b16 v[60:61],v101 offset:40960\n"
"v_mfma_f32_16x16x32_bf16 v[168:171],v[64:67],a[104:107],v[168:171]\n"
"s_add_u32 m0,s85,156672\n"
"ds_read_b64_tr_b16 v[62:63],v101 offset:41984\n"
"v_mfma_f32_16x16x32_bf16 v[200:203],v[64:67],a[112:115],v[200:203]\n"
"buffer_load_dwordx4 v111,s[28:31],s86 offen lds\n"
"v_mfma_f32_16x16x32_bf16 v[200:203],v[68:71],a[116:119],v[200:203]\n"
"s_xor_b32 s86,s86,786432\n"
"v_mfma_f32_16x16x32_bf16 v[232:235],v[68:71],a[124:127],v[232:235]\n"
"v_mfma_f32_16x16x32_bf16 v[232:235],v[64:67],a[120:123],v[232:235]\n"
"v_mfma_f32_16x16x32_bf16 v[236:239],v[72:75],a[120:123],v[236:239]\n"
"ds_read_b64_tr_b16 v[64:65],v102 offset:32768\n"
"v_mfma_f32_16x16x32_bf16 v[236:239],v[76:79],a[124:127],v[236:239]\n"
"ds_read_b64_tr_b16 v[66:67],v102 offset:33792\n"
"v_mfma_f32_16x16x32_bf16 v[204:207],v[76:79],a[116:119],v[204:207]\n"
"ds_read_b64_tr_b16 v[68:69],v102 offset:40960\n"
"v_mfma_f32_16x16x32_bf16 v[204:207],v[72:75],a[112:115],v[204:207]\n"
"s_add_u32 m0,s78,65536\n"
"ds_read_b64_tr_b16 v[70:71],v102 offset:41984\n"
"v_mfma_f32_16x16x32_bf16 v[172:175],v[72:75],a[104:107],v[172:175]\n"
"buffer_load_dwordx4 v112,s[24:27],s76 offen offset:0 sc1 lds\n"
"v_mfma_f32_16x16x32_bf16 v[172:175],v[76:79],a[108:111],v[172:175]\n"
"v_mfma_f32_16x16x32_bf16 v[140:143],v[76:79],a[100:103],v[140:143]\n"
"buffer_load_dwordx4 v112,s[24:27],s76 offen offset:1024 sc1 lds\n"
"v_mfma_f32_16x16x32_bf16 v[140:143],v[72:75],a[96:99],v[140:143]\n"
"s_waitcnt vmcnt(18)\n"
"v_mfma_f32_16x16x32_bf16 v[144:147],v[80:83],a[96:99],v[144:147]\n"
"ds_read_b64_tr_b16 v[72:73],v103 offset:32768\n"
"v_accvgpr_write_b32 a156,0\n"
"v_mfma_f32_16x16x32_bf16 v[144:147],v[84:87],a[100:103],v[144:147]\n"
"ds_read_b64_tr_b16 v[74:75],v103 offset:33792\n"
"v_accvgpr_write_b32 a157,0\n"
"v_mfma_f32_16x16x32_bf16 v[176:179],v[84:87],a[108:111],v[176:179]\n"
"ds_read_b64_tr_b16 v[76:77],v103 offset:40960\n"
"v_accvgpr_write_b32 a158,0\n"
"v_mfma_f32_16x16x32_bf16 v[176:179],v[80:83],a[104:107],v[176:179]\n"
"ds_read_b64_tr_b16 v[78:79],v103 offset:41984\n"
"v_accvgpr_write_b32 a159,0\n"
"v_mfma_f32_16x16x32_bf16 v[208:211],v[80:83],a[112:115],v[208:211]\n"
"buffer_load_dwordx4 v112,s[24:27],s76 offen offset:2048 sc1 lds\n"
"v_mfma_f32_16x16x32_bf16 v[208:211],v[84:87],a[116:119],v[208:211]\n"
"ds_read_b128 v[0:3],v96 offset:0\n"
"v_mfma_f32_16x16x32_bf16 v[240:243],v[84:87],a[124:127],v[240:243]\n"
"v_mfma_f32_16x16x32_bf16 v[240:243],v[80:83],a[120:123],v[240:243]\n"
"ds_read_b128 v[8:11],v96 offset:1024\n"
"v_mfma_f32_16x16x32_bf16 v[244:247],v[88:91],a[120:123],v[244:247]\n"
"ds_read_b64_tr_b16 v[80:81],v104 offset:32768\n"
"v_mfma_f32_16x16x32_bf16 v[244:247],v[92:95],a[124:127],v[244:247]\n"
"ds_read_b64_tr_b16 v[82:83],v104 offset:33792\n"
"v_mfma_f32_16x16x32_bf16 v[212:215],v[92:95],a[116:119],v[212:215]\n"
"ds_read_b64_tr_b16 v[84:85],v104 offset:40960\n"
"v_mfma_f32_16x16x32_bf16 v[212:215],v[88:91],a[112:115],v[212:215]\n"
"ds_read_b64_tr_b16 v[86:87],v104 offset:41984\n"
"v_mfma_f32_16x16x32_bf16 v[180:183],v[88:91],a[104:107],v[180:183]\n"
"buffer_load_dwordx4 v112,s[24:27],s76 offen offset:3072 sc1 lds\n"
"v_mfma_f32_16x16x32_bf16 v[180:183],v[92:95],a[108:111],v[180:183]\n"
"s_add_u32 m0,s78,81920\n"
"s_xor_b32 s76,s76,16384\n"
"ds_read_b128 v[16:19],v96 offset:2048\n"
"v_mfma_f32_16x16x32_bf16 v[148:151],v[92:95],a[100:103],v[148:151]\n"
"buffer_load_dwordx4 v112,s[24:27],s76 offen offset:0 sc1 lds\n"
"v_mfma_f32_16x16x32_bf16 v[148:151],v[88:91],a[96:99],v[148:151]\n"
"ds_read_b128 v[24:27],v96 offset:3072\n"
"v_mfma_f32_16x16x32_bf16 v[152:155],a[128:131],a[96:99],v[152:155]\n"
"ds_read_b64_tr_b16 v[88:89],v105 offset:32768\n"
"v_mfma_f32_16x16x32_bf16 v[152:155],a[132:135],a[100:103],v[152:155]\n"
"ds_read_b64_tr_b16 v[90:91],v105 offset:33792\n"
"v_mfma_f32_16x16x32_bf16 v[184:187],a[132:135],a[108:111],v[184:187]\n"
"ds_read_b64_tr_b16 v[92:93],v105 offset:40960\n"
"v_mfma_f32_16x16x32_bf16 v[184:187],a[128:131],a[104:107],v[184:187]\n"
"ds_read_b64_tr_b16 v[94:95],v105 offset:41984\n"
"v_mfma_f32_16x16x32_bf16 v[216:219],a[128:131],a[112:115],v[216:219]\n"
"ds_read_b128 v[4:7],v96 offset:16384\n"
"v_mfma_f32_16x16x32_bf16 v[216:219],a[132:135],a[116:119],v[216:219]\n"
"ds_read_b128 v[12:15],v96 offset:17408\n"
"v_mfma_f32_16x16x32_bf16 v[248:251],a[132:135],a[124:127],v[248:251]\n"
"ds_read_b128 v[20:23],v96 offset:18432\n"
"v_mfma_f32_16x16x32_bf16 v[248:251],a[128:131],a[120:123],v[248:251]\n"
"ds_read_b128 v[28:31],v96 offset:19456\n"
"v_mfma_f32_16x16x32_bf16 v[252:255],a[136:139],a[120:123],v[252:255]\n"
"buffer_load_dwordx4 v112,s[24:27],s76 offen offset:1024 sc1 lds\n"
"v_mfma_f32_16x16x32_bf16 v[252:255],a[140:143],a[124:127],v[252:255]\n"
"v_mfma_f32_16x16x32_bf16 v[220:223],a[140:143],a[116:119],v[220:223]\n"
"v_mfma_f32_16x16x32_bf16 v[220:223],a[136:139],a[112:115],v[220:223]\n"
"buffer_load_dwordx4 v112,s[24:27],s76 offen offset:2048 sc1 lds\n"
"v_mfma_f32_16x16x32_bf16 v[188:191],a[136:139],a[104:107],v[188:191]\n"
"v_mfma_f32_16x16x32_bf16 v[188:191],a[140:143],a[108:111],v[188:191]\n"
"v_mfma_f32_16x16x32_bf16 v[156:159],a[140:143],a[100:103],v[156:159]\n"
"buffer_load_dwordx4 v112,s[24:27],s76 offen offset:3072 sc1 lds\n"
"v_mfma_f32_16x16x32_bf16 v[156:159],a[136:139],a[96:99],v[156:159]\n"
"s_xor_b32 s76,s76,49152\n"
"s_waitcnt vmcnt(20) lgkmcnt(0)\n"
"s_barrier\n"
"v_mfma_f32_16x16x32_bf16 v[128:131],v[32:35],v[0:3],v[128:131]\n"
"ds_read_b64_tr_b16 a[128:129],v98 offset:0\n"
"ds_read_b64_tr_b16 a[136:137],v99 offset:0\n"
"v_mfma_f32_16x16x32_bf16 v[128:131],v[36:39],v[4:7],v[128:131]\n"
"ds_read_b64_tr_b16 a[130:131],v98 offset:1024\n"
"ds_read_b64_tr_b16 a[138:139],v99 offset:1024\n"
"v_mfma_f32_16x16x32_bf16 v[160:163],v[36:39],v[12:15],v[160:163]\n"
"ds_read_b64_tr_b16 a[132:133],v98 offset:8192\n"
"ds_read_b64_tr_b16 a[140:141],v99 offset:8192\n"
"v_mfma_f32_16x16x32_bf16 v[160:163],v[32:35],v[8:11],v[160:163]\n"
"s_add_u32 m0,s85,131072\n"
"ds_read_b64_tr_b16 a[134:135],v98 offset:9216\n"
"ds_read_b64_tr_b16 a[142:143],v99 offset:9216\n"
"v_mfma_f32_16x16x32_bf16 v[192:195],v[32:35],v[16:19],v[192:195]\n"
"buffer_load_dwordx4 v110,s[28:31],s86 offen lds\n"
"v_mfma_f32_16x16x32_bf16 v[192:195],v[36:39],v[20:23],v[192:195]\n"
"s_add_u32 m0,s85,132096\n"
"v_mfma_f32_16x16x32_bf16 v[224:227],v[36:39],v[28:31],v[224:227]\n"
"buffer_load_dwordx4 v111,s[28:31],s86 offen lds\n"
"v_mfma_f32_16x16x32_bf16 v[224:227],v[32:35],v[24:27],v[224:227]\n"
"s_xor_b32 s86,s86,262144\n"
"v_mfma_f32_16x16x32_bf16 v[228:231],v[40:43],v[24:27],v[228:231]\n"
"ds_read_b64_tr_b16 v[32:33],v100 offset:0\n"
"v_mfma_f32_16x16x32_bf16 v[228:231],v[44:47],v[28:31],v[228:231]\n"
"ds_read_b64_tr_b16 v[34:35],v100 offset:1024\n"
"v_mfma_f32_16x16x32_bf16 v[196:199],v[44:47],v[20:23],v[196:199]\n"
"ds_read_b64_tr_b16 v[36:37],v100 offset:8192\n"
"v_mfma_f32_16x16x32_bf16 v[196:199],v[40:43],v[16:19],v[196:199]\n"
"s_add_u32 m0,s85,139264\n"
"ds_read_b64_tr_b16 v[38:39],v100 offset:9216\n"
"v_mfma_f32_16x16x32_bf16 v[164:167],v[40:43],v[8:11],v[164:167]\n"
"buffer_load_dwordx4 v110,s[28:31],s86 offen lds\n"
"v_mfma_f32_16x16x32_bf16 v[164:167],v[44:47],v[12:15],v[164:167]\n"
"s_add_u32 m0,s85,140288\n"
"v_mfma_f32_16x16x32_bf16 v[132:135],v[44:47],v[4:7],v[132:135]\n"
"buffer_load_dwordx4 v111,s[28:31],s86 offen lds\n"
"v_mfma_f32_16x16x32_bf16 v[132:135],v[40:43],v[0:3],v[132:135]\n"
"s_xor_b32 s86,s86,1835008\n"
"v_mfma_f32_16x16x32_bf16 v[136:139],v[48:51],v[0:3],v[136:139]\n"
"ds_read_b64_tr_b16 v[40:41],v101 offset:0\n"
"v_mfma_f32_16x16x32_bf16 v[136:139],v[52:55],v[4:7],v[136:139]\n"
"ds_read_b64_tr_b16 v[42:43],v101 offset:1024\n"
"v_mfma_f32_16x16x32_bf16 v[168:171],v[52:55],v[12:15],v[168:171]\n"
"ds_read_b64_tr_b16 v[44:45],v101 offset:8192\n"
"v_mfma_f32_16x16x32_bf16 v[168:171],v[48:51],v[8:11],v[168:171]\n"
"s_add_u32 m0,s78,114688\n"
"ds_read_b64_tr_b16 v[46:47],v101 offset:9216\n"
"v_mfma_f32_16x16x32_bf16 v[200:203],v[48:51],v[16:19],v[200:203]\n"
"buffer_load_dwordx4 v106,s[4:7],s13 offen offset:0 nt lds\n"
"v_mfma_f32_16x16x32_bf16 v[200:203],v[52:55],v[20:23],v[200:203]\n"
"s_add_u32 s13,s13,7168\n"
"v_mfma_f32_16x16x32_bf16 v[232:235],v[52:55],v[28:31],v[232:235]\n"
"buffer_load_dwordx4 v106,s[4:7],s13 offen offset:1024 nt lds\n"
"v_mfma_f32_16x16x32_bf16 v[232:235],v[48:51],v[24:27],v[232:235]\n"
"s_add_u32 s13,s13,7168\n"
"v_mfma_f32_16x16x32_bf16 v[236:239],v[56:59],v[24:27],v[236:239]\n"
"ds_read_b64_tr_b16 v[48:49],v102 offset:0\n"
"v_mfma_f32_16x16x32_bf16 v[236:239],v[60:63],v[28:31],v[236:239]\n"
"ds_read_b64_tr_b16 v[50:51],v102 offset:1024\n"
"v_mfma_f32_16x16x32_bf16 v[204:207],v[60:63],v[20:23],v[204:207]\n"
"ds_read_b64_tr_b16 v[52:53],v102 offset:8192\n"
"v_mfma_f32_16x16x32_bf16 v[204:207],v[56:59],v[16:19],v[204:207]\n"
"ds_read_b64_tr_b16 v[54:55],v102 offset:9216\n"
"v_mfma_f32_16x16x32_bf16 v[172:175],v[56:59],v[8:11],v[172:175]\n"
"buffer_load_dwordx4 v106,s[4:7],s13 offen offset:2048 nt lds\n"
"v_mfma_f32_16x16x32_bf16 v[172:175],v[60:63],v[12:15],v[172:175]\n"
"s_add_u32 s13,s13,7168\n"
"v_mfma_f32_16x16x32_bf16 v[140:143],v[60:63],v[4:7],v[140:143]\n"
"buffer_load_dwordx4 v106,s[4:7],s13 offen offset:3072 nt lds\n"
"v_mfma_f32_16x16x32_bf16 v[140:143],v[56:59],v[0:3],v[140:143]\n"
"s_add_u32 s13,s13,109568\n"
"s_waitcnt vmcnt(20)\n"
"v_mfma_f32_16x16x32_bf16 v[144:147],v[64:67],v[0:3],v[144:147]\n"
"ds_read_b64_tr_b16 v[56:57],v103 offset:0\n"
"v_mfma_f32_16x16x32_bf16 v[144:147],v[68:71],v[4:7],v[144:147]\n"
"ds_read_b64_tr_b16 v[58:59],v103 offset:1024\n"
"v_mfma_f32_16x16x32_bf16 v[176:179],v[68:71],v[12:15],v[176:179]\n"
"ds_read_b64_tr_b16 v[60:61],v103 offset:8192\n"
"v_mfma_f32_16x16x32_bf16 v[176:179],v[64:67],v[8:11],v[176:179]\n"
"s_add_u32 m0,s78,0\n"
"ds_read_b64_tr_b16 v[62:63],v103 offset:9216\n"
"v_mfma_f32_16x16x32_bf16 v[208:211],v[64:67],v[16:19],v[208:211]\n"
"buffer_load_dwordx4 v112,s[24:27],s76 offen offset:0 sc1 lds\n"
"v_mfma_f32_16x16x32_bf16 v[208:211],v[68:71],v[20:23],v[208:211]\n"
"ds_read_b128 a[96:99],v96 offset:32768\n"
"v_mfma_f32_16x16x32_bf16 v[240:243],v[68:71],v[28:31],v[240:243]\n"
"buffer_load_dwordx4 v112,s[24:27],s76 offen offset:1024 sc1 lds\n"
"v_mfma_f32_16x16x32_bf16 v[240:243],v[64:67],v[24:27],v[240:243]\n"
"ds_read_b128 a[104:107],v96 offset:33792\n"
"v_mfma_f32_16x16x32_bf16 v[244:247],v[72:75],v[24:27],v[244:247]\n"
"ds_read_b64_tr_b16 v[64:65],v104 offset:0\n"
"v_mfma_f32_16x16x32_bf16 v[244:247],v[76:79],v[28:31],v[244:247]\n"
"ds_read_b64_tr_b16 v[66:67],v104 offset:1024\n"
"v_mfma_f32_16x16x32_bf16 v[212:215],v[76:79],v[20:23],v[212:215]\n"
"ds_read_b64_tr_b16 v[68:69],v104 offset:8192\n"
"v_mfma_f32_16x16x32_bf16 v[212:215],v[72:75],v[16:19],v[212:215]\n"
"ds_read_b64_tr_b16 v[70:71],v104 offset:9216\n"
"v_mfma_f32_16x16x32_bf16 v[180:183],v[72:75],v[8:11],v[180:183]\n"
"buffer_load_dwordx4 v112,s[24:27],s76 offen offset:2048 sc1 lds\n"
"v_mfma_f32_16x16x32_bf16 v[180:183],v[76:79],v[12:15],v[180:183]\n"
"ds_read_b128 a[112:115],v96 offset:34816\n"
"v_mfma_f32_16x16x32_bf16 v[148:151],v[76:79],v[4:7],v[148:151]\n"
"buffer_load_dwordx4 v112,s[24:27],s76 offen offset:3072 sc1 lds\n"
"v_mfma_f32_16x16x32_bf16 v[148:151],v[72:75],v[0:3],v[148:151]\n"
"s_xor_b32 s76,s76,16384\n"
"ds_read_b128 a[120:123],v96 offset:35840\n"
"v_mfma_f32_16x16x32_bf16 v[152:155],v[80:83],v[0:3],v[152:155]\n"
"ds_read_b64_tr_b16 v[72:73],v105 offset:0\n"
"v_mfma_f32_16x16x32_bf16 v[152:155],v[84:87],v[4:7],v[152:155]\n"
"ds_read_b64_tr_b16 v[74:75],v105 offset:1024\n"
"v_mfma_f32_16x16x32_bf16 v[184:187],v[84:87],v[12:15],v[184:187]\n"
"ds_read_b64_tr_b16 v[76:77],v105 offset:8192\n"
"v_mfma_f32_16x16x32_bf16 v[184:187],v[80:83],v[8:11],v[184:187]\n"
"ds_read_b64_tr_b16 v[78:79],v105 offset:9216\n"
"v_mfma_f32_16x16x32_bf16 v[216:219],v[80:83],v[16:19],v[216:219]\n"
"ds_read_b128 a[100:103],v96 offset:49152\n"
"v_mfma_f32_16x16x32_bf16 v[216:219],v[84:87],v[20:23],v[216:219]\n"
"ds_read_b128 a[108:111],v96 offset:50176\n"
"ds_read_b128 a[116:119],v96 offset:51200\n"
"v_mfma_f32_16x16x32_bf16 v[248:251],v[84:87],v[28:31],v[248:251]\n"
"s_load_dwordx2 s[32:33],s[0:1],56\n"
"s_load_dwordx2 s[36:37],s[0:1],64\n"
"s_load_dwordx2 s[40:41],s[0:1],72\n"
"v_mfma_f32_16x16x32_bf16 v[248:251],v[80:83],v[24:27],v[248:251]\n"
"s_add_u32 m0,s78,16384\n"
"ds_read_b128 a[124:127],v96 offset:52224\n"
"v_mfma_f32_16x16x32_bf16 v[252:255],v[88:91],v[24:27],v[252:255]\n"
"buffer_load_dwordx4 v112,s[24:27],s76 offen offset:0 sc1 lds\n"
"v_mfma_f32_16x16x32_bf16 v[252:255],v[92:95],v[28:31],v[252:255]\n"
"v_mfma_f32_16x16x32_bf16 v[220:223],v[92:95],v[20:23],v[220:223]\n"
"buffer_load_dwordx4 v112,s[24:27],s76 offen offset:1024 sc1 lds\n"
"v_mfma_f32_16x16x32_bf16 v[220:223],v[88:91],v[16:19],v[220:223]\n"
"v_mfma_f32_16x16x32_bf16 v[188:191],v[88:91],v[8:11],v[188:191]\n"
"buffer_load_dwordx4 v112,s[24:27],s76 offen offset:2048 sc1 lds\n"
"v_mfma_f32_16x16x32_bf16 v[188:191],v[92:95],v[12:15],v[188:191]\n"
"v_mfma_f32_16x16x32_bf16 v[156:159],v[92:95],v[4:7],v[156:159]\n"
"buffer_load_dwordx4 v112,s[24:27],s76 offen offset:3072 sc1 lds\n"
"v_mfma_f32_16x16x32_bf16 v[156:159],v[88:91],v[0:3],v[156:159]\n"
"s_xor_b32 s76,s76,114688\n"
"s_waitcnt vmcnt(24) lgkmcnt(0)\n"
"s_barrier\n"
"v_mfma_f32_16x16x32_bf16 v[128:131],a[128:131],a[96:99],v[128:131]\n"
"ds_read_b64_tr_b16 v[80:81],v98 offset:49152\n"
"ds_read_b64_tr_b16 v[88:89],v99 offset:49152\n"
"v_mfma_f32_16x16x32_bf16 v[128:131],a[132:135],a[100:103],v[128:131]\n"
"ds_read_b64_tr_b16 v[82:83],v98 offset:50176\n"
"ds_read_b64_tr_b16 v[90:91],v99 offset:50176\n"
"v_mfma_f32_16x16x32_bf16 v[160:163],a[132:135],a[108:111],v[160:163]\n"
"ds_read_b64_tr_b16 v[84:85],v98 offset:57344\n"
"ds_read_b64_tr_b16 v[92:93],v99 offset:57344\n"
"v_mfma_f32_16x16x32_bf16 v[160:163],a[128:131],a[104:107],v[160:163]\n"
"s_add_u32 m0,s85,98304\n"
"ds_read_b64_tr_b16 v[86:87],v98 offset:58368\n"
"ds_read_b64_tr_b16 v[94:95],v99 offset:58368\n"
"v_mfma_f32_16x16x32_bf16 v[192:195],a[128:131],a[112:115],v[192:195]\n"
"buffer_load_dwordx4 v110,s[28:31],s86 offen lds\n"
"v_mfma_f32_16x16x32_bf16 v[192:195],a[132:135],a[116:119],v[192:195]\n"
"v_accvgpr_write_b32 a154,v124\n"
"v_accvgpr_write_b32 a155,v125\n"
"v_mfma_f32_16x16x32_bf16 v[224:227],a[132:135],a[124:127],v[224:227]\n"
"v_accvgpr_write_b32 a152,1\n"
"v_lshrrev_b32 v124,2,v112\n"
"v_mfma_f32_16x16x32_bf16 v[224:227],a[128:131],a[120:123],v[224:227]\n"
"v_add_u32 v124,147456,v124\n"
"v_mov_b32 v125,0\n"
"v_mfma_f32_16x16x32_bf16 v[228:231],a[136:139],a[120:123],v[228:231]\n"
"ds_read_b64_tr_b16 a[128:129],v100 offset:49152\n"
"s_mov_b32 s101,0\n"
"v_mfma_f32_16x16x32_bf16 v[228:231],a[140:143],a[124:127],v[228:231]\n"
"ds_read_b64_tr_b16 a[130:131],v100 offset:50176\n"
"v_mfma_f32_16x16x32_bf16 v[196:199],a[140:143],a[116:119],v[196:199]\n"
"ds_read_b64_tr_b16 a[132:133],v100 offset:57344\n"
"v_mfma_f32_16x16x32_bf16 v[196:199],a[136:139],a[112:115],v[196:199]\n"
"s_add_u32 m0,s85,99328\n"
"ds_read_b64_tr_b16 a[134:135],v100 offset:58368\n"
"v_mfma_f32_16x16x32_bf16 v[164:167],a[136:139],a[104:107],v[164:167]\n"
"buffer_load_dwordx4 v111,s[28:31],s86 offen lds\n"
"v_mfma_f32_16x16x32_bf16 v[164:167],a[140:143],a[108:111],v[164:167]\n"
"s_add_u32 m0,s85,106496\n"
"s_xor_b32 s86,s86,262144\n"
"v_mfma_f32_16x16x32_bf16 v[132:135],a[140:143],a[100:103],v[132:135]\n"
"buffer_load_dwordx4 v110,s[28:31],s86 offen lds\n"
"v_mfma_f32_16x16x32_bf16 v[132:135],a[136:139],a[96:99],v[132:135]\n"
"v_mfma_f32_16x16x32_bf16 v[136:139],v[32:35],a[96:99],v[136:139]\n"
"ds_read_b64_tr_b16 a[136:137],v101 offset:49152\n"
"v_mfma_f32_16x16x32_bf16 v[136:139],v[36:39],a[100:103],v[136:139]\n"
"ds_read_b64_tr_b16 a[138:139],v101 offset:50176\n"
"v_mfma_f32_16x16x32_bf16 v[168:171],v[36:39],a[108:111],v[168:171]\n"
"ds_read_b64_tr_b16 a[140:141],v101 offset:57344\n"
"v_mfma_f32_16x16x32_bf16 v[168:171],v[32:35],a[104:107],v[168:171]\n"
"s_add_u32 m0,s85,107520\n"
"ds_read_b64_tr_b16 a[142:143],v101 offset:58368\n"
"v_mfma_f32_16x16x32_bf16 v[200:203],v[32:35],a[112:115],v[200:203]\n"
"buffer_load_dwordx4 v111,s[28:31],s86 offen lds\n"
"v_mfma_f32_16x16x32_bf16 v[200:203],v[36:39],a[116:119],v[200:203]\n"
"s_xor_b32 s86,s86,786432\n"
"v_mfma_f32_16x16x32_bf16 v[232:235],v[36:39],a[124:127],v[232:235]\n"
"v_mfma_f32_16x16x32_bf16 v[232:235],v[32:35],a[120:123],v[232:235]\n"
"v_mfma_f32_16x16x32_bf16 v[236:239],v[40:43],a[120:123],v[236:239]\n"
"ds_read_b64_tr_b16 v[32:33],v102 offset:49152\n"
"v_mfma_f32_16x16x32_bf16 v[236:239],v[44:47],a[124:127],v[236:239]\n"
"ds_read_b64_tr_b16 v[34:35],v102 offset:50176\n"
"v_mfma_f32_16x16x32_bf16 v[204:207],v[44:47],a[116:119],v[204:207]\n"
"ds_read_b64_tr_b16 v[36:37],v102 offset:57344\n"
"v_mfma_f32_16x16x32_bf16 v[204:207],v[40:43],a[112:115],v[204:207]\n"
"s_add_u32 m0,s78,32768\n"
"ds_read_b64_tr_b16 v[38:39],v102 offset:58368\n"
"buffer_load_dwordx4 v112,s[24:27],s76 offen offset:0 sc1 lds\n"
"v_mfma_f32_16x16x32_bf16 v[172:175],v[40:43],a[104:107],v[172:175]\n"
"buffer_load_dwordx2 v[118:119],v120,s[24:27],s63 offen sc1\n"
"v_mfma_f32_16x16x32_bf16 v[172:175],v[44:47],a[108:111],v[172:175]\n"
"v_mfma_f32_16x16x32_bf16 v[140:143],v[44:47],a[100:103],v[140:143]\n"
"buffer_load_dwordx4 v112,s[24:27],s76 offen offset:1024 sc1 lds\n"
"v_mfma_f32_16x16x32_bf16 v[140:143],v[40:43],a[96:99],v[140:143]\n"
"s_waitcnt vmcnt(23)\n"
"v_mfma_f32_16x16x32_bf16 v[144:147],v[48:51],a[96:99],v[144:147]\n"
"ds_read_b64_tr_b16 v[40:41],v103 offset:49152\n"
"s_and_b32 s82,s100,7\n"
"v_mfma_f32_16x16x32_bf16 v[144:147],v[52:55],a[100:103],v[144:147]\n"
"ds_read_b64_tr_b16 v[42:43],v103 offset:50176\n"
"s_mul_i32 s82,s82,28\n"
"v_mfma_f32_16x16x32_bf16 v[176:179],v[52:55],a[108:111],v[176:179]\n"
"ds_read_b64_tr_b16 v[44:45],v103 offset:57344\n"
"s_lshr_b32 s83,s100,3\n"
"v_mfma_f32_16x16x32_bf16 v[176:179],v[48:51],a[104:107],v[176:179]\n"
"ds_read_b64_tr_b16 v[46:47],v103 offset:58368\n"
"s_add_u32 s23,s82,s83\n"
"v_mfma_f32_16x16x32_bf16 v[208:211],v[48:51],a[112:115],v[208:211]\n"
"buffer_load_dwordx4 v112,s[24:27],s76 offen offset:2048 sc1 lds\n"
"v_mfma_f32_16x16x32_bf16 v[208:211],v[52:55],a[116:119],v[208:211]\n"
"s_cmp_lt_u32 s83,28\n"
"s_cselect_b32 s34,1827872,0\n"
"s_cselect_b32 s42,8192,0\n"
"s_cselect_b32 s23,s23,0\n"
"v_mfma_f32_16x16x32_bf16 v[240:243],v[52:55],a[124:127],v[240:243]\n"
"s_mov_b32 s35,0x20000\n"
"ds_read_b128 v[0:3],v97 offset:0\n"
"s_mov_b32 s43,0x20000\n"
"v_mfma_f32_16x16x32_bf16 v[240:243],v[48:51],a[120:123],v[240:243]\n"
"ds_read_b128 v[8:11],v97 offset:1024\n"
"s_lshl2_add_u32 s82,s83,s52\n"
"v_mfma_f32_16x16x32_bf16 v[244:247],v[56:59],a[120:123],v[244:247]\n"
"ds_read_b64_tr_b16 v[48:49],v104 offset:49152\n"
"s_mul_i32 s82,s82,57344\n"
"v_mfma_f32_16x16x32_bf16 v[244:247],v[60:63],a[124:127],v[244:247]\n"
"ds_read_b64_tr_b16 v[50:51],v104 offset:50176\n"
"s_and_b32 s83,s100,7\n"
"v_mfma_f32_16x16x32_bf16 v[212:215],v[60:63],a[116:119],v[212:215]\n"
"ds_read_b64_tr_b16 v[52:53],v104 offset:57344\n"
"s_mul_i32 s83,s83,896\n"
"v_mfma_f32_16x16x32_bf16 v[212:215],v[56:59],a[112:115],v[212:215]\n"
"ds_read_b64_tr_b16 v[54:55],v104 offset:58368\n"
"s_add_u32 s82,s82,s83\n"
"v_mfma_f32_16x16x32_bf16 v[180:183],v[56:59],a[104:107],v[180:183]\n"
"buffer_load_dwordx4 v112,s[24:27],s76 offen offset:3072 sc1 lds\n"
"v_mfma_f32_16x16x32_bf16 v[180:183],v[60:63],a[108:111],v[180:183]\n"
"s_add_u32 m0,s78,49152\n"
"s_xor_b32 s76,s76,16384\n"
"ds_read_b128 v[16:19],v97 offset:2048\n"
"v_mfma_f32_16x16x32_bf16 v[148:151],v[60:63],a[100:103],v[148:151]\n"
"buffer_load_dwordx4 v112,s[24:27],s76 offen offset:0 sc1 lds\n"
"v_mfma_f32_16x16x32_bf16 v[148:151],v[56:59],a[96:99],v[148:151]\n"
"s_add_u32 s44,s32,s82\n"
"ds_read_b128 v[24:27],v97 offset:3072\n"
"s_addc_u32 s45,s33,0\n"
"v_mfma_f32_16x16x32_bf16 v[152:155],v[64:67],a[96:99],v[152:155]\n"
"s_add_u32 s48,s36,s82\n"
"ds_read_b64_tr_b16 v[56:57],v105 offset:49152\n"
"s_addc_u32 s49,s37,0\n"
"v_mfma_f32_16x16x32_bf16 v[152:155],v[68:71],a[100:103],v[152:155]\n"
"ds_read_b64_tr_b16 v[58:59],v105 offset:50176\n"
"s_mov_b32 s46,51072\n"
"v_mfma_f32_16x16x32_bf16 v[184:187],v[68:71],a[108:111],v[184:187]\n"
"ds_read_b64_tr_b16 v[60:61],v105 offset:57344\n"
"s_mov_b32 s50,s46\n"
"v_mfma_f32_16x16x32_bf16 v[184:187],v[64:67],a[104:107],v[184:187]\n"
"ds_read_b64_tr_b16 v[62:63],v105 offset:58368\n"
"s_mov_b32 s47,0x20000\n"
"v_mfma_f32_16x16x32_bf16 v[216:219],v[64:67],a[112:115],v[216:219]\n"
"ds_read_b128 v[4:7],v97 offset:16384\n"
"s_mov_b32 s51,0x20000\n"
"v_mfma_f32_16x16x32_bf16 v[216:219],v[68:71],a[116:119],v[216:219]\n"
"ds_read_b128 v[12:15],v97 offset:17408\n"
"s_mul_i32 s82,s52,1835008\n"
"v_mfma_f32_16x16x32_bf16 v[248:251],v[68:71],a[124:127],v[248:251]\n"
"ds_read_b128 v[20:23],v97 offset:18432\n"
"s_lshl_b32 s83,s23,5\n"
"v_mfma_f32_16x16x32_bf16 v[248:251],v[64:67],a[120:123],v[248:251]\n"
"ds_read_b128 v[28:31],v97 offset:19456\n"
"s_add_u32 s82,s82,s83\n"
"v_mfma_f32_16x16x32_bf16 v[252:255],v[72:75],a[120:123],v[252:255]\n"
"buffer_load_dwordx4 v112,s[24:27],s76 offen offset:1024 sc1 lds\n"
"v_mfma_f32_16x16x32_bf16 v[252:255],v[76:79],a[124:127],v[252:255]\n"
"s_add_u32 s32,s32,s82\n"
"s_addc_u32 s33,s33,0\n"
"s_add_u32 s36,s36,s82\n"
"v_mfma_f32_16x16x32_bf16 v[220:223],v[76:79],a[116:119],v[220:223]\n"
"s_addc_u32 s37,s37,0\n"
"s_lshl2_add_u32 s82,s23,s52\n"
"s_lshl_b32 s82,s82,13\n"
"v_mfma_f32_16x16x32_bf16 v[220:223],v[72:75],a[112:115],v[220:223]\n"
"buffer_load_dwordx4 v112,s[24:27],s76 offen offset:2048 sc1 lds\n"
"v_mfma_f32_16x16x32_bf16 v[188:191],v[72:75],a[104:107],v[188:191]\n"
"s_add_u32 s40,s40,s82\n"
"s_addc_u32 s41,s41,0\n"
"v_lshrrev_b32 v121,5,v112\n"
"v_mfma_f32_16x16x32_bf16 v[188:191],v[76:79],a[108:111],v[188:191]\n"
"v_mul_u32_u24 v121,7168,v121\n"
"v_and_b32 v122,16,v112\n"
"v_mfma_f32_16x16x32_bf16 v[156:159],v[76:79],a[100:103],v[156:159]\n"
"buffer_load_dwordx4 v112,s[24:27],s76 offen offset:3072 sc1 lds\n"
"v_mfma_f32_16x16x32_bf16 v[156:159],v[72:75],a[96:99],v[156:159]\n"
"s_xor_b32 s76,s76,49152\n"
"v_add_u32 v115,v121,v122\n"
"s_waitcnt vmcnt(21) lgkmcnt(0)\n"
"s_barrier\n"
"v_mfma_f32_16x16x32_bf16 v[128:131],v[80:83],v[0:3],v[128:131]\n"
"ds_read_b64_tr_b16 v[64:65],v98 offset:32768\n"
"ds_read_b64_tr_b16 v[72:73],v99 offset:32768\n"
"ds_read_b64_tr_b16 a[176:177],v107 offset:0\n"
"v_mfma_f32_16x16x32_bf16 v[128:131],v[84:87],v[4:7],v[128:131]\n"
"ds_read_b64_tr_b16 v[66:67],v98 offset:33792\n"
"ds_read_b64_tr_b16 v[74:75],v99 offset:33792\n"
"ds_read_b64_tr_b16 a[178:179],v107 offset:4096\n"
"v_mfma_f32_16x16x32_bf16 v[160:163],v[84:87],v[12:15],v[160:163]\n"
"ds_read_b64_tr_b16 v[68:69],v98 offset:40960\n"
"ds_read_b64_tr_b16 v[76:77],v99 offset:40960\n"
"ds_read_b64_tr_b16 a[180:181],v107 offset:8\n"
"v_mfma_f32_16x16x32_bf16 v[160:163],v[80:83],v[8:11],v[160:163]\n"
"s_add_u32 m0,s85,147456\n"
"ds_read_b64_tr_b16 v[70:71],v98 offset:41984\n"
"ds_read_b64_tr_b16 v[78:79],v99 offset:41984\n"
"v_mfma_f32_16x16x32_bf16 v[192:195],v[80:83],v[16:19],v[192:195]\n"
"buffer_load_dwordx4 v110,s[28:31],s86 offen lds\n"
"ds_read_b64_tr_b16 a[182:183],v107 offset:4104\n"
"v_mfma_f32_16x16x32_bf16 v[192:195],v[84:87],v[20:23],v[192:195]\n"
"s_add_u32 m0,s85,148480\n"
"ds_read_b64_tr_b16 a[184:185],v107 offset:8192\n"
"ds_read_b64_tr_b16 a[186:187],v107 offset:12288\n"
"v_mfma_f32_16x16x32_bf16 v[224:227],v[84:87],v[28:31],v[224:227]\n"
"buffer_load_dwordx4 v111,s[28:31],s86 offen lds\n"
"v_mfma_f32_16x16x32_bf16 v[224:227],v[80:83],v[24:27],v[224:227]\n"
"s_xor_b32 s86,s86,262144\n"
"ds_read_b64_tr_b16 a[188:189],v107 offset:8200\n"
"ds_read_b64_tr_b16 a[190:191],v107 offset:12296\n"
"v_mfma_f32_16x16x32_bf16 v[228:231],v[88:91],v[24:27],v[228:231]\n"
"ds_read_b64_tr_b16 v[80:81],v100 offset:32768\n"
"s_waitcnt vmcnt(9)\n"
"v_xor_b32 v116,s80,v118\n"
"v_mfma_f32_16x16x32_bf16 v[228:231],v[92:95],v[28:31],v[228:231]\n"
"ds_read_b64_tr_b16 v[82:83],v100 offset:33792\n"
"v_xor_b32 v117,s81,v119\n"
"v_mfma_f32_16x16x32_bf16 v[196:199],v[92:95],v[20:23],v[196:199]\n"
"ds_read_b64_tr_b16 v[84:85],v100 offset:40960\n"
"v_or_b32 v116,v116,v117\n"
"v_mfma_f32_16x16x32_bf16 v[196:199],v[88:91],v[16:19],v[196:199]\n"
"s_add_u32 m0,s85,155648\n"
"ds_read_b64_tr_b16 v[86:87],v100 offset:41984\n"
"v_mfma_f32_16x16x32_bf16 v[164:167],v[88:91],v[8:11],v[164:167]\n"
"buffer_load_dwordx4 v110,s[28:31],s86 offen lds\n"
"v_mfma_f32_16x16x32_bf16 v[164:167],v[92:95],v[12:15],v[164:167]\n"
"s_add_u32 m0,s85,156672\n"
"v_cmp_ne_u32 vcc,0,v116\n"
"v_mfma_f32_16x16x32_bf16 v[132:135],v[92:95],v[4:7],v[132:135]\n"
"buffer_load_dwordx4 v111,s[28:31],s86 offen lds\n"
"v_mfma_f32_16x16x32_bf16 v[132:135],v[88:91],v[0:3],v[132:135]\n"
"s_xor_b32 s86,s86,16515072\n"
"s_cbranch_vccnz .Lfk_kv_s\n"
".Lfk_kv_ok:\n"
"v_mfma_f32_16x16x32_bf16 v[136:139],a[128:131],v[0:3],v[136:139]\n"
"ds_read_b64_tr_b16 v[88:89],v101 offset:32768\n"
"s_mov_b32 s54,-229376\n"
"v_mfma_f32_16x16x32_bf16 v[136:139],a[132:135],v[4:7],v[136:139]\n"
"ds_read_b64_tr_b16 v[90:91],v101 offset:33792\n"
"s_lshl_b32 s55,s52,10\n"
"v_mfma_f32_16x16x32_bf16 v[168:171],a[132:135],v[12:15],v[168:171]\n"
"ds_read_b64_tr_b16 v[92:93],v101 offset:40960\n"
"s_add_u32 s55,s55,131072\n"
"v_mfma_f32_16x16x32_bf16 v[168:171],a[128:131],v[8:11],v[168:171]\n"
"s_add_u32 m0,s85,81920\n"
"s_add_u32 s77,s73,16384\n"
"ds_read_b64_tr_b16 v[94:95],v101 offset:41984\n"
"v_mfma_f32_16x16x32_bf16 v[200:203],a[128:131],v[16:19],v[200:203]\n"
"buffer_load_dwordx4 v112,s[24:27],s77 offen offset:0 sc1 lds\n"
"v_mfma_f32_16x16x32_bf16 v[200:203],a[132:135],v[20:23],v[200:203]\n"
"s_mov_b32 s22,229376\n"
"v_lshrrev_b32 v121,7,v112\n"
"v_mfma_f32_16x16x32_bf16 v[232:235],a[132:135],v[28:31],v[232:235]\n"
"buffer_load_dwordx4 v112,s[24:27],s77 offen offset:1024 sc1 lds\n"
"v_mfma_f32_16x16x32_bf16 v[232:235],a[128:131],v[24:27],v[232:235]\n"
"v_mul_u32_u24 v121,7168,v121\n"
"v_lshlrev_b32 v123,3,v112\n"
"v_mfma_f32_16x16x32_bf16 v[236:239],a[136:139],v[24:27],v[236:239]\n"
"ds_read_b64_tr_b16 a[128:129],v102 offset:32768\n"
"v_and_b32 v122,0x380,v123\n"
"v_mfma_f32_16x16x32_bf16 v[236:239],a[140:143],v[28:31],v[236:239]\n"
"ds_read_b64_tr_b16 a[130:131],v102 offset:33792\n"
"v_min_u32 v122,0x300,v122\n"
"v_mfma_f32_16x16x32_bf16 v[204:207],a[140:143],v[20:23],v[204:207]\n"
"ds_read_b64_tr_b16 a[132:133],v102 offset:40960\n"
"v_add_u32 v122,v121,v122\n"
"v_mfma_f32_16x16x32_bf16 v[204:207],a[136:139],v[16:19],v[204:207]\n"
"s_add_u32 m0,s85,65536\n"
"s_add_u32 s77,s73,65536\n"
"ds_read_b64_tr_b16 a[134:135],v102 offset:41984\n"
"v_mfma_f32_16x16x32_bf16 v[172:175],a[136:139],v[8:11],v[172:175]\n"
"buffer_load_dwordx4 v112,s[24:27],s77 offen offset:0 sc1 lds\n"
"v_mfma_f32_16x16x32_bf16 v[172:175],a[140:143],v[12:15],v[172:175]\n"
"v_mfma_f32_16x16x32_bf16 v[140:143],a[140:143],v[4:7],v[140:143]\n"
"buffer_load_dwordx4 v112,s[24:27],s77 offen offset:1024 sc1 lds\n"
"v_mfma_f32_16x16x32_bf16 v[140:143],a[136:139],v[0:3],v[140:143]\n"
"v_mfma_f32_16x16x32_bf16 v[144:147],v[32:35],v[0:3],v[144:147]\n"
"ds_read_b64_tr_b16 a[136:137],v103 offset:32768\n"
"v_mfma_f32_16x16x32_bf16 v[144:147],v[36:39],v[4:7],v[144:147]\n"
"ds_read_b64_tr_b16 a[138:139],v103 offset:33792\n"
"v_mfma_f32_16x16x32_bf16 v[176:179],v[36:39],v[12:15],v[176:179]\n"
"ds_read_b64_tr_b16 a[140:141],v103 offset:40960\n"
"v_mfma_f32_16x16x32_bf16 v[176:179],v[32:35],v[8:11],v[176:179]\n"
"s_add_u32 m0,s85,73728\n"
"s_add_u32 s77,s73,24576\n"
"ds_read_b64_tr_b16 a[142:143],v103 offset:41984\n"
"v_mfma_f32_16x16x32_bf16 v[208:211],v[32:35],v[16:19],v[208:211]\n"
"buffer_load_dwordx4 v112,s[24:27],s77 offen offset:0 sc1 lds\n"
"v_mfma_f32_16x16x32_bf16 v[208:211],v[36:39],v[20:23],v[208:211]\n"
"ds_read_b128 a[96:99],v96 offset:0\n"
"v_mfma_f32_16x16x32_bf16 v[240:243],v[36:39],v[28:31],v[240:243]\n"
"buffer_load_dwordx4 v112,s[24:27],s77 offen offset:1024 sc1 lds\n"
"v_mfma_f32_16x16x32_bf16 v[240:243],v[32:35],v[24:27],v[240:243]\n"
"ds_read_b128 a[104:107],v96 offset:1024\n"
"v_mfma_f32_16x16x32_bf16 v[244:247],v[40:43],v[24:27],v[244:247]\n"
"ds_read_b64_tr_b16 v[32:33],v104 offset:32768\n"
"v_mfma_f32_16x16x32_bf16 v[244:247],v[44:47],v[28:31],v[244:247]\n"
"ds_read_b64_tr_b16 v[34:35],v104 offset:33792\n"
"v_mfma_f32_16x16x32_bf16 v[212:215],v[44:47],v[20:23],v[212:215]\n"
"ds_read_b64_tr_b16 v[36:37],v104 offset:40960\n"
"v_mfma_f32_16x16x32_bf16 v[212:215],v[40:43],v[16:19],v[212:215]\n"
"ds_read_b64_tr_b16 v[38:39],v104 offset:41984\n"
"v_mfma_f32_16x16x32_bf16 v[180:183],v[40:43],v[8:11],v[180:183]\n"
"v_mfma_f32_16x16x32_bf16 v[180:183],v[44:47],v[12:15],v[180:183]\n"
"ds_read_b128 a[112:115],v96 offset:2048\n"
"v_mfma_f32_16x16x32_bf16 v[148:151],v[44:47],v[4:7],v[148:151]\n"
"v_mfma_f32_16x16x32_bf16 v[148:151],v[40:43],v[0:3],v[148:151]\n"
"ds_read_b128 a[120:123],v96 offset:3072\n"
"v_mfma_f32_16x16x32_bf16 v[152:155],v[48:51],v[0:3],v[152:155]\n"
"ds_read_b64_tr_b16 v[40:41],v105 offset:32768\n"
"v_mfma_f32_16x16x32_bf16 v[152:155],v[52:55],v[4:7],v[152:155]\n"
"ds_read_b64_tr_b16 v[42:43],v105 offset:33792\n"
"v_mfma_f32_16x16x32_bf16 v[184:187],v[52:55],v[12:15],v[184:187]\n"
"ds_read_b64_tr_b16 v[44:45],v105 offset:40960\n"
"v_mfma_f32_16x16x32_bf16 v[184:187],v[48:51],v[8:11],v[184:187]\n"
"ds_read_b64_tr_b16 v[46:47],v105 offset:41984\n"
"v_mfma_f32_16x16x32_bf16 v[216:219],v[48:51],v[16:19],v[216:219]\n"
"ds_read_b128 a[100:103],v96 offset:16384\n"
"v_mfma_f32_16x16x32_bf16 v[216:219],v[52:55],v[20:23],v[216:219]\n"
"ds_read_b128 a[108:111],v96 offset:17408\n"
"v_mfma_f32_16x16x32_bf16 v[248:251],v[52:55],v[28:31],v[248:251]\n"
"ds_read_b128 a[116:119],v96 offset:18432\n"
"v_mfma_f32_16x16x32_bf16 v[248:251],v[48:51],v[24:27],v[248:251]\n"
"ds_read_b128 a[124:127],v96 offset:19456\n"
"v_mfma_f32_16x16x32_bf16 v[252:255],v[56:59],v[24:27],v[252:255]\n"
"v_mfma_f32_16x16x32_bf16 v[252:255],v[60:63],v[28:31],v[252:255]\n"
"v_mfma_f32_16x16x32_bf16 v[220:223],v[60:63],v[20:23],v[220:223]\n"
"v_mfma_f32_16x16x32_bf16 v[220:223],v[56:59],v[16:19],v[220:223]\n"
"v_mfma_f32_16x16x32_bf16 v[188:191],v[56:59],v[8:11],v[188:191]\n"
"v_mfma_f32_16x16x32_bf16 v[188:191],v[60:63],v[12:15],v[188:191]\n"
"v_mfma_f32_16x16x32_bf16 v[156:159],v[60:63],v[4:7],v[156:159]\n"
"v_mfma_f32_16x16x32_bf16 v[156:159],v[56:59],v[0:3],v[156:159]\n"
"s_waitcnt lgkmcnt(0)\n"
"s_barrier\n"
"v_mfma_f32_16x16x32_bf16 v[128:131],v[64:67],a[96:99],v[128:131]\n"
"ds_read_b64_tr_b16 v[48:49],v98 offset:0\n"
"ds_read_b64_tr_b16 v[56:57],v99 offset:0\n"
"v_mfma_f32_16x16x32_bf16 v[128:131],v[68:71],a[100:103],v[128:131]\n"
"ds_read_b64_tr_b16 v[50:51],v98 offset:1024\n"
"ds_read_b64_tr_b16 v[58:59],v99 offset:1024\n"
"v_mfma_f32_16x16x32_bf16 v[160:163],v[68:71],a[108:111],v[160:163]\n"
"ds_read_b64_tr_b16 v[52:53],v98 offset:8192\n"
"ds_read_b64_tr_b16 v[60:61],v99 offset:8192\n"
"v_mfma_f32_16x16x32_bf16 v[160:163],v[64:67],a[104:107],v[160:163]\n"
"s_add_u32 m0,s78,0\n"
"ds_read_b64_tr_b16 v[54:55],v98 offset:9216\n"
"ds_read_b64_tr_b16 v[62:63],v99 offset:9216\n"
"v_mfma_f32_16x16x32_bf16 v[192:195],v[64:67],a[112:115],v[192:195]\n"
"buffer_load_dwordx4 v112,s[24:27],s76 offen offset:0 sc1 lds\n"
"v_mfma_f32_16x16x32_bf16 v[192:195],v[68:71],a[116:119],v[192:195]\n"
"v_mfma_f32_16x16x32_bf16 v[224:227],v[68:71],a[124:127],v[224:227]\n"
"v_mfma_f32_16x16x32_bf16 v[224:227],v[64:67],a[120:123],v[224:227]\n"
"v_mfma_f32_16x16x32_bf16 v[228:231],v[72:75],a[120:123],v[228:231]\n"
"ds_read_b64_tr_b16 v[64:65],v100 offset:0\n"
"v_mfma_f32_16x16x32_bf16 v[228:231],v[76:79],a[124:127],v[228:231]\n"
"ds_read_b64_tr_b16 v[66:67],v100 offset:1024\n"
"v_mfma_f32_16x16x32_bf16 v[196:199],v[76:79],a[116:119],v[196:199]\n"
"ds_read_b64_tr_b16 v[68:69],v100 offset:8192\n"
"v_mfma_f32_16x16x32_bf16 v[196:199],v[72:75],a[112:115],v[196:199]\n"
"ds_read_b64_tr_b16 v[70:71],v100 offset:9216\n"
"v_mfma_f32_16x16x32_bf16 v[164:167],v[72:75],a[104:107],v[164:167]\n"
"buffer_load_dwordx4 v112,s[24:27],s76 offen offset:1024 sc1 lds\n"
"v_mfma_f32_16x16x32_bf16 v[164:167],v[76:79],a[108:111],v[164:167]\n"
"v_mfma_f32_16x16x32_bf16 v[132:135],v[76:79],a[100:103],v[132:135]\n"
"buffer_load_dwordx4 v112,s[24:27],s76 offen offset:2048 sc1 lds\n"
"v_mfma_f32_16x16x32_bf16 v[132:135],v[72:75],a[96:99],v[132:135]\n"
"v_mfma_f32_16x16x32_bf16 v[136:139],v[80:83],a[96:99],v[136:139]\n"
"ds_read_b64_tr_b16 v[72:73],v101 offset:0\n"
"v_mfma_f32_16x16x32_bf16 v[136:139],v[84:87],a[100:103],v[136:139]\n"
"ds_read_b64_tr_b16 v[74:75],v101 offset:1024\n"
"v_mfma_f32_16x16x32_bf16 v[168:171],v[84:87],a[108:111],v[168:171]\n"
"ds_read_b64_tr_b16 v[76:77],v101 offset:8192\n"
"v_mfma_f32_16x16x32_bf16 v[168:171],v[80:83],a[104:107],v[168:171]\n"
"ds_read_b64_tr_b16 v[78:79],v101 offset:9216\n"
"v_mfma_f32_16x16x32_bf16 v[200:203],v[80:83],a[112:115],v[200:203]\n"
"buffer_load_dwordx4 v112,s[24:27],s76 offen offset:3072 sc1 lds\n"
"v_mfma_f32_16x16x32_bf16 v[200:203],v[84:87],a[116:119],v[200:203]\n"
"s_xor_b32 s76,s76,16384\n"
"v_mfma_f32_16x16x32_bf16 v[232:235],v[84:87],a[124:127],v[232:235]\n"
"v_mfma_f32_16x16x32_bf16 v[232:235],v[80:83],a[120:123],v[232:235]\n"
"v_mfma_f32_16x16x32_bf16 v[236:239],v[88:91],a[120:123],v[236:239]\n"
"ds_read_b64_tr_b16 v[80:81],v102 offset:0\n"
"v_mfma_f32_16x16x32_bf16 v[236:239],v[92:95],a[124:127],v[236:239]\n"
"ds_read_b64_tr_b16 v[82:83],v102 offset:1024\n"
"v_mfma_f32_16x16x32_bf16 v[204:207],v[92:95],a[116:119],v[204:207]\n"
"ds_read_b64_tr_b16 v[84:85],v102 offset:8192\n"
"v_mfma_f32_16x16x32_bf16 v[204:207],v[88:91],a[112:115],v[204:207]\n"
"s_add_u32 m0,s78,16384\n"
"ds_read_b64_tr_b16 v[86:87],v102 offset:9216\n"
"v_mfma_f32_16x16x32_bf16 v[172:175],v[88:91],a[104:107],v[172:175]\n"
"buffer_load_dwordx4 v112,s[24:27],s76 offen offset:0 sc1 lds\n"
"v_mfma_f32_16x16x32_bf16 v[172:175],v[92:95],a[108:111],v[172:175]\n"
"v_mfma_f32_16x16x32_bf16 v[140:143],v[92:95],a[100:103],v[140:143]\n"
"buffer_load_dwordx4 v112,s[24:27],s76 offen offset:1024 sc1 lds\n"
"v_mfma_f32_16x16x32_bf16 v[140:143],v[88:91],a[96:99],v[140:143]\n"
".Lfk_q27:\n"
"s_waitcnt vmcnt(16)\n"
"v_mfma_f32_16x16x32_bf16 v[144:147],a[128:131],a[96:99],v[144:147]\n"
"ds_read_b64_tr_b16 v[88:89],v103 offset:0\n"
"v_mfma_f32_16x16x32_bf16 v[144:147],a[132:135],a[100:103],v[144:147]\n"
"ds_read_b64_tr_b16 v[90:91],v103 offset:1024\n"
"v_mfma_f32_16x16x32_bf16 v[176:179],a[132:135],a[108:111],v[176:179]\n"
"ds_read_b64_tr_b16 v[92:93],v103 offset:8192\n"
"v_mfma_f32_16x16x32_bf16 v[176:179],a[128:131],a[104:107],v[176:179]\n"
"ds_read_b64_tr_b16 v[94:95],v103 offset:9216\n"
"v_mfma_f32_16x16x32_bf16 v[208:211],a[128:131],a[112:115],v[208:211]\n"
"buffer_load_dwordx4 v112,s[24:27],s76 offen offset:2048 sc1 lds\n"
"v_mfma_f32_16x16x32_bf16 v[208:211],a[132:135],a[116:119],v[208:211]\n"
"ds_read_b128 v[0:3],v96 offset:32768\n"
"v_mfma_f32_16x16x32_bf16 v[240:243],a[132:135],a[124:127],v[240:243]\n"
"v_mfma_f32_16x16x32_bf16 v[240:243],a[128:131],a[120:123],v[240:243]\n"
"ds_read_b128 v[8:11],v96 offset:33792\n"
"v_mfma_f32_16x16x32_bf16 v[244:247],a[136:139],a[120:123],v[244:247]\n"
"ds_read_b64_tr_b16 a[128:129],v104 offset:0\n"
"v_mfma_f32_16x16x32_bf16 v[244:247],a[140:143],a[124:127],v[244:247]\n"
"ds_read_b64_tr_b16 a[130:131],v104 offset:1024\n"
"v_mfma_f32_16x16x32_bf16 v[212:215],a[140:143],a[116:119],v[212:215]\n"
"ds_read_b64_tr_b16 a[132:133],v104 offset:8192\n"
"v_mfma_f32_16x16x32_bf16 v[212:215],a[136:139],a[112:115],v[212:215]\n"
"ds_read_b64_tr_b16 a[134:135],v104 offset:9216\n"
"v_mfma_f32_16x16x32_bf16 v[180:183],a[136:139],a[104:107],v[180:183]\n"
"buffer_load_dwordx4 v112,s[24:27],s76 offen offset:3072 sc1 lds\n"
"v_mfma_f32_16x16x32_bf16 v[180:183],a[140:143],a[108:111],v[180:183]\n"
"s_add_u32 m0,s85,122880\n"
"s_add_u32 s77,s73,0\n"
"ds_read_b128 v[16:19],v96 offset:34816\n"
"v_mfma_f32_16x16x32_bf16 v[148:151],a[140:143],a[100:103],v[148:151]\n"
"buffer_load_dwordx4 v112,s[24:27],s77 offen offset:0 sc1 lds\n"
"v_mfma_f32_16x16x32_bf16 v[148:151],a[136:139],a[96:99],v[148:151]\n"
"s_xor_b32 s76,s76,1032192\n"
"ds_read_b128 v[24:27],v96 offset:35840\n"
"v_mfma_f32_16x16x32_bf16 v[152:155],v[32:35],a[96:99],v[152:155]\n"
"ds_read_b64_tr_b16 a[136:137],v105 offset:0\n"
"v_mfma_f32_16x16x32_bf16 v[152:155],v[36:39],a[100:103],v[152:155]\n"
"ds_read_b64_tr_b16 a[138:139],v105 offset:1024\n"
"v_mfma_f32_16x16x32_bf16 v[184:187],v[36:39],a[108:111],v[184:187]\n"
"ds_read_b64_tr_b16 a[140:141],v105 offset:8192\n"
"v_mfma_f32_16x16x32_bf16 v[184:187],v[32:35],a[104:107],v[184:187]\n"
"ds_read_b64_tr_b16 a[142:143],v105 offset:9216\n"
"v_mfma_f32_16x16x32_bf16 v[216:219],v[32:35],a[112:115],v[216:219]\n"
"ds_read_b128 v[4:7],v96 offset:49152\n"
"v_mfma_f32_16x16x32_bf16 v[216:219],v[36:39],a[116:119],v[216:219]\n"
"ds_read_b128 v[12:15],v96 offset:50176\n"
"v_mfma_f32_16x16x32_bf16 v[248:251],v[36:39],a[124:127],v[248:251]\n"
"ds_read_b128 v[20:23],v96 offset:51200\n"
"v_mfma_f32_16x16x32_bf16 v[248:251],v[32:35],a[120:123],v[248:251]\n"
"ds_read_b128 v[28:31],v96 offset:52224\n"
"v_mfma_f32_16x16x32_bf16 v[252:255],v[40:43],a[120:123],v[252:255]\n"
"buffer_load_dwordx4 v112,s[24:27],s77 offen offset:1024 sc1 lds\n"
"v_mfma_f32_16x16x32_bf16 v[252:255],v[44:47],a[124:127],v[252:255]\n"
"v_mfma_f32_16x16x32_bf16 v[220:223],v[44:47],a[116:119],v[220:223]\n"
"s_add_u32 m0,s85,114688\n"
"s_add_u32 s77,s73,32768\n"
"v_mfma_f32_16x16x32_bf16 v[220:223],v[40:43],a[112:115],v[220:223]\n"
"buffer_load_dwordx4 v112,s[24:27],s77 offen offset:0 sc1 lds\n"
"v_mfma_f32_16x16x32_bf16 v[188:191],v[40:43],a[104:107],v[188:191]\n"
"v_mfma_f32_16x16x32_bf16 v[188:191],v[44:47],a[108:111],v[188:191]\n"
"v_mfma_f32_16x16x32_bf16 v[156:159],v[44:47],a[100:103],v[156:159]\n"
"buffer_load_dwordx4 v112,s[24:27],s77 offen offset:1024 sc1 lds\n"
"v_mfma_f32_16x16x32_bf16 v[156:159],v[40:43],a[96:99],v[156:159]\n"
"s_waitcnt vmcnt(18) lgkmcnt(0)\n"
"s_barrier\n"
"v_mfma_f32_16x16x32_bf16 v[128:131],v[48:51],v[0:3],v[128:131]\n"
"ds_read_b64_tr_b16 v[32:33],v98 offset:49152\n"
"ds_read_b64_tr_b16 v[40:41],v99 offset:49152\n"
"v_mfma_f32_16x16x32_bf16 v[128:131],v[52:55],v[4:7],v[128:131]\n"
"ds_read_b64_tr_b16 v[34:35],v98 offset:50176\n"
"ds_read_b64_tr_b16 v[42:43],v99 offset:50176\n"
"v_mfma_f32_16x16x32_bf16 v[160:163],v[52:55],v[12:15],v[160:163]\n"
"s_add_u32 m0,s85,98304\n"
"ds_read_b64_tr_b16 v[36:37],v98 offset:57344\n"
"ds_read_b64_tr_b16 v[44:45],v99 offset:57344\n"
"v_mfma_f32_16x16x32_bf16 v[160:163],v[48:51],v[8:11],v[160:163]\n"
"s_add_u32 s77,s73,49152\n"
"ds_read_b64_tr_b16 v[38:39],v98 offset:58368\n"
"ds_read_b64_tr_b16 v[46:47],v99 offset:58368\n"
"v_mfma_f32_16x16x32_bf16 v[192:195],v[48:51],v[16:19],v[192:195]\n"
"buffer_load_dwordx4 v112,s[24:27],s77 offen offset:0 sc1 lds\n"
"v_mfma_f32_16x16x32_bf16 v[192:195],v[52:55],v[20:23],v[192:195]\n"
"v_mfma_f32_16x16x32_bf16 v[224:227],v[52:55],v[28:31],v[224:227]\n"
"buffer_load_dwordx4 v112,s[24:27],s77 offen offset:1024 sc1 lds\n"
"v_mfma_f32_16x16x32_bf16 v[224:227],v[48:51],v[24:27],v[224:227]\n"
"v_mfma_f32_16x16x32_bf16 v[228:231],v[56:59],v[24:27],v[228:231]\n"
"ds_read_b64_tr_b16 v[48:49],v100 offset:49152\n"
"v_mfma_f32_16x16x32_bf16 v[228:231],v[60:63],v[28:31],v[228:231]\n"
"ds_read_b64_tr_b16 v[50:51],v100 offset:50176\n"
"v_mfma_f32_16x16x32_bf16 v[196:199],v[60:63],v[20:23],v[196:199]\n"
"ds_read_b64_tr_b16 v[52:53],v100 offset:57344\n"
"v_mfma_f32_16x16x32_bf16 v[196:199],v[56:59],v[16:19],v[196:199]\n"
"s_add_u32 m0,s85,106496\n"
"s_add_u32 s77,s73,8192\n"
"ds_read_b64_tr_b16 v[54:55],v100 offset:58368\n"
"v_mfma_f32_16x16x32_bf16 v[164:167],v[56:59],v[8:11],v[164:167]\n"
"buffer_load_dwordx4 v112,s[24:27],s77 offen offset:0 sc1 lds\n"
"v_mfma_f32_16x16x32_bf16 v[164:167],v[60:63],v[12:15],v[164:167]\n"
"v_mfma_f32_16x16x32_bf16 v[132:135],v[60:63],v[4:7],v[132:135]\n"
"buffer_load_dwordx4 v112,s[24:27],s77 offen offset:1024 sc1 lds\n"
"v_mfma_f32_16x16x32_bf16 v[132:135],v[56:59],v[0:3],v[132:135]\n"
"v_mfma_f32_16x16x32_bf16 v[136:139],v[64:67],v[0:3],v[136:139]\n"
"ds_read_b64_tr_b16 v[56:57],v101 offset:49152\n"
"v_mfma_f32_16x16x32_bf16 v[136:139],v[68:71],v[4:7],v[136:139]\n"
"ds_read_b64_tr_b16 v[58:59],v101 offset:50176\n"
"v_mfma_f32_16x16x32_bf16 v[168:171],v[68:71],v[12:15],v[168:171]\n"
"ds_read_b64_tr_b16 v[60:61],v101 offset:57344\n"
"v_mfma_f32_16x16x32_bf16 v[168:171],v[64:67],v[8:11],v[168:171]\n"
"ds_read_b64_tr_b16 v[62:63],v101 offset:58368\n"
"v_mfma_f32_16x16x32_bf16 v[200:203],v[64:67],v[16:19],v[200:203]\n"
"buffer_load_dword a148,v122,s[48:51],0 offen\n"
"v_mfma_f32_16x16x32_bf16 v[200:203],v[68:71],v[20:23],v[200:203]\n"
"v_mfma_f32_16x16x32_bf16 v[232:235],v[68:71],v[28:31],v[232:235]\n"
"v_mfma_f32_16x16x32_bf16 v[232:235],v[64:67],v[24:27],v[232:235]\n"
"v_mfma_f32_16x16x32_bf16 v[236:239],v[72:75],v[24:27],v[236:239]\n"
"ds_read_b64_tr_b16 v[64:65],v102 offset:49152\n"
"v_mfma_f32_16x16x32_bf16 v[236:239],v[76:79],v[28:31],v[236:239]\n"
"ds_read_b64_tr_b16 v[66:67],v102 offset:50176\n"
"v_mfma_f32_16x16x32_bf16 v[204:207],v[76:79],v[20:23],v[204:207]\n"
"ds_read_b64_tr_b16 v[68:69],v102 offset:57344\n"
"v_mfma_f32_16x16x32_bf16 v[204:207],v[72:75],v[16:19],v[204:207]\n"
"ds_read_b64_tr_b16 v[70:71],v102 offset:58368\n"
"v_mfma_f32_16x16x32_bf16 v[172:175],v[72:75],v[8:11],v[172:175]\n"
"v_mfma_f32_16x16x32_bf16 v[172:175],v[76:79],v[12:15],v[172:175]\n"
"v_mfma_f32_16x16x32_bf16 v[140:143],v[76:79],v[4:7],v[140:143]\n"
"v_mfma_f32_16x16x32_bf16 v[140:143],v[72:75],v[0:3],v[140:143]\n"
"s_waitcnt vmcnt(9)\n"
"v_mfma_f32_16x16x32_bf16 v[144:147],v[80:83],v[0:3],v[144:147]\n"
"ds_read_b64_tr_b16 v[72:73],v103 offset:49152\n"
"s_mov_b32 s90,0\n"
"v_mfma_f32_16x16x32_bf16 v[144:147],v[84:87],v[4:7],v[144:147]\n"
"ds_read_b64_tr_b16 v[74:75],v103 offset:50176\n"
"s_mov_b32 s16,0xc21c0000\n"
"v_mfma_f32_16x16x32_bf16 v[176:179],v[84:87],v[12:15],v[176:179]\n"
"ds_read_b64_tr_b16 v[76:77],v103 offset:57344\n"
"s_mov_b32 s17,0x41a00000\n"
"v_mfma_f32_16x16x32_bf16 v[176:179],v[80:83],v[8:11],v[176:179]\n"
"ds_read_b64_tr_b16 v[78:79],v103 offset:58368\n"
"s_mov_b32 s89,0\n"
"v_mfma_f32_16x16x32_bf16 v[208:211],v[80:83],v[16:19],v[208:211]\n"
"s_xor_b32 s82,s78,8192\n"
"v_add_u32 v116,s82,v112\n"
"v_mfma_f32_16x16x32_bf16 v[208:211],v[84:87],v[20:23],v[208:211]\n"
"ds_read_b128 a[96:99],v96 offset:0\n"
"v_add_u32 v117,4096,v112\n"
"v_mfma_f32_16x16x32_bf16 v[240:243],v[84:87],v[28:31],v[240:243]\n"
"s_getpc_b64 s[20:21]\n"
".Lfk_evp:\n"
"s_add_u32 s20,s20,.Lfk_ev-.Lfk_evp\n"
"s_addc_u32 s21,s21,0\n"
"v_mfma_f32_16x16x32_bf16 v[240:243],v[80:83],v[24:27],v[240:243]\n"
"ds_read_b128 a[104:107],v96 offset:1024\n"
"v_mfma_f32_16x16x32_bf16 v[244:247],v[88:91],v[24:27],v[244:247]\n"
"ds_read_b64_tr_b16 v[80:81],v104 offset:49152\n"
"v_mfma_f32_16x16x32_bf16 v[244:247],v[92:95],v[28:31],v[244:247]\n"
"ds_read_b64_tr_b16 v[82:83],v104 offset:50176\n"
"v_mfma_f32_16x16x32_bf16 v[212:215],v[92:95],v[20:23],v[212:215]\n"
"ds_read_b64_tr_b16 v[84:85],v104 offset:57344\n"
"v_mfma_f32_16x16x32_bf16 v[212:215],v[88:91],v[16:19],v[212:215]\n"
"ds_read_b64_tr_b16 v[86:87],v104 offset:58368\n"
"v_mfma_f32_16x16x32_bf16 v[180:183],v[88:91],v[8:11],v[180:183]\n"
"buffer_store_dwordx4 a[156:159],v112,s[24:27],s74 offen offset:0 sc1\n"
"v_mfma_f32_16x16x32_bf16 v[180:183],v[92:95],v[12:15],v[180:183]\n"
"ds_read_b128 a[112:115],v96 offset:2048\n"
"v_mfma_f32_16x16x32_bf16 v[148:151],v[92:95],v[4:7],v[148:151]\n"
"v_mfma_f32_16x16x32_bf16 v[148:151],v[88:91],v[0:3],v[148:151]\n"
"ds_read_b128 a[120:123],v96 offset:3072\n"
"v_mfma_f32_16x16x32_bf16 v[152:155],a[128:131],v[0:3],v[152:155]\n"
"ds_read_b64_tr_b16 v[88:89],v105 offset:49152\n"
"v_mfma_f32_16x16x32_bf16 v[152:155],a[132:135],v[4:7],v[152:155]\n"
"ds_read_b64_tr_b16 v[90:91],v105 offset:50176\n"
"v_mfma_f32_16x16x32_bf16 v[184:187],a[132:135],v[12:15],v[184:187]\n"
"ds_read_b64_tr_b16 v[92:93],v105 offset:57344\n"
"v_mfma_f32_16x16x32_bf16 v[184:187],a[128:131],v[8:11],v[184:187]\n"
"ds_read_b64_tr_b16 v[94:95],v105 offset:58368\n"
"v_mfma_f32_16x16x32_bf16 v[216:219],a[128:131],v[16:19],v[216:219]\n"
"ds_read_b128 a[100:103],v96 offset:16384\n"
"v_mfma_f32_16x16x32_bf16 v[216:219],a[132:135],v[20:23],v[216:219]\n"
"ds_read_b128 a[108:111],v96 offset:17408\n"
"v_add_u32 v113,65536,v112\n"
"v_mfma_f32_16x16x32_bf16 v[248:251],a[132:135],v[28:31],v[248:251]\n"
"ds_read_b128 a[116:119],v96 offset:18432\n"
"v_add_u32 v114,65536,v112\n"
"v_mfma_f32_16x16x32_bf16 v[248:251],a[128:131],v[24:27],v[248:251]\n"
"ds_read_b128 a[124:127],v96 offset:19456\n"
"v_mfma_f32_16x16x32_bf16 v[252:255],a[136:139],v[24:27],v[252:255]\n"
"v_mfma_f32_16x16x32_bf16 v[252:255],a[140:143],v[28:31],v[252:255]\n"
"buffer_store_dwordx4 a[156:159],v112,s[24:27],s74 offen offset:1024 sc1\n"
"v_mfma_f32_16x16x32_bf16 v[220:223],a[140:143],v[20:23],v[220:223]\n"
"v_mfma_f32_16x16x32_bf16 v[220:223],a[136:139],v[16:19],v[220:223]\n"
"v_mfma_f32_16x16x32_bf16 v[188:191],a[136:139],v[8:11],v[188:191]\n"
"v_mfma_f32_16x16x32_bf16 v[188:191],a[140:143],v[12:15],v[188:191]\n"
"buffer_store_dword a156,off,s[24:27],s72 sc1\n"
"v_mfma_f32_16x16x32_bf16 v[156:159],a[140:143],v[4:7],v[156:159]\n"
"v_mfma_f32_16x16x32_bf16 v[156:159],a[136:139],v[0:3],v[156:159]\n"
"s_waitcnt vmcnt(10) lgkmcnt(0)\n"
"s_barrier\n"
"v_mfma_f32_16x16x32_bf16 v[224:227],v[32:35],a[120:123],v[224:227]\n"
"ds_read_b128 a[64:67],v113 offset:57344\n"
"v_mov_b32 v96,0xc1980000\n"
"v_mfma_f32_16x16x32_bf16 v[224:227],v[36:39],a[124:127],v[224:227]\n"
"ds_read_b128 a[68:71],v113 offset:58368\n"
"v_mov_b32 v97,0xc1980000\n"
"v_mfma_f32_16x16x32_bf16 v[228:231],v[44:47],a[124:127],v[228:231]\n"
"ds_read_b128 a[72:75],v113 offset:59392\n"
"v_mov_b32 v98,0xc1980000\n"
"v_mfma_f32_16x16x32_bf16 v[228:231],v[40:43],a[120:123],v[228:231]\n"
"ds_read_b128 a[76:79],v113 offset:60416\n"
"v_mov_b32 v99,0xc1980000\n"
"v_mfma_f32_16x16x32_bf16 v[232:235],v[48:51],a[120:123],v[232:235]\n"
"ds_read_b128 a[80:83],v113 offset:61440\n"
"v_mov_b32 v100,0xc1980000\n"
"v_mfma_f32_16x16x32_bf16 v[232:235],v[52:55],a[124:127],v[232:235]\n"
"ds_read_b128 a[84:87],v113 offset:62464\n"
"v_mov_b32 v101,0xc1980000\n"
"v_mfma_f32_16x16x32_bf16 v[236:239],v[60:63],a[124:127],v[236:239]\n"
"ds_read_b128 a[88:91],v113 offset:63488\n"
"v_mov_b32 v102,0xc1980000\n"
"v_mfma_f32_16x16x32_bf16 v[236:239],v[56:59],a[120:123],v[236:239]\n"
"ds_read_b128 a[92:95],v113 offset:64512\n"
"v_mov_b32 v103,0xc1980000\n"
"v_mfma_f32_16x16x32_bf16 v[240:243],v[64:67],a[120:123],v[240:243]\n"
"v_mov_b32 v104,0xc1980000\n"
"v_mov_b32 v105,0xc1980000\n"
"v_mfma_f32_16x16x32_bf16 v[240:243],v[68:71],a[124:127],v[240:243]\n"
"ds_write_b32 v124,v125\n"
"v_mfma_f32_16x16x32_bf16 v[244:247],v[76:79],a[124:127],v[244:247]\n"
"v_mov_b32 v107,0xc1980000\n"
"v_mov_b32 v108,0xc1980000\n"
"v_mfma_f32_16x16x32_bf16 v[244:247],v[72:75],a[120:123],v[244:247]\n"
"v_mov_b32 v109,0xc1980000\n"
"v_mov_b32 v110,0xc1980000\n"
"v_mfma_f32_16x16x32_bf16 v[248:251],v[80:83],a[120:123],v[248:251]\n"
"v_mov_b32 v106,0xc1980000\n"
"v_mov_b32 v111,0xc1980000\n"
"v_mfma_f32_16x16x32_bf16 v[248:251],v[84:87],a[124:127],v[248:251]\n"
"v_mfma_f32_16x16x32_bf16 v[252:255],v[92:95],a[124:127],v[252:255]\n"
"v_mfma_f32_16x16x32_bf16 v[252:255],v[88:91],a[120:123],v[252:255]\n"
"v_mfma_f32_16x16x32_bf16 v[128:131],v[32:35],a[96:99],v[128:131]\n"
"v_mfma_f32_16x16x32_bf16 v[128:131],v[36:39],a[100:103],v[128:131]\n"
"v_cvt_pk_bf16_f32 v232,v232,v233\n"
"v_cvt_pk_bf16_f32 v233,v234,v235\n"
"v_mfma_f32_16x16x32_bf16 v[132:135],v[44:47],a[100:103],v[132:135]\n"
"v_cvt_pk_bf16_f32 v224,v224,v225\n"
"v_cvt_pk_bf16_f32 v225,v226,v227\n"
"v_mfma_f32_16x16x32_bf16 v[132:135],v[40:43],a[96:99],v[132:135]\n"
"v_cvt_pk_bf16_f32 v226,v228,v229\n"
"v_cvt_pk_bf16_f32 v227,v230,v231\n"
"v_mfma_f32_16x16x32_bf16 v[136:139],v[48:51],a[96:99],v[136:139]\n"
"ds_write_b128 v116,v[224:227] offset:0\n"
"v_mfma_f32_16x16x32_bf16 v[136:139],v[52:55],a[100:103],v[136:139]\n"
"v_cvt_pk_bf16_f32 v234,v236,v237\n"
"v_cvt_pk_bf16_f32 v235,v238,v239\n"
"v_mfma_f32_16x16x32_bf16 v[140:143],v[60:63],a[100:103],v[140:143]\n"
"ds_write_b128 v116,v[232:235] offset:1024\n"
"v_mfma_f32_16x16x32_bf16 v[140:143],v[56:59],a[96:99],v[140:143]\n"
"v_cvt_pk_bf16_f32 v240,v240,v241\n"
"v_cvt_pk_bf16_f32 v241,v242,v243\n"
"v_mfma_f32_16x16x32_bf16 v[144:147],v[64:67],a[96:99],v[144:147]\n"
"v_cvt_pk_bf16_f32 v242,v244,v245\n"
"v_cvt_pk_bf16_f32 v243,v246,v247\n"
"v_mfma_f32_16x16x32_bf16 v[144:147],v[68:71],a[100:103],v[144:147]\n"
"ds_write_b128 v116,v[240:243] offset:2048\n"
"v_cvt_pk_bf16_f32 v248,v248,v249\n"
"v_mfma_f32_16x16x32_bf16 v[148:151],v[76:79],a[100:103],v[148:151]\n"
"v_cvt_pk_bf16_f32 v249,v250,v251\n"
"v_cvt_pk_bf16_f32 v250,v252,v253\n"
"v_cvt_pk_bf16_f32 v251,v254,v255\n"
"v_mfma_f32_16x16x32_bf16 v[148:151],v[72:75],a[96:99],v[148:151]\n"
"ds_write_b128 v116,v[248:251] offset:3072\n"
"v_mfma_f32_16x16x32_bf16 v[152:155],v[80:83],a[96:99],v[152:155]\n"
"ds_read_b128 a[48:51],v116 offset:0\n"
"ds_read_b128 a[52:55],v116 offset:1024\n"
"v_mfma_f32_16x16x32_bf16 v[152:155],v[84:87],a[100:103],v[152:155]\n"
"ds_read_b128 a[56:59],v116 offset:2048\n"
"ds_read_b128 a[60:63],v116 offset:3072\n"
"v_mfma_f32_16x16x32_bf16 v[156:159],v[92:95],a[100:103],v[156:159]\n"
"v_mfma_f32_16x16x32_bf16 v[156:159],v[88:91],a[96:99],v[156:159]\n"
"v_mfma_f32_16x16x32_bf16 v[160:163],v[32:35],a[104:107],v[160:163]\n"
"v_mfma_f32_16x16x32_bf16 v[160:163],v[36:39],a[108:111],v[160:163]\n"
"v_cvt_pk_bf16_f32 v136,v136,v137\n"
"v_cvt_pk_bf16_f32 v137,v138,v139\n"
"v_mfma_f32_16x16x32_bf16 v[164:167],v[44:47],a[108:111],v[164:167]\n"
"v_cvt_pk_bf16_f32 v128,v128,v129\n"
"v_cvt_pk_bf16_f32 v129,v130,v131\n"
"v_mfma_f32_16x16x32_bf16 v[164:167],v[40:43],a[104:107],v[164:167]\n"
"v_cvt_pk_bf16_f32 v130,v132,v133\n"
"v_cvt_pk_bf16_f32 v131,v134,v135\n"
"v_mfma_f32_16x16x32_bf16 v[168:171],v[48:51],a[104:107],v[168:171]\n"
"ds_write_b128 v116,v[128:131] offset:16384\n"
"v_mfma_f32_16x16x32_bf16 v[168:171],v[52:55],a[108:111],v[168:171]\n"
"v_cvt_pk_bf16_f32 v138,v140,v141\n"
"v_cvt_pk_bf16_f32 v139,v142,v143\n"
"v_mfma_f32_16x16x32_bf16 v[172:175],v[60:63],a[108:111],v[172:175]\n"
"ds_write_b128 v116,v[136:139] offset:17408\n"
"v_mfma_f32_16x16x32_bf16 v[172:175],v[56:59],a[104:107],v[172:175]\n"
"v_cvt_pk_bf16_f32 v144,v144,v145\n"
"v_cvt_pk_bf16_f32 v145,v146,v147\n"
"v_mfma_f32_16x16x32_bf16 v[176:179],v[64:67],a[104:107],v[176:179]\n"
"v_cvt_pk_bf16_f32 v146,v148,v149\n"
"v_cvt_pk_bf16_f32 v147,v150,v151\n"
"v_mfma_f32_16x16x32_bf16 v[176:179],v[68:71],a[108:111],v[176:179]\n"
"ds_write_b128 v116,v[144:147] offset:18432\n"
"v_cvt_pk_bf16_f32 v152,v152,v153\n"
"v_mfma_f32_16x16x32_bf16 v[180:183],v[76:79],a[108:111],v[180:183]\n"
"v_cvt_pk_bf16_f32 v153,v154,v155\n"
"v_cvt_pk_bf16_f32 v154,v156,v157\n"
"v_cvt_pk_bf16_f32 v155,v158,v159\n"
"v_mfma_f32_16x16x32_bf16 v[180:183],v[72:75],a[104:107],v[180:183]\n"
"ds_write_b128 v116,v[152:155] offset:19456\n"
"v_mfma_f32_16x16x32_bf16 v[184:187],v[80:83],a[104:107],v[184:187]\n"
"ds_read_b128 a[0:3],v116 offset:16384\n"
"ds_read_b128 a[4:7],v116 offset:17408\n"
"v_mfma_f32_16x16x32_bf16 v[184:187],v[84:87],a[108:111],v[184:187]\n"
"ds_read_b128 a[8:11],v116 offset:18432\n"
"ds_read_b128 a[12:15],v116 offset:19456\n"
"v_mfma_f32_16x16x32_bf16 v[188:191],v[92:95],a[108:111],v[188:191]\n"
"v_mfma_f32_16x16x32_bf16 v[188:191],v[88:91],a[104:107],v[188:191]\n"
"v_mfma_f32_16x16x32_bf16 v[192:195],v[32:35],a[112:115],v[192:195]\n"
"v_mfma_f32_16x16x32_bf16 v[192:195],v[36:39],a[116:119],v[192:195]\n"
"v_cvt_pk_bf16_f32 v168,v168,v169\n"
"v_cvt_pk_bf16_f32 v169,v170,v171\n"
"v_mfma_f32_16x16x32_bf16 v[196:199],v[44:47],a[116:119],v[196:199]\n"
"v_cvt_pk_bf16_f32 v160,v160,v161\n"
"v_cvt_pk_bf16_f32 v161,v162,v163\n"
"v_mfma_f32_16x16x32_bf16 v[196:199],v[40:43],a[112:115],v[196:199]\n"
"v_cvt_pk_bf16_f32 v162,v164,v165\n"
"v_cvt_pk_bf16_f32 v163,v166,v167\n"
"v_mfma_f32_16x16x32_bf16 v[200:203],v[48:51],a[112:115],v[200:203]\n"
"ds_write_b128 v116,v[160:163] offset:49152\n"
"v_mfma_f32_16x16x32_bf16 v[200:203],v[52:55],a[116:119],v[200:203]\n"
"v_cvt_pk_bf16_f32 v170,v172,v173\n"
"v_cvt_pk_bf16_f32 v171,v174,v175\n"
"v_mfma_f32_16x16x32_bf16 v[204:207],v[60:63],a[116:119],v[204:207]\n"
"ds_write_b128 v116,v[168:171] offset:50176\n"
"v_mfma_f32_16x16x32_bf16 v[204:207],v[56:59],a[112:115],v[204:207]\n"
"v_cvt_pk_bf16_f32 v176,v176,v177\n"
"v_cvt_pk_bf16_f32 v177,v178,v179\n"
"v_mfma_f32_16x16x32_bf16 v[208:211],v[64:67],a[112:115],v[208:211]\n"
"v_cvt_pk_bf16_f32 v178,v180,v181\n"
"v_cvt_pk_bf16_f32 v179,v182,v183\n"
"v_mfma_f32_16x16x32_bf16 v[208:211],v[68:71],a[116:119],v[208:211]\n"
"ds_write_b128 v116,v[176:179] offset:51200\n"
"v_cvt_pk_bf16_f32 v184,v184,v185\n"
"v_mfma_f32_16x16x32_bf16 v[212:215],v[76:79],a[116:119],v[212:215]\n"
"v_cvt_pk_bf16_f32 v185,v186,v187\n"
"v_cvt_pk_bf16_f32 v186,v188,v189\n"
"v_cvt_pk_bf16_f32 v187,v190,v191\n"
"v_mfma_f32_16x16x32_bf16 v[212:215],v[72:75],a[112:115],v[212:215]\n"
"ds_write_b128 v116,v[184:187] offset:52224\n"
"v_mfma_f32_16x16x32_bf16 v[216:219],v[80:83],a[112:115],v[216:219]\n"
"ds_read_b128 a[16:19],v116 offset:49152\n"
"ds_read_b128 a[20:23],v116 offset:50176\n"
"v_mfma_f32_16x16x32_bf16 v[216:219],v[84:87],a[116:119],v[216:219]\n"
"ds_read_b128 a[24:27],v116 offset:51200\n"
"ds_read_b128 a[28:31],v116 offset:52224\n"
"v_mfma_f32_16x16x32_bf16 v[220:223],v[92:95],a[116:119],v[220:223]\n"
"s_lshl2_add_u32 s82,s60,s52\n"
"s_lshr_b32 s82,s82,1\n"
"s_cmp_eq_u32 s82,1\n"
"v_mfma_f32_16x16x32_bf16 v[220:223],v[88:91],a[112:115],v[220:223]\n"
"s_cbranch_scc1 .Lfk_head0\n"
"s_waitcnt lgkmcnt(0)\n"
"s_barrier\n"
"v_mfma_f32_16x16x32_bf16 v[24:27],a[64:67],a[48:51],v[108:111]\n"
"s_andn2_b32 m0,s55,16384\n"
"s_add_u32 s54,s54,s22\n"
"v_cvt_pk_bf16_f32 v200,v200,v201\n"
"v_mfma_f32_16x16x32_bf16 v[24:27],a[68:71],a[52:55],v[24:27]\n"
"buffer_load_dwordx4 v115,s[32:35],s54 offen sc1 lds\n"
"v_mfma_f32_16x16x32_bf16 v[24:27],a[72:75],a[56:59],v[24:27]\n"
"v_cvt_pk_bf16_f32 v192,v192,v193\n"
"v_cvt_pk_bf16_f32 v193,v194,v195\n"
"v_mfma_f32_16x16x32_bf16 v[24:27],a[76:79],a[60:63],v[24:27]\n"
"v_cvt_pk_bf16_f32 v194,v196,v197\n"
"v_cvt_pk_bf16_f32 v195,v198,v199\n"
"v_mfma_f32_16x16x32_bf16 v[28:31],a[92:95],a[60:63],v[108:111]\n"
"ds_write_b128 v116,v[192:195] offset:32768\n"
"v_mfma_f32_16x16x32_bf16 v[28:31],a[88:91],a[56:59],v[28:31]\n"
"buffer_store_dwordx4 a[160:163],v112,s[24:27],s3 offen offset:0 sc1\n"
"v_cvt_pk_bf16_f32 v201,v202,v203\n"
"v_mfma_f32_16x16x32_bf16 v[28:31],a[84:87],a[52:55],v[28:31]\n"
"v_cvt_pk_bf16_f32 v202,v204,v205\n"
"v_cvt_pk_bf16_f32 v203,v206,v207\n"
"v_mfma_f32_16x16x32_bf16 v[28:31],a[80:83],a[48:51],v[28:31]\n"
"ds_write_b128 v116,v[200:203] offset:33792\n"
"v_cvt_pk_bf16_f32 v208,v208,v209\n"
"v_mfma_f32_16x16x32_bf16 v[4:7],a[80:83],a[0:3],v[96:99]\n"
"v_cvt_pk_bf16_f32 v209,v210,v211\n"
"v_cvt_pk_bf16_f32 v210,v212,v213\n"
"v_mfma_f32_16x16x32_bf16 v[4:7],a[84:87],a[4:7],v[4:7]\n"
"ds_read_b128 a[96:99],v113 offset:16384\n"
"ds_read_b128 a[100:103],v113 offset:17408\n"
"v_mfma_f32_16x16x32_bf16 v[4:7],a[88:91],a[8:11],v[4:7]\n"
"ds_read_b128 a[104:107],v113 offset:18432\n"
"ds_read_b128 a[108:111],v113 offset:19456\n"
"v_mfma_f32_16x16x32_bf16 v[4:7],a[92:95],a[12:15],v[4:7]\n"
"ds_read_b128 a[112:115],v113 offset:20480\n"
"ds_read_b128 a[116:119],v113 offset:21504\n"
"v_mfma_f32_16x16x32_bf16 v[0:3],a[76:79],a[12:15],v[96:99]\n"
"ds_read_b128 a[120:123],v113 offset:22528\n"
"ds_read_b128 a[124:127],v113 offset:23552\n"
"v_mfma_f32_16x16x32_bf16 v[0:3],a[72:75],a[8:11],v[0:3]\n"
"v_max3_f32 v118,v24,v25,v26\n"
"v_max3_f32 v118,v118,v27,v28\n"
"v_mfma_f32_16x16x32_bf16 v[0:3],a[68:71],a[4:7],v[0:3]\n"
"v_max3_f32 v118,v118,v29,v30\n"
"v_max_f32 v118,v118,v31\n"
"v_mfma_f32_16x16x32_bf16 v[0:3],a[64:67],a[0:3],v[0:3]\n"
"v_mov_b32 v120,v118\n"
"v_cvt_pk_bf16_f32 v211,v214,v215\n"
"v_mfma_f32_16x16x32_bf16 v[8:11],a[64:67],a[16:19],v[100:103]\n"
"buffer_store_dwordx4 a[164:167],v112,s[24:27],s3 offen offset:1024 sc1\n"
"ds_write_b128 v116,v[208:211] offset:34816\n"
"v_mfma_f32_16x16x32_bf16 v[8:11],a[68:71],a[20:23],v[8:11]\n"
"v_cvt_pk_bf16_f32 v216,v216,v217\n"
"v_cvt_pk_bf16_f32 v217,v218,v219\n"
"v_mfma_f32_16x16x32_bf16 v[8:11],a[72:75],a[24:27],v[8:11]\n"
"v_cvt_pk_bf16_f32 v218,v220,v221\n"
"v_cvt_pk_bf16_f32 v219,v222,v223\n"
"v_mfma_f32_16x16x32_bf16 v[8:11],a[76:79],a[28:31],v[8:11]\n"
"v_max3_f32 v119,v0,v1,v2\n"
"v_max3_f32 v119,v119,v3,v4\n"
"v_mfma_f32_16x16x32_bf16 v[12:15],a[92:95],a[28:31],v[100:103]\n"
"v_max3_f32 v119,v119,v5,v6\n"
"v_max_f32 v119,v119,v7\n"
"v_mfma_f32_16x16x32_bf16 v[12:15],a[88:91],a[24:27],v[12:15]\n"
"v_min_f32 v120,v120,v119\n"
"v_max_f32 v118,v118,v119\n"
"v_mfma_f32_16x16x32_bf16 v[12:15],a[84:87],a[20:23],v[12:15]\n"
"ds_write_b128 v116,v[216:219] offset:35840\n"
"ds_read_b128 a[32:35],v116 offset:32768\n"
"v_mfma_f32_16x16x32_bf16 v[12:15],a[80:83],a[16:19],v[12:15]\n"
"ds_read_b128 a[36:39],v116 offset:33792\n"
"ds_read_b128 a[40:43],v116 offset:34816\n"
"ds_read_b128 a[44:47],v116 offset:35840\n"
"s_waitcnt lgkmcnt(0)\n"
"v_mfma_f32_16x16x32_bf16 v[20:23],a[80:83],a[32:35],v[104:107]\n"
"v_mfma_f32_16x16x32_bf16 v[20:23],a[84:87],a[36:39],v[20:23]\n"
"v_mfma_f32_16x16x32_bf16 v[20:23],a[88:91],a[40:43],v[20:23]\n"
"buffer_store_dwordx4 a[224:227],v112,s[24:27],s56 offen offset:0 sc1\n"
"v_mfma_f32_16x16x32_bf16 v[20:23],a[92:95],a[44:47],v[20:23]\n"
"v_max3_f32 v119,v8,v9,v10\n"
"v_mfma_f32_16x16x32_bf16 v[16:19],a[76:79],a[44:47],v[104:107]\n"
"v_max3_f32 v119,v119,v11,v12\n"
"v_max3_f32 v119,v119,v13,v14\n"
"v_mfma_f32_16x16x32_bf16 v[16:19],a[72:75],a[40:43],v[16:19]\n"
"v_max_f32 v119,v119,v15\n"
"v_min_f32 v120,v120,v119\n"
"v_mfma_f32_16x16x32_bf16 v[16:19],a[68:71],a[36:39],v[16:19]\n"
"v_max_f32 v118,v118,v119\n"
"v_mfma_f32_16x16x32_bf16 v[16:19],a[64:67],a[32:35],v[16:19]\n"
"s_nop 7\n"
"v_max3_f32 v119,v16,v17,v18\n"
"v_max3_f32 v119,v119,v19,v20\n"
"v_max3_f32 v119,v119,v21,v22\n"
"v_max_f32 v119,v119,v23\n"
"v_min_f32 v120,v120,v119\n"
"v_max_f32 v118,v118,v119\n"
"v_cmp_gt_f32 s[96:97],s16,v120\n"
"v_cmp_lt_f32 vcc,s93,v118\n"
"s_nop 0\n"
"s_or_b64 s[96:97],s[96:97],vcc\n"
"s_cbranch_scc1 .Lfk_cs_h3\n"
".Lfk_cr_h3:\n"
"s_cmp_ge_u32 s52,2\n"
"s_cbranch_scc1 .Lfk_g0\n"
"s_waitcnt vmcnt(12) lgkmcnt(0)\n"
"s_barrier\n"
"v_mfma_f32_16x16x32_bf16 v[32:35],a[96:99],a[0:3],v[96:99]\n"
"s_add_u32 m0,s55,4096\n"
"v_exp_f32 v0,v0\n"
"v_mfma_f32_16x16x32_bf16 v[32:35],a[100:103],a[4:7],v[32:35]\n"
"s_add_u32 s54,s54,s22\n"
"v_exp_f32 v1,v1\n"
"v_mfma_f32_16x16x32_bf16 v[32:35],a[104:107],a[8:11],v[32:35]\n"
"buffer_load_dwordx4 v115,s[32:35],s54 offen sc1 lds\n"
"v_exp_f32 v2,v2\n"
"v_cvt_pk_bf16_f32 v64,v0,v1\n"
"v_mfma_f32_16x16x32_bf16 v[32:35],a[108:111],a[12:15],v[32:35]\n"
"s_add_u32 m0,s67,16384\n"
"v_exp_f32 v3,v3\n"
"v_mfma_f32_16x16x32_bf16 v[36:39],a[124:127],a[12:15],v[96:99]\n"
"v_exp_f32 v4,v4\n"
"v_cvt_pk_bf16_f32 v65,v2,v3\n"
"v_exp_f32 v5,v5\n"
"v_mfma_f32_16x16x32_bf16 v[36:39],a[120:123],a[8:11],v[36:39]\n"
"buffer_load_dwordx4 v112,s[24:27],s64 offen offset:0 sc1 lds\n"
"v_mfma_f32_16x16x32_bf16 v[36:39],a[116:119],a[4:7],v[36:39]\n"
"v_exp_f32 v6,v6\n"
"v_cvt_pk_bf16_f32 v66,v4,v5\n"
"v_exp_f32 v7,v7\n"
"v_mfma_f32_16x16x32_bf16 v[36:39],a[112:115],a[0:3],v[36:39]\n"
"buffer_load_dwordx4 v112,s[24:27],s64 offen offset:1024 sc1 lds\n"
"v_mfma_f32_16x16x32_bf16 v[44:47],a[112:115],a[16:19],v[100:103]\n"
"v_exp_f32 v8,v8\n"
"v_cvt_pk_bf16_f32 v67,v6,v7\n"
"v_exp_f32 v9,v9\n"
"v_mfma_f32_16x16x32_bf16 v[44:47],a[116:119],a[20:23],v[44:47]\n"
"ds_read_b128 a[64:67],v113 offset:49152\n"
"ds_read_b128 a[68:71],v113 offset:50176\n"
"v_exp_f32 v10,v10\n"
"v_mfma_f32_16x16x32_bf16 v[44:47],a[120:123],a[24:27],v[44:47]\n"
"ds_read_b128 a[72:75],v113 offset:51200\n"
"ds_read_b128 a[76:79],v113 offset:52224\n"
"v_exp_f32 v11,v11\n"
"v_mfma_f32_16x16x32_bf16 v[44:47],a[124:127],a[28:31],v[44:47]\n"
"ds_read_b128 a[80:83],v113 offset:53248\n"
"ds_read_b128 a[84:87],v113 offset:54272\n"
"v_exp_f32 v12,v12\n"
"v_mfma_f32_16x16x32_bf16 v[40:43],a[108:111],a[28:31],v[100:103]\n"
"ds_read_b128 a[88:91],v113 offset:55296\n"
"ds_read_b128 a[92:95],v113 offset:56320\n"
"v_exp_f32 v13,v13\n"
"v_mfma_f32_16x16x32_bf16 v[40:43],a[104:107],a[24:27],v[40:43]\n"
"buffer_load_dwordx4 v112,s[24:27],s64 offen offset:2048 sc1 lds\n"
"v_exp_f32 v14,v14\n"
"v_mfma_f32_16x16x32_bf16 v[40:43],a[100:103],a[20:23],v[40:43]\n"
"buffer_load_dwordx4 v112,s[24:27],s64 offen offset:3072 sc1 lds\n"
"v_mfma_f32_16x16x32_bf16 v[40:43],a[96:99],a[16:19],v[40:43]\n"
"s_add_u32 m0,s67,20480\n"
"v_exp_f32 v15,v15\n"
"v_mfma_f32_16x16x32_bf16 v[48:51],a[96:99],a[32:35],v[104:107]\n"
"buffer_load_dwordx4 v117,s[24:27],s64 offen offset:0 sc1 lds\n"
"buffer_load_dwordx4 v117,s[24:27],s64 offen offset:1024 sc1 lds\n"
"v_exp_f32 v16,v16\n"
"v_exp_f32 v17,v17\n"
"v_mfma_f32_16x16x32_bf16 v[48:51],a[100:103],a[36:39],v[48:51]\n"
"buffer_load_dwordx4 v117,s[24:27],s64 offen offset:2048 sc1 lds\n"
"v_mfma_f32_16x16x32_bf16 v[48:51],a[104:107],a[40:43],v[48:51]\n"
"v_exp_f32 v18,v18\n"
"v_cvt_pk_bf16_f32 v68,v8,v9\n"
"v_cvt_pk_bf16_f32 v69,v10,v11\n"
"v_exp_f32 v19,v19\n"
"v_mfma_f32_16x16x32_bf16 v[48:51],a[108:111],a[44:47],v[48:51]\n"
"buffer_load_dwordx4 v117,s[24:27],s64 offen offset:3072 sc1 lds\n"
"v_mfma_f32_16x16x32_bf16 v[52:55],a[124:127],a[44:47],v[104:107]\n"
"s_add_u32 s64,s64,16384\n"
"v_exp_f32 v20,v20\n"
"v_cvt_pk_bf16_f32 v70,v12,v13\n"
"v_cvt_pk_bf16_f32 v71,v14,v15\n"
"v_mfma_f32_16x16x32_bf16 v[52:55],a[120:123],a[40:43],v[52:55]\n"
"v_exp_f32 v21,v21\n"
"v_cvt_pk_bf16_f32 v72,v16,v17\n"
"v_cvt_pk_bf16_f32 v73,v18,v19\n"
"v_mfma_f32_16x16x32_bf16 v[52:55],a[116:119],a[36:39],v[52:55]\n"
"v_exp_f32 v22,v22\n"
"v_cvt_pk_bf16_f32 v74,v20,v21\n"
"v_mfma_f32_16x16x32_bf16 v[52:55],a[112:115],a[32:35],v[52:55]\n"
"v_exp_f32 v23,v23\n"
"v_mfma_f32_16x16x32_bf16 v[60:63],a[112:115],a[48:51],v[108:111]\n"
"v_exp_f32 v24,v24\n"
"v_cvt_pk_bf16_f32 v75,v22,v23\n"
"v_mfma_f32_16x16x32_bf16 v[60:63],a[116:119],a[52:55],v[60:63]\n"
"v_exp_f32 v25,v25\n"
"v_mfma_f32_16x16x32_bf16 v[60:63],a[120:123],a[56:59],v[60:63]\n"
"v_exp_f32 v26,v26\n"
"v_exp_f32 v27,v27\n"
"v_mfma_f32_16x16x32_bf16 v[60:63],a[124:127],a[60:63],v[60:63]\n"
"buffer_store_dwordx4 a[228:231],v112,s[24:27],s56 offen offset:1024 sc1\n"
"v_mfma_f32_16x16x32_bf16 v[56:59],a[108:111],a[60:63],v[108:111]\n"
"v_exp_f32 v28,v28\n"
"v_cvt_pk_bf16_f32 v77,v26,v27\n"
"v_mfma_f32_16x16x32_bf16 v[56:59],a[104:107],a[56:59],v[56:59]\n"
"v_exp_f32 v29,v29\n"
"v_mfma_f32_16x16x32_bf16 v[56:59],a[100:103],a[52:55],v[56:59]\n"
"v_exp_f32 v30,v30\n"
"v_cvt_pk_bf16_f32 v78,v28,v29\n"
"v_mfma_f32_16x16x32_bf16 v[56:59],a[96:99],a[48:51],v[56:59]\n"
"s_waitcnt vmcnt(18) lgkmcnt(0)\n"
"v_exp_f32 v31,v31\n"
"v_cvt_pk_bf16_f32 v76,v24,v25\n"
"v_cvt_pk_bf16_f32 v79,v30,v31\n"
"s_barrier\n"
"v_mfma_f32_16x16x32_bf16 v[0:3],a[64:67],a[0:3],v[96:99]\n"
"s_add_u32 m0,s55,8192\n"
"v_exp_f32 v122,v32\n"
"v_mfma_f32_16x16x32_bf16 v[0:3],a[68:71],a[4:7],v[0:3]\n"
"s_add_u32 s54,s54,s22\n"
"v_exp_f32 v123,v33\n"
"v_mfma_f32_16x16x32_bf16 v[0:3],a[72:75],a[8:11],v[0:3]\n"
"ds_read_b128 a[96:99],v114 offset:40960\n"
"ds_read_b128 a[100:103],v114 offset:41984\n"
"v_mfma_f32_16x16x32_bf16 v[0:3],a[76:79],a[12:15],v[0:3]\n"
"v_exp_f32 v119,v34\n"
"v_mfma_f32_16x16x32_bf16 v[4:7],a[92:95],a[12:15],v[96:99]\n"
"ds_read_b128 a[104:107],v114 offset:43008\n"
"v_cvt_pk_bf16_f32 v80,v122,v123\n"
"v_mfma_f32_16x16x32_bf16 v[4:7],a[88:91],a[8:11],v[4:7]\n"
"v_exp_f32 v120,v35\n"
"v_mfma_f32_16x16x32_bf16 v[4:7],a[84:87],a[4:7],v[4:7]\n"
"ds_read_b128 a[108:111],v114 offset:44032\n"
"ds_read_b128 a[112:115],v114 offset:45056\n"
"v_mfma_f32_16x16x32_bf16 v[4:7],a[80:83],a[0:3],v[4:7]\n"
"v_exp_f32 v122,v36\n"
"v_mfma_f32_16x16x32_bf16 v[12:15],a[80:83],a[16:19],v[100:103]\n"
"ds_read_b128 a[116:119],v114 offset:46080\n"
"v_cvt_pk_bf16_f32 v81,v119,v120\n"
"v_mfma_f32_16x16x32_bf16 v[12:15],a[84:87],a[20:23],v[12:15]\n"
"v_exp_f32 v123,v37\n"
"v_mfma_f32_16x16x32_bf16 v[12:15],a[88:91],a[24:27],v[12:15]\n"
"ds_read_b128 a[120:123],v114 offset:47104\n"
"ds_read_b128 a[124:127],v114 offset:48128\n"
"v_mfma_f32_16x16x32_bf16 v[12:15],a[92:95],a[28:31],v[12:15]\n"
"v_exp_f32 v119,v38\n"
"v_cvt_pk_bf16_f32 v82,v122,v123\n"
"v_mfma_f32_16x16x32_bf16 v[8:11],a[76:79],a[28:31],v[100:103]\n"
"buffer_store_dwordx4 a[168:171],v112,s[24:27],s3 offen offset:2048 sc1\n"
"v_mfma_f32_16x16x32_bf16 v[8:11],a[72:75],a[24:27],v[8:11]\n"
"v_exp_f32 v120,v39\n"
"v_mfma_f32_16x16x32_bf16 v[8:11],a[68:71],a[20:23],v[8:11]\n"
"buffer_load_dwordx4 v115,s[32:35],s54 offen sc1 lds\n"
"v_mfma_f32_16x16x32_bf16 v[8:11],a[64:67],a[16:19],v[8:11]\n"
"s_add_u32 m0,s67,49152\n"
"v_exp_f32 v122,v40\n"
"v_mfma_f32_16x16x32_bf16 v[16:19],a[64:67],a[32:35],v[104:107]\n"
"v_cvt_pk_bf16_f32 v83,v119,v120\n"
"v_or3_b32 v118,v80,v81,v82\n"
"v_mfma_f32_16x16x32_bf16 v[16:19],a[68:71],a[36:39],v[16:19]\n"
"v_exp_f32 v123,v41\n"
"v_mfma_f32_16x16x32_bf16 v[16:19],a[72:75],a[40:43],v[16:19]\n"
"buffer_load_dwordx4 v112,s[24:27],s64 offen offset:0 sc1 lds\n"
"v_mfma_f32_16x16x32_bf16 v[16:19],a[76:79],a[44:47],v[16:19]\n"
"v_exp_f32 v119,v42\n"
"v_mfma_f32_16x16x32_bf16 v[20:23],a[92:95],a[44:47],v[104:107]\n"
"v_cvt_pk_bf16_f32 v84,v122,v123\n"
"v_or3_b32 v118,v118,v83,v84\n"
"v_mfma_f32_16x16x32_bf16 v[20:23],a[88:91],a[40:43],v[20:23]\n"
"v_exp_f32 v120,v43\n"
"v_mfma_f32_16x16x32_bf16 v[20:23],a[84:87],a[36:39],v[20:23]\n"
"buffer_load_dwordx4 v112,s[24:27],s64 offen offset:1024 sc1 lds\n"
"v_mfma_f32_16x16x32_bf16 v[20:23],a[80:83],a[32:35],v[20:23]\n"
"v_exp_f32 v122,v44\n"
"v_mfma_f32_16x16x32_bf16 v[28:31],a[80:83],a[48:51],v[108:111]\n"
"v_cvt_pk_bf16_f32 v85,v119,v120\n"
"v_mfma_f32_16x16x32_bf16 v[28:31],a[84:87],a[52:55],v[28:31]\n"
"v_exp_f32 v123,v45\n"
"v_mfma_f32_16x16x32_bf16 v[28:31],a[88:91],a[56:59],v[28:31]\n"
"buffer_load_dwordx4 v112,s[24:27],s64 offen offset:2048 sc1 lds\n"
"v_mfma_f32_16x16x32_bf16 v[28:31],a[92:95],a[60:63],v[28:31]\n"
"v_exp_f32 v119,v46\n"
"v_cvt_pk_bf16_f32 v86,v122,v123\n"
"v_mfma_f32_16x16x32_bf16 v[24:27],a[76:79],a[60:63],v[108:111]\n"
"buffer_store_dwordx4 a[172:175],v112,s[24:27],s3 offen offset:3072 sc1\n"
"v_mfma_f32_16x16x32_bf16 v[24:27],a[72:75],a[56:59],v[24:27]\n"
"v_exp_f32 v120,v47\n"
"v_mfma_f32_16x16x32_bf16 v[24:27],a[68:71],a[52:55],v[24:27]\n"
"buffer_load_dwordx4 v112,s[24:27],s64 offen offset:3072 sc1 lds\n"
"v_mfma_f32_16x16x32_bf16 v[24:27],a[64:67],a[48:51],v[24:27]\n"
"s_waitcnt lgkmcnt(0)\n"
"v_exp_f32 v122,v48\n"
"v_mfma_f32_16x16x32_bf16 v[128:131],a[96:99],v[64:67],0\n"
"v_exp_f32 v123,v49\n"
"v_mfma_f32_16x16x32_bf16 v[160:163],a[96:99],v[68:71],0\n"
"ds_read_b128 a[64:67],v113 offset:32768\n"
"v_cvt_pk_bf16_f32 v87,v119,v120\n"
"v_mfma_f32_16x16x32_bf16 v[192:195],a[96:99],v[72:75],0\n"
"s_add_u32 m0,s67,53248\n"
"v_exp_f32 v119,v50\n"
"v_mfma_f32_16x16x32_bf16 v[224:227],a[96:99],v[76:79],0\n"
"ds_read_b128 a[68:71],v113 offset:33792\n"
"v_cvt_pk_bf16_f32 v88,v122,v123\n"
"v_mfma_f32_16x16x32_bf16 a[140:143],a[144:147],v[76:79],0\n"
"v_exp_f32 v120,v51\n"
"v_mfma_f32_16x16x32_bf16 v[228:231],a[100:103],v[76:79],0\n"
"ds_read_b128 a[72:75],v113 offset:34816\n"
"ds_read_b128 a[76:79],v113 offset:35840\n"
"v_mfma_f32_16x16x32_bf16 v[132:135],a[100:103],v[64:67],0\n"
"v_exp_f32 v122,v52\n"
"v_mfma_f32_16x16x32_bf16 v[164:167],a[100:103],v[68:71],0\n"
"ds_read_b128 a[80:83],v113 offset:36864\n"
"v_cvt_pk_bf16_f32 v89,v119,v120\n"
"v_mfma_f32_16x16x32_bf16 v[196:199],a[100:103],v[72:75],0\n"
"v_exp_f32 v123,v53\n"
"v_mfma_f32_16x16x32_bf16 a[136:139],a[144:147],v[72:75],0\n"
"ds_read_b128 a[84:87],v113 offset:37888\n"
"ds_read_b128 a[88:91],v113 offset:38912\n"
"v_mfma_f32_16x16x32_bf16 v[200:203],a[104:107],v[72:75],0\n"
"v_exp_f32 v119,v54\n"
"v_mfma_f32_16x16x32_bf16 v[232:235],a[104:107],v[76:79],0\n"
"ds_read_b128 a[92:95],v113 offset:39936\n"
"v_cvt_pk_bf16_f32 v90,v122,v123\n"
"v_mfma_f32_16x16x32_bf16 v[136:139],a[104:107],v[64:67],0\n"
"v_exp_f32 v120,v55\n"
"v_mfma_f32_16x16x32_bf16 v[168:171],a[104:107],v[68:71],0\n"
"buffer_load_dwordx4 v117,s[24:27],s64 offen offset:0 sc1 lds\n"
"v_mfma_f32_16x16x32_bf16 a[132:135],a[144:147],v[68:71],0\n"
"v_exp_f32 v122,v56\n"
"v_mfma_f32_16x16x32_bf16 v[172:175],a[108:111],v[68:71],0\n"
"v_cvt_pk_bf16_f32 v91,v119,v120\n"
"s_sub_u32 s3,s3,16384\n"
"v_mfma_f32_16x16x32_bf16 v[204:207],a[108:111],v[72:75],0\n"
"v_exp_f32 v123,v57\n"
"v_mfma_f32_16x16x32_bf16 v[236:239],a[108:111],v[76:79],0\n"
"buffer_load_dwordx4 v117,s[24:27],s64 offen offset:1024 sc1 lds\n"
"v_mfma_f32_16x16x32_bf16 v[140:143],a[108:111],v[64:67],0\n"
"v_exp_f32 v119,v58\n"
"v_cvt_pk_bf16_f32 v92,v122,v123\n"
"v_mfma_f32_16x16x32_bf16 a[128:131],a[144:147],v[64:67],0\n"
"buffer_store_dwordx4 a[176:179],v112,s[24:27],s3 offen offset:0 sc1\n"
"v_mfma_f32_16x16x32_bf16 v[144:147],a[112:115],v[64:67],0\n"
"v_exp_f32 v120,v59\n"
"v_mfma_f32_16x16x32_bf16 v[176:179],a[112:115],v[68:71],0\n"
"buffer_load_dwordx4 v117,s[24:27],s64 offen offset:2048 sc1 lds\n"
"v_mfma_f32_16x16x32_bf16 v[208:211],a[112:115],v[72:75],0\n"
"v_exp_f32 v122,v60\n"
"v_mfma_f32_16x16x32_bf16 v[240:243],a[112:115],v[76:79],0\n"
"v_cvt_pk_bf16_f32 v93,v119,v120\n"
"v_or3_b32 v118,v118,v85,v86\n"
"v_mfma_f32_16x16x32_bf16 v[244:247],a[116:119],v[76:79],0\n"
"v_exp_f32 v123,v61\n"
"v_mfma_f32_16x16x32_bf16 v[148:151],a[116:119],v[64:67],0\n"
"buffer_load_dwordx4 v117,s[24:27],s64 offen offset:3072 sc1 lds\n"
"v_mfma_f32_16x16x32_bf16 v[180:183],a[116:119],v[68:71],0\n"
"s_add_u32 s64,s64,16384\n"
"v_exp_f32 v119,v62\n"
"v_cvt_pk_bf16_f32 v94,v122,v123\n"
"v_mfma_f32_16x16x32_bf16 v[212:215],a[116:119],v[72:75],0\n"
"buffer_store_dwordx4 a[232:235],v112,s[24:27],s56 offen offset:2048 sc1\n"
"v_mfma_f32_16x16x32_bf16 v[216:219],a[120:123],v[72:75],0\n"
"v_exp_f32 v120,v63\n"
"v_mfma_f32_16x16x32_bf16 v[248:251],a[120:123],v[76:79],0\n"
"v_cvt_pk_bf16_f32 v95,v119,v120\n"
"v_or3_b32 v118,v118,v87,v88\n"
"v_mfma_f32_16x16x32_bf16 v[152:155],a[120:123],v[64:67],0\n"
"v_or3_b32 v118,v118,v89,v90\n"
"v_or3_b32 v118,v118,v91,v92\n"
"v_mfma_f32_16x16x32_bf16 v[184:187],a[120:123],v[68:71],0\n"
"v_or3_b32 v118,v118,v93,v94\n"
"v_or_b32 v118,v118,v95\n"
"v_mfma_f32_16x16x32_bf16 v[188:191],a[124:127],v[68:71],0\n"
"v_and_b32 v118,0x40004000,v118\n"
"v_cmp_ne_u32 vcc,0,v118\n"
"v_mfma_f32_16x16x32_bf16 v[220:223],a[124:127],v[72:75],0\n"
"v_mfma_f32_16x16x32_bf16 v[252:255],a[124:127],v[76:79],0\n"
"v_mfma_f32_16x16x32_bf16 v[156:159],a[124:127],v[64:67],0\n"
"s_cbranch_vccnz .Lfk_cs_h1\n"
".Lfk_cr_h1:\n"
"s_waitcnt lgkmcnt(0)\n"
"s_barrier\n"
"v_mfma_f32_16x16x32_bf16 v[32:35],a[64:67],a[0:3],v[96:99]\n"
"s_add_u32 m0,s55,12288\n"
"v_exp_f32 v122,v0\n"
"v_mfma_f32_16x16x32_bf16 v[32:35],a[68:71],a[4:7],v[32:35]\n"
"s_add_u32 s54,s54,s22\n"
"v_exp_f32 v123,v1\n"
"v_mfma_f32_16x16x32_bf16 v[32:35],a[72:75],a[8:11],v[32:35]\n"
"ds_read_b128 a[96:99],v114 offset:8192\n"
"ds_read_b128 a[100:103],v114 offset:9216\n"
"v_mfma_f32_16x16x32_bf16 v[32:35],a[76:79],a[12:15],v[32:35]\n"
"v_exp_f32 v119,v2\n"
"v_mfma_f32_16x16x32_bf16 v[36:39],a[92:95],a[12:15],v[96:99]\n"
"ds_read_b128 a[104:107],v114 offset:10240\n"
"v_cvt_pk_bf16_f32 v64,v122,v123\n"
"v_mfma_f32_16x16x32_bf16 v[36:39],a[88:91],a[8:11],v[36:39]\n"
"v_exp_f32 v120,v3\n"
"v_mfma_f32_16x16x32_bf16 v[36:39],a[84:87],a[4:7],v[36:39]\n"
"ds_read_b128 a[108:111],v114 offset:11264\n"
"ds_read_b128 a[112:115],v114 offset:12288\n"
"v_mfma_f32_16x16x32_bf16 v[36:39],a[80:83],a[0:3],v[36:39]\n"
"v_exp_f32 v122,v4\n"
"v_mfma_f32_16x16x32_bf16 v[44:47],a[80:83],a[16:19],v[100:103]\n"
"ds_read_b128 a[116:119],v114 offset:13312\n"
"v_cvt_pk_bf16_f32 v65,v119,v120\n"
"v_mfma_f32_16x16x32_bf16 v[44:47],a[84:87],a[20:23],v[44:47]\n"
"v_exp_f32 v123,v5\n"
"v_mfma_f32_16x16x32_bf16 v[44:47],a[88:91],a[24:27],v[44:47]\n"
"ds_read_b128 a[120:123],v114 offset:14336\n"
"ds_read_b128 a[124:127],v114 offset:15360\n"
"v_mfma_f32_16x16x32_bf16 v[44:47],a[92:95],a[28:31],v[44:47]\n"
"v_exp_f32 v119,v6\n"
"v_cvt_pk_bf16_f32 v66,v122,v123\n"
"v_mfma_f32_16x16x32_bf16 v[40:43],a[76:79],a[28:31],v[100:103]\n"
"buffer_store_dwordx4 a[180:183],v112,s[24:27],s3 offen offset:1024 sc1\n"
"v_mfma_f32_16x16x32_bf16 v[40:43],a[72:75],a[24:27],v[40:43]\n"
"v_exp_f32 v120,v7\n"
"v_mfma_f32_16x16x32_bf16 v[40:43],a[68:71],a[20:23],v[40:43]\n"
"buffer_load_dwordx4 v115,s[32:35],s54 offen sc1 lds\n"
"v_mfma_f32_16x16x32_bf16 v[40:43],a[64:67],a[16:19],v[40:43]\n"
"s_add_u32 m0,s67,32768\n"
"v_exp_f32 v122,v8\n"
"v_mfma_f32_16x16x32_bf16 v[48:51],a[64:67],a[32:35],v[104:107]\n"
"v_cvt_pk_bf16_f32 v67,v119,v120\n"
"v_or3_b32 v118,v64,v65,v66\n"
"v_mfma_f32_16x16x32_bf16 v[48:51],a[68:71],a[36:39],v[48:51]\n"
"v_exp_f32 v123,v9\n"
"v_mfma_f32_16x16x32_bf16 v[48:51],a[72:75],a[40:43],v[48:51]\n"
"buffer_load_dwordx4 v112,s[24:27],s64 offen offset:0 sc1 lds\n"
"v_mfma_f32_16x16x32_bf16 v[48:51],a[76:79],a[44:47],v[48:51]\n"
"v_exp_f32 v119,v10\n"
"v_mfma_f32_16x16x32_bf16 v[52:55],a[92:95],a[44:47],v[104:107]\n"
"v_cvt_pk_bf16_f32 v68,v122,v123\n"
"v_or3_b32 v118,v118,v67,v68\n"
"v_mfma_f32_16x16x32_bf16 v[52:55],a[88:91],a[40:43],v[52:55]\n"
"v_exp_f32 v120,v11\n"
"v_mfma_f32_16x16x32_bf16 v[52:55],a[84:87],a[36:39],v[52:55]\n"
"buffer_load_dwordx4 v112,s[24:27],s64 offen offset:1024 sc1 lds\n"
"v_mfma_f32_16x16x32_bf16 v[52:55],a[80:83],a[32:35],v[52:55]\n"
"v_exp_f32 v122,v12\n"
"v_mfma_f32_16x16x32_bf16 v[60:63],a[80:83],a[48:51],v[108:111]\n"
"v_cvt_pk_bf16_f32 v69,v119,v120\n"
"v_mfma_f32_16x16x32_bf16 v[60:63],a[84:87],a[52:55],v[60:63]\n"
"v_exp_f32 v123,v13\n"
"v_mfma_f32_16x16x32_bf16 v[60:63],a[88:91],a[56:59],v[60:63]\n"
"buffer_load_dwordx4 v112,s[24:27],s64 offen offset:2048 sc1 lds\n"
"v_mfma_f32_16x16x32_bf16 v[60:63],a[92:95],a[60:63],v[60:63]\n"
"v_exp_f32 v119,v14\n"
"v_cvt_pk_bf16_f32 v70,v122,v123\n"
"v_mfma_f32_16x16x32_bf16 v[56:59],a[76:79],a[60:63],v[108:111]\n"
"buffer_store_dwordx4 a[184:187],v112,s[24:27],s3 offen offset:2048 sc1\n"
"v_mfma_f32_16x16x32_bf16 v[56:59],a[72:75],a[56:59],v[56:59]\n"
"v_exp_f32 v120,v15\n"
"v_mfma_f32_16x16x32_bf16 v[56:59],a[68:71],a[52:55],v[56:59]\n"
"buffer_load_dwordx4 v112,s[24:27],s64 offen offset:3072 sc1 lds\n"
"v_mfma_f32_16x16x32_bf16 v[56:59],a[64:67],a[48:51],v[56:59]\n"
"s_waitcnt lgkmcnt(0)\n"
"v_exp_f32 v122,v16\n"
"v_mfma_f32_16x16x32_bf16 v[128:131],a[96:99],v[80:83],v[128:131]\n"
"v_exp_f32 v123,v17\n"
"v_mfma_f32_16x16x32_bf16 v[160:163],a[96:99],v[84:87],v[160:163]\n"
"ds_read_b128 a[64:67],v113 offset:0\n"
"v_cvt_pk_bf16_f32 v71,v119,v120\n"
"v_mfma_f32_16x16x32_bf16 v[192:195],a[96:99],v[88:91],v[192:195]\n"
"s_add_u32 m0,s67,36864\n"
"v_exp_f32 v119,v18\n"
"v_mfma_f32_16x16x32_bf16 v[224:227],a[96:99],v[92:95],v[224:227]\n"
"ds_read_b128 a[68:71],v113 offset:1024\n"
"v_cvt_pk_bf16_f32 v72,v122,v123\n"
"v_mfma_f32_16x16x32_bf16 a[140:143],a[144:147],v[92:95],a[140:143]\n"
"v_exp_f32 v120,v19\n"
"v_mfma_f32_16x16x32_bf16 v[228:231],a[100:103],v[92:95],v[228:231]\n"
"ds_read_b128 a[72:75],v113 offset:2048\n"
"ds_read_b128 a[76:79],v113 offset:3072\n"
"v_mfma_f32_16x16x32_bf16 v[132:135],a[100:103],v[80:83],v[132:135]\n"
"v_exp_f32 v122,v20\n"
"v_mfma_f32_16x16x32_bf16 v[164:167],a[100:103],v[84:87],v[164:167]\n"
"ds_read_b128 a[80:83],v113 offset:4096\n"
"v_cvt_pk_bf16_f32 v73,v119,v120\n"
"v_mfma_f32_16x16x32_bf16 v[196:199],a[100:103],v[88:91],v[196:199]\n"
"v_exp_f32 v123,v21\n"
"v_mfma_f32_16x16x32_bf16 a[136:139],a[144:147],v[88:91],a[136:139]\n"
"ds_read_b128 a[84:87],v113 offset:5120\n"
"ds_read_b128 a[88:91],v113 offset:6144\n"
"v_mfma_f32_16x16x32_bf16 v[200:203],a[104:107],v[88:91],v[200:203]\n"
"v_exp_f32 v119,v22\n"
"v_mfma_f32_16x16x32_bf16 v[232:235],a[104:107],v[92:95],v[232:235]\n"
"ds_read_b128 a[92:95],v113 offset:7168\n"
"v_cvt_pk_bf16_f32 v74,v122,v123\n"
"v_mfma_f32_16x16x32_bf16 v[136:139],a[104:107],v[80:83],v[136:139]\n"
"v_exp_f32 v120,v23\n"
"v_mfma_f32_16x16x32_bf16 v[168:171],a[104:107],v[84:87],v[168:171]\n"
"buffer_load_dwordx4 v117,s[24:27],s64 offen offset:0 sc1 lds\n"
"v_mfma_f32_16x16x32_bf16 a[132:135],a[144:147],v[84:87],a[132:135]\n"
"v_exp_f32 v122,v24\n"
"v_cvt_pk_bf16_f32 v75,v119,v120\n"
"v_mfma_f32_16x16x32_bf16 v[172:175],a[108:111],v[84:87],v[172:175]\n"
"buffer_store_dwordx4 a[188:191],v112,s[24:27],s3 offen offset:3072 sc1\n"
"v_mfma_f32_16x16x32_bf16 v[204:207],a[108:111],v[88:91],v[204:207]\n"
"v_exp_f32 v123,v25\n"
"v_mfma_f32_16x16x32_bf16 v[236:239],a[108:111],v[92:95],v[236:239]\n"
"buffer_load_dwordx4 v117,s[24:27],s64 offen offset:1024 sc1 lds\n"
"v_mfma_f32_16x16x32_bf16 v[140:143],a[108:111],v[80:83],v[140:143]\n"
"v_exp_f32 v119,v26\n"
"v_mfma_f32_16x16x32_bf16 a[128:131],a[144:147],v[80:83],a[128:131]\n"
"v_cvt_pk_bf16_f32 v76,v122,v123\n"
"s_or_b32 s3,s3,0x3c000\n"
"v_mfma_f32_16x16x32_bf16 v[144:147],a[112:115],v[80:83],v[144:147]\n"
"v_exp_f32 v120,v27\n"
"v_mfma_f32_16x16x32_bf16 v[176:179],a[112:115],v[84:87],v[176:179]\n"
"buffer_load_dwordx4 v117,s[24:27],s64 offen offset:2048 sc1 lds\n"
"v_mfma_f32_16x16x32_bf16 v[208:211],a[112:115],v[88:91],v[208:211]\n"
"v_exp_f32 v122,v28\n"
"v_mfma_f32_16x16x32_bf16 v[240:243],a[112:115],v[92:95],v[240:243]\n"
"v_cvt_pk_bf16_f32 v77,v119,v120\n"
"v_or3_b32 v118,v118,v69,v70\n"
"v_mfma_f32_16x16x32_bf16 v[244:247],a[116:119],v[92:95],v[244:247]\n"
"v_exp_f32 v123,v29\n"
"v_mfma_f32_16x16x32_bf16 v[148:151],a[116:119],v[80:83],v[148:151]\n"
"buffer_load_dwordx4 v117,s[24:27],s64 offen offset:3072 sc1 lds\n"
"v_mfma_f32_16x16x32_bf16 v[180:183],a[116:119],v[84:87],v[180:183]\n"
"s_add_u32 s64,s64,16384\n"
"v_exp_f32 v119,v30\n"
"v_cvt_pk_bf16_f32 v78,v122,v123\n"
"v_mfma_f32_16x16x32_bf16 v[212:215],a[116:119],v[88:91],v[212:215]\n"
"buffer_store_dwordx4 a[236:239],v112,s[24:27],s56 offen offset:3072 sc1\n"
"v_mfma_f32_16x16x32_bf16 v[216:219],a[120:123],v[88:91],v[216:219]\n"
"v_exp_f32 v120,v31\n"
"v_mfma_f32_16x16x32_bf16 v[248:251],a[120:123],v[92:95],v[248:251]\n"
"v_cvt_pk_bf16_f32 v79,v119,v120\n"
"v_or3_b32 v118,v118,v71,v72\n"
"v_mfma_f32_16x16x32_bf16 v[152:155],a[120:123],v[80:83],v[152:155]\n"
"v_or3_b32 v118,v118,v73,v74\n"
"v_or3_b32 v118,v118,v75,v76\n"
"v_mfma_f32_16x16x32_bf16 v[184:187],a[120:123],v[84:87],v[184:187]\n"
"v_or3_b32 v118,v118,v77,v78\n"
"v_or_b32 v118,v118,v79\n"
"v_mfma_f32_16x16x32_bf16 v[188:191],a[124:127],v[84:87],v[188:191]\n"
"v_and_b32 v118,0x40004000,v118\n"
"v_cmp_ne_u32 vcc,0,v118\n"
"v_mfma_f32_16x16x32_bf16 v[220:223],a[124:127],v[88:91],v[220:223]\n"
"v_mfma_f32_16x16x32_bf16 v[252:255],a[124:127],v[92:95],v[252:255]\n"
"v_mfma_f32_16x16x32_bf16 v[156:159],a[124:127],v[80:83],v[156:159]\n"
"s_cbranch_vccnz .Lfk_cs_h2\n"
".Lfk_cr_h2:\n"
"s_waitcnt vmcnt(27) lgkmcnt(0)\n"
"s_barrier\n"
".Lfk_g1:\n"
"s_and_b32 s82,s78,4096\n"
"s_lshl1_add_u32 s82,s82,69632\n"
"s_cmp_lt_u32 s52,2\n"
"s_cselect_b32 s64,s64,s62\n"
"s_cselect_b32 s67,s67,s82\n"
"s_add_u32 s89,s89,1\n"
"s_cmp_ge_u32 s89,s60\n"
"s_cbranch_scc1 .Lfk_Fx\n"
".Lfk_F:\n"
"v_mfma_f32_16x16x32_bf16 v[0:3],a[64:67],a[0:3],v[96:99]\n"
"s_swappc_b64 s[94:95],s[20:21]\n"
"v_exp_f32 v122,v32\n"
"v_mfma_f32_16x16x32_bf16 v[0:3],a[68:71],a[4:7],v[0:3]\n"
"v_exp_f32 v123,v33\n"
"v_mfma_f32_16x16x32_bf16 v[0:3],a[72:75],a[8:11],v[0:3]\n"
"ds_read_b128 a[96:99],v114 offset:24576\n"
"ds_read_b128 a[100:103],v114 offset:25600\n"
"v_mfma_f32_16x16x32_bf16 v[0:3],a[76:79],a[12:15],v[0:3]\n"
"v_exp_f32 v119,v34\n"
"v_mfma_f32_16x16x32_bf16 v[4:7],a[92:95],a[12:15],v[96:99]\n"
"s_add_u32 s101,s101,4\n"
"ds_read_b128 a[104:107],v114 offset:26624\n"
"v_cvt_pk_bf16_f32 v80,v122,v123\n"
"v_mfma_f32_16x16x32_bf16 v[4:7],a[88:91],a[8:11],v[4:7]\n"
"v_exp_f32 v120,v35\n"
"v_mfma_f32_16x16x32_bf16 v[4:7],a[84:87],a[4:7],v[4:7]\n"
"ds_read_b128 a[108:111],v114 offset:27648\n"
"ds_read_b128 a[112:115],v114 offset:28672\n"
"v_mfma_f32_16x16x32_bf16 v[4:7],a[80:83],a[0:3],v[4:7]\n"
"v_exp_f32 v122,v36\n"
"v_mfma_f32_16x16x32_bf16 v[12:15],a[80:83],a[16:19],v[100:103]\n"
"ds_read_b128 a[116:119],v114 offset:29696\n"
"v_cvt_pk_bf16_f32 v81,v119,v120\n"
"v_mfma_f32_16x16x32_bf16 v[12:15],a[84:87],a[20:23],v[12:15]\n"
"v_exp_f32 v123,v37\n"
"v_mfma_f32_16x16x32_bf16 v[12:15],a[88:91],a[24:27],v[12:15]\n"
"ds_read_b128 a[120:123],v114 offset:30720\n"
"ds_read_b128 a[124:127],v114 offset:31744\n"
"v_mfma_f32_16x16x32_bf16 v[12:15],a[92:95],a[28:31],v[12:15]\n"
"s_add_u32 m0,s67,0\n"
"v_exp_f32 v119,v38\n"
"v_mfma_f32_16x16x32_bf16 v[8:11],a[76:79],a[28:31],v[100:103]\n"
"v_cvt_pk_bf16_f32 v82,v122,v123\n"
"v_or3_b32 v118,v80,v81,v82\n"
"v_mfma_f32_16x16x32_bf16 v[8:11],a[72:75],a[24:27],v[8:11]\n"
"v_exp_f32 v120,v39\n"
"v_mfma_f32_16x16x32_bf16 v[8:11],a[68:71],a[20:23],v[8:11]\n"
"buffer_load_dwordx4 v112,s[24:27],s64 offen offset:0 sc1 lds\n"
"v_mfma_f32_16x16x32_bf16 v[8:11],a[64:67],a[16:19],v[8:11]\n"
"v_exp_f32 v122,v40\n"
"v_mfma_f32_16x16x32_bf16 v[16:19],a[64:67],a[32:35],v[104:107]\n"
"v_cvt_pk_bf16_f32 v83,v119,v120\n"
"v_mfma_f32_16x16x32_bf16 v[16:19],a[68:71],a[36:39],v[16:19]\n"
"v_exp_f32 v123,v41\n"
"v_mfma_f32_16x16x32_bf16 v[16:19],a[72:75],a[40:43],v[16:19]\n"
"buffer_load_dwordx4 v112,s[24:27],s64 offen offset:1024 sc1 lds\n"
"v_mfma_f32_16x16x32_bf16 v[16:19],a[76:79],a[44:47],v[16:19]\n"
"v_exp_f32 v119,v42\n"
"v_mfma_f32_16x16x32_bf16 v[20:23],a[92:95],a[44:47],v[104:107]\n"
"v_cvt_pk_bf16_f32 v84,v122,v123\n"
"v_or3_b32 v118,v118,v83,v84\n"
"v_mfma_f32_16x16x32_bf16 v[20:23],a[88:91],a[40:43],v[20:23]\n"
"v_exp_f32 v120,v43\n"
"v_mfma_f32_16x16x32_bf16 v[20:23],a[84:87],a[36:39],v[20:23]\n"
"v_cvt_pk_bf16_f32 v85,v119,v120\n"
"v_mfma_f32_16x16x32_bf16 v[20:23],a[80:83],a[32:35],v[20:23]\n"
"v_exp_f32 v122,v44\n"
"v_mfma_f32_16x16x32_bf16 v[28:31],a[80:83],a[48:51],v[108:111]\n"
"buffer_load_dwordx4 v112,s[24:27],s64 offen offset:2048 sc1 lds\n"
"v_mfma_f32_16x16x32_bf16 v[28:31],a[84:87],a[52:55],v[28:31]\n"
"v_exp_f32 v123,v45\n"
"v_mfma_f32_16x16x32_bf16 v[28:31],a[88:91],a[56:59],v[28:31]\n"
"v_cvt_pk_bf16_f32 v86,v122,v123\n"
"v_or3_b32 v118,v118,v85,v86\n"
"v_mfma_f32_16x16x32_bf16 v[28:31],a[92:95],a[60:63],v[28:31]\n"
"v_exp_f32 v119,v46\n"
"v_mfma_f32_16x16x32_bf16 v[24:27],a[76:79],a[60:63],v[108:111]\n"
"v_exp_f32 v122,v48\n"
"v_mfma_f32_16x16x32_bf16 v[24:27],a[72:75],a[56:59],v[24:27]\n"
"v_exp_f32 v120,v47\n"
"v_mfma_f32_16x16x32_bf16 v[24:27],a[68:71],a[52:55],v[24:27]\n"
"buffer_load_dwordx4 v112,s[24:27],s64 offen offset:3072 sc1 lds\n"
"v_mfma_f32_16x16x32_bf16 v[24:27],a[64:67],a[48:51],v[24:27]\n"
"s_add_u32 s64,s64,16384\n"
"s_waitcnt lgkmcnt(0)\n"
"v_mfma_f32_16x16x32_bf16 v[128:131],a[96:99],v[64:67],v[128:131]\n"
"s_andn2_b32 m0,s55,16384\n"
"v_exp_f32 v123,v49\n"
"v_mfma_f32_16x16x32_bf16 v[160:163],a[96:99],v[68:71],v[160:163]\n"
"ds_read_b128 a[64:67],v113 offset:16384\n"
"v_cvt_pk_bf16_f32 v87,v119,v120\n"
"v_mfma_f32_16x16x32_bf16 v[192:195],a[96:99],v[72:75],v[192:195]\n"
"s_add_u32 s54,s54,s22\n"
"v_exp_f32 v119,v50\n"
"v_mfma_f32_16x16x32_bf16 v[224:227],a[96:99],v[76:79],v[224:227]\n"
"ds_read_b128 a[68:71],v113 offset:17408\n"
"v_cvt_pk_bf16_f32 v88,v122,v123\n"
"v_mfma_f32_16x16x32_bf16 a[140:143],a[144:147],v[76:79],a[140:143]\n"
"v_exp_f32 v120,v51\n"
"v_mfma_f32_16x16x32_bf16 v[228:231],a[100:103],v[76:79],v[228:231]\n"
"ds_read_b128 a[72:75],v113 offset:18432\n"
"ds_read_b128 a[76:79],v113 offset:19456\n"
"v_mfma_f32_16x16x32_bf16 v[132:135],a[100:103],v[64:67],v[132:135]\n"
"v_exp_f32 v122,v52\n"
"v_mfma_f32_16x16x32_bf16 v[164:167],a[100:103],v[68:71],v[164:167]\n"
"ds_read_b128 a[80:83],v113 offset:20480\n"
"v_cvt_pk_bf16_f32 v89,v119,v120\n"
"v_mfma_f32_16x16x32_bf16 v[196:199],a[100:103],v[72:75],v[196:199]\n"
"v_exp_f32 v123,v53\n"
"v_mfma_f32_16x16x32_bf16 a[136:139],a[144:147],v[72:75],a[136:139]\n"
"ds_read_b128 a[84:87],v113 offset:21504\n"
"ds_read_b128 a[88:91],v113 offset:22528\n"
"v_mfma_f32_16x16x32_bf16 v[200:203],a[104:107],v[72:75],v[200:203]\n"
"v_exp_f32 v119,v54\n"
"v_mfma_f32_16x16x32_bf16 v[232:235],a[104:107],v[76:79],v[232:235]\n"
"ds_read_b128 a[92:95],v113 offset:23552\n"
"v_cvt_pk_bf16_f32 v90,v122,v123\n"
"v_mfma_f32_16x16x32_bf16 v[136:139],a[104:107],v[64:67],v[136:139]\n"
"v_exp_f32 v120,v55\n"
"v_mfma_f32_16x16x32_bf16 v[168:171],a[104:107],v[68:71],v[168:171]\n"
"s_waitcnt vmcnt(11)\n"
"ds_add_u32 v124,a152\n"
"v_mfma_f32_16x16x32_bf16 a[132:135],a[144:147],v[68:71],a[132:135]\n"
"v_exp_f32 v122,v56\n"
"v_mfma_f32_16x16x32_bf16 v[172:175],a[108:111],v[68:71],v[172:175]\n"
"v_cvt_pk_bf16_f32 v91,v119,v120\n"
"v_or3_b32 v118,v118,v87,v88\n"
"v_mfma_f32_16x16x32_bf16 v[204:207],a[108:111],v[72:75],v[204:207]\n"
"v_exp_f32 v123,v57\n"
"v_mfma_f32_16x16x32_bf16 v[236:239],a[108:111],v[76:79],v[236:239]\n"
"buffer_load_dwordx4 v115,s[32:35],s54 offen sc1 lds\n"
"v_mfma_f32_16x16x32_bf16 v[140:143],a[108:111],v[64:67],v[140:143]\n"
"v_exp_f32 v119,v58\n"
"v_mfma_f32_16x16x32_bf16 a[128:131],a[144:147],v[64:67],a[128:131]\n"
"v_cvt_pk_bf16_f32 v92,v122,v123\n"
"v_or3_b32 v118,v118,v89,v90\n"
"v_mfma_f32_16x16x32_bf16 v[144:147],a[112:115],v[64:67],v[144:147]\n"
"v_exp_f32 v120,v59\n"
"v_mfma_f32_16x16x32_bf16 v[176:179],a[112:115],v[68:71],v[176:179]\n"
"v_cvt_pk_bf16_f32 v93,v119,v120\n"
"v_or3_b32 v118,v118,v91,v92\n"
"v_mfma_f32_16x16x32_bf16 v[208:211],a[112:115],v[72:75],v[208:211]\n"
"v_exp_f32 v122,v60\n"
"v_mfma_f32_16x16x32_bf16 v[240:243],a[112:115],v[76:79],v[240:243]\n"
"v_mfma_f32_16x16x32_bf16 v[244:247],a[116:119],v[76:79],v[244:247]\n"
"v_exp_f32 v123,v61\n"
"v_mfma_f32_16x16x32_bf16 v[148:151],a[116:119],v[64:67],v[148:151]\n"
"v_cvt_pk_bf16_f32 v94,v122,v123\n"
"v_or3_b32 v118,v118,v93,v94\n"
"v_mfma_f32_16x16x32_bf16 v[180:183],a[116:119],v[68:71],v[180:183]\n"
"v_exp_f32 v119,v62\n"
"v_mfma_f32_16x16x32_bf16 v[212:215],a[116:119],v[72:75],v[212:215]\n"
"ds_read_b32 v125,v124\n"
"buffer_store_dwordx4 a[240:243],v112,s[24:27],s56 offen offset:0 sc1\n"
"v_mfma_f32_16x16x32_bf16 v[216:219],a[120:123],v[72:75],v[216:219]\n"
"v_exp_f32 v120,v63\n"
"v_mfma_f32_16x16x32_bf16 v[248:251],a[120:123],v[76:79],v[248:251]\n"
"v_cvt_pk_bf16_f32 v95,v119,v120\n"
"v_mfma_f32_16x16x32_bf16 v[152:155],a[120:123],v[64:67],v[152:155]\n"
"v_mfma_f32_16x16x32_bf16 v[184:187],a[120:123],v[68:71],v[184:187]\n"
"v_mfma_f32_16x16x32_bf16 v[188:191],a[124:127],v[68:71],v[188:191]\n"
"s_waitcnt lgkmcnt(0)\n"
"v_subrev_u32 v125,s101,v125\n"
"v_or3_b32 v125,v118,v95,v125\n"
"v_mfma_f32_16x16x32_bf16 v[220:223],a[124:127],v[72:75],v[220:223]\n"
"v_and_b32 v125,0x40004000,v125\n"
"v_cmp_ne_u32 vcc,0,v125\n"
"v_mfma_f32_16x16x32_bf16 v[252:255],a[124:127],v[76:79],v[252:255]\n"
"v_mfma_f32_16x16x32_bf16 v[156:159],a[124:127],v[64:67],v[156:159]\n"
"s_cbranch_vccnz .Lfk_cs_f3\n"
".Lfk_cr_f3:\n"
"v_mfma_f32_16x16x32_bf16 v[32:35],a[64:67],a[0:3],v[96:99]\n"
"s_add_u32 s101,s101,4\n"
"v_exp_f32 v122,v0\n"
"v_mfma_f32_16x16x32_bf16 v[32:35],a[68:71],a[4:7],v[32:35]\n"
"s_add_u32 m0,s55,4096\n"
"v_exp_f32 v123,v1\n"
"v_mfma_f32_16x16x32_bf16 v[32:35],a[72:75],a[8:11],v[32:35]\n"
"ds_read_b128 a[96:99],v114 offset:57344\n"
"ds_read_b128 a[100:103],v114 offset:58368\n"
"v_mfma_f32_16x16x32_bf16 v[32:35],a[76:79],a[12:15],v[32:35]\n"
"s_add_u32 s54,s54,s22\n"
"v_exp_f32 v119,v2\n"
"v_mfma_f32_16x16x32_bf16 v[36:39],a[92:95],a[12:15],v[96:99]\n"
"ds_read_b128 a[104:107],v114 offset:59392\n"
"v_cvt_pk_bf16_f32 v64,v122,v123\n"
"v_mfma_f32_16x16x32_bf16 v[36:39],a[88:91],a[8:11],v[36:39]\n"
"v_exp_f32 v120,v3\n"
"v_mfma_f32_16x16x32_bf16 v[36:39],a[84:87],a[4:7],v[36:39]\n"
"ds_read_b128 a[108:111],v114 offset:60416\n"
"ds_read_b128 a[112:115],v114 offset:61440\n"
"v_mfma_f32_16x16x32_bf16 v[36:39],a[80:83],a[0:3],v[36:39]\n"
"v_exp_f32 v122,v4\n"
"v_mfma_f32_16x16x32_bf16 v[44:47],a[80:83],a[16:19],v[100:103]\n"
"ds_read_b128 a[116:119],v114 offset:62464\n"
"v_cvt_pk_bf16_f32 v65,v119,v120\n"
"v_mfma_f32_16x16x32_bf16 v[44:47],a[84:87],a[20:23],v[44:47]\n"
"v_exp_f32 v123,v5\n"
"v_mfma_f32_16x16x32_bf16 v[44:47],a[88:91],a[24:27],v[44:47]\n"
"ds_read_b128 a[120:123],v114 offset:63488\n"
"ds_read_b128 a[124:127],v114 offset:64512\n"
"v_mfma_f32_16x16x32_bf16 v[44:47],a[92:95],a[28:31],v[44:47]\n"
"v_exp_f32 v119,v6\n"
"v_mfma_f32_16x16x32_bf16 v[40:43],a[76:79],a[28:31],v[100:103]\n"
"v_cvt_pk_bf16_f32 v66,v122,v123\n"
"v_or3_b32 v118,v64,v65,v66\n"
"v_mfma_f32_16x16x32_bf16 v[40:43],a[72:75],a[24:27],v[40:43]\n"
"v_exp_f32 v120,v7\n"
"v_mfma_f32_16x16x32_bf16 v[40:43],a[68:71],a[20:23],v[40:43]\n"
"buffer_load_dwordx4 v115,s[32:35],s54 offen sc1 lds\n"
"v_mfma_f32_16x16x32_bf16 v[40:43],a[64:67],a[16:19],v[40:43]\n"
"s_add_u32 m0,s67,16384\n"
"v_exp_f32 v122,v8\n"
"v_mfma_f32_16x16x32_bf16 v[48:51],a[64:67],a[32:35],v[104:107]\n"
"v_cvt_pk_bf16_f32 v67,v119,v120\n"
"v_mfma_f32_16x16x32_bf16 v[48:51],a[68:71],a[36:39],v[48:51]\n"
"v_exp_f32 v123,v9\n"
"v_mfma_f32_16x16x32_bf16 v[48:51],a[72:75],a[40:43],v[48:51]\n"
"buffer_load_dwordx4 v112,s[24:27],s64 offen offset:0 sc1 lds\n"
"v_mfma_f32_16x16x32_bf16 v[48:51],a[76:79],a[44:47],v[48:51]\n"
"v_exp_f32 v119,v10\n"
"v_mfma_f32_16x16x32_bf16 v[52:55],a[92:95],a[44:47],v[104:107]\n"
"v_cvt_pk_bf16_f32 v68,v122,v123\n"
"v_or3_b32 v118,v118,v67,v68\n"
"v_mfma_f32_16x16x32_bf16 v[52:55],a[88:91],a[40:43],v[52:55]\n"
"v_exp_f32 v120,v11\n"
"v_mfma_f32_16x16x32_bf16 v[52:55],a[84:87],a[36:39],v[52:55]\n"
"buffer_load_dwordx4 v112,s[24:27],s64 offen offset:1024 sc1 lds\n"
"v_mfma_f32_16x16x32_bf16 v[52:55],a[80:83],a[32:35],v[52:55]\n"
"v_exp_f32 v122,v12\n"
"v_mfma_f32_16x16x32_bf16 v[60:63],a[80:83],a[48:51],v[108:111]\n"
"v_cvt_pk_bf16_f32 v69,v119,v120\n"
"v_mfma_f32_16x16x32_bf16 v[60:63],a[84:87],a[52:55],v[60:63]\n"
"v_exp_f32 v123,v13\n"
"v_mfma_f32_16x16x32_bf16 v[60:63],a[88:91],a[56:59],v[60:63]\n"
"buffer_load_dwordx4 v112,s[24:27],s64 offen offset:2048 sc1 lds\n"
"v_mfma_f32_16x16x32_bf16 v[60:63],a[92:95],a[60:63],v[60:63]\n"
"v_exp_f32 v119,v14\n"
"v_mfma_f32_16x16x32_bf16 v[56:59],a[76:79],a[60:63],v[108:111]\n"
"v_cvt_pk_bf16_f32 v70,v122,v123\n"
"v_or3_b32 v118,v118,v69,v70\n"
"v_mfma_f32_16x16x32_bf16 v[56:59],a[72:75],a[56:59],v[56:59]\n"
"v_exp_f32 v120,v15\n"
"v_mfma_f32_16x16x32_bf16 v[56:59],a[68:71],a[52:55],v[56:59]\n"
"buffer_load_dwordx4 v112,s[24:27],s64 offen offset:3072 sc1 lds\n"
"v_mfma_f32_16x16x32_bf16 v[56:59],a[64:67],a[48:51],v[56:59]\n"
"s_waitcnt lgkmcnt(0)\n"
"v_exp_f32 v122,v16\n"
"v_mfma_f32_16x16x32_bf16 v[128:131],a[96:99],v[80:83],v[128:131]\n"
"v_exp_f32 v123,v17\n"
"v_mfma_f32_16x16x32_bf16 v[160:163],a[96:99],v[84:87],v[160:163]\n"
"s_add_u32 s64,s64,16384\n"
"ds_read_b128 a[64:67],v113 offset:49152\n"
"v_cvt_pk_bf16_f32 v71,v119,v120\n"
"v_mfma_f32_16x16x32_bf16 v[192:195],a[96:99],v[88:91],v[192:195]\n"
"v_exp_f32 v119,v18\n"
"v_mfma_f32_16x16x32_bf16 v[224:227],a[96:99],v[92:95],v[224:227]\n"
"ds_read_b128 a[68:71],v113 offset:50176\n"
"v_cvt_pk_bf16_f32 v72,v122,v123\n"
"v_mfma_f32_16x16x32_bf16 a[140:143],a[144:147],v[92:95],a[140:143]\n"
"v_exp_f32 v120,v19\n"
"v_mfma_f32_16x16x32_bf16 v[228:231],a[100:103],v[92:95],v[228:231]\n"
"ds_read_b128 a[72:75],v113 offset:51200\n"
"ds_read_b128 a[76:79],v113 offset:52224\n"
"v_mfma_f32_16x16x32_bf16 v[132:135],a[100:103],v[80:83],v[132:135]\n"
"v_exp_f32 v122,v20\n"
"v_mfma_f32_16x16x32_bf16 v[164:167],a[100:103],v[84:87],v[164:167]\n"
"ds_read_b128 a[80:83],v113 offset:53248\n"
"v_cvt_pk_bf16_f32 v73,v119,v120\n"
"v_mfma_f32_16x16x32_bf16 v[196:199],a[100:103],v[88:91],v[196:199]\n"
"v_exp_f32 v123,v21\n"
"v_mfma_f32_16x16x32_bf16 a[136:139],a[144:147],v[88:91],a[136:139]\n"
"ds_read_b128 a[84:87],v113 offset:54272\n"
"ds_read_b128 a[88:91],v113 offset:55296\n"
"v_mfma_f32_16x16x32_bf16 v[200:203],a[104:107],v[88:91],v[200:203]\n"
"v_exp_f32 v119,v22\n"
"v_mfma_f32_16x16x32_bf16 v[232:235],a[104:107],v[92:95],v[232:235]\n"
"ds_read_b128 a[92:95],v113 offset:56320\n"
"v_cvt_pk_bf16_f32 v74,v122,v123\n"
"v_mfma_f32_16x16x32_bf16 v[136:139],a[104:107],v[80:83],v[136:139]\n"
"v_exp_f32 v120,v23\n"
"v_mfma_f32_16x16x32_bf16 v[168:171],a[104:107],v[84:87],v[168:171]\n"
"s_waitcnt vmcnt(12)\n"
"ds_add_u32 v124,a152\n"
"v_mfma_f32_16x16x32_bf16 a[132:135],a[144:147],v[84:87],a[132:135]\n"
"v_exp_f32 v122,v24\n"
"v_mfma_f32_16x16x32_bf16 v[172:175],a[108:111],v[84:87],v[172:175]\n"
"v_cvt_pk_bf16_f32 v75,v119,v120\n"
"v_or3_b32 v118,v118,v71,v72\n"
"v_mfma_f32_16x16x32_bf16 v[204:207],a[108:111],v[88:91],v[204:207]\n"
"v_exp_f32 v123,v25\n"
"v_mfma_f32_16x16x32_bf16 v[236:239],a[108:111],v[92:95],v[236:239]\n"
"v_cvt_pk_bf16_f32 v76,v122,v123\n"
"v_or3_b32 v118,v118,v73,v74\n"
"v_mfma_f32_16x16x32_bf16 v[140:143],a[108:111],v[80:83],v[140:143]\n"
"v_exp_f32 v119,v26\n"
"v_mfma_f32_16x16x32_bf16 a[128:131],a[144:147],v[80:83],a[128:131]\n"
"v_or3_b32 v118,v118,v75,v76\n"
"v_mfma_f32_16x16x32_bf16 v[144:147],a[112:115],v[80:83],v[144:147]\n"
"v_exp_f32 v120,v27\n"
"v_mfma_f32_16x16x32_bf16 v[176:179],a[112:115],v[84:87],v[176:179]\n"
"v_cvt_pk_bf16_f32 v77,v119,v120\n"
"v_mfma_f32_16x16x32_bf16 v[208:211],a[112:115],v[88:91],v[208:211]\n"
"v_exp_f32 v122,v28\n"
"v_mfma_f32_16x16x32_bf16 v[240:243],a[112:115],v[92:95],v[240:243]\n"
"v_mfma_f32_16x16x32_bf16 v[244:247],a[116:119],v[92:95],v[244:247]\n"
"v_exp_f32 v123,v29\n"
"v_mfma_f32_16x16x32_bf16 v[148:151],a[116:119],v[80:83],v[148:151]\n"
"v_cvt_pk_bf16_f32 v78,v122,v123\n"
"v_or3_b32 v118,v118,v77,v78\n"
"v_mfma_f32_16x16x32_bf16 v[180:183],a[116:119],v[84:87],v[180:183]\n"
"v_exp_f32 v119,v30\n"
"v_mfma_f32_16x16x32_bf16 v[212:215],a[116:119],v[88:91],v[212:215]\n"
"ds_read_b32 v125,v124\n"
"buffer_store_dwordx4 a[244:247],v112,s[24:27],s56 offen offset:1024 sc1\n"
"v_mfma_f32_16x16x32_bf16 v[216:219],a[120:123],v[88:91],v[216:219]\n"
"v_exp_f32 v120,v31\n"
"v_mfma_f32_16x16x32_bf16 v[248:251],a[120:123],v[92:95],v[248:251]\n"
"v_cvt_pk_bf16_f32 v79,v119,v120\n"
"v_mfma_f32_16x16x32_bf16 v[152:155],a[120:123],v[80:83],v[152:155]\n"
"v_mfma_f32_16x16x32_bf16 v[184:187],a[120:123],v[84:87],v[184:187]\n"
"v_mfma_f32_16x16x32_bf16 v[188:191],a[124:127],v[84:87],v[188:191]\n"
"s_waitcnt lgkmcnt(0)\n"
"v_subrev_u32 v125,s101,v125\n"
"v_or3_b32 v125,v118,v79,v125\n"
"v_mfma_f32_16x16x32_bf16 v[220:223],a[124:127],v[88:91],v[220:223]\n"
"v_and_b32 v125,0x40004000,v125\n"
"v_cmp_ne_u32 vcc,0,v125\n"
"v_mfma_f32_16x16x32_bf16 v[252:255],a[124:127],v[92:95],v[252:255]\n"
"v_mfma_f32_16x16x32_bf16 v[156:159],a[124:127],v[80:83],v[156:159]\n"
"s_cbranch_vccnz .Lfk_cs_f0\n"
".Lfk_cr_f0:\n"
"v_mfma_f32_16x16x32_bf16 v[0:3],a[64:67],a[0:3],v[96:99]\n"
"s_add_u32 s101,s101,4\n"
"v_exp_f32 v122,v32\n"
"v_mfma_f32_16x16x32_bf16 v[0:3],a[68:71],a[4:7],v[0:3]\n"
"s_add_u32 m0,s55,8192\n"
"v_exp_f32 v123,v33\n"
"v_mfma_f32_16x16x32_bf16 v[0:3],a[72:75],a[8:11],v[0:3]\n"
"ds_read_b128 a[96:99],v114 offset:40960\n"
"ds_read_b128 a[100:103],v114 offset:41984\n"
"v_mfma_f32_16x16x32_bf16 v[0:3],a[76:79],a[12:15],v[0:3]\n"
"s_add_u32 s54,s54,s22\n"
"v_exp_f32 v119,v34\n"
"v_mfma_f32_16x16x32_bf16 v[4:7],a[92:95],a[12:15],v[96:99]\n"
"ds_read_b128 a[104:107],v114 offset:43008\n"
"v_cvt_pk_bf16_f32 v80,v122,v123\n"
"v_mfma_f32_16x16x32_bf16 v[4:7],a[88:91],a[8:11],v[4:7]\n"
"v_exp_f32 v120,v35\n"
"v_mfma_f32_16x16x32_bf16 v[4:7],a[84:87],a[4:7],v[4:7]\n"
"ds_read_b128 a[108:111],v114 offset:44032\n"
"ds_read_b128 a[112:115],v114 offset:45056\n"
"v_mfma_f32_16x16x32_bf16 v[4:7],a[80:83],a[0:3],v[4:7]\n"
"v_exp_f32 v122,v36\n"
"v_mfma_f32_16x16x32_bf16 v[12:15],a[80:83],a[16:19],v[100:103]\n"
"ds_read_b128 a[116:119],v114 offset:46080\n"
"v_cvt_pk_bf16_f32 v81,v119,v120\n"
"v_mfma_f32_16x16x32_bf16 v[12:15],a[84:87],a[20:23],v[12:15]\n"
"v_exp_f32 v123,v37\n"
"v_mfma_f32_16x16x32_bf16 v[12:15],a[88:91],a[24:27],v[12:15]\n"
"ds_read_b128 a[120:123],v114 offset:47104\n"
"ds_read_b128 a[124:127],v114 offset:48128\n"
"v_mfma_f32_16x16x32_bf16 v[12:15],a[92:95],a[28:31],v[12:15]\n"
"v_exp_f32 v119,v38\n"
"v_mfma_f32_16x16x32_bf16 v[8:11],a[76:79],a[28:31],v[100:103]\n"
"v_cvt_pk_bf16_f32 v82,v122,v123\n"
"v_or3_b32 v118,v80,v81,v82\n"
"v_mfma_f32_16x16x32_bf16 v[8:11],a[72:75],a[24:27],v[8:11]\n"
"v_exp_f32 v120,v39\n"
"v_mfma_f32_16x16x32_bf16 v[8:11],a[68:71],a[20:23],v[8:11]\n"
"buffer_load_dwordx4 v115,s[32:35],s54 offen sc1 lds\n"
"v_mfma_f32_16x16x32_bf16 v[8:11],a[64:67],a[16:19],v[8:11]\n"
"s_add_u32 m0,s67,49152\n"
"v_exp_f32 v122,v40\n"
"v_mfma_f32_16x16x32_bf16 v[16:19],a[64:67],a[32:35],v[104:107]\n"
"v_cvt_pk_bf16_f32 v83,v119,v120\n"
"v_mfma_f32_16x16x32_bf16 v[16:19],a[68:71],a[36:39],v[16:19]\n"
"v_exp_f32 v123,v41\n"
"v_mfma_f32_16x16x32_bf16 v[16:19],a[72:75],a[40:43],v[16:19]\n"
"buffer_load_dwordx4 v112,s[24:27],s64 offen offset:0 sc1 lds\n"
"v_mfma_f32_16x16x32_bf16 v[16:19],a[76:79],a[44:47],v[16:19]\n"
"v_exp_f32 v119,v42\n"
"v_mfma_f32_16x16x32_bf16 v[20:23],a[92:95],a[44:47],v[104:107]\n"
"v_cvt_pk_bf16_f32 v84,v122,v123\n"
"v_or3_b32 v118,v118,v83,v84\n"
"v_mfma_f32_16x16x32_bf16 v[20:23],a[88:91],a[40:43],v[20:23]\n"
"v_exp_f32 v120,v43\n"
"v_mfma_f32_16x16x32_bf16 v[20:23],a[84:87],a[36:39],v[20:23]\n"
"buffer_load_dwordx4 v112,s[24:27],s64 offen offset:1024 sc1 lds\n"
"v_mfma_f32_16x16x32_bf16 v[20:23],a[80:83],a[32:35],v[20:23]\n"
"v_exp_f32 v122,v44\n"
"v_mfma_f32_16x16x32_bf16 v[28:31],a[80:83],a[48:51],v[108:111]\n"
"v_cvt_pk_bf16_f32 v85,v119,v120\n"
"v_mfma_f32_16x16x32_bf16 v[28:31],a[84:87],a[52:55],v[28:31]\n"
"v_exp_f32 v123,v45\n"
"v_mfma_f32_16x16x32_bf16 v[28:31],a[88:91],a[56:59],v[28:31]\n"
"buffer_load_dwordx4 v112,s[24:27],s64 offen offset:2048 sc1 lds\n"
"v_mfma_f32_16x16x32_bf16 v[28:31],a[92:95],a[60:63],v[28:31]\n"
"v_exp_f32 v119,v46\n"
"v_mfma_f32_16x16x32_bf16 v[24:27],a[76:79],a[60:63],v[108:111]\n"
"v_cvt_pk_bf16_f32 v86,v122,v123\n"
"v_or3_b32 v118,v118,v85,v86\n"
"v_mfma_f32_16x16x32_bf16 v[24:27],a[72:75],a[56:59],v[24:27]\n"
"v_exp_f32 v120,v47\n"
"v_mfma_f32_16x16x32_bf16 v[24:27],a[68:71],a[52:55],v[24:27]\n"
"buffer_load_dwordx4 v112,s[24:27],s64 offen offset:3072 sc1 lds\n"
"v_mfma_f32_16x16x32_bf16 v[24:27],a[64:67],a[48:51],v[24:27]\n"
"s_waitcnt lgkmcnt(0)\n"
"v_exp_f32 v122,v48\n"
"v_mfma_f32_16x16x32_bf16 v[128:131],a[96:99],v[64:67],v[128:131]\n"
"v_exp_f32 v123,v49\n"
"v_mfma_f32_16x16x32_bf16 v[160:163],a[96:99],v[68:71],v[160:163]\n"
"s_add_u32 s64,s64,16384\n"
"ds_read_b128 a[64:67],v113 offset:32768\n"
"v_cvt_pk_bf16_f32 v87,v119,v120\n"
"v_mfma_f32_16x16x32_bf16 v[192:195],a[96:99],v[72:75],v[192:195]\n"
"v_exp_f32 v119,v50\n"
"v_mfma_f32_16x16x32_bf16 v[224:227],a[96:99],v[76:79],v[224:227]\n"
"ds_read_b128 a[68:71],v113 offset:33792\n"
"v_cvt_pk_bf16_f32 v88,v122,v123\n"
"v_mfma_f32_16x16x32_bf16 a[140:143],a[144:147],v[76:79],a[140:143]\n"
"v_exp_f32 v120,v51\n"
"v_mfma_f32_16x16x32_bf16 v[228:231],a[100:103],v[76:79],v[228:231]\n"
"ds_read_b128 a[72:75],v113 offset:34816\n"
"ds_read_b128 a[76:79],v113 offset:35840\n"
"v_mfma_f32_16x16x32_bf16 v[132:135],a[100:103],v[64:67],v[132:135]\n"
"v_exp_f32 v122,v52\n"
"v_mfma_f32_16x16x32_bf16 v[164:167],a[100:103],v[68:71],v[164:167]\n"
"ds_read_b128 a[80:83],v113 offset:36864\n"
"v_cvt_pk_bf16_f32 v89,v119,v120\n"
"v_mfma_f32_16x16x32_bf16 v[196:199],a[100:103],v[72:75],v[196:199]\n"
"v_exp_f32 v123,v53\n"
"v_mfma_f32_16x16x32_bf16 a[136:139],a[144:147],v[72:75],a[136:139]\n"
"ds_read_b128 a[84:87],v113 offset:37888\n"
"ds_read_b128 a[88:91],v113 offset:38912\n"
"v_mfma_f32_16x16x32_bf16 v[200:203],a[104:107],v[72:75],v[200:203]\n"
"v_exp_f32 v119,v54\n"
"v_mfma_f32_16x16x32_bf16 v[232:235],a[104:107],v[76:79],v[232:235]\n"
"ds_read_b128 a[92:95],v113 offset:39936\n"
"v_cvt_pk_bf16_f32 v90,v122,v123\n"
"v_mfma_f32_16x16x32_bf16 v[136:139],a[104:107],v[64:67],v[136:139]\n"
"v_exp_f32 v120,v55\n"
"v_mfma_f32_16x16x32_bf16 v[168:171],a[104:107],v[68:71],v[168:171]\n"
"s_waitcnt vmcnt(12)\n"
"ds_add_u32 v124,a152\n"
"v_mfma_f32_16x16x32_bf16 a[132:135],a[144:147],v[68:71],a[132:135]\n"
"v_exp_f32 v122,v56\n"
"v_mfma_f32_16x16x32_bf16 v[172:175],a[108:111],v[68:71],v[172:175]\n"
"v_cvt_pk_bf16_f32 v91,v119,v120\n"
"v_or3_b32 v118,v118,v87,v88\n"
"v_mfma_f32_16x16x32_bf16 v[204:207],a[108:111],v[72:75],v[204:207]\n"
"v_exp_f32 v123,v57\n"
"v_mfma_f32_16x16x32_bf16 v[236:239],a[108:111],v[76:79],v[236:239]\n"
"v_cvt_pk_bf16_f32 v92,v122,v123\n"
"v_or3_b32 v118,v118,v89,v90\n"
"v_mfma_f32_16x16x32_bf16 v[140:143],a[108:111],v[64:67],v[140:143]\n"
"v_exp_f32 v119,v58\n"
"v_mfma_f32_16x16x32_bf16 a[128:131],a[144:147],v[64:67],a[128:131]\n"
"v_or3_b32 v118,v118,v91,v92\n"
"v_mfma_f32_16x16x32_bf16 v[144:147],a[112:115],v[64:67],v[144:147]\n"
"v_exp_f32 v120,v59\n"
"v_mfma_f32_16x16x32_bf16 v[176:179],a[112:115],v[68:71],v[176:179]\n"
"v_cvt_pk_bf16_f32 v93,v119,v120\n"
"v_mfma_f32_16x16x32_bf16 v[208:211],a[112:115],v[72:75],v[208:211]\n"
"v_exp_f32 v122,v60\n"
"v_mfma_f32_16x16x32_bf16 v[240:243],a[112:115],v[76:79],v[240:243]\n"
"v_mfma_f32_16x16x32_bf16 v[244:247],a[116:119],v[76:79],v[244:247]\n"
"v_exp_f32 v123,v61\n"
"v_mfma_f32_16x16x32_bf16 v[148:151],a[116:119],v[64:67],v[148:151]\n"
"v_cvt_pk_bf16_f32 v94,v122,v123\n"
"v_or3_b32 v118,v118,v93,v94\n"
"v_mfma_f32_16x16x32_bf16 v[180:183],a[116:119],v[68:71],v[180:183]\n"
"v_exp_f32 v119,v62\n"
"v_mfma_f32_16x16x32_bf16 v[212:215],a[116:119],v[72:75],v[212:215]\n"
"ds_read_b32 v125,v124\n"
"buffer_store_dwordx4 a[248:251],v112,s[24:27],s56 offen offset:2048 sc1\n"
"v_mfma_f32_16x16x32_bf16 v[216:219],a[120:123],v[72:75],v[216:219]\n"
"v_exp_f32 v120,v63\n"
"v_mfma_f32_16x16x32_bf16 v[248:251],a[120:123],v[76:79],v[248:251]\n"
"v_cvt_pk_bf16_f32 v95,v119,v120\n"
"v_mfma_f32_16x16x32_bf16 v[152:155],a[120:123],v[64:67],v[152:155]\n"
"v_mfma_f32_16x16x32_bf16 v[184:187],a[120:123],v[68:71],v[184:187]\n"
"v_mfma_f32_16x16x32_bf16 v[188:191],a[124:127],v[68:71],v[188:191]\n"
"s_waitcnt lgkmcnt(0)\n"
"v_subrev_u32 v125,s101,v125\n"
"v_or3_b32 v125,v118,v95,v125\n"
"v_mfma_f32_16x16x32_bf16 v[220:223],a[124:127],v[72:75],v[220:223]\n"
"v_and_b32 v125,0x40004000,v125\n"
"v_cmp_ne_u32 vcc,0,v125\n"
"v_mfma_f32_16x16x32_bf16 v[252:255],a[124:127],v[76:79],v[252:255]\n"
"v_mfma_f32_16x16x32_bf16 v[156:159],a[124:127],v[64:67],v[156:159]\n"
"s_cbranch_vccnz .Lfk_cs_f1\n"
".Lfk_cr_f1:\n"
"v_mfma_f32_16x16x32_bf16 v[32:35],a[64:67],a[0:3],v[96:99]\n"
"s_add_u32 s101,s101,4\n"
"v_exp_f32 v122,v0\n"
"v_mfma_f32_16x16x32_bf16 v[32:35],a[68:71],a[4:7],v[32:35]\n"
"s_add_u32 m0,s55,12288\n"
"v_exp_f32 v123,v1\n"
"v_mfma_f32_16x16x32_bf16 v[32:35],a[72:75],a[8:11],v[32:35]\n"
"ds_read_b128 a[96:99],v114 offset:8192\n"
"ds_read_b128 a[100:103],v114 offset:9216\n"
"v_mfma_f32_16x16x32_bf16 v[32:35],a[76:79],a[12:15],v[32:35]\n"
"s_add_u32 s54,s54,s22\n"
"v_exp_f32 v119,v2\n"
"v_mfma_f32_16x16x32_bf16 v[36:39],a[92:95],a[12:15],v[96:99]\n"
"ds_read_b128 a[104:107],v114 offset:10240\n"
"v_cvt_pk_bf16_f32 v64,v122,v123\n"
"v_mfma_f32_16x16x32_bf16 v[36:39],a[88:91],a[8:11],v[36:39]\n"
"v_exp_f32 v120,v3\n"
"v_mfma_f32_16x16x32_bf16 v[36:39],a[84:87],a[4:7],v[36:39]\n"
"ds_read_b128 a[108:111],v114 offset:11264\n"
"ds_read_b128 a[112:115],v114 offset:12288\n"
"v_mfma_f32_16x16x32_bf16 v[36:39],a[80:83],a[0:3],v[36:39]\n"
"v_exp_f32 v122,v4\n"
"v_mfma_f32_16x16x32_bf16 v[44:47],a[80:83],a[16:19],v[100:103]\n"
"ds_read_b128 a[116:119],v114 offset:13312\n"
"v_cvt_pk_bf16_f32 v65,v119,v120\n"
"v_mfma_f32_16x16x32_bf16 v[44:47],a[84:87],a[20:23],v[44:47]\n"
"v_exp_f32 v123,v5\n"
"v_mfma_f32_16x16x32_bf16 v[44:47],a[88:91],a[24:27],v[44:47]\n"
"ds_read_b128 a[120:123],v114 offset:14336\n"
"ds_read_b128 a[124:127],v114 offset:15360\n"
"v_mfma_f32_16x16x32_bf16 v[44:47],a[92:95],a[28:31],v[44:47]\n"
"v_exp_f32 v119,v6\n"
"v_mfma_f32_16x16x32_bf16 v[40:43],a[76:79],a[28:31],v[100:103]\n"
"v_cvt_pk_bf16_f32 v66,v122,v123\n"
"v_or3_b32 v118,v64,v65,v66\n"
"v_mfma_f32_16x16x32_bf16 v[40:43],a[72:75],a[24:27],v[40:43]\n"
"v_exp_f32 v120,v7\n"
"v_mfma_f32_16x16x32_bf16 v[40:43],a[68:71],a[20:23],v[40:43]\n"
"buffer_load_dwordx4 v115,s[32:35],s54 offen sc1 lds\n"
"v_mfma_f32_16x16x32_bf16 v[40:43],a[64:67],a[16:19],v[40:43]\n"
"s_add_u32 m0,s67,32768\n"
"v_exp_f32 v122,v8\n"
"v_mfma_f32_16x16x32_bf16 v[48:51],a[64:67],a[32:35],v[104:107]\n"
"v_cvt_pk_bf16_f32 v67,v119,v120\n"
"v_mfma_f32_16x16x32_bf16 v[48:51],a[68:71],a[36:39],v[48:51]\n"
"v_exp_f32 v123,v9\n"
"v_mfma_f32_16x16x32_bf16 v[48:51],a[72:75],a[40:43],v[48:51]\n"
"buffer_load_dwordx4 v112,s[24:27],s64 offen offset:0 sc1 lds\n"
"v_mfma_f32_16x16x32_bf16 v[48:51],a[76:79],a[44:47],v[48:51]\n"
"v_exp_f32 v119,v10\n"
"v_mfma_f32_16x16x32_bf16 v[52:55],a[92:95],a[44:47],v[104:107]\n"
"v_cvt_pk_bf16_f32 v68,v122,v123\n"
"v_or3_b32 v118,v118,v67,v68\n"
"v_mfma_f32_16x16x32_bf16 v[52:55],a[88:91],a[40:43],v[52:55]\n"
"v_exp_f32 v120,v11\n"
"v_mfma_f32_16x16x32_bf16 v[52:55],a[84:87],a[36:39],v[52:55]\n"
"buffer_load_dwordx4 v112,s[24:27],s64 offen offset:1024 sc1 lds\n"
"v_mfma_f32_16x16x32_bf16 v[52:55],a[80:83],a[32:35],v[52:55]\n"
"v_exp_f32 v122,v12\n"
"v_mfma_f32_16x16x32_bf16 v[60:63],a[80:83],a[48:51],v[108:111]\n"
"v_cvt_pk_bf16_f32 v69,v119,v120\n"
"v_mfma_f32_16x16x32_bf16 v[60:63],a[84:87],a[52:55],v[60:63]\n"
"v_exp_f32 v123,v13\n"
"v_mfma_f32_16x16x32_bf16 v[60:63],a[88:91],a[56:59],v[60:63]\n"
"buffer_load_dwordx4 v112,s[24:27],s64 offen offset:2048 sc1 lds\n"
"v_mfma_f32_16x16x32_bf16 v[60:63],a[92:95],a[60:63],v[60:63]\n"
"v_exp_f32 v119,v14\n"
"v_mfma_f32_16x16x32_bf16 v[56:59],a[76:79],a[60:63],v[108:111]\n"
"v_cvt_pk_bf16_f32 v70,v122,v123\n"
"v_or3_b32 v118,v118,v69,v70\n"
"v_mfma_f32_16x16x32_bf16 v[56:59],a[72:75],a[56:59],v[56:59]\n"
"v_exp_f32 v120,v15\n"
"v_mfma_f32_16x16x32_bf16 v[56:59],a[68:71],a[52:55],v[56:59]\n"
"buffer_load_dwordx4 v112,s[24:27],s64 offen offset:3072 sc1 lds\n"
"v_mfma_f32_16x16x32_bf16 v[56:59],a[64:67],a[48:51],v[56:59]\n"
"s_waitcnt lgkmcnt(0)\n"
"v_exp_f32 v122,v16\n"
"v_mfma_f32_16x16x32_bf16 v[128:131],a[96:99],v[80:83],v[128:131]\n"
"v_exp_f32 v123,v17\n"
"v_mfma_f32_16x16x32_bf16 v[160:163],a[96:99],v[84:87],v[160:163]\n"
"s_add_u32 s64,s64,16384\n"
"ds_read_b128 a[64:67],v113 offset:0\n"
"v_cvt_pk_bf16_f32 v71,v119,v120\n"
"v_mfma_f32_16x16x32_bf16 v[192:195],a[96:99],v[88:91],v[192:195]\n"
"v_exp_f32 v119,v18\n"
"v_mfma_f32_16x16x32_bf16 v[224:227],a[96:99],v[92:95],v[224:227]\n"
"ds_read_b128 a[68:71],v113 offset:1024\n"
"v_cvt_pk_bf16_f32 v72,v122,v123\n"
"v_mfma_f32_16x16x32_bf16 a[140:143],a[144:147],v[92:95],a[140:143]\n"
"v_exp_f32 v120,v19\n"
"v_mfma_f32_16x16x32_bf16 v[228:231],a[100:103],v[92:95],v[228:231]\n"
"ds_read_b128 a[72:75],v113 offset:2048\n"
"ds_read_b128 a[76:79],v113 offset:3072\n"
"v_mfma_f32_16x16x32_bf16 v[132:135],a[100:103],v[80:83],v[132:135]\n"
"v_exp_f32 v122,v20\n"
"v_mfma_f32_16x16x32_bf16 v[164:167],a[100:103],v[84:87],v[164:167]\n"
"ds_read_b128 a[80:83],v113 offset:4096\n"
"v_cvt_pk_bf16_f32 v73,v119,v120\n"
"v_mfma_f32_16x16x32_bf16 v[196:199],a[100:103],v[88:91],v[196:199]\n"
"v_exp_f32 v123,v21\n"
"v_mfma_f32_16x16x32_bf16 a[136:139],a[144:147],v[88:91],a[136:139]\n"
"ds_read_b128 a[84:87],v113 offset:5120\n"
"ds_read_b128 a[88:91],v113 offset:6144\n"
"v_mfma_f32_16x16x32_bf16 v[200:203],a[104:107],v[88:91],v[200:203]\n"
"v_exp_f32 v119,v22\n"
"v_mfma_f32_16x16x32_bf16 v[232:235],a[104:107],v[92:95],v[232:235]\n"
"ds_read_b128 a[92:95],v113 offset:7168\n"
"v_cvt_pk_bf16_f32 v74,v122,v123\n"
"v_mfma_f32_16x16x32_bf16 v[136:139],a[104:107],v[80:83],v[136:139]\n"
"v_exp_f32 v120,v23\n"
"v_mfma_f32_16x16x32_bf16 v[168:171],a[104:107],v[84:87],v[168:171]\n"
"s_waitcnt vmcnt(12)\n"
"ds_add_u32 v124,a152\n"
"v_mfma_f32_16x16x32_bf16 a[132:135],a[144:147],v[84:87],a[132:135]\n"
"v_exp_f32 v122,v24\n"
"v_mfma_f32_16x16x32_bf16 v[172:175],a[108:111],v[84:87],v[172:175]\n"
"v_cvt_pk_bf16_f32 v75,v119,v120\n"
"v_or3_b32 v118,v118,v71,v72\n"
"v_mfma_f32_16x16x32_bf16 v[204:207],a[108:111],v[88:91],v[204:207]\n"
"v_exp_f32 v123,v25\n"
"v_mfma_f32_16x16x32_bf16 v[236:239],a[108:111],v[92:95],v[236:239]\n"
"v_cvt_pk_bf16_f32 v76,v122,v123\n"
"v_or3_b32 v118,v118,v73,v74\n"
"v_mfma_f32_16x16x32_bf16 v[140:143],a[108:111],v[80:83],v[140:143]\n"
"v_exp_f32 v119,v26\n"
"v_mfma_f32_16x16x32_bf16 a[128:131],a[144:147],v[80:83],a[128:131]\n"
"v_or3_b32 v118,v118,v75,v76\n"
"v_mfma_f32_16x16x32_bf16 v[144:147],a[112:115],v[80:83],v[144:147]\n"
"v_exp_f32 v120,v27\n"
"v_mfma_f32_16x16x32_bf16 v[176:179],a[112:115],v[84:87],v[176:179]\n"
"v_cvt_pk_bf16_f32 v77,v119,v120\n"
"v_mfma_f32_16x16x32_bf16 v[208:211],a[112:115],v[88:91],v[208:211]\n"
"v_exp_f32 v122,v28\n"
"v_mfma_f32_16x16x32_bf16 v[240:243],a[112:115],v[92:95],v[240:243]\n"
"v_mfma_f32_16x16x32_bf16 v[244:247],a[116:119],v[92:95],v[244:247]\n"
"v_exp_f32 v123,v29\n"
"v_mfma_f32_16x16x32_bf16 v[148:151],a[116:119],v[80:83],v[148:151]\n"
"v_cvt_pk_bf16_f32 v78,v122,v123\n"
"v_or3_b32 v118,v118,v77,v78\n"
"v_mfma_f32_16x16x32_bf16 v[180:183],a[116:119],v[84:87],v[180:183]\n"
"v_exp_f32 v119,v30\n"
"v_mfma_f32_16x16x32_bf16 v[212:215],a[116:119],v[88:91],v[212:215]\n"
"ds_read_b32 v125,v124\n"
"buffer_store_dwordx4 a[252:255],v112,s[24:27],s56 offen offset:3072 sc1\n"
"v_mfma_f32_16x16x32_bf16 v[216:219],a[120:123],v[88:91],v[216:219]\n"
"s_add_u32 s89,s89,1\n"
"v_exp_f32 v120,v31\n"
"v_mfma_f32_16x16x32_bf16 v[248:251],a[120:123],v[92:95],v[248:251]\n"
"v_cvt_pk_bf16_f32 v79,v119,v120\n"
"v_mfma_f32_16x16x32_bf16 v[152:155],a[120:123],v[80:83],v[152:155]\n"
"v_mfma_f32_16x16x32_bf16 v[184:187],a[120:123],v[84:87],v[184:187]\n"
"v_mfma_f32_16x16x32_bf16 v[188:191],a[124:127],v[84:87],v[188:191]\n"
"s_waitcnt lgkmcnt(0)\n"
"v_subrev_u32 v125,s101,v125\n"
"v_or3_b32 v125,v118,v79,v125\n"
"v_mfma_f32_16x16x32_bf16 v[220:223],a[124:127],v[88:91],v[220:223]\n"
"v_and_b32 v125,0x40004000,v125\n"
"v_cmp_ne_u32 vcc,0,v125\n"
"v_mfma_f32_16x16x32_bf16 v[252:255],a[124:127],v[92:95],v[252:255]\n"
"v_mfma_f32_16x16x32_bf16 v[156:159],a[124:127],v[80:83],v[156:159]\n"
"s_cbranch_vccnz .Lfk_cs_f2\n"
".Lfk_cr_f2:\n"
"s_cmp_lt_u32 s89,s60\n"
"s_cbranch_scc1 .Lfk_F\n"
"s_branch .Lfk_Fx\n"
".Lfk_cs_f3:\n"
"s_call_b64 s[94:95],.Lfk_meet3\n"
"v_or_b32 v118,v118,v95\n"
"v_and_b32 v118,0x40004000,v118\n"
"v_cmp_ne_u32 vcc,0,v118\n"
"s_nop 0\n"
"s_cbranch_vccz .Lfk_cr_f3\n"
"s_mov_b32 s91,15\n"
"s_call_b64 s[94:95],.Lfk_rare3\n"
"s_branch .Lfk_cr_f3\n"
".Lfk_cs_f0:\n"
"s_call_b64 s[94:95],.Lfk_meet3\n"
"v_or_b32 v118,v118,v79\n"
"v_and_b32 v118,0x40004000,v118\n"
"v_cmp_ne_u32 vcc,0,v118\n"
"s_nop 0\n"
"s_cbranch_vccz .Lfk_cr_f0\n"
"s_mov_b32 s91,15\n"
"s_call_b64 s[94:95],.Lfk_rare2\n"
"s_branch .Lfk_cr_f0\n"
".Lfk_cs_f1:\n"
"s_call_b64 s[94:95],.Lfk_meet3\n"
"v_or_b32 v118,v118,v95\n"
"v_and_b32 v118,0x40004000,v118\n"
"v_cmp_ne_u32 vcc,0,v118\n"
"s_nop 0\n"
"s_cbranch_vccz .Lfk_cr_f1\n"
"s_mov_b32 s91,15\n"
"s_call_b64 s[94:95],.Lfk_rare3\n"
"s_branch .Lfk_cr_f1\n"
".Lfk_cs_f2:\n"
"s_call_b64 s[94:95],.Lfk_meet3\n"
"v_or_b32 v118,v118,v79\n"
"v_and_b32 v118,0x40004000,v118\n"
"v_cmp_ne_u32 vcc,0,v118\n"
"s_nop 0\n"
"s_cbranch_vccz .Lfk_cr_f2\n"
"s_mov_b32 s91,15\n"
"s_call_b64 s[94:95],.Lfk_rare2\n"
"s_branch .Lfk_cr_f2\n"
".Lfk_cs_g3:\n"
"s_call_b64 s[94:95],.Lfk_meet3\n"
"v_or_b32 v118,v118,v95\n"
"v_and_b32 v118,0x40004000,v118\n"
"v_cmp_ne_u32 vcc,0,v118\n"
"s_nop 0\n"
"s_cbranch_vccz .Lfk_cr_g3\n"
"s_mov_b32 s91,15\n"
"s_call_b64 s[94:95],.Lfk_rare3\n"
"s_branch .Lfk_cr_g3\n"
".Lfk_cs_g0:\n"
"s_call_b64 s[94:95],.Lfk_meet3\n"
"v_or_b32 v118,v118,v79\n"
"v_and_b32 v118,0x40004000,v118\n"
"v_cmp_ne_u32 vcc,0,v118\n"
"s_nop 0\n"
"s_cbranch_vccz .Lfk_cr_g0\n"
"s_mov_b32 s91,15\n"
"s_call_b64 s[94:95],.Lfk_rare2\n"
"s_branch .Lfk_cr_g0\n"
".Lfk_cs_g1:\n"
"s_call_b64 s[94:95],.Lfk_meet3\n"
"v_or_b32 v118,v118,v95\n"
"v_and_b32 v118,0x40004000,v118\n"
"v_cmp_ne_u32 vcc,0,v118\n"
"s_nop 0\n"
"s_cbranch_vccz .Lfk_cr_g1\n"
"s_mov_b32 s91,15\n"
"s_call_b64 s[94:95],.Lfk_rare3\n"
"s_branch .Lfk_cr_g1\n"
".Lfk_cs_g2:\n"
"s_call_b64 s[94:95],.Lfk_meet3\n"
"v_or_b32 v118,v118,v79\n"
"v_and_b32 v118,0x40004000,v118\n"
"v_cmp_ne_u32 vcc,0,v118\n"
"s_nop 0\n"
"s_cbranch_vccz .Lfk_cr_g2\n"
"s_mov_b32 s91,15\n"
"s_call_b64 s[94:95],.Lfk_rare2\n"
"s_branch .Lfk_cr_g2\n"
".Lfk_meet3:\n"
"s_sub_u32 s82,s101,0\n"
"s_mov_b32 s84,0x1000000\n"
".Lfk_meetl3:\n"
"ds_read_b32 v125,v124\n"
"s_sub_u32 s84,s84,1\n"
"s_cbranch_scc1 .Lfk_meetx3\n"
"s_waitcnt lgkmcnt(0)\n"
"v_cmp_gt_u32 vcc,s82,v125\n"
"s_nop 0\n"
"s_cbranch_vccnz .Lfk_meetl3\n"
".Lfk_meetx3:\n"
"s_setpc_b64 s[94:95]\n"
".Lfk_Fx:\n"
"s_mov_b32 s82,2146435072\n"
"buffer_load_dword a148,off,s[24:27],s82\n"
"buffer_load_dword a148,off,s[24:27],s82\n"
"buffer_load_dword a148,off,s[24:27],s82\n"
"buffer_load_dword a148,off,s[24:27],s82\n"
"s_cmp_ge_u32 s52,2\n"
"s_cbranch_scc1 .Lfk_winA\n"
".Lfk_H:\n"
"v_mfma_f32_16x16x32_bf16 v[0:3],a[64:67],a[0:3],v[96:99]\n"
"s_swappc_b64 s[94:95],s[20:21]\n"
"s_add_u32 s101,s101,4\n"
"v_exp_f32 v122,v32\n"
"v_mfma_f32_16x16x32_bf16 v[0:3],a[68:71],a[4:7],v[0:3]\n"
"s_cmp_eq_u32 s64,s65\n"
"v_exp_f32 v123,v33\n"
"v_mfma_f32_16x16x32_bf16 v[0:3],a[72:75],a[8:11],v[0:3]\n"
"ds_read_b128 a[96:99],v114 offset:24576\n"
"ds_read_b128 a[100:103],v114 offset:25600\n"
"v_mfma_f32_16x16x32_bf16 v[0:3],a[76:79],a[12:15],v[0:3]\n"
"s_cselect_b32 s64,s66,s64\n"
"v_exp_f32 v119,v34\n"
"v_mfma_f32_16x16x32_bf16 v[4:7],a[92:95],a[12:15],v[96:99]\n"
"ds_read_b128 a[104:107],v114 offset:26624\n"
"v_cvt_pk_bf16_f32 v80,v122,v123\n"
"v_mfma_f32_16x16x32_bf16 v[4:7],a[88:91],a[8:11],v[4:7]\n"
"v_exp_f32 v120,v35\n"
"v_mfma_f32_16x16x32_bf16 v[4:7],a[84:87],a[4:7],v[4:7]\n"
"ds_read_b128 a[108:111],v114 offset:27648\n"
"ds_read_b128 a[112:115],v114 offset:28672\n"
"v_mfma_f32_16x16x32_bf16 v[4:7],a[80:83],a[0:3],v[4:7]\n"
"v_exp_f32 v122,v36\n"
"v_mfma_f32_16x16x32_bf16 v[12:15],a[80:83],a[16:19],v[100:103]\n"
"ds_read_b128 a[116:119],v114 offset:29696\n"
"v_cvt_pk_bf16_f32 v81,v119,v120\n"
"v_mfma_f32_16x16x32_bf16 v[12:15],a[84:87],a[20:23],v[12:15]\n"
"v_exp_f32 v123,v37\n"
"v_mfma_f32_16x16x32_bf16 v[12:15],a[88:91],a[24:27],v[12:15]\n"
"ds_read_b128 a[120:123],v114 offset:30720\n"
"ds_read_b128 a[124:127],v114 offset:31744\n"
"v_mfma_f32_16x16x32_bf16 v[12:15],a[92:95],a[28:31],v[12:15]\n"
"s_add_u32 m0,s67,0\n"
"v_exp_f32 v119,v38\n"
"v_mfma_f32_16x16x32_bf16 v[8:11],a[76:79],a[28:31],v[100:103]\n"
"v_cvt_pk_bf16_f32 v82,v122,v123\n"
"v_or3_b32 v118,v80,v81,v82\n"
"v_mfma_f32_16x16x32_bf16 v[8:11],a[72:75],a[24:27],v[8:11]\n"
"v_exp_f32 v120,v39\n"
"v_mfma_f32_16x16x32_bf16 v[8:11],a[68:71],a[20:23],v[8:11]\n"
"buffer_load_dwordx4 v112,s[24:27],s64 offen offset:0 sc1 lds\n"
"v_mfma_f32_16x16x32_bf16 v[8:11],a[64:67],a[16:19],v[8:11]\n"
"v_exp_f32 v122,v40\n"
"v_mfma_f32_16x16x32_bf16 v[16:19],a[64:67],a[32:35],v[104:107]\n"
"v_cvt_pk_bf16_f32 v83,v119,v120\n"
"v_mfma_f32_16x16x32_bf16 v[16:19],a[68:71],a[36:39],v[16:19]\n"
"v_exp_f32 v123,v41\n"
"v_mfma_f32_16x16x32_bf16 v[16:19],a[72:75],a[40:43],v[16:19]\n"
"buffer_load_dwordx4 v112,s[24:27],s64 offen offset:1024 sc1 lds\n"
"v_mfma_f32_16x16x32_bf16 v[16:19],a[76:79],a[44:47],v[16:19]\n"
"v_exp_f32 v119,v42\n"
"v_mfma_f32_16x16x32_bf16 v[20:23],a[92:95],a[44:47],v[104:107]\n"
"v_cvt_pk_bf16_f32 v84,v122,v123\n"
"v_or3_b32 v118,v118,v83,v84\n"
"v_mfma_f32_16x16x32_bf16 v[20:23],a[88:91],a[40:43],v[20:23]\n"
"v_exp_f32 v120,v43\n"
"v_mfma_f32_16x16x32_bf16 v[20:23],a[84:87],a[36:39],v[20:23]\n"
"v_cvt_pk_bf16_f32 v85,v119,v120\n"
"v_mfma_f32_16x16x32_bf16 v[20:23],a[80:83],a[32:35],v[20:23]\n"
"v_exp_f32 v122,v44\n"
"v_mfma_f32_16x16x32_bf16 v[28:31],a[80:83],a[48:51],v[108:111]\n"
"buffer_load_dwordx4 v112,s[24:27],s64 offen offset:2048 sc1 lds\n"
"v_mfma_f32_16x16x32_bf16 v[28:31],a[84:87],a[52:55],v[28:31]\n"
"v_exp_f32 v123,v45\n"
"v_mfma_f32_16x16x32_bf16 v[28:31],a[88:91],a[56:59],v[28:31]\n"
"v_cvt_pk_bf16_f32 v86,v122,v123\n"
"v_or3_b32 v118,v118,v85,v86\n"
"v_mfma_f32_16x16x32_bf16 v[28:31],a[92:95],a[60:63],v[28:31]\n"
"v_exp_f32 v119,v46\n"
"v_mfma_f32_16x16x32_bf16 v[24:27],a[76:79],a[60:63],v[108:111]\n"
"v_mfma_f32_16x16x32_bf16 v[24:27],a[72:75],a[56:59],v[24:27]\n"
"v_exp_f32 v120,v47\n"
"v_mfma_f32_16x16x32_bf16 v[24:27],a[68:71],a[52:55],v[24:27]\n"
"buffer_load_dwordx4 v112,s[24:27],s64 offen offset:3072 sc1 lds\n"
"v_mfma_f32_16x16x32_bf16 v[24:27],a[64:67],a[48:51],v[24:27]\n"
"s_waitcnt lgkmcnt(0)\n"
"v_exp_f32 v122,v48\n"
"v_mfma_f32_16x16x32_bf16 v[128:131],a[96:99],v[64:67],v[128:131]\n"
"v_exp_f32 v123,v49\n"
"v_mfma_f32_16x16x32_bf16 v[160:163],a[96:99],v[68:71],v[160:163]\n"
"ds_read_b128 a[64:67],v113 offset:16384\n"
"v_cvt_pk_bf16_f32 v87,v119,v120\n"
"v_mfma_f32_16x16x32_bf16 v[192:195],a[96:99],v[72:75],v[192:195]\n"
"s_add_u32 m0,s67,4096\n"
"v_exp_f32 v119,v50\n"
"v_mfma_f32_16x16x32_bf16 v[224:227],a[96:99],v[76:79],v[224:227]\n"
"ds_read_b128 a[68:71],v113 offset:17408\n"
"v_cvt_pk_bf16_f32 v88,v122,v123\n"
"v_mfma_f32_16x16x32_bf16 a[140:143],a[144:147],v[76:79],a[140:143]\n"
"v_exp_f32 v120,v51\n"
"v_mfma_f32_16x16x32_bf16 v[228:231],a[100:103],v[76:79],v[228:231]\n"
"ds_read_b128 a[72:75],v113 offset:18432\n"
"ds_read_b128 a[76:79],v113 offset:19456\n"
"v_mfma_f32_16x16x32_bf16 v[132:135],a[100:103],v[64:67],v[132:135]\n"
"v_exp_f32 v122,v52\n"
"v_mfma_f32_16x16x32_bf16 v[164:167],a[100:103],v[68:71],v[164:167]\n"
"ds_read_b128 a[80:83],v113 offset:20480\n"
"v_cvt_pk_bf16_f32 v89,v119,v120\n"
"v_mfma_f32_16x16x32_bf16 v[196:199],a[100:103],v[72:75],v[196:199]\n"
"v_exp_f32 v123,v53\n"
"v_mfma_f32_16x16x32_bf16 a[136:139],a[144:147],v[72:75],a[136:139]\n"
"ds_read_b128 a[84:87],v113 offset:21504\n"
"ds_read_b128 a[88:91],v113 offset:22528\n"
"v_mfma_f32_16x16x32_bf16 v[200:203],a[104:107],v[72:75],v[200:203]\n"
"v_exp_f32 v119,v54\n"
"v_mfma_f32_16x16x32_bf16 v[232:235],a[104:107],v[76:79],v[232:235]\n"
"ds_read_b128 a[92:95],v113 offset:23552\n"
"v_cvt_pk_bf16_f32 v90,v122,v123\n"
"v_mfma_f32_16x16x32_bf16 v[136:139],a[104:107],v[64:67],v[136:139]\n"
"v_exp_f32 v120,v55\n"
"v_mfma_f32_16x16x32_bf16 v[168:171],a[104:107],v[68:71],v[168:171]\n"
"s_waitcnt vmcnt(15)\n"
"ds_add_u32 v124,a152\n"
"v_mfma_f32_16x16x32_bf16 a[132:135],a[144:147],v[68:71],a[132:135]\n"
"v_exp_f32 v122,v56\n"
"v_mfma_f32_16x16x32_bf16 v[172:175],a[108:111],v[68:71],v[172:175]\n"
"v_cvt_pk_bf16_f32 v91,v119,v120\n"
"v_or3_b32 v118,v118,v87,v88\n"
"v_mfma_f32_16x16x32_bf16 v[204:207],a[108:111],v[72:75],v[204:207]\n"
"v_exp_f32 v123,v57\n"
"v_mfma_f32_16x16x32_bf16 v[236:239],a[108:111],v[76:79],v[236:239]\n"
"buffer_load_dwordx4 v117,s[24:27],s64 offen offset:0 sc1 lds\n"
"v_mfma_f32_16x16x32_bf16 v[140:143],a[108:111],v[64:67],v[140:143]\n"
"v_exp_f32 v119,v58\n"
"v_mfma_f32_16x16x32_bf16 a[128:131],a[144:147],v[64:67],a[128:131]\n"
"v_cvt_pk_bf16_f32 v92,v122,v123\n"
"v_or3_b32 v118,v118,v89,v90\n"
"v_mfma_f32_16x16x32_bf16 v[144:147],a[112:115],v[64:67],v[144:147]\n"
"v_exp_f32 v120,v59\n"
"v_mfma_f32_16x16x32_bf16 v[176:179],a[112:115],v[68:71],v[176:179]\n"
"buffer_load_dwordx4 v117,s[24:27],s64 offen offset:1024 sc1 lds\n"
"v_mfma_f32_16x16x32_bf16 v[208:211],a[112:115],v[72:75],v[208:211]\n"
"v_exp_f32 v122,v60\n"
"v_mfma_f32_16x16x32_bf16 v[240:243],a[112:115],v[76:79],v[240:243]\n"
"v_cvt_pk_bf16_f32 v93,v119,v120\n"
"v_or3_b32 v118,v118,v91,v92\n"
"v_mfma_f32_16x16x32_bf16 v[244:247],a[116:119],v[76:79],v[244:247]\n"
"v_exp_f32 v123,v61\n"
"v_mfma_f32_16x16x32_bf16 v[148:151],a[116:119],v[64:67],v[148:151]\n"
"buffer_load_dwordx4 v117,s[24:27],s64 offen offset:2048 sc1 lds\n"
"v_mfma_f32_16x16x32_bf16 v[180:183],a[116:119],v[68:71],v[180:183]\n"
"v_exp_f32 v119,v62\n"
"v_mfma_f32_16x16x32_bf16 v[212:215],a[116:119],v[72:75],v[212:215]\n"
"ds_read_b32 v125,v124\n"
"v_cvt_pk_bf16_f32 v94,v122,v123\n"
"v_mfma_f32_16x16x32_bf16 v[216:219],a[120:123],v[72:75],v[216:219]\n"
"v_exp_f32 v120,v63\n"
"v_mfma_f32_16x16x32_bf16 v[248:251],a[120:123],v[76:79],v[248:251]\n"
"buffer_load_dwordx4 v117,s[24:27],s64 offen offset:3072 sc1 lds\n"
"v_mfma_f32_16x16x32_bf16 v[152:155],a[120:123],v[64:67],v[152:155]\n"
"s_add_u32 s64,s64,16384\n"
"s_andn2_b32 m0,s55,16384\n"
"v_mfma_f32_16x16x32_bf16 v[184:187],a[120:123],v[68:71],v[184:187]\n"
"s_add_u32 s54,s54,s22\n"
"v_cvt_pk_bf16_f32 v95,v119,v120\n"
"v_mfma_f32_16x16x32_bf16 v[188:191],a[124:127],v[68:71],v[188:191]\n"
"s_waitcnt lgkmcnt(0)\n"
"v_subrev_u32 v125,s101,v125\n"
"v_or3_b32 v118,v118,v93,v94\n"
"v_mfma_f32_16x16x32_bf16 v[220:223],a[124:127],v[72:75],v[220:223]\n"
"v_or3_b32 v125,v118,v95,v125\n"
"v_and_b32 v125,0x40004000,v125\n"
"v_mfma_f32_16x16x32_bf16 v[252:255],a[124:127],v[76:79],v[252:255]\n"
"buffer_load_dwordx4 v115,s[32:35],s54 offen sc1 lds\n"
"buffer_store_dwordx4 a[240:243],v112,s[24:27],s56 offen offset:0 sc1\n"
"v_cmp_ne_u32 vcc,0,v125\n"
"v_mfma_f32_16x16x32_bf16 v[156:159],a[124:127],v[64:67],v[156:159]\n"
"s_cbranch_vccnz .Lfk_cs_g3\n"
".Lfk_cr_g3:\n"
"v_mfma_f32_16x16x32_bf16 v[32:35],a[64:67],a[0:3],v[96:99]\n"
"s_add_u32 s101,s101,4\n"
"v_exp_f32 v122,v0\n"
"v_mfma_f32_16x16x32_bf16 v[32:35],a[68:71],a[4:7],v[32:35]\n"
"s_add_u32 m0,s55,4096\n"
"v_exp_f32 v123,v1\n"
"v_mfma_f32_16x16x32_bf16 v[32:35],a[72:75],a[8:11],v[32:35]\n"
"ds_read_b128 a[96:99],v114 offset:57344\n"
"ds_read_b128 a[100:103],v114 offset:58368\n"
"v_mfma_f32_16x16x32_bf16 v[32:35],a[76:79],a[12:15],v[32:35]\n"
"s_add_u32 s54,s54,s22\n"
"v_exp_f32 v119,v2\n"
"v_mfma_f32_16x16x32_bf16 v[36:39],a[92:95],a[12:15],v[96:99]\n"
"ds_read_b128 a[104:107],v114 offset:59392\n"
"v_cvt_pk_bf16_f32 v64,v122,v123\n"
"v_mfma_f32_16x16x32_bf16 v[36:39],a[88:91],a[8:11],v[36:39]\n"
"v_exp_f32 v120,v3\n"
"v_mfma_f32_16x16x32_bf16 v[36:39],a[84:87],a[4:7],v[36:39]\n"
"ds_read_b128 a[108:111],v114 offset:60416\n"
"ds_read_b128 a[112:115],v114 offset:61440\n"
"v_mfma_f32_16x16x32_bf16 v[36:39],a[80:83],a[0:3],v[36:39]\n"
"v_exp_f32 v122,v4\n"
"v_mfma_f32_16x16x32_bf16 v[44:47],a[80:83],a[16:19],v[100:103]\n"
"ds_read_b128 a[116:119],v114 offset:62464\n"
"v_cvt_pk_bf16_f32 v65,v119,v120\n"
"v_mfma_f32_16x16x32_bf16 v[44:47],a[84:87],a[20:23],v[44:47]\n"
"v_exp_f32 v123,v5\n"
"v_mfma_f32_16x16x32_bf16 v[44:47],a[88:91],a[24:27],v[44:47]\n"
"ds_read_b128 a[120:123],v114 offset:63488\n"
"ds_read_b128 a[124:127],v114 offset:64512\n"
"v_mfma_f32_16x16x32_bf16 v[44:47],a[92:95],a[28:31],v[44:47]\n"
"v_exp_f32 v119,v6\n"
"v_mfma_f32_16x16x32_bf16 v[40:43],a[76:79],a[28:31],v[100:103]\n"
"v_cvt_pk_bf16_f32 v66,v122,v123\n"
"v_or3_b32 v118,v64,v65,v66\n"
"v_mfma_f32_16x16x32_bf16 v[40:43],a[72:75],a[24:27],v[40:43]\n"
"v_exp_f32 v120,v7\n"
"v_mfma_f32_16x16x32_bf16 v[40:43],a[68:71],a[20:23],v[40:43]\n"
"buffer_load_dwordx4 v115,s[32:35],s54 offen sc1 lds\n"
"v_mfma_f32_16x16x32_bf16 v[40:43],a[64:67],a[16:19],v[40:43]\n"
"s_add_u32 m0,s67,16384\n"
"v_exp_f32 v122,v8\n"
"v_mfma_f32_16x16x32_bf16 v[48:51],a[64:67],a[32:35],v[104:107]\n"
"v_cvt_pk_bf16_f32 v67,v119,v120\n"
"v_mfma_f32_16x16x32_bf16 v[48:51],a[68:71],a[36:39],v[48:51]\n"
"v_exp_f32 v123,v9\n"
"v_mfma_f32_16x16x32_bf16 v[48:51],a[72:75],a[40:43],v[48:51]\n"
"buffer_load_dwordx4 v112,s[24:27],s64 offen offset:0 sc1 lds\n"
"v_mfma_f32_16x16x32_bf16 v[48:51],a[76:79],a[44:47],v[48:51]\n"
"v_exp_f32 v119,v10\n"
"v_mfma_f32_16x16x32_bf16 v[52:55],a[92:95],a[44:47],v[104:107]\n"
"v_cvt_pk_bf16_f32 v68,v122,v123\n"
"v_or3_b32 v118,v118,v67,v68\n"
"v_mfma_f32_16x16x32_bf16 v[52:55],a[88:91],a[40:43],v[52:55]\n"
"v_exp_f32 v120,v11\n"
"v_mfma_f32_16x16x32_bf16 v[52:55],a[84:87],a[36:39],v[52:55]\n"
"buffer_load_dwordx4 v112,s[24:27],s64 offen offset:1024 sc1 lds\n"
"v_mfma_f32_16x16x32_bf16 v[52:55],a[80:83],a[32:35],v[52:55]\n"
"v_exp_f32 v122,v12\n"
"v_mfma_f32_16x16x32_bf16 v[60:63],a[80:83],a[48:51],v[108:111]\n"
"v_cvt_pk_bf16_f32 v69,v119,v120\n"
"v_mfma_f32_16x16x32_bf16 v[60:63],a[84:87],a[52:55],v[60:63]\n"
"v_exp_f32 v123,v13\n"
"v_mfma_f32_16x16x32_bf16 v[60:63],a[88:91],a[56:59],v[60:63]\n"
"buffer_load_dwordx4 v112,s[24:27],s64 offen offset:2048 sc1 lds\n"
"v_mfma_f32_16x16x32_bf16 v[60:63],a[92:95],a[60:63],v[60:63]\n"
"v_exp_f32 v119,v14\n"
"v_mfma_f32_16x16x32_bf16 v[56:59],a[76:79],a[60:63],v[108:111]\n"
"v_cvt_pk_bf16_f32 v70,v122,v123\n"
"v_or3_b32 v118,v118,v69,v70\n"
"v_mfma_f32_16x16x32_bf16 v[56:59],a[72:75],a[56:59],v[56:59]\n"
"v_exp_f32 v120,v15\n"
"v_mfma_f32_16x16x32_bf16 v[56:59],a[68:71],a[52:55],v[56:59]\n"
"buffer_load_dwordx4 v112,s[24:27],s64 offen offset:3072 sc1 lds\n"
"v_mfma_f32_16x16x32_bf16 v[56:59],a[64:67],a[48:51],v[56:59]\n"
"s_waitcnt lgkmcnt(0)\n"
"v_exp_f32 v122,v16\n"
"v_mfma_f32_16x16x32_bf16 v[128:131],a[96:99],v[80:83],v[128:131]\n"
"v_exp_f32 v123,v17\n"
"v_mfma_f32_16x16x32_bf16 v[160:163],a[96:99],v[84:87],v[160:163]\n"
"ds_read_b128 a[64:67],v113 offset:49152\n"
"v_cvt_pk_bf16_f32 v71,v119,v120\n"
"v_mfma_f32_16x16x32_bf16 v[192:195],a[96:99],v[88:91],v[192:195]\n"
"s_add_u32 m0,s67,20480\n"
"v_exp_f32 v119,v18\n"
"v_mfma_f32_16x16x32_bf16 v[224:227],a[96:99],v[92:95],v[224:227]\n"
"ds_read_b128 a[68:71],v113 offset:50176\n"
"v_cvt_pk_bf16_f32 v72,v122,v123\n"
"v_mfma_f32_16x16x32_bf16 a[140:143],a[144:147],v[92:95],a[140:143]\n"
"v_exp_f32 v120,v19\n"
"v_mfma_f32_16x16x32_bf16 v[228:231],a[100:103],v[92:95],v[228:231]\n"
"ds_read_b128 a[72:75],v113 offset:51200\n"
"ds_read_b128 a[76:79],v113 offset:52224\n"
"v_mfma_f32_16x16x32_bf16 v[132:135],a[100:103],v[80:83],v[132:135]\n"
"v_exp_f32 v122,v20\n"
"v_mfma_f32_16x16x32_bf16 v[164:167],a[100:103],v[84:87],v[164:167]\n"
"ds_read_b128 a[80:83],v113 offset:53248\n"
"v_cvt_pk_bf16_f32 v73,v119,v120\n"
"v_mfma_f32_16x16x32_bf16 v[196:199],a[100:103],v[88:91],v[196:199]\n"
"v_exp_f32 v123,v21\n"
"v_mfma_f32_16x16x32_bf16 a[136:139],a[144:147],v[88:91],a[136:139]\n"
"ds_read_b128 a[84:87],v113 offset:54272\n"
"ds_read_b128 a[88:91],v113 offset:55296\n"
"v_mfma_f32_16x16x32_bf16 v[200:203],a[104:107],v[88:91],v[200:203]\n"
"v_exp_f32 v119,v22\n"
"v_mfma_f32_16x16x32_bf16 v[232:235],a[104:107],v[92:95],v[232:235]\n"
"ds_read_b128 a[92:95],v113 offset:56320\n"
"v_cvt_pk_bf16_f32 v74,v122,v123\n"
"v_mfma_f32_16x16x32_bf16 v[136:139],a[104:107],v[80:83],v[136:139]\n"
"v_exp_f32 v120,v23\n"
"v_mfma_f32_16x16x32_bf16 v[168:171],a[104:107],v[84:87],v[168:171]\n"
"s_waitcnt vmcnt(16)\n"
"ds_add_u32 v124,a152\n"
"v_mfma_f32_16x16x32_bf16 a[132:135],a[144:147],v[84:87],a[132:135]\n"
"v_exp_f32 v122,v24\n"
"v_mfma_f32_16x16x32_bf16 v[172:175],a[108:111],v[84:87],v[172:175]\n"
"v_cvt_pk_bf16_f32 v75,v119,v120\n"
"v_or3_b32 v118,v118,v71,v72\n"
"v_mfma_f32_16x16x32_bf16 v[204:207],a[108:111],v[88:91],v[204:207]\n"
"v_exp_f32 v123,v25\n"
"v_mfma_f32_16x16x32_bf16 v[236:239],a[108:111],v[92:95],v[236:239]\n"
"buffer_load_dwordx4 v117,s[24:27],s64 offen offset:0 sc1 lds\n"
"v_mfma_f32_16x16x32_bf16 v[140:143],a[108:111],v[80:83],v[140:143]\n"
"v_exp_f32 v119,v26\n"
"v_mfma_f32_16x16x32_bf16 a[128:131],a[144:147],v[80:83],a[128:131]\n"
"v_cvt_pk_bf16_f32 v76,v122,v123\n"
"v_or3_b32 v118,v118,v73,v74\n"
"v_mfma_f32_16x16x32_bf16 v[144:147],a[112:115],v[80:83],v[144:147]\n"
"v_exp_f32 v120,v27\n"
"v_mfma_f32_16x16x32_bf16 v[176:179],a[112:115],v[84:87],v[176:179]\n"
"buffer_load_dwordx4 v117,s[24:27],s64 offen offset:1024 sc1 lds\n"
"v_mfma_f32_16x16x32_bf16 v[208:211],a[112:115],v[88:91],v[208:211]\n"
"v_exp_f32 v122,v28\n"
"v_mfma_f32_16x16x32_bf16 v[240:243],a[112:115],v[92:95],v[240:243]\n"
"v_cvt_pk_bf16_f32 v77,v119,v120\n"
"v_or3_b32 v118,v118,v75,v76\n"
"v_mfma_f32_16x16x32_bf16 v[244:247],a[116:119],v[92:95],v[244:247]\n"
"v_exp_f32 v123,v29\n"
"v_mfma_f32_16x16x32_bf16 v[148:151],a[116:119],v[80:83],v[148:151]\n"
"buffer_load_dwordx4 v117,s[24:27],s64 offen offset:2048 sc1 lds\n"
"v_mfma_f32_16x16x32_bf16 v[180:183],a[116:119],v[84:87],v[180:183]\n"
"v_exp_f32 v119,v30\n"
"v_mfma_f32_16x16x32_bf16 v[212:215],a[116:119],v[88:91],v[212:215]\n"
"ds_read_b32 v125,v124\n"
"v_cvt_pk_bf16_f32 v78,v122,v123\n"
"v_mfma_f32_16x16x32_bf16 v[216:219],a[120:123],v[88:91],v[216:219]\n"
"v_exp_f32 v120,v31\n"
"v_mfma_f32_16x16x32_bf16 v[248:251],a[120:123],v[92:95],v[248:251]\n"
"buffer_load_dwordx4 v117,s[24:27],s64 offen offset:3072 sc1 lds\n"
"v_mfma_f32_16x16x32_bf16 v[152:155],a[120:123],v[80:83],v[152:155]\n"
"buffer_store_dwordx4 a[244:247],v112,s[24:27],s56 offen offset:1024 sc1\n"
"v_mfma_f32_16x16x32_bf16 v[184:187],a[120:123],v[84:87],v[184:187]\n"
"s_add_u32 s64,s64,16384\n"
"v_cvt_pk_bf16_f32 v79,v119,v120\n"
"v_or3_b32 v118,v118,v77,v78\n"
"v_mfma_f32_16x16x32_bf16 v[188:191],a[124:127],v[84:87],v[188:191]\n"
"s_waitcnt lgkmcnt(0)\n"
"v_subrev_u32 v125,s101,v125\n"
"v_or3_b32 v125,v118,v79,v125\n"
"v_mfma_f32_16x16x32_bf16 v[220:223],a[124:127],v[88:91],v[220:223]\n"
"v_and_b32 v125,0x40004000,v125\n"
"v_cmp_ne_u32 vcc,0,v125\n"
"v_mfma_f32_16x16x32_bf16 v[252:255],a[124:127],v[92:95],v[252:255]\n"
"v_mfma_f32_16x16x32_bf16 v[156:159],a[124:127],v[80:83],v[156:159]\n"
"s_cbranch_vccnz .Lfk_cs_g0\n"
".Lfk_cr_g0:\n"
"v_mfma_f32_16x16x32_bf16 v[0:3],a[64:67],a[0:3],v[96:99]\n"
"s_add_u32 s101,s101,4\n"
"v_exp_f32 v122,v32\n"
"v_mfma_f32_16x16x32_bf16 v[0:3],a[68:71],a[4:7],v[0:3]\n"
"s_add_u32 m0,s55,8192\n"
"v_exp_f32 v123,v33\n"
"v_mfma_f32_16x16x32_bf16 v[0:3],a[72:75],a[8:11],v[0:3]\n"
"ds_read_b128 a[96:99],v114 offset:40960\n"
"ds_read_b128 a[100:103],v114 offset:41984\n"
"v_mfma_f32_16x16x32_bf16 v[0:3],a[76:79],a[12:15],v[0:3]\n"
"s_add_u32 s54,s54,s22\n"
"v_exp_f32 v119,v34\n"
"v_mfma_f32_16x16x32_bf16 v[4:7],a[92:95],a[12:15],v[96:99]\n"
"ds_read_b128 a[104:107],v114 offset:43008\n"
"v_cvt_pk_bf16_f32 v80,v122,v123\n"
"v_mfma_f32_16x16x32_bf16 v[4:7],a[88:91],a[8:11],v[4:7]\n"
"v_exp_f32 v120,v35\n"
"v_mfma_f32_16x16x32_bf16 v[4:7],a[84:87],a[4:7],v[4:7]\n"
"ds_read_b128 a[108:111],v114 offset:44032\n"
"ds_read_b128 a[112:115],v114 offset:45056\n"
"v_mfma_f32_16x16x32_bf16 v[4:7],a[80:83],a[0:3],v[4:7]\n"
"v_exp_f32 v122,v36\n"
"v_mfma_f32_16x16x32_bf16 v[12:15],a[80:83],a[16:19],v[100:103]\n"
"ds_read_b128 a[116:119],v114 offset:46080\n"
"v_cvt_pk_bf16_f32 v81,v119,v120\n"
"v_mfma_f32_16x16x32_bf16 v[12:15],a[84:87],a[20:23],v[12:15]\n"
"v_exp_f32 v123,v37\n"
"v_mfma_f32_16x16x32_bf16 v[12:15],a[88:91],a[24:27],v[12:15]\n"
"ds_read_b128 a[120:123],v114 offset:47104\n"
"ds_read_b128 a[124:127],v114 offset:48128\n"
"v_mfma_f32_16x16x32_bf16 v[12:15],a[92:95],a[28:31],v[12:15]\n"
"v_exp_f32 v119,v38\n"
"v_mfma_f32_16x16x32_bf16 v[8:11],a[76:79],a[28:31],v[100:103]\n"
"v_cvt_pk_bf16_f32 v82,v122,v123\n"
"v_or3_b32 v118,v80,v81,v82\n"
"v_mfma_f32_16x16x32_bf16 v[8:11],a[72:75],a[24:27],v[8:11]\n"
"v_exp_f32 v120,v39\n"
"v_mfma_f32_16x16x32_bf16 v[8:11],a[68:71],a[20:23],v[8:11]\n"
"buffer_load_dwordx4 v115,s[32:35],s54 offen sc1 lds\n"
"v_mfma_f32_16x16x32_bf16 v[8:11],a[64:67],a[16:19],v[8:11]\n"
"s_add_u32 m0,s67,49152\n"
"v_exp_f32 v122,v40\n"
"v_mfma_f32_16x16x32_bf16 v[16:19],a[64:67],a[32:35],v[104:107]\n"
"v_cvt_pk_bf16_f32 v83,v119,v120\n"
"v_mfma_f32_16x16x32_bf16 v[16:19],a[68:71],a[36:39],v[16:19]\n"
"v_exp_f32 v123,v41\n"
"v_mfma_f32_16x16x32_bf16 v[16:19],a[72:75],a[40:43],v[16:19]\n"
"buffer_load_dwordx4 v112,s[24:27],s64 offen offset:0 sc1 lds\n"
"v_mfma_f32_16x16x32_bf16 v[16:19],a[76:79],a[44:47],v[16:19]\n"
"v_exp_f32 v119,v42\n"
"v_mfma_f32_16x16x32_bf16 v[20:23],a[92:95],a[44:47],v[104:107]\n"
"v_cvt_pk_bf16_f32 v84,v122,v123\n"
"v_or3_b32 v118,v118,v83,v84\n"
"v_mfma_f32_16x16x32_bf16 v[20:23],a[88:91],a[40:43],v[20:23]\n"
"v_exp_f32 v120,v43\n"
"v_mfma_f32_16x16x32_bf16 v[20:23],a[84:87],a[36:39],v[20:23]\n"
"buffer_load_dwordx4 v112,s[24:27],s64 offen offset:1024 sc1 lds\n"
"v_mfma_f32_16x16x32_bf16 v[20:23],a[80:83],a[32:35],v[20:23]\n"
"v_exp_f32 v122,v44\n"
"v_mfma_f32_16x16x32_bf16 v[28:31],a[80:83],a[48:51],v[108:111]\n"
"v_cvt_pk_bf16_f32 v85,v119,v120\n"
"v_mfma_f32_16x16x32_bf16 v[28:31],a[84:87],a[52:55],v[28:31]\n"
"v_exp_f32 v123,v45\n"
"v_mfma_f32_16x16x32_bf16 v[28:31],a[88:91],a[56:59],v[28:31]\n"
"buffer_load_dwordx4 v112,s[24:27],s64 offen offset:2048 sc1 lds\n"
"v_mfma_f32_16x16x32_bf16 v[28:31],a[92:95],a[60:63],v[28:31]\n"
"v_exp_f32 v119,v46\n"
"v_mfma_f32_16x16x32_bf16 v[24:27],a[76:79],a[60:63],v[108:111]\n"
"v_cvt_pk_bf16_f32 v86,v122,v123\n"
"v_or3_b32 v118,v118,v85,v86\n"
"v_mfma_f32_16x16x32_bf16 v[24:27],a[72:75],a[56:59],v[24:27]\n"
"v_exp_f32 v120,v47\n"
"v_mfma_f32_16x16x32_bf16 v[24:27],a[68:71],a[52:55],v[24:27]\n"
"buffer_load_dwordx4 v112,s[24:27],s64 offen offset:3072 sc1 lds\n"
"v_mfma_f32_16x16x32_bf16 v[24:27],a[64:67],a[48:51],v[24:27]\n"
"s_waitcnt lgkmcnt(0)\n"
"v_exp_f32 v122,v48\n"
"v_mfma_f32_16x16x32_bf16 v[128:131],a[96:99],v[64:67],v[128:131]\n"
"v_exp_f32 v123,v49\n"
"v_mfma_f32_16x16x32_bf16 v[160:163],a[96:99],v[68:71],v[160:163]\n"
"ds_read_b128 a[64:67],v113 offset:32768\n"
"v_cvt_pk_bf16_f32 v87,v119,v120\n"
"v_mfma_f32_16x16x32_bf16 v[192:195],a[96:99],v[72:75],v[192:195]\n"
"s_add_u32 m0,s67,53248\n"
"v_exp_f32 v119,v50\n"
"v_mfma_f32_16x16x32_bf16 v[224:227],a[96:99],v[76:79],v[224:227]\n"
"ds_read_b128 a[68:71],v113 offset:33792\n"
"v_cvt_pk_bf16_f32 v88,v122,v123\n"
"v_mfma_f32_16x16x32_bf16 a[140:143],a[144:147],v[76:79],a[140:143]\n"
"v_exp_f32 v120,v51\n"
"v_mfma_f32_16x16x32_bf16 v[228:231],a[100:103],v[76:79],v[228:231]\n"
"ds_read_b128 a[72:75],v113 offset:34816\n"
"ds_read_b128 a[76:79],v113 offset:35840\n"
"v_mfma_f32_16x16x32_bf16 v[132:135],a[100:103],v[64:67],v[132:135]\n"
"v_exp_f32 v122,v52\n"
"v_mfma_f32_16x16x32_bf16 v[164:167],a[100:103],v[68:71],v[164:167]\n"
"ds_read_b128 a[80:83],v113 offset:36864\n"
"v_cvt_pk_bf16_f32 v89,v119,v120\n"
"v_mfma_f32_16x16x32_bf16 v[196:199],a[100:103],v[72:75],v[196:199]\n"
"v_exp_f32 v123,v53\n"
"v_mfma_f32_16x16x32_bf16 a[136:139],a[144:147],v[72:75],a[136:139]\n"
"ds_read_b128 a[84:87],v113 offset:37888\n"
"ds_read_b128 a[88:91],v113 offset:38912\n"
"v_mfma_f32_16x16x32_bf16 v[200:203],a[104:107],v[72:75],v[200:203]\n"
"v_exp_f32 v119,v54\n"
"v_mfma_f32_16x16x32_bf16 v[232:235],a[104:107],v[76:79],v[232:235]\n"
"ds_read_b128 a[92:95],v113 offset:39936\n"
"v_cvt_pk_bf16_f32 v90,v122,v123\n"
"v_mfma_f32_16x16x32_bf16 v[136:139],a[104:107],v[64:67],v[136:139]\n"
"v_exp_f32 v120,v55\n"
"v_mfma_f32_16x16x32_bf16 v[168:171],a[104:107],v[68:71],v[168:171]\n"
"s_waitcnt vmcnt(16)\n"
"ds_add_u32 v124,a152\n"
"v_mfma_f32_16x16x32_bf16 a[132:135],a[144:147],v[68:71],a[132:135]\n"
"v_exp_f32 v122,v56\n"
"v_mfma_f32_16x16x32_bf16 v[172:175],a[108:111],v[68:71],v[172:175]\n"
"v_cvt_pk_bf16_f32 v91,v119,v120\n"
"v_or3_b32 v118,v118,v87,v88\n"
"v_mfma_f32_16x16x32_bf16 v[204:207],a[108:111],v[72:75],v[204:207]\n"
"v_exp_f32 v123,v57\n"
"v_mfma_f32_16x16x32_bf16 v[236:239],a[108:111],v[76:79],v[236:239]\n"
"buffer_load_dwordx4 v117,s[24:27],s64 offen offset:0 sc1 lds\n"
"v_mfma_f32_16x16x32_bf16 v[140:143],a[108:111],v[64:67],v[140:143]\n"
"v_exp_f32 v119,v58\n"
"v_mfma_f32_16x16x32_bf16 a[128:131],a[144:147],v[64:67],a[128:131]\n"
"v_cvt_pk_bf16_f32 v92,v122,v123\n"
"v_or3_b32 v118,v118,v89,v90\n"
"v_mfma_f32_16x16x32_bf16 v[144:147],a[112:115],v[64:67],v[144:147]\n"
"v_exp_f32 v120,v59\n"
"v_mfma_f32_16x16x32_bf16 v[176:179],a[112:115],v[68:71],v[176:179]\n"
"buffer_load_dwordx4 v117,s[24:27],s64 offen offset:1024 sc1 lds\n"
"v_mfma_f32_16x16x32_bf16 v[208:211],a[112:115],v[72:75],v[208:211]\n"
"v_exp_f32 v122,v60\n"
"v_mfma_f32_16x16x32_bf16 v[240:243],a[112:115],v[76:79],v[240:243]\n"
"v_cvt_pk_bf16_f32 v93,v119,v120\n"
"v_or3_b32 v118,v118,v91,v92\n"
"v_mfma_f32_16x16x32_bf16 v[244:247],a[116:119],v[76:79],v[244:247]\n"
"v_exp_f32 v123,v61\n"
"v_mfma_f32_16x16x32_bf16 v[148:151],a[116:119],v[64:67],v[148:151]\n"
"buffer_load_dwordx4 v117,s[24:27],s64 offen offset:2048 sc1 lds\n"
"v_mfma_f32_16x16x32_bf16 v[180:183],a[116:119],v[68:71],v[180:183]\n"
"v_exp_f32 v119,v62\n"
"v_mfma_f32_16x16x32_bf16 v[212:215],a[116:119],v[72:75],v[212:215]\n"
"ds_read_b32 v125,v124\n"
"v_cvt_pk_bf16_f32 v94,v122,v123\n"
"v_mfma_f32_16x16x32_bf16 v[216:219],a[120:123],v[72:75],v[216:219]\n"
"v_exp_f32 v120,v63\n"
"v_mfma_f32_16x16x32_bf16 v[248:251],a[120:123],v[76:79],v[248:251]\n"
"buffer_load_dwordx4 v117,s[24:27],s64 offen offset:3072 sc1 lds\n"
"v_mfma_f32_16x16x32_bf16 v[152:155],a[120:123],v[64:67],v[152:155]\n"
"buffer_store_dwordx4 a[248:251],v112,s[24:27],s56 offen offset:2048 sc1\n"
"v_mfma_f32_16x16x32_bf16 v[184:187],a[120:123],v[68:71],v[184:187]\n"
"s_add_u32 s64,s64,16384\n"
"v_cvt_pk_bf16_f32 v95,v119,v120\n"
"v_or3_b32 v118,v118,v93,v94\n"
"v_mfma_f32_16x16x32_bf16 v[188:191],a[124:127],v[68:71],v[188:191]\n"
"s_waitcnt lgkmcnt(0)\n"
"v_subrev_u32 v125,s101,v125\n"
"v_or3_b32 v125,v118,v95,v125\n"
"v_mfma_f32_16x16x32_bf16 v[220:223],a[124:127],v[72:75],v[220:223]\n"
"v_and_b32 v125,0x40004000,v125\n"
"v_cmp_ne_u32 vcc,0,v125\n"
"v_mfma_f32_16x16x32_bf16 v[252:255],a[124:127],v[76:79],v[252:255]\n"
"v_mfma_f32_16x16x32_bf16 v[156:159],a[124:127],v[64:67],v[156:159]\n"
"s_cbranch_vccnz .Lfk_cs_g1\n"
".Lfk_cr_g1:\n"
"v_mfma_f32_16x16x32_bf16 v[32:35],a[64:67],a[0:3],v[96:99]\n"
"s_add_u32 s101,s101,4\n"
"v_exp_f32 v122,v0\n"
"v_mfma_f32_16x16x32_bf16 v[32:35],a[68:71],a[4:7],v[32:35]\n"
"s_add_u32 m0,s55,12288\n"
"v_exp_f32 v123,v1\n"
"v_mfma_f32_16x16x32_bf16 v[32:35],a[72:75],a[8:11],v[32:35]\n"
"ds_read_b128 a[96:99],v114 offset:8192\n"
"ds_read_b128 a[100:103],v114 offset:9216\n"
"v_mfma_f32_16x16x32_bf16 v[32:35],a[76:79],a[12:15],v[32:35]\n"
"s_add_u32 s54,s54,s22\n"
"v_exp_f32 v119,v2\n"
"v_mfma_f32_16x16x32_bf16 v[36:39],a[92:95],a[12:15],v[96:99]\n"
"ds_read_b128 a[104:107],v114 offset:10240\n"
"v_cvt_pk_bf16_f32 v64,v122,v123\n"
"v_mfma_f32_16x16x32_bf16 v[36:39],a[88:91],a[8:11],v[36:39]\n"
"v_exp_f32 v120,v3\n"
"v_mfma_f32_16x16x32_bf16 v[36:39],a[84:87],a[4:7],v[36:39]\n"
"ds_read_b128 a[108:111],v114 offset:11264\n"
"ds_read_b128 a[112:115],v114 offset:12288\n"
"v_mfma_f32_16x16x32_bf16 v[36:39],a[80:83],a[0:3],v[36:39]\n"
"v_exp_f32 v122,v4\n"
"v_mfma_f32_16x16x32_bf16 v[44:47],a[80:83],a[16:19],v[100:103]\n"
"ds_read_b128 a[116:119],v114 offset:13312\n"
"v_cvt_pk_bf16_f32 v65,v119,v120\n"
"v_mfma_f32_16x16x32_bf16 v[44:47],a[84:87],a[20:23],v[44:47]\n"
"v_exp_f32 v123,v5\n"
"v_mfma_f32_16x16x32_bf16 v[44:47],a[88:91],a[24:27],v[44:47]\n"
"ds_read_b128 a[120:123],v114 offset:14336\n"
"ds_read_b128 a[124:127],v114 offset:15360\n"
"v_mfma_f32_16x16x32_bf16 v[44:47],a[92:95],a[28:31],v[44:47]\n"
"v_exp_f32 v119,v6\n"
"v_mfma_f32_16x16x32_bf16 v[40:43],a[76:79],a[28:31],v[100:103]\n"
"v_cvt_pk_bf16_f32 v66,v122,v123\n"
"v_or3_b32 v118,v64,v65,v66\n"
"v_mfma_f32_16x16x32_bf16 v[40:43],a[72:75],a[24:27],v[40:43]\n"
"v_exp_f32 v120,v7\n"
"v_mfma_f32_16x16x32_bf16 v[40:43],a[68:71],a[20:23],v[40:43]\n"
"buffer_load_dwordx4 v115,s[32:35],s54 offen sc1 lds\n"
"v_mfma_f32_16x16x32_bf16 v[40:43],a[64:67],a[16:19],v[40:43]\n"
"s_cmp_eq_u32 s64,s65\n"
"v_exp_f32 v122,v8\n"
"v_mfma_f32_16x16x32_bf16 v[48:51],a[64:67],a[32:35],v[104:107]\n"
"v_cvt_pk_bf16_f32 v67,v119,v120\n"
"s_cselect_b32 s64,s66,s64\n"
"v_mfma_f32_16x16x32_bf16 v[48:51],a[68:71],a[36:39],v[48:51]\n"
"s_add_u32 m0,s67,32768\n"
"v_exp_f32 v123,v9\n"
"v_mfma_f32_16x16x32_bf16 v[48:51],a[72:75],a[40:43],v[48:51]\n"
"buffer_load_dwordx4 v112,s[24:27],s64 offen offset:0 sc1 lds\n"
"v_mfma_f32_16x16x32_bf16 v[48:51],a[76:79],a[44:47],v[48:51]\n"
"v_exp_f32 v119,v10\n"
"v_mfma_f32_16x16x32_bf16 v[52:55],a[92:95],a[44:47],v[104:107]\n"
"v_cvt_pk_bf16_f32 v68,v122,v123\n"
"v_or3_b32 v118,v118,v67,v68\n"
"v_mfma_f32_16x16x32_bf16 v[52:55],a[88:91],a[40:43],v[52:55]\n"
"v_exp_f32 v120,v11\n"
"v_mfma_f32_16x16x32_bf16 v[52:55],a[84:87],a[36:39],v[52:55]\n"
"buffer_load_dwordx4 v112,s[24:27],s64 offen offset:1024 sc1 lds\n"
"v_mfma_f32_16x16x32_bf16 v[52:55],a[80:83],a[32:35],v[52:55]\n"
"v_exp_f32 v122,v12\n"
"v_mfma_f32_16x16x32_bf16 v[60:63],a[80:83],a[48:51],v[108:111]\n"
"v_cvt_pk_bf16_f32 v69,v119,v120\n"
"v_mfma_f32_16x16x32_bf16 v[60:63],a[84:87],a[52:55],v[60:63]\n"
"v_exp_f32 v123,v13\n"
"v_mfma_f32_16x16x32_bf16 v[60:63],a[88:91],a[56:59],v[60:63]\n"
"buffer_load_dwordx4 v112,s[24:27],s64 offen offset:2048 sc1 lds\n"
"v_mfma_f32_16x16x32_bf16 v[60:63],a[92:95],a[60:63],v[60:63]\n"
"v_exp_f32 v119,v14\n"
"v_mfma_f32_16x16x32_bf16 v[56:59],a[76:79],a[60:63],v[108:111]\n"
"v_cvt_pk_bf16_f32 v70,v122,v123\n"
"v_or3_b32 v118,v118,v69,v70\n"
"v_mfma_f32_16x16x32_bf16 v[56:59],a[72:75],a[56:59],v[56:59]\n"
"v_exp_f32 v120,v15\n"
"v_mfma_f32_16x16x32_bf16 v[56:59],a[68:71],a[52:55],v[56:59]\n"
"buffer_load_dwordx4 v112,s[24:27],s64 offen offset:3072 sc1 lds\n"
"v_mfma_f32_16x16x32_bf16 v[56:59],a[64:67],a[48:51],v[56:59]\n"
"s_waitcnt lgkmcnt(0)\n"
"v_exp_f32 v122,v16\n"
"v_mfma_f32_16x16x32_bf16 v[128:131],a[96:99],v[80:83],v[128:131]\n"
"v_exp_f32 v123,v17\n"
"v_mfma_f32_16x16x32_bf16 v[160:163],a[96:99],v[84:87],v[160:163]\n"
"ds_read_b128 a[64:67],v113 offset:0\n"
"v_cvt_pk_bf16_f32 v71,v119,v120\n"
"v_mfma_f32_16x16x32_bf16 v[192:195],a[96:99],v[88:91],v[192:195]\n"
"s_add_u32 m0,s67,36864\n"
"v_exp_f32 v119,v18\n"
"v_mfma_f32_16x16x32_bf16 v[224:227],a[96:99],v[92:95],v[224:227]\n"
"ds_read_b128 a[68:71],v113 offset:1024\n"
"v_cvt_pk_bf16_f32 v72,v122,v123\n"
"v_mfma_f32_16x16x32_bf16 a[140:143],a[144:147],v[92:95],a[140:143]\n"
"v_exp_f32 v120,v19\n"
"v_mfma_f32_16x16x32_bf16 v[228:231],a[100:103],v[92:95],v[228:231]\n"
"ds_read_b128 a[72:75],v113 offset:2048\n"
"ds_read_b128 a[76:79],v113 offset:3072\n"
"v_mfma_f32_16x16x32_bf16 v[132:135],a[100:103],v[80:83],v[132:135]\n"
"v_exp_f32 v122,v20\n"
"v_mfma_f32_16x16x32_bf16 v[164:167],a[100:103],v[84:87],v[164:167]\n"
"ds_read_b128 a[80:83],v113 offset:4096\n"
"v_cvt_pk_bf16_f32 v73,v119,v120\n"
"v_mfma_f32_16x16x32_bf16 v[196:199],a[100:103],v[88:91],v[196:199]\n"
"v_exp_f32 v123,v21\n"
"v_mfma_f32_16x16x32_bf16 a[136:139],a[144:147],v[88:91],a[136:139]\n"
"ds_read_b128 a[84:87],v113 offset:5120\n"
"ds_read_b128 a[88:91],v113 offset:6144\n"
"v_mfma_f32_16x16x32_bf16 v[200:203],a[104:107],v[88:91],v[200:203]\n"
"v_exp_f32 v119,v22\n"
"v_mfma_f32_16x16x32_bf16 v[232:235],a[104:107],v[92:95],v[232:235]\n"
"ds_read_b128 a[92:95],v113 offset:7168\n"
"v_cvt_pk_bf16_f32 v74,v122,v123\n"
"v_mfma_f32_16x16x32_bf16 v[136:139],a[104:107],v[80:83],v[136:139]\n"
"v_exp_f32 v120,v23\n"
"v_mfma_f32_16x16x32_bf16 v[168:171],a[104:107],v[84:87],v[168:171]\n"
"s_waitcnt vmcnt(16)\n"
"ds_add_u32 v124,a152\n"
"v_mfma_f32_16x16x32_bf16 a[132:135],a[144:147],v[84:87],a[132:135]\n"
"v_exp_f32 v122,v24\n"
"v_mfma_f32_16x16x32_bf16 v[172:175],a[108:111],v[84:87],v[172:175]\n"
"v_cvt_pk_bf16_f32 v75,v119,v120\n"
"v_or3_b32 v118,v118,v71,v72\n"
"v_mfma_f32_16x16x32_bf16 v[204:207],a[108:111],v[88:91],v[204:207]\n"
"v_exp_f32 v123,v25\n"
"v_mfma_f32_16x16x32_bf16 v[236:239],a[108:111],v[92:95],v[236:239]\n"
"buffer_load_dwordx4 v117,s[24:27],s64 offen offset:0 sc1 lds\n"
"v_mfma_f32_16x16x32_bf16 v[140:143],a[108:111],v[80:83],v[140:143]\n"
"v_exp_f32 v119,v26\n"
"v_mfma_f32_16x16x32_bf16 a[128:131],a[144:147],v[80:83],a[128:131]\n"
"v_cvt_pk_bf16_f32 v76,v122,v123\n"
"v_or3_b32 v118,v118,v73,v74\n"
"v_mfma_f32_16x16x32_bf16 v[144:147],a[112:115],v[80:83],v[144:147]\n"
"v_exp_f32 v120,v27\n"
"v_mfma_f32_16x16x32_bf16 v[176:179],a[112:115],v[84:87],v[176:179]\n"
"buffer_load_dwordx4 v117,s[24:27],s64 offen offset:1024 sc1 lds\n"
"v_mfma_f32_16x16x32_bf16 v[208:211],a[112:115],v[88:91],v[208:211]\n"
"v_exp_f32 v122,v28\n"
"v_mfma_f32_16x16x32_bf16 v[240:243],a[112:115],v[92:95],v[240:243]\n"
"v_cvt_pk_bf16_f32 v77,v119,v120\n"
"v_or3_b32 v118,v118,v75,v76\n"
"v_mfma_f32_16x16x32_bf16 v[244:247],a[116:119],v[92:95],v[244:247]\n"
"v_exp_f32 v123,v29\n"
"v_mfma_f32_16x16x32_bf16 v[148:151],a[116:119],v[80:83],v[148:151]\n"
"buffer_load_dwordx4 v117,s[24:27],s64 offen offset:2048 sc1 lds\n"
"v_mfma_f32_16x16x32_bf16 v[180:183],a[116:119],v[84:87],v[180:183]\n"
"v_exp_f32 v119,v30\n"
"v_mfma_f32_16x16x32_bf16 v[212:215],a[116:119],v[88:91],v[212:215]\n"
"ds_read_b32 v125,v124\n"
"v_cvt_pk_bf16_f32 v78,v122,v123\n"
"v_mfma_f32_16x16x32_bf16 v[216:219],a[120:123],v[88:91],v[216:219]\n"
"v_exp_f32 v120,v31\n"
"v_mfma_f32_16x16x32_bf16 v[248:251],a[120:123],v[92:95],v[248:251]\n"
"buffer_load_dwordx4 v117,s[24:27],s64 offen offset:3072 sc1 lds\n"
"v_mfma_f32_16x16x32_bf16 v[152:155],a[120:123],v[80:83],v[152:155]\n"
"buffer_store_dwordx4 a[252:255],v112,s[24:27],s56 offen offset:3072 sc1\n"
"v_mfma_f32_16x16x32_bf16 v[184:187],a[120:123],v[84:87],v[184:187]\n"
"s_add_u32 s64,s64,16384\n"
"s_add_u32 s89,s89,1\n"
"v_cvt_pk_bf16_f32 v79,v119,v120\n"
"v_mfma_f32_16x16x32_bf16 v[188:191],a[124:127],v[84:87],v[188:191]\n"
"s_waitcnt lgkmcnt(0)\n"
"v_subrev_u32 v125,s101,v125\n"
"v_or3_b32 v118,v118,v77,v78\n"
"v_mfma_f32_16x16x32_bf16 v[220:223],a[124:127],v[88:91],v[220:223]\n"
"v_or3_b32 v125,v118,v79,v125\n"
"v_and_b32 v125,0x40004000,v125\n"
"v_mfma_f32_16x16x32_bf16 v[252:255],a[124:127],v[92:95],v[252:255]\n"
"v_cmp_ne_u32 vcc,0,v125\n"
"v_mfma_f32_16x16x32_bf16 v[156:159],a[124:127],v[80:83],v[156:159]\n"
"s_cbranch_vccnz .Lfk_cs_g2\n"
".Lfk_cr_g2:\n"
".Lfk_Hx:\n"
"s_cmp_lt_u32 s89,8\n"
"s_cbranch_scc1 .Lfk_H\n"
"v_max3_f32 v118,v32,v33,v34\n"
"v_max3_f32 v119,v35,v36,v37\n"
"v_max3_f32 v119,v119,v38,v39\n"
"v_max3_f32 v118,v118,v40,v41\n"
"v_max3_f32 v119,v119,v42,v43\n"
"v_max3_f32 v118,v118,v44,v45\n"
"v_max3_f32 v119,v119,v46,v47\n"
"v_max3_f32 v118,v118,v48,v49\n"
"v_max3_f32 v119,v119,v50,v51\n"
"v_max3_f32 v118,v118,v52,v53\n"
"v_max3_f32 v119,v119,v54,v55\n"
"v_max3_f32 v118,v118,v56,v57\n"
"v_max3_f32 v119,v119,v58,v59\n"
"v_max3_f32 v118,v118,v60,v61\n"
"v_max3_f32 v119,v119,v62,v63\n"
"v_max_f32 v118,v118,v119\n"
"v_cmp_lt_f32 vcc,s93,v118\n"
"s_nop 0\n"
"s_cbranch_vccnz .Lfk_cs_x1\n"
".Lfk_cr_x1:\n"
"s_cmp_ge_u32 s52,2\n"
"s_cbranch_scc1 .Lfk_tailB\n"
".Lfk_tailA:\n"
"s_add_u32 s82,s66,28672\n"
"s_cmp_eq_u32 s52,0\n"
"s_cselect_b32 s67,77824,s67\n"
"s_cselect_b32 s64,s82,s64\n"
"s_waitcnt vmcnt(11) lgkmcnt(0)\n"
"s_barrier\n"
"v_mfma_f32_16x16x32_bf16 v[16:19],a[64:67],a[32:35],v[104:107]\n"
"ds_read_b128 a[96:99],v114 offset:24576\n"
"ds_read_b128 a[100:103],v114 offset:25600\n"
"ds_read_b128 a[104:107],v114 offset:26624\n"
"v_mfma_f32_16x16x32_bf16 v[16:19],a[68:71],a[36:39],v[16:19]\n"
"ds_read_b128 a[108:111],v114 offset:27648\n"
"ds_read_b128 a[112:115],v114 offset:28672\n"
"ds_read_b128 a[116:119],v114 offset:29696\n"
"v_mfma_f32_16x16x32_bf16 v[16:19],a[72:75],a[40:43],v[16:19]\n"
"ds_read_b128 a[120:123],v114 offset:30720\n"
"ds_read_b128 a[124:127],v114 offset:31744\n"
"v_add_f32 v8,0x80000000,v126\n"
"v_mfma_f32_16x16x32_bf16 v[16:19],a[76:79],a[44:47],v[16:19]\n"
"v_min_f32 v8,v8,v100\n"
"v_add_f32 v9,0xf149f2ca,v126\n"
"v_min_f32 v9,v9,v101\n"
"v_mfma_f32_16x16x32_bf16 v[20:23],a[92:95],a[44:47],v[104:107]\n"
"v_add_f32 v10,0xf1c9f2ca,v126\n"
"v_min_f32 v10,v10,v102\n"
"v_add_f32 v11,0xf2177617,v126\n"
"v_mfma_f32_16x16x32_bf16 v[20:23],a[88:91],a[40:43],v[20:23]\n"
"v_min_f32 v11,v11,v103\n"
"v_add_f32 v12,0xf349f2ca,v126\n"
"v_min_f32 v12,v12,v100\n"
"v_mfma_f32_16x16x32_bf16 v[20:23],a[84:87],a[36:39],v[20:23]\n"
"v_add_f32 v13,0xf35691f6,v126\n"
"v_min_f32 v13,v13,v101\n"
"v_add_f32 v14,0xf3633123,v126\n"
"v_mfma_f32_16x16x32_bf16 v[20:23],a[80:83],a[32:35],v[20:23]\n"
"v_min_f32 v14,v14,v102\n"
"v_add_f32 v15,0xf36fd050,v126\n"
"v_min_f32 v15,v15,v103\n"
"v_mfma_f32_16x16x32_bf16 v[28:31],a[80:83],a[48:51],v[108:111]\n"
"s_add_u32 m0,s67,0\n"
"v_exp_f32 v32,v32\n"
"v_mfma_f32_16x16x32_bf16 v[28:31],a[84:87],a[52:55],v[28:31]\n"
"buffer_load_dwordx4 v112,s[24:27],s64 offen offset:0 sc1 lds\n"
"buffer_load_dwordx4 v112,s[24:27],s64 offen offset:1024 sc1 lds\n"
"v_exp_f32 v33,v33\n"
"v_mfma_f32_16x16x32_bf16 v[28:31],a[88:91],a[56:59],v[28:31]\n"
"v_exp_f32 v34,v34\n"
"v_cvt_pk_bf16_f32 v80,v32,v33\n"
"v_mfma_f32_16x16x32_bf16 v[28:31],a[92:95],a[60:63],v[28:31]\n"
"v_exp_f32 v35,v35\n"
"v_max3_f32 v118,v16,v17,v18\n"
"v_exp_f32 v36,v36\n"
"v_mfma_f32_16x16x32_bf16 v[24:27],a[76:79],a[60:63],v[108:111]\n"
"buffer_load_dwordx4 v112,s[24:27],s64 offen offset:2048 sc1 lds\n"
"v_mfma_f32_16x16x32_bf16 v[24:27],a[72:75],a[56:59],v[24:27]\n"
"v_exp_f32 v37,v37\n"
"v_cvt_pk_bf16_f32 v81,v34,v35\n"
"v_mfma_f32_16x16x32_bf16 v[24:27],a[68:71],a[52:55],v[24:27]\n"
"v_exp_f32 v38,v38\n"
"v_cvt_pk_bf16_f32 v82,v36,v37\n"
"v_exp_f32 v39,v39\n"
"v_mfma_f32_16x16x32_bf16 v[24:27],a[64:67],a[48:51],v[24:27]\n"
"buffer_load_dwordx4 v112,s[24:27],s64 offen offset:3072 sc1 lds\n"
"v_mfma_f32_16x16x32_bf16 v[8:11],a[64:67],a[16:19],v[8:11]\n"
"s_add_u32 s64,s64,16384\n"
"v_exp_f32 v40,v40\n"
"v_accvgpr_read_b32 v119,a150\n"
"v_mfma_f32_16x16x32_bf16 v[8:11],a[68:71],a[20:23],v[8:11]\n"
"v_exp_f32 v41,v41\n"
"v_accvgpr_read_b32 v120,a151\n"
"v_mfma_f32_16x16x32_bf16 v[8:11],a[72:75],a[24:27],v[8:11]\n"
"v_exp_f32 v42,v42\n"
"v_xor_b32 v119,s80,v119\n"
"v_mfma_f32_16x16x32_bf16 v[8:11],a[76:79],a[28:31],v[8:11]\n"
"v_exp_f32 v43,v43\n"
"v_xor_b32 v120,s81,v120\n"
"v_mfma_f32_16x16x32_bf16 v[12:15],a[92:95],a[28:31],v[12:15]\n"
"v_exp_f32 v44,v44\n"
"v_or_b32 v119,v119,v120\n"
"v_mfma_f32_16x16x32_bf16 v[12:15],a[88:91],a[24:27],v[12:15]\n"
"s_add_u32 s18,s8,0\n"
"v_exp_f32 v45,v45\n"
"v_cmp_ne_u32 s[14:15],0,v119\n"
"v_mfma_f32_16x16x32_bf16 v[12:15],a[84:87],a[20:23],v[12:15]\n"
"s_cmp_lg_u32 s14,0\n"
"s_cselect_b32 s18,2146435072,s18\n"
"v_exp_f32 v46,v46\n"
"v_mfma_f32_16x16x32_bf16 v[12:15],a[80:83],a[16:19],v[12:15]\n"
"s_add_u32 m0,s30,0\n"
"ds_read_b128 a[64:67],v113 offset:16384\n"
"s_waitcnt lgkmcnt(1)\n"
"v_mfma_f32_16x16x32_bf16 v[128:131],a[96:99],v[64:67],v[128:131]\n"
"ds_read_b128 a[68:71],v113 offset:17408\n"
"ds_read_b128 a[72:75],v113 offset:18432\n"
"ds_read_b128 a[76:79],v113 offset:19456\n"
"v_mfma_f32_16x16x32_bf16 v[160:163],a[96:99],v[68:71],v[160:163]\n"
"ds_read_b128 a[80:83],v113 offset:20480\n"
"v_exp_f32 v47,v47\n"
"v_mfma_f32_16x16x32_bf16 v[192:195],a[96:99],v[72:75],v[192:195]\n"
"ds_read_b128 a[84:87],v113 offset:21504\n"
"ds_read_b128 a[88:91],v113 offset:22528\n"
"ds_read_b128 a[92:95],v113 offset:23552\n"
"v_exp_f32 v48,v48\n"
"v_mfma_f32_16x16x32_bf16 v[224:227],a[96:99],v[76:79],v[224:227]\n"
"buffer_load_dwordx4 v112,s[24:27],s18 offen offset:0 sc1 lds\n"
"v_exp_f32 v49,v49\n"
"v_mfma_f32_16x16x32_bf16 a[140:143],a[144:147],v[76:79],a[140:143]\n"
"buffer_load_dwordx4 v112,s[24:27],s18 offen offset:1024 sc1 lds\n"
"v_mfma_f32_16x16x32_bf16 v[228:231],a[100:103],v[76:79],v[228:231]\n"
"buffer_load_dwordx4 v112,s[24:27],s18 offen offset:2048 sc1 lds\n"
"buffer_load_dwordx4 v112,s[24:27],s18 offen offset:3072 sc1 lds\n"
"v_mfma_f32_16x16x32_bf16 v[132:135],a[100:103],v[64:67],v[132:135]\n"
"s_add_u32 m0,m0,4096\n"
"v_exp_f32 v50,v50\n"
"v_cvt_pk_bf16_f32 v83,v38,v39\n"
"v_mfma_f32_16x16x32_bf16 v[164:167],a[100:103],v[68:71],v[164:167]\n"
"buffer_load_dwordx4 v117,s[24:27],s18 offen offset:0 sc1 lds\n"
"v_cvt_pk_bf16_f32 v84,v40,v41\n"
"v_cvt_pk_bf16_f32 v85,v42,v43\n"
"v_mfma_f32_16x16x32_bf16 v[196:199],a[100:103],v[72:75],v[196:199]\n"
"v_exp_f32 v51,v51\n"
"v_cvt_pk_bf16_f32 v86,v44,v45\n"
"v_mfma_f32_16x16x32_bf16 a[136:139],a[144:147],v[72:75],a[136:139]\n"
"v_exp_f32 v52,v52\n"
"v_cvt_pk_bf16_f32 v87,v46,v47\n"
"v_mfma_f32_16x16x32_bf16 v[200:203],a[104:107],v[72:75],v[200:203]\n"
"buffer_load_dwordx4 v117,s[24:27],s18 offen offset:1024 sc1 lds\n"
"v_cvt_pk_bf16_f32 v88,v48,v49\n"
"v_cvt_pk_bf16_f32 v89,v50,v51\n"
"v_mfma_f32_16x16x32_bf16 v[232:235],a[104:107],v[76:79],v[232:235]\n"
"v_exp_f32 v53,v53\n"
"v_max3_f32 v118,v118,v19,v20\n"
"v_mfma_f32_16x16x32_bf16 v[136:139],a[104:107],v[64:67],v[136:139]\n"
"v_cvt_pk_bf16_f32 v90,v52,v53\n"
"v_max3_f32 v118,v118,v21,v22\n"
"v_max3_f32 v118,v118,v23,v28\n"
"v_exp_f32 v54,v54\n"
"v_mfma_f32_16x16x32_bf16 v[168:171],a[104:107],v[68:71],v[168:171]\n"
"buffer_load_dwordx4 v117,s[24:27],s18 offen offset:2048 sc1 lds\n"
"v_mfma_f32_16x16x32_bf16 a[132:135],a[144:147],v[68:71],a[132:135]\n"
"v_exp_f32 v55,v55\n"
"v_max3_f32 v118,v118,v29,v30\n"
"v_mfma_f32_16x16x32_bf16 v[172:175],a[108:111],v[68:71],v[172:175]\n"
"v_cvt_pk_bf16_f32 v91,v54,v55\n"
"v_max3_f32 v118,v118,v31,v24\n"
"v_max3_f32 v118,v118,v25,v26\n"
"v_exp_f32 v56,v56\n"
"v_mfma_f32_16x16x32_bf16 v[204:207],a[108:111],v[72:75],v[204:207]\n"
"buffer_load_dwordx4 v117,s[24:27],s18 offen offset:3072 sc1 lds\n"
"v_mfma_f32_16x16x32_bf16 v[236:239],a[108:111],v[76:79],v[236:239]\n"
"s_sub_u32 s82,s3,32768\n"
"v_max3_f32 v118,v118,v27,v8\n"
"v_max3_f32 v118,v118,v9,v10\n"
"v_mfma_f32_16x16x32_bf16 v[140:143],a[108:111],v[64:67],v[140:143]\n"
"v_exp_f32 v57,v57\n"
"v_max3_f32 v118,v118,v11,v12\n"
"v_exp_f32 v58,v58\n"
"v_mfma_f32_16x16x32_bf16 a[128:131],a[144:147],v[64:67],a[128:131]\n"
"buffer_load_dwordx4 a[0:3],v112,s[24:27],s82 offen offset:0 sc1\n"
"v_mfma_f32_16x16x32_bf16 v[144:147],a[112:115],v[64:67],v[144:147]\n"
"v_max3_f32 v118,v118,v13,v14\n"
"v_max_f32 v118,v118,v15\n"
"v_mfma_f32_16x16x32_bf16 v[176:179],a[112:115],v[68:71],v[176:179]\n"
"v_exp_f32 v59,v59\n"
"v_mfma_f32_16x16x32_bf16 v[208:211],a[112:115],v[72:75],v[208:211]\n"
"v_cvt_pk_bf16_f32 v92,v56,v57\n"
"v_cvt_pk_bf16_f32 v93,v58,v59\n"
"v_exp_f32 v60,v60\n"
"v_mfma_f32_16x16x32_bf16 v[240:243],a[112:115],v[76:79],v[240:243]\n"
"buffer_load_dwordx4 a[4:7],v112,s[24:27],s82 offen offset:1024 sc1\n"
"v_mfma_f32_16x16x32_bf16 v[244:247],a[116:119],v[76:79],v[244:247]\n"
"v_exp_f32 v61,v61\n"
"v_mfma_f32_16x16x32_bf16 v[148:151],a[116:119],v[64:67],v[148:151]\n"
"v_cmp_lt_f32 vcc,s93,v118\n"
"v_cvt_pk_bf16_f32 v94,v60,v61\n"
"v_mfma_f32_16x16x32_bf16 v[180:183],a[116:119],v[68:71],v[180:183]\n"
"buffer_load_dwordx4 a[8:11],v112,s[24:27],s82 offen offset:2048 sc1\n"
"v_mfma_f32_16x16x32_bf16 v[212:215],a[116:119],v[72:75],v[212:215]\n"
"v_exp_f32 v62,v62\n"
"v_mfma_f32_16x16x32_bf16 v[216:219],a[120:123],v[72:75],v[216:219]\n"
"v_exp_f32 v63,v63\n"
"v_mfma_f32_16x16x32_bf16 v[248:251],a[120:123],v[76:79],v[248:251]\n"
"v_cvt_pk_bf16_f32 v95,v62,v63\n"
"v_mfma_f32_16x16x32_bf16 v[152:155],a[120:123],v[64:67],v[152:155]\n"
"buffer_load_dwordx4 a[12:15],v112,s[24:27],s82 offen offset:3072 sc1\n"
"v_mfma_f32_16x16x32_bf16 v[184:187],a[120:123],v[68:71],v[184:187]\n"
"v_mfma_f32_16x16x32_bf16 v[188:191],a[124:127],v[68:71],v[188:191]\n"
"v_mfma_f32_16x16x32_bf16 v[220:223],a[124:127],v[72:75],v[220:223]\n"
"v_mfma_f32_16x16x32_bf16 v[252:255],a[124:127],v[76:79],v[252:255]\n"
"v_mfma_f32_16x16x32_bf16 v[156:159],a[124:127],v[64:67],v[156:159]\n"
"s_cbranch_vccnz .Lfk_cs_a3\n"
".Lfk_cr_a3:\n"
"s_waitcnt vmcnt(17) lgkmcnt(0)\n"
"s_barrier\n"
"v_mfma_f32_16x16x32_bf16 v[56:59],a[64:67],a[48:51],v[108:111]\n"
"ds_read_b128 a[96:99],v114 offset:57344\n"
"ds_read_b128 a[100:103],v114 offset:58368\n"
"ds_read_b128 a[104:107],v114 offset:59392\n"
"v_mfma_f32_16x16x32_bf16 v[56:59],a[68:71],a[52:55],v[56:59]\n"
"ds_read_b128 a[108:111],v114 offset:60416\n"
"ds_read_b128 a[112:115],v114 offset:61440\n"
"ds_read_b128 a[116:119],v114 offset:62464\n"
"v_mfma_f32_16x16x32_bf16 v[56:59],a[72:75],a[56:59],v[56:59]\n"
"ds_read_b128 a[120:123],v114 offset:63488\n"
"ds_read_b128 a[124:127],v114 offset:64512\n"
"v_add_f32 v48,0x80000000,v126\n"
"v_mfma_f32_16x16x32_bf16 v[56:59],a[76:79],a[60:63],v[56:59]\n"
"v_min_f32 v48,v48,v104\n"
"v_add_f32 v49,0xf149f2ca,v126\n"
"v_min_f32 v49,v49,v105\n"
"v_mfma_f32_16x16x32_bf16 v[60:63],a[92:95],a[60:63],v[108:111]\n"
"v_add_f32 v50,0xf1c9f2ca,v126\n"
"v_add_f32 v51,0xf2177617,v126\n"
"v_mfma_f32_16x16x32_bf16 v[60:63],a[88:91],a[56:59],v[60:63]\n"
"v_add_f32 v52,0xf349f2ca,v126\n"
"v_min_f32 v52,v52,v104\n"
"v_mfma_f32_16x16x32_bf16 v[60:63],a[84:87],a[52:55],v[60:63]\n"
"v_add_f32 v53,0xf35691f6,v126\n"
"v_min_f32 v53,v53,v105\n"
"v_add_f32 v54,0xf3633123,v126\n"
"v_mfma_f32_16x16x32_bf16 v[60:63],a[80:83],a[48:51],v[60:63]\n"
"v_min_f32 v54,v54,v106\n"
"v_add_f32 v55,0xf36fd050,v126\n"
"v_min_f32 v55,v55,v107\n"
"v_min_f32 v51,v51,v107\n"
"v_min_f32 v50,v50,v106\n"
"v_mfma_f32_16x16x32_bf16 v[52:55],a[80:83],a[32:35],v[52:55]\n"
"s_add_u32 m0,s67,16384\n"
"v_exp_f32 v8,v8\n"
"v_mfma_f32_16x16x32_bf16 v[52:55],a[84:87],a[36:39],v[52:55]\n"
"buffer_load_dwordx4 v112,s[24:27],s64 offen offset:0 sc1 lds\n"
"buffer_load_dwordx4 v112,s[24:27],s64 offen offset:1024 sc1 lds\n"
"v_exp_f32 v9,v9\n"
"v_mfma_f32_16x16x32_bf16 v[52:55],a[88:91],a[40:43],v[52:55]\n"
"v_exp_f32 v10,v10\n"
"v_cvt_pk_bf16_f32 v68,v8,v9\n"
"v_mfma_f32_16x16x32_bf16 v[52:55],a[92:95],a[44:47],v[52:55]\n"
"v_exp_f32 v11,v11\n"
"v_max3_f32 v118,v56,v57,v58\n"
"v_exp_f32 v12,v12\n"
"v_mfma_f32_16x16x32_bf16 v[48:51],a[76:79],a[44:47],v[48:51]\n"
"buffer_load_dwordx4 v112,s[24:27],s64 offen offset:2048 sc1 lds\n"
"v_mfma_f32_16x16x32_bf16 v[48:51],a[72:75],a[40:43],v[48:51]\n"
"v_exp_f32 v13,v13\n"
"v_cvt_pk_bf16_f32 v69,v10,v11\n"
"v_mfma_f32_16x16x32_bf16 v[48:51],a[68:71],a[36:39],v[48:51]\n"
"v_exp_f32 v14,v14\n"
"v_cvt_pk_bf16_f32 v70,v12,v13\n"
"v_exp_f32 v15,v15\n"
"v_mfma_f32_16x16x32_bf16 v[48:51],a[64:67],a[32:35],v[48:51]\n"
"ds_read_b128 a[64:67],v113 offset:49152\n"
"s_waitcnt lgkmcnt(1)\n"
"v_mfma_f32_16x16x32_bf16 v[128:131],a[96:99],v[80:83],v[128:131]\n"
"ds_read_b128 a[68:71],v113 offset:50176\n"
"v_exp_f32 v16,v16\n"
"v_mfma_f32_16x16x32_bf16 v[160:163],a[96:99],v[84:87],v[160:163]\n"
"ds_read_b128 a[72:75],v113 offset:51200\n"
"v_exp_f32 v17,v17\n"
"v_mfma_f32_16x16x32_bf16 v[192:195],a[96:99],v[88:91],v[192:195]\n"
"ds_read_b128 a[76:79],v113 offset:52224\n"
"ds_read_b128 a[80:83],v113 offset:53248\n"
"ds_read_b128 a[84:87],v113 offset:54272\n"
"v_mfma_f32_16x16x32_bf16 v[224:227],a[96:99],v[92:95],v[224:227]\n"
"ds_read_b128 a[88:91],v113 offset:55296\n"
"v_exp_f32 v18,v18\n"
"v_mfma_f32_16x16x32_bf16 a[140:143],a[144:147],v[92:95],a[140:143]\n"
"ds_read_b128 a[92:95],v113 offset:56320\n"
"buffer_load_dwordx4 v112,s[24:27],s64 offen offset:3072 sc1 lds\n"
"s_add_u32 s64,s64,16384\n"
"v_mfma_f32_16x16x32_bf16 v[228:231],a[100:103],v[92:95],v[228:231]\n"
"s_sub_u32 s82,s3,49152\n"
"v_exp_f32 v19,v19\n"
"v_mfma_f32_16x16x32_bf16 v[132:135],a[100:103],v[80:83],v[132:135]\n"
"buffer_load_dwordx4 a[16:19],v112,s[24:27],s82 offen offset:0 sc1\n"
"v_cvt_pk_bf16_f32 v71,v14,v15\n"
"v_cvt_pk_bf16_f32 v72,v16,v17\n"
"v_cvt_pk_bf16_f32 v73,v18,v19\n"
"v_exp_f32 v20,v20\n"
"v_mfma_f32_16x16x32_bf16 v[164:167],a[100:103],v[84:87],v[164:167]\n"
"buffer_load_dwordx4 a[20:23],v112,s[24:27],s82 offen offset:1024 sc1\n"
"v_mfma_f32_16x16x32_bf16 v[196:199],a[100:103],v[88:91],v[196:199]\n"
"v_exp_f32 v21,v21\n"
"v_max3_f32 v118,v118,v59,v60\n"
"v_mfma_f32_16x16x32_bf16 a[136:139],a[144:147],v[88:91],a[136:139]\n"
"v_cvt_pk_bf16_f32 v74,v20,v21\n"
"v_max3_f32 v118,v118,v61,v62\n"
"v_max3_f32 v118,v118,v63,v52\n"
"v_mfma_f32_16x16x32_bf16 v[200:203],a[104:107],v[88:91],v[200:203]\n"
"v_exp_f32 v22,v22\n"
"v_max3_f32 v118,v118,v53,v54\n"
"v_mfma_f32_16x16x32_bf16 v[232:235],a[104:107],v[92:95],v[232:235]\n"
"buffer_load_dwordx4 a[24:27],v112,s[24:27],s82 offen offset:2048 sc1\n"
"v_max3_f32 v118,v118,v55,v48\n"
"v_max3_f32 v118,v118,v49,v50\n"
"v_mfma_f32_16x16x32_bf16 v[136:139],a[104:107],v[80:83],v[136:139]\n"
"v_exp_f32 v23,v23\n"
"v_max_f32 v118,v118,v51\n"
"v_mfma_f32_16x16x32_bf16 v[168:171],a[104:107],v[84:87],v[168:171]\n"
"v_cvt_pk_bf16_f32 v75,v22,v23\n"
"v_cmp_lt_f32 vcc,s93,v118\n"
"v_mfma_f32_16x16x32_bf16 a[132:135],a[144:147],v[84:87],a[132:135]\n"
"buffer_load_dwordx4 a[28:31],v112,s[24:27],s82 offen offset:3072 sc1\n"
"v_mfma_f32_16x16x32_bf16 v[172:175],a[108:111],v[84:87],v[172:175]\n"
"v_exp_f32 v24,v24\n"
"v_mfma_f32_16x16x32_bf16 v[204:207],a[108:111],v[88:91],v[204:207]\n"
"s_sub_u32 s82,s3,65536\n"
"v_exp_f32 v25,v25\n"
"v_mfma_f32_16x16x32_bf16 v[236:239],a[108:111],v[92:95],v[236:239]\n"
"v_cvt_pk_bf16_f32 v76,v24,v25\n"
"v_mfma_f32_16x16x32_bf16 v[140:143],a[108:111],v[80:83],v[140:143]\n"
"buffer_load_dwordx4 v[32:35],v112,s[24:27],s82 offen offset:0 sc1\n"
"v_mfma_f32_16x16x32_bf16 a[128:131],a[144:147],v[80:83],a[128:131]\n"
"v_exp_f32 v26,v26\n"
"v_mfma_f32_16x16x32_bf16 v[144:147],a[112:115],v[80:83],v[144:147]\n"
"v_exp_f32 v27,v27\n"
"v_mfma_f32_16x16x32_bf16 v[176:179],a[112:115],v[84:87],v[176:179]\n"
"buffer_load_dwordx4 v[36:39],v112,s[24:27],s82 offen offset:1024 sc1\n"
"v_mfma_f32_16x16x32_bf16 v[208:211],a[112:115],v[88:91],v[208:211]\n"
"v_exp_f32 v28,v28\n"
"v_mfma_f32_16x16x32_bf16 v[240:243],a[112:115],v[92:95],v[240:243]\n"
"v_cvt_pk_bf16_f32 v77,v26,v27\n"
"v_mfma_f32_16x16x32_bf16 v[244:247],a[116:119],v[92:95],v[244:247]\n"
"v_exp_f32 v29,v29\n"
"v_mfma_f32_16x16x32_bf16 v[148:151],a[116:119],v[80:83],v[148:151]\n"
"buffer_load_dwordx4 v[40:43],v112,s[24:27],s82 offen offset:2048 sc1\n"
"v_mfma_f32_16x16x32_bf16 v[180:183],a[116:119],v[84:87],v[180:183]\n"
"v_exp_f32 v30,v30\n"
"v_mfma_f32_16x16x32_bf16 v[212:215],a[116:119],v[88:91],v[212:215]\n"
"v_cvt_pk_bf16_f32 v78,v28,v29\n"
"v_mfma_f32_16x16x32_bf16 v[216:219],a[120:123],v[88:91],v[216:219]\n"
"v_exp_f32 v31,v31\n"
"v_mfma_f32_16x16x32_bf16 v[248:251],a[120:123],v[92:95],v[248:251]\n"
"v_cvt_pk_bf16_f32 v79,v30,v31\n"
"v_mfma_f32_16x16x32_bf16 v[152:155],a[120:123],v[80:83],v[152:155]\n"
"buffer_load_dwordx4 v[44:47],v112,s[24:27],s82 offen offset:3072 sc1\n"
"v_mfma_f32_16x16x32_bf16 v[184:187],a[120:123],v[84:87],v[184:187]\n"
"v_mfma_f32_16x16x32_bf16 v[188:191],a[124:127],v[84:87],v[188:191]\n"
"v_mfma_f32_16x16x32_bf16 v[220:223],a[124:127],v[88:91],v[220:223]\n"
"v_mfma_f32_16x16x32_bf16 v[252:255],a[124:127],v[92:95],v[252:255]\n"
"v_mfma_f32_16x16x32_bf16 v[156:159],a[124:127],v[80:83],v[156:159]\n"
"s_cbranch_vccnz .Lfk_cs_a0\n"
".Lfk_cr_a0:\n"
"s_load_dwordx2 s[36:37],s[0:1],0\n"
"s_load_dwordx2 s[44:45],s[0:1],80\n"
"s_load_dwordx2 s[48:49],s[0:1],88\n"
"s_lshl_b32 s82,s78,2\n"
"s_add_u32 s82,s82,65536\n"
"v_add_u32 v113,s82,v112\n"
"s_lshr_b32 s82,s78,2\n"
"s_add_u32 s82,s82,122880\n"
"v_lshrrev_b32 v115,1,v112\n"
"v_add_u32 v116,s82,v115\n"
"s_waitcnt lgkmcnt(0)\n"
"s_barrier\n"
"v_add_f32 v24,0x80000000,v126\n"
"v_min_f32 v24,v24,v108\n"
"v_add_f32 v25,0xf149f2ca,v126\n"
"v_min_f32 v25,v25,v109\n"
"v_add_f32 v26,0xf1c9f2ca,v126\n"
"v_min_f32 v26,v26,v110\n"
"v_add_f32 v27,0xf2177617,v126\n"
"v_min_f32 v27,v27,v111\n"
"v_add_f32 v28,0xf349f2ca,v126\n"
"v_min_f32 v28,v28,v108\n"
"v_add_f32 v29,0xf35691f6,v126\n"
"v_min_f32 v29,v29,v109\n"
"v_add_f32 v30,0xf3633123,v126\n"
"v_min_f32 v30,v30,v110\n"
"v_add_f32 v31,0xf36fd050,v126\n"
"v_min_f32 v31,v31,v111\n"
"v_exp_f32 v48,v48\n"
"v_mfma_f32_16x16x32_bf16 v[24:27],a[64:67],a[48:51],v[24:27]\n"
"s_add_u32 s83,s18,2097152\n"
"ds_read_b128 a[96:99],v114 offset:40960\n"
"ds_read_b128 a[100:103],v114 offset:41984\n"
"v_exp_f32 v49,v49\n"
"v_mfma_f32_16x16x32_bf16 v[24:27],a[68:71],a[52:55],v[24:27]\n"
"s_add_u32 m0,s30,32768\n"
"ds_read_b128 a[104:107],v114 offset:43008\n"
"ds_read_b128 a[108:111],v114 offset:44032\n"
"v_mfma_f32_16x16x32_bf16 v[24:27],a[72:75],a[56:59],v[24:27]\n"
"ds_read_b128 a[112:115],v114 offset:45056\n"
"ds_read_b128 a[116:119],v114 offset:46080\n"
"ds_read_b128 a[120:123],v114 offset:47104\n"
"ds_read_b128 a[124:127],v114 offset:48128\n"
"v_exp_f32 v50,v50\n"
"v_mfma_f32_16x16x32_bf16 v[24:27],a[76:79],a[60:63],v[24:27]\n"
"buffer_load_dwordx4 v112,s[24:27],s83 offen offset:0 sc1 lds\n"
"v_cvt_pk_bf16_f32 v88,v48,v49\n"
"v_mfma_f32_16x16x32_bf16 v[28:31],a[92:95],a[60:63],v[28:31]\n"
"buffer_load_dwordx4 v112,s[24:27],s83 offen offset:1024 sc1 lds\n"
"v_accvgpr_read_b32 v97,a128\n"
"v_cvt_pk_bf16_f32 v128,v128,v129\n"
"v_cvt_pk_bf16_f32 v129,v130,v131\n"
"v_mfma_f32_16x16x32_bf16 v[28:31],a[88:91],a[56:59],v[28:31]\n"
"v_exp_f32 v51,v51\n"
"v_cvt_pk_bf16_f32 v130,v132,v133\n"
"v_cvt_pk_bf16_f32 v131,v134,v135\n"
"v_cvt_pk_bf16_f32 v89,v50,v51\n"
"v_mfma_f32_16x16x32_bf16 v[28:31],a[84:87],a[52:55],v[28:31]\n"
"ds_write_b128 v113,v[128:131] offset:0\n"
"v_cvt_pk_bf16_f32 v136,v136,v137\n"
"v_cvt_pk_bf16_f32 v137,v138,v139\n"
"v_exp_f32 v52,v52\n"
"v_mfma_f32_16x16x32_bf16 v[28:31],a[80:83],a[48:51],v[28:31]\n"
"buffer_load_dwordx4 v112,s[24:27],s83 offen offset:2048 sc1 lds\n"
"v_max3_f32 v118,v24,v25,v26\n"
"s_waitcnt lgkmcnt(1)\n"
"v_mfma_f32_16x16x32_bf16 v[160:163],a[96:99],v[68:71],v[160:163]\n"
"v_cvt_pk_bf16_f32 v138,v140,v141\n"
"v_cvt_pk_bf16_f32 v139,v142,v143\n"
"v_cvt_pk_bf16_f32 v144,v144,v145\n"
"v_mfma_f32_16x16x32_bf16 a[132:135],a[144:147],v[68:71],a[132:135]\n"
"ds_write_b128 v113,v[136:139] offset:1024\n"
"buffer_load_dwordx4 v112,s[24:27],s83 offen offset:3072 sc1 lds\n"
"v_exp_f32 v53,v53\n"
"v_cvt_pk_bf16_f32 v145,v146,v147\n"
"v_mfma_f32_16x16x32_bf16 v[164:167],a[100:103],v[68:71],v[164:167]\n"
"s_add_u32 m0,m0,4096\n"
"v_exp_f32 v54,v54\n"
"v_cvt_pk_bf16_f32 v90,v52,v53\n"
"v_cvt_pk_bf16_f32 v146,v148,v149\n"
"v_mfma_f32_16x16x32_bf16 v[168:171],a[104:107],v[68:71],v[168:171]\n"
"v_max3_f32 v118,v118,v27,v28\n"
"v_max3_f32 v118,v118,v29,v30\n"
"v_max_f32 v118,v118,v31\n"
"v_cmp_lt_f32 vcc,s93,v118\n"
"v_exp_f32 v55,v55\n"
"v_mfma_f32_16x16x32_bf16 v[172:175],a[108:111],v[68:71],v[172:175]\n"
"buffer_load_dwordx4 v117,s[24:27],s83 offen offset:0 sc1 lds\n"
"v_cvt_pk_bf16_f32 v147,v150,v151\n"
"v_cvt_pk_bf16_f32 v91,v54,v55\n"
"v_mfma_f32_16x16x32_bf16 v[176:179],a[112:115],v[68:71],v[176:179]\n"
"ds_write_b128 v113,v[144:147] offset:2048\n"
"v_cvt_pk_bf16_f32 v152,v152,v153\n"
"v_cvt_pk_bf16_f32 v153,v154,v155\n"
"v_exp_f32 v56,v56\n"
"v_mfma_f32_16x16x32_bf16 v[180:183],a[116:119],v[68:71],v[180:183]\n"
"buffer_load_dwordx4 v117,s[24:27],s83 offen offset:1024 sc1 lds\n"
"v_cvt_pk_bf16_f32 v154,v156,v157\n"
"v_cvt_pk_bf16_f32 v155,v158,v159\n"
"v_mfma_f32_16x16x32_bf16 v[184:187],a[120:123],v[68:71],v[184:187]\n"
"ds_write_b128 v113,v[152:155] offset:3072\n"
"ds_write_b64 v116,v[96:97] offset:0\n"
"v_mfma_f32_16x16x32_bf16 v[188:191],a[124:127],v[68:71],v[188:191]\n"
"v_exp_f32 v57,v57\n"
"v_mfma_f32_16x16x32_bf16 v[192:195],a[96:99],v[72:75],v[192:195]\n"
"buffer_load_dwordx4 v117,s[24:27],s83 offen offset:2048 sc1 lds\n"
"v_cvt_pk_bf16_f32 v92,v56,v57\n"
"v_mfma_f32_16x16x32_bf16 a[136:139],a[144:147],v[72:75],a[136:139]\n"
"v_exp_f32 v58,v58\n"
"v_exp_f32 v59,v59\n"
"v_mfma_f32_16x16x32_bf16 v[196:199],a[100:103],v[72:75],v[196:199]\n"
"buffer_load_dwordx4 v117,s[24:27],s83 offen offset:3072 sc1 lds\n"
"v_accvgpr_read_b32 v101,a132\n"
"v_mfma_f32_16x16x32_bf16 v[200:203],a[104:107],v[72:75],v[200:203]\n"
"s_sub_u32 s82,s3,81920\n"
"v_cvt_pk_bf16_f32 v93,v58,v59\n"
"v_cvt_pk_bf16_f32 v160,v160,v161\n"
"v_cvt_pk_bf16_f32 v161,v162,v163\n"
"v_mfma_f32_16x16x32_bf16 v[204:207],a[108:111],v[72:75],v[204:207]\n"
"v_exp_f32 v60,v60\n"
"v_cvt_pk_bf16_f32 v162,v164,v165\n"
"v_cvt_pk_bf16_f32 v163,v166,v167\n"
"v_mfma_f32_16x16x32_bf16 v[208:211],a[112:115],v[72:75],v[208:211]\n"
"buffer_load_dwordx4 a[32:35],v112,s[24:27],s82 offen offset:0 sc1\n"
"ds_write_b128 v113,v[160:163] offset:4096\n"
"v_cvt_pk_bf16_f32 v168,v168,v169\n"
"v_cvt_pk_bf16_f32 v169,v170,v171\n"
"v_mfma_f32_16x16x32_bf16 v[212:215],a[116:119],v[72:75],v[212:215]\n"
"v_exp_f32 v61,v61\n"
"v_cvt_pk_bf16_f32 v170,v172,v173\n"
"v_cvt_pk_bf16_f32 v171,v174,v175\n"
"v_cvt_pk_bf16_f32 v94,v60,v61\n"
"v_mfma_f32_16x16x32_bf16 v[216:219],a[120:123],v[72:75],v[216:219]\n"
"ds_write_b128 v113,v[168:171] offset:5120\n"
"v_cvt_pk_bf16_f32 v176,v176,v177\n"
"v_cvt_pk_bf16_f32 v177,v178,v179\n"
"v_exp_f32 v62,v62\n"
"v_mfma_f32_16x16x32_bf16 v[220:223],a[124:127],v[72:75],v[220:223]\n"
"buffer_load_dwordx4 a[36:39],v112,s[24:27],s82 offen offset:1024 sc1\n"
"v_cvt_pk_bf16_f32 v178,v180,v181\n"
"v_cvt_pk_bf16_f32 v179,v182,v183\n"
"v_mfma_f32_16x16x32_bf16 v[224:227],a[96:99],v[76:79],v[224:227]\n"
"ds_write_b128 v113,v[176:179] offset:6144\n"
"v_cvt_pk_bf16_f32 v184,v184,v185\n"
"v_cvt_pk_bf16_f32 v185,v186,v187\n"
"v_mfma_f32_16x16x32_bf16 a[140:143],a[144:147],v[76:79],a[140:143]\n"
"v_exp_f32 v63,v63\n"
"v_cvt_pk_bf16_f32 v186,v188,v189\n"
"v_cvt_pk_bf16_f32 v187,v190,v191\n"
"v_mfma_f32_16x16x32_bf16 v[228:231],a[100:103],v[76:79],v[228:231]\n"
"buffer_load_dwordx4 a[40:43],v112,s[24:27],s82 offen offset:2048 sc1\n"
"ds_write_b128 v113,v[184:187] offset:7168\n"
"v_mfma_f32_16x16x32_bf16 v[232:235],a[104:107],v[76:79],v[232:235]\n"
"ds_write_b64 v116,v[100:101] offset:512\n"
"v_cvt_pk_bf16_f32 v95,v62,v63\n"
"v_mfma_f32_16x16x32_bf16 v[236:239],a[108:111],v[76:79],v[236:239]\n"
"buffer_load_dwordx4 a[44:47],v112,s[24:27],s82 offen offset:3072 sc1\n"
"v_mfma_f32_16x16x32_bf16 v[240:243],a[112:115],v[76:79],v[240:243]\n"
"s_sub_u32 s82,s3,131072\n"
"buffer_load_dwordx4 v[128:131],v112,s[24:27],s82 offen offset:0 sc1\n"
"buffer_load_dwordx4 v[132:135],v112,s[24:27],s82 offen offset:1024 sc1\n"
"buffer_load_dwordx4 v[136:139],v112,s[24:27],s82 offen offset:2048 sc1\n"
"buffer_load_dwordx4 v[140:143],v112,s[24:27],s82 offen offset:3072 sc1\n"
"v_mfma_f32_16x16x32_bf16 v[244:247],a[116:119],v[76:79],v[244:247]\n"
"s_sub_u32 s82,s3,147456\n"
"buffer_load_dwordx4 v[144:147],v112,s[24:27],s82 offen offset:0 sc1\n"
"buffer_load_dwordx4 v[148:151],v112,s[24:27],s82 offen offset:1024 sc1\n"
"v_mfma_f32_16x16x32_bf16 v[248:251],a[120:123],v[76:79],v[248:251]\n"
"buffer_load_dwordx4 v[152:155],v112,s[24:27],s82 offen offset:2048 sc1\n"
"buffer_load_dwordx4 v[156:159],v112,s[24:27],s82 offen offset:3072 sc1\n"
"v_mfma_f32_16x16x32_bf16 v[252:255],a[124:127],v[76:79],v[252:255]\n"
"s_cbranch_vccnz .Lfk_cs_a1\n"
".Lfk_cr_a1:\n"
"s_waitcnt vmcnt(28) lgkmcnt(0)\n"
"s_barrier\n"
"ds_read_b128 a[96:99],v114 offset:8192\n"
"ds_read_b128 a[100:103],v114 offset:9216\n"
"ds_read_b128 a[104:107],v114 offset:10240\n"
"ds_read_b128 a[108:111],v114 offset:11264\n"
"ds_read_b128 a[112:115],v114 offset:12288\n"
"ds_read_b128 a[116:119],v114 offset:13312\n"
"ds_read_b128 a[120:123],v114 offset:14336\n"
"ds_read_b128 a[124:127],v114 offset:15360\n"
"v_xor_b32 v113,32768,v113\n"
"v_xor_b32 v116,2048,v116\n"
"ds_read_b64 v[98:99],v116 offset:0\n"
"ds_read_b128 a[64:67],v113 offset:0\n"
"ds_read_b128 a[68:71],v113 offset:1024\n"
"ds_read_b128 a[72:75],v113 offset:2048\n"
"ds_read_b128 a[76:79],v113 offset:3072\n"
"ds_read_b64 v[102:103],v116 offset:512\n"
"ds_read_b128 a[80:83],v113 offset:4096\n"
"ds_read_b128 a[84:87],v113 offset:5120\n"
"ds_read_b128 a[88:91],v113 offset:6144\n"
"ds_read_b128 a[92:95],v113 offset:7168\n"
"v_exp_f32 v24,v24\n"
"v_exp_f32 v25,v25\n"
"v_exp_f32 v26,v26\n"
"v_exp_f32 v27,v27\n"
"v_exp_f32 v28,v28\n"
"v_exp_f32 v29,v29\n"
"v_exp_f32 v30,v30\n"
"v_exp_f32 v31,v31\n"
"v_cvt_pk_bf16_f32 v76,v24,v25\n"
"v_cvt_pk_bf16_f32 v77,v26,v27\n"
"v_cvt_pk_bf16_f32 v78,v28,v29\n"
"v_cvt_pk_bf16_f32 v79,v30,v31\n"
"s_waitcnt lgkmcnt(10)\n"
"v_mfma_f32_16x16x32_bf16 a[136:139],a[144:147],v[88:91],a[136:139]\n"
"s_sub_u32 s82,s3,163840\n"
"v_lshrrev_b32 v70,4,v112\n"
"v_and_b32 v71,15,v70\n"
"v_mfma_f32_16x16x32_bf16 a[140:143],a[144:147],v[92:95],a[140:143]\n"
"buffer_load_dwordx4 v[0:3],v112,s[24:27],s82 offen offset:0 sc1\n"
"v_lshrrev_b32 v70,4,v70\n"
"v_lshlrev_b32 v70,2,v70\n"
"v_mfma_f32_16x16x32_bf16 v[192:195],a[96:99],v[88:91],v[192:195]\n"
"buffer_load_dwordx4 v[4:7],v112,s[24:27],s82 offen offset:1024 sc1\n"
"v_sub_u32 v70,v71,v70\n"
"v_mov_b32 v64,0\n"
"v_mfma_f32_16x16x32_bf16 v[224:227],a[96:99],v[92:95],v[224:227]\n"
"ds_read_b128 a[96:99],v114 offset:24576\n"
"buffer_load_dwordx4 v[8:11],v112,s[24:27],s82 offen offset:2048 sc1\n"
"v_mfma_f32_16x16x32_bf16 v[196:199],a[100:103],v[88:91],v[196:199]\n"
"buffer_load_dwordx4 v[12:15],v112,s[24:27],s82 offen offset:3072 sc1\n"
"v_mov_b32 v69,0\n"
"v_mfma_f32_16x16x32_bf16 v[228:231],a[100:103],v[92:95],v[228:231]\n"
"s_sub_u32 s82,s3,180224\n"
"ds_read_b128 a[100:103],v114 offset:25600\n"
"v_mov_b32 v71,0x3f80\n"
"v_mfma_f32_16x16x32_bf16 v[200:203],a[104:107],v[88:91],v[200:203]\n"
"buffer_load_dwordx4 v[160:163],v112,s[24:27],s82 offen offset:0 sc1\n"
"v_cmp_eq_u32 vcc,0,v70\n"
"v_mov_b32 v68,0\n"
"v_mov_b32 v65,0\n"
"v_cndmask_b32 v66,0,v71,vcc\n"
"v_cmp_eq_u32 vcc,1,v70\n"
"v_mfma_f32_16x16x32_bf16 v[232:235],a[104:107],v[92:95],v[232:235]\n"
"ds_read_b128 a[104:107],v114 offset:26624\n"
"buffer_load_dwordx4 v[164:167],v112,s[24:27],s82 offen offset:1024 sc1\n"
"v_mov_b32 v72,0x3f800000\n"
"v_cndmask_b32 v66,v66,v72,vcc\n"
"v_cmp_eq_u32 vcc,2,v70\n"
"v_mfma_f32_16x16x32_bf16 v[204:207],a[108:111],v[88:91],v[204:207]\n"
"buffer_load_dwordx4 v[168:171],v112,s[24:27],s82 offen offset:2048 sc1\n"
"v_cndmask_b32 v67,0,v71,vcc\n"
"v_cmp_eq_u32 vcc,3,v70\n"
"v_mfma_f32_16x16x32_bf16 v[236:239],a[108:111],v[92:95],v[236:239]\n"
"ds_read_b128 a[108:111],v114 offset:27648\n"
"buffer_load_dwordx4 v[172:175],v112,s[24:27],s82 offen offset:3072 sc1\n"
"v_mfma_f32_16x16x32_bf16 v[208:211],a[112:115],v[88:91],v[208:211]\n"
"v_cndmask_b32 v67,v67,v72,vcc\n"
"s_sub_u32 s82,s3,196608\n"
"v_mfma_f32_16x16x32_bf16 v[240:243],a[112:115],v[92:95],v[240:243]\n"
"ds_read_b128 a[112:115],v114 offset:28672\n"
"buffer_load_dwordx4 v[176:179],v112,s[24:27],s82 offen offset:0 sc1\n"
"v_mfma_f32_16x16x32_bf16 v[212:215],a[116:119],v[88:91],v[212:215]\n"
"buffer_load_dwordx4 v[180:183],v112,s[24:27],s82 offen offset:1024 sc1\n"
"v_mfma_f32_16x16x32_bf16 v[244:247],a[116:119],v[92:95],v[244:247]\n"
"ds_read_b128 a[116:119],v114 offset:29696\n"
"buffer_load_dwordx4 v[184:187],v112,s[24:27],s82 offen offset:2048 sc1\n"
"v_mfma_f32_16x16x32_bf16 v[216:219],a[120:123],v[88:91],v[216:219]\n"
"buffer_load_dwordx4 v[188:191],v112,s[24:27],s82 offen offset:3072 sc1\n"
"v_mfma_f32_16x16x32_bf16 v[248:251],a[120:123],v[92:95],v[248:251]\n"
"ds_read_b128 a[120:123],v114 offset:30720\n"
"v_mfma_f32_16x16x32_bf16 v[220:223],a[124:127],v[88:91],v[220:223]\n"
"v_mfma_f32_16x16x32_bf16 v[252:255],a[124:127],v[92:95],v[252:255]\n"
"ds_read_b128 a[124:127],v114 offset:31744\n"
"s_waitcnt lgkmcnt(13)\n"
"v_cmp_neq_f32 vcc,v98,v104\n"
"s_nop 0\n"
"s_cbranch_vccnz .Lfk_ms_a0\n"
".Lfk_mr_a0:\n"
"v_mfma_f32_16x16x32_bf16 v[192:195],v[66:69],a[64:67],v[192:195]\n"
"v_mfma_f32_16x16x32_bf16 v[196:199],v[64:67],a[64:67],v[196:199]\n"
"v_mfma_f32_16x16x32_bf16 v[200:203],v[66:69],a[68:71],v[200:203]\n"
"v_mfma_f32_16x16x32_bf16 v[204:207],v[64:67],a[68:71],v[204:207]\n"
"v_mfma_f32_16x16x32_bf16 v[208:211],v[66:69],a[72:75],v[208:211]\n"
"v_mfma_f32_16x16x32_bf16 v[212:215],v[64:67],a[72:75],v[212:215]\n"
"v_mfma_f32_16x16x32_bf16 v[216:219],v[66:69],a[76:79],v[216:219]\n"
"v_mfma_f32_16x16x32_bf16 v[220:223],v[64:67],a[76:79],v[220:223]\n"
"s_waitcnt lgkmcnt(0)\n"
"v_mfma_f32_16x16x32_bf16 a[140:143],a[144:147],v[76:79],a[140:143]\n"
"v_mfma_f32_16x16x32_bf16 v[224:227],a[96:99],v[76:79],v[224:227]\n"
"v_mfma_f32_16x16x32_bf16 v[228:231],a[100:103],v[76:79],v[228:231]\n"
"v_accvgpr_read_b32 v122,a136\n"
"v_add_f32 v122,v122,v99\n"
"v_mfma_f32_16x16x32_bf16 v[232:235],a[104:107],v[76:79],v[232:235]\n"
"v_rcp_f32 v122,v122\n"
"v_mfma_f32_16x16x32_bf16 v[236:239],a[108:111],v[76:79],v[236:239]\n"
"v_pk_mul_f32 v[192:193],v[192:193],v[122:123] op_sel_hi:[1,0]\n"
"v_pk_mul_f32 v[194:195],v[194:195],v[122:123] op_sel_hi:[1,0]\n"
"v_pk_mul_f32 v[196:197],v[196:197],v[122:123] op_sel_hi:[1,0]\n"
"v_pk_mul_f32 v[198:199],v[198:199],v[122:123] op_sel_hi:[1,0]\n"
"v_mfma_f32_16x16x32_bf16 v[240:243],a[112:115],v[76:79],v[240:243]\n"
"v_cvt_pk_bf16_f32 v192,v192,v193\n"
"v_cvt_pk_bf16_f32 v193,v194,v195\n"
"v_mfma_f32_16x16x32_bf16 v[244:247],a[116:119],v[76:79],v[244:247]\n"
"v_cvt_pk_bf16_f32 v194,v196,v197\n"
"v_cvt_pk_bf16_f32 v195,v198,v199\n"
"v_mfma_f32_16x16x32_bf16 v[248:251],a[120:123],v[76:79],v[248:251]\n"
"buffer_store_dwordx4 v[192:195],v112,s[24:27],s70 offen offset:0 sc1\n"
"v_pk_mul_f32 v[200:201],v[200:201],v[122:123] op_sel_hi:[1,0]\n"
"v_pk_mul_f32 v[202:203],v[202:203],v[122:123] op_sel_hi:[1,0]\n"
"v_pk_mul_f32 v[204:205],v[204:205],v[122:123] op_sel_hi:[1,0]\n"
"v_pk_mul_f32 v[206:207],v[206:207],v[122:123] op_sel_hi:[1,0]\n"
"v_mfma_f32_16x16x32_bf16 v[252:255],a[124:127],v[76:79],v[252:255]\n"
"v_cvt_pk_bf16_f32 v200,v200,v201\n"
"v_cvt_pk_bf16_f32 v201,v202,v203\n"
"v_cmp_neq_f32 vcc,v102,v108\n"
"s_nop 0\n"
"s_cbranch_vccnz .Lfk_ms_a1\n"
".Lfk_mr_a1:\n"
"v_mfma_f32_16x16x32_bf16 v[224:227],v[66:69],a[80:83],v[224:227]\n"
"v_cvt_pk_bf16_f32 v202,v204,v205\n"
"v_cvt_pk_bf16_f32 v203,v206,v207\n"
"v_mfma_f32_16x16x32_bf16 v[228:231],v[64:67],a[80:83],v[228:231]\n"
"buffer_store_dwordx4 v[200:203],v112,s[24:27],s70 offen offset:1024 sc1\n"
"v_pk_mul_f32 v[208:209],v[208:209],v[122:123] op_sel_hi:[1,0]\n"
"v_pk_mul_f32 v[210:211],v[210:211],v[122:123] op_sel_hi:[1,0]\n"
"v_pk_mul_f32 v[212:213],v[212:213],v[122:123] op_sel_hi:[1,0]\n"
"v_pk_mul_f32 v[214:215],v[214:215],v[122:123] op_sel_hi:[1,0]\n"
"v_mfma_f32_16x16x32_bf16 v[232:235],v[66:69],a[84:87],v[232:235]\n"
"v_cvt_pk_bf16_f32 v208,v208,v209\n"
"v_cvt_pk_bf16_f32 v209,v210,v211\n"
"v_mfma_f32_16x16x32_bf16 v[236:239],v[64:67],a[84:87],v[236:239]\n"
"v_cvt_pk_bf16_f32 v210,v212,v213\n"
"v_cvt_pk_bf16_f32 v211,v214,v215\n"
"v_mfma_f32_16x16x32_bf16 v[240:243],v[66:69],a[88:91],v[240:243]\n"
"buffer_store_dwordx4 v[208:211],v112,s[24:27],s70 offen offset:2048 sc1\n"
"v_pk_mul_f32 v[216:217],v[216:217],v[122:123] op_sel_hi:[1,0]\n"
"v_pk_mul_f32 v[218:219],v[218:219],v[122:123] op_sel_hi:[1,0]\n"
"v_pk_mul_f32 v[220:221],v[220:221],v[122:123] op_sel_hi:[1,0]\n"
"v_pk_mul_f32 v[222:223],v[222:223],v[122:123] op_sel_hi:[1,0]\n"
"v_mfma_f32_16x16x32_bf16 v[244:247],v[64:67],a[88:91],v[244:247]\n"
"v_cvt_pk_bf16_f32 v216,v216,v217\n"
"v_cvt_pk_bf16_f32 v217,v218,v219\n"
"v_mfma_f32_16x16x32_bf16 v[248:251],v[66:69],a[92:95],v[248:251]\n"
"v_cvt_pk_bf16_f32 v218,v220,v221\n"
"v_cvt_pk_bf16_f32 v219,v222,v223\n"
"v_mfma_f32_16x16x32_bf16 v[252:255],v[64:67],a[92:95],v[252:255]\n"
"buffer_store_dwordx4 v[216:219],v112,s[24:27],s70 offen offset:3072 sc1\n"
"s_add_u32 s70,s70,262144\n"
"s_branch .Lfk_p1\n"
".Lfk_look:\n"
"s_lshr_b64 s[28:29],s[14:15],s21\n"
"s_and_b32 s83,s28,s19\n"
"s_cbranch_scc0 .Lfk_lk1\n"
"s_mov_b32 s84,0x40000\n"
"s_waitcnt vmcnt(10)\n"
".Lfk_lk0:\n"
"v_xor_b32 v119,s80,v122\n"
"v_xor_b32 v120,s81,v123\n"
"v_or_b32 v119,v119,v120\n"
"v_cmp_ne_u32 s[14:15],0,v119\n"
"s_nop 0\n"
"s_lshr_b64 s[28:29],s[14:15],s21\n"
"s_and_b32 s83,s28,s19\n"
"s_cbranch_scc0 .Lfk_lk1\n"
"s_sleep 2\n"
"buffer_load_dwordx2 v[122:123],v115,s[24:27],s11 offen sc1\n"
"s_waitcnt vmcnt(0)\n"
"s_sub_u32 s84,s84,1\n"
"s_cbranch_scc0 .Lfk_lk0\n"
".Lfk_lk1:\n"
"s_andn2_b32 s28,s28,s19\n"
"s_lshl_b64 s[14:15],s[28:29],s21\n"
"s_lshl_b32 s83,s19,8\n"
"s_and_b32 s83,s28,s83\n"
"s_cbranch_scc0 .Lfk_lk2\n"
"buffer_load_dwordx2 v[122:123],v115,s[24:27],s11 offen sc1\n"
".Lfk_lk2:\n"
"s_nop 1\n"
"s_setpc_b64 s[94:95]\n"
".Lfk_meet4:\n"
"s_mov_b32 s84,0x1000000\n"
".Lfk_meetl4:\n"
"ds_read_b32 v125,v114\n"
"s_sub_u32 s84,s84,1\n"
"s_cbranch_scc1 .Lfk_meetx4\n"
"s_waitcnt lgkmcnt(0)\n"
"v_cmp_gt_u32 vcc,s16,v125\n"
"s_nop 0\n"
"s_cbranch_vccnz .Lfk_meetl4\n"
".Lfk_meetx4:\n"
"s_setpc_b64 s[94:95]\n"
".Lfk_es_lkone:\n"
"s_call_b64 s[94:95],.Lfk_look\n"
"s_branch .Lfk_er_lkone\n"
".Lfk_z0:\n"
"s_call_b64 s[94:95],.Lfk_meet4\n"
"v_xor_b32 v119,s80,v104\n"
"v_xor_b32 v120,s81,v105\n"
"v_or_b32 v119,v119,v120\n"
"v_xor_b32 v120,s80,v106\n"
"v_or_b32 v119,v119,v120\n"
"v_xor_b32 v120,s81,v107\n"
"v_or_b32 v119,v119,v120\n"
"v_xor_b32 v120,s80,v108\n"
"v_or_b32 v119,v119,v120\n"
"v_xor_b32 v120,s81,v109\n"
"v_or_b32 v119,v119,v120\n"
"v_xor_b32 v120,s80,v110\n"
"v_or_b32 v119,v119,v120\n"
"v_xor_b32 v120,s81,v111\n"
"v_or_b32 v119,v119,v120\n"
"v_cmp_ne_u32 vcc,0,v119\n"
"s_nop 0\n"
"s_cbranch_vccz .Lfk_z1\n"
"s_mov_b32 s84,0x40000\n"
"s_mov_b32 s83,148946944\n"
"buffer_load_dwordx4 v[104:107],v112,s[24:27],s83 offen offset:0 sc1\n"
"buffer_load_dwordx4 v[108:111],v112,s[24:27],s83 offen offset:1024 sc1\n"
"s_waitcnt vmcnt(0)\n"
".Lfk_w0:\n"
"v_xor_b32 v119,s80,v104\n"
"v_xor_b32 v120,s81,v105\n"
"v_or_b32 v119,v119,v120\n"
"v_xor_b32 v120,s80,v106\n"
"v_or_b32 v119,v119,v120\n"
"v_xor_b32 v120,s81,v107\n"
"v_or_b32 v119,v119,v120\n"
"v_xor_b32 v120,s80,v108\n"
"v_or_b32 v119,v119,v120\n"
"v_xor_b32 v120,s81,v109\n"
"v_or_b32 v119,v119,v120\n"
"v_xor_b32 v120,s80,v110\n"
"v_or_b32 v119,v119,v120\n"
"v_xor_b32 v120,s81,v111\n"
"v_or_b32 v119,v119,v120\n"
"v_cmp_ne_u32 vcc,0,v119\n"
"s_nop 0\n"
"s_cbranch_vccz .Lfk_w0_x\n"
"s_sleep 8\n"
"s_mov_b64 exec,vcc\n"
"s_mov_b32 s83,148946944\n"
"buffer_load_dwordx4 v[104:107],v112,s[24:27],s83 offen offset:0 sc1\n"
"buffer_load_dwordx4 v[108:111],v112,s[24:27],s83 offen offset:1024 sc1\n"
"s_mov_b64 exec,-1\n"
"s_sub_u32 s84,s84,1\n"
"s_waitcnt vmcnt(0)\n"
"s_cbranch_scc0 .Lfk_w0\n"
".Lfk_w0_x:\n"
"s_nop 1\n"
"s_branch .Lfk_z1\n"
".Lfk_es_lkonem:\n"
"s_call_b64 s[94:95],.Lfk_look\n"
"s_branch .Lfk_er_lkonem\n"
".Lfk_es_mone:\n"
"s_call_b64 s[94:95],.Lfk_meet4\n"
"s_branch .Lfk_er_mone\n"
".Lfk_es_lktwom:\n"
"s_call_b64 s[94:95],.Lfk_look\n"
"s_branch .Lfk_er_lktwom\n"
".Lfk_es_mtwo:\n"
"s_call_b64 s[94:95],.Lfk_meet4\n"
"s_branch .Lfk_er_mtwo\n"
".Lfk_es_lkloopm:\n"
"s_call_b64 s[94:95],.Lfk_look\n"
"s_branch .Lfk_er_lkloopm\n"
".Lfk_es_me3:\n"
"s_call_b64 s[94:95],.Lfk_meet4\n"
"s_branch .Lfk_er_me3\n"
".Lfk_es_me2:\n"
"s_call_b64 s[94:95],.Lfk_meet4\n"
"s_branch .Lfk_er_me2\n"
".Lfk_p1:\n"
"s_cmp_eq_u32 s18,2146435072\n"
"s_cbranch_scc1 .Lfk_p2\n"
".Lfk_p3:\n"
"s_lshr_b32 s22,s8,2\n"
"s_and_b32 s31,s8,0xfff1ffff\n"
"s_add_u32 s18,s18,16384\n"
"s_andn2_b32 s91,s55,16384\n"
"v_add_u32 v116,s91,v112\n"
"v_accvgpr_read_b32 v122,a140\n"
"v_add_f32 v122,v122,v103\n"
"v_rcp_f32 v122,v122\n"
"s_nop 0\n"
"v_pk_mul_f32 v[224:225],v[224:225],v[122:123] op_sel_hi:[1,0]\n"
"v_pk_mul_f32 v[226:227],v[226:227],v[122:123] op_sel_hi:[1,0]\n"
"v_pk_mul_f32 v[228:229],v[228:229],v[122:123] op_sel_hi:[1,0]\n"
"v_pk_mul_f32 v[230:231],v[230:231],v[122:123] op_sel_hi:[1,0]\n"
"v_pk_mul_f32 v[232:233],v[232:233],v[122:123] op_sel_hi:[1,0]\n"
"v_pk_mul_f32 v[234:235],v[234:235],v[122:123] op_sel_hi:[1,0]\n"
"v_pk_mul_f32 v[236:237],v[236:237],v[122:123] op_sel_hi:[1,0]\n"
"v_pk_mul_f32 v[238:239],v[238:239],v[122:123] op_sel_hi:[1,0]\n"
"v_pk_mul_f32 v[240:241],v[240:241],v[122:123] op_sel_hi:[1,0]\n"
"v_pk_mul_f32 v[242:243],v[242:243],v[122:123] op_sel_hi:[1,0]\n"
"v_pk_mul_f32 v[244:245],v[244:245],v[122:123] op_sel_hi:[1,0]\n"
"v_pk_mul_f32 v[246:247],v[246:247],v[122:123] op_sel_hi:[1,0]\n"
"v_pk_mul_f32 v[248:249],v[248:249],v[122:123] op_sel_hi:[1,0]\n"
"v_pk_mul_f32 v[250:251],v[250:251],v[122:123] op_sel_hi:[1,0]\n"
"v_pk_mul_f32 v[252:253],v[252:253],v[122:123] op_sel_hi:[1,0]\n"
"v_pk_mul_f32 v[254:255],v[254:255],v[122:123] op_sel_hi:[1,0]\n"
"s_waitcnt vmcnt(24) lgkmcnt(0)\n"
"s_barrier\n"
"ds_read_b128 v[192:195],v112 offset:0\n"
"ds_read_b128 v[196:199],v112 offset:1024\n"
"ds_read_b128 v[200:203],v112 offset:2048\n"
"ds_read_b128 v[204:207],v112 offset:3072\n"
"ds_read_b128 v[208:211],v112 offset:4096\n"
"ds_read_b128 v[212:215],v112 offset:5120\n"
"ds_read_b128 v[216:219],v112 offset:6144\n"
"ds_read_b128 v[220:223],v112 offset:7168\n"
"v_cvt_pk_bf16_f32 v224,v224,v225\n"
"v_cvt_pk_bf16_f32 v225,v226,v227\n"
"v_cvt_pk_bf16_f32 v226,v228,v229\n"
"v_cvt_pk_bf16_f32 v227,v230,v231\n"
"v_cvt_pk_bf16_f32 v232,v232,v233\n"
"v_cvt_pk_bf16_f32 v233,v234,v235\n"
"v_cvt_pk_bf16_f32 v234,v236,v237\n"
"v_cvt_pk_bf16_f32 v235,v238,v239\n"
"v_cvt_pk_bf16_f32 v240,v240,v241\n"
"v_cvt_pk_bf16_f32 v241,v242,v243\n"
"v_cvt_pk_bf16_f32 v242,v244,v245\n"
"v_cvt_pk_bf16_f32 v243,v246,v247\n"
"v_cvt_pk_bf16_f32 v248,v248,v249\n"
"v_cvt_pk_bf16_f32 v249,v250,v251\n"
"v_cvt_pk_bf16_f32 v250,v252,v253\n"
"v_cvt_pk_bf16_f32 v251,v254,v255\n"
"buffer_store_dwordx4 v[224:227],v112,s[24:27],s70 offen offset:0 sc1\n"
"buffer_store_dwordx4 v[232:235],v112,s[24:27],s70 offen offset:1024 sc1\n"
"buffer_store_dwordx4 v[240:243],v112,s[24:27],s70 offen offset:2048 sc1\n"
"buffer_store_dwordx4 v[248:251],v112,s[24:27],s70 offen offset:3072 sc1\n"
"s_waitcnt lgkmcnt(4)\n"
"v_mfma_f32_16x16x32_bf16 v[80:83],a[224:227],v[192:195],0\n"
"v_mfma_f32_16x16x32_bf16 v[80:83],a[232:235],v[196:199],v[80:83]\n"
"ds_read_b128 v[224:227],v112 offset:8192\n"
"ds_read_b128 v[228:231],v112 offset:9216\n"
"v_mfma_f32_16x16x32_bf16 v[80:83],a[240:243],v[200:203],v[80:83]\n"
"ds_read_b128 v[232:235],v112 offset:10240\n"
"ds_read_b128 v[236:239],v112 offset:11264\n"
"v_mfma_f32_16x16x32_bf16 v[80:83],a[248:251],v[204:207],v[80:83]\n"
"ds_read_b128 v[240:243],v112 offset:12288\n"
"ds_read_b128 v[244:247],v112 offset:13312\n"
"v_mfma_f32_16x16x32_bf16 v[84:87],a[252:255],v[204:207],0\n"
"ds_read_b128 v[248:251],v112 offset:14336\n"
"ds_read_b128 v[252:255],v112 offset:15360\n"
"v_mfma_f32_16x16x32_bf16 v[84:87],a[244:247],v[200:203],v[84:87]\n"
"ds_read_b128 a[68:71],v116 offset:4096\n"
"ds_read_b128 a[72:75],v116 offset:8192\n"
"v_mfma_f32_16x16x32_bf16 v[84:87],a[236:239],v[196:199],v[84:87]\n"
"ds_read_b128 a[76:79],v116 offset:12288\n"
"ds_read_b128 a[80:83],v116 offset:0\n"
"v_mfma_f32_16x16x32_bf16 v[84:87],a[228:231],v[192:195],v[84:87]\n"
"ds_read_b128 a[84:87],v116 offset:20480\n"
"ds_read_b128 a[88:91],v116 offset:24576\n"
"s_waitcnt lgkmcnt(14)\n"
"v_mfma_f32_16x16x32_bf16 v[84:87],a[4:7],v[208:211],v[84:87]\n"
"ds_read_b128 a[92:95],v116 offset:28672\n"
"v_mfma_f32_16x16x32_bf16 v[84:87],a[12:15],v[212:215],v[84:87]\n"
"ds_read_b128 v[204:207],v112 offset:19456\n"
"s_lshl_b32 s91,s78,3\n"
"v_mfma_f32_16x16x32_bf16 v[84:87],a[20:23],v[216:219],v[84:87]\n"
"ds_read_b128 v[200:203],v112 offset:18432\n"
"s_add_u32 s91,s91,131072\n"
"v_mfma_f32_16x16x32_bf16 v[84:87],a[28:31],v[220:223],v[84:87]\n"
"ds_read_b128 v[196:199],v112 offset:17408\n"
"v_lshrrev_b32 v101,2,v112\n"
"v_mfma_f32_16x16x32_bf16 v[80:83],a[24:27],v[220:223],v[80:83]\n"
"ds_read_b128 v[192:195],v112 offset:16384\n"
"v_add_u32 v101,s91,v101\n"
"v_mfma_f32_16x16x32_bf16 v[80:83],a[16:19],v[216:219],v[80:83]\n"
"ds_read_b128 v[220:223],v112 offset:23552\n"
"v_lshrrev_b32 v114,2,v112\n"
"v_mfma_f32_16x16x32_bf16 v[80:83],a[8:11],v[212:215],v[80:83]\n"
"ds_read_b128 v[216:219],v112 offset:22528\n"
"v_add_u32 v114,131328,v114\n"
"v_mfma_f32_16x16x32_bf16 v[80:83],a[0:3],v[208:211],v[80:83]\n"
"ds_read_b128 v[212:215],v112 offset:21504\n"
"s_waitcnt lgkmcnt(15)\n"
"v_mfma_f32_16x16x32_bf16 v[80:83],v[32:35],v[224:227],v[80:83]\n"
"ds_read_b128 v[208:211],v112 offset:20480\n"
"s_mov_b32 s83,148946944\n"
"v_mfma_f32_16x16x32_bf16 v[80:83],v[40:43],v[228:231],v[80:83]\n"
"buffer_load_dwordx4 v[104:107],v112,s[24:27],s83 offen offset:0 sc1\n"
"v_mov_b32 v116,0\n"
"v_mfma_f32_16x16x32_bf16 v[80:83],a[32:35],v[232:235],v[80:83]\n"
"ds_write_b32 v101,v116 offset:256\n"
"v_mov_b32 v116,1\n"
"v_mfma_f32_16x16x32_bf16 v[80:83],a[40:43],v[236:239],v[80:83]\n"
"buffer_load_dwordx4 v[108:111],v112,s[24:27],s83 offen offset:1024 sc1\n"
"v_mfma_f32_16x16x32_bf16 v[84:87],a[44:47],v[236:239],v[84:87]\n"
"s_mov_b32 s16,4\n"
"ds_read_b128 v[236:239],v112 offset:27648\n"
"v_add_u32 v113,65536,v112\n"
"v_mfma_f32_16x16x32_bf16 v[84:87],a[36:39],v[232:235],v[84:87]\n"
"ds_read_b128 v[232:235],v112 offset:26624\n"
"s_add_u32 s31,s31,8388608\n"
"v_mfma_f32_16x16x32_bf16 v[84:87],v[44:47],v[228:231],v[84:87]\n"
"ds_read_b128 v[228:231],v112 offset:25600\n"
"s_add_u32 s53,s22,786432\n"
"v_mfma_f32_16x16x32_bf16 v[84:87],v[36:39],v[224:227],v[84:87]\n"
"s_add_u32 m0,s30,65536\n"
"ds_read_b128 v[224:227],v112 offset:24576\n"
"s_waitcnt lgkmcnt(13)\n"
"v_mfma_f32_16x16x32_bf16 v[84:87],a[68:71],v[240:243],v[84:87]\n"
"buffer_load_dwordx4 v112,s[24:27],s18 offen offset:0 sc1 lds\n"
"v_mfma_f32_16x16x32_bf16 v[84:87],a[76:79],v[244:247],v[84:87]\n"
"buffer_load_dwordx4 v112,s[24:27],s18 offen offset:1024 sc1 lds\n"
"v_mfma_f32_16x16x32_bf16 v[84:87],a[84:87],v[248:251],v[84:87]\n"
"s_add_u32 s56,s22,1048576\n"
"s_add_u32 s58,s22,1310720\n"
"v_mfma_f32_16x16x32_bf16 v[84:87],a[92:95],v[252:255],v[84:87]\n"
"buffer_load_dwordx4 v112,s[24:27],s18 offen offset:2048 sc1 lds\n"
"v_mfma_f32_16x16x32_bf16 v[80:83],a[88:91],v[252:255],v[80:83]\n"
"ds_read_b128 v[252:255],v112 offset:31744\n"
"s_add_u32 s59,s22,1572864\n"
"v_mfma_f32_16x16x32_bf16 v[80:83],a[80:83],v[248:251],v[80:83]\n"
"ds_read_b128 v[248:251],v112 offset:30720\n"
"s_bfe_u32 s10,s3,0x20015\n"
"v_mfma_f32_16x16x32_bf16 v[80:83],a[72:75],v[244:247],v[80:83]\n"
"ds_read_b128 v[244:247],v112 offset:29696\n"
"s_mul_i32 s10,s10,4186112\n"
"v_mfma_f32_16x16x32_bf16 v[80:83],a[156:159],v[240:243],v[80:83]\n"
"ds_read_b128 v[240:243],v112 offset:28672\n"
"s_waitcnt lgkmcnt(13)\n"
"v_mfma_f32_16x16x32_bf16 v[64:67],a[224:227],v[192:195],0\n"
"buffer_load_dwordx4 v112,s[24:27],s18 offen offset:3072 sc1 lds\n"
"s_add_u32 m0,m0,4096\n"
"v_mfma_f32_16x16x32_bf16 v[64:67],a[232:235],v[196:199],v[64:67]\n"
"buffer_load_dwordx4 v117,s[24:27],s18 offen offset:0 sc1 lds\n"
"v_mfma_f32_16x16x32_bf16 v[64:67],a[240:243],v[200:203],v[64:67]\n"
"s_add_u32 s12,s12,s10\n"
"s_bfe_u32 s10,s3,0x30012\n"
"v_mfma_f32_16x16x32_bf16 v[64:67],a[248:251],v[204:207],v[64:67]\n"
"buffer_load_dwordx4 v117,s[24:27],s18 offen offset:1024 sc1 lds\n"
"v_mfma_f32_16x16x32_bf16 v[68:71],a[252:255],v[204:207],0\n"
"ds_read_b128 v[204:207],v112 offset:35840\n"
"s_lshl_b32 s10,s10,12\n"
"v_mfma_f32_16x16x32_bf16 v[68:71],a[244:247],v[200:203],v[68:71]\n"
"ds_read_b128 v[200:203],v112 offset:34816\n"
"s_add_u32 s12,s12,s10\n"
"v_mfma_f32_16x16x32_bf16 v[68:71],a[236:239],v[196:199],v[68:71]\n"
"ds_read_b128 v[196:199],v112 offset:33792\n"
"s_bfe_u32 s10,s3,0x50012\n"
"v_mfma_f32_16x16x32_bf16 v[68:71],a[228:231],v[192:195],v[68:71]\n"
"ds_read_b128 v[192:195],v112 offset:32768\n"
"s_waitcnt lgkmcnt(13)\n"
"s_waitcnt vmcnt(8) lgkmcnt(12)\n"
"s_barrier\n"
"v_mfma_f32_16x16x32_bf16 v[68:71],a[4:7],v[208:211],v[68:71]\n"
"buffer_store_dwordx2 a[154:155],off,s[24:27],s71 sc1\n"
"buffer_load_dwordx4 v117,s[24:27],s18 offen offset:2048 sc1 lds\n"
"v_mfma_f32_16x16x32_bf16 v[68:71],a[12:15],v[212:215],v[68:71]\n"
"buffer_load_dwordx4 v117,s[24:27],s18 offen offset:3072 sc1 lds\n"
"v_mfma_f32_16x16x32_bf16 v[68:71],a[20:23],v[216:219],v[68:71]\n"
"s_sub_u32 s82,s3,212992\n"
"s_lshl_b32 s10,s10,8\n"
"v_mfma_f32_16x16x32_bf16 v[68:71],a[28:31],v[220:223],v[68:71]\n"
"buffer_load_dwordx4 a[48:51],v112,s[24:27],s82 offen offset:0 sc1\n"
"v_mfma_f32_16x16x32_bf16 v[64:67],a[24:27],v[220:223],v[64:67]\n"
"s_add_u32 s10,s10,149061632\n"
"ds_read_b128 v[220:223],v112 offset:39936\n"
"s_mov_b32 s14,0\n"
"v_mfma_f32_16x16x32_bf16 v[64:67],a[16:19],v[216:219],v[64:67]\n"
"ds_read_b128 v[216:219],v112 offset:38912\n"
"v_not_b32 v122,s80\n"
"v_mfma_f32_16x16x32_bf16 v[64:67],a[8:11],v[212:215],v[64:67]\n"
"s_cmp_eq_u32 s52,0\n"
"ds_read_b128 v[212:215],v112 offset:37888\n"
"s_cselect_b64 s[14:15],s[14:15],0\n"
"v_mfma_f32_16x16x32_bf16 v[64:67],a[0:3],v[208:211],v[64:67]\n"
"s_cselect_b32 s10,s10,2146435072\n"
"ds_read_b128 v[208:211],v112 offset:36864\n"
"s_waitcnt lgkmcnt(12)\n"
"v_mfma_f32_16x16x32_bf16 v[64:67],v[32:35],v[224:227],v[64:67]\n"
"buffer_load_dwordx4 a[52:55],v112,s[24:27],s82 offen offset:1024 sc1\n"
"s_cmp_lg_u32 s15,0\n"
"s_cselect_b32 s91,s11,2146435072\n"
"v_mfma_f32_16x16x32_bf16 v[64:67],v[40:43],v[228:231],v[64:67]\n"
"buffer_load_dwordx4 a[56:59],v112,s[24:27],s82 offen offset:2048 sc1\n"
"buffer_load_dwordx2 v[122:123],v115,s[24:27],s91 offen sc1\n"
"v_mfma_f32_16x16x32_bf16 v[64:67],a[32:35],v[232:235],v[64:67]\n"
"s_mov_b32 s89,0\n"
"s_mov_b32 s20,32768\n"
"v_mfma_f32_16x16x32_bf16 v[64:67],a[40:43],v[236:239],v[64:67]\n"
"buffer_load_dwordx4 a[60:63],v112,s[24:27],s82 offen offset:3072 sc1\n"
"v_mfma_f32_16x16x32_bf16 v[68:71],a[44:47],v[236:239],v[68:71]\n"
"s_mov_b32 s19,0xffff\n"
"ds_read_b128 v[236:239],v112 offset:44032\n"
"s_mov_b32 s21,32\n"
"v_mfma_f32_16x16x32_bf16 v[68:71],a[36:39],v[232:235],v[68:71]\n"
"ds_read_b128 v[232:235],v112 offset:43008\n"
"v_mfma_f32_16x16x32_bf16 v[68:71],v[44:47],v[228:231],v[68:71]\n"
"ds_read_b128 v[228:231],v112 offset:41984\n"
"v_mfma_f32_16x16x32_bf16 v[68:71],v[36:39],v[224:227],v[68:71]\n"
"s_sub_u32 s82,s3,229376\n"
"ds_read_b128 v[224:227],v112 offset:40960\n"
"s_waitcnt lgkmcnt(12)\n"
"v_mfma_f32_16x16x32_bf16 v[68:71],a[68:71],v[240:243],v[68:71]\n"
"buffer_load_dwordx4 v[48:51],v112,s[24:27],s82 offen offset:0 sc1\n"
"v_mfma_f32_16x16x32_bf16 v[68:71],a[76:79],v[244:247],v[68:71]\n"
"buffer_load_dwordx4 v[52:55],v112,s[24:27],s82 offen offset:1024 sc1\n"
"v_mfma_f32_16x16x32_bf16 v[68:71],a[84:87],v[248:251],v[68:71]\n"
"buffer_load_dwordx4 v[56:59],v112,s[24:27],s82 offen offset:2048 sc1\n"
"v_mfma_f32_16x16x32_bf16 v[68:71],a[92:95],v[252:255],v[68:71]\n"
"s_waitcnt vmcnt(17)\n"
"v_mfma_f32_16x16x32_bf16 v[64:67],a[88:91],v[252:255],v[64:67]\n"
"ds_read_b128 v[252:255],v112 offset:48128\n"
"v_xor_b32 v119,s80,v104\n"
"v_mfma_f32_16x16x32_bf16 v[64:67],a[80:83],v[248:251],v[64:67]\n"
"ds_read_b128 v[248:251],v112 offset:47104\n"
"v_xor_b32 v120,s81,v105\n"
"v_mfma_f32_16x16x32_bf16 v[64:67],a[72:75],v[244:247],v[64:67]\n"
"ds_read_b128 v[244:247],v112 offset:46080\n"
"v_or_b32 v119,v119,v120\n"
"v_mfma_f32_16x16x32_bf16 v[64:67],a[156:159],v[240:243],v[64:67]\n"
"ds_read_b128 v[240:243],v112 offset:45056\n"
"s_waitcnt lgkmcnt(12)\n"
"v_mfma_f32_16x16x32_bf16 v[72:75],a[224:227],v[192:195],0\n"
"buffer_load_dwordx4 v[60:63],v112,s[24:27],s82 offen offset:3072 sc1\n"
"s_sub_u32 s82,s3,245760\n"
"v_mfma_f32_16x16x32_bf16 v[72:75],a[232:235],v[196:199],v[72:75]\n"
"buffer_load_dwordx4 v[16:19],v112,s[24:27],s82 offen offset:0 sc1\n"
"v_xor_b32 v120,s80,v106\n"
"v_mfma_f32_16x16x32_bf16 v[72:75],a[240:243],v[200:203],v[72:75]\n"
"v_or_b32 v119,v119,v120\n"
"v_xor_b32 v120,s81,v107\n"
"v_mfma_f32_16x16x32_bf16 v[72:75],a[248:251],v[204:207],v[72:75]\n"
"buffer_load_dwordx4 v[20:23],v112,s[24:27],s82 offen offset:1024 sc1\n"
"v_or_b32 v119,v119,v120\n"
"v_mfma_f32_16x16x32_bf16 v[76:79],a[252:255],v[204:207],0\n"
"ds_read_b128 v[204:207],v112 offset:52224\n"
"v_xor_b32 v120,s80,v108\n"
"v_mfma_f32_16x16x32_bf16 v[76:79],a[244:247],v[200:203],v[76:79]\n"
"ds_read_b128 v[200:203],v112 offset:51200\n"
"v_or_b32 v119,v119,v120\n"
"v_mfma_f32_16x16x32_bf16 v[76:79],a[236:239],v[196:199],v[76:79]\n"
"ds_read_b128 v[196:199],v112 offset:50176\n"
"v_xor_b32 v120,s81,v109\n"
"v_mfma_f32_16x16x32_bf16 v[76:79],a[228:231],v[192:195],v[76:79]\n"
"ds_read_b128 v[192:195],v112 offset:49152\n"
"s_waitcnt lgkmcnt(12)\n"
"v_mfma_f32_16x16x32_bf16 v[76:79],a[4:7],v[208:211],v[76:79]\n"
"buffer_load_dwordx4 v[24:27],v112,s[24:27],s82 offen offset:2048 sc1\n"
"v_or_b32 v119,v119,v120\n"
"v_mfma_f32_16x16x32_bf16 v[76:79],a[12:15],v[212:215],v[76:79]\n"
"buffer_load_dwordx4 v[28:31],v112,s[24:27],s82 offen offset:3072 sc1\n"
"v_mfma_f32_16x16x32_bf16 v[76:79],a[20:23],v[216:219],v[76:79]\n"
"s_add_u32 s18,s18,2097152\n"
"v_xor_b32 v120,s80,v110\n"
"s_add_u32 m0,s30,98304\n"
"v_mfma_f32_16x16x32_bf16 v[76:79],a[28:31],v[220:223],v[76:79]\n"
"buffer_load_dwordx4 v112,s[24:27],s18 offen offset:0 sc1 lds\n"
"v_mfma_f32_16x16x32_bf16 v[72:75],a[24:27],v[220:223],v[72:75]\n"
"ds_read_b128 v[220:223],v112 offset:56320\n"
"v_or_b32 v119,v119,v120\n"
"v_mfma_f32_16x16x32_bf16 v[72:75],a[16:19],v[216:219],v[72:75]\n"
"ds_read_b128 v[216:219],v112 offset:55296\n"
"v_xor_b32 v120,s81,v111\n"
"v_mfma_f32_16x16x32_bf16 v[72:75],a[8:11],v[212:215],v[72:75]\n"
"ds_read_b128 v[212:215],v112 offset:54272\n"
"v_or_b32 v119,v119,v120\n"
"v_mfma_f32_16x16x32_bf16 v[72:75],a[0:3],v[208:211],v[72:75]\n"
"ds_read_b128 v[208:211],v112 offset:53248\n"
"s_waitcnt lgkmcnt(12)\n"
"v_mfma_f32_16x16x32_bf16 v[72:75],v[32:35],v[224:227],v[72:75]\n"
"buffer_load_dwordx4 v112,s[24:27],s18 offen offset:1024 sc1 lds\n"
"v_mfma_f32_16x16x32_bf16 v[72:75],v[40:43],v[228:231],v[72:75]\n"
"buffer_load_dwordx4 v112,s[24:27],s18 offen offset:2048 sc1 lds\n"
"v_mfma_f32_16x16x32_bf16 v[72:75],a[32:35],v[232:235],v[72:75]\n"
"v_cmp_ne_u32 vcc,0,v119\n"
"v_mfma_f32_16x16x32_bf16 v[72:75],a[40:43],v[236:239],v[72:75]\n"
"buffer_load_dwordx4 v112,s[24:27],s18 offen offset:3072 sc1 lds\n"
"v_mfma_f32_16x16x32_bf16 v[76:79],a[44:47],v[236:239],v[76:79]\n"
"ds_read_b128 v[236:239],v112 offset:60416\n"
"v_mfma_f32_16x16x32_bf16 v[76:79],a[36:39],v[232:235],v[76:79]\n"
"ds_read_b128 v[232:235],v112 offset:59392\n"
"v_xor_b32 v119,s80,v122\n"
"v_mfma_f32_16x16x32_bf16 v[76:79],v[44:47],v[228:231],v[76:79]\n"
"ds_read_b128 v[228:231],v112 offset:58368\n"
"v_xor_b32 v120,s81,v123\n"
"v_mfma_f32_16x16x32_bf16 v[76:79],v[36:39],v[224:227],v[76:79]\n"
"s_add_u32 m0,m0,4096\n"
"ds_read_b128 v[224:227],v112 offset:57344\n"
"s_waitcnt lgkmcnt(12)\n"
"v_mfma_f32_16x16x32_bf16 v[76:79],a[68:71],v[240:243],v[76:79]\n"
"buffer_load_dwordx4 v117,s[24:27],s18 offen offset:0 sc1 lds\n"
"v_or_b32 v119,v119,v120\n"
"v_mfma_f32_16x16x32_bf16 v[76:79],a[76:79],v[244:247],v[76:79]\n"
"buffer_load_dwordx4 v117,s[24:27],s18 offen offset:1024 sc1 lds\n"
"v_cmp_ne_u32 s[28:29],0,v119\n"
"v_mfma_f32_16x16x32_bf16 v[76:79],a[84:87],v[248:251],v[76:79]\n"
"s_and_b64 s[14:15],s[14:15],s[28:29]\n"
"s_cselect_b32 s91,s11,2146435072\n"
"buffer_load_dwordx2 v[122:123],v115,s[24:27],s91 offen sc1\n"
"v_mfma_f32_16x16x32_bf16 v[76:79],a[92:95],v[252:255],v[76:79]\n"
"buffer_load_dwordx4 v117,s[24:27],s18 offen offset:2048 sc1 lds\n"
"v_mfma_f32_16x16x32_bf16 v[72:75],a[88:91],v[252:255],v[72:75]\n"
"ds_read_b128 v[252:255],v112 offset:64512\n"
"v_mfma_f32_16x16x32_bf16 v[72:75],a[80:83],v[248:251],v[72:75]\n"
"ds_read_b128 v[248:251],v112 offset:63488\n"
"v_mfma_f32_16x16x32_bf16 v[72:75],a[72:75],v[244:247],v[72:75]\n"
"ds_read_b128 v[244:247],v112 offset:62464\n"
"v_mfma_f32_16x16x32_bf16 v[72:75],a[156:159],v[240:243],v[72:75]\n"
"s_and_b32 s83,s15,0xffff\n"
"ds_read_b128 v[240:243],v112 offset:61440\n"
"s_waitcnt lgkmcnt(12)\n"
"v_mfma_f32_16x16x32_bf16 v[88:91],a[224:227],v[192:195],0\n"
"buffer_load_dwordx4 v117,s[24:27],s18 offen offset:3072 sc1 lds\n"
"v_mfma_f32_16x16x32_bf16 v[88:91],a[232:235],v[196:199],v[88:91]\n"
"s_cbranch_scc1 .Lfk_es_lkone\n"
".Lfk_er_lkone:\n"
"v_mfma_f32_16x16x32_bf16 v[88:91],a[240:243],v[200:203],v[88:91]\n"
"s_waitcnt vmcnt(22)\n"
"ds_add_u32 v114,v116\n"
"v_mfma_f32_16x16x32_bf16 v[88:91],a[248:251],v[204:207],v[88:91]\n"
"v_and_b32 v118,0xf0,v112\n"
"v_lshrrev_b32 v120,8,v112\n"
"v_mfma_f32_16x16x32_bf16 v[92:95],a[252:255],v[204:207],0\n"
"v_lshlrev_b32 v120,4,v120\n"
"v_lshl_add_u32 v118,v118,2,v120\n"
"v_mfma_f32_16x16x32_bf16 v[92:95],a[244:247],v[200:203],v[92:95]\n"
"s_lshr_b32 s82,s78,20\n"
"v_xor_b32 v118,s82,v118\n"
"v_mov_b32 v100,1\n"
"v_mfma_f32_16x16x32_bf16 v[92:95],a[236:239],v[196:199],v[92:95]\n"
"v_add_u32 v118,s12,v118\n"
"v_mov_b32 v102,131076\n"
"v_mfma_f32_16x16x32_bf16 v[92:95],a[228:231],v[192:195],v[92:95]\n"
"s_waitcnt lgkmcnt(9)\n"
"v_mfma_f32_16x16x32_bf16 v[92:95],a[4:7],v[208:211],v[92:95]\n"
"v_mfma_f32_16x16x32_bf16 v[92:95],a[12:15],v[212:215],v[92:95]\n"
"ds_read_b32 v125,v114\n"
"v_mfma_f32_16x16x32_bf16 v[92:95],a[20:23],v[216:219],v[92:95]\n"
"v_mfma_f32_16x16x32_bf16 v[92:95],a[28:31],v[220:223],v[92:95]\n"
"v_mfma_f32_16x16x32_bf16 v[88:91],a[24:27],v[220:223],v[88:91]\n"
"v_mfma_f32_16x16x32_bf16 v[88:91],a[16:19],v[216:219],v[88:91]\n"
"s_waitcnt lgkmcnt(0)\n"
"v_cmp_gt_u32 s[28:29],s16,v125\n"
"v_mfma_f32_16x16x32_bf16 v[88:91],a[8:11],v[212:215],v[88:91]\n"
"s_add_u32 s18,s8,4194304\n"
"s_add_u32 s8,s8,5242880\n"
"s_or_b64 vcc,vcc,s[28:29]\n"
"v_mfma_f32_16x16x32_bf16 v[88:91],a[0:3],v[208:211],v[88:91]\n"
"s_cbranch_vccnz .Lfk_z0\n"
".Lfk_z1:\n"
"ds_read_b128 v[192:195],v113 offset:0\n"
"v_mfma_f32_16x16x32_bf16 v[88:91],v[32:35],v[224:227],v[88:91]\n"
"s_mov_b32 s89,1\n"
"s_mov_b32 s19,0xff\n"
"s_lshl_b64 exec,1,s89\n"
"buffer_atomic_add v100,off,s[24:27],s10 sc0\n"
"s_mov_b64 exec,-1\n"
"v_mfma_f32_16x16x32_bf16 v[88:91],v[40:43],v[228:231],v[88:91]\n"
"ds_read_b128 v[196:199],v113 offset:1024\n"
"s_add_u32 m0,s30,0\n"
"v_mfma_f32_16x16x32_bf16 v[88:91],a[32:35],v[232:235],v[88:91]\n"
"ds_read_b128 v[200:203],v113 offset:2048\n"
"buffer_load_dwordx4 v112,s[24:27],s18 offen offset:0 sc1 lds\n"
"v_mfma_f32_16x16x32_bf16 v[88:91],a[40:43],v[236:239],v[88:91]\n"
"s_add_u32 s89,s89,1\n"
"ds_read_b128 v[204:207],v113 offset:3072\n"
"s_add_u32 s21,s21,16\n"
"v_mfma_f32_16x16x32_bf16 v[92:95],a[44:47],v[236:239],v[92:95]\n"
"ds_read_b128 v[208:211],v113 offset:16384\n"
"v_mfma_f32_16x16x32_bf16 v[92:95],a[36:39],v[232:235],v[92:95]\n"
"ds_read_b128 v[212:215],v113 offset:17408\n"
"s_add_u32 s16,s16,4\n"
"v_mfma_f32_16x16x32_bf16 v[92:95],v[44:47],v[228:231],v[92:95]\n"
"ds_read_b128 v[216:219],v113 offset:18432\n"
"buffer_load_dwordx4 v112,s[24:27],s18 offen offset:1024 sc1 lds\n"
"v_mfma_f32_16x16x32_bf16 v[92:95],v[36:39],v[224:227],v[92:95]\n"
"ds_read_b128 v[220:223],v113 offset:19456\n"
"v_mfma_f32_16x16x32_bf16 v[92:95],a[68:71],v[240:243],v[92:95]\n"
"ds_read_b128 v[236:239],v113 offset:7168\n"
"v_mfma_f32_16x16x32_bf16 v[92:95],a[76:79],v[244:247],v[92:95]\n"
"ds_read_b128 v[232:235],v113 offset:6144\n"
"ds_read_b128 v[228:231],v113 offset:5120\n"
"v_mfma_f32_16x16x32_bf16 v[92:95],a[84:87],v[248:251],v[92:95]\n"
"buffer_load_dwordx4 v112,s[24:27],s18 offen offset:2048 sc1 lds\n"
"v_mfma_f32_16x16x32_bf16 v[92:95],a[92:95],v[252:255],v[92:95]\n"
"ds_read_b128 v[224:227],v113 offset:4096\n"
"v_mfma_f32_16x16x32_bf16 v[88:91],a[88:91],v[252:255],v[88:91]\n"
"ds_read_b128 v[252:255],v113 offset:23552\n"
"v_mfma_f32_16x16x32_bf16 v[88:91],a[80:83],v[248:251],v[88:91]\n"
"ds_read_b128 v[248:251],v113 offset:22528\n"
"v_mfma_f32_16x16x32_bf16 v[88:91],a[72:75],v[244:247],v[88:91]\n"
"ds_read_b128 v[244:247],v113 offset:21504\n"
"v_mfma_f32_16x16x32_bf16 v[88:91],a[156:159],v[240:243],v[88:91]\n"
"ds_read_b128 v[240:243],v113 offset:20480\n"
"s_waitcnt lgkmcnt(12)\n"
"v_mfma_f32_16x16x32_bf16 v[80:83],v[128:131],v[192:195],v[80:83]\n"
"buffer_load_dwordx4 v112,s[24:27],s18 offen offset:3072 sc1 lds\n"
"v_mfma_f32_16x16x32_bf16 v[80:83],v[136:139],v[196:199],v[80:83]\n"
"v_mfma_f32_16x16x32_bf16 v[80:83],v[144:147],v[200:203],v[80:83]\n"
"s_add_u32 m0,m0,4096\n"
"v_mfma_f32_16x16x32_bf16 v[80:83],v[152:155],v[204:207],v[80:83]\n"
"buffer_load_dwordx4 v117,s[24:27],s18 offen offset:0 sc1 lds\n"
"v_mfma_f32_16x16x32_bf16 v[84:87],v[156:159],v[204:207],v[84:87]\n"
"ds_read_b128 v[204:207],v113 offset:11264\n"
"s_add_u32 s20,s20,32768\n"
"v_mfma_f32_16x16x32_bf16 v[84:87],v[148:151],v[200:203],v[84:87]\n"
"ds_read_b128 v[200:203],v113 offset:10240\n"
"s_and_b32 s20,s20,0x1ffff\n"
"v_mfma_f32_16x16x32_bf16 v[84:87],v[140:143],v[196:199],v[84:87]\n"
"ds_read_b128 v[196:199],v113 offset:9216\n"
"v_mfma_f32_16x16x32_bf16 v[84:87],v[132:135],v[192:195],v[84:87]\n"
"ds_read_b128 v[192:195],v113 offset:8192\n"
"s_waitcnt lgkmcnt(12)\n"
"v_mfma_f32_16x16x32_bf16 v[68:71],v[132:135],v[208:211],v[68:71]\n"
"buffer_load_dwordx4 v117,s[24:27],s18 offen offset:1024 sc1 lds\n"
"v_mfma_f32_16x16x32_bf16 v[68:71],v[140:143],v[212:215],v[68:71]\n"
"v_mfma_f32_16x16x32_bf16 v[68:71],v[148:151],v[216:219],v[68:71]\n"
"v_mfma_f32_16x16x32_bf16 v[68:71],v[156:159],v[220:223],v[68:71]\n"
"buffer_load_dwordx4 v117,s[24:27],s18 offen offset:2048 sc1 lds\n"
"v_mfma_f32_16x16x32_bf16 v[64:67],v[152:155],v[220:223],v[64:67]\n"
"ds_read_b128 v[220:223],v113 offset:27648\n"
"v_xor_b32 v119,s80,v122\n"
"v_mfma_f32_16x16x32_bf16 v[64:67],v[144:147],v[216:219],v[64:67]\n"
"ds_read_b128 v[216:219],v113 offset:26624\n"
"v_xor_b32 v120,s81,v123\n"
"v_mfma_f32_16x16x32_bf16 v[64:67],v[136:139],v[212:215],v[64:67]\n"
"ds_read_b128 v[212:215],v113 offset:25600\n"
"v_or_b32 v119,v119,v120\n"
"v_mfma_f32_16x16x32_bf16 v[64:67],v[128:131],v[208:211],v[64:67]\n"
"ds_read_b128 v[208:211],v113 offset:24576\n"
"s_waitcnt lgkmcnt(12)\n"
"v_mfma_f32_16x16x32_bf16 v[80:83],v[0:3],v[224:227],v[80:83]\n"
"buffer_load_dwordx4 v117,s[24:27],s18 offen offset:3072 sc1 lds\n"
"v_cmp_ne_u32 s[28:29],0,v119\n"
"v_mfma_f32_16x16x32_bf16 v[80:83],v[8:11],v[228:231],v[80:83]\n"
"s_and_b64 s[14:15],s[14:15],s[28:29]\n"
"s_cselect_b32 s91,s11,2146435072\n"
"s_add_u32 m0,s30,32768\n"
"v_mfma_f32_16x16x32_bf16 v[80:83],v[160:163],v[232:235],v[80:83]\n"
"buffer_load_dwordx2 v[122:123],v115,s[24:27],s91 offen sc1\n"
"v_mfma_f32_16x16x32_bf16 v[80:83],v[168:171],v[236:239],v[80:83]\n"
"buffer_load_dwordx4 v112,s[24:27],s8 offen offset:0 sc1 lds\n"
"v_mfma_f32_16x16x32_bf16 v[84:87],v[172:175],v[236:239],v[84:87]\n"
"ds_read_b128 v[236:239],v113 offset:15360\n"
"v_mfma_f32_16x16x32_bf16 v[84:87],v[164:167],v[232:235],v[84:87]\n"
"ds_read_b128 v[232:235],v113 offset:14336\n"
"v_mfma_f32_16x16x32_bf16 v[84:87],v[12:15],v[228:231],v[84:87]\n"
"ds_read_b128 v[228:231],v113 offset:13312\n"
"v_mfma_f32_16x16x32_bf16 v[84:87],v[4:7],v[224:227],v[84:87]\n"
"s_and_b32 s83,s15,0xff0000\n"
"ds_read_b128 v[224:227],v113 offset:12288\n"
"s_waitcnt lgkmcnt(12)\n"
"v_mfma_f32_16x16x32_bf16 v[68:71],v[4:7],v[240:243],v[68:71]\n"
"buffer_load_dwordx4 v112,s[24:27],s8 offen offset:1024 sc1 lds\n"
"v_mfma_f32_16x16x32_bf16 v[68:71],v[12:15],v[244:247],v[68:71]\n"
"s_cbranch_scc1 .Lfk_es_lkonem\n"
".Lfk_er_lkonem:\n"
"v_mfma_f32_16x16x32_bf16 v[68:71],v[164:167],v[248:251],v[68:71]\n"
"v_mfma_f32_16x16x32_bf16 v[68:71],v[172:175],v[252:255],v[68:71]\n"
"buffer_load_dwordx4 v112,s[24:27],s8 offen offset:2048 sc1 lds\n"
"v_mfma_f32_16x16x32_bf16 v[64:67],v[168:171],v[252:255],v[64:67]\n"
"ds_read_b128 v[252:255],v113 offset:31744\n"
"v_mfma_f32_16x16x32_bf16 v[64:67],v[160:163],v[248:251],v[64:67]\n"
"ds_read_b128 v[248:251],v113 offset:30720\n"
"v_mfma_f32_16x16x32_bf16 v[64:67],v[8:11],v[244:247],v[64:67]\n"
"ds_read_b128 v[244:247],v113 offset:29696\n"
"v_mfma_f32_16x16x32_bf16 v[64:67],v[0:3],v[240:243],v[64:67]\n"
"ds_read_b128 v[240:243],v113 offset:28672\n"
"s_waitcnt lgkmcnt(12)\n"
"v_mfma_f32_16x16x32_bf16 v[80:83],v[176:179],v[192:195],v[80:83]\n"
"s_waitcnt vmcnt(13)\n"
"ds_add_u32 v114,v116\n"
"v_mfma_f32_16x16x32_bf16 v[80:83],v[184:187],v[196:199],v[80:83]\n"
"buffer_load_dwordx4 v112,s[24:27],s8 offen offset:3072 sc1 lds\n"
"v_mfma_f32_16x16x32_bf16 v[80:83],a[48:51],v[200:203],v[80:83]\n"
"s_add_u32 m0,m0,4096\n"
"v_mfma_f32_16x16x32_bf16 v[80:83],a[56:59],v[204:207],v[80:83]\n"
"s_add_u32 s83,s20,32768\n"
"s_and_b32 s83,s83,0x1ffff\n"
"v_add_u32 v113,s83,v112\n"
"v_mfma_f32_16x16x32_bf16 v[84:87],a[60:63],v[204:207],v[84:87]\n"
"buffer_load_dwordx4 v117,s[24:27],s8 offen offset:0 sc1 lds\n"
"v_mfma_f32_16x16x32_bf16 v[84:87],a[52:55],v[200:203],v[84:87]\n"
"s_cmp_eq_u32 s52,0\n"
"v_not_b32 v104,s80\n"
"s_cselect_b32 s9,s9,2146435072\n"
"v_mfma_f32_16x16x32_bf16 v[84:87],v[188:191],v[196:199],v[84:87]\n"
"buffer_load_dwordx2 v[104:105],v115,s[24:27],s9 offen sc1\n"
"v_mfma_f32_16x16x32_bf16 v[84:87],v[180:183],v[192:195],v[84:87]\n"
"buffer_load_dwordx4 v117,s[24:27],s8 offen offset:1024 sc1 lds\n"
"s_waitcnt lgkmcnt(9)\n"
"v_mfma_f32_16x16x32_bf16 v[68:71],v[180:183],v[208:211],v[68:71]\n"
"v_mfma_f32_16x16x32_bf16 v[68:71],v[188:191],v[212:215],v[68:71]\n"
"ds_read_b32 v125,v114\n"
"v_mfma_f32_16x16x32_bf16 v[68:71],a[52:55],v[216:219],v[68:71]\n"
"buffer_load_dwordx4 v117,s[24:27],s8 offen offset:2048 sc1 lds\n"
"v_mfma_f32_16x16x32_bf16 v[68:71],a[60:63],v[220:223],v[68:71]\n"
"v_mfma_f32_16x16x32_bf16 v[64:67],a[56:59],v[220:223],v[64:67]\n"
"v_mfma_f32_16x16x32_bf16 v[64:67],a[48:51],v[216:219],v[64:67]\n"
"s_waitcnt lgkmcnt(0)\n"
"v_cmp_gt_u32 vcc,s16,v125\n"
"v_mfma_f32_16x16x32_bf16 v[64:67],v[184:187],v[212:215],v[64:67]\n"
"buffer_load_dwordx4 v117,s[24:27],s8 offen offset:3072 sc1 lds\n"
"v_mfma_f32_16x16x32_bf16 v[64:67],v[176:179],v[208:211],v[64:67]\n"
"s_lshl_b64 exec,1,s89\n"
"buffer_atomic_add v100,off,s[24:27],s10 sc0\n"
"s_mov_b64 exec,-1\n"
"s_add_u32 s8,s8,1048576\n"
"s_cbranch_vccnz .Lfk_es_mone\n"
".Lfk_er_mone:\n"
"v_mfma_f32_16x16x32_bf16 v[80:83],v[48:51],v[224:227],v[80:83]\n"
"ds_read_b128 v[192:195],v113 offset:0\n"
"v_mfma_f32_16x16x32_bf16 v[80:83],v[56:59],v[228:231],v[80:83]\n"
"ds_read_b128 v[196:199],v113 offset:1024\n"
"v_mfma_f32_16x16x32_bf16 v[80:83],v[16:19],v[232:235],v[80:83]\n"
"ds_read_b128 v[200:203],v113 offset:2048\n"
"s_add_u32 s89,s89,1\n"
"v_mfma_f32_16x16x32_bf16 v[80:83],v[24:27],v[236:239],v[80:83]\n"
"ds_read_b128 v[204:207],v113 offset:3072\n"
"s_add_u32 s21,s21,8\n"
"v_mfma_f32_16x16x32_bf16 v[84:87],v[28:31],v[236:239],v[84:87]\n"
"ds_read_b128 v[208:211],v113 offset:4096\n"
"v_mfma_f32_16x16x32_bf16 v[84:87],v[20:23],v[232:235],v[84:87]\n"
"ds_read_b128 v[212:215],v113 offset:5120\n"
"s_add_u32 s16,s16,4\n"
"v_mfma_f32_16x16x32_bf16 v[84:87],v[60:63],v[228:231],v[84:87]\n"
"ds_read_b128 v[216:219],v113 offset:6144\n"
"v_mfma_f32_16x16x32_bf16 v[84:87],v[52:55],v[224:227],v[84:87]\n"
"ds_read_b128 v[220:223],v113 offset:7168\n"
"v_mfma_f32_16x16x32_bf16 v[68:71],v[52:55],v[240:243],v[68:71]\n"
"ds_read_b128 v[236:239],v113 offset:11264\n"
"v_mfma_f32_16x16x32_bf16 v[68:71],v[60:63],v[244:247],v[68:71]\n"
"ds_read_b128 v[232:235],v113 offset:10240\n"
"v_mfma_f32_16x16x32_bf16 v[68:71],v[20:23],v[248:251],v[68:71]\n"
"ds_read_b128 v[228:231],v113 offset:9216\n"
"ds_read_b128 v[224:227],v113 offset:8192\n"
"v_mfma_f32_16x16x32_bf16 v[68:71],v[28:31],v[252:255],v[68:71]\n"
"v_cvt_pk_bf16_f32 v80,v80,v81\n"
"v_cvt_pk_bf16_f32 v81,v82,v83\n"
"v_mfma_f32_16x16x32_bf16 v[64:67],v[24:27],v[252:255],v[64:67]\n"
"ds_read_b128 v[252:255],v113 offset:15360\n"
"v_cvt_pk_bf16_f32 v82,v84,v85\n"
"v_cvt_pk_bf16_f32 v83,v86,v87\n"
"v_mfma_f32_16x16x32_bf16 v[64:67],v[16:19],v[248:251],v[64:67]\n"
"ds_read_b128 v[248:251],v113 offset:14336\n"
"v_max_u32 v80,1,v80\n"
"v_max_u32 v81,1,v81\n"
"v_mfma_f32_16x16x32_bf16 v[64:67],v[56:59],v[244:247],v[64:67]\n"
"ds_read_b128 v[244:247],v113 offset:13312\n"
"v_max_u32 v82,1,v82\n"
"v_max_u32 v83,1,v83\n"
"v_mfma_f32_16x16x32_bf16 v[64:67],v[48:51],v[240:243],v[64:67]\n"
"ds_read_b128 v[240:243],v113 offset:12288\n"
"buffer_store_dwordx4 v[80:83],v118,s[24:27],s22 offen sc1\n"
"s_waitcnt lgkmcnt(12)\n"
"v_mfma_f32_16x16x32_bf16 v[72:75],v[128:131],v[192:195],v[72:75]\n"
"s_add_u32 m0,s30,s20\n"
"s_add_u32 s22,s22,262144\n"
"v_mfma_f32_16x16x32_bf16 v[72:75],v[136:139],v[196:199],v[72:75]\n"
"buffer_load_dwordx4 v112,s[24:27],s8 offen offset:0 sc1 lds\n"
"v_mfma_f32_16x16x32_bf16 v[72:75],v[144:147],v[200:203],v[72:75]\n"
"v_mfma_f32_16x16x32_bf16 v[72:75],v[152:155],v[204:207],v[72:75]\n"
"v_cvt_pk_bf16_f32 v64,v64,v65\n"
"v_mfma_f32_16x16x32_bf16 v[76:79],v[156:159],v[204:207],v[76:79]\n"
"ds_read_b128 v[204:207],v113 offset:19456\n"
"s_add_u32 s20,s20,32768\n"
"v_mfma_f32_16x16x32_bf16 v[76:79],v[148:151],v[200:203],v[76:79]\n"
"ds_read_b128 v[200:203],v113 offset:18432\n"
"s_and_b32 s20,s20,0x1ffff\n"
"v_mfma_f32_16x16x32_bf16 v[76:79],v[140:143],v[196:199],v[76:79]\n"
"ds_read_b128 v[196:199],v113 offset:17408\n"
"v_cvt_pk_bf16_f32 v65,v66,v67\n"
"v_mfma_f32_16x16x32_bf16 v[76:79],v[132:135],v[192:195],v[76:79]\n"
"ds_read_b128 v[192:195],v113 offset:16384\n"
"v_cvt_pk_bf16_f32 v66,v68,v69\n"
"s_waitcnt lgkmcnt(12)\n"
"v_mfma_f32_16x16x32_bf16 v[76:79],v[4:7],v[208:211],v[76:79]\n"
"buffer_load_dwordx4 v112,s[24:27],s8 offen offset:1024 sc1 lds\n"
"v_cvt_pk_bf16_f32 v67,v70,v71\n"
"v_mfma_f32_16x16x32_bf16 v[76:79],v[12:15],v[212:215],v[76:79]\n"
"v_max_u32 v64,1,v64\n"
"v_max_u32 v65,1,v65\n"
"v_max_u32 v66,1,v66\n"
"v_max_u32 v67,1,v67\n"
"v_mfma_f32_16x16x32_bf16 v[76:79],v[164:167],v[216:219],v[76:79]\n"
"buffer_store_dwordx4 v[64:67],v118,s[24:27],s22 offen sc1\n"
"v_mfma_f32_16x16x32_bf16 v[76:79],v[172:175],v[220:223],v[76:79]\n"
"buffer_load_dwordx4 v112,s[24:27],s8 offen offset:2048 sc1 lds\n"
"v_mfma_f32_16x16x32_bf16 v[72:75],v[168:171],v[220:223],v[72:75]\n"
"s_add_u32 s22,s22,262144\n"
"ds_read_b128 v[220:223],v113 offset:23552\n"
"v_mfma_f32_16x16x32_bf16 v[72:75],v[160:163],v[216:219],v[72:75]\n"
"ds_read_b128 v[216:219],v113 offset:22528\n"
"v_mfma_f32_16x16x32_bf16 v[72:75],v[8:11],v[212:215],v[72:75]\n"
"ds_read_b128 v[212:215],v113 offset:21504\n"
"v_mfma_f32_16x16x32_bf16 v[72:75],v[0:3],v[208:211],v[72:75]\n"
"ds_read_b128 v[208:211],v113 offset:20480\n"
"s_waitcnt lgkmcnt(12)\n"
"v_mfma_f32_16x16x32_bf16 v[72:75],v[176:179],v[224:227],v[72:75]\n"
"buffer_load_dwordx4 v112,s[24:27],s8 offen offset:3072 sc1 lds\n"
"v_mfma_f32_16x16x32_bf16 v[72:75],v[184:187],v[228:231],v[72:75]\n"
"v_xor_b32 v119,s80,v122\n"
"v_xor_b32 v120,s81,v123\n"
"v_mfma_f32_16x16x32_bf16 v[72:75],a[48:51],v[232:235],v[72:75]\n"
"v_or_b32 v119,v119,v120\n"
"v_cmp_ne_u32 s[28:29],0,v119\n"
"v_mfma_f32_16x16x32_bf16 v[72:75],a[56:59],v[236:239],v[72:75]\n"
"s_and_b64 s[14:15],s[14:15],s[28:29]\n"
"v_mfma_f32_16x16x32_bf16 v[76:79],a[60:63],v[236:239],v[76:79]\n"
"ds_read_b128 v[236:239],v113 offset:27648\n"
"v_mfma_f32_16x16x32_bf16 v[76:79],a[52:55],v[232:235],v[76:79]\n"
"ds_read_b128 v[232:235],v113 offset:26624\n"
"v_mfma_f32_16x16x32_bf16 v[76:79],v[188:191],v[228:231],v[76:79]\n"
"ds_read_b128 v[228:231],v113 offset:25600\n"
"v_mfma_f32_16x16x32_bf16 v[76:79],v[180:183],v[224:227],v[76:79]\n"
"s_add_u32 m0,m0,4096\n"
"ds_read_b128 v[224:227],v113 offset:24576\n"
"s_waitcnt lgkmcnt(12)\n"
"v_mfma_f32_16x16x32_bf16 v[76:79],v[52:55],v[240:243],v[76:79]\n"
"buffer_load_dwordx4 v117,s[24:27],s8 offen offset:0 sc1 lds\n"
"v_mfma_f32_16x16x32_bf16 v[76:79],v[60:63],v[244:247],v[76:79]\n"
"s_and_b32 s83,s15,0xff000000\n"
"s_cbranch_scc1 .Lfk_es_lktwom\n"
".Lfk_er_lktwom:\n"
"v_mfma_f32_16x16x32_bf16 v[76:79],v[20:23],v[248:251],v[76:79]\n"
"v_xor_b32 v119,s80,v104\n"
"v_xor_b32 v120,s81,v105\n"
"v_or_b32 v119,v119,v120\n"
"v_mfma_f32_16x16x32_bf16 v[76:79],v[28:31],v[252:255],v[76:79]\n"
"buffer_load_dwordx4 v117,s[24:27],s8 offen offset:1024 sc1 lds\n"
"v_cmp_ne_u32 s[28:29],0,v119\n"
"v_mfma_f32_16x16x32_bf16 v[72:75],v[24:27],v[252:255],v[72:75]\n"
"s_cmp_eq_u32 s52,0\n"
"ds_read_b128 v[252:255],v113 offset:31744\n"
"s_cselect_b64 s[14:15],-1,0\n"
"v_mfma_f32_16x16x32_bf16 v[72:75],v[16:19],v[248:251],v[72:75]\n"
"s_mov_b32 s11,s9\n"
"ds_read_b128 v[248:251],v113 offset:30720\n"
"s_waitcnt vmcnt(18)\n"
"v_not_b32 v122,s80\n"
"v_mfma_f32_16x16x32_bf16 v[72:75],v[56:59],v[244:247],v[72:75]\n"
"s_and_b64 s[14:15],s[14:15],s[28:29]\n"
"s_cselect_b32 s91,s11,2146435072\n"
"ds_read_b128 v[244:247],v113 offset:29696\n"
"buffer_load_dwordx2 v[122:123],v115,s[24:27],s91 offen sc1\n"
"v_mfma_f32_16x16x32_bf16 v[72:75],v[48:51],v[240:243],v[72:75]\n"
"ds_read_b128 v[240:243],v113 offset:28672\n"
"s_waitcnt lgkmcnt(12)\n"
"v_mfma_f32_16x16x32_bf16 v[88:91],v[128:131],v[192:195],v[88:91]\n"
"ds_write_b32 v101,v100\n"
"v_mfma_f32_16x16x32_bf16 v[88:91],v[136:139],v[196:199],v[88:91]\n"
"ds_add_u32 v114,v116\n"
"v_mfma_f32_16x16x32_bf16 v[88:91],v[144:147],v[200:203],v[88:91]\n"
"buffer_load_dwordx4 v117,s[24:27],s8 offen offset:2048 sc1 lds\n"
"v_mfma_f32_16x16x32_bf16 v[88:91],v[152:155],v[204:207],v[88:91]\n"
"s_add_u32 s83,s20,32768\n"
"s_and_b32 s83,s83,0x1ffff\n"
"v_add_u32 v113,s83,v112\n"
"v_mfma_f32_16x16x32_bf16 v[92:95],v[156:159],v[204:207],v[92:95]\n"
"v_cvt_pk_bf16_f32 v72,v72,v73\n"
"v_cvt_pk_bf16_f32 v73,v74,v75\n"
"v_cvt_pk_bf16_f32 v74,v76,v77\n"
"v_mfma_f32_16x16x32_bf16 v[92:95],v[148:151],v[200:203],v[92:95]\n"
"v_cvt_pk_bf16_f32 v75,v78,v79\n"
"v_max_u32 v72,1,v72\n"
"v_max_u32 v73,1,v73\n"
"v_mfma_f32_16x16x32_bf16 v[92:95],v[140:143],v[196:199],v[92:95]\n"
"v_max_u32 v74,1,v74\n"
"v_max_u32 v75,1,v75\n"
"v_mfma_f32_16x16x32_bf16 v[92:95],v[132:135],v[192:195],v[92:95]\n"
"buffer_store_dwordx4 v[72:75],v118,s[24:27],s22 offen sc1\n"
"s_waitcnt lgkmcnt(10)\n"
"v_mfma_f32_16x16x32_bf16 v[92:95],v[4:7],v[208:211],v[92:95]\n"
"buffer_load_dwordx4 v117,s[24:27],s8 offen offset:3072 sc1 lds\n"
"v_mfma_f32_16x16x32_bf16 v[92:95],v[12:15],v[212:215],v[92:95]\n"
"s_add_u32 s22,s22,262144\n"
"ds_read_b32 v125,v114\n"
"v_mfma_f32_16x16x32_bf16 v[92:95],v[164:167],v[216:219],v[92:95]\n"
"v_mfma_f32_16x16x32_bf16 v[92:95],v[172:175],v[220:223],v[92:95]\n"
"v_mfma_f32_16x16x32_bf16 v[88:91],v[168:171],v[220:223],v[88:91]\n"
"s_lshl_b64 exec,1,s89\n"
"buffer_atomic_add v100,off,s[24:27],s10 sc0\n"
"s_mov_b64 exec,-1\n"
"v_mfma_f32_16x16x32_bf16 v[88:91],v[160:163],v[216:219],v[88:91]\n"
"s_waitcnt lgkmcnt(0)\n"
"v_cmp_gt_u32 vcc,s16,v125\n"
"v_mfma_f32_16x16x32_bf16 v[88:91],v[8:11],v[212:215],v[88:91]\n"
"s_add_u32 s8,s8,1048576\n"
"v_mfma_f32_16x16x32_bf16 v[88:91],v[0:3],v[208:211],v[88:91]\n"
"s_cbranch_vccnz .Lfk_es_mtwo\n"
".Lfk_er_mtwo:\n"
".Lfk_l4:\n"
"v_mfma_f32_16x16x32_bf16 v[88:91],v[176:179],v[224:227],v[88:91]\n"
"ds_read_b128 v[192:195],v113 offset:0\n"
"v_mfma_f32_16x16x32_bf16 v[88:91],v[184:187],v[228:231],v[88:91]\n"
"ds_read_b128 v[196:199],v113 offset:1024\n"
"v_mfma_f32_16x16x32_bf16 v[88:91],a[48:51],v[232:235],v[88:91]\n"
"ds_read_b128 v[200:203],v113 offset:2048\n"
"s_add_u32 s89,s89,1\n"
"v_mfma_f32_16x16x32_bf16 v[88:91],a[56:59],v[236:239],v[88:91]\n"
"ds_read_b128 v[204:207],v113 offset:3072\n"
"v_mfma_f32_16x16x32_bf16 v[92:95],a[60:63],v[236:239],v[92:95]\n"
"ds_read_b128 v[208:211],v113 offset:4096\n"
"v_mfma_f32_16x16x32_bf16 v[92:95],a[52:55],v[232:235],v[92:95]\n"
"ds_read_b128 v[212:215],v113 offset:5120\n"
"s_add_u32 s16,s16,4\n"
"v_mfma_f32_16x16x32_bf16 v[92:95],v[188:191],v[228:231],v[92:95]\n"
"ds_read_b128 v[216:219],v113 offset:6144\n"
"v_mfma_f32_16x16x32_bf16 v[92:95],v[180:183],v[224:227],v[92:95]\n"
"ds_read_b128 v[220:223],v113 offset:7168\n"
"v_mfma_f32_16x16x32_bf16 v[92:95],v[52:55],v[240:243],v[92:95]\n"
"ds_read_b128 v[236:239],v113 offset:11264\n"
"v_mfma_f32_16x16x32_bf16 v[92:95],v[60:63],v[244:247],v[92:95]\n"
"ds_read_b128 v[232:235],v113 offset:10240\n"
"v_mfma_f32_16x16x32_bf16 v[92:95],v[20:23],v[248:251],v[92:95]\n"
"ds_read_b128 v[228:231],v113 offset:9216\n"
"v_mfma_f32_16x16x32_bf16 v[92:95],v[28:31],v[252:255],v[92:95]\n"
"ds_read_b128 v[224:227],v113 offset:8192\n"
"v_mfma_f32_16x16x32_bf16 v[88:91],v[24:27],v[252:255],v[88:91]\n"
"ds_read_b128 v[252:255],v113 offset:15360\n"
"v_mfma_f32_16x16x32_bf16 v[88:91],v[16:19],v[248:251],v[88:91]\n"
"ds_read_b128 v[248:251],v113 offset:14336\n"
"v_mfma_f32_16x16x32_bf16 v[88:91],v[56:59],v[244:247],v[88:91]\n"
"ds_read_b128 v[244:247],v113 offset:13312\n"
"v_mfma_f32_16x16x32_bf16 v[88:91],v[48:51],v[240:243],v[88:91]\n"
"ds_read_b128 v[240:243],v113 offset:12288\n"
"s_waitcnt lgkmcnt(12)\n"
"v_mfma_f32_16x16x32_bf16 v[80:83],a[224:227],v[192:195],0\n"
"s_add_u32 m0,s30,s20\n"
"ds_read_b32 v103,v102\n"
"v_mfma_f32_16x16x32_bf16 v[80:83],a[232:235],v[196:199],v[80:83]\n"
"buffer_load_dwordx4 v112,s[24:27],s8 offen offset:0 sc1 lds\n"
"v_mfma_f32_16x16x32_bf16 v[80:83],a[240:243],v[200:203],v[80:83]\n"
"v_add_u32 v102,4,v102\n"
"v_and_b32 v102,0x200ff,v102\n"
"v_mfma_f32_16x16x32_bf16 v[80:83],a[248:251],v[204:207],v[80:83]\n"
"v_cvt_pk_bf16_f32 v88,v88,v89\n"
"v_cvt_pk_bf16_f32 v89,v90,v91\n"
"v_mfma_f32_16x16x32_bf16 v[84:87],a[252:255],v[204:207],0\n"
"ds_read_b128 v[204:207],v113 offset:19456\n"
"s_add_u32 s20,s20,32768\n"
"v_mfma_f32_16x16x32_bf16 v[84:87],a[244:247],v[200:203],v[84:87]\n"
"ds_read_b128 v[200:203],v113 offset:18432\n"
"s_and_b32 s20,s20,0x1ffff\n"
"v_mfma_f32_16x16x32_bf16 v[84:87],a[236:239],v[196:199],v[84:87]\n"
"ds_read_b128 v[196:199],v113 offset:17408\n"
"v_cvt_pk_bf16_f32 v90,v92,v93\n"
"v_mfma_f32_16x16x32_bf16 v[84:87],a[228:231],v[192:195],v[84:87]\n"
"ds_read_b128 v[192:195],v113 offset:16384\n"
"s_waitcnt lgkmcnt(13)\n"
"v_mfma_f32_16x16x32_bf16 v[84:87],a[4:7],v[208:211],v[84:87]\n"
"buffer_load_dwordx4 v112,s[24:27],s8 offen offset:1024 sc1 lds\n"
"v_mfma_f32_16x16x32_bf16 v[84:87],a[12:15],v[212:215],v[84:87]\n"
"v_cvt_pk_bf16_f32 v91,v94,v95\n"
"v_max_u32 v88,1,v88\n"
"v_mfma_f32_16x16x32_bf16 v[84:87],a[20:23],v[216:219],v[84:87]\n"
"v_max_u32 v89,1,v89\n"
"v_max_u32 v90,1,v90\n"
"v_mfma_f32_16x16x32_bf16 v[84:87],a[28:31],v[220:223],v[84:87]\n"
"buffer_load_dwordx4 v112,s[24:27],s8 offen offset:2048 sc1 lds\n"
"v_mfma_f32_16x16x32_bf16 v[80:83],a[24:27],v[220:223],v[80:83]\n"
"ds_read_b128 v[220:223],v113 offset:23552\n"
"v_max_u32 v91,1,v91\n"
"v_mfma_f32_16x16x32_bf16 v[80:83],a[16:19],v[216:219],v[80:83]\n"
"ds_read_b128 v[216:219],v113 offset:22528\n"
"buffer_store_dwordx4 v[88:91],v118,s[24:27],s53 offen sc1\n"
"v_mfma_f32_16x16x32_bf16 v[80:83],a[8:11],v[212:215],v[80:83]\n"
"ds_read_b128 v[212:215],v113 offset:21504\n"
"v_mfma_f32_16x16x32_bf16 v[80:83],a[0:3],v[208:211],v[80:83]\n"
"ds_read_b128 v[208:211],v113 offset:20480\n"
"s_waitcnt lgkmcnt(13)\n"
"v_mfma_f32_16x16x32_bf16 v[80:83],v[32:35],v[224:227],v[80:83]\n"
"v_mfma_f32_16x16x32_bf16 v[80:83],v[40:43],v[228:231],v[80:83]\n"
"buffer_load_dwordx4 v112,s[24:27],s8 offen offset:3072 sc1 lds\n"
"v_mfma_f32_16x16x32_bf16 v[80:83],a[32:35],v[232:235],v[80:83]\n"
"s_waitcnt lgkmcnt(8)\n"
"v_readfirstlane_b32 s22,v103\n"
"v_mfma_f32_16x16x32_bf16 v[80:83],a[40:43],v[236:239],v[80:83]\n"
"v_mfma_f32_16x16x32_bf16 v[84:87],a[44:47],v[236:239],v[84:87]\n"
"ds_read_b128 v[236:239],v113 offset:27648\n"
"s_and_b32 s21,s22,0x38\n"
"v_mfma_f32_16x16x32_bf16 v[84:87],a[36:39],v[232:235],v[84:87]\n"
"ds_read_b128 v[232:235],v113 offset:26624\n"
"v_mfma_f32_16x16x32_bf16 v[84:87],v[44:47],v[228:231],v[84:87]\n"
"ds_read_b128 v[228:231],v113 offset:25600\n"
"v_mfma_f32_16x16x32_bf16 v[84:87],v[36:39],v[224:227],v[84:87]\n"
"ds_read_b128 v[224:227],v113 offset:24576\n"
"s_add_u32 m0,m0,4096\n"
"v_mfma_f32_16x16x32_bf16 v[84:87],a[68:71],v[240:243],v[84:87]\n"
"buffer_load_dwordx4 v117,s[24:27],s8 offen offset:0 sc1 lds\n"
"v_mfma_f32_16x16x32_bf16 v[84:87],a[76:79],v[244:247],v[84:87]\n"
"s_cmp_lg_u64 s[14:15],0\n"
"s_cbranch_scc1 .Lfk_es_lkloopm\n"
".Lfk_er_lkloopm:\n"
"v_mfma_f32_16x16x32_bf16 v[84:87],a[84:87],v[248:251],v[84:87]\n"
"v_mfma_f32_16x16x32_bf16 v[84:87],a[92:95],v[252:255],v[84:87]\n"
"buffer_load_dwordx4 v117,s[24:27],s8 offen offset:1024 sc1 lds\n"
"v_mfma_f32_16x16x32_bf16 v[80:83],a[88:91],v[252:255],v[80:83]\n"
"ds_read_b128 v[252:255],v113 offset:31744\n"
"s_mov_b32 s53,s56\n"
"v_mfma_f32_16x16x32_bf16 v[80:83],a[80:83],v[248:251],v[80:83]\n"
"ds_read_b128 v[248:251],v113 offset:30720\n"
"s_mov_b32 s56,s58\n"
"v_mfma_f32_16x16x32_bf16 v[80:83],a[72:75],v[244:247],v[80:83]\n"
"ds_read_b128 v[244:247],v113 offset:29696\n"
"s_mov_b32 s58,s59\n"
"v_mfma_f32_16x16x32_bf16 v[80:83],a[156:159],v[240:243],v[80:83]\n"
"ds_read_b128 v[240:243],v113 offset:28672\n"
"s_waitcnt lgkmcnt(12)\n"
"v_mfma_f32_16x16x32_bf16 v[88:91],v[128:131],v[192:195],v[80:83]\n"
"s_waitcnt vmcnt(17)\n"
"ds_write_b32 v101,v100\n"
"v_mfma_f32_16x16x32_bf16 v[88:91],v[136:139],v[196:199],v[88:91]\n"
"ds_add_u32 v114,v116\n"
"v_mfma_f32_16x16x32_bf16 v[88:91],v[144:147],v[200:203],v[88:91]\n"
"buffer_load_dwordx4 v117,s[24:27],s8 offen offset:2048 sc1 lds\n"
"v_mfma_f32_16x16x32_bf16 v[88:91],v[152:155],v[204:207],v[88:91]\n"
"s_add_u32 s83,s20,32768\n"
"s_and_b32 s83,s83,0x1ffff\n"
"v_add_u32 v113,s83,v112\n"
"v_mfma_f32_16x16x32_bf16 v[92:95],v[156:159],v[204:207],v[84:87]\n"
"s_cmp_lt_u32 s22,64\n"
"s_cselect_b32 s17,s16,-1\n"
"v_mfma_f32_16x16x32_bf16 v[92:95],v[148:151],v[200:203],v[92:95]\n"
"v_mfma_f32_16x16x32_bf16 v[92:95],v[140:143],v[196:199],v[92:95]\n"
"v_mfma_f32_16x16x32_bf16 v[92:95],v[132:135],v[192:195],v[92:95]\n"
"s_waitcnt lgkmcnt(10)\n"
"v_mfma_f32_16x16x32_bf16 v[92:95],v[4:7],v[208:211],v[92:95]\n"
"buffer_load_dwordx4 v117,s[24:27],s8 offen offset:3072 sc1 lds\n"
"v_mfma_f32_16x16x32_bf16 v[92:95],v[12:15],v[212:215],v[92:95]\n"
"s_lshr_b32 s59,s8,2\n"
"ds_read_b32 v125,v114\n"
"s_lshl_b32 s8,s22,17\n"
"v_mfma_f32_16x16x32_bf16 v[92:95],v[164:167],v[216:219],v[92:95]\n"
"s_add_u32 s8,s8,s31\n"
"v_mfma_f32_16x16x32_bf16 v[92:95],v[172:175],v[220:223],v[92:95]\n"
"v_mfma_f32_16x16x32_bf16 v[88:91],v[168:171],v[220:223],v[88:91]\n"
"s_lshl_b64 exec,1,s89\n"
"buffer_atomic_add v100,off,s[24:27],s10 sc0\n"
"s_mov_b64 exec,-1\n"
"v_mfma_f32_16x16x32_bf16 v[88:91],v[160:163],v[216:219],v[88:91]\n"
"s_waitcnt lgkmcnt(0)\n"
"v_cmp_gt_u32 vcc,s17,v125\n"
"v_mfma_f32_16x16x32_bf16 v[88:91],v[8:11],v[212:215],v[88:91]\n"
"v_mfma_f32_16x16x32_bf16 v[88:91],v[0:3],v[208:211],v[88:91]\n"
"s_cbranch_vccz .Lfk_l4\n"
"s_call_b64 s[94:95],.Lfk_meet4\n"
"s_cmp_lt_u32 s22,64\n"
"s_cbranch_scc1 .Lfk_l4\n"
"ds_read_b128 v[192:195],v113 offset:0\n"
"v_mfma_f32_16x16x32_bf16 v[88:91],v[176:179],v[224:227],v[88:91]\n"
"s_getpc_b64 s[96:97]\n"
".Lfk_rt2:\n"
"s_add_u32 s96,s96,.Lfk_tail-.Lfk_rt2\n"
"s_addc_u32 s97,s97,0\n"
"s_mov_b32 s98,.Lfk_cold-.Lfk_tail\n"
"s_mov_b32 s99,0x20000\n"
"s_lshr_b32 s82,s100,3\n"
"s_lshl2_add_u32 s82,s82,s52\n"
"v_lshrrev_b32 v121,2,v112\n"
"s_lshl_b32 s82,s82,8\n"
"v_mfma_f32_16x16x32_bf16 v[88:91],v[184:187],v[228:231],v[88:91]\n"
"buffer_load_dword v120,v121,s[96:99],s82 offen\n"
"ds_read_b128 v[196:199],v113 offset:1024\n"
"v_mfma_f32_16x16x32_bf16 v[88:91],a[48:51],v[232:235],v[88:91]\n"
"s_mov_b32 s77,0\n"
"ds_read_b128 v[200:203],v113 offset:2048\n"
"s_and_b32 s91,s52,1\n"
"v_mfma_f32_16x16x32_bf16 v[88:91],a[56:59],v[236:239],v[88:91]\n"
"ds_read_b128 v[204:207],v113 offset:3072\n"
"s_lshl_b32 s91,s91,7\n"
"v_mfma_f32_16x16x32_bf16 v[92:95],a[60:63],v[236:239],v[92:95]\n"
"ds_read_b128 v[208:211],v113 offset:4096\n"
"v_xor_b32 v96,s91,v112\n"
"v_mfma_f32_16x16x32_bf16 v[92:95],a[52:55],v[232:235],v[92:95]\n"
"ds_read_b128 v[212:215],v113 offset:5120\n"
"s_add_u32 s16,s16,4\n"
"v_mfma_f32_16x16x32_bf16 v[92:95],v[188:191],v[228:231],v[92:95]\n"
"ds_read_b128 v[216:219],v113 offset:6144\n"
"v_xor_b32 v97,32,v96\n"
"v_mfma_f32_16x16x32_bf16 v[92:95],v[180:183],v[224:227],v[92:95]\n"
"ds_read_b128 v[220:223],v113 offset:7168\n"
"v_xor_b32 v98,64,v96\n"
"v_mfma_f32_16x16x32_bf16 v[92:95],v[52:55],v[240:243],v[92:95]\n"
"ds_read_b128 v[236:239],v113 offset:11264\n"
"v_xor_b32 v99,96,v96\n"
"v_mfma_f32_16x16x32_bf16 v[92:95],v[60:63],v[244:247],v[92:95]\n"
"ds_read_b128 v[232:235],v113 offset:10240\n"
"s_mov_b32 s38,4194304\n"
"v_mfma_f32_16x16x32_bf16 v[92:95],v[20:23],v[248:251],v[92:95]\n"
"ds_read_b128 v[228:231],v113 offset:9216\n"
"s_mov_b32 s39,0x20000\n"
"v_mfma_f32_16x16x32_bf16 v[92:95],v[28:31],v[252:255],v[92:95]\n"
"ds_read_b128 v[224:227],v113 offset:8192\n"
"s_mov_b32 s46,2048\n"
"v_mfma_f32_16x16x32_bf16 v[88:91],v[24:27],v[252:255],v[88:91]\n"
"ds_read_b128 v[252:255],v113 offset:15360\n"
"s_mov_b32 s47,0x20000\n"
"v_mfma_f32_16x16x32_bf16 v[88:91],v[16:19],v[248:251],v[88:91]\n"
"ds_read_b128 v[248:251],v113 offset:14336\n"
"s_mov_b32 s50,4194304\n"
"v_mfma_f32_16x16x32_bf16 v[88:91],v[56:59],v[244:247],v[88:91]\n"
"ds_read_b128 v[244:247],v113 offset:13312\n"
"s_mov_b32 s51,0x20000\n"
"v_mfma_f32_16x16x32_bf16 v[88:91],v[48:51],v[240:243],v[88:91]\n"
"ds_read_b128 v[240:243],v113 offset:12288\n"
"s_waitcnt lgkmcnt(12)\n"
"v_mfma_f32_16x16x32_bf16 v[80:83],a[224:227],v[192:195],0\n"
"s_and_b32 s60,s100,7\n"
"s_lshl_b32 s60,s60,5\n"
"v_mfma_f32_16x16x32_bf16 v[80:83],a[232:235],v[196:199],v[80:83]\n"
"s_lshr_b32 s73,s100,3\n"
"s_add_u32 s73,s73,s60\n"
"v_mfma_f32_16x16x32_bf16 v[80:83],a[240:243],v[200:203],v[80:83]\n"
"s_bfe_u32 s60,s100,0x00003\n"
"s_xor_b32 s60,s60,s100\n"
"v_mfma_f32_16x16x32_bf16 v[80:83],a[248:251],v[204:207],v[80:83]\n"
"v_cvt_pk_bf16_f32 v88,v88,v89\n"
"v_cvt_pk_bf16_f32 v89,v90,v91\n"
"v_mfma_f32_16x16x32_bf16 v[84:87],a[252:255],v[204:207],0\n"
"ds_read_b128 v[204:207],v113 offset:19456\n"
"s_add_u32 s20,s20,32768\n"
"v_mfma_f32_16x16x32_bf16 v[84:87],a[244:247],v[200:203],v[84:87]\n"
"ds_read_b128 v[200:203],v113 offset:18432\n"
"s_and_b32 s20,s20,0x1ffff\n"
"v_mfma_f32_16x16x32_bf16 v[84:87],a[236:239],v[196:199],v[84:87]\n"
"ds_read_b128 v[196:199],v113 offset:17408\n"
"v_cvt_pk_bf16_f32 v90,v92,v93\n"
"v_mfma_f32_16x16x32_bf16 v[84:87],a[228:231],v[192:195],v[84:87]\n"
"ds_read_b128 v[192:195],v113 offset:16384\n"
"s_waitcnt lgkmcnt(12)\n"
"v_mfma_f32_16x16x32_bf16 v[84:87],a[4:7],v[208:211],v[84:87]\n"
"v_cvt_pk_bf16_f32 v91,v94,v95\n"
"v_max_u32 v88,1,v88\n"
"v_mfma_f32_16x16x32_bf16 v[84:87],a[12:15],v[212:215],v[84:87]\n"
"v_max_u32 v89,1,v89\n"
"v_max_u32 v90,1,v90\n"
"v_max_u32 v91,1,v91\n"
"v_mfma_f32_16x16x32_bf16 v[84:87],a[20:23],v[216:219],v[84:87]\n"
"buffer_store_dwordx4 v[88:91],v118,s[24:27],s53 offen sc1\n"
"v_mfma_f32_16x16x32_bf16 v[84:87],a[28:31],v[220:223],v[84:87]\n"
"v_mfma_f32_16x16x32_bf16 v[80:83],a[24:27],v[220:223],v[80:83]\n"
"ds_read_b128 v[220:223],v113 offset:23552\n"
"v_mfma_f32_16x16x32_bf16 v[80:83],a[16:19],v[216:219],v[80:83]\n"
"ds_read_b128 v[216:219],v113 offset:22528\n"
"v_mfma_f32_16x16x32_bf16 v[80:83],a[8:11],v[212:215],v[80:83]\n"
"ds_read_b128 v[212:215],v113 offset:21504\n"
"v_mfma_f32_16x16x32_bf16 v[80:83],a[0:3],v[208:211],v[80:83]\n"
"ds_read_b128 v[208:211],v113 offset:20480\n"
"s_waitcnt lgkmcnt(12)\n"
"v_mfma_f32_16x16x32_bf16 v[80:83],v[32:35],v[224:227],v[80:83]\n"
"v_mfma_f32_16x16x32_bf16 v[80:83],v[40:43],v[228:231],v[80:83]\n"
"v_mfma_f32_16x16x32_bf16 v[80:83],a[32:35],v[232:235],v[80:83]\n"
"v_mfma_f32_16x16x32_bf16 v[80:83],a[40:43],v[236:239],v[80:83]\n"
"v_mfma_f32_16x16x32_bf16 v[84:87],a[44:47],v[236:239],v[84:87]\n"
"ds_read_b128 v[236:239],v113 offset:27648\n"
"v_mfma_f32_16x16x32_bf16 v[84:87],a[36:39],v[232:235],v[84:87]\n"
"ds_read_b128 v[232:235],v113 offset:26624\n"
"v_mfma_f32_16x16x32_bf16 v[84:87],v[44:47],v[228:231],v[84:87]\n"
"ds_read_b128 v[228:231],v113 offset:25600\n"
"v_mfma_f32_16x16x32_bf16 v[84:87],v[36:39],v[224:227],v[84:87]\n"
"ds_read_b128 v[224:227],v113 offset:24576\n"
"s_waitcnt lgkmcnt(12)\n"
"v_mfma_f32_16x16x32_bf16 v[84:87],a[68:71],v[240:243],v[84:87]\n"
"s_lshl2_add_u32 s60,s60,s52\n"
"s_lshl_b32 s61,s60,11\n"
"s_lshr_b32 s62,s60,4\n"
"v_mfma_f32_16x16x32_bf16 v[84:87],a[76:79],v[244:247],v[84:87]\n"
"s_lshl_b32 s62,s62,15\n"
"s_and_b32 s91,s60,15\n"
"s_lshl_b32 s91,s91,6\n"
"v_mfma_f32_16x16x32_bf16 v[84:87],a[84:87],v[248:251],v[84:87]\n"
"s_add_u32 s62,s62,71303168\n"
"v_lshlrev_b32 v102,2,v112\n"
"v_and_b32 v102,0x0,v102\n"
"v_xor_b32 v102,s91,v102\n"
"v_mfma_f32_16x16x32_bf16 v[84:87],a[92:95],v[252:255],v[84:87]\n"
"s_cmp_eq_u32 s42,0\n"
"s_cselect_b32 s72,0x3ff80,s23\n"
"s_add_u32 s91,s61,2097152\n"
"v_mfma_f32_16x16x32_bf16 v[80:83],a[88:91],v[252:255],v[80:83]\n"
"s_mov_b32 s53,s56\n"
"ds_read_b128 v[252:255],v113 offset:31744\n"
"v_lshrrev_b32 v126,6,v112\n"
"v_mfma_f32_16x16x32_bf16 v[80:83],a[80:83],v[248:251],v[80:83]\n"
"s_mov_b32 s56,s58\n"
"ds_read_b128 v[248:251],v113 offset:30720\n"
"v_and_or_b32 v103,v112,48,v102\n"
"v_mfma_f32_16x16x32_bf16 v[80:83],a[72:75],v[244:247],v[80:83]\n"
"s_mov_b32 s58,s59\n"
"ds_read_b128 v[244:247],v113 offset:29696\n"
"v_lshl_add_u32 v126,v126,10,v103\n"
"v_mfma_f32_16x16x32_bf16 v[80:83],a[156:159],v[240:243],v[80:83]\n"
"ds_read_b128 v[240:243],v113 offset:28672\n"
"s_waitcnt lgkmcnt(12)\n"
"v_mfma_f32_16x16x32_bf16 v[88:91],v[128:131],v[192:195],v[80:83]\n"
"s_waitcnt vmcnt(13)\n"
"ds_add_u32 v114,v116\n"
"v_mfma_f32_16x16x32_bf16 v[88:91],v[136:139],v[196:199],v[88:91]\n"
"v_add_u32 v117,16384,v126\n"
"v_mfma_f32_16x16x32_bf16 v[88:91],v[144:147],v[200:203],v[88:91]\n"
"v_mfma_f32_16x16x32_bf16 v[88:91],v[152:155],v[204:207],v[88:91]\n"
"s_add_u32 s83,s20,32768\n"
"s_and_b32 s83,s83,0x1ffff\n"
"v_add_u32 v113,s83,v112\n"
"v_mfma_f32_16x16x32_bf16 v[92:95],v[156:159],v[204:207],v[84:87]\n"
"v_mfma_f32_16x16x32_bf16 v[92:95],v[148:151],v[200:203],v[92:95]\n"
"v_mfma_f32_16x16x32_bf16 v[92:95],v[140:143],v[196:199],v[92:95]\n"
"v_mfma_f32_16x16x32_bf16 v[92:95],v[132:135],v[192:195],v[92:95]\n"
"s_waitcnt lgkmcnt(9)\n"
"v_mfma_f32_16x16x32_bf16 v[92:95],v[4:7],v[208:211],v[92:95]\n"
"v_mfma_f32_16x16x32_bf16 v[92:95],v[12:15],v[212:215],v[92:95]\n"
"ds_read_b32 v125,v114\n"
"v_mfma_f32_16x16x32_bf16 v[92:95],v[164:167],v[216:219],v[92:95]\n"
"v_mfma_f32_16x16x32_bf16 v[92:95],v[172:175],v[220:223],v[92:95]\n"
"v_mfma_f32_16x16x32_bf16 v[88:91],v[168:171],v[220:223],v[88:91]\n"
"v_mfma_f32_16x16x32_bf16 v[88:91],v[160:163],v[216:219],v[88:91]\n"
"s_waitcnt lgkmcnt(0)\n"
"v_cmp_gt_u32 vcc,s16,v125\n"
"v_mfma_f32_16x16x32_bf16 v[88:91],v[8:11],v[212:215],v[88:91]\n"
"v_mfma_f32_16x16x32_bf16 v[88:91],v[0:3],v[208:211],v[88:91]\n"
"s_cbranch_vccnz .Lfk_es_me3\n"
".Lfk_er_me3:\n"
"ds_read_b128 v[192:195],v113 offset:0\n"
"v_mfma_f32_16x16x32_bf16 v[88:91],v[176:179],v[224:227],v[88:91]\n"
"buffer_load_dwordx4 a[96:99],v112,s[36:39],s61 offen offset:0\n"
"v_mfma_f32_16x16x32_bf16 v[88:91],v[184:187],v[228:231],v[88:91]\n"
"ds_read_b128 v[196:199],v113 offset:1024\n"
"v_lshrrev_b32 v101,4,v112\n"
"v_mfma_f32_16x16x32_bf16 v[88:91],a[48:51],v[232:235],v[88:91]\n"
"ds_read_b128 v[200:203],v113 offset:2048\n"
"v_and_b32 v102,15,v101\n"
"v_mfma_f32_16x16x32_bf16 v[88:91],a[56:59],v[236:239],v[88:91]\n"
"ds_read_b128 v[204:207],v113 offset:3072\n"
"buffer_load_dwordx4 a[100:103],v112,s[36:39],s61 offen offset:1024\n"
"v_mfma_f32_16x16x32_bf16 v[92:95],a[60:63],v[236:239],v[92:95]\n"
"ds_read_b128 v[208:211],v113 offset:4096\n"
"v_lshrrev_b32 v101,4,v101\n"
"v_mfma_f32_16x16x32_bf16 v[92:95],a[52:55],v[232:235],v[92:95]\n"
"ds_read_b128 v[212:215],v113 offset:5120\n"
"s_add_u32 s16,s16,4\n"
"v_mfma_f32_16x16x32_bf16 v[92:95],v[188:191],v[228:231],v[92:95]\n"
"ds_read_b128 v[216:219],v113 offset:6144\n"
"buffer_load_dwordx4 a[104:107],v112,s[36:39],s91 offen offset:0\n"
"v_mfma_f32_16x16x32_bf16 v[92:95],v[180:183],v[224:227],v[92:95]\n"
"ds_read_b128 v[220:223],v113 offset:7168\n"
"v_lshlrev_b32 v101,2,v101\n"
"v_mfma_f32_16x16x32_bf16 v[92:95],v[52:55],v[240:243],v[92:95]\n"
"ds_read_b128 v[236:239],v113 offset:11264\n"
"v_sub_u32 v101,v102,v101\n"
"v_mfma_f32_16x16x32_bf16 v[92:95],v[60:63],v[244:247],v[92:95]\n"
"ds_read_b128 v[232:235],v113 offset:10240\n"
"buffer_load_dwordx4 a[108:111],v112,s[36:39],s91 offen offset:1024\n"
"v_mfma_f32_16x16x32_bf16 v[92:95],v[20:23],v[248:251],v[92:95]\n"
"ds_read_b128 v[228:231],v113 offset:9216\n"
"v_mov_b32 v64,0\n"
"v_mfma_f32_16x16x32_bf16 v[92:95],v[28:31],v[252:255],v[92:95]\n"
"ds_read_b128 v[224:227],v113 offset:8192\n"
"v_mov_b32 v65,0\n"
"v_mfma_f32_16x16x32_bf16 v[88:91],v[24:27],v[252:255],v[88:91]\n"
"ds_read_b128 v[252:255],v113 offset:15360\n"
"buffer_load_dwordx4 v[72:75],v112,s[44:47],0 offen offset:0\n"
"v_mfma_f32_16x16x32_bf16 v[88:91],v[16:19],v[248:251],v[88:91]\n"
"ds_read_b128 v[248:251],v113 offset:14336\n"
"v_mov_b32 v68,0\n"
"v_mfma_f32_16x16x32_bf16 v[88:91],v[56:59],v[244:247],v[88:91]\n"
"ds_read_b128 v[244:247],v113 offset:13312\n"
"v_mov_b32 v69,0\n"
"v_mfma_f32_16x16x32_bf16 v[88:91],v[48:51],v[240:243],v[88:91]\n"
"ds_read_b128 v[240:243],v113 offset:12288\n"
"buffer_load_dwordx4 v[76:79],v112,s[44:47],0 offen offset:1024\n"
"s_waitcnt lgkmcnt(12)\n"
"v_mfma_f32_16x16x32_bf16 v[80:83],a[224:227],v[192:195],0\n"
"v_mov_b32 v102,0x3f80\n"
"v_mov_b32 v103,0x3f800000\n"
"v_cmp_eq_u32 vcc,0,v101\n"
"v_mfma_f32_16x16x32_bf16 v[80:83],a[232:235],v[196:199],v[80:83]\n"
"s_nop 0\n"
"v_cndmask_b32 v66,0,v102,vcc\n"
"v_cmp_eq_u32 vcc,1,v101\n"
"v_mfma_f32_16x16x32_bf16 v[80:83],a[240:243],v[200:203],v[80:83]\n"
"s_mov_b32 s91,s62\n"
"v_cndmask_b32 v66,v66,v103,vcc\n"
"v_cmp_eq_u32 vcc,2,v101\n"
"v_mfma_f32_16x16x32_bf16 v[80:83],a[248:251],v[204:207],v[80:83]\n"
"s_nop 0\n"
"v_cndmask_b32 v67,0,v102,vcc\n"
"v_cvt_pk_bf16_f32 v88,v88,v89\n"
"v_cvt_pk_bf16_f32 v89,v90,v91\n"
"v_mfma_f32_16x16x32_bf16 v[84:87],a[252:255],v[204:207],0\n"
"ds_read_b128 v[204:207],v113 offset:19456\n"
"s_add_u32 s20,s20,32768\n"
"v_mfma_f32_16x16x32_bf16 v[84:87],a[244:247],v[200:203],v[84:87]\n"
"s_and_b32 s20,s20,0x1ffff\n"
"ds_read_b128 v[200:203],v113 offset:18432\n"
"buffer_load_dwordx4 a[112:115],v126,s[24:27],s91 offen sc1\n"
"v_mfma_f32_16x16x32_bf16 v[84:87],a[236:239],v[196:199],v[84:87]\n"
"ds_read_b128 v[196:199],v113 offset:17408\n"
"v_cvt_pk_bf16_f32 v90,v92,v93\n"
"v_mfma_f32_16x16x32_bf16 v[84:87],a[228:231],v[192:195],v[84:87]\n"
"ds_read_b128 v[192:195],v113 offset:16384\n"
"s_waitcnt lgkmcnt(12)\n"
"v_mfma_f32_16x16x32_bf16 v[84:87],a[4:7],v[208:211],v[84:87]\n"
"v_cvt_pk_bf16_f32 v91,v94,v95\n"
"v_max_u32 v88,1,v88\n"
"v_mfma_f32_16x16x32_bf16 v[84:87],a[12:15],v[212:215],v[84:87]\n"
"buffer_load_dwordx4 a[116:119],v117,s[24:27],s91 offen sc1\n"
"v_max_u32 v89,1,v89\n"
"v_max_u32 v90,1,v90\n"
"v_max_u32 v91,1,v91\n"
"v_mfma_f32_16x16x32_bf16 v[84:87],a[20:23],v[216:219],v[84:87]\n"
"buffer_store_dwordx4 v[88:91],v118,s[24:27],s53 offen sc1\n"
"v_cmp_eq_u32 vcc,3,v101\n"
"v_mfma_f32_16x16x32_bf16 v[84:87],a[28:31],v[220:223],v[84:87]\n"
"s_add_u32 s91,s91,4194304\n"
"v_cndmask_b32 v67,v67,v103,vcc\n"
"v_mfma_f32_16x16x32_bf16 v[80:83],a[24:27],v[220:223],v[80:83]\n"
"ds_read_b128 v[220:223],v113 offset:23552\n"
"v_mfma_f32_16x16x32_bf16 v[80:83],a[16:19],v[216:219],v[80:83]\n"
"ds_read_b128 v[216:219],v113 offset:22528\n"
"v_mfma_f32_16x16x32_bf16 v[80:83],a[8:11],v[212:215],v[80:83]\n"
"ds_read_b128 v[212:215],v113 offset:21504\n"
"buffer_load_dwordx4 a[120:123],v126,s[24:27],s91 offen sc1\n"
"v_mfma_f32_16x16x32_bf16 v[80:83],a[0:3],v[208:211],v[80:83]\n"
"ds_read_b128 v[208:211],v113 offset:20480\n"
"s_waitcnt lgkmcnt(12)\n"
"v_mfma_f32_16x16x32_bf16 v[80:83],v[32:35],v[224:227],v[80:83]\n"
"v_mfma_f32_16x16x32_bf16 v[80:83],v[40:43],v[228:231],v[80:83]\n"
"buffer_load_dwordx4 a[124:127],v117,s[24:27],s91 offen sc1\n"
"v_mfma_f32_16x16x32_bf16 v[80:83],a[32:35],v[232:235],v[80:83]\n"
"v_mfma_f32_16x16x32_bf16 v[80:83],a[40:43],v[236:239],v[80:83]\n"
"v_mfma_f32_16x16x32_bf16 v[84:87],a[44:47],v[236:239],v[84:87]\n"
"ds_read_b128 v[236:239],v113 offset:27648\n"
"s_add_u32 s91,s91,4194304\n"
"v_mfma_f32_16x16x32_bf16 v[84:87],a[36:39],v[232:235],v[84:87]\n"
"ds_read_b128 v[232:235],v113 offset:26624\n"
"v_mfma_f32_16x16x32_bf16 v[84:87],v[44:47],v[228:231],v[84:87]\n"
"ds_read_b128 v[228:231],v113 offset:25600\n"
"v_mfma_f32_16x16x32_bf16 v[84:87],v[36:39],v[224:227],v[84:87]\n"
"ds_read_b128 v[224:227],v113 offset:24576\n"
"buffer_load_dwordx4 a[128:131],v126,s[24:27],s91 offen sc1\n"
"s_waitcnt lgkmcnt(12)\n"
"v_mfma_f32_16x16x32_bf16 v[84:87],a[68:71],v[240:243],v[84:87]\n"
"v_mfma_f32_16x16x32_bf16 v[84:87],a[76:79],v[244:247],v[84:87]\n"
"v_mfma_f32_16x16x32_bf16 v[84:87],a[84:87],v[248:251],v[84:87]\n"
"buffer_load_dwordx4 a[132:135],v117,s[24:27],s91 offen sc1\n"
"v_mfma_f32_16x16x32_bf16 v[84:87],a[92:95],v[252:255],v[84:87]\n"
"v_mfma_f32_16x16x32_bf16 v[80:83],a[88:91],v[252:255],v[80:83]\n"
"ds_read_b128 v[252:255],v113 offset:31744\n"
"s_mov_b32 s53,s56\n"
"v_mfma_f32_16x16x32_bf16 v[80:83],a[80:83],v[248:251],v[80:83]\n"
"s_mov_b32 s56,s58\n"
"ds_read_b128 v[248:251],v113 offset:30720\n"
"s_add_u32 s91,s91,4194304\n"
"v_mfma_f32_16x16x32_bf16 v[80:83],a[72:75],v[244:247],v[80:83]\n"
"ds_read_b128 v[244:247],v113 offset:29696\n"
"s_mov_b32 s58,s59\n"
"v_mfma_f32_16x16x32_bf16 v[80:83],a[156:159],v[240:243],v[80:83]\n"
"ds_read_b128 v[240:243],v113 offset:28672\n"
"s_waitcnt lgkmcnt(12)\n"
"v_mfma_f32_16x16x32_bf16 v[88:91],v[128:131],v[192:195],v[80:83]\n"
"s_waitcnt vmcnt(16)\n"
"ds_add_u32 v114,v116\n"
"v_mfma_f32_16x16x32_bf16 v[88:91],v[136:139],v[196:199],v[88:91]\n"
"buffer_load_dwordx4 a[136:139],v126,s[24:27],s91 offen sc1\n"
"v_mfma_f32_16x16x32_bf16 v[88:91],v[144:147],v[200:203],v[88:91]\n"
"s_add_u32 s83,s20,32768\n"
"s_and_b32 s83,s83,0x1ffff\n"
"v_mfma_f32_16x16x32_bf16 v[88:91],v[152:155],v[204:207],v[88:91]\n"
"buffer_load_dwordx4 a[140:143],v117,s[24:27],s91 offen sc1\n"
"v_mfma_f32_16x16x32_bf16 v[92:95],v[156:159],v[204:207],v[84:87]\n"
"v_add_u32 v113,s83,v112\n"
"v_mfma_f32_16x16x32_bf16 v[92:95],v[148:151],v[200:203],v[92:95]\n"
"v_mfma_f32_16x16x32_bf16 v[92:95],v[140:143],v[196:199],v[92:95]\n"
"v_mfma_f32_16x16x32_bf16 v[92:95],v[132:135],v[192:195],v[92:95]\n"
"s_waitcnt lgkmcnt(9)\n"
"v_mfma_f32_16x16x32_bf16 v[92:95],v[4:7],v[208:211],v[92:95]\n"
"v_mfma_f32_16x16x32_bf16 v[92:95],v[12:15],v[212:215],v[92:95]\n"
"ds_read_b32 v125,v114\n"
"v_mfma_f32_16x16x32_bf16 v[92:95],v[164:167],v[216:219],v[92:95]\n"
"v_mfma_f32_16x16x32_bf16 v[92:95],v[172:175],v[220:223],v[92:95]\n"
"v_mfma_f32_16x16x32_bf16 v[88:91],v[168:171],v[220:223],v[88:91]\n"
"v_mfma_f32_16x16x32_bf16 v[88:91],v[160:163],v[216:219],v[88:91]\n"
"s_waitcnt lgkmcnt(0)\n"
"v_cmp_gt_u32 vcc,s16,v125\n"
"v_mfma_f32_16x16x32_bf16 v[88:91],v[8:11],v[212:215],v[88:91]\n"
"v_mfma_f32_16x16x32_bf16 v[88:91],v[0:3],v[208:211],v[88:91]\n"
"s_cbranch_vccnz .Lfk_es_me2\n"
".Lfk_er_me2:\n"
"v_mfma_f32_16x16x32_bf16 v[88:91],v[176:179],v[224:227],v[88:91]\n"
"ds_read_b128 v[192:195],v113 offset:0\n"
"v_mfma_f32_16x16x32_bf16 v[88:91],v[184:187],v[228:231],v[88:91]\n"
"ds_read_b128 v[196:199],v113 offset:1024\n"
"v_mfma_f32_16x16x32_bf16 v[88:91],a[48:51],v[232:235],v[88:91]\n"
"ds_read_b128 v[200:203],v113 offset:2048\n"
"v_mfma_f32_16x16x32_bf16 v[88:91],a[56:59],v[236:239],v[88:91]\n"
"ds_read_b128 v[204:207],v113 offset:3072\n"
"v_mfma_f32_16x16x32_bf16 v[92:95],a[60:63],v[236:239],v[92:95]\n"
"ds_read_b128 v[208:211],v113 offset:4096\n"
"v_mfma_f32_16x16x32_bf16 v[92:95],a[52:55],v[232:235],v[92:95]\n"
"ds_read_b128 v[212:215],v113 offset:5120\n"
"v_mfma_f32_16x16x32_bf16 v[92:95],v[188:191],v[228:231],v[92:95]\n"
"ds_read_b128 v[216:219],v113 offset:6144\n"
"v_mfma_f32_16x16x32_bf16 v[92:95],v[180:183],v[224:227],v[92:95]\n"
"ds_read_b128 v[220:223],v113 offset:7168\n"
"v_mfma_f32_16x16x32_bf16 v[92:95],v[52:55],v[240:243],v[92:95]\n"
"ds_read_b128 v[236:239],v113 offset:11264\n"
"v_mfma_f32_16x16x32_bf16 v[92:95],v[60:63],v[244:247],v[92:95]\n"
"ds_read_b128 v[232:235],v113 offset:10240\n"
"v_mfma_f32_16x16x32_bf16 v[92:95],v[20:23],v[248:251],v[92:95]\n"
"ds_read_b128 v[228:231],v113 offset:9216\n"
"v_mfma_f32_16x16x32_bf16 v[92:95],v[28:31],v[252:255],v[92:95]\n"
"ds_read_b128 v[224:227],v113 offset:8192\n"
"v_mfma_f32_16x16x32_bf16 v[88:91],v[24:27],v[252:255],v[88:91]\n"
"ds_read_b128 v[252:255],v113 offset:15360\n"
"v_mfma_f32_16x16x32_bf16 v[88:91],v[16:19],v[248:251],v[88:91]\n"
"ds_read_b128 v[248:251],v113 offset:14336\n"
"v_mfma_f32_16x16x32_bf16 v[88:91],v[56:59],v[244:247],v[88:91]\n"
"ds_read_b128 v[244:247],v113 offset:13312\n"
"v_mfma_f32_16x16x32_bf16 v[88:91],v[48:51],v[240:243],v[88:91]\n"
"ds_read_b128 v[240:243],v113 offset:12288\n"
"s_waitcnt lgkmcnt(12)\n"
"v_mfma_f32_16x16x32_bf16 v[80:83],a[224:227],v[192:195],0\n"
"v_mfma_f32_16x16x32_bf16 v[80:83],a[232:235],v[196:199],v[80:83]\n"
"v_mfma_f32_16x16x32_bf16 v[80:83],a[240:243],v[200:203],v[80:83]\n"
"s_nop 2\n"
"v_cvt_pk_bf16_f32 v88,v88,v89\n"
"v_mfma_f32_16x16x32_bf16 v[80:83],a[248:251],v[204:207],v[80:83]\n"
"v_cvt_pk_bf16_f32 v89,v90,v91\n"
"v_mfma_f32_16x16x32_bf16 v[84:87],a[252:255],v[204:207],0\n"
"ds_read_b128 v[204:207],v113 offset:19456\n"
"v_cvt_pk_bf16_f32 v90,v92,v93\n"
"v_mfma_f32_16x16x32_bf16 v[84:87],a[244:247],v[200:203],v[84:87]\n"
"ds_read_b128 v[200:203],v113 offset:18432\n"
"v_cvt_pk_bf16_f32 v91,v94,v95\n"
"v_mfma_f32_16x16x32_bf16 v[84:87],a[236:239],v[196:199],v[84:87]\n"
"ds_read_b128 v[196:199],v113 offset:17408\n"
"v_max_u32 v88,1,v88\n"
"v_mfma_f32_16x16x32_bf16 v[84:87],a[228:231],v[192:195],v[84:87]\n"
"ds_read_b128 v[192:195],v113 offset:16384\n"
"s_waitcnt lgkmcnt(12)\n"
"v_mfma_f32_16x16x32_bf16 v[84:87],a[4:7],v[208:211],v[84:87]\n"
"v_max_u32 v89,1,v89\n"
"v_max_u32 v90,1,v90\n"
"v_max_u32 v91,1,v91\n"
"v_mfma_f32_16x16x32_bf16 v[84:87],a[12:15],v[212:215],v[84:87]\n"
"buffer_store_dwordx4 v[88:91],v118,s[24:27],s53 offen sc1\n"
"v_mfma_f32_16x16x32_bf16 v[84:87],a[20:23],v[216:219],v[84:87]\n"
"v_mfma_f32_16x16x32_bf16 v[84:87],a[28:31],v[220:223],v[84:87]\n"
"v_mfma_f32_16x16x32_bf16 v[80:83],a[24:27],v[220:223],v[80:83]\n"
"ds_read_b128 v[220:223],v113 offset:23552\n"
"v_mfma_f32_16x16x32_bf16 v[80:83],a[16:19],v[216:219],v[80:83]\n"
"ds_read_b128 v[216:219],v113 offset:22528\n"
"v_mfma_f32_16x16x32_bf16 v[80:83],a[8:11],v[212:215],v[80:83]\n"
"ds_read_b128 v[212:215],v113 offset:21504\n"
"v_mfma_f32_16x16x32_bf16 v[80:83],a[0:3],v[208:211],v[80:83]\n"
"ds_read_b128 v[208:211],v113 offset:20480\n"
"s_waitcnt lgkmcnt(12)\n"
"v_mfma_f32_16x16x32_bf16 v[80:83],v[32:35],v[224:227],v[80:83]\n"
"v_mfma_f32_16x16x32_bf16 v[80:83],v[40:43],v[228:231],v[80:83]\n"
"v_mfma_f32_16x16x32_bf16 v[80:83],a[32:35],v[232:235],v[80:83]\n"
"v_mfma_f32_16x16x32_bf16 v[80:83],a[40:43],v[236:239],v[80:83]\n"
"v_mfma_f32_16x16x32_bf16 v[84:87],a[44:47],v[236:239],v[84:87]\n"
"ds_read_b128 v[236:239],v113 offset:27648\n"
"v_mfma_f32_16x16x32_bf16 v[84:87],a[36:39],v[232:235],v[84:87]\n"
"ds_read_b128 v[232:235],v113 offset:26624\n"
"v_mfma_f32_16x16x32_bf16 v[84:87],v[44:47],v[228:231],v[84:87]\n"
"ds_read_b128 v[228:231],v113 offset:25600\n"
"v_mfma_f32_16x16x32_bf16 v[84:87],v[36:39],v[224:227],v[84:87]\n"
"ds_read_b128 v[224:227],v113 offset:24576\n"
"s_waitcnt lgkmcnt(12)\n"
"v_mfma_f32_16x16x32_bf16 v[84:87],a[68:71],v[240:243],v[84:87]\n"
"v_mfma_f32_16x16x32_bf16 v[84:87],a[76:79],v[244:247],v[84:87]\n"
"v_mfma_f32_16x16x32_bf16 v[84:87],a[84:87],v[248:251],v[84:87]\n"
"v_mfma_f32_16x16x32_bf16 v[84:87],a[92:95],v[252:255],v[84:87]\n"
"v_mfma_f32_16x16x32_bf16 v[80:83],a[88:91],v[252:255],v[80:83]\n"
"ds_read_b128 v[252:255],v113 offset:31744\n"
"s_mov_b32 s53,s56\n"
"v_mfma_f32_16x16x32_bf16 v[80:83],a[80:83],v[248:251],v[80:83]\n"
"ds_read_b128 v[248:251],v113 offset:30720\n"
"s_mov_b32 s56,s58\n"
"v_mfma_f32_16x16x32_bf16 v[80:83],a[72:75],v[244:247],v[80:83]\n"
"ds_read_b128 v[244:247],v113 offset:29696\n"
"s_mov_b32 s58,s59\n"
"v_mfma_f32_16x16x32_bf16 v[80:83],a[156:159],v[240:243],v[80:83]\n"
"ds_read_b128 v[240:243],v113 offset:28672\n"
"s_waitcnt lgkmcnt(12)\n"
"v_mfma_f32_16x16x32_bf16 v[88:91],v[128:131],v[192:195],v[80:83]\n"
"v_mfma_f32_16x16x32_bf16 v[88:91],v[136:139],v[196:199],v[88:91]\n"
"v_mfma_f32_16x16x32_bf16 v[88:91],v[144:147],v[200:203],v[88:91]\n"
"v_mfma_f32_16x16x32_bf16 v[88:91],v[152:155],v[204:207],v[88:91]\n"
"v_mfma_f32_16x16x32_bf16 v[92:95],v[156:159],v[204:207],v[84:87]\n"
"v_mfma_f32_16x16x32_bf16 v[92:95],v[148:151],v[200:203],v[92:95]\n"
"v_mfma_f32_16x16x32_bf16 v[92:95],v[140:143],v[196:199],v[92:95]\n"
"v_mfma_f32_16x16x32_bf16 v[92:95],v[132:135],v[192:195],v[92:95]\n"
"s_waitcnt lgkmcnt(8)\n"
"v_mfma_f32_16x16x32_bf16 v[92:95],v[4:7],v[208:211],v[92:95]\n"
"v_mfma_f32_16x16x32_bf16 v[92:95],v[12:15],v[212:215],v[92:95]\n"
"v_mfma_f32_16x16x32_bf16 v[92:95],v[164:167],v[216:219],v[92:95]\n"
"v_mfma_f32_16x16x32_bf16 v[92:95],v[172:175],v[220:223],v[92:95]\n"
"v_mfma_f32_16x16x32_bf16 v[88:91],v[168:171],v[220:223],v[88:91]\n"
"s_mov_b32 s91,s62\n"
"v_mfma_f32_16x16x32_bf16 v[88:91],v[160:163],v[216:219],v[88:91]\n"
"v_mfma_f32_16x16x32_bf16 v[88:91],v[8:11],v[212:215],v[88:91]\n"
"v_mfma_f32_16x16x32_bf16 v[88:91],v[0:3],v[208:211],v[88:91]\n"
"s_waitcnt lgkmcnt(0)\n"
"s_waitcnt vmcnt(1)\n"
"v_mfma_f32_16x16x32_bf16 v[88:91],v[176:179],v[224:227],v[88:91]\n"
"v_accvgpr_read_b32 v128,a112\n"
"v_accvgpr_read_b32 v129,a113\n"
"v_mfma_f32_16x16x32_bf16 v[88:91],v[184:187],v[228:231],v[88:91]\n"
"v_accvgpr_read_b32 v130,a114\n"
"v_accvgpr_read_b32 v131,a115\n"
"v_mfma_f32_16x16x32_bf16 v[88:91],a[48:51],v[232:235],v[88:91]\n"
"v_accvgpr_read_b32 v132,a116\n"
"v_accvgpr_read_b32 v133,a117\n"
"v_mfma_f32_16x16x32_bf16 v[88:91],a[56:59],v[236:239],v[88:91]\n"
"v_accvgpr_read_b32 v134,a118\n"
"v_accvgpr_read_b32 v135,a119\n"
"v_mfma_f32_16x16x32_bf16 v[92:95],a[60:63],v[236:239],v[92:95]\n"
"v_accvgpr_read_b32 v136,a120\n"
"v_accvgpr_read_b32 v137,a121\n"
"v_mfma_f32_16x16x32_bf16 v[92:95],a[52:55],v[232:235],v[92:95]\n"
"v_accvgpr_read_b32 v138,a122\n"
"v_accvgpr_read_b32 v139,a123\n"
"v_mfma_f32_16x16x32_bf16 v[92:95],v[188:191],v[228:231],v[92:95]\n"
"v_accvgpr_read_b32 v140,a124\n"
"v_accvgpr_read_b32 v141,a125\n"
"v_mfma_f32_16x16x32_bf16 v[92:95],v[180:183],v[224:227],v[92:95]\n"
"v_accvgpr_read_b32 v142,a126\n"
"v_accvgpr_read_b32 v143,a127\n"
"v_mfma_f32_16x16x32_bf16 v[92:95],v[52:55],v[240:243],v[92:95]\n"
"v_accvgpr_read_b32 v144,a128\n"
"v_accvgpr_read_b32 v145,a129\n"
"v_mfma_f32_16x16x32_bf16 v[92:95],v[60:63],v[244:247],v[92:95]\n"
"v_accvgpr_read_b32 v146,a130\n"
"v_accvgpr_read_b32 v147,a131\n"
"v_mfma_f32_16x16x32_bf16 v[92:95],v[20:23],v[248:251],v[92:95]\n"
"v_accvgpr_read_b32 v148,a132\n"
"v_accvgpr_read_b32 v149,a133\n"
"v_mfma_f32_16x16x32_bf16 v[92:95],v[28:31],v[252:255],v[92:95]\n"
"v_accvgpr_read_b32 v150,a134\n"
"v_accvgpr_read_b32 v151,a135\n"
"v_mfma_f32_16x16x32_bf16 v[88:91],v[24:27],v[252:255],v[88:91]\n"
"v_accvgpr_read_b32 v152,a136\n"
"v_accvgpr_read_b32 v153,a137\n"
"v_mfma_f32_16x16x32_bf16 v[88:91],v[16:19],v[248:251],v[88:91]\n"
"v_accvgpr_read_b32 v154,a138\n"
"v_accvgpr_read_b32 v155,a139\n"
"v_mfma_f32_16x16x32_bf16 v[88:91],v[56:59],v[244:247],v[88:91]\n"
"v_accvgpr_read_b32 v156,a140\n"
"v_accvgpr_read_b32 v157,a141\n"
"v_mfma_f32_16x16x32_bf16 v[88:91],v[48:51],v[240:243],v[88:91]\n"
"v_accvgpr_read_b32 v158,a142\n"
"v_accvgpr_read_b32 v159,a143\n"
".Lfk_tail:\n"
"v_lshlrev_b32 v224,16,v72\n"
"v_and_b32 v225,0xffff0000,v72\n"
"v_lshlrev_b32 v226,16,v73\n"
"v_and_b32 v227,0xffff0000,v73\n"
"v_lshlrev_b32 v228,16,v74\n"
"s_nop 0\n"
"v_cvt_pk_bf16_f32 v88,v88,v89\n"
"v_cvt_pk_bf16_f32 v89,v90,v91\n"
"v_cvt_pk_bf16_f32 v90,v92,v93\n"
"v_cvt_pk_bf16_f32 v91,v94,v95\n"
"v_max_u32 v88,1,v88\n"
"v_max_u32 v89,1,v89\n"
"v_max_u32 v90,1,v90\n"
"v_max_u32 v91,1,v91\n"
"v_mfma_f32_16x16x32_bf16 v[0:3],v[66:69],a[96:99],0\n"
"buffer_store_dwordx4 v[88:91],v118,s[24:27],s53 offen sc1\n"
"v_mfma_f32_16x16x32_bf16 v[4:7],v[64:67],a[96:99],0\n"
"s_mov_b32 s101,s22\n"
"v_and_b32 v231,0xffff0000,v75\n"
"v_lshlrev_b32 v232,16,v76\n"
"v_mfma_f32_16x16x32_bf16 v[8:11],v[66:69],a[100:103],0\n"
"v_and_b32 v229,0xffff0000,v74\n"
"v_lshlrev_b32 v230,16,v75\n"
"v_mfma_f32_16x16x32_bf16 v[12:15],v[64:67],a[100:103],0\n"
"s_mov_b32 s67,0x3a800000\n"
"v_and_b32 v233,0xffff0000,v76\n"
"v_lshlrev_b32 v234,16,v77\n"
"v_and_b32 v235,0xffff0000,v77\n"
"v_lshlrev_b32 v236,16,v78\n"
"v_and_b32 v237,0xffff0000,v78\n"
"v_lshlrev_b32 v238,16,v79\n"
"v_and_b32 v239,0xffff0000,v79\n"
"v_pk_add_f32 v[70:71],v[224:225],v[226:227]\n"
"v_pk_add_f32 v[70:71],v[70:71],v[228:229]\n"
"v_pk_add_f32 v[70:71],v[70:71],v[230:231]\n"
"v_pk_add_f32 v[70:71],v[70:71],v[232:233]\n"
"v_pk_add_f32 v[70:71],v[70:71],v[234:235]\n"
"v_pk_add_f32 v[70:71],v[70:71],v[236:237]\n"
"v_pk_add_f32 v[70:71],v[70:71],v[238:239]\n"
"v_add_f32 v70,v70,v71\n"
"s_nop 1\n"
"v_add_f32_dpp v70,v70,v70 quad_perm:[1,0,3,2] row_mask:0xf bank_mask:0xf\n"
"s_nop 1\n"
"v_add_f32_dpp v70,v70,v70 quad_perm:[2,3,0,1] row_mask:0xf bank_mask:0xf\n"
"s_nop 1\n"
"v_add_f32_dpp v70,v70,v70 row_half_mirror row_mask:0xf bank_mask:0xf\n"
"s_nop 1\n"
"v_add_f32_dpp v70,v70,v70 row_mirror row_mask:0xf bank_mask:0xf\n"
"v_mov_b32 v71,v70\n"
"s_nop 1\n"
"v_permlane16_swap_b32 v71,v70\n"
"v_add_f32 v70,v70,v71\n"
"v_mov_b32 v71,v70\n"
"s_nop 1\n"
"v_permlane32_swap_b32 v71,v70\n"
"v_add_f32 v70,v70,v71\n"
"v_min3_u32 v36,v128,v129,v130\n"
"v_min3_u32 v37,v131,v132,v133\n"
"v_min3_u32 v36,v36,v134,v135\n"
"v_min3_u32 v37,v37,v136,v137\n"
"v_min3_u32 v36,v36,v138,v139\n"
"v_min3_u32 v37,v37,v140,v141\n"
"v_min3_u32 v36,v36,v142,v143\n"
"v_min3_u32 v37,v37,v144,v145\n"
"v_min3_u32 v36,v36,v146,v147\n"
"v_min3_u32 v37,v37,v148,v149\n"
"v_min3_u32 v36,v36,v150,v151\n"
"v_min3_u32 v37,v37,v152,v153\n"
"v_min3_u32 v36,v36,v154,v155\n"
"v_min3_u32 v37,v37,v156,v157\n"
"v_min3_u32 v36,v36,v158,v159\n"
"v_min_u32 v36,v36,v37\n"
"v_cmp_eq_u32 vcc,0,v36\n"
"s_nop 0\n"
"s_cbranch_vccz .Lfk_t0\n"
"s_mov_b32 s84,0x40000\n"
"s_mov_b32 s91,s62\n"
"buffer_load_dwordx4 v[128:131],v126,s[24:27],s91 offen sc1\n"
"buffer_load_dwordx4 v[132:135],v117,s[24:27],s91 offen sc1\n"
"s_add_u32 s91,s91,4194304\n"
"buffer_load_dwordx4 v[136:139],v126,s[24:27],s91 offen sc1\n"
"buffer_load_dwordx4 v[140:143],v117,s[24:27],s91 offen sc1\n"
"s_add_u32 s91,s91,4194304\n"
"buffer_load_dwordx4 v[144:147],v126,s[24:27],s91 offen sc1\n"
"buffer_load_dwordx4 v[148:151],v117,s[24:27],s91 offen sc1\n"
"s_add_u32 s91,s91,4194304\n"
"buffer_load_dwordx4 v[152:155],v126,s[24:27],s91 offen sc1\n"
"buffer_load_dwordx4 v[156:159],v117,s[24:27],s91 offen sc1\n"
"s_waitcnt vmcnt(0)\n"
".Lfk_wa:\n"
"v_min3_u32 v36,v128,v129,v130\n"
"v_min3_u32 v37,v131,v132,v133\n"
"v_min3_u32 v36,v36,v134,v135\n"
"v_min3_u32 v37,v37,v136,v137\n"
"v_min3_u32 v36,v36,v138,v139\n"
"v_min3_u32 v37,v37,v140,v141\n"
"v_min3_u32 v36,v36,v142,v143\n"
"v_min3_u32 v37,v37,v144,v145\n"
"v_min3_u32 v36,v36,v146,v147\n"
"v_min3_u32 v37,v37,v148,v149\n"
"v_min3_u32 v36,v36,v150,v151\n"
"v_min3_u32 v37,v37,v152,v153\n"
"v_min3_u32 v36,v36,v154,v155\n"
"v_min3_u32 v37,v37,v156,v157\n"
"v_min3_u32 v36,v36,v158,v159\n"
"v_min_u32 v36,v36,v37\n"
"v_cmp_eq_u32 vcc,0,v36\n"
"s_nop 0\n"
"s_cbranch_vccz .Lfk_wa_x\n"
"s_sleep 2\n"
"s_mov_b64 exec,vcc\n"
"s_mov_b32 s91,s62\n"
"buffer_load_dwordx4 v[128:131],v126,s[24:27],s91 offen sc1\n"
"buffer_load_dwordx4 v[132:135],v117,s[24:27],s91 offen sc1\n"
"s_add_u32 s91,s91,4194304\n"
"buffer_load_dwordx4 v[136:139],v126,s[24:27],s91 offen sc1\n"
"buffer_load_dwordx4 v[140:143],v117,s[24:27],s91 offen sc1\n"
"s_add_u32 s91,s91,4194304\n"
"buffer_load_dwordx4 v[144:147],v126,s[24:27],s91 offen sc1\n"
"buffer_load_dwordx4 v[148:151],v117,s[24:27],s91 offen sc1\n"
"s_add_u32 s91,s91,4194304\n"
"buffer_load_dwordx4 v[152:155],v126,s[24:27],s91 offen sc1\n"
"buffer_load_dwordx4 v[156:159],v117,s[24:27],s91 offen sc1\n"
"s_mov_b64 exec,-1\n"
"s_sub_u32 s84,s84,1\n"
"s_waitcnt vmcnt(0)\n"
"s_cbranch_scc0 .Lfk_wa\n"
".Lfk_wa_x:\n"
".Lfk_t0:\n"
"s_getpc_b64 s[94:95]\n"
".Lfk_rm0:\n"
"s_add_u32 s94,s94,.Lfk_rm1-.Lfk_rm0\n"
"v_mfma_f32_16x16x32_bf16 v[0:3],v[66:69],v[128:131],v[0:3]\n"
"s_addc_u32 s95,s95,0\n"
"v_mfma_f32_16x16x32_bf16 v[4:7],v[64:67],v[128:131],v[4:7]\n"
"v_mfma_f32_16x16x32_bf16 v[8:11],v[66:69],v[132:135],v[8:11]\n"
"v_mfma_f32_16x16x32_bf16 v[12:15],v[64:67],v[132:135],v[12:15]\n"
"v_mfma_f32_16x16x32_bf16 v[0:3],v[66:69],v[136:139],v[0:3]\n"
"v_mfma_f32_16x16x32_bf16 v[4:7],v[64:67],v[136:139],v[4:7]\n"
"v_mfma_f32_16x16x32_bf16 v[8:11],v[66:69],v[140:143],v[8:11]\n"
"v_mfma_f32_16x16x32_bf16 v[12:15],v[64:67],v[140:143],v[12:15]\n"
"v_mfma_f32_16x16x32_bf16 v[0:3],v[66:69],v[144:147],v[0:3]\n"
"v_mfma_f32_16x16x32_bf16 v[4:7],v[64:67],v[144:147],v[4:7]\n"
"v_mfma_f32_16x16x32_bf16 v[8:11],v[66:69],v[148:151],v[8:11]\n"
"v_mfma_f32_16x16x32_bf16 v[12:15],v[64:67],v[148:151],v[12:15]\n"
"v_mfma_f32_16x16x32_bf16 v[0:3],v[66:69],v[152:155],v[0:3]\n"
"v_mfma_f32_16x16x32_bf16 v[4:7],v[64:67],v[152:155],v[4:7]\n"
"v_mfma_f32_16x16x32_bf16 v[8:11],v[66:69],v[156:159],v[8:11]\n"
"v_mfma_f32_16x16x32_bf16 v[12:15],v[64:67],v[156:159],v[12:15]\n"
"s_nop 7\n"
".Lfk_rm:\n"
"v_readfirstlane_b32 s64,v0\n"
"s_nop 1\n"
"v_mov_b32 v38,s64\n"
"v_pk_add_f32 v[16:17],v[0:1],v[38:39] op_sel_hi:[1,0] neg_lo:[0,1] neg_hi:[0,1]\n"
"v_pk_add_f32 v[18:19],v[2:3],v[38:39] op_sel_hi:[1,0] neg_lo:[0,1] neg_hi:[0,1]\n"
"v_pk_add_f32 v[20:21],v[4:5],v[38:39] op_sel_hi:[1,0] neg_lo:[0,1] neg_hi:[0,1]\n"
"v_pk_add_f32 v[22:23],v[6:7],v[38:39] op_sel_hi:[1,0] neg_lo:[0,1] neg_hi:[0,1]\n"
"v_pk_add_f32 v[24:25],v[8:9],v[38:39] op_sel_hi:[1,0] neg_lo:[0,1] neg_hi:[0,1]\n"
"v_pk_add_f32 v[26:27],v[10:11],v[38:39] op_sel_hi:[1,0] neg_lo:[0,1] neg_hi:[0,1]\n"
"v_pk_add_f32 v[28:29],v[12:13],v[38:39] op_sel_hi:[1,0] neg_lo:[0,1] neg_hi:[0,1]\n"
"v_pk_add_f32 v[30:31],v[14:15],v[38:39] op_sel_hi:[1,0] neg_lo:[0,1] neg_hi:[0,1]\n"
"v_pk_add_f32 v[32:33],v[16:17],v[18:19]\n"
"v_pk_mul_f32 v[34:35],v[16:17],v[16:17]\n"
"v_pk_mul_f32 v[36:37],v[16:17],v[224:225]\n"
"v_pk_fma_f32 v[34:35],v[18:19],v[18:19],v[34:35]\n"
"v_pk_fma_f32 v[36:37],v[18:19],v[226:227],v[36:37]\n"
"v_pk_add_f32 v[32:33],v[32:33],v[20:21]\n"
"v_pk_fma_f32 v[34:35],v[20:21],v[20:21],v[34:35]\n"
"v_pk_fma_f32 v[36:37],v[20:21],v[228:229],v[36:37]\n"
"v_pk_add_f32 v[32:33],v[32:33],v[22:23]\n"
"v_pk_fma_f32 v[34:35],v[22:23],v[22:23],v[34:35]\n"
"v_pk_fma_f32 v[36:37],v[22:23],v[230:231],v[36:37]\n"
"v_pk_add_f32 v[32:33],v[32:33],v[24:25]\n"
"v_pk_fma_f32 v[34:35],v[24:25],v[24:25],v[34:35]\n"
"v_pk_fma_f32 v[36:37],v[24:25],v[232:233],v[36:37]\n"
"v_pk_add_f32 v[32:33],v[32:33],v[26:27]\n"
"v_pk_fma_f32 v[34:35],v[26:27],v[26:27],v[34:35]\n"
"v_pk_fma_f32 v[36:37],v[26:27],v[234:235],v[36:37]\n"
"v_pk_add_f32 v[32:33],v[32:33],v[28:29]\n"
"v_pk_fma_f32 v[34:35],v[28:29],v[28:29],v[34:35]\n"
"v_pk_fma_f32 v[36:37],v[28:29],v[236:237],v[36:37]\n"
"v_pk_add_f32 v[32:33],v[32:33],v[30:31]\n"
"v_pk_fma_f32 v[34:35],v[30:31],v[30:31],v[34:35]\n"
"v_pk_fma_f32 v[36:37],v[30:31],v[238:239],v[36:37]\n"
"v_add_f32 v32,v32,v33\n"
"v_add_f32 v33,v34,v35\n"
"v_add_f32 v34,v36,v37\n"
"v_add_f32_dpp v32,v32,v32 quad_perm:[1,0,3,2] row_mask:0xf bank_mask:0xf\n"
"v_add_f32_dpp v33,v33,v33 quad_perm:[1,0,3,2] row_mask:0xf bank_mask:0xf\n"
"v_add_f32_dpp v34,v34,v34 quad_perm:[1,0,3,2] row_mask:0xf bank_mask:0xf\n"
"v_add_f32_dpp v32,v32,v32 quad_perm:[2,3,0,1] row_mask:0xf bank_mask:0xf\n"
"v_add_f32_dpp v33,v33,v33 quad_perm:[2,3,0,1] row_mask:0xf bank_mask:0xf\n"
"v_add_f32_dpp v34,v34,v34 quad_perm:[2,3,0,1] row_mask:0xf bank_mask:0xf\n"
"v_add_f32_dpp v32,v32,v32 row_half_mirror row_mask:0xf bank_mask:0xf\n"
"v_add_f32_dpp v33,v33,v33 row_half_mirror row_mask:0xf bank_mask:0xf\n"
"v_add_f32_dpp v34,v34,v34 row_half_mirror row_mask:0xf bank_mask:0xf\n"
"v_add_f32_dpp v32,v32,v32 row_mirror row_mask:0xf bank_mask:0xf\n"
"v_add_f32_dpp v33,v33,v33 row_mirror row_mask:0xf bank_mask:0xf\n"
"v_add_f32_dpp v34,v34,v34 row_mirror row_mask:0xf bank_mask:0xf\n"
"v_add_f32_dpp v32,v32,v32 row_bcast:15 row_mask:0xa bank_mask:0xf\n"
"v_add_f32_dpp v33,v33,v33 row_bcast:15 row_mask:0xa bank_mask:0xf\n"
"v_add_f32_dpp v34,v34,v34 row_bcast:15 row_mask:0xa bank_mask:0xf\n"
"v_add_f32_dpp v32,v32,v32 row_bcast:31 row_mask:0xc bank_mask:0xf\n"
"v_add_f32_dpp v33,v33,v33 row_bcast:31 row_mask:0xc bank_mask:0xf\n"
"v_add_f32_dpp v34,v34,v34 row_bcast:31 row_mask:0xc bank_mask:0xf\n"
"v_mul_f32 v32,s67,v32\n"
"v_add_f32 v35,v32,v38\n"
"v_mul_f32 v32,v32,v32\n"
"v_cvt_pk_bf16_f32 v35,v35,v35\n"
"v_lshlrev_b32 v35,16,v35\n"
"v_fma_f32 v33,v33,s67,-v32\n"
"v_sub_f32 v36,v38,v35\n"
"v_max_f32 v33,0,v33\n"
"v_fmac_f32 v34,v36,v70\n"
"v_cvt_pk_bf16_f32 v33,v33,v33\n"
"v_lshlrev_b32 v33,16,v33\n"
"v_add_f32 v33,0x3727c5ac,v33\n"
"v_cvt_pk_bf16_f32 v33,v33,v33\n"
"v_lshlrev_b32 v33,16,v33\n"
"v_sqrt_f32 v33,v33\n"
"s_nop 0\n"
"v_cvt_pk_bf16_f32 v33,v33,v33\n"
"v_lshlrev_b32 v33,16,v33\n"
"v_rcp_f32 v36,v33\n"
"v_lshrrev_b32 v33,16,v33\n"
"v_mul_f32 v37,v34,v36\n"
"v_or_b32 v33,v33,v35\n"
"v_cvt_pk_bf16_f32 v37,v37,v37\n"
"v_ashrrev_i32 v36,31,v37\n"
"v_or_b32 v36,0x80000000,v36\n"
"v_xor_b32 v37,v37,v36\n"
"v_and_b32 v37,0xffff0000,v37\n"
"v_readlane_b32 s64,v33,63\n"
"v_readlane_b32 s63,v37,63\n"
"s_nop 1\n"
"s_setpc_b64 s[94:95]\n"
".Lfk_rm1:\n"
"s_add_u32 s91,s62,2097152\n"
"buffer_load_dwordx4 v[160:163],v126,s[24:27],s91 offen sc1\n"
"buffer_load_dwordx4 v[164:167],v117,s[24:27],s91 offen sc1\n"
"s_add_u32 s91,s91,4194304\n"
"buffer_load_dwordx4 v[168:171],v126,s[24:27],s91 offen sc1\n"
"buffer_load_dwordx4 v[172:175],v117,s[24:27],s91 offen sc1\n"
"s_add_u32 s91,s91,4194304\n"
"buffer_load_dwordx4 v[176:179],v126,s[24:27],s91 offen sc1\n"
"buffer_load_dwordx4 v[180:183],v117,s[24:27],s91 offen sc1\n"
"s_add_u32 s91,s91,4194304\n"
"buffer_load_dwordx4 v[184:187],v126,s[24:27],s91 offen sc1\n"
"buffer_load_dwordx4 v[188:191],v117,s[24:27],s91 offen sc1\n"
"s_sub_u32 s65,0xffff,s60\n"
"s_or_b32 s65,s65,s63\n"
"s_mov_b32 s66,s64\n"
"v_lshlrev_b32 v34,16,s64\n"
"v_mov_b32 v35,s64\n"
"v_rcp_f32 v34,v34\n"
"v_and_b32 v35,0xffff0000,v35\n"
"v_cvt_pk_bf16_f32 v240,v0,v1\n"
"v_cvt_pk_bf16_f32 v241,v2,v3\n"
"v_cvt_pk_bf16_f32 v242,v4,v5\n"
"v_cvt_pk_bf16_f32 v243,v6,v7\n"
"v_cvt_pk_bf16_f32 v244,v8,v9\n"
"v_cvt_pk_bf16_f32 v245,v10,v11\n"
"v_cvt_pk_bf16_f32 v246,v12,v13\n"
"v_cvt_pk_bf16_f32 v247,v14,v15\n"
"v_lshlrev_b32 v0,16,v240\n"
"v_and_b32 v1,0xffff0000,v240\n"
"v_lshlrev_b32 v2,16,v241\n"
"v_and_b32 v3,0xffff0000,v241\n"
"v_lshlrev_b32 v4,16,v242\n"
"v_and_b32 v5,0xffff0000,v242\n"
"v_lshlrev_b32 v6,16,v243\n"
"v_and_b32 v7,0xffff0000,v243\n"
"v_lshlrev_b32 v8,16,v244\n"
"v_and_b32 v9,0xffff0000,v244\n"
"v_lshlrev_b32 v10,16,v245\n"
"v_and_b32 v11,0xffff0000,v245\n"
"v_lshlrev_b32 v12,16,v246\n"
"v_and_b32 v13,0xffff0000,v246\n"
"v_lshlrev_b32 v14,16,v247\n"
"v_and_b32 v15,0xffff0000,v247\n"
"v_pk_add_f32 v[16:17],v[0:1],v[34:35] op_sel:[0,1] neg_lo:[0,1] neg_hi:[0,1]\n"
"v_pk_add_f32 v[18:19],v[2:3],v[34:35] op_sel:[0,1] neg_lo:[0,1] neg_hi:[0,1]\n"
"v_pk_add_f32 v[20:21],v[4:5],v[34:35] op_sel:[0,1] neg_lo:[0,1] neg_hi:[0,1]\n"
"v_pk_add_f32 v[22:23],v[6:7],v[34:35] op_sel:[0,1] neg_lo:[0,1] neg_hi:[0,1]\n"
"v_pk_add_f32 v[24:25],v[8:9],v[34:35] op_sel:[0,1] neg_lo:[0,1] neg_hi:[0,1]\n"
"v_pk_add_f32 v[26:27],v[10:11],v[34:35] op_sel:[0,1] neg_lo:[0,1] neg_hi:[0,1]\n"
"v_pk_add_f32 v[28:29],v[12:13],v[34:35] op_sel:[0,1] neg_lo:[0,1] neg_hi:[0,1]\n"
"v_pk_add_f32 v[30:31],v[14:15],v[34:35] op_sel:[0,1] neg_lo:[0,1] neg_hi:[0,1]\n"
"v_cvt_pk_bf16_f32 v240,v16,v17\n"
"v_cvt_pk_bf16_f32 v241,v18,v19\n"
"v_cvt_pk_bf16_f32 v242,v20,v21\n"
"v_cvt_pk_bf16_f32 v243,v22,v23\n"
"v_cvt_pk_bf16_f32 v244,v24,v25\n"
"v_cvt_pk_bf16_f32 v245,v26,v27\n"
"v_cvt_pk_bf16_f32 v246,v28,v29\n"
"v_cvt_pk_bf16_f32 v247,v30,v31\n"
"v_lshlrev_b32 v16,16,v240\n"
"v_and_b32 v17,0xffff0000,v240\n"
"v_lshlrev_b32 v18,16,v241\n"
"v_and_b32 v19,0xffff0000,v241\n"
"v_lshlrev_b32 v20,16,v242\n"
"v_and_b32 v21,0xffff0000,v242\n"
"v_lshlrev_b32 v22,16,v243\n"
"v_and_b32 v23,0xffff0000,v243\n"
"v_lshlrev_b32 v24,16,v244\n"
"v_and_b32 v25,0xffff0000,v244\n"
"v_lshlrev_b32 v26,16,v245\n"
"v_and_b32 v27,0xffff0000,v245\n"
"v_lshlrev_b32 v28,16,v246\n"
"v_and_b32 v29,0xffff0000,v246\n"
"v_lshlrev_b32 v30,16,v247\n"
"v_and_b32 v31,0xffff0000,v247\n"
"v_pk_mul_f32 v[16:17],v[16:17],v[34:35] op_sel_hi:[1,0]\n"
"v_pk_mul_f32 v[18:19],v[18:19],v[34:35] op_sel_hi:[1,0]\n"
"v_pk_mul_f32 v[20:21],v[20:21],v[34:35] op_sel_hi:[1,0]\n"
"v_pk_mul_f32 v[22:23],v[22:23],v[34:35] op_sel_hi:[1,0]\n"
"v_pk_mul_f32 v[24:25],v[24:25],v[34:35] op_sel_hi:[1,0]\n"
"v_pk_mul_f32 v[26:27],v[26:27],v[34:35] op_sel_hi:[1,0]\n"
"v_pk_mul_f32 v[28:29],v[28:29],v[34:35] op_sel_hi:[1,0]\n"
"v_pk_mul_f32 v[30:31],v[30:31],v[34:35] op_sel_hi:[1,0]\n"
"v_cvt_pk_bf16_f32 v240,v16,v17\n"
"v_cvt_pk_bf16_f32 v241,v18,v19\n"
"v_cvt_pk_bf16_f32 v242,v20,v21\n"
"v_cvt_pk_bf16_f32 v243,v22,v23\n"
"v_cvt_pk_bf16_f32 v244,v24,v25\n"
"v_cvt_pk_bf16_f32 v245,v26,v27\n"
"v_cvt_pk_bf16_f32 v246,v28,v29\n"
"v_cvt_pk_bf16_f32 v247,v30,v31\n"
"v_lshlrev_b32 v16,16,v240\n"
"v_and_b32 v17,0xffff0000,v240\n"
"v_lshlrev_b32 v18,16,v241\n"
"v_and_b32 v19,0xffff0000,v241\n"
"v_lshlrev_b32 v20,16,v242\n"
"v_and_b32 v21,0xffff0000,v242\n"
"v_lshlrev_b32 v22,16,v243\n"
"v_and_b32 v23,0xffff0000,v243\n"
"v_lshlrev_b32 v24,16,v244\n"
"v_and_b32 v25,0xffff0000,v244\n"
"v_lshlrev_b32 v26,16,v245\n"
"v_and_b32 v27,0xffff0000,v245\n"
"v_lshlrev_b32 v28,16,v246\n"
"v_and_b32 v29,0xffff0000,v246\n"
"v_lshlrev_b32 v30,16,v247\n"
"v_and_b32 v31,0xffff0000,v247\n"
"v_pk_add_f32 v[16:17],v[16:17],v[0:1]\n"
"v_pk_add_f32 v[18:19],v[18:19],v[2:3]\n"
"v_pk_add_f32 v[20:21],v[20:21],v[4:5]\n"
"v_pk_add_f32 v[22:23],v[22:23],v[6:7]\n"
"v_pk_add_f32 v[24:25],v[24:25],v[8:9]\n"
"v_pk_add_f32 v[26:27],v[26:27],v[10:11]\n"
"v_pk_add_f32 v[28:29],v[28:29],v[12:13]\n"
"v_pk_add_f32 v[30:31],v[30:31],v[14:15]\n"
"v_cvt_pk_bf16_f32 v240,v16,v17\n"
"v_cvt_pk_bf16_f32 v241,v18,v19\n"
"v_cvt_pk_bf16_f32 v242,v20,v21\n"
"v_cvt_pk_bf16_f32 v243,v22,v23\n"
"v_cvt_pk_bf16_f32 v244,v24,v25\n"
"v_cvt_pk_bf16_f32 v245,v26,v27\n"
"v_cvt_pk_bf16_f32 v246,v28,v29\n"
"v_cvt_pk_bf16_f32 v247,v30,v31\n"
"buffer_store_dwordx4 v[240:243],v112,s[48:51],s61 offen offset:0 sc1\n"
"buffer_store_dwordx4 v[244:247],v112,s[48:51],s61 offen offset:1024 sc1\n"
"s_barrier\n"
"s_lshl_b32 s82,s52,13\n"
"s_add_u32 s82,s82,0\n"
"v_bfe_u32 v41,v112,5,2\n"
"v_lshlrev_b32 v41,13,v41\n"
"v_bfe_u32 v42,v112,4,1\n"
"v_lshl_add_u32 v41,v42,3,v41\n"
"s_lshl_b32 s83,s60,2\n"
"v_mov_b32 v49,s80\n"
"v_mfma_f32_16x16x32_bf16 v[200:203],v[66:69],a[104:107],0\n"
"s_add_u32 s83,s83,149946368\n"
"v_mov_b32 v45,s66\n"
"v_add_u32 v41,0,v41\n"
"v_mfma_f32_16x16x32_bf16 v[204:207],v[64:67],a[104:107],0\n"
"v_mov_b32 v40,s82\n"
"v_mov_b32 v51,s80\n"
"v_mfma_f32_16x16x32_bf16 v[208:211],v[66:69],a[108:111],0\n"
"v_lshlrev_b32 v43,8,v112\n"
"v_mov_b32 v50,0\n"
"v_mfma_f32_16x16x32_bf16 v[212:215],v[64:67],a[108:111],0\n"
"s_mov_b32 s84,0x40000\n"
"v_mov_b32 v44,s65\n"
"s_waitcnt vmcnt(2)\n"
".Lfk_w1:\n"
"v_mfma_f32_16x16x32_bf16 v[0:3],v[66:69],v[160:163],v[200:203]\n"
"v_min3_u32 v36,v160,v161,v162\n"
"v_min3_u32 v37,v163,v164,v165\n"
"v_mfma_f32_16x16x32_bf16 v[4:7],v[64:67],v[160:163],v[204:207]\n"
"v_min3_u32 v36,v36,v166,v167\n"
"v_min3_u32 v37,v37,v168,v169\n"
"v_mfma_f32_16x16x32_bf16 v[8:11],v[66:69],v[164:167],v[208:211]\n"
"v_min3_u32 v36,v36,v170,v171\n"
"v_min3_u32 v37,v37,v172,v173\n"
"v_mfma_f32_16x16x32_bf16 v[12:15],v[64:67],v[164:167],v[212:215]\n"
"v_min3_u32 v36,v36,v174,v175\n"
"v_min3_u32 v37,v37,v176,v177\n"
"v_mfma_f32_16x16x32_bf16 v[0:3],v[66:69],v[168:171],v[0:3]\n"
"v_min3_u32 v36,v36,v178,v179\n"
"v_min3_u32 v37,v37,v180,v181\n"
"v_mfma_f32_16x16x32_bf16 v[4:7],v[64:67],v[168:171],v[4:7]\n"
"v_min3_u32 v36,v36,v182,v183\n"
"v_min3_u32 v37,v37,v184,v185\n"
"v_mfma_f32_16x16x32_bf16 v[8:11],v[66:69],v[172:175],v[8:11]\n"
"v_min3_u32 v36,v36,v186,v187\n"
"v_min3_u32 v37,v37,v188,v189\n"
"v_mfma_f32_16x16x32_bf16 v[12:15],v[64:67],v[172:175],v[12:15]\n"
"v_min3_u32 v36,v36,v190,v191\n"
"v_min_u32 v36,v36,v37\n"
"v_cmp_eq_u32 vcc,0,v36\n"
"v_mfma_f32_16x16x32_bf16 v[0:3],v[66:69],v[176:179],v[0:3]\n"
"s_cbranch_vccz .Lfk_w1_x\n"
"s_mov_b64 exec,vcc\n"
"s_add_u32 s91,s62,2097152\n"
"buffer_load_dwordx4 v[160:163],v126,s[24:27],s91 offen sc1\n"
"buffer_load_dwordx4 v[164:167],v117,s[24:27],s91 offen sc1\n"
"s_add_u32 s91,s91,4194304\n"
"buffer_load_dwordx4 v[168:171],v126,s[24:27],s91 offen sc1\n"
"buffer_load_dwordx4 v[172:175],v117,s[24:27],s91 offen sc1\n"
"s_add_u32 s91,s91,4194304\n"
"buffer_load_dwordx4 v[176:179],v126,s[24:27],s91 offen sc1\n"
"buffer_load_dwordx4 v[180:183],v117,s[24:27],s91 offen sc1\n"
"s_add_u32 s91,s91,4194304\n"
"buffer_load_dwordx4 v[184:187],v126,s[24:27],s91 offen sc1\n"
"buffer_load_dwordx4 v[188:191],v117,s[24:27],s91 offen sc1\n"
"s_mov_b64 exec,-1\n"
"s_sub_u32 s84,s84,1\n"
"s_waitcnt vmcnt(0)\n"
"s_cbranch_scc0 .Lfk_w1\n"
".Lfk_w1_x:\n"
"v_mfma_f32_16x16x32_bf16 v[4:7],v[64:67],v[176:179],v[4:7]\n"
"v_mfma_f32_16x16x32_bf16 v[8:11],v[66:69],v[180:183],v[8:11]\n"
"v_mfma_f32_16x16x32_bf16 v[12:15],v[64:67],v[180:183],v[12:15]\n"
"v_mfma_f32_16x16x32_bf16 v[0:3],v[66:69],v[184:187],v[0:3]\n"
"v_mfma_f32_16x16x32_bf16 v[4:7],v[64:67],v[184:187],v[4:7]\n"
"v_mfma_f32_16x16x32_bf16 v[8:11],v[66:69],v[188:191],v[8:11]\n"
"v_mfma_f32_16x16x32_bf16 v[12:15],v[64:67],v[188:191],v[12:15]\n"
"s_nop 6\n"
"s_call_b64 s[94:95],.Lfk_rm\n"
"s_sub_u32 s82,64511,s60\n"
"s_or_b32 s63,s63,s82\n"
"v_mov_b32 v46,s63\n"
"v_mov_b32 v47,s64\n"
"ds_write_b128 v40,v[44:47]\n"
"s_waitcnt vmcnt(0) lgkmcnt(0)\n"
"s_barrier\n"
"ds_read_b64 v[52:53],v41\n"
"s_waitcnt lgkmcnt(0)\n"
"v_mov_b32 v48,v52\n"
"s_nop 1\n"
"v_min_u32_dpp v54,v48,v48 quad_perm:[1,0,3,2] row_mask:0xf bank_mask:0xf\n"
"v_max_u32_dpp v48,v48,v48 quad_perm:[1,0,3,2] row_mask:0xf bank_mask:0xf\n"
"v_max_u32_dpp v50,v50,v50 quad_perm:[1,0,3,2] row_mask:0xf bank_mask:0xf\n"
"v_max_u32 v50,v50,v54\n"
"v_min_u32_dpp v54,v48,v48 quad_perm:[2,3,0,1] row_mask:0xf bank_mask:0xf\n"
"v_max_u32_dpp v48,v48,v48 quad_perm:[2,3,0,1] row_mask:0xf bank_mask:0xf\n"
"v_max_u32_dpp v50,v50,v50 quad_perm:[2,3,0,1] row_mask:0xf bank_mask:0xf\n"
"v_max_u32 v50,v50,v54\n"
"v_min_u32_dpp v54,v48,v48 row_half_mirror row_mask:0xf bank_mask:0xf\n"
"v_max_u32_dpp v48,v48,v48 row_half_mirror row_mask:0xf bank_mask:0xf\n"
"v_max_u32_dpp v50,v50,v50 row_half_mirror row_mask:0xf bank_mask:0xf\n"
"v_max_u32 v50,v50,v54\n"
"s_cmp_lg_u32 s52,0\n"
"s_cbranch_scc1 .Lfk_t1\n"
"s_mov_b64 exec,0xff\n"
"buffer_store_dwordx4 v[48:51],v43,s[24:27],s83 offen sc1\n"
"s_mov_b64 exec,-1\n"
"v_cmp_eq_u32 vcc,v48,v52\n"
"s_nop 0\n"
"s_ff1_i32_b64 s82,vcc\n"
"v_readlane_b32 s90,v53,s82\n"
"s_nop 1\n"
"v_mov_b32 v44,s90\n"
"v_mov_b32 v45,s80\n"
"v_cmp_eq_u32 vcc,v50,v52\n"
"s_nop 0\n"
"s_ff1_i32_b64 s82,vcc\n"
"v_readlane_b32 s90,v53,s82\n"
"s_nop 1\n"
"v_mov_b32 v46,s90\n"
"v_mov_b32 v47,s80\n"
"s_mov_b64 exec,0x100\n"
"buffer_store_dwordx4 v[44:47],v43,s[24:27],s83 offen sc1\n"
"s_mov_b64 exec,-1\n"
".Lfk_t1:\n"
"v_readfirstlane_b32 s89,v50\n"
"s_add_u32 m0,s30,131072\n"
"s_nop 0\n"
"buffer_load_dwordx4 v96,s[40:43],s77 offen offset:0 nt lds\n"
"buffer_load_dwordx4 v96,s[40:43],s77 offen offset:1024 nt lds\n"
"buffer_load_dwordx4 v97,s[40:43],s77 offen offset:2048 nt lds\n"
"buffer_load_dwordx4 v97,s[40:43],s77 offen offset:3072 nt lds\n"
"s_add_u32 s77,s77,4096\n"
"s_add_u32 m0,s30,135168\n"
"s_nop 0\n"
"buffer_load_dwordx4 v98,s[40:43],s77 offen offset:0 nt lds\n"
"buffer_load_dwordx4 v98,s[40:43],s77 offen offset:1024 nt lds\n"
"buffer_load_dwordx4 v99,s[40:43],s77 offen offset:2048 nt lds\n"
"buffer_load_dwordx4 v99,s[40:43],s77 offen offset:3072 nt lds\n"
"v_lshlrev_b32 v34,16,s64\n"
"v_mov_b32 v35,s64\n"
"v_rcp_f32 v34,v34\n"
"v_and_b32 v35,0xffff0000,v35\n"
"v_cvt_pk_bf16_f32 v248,v0,v1\n"
"v_cvt_pk_bf16_f32 v249,v2,v3\n"
"v_cvt_pk_bf16_f32 v250,v4,v5\n"
"v_cvt_pk_bf16_f32 v251,v6,v7\n"
"v_cvt_pk_bf16_f32 v252,v8,v9\n"
"v_cvt_pk_bf16_f32 v253,v10,v11\n"
"v_cvt_pk_bf16_f32 v254,v12,v13\n"
"v_cvt_pk_bf16_f32 v255,v14,v15\n"
"v_lshlrev_b32 v0,16,v248\n"
"v_and_b32 v1,0xffff0000,v248\n"
"v_lshlrev_b32 v2,16,v249\n"
"v_and_b32 v3,0xffff0000,v249\n"
"v_lshlrev_b32 v4,16,v250\n"
"v_and_b32 v5,0xffff0000,v250\n"
"v_lshlrev_b32 v6,16,v251\n"
"v_and_b32 v7,0xffff0000,v251\n"
"v_lshlrev_b32 v8,16,v252\n"
"v_and_b32 v9,0xffff0000,v252\n"
"v_lshlrev_b32 v10,16,v253\n"
"v_and_b32 v11,0xffff0000,v253\n"
"v_lshlrev_b32 v12,16,v254\n"
"v_and_b32 v13,0xffff0000,v254\n"
"v_lshlrev_b32 v14,16,v255\n"
"v_and_b32 v15,0xffff0000,v255\n"
"v_pk_add_f32 v[16:17],v[0:1],v[34:35] op_sel:[0,1] neg_lo:[0,1] neg_hi:[0,1]\n"
"v_pk_add_f32 v[18:19],v[2:3],v[34:35] op_sel:[0,1] neg_lo:[0,1] neg_hi:[0,1]\n"
"v_pk_add_f32 v[20:21],v[4:5],v[34:35] op_sel:[0,1] neg_lo:[0,1] neg_hi:[0,1]\n"
"v_pk_add_f32 v[22:23],v[6:7],v[34:35] op_sel:[0,1] neg_lo:[0,1] neg_hi:[0,1]\n"
"v_pk_add_f32 v[24:25],v[8:9],v[34:35] op_sel:[0,1] neg_lo:[0,1] neg_hi:[0,1]\n"
"v_pk_add_f32 v[26:27],v[10:11],v[34:35] op_sel:[0,1] neg_lo:[0,1] neg_hi:[0,1]\n"
"v_pk_add_f32 v[28:29],v[12:13],v[34:35] op_sel:[0,1] neg_lo:[0,1] neg_hi:[0,1]\n"
"v_pk_add_f32 v[30:31],v[14:15],v[34:35] op_sel:[0,1] neg_lo:[0,1] neg_hi:[0,1]\n"
"v_cvt_pk_bf16_f32 v248,v16,v17\n"
"v_cvt_pk_bf16_f32 v249,v18,v19\n"
"v_cvt_pk_bf16_f32 v250,v20,v21\n"
"v_cvt_pk_bf16_f32 v251,v22,v23\n"
"v_cvt_pk_bf16_f32 v252,v24,v25\n"
"v_cvt_pk_bf16_f32 v253,v26,v27\n"
"v_cvt_pk_bf16_f32 v254,v28,v29\n"
"v_cvt_pk_bf16_f32 v255,v30,v31\n"
"v_lshlrev_b32 v16,16,v248\n"
"v_and_b32 v17,0xffff0000,v248\n"
"v_lshlrev_b32 v18,16,v249\n"
"v_and_b32 v19,0xffff0000,v249\n"
"v_lshlrev_b32 v20,16,v250\n"
"v_and_b32 v21,0xffff0000,v250\n"
"v_lshlrev_b32 v22,16,v251\n"
"v_and_b32 v23,0xffff0000,v251\n"
"v_lshlrev_b32 v24,16,v252\n"
"v_and_b32 v25,0xffff0000,v252\n"
"v_lshlrev_b32 v26,16,v253\n"
"v_and_b32 v27,0xffff0000,v253\n"
"v_lshlrev_b32 v28,16,v254\n"
"v_and_b32 v29,0xffff0000,v254\n"
"v_lshlrev_b32 v30,16,v255\n"
"v_and_b32 v31,0xffff0000,v255\n"
"v_pk_mul_f32 v[16:17],v[16:17],v[34:35] op_sel_hi:[1,0]\n"
"v_pk_mul_f32 v[18:19],v[18:19],v[34:35] op_sel_hi:[1,0]\n"
"v_pk_mul_f32 v[20:21],v[20:21],v[34:35] op_sel_hi:[1,0]\n"
"v_pk_mul_f32 v[22:23],v[22:23],v[34:35] op_sel_hi:[1,0]\n"
"v_pk_mul_f32 v[24:25],v[24:25],v[34:35] op_sel_hi:[1,0]\n"
"v_pk_mul_f32 v[26:27],v[26:27],v[34:35] op_sel_hi:[1,0]\n"
"v_pk_mul_f32 v[28:29],v[28:29],v[34:35] op_sel_hi:[1,0]\n"
"v_pk_mul_f32 v[30:31],v[30:31],v[34:35] op_sel_hi:[1,0]\n"
"v_cvt_pk_bf16_f32 v248,v16,v17\n"
"v_cvt_pk_bf16_f32 v249,v18,v19\n"
"v_cvt_pk_bf16_f32 v250,v20,v21\n"
"v_cvt_pk_bf16_f32 v251,v22,v23\n"
"v_cvt_pk_bf16_f32 v252,v24,v25\n"
"v_cvt_pk_bf16_f32 v253,v26,v27\n"
"v_cvt_pk_bf16_f32 v254,v28,v29\n"
"v_cvt_pk_bf16_f32 v255,v30,v31\n"
"v_lshlrev_b32 v16,16,v248\n"
"v_and_b32 v17,0xffff0000,v248\n"
"v_lshlrev_b32 v18,16,v249\n"
"v_and_b32 v19,0xffff0000,v249\n"
"v_lshlrev_b32 v20,16,v250\n"
"v_and_b32 v21,0xffff0000,v250\n"
"v_lshlrev_b32 v22,16,v251\n"
"v_and_b32 v23,0xffff0000,v251\n"
"v_lshlrev_b32 v24,16,v252\n"
"v_and_b32 v25,0xffff0000,v252\n"
"v_lshlrev_b32 v26,16,v253\n"
"v_and_b32 v27,0xffff0000,v253\n"
"v_lshlrev_b32 v28,16,v254\n"
"v_and_b32 v29,0xffff0000,v254\n"
"v_lshlrev_b32 v30,16,v255\n"
"v_and_b32 v31,0xffff0000,v255\n"
"v_pk_add_f32 v[16:17],v[16:17],v[0:1]\n"
"v_pk_add_f32 v[18:19],v[18:19],v[2:3]\n"
"v_pk_add_f32 v[20:21],v[20:21],v[4:5]\n"
"v_pk_add_f32 v[22:23],v[22:23],v[6:7]\n"
"v_pk_add_f32 v[24:25],v[24:25],v[8:9]\n"
"v_pk_add_f32 v[26:27],v[26:27],v[10:11]\n"
"v_pk_add_f32 v[28:29],v[28:29],v[12:13]\n"
"v_pk_add_f32 v[30:31],v[30:31],v[14:15]\n"
"v_cvt_pk_bf16_f32 v248,v16,v17\n"
"v_cvt_pk_bf16_f32 v249,v18,v19\n"
"v_cvt_pk_bf16_f32 v250,v20,v21\n"
"v_cvt_pk_bf16_f32 v251,v22,v23\n"
"v_cvt_pk_bf16_f32 v252,v24,v25\n"
"v_cvt_pk_bf16_f32 v253,v26,v27\n"
"v_cvt_pk_bf16_f32 v254,v28,v29\n"
"v_cvt_pk_bf16_f32 v255,v30,v31\n"
"s_add_u32 s68,s61,2097152\n"
"s_cmp_lt_u32 s63,s89\n"
"s_cselect_b32 s83,s68,2146435072\n"
"s_cselect_b32 s68,2146435072,s68\n"
"buffer_store_dwordx4 v[248:251],v112,s[48:51],s83 offen offset:0 sc1\n"
"buffer_store_dwordx4 v[252:255],v112,s[48:51],s83 offen offset:1024 sc1\n"
"v_lshrrev_b32 v40,6,v112\n"
"v_and_b32 v41,7,v40\n"
"v_lshlrev_b32 v41,5,v41\n"
"v_bfe_u32 v42,v112,4,2\n"
"v_lshlrev_b32 v42,3,v42\n"
"v_lshl_add_u32 v40,v40,11,v42\n"
"s_lshl_b32 s82,s52,9\n"
"v_add_u32 v40,s82,v40\n"
"v_add_u32 v40,131072,v40\n"
"v_xor_b32 v42,0,v41\n"
"v_add_u32 v192,v40,v42\n"
"v_xor_b32 v42,32,v41\n"
"v_add_u32 v193,v40,v42\n"
"v_xor_b32 v42,64,v41\n"
"v_add_u32 v194,v40,v42\n"
"v_xor_b32 v42,96,v41\n"
"v_add_u32 v195,v40,v42\n"
"v_xor_b32 v42,128,v41\n"
"v_add_u32 v196,v40,v42\n"
"v_xor_b32 v42,160,v41\n"
"v_add_u32 v197,v40,v42\n"
"v_xor_b32 v42,192,v41\n"
"v_add_u32 v198,v40,v42\n"
"v_xor_b32 v42,224,v41\n"
"v_add_u32 v199,v40,v42\n"
"v_bfe_u32 v56,v112,4,1\n"
"v_lshrrev_b32 v57,1,v112\n"
"v_and_b32 v57,0x1f0,v57\n"
"s_lshl_b32 s83,s52,13\n"
"s_add_u32 s83,s83,1024\n"
"v_lshl_add_u32 v58,v56,9,s83\n"
"v_lshrrev_b32 v59,8,v112\n"
"v_lshl_add_u32 v59,v59,3,v58\n"
"v_add_u32 v58,v58,v57\n"
"v_add_u32 v57,s82,v57\n"
"v_and_b32 v55,0x7c0,v57\n"
"v_and_b32 v54,0x30,v57\n"
"v_lshl_add_u32 v55,v55,4,v54\n"
"v_lshrrev_b32 v54,2,v55\n"
"v_and_b32 v54,0x0,v54\n"
"v_or_b32 v55,v55,v54\n"
"v_add_u32 v60,s83,v112\n"
"v_add_u32 v61,1024,v112\n"
"v_bfe_u32 v62,v112,5,3\n"
"v_cmp_eq_u32 s[8:9],0,v62\n"
"v_cmp_eq_u32 s[10:11],1,v62\n"
"v_cmp_eq_u32 s[12:13],2,v62\n"
"v_cmp_eq_u32 s[14:15],3,v62\n"
"v_cmp_eq_u32 s[16:17],4,v62\n"
"v_cmp_eq_u32 s[18:19],5,v62\n"
"v_cmp_eq_u32 s[20:21],6,v62\n"
"v_cmp_eq_u32 s[22:23],7,v62\n"
"v_lshlrev_b32 v62,6,v62\n"
"v_lshl_add_u32 v62,v56,12,v62\n"
"v_lshrrev_b32 v63,8,v112\n"
"v_lshl_add_u32 v62,v63,4,v62\n"
"s_lshl_b32 s69,s72,13\n"
"s_lshl_b32 s82,s52,10\n"
"s_add_u32 s69,s69,s82\n"
"s_add_u32 s69,s69,171966464\n"
"v_lshrrev_b32 v63,5,v112\n"
"v_min_u32 v63,27,v63\n"
"v_lshlrev_b32 v63,13,v63\n"
"v_lshl_add_u32 v63,v56,4,v63\n"
"s_mul_i32 s70,s52,458752\n"
"s_lshl_b32 s82,s73,5\n"
"s_add_u32 s70,s70,s82\n"
"s_add_u32 s70,s70,171966464\n"
"s_add_u32 s71,s70,229376\n"
"s_and_b32 s83,s100,7\n"
"s_lshl_b32 s83,s83,12\n"
"s_add_u32 s83,s83,149946368\n"
"v_add_u32 v20,0,v112\n"
"v_lshlrev_b32 v20,0,v20\n"
"v_add_u32 v21,1024,v112\n"
"v_lshlrev_b32 v21,0,v21\n"
"v_add_u32 v22,2048,v112\n"
"v_lshlrev_b32 v22,0,v22\n"
"v_add_u32 v23,3072,v112\n"
"v_lshlrev_b32 v23,0,v23\n"
"s_sleep 16\n"
".Lfk_nn:\n"
"s_mov_b32 s84,0x40000\n"
"buffer_load_dwordx4 v[0:3],v20,s[24:27],s83 offen sc1\n"
"buffer_load_dwordx4 v[4:7],v21,s[24:27],s83 offen sc1\n"
"buffer_load_dwordx4 v[8:11],v22,s[24:27],s83 offen sc1\n"
"buffer_load_dwordx4 v[12:15],v23,s[24:27],s83 offen sc1\n"
"s_waitcnt vmcnt(0)\n"
".Lfk_wc:\n"
"v_xor_b32 v24,s80,v1\n"
"v_xor_b32 v25,s80,v3\n"
"v_xor_b32 v26,s80,v5\n"
"v_xor_b32 v27,s80,v7\n"
"v_xor_b32 v28,s80,v9\n"
"v_xor_b32 v29,s80,v11\n"
"v_xor_b32 v30,s80,v13\n"
"v_xor_b32 v31,s80,v15\n"
"v_or3_b32 v16,v24,v25,v26\n"
"v_or3_b32 v17,v27,v28,v29\n"
"v_or3_b32 v16,v16,v30,v31\n"
"v_or_b32 v16,v16,v17\n"
"v_cmp_ne_u32 vcc,0,v16\n"
"s_nop 0\n"
"s_cbranch_vccz .Lfk_wc_x\n"
"s_sleep 2\n"
"s_mov_b64 exec,vcc\n"
"buffer_load_dwordx4 v[0:3],v20,s[24:27],s83 offen sc1\n"
"buffer_load_dwordx4 v[4:7],v21,s[24:27],s83 offen sc1\n"
"buffer_load_dwordx4 v[8:11],v22,s[24:27],s83 offen sc1\n"
"buffer_load_dwordx4 v[12:15],v23,s[24:27],s83 offen sc1\n"
"s_mov_b64 exec,-1\n"
"s_sub_u32 s84,s84,1\n"
"s_waitcnt vmcnt(0)\n"
"s_cbranch_scc0 .Lfk_wc\n"
".Lfk_wc_x:\n"
"v_min_u32 v16,v0,v4\n"
"v_max_u32 v0,v0,v4\n"
"v_max3_u32 v2,v2,v6,v16\n"
"v_min_u32 v16,v8,v12\n"
"v_max_u32 v8,v8,v12\n"
"v_max3_u32 v10,v10,v14,v16\n"
"v_min_u32 v16,v0,v8\n"
"v_max_u32 v0,v0,v8\n"
"v_max3_u32 v2,v2,v10,v16\n"
"v_cmp_eq_u32 vcc,0,v56\n"
"v_min_u32_dpp v16,v0,v0 quad_perm:[1,0,3,2] row_mask:0xf bank_mask:0xf\n"
"v_max_u32_dpp v0,v0,v0 quad_perm:[1,0,3,2] row_mask:0xf bank_mask:0xf\n"
"v_max_u32_dpp v2,v2,v2 quad_perm:[1,0,3,2] row_mask:0xf bank_mask:0xf\n"
"v_max_u32 v2,v2,v16\n"
"v_min_u32_dpp v16,v0,v0 quad_perm:[2,3,0,1] row_mask:0xf bank_mask:0xf\n"
"v_max_u32_dpp v0,v0,v0 quad_perm:[2,3,0,1] row_mask:0xf bank_mask:0xf\n"
"v_max_u32_dpp v2,v2,v2 quad_perm:[2,3,0,1] row_mask:0xf bank_mask:0xf\n"
"v_max_u32 v2,v2,v16\n"
"v_min_u32_dpp v16,v0,v0 row_half_mirror row_mask:0xf bank_mask:0xf\n"
"v_max_u32_dpp v0,v0,v0 row_half_mirror row_mask:0xf bank_mask:0xf\n"
"v_max_u32_dpp v2,v2,v2 row_half_mirror row_mask:0xf bank_mask:0xf\n"
"v_max_u32 v2,v2,v16\n"
"v_min_u32_dpp v16,v0,v0 row_mirror row_mask:0xf bank_mask:0xf\n"
"v_max_u32_dpp v0,v0,v0 row_mirror row_mask:0xf bank_mask:0xf\n"
"v_max_u32_dpp v2,v2,v2 row_mirror row_mask:0xf bank_mask:0xf\n"
"v_max_u32 v2,v2,v16\n"
"v_min_u32_dpp v16,v0,v0 row_bcast:15 row_mask:0xa bank_mask:0xf\n"
"v_max_u32_dpp v0,v0,v0 row_bcast:15 row_mask:0xa bank_mask:0xf\n"
"v_max_u32_dpp v2,v2,v2 row_bcast:15 row_mask:0xa bank_mask:0xf\n"
"v_max_u32 v2,v2,v16\n"
"v_min_u32_dpp v16,v0,v0 row_bcast:31 row_mask:0xc bank_mask:0xf\n"
"v_max_u32_dpp v0,v0,v0 row_bcast:31 row_mask:0xc bank_mask:0xf\n"
"v_max_u32_dpp v2,v2,v2 row_bcast:31 row_mask:0xc bank_mask:0xf\n"
"v_max_u32 v2,v2,v16\n"
"v_readlane_b32 s90,v0,63\n"
"v_readlane_b32 s92,v2,63\n"
"s_nop 0\n"
"v_mov_b32 v4,s90\n"
"v_mov_b32 v5,s92\n"
"v_cndmask_b32 v6,v5,v4,vcc\n"
"v_not_b32 v7,v6\n"
"v_and_b32 v7,0x7ff,v7\n"
"v_lshl_add_u32 v8,v7,11,v57\n"
"v_and_b32 v9,15,v7\n"
"v_lshlrev_b32 v9,6,v9\n"
"v_xor_b32 v9,v9,v55\n"
"v_and_b32 v10,0x7f0,v7\n"
"v_lshl_add_u32 v10,v10,11,v9\n"
"s_mov_b32 s82,71303168\n"
"buffer_load_dwordx4 a[0:3],v10,s[24:27],s82 offen sc1\n"
"s_add_u32 s82,s82,4194304\n"
"buffer_load_dwordx4 a[4:7],v10,s[24:27],s82 offen sc1\n"
"s_add_u32 s82,s82,4194304\n"
"buffer_load_dwordx4 a[8:11],v10,s[24:27],s82 offen sc1\n"
"s_add_u32 s82,s82,4194304\n"
"buffer_load_dwordx4 a[12:15],v10,s[24:27],s82 offen sc1\n"
"buffer_load_dwordx4 a[16:19],v8,s[36:39],0 offen\n"
"v_bfe_u32 v9,v7,2,8\n"
"v_lshlrev_b32 v9,4,v9\n"
"buffer_load_dwordx4 v[12:15],v9,s[24:27],s83 offen sc1\n"
"s_mov_b32 s82,149979136\n"
"buffer_load_dwordx4 v[16:19],v9,s[24:27],s82 offen sc1\n"
"s_barrier\n"
"ds_read_b64_tr_b16 a[224:225],v192 offset:0\n"
"ds_read_b64_tr_b16 a[240:241],v192 offset:256\n"
"ds_read_b64_tr_b16 a[226:227],v193 offset:0\n"
"ds_read_b64_tr_b16 a[242:243],v193 offset:256\n"
"ds_read_b64_tr_b16 a[228:229],v194 offset:0\n"
"ds_read_b64_tr_b16 a[244:245],v194 offset:256\n"
"ds_read_b64_tr_b16 a[230:231],v195 offset:0\n"
"ds_read_b64_tr_b16 a[246:247],v195 offset:256\n"
"ds_read_b64_tr_b16 a[232:233],v196 offset:0\n"
"ds_read_b64_tr_b16 a[248:249],v196 offset:256\n"
"ds_read_b64_tr_b16 a[234:235],v197 offset:0\n"
"ds_read_b64_tr_b16 a[250:251],v197 offset:256\n"
"ds_read_b64_tr_b16 a[236:237],v198 offset:0\n"
"ds_read_b64_tr_b16 a[252:253],v198 offset:256\n"
"ds_read_b64_tr_b16 a[238:239],v199 offset:0\n"
"ds_read_b64_tr_b16 a[254:255],v199 offset:256\n"
"s_mov_b32 s84,0x40000\n"
"s_waitcnt vmcnt(0)\n"
".Lfk_ws:\n"
"v_xor_b32 v20,s80,v17\n"
"v_xor_b32 v21,s80,v19\n"
"v_or_b32 v20,v20,v21\n"
"v_cmp_ne_u32 vcc,0,v20\n"
"s_nop 0\n"
"s_cbranch_vccz .Lfk_ws_x\n"
"s_mov_b64 exec,vcc\n"
"s_mov_b32 s82,149979136\n"
"buffer_load_dwordx4 v[12:15],v9,s[24:27],s83 offen sc1\n"
"buffer_load_dwordx4 v[16:19],v9,s[24:27],s82 offen sc1\n"
"s_mov_b64 exec,-1\n"
"s_sub_u32 s84,s84,1\n"
"s_waitcnt vmcnt(0)\n"
"s_cbranch_scc0 .Lfk_ws\n"
".Lfk_ws_x:\n"
"v_mfma_f32_16x16x32_bf16 v[24:27],v[66:69],a[0:3],0\n"
"v_mfma_f32_16x16x32_bf16 v[28:31],v[64:67],a[0:3],0\n"
"v_mfma_f32_16x16x32_bf16 v[24:27],v[66:69],a[4:7],v[24:27]\n"
"v_mfma_f32_16x16x32_bf16 v[28:31],v[64:67],a[4:7],v[28:31]\n"
"v_mfma_f32_16x16x32_bf16 v[24:27],v[66:69],a[8:11],v[24:27]\n"
"v_mfma_f32_16x16x32_bf16 v[28:31],v[64:67],a[8:11],v[28:31]\n"
"v_mfma_f32_16x16x32_bf16 v[24:27],v[66:69],a[12:15],v[24:27]\n"
"v_mfma_f32_16x16x32_bf16 v[28:31],v[64:67],a[12:15],v[28:31]\n"
"v_mfma_f32_16x16x32_bf16 v[24:27],v[66:69],a[16:19],v[24:27]\n"
"v_cmp_eq_u32 vcc,v12,v6\n"
"v_mfma_f32_16x16x32_bf16 v[28:31],v[64:67],a[16:19],v[28:31]\n"
"s_nop 0\n"
"v_cndmask_b32 v10,v18,v16,vcc\n"
"v_lshlrev_b32 v11,16,v10\n"
"v_rcp_f32 v11,v11\n"
"v_and_b32 v10,0xffff0000,v10\n"
"s_nop 0\n"
"v_cvt_pk_bf16_f32 v20,v24,v25\n"
"v_cvt_pk_bf16_f32 v21,v26,v27\n"
"v_lshlrev_b32 v24,16,v20\n"
"v_and_b32 v25,0xffff0000,v20\n"
"v_cvt_pk_bf16_f32 v23,v30,v31\n"
"v_cvt_pk_bf16_f32 v22,v28,v29\n"
"v_lshlrev_b32 v26,16,v21\n"
"v_and_b32 v27,0xffff0000,v21\n"
"v_lshlrev_b32 v28,16,v22\n"
"v_and_b32 v29,0xffff0000,v22\n"
"v_lshlrev_b32 v30,16,v23\n"
"v_and_b32 v31,0xffff0000,v23\n"
"v_pk_add_f32 v[32:33],v[24:25],v[10:11] op_sel_hi:[1,0] neg_lo:[0,1] neg_hi:[0,1]\n"
"v_pk_add_f32 v[34:35],v[26:27],v[10:11] op_sel_hi:[1,0] neg_lo:[0,1] neg_hi:[0,1]\n"
"v_pk_add_f32 v[36:37],v[28:29],v[10:11] op_sel_hi:[1,0] neg_lo:[0,1] neg_hi:[0,1]\n"
"v_pk_add_f32 v[38:39],v[30:31],v[10:11] op_sel_hi:[1,0] neg_lo:[0,1] neg_hi:[0,1]\n"
"v_cvt_pk_bf16_f32 v20,v32,v33\n"
"v_cvt_pk_bf16_f32 v21,v34,v35\n"
"v_cvt_pk_bf16_f32 v22,v36,v37\n"
"v_cvt_pk_bf16_f32 v23,v38,v39\n"
"v_lshlrev_b32 v32,16,v20\n"
"v_and_b32 v33,0xffff0000,v20\n"
"v_lshlrev_b32 v34,16,v21\n"
"v_and_b32 v35,0xffff0000,v21\n"
"v_lshlrev_b32 v36,16,v22\n"
"v_and_b32 v37,0xffff0000,v22\n"
"v_lshlrev_b32 v38,16,v23\n"
"v_and_b32 v39,0xffff0000,v23\n"
"v_pk_mul_f32 v[32:33],v[32:33],v[10:11] op_sel:[0,1]\n"
"v_pk_mul_f32 v[34:35],v[34:35],v[10:11] op_sel:[0,1]\n"
"v_pk_mul_f32 v[36:37],v[36:37],v[10:11] op_sel:[0,1]\n"
"v_pk_mul_f32 v[38:39],v[38:39],v[10:11] op_sel:[0,1]\n"
"v_cvt_pk_bf16_f32 v20,v32,v33\n"
"v_cvt_pk_bf16_f32 v21,v34,v35\n"
"v_cvt_pk_bf16_f32 v22,v36,v37\n"
"v_cvt_pk_bf16_f32 v23,v38,v39\n"
"ds_write_b128 v58,v[20:23]\n"
"ds_read2_b64 v[128:131],v59 offset0:0 offset1:4\n"
"ds_read2_b64 v[132:135],v59 offset0:8 offset1:12\n"
"ds_read2_b64 v[136:139],v59 offset0:16 offset1:20\n"
"ds_read2_b64 v[140:143],v59 offset0:24 offset1:28\n"
"ds_read2_b64 v[144:147],v59 offset0:32 offset1:36\n"
"ds_read2_b64 v[148:151],v59 offset0:40 offset1:44\n"
"ds_read2_b64 v[152:155],v59 offset0:48 offset1:52\n"
"ds_read2_b64 v[156:159],v59 offset0:56 offset1:60\n"
"ds_write_b128 v60,v[24:27] offset:3072\n"
"ds_write_b128 v60,v[28:31] offset:4096\n"
"s_waitcnt lgkmcnt(9)\n"
"v_mfma_f32_16x16x32_bf16 v[40:43],a[160:163],v[128:131],0\n"
"v_mfma_f32_16x16x32_bf16 v[44:47],a[192:195],v[128:131],0\n"
"s_waitcnt lgkmcnt(8)\n"
"v_mfma_f32_16x16x32_bf16 v[40:43],a[164:167],v[132:135],v[40:43]\n"
"v_mfma_f32_16x16x32_bf16 v[44:47],a[196:199],v[132:135],v[44:47]\n"
"s_waitcnt lgkmcnt(7)\n"
"v_mfma_f32_16x16x32_bf16 v[40:43],a[168:171],v[136:139],v[40:43]\n"
"v_mfma_f32_16x16x32_bf16 v[44:47],a[200:203],v[136:139],v[44:47]\n"
"s_waitcnt lgkmcnt(6)\n"
"v_mfma_f32_16x16x32_bf16 v[40:43],a[172:175],v[140:143],v[40:43]\n"
"v_mfma_f32_16x16x32_bf16 v[44:47],a[204:207],v[140:143],v[44:47]\n"
"s_waitcnt lgkmcnt(5)\n"
"v_mfma_f32_16x16x32_bf16 v[40:43],a[176:179],v[144:147],v[40:43]\n"
"v_mfma_f32_16x16x32_bf16 v[44:47],a[208:211],v[144:147],v[44:47]\n"
"s_waitcnt lgkmcnt(4)\n"
"v_mfma_f32_16x16x32_bf16 v[40:43],a[180:183],v[148:151],v[40:43]\n"
"v_mfma_f32_16x16x32_bf16 v[44:47],a[212:215],v[148:151],v[44:47]\n"
"s_waitcnt lgkmcnt(3)\n"
"v_mfma_f32_16x16x32_bf16 v[40:43],a[184:187],v[152:155],v[40:43]\n"
"v_mfma_f32_16x16x32_bf16 v[44:47],a[216:219],v[152:155],v[44:47]\n"
"s_waitcnt lgkmcnt(2)\n"
"v_mfma_f32_16x16x32_bf16 v[40:43],a[188:191],v[156:159],v[40:43]\n"
"v_mfma_f32_16x16x32_bf16 v[44:47],a[220:223],v[156:159],v[44:47]\n"
"s_nop 6\n"
"ds_write_b128 v60,v[40:43] offset:1024\n"
"ds_write_b128 v60,v[44:47] offset:2048\n"
"s_waitcnt lgkmcnt(0)\n"
"s_barrier\n"
"ds_read_b128 v[128:131],v61 offset:1024\n"
"ds_read_b128 v[144:147],v61 offset:2048\n"
"ds_read_b128 v[132:135],v61 offset:9216\n"
"ds_read_b128 v[148:151],v61 offset:10240\n"
"ds_read_b128 v[136:139],v61 offset:17408\n"
"ds_read_b128 v[152:155],v61 offset:18432\n"
"ds_read_b128 v[140:143],v61 offset:25600\n"
"ds_read_b128 v[156:159],v61 offset:26624\n"
"s_waitcnt lgkmcnt(0)\n"
"v_pk_add_f32 v[128:129],v[128:129],v[132:133]\n"
"v_pk_add_f32 v[136:137],v[136:137],v[140:141]\n"
"v_pk_add_f32 v[130:131],v[130:131],v[134:135]\n"
"v_pk_add_f32 v[138:139],v[138:139],v[142:143]\n"
"v_pk_add_f32 v[128:129],v[128:129],v[136:137]\n"
"v_pk_add_f32 v[130:131],v[130:131],v[138:139]\n"
"v_pk_add_f32 v[144:145],v[144:145],v[148:149]\n"
"v_pk_add_f32 v[152:153],v[152:153],v[156:157]\n"
"v_pk_add_f32 v[146:147],v[146:147],v[150:151]\n"
"v_pk_add_f32 v[154:155],v[154:155],v[158:159]\n"
"v_pk_add_f32 v[144:145],v[144:145],v[152:153]\n"
"v_pk_add_f32 v[146:147],v[146:147],v[154:155]\n"
"v_mul_f32 v132,0xbfb8aa3b,v128\n"
"v_mul_f32 v133,0xbfb8aa3b,v129\n"
"v_mul_f32 v134,0xbfb8aa3b,v130\n"
"v_mul_f32 v135,0xbfb8aa3b,v131\n"
"v_exp_f32 v132,v132\n"
"v_exp_f32 v133,v133\n"
"v_exp_f32 v134,v134\n"
"v_exp_f32 v135,v135\n"
"v_add_f32 v132,1.0,v132\n"
"v_add_f32 v133,1.0,v133\n"
"v_add_f32 v134,1.0,v134\n"
"v_add_f32 v135,1.0,v135\n"
"v_rcp_f32 v132,v132\n"
"v_rcp_f32 v133,v133\n"
"v_rcp_f32 v134,v134\n"
"v_rcp_f32 v135,v135\n"
"v_pk_mul_f32 v[128:129],v[128:129],v[132:133]\n"
"v_pk_mul_f32 v[130:131],v[130:131],v[134:135]\n"
"v_pk_mul_f32 v[128:129],v[128:129],v[144:145]\n"
"v_pk_mul_f32 v[130:131],v[130:131],v[146:147]\n"
"v_cvt_pk_bf16_f32 v132,v128,v129\n"
"v_cvt_pk_bf16_f32 v133,v130,v131\n"
"v_cndmask_b32 v136,0,v132,s[8:9]\n"
"v_cndmask_b32 v137,0,v133,s[8:9]\n"
"v_cndmask_b32 v138,0,v132,s[10:11]\n"
"v_cndmask_b32 v139,0,v133,s[10:11]\n"
"v_cndmask_b32 v141,0,v133,s[12:13]\n"
"v_cndmask_b32 v143,0,v133,s[14:15]\n"
"v_mfma_f32_16x16x32_bf16 v[40:43],a[224:227],v[136:139],0\n"
"v_cndmask_b32 v142,0,v132,s[14:15]\n"
"v_cndmask_b32 v140,0,v132,s[12:13]\n"
"v_mfma_f32_16x16x32_bf16 v[44:47],a[240:243],v[136:139],0\n"
"v_cndmask_b32 v145,0,v133,s[16:17]\n"
"v_cndmask_b32 v147,0,v133,s[18:19]\n"
"v_mfma_f32_16x16x32_bf16 v[40:43],a[228:231],v[140:143],v[40:43]\n"
"v_cndmask_b32 v146,0,v132,s[18:19]\n"
"v_cndmask_b32 v144,0,v132,s[16:17]\n"
"v_mfma_f32_16x16x32_bf16 v[44:47],a[244:247],v[140:143],v[44:47]\n"
"v_cndmask_b32 v149,0,v133,s[20:21]\n"
"v_cndmask_b32 v151,0,v133,s[22:23]\n"
"v_mfma_f32_16x16x32_bf16 v[40:43],a[232:235],v[144:147],v[40:43]\n"
"v_cndmask_b32 v150,0,v132,s[22:23]\n"
"v_cndmask_b32 v148,0,v132,s[20:21]\n"
"v_mfma_f32_16x16x32_bf16 v[44:47],a[248:251],v[144:147],v[44:47]\n"
"s_nop 0\n"
"v_mfma_f32_16x16x32_bf16 v[40:43],a[236:239],v[148:151],v[40:43]\n"
"v_mfma_f32_16x16x32_bf16 v[44:47],a[252:255],v[148:151],v[44:47]\n"
"s_cmp_ge_u32 s63,s92\n"
"s_cselect_b32 s68,2146435072,s68\n"
"s_nop 4\n"
"v_or_b32 v40,1,v40\n"
"v_or_b32 v41,1,v41\n"
"v_or_b32 v42,1,v42\n"
"v_or_b32 v43,1,v43\n"
"v_or_b32 v44,1,v44\n"
"v_or_b32 v45,1,v45\n"
"v_or_b32 v46,1,v46\n"
"v_or_b32 v47,1,v47\n"
"buffer_store_dwordx4 v[40:43],v62,s[24:27],s69 offen offset:0 sc1\n"
"buffer_store_dwordx4 v[44:47],v62,s[24:27],s69 offen offset:512 sc1\n"
"buffer_store_dwordx4 v[248:251],v112,s[48:51],s68 offen offset:0 sc1\n"
"buffer_store_dwordx4 v[252:255],v112,s[48:51],s68 offen offset:1024 sc1\n"
"s_cmp_eq_u32 s60,0\n"
"s_cselect_b32 s82,s75,2146435072\n"
"buffer_store_dwordx2 a[154:155],off,s[24:27],s82 sc1\n"
"s_bfe_u32 s82,s73,0x20005\n"
"s_lshl_b32 s82,s82,13\n"
"s_and_b32 s83,s73,31\n"
"s_lshr_b32 s91,s73,7\n"
"s_lshl_b32 s89,s91,9\n"
"s_lshl4_add_u32 s89,s83,s89\n"
"s_add_u32 s89,s89,s82\n"
"s_add_u32 s89,s89,1024\n"
"s_lshl1_add_u32 s83,s83,s91\n"
"s_lshl4_add_u32 s82,s83,s82\n"
"s_add_u32 s82,s82,4096\n"
"v_bfe_u32 v0,v112,4,3\n"
"v_lshl_add_u32 v1,v0,1,s89\n"
"ds_read_u16 v2,v1\n"
"v_and_b32 v3,3,v0\n"
"v_lshl_add_u32 v3,v3,2,s82\n"
"v_lshrrev_b32 v4,2,v0\n"
"v_lshl_add_u32 v3,v4,10,v3\n"
"ds_read_b32 v3,v3\n"
"s_cmp_eq_u32 s91,0\n"
"s_cselect_b32 s82,s90,s92\n"
"s_not_b32 s83,s82\n"
"s_and_b32 s89,s83,0x7ff\n"
"s_lshl_b32 s89,s89,11\n"
"s_and_b32 s91,s73,127\n"
"s_lshl4_add_u32 s89,s91,s89\n"
"s_ashr_i32 s83,s83,31\n"
"s_or_b32 s83,s83,0x80000000\n"
"s_xor_b32 s82,s82,s83\n"
"s_and_b32 s82,s82,0xffff0000\n"
"v_mov_b32 v5,0xbfb8aa3b\n"
"v_mul_f32 v5,s82,v5\n"
"v_exp_f32 v5,v5\n"
"s_nop 0\n"
"v_add_f32 v5,1.0,v5\n"
"v_rcp_f32 v5,v5\n"
"s_nop 0\n"
"v_cvt_pk_bf16_f32 v5,v5,v5\n"
"v_lshlrev_b32 v5,16,v5\n"
"s_lshl_b32 s82,s52,7\n"
"s_add_u32 s82,s82,64\n"
"v_lshrrev_b32 v6,3,v112\n"
"v_and_b32 v6,0x60,v6\n"
"v_and_b32 v7,16,v112\n"
"v_add3_u32 v6,v6,v7,s82\n"
"v_lshrrev_b32 v7,2,v112\n"
"v_add_u32 v7,64,v7\n"
"v_lshlrev_b32 v8,1,v0\n"
"s_waitcnt lgkmcnt(0)\n"
"v_lshlrev_b32 v2,16,v2\n"
"s_sleep 6\n"
"s_mov_b32 s84,0x40000\n"
"buffer_load_dwordx4 v[16:19],v63,s[24:27],s70 offen sc1\n"
"buffer_load_dwordx4 v[20:23],v63,s[24:27],s71 offen sc1\n"
"s_waitcnt vmcnt(0)\n"
".Lfk_wy:\n"
"v_min3_u32 v24,v16,v17,v18\n"
"v_min3_u32 v25,v19,v20,v21\n"
"v_min3_u32 v24,v24,v22,v23\n"
"v_min_u32 v24,v24,v25\n"
"v_cmp_eq_u32 vcc,0,v24\n"
"s_nop 0\n"
"s_cbranch_vccz .Lfk_wy_x\n"
"s_sleep 1\n"
"s_mov_b64 exec,vcc\n"
"buffer_load_dwordx4 v[16:19],v63,s[24:27],s70 offen sc1\n"
"buffer_load_dwordx4 v[20:23],v63,s[24:27],s71 offen sc1\n"
"s_mov_b64 exec,-1\n"
"s_sub_u32 s84,s84,1\n"
"s_waitcnt vmcnt(0)\n"
"s_cbranch_scc0 .Lfk_wy\n"
".Lfk_wy_x:\n"
"s_mov_b32 vcc_lo,-1\n"
"s_mov_b32 vcc_hi,0x00ffffff\n"
"v_add_f32 v16,v16,v20\n"
"v_add_f32 v17,v17,v21\n"
"v_add_f32 v18,v18,v22\n"
"v_add_f32 v19,v19,v23\n"
"v_cndmask_b32 v16,0,v16,vcc\n"
"v_cndmask_b32 v17,0,v17,vcc\n"
"v_cndmask_b32 v18,0,v18,vcc\n"
"v_cndmask_b32 v19,0,v19,vcc\n"
"v_add_f32_dpp v16,v16,v16 quad_perm:[2,3,0,1] row_mask:0xf bank_mask:0xf\n"
"v_add_f32_dpp v17,v17,v17 quad_perm:[2,3,0,1] row_mask:0xf bank_mask:0xf\n"
"v_add_f32_dpp v18,v18,v18 quad_perm:[2,3,0,1] row_mask:0xf bank_mask:0xf\n"
"v_add_f32_dpp v19,v19,v19 quad_perm:[2,3,0,1] row_mask:0xf bank_mask:0xf\n"
"v_add_f32_dpp v16,v16,v16 row_ror:4 row_mask:0xf bank_mask:0xf\n"
"v_add_f32_dpp v17,v17,v17 row_ror:4 row_mask:0xf bank_mask:0xf\n"
"v_add_f32_dpp v18,v18,v18 row_ror:4 row_mask:0xf bank_mask:0xf\n"
"v_add_f32_dpp v19,v19,v19 row_ror:4 row_mask:0xf bank_mask:0xf\n"
"v_add_f32_dpp v16,v16,v16 row_ror:8 row_mask:0xf bank_mask:0xf\n"
"v_add_f32_dpp v17,v17,v17 row_ror:8 row_mask:0xf bank_mask:0xf\n"
"v_add_f32_dpp v18,v18,v18 row_ror:8 row_mask:0xf bank_mask:0xf\n"
"v_add_f32_dpp v19,v19,v19 row_ror:8 row_mask:0xf bank_mask:0xf\n"
"s_mov_b32 exec_lo,0x30003\n"
"s_mov_b32 exec_hi,0x30003\n"
"ds_write_b128 v6,v[16:19]\n"
"s_mov_b64 exec,-1\n"
"s_waitcnt lgkmcnt(0)\n"
"s_barrier\n"
"s_cmp_lg_u32 s52,0\n"
"s_cbranch_scc1 .Lfk_t2\n"
"ds_read2_b32 v[16:17],v7 offset0:0 offset1:64\n"
"s_waitcnt lgkmcnt(0)\n"
"v_add_f32 v16,v16,v17\n"
"s_nop 1\n"
"v_add_f32_dpp v16,v16,v16 row_ror:8 row_mask:0xf bank_mask:0xf\n"
"v_mov_b32 v17,v16\n"
"s_nop 1\n"
"v_permlane16_swap_b32 v17,v16\n"
"v_add_f32 v16,v16,v17\n"
"v_mov_b32 v17,v16\n"
"s_nop 1\n"
"v_permlane32_swap_b32 v17,v16\n"
"v_add_f32 v16,v16,v17\n"
"v_cvt_pk_bf16_f32 v16,v16,v16\n"
"v_lshlrev_b32 v16,16,v16\n"
"v_mul_f32 v16,v16,v5\n"
"v_cvt_pk_bf16_f32 v16,v16,v16\n"
"v_lshlrev_b32 v16,16,v16\n"
"v_add_f32 v16,v16,v2\n"
"v_cvt_pk_bf16_f32 v16,v16,v16\n"
"v_lshlrev_b32 v16,16,v16\n"
"v_add_f32 v16,v16,v3\n"
"s_nop 1\n"
"v_mov_b32_dpp v17,v16 quad_perm:[1,0,3,2] row_mask:0xf bank_mask:0xf\n"
"v_cvt_pk_bf16_f32 v16,v16,v17\n"
"s_mov_b64 exec,0x55\n"
"buffer_store_dword v16,v8,s[48:51],s89 offen sc1\n"
"s_mov_b64 exec,-1\n"
".Lfk_t2:\n"
"s_branch .Lfk_end\n"
".Lfk_g0:\n"
"s_waitcnt vmcnt(12) lgkmcnt(0)\n"
"s_barrier\n"
"v_mfma_f32_16x16x32_bf16 v[32:35],a[96:99],a[0:3],v[96:99]\n"
"s_add_u32 m0,s55,4096\n"
"v_exp_f32 v0,v0\n"
"v_mfma_f32_16x16x32_bf16 v[32:35],a[100:103],a[4:7],v[32:35]\n"
"s_add_u32 s54,s54,s22\n"
"v_exp_f32 v1,v1\n"
"v_mfma_f32_16x16x32_bf16 v[32:35],a[104:107],a[8:11],v[32:35]\n"
"buffer_load_dwordx4 v115,s[32:35],s54 offen sc1 lds\n"
"v_exp_f32 v2,v2\n"
"v_cvt_pk_bf16_f32 v64,v0,v1\n"
"v_mfma_f32_16x16x32_bf16 v[32:35],a[108:111],a[12:15],v[32:35]\n"
"v_exp_f32 v3,v3\n"
"v_mfma_f32_16x16x32_bf16 v[36:39],a[124:127],a[12:15],v[96:99]\n"
"v_exp_f32 v4,v4\n"
"v_cvt_pk_bf16_f32 v65,v2,v3\n"
"v_mfma_f32_16x16x32_bf16 v[36:39],a[120:123],a[8:11],v[36:39]\n"
"v_exp_f32 v5,v5\n"
"v_mfma_f32_16x16x32_bf16 v[36:39],a[116:119],a[4:7],v[36:39]\n"
"v_exp_f32 v6,v6\n"
"v_cvt_pk_bf16_f32 v66,v4,v5\n"
"v_mfma_f32_16x16x32_bf16 v[36:39],a[112:115],a[0:3],v[36:39]\n"
"v_exp_f32 v7,v7\n"
"v_mfma_f32_16x16x32_bf16 v[44:47],a[112:115],a[16:19],v[100:103]\n"
"v_exp_f32 v8,v8\n"
"v_cvt_pk_bf16_f32 v67,v6,v7\n"
"v_exp_f32 v9,v9\n"
"v_mfma_f32_16x16x32_bf16 v[44:47],a[116:119],a[20:23],v[44:47]\n"
"ds_read_b128 a[64:67],v113 offset:49152\n"
"ds_read_b128 a[68:71],v113 offset:50176\n"
"v_exp_f32 v10,v10\n"
"v_mfma_f32_16x16x32_bf16 v[44:47],a[120:123],a[24:27],v[44:47]\n"
"ds_read_b128 a[72:75],v113 offset:51200\n"
"ds_read_b128 a[76:79],v113 offset:52224\n"
"v_exp_f32 v11,v11\n"
"v_mfma_f32_16x16x32_bf16 v[44:47],a[124:127],a[28:31],v[44:47]\n"
"ds_read_b128 a[80:83],v113 offset:53248\n"
"ds_read_b128 a[84:87],v113 offset:54272\n"
"v_exp_f32 v12,v12\n"
"v_mfma_f32_16x16x32_bf16 v[40:43],a[108:111],a[28:31],v[100:103]\n"
"ds_read_b128 a[88:91],v113 offset:55296\n"
"ds_read_b128 a[92:95],v113 offset:56320\n"
"v_mfma_f32_16x16x32_bf16 v[40:43],a[104:107],a[24:27],v[40:43]\n"
"v_exp_f32 v13,v13\n"
"v_cvt_pk_bf16_f32 v68,v8,v9\n"
"v_cvt_pk_bf16_f32 v69,v10,v11\n"
"v_mfma_f32_16x16x32_bf16 v[40:43],a[100:103],a[20:23],v[40:43]\n"
"v_exp_f32 v14,v14\n"
"v_cvt_pk_bf16_f32 v70,v12,v13\n"
"v_mfma_f32_16x16x32_bf16 v[40:43],a[96:99],a[16:19],v[40:43]\n"
"v_exp_f32 v15,v15\n"
"v_mfma_f32_16x16x32_bf16 v[48:51],a[96:99],a[32:35],v[104:107]\n"
"v_exp_f32 v16,v16\n"
"v_cvt_pk_bf16_f32 v71,v14,v15\n"
"v_mfma_f32_16x16x32_bf16 v[48:51],a[100:103],a[36:39],v[48:51]\n"
"v_exp_f32 v17,v17\n"
"v_mfma_f32_16x16x32_bf16 v[48:51],a[104:107],a[40:43],v[48:51]\n"
"v_exp_f32 v18,v18\n"
"v_cvt_pk_bf16_f32 v72,v16,v17\n"
"v_mfma_f32_16x16x32_bf16 v[48:51],a[108:111],a[44:47],v[48:51]\n"
"v_exp_f32 v19,v19\n"
"v_mfma_f32_16x16x32_bf16 v[52:55],a[124:127],a[44:47],v[104:107]\n"
"v_exp_f32 v20,v20\n"
"v_cvt_pk_bf16_f32 v73,v18,v19\n"
"v_mfma_f32_16x16x32_bf16 v[52:55],a[120:123],a[40:43],v[52:55]\n"
"v_exp_f32 v21,v21\n"
"v_mfma_f32_16x16x32_bf16 v[52:55],a[116:119],a[36:39],v[52:55]\n"
"v_exp_f32 v22,v22\n"
"v_cvt_pk_bf16_f32 v74,v20,v21\n"
"v_mfma_f32_16x16x32_bf16 v[52:55],a[112:115],a[32:35],v[52:55]\n"
"v_exp_f32 v23,v23\n"
"v_mfma_f32_16x16x32_bf16 v[60:63],a[112:115],a[48:51],v[108:111]\n"
"v_exp_f32 v24,v24\n"
"v_cvt_pk_bf16_f32 v75,v22,v23\n"
"v_mfma_f32_16x16x32_bf16 v[60:63],a[116:119],a[52:55],v[60:63]\n"
"v_exp_f32 v25,v25\n"
"v_mfma_f32_16x16x32_bf16 v[60:63],a[120:123],a[56:59],v[60:63]\n"
"v_exp_f32 v26,v26\n"
"v_exp_f32 v27,v27\n"
"v_mfma_f32_16x16x32_bf16 v[60:63],a[124:127],a[60:63],v[60:63]\n"
"buffer_store_dwordx4 a[228:231],v112,s[24:27],s56 offen offset:1024 sc1\n"
"v_mfma_f32_16x16x32_bf16 v[56:59],a[108:111],a[60:63],v[108:111]\n"
"v_exp_f32 v28,v28\n"
"v_cvt_pk_bf16_f32 v77,v26,v27\n"
"v_mfma_f32_16x16x32_bf16 v[56:59],a[104:107],a[56:59],v[56:59]\n"
"v_exp_f32 v29,v29\n"
"v_mfma_f32_16x16x32_bf16 v[56:59],a[100:103],a[52:55],v[56:59]\n"
"v_exp_f32 v30,v30\n"
"v_cvt_pk_bf16_f32 v78,v28,v29\n"
"v_mfma_f32_16x16x32_bf16 v[56:59],a[96:99],a[48:51],v[56:59]\n"
"s_waitcnt vmcnt(10) lgkmcnt(0)\n"
"v_exp_f32 v31,v31\n"
"v_cvt_pk_bf16_f32 v76,v24,v25\n"
"v_cvt_pk_bf16_f32 v79,v30,v31\n"
"s_barrier\n"
"v_mfma_f32_16x16x32_bf16 v[0:3],a[64:67],a[0:3],v[96:99]\n"
"s_add_u32 m0,s55,8192\n"
"v_exp_f32 v122,v32\n"
"v_mfma_f32_16x16x32_bf16 v[0:3],a[68:71],a[4:7],v[0:3]\n"
"s_add_u32 s54,s54,s22\n"
"v_exp_f32 v123,v33\n"
"v_mfma_f32_16x16x32_bf16 v[0:3],a[72:75],a[8:11],v[0:3]\n"
"ds_read_b128 a[96:99],v114 offset:40960\n"
"ds_read_b128 a[100:103],v114 offset:41984\n"
"v_mfma_f32_16x16x32_bf16 v[0:3],a[76:79],a[12:15],v[0:3]\n"
"v_exp_f32 v119,v34\n"
"v_mfma_f32_16x16x32_bf16 v[4:7],a[92:95],a[12:15],v[96:99]\n"
"ds_read_b128 a[104:107],v114 offset:43008\n"
"v_cvt_pk_bf16_f32 v80,v122,v123\n"
"v_mfma_f32_16x16x32_bf16 v[4:7],a[88:91],a[8:11],v[4:7]\n"
"v_exp_f32 v120,v35\n"
"v_mfma_f32_16x16x32_bf16 v[4:7],a[84:87],a[4:7],v[4:7]\n"
"ds_read_b128 a[108:111],v114 offset:44032\n"
"ds_read_b128 a[112:115],v114 offset:45056\n"
"v_mfma_f32_16x16x32_bf16 v[4:7],a[80:83],a[0:3],v[4:7]\n"
"v_exp_f32 v122,v36\n"
"v_mfma_f32_16x16x32_bf16 v[12:15],a[80:83],a[16:19],v[100:103]\n"
"ds_read_b128 a[116:119],v114 offset:46080\n"
"v_cvt_pk_bf16_f32 v81,v119,v120\n"
"v_mfma_f32_16x16x32_bf16 v[12:15],a[84:87],a[20:23],v[12:15]\n"
"v_exp_f32 v123,v37\n"
"v_mfma_f32_16x16x32_bf16 v[12:15],a[88:91],a[24:27],v[12:15]\n"
"ds_read_b128 a[120:123],v114 offset:47104\n"
"ds_read_b128 a[124:127],v114 offset:48128\n"
"v_mfma_f32_16x16x32_bf16 v[12:15],a[92:95],a[28:31],v[12:15]\n"
"v_exp_f32 v119,v38\n"
"v_cvt_pk_bf16_f32 v82,v122,v123\n"
"v_mfma_f32_16x16x32_bf16 v[8:11],a[76:79],a[28:31],v[100:103]\n"
"buffer_store_dwordx4 a[168:171],v112,s[24:27],s3 offen offset:2048 sc1\n"
"v_mfma_f32_16x16x32_bf16 v[8:11],a[72:75],a[24:27],v[8:11]\n"
"v_exp_f32 v120,v39\n"
"v_mfma_f32_16x16x32_bf16 v[8:11],a[68:71],a[20:23],v[8:11]\n"
"buffer_load_dwordx4 v115,s[32:35],s54 offen sc1 lds\n"
"v_mfma_f32_16x16x32_bf16 v[8:11],a[64:67],a[16:19],v[8:11]\n"
"v_exp_f32 v122,v40\n"
"v_mfma_f32_16x16x32_bf16 v[16:19],a[64:67],a[32:35],v[104:107]\n"
"v_cvt_pk_bf16_f32 v83,v119,v120\n"
"v_or3_b32 v118,v80,v81,v82\n"
"v_mfma_f32_16x16x32_bf16 v[16:19],a[68:71],a[36:39],v[16:19]\n"
"v_exp_f32 v123,v41\n"
"v_mfma_f32_16x16x32_bf16 v[16:19],a[72:75],a[40:43],v[16:19]\n"
"v_cvt_pk_bf16_f32 v84,v122,v123\n"
"v_or3_b32 v118,v118,v83,v84\n"
"v_mfma_f32_16x16x32_bf16 v[16:19],a[76:79],a[44:47],v[16:19]\n"
"v_exp_f32 v119,v42\n"
"v_mfma_f32_16x16x32_bf16 v[20:23],a[92:95],a[44:47],v[104:107]\n"
"v_mfma_f32_16x16x32_bf16 v[20:23],a[88:91],a[40:43],v[20:23]\n"
"v_exp_f32 v120,v43\n"
"v_mfma_f32_16x16x32_bf16 v[20:23],a[84:87],a[36:39],v[20:23]\n"
"v_cvt_pk_bf16_f32 v85,v119,v120\n"
"v_mfma_f32_16x16x32_bf16 v[20:23],a[80:83],a[32:35],v[20:23]\n"
"v_exp_f32 v122,v44\n"
"v_mfma_f32_16x16x32_bf16 v[28:31],a[80:83],a[48:51],v[108:111]\n"
"buffer_store_dwordx4 a[172:175],v112,s[24:27],s3 offen offset:3072 sc1\n"
"v_mfma_f32_16x16x32_bf16 v[28:31],a[84:87],a[52:55],v[28:31]\n"
"v_exp_f32 v123,v45\n"
"v_mfma_f32_16x16x32_bf16 v[28:31],a[88:91],a[56:59],v[28:31]\n"
"s_sub_u32 s3,s3,16384\n"
"v_mfma_f32_16x16x32_bf16 v[28:31],a[92:95],a[60:63],v[28:31]\n"
"v_exp_f32 v119,v46\n"
"v_mfma_f32_16x16x32_bf16 v[24:27],a[76:79],a[60:63],v[108:111]\n"
"v_cvt_pk_bf16_f32 v86,v122,v123\n"
"v_or3_b32 v118,v118,v85,v86\n"
"v_mfma_f32_16x16x32_bf16 v[24:27],a[72:75],a[56:59],v[24:27]\n"
"v_exp_f32 v120,v47\n"
"v_mfma_f32_16x16x32_bf16 v[24:27],a[68:71],a[52:55],v[24:27]\n"
"v_cvt_pk_bf16_f32 v87,v119,v120\n"
"v_mfma_f32_16x16x32_bf16 v[24:27],a[64:67],a[48:51],v[24:27]\n"
"s_waitcnt lgkmcnt(0)\n"
"v_exp_f32 v122,v48\n"
"v_mfma_f32_16x16x32_bf16 v[128:131],a[96:99],v[64:67],0\n"
"v_exp_f32 v123,v49\n"
"v_mfma_f32_16x16x32_bf16 v[160:163],a[96:99],v[68:71],0\n"
"ds_read_b128 a[64:67],v113 offset:32768\n"
"ds_read_b128 a[68:71],v113 offset:33792\n"
"v_mfma_f32_16x16x32_bf16 v[192:195],a[96:99],v[72:75],0\n"
"v_exp_f32 v119,v50\n"
"v_mfma_f32_16x16x32_bf16 v[224:227],a[96:99],v[76:79],0\n"
"ds_read_b128 a[72:75],v113 offset:34816\n"
"v_cvt_pk_bf16_f32 v88,v122,v123\n"
"v_mfma_f32_16x16x32_bf16 a[140:143],a[144:147],v[76:79],0\n"
"v_exp_f32 v120,v51\n"
"v_mfma_f32_16x16x32_bf16 v[228:231],a[100:103],v[76:79],0\n"
"ds_read_b128 a[76:79],v113 offset:35840\n"
"ds_read_b128 a[80:83],v113 offset:36864\n"
"v_mfma_f32_16x16x32_bf16 v[132:135],a[100:103],v[64:67],0\n"
"v_exp_f32 v122,v52\n"
"v_mfma_f32_16x16x32_bf16 v[164:167],a[100:103],v[68:71],0\n"
"ds_read_b128 a[84:87],v113 offset:37888\n"
"v_cvt_pk_bf16_f32 v89,v119,v120\n"
"v_mfma_f32_16x16x32_bf16 v[196:199],a[100:103],v[72:75],0\n"
"v_exp_f32 v123,v53\n"
"v_mfma_f32_16x16x32_bf16 a[136:139],a[144:147],v[72:75],0\n"
"ds_read_b128 a[88:91],v113 offset:38912\n"
"ds_read_b128 a[92:95],v113 offset:39936\n"
"v_mfma_f32_16x16x32_bf16 v[200:203],a[104:107],v[72:75],0\n"
"v_exp_f32 v119,v54\n"
"v_cvt_pk_bf16_f32 v90,v122,v123\n"
"v_mfma_f32_16x16x32_bf16 v[232:235],a[104:107],v[76:79],0\n"
"buffer_store_dwordx4 a[176:179],v112,s[24:27],s3 offen offset:0 sc1\n"
"v_mfma_f32_16x16x32_bf16 v[136:139],a[104:107],v[64:67],0\n"
"v_exp_f32 v120,v55\n"
"v_mfma_f32_16x16x32_bf16 v[168:171],a[104:107],v[68:71],0\n"
"v_cvt_pk_bf16_f32 v91,v119,v120\n"
"v_or3_b32 v118,v118,v87,v88\n"
"v_mfma_f32_16x16x32_bf16 a[132:135],a[144:147],v[68:71],0\n"
"v_exp_f32 v122,v56\n"
"v_mfma_f32_16x16x32_bf16 v[172:175],a[108:111],v[68:71],0\n"
"v_or3_b32 v118,v118,v89,v90\n"
"v_mfma_f32_16x16x32_bf16 v[204:207],a[108:111],v[72:75],0\n"
"v_exp_f32 v123,v57\n"
"v_mfma_f32_16x16x32_bf16 v[236:239],a[108:111],v[76:79],0\n"
"v_cvt_pk_bf16_f32 v92,v122,v123\n"
"v_or3_b32 v118,v118,v91,v92\n"
"v_mfma_f32_16x16x32_bf16 v[140:143],a[108:111],v[64:67],0\n"
"v_exp_f32 v119,v58\n"
"v_mfma_f32_16x16x32_bf16 a[128:131],a[144:147],v[64:67],0\n"
"v_mfma_f32_16x16x32_bf16 v[144:147],a[112:115],v[64:67],0\n"
"v_exp_f32 v120,v59\n"
"v_mfma_f32_16x16x32_bf16 v[176:179],a[112:115],v[68:71],0\n"
"v_cvt_pk_bf16_f32 v93,v119,v120\n"
"v_mfma_f32_16x16x32_bf16 v[208:211],a[112:115],v[72:75],0\n"
"v_exp_f32 v122,v60\n"
"v_mfma_f32_16x16x32_bf16 v[240:243],a[112:115],v[76:79],0\n"
"v_mfma_f32_16x16x32_bf16 v[244:247],a[116:119],v[76:79],0\n"
"v_exp_f32 v123,v61\n"
"v_mfma_f32_16x16x32_bf16 v[148:151],a[116:119],v[64:67],0\n"
"v_cvt_pk_bf16_f32 v94,v122,v123\n"
"v_or3_b32 v118,v118,v93,v94\n"
"v_mfma_f32_16x16x32_bf16 v[180:183],a[116:119],v[68:71],0\n"
"v_exp_f32 v119,v62\n"
"v_mfma_f32_16x16x32_bf16 v[212:215],a[116:119],v[72:75],0\n"
"buffer_store_dwordx4 a[232:235],v112,s[24:27],s56 offen offset:2048 sc1\n"
"v_mfma_f32_16x16x32_bf16 v[216:219],a[120:123],v[72:75],0\n"
"v_exp_f32 v120,v63\n"
"v_mfma_f32_16x16x32_bf16 v[248:251],a[120:123],v[76:79],0\n"
"v_cvt_pk_bf16_f32 v95,v119,v120\n"
"v_or_b32 v118,v118,v95\n"
"v_mfma_f32_16x16x32_bf16 v[152:155],a[120:123],v[64:67],0\n"
"v_and_b32 v118,0x40004000,v118\n"
"v_cmp_ne_u32 vcc,0,v118\n"
"v_mfma_f32_16x16x32_bf16 v[184:187],a[120:123],v[68:71],0\n"
"v_mfma_f32_16x16x32_bf16 v[188:191],a[124:127],v[68:71],0\n"
"v_mfma_f32_16x16x32_bf16 v[220:223],a[124:127],v[72:75],0\n"
"v_mfma_f32_16x16x32_bf16 v[252:255],a[124:127],v[76:79],0\n"
"v_mfma_f32_16x16x32_bf16 v[156:159],a[124:127],v[64:67],0\n"
"s_cbranch_vccnz .Lfk_cs_k1\n"
".Lfk_cr_k1:\n"
"s_waitcnt lgkmcnt(0)\n"
"s_barrier\n"
"v_mfma_f32_16x16x32_bf16 v[32:35],a[64:67],a[0:3],v[96:99]\n"
"s_add_u32 m0,s55,12288\n"
"v_exp_f32 v122,v0\n"
"v_mfma_f32_16x16x32_bf16 v[32:35],a[68:71],a[4:7],v[32:35]\n"
"s_add_u32 s54,s54,s22\n"
"v_exp_f32 v123,v1\n"
"v_mfma_f32_16x16x32_bf16 v[32:35],a[72:75],a[8:11],v[32:35]\n"
"ds_read_b128 a[96:99],v114 offset:8192\n"
"ds_read_b128 a[100:103],v114 offset:9216\n"
"v_mfma_f32_16x16x32_bf16 v[32:35],a[76:79],a[12:15],v[32:35]\n"
"v_exp_f32 v119,v2\n"
"v_mfma_f32_16x16x32_bf16 v[36:39],a[92:95],a[12:15],v[96:99]\n"
"ds_read_b128 a[104:107],v114 offset:10240\n"
"v_cvt_pk_bf16_f32 v64,v122,v123\n"
"v_mfma_f32_16x16x32_bf16 v[36:39],a[88:91],a[8:11],v[36:39]\n"
"v_exp_f32 v120,v3\n"
"v_mfma_f32_16x16x32_bf16 v[36:39],a[84:87],a[4:7],v[36:39]\n"
"ds_read_b128 a[108:111],v114 offset:11264\n"
"ds_read_b128 a[112:115],v114 offset:12288\n"
"v_mfma_f32_16x16x32_bf16 v[36:39],a[80:83],a[0:3],v[36:39]\n"
"v_exp_f32 v122,v4\n"
"v_mfma_f32_16x16x32_bf16 v[44:47],a[80:83],a[16:19],v[100:103]\n"
"ds_read_b128 a[116:119],v114 offset:13312\n"
"v_cvt_pk_bf16_f32 v65,v119,v120\n"
"v_mfma_f32_16x16x32_bf16 v[44:47],a[84:87],a[20:23],v[44:47]\n"
"v_exp_f32 v123,v5\n"
"v_mfma_f32_16x16x32_bf16 v[44:47],a[88:91],a[24:27],v[44:47]\n"
"ds_read_b128 a[120:123],v114 offset:14336\n"
"ds_read_b128 a[124:127],v114 offset:15360\n"
"v_mfma_f32_16x16x32_bf16 v[44:47],a[92:95],a[28:31],v[44:47]\n"
"v_exp_f32 v119,v6\n"
"v_cvt_pk_bf16_f32 v66,v122,v123\n"
"v_mfma_f32_16x16x32_bf16 v[40:43],a[76:79],a[28:31],v[100:103]\n"
"buffer_store_dwordx4 a[180:183],v112,s[24:27],s3 offen offset:1024 sc1\n"
"v_mfma_f32_16x16x32_bf16 v[40:43],a[72:75],a[24:27],v[40:43]\n"
"v_exp_f32 v120,v7\n"
"v_mfma_f32_16x16x32_bf16 v[40:43],a[68:71],a[20:23],v[40:43]\n"
"buffer_load_dwordx4 v115,s[32:35],s54 offen sc1 lds\n"
"v_mfma_f32_16x16x32_bf16 v[40:43],a[64:67],a[16:19],v[40:43]\n"
"v_exp_f32 v122,v8\n"
"v_mfma_f32_16x16x32_bf16 v[48:51],a[64:67],a[32:35],v[104:107]\n"
"v_cvt_pk_bf16_f32 v67,v119,v120\n"
"v_or3_b32 v118,v64,v65,v66\n"
"v_mfma_f32_16x16x32_bf16 v[48:51],a[68:71],a[36:39],v[48:51]\n"
"v_exp_f32 v123,v9\n"
"v_mfma_f32_16x16x32_bf16 v[48:51],a[72:75],a[40:43],v[48:51]\n"
"v_cvt_pk_bf16_f32 v68,v122,v123\n"
"v_or3_b32 v118,v118,v67,v68\n"
"v_mfma_f32_16x16x32_bf16 v[48:51],a[76:79],a[44:47],v[48:51]\n"
"v_exp_f32 v119,v10\n"
"v_mfma_f32_16x16x32_bf16 v[52:55],a[92:95],a[44:47],v[104:107]\n"
"v_mfma_f32_16x16x32_bf16 v[52:55],a[88:91],a[40:43],v[52:55]\n"
"v_exp_f32 v120,v11\n"
"v_mfma_f32_16x16x32_bf16 v[52:55],a[84:87],a[36:39],v[52:55]\n"
"v_cvt_pk_bf16_f32 v69,v119,v120\n"
"v_mfma_f32_16x16x32_bf16 v[52:55],a[80:83],a[32:35],v[52:55]\n"
"v_exp_f32 v122,v12\n"
"v_mfma_f32_16x16x32_bf16 v[60:63],a[80:83],a[48:51],v[108:111]\n"
"buffer_store_dwordx4 a[184:187],v112,s[24:27],s3 offen offset:2048 sc1\n"
"v_mfma_f32_16x16x32_bf16 v[60:63],a[84:87],a[52:55],v[60:63]\n"
"v_exp_f32 v123,v13\n"
"v_mfma_f32_16x16x32_bf16 v[60:63],a[88:91],a[56:59],v[60:63]\n"
"v_cvt_pk_bf16_f32 v70,v122,v123\n"
"v_mfma_f32_16x16x32_bf16 v[60:63],a[92:95],a[60:63],v[60:63]\n"
"v_exp_f32 v119,v14\n"
"v_mfma_f32_16x16x32_bf16 v[56:59],a[76:79],a[60:63],v[108:111]\n"
"v_or3_b32 v118,v118,v69,v70\n"
"v_mfma_f32_16x16x32_bf16 v[56:59],a[72:75],a[56:59],v[56:59]\n"
"v_exp_f32 v120,v15\n"
"v_mfma_f32_16x16x32_bf16 v[56:59],a[68:71],a[52:55],v[56:59]\n"
"v_cvt_pk_bf16_f32 v71,v119,v120\n"
"v_mfma_f32_16x16x32_bf16 v[56:59],a[64:67],a[48:51],v[56:59]\n"
"s_waitcnt lgkmcnt(0)\n"
"v_exp_f32 v122,v16\n"
"v_mfma_f32_16x16x32_bf16 v[128:131],a[96:99],v[80:83],v[128:131]\n"
"v_exp_f32 v123,v17\n"
"v_mfma_f32_16x16x32_bf16 v[160:163],a[96:99],v[84:87],v[160:163]\n"
"ds_read_b128 a[64:67],v113 offset:0\n"
"ds_read_b128 a[68:71],v113 offset:1024\n"
"v_mfma_f32_16x16x32_bf16 v[192:195],a[96:99],v[88:91],v[192:195]\n"
"v_exp_f32 v119,v18\n"
"v_mfma_f32_16x16x32_bf16 v[224:227],a[96:99],v[92:95],v[224:227]\n"
"ds_read_b128 a[72:75],v113 offset:2048\n"
"v_cvt_pk_bf16_f32 v72,v122,v123\n"
"v_mfma_f32_16x16x32_bf16 a[140:143],a[144:147],v[92:95],a[140:143]\n"
"v_exp_f32 v120,v19\n"
"v_mfma_f32_16x16x32_bf16 v[228:231],a[100:103],v[92:95],v[228:231]\n"
"ds_read_b128 a[76:79],v113 offset:3072\n"
"ds_read_b128 a[80:83],v113 offset:4096\n"
"v_mfma_f32_16x16x32_bf16 v[132:135],a[100:103],v[80:83],v[132:135]\n"
"v_exp_f32 v122,v20\n"
"v_mfma_f32_16x16x32_bf16 v[164:167],a[100:103],v[84:87],v[164:167]\n"
"ds_read_b128 a[84:87],v113 offset:5120\n"
"v_cvt_pk_bf16_f32 v73,v119,v120\n"
"v_mfma_f32_16x16x32_bf16 v[196:199],a[100:103],v[88:91],v[196:199]\n"
"v_exp_f32 v123,v21\n"
"v_mfma_f32_16x16x32_bf16 a[136:139],a[144:147],v[88:91],a[136:139]\n"
"ds_read_b128 a[88:91],v113 offset:6144\n"
"ds_read_b128 a[92:95],v113 offset:7168\n"
"v_mfma_f32_16x16x32_bf16 v[200:203],a[104:107],v[88:91],v[200:203]\n"
"v_exp_f32 v119,v22\n"
"v_mfma_f32_16x16x32_bf16 v[232:235],a[104:107],v[92:95],v[232:235]\n"
"buffer_store_dwordx4 a[188:191],v112,s[24:27],s3 offen offset:3072 sc1\n"
"v_mfma_f32_16x16x32_bf16 v[136:139],a[104:107],v[80:83],v[136:139]\n"
"v_exp_f32 v120,v23\n"
"v_mfma_f32_16x16x32_bf16 v[168:171],a[104:107],v[84:87],v[168:171]\n"
"s_or_b32 s3,s3,0x3c000\n"
"v_cvt_pk_bf16_f32 v74,v122,v123\n"
"v_cvt_pk_bf16_f32 v75,v119,v120\n"
"v_mfma_f32_16x16x32_bf16 a[132:135],a[144:147],v[84:87],a[132:135]\n"
"v_exp_f32 v122,v24\n"
"v_mfma_f32_16x16x32_bf16 v[172:175],a[108:111],v[84:87],v[172:175]\n"
"v_or3_b32 v118,v118,v71,v72\n"
"v_or3_b32 v118,v118,v73,v74\n"
"v_mfma_f32_16x16x32_bf16 v[204:207],a[108:111],v[88:91],v[204:207]\n"
"v_exp_f32 v123,v25\n"
"v_mfma_f32_16x16x32_bf16 v[236:239],a[108:111],v[92:95],v[236:239]\n"
"v_cvt_pk_bf16_f32 v76,v122,v123\n"
"v_or3_b32 v118,v118,v75,v76\n"
"v_mfma_f32_16x16x32_bf16 v[140:143],a[108:111],v[80:83],v[140:143]\n"
"v_exp_f32 v119,v26\n"
"v_mfma_f32_16x16x32_bf16 a[128:131],a[144:147],v[80:83],a[128:131]\n"
"v_mfma_f32_16x16x32_bf16 v[144:147],a[112:115],v[80:83],v[144:147]\n"
"v_exp_f32 v120,v27\n"
"v_mfma_f32_16x16x32_bf16 v[176:179],a[112:115],v[84:87],v[176:179]\n"
"v_cvt_pk_bf16_f32 v77,v119,v120\n"
"v_mfma_f32_16x16x32_bf16 v[208:211],a[112:115],v[88:91],v[208:211]\n"
"v_exp_f32 v122,v28\n"
"v_mfma_f32_16x16x32_bf16 v[240:243],a[112:115],v[92:95],v[240:243]\n"
"v_mfma_f32_16x16x32_bf16 v[244:247],a[116:119],v[92:95],v[244:247]\n"
"v_exp_f32 v123,v29\n"
"v_mfma_f32_16x16x32_bf16 v[148:151],a[116:119],v[80:83],v[148:151]\n"
"v_cvt_pk_bf16_f32 v78,v122,v123\n"
"v_or3_b32 v118,v118,v77,v78\n"
"v_mfma_f32_16x16x32_bf16 v[180:183],a[116:119],v[84:87],v[180:183]\n"
"v_exp_f32 v119,v30\n"
"v_mfma_f32_16x16x32_bf16 v[212:215],a[116:119],v[88:91],v[212:215]\n"
"buffer_store_dwordx4 a[236:239],v112,s[24:27],s56 offen offset:3072 sc1\n"
"v_mfma_f32_16x16x32_bf16 v[216:219],a[120:123],v[88:91],v[216:219]\n"
"v_exp_f32 v120,v31\n"
"v_mfma_f32_16x16x32_bf16 v[248:251],a[120:123],v[92:95],v[248:251]\n"
"v_cvt_pk_bf16_f32 v79,v119,v120\n"
"v_or_b32 v118,v118,v79\n"
"v_mfma_f32_16x16x32_bf16 v[152:155],a[120:123],v[80:83],v[152:155]\n"
"v_and_b32 v118,0x40004000,v118\n"
"v_cmp_ne_u32 vcc,0,v118\n"
"v_mfma_f32_16x16x32_bf16 v[184:187],a[120:123],v[84:87],v[184:187]\n"
"v_mfma_f32_16x16x32_bf16 v[188:191],a[124:127],v[84:87],v[188:191]\n"
"v_mfma_f32_16x16x32_bf16 v[220:223],a[124:127],v[88:91],v[220:223]\n"
"v_mfma_f32_16x16x32_bf16 v[252:255],a[124:127],v[92:95],v[252:255]\n"
"v_mfma_f32_16x16x32_bf16 v[156:159],a[124:127],v[80:83],v[156:159]\n"
"s_cbranch_vccnz .Lfk_cs_k2\n"
".Lfk_cr_k2:\n"
"s_waitcnt vmcnt(15) lgkmcnt(0)\n"
"s_barrier\n"
"s_branch .Lfk_g1\n"
".Lfk_winA:\n"
"v_max3_f32 v118,v32,v33,v34\n"
"v_max3_f32 v119,v35,v36,v37\n"
"v_max3_f32 v119,v119,v38,v39\n"
"v_max3_f32 v118,v118,v40,v41\n"
"v_max3_f32 v119,v119,v42,v43\n"
"v_max3_f32 v118,v118,v44,v45\n"
"v_max3_f32 v119,v119,v46,v47\n"
"v_max3_f32 v118,v118,v48,v49\n"
"v_max3_f32 v119,v119,v50,v51\n"
"v_max3_f32 v118,v118,v52,v53\n"
"v_max3_f32 v119,v119,v54,v55\n"
"v_max3_f32 v118,v118,v56,v57\n"
"v_max3_f32 v119,v119,v58,v59\n"
"v_max3_f32 v118,v118,v60,v61\n"
"v_max3_f32 v119,v119,v62,v63\n"
"v_max_f32 v118,v118,v119\n"
"v_cmp_lt_f32 vcc,s93,v118\n"
"s_nop 0\n"
"s_cbranch_vccnz .Lfk_cs_x2\n"
".Lfk_cr_x2:\n"
"s_mov_b32 s64,s61\n"
"s_cmp_eq_u32 s52,3\n"
"s_cselect_b32 s67,172032,0\n"
"v_mfma_f32_16x16x32_bf16 v[8:11],a[64:67],a[16:19],v[100:103]\n"
"s_swappc_b64 s[94:95],s[20:21]\n"
"ds_read_b128 a[96:99],v114 offset:24576\n"
"ds_read_b128 a[100:103],v114 offset:25600\n"
"v_mfma_f32_16x16x32_bf16 v[8:11],a[68:71],a[20:23],v[8:11]\n"
"ds_read_b128 a[104:107],v114 offset:26624\n"
"ds_read_b128 a[108:111],v114 offset:27648\n"
"v_mfma_f32_16x16x32_bf16 v[8:11],a[72:75],a[24:27],v[8:11]\n"
"ds_read_b128 a[112:115],v114 offset:28672\n"
"ds_read_b128 a[116:119],v114 offset:29696\n"
"v_mfma_f32_16x16x32_bf16 v[8:11],a[76:79],a[28:31],v[8:11]\n"
"ds_read_b128 a[120:123],v114 offset:30720\n"
"ds_read_b128 a[124:127],v114 offset:31744\n"
"v_mfma_f32_16x16x32_bf16 v[12:15],a[92:95],a[28:31],v[100:103]\n"
"v_add_f32 v0,0x80000000,v126\n"
"v_min_f32 v0,v0,v96\n"
"v_mfma_f32_16x16x32_bf16 v[12:15],a[88:91],a[24:27],v[12:15]\n"
"v_add_f32 v1,0xf149f2ca,v126\n"
"v_min_f32 v1,v1,v97\n"
"v_mfma_f32_16x16x32_bf16 v[12:15],a[84:87],a[20:23],v[12:15]\n"
"v_add_f32 v2,0xf1c9f2ca,v126\n"
"v_min_f32 v2,v2,v98\n"
"v_mfma_f32_16x16x32_bf16 v[12:15],a[80:83],a[16:19],v[12:15]\n"
"v_add_f32 v3,0xf2177617,v126\n"
"v_min_f32 v3,v3,v99\n"
"v_mfma_f32_16x16x32_bf16 v[20:23],a[80:83],a[32:35],v[104:107]\n"
"v_add_f32 v4,0xf349f2ca,v126\n"
"v_min_f32 v4,v4,v96\n"
"v_mfma_f32_16x16x32_bf16 v[20:23],a[84:87],a[36:39],v[20:23]\n"
"v_add_f32 v5,0xf35691f6,v126\n"
"v_min_f32 v5,v5,v97\n"
"v_mfma_f32_16x16x32_bf16 v[20:23],a[88:91],a[40:43],v[20:23]\n"
"v_add_f32 v6,0xf3633123,v126\n"
"v_min_f32 v6,v6,v98\n"
"v_mfma_f32_16x16x32_bf16 v[20:23],a[92:95],a[44:47],v[20:23]\n"
"v_add_f32 v7,0xf36fd050,v126\n"
"v_min_f32 v7,v7,v99\n"
"v_mfma_f32_16x16x32_bf16 v[16:19],a[76:79],a[44:47],v[104:107]\n"
"s_add_u32 s101,s101,4\n"
"v_exp_f32 v32,v32\n"
"v_mfma_f32_16x16x32_bf16 v[16:19],a[72:75],a[40:43],v[16:19]\n"
"s_add_u32 m0,s67,0\n"
"v_exp_f32 v33,v33\n"
"v_mfma_f32_16x16x32_bf16 v[16:19],a[68:71],a[36:39],v[16:19]\n"
"v_exp_f32 v34,v34\n"
"v_mfma_f32_16x16x32_bf16 v[16:19],a[64:67],a[32:35],v[16:19]\n"
"v_exp_f32 v35,v35\n"
"v_mfma_f32_16x16x32_bf16 v[24:27],a[64:67],a[48:51],v[108:111]\n"
"v_exp_f32 v36,v36\n"
"v_mfma_f32_16x16x32_bf16 v[24:27],a[68:71],a[52:55],v[24:27]\n"
"v_exp_f32 v37,v37\n"
"v_mfma_f32_16x16x32_bf16 v[24:27],a[72:75],a[56:59],v[24:27]\n"
"v_exp_f32 v38,v38\n"
"v_mfma_f32_16x16x32_bf16 v[24:27],a[76:79],a[60:63],v[24:27]\n"
"v_exp_f32 v39,v39\n"
"v_mfma_f32_16x16x32_bf16 v[28:31],a[92:95],a[60:63],v[108:111]\n"
"v_exp_f32 v40,v40\n"
"v_mfma_f32_16x16x32_bf16 v[28:31],a[88:91],a[56:59],v[28:31]\n"
"v_exp_f32 v41,v41\n"
"v_mfma_f32_16x16x32_bf16 v[28:31],a[84:87],a[52:55],v[28:31]\n"
"v_exp_f32 v42,v42\n"
"v_mfma_f32_16x16x32_bf16 v[28:31],a[80:83],a[48:51],v[28:31]\n"
"v_exp_f32 v43,v43\n"
"v_mfma_f32_16x16x32_bf16 v[4:7],a[80:83],a[0:3],v[4:7]\n"
"v_exp_f32 v44,v44\n"
"v_mfma_f32_16x16x32_bf16 v[4:7],a[84:87],a[4:7],v[4:7]\n"
"v_exp_f32 v45,v45\n"
"v_mfma_f32_16x16x32_bf16 v[4:7],a[88:91],a[8:11],v[4:7]\n"
"buffer_load_dwordx4 v112,s[24:27],s64 offen offset:0 sc1 lds\n"
"v_mfma_f32_16x16x32_bf16 v[4:7],a[92:95],a[12:15],v[4:7]\n"
"v_exp_f32 v46,v46\n"
"v_mfma_f32_16x16x32_bf16 v[0:3],a[76:79],a[12:15],v[0:3]\n"
"buffer_load_dwordx4 v112,s[24:27],s64 offen offset:1024 sc1 lds\n"
"v_mfma_f32_16x16x32_bf16 v[0:3],a[72:75],a[8:11],v[0:3]\n"
"v_exp_f32 v47,v47\n"
"v_mfma_f32_16x16x32_bf16 v[0:3],a[68:71],a[4:7],v[0:3]\n"
"buffer_load_dwordx4 v112,s[24:27],s64 offen offset:2048 sc1 lds\n"
"v_mfma_f32_16x16x32_bf16 v[0:3],a[64:67],a[0:3],v[0:3]\n"
"s_waitcnt lgkmcnt(0)\n"
"v_exp_f32 v48,v48\n"
"v_mfma_f32_16x16x32_bf16 v[128:131],a[96:99],v[64:67],v[128:131]\n"
"v_exp_f32 v49,v49\n"
"v_mfma_f32_16x16x32_bf16 v[160:163],a[96:99],v[68:71],v[160:163]\n"
"ds_read_b128 a[64:67],v113 offset:16384\n"
"ds_read_b128 a[68:71],v113 offset:17408\n"
"v_mfma_f32_16x16x32_bf16 v[192:195],a[96:99],v[72:75],v[192:195]\n"
"v_exp_f32 v50,v50\n"
"v_mfma_f32_16x16x32_bf16 v[224:227],a[96:99],v[76:79],v[224:227]\n"
"ds_read_b128 a[72:75],v113 offset:18432\n"
"ds_read_b128 a[76:79],v113 offset:19456\n"
"v_mfma_f32_16x16x32_bf16 a[140:143],a[144:147],v[76:79],a[140:143]\n"
"v_exp_f32 v51,v51\n"
"v_mfma_f32_16x16x32_bf16 v[228:231],a[100:103],v[76:79],v[228:231]\n"
"ds_read_b128 a[80:83],v113 offset:20480\n"
"ds_read_b128 a[84:87],v113 offset:21504\n"
"v_mfma_f32_16x16x32_bf16 v[132:135],a[100:103],v[64:67],v[132:135]\n"
"v_exp_f32 v52,v52\n"
"v_mfma_f32_16x16x32_bf16 v[164:167],a[100:103],v[68:71],v[164:167]\n"
"ds_read_b128 a[88:91],v113 offset:22528\n"
"ds_read_b128 a[92:95],v113 offset:23552\n"
"v_mfma_f32_16x16x32_bf16 v[196:199],a[100:103],v[72:75],v[196:199]\n"
"v_exp_f32 v53,v53\n"
"v_mfma_f32_16x16x32_bf16 a[136:139],a[144:147],v[72:75],a[136:139]\n"
"s_waitcnt vmcnt(14)\n"
"ds_add_u32 v124,a152\n"
"v_mfma_f32_16x16x32_bf16 v[200:203],a[104:107],v[72:75],v[200:203]\n"
"v_exp_f32 v54,v54\n"
"v_mfma_f32_16x16x32_bf16 v[232:235],a[104:107],v[76:79],v[232:235]\n"
"buffer_load_dwordx4 v112,s[24:27],s64 offen offset:3072 sc1 lds\n"
"v_mfma_f32_16x16x32_bf16 v[136:139],a[104:107],v[64:67],v[136:139]\n"
"s_add_u32 m0,s67,4096\n"
"v_exp_f32 v55,v55\n"
"v_mfma_f32_16x16x32_bf16 v[168:171],a[104:107],v[68:71],v[168:171]\n"
"buffer_load_dwordx4 v117,s[24:27],s64 offen offset:0 sc1 lds\n"
"v_mfma_f32_16x16x32_bf16 a[132:135],a[144:147],v[68:71],a[132:135]\n"
"v_exp_f32 v56,v56\n"
"v_mfma_f32_16x16x32_bf16 v[172:175],a[108:111],v[68:71],v[172:175]\n"
"buffer_load_dwordx4 v117,s[24:27],s64 offen offset:1024 sc1 lds\n"
"v_mfma_f32_16x16x32_bf16 v[204:207],a[108:111],v[72:75],v[204:207]\n"
"v_exp_f32 v57,v57\n"
"v_mfma_f32_16x16x32_bf16 v[236:239],a[108:111],v[76:79],v[236:239]\n"
"buffer_load_dwordx4 v117,s[24:27],s64 offen offset:2048 sc1 lds\n"
"v_mfma_f32_16x16x32_bf16 v[140:143],a[108:111],v[64:67],v[140:143]\n"
"v_exp_f32 v58,v58\n"
"v_mfma_f32_16x16x32_bf16 a[128:131],a[144:147],v[64:67],a[128:131]\n"
"v_cvt_pk_bf16_f32 v80,v32,v33\n"
"v_cvt_pk_bf16_f32 v81,v34,v35\n"
"v_mfma_f32_16x16x32_bf16 v[144:147],a[112:115],v[64:67],v[144:147]\n"
"v_exp_f32 v59,v59\n"
"v_mfma_f32_16x16x32_bf16 v[176:179],a[112:115],v[68:71],v[176:179]\n"
"v_cvt_pk_bf16_f32 v82,v36,v37\n"
"v_cvt_pk_bf16_f32 v83,v38,v39\n"
"v_mfma_f32_16x16x32_bf16 v[208:211],a[112:115],v[72:75],v[208:211]\n"
"v_exp_f32 v60,v60\n"
"v_mfma_f32_16x16x32_bf16 v[240:243],a[112:115],v[76:79],v[240:243]\n"
"buffer_load_dwordx4 v117,s[24:27],s64 offen offset:3072 sc1 lds\n"
"v_mfma_f32_16x16x32_bf16 v[244:247],a[116:119],v[76:79],v[244:247]\n"
"s_add_u32 s64,s64,16384\n"
"v_exp_f32 v61,v61\n"
"v_mfma_f32_16x16x32_bf16 v[148:151],a[116:119],v[64:67],v[148:151]\n"
"s_andn2_b32 m0,s55,16384\n"
"s_add_u32 s54,s54,s22\n"
"v_cvt_pk_bf16_f32 v85,v42,v43\n"
"v_mfma_f32_16x16x32_bf16 v[180:183],a[116:119],v[68:71],v[180:183]\n"
"v_exp_f32 v62,v62\n"
"v_mfma_f32_16x16x32_bf16 v[212:215],a[116:119],v[72:75],v[212:215]\n"
"ds_read_b32 v125,v124\n"
"v_cvt_pk_bf16_f32 v84,v40,v41\n"
"v_mfma_f32_16x16x32_bf16 v[216:219],a[120:123],v[72:75],v[216:219]\n"
"v_exp_f32 v63,v63\n"
"v_mfma_f32_16x16x32_bf16 v[248:251],a[120:123],v[76:79],v[248:251]\n"
"buffer_load_dwordx4 v115,s[32:35],s54 offen sc1 lds\n"
"v_mfma_f32_16x16x32_bf16 v[152:155],a[120:123],v[64:67],v[152:155]\n"
"buffer_store_dwordx4 a[240:243],v112,s[24:27],s56 offen offset:0 sc1\n"
"v_mfma_f32_16x16x32_bf16 v[184:187],a[120:123],v[68:71],v[184:187]\n"
"v_cvt_pk_bf16_f32 v86,v44,v45\n"
"v_cvt_pk_bf16_f32 v87,v46,v47\n"
"v_mfma_f32_16x16x32_bf16 v[188:191],a[124:127],v[68:71],v[188:191]\n"
"s_waitcnt lgkmcnt(0)\n"
"v_cvt_pk_bf16_f32 v88,v48,v49\n"
"v_cvt_pk_bf16_f32 v89,v50,v51\n"
"v_mfma_f32_16x16x32_bf16 v[220:223],a[124:127],v[72:75],v[220:223]\n"
"v_cvt_pk_bf16_f32 v90,v52,v53\n"
"v_cvt_pk_bf16_f32 v91,v54,v55\n"
"v_mfma_f32_16x16x32_bf16 v[252:255],a[124:127],v[76:79],v[252:255]\n"
"v_cvt_pk_bf16_f32 v92,v56,v57\n"
"v_cvt_pk_bf16_f32 v93,v58,v59\n"
"v_mfma_f32_16x16x32_bf16 v[156:159],a[124:127],v[64:67],v[156:159]\n"
"v_cvt_pk_bf16_f32 v94,v60,v61\n"
"v_cvt_pk_bf16_f32 v95,v62,v63\n"
"v_max3_f32 v118,v8,v9,v10\n"
"v_max3_f32 v118,v118,v11,v12\n"
"v_max3_f32 v118,v118,v13,v14\n"
"v_max3_f32 v118,v118,v15,v20\n"
"v_max3_f32 v118,v118,v21,v22\n"
"v_max3_f32 v118,v118,v23,v16\n"
"v_max3_f32 v118,v118,v17,v18\n"
"v_max3_f32 v118,v118,v19,v24\n"
"v_max3_f32 v118,v118,v25,v26\n"
"v_max3_f32 v118,v118,v27,v28\n"
"v_max3_f32 v118,v118,v29,v30\n"
"v_max3_f32 v118,v118,v31,v4\n"
"v_max3_f32 v118,v118,v5,v6\n"
"v_max3_f32 v118,v118,v7,v0\n"
"v_max3_f32 v118,v118,v1,v2\n"
"v_max_f32 v118,v118,v3\n"
"v_cmp_lt_f32 vcc,s93,v118\n"
"s_nop 0\n"
"s_cbranch_vccnz .Lfk_cs_u3\n"
".Lfk_cr_u3:\n"
"v_cmp_gt_u32 vcc,s101,v125\n"
"s_nop 0\n"
"s_cbranch_vccnz .Lfk_cs_mu3\n"
".Lfk_cr_mu3:\n"
"v_mfma_f32_16x16x32_bf16 v[48:51],a[64:67],a[32:35],v[104:107]\n"
"ds_read_b128 a[96:99],v114 offset:57344\n"
"ds_read_b128 a[100:103],v114 offset:58368\n"
"v_mfma_f32_16x16x32_bf16 v[48:51],a[68:71],a[36:39],v[48:51]\n"
"ds_read_b128 a[104:107],v114 offset:59392\n"
"ds_read_b128 a[108:111],v114 offset:60416\n"
"v_mfma_f32_16x16x32_bf16 v[48:51],a[72:75],a[40:43],v[48:51]\n"
"ds_read_b128 a[112:115],v114 offset:61440\n"
"ds_read_b128 a[116:119],v114 offset:62464\n"
"v_mfma_f32_16x16x32_bf16 v[48:51],a[76:79],a[44:47],v[48:51]\n"
"ds_read_b128 a[120:123],v114 offset:63488\n"
"ds_read_b128 a[124:127],v114 offset:64512\n"
"v_mfma_f32_16x16x32_bf16 v[52:55],a[92:95],a[44:47],v[104:107]\n"
"ds_read_b128 a[0:3],v116 offset:40960\n"
"ds_read_b128 a[4:7],v116 offset:41984\n"
"v_mfma_f32_16x16x32_bf16 v[52:55],a[88:91],a[40:43],v[52:55]\n"
"ds_read_b128 a[8:11],v116 offset:43008\n"
"ds_read_b128 a[12:15],v116 offset:44032\n"
"v_mfma_f32_16x16x32_bf16 v[52:55],a[84:87],a[36:39],v[52:55]\n"
"v_add_f32 v40,0x80000000,v126\n"
"v_min_f32 v40,v40,v100\n"
"v_mfma_f32_16x16x32_bf16 v[52:55],a[80:83],a[32:35],v[52:55]\n"
"v_add_f32 v41,0xf149f2ca,v126\n"
"v_min_f32 v41,v41,v101\n"
"v_mfma_f32_16x16x32_bf16 v[60:63],a[80:83],a[48:51],v[108:111]\n"
"v_add_f32 v42,0xf1c9f2ca,v126\n"
"v_min_f32 v42,v42,v102\n"
"v_mfma_f32_16x16x32_bf16 v[60:63],a[84:87],a[52:55],v[60:63]\n"
"v_add_f32 v43,0xf2177617,v126\n"
"v_min_f32 v43,v43,v103\n"
"v_mfma_f32_16x16x32_bf16 v[60:63],a[88:91],a[56:59],v[60:63]\n"
"v_add_f32 v44,0xf349f2ca,v126\n"
"v_min_f32 v44,v44,v100\n"
"v_mfma_f32_16x16x32_bf16 v[60:63],a[92:95],a[60:63],v[60:63]\n"
"v_add_f32 v45,0xf35691f6,v126\n"
"v_min_f32 v45,v45,v101\n"
"v_mfma_f32_16x16x32_bf16 v[56:59],a[76:79],a[60:63],v[108:111]\n"
"v_add_f32 v46,0xf3633123,v126\n"
"v_min_f32 v46,v46,v102\n"
"v_mfma_f32_16x16x32_bf16 v[56:59],a[72:75],a[56:59],v[56:59]\n"
"v_add_f32 v47,0xf36fd050,v126\n"
"v_min_f32 v47,v47,v103\n"
"v_mfma_f32_16x16x32_bf16 v[56:59],a[68:71],a[52:55],v[56:59]\n"
"s_add_u32 s101,s101,4\n"
"v_exp_f32 v0,v0\n"
"v_mfma_f32_16x16x32_bf16 v[56:59],a[64:67],a[48:51],v[56:59]\n"
"s_add_u32 m0,s55,4096\n"
"v_exp_f32 v1,v1\n"
"v_mfma_f32_16x16x32_bf16 v[40:43],a[64:67],a[16:19],v[40:43]\n"
"s_add_u32 s54,s54,s22\n"
"v_exp_f32 v2,v2\n"
"v_mfma_f32_16x16x32_bf16 v[40:43],a[68:71],a[20:23],v[40:43]\n"
"v_exp_f32 v3,v3\n"
"v_mfma_f32_16x16x32_bf16 v[40:43],a[72:75],a[24:27],v[40:43]\n"
"v_exp_f32 v4,v4\n"
"v_mfma_f32_16x16x32_bf16 v[40:43],a[76:79],a[28:31],v[40:43]\n"
"v_exp_f32 v5,v5\n"
"v_mfma_f32_16x16x32_bf16 v[44:47],a[92:95],a[28:31],v[44:47]\n"
"v_exp_f32 v6,v6\n"
"v_mfma_f32_16x16x32_bf16 v[44:47],a[88:91],a[24:27],v[44:47]\n"
"v_exp_f32 v7,v7\n"
"v_mfma_f32_16x16x32_bf16 v[44:47],a[84:87],a[20:23],v[44:47]\n"
"v_exp_f32 v8,v8\n"
"v_mfma_f32_16x16x32_bf16 v[44:47],a[80:83],a[16:19],v[44:47]\n"
"s_waitcnt lgkmcnt(4)\n"
"v_exp_f32 v9,v9\n"
"v_mfma_f32_16x16x32_bf16 v[128:131],a[96:99],v[80:83],v[128:131]\n"
"v_exp_f32 v10,v10\n"
"v_mfma_f32_16x16x32_bf16 v[160:163],a[96:99],v[84:87],v[160:163]\n"
"v_exp_f32 v11,v11\n"
"v_mfma_f32_16x16x32_bf16 v[192:195],a[96:99],v[88:91],v[192:195]\n"
"v_exp_f32 v12,v12\n"
"v_mfma_f32_16x16x32_bf16 v[224:227],a[96:99],v[92:95],v[224:227]\n"
"v_exp_f32 v13,v13\n"
"v_mfma_f32_16x16x32_bf16 a[140:143],a[144:147],v[92:95],a[140:143]\n"
"v_exp_f32 v14,v14\n"
"v_mfma_f32_16x16x32_bf16 v[228:231],a[100:103],v[92:95],v[228:231]\n"
"v_exp_f32 v15,v15\n"
"v_mfma_f32_16x16x32_bf16 v[132:135],a[100:103],v[80:83],v[132:135]\n"
"v_exp_f32 v16,v16\n"
"v_mfma_f32_16x16x32_bf16 v[164:167],a[100:103],v[84:87],v[164:167]\n"
"v_exp_f32 v17,v17\n"
"v_mfma_f32_16x16x32_bf16 v[196:199],a[100:103],v[88:91],v[196:199]\n"
"v_exp_f32 v18,v18\n"
"v_mfma_f32_16x16x32_bf16 a[136:139],a[144:147],v[88:91],a[136:139]\n"
"v_exp_f32 v19,v19\n"
"v_mfma_f32_16x16x32_bf16 v[200:203],a[104:107],v[88:91],v[200:203]\n"
"v_exp_f32 v20,v20\n"
"v_mfma_f32_16x16x32_bf16 v[232:235],a[104:107],v[92:95],v[232:235]\n"
"v_exp_f32 v21,v21\n"
"v_mfma_f32_16x16x32_bf16 v[136:139],a[104:107],v[80:83],v[136:139]\n"
"ds_read_b128 a[64:67],v113 offset:49152\n"
"ds_read_b128 a[68:71],v113 offset:50176\n"
"v_mfma_f32_16x16x32_bf16 v[168:171],a[104:107],v[84:87],v[168:171]\n"
"v_exp_f32 v22,v22\n"
"v_mfma_f32_16x16x32_bf16 a[132:135],a[144:147],v[84:87],a[132:135]\n"
"v_exp_f32 v23,v23\n"
"v_mfma_f32_16x16x32_bf16 v[172:175],a[108:111],v[84:87],v[172:175]\n"
"ds_read_b128 a[72:75],v113 offset:51200\n"
"ds_read_b128 a[76:79],v113 offset:52224\n"
"v_mfma_f32_16x16x32_bf16 v[204:207],a[108:111],v[88:91],v[204:207]\n"
"v_exp_f32 v24,v24\n"
"v_mfma_f32_16x16x32_bf16 v[236:239],a[108:111],v[92:95],v[236:239]\n"
"ds_read_b128 a[80:83],v113 offset:53248\n"
"ds_read_b128 a[84:87],v113 offset:54272\n"
"v_mfma_f32_16x16x32_bf16 v[140:143],a[108:111],v[80:83],v[140:143]\n"
"v_exp_f32 v25,v25\n"
"v_mfma_f32_16x16x32_bf16 a[128:131],a[144:147],v[80:83],a[128:131]\n"
"v_exp_f32 v26,v26\n"
"v_mfma_f32_16x16x32_bf16 v[144:147],a[112:115],v[80:83],v[144:147]\n"
"ds_read_b128 a[88:91],v113 offset:55296\n"
"ds_read_b128 a[92:95],v113 offset:56320\n"
"v_mfma_f32_16x16x32_bf16 v[176:179],a[112:115],v[84:87],v[176:179]\n"
"v_exp_f32 v27,v27\n"
"v_mfma_f32_16x16x32_bf16 v[208:211],a[112:115],v[88:91],v[208:211]\n"
"s_waitcnt vmcnt(11)\n"
"ds_add_u32 v124,a152\n"
"v_mfma_f32_16x16x32_bf16 v[240:243],a[112:115],v[92:95],v[240:243]\n"
"v_exp_f32 v28,v28\n"
"v_mfma_f32_16x16x32_bf16 v[244:247],a[116:119],v[92:95],v[244:247]\n"
"v_exp_f32 v29,v29\n"
"v_mfma_f32_16x16x32_bf16 v[148:151],a[116:119],v[80:83],v[148:151]\n"
"buffer_load_dwordx4 v115,s[32:35],s54 offen sc1 lds\n"
"v_mfma_f32_16x16x32_bf16 v[180:183],a[116:119],v[84:87],v[180:183]\n"
"s_add_u32 m0,s67,16384\n"
"v_exp_f32 v30,v30\n"
"v_mfma_f32_16x16x32_bf16 v[212:215],a[116:119],v[88:91],v[212:215]\n"
"ds_read_b32 v125,v124\n"
"v_cvt_pk_bf16_f32 v64,v0,v1\n"
"v_mfma_f32_16x16x32_bf16 v[216:219],a[120:123],v[88:91],v[216:219]\n"
"v_exp_f32 v31,v31\n"
"v_mfma_f32_16x16x32_bf16 v[248:251],a[120:123],v[92:95],v[248:251]\n"
"buffer_load_dwordx4 v112,s[24:27],s64 offen offset:0 sc1 lds\n"
"v_mfma_f32_16x16x32_bf16 v[152:155],a[120:123],v[80:83],v[152:155]\n"
"buffer_load_dwordx4 v112,s[24:27],s64 offen offset:1024 sc1 lds\n"
"v_mfma_f32_16x16x32_bf16 v[184:187],a[120:123],v[84:87],v[184:187]\n"
"buffer_load_dwordx4 v112,s[24:27],s64 offen offset:2048 sc1 lds\n"
"v_mfma_f32_16x16x32_bf16 v[188:191],a[124:127],v[84:87],v[188:191]\n"
"s_waitcnt lgkmcnt(0)\n"
"buffer_load_dwordx4 v112,s[24:27],s64 offen offset:3072 sc1 lds\n"
"v_mfma_f32_16x16x32_bf16 v[220:223],a[124:127],v[88:91],v[220:223]\n"
"s_add_u32 m0,s67,20480\n"
"v_cvt_pk_bf16_f32 v65,v2,v3\n"
"v_cvt_pk_bf16_f32 v66,v4,v5\n"
"v_mfma_f32_16x16x32_bf16 v[252:255],a[124:127],v[92:95],v[252:255]\n"
"buffer_load_dwordx4 v117,s[24:27],s64 offen offset:0 sc1 lds\n"
"v_mfma_f32_16x16x32_bf16 v[156:159],a[124:127],v[80:83],v[156:159]\n"
"buffer_load_dwordx4 v117,s[24:27],s64 offen offset:1024 sc1 lds\n"
"buffer_load_dwordx4 v117,s[24:27],s64 offen offset:2048 sc1 lds\n"
"buffer_load_dwordx4 v117,s[24:27],s64 offen offset:3072 sc1 lds\n"
"buffer_store_dwordx4 a[244:247],v112,s[24:27],s56 offen offset:1024 sc1\n"
"s_add_u32 s64,s64,16384\n"
"v_cvt_pk_bf16_f32 v67,v6,v7\n"
"v_cvt_pk_bf16_f32 v68,v8,v9\n"
"v_cvt_pk_bf16_f32 v69,v10,v11\n"
"v_cvt_pk_bf16_f32 v70,v12,v13\n"
"v_cvt_pk_bf16_f32 v71,v14,v15\n"
"v_cvt_pk_bf16_f32 v72,v16,v17\n"
"v_cvt_pk_bf16_f32 v73,v18,v19\n"
"v_cvt_pk_bf16_f32 v74,v20,v21\n"
"v_cvt_pk_bf16_f32 v75,v22,v23\n"
"v_cvt_pk_bf16_f32 v76,v24,v25\n"
"v_cvt_pk_bf16_f32 v77,v26,v27\n"
"v_cvt_pk_bf16_f32 v78,v28,v29\n"
"v_cvt_pk_bf16_f32 v79,v30,v31\n"
"v_max3_f32 v118,v48,v49,v50\n"
"v_max3_f32 v118,v118,v51,v52\n"
"v_max3_f32 v118,v118,v53,v54\n"
"v_max3_f32 v118,v118,v55,v60\n"
"v_max3_f32 v118,v118,v61,v62\n"
"v_max3_f32 v118,v118,v63,v56\n"
"v_max3_f32 v118,v118,v57,v58\n"
"v_max3_f32 v118,v118,v59,v40\n"
"v_max3_f32 v118,v118,v41,v42\n"
"v_max3_f32 v118,v118,v43,v44\n"
"v_max3_f32 v118,v118,v45,v46\n"
"v_max_f32 v118,v118,v47\n"
"v_cmp_lt_f32 vcc,s93,v118\n"
"s_nop 0\n"
"s_cbranch_vccnz .Lfk_cs_u0\n"
".Lfk_cr_u0:\n"
"v_cmp_gt_u32 vcc,s101,v125\n"
"s_nop 0\n"
"s_cbranch_vccnz .Lfk_cs_mu0\n"
".Lfk_cr_mu0:\n"
"v_mfma_f32_16x16x32_bf16 v[24:27],a[64:67],a[48:51],v[108:111]\n"
"ds_read_b128 a[96:99],v114 offset:40960\n"
"ds_read_b128 a[100:103],v114 offset:41984\n"
"v_mfma_f32_16x16x32_bf16 v[24:27],a[68:71],a[52:55],v[24:27]\n"
"ds_read_b128 a[104:107],v114 offset:43008\n"
"ds_read_b128 a[108:111],v114 offset:44032\n"
"v_mfma_f32_16x16x32_bf16 v[24:27],a[72:75],a[56:59],v[24:27]\n"
"ds_read_b128 a[112:115],v114 offset:45056\n"
"ds_read_b128 a[116:119],v114 offset:46080\n"
"v_mfma_f32_16x16x32_bf16 v[24:27],a[76:79],a[60:63],v[24:27]\n"
"ds_read_b128 a[120:123],v114 offset:47104\n"
"ds_read_b128 a[124:127],v114 offset:48128\n"
"v_mfma_f32_16x16x32_bf16 v[28:31],a[92:95],a[60:63],v[108:111]\n"
"ds_read_b128 a[16:19],v116 offset:8192\n"
"ds_read_b128 a[20:23],v116 offset:9216\n"
"v_mfma_f32_16x16x32_bf16 v[28:31],a[88:91],a[56:59],v[28:31]\n"
"ds_read_b128 a[24:27],v116 offset:10240\n"
"ds_read_b128 a[28:31],v116 offset:11264\n"
"v_mfma_f32_16x16x32_bf16 v[28:31],a[84:87],a[52:55],v[28:31]\n"
"v_add_f32 v16,0x80000000,v126\n"
"v_min_f32 v16,v16,v104\n"
"v_mfma_f32_16x16x32_bf16 v[28:31],a[80:83],a[48:51],v[28:31]\n"
"v_add_f32 v17,0xf149f2ca,v126\n"
"v_add_f32 v18,0xf1c9f2ca,v126\n"
"v_add_f32 v19,0xf2177617,v126\n"
"v_min_f32 v19,v19,v107\n"
"v_add_f32 v20,0xf349f2ca,v126\n"
"v_min_f32 v20,v20,v104\n"
"v_add_f32 v21,0xf35691f6,v126\n"
"v_min_f32 v21,v21,v105\n"
"v_add_f32 v22,0xf3633123,v126\n"
"v_min_f32 v22,v22,v106\n"
"v_add_f32 v23,0xf36fd050,v126\n"
"v_min_f32 v23,v23,v107\n"
"v_min_f32 v18,v18,v106\n"
"v_min_f32 v17,v17,v105\n"
"v_mfma_f32_16x16x32_bf16 v[20:23],a[80:83],a[32:35],v[20:23]\n"
"s_add_u32 s101,s101,4\n"
"v_exp_f32 v40,v40\n"
"v_mfma_f32_16x16x32_bf16 v[20:23],a[84:87],a[36:39],v[20:23]\n"
"s_add_u32 m0,s55,8192\n"
"v_exp_f32 v41,v41\n"
"v_mfma_f32_16x16x32_bf16 v[20:23],a[88:91],a[40:43],v[20:23]\n"
"s_add_u32 s54,s54,s22\n"
"v_exp_f32 v42,v42\n"
"v_mfma_f32_16x16x32_bf16 v[20:23],a[92:95],a[44:47],v[20:23]\n"
"v_exp_f32 v43,v43\n"
"v_mfma_f32_16x16x32_bf16 v[16:19],a[76:79],a[44:47],v[16:19]\n"
"v_exp_f32 v44,v44\n"
"v_mfma_f32_16x16x32_bf16 v[16:19],a[72:75],a[40:43],v[16:19]\n"
"v_exp_f32 v45,v45\n"
"v_mfma_f32_16x16x32_bf16 v[16:19],a[68:71],a[36:39],v[16:19]\n"
"v_exp_f32 v46,v46\n"
"v_mfma_f32_16x16x32_bf16 v[16:19],a[64:67],a[32:35],v[16:19]\n"
"s_waitcnt lgkmcnt(4)\n"
"v_exp_f32 v47,v47\n"
"v_mfma_f32_16x16x32_bf16 v[128:131],a[96:99],v[64:67],v[128:131]\n"
"v_exp_f32 v48,v48\n"
"v_mfma_f32_16x16x32_bf16 v[160:163],a[96:99],v[68:71],v[160:163]\n"
"v_exp_f32 v49,v49\n"
"v_mfma_f32_16x16x32_bf16 v[192:195],a[96:99],v[72:75],v[192:195]\n"
"ds_read_b128 a[64:67],v113 offset:32768\n"
"ds_read_b128 a[68:71],v113 offset:33792\n"
"v_mfma_f32_16x16x32_bf16 v[224:227],a[96:99],v[76:79],v[224:227]\n"
"v_exp_f32 v50,v50\n"
"v_mfma_f32_16x16x32_bf16 a[140:143],a[144:147],v[76:79],a[140:143]\n"
"ds_read_b128 a[72:75],v113 offset:34816\n"
"ds_read_b128 a[76:79],v113 offset:35840\n"
"v_mfma_f32_16x16x32_bf16 v[228:231],a[100:103],v[76:79],v[228:231]\n"
"v_exp_f32 v51,v51\n"
"v_mfma_f32_16x16x32_bf16 v[132:135],a[100:103],v[64:67],v[132:135]\n"
"ds_read_b128 a[80:83],v113 offset:36864\n"
"ds_read_b128 a[84:87],v113 offset:37888\n"
"v_mfma_f32_16x16x32_bf16 v[164:167],a[100:103],v[68:71],v[164:167]\n"
"v_exp_f32 v52,v52\n"
"v_mfma_f32_16x16x32_bf16 v[196:199],a[100:103],v[72:75],v[196:199]\n"
"v_exp_f32 v53,v53\n"
"v_mfma_f32_16x16x32_bf16 a[136:139],a[144:147],v[72:75],a[136:139]\n"
"ds_read_b128 a[88:91],v113 offset:38912\n"
"ds_read_b128 a[92:95],v113 offset:39936\n"
"v_mfma_f32_16x16x32_bf16 v[200:203],a[104:107],v[72:75],v[200:203]\n"
"v_exp_f32 v54,v54\n"
"v_mfma_f32_16x16x32_bf16 v[232:235],a[104:107],v[76:79],v[232:235]\n"
"s_waitcnt vmcnt(11)\n"
"ds_add_u32 v124,a152\n"
"v_mfma_f32_16x16x32_bf16 v[136:139],a[104:107],v[64:67],v[136:139]\n"
"v_exp_f32 v55,v55\n"
"v_mfma_f32_16x16x32_bf16 v[168:171],a[104:107],v[68:71],v[168:171]\n"
"buffer_load_dwordx4 v115,s[32:35],s54 offen sc1 lds\n"
"v_mfma_f32_16x16x32_bf16 a[132:135],a[144:147],v[68:71],a[132:135]\n"
"s_add_u32 m0,s67,49152\n"
"v_exp_f32 v56,v56\n"
"v_mfma_f32_16x16x32_bf16 v[172:175],a[108:111],v[68:71],v[172:175]\n"
"buffer_load_dwordx4 v112,s[24:27],s64 offen offset:0 sc1 lds\n"
"v_mfma_f32_16x16x32_bf16 v[204:207],a[108:111],v[72:75],v[204:207]\n"
"v_exp_f32 v57,v57\n"
"v_mfma_f32_16x16x32_bf16 v[236:239],a[108:111],v[76:79],v[236:239]\n"
"buffer_load_dwordx4 v112,s[24:27],s64 offen offset:1024 sc1 lds\n"
"v_mfma_f32_16x16x32_bf16 v[140:143],a[108:111],v[64:67],v[140:143]\n"
"v_exp_f32 v58,v58\n"
"v_mfma_f32_16x16x32_bf16 a[128:131],a[144:147],v[64:67],a[128:131]\n"
"buffer_load_dwordx4 v112,s[24:27],s64 offen offset:2048 sc1 lds\n"
"v_mfma_f32_16x16x32_bf16 v[144:147],a[112:115],v[64:67],v[144:147]\n"
"v_exp_f32 v59,v59\n"
"v_mfma_f32_16x16x32_bf16 v[176:179],a[112:115],v[68:71],v[176:179]\n"
"buffer_load_dwordx4 v112,s[24:27],s64 offen offset:3072 sc1 lds\n"
"v_mfma_f32_16x16x32_bf16 v[208:211],a[112:115],v[72:75],v[208:211]\n"
"s_add_u32 m0,s67,53248\n"
"v_exp_f32 v60,v60\n"
"v_mfma_f32_16x16x32_bf16 v[240:243],a[112:115],v[76:79],v[240:243]\n"
"buffer_load_dwordx4 v117,s[24:27],s64 offen offset:0 sc1 lds\n"
"v_mfma_f32_16x16x32_bf16 v[244:247],a[116:119],v[76:79],v[244:247]\n"
"v_exp_f32 v61,v61\n"
"v_mfma_f32_16x16x32_bf16 v[148:151],a[116:119],v[64:67],v[148:151]\n"
"buffer_load_dwordx4 v117,s[24:27],s64 offen offset:1024 sc1 lds\n"
"v_mfma_f32_16x16x32_bf16 v[180:183],a[116:119],v[68:71],v[180:183]\n"
"v_exp_f32 v62,v62\n"
"v_mfma_f32_16x16x32_bf16 v[212:215],a[116:119],v[72:75],v[212:215]\n"
"ds_read_b32 v125,v124\n"
"v_cvt_pk_bf16_f32 v84,v40,v41\n"
"v_mfma_f32_16x16x32_bf16 v[216:219],a[120:123],v[72:75],v[216:219]\n"
"v_exp_f32 v63,v63\n"
"v_mfma_f32_16x16x32_bf16 v[248:251],a[120:123],v[76:79],v[248:251]\n"
"buffer_load_dwordx4 v117,s[24:27],s64 offen offset:2048 sc1 lds\n"
"v_mfma_f32_16x16x32_bf16 v[152:155],a[120:123],v[64:67],v[152:155]\n"
"buffer_load_dwordx4 v117,s[24:27],s64 offen offset:3072 sc1 lds\n"
"v_mfma_f32_16x16x32_bf16 v[184:187],a[120:123],v[68:71],v[184:187]\n"
"s_add_u32 s64,s64,16384\n"
"buffer_store_dwordx4 a[248:251],v112,s[24:27],s56 offen offset:2048 sc1\n"
"v_mfma_f32_16x16x32_bf16 v[188:191],a[124:127],v[68:71],v[188:191]\n"
"s_waitcnt lgkmcnt(0)\n"
"s_and_b32 s67,s67,8192\n"
"v_cvt_pk_bf16_f32 v85,v42,v43\n"
"v_mfma_f32_16x16x32_bf16 v[220:223],a[124:127],v[72:75],v[220:223]\n"
"v_cvt_pk_bf16_f32 v86,v44,v45\n"
"v_cvt_pk_bf16_f32 v87,v46,v47\n"
"v_mfma_f32_16x16x32_bf16 v[252:255],a[124:127],v[76:79],v[252:255]\n"
"v_cvt_pk_bf16_f32 v88,v48,v49\n"
"v_cvt_pk_bf16_f32 v89,v50,v51\n"
"v_mfma_f32_16x16x32_bf16 v[156:159],a[124:127],v[64:67],v[156:159]\n"
"v_cvt_pk_bf16_f32 v90,v52,v53\n"
"v_cvt_pk_bf16_f32 v91,v54,v55\n"
"v_cvt_pk_bf16_f32 v92,v56,v57\n"
"v_cvt_pk_bf16_f32 v93,v58,v59\n"
"v_cvt_pk_bf16_f32 v94,v60,v61\n"
"v_cvt_pk_bf16_f32 v95,v62,v63\n"
"v_max3_f32 v118,v24,v25,v26\n"
"v_max3_f32 v118,v118,v27,v28\n"
"v_max3_f32 v118,v118,v29,v30\n"
"v_max3_f32 v118,v118,v31,v20\n"
"v_max3_f32 v118,v118,v21,v22\n"
"v_max3_f32 v118,v118,v23,v16\n"
"v_max3_f32 v118,v118,v17,v18\n"
"v_max_f32 v118,v118,v19\n"
"v_cmp_lt_f32 vcc,s93,v118\n"
"s_nop 0\n"
"s_cbranch_vccnz .Lfk_cs_u1\n"
".Lfk_cr_u1:\n"
"v_cmp_gt_u32 vcc,s101,v125\n"
"s_nop 0\n"
"s_cbranch_vccnz .Lfk_cs_mu1\n"
".Lfk_cr_mu1:\n"
"v_mov_b32 v113,v112\n"
"v_add_f32 v56,0x80000000,v126\n"
"v_min_f32 v56,v56,v108\n"
"v_add_f32 v57,0xf149f2ca,v126\n"
"v_min_f32 v57,v57,v109\n"
"v_add_f32 v58,0xf1c9f2ca,v126\n"
"v_min_f32 v58,v58,v110\n"
"v_add_f32 v59,0xf2177617,v126\n"
"v_min_f32 v59,v59,v111\n"
"v_add_f32 v60,0xf349f2ca,v126\n"
"v_min_f32 v60,v60,v108\n"
"v_add_f32 v61,0xf35691f6,v126\n"
"v_min_f32 v61,v61,v109\n"
"v_add_f32 v62,0xf3633123,v126\n"
"v_min_f32 v62,v62,v110\n"
"v_add_f32 v63,0xf36fd050,v126\n"
"v_min_f32 v63,v63,v111\n"
"v_exp_f32 v16,v16\n"
"v_mfma_f32_16x16x32_bf16 v[56:59],a[64:67],a[48:51],v[56:59]\n"
"s_add_u32 s101,s101,4\n"
"ds_read_b128 a[96:99],v114 offset:8192\n"
"ds_read_b128 a[100:103],v114 offset:9216\n"
"ds_read_b128 a[104:107],v114 offset:10240\n"
"v_exp_f32 v17,v17\n"
"v_mfma_f32_16x16x32_bf16 v[56:59],a[68:71],a[52:55],v[56:59]\n"
"s_add_u32 m0,s55,12288\n"
"ds_read_b128 a[108:111],v114 offset:11264\n"
"ds_read_b128 a[112:115],v114 offset:12288\n"
"ds_read_b128 a[116:119],v114 offset:13312\n"
"v_mfma_f32_16x16x32_bf16 v[56:59],a[72:75],a[56:59],v[56:59]\n"
"ds_read_b128 a[120:123],v114 offset:14336\n"
"ds_read_b128 a[124:127],v114 offset:15360\n"
"ds_read_b128 a[32:35],v116 offset:24576\n"
"ds_read_b128 a[36:39],v116 offset:25600\n"
"ds_read_b128 a[40:43],v116 offset:26624\n"
"v_mfma_f32_16x16x32_bf16 v[56:59],a[76:79],a[60:63],v[56:59]\n"
"s_add_u32 s54,s54,s22\n"
"ds_read_b128 a[44:47],v116 offset:27648\n"
"v_exp_f32 v18,v18\n"
"v_mfma_f32_16x16x32_bf16 v[60:63],a[92:95],a[60:63],v[60:63]\n"
"buffer_load_dwordx4 v115,s[32:35],s54 offen sc1 lds\n"
"v_cvt_pk_bf16_f32 v72,v16,v17\n"
"v_mfma_f32_16x16x32_bf16 v[60:63],a[88:91],a[56:59],v[60:63]\n"
"s_add_u32 m0,s67,32768\n"
"v_exp_f32 v19,v19\n"
"v_mfma_f32_16x16x32_bf16 v[60:63],a[84:87],a[52:55],v[60:63]\n"
"buffer_load_dwordx4 v112,s[24:27],s64 offen offset:0 sc1 lds\n"
"v_cvt_pk_bf16_f32 v73,v18,v19\n"
"v_exp_f32 v20,v20\n"
"v_mfma_f32_16x16x32_bf16 v[60:63],a[80:83],a[48:51],v[60:63]\n"
"ds_read_b128 a[64:67],v113 offset:0\n"
"ds_read_b128 a[68:71],v113 offset:1024\n"
"ds_read_b128 a[72:75],v113 offset:2048\n"
"s_waitcnt lgkmcnt(7)\n"
"v_mfma_f32_16x16x32_bf16 v[160:163],a[96:99],v[84:87],v[160:163]\n"
"ds_read_b128 a[76:79],v113 offset:3072\n"
"ds_read_b128 a[80:83],v113 offset:4096\n"
"ds_read_b128 a[84:87],v113 offset:5120\n"
"ds_read_b128 a[88:91],v113 offset:6144\n"
"ds_read_b128 a[92:95],v113 offset:7168\n"
"v_exp_f32 v21,v21\n"
"v_mfma_f32_16x16x32_bf16 a[132:135],a[144:147],v[84:87],a[132:135]\n"
"buffer_load_dwordx4 v112,s[24:27],s64 offen offset:1024 sc1 lds\n"
"v_max3_f32 v118,v56,v57,v58\n"
"v_mfma_f32_16x16x32_bf16 v[164:167],a[100:103],v[84:87],v[164:167]\n"
"v_exp_f32 v22,v22\n"
"v_cvt_pk_bf16_f32 v74,v20,v21\n"
"ds_read_b128 a[48:51],v116 offset:57344\n"
"ds_read_b128 a[52:55],v116 offset:58368\n"
"v_mfma_f32_16x16x32_bf16 v[168:171],a[104:107],v[84:87],v[168:171]\n"
"v_max3_f32 v118,v118,v59,v60\n"
"v_max3_f32 v118,v118,v61,v62\n"
"v_max_f32 v118,v118,v63\n"
"v_exp_f32 v23,v23\n"
"v_mfma_f32_16x16x32_bf16 v[172:175],a[108:111],v[84:87],v[172:175]\n"
"ds_read_b128 a[56:59],v116 offset:59392\n"
"buffer_load_dwordx4 v112,s[24:27],s64 offen offset:2048 sc1 lds\n"
"ds_read_b128 a[60:63],v116 offset:60416\n"
"v_mfma_f32_16x16x32_bf16 v[176:179],a[112:115],v[84:87],v[176:179]\n"
"s_waitcnt vmcnt(15)\n"
"v_cvt_pk_bf16_f32 v75,v22,v23\n"
"v_accvgpr_read_b32 v122,a128\n"
"v_rcp_f32 v122,v122\n"
"v_mfma_f32_16x16x32_bf16 v[180:183],a[116:119],v[84:87],v[180:183]\n"
"ds_add_u32 v124,a152\n"
"v_cmp_lt_f32 vcc,s93,v118\n"
"v_exp_f32 v24,v24\n"
"v_pk_mul_f32 v[128:129],v[128:129],v[122:123] op_sel_hi:[1,0]\n"
"v_pk_mul_f32 v[130:131],v[130:131],v[122:123] op_sel_hi:[1,0]\n"
"v_pk_mul_f32 v[132:133],v[132:133],v[122:123] op_sel_hi:[1,0]\n"
"v_pk_mul_f32 v[134:135],v[134:135],v[122:123] op_sel_hi:[1,0]\n"
"v_mfma_f32_16x16x32_bf16 v[184:187],a[120:123],v[84:87],v[184:187]\n"
"buffer_load_dwordx4 v112,s[24:27],s64 offen offset:3072 sc1 lds\n"
"v_cvt_pk_bf16_f32 v128,v128,v129\n"
"v_cvt_pk_bf16_f32 v129,v130,v131\n"
"v_mfma_f32_16x16x32_bf16 v[188:191],a[124:127],v[84:87],v[188:191]\n"
"s_add_u32 m0,s67,36864\n"
"v_cvt_pk_bf16_f32 v130,v132,v133\n"
"v_cvt_pk_bf16_f32 v131,v134,v135\n"
"v_exp_f32 v25,v25\n"
"v_mfma_f32_16x16x32_bf16 v[192:195],a[96:99],v[88:91],v[192:195]\n"
"buffer_store_dwordx4 v[128:131],v112,s[24:27],s70 offen offset:0 sc1\n"
"v_cvt_pk_bf16_f32 v76,v24,v25\n"
"v_pk_mul_f32 v[136:137],v[136:137],v[122:123] op_sel_hi:[1,0]\n"
"v_pk_mul_f32 v[138:139],v[138:139],v[122:123] op_sel_hi:[1,0]\n"
"v_pk_mul_f32 v[140:141],v[140:141],v[122:123] op_sel_hi:[1,0]\n"
"v_pk_mul_f32 v[142:143],v[142:143],v[122:123] op_sel_hi:[1,0]\n"
"v_exp_f32 v26,v26\n"
"v_mfma_f32_16x16x32_bf16 a[136:139],a[144:147],v[88:91],a[136:139]\n"
"buffer_load_dwordx4 v117,s[24:27],s64 offen offset:0 sc1 lds\n"
"v_cvt_pk_bf16_f32 v136,v136,v137\n"
"v_mfma_f32_16x16x32_bf16 v[196:199],a[100:103],v[88:91],v[196:199]\n"
"v_exp_f32 v27,v27\n"
"v_cvt_pk_bf16_f32 v137,v138,v139\n"
"v_cvt_pk_bf16_f32 v138,v140,v141\n"
"v_cvt_pk_bf16_f32 v139,v142,v143\n"
"v_cvt_pk_bf16_f32 v77,v26,v27\n"
"v_mfma_f32_16x16x32_bf16 v[200:203],a[104:107],v[88:91],v[200:203]\n"
"buffer_store_dwordx4 v[136:139],v112,s[24:27],s70 offen offset:1024 sc1\n"
"v_pk_mul_f32 v[144:145],v[144:145],v[122:123] op_sel_hi:[1,0]\n"
"v_pk_mul_f32 v[146:147],v[146:147],v[122:123] op_sel_hi:[1,0]\n"
"v_pk_mul_f32 v[148:149],v[148:149],v[122:123] op_sel_hi:[1,0]\n"
"v_pk_mul_f32 v[150:151],v[150:151],v[122:123] op_sel_hi:[1,0]\n"
"v_mfma_f32_16x16x32_bf16 v[204:207],a[108:111],v[88:91],v[204:207]\n"
"buffer_load_dwordx4 v117,s[24:27],s64 offen offset:1024 sc1 lds\n"
"v_exp_f32 v28,v28\n"
"v_mfma_f32_16x16x32_bf16 v[208:211],a[112:115],v[88:91],v[208:211]\n"
"v_cvt_pk_bf16_f32 v144,v144,v145\n"
"v_cvt_pk_bf16_f32 v145,v146,v147\n"
"v_cvt_pk_bf16_f32 v146,v148,v149\n"
"v_cvt_pk_bf16_f32 v147,v150,v151\n"
"v_mfma_f32_16x16x32_bf16 v[212:215],a[116:119],v[88:91],v[212:215]\n"
"buffer_store_dwordx4 v[144:147],v112,s[24:27],s70 offen offset:2048 sc1\n"
"v_pk_mul_f32 v[152:153],v[152:153],v[122:123] op_sel_hi:[1,0]\n"
"v_pk_mul_f32 v[154:155],v[154:155],v[122:123] op_sel_hi:[1,0]\n"
"v_pk_mul_f32 v[156:157],v[156:157],v[122:123] op_sel_hi:[1,0]\n"
"v_mfma_f32_16x16x32_bf16 v[216:219],a[120:123],v[88:91],v[216:219]\n"
"buffer_load_dwordx4 v117,s[24:27],s64 offen offset:2048 sc1 lds\n"
"v_exp_f32 v29,v29\n"
"v_cvt_pk_bf16_f32 v152,v152,v153\n"
"v_cvt_pk_bf16_f32 v78,v28,v29\n"
"v_pk_mul_f32 v[158:159],v[158:159],v[122:123] op_sel_hi:[1,0]\n"
"v_mfma_f32_16x16x32_bf16 v[220:223],a[124:127],v[88:91],v[220:223]\n"
"v_cvt_pk_bf16_f32 v153,v154,v155\n"
"v_cvt_pk_bf16_f32 v154,v156,v157\n"
"v_cvt_pk_bf16_f32 v155,v158,v159\n"
"v_mfma_f32_16x16x32_bf16 v[224:227],a[96:99],v[92:95],v[224:227]\n"
"ds_read_b32 v125,v124\n"
"buffer_store_dwordx4 v[152:155],v112,s[24:27],s70 offen offset:3072 sc1\n"
"buffer_load_dwordx4 v117,s[24:27],s64 offen offset:3072 sc1 lds\n"
"v_accvgpr_read_b32 v122,a132\n"
"v_mfma_f32_16x16x32_bf16 a[140:143],a[144:147],v[92:95],a[140:143]\n"
"s_add_u32 s70,s70,262144\n"
"v_exp_f32 v31,v31\n"
"v_mfma_f32_16x16x32_bf16 v[228:231],a[100:103],v[92:95],v[228:231]\n"
"s_add_u32 s64,s64,16384\n"
"v_exp_f32 v30,v30\n"
"v_rcp_f32 v122,v122\n"
"v_cvt_pk_bf16_f32 v79,v30,v31\n"
"v_pk_mul_f32 v[160:161],v[160:161],v[122:123] op_sel_hi:[1,0]\n"
"v_pk_mul_f32 v[162:163],v[162:163],v[122:123] op_sel_hi:[1,0]\n"
"v_pk_mul_f32 v[164:165],v[164:165],v[122:123] op_sel_hi:[1,0]\n"
"v_pk_mul_f32 v[166:167],v[166:167],v[122:123] op_sel_hi:[1,0]\n"
"v_mfma_f32_16x16x32_bf16 v[232:235],a[104:107],v[92:95],v[232:235]\n"
"buffer_store_dwordx4 a[252:255],v112,s[24:27],s56 offen offset:3072 sc1\n"
"v_cvt_pk_bf16_f32 v160,v160,v161\n"
"v_cvt_pk_bf16_f32 v161,v162,v163\n"
"v_cvt_pk_bf16_f32 v162,v164,v165\n"
"v_cvt_pk_bf16_f32 v163,v166,v167\n"
"v_mfma_f32_16x16x32_bf16 v[236:239],a[108:111],v[92:95],v[236:239]\n"
"buffer_store_dwordx4 v[160:163],v112,s[24:27],s70 offen offset:0 sc1\n"
"v_pk_mul_f32 v[168:169],v[168:169],v[122:123] op_sel_hi:[1,0]\n"
"v_pk_mul_f32 v[170:171],v[170:171],v[122:123] op_sel_hi:[1,0]\n"
"v_pk_mul_f32 v[172:173],v[172:173],v[122:123] op_sel_hi:[1,0]\n"
"v_pk_mul_f32 v[174:175],v[174:175],v[122:123] op_sel_hi:[1,0]\n"
"v_mfma_f32_16x16x32_bf16 v[240:243],a[112:115],v[92:95],v[240:243]\n"
"s_waitcnt lgkmcnt(0)\n"
"v_cvt_pk_bf16_f32 v168,v168,v169\n"
"v_cvt_pk_bf16_f32 v169,v170,v171\n"
"v_cvt_pk_bf16_f32 v170,v172,v173\n"
"v_cvt_pk_bf16_f32 v171,v174,v175\n"
"v_mfma_f32_16x16x32_bf16 v[244:247],a[116:119],v[92:95],v[244:247]\n"
"buffer_store_dwordx4 v[168:171],v112,s[24:27],s70 offen offset:1024 sc1\n"
"v_pk_mul_f32 v[176:177],v[176:177],v[122:123] op_sel_hi:[1,0]\n"
"v_pk_mul_f32 v[178:179],v[178:179],v[122:123] op_sel_hi:[1,0]\n"
"v_pk_mul_f32 v[180:181],v[180:181],v[122:123] op_sel_hi:[1,0]\n"
"v_pk_mul_f32 v[182:183],v[182:183],v[122:123] op_sel_hi:[1,0]\n"
"v_mfma_f32_16x16x32_bf16 v[248:251],a[120:123],v[92:95],v[248:251]\n"
"v_cvt_pk_bf16_f32 v176,v176,v177\n"
"v_cvt_pk_bf16_f32 v177,v178,v179\n"
"v_cvt_pk_bf16_f32 v178,v180,v181\n"
"v_cvt_pk_bf16_f32 v179,v182,v183\n"
"v_mfma_f32_16x16x32_bf16 v[252:255],a[124:127],v[92:95],v[252:255]\n"
"buffer_store_dwordx4 v[176:179],v112,s[24:27],s70 offen offset:2048 sc1\n"
"v_pk_mul_f32 v[184:185],v[184:185],v[122:123] op_sel_hi:[1,0]\n"
"v_pk_mul_f32 v[186:187],v[186:187],v[122:123] op_sel_hi:[1,0]\n"
"v_pk_mul_f32 v[188:189],v[188:189],v[122:123] op_sel_hi:[1,0]\n"
"v_pk_mul_f32 v[190:191],v[190:191],v[122:123] op_sel_hi:[1,0]\n"
"v_cvt_pk_bf16_f32 v184,v184,v185\n"
"v_cvt_pk_bf16_f32 v185,v186,v187\n"
"v_cvt_pk_bf16_f32 v186,v188,v189\n"
"v_cvt_pk_bf16_f32 v187,v190,v191\n"
"buffer_store_dwordx4 v[184:187],v112,s[24:27],s70 offen offset:3072 sc1\n"
"s_add_u32 s70,s70,262144\n"
"s_cbranch_vccnz .Lfk_cs_u2\n"
".Lfk_cr_u2:\n"
"v_cmp_gt_u32 vcc,s101,v125\n"
"s_nop 0\n"
"s_cbranch_vccnz .Lfk_cs_mu2\n"
".Lfk_cr_mu2:\n"
"s_add_u32 s89,s89,1\n"
"s_cmp_eq_u32 s89,8\n"
"s_cbranch_scc1 .Lfk_tailB7\n"
".Lfk_winB:\n"
"v_mfma_f32_16x16x32_bf16 v[0:3],a[64:67],a[0:3],v[96:99]\n"
"s_swappc_b64 s[94:95],s[20:21]\n"
"ds_read_b128 a[96:99],v114 offset:24576\n"
"v_mfma_f32_16x16x32_bf16 v[0:3],a[68:71],a[4:7],v[0:3]\n"
"s_add_u32 s101,s101,4\n"
"ds_read_b128 a[100:103],v114 offset:25600\n"
"ds_read_b128 a[104:107],v114 offset:26624\n"
"ds_read_b128 a[108:111],v114 offset:27648\n"
"v_mfma_f32_16x16x32_bf16 v[0:3],a[72:75],a[8:11],v[0:3]\n"
"ds_read_b128 a[112:115],v114 offset:28672\n"
"ds_read_b128 a[116:119],v114 offset:29696\n"
"v_mfma_f32_16x16x32_bf16 v[0:3],a[76:79],a[12:15],v[0:3]\n"
"ds_read_b128 a[120:123],v114 offset:30720\n"
"ds_read_b128 a[124:127],v114 offset:31744\n"
"v_mfma_f32_16x16x32_bf16 v[4:7],a[92:95],a[12:15],v[96:99]\n"
"v_exp_f32 v56,v56\n"
"v_mfma_f32_16x16x32_bf16 v[4:7],a[88:91],a[8:11],v[4:7]\n"
"v_exp_f32 v57,v57\n"
"v_mfma_f32_16x16x32_bf16 v[4:7],a[84:87],a[4:7],v[4:7]\n"
"v_cvt_pk_bf16_f32 v92,v56,v57\n"
"v_mfma_f32_16x16x32_bf16 v[4:7],a[80:83],a[0:3],v[4:7]\n"
"s_add_u32 m0,s67,0\n"
"s_waitcnt lgkmcnt(0)\n"
"v_mfma_f32_16x16x32_bf16 v[192:195],a[96:99],v[72:75],v[192:195]\n"
"buffer_load_dwordx4 v112,s[24:27],s64 offen offset:0 sc1 lds\n"
"v_mfma_f32_16x16x32_bf16 a[136:139],a[144:147],v[72:75],a[136:139]\n"
"buffer_load_dwordx4 v112,s[24:27],s64 offen offset:1024 sc1 lds\n"
"v_mfma_f32_16x16x32_bf16 v[196:199],a[100:103],v[72:75],v[196:199]\n"
"v_exp_f32 v58,v58\n"
"v_mfma_f32_16x16x32_bf16 v[200:203],a[104:107],v[72:75],v[200:203]\n"
"v_max3_f32 v118,v0,v1,v2\n"
"v_mfma_f32_16x16x32_bf16 v[204:207],a[108:111],v[72:75],v[204:207]\n"
"v_max3_f32 v118,v118,v3,v4\n"
"v_max3_f32 v118,v118,v5,v6\n"
"v_mfma_f32_16x16x32_bf16 v[208:211],a[112:115],v[72:75],v[208:211]\n"
"v_max_f32 v118,v118,v7\n"
"v_mov_b32 v120,v118\n"
"v_mfma_f32_16x16x32_bf16 v[212:215],a[116:119],v[72:75],v[212:215]\n"
"v_mfma_f32_16x16x32_bf16 v[216:219],a[120:123],v[72:75],v[216:219]\n"
"v_mfma_f32_16x16x32_bf16 v[220:223],a[124:127],v[72:75],v[220:223]\n"
"v_exp_f32 v59,v59\n"
"v_mfma_f32_16x16x32_bf16 v[224:227],a[96:99],v[76:79],v[224:227]\n"
"v_cvt_pk_bf16_f32 v93,v58,v59\n"
"v_accvgpr_read_b32 v122,a136\n"
"v_mfma_f32_16x16x32_bf16 a[140:143],a[144:147],v[76:79],a[140:143]\n"
"buffer_load_dwordx4 v112,s[24:27],s64 offen offset:2048 sc1 lds\n"
"v_mfma_f32_16x16x32_bf16 v[228:231],a[100:103],v[76:79],v[228:231]\n"
"v_rcp_f32 v122,v122\n"
"v_mfma_f32_16x16x32_bf16 v[232:235],a[104:107],v[76:79],v[232:235]\n"
"buffer_load_dwordx4 v112,s[24:27],s64 offen offset:3072 sc1 lds\n"
"v_pk_mul_f32 v[192:193],v[192:193],v[122:123] op_sel_hi:[1,0]\n"
"v_pk_mul_f32 v[194:195],v[194:195],v[122:123] op_sel_hi:[1,0]\n"
"v_mfma_f32_16x16x32_bf16 v[236:239],a[108:111],v[76:79],v[236:239]\n"
"v_cvt_pk_bf16_f32 v192,v192,v193\n"
"v_exp_f32 v60,v60\n"
"v_cvt_pk_bf16_f32 v193,v194,v195\n"
"v_pk_mul_f32 v[196:197],v[196:197],v[122:123] op_sel_hi:[1,0]\n"
"v_pk_mul_f32 v[198:199],v[198:199],v[122:123] op_sel_hi:[1,0]\n"
"v_mfma_f32_16x16x32_bf16 v[240:243],a[112:115],v[76:79],v[240:243]\n"
"s_add_u32 m0,s67,4096\n"
"v_cvt_pk_bf16_f32 v194,v196,v197\n"
"v_cvt_pk_bf16_f32 v195,v198,v199\n"
"v_mfma_f32_16x16x32_bf16 v[244:247],a[116:119],v[76:79],v[244:247]\n"
"v_mfma_f32_16x16x32_bf16 v[248:251],a[120:123],v[76:79],v[248:251]\n"
"buffer_store_dwordx4 v[192:195],v112,s[24:27],s70 offen offset:0 sc1\n"
"v_pk_mul_f32 v[200:201],v[200:201],v[122:123] op_sel_hi:[1,0]\n"
"v_mfma_f32_16x16x32_bf16 v[252:255],a[124:127],v[76:79],v[252:255]\n"
"buffer_load_dwordx4 v117,s[24:27],s64 offen offset:0 sc1 lds\n"
"v_cvt_pk_bf16_f32 v200,v200,v201\n"
"v_exp_f32 v61,v61\n"
"v_pk_mul_f32 v[202:203],v[202:203],v[122:123] op_sel_hi:[1,0]\n"
"v_pk_mul_f32 v[204:205],v[204:205],v[122:123] op_sel_hi:[1,0]\n"
"v_pk_mul_f32 v[206:207],v[206:207],v[122:123] op_sel_hi:[1,0]\n"
"v_mfma_f32_16x16x32_bf16 v[12:15],a[80:83],a[16:19],v[100:103]\n"
"v_cvt_pk_bf16_f32 v201,v202,v203\n"
"v_cvt_pk_bf16_f32 v202,v204,v205\n"
"v_mfma_f32_16x16x32_bf16 v[12:15],a[84:87],a[20:23],v[12:15]\n"
"v_cvt_pk_bf16_f32 v203,v206,v207\n"
"v_cvt_pk_bf16_f32 v94,v60,v61\n"
"v_mfma_f32_16x16x32_bf16 v[12:15],a[88:91],a[24:27],v[12:15]\n"
"buffer_load_dwordx4 v117,s[24:27],s64 offen offset:1024 sc1 lds\n"
"v_mfma_f32_16x16x32_bf16 v[12:15],a[92:95],a[28:31],v[12:15]\n"
"buffer_store_dwordx4 v[200:203],v112,s[24:27],s70 offen offset:1024 sc1\n"
"v_pk_mul_f32 v[208:209],v[208:209],v[122:123] op_sel_hi:[1,0]\n"
"v_pk_mul_f32 v[210:211],v[210:211],v[122:123] op_sel_hi:[1,0]\n"
"v_mfma_f32_16x16x32_bf16 v[8:11],a[76:79],a[28:31],v[100:103]\n"
"buffer_load_dwordx4 v117,s[24:27],s64 offen offset:2048 sc1 lds\n"
"v_cvt_pk_bf16_f32 v208,v208,v209\n"
"v_cvt_pk_bf16_f32 v209,v210,v211\n"
"v_pk_mul_f32 v[212:213],v[212:213],v[122:123] op_sel_hi:[1,0]\n"
"v_pk_mul_f32 v[214:215],v[214:215],v[122:123] op_sel_hi:[1,0]\n"
"v_mfma_f32_16x16x32_bf16 v[8:11],a[72:75],a[24:27],v[8:11]\n"
"v_cvt_pk_bf16_f32 v210,v212,v213\n"
"v_cvt_pk_bf16_f32 v211,v214,v215\n"
"v_mfma_f32_16x16x32_bf16 v[8:11],a[68:71],a[20:23],v[8:11]\n"
"v_exp_f32 v62,v62\n"
"v_mfma_f32_16x16x32_bf16 v[8:11],a[64:67],a[16:19],v[8:11]\n"
"buffer_store_dwordx4 v[208:211],v112,s[24:27],s70 offen offset:2048 sc1\n"
"v_pk_mul_f32 v[216:217],v[216:217],v[122:123] op_sel_hi:[1,0]\n"
"v_mfma_f32_16x16x32_bf16 v[16:19],a[64:67],a[32:35],v[104:107]\n"
"v_cvt_pk_bf16_f32 v216,v216,v217\n"
"buffer_load_dwordx4 v117,s[24:27],s64 offen offset:3072 sc1 lds\n"
"v_pk_mul_f32 v[218:219],v[218:219],v[122:123] op_sel_hi:[1,0]\n"
"v_pk_mul_f32 v[220:221],v[220:221],v[122:123] op_sel_hi:[1,0]\n"
"v_pk_mul_f32 v[222:223],v[222:223],v[122:123] op_sel_hi:[1,0]\n"
"v_mfma_f32_16x16x32_bf16 v[16:19],a[68:71],a[36:39],v[16:19]\n"
"v_cvt_pk_bf16_f32 v217,v218,v219\n"
"v_cvt_pk_bf16_f32 v218,v220,v221\n"
"v_mfma_f32_16x16x32_bf16 v[16:19],a[72:75],a[40:43],v[16:19]\n"
"s_add_u32 s64,s64,16384\n"
"v_exp_f32 v63,v63\n"
"v_mfma_f32_16x16x32_bf16 v[16:19],a[76:79],a[44:47],v[16:19]\n"
"s_andn2_b32 m0,s55,16384\n"
"s_add_u32 s54,s54,s22\n"
"v_max3_f32 v119,v8,v9,v10\n"
"v_mfma_f32_16x16x32_bf16 v[20:23],a[92:95],a[44:47],v[104:107]\n"
"v_max3_f32 v119,v119,v11,v12\n"
"v_cvt_pk_bf16_f32 v95,v62,v63\n"
"v_mfma_f32_16x16x32_bf16 v[20:23],a[88:91],a[40:43],v[20:23]\n"
"buffer_load_dwordx4 v115,s[32:35],s54 offen sc1 lds\n"
"v_mfma_f32_16x16x32_bf16 v[20:23],a[84:87],a[36:39],v[20:23]\n"
"v_max3_f32 v119,v119,v13,v14\n"
"v_max_f32 v119,v119,v15\n"
"v_mfma_f32_16x16x32_bf16 v[20:23],a[80:83],a[32:35],v[20:23]\n"
"v_cvt_pk_bf16_f32 v219,v222,v223\n"
"v_mfma_f32_16x16x32_bf16 v[28:31],a[80:83],a[48:51],v[108:111]\n"
"buffer_store_dwordx4 v[216:219],v112,s[24:27],s70 offen offset:3072 sc1\n"
"buffer_store_dwordx4 a[240:243],v112,s[24:27],s56 offen offset:0 sc1\n"
"v_mfma_f32_16x16x32_bf16 v[28:31],a[84:87],a[52:55],v[28:31]\n"
"s_add_u32 s70,s70,262144\n"
"v_max_f32 v118,v118,v119\n"
"v_min_f32 v120,v120,v119\n"
"v_mfma_f32_16x16x32_bf16 v[28:31],a[88:91],a[56:59],v[28:31]\n"
"v_max3_f32 v119,v16,v17,v18\n"
"v_mfma_f32_16x16x32_bf16 v[28:31],a[92:95],a[60:63],v[28:31]\n"
"v_max3_f32 v119,v119,v19,v20\n"
"v_max3_f32 v119,v119,v21,v22\n"
"v_mfma_f32_16x16x32_bf16 v[24:27],a[76:79],a[60:63],v[108:111]\n"
"v_max_f32 v119,v119,v23\n"
"v_max_f32 v118,v118,v119\n"
"v_mfma_f32_16x16x32_bf16 v[24:27],a[72:75],a[56:59],v[24:27]\n"
"v_min_f32 v120,v120,v119\n"
"v_mfma_f32_16x16x32_bf16 v[24:27],a[68:71],a[52:55],v[24:27]\n"
"v_mfma_f32_16x16x32_bf16 v[24:27],a[64:67],a[48:51],v[24:27]\n"
"ds_read_b128 a[64:67],v113 offset:16384\n"
"ds_read_b128 a[68:71],v113 offset:17408\n"
"ds_read_b128 a[72:75],v113 offset:18432\n"
"ds_read_b128 a[76:79],v113 offset:19456\n"
"ds_read_b128 a[80:83],v113 offset:20480\n"
"ds_read_b128 a[84:87],v113 offset:21504\n"
"ds_read_b128 a[88:91],v113 offset:22528\n"
"ds_read_b128 a[92:95],v113 offset:23552\n"
"v_max3_f32 v119,v24,v25,v26\n"
"v_max3_f32 v119,v119,v27,v28\n"
"v_max3_f32 v119,v119,v29,v30\n"
"v_max_f32 v119,v119,v31\n"
"v_min_f32 v120,v120,v119\n"
"v_max_f32 v118,v118,v119\n"
"v_cmp_gt_f32 s[96:97],s16,v120\n"
"v_cmp_lt_f32 vcc,s93,v118\n"
"s_waitcnt vmcnt(25)\n"
"ds_add_u32 v124,a152\n"
"ds_read_b32 v125,v124\n"
"s_waitcnt lgkmcnt(0)\n"
"s_or_b64 s[96:97],s[96:97],vcc\n"
"s_cbranch_scc1 .Lfk_cs_v3\n"
".Lfk_cr_v3:\n"
"v_cmp_gt_u32 vcc,s101,v125\n"
"s_nop 0\n"
"s_cbranch_vccnz .Lfk_cs_mv3\n"
".Lfk_cr_mv3:\n"
"v_exp_f32 v0,v0\n"
"v_mfma_f32_16x16x32_bf16 v[32:35],a[64:67],a[0:3],v[96:99]\n"
"s_add_u32 s101,s101,4\n"
"ds_read_b128 a[96:99],v114 offset:57344\n"
"ds_read_b128 a[100:103],v114 offset:58368\n"
"v_exp_f32 v1,v1\n"
"v_mfma_f32_16x16x32_bf16 v[32:35],a[68:71],a[4:7],v[32:35]\n"
"s_add_u32 m0,s55,4096\n"
"ds_read_b128 a[104:107],v114 offset:59392\n"
"ds_read_b128 a[108:111],v114 offset:60416\n"
"v_exp_f32 v2,v2\n"
"v_mfma_f32_16x16x32_bf16 v[32:35],a[72:75],a[8:11],v[32:35]\n"
"s_add_u32 s54,s54,s22\n"
"ds_read_b128 a[112:115],v114 offset:61440\n"
"ds_read_b128 a[116:119],v114 offset:62464\n"
"v_exp_f32 v3,v3\n"
"v_mfma_f32_16x16x32_bf16 v[32:35],a[76:79],a[12:15],v[32:35]\n"
"ds_read_b128 a[120:123],v114 offset:63488\n"
"ds_read_b128 a[124:127],v114 offset:64512\n"
"v_exp_f32 v4,v4\n"
"v_mfma_f32_16x16x32_bf16 v[36:39],a[92:95],a[12:15],v[96:99]\n"
"buffer_load_dwordx4 v115,s[32:35],s54 offen sc1 lds\n"
"v_mfma_f32_16x16x32_bf16 v[36:39],a[88:91],a[8:11],v[36:39]\n"
"s_add_u32 m0,s67,16384\n"
"v_exp_f32 v5,v5\n"
"v_cvt_pk_bf16_f32 v64,v0,v1\n"
"v_cvt_pk_bf16_f32 v65,v2,v3\n"
"v_mfma_f32_16x16x32_bf16 v[36:39],a[84:87],a[4:7],v[36:39]\n"
"v_exp_f32 v6,v6\n"
"v_exp_f32 v7,v7\n"
"v_mfma_f32_16x16x32_bf16 v[36:39],a[80:83],a[0:3],v[36:39]\n"
"buffer_load_dwordx4 v112,s[24:27],s64 offen offset:0 sc1 lds\n"
"s_waitcnt lgkmcnt(0)\n"
"v_mfma_f32_16x16x32_bf16 v[224:227],a[96:99],v[92:95],v[224:227]\n"
"v_exp_f32 v8,v8\n"
"v_cvt_pk_bf16_f32 v67,v6,v7\n"
"v_mfma_f32_16x16x32_bf16 a[140:143],a[144:147],v[92:95],a[140:143]\n"
"v_exp_f32 v9,v9\n"
"v_exp_f32 v10,v10\n"
"v_mfma_f32_16x16x32_bf16 v[228:231],a[100:103],v[92:95],v[228:231]\n"
"buffer_load_dwordx4 v112,s[24:27],s64 offen offset:1024 sc1 lds\n"
"v_mfma_f32_16x16x32_bf16 v[232:235],a[104:107],v[92:95],v[232:235]\n"
"v_cvt_pk_bf16_f32 v66,v4,v5\n"
"v_cvt_pk_bf16_f32 v68,v8,v9\n"
"v_mfma_f32_16x16x32_bf16 v[236:239],a[108:111],v[92:95],v[236:239]\n"
"v_exp_f32 v11,v11\n"
"v_exp_f32 v12,v12\n"
"v_mfma_f32_16x16x32_bf16 v[240:243],a[112:115],v[92:95],v[240:243]\n"
"buffer_load_dwordx4 v112,s[24:27],s64 offen offset:2048 sc1 lds\n"
"v_mfma_f32_16x16x32_bf16 v[244:247],a[116:119],v[92:95],v[244:247]\n"
"v_exp_f32 v13,v13\n"
"v_cvt_pk_bf16_f32 v69,v10,v11\n"
"v_mfma_f32_16x16x32_bf16 v[248:251],a[120:123],v[92:95],v[248:251]\n"
"v_exp_f32 v14,v14\n"
"v_cvt_pk_bf16_f32 v70,v12,v13\n"
"v_exp_f32 v15,v15\n"
"v_mfma_f32_16x16x32_bf16 v[252:255],a[124:127],v[92:95],v[252:255]\n"
"buffer_load_dwordx4 v112,s[24:27],s64 offen offset:3072 sc1 lds\n"
"v_mfma_f32_16x16x32_bf16 v[44:47],a[80:83],a[16:19],v[100:103]\n"
"s_add_u32 m0,s67,20480\n"
"v_exp_f32 v16,v16\n"
"v_cvt_pk_bf16_f32 v71,v14,v15\n"
"v_mfma_f32_16x16x32_bf16 v[44:47],a[84:87],a[20:23],v[44:47]\n"
"v_exp_f32 v17,v17\n"
"v_exp_f32 v18,v18\n"
"v_mfma_f32_16x16x32_bf16 v[44:47],a[88:91],a[24:27],v[44:47]\n"
"buffer_load_dwordx4 v117,s[24:27],s64 offen offset:0 sc1 lds\n"
"v_mfma_f32_16x16x32_bf16 v[44:47],a[92:95],a[28:31],v[44:47]\n"
"v_exp_f32 v19,v19\n"
"v_accvgpr_read_b32 v122,a140\n"
"v_mfma_f32_16x16x32_bf16 v[40:43],a[76:79],a[28:31],v[100:103]\n"
"v_cvt_pk_bf16_f32 v73,v18,v19\n"
"v_rcp_f32 v122,v122\n"
"v_mfma_f32_16x16x32_bf16 v[40:43],a[72:75],a[24:27],v[40:43]\n"
"buffer_load_dwordx4 v117,s[24:27],s64 offen offset:1024 sc1 lds\n"
"v_mfma_f32_16x16x32_bf16 v[40:43],a[68:71],a[20:23],v[40:43]\n"
"v_exp_f32 v21,v21\n"
"v_exp_f32 v20,v20\n"
"v_cvt_pk_bf16_f32 v72,v16,v17\n"
"v_pk_mul_f32 v[224:225],v[224:225],v[122:123] op_sel_hi:[1,0]\n"
"v_pk_mul_f32 v[226:227],v[226:227],v[122:123] op_sel_hi:[1,0]\n"
"v_pk_mul_f32 v[228:229],v[228:229],v[122:123] op_sel_hi:[1,0]\n"
"v_pk_mul_f32 v[230:231],v[230:231],v[122:123] op_sel_hi:[1,0]\n"
"v_mfma_f32_16x16x32_bf16 v[40:43],a[64:67],a[16:19],v[40:43]\n"
"v_cvt_pk_bf16_f32 v74,v20,v21\n"
"v_exp_f32 v22,v22\n"
"v_cvt_pk_bf16_f32 v224,v224,v225\n"
"v_exp_f32 v23,v23\n"
"v_mfma_f32_16x16x32_bf16 v[48:51],a[64:67],a[32:35],v[104:107]\n"
"buffer_load_dwordx4 v117,s[24:27],s64 offen offset:2048 sc1 lds\n"
"v_mfma_f32_16x16x32_bf16 v[48:51],a[68:71],a[36:39],v[48:51]\n"
"v_exp_f32 v24,v24\n"
"v_cvt_pk_bf16_f32 v225,v226,v227\n"
"v_mfma_f32_16x16x32_bf16 v[48:51],a[72:75],a[40:43],v[48:51]\n"
"v_exp_f32 v25,v25\n"
"v_cvt_pk_bf16_f32 v226,v228,v229\n"
"v_cvt_pk_bf16_f32 v227,v230,v231\n"
"v_mfma_f32_16x16x32_bf16 v[48:51],a[76:79],a[44:47],v[48:51]\n"
"buffer_load_dwordx4 v117,s[24:27],s64 offen offset:3072 sc1 lds\n"
"v_mfma_f32_16x16x32_bf16 v[52:55],a[92:95],a[44:47],v[104:107]\n"
"buffer_store_dwordx4 v[224:227],v112,s[24:27],s70 offen offset:0 sc1\n"
"v_exp_f32 v27,v27\n"
"v_mfma_f32_16x16x32_bf16 v[52:55],a[88:91],a[40:43],v[52:55]\n"
"s_add_u32 s64,s64,16384\n"
"v_exp_f32 v26,v26\n"
"v_exp_f32 v28,v28\n"
"v_cvt_pk_bf16_f32 v77,v26,v27\n"
"v_pk_mul_f32 v[232:233],v[232:233],v[122:123] op_sel_hi:[1,0]\n"
"v_mfma_f32_16x16x32_bf16 v[52:55],a[84:87],a[36:39],v[52:55]\n"
"v_cvt_pk_bf16_f32 v75,v22,v23\n"
"v_cvt_pk_bf16_f32 v76,v24,v25\n"
"v_exp_f32 v29,v29\n"
"v_exp_f32 v31,v31\n"
"v_pk_mul_f32 v[234:235],v[234:235],v[122:123] op_sel_hi:[1,0]\n"
"v_pk_mul_f32 v[236:237],v[236:237],v[122:123] op_sel_hi:[1,0]\n"
"v_pk_mul_f32 v[238:239],v[238:239],v[122:123] op_sel_hi:[1,0]\n"
"v_mfma_f32_16x16x32_bf16 v[52:55],a[80:83],a[32:35],v[52:55]\n"
"v_exp_f32 v30,v30\n"
"v_cvt_pk_bf16_f32 v78,v28,v29\n"
"v_mfma_f32_16x16x32_bf16 v[60:63],a[80:83],a[48:51],v[108:111]\n"
"v_cvt_pk_bf16_f32 v232,v232,v233\n"
"v_cvt_pk_bf16_f32 v233,v234,v235\n"
"v_mfma_f32_16x16x32_bf16 v[60:63],a[84:87],a[52:55],v[60:63]\n"
"buffer_store_dwordx4 a[244:247],v112,s[24:27],s56 offen offset:1024 sc1\n"
"v_cvt_pk_bf16_f32 v234,v236,v237\n"
"v_cvt_pk_bf16_f32 v235,v238,v239\n"
"v_mfma_f32_16x16x32_bf16 v[60:63],a[88:91],a[56:59],v[60:63]\n"
"buffer_store_dwordx4 v[232:235],v112,s[24:27],s70 offen offset:1024 sc1\n"
"v_pk_mul_f32 v[240:241],v[240:241],v[122:123] op_sel_hi:[1,0]\n"
"v_pk_mul_f32 v[242:243],v[242:243],v[122:123] op_sel_hi:[1,0]\n"
"v_pk_mul_f32 v[244:245],v[244:245],v[122:123] op_sel_hi:[1,0]\n"
"v_pk_mul_f32 v[246:247],v[246:247],v[122:123] op_sel_hi:[1,0]\n"
"v_mfma_f32_16x16x32_bf16 v[60:63],a[92:95],a[60:63],v[60:63]\n"
"v_cvt_pk_bf16_f32 v240,v240,v241\n"
"v_cvt_pk_bf16_f32 v241,v242,v243\n"
"v_cvt_pk_bf16_f32 v242,v244,v245\n"
"v_cvt_pk_bf16_f32 v243,v246,v247\n"
"v_mfma_f32_16x16x32_bf16 v[56:59],a[76:79],a[60:63],v[108:111]\n"
"buffer_store_dwordx4 v[240:243],v112,s[24:27],s70 offen offset:2048 sc1\n"
"v_pk_mul_f32 v[248:249],v[248:249],v[122:123] op_sel_hi:[1,0]\n"
"v_pk_mul_f32 v[250:251],v[250:251],v[122:123] op_sel_hi:[1,0]\n"
"v_mfma_f32_16x16x32_bf16 v[56:59],a[72:75],a[56:59],v[56:59]\n"
"v_cvt_pk_bf16_f32 v79,v30,v31\n"
"v_cvt_pk_bf16_f32 v248,v248,v249\n"
"v_cvt_pk_bf16_f32 v249,v250,v251\n"
"v_pk_mul_f32 v[252:253],v[252:253],v[122:123] op_sel_hi:[1,0]\n"
"v_pk_mul_f32 v[254:255],v[254:255],v[122:123] op_sel_hi:[1,0]\n"
"v_mfma_f32_16x16x32_bf16 v[56:59],a[68:71],a[52:55],v[56:59]\n"
"v_cvt_pk_bf16_f32 v250,v252,v253\n"
"v_cvt_pk_bf16_f32 v251,v254,v255\n"
"v_mfma_f32_16x16x32_bf16 v[56:59],a[64:67],a[48:51],v[56:59]\n"
"buffer_store_dwordx4 v[248:251],v112,s[24:27],s70 offen offset:3072 sc1\n"
"s_add_u32 s70,s70,262144\n"
"ds_read_b128 a[64:67],v113 offset:49152\n"
"ds_read_b128 a[68:71],v113 offset:50176\n"
"ds_read_b128 a[72:75],v113 offset:51200\n"
"ds_read_b128 a[76:79],v113 offset:52224\n"
"ds_read_b128 a[80:83],v113 offset:53248\n"
"ds_read_b128 a[84:87],v113 offset:54272\n"
"ds_read_b128 a[88:91],v113 offset:55296\n"
"ds_read_b128 a[92:95],v113 offset:56320\n"
"v_mov_b32 v114,v112\n"
"s_mov_b32 s70,s68\n"
"s_waitcnt vmcnt(25)\n"
"ds_add_u32 v124,a152\n"
"ds_read_b32 v125,v124\n"
"s_waitcnt lgkmcnt(0)\n"
"v_cmp_gt_u32 vcc,s101,v125\n"
"s_nop 0\n"
"s_cbranch_vccnz .Lfk_cs_mv0\n"
".Lfk_cr_mv0:\n"
"v_mfma_f32_16x16x32_bf16 v[0:3],a[64:67],a[0:3],v[96:99]\n"
"s_add_u32 s101,s101,4\n"
"v_exp_f32 v122,v32\n"
"v_mfma_f32_16x16x32_bf16 v[0:3],a[68:71],a[4:7],v[0:3]\n"
"s_add_u32 m0,s55,8192\n"
"v_exp_f32 v123,v33\n"
"v_mfma_f32_16x16x32_bf16 v[0:3],a[72:75],a[8:11],v[0:3]\n"
"ds_read_b128 a[96:99],v114 offset:40960\n"
"ds_read_b128 a[100:103],v114 offset:41984\n"
"v_mfma_f32_16x16x32_bf16 v[0:3],a[76:79],a[12:15],v[0:3]\n"
"s_add_u32 s54,s54,s22\n"
"v_exp_f32 v119,v34\n"
"v_mfma_f32_16x16x32_bf16 v[4:7],a[92:95],a[12:15],v[96:99]\n"
"ds_read_b128 a[104:107],v114 offset:43008\n"
"v_cvt_pk_bf16_f32 v80,v122,v123\n"
"v_mfma_f32_16x16x32_bf16 v[4:7],a[88:91],a[8:11],v[4:7]\n"
"v_exp_f32 v120,v35\n"
"v_mfma_f32_16x16x32_bf16 v[4:7],a[84:87],a[4:7],v[4:7]\n"
"ds_read_b128 a[108:111],v114 offset:44032\n"
"ds_read_b128 a[112:115],v114 offset:45056\n"
"v_mfma_f32_16x16x32_bf16 v[4:7],a[80:83],a[0:3],v[4:7]\n"
"v_exp_f32 v122,v36\n"
"v_mfma_f32_16x16x32_bf16 v[12:15],a[80:83],a[16:19],v[100:103]\n"
"ds_read_b128 a[116:119],v114 offset:46080\n"
"v_cvt_pk_bf16_f32 v81,v119,v120\n"
"v_mfma_f32_16x16x32_bf16 v[12:15],a[84:87],a[20:23],v[12:15]\n"
"v_exp_f32 v123,v37\n"
"v_mfma_f32_16x16x32_bf16 v[12:15],a[88:91],a[24:27],v[12:15]\n"
"ds_read_b128 a[120:123],v114 offset:47104\n"
"ds_read_b128 a[124:127],v114 offset:48128\n"
"v_mfma_f32_16x16x32_bf16 v[12:15],a[92:95],a[28:31],v[12:15]\n"
"v_exp_f32 v119,v38\n"
"v_mfma_f32_16x16x32_bf16 v[8:11],a[76:79],a[28:31],v[100:103]\n"
"v_cvt_pk_bf16_f32 v82,v122,v123\n"
"v_or3_b32 v118,v80,v81,v82\n"
"v_mfma_f32_16x16x32_bf16 v[8:11],a[72:75],a[24:27],v[8:11]\n"
"v_exp_f32 v120,v39\n"
"v_mfma_f32_16x16x32_bf16 v[8:11],a[68:71],a[20:23],v[8:11]\n"
"buffer_load_dwordx4 v115,s[32:35],s54 offen sc1 lds\n"
"v_mfma_f32_16x16x32_bf16 v[8:11],a[64:67],a[16:19],v[8:11]\n"
"s_add_u32 m0,s67,49152\n"
"v_exp_f32 v122,v40\n"
"v_mfma_f32_16x16x32_bf16 v[16:19],a[64:67],a[32:35],v[104:107]\n"
"v_cvt_pk_bf16_f32 v83,v119,v120\n"
"v_mfma_f32_16x16x32_bf16 v[16:19],a[68:71],a[36:39],v[16:19]\n"
"v_exp_f32 v123,v41\n"
"v_mfma_f32_16x16x32_bf16 v[16:19],a[72:75],a[40:43],v[16:19]\n"
"buffer_load_dwordx4 v112,s[24:27],s64 offen offset:0 sc1 lds\n"
"v_mfma_f32_16x16x32_bf16 v[16:19],a[76:79],a[44:47],v[16:19]\n"
"v_exp_f32 v119,v42\n"
"v_mfma_f32_16x16x32_bf16 v[20:23],a[92:95],a[44:47],v[104:107]\n"
"v_cvt_pk_bf16_f32 v84,v122,v123\n"
"v_or3_b32 v118,v118,v83,v84\n"
"v_mfma_f32_16x16x32_bf16 v[20:23],a[88:91],a[40:43],v[20:23]\n"
"v_exp_f32 v120,v43\n"
"v_mfma_f32_16x16x32_bf16 v[20:23],a[84:87],a[36:39],v[20:23]\n"
"buffer_load_dwordx4 v112,s[24:27],s64 offen offset:1024 sc1 lds\n"
"v_mfma_f32_16x16x32_bf16 v[20:23],a[80:83],a[32:35],v[20:23]\n"
"v_exp_f32 v122,v44\n"
"v_mfma_f32_16x16x32_bf16 v[28:31],a[80:83],a[48:51],v[108:111]\n"
"v_cvt_pk_bf16_f32 v85,v119,v120\n"
"v_mfma_f32_16x16x32_bf16 v[28:31],a[84:87],a[52:55],v[28:31]\n"
"v_exp_f32 v123,v45\n"
"v_mfma_f32_16x16x32_bf16 v[28:31],a[88:91],a[56:59],v[28:31]\n"
"buffer_load_dwordx4 v112,s[24:27],s64 offen offset:2048 sc1 lds\n"
"v_mfma_f32_16x16x32_bf16 v[28:31],a[92:95],a[60:63],v[28:31]\n"
"v_exp_f32 v119,v46\n"
"v_mfma_f32_16x16x32_bf16 v[24:27],a[76:79],a[60:63],v[108:111]\n"
"v_cvt_pk_bf16_f32 v86,v122,v123\n"
"v_or3_b32 v118,v118,v85,v86\n"
"v_mfma_f32_16x16x32_bf16 v[24:27],a[72:75],a[56:59],v[24:27]\n"
"v_exp_f32 v120,v47\n"
"v_mfma_f32_16x16x32_bf16 v[24:27],a[68:71],a[52:55],v[24:27]\n"
"buffer_load_dwordx4 v112,s[24:27],s64 offen offset:3072 sc1 lds\n"
"v_mfma_f32_16x16x32_bf16 v[24:27],a[64:67],a[48:51],v[24:27]\n"
"s_waitcnt lgkmcnt(0)\n"
"v_exp_f32 v122,v48\n"
"v_mfma_f32_16x16x32_bf16 v[128:131],a[96:99],v[64:67],0\n"
"v_exp_f32 v123,v49\n"
"v_mfma_f32_16x16x32_bf16 v[160:163],a[96:99],v[68:71],0\n"
"ds_read_b128 a[64:67],v113 offset:32768\n"
"v_cvt_pk_bf16_f32 v87,v119,v120\n"
"v_mfma_f32_16x16x32_bf16 v[192:195],a[96:99],v[72:75],0\n"
"s_add_u32 m0,s67,53248\n"
"v_exp_f32 v119,v50\n"
"v_mfma_f32_16x16x32_bf16 v[224:227],a[96:99],v[76:79],0\n"
"ds_read_b128 a[68:71],v113 offset:33792\n"
"v_cvt_pk_bf16_f32 v88,v122,v123\n"
"v_mfma_f32_16x16x32_bf16 a[140:143],a[144:147],v[76:79],0\n"
"v_exp_f32 v120,v51\n"
"v_mfma_f32_16x16x32_bf16 v[228:231],a[100:103],v[76:79],0\n"
"ds_read_b128 a[72:75],v113 offset:34816\n"
"ds_read_b128 a[76:79],v113 offset:35840\n"
"v_mfma_f32_16x16x32_bf16 v[132:135],a[100:103],v[64:67],0\n"
"v_exp_f32 v122,v52\n"
"v_mfma_f32_16x16x32_bf16 v[164:167],a[100:103],v[68:71],0\n"
"ds_read_b128 a[80:83],v113 offset:36864\n"
"v_cvt_pk_bf16_f32 v89,v119,v120\n"
"v_mfma_f32_16x16x32_bf16 v[196:199],a[100:103],v[72:75],0\n"
"v_exp_f32 v123,v53\n"
"v_mfma_f32_16x16x32_bf16 a[136:139],a[144:147],v[72:75],0\n"
"ds_read_b128 a[84:87],v113 offset:37888\n"
"ds_read_b128 a[88:91],v113 offset:38912\n"
"v_mfma_f32_16x16x32_bf16 v[200:203],a[104:107],v[72:75],0\n"
"v_exp_f32 v119,v54\n"
"v_mfma_f32_16x16x32_bf16 v[232:235],a[104:107],v[76:79],0\n"
"ds_read_b128 a[92:95],v113 offset:39936\n"
"v_cvt_pk_bf16_f32 v90,v122,v123\n"
"v_mfma_f32_16x16x32_bf16 v[136:139],a[104:107],v[64:67],0\n"
"v_exp_f32 v120,v55\n"
"v_mfma_f32_16x16x32_bf16 v[168:171],a[104:107],v[68:71],0\n"
"s_waitcnt vmcnt(16)\n"
"ds_add_u32 v124,a152\n"
"v_mfma_f32_16x16x32_bf16 a[132:135],a[144:147],v[68:71],0\n"
"v_exp_f32 v122,v56\n"
"v_mfma_f32_16x16x32_bf16 v[172:175],a[108:111],v[68:71],0\n"
"v_cvt_pk_bf16_f32 v91,v119,v120\n"
"v_or3_b32 v118,v118,v87,v88\n"
"v_mfma_f32_16x16x32_bf16 v[204:207],a[108:111],v[72:75],0\n"
"v_exp_f32 v123,v57\n"
"v_mfma_f32_16x16x32_bf16 v[236:239],a[108:111],v[76:79],0\n"
"buffer_load_dwordx4 v117,s[24:27],s64 offen offset:0 sc1 lds\n"
"v_mfma_f32_16x16x32_bf16 v[140:143],a[108:111],v[64:67],0\n"
"v_exp_f32 v119,v58\n"
"v_mfma_f32_16x16x32_bf16 a[128:131],a[144:147],v[64:67],0\n"
"v_cvt_pk_bf16_f32 v92,v122,v123\n"
"v_or3_b32 v118,v118,v89,v90\n"
"v_mfma_f32_16x16x32_bf16 v[144:147],a[112:115],v[64:67],0\n"
"v_exp_f32 v120,v59\n"
"v_mfma_f32_16x16x32_bf16 v[176:179],a[112:115],v[68:71],0\n"
"buffer_load_dwordx4 v117,s[24:27],s64 offen offset:1024 sc1 lds\n"
"v_mfma_f32_16x16x32_bf16 v[208:211],a[112:115],v[72:75],0\n"
"v_exp_f32 v122,v60\n"
"v_mfma_f32_16x16x32_bf16 v[240:243],a[112:115],v[76:79],0\n"
"v_cvt_pk_bf16_f32 v93,v119,v120\n"
"v_or3_b32 v118,v118,v91,v92\n"
"v_mfma_f32_16x16x32_bf16 v[244:247],a[116:119],v[76:79],0\n"
"v_exp_f32 v123,v61\n"
"v_mfma_f32_16x16x32_bf16 v[148:151],a[116:119],v[64:67],0\n"
"buffer_load_dwordx4 v117,s[24:27],s64 offen offset:2048 sc1 lds\n"
"v_mfma_f32_16x16x32_bf16 v[180:183],a[116:119],v[68:71],0\n"
"v_exp_f32 v119,v62\n"
"v_mfma_f32_16x16x32_bf16 v[212:215],a[116:119],v[72:75],0\n"
"ds_read_b32 v125,v124\n"
"v_cvt_pk_bf16_f32 v94,v122,v123\n"
"v_mfma_f32_16x16x32_bf16 v[216:219],a[120:123],v[72:75],0\n"
"v_exp_f32 v120,v63\n"
"v_mfma_f32_16x16x32_bf16 v[248:251],a[120:123],v[76:79],0\n"
"buffer_load_dwordx4 v117,s[24:27],s64 offen offset:3072 sc1 lds\n"
"v_mfma_f32_16x16x32_bf16 v[152:155],a[120:123],v[64:67],0\n"
"buffer_store_dwordx4 a[248:251],v112,s[24:27],s56 offen offset:2048 sc1\n"
"v_mfma_f32_16x16x32_bf16 v[184:187],a[120:123],v[68:71],0\n"
"s_add_u32 s64,s64,16384\n"
"v_cvt_pk_bf16_f32 v95,v119,v120\n"
"v_or3_b32 v118,v118,v93,v94\n"
"v_mfma_f32_16x16x32_bf16 v[188:191],a[124:127],v[68:71],0\n"
"s_waitcnt lgkmcnt(0)\n"
"v_subrev_u32 v125,s101,v125\n"
"v_or3_b32 v125,v118,v95,v125\n"
"v_mfma_f32_16x16x32_bf16 v[220:223],a[124:127],v[72:75],0\n"
"v_and_b32 v125,0x40004000,v125\n"
"v_cmp_ne_u32 vcc,0,v125\n"
"v_mfma_f32_16x16x32_bf16 v[252:255],a[124:127],v[76:79],0\n"
"v_mfma_f32_16x16x32_bf16 v[156:159],a[124:127],v[64:67],0\n"
"s_cbranch_vccnz .Lfk_cs_v1\n"
".Lfk_cr_v1:\n"
"v_mfma_f32_16x16x32_bf16 v[32:35],a[64:67],a[0:3],v[96:99]\n"
"s_add_u32 s101,s101,4\n"
"v_exp_f32 v122,v0\n"
"v_mfma_f32_16x16x32_bf16 v[32:35],a[68:71],a[4:7],v[32:35]\n"
"s_add_u32 m0,s55,12288\n"
"v_exp_f32 v123,v1\n"
"v_mfma_f32_16x16x32_bf16 v[32:35],a[72:75],a[8:11],v[32:35]\n"
"ds_read_b128 a[96:99],v114 offset:8192\n"
"ds_read_b128 a[100:103],v114 offset:9216\n"
"v_mfma_f32_16x16x32_bf16 v[32:35],a[76:79],a[12:15],v[32:35]\n"
"s_add_u32 s54,s54,s22\n"
"v_exp_f32 v119,v2\n"
"v_mfma_f32_16x16x32_bf16 v[36:39],a[92:95],a[12:15],v[96:99]\n"
"ds_read_b128 a[104:107],v114 offset:10240\n"
"v_cvt_pk_bf16_f32 v64,v122,v123\n"
"v_mfma_f32_16x16x32_bf16 v[36:39],a[88:91],a[8:11],v[36:39]\n"
"v_exp_f32 v120,v3\n"
"v_mfma_f32_16x16x32_bf16 v[36:39],a[84:87],a[4:7],v[36:39]\n"
"ds_read_b128 a[108:111],v114 offset:11264\n"
"ds_read_b128 a[112:115],v114 offset:12288\n"
"v_mfma_f32_16x16x32_bf16 v[36:39],a[80:83],a[0:3],v[36:39]\n"
"v_exp_f32 v122,v4\n"
"v_mfma_f32_16x16x32_bf16 v[44:47],a[80:83],a[16:19],v[100:103]\n"
"ds_read_b128 a[116:119],v114 offset:13312\n"
"v_cvt_pk_bf16_f32 v65,v119,v120\n"
"v_mfma_f32_16x16x32_bf16 v[44:47],a[84:87],a[20:23],v[44:47]\n"
"v_exp_f32 v123,v5\n"
"v_mfma_f32_16x16x32_bf16 v[44:47],a[88:91],a[24:27],v[44:47]\n"
"ds_read_b128 a[120:123],v114 offset:14336\n"
"ds_read_b128 a[124:127],v114 offset:15360\n"
"v_mfma_f32_16x16x32_bf16 v[44:47],a[92:95],a[28:31],v[44:47]\n"
"v_exp_f32 v119,v6\n"
"v_mfma_f32_16x16x32_bf16 v[40:43],a[76:79],a[28:31],v[100:103]\n"
"v_cvt_pk_bf16_f32 v66,v122,v123\n"
"v_or3_b32 v118,v64,v65,v66\n"
"v_mfma_f32_16x16x32_bf16 v[40:43],a[72:75],a[24:27],v[40:43]\n"
"v_exp_f32 v120,v7\n"
"v_mfma_f32_16x16x32_bf16 v[40:43],a[68:71],a[20:23],v[40:43]\n"
"buffer_load_dwordx4 v115,s[32:35],s54 offen sc1 lds\n"
"v_mfma_f32_16x16x32_bf16 v[40:43],a[64:67],a[16:19],v[40:43]\n"
"s_add_u32 m0,s67,32768\n"
"v_exp_f32 v122,v8\n"
"v_mfma_f32_16x16x32_bf16 v[48:51],a[64:67],a[32:35],v[104:107]\n"
"v_cvt_pk_bf16_f32 v67,v119,v120\n"
"v_mfma_f32_16x16x32_bf16 v[48:51],a[68:71],a[36:39],v[48:51]\n"
"v_exp_f32 v123,v9\n"
"v_mfma_f32_16x16x32_bf16 v[48:51],a[72:75],a[40:43],v[48:51]\n"
"buffer_load_dwordx4 v112,s[24:27],s64 offen offset:0 sc1 lds\n"
"v_mfma_f32_16x16x32_bf16 v[48:51],a[76:79],a[44:47],v[48:51]\n"
"v_exp_f32 v119,v10\n"
"v_mfma_f32_16x16x32_bf16 v[52:55],a[92:95],a[44:47],v[104:107]\n"
"v_cvt_pk_bf16_f32 v68,v122,v123\n"
"v_or3_b32 v118,v118,v67,v68\n"
"v_mfma_f32_16x16x32_bf16 v[52:55],a[88:91],a[40:43],v[52:55]\n"
"v_exp_f32 v120,v11\n"
"v_mfma_f32_16x16x32_bf16 v[52:55],a[84:87],a[36:39],v[52:55]\n"
"buffer_load_dwordx4 v112,s[24:27],s64 offen offset:1024 sc1 lds\n"
"v_mfma_f32_16x16x32_bf16 v[52:55],a[80:83],a[32:35],v[52:55]\n"
"v_exp_f32 v122,v12\n"
"v_mfma_f32_16x16x32_bf16 v[60:63],a[80:83],a[48:51],v[108:111]\n"
"v_cvt_pk_bf16_f32 v69,v119,v120\n"
"v_mfma_f32_16x16x32_bf16 v[60:63],a[84:87],a[52:55],v[60:63]\n"
"v_exp_f32 v123,v13\n"
"v_mfma_f32_16x16x32_bf16 v[60:63],a[88:91],a[56:59],v[60:63]\n"
"buffer_load_dwordx4 v112,s[24:27],s64 offen offset:2048 sc1 lds\n"
"v_mfma_f32_16x16x32_bf16 v[60:63],a[92:95],a[60:63],v[60:63]\n"
"v_exp_f32 v119,v14\n"
"v_mfma_f32_16x16x32_bf16 v[56:59],a[76:79],a[60:63],v[108:111]\n"
"v_cvt_pk_bf16_f32 v70,v122,v123\n"
"v_or3_b32 v118,v118,v69,v70\n"
"v_mfma_f32_16x16x32_bf16 v[56:59],a[72:75],a[56:59],v[56:59]\n"
"v_exp_f32 v120,v15\n"
"v_mfma_f32_16x16x32_bf16 v[56:59],a[68:71],a[52:55],v[56:59]\n"
"buffer_load_dwordx4 v112,s[24:27],s64 offen offset:3072 sc1 lds\n"
"v_mfma_f32_16x16x32_bf16 v[56:59],a[64:67],a[48:51],v[56:59]\n"
"s_waitcnt lgkmcnt(0)\n"
"v_exp_f32 v122,v16\n"
"v_mfma_f32_16x16x32_bf16 v[128:131],a[96:99],v[80:83],v[128:131]\n"
"v_exp_f32 v123,v17\n"
"v_mfma_f32_16x16x32_bf16 v[160:163],a[96:99],v[84:87],v[160:163]\n"
"ds_read_b128 a[64:67],v113 offset:0\n"
"v_cvt_pk_bf16_f32 v71,v119,v120\n"
"v_mfma_f32_16x16x32_bf16 v[192:195],a[96:99],v[88:91],v[192:195]\n"
"s_add_u32 m0,s67,36864\n"
"v_exp_f32 v119,v18\n"
"v_mfma_f32_16x16x32_bf16 v[224:227],a[96:99],v[92:95],v[224:227]\n"
"ds_read_b128 a[68:71],v113 offset:1024\n"
"v_cvt_pk_bf16_f32 v72,v122,v123\n"
"v_mfma_f32_16x16x32_bf16 a[140:143],a[144:147],v[92:95],a[140:143]\n"
"v_exp_f32 v120,v19\n"
"v_mfma_f32_16x16x32_bf16 v[228:231],a[100:103],v[92:95],v[228:231]\n"
"ds_read_b128 a[72:75],v113 offset:2048\n"
"ds_read_b128 a[76:79],v113 offset:3072\n"
"v_mfma_f32_16x16x32_bf16 v[132:135],a[100:103],v[80:83],v[132:135]\n"
"v_exp_f32 v122,v20\n"
"v_mfma_f32_16x16x32_bf16 v[164:167],a[100:103],v[84:87],v[164:167]\n"
"ds_read_b128 a[80:83],v113 offset:4096\n"
"v_cvt_pk_bf16_f32 v73,v119,v120\n"
"v_mfma_f32_16x16x32_bf16 v[196:199],a[100:103],v[88:91],v[196:199]\n"
"v_exp_f32 v123,v21\n"
"v_mfma_f32_16x16x32_bf16 a[136:139],a[144:147],v[88:91],a[136:139]\n"
"ds_read_b128 a[84:87],v113 offset:5120\n"
"ds_read_b128 a[88:91],v113 offset:6144\n"
"v_mfma_f32_16x16x32_bf16 v[200:203],a[104:107],v[88:91],v[200:203]\n"
"v_exp_f32 v119,v22\n"
"v_mfma_f32_16x16x32_bf16 v[232:235],a[104:107],v[92:95],v[232:235]\n"
"ds_read_b128 a[92:95],v113 offset:7168\n"
"v_cvt_pk_bf16_f32 v74,v122,v123\n"
"v_mfma_f32_16x16x32_bf16 v[136:139],a[104:107],v[80:83],v[136:139]\n"
"v_exp_f32 v120,v23\n"
"v_mfma_f32_16x16x32_bf16 v[168:171],a[104:107],v[84:87],v[168:171]\n"
"s_waitcnt vmcnt(16)\n"
"ds_add_u32 v124,a152\n"
"v_mfma_f32_16x16x32_bf16 a[132:135],a[144:147],v[84:87],a[132:135]\n"
"v_exp_f32 v122,v24\n"
"v_mfma_f32_16x16x32_bf16 v[172:175],a[108:111],v[84:87],v[172:175]\n"
"v_cvt_pk_bf16_f32 v75,v119,v120\n"
"v_or3_b32 v118,v118,v71,v72\n"
"v_mfma_f32_16x16x32_bf16 v[204:207],a[108:111],v[88:91],v[204:207]\n"
"v_exp_f32 v123,v25\n"
"v_mfma_f32_16x16x32_bf16 v[236:239],a[108:111],v[92:95],v[236:239]\n"
"buffer_load_dwordx4 v117,s[24:27],s64 offen offset:0 sc1 lds\n"
"v_mfma_f32_16x16x32_bf16 v[140:143],a[108:111],v[80:83],v[140:143]\n"
"v_exp_f32 v119,v26\n"
"v_mfma_f32_16x16x32_bf16 a[128:131],a[144:147],v[80:83],a[128:131]\n"
"v_cvt_pk_bf16_f32 v76,v122,v123\n"
"v_or3_b32 v118,v118,v73,v74\n"
"v_mfma_f32_16x16x32_bf16 v[144:147],a[112:115],v[80:83],v[144:147]\n"
"v_exp_f32 v120,v27\n"
"v_mfma_f32_16x16x32_bf16 v[176:179],a[112:115],v[84:87],v[176:179]\n"
"buffer_load_dwordx4 v117,s[24:27],s64 offen offset:1024 sc1 lds\n"
"v_mfma_f32_16x16x32_bf16 v[208:211],a[112:115],v[88:91],v[208:211]\n"
"v_exp_f32 v122,v28\n"
"v_mfma_f32_16x16x32_bf16 v[240:243],a[112:115],v[92:95],v[240:243]\n"
"v_cvt_pk_bf16_f32 v77,v119,v120\n"
"v_or3_b32 v118,v118,v75,v76\n"
"v_mfma_f32_16x16x32_bf16 v[244:247],a[116:119],v[92:95],v[244:247]\n"
"v_exp_f32 v123,v29\n"
"v_mfma_f32_16x16x32_bf16 v[148:151],a[116:119],v[80:83],v[148:151]\n"
"buffer_load_dwordx4 v117,s[24:27],s64 offen offset:2048 sc1 lds\n"
"v_mfma_f32_16x16x32_bf16 v[180:183],a[116:119],v[84:87],v[180:183]\n"
"v_exp_f32 v119,v30\n"
"v_mfma_f32_16x16x32_bf16 v[212:215],a[116:119],v[88:91],v[212:215]\n"
"ds_read_b32 v125,v124\n"
"v_cvt_pk_bf16_f32 v78,v122,v123\n"
"v_mfma_f32_16x16x32_bf16 v[216:219],a[120:123],v[88:91],v[216:219]\n"
"v_exp_f32 v120,v31\n"
"v_mfma_f32_16x16x32_bf16 v[248:251],a[120:123],v[92:95],v[248:251]\n"
"buffer_load_dwordx4 v117,s[24:27],s64 offen offset:3072 sc1 lds\n"
"v_mfma_f32_16x16x32_bf16 v[152:155],a[120:123],v[80:83],v[152:155]\n"
"buffer_store_dwordx4 a[252:255],v112,s[24:27],s56 offen offset:3072 sc1\n"
"v_mfma_f32_16x16x32_bf16 v[184:187],a[120:123],v[84:87],v[184:187]\n"
"s_add_u32 s64,s64,16384\n"
"s_add_u32 s89,s89,1\n"
"v_cvt_pk_bf16_f32 v79,v119,v120\n"
"v_mfma_f32_16x16x32_bf16 v[188:191],a[124:127],v[84:87],v[188:191]\n"
"s_waitcnt lgkmcnt(0)\n"
"v_subrev_u32 v125,s101,v125\n"
"v_or3_b32 v118,v118,v77,v78\n"
"v_mfma_f32_16x16x32_bf16 v[220:223],a[124:127],v[88:91],v[220:223]\n"
"v_or3_b32 v125,v118,v79,v125\n"
"v_and_b32 v125,0x40004000,v125\n"
"v_mfma_f32_16x16x32_bf16 v[252:255],a[124:127],v[92:95],v[252:255]\n"
"v_cmp_ne_u32 vcc,0,v125\n"
"v_mfma_f32_16x16x32_bf16 v[156:159],a[124:127],v[80:83],v[156:159]\n"
"s_cbranch_vccnz .Lfk_cs_v2\n"
".Lfk_cr_v2:\n"
"s_waitcnt vmcnt(20)\n"
"buffer_store_dwordx2 a[154:155],off,s[24:27],s69 sc1\n"
"s_branch .Lfk_Hx\n"
".Lfk_tailB:\n"
"s_waitcnt vmcnt(11) lgkmcnt(0)\n"
"s_barrier\n"
"v_mfma_f32_16x16x32_bf16 v[0:3],a[64:67],a[0:3],v[96:99]\n"
"ds_read_b128 a[96:99],v114 offset:24576\n"
"ds_read_b128 a[100:103],v114 offset:25600\n"
"ds_read_b128 a[104:107],v114 offset:26624\n"
"v_mfma_f32_16x16x32_bf16 v[0:3],a[68:71],a[4:7],v[0:3]\n"
"ds_read_b128 a[108:111],v114 offset:27648\n"
"ds_read_b128 a[112:115],v114 offset:28672\n"
"ds_read_b128 a[116:119],v114 offset:29696\n"
"v_mfma_f32_16x16x32_bf16 v[0:3],a[72:75],a[8:11],v[0:3]\n"
"ds_read_b128 a[120:123],v114 offset:30720\n"
"ds_read_b128 a[124:127],v114 offset:31744\n"
"v_add_f32 v16,0x80000000,v126\n"
"v_mfma_f32_16x16x32_bf16 v[0:3],a[76:79],a[12:15],v[0:3]\n"
"v_min_f32 v16,v16,v104\n"
"v_add_f32 v17,0xf149f2ca,v126\n"
"v_min_f32 v17,v17,v105\n"
"v_mfma_f32_16x16x32_bf16 v[4:7],a[92:95],a[12:15],v[96:99]\n"
"v_add_f32 v18,0xf1c9f2ca,v126\n"
"v_min_f32 v18,v18,v106\n"
"v_add_f32 v19,0xf2177617,v126\n"
"v_mfma_f32_16x16x32_bf16 v[4:7],a[88:91],a[8:11],v[4:7]\n"
"v_min_f32 v19,v19,v107\n"
"v_add_f32 v20,0xf349f2ca,v126\n"
"v_min_f32 v20,v20,v104\n"
"v_mfma_f32_16x16x32_bf16 v[4:7],a[84:87],a[4:7],v[4:7]\n"
"v_add_f32 v21,0xf35691f6,v126\n"
"v_min_f32 v21,v21,v105\n"
"v_add_f32 v22,0xf3633123,v126\n"
"v_mfma_f32_16x16x32_bf16 v[4:7],a[80:83],a[0:3],v[4:7]\n"
"v_min_f32 v22,v22,v106\n"
"v_add_f32 v23,0xf36fd050,v126\n"
"v_min_f32 v23,v23,v107\n"
"v_mfma_f32_16x16x32_bf16 v[12:15],a[80:83],a[16:19],v[100:103]\n"
"v_exp_f32 v32,v32\n"
"v_max3_f32 v118,v0,v1,v2\n"
"v_mfma_f32_16x16x32_bf16 v[12:15],a[84:87],a[20:23],v[12:15]\n"
"v_exp_f32 v33,v33\n"
"v_mfma_f32_16x16x32_bf16 v[12:15],a[88:91],a[24:27],v[12:15]\n"
"v_exp_f32 v34,v34\n"
"v_cvt_pk_bf16_f32 v80,v32,v33\n"
"v_mfma_f32_16x16x32_bf16 v[12:15],a[92:95],a[28:31],v[12:15]\n"
"v_exp_f32 v35,v35\n"
"v_max3_f32 v118,v118,v3,v4\n"
"v_mfma_f32_16x16x32_bf16 v[8:11],a[76:79],a[28:31],v[100:103]\n"
"v_exp_f32 v36,v36\n"
"v_cvt_pk_bf16_f32 v81,v34,v35\n"
"v_mfma_f32_16x16x32_bf16 v[8:11],a[72:75],a[24:27],v[8:11]\n"
"v_exp_f32 v37,v37\n"
"v_max3_f32 v118,v118,v5,v6\n"
"v_mfma_f32_16x16x32_bf16 v[8:11],a[68:71],a[20:23],v[8:11]\n"
"v_exp_f32 v38,v38\n"
"v_mfma_f32_16x16x32_bf16 v[8:11],a[64:67],a[16:19],v[8:11]\n"
"v_exp_f32 v39,v39\n"
"v_max3_f32 v118,v118,v7,v12\n"
"v_mfma_f32_16x16x32_bf16 v[24:27],a[64:67],a[48:51],v[108:111]\n"
"v_exp_f32 v40,v40\n"
"v_mfma_f32_16x16x32_bf16 v[24:27],a[68:71],a[52:55],v[24:27]\n"
"v_exp_f32 v41,v41\n"
"v_max3_f32 v118,v118,v13,v14\n"
"v_mfma_f32_16x16x32_bf16 v[24:27],a[72:75],a[56:59],v[24:27]\n"
"v_cvt_pk_bf16_f32 v82,v36,v37\n"
"v_cvt_pk_bf16_f32 v84,v40,v41\n"
"v_mfma_f32_16x16x32_bf16 v[24:27],a[76:79],a[60:63],v[24:27]\n"
"v_exp_f32 v42,v42\n"
"v_max3_f32 v118,v118,v15,v8\n"
"v_mfma_f32_16x16x32_bf16 v[28:31],a[92:95],a[60:63],v[108:111]\n"
"v_cvt_pk_bf16_f32 v83,v38,v39\n"
"v_max3_f32 v118,v118,v9,v10\n"
"v_mfma_f32_16x16x32_bf16 v[28:31],a[88:91],a[56:59],v[28:31]\n"
"v_exp_f32 v43,v43\n"
"v_mfma_f32_16x16x32_bf16 v[28:31],a[84:87],a[52:55],v[28:31]\n"
"v_cvt_pk_bf16_f32 v85,v42,v43\n"
"v_mfma_f32_16x16x32_bf16 v[28:31],a[80:83],a[48:51],v[28:31]\n"
"v_exp_f32 v44,v44\n"
"v_mfma_f32_16x16x32_bf16 v[20:23],a[80:83],a[32:35],v[20:23]\n"
"v_max3_f32 v118,v118,v11,v24\n"
"v_max3_f32 v118,v118,v25,v26\n"
"v_mfma_f32_16x16x32_bf16 v[20:23],a[84:87],a[36:39],v[20:23]\n"
"v_exp_f32 v45,v45\n"
"v_mfma_f32_16x16x32_bf16 v[20:23],a[88:91],a[40:43],v[20:23]\n"
"v_cvt_pk_bf16_f32 v86,v44,v45\n"
"v_mfma_f32_16x16x32_bf16 v[20:23],a[92:95],a[44:47],v[20:23]\n"
"v_exp_f32 v46,v46\n"
"v_mfma_f32_16x16x32_bf16 v[16:19],a[76:79],a[44:47],v[16:19]\n"
"v_max3_f32 v118,v118,v27,v28\n"
"v_max3_f32 v118,v118,v29,v30\n"
"v_mfma_f32_16x16x32_bf16 v[16:19],a[72:75],a[40:43],v[16:19]\n"
"v_exp_f32 v47,v47\n"
"v_mfma_f32_16x16x32_bf16 v[16:19],a[68:71],a[36:39],v[16:19]\n"
"v_cvt_pk_bf16_f32 v87,v46,v47\n"
"v_exp_f32 v48,v48\n"
"v_mfma_f32_16x16x32_bf16 v[16:19],a[64:67],a[32:35],v[16:19]\n"
"v_max3_f32 v118,v118,v31,v20\n"
"s_waitcnt lgkmcnt(0)\n"
"v_mfma_f32_16x16x32_bf16 v[128:131],a[96:99],v[64:67],v[128:131]\n"
"v_exp_f32 v49,v49\n"
"v_max3_f32 v118,v118,v21,v22\n"
"v_cvt_pk_bf16_f32 v88,v48,v49\n"
"v_mfma_f32_16x16x32_bf16 v[160:163],a[96:99],v[68:71],v[160:163]\n"
"ds_read_b128 a[64:67],v114 offset:57344\n"
"ds_read_b128 a[68:71],v114 offset:58368\n"
"v_mfma_f32_16x16x32_bf16 v[192:195],a[96:99],v[72:75],v[192:195]\n"
"ds_read_b128 a[72:75],v114 offset:59392\n"
"v_exp_f32 v50,v50\n"
"v_mfma_f32_16x16x32_bf16 v[224:227],a[96:99],v[76:79],v[224:227]\n"
"v_max3_f32 v118,v118,v23,v16\n"
"v_max3_f32 v118,v118,v17,v18\n"
"v_max_f32 v118,v118,v19\n"
"v_mfma_f32_16x16x32_bf16 a[140:143],a[144:147],v[76:79],a[140:143]\n"
"v_exp_f32 v51,v51\n"
"v_cmp_lt_f32 vcc,s93,v118\n"
"v_cvt_pk_bf16_f32 v89,v50,v51\n"
"v_mfma_f32_16x16x32_bf16 v[228:231],a[100:103],v[76:79],v[228:231]\n"
"ds_read_b128 a[76:79],v114 offset:60416\n"
"ds_read_b128 a[80:83],v114 offset:61440\n"
"v_mfma_f32_16x16x32_bf16 v[132:135],a[100:103],v[64:67],v[132:135]\n"
"ds_read_b128 a[84:87],v114 offset:62464\n"
"v_exp_f32 v52,v52\n"
"v_mfma_f32_16x16x32_bf16 v[164:167],a[100:103],v[68:71],v[164:167]\n"
"s_sub_u32 s82,s3,32768\n"
"ds_read_b128 a[88:91],v114 offset:63488\n"
"ds_read_b128 a[92:95],v114 offset:64512\n"
"v_mfma_f32_16x16x32_bf16 v[196:199],a[100:103],v[72:75],v[196:199]\n"
"buffer_load_dwordx4 a[0:3],v112,s[24:27],s82 offen offset:0 sc1\n"
"v_exp_f32 v53,v53\n"
"v_mfma_f32_16x16x32_bf16 a[136:139],a[144:147],v[72:75],a[136:139]\n"
"buffer_load_dwordx4 a[4:7],v112,s[24:27],s82 offen offset:1024 sc1\n"
"buffer_load_dwordx4 a[8:11],v112,s[24:27],s82 offen offset:2048 sc1\n"
"v_cvt_pk_bf16_f32 v90,v52,v53\n"
"v_exp_f32 v54,v54\n"
"v_mfma_f32_16x16x32_bf16 v[200:203],a[104:107],v[72:75],v[200:203]\n"
"buffer_load_dwordx4 a[12:15],v112,s[24:27],s82 offen offset:3072 sc1\n"
"v_mfma_f32_16x16x32_bf16 v[232:235],a[104:107],v[76:79],v[232:235]\n"
"s_sub_u32 s82,s3,49152\n"
"buffer_load_dwordx4 a[16:19],v112,s[24:27],s82 offen offset:0 sc1\n"
"buffer_load_dwordx4 a[20:23],v112,s[24:27],s82 offen offset:1024 sc1\n"
"v_exp_f32 v55,v55\n"
"v_mfma_f32_16x16x32_bf16 v[136:139],a[104:107],v[64:67],v[136:139]\n"
"buffer_load_dwordx4 a[24:27],v112,s[24:27],s82 offen offset:2048 sc1\n"
"v_mfma_f32_16x16x32_bf16 v[168:171],a[104:107],v[68:71],v[168:171]\n"
"buffer_load_dwordx4 a[28:31],v112,s[24:27],s82 offen offset:3072 sc1\n"
"v_mfma_f32_16x16x32_bf16 a[132:135],a[144:147],v[68:71],a[132:135]\n"
"v_exp_f32 v56,v56\n"
"v_mfma_f32_16x16x32_bf16 v[172:175],a[108:111],v[68:71],v[172:175]\n"
"v_cvt_pk_bf16_f32 v91,v54,v55\n"
"v_mfma_f32_16x16x32_bf16 v[204:207],a[108:111],v[72:75],v[204:207]\n"
"v_exp_f32 v57,v57\n"
"v_mfma_f32_16x16x32_bf16 v[236:239],a[108:111],v[76:79],v[236:239]\n"
"v_cvt_pk_bf16_f32 v92,v56,v57\n"
"v_mfma_f32_16x16x32_bf16 v[140:143],a[108:111],v[64:67],v[140:143]\n"
"v_exp_f32 v58,v58\n"
"v_mfma_f32_16x16x32_bf16 a[128:131],a[144:147],v[64:67],a[128:131]\n"
"v_mfma_f32_16x16x32_bf16 v[144:147],a[112:115],v[64:67],v[144:147]\n"
"v_exp_f32 v59,v59\n"
"v_mfma_f32_16x16x32_bf16 v[176:179],a[112:115],v[68:71],v[176:179]\n"
"v_cvt_pk_bf16_f32 v93,v58,v59\n"
"v_mfma_f32_16x16x32_bf16 v[208:211],a[112:115],v[72:75],v[208:211]\n"
"v_exp_f32 v60,v60\n"
"v_mfma_f32_16x16x32_bf16 v[240:243],a[112:115],v[76:79],v[240:243]\n"
"v_mfma_f32_16x16x32_bf16 v[244:247],a[116:119],v[76:79],v[244:247]\n"
"v_exp_f32 v61,v61\n"
"v_mfma_f32_16x16x32_bf16 v[148:151],a[116:119],v[64:67],v[148:151]\n"
"v_cvt_pk_bf16_f32 v94,v60,v61\n"
"v_mfma_f32_16x16x32_bf16 v[180:183],a[116:119],v[68:71],v[180:183]\n"
"v_exp_f32 v62,v62\n"
"v_mfma_f32_16x16x32_bf16 v[212:215],a[116:119],v[72:75],v[212:215]\n"
"v_mfma_f32_16x16x32_bf16 v[216:219],a[120:123],v[72:75],v[216:219]\n"
"v_exp_f32 v63,v63\n"
"v_mfma_f32_16x16x32_bf16 v[248:251],a[120:123],v[76:79],v[248:251]\n"
"v_cvt_pk_bf16_f32 v95,v62,v63\n"
"v_mfma_f32_16x16x32_bf16 v[152:155],a[120:123],v[64:67],v[152:155]\n"
"v_mfma_f32_16x16x32_bf16 v[184:187],a[120:123],v[68:71],v[184:187]\n"
"v_mfma_f32_16x16x32_bf16 v[188:191],a[124:127],v[68:71],v[188:191]\n"
"v_mfma_f32_16x16x32_bf16 v[220:223],a[124:127],v[72:75],v[220:223]\n"
"v_mfma_f32_16x16x32_bf16 v[252:255],a[124:127],v[76:79],v[252:255]\n"
"v_mfma_f32_16x16x32_bf16 v[156:159],a[124:127],v[64:67],v[156:159]\n"
"s_cbranch_vccnz .Lfk_cs_b3\n"
".Lfk_cr_b3:\n"
"s_waitcnt vmcnt(9) lgkmcnt(0)\n"
"s_barrier\n"
"v_mfma_f32_16x16x32_bf16 v[128:131],a[64:67],v[80:83],v[128:131]\n"
"v_exp_f32 v0,v0\n"
"v_mfma_f32_16x16x32_bf16 v[160:163],a[64:67],v[84:87],v[160:163]\n"
"v_exp_f32 v1,v1\n"
"v_mfma_f32_16x16x32_bf16 v[192:195],a[64:67],v[88:91],v[192:195]\n"
"v_exp_f32 v2,v2\n"
"v_cvt_pk_bf16_f32 v64,v0,v1\n"
"v_mfma_f32_16x16x32_bf16 v[224:227],a[64:67],v[92:95],v[224:227]\n"
"v_exp_f32 v3,v3\n"
"v_mfma_f32_16x16x32_bf16 a[140:143],a[144:147],v[92:95],a[140:143]\n"
"v_exp_f32 v4,v4\n"
"v_cvt_pk_bf16_f32 v65,v2,v3\n"
"v_mfma_f32_16x16x32_bf16 v[228:231],a[68:71],v[92:95],v[228:231]\n"
"v_exp_f32 v5,v5\n"
"v_mfma_f32_16x16x32_bf16 v[132:135],a[68:71],v[80:83],v[132:135]\n"
"v_exp_f32 v6,v6\n"
"v_cvt_pk_bf16_f32 v66,v4,v5\n"
"v_exp_f32 v7,v7\n"
"v_mfma_f32_16x16x32_bf16 v[164:167],a[68:71],v[84:87],v[164:167]\n"
"ds_read_b128 a[96:99],v114 offset:40960\n"
"ds_read_b128 a[100:103],v114 offset:41984\n"
"v_exp_f32 v8,v8\n"
"v_mfma_f32_16x16x32_bf16 v[196:199],a[68:71],v[88:91],v[196:199]\n"
"ds_read_b128 a[104:107],v114 offset:43008\n"
"ds_read_b128 a[108:111],v114 offset:44032\n"
"v_exp_f32 v9,v9\n"
"v_mfma_f32_16x16x32_bf16 a[136:139],a[144:147],v[88:91],a[136:139]\n"
"ds_read_b128 a[112:115],v114 offset:45056\n"
"ds_read_b128 a[116:119],v114 offset:46080\n"
"v_exp_f32 v10,v10\n"
"v_mfma_f32_16x16x32_bf16 v[200:203],a[72:75],v[88:91],v[200:203]\n"
"ds_read_b128 a[120:123],v114 offset:47104\n"
"ds_read_b128 a[124:127],v114 offset:48128\n"
"v_mfma_f32_16x16x32_bf16 v[232:235],a[72:75],v[92:95],v[232:235]\n"
"v_exp_f32 v11,v11\n"
"v_accvgpr_read_b32 v119,a150\n"
"v_accvgpr_read_b32 v120,a151\n"
"v_mfma_f32_16x16x32_bf16 v[136:139],a[72:75],v[80:83],v[136:139]\n"
"v_exp_f32 v12,v12\n"
"v_xor_b32 v119,s80,v119\n"
"v_xor_b32 v120,s81,v120\n"
"v_mfma_f32_16x16x32_bf16 v[168:171],a[72:75],v[84:87],v[168:171]\n"
"v_exp_f32 v13,v13\n"
"v_or_b32 v119,v119,v120\n"
"v_cmp_ne_u32 s[14:15],0,v119\n"
"v_mfma_f32_16x16x32_bf16 a[132:135],a[144:147],v[84:87],a[132:135]\n"
"s_add_u32 s18,s8,1032192\n"
"v_exp_f32 v14,v14\n"
"s_cmp_lg_u32 s14,0\n"
"s_cselect_b32 s18,2146435072,s18\n"
"v_mfma_f32_16x16x32_bf16 v[172:175],a[76:79],v[84:87],v[172:175]\n"
"s_add_u32 m0,s30,0\n"
"v_exp_f32 v15,v15\n"
"buffer_load_dwordx4 v112,s[24:27],s18 offen offset:0 sc1 lds\n"
"buffer_load_dwordx4 v112,s[24:27],s18 offen offset:1024 sc1 lds\n"
"v_exp_f32 v16,v16\n"
"v_mfma_f32_16x16x32_bf16 v[204:207],a[76:79],v[88:91],v[204:207]\n"
"buffer_load_dwordx4 v112,s[24:27],s18 offen offset:2048 sc1 lds\n"
"buffer_load_dwordx4 v112,s[24:27],s18 offen offset:3072 sc1 lds\n"
"v_mfma_f32_16x16x32_bf16 v[236:239],a[76:79],v[92:95],v[236:239]\n"
"s_add_u32 m0,m0,4096\n"
"v_exp_f32 v17,v17\n"
"v_cvt_pk_bf16_f32 v67,v6,v7\n"
"v_mfma_f32_16x16x32_bf16 v[140:143],a[76:79],v[80:83],v[140:143]\n"
"buffer_load_dwordx4 v117,s[24:27],s18 offen offset:0 sc1 lds\n"
"v_exp_f32 v18,v18\n"
"v_cvt_pk_bf16_f32 v68,v8,v9\n"
"v_cvt_pk_bf16_f32 v69,v10,v11\n"
"v_exp_f32 v19,v19\n"
"v_mfma_f32_16x16x32_bf16 a[128:131],a[144:147],v[80:83],a[128:131]\n"
"buffer_load_dwordx4 v117,s[24:27],s18 offen offset:1024 sc1 lds\n"
"v_cvt_pk_bf16_f32 v70,v12,v13\n"
"v_mfma_f32_16x16x32_bf16 v[144:147],a[80:83],v[80:83],v[144:147]\n"
"v_exp_f32 v20,v20\n"
"v_cvt_pk_bf16_f32 v71,v14,v15\n"
"v_cvt_pk_bf16_f32 v72,v16,v17\n"
"v_exp_f32 v21,v21\n"
"v_mfma_f32_16x16x32_bf16 v[176:179],a[80:83],v[84:87],v[176:179]\n"
"buffer_load_dwordx4 v117,s[24:27],s18 offen offset:2048 sc1 lds\n"
"v_cvt_pk_bf16_f32 v73,v18,v19\n"
"v_mfma_f32_16x16x32_bf16 v[208:211],a[80:83],v[88:91],v[208:211]\n"
"v_exp_f32 v22,v22\n"
"v_cvt_pk_bf16_f32 v74,v20,v21\n"
"v_exp_f32 v23,v23\n"
"v_mfma_f32_16x16x32_bf16 v[240:243],a[80:83],v[92:95],v[240:243]\n"
"buffer_load_dwordx4 v117,s[24:27],s18 offen offset:3072 sc1 lds\n"
"v_mfma_f32_16x16x32_bf16 v[244:247],a[84:87],v[92:95],v[244:247]\n"
"s_sub_u32 s82,s3,65536\n"
"v_exp_f32 v24,v24\n"
"v_cvt_pk_bf16_f32 v75,v22,v23\n"
"v_exp_f32 v25,v25\n"
"v_mfma_f32_16x16x32_bf16 v[148:151],a[84:87],v[80:83],v[148:151]\n"
"buffer_load_dwordx4 v[32:35],v112,s[24:27],s82 offen offset:0 sc1\n"
"v_mfma_f32_16x16x32_bf16 v[180:183],a[84:87],v[84:87],v[180:183]\n"
"v_exp_f32 v26,v26\n"
"v_cvt_pk_bf16_f32 v76,v24,v25\n"
"v_exp_f32 v27,v27\n"
"v_mfma_f32_16x16x32_bf16 v[212:215],a[84:87],v[88:91],v[212:215]\n"
"buffer_load_dwordx4 v[36:39],v112,s[24:27],s82 offen offset:1024 sc1\n"
"v_mfma_f32_16x16x32_bf16 v[216:219],a[88:91],v[88:91],v[216:219]\n"
"v_exp_f32 v28,v28\n"
"v_exp_f32 v29,v29\n"
"v_mfma_f32_16x16x32_bf16 v[248:251],a[88:91],v[92:95],v[248:251]\n"
"buffer_load_dwordx4 v[40:43],v112,s[24:27],s82 offen offset:2048 sc1\n"
"v_mfma_f32_16x16x32_bf16 v[152:155],a[88:91],v[80:83],v[152:155]\n"
"v_exp_f32 v30,v30\n"
"v_mfma_f32_16x16x32_bf16 v[184:187],a[88:91],v[84:87],v[184:187]\n"
"v_exp_f32 v31,v31\n"
"v_mfma_f32_16x16x32_bf16 v[188:191],a[92:95],v[84:87],v[188:191]\n"
"buffer_load_dwordx4 v[44:47],v112,s[24:27],s82 offen offset:3072 sc1\n"
"v_mfma_f32_16x16x32_bf16 v[220:223],a[92:95],v[88:91],v[220:223]\n"
"v_cvt_pk_bf16_f32 v78,v28,v29\n"
"v_cvt_pk_bf16_f32 v77,v26,v27\n"
"v_mfma_f32_16x16x32_bf16 v[252:255],a[92:95],v[92:95],v[252:255]\n"
"v_cvt_pk_bf16_f32 v79,v30,v31\n"
"v_mfma_f32_16x16x32_bf16 v[156:159],a[92:95],v[80:83],v[156:159]\n"
"s_load_dwordx2 s[36:37],s[0:1],0\n"
"s_load_dwordx2 s[44:45],s[0:1],80\n"
"s_load_dwordx2 s[48:49],s[0:1],88\n"
"s_lshl_b32 s82,s78,2\n"
"s_add_u32 s82,s82,65536\n"
"v_add_u32 v113,s82,v112\n"
"s_lshr_b32 s82,s78,2\n"
"s_add_u32 s82,s82,122880\n"
"v_lshrrev_b32 v115,1,v112\n"
"v_add_u32 v116,s82,v115\n"
"s_waitcnt lgkmcnt(0)\n"
"s_barrier\n"
"v_mfma_f32_16x16x32_bf16 v[128:131],a[96:99],v[64:67],v[128:131]\n"
"s_add_u32 s83,s18,2097152\n"
"s_add_u32 m0,s30,32768\n"
"v_mfma_f32_16x16x32_bf16 a[128:131],a[144:147],v[64:67],a[128:131]\n"
"buffer_load_dwordx4 v112,s[24:27],s83 offen offset:0 sc1 lds\n"
"v_mfma_f32_16x16x32_bf16 v[132:135],a[100:103],v[64:67],v[132:135]\n"
"buffer_load_dwordx4 v112,s[24:27],s83 offen offset:1024 sc1 lds\n"
"v_mfma_f32_16x16x32_bf16 v[136:139],a[104:107],v[64:67],v[136:139]\n"
"buffer_load_dwordx4 v112,s[24:27],s83 offen offset:2048 sc1 lds\n"
"v_mfma_f32_16x16x32_bf16 v[140:143],a[108:111],v[64:67],v[140:143]\n"
"v_mfma_f32_16x16x32_bf16 v[144:147],a[112:115],v[64:67],v[144:147]\n"
"buffer_load_dwordx4 v112,s[24:27],s83 offen offset:3072 sc1 lds\n"
"v_mfma_f32_16x16x32_bf16 v[148:151],a[116:119],v[64:67],v[148:151]\n"
"s_add_u32 m0,m0,4096\n"
"v_mfma_f32_16x16x32_bf16 v[152:155],a[120:123],v[64:67],v[152:155]\n"
"v_mfma_f32_16x16x32_bf16 v[156:159],a[124:127],v[64:67],v[156:159]\n"
"buffer_load_dwordx4 v117,s[24:27],s83 offen offset:0 sc1 lds\n"
"v_mfma_f32_16x16x32_bf16 v[160:163],a[96:99],v[68:71],v[160:163]\n"
"v_accvgpr_read_b32 v97,a128\n"
"v_cvt_pk_bf16_f32 v136,v136,v137\n"
"v_mfma_f32_16x16x32_bf16 a[132:135],a[144:147],v[68:71],a[132:135]\n"
"buffer_load_dwordx4 v117,s[24:27],s83 offen offset:1024 sc1 lds\n"
"v_mfma_f32_16x16x32_bf16 v[164:167],a[100:103],v[68:71],v[164:167]\n"
"v_cvt_pk_bf16_f32 v128,v128,v129\n"
"v_cvt_pk_bf16_f32 v129,v130,v131\n"
"v_mfma_f32_16x16x32_bf16 v[168:171],a[104:107],v[68:71],v[168:171]\n"
"buffer_load_dwordx4 v117,s[24:27],s83 offen offset:2048 sc1 lds\n"
"v_cvt_pk_bf16_f32 v130,v132,v133\n"
"v_cvt_pk_bf16_f32 v131,v134,v135\n"
"v_mfma_f32_16x16x32_bf16 v[172:175],a[108:111],v[68:71],v[172:175]\n"
"ds_write_b128 v113,v[128:131] offset:0\n"
"v_cvt_pk_bf16_f32 v137,v138,v139\n"
"v_mfma_f32_16x16x32_bf16 v[176:179],a[112:115],v[68:71],v[176:179]\n"
"buffer_load_dwordx4 v117,s[24:27],s83 offen offset:3072 sc1 lds\n"
"v_cvt_pk_bf16_f32 v138,v140,v141\n"
"v_cvt_pk_bf16_f32 v139,v142,v143\n"
"v_mfma_f32_16x16x32_bf16 v[180:183],a[116:119],v[68:71],v[180:183]\n"
"s_sub_u32 s82,s3,81920\n"
"ds_write_b128 v113,v[136:139] offset:1024\n"
"v_cvt_pk_bf16_f32 v144,v144,v145\n"
"v_mfma_f32_16x16x32_bf16 v[184:187],a[120:123],v[68:71],v[184:187]\n"
"buffer_load_dwordx4 a[32:35],v112,s[24:27],s82 offen offset:0 sc1\n"
"v_cvt_pk_bf16_f32 v145,v146,v147\n"
"v_cvt_pk_bf16_f32 v146,v148,v149\n"
"v_cvt_pk_bf16_f32 v147,v150,v151\n"
"v_mfma_f32_16x16x32_bf16 v[188:191],a[124:127],v[68:71],v[188:191]\n"
"ds_write_b128 v113,v[144:147] offset:2048\n"
"v_cvt_pk_bf16_f32 v152,v152,v153\n"
"v_mfma_f32_16x16x32_bf16 v[192:195],a[96:99],v[72:75],v[192:195]\n"
"buffer_load_dwordx4 a[36:39],v112,s[24:27],s82 offen offset:1024 sc1\n"
"v_cvt_pk_bf16_f32 v153,v154,v155\n"
"v_cvt_pk_bf16_f32 v154,v156,v157\n"
"v_cvt_pk_bf16_f32 v155,v158,v159\n"
"v_mfma_f32_16x16x32_bf16 a[136:139],a[144:147],v[72:75],a[136:139]\n"
"ds_write_b128 v113,v[152:155] offset:3072\n"
"ds_write_b64 v116,v[96:97] offset:0\n"
"v_mfma_f32_16x16x32_bf16 v[196:199],a[100:103],v[72:75],v[196:199]\n"
"buffer_load_dwordx4 a[40:43],v112,s[24:27],s82 offen offset:2048 sc1\n"
"v_accvgpr_read_b32 v101,a132\n"
"v_cvt_pk_bf16_f32 v160,v160,v161\n"
"v_mfma_f32_16x16x32_bf16 v[200:203],a[104:107],v[72:75],v[200:203]\n"
"v_cvt_pk_bf16_f32 v161,v162,v163\n"
"v_cvt_pk_bf16_f32 v162,v164,v165\n"
"v_cvt_pk_bf16_f32 v163,v166,v167\n"
"v_mfma_f32_16x16x32_bf16 v[204:207],a[108:111],v[72:75],v[204:207]\n"
"ds_write_b128 v113,v[160:163] offset:4096\n"
"v_cvt_pk_bf16_f32 v168,v168,v169\n"
"v_cvt_pk_bf16_f32 v169,v170,v171\n"
"v_mfma_f32_16x16x32_bf16 v[208:211],a[112:115],v[72:75],v[208:211]\n"
"buffer_load_dwordx4 a[44:47],v112,s[24:27],s82 offen offset:3072 sc1\n"
"v_cvt_pk_bf16_f32 v170,v172,v173\n"
"v_cvt_pk_bf16_f32 v171,v174,v175\n"
"v_mfma_f32_16x16x32_bf16 v[212:215],a[116:119],v[72:75],v[212:215]\n"
"ds_write_b128 v113,v[168:171] offset:5120\n"
"v_cvt_pk_bf16_f32 v176,v176,v177\n"
"v_cvt_pk_bf16_f32 v177,v178,v179\n"
"v_mfma_f32_16x16x32_bf16 v[216:219],a[120:123],v[72:75],v[216:219]\n"
"v_cvt_pk_bf16_f32 v178,v180,v181\n"
"v_cvt_pk_bf16_f32 v179,v182,v183\n"
"v_mfma_f32_16x16x32_bf16 v[220:223],a[124:127],v[72:75],v[220:223]\n"
"ds_write_b128 v113,v[176:179] offset:6144\n"
"v_cvt_pk_bf16_f32 v184,v184,v185\n"
"v_cvt_pk_bf16_f32 v185,v186,v187\n"
"v_cvt_pk_bf16_f32 v186,v188,v189\n"
"v_cvt_pk_bf16_f32 v187,v190,v191\n"
"v_mfma_f32_16x16x32_bf16 v[224:227],a[96:99],v[76:79],v[224:227]\n"
"ds_write_b128 v113,v[184:187] offset:7168\n"
"v_mfma_f32_16x16x32_bf16 a[140:143],a[144:147],v[76:79],a[140:143]\n"
"s_sub_u32 s82,s3,131072\n"
"ds_write_b64 v116,v[100:101] offset:512\n"
"buffer_load_dwordx4 v[128:131],v112,s[24:27],s82 offen offset:0 sc1\n"
"buffer_load_dwordx4 v[132:135],v112,s[24:27],s82 offen offset:1024 sc1\n"
"v_mfma_f32_16x16x32_bf16 v[228:231],a[100:103],v[76:79],v[228:231]\n"
"buffer_load_dwordx4 v[136:139],v112,s[24:27],s82 offen offset:2048 sc1\n"
"buffer_load_dwordx4 v[140:143],v112,s[24:27],s82 offen offset:3072 sc1\n"
"s_sub_u32 s82,s3,147456\n"
"v_mfma_f32_16x16x32_bf16 v[232:235],a[104:107],v[76:79],v[232:235]\n"
"buffer_load_dwordx4 v[144:147],v112,s[24:27],s82 offen offset:0 sc1\n"
"buffer_load_dwordx4 v[148:151],v112,s[24:27],s82 offen offset:1024 sc1\n"
"buffer_load_dwordx4 v[152:155],v112,s[24:27],s82 offen offset:2048 sc1\n"
"v_mfma_f32_16x16x32_bf16 v[236:239],a[108:111],v[76:79],v[236:239]\n"
"buffer_load_dwordx4 v[156:159],v112,s[24:27],s82 offen offset:3072 sc1\n"
"v_mfma_f32_16x16x32_bf16 v[240:243],a[112:115],v[76:79],v[240:243]\n"
"v_mfma_f32_16x16x32_bf16 v[244:247],a[116:119],v[76:79],v[244:247]\n"
"v_mfma_f32_16x16x32_bf16 v[248:251],a[120:123],v[76:79],v[248:251]\n"
"v_mfma_f32_16x16x32_bf16 v[252:255],a[124:127],v[76:79],v[252:255]\n"
".Lfk_endB:\n"
"v_lshrrev_b32 v70,4,v112\n"
"v_and_b32 v71,15,v70\n"
"v_lshrrev_b32 v70,4,v70\n"
"v_lshlrev_b32 v70,2,v70\n"
"v_sub_u32 v70,v71,v70\n"
"v_mov_b32 v71,0x3f80\n"
"v_cmp_eq_u32 vcc,0,v70\n"
"v_mov_b32 v68,0\n"
"v_mov_b32 v64,0\n"
"v_cndmask_b32 v66,0,v71,vcc\n"
"v_cmp_eq_u32 vcc,1,v70\n"
"v_mov_b32 v65,0\n"
"v_mov_b32 v72,0x3f800000\n"
"v_cndmask_b32 v66,v66,v72,vcc\n"
"v_cmp_eq_u32 vcc,2,v70\n"
"v_mov_b32 v69,0\n"
"s_nop 0\n"
"v_cndmask_b32 v67,0,v71,vcc\n"
"v_cmp_eq_u32 vcc,3,v70\n"
"s_nop 1\n"
"v_cndmask_b32 v67,v67,v72,vcc\n"
"s_sub_u32 s82,s3,163840\n"
"buffer_load_dwordx4 v[0:3],v112,s[24:27],s82 offen offset:0 sc1\n"
"buffer_load_dwordx4 v[4:7],v112,s[24:27],s82 offen offset:1024 sc1\n"
"buffer_load_dwordx4 v[8:11],v112,s[24:27],s82 offen offset:2048 sc1\n"
"buffer_load_dwordx4 v[12:15],v112,s[24:27],s82 offen offset:3072 sc1\n"
"s_sub_u32 s82,s3,180224\n"
"buffer_load_dwordx4 v[160:163],v112,s[24:27],s82 offen offset:0 sc1\n"
"buffer_load_dwordx4 v[164:167],v112,s[24:27],s82 offen offset:1024 sc1\n"
"buffer_load_dwordx4 v[168:171],v112,s[24:27],s82 offen offset:2048 sc1\n"
"buffer_load_dwordx4 v[172:175],v112,s[24:27],s82 offen offset:3072 sc1\n"
"s_sub_u32 s82,s3,196608\n"
"buffer_load_dwordx4 v[176:179],v112,s[24:27],s82 offen offset:0 sc1\n"
"buffer_load_dwordx4 v[180:183],v112,s[24:27],s82 offen offset:1024 sc1\n"
"buffer_load_dwordx4 v[184:187],v112,s[24:27],s82 offen offset:2048 sc1\n"
"buffer_load_dwordx4 v[188:191],v112,s[24:27],s82 offen offset:3072 sc1\n"
"s_waitcnt lgkmcnt(0)\n"
"s_barrier\n"
"v_xor_b32 v113,32768,v113\n"
"v_xor_b32 v116,2048,v116\n"
"ds_read_b64 v[98:99],v116 offset:0\n"
"ds_read_b128 a[64:67],v113 offset:0\n"
"ds_read_b128 a[68:71],v113 offset:1024\n"
"ds_read_b128 a[72:75],v113 offset:2048\n"
"ds_read_b128 a[76:79],v113 offset:3072\n"
"ds_read_b64 v[102:103],v116 offset:512\n"
"ds_read_b128 a[80:83],v113 offset:4096\n"
"ds_read_b128 a[84:87],v113 offset:5120\n"
"ds_read_b128 a[88:91],v113 offset:6144\n"
"ds_read_b128 a[92:95],v113 offset:7168\n"
"s_waitcnt lgkmcnt(5)\n"
"v_cmp_neq_f32 vcc,v98,v104\n"
"s_nop 0\n"
"s_cbranch_vccnz .Lfk_ms_b0\n"
".Lfk_mr_b0:\n"
"v_mfma_f32_16x16x32_bf16 v[192:195],v[66:69],a[64:67],v[192:195]\n"
"v_mfma_f32_16x16x32_bf16 v[196:199],v[64:67],a[64:67],v[196:199]\n"
"v_mfma_f32_16x16x32_bf16 v[200:203],v[66:69],a[68:71],v[200:203]\n"
"v_mfma_f32_16x16x32_bf16 v[204:207],v[64:67],a[68:71],v[204:207]\n"
"v_mfma_f32_16x16x32_bf16 v[208:211],v[66:69],a[72:75],v[208:211]\n"
"v_mfma_f32_16x16x32_bf16 v[212:215],v[64:67],a[72:75],v[212:215]\n"
"v_mfma_f32_16x16x32_bf16 v[216:219],v[66:69],a[76:79],v[216:219]\n"
"v_mfma_f32_16x16x32_bf16 v[220:223],v[64:67],a[76:79],v[220:223]\n"
"s_waitcnt lgkmcnt(0)\n"
"v_cmp_neq_f32 vcc,v102,v108\n"
"s_nop 0\n"
"s_cbranch_vccnz .Lfk_ms_b1\n"
".Lfk_mr_b1:\n"
"v_mfma_f32_16x16x32_bf16 v[224:227],v[66:69],a[80:83],v[224:227]\n"
"v_mfma_f32_16x16x32_bf16 v[228:231],v[64:67],a[80:83],v[228:231]\n"
"v_accvgpr_read_b32 v122,a136\n"
"v_add_f32 v122,v122,v99\n"
"v_mfma_f32_16x16x32_bf16 v[232:235],v[66:69],a[84:87],v[232:235]\n"
"v_rcp_f32 v122,v122\n"
"v_mfma_f32_16x16x32_bf16 v[236:239],v[64:67],a[84:87],v[236:239]\n"
"v_pk_mul_f32 v[192:193],v[192:193],v[122:123] op_sel_hi:[1,0]\n"
"v_pk_mul_f32 v[194:195],v[194:195],v[122:123] op_sel_hi:[1,0]\n"
"v_pk_mul_f32 v[196:197],v[196:197],v[122:123] op_sel_hi:[1,0]\n"
"v_pk_mul_f32 v[198:199],v[198:199],v[122:123] op_sel_hi:[1,0]\n"
"v_mfma_f32_16x16x32_bf16 v[240:243],v[66:69],a[88:91],v[240:243]\n"
"v_cvt_pk_bf16_f32 v192,v192,v193\n"
"v_cvt_pk_bf16_f32 v193,v194,v195\n"
"v_mfma_f32_16x16x32_bf16 v[244:247],v[64:67],a[88:91],v[244:247]\n"
"v_cvt_pk_bf16_f32 v194,v196,v197\n"
"v_cvt_pk_bf16_f32 v195,v198,v199\n"
"v_mfma_f32_16x16x32_bf16 v[248:251],v[66:69],a[92:95],v[248:251]\n"
"buffer_store_dwordx4 v[192:195],v112,s[24:27],s70 offen offset:0 sc1\n"
"v_pk_mul_f32 v[200:201],v[200:201],v[122:123] op_sel_hi:[1,0]\n"
"v_pk_mul_f32 v[202:203],v[202:203],v[122:123] op_sel_hi:[1,0]\n"
"v_pk_mul_f32 v[204:205],v[204:205],v[122:123] op_sel_hi:[1,0]\n"
"v_pk_mul_f32 v[206:207],v[206:207],v[122:123] op_sel_hi:[1,0]\n"
"v_mfma_f32_16x16x32_bf16 v[252:255],v[64:67],a[92:95],v[252:255]\n"
"v_cvt_pk_bf16_f32 v200,v200,v201\n"
"v_cvt_pk_bf16_f32 v201,v202,v203\n"
"v_cvt_pk_bf16_f32 v202,v204,v205\n"
"v_cvt_pk_bf16_f32 v203,v206,v207\n"
"v_pk_mul_f32 v[208:209],v[208:209],v[122:123] op_sel_hi:[1,0]\n"
"v_pk_mul_f32 v[210:211],v[210:211],v[122:123] op_sel_hi:[1,0]\n"
"v_pk_mul_f32 v[212:213],v[212:213],v[122:123] op_sel_hi:[1,0]\n"
"v_pk_mul_f32 v[214:215],v[214:215],v[122:123] op_sel_hi:[1,0]\n"
"v_cvt_pk_bf16_f32 v208,v208,v209\n"
"v_cvt_pk_bf16_f32 v209,v210,v211\n"
"v_cvt_pk_bf16_f32 v210,v212,v213\n"
"v_cvt_pk_bf16_f32 v211,v214,v215\n"
"buffer_store_dwordx4 v[200:203],v112,s[24:27],s70 offen offset:1024 sc1\n"
"buffer_store_dwordx4 v[208:211],v112,s[24:27],s70 offen offset:2048 sc1\n"
"v_pk_mul_f32 v[216:217],v[216:217],v[122:123] op_sel_hi:[1,0]\n"
"v_pk_mul_f32 v[218:219],v[218:219],v[122:123] op_sel_hi:[1,0]\n"
"v_pk_mul_f32 v[220:221],v[220:221],v[122:123] op_sel_hi:[1,0]\n"
"v_pk_mul_f32 v[222:223],v[222:223],v[122:123] op_sel_hi:[1,0]\n"
"v_cvt_pk_bf16_f32 v216,v216,v217\n"
"v_cvt_pk_bf16_f32 v217,v218,v219\n"
"v_cvt_pk_bf16_f32 v218,v220,v221\n"
"v_cvt_pk_bf16_f32 v219,v222,v223\n"
"buffer_store_dwordx4 v[216:219],v112,s[24:27],s70 offen offset:3072 sc1\n"
"s_add_u32 s70,s70,262144\n"
"s_branch .Lfk_p1\n"
".Lfk_tailB7:\n"
"s_waitcnt vmcnt(11) lgkmcnt(0)\n"
"s_barrier\n"
"v_mfma_f32_16x16x32_bf16 v[0:3],a[64:67],a[0:3],v[96:99]\n"
"ds_read_b128 a[96:99],v114 offset:24576\n"
"ds_read_b128 a[100:103],v114 offset:25600\n"
"ds_read_b128 a[104:107],v114 offset:26624\n"
"v_mfma_f32_16x16x32_bf16 v[0:3],a[68:71],a[4:7],v[0:3]\n"
"ds_read_b128 a[108:111],v114 offset:27648\n"
"ds_read_b128 a[112:115],v114 offset:28672\n"
"ds_read_b128 a[116:119],v114 offset:29696\n"
"v_mfma_f32_16x16x32_bf16 v[0:3],a[72:75],a[8:11],v[0:3]\n"
"ds_read_b128 a[120:123],v114 offset:30720\n"
"ds_read_b128 a[124:127],v114 offset:31744\n"
"v_add_f32 v16,0x80000000,v126\n"
"v_mfma_f32_16x16x32_bf16 v[0:3],a[76:79],a[12:15],v[0:3]\n"
"v_min_f32 v16,v16,v104\n"
"v_add_f32 v17,0xf149f2ca,v126\n"
"v_min_f32 v17,v17,v105\n"
"v_mfma_f32_16x16x32_bf16 v[4:7],a[92:95],a[12:15],v[96:99]\n"
"v_add_f32 v18,0xf1c9f2ca,v126\n"
"v_min_f32 v18,v18,v106\n"
"v_add_f32 v19,0xf2177617,v126\n"
"v_mfma_f32_16x16x32_bf16 v[4:7],a[88:91],a[8:11],v[4:7]\n"
"v_min_f32 v19,v19,v107\n"
"v_add_f32 v20,0xf349f2ca,v126\n"
"v_mfma_f32_16x16x32_bf16 v[4:7],a[84:87],a[4:7],v[4:7]\n"
"v_add_f32 v21,0xf35691f6,v126\n"
"v_min_f32 v21,v21,v105\n"
"v_add_f32 v22,0xf3633123,v126\n"
"v_mfma_f32_16x16x32_bf16 v[4:7],a[80:83],a[0:3],v[4:7]\n"
"s_waitcnt lgkmcnt(0)\n"
"v_min_f32 v22,v22,v106\n"
"v_add_f32 v23,0xf36fd050,v126\n"
"v_mfma_f32_16x16x32_bf16 v[192:195],a[96:99],v[72:75],v[192:195]\n"
"v_exp_f32 v56,v56\n"
"v_mfma_f32_16x16x32_bf16 a[136:139],a[144:147],v[72:75],a[136:139]\n"
"v_exp_f32 v57,v57\n"
"v_mfma_f32_16x16x32_bf16 v[196:199],a[100:103],v[72:75],v[196:199]\n"
"v_min_f32 v20,v20,v104\n"
"v_cvt_pk_bf16_f32 v92,v56,v57\n"
"v_mfma_f32_16x16x32_bf16 v[200:203],a[104:107],v[72:75],v[200:203]\n"
"v_max3_f32 v118,v0,v1,v2\n"
"v_max3_f32 v118,v118,v3,v4\n"
"v_mfma_f32_16x16x32_bf16 v[204:207],a[108:111],v[72:75],v[204:207]\n"
"v_max3_f32 v118,v118,v5,v6\n"
"v_max_f32 v118,v118,v7\n"
"v_mfma_f32_16x16x32_bf16 v[208:211],a[112:115],v[72:75],v[208:211]\n"
"v_min_f32 v23,v23,v107\n"
"v_mov_b32 v120,v118\n"
"v_mfma_f32_16x16x32_bf16 v[212:215],a[116:119],v[72:75],v[212:215]\n"
"v_exp_f32 v58,v58\n"
"v_mfma_f32_16x16x32_bf16 v[216:219],a[120:123],v[72:75],v[216:219]\n"
"v_mfma_f32_16x16x32_bf16 v[220:223],a[124:127],v[72:75],v[220:223]\n"
"v_exp_f32 v59,v59\n"
"v_mfma_f32_16x16x32_bf16 v[224:227],a[96:99],v[76:79],v[224:227]\n"
"v_cvt_pk_bf16_f32 v93,v58,v59\n"
"v_mfma_f32_16x16x32_bf16 a[140:143],a[144:147],v[76:79],a[140:143]\n"
"v_accvgpr_read_b32 v122,a136\n"
"v_mfma_f32_16x16x32_bf16 v[228:231],a[100:103],v[76:79],v[228:231]\n"
"v_rcp_f32 v122,v122\n"
"v_mfma_f32_16x16x32_bf16 v[232:235],a[104:107],v[76:79],v[232:235]\n"
"v_pk_mul_f32 v[192:193],v[192:193],v[122:123] op_sel_hi:[1,0]\n"
"v_pk_mul_f32 v[194:195],v[194:195],v[122:123] op_sel_hi:[1,0]\n"
"v_pk_mul_f32 v[196:197],v[196:197],v[122:123] op_sel_hi:[1,0]\n"
"v_pk_mul_f32 v[198:199],v[198:199],v[122:123] op_sel_hi:[1,0]\n"
"v_mfma_f32_16x16x32_bf16 v[236:239],a[108:111],v[76:79],v[236:239]\n"
"v_cvt_pk_bf16_f32 v192,v192,v193\n"
"v_cvt_pk_bf16_f32 v193,v194,v195\n"
"v_mfma_f32_16x16x32_bf16 v[240:243],a[112:115],v[76:79],v[240:243]\n"
"v_cvt_pk_bf16_f32 v194,v196,v197\n"
"v_cvt_pk_bf16_f32 v195,v198,v199\n"
"v_mfma_f32_16x16x32_bf16 v[244:247],a[116:119],v[76:79],v[244:247]\n"
"buffer_store_dwordx4 v[192:195],v112,s[24:27],s70 offen offset:0 sc1\n"
"v_pk_mul_f32 v[200:201],v[200:201],v[122:123] op_sel_hi:[1,0]\n"
"v_pk_mul_f32 v[202:203],v[202:203],v[122:123] op_sel_hi:[1,0]\n"
"v_mfma_f32_16x16x32_bf16 v[248:251],a[120:123],v[76:79],v[248:251]\n"
"v_exp_f32 v60,v60\n"
"v_cvt_pk_bf16_f32 v200,v200,v201\n"
"v_cvt_pk_bf16_f32 v201,v202,v203\n"
"v_pk_mul_f32 v[204:205],v[204:205],v[122:123] op_sel_hi:[1,0]\n"
"v_pk_mul_f32 v[206:207],v[206:207],v[122:123] op_sel_hi:[1,0]\n"
"v_mfma_f32_16x16x32_bf16 v[252:255],a[124:127],v[76:79],v[252:255]\n"
"v_cvt_pk_bf16_f32 v202,v204,v205\n"
"v_cvt_pk_bf16_f32 v203,v206,v207\n"
"v_mfma_f32_16x16x32_bf16 v[12:15],a[80:83],a[16:19],v[100:103]\n"
"buffer_store_dwordx4 v[200:203],v112,s[24:27],s70 offen offset:1024 sc1\n"
"v_pk_mul_f32 v[208:209],v[208:209],v[122:123] op_sel_hi:[1,0]\n"
"v_pk_mul_f32 v[210:211],v[210:211],v[122:123] op_sel_hi:[1,0]\n"
"v_pk_mul_f32 v[212:213],v[212:213],v[122:123] op_sel_hi:[1,0]\n"
"v_pk_mul_f32 v[214:215],v[214:215],v[122:123] op_sel_hi:[1,0]\n"
"v_mfma_f32_16x16x32_bf16 v[12:15],a[84:87],a[20:23],v[12:15]\n"
"v_exp_f32 v61,v61\n"
"v_mfma_f32_16x16x32_bf16 v[12:15],a[88:91],a[24:27],v[12:15]\n"
"v_cvt_pk_bf16_f32 v94,v60,v61\n"
"v_cvt_pk_bf16_f32 v208,v208,v209\n"
"v_mfma_f32_16x16x32_bf16 v[12:15],a[92:95],a[28:31],v[12:15]\n"
"v_cvt_pk_bf16_f32 v209,v210,v211\n"
"v_cvt_pk_bf16_f32 v210,v212,v213\n"
"v_cvt_pk_bf16_f32 v211,v214,v215\n"
"v_mfma_f32_16x16x32_bf16 v[8:11],a[76:79],a[28:31],v[100:103]\n"
"buffer_store_dwordx4 v[208:211],v112,s[24:27],s70 offen offset:2048 sc1\n"
"v_pk_mul_f32 v[216:217],v[216:217],v[122:123] op_sel_hi:[1,0]\n"
"v_pk_mul_f32 v[218:219],v[218:219],v[122:123] op_sel_hi:[1,0]\n"
"v_mfma_f32_16x16x32_bf16 v[8:11],a[72:75],a[24:27],v[8:11]\n"
"v_cvt_pk_bf16_f32 v216,v216,v217\n"
"v_cvt_pk_bf16_f32 v217,v218,v219\n"
"v_pk_mul_f32 v[220:221],v[220:221],v[122:123] op_sel_hi:[1,0]\n"
"v_pk_mul_f32 v[222:223],v[222:223],v[122:123] op_sel_hi:[1,0]\n"
"v_mfma_f32_16x16x32_bf16 v[8:11],a[68:71],a[20:23],v[8:11]\n"
"v_cvt_pk_bf16_f32 v218,v220,v221\n"
"v_cvt_pk_bf16_f32 v219,v222,v223\n"
"v_mfma_f32_16x16x32_bf16 v[8:11],a[64:67],a[16:19],v[8:11]\n"
"buffer_store_dwordx4 v[216:219],v112,s[24:27],s70 offen offset:3072 sc1\n"
"v_mfma_f32_16x16x32_bf16 v[24:27],a[64:67],a[48:51],v[108:111]\n"
"s_add_u32 s70,s70,262144\n"
"v_exp_f32 v62,v62\n"
"v_mfma_f32_16x16x32_bf16 v[24:27],a[68:71],a[52:55],v[24:27]\n"
"v_exp_f32 v63,v63\n"
"v_mfma_f32_16x16x32_bf16 v[24:27],a[72:75],a[56:59],v[24:27]\n"
"s_nop 0\n"
"v_max3_f32 v119,v8,v9,v10\n"
"v_mfma_f32_16x16x32_bf16 v[24:27],a[76:79],a[60:63],v[24:27]\n"
"v_max3_f32 v119,v119,v11,v12\n"
"v_max3_f32 v119,v119,v13,v14\n"
"v_mfma_f32_16x16x32_bf16 v[28:31],a[92:95],a[60:63],v[108:111]\n"
"v_max_f32 v119,v119,v15\n"
"v_mfma_f32_16x16x32_bf16 v[28:31],a[88:91],a[56:59],v[28:31]\n"
"v_min_f32 v120,v120,v119\n"
"v_max_f32 v118,v118,v119\n"
"v_mfma_f32_16x16x32_bf16 v[28:31],a[84:87],a[52:55],v[28:31]\n"
"v_mfma_f32_16x16x32_bf16 v[28:31],a[80:83],a[48:51],v[28:31]\n"
"v_cvt_pk_bf16_f32 v95,v62,v63\n"
"v_mfma_f32_16x16x32_bf16 v[20:23],a[80:83],a[32:35],v[20:23]\n"
"v_mfma_f32_16x16x32_bf16 v[20:23],a[84:87],a[36:39],v[20:23]\n"
"v_mfma_f32_16x16x32_bf16 v[20:23],a[88:91],a[40:43],v[20:23]\n"
"v_max3_f32 v119,v24,v25,v26\n"
"v_mfma_f32_16x16x32_bf16 v[20:23],a[92:95],a[44:47],v[20:23]\n"
"s_nop 1\n"
"v_max3_f32 v119,v119,v27,v28\n"
"v_mfma_f32_16x16x32_bf16 v[16:19],a[76:79],a[44:47],v[16:19]\n"
"v_max3_f32 v119,v119,v29,v30\n"
"v_max_f32 v119,v119,v31\n"
"v_mfma_f32_16x16x32_bf16 v[16:19],a[72:75],a[40:43],v[16:19]\n"
"v_mfma_f32_16x16x32_bf16 v[16:19],a[68:71],a[36:39],v[16:19]\n"
"v_mfma_f32_16x16x32_bf16 v[16:19],a[64:67],a[32:35],v[16:19]\n"
"ds_read_b128 a[64:67],v114 offset:57344\n"
"ds_read_b128 a[68:71],v114 offset:58368\n"
"ds_read_b128 a[72:75],v114 offset:59392\n"
"v_max_f32 v118,v118,v119\n"
"v_min_f32 v120,v120,v119\n"
"v_cmp_gt_f32 s[96:97],s16,v120\n"
"s_nop 1\n"
"v_max3_f32 v119,v16,v17,v18\n"
"v_max3_f32 v119,v119,v19,v20\n"
"v_max3_f32 v119,v119,v21,v22\n"
"v_max_f32 v119,v119,v23\n"
"v_cmp_gt_f32 s[28:29],s16,v119\n"
"v_max_f32 v118,v118,v119\n"
"v_cmp_lt_f32 vcc,s93,v118\n"
"ds_read_b128 a[76:79],v114 offset:60416\n"
"ds_read_b128 a[80:83],v114 offset:61440\n"
"ds_read_b128 a[84:87],v114 offset:62464\n"
"ds_read_b128 a[88:91],v114 offset:63488\n"
"ds_read_b128 a[92:95],v114 offset:64512\n"
"v_mov_b32 v114,v112\n"
"s_sub_u32 s82,s3,32768\n"
"buffer_load_dwordx4 a[0:3],v112,s[24:27],s82 offen offset:0 sc1\n"
"buffer_load_dwordx4 a[4:7],v112,s[24:27],s82 offen offset:1024 sc1\n"
"buffer_load_dwordx4 a[8:11],v112,s[24:27],s82 offen offset:2048 sc1\n"
"buffer_load_dwordx4 a[12:15],v112,s[24:27],s82 offen offset:3072 sc1\n"
"s_sub_u32 s82,s3,49152\n"
"buffer_load_dwordx4 a[16:19],v112,s[24:27],s82 offen offset:0 sc1\n"
"buffer_load_dwordx4 a[20:23],v112,s[24:27],s82 offen offset:1024 sc1\n"
"buffer_load_dwordx4 a[24:27],v112,s[24:27],s82 offen offset:2048 sc1\n"
"buffer_load_dwordx4 a[28:31],v112,s[24:27],s82 offen offset:3072 sc1\n"
"s_and_b32 s28,s28,0xffff\n"
"s_or_b32 s96,s96,s28\n"
"s_or_b64 s[96:97],s[96:97],vcc\n"
"s_cbranch_scc1 .Lfk_cs_c3\n"
".Lfk_cr_c3:\n"
"s_waitcnt vmcnt(13) lgkmcnt(0)\n"
"s_barrier\n"
"v_mfma_f32_16x16x32_bf16 v[224:227],a[64:67],v[92:95],v[224:227]\n"
"ds_read_b128 a[96:99],v114 offset:40960\n"
"v_exp_f32 v0,v0\n"
"v_mfma_f32_16x16x32_bf16 a[140:143],a[144:147],v[92:95],a[140:143]\n"
"ds_read_b128 a[100:103],v114 offset:41984\n"
"v_exp_f32 v1,v1\n"
"v_mfma_f32_16x16x32_bf16 v[228:231],a[68:71],v[92:95],v[228:231]\n"
"ds_read_b128 a[104:107],v114 offset:43008\n"
"v_exp_f32 v2,v2\n"
"v_mfma_f32_16x16x32_bf16 v[232:235],a[72:75],v[92:95],v[232:235]\n"
"ds_read_b128 a[108:111],v114 offset:44032\n"
"v_exp_f32 v3,v3\n"
"v_mfma_f32_16x16x32_bf16 v[236:239],a[76:79],v[92:95],v[236:239]\n"
"ds_read_b128 a[112:115],v114 offset:45056\n"
"v_exp_f32 v4,v4\n"
"v_mfma_f32_16x16x32_bf16 v[240:243],a[80:83],v[92:95],v[240:243]\n"
"ds_read_b128 a[116:119],v114 offset:46080\n"
"v_exp_f32 v5,v5\n"
"v_mfma_f32_16x16x32_bf16 v[244:247],a[84:87],v[92:95],v[244:247]\n"
"ds_read_b128 a[120:123],v114 offset:47104\n"
"v_exp_f32 v6,v6\n"
"v_mfma_f32_16x16x32_bf16 v[248:251],a[88:91],v[92:95],v[248:251]\n"
"v_exp_f32 v7,v7\n"
"v_mfma_f32_16x16x32_bf16 v[252:255],a[92:95],v[92:95],v[252:255]\n"
"v_exp_f32 v8,v8\n"
"v_accvgpr_read_b32 v119,a150\n"
"v_accvgpr_read_b32 v120,a151\n"
"v_xor_b32 v119,s80,v119\n"
"v_xor_b32 v120,s81,v120\n"
"v_or_b32 v119,v119,v120\n"
"v_cmp_ne_u32 s[14:15],0,v119\n"
"s_add_u32 s18,s8,1032192\n"
"s_cmp_lg_u32 s14,0\n"
"s_cselect_b32 s18,2146435072,s18\n"
"s_add_u32 m0,s30,0\n"
"ds_read_b128 a[124:127],v114 offset:48128\n"
"buffer_load_dwordx4 v112,s[24:27],s18 offen offset:0 sc1 lds\n"
"buffer_load_dwordx4 v112,s[24:27],s18 offen offset:1024 sc1 lds\n"
"buffer_load_dwordx4 v112,s[24:27],s18 offen offset:2048 sc1 lds\n"
"buffer_load_dwordx4 v112,s[24:27],s18 offen offset:3072 sc1 lds\n"
"s_add_u32 m0,m0,4096\n"
"s_nop 0\n"
"buffer_load_dwordx4 v117,s[24:27],s18 offen offset:0 sc1 lds\n"
"buffer_load_dwordx4 v117,s[24:27],s18 offen offset:1024 sc1 lds\n"
"buffer_load_dwordx4 v117,s[24:27],s18 offen offset:2048 sc1 lds\n"
"buffer_load_dwordx4 v117,s[24:27],s18 offen offset:3072 sc1 lds\n"
"s_sub_u32 s82,s3,65536\n"
"buffer_load_dwordx4 v[32:35],v112,s[24:27],s82 offen offset:0 sc1\n"
"buffer_load_dwordx4 v[36:39],v112,s[24:27],s82 offen offset:1024 sc1\n"
"buffer_load_dwordx4 v[40:43],v112,s[24:27],s82 offen offset:2048 sc1\n"
"buffer_load_dwordx4 v[44:47],v112,s[24:27],s82 offen offset:3072 sc1\n"
"v_exp_f32 v9,v9\n"
"v_cvt_pk_bf16_f32 v64,v0,v1\n"
"v_exp_f32 v10,v10\n"
"v_cvt_pk_bf16_f32 v65,v2,v3\n"
"v_exp_f32 v11,v11\n"
"v_cvt_pk_bf16_f32 v66,v4,v5\n"
"v_exp_f32 v12,v12\n"
"v_cvt_pk_bf16_f32 v67,v6,v7\n"
"v_exp_f32 v13,v13\n"
"v_cvt_pk_bf16_f32 v68,v8,v9\n"
"v_exp_f32 v14,v14\n"
"v_cvt_pk_bf16_f32 v69,v10,v11\n"
"v_exp_f32 v15,v15\n"
"v_cvt_pk_bf16_f32 v70,v12,v13\n"
"v_exp_f32 v16,v16\n"
"v_cvt_pk_bf16_f32 v71,v14,v15\n"
"v_exp_f32 v17,v17\n"
"v_exp_f32 v18,v18\n"
"v_cvt_pk_bf16_f32 v72,v16,v17\n"
"v_exp_f32 v19,v19\n"
"v_exp_f32 v20,v20\n"
"v_cvt_pk_bf16_f32 v73,v18,v19\n"
"v_exp_f32 v21,v21\n"
"v_exp_f32 v22,v22\n"
"v_cvt_pk_bf16_f32 v74,v20,v21\n"
"v_exp_f32 v23,v23\n"
"v_exp_f32 v24,v24\n"
"v_cvt_pk_bf16_f32 v75,v22,v23\n"
"v_exp_f32 v25,v25\n"
"v_exp_f32 v26,v26\n"
"v_cvt_pk_bf16_f32 v76,v24,v25\n"
"v_exp_f32 v27,v27\n"
"v_exp_f32 v28,v28\n"
"v_cvt_pk_bf16_f32 v77,v26,v27\n"
"v_exp_f32 v29,v29\n"
"v_exp_f32 v30,v30\n"
"v_cvt_pk_bf16_f32 v78,v28,v29\n"
"v_exp_f32 v31,v31\n"
"s_nop 0\n"
"v_cvt_pk_bf16_f32 v79,v30,v31\n"
"v_accvgpr_read_b32 v122,a140\n"
"v_rcp_f32 v122,v122\n"
"s_nop 0\n"
"v_pk_mul_f32 v[224:225],v[224:225],v[122:123] op_sel_hi:[1,0]\n"
"v_pk_mul_f32 v[226:227],v[226:227],v[122:123] op_sel_hi:[1,0]\n"
"v_pk_mul_f32 v[228:229],v[228:229],v[122:123] op_sel_hi:[1,0]\n"
"v_pk_mul_f32 v[230:231],v[230:231],v[122:123] op_sel_hi:[1,0]\n"
"v_cvt_pk_bf16_f32 v224,v224,v225\n"
"v_cvt_pk_bf16_f32 v225,v226,v227\n"
"v_cvt_pk_bf16_f32 v226,v228,v229\n"
"v_cvt_pk_bf16_f32 v227,v230,v231\n"
"buffer_store_dwordx4 v[224:227],v112,s[24:27],s70 offen offset:0 sc1\n"
"v_pk_mul_f32 v[232:233],v[232:233],v[122:123] op_sel_hi:[1,0]\n"
"v_pk_mul_f32 v[234:235],v[234:235],v[122:123] op_sel_hi:[1,0]\n"
"v_pk_mul_f32 v[236:237],v[236:237],v[122:123] op_sel_hi:[1,0]\n"
"v_pk_mul_f32 v[238:239],v[238:239],v[122:123] op_sel_hi:[1,0]\n"
"v_cvt_pk_bf16_f32 v232,v232,v233\n"
"v_cvt_pk_bf16_f32 v233,v234,v235\n"
"v_cvt_pk_bf16_f32 v234,v236,v237\n"
"v_cvt_pk_bf16_f32 v235,v238,v239\n"
"buffer_store_dwordx4 v[232:235],v112,s[24:27],s70 offen offset:1024 sc1\n"
"v_pk_mul_f32 v[240:241],v[240:241],v[122:123] op_sel_hi:[1,0]\n"
"v_pk_mul_f32 v[242:243],v[242:243],v[122:123] op_sel_hi:[1,0]\n"
"v_pk_mul_f32 v[244:245],v[244:245],v[122:123] op_sel_hi:[1,0]\n"
"v_pk_mul_f32 v[246:247],v[246:247],v[122:123] op_sel_hi:[1,0]\n"
"v_cvt_pk_bf16_f32 v240,v240,v241\n"
"v_cvt_pk_bf16_f32 v241,v242,v243\n"
"v_cvt_pk_bf16_f32 v242,v244,v245\n"
"v_cvt_pk_bf16_f32 v243,v246,v247\n"
"buffer_store_dwordx4 v[240:243],v112,s[24:27],s70 offen offset:2048 sc1\n"
"v_pk_mul_f32 v[248:249],v[248:249],v[122:123] op_sel_hi:[1,0]\n"
"v_pk_mul_f32 v[250:251],v[250:251],v[122:123] op_sel_hi:[1,0]\n"
"v_pk_mul_f32 v[252:253],v[252:253],v[122:123] op_sel_hi:[1,0]\n"
"v_pk_mul_f32 v[254:255],v[254:255],v[122:123] op_sel_hi:[1,0]\n"
"v_cvt_pk_bf16_f32 v248,v248,v249\n"
"v_cvt_pk_bf16_f32 v249,v250,v251\n"
"v_cvt_pk_bf16_f32 v250,v252,v253\n"
"v_cvt_pk_bf16_f32 v251,v254,v255\n"
"buffer_store_dwordx4 v[248:251],v112,s[24:27],s70 offen offset:3072 sc1\n"
"s_add_u32 s70,s70,262144\n"
"s_mov_b32 s70,s68\n"
"s_load_dwordx2 s[36:37],s[0:1],0\n"
"s_load_dwordx2 s[44:45],s[0:1],80\n"
"s_load_dwordx2 s[48:49],s[0:1],88\n"
"s_lshl_b32 s82,s78,2\n"
"s_add_u32 s82,s82,65536\n"
"v_add_u32 v113,s82,v112\n"
"s_lshr_b32 s82,s78,2\n"
"s_add_u32 s82,s82,122880\n"
"v_lshrrev_b32 v115,1,v112\n"
"v_add_u32 v116,s82,v115\n"
"s_waitcnt lgkmcnt(0)\n"
"s_barrier\n"
"v_mfma_f32_16x16x32_bf16 v[128:131],a[96:99],v[64:67],0\n"
"s_add_u32 s83,s18,2097152\n"
"s_add_u32 m0,s30,32768\n"
"v_mfma_f32_16x16x32_bf16 a[128:131],a[144:147],v[64:67],0\n"
"buffer_load_dwordx4 v112,s[24:27],s83 offen offset:0 sc1 lds\n"
"v_mfma_f32_16x16x32_bf16 v[132:135],a[100:103],v[64:67],0\n"
"buffer_load_dwordx4 v112,s[24:27],s83 offen offset:1024 sc1 lds\n"
"v_mfma_f32_16x16x32_bf16 v[136:139],a[104:107],v[64:67],0\n"
"buffer_load_dwordx4 v112,s[24:27],s83 offen offset:2048 sc1 lds\n"
"v_mfma_f32_16x16x32_bf16 v[140:143],a[108:111],v[64:67],0\n"
"v_mfma_f32_16x16x32_bf16 v[144:147],a[112:115],v[64:67],0\n"
"buffer_load_dwordx4 v112,s[24:27],s83 offen offset:3072 sc1 lds\n"
"v_mfma_f32_16x16x32_bf16 v[148:151],a[116:119],v[64:67],0\n"
"s_add_u32 m0,m0,4096\n"
"v_mfma_f32_16x16x32_bf16 v[152:155],a[120:123],v[64:67],0\n"
"v_mfma_f32_16x16x32_bf16 v[156:159],a[124:127],v[64:67],0\n"
"buffer_load_dwordx4 v117,s[24:27],s83 offen offset:0 sc1 lds\n"
"v_mfma_f32_16x16x32_bf16 v[160:163],a[96:99],v[68:71],0\n"
"v_accvgpr_read_b32 v97,a128\n"
"v_cvt_pk_bf16_f32 v136,v136,v137\n"
"v_mfma_f32_16x16x32_bf16 a[132:135],a[144:147],v[68:71],0\n"
"buffer_load_dwordx4 v117,s[24:27],s83 offen offset:1024 sc1 lds\n"
"v_mfma_f32_16x16x32_bf16 v[164:167],a[100:103],v[68:71],0\n"
"v_cvt_pk_bf16_f32 v128,v128,v129\n"
"v_cvt_pk_bf16_f32 v129,v130,v131\n"
"v_mfma_f32_16x16x32_bf16 v[168:171],a[104:107],v[68:71],0\n"
"buffer_load_dwordx4 v117,s[24:27],s83 offen offset:2048 sc1 lds\n"
"v_cvt_pk_bf16_f32 v130,v132,v133\n"
"v_cvt_pk_bf16_f32 v131,v134,v135\n"
"v_mfma_f32_16x16x32_bf16 v[172:175],a[108:111],v[68:71],0\n"
"ds_write_b128 v113,v[128:131] offset:0\n"
"v_cvt_pk_bf16_f32 v137,v138,v139\n"
"v_mfma_f32_16x16x32_bf16 v[176:179],a[112:115],v[68:71],0\n"
"buffer_load_dwordx4 v117,s[24:27],s83 offen offset:3072 sc1 lds\n"
"v_cvt_pk_bf16_f32 v138,v140,v141\n"
"v_cvt_pk_bf16_f32 v139,v142,v143\n"
"v_mfma_f32_16x16x32_bf16 v[180:183],a[116:119],v[68:71],0\n"
"s_sub_u32 s82,s3,81920\n"
"ds_write_b128 v113,v[136:139] offset:1024\n"
"v_cvt_pk_bf16_f32 v144,v144,v145\n"
"v_mfma_f32_16x16x32_bf16 v[184:187],a[120:123],v[68:71],0\n"
"buffer_load_dwordx4 a[32:35],v112,s[24:27],s82 offen offset:0 sc1\n"
"v_cvt_pk_bf16_f32 v145,v146,v147\n"
"v_cvt_pk_bf16_f32 v146,v148,v149\n"
"v_cvt_pk_bf16_f32 v147,v150,v151\n"
"v_mfma_f32_16x16x32_bf16 v[188:191],a[124:127],v[68:71],0\n"
"ds_write_b128 v113,v[144:147] offset:2048\n"
"v_cvt_pk_bf16_f32 v152,v152,v153\n"
"v_mfma_f32_16x16x32_bf16 v[192:195],a[96:99],v[72:75],0\n"
"buffer_load_dwordx4 a[36:39],v112,s[24:27],s82 offen offset:1024 sc1\n"
"v_cvt_pk_bf16_f32 v153,v154,v155\n"
"v_cvt_pk_bf16_f32 v154,v156,v157\n"
"v_cvt_pk_bf16_f32 v155,v158,v159\n"
"v_mfma_f32_16x16x32_bf16 a[136:139],a[144:147],v[72:75],0\n"
"ds_write_b128 v113,v[152:155] offset:3072\n"
"ds_write_b64 v116,v[96:97] offset:0\n"
"v_mfma_f32_16x16x32_bf16 v[196:199],a[100:103],v[72:75],0\n"
"buffer_load_dwordx4 a[40:43],v112,s[24:27],s82 offen offset:2048 sc1\n"
"v_accvgpr_read_b32 v101,a132\n"
"v_cvt_pk_bf16_f32 v160,v160,v161\n"
"v_mfma_f32_16x16x32_bf16 v[200:203],a[104:107],v[72:75],0\n"
"v_cvt_pk_bf16_f32 v161,v162,v163\n"
"v_cvt_pk_bf16_f32 v162,v164,v165\n"
"v_cvt_pk_bf16_f32 v163,v166,v167\n"
"v_mfma_f32_16x16x32_bf16 v[204:207],a[108:111],v[72:75],0\n"
"ds_write_b128 v113,v[160:163] offset:4096\n"
"v_cvt_pk_bf16_f32 v168,v168,v169\n"
"v_cvt_pk_bf16_f32 v169,v170,v171\n"
"v_mfma_f32_16x16x32_bf16 v[208:211],a[112:115],v[72:75],0\n"
"buffer_load_dwordx4 a[44:47],v112,s[24:27],s82 offen offset:3072 sc1\n"
"v_cvt_pk_bf16_f32 v170,v172,v173\n"
"v_cvt_pk_bf16_f32 v171,v174,v175\n"
"v_mfma_f32_16x16x32_bf16 v[212:215],a[116:119],v[72:75],0\n"
"ds_write_b128 v113,v[168:171] offset:5120\n"
"v_cvt_pk_bf16_f32 v176,v176,v177\n"
"v_cvt_pk_bf16_f32 v177,v178,v179\n"
"v_mfma_f32_16x16x32_bf16 v[216:219],a[120:123],v[72:75],0\n"
"v_cvt_pk_bf16_f32 v178,v180,v181\n"
"v_cvt_pk_bf16_f32 v179,v182,v183\n"
"v_mfma_f32_16x16x32_bf16 v[220:223],a[124:127],v[72:75],0\n"
"ds_write_b128 v113,v[176:179] offset:6144\n"
"v_cvt_pk_bf16_f32 v184,v184,v185\n"
"v_cvt_pk_bf16_f32 v185,v186,v187\n"
"v_cvt_pk_bf16_f32 v186,v188,v189\n"
"v_cvt_pk_bf16_f32 v187,v190,v191\n"
"v_mfma_f32_16x16x32_bf16 v[224:227],a[96:99],v[76:79],0\n"
"ds_write_b128 v113,v[184:187] offset:7168\n"
"v_mfma_f32_16x16x32_bf16 a[140:143],a[144:147],v[76:79],0\n"
"s_sub_u32 s82,s3,131072\n"
"ds_write_b64 v116,v[100:101] offset:512\n"
"buffer_load_dwordx4 v[128:131],v112,s[24:27],s82 offen offset:0 sc1\n"
"buffer_load_dwordx4 v[132:135],v112,s[24:27],s82 offen offset:1024 sc1\n"
"v_mfma_f32_16x16x32_bf16 v[228:231],a[100:103],v[76:79],0\n"
"buffer_load_dwordx4 v[136:139],v112,s[24:27],s82 offen offset:2048 sc1\n"
"buffer_load_dwordx4 v[140:143],v112,s[24:27],s82 offen offset:3072 sc1\n"
"s_sub_u32 s82,s3,147456\n"
"v_mfma_f32_16x16x32_bf16 v[232:235],a[104:107],v[76:79],0\n"
"buffer_load_dwordx4 v[144:147],v112,s[24:27],s82 offen offset:0 sc1\n"
"buffer_load_dwordx4 v[148:151],v112,s[24:27],s82 offen offset:1024 sc1\n"
"buffer_load_dwordx4 v[152:155],v112,s[24:27],s82 offen offset:2048 sc1\n"
"v_mfma_f32_16x16x32_bf16 v[236:239],a[108:111],v[76:79],0\n"
"buffer_load_dwordx4 v[156:159],v112,s[24:27],s82 offen offset:3072 sc1\n"
"v_mfma_f32_16x16x32_bf16 v[240:243],a[112:115],v[76:79],0\n"
"v_mfma_f32_16x16x32_bf16 v[244:247],a[116:119],v[76:79],0\n"
"v_mfma_f32_16x16x32_bf16 v[248:251],a[120:123],v[76:79],0\n"
"v_mfma_f32_16x16x32_bf16 v[252:255],a[124:127],v[76:79],0\n"
"s_waitcnt vmcnt(20)\n"
"buffer_store_dwordx2 a[154:155],off,s[24:27],s69 sc1\n"
"s_branch .Lfk_endB\n"
".Lfk_head0:\n"
"s_mov_b32 s64,s61\n"
"s_cmp_eq_u32 s52,3\n"
"s_cselect_b32 s67,172032,0\n"
"s_waitcnt lgkmcnt(0)\n"
"s_barrier\n"
"v_mfma_f32_16x16x32_bf16 v[24:27],a[64:67],a[48:51],v[108:111]\n"
"v_add_f32 v0,0x80000000,v126\n"
"v_min_f32 v0,v0,v96\n"
"v_mfma_f32_16x16x32_bf16 v[24:27],a[68:71],a[52:55],v[24:27]\n"
"v_add_f32 v1,0xf149f2ca,v126\n"
"v_min_f32 v1,v1,v97\n"
"v_mfma_f32_16x16x32_bf16 v[24:27],a[72:75],a[56:59],v[24:27]\n"
"v_add_f32 v2,0xf1c9f2ca,v126\n"
"v_min_f32 v2,v2,v98\n"
"v_mfma_f32_16x16x32_bf16 v[24:27],a[76:79],a[60:63],v[24:27]\n"
"v_add_f32 v3,0xf2177617,v126\n"
"v_min_f32 v3,v3,v99\n"
"v_mfma_f32_16x16x32_bf16 v[28:31],a[92:95],a[60:63],v[108:111]\n"
"v_add_f32 v4,0xf349f2ca,v126\n"
"v_min_f32 v4,v4,v96\n"
"v_mfma_f32_16x16x32_bf16 v[28:31],a[88:91],a[56:59],v[28:31]\n"
"v_add_f32 v5,0xf35691f6,v126\n"
"v_min_f32 v5,v5,v97\n"
"v_mfma_f32_16x16x32_bf16 v[28:31],a[84:87],a[52:55],v[28:31]\n"
"v_add_f32 v6,0xf3633123,v126\n"
"v_min_f32 v6,v6,v98\n"
"v_mfma_f32_16x16x32_bf16 v[28:31],a[80:83],a[48:51],v[28:31]\n"
"v_add_f32 v7,0xf36fd050,v126\n"
"v_min_f32 v7,v7,v99\n"
"s_nop 1\n"
"v_mfma_f32_16x16x32_bf16 v[4:7],a[80:83],a[0:3],v[4:7]\n"
"s_andn2_b32 m0,s55,16384\n"
"s_add_u32 s54,s54,s22\n"
"v_mfma_f32_16x16x32_bf16 v[4:7],a[84:87],a[4:7],v[4:7]\n"
"ds_read_b128 a[96:99],v113 offset:16384\n"
"ds_read_b128 a[100:103],v113 offset:17408\n"
"v_mfma_f32_16x16x32_bf16 v[4:7],a[88:91],a[8:11],v[4:7]\n"
"ds_read_b128 a[104:107],v113 offset:18432\n"
"ds_read_b128 a[108:111],v113 offset:19456\n"
"v_mfma_f32_16x16x32_bf16 v[4:7],a[92:95],a[12:15],v[4:7]\n"
"ds_read_b128 a[112:115],v113 offset:20480\n"
"ds_read_b128 a[116:119],v113 offset:21504\n"
"v_mfma_f32_16x16x32_bf16 v[0:3],a[76:79],a[12:15],v[0:3]\n"
"ds_read_b128 a[120:123],v113 offset:22528\n"
"ds_read_b128 a[124:127],v113 offset:23552\n"
"v_mfma_f32_16x16x32_bf16 v[0:3],a[72:75],a[8:11],v[0:3]\n"
"buffer_load_dwordx4 v115,s[32:35],s54 offen sc1 lds\n"
"v_mfma_f32_16x16x32_bf16 v[0:3],a[68:71],a[4:7],v[0:3]\n"
"s_add_u32 m0,s67,0\n"
"v_max3_f32 v118,v24,v25,v26\n"
"v_mfma_f32_16x16x32_bf16 v[0:3],a[64:67],a[0:3],v[0:3]\n"
"buffer_load_dwordx4 v112,s[24:27],s64 offen offset:0 sc1 lds\n"
"v_mfma_f32_16x16x32_bf16 v[8:11],a[64:67],a[16:19],v[100:103]\n"
"buffer_load_dwordx4 v112,s[24:27],s64 offen offset:1024 sc1 lds\n"
"v_mfma_f32_16x16x32_bf16 v[8:11],a[68:71],a[20:23],v[8:11]\n"
"buffer_load_dwordx4 v112,s[24:27],s64 offen offset:2048 sc1 lds\n"
"v_mfma_f32_16x16x32_bf16 v[8:11],a[72:75],a[24:27],v[8:11]\n"
"buffer_load_dwordx4 v112,s[24:27],s64 offen offset:3072 sc1 lds\n"
"v_mfma_f32_16x16x32_bf16 v[8:11],a[76:79],a[28:31],v[8:11]\n"
"s_add_u32 m0,s67,4096\n"
"v_max3_f32 v118,v118,v27,v28\n"
"v_cvt_pk_bf16_f32 v192,v192,v193\n"
"v_mfma_f32_16x16x32_bf16 v[12:15],a[92:95],a[28:31],v[100:103]\n"
"buffer_load_dwordx4 v117,s[24:27],s64 offen offset:0 sc1 lds\n"
"v_mfma_f32_16x16x32_bf16 v[12:15],a[88:91],a[24:27],v[12:15]\n"
"buffer_load_dwordx4 v117,s[24:27],s64 offen offset:1024 sc1 lds\n"
"v_mfma_f32_16x16x32_bf16 v[12:15],a[84:87],a[20:23],v[12:15]\n"
"buffer_load_dwordx4 v117,s[24:27],s64 offen offset:2048 sc1 lds\n"
"v_mfma_f32_16x16x32_bf16 v[12:15],a[80:83],a[16:19],v[12:15]\n"
"buffer_load_dwordx4 v117,s[24:27],s64 offen offset:3072 sc1 lds\n"
"v_cvt_pk_bf16_f32 v193,v194,v195\n"
"v_cvt_pk_bf16_f32 v194,v196,v197\n"
"v_cvt_pk_bf16_f32 v195,v198,v199\n"
"v_cvt_pk_bf16_f32 v200,v200,v201\n"
"v_cvt_pk_bf16_f32 v201,v202,v203\n"
"v_cvt_pk_bf16_f32 v202,v204,v205\n"
"v_cvt_pk_bf16_f32 v203,v206,v207\n"
"ds_write_b128 v116,v[192:195] offset:32768\n"
"ds_write_b128 v116,v[200:203] offset:33792\n"
"v_cvt_pk_bf16_f32 v208,v208,v209\n"
"v_cvt_pk_bf16_f32 v209,v210,v211\n"
"v_cvt_pk_bf16_f32 v210,v212,v213\n"
"v_cvt_pk_bf16_f32 v211,v214,v215\n"
"v_cvt_pk_bf16_f32 v216,v216,v217\n"
"v_cvt_pk_bf16_f32 v217,v218,v219\n"
"v_cvt_pk_bf16_f32 v218,v220,v221\n"
"v_cvt_pk_bf16_f32 v219,v222,v223\n"
"ds_write_b128 v116,v[208:211] offset:34816\n"
"ds_write_b128 v116,v[216:219] offset:35840\n"
"ds_read_b128 a[32:35],v116 offset:32768\n"
"ds_read_b128 a[36:39],v116 offset:33792\n"
"ds_read_b128 a[40:43],v116 offset:34816\n"
"ds_read_b128 a[44:47],v116 offset:35840\n"
"s_waitcnt lgkmcnt(0)\n"
"v_mfma_f32_16x16x32_bf16 v[20:23],a[80:83],a[32:35],v[104:107]\n"
"s_add_u32 s64,s64,16384\n"
"v_max3_f32 v118,v118,v29,v30\n"
"v_mfma_f32_16x16x32_bf16 v[20:23],a[84:87],a[36:39],v[20:23]\n"
"v_max_f32 v118,v118,v31\n"
"v_mov_b32 v120,v118\n"
"v_mfma_f32_16x16x32_bf16 v[20:23],a[88:91],a[40:43],v[20:23]\n"
"v_max3_f32 v119,v0,v1,v2\n"
"v_max3_f32 v119,v119,v3,v4\n"
"v_mfma_f32_16x16x32_bf16 v[20:23],a[92:95],a[44:47],v[20:23]\n"
"buffer_store_dwordx4 a[224:227],v112,s[24:27],s56 offen offset:0 sc1\n"
"v_max3_f32 v119,v119,v5,v6\n"
"v_mfma_f32_16x16x32_bf16 v[16:19],a[76:79],a[44:47],v[104:107]\n"
"v_max_f32 v119,v119,v7\n"
"v_cmp_gt_f32 s[28:29],s16,v119\n"
"v_mfma_f32_16x16x32_bf16 v[16:19],a[72:75],a[40:43],v[16:19]\n"
"v_max_f32 v118,v118,v119\n"
"v_max3_f32 v119,v8,v9,v10\n"
"v_mfma_f32_16x16x32_bf16 v[16:19],a[68:71],a[36:39],v[16:19]\n"
"v_max3_f32 v119,v119,v11,v12\n"
"v_max3_f32 v119,v119,v13,v14\n"
"v_mfma_f32_16x16x32_bf16 v[16:19],a[64:67],a[32:35],v[16:19]\n"
"s_and_b32 s28,s28,0xffff\n"
"v_max_f32 v119,v119,v15\n"
"v_min_f32 v120,v120,v119\n"
"v_max_f32 v118,v118,v119\n"
"s_nop 3\n"
"v_max3_f32 v119,v16,v17,v18\n"
"v_max3_f32 v119,v119,v19,v20\n"
"v_max3_f32 v119,v119,v21,v22\n"
"v_max_f32 v119,v119,v23\n"
"v_min_f32 v120,v120,v119\n"
"v_max_f32 v118,v118,v119\n"
"v_cmp_gt_f32 s[96:97],s16,v120\n"
"v_cmp_lt_f32 vcc,s93,v118\n"
"s_or_b32 s96,s96,s28\n"
"s_or_b64 s[96:97],s[96:97],vcc\n"
"s_cbranch_scc1 .Lfk_cs_z3\n"
".Lfk_cr_z3:\n"
"s_waitcnt vmcnt(18) lgkmcnt(0)\n"
"s_barrier\n"
"v_mfma_f32_16x16x32_bf16 v[48:51],a[96:99],a[32:35],v[104:107]\n"
"v_add_f32 v40,0x80000000,v126\n"
"v_min_f32 v40,v40,v100\n"
"v_add_f32 v41,0xf149f2ca,v126\n"
"v_min_f32 v41,v41,v101\n"
"v_mfma_f32_16x16x32_bf16 v[48:51],a[100:103],a[36:39],v[48:51]\n"
"v_add_f32 v42,0xf1c9f2ca,v126\n"
"v_min_f32 v42,v42,v102\n"
"v_add_f32 v43,0xf2177617,v126\n"
"v_min_f32 v43,v43,v103\n"
"v_mfma_f32_16x16x32_bf16 v[48:51],a[104:107],a[40:43],v[48:51]\n"
"v_add_f32 v44,0xf349f2ca,v126\n"
"v_min_f32 v44,v44,v100\n"
"v_add_f32 v45,0xf35691f6,v126\n"
"v_min_f32 v45,v45,v101\n"
"v_mfma_f32_16x16x32_bf16 v[48:51],a[108:111],a[44:47],v[48:51]\n"
"v_add_f32 v46,0xf3633123,v126\n"
"v_min_f32 v46,v46,v102\n"
"v_add_f32 v47,0xf36fd050,v126\n"
"v_min_f32 v47,v47,v103\n"
"v_mfma_f32_16x16x32_bf16 v[52:55],a[124:127],a[44:47],v[104:107]\n"
"s_add_u32 m0,s55,4096\n"
"v_exp_f32 v0,v0\n"
"s_add_u32 s54,s54,s22\n"
"v_mfma_f32_16x16x32_bf16 v[52:55],a[120:123],a[40:43],v[52:55]\n"
"buffer_store_dwordx4 a[160:163],v112,s[24:27],s3 offen offset:0 sc1\n"
"buffer_load_dwordx4 v115,s[32:35],s54 offen sc1 lds\n"
"v_exp_f32 v1,v1\n"
"v_mfma_f32_16x16x32_bf16 v[52:55],a[116:119],a[36:39],v[52:55]\n"
"s_add_u32 m0,s67,16384\n"
"v_exp_f32 v2,v2\n"
"v_mfma_f32_16x16x32_bf16 v[52:55],a[112:115],a[32:35],v[52:55]\n"
"buffer_load_dwordx4 v112,s[24:27],s64 offen offset:0 sc1 lds\n"
"buffer_load_dwordx4 v112,s[24:27],s64 offen offset:1024 sc1 lds\n"
"v_exp_f32 v3,v3\n"
"v_exp_f32 v4,v4\n"
"v_mfma_f32_16x16x32_bf16 v[60:63],a[112:115],a[48:51],v[108:111]\n"
"buffer_load_dwordx4 v112,s[24:27],s64 offen offset:2048 sc1 lds\n"
"v_exp_f32 v5,v5\n"
"v_mfma_f32_16x16x32_bf16 v[60:63],a[116:119],a[52:55],v[60:63]\n"
"ds_read_b128 a[64:67],v113 offset:49152\n"
"ds_read_b128 a[68:71],v113 offset:50176\n"
"v_exp_f32 v6,v6\n"
"v_mfma_f32_16x16x32_bf16 v[60:63],a[120:123],a[56:59],v[60:63]\n"
"ds_read_b128 a[72:75],v113 offset:51200\n"
"ds_read_b128 a[76:79],v113 offset:52224\n"
"v_exp_f32 v7,v7\n"
"v_mfma_f32_16x16x32_bf16 v[60:63],a[124:127],a[60:63],v[60:63]\n"
"ds_read_b128 a[80:83],v113 offset:53248\n"
"ds_read_b128 a[84:87],v113 offset:54272\n"
"v_exp_f32 v8,v8\n"
"v_mfma_f32_16x16x32_bf16 v[56:59],a[108:111],a[60:63],v[108:111]\n"
"ds_read_b128 a[88:91],v113 offset:55296\n"
"ds_read_b128 a[92:95],v113 offset:56320\n"
"v_exp_f32 v9,v9\n"
"v_mfma_f32_16x16x32_bf16 v[56:59],a[104:107],a[56:59],v[56:59]\n"
"buffer_load_dwordx4 v112,s[24:27],s64 offen offset:3072 sc1 lds\n"
"v_mfma_f32_16x16x32_bf16 v[56:59],a[100:103],a[52:55],v[56:59]\n"
"s_add_u32 m0,s67,20480\n"
"v_exp_f32 v10,v10\n"
"v_mfma_f32_16x16x32_bf16 v[56:59],a[96:99],a[48:51],v[56:59]\n"
"buffer_load_dwordx4 v117,s[24:27],s64 offen offset:0 sc1 lds\n"
"buffer_load_dwordx4 v117,s[24:27],s64 offen offset:1024 sc1 lds\n"
"v_exp_f32 v11,v11\n"
"v_exp_f32 v12,v12\n"
"v_mfma_f32_16x16x32_bf16 v[40:43],a[96:99],a[16:19],v[40:43]\n"
"buffer_load_dwordx4 v117,s[24:27],s64 offen offset:2048 sc1 lds\n"
"v_exp_f32 v13,v13\n"
"v_mfma_f32_16x16x32_bf16 v[40:43],a[100:103],a[20:23],v[40:43]\n"
"buffer_store_dwordx4 a[164:167],v112,s[24:27],s3 offen offset:1024 sc1\n"
"v_cvt_pk_bf16_f32 v64,v0,v1\n"
"v_exp_f32 v14,v14\n"
"v_mfma_f32_16x16x32_bf16 v[40:43],a[104:107],a[24:27],v[40:43]\n"
"buffer_load_dwordx4 v117,s[24:27],s64 offen offset:3072 sc1 lds\n"
"v_mfma_f32_16x16x32_bf16 v[40:43],a[108:111],a[28:31],v[40:43]\n"
"s_add_u32 s64,s64,16384\n"
"v_exp_f32 v15,v15\n"
"v_cvt_pk_bf16_f32 v65,v2,v3\n"
"v_cvt_pk_bf16_f32 v66,v4,v5\n"
"v_exp_f32 v16,v16\n"
"v_mfma_f32_16x16x32_bf16 v[44:47],a[124:127],a[28:31],v[44:47]\n"
"buffer_store_dwordx4 a[228:231],v112,s[24:27],s56 offen offset:1024 sc1\n"
"v_cvt_pk_bf16_f32 v67,v6,v7\n"
"v_mfma_f32_16x16x32_bf16 v[44:47],a[120:123],a[24:27],v[44:47]\n"
"v_exp_f32 v17,v17\n"
"v_cvt_pk_bf16_f32 v68,v8,v9\n"
"v_cvt_pk_bf16_f32 v69,v10,v11\n"
"v_mfma_f32_16x16x32_bf16 v[44:47],a[116:119],a[20:23],v[44:47]\n"
"v_exp_f32 v18,v18\n"
"v_cvt_pk_bf16_f32 v70,v12,v13\n"
"v_cvt_pk_bf16_f32 v71,v14,v15\n"
"v_mfma_f32_16x16x32_bf16 v[44:47],a[112:115],a[16:19],v[44:47]\n"
"v_exp_f32 v19,v19\n"
"v_cvt_pk_bf16_f32 v72,v16,v17\n"
"v_max3_f32 v118,v48,v49,v50\n"
"v_exp_f32 v20,v20\n"
"v_cvt_pk_bf16_f32 v73,v18,v19\n"
"v_exp_f32 v21,v21\n"
"ds_read_b128 a[0:3],v116 offset:40960\n"
"ds_read_b128 a[4:7],v116 offset:41984\n"
"ds_read_b128 a[8:11],v116 offset:43008\n"
"ds_read_b128 a[12:15],v116 offset:44032\n"
"v_exp_f32 v22,v22\n"
"v_exp_f32 v23,v23\n"
"v_exp_f32 v24,v24\n"
"v_cvt_pk_bf16_f32 v75,v22,v23\n"
"v_exp_f32 v25,v25\n"
"v_exp_f32 v26,v26\n"
"v_cvt_pk_bf16_f32 v76,v24,v25\n"
"v_exp_f32 v27,v27\n"
"v_exp_f32 v28,v28\n"
"v_cvt_pk_bf16_f32 v77,v26,v27\n"
"v_exp_f32 v29,v29\n"
"v_exp_f32 v30,v30\n"
"v_cvt_pk_bf16_f32 v78,v28,v29\n"
"v_exp_f32 v31,v31\n"
"v_cvt_pk_bf16_f32 v74,v20,v21\n"
"v_cvt_pk_bf16_f32 v79,v30,v31\n"
"v_max3_f32 v118,v118,v51,v52\n"
"v_max3_f32 v118,v118,v53,v54\n"
"v_max3_f32 v118,v118,v55,v60\n"
"v_max3_f32 v118,v118,v61,v62\n"
"v_max3_f32 v118,v118,v63,v56\n"
"v_max3_f32 v118,v118,v57,v58\n"
"v_max3_f32 v118,v118,v59,v40\n"
"v_max3_f32 v118,v118,v41,v42\n"
"v_max3_f32 v118,v118,v43,v44\n"
"v_max3_f32 v118,v118,v45,v46\n"
"v_max_f32 v118,v118,v47\n"
"v_cmp_lt_f32 vcc,s93,v118\n"
"s_nop 0\n"
"s_cbranch_vccnz .Lfk_cs_z0\n"
".Lfk_cr_z0:\n"
"s_waitcnt vmcnt(26) lgkmcnt(0)\n"
"s_barrier\n"
"v_mfma_f32_16x16x32_bf16 v[24:27],a[64:67],a[48:51],v[108:111]\n"
"ds_read_b128 a[96:99],v114 offset:40960\n"
"ds_read_b128 a[100:103],v114 offset:41984\n"
"v_mfma_f32_16x16x32_bf16 v[24:27],a[68:71],a[52:55],v[24:27]\n"
"ds_read_b128 a[104:107],v114 offset:43008\n"
"ds_read_b128 a[108:111],v114 offset:44032\n"
"v_mfma_f32_16x16x32_bf16 v[24:27],a[72:75],a[56:59],v[24:27]\n"
"ds_read_b128 a[112:115],v114 offset:45056\n"
"ds_read_b128 a[116:119],v114 offset:46080\n"
"v_mfma_f32_16x16x32_bf16 v[24:27],a[76:79],a[60:63],v[24:27]\n"
"ds_read_b128 a[120:123],v114 offset:47104\n"
"ds_read_b128 a[124:127],v114 offset:48128\n"
"v_mfma_f32_16x16x32_bf16 v[28:31],a[92:95],a[60:63],v[108:111]\n"
"v_add_f32 v16,0x80000000,v126\n"
"v_min_f32 v16,v16,v104\n"
"v_mfma_f32_16x16x32_bf16 v[28:31],a[88:91],a[56:59],v[28:31]\n"
"v_add_f32 v17,0xf149f2ca,v126\n"
"v_min_f32 v17,v17,v105\n"
"v_mfma_f32_16x16x32_bf16 v[28:31],a[84:87],a[52:55],v[28:31]\n"
"v_add_f32 v18,0xf1c9f2ca,v126\n"
"v_min_f32 v18,v18,v106\n"
"v_mfma_f32_16x16x32_bf16 v[28:31],a[80:83],a[48:51],v[28:31]\n"
"v_add_f32 v20,0xf349f2ca,v126\n"
"v_min_f32 v20,v20,v104\n"
"v_add_f32 v21,0xf35691f6,v126\n"
"v_min_f32 v21,v21,v105\n"
"v_add_f32 v22,0xf3633123,v126\n"
"v_min_f32 v22,v22,v106\n"
"v_add_f32 v23,0xf36fd050,v126\n"
"v_min_f32 v23,v23,v107\n"
"v_add_f32 v19,0xf2177617,v126\n"
"v_min_f32 v19,v19,v107\n"
"v_mfma_f32_16x16x32_bf16 v[20:23],a[80:83],a[32:35],v[20:23]\n"
"s_add_u32 m0,s55,8192\n"
"v_exp_f32 v40,v40\n"
"v_mfma_f32_16x16x32_bf16 v[20:23],a[84:87],a[36:39],v[20:23]\n"
"s_add_u32 s54,s54,s22\n"
"v_exp_f32 v41,v41\n"
"v_mfma_f32_16x16x32_bf16 v[20:23],a[88:91],a[40:43],v[20:23]\n"
"v_exp_f32 v42,v42\n"
"v_mfma_f32_16x16x32_bf16 v[20:23],a[92:95],a[44:47],v[20:23]\n"
"v_exp_f32 v43,v43\n"
"v_mfma_f32_16x16x32_bf16 v[16:19],a[76:79],a[44:47],v[16:19]\n"
"v_exp_f32 v44,v44\n"
"v_mfma_f32_16x16x32_bf16 v[16:19],a[72:75],a[40:43],v[16:19]\n"
"v_exp_f32 v45,v45\n"
"v_mfma_f32_16x16x32_bf16 v[16:19],a[68:71],a[36:39],v[16:19]\n"
"v_exp_f32 v46,v46\n"
"v_mfma_f32_16x16x32_bf16 v[16:19],a[64:67],a[32:35],v[16:19]\n"
"s_waitcnt lgkmcnt(0)\n"
"v_exp_f32 v47,v47\n"
"v_mfma_f32_16x16x32_bf16 v[128:131],a[96:99],v[64:67],0\n"
"v_exp_f32 v48,v48\n"
"v_mfma_f32_16x16x32_bf16 v[160:163],a[96:99],v[68:71],0\n"
"v_exp_f32 v49,v49\n"
"v_mfma_f32_16x16x32_bf16 v[192:195],a[96:99],v[72:75],0\n"
"ds_read_b128 a[64:67],v113 offset:32768\n"
"ds_read_b128 a[68:71],v113 offset:33792\n"
"v_mfma_f32_16x16x32_bf16 v[224:227],a[96:99],v[76:79],0\n"
"v_exp_f32 v50,v50\n"
"v_mfma_f32_16x16x32_bf16 a[140:143],a[144:147],v[76:79],0\n"
"ds_read_b128 a[72:75],v113 offset:34816\n"
"ds_read_b128 a[76:79],v113 offset:35840\n"
"v_mfma_f32_16x16x32_bf16 v[228:231],a[100:103],v[76:79],0\n"
"v_exp_f32 v51,v51\n"
"v_mfma_f32_16x16x32_bf16 v[132:135],a[100:103],v[64:67],0\n"
"ds_read_b128 a[80:83],v113 offset:36864\n"
"ds_read_b128 a[84:87],v113 offset:37888\n"
"v_mfma_f32_16x16x32_bf16 v[164:167],a[100:103],v[68:71],0\n"
"v_exp_f32 v52,v52\n"
"v_mfma_f32_16x16x32_bf16 v[196:199],a[100:103],v[72:75],0\n"
"v_exp_f32 v53,v53\n"
"v_mfma_f32_16x16x32_bf16 a[136:139],a[144:147],v[72:75],0\n"
"ds_read_b128 a[88:91],v113 offset:38912\n"
"ds_read_b128 a[92:95],v113 offset:39936\n"
"v_mfma_f32_16x16x32_bf16 v[200:203],a[104:107],v[72:75],0\n"
"v_exp_f32 v54,v54\n"
"v_mfma_f32_16x16x32_bf16 v[232:235],a[104:107],v[76:79],0\n"
"buffer_load_dwordx4 v115,s[32:35],s54 offen sc1 lds\n"
"v_mfma_f32_16x16x32_bf16 v[136:139],a[104:107],v[64:67],0\n"
"s_add_u32 m0,s67,49152\n"
"v_exp_f32 v55,v55\n"
"v_mfma_f32_16x16x32_bf16 v[168:171],a[104:107],v[68:71],0\n"
"buffer_load_dwordx4 v112,s[24:27],s64 offen offset:0 sc1 lds\n"
"v_mfma_f32_16x16x32_bf16 a[132:135],a[144:147],v[68:71],0\n"
"v_exp_f32 v56,v56\n"
"v_mfma_f32_16x16x32_bf16 v[172:175],a[108:111],v[68:71],0\n"
"buffer_load_dwordx4 v112,s[24:27],s64 offen offset:1024 sc1 lds\n"
"v_mfma_f32_16x16x32_bf16 v[204:207],a[108:111],v[72:75],0\n"
"v_exp_f32 v57,v57\n"
"v_mfma_f32_16x16x32_bf16 v[236:239],a[108:111],v[76:79],0\n"
"buffer_load_dwordx4 v112,s[24:27],s64 offen offset:2048 sc1 lds\n"
"v_mfma_f32_16x16x32_bf16 v[140:143],a[108:111],v[64:67],0\n"
"v_exp_f32 v58,v58\n"
"v_mfma_f32_16x16x32_bf16 a[128:131],a[144:147],v[64:67],0\n"
"buffer_load_dwordx4 v112,s[24:27],s64 offen offset:3072 sc1 lds\n"
"v_mfma_f32_16x16x32_bf16 v[144:147],a[112:115],v[64:67],0\n"
"s_add_u32 m0,s67,53248\n"
"v_exp_f32 v59,v59\n"
"v_mfma_f32_16x16x32_bf16 v[176:179],a[112:115],v[68:71],0\n"
"buffer_load_dwordx4 v117,s[24:27],s64 offen offset:0 sc1 lds\n"
"v_mfma_f32_16x16x32_bf16 v[208:211],a[112:115],v[72:75],0\n"
"v_exp_f32 v60,v60\n"
"v_mfma_f32_16x16x32_bf16 v[240:243],a[112:115],v[76:79],0\n"
"buffer_load_dwordx4 v117,s[24:27],s64 offen offset:1024 sc1 lds\n"
"v_mfma_f32_16x16x32_bf16 v[244:247],a[116:119],v[76:79],0\n"
"v_exp_f32 v61,v61\n"
"v_mfma_f32_16x16x32_bf16 v[148:151],a[116:119],v[64:67],0\n"
"buffer_load_dwordx4 v117,s[24:27],s64 offen offset:2048 sc1 lds\n"
"v_mfma_f32_16x16x32_bf16 v[180:183],a[116:119],v[68:71],0\n"
"v_exp_f32 v62,v62\n"
"v_mfma_f32_16x16x32_bf16 v[212:215],a[116:119],v[72:75],0\n"
"buffer_load_dwordx4 v117,s[24:27],s64 offen offset:3072 sc1 lds\n"
"v_mfma_f32_16x16x32_bf16 v[216:219],a[120:123],v[72:75],0\n"
"s_add_u32 s64,s64,16384\n"
"v_exp_f32 v63,v63\n"
"v_mfma_f32_16x16x32_bf16 v[248:251],a[120:123],v[76:79],0\n"
"buffer_store_dwordx4 a[232:235],v112,s[24:27],s56 offen offset:2048 sc1\n"
"s_and_b32 s67,s67,8192\n"
"v_mfma_f32_16x16x32_bf16 v[152:155],a[120:123],v[64:67],0\n"
"buffer_store_dwordx4 a[168:171],v112,s[24:27],s3 offen offset:2048 sc1\n"
"buffer_store_dwordx4 a[172:175],v112,s[24:27],s3 offen offset:3072 sc1\n"
"v_mfma_f32_16x16x32_bf16 v[184:187],a[120:123],v[68:71],0\n"
"s_sub_u32 s3,s3,16384\n"
"buffer_store_dwordx4 a[176:179],v112,s[24:27],s3 offen offset:0 sc1\n"
"v_mfma_f32_16x16x32_bf16 v[188:191],a[124:127],v[68:71],0\n"
"v_cvt_pk_bf16_f32 v84,v40,v41\n"
"v_cvt_pk_bf16_f32 v85,v42,v43\n"
"v_mfma_f32_16x16x32_bf16 v[220:223],a[124:127],v[72:75],0\n"
"v_cvt_pk_bf16_f32 v86,v44,v45\n"
"v_cvt_pk_bf16_f32 v87,v46,v47\n"
"v_mfma_f32_16x16x32_bf16 v[252:255],a[124:127],v[76:79],0\n"
"v_cvt_pk_bf16_f32 v88,v48,v49\n"
"v_cvt_pk_bf16_f32 v89,v50,v51\n"
"v_mfma_f32_16x16x32_bf16 v[156:159],a[124:127],v[64:67],0\n"
"v_cvt_pk_bf16_f32 v90,v52,v53\n"
"v_cvt_pk_bf16_f32 v91,v54,v55\n"
"v_cvt_pk_bf16_f32 v92,v56,v57\n"
"ds_read_b128 a[16:19],v116 offset:8192\n"
"ds_read_b128 a[20:23],v116 offset:9216\n"
"ds_read_b128 a[24:27],v116 offset:10240\n"
"ds_read_b128 a[28:31],v116 offset:11264\n"
"v_cvt_pk_bf16_f32 v93,v58,v59\n"
"v_cvt_pk_bf16_f32 v94,v60,v61\n"
"v_cvt_pk_bf16_f32 v95,v62,v63\n"
"v_max3_f32 v118,v24,v25,v26\n"
"v_max3_f32 v118,v118,v27,v28\n"
"v_max3_f32 v118,v118,v29,v30\n"
"v_max3_f32 v118,v118,v31,v20\n"
"v_max3_f32 v118,v118,v21,v22\n"
"v_max3_f32 v118,v118,v23,v16\n"
"v_max3_f32 v118,v118,v17,v18\n"
"v_max_f32 v118,v118,v19\n"
"v_cmp_lt_f32 vcc,s93,v118\n"
"s_nop 0\n"
"s_cbranch_vccnz .Lfk_cs_z1\n"
".Lfk_cr_z1:\n"
"v_mov_b32 v113,v112\n"
"s_waitcnt vmcnt(21) lgkmcnt(0)\n"
"s_barrier\n"
"v_add_f32 v56,0x80000000,v126\n"
"v_min_f32 v56,v56,v108\n"
"v_add_f32 v57,0xf149f2ca,v126\n"
"v_min_f32 v57,v57,v109\n"
"v_add_f32 v58,0xf1c9f2ca,v126\n"
"v_min_f32 v58,v58,v110\n"
"v_add_f32 v59,0xf2177617,v126\n"
"v_min_f32 v59,v59,v111\n"
"v_add_f32 v60,0xf349f2ca,v126\n"
"v_min_f32 v60,v60,v108\n"
"v_add_f32 v61,0xf35691f6,v126\n"
"v_min_f32 v61,v61,v109\n"
"v_add_f32 v62,0xf3633123,v126\n"
"v_min_f32 v62,v62,v110\n"
"v_add_f32 v63,0xf36fd050,v126\n"
"v_min_f32 v63,v63,v111\n"
"v_exp_f32 v16,v16\n"
"v_mfma_f32_16x16x32_bf16 v[56:59],a[64:67],a[48:51],v[56:59]\n"
"s_add_u32 m0,s55,12288\n"
"ds_read_b128 a[96:99],v114 offset:8192\n"
"ds_read_b128 a[100:103],v114 offset:9216\n"
"ds_read_b128 a[104:107],v114 offset:10240\n"
"v_exp_f32 v17,v17\n"
"v_mfma_f32_16x16x32_bf16 v[56:59],a[68:71],a[52:55],v[56:59]\n"
"s_add_u32 s54,s54,s22\n"
"ds_read_b128 a[108:111],v114 offset:11264\n"
"ds_read_b128 a[112:115],v114 offset:12288\n"
"ds_read_b128 a[116:119],v114 offset:13312\n"
"v_mfma_f32_16x16x32_bf16 v[56:59],a[72:75],a[56:59],v[56:59]\n"
"ds_read_b128 a[120:123],v114 offset:14336\n"
"ds_read_b128 a[124:127],v114 offset:15360\n"
"buffer_load_dwordx4 v115,s[32:35],s54 offen sc1 lds\n"
"v_cvt_pk_bf16_f32 v72,v16,v17\n"
"v_exp_f32 v18,v18\n"
"v_mfma_f32_16x16x32_bf16 v[56:59],a[76:79],a[60:63],v[56:59]\n"
"ds_read_b128 a[32:35],v116 offset:24576\n"
"ds_read_b128 a[36:39],v116 offset:25600\n"
"ds_read_b128 a[40:43],v116 offset:26624\n"
"v_mfma_f32_16x16x32_bf16 v[60:63],a[92:95],a[60:63],v[60:63]\n"
"buffer_store_dwordx4 a[180:183],v112,s[24:27],s3 offen offset:1024 sc1\n"
"ds_read_b128 a[44:47],v116 offset:27648\n"
"v_mfma_f32_16x16x32_bf16 v[60:63],a[88:91],a[56:59],v[60:63]\n"
"s_add_u32 m0,s67,32768\n"
"v_exp_f32 v19,v19\n"
"v_mfma_f32_16x16x32_bf16 v[60:63],a[84:87],a[52:55],v[60:63]\n"
"buffer_load_dwordx4 v112,s[24:27],s64 offen offset:0 sc1 lds\n"
"v_cvt_pk_bf16_f32 v73,v18,v19\n"
"v_exp_f32 v20,v20\n"
"v_mfma_f32_16x16x32_bf16 v[60:63],a[80:83],a[48:51],v[60:63]\n"
"ds_read_b128 a[64:67],v113 offset:0\n"
"ds_read_b128 a[68:71],v113 offset:1024\n"
"ds_read_b128 a[72:75],v113 offset:2048\n"
"s_waitcnt lgkmcnt(7)\n"
"v_mfma_f32_16x16x32_bf16 v[160:163],a[96:99],v[84:87],v[160:163]\n"
"ds_read_b128 a[76:79],v113 offset:3072\n"
"ds_read_b128 a[80:83],v113 offset:4096\n"
"ds_read_b128 a[84:87],v113 offset:5120\n"
"ds_read_b128 a[88:91],v113 offset:6144\n"
"ds_read_b128 a[92:95],v113 offset:7168\n"
"v_exp_f32 v21,v21\n"
"v_mfma_f32_16x16x32_bf16 a[132:135],a[144:147],v[84:87],a[132:135]\n"
"buffer_load_dwordx4 v112,s[24:27],s64 offen offset:1024 sc1 lds\n"
"v_max3_f32 v118,v56,v57,v58\n"
"v_mfma_f32_16x16x32_bf16 v[164:167],a[100:103],v[84:87],v[164:167]\n"
"v_exp_f32 v22,v22\n"
"v_cvt_pk_bf16_f32 v74,v20,v21\n"
"ds_read_b128 a[48:51],v116 offset:57344\n"
"ds_read_b128 a[52:55],v116 offset:58368\n"
"v_mfma_f32_16x16x32_bf16 v[168:171],a[104:107],v[84:87],v[168:171]\n"
"v_max3_f32 v118,v118,v59,v60\n"
"v_max3_f32 v118,v118,v61,v62\n"
"v_max_f32 v118,v118,v63\n"
"v_exp_f32 v23,v23\n"
"v_mfma_f32_16x16x32_bf16 v[172:175],a[108:111],v[84:87],v[172:175]\n"
"ds_read_b128 a[56:59],v116 offset:59392\n"
"buffer_load_dwordx4 v112,s[24:27],s64 offen offset:2048 sc1 lds\n"
"ds_read_b128 a[60:63],v116 offset:60416\n"
"v_mfma_f32_16x16x32_bf16 v[176:179],a[112:115],v[84:87],v[176:179]\n"
"v_cvt_pk_bf16_f32 v75,v22,v23\n"
"v_accvgpr_read_b32 v122,a128\n"
"v_rcp_f32 v122,v122\n"
"v_cmp_lt_f32 vcc,s93,v118\n"
"v_pk_mul_f32 v[128:129],v[128:129],v[122:123] op_sel_hi:[1,0]\n"
"v_mfma_f32_16x16x32_bf16 v[180:183],a[116:119],v[84:87],v[180:183]\n"
"buffer_store_dwordx4 a[184:187],v112,s[24:27],s3 offen offset:2048 sc1\n"
"v_exp_f32 v24,v24\n"
"v_pk_mul_f32 v[130:131],v[130:131],v[122:123] op_sel_hi:[1,0]\n"
"v_pk_mul_f32 v[132:133],v[132:133],v[122:123] op_sel_hi:[1,0]\n"
"v_pk_mul_f32 v[134:135],v[134:135],v[122:123] op_sel_hi:[1,0]\n"
"v_mfma_f32_16x16x32_bf16 v[184:187],a[120:123],v[84:87],v[184:187]\n"
"buffer_load_dwordx4 v112,s[24:27],s64 offen offset:3072 sc1 lds\n"
"v_cvt_pk_bf16_f32 v128,v128,v129\n"
"v_cvt_pk_bf16_f32 v129,v130,v131\n"
"v_cvt_pk_bf16_f32 v130,v132,v133\n"
"v_cvt_pk_bf16_f32 v131,v134,v135\n"
"v_mfma_f32_16x16x32_bf16 v[188:191],a[124:127],v[84:87],v[188:191]\n"
"buffer_store_dwordx4 v[128:131],v112,s[24:27],s70 offen offset:0 sc1\n"
"v_pk_mul_f32 v[136:137],v[136:137],v[122:123] op_sel_hi:[1,0]\n"
"v_mfma_f32_16x16x32_bf16 v[192:195],a[96:99],v[88:91],v[192:195]\n"
"s_add_u32 m0,s67,36864\n"
"v_exp_f32 v25,v25\n"
"v_exp_f32 v27,v27\n"
"v_cvt_pk_bf16_f32 v76,v24,v25\n"
"v_cvt_pk_bf16_f32 v136,v136,v137\n"
"v_pk_mul_f32 v[138:139],v[138:139],v[122:123] op_sel_hi:[1,0]\n"
"v_pk_mul_f32 v[140:141],v[140:141],v[122:123] op_sel_hi:[1,0]\n"
"v_pk_mul_f32 v[142:143],v[142:143],v[122:123] op_sel_hi:[1,0]\n"
"v_mfma_f32_16x16x32_bf16 a[136:139],a[144:147],v[88:91],a[136:139]\n"
"buffer_load_dwordx4 v117,s[24:27],s64 offen offset:0 sc1 lds\n"
"v_cvt_pk_bf16_f32 v137,v138,v139\n"
"v_mfma_f32_16x16x32_bf16 v[196:199],a[100:103],v[88:91],v[196:199]\n"
"v_cvt_pk_bf16_f32 v138,v140,v141\n"
"v_cvt_pk_bf16_f32 v139,v142,v143\n"
"v_exp_f32 v26,v26\n"
"v_mfma_f32_16x16x32_bf16 v[200:203],a[104:107],v[88:91],v[200:203]\n"
"buffer_store_dwordx4 v[136:139],v112,s[24:27],s70 offen offset:1024 sc1\n"
"v_cvt_pk_bf16_f32 v77,v26,v27\n"
"v_pk_mul_f32 v[144:145],v[144:145],v[122:123] op_sel_hi:[1,0]\n"
"v_pk_mul_f32 v[146:147],v[146:147],v[122:123] op_sel_hi:[1,0]\n"
"v_pk_mul_f32 v[148:149],v[148:149],v[122:123] op_sel_hi:[1,0]\n"
"v_pk_mul_f32 v[150:151],v[150:151],v[122:123] op_sel_hi:[1,0]\n"
"v_exp_f32 v28,v28\n"
"v_mfma_f32_16x16x32_bf16 v[204:207],a[108:111],v[88:91],v[204:207]\n"
"buffer_load_dwordx4 v117,s[24:27],s64 offen offset:1024 sc1 lds\n"
"buffer_store_dwordx4 a[188:191],v112,s[24:27],s3 offen offset:3072 sc1\n"
"v_mfma_f32_16x16x32_bf16 v[208:211],a[112:115],v[88:91],v[208:211]\n"
"s_or_b32 s3,s3,0x3c000\n"
"v_cvt_pk_bf16_f32 v144,v144,v145\n"
"v_cvt_pk_bf16_f32 v145,v146,v147\n"
"v_cvt_pk_bf16_f32 v146,v148,v149\n"
"v_cvt_pk_bf16_f32 v147,v150,v151\n"
"v_mfma_f32_16x16x32_bf16 v[212:215],a[116:119],v[88:91],v[212:215]\n"
"buffer_store_dwordx4 v[144:147],v112,s[24:27],s70 offen offset:2048 sc1\n"
"v_pk_mul_f32 v[152:153],v[152:153],v[122:123] op_sel_hi:[1,0]\n"
"v_pk_mul_f32 v[154:155],v[154:155],v[122:123] op_sel_hi:[1,0]\n"
"v_mfma_f32_16x16x32_bf16 v[216:219],a[120:123],v[88:91],v[216:219]\n"
"buffer_load_dwordx4 v117,s[24:27],s64 offen offset:2048 sc1 lds\n"
"v_exp_f32 v29,v29\n"
"v_exp_f32 v31,v31\n"
"v_cvt_pk_bf16_f32 v78,v28,v29\n"
"v_pk_mul_f32 v[156:157],v[156:157],v[122:123] op_sel_hi:[1,0]\n"
"v_pk_mul_f32 v[158:159],v[158:159],v[122:123] op_sel_hi:[1,0]\n"
"v_mfma_f32_16x16x32_bf16 v[220:223],a[124:127],v[88:91],v[220:223]\n"
"v_cvt_pk_bf16_f32 v152,v152,v153\n"
"v_cvt_pk_bf16_f32 v153,v154,v155\n"
"v_cvt_pk_bf16_f32 v154,v156,v157\n"
"v_cvt_pk_bf16_f32 v155,v158,v159\n"
"v_mfma_f32_16x16x32_bf16 v[224:227],a[96:99],v[92:95],v[224:227]\n"
"buffer_store_dwordx4 v[152:155],v112,s[24:27],s70 offen offset:3072 sc1\n"
"v_accvgpr_read_b32 v122,a132\n"
"v_mfma_f32_16x16x32_bf16 a[140:143],a[144:147],v[92:95],a[140:143]\n"
"s_add_u32 s70,s70,262144\n"
"buffer_load_dwordx4 v117,s[24:27],s64 offen offset:3072 sc1 lds\n"
"v_mfma_f32_16x16x32_bf16 v[228:231],a[100:103],v[92:95],v[228:231]\n"
"s_add_u32 s64,s64,16384\n"
"v_exp_f32 v30,v30\n"
"v_rcp_f32 v122,v122\n"
"v_cvt_pk_bf16_f32 v79,v30,v31\n"
"v_pk_mul_f32 v[160:161],v[160:161],v[122:123] op_sel_hi:[1,0]\n"
"v_pk_mul_f32 v[162:163],v[162:163],v[122:123] op_sel_hi:[1,0]\n"
"v_pk_mul_f32 v[164:165],v[164:165],v[122:123] op_sel_hi:[1,0]\n"
"v_pk_mul_f32 v[166:167],v[166:167],v[122:123] op_sel_hi:[1,0]\n"
"v_mfma_f32_16x16x32_bf16 v[232:235],a[104:107],v[92:95],v[232:235]\n"
"buffer_store_dwordx4 a[236:239],v112,s[24:27],s56 offen offset:3072 sc1\n"
"v_cvt_pk_bf16_f32 v160,v160,v161\n"
"v_cvt_pk_bf16_f32 v161,v162,v163\n"
"v_cvt_pk_bf16_f32 v162,v164,v165\n"
"v_cvt_pk_bf16_f32 v163,v166,v167\n"
"v_mfma_f32_16x16x32_bf16 v[236:239],a[108:111],v[92:95],v[236:239]\n"
"buffer_store_dwordx4 v[160:163],v112,s[24:27],s70 offen offset:0 sc1\n"
"v_pk_mul_f32 v[168:169],v[168:169],v[122:123] op_sel_hi:[1,0]\n"
"v_pk_mul_f32 v[170:171],v[170:171],v[122:123] op_sel_hi:[1,0]\n"
"v_pk_mul_f32 v[172:173],v[172:173],v[122:123] op_sel_hi:[1,0]\n"
"v_pk_mul_f32 v[174:175],v[174:175],v[122:123] op_sel_hi:[1,0]\n"
"v_mfma_f32_16x16x32_bf16 v[240:243],a[112:115],v[92:95],v[240:243]\n"
"v_cvt_pk_bf16_f32 v168,v168,v169\n"
"v_cvt_pk_bf16_f32 v169,v170,v171\n"
"v_cvt_pk_bf16_f32 v170,v172,v173\n"
"v_cvt_pk_bf16_f32 v171,v174,v175\n"
"v_mfma_f32_16x16x32_bf16 v[244:247],a[116:119],v[92:95],v[244:247]\n"
"buffer_store_dwordx4 v[168:171],v112,s[24:27],s70 offen offset:1024 sc1\n"
"v_pk_mul_f32 v[176:177],v[176:177],v[122:123] op_sel_hi:[1,0]\n"
"v_pk_mul_f32 v[178:179],v[178:179],v[122:123] op_sel_hi:[1,0]\n"
"v_pk_mul_f32 v[180:181],v[180:181],v[122:123] op_sel_hi:[1,0]\n"
"v_pk_mul_f32 v[182:183],v[182:183],v[122:123] op_sel_hi:[1,0]\n"
"v_mfma_f32_16x16x32_bf16 v[248:251],a[120:123],v[92:95],v[248:251]\n"
"v_cvt_pk_bf16_f32 v176,v176,v177\n"
"v_cvt_pk_bf16_f32 v177,v178,v179\n"
"v_cvt_pk_bf16_f32 v178,v180,v181\n"
"v_cvt_pk_bf16_f32 v179,v182,v183\n"
"v_mfma_f32_16x16x32_bf16 v[252:255],a[124:127],v[92:95],v[252:255]\n"
"buffer_store_dwordx4 v[176:179],v112,s[24:27],s70 offen offset:2048 sc1\n"
"v_pk_mul_f32 v[184:185],v[184:185],v[122:123] op_sel_hi:[1,0]\n"
"v_pk_mul_f32 v[186:187],v[186:187],v[122:123] op_sel_hi:[1,0]\n"
"v_pk_mul_f32 v[188:189],v[188:189],v[122:123] op_sel_hi:[1,0]\n"
"v_pk_mul_f32 v[190:191],v[190:191],v[122:123] op_sel_hi:[1,0]\n"
"v_cvt_pk_bf16_f32 v184,v184,v185\n"
"v_cvt_pk_bf16_f32 v185,v186,v187\n"
"v_cvt_pk_bf16_f32 v186,v188,v189\n"
"v_cvt_pk_bf16_f32 v187,v190,v191\n"
"buffer_store_dwordx4 v[184:187],v112,s[24:27],s70 offen offset:3072 sc1\n"
"s_add_u32 s70,s70,262144\n"
"s_cbranch_vccnz .Lfk_cs_z2\n"
".Lfk_cr_z2:\n"
"s_waitcnt vmcnt(35) lgkmcnt(0)\n"
"s_barrier\n"
"s_add_u32 s89,s89,1\n"
"s_branch .Lfk_winB\n"
".Lfk_ev:\n"
"s_xor_b32 s55,s55,16384\n"
"s_andn2_b32 s91,s55,16384\n"
"v_lshrrev_b32 v121,1,v112\n"
"v_add_u32 v121,s91,v121\n"
"ds_read_b64_tr_b16 a[160:161],v121 offset:0\n"
"ds_read_b64_tr_b16 a[162:163],v121 offset:512\n"
"s_add_u32 s56,s56,4096\n"
"s_swappc_b64 s[20:21],s[94:95]\n"
"s_xor_b32 s55,s55,16384\n"
"s_andn2_b32 s91,s55,16384\n"
"v_lshrrev_b32 v121,1,v112\n"
"v_add_u32 v121,s91,v121\n"
"ds_read_b64_tr_b16 a[164:165],v121 offset:4096\n"
"ds_read_b64_tr_b16 a[166:167],v121 offset:4608\n"
"ds_read_b64_tr_b16 a[168:169],v121 offset:8192\n"
"ds_read_b64_tr_b16 a[170:171],v121 offset:8704\n"
"ds_read_b64_tr_b16 a[172:173],v121 offset:12288\n"
"ds_read_b64_tr_b16 a[174:175],v121 offset:12800\n"
"ds_read_b64_tr_b16 a[176:177],v121 offset:0\n"
"ds_read_b64_tr_b16 a[178:179],v121 offset:512\n"
"buffer_store_dwordx2 a[154:155],off,s[24:27],s2 sc1\n"
"s_and_b32 s2,s2,0xfffffc00\n"
"s_mov_b32 s56,s10\n"
"v_accvgpr_write_b32 a240,0\n"
"v_accvgpr_write_b32 a241,0\n"
"v_accvgpr_write_b32 a242,0\n"
"v_accvgpr_write_b32 a243,0\n"
"v_accvgpr_write_b32 a244,0\n"
"v_accvgpr_write_b32 a245,0\n"
"v_accvgpr_write_b32 a246,0\n"
"v_accvgpr_write_b32 a247,0\n"
"v_accvgpr_write_b32 a248,0\n"
"v_accvgpr_write_b32 a249,0\n"
"v_accvgpr_write_b32 a250,0\n"
"v_accvgpr_write_b32 a251,0\n"
"v_accvgpr_write_b32 a252,0\n"
"v_accvgpr_write_b32 a253,0\n"
"v_accvgpr_write_b32 a254,0\n"
"v_accvgpr_write_b32 a255,0\n"
"s_mov_b64 s[32:33],s[36:37]\n"
"s_mov_b32 s54,-229376\n"
"s_swappc_b64 s[20:21],s[94:95]\n"
"s_xor_b32 s55,s55,16384\n"
"s_add_u32 s56,s56,131072\n"
"s_andn2_b32 s91,s55,16384\n"
"v_lshrrev_b32 v121,1,v112\n"
"v_add_u32 v121,s91,v121\n"
"ds_read_b64_tr_b16 a[180:181],v121 offset:20480\n"
"ds_read_b64_tr_b16 a[182:183],v121 offset:20992\n"
"ds_read_b64_tr_b16 a[184:185],v121 offset:24576\n"
"ds_read_b64_tr_b16 a[186:187],v121 offset:25088\n"
"ds_read_b64_tr_b16 a[188:189],v121 offset:28672\n"
"ds_read_b64_tr_b16 a[190:191],v121 offset:29184\n"
"ds_read_b64_tr_b16 a[192:193],v121 offset:0\n"
"ds_read_b64_tr_b16 a[194:195],v121 offset:512\n"
"buffer_load_dwordx2 a[150:151],v112,s[24:27],s2 offen sc1\n"
"s_add_u32 s82,s59,8192\n"
"buffer_store_dwordx2 a[154:155],off,s[24:27],s82 sc1\n"
"s_swappc_b64 s[20:21],s[94:95]\n"
"s_xor_b32 s55,s55,16384\n"
"s_add_u32 s56,s56,131072\n"
"s_andn2_b32 s91,s55,16384\n"
"v_lshrrev_b32 v121,1,v112\n"
"v_add_u32 v121,s91,v121\n"
"ds_read_b64_tr_b16 a[196:197],v121 offset:4096\n"
"ds_read_b64_tr_b16 a[198:199],v121 offset:4608\n"
"ds_read_b64_tr_b16 a[200:201],v121 offset:8192\n"
"ds_read_b64_tr_b16 a[202:203],v121 offset:8704\n"
"ds_read_b64_tr_b16 a[204:205],v121 offset:12288\n"
"ds_read_b64_tr_b16 a[206:207],v121 offset:12800\n"
"ds_read_b64_tr_b16 a[208:209],v121 offset:0\n"
"ds_read_b64_tr_b16 a[210:211],v121 offset:512\n"
"s_mov_b32 s84,0x40000\n"
".Lfk_w_chk:\n"
"v_accvgpr_read_b32 v119,a150\n"
"v_accvgpr_read_b32 v120,a151\n"
"v_xor_b32 v119,s80,v119\n"
"v_xor_b32 v120,s81,v120\n"
"v_or_b32 v119,v119,v120\n"
"v_cmp_ne_u32 vcc,0,v119\n"
"s_nop 0\n"
"s_cbranch_vccz .Lfk_w_ok\n"
"s_sub_u32 s84,s84,1\n"
"s_cbranch_scc1 .Lfk_w_ok\n"
"s_sleep 4\n"
"buffer_load_dwordx2 a[150:151],v112,s[24:27],s2 offen sc1\n"
"s_waitcnt vmcnt(0)\n"
"s_branch .Lfk_w_chk\n"
".Lfk_w_ok:\n"
"v_lshrrev_b32 v121,1,v112\n"
"v_and_b32 v121,0xf8,v121\n"
"s_add_u32 s82,s63,8192\n"
"buffer_load_dwordx2 a[150:151],v121,s[24:27],s82 offen sc1\n"
"s_mov_b64 s[32:33],s[24:25]\n"
"s_mov_b32 s34,s26\n"
"s_mov_b32 s22,1024\n"
"v_mov_b32 v115,v112\n"
"s_sub_u32 s54,s3,1024\n"
"s_swappc_b64 s[20:21],s[94:95]\n"
"s_xor_b32 s55,s55,16384\n"
"s_add_u32 s56,s56,131072\n"
"s_andn2_b32 s91,s55,16384\n"
"v_lshrrev_b32 v121,1,v112\n"
"v_add_u32 v121,s91,v121\n"
"ds_read_b64_tr_b16 a[212:213],v121 offset:20480\n"
"ds_read_b64_tr_b16 a[214:215],v121 offset:20992\n"
"ds_read_b64_tr_b16 a[216:217],v121 offset:24576\n"
"ds_read_b64_tr_b16 a[218:219],v121 offset:25088\n"
"ds_read_b64_tr_b16 a[220:221],v121 offset:28672\n"
"ds_read_b64_tr_b16 a[222:223],v121 offset:29184\n"
"v_add_u32 v121,s91,v112\n"
"ds_read_b128 a[224:227],v121 offset:0\n"
"s_sub_u32 s54,s3,17408\n"
"s_mov_b32 s84,0x40000\n"
".Lfk_k_chk:\n"
"v_accvgpr_read_b32 v119,a150\n"
"v_accvgpr_read_b32 v120,a151\n"
"v_xor_b32 v119,s80,v119\n"
"v_xor_b32 v120,s81,v120\n"
"v_or_b32 v119,v119,v120\n"
"v_cmp_ne_u32 vcc,0,v119\n"
"s_nop 0\n"
"s_cbranch_vccz .Lfk_k_ok\n"
"s_sub_u32 s84,s84,1\n"
"s_cbranch_scc1 .Lfk_k_ok\n"
"s_sleep 4\n"
"v_lshrrev_b32 v121,1,v112\n"
"v_and_b32 v121,0xf8,v121\n"
"s_add_u32 s82,s63,8192\n"
"buffer_load_dwordx2 a[150:151],v121,s[24:27],s82 offen sc1\n"
"s_waitcnt vmcnt(0)\n"
"s_branch .Lfk_k_chk\n"
".Lfk_k_ok:\n"
"s_swappc_b64 s[20:21],s[94:95]\n"
"s_xor_b32 s55,s55,16384\n"
"s_andn2_b32 s91,s55,16384\n"
"v_add_u32 v121,s91,v112\n"
"ds_read_b128 a[228:231],v121 offset:4096\n"
"ds_read_b128 a[232:235],v121 offset:8192\n"
"ds_read_b128 a[236:239],v121 offset:12288\n"
"ds_read_b128 a[240:243],v121 offset:0\n"
"s_sub_u32 s54,s3,99328\n"
"s_mov_b32 s56,2146435072\n"
"s_getpc_b64 s[96:97]\n"
".Lfk_rt1:\n"
"s_sub_u32 s96,s96,.Lfk_rt1-.Lfk_tailA\n"
"s_subb_u32 s97,s97,0\n"
"s_mov_b32 s98,.Lfk_cold-.Lfk_tailA\n"
"s_mov_b32 s99,0x20000\n"
"s_lshr_b32 s82,s100,3\n"
"s_lshl2_add_u32 s82,s82,s52\n"
"s_lshl_b32 s82,s82,9\n"
"v_lshrrev_b32 v121,1,v112\n"
"buffer_load_dword a148,v121,s[96:99],s82 offen\n"
"s_swappc_b64 s[20:21],s[94:95]\n"
"s_xor_b32 s55,s55,16384\n"
"s_andn2_b32 s91,s55,16384\n"
"v_add_u32 v121,s91,v112\n"
"ds_read_b128 a[244:247],v121 offset:20480\n"
"ds_read_b128 a[248:251],v121 offset:24576\n"
"ds_read_b128 a[252:255],v121 offset:28672\n"
"ds_read_b128 a[156:159],v121 offset:0\n"
"s_sub_u32 s54,s3,115712\n"
"s_lshl_b32 s82,s100,3\n"
"s_add_u32 s82,s82,148946944\n"
"s_cmp_eq_u32 s52,0\n"
"s_cselect_b32 s82,s82,2146435072\n"
"buffer_store_dwordx2 a[154:155],off,s[24:27],s82 sc1\n"
"s_lshr_b32 s82,s3,9\n"
"s_and_b32 s82,s82,0x3000\n"
"s_add_u32 s11,s11,s82\n"
"s_add_u32 s9,s9,s82\n"
"s_lshl3_add_u32 s8,s82,s8\n"
"s_lshr_b32 s82,s3,18\n"
"s_sub_u32 s82,s82,s60\n"
"s_and_b32 s82,s82,7\n"
"s_lshl_b32 s82,s82,17\n"
"s_add_u32 s8,s8,s82\n"
"v_lshrrev_b32 v120,1,v112\n"
"buffer_load_dwordx2 a[150:151],v120,s[24:27],s11 offen sc1\n"
"s_lshl_b32 s30,s78,1\n"
"s_swappc_b64 s[20:21],s[94:95]\n"
"s_nop 1\n"
".Lfk_xn0_s:\n"
"s_mov_b32 s84,0x40000\n"
"buffer_load_dwordx4 v[74:77],v112,s[24:27],s58 offen sc1\n"
"s_waitcnt vmcnt(0)\n"
".Lfk_xn0:\n"
"v_xor_b32 v116,s80,v74\n"
"v_xor_b32 v117,s81,v75\n"
"v_or_b32 v116,v116,v117\n"
"v_xor_b32 v117,s80,v76\n"
"v_or_b32 v116,v116,v117\n"
"v_xor_b32 v117,s81,v77\n"
"v_or_b32 v116,v116,v117\n"
"v_cmp_ne_u32 vcc,0,v116\n"
"s_nop 0\n"
"s_cbranch_vccz .Lfk_xn0_x\n"
"s_sleep 4\n"
"s_mov_b64 exec,vcc\n"
"buffer_load_dwordx4 v[74:77],v112,s[24:27],s58 offen sc1\n"
"s_mov_b64 exec,-1\n"
"s_sub_u32 s84,s84,1\n"
"s_waitcnt vmcnt(0)\n"
"s_cbranch_scc0 .Lfk_xn0\n"
".Lfk_xn0_x:\n"
"s_nop 1\n"
"s_branch .Lfk_xn0_ok\n"
".Lfk_xn1_s:\n"
"s_mov_b32 s84,0x40000\n"
"buffer_load_dwordx4 v[74:77],v112,s[24:27],s58 offen sc1\n"
"s_waitcnt vmcnt(0)\n"
".Lfk_xn1:\n"
"v_xor_b32 v116,s80,v74\n"
"v_xor_b32 v117,s81,v75\n"
"v_or_b32 v116,v116,v117\n"
"v_xor_b32 v117,s80,v76\n"
"v_or_b32 v116,v116,v117\n"
"v_xor_b32 v117,s81,v77\n"
"v_or_b32 v116,v116,v117\n"
"v_cmp_ne_u32 vcc,0,v116\n"
"s_nop 0\n"
"s_cbranch_vccz .Lfk_xn1_x\n"
"s_sleep 4\n"
"s_mov_b64 exec,vcc\n"
"buffer_load_dwordx4 v[74:77],v112,s[24:27],s58 offen sc1\n"
"s_mov_b64 exec,-1\n"
"s_sub_u32 s84,s84,1\n"
"s_waitcnt vmcnt(0)\n"
"s_cbranch_scc0 .Lfk_xn1\n"
".Lfk_xn1_x:\n"
"s_nop 1\n"
"s_branch .Lfk_xn1_ok\n"
".Lfk_kv_s:\n"
"s_mov_b32 s84,0x40000\n"
"buffer_load_dwordx2 v[118:119],v120,s[24:27],s63 offen sc1\n"
"s_waitcnt vmcnt(0)\n"
".Lfk_kv:\n"
"v_xor_b32 v116,s80,v118\n"
"v_xor_b32 v117,s81,v119\n"
"v_or_b32 v116,v116,v117\n"
"v_cmp_ne_u32 vcc,0,v116\n"
"s_nop 0\n"
"s_cbranch_vccz .Lfk_kv_x\n"
"s_sleep 4\n"
"s_mov_b64 exec,vcc\n"
"buffer_load_dwordx2 v[118:119],v120,s[24:27],s63 offen sc1\n"
"s_mov_b64 exec,-1\n"
"s_sub_u32 s84,s84,1\n"
"s_waitcnt vmcnt(0)\n"
"s_cbranch_scc0 .Lfk_kv\n"
".Lfk_kv_x:\n"
"s_nop 1\n"
"s_branch .Lfk_kv_ok\n"
".Lfk_cs_h3:\n"
"s_mov_b32 s91,15\n"
"s_mov_b32 s90,15\n"
"s_call_b64 s[94:95],.Lfk_rare0\n"
"s_branch .Lfk_cr_h3\n"
".Lfk_cs_h1:\n"
"s_mov_b32 s91,15\n"
"s_call_b64 s[94:95],.Lfk_rare3\n"
"s_branch .Lfk_cr_h1\n"
".Lfk_cs_h2:\n"
"s_mov_b32 s91,15\n"
"s_call_b64 s[94:95],.Lfk_rare2\n"
"s_branch .Lfk_cr_h2\n"
".Lfk_cs_x1:\n"
"s_mov_b32 s91,15\n"
"s_call_b64 s[94:95],.Lfk_rare1\n"
"s_branch .Lfk_cr_x1\n"
".Lfk_cs_a3:\n"
"s_mov_b32 s91,14\n"
"s_call_b64 s[94:95],.Lfk_rare0\n"
"s_branch .Lfk_cr_a3\n"
".Lfk_cs_a0:\n"
"s_mov_b32 s91,12\n"
"s_call_b64 s[94:95],.Lfk_rare1\n"
"s_branch .Lfk_cr_a0\n"
".Lfk_cs_a1:\n"
"s_mov_b32 s91,8\n"
"s_call_b64 s[94:95],.Lfk_rare0\n"
"s_branch .Lfk_cr_a1\n"
".Lfk_ms_a0:\n"
"s_call_b64 s[94:95],.Lfk_mslow0\n"
"s_branch .Lfk_mr_a0\n"
".Lfk_ms_a1:\n"
"s_call_b64 s[94:95],.Lfk_mslow1\n"
"s_branch .Lfk_mr_a1\n"
".Lfk_p2:\n"
"s_nop 7\n"
"s_mov_b32 s84,0x40000\n"
"buffer_load_dwordx2 v[122:123],v115,s[24:27],s11 offen sc1\n"
"s_waitcnt vmcnt(0)\n"
".Lfk_pj:\n"
"v_xor_b32 v119,s80,v122\n"
"v_xor_b32 v120,s81,v123\n"
"v_or_b32 v119,v119,v120\n"
"v_cmp_ne_u32 s[14:15],0,v119\n"
"s_nop 0\n"
"s_mov_b32 vcc_lo,s14\n"
"s_mov_b32 vcc_hi,0\n"
"s_cbranch_vccz .Lfk_pj_x\n"
"s_sleep 4\n"
"s_mov_b64 exec,vcc\n"
"buffer_load_dwordx2 v[122:123],v115,s[24:27],s11 offen sc1\n"
"s_mov_b64 exec,-1\n"
"s_sub_u32 s84,s84,1\n"
"s_waitcnt vmcnt(0)\n"
"s_cbranch_scc0 .Lfk_pj\n"
".Lfk_pj_x:\n"
"s_cmp_ge_u32 s52,2\n"
"s_cselect_b32 s18,1032192,0\n"
"s_add_u32 s18,s18,s8\n"
"s_add_u32 m0,s30,0\n"
"s_nop 0\n"
"buffer_load_dwordx4 v112,s[24:27],s18 offen offset:0 sc1 lds\n"
"buffer_load_dwordx4 v112,s[24:27],s18 offen offset:1024 sc1 lds\n"
"buffer_load_dwordx4 v112,s[24:27],s18 offen offset:2048 sc1 lds\n"
"buffer_load_dwordx4 v112,s[24:27],s18 offen offset:3072 sc1 lds\n"
"s_add_u32 m0,m0,4096\n"
"s_nop 0\n"
"buffer_load_dwordx4 v117,s[24:27],s18 offen offset:0 sc1 lds\n"
"buffer_load_dwordx4 v117,s[24:27],s18 offen offset:1024 sc1 lds\n"
"buffer_load_dwordx4 v117,s[24:27],s18 offen offset:2048 sc1 lds\n"
"buffer_load_dwordx4 v117,s[24:27],s18 offen offset:3072 sc1 lds\n"
"s_add_u32 s83,s18,2097152\n"
"s_add_u32 m0,s30,32768\n"
"s_nop 0\n"
"buffer_load_dwordx4 v112,s[24:27],s83 offen offset:0 sc1 lds\n"
"buffer_load_dwordx4 v112,s[24:27],s83 offen offset:1024 sc1 lds\n"
"buffer_load_dwordx4 v112,s[24:27],s83 offen offset:2048 sc1 lds\n"
"buffer_load_dwordx4 v112,s[24:27],s83 offen offset:3072 sc1 lds\n"
"s_add_u32 m0,m0,4096\n"
"s_nop 0\n"
"buffer_load_dwordx4 v117,s[24:27],s83 offen offset:0 sc1 lds\n"
"buffer_load_dwordx4 v117,s[24:27],s83 offen offset:1024 sc1 lds\n"
"buffer_load_dwordx4 v117,s[24:27],s83 offen offset:2048 sc1 lds\n"
"buffer_load_dwordx4 v117,s[24:27],s83 offen offset:3072 sc1 lds\n"
"s_waitcnt vmcnt(0)\n"
"s_nop 1\n"
"s_branch .Lfk_p3\n"
".Lfk_cs_k1:\n"
"s_mov_b32 s91,15\n"
"s_call_b64 s[94:95],.Lfk_rare3\n"
"s_branch .Lfk_cr_k1\n"
".Lfk_cs_k2:\n"
"s_mov_b32 s91,15\n"
"s_call_b64 s[94:95],.Lfk_rare2\n"
"s_branch .Lfk_cr_k2\n"
".Lfk_cs_x2:\n"
"s_mov_b32 s91,15\n"
"s_call_b64 s[94:95],.Lfk_rare1\n"
"s_branch .Lfk_cr_x2\n"
".Lfk_cs_u3:\n"
"s_mov_b32 s91,15\n"
"s_call_b64 s[94:95],.Lfk_rare0\n"
"s_branch .Lfk_cr_u3\n"
".Lfk_cs_mu3:\n"
"s_call_b64 s[94:95],.Lfk_meet3\n"
"s_branch .Lfk_cr_mu3\n"
".Lfk_cs_u0:\n"
"s_mov_b32 s91,14\n"
"s_call_b64 s[94:95],.Lfk_rare1\n"
"s_branch .Lfk_cr_u0\n"
".Lfk_cs_mu0:\n"
"s_call_b64 s[94:95],.Lfk_meet3\n"
"s_branch .Lfk_cr_mu0\n"
".Lfk_cs_u1:\n"
"s_mov_b32 s91,12\n"
"s_call_b64 s[94:95],.Lfk_rare0\n"
"s_branch .Lfk_cr_u1\n"
".Lfk_cs_mu1:\n"
"s_call_b64 s[94:95],.Lfk_meet3\n"
"s_branch .Lfk_cr_mu1\n"
".Lfk_cs_u2:\n"
"s_mov_b32 s91,8\n"
"s_call_b64 s[94:95],.Lfk_rare1\n"
"s_branch .Lfk_cr_u2\n"
".Lfk_cs_mu2:\n"
"s_call_b64 s[94:95],.Lfk_meet3\n"
"s_branch .Lfk_cr_mu2\n"
".Lfk_cs_v3:\n"
"s_mov_b32 s91,15\n"
"s_mov_b32 s90,15\n"
"s_call_b64 s[94:95],.Lfk_rare0\n"
"s_branch .Lfk_cr_v3\n"
".Lfk_cs_mv3:\n"
"s_call_b64 s[94:95],.Lfk_meet3\n"
"s_branch .Lfk_cr_mv3\n"
".Lfk_cs_mv0:\n"
"s_call_b64 s[94:95],.Lfk_meet3\n"
"s_branch .Lfk_cr_mv0\n"
".Lfk_cs_v1:\n"
"s_call_b64 s[94:95],.Lfk_meet3\n"
"v_or_b32 v118,v118,v95\n"
"v_and_b32 v118,0x40004000,v118\n"
"v_cmp_ne_u32 vcc,0,v118\n"
"s_nop 0\n"
"s_cbranch_vccz .Lfk_cr_v1\n"
"s_mov_b32 s91,15\n"
"s_call_b64 s[94:95],.Lfk_rare3\n"
"s_branch .Lfk_cr_v1\n"
".Lfk_cs_v2:\n"
"s_call_b64 s[94:95],.Lfk_meet3\n"
"v_or_b32 v118,v118,v79\n"
"v_and_b32 v118,0x40004000,v118\n"
"v_cmp_ne_u32 vcc,0,v118\n"
"s_nop 0\n"
"s_cbranch_vccz .Lfk_cr_v2\n"
"s_mov_b32 s91,15\n"
"s_call_b64 s[94:95],.Lfk_rare2\n"
"s_branch .Lfk_cr_v2\n"
".Lfk_cs_b3:\n"
"s_mov_b32 s91,15\n"
"s_call_b64 s[94:95],.Lfk_rare0\n"
"s_branch .Lfk_cr_b3\n"
".Lfk_ms_b0:\n"
"s_call_b64 s[94:95],.Lfk_mslow0\n"
"s_branch .Lfk_mr_b0\n"
".Lfk_ms_b1:\n"
"s_call_b64 s[94:95],.Lfk_mslow1\n"
"s_branch .Lfk_mr_b1\n"
".Lfk_cs_c3:\n"
"s_mov_b32 s91,15\n"
"s_mov_b32 s90,15\n"
"s_call_b64 s[94:95],.Lfk_rare0\n"
"s_branch .Lfk_cr_c3\n"
".Lfk_cs_z3:\n"
"s_mov_b32 s91,15\n"
"s_mov_b32 s90,15\n"
"s_call_b64 s[94:95],.Lfk_rare0\n"
"s_branch .Lfk_cr_z3\n"
".Lfk_cs_z0:\n"
"s_mov_b32 s91,14\n"
"s_call_b64 s[94:95],.Lfk_rare1\n"
"s_branch .Lfk_cr_z0\n"
".Lfk_cs_z1:\n"
"s_mov_b32 s91,12\n"
"s_call_b64 s[94:95],.Lfk_rare0\n"
"s_branch .Lfk_cr_z1\n"
".Lfk_cs_z2:\n"
"s_mov_b32 s91,8\n"
"s_call_b64 s[94:95],.Lfk_rare1\n"
"s_branch .Lfk_cr_z2\n"
".Lfk_cold:\n"
".Lfk_rare0:\n"
"s_nop 7\n"
"s_bitcmp1_b32 s91,0\n"
"s_cbranch_scc0 .Lfk_rare0_0\n"
"v_max3_f32 v119,v0,v1,v2\n"
"v_max3_f32 v120,v3,v4,v5\n"
"v_max3_f32 v119,v119,v6,v7\n"
"v_max_f32 v119,v119,v120\n"
"v_mov_b32 v120,v119\n"
"s_nop 1\n"
"v_permlane16_swap_b32 v120,v119\n"
"v_max_f32 v119,v119,v120\n"
"v_mov_b32 v120,v119\n"
"s_nop 1\n"
"v_permlane32_swap_b32 v120,v119\n"
"v_max_f32 v119,v119,v120\n"
"s_bitcmp1_b32 s90,0\n"
"s_cselect_b32 s82,0xff800000,0\n"
"v_add_f32 v119,0x41980000,v119\n"
"v_max_f32 v119,s82,v119\n"
"v_cmp_lt_f32 vcc,s17,|v119|\n"
"s_nop 0\n"
"s_cbranch_vccz .Lfk_rare0_0\n"
"v_ceil_f32 v119,v119\n"
"v_sub_f32 v0,v0,v119\n"
"v_sub_f32 v1,v1,v119\n"
"v_sub_f32 v2,v2,v119\n"
"v_sub_f32 v3,v3,v119\n"
"v_sub_f32 v4,v4,v119\n"
"v_sub_f32 v5,v5,v119\n"
"v_sub_f32 v6,v6,v119\n"
"v_sub_f32 v7,v7,v119\n"
"v_sub_f32 v96,v96,v119\n"
"v_sub_f32 v97,v97,v119\n"
"v_sub_f32 v98,v98,v119\n"
"v_sub_f32 v99,v99,v119\n"
"s_cbranch_scc1 .Lfk_rare0_0\n"
"v_exp_f32 v119,-v119\n"
"s_nop 0\n"
"v_mul_f32 v128,v128,v119\n"
"v_mul_f32 v129,v129,v119\n"
"v_mul_f32 v130,v130,v119\n"
"v_mul_f32 v131,v131,v119\n"
"v_mul_f32 v132,v132,v119\n"
"v_mul_f32 v133,v133,v119\n"
"v_mul_f32 v134,v134,v119\n"
"v_mul_f32 v135,v135,v119\n"
"v_mul_f32 v136,v136,v119\n"
"v_mul_f32 v137,v137,v119\n"
"v_mul_f32 v138,v138,v119\n"
"v_mul_f32 v139,v139,v119\n"
"v_mul_f32 v140,v140,v119\n"
"v_mul_f32 v141,v141,v119\n"
"v_mul_f32 v142,v142,v119\n"
"v_mul_f32 v143,v143,v119\n"
"v_mul_f32 v144,v144,v119\n"
"v_mul_f32 v145,v145,v119\n"
"v_mul_f32 v146,v146,v119\n"
"v_mul_f32 v147,v147,v119\n"
"v_mul_f32 v148,v148,v119\n"
"v_mul_f32 v149,v149,v119\n"
"v_mul_f32 v150,v150,v119\n"
"v_mul_f32 v151,v151,v119\n"
"v_mul_f32 v152,v152,v119\n"
"v_mul_f32 v153,v153,v119\n"
"v_mul_f32 v154,v154,v119\n"
"v_mul_f32 v155,v155,v119\n"
"v_mul_f32 v156,v156,v119\n"
"v_mul_f32 v157,v157,v119\n"
"v_mul_f32 v158,v158,v119\n"
"v_mul_f32 v159,v159,v119\n"
"v_accvgpr_read_b32 v120,a128\n"
"v_mul_f32 v120,v120,v119\n"
"v_accvgpr_write_b32 a128,v120\n"
"v_lshlrev_b32 v120,16,v80\n"
"v_and_b32 v121,0xffff0000,v80\n"
"v_mul_f32 v120,v120,v119\n"
"v_mul_f32 v121,v121,v119\n"
"v_cvt_pk_bf16_f32 v80,v120,v121\n"
"v_accvgpr_read_b32 v120,a129\n"
"v_mul_f32 v120,v120,v119\n"
"v_accvgpr_write_b32 a129,v120\n"
"v_lshlrev_b32 v120,16,v81\n"
"v_and_b32 v121,0xffff0000,v81\n"
"v_mul_f32 v120,v120,v119\n"
"v_mul_f32 v121,v121,v119\n"
"v_cvt_pk_bf16_f32 v81,v120,v121\n"
"v_accvgpr_read_b32 v120,a130\n"
"v_mul_f32 v120,v120,v119\n"
"v_accvgpr_write_b32 a130,v120\n"
"v_lshlrev_b32 v120,16,v82\n"
"v_and_b32 v121,0xffff0000,v82\n"
"v_mul_f32 v120,v120,v119\n"
"v_mul_f32 v121,v121,v119\n"
"v_cvt_pk_bf16_f32 v82,v120,v121\n"
"v_accvgpr_read_b32 v120,a131\n"
"v_mul_f32 v120,v120,v119\n"
"v_accvgpr_write_b32 a131,v120\n"
"v_lshlrev_b32 v120,16,v83\n"
"v_and_b32 v121,0xffff0000,v83\n"
"v_mul_f32 v120,v120,v119\n"
"v_mul_f32 v121,v121,v119\n"
"v_cvt_pk_bf16_f32 v83,v120,v121\n"
".Lfk_rare0_0:\n"
"s_bitcmp1_b32 s91,1\n"
"s_cbranch_scc0 .Lfk_rare0_1\n"
"v_max3_f32 v119,v8,v9,v10\n"
"v_max3_f32 v120,v11,v12,v13\n"
"v_max3_f32 v119,v119,v14,v15\n"
"v_max_f32 v119,v119,v120\n"
"v_mov_b32 v120,v119\n"
"s_nop 1\n"
"v_permlane16_swap_b32 v120,v119\n"
"v_max_f32 v119,v119,v120\n"
"v_mov_b32 v120,v119\n"
"s_nop 1\n"
"v_permlane32_swap_b32 v120,v119\n"
"v_max_f32 v119,v119,v120\n"
"s_bitcmp1_b32 s90,1\n"
"s_cselect_b32 s82,0xff800000,0\n"
"v_add_f32 v119,0x41980000,v119\n"
"v_max_f32 v119,s82,v119\n"
"v_cmp_lt_f32 vcc,s17,|v119|\n"
"s_nop 0\n"
"s_cbranch_vccz .Lfk_rare0_1\n"
"v_ceil_f32 v119,v119\n"
"v_sub_f32 v8,v8,v119\n"
"v_sub_f32 v9,v9,v119\n"
"v_sub_f32 v10,v10,v119\n"
"v_sub_f32 v11,v11,v119\n"
"v_sub_f32 v12,v12,v119\n"
"v_sub_f32 v13,v13,v119\n"
"v_sub_f32 v14,v14,v119\n"
"v_sub_f32 v15,v15,v119\n"
"v_sub_f32 v100,v100,v119\n"
"v_sub_f32 v101,v101,v119\n"
"v_sub_f32 v102,v102,v119\n"
"v_sub_f32 v103,v103,v119\n"
"s_cbranch_scc1 .Lfk_rare0_1\n"
"v_exp_f32 v119,-v119\n"
"s_nop 0\n"
"v_mul_f32 v160,v160,v119\n"
"v_mul_f32 v161,v161,v119\n"
"v_mul_f32 v162,v162,v119\n"
"v_mul_f32 v163,v163,v119\n"
"v_mul_f32 v164,v164,v119\n"
"v_mul_f32 v165,v165,v119\n"
"v_mul_f32 v166,v166,v119\n"
"v_mul_f32 v167,v167,v119\n"
"v_mul_f32 v168,v168,v119\n"
"v_mul_f32 v169,v169,v119\n"
"v_mul_f32 v170,v170,v119\n"
"v_mul_f32 v171,v171,v119\n"
"v_mul_f32 v172,v172,v119\n"
"v_mul_f32 v173,v173,v119\n"
"v_mul_f32 v174,v174,v119\n"
"v_mul_f32 v175,v175,v119\n"
"v_mul_f32 v176,v176,v119\n"
"v_mul_f32 v177,v177,v119\n"
"v_mul_f32 v178,v178,v119\n"
"v_mul_f32 v179,v179,v119\n"
"v_mul_f32 v180,v180,v119\n"
"v_mul_f32 v181,v181,v119\n"
"v_mul_f32 v182,v182,v119\n"
"v_mul_f32 v183,v183,v119\n"
"v_mul_f32 v184,v184,v119\n"
"v_mul_f32 v185,v185,v119\n"
"v_mul_f32 v186,v186,v119\n"
"v_mul_f32 v187,v187,v119\n"
"v_mul_f32 v188,v188,v119\n"
"v_mul_f32 v189,v189,v119\n"
"v_mul_f32 v190,v190,v119\n"
"v_mul_f32 v191,v191,v119\n"
"v_accvgpr_read_b32 v120,a132\n"
"v_mul_f32 v120,v120,v119\n"
"v_accvgpr_write_b32 a132,v120\n"
"v_lshlrev_b32 v120,16,v84\n"
"v_and_b32 v121,0xffff0000,v84\n"
"v_mul_f32 v120,v120,v119\n"
"v_mul_f32 v121,v121,v119\n"
"v_cvt_pk_bf16_f32 v84,v120,v121\n"
"v_accvgpr_read_b32 v120,a133\n"
"v_mul_f32 v120,v120,v119\n"
"v_accvgpr_write_b32 a133,v120\n"
"v_lshlrev_b32 v120,16,v85\n"
"v_and_b32 v121,0xffff0000,v85\n"
"v_mul_f32 v120,v120,v119\n"
"v_mul_f32 v121,v121,v119\n"
"v_cvt_pk_bf16_f32 v85,v120,v121\n"
"v_accvgpr_read_b32 v120,a134\n"
"v_mul_f32 v120,v120,v119\n"
"v_accvgpr_write_b32 a134,v120\n"
"v_lshlrev_b32 v120,16,v86\n"
"v_and_b32 v121,0xffff0000,v86\n"
"v_mul_f32 v120,v120,v119\n"
"v_mul_f32 v121,v121,v119\n"
"v_cvt_pk_bf16_f32 v86,v120,v121\n"
"v_accvgpr_read_b32 v120,a135\n"
"v_mul_f32 v120,v120,v119\n"
"v_accvgpr_write_b32 a135,v120\n"
"v_lshlrev_b32 v120,16,v87\n"
"v_and_b32 v121,0xffff0000,v87\n"
"v_mul_f32 v120,v120,v119\n"
"v_mul_f32 v121,v121,v119\n"
"v_cvt_pk_bf16_f32 v87,v120,v121\n"
".Lfk_rare0_1:\n"
"s_bitcmp1_b32 s91,2\n"
"s_cbranch_scc0 .Lfk_rare0_2\n"
"v_max3_f32 v119,v16,v17,v18\n"
"v_max3_f32 v120,v19,v20,v21\n"
"v_max3_f32 v119,v119,v22,v23\n"
"v_max_f32 v119,v119,v120\n"
"v_mov_b32 v120,v119\n"
"s_nop 1\n"
"v_permlane16_swap_b32 v120,v119\n"
"v_max_f32 v119,v119,v120\n"
"v_mov_b32 v120,v119\n"
"s_nop 1\n"
"v_permlane32_swap_b32 v120,v119\n"
"v_max_f32 v119,v119,v120\n"
"s_bitcmp1_b32 s90,2\n"
"s_cselect_b32 s82,0xff800000,0\n"
"v_add_f32 v119,0x41980000,v119\n"
"v_max_f32 v119,s82,v119\n"
"v_cmp_lt_f32 vcc,s17,|v119|\n"
"s_nop 0\n"
"s_cbranch_vccz .Lfk_rare0_2\n"
"v_ceil_f32 v119,v119\n"
"v_sub_f32 v16,v16,v119\n"
"v_sub_f32 v17,v17,v119\n"
"v_sub_f32 v18,v18,v119\n"
"v_sub_f32 v19,v19,v119\n"
"v_sub_f32 v20,v20,v119\n"
"v_sub_f32 v21,v21,v119\n"
"v_sub_f32 v22,v22,v119\n"
"v_sub_f32 v23,v23,v119\n"
"v_sub_f32 v104,v104,v119\n"
"v_sub_f32 v105,v105,v119\n"
"v_sub_f32 v106,v106,v119\n"
"v_sub_f32 v107,v107,v119\n"
"s_cbranch_scc1 .Lfk_rare0_2\n"
"v_exp_f32 v119,-v119\n"
"s_nop 0\n"
"v_mul_f32 v192,v192,v119\n"
"v_mul_f32 v193,v193,v119\n"
"v_mul_f32 v194,v194,v119\n"
"v_mul_f32 v195,v195,v119\n"
"v_mul_f32 v196,v196,v119\n"
"v_mul_f32 v197,v197,v119\n"
"v_mul_f32 v198,v198,v119\n"
"v_mul_f32 v199,v199,v119\n"
"v_mul_f32 v200,v200,v119\n"
"v_mul_f32 v201,v201,v119\n"
"v_mul_f32 v202,v202,v119\n"
"v_mul_f32 v203,v203,v119\n"
"v_mul_f32 v204,v204,v119\n"
"v_mul_f32 v205,v205,v119\n"
"v_mul_f32 v206,v206,v119\n"
"v_mul_f32 v207,v207,v119\n"
"v_mul_f32 v208,v208,v119\n"
"v_mul_f32 v209,v209,v119\n"
"v_mul_f32 v210,v210,v119\n"
"v_mul_f32 v211,v211,v119\n"
"v_mul_f32 v212,v212,v119\n"
"v_mul_f32 v213,v213,v119\n"
"v_mul_f32 v214,v214,v119\n"
"v_mul_f32 v215,v215,v119\n"
"v_mul_f32 v216,v216,v119\n"
"v_mul_f32 v217,v217,v119\n"
"v_mul_f32 v218,v218,v119\n"
"v_mul_f32 v219,v219,v119\n"
"v_mul_f32 v220,v220,v119\n"
"v_mul_f32 v221,v221,v119\n"
"v_mul_f32 v222,v222,v119\n"
"v_mul_f32 v223,v223,v119\n"
"v_accvgpr_read_b32 v120,a136\n"
"v_mul_f32 v120,v120,v119\n"
"v_accvgpr_write_b32 a136,v120\n"
"v_lshlrev_b32 v120,16,v88\n"
"v_and_b32 v121,0xffff0000,v88\n"
"v_mul_f32 v120,v120,v119\n"
"v_mul_f32 v121,v121,v119\n"
"v_cvt_pk_bf16_f32 v88,v120,v121\n"
"v_accvgpr_read_b32 v120,a137\n"
"v_mul_f32 v120,v120,v119\n"
"v_accvgpr_write_b32 a137,v120\n"
"v_lshlrev_b32 v120,16,v89\n"
"v_and_b32 v121,0xffff0000,v89\n"
"v_mul_f32 v120,v120,v119\n"
"v_mul_f32 v121,v121,v119\n"
"v_cvt_pk_bf16_f32 v89,v120,v121\n"
"v_accvgpr_read_b32 v120,a138\n"
"v_mul_f32 v120,v120,v119\n"
"v_accvgpr_write_b32 a138,v120\n"
"v_lshlrev_b32 v120,16,v90\n"
"v_and_b32 v121,0xffff0000,v90\n"
"v_mul_f32 v120,v120,v119\n"
"v_mul_f32 v121,v121,v119\n"
"v_cvt_pk_bf16_f32 v90,v120,v121\n"
"v_accvgpr_read_b32 v120,a139\n"
"v_mul_f32 v120,v120,v119\n"
"v_accvgpr_write_b32 a139,v120\n"
"v_lshlrev_b32 v120,16,v91\n"
"v_and_b32 v121,0xffff0000,v91\n"
"v_mul_f32 v120,v120,v119\n"
"v_mul_f32 v121,v121,v119\n"
"v_cvt_pk_bf16_f32 v91,v120,v121\n"
".Lfk_rare0_2:\n"
"s_bitcmp1_b32 s91,3\n"
"s_cbranch_scc0 .Lfk_rare0_3\n"
"v_max3_f32 v119,v24,v25,v26\n"
"v_max3_f32 v120,v27,v28,v29\n"
"v_max3_f32 v119,v119,v30,v31\n"
"v_max_f32 v119,v119,v120\n"
"v_mov_b32 v120,v119\n"
"s_nop 1\n"
"v_permlane16_swap_b32 v120,v119\n"
"v_max_f32 v119,v119,v120\n"
"v_mov_b32 v120,v119\n"
"s_nop 1\n"
"v_permlane32_swap_b32 v120,v119\n"
"v_max_f32 v119,v119,v120\n"
"s_bitcmp1_b32 s90,3\n"
"s_cselect_b32 s82,0xff800000,0\n"
"v_add_f32 v119,0x41980000,v119\n"
"v_max_f32 v119,s82,v119\n"
"v_cmp_lt_f32 vcc,s17,|v119|\n"
"s_nop 0\n"
"s_cbranch_vccz .Lfk_rare0_3\n"
"v_ceil_f32 v119,v119\n"
"v_sub_f32 v24,v24,v119\n"
"v_sub_f32 v25,v25,v119\n"
"v_sub_f32 v26,v26,v119\n"
"v_sub_f32 v27,v27,v119\n"
"v_sub_f32 v28,v28,v119\n"
"v_sub_f32 v29,v29,v119\n"
"v_sub_f32 v30,v30,v119\n"
"v_sub_f32 v31,v31,v119\n"
"v_sub_f32 v108,v108,v119\n"
"v_sub_f32 v109,v109,v119\n"
"v_sub_f32 v110,v110,v119\n"
"v_sub_f32 v111,v111,v119\n"
"s_cbranch_scc1 .Lfk_rare0_3\n"
"v_exp_f32 v119,-v119\n"
"s_nop 0\n"
"v_mul_f32 v224,v224,v119\n"
"v_mul_f32 v225,v225,v119\n"
"v_mul_f32 v226,v226,v119\n"
"v_mul_f32 v227,v227,v119\n"
"v_mul_f32 v228,v228,v119\n"
"v_mul_f32 v229,v229,v119\n"
"v_mul_f32 v230,v230,v119\n"
"v_mul_f32 v231,v231,v119\n"
"v_mul_f32 v232,v232,v119\n"
"v_mul_f32 v233,v233,v119\n"
"v_mul_f32 v234,v234,v119\n"
"v_mul_f32 v235,v235,v119\n"
"v_mul_f32 v236,v236,v119\n"
"v_mul_f32 v237,v237,v119\n"
"v_mul_f32 v238,v238,v119\n"
"v_mul_f32 v239,v239,v119\n"
"v_mul_f32 v240,v240,v119\n"
"v_mul_f32 v241,v241,v119\n"
"v_mul_f32 v242,v242,v119\n"
"v_mul_f32 v243,v243,v119\n"
"v_mul_f32 v244,v244,v119\n"
"v_mul_f32 v245,v245,v119\n"
"v_mul_f32 v246,v246,v119\n"
"v_mul_f32 v247,v247,v119\n"
"v_mul_f32 v248,v248,v119\n"
"v_mul_f32 v249,v249,v119\n"
"v_mul_f32 v250,v250,v119\n"
"v_mul_f32 v251,v251,v119\n"
"v_mul_f32 v252,v252,v119\n"
"v_mul_f32 v253,v253,v119\n"
"v_mul_f32 v254,v254,v119\n"
"v_mul_f32 v255,v255,v119\n"
"v_accvgpr_read_b32 v120,a140\n"
"v_mul_f32 v120,v120,v119\n"
"v_accvgpr_write_b32 a140,v120\n"
"v_lshlrev_b32 v120,16,v92\n"
"v_and_b32 v121,0xffff0000,v92\n"
"v_mul_f32 v120,v120,v119\n"
"v_mul_f32 v121,v121,v119\n"
"v_cvt_pk_bf16_f32 v92,v120,v121\n"
"v_accvgpr_read_b32 v120,a141\n"
"v_mul_f32 v120,v120,v119\n"
"v_accvgpr_write_b32 a141,v120\n"
"v_lshlrev_b32 v120,16,v93\n"
"v_and_b32 v121,0xffff0000,v93\n"
"v_mul_f32 v120,v120,v119\n"
"v_mul_f32 v121,v121,v119\n"
"v_cvt_pk_bf16_f32 v93,v120,v121\n"
"v_accvgpr_read_b32 v120,a142\n"
"v_mul_f32 v120,v120,v119\n"
"v_accvgpr_write_b32 a142,v120\n"
"v_lshlrev_b32 v120,16,v94\n"
"v_and_b32 v121,0xffff0000,v94\n"
"v_mul_f32 v120,v120,v119\n"
"v_mul_f32 v121,v121,v119\n"
"v_cvt_pk_bf16_f32 v94,v120,v121\n"
"v_accvgpr_read_b32 v120,a143\n"
"v_mul_f32 v120,v120,v119\n"
"v_accvgpr_write_b32 a143,v120\n"
"v_lshlrev_b32 v120,16,v95\n"
"v_and_b32 v121,0xffff0000,v95\n"
"v_mul_f32 v120,v120,v119\n"
"v_mul_f32 v121,v121,v119\n"
"v_cvt_pk_bf16_f32 v95,v120,v121\n"
".Lfk_rare0_3:\n"
"s_mov_b32 s90,0\n"
"s_nop 1\n"
"s_setpc_b64 s[94:95]\n"
".Lfk_rare2:\n"
"s_nop 7\n"
"v_max3_f32 v119,v0,v1,v2\n"
"v_max3_f32 v120,v3,v4,v5\n"
"v_max3_f32 v119,v119,v6,v7\n"
"v_max_f32 v119,v119,v120\n"
"v_mov_b32 v120,v119\n"
"s_nop 1\n"
"v_permlane16_swap_b32 v120,v119\n"
"v_max_f32 v119,v119,v120\n"
"v_mov_b32 v120,v119\n"
"s_nop 1\n"
"v_permlane32_swap_b32 v120,v119\n"
"v_max_f32 v119,v119,v120\n"
"s_bitcmp1_b32 s90,0\n"
"s_cselect_b32 s82,0xff800000,0\n"
"v_add_f32 v119,0x41980000,v119\n"
"v_max_f32 v119,s82,v119\n"
"v_cmp_lt_f32 vcc,s17,|v119|\n"
"s_nop 0\n"
"s_cbranch_vccz .Lfk_rare2_0\n"
"v_ceil_f32 v119,v119\n"
"v_sub_f32 v0,v0,v119\n"
"v_sub_f32 v1,v1,v119\n"
"v_sub_f32 v2,v2,v119\n"
"v_sub_f32 v3,v3,v119\n"
"v_sub_f32 v4,v4,v119\n"
"v_sub_f32 v5,v5,v119\n"
"v_sub_f32 v6,v6,v119\n"
"v_sub_f32 v7,v7,v119\n"
"v_sub_f32 v96,v96,v119\n"
"v_sub_f32 v97,v97,v119\n"
"v_sub_f32 v98,v98,v119\n"
"v_sub_f32 v99,v99,v119\n"
"v_sub_f32 v32,v32,v119\n"
"v_sub_f32 v33,v33,v119\n"
"v_sub_f32 v34,v34,v119\n"
"v_sub_f32 v35,v35,v119\n"
"v_sub_f32 v36,v36,v119\n"
"v_sub_f32 v37,v37,v119\n"
"v_sub_f32 v38,v38,v119\n"
"v_sub_f32 v39,v39,v119\n"
"v_exp_f32 v0,v0\n"
"v_exp_f32 v1,v1\n"
"v_exp_f32 v2,v2\n"
"v_exp_f32 v3,v3\n"
"v_exp_f32 v4,v4\n"
"v_exp_f32 v5,v5\n"
"v_exp_f32 v6,v6\n"
"v_exp_f32 v7,v7\n"
"v_cvt_pk_bf16_f32 v64,v0,v1\n"
"v_cvt_pk_bf16_f32 v65,v2,v3\n"
"v_cvt_pk_bf16_f32 v66,v4,v5\n"
"v_cvt_pk_bf16_f32 v67,v6,v7\n"
"s_cbranch_scc1 .Lfk_rare2_0\n"
"v_exp_f32 v119,-v119\n"
"s_nop 0\n"
"v_mul_f32 v128,v128,v119\n"
"v_mul_f32 v129,v129,v119\n"
"v_mul_f32 v130,v130,v119\n"
"v_mul_f32 v131,v131,v119\n"
"v_mul_f32 v132,v132,v119\n"
"v_mul_f32 v133,v133,v119\n"
"v_mul_f32 v134,v134,v119\n"
"v_mul_f32 v135,v135,v119\n"
"v_mul_f32 v136,v136,v119\n"
"v_mul_f32 v137,v137,v119\n"
"v_mul_f32 v138,v138,v119\n"
"v_mul_f32 v139,v139,v119\n"
"v_mul_f32 v140,v140,v119\n"
"v_mul_f32 v141,v141,v119\n"
"v_mul_f32 v142,v142,v119\n"
"v_mul_f32 v143,v143,v119\n"
"v_mul_f32 v144,v144,v119\n"
"v_mul_f32 v145,v145,v119\n"
"v_mul_f32 v146,v146,v119\n"
"v_mul_f32 v147,v147,v119\n"
"v_mul_f32 v148,v148,v119\n"
"v_mul_f32 v149,v149,v119\n"
"v_mul_f32 v150,v150,v119\n"
"v_mul_f32 v151,v151,v119\n"
"v_mul_f32 v152,v152,v119\n"
"v_mul_f32 v153,v153,v119\n"
"v_mul_f32 v154,v154,v119\n"
"v_mul_f32 v155,v155,v119\n"
"v_mul_f32 v156,v156,v119\n"
"v_mul_f32 v157,v157,v119\n"
"v_mul_f32 v158,v158,v119\n"
"v_mul_f32 v159,v159,v119\n"
"v_accvgpr_read_b32 v120,a128\n"
"v_mul_f32 v120,v120,v119\n"
"v_accvgpr_write_b32 a128,v120\n"
"v_accvgpr_read_b32 v120,a129\n"
"v_mul_f32 v120,v120,v119\n"
"v_accvgpr_write_b32 a129,v120\n"
"v_accvgpr_read_b32 v120,a130\n"
"v_mul_f32 v120,v120,v119\n"
"v_accvgpr_write_b32 a130,v120\n"
"v_accvgpr_read_b32 v120,a131\n"
"v_mul_f32 v120,v120,v119\n"
"v_accvgpr_write_b32 a131,v120\n"
".Lfk_rare2_0:\n"
"v_max3_f32 v119,v8,v9,v10\n"
"v_max3_f32 v120,v11,v12,v13\n"
"v_max3_f32 v119,v119,v14,v15\n"
"v_max_f32 v119,v119,v120\n"
"v_mov_b32 v120,v119\n"
"s_nop 1\n"
"v_permlane16_swap_b32 v120,v119\n"
"v_max_f32 v119,v119,v120\n"
"v_mov_b32 v120,v119\n"
"s_nop 1\n"
"v_permlane32_swap_b32 v120,v119\n"
"v_max_f32 v119,v119,v120\n"
"s_bitcmp1_b32 s90,1\n"
"s_cselect_b32 s82,0xff800000,0\n"
"v_add_f32 v119,0x41980000,v119\n"
"v_max_f32 v119,s82,v119\n"
"v_cmp_lt_f32 vcc,s17,|v119|\n"
"s_nop 0\n"
"s_cbranch_vccz .Lfk_rare2_1\n"
"v_ceil_f32 v119,v119\n"
"v_sub_f32 v8,v8,v119\n"
"v_sub_f32 v9,v9,v119\n"
"v_sub_f32 v10,v10,v119\n"
"v_sub_f32 v11,v11,v119\n"
"v_sub_f32 v12,v12,v119\n"
"v_sub_f32 v13,v13,v119\n"
"v_sub_f32 v14,v14,v119\n"
"v_sub_f32 v15,v15,v119\n"
"v_sub_f32 v100,v100,v119\n"
"v_sub_f32 v101,v101,v119\n"
"v_sub_f32 v102,v102,v119\n"
"v_sub_f32 v103,v103,v119\n"
"v_sub_f32 v40,v40,v119\n"
"v_sub_f32 v41,v41,v119\n"
"v_sub_f32 v42,v42,v119\n"
"v_sub_f32 v43,v43,v119\n"
"v_sub_f32 v44,v44,v119\n"
"v_sub_f32 v45,v45,v119\n"
"v_sub_f32 v46,v46,v119\n"
"v_sub_f32 v47,v47,v119\n"
"v_exp_f32 v8,v8\n"
"v_exp_f32 v9,v9\n"
"v_exp_f32 v10,v10\n"
"v_exp_f32 v11,v11\n"
"v_exp_f32 v12,v12\n"
"v_exp_f32 v13,v13\n"
"v_exp_f32 v14,v14\n"
"v_exp_f32 v15,v15\n"
"v_cvt_pk_bf16_f32 v68,v8,v9\n"
"v_cvt_pk_bf16_f32 v69,v10,v11\n"
"v_cvt_pk_bf16_f32 v70,v12,v13\n"
"v_cvt_pk_bf16_f32 v71,v14,v15\n"
"s_cbranch_scc1 .Lfk_rare2_1\n"
"v_exp_f32 v119,-v119\n"
"s_nop 0\n"
"v_mul_f32 v160,v160,v119\n"
"v_mul_f32 v161,v161,v119\n"
"v_mul_f32 v162,v162,v119\n"
"v_mul_f32 v163,v163,v119\n"
"v_mul_f32 v164,v164,v119\n"
"v_mul_f32 v165,v165,v119\n"
"v_mul_f32 v166,v166,v119\n"
"v_mul_f32 v167,v167,v119\n"
"v_mul_f32 v168,v168,v119\n"
"v_mul_f32 v169,v169,v119\n"
"v_mul_f32 v170,v170,v119\n"
"v_mul_f32 v171,v171,v119\n"
"v_mul_f32 v172,v172,v119\n"
"v_mul_f32 v173,v173,v119\n"
"v_mul_f32 v174,v174,v119\n"
"v_mul_f32 v175,v175,v119\n"
"v_mul_f32 v176,v176,v119\n"
"v_mul_f32 v177,v177,v119\n"
"v_mul_f32 v178,v178,v119\n"
"v_mul_f32 v179,v179,v119\n"
"v_mul_f32 v180,v180,v119\n"
"v_mul_f32 v181,v181,v119\n"
"v_mul_f32 v182,v182,v119\n"
"v_mul_f32 v183,v183,v119\n"
"v_mul_f32 v184,v184,v119\n"
"v_mul_f32 v185,v185,v119\n"
"v_mul_f32 v186,v186,v119\n"
"v_mul_f32 v187,v187,v119\n"
"v_mul_f32 v188,v188,v119\n"
"v_mul_f32 v189,v189,v119\n"
"v_mul_f32 v190,v190,v119\n"
"v_mul_f32 v191,v191,v119\n"
"v_accvgpr_read_b32 v120,a132\n"
"v_mul_f32 v120,v120,v119\n"
"v_accvgpr_write_b32 a132,v120\n"
"v_accvgpr_read_b32 v120,a133\n"
"v_mul_f32 v120,v120,v119\n"
"v_accvgpr_write_b32 a133,v120\n"
"v_accvgpr_read_b32 v120,a134\n"
"v_mul_f32 v120,v120,v119\n"
"v_accvgpr_write_b32 a134,v120\n"
"v_accvgpr_read_b32 v120,a135\n"
"v_mul_f32 v120,v120,v119\n"
"v_accvgpr_write_b32 a135,v120\n"
".Lfk_rare2_1:\n"
"v_max3_f32 v119,v16,v17,v18\n"
"v_max3_f32 v120,v19,v20,v21\n"
"v_max3_f32 v119,v119,v22,v23\n"
"v_max_f32 v119,v119,v120\n"
"v_mov_b32 v120,v119\n"
"s_nop 1\n"
"v_permlane16_swap_b32 v120,v119\n"
"v_max_f32 v119,v119,v120\n"
"v_mov_b32 v120,v119\n"
"s_nop 1\n"
"v_permlane32_swap_b32 v120,v119\n"
"v_max_f32 v119,v119,v120\n"
"s_bitcmp1_b32 s90,2\n"
"s_cselect_b32 s82,0xff800000,0\n"
"v_add_f32 v119,0x41980000,v119\n"
"v_max_f32 v119,s82,v119\n"
"v_cmp_lt_f32 vcc,s17,|v119|\n"
"s_nop 0\n"
"s_cbranch_vccz .Lfk_rare2_2\n"
"v_ceil_f32 v119,v119\n"
"v_sub_f32 v16,v16,v119\n"
"v_sub_f32 v17,v17,v119\n"
"v_sub_f32 v18,v18,v119\n"
"v_sub_f32 v19,v19,v119\n"
"v_sub_f32 v20,v20,v119\n"
"v_sub_f32 v21,v21,v119\n"
"v_sub_f32 v22,v22,v119\n"
"v_sub_f32 v23,v23,v119\n"
"v_sub_f32 v104,v104,v119\n"
"v_sub_f32 v105,v105,v119\n"
"v_sub_f32 v106,v106,v119\n"
"v_sub_f32 v107,v107,v119\n"
"v_sub_f32 v48,v48,v119\n"
"v_sub_f32 v49,v49,v119\n"
"v_sub_f32 v50,v50,v119\n"
"v_sub_f32 v51,v51,v119\n"
"v_sub_f32 v52,v52,v119\n"
"v_sub_f32 v53,v53,v119\n"
"v_sub_f32 v54,v54,v119\n"
"v_sub_f32 v55,v55,v119\n"
"v_exp_f32 v16,v16\n"
"v_exp_f32 v17,v17\n"
"v_exp_f32 v18,v18\n"
"v_exp_f32 v19,v19\n"
"v_exp_f32 v20,v20\n"
"v_exp_f32 v21,v21\n"
"v_exp_f32 v22,v22\n"
"v_exp_f32 v23,v23\n"
"v_cvt_pk_bf16_f32 v72,v16,v17\n"
"v_cvt_pk_bf16_f32 v73,v18,v19\n"
"v_cvt_pk_bf16_f32 v74,v20,v21\n"
"v_cvt_pk_bf16_f32 v75,v22,v23\n"
"s_cbranch_scc1 .Lfk_rare2_2\n"
"v_exp_f32 v119,-v119\n"
"s_nop 0\n"
"v_mul_f32 v192,v192,v119\n"
"v_mul_f32 v193,v193,v119\n"
"v_mul_f32 v194,v194,v119\n"
"v_mul_f32 v195,v195,v119\n"
"v_mul_f32 v196,v196,v119\n"
"v_mul_f32 v197,v197,v119\n"
"v_mul_f32 v198,v198,v119\n"
"v_mul_f32 v199,v199,v119\n"
"v_mul_f32 v200,v200,v119\n"
"v_mul_f32 v201,v201,v119\n"
"v_mul_f32 v202,v202,v119\n"
"v_mul_f32 v203,v203,v119\n"
"v_mul_f32 v204,v204,v119\n"
"v_mul_f32 v205,v205,v119\n"
"v_mul_f32 v206,v206,v119\n"
"v_mul_f32 v207,v207,v119\n"
"v_mul_f32 v208,v208,v119\n"
"v_mul_f32 v209,v209,v119\n"
"v_mul_f32 v210,v210,v119\n"
"v_mul_f32 v211,v211,v119\n"
"v_mul_f32 v212,v212,v119\n"
"v_mul_f32 v213,v213,v119\n"
"v_mul_f32 v214,v214,v119\n"
"v_mul_f32 v215,v215,v119\n"
"v_mul_f32 v216,v216,v119\n"
"v_mul_f32 v217,v217,v119\n"
"v_mul_f32 v218,v218,v119\n"
"v_mul_f32 v219,v219,v119\n"
"v_mul_f32 v220,v220,v119\n"
"v_mul_f32 v221,v221,v119\n"
"v_mul_f32 v222,v222,v119\n"
"v_mul_f32 v223,v223,v119\n"
"v_accvgpr_read_b32 v120,a136\n"
"v_mul_f32 v120,v120,v119\n"
"v_accvgpr_write_b32 a136,v120\n"
"v_accvgpr_read_b32 v120,a137\n"
"v_mul_f32 v120,v120,v119\n"
"v_accvgpr_write_b32 a137,v120\n"
"v_accvgpr_read_b32 v120,a138\n"
"v_mul_f32 v120,v120,v119\n"
"v_accvgpr_write_b32 a138,v120\n"
"v_accvgpr_read_b32 v120,a139\n"
"v_mul_f32 v120,v120,v119\n"
"v_accvgpr_write_b32 a139,v120\n"
".Lfk_rare2_2:\n"
"v_max3_f32 v119,v24,v25,v26\n"
"v_max3_f32 v120,v27,v28,v29\n"
"v_max3_f32 v119,v119,v30,v31\n"
"v_max_f32 v119,v119,v120\n"
"v_mov_b32 v120,v119\n"
"s_nop 1\n"
"v_permlane16_swap_b32 v120,v119\n"
"v_max_f32 v119,v119,v120\n"
"v_mov_b32 v120,v119\n"
"s_nop 1\n"
"v_permlane32_swap_b32 v120,v119\n"
"v_max_f32 v119,v119,v120\n"
"s_bitcmp1_b32 s90,3\n"
"s_cselect_b32 s82,0xff800000,0\n"
"v_add_f32 v119,0x41980000,v119\n"
"v_max_f32 v119,s82,v119\n"
"v_cmp_lt_f32 vcc,s17,|v119|\n"
"s_nop 0\n"
"s_cbranch_vccz .Lfk_rare2_3\n"
"v_ceil_f32 v119,v119\n"
"v_sub_f32 v24,v24,v119\n"
"v_sub_f32 v25,v25,v119\n"
"v_sub_f32 v26,v26,v119\n"
"v_sub_f32 v27,v27,v119\n"
"v_sub_f32 v28,v28,v119\n"
"v_sub_f32 v29,v29,v119\n"
"v_sub_f32 v30,v30,v119\n"
"v_sub_f32 v31,v31,v119\n"
"v_sub_f32 v108,v108,v119\n"
"v_sub_f32 v109,v109,v119\n"
"v_sub_f32 v110,v110,v119\n"
"v_sub_f32 v111,v111,v119\n"
"v_sub_f32 v56,v56,v119\n"
"v_sub_f32 v57,v57,v119\n"
"v_sub_f32 v58,v58,v119\n"
"v_sub_f32 v59,v59,v119\n"
"v_sub_f32 v60,v60,v119\n"
"v_sub_f32 v61,v61,v119\n"
"v_sub_f32 v62,v62,v119\n"
"v_sub_f32 v63,v63,v119\n"
"v_exp_f32 v24,v24\n"
"v_exp_f32 v25,v25\n"
"v_exp_f32 v26,v26\n"
"v_exp_f32 v27,v27\n"
"v_exp_f32 v28,v28\n"
"v_exp_f32 v29,v29\n"
"v_exp_f32 v30,v30\n"
"v_exp_f32 v31,v31\n"
"v_cvt_pk_bf16_f32 v76,v24,v25\n"
"v_cvt_pk_bf16_f32 v77,v26,v27\n"
"v_cvt_pk_bf16_f32 v78,v28,v29\n"
"v_cvt_pk_bf16_f32 v79,v30,v31\n"
"s_cbranch_scc1 .Lfk_rare2_3\n"
"v_exp_f32 v119,-v119\n"
"s_nop 0\n"
"v_mul_f32 v224,v224,v119\n"
"v_mul_f32 v225,v225,v119\n"
"v_mul_f32 v226,v226,v119\n"
"v_mul_f32 v227,v227,v119\n"
"v_mul_f32 v228,v228,v119\n"
"v_mul_f32 v229,v229,v119\n"
"v_mul_f32 v230,v230,v119\n"
"v_mul_f32 v231,v231,v119\n"
"v_mul_f32 v232,v232,v119\n"
"v_mul_f32 v233,v233,v119\n"
"v_mul_f32 v234,v234,v119\n"
"v_mul_f32 v235,v235,v119\n"
"v_mul_f32 v236,v236,v119\n"
"v_mul_f32 v237,v237,v119\n"
"v_mul_f32 v238,v238,v119\n"
"v_mul_f32 v239,v239,v119\n"
"v_mul_f32 v240,v240,v119\n"
"v_mul_f32 v241,v241,v119\n"
"v_mul_f32 v242,v242,v119\n"
"v_mul_f32 v243,v243,v119\n"
"v_mul_f32 v244,v244,v119\n"
"v_mul_f32 v245,v245,v119\n"
"v_mul_f32 v246,v246,v119\n"
"v_mul_f32 v247,v247,v119\n"
"v_mul_f32 v248,v248,v119\n"
"v_mul_f32 v249,v249,v119\n"
"v_mul_f32 v250,v250,v119\n"
"v_mul_f32 v251,v251,v119\n"
"v_mul_f32 v252,v252,v119\n"
"v_mul_f32 v253,v253,v119\n"
"v_mul_f32 v254,v254,v119\n"
"v_mul_f32 v255,v255,v119\n"
"v_accvgpr_read_b32 v120,a140\n"
"v_mul_f32 v120,v120,v119\n"
"v_accvgpr_write_b32 a140,v120\n"
"v_accvgpr_read_b32 v120,a141\n"
"v_mul_f32 v120,v120,v119\n"
"v_accvgpr_write_b32 a141,v120\n"
"v_accvgpr_read_b32 v120,a142\n"
"v_mul_f32 v120,v120,v119\n"
"v_accvgpr_write_b32 a142,v120\n"
"v_accvgpr_read_b32 v120,a143\n"
"v_mul_f32 v120,v120,v119\n"
"v_accvgpr_write_b32 a143,v120\n"
".Lfk_rare2_3:\n"
"s_mov_b32 s90,0\n"
"s_nop 1\n"
"s_setpc_b64 s[94:95]\n"
".Lfk_mslow0:\n"
"s_nop 7\n"
"v_min_f32 v119,v98,v104\n"
"v_sub_f32 v120,v119,v104\n"
"v_sub_f32 v119,v119,v98\n"
"v_exp_f32 v120,v120\n"
"v_exp_f32 v119,v119\n"
"v_mul_f32 v192,v192,v120\n"
"v_mul_f32 v193,v193,v120\n"
"v_mul_f32 v194,v194,v120\n"
"v_mul_f32 v195,v195,v120\n"
"v_mul_f32 v196,v196,v120\n"
"v_mul_f32 v197,v197,v120\n"
"v_mul_f32 v198,v198,v120\n"
"v_mul_f32 v199,v199,v120\n"
"v_mul_f32 v200,v200,v120\n"
"v_mul_f32 v201,v201,v120\n"
"v_mul_f32 v202,v202,v120\n"
"v_mul_f32 v203,v203,v120\n"
"v_mul_f32 v204,v204,v120\n"
"v_mul_f32 v205,v205,v120\n"
"v_mul_f32 v206,v206,v120\n"
"v_mul_f32 v207,v207,v120\n"
"v_mul_f32 v208,v208,v120\n"
"v_mul_f32 v209,v209,v120\n"
"v_mul_f32 v210,v210,v120\n"
"v_mul_f32 v211,v211,v120\n"
"v_mul_f32 v212,v212,v120\n"
"v_mul_f32 v213,v213,v120\n"
"v_mul_f32 v214,v214,v120\n"
"v_mul_f32 v215,v215,v120\n"
"v_mul_f32 v216,v216,v120\n"
"v_mul_f32 v217,v217,v120\n"
"v_mul_f32 v218,v218,v120\n"
"v_mul_f32 v219,v219,v120\n"
"v_mul_f32 v220,v220,v120\n"
"v_mul_f32 v221,v221,v120\n"
"v_mul_f32 v222,v222,v120\n"
"v_mul_f32 v223,v223,v120\n"
"v_accvgpr_read_b32 v121,a136\n"
"v_mul_f32 v121,v121,v120\n"
"v_accvgpr_write_b32 a136,v121\n"
"v_mul_f32 v99,v99,v119\n"
"v_accvgpr_read_b32 v121,a64\n"
"v_lshlrev_b32 v120,16,v121\n"
"v_and_b32 v121,0xffff0000,v121\n"
"v_mul_f32 v120,v120,v119\n"
"v_mul_f32 v121,v121,v119\n"
"v_cvt_pk_bf16_f32 v121,v120,v121\n"
"v_accvgpr_write_b32 a64,v121\n"
"v_accvgpr_read_b32 v121,a65\n"
"v_lshlrev_b32 v120,16,v121\n"
"v_and_b32 v121,0xffff0000,v121\n"
"v_mul_f32 v120,v120,v119\n"
"v_mul_f32 v121,v121,v119\n"
"v_cvt_pk_bf16_f32 v121,v120,v121\n"
"v_accvgpr_write_b32 a65,v121\n"
"v_accvgpr_read_b32 v121,a66\n"
"v_lshlrev_b32 v120,16,v121\n"
"v_and_b32 v121,0xffff0000,v121\n"
"v_mul_f32 v120,v120,v119\n"
"v_mul_f32 v121,v121,v119\n"
"v_cvt_pk_bf16_f32 v121,v120,v121\n"
"v_accvgpr_write_b32 a66,v121\n"
"v_accvgpr_read_b32 v121,a67\n"
"v_lshlrev_b32 v120,16,v121\n"
"v_and_b32 v121,0xffff0000,v121\n"
"v_mul_f32 v120,v120,v119\n"
"v_mul_f32 v121,v121,v119\n"
"v_cvt_pk_bf16_f32 v121,v120,v121\n"
"v_accvgpr_write_b32 a67,v121\n"
"v_accvgpr_read_b32 v121,a68\n"
"v_lshlrev_b32 v120,16,v121\n"
"v_and_b32 v121,0xffff0000,v121\n"
"v_mul_f32 v120,v120,v119\n"
"v_mul_f32 v121,v121,v119\n"
"v_cvt_pk_bf16_f32 v121,v120,v121\n"
"v_accvgpr_write_b32 a68,v121\n"
"v_accvgpr_read_b32 v121,a69\n"
"v_lshlrev_b32 v120,16,v121\n"
"v_and_b32 v121,0xffff0000,v121\n"
"v_mul_f32 v120,v120,v119\n"
"v_mul_f32 v121,v121,v119\n"
"v_cvt_pk_bf16_f32 v121,v120,v121\n"
"v_accvgpr_write_b32 a69,v121\n"
"v_accvgpr_read_b32 v121,a70\n"
"v_lshlrev_b32 v120,16,v121\n"
"v_and_b32 v121,0xffff0000,v121\n"
"v_mul_f32 v120,v120,v119\n"
"v_mul_f32 v121,v121,v119\n"
"v_cvt_pk_bf16_f32 v121,v120,v121\n"
"v_accvgpr_write_b32 a70,v121\n"
"v_accvgpr_read_b32 v121,a71\n"
"v_lshlrev_b32 v120,16,v121\n"
"v_and_b32 v121,0xffff0000,v121\n"
"v_mul_f32 v120,v120,v119\n"
"v_mul_f32 v121,v121,v119\n"
"v_cvt_pk_bf16_f32 v121,v120,v121\n"
"v_accvgpr_write_b32 a71,v121\n"
"v_accvgpr_read_b32 v121,a72\n"
"v_lshlrev_b32 v120,16,v121\n"
"v_and_b32 v121,0xffff0000,v121\n"
"v_mul_f32 v120,v120,v119\n"
"v_mul_f32 v121,v121,v119\n"
"v_cvt_pk_bf16_f32 v121,v120,v121\n"
"v_accvgpr_write_b32 a72,v121\n"
"v_accvgpr_read_b32 v121,a73\n"
"v_lshlrev_b32 v120,16,v121\n"
"v_and_b32 v121,0xffff0000,v121\n"
"v_mul_f32 v120,v120,v119\n"
"v_mul_f32 v121,v121,v119\n"
"v_cvt_pk_bf16_f32 v121,v120,v121\n"
"v_accvgpr_write_b32 a73,v121\n"
"v_accvgpr_read_b32 v121,a74\n"
"v_lshlrev_b32 v120,16,v121\n"
"v_and_b32 v121,0xffff0000,v121\n"
"v_mul_f32 v120,v120,v119\n"
"v_mul_f32 v121,v121,v119\n"
"v_cvt_pk_bf16_f32 v121,v120,v121\n"
"v_accvgpr_write_b32 a74,v121\n"
"v_accvgpr_read_b32 v121,a75\n"
"v_lshlrev_b32 v120,16,v121\n"
"v_and_b32 v121,0xffff0000,v121\n"
"v_mul_f32 v120,v120,v119\n"
"v_mul_f32 v121,v121,v119\n"
"v_cvt_pk_bf16_f32 v121,v120,v121\n"
"v_accvgpr_write_b32 a75,v121\n"
"v_accvgpr_read_b32 v121,a76\n"
"v_lshlrev_b32 v120,16,v121\n"
"v_and_b32 v121,0xffff0000,v121\n"
"v_mul_f32 v120,v120,v119\n"
"v_mul_f32 v121,v121,v119\n"
"v_cvt_pk_bf16_f32 v121,v120,v121\n"
"v_accvgpr_write_b32 a76,v121\n"
"v_accvgpr_read_b32 v121,a77\n"
"v_lshlrev_b32 v120,16,v121\n"
"v_and_b32 v121,0xffff0000,v121\n"
"v_mul_f32 v120,v120,v119\n"
"v_mul_f32 v121,v121,v119\n"
"v_cvt_pk_bf16_f32 v121,v120,v121\n"
"v_accvgpr_write_b32 a77,v121\n"
"v_accvgpr_read_b32 v121,a78\n"
"v_lshlrev_b32 v120,16,v121\n"
"v_and_b32 v121,0xffff0000,v121\n"
"v_mul_f32 v120,v120,v119\n"
"v_mul_f32 v121,v121,v119\n"
"v_cvt_pk_bf16_f32 v121,v120,v121\n"
"v_accvgpr_write_b32 a78,v121\n"
"v_accvgpr_read_b32 v121,a79\n"
"v_lshlrev_b32 v120,16,v121\n"
"v_and_b32 v121,0xffff0000,v121\n"
"v_mul_f32 v120,v120,v119\n"
"v_mul_f32 v121,v121,v119\n"
"v_cvt_pk_bf16_f32 v121,v120,v121\n"
"v_accvgpr_write_b32 a79,v121\n"
"s_nop 1\n"
"s_setpc_b64 s[94:95]\n"
".Lfk_rare1:\n"
"s_nop 7\n"
"s_bitcmp1_b32 s91,0\n"
"s_cbranch_scc0 .Lfk_rare1_0\n"
"v_max3_f32 v119,v32,v33,v34\n"
"v_max3_f32 v120,v35,v36,v37\n"
"v_max3_f32 v119,v119,v38,v39\n"
"v_max_f32 v119,v119,v120\n"
"v_mov_b32 v120,v119\n"
"s_nop 1\n"
"v_permlane16_swap_b32 v120,v119\n"
"v_max_f32 v119,v119,v120\n"
"v_mov_b32 v120,v119\n"
"s_nop 1\n"
"v_permlane32_swap_b32 v120,v119\n"
"v_max_f32 v119,v119,v120\n"
"s_bitcmp1_b32 s90,0\n"
"s_cselect_b32 s82,0xff800000,0\n"
"v_add_f32 v119,0x41980000,v119\n"
"v_max_f32 v119,s82,v119\n"
"v_cmp_lt_f32 vcc,s17,|v119|\n"
"s_nop 0\n"
"s_cbranch_vccz .Lfk_rare1_0\n"
"v_ceil_f32 v119,v119\n"
"v_sub_f32 v32,v32,v119\n"
"v_sub_f32 v33,v33,v119\n"
"v_sub_f32 v34,v34,v119\n"
"v_sub_f32 v35,v35,v119\n"
"v_sub_f32 v36,v36,v119\n"
"v_sub_f32 v37,v37,v119\n"
"v_sub_f32 v38,v38,v119\n"
"v_sub_f32 v39,v39,v119\n"
"v_sub_f32 v96,v96,v119\n"
"v_sub_f32 v97,v97,v119\n"
"v_sub_f32 v98,v98,v119\n"
"v_sub_f32 v99,v99,v119\n"
"s_cbranch_scc1 .Lfk_rare1_0\n"
"v_exp_f32 v119,-v119\n"
"s_nop 0\n"
"v_mul_f32 v128,v128,v119\n"
"v_mul_f32 v129,v129,v119\n"
"v_mul_f32 v130,v130,v119\n"
"v_mul_f32 v131,v131,v119\n"
"v_mul_f32 v132,v132,v119\n"
"v_mul_f32 v133,v133,v119\n"
"v_mul_f32 v134,v134,v119\n"
"v_mul_f32 v135,v135,v119\n"
"v_mul_f32 v136,v136,v119\n"
"v_mul_f32 v137,v137,v119\n"
"v_mul_f32 v138,v138,v119\n"
"v_mul_f32 v139,v139,v119\n"
"v_mul_f32 v140,v140,v119\n"
"v_mul_f32 v141,v141,v119\n"
"v_mul_f32 v142,v142,v119\n"
"v_mul_f32 v143,v143,v119\n"
"v_mul_f32 v144,v144,v119\n"
"v_mul_f32 v145,v145,v119\n"
"v_mul_f32 v146,v146,v119\n"
"v_mul_f32 v147,v147,v119\n"
"v_mul_f32 v148,v148,v119\n"
"v_mul_f32 v149,v149,v119\n"
"v_mul_f32 v150,v150,v119\n"
"v_mul_f32 v151,v151,v119\n"
"v_mul_f32 v152,v152,v119\n"
"v_mul_f32 v153,v153,v119\n"
"v_mul_f32 v154,v154,v119\n"
"v_mul_f32 v155,v155,v119\n"
"v_mul_f32 v156,v156,v119\n"
"v_mul_f32 v157,v157,v119\n"
"v_mul_f32 v158,v158,v119\n"
"v_mul_f32 v159,v159,v119\n"
"v_accvgpr_read_b32 v120,a128\n"
"v_mul_f32 v120,v120,v119\n"
"v_accvgpr_write_b32 a128,v120\n"
"v_lshlrev_b32 v120,16,v64\n"
"v_and_b32 v121,0xffff0000,v64\n"
"v_mul_f32 v120,v120,v119\n"
"v_mul_f32 v121,v121,v119\n"
"v_cvt_pk_bf16_f32 v64,v120,v121\n"
"v_accvgpr_read_b32 v120,a129\n"
"v_mul_f32 v120,v120,v119\n"
"v_accvgpr_write_b32 a129,v120\n"
"v_lshlrev_b32 v120,16,v65\n"
"v_and_b32 v121,0xffff0000,v65\n"
"v_mul_f32 v120,v120,v119\n"
"v_mul_f32 v121,v121,v119\n"
"v_cvt_pk_bf16_f32 v65,v120,v121\n"
"v_accvgpr_read_b32 v120,a130\n"
"v_mul_f32 v120,v120,v119\n"
"v_accvgpr_write_b32 a130,v120\n"
"v_lshlrev_b32 v120,16,v66\n"
"v_and_b32 v121,0xffff0000,v66\n"
"v_mul_f32 v120,v120,v119\n"
"v_mul_f32 v121,v121,v119\n"
"v_cvt_pk_bf16_f32 v66,v120,v121\n"
"v_accvgpr_read_b32 v120,a131\n"
"v_mul_f32 v120,v120,v119\n"
"v_accvgpr_write_b32 a131,v120\n"
"v_lshlrev_b32 v120,16,v67\n"
"v_and_b32 v121,0xffff0000,v67\n"
"v_mul_f32 v120,v120,v119\n"
"v_mul_f32 v121,v121,v119\n"
"v_cvt_pk_bf16_f32 v67,v120,v121\n"
".Lfk_rare1_0:\n"
"s_bitcmp1_b32 s91,1\n"
"s_cbranch_scc0 .Lfk_rare1_1\n"
"v_max3_f32 v119,v40,v41,v42\n"
"v_max3_f32 v120,v43,v44,v45\n"
"v_max3_f32 v119,v119,v46,v47\n"
"v_max_f32 v119,v119,v120\n"
"v_mov_b32 v120,v119\n"
"s_nop 1\n"
"v_permlane16_swap_b32 v120,v119\n"
"v_max_f32 v119,v119,v120\n"
"v_mov_b32 v120,v119\n"
"s_nop 1\n"
"v_permlane32_swap_b32 v120,v119\n"
"v_max_f32 v119,v119,v120\n"
"s_bitcmp1_b32 s90,1\n"
"s_cselect_b32 s82,0xff800000,0\n"
"v_add_f32 v119,0x41980000,v119\n"
"v_max_f32 v119,s82,v119\n"
"v_cmp_lt_f32 vcc,s17,|v119|\n"
"s_nop 0\n"
"s_cbranch_vccz .Lfk_rare1_1\n"
"v_ceil_f32 v119,v119\n"
"v_sub_f32 v40,v40,v119\n"
"v_sub_f32 v41,v41,v119\n"
"v_sub_f32 v42,v42,v119\n"
"v_sub_f32 v43,v43,v119\n"
"v_sub_f32 v44,v44,v119\n"
"v_sub_f32 v45,v45,v119\n"
"v_sub_f32 v46,v46,v119\n"
"v_sub_f32 v47,v47,v119\n"
"v_sub_f32 v100,v100,v119\n"
"v_sub_f32 v101,v101,v119\n"
"v_sub_f32 v102,v102,v119\n"
"v_sub_f32 v103,v103,v119\n"
"s_cbranch_scc1 .Lfk_rare1_1\n"
"v_exp_f32 v119,-v119\n"
"s_nop 0\n"
"v_mul_f32 v160,v160,v119\n"
"v_mul_f32 v161,v161,v119\n"
"v_mul_f32 v162,v162,v119\n"
"v_mul_f32 v163,v163,v119\n"
"v_mul_f32 v164,v164,v119\n"
"v_mul_f32 v165,v165,v119\n"
"v_mul_f32 v166,v166,v119\n"
"v_mul_f32 v167,v167,v119\n"
"v_mul_f32 v168,v168,v119\n"
"v_mul_f32 v169,v169,v119\n"
"v_mul_f32 v170,v170,v119\n"
"v_mul_f32 v171,v171,v119\n"
"v_mul_f32 v172,v172,v119\n"
"v_mul_f32 v173,v173,v119\n"
"v_mul_f32 v174,v174,v119\n"
"v_mul_f32 v175,v175,v119\n"
"v_mul_f32 v176,v176,v119\n"
"v_mul_f32 v177,v177,v119\n"
"v_mul_f32 v178,v178,v119\n"
"v_mul_f32 v179,v179,v119\n"
"v_mul_f32 v180,v180,v119\n"
"v_mul_f32 v181,v181,v119\n"
"v_mul_f32 v182,v182,v119\n"
"v_mul_f32 v183,v183,v119\n"
"v_mul_f32 v184,v184,v119\n"
"v_mul_f32 v185,v185,v119\n"
"v_mul_f32 v186,v186,v119\n"
"v_mul_f32 v187,v187,v119\n"
"v_mul_f32 v188,v188,v119\n"
"v_mul_f32 v189,v189,v119\n"
"v_mul_f32 v190,v190,v119\n"
"v_mul_f32 v191,v191,v119\n"
"v_accvgpr_read_b32 v120,a132\n"
"v_mul_f32 v120,v120,v119\n"
"v_accvgpr_write_b32 a132,v120\n"
"v_lshlrev_b32 v120,16,v68\n"
"v_and_b32 v121,0xffff0000,v68\n"
"v_mul_f32 v120,v120,v119\n"
"v_mul_f32 v121,v121,v119\n"
"v_cvt_pk_bf16_f32 v68,v120,v121\n"
"v_accvgpr_read_b32 v120,a133\n"
"v_mul_f32 v120,v120,v119\n"
"v_accvgpr_write_b32 a133,v120\n"
"v_lshlrev_b32 v120,16,v69\n"
"v_and_b32 v121,0xffff0000,v69\n"
"v_mul_f32 v120,v120,v119\n"
"v_mul_f32 v121,v121,v119\n"
"v_cvt_pk_bf16_f32 v69,v120,v121\n"
"v_accvgpr_read_b32 v120,a134\n"
"v_mul_f32 v120,v120,v119\n"
"v_accvgpr_write_b32 a134,v120\n"
"v_lshlrev_b32 v120,16,v70\n"
"v_and_b32 v121,0xffff0000,v70\n"
"v_mul_f32 v120,v120,v119\n"
"v_mul_f32 v121,v121,v119\n"
"v_cvt_pk_bf16_f32 v70,v120,v121\n"
"v_accvgpr_read_b32 v120,a135\n"
"v_mul_f32 v120,v120,v119\n"
"v_accvgpr_write_b32 a135,v120\n"
"v_lshlrev_b32 v120,16,v71\n"
"v_and_b32 v121,0xffff0000,v71\n"
"v_mul_f32 v120,v120,v119\n"
"v_mul_f32 v121,v121,v119\n"
"v_cvt_pk_bf16_f32 v71,v120,v121\n"
".Lfk_rare1_1:\n"
"s_bitcmp1_b32 s91,2\n"
"s_cbranch_scc0 .Lfk_rare1_2\n"
"v_max3_f32 v119,v48,v49,v50\n"
"v_max3_f32 v120,v51,v52,v53\n"
"v_max3_f32 v119,v119,v54,v55\n"
"v_max_f32 v119,v119,v120\n"
"v_mov_b32 v120,v119\n"
"s_nop 1\n"
"v_permlane16_swap_b32 v120,v119\n"
"v_max_f32 v119,v119,v120\n"
"v_mov_b32 v120,v119\n"
"s_nop 1\n"
"v_permlane32_swap_b32 v120,v119\n"
"v_max_f32 v119,v119,v120\n"
"s_bitcmp1_b32 s90,2\n"
"s_cselect_b32 s82,0xff800000,0\n"
"v_add_f32 v119,0x41980000,v119\n"
"v_max_f32 v119,s82,v119\n"
"v_cmp_lt_f32 vcc,s17,|v119|\n"
"s_nop 0\n"
"s_cbranch_vccz .Lfk_rare1_2\n"
"v_ceil_f32 v119,v119\n"
"v_sub_f32 v48,v48,v119\n"
"v_sub_f32 v49,v49,v119\n"
"v_sub_f32 v50,v50,v119\n"
"v_sub_f32 v51,v51,v119\n"
"v_sub_f32 v52,v52,v119\n"
"v_sub_f32 v53,v53,v119\n"
"v_sub_f32 v54,v54,v119\n"
"v_sub_f32 v55,v55,v119\n"
"v_sub_f32 v104,v104,v119\n"
"v_sub_f32 v105,v105,v119\n"
"v_sub_f32 v106,v106,v119\n"
"v_sub_f32 v107,v107,v119\n"
"s_cbranch_scc1 .Lfk_rare1_2\n"
"v_exp_f32 v119,-v119\n"
"s_nop 0\n"
"v_mul_f32 v192,v192,v119\n"
"v_mul_f32 v193,v193,v119\n"
"v_mul_f32 v194,v194,v119\n"
"v_mul_f32 v195,v195,v119\n"
"v_mul_f32 v196,v196,v119\n"
"v_mul_f32 v197,v197,v119\n"
"v_mul_f32 v198,v198,v119\n"
"v_mul_f32 v199,v199,v119\n"
"v_mul_f32 v200,v200,v119\n"
"v_mul_f32 v201,v201,v119\n"
"v_mul_f32 v202,v202,v119\n"
"v_mul_f32 v203,v203,v119\n"
"v_mul_f32 v204,v204,v119\n"
"v_mul_f32 v205,v205,v119\n"
"v_mul_f32 v206,v206,v119\n"
"v_mul_f32 v207,v207,v119\n"
"v_mul_f32 v208,v208,v119\n"
"v_mul_f32 v209,v209,v119\n"
"v_mul_f32 v210,v210,v119\n"
"v_mul_f32 v211,v211,v119\n"
"v_mul_f32 v212,v212,v119\n"
"v_mul_f32 v213,v213,v119\n"
"v_mul_f32 v214,v214,v119\n"
"v_mul_f32 v215,v215,v119\n"
"v_mul_f32 v216,v216,v119\n"
"v_mul_f32 v217,v217,v119\n"
"v_mul_f32 v218,v218,v119\n"
"v_mul_f32 v219,v219,v119\n"
"v_mul_f32 v220,v220,v119\n"
"v_mul_f32 v221,v221,v119\n"
"v_mul_f32 v222,v222,v119\n"
"v_mul_f32 v223,v223,v119\n"
"v_accvgpr_read_b32 v120,a136\n"
"v_mul_f32 v120,v120,v119\n"
"v_accvgpr_write_b32 a136,v120\n"
"v_lshlrev_b32 v120,16,v72\n"
"v_and_b32 v121,0xffff0000,v72\n"
"v_mul_f32 v120,v120,v119\n"
"v_mul_f32 v121,v121,v119\n"
"v_cvt_pk_bf16_f32 v72,v120,v121\n"
"v_accvgpr_read_b32 v120,a137\n"
"v_mul_f32 v120,v120,v119\n"
"v_accvgpr_write_b32 a137,v120\n"
"v_lshlrev_b32 v120,16,v73\n"
"v_and_b32 v121,0xffff0000,v73\n"
"v_mul_f32 v120,v120,v119\n"
"v_mul_f32 v121,v121,v119\n"
"v_cvt_pk_bf16_f32 v73,v120,v121\n"
"v_accvgpr_read_b32 v120,a138\n"
"v_mul_f32 v120,v120,v119\n"
"v_accvgpr_write_b32 a138,v120\n"
"v_lshlrev_b32 v120,16,v74\n"
"v_and_b32 v121,0xffff0000,v74\n"
"v_mul_f32 v120,v120,v119\n"
"v_mul_f32 v121,v121,v119\n"
"v_cvt_pk_bf16_f32 v74,v120,v121\n"
"v_accvgpr_read_b32 v120,a139\n"
"v_mul_f32 v120,v120,v119\n"
"v_accvgpr_write_b32 a139,v120\n"
"v_lshlrev_b32 v120,16,v75\n"
"v_and_b32 v121,0xffff0000,v75\n"
"v_mul_f32 v120,v120,v119\n"
"v_mul_f32 v121,v121,v119\n"
"v_cvt_pk_bf16_f32 v75,v120,v121\n"
".Lfk_rare1_2:\n"
"s_bitcmp1_b32 s91,3\n"
"s_cbranch_scc0 .Lfk_rare1_3\n"
"v_max3_f32 v119,v56,v57,v58\n"
"v_max3_f32 v120,v59,v60,v61\n"
"v_max3_f32 v119,v119,v62,v63\n"
"v_max_f32 v119,v119,v120\n"
"v_mov_b32 v120,v119\n"
"s_nop 1\n"
"v_permlane16_swap_b32 v120,v119\n"
"v_max_f32 v119,v119,v120\n"
"v_mov_b32 v120,v119\n"
"s_nop 1\n"
"v_permlane32_swap_b32 v120,v119\n"
"v_max_f32 v119,v119,v120\n"
"s_bitcmp1_b32 s90,3\n"
"s_cselect_b32 s82,0xff800000,0\n"
"v_add_f32 v119,0x41980000,v119\n"
"v_max_f32 v119,s82,v119\n"
"v_cmp_lt_f32 vcc,s17,|v119|\n"
"s_nop 0\n"
"s_cbranch_vccz .Lfk_rare1_3\n"
"v_ceil_f32 v119,v119\n"
"v_sub_f32 v56,v56,v119\n"
"v_sub_f32 v57,v57,v119\n"
"v_sub_f32 v58,v58,v119\n"
"v_sub_f32 v59,v59,v119\n"
"v_sub_f32 v60,v60,v119\n"
"v_sub_f32 v61,v61,v119\n"
"v_sub_f32 v62,v62,v119\n"
"v_sub_f32 v63,v63,v119\n"
"v_sub_f32 v108,v108,v119\n"
"v_sub_f32 v109,v109,v119\n"
"v_sub_f32 v110,v110,v119\n"
"v_sub_f32 v111,v111,v119\n"
"s_cbranch_scc1 .Lfk_rare1_3\n"
"v_exp_f32 v119,-v119\n"
"s_nop 0\n"
"v_mul_f32 v224,v224,v119\n"
"v_mul_f32 v225,v225,v119\n"
"v_mul_f32 v226,v226,v119\n"
"v_mul_f32 v227,v227,v119\n"
"v_mul_f32 v228,v228,v119\n"
"v_mul_f32 v229,v229,v119\n"
"v_mul_f32 v230,v230,v119\n"
"v_mul_f32 v231,v231,v119\n"
"v_mul_f32 v232,v232,v119\n"
"v_mul_f32 v233,v233,v119\n"
"v_mul_f32 v234,v234,v119\n"
"v_mul_f32 v235,v235,v119\n"
"v_mul_f32 v236,v236,v119\n"
"v_mul_f32 v237,v237,v119\n"
"v_mul_f32 v238,v238,v119\n"
"v_mul_f32 v239,v239,v119\n"
"v_mul_f32 v240,v240,v119\n"
"v_mul_f32 v241,v241,v119\n"
"v_mul_f32 v242,v242,v119\n"
"v_mul_f32 v243,v243,v119\n"
"v_mul_f32 v244,v244,v119\n"
"v_mul_f32 v245,v245,v119\n"
"v_mul_f32 v246,v246,v119\n"
"v_mul_f32 v247,v247,v119\n"
"v_mul_f32 v248,v248,v119\n"
"v_mul_f32 v249,v249,v119\n"
"v_mul_f32 v250,v250,v119\n"
"v_mul_f32 v251,v251,v119\n"
"v_mul_f32 v252,v252,v119\n"
"v_mul_f32 v253,v253,v119\n"
"v_mul_f32 v254,v254,v119\n"
"v_mul_f32 v255,v255,v119\n"
"v_accvgpr_read_b32 v120,a140\n"
"v_mul_f32 v120,v120,v119\n"
"v_accvgpr_write_b32 a140,v120\n"
"v_lshlrev_b32 v120,16,v76\n"
"v_and_b32 v121,0xffff0000,v76\n"
"v_mul_f32 v120,v120,v119\n"
"v_mul_f32 v121,v121,v119\n"
"v_cvt_pk_bf16_f32 v76,v120,v121\n"
"v_accvgpr_read_b32 v120,a141\n"
"v_mul_f32 v120,v120,v119\n"
"v_accvgpr_write_b32 a141,v120\n"
"v_lshlrev_b32 v120,16,v77\n"
"v_and_b32 v121,0xffff0000,v77\n"
"v_mul_f32 v120,v120,v119\n"
"v_mul_f32 v121,v121,v119\n"
"v_cvt_pk_bf16_f32 v77,v120,v121\n"
"v_accvgpr_read_b32 v120,a142\n"
"v_mul_f32 v120,v120,v119\n"
"v_accvgpr_write_b32 a142,v120\n"
"v_lshlrev_b32 v120,16,v78\n"
"v_and_b32 v121,0xffff0000,v78\n"
"v_mul_f32 v120,v120,v119\n"
"v_mul_f32 v121,v121,v119\n"
"v_cvt_pk_bf16_f32 v78,v120,v121\n"
"v_accvgpr_read_b32 v120,a143\n"
"v_mul_f32 v120,v120,v119\n"
"v_accvgpr_write_b32 a143,v120\n"
"v_lshlrev_b32 v120,16,v79\n"
"v_and_b32 v121,0xffff0000,v79\n"
"v_mul_f32 v120,v120,v119\n"
"v_mul_f32 v121,v121,v119\n"
"v_cvt_pk_bf16_f32 v79,v120,v121\n"
".Lfk_rare1_3:\n"
"s_mov_b32 s90,0\n"
"s_nop 1\n"
"s_setpc_b64 s[94:95]\n"
".Lfk_rare3:\n"
"s_nop 7\n"
"v_max3_f32 v119,v32,v33,v34\n"
"v_max3_f32 v120,v35,v36,v37\n"
"v_max3_f32 v119,v119,v38,v39\n"
"v_max_f32 v119,v119,v120\n"
"v_mov_b32 v120,v119\n"
"s_nop 1\n"
"v_permlane16_swap_b32 v120,v119\n"
"v_max_f32 v119,v119,v120\n"
"v_mov_b32 v120,v119\n"
"s_nop 1\n"
"v_permlane32_swap_b32 v120,v119\n"
"v_max_f32 v119,v119,v120\n"
"s_bitcmp1_b32 s90,0\n"
"s_cselect_b32 s82,0xff800000,0\n"
"v_add_f32 v119,0x41980000,v119\n"
"v_max_f32 v119,s82,v119\n"
"v_cmp_lt_f32 vcc,s17,|v119|\n"
"s_nop 0\n"
"s_cbranch_vccz .Lfk_rare3_0\n"
"v_ceil_f32 v119,v119\n"
"v_sub_f32 v32,v32,v119\n"
"v_sub_f32 v33,v33,v119\n"
"v_sub_f32 v34,v34,v119\n"
"v_sub_f32 v35,v35,v119\n"
"v_sub_f32 v36,v36,v119\n"
"v_sub_f32 v37,v37,v119\n"
"v_sub_f32 v38,v38,v119\n"
"v_sub_f32 v39,v39,v119\n"
"v_sub_f32 v96,v96,v119\n"
"v_sub_f32 v97,v97,v119\n"
"v_sub_f32 v98,v98,v119\n"
"v_sub_f32 v99,v99,v119\n"
"v_sub_f32 v0,v0,v119\n"
"v_sub_f32 v1,v1,v119\n"
"v_sub_f32 v2,v2,v119\n"
"v_sub_f32 v3,v3,v119\n"
"v_sub_f32 v4,v4,v119\n"
"v_sub_f32 v5,v5,v119\n"
"v_sub_f32 v6,v6,v119\n"
"v_sub_f32 v7,v7,v119\n"
"v_exp_f32 v32,v32\n"
"v_exp_f32 v33,v33\n"
"v_exp_f32 v34,v34\n"
"v_exp_f32 v35,v35\n"
"v_exp_f32 v36,v36\n"
"v_exp_f32 v37,v37\n"
"v_exp_f32 v38,v38\n"
"v_exp_f32 v39,v39\n"
"v_cvt_pk_bf16_f32 v80,v32,v33\n"
"v_cvt_pk_bf16_f32 v81,v34,v35\n"
"v_cvt_pk_bf16_f32 v82,v36,v37\n"
"v_cvt_pk_bf16_f32 v83,v38,v39\n"
"s_cbranch_scc1 .Lfk_rare3_0\n"
"v_exp_f32 v119,-v119\n"
"s_nop 0\n"
"v_mul_f32 v128,v128,v119\n"
"v_mul_f32 v129,v129,v119\n"
"v_mul_f32 v130,v130,v119\n"
"v_mul_f32 v131,v131,v119\n"
"v_mul_f32 v132,v132,v119\n"
"v_mul_f32 v133,v133,v119\n"
"v_mul_f32 v134,v134,v119\n"
"v_mul_f32 v135,v135,v119\n"
"v_mul_f32 v136,v136,v119\n"
"v_mul_f32 v137,v137,v119\n"
"v_mul_f32 v138,v138,v119\n"
"v_mul_f32 v139,v139,v119\n"
"v_mul_f32 v140,v140,v119\n"
"v_mul_f32 v141,v141,v119\n"
"v_mul_f32 v142,v142,v119\n"
"v_mul_f32 v143,v143,v119\n"
"v_mul_f32 v144,v144,v119\n"
"v_mul_f32 v145,v145,v119\n"
"v_mul_f32 v146,v146,v119\n"
"v_mul_f32 v147,v147,v119\n"
"v_mul_f32 v148,v148,v119\n"
"v_mul_f32 v149,v149,v119\n"
"v_mul_f32 v150,v150,v119\n"
"v_mul_f32 v151,v151,v119\n"
"v_mul_f32 v152,v152,v119\n"
"v_mul_f32 v153,v153,v119\n"
"v_mul_f32 v154,v154,v119\n"
"v_mul_f32 v155,v155,v119\n"
"v_mul_f32 v156,v156,v119\n"
"v_mul_f32 v157,v157,v119\n"
"v_mul_f32 v158,v158,v119\n"
"v_mul_f32 v159,v159,v119\n"
"v_accvgpr_read_b32 v120,a128\n"
"v_mul_f32 v120,v120,v119\n"
"v_accvgpr_write_b32 a128,v120\n"
"v_accvgpr_read_b32 v120,a129\n"
"v_mul_f32 v120,v120,v119\n"
"v_accvgpr_write_b32 a129,v120\n"
"v_accvgpr_read_b32 v120,a130\n"
"v_mul_f32 v120,v120,v119\n"
"v_accvgpr_write_b32 a130,v120\n"
"v_accvgpr_read_b32 v120,a131\n"
"v_mul_f32 v120,v120,v119\n"
"v_accvgpr_write_b32 a131,v120\n"
".Lfk_rare3_0:\n"
"v_max3_f32 v119,v40,v41,v42\n"
"v_max3_f32 v120,v43,v44,v45\n"
"v_max3_f32 v119,v119,v46,v47\n"
"v_max_f32 v119,v119,v120\n"
"v_mov_b32 v120,v119\n"
"s_nop 1\n"
"v_permlane16_swap_b32 v120,v119\n"
"v_max_f32 v119,v119,v120\n"
"v_mov_b32 v120,v119\n"
"s_nop 1\n"
"v_permlane32_swap_b32 v120,v119\n"
"v_max_f32 v119,v119,v120\n"
"s_bitcmp1_b32 s90,1\n"
"s_cselect_b32 s82,0xff800000,0\n"
"v_add_f32 v119,0x41980000,v119\n"
"v_max_f32 v119,s82,v119\n"
"v_cmp_lt_f32 vcc,s17,|v119|\n"
"s_nop 0\n"
"s_cbranch_vccz .Lfk_rare3_1\n"
"v_ceil_f32 v119,v119\n"
"v_sub_f32 v40,v40,v119\n"
"v_sub_f32 v41,v41,v119\n"
"v_sub_f32 v42,v42,v119\n"
"v_sub_f32 v43,v43,v119\n"
"v_sub_f32 v44,v44,v119\n"
"v_sub_f32 v45,v45,v119\n"
"v_sub_f32 v46,v46,v119\n"
"v_sub_f32 v47,v47,v119\n"
"v_sub_f32 v100,v100,v119\n"
"v_sub_f32 v101,v101,v119\n"
"v_sub_f32 v102,v102,v119\n"
"v_sub_f32 v103,v103,v119\n"
"v_sub_f32 v8,v8,v119\n"
"v_sub_f32 v9,v9,v119\n"
"v_sub_f32 v10,v10,v119\n"
"v_sub_f32 v11,v11,v119\n"
"v_sub_f32 v12,v12,v119\n"
"v_sub_f32 v13,v13,v119\n"
"v_sub_f32 v14,v14,v119\n"
"v_sub_f32 v15,v15,v119\n"
"v_exp_f32 v40,v40\n"
"v_exp_f32 v41,v41\n"
"v_exp_f32 v42,v42\n"
"v_exp_f32 v43,v43\n"
"v_exp_f32 v44,v44\n"
"v_exp_f32 v45,v45\n"
"v_exp_f32 v46,v46\n"
"v_exp_f32 v47,v47\n"
"v_cvt_pk_bf16_f32 v84,v40,v41\n"
"v_cvt_pk_bf16_f32 v85,v42,v43\n"
"v_cvt_pk_bf16_f32 v86,v44,v45\n"
"v_cvt_pk_bf16_f32 v87,v46,v47\n"
"s_cbranch_scc1 .Lfk_rare3_1\n"
"v_exp_f32 v119,-v119\n"
"s_nop 0\n"
"v_mul_f32 v160,v160,v119\n"
"v_mul_f32 v161,v161,v119\n"
"v_mul_f32 v162,v162,v119\n"
"v_mul_f32 v163,v163,v119\n"
"v_mul_f32 v164,v164,v119\n"
"v_mul_f32 v165,v165,v119\n"
"v_mul_f32 v166,v166,v119\n"
"v_mul_f32 v167,v167,v119\n"
"v_mul_f32 v168,v168,v119\n"
"v_mul_f32 v169,v169,v119\n"
"v_mul_f32 v170,v170,v119\n"
"v_mul_f32 v171,v171,v119\n"
"v_mul_f32 v172,v172,v119\n"
"v_mul_f32 v173,v173,v119\n"
"v_mul_f32 v174,v174,v119\n"
"v_mul_f32 v175,v175,v119\n"
"v_mul_f32 v176,v176,v119\n"
"v_mul_f32 v177,v177,v119\n"
"v_mul_f32 v178,v178,v119\n"
"v_mul_f32 v179,v179,v119\n"
"v_mul_f32 v180,v180,v119\n"
"v_mul_f32 v181,v181,v119\n"
"v_mul_f32 v182,v182,v119\n"
"v_mul_f32 v183,v183,v119\n"
"v_mul_f32 v184,v184,v119\n"
"v_mul_f32 v185,v185,v119\n"
"v_mul_f32 v186,v186,v119\n"
"v_mul_f32 v187,v187,v119\n"
"v_mul_f32 v188,v188,v119\n"
"v_mul_f32 v189,v189,v119\n"
"v_mul_f32 v190,v190,v119\n"
"v_mul_f32 v191,v191,v119\n"
"v_accvgpr_read_b32 v120,a132\n"
"v_mul_f32 v120,v120,v119\n"
"v_accvgpr_write_b32 a132,v120\n"
"v_accvgpr_read_b32 v120,a133\n"
"v_mul_f32 v120,v120,v119\n"
"v_accvgpr_write_b32 a133,v120\n"
"v_accvgpr_read_b32 v120,a134\n"
"v_mul_f32 v120,v120,v119\n"
"v_accvgpr_write_b32 a134,v120\n"
"v_accvgpr_read_b32 v120,a135\n"
"v_mul_f32 v120,v120,v119\n"
"v_accvgpr_write_b32 a135,v120\n"
".Lfk_rare3_1:\n"
"v_max3_f32 v119,v48,v49,v50\n"
"v_max3_f32 v120,v51,v52,v53\n"
"v_max3_f32 v119,v119,v54,v55\n"
"v_max_f32 v119,v119,v120\n"
"v_mov_b32 v120,v119\n"
"s_nop 1\n"
"v_permlane16_swap_b32 v120,v119\n"
"v_max_f32 v119,v119,v120\n"
"v_mov_b32 v120,v119\n"
"s_nop 1\n"
"v_permlane32_swap_b32 v120,v119\n"
"v_max_f32 v119,v119,v120\n"
"s_bitcmp1_b32 s90,2\n"
"s_cselect_b32 s82,0xff800000,0\n"
"v_add_f32 v119,0x41980000,v119\n"
"v_max_f32 v119,s82,v119\n"
"v_cmp_lt_f32 vcc,s17,|v119|\n"
"s_nop 0\n"
"s_cbranch_vccz .Lfk_rare3_2\n"
"v_ceil_f32 v119,v119\n"
"v_sub_f32 v48,v48,v119\n"
"v_sub_f32 v49,v49,v119\n"
"v_sub_f32 v50,v50,v119\n"
"v_sub_f32 v51,v51,v119\n"
"v_sub_f32 v52,v52,v119\n"
"v_sub_f32 v53,v53,v119\n"
"v_sub_f32 v54,v54,v119\n"
"v_sub_f32 v55,v55,v119\n"
"v_sub_f32 v104,v104,v119\n"
"v_sub_f32 v105,v105,v119\n"
"v_sub_f32 v106,v106,v119\n"
"v_sub_f32 v107,v107,v119\n"
"v_sub_f32 v16,v16,v119\n"
"v_sub_f32 v17,v17,v119\n"
"v_sub_f32 v18,v18,v119\n"
"v_sub_f32 v19,v19,v119\n"
"v_sub_f32 v20,v20,v119\n"
"v_sub_f32 v21,v21,v119\n"
"v_sub_f32 v22,v22,v119\n"
"v_sub_f32 v23,v23,v119\n"
"v_exp_f32 v48,v48\n"
"v_exp_f32 v49,v49\n"
"v_exp_f32 v50,v50\n"
"v_exp_f32 v51,v51\n"
"v_exp_f32 v52,v52\n"
"v_exp_f32 v53,v53\n"
"v_exp_f32 v54,v54\n"
"v_exp_f32 v55,v55\n"
"v_cvt_pk_bf16_f32 v88,v48,v49\n"
"v_cvt_pk_bf16_f32 v89,v50,v51\n"
"v_cvt_pk_bf16_f32 v90,v52,v53\n"
"v_cvt_pk_bf16_f32 v91,v54,v55\n"
"s_cbranch_scc1 .Lfk_rare3_2\n"
"v_exp_f32 v119,-v119\n"
"s_nop 0\n"
"v_mul_f32 v192,v192,v119\n"
"v_mul_f32 v193,v193,v119\n"
"v_mul_f32 v194,v194,v119\n"
"v_mul_f32 v195,v195,v119\n"
"v_mul_f32 v196,v196,v119\n"
"v_mul_f32 v197,v197,v119\n"
"v_mul_f32 v198,v198,v119\n"
"v_mul_f32 v199,v199,v119\n"
"v_mul_f32 v200,v200,v119\n"
"v_mul_f32 v201,v201,v119\n"
"v_mul_f32 v202,v202,v119\n"
"v_mul_f32 v203,v203,v119\n"
"v_mul_f32 v204,v204,v119\n"
"v_mul_f32 v205,v205,v119\n"
"v_mul_f32 v206,v206,v119\n"
"v_mul_f32 v207,v207,v119\n"
"v_mul_f32 v208,v208,v119\n"
"v_mul_f32 v209,v209,v119\n"
"v_mul_f32 v210,v210,v119\n"
"v_mul_f32 v211,v211,v119\n"
"v_mul_f32 v212,v212,v119\n"
"v_mul_f32 v213,v213,v119\n"
"v_mul_f32 v214,v214,v119\n"
"v_mul_f32 v215,v215,v119\n"
"v_mul_f32 v216,v216,v119\n"
"v_mul_f32 v217,v217,v119\n"
"v_mul_f32 v218,v218,v119\n"
"v_mul_f32 v219,v219,v119\n"
"v_mul_f32 v220,v220,v119\n"
"v_mul_f32 v221,v221,v119\n"
"v_mul_f32 v222,v222,v119\n"
"v_mul_f32 v223,v223,v119\n"
"v_accvgpr_read_b32 v120,a136\n"
"v_mul_f32 v120,v120,v119\n"
"v_accvgpr_write_b32 a136,v120\n"
"v_accvgpr_read_b32 v120,a137\n"
"v_mul_f32 v120,v120,v119\n"
"v_accvgpr_write_b32 a137,v120\n"
"v_accvgpr_read_b32 v120,a138\n"
"v_mul_f32 v120,v120,v119\n"
"v_accvgpr_write_b32 a138,v120\n"
"v_accvgpr_read_b32 v120,a139\n"
"v_mul_f32 v120,v120,v119\n"
"v_accvgpr_write_b32 a139,v120\n"
".Lfk_rare3_2:\n"
"v_max3_f32 v119,v56,v57,v58\n"
"v_max3_f32 v120,v59,v60,v61\n"
"v_max3_f32 v119,v119,v62,v63\n"
"v_max_f32 v119,v119,v120\n"
"v_mov_b32 v120,v119\n"
"s_nop 1\n"
"v_permlane16_swap_b32 v120,v119\n"
"v_max_f32 v119,v119,v120\n"
"v_mov_b32 v120,v119\n"
"s_nop 1\n"
"v_permlane32_swap_b32 v120,v119\n"
"v_max_f32 v119,v119,v120\n"
"s_bitcmp1_b32 s90,3\n"
"s_cselect_b32 s82,0xff800000,0\n"
"v_add_f32 v119,0x41980000,v119\n"
"v_max_f32 v119,s82,v119\n"
"v_cmp_lt_f32 vcc,s17,|v119|\n"
"s_nop 0\n"
"s_cbranch_vccz .Lfk_rare3_3\n"
"v_ceil_f32 v119,v119\n"
"v_sub_f32 v56,v56,v119\n"
"v_sub_f32 v57,v57,v119\n"
"v_sub_f32 v58,v58,v119\n"
"v_sub_f32 v59,v59,v119\n"
"v_sub_f32 v60,v60,v119\n"
"v_sub_f32 v61,v61,v119\n"
"v_sub_f32 v62,v62,v119\n"
"v_sub_f32 v63,v63,v119\n"
"v_sub_f32 v108,v108,v119\n"
"v_sub_f32 v109,v109,v119\n"
"v_sub_f32 v110,v110,v119\n"
"v_sub_f32 v111,v111,v119\n"
"v_sub_f32 v24,v24,v119\n"
"v_sub_f32 v25,v25,v119\n"
"v_sub_f32 v26,v26,v119\n"
"v_sub_f32 v27,v27,v119\n"
"v_sub_f32 v28,v28,v119\n"
"v_sub_f32 v29,v29,v119\n"
"v_sub_f32 v30,v30,v119\n"
"v_sub_f32 v31,v31,v119\n"
"v_exp_f32 v56,v56\n"
"v_exp_f32 v57,v57\n"
"v_exp_f32 v58,v58\n"
"v_exp_f32 v59,v59\n"
"v_exp_f32 v60,v60\n"
"v_exp_f32 v61,v61\n"
"v_exp_f32 v62,v62\n"
"v_exp_f32 v63,v63\n"
"v_cvt_pk_bf16_f32 v92,v56,v57\n"
"v_cvt_pk_bf16_f32 v93,v58,v59\n"
"v_cvt_pk_bf16_f32 v94,v60,v61\n"
"v_cvt_pk_bf16_f32 v95,v62,v63\n"
"s_cbranch_scc1 .Lfk_rare3_3\n"
"v_exp_f32 v119,-v119\n"
"s_nop 0\n"
"v_mul_f32 v224,v224,v119\n"
"v_mul_f32 v225,v225,v119\n"
"v_mul_f32 v226,v226,v119\n"
"v_mul_f32 v227,v227,v119\n"
"v_mul_f32 v228,v228,v119\n"
"v_mul_f32 v229,v229,v119\n"
"v_mul_f32 v230,v230,v119\n"
"v_mul_f32 v231,v231,v119\n"
"v_mul_f32 v232,v232,v119\n"
"v_mul_f32 v233,v233,v119\n"
"v_mul_f32 v234,v234,v119\n"
"v_mul_f32 v235,v235,v119\n"
"v_mul_f32 v236,v236,v119\n"
"v_mul_f32 v237,v237,v119\n"
"v_mul_f32 v238,v238,v119\n"
"v_mul_f32 v239,v239,v119\n"
"v_mul_f32 v240,v240,v119\n"
"v_mul_f32 v241,v241,v119\n"
"v_mul_f32 v242,v242,v119\n"
"v_mul_f32 v243,v243,v119\n"
"v_mul_f32 v244,v244,v119\n"
"v_mul_f32 v245,v245,v119\n"
"v_mul_f32 v246,v246,v119\n"
"v_mul_f32 v247,v247,v119\n"
"v_mul_f32 v248,v248,v119\n"
"v_mul_f32 v249,v249,v119\n"
"v_mul_f32 v250,v250,v119\n"
"v_mul_f32 v251,v251,v119\n"
"v_mul_f32 v252,v252,v119\n"
"v_mul_f32 v253,v253,v119\n"
"v_mul_f32 v254,v254,v119\n"
"v_mul_f32 v255,v255,v119\n"
"v_accvgpr_read_b32 v120,a140\n"
"v_mul_f32 v120,v120,v119\n"
"v_accvgpr_write_b32 a140,v120\n"
"v_accvgpr_read_b32 v120,a141\n"
"v_mul_f32 v120,v120,v119\n"
"v_accvgpr_write_b32 a141,v120\n"
"v_accvgpr_read_b32 v120,a142\n"
"v_mul_f32 v120,v120,v119\n"
"v_accvgpr_write_b32 a142,v120\n"
"v_accvgpr_read_b32 v120,a143\n"
"v_mul_f32 v120,v120,v119\n"
"v_accvgpr_write_b32 a143,v120\n"
".Lfk_rare3_3:\n"
"s_mov_b32 s90,0\n"
"s_nop 1\n"
"s_setpc_b64 s[94:95]\n"
".Lfk_mslow1:\n"
"s_nop 7\n"
"v_min_f32 v119,v102,v108\n"
"v_sub_f32 v120,v119,v108\n"
"v_sub_f32 v119,v119,v102\n"
"v_exp_f32 v120,v120\n"
"v_exp_f32 v119,v119\n"
"v_mul_f32 v224,v224,v120\n"
"v_mul_f32 v225,v225,v120\n"
"v_mul_f32 v226,v226,v120\n"
"v_mul_f32 v227,v227,v120\n"
"v_mul_f32 v228,v228,v120\n"
"v_mul_f32 v229,v229,v120\n"
"v_mul_f32 v230,v230,v120\n"
"v_mul_f32 v231,v231,v120\n"
"v_mul_f32 v232,v232,v120\n"
"v_mul_f32 v233,v233,v120\n"
"v_mul_f32 v234,v234,v120\n"
"v_mul_f32 v235,v235,v120\n"
"v_mul_f32 v236,v236,v120\n"
"v_mul_f32 v237,v237,v120\n"
"v_mul_f32 v238,v238,v120\n"
"v_mul_f32 v239,v239,v120\n"
"v_mul_f32 v240,v240,v120\n"
"v_mul_f32 v241,v241,v120\n"
"v_mul_f32 v242,v242,v120\n"
"v_mul_f32 v243,v243,v120\n"
"v_mul_f32 v244,v244,v120\n"
"v_mul_f32 v245,v245,v120\n"
"v_mul_f32 v246,v246,v120\n"
"v_mul_f32 v247,v247,v120\n"
"v_mul_f32 v248,v248,v120\n"
"v_mul_f32 v249,v249,v120\n"
"v_mul_f32 v250,v250,v120\n"
"v_mul_f32 v251,v251,v120\n"
"v_mul_f32 v252,v252,v120\n"
"v_mul_f32 v253,v253,v120\n"
"v_mul_f32 v254,v254,v120\n"
"v_mul_f32 v255,v255,v120\n"
"v_accvgpr_read_b32 v121,a140\n"
"v_mul_f32 v121,v121,v120\n"
"v_accvgpr_write_b32 a140,v121\n"
"v_mul_f32 v103,v103,v119\n"
"v_accvgpr_read_b32 v121,a80\n"
"v_lshlrev_b32 v120,16,v121\n"
"v_and_b32 v121,0xffff0000,v121\n"
"v_mul_f32 v120,v120,v119\n"
"v_mul_f32 v121,v121,v119\n"
"v_cvt_pk_bf16_f32 v121,v120,v121\n"
"v_accvgpr_write_b32 a80,v121\n"
"v_accvgpr_read_b32 v121,a81\n"
"v_lshlrev_b32 v120,16,v121\n"
"v_and_b32 v121,0xffff0000,v121\n"
"v_mul_f32 v120,v120,v119\n"
"v_mul_f32 v121,v121,v119\n"
"v_cvt_pk_bf16_f32 v121,v120,v121\n"
"v_accvgpr_write_b32 a81,v121\n"
"v_accvgpr_read_b32 v121,a82\n"
"v_lshlrev_b32 v120,16,v121\n"
"v_and_b32 v121,0xffff0000,v121\n"
"v_mul_f32 v120,v120,v119\n"
"v_mul_f32 v121,v121,v119\n"
"v_cvt_pk_bf16_f32 v121,v120,v121\n"
"v_accvgpr_write_b32 a82,v121\n"
"v_accvgpr_read_b32 v121,a83\n"
"v_lshlrev_b32 v120,16,v121\n"
"v_and_b32 v121,0xffff0000,v121\n"
"v_mul_f32 v120,v120,v119\n"
"v_mul_f32 v121,v121,v119\n"
"v_cvt_pk_bf16_f32 v121,v120,v121\n"
"v_accvgpr_write_b32 a83,v121\n"
"v_accvgpr_read_b32 v121,a84\n"
"v_lshlrev_b32 v120,16,v121\n"
"v_and_b32 v121,0xffff0000,v121\n"
"v_mul_f32 v120,v120,v119\n"
"v_mul_f32 v121,v121,v119\n"
"v_cvt_pk_bf16_f32 v121,v120,v121\n"
"v_accvgpr_write_b32 a84,v121\n"
"v_accvgpr_read_b32 v121,a85\n"
"v_lshlrev_b32 v120,16,v121\n"
"v_and_b32 v121,0xffff0000,v121\n"
"v_mul_f32 v120,v120,v119\n"
"v_mul_f32 v121,v121,v119\n"
"v_cvt_pk_bf16_f32 v121,v120,v121\n"
"v_accvgpr_write_b32 a85,v121\n"
"v_accvgpr_read_b32 v121,a86\n"
"v_lshlrev_b32 v120,16,v121\n"
"v_and_b32 v121,0xffff0000,v121\n"
"v_mul_f32 v120,v120,v119\n"
"v_mul_f32 v121,v121,v119\n"
"v_cvt_pk_bf16_f32 v121,v120,v121\n"
"v_accvgpr_write_b32 a86,v121\n"
"v_accvgpr_read_b32 v121,a87\n"
"v_lshlrev_b32 v120,16,v121\n"
"v_and_b32 v121,0xffff0000,v121\n"
"v_mul_f32 v120,v120,v119\n"
"v_mul_f32 v121,v121,v119\n"
"v_cvt_pk_bf16_f32 v121,v120,v121\n"
"v_accvgpr_write_b32 a87,v121\n"
"v_accvgpr_read_b32 v121,a88\n"
"v_lshlrev_b32 v120,16,v121\n"
"v_and_b32 v121,0xffff0000,v121\n"
"v_mul_f32 v120,v120,v119\n"
"v_mul_f32 v121,v121,v119\n"
"v_cvt_pk_bf16_f32 v121,v120,v121\n"
"v_accvgpr_write_b32 a88,v121\n"
"v_accvgpr_read_b32 v121,a89\n"
"v_lshlrev_b32 v120,16,v121\n"
"v_and_b32 v121,0xffff0000,v121\n"
"v_mul_f32 v120,v120,v119\n"
"v_mul_f32 v121,v121,v119\n"
"v_cvt_pk_bf16_f32 v121,v120,v121\n"
"v_accvgpr_write_b32 a89,v121\n"
"v_accvgpr_read_b32 v121,a90\n"
"v_lshlrev_b32 v120,16,v121\n"
"v_and_b32 v121,0xffff0000,v121\n"
"v_mul_f32 v120,v120,v119\n"
"v_mul_f32 v121,v121,v119\n"
"v_cvt_pk_bf16_f32 v121,v120,v121\n"
"v_accvgpr_write_b32 a90,v121\n"
"v_accvgpr_read_b32 v121,a91\n"
"v_lshlrev_b32 v120,16,v121\n"
"v_and_b32 v121,0xffff0000,v121\n"
"v_mul_f32 v120,v120,v119\n"
"v_mul_f32 v121,v121,v119\n"
"v_cvt_pk_bf16_f32 v121,v120,v121\n"
"v_accvgpr_write_b32 a91,v121\n"
"v_accvgpr_read_b32 v121,a92\n"
"v_lshlrev_b32 v120,16,v121\n"
"v_and_b32 v121,0xffff0000,v121\n"
"v_mul_f32 v120,v120,v119\n"
"v_mul_f32 v121,v121,v119\n"
"v_cvt_pk_bf16_f32 v121,v120,v121\n"
"v_accvgpr_write_b32 a92,v121\n"
"v_accvgpr_read_b32 v121,a93\n"
"v_lshlrev_b32 v120,16,v121\n"
"v_and_b32 v121,0xffff0000,v121\n"
"v_mul_f32 v120,v120,v119\n"
"v_mul_f32 v121,v121,v119\n"
"v_cvt_pk_bf16_f32 v121,v120,v121\n"
"v_accvgpr_write_b32 a93,v121\n"
"v_accvgpr_read_b32 v121,a94\n"
"v_lshlrev_b32 v120,16,v121\n"
"v_and_b32 v121,0xffff0000,v121\n"
"v_mul_f32 v120,v120,v119\n"
"v_mul_f32 v121,v121,v119\n"
"v_cvt_pk_bf16_f32 v121,v120,v121\n"
"v_accvgpr_write_b32 a94,v121\n"
"v_accvgpr_read_b32 v121,a95\n"
"v_lshlrev_b32 v120,16,v121\n"
"v_and_b32 v121,0xffff0000,v121\n"
"v_mul_f32 v120,v120,v119\n"
"v_mul_f32 v121,v121,v119\n"
"v_cvt_pk_bf16_f32 v121,v120,v121\n"
"v_accvgpr_write_b32 a95,v121\n"
"s_nop 1\n"
"s_setpc_b64 s[94:95]\n"
".Lfk_end:\n"
        : "+{v0}"(tid), "+{s2}"(bid)
        : "{s[0:1]}"(__builtin_amdgcn_kernarg_segment_ptr())
        : "v1","v2","v3","v4","v5","v6","v7","v8","v9","v10","v11","v12","v13","v14","v15","v16","v17","v18","v19","v20","v21","v22","v23","v24","v25","v26","v27","v28","v29","v30","v31","v32","v33","v34","v35","v36","v37","v38","v39","v40","v41","v42","v43","v44","v45","v46","v47","v48","v49","v50","v51","v52","v53","v54","v55","v56","v57","v58","v59","v60","v61","v62","v63","v64","v65","v66","v67","v68","v69","v70","v71","v72","v73","v74","v75","v76","v77","v78","v79","v80","v81","v82","v83","v84","v85","v86","v87","v88","v89","v90","v91","v92","v93","v94","v95","v96","v97","v98","v99","v100","v101","v102","v103","v104","v105","v106","v107","v108","v109","v110","v111","v112","v113","v114","v115","v116","v117","v118","v119","v120","v121","v122","v123","v124","v125","v126","v127","v128","v129","v130","v131","v132","v133","v134","v135","v136","v137","v138","v139","v140","v141","v142","v143","v144","v145","v146","v147","v148","v149","v150","v151","v152","v153","v154","v155","v156","v157","v158","v159","v160","v161","v162","v163","v164","v165","v166","v167","v168","v169","v170","v171","v172","v173","v174","v175","v176","v177","v178","v179","v180","v181","v182","v183","v184","v185","v186","v187","v188","v189","v190","v191","v192","v193","v194","v195","v196","v197","v198","v199","v200","v201","v202","v203","v204","v205","v206","v207","v208","v209","v210","v211","v212","v213","v214","v215","v216","v217","v218","v219","v220","v221","v222","v223","v224","v225","v226","v227","v228","v229","v230","v231","v232","v233","v234","v235","v236","v237","v238","v239","v240","v241","v242","v243","v244","v245","v246","v247","v248","v249","v250","v251","v252","v253","v254","v255",
          "a0","a1","a2","a3","a4","a5","a6","a7","a8","a9","a10","a11","a12","a13","a14","a15","a16","a17","a18","a19","a20","a21","a22","a23","a24","a25","a26","a27","a28","a29","a30","a31","a32","a33","a34","a35","a36","a37","a38","a39","a40","a41","a42","a43","a44","a45","a46","a47","a48","a49","a50","a51","a52","a53","a54","a55","a56","a57","a58","a59","a60","a61","a62","a63","a64","a65","a66","a67","a68","a69","a70","a71","a72","a73","a74","a75","a76","a77","a78","a79","a80","a81","a82","a83","a84","a85","a86","a87","a88","a89","a90","a91","a92","a93","a94","a95","a96","a97","a98","a99","a100","a101","a102","a103","a104","a105","a106","a107","a108","a109","a110","a111","a112","a113","a114","a115","a116","a117","a118","a119","a120","a121","a122","a123","a124","a125","a126","a127","a128","a129","a130","a131","a132","a133","a134","a135","a136","a137","a138","a139","a140","a141","a142","a143","a144","a145","a146","a147","a148","a149","a150","a151","a152","a153","a154","a155","a156","a157","a158","a159","a160","a161","a162","a163","a164","a165","a166","a167","a168","a169","a170","a171","a172","a173","a174","a175","a176","a177","a178","a179","a180","a181","a182","a183","a184","a185","a186","a187","a188","a189","a190","a191","a192","a193","a194","a195","a196","a197","a198","a199","a200","a201","a202","a203","a204","a205","a206","a207","a208","a209","a210","a211","a212","a213","a214","a215","a216","a217","a218","a219","a220","a221","a222","a223","a224","a225","a226","a227","a228","a229","a230","a231","a232","a233","a234","a235","a236","a237","a238","a239","a240","a241","a242","a243","a244","a245","a246","a247","a248","a249","a250","a251","a252","a253","a254","a255",
          "s3","s4","s5","s6","s7","s8","s9","s10","s11","s12","s13","s14","s15","s16","s17","s18","s19","s20","s21","s22","s23","s24","s25","s26","s27","s28","s29","s30","s31","s32","s33","s34","s35","s36","s37","s38","s39","s40","s41","s42","s43","s44","s45","s46","s47","s48","s49","s50","s51","s52","s53","s54","s55","s56","s57","s58","s59","s60","s61","s62","s63","s64","s65","s66","s67","s68","s69","s70","s71","s72","s73","s74","s75","s76","s77","s78","s79","s80","s81","s82","s83","s84","s85","s86","s87","s88","s89","s90","s91","s92","s93","s94","s95","s96","s97","s98","s99","s100","s101","vcc","scc","m0","memory");
#endif
}

extern "C" void kernel_launch(void* const* d_in, const int* in_sizes, int n_in, void* d_out, int out_size, void* d_ws, size_t ws_size,
                              hipStream_t stream) {
    (void)hipFuncSetAttribute((const void*)fused_kernel, hipFuncAttributeMaxDynamicSharedMemorySize, 163840);
    fused_kernel<<<256, 256, 163840, stream>>>((const char*)d_in[0], (const char*)d_in[1], (const char*)d_in[2], (const char*)d_in[3], (const char*)d_in[4], (char*)d_ws,
                                               (const char*)d_in[5], (const char*)d_in[8], (const char*)d_in[9], (const char*)d_in[10], (const char*)d_in[7], (char*)d_out);
}
```

```cpp
#include <hip/hip_runtime.h>

extern "C" __global__ __launch_bounds__(256, 1) void fused_kernel(const char* __restrict__ x, const char* __restrict__ ckv, const char* __restrict__ wq,
                                                       const char* __restrict__ wuk, const char* __restrict__ wuv, char* ws,
                                                       const char* __restrict__ pwo, const char* __restrict__ pwg, const char* __restrict__ pwu, const char* __restrict__ pwd,
                                                       const char* __restrict__ pwr, char* pout) {
    unsigned tid = threadIdx.x, bid = blockIdx.x;
#if defined(__HIP_DEVICE_COMPILE__)
    asm volatile(
"s_load_dwordx2 s[16:17],s[0:1],8\n"
"s_load_dword s3,s[0:1],72\n"
"s_getpc_b64 s[8:9]\n"
".Lfk_pc:\n"
"s_lshr_b32 s82,s2,3\n"
"s_lshl_b32 s82,s82,12\n"
"v_lshlrev_b32 v1,4,v0\n"
"s_mov_b32 s10,.Lfk_cold-.Lfk_pc\n"
"s_mov_b32 s11,0x20000\n"
"buffer_load_dword v115,v1,s[8:11],s82 offen\n"
"v_readfirstlane_b32 s52,v0\n"
"s_load_dwordx2 s[32:33],s[0:1],0\n"
"s_load_dwordx2 s[24:25],s[0:1],40\n"
"s_load_dwordx2 s[44:45],s[0:1],8\n"
"s_load_dwordx2 s[28:29],s[0:1],16\n"
"s_load_dwordx2 s[40:41],s[0:1],16\n"
"s_load_dwordx2 s[4:5],s[0:1],48\n"
"s_and_b32 s82,s2,8\n"
"s_add_u32 s82,s82,24\n"
"s_load_dwordx2 s[20:21],s[0:1],s82\n"
"s_lshr_b32 s52,s52,6\n"
"s_lshr_b32 s83,s2,7\n"
"s_lshl_b32 s83,s83,3\n"
"s_add_u32 s83,s83,24\n"
"s_load_dwordx2 s[36:37],s[0:1],s83\n"
"s_mov_b32 s100,s2\n"
"s_lshl_b32 s92,s2,2\n"
"s_add_u32 s92,s92,s52\n"
"v_and_b32 v0,63,v0\n"
"v_lshlrev_b32 v112,4,v0\n"
"s_and_b32 s88,s2,7\n"
"s_lshr_b32 s89,s2,3\n"
"s_lshl_b32 s93,s89,2\n"
"s_add_u32 s93,s93,s52\n"
"v_lshrrev_b32 v2,3,v0\n"
"v_xor_b32 v3,v2,v0\n"
"v_and_b32 v3,7,v3\n"
"v_lshlrev_b32 v3,4,v3\n"
"s_lshl_b32 s82,s52,9\n"
"v_add_u32 v2,s82,v2\n"
"v_lshl_add_u32 v106,v2,10,v3\n"
"v_add_u32 v107,7168,v106\n"
"v_add_u32 v108,14336,v106\n"
"v_add_u32 v109,21504,v106\n"
"v_lshrrev_b32 v4,4,v0\n"
"v_and_b32 v5,15,v0\n"
"v_lshlrev_b32 v6,1,v4\n"
"v_xor_b32 v5,v5,v6\n"
"v_lshlrev_b32 v5,4,v5\n"
"v_xor_b32 v6,0x80,v5\n"
"s_and_b32 s82,s52,1\n"
"s_lshl_b32 s82,s82,17\n"
"s_lshr_b32 s83,s52,1\n"
"s_lshl_b32 s83,s83,8\n"
"s_add_u32 s82,s82,s83\n"
"v_lshl_add_u32 v7,v4,13,s82\n"
"v_add_u32 v110,v7,v5\n"
"v_add_u32 v111,31744,v110\n"
"v_add_u32 v113,v7,v6\n"
"v_add_u32 v113,63488,v113\n"
"v_add_u32 v114,31744,v113\n"
"s_lshl_b32 s78,s52,12\n"
"s_lshl_b32 s79,s52,13\n"
"s_lshl_b32 s85,s52,11\n"
"s_lshr_b32 s82,s2,5\n"
"s_lshl_b32 s82,s82,15\n"
"s_lshl_b32 s83,s88,10\n"
"s_bfe_u32 s97,s2,0x10004\n"
"s_lshl_b32 s97,s97,9\n"
"s_add_u32 s83,s83,s97\n"
"v_lshrrev_b32 v60,3,v0\n"
"v_lshlrev_b32 v60,19,v60\n"
"v_and_b32 v61,7,v0\n"
"v_lshl_add_u32 v60,v61,7,v60\n"
"s_and_b32 s12,s93,63\n"
"s_lshl3_add_u32 s12,s12,s88\n"
"s_lshl_b32 s12,s12,10\n"
"s_add_u32 s38,s12,1049600\n"
"s_mov_b32 s39,0x20000\n"
"s_and_b32 s14,s52,1\n"
"s_mul_i32 s13,s14,507904\n"
"v_subrev_u32 v62,s13,v106\n"
"v_subrev_u32 v63,s13,v107\n"
"s_mul_i32 s14,s14,6144\n"
"s_sub_u32 s13,s79,s14\n"
"s_waitcnt lgkmcnt(0)\n"
"buffer_load_dword v115,v60,s[36:39],s12 offen\n"
"s_lshl_b32 s87,s92,11\n"
"s_add_u32 s44,s44,s87\n"
"s_addc_u32 s45,s45,0\n"
"s_mov_b32 s46,2048\n"
"s_mov_b32 s47,0x20000\n"
"v_lshlrev_b32 v1,5,v0\n"
"buffer_load_dword v115,v1,s[44:47],0 offen\n"
"s_add_u32 s16,s16,s82\n"
"s_addc_u32 s17,s17,0\n"
"s_sub_u32 s18,2097152,s82\n"
"s_mov_b32 s19,0x20000\n"
"s_add_u32 s20,s20,s83\n"
"s_addc_u32 s21,s21,0\n"
"s_sub_u32 s22,4194304,s83\n"
"s_mov_b32 s23,0x20000\n"
"s_mov_b32 s77,0\n"
"s_add_u32 m0,s78,147456\n"
"s_nop 0\n"
"buffer_load_dwordx4 v110,s[20:23],s77 offen offset:0 lds\n"
"buffer_load_dwordx4 v111,s[20:23],s77 offen offset:1024 lds\n"
"buffer_load_dwordx4 v113,s[20:23],s77 offen offset:2048 lds\n"
"buffer_load_dwordx4 v114,s[20:23],s77 offen offset:3072 lds\n"
"s_mov_b32 s76,786432\n"
"s_add_u32 m0,s13,12288\n"
"s_nop 0\n"
"buffer_load_dwordx4 v62,s[16:19],s76 offen offset:0 lds\n"
"buffer_load_dwordx4 v63,s[16:19],s76 offen offset:1024 lds\n"
"s_mov_b32 s77,262144\n"
"s_add_u32 m0,s78,131072\n"
"s_nop 0\n"
"buffer_load_dwordx4 v110,s[20:23],s77 offen offset:0 lds\n"
"buffer_load_dwordx4 v111,s[20:23],s77 offen offset:1024 lds\n"
"buffer_load_dwordx4 v113,s[20:23],s77 offen offset:2048 lds\n"
"buffer_load_dwordx4 v114,s[20:23],s77 offen offset:3072 lds\n"
"s_mov_b32 s76,0\n"
"s_add_u32 m0,s13,0\n"
"s_nop 0\n"
"buffer_load_dwordx4 v62,s[16:19],s76 offen offset:0 lds\n"
"buffer_load_dwordx4 v63,s[16:19],s76 offen offset:1024 lds\n"
"s_mov_b32 s76,262144\n"
"s_add_u32 m0,s13,4096\n"
"s_nop 0\n"
"buffer_load_dwordx4 v62,s[16:19],s76 offen offset:0 lds\n"
"buffer_load_dwordx4 v63,s[16:19],s76 offen offset:1024 lds\n"
"s_mov_b32 s76,524288\n"
"s_add_u32 m0,s13,8192\n"
"s_nop 0\n"
"buffer_load_dwordx4 v62,s[16:19],s76 offen offset:0 lds\n"
"buffer_load_dwordx4 v63,s[16:19],s76 offen offset:1024 lds\n"
"s_lshl_b32 s82,s92,12\n"
"s_add_u32 s32,s32,s82\n"
"s_addc_u32 s33,s33,0\n"
"s_mov_b32 s34,4096\n"
"s_mov_b32 s35,0x20000\n"
"s_mov_b32 s26,176160768\n"
"s_mov_b32 s27,0x20000\n"
"s_mov_b32 s75,148897792\n"
"s_lshr_b32 s54,s2,5\n"
"s_lshl_b32 s54,s54,19\n"
"s_and_b32 s82,s92,0x47\n"
"s_bfe_u32 s83,s92,0x10003\n"
"s_lshl_b32 s83,s83,5\n"
"s_or_b32 s82,s82,s83\n"
"s_bfe_u32 s83,s92,0x20004\n"
"s_lshl3_add_u32 s82,s83,s82\n"
"s_lshl_b32 s82,s82,7\n"
"s_add_u32 s54,s54,s82\n"
"v_lshrrev_b32 v1,3,v0\n"
"v_bfe_u32 v2,v0,2,1\n"
"v_and_b32 v3,3,v0\n"
"v_lshlrev_b32 v3,4,v3\n"
"v_lshl_add_u32 v70,v1,6,v3\n"
"v_lshl_add_u32 v70,v2,11,v70\n"
"s_bfe_u32 s82,s54,0x10009\n"
"s_lshl_b32 s82,s82,5\n"
"v_xor_b32 v3,s82,v3\n"
"v_lshl_add_u32 v71,v2,6,v3\n"
"v_lshl_add_u32 v71,v1,14,v71\n"
"buffer_load_dwordx2 v[78:79],off,s[24:27],s75 sc1\n"
"s_lshl_b32 s82,s52,16\n"
"v_lshl_add_u32 v7,v4,13,s82\n"
"s_bitcmp1_b32 s52,0\n"
"s_cselect_b32 s83,0x80,0\n"
"v_xor_b32 v5,s83,v5\n"
"v_add_u32 v72,v7,v5\n"
"v_add_u32 v73,32768,v72\n"
"s_mov_b32 s86,0\n"
"v_and_b32 v1,15,v0\n"
"v_lshrrev_b32 v2,4,v0\n"
"v_lshlrev_b32 v3,6,v1\n"
"v_lshl_add_u32 v3,v2,4,v3\n"
"v_lshrrev_b32 v4,4,v3\n"
"v_and_b32 v4,32,v4\n"
"v_xor_b32 v97,v3,v4\n"
"s_lshr_b32 s82,s52,1\n"
"s_lshl_b32 s82,s82,14\n"
"v_and_b32 v3,7,v1\n"
"v_xor_b32 v3,v3,v2\n"
"v_lshlrev_b32 v3,4,v3\n"
"v_lshl_add_u32 v3,v1,7,v3\n"
"v_add_u32 v80,s82,v3\n"
"v_xor_b32 v81,64,v80\n"
"v_add_u32 v82,65536,v80\n"
"v_add_u32 v83,65536,v81\n"
"v_bfe_u32 v3,v0,2,2\n"
"v_lshl_add_u32 v4,v2,3,v3\n"
"v_lshlrev_b32 v4,8,v4\n"
"v_and_b32 v5,1,v2\n"
"v_lshl_add_u32 v5,v5,2,v3\n"
"v_lshlrev_b32 v5,5,v5\n"
"v_and_b32 v6,3,v0\n"
"v_lshl_add_u32 v4,v6,3,v4\n"
"s_and_b32 s82,s52,1\n"
"s_lshl_b32 s82,s82,13\n"
"s_add_u32 s82,s82,98304\n"
"v_add_u32 v4,s82,v4\n"
"v_xor_b32 v6,0,v5\n"
"v_add_u32 v98,v4,v6\n"
"v_xor_b32 v6,32,v5\n"
"v_add_u32 v99,v4,v6\n"
"v_xor_b32 v6,64,v5\n"
"v_add_u32 v100,v4,v6\n"
"v_xor_b32 v6,96,v5\n"
"v_add_u32 v101,v4,v6\n"
"v_xor_b32 v6,128,v5\n"
"v_add_u32 v102,v4,v6\n"
"v_xor_b32 v6,160,v5\n"
"v_add_u32 v103,v4,v6\n"
"v_xor_b32 v6,192,v5\n"
"v_add_u32 v104,v4,v6\n"
"v_xor_b32 v6,224,v5\n"
"v_add_u32 v105,v4,v6\n"
"s_and_b32 s53,s89,1\n"
"s_lshl_b32 s83,s88,10\n"
"s_and_b32 s82,s93,63\n"
"s_add_u32 s82,s82,128\n"
"s_lshl_b32 s82,s82,13\n"
"s_add_u32 s82,s82,s83\n"
"s_add_u32 s36,s36,s82\n"
"s_addc_u32 s37,s37,0\n"
"s_mov_b32 s38,2622464\n"
"s_mov_b32 s39,0x20000\n"
"s_lshl_b32 s82,s93,13\n"
"s_add_u32 s82,s82,s83\n"
"s_add_u32 s40,s40,s82\n"
"s_addc_u32 s41,s41,0\n"
"s_mov_b32 s42,7341056\n"
"s_mov_b32 s43,0x20000\n"
"s_cmp_lg_u32 s53,0\n"
"s_cbranch_scc1 .Lfk_v\n"
"s_waitcnt vmcnt(11)\n"
"s_barrier\n"
"ds_read_b128 v[152:155],v80 offset:12288\n"
"ds_read_b128 v[156:159],v80 offset:14336\n"
"ds_read_b64_tr_b16 v[160:161],v98 offset:49152\n"
"ds_read_b64_tr_b16 v[162:163],v98 offset:50176\n"
"ds_read_b64_tr_b16 v[164:165],v99 offset:49152\n"
"ds_read_b64_tr_b16 v[166:167],v99 offset:50176\n"
"ds_read_b64_tr_b16 v[168:169],v100 offset:49152\n"
"ds_read_b64_tr_b16 v[170:171],v100 offset:50176\n"
"ds_read_b64_tr_b16 v[172:173],v101 offset:49152\n"
"ds_read_b64_tr_b16 v[174:175],v101 offset:50176\n"
"ds_read_b64_tr_b16 v[176:177],v102 offset:49152\n"
"ds_read_b64_tr_b16 v[178:179],v102 offset:50176\n"
"ds_read_b64_tr_b16 v[180:181],v103 offset:49152\n"
"ds_read_b64_tr_b16 v[182:183],v103 offset:50176\n"
"ds_read_b64_tr_b16 v[184:185],v104 offset:49152\n"
"ds_read_b64_tr_b16 v[186:187],v104 offset:50176\n"
"ds_read_b64_tr_b16 v[188:189],v105 offset:49152\n"
"ds_read_b64_tr_b16 v[190:191],v105 offset:50176\n"
"s_waitcnt lgkmcnt(12)\n"
"v_mfma_f32_16x16x32_bf16 a[192:195],v[160:163],v[152:155],0\n"
"s_and_b32 s91,s89,7\n"
"s_mov_b32 s76,128\n"
"s_add_u32 m0,s79,32768\n"
"v_mfma_f32_16x16x32_bf16 a[224:227],v[160:163],v[156:159],0\n"
"buffer_load_dwordx4 v106,s[16:19],s76 offen offset:0 lds\n"
"v_mfma_f32_16x16x32_bf16 a[228:231],v[164:167],v[156:159],0\n"
"s_lshr_b32 s97,s89,3\n"
"s_lshl2_add_u32 s90,s88,s97\n"
"v_mfma_f32_16x16x32_bf16 a[196:199],v[164:167],v[152:155],0\n"
"buffer_load_dwordx4 v107,s[16:19],s76 offen offset:1024 lds\n"
"s_waitcnt lgkmcnt(8)\n"
"v_mfma_f32_16x16x32_bf16 a[200:203],v[168:171],v[152:155],0\n"
"s_lshl_b32 vcc_lo,s90,8\n"
"s_add_u32 s28,s28,vcc_lo\n"
"s_addc_u32 s29,s29,0\n"
"v_mfma_f32_16x16x32_bf16 a[232:235],v[168:171],v[156:159],0\n"
"buffer_load_dwordx4 v108,s[16:19],s76 offen offset:2048 lds\n"
"v_mfma_f32_16x16x32_bf16 a[236:239],v[172:175],v[156:159],0\n"
"s_sub_u32 s30,8388608,vcc_lo\n"
"s_mov_b32 s31,0x20000\n"
"buffer_load_dwordx4 v109,s[16:19],s76 offen offset:3072 lds\n"
"v_mfma_f32_16x16x32_bf16 a[204:207],v[172:175],v[152:155],0\n"
"s_waitcnt vmcnt(9)\n"
"s_barrier\n"
"s_waitcnt lgkmcnt(4)\n"
"v_mfma_f32_16x16x32_bf16 a[208:211],v[176:179],v[152:155],0\n"
"ds_read_b128 v[128:131],v80 offset:0\n"
"ds_read_b128 v[192:195],v81 offset:0\n"
"v_mfma_f32_16x16x32_bf16 a[240:243],v[176:179],v[156:159],0\n"
"ds_read_b64_tr_b16 v[224:225],v98 offset:32768\n"
"ds_read_b64_tr_b16 v[226:227],v98 offset:33792\n"
"v_mfma_f32_16x16x32_bf16 a[244:247],v[180:183],v[156:159],0\n"
"ds_read_b64_tr_b16 v[228:229],v99 offset:32768\n"
"ds_read_b64_tr_b16 v[230:231],v99 offset:33792\n"
"v_mfma_f32_16x16x32_bf16 a[212:215],v[180:183],v[152:155],0\n"
"ds_read_b64_tr_b16 v[232:233],v100 offset:32768\n"
"ds_read_b64_tr_b16 v[234:235],v100 offset:33792\n"
"s_waitcnt lgkmcnt(8)\n"
"v_mfma_f32_16x16x32_bf16 a[216:219],v[184:187],v[152:155],0\n"
"ds_read_b64_tr_b16 v[236:237],v101 offset:32768\n"
"ds_read_b64_tr_b16 v[238:239],v101 offset:33792\n"
"v_mfma_f32_16x16x32_bf16 a[248:251],v[184:187],v[156:159],0\n"
"ds_read_b64_tr_b16 v[240:241],v102 offset:32768\n"
"ds_read_b64_tr_b16 v[242:243],v102 offset:33792\n"
"v_mfma_f32_16x16x32_bf16 a[252:255],v[188:191],v[156:159],0\n"
"ds_read_b64_tr_b16 v[244:245],v103 offset:32768\n"
"ds_read_b64_tr_b16 v[246:247],v103 offset:33792\n"
"v_mfma_f32_16x16x32_bf16 a[220:223],v[188:191],v[152:155],0\n"
"ds_read_b64_tr_b16 v[248:249],v104 offset:32768\n"
"ds_read_b64_tr_b16 v[250:251],v104 offset:33792\n"
"s_waitcnt lgkmcnt(10)\n"
"v_mfma_f32_16x16x32_bf16 a[0:3],v[160:163],v[128:131],0\n"
"ds_read_b64_tr_b16 v[252:253],v105 offset:32768\n"
"ds_read_b64_tr_b16 v[254:255],v105 offset:33792\n"
"v_mfma_f32_16x16x32_bf16 a[0:3],v[224:227],v[192:195],a[0:3]\n"
"ds_read_b128 v[132:135],v80 offset:2048\n"
"ds_read_b128 v[196:199],v81 offset:2048\n"
"v_mfma_f32_16x16x32_bf16 a[4:7],v[228:231],v[192:195],0\n"
"ds_read_b128 v[216:219],v81 offset:12288\n"
"ds_read_b128 v[220:223],v81 offset:14336\n"
"v_mfma_f32_16x16x32_bf16 a[4:7],v[164:167],v[128:131],a[4:7]\n"
"s_waitcnt lgkmcnt(12)\n"
"s_mov_b32 s76,262272\n"
"s_add_u32 m0,s79,36864\n"
"v_mfma_f32_16x16x32_bf16 a[8:11],v[168:171],v[128:131],0\n"
"buffer_load_dwordx4 v106,s[16:19],s76 offen offset:0 lds\n"
"v_mfma_f32_16x16x32_bf16 a[8:11],v[232:235],v[192:195],a[8:11]\n"
"buffer_load_dwordx4 v107,s[16:19],s76 offen offset:1024 lds\n"
"v_mfma_f32_16x16x32_bf16 a[12:15],v[236:239],v[192:195],0\n"
"buffer_load_dwordx4 v108,s[16:19],s76 offen offset:2048 lds\n"
"buffer_load_dwordx4 v109,s[16:19],s76 offen offset:3072 lds\n"
"v_mfma_f32_16x16x32_bf16 a[12:15],v[172:175],v[128:131],a[12:15]\n"
"s_waitcnt lgkmcnt(8)\n"
"s_mov_b32 s77,524288\n"
"s_add_u32 m0,s78,98304\n"
"v_mfma_f32_16x16x32_bf16 a[16:19],v[176:179],v[128:131],0\n"
"buffer_load_dwordx4 v110,s[20:23],s77 offen offset:0 lds\n"
"v_mfma_f32_16x16x32_bf16 a[16:19],v[240:243],v[192:195],a[16:19]\n"
"buffer_load_dwordx4 v111,s[20:23],s77 offen offset:1024 lds\n"
"v_mfma_f32_16x16x32_bf16 a[20:23],v[244:247],v[192:195],0\n"
"buffer_load_dwordx4 v113,s[20:23],s77 offen offset:2048 lds\n"
"buffer_load_dwordx4 v114,s[20:23],s77 offen offset:3072 lds\n"
"v_mfma_f32_16x16x32_bf16 a[20:23],v[180:183],v[128:131],a[20:23]\n"
"s_waitcnt lgkmcnt(4)\n"
"s_mov_b32 s77,786432\n"
"s_add_u32 m0,s78,114688\n"
"v_mfma_f32_16x16x32_bf16 a[24:27],v[184:187],v[128:131],0\n"
"buffer_load_dwordx4 v110,s[20:23],s77 offen offset:0 lds\n"
"v_mfma_f32_16x16x32_bf16 a[24:27],v[248:251],v[192:195],a[24:27]\n"
"buffer_load_dwordx4 v111,s[20:23],s77 offen offset:1024 lds\n"
"v_mfma_f32_16x16x32_bf16 a[28:31],v[252:255],v[192:195],0\n"
"buffer_load_dwordx4 v113,s[20:23],s77 offen offset:2048 lds\n"
"buffer_load_dwordx4 v114,s[20:23],s77 offen offset:3072 lds\n"
"v_mfma_f32_16x16x32_bf16 a[28:31],v[188:191],v[128:131],a[28:31]\n"
"s_waitcnt lgkmcnt(2)\n"
"s_mov_b32 s76,256\n"
"s_add_u32 m0,s79,65536\n"
"v_mfma_f32_16x16x32_bf16 a[32:35],v[160:163],v[132:135],0\n"
"buffer_load_dwordx4 v106,s[16:19],s76 offen offset:0 lds\n"
"v_mfma_f32_16x16x32_bf16 a[32:35],v[224:227],v[196:199],a[32:35]\n"
"buffer_load_dwordx4 v107,s[16:19],s76 offen offset:1024 lds\n"
"v_mfma_f32_16x16x32_bf16 a[36:39],v[228:231],v[196:199],0\n"
"buffer_load_dwordx4 v108,s[16:19],s76 offen offset:2048 lds\n"
"buffer_load_dwordx4 v109,s[16:19],s76 offen offset:3072 lds\n"
"v_mfma_f32_16x16x32_bf16 a[36:39],v[164:167],v[132:135],a[36:39]\n"
"s_waitcnt vmcnt(21)\n"
"s_barrier\n"
"v_mfma_f32_16x16x32_bf16 a[40:43],v[168:171],v[132:135],0\n"
"ds_read_b128 v[136:139],v80 offset:4096\n"
"ds_read_b128 v[200:203],v81 offset:4096\n"
"v_mfma_f32_16x16x32_bf16 a[40:43],v[232:235],v[196:199],a[40:43]\n"
"ds_read_b128 v[140:143],v80 offset:6144\n"
"ds_read_b128 v[204:207],v81 offset:6144\n"
"v_mfma_f32_16x16x32_bf16 a[44:47],v[236:239],v[196:199],0\n"
"ds_read_b128 v[144:147],v80 offset:8192\n"
"ds_read_b128 v[208:211],v81 offset:8192\n"
"v_mfma_f32_16x16x32_bf16 a[44:47],v[172:175],v[132:135],a[44:47]\n"
"ds_read_b128 v[148:151],v80 offset:10240\n"
"ds_read_b128 v[212:215],v81 offset:10240\n"
"v_mfma_f32_16x16x32_bf16 a[48:51],v[176:179],v[132:135],0\n"
"s_mov_b32 s60,s91\n"
"s_mov_b32 s76,262400\n"
"s_add_u32 m0,s79,69632\n"
"v_mfma_f32_16x16x32_bf16 a[48:51],v[240:243],v[196:199],a[48:51]\n"
"buffer_load_dwordx4 v106,s[16:19],s76 offen offset:0 lds\n"
"v_mfma_f32_16x16x32_bf16 a[52:55],v[244:247],v[196:199],0\n"
"buffer_load_dwordx4 v107,s[16:19],s76 offen offset:1024 lds\n"
"v_mfma_f32_16x16x32_bf16 a[52:55],v[180:183],v[132:135],a[52:55]\n"
"buffer_load_dwordx4 v108,s[16:19],s76 offen offset:2048 lds\n"
"v_mfma_f32_16x16x32_bf16 a[56:59],v[184:187],v[132:135],0\n"
"buffer_load_dwordx4 v109,s[16:19],s76 offen offset:3072 lds\n"
"v_mfma_f32_16x16x32_bf16 a[56:59],v[248:251],v[196:199],a[56:59]\n"
"s_sub_u32 s94,15,s91\n"
"s_waitcnt lgkmcnt(0)\n"
"s_barrier\n"
"v_mfma_f32_16x16x32_bf16 a[60:63],v[252:255],v[196:199],0\n"
"s_cmp_lt_u32 s52,2\n"
"s_cselect_b32 s95,s94,s91\n"
"s_mov_b32 s77,1048576\n"
"s_add_u32 m0,s78,147456\n"
"v_mfma_f32_16x16x32_bf16 a[60:63],v[188:191],v[132:135],a[60:63]\n"
"buffer_load_dwordx4 v110,s[20:23],s77 offen offset:0 lds\n"
"v_mfma_f32_16x16x32_bf16 a[64:67],v[160:163],v[136:139],0\n"
"s_lshr_b32 vcc_lo,s95,1\n"
"s_lshl_b32 s55,vcc_lo,19\n"
"v_mfma_f32_16x16x32_bf16 a[64:67],v[224:227],v[200:203],a[64:67]\n"
"s_lshl_b32 s58,vcc_lo,10\n"
"s_add_u32 s58,s58,148898816\n"
"v_mfma_f32_16x16x32_bf16 a[68:71],v[228:231],v[200:203],0\n"
"s_and_b32 vcc_lo,s95,1\n"
"s_lshl_b32 vcc_lo,vcc_lo,13\n"
"v_mfma_f32_16x16x32_bf16 a[68:71],v[164:167],v[136:139],a[68:71]\n"
"buffer_load_dwordx4 v111,s[20:23],s77 offen offset:1024 lds\n"
"v_mfma_f32_16x16x32_bf16 a[72:75],v[168:171],v[136:139],0\n"
"s_and_b32 vcc_hi,s78,4096\n"
"s_add_u32 s55,s55,vcc_lo\n"
"v_mfma_f32_16x16x32_bf16 a[72:75],v[232:235],v[200:203],a[72:75]\n"
"s_add_u32 s55,s55,vcc_hi\n"
"s_and_b32 vcc_lo,s89,2\n"
"v_mfma_f32_16x16x32_bf16 a[76:79],v[236:239],v[200:203],0\n"
"s_lshl2_add_u32 vcc_lo,s88,vcc_lo\n"
"s_and_b32 vcc_hi,s52,1\n"
"v_mfma_f32_16x16x32_bf16 a[76:79],v[172:175],v[136:139],a[76:79]\n"
"buffer_load_dwordx4 v113,s[20:23],s77 offen offset:2048 lds\n"
"v_mfma_f32_16x16x32_bf16 a[80:83],v[176:179],v[136:139],0\n"
"s_add_u32 vcc_lo,vcc_lo,vcc_hi\n"
"s_lshr_b32 vcc_hi,s89,2\n"
"v_mfma_f32_16x16x32_bf16 a[80:83],v[240:243],v[200:203],a[80:83]\n"
"s_lshr_b32 s84,s52,1\n"
"s_lshl1_add_u32 vcc_hi,vcc_hi,s84\n"
"v_mfma_f32_16x16x32_bf16 a[84:87],v[244:247],v[200:203],0\n"
"s_mul_i32 s56,vcc_lo,1179648\n"
"s_lshl_b32 s84,s84,19\n"
"v_mfma_f32_16x16x32_bf16 a[84:87],v[180:183],v[136:139],a[84:87]\n"
"buffer_load_dwordx4 v114,s[20:23],s77 offen offset:3072 lds\n"
"v_mfma_f32_16x16x32_bf16 a[88:91],v[184:187],v[136:139],0\n"
"s_add_u32 s56,s56,s84\n"
"s_lshr_b32 s84,s89,2\n"
"v_mfma_f32_16x16x32_bf16 a[88:91],v[248:251],v[200:203],a[88:91]\n"
"s_lshl_b32 s84,s84,14\n"
"s_add_u32 s56,s56,s84\n"
"v_mfma_f32_16x16x32_bf16 a[92:95],v[252:255],v[200:203],0\n"
"s_lshl_b32 s84,s53,13\n"
"s_mov_b32 s77,1310720\n"
"s_add_u32 m0,s78,131072\n"
"v_mfma_f32_16x16x32_bf16 a[92:95],v[188:191],v[136:139],a[92:95]\n"
"buffer_load_dwordx4 v110,s[20:23],s77 offen offset:0 lds\n"
"v_mfma_f32_16x16x32_bf16 a[96:99],v[160:163],v[140:143],0\n"
"s_add_u32 s56,s56,s84\n"
"s_add_u32 s56,s56,4194304\n"
"v_mfma_f32_16x16x32_bf16 a[96:99],v[224:227],v[204:207],a[96:99]\n"
"s_lshl4_add_u32 vcc_hi,s53,vcc_hi\n"
"s_lshl_b32 vcc_lo,vcc_lo,8\n"
"v_mfma_f32_16x16x32_bf16 a[100:103],v[228:231],v[204:207],0\n"
"buffer_load_dwordx4 v111,s[20:23],s77 offen offset:1024 lds\n"
"v_mfma_f32_16x16x32_bf16 a[100:103],v[164:167],v[140:143],a[100:103]\n"
"s_lshl3_add_u32 s59,vcc_hi,vcc_lo\n"
"s_add_u32 s59,s59,148914176\n"
"v_mfma_f32_16x16x32_bf16 a[104:107],v[168:171],v[140:143],0\n"
"s_lshl_b32 s57,s92,3\n"
"s_add_u32 s57,s57,148898816\n"
"v_mfma_f32_16x16x32_bf16 a[104:107],v[232:235],v[204:207],a[104:107]\n"
"s_lshl_b32 s63,s90,8\n"
"s_add_u32 s63,s63,148914176\n"
"v_mfma_f32_16x16x32_bf16 a[108:111],v[236:239],v[204:207],0\n"
"buffer_load_dwordx4 v113,s[20:23],s77 offen offset:2048 lds\n"
"v_mfma_f32_16x16x32_bf16 a[108:111],v[172:175],v[140:143],a[108:111]\n"
"s_lshl_b32 s74,s92,11\n"
"s_add_u32 s74,s74,171966464\n"
"v_mfma_f32_16x16x32_bf16 a[112:115],v[176:179],v[140:143],0\n"
"s_mul_i32 s96,s90,1179648\n"
"s_add_u32 s96,s96,4194304\n"
"v_mfma_f32_16x16x32_bf16 a[112:115],v[240:243],v[204:207],a[112:115]\n"
"s_add_u32 s73,s96,s85\n"
"s_and_b32 vcc_hi,s78,4096\n"
"v_mfma_f32_16x16x32_bf16 a[116:119],v[244:247],v[204:207],0\n"
"buffer_load_dwordx4 v114,s[20:23],s77 offen offset:3072 lds\n"
"v_mfma_f32_16x16x32_bf16 a[116:119],v[180:183],v[140:143],a[116:119]\n"
"s_lshl_b32 vcc_hi,vcc_hi,1\n"
"s_add_u32 vcc_lo,s96,vcc_hi\n"
"v_mfma_f32_16x16x32_bf16 a[120:123],v[184:187],v[140:143],0\n"
"s_add_u32 s65,vcc_lo,524288\n"
"s_lshl_b32 s66,s91,16\n"
"v_mfma_f32_16x16x32_bf16 a[120:123],v[248:251],v[204:207],a[120:123]\n"
"s_sub_u32 s66,vcc_lo,s66\n"
"s_mov_b32 s76,384\n"
"s_add_u32 m0,s79,0\n"
"v_mfma_f32_16x16x32_bf16 a[124:127],v[252:255],v[204:207],0\n"
"buffer_load_dwordx4 v106,s[16:19],s76 offen offset:0 lds\n"
"v_mfma_f32_16x16x32_bf16 a[124:127],v[188:191],v[140:143],a[124:127]\n"
"s_add_u32 s66,s66,999424\n"
"s_add_u32 s67,vcc_hi,65536\n"
"v_mfma_f32_16x16x32_bf16 a[128:131],v[160:163],v[144:147],0\n"
"s_mul_i32 vcc_lo,vcc_hi,5\n"
"s_sub_u32 vcc_lo,s96,vcc_lo\n"
"v_mfma_f32_16x16x32_bf16 a[128:131],v[224:227],v[208:211],a[128:131]\n"
"s_add_u32 s64,vcc_lo,81920\n"
"s_add_u32 s62,vcc_lo,135168\n"
"s_add_u32 s61,vcc_lo,524288\n"
"v_mfma_f32_16x16x32_bf16 a[132:135],v[228:231],v[208:211],0\n"
"buffer_load_dwordx4 v107,s[16:19],s76 offen offset:1024 lds\n"
"v_mfma_f32_16x16x32_bf16 a[132:135],v[164:167],v[144:147],a[132:135]\n"
"s_cmp_lt_u32 s52,2\n"
"s_cselect_b32 s64,s64,2146435072\n"
"s_cselect_b32 s67,s67,163840\n"
"s_cselect_b32 s65,s65,1\n"
"v_mfma_f32_16x16x32_bf16 a[136:139],v[168:171],v[144:147],0\n"
"s_cselect_b32 vcc_lo,4,0\n"
"s_and_b32 vcc_hi,s52,1\n"
"s_add_u32 vcc_lo,vcc_lo,vcc_hi\n"
"v_mfma_f32_16x16x32_bf16 a[136:139],v[232:235],v[208:211],a[136:139]\n"
"s_lshl_b32 vcc_hi,s90,12\n"
"s_add_u32 vcc_hi,vcc_hi,54525952\n"
"v_mfma_f32_16x16x32_bf16 a[140:143],v[236:239],v[208:211],0\n"
"buffer_load_dwordx4 v108,s[16:19],s76 offen offset:2048 lds\n"
"v_mfma_f32_16x16x32_bf16 a[140:143],v[172:175],v[144:147],a[140:143]\n"
"s_lshl3_add_u32 s70,s95,vcc_lo\n"
"s_lshl_b32 s70,s70,17\n"
"v_mfma_f32_16x16x32_bf16 a[144:147],v[176:179],v[144:147],0\n"
"s_add_u32 s70,s70,vcc_hi\n"
"s_lshl3_add_u32 s68,s94,vcc_lo\n"
"v_mfma_f32_16x16x32_bf16 a[144:147],v[240:243],v[208:211],a[144:147]\n"
"s_lshl_b32 s68,s68,17\n"
"s_add_u32 s68,s68,vcc_hi\n"
"v_mfma_f32_16x16x32_bf16 a[148:151],v[244:247],v[208:211],0\n"
"buffer_load_dwordx4 v109,s[16:19],s76 offen offset:3072 lds\n"
"v_mfma_f32_16x16x32_bf16 a[148:151],v[180:183],v[144:147],a[148:151]\n"
"s_lshr_b32 vcc_lo,s90,3\n"
"s_lshl2_add_u32 vcc_lo,vcc_lo,s52\n"
"v_mfma_f32_16x16x32_bf16 a[152:155],v[184:187],v[144:147],0\n"
"s_lshl_b32 vcc_lo,vcc_lo,7\n"
"s_and_b32 vcc_hi,s90,7\n"
"v_mfma_f32_16x16x32_bf16 a[152:155],v[248:251],v[208:211],a[152:155]\n"
"s_add_u32 vcc_lo,vcc_lo,vcc_hi\n"
"s_mov_b32 s76,262528\n"
"s_add_u32 m0,s79,4096\n"
"v_mfma_f32_16x16x32_bf16 a[156:159],v[252:255],v[208:211],0\n"
"buffer_load_dwordx4 v106,s[16:19],s76 offen offset:0 lds\n"
"v_mfma_f32_16x16x32_bf16 a[156:159],v[188:191],v[144:147],a[156:159]\n"
"s_lshl3_add_u32 s69,s91,vcc_lo\n"
"s_lshl_b32 s69,s69,3\n"
"v_mfma_f32_16x16x32_bf16 a[160:163],v[160:163],v[148:151],0\n"
"s_add_u32 s69,s69,148930560\n"
"s_lshl3_add_u32 s71,s94,vcc_lo\n"
"v_mfma_f32_16x16x32_bf16 a[160:163],v[224:227],v[212:215],a[160:163]\n"
"s_lshl_b32 s71,s71,3\n"
"s_add_u32 s71,s71,148930560\n"
"v_mfma_f32_16x16x32_bf16 a[164:167],v[228:231],v[212:215],0\n"
"buffer_load_dwordx4 v107,s[16:19],s76 offen offset:1024 lds\n"
"v_mfma_f32_16x16x32_bf16 a[164:167],v[164:167],v[148:151],a[164:167]\n"
"s_mov_b32 s6,8388608\n"
"s_mov_b32 s7,0x20000\n"
"v_mfma_f32_16x16x32_bf16 a[168:171],v[168:171],v[148:151],0\n"
"s_lshr_b32 s94,s88,1\n"
"s_and_b32 vcc_lo,s91,1\n"
"v_mfma_f32_16x16x32_bf16 a[168:171],v[232:235],v[212:215],a[168:171]\n"
"s_lshl1_add_u32 s95,s97,vcc_lo\n"
"s_and_b32 vcc_lo,s88,1\n"
"v_mfma_f32_16x16x32_bf16 a[172:175],v[236:239],v[212:215],0\n"
"buffer_load_dwordx4 v108,s[16:19],s76 offen offset:2048 lds\n"
"v_mfma_f32_16x16x32_bf16 a[172:175],v[172:175],v[148:151],a[172:175]\n"
"s_lshr_b32 vcc_hi,s91,1\n"
"s_lshl2_add_u32 s96,vcc_lo,vcc_hi\n"
"v_mfma_f32_16x16x32_bf16 a[176:179],v[176:179],v[148:151],0\n"
"s_lshr_b32 vcc_lo,s88,2\n"
"s_lshl2_add_u32 s96,vcc_lo,vcc_hi\n"
"v_mfma_f32_16x16x32_bf16 a[176:179],v[240:243],v[212:215],a[176:179]\n"
"s_sub_u32 vcc_lo,7,s88\n"
"s_min_u32 s94,vcc_lo,s88\n"
"v_mfma_f32_16x16x32_bf16 a[180:183],v[244:247],v[212:215],0\n"
"buffer_load_dwordx4 v109,s[16:19],s76 offen offset:3072 lds\n"
"v_mfma_f32_16x16x32_bf16 a[180:183],v[180:183],v[148:151],a[180:183]\n"
"s_and_b32 s96,s88,4\n"
"s_add_u32 s96,s96,s97\n"
"s_add_u32 s95,s91,s96\n"
"v_mfma_f32_16x16x32_bf16 a[184:187],v[184:187],v[148:151],0\n"
"buffer_load_dwordx4 v[8:11],v70,s[32:35],0 offen offset:0\n"
"v_mfma_f32_16x16x32_bf16 a[184:187],v[248:251],v[212:215],a[184:187]\n"
"buffer_load_dwordx4 v[12:15],v70,s[32:35],0 offen offset:512\n"
"s_and_b32 s95,s95,7\n"
"v_mfma_f32_16x16x32_bf16 a[188:191],v[252:255],v[212:215],0\n"
"buffer_load_dwordx4 v[16:19],v70,s[32:35],0 offen offset:1024\n"
"buffer_load_dwordx4 v[20:23],v70,s[32:35],0 offen offset:1536\n"
"s_lshl3_add_u32 vcc_lo,s94,s96\n"
"v_mfma_f32_16x16x32_bf16 a[188:191],v[188:191],v[148:151],a[188:191]\n"
"s_waitcnt vmcnt(28)\n"
"s_barrier\n"
"v_mfma_f32_16x16x32_bf16 a[192:195],v[224:227],v[216:219],a[192:195]\n"
"ds_read_b128 v[152:155],v80 offset:45056\n"
"s_mov_b32 s87,524288\n"
"v_mfma_f32_16x16x32_bf16 a[224:227],v[224:227],v[220:223],a[224:227]\n"
"ds_read_b128 v[156:159],v80 offset:47104\n"
"buffer_load_dword v115,v112,s[36:39],s87 offen\n"
"v_mfma_f32_16x16x32_bf16 a[228:231],v[228:231],v[220:223],a[228:231]\n"
"ds_read_b64_tr_b16 v[160:161],v98 offset:0\n"
"s_mov_b32 s87,1048576\n"
"v_mfma_f32_16x16x32_bf16 a[196:199],v[228:231],v[216:219],a[196:199]\n"
"ds_read_b64_tr_b16 v[162:163],v98 offset:1024\n"
"buffer_load_dword v115,v112,s[36:39],s87 offen\n"
"v_mfma_f32_16x16x32_bf16 a[200:203],v[232:235],v[216:219],a[200:203]\n"
"ds_read_b64_tr_b16 v[164:165],v99 offset:0\n"
"s_mov_b32 s87,1572864\n"
"v_mfma_f32_16x16x32_bf16 a[232:235],v[232:235],v[220:223],a[232:235]\n"
"ds_read_b64_tr_b16 v[166:167],v99 offset:1024\n"
"buffer_load_dword v115,v112,s[36:39],s87 offen\n"
"v_mfma_f32_16x16x32_bf16 a[236:239],v[236:239],v[220:223],a[236:239]\n"
"ds_read_b64_tr_b16 v[168:169],v100 offset:0\n"
"s_lshl3_add_u32 vcc_lo,vcc_lo,s52\n"
"v_mfma_f32_16x16x32_bf16 a[204:207],v[236:239],v[216:219],a[204:207]\n"
"ds_read_b64_tr_b16 v[170:171],v100 offset:1024\n"
"s_lshl_b32 s13,vcc_lo,15\n"
"v_mfma_f32_16x16x32_bf16 a[208:211],v[240:243],v[216:219],a[208:211]\n"
"ds_read_b64_tr_b16 v[172:173],v101 offset:0\n"
"s_lshl_b32 vcc_lo,s95,8\n"
"v_mfma_f32_16x16x32_bf16 a[240:243],v[240:243],v[220:223],a[240:243]\n"
"ds_read_b64_tr_b16 v[174:175],v101 offset:1024\n"
"s_add_u32 s13,s13,vcc_lo\n"
"v_mfma_f32_16x16x32_bf16 a[244:247],v[244:247],v[220:223],a[244:247]\n"
"ds_read_b64_tr_b16 v[176:177],v102 offset:0\n"
"s_lshl3_add_u32 vcc_hi,s94,s95\n"
"v_mfma_f32_16x16x32_bf16 a[212:215],v[244:247],v[216:219],a[212:215]\n"
"ds_read_b64_tr_b16 v[178:179],v102 offset:1024\n"
"s_lshl_b32 s72,vcc_hi,8\n"
"v_mfma_f32_16x16x32_bf16 a[216:219],v[248:251],v[216:219],a[216:219]\n"
"ds_read_b64_tr_b16 v[180:181],v103 offset:0\n"
"s_add_u32 s72,s72,149061632\n"
"v_mfma_f32_16x16x32_bf16 a[248:251],v[248:251],v[220:223],a[248:251]\n"
"s_or_b32 vcc_lo,s96,s52\n"
"ds_read_b64_tr_b16 v[182:183],v103 offset:1024\n"
"s_cselect_b32 s72,2146435072,s72\n"
"v_mfma_f32_16x16x32_bf16 a[252:255],v[252:255],v[220:223],a[252:255]\n"
"ds_read_b64_tr_b16 v[184:185],v104 offset:0\n"
"s_lshl_b32 s3,vcc_hi,18\n"
"v_mfma_f32_16x16x32_bf16 a[220:223],v[252:255],v[216:219],a[220:223]\n"
"s_lshl_b32 vcc_lo,s96,15\n"
"ds_read_b64_tr_b16 v[186:187],v104 offset:1024\n"
"s_waitcnt lgkmcnt(10)\n"
"v_mfma_f32_16x16x32_bf16 a[192:195],v[160:163],v[152:155],a[192:195]\n"
"ds_read_b64_tr_b16 v[188:189],v105 offset:0\n"
"s_sub_u32 s3,s3,vcc_lo\n"
"v_mfma_f32_16x16x32_bf16 a[224:227],v[160:163],v[156:159],a[224:227]\n"
"ds_read_b64_tr_b16 v[190:191],v105 offset:1024\n"
"s_add_u32 s3,s3,s78\n"
"v_mfma_f32_16x16x32_bf16 a[228:231],v[164:167],v[156:159],a[228:231]\n"
"ds_read_b128 v[128:131],v80 offset:32768\n"
"s_add_u32 s3,s3,100909056\n"
"v_mfma_f32_16x16x32_bf16 a[196:199],v[164:167],v[152:155],a[196:199]\n"
"ds_read_b128 v[192:195],v81 offset:32768\n"
"s_waitcnt lgkmcnt(10)\n"
"v_mfma_f32_16x16x32_bf16 a[200:203],v[168:171],v[152:155],a[200:203]\n"
"ds_read_b64_tr_b16 v[224:225],v98 offset:16384\n"
"s_lshl3_add_u32 vcc_hi,vcc_hi,s96\n"
"v_mfma_f32_16x16x32_bf16 a[232:235],v[168:171],v[156:159],a[232:235]\n"
"ds_read_b64_tr_b16 v[226:227],v98 offset:17408\n"
"s_lshl2_add_u32 vcc_hi,vcc_hi,s52\n"
"v_mfma_f32_16x16x32_bf16 a[236:239],v[172:175],v[156:159],a[236:239]\n"
"ds_read_b64_tr_b16 v[228:229],v99 offset:16384\n"
"s_lshl_b32 s2,vcc_hi,4\n"
"v_mfma_f32_16x16x32_bf16 a[204:207],v[172:175],v[152:155],a[204:207]\n"
"ds_read_b64_tr_b16 v[230:231],v99 offset:17408\n"
"s_waitcnt lgkmcnt(10)\n"
"v_mfma_f32_16x16x32_bf16 a[208:211],v[176:179],v[152:155],a[208:211]\n"
"ds_read_b64_tr_b16 v[232:233],v100 offset:16384\n"
"s_add_u32 s2,s2,149028864\n"
"v_mfma_f32_16x16x32_bf16 a[240:243],v[176:179],v[156:159],a[240:243]\n"
"ds_read_b64_tr_b16 v[234:235],v100 offset:17408\n"
"s_lshl2_add_u32 vcc_lo,s95,s52\n"
"v_mfma_f32_16x16x32_bf16 a[244:247],v[180:183],v[156:159],a[244:247]\n"
"ds_read_b64_tr_b16 v[236:237],v101 offset:16384\n"
"s_lshl_b32 vcc_lo,vcc_lo,10\n"
"v_mfma_f32_16x16x32_bf16 a[212:215],v[180:183],v[152:155],a[212:215]\n"
"ds_read_b64_tr_b16 v[238:239],v101 offset:17408\n"
"s_waitcnt lgkmcnt(10)\n"
"v_mfma_f32_16x16x32_bf16 a[216:219],v[184:187],v[152:155],a[216:219]\n"
"ds_read_b64_tr_b16 v[240:241],v102 offset:16384\n"
"s_lshl_b32 vcc_hi,s94,22\n"
"v_mfma_f32_16x16x32_bf16 a[248:251],v[184:187],v[156:159],a[248:251]\n"
"ds_read_b64_tr_b16 v[242:243],v102 offset:17408\n"
"s_add_u32 s12,vcc_lo,vcc_hi\n"
"v_mfma_f32_16x16x32_bf16 a[252:255],v[188:191],v[156:159],a[252:255]\n"
"ds_read_b64_tr_b16 v[244:245],v103 offset:16384\n"
"s_lshl_b32 vcc_lo,s96,19\n"
"v_mfma_f32_16x16x32_bf16 a[220:223],v[188:191],v[152:155],a[220:223]\n"
"ds_read_b64_tr_b16 v[246:247],v103 offset:17408\n"
"s_waitcnt lgkmcnt(8)\n"
"v_mfma_f32_16x16x32_bf16 a[0:3],v[160:163],v[128:131],a[0:3]\n"
"ds_read_b64_tr_b16 v[248:249],v104 offset:16384\n"
"s_add_u32 s10,s12,vcc_lo\n"
"v_mfma_f32_16x16x32_bf16 a[0:3],v[224:227],v[192:195],a[0:3]\n"
"ds_read_b64_tr_b16 v[250:251],v104 offset:17408\n"
"s_add_u32 s10,s10,71303168\n"
"v_mfma_f32_16x16x32_bf16 a[4:7],v[228:231],v[192:195],a[4:7]\n"
"ds_read_b64_tr_b16 v[252:253],v105 offset:16384\n"
"s_mul_i32 vcc_lo,s52,31744\n"
"v_mfma_f32_16x16x32_bf16 a[4:7],v[164:167],v[128:131],a[4:7]\n"
"ds_read_b64_tr_b16 v[254:255],v105 offset:17408\n"
"s_waitcnt lgkmcnt(8)\n"
"v_mfma_f32_16x16x32_bf16 a[8:11],v[168:171],v[128:131],a[8:11]\n"
"ds_read_b128 v[132:135],v80 offset:34816\n"
"s_add_u32 s10,s10,vcc_lo\n"
"v_mfma_f32_16x16x32_bf16 a[8:11],v[232:235],v[192:195],a[8:11]\n"
"ds_read_b128 v[196:199],v81 offset:34816\n"
"s_lshl_b32 vcc_lo,s52,10\n"
"v_mfma_f32_16x16x32_bf16 a[12:15],v[236:239],v[192:195],a[12:15]\n"
"ds_read_b128 v[136:139],v80 offset:36864\n"
"s_sub_u32 s12,57671680,vcc_lo\n"
"v_mfma_f32_16x16x32_bf16 a[12:15],v[172:175],v[128:131],a[12:15]\n"
"ds_read_b128 v[200:203],v81 offset:36864\n"
"s_waitcnt lgkmcnt(8)\n"
"v_mfma_f32_16x16x32_bf16 a[16:19],v[176:179],v[128:131],a[16:19]\n"
"ds_read_b128 v[140:143],v80 offset:38912\n"
"s_add_u32 s8,s79,54525952\n"
"v_mfma_f32_16x16x32_bf16 a[16:19],v[240:243],v[192:195],a[16:19]\n"
"ds_read_b128 v[204:207],v81 offset:38912\n"
"s_and_b32 vcc_lo,s96,1\n"
"v_mfma_f32_16x16x32_bf16 a[20:23],v[244:247],v[192:195],a[20:23]\n"
"ds_read_b128 v[144:147],v80 offset:40960\n"
"s_lshl_b32 s11,vcc_lo,10\n"
"v_mfma_f32_16x16x32_bf16 a[20:23],v[180:183],v[128:131],a[20:23]\n"
"ds_read_b128 v[208:211],v81 offset:40960\n"
"s_waitcnt lgkmcnt(8)\n"
"v_mfma_f32_16x16x32_bf16 a[24:27],v[184:187],v[128:131],a[24:27]\n"
"ds_read_b128 v[148:151],v80 offset:43008\n"
"s_add_u32 s11,s11,148932608\n"
"v_mfma_f32_16x16x32_bf16 a[24:27],v[248:251],v[192:195],a[24:27]\n"
"ds_read_b128 v[212:215],v81 offset:43008\n"
"s_mov_b32 s9,148931072\n"
"v_mfma_f32_16x16x32_bf16 a[28:31],v[252:255],v[192:195],a[28:31]\n"
"s_cmp_eq_u32 s52,0\n"
"ds_read_b128 v[216:219],v81 offset:45056\n"
"s_cselect_b32 s71,s71,2146435072\n"
"v_mfma_f32_16x16x32_bf16 a[28:31],v[188:191],v[128:131],a[28:31]\n"
"ds_read_b128 v[220:223],v81 offset:47104\n"
"s_waitcnt lgkmcnt(10)\n"
"v_mfma_f32_16x16x32_bf16 a[32:35],v[160:163],v[132:135],a[32:35]\n"
"v_mfma_f32_16x16x32_bf16 a[32:35],v[224:227],v[196:199],a[32:35]\n"
"v_mfma_f32_16x16x32_bf16 a[36:39],v[228:231],v[196:199],a[36:39]\n"
"v_mfma_f32_16x16x32_bf16 a[36:39],v[164:167],v[132:135],a[36:39]\n"
"v_mfma_f32_16x16x32_bf16 a[40:43],v[168:171],v[132:135],a[40:43]\n"
"s_waitcnt lgkmcnt(0)\n"
"s_barrier\n"
"v_mfma_f32_16x16x32_bf16 a[40:43],v[232:235],v[196:199],a[40:43]\n"
"s_mov_b32 s77,1572864\n"
"s_add_u32 m0,s78,98304\n"
"v_mfma_f32_16x16x32_bf16 a[44:47],v[236:239],v[196:199],a[44:47]\n"
"buffer_load_dwordx4 v110,s[20:23],s77 offen offset:0 lds\n"
"v_mfma_f32_16x16x32_bf16 a[44:47],v[172:175],v[132:135],a[44:47]\n"
"v_mfma_f32_16x16x32_bf16 a[48:51],v[176:179],v[132:135],a[48:51]\n"
"v_mfma_f32_16x16x32_bf16 a[48:51],v[240:243],v[196:199],a[48:51]\n"
"v_mfma_f32_16x16x32_bf16 a[52:55],v[244:247],v[196:199],a[52:55]\n"
"buffer_load_dwordx4 v111,s[20:23],s77 offen offset:1024 lds\n"
"v_mfma_f32_16x16x32_bf16 a[52:55],v[180:183],v[132:135],a[52:55]\n"
"v_mfma_f32_16x16x32_bf16 a[56:59],v[184:187],v[132:135],a[56:59]\n"
"v_mfma_f32_16x16x32_bf16 a[56:59],v[248:251],v[196:199],a[56:59]\n"
"v_mfma_f32_16x16x32_bf16 a[60:63],v[252:255],v[196:199],a[60:63]\n"
"buffer_load_dwordx4 v113,s[20:23],s77 offen offset:2048 lds\n"
"v_mfma_f32_16x16x32_bf16 a[60:63],v[188:191],v[132:135],a[60:63]\n"
"v_mfma_f32_16x16x32_bf16 a[64:67],v[160:163],v[136:139],a[64:67]\n"
"v_mfma_f32_16x16x32_bf16 a[64:67],v[224:227],v[200:203],a[64:67]\n"
"v_mfma_f32_16x16x32_bf16 a[68:71],v[228:231],v[200:203],a[68:71]\n"
"buffer_load_dwordx4 v114,s[20:23],s77 offen offset:3072 lds\n"
"v_mfma_f32_16x16x32_bf16 a[68:71],v[164:167],v[136:139],a[68:71]\n"
"v_mfma_f32_16x16x32_bf16 a[72:75],v[168:171],v[136:139],a[72:75]\n"
"v_mfma_f32_16x16x32_bf16 a[72:75],v[232:235],v[200:203],a[72:75]\n"
"s_mov_b32 s77,1835008\n"
"s_add_u32 m0,s78,114688\n"
"v_mfma_f32_16x16x32_bf16 a[76:79],v[236:239],v[200:203],a[76:79]\n"
"buffer_load_dwordx4 v110,s[20:23],s77 offen offset:0 lds\n"
"v_mfma_f32_16x16x32_bf16 a[76:79],v[172:175],v[136:139],a[76:79]\n"
"v_mfma_f32_16x16x32_bf16 a[80:83],v[176:179],v[136:139],a[80:83]\n"
"v_mfma_f32_16x16x32_bf16 a[80:83],v[240:243],v[200:203],a[80:83]\n"
"v_mfma_f32_16x16x32_bf16 a[84:87],v[244:247],v[200:203],a[84:87]\n"
"buffer_load_dwordx4 v111,s[20:23],s77 offen offset:1024 lds\n"
"v_mfma_f32_16x16x32_bf16 a[84:87],v[180:183],v[136:139],a[84:87]\n"
"v_mfma_f32_16x16x32_bf16 a[88:91],v[184:187],v[136:139],a[88:91]\n"
"v_mfma_f32_16x16x32_bf16 a[88:91],v[248:251],v[200:203],a[88:91]\n"
"v_mfma_f32_16x16x32_bf16 a[92:95],v[252:255],v[200:203],a[92:95]\n"
"buffer_load_dwordx4 v113,s[20:23],s77 offen offset:2048 lds\n"
"v_mfma_f32_16x16x32_bf16 a[92:95],v[188:191],v[136:139],a[92:95]\n"
"s_waitcnt vmcnt(10)\n"
"v_mfma_f32_16x16x32_bf16 a[96:99],v[160:163],v[140:143],a[96:99]\n"
"v_lshlrev_b32 v24,16,v8\n"
"v_and_b32 v25,0xffff0000,v8\n"
"v_mfma_f32_16x16x32_bf16 a[96:99],v[224:227],v[204:207],a[96:99]\n"
"v_lshlrev_b32 v26,16,v9\n"
"v_and_b32 v27,0xffff0000,v9\n"
"v_mfma_f32_16x16x32_bf16 a[100:103],v[228:231],v[204:207],a[100:103]\n"
"buffer_load_dwordx4 v114,s[20:23],s77 offen offset:3072 lds\n"
"v_mfma_f32_16x16x32_bf16 a[100:103],v[164:167],v[140:143],a[100:103]\n"
"v_lshlrev_b32 v28,16,v10\n"
"v_and_b32 v29,0xffff0000,v10\n"
"v_mfma_f32_16x16x32_bf16 a[104:107],v[168:171],v[140:143],a[104:107]\n"
"v_lshlrev_b32 v30,16,v11\n"
"v_and_b32 v31,0xffff0000,v11\n"
"v_mfma_f32_16x16x32_bf16 a[104:107],v[232:235],v[204:207],a[104:107]\n"
"v_lshlrev_b32 v32,16,v12\n"
"v_and_b32 v33,0xffff0000,v12\n"
"v_mfma_f32_16x16x32_bf16 a[108:111],v[236:239],v[204:207],a[108:111]\n"
"s_mov_b32 s76,512\n"
"v_lshlrev_b32 v34,16,v13\n"
"s_add_u32 m0,s79,32768\n"
"v_mfma_f32_16x16x32_bf16 a[108:111],v[172:175],v[140:143],a[108:111]\n"
"buffer_load_dwordx4 v106,s[16:19],s76 offen offset:0 lds\n"
"v_mfma_f32_16x16x32_bf16 a[112:115],v[176:179],v[140:143],a[112:115]\n"
"v_and_b32 v35,0xffff0000,v13\n"
"v_lshlrev_b32 v36,16,v14\n"
"v_mfma_f32_16x16x32_bf16 a[112:115],v[240:243],v[204:207],a[112:115]\n"
"v_and_b32 v37,0xffff0000,v14\n"
"v_lshlrev_b32 v38,16,v15\n"
"v_mfma_f32_16x16x32_bf16 a[116:119],v[244:247],v[204:207],a[116:119]\n"
"v_and_b32 v39,0xffff0000,v15\n"
"v_lshlrev_b32 v40,16,v16\n"
"v_mfma_f32_16x16x32_bf16 a[116:119],v[180:183],v[140:143],a[116:119]\n"
"v_and_b32 v41,0xffff0000,v16\n"
"v_lshlrev_b32 v42,16,v17\n"
"v_mfma_f32_16x16x32_bf16 a[120:123],v[184:187],v[140:143],a[120:123]\n"
"buffer_load_dwordx4 v107,s[16:19],s76 offen offset:1024 lds\n"
"v_mfma_f32_16x16x32_bf16 a[120:123],v[248:251],v[204:207],a[120:123]\n"
"v_and_b32 v43,0xffff0000,v17\n"
"v_lshlrev_b32 v44,16,v18\n"
"v_mfma_f32_16x16x32_bf16 a[124:127],v[252:255],v[204:207],a[124:127]\n"
"v_and_b32 v45,0xffff0000,v18\n"
"v_lshlrev_b32 v46,16,v19\n"
"v_mfma_f32_16x16x32_bf16 a[124:127],v[188:191],v[140:143],a[124:127]\n"
"v_and_b32 v47,0xffff0000,v19\n"
"v_lshlrev_b32 v48,16,v20\n"
"v_mfma_f32_16x16x32_bf16 a[128:131],v[160:163],v[144:147],a[128:131]\n"
"v_and_b32 v49,0xffff0000,v20\n"
"v_lshlrev_b32 v50,16,v21\n"
"v_mfma_f32_16x16x32_bf16 a[128:131],v[224:227],v[208:211],a[128:131]\n"
"buffer_load_dwordx4 v108,s[16:19],s76 offen offset:2048 lds\n"
"v_mfma_f32_16x16x32_bf16 a[132:135],v[228:231],v[208:211],a[132:135]\n"
"v_and_b32 v51,0xffff0000,v21\n"
"v_lshlrev_b32 v52,16,v22\n"
"v_mfma_f32_16x16x32_bf16 a[132:135],v[164:167],v[144:147],a[132:135]\n"
"v_and_b32 v53,0xffff0000,v22\n"
"v_lshlrev_b32 v54,16,v23\n"
"v_mfma_f32_16x16x32_bf16 a[136:139],v[168:171],v[144:147],a[136:139]\n"
"v_and_b32 v55,0xffff0000,v23\n"
"v_add_f32 v56,v24,v25\n"
"v_mfma_f32_16x16x32_bf16 a[136:139],v[232:235],v[208:211],a[136:139]\n"
"v_add_f32 v57,v26,v27\n"
"v_add_f32 v56,v56,v28\n"
"v_mfma_f32_16x16x32_bf16 a[140:143],v[236:239],v[208:211],a[140:143]\n"
"buffer_load_dwordx4 v109,s[16:19],s76 offen offset:3072 lds\n"
"v_mfma_f32_16x16x32_bf16 a[140:143],v[172:175],v[144:147],a[140:143]\n"
"v_add_f32 v57,v57,v29\n"
"v_add_f32 v56,v56,v30\n"
"v_mfma_f32_16x16x32_bf16 a[144:147],v[176:179],v[144:147],a[144:147]\n"
"v_add_f32 v57,v57,v31\n"
"v_add_f32 v56,v56,v32\n"
"v_mfma_f32_16x16x32_bf16 a[144:147],v[240:243],v[208:211],a[144:147]\n"
"v_readfirstlane_b32 s82,v78\n"
"v_readfirstlane_b32 s83,v79\n"
"v_mfma_f32_16x16x32_bf16 a[148:151],v[244:247],v[208:211],a[148:151]\n"
"s_mov_b32 s76,262656\n"
"v_add_f32 v57,v57,v33\n"
"s_add_u32 m0,s79,36864\n"
"v_mfma_f32_16x16x32_bf16 a[148:151],v[180:183],v[144:147],a[148:151]\n"
"buffer_load_dwordx4 v106,s[16:19],s76 offen offset:0 lds\n"
"s_mul_i32 s80,s82,0x9e3779b1\n"
"v_mfma_f32_16x16x32_bf16 a[152:155],v[184:187],v[144:147],a[152:155]\n"
"s_add_u32 s80,s80,0x7f4a7c15\n"
"v_add_f32 v56,v56,v34\n"
"v_add_f32 v57,v57,v35\n"
"v_mfma_f32_16x16x32_bf16 a[152:155],v[248:251],v[208:211],a[152:155]\n"
"s_add_u32 s81,s83,s80\n"
"s_add_u32 s81,s81,1\n"
"v_mfma_f32_16x16x32_bf16 a[156:159],v[252:255],v[208:211],a[156:159]\n"
"v_mov_b32 v124,s80\n"
"v_mov_b32 v125,s81\n"
"v_mfma_f32_16x16x32_bf16 a[156:159],v[188:191],v[144:147],a[156:159]\n"
"v_add_f32 v56,v56,v36\n"
"v_add_f32 v57,v57,v37\n"
"v_mfma_f32_16x16x32_bf16 a[160:163],v[160:163],v[148:151],a[160:163]\n"
"buffer_load_dwordx4 v107,s[16:19],s76 offen offset:1024 lds\n"
"v_mfma_f32_16x16x32_bf16 a[160:163],v[224:227],v[212:215],a[160:163]\n"
"v_add_f32 v56,v56,v38\n"
"v_add_f32 v57,v57,v39\n"
"v_mfma_f32_16x16x32_bf16 a[164:167],v[228:231],v[212:215],a[164:167]\n"
"v_add_f32 v56,v56,v40\n"
"v_add_f32 v57,v57,v41\n"
"v_mfma_f32_16x16x32_bf16 a[164:167],v[164:167],v[148:151],a[164:167]\n"
"v_add_f32 v56,v56,v42\n"
"v_add_f32 v57,v57,v43\n"
"v_mfma_f32_16x16x32_bf16 a[168:171],v[168:171],v[148:151],a[168:171]\n"
"v_add_f32 v56,v56,v44\n"
"v_add_f32 v57,v57,v45\n"
"v_mfma_f32_16x16x32_bf16 a[168:171],v[232:235],v[212:215],a[168:171]\n"
"buffer_load_dwordx4 v108,s[16:19],s76 offen offset:2048 lds\n"
"v_mfma_f32_16x16x32_bf16 a[172:175],v[236:239],v[212:215],a[172:175]\n"
"v_add_f32 v56,v56,v46\n"
"v_add_f32 v57,v57,v47\n"
"v_mfma_f32_16x16x32_bf16 a[172:175],v[172:175],v[148:151],a[172:175]\n"
"v_add_f32 v56,v56,v48\n"
"v_add_f32 v57,v57,v49\n"
"v_mfma_f32_16x16x32_bf16 a[176:179],v[176:179],v[148:151],a[176:179]\n"
"v_add_f32 v56,v56,v50\n"
"v_add_f32 v57,v57,v51\n"
"v_mfma_f32_16x16x32_bf16 a[176:179],v[240:243],v[212:215],a[176:179]\n"
"v_add_f32 v56,v56,v52\n"
"v_add_f32 v57,v57,v53\n"
"v_mfma_f32_16x16x32_bf16 a[180:183],v[244:247],v[212:215],a[180:183]\n"
"buffer_load_dwordx4 v109,s[16:19],s76 offen offset:3072 lds\n"
"v_mfma_f32_16x16x32_bf16 a[180:183],v[180:183],v[148:151],a[180:183]\n"
"s_mov_b32 s87,2097152\n"
"v_add_f32 v56,v56,v54\n"
"v_add_f32 v57,v57,v55\n"
"v_mfma_f32_16x16x32_bf16 a[184:187],v[184:187],v[148:151],a[184:187]\n"
"buffer_load_dword v115,v112,s[36:39],s87 offen\n"
"v_mfma_f32_16x16x32_bf16 a[184:187],v[248:251],v[212:215],a[184:187]\n"
"s_mov_b32 s87,2621440\n"
"v_add_f32 v56,v56,v57\n"
"v_mfma_f32_16x16x32_bf16 a[188:191],v[252:255],v[212:215],a[188:191]\n"
"buffer_load_dword v115,v112,s[36:39],s87 offen\n"
"v_add_f32_dpp v56,v56,v56 quad_perm:[1,0,3,2] row_mask:0xf bank_mask:0xf\n"
"v_mfma_f32_16x16x32_bf16 a[188:191],v[188:191],v[148:151],a[188:191]\n"
"s_barrier\n"
"v_mfma_f32_16x16x32_bf16 a[192:195],v[224:227],v[216:219],a[192:195]\n"
"ds_read_b128 v[152:155],v82 offset:12288\n"
"v_mfma_f32_16x16x32_bf16 a[224:227],v[224:227],v[220:223],a[224:227]\n"
"ds_read_b128 v[156:159],v82 offset:14336\n"
"v_add_f32_dpp v56,v56,v56 quad_perm:[2,3,0,1] row_mask:0xf bank_mask:0xf\n"
"v_mfma_f32_16x16x32_bf16 a[228:231],v[228:231],v[220:223],a[228:231]\n"
"ds_read_b64_tr_b16 v[160:161],v98 offset:49152\n"
"v_mfma_f32_16x16x32_bf16 a[196:199],v[228:231],v[216:219],a[196:199]\n"
"ds_read_b64_tr_b16 v[162:163],v98 offset:50176\n"
"v_add_f32_dpp v56,v56,v56 row_ror:8 row_mask:0xf bank_mask:0xf\n"
"v_mfma_f32_16x16x32_bf16 a[200:203],v[232:235],v[216:219],a[200:203]\n"
"ds_read_b64_tr_b16 v[164:165],v99 offset:49152\n"
"v_mov_b32 v57,v56\n"
"v_mfma_f32_16x16x32_bf16 a[232:235],v[232:235],v[220:223],a[232:235]\n"
"ds_read_b64_tr_b16 v[166:167],v99 offset:50176\n"
"v_mfma_f32_16x16x32_bf16 a[236:239],v[236:239],v[220:223],a[236:239]\n"
"ds_read_b64_tr_b16 v[168:169],v100 offset:49152\n"
"v_permlane16_swap_b32 v57,v56\n"
"v_mfma_f32_16x16x32_bf16 a[204:207],v[236:239],v[216:219],a[204:207]\n"
"ds_read_b64_tr_b16 v[170:171],v100 offset:50176\n"
"v_add_f32 v56,v56,v57\n"
"v_mfma_f32_16x16x32_bf16 a[208:211],v[240:243],v[216:219],a[208:211]\n"
"ds_read_b64_tr_b16 v[172:173],v101 offset:49152\n"
"v_mov_b32 v57,v56\n"
"v_mfma_f32_16x16x32_bf16 a[240:243],v[240:243],v[220:223],a[240:243]\n"
"ds_read_b64_tr_b16 v[174:175],v101 offset:50176\n"
"v_mfma_f32_16x16x32_bf16 a[244:247],v[244:247],v[220:223],a[244:247]\n"
"ds_read_b64_tr_b16 v[176:177],v102 offset:49152\n"
"v_permlane32_swap_b32 v57,v56\n"
"v_mfma_f32_16x16x32_bf16 a[212:215],v[244:247],v[216:219],a[212:215]\n"
"ds_read_b64_tr_b16 v[178:179],v102 offset:50176\n"
"v_add_f32 v56,v56,v57\n"
"v_mfma_f32_16x16x32_bf16 a[216:219],v[248:251],v[216:219],a[216:219]\n"
"ds_read_b64_tr_b16 v[180:181],v103 offset:49152\n"
"v_mul_f32 v58,0x3a800000,v56\n"
"v_mfma_f32_16x16x32_bf16 a[248:251],v[248:251],v[220:223],a[248:251]\n"
"ds_read_b64_tr_b16 v[182:183],v103 offset:50176\n"
"v_sub_f32 v62,v24,v58\n"
"v_mfma_f32_16x16x32_bf16 a[252:255],v[252:255],v[220:223],a[252:255]\n"
"ds_read_b64_tr_b16 v[184:185],v104 offset:49152\n"
"v_mul_f32 v59,v62,v62\n"
"v_mfma_f32_16x16x32_bf16 a[220:223],v[252:255],v[216:219],a[220:223]\n"
"ds_read_b64_tr_b16 v[186:187],v104 offset:50176\n"
"v_sub_f32 v63,v25,v58\n"
"s_waitcnt lgkmcnt(10)\n"
"v_mfma_f32_16x16x32_bf16 a[192:195],v[160:163],v[152:155],a[192:195]\n"
"ds_read_b64_tr_b16 v[188:189],v105 offset:49152\n"
"v_mul_f32 v60,v63,v63\n"
"v_mfma_f32_16x16x32_bf16 a[224:227],v[160:163],v[156:159],a[224:227]\n"
"ds_read_b64_tr_b16 v[190:191],v105 offset:50176\n"
"v_sub_f32 v64,v26,v58\n"
"v_mfma_f32_16x16x32_bf16 a[228:231],v[164:167],v[156:159],a[228:231]\n"
"ds_read_b128 v[128:131],v82 offset:0\n"
"v_fmac_f32 v59,v64,v64\n"
"v_mfma_f32_16x16x32_bf16 a[196:199],v[164:167],v[152:155],a[196:199]\n"
"ds_read_b128 v[192:195],v83 offset:0\n"
"s_waitcnt lgkmcnt(10)\n"
"v_mfma_f32_16x16x32_bf16 a[200:203],v[168:171],v[152:155],a[200:203]\n"
"ds_read_b64_tr_b16 v[224:225],v98 offset:32768\n"
"v_sub_f32 v65,v27,v58\n"
"v_mfma_f32_16x16x32_bf16 a[232:235],v[168:171],v[156:159],a[232:235]\n"
"ds_read_b64_tr_b16 v[226:227],v98 offset:33792\n"
"v_fmac_f32 v60,v65,v65\n"
"v_mfma_f32_16x16x32_bf16 a[236:239],v[172:175],v[156:159],a[236:239]\n"
"ds_read_b64_tr_b16 v[228:229],v99 offset:32768\n"
"v_sub_f32 v62,v28,v58\n"
"v_mfma_f32_16x16x32_bf16 a[204:207],v[172:175],v[152:155],a[204:207]\n"
"ds_read_b64_tr_b16 v[230:231],v99 offset:33792\n"
"s_waitcnt lgkmcnt(10)\n"
"v_mfma_f32_16x16x32_bf16 a[208:211],v[176:179],v[152:155],a[208:211]\n"
"ds_read_b64_tr_b16 v[232:233],v100 offset:32768\n"
"v_fmac_f32 v59,v62,v62\n"
"v_mfma_f32_16x16x32_bf16 a[240:243],v[176:179],v[156:159],a[240:243]\n"
"ds_read_b64_tr_b16 v[234:235],v100 offset:33792\n"
"v_sub_f32 v63,v29,v58\n"
"v_mfma_f32_16x16x32_bf16 a[244:247],v[180:183],v[156:159],a[244:247]\n"
"ds_read_b64_tr_b16 v[236:237],v101 offset:32768\n"
"v_fmac_f32 v60,v63,v63\n"
"v_mfma_f32_16x16x32_bf16 a[212:215],v[180:183],v[152:155],a[212:215]\n"
"ds_read_b64_tr_b16 v[238:239],v101 offset:33792\n"
"s_waitcnt lgkmcnt(10)\n"
"v_mfma_f32_16x16x32_bf16 a[216:219],v[184:187],v[152:155],a[216:219]\n"
"ds_read_b64_tr_b16 v[240:241],v102 offset:32768\n"
"v_sub_f32 v64,v30,v58\n"
"v_mfma_f32_16x16x32_bf16 a[248:251],v[184:187],v[156:159],a[248:251]\n"
"ds_read_b64_tr_b16 v[242:243],v102 offset:33792\n"
"v_fmac_f32 v59,v64,v64\n"
"v_mfma_f32_16x16x32_bf16 a[252:255],v[188:191],v[156:159],a[252:255]\n"
"ds_read_b64_tr_b16 v[244:245],v103 offset:32768\n"
"v_sub_f32 v65,v31,v58\n"
"v_mfma_f32_16x16x32_bf16 a[220:223],v[188:191],v[152:155],a[220:223]\n"
"ds_read_b64_tr_b16 v[246:247],v103 offset:33792\n"
"s_waitcnt lgkmcnt(8)\n"
"v_mfma_f32_16x16x32_bf16 a[0:3],v[160:163],v[128:131],a[0:3]\n"
"ds_read_b64_tr_b16 v[248:249],v104 offset:32768\n"
"v_fmac_f32 v60,v65,v65\n"
"v_mfma_f32_16x16x32_bf16 a[0:3],v[224:227],v[192:195],a[0:3]\n"
"ds_read_b64_tr_b16 v[250:251],v104 offset:33792\n"
"v_sub_f32 v62,v32,v58\n"
"v_mfma_f32_16x16x32_bf16 a[4:7],v[228:231],v[192:195],a[4:7]\n"
"ds_read_b64_tr_b16 v[252:253],v105 offset:32768\n"
"v_fmac_f32 v59,v62,v62\n"
"v_mfma_f32_16x16x32_bf16 a[4:7],v[164:167],v[128:131],a[4:7]\n"
"ds_read_b64_tr_b16 v[254:255],v105 offset:33792\n"
"s_waitcnt lgkmcnt(8)\n"
"v_mfma_f32_16x16x32_bf16 a[8:11],v[168:171],v[128:131],a[8:11]\n"
"ds_read_b128 v[132:135],v82 offset:2048\n"
"v_sub_f32 v63,v33,v58\n"
"v_mfma_f32_16x16x32_bf16 a[8:11],v[232:235],v[192:195],a[8:11]\n"
"ds_read_b128 v[196:199],v83 offset:2048\n"
"v_fmac_f32 v60,v63,v63\n"
"v_mfma_f32_16x16x32_bf16 a[12:15],v[236:239],v[192:195],a[12:15]\n"
"ds_read_b128 v[136:139],v82 offset:4096\n"
"v_sub_f32 v64,v34,v58\n"
"v_mfma_f32_16x16x32_bf16 a[12:15],v[172:175],v[128:131],a[12:15]\n"
"ds_read_b128 v[200:203],v83 offset:4096\n"
"s_waitcnt lgkmcnt(8)\n"
"v_mfma_f32_16x16x32_bf16 a[16:19],v[176:179],v[128:131],a[16:19]\n"
"ds_read_b128 v[140:143],v82 offset:6144\n"
"v_fmac_f32 v59,v64,v64\n"
"v_mfma_f32_16x16x32_bf16 a[16:19],v[240:243],v[192:195],a[16:19]\n"
"ds_read_b128 v[204:207],v83 offset:6144\n"
"v_sub_f32 v65,v35,v58\n"
"v_mfma_f32_16x16x32_bf16 a[20:23],v[244:247],v[192:195],a[20:23]\n"
"ds_read_b128 v[144:147],v82 offset:8192\n"
"v_fmac_f32 v60,v65,v65\n"
"v_mfma_f32_16x16x32_bf16 a[20:23],v[180:183],v[128:131],a[20:23]\n"
"ds_read_b128 v[208:211],v83 offset:8192\n"
"s_waitcnt lgkmcnt(8)\n"
"v_mfma_f32_16x16x32_bf16 a[24:27],v[184:187],v[128:131],a[24:27]\n"
"ds_read_b128 v[148:151],v82 offset:10240\n"
"v_sub_f32 v62,v36,v58\n"
"v_mfma_f32_16x16x32_bf16 a[24:27],v[248:251],v[192:195],a[24:27]\n"
"ds_read_b128 v[212:215],v83 offset:10240\n"
"v_fmac_f32 v59,v62,v62\n"
"v_mfma_f32_16x16x32_bf16 a[28:31],v[252:255],v[192:195],a[28:31]\n"
"ds_read_b128 v[216:219],v83 offset:12288\n"
"v_sub_f32 v63,v37,v58\n"
"v_mfma_f32_16x16x32_bf16 a[28:31],v[188:191],v[128:131],a[28:31]\n"
"ds_read_b128 v[220:223],v83 offset:14336\n"
"s_waitcnt lgkmcnt(10)\n"
"v_mfma_f32_16x16x32_bf16 a[32:35],v[160:163],v[132:135],a[32:35]\n"
"v_fmac_f32 v60,v63,v63\n"
"v_sub_f32 v64,v38,v58\n"
"v_mfma_f32_16x16x32_bf16 a[32:35],v[224:227],v[196:199],a[32:35]\n"
"v_fmac_f32 v59,v64,v64\n"
"v_sub_f32 v65,v39,v58\n"
"v_mfma_f32_16x16x32_bf16 a[36:39],v[228:231],v[196:199],a[36:39]\n"
"v_fmac_f32 v60,v65,v65\n"
"v_sub_f32 v62,v40,v58\n"
"v_mfma_f32_16x16x32_bf16 a[36:39],v[164:167],v[132:135],a[36:39]\n"
"v_fmac_f32 v59,v62,v62\n"
"v_sub_f32 v63,v41,v58\n"
"v_mfma_f32_16x16x32_bf16 a[40:43],v[168:171],v[132:135],a[40:43]\n"
"s_waitcnt lgkmcnt(0)\n"
"s_barrier\n"
"v_mfma_f32_16x16x32_bf16 a[40:43],v[232:235],v[196:199],a[40:43]\n"
"s_mov_b32 s77,2097152\n"
"v_fmac_f32 v60,v63,v63\n"
"s_add_u32 m0,s78,147456\n"
"v_mfma_f32_16x16x32_bf16 a[44:47],v[236:239],v[196:199],a[44:47]\n"
"buffer_load_dwordx4 v110,s[20:23],s77 offen offset:0 lds\n"
"v_mfma_f32_16x16x32_bf16 a[44:47],v[172:175],v[132:135],a[44:47]\n"
"v_sub_f32 v64,v42,v58\n"
"v_fmac_f32 v59,v64,v64\n"
"v_mfma_f32_16x16x32_bf16 a[48:51],v[176:179],v[132:135],a[48:51]\n"
"v_sub_f32 v65,v43,v58\n"
"v_fmac_f32 v60,v65,v65\n"
"v_mfma_f32_16x16x32_bf16 a[48:51],v[240:243],v[196:199],a[48:51]\n"
"v_sub_f32 v62,v44,v58\n"
"v_fmac_f32 v59,v62,v62\n"
"v_mfma_f32_16x16x32_bf16 a[52:55],v[244:247],v[196:199],a[52:55]\n"
"buffer_load_dwordx4 v111,s[20:23],s77 offen offset:1024 lds\n"
"v_mfma_f32_16x16x32_bf16 a[52:55],v[180:183],v[132:135],a[52:55]\n"
"v_sub_f32 v63,v45,v58\n"
"v_fmac_f32 v60,v63,v63\n"
"v_mfma_f32_16x16x32_bf16 a[56:59],v[184:187],v[132:135],a[56:59]\n"
"v_sub_f32 v64,v46,v58\n"
"v_fmac_f32 v59,v64,v64\n"
"v_mfma_f32_16x16x32_bf16 a[56:59],v[248:251],v[196:199],a[56:59]\n"
"v_sub_f32 v65,v47,v58\n"
"v_fmac_f32 v60,v65,v65\n"
"v_mfma_f32_16x16x32_bf16 a[60:63],v[252:255],v[196:199],a[60:63]\n"
"buffer_load_dwordx4 v113,s[20:23],s77 offen offset:2048 lds\n"
"v_mfma_f32_16x16x32_bf16 a[60:63],v[188:191],v[132:135],a[60:63]\n"
"v_sub_f32 v62,v48,v58\n"
"v_fmac_f32 v59,v62,v62\n"
"v_mfma_f32_16x16x32_bf16 a[64:67],v[160:163],v[136:139],a[64:67]\n"
"v_sub_f32 v63,v49,v58\n"
"v_fmac_f32 v60,v63,v63\n"
"v_mfma_f32_16x16x32_bf16 a[64:67],v[224:227],v[200:203],a[64:67]\n"
"v_sub_f32 v64,v50,v58\n"
"v_fmac_f32 v59,v64,v64\n"
"v_mfma_f32_16x16x32_bf16 a[68:71],v[228:231],v[200:203],a[68:71]\n"
"buffer_load_dwordx4 v114,s[20:23],s77 offen offset:3072 lds\n"
"v_mfma_f32_16x16x32_bf16 a[68:71],v[164:167],v[136:139],a[68:71]\n"
"v_sub_f32 v65,v51,v58\n"
"v_fmac_f32 v60,v65,v65\n"
"v_mfma_f32_16x16x32_bf16 a[72:75],v[168:171],v[136:139],a[72:75]\n"
"v_sub_f32 v62,v52,v58\n"
"v_fmac_f32 v59,v62,v62\n"
"v_mfma_f32_16x16x32_bf16 a[72:75],v[232:235],v[200:203],a[72:75]\n"
"s_mov_b32 s77,2359296\n"
"v_sub_f32 v63,v53,v58\n"
"s_add_u32 m0,s78,131072\n"
"v_mfma_f32_16x16x32_bf16 a[76:79],v[236:239],v[200:203],a[76:79]\n"
"buffer_load_dwordx4 v110,s[20:23],s77 offen offset:0 lds\n"
"v_mfma_f32_16x16x32_bf16 a[76:79],v[172:175],v[136:139],a[76:79]\n"
"v_fmac_f32 v60,v63,v63\n"
"v_sub_f32 v64,v54,v58\n"
"v_mfma_f32_16x16x32_bf16 a[80:83],v[176:179],v[136:139],a[80:83]\n"
"v_fmac_f32 v59,v64,v64\n"
"v_sub_f32 v65,v55,v58\n"
"v_mfma_f32_16x16x32_bf16 a[80:83],v[240:243],v[200:203],a[80:83]\n"
"v_fmac_f32 v60,v65,v65\n"
"v_add_f32 v59,v59,v60\n"
"v_mfma_f32_16x16x32_bf16 a[84:87],v[244:247],v[200:203],a[84:87]\n"
"buffer_load_dwordx4 v111,s[20:23],s77 offen offset:1024 lds\n"
"v_mfma_f32_16x16x32_bf16 a[84:87],v[180:183],v[136:139],a[84:87]\n"
"v_mfma_f32_16x16x32_bf16 a[88:91],v[184:187],v[136:139],a[88:91]\n"
"v_add_f32_dpp v59,v59,v59 quad_perm:[1,0,3,2] row_mask:0xf bank_mask:0xf\n"
"v_mfma_f32_16x16x32_bf16 a[88:91],v[248:251],v[200:203],a[88:91]\n"
"s_nop 0\n"
"v_add_f32_dpp v59,v59,v59 quad_perm:[2,3,0,1] row_mask:0xf bank_mask:0xf\n"
"v_mfma_f32_16x16x32_bf16 a[92:95],v[252:255],v[200:203],a[92:95]\n"
"buffer_load_dwordx4 v113,s[20:23],s77 offen offset:2048 lds\n"
"v_mfma_f32_16x16x32_bf16 a[92:95],v[188:191],v[136:139],a[92:95]\n"
"v_add_f32_dpp v59,v59,v59 row_ror:8 row_mask:0xf bank_mask:0xf\n"
"v_mov_b32 v57,v59\n"
"v_mfma_f32_16x16x32_bf16 a[96:99],v[160:163],v[140:143],a[96:99]\n"
"v_mfma_f32_16x16x32_bf16 a[96:99],v[224:227],v[204:207],a[96:99]\n"
"v_permlane16_swap_b32 v57,v59\n"
"v_add_f32 v59,v59,v57\n"
"v_mfma_f32_16x16x32_bf16 a[100:103],v[228:231],v[204:207],a[100:103]\n"
"buffer_load_dwordx4 v114,s[20:23],s77 offen offset:3072 lds\n"
"v_mfma_f32_16x16x32_bf16 a[100:103],v[164:167],v[140:143],a[100:103]\n"
"v_mov_b32 v57,v59\n"
"v_mfma_f32_16x16x32_bf16 a[104:107],v[168:171],v[140:143],a[104:107]\n"
"s_nop 0\n"
"v_permlane32_swap_b32 v57,v59\n"
"v_add_f32 v59,v59,v57\n"
"v_mfma_f32_16x16x32_bf16 a[104:107],v[232:235],v[204:207],a[104:107]\n"
"v_mul_f32 v59,0x3a800000,v59\n"
"v_cvt_pk_bf16_f32 v59,v59,v59\n"
"v_mfma_f32_16x16x32_bf16 a[108:111],v[236:239],v[204:207],a[108:111]\n"
"s_mov_b32 s76,640\n"
"v_lshlrev_b32 v59,16,v59\n"
"s_add_u32 m0,s79,65536\n"
"v_mfma_f32_16x16x32_bf16 a[108:111],v[172:175],v[140:143],a[108:111]\n"
"buffer_load_dwordx4 v106,s[16:19],s76 offen offset:0 lds\n"
"v_mfma_f32_16x16x32_bf16 a[112:115],v[176:179],v[140:143],a[112:115]\n"
"v_add_f32 v59,0x3727c5ac,v59\n"
"v_cvt_pk_bf16_f32 v59,v59,v59\n"
"v_mfma_f32_16x16x32_bf16 a[112:115],v[240:243],v[204:207],a[112:115]\n"
"v_lshlrev_b32 v59,16,v59\n"
"v_sqrt_f32 v59,v59\n"
"v_mfma_f32_16x16x32_bf16 a[116:119],v[244:247],v[204:207],a[116:119]\n"
"v_cvt_pk_bf16_f32 v58,v58,v58\n"
"v_lshlrev_b32 v58,16,v58\n"
"v_mfma_f32_16x16x32_bf16 a[116:119],v[180:183],v[140:143],a[116:119]\n"
"v_cvt_pk_bf16_f32 v59,v59,v59\n"
"v_lshlrev_b32 v59,16,v59\n"
"v_mfma_f32_16x16x32_bf16 a[120:123],v[184:187],v[140:143],a[120:123]\n"
"buffer_load_dwordx4 v107,s[16:19],s76 offen offset:1024 lds\n"
"v_mfma_f32_16x16x32_bf16 a[120:123],v[248:251],v[204:207],a[120:123]\n"
"v_rcp_f32 v66,v59\n"
"v_mfma_f32_16x16x32_bf16 a[124:127],v[252:255],v[204:207],a[124:127]\n"
"v_mul_f32 v66,0x3e0293ee,v66\n"
"v_mul_f32 v58,v58,v66\n"
"v_mfma_f32_16x16x32_bf16 a[124:127],v[188:191],v[140:143],a[124:127]\n"
"v_fma_f32 v24,v24,v66,-v58\n"
"v_fma_f32 v25,v25,v66,-v58\n"
"v_mfma_f32_16x16x32_bf16 a[128:131],v[160:163],v[144:147],a[128:131]\n"
"v_cvt_pk_bf16_f32 v8,v24,v25\n"
"v_fma_f32 v26,v26,v66,-v58\n"
"v_mfma_f32_16x16x32_bf16 a[128:131],v[224:227],v[208:211],a[128:131]\n"
"buffer_load_dwordx4 v108,s[16:19],s76 offen offset:2048 lds\n"
"v_mfma_f32_16x16x32_bf16 a[132:135],v[228:231],v[208:211],a[132:135]\n"
"v_fma_f32 v27,v27,v66,-v58\n"
"v_cvt_pk_bf16_f32 v9,v26,v27\n"
"v_mfma_f32_16x16x32_bf16 a[132:135],v[164:167],v[144:147],a[132:135]\n"
"v_fma_f32 v28,v28,v66,-v58\n"
"v_fma_f32 v29,v29,v66,-v58\n"
"v_mfma_f32_16x16x32_bf16 a[136:139],v[168:171],v[144:147],a[136:139]\n"
"v_cvt_pk_bf16_f32 v10,v28,v29\n"
"v_fma_f32 v30,v30,v66,-v58\n"
"v_mfma_f32_16x16x32_bf16 a[136:139],v[232:235],v[208:211],a[136:139]\n"
"v_fma_f32 v31,v31,v66,-v58\n"
"v_cvt_pk_bf16_f32 v11,v30,v31\n"
"v_mfma_f32_16x16x32_bf16 a[140:143],v[236:239],v[208:211],a[140:143]\n"
"buffer_load_dwordx4 v109,s[16:19],s76 offen offset:3072 lds\n"
"v_mfma_f32_16x16x32_bf16 a[140:143],v[172:175],v[144:147],a[140:143]\n"
"buffer_store_dwordx4 v[8:11],v71,s[24:27],s54 offen sc1\n"
"v_mfma_f32_16x16x32_bf16 a[144:147],v[176:179],v[144:147],a[144:147]\n"
"s_add_u32 s54,s54,131072\n"
"v_fma_f32 v32,v32,v66,-v58\n"
"v_fma_f32 v33,v33,v66,-v58\n"
"v_mfma_f32_16x16x32_bf16 a[144:147],v[240:243],v[208:211],a[144:147]\n"
"v_cvt_pk_bf16_f32 v12,v32,v33\n"
"v_fma_f32 v34,v34,v66,-v58\n"
"v_mfma_f32_16x16x32_bf16 a[148:151],v[244:247],v[208:211],a[148:151]\n"
"s_mov_b32 s76,262784\n"
"v_fma_f32 v35,v35,v66,-v58\n"
"s_add_u32 m0,s79,69632\n"
"v_mfma_f32_16x16x32_bf16 a[148:151],v[180:183],v[144:147],a[148:151]\n"
"buffer_load_dwordx4 v106,s[16:19],s76 offen offset:0 lds\n"
"v_mfma_f32_16x16x32_bf16 a[152:155],v[184:187],v[144:147],a[152:155]\n"
"v_cvt_pk_bf16_f32 v13,v34,v35\n"
"v_fma_f32 v36,v36,v66,-v58\n"
"v_mfma_f32_16x16x32_bf16 a[152:155],v[248:251],v[208:211],a[152:155]\n"
"v_fma_f32 v37,v37,v66,-v58\n"
"v_cvt_pk_bf16_f32 v14,v36,v37\n"
"v_mfma_f32_16x16x32_bf16 a[156:159],v[252:255],v[208:211],a[156:159]\n"
"v_fma_f32 v38,v38,v66,-v58\n"
"v_fma_f32 v39,v39,v66,-v58\n"
"v_cvt_pk_bf16_f32 v15,v38,v39\n"
"v_mfma_f32_16x16x32_bf16 a[156:159],v[188:191],v[144:147],a[156:159]\n"
"buffer_store_dwordx4 v[12:15],v71,s[24:27],s54 offen sc1\n"
"v_mfma_f32_16x16x32_bf16 a[160:163],v[160:163],v[148:151],a[160:163]\n"
"buffer_load_dwordx4 v107,s[16:19],s76 offen offset:1024 lds\n"
"v_mfma_f32_16x16x32_bf16 a[160:163],v[224:227],v[212:215],a[160:163]\n"
"s_add_u32 s54,s54,131072\n"
"v_fma_f32 v40,v40,v66,-v58\n"
"v_mfma_f32_16x16x32_bf16 a[164:167],v[228:231],v[212:215],a[164:167]\n"
"v_fma_f32 v41,v41,v66,-v58\n"
"v_cvt_pk_bf16_f32 v16,v40,v41\n"
"v_mfma_f32_16x16x32_bf16 a[164:167],v[164:167],v[148:151],a[164:167]\n"
"v_fma_f32 v42,v42,v66,-v58\n"
"v_fma_f32 v43,v43,v66,-v58\n"
"v_mfma_f32_16x16x32_bf16 a[168:171],v[168:171],v[148:151],a[168:171]\n"
"v_cvt_pk_bf16_f32 v17,v42,v43\n"
"v_fma_f32 v44,v44,v66,-v58\n"
"v_mfma_f32_16x16x32_bf16 a[168:171],v[232:235],v[212:215],a[168:171]\n"
"buffer_load_dwordx4 v108,s[16:19],s76 offen offset:2048 lds\n"
"v_mfma_f32_16x16x32_bf16 a[172:175],v[236:239],v[212:215],a[172:175]\n"
"v_fma_f32 v45,v45,v66,-v58\n"
"v_cvt_pk_bf16_f32 v18,v44,v45\n"
"v_mfma_f32_16x16x32_bf16 a[172:175],v[172:175],v[148:151],a[172:175]\n"
"v_fma_f32 v46,v46,v66,-v58\n"
"v_fma_f32 v47,v47,v66,-v58\n"
"v_cvt_pk_bf16_f32 v19,v46,v47\n"
"v_mfma_f32_16x16x32_bf16 a[176:179],v[176:179],v[148:151],a[176:179]\n"
"buffer_store_dwordx4 v[16:19],v71,s[24:27],s54 offen sc1\n"
"v_mfma_f32_16x16x32_bf16 a[176:179],v[240:243],v[212:215],a[176:179]\n"
"s_add_u32 s54,s54,131072\n"
"v_fma_f32 v48,v48,v66,-v58\n"
"v_mfma_f32_16x16x32_bf16 a[180:183],v[244:247],v[212:215],a[180:183]\n"
"buffer_load_dwordx4 v109,s[16:19],s76 offen offset:3072 lds\n"
"v_mfma_f32_16x16x32_bf16 a[180:183],v[180:183],v[148:151],a[180:183]\n"
"s_mov_b32 s87,0\n"
"v_fma_f32 v49,v49,v66,-v58\n"
"v_cvt_pk_bf16_f32 v20,v48,v49\n"
"v_mfma_f32_16x16x32_bf16 a[184:187],v[184:187],v[148:151],a[184:187]\n"
"buffer_load_dword v115,v112,s[40:43],s87 offen\n"
"v_mfma_f32_16x16x32_bf16 a[184:187],v[248:251],v[212:215],a[184:187]\n"
"s_mov_b32 s87,1048576\n"
"v_fma_f32 v50,v50,v66,-v58\n"
"v_fma_f32 v51,v51,v66,-v58\n"
"v_mfma_f32_16x16x32_bf16 a[188:191],v[252:255],v[212:215],a[188:191]\n"
"buffer_load_dword v115,v112,s[40:43],s87 offen\n"
"v_mfma_f32_16x16x32_bf16 a[188:191],v[188:191],v[148:151],a[188:191]\n"
"s_mov_b32 s87,2097152\n"
"s_waitcnt vmcnt(31)\n"
"s_barrier\n"
"v_mfma_f32_16x16x32_bf16 a[192:195],v[224:227],v[216:219],a[192:195]\n"
"ds_read_b128 v[152:155],v80 offset:12288\n"
"buffer_load_dword v115,v112,s[40:43],s87 offen\n"
"v_mfma_f32_16x16x32_bf16 a[224:227],v[224:227],v[220:223],a[224:227]\n"
"ds_read_b128 v[156:159],v80 offset:14336\n"
"v_cvt_pk_bf16_f32 v21,v50,v51\n"
"v_mfma_f32_16x16x32_bf16 a[228:231],v[228:231],v[220:223],a[228:231]\n"
"ds_read_b64_tr_b16 v[160:161],v98 offset:0\n"
"v_fma_f32 v52,v52,v66,-v58\n"
"v_mfma_f32_16x16x32_bf16 a[196:199],v[228:231],v[216:219],a[196:199]\n"
"ds_read_b64_tr_b16 v[162:163],v98 offset:1024\n"
"v_fma_f32 v53,v53,v66,-v58\n"
"v_mfma_f32_16x16x32_bf16 a[200:203],v[232:235],v[216:219],a[200:203]\n"
"ds_read_b64_tr_b16 v[164:165],v99 offset:0\n"
"v_cvt_pk_bf16_f32 v22,v52,v53\n"
"v_mfma_f32_16x16x32_bf16 a[232:235],v[232:235],v[220:223],a[232:235]\n"
"ds_read_b64_tr_b16 v[166:167],v99 offset:1024\n"
"v_fma_f32 v54,v54,v66,-v58\n"
"v_mfma_f32_16x16x32_bf16 a[236:239],v[236:239],v[220:223],a[236:239]\n"
"ds_read_b64_tr_b16 v[168:169],v100 offset:0\n"
"v_fma_f32 v55,v55,v66,-v58\n"
"v_mfma_f32_16x16x32_bf16 a[204:207],v[236:239],v[216:219],a[204:207]\n"
"ds_read_b64_tr_b16 v[170:171],v100 offset:1024\n"
"v_cvt_pk_bf16_f32 v23,v54,v55\n"
"v_mfma_f32_16x16x32_bf16 a[208:211],v[240:243],v[216:219],a[208:211]\n"
"ds_read_b64_tr_b16 v[172:173],v101 offset:0\n"
"buffer_store_dwordx4 v[20:23],v71,s[24:27],s54 offen sc1\n"
"v_mfma_f32_16x16x32_bf16 a[240:243],v[240:243],v[220:223],a[240:243]\n"
"ds_read_b64_tr_b16 v[174:175],v101 offset:1024\n"
"v_mfma_f32_16x16x32_bf16 a[244:247],v[244:247],v[220:223],a[244:247]\n"
"ds_read_b64_tr_b16 v[176:177],v102 offset:0\n"
"v_mfma_f32_16x16x32_bf16 a[212:215],v[244:247],v[216:219],a[212:215]\n"
"ds_read_b64_tr_b16 v[178:179],v102 offset:1024\n"
"v_mfma_f32_16x16x32_bf16 a[216:219],v[248:251],v[216:219],a[216:219]\n"
"ds_read_b64_tr_b16 v[180:181],v103 offset:0\n"
"v_mfma_f32_16x16x32_bf16 a[248:251],v[248:251],v[220:223],a[248:251]\n"
"ds_read_b64_tr_b16 v[182:183],v103 offset:1024\n"
"v_mfma_f32_16x16x32_bf16 a[252:255],v[252:255],v[220:223],a[252:255]\n"
"ds_read_b64_tr_b16 v[184:185],v104 offset:0\n"
"v_mfma_f32_16x16x32_bf16 a[220:223],v[252:255],v[216:219],a[220:223]\n"
"ds_read_b64_tr_b16 v[186:187],v104 offset:1024\n"
"s_waitcnt lgkmcnt(10)\n"
"v_mfma_f32_16x16x32_bf16 a[192:195],v[160:163],v[152:155],a[192:195]\n"
"ds_read_b64_tr_b16 v[188:189],v105 offset:0\n"
"v_mfma_f32_16x16x32_bf16 a[224:227],v[160:163],v[156:159],a[224:227]\n"
"ds_read_b64_tr_b16 v[190:191],v105 offset:1024\n"
"v_mfma_f32_16x16x32_bf16 a[228:231],v[164:167],v[156:159],a[228:231]\n"
"ds_read_b128 v[128:131],v80 offset:0\n"
"v_mfma_f32_16x16x32_bf16 a[196:199],v[164:167],v[152:155],a[196:199]\n"
"ds_read_b128 v[192:195],v81 offset:0\n"
"s_waitcnt lgkmcnt(10)\n"
"v_mfma_f32_16x16x32_bf16 a[200:203],v[168:171],v[152:155],a[200:203]\n"
"ds_read_b64_tr_b16 v[224:225],v98 offset:16384\n"
"v_mfma_f32_16x16x32_bf16 a[232:235],v[168:171],v[156:159],a[232:235]\n"
"ds_read_b64_tr_b16 v[226:227],v98 offset:17408\n"
"v_mfma_f32_16x16x32_bf16 a[236:239],v[172:175],v[156:159],a[236:239]\n"
"ds_read_b64_tr_b16 v[228:229],v99 offset:16384\n"
"v_mfma_f32_16x16x32_bf16 a[204:207],v[172:175],v[152:155],a[204:207]\n"
"ds_read_b64_tr_b16 v[230:231],v99 offset:17408\n"
"s_waitcnt lgkmcnt(10)\n"
"v_mfma_f32_16x16x32_bf16 a[208:211],v[176:179],v[152:155],a[208:211]\n"
"ds_read_b64_tr_b16 v[232:233],v100 offset:16384\n"
"v_mfma_f32_16x16x32_bf16 a[240:243],v[176:179],v[156:159],a[240:243]\n"
"ds_read_b64_tr_b16 v[234:235],v100 offset:17408\n"
"v_mfma_f32_16x16x32_bf16 a[244:247],v[180:183],v[156:159],a[244:247]\n"
"ds_read_b64_tr_b16 v[236:237],v101 offset:16384\n"
"v_mfma_f32_16x16x32_bf16 a[212:215],v[180:183],v[152:155],a[212:215]\n"
"ds_read_b64_tr_b16 v[238:239],v101 offset:17408\n"
"s_waitcnt lgkmcnt(10)\n"
"v_mfma_f32_16x16x32_bf16 a[216:219],v[184:187],v[152:155],a[216:219]\n"
"ds_read_b64_tr_b16 v[240:241],v102 offset:16384\n"
"v_mfma_f32_16x16x32_bf16 a[248:251],v[184:187],v[156:159],a[248:251]\n"
"ds_read_b64_tr_b16 v[242:243],v102 offset:17408\n"
"v_mfma_f32_16x16x32_bf16 a[252:255],v[188:191],v[156:159],a[252:255]\n"
"ds_read_b64_tr_b16 v[244:245],v103 offset:16384\n"
"v_mfma_f32_16x16x32_bf16 a[220:223],v[188:191],v[152:155],a[220:223]\n"
"ds_read_b64_tr_b16 v[246:247],v103 offset:17408\n"
"s_waitcnt lgkmcnt(8)\n"
"v_mfma_f32_16x16x32_bf16 a[0:3],v[160:163],v[128:131],a[0:3]\n"
"ds_read_b64_tr_b16 v[248:249],v104 offset:16384\n"
"v_mfma_f32_16x16x32_bf16 a[0:3],v[224:227],v[192:195],a[0:3]\n"
"ds_read_b64_tr_b16 v[250:251],v104 offset:17408\n"
"v_mfma_f32_16x16x32_bf16 a[4:7],v[228:231],v[192:195],a[4:7]\n"
"ds_read_b64_tr_b16 v[252:253],v105 offset:16384\n"
"v_mfma_f32_16x16x32_bf16 a[4:7],v[164:167],v[128:131],a[4:7]\n"
"ds_read_b64_tr_b16 v[254:255],v105 offset:17408\n"
"s_waitcnt lgkmcnt(8)\n"
"v_mfma_f32_16x16x32_bf16 a[8:11],v[168:171],v[128:131],a[8:11]\n"
"ds_read_b128 v[132:135],v80 offset:2048\n"
"v_mfma_f32_16x16x32_bf16 a[8:11],v[232:235],v[192:195],a[8:11]\n"
"ds_read_b128 v[196:199],v81 offset:2048\n"
"v_mfma_f32_16x16x32_bf16 a[12:15],v[236:239],v[192:195],a[12:15]\n"
"ds_read_b128 v[136:139],v80 offset:4096\n"
"v_mfma_f32_16x16x32_bf16 a[12:15],v[172:175],v[128:131],a[12:15]\n"
"ds_read_b128 v[200:203],v81 offset:4096\n"
"s_waitcnt lgkmcnt(8)\n"
"v_mfma_f32_16x16x32_bf16 a[16:19],v[176:179],v[128:131],a[16:19]\n"
"ds_read_b128 v[140:143],v80 offset:6144\n"
"v_mfma_f32_16x16x32_bf16 a[16:19],v[240:243],v[192:195],a[16:19]\n"
"ds_read_b128 v[204:207],v81 offset:6144\n"
"v_mfma_f32_16x16x32_bf16 a[20:23],v[244:247],v[192:195],a[20:23]\n"
"ds_read_b128 v[144:147],v80 offset:8192\n"
"v_mfma_f32_16x16x32_bf16 a[20:23],v[180:183],v[128:131],a[20:23]\n"
"ds_read_b128 v[208:211],v81 offset:8192\n"
"s_waitcnt lgkmcnt(8)\n"
"v_mfma_f32_16x16x32_bf16 a[24:27],v[184:187],v[128:131],a[24:27]\n"
"ds_read_b128 v[148:151],v80 offset:10240\n"
"v_mfma_f32_16x16x32_bf16 a[24:27],v[248:251],v[192:195],a[24:27]\n"
"ds_read_b128 v[212:215],v81 offset:10240\n"
"v_mfma_f32_16x16x32_bf16 a[28:31],v[252:255],v[192:195],a[28:31]\n"
"ds_read_b128 v[216:219],v81 offset:12288\n"
"v_mfma_f32_16x16x32_bf16 a[28:31],v[188:191],v[128:131],a[28:31]\n"
"ds_read_b128 v[220:223],v81 offset:14336\n"
"s_waitcnt lgkmcnt(10)\n"
"v_mfma_f32_16x16x32_bf16 a[32:35],v[160:163],v[132:135],a[32:35]\n"
"v_mfma_f32_16x16x32_bf16 a[32:35],v[224:227],v[196:199],a[32:35]\n"
"v_mfma_f32_16x16x32_bf16 a[36:39],v[228:231],v[196:199],a[36:39]\n"
"v_mfma_f32_16x16x32_bf16 a[36:39],v[164:167],v[132:135],a[36:39]\n"
"v_mfma_f32_16x16x32_bf16 a[40:43],v[168:171],v[132:135],a[40:43]\n"
"s_waitcnt lgkmcnt(0)\n"
"s_barrier\n"
"v_mfma_f32_16x16x32_bf16 a[40:43],v[232:235],v[196:199],a[40:43]\n"
"s_mov_b32 s77,2621440\n"
"s_add_u32 m0,s78,98304\n"
"v_mfma_f32_16x16x32_bf16 a[44:47],v[236:239],v[196:199],a[44:47]\n"
"buffer_load_dwordx4 v110,s[20:23],s77 offen offset:0 lds\n"
"v_mfma_f32_16x16x32_bf16 a[44:47],v[172:175],v[132:135],a[44:47]\n"
"v_mfma_f32_16x16x32_bf16 a[48:51],v[176:179],v[132:135],a[48:51]\n"
"v_mfma_f32_16x16x32_bf16 a[48:51],v[240:243],v[196:199],a[48:51]\n"
"v_mfma_f32_16x16x32_bf16 a[52:55],v[244:247],v[196:199],a[52:55]\n"
"buffer_load_dwordx4 v111,s[20:23],s77 offen offset:1024 lds\n"
"v_mfma_f32_16x16x32_bf16 a[52:55],v[180:183],v[132:135],a[52:55]\n"
"v_mfma_f32_16x16x32_bf16 a[56:59],v[184:187],v[132:135],a[56:59]\n"
"v_mfma_f32_16x16x32_bf16 a[56:59],v[248:251],v[196:199],a[56:59]\n"
"v_mfma_f32_16x16x32_bf16 a[60:63],v[252:255],v[196:199],a[60:63]\n"
"buffer_load_dwordx4 v113,s[20:23],s77 offen offset:2048 lds\n"
"v_mfma_f32_16x16x32_bf16 a[60:63],v[188:191],v[132:135],a[60:63]\n"
"v_mfma_f32_16x16x32_bf16 a[64:67],v[160:163],v[136:139],a[64:67]\n"
"v_mfma_f32_16x16x32_bf16 a[64:67],v[224:227],v[200:203],a[64:67]\n"
"v_mfma_f32_16x16x32_bf16 a[68:71],v[228:231],v[200:203],a[68:71]\n"
"buffer_load_dwordx4 v114,s[20:23],s77 offen offset:3072 lds\n"
"v_mfma_f32_16x16x32_bf16 a[68:71],v[164:167],v[136:139],a[68:71]\n"
"v_mfma_f32_16x16x32_bf16 a[72:75],v[168:171],v[136:139],a[72:75]\n"
"v_mfma_f32_16x16x32_bf16 a[72:75],v[232:235],v[200:203],a[72:75]\n"
"s_mov_b32 s77,2883584\n"
"s_add_u32 m0,s78,114688\n"
"v_mfma_f32_16x16x32_bf16 a[76:79],v[236:239],v[200:203],a[76:79]\n"
"buffer_load_dwordx4 v110,s[20:23],s77 offen offset:0 lds\n"
"v_mfma_f32_16x16x32_bf16 a[76:79],v[172:175],v[136:139],a[76:79]\n"
"v_mfma_f32_16x16x32_bf16 a[80:83],v[176:179],v[136:139],a[80:83]\n"
"v_mfma_f32_16x16x32_bf16 a[80:83],v[240:243],v[200:203],a[80:83]\n"
"v_mfma_f32_16x16x32_bf16 a[84:87],v[244:247],v[200:203],a[84:87]\n"
"buffer_load_dwordx4 v111,s[20:23],s77 offen offset:1024 lds\n"
"v_mfma_f32_16x16x32_bf16 a[84:87],v[180:183],v[136:139],a[84:87]\n"
"v_mfma_f32_16x16x32_bf16 a[88:91],v[184:187],v[136:139],a[88:91]\n"
"v_mfma_f32_16x16x32_bf16 a[88:91],v[248:251],v[200:203],a[88:91]\n"
"v_mfma_f32_16x16x32_bf16 a[92:95],v[252:255],v[200:203],a[92:95]\n"
"buffer_load_dwordx4 v113,s[20:23],s77 offen offset:2048 lds\n"
"v_mfma_f32_16x16x32_bf16 a[92:95],v[188:191],v[136:139],a[92:95]\n"
"v_mfma_f32_16x16x32_bf16 a[96:99],v[160:163],v[140:143],a[96:99]\n"
"v_mfma_f32_16x16x32_bf16 a[96:99],v[224:227],v[204:207],a[96:99]\n"
"v_mfma_f32_16x16x32_bf16 a[100:103],v[228:231],v[204:207],a[100:103]\n"
"buffer_load_dwordx4 v114,s[20:23],s77 offen offset:3072 lds\n"
"v_mfma_f32_16x16x32_bf16 a[100:103],v[164:167],v[140:143],a[100:103]\n"
"v_mfma_f32_16x16x32_bf16 a[104:107],v[168:171],v[140:143],a[104:107]\n"
"v_mfma_f32_16x16x32_bf16 a[104:107],v[232:235],v[204:207],a[104:107]\n"
"v_mfma_f32_16x16x32_bf16 a[108:111],v[236:239],v[204:207],a[108:111]\n"
"s_mov_b32 s76,768\n"
"s_add_u32 m0,s79,0\n"
"v_mfma_f32_16x16x32_bf16 a[108:111],v[172:175],v[140:143],a[108:111]\n"
"buffer_load_dwordx4 v106,s[16:19],s76 offen offset:0 lds\n"
"v_mfma_f32_16x16x32_bf16 a[112:115],v[176:179],v[140:143],a[112:115]\n"
"v_mfma_f32_16x16x32_bf16 a[112:115],v[240:243],v[204:207],a[112:115]\n"
"v_mfma_f32_16x16x32_bf16 a[116:119],v[244:247],v[204:207],a[116:119]\n"
"v_mfma_f32_16x16x32_bf16 a[116:119],v[180:183],v[140:143],a[116:119]\n"
"v_mfma_f32_16x16x32_bf16 a[120:123],v[184:187],v[140:143],a[120:123]\n"
"buffer_load_dwordx4 v107,s[16:19],s76 offen offset:1024 lds\n"
"v_mfma_f32_16x16x32_bf16 a[120:123],v[248:251],v[204:207],a[120:123]\n"
"v_mfma_f32_16x16x32_bf16 a[124:127],v[252:255],v[204:207],a[124:127]\n"
"v_mfma_f32_16x16x32_bf16 a[124:127],v[188:191],v[140:143],a[124:127]\n"
"v_mfma_f32_16x16x32_bf16 a[128:131],v[160:163],v[144:147],a[128:131]\n"
"v_mfma_f32_16x16x32_bf16 a[128:131],v[224:227],v[208:211],a[128:131]\n"
"buffer_load_dwordx4 v108,s[16:19],s76 offen offset:2048 lds\n"
"v_mfma_f32_16x16x32_bf16 a[132:135],v[228:231],v[208:211],a[132:135]\n"
"v_mfma_f32_16x16x32_bf16 a[132:135],v[164:167],v[144:147],a[132:135]\n"
"v_mfma_f32_16x16x32_bf16 a[136:139],v[168:171],v[144:147],a[136:139]\n"
"v_mfma_f32_16x16x32_bf16 a[136:139],v[232:235],v[208:211],a[136:139]\n"
"v_mfma_f32_16x16x32_bf16 a[140:143],v[236:239],v[208:211],a[140:143]\n"
"buffer_load_dwordx4 v109,s[16:19],s76 offen offset:3072 lds\n"
"v_mfma_f32_16x16x32_bf16 a[140:143],v[172:175],v[144:147],a[140:143]\n"
"v_mfma_f32_16x16x32_bf16 a[144:147],v[176:179],v[144:147],a[144:147]\n"
"v_mfma_f32_16x16x32_bf16 a[144:147],v[240:243],v[208:211],a[144:147]\n"
"v_mfma_f32_16x16x32_bf16 a[148:151],v[244:247],v[208:211],a[148:151]\n"
"s_mov_b32 s76,262912\n"
"s_add_u32 m0,s79,4096\n"
"v_mfma_f32_16x16x32_bf16 a[148:151],v[180:183],v[144:147],a[148:151]\n"
"buffer_load_dwordx4 v106,s[16:19],s76 offen offset:0 lds\n"
"v_mfma_f32_16x16x32_bf16 a[152:155],v[184:187],v[144:147],a[152:155]\n"
"v_mfma_f32_16x16x32_bf16 a[152:155],v[248:251],v[208:211],a[152:155]\n"
"v_mfma_f32_16x16x32_bf16 a[156:159],v[252:255],v[208:211],a[156:159]\n"
"v_mfma_f32_16x16x32_bf16 a[156:159],v[188:191],v[144:147],a[156:159]\n"
"v_mfma_f32_16x16x32_bf16 a[160:163],v[160:163],v[148:151],a[160:163]\n"
"buffer_load_dwordx4 v107,s[16:19],s76 offen offset:1024 lds\n"
"v_mfma_f32_16x16x32_bf16 a[160:163],v[224:227],v[212:215],a[160:163]\n"
"v_mfma_f32_16x16x32_bf16 a[164:167],v[228:231],v[212:215],a[164:167]\n"
"v_mfma_f32_16x16x32_bf16 a[164:167],v[164:167],v[148:151],a[164:167]\n"
"v_mfma_f32_16x16x32_bf16 a[168:171],v[168:171],v[148:151],a[168:171]\n"
"v_mfma_f32_16x16x32_bf16 a[168:171],v[232:235],v[212:215],a[168:171]\n"
"buffer_load_dwordx4 v108,s[16:19],s76 offen offset:2048 lds\n"
"v_mfma_f32_16x16x32_bf16 a[172:175],v[236:239],v[212:215],a[172:175]\n"
"v_mfma_f32_16x16x32_bf16 a[172:175],v[172:175],v[148:151],a[172:175]\n"
"v_mfma_f32_16x16x32_bf16 a[176:179],v[176:179],v[148:151],a[176:179]\n"
"v_mfma_f32_16x16x32_bf16 a[176:179],v[240:243],v[212:215],a[176:179]\n"
"v_mfma_f32_16x16x32_bf16 a[180:183],v[244:247],v[212:215],a[180:183]\n"
"buffer_load_dwordx4 v109,s[16:19],s76 offen offset:3072 lds\n"
"v_mfma_f32_16x16x32_bf16 a[180:183],v[180:183],v[148:151],a[180:183]\n"
"s_mov_b32 s87,3145728\n"
"v_mfma_f32_16x16x32_bf16 a[184:187],v[184:187],v[148:151],a[184:187]\n"
"buffer_load_dword v115,v112,s[40:43],s87 offen\n"
"v_mfma_f32_16x16x32_bf16 a[184:187],v[248:251],v[212:215],a[184:187]\n"
"s_mov_b32 s87,4194304\n"
"v_mfma_f32_16x16x32_bf16 a[188:191],v[252:255],v[212:215],a[188:191]\n"
"buffer_load_dword v115,v112,s[40:43],s87 offen\n"
"v_mfma_f32_16x16x32_bf16 a[188:191],v[188:191],v[148:151],a[188:191]\n"
"s_mov_b32 s87,5242880\n"
"s_waitcnt vmcnt(33)\n"
"s_barrier\n"
"v_mfma_f32_16x16x32_bf16 a[192:195],v[224:227],v[216:219],a[192:195]\n"
"ds_read_b128 v[152:155],v80 offset:45056\n"
"buffer_load_dword v115,v112,s[40:43],s87 offen\n"
"v_mfma_f32_16x16x32_bf16 a[224:227],v[224:227],v[220:223],a[224:227]\n"
"ds_read_b128 v[156:159],v80 offset:47104\n"
"v_mfma_f32_16x16x32_bf16 a[228:231],v[228:231],v[220:223],a[228:231]\n"
"ds_read_b64_tr_b16 v[160:161],v98 offset:49152\n"
"v_mfma_f32_16x16x32_bf16 a[196:199],v[228:231],v[216:219],a[196:199]\n"
"ds_read_b64_tr_b16 v[162:163],v98 offset:50176\n"
"v_mfma_f32_16x16x32_bf16 a[200:203],v[232:235],v[216:219],a[200:203]\n"
"ds_read_b64_tr_b16 v[164:165],v99 offset:49152\n"
"v_mfma_f32_16x16x32_bf16 a[232:235],v[232:235],v[220:223],a[232:235]\n"
"ds_read_b64_tr_b16 v[166:167],v99 offset:50176\n"
"v_mfma_f32_16x16x32_bf16 a[236:239],v[236:239],v[220:223],a[236:239]\n"
"ds_read_b64_tr_b16 v[168:169],v100 offset:49152\n"
"v_mfma_f32_16x16x32_bf16 a[204:207],v[236:239],v[216:219],a[204:207]\n"
"ds_read_b64_tr_b16 v[170:171],v100 offset:50176\n"
"v_mfma_f32_16x16x32_bf16 a[208:211],v[240:243],v[216:219],a[208:211]\n"
"ds_read_b64_tr_b16 v[172:173],v101 offset:49152\n"
"v_mfma_f32_16x16x32_bf16 a[240:243],v[240:243],v[220:223],a[240:243]\n"
"ds_read_b64_tr_b16 v[174:175],v101 offset:50176\n"
"v_mfma_f32_16x16x32_bf16 a[244:247],v[244:247],v[220:223],a[244:247]\n"
"ds_read_b64_tr_b16 v[176:177],v102 offset:49152\n"
"v_mfma_f32_16x16x32_bf16 a[212:215],v[244:247],v[216:219],a[212:215]\n"
"ds_read_b64_tr_b16 v[178:179],v102 offset:50176\n"
"v_mfma_f32_16x16x32_bf16 a[216:219],v[248:251],v[216:219],a[216:219]\n"
"ds_read_b64_tr_b16 v[180:181],v103 offset:49152\n"
"v_mfma_f32_16x16x32_bf16 a[248:251],v[248:251],v[220:223],a[248:251]\n"
"ds_read_b64_tr_b16 v[182:183],v103 offset:50176\n"
"v_mfma_f32_16x16x32_bf16 a[252:255],v[252:255],v[220:223],a[252:255]\n"
"ds_read_b64_tr_b16 v[184:185],v104 offset:49152\n"
"ds_read_b64_tr_b16 v[186:187],v104 offset:50176\n"
"v_mfma_f32_16x16x32_bf16 a[220:223],v[252:255],v[216:219],a[220:223]\n"
"s_waitcnt vmcnt(19)\n"
"s_waitcnt lgkmcnt(10)\n"
"v_mfma_f32_16x16x32_bf16 a[192:195],v[160:163],v[152:155],a[192:195]\n"
"ds_read_b64_tr_b16 v[188:189],v105 offset:49152\n"
"ds_read_b64_tr_b16 v[190:191],v105 offset:50176\n"
"v_mfma_f32_16x16x32_bf16 a[224:227],v[160:163],v[156:159],a[224:227]\n"
"buffer_store_dwordx2 v[124:125],off,s[24:27],s57 sc1\n"
"v_mfma_f32_16x16x32_bf16 a[228:231],v[164:167],v[156:159],a[228:231]\n"
"ds_read_b128 v[128:131],v80 offset:32768\n"
"v_mfma_f32_16x16x32_bf16 a[196:199],v[164:167],v[152:155],a[196:199]\n"
"ds_read_b128 v[192:195],v81 offset:32768\n"
"s_waitcnt lgkmcnt(10)\n"
"v_mfma_f32_16x16x32_bf16 a[200:203],v[168:171],v[152:155],a[200:203]\n"
"ds_read_b64_tr_b16 v[224:225],v98 offset:32768\n"
"v_mfma_f32_16x16x32_bf16 a[232:235],v[168:171],v[156:159],a[232:235]\n"
"ds_read_b64_tr_b16 v[226:227],v98 offset:33792\n"
"v_mfma_f32_16x16x32_bf16 a[236:239],v[172:175],v[156:159],a[236:239]\n"
"ds_read_b64_tr_b16 v[228:229],v99 offset:32768\n"
"v_mfma_f32_16x16x32_bf16 a[204:207],v[172:175],v[152:155],a[204:207]\n"
"ds_read_b64_tr_b16 v[230:231],v99 offset:33792\n"
"s_waitcnt lgkmcnt(10)\n"
"v_mfma_f32_16x16x32_bf16 a[208:211],v[176:179],v[152:155],a[208:211]\n"
"ds_read_b64_tr_b16 v[232:233],v100 offset:32768\n"
"v_mfma_f32_16x16x32_bf16 a[240:243],v[176:179],v[156:159],a[240:243]\n"
"ds_read_b64_tr_b16 v[234:235],v100 offset:33792\n"
"v_mfma_f32_16x16x32_bf16 a[244:247],v[180:183],v[156:159],a[244:247]\n"
"ds_read_b64_tr_b16 v[236:237],v101 offset:32768\n"
"v_mfma_f32_16x16x32_bf16 a[212:215],v[180:183],v[152:155],a[212:215]\n"
"ds_read_b64_tr_b16 v[238:239],v101 offset:33792\n"
"s_waitcnt lgkmcnt(10)\n"
"v_mfma_f32_16x16x32_bf16 a[216:219],v[184:187],v[152:155],a[216:219]\n"
"ds_read_b64_tr_b16 v[240:241],v102 offset:32768\n"
"v_mfma_f32_16x16x32_bf16 a[248:251],v[184:187],v[156:159],a[248:251]\n"
"ds_read_b64_tr_b16 v[242:243],v102 offset:33792\n"
"v_mfma_f32_16x16x32_bf16 a[252:255],v[188:191],v[156:159],a[252:255]\n"
"ds_read_b64_tr_b16 v[244:245],v103 offset:32768\n"
"v_mfma_f32_16x16x32_bf16 a[220:223],v[188:191],v[152:155],a[220:223]\n"
"ds_read_b64_tr_b16 v[246:247],v103 offset:33792\n"
"s_waitcnt lgkmcnt(8)\n"
"v_mfma_f32_16x16x32_bf16 a[0:3],v[160:163],v[128:131],a[0:3]\n"
"ds_read_b64_tr_b16 v[248:249],v104 offset:32768\n"
"v_mfma_f32_16x16x32_bf16 a[0:3],v[224:227],v[192:195],a[0:3]\n"
"ds_read_b64_tr_b16 v[250:251],v104 offset:33792\n"
"v_mfma_f32_16x16x32_bf16 a[4:7],v[228:231],v[192:195],a[4:7]\n"
"ds_read_b64_tr_b16 v[252:253],v105 offset:32768\n"
"v_mfma_f32_16x16x32_bf16 a[4:7],v[164:167],v[128:131],a[4:7]\n"
"ds_read_b64_tr_b16 v[254:255],v105 offset:33792\n"
"s_waitcnt lgkmcnt(8)\n"
"v_mfma_f32_16x16x32_bf16 a[8:11],v[168:171],v[128:131],a[8:11]\n"
"ds_read_b128 v[132:135],v80 offset:34816\n"
"v_mfma_f32_16x16x32_bf16 a[8:11],v[232:235],v[192:195],a[8:11]\n"
"ds_read_b128 v[196:199],v81 offset:34816\n"
"v_mfma_f32_16x16x32_bf16 a[12:15],v[236:239],v[192:195],a[12:15]\n"
"ds_read_b128 v[136:139],v80 offset:36864\n"
"v_mfma_f32_16x16x32_bf16 a[12:15],v[172:175],v[128:131],a[12:15]\n"
"ds_read_b128 v[200:203],v81 offset:36864\n"
"s_waitcnt lgkmcnt(8)\n"
"v_mfma_f32_16x16x32_bf16 a[16:19],v[176:179],v[128:131],a[16:19]\n"
"ds_read_b128 v[140:143],v80 offset:38912\n"
"v_mfma_f32_16x16x32_bf16 a[16:19],v[240:243],v[192:195],a[16:19]\n"
"ds_read_b128 v[204:207],v81 offset:38912\n"
"v_mfma_f32_16x16x32_bf16 a[20:23],v[244:247],v[192:195],a[20:23]\n"
"ds_read_b128 v[144:147],v80 offset:40960\n"
"v_mfma_f32_16x16x32_bf16 a[20:23],v[180:183],v[128:131],a[20:23]\n"
"ds_read_b128 v[208:211],v81 offset:40960\n"
"s_waitcnt lgkmcnt(8)\n"
"v_mfma_f32_16x16x32_bf16 a[24:27],v[184:187],v[128:131],a[24:27]\n"
"ds_read_b128 v[148:151],v80 offset:43008\n"
"v_mfma_f32_16x16x32_bf16 a[24:27],v[248:251],v[192:195],a[24:27]\n"
"ds_read_b128 v[212:215],v81 offset:43008\n"
"v_mfma_f32_16x16x32_bf16 a[28:31],v[252:255],v[192:195],a[28:31]\n"
"ds_read_b128 v[216:219],v81 offset:45056\n"
"v_mfma_f32_16x16x32_bf16 a[28:31],v[188:191],v[128:131],a[28:31]\n"
"ds_read_b128 v[220:223],v81 offset:47104\n"
"s_waitcnt lgkmcnt(10)\n"
"v_mfma_f32_16x16x32_bf16 a[32:35],v[160:163],v[132:135],a[32:35]\n"
"v_mfma_f32_16x16x32_bf16 a[32:35],v[224:227],v[196:199],a[32:35]\n"
"v_mfma_f32_16x16x32_bf16 a[36:39],v[228:231],v[196:199],a[36:39]\n"
"v_mfma_f32_16x16x32_bf16 a[36:39],v[164:167],v[132:135],a[36:39]\n"
"v_mfma_f32_16x16x32_bf16 a[40:43],v[168:171],v[132:135],a[40:43]\n"
"s_waitcnt lgkmcnt(0)\n"
"s_barrier\n"
"v_mfma_f32_16x16x32_bf16 a[40:43],v[232:235],v[196:199],a[40:43]\n"
"s_mov_b32 s77,3145728\n"
"s_add_u32 m0,s78,147456\n"
"v_mfma_f32_16x16x32_bf16 a[44:47],v[236:239],v[196:199],a[44:47]\n"
"buffer_load_dwordx4 v110,s[20:23],s77 offen offset:0 lds\n"
"v_mfma_f32_16x16x32_bf16 a[44:47],v[172:175],v[132:135],a[44:47]\n"
"v_mfma_f32_16x16x32_bf16 a[48:51],v[176:179],v[132:135],a[48:51]\n"
"v_mfma_f32_16x16x32_bf16 a[48:51],v[240:243],v[196:199],a[48:51]\n"
"v_mfma_f32_16x16x32_bf16 a[52:55],v[244:247],v[196:199],a[52:55]\n"
"buffer_load_dwordx4 v111,s[20:23],s77 offen offset:1024 lds\n"
"v_mfma_f32_16x16x32_bf16 a[52:55],v[180:183],v[132:135],a[52:55]\n"
"v_mfma_f32_16x16x32_bf16 a[56:59],v[184:187],v[132:135],a[56:59]\n"
"v_mfma_f32_16x16x32_bf16 a[56:59],v[248:251],v[196:199],a[56:59]\n"
"v_mfma_f32_16x16x32_bf16 a[60:63],v[252:255],v[196:199],a[60:63]\n"
"buffer_load_dwordx4 v113,s[20:23],s77 offen offset:2048 lds\n"
"v_mfma_f32_16x16x32_bf16 a[60:63],v[188:191],v[132:135],a[60:63]\n"
"v_mfma_f32_16x16x32_bf16 a[64:67],v[160:163],v[136:139],a[64:67]\n"
"v_mfma_f32_16x16x32_bf16 a[64:67],v[224:227],v[200:203],a[64:67]\n"
"v_mfma_f32_16x16x32_bf16 a[68:71],v[228:231],v[200:203],a[68:71]\n"
"buffer_load_dwordx4 v114,s[20:23],s77 offen offset:3072 lds\n"
"v_mfma_f32_16x16x32_bf16 a[68:71],v[164:167],v[136:139],a[68:71]\n"
"v_mfma_f32_16x16x32_bf16 a[72:75],v[168:171],v[136:139],a[72:75]\n"
"v_mfma_f32_16x16x32_bf16 a[72:75],v[232:235],v[200:203],a[72:75]\n"
"s_mov_b32 s77,3407872\n"
"s_add_u32 m0,s78,131072\n"
"v_mfma_f32_16x16x32_bf16 a[76:79],v[236:239],v[200:203],a[76:79]\n"
"buffer_load_dwordx4 v110,s[20:23],s77 offen offset:0 lds\n"
"v_mfma_f32_16x16x32_bf16 a[76:79],v[172:175],v[136:139],a[76:79]\n"
"v_mfma_f32_16x16x32_bf16 a[80:83],v[176:179],v[136:139],a[80:83]\n"
"v_mfma_f32_16x16x32_bf16 a[80:83],v[240:243],v[200:203],a[80:83]\n"
"v_mfma_f32_16x16x32_bf16 a[84:87],v[244:247],v[200:203],a[84:87]\n"
"buffer_load_dwordx4 v111,s[20:23],s77 offen offset:1024 lds\n"
"v_mfma_f32_16x16x32_bf16 a[84:87],v[180:183],v[136:139],a[84:87]\n"
"v_mfma_f32_16x16x32_bf16 a[88:91],v[184:187],v[136:139],a[88:91]\n"
"v_mfma_f32_16x16x32_bf16 a[88:91],v[248:251],v[200:203],a[88:91]\n"
"v_mfma_f32_16x16x32_bf16 a[92:95],v[252:255],v[200:203],a[92:95]\n"
"buffer_load_dwordx4 v113,s[20:23],s77 offen offset:2048 lds\n"
"v_mfma_f32_16x16x32_bf16 a[92:95],v[188:191],v[136:139],a[92:95]\n"
"v_mfma_f32_16x16x32_bf16 a[96:99],v[160:163],v[140:143],a[96:99]\n"
"v_mfma_f32_16x16x32_bf16 a[96:99],v[224:227],v[204:207],a[96:99]\n"
"v_mfma_f32_16x16x32_bf16 a[100:103],v[228:231],v[204:207],a[100:103]\n"
"buffer_load_dwordx4 v114,s[20:23],s77 offen offset:3072 lds\n"
"v_mfma_f32_16x16x32_bf16 a[100:103],v[164:167],v[140:143],a[100:103]\n"
"v_mfma_f32_16x16x32_bf16 a[104:107],v[168:171],v[140:143],a[104:107]\n"
"v_mfma_f32_16x16x32_bf16 a[104:107],v[232:235],v[204:207],a[104:107]\n"
"v_mfma_f32_16x16x32_bf16 a[108:111],v[236:239],v[204:207],a[108:111]\n"
"s_mov_b32 s76,896\n"
"s_add_u32 m0,s79,32768\n"
"v_mfma_f32_16x16x32_bf16 a[108:111],v[172:175],v[140:143],a[108:111]\n"
"buffer_load_dwordx4 v106,s[16:19],s76 offen offset:0 lds\n"
"v_mfma_f32_16x16x32_bf16 a[112:115],v[176:179],v[140:143],a[112:115]\n"
"v_mfma_f32_16x16x32_bf16 a[112:115],v[240:243],v[204:207],a[112:115]\n"
"v_mfma_f32_16x16x32_bf16 a[116:119],v[244:247],v[204:207],a[116:119]\n"
"v_mfma_f32_16x16x32_bf16 a[116:119],v[180:183],v[140:143],a[116:119]\n"
"v_mfma_f32_16x16x32_bf16 a[120:123],v[184:187],v[140:143],a[120:123]\n"
"buffer_load_dwordx4 v107,s[16:19],s76 offen offset:1024 lds\n"
"v_mfma_f32_16x16x32_bf16 a[120:123],v[248:251],v[204:207],a[120:123]\n"
"v_mfma_f32_16x16x32_bf16 a[124:127],v[252:255],v[204:207],a[124:127]\n"
"v_mfma_f32_16x16x32_bf16 a[124:127],v[188:191],v[140:143],a[124:127]\n"
"v_mfma_f32_16x16x32_bf16 a[128:131],v[160:163],v[144:147],a[128:131]\n"
"v_mfma_f32_16x16x32_bf16 a[128:131],v[224:227],v[208:211],a[128:131]\n"
"buffer_load_dwordx4 v108,s[16:19],s76 offen offset:2048 lds\n"
"v_mfma_f32_16x16x32_bf16 a[132:135],v[228:231],v[208:211],a[132:135]\n"
"v_mfma_f32_16x16x32_bf16 a[132:135],v[164:167],v[144:147],a[132:135]\n"
"v_mfma_f32_16x16x32_bf16 a[136:139],v[168:171],v[144:147],a[136:139]\n"
"v_mfma_f32_16x16x32_bf16 a[136:139],v[232:235],v[208:211],a[136:139]\n"
"v_mfma_f32_16x16x32_bf16 a[140:143],v[236:239],v[208:211],a[140:143]\n"
"buffer_load_dwordx4 v109,s[16:19],s76 offen offset:3072 lds\n"
"v_mfma_f32_16x16x32_bf16 a[140:143],v[172:175],v[144:147],a[140:143]\n"
"v_mfma_f32_16x16x32_bf16 a[144:147],v[176:179],v[144:147],a[144:147]\n"
"v_mfma_f32_16x16x32_bf16 a[144:147],v[240:243],v[208:211],a[144:147]\n"
"v_mfma_f32_16x16x32_bf16 a[148:151],v[244:247],v[208:211],a[148:151]\n"
"s_mov_b32 s76,263040\n"
"s_add_u32 m0,s79,36864\n"
"v_mfma_f32_16x16x32_bf16 a[148:151],v[180:183],v[144:147],a[148:151]\n"
"buffer_load_dwordx4 v106,s[16:19],s76 offen offset:0 lds\n"
"v_mfma_f32_16x16x32_bf16 a[152:155],v[184:187],v[144:147],a[152:155]\n"
"v_mfma_f32_16x16x32_bf16 a[152:155],v[248:251],v[208:211],a[152:155]\n"
"v_mfma_f32_16x16x32_bf16 a[156:159],v[252:255],v[208:211],a[156:159]\n"
"v_mfma_f32_16x16x32_bf16 a[156:159],v[188:191],v[144:147],a[156:159]\n"
"v_mfma_f32_16x16x32_bf16 a[160:163],v[160:163],v[148:151],a[160:163]\n"
"buffer_load_dwordx4 v107,s[16:19],s76 offen offset:1024 lds\n"
"v_mfma_f32_16x16x32_bf16 a[160:163],v[224:227],v[212:215],a[160:163]\n"
"v_mfma_f32_16x16x32_bf16 a[164:167],v[228:231],v[212:215],a[164:167]\n"
"v_mfma_f32_16x16x32_bf16 a[164:167],v[164:167],v[148:151],a[164:167]\n"
"v_mfma_f32_16x16x32_bf16 a[168:171],v[168:171],v[148:151],a[168:171]\n"
"v_mfma_f32_16x16x32_bf16 a[168:171],v[232:235],v[212:215],a[168:171]\n"
"buffer_load_dwordx4 v108,s[16:19],s76 offen offset:2048 lds\n"
"v_mfma_f32_16x16x32_bf16 a[172:175],v[236:239],v[212:215],a[172:175]\n"
"v_mfma_f32_16x16x32_bf16 a[172:175],v[172:175],v[148:151],a[172:175]\n"
"v_mfma_f32_16x16x32_bf16 a[176:179],v[176:179],v[148:151],a[176:179]\n"
"v_mfma_f32_16x16x32_bf16 a[176:179],v[240:243],v[212:215],a[176:179]\n"
"v_mfma_f32_16x16x32_bf16 a[180:183],v[244:247],v[212:215],a[180:183]\n"
"buffer_load_dwordx4 v109,s[16:19],s76 offen offset:3072 lds\n"
"v_mfma_f32_16x16x32_bf16 a[180:183],v[180:183],v[148:151],a[180:183]\n"
"s_mov_b32 s87,6291456\n"
"v_mfma_f32_16x16x32_bf16 a[184:187],v[184:187],v[148:151],a[184:187]\n"
"buffer_load_dword v115,v112,s[40:43],s87 offen\n"
"v_mfma_f32_16x16x32_bf16 a[184:187],v[248:251],v[212:215],a[184:187]\n"
"s_mov_b32 s87,7340032\n"
"v_mfma_f32_16x16x32_bf16 a[188:191],v[252:255],v[212:215],a[188:191]\n"
"buffer_load_dword v115,v112,s[40:43],s87 offen\n"
"v_mfma_f32_16x16x32_bf16 a[188:191],v[188:191],v[148:151],a[188:191]\n"
"s_waitcnt vmcnt(30)\n"
"s_barrier\n"
"v_mfma_f32_16x16x32_bf16 a[192:195],v[224:227],v[216:219],a[192:195]\n"
"ds_read_b128 v[152:155],v82 offset:12288\n"
"v_mfma_f32_16x16x32_bf16 a[224:227],v[224:227],v[220:223],a[224:227]\n"
"ds_read_b128 v[156:159],v82 offset:14336\n"
"v_mfma_f32_16x16x32_bf16 a[228:231],v[228:231],v[220:223],a[228:231]\n"
"ds_read_b64_tr_b16 v[160:161],v98 offset:0\n"
"v_mfma_f32_16x16x32_bf16 a[196:199],v[228:231],v[216:219],a[196:199]\n"
"ds_read_b64_tr_b16 v[162:163],v98 offset:1024\n"
"v_mfma_f32_16x16x32_bf16 a[200:203],v[232:235],v[216:219],a[200:203]\n"
"ds_read_b64_tr_b16 v[164:165],v99 offset:0\n"
"v_mfma_f32_16x16x32_bf16 a[232:235],v[232:235],v[220:223],a[232:235]\n"
"ds_read_b64_tr_b16 v[166:167],v99 offset:1024\n"
"v_mfma_f32_16x16x32_bf16 a[236:239],v[236:239],v[220:223],a[236:239]\n"
"ds_read_b64_tr_b16 v[168:169],v100 offset:0\n"
"v_mfma_f32_16x16x32_bf16 a[204:207],v[236:239],v[216:219],a[204:207]\n"
"ds_read_b64_tr_b16 v[170:171],v100 offset:1024\n"
"v_mfma_f32_16x16x32_bf16 a[208:211],v[240:243],v[216:219],a[208:211]\n"
"ds_read_b64_tr_b16 v[172:173],v101 offset:0\n"
"v_mfma_f32_16x16x32_bf16 a[240:243],v[240:243],v[220:223],a[240:243]\n"
"ds_read_b64_tr_b16 v[174:175],v101 offset:1024\n"
"v_mfma_f32_16x16x32_bf16 a[244:247],v[244:247],v[220:223],a[244:247]\n"
"ds_read_b64_tr_b16 v[176:177],v102 offset:0\n"
"v_mfma_f32_16x16x32_bf16 a[212:215],v[244:247],v[216:219],a[212:215]\n"
"ds_read_b64_tr_b16 v[178:179],v102 offset:1024\n"
"v_mfma_f32_16x16x32_bf16 a[216:219],v[248:251],v[216:219],a[216:219]\n"
"ds_read_b64_tr_b16 v[180:181],v103 offset:0\n"
"v_mfma_f32_16x16x32_bf16 a[248:251],v[248:251],v[220:223],a[248:251]\n"
"ds_read_b64_tr_b16 v[182:183],v103 offset:1024\n"
"v_mfma_f32_16x16x32_bf16 a[252:255],v[252:255],v[220:223],a[252:255]\n"
"ds_read_b64_tr_b16 v[184:185],v104 offset:0\n"
"v_mfma_f32_16x16x32_bf16 a[220:223],v[252:255],v[216:219],a[220:223]\n"
"ds_read_b64_tr_b16 v[186:187],v104 offset:1024\n"
"s_waitcnt lgkmcnt(10)\n"
"v_mfma_f32_16x16x32_bf16 a[192:195],v[160:163],v[152:155],a[192:195]\n"
"ds_read_b64_tr_b16 v[188:189],v105 offset:0\n"
"v_mfma_f32_16x16x32_bf16 a[224:227],v[160:163],v[156:159],a[224:227]\n"
"ds_read_b64_tr_b16 v[190:191],v105 offset:1024\n"
"v_mfma_f32_16x16x32_bf16 a[228:231],v[164:167],v[156:159],a[228:231]\n"
"ds_read_b128 v[128:131],v82 offset:0\n"
"v_mfma_f32_16x16x32_bf16 a[196:199],v[164:167],v[152:155],a[196:199]\n"
"ds_read_b128 v[192:195],v83 offset:0\n"
"s_waitcnt lgkmcnt(10)\n"
"v_mfma_f32_16x16x32_bf16 a[200:203],v[168:171],v[152:155],a[200:203]\n"
"ds_read_b64_tr_b16 v[224:225],v98 offset:16384\n"
"v_mfma_f32_16x16x32_bf16 a[232:235],v[168:171],v[156:159],a[232:235]\n"
"ds_read_b64_tr_b16 v[226:227],v98 offset:17408\n"
"v_mfma_f32_16x16x32_bf16 a[236:239],v[172:175],v[156:159],a[236:239]\n"
"ds_read_b64_tr_b16 v[228:229],v99 offset:16384\n"
"v_mfma_f32_16x16x32_bf16 a[204:207],v[172:175],v[152:155],a[204:207]\n"
"ds_read_b64_tr_b16 v[230:231],v99 offset:17408\n"
"s_waitcnt lgkmcnt(10)\n"
"v_mfma_f32_16x16x32_bf16 a[208:211],v[176:179],v[152:155],a[208:211]\n"
"ds_read_b64_tr_b16 v[232:233],v100 offset:16384\n"
"v_mfma_f32_16x16x32_bf16 a[240:243],v[176:179],v[156:159],a[240:243]\n"
"ds_read_b64_tr_b16 v[234:235],v100 offset:17408\n"
"v_mfma_f32_16x16x32_bf16 a[244:247],v[180:183],v[156:159],a[244:247]\n"
"ds_read_b64_tr_b16 v[236:237],v101 offset:16384\n"
"v_mfma_f32_16x16x32_bf16 a[212:215],v[180:183],v[152:155],a[212:215]\n"
"ds_read_b64_tr_b16 v[238:239],v101 offset:17408\n"
"s_waitcnt lgkmcnt(10)\n"
"v_mfma_f32_16x16x32_bf16 a[216:219],v[184:187],v[152:155],a[216:219]\n"
"ds_read_b64_tr_b16 v[240:241],v102 offset:16384\n"
"v_mfma_f32_16x16x32_bf16 a[248:251],v[184:187],v[156:159],a[248:251]\n"
"ds_read_b64_tr_b16 v[242:243],v102 offset:17408\n"
"v_mfma_f32_16x16x32_bf16 a[252:255],v[188:191],v[156:159],a[252:255]\n"
"ds_read_b64_tr_b16 v[244:245],v103 offset:16384\n"
"v_mfma_f32_16x16x32_bf16 a[220:223],v[188:191],v[152:155],a[220:223]\n"
"ds_read_b64_tr_b16 v[246:247],v103 offset:17408\n"
"s_waitcnt lgkmcnt(8)\n"
"v_mfma_f32_16x16x32_bf16 a[0:3],v[160:163],v[128:131],a[0:3]\n"
"ds_read_b64_tr_b16 v[248:249],v104 offset:16384\n"
"v_mfma_f32_16x16x32_bf16 a[0:3],v[224:227],v[192:195],a[0:3]\n"
"ds_read_b64_tr_b16 v[250:251],v104 offset:17408\n"
"v_mfma_f32_16x16x32_bf16 a[4:7],v[228:231],v[192:195],a[4:7]\n"
"ds_read_b64_tr_b16 v[252:253],v105 offset:16384\n"
"v_mfma_f32_16x16x32_bf16 a[4:7],v[164:167],v[128:131],a[4:7]\n"
"ds_read_b64_tr_b16 v[254:255],v105 offset:17408\n"
"s_waitcnt lgkmcnt(8)\n"
"v_mfma_f32_16x16x32_bf16 a[8:11],v[168:171],v[128:131],a[8:11]\n"
"ds_read_b128 v[132:135],v82 offset:2048\n"
"v_mfma_f32_16x16x32_bf16 a[8:11],v[232:235],v[192:195],a[8:11]\n"
"ds_read_b128 v[196:199],v83 offset:2048\n"
"v_mfma_f32_16x16x32_bf16 a[12:15],v[236:239],v[192:195],a[12:15]\n"
"ds_read_b128 v[136:139],v82 offset:4096\n"
"v_mfma_f32_16x16x32_bf16 a[12:15],v[172:175],v[128:131],a[12:15]\n"
"ds_read_b128 v[200:203],v83 offset:4096\n"
"s_waitcnt lgkmcnt(8)\n"
"v_mfma_f32_16x16x32_bf16 a[16:19],v[176:179],v[128:131],a[16:19]\n"
"ds_read_b128 v[140:143],v82 offset:6144\n"
"v_mfma_f32_16x16x32_bf16 a[16:19],v[240:243],v[192:195],a[16:19]\n"
"ds_read_b128 v[204:207],v83 offset:6144\n"
"v_mfma_f32_16x16x32_bf16 a[20:23],v[244:247],v[192:195],a[20:23]\n"
"ds_read_b128 v[144:147],v82 offset:8192\n"
"v_mfma_f32_16x16x32_bf16 a[20:23],v[180:183],v[128:131],a[20:23]\n"
"ds_read_b128 v[208:211],v83 offset:8192\n"
"s_waitcnt lgkmcnt(8)\n"
"v_mfma_f32_16x16x32_bf16 a[24:27],v[184:187],v[128:131],a[24:27]\n"
"ds_read_b128 v[148:151],v82 offset:10240\n"
"v_mfma_f32_16x16x32_bf16 a[24:27],v[248:251],v[192:195],a[24:27]\n"
"ds_read_b128 v[212:215],v83 offset:10240\n"
"v_mfma_f32_16x16x32_bf16 a[28:31],v[252:255],v[192:195],a[28:31]\n"
"ds_read_b128 v[216:219],v83 offset:12288\n"
"v_mfma_f32_16x16x32_bf16 a[28:31],v[188:191],v[128:131],a[28:31]\n"
"ds_read_b128 v[220:223],v83 offset:14336\n"
"s_waitcnt lgkmcnt(10)\n"
"v_mfma_f32_16x16x32_bf16 a[32:35],v[160:163],v[132:135],a[32:35]\n"
"v_mfma_f32_16x16x32_bf16 a[32:35],v[224:227],v[196:199],a[32:35]\n"
"buffer_load_dwordx4 v[74:77],v112,s[24:27],s58 offen sc1\n"
"v_mfma_f32_16x16x32_bf16 a[36:39],v[228:231],v[196:199],a[36:39]\n"
"v_mfma_f32_16x16x32_bf16 a[36:39],v[164:167],v[132:135],a[36:39]\n"
"v_mfma_f32_16x16x32_bf16 a[40:43],v[168:171],v[132:135],a[40:43]\n"
"s_waitcnt lgkmcnt(0)\n"
"s_barrier\n"
"v_mfma_f32_16x16x32_bf16 a[40:43],v[232:235],v[196:199],a[40:43]\n"
"s_mov_b32 s77,3670016\n"
"s_add_u32 m0,s78,98304\n"
"v_mfma_f32_16x16x32_bf16 a[44:47],v[236:239],v[196:199],a[44:47]\n"
"buffer_load_dwordx4 v110,s[20:23],s77 offen offset:0 lds\n"
"v_mfma_f32_16x16x32_bf16 a[44:47],v[172:175],v[132:135],a[44:47]\n"
"v_mfma_f32_16x16x32_bf16 a[48:51],v[176:179],v[132:135],a[48:51]\n"
"s_load_dwordx2 s[48:49],s[0:1],56\n"
"v_mfma_f32_16x16x32_bf16 a[48:51],v[240:243],v[196:199],a[48:51]\n"
"v_mfma_f32_16x16x32_bf16 a[52:55],v[244:247],v[196:199],a[52:55]\n"
"buffer_load_dwordx4 v111,s[20:23],s77 offen offset:1024 lds\n"
"v_mfma_f32_16x16x32_bf16 a[52:55],v[180:183],v[132:135],a[52:55]\n"
"v_mfma_f32_16x16x32_bf16 a[56:59],v[184:187],v[132:135],a[56:59]\n"
"v_mfma_f32_16x16x32_bf16 a[56:59],v[248:251],v[196:199],a[56:59]\n"
"v_mfma_f32_16x16x32_bf16 a[60:63],v[252:255],v[196:199],a[60:63]\n"
"buffer_load_dwordx4 v113,s[20:23],s77 offen offset:2048 lds\n"
"v_mfma_f32_16x16x32_bf16 a[60:63],v[188:191],v[132:135],a[60:63]\n"
"v_mfma_f32_16x16x32_bf16 a[64:67],v[160:163],v[136:139],a[64:67]\n"
"v_mfma_f32_16x16x32_bf16 a[64:67],v[224:227],v[200:203],a[64:67]\n"
"v_mfma_f32_16x16x32_bf16 a[68:71],v[228:231],v[200:203],a[68:71]\n"
"buffer_load_dwordx4 v114,s[20:23],s77 offen offset:3072 lds\n"
"v_mfma_f32_16x16x32_bf16 a[68:71],v[164:167],v[136:139],a[68:71]\n"
"v_mfma_f32_16x16x32_bf16 a[72:75],v[168:171],v[136:139],a[72:75]\n"
"v_mfma_f32_16x16x32_bf16 a[72:75],v[232:235],v[200:203],a[72:75]\n"
"s_mov_b32 s77,3932160\n"
"s_add_u32 m0,s78,114688\n"
"v_mfma_f32_16x16x32_bf16 a[76:79],v[236:239],v[200:203],a[76:79]\n"
"buffer_load_dwordx4 v110,s[20:23],s77 offen offset:0 lds\n"
"v_mfma_f32_16x16x32_bf16 a[76:79],v[172:175],v[136:139],a[76:79]\n"
"v_mfma_f32_16x16x32_bf16 a[80:83],v[176:179],v[136:139],a[80:83]\n"
"v_mfma_f32_16x16x32_bf16 a[80:83],v[240:243],v[200:203],a[80:83]\n"
"v_mfma_f32_16x16x32_bf16 a[84:87],v[244:247],v[200:203],a[84:87]\n"
"buffer_load_dwordx4 v111,s[20:23],s77 offen offset:1024 lds\n"
"v_mfma_f32_16x16x32_bf16 a[84:87],v[180:183],v[136:139],a[84:87]\n"
"v_mfma_f32_16x16x32_bf16 a[88:91],v[184:187],v[136:139],a[88:91]\n"
"v_mfma_f32_16x16x32_bf16 a[88:91],v[248:251],v[200:203],a[88:91]\n"
"v_mfma_f32_16x16x32_bf16 a[92:95],v[252:255],v[200:203],a[92:95]\n"
"buffer_load_dwordx4 v113,s[20:23],s77 offen offset:2048 lds\n"
"v_mfma_f32_16x16x32_bf16 a[92:95],v[188:191],v[136:139],a[92:95]\n"
"s_mov_b32 s76,s55\n"
"s_waitcnt vmcnt(7)\n"
"v_mfma_f32_16x16x32_bf16 a[96:99],v[160:163],v[140:143],a[96:99]\n"
"v_xor_b32 v116,s80,v74\n"
"v_xor_b32 v117,s81,v75\n"
"v_mfma_f32_16x16x32_bf16 a[96:99],v[224:227],v[204:207],a[96:99]\n"
"v_or_b32 v116,v116,v117\n"
"v_xor_b32 v117,s80,v76\n"
"v_mfma_f32_16x16x32_bf16 a[100:103],v[228:231],v[204:207],a[100:103]\n"
"buffer_load_dwordx4 v114,s[20:23],s77 offen offset:3072 lds\n"
"v_mfma_f32_16x16x32_bf16 a[100:103],v[164:167],v[140:143],a[100:103]\n"
"v_or_b32 v116,v116,v117\n"
"v_xor_b32 v117,s81,v77\n"
"v_mfma_f32_16x16x32_bf16 a[104:107],v[168:171],v[140:143],a[104:107]\n"
"v_or_b32 v116,v116,v117\n"
"v_cmp_ne_u32 vcc,0,v116\n"
"v_mfma_f32_16x16x32_bf16 a[104:107],v[232:235],v[204:207],a[104:107]\n"
"s_cbranch_vccnz .Lfk_xn0_s\n"
".Lfk_xn0_ok:\n"
"v_mfma_f32_16x16x32_bf16 a[108:111],v[236:239],v[204:207],a[108:111]\n"
"s_add_u32 m0,s78,65536\n"
"v_mfma_f32_16x16x32_bf16 a[108:111],v[172:175],v[140:143],a[108:111]\n"
"buffer_load_dwordx4 v112,s[24:27],s76 offen offset:0 sc1 lds\n"
"v_mfma_f32_16x16x32_bf16 a[112:115],v[176:179],v[140:143],a[112:115]\n"
"s_waitcnt lgkmcnt(0)\n"
"s_lshl2_add_u32 s88,s100,s52\n"
"v_mfma_f32_16x16x32_bf16 a[112:115],v[240:243],v[204:207],a[112:115]\n"
"s_mul_i32 s88,s88,7168\n"
"s_add_u32 s88,s88,0\n"
"v_mfma_f32_16x16x32_bf16 a[116:119],v[244:247],v[204:207],a[116:119]\n"
"s_add_u32 s48,s48,s88\n"
"s_addc_u32 s49,s49,0\n"
"s_mov_b32 s50,3584\n"
"v_mfma_f32_16x16x32_bf16 a[116:119],v[180:183],v[140:143],a[116:119]\n"
"s_mov_b32 s51,0x20000\n"
"v_lshlrev_b32 v8,3,v112\n"
"v_mfma_f32_16x16x32_bf16 a[120:123],v[184:187],v[140:143],a[120:123]\n"
"buffer_load_dwordx4 v112,s[24:27],s76 offen offset:1024 sc1 lds\n"
"v_mfma_f32_16x16x32_bf16 a[120:123],v[248:251],v[204:207],a[120:123]\n"
"v_mfma_f32_16x16x32_bf16 a[124:127],v[252:255],v[204:207],a[124:127]\n"
"v_mfma_f32_16x16x32_bf16 a[124:127],v[188:191],v[140:143],a[124:127]\n"
"v_mfma_f32_16x16x32_bf16 a[128:131],v[160:163],v[144:147],a[128:131]\n"
"v_mfma_f32_16x16x32_bf16 a[128:131],v[224:227],v[208:211],a[128:131]\n"
"buffer_load_dwordx4 v112,s[24:27],s76 offen offset:2048 sc1 lds\n"
"v_mfma_f32_16x16x32_bf16 a[132:135],v[228:231],v[208:211],a[132:135]\n"
"v_mfma_f32_16x16x32_bf16 a[132:135],v[164:167],v[144:147],a[132:135]\n"
"v_mfma_f32_16x16x32_bf16 a[136:139],v[168:171],v[144:147],a[136:139]\n"
"v_mfma_f32_16x16x32_bf16 a[136:139],v[232:235],v[208:211],a[136:139]\n"
"buffer_load_dwordx4 v112,s[24:27],s76 offen offset:3072 sc1 lds\n"
"v_mfma_f32_16x16x32_bf16 a[140:143],v[236:239],v[208:211],a[140:143]\n"
"s_xor_b32 s76,s76,16384\n"
"v_mfma_f32_16x16x32_bf16 a[140:143],v[172:175],v[144:147],a[140:143]\n"
"v_mfma_f32_16x16x32_bf16 a[144:147],v[176:179],v[144:147],a[144:147]\n"
"v_mfma_f32_16x16x32_bf16 a[144:147],v[240:243],v[208:211],a[144:147]\n"
"v_mfma_f32_16x16x32_bf16 a[148:151],v[244:247],v[208:211],a[148:151]\n"
"s_add_u32 m0,s78,81920\n"
"v_mfma_f32_16x16x32_bf16 a[148:151],v[180:183],v[144:147],a[148:151]\n"
"buffer_load_dwordx4 v112,s[24:27],s76 offen offset:0 sc1 lds\n"
"v_mfma_f32_16x16x32_bf16 a[152:155],v[184:187],v[144:147],a[152:155]\n"
"v_mfma_f32_16x16x32_bf16 a[152:155],v[248:251],v[208:211],a[152:155]\n"
"v_mfma_f32_16x16x32_bf16 a[156:159],v[252:255],v[208:211],a[156:159]\n"
"v_mfma_f32_16x16x32_bf16 a[156:159],v[188:191],v[144:147],a[156:159]\n"
"v_mfma_f32_16x16x32_bf16 a[160:163],v[160:163],v[148:151],a[160:163]\n"
"buffer_load_dwordx4 v112,s[24:27],s76 offen offset:1024 sc1 lds\n"
"v_mfma_f32_16x16x32_bf16 a[160:163],v[224:227],v[212:215],a[160:163]\n"
"v_mfma_f32_16x16x32_bf16 a[164:167],v[228:231],v[212:215],a[164:167]\n"
"v_mfma_f32_16x16x32_bf16 a[164:167],v[164:167],v[148:151],a[164:167]\n"
"v_mfma_f32_16x16x32_bf16 a[168:171],v[168:171],v[148:151],a[168:171]\n"
"v_mfma_f32_16x16x32_bf16 a[168:171],v[232:235],v[212:215],a[168:171]\n"
"buffer_load_dwordx4 v112,s[24:27],s76 offen offset:2048 sc1 lds\n"
"v_mfma_f32_16x16x32_bf16 a[172:175],v[236:239],v[212:215],a[172:175]\n"
"v_mfma_f32_16x16x32_bf16 a[172:175],v[172:175],v[148:151],a[172:175]\n"
"v_mfma_f32_16x16x32_bf16 a[176:179],v[176:179],v[148:151],a[176:179]\n"
"v_mfma_f32_16x16x32_bf16 a[176:179],v[240:243],v[212:215],a[176:179]\n"
"buffer_load_dwordx4 v112,s[24:27],s76 offen offset:3072 sc1 lds\n"
"v_mfma_f32_16x16x32_bf16 a[180:183],v[244:247],v[212:215],a[180:183]\n"
"s_xor_b32 s76,s76,49152\n"
"v_mfma_f32_16x16x32_bf16 a[180:183],v[180:183],v[148:151],a[180:183]\n"
"v_mfma_f32_16x16x32_bf16 a[184:187],v[184:187],v[148:151],a[184:187]\n"
"buffer_load_dword v115,v8,s[48:51],0 offen\n"
"v_mfma_f32_16x16x32_bf16 a[184:187],v[248:251],v[212:215],a[184:187]\n"
"v_mfma_f32_16x16x32_bf16 a[188:191],v[252:255],v[212:215],a[188:191]\n"
"v_mfma_f32_16x16x32_bf16 a[188:191],v[188:191],v[148:151],a[188:191]\n"
"s_barrier\n"
"v_mfma_f32_16x16x32_bf16 a[192:195],v[224:227],v[216:219],a[192:195]\n"
"ds_read_b128 v[152:155],v80 offset:12288\n"
"v_mfma_f32_16x16x32_bf16 a[224:227],v[224:227],v[220:223],a[224:227]\n"
"ds_read_b128 v[156:159],v80 offset:14336\n"
"v_mfma_f32_16x16x32_bf16 a[228:231],v[228:231],v[220:223],a[228:231]\n"
"ds_read_b64_tr_b16 v[160:161],v98 offset:49152\n"
"v_mfma_f32_16x16x32_bf16 a[196:199],v[228:231],v[216:219],a[196:199]\n"
"ds_read_b64_tr_b16 v[162:163],v98 offset:50176\n"
"v_mfma_f32_16x16x32_bf16 a[200:203],v[232:235],v[216:219],a[200:203]\n"
"ds_read_b64_tr_b16 v[164:165],v99 offset:49152\n"
"v_mfma_f32_16x16x32_bf16 a[232:235],v[232:235],v[220:223],a[232:235]\n"
"ds_read_b64_tr_b16 v[166:167],v99 offset:50176\n"
"v_mfma_f32_16x16x32_bf16 a[236:239],v[236:239],v[220:223],a[236:239]\n"
"ds_read_b64_tr_b16 v[168:169],v100 offset:49152\n"
"v_mfma_f32_16x16x32_bf16 a[204:207],v[236:239],v[216:219],a[204:207]\n"
"ds_read_b64_tr_b16 v[170:171],v100 offset:50176\n"
"v_mfma_f32_16x16x32_bf16 a[208:211],v[240:243],v[216:219],a[208:211]\n"
"ds_read_b64_tr_b16 v[172:173],v101 offset:49152\n"
"v_mfma_f32_16x16x32_bf16 a[240:243],v[240:243],v[220:223],a[240:243]\n"
"ds_read_b64_tr_b16 v[174:175],v101 offset:50176\n"
"v_mfma_f32_16x16x32_bf16 a[244:247],v[244:247],v[220:223],a[244:247]\n"
"ds_read_b64_tr_b16 v[176:177],v102 offset:49152\n"
"v_mfma_f32_16x16x32_bf16 a[212:215],v[244:247],v[216:219],a[212:215]\n"
"ds_read_b64_tr_b16 v[178:179],v102 offset:50176\n"
"v_mfma_f32_16x16x32_bf16 a[216:219],v[248:251],v[216:219],a[216:219]\n"
"ds_read_b64_tr_b16 v[180:181],v103 offset:49152\n"
"v_mfma_f32_16x16x32_bf16 a[248:251],v[248:251],v[220:223],a[248:251]\n"
"ds_read_b64_tr_b16 v[182:183],v103 offset:50176\n"
"ds_read_b64_tr_b16 v[184:185],v104 offset:49152\n"
"v_mfma_f32_16x16x32_bf16 a[252:255],v[252:255],v[220:223],a[252:255]\n"
"v_mov_b32 v110,v72\n"
"v_mov_b32 v111,v73\n"
"v_mfma_f32_16x16x32_bf16 a[220:223],v[252:255],v[216:219],a[220:223]\n"
"ds_read_b64_tr_b16 v[186:187],v104 offset:50176\n"
"s_waitcnt lgkmcnt(10)\n"
"v_mfma_f32_16x16x32_bf16 a[192:195],v[160:163],v[152:155],a[192:195]\n"
"ds_read_b64_tr_b16 v[188:189],v105 offset:49152\n"
"v_mfma_f32_16x16x32_bf16 a[224:227],v[160:163],v[156:159],a[224:227]\n"
"ds_read_b64_tr_b16 v[190:191],v105 offset:50176\n"
"v_mfma_f32_16x16x32_bf16 a[228:231],v[164:167],v[156:159],a[228:231]\n"
"ds_read_b128 v[128:131],v80 offset:0\n"
"v_mfma_f32_16x16x32_bf16 a[196:199],v[164:167],v[152:155],a[196:199]\n"
"ds_read_b128 v[192:195],v81 offset:0\n"
"s_waitcnt lgkmcnt(10)\n"
"v_mfma_f32_16x16x32_bf16 a[200:203],v[168:171],v[152:155],a[200:203]\n"
"ds_read_b64_tr_b16 v[224:225],v98 offset:32768\n"
"v_mfma_f32_16x16x32_bf16 a[232:235],v[168:171],v[156:159],a[232:235]\n"
"ds_read_b64_tr_b16 v[226:227],v98 offset:33792\n"
"v_mfma_f32_16x16x32_bf16 a[236:239],v[172:175],v[156:159],a[236:239]\n"
"ds_read_b64_tr_b16 v[228:229],v99 offset:32768\n"
"v_mfma_f32_16x16x32_bf16 a[204:207],v[172:175],v[152:155],a[204:207]\n"
"ds_read_b64_tr_b16 v[230:231],v99 offset:33792\n"
"s_waitcnt lgkmcnt(10)\n"
"v_mfma_f32_16x16x32_bf16 a[208:211],v[176:179],v[152:155],a[208:211]\n"
"ds_read_b64_tr_b16 v[232:233],v100 offset:32768\n"
"v_mfma_f32_16x16x32_bf16 a[240:243],v[176:179],v[156:159],a[240:243]\n"
"ds_read_b64_tr_b16 v[234:235],v100 offset:33792\n"
"v_mfma_f32_16x16x32_bf16 a[244:247],v[180:183],v[156:159],a[244:247]\n"
"ds_read_b64_tr_b16 v[236:237],v101 offset:32768\n"
"v_mfma_f32_16x16x32_bf16 a[212:215],v[180:183],v[152:155],a[212:215]\n"
"ds_read_b64_tr_b16 v[238:239],v101 offset:33792\n"
"s_waitcnt lgkmcnt(10)\n"
"v_mfma_f32_16x16x32_bf16 a[216:219],v[184:187],v[152:155],a[216:219]\n"
"ds_read_b64_tr_b16 v[240:241],v102 offset:32768\n"
"v_mfma_f32_16x16x32_bf16 a[248:251],v[184:187],v[156:159],a[248:251]\n"
"ds_read_b64_tr_b16 v[242:243],v102 offset:33792\n"
"v_mfma_f32_16x16x32_bf16 a[252:255],v[188:191],v[156:159],a[252:255]\n"
"ds_read_b64_tr_b16 v[244:245],v103 offset:32768\n"
"v_mfma_f32_16x16x32_bf16 a[220:223],v[188:191],v[152:155],a[220:223]\n"
"ds_read_b64_tr_b16 v[246:247],v103 offset:33792\n"
"s_waitcnt lgkmcnt(8)\n"
"v_mfma_f32_16x16x32_bf16 a[0:3],v[160:163],v[128:131],a[0:3]\n"
"ds_read_b64_tr_b16 v[248:249],v104 offset:32768\n"
"v_mfma_f32_16x16x32_bf16 a[0:3],v[224:227],v[192:195],a[0:3]\n"
"ds_read_b64_tr_b16 v[250:251],v104 offset:33792\n"
"v_mfma_f32_16x16x32_bf16 a[4:7],v[228:231],v[192:195],a[4:7]\n"
"ds_read_b64_tr_b16 v[252:253],v105 offset:32768\n"
"v_mfma_f32_16x16x32_bf16 a[4:7],v[164:167],v[128:131],a[4:7]\n"
"ds_read_b64_tr_b16 v[254:255],v105 offset:33792\n"
"s_waitcnt lgkmcnt(8)\n"
"v_mfma_f32_16x16x32_bf16 a[8:11],v[168:171],v[128:131],a[8:11]\n"
"ds_read_b128 v[132:135],v80 offset:2048\n"
"v_mfma_f32_16x16x32_bf16 a[8:11],v[232:235],v[192:195],a[8:11]\n"
"ds_read_b128 v[196:199],v81 offset:2048\n"
"v_mfma_f32_16x16x32_bf16 a[12:15],v[236:239],v[192:195],a[12:15]\n"
"ds_read_b128 v[136:139],v80 offset:4096\n"
"v_mfma_f32_16x16x32_bf16 a[12:15],v[172:175],v[128:131],a[12:15]\n"
"ds_read_b128 v[200:203],v81 offset:4096\n"
"s_waitcnt lgkmcnt(8)\n"
"v_mfma_f32_16x16x32_bf16 a[16:19],v[176:179],v[128:131],a[16:19]\n"
"ds_read_b128 v[140:143],v80 offset:6144\n"
"v_mfma_f32_16x16x32_bf16 a[16:19],v[240:243],v[192:195],a[16:19]\n"
"ds_read_b128 v[204:207],v81 offset:6144\n"
"v_mfma_f32_16x16x32_bf16 a[20:23],v[244:247],v[192:195],a[20:23]\n"
"ds_read_b128 v[144:147],v80 offset:8192\n"
"v_mfma_f32_16x16x32_bf16 a[20:23],v[180:183],v[128:131],a[20:23]\n"
"ds_read_b128 v[208:211],v81 offset:8192\n"
"s_waitcnt lgkmcnt(8)\n"
"v_mfma_f32_16x16x32_bf16 a[24:27],v[184:187],v[128:131],a[24:27]\n"
"ds_read_b128 v[148:151],v80 offset:10240\n"
"v_mfma_f32_16x16x32_bf16 a[24:27],v[248:251],v[192:195],a[24:27]\n"
"ds_read_b128 v[212:215],v81 offset:10240\n"
"v_mfma_f32_16x16x32_bf16 a[28:31],v[252:255],v[192:195],a[28:31]\n"
"ds_read_b128 v[216:219],v81 offset:12288\n"
"v_mfma_f32_16x16x32_bf16 a[28:31],v[188:191],v[128:131],a[28:31]\n"
"ds_read_b128 v[220:223],v81 offset:14336\n"
"s_waitcnt lgkmcnt(10)\n"
"v_mfma_f32_16x16x32_bf16 a[32:35],v[160:163],v[132:135],a[32:35]\n"
"v_mfma_f32_16x16x32_bf16 a[32:35],v[224:227],v[196:199],a[32:35]\n"
"v_mfma_f32_16x16x32_bf16 a[36:39],v[228:231],v[196:199],a[36:39]\n"
"v_mfma_f32_16x16x32_bf16 a[36:39],v[164:167],v[132:135],a[36:39]\n"
"v_mfma_f32_16x16x32_bf16 a[40:43],v[168:171],v[132:135],a[40:43]\n"
"s_waitcnt lgkmcnt(0)\n"
"s_barrier\n"
"v_mfma_f32_16x16x32_bf16 a[40:43],v[232:235],v[196:199],a[40:43]\n"
"s_add_u32 m0,s85,147456\n"
"v_mfma_f32_16x16x32_bf16 a[44:47],v[236:239],v[196:199],a[44:47]\n"
"buffer_load_dwordx4 v110,s[28:31],s86 offen lds\n"
"v_mfma_f32_16x16x32_bf16 a[44:47],v[172:175],v[132:135],a[44:47]\n"
"v_mfma_f32_16x16x32_bf16 a[48:51],v[176:179],v[132:135],a[48:51]\n"
"s_load_dwordx2 s[48:49],s[0:1],56\n"
"v_mfma_f32_16x16x32_bf16 a[48:51],v[240:243],v[196:199],a[48:51]\n"
"s_add_u32 m0,s85,148480\n"
"v_mfma_f32_16x16x32_bf16 a[52:55],v[244:247],v[196:199],a[52:55]\n"
"buffer_load_dwordx4 v111,s[28:31],s86 offen lds\n"
"v_mfma_f32_16x16x32_bf16 a[52:55],v[180:183],v[132:135],a[52:55]\n"
"s_xor_b32 s86,s86,262144\n"
"v_mfma_f32_16x16x32_bf16 a[56:59],v[184:187],v[132:135],a[56:59]\n"
"v_mfma_f32_16x16x32_bf16 a[56:59],v[248:251],v[196:199],a[56:59]\n"
"s_add_u32 m0,s85,155648\n"
"v_mfma_f32_16x16x32_bf16 a[60:63],v[252:255],v[196:199],a[60:63]\n"
"buffer_load_dwordx4 v110,s[28:31],s86 offen lds\n"
"v_mfma_f32_16x16x32_bf16 a[60:63],v[188:191],v[132:135],a[60:63]\n"
"v_mfma_f32_16x16x32_bf16 a[64:67],v[160:163],v[136:139],a[64:67]\n"
"v_mfma_f32_16x16x32_bf16 a[64:67],v[224:227],v[200:203],a[64:67]\n"
"s_add_u32 m0,s85,156672\n"
"v_mfma_f32_16x16x32_bf16 a[68:71],v[228:231],v[200:203],a[68:71]\n"
"buffer_load_dwordx4 v111,s[28:31],s86 offen lds\n"
"v_mfma_f32_16x16x32_bf16 a[68:71],v[164:167],v[136:139],a[68:71]\n"
"s_xor_b32 s86,s86,786432\n"
"v_mfma_f32_16x16x32_bf16 a[72:75],v[168:171],v[136:139],a[72:75]\n"
"v_mfma_f32_16x16x32_bf16 a[72:75],v[232:235],v[200:203],a[72:75]\n"
"s_add_u32 m0,s85,131072\n"
"v_mfma_f32_16x16x32_bf16 a[76:79],v[236:239],v[200:203],a[76:79]\n"
"buffer_load_dwordx4 v110,s[28:31],s86 offen lds\n"
"v_mfma_f32_16x16x32_bf16 a[76:79],v[172:175],v[136:139],a[76:79]\n"
"v_mfma_f32_16x16x32_bf16 a[80:83],v[176:179],v[136:139],a[80:83]\n"
"v_mfma_f32_16x16x32_bf16 a[80:83],v[240:243],v[200:203],a[80:83]\n"
"s_add_u32 m0,s85,132096\n"
"v_mfma_f32_16x16x32_bf16 a[84:87],v[244:247],v[200:203],a[84:87]\n"
"buffer_load_dwordx4 v111,s[28:31],s86 offen lds\n"
"v_mfma_f32_16x16x32_bf16 a[84:87],v[180:183],v[136:139],a[84:87]\n"
"s_xor_b32 s86,s86,262144\n"
"v_mfma_f32_16x16x32_bf16 a[88:91],v[184:187],v[136:139],a[88:91]\n"
"v_mfma_f32_16x16x32_bf16 a[88:91],v[248:251],v[200:203],a[88:91]\n"
"v_mfma_f32_16x16x32_bf16 a[92:95],v[252:255],v[200:203],a[92:95]\n"
"v_mfma_f32_16x16x32_bf16 a[92:95],v[188:191],v[136:139],a[92:95]\n"
"v_mfma_f32_16x16x32_bf16 a[96:99],v[160:163],v[140:143],a[96:99]\n"
"v_mfma_f32_16x16x32_bf16 a[96:99],v[224:227],v[204:207],a[96:99]\n"
"s_add_u32 m0,s85,139264\n"
"v_mfma_f32_16x16x32_bf16 a[100:103],v[228:231],v[204:207],a[100:103]\n"
"buffer_load_dwordx4 v110,s[28:31],s86 offen lds\n"
"v_mfma_f32_16x16x32_bf16 a[100:103],v[164:167],v[140:143],a[100:103]\n"
"v_mfma_f32_16x16x32_bf16 a[104:107],v[168:171],v[140:143],a[104:107]\n"
"v_mfma_f32_16x16x32_bf16 a[104:107],v[232:235],v[204:207],a[104:107]\n"
"s_add_u32 m0,s85,140288\n"
"v_mfma_f32_16x16x32_bf16 a[108:111],v[236:239],v[204:207],a[108:111]\n"
"buffer_load_dwordx4 v111,s[28:31],s86 offen lds\n"
"v_mfma_f32_16x16x32_bf16 a[108:111],v[172:175],v[140:143],a[108:111]\n"
"s_xor_b32 s86,s86,1835008\n"
"v_mfma_f32_16x16x32_bf16 a[112:115],v[176:179],v[140:143],a[112:115]\n"
"s_waitcnt lgkmcnt(0)\n"
"s_lshl2_add_u32 s88,s100,s52\n"
"v_mfma_f32_16x16x32_bf16 a[112:115],v[240:243],v[204:207],a[112:115]\n"
"s_mul_i32 s88,s88,7168\n"
"s_add_u32 m0,s78,0\n"
"v_mfma_f32_16x16x32_bf16 a[116:119],v[244:247],v[204:207],a[116:119]\n"
"buffer_load_dwordx4 v112,s[24:27],s76 offen offset:0 sc1 lds\n"
"v_mfma_f32_16x16x32_bf16 a[116:119],v[180:183],v[140:143],a[116:119]\n"
"s_add_u32 s88,s88,3584\n"
"s_add_u32 s48,s48,s88\n"
"s_addc_u32 s49,s49,0\n"
"v_mfma_f32_16x16x32_bf16 a[120:123],v[184:187],v[140:143],a[120:123]\n"
"s_mov_b32 s50,3584\n"
"s_mov_b32 s51,0x20000\n"
"v_mfma_f32_16x16x32_bf16 a[120:123],v[248:251],v[204:207],a[120:123]\n"
"v_lshlrev_b32 v8,3,v112\n"
"v_mfma_f32_16x16x32_bf16 a[124:127],v[252:255],v[204:207],a[124:127]\n"
"buffer_load_dwordx4 v112,s[24:27],s76 offen offset:1024 sc1 lds\n"
"v_mfma_f32_16x16x32_bf16 a[124:127],v[188:191],v[140:143],a[124:127]\n"
"v_mfma_f32_16x16x32_bf16 a[128:131],v[160:163],v[144:147],a[128:131]\n"
"v_mfma_f32_16x16x32_bf16 a[128:131],v[224:227],v[208:211],a[128:131]\n"
"v_mfma_f32_16x16x32_bf16 a[132:135],v[228:231],v[208:211],a[132:135]\n"
"buffer_load_dwordx4 v112,s[24:27],s76 offen offset:2048 sc1 lds\n"
"v_mfma_f32_16x16x32_bf16 a[132:135],v[164:167],v[144:147],a[132:135]\n"
"v_mfma_f32_16x16x32_bf16 a[136:139],v[168:171],v[144:147],a[136:139]\n"
"v_mfma_f32_16x16x32_bf16 a[136:139],v[232:235],v[208:211],a[136:139]\n"
"buffer_load_dwordx4 v112,s[24:27],s76 offen offset:3072 sc1 lds\n"
"v_mfma_f32_16x16x32_bf16 a[140:143],v[236:239],v[208:211],a[140:143]\n"
"s_xor_b32 s76,s76,16384\n"
"v_mfma_f32_16x16x32_bf16 a[140:143],v[172:175],v[144:147],a[140:143]\n"
"v_mfma_f32_16x16x32_bf16 a[144:147],v[176:179],v[144:147],a[144:147]\n"
"v_mfma_f32_16x16x32_bf16 a[144:147],v[240:243],v[208:211],a[144:147]\n"
"s_add_u32 m0,s78,16384\n"
"v_mfma_f32_16x16x32_bf16 a[148:151],v[244:247],v[208:211],a[148:151]\n"
"buffer_load_dwordx4 v112,s[24:27],s76 offen offset:0 sc1 lds\n"
"v_mfma_f32_16x16x32_bf16 a[148:151],v[180:183],v[144:147],a[148:151]\n"
"v_mfma_f32_16x16x32_bf16 a[152:155],v[184:187],v[144:147],a[152:155]\n"
"v_mfma_f32_16x16x32_bf16 a[152:155],v[248:251],v[208:211],a[152:155]\n"
"v_mfma_f32_16x16x32_bf16 a[156:159],v[252:255],v[208:211],a[156:159]\n"
"buffer_load_dwordx4 v112,s[24:27],s76 offen offset:1024 sc1 lds\n"
"v_mfma_f32_16x16x32_bf16 a[156:159],v[188:191],v[144:147],a[156:159]\n"
"v_mfma_f32_16x16x32_bf16 a[160:163],v[160:163],v[148:151],a[160:163]\n"
"v_mfma_f32_16x16x32_bf16 a[160:163],v[224:227],v[212:215],a[160:163]\n"
"v_mfma_f32_16x16x32_bf16 a[164:167],v[228:231],v[212:215],a[164:167]\n"
"buffer_load_dwordx4 v112,s[24:27],s76 offen offset:2048 sc1 lds\n"
"v_mfma_f32_16x16x32_bf16 a[164:167],v[164:167],v[148:151],a[164:167]\n"
"v_mfma_f32_16x16x32_bf16 a[168:171],v[168:171],v[148:151],a[168:171]\n"
"v_mfma_f32_16x16x32_bf16 a[168:171],v[232:235],v[212:215],a[168:171]\n"
"buffer_load_dwordx4 v112,s[24:27],s76 offen offset:3072 sc1 lds\n"
"v_mfma_f32_16x16x32_bf16 a[172:175],v[236:239],v[212:215],a[172:175]\n"
"s_xor_b32 s76,s76,114688\n"
"v_mfma_f32_16x16x32_bf16 a[172:175],v[172:175],v[148:151],a[172:175]\n"
"v_mfma_f32_16x16x32_bf16 a[176:179],v[176:179],v[148:151],a[176:179]\n"
"buffer_load_dword v115,v8,s[48:51],0 offen\n"
"v_mfma_f32_16x16x32_bf16 a[176:179],v[240:243],v[212:215],a[176:179]\n"
"v_mfma_f32_16x16x32_bf16 a[180:183],v[244:247],v[212:215],a[180:183]\n"
"v_mfma_f32_16x16x32_bf16 a[180:183],v[180:183],v[148:151],a[180:183]\n"
"v_mfma_f32_16x16x32_bf16 a[184:187],v[184:187],v[148:151],a[184:187]\n"
"v_mfma_f32_16x16x32_bf16 a[184:187],v[248:251],v[212:215],a[184:187]\n"
"v_mfma_f32_16x16x32_bf16 a[188:191],v[252:255],v[212:215],a[188:191]\n"
"v_mfma_f32_16x16x32_bf16 a[188:191],v[188:191],v[148:151],a[188:191]\n"
"s_waitcnt vmcnt(26)\n"
"s_barrier\n"
"v_mfma_f32_16x16x32_bf16 a[192:195],v[224:227],v[216:219],a[192:195]\n"
"ds_read_b128 v[152:155],v80 offset:45056\n"
"v_mfma_f32_16x16x32_bf16 a[224:227],v[224:227],v[220:223],a[224:227]\n"
"ds_read_b128 v[156:159],v80 offset:47104\n"
"v_mfma_f32_16x16x32_bf16 a[228:231],v[228:231],v[220:223],a[228:231]\n"
"ds_read_b64_tr_b16 v[160:161],v98 offset:0\n"
"v_mfma_f32_16x16x32_bf16 a[196:199],v[228:231],v[216:219],a[196:199]\n"
"ds_read_b64_tr_b16 v[162:163],v98 offset:1024\n"
"v_mfma_f32_16x16x32_bf16 a[200:203],v[232:235],v[216:219],a[200:203]\n"
"ds_read_b64_tr_b16 v[164:165],v99 offset:0\n"
"v_mfma_f32_16x16x32_bf16 a[232:235],v[232:235],v[220:223],a[232:235]\n"
"ds_read_b64_tr_b16 v[166:167],v99 offset:1024\n"
"v_mfma_f32_16x16x32_bf16 a[236:239],v[236:239],v[220:223],a[236:239]\n"
"ds_read_b64_tr_b16 v[168:169],v100 offset:0\n"
"v_mfma_f32_16x16x32_bf16 a[204:207],v[236:239],v[216:219],a[204:207]\n"
"ds_read_b64_tr_b16 v[170:171],v100 offset:1024\n"
"v_mfma_f32_16x16x32_bf16 a[208:211],v[240:243],v[216:219],a[208:211]\n"
"ds_read_b64_tr_b16 v[172:173],v101 offset:0\n"
"v_mfma_f32_16x16x32_bf16 a[240:243],v[240:243],v[220:223],a[240:243]\n"
"ds_read_b64_tr_b16 v[174:175],v101 offset:1024\n"
"v_mfma_f32_16x16x32_bf16 a[244:247],v[244:247],v[220:223],a[244:247]\n"
"ds_read_b64_tr_b16 v[176:177],v102 offset:0\n"
"v_mfma_f32_16x16x32_bf16 a[212:215],v[244:247],v[216:219],a[212:215]\n"
"ds_read_b64_tr_b16 v[178:179],v102 offset:1024\n"
"v_mfma_f32_16x16x32_bf16 a[216:219],v[248:251],v[216:219],a[216:219]\n"
"ds_read_b64_tr_b16 v[180:181],v103 offset:0\n"
"v_mfma_f32_16x16x32_bf16 a[248:251],v[248:251],v[220:223],a[248:251]\n"
"ds_read_b64_tr_b16 v[182:183],v103 offset:1024\n"
"v_mfma_f32_16x16x32_bf16 a[252:255],v[252:255],v[220:223],a[252:255]\n"
"ds_read_b64_tr_b16 v[184:185],v104 offset:0\n"
"v_mfma_f32_16x16x32_bf16 a[220:223],v[252:255],v[216:219],a[220:223]\n"
"ds_read_b64_tr_b16 v[186:187],v104 offset:1024\n"
"s_waitcnt lgkmcnt(10)\n"
"v_mfma_f32_16x16x32_bf16 a[192:195],v[160:163],v[152:155],a[192:195]\n"
"ds_read_b64_tr_b16 v[188:189],v105 offset:0\n"
"v_mfma_f32_16x16x32_bf16 a[224:227],v[160:163],v[156:159],a[224:227]\n"
"ds_read_b64_tr_b16 v[190:191],v105 offset:1024\n"
"v_mfma_f32_16x16x32_bf16 a[228:231],v[164:167],v[156:159],a[228:231]\n"
"ds_read_b128 v[128:131],v80 offset:32768\n"
"v_mfma_f32_16x16x32_bf16 a[196:199],v[164:167],v[152:155],a[196:199]\n"
"ds_read_b128 v[192:195],v81 offset:32768\n"
"s_waitcnt lgkmcnt(10)\n"
"v_mfma_f32_16x16x32_bf16 a[200:203],v[168:171],v[152:155],a[200:203]\n"
"ds_read_b64_tr_b16 v[224:225],v98 offset:16384\n"
"v_mfma_f32_16x16x32_bf16 a[232:235],v[168:171],v[156:159],a[232:235]\n"
"ds_read_b64_tr_b16 v[226:227],v98 offset:17408\n"
"v_mfma_f32_16x16x32_bf16 a[236:239],v[172:175],v[156:159],a[236:239]\n"
"ds_read_b64_tr_b16 v[228:229],v99 offset:16384\n"
"v_mfma_f32_16x16x32_bf16 a[204:207],v[172:175],v[152:155],a[204:207]\n"
"ds_read_b64_tr_b16 v[230:231],v99 offset:17408\n"
"s_waitcnt lgkmcnt(10)\n"
"v_mfma_f32_16x16x32_bf16 a[208:211],v[176:179],v[152:155],a[208:211]\n"
"ds_read_b64_tr_b16 v[232:233],v100 offset:16384\n"
"v_mfma_f32_16x16x32_bf16 a[240:243],v[176:179],v[156:159],a[240:243]\n"
"ds_read_b64_tr_b16 v[234:235],v100 offset:17408\n"
"v_mfma_f32_16x16x32_bf16 a[244:247],v[180:183],v[156:159],a[244:247]\n"
"ds_read_b64_tr_b16 v[236:237],v101 offset:16384\n"
"v_mfma_f32_16x16x32_bf16 a[212:215],v[180:183],v[152:155],a[212:215]\n"
"ds_read_b64_tr_b16 v[238:239],v101 offset:17408\n"
"s_waitcnt lgkmcnt(10)\n"
"v_mfma_f32_16x16x32_bf16 a[216:219],v[184:187],v[152:155],a[216:219]\n"
"ds_read_b64_tr_b16 v[240:241],v102 offset:16384\n"
"v_mfma_f32_16x16x32_bf16 a[248:251],v[184:187],v[156:159],a[248:251]\n"
"ds_read_b64_tr_b16 v[242:243],v102 offset:17408\n"
"v_mfma_f32_16x16x32_bf16 a[252:255],v[188:191],v[156:159],a[252:255]\n"
"ds_read_b64_tr_b16 v[244:245],v103 offset:16384\n"
"v_mfma_f32_16x16x32_bf16 a[220:223],v[188:191],v[152:155],a[220:223]\n"
"ds_read_b64_tr_b16 v[246:247],v103 offset:17408\n"
"s_waitcnt lgkmcnt(8)\n"
"v_mfma_f32_16x16x32_bf16 a[0:3],v[160:163],v[128:131],a[0:3]\n"
"ds_read_b64_tr_b16 v[248:249],v104 offset:16384\n"
"v_mfma_f32_16x16x32_bf16 a[0:3],v[224:227],v[192:195],a[0:3]\n"
"ds_read_b64_tr_b16 v[250:251],v104 offset:17408\n"
"v_mfma_f32_16x16x32_bf16 a[4:7],v[228:231],v[192:195],a[4:7]\n"
"ds_read_b64_tr_b16 v[252:253],v105 offset:16384\n"
"v_mfma_f32_16x16x32_bf16 a[4:7],v[164:167],v[128:131],a[4:7]\n"
"ds_read_b64_tr_b16 v[254:255],v105 offset:17408\n"
"s_waitcnt lgkmcnt(8)\n"
"v_mfma_f32_16x16x32_bf16 a[8:11],v[168:171],v[128:131],a[8:11]\n"
"ds_read_b128 v[132:135],v80 offset:34816\n"
"v_mfma_f32_16x16x32_bf16 a[8:11],v[232:235],v[192:195],a[8:11]\n"
"ds_read_b128 v[196:199],v81 offset:34816\n"
"v_mfma_f32_16x16x32_bf16 a[12:15],v[236:239],v[192:195],a[12:15]\n"
"ds_read_b128 v[136:139],v80 offset:36864\n"
"v_mfma_f32_16x16x32_bf16 a[12:15],v[172:175],v[128:131],a[12:15]\n"
"ds_read_b128 v[200:203],v81 offset:36864\n"
"s_waitcnt lgkmcnt(8)\n"
"v_mfma_f32_16x16x32_bf16 a[16:19],v[176:179],v[128:131],a[16:19]\n"
"s_and_b32 s82,s52,1\n"
"ds_read_b128 v[140:143],v80 offset:38912\n"
"s_lshl_b32 s82,s82,13\n"
"v_mfma_f32_16x16x32_bf16 a[16:19],v[240:243],v[192:195],a[16:19]\n"
"ds_read_b128 v[204:207],v81 offset:38912\n"
"v_mfma_f32_16x16x32_bf16 a[20:23],v[244:247],v[192:195],a[20:23]\n"
"ds_read_b128 v[144:147],v80 offset:40960\n"
"v_mfma_f32_16x16x32_bf16 a[20:23],v[180:183],v[128:131],a[20:23]\n"
"ds_read_b128 v[208:211],v81 offset:40960\n"
"s_waitcnt lgkmcnt(8)\n"
"v_mfma_f32_16x16x32_bf16 a[24:27],v[184:187],v[128:131],a[24:27]\n"
"ds_read_b128 v[148:151],v80 offset:43008\n"
"v_mfma_f32_16x16x32_bf16 a[24:27],v[248:251],v[192:195],a[24:27]\n"
"ds_read_b128 v[212:215],v81 offset:43008\n"
"v_subrev_u32 v98,s82,v98\n"
"v_mfma_f32_16x16x32_bf16 a[28:31],v[252:255],v[192:195],a[28:31]\n"
"ds_read_b128 v[216:219],v81 offset:45056\n"
"v_add_u32 v96,s78,v97\n"
"v_mfma_f32_16x16x32_bf16 a[28:31],v[188:191],v[128:131],a[28:31]\n"
"ds_read_b128 v[220:223],v81 offset:47104\n"
"s_waitcnt lgkmcnt(10)\n"
"v_mfma_f32_16x16x32_bf16 a[32:35],v[160:163],v[132:135],a[32:35]\n"
"v_add_u32 v97,65536,v96\n"
"v_mfma_f32_16x16x32_bf16 a[32:35],v[224:227],v[196:199],a[32:35]\n"
"v_subrev_u32 v99,s82,v99\n"
"v_mfma_f32_16x16x32_bf16 a[36:39],v[228:231],v[196:199],a[36:39]\n"
"v_mfma_f32_16x16x32_bf16 a[36:39],v[164:167],v[132:135],a[36:39]\n"
"v_mfma_f32_16x16x32_bf16 a[40:43],v[168:171],v[132:135],a[40:43]\n"
"s_waitcnt lgkmcnt(0)\n"
"s_barrier\n"
"v_mfma_f32_16x16x32_bf16 a[40:43],v[232:235],v[196:199],a[40:43]\n"
"v_subrev_u32 v100,s82,v100\n"
"s_add_u32 m0,s85,98304\n"
"v_mfma_f32_16x16x32_bf16 a[44:47],v[236:239],v[196:199],a[44:47]\n"
"buffer_load_dwordx4 v110,s[28:31],s86 offen lds\n"
"v_mfma_f32_16x16x32_bf16 a[44:47],v[172:175],v[132:135],a[44:47]\n"
"v_mfma_f32_16x16x32_bf16 a[48:51],v[176:179],v[132:135],a[48:51]\n"
"v_mfma_f32_16x16x32_bf16 a[48:51],v[240:243],v[196:199],a[48:51]\n"
"v_subrev_u32 v101,s82,v101\n"
"s_add_u32 m0,s85,99328\n"
"v_mfma_f32_16x16x32_bf16 a[52:55],v[244:247],v[196:199],a[52:55]\n"
"buffer_load_dwordx4 v111,s[28:31],s86 offen lds\n"
"v_mfma_f32_16x16x32_bf16 a[52:55],v[180:183],v[132:135],a[52:55]\n"
"s_xor_b32 s86,s86,262144\n"
"v_mfma_f32_16x16x32_bf16 a[56:59],v[184:187],v[132:135],a[56:59]\n"
"v_mfma_f32_16x16x32_bf16 a[56:59],v[248:251],v[196:199],a[56:59]\n"
"v_subrev_u32 v102,s82,v102\n"
"s_add_u32 m0,s78,32768\n"
"v_mfma_f32_16x16x32_bf16 a[60:63],v[252:255],v[196:199],a[60:63]\n"
"buffer_load_dwordx4 v112,s[24:27],s76 offen offset:0 sc1 lds\n"
"v_mfma_f32_16x16x32_bf16 a[60:63],v[188:191],v[132:135],a[60:63]\n"
"v_mfma_f32_16x16x32_bf16 a[64:67],v[160:163],v[136:139],a[64:67]\n"
"v_mfma_f32_16x16x32_bf16 a[64:67],v[224:227],v[200:203],a[64:67]\n"
"v_subrev_u32 v103,s82,v103\n"
"v_mfma_f32_16x16x32_bf16 a[68:71],v[228:231],v[200:203],a[68:71]\n"
"buffer_load_dwordx4 v112,s[24:27],s76 offen offset:1024 sc1 lds\n"
"v_mfma_f32_16x16x32_bf16 a[68:71],v[164:167],v[136:139],a[68:71]\n"
"v_mfma_f32_16x16x32_bf16 a[72:75],v[168:171],v[136:139],a[72:75]\n"
"v_accvgpr_read_b32 v116,a0\n"
"v_accvgpr_read_b32 v117,a1\n"
"v_mfma_f32_16x16x32_bf16 a[72:75],v[232:235],v[200:203],a[72:75]\n"
"v_subrev_u32 v104,s82,v104\n"
"v_accvgpr_read_b32 v118,a2\n"
"v_mfma_f32_16x16x32_bf16 a[76:79],v[236:239],v[200:203],a[76:79]\n"
"buffer_load_dwordx4 v112,s[24:27],s76 offen offset:2048 sc1 lds\n"
"v_mfma_f32_16x16x32_bf16 a[76:79],v[172:175],v[136:139],a[76:79]\n"
"v_accvgpr_read_b32 v119,a3\n"
"v_accvgpr_read_b32 v120,a4\n"
"v_mfma_f32_16x16x32_bf16 a[80:83],v[176:179],v[136:139],a[80:83]\n"
"v_accvgpr_read_b32 v121,a5\n"
"v_accvgpr_read_b32 v122,a6\n"
"v_mfma_f32_16x16x32_bf16 a[80:83],v[240:243],v[200:203],a[80:83]\n"
"v_subrev_u32 v105,s82,v105\n"
"v_accvgpr_read_b32 v123,a7\n"
"v_mfma_f32_16x16x32_bf16 a[84:87],v[244:247],v[200:203],a[84:87]\n"
"buffer_load_dwordx4 v112,s[24:27],s76 offen offset:3072 sc1 lds\n"
"v_mfma_f32_16x16x32_bf16 a[84:87],v[180:183],v[136:139],a[84:87]\n"
"s_xor_b32 s76,s76,16384\n"
"v_cvt_pk_bf16_f32 v116,v116,v117\n"
"v_cvt_pk_bf16_f32 v117,v118,v119\n"
"v_mfma_f32_16x16x32_bf16 a[88:91],v[184:187],v[136:139],a[88:91]\n"
"v_cvt_pk_bf16_f32 v118,v120,v121\n"
"v_cvt_pk_bf16_f32 v119,v122,v123\n"
"v_mfma_f32_16x16x32_bf16 a[88:91],v[248:251],v[200:203],a[88:91]\n"
"buffer_store_dwordx4 v[116:119],v112,s[24:27],s56 offen offset:0 sc1\n"
"v_mfma_f32_16x16x32_bf16 a[92:95],v[252:255],v[200:203],a[92:95]\n"
"s_nop 0\n"
"v_accvgpr_read_b32 v116,a8\n"
"v_accvgpr_read_b32 v117,a9\n"
"v_accvgpr_read_b32 v118,a10\n"
"v_mfma_f32_16x16x32_bf16 a[92:95],v[188:191],v[136:139],a[92:95]\n"
"s_waitcnt vmcnt(20)\n"
"v_accvgpr_read_b32 v119,a11\n"
"v_accvgpr_read_b32 v120,a12\n"
"s_barrier\n"
"v_mfma_f32_16x16x32_bf16 a[96:99],v[160:163],v[140:143],a[96:99]\n"
"ds_read_b128 v[0:3],v97 offset:0\n"
"v_accvgpr_read_b32 v121,a13\n"
"v_mfma_f32_16x16x32_bf16 a[96:99],v[224:227],v[204:207],a[96:99]\n"
"ds_read_b128 v[8:11],v97 offset:1024\n"
"v_accvgpr_read_b32 v122,a14\n"
"v_mfma_f32_16x16x32_bf16 a[100:103],v[228:231],v[204:207],a[100:103]\n"
"ds_read_b128 v[16:19],v97 offset:2048\n"
"v_accvgpr_read_b32 v123,a15\n"
"v_mfma_f32_16x16x32_bf16 a[100:103],v[164:167],v[140:143],a[100:103]\n"
"ds_read_b128 v[24:27],v97 offset:3072\n"
"v_cvt_pk_bf16_f32 v116,v116,v117\n"
"v_mfma_f32_16x16x32_bf16 a[104:107],v[168:171],v[140:143],a[104:107]\n"
"ds_read_b128 v[4:7],v97 offset:16384\n"
"v_cvt_pk_bf16_f32 v117,v118,v119\n"
"v_mfma_f32_16x16x32_bf16 a[104:107],v[232:235],v[204:207],a[104:107]\n"
"ds_read_b128 v[12:15],v97 offset:17408\n"
"v_cvt_pk_bf16_f32 v118,v120,v121\n"
"v_mfma_f32_16x16x32_bf16 a[108:111],v[236:239],v[204:207],a[108:111]\n"
"ds_read_b128 v[20:23],v97 offset:18432\n"
"v_cvt_pk_bf16_f32 v119,v122,v123\n"
"v_mfma_f32_16x16x32_bf16 a[108:111],v[172:175],v[140:143],a[108:111]\n"
"ds_read_b128 v[28:31],v97 offset:19456\n"
"buffer_store_dwordx4 v[116:119],v112,s[24:27],s56 offen offset:1024 sc1\n"
"v_mfma_f32_16x16x32_bf16 a[112:115],v[176:179],v[140:143],a[112:115]\n"
"ds_read_b64_tr_b16 v[32:33],v98 offset:49152\n"
"v_accvgpr_read_b32 v116,a16\n"
"v_mfma_f32_16x16x32_bf16 a[112:115],v[240:243],v[204:207],a[112:115]\n"
"ds_read_b64_tr_b16 v[34:35],v98 offset:50176\n"
"v_accvgpr_read_b32 v117,a17\n"
"v_mfma_f32_16x16x32_bf16 a[116:119],v[244:247],v[204:207],a[116:119]\n"
"ds_read_b64_tr_b16 v[36:37],v98 offset:57344\n"
"v_accvgpr_read_b32 v118,a18\n"
"v_mfma_f32_16x16x32_bf16 a[116:119],v[180:183],v[140:143],a[116:119]\n"
"ds_read_b64_tr_b16 v[38:39],v98 offset:58368\n"
"v_accvgpr_read_b32 v119,a19\n"
"v_mfma_f32_16x16x32_bf16 a[120:123],v[184:187],v[140:143],a[120:123]\n"
"ds_read_b64_tr_b16 v[40:41],v99 offset:49152\n"
"v_accvgpr_read_b32 v120,a20\n"
"v_mfma_f32_16x16x32_bf16 a[120:123],v[248:251],v[204:207],a[120:123]\n"
"ds_read_b64_tr_b16 v[42:43],v99 offset:50176\n"
"v_accvgpr_read_b32 v121,a21\n"
"v_mfma_f32_16x16x32_bf16 a[124:127],v[252:255],v[204:207],a[124:127]\n"
"ds_read_b64_tr_b16 v[44:45],v99 offset:57344\n"
"v_accvgpr_read_b32 v122,a22\n"
"v_mfma_f32_16x16x32_bf16 a[124:127],v[188:191],v[140:143],a[124:127]\n"
"ds_read_b64_tr_b16 v[46:47],v99 offset:58368\n"
"v_accvgpr_read_b32 v123,a23\n"
"v_mfma_f32_16x16x32_bf16 a[128:131],v[160:163],v[144:147],a[128:131]\n"
"ds_read_b64_tr_b16 v[48:49],v100 offset:49152\n"
"v_cvt_pk_bf16_f32 v116,v116,v117\n"
"v_mfma_f32_16x16x32_bf16 a[128:131],v[224:227],v[208:211],a[128:131]\n"
"ds_read_b64_tr_b16 v[50:51],v100 offset:50176\n"
"v_cvt_pk_bf16_f32 v117,v118,v119\n"
"v_mfma_f32_16x16x32_bf16 a[132:135],v[228:231],v[208:211],a[132:135]\n"
"ds_read_b64_tr_b16 v[52:53],v100 offset:57344\n"
"v_cvt_pk_bf16_f32 v118,v120,v121\n"
"v_mfma_f32_16x16x32_bf16 a[132:135],v[164:167],v[144:147],a[132:135]\n"
"ds_read_b64_tr_b16 v[54:55],v100 offset:58368\n"
"v_cvt_pk_bf16_f32 v119,v122,v123\n"
"v_mfma_f32_16x16x32_bf16 a[136:139],v[168:171],v[144:147],a[136:139]\n"
"ds_read_b64_tr_b16 v[56:57],v101 offset:49152\n"
"buffer_store_dwordx4 v[116:119],v112,s[24:27],s56 offen offset:2048 sc1\n"
"v_mfma_f32_16x16x32_bf16 a[136:139],v[232:235],v[208:211],a[136:139]\n"
"ds_read_b64_tr_b16 v[58:59],v101 offset:50176\n"
"v_accvgpr_read_b32 v116,a24\n"
"v_mfma_f32_16x16x32_bf16 a[140:143],v[236:239],v[208:211],a[140:143]\n"
"ds_read_b64_tr_b16 v[60:61],v101 offset:57344\n"
"v_accvgpr_read_b32 v117,a25\n"
"v_mfma_f32_16x16x32_bf16 a[140:143],v[172:175],v[144:147],a[140:143]\n"
"ds_read_b64_tr_b16 v[62:63],v101 offset:58368\n"
"v_accvgpr_read_b32 v118,a26\n"
"v_mfma_f32_16x16x32_bf16 a[144:147],v[176:179],v[144:147],a[144:147]\n"
"ds_read_b64_tr_b16 v[64:65],v102 offset:49152\n"
"v_accvgpr_read_b32 v119,a27\n"
"v_mfma_f32_16x16x32_bf16 a[144:147],v[240:243],v[208:211],a[144:147]\n"
"ds_read_b64_tr_b16 v[66:67],v102 offset:50176\n"
"v_accvgpr_read_b32 v120,a28\n"
"v_mfma_f32_16x16x32_bf16 a[148:151],v[244:247],v[208:211],a[148:151]\n"
"ds_read_b64_tr_b16 v[68:69],v102 offset:57344\n"
"v_accvgpr_read_b32 v121,a29\n"
"v_mfma_f32_16x16x32_bf16 a[148:151],v[180:183],v[144:147],a[148:151]\n"
"ds_read_b64_tr_b16 v[70:71],v102 offset:58368\n"
"v_accvgpr_read_b32 v122,a30\n"
"v_mfma_f32_16x16x32_bf16 a[152:155],v[184:187],v[144:147],a[152:155]\n"
"ds_read_b64_tr_b16 v[72:73],v103 offset:49152\n"
"v_accvgpr_read_b32 v123,a31\n"
"v_mfma_f32_16x16x32_bf16 a[152:155],v[248:251],v[208:211],a[152:155]\n"
"ds_read_b64_tr_b16 v[74:75],v103 offset:50176\n"
"v_cvt_pk_bf16_f32 v116,v116,v117\n"
"v_mfma_f32_16x16x32_bf16 a[156:159],v[252:255],v[208:211],a[156:159]\n"
"ds_read_b64_tr_b16 v[76:77],v103 offset:57344\n"
"v_cvt_pk_bf16_f32 v117,v118,v119\n"
"v_mfma_f32_16x16x32_bf16 a[156:159],v[188:191],v[144:147],a[156:159]\n"
"ds_read_b64_tr_b16 v[78:79],v103 offset:58368\n"
"v_cvt_pk_bf16_f32 v118,v120,v121\n"
"v_mfma_f32_16x16x32_bf16 a[160:163],v[160:163],v[148:151],a[160:163]\n"
"ds_read_b64_tr_b16 v[80:81],v104 offset:49152\n"
"v_cvt_pk_bf16_f32 v119,v122,v123\n"
"v_mfma_f32_16x16x32_bf16 a[160:163],v[224:227],v[212:215],a[160:163]\n"
"ds_read_b64_tr_b16 v[82:83],v104 offset:50176\n"
"buffer_store_dwordx4 v[116:119],v112,s[24:27],s56 offen offset:3072 sc1\n"
"v_mfma_f32_16x16x32_bf16 a[164:167],v[228:231],v[212:215],a[164:167]\n"
"ds_read_b64_tr_b16 v[84:85],v104 offset:57344\n"
"v_mfma_f32_16x16x32_bf16 a[164:167],v[164:167],v[148:151],a[164:167]\n"
"ds_read_b64_tr_b16 v[86:87],v104 offset:58368\n"
"v_mfma_f32_16x16x32_bf16 a[168:171],v[168:171],v[148:151],a[168:171]\n"
"ds_read_b64_tr_b16 v[88:89],v105 offset:49152\n"
"v_mfma_f32_16x16x32_bf16 a[168:171],v[232:235],v[212:215],a[168:171]\n"
"ds_read_b64_tr_b16 v[90:91],v105 offset:50176\n"
"v_mfma_f32_16x16x32_bf16 a[172:175],v[236:239],v[212:215],a[172:175]\n"
"ds_read_b64_tr_b16 v[92:93],v105 offset:57344\n"
"v_mfma_f32_16x16x32_bf16 a[172:175],v[172:175],v[148:151],a[172:175]\n"
"ds_read_b64_tr_b16 v[94:95],v105 offset:58368\n"
"v_mfma_f32_16x16x32_bf16 a[176:179],v[176:179],v[148:151],a[176:179]\n"
"s_add_u32 m0,s85,106496\n"
"v_mfma_f32_16x16x32_bf16 a[176:179],v[240:243],v[212:215],a[176:179]\n"
"buffer_load_dwordx4 v110,s[28:31],s86 offen lds\n"
"v_mfma_f32_16x16x32_bf16 a[180:183],v[244:247],v[212:215],a[180:183]\n"
"v_mfma_f32_16x16x32_bf16 a[180:183],v[180:183],v[148:151],a[180:183]\n"
"s_add_u32 m0,s85,107520\n"
"v_mfma_f32_16x16x32_bf16 a[184:187],v[184:187],v[148:151],a[184:187]\n"
"buffer_load_dwordx4 v111,s[28:31],s86 offen lds\n"
"v_mfma_f32_16x16x32_bf16 a[184:187],v[248:251],v[212:215],a[184:187]\n"
"s_xor_b32 s86,s86,786432\n"
"v_mfma_f32_16x16x32_bf16 a[188:191],v[252:255],v[212:215],a[188:191]\n"
"s_add_u32 m0,s78,49152\n"
"v_mfma_f32_16x16x32_bf16 a[188:191],v[188:191],v[148:151],a[188:191]\n"
"buffer_load_dwordx4 v112,s[24:27],s76 offen offset:0 sc1 lds\n"
"v_mfma_f32_16x16x32_bf16 a[192:195],v[224:227],v[216:219],a[192:195]\n"
"v_mfma_f32_16x16x32_bf16 a[224:227],v[224:227],v[220:223],a[224:227]\n"
"v_mfma_f32_16x16x32_bf16 a[228:231],v[228:231],v[220:223],a[228:231]\n"
"buffer_load_dwordx4 v112,s[24:27],s76 offen offset:1024 sc1 lds\n"
"v_mfma_f32_16x16x32_bf16 a[196:199],v[228:231],v[216:219],a[196:199]\n"
"v_mfma_f32_16x16x32_bf16 a[200:203],v[232:235],v[216:219],a[200:203]\n"
"v_mfma_f32_16x16x32_bf16 a[232:235],v[232:235],v[220:223],a[232:235]\n"
"buffer_load_dwordx4 v112,s[24:27],s76 offen offset:2048 sc1 lds\n"
"v_mfma_f32_16x16x32_bf16 a[236:239],v[236:239],v[220:223],a[236:239]\n"
"v_mfma_f32_16x16x32_bf16 a[204:207],v[236:239],v[216:219],a[204:207]\n"
"buffer_load_dwordx4 v112,s[24:27],s76 offen offset:3072 sc1 lds\n"
"v_mfma_f32_16x16x32_bf16 a[208:211],v[240:243],v[216:219],a[208:211]\n"
"s_xor_b32 s76,s76,49152\n"
"v_mfma_f32_16x16x32_bf16 a[240:243],v[240:243],v[220:223],a[240:243]\n"
"v_mfma_f32_16x16x32_bf16 a[244:247],v[244:247],v[220:223],a[244:247]\n"
"v_mfma_f32_16x16x32_bf16 a[212:215],v[244:247],v[216:219],a[212:215]\n"
"v_mfma_f32_16x16x32_bf16 a[216:219],v[248:251],v[216:219],a[216:219]\n"
"v_mfma_f32_16x16x32_bf16 a[248:251],v[248:251],v[220:223],a[248:251]\n"
"v_mfma_f32_16x16x32_bf16 a[252:255],v[252:255],v[220:223],a[252:255]\n"
"v_mfma_f32_16x16x32_bf16 a[220:223],v[252:255],v[216:219],a[220:223]\n"
"s_branch .Lfk_ph2\n"
".Lfk_v:\n"
"s_waitcnt vmcnt(11)\n"
"s_barrier\n"
"ds_read_b128 v[152:155],v80 offset:12288\n"
"ds_read_b128 v[156:159],v80 offset:14336\n"
"ds_read_b64_tr_b16 v[160:161],v98 offset:49152\n"
"ds_read_b64_tr_b16 v[162:163],v98 offset:50176\n"
"ds_read_b64_tr_b16 v[164:165],v99 offset:49152\n"
"ds_read_b64_tr_b16 v[166:167],v99 offset:50176\n"
"ds_read_b64_tr_b16 v[168:169],v100 offset:49152\n"
"ds_read_b64_tr_b16 v[170:171],v100 offset:50176\n"
"ds_read_b64_tr_b16 v[172:173],v101 offset:49152\n"
"ds_read_b64_tr_b16 v[174:175],v101 offset:50176\n"
"ds_read_b64_tr_b16 v[176:177],v102 offset:49152\n"
"ds_read_b64_tr_b16 v[178:179],v102 offset:50176\n"
"ds_read_b64_tr_b16 v[180:181],v103 offset:49152\n"
"ds_read_b64_tr_b16 v[182:183],v103 offset:50176\n"
"ds_read_b64_tr_b16 v[184:185],v104 offset:49152\n"
"ds_read_b64_tr_b16 v[186:187],v104 offset:50176\n"
"ds_read_b64_tr_b16 v[188:189],v105 offset:49152\n"
"ds_read_b64_tr_b16 v[190:191],v105 offset:50176\n"
"s_waitcnt lgkmcnt(12)\n"
"v_mfma_f32_16x16x32_bf16 a[192:195],v[152:155],v[160:163],0\n"
"s_and_b32 s91,s89,7\n"
"s_mov_b32 s76,128\n"
"s_add_u32 m0,s79,32768\n"
"v_mfma_f32_16x16x32_bf16 a[196:199],v[156:159],v[160:163],0\n"
"buffer_load_dwordx4 v106,s[16:19],s76 offen offset:0 lds\n"
"v_mfma_f32_16x16x32_bf16 a[204:207],v[156:159],v[164:167],0\n"
"s_lshr_b32 s97,s89,3\n"
"s_lshl2_add_u32 s90,s88,s97\n"
"v_mfma_f32_16x16x32_bf16 a[200:203],v[152:155],v[164:167],0\n"
"buffer_load_dwordx4 v107,s[16:19],s76 offen offset:1024 lds\n"
"s_waitcnt lgkmcnt(8)\n"
"v_mfma_f32_16x16x32_bf16 a[208:211],v[152:155],v[168:171],0\n"
"s_lshl_b32 vcc_lo,s90,8\n"
"s_add_u32 s28,s28,vcc_lo\n"
"s_addc_u32 s29,s29,0\n"
"v_mfma_f32_16x16x32_bf16 a[212:215],v[156:159],v[168:171],0\n"
"buffer_load_dwordx4 v108,s[16:19],s76 offen offset:2048 lds\n"
"v_mfma_f32_16x16x32_bf16 a[220:223],v[156:159],v[172:175],0\n"
"s_sub_u32 s30,8388608,vcc_lo\n"
"s_mov_b32 s31,0x20000\n"
"buffer_load_dwordx4 v109,s[16:19],s76 offen offset:3072 lds\n"
"v_mfma_f32_16x16x32_bf16 a[216:219],v[152:155],v[172:175],0\n"
"s_waitcnt vmcnt(9)\n"
"s_barrier\n"
"s_waitcnt lgkmcnt(4)\n"
"v_mfma_f32_16x16x32_bf16 a[224:227],v[152:155],v[176:179],0\n"
"ds_read_b128 v[128:131],v80 offset:0\n"
"ds_read_b128 v[192:195],v81 offset:0\n"
"v_mfma_f32_16x16x32_bf16 a[228:231],v[156:159],v[176:179],0\n"
"ds_read_b64_tr_b16 v[224:225],v98 offset:32768\n"
"ds_read_b64_tr_b16 v[226:227],v98 offset:33792\n"
"v_mfma_f32_16x16x32_bf16 a[236:239],v[156:159],v[180:183],0\n"
"ds_read_b64_tr_b16 v[228:229],v99 offset:32768\n"
"ds_read_b64_tr_b16 v[230:231],v99 offset:33792\n"
"v_mfma_f32_16x16x32_bf16 a[232:235],v[152:155],v[180:183],0\n"
"ds_read_b64_tr_b16 v[232:233],v100 offset:32768\n"
"ds_read_b64_tr_b16 v[234:235],v100 offset:33792\n"
"s_waitcnt lgkmcnt(8)\n"
"v_mfma_f32_16x16x32_bf16 a[240:243],v[152:155],v[184:187],0\n"
"ds_read_b64_tr_b16 v[236:237],v101 offset:32768\n"
"ds_read_b64_tr_b16 v[238:239],v101 offset:33792\n"
"v_mfma_f32_16x16x32_bf16 a[244:247],v[156:159],v[184:187],0\n"
"ds_read_b64_tr_b16 v[240:241],v102 offset:32768\n"
"ds_read_b64_tr_b16 v[242:243],v102 offset:33792\n"
"v_mfma_f32_16x16x32_bf16 a[252:255],v[156:159],v[188:191],0\n"
"ds_read_b64_tr_b16 v[244:245],v103 offset:32768\n"
"ds_read_b64_tr_b16 v[246:247],v103 offset:33792\n"
"v_mfma_f32_16x16x32_bf16 a[248:251],v[152:155],v[188:191],0\n"
"ds_read_b64_tr_b16 v[248:249],v104 offset:32768\n"
"ds_read_b64_tr_b16 v[250:251],v104 offset:33792\n"
"s_waitcnt lgkmcnt(10)\n"
"v_mfma_f32_16x16x32_bf16 a[0:3],v[128:131],v[160:163],0\n"
"ds_read_b64_tr_b16 v[252:253],v105 offset:32768\n"
"ds_read_b64_tr_b16 v[254:255],v105 offset:33792\n"
"v_mfma_f32_16x16x32_bf16 a[0:3],v[192:195],v[224:227],a[0:3]\n"
"ds_read_b128 v[132:135],v80 offset:2048\n"
"ds_read_b128 v[196:199],v81 offset:2048\n"
"v_mfma_f32_16x16x32_bf16 a[8:11],v[192:195],v[228:231],0\n"
"ds_read_b128 v[216:219],v81 offset:12288\n"
"ds_read_b128 v[220:223],v81 offset:14336\n"
"v_mfma_f32_16x16x32_bf16 a[8:11],v[128:131],v[164:167],a[8:11]\n"
"s_waitcnt lgkmcnt(12)\n"
"s_mov_b32 s76,262272\n"
"s_add_u32 m0,s79,36864\n"
"v_mfma_f32_16x16x32_bf16 a[16:19],v[128:131],v[168:171],0\n"
"buffer_load_dwordx4 v106,s[16:19],s76 offen offset:0 lds\n"
"v_mfma_f32_16x16x32_bf16 a[16:19],v[192:195],v[232:235],a[16:19]\n"
"buffer_load_dwordx4 v107,s[16:19],s76 offen offset:1024 lds\n"
"v_mfma_f32_16x16x32_bf16 a[24:27],v[192:195],v[236:239],0\n"
"buffer_load_dwordx4 v108,s[16:19],s76 offen offset:2048 lds\n"
"buffer_load_dwordx4 v109,s[16:19],s76 offen offset:3072 lds\n"
"v_mfma_f32_16x16x32_bf16 a[24:27],v[128:131],v[172:175],a[24:27]\n"
"s_waitcnt lgkmcnt(8)\n"
"s_mov_b32 s77,524288\n"
"s_add_u32 m0,s78,98304\n"
"v_mfma_f32_16x16x32_bf16 a[32:35],v[128:131],v[176:179],0\n"
"buffer_load_dwordx4 v110,s[20:23],s77 offen offset:0 lds\n"
"v_mfma_f32_16x16x32_bf16 a[32:35],v[192:195],v[240:243],a[32:35]\n"
"buffer_load_dwordx4 v111,s[20:23],s77 offen offset:1024 lds\n"
"v_mfma_f32_16x16x32_bf16 a[40:43],v[192:195],v[244:247],0\n"
"buffer_load_dwordx4 v113,s[20:23],s77 offen offset:2048 lds\n"
"buffer_load_dwordx4 v114,s[20:23],s77 offen offset:3072 lds\n"
"v_mfma_f32_16x16x32_bf16 a[40:43],v[128:131],v[180:183],a[40:43]\n"
"s_waitcnt lgkmcnt(4)\n"
"s_mov_b32 s77,786432\n"
"s_add_u32 m0,s78,114688\n"
"v_mfma_f32_16x16x32_bf16 a[48:51],v[128:131],v[184:187],0\n"
"buffer_load_dwordx4 v110,s[20:23],s77 offen offset:0 lds\n"
"v_mfma_f32_16x16x32_bf16 a[48:51],v[192:195],v[248:251],a[48:51]\n"
"buffer_load_dwordx4 v111,s[20:23],s77 offen offset:1024 lds\n"
"v_mfma_f32_16x16x32_bf16 a[56:59],v[192:195],v[252:255],0\n"
"buffer_load_dwordx4 v113,s[20:23],s77 offen offset:2048 lds\n"
"buffer_load_dwordx4 v114,s[20:23],s77 offen offset:3072 lds\n"
"v_mfma_f32_16x16x32_bf16 a[56:59],v[128:131],v[188:191],a[56:59]\n"
"s_waitcnt lgkmcnt(2)\n"
"s_mov_b32 s76,256\n"
"s_add_u32 m0,s79,65536\n"
"v_mfma_f32_16x16x32_bf16 a[4:7],v[132:135],v[160:163],0\n"
"buffer_load_dwordx4 v106,s[16:19],s76 offen offset:0 lds\n"
"v_mfma_f32_16x16x32_bf16 a[4:7],v[196:199],v[224:227],a[4:7]\n"
"buffer_load_dwordx4 v107,s[16:19],s76 offen offset:1024 lds\n"
"v_mfma_f32_16x16x32_bf16 a[12:15],v[196:199],v[228:231],0\n"
"buffer_load_dwordx4 v108,s[16:19],s76 offen offset:2048 lds\n"
"buffer_load_dwordx4 v109,s[16:19],s76 offen offset:3072 lds\n"
"v_mfma_f32_16x16x32_bf16 a[12:15],v[132:135],v[164:167],a[12:15]\n"
"s_waitcnt vmcnt(21)\n"
"s_barrier\n"
"v_mfma_f32_16x16x32_bf16 a[20:23],v[132:135],v[168:171],0\n"
"ds_read_b128 v[136:139],v80 offset:4096\n"
"ds_read_b128 v[200:203],v81 offset:4096\n"
"v_mfma_f32_16x16x32_bf16 a[20:23],v[196:199],v[232:235],a[20:23]\n"
"ds_read_b128 v[140:143],v80 offset:6144\n"
"ds_read_b128 v[204:207],v81 offset:6144\n"
"v_mfma_f32_16x16x32_bf16 a[28:31],v[196:199],v[236:239],0\n"
"ds_read_b128 v[144:147],v80 offset:8192\n"
"ds_read_b128 v[208:211],v81 offset:8192\n"
"v_mfma_f32_16x16x32_bf16 a[28:31],v[132:135],v[172:175],a[28:31]\n"
"ds_read_b128 v[148:151],v80 offset:10240\n"
"ds_read_b128 v[212:215],v81 offset:10240\n"
"v_mfma_f32_16x16x32_bf16 a[36:39],v[132:135],v[176:179],0\n"
"s_mov_b32 s60,s91\n"
"s_mov_b32 s76,262400\n"
"s_add_u32 m0,s79,69632\n"
"v_mfma_f32_16x16x32_bf16 a[36:39],v[196:199],v[240:243],a[36:39]\n"
"buffer_load_dwordx4 v106,s[16:19],s76 offen offset:0 lds\n"
"v_mfma_f32_16x16x32_bf16 a[44:47],v[196:199],v[244:247],0\n"
"buffer_load_dwordx4 v107,s[16:19],s76 offen offset:1024 lds\n"
"v_mfma_f32_16x16x32_bf16 a[44:47],v[132:135],v[180:183],a[44:47]\n"
"buffer_load_dwordx4 v108,s[16:19],s76 offen offset:2048 lds\n"
"v_mfma_f32_16x16x32_bf16 a[52:55],v[132:135],v[184:187],0\n"
"buffer_load_dwordx4 v109,s[16:19],s76 offen offset:3072 lds\n"
"v_mfma_f32_16x16x32_bf16 a[52:55],v[196:199],v[248:251],a[52:55]\n"
"s_sub_u32 s94,15,s91\n"
"s_waitcnt lgkmcnt(0)\n"
"s_barrier\n"
"v_mfma_f32_16x16x32_bf16 a[60:63],v[196:199],v[252:255],0\n"
"s_cmp_lt_u32 s52,2\n"
"s_cselect_b32 s95,s94,s91\n"
"s_mov_b32 s77,1048576\n"
"s_add_u32 m0,s78,147456\n"
"v_mfma_f32_16x16x32_bf16 a[60:63],v[132:135],v[188:191],a[60:63]\n"
"buffer_load_dwordx4 v110,s[20:23],s77 offen offset:0 lds\n"
"v_mfma_f32_16x16x32_bf16 a[64:67],v[136:139],v[160:163],0\n"
"s_lshr_b32 vcc_lo,s95,1\n"
"s_lshl_b32 s55,vcc_lo,19\n"
"v_mfma_f32_16x16x32_bf16 a[64:67],v[200:203],v[224:227],a[64:67]\n"
"s_lshl_b32 s58,vcc_lo,10\n"
"s_add_u32 s58,s58,148898816\n"
"v_mfma_f32_16x16x32_bf16 a[72:75],v[200:203],v[228:231],0\n"
"s_and_b32 vcc_lo,s95,1\n"
"s_lshl_b32 vcc_lo,vcc_lo,13\n"
"v_mfma_f32_16x16x32_bf16 a[72:75],v[136:139],v[164:167],a[72:75]\n"
"buffer_load_dwordx4 v111,s[20:23],s77 offen offset:1024 lds\n"
"v_mfma_f32_16x16x32_bf16 a[80:83],v[136:139],v[168:171],0\n"
"s_and_b32 vcc_hi,s78,4096\n"
"s_add_u32 s55,s55,vcc_lo\n"
"v_mfma_f32_16x16x32_bf16 a[80:83],v[200:203],v[232:235],a[80:83]\n"
"s_add_u32 s55,s55,vcc_hi\n"
"s_and_b32 vcc_lo,s89,2\n"
"v_mfma_f32_16x16x32_bf16 a[88:91],v[200:203],v[236:239],0\n"
"s_lshl2_add_u32 vcc_lo,s88,vcc_lo\n"
"s_and_b32 vcc_hi,s52,1\n"
"v_mfma_f32_16x16x32_bf16 a[88:91],v[136:139],v[172:175],a[88:91]\n"
"buffer_load_dwordx4 v113,s[20:23],s77 offen offset:2048 lds\n"
"v_mfma_f32_16x16x32_bf16 a[96:99],v[136:139],v[176:179],0\n"
"s_add_u32 vcc_lo,vcc_lo,vcc_hi\n"
"s_lshr_b32 vcc_hi,s89,2\n"
"v_mfma_f32_16x16x32_bf16 a[96:99],v[200:203],v[240:243],a[96:99]\n"
"s_lshr_b32 s84,s52,1\n"
"s_lshl1_add_u32 vcc_hi,vcc_hi,s84\n"
"v_mfma_f32_16x16x32_bf16 a[104:107],v[200:203],v[244:247],0\n"
"s_mul_i32 s56,vcc_lo,1179648\n"
"s_lshl_b32 s84,s84,19\n"
"v_mfma_f32_16x16x32_bf16 a[104:107],v[136:139],v[180:183],a[104:107]\n"
"buffer_load_dwordx4 v114,s[20:23],s77 offen offset:3072 lds\n"
"v_mfma_f32_16x16x32_bf16 a[112:115],v[136:139],v[184:187],0\n"
"s_add_u32 s56,s56,s84\n"
"s_lshr_b32 s84,s89,2\n"
"v_mfma_f32_16x16x32_bf16 a[112:115],v[200:203],v[248:251],a[112:115]\n"
"s_lshl_b32 s84,s84,14\n"
"s_add_u32 s56,s56,s84\n"
"v_mfma_f32_16x16x32_bf16 a[120:123],v[200:203],v[252:255],0\n"
"s_lshl_b32 s84,s53,13\n"
"s_mov_b32 s77,1310720\n"
"s_add_u32 m0,s78,131072\n"
"v_mfma_f32_16x16x32_bf16 a[120:123],v[136:139],v[188:191],a[120:123]\n"
"buffer_load_dwordx4 v110,s[20:23],s77 offen offset:0 lds\n"
"v_mfma_f32_16x16x32_bf16 a[68:71],v[140:143],v[160:163],0\n"
"s_add_u32 s56,s56,s84\n"
"s_add_u32 s56,s56,4194304\n"
"v_mfma_f32_16x16x32_bf16 a[68:71],v[204:207],v[224:227],a[68:71]\n"
"s_lshl4_add_u32 vcc_hi,s53,vcc_hi\n"
"s_lshl_b32 vcc_lo,vcc_lo,8\n"
"v_mfma_f32_16x16x32_bf16 a[76:79],v[204:207],v[228:231],0\n"
"buffer_load_dwordx4 v111,s[20:23],s77 offen offset:1024 lds\n"
"v_mfma_f32_16x16x32_bf16 a[76:79],v[140:143],v[164:167],a[76:79]\n"
"s_lshl3_add_u32 s59,vcc_hi,vcc_lo\n"
"s_add_u32 s59,s59,148914176\n"
"v_mfma_f32_16x16x32_bf16 a[84:87],v[140:143],v[168:171],0\n"
"s_lshl_b32 s57,s92,3\n"
"s_add_u32 s57,s57,148898816\n"
"v_mfma_f32_16x16x32_bf16 a[84:87],v[204:207],v[232:235],a[84:87]\n"
"s_lshl_b32 s63,s90,8\n"
"s_add_u32 s63,s63,148914176\n"
"v_mfma_f32_16x16x32_bf16 a[92:95],v[204:207],v[236:239],0\n"
"buffer_load_dwordx4 v113,s[20:23],s77 offen offset:2048 lds\n"
"v_mfma_f32_16x16x32_bf16 a[92:95],v[140:143],v[172:175],a[92:95]\n"
"s_lshl_b32 s74,s92,11\n"
"s_add_u32 s74,s74,171966464\n"
"v_mfma_f32_16x16x32_bf16 a[100:103],v[140:143],v[176:179],0\n"
"s_mul_i32 s96,s90,1179648\n"
"s_add_u32 s96,s96,4194304\n"
"v_mfma_f32_16x16x32_bf16 a[100:103],v[204:207],v[240:243],a[100:103]\n"
"s_add_u32 s73,s96,s85\n"
"s_and_b32 vcc_hi,s78,4096\n"
"v_mfma_f32_16x16x32_bf16 a[108:111],v[204:207],v[244:247],0\n"
"buffer_load_dwordx4 v114,s[20:23],s77 offen offset:3072 lds\n"
"v_mfma_f32_16x16x32_bf16 a[108:111],v[140:143],v[180:183],a[108:111]\n"
"s_lshl_b32 vcc_hi,vcc_hi,1\n"
"s_add_u32 vcc_lo,s96,vcc_hi\n"
"v_mfma_f32_16x16x32_bf16 a[116:119],v[140:143],v[184:187],0\n"
"s_add_u32 s65,vcc_lo,524288\n"
"s_lshl_b32 s66,s91,16\n"
"v_mfma_f32_16x16x32_bf16 a[116:119],v[204:207],v[248:251],a[116:119]\n"
"s_sub_u32 s66,vcc_lo,s66\n"
"s_mov_b32 s76,384\n"
"s_add_u32 m0,s79,0\n"
"v_mfma_f32_16x16x32_bf16 a[124:127],v[204:207],v[252:255],0\n"
"buffer_load_dwordx4 v106,s[16:19],s76 offen offset:0 lds\n"
"v_mfma_f32_16x16x32_bf16 a[124:127],v[140:143],v[188:191],a[124:127]\n"
"s_add_u32 s66,s66,999424\n"
"s_add_u32 s67,vcc_hi,65536\n"
"v_mfma_f32_16x16x32_bf16 a[128:131],v[144:147],v[160:163],0\n"
"s_mul_i32 vcc_lo,vcc_hi,5\n"
"s_sub_u32 vcc_lo,s96,vcc_lo\n"
"v_mfma_f32_16x16x32_bf16 a[128:131],v[208:211],v[224:227],a[128:131]\n"
"s_add_u32 s64,vcc_lo,81920\n"
"s_add_u32 s62,vcc_lo,135168\n"
"s_add_u32 s61,vcc_lo,524288\n"
"v_mfma_f32_16x16x32_bf16 a[136:139],v[208:211],v[228:231],0\n"
"buffer_load_dwordx4 v107,s[16:19],s76 offen offset:1024 lds\n"
"v_mfma_f32_16x16x32_bf16 a[136:139],v[144:147],v[164:167],a[136:139]\n"
"s_cmp_lt_u32 s52,2\n"
"s_cselect_b32 s64,s64,2146435072\n"
"s_cselect_b32 s67,s67,163840\n"
"s_cselect_b32 s65,s65,1\n"
"v_mfma_f32_16x16x32_bf16 a[144:147],v[144:147],v[168:171],0\n"
"s_cselect_b32 vcc_lo,4,0\n"
"s_and_b32 vcc_hi,s52,1\n"
"s_add_u32 vcc_lo,vcc_lo,vcc_hi\n"
"v_mfma_f32_16x16x32_bf16 a[144:147],v[208:211],v[232:235],a[144:147]\n"
"s_lshl_b32 vcc_hi,s90,12\n"
"s_add_u32 vcc_hi,vcc_hi,54525952\n"
"v_mfma_f32_16x16x32_bf16 a[152:155],v[208:211],v[236:239],0\n"
"buffer_load_dwordx4 v108,s[16:19],s76 offen offset:2048 lds\n"
"v_mfma_f32_16x16x32_bf16 a[152:155],v[144:147],v[172:175],a[152:155]\n"
"s_lshl3_add_u32 s70,s95,vcc_lo\n"
"s_lshl_b32 s70,s70,17\n"
"v_mfma_f32_16x16x32_bf16 a[160:163],v[144:147],v[176:179],0\n"
"s_add_u32 s70,s70,vcc_hi\n"
"s_lshl3_add_u32 s68,s94,vcc_lo\n"
"v_mfma_f32_16x16x32_bf16 a[160:163],v[208:211],v[240:243],a[160:163]\n"
"s_lshl_b32 s68,s68,17\n"
"s_add_u32 s68,s68,vcc_hi\n"
"v_mfma_f32_16x16x32_bf16 a[168:171],v[208:211],v[244:247],0\n"
"buffer_load_dwordx4 v109,s[16:19],s76 offen offset:3072 lds\n"
"v_mfma_f32_16x16x32_bf16 a[168:171],v[144:147],v[180:183],a[168:171]\n"
"s_lshr_b32 vcc_lo,s90,3\n"
"s_lshl2_add_u32 vcc_lo,vcc_lo,s52\n"
"v_mfma_f32_16x16x32_bf16 a[176:179],v[144:147],v[184:187],0\n"
"s_lshl_b32 vcc_lo,vcc_lo,7\n"
"s_and_b32 vcc_hi,s90,7\n"
"v_mfma_f32_16x16x32_bf16 a[176:179],v[208:211],v[248:251],a[176:179]\n"
"s_add_u32 vcc_lo,vcc_lo,vcc_hi\n"
"s_mov_b32 s76,262528\n"
"s_add_u32 m0,s79,4096\n"
"v_mfma_f32_16x16x32_bf16 a[184:187],v[208:211],v[252:255],0\n"
"buffer_load_dwordx4 v106,s[16:19],s76 offen offset:0 lds\n"
"v_mfma_f32_16x16x32_bf16 a[184:187],v[144:147],v[188:191],a[184:187]\n"
"s_lshl3_add_u32 s69,s91,vcc_lo\n"
"s_lshl_b32 s69,s69,3\n"
"v_mfma_f32_16x16x32_bf16 a[132:135],v[148:151],v[160:163],0\n"
"s_add_u32 s69,s69,148930560\n"
"s_lshl3_add_u32 s71,s94,vcc_lo\n"
"v_mfma_f32_16x16x32_bf16 a[132:135],v[212:215],v[224:227],a[132:135]\n"
"s_lshl_b32 s71,s71,3\n"
"s_add_u32 s71,s71,148930560\n"
"v_mfma_f32_16x16x32_bf16 a[140:143],v[212:215],v[228:231],0\n"
"buffer_load_dwordx4 v107,s[16:19],s76 offen offset:1024 lds\n"
"v_mfma_f32_16x16x32_bf16 a[140:143],v[148:151],v[164:167],a[140:143]\n"
"s_mov_b32 s6,8388608\n"
"s_mov_b32 s7,0x20000\n"
"v_mfma_f32_16x16x32_bf16 a[148:151],v[148:151],v[168:171],0\n"
"s_lshr_b32 s94,s88,1\n"
"s_and_b32 vcc_lo,s91,1\n"
"v_mfma_f32_16x16x32_bf16 a[148:151],v[212:215],v[232:235],a[148:151]\n"
"s_lshl1_add_u32 s95,s97,vcc_lo\n"
"s_and_b32 vcc_lo,s88,1\n"
"v_mfma_f32_16x16x32_bf16 a[156:159],v[212:215],v[236:239],0\n"
"buffer_load_dwordx4 v108,s[16:19],s76 offen offset:2048 lds\n"
"v_mfma_f32_16x16x32_bf16 a[156:159],v[148:151],v[172:175],a[156:159]\n"
"s_lshr_b32 vcc_hi,s91,1\n"
"s_lshl2_add_u32 s96,vcc_lo,vcc_hi\n"
"v_mfma_f32_16x16x32_bf16 a[164:167],v[148:151],v[176:179],0\n"
"s_lshr_b32 vcc_lo,s88,2\n"
"s_lshl2_add_u32 s96,vcc_lo,vcc_hi\n"
"v_mfma_f32_16x16x32_bf16 a[164:167],v[212:215],v[240:243],a[164:167]\n"
"s_sub_u32 vcc_lo,7,s88\n"
"s_min_u32 s94,vcc_lo,s88\n"
"v_mfma_f32_16x16x32_bf16 a[172:175],v[212:215],v[244:247],0\n"
"buffer_load_dwordx4 v109,s[16:19],s76 offen offset:3072 lds\n"
"v_mfma_f32_16x16x32_bf16 a[172:175],v[148:151],v[180:183],a[172:175]\n"
"s_and_b32 s96,s88,4\n"
"s_add_u32 s96,s96,s97\n"
"s_add_u32 s95,s91,s96\n"
"v_mfma_f32_16x16x32_bf16 a[180:183],v[148:151],v[184:187],0\n"
"buffer_load_dwordx4 v[8:11],v70,s[32:35],0 offen offset:0\n"
"v_mfma_f32_16x16x32_bf16 a[180:183],v[212:215],v[248:251],a[180:183]\n"
"buffer_load_dwordx4 v[12:15],v70,s[32:35],0 offen offset:512\n"
"s_and_b32 s95,s95,7\n"
"v_mfma_f32_16x16x32_bf16 a[188:191],v[212:215],v[252:255],0\n"
"buffer_load_dwordx4 v[16:19],v70,s[32:35],0 offen offset:1024\n"
"buffer_load_dwordx4 v[20:23],v70,s[32:35],0 offen offset:1536\n"
"s_lshl3_add_u32 vcc_lo,s94,s96\n"
"v_mfma_f32_16x16x32_bf16 a[188:191],v[148:151],v[188:191],a[188:191]\n"
"s_waitcnt vmcnt(28)\n"
"s_barrier\n"
"v_mfma_f32_16x16x32_bf16 a[192:195],v[216:219],v[224:227],a[192:195]\n"
"ds_read_b128 v[152:155],v80 offset:45056\n"
"s_mov_b32 s87,524288\n"
"v_mfma_f32_16x16x32_bf16 a[196:199],v[220:223],v[224:227],a[196:199]\n"
"ds_read_b128 v[156:159],v80 offset:47104\n"
"buffer_load_dword v115,v112,s[36:39],s87 offen\n"
"v_mfma_f32_16x16x32_bf16 a[204:207],v[220:223],v[228:231],a[204:207]\n"
"ds_read_b64_tr_b16 v[160:161],v98 offset:0\n"
"s_mov_b32 s87,1048576\n"
"v_mfma_f32_16x16x32_bf16 a[200:203],v[216:219],v[228:231],a[200:203]\n"
"ds_read_b64_tr_b16 v[162:163],v98 offset:1024\n"
"buffer_load_dword v115,v112,s[36:39],s87 offen\n"
"v_mfma_f32_16x16x32_bf16 a[208:211],v[216:219],v[232:235],a[208:211]\n"
"ds_read_b64_tr_b16 v[164:165],v99 offset:0\n"
"s_mov_b32 s87,1572864\n"
"v_mfma_f32_16x16x32_bf16 a[212:215],v[220:223],v[232:235],a[212:215]\n"
"ds_read_b64_tr_b16 v[166:167],v99 offset:1024\n"
"buffer_load_dword v115,v112,s[36:39],s87 offen\n"
"v_mfma_f32_16x16x32_bf16 a[220:223],v[220:223],v[236:239],a[220:223]\n"
"ds_read_b64_tr_b16 v[168:169],v100 offset:0\n"
"s_lshl3_add_u32 vcc_lo,vcc_lo,s52\n"
"v_mfma_f32_16x16x32_bf16 a[216:219],v[216:219],v[236:239],a[216:219]\n"
"ds_read_b64_tr_b16 v[170:171],v100 offset:1024\n"
"s_lshl_b32 s13,vcc_lo,15\n"
"v_mfma_f32_16x16x32_bf16 a[224:227],v[216:219],v[240:243],a[224:227]\n"
"ds_read_b64_tr_b16 v[172:173],v101 offset:0\n"
"s_lshl_b32 vcc_lo,s95,8\n"
"v_mfma_f32_16x16x32_bf16 a[228:231],v[220:223],v[240:243],a[228:231]\n"
"ds_read_b64_tr_b16 v[174:175],v101 offset:1024\n"
"s_add_u32 s13,s13,vcc_lo\n"
"v_mfma_f32_16x16x32_bf16 a[236:239],v[220:223],v[244:247],a[236:239]\n"
"ds_read_b64_tr_b16 v[176:177],v102 offset:0\n"
"s_lshl3_add_u32 vcc_hi,s94,s95\n"
"v_mfma_f32_16x16x32_bf16 a[232:235],v[216:219],v[244:247],a[232:235]\n"
"ds_read_b64_tr_b16 v[178:179],v102 offset:1024\n"
"s_lshl_b32 s72,vcc_hi,8\n"
"v_mfma_f32_16x16x32_bf16 a[240:243],v[216:219],v[248:251],a[240:243]\n"
"ds_read_b64_tr_b16 v[180:181],v103 offset:0\n"
"s_add_u32 s72,s72,149061632\n"
"v_mfma_f32_16x16x32_bf16 a[244:247],v[220:223],v[248:251],a[244:247]\n"
"s_or_b32 vcc_lo,s96,s52\n"
"ds_read_b64_tr_b16 v[182:183],v103 offset:1024\n"
"s_cselect_b32 s72,2146435072,s72\n"
"v_mfma_f32_16x16x32_bf16 a[252:255],v[220:223],v[252:255],a[252:255]\n"
"ds_read_b64_tr_b16 v[184:185],v104 offset:0\n"
"s_lshl_b32 s3,vcc_hi,18\n"
"v_mfma_f32_16x16x32_bf16 a[248:251],v[216:219],v[252:255],a[248:251]\n"
"s_lshl_b32 vcc_lo,s96,15\n"
"ds_read_b64_tr_b16 v[186:187],v104 offset:1024\n"
"s_waitcnt lgkmcnt(10)\n"
"v_mfma_f32_16x16x32_bf16 a[192:195],v[152:155],v[160:163],a[192:195]\n"
"ds_read_b64_tr_b16 v[188:189],v105 offset:0\n"
"s_sub_u32 s3,s3,vcc_lo\n"
"v_mfma_f32_16x16x32_bf16 a[196:199],v[156:159],v[160:163],a[196:199]\n"
"ds_read_b64_tr_b16 v[190:191],v105 offset:1024\n"
"s_add_u32 s3,s3,s78\n"
"v_mfma_f32_16x16x32_bf16 a[204:207],v[156:159],v[164:167],a[204:207]\n"
"ds_read_b128 v[128:131],v80 offset:32768\n"
"s_add_u32 s3,s3,100909056\n"
"v_mfma_f32_16x16x32_bf16 a[200:203],v[152:155],v[164:167],a[200:203]\n"
"ds_read_b128 v[192:195],v81 offset:32768\n"
"s_waitcnt lgkmcnt(10)\n"
"v_mfma_f32_16x16x32_bf16 a[208:211],v[152:155],v[168:171],a[208:211]\n"
"ds_read_b64_tr_b16 v[224:225],v98 offset:16384\n"
"s_lshl3_add_u32 vcc_hi,vcc_hi,s96\n"
"v_mfma_f32_16x16x32_bf16 a[212:215],v[156:159],v[168:171],a[212:215]\n"
"ds_read_b64_tr_b16 v[226:227],v98 offset:17408\n"
"s_lshl2_add_u32 vcc_hi,vcc_hi,s52\n"
"v_mfma_f32_16x16x32_bf16 a[220:223],v[156:159],v[172:175],a[220:223]\n"
"ds_read_b64_tr_b16 v[228:229],v99 offset:16384\n"
"s_lshl_b32 s2,vcc_hi,4\n"
"v_mfma_f32_16x16x32_bf16 a[216:219],v[152:155],v[172:175],a[216:219]\n"
"ds_read_b64_tr_b16 v[230:231],v99 offset:17408\n"
"s_waitcnt lgkmcnt(10)\n"
"v_mfma_f32_16x16x32_bf16 a[224:227],v[152:155],v[176:179],a[224:227]\n"
"ds_read_b64_tr_b16 v[232:233],v100 offset:16384\n"
"s_add_u32 s2,s2,149028864\n"
"v_mfma_f32_16x16x32_bf16 a[228:231],v[156:159],v[176:179],a[228:231]\n"
"ds_read_b64_tr_b16 v[234:235],v100 offset:17408\n"
"s_lshl2_add_u32 vcc_lo,s95,s52\n"
"v_mfma_f32_16x16x32_bf16 a[236:239],v[156:159],v[180:183],a[236:239]\n"
"ds_read_b64_tr_b16 v[236:237],v101 offset:16384\n"
"s_lshl_b32 vcc_lo,vcc_lo,10\n"
"v_mfma_f32_16x16x32_bf16 a[232:235],v[152:155],v[180:183],a[232:235]\n"
"ds_read_b64_tr_b16 v[238:239],v101 offset:17408\n"
"s_waitcnt lgkmcnt(10)\n"
"v_mfma_f32_16x16x32_bf16 a[240:243],v[152:155],v[184:187],a[240:243]\n"
"ds_read_b64_tr_b16 v[240:241],v102 offset:16384\n"
"s_lshl_b32 vcc_hi,s94,22\n"
"v_mfma_f32_16x16x32_bf16 a[244:247],v[156:159],v[184:187],a[244:247]\n"
"ds_read_b64_tr_b16 v[242:243],v102 offset:17408\n"
"s_add_u32 s12,vcc_lo,vcc_hi\n"
"v_mfma_f32_16x16x32_bf16 a[252:255],v[156:159],v[188:191],a[252:255]\n"
"ds_read_b64_tr_b16 v[244:245],v103 offset:16384\n"
"s_lshl_b32 vcc_lo,s96,19\n"
"v_mfma_f32_16x16x32_bf16 a[248:251],v[152:155],v[188:191],a[248:251]\n"
"ds_read_b64_tr_b16 v[246:247],v103 offset:17408\n"
"s_waitcnt lgkmcnt(8)\n"
"v_mfma_f32_16x16x32_bf16 a[0:3],v[128:131],v[160:163],a[0:3]\n"
"ds_read_b64_tr_b16 v[248:249],v104 offset:16384\n"
"s_add_u32 s10,s12,vcc_lo\n"
"v_mfma_f32_16x16x32_bf16 a[0:3],v[192:195],v[224:227],a[0:3]\n"
"ds_read_b64_tr_b16 v[250:251],v104 offset:17408\n"
"s_add_u32 s10,s10,71303168\n"
"v_mfma_f32_16x16x32_bf16 a[8:11],v[192:195],v[228:231],a[8:11]\n"
"ds_read_b64_tr_b16 v[252:253],v105 offset:16384\n"
"s_mul_i32 vcc_lo,s52,31744\n"
"v_mfma_f32_16x16x32_bf16 a[8:11],v[128:131],v[164:167],a[8:11]\n"
"ds_read_b64_tr_b16 v[254:255],v105 offset:17408\n"
"s_waitcnt lgkmcnt(8)\n"
"v_mfma_f32_16x16x32_bf16 a[16:19],v[128:131],v[168:171],a[16:19]\n"
"ds_read_b128 v[132:135],v80 offset:34816\n"
"s_add_u32 s10,s10,vcc_lo\n"
"v_mfma_f32_16x16x32_bf16 a[16:19],v[192:195],v[232:235],a[16:19]\n"
"ds_read_b128 v[196:199],v81 offset:34816\n"
"s_lshl_b32 vcc_lo,s52,10\n"
"v_mfma_f32_16x16x32_bf16 a[24:27],v[192:195],v[236:239],a[24:27]\n"
"ds_read_b128 v[136:139],v80 offset:36864\n"
"s_sub_u32 s12,57671680,vcc_lo\n"
"v_mfma_f32_16x16x32_bf16 a[24:27],v[128:131],v[172:175],a[24:27]\n"
"ds_read_b128 v[200:203],v81 offset:36864\n"
"s_waitcnt lgkmcnt(8)\n"
"v_mfma_f32_16x16x32_bf16 a[32:35],v[128:131],v[176:179],a[32:35]\n"
"ds_read_b128 v[140:143],v80 offset:38912\n"
"s_add_u32 s8,s79,54525952\n"
"v_mfma_f32_16x16x32_bf16 a[32:35],v[192:195],v[240:243],a[32:35]\n"
"ds_read_b128 v[204:207],v81 offset:38912\n"
"s_and_b32 vcc_lo,s96,1\n"
"v_mfma_f32_16x16x32_bf16 a[40:43],v[192:195],v[244:247],a[40:43]\n"
"ds_read_b128 v[144:147],v80 offset:40960\n"
"s_lshl_b32 s11,vcc_lo,10\n"
"v_mfma_f32_16x16x32_bf16 a[40:43],v[128:131],v[180:183],a[40:43]\n"
"ds_read_b128 v[208:211],v81 offset:40960\n"
"s_waitcnt lgkmcnt(8)\n"
"v_mfma_f32_16x16x32_bf16 a[48:51],v[128:131],v[184:187],a[48:51]\n"
"ds_read_b128 v[148:151],v80 offset:43008\n"
"s_add_u32 s11,s11,148932608\n"
"v_mfma_f32_16x16x32_bf16 a[48:51],v[192:195],v[248:251],a[48:51]\n"
"ds_read_b128 v[212:215],v81 offset:43008\n"
"s_mov_b32 s9,148931072\n"
"v_mfma_f32_16x16x32_bf16 a[56:59],v[192:195],v[252:255],a[56:59]\n"
"s_cmp_eq_u32 s52,0\n"
"ds_read_b128 v[216:219],v81 offset:45056\n"
"s_cselect_b32 s71,s71,2146435072\n"
"v_mfma_f32_16x16x32_bf16 a[56:59],v[128:131],v[188:191],a[56:59]\n"
"ds_read_b128 v[220:223],v81 offset:47104\n"
"s_waitcnt lgkmcnt(10)\n"
"v_mfma_f32_16x16x32_bf16 a[4:7],v[132:135],v[160:163],a[4:7]\n"
"v_mfma_f32_16x16x32_bf16 a[4:7],v[196:199],v[224:227],a[4:7]\n"
"v_mfma_f32_16x16x32_bf16 a[12:15],v[196:199],v[228:231],a[12:15]\n"
"v_mfma_f32_16x16x32_bf16 a[12:15],v[132:135],v[164:167],a[12:15]\n"
"v_mfma_f32_16x16x32_bf16 a[20:23],v[132:135],v[168:171],a[20:23]\n"
"s_waitcnt lgkmcnt(0)\n"
"s_barrier\n"
"v_mfma_f32_16x16x32_bf16 a[20:23],v[196:199],v[232:235],a[20:23]\n"
"s_mov_b32 s77,1572864\n"
"s_add_u32 m0,s78,98304\n"
"v_mfma_f32_16x16x32_bf16 a[28:31],v[196:199],v[236:239],a[28:31]\n"
"buffer_load_dwordx4 v110,s[20:23],s77 offen offset:0 lds\n"
"v_mfma_f32_16x16x32_bf16 a[28:31],v[132:135],v[172:175],a[28:31]\n"
"v_mfma_f32_16x16x32_bf16 a[36:39],v[132:135],v[176:179],a[36:39]\n"
"v_mfma_f32_16x16x32_bf16 a[36:39],v[196:199],v[240:243],a[36:39]\n"
"v_mfma_f32_16x16x32_bf16 a[44:47],v[196:199],v[244:247],a[44:47]\n"
"buffer_load_dwordx4 v111,s[20:23],s77 offen offset:1024 lds\n"
"v_mfma_f32_16x16x32_bf16 a[44:47],v[132:135],v[180:183],a[44:47]\n"
"v_mfma_f32_16x16x32_bf16 a[52:55],v[132:135],v[184:187],a[52:55]\n"
"v_mfma_f32_16x16x32_bf16 a[52:55],v[196:199],v[248:251],a[52:55]\n"
"v_mfma_f32_16x16x32_bf16 a[60:63],v[196:199],v[252:255],a[60:63]\n"
"buffer_load_dwordx4 v113,s[20:23],s77 offen offset:2048 lds\n"
"v_mfma_f32_16x16x32_bf16 a[60:63],v[132:135],v[188:191],a[60:63]\n"
"v_mfma_f32_16x16x32_bf16 a[64:67],v[136:139],v[160:163],a[64:67]\n"
"v_mfma_f32_16x16x32_bf16 a[64:67],v[200:203],v[224:227],a[64:67]\n"
"v_mfma_f32_16x16x32_bf16 a[72:75],v[200:203],v[228:231],a[72:75]\n"
"buffer_load_dwordx4 v114,s[20:23],s77 offen offset:3072 lds\n"
"v_mfma_f32_16x16x32_bf16 a[72:75],v[136:139],v[164:167],a[72:75]\n"
"v_mfma_f32_16x16x32_bf16 a[80:83],v[136:139],v[168:171],a[80:83]\n"
"v_mfma_f32_16x16x32_bf16 a[80:83],v[200:203],v[232:235],a[80:83]\n"
"s_mov_b32 s77,1835008\n"
"s_add_u32 m0,s78,114688\n"
"v_mfma_f32_16x16x32_bf16 a[88:91],v[200:203],v[236:239],a[88:91]\n"
"buffer_load_dwordx4 v110,s[20:23],s77 offen offset:0 lds\n"
"v_mfma_f32_16x16x32_bf16 a[88:91],v[136:139],v[172:175],a[88:91]\n"
"v_mfma_f32_16x16x32_bf16 a[96:99],v[136:139],v[176:179],a[96:99]\n"
"v_mfma_f32_16x16x32_bf16 a[96:99],v[200:203],v[240:243],a[96:99]\n"
"v_mfma_f32_16x16x32_bf16 a[104:107],v[200:203],v[244:247],a[104:107]\n"
"buffer_load_dwordx4 v111,s[20:23],s77 offen offset:1024 lds\n"
"v_mfma_f32_16x16x32_bf16 a[104:107],v[136:139],v[180:183],a[104:107]\n"
"v_mfma_f32_16x16x32_bf16 a[112:115],v[136:139],v[184:187],a[112:115]\n"
"v_mfma_f32_16x16x32_bf16 a[112:115],v[200:203],v[248:251],a[112:115]\n"
"v_mfma_f32_16x16x32_bf16 a[120:123],v[200:203],v[252:255],a[120:123]\n"
"buffer_load_dwordx4 v113,s[20:23],s77 offen offset:2048 lds\n"
"v_mfma_f32_16x16x32_bf16 a[120:123],v[136:139],v[188:191],a[120:123]\n"
"s_waitcnt vmcnt(10)\n"
"v_mfma_f32_16x16x32_bf16 a[68:71],v[140:143],v[160:163],a[68:71]\n"
"v_lshlrev_b32 v24,16,v8\n"
"v_and_b32 v25,0xffff0000,v8\n"
"v_mfma_f32_16x16x32_bf16 a[68:71],v[204:207],v[224:227],a[68:71]\n"
"v_lshlrev_b32 v26,16,v9\n"
"v_and_b32 v27,0xffff0000,v9\n"
"v_mfma_f32_16x16x32_bf16 a[76:79],v[204:207],v[228:231],a[76:79]\n"
"buffer_load_dwordx4 v114,s[20:23],s77 offen offset:3072 lds\n"
"v_mfma_f32_16x16x32_bf16 a[76:79],v[140:143],v[164:167],a[76:79]\n"
"v_lshlrev_b32 v28,16,v10\n"
"v_and_b32 v29,0xffff0000,v10\n"
"v_mfma_f32_16x16x32_bf16 a[84:87],v[140:143],v[168:171],a[84:87]\n"
"v_lshlrev_b32 v30,16,v11\n"
"v_and_b32 v31,0xffff0000,v11\n"
"v_mfma_f32_16x16x32_bf16 a[84:87],v[204:207],v[232:235],a[84:87]\n"
"v_lshlrev_b32 v32,16,v12\n"
"v_and_b32 v33,0xffff0000,v12\n"
"v_mfma_f32_16x16x32_bf16 a[92:95],v[204:207],v[236:239],a[92:95]\n"
"s_mov_b32 s76,512\n"
"v_lshlrev_b32 v34,16,v13\n"
"s_add_u32 m0,s79,32768\n"
"v_mfma_f32_16x16x32_bf16 a[92:95],v[140:143],v[172:175],a[92:95]\n"
"buffer_load_dwordx4 v106,s[16:19],s76 offen offset:0 lds\n"
"v_mfma_f32_16x16x32_bf16 a[100:103],v[140:143],v[176:179],a[100:103]\n"
"v_and_b32 v35,0xffff0000,v13\n"
"v_lshlrev_b32 v36,16,v14\n"
"v_mfma_f32_16x16x32_bf16 a[100:103],v[204:207],v[240:243],a[100:103]\n"
"v_and_b32 v37,0xffff0000,v14\n"
"v_lshlrev_b32 v38,16,v15\n"
"v_mfma_f32_16x16x32_bf16 a[108:111],v[204:207],v[244:247],a[108:111]\n"
"v_and_b32 v39,0xffff0000,v15\n"
"v_lshlrev_b32 v40,16,v16\n"
"v_mfma_f32_16x16x32_bf16 a[108:111],v[140:143],v[180:183],a[108:111]\n"
"v_and_b32 v41,0xffff0000,v16\n"
"v_lshlrev_b32 v42,16,v17\n"
"v_mfma_f32_16x16x32_bf16 a[116:119],v[140:143],v[184:187],a[116:119]\n"
"buffer_load_dwordx4 v107,s[16:19],s76 offen offset:1024 lds\n"
"v_mfma_f32_16x16x32_bf16 a[116:119],v[204:207],v[248:251],a[116:119]\n"
"v_and_b32 v43,0xffff0000,v17\n"
"v_lshlrev_b32 v44,16,v18\n"
"v_mfma_f32_16x16x32_bf16 a[124:127],v[204:207],v[252:255],a[124:127]\n"
"v_and_b32 v45,0xffff0000,v18\n"
"v_lshlrev_b32 v46,16,v19\n"
"v_mfma_f32_16x16x32_bf16 a[124:127],v[140:143],v[188:191],a[124:127]\n"
"v_and_b32 v47,0xffff0000,v19\n"
"v_lshlrev_b32 v48,16,v20\n"
"v_mfma_f32_16x16x32_bf16 a[128:131],v[144:147],v[160:163],a[128:131]\n"
"v_and_b32 v49,0xffff0000,v20\n"
"v_lshlrev_b32 v50,16,v21\n"
"v_mfma_f32_16x16x32_bf16 a[128:131],v[208:211],v[224:227],a[128:131]\n"
"buffer_load_dwordx4 v108,s[16:19],s76 offen offset:2048 lds\n"
"v_mfma_f32_16x16x32_bf16 a[136:139],v[208:211],v[228:231],a[136:139]\n"
"v_and_b32 v51,0xffff0000,v21\n"
"v_lshlrev_b32 v52,16,v22\n"
"v_mfma_f32_16x16x32_bf16 a[136:139],v[144:147],v[164:167],a[136:139]\n"
"v_and_b32 v53,0xffff0000,v22\n"
"v_lshlrev_b32 v54,16,v23\n"
"v_mfma_f32_16x16x32_bf16 a[144:147],v[144:147],v[168:171],a[144:147]\n"
"v_and_b32 v55,0xffff0000,v23\n"
"v_add_f32 v56,v24,v25\n"
"v_mfma_f32_16x16x32_bf16 a[144:147],v[208:211],v[232:235],a[144:147]\n"
"v_add_f32 v57,v26,v27\n"
"v_add_f32 v56,v56,v28\n"
"v_mfma_f32_16x16x32_bf16 a[152:155],v[208:211],v[236:239],a[152:155]\n"
"buffer_load_dwordx4 v109,s[16:19],s76 offen offset:3072 lds\n"
"v_mfma_f32_16x16x32_bf16 a[152:155],v[144:147],v[172:175],a[152:155]\n"
"v_add_f32 v57,v57,v29\n"
"v_add_f32 v56,v56,v30\n"
"v_mfma_f32_16x16x32_bf16 a[160:163],v[144:147],v[176:179],a[160:163]\n"
"v_add_f32 v57,v57,v31\n"
"v_add_f32 v56,v56,v32\n"
"v_mfma_f32_16x16x32_bf16 a[160:163],v[208:211],v[240:243],a[160:163]\n"
"v_readfirstlane_b32 s82,v78\n"
"v_readfirstlane_b32 s83,v79\n"
"v_mfma_f32_16x16x32_bf16 a[168:171],v[208:211],v[244:247],a[168:171]\n"
"s_mov_b32 s76,262656\n"
"v_add_f32 v57,v57,v33\n"
"s_add_u32 m0,s79,36864\n"
"v_mfma_f32_16x16x32_bf16 a[168:171],v[144:147],v[180:183],a[168:171]\n"
"buffer_load_dwordx4 v106,s[16:19],s76 offen offset:0 lds\n"
"s_mul_i32 s80,s82,0x9e3779b1\n"
"v_mfma_f32_16x16x32_bf16 a[176:179],v[144:147],v[184:187],a[176:179]\n"
"s_add_u32 s80,s80,0x7f4a7c15\n"
"v_add_f32 v56,v56,v34\n"
"v_add_f32 v57,v57,v35\n"
"v_mfma_f32_16x16x32_bf16 a[176:179],v[208:211],v[248:251],a[176:179]\n"
"s_add_u32 s81,s83,s80\n"
"s_add_u32 s81,s81,1\n"
"v_mfma_f32_16x16x32_bf16 a[184:187],v[208:211],v[252:255],a[184:187]\n"
"v_mov_b32 v124,s80\n"
"v_mov_b32 v125,s81\n"
"v_mfma_f32_16x16x32_bf16 a[184:187],v[144:147],v[188:191],a[184:187]\n"
"v_add_f32 v56,v56,v36\n"
"v_add_f32 v57,v57,v37\n"
"v_mfma_f32_16x16x32_bf16 a[132:135],v[148:151],v[160:163],a[132:135]\n"
"buffer_load_dwordx4 v107,s[16:19],s76 offen offset:1024 lds\n"
"v_mfma_f32_16x16x32_bf16 a[132:135],v[212:215],v[224:227],a[132:135]\n"
"v_add_f32 v56,v56,v38\n"
"v_add_f32 v57,v57,v39\n"
"v_mfma_f32_16x16x32_bf16 a[140:143],v[212:215],v[228:231],a[140:143]\n"
"v_add_f32 v56,v56,v40\n"
"v_add_f32 v57,v57,v41\n"
"v_mfma_f32_16x16x32_bf16 a[140:143],v[148:151],v[164:167],a[140:143]\n"
"v_add_f32 v56,v56,v42\n"
"v_add_f32 v57,v57,v43\n"
"v_mfma_f32_16x16x32_bf16 a[148:151],v[148:151],v[168:171],a[148:151]\n"
"v_add_f32 v56,v56,v44\n"
"v_add_f32 v57,v57,v45\n"
"v_mfma_f32_16x16x32_bf16 a[148:151],v[212:215],v[232:235],a[148:151]\n"
"buffer_load_dwordx4 v108,s[16:19],s76 offen offset:2048 lds\n"
"v_mfma_f32_16x16x32_bf16 a[156:159],v[212:215],v[236:239],a[156:159]\n"
"v_add_f32 v56,v56,v46\n"
"v_add_f32 v57,v57,v47\n"
"v_mfma_f32_16x16x32_bf16 a[156:159],v[148:151],v[172:175],a[156:159]\n"
"v_add_f32 v56,v56,v48\n"
"v_add_f32 v57,v57,v49\n"
"v_mfma_f32_16x16x32_bf16 a[164:167],v[148:151],v[176:179],a[164:167]\n"
"v_add_f32 v56,v56,v50\n"
"v_add_f32 v57,v57,v51\n"
"v_mfma_f32_16x16x32_bf16 a[164:167],v[212:215],v[240:243],a[164:167]\n"
"v_add_f32 v56,v56,v52\n"
"v_add_f32 v57,v57,v53\n"
"v_mfma_f32_16x16x32_bf16 a[172:175],v[212:215],v[244:247],a[172:175]\n"
"buffer_load_dwordx4 v109,s[16:19],s76 offen offset:3072 lds\n"
"v_mfma_f32_16x16x32_bf16 a[172:175],v[148:151],v[180:183],a[172:175]\n"
"s_mov_b32 s87,2097152\n"
"v_add_f32 v56,v56,v54\n"
"v_add_f32 v57,v57,v55\n"
"v_mfma_f32_16x16x32_bf16 a[180:183],v[148:151],v[184:187],a[180:183]\n"
"buffer_load_dword v115,v112,s[36:39],s87 offen\n"
"v_mfma_f32_16x16x32_bf16 a[180:183],v[212:215],v[248:251],a[180:183]\n"
"s_mov_b32 s87,2621440\n"
"v_add_f32 v56,v56,v57\n"
"v_mfma_f32_16x16x32_bf16 a[188:191],v[212:215],v[252:255],a[188:191]\n"
"buffer_load_dword v115,v112,s[36:39],s87 offen\n"
"v_add_f32_dpp v56,v56,v56 quad_perm:[1,0,3,2] row_mask:0xf bank_mask:0xf\n"
"v_mfma_f32_16x16x32_bf16 a[188:191],v[148:151],v[188:191],a[188:191]\n"
"s_barrier\n"
"v_mfma_f32_16x16x32_bf16 a[192:195],v[216:219],v[224:227],a[192:195]\n"
"ds_read_b128 v[152:155],v82 offset:12288\n"
"v_mfma_f32_16x16x32_bf16 a[196:199],v[220:223],v[224:227],a[196:199]\n"
"ds_read_b128 v[156:159],v82 offset:14336\n"
"v_add_f32_dpp v56,v56,v56 quad_perm:[2,3,0,1] row_mask:0xf bank_mask:0xf\n"
"v_mfma_f32_16x16x32_bf16 a[204:207],v[220:223],v[228:231],a[204:207]\n"
"ds_read_b64_tr_b16 v[160:161],v98 offset:49152\n"
"v_mfma_f32_16x16x32_bf16 a[200:203],v[216:219],v[228:231],a[200:203]\n"
"ds_read_b64_tr_b16 v[162:163],v98 offset:50176\n"
"v_add_f32_dpp v56,v56,v56 row_ror:8 row_mask:0xf bank_mask:0xf\n"
"v_mfma_f32_16x16x32_bf16 a[208:211],v[216:219],v[232:235],a[208:211]\n"
"ds_read_b64_tr_b16 v[164:165],v99 offset:49152\n"
"v_mov_b32 v57,v56\n"
"v_mfma_f32_16x16x32_bf16 a[212:215],v[220:223],v[232:235],a[212:215]\n"
"ds_read_b64_tr_b16 v[166:167],v99 offset:50176\n"
"v_mfma_f32_16x16x32_bf16 a[220:223],v[220:223],v[236:239],a[220:223]\n"
"ds_read_b64_tr_b16 v[168:169],v100 offset:49152\n"
"v_permlane16_swap_b32 v57,v56\n"
"v_mfma_f32_16x16x32_bf16 a[216:219],v[216:219],v[236:239],a[216:219]\n"
"ds_read_b64_tr_b16 v[170:171],v100 offset:50176\n"
"v_add_f32 v56,v56,v57\n"
"v_mfma_f32_16x16x32_bf16 a[224:227],v[216:219],v[240:243],a[224:227]\n"
"ds_read_b64_tr_b16 v[172:173],v101 offset:49152\n"
"v_mov_b32 v57,v56\n"
"v_mfma_f32_16x16x32_bf16 a[228:231],v[220:223],v[240:243],a[228:231]\n"
"ds_read_b64_tr_b16 v[174:175],v101 offset:50176\n"
"v_mfma_f32_16x16x32_bf16 a[236:239],v[220:223],v[244:247],a[236:239]\n"
"ds_read_b64_tr_b16 v[176:177],v102 offset:49152\n"
"v_permlane32_swap_b32 v57,v56\n"
"v_mfma_f32_16x16x32_bf16 a[232:235],v[216:219],v[244:247],a[232:235]\n"
"ds_read_b64_tr_b16 v[178:179],v102 offset:50176\n"
"v_add_f32 v56,v56,v57\n"
"v_mfma_f32_16x16x32_bf16 a[240:243],v[216:219],v[248:251],a[240:243]\n"
"ds_read_b64_tr_b16 v[180:181],v103 offset:49152\n"
"v_mul_f32 v58,0x3a800000,v56\n"
"v_mfma_f32_16x16x32_bf16 a[244:247],v[220:223],v[248:251],a[244:247]\n"
"ds_read_b64_tr_b16 v[182:183],v103 offset:50176\n"
"v_sub_f32 v62,v24,v58\n"
"v_mfma_f32_16x16x32_bf16 a[252:255],v[220:223],v[252:255],a[252:255]\n"
"ds_read_b64_tr_b16 v[184:185],v104 offset:49152\n"
"v_mul_f32 v59,v62,v62\n"
"v_mfma_f32_16x16x32_bf16 a[248:251],v[216:219],v[252:255],a[248:251]\n"
"ds_read_b64_tr_b16 v[186:187],v104 offset:50176\n"
"v_sub_f32 v63,v25,v58\n"
"s_waitcnt lgkmcnt(10)\n"
"v_mfma_f32_16x16x32_bf16 a[192:195],v[152:155],v[160:163],a[192:195]\n"
"ds_read_b64_tr_b16 v[188:189],v105 offset:49152\n"
"v_mul_f32 v60,v63,v63\n"
"v_mfma_f32_16x16x32_bf16 a[196:199],v[156:159],v[160:163],a[196:199]\n"
"ds_read_b64_tr_b16 v[190:191],v105 offset:50176\n"
"v_sub_f32 v64,v26,v58\n"
"v_mfma_f32_16x16x32_bf16 a[204:207],v[156:159],v[164:167],a[204:207]\n"
"ds_read_b128 v[128:131],v82 offset:0\n"
"v_fmac_f32 v59,v64,v64\n"
"v_mfma_f32_16x16x32_bf16 a[200:203],v[152:155],v[164:167],a[200:203]\n"
"ds_read_b128 v[192:195],v83 offset:0\n"
"s_waitcnt lgkmcnt(10)\n"
"v_mfma_f32_16x16x32_bf16 a[208:211],v[152:155],v[168:171],a[208:211]\n"
"ds_read_b64_tr_b16 v[224:225],v98 offset:32768\n"
"v_sub_f32 v65,v27,v58\n"
"v_mfma_f32_16x16x32_bf16 a[212:215],v[156:159],v[168:171],a[212:215]\n"
"ds_read_b64_tr_b16 v[226:227],v98 offset:33792\n"
"v_fmac_f32 v60,v65,v65\n"
"v_mfma_f32_16x16x32_bf16 a[220:223],v[156:159],v[172:175],a[220:223]\n"
"ds_read_b64_tr_b16 v[228:229],v99 offset:32768\n"
"v_sub_f32 v62,v28,v58\n"
"v_mfma_f32_16x16x32_bf16 a[216:219],v[152:155],v[172:175],a[216:219]\n"
"ds_read_b64_tr_b16 v[230:231],v99 offset:33792\n"
"s_waitcnt lgkmcnt(10)\n"
"v_mfma_f32_16x16x32_bf16 a[224:227],v[152:155],v[176:179],a[224:227]\n"
"ds_read_b64_tr_b16 v[232:233],v100 offset:32768\n"
"v_fmac_f32 v59,v62,v62\n"
"v_mfma_f32_16x16x32_bf16 a[228:231],v[156:159],v[176:179],a[228:231]\n"
"ds_read_b64_tr_b16 v[234:235],v100 offset:33792\n"
"v_sub_f32 v63,v29,v58\n"
"v_mfma_f32_16x16x32_bf16 a[236:239],v[156:159],v[180:183],a[236:239]\n"
"ds_read_b64_tr_b16 v[236:237],v101 offset:32768\n"
"v_fmac_f32 v60,v63,v63\n"
"v_mfma_f32_16x16x32_bf16 a[232:235],v[152:155],v[180:183],a[232:235]\n"
"ds_read_b64_tr_b16 v[238:239],v101 offset:33792\n"
"s_waitcnt lgkmcnt(10)\n"
"v_mfma_f32_16x16x32_bf16 a[240:243],v[152:155],v[184:187],a[240:243]\n"
"ds_read_b64_tr_b16 v[240:241],v102 offset:32768\n"
"v_sub_f32 v64,v30,v58\n"
"v_mfma_f32_16x16x32_bf16 a[244:247],v[156:159],v[184:187],a[244:247]\n"
"ds_read_b64_tr_b16 v[242:243],v102 offset:33792\n"
"v_fmac_f32 v59,v64,v64\n"
"v_mfma_f32_16x16x32_bf16 a[252:255],v[156:159],v[188:191],a[252:255]\n"
"ds_read_b64_tr_b16 v[244:245],v103 offset:32768\n"
"v_sub_f32 v65,v31,v58\n"
"v_mfma_f32_16x16x32_bf16 a[248:251],v[152:155],v[188:191],a[248:251]\n"
"ds_read_b64_tr_b16 v[246:247],v103 offset:33792\n"
"s_waitcnt lgkmcnt(8)\n"
"v_mfma_f32_16x16x32_bf16 a[0:3],v[128:131],v[160:163],a[0:3]\n"
"ds_read_b64_tr_b16 v[248:249],v104 offset:32768\n"
"v_fmac_f32 v60,v65,v65\n"
"v_mfma_f32_16x16x32_bf16 a[0:3],v[192:195],v[224:227],a[0:3]\n"
"ds_read_b64_tr_b16 v[250:251],v104 offset:33792\n"
"v_sub_f32 v62,v32,v58\n"
"v_mfma_f32_16x16x32_bf16 a[8:11],v[192:195],v[228:231],a[8:11]\n"
"ds_read_b64_tr_b16 v[252:253],v105 offset:32768\n"
"v_fmac_f32 v59,v62,v62\n"
"v_mfma_f32_16x16x32_bf16 a[8:11],v[128:131],v[164:167],a[8:11]\n"
"ds_read_b64_tr_b16 v[254:255],v105 offset:33792\n"
"s_waitcnt lgkmcnt(8)\n"
"v_mfma_f32_16x16x32_bf16 a[16:19],v[128:131],v[168:171],a[16:19]\n"
"ds_read_b128 v[132:135],v82 offset:2048\n"
"v_sub_f32 v63,v33,v58\n"
"v_mfma_f32_16x16x32_bf16 a[16:19],v[192:195],v[232:235],a[16:19]\n"
"ds_read_b128 v[196:199],v83 offset:2048\n"
"v_fmac_f32 v60,v63,v63\n"
"v_mfma_f32_16x16x32_bf16 a[24:27],v[192:195],v[236:239],a[24:27]\n"
"ds_read_b128 v[136:139],v82 offset:4096\n"
"v_sub_f32 v64,v34,v58\n"
"v_mfma_f32_16x16x32_bf16 a[24:27],v[128:131],v[172:175],a[24:27]\n"
"ds_read_b128 v[200:203],v83 offset:4096\n"
"s_waitcnt lgkmcnt(8)\n"
"v_mfma_f32_16x16x32_bf16 a[32:35],v[128:131],v[176:179],a[32:35]\n"
"ds_read_b128 v[140:143],v82 offset:6144\n"
"v_fmac_f32 v59,v64,v64\n"
"v_mfma_f32_16x16x32_bf16 a[32:35],v[192:195],v[240:243],a[32:35]\n"
"ds_read_b128 v[204:207],v83 offset:6144\n"
"v_sub_f32 v65,v35,v58\n"
"v_mfma_f32_16x16x32_bf16 a[40:43],v[192:195],v[244:247],a[40:43]\n"
"ds_read_b128 v[144:147],v82 offset:8192\n"
"v_fmac_f32 v60,v65,v65\n"
"v_mfma_f32_16x16x32_bf16 a[40:43],v[128:131],v[180:183],a[40:43]\n"
"ds_read_b128 v[208:211],v83 offset:8192\n"
"s_waitcnt lgkmcnt(8)\n"
"v_mfma_f32_16x16x32_bf16 a[48:51],v[128:131],v[184:187],a[48:51]\n"
"ds_read_b128 v[148:151],v82 offset:10240\n"
"v_sub_f32 v62,v36,v58\n"
"v_mfma_f32_16x16x32_bf16 a[48:51],v[192:195],v[248:251],a[48:51]\n"
"ds_read_b128 v[212:215],v83 offset:10240\n"
"v_fmac_f32 v59,v62,v62\n"
"v_mfma_f32_16x16x32_bf16 a[56:59],v[192:195],v[252:255],a[56:59]\n"
"ds_read_b128 v[216:219],v83 offset:12288\n"
"v_sub_f32 v63,v37,v58\n"
"v_mfma_f32_16x16x32_bf16 a[56:59],v[128:131],v[188:191],a[56:59]\n"
"ds_read_b128 v[220:223],v83 offset:14336\n"
"s_waitcnt lgkmcnt(10)\n"
"v_mfma_f32_16x16x32_bf16 a[4:7],v[132:135],v[160:163],a[4:7]\n"
"v_fmac_f32 v60,v63,v63\n"
"v_sub_f32 v64,v38,v58\n"
"v_mfma_f32_16x16x32_bf16 a[4:7],v[196:199],v[224:227],a[4:7]\n"
"v_fmac_f32 v59,v64,v64\n"
"v_sub_f32 v65,v39,v58\n"
"v_mfma_f32_16x16x32_bf16 a[12:15],v[196:199],v[228:231],a[12:15]\n"
"v_fmac_f32 v60,v65,v65\n"
"v_sub_f32 v62,v40,v58\n"
"v_mfma_f32_16x16x32_bf16 a[12:15],v[132:135],v[164:167],a[12:15]\n"
"v_fmac_f32 v59,v62,v62\n"
"v_sub_f32 v63,v41,v58\n"
"v_mfma_f32_16x16x32_bf16 a[20:23],v[132:135],v[168:171],a[20:23]\n"
"s_waitcnt lgkmcnt(0)\n"
"s_barrier\n"
"v_mfma_f32_16x16x32_bf16 a[20:23],v[196:199],v[232:235],a[20:23]\n"
"s_mov_b32 s77,2097152\n"
"v_fmac_f32 v60,v63,v63\n"
"s_add_u32 m0,s78,147456\n"
"v_mfma_f32_16x16x32_bf16 a[28:31],v[196:199],v[236:239],a[28:31]\n"
"buffer_load_dwordx4 v110,s[20:23],s77 offen offset:0 lds\n"
"v_mfma_f32_16x16x32_bf16 a[28:31],v[132:135],v[172:175],a[28:31]\n"
"v_sub_f32 v64,v42,v58\n"
"v_fmac_f32 v59,v64,v64\n"
"v_mfma_f32_16x16x32_bf16 a[36:39],v[132:135],v[176:179],a[36:39]\n"
"v_sub_f32 v65,v43,v58\n"
"v_fmac_f32 v60,v65,v65\n"
"v_mfma_f32_16x16x32_bf16 a[36:39],v[196:199],v[240:243],a[36:39]\n"
"v_sub_f32 v62,v44,v58\n"
"v_fmac_f32 v59,v62,v62\n"
"v_mfma_f32_16x16x32_bf16 a[44:47],v[196:199],v[244:247],a[44:47]\n"
"buffer_load_dwordx4 v111,s[20:23],s77 offen offset:1024 lds\n"
"v_mfma_f32_16x16x32_bf16 a[44:47],v[132:135],v[180:183],a[44:47]\n"
"v_sub_f32 v63,v45,v58\n"
"v_fmac_f32 v60,v63,v63\n"
"v_mfma_f32_16x16x32_bf16 a[52:55],v[132:135],v[184:187],a[52:55]\n"
"v_sub_f32 v64,v46,v58\n"
"v_fmac_f32 v59,v64,v64\n"
"v_mfma_f32_16x16x32_bf16 a[52:55],v[196:199],v[248:251],a[52:55]\n"
"v_sub_f32 v65,v47,v58\n"
"v_fmac_f32 v60,v65,v65\n"
"v_mfma_f32_16x16x32_bf16 a[60:63],v[196:199],v[252:255],a[60:63]\n"
"buffer_load_dwordx4 v113,s[20:23],s77 offen offset:2048 lds\n"
"v_mfma_f32_16x16x32_bf16 a[60:63],v[132:135],v[188:191],a[60:63]\n"
"v_sub_f32 v62,v48,v58\n"
"v_fmac_f32 v59,v62,v62\n"
"v_mfma_f32_16x16x32_bf16 a[64:67],v[136:139],v[160:163],a[64:67]\n"
"v_sub_f32 v63,v49,v58\n"
"v_fmac_f32 v60,v63,v63\n"
"v_mfma_f32_16x16x32_bf16 a[64:67],v[200:203],v[224:227],a[64:67]\n"
"v_sub_f32 v64,v50,v58\n"
"v_fmac_f32 v59,v64,v64\n"
"v_mfma_f32_16x16x32_bf16 a[72:75],v[200:203],v[228:231],a[72:75]\n"
"buffer_load_dwordx4 v114,s[20:23],s77 offen offset:3072 lds\n"
"v_mfma_f32_16x16x32_bf16 a[72:75],v[136:139],v[164:167],a[72:75]\n"
"v_sub_f32 v65,v51,v58\n"
"v_fmac_f32 v60,v65,v65\n"
"v_mfma_f32_16x16x32_bf16 a[80:83],v[136:139],v[168:171],a[80:83]\n"
"v_sub_f32 v62,v52,v58\n"
"v_fmac_f32 v59,v62,v62\n"
"v_mfma_f32_16x16x32_bf16 a[80:83],v[200:203],v[232:235],a[80:83]\n"
"s_mov_b32 s77,2359296\n"
"v_sub_f32 v63,v53,v58\n"
"s_add_u32 m0,s78,131072\n"
"v_mfma_f32_16x16x32_bf16 a[88:91],v[200:203],v[236:239],a[88:91]\n"
"buffer_load_dwordx4 v110,s[20:23],s77 offen offset:0 lds\n"
"v_mfma_f32_16x16x32_bf16 a[88:91],v[136:139],v[172:175],a[88:91]\n"
"v_fmac_f32 v60,v63,v63\n"
"v_sub_f32 v64,v54,v58\n"
"v_mfma_f32_16x16x32_bf16 a[96:99],v[136:139],v[176:179],a[96:99]\n"
"v_fmac_f32 v59,v64,v64\n"
"v_sub_f32 v65,v55,v58\n"
"v_mfma_f32_16x16x32_bf16 a[96:99],v[200:203],v[240:243],a[96:99]\n"
"v_fmac_f32 v60,v65,v65\n"
"v_add_f32 v59,v59,v60\n"
"v_mfma_f32_16x16x32_bf16 a[104:107],v[200:203],v[244:247],a[104:107]\n"
"buffer_load_dwordx4 v111,s[20:23],s77 offen offset:1024 lds\n"
"v_mfma_f32_16x16x32_bf16 a[104:107],v[136:139],v[180:183],a[104:107]\n"
"v_mfma_f32_16x16x32_bf16 a[112:115],v[136:139],v[184:187],a[112:115]\n"
"v_add_f32_dpp v59,v59,v59 quad_perm:[1,0,3,2] row_mask:0xf bank_mask:0xf\n"
"v_mfma_f32_16x16x32_bf16 a[112:115],v[200:203],v[248:251],a[112:115]\n"
"s_nop 0\n"
"v_add_f32_dpp v59,v59,v59 quad_perm:[2,3,0,1] row_mask:0xf bank_mask:0xf\n"
"v_mfma_f32_16x16x32_bf16 a[120:123],v[200:203],v[252:255],a[120:123]\n"
"buffer_load_dwordx4 v113,s[20:23],s77 offen offset:2048 lds\n"
"v_mfma_f32_16x16x32_bf16 a[120:123],v[136:139],v[188:191],a[120:123]\n"
"v_add_f32_dpp v59,v59,v59 row_ror:8 row_mask:0xf bank_mask:0xf\n"
"v_mov_b32 v57,v59\n"
"v_mfma_f32_16x16x32_bf16 a[68:71],v[140:143],v[160:163],a[68:71]\n"
"v_mfma_f32_16x16x32_bf16 a[68:71],v[204:207],v[224:227],a[68:71]\n"
"v_permlane16_swap_b32 v57,v59\n"
"v_add_f32 v59,v59,v57\n"
"v_mfma_f32_16x16x32_bf16 a[76:79],v[204:207],v[228:231],a[76:79]\n"
"buffer_load_dwordx4 v114,s[20:23],s77 offen offset:3072 lds\n"
"v_mfma_f32_16x16x32_bf16 a[76:79],v[140:143],v[164:167],a[76:79]\n"
"v_mov_b32 v57,v59\n"
"v_mfma_f32_16x16x32_bf16 a[84:87],v[140:143],v[168:171],a[84:87]\n"
"s_nop 0\n"
"v_permlane32_swap_b32 v57,v59\n"
"v_add_f32 v59,v59,v57\n"
"v_mfma_f32_16x16x32_bf16 a[84:87],v[204:207],v[232:235],a[84:87]\n"
"v_mul_f32 v59,0x3a800000,v59\n"
"v_cvt_pk_bf16_f32 v59,v59,v59\n"
"v_mfma_f32_16x16x32_bf16 a[92:95],v[204:207],v[236:239],a[92:95]\n"
"s_mov_b32 s76,640\n"
"v_lshlrev_b32 v59,16,v59\n"
"s_add_u32 m0,s79,65536\n"
"v_mfma_f32_16x16x32_bf16 a[92:95],v[140:143],v[172:175],a[92:95]\n"
"buffer_load_dwordx4 v106,s[16:19],s76 offen offset:0 lds\n"
"v_mfma_f32_16x16x32_bf16 a[100:103],v[140:143],v[176:179],a[100:103]\n"
"v_add_f32 v59,0x3727c5ac,v59\n"
"v_cvt_pk_bf16_f32 v59,v59,v59\n"
"v_mfma_f32_16x16x32_bf16 a[100:103],v[204:207],v[240:243],a[100:103]\n"
"v_lshlrev_b32 v59,16,v59\n"
"v_sqrt_f32 v59,v59\n"
"v_mfma_f32_16x16x32_bf16 a[108:111],v[204:207],v[244:247],a[108:111]\n"
"v_cvt_pk_bf16_f32 v58,v58,v58\n"
"v_lshlrev_b32 v58,16,v58\n"
"v_mfma_f32_16x16x32_bf16 a[108:111],v[140:143],v[180:183],a[108:111]\n"
"v_cvt_pk_bf16_f32 v59,v59,v59\n"
"v_lshlrev_b32 v59,16,v59\n"
"v_mfma_f32_16x16x32_bf16 a[116:119],v[140:143],v[184:187],a[116:119]\n"
"buffer_load_dwordx4 v107,s[16:19],s76 offen offset:1024 lds\n"
"v_mfma_f32_16x16x32_bf16 a[116:119],v[204:207],v[248:251],a[116:119]\n"
"v_rcp_f32 v66,v59\n"
"v_mfma_f32_16x16x32_bf16 a[124:127],v[204:207],v[252:255],a[124:127]\n"
"v_mul_f32 v66,0x3e0293ee,v66\n"
"v_mul_f32 v58,v58,v66\n"
"v_mfma_f32_16x16x32_bf16 a[124:127],v[140:143],v[188:191],a[124:127]\n"
"v_fma_f32 v24,v24,v66,-v58\n"
"v_fma_f32 v25,v25,v66,-v58\n"
"v_mfma_f32_16x16x32_bf16 a[128:131],v[144:147],v[160:163],a[128:131]\n"
"v_cvt_pk_bf16_f32 v8,v24,v25\n"
"v_fma_f32 v26,v26,v66,-v58\n"
"v_mfma_f32_16x16x32_bf16 a[128:131],v[208:211],v[224:227],a[128:131]\n"
"buffer_load_dwordx4 v108,s[16:19],s76 offen offset:2048 lds\n"
"v_mfma_f32_16x16x32_bf16 a[136:139],v[208:211],v[228:231],a[136:139]\n"
"v_fma_f32 v27,v27,v66,-v58\n"
"v_cvt_pk_bf16_f32 v9,v26,v27\n"
"v_mfma_f32_16x16x32_bf16 a[136:139],v[144:147],v[164:167],a[136:139]\n"
"v_fma_f32 v28,v28,v66,-v58\n"
"v_fma_f32 v29,v29,v66,-v58\n"
"v_mfma_f32_16x16x32_bf16 a[144:147],v[144:147],v[168:171],a[144:147]\n"
"v_cvt_pk_bf16_f32 v10,v28,v29\n"
"v_fma_f32 v30,v30,v66,-v58\n"
"v_mfma_f32_16x16x32_bf16 a[144:147],v[208:211],v[232:235],a[144:147]\n"
"v_fma_f32 v31,v31,v66,-v58\n"
"v_cvt_pk_bf16_f32 v11,v30,v31\n"
"v_mfma_f32_16x16x32_bf16 a[152:155],v[208:211],v[236:239],a[152:155]\n"
"buffer_load_dwordx4 v109,s[16:19],s76 offen offset:3072 lds\n"
"v_mfma_f32_16x16x32_bf16 a[152:155],v[144:147],v[172:175],a[152:155]\n"
"buffer_store_dwordx4 v[8:11],v71,s[24:27],s54 offen sc1\n"
"v_mfma_f32_16x16x32_bf16 a[160:163],v[144:147],v[176:179],a[160:163]\n"
"s_add_u32 s54,s54,131072\n"
"v_fma_f32 v32,v32,v66,-v58\n"
"v_fma_f32 v33,v33,v66,-v58\n"
"v_mfma_f32_16x16x32_bf16 a[160:163],v[208:211],v[240:243],a[160:163]\n"
"v_cvt_pk_bf16_f32 v12,v32,v33\n"
"v_fma_f32 v34,v34,v66,-v58\n"
"v_mfma_f32_16x16x32_bf16 a[168:171],v[208:211],v[244:247],a[168:171]\n"
"s_mov_b32 s76,262784\n"
"v_fma_f32 v35,v35,v66,-v58\n"
"s_add_u32 m0,s79,69632\n"
"v_mfma_f32_16x16x32_bf16 a[168:171],v[144:147],v[180:183],a[168:171]\n"
"buffer_load_dwordx4 v106,s[16:19],s76 offen offset:0 lds\n"
"v_mfma_f32_16x16x32_bf16 a[176:179],v[144:147],v[184:187],a[176:179]\n"
"v_cvt_pk_bf16_f32 v13,v34,v35\n"
"v_fma_f32 v36,v36,v66,-v58\n"
"v_mfma_f32_16x16x32_bf16 a[176:179],v[208:211],v[248:251],a[176:179]\n"
"v_fma_f32 v37,v37,v66,-v58\n"
"v_cvt_pk_bf16_f32 v14,v36,v37\n"
"v_mfma_f32_16x16x32_bf16 a[184:187],v[208:211],v[252:255],a[184:187]\n"
"v_fma_f32 v38,v38,v66,-v58\n"
"v_fma_f32 v39,v39,v66,-v58\n"
"v_cvt_pk_bf16_f32 v15,v38,v39\n"
"v_mfma_f32_16x16x32_bf16 a[184:187],v[144:147],v[188:191],a[184:187]\n"
"buffer_store_dwordx4 v[12:15],v71,s[24:27],s54 offen sc1\n"
"v_mfma_f32_16x16x32_bf16 a[132:135],v[148:151],v[160:163],a[132:135]\n"
"buffer_load_dwordx4 v107,s[16:19],s76 offen offset:1024 lds\n"
"v_mfma_f32_16x16x32_bf16 a[132:135],v[212:215],v[224:227],a[132:135]\n"
"s_add_u32 s54,s54,131072\n"
"v_fma_f32 v40,v40,v66,-v58\n"
"v_mfma_f32_16x16x32_bf16 a[140:143],v[212:215],v[228:231],a[140:143]\n"
"v_fma_f32 v41,v41,v66,-v58\n"
"v_cvt_pk_bf16_f32 v16,v40,v41\n"
"v_mfma_f32_16x16x32_bf16 a[140:143],v[148:151],v[164:167],a[140:143]\n"
"v_fma_f32 v42,v42,v66,-v58\n"
"v_fma_f32 v43,v43,v66,-v58\n"
"v_mfma_f32_16x16x32_bf16 a[148:151],v[148:151],v[168:171],a[148:151]\n"
"v_cvt_pk_bf16_f32 v17,v42,v43\n"
"v_fma_f32 v44,v44,v66,-v58\n"
"v_mfma_f32_16x16x32_bf16 a[148:151],v[212:215],v[232:235],a[148:151]\n"
"buffer_load_dwordx4 v108,s[16:19],s76 offen offset:2048 lds\n"
"v_mfma_f32_16x16x32_bf16 a[156:159],v[212:215],v[236:239],a[156:159]\n"
"v_fma_f32 v45,v45,v66,-v58\n"
"v_cvt_pk_bf16_f32 v18,v44,v45\n"
"v_mfma_f32_16x16x32_bf16 a[156:159],v[148:151],v[172:175],a[156:159]\n"
"v_fma_f32 v46,v46,v66,-v58\n"
"v_fma_f32 v47,v47,v66,-v58\n"
"v_cvt_pk_bf16_f32 v19,v46,v47\n"
"v_mfma_f32_16x16x32_bf16 a[164:167],v[148:151],v[176:179],a[164:167]\n"
"buffer_store_dwordx4 v[16:19],v71,s[24:27],s54 offen sc1\n"
"v_mfma_f32_16x16x32_bf16 a[164:167],v[212:215],v[240:243],a[164:167]\n"
"s_add_u32 s54,s54,131072\n"
"v_fma_f32 v48,v48,v66,-v58\n"
"v_mfma_f32_16x16x32_bf16 a[172:175],v[212:215],v[244:247],a[172:175]\n"
"buffer_load_dwordx4 v109,s[16:19],s76 offen offset:3072 lds\n"
"v_mfma_f32_16x16x32_bf16 a[172:175],v[148:151],v[180:183],a[172:175]\n"
"s_mov_b32 s87,0\n"
"v_fma_f32 v49,v49,v66,-v58\n"
"v_cvt_pk_bf16_f32 v20,v48,v49\n"
"v_mfma_f32_16x16x32_bf16 a[180:183],v[148:151],v[184:187],a[180:183]\n"
"buffer_load_dword v115,v112,s[40:43],s87 offen\n"
"v_mfma_f32_16x16x32_bf16 a[180:183],v[212:215],v[248:251],a[180:183]\n"
"s_mov_b32 s87,1048576\n"
"v_fma_f32 v50,v50,v66,-v58\n"
"v_fma_f32 v51,v51,v66,-v58\n"
"v_mfma_f32_16x16x32_bf16 a[188:191],v[212:215],v[252:255],a[188:191]\n"
"buffer_load_dword v115,v112,s[40:43],s87 offen\n"
"v_mfma_f32_16x16x32_bf16 a[188:191],v[148:151],v[188:191],a[188:191]\n"
"s_mov_b32 s87,2097152\n"
"s_waitcnt vmcnt(31)\n"
"s_barrier\n"
"v_mfma_f32_16x16x32_bf16 a[192:195],v[216:219],v[224:227],a[192:195]\n"
"ds_read_b128 v[152:155],v80 offset:12288\n"
"buffer_load_dword v115,v112,s[40:43],s87 offen\n"
"v_mfma_f32_16x16x32_bf16 a[196:199],v[220:223],v[224:227],a[196:199]\n"
"ds_read_b128 v[156:159],v80 offset:14336\n"
"v_cvt_pk_bf16_f32 v21,v50,v51\n"
"v_mfma_f32_16x16x32_bf16 a[204:207],v[220:223],v[228:231],a[204:207]\n"
"ds_read_b64_tr_b16 v[160:161],v98 offset:0\n"
"v_fma_f32 v52,v52,v66,-v58\n"
"v_mfma_f32_16x16x32_bf16 a[200:203],v[216:219],v[228:231],a[200:203]\n"
"ds_read_b64_tr_b16 v[162:163],v98 offset:1024\n"
"v_fma_f32 v53,v53,v66,-v58\n"
"v_mfma_f32_16x16x32_bf16 a[208:211],v[216:219],v[232:235],a[208:211]\n"
"ds_read_b64_tr_b16 v[164:165],v99 offset:0\n"
"v_cvt_pk_bf16_f32 v22,v52,v53\n"
"v_mfma_f32_16x16x32_bf16 a[212:215],v[220:223],v[232:235],a[212:215]\n"
"ds_read_b64_tr_b16 v[166:167],v99 offset:1024\n"
"v_fma_f32 v54,v54,v66,-v58\n"
"v_mfma_f32_16x16x32_bf16 a[220:223],v[220:223],v[236:239],a[220:223]\n"
"ds_read_b64_tr_b16 v[168:169],v100 offset:0\n"
"v_fma_f32 v55,v55,v66,-v58\n"
"v_mfma_f32_16x16x32_bf16 a[216:219],v[216:219],v[236:239],a[216:219]\n"
"ds_read_b64_tr_b16 v[170:171],v100 offset:1024\n"
"v_cvt_pk_bf16_f32 v23,v54,v55\n"
"v_mfma_f32_16x16x32_bf16 a[224:227],v[216:219],v[240:243],a[224:227]\n"
"ds_read_b64_tr_b16 v[172:173],v101 offset:0\n"
"buffer_store_dwordx4 v[20:23],v71,s[24:27],s54 offen sc1\n"
"v_mfma_f32_16x16x32_bf16 a[228:231],v[220:223],v[240:243],a[228:231]\n"
"ds_read_b64_tr_b16 v[174:175],v101 offset:1024\n"
"v_mfma_f32_16x16x32_bf16 a[236:239],v[220:223],v[244:247],a[236:239]\n"
"ds_read_b64_tr_b16 v[176:177],v102 offset:0\n"
"v_mfma_f32_16x16x32_bf16 a[232:235],v[216:219],v[244:247],a[232:235]\n"
"ds_read_b64_tr_b16 v[178:179],v102 offset:1024\n"
"v_mfma_f32_16x16x32_bf16 a[240:243],v[216:219],v[248:251],a[240:243]\n"
"ds_read_b64_tr_b16 v[180:181],v103 offset:0\n"
"v_mfma_f32_16x16x32_bf16 a[244:247],v[220:223],v[248:251],a[244:247]\n"
"ds_read_b64_tr_b16 v[182:183],v103 offset:1024\n"
"v_mfma_f32_16x16x32_bf16 a[252:255],v[220:223],v[252:255],a[252:255]\n"
"ds_read_b64_tr_b16 v[184:185],v104 offset:0\n"
"v_mfma_f32_16x16x32_bf16 a[248:251],v[216:219],v[252:255],a[248:251]\n"
"ds_read_b64_tr_b16 v[186:187],v104 offset:1024\n"
"s_waitcnt lgkmcnt(10)\n"
"v_mfma_f32_16x16x32_bf16 a[192:195],v[152:155],v[160:163],a[192:195]\n"
"ds_read_b64_tr_b16 v[188:189],v105 offset:0\n"
"v_mfma_f32_16x16x32_bf16 a[196:199],v[156:159],v[160:163],a[196:199]\n"
"ds_read_b64_tr_b16 v[190:191],v105 offset:1024\n"
"v_mfma_f32_16x16x32_bf16 a[204:207],v[156:159],v[164:167],a[204:207]\n"
"ds_read_b128 v[128:131],v80 offset:0\n"
"v_mfma_f32_16x16x32_bf16 a[200:203],v[152:155],v[164:167],a[200:203]\n"
"ds_read_b128 v[192:195],v81 offset:0\n"
"s_waitcnt lgkmcnt(10)\n"
"v_mfma_f32_16x16x32_bf16 a[208:211],v[152:155],v[168:171],a[208:211]\n"
"ds_read_b64_tr_b16 v[224:225],v98 offset:16384\n"
"v_mfma_f32_16x16x32_bf16 a[212:215],v[156:159],v[168:171],a[212:215]\n"
"ds_read_b64_tr_b16 v[226:227],v98 offset:17408\n"
"v_mfma_f32_16x16x32_bf16 a[220:223],v[156:159],v[172:175],a[220:223]\n"
"ds_read_b64_tr_b16 v[228:229],v99 offset:16384\n"
"v_mfma_f32_16x16x32_bf16 a[216:219],v[152:155],v[172:175],a[216:219]\n"
"ds_read_b64_tr_b16 v[230:231],v99 offset:17408\n"
"s_waitcnt lgkmcnt(10)\n"
"v_mfma_f32_16x16x32_bf16 a[224:227],v[152:155],v[176:179],a[224:227]\n"
"ds_read_b64_tr_b16 v[232:233],v100 offset:16384\n"
"v_mfma_f32_16x16x32_bf16 a[228:231],v[156:159],v[176:179],a[228:231]\n"
"ds_read_b64_tr_b16 v[234:235],v100 offset:17408\n"
"v_mfma_f32_16x16x32_bf16 a[236:239],v[156:159],v[180:183],a[236:239]\n"
"ds_read_b64_tr_b16 v[236:237],v101 offset:16384\n"
"v_mfma_f32_16x16x32_bf16 a[232:235],v[152:155],v[180:183],a[232:235]\n"
"ds_read_b64_tr_b16 v[238:239],v101 offset:17408\n"
"s_waitcnt lgkmcnt(10)\n"
"v_mfma_f32_16x16x32_bf16 a[240:243],v[152:155],v[184:187],a[240:243]\n"
"ds_read_b64_tr_b16 v[240:241],v102 offset:16384\n"
"v_mfma_f32_16x16x32_bf16 a[244:247],v[156:159],v[184:187],a[244:247]\n"
"ds_read_b64_tr_b16 v[242:243],v102 offset:17408\n"
"v_mfma_f32_16x16x32_bf16 a[252:255],v[156:159],v[188:191],a[252:255]\n"
"ds_read_b64_tr_b16 v[244:245],v103 offset:16384\n"
"v_mfma_f32_16x16x32_bf16 a[248:251],v[152:155],v[188:191],a[248:251]\n"
"ds_read_b64_tr_b16 v[246:247],v103 offset:17408\n"
"s_waitcnt lgkmcnt(8)\n"
"v_mfma_f32_16x16x32_bf16 a[0:3],v[128:131],v[160:163],a[0:3]\n"
"ds_read_b64_tr_b16 v[248:249],v104 offset:16384\n"
"v_mfma_f32_16x16x32_bf16 a[0:3],v[192:195],v[224:227],a[0:3]\n"
"ds_read_b64_tr_b16 v[250:251],v104 offset:17408\n"
"v_mfma_f32_16x16x32_bf16 a[8:11],v[192:195],v[228:231],a[8:11]\n"
"ds_read_b64_tr_b16 v[252:253],v105 offset:16384\n"
"v_mfma_f32_16x16x32_bf16 a[8:11],v[128:131],v[164:167],a[8:11]\n"
"ds_read_b64_tr_b16 v[254:255],v105 offset:17408\n"
"s_waitcnt lgkmcnt(8)\n"
"v_mfma_f32_16x16x32_bf16 a[16:19],v[128:131],v[168:171],a[16:19]\n"
"ds_read_b128 v[132:135],v80 offset:2048\n"
"v_mfma_f32_16x16x32_bf16 a[16:19],v[192:195],v[232:235],a[16:19]\n"
"ds_read_b128 v[196:199],v81 offset:2048\n"
"v_mfma_f32_16x16x32_bf16 a[24:27],v[192:195],v[236:239],a[24:27]\n"
"ds_read_b128 v[136:139],v80 offset:4096\n"
"v_mfma_f32_16x16x32_bf16 a[24:27],v[128:131],v[172:175],a[24:27]\n"
"ds_read_b128 v[200:203],v81 offset:4096\n"
"s_waitcnt lgkmcnt(8)\n"
"v_mfma_f32_16x16x32_bf16 a[32:35],v[128:131],v[176:179],a[32:35]\n"
"ds_read_b128 v[140:143],v80 offset:6144\n"
"v_mfma_f32_16x16x32_bf16 a[32:35],v[192:195],v[240:243],a[32:35]\n"
"ds_read_b128 v[204:207],v81 offset:6144\n"
"v_mfma_f32_16x16x32_bf16 a[40:43],v[192:195],v[244:247],a[40:43]\n"
"ds_read_b128 v[144:147],v80 offset:8192\n"
"v_mfma_f32_16x16x32_bf16 a[40:43],v[128:131],v[180:183],a[40:43]\n"
"ds_read_b128 v[208:211],v81 offset:8192\n"
"s_waitcnt lgkmcnt(8)\n"
"v_mfma_f32_16x16x32_bf16 a[48:51],v[128:131],v[184:187],a[48:51]\n"
"ds_read_b128 v[148:151],v80 offset:10240\n"
"v_mfma_f32_16x16x32_bf16 a[48:51],v[192:195],v[248:251],a[48:51]\n"
"ds_read_b128 v[212:215],v81 offset:10240\n"
"v_mfma_f32_16x16x32_bf16 a[56:59],v[192:195],v[252:255],a[56:59]\n"
"ds_read_b128 v[216:219],v81 offset:12288\n"
"v_mfma_f32_16x16x32_bf16 a[56:59],v[128:131],v[188:191],a[56:59]\n"
"ds_read_b128 v[220:223],v81 offset:14336\n"
"s_waitcnt lgkmcnt(10)\n"
"v_mfma_f32_16x16x32_bf16 a[4:7],v[132:135],v[160:163],a[4:7]\n"
"v_mfma_f32_16x16x32_bf16 a[4:7],v[196:199],v[224:227],a[4:7]\n"
"v_mfma_f32_16x16x32_bf16 a[12:15],v[196:199],v[228:231],a[12:15]\n"
"v_mfma_f32_16x16x32_bf16 a[12:15],v[132:135],v[164:167],a[12:15]\n"
"v_mfma_f32_16x16x32_bf16 a[20:23],v[132:135],v[168:171],a[20:23]\n"
"s_waitcnt lgkmcnt(0)\n"
"s_barrier\n"
"v_mfma_f32_16x16x32_bf16 a[20:23],v[196:199],v[232:235],a[20:23]\n"
"s_mov_b32 s77,2621440\n"
"s_add_u32 m0,s78,98304\n"
"v_mfma_f32_16x16x32_bf16 a[28:31],v[196:199],v[236:239],a[28:31]\n"
"buffer_load_dwordx4 v110,s[20:23],s77 offen offset:0 lds\n"
"v_mfma_f32_16x16x32_bf16 a[28:31],v[132:135],v[172:175],a[28:31]\n"
"v_mfma_f32_16x16x32_bf16 a[36:39],v[132:135],v[176:179],a[36:39]\n"
"v_mfma_f32_16x16x32_bf16 a[36:39],v[196:199],v[240:243],a[36:39]\n"
"v_mfma_f32_16x16x32_bf16 a[44:47],v[196:199],v[244:247],a[44:47]\n"
"buffer_load_dwordx4 v111,s[20:23],s77 offen offset:1024 lds\n"
"v_mfma_f32_16x16x32_bf16 a[44:47],v[132:135],v[180:183],a[44:47]\n"
"v_mfma_f32_16x16x32_bf16 a[52:55],v[132:135],v[184:187],a[52:55]\n"
"v_mfma_f32_16x16x32_bf16 a[52:55],v[196:199],v[248:251],a[52:55]\n"
"v_mfma_f32_16x16x32_bf16 a[60:63],v[196:199],v[252:255],a[60:63]\n"
"buffer_load_dwordx4 v113,s[20:23],s77 offen offset:2048 lds\n"
"v_mfma_f32_16x16x32_bf16 a[60:63],v[132:135],v[188:191],a[60:63]\n"
"v_mfma_f32_16x16x32_bf16 a[64:67],v[136:139],v[160:163],a[64:67]\n"
"v_mfma_f32_16x16x32_bf16 a[64:67],v[200:203],v[224:227],a[64:67]\n"
"v_mfma_f32_16x16x32_bf16 a[72:75],v[200:203],v[228:231],a[72:75]\n"
"buffer_load_dwordx4 v114,s[20:23],s77 offen offset:3072 lds\n"
"v_mfma_f32_16x16x32_bf16 a[72:75],v[136:139],v[164:167],a[72:75]\n"
"v_mfma_f32_16x16x32_bf16 a[80:83],v[136:139],v[168:171],a[80:83]\n"
"v_mfma_f32_16x16x32_bf16 a[80:83],v[200:203],v[232:235],a[80:83]\n"
"s_mov_b32 s77,2883584\n"
"s_add_u32 m0,s78,114688\n"
"v_mfma_f32_16x16x32_bf16 a[88:91],v[200:203],v[236:239],a[88:91]\n"
"buffer_load_dwordx4 v110,s[20:23],s77 offen offset:0 lds\n"
"v_mfma_f32_16x16x32_bf16 a[88:91],v[136:139],v[172:175],a[88:91]\n"
"v_mfma_f32_16x16x32_bf16 a[96:99],v[136:139],v[176:179],a[96:99]\n"
"v_mfma_f32_16x16x32_bf16 a[96:99],v[200:203],v[240:243],a[96:99]\n"
"v_mfma_f32_16x16x32_bf16 a[104:107],v[200:203],v[244:247],a[104:107]\n"
"buffer_load_dwordx4 v111,s[20:23],s77 offen offset:1024 lds\n"
"v_mfma_f32_16x16x32_bf16 a[104:107],v[136:139],v[180:183],a[104:107]\n"
"v_mfma_f32_16x16x32_bf16 a[112:115],v[136:139],v[184:187],a[112:115]\n"
"v_mfma_f32_16x16x32_bf16 a[112:115],v[200:203],v[248:251],a[112:115]\n"
"v_mfma_f32_16x16x32_bf16 a[120:123],v[200:203],v[252:255],a[120:123]\n"
"buffer_load_dwordx4 v113,s[20:23],s77 offen offset:2048 lds\n"
"v_mfma_f32_16x16x32_bf16 a[120:123],v[136:139],v[188:191],a[120:123]\n"
"v_mfma_f32_16x16x32_bf16 a[68:71],v[140:143],v[160:163],a[68:71]\n"
"v_mfma_f32_16x16x32_bf16 a[68:71],v[204:207],v[224:227],a[68:71]\n"
"v_mfma_f32_16x16x32_bf16 a[76:79],v[204:207],v[228:231],a[76:79]\n"
"buffer_load_dwordx4 v114,s[20:23],s77 offen offset:3072 lds\n"
"v_mfma_f32_16x16x32_bf16 a[76:79],v[140:143],v[164:167],a[76:79]\n"
"v_mfma_f32_16x16x32_bf16 a[84:87],v[140:143],v[168:171],a[84:87]\n"
"v_mfma_f32_16x16x32_bf16 a[84:87],v[204:207],v[232:235],a[84:87]\n"
"v_mfma_f32_16x16x32_bf16 a[92:95],v[204:207],v[236:239],a[92:95]\n"
"s_mov_b32 s76,768\n"
"s_add_u32 m0,s79,0\n"
"v_mfma_f32_16x16x32_bf16 a[92:95],v[140:143],v[172:175],a[92:95]\n"
"buffer_load_dwordx4 v106,s[16:19],s76 offen offset:0 lds\n"
"v_mfma_f32_16x16x32_bf16 a[100:103],v[140:143],v[176:179],a[100:103]\n"
"v_mfma_f32_16x16x32_bf16 a[100:103],v[204:207],v[240:243],a[100:103]\n"
"v_mfma_f32_16x16x32_bf16 a[108:111],v[204:207],v[244:247],a[108:111]\n"
"v_mfma_f32_16x16x32_bf16 a[108:111],v[140:143],v[180:183],a[108:111]\n"
"v_mfma_f32_16x16x32_bf16 a[116:119],v[140:143],v[184:187],a[116:119]\n"
"buffer_load_dwordx4 v107,s[16:19],s76 offen offset:1024 lds\n"
"v_mfma_f32_16x16x32_bf16 a[116:119],v[204:207],v[248:251],a[116:119]\n"
"v_mfma_f32_16x16x32_bf16 a[124:127],v[204:207],v[252:255],a[124:127]\n"
"v_mfma_f32_16x16x32_bf16 a[124:127],v[140:143],v[188:191],a[124:127]\n"
"v_mfma_f32_16x16x32_bf16 a[128:131],v[144:147],v[160:163],a[128:131]\n"
"v_mfma_f32_16x16x32_bf16 a[128:131],v[208:211],v[224:227],a[128:131]\n"
"buffer_load_dwordx4 v108,s[16:19],s76 offen offset:2048 lds\n"
"v_mfma_f32_16x16x32_bf16 a[136:139],v[208:211],v[228:231],a[136:139]\n"
"v_mfma_f32_16x16x32_bf16 a[136:139],v[144:147],v[164:167],a[136:139]\n"
"v_mfma_f32_16x16x32_bf16 a[144:147],v[144:147],v[168:171],a[144:147]\n"
"v_mfma_f32_16x16x32_bf16 a[144:147],v[208:211],v[232:235],a[144:147]\n"
"v_mfma_f32_16x16x32_bf16 a[152:155],v[208:211],v[236:239],a[152:155]\n"
"buffer_load_dwordx4 v109,s[16:19],s76 offen offset:3072 lds\n"
"v_mfma_f32_16x16x32_bf16 a[152:155],v[144:147],v[172:175],a[152:155]\n"
"v_mfma_f32_16x16x32_bf16 a[160:163],v[144:147],v[176:179],a[160:163]\n"
"v_mfma_f32_16x16x32_bf16 a[160:163],v[208:211],v[240:243],a[160:163]\n"
"v_mfma_f32_16x16x32_bf16 a[168:171],v[208:211],v[244:247],a[168:171]\n"
"s_mov_b32 s76,262912\n"
"s_add_u32 m0,s79,4096\n"
"v_mfma_f32_16x16x32_bf16 a[168:171],v[144:147],v[180:183],a[168:171]\n"
"buffer_load_dwordx4 v106,s[16:19],s76 offen offset:0 lds\n"
"v_mfma_f32_16x16x32_bf16 a[176:179],v[144:147],v[184:187],a[176:179]\n"
"v_mfma_f32_16x16x32_bf16 a[176:179],v[208:211],v[248:251],a[176:179]\n"
"v_mfma_f32_16x16x32_bf16 a[184:187],v[208:211],v[252:255],a[184:187]\n"
"v_mfma_f32_16x16x32_bf16 a[184:187],v[144:147],v[188:191],a[184:187]\n"
"v_mfma_f32_16x16x32_bf16 a[132:135],v[148:151],v[160:163],a[132:135]\n"
"buffer_load_dwordx4 v107,s[16:19],s76 offen offset:1024 lds\n"
"v_mfma_f32_16x16x32_bf16 a[132:135],v[212:215],v[224:227],a[132:135]\n"
"v_mfma_f32_16x16x32_bf16 a[140:143],v[212:215],v[228:231],a[140:143]\n"
"v_mfma_f32_16x16x32_bf16 a[140:143],v[148:151],v[164:167],a[140:143]\n"
"v_mfma_f32_16x16x32_bf16 a[148:151],v[148:151],v[168:171],a[148:151]\n"
"v_mfma_f32_16x16x32_bf16 a[148:151],v[212:215],v[232:235],a[148:151]\n"
"buffer_load_dwordx4 v108,s[16:19],s76 offen offset:2048 lds\n"
"v_mfma_f32_16x16x32_bf16 a[156:159],v[212:215],v[236:239],a[156:159]\n"
"v_mfma_f32_16x16x32_bf16 a[156:159],v[148:151],v[172:175],a[156:159]\n"
"v_mfma_f32_16x16x32_bf16 a[164:167],v[148:151],v[176:179],a[164:167]\n"
"v_mfma_f32_16x16x32_bf16 a[164:167],v[212:215],v[240:243],a[164:167]\n"
"v_mfma_f32_16x16x32_bf16 a[172:175],v[212:215],v[244:247],a[172:175]\n"
"buffer_load_dwordx4 v109,s[16:19],s76 offen offset:3072 lds\n"
"v_mfma_f32_16x16x32_bf16 a[172:175],v[148:151],v[180:183],a[172:175]\n"
"s_mov_b32 s87,3145728\n"
"v_mfma_f32_16x16x32_bf16 a[180:183],v[148:151],v[184:187],a[180:183]\n"
"buffer_load_dword v115,v112,s[40:43],s87 offen\n"
"v_mfma_f32_16x16x32_bf16 a[180:183],v[212:215],v[248:251],a[180:183]\n"
"s_mov_b32 s87,4194304\n"
"v_mfma_f32_16x16x32_bf16 a[188:191],v[212:215],v[252:255],a[188:191]\n"
"buffer_load_dword v115,v112,s[40:43],s87 offen\n"
"v_mfma_f32_16x16x32_bf16 a[188:191],v[148:151],v[188:191],a[188:191]\n"
"s_mov_b32 s87,5242880\n"
"s_waitcnt vmcnt(33)\n"
"s_barrier\n"
"v_mfma_f32_16x16x32_bf16 a[192:195],v[216:219],v[224:227],a[192:195]\n"
"ds_read_b128 v[152:155],v80 offset:45056\n"
"buffer_load_dword v115,v112,s[40:43],s87 offen\n"
"v_mfma_f32_16x16x32_bf16 a[196:199],v[220:223],v[224:227],a[196:199]\n"
"ds_read_b128 v[156:159],v80 offset:47104\n"
"v_mfma_f32_16x16x32_bf16 a[204:207],v[220:223],v[228:231],a[204:207]\n"
"ds_read_b64_tr_b16 v[160:161],v98 offset:49152\n"
"v_mfma_f32_16x16x32_bf16 a[200:203],v[216:219],v[228:231],a[200:203]\n"
"ds_read_b64_tr_b16 v[162:163],v98 offset:50176\n"
"v_mfma_f32_16x16x32_bf16 a[208:211],v[216:219],v[232:235],a[208:211]\n"
"ds_read_b64_tr_b16 v[164:165],v99 offset:49152\n"
"v_mfma_f32_16x16x32_bf16 a[212:215],v[220:223],v[232:235],a[212:215]\n"
"ds_read_b64_tr_b16 v[166:167],v99 offset:50176\n"
"v_mfma_f32_16x16x32_bf16 a[220:223],v[220:223],v[236:239],a[220:223]\n"
"ds_read_b64_tr_b16 v[168:169],v100 offset:49152\n"
"v_mfma_f32_16x16x32_bf16 a[216:219],v[216:219],v[236:239],a[216:219]\n"
"ds_read_b64_tr_b16 v[170:171],v100 offset:50176\n"
"v_mfma_f32_16x16x32_bf16 a[224:227],v[216:219],v[240:243],a[224:227]\n"
"ds_read_b64_tr_b16 v[172:173],v101 offset:49152\n"
"v_mfma_f32_16x16x32_bf16 a[228:231],v[220:223],v[240:243],a[228:231]\n"
"ds_read_b64_tr_b16 v[174:175],v101 offset:50176\n"
"v_mfma_f32_16x16x32_bf16 a[236:239],v[220:223],v[244:247],a[236:239]\n"
"ds_read_b64_tr_b16 v[176:177],v102 offset:49152\n"
"v_mfma_f32_16x16x32_bf16 a[232:235],v[216:219],v[244:247],a[232:235]\n"
"ds_read_b64_tr_b16 v[178:179],v102 offset:50176\n"
"v_mfma_f32_16x16x32_bf16 a[240:243],v[216:219],v[248:251],a[240:243]\n"
"ds_read_b64_tr_b16 v[180:181],v103 offset:49152\n"
"v_mfma_f32_16x16x32_bf16 a[244:247],v[220:223],v[248:251],a[244:247]\n"
"ds_read_b64_tr_b16 v[182:183],v103 offset:50176\n"
"v_mfma_f32_16x16x32_bf16 a[252:255],v[220:223],v[252:255],a[252:255]\n"
"ds_read_b64_tr_b16 v[184:185],v104 offset:49152\n"
"ds_read_b64_tr_b16 v[186:187],v104 offset:50176\n"
"v_mfma_f32_16x16x32_bf16 a[248:251],v[216:219],v[252:255],a[248:251]\n"
"s_waitcnt vmcnt(19)\n"
"s_waitcnt lgkmcnt(10)\n"
"v_mfma_f32_16x16x32_bf16 a[192:195],v[152:155],v[160:163],a[192:195]\n"
"ds_read_b64_tr_b16 v[188:189],v105 offset:49152\n"
"ds_read_b64_tr_b16 v[190:191],v105 offset:50176\n"
"v_mfma_f32_16x16x32_bf16 a[196:199],v[156:159],v[160:163],a[196:199]\n"
"buffer_store_dwordx2 v[124:125],off,s[24:27],s57 sc1\n"
"v_mfma_f32_16x16x32_bf16 a[204:207],v[156:159],v[164:167],a[204:207]\n"
"ds_read_b128 v[128:131],v80 offset:32768\n"
"v_mfma_f32_16x16x32_bf16 a[200:203],v[152:155],v[164:167],a[200:203]\n"
"ds_read_b128 v[192:195],v81 offset:32768\n"
"s_waitcnt lgkmcnt(10)\n"
"v_mfma_f32_16x16x32_bf16 a[208:211],v[152:155],v[168:171],a[208:211]\n"
"ds_read_b64_tr_b16 v[224:225],v98 offset:32768\n"
"v_mfma_f32_16x16x32_bf16 a[212:215],v[156:159],v[168:171],a[212:215]\n"
"ds_read_b64_tr_b16 v[226:227],v98 offset:33792\n"
"v_mfma_f32_16x16x32_bf16 a[220:223],v[156:159],v[172:175],a[220:223]\n"
"ds_read_b64_tr_b16 v[228:229],v99 offset:32768\n"
"v_mfma_f32_16x16x32_bf16 a[216:219],v[152:155],v[172:175],a[216:219]\n"
"ds_read_b64_tr_b16 v[230:231],v99 offset:33792\n"
"s_waitcnt lgkmcnt(10)\n"
"v_mfma_f32_16x16x32_bf16 a[224:227],v[152:155],v[176:179],a[224:227]\n"
"ds_read_b64_tr_b16 v[232:233],v100 offset:32768\n"
"v_mfma_f32_16x16x32_bf16 a[228:231],v[156:159],v[176:179],a[228:231]\n"
"ds_read_b64_tr_b16 v[234:235],v100 offset:33792\n"
"v_mfma_f32_16x16x32_bf16 a[236:239],v[156:159],v[180:183],a[236:239]\n"
"ds_read_b64_tr_b16 v[236:237],v101 offset:32768\n"
"v_mfma_f32_16x16x32_bf16 a[232:235],v[152:155],v[180:183],a[232:235]\n"
"ds_read_b64_tr_b16 v[238:239],v101 offset:33792\n"
"s_waitcnt lgkmcnt(10)\n"
"v_mfma_f32_16x16x32_bf16 a[240:243],v[152:155],v[184:187],a[240:243]\n"
"ds_read_b64_tr_b16 v[240:241],v102 offset:32768\n"
"v_mfma_f32_16x16x32_bf16 a[244:247],v[156:159],v[184:187],a[244:247]\n"
"ds_read_b64_tr_b16 v[242:243],v102 offset:33792\n"
"v_mfma_f32_16x16x32_bf16 a[252:255],v[156:159],v[188:191],a[252:255]\n"
"ds_read_b64_tr_b16 v[244:245],v103 offset:32768\n"
"v_mfma_f32_16x16x32_bf16 a[248:251],v[152:155],v[188:191],a[248:251]\n"
"ds_read_b64_tr_b16 v[246:247],v103 offset:33792\n"
"s_waitcnt lgkmcnt(8)\n"
"v_mfma_f32_16x16x32_bf16 a[0:3],v[128:131],v[160:163],a[0:3]\n"
"ds_read_b64_tr_b16 v[248:249],v104 offset:32768\n"
"v_mfma_f32_16x16x32_bf16 a[0:3],v[192:195],v[224:227],a[0:3]\n"
"ds_read_b64_tr_b16 v[250:251],v104 offset:33792\n"
"v_mfma_f32_16x16x32_bf16 a[8:11],v[192:195],v[228:231],a[8:11]\n"
"ds_read_b64_tr_b16 v[252:253],v105 offset:32768\n"
"v_mfma_f32_16x16x32_bf16 a[8:11],v[128:131],v[164:167],a[8:11]\n"
"ds_read_b64_tr_b16 v[254:255],v105 offset:33792\n"
"s_waitcnt lgkmcnt(8)\n"
"v_mfma_f32_16x16x32_bf16 a[16:19],v[128:131],v[168:171],a[16:19]\n"
"ds_read_b128 v[132:135],v80 offset:34816\n"
"v_mfma_f32_16x16x32_bf16 a[16:19],v[192:195],v[232:235],a[16:19]\n"
"ds_read_b128 v[196:199],v81 offset:34816\n"
"v_mfma_f32_16x16x32_bf16 a[24:27],v[192:195],v[236:239],a[24:27]\n"
"ds_read_b128 v[136:139],v80 offset:36864\n"
"v_mfma_f32_16x16x32_bf16 a[24:27],v[128:131],v[172:175],a[24:27]\n"
"ds_read_b128 v[200:203],v81 offset:36864\n"
"s_waitcnt lgkmcnt(8)\n"
"v_mfma_f32_16x16x32_bf16 a[32:35],v[128:131],v[176:179],a[32:35]\n"
"ds_read_b128 v[140:143],v80 offset:38912\n"
"v_mfma_f32_16x16x32_bf16 a[32:35],v[192:195],v[240:243],a[32:35]\n"
"ds_read_b128 v[204:207],v81 offset:38912\n"
"v_mfma_f32_16x16x32_bf16 a[40:43],v[192:195],v[244:247],a[40:43]\n"
"ds_read_b128 v[144:147],v80 offset:40960\n"
"v_mfma_f32_16x16x32_bf16 a[40:43],v[128:131],v[180:183],a[40:43]\n"
"ds_read_b128 v[208:211],v81 offset:40960\n"
"s_waitcnt lgkmcnt(8)\n"
"v_mfma_f32_16x16x32_bf16 a[48:51],v[128:131],v[184:187],a[48:51]\n"
"ds_read_b128 v[148:151],v80 offset:43008\n"
"v_mfma_f32_16x16x32_bf16 a[48:51],v[192:195],v[248:251],a[48:51]\n"
"ds_read_b128 v[212:215],v81 offset:43008\n"
"v_mfma_f32_16x16x32_bf16 a[56:59],v[192:195],v[252:255],a[56:59]\n"
"ds_read_b128 v[216:219],v81 offset:45056\n"
"v_mfma_f32_16x16x32_bf16 a[56:59],v[128:131],v[188:191],a[56:59]\n"
"ds_read_b128 v[220:223],v81 offset:47104\n"
"s_waitcnt lgkmcnt(10)\n"
"v_mfma_f32_16x16x32_bf16 a[4:7],v[132:135],v[160:163],a[4:7]\n"
"v_mfma_f32_16x16x32_bf16 a[4:7],v[196:199],v[224:227],a[4:7]\n"
"v_mfma_f32_16x16x32_bf16 a[12:15],v[196:199],v[228:231],a[12:15]\n"
"v_mfma_f32_16x16x32_bf16 a[12:15],v[132:135],v[164:167],a[12:15]\n"
"v_mfma_f32_16x16x32_bf16 a[20:23],v[132:135],v[168:171],a[20:23]\n"
"s_waitcnt lgkmcnt(0)\n"
"s_barrier\n"
"v_mfma_f32_16x16x32_bf16 a[20:23],v[196:199],v[232:235],a[20:23]\n"
"s_mov_b32 s77,3145728\n"
"s_add_u32 m0,s78,147456\n"
"v_mfma_f32_16x16x32_bf16 a[28:31],v[196:199],v[236:239],a[28:31]\n"
"buffer_load_dwordx4 v110,s[20:23],s77 offen offset:0 lds\n"
"v_mfma_f32_16x16x32_bf16 a[28:31],v[132:135],v[172:175],a[28:31]\n"
"v_mfma_f32_16x16x32_bf16 a[36:39],v[132:135],v[176:179],a[36:39]\n"
"v_mfma_f32_16x16x32_bf16 a[36:39],v[196:199],v[240:243],a[36:39]\n"
"v_mfma_f32_16x16x32_bf16 a[44:47],v[196:199],v[244:247],a[44:47]\n"
"buffer_load_dwordx4 v111,s[20:23],s77 offen offset:1024 lds\n"
"v_mfma_f32_16x16x32_bf16 a[44:47],v[132:135],v[180:183],a[44:47]\n"
"v_mfma_f32_16x16x32_bf16 a[52:55],v[132:135],v[184:187],a[52:55]\n"
"v_mfma_f32_16x16x32_bf16 a[52:55],v[196:199],v[248:251],a[52:55]\n"
"v_mfma_f32_16x16x32_bf16 a[60:63],v[196:199],v[252:255],a[60:63]\n"
"buffer_load_dwordx4 v113,s[20:23],s77 offen offset:2048 lds\n"
"v_mfma_f32_16x16x32_bf16 a[60:63],v[132:135],v[188:191],a[60:63]\n"
"v_mfma_f32_16x16x32_bf16 a[64:67],v[136:139],v[160:163],a[64:67]\n"
"v_mfma_f32_16x16x32_bf16 a[64:67],v[200:203],v[224:227],a[64:67]\n"
"v_mfma_f32_16x16x32_bf16 a[72:75],v[200:203],v[228:231],a[72:75]\n"
"buffer_load_dwordx4 v114,s[20:23],s77 offen offset:3072 lds\n"
"v_mfma_f32_16x16x32_bf16 a[72:75],v[136:139],v[164:167],a[72:75]\n"
"v_mfma_f32_16x16x32_bf16 a[80:83],v[136:139],v[168:171],a[80:83]\n"
"v_mfma_f32_16x16x32_bf16 a[80:83],v[200:203],v[232:235],a[80:83]\n"
"s_mov_b32 s77,3407872\n"
"s_add_u32 m0,s78,131072\n"
"v_mfma_f32_16x16x32_bf16 a[88:91],v[200:203],v[236:239],a[88:91]\n"
"buffer_load_dwordx4 v110,s[20:23],s77 offen offset:0 lds\n"
"v_mfma_f32_16x16x32_bf16 a[88:91],v[136:139],v[172:175],a[88:91]\n"
"v_mfma_f32_16x16x32_bf16 a[96:99],v[136:139],v[176:179],a[96:99]\n"
"v_mfma_f32_16x16x32_bf16 a[96:99],v[200:203],v[240:243],a[96:99]\n"
"v_mfma_f32_16x16x32_bf16 a[104:107],v[200:203],v[244:247],a[104:107]\n"
"buffer_load_dwordx4 v111,s[20:23],s77 offen offset:1024 lds\n"
"v_mfma_f32_16x16x32_bf16 a[104:107],v[136:139],v[180:183],a[104:107]\n"
"v_mfma_f32_16x16x32_bf16 a[112:115],v[136:139],v[184:187],a[112:115]\n"
"v_mfma_f32_16x16x32_bf16 a[112:115],v[200:203],v[248:251],a[112:115]\n"
"v_mfma_f32_16x16x32_bf16 a[120:123],v[200:203],v[252:255],a[120:123]\n"
"buffer_load_dwordx4 v113,s[20:23],s77 offen offset:2048 lds\n"
"v_mfma_f32_16x16x32_bf16 a[120:123],v[136:139],v[188:191],a[120:123]\n"
"v_mfma_f32_16x16x32_bf16 a[68:71],v[140:143],v[160:163],a[68:71]\n"
"v_mfma_f32_16x16x32_bf16 a[68:71],v[204:207],v[224:227],a[68:71]\n"
"v_mfma_f32_16x16x32_bf16 a[76:79],v[204:207],v[228:231],a[76:79]\n"
"buffer_load_dwordx4 v114,s[20:23],s77 offen offset:3072 lds\n"
"v_mfma_f32_16x16x32_bf16 a[76:79],v[140:143],v[164:167],a[76:79]\n"
"v_mfma_f32_16x16x32_bf16 a[84:87],v[140:143],v[168:171],a[84:87]\n"
"v_mfma_f32_16x16x32_bf16 a[84:87],v[204:207],v[232:235],a[84:87]\n"
"v_mfma_f32_16x16x32_bf16 a[92:95],v[204:207],v[236:239],a[92:95]\n"
"s_mov_b32 s76,896\n"
"s_add_u32 m0,s79,32768\n"
"v_mfma_f32_16x16x32_bf16 a[92:95],v[140:143],v[172:175],a[92:95]\n"
"buffer_load_dwordx4 v106,s[16:19],s76 offen offset:0 lds\n"
"v_mfma_f32_16x16x32_bf16 a[100:103],v[140:143],v[176:179],a[100:103]\n"
"v_mfma_f32_16x16x32_bf16 a[100:103],v[204:207],v[240:243],a[100:103]\n"
"v_mfma_f32_16x16x32_bf16 a[108:111],v[204:207],v[244:247],a[108:111]\n"
"v_mfma_f32_16x16x32_bf16 a[108:111],v[140:143],v[180:183],a[108:111]\n"
"v_mfma_f32_16x16x32_bf16 a[116:119],v[140:143],v[184:187],a[116:119]\n"
"buffer_load_dwordx4 v107,s[16:19],s76 offen offset:1024 lds\n"
"v_mfma_f32_16x16x32_bf16 a[116:119],v[204:207],v[248:251],a[116:119]\n"
"v_mfma_f32_16x16x32_bf16 a[124:127],v[204:207],v[252:255],a[124:127]\n"
"v_mfma_f32_16x16x32_bf16 a[124:127],v[140:143],v[188:191],a[124:127]\n"
"v_mfma_f32_16x16x32_bf16 a[128:131],v[144:147],v[160:163],a[128:131]\n"
"v_mfma_f32_16x16x32_bf16 a[128:131],v[208:211],v[224:227],a[128:131]\n"
"buffer_load_dwordx4 v108,s[16:19],s76 offen offset:2048 lds\n"
"v_mfma_f32_16x16x32_bf16 a[136:139],v[208:211],v[228:231],a[136:139]\n"
"v_mfma_f32_16x16x32_bf16 a[136:139],v[144:147],v[164:167],a[136:139]\n"
"v_mfma_f32_16x16x32_bf16 a[144:147],v[144:147],v[168:171],a[144:147]\n"
"v_mfma_f32_16x16x32_bf16 a[144:147],v[208:211],v[232:235],a[144:147]\n"
"v_mfma_f32_16x16x32_bf16 a[152:155],v[208:211],v[236:239],a[152:155]\n"
"buffer_load_dwordx4 v109,s[16:19],s76 offen offset:3072 lds\n"
"v_mfma_f32_16x16x32_bf16 a[152:155],v[144:147],v[172:175],a[152:155]\n"
"v_mfma_f32_16x16x32_bf16 a[160:163],v[144:147],v[176:179],a[160:163]\n"
"v_mfma_f32_16x16x32_bf16 a[160:163],v[208:211],v[240:243],a[160:163]\n"
"v_mfma_f32_16x16x32_bf16 a[168:171],v[208:211],v[244:247],a[168:171]\n"
"s_mov_b32 s76,263040\n"
"s_add_u32 m0,s79,36864\n"
"v_mfma_f32_16x16x32_bf16 a[168:171],v[144:147],v[180:183],a[168:171]\n"
"buffer_load_dwordx4 v106,s[16:19],s76 offen offset:0 lds\n"
"v_mfma_f32_16x16x32_bf16 a[176:179],v[144:147],v[184:187],a[176:179]\n"
"v_mfma_f32_16x16x32_bf16 a[176:179],v[208:211],v[248:251],a[176:179]\n"
"v_mfma_f32_16x16x32_bf16 a[184:187],v[208:211],v[252:255],a[184:187]\n"
"v_mfma_f32_16x16x32_bf16 a[184:187],v[144:147],v[188:191],a[184:187]\n"
"v_mfma_f32_16x16x32_bf16 a[132:135],v[148:151],v[160:163],a[132:135]\n"
"buffer_load_dwordx4 v107,s[16:19],s76 offen offset:1024 lds\n"
"v_mfma_f32_16x16x32_bf16 a[132:135],v[212:215],v[224:227],a[132:135]\n"
"v_mfma_f32_16x16x32_bf16 a[140:143],v[212:215],v[228:231],a[140:143]\n"
"v_mfma_f32_16x16x32_bf16 a[140:143],v[148:151],v[164:167],a[140:143]\n"
"v_mfma_f32_16x16x32_bf16 a[148:151],v[148:151],v[168:171],a[148:151]\n"
"v_mfma_f32_16x16x32_bf16 a[148:151],v[212:215],v[232:235],a[148:151]\n"
"buffer_load_dwordx4 v108,s[16:19],s76 offen offset:2048 lds\n"
"v_mfma_f32_16x16x32_bf16 a[156:159],v[212:215],v[236:239],a[156:159]\n"
"v_mfma_f32_16x16x32_bf16 a[156:159],v[148:151],v[172:175],a[156:159]\n"
"v_mfma_f32_16x16x32_bf16 a[164:167],v[148:151],v[176:179],a[164:167]\n"
"v_mfma_f32_16x16x32_bf16 a[164:167],v[212:215],v[240:243],a[164:167]\n"
"v_mfma_f32_16x16x32_bf16 a[172:175],v[212:215],v[244:247],a[172:175]\n"
"buffer_load_dwordx4 v109,s[16:19],s76 offen offset:3072 lds\n"
"v_mfma_f32_16x16x32_bf16 a[172:175],v[148:151],v[180:183],a[172:175]\n"
"s_mov_b32 s87,6291456\n"
"v_mfma_f32_16x16x32_bf16 a[180:183],v[148:151],v[184:187],a[180:183]\n"
"buffer_load_dword v115,v112,s[40:43],s87 offen\n"
"v_mfma_f32_16x16x32_bf16 a[180:183],v[212:215],v[248:251],a[180:183]\n"
"s_mov_b32 s87,7340032\n"
"v_mfma_f32_16x16x32_bf16 a[188:191],v[212:215],v[252:255],a[188:191]\n"
"buffer_load_dword v115,v112,s[40:43],s87 offen\n"
"v_mfma_f32_16x16x32_bf16 a[188:191],v[148:151],v[188:191],a[188:191]\n"
"s_waitcnt vmcnt(30)\n"
"s_barrier\n"
"v_mfma_f32_16x16x32_bf16 a[192:195],v[216:219],v[224:227],a[192:195]\n"
"ds_read_b128 v[152:155],v82 offset:12288\n"
"v_mfma_f32_16x16x32_bf16 a[196:199],v[220:223],v[224:227],a[196:199]\n"
"ds_read_b128 v[156:159],v82 offset:14336\n"
"v_mfma_f32_16x16x32_bf16 a[204:207],v[220:223],v[228:231],a[204:207]\n"
"ds_read_b64_tr_b16 v[160:161],v98 offset:0\n"
"v_mfma_f32_16x16x32_bf16 a[200:203],v[216:219],v[228:231],a[200:203]\n"
"ds_read_b64_tr_b16 v[162:163],v98 offset:1024\n"
"v_mfma_f32_16x16x32_bf16 a[208:211],v[216:219],v[232:235],a[208:211]\n"
"ds_read_b64_tr_b16 v[164:165],v99 offset:0\n"
"v_mfma_f32_16x16x32_bf16 a[212:215],v[220:223],v[232:235],a[212:215]\n"
"ds_read_b64_tr_b16 v[166:167],v99 offset:1024\n"
"v_mfma_f32_16x16x32_bf16 a[220:223],v[220:223],v[236:239],a[220:223]\n"
"ds_read_b64_tr_b16 v[168:169],v100 offset:0\n"
"v_mfma_f32_16x16x32_bf16 a[216:219],v[216:219],v[236:239],a[216:219]\n"
"ds_read_b64_tr_b16 v[170:171],v100 offset:1024\n"
"v_mfma_f32_16x16x32_bf16 a[224:227],v[216:219],v[240:243],a[224:227]\n"
"ds_read_b64_tr_b16 v[172:173],v101 offset:0\n"
"v_mfma_f32_16x16x32_bf16 a[228:231],v[220:223],v[240:243],a[228:231]\n"
"ds_read_b64_tr_b16 v[174:175],v101 offset:1024\n"
"v_mfma_f32_16x16x32_bf16 a[236:239],v[220:223],v[244:247],a[236:239]\n"
"ds_read_b64_tr_b16 v[176:177],v102 offset:0\n"
"v_mfma_f32_16x16x32_bf16 a[232:235],v[216:219],v[244:247],a[232:235]\n"
"ds_read_b64_tr_b16 v[178:179],v102 offset:1024\n"
"v_mfma_f32_16x16x32_bf16 a[240:243],v[216:219],v[248:251],a[240:243]\n"
"ds_read_b64_tr_b16 v[180:181],v103 offset:0\n"
"v_mfma_f32_16x16x32_bf16 a[244:247],v[220:223],v[248:251],a[244:247]\n"
"ds_read_b64_tr_b16 v[182:183],v103 offset:1024\n"
"v_mfma_f32_16x16x32_bf16 a[252:255],v[220:223],v[252:255],a[252:255]\n"
"ds_read_b64_tr_b16 v[184:185],v104 offset:0\n"
"v_mfma_f32_16x16x32_bf16 a[248:251],v[216:219],v[252:255],a[248:251]\n"
"ds_read_b64_tr_b16 v[186:187],v104 offset:1024\n"
"s_waitcnt lgkmcnt(10)\n"
"v_mfma_f32_16x16x32_bf16 a[192:195],v[152:155],v[160:163],a[192:195]\n"
"ds_read_b64_tr_b16 v[188:189],v105 offset:0\n"
"v_mfma_f32_16x16x32_bf16 a[196:199],v[156:159],v[160:163],a[196:199]\n"
"ds_read_b64_tr_b16 v[190:191],v105 offset:1024\n"
"v_mfma_f32_16x16x32_bf16 a[204:207],v[156:159],v[164:167],a[204:207]\n"
"ds_read_b128 v[128:131],v82 offset:0\n"
"v_mfma_f32_16x16x32_bf16 a[200:203],v[152:155],v[164:167],a[200:203]\n"
"ds_read_b128 v[192:195],v83 offset:0\n"
"s_waitcnt lgkmcnt(10)\n"
"v_mfma_f32_16x16x32_bf16 a[208:211],v[152:155],v[168:171],a[208:211]\n"
"ds_read_b64_tr_b16 v[224:225],v98 offset:16384\n"
"v_mfma_f32_16x16x32_bf16 a[212:215],v[156:159],v[168:171],a[212:215]\n"
"ds_read_b64_tr_b16 v[226:227],v98 offset:17408\n"
"v_mfma_f32_16x16x32_bf16 a[220:223],v[156:159],v[172:175],a[220:223]\n"
"ds_read_b64_tr_b16 v[228:229],v99 offset:16384\n"
"v_mfma_f32_16x16x32_bf16 a[216:219],v[152:155],v[172:175],a[216:219]\n"
"ds_read_b64_tr_b16 v[230:231],v99 offset:17408\n"
"s_waitcnt lgkmcnt(10)\n"
"v_mfma_f32_16x16x32_bf16 a[224:227],v[152:155],v[176:179],a[224:227]\n"
"ds_read_b64_tr_b16 v[232:233],v100 offset:16384\n"
"v_mfma_f32_16x16x32_bf16 a[228:231],v[156:159],v[176:179],a[228:231]\n"
"ds_read_b64_tr_b16 v[234:235],v100 offset:17408\n"
"v_mfma_f32_16x16x32_bf16 a[236:239],v[156:159],v[180:183],a[236:239]\n"
"ds_read_b64_tr_b16 v[236:237],v101 offset:16384\n"
"v_mfma_f32_16x16x32_bf16 a[232:235],v[152:155],v[180:183],a[232:235]\n"
"ds_read_b64_tr_b16 v[238:239],v101 offset:17408\n"
"s_waitcnt lgkmcnt(10)\n"
"v_mfma_f32_16x16x32_bf16 a[240:243],v[152:155],v[184:187],a[240:243]\n"
"ds_read_b64_tr_b16 v[240:241],v102 offset:16384\n"
"v_mfma_f32_16x16x32_bf16 a[244:247],v[156:159],v[184:187],a[244:247]\n"
"ds_read_b64_tr_b16 v[242:243],v102 offset:17408\n"
"v_mfma_f32_16x16x32_bf16 a[252:255],v[156:159],v[188:191],a[252:255]\n"
"ds_read_b64_tr_b16 v[244:245],v103 offset:16384\n"
"v_mfma_f32_16x16x32_bf16 a[248:251],v[152:155],v[188:191],a[248:251]\n"
"ds_read_b64_tr_b16 v[246:247],v103 offset:17408\n"
"s_waitcnt lgkmcnt(8)\n"
"v_mfma_f32_16x16x32_bf16 a[0:3],v[128:131],v[160:163],a[0:3]\n"
"ds_read_b64_tr_b16 v[248:249],v104 offset:16384\n"
"v_mfma_f32_16x16x32_bf16 a[0:3],v[192:195],v[224:227],a[0:3]\n"
"ds_read_b64_tr_b16 v[250:251],v104 offset:17408\n"
"v_mfma_f32_16x16x32_bf16 a[8:11],v[192:195],v[228:231],a[8:11]\n"
"ds_read_b64_tr_b16 v[252:253],v105 offset:16384\n"
"v_mfma_f32_16x16x32_bf16 a[8:11],v[128:131],v[164:167],a[8:11]\n"
"ds_read_b64_tr_b16 v[254:255],v105 offset:17408\n"
"s_waitcnt lgkmcnt(8)\n"
"v_mfma_f32_16x16x32_bf16 a[16:19],v[128:131],v[168:171],a[16:19]\n"
"ds_read_b128 v[132:135],v82 offset:2048\n"
"v_mfma_f32_16x16x32_bf16 a[16:19],v[192:195],v[232:235],a[16:19]\n"
"ds_read_b128 v[196:199],v83 offset:2048\n"
"v_mfma_f32_16x16x32_bf16 a[24:27],v[192:195],v[236:239],a[24:27]\n"
"ds_read_b128 v[136:139],v82 offset:4096\n"
"v_mfma_f32_16x16x32_bf16 a[24:27],v[128:131],v[172:175],a[24:27]\n"
"ds_read_b128 v[200:203],v83 offset:4096\n"
"s_waitcnt lgkmcnt(8)\n"
"v_mfma_f32_16x16x32_bf16 a[32:35],v[128:131],v[176:179],a[32:35]\n"
"ds_read_b128 v[140:143],v82 offset:6144\n"
"v_mfma_f32_16x16x32_bf16 a[32:35],v[192:195],v[240:243],a[32:35]\n"
"ds_read_b128 v[204:207],v83 offset:6144\n"
"v_mfma_f32_16x16x32_bf16 a[40:43],v[192:195],v[244:247],a[40:43]\n"
"ds_read_b128 v[144:147],v82 offset:8192\n"
"v_mfma_f32_16x16x32_bf16 a[40:43],v[128:131],v[180:183],a[40:43]\n"
"ds_read_b128 v[208:211],v83 offset:8192\n"
"s_waitcnt lgkmcnt(8)\n"
"v_mfma_f32_16x16x32_bf16 a[48:51],v[128:131],v[184:187],a[48:51]\n"
"ds_read_b128 v[148:151],v82 offset:10240\n"
"v_mfma_f32_16x16x32_bf16 a[48:51],v[192:195],v[248:251],a[48:51]\n"
"ds_read_b128 v[212:215],v83 offset:10240\n"
"v_mfma_f32_16x16x32_bf16 a[56:59],v[192:195],v[252:255],a[56:59]\n"
"ds_read_b128 v[216:219],v83 offset:12288\n"
"v_mfma_f32_16x16x32_bf16 a[56:59],v[128:131],v[188:191],a[56:59]\n"
"ds_read_b128 v[220:223],v83 offset:14336\n"
"s_waitcnt lgkmcnt(10)\n"
"v_mfma_f32_16x16x32_bf16 a[4:7],v[132:135],v[160:163],a[4:7]\n"
"v_mfma_f32_16x16x32_bf16 a[4:7],v[196:199],v[224:227],a[4:7]\n"
"buffer_load_dwordx4 v[74:77],v112,s[24:27],s58 offen sc1\n"
"v_mfma_f32_16x16x32_bf16 a[12:15],v[196:199],v[228:231],a[12:15]\n"
"v_mfma_f32_16x16x32_bf16 a[12:15],v[132:135],v[164:167],a[12:15]\n"
"v_mfma_f32_16x16x32_bf16 a[20:23],v[132:135],v[168:171],a[20:23]\n"
"s_waitcnt lgkmcnt(0)\n"
"s_barrier\n"
"v_mfma_f32_16x16x32_bf16 a[20:23],v[196:199],v[232:235],a[20:23]\n"
"s_mov_b32 s77,3670016\n"
"s_add_u32 m0,s78,98304\n"
"v_mfma_f32_16x16x32_bf16 a[28:31],v[196:199],v[236:239],a[28:31]\n"
"buffer_load_dwordx4 v110,s[20:23],s77 offen offset:0 lds\n"
"v_mfma_f32_16x16x32_bf16 a[28:31],v[132:135],v[172:175],a[28:31]\n"
"v_mfma_f32_16x16x32_bf16 a[36:39],v[132:135],v[176:179],a[36:39]\n"
"s_load_dwordx2 s[48:49],s[0:1],56\n"
"v_mfma_f32_16x16x32_bf16 a[36:39],v[196:199],v[240:243],a[36:39]\n"
"v_mfma_f32_16x16x32_bf16 a[44:47],v[196:199],v[244:247],a[44:47]\n"
"buffer_load_dwordx4 v111,s[20:23],s77 offen offset:1024 lds\n"
"v_mfma_f32_16x16x32_bf16 a[44:47],v[132:135],v[180:183],a[44:47]\n"
"v_mfma_f32_16x16x32_bf16 a[52:55],v[132:135],v[184:187],a[52:55]\n"
"v_mfma_f32_16x16x32_bf16 a[52:55],v[196:199],v[248:251],a[52:55]\n"
"v_mfma_f32_16x16x32_bf16 a[60:63],v[196:199],v[252:255],a[60:63]\n"
"buffer_load_dwordx4 v113,s[20:23],s77 offen offset:2048 lds\n"
"v_mfma_f32_16x16x32_bf16 a[60:63],v[132:135],v[188:191],a[60:63]\n"
"v_mfma_f32_16x16x32_bf16 a[64:67],v[136:139],v[160:163],a[64:67]\n"
"v_mfma_f32_16x16x32_bf16 a[64:67],v[200:203],v[224:227],a[64:67]\n"
"v_mfma_f32_16x16x32_bf16 a[72:75],v[200:203],v[228:231],a[72:75]\n"
"buffer_load_dwordx4 v114,s[20:23],s77 offen offset:3072 lds\n"
"v_mfma_f32_16x16x32_bf16 a[72:75],v[136:139],v[164:167],a[72:75]\n"
"v_mfma_f32_16x16x32_bf16 a[80:83],v[136:139],v[168:171],a[80:83]\n"
"v_mfma_f32_16x16x32_bf16 a[80:83],v[200:203],v[232:235],a[80:83]\n"
"s_mov_b32 s77,3932160\n"
"s_add_u32 m0,s78,114688\n"
"v_mfma_f32_16x16x32_bf16 a[88:91],v[200:203],v[236:239],a[88:91]\n"
"buffer_load_dwordx4 v110,s[20:23],s77 offen offset:0 lds\n"
"v_mfma_f32_16x16x32_bf16 a[88:91],v[136:139],v[172:175],a[88:91]\n"
"v_mfma_f32_16x16x32_bf16 a[96:99],v[136:139],v[176:179],a[96:99]\n"
"v_mfma_f32_16x16x32_bf16 a[96:99],v[200:203],v[240:243],a[96:99]\n"
"v_mfma_f32_16x16x32_bf16 a[104:107],v[200:203],v[244:247],a[104:107]\n"
"buffer_load_dwordx4 v111,s[20:23],s77 offen offset:1024 lds\n"
"v_mfma_f32_16x16x32_bf16 a[104:107],v[136:139],v[180:183],a[104:107]\n"
"v_mfma_f32_16x16x32_bf16 a[112:115],v[136:139],v[184:187],a[112:115]\n"
"v_mfma_f32_16x16x32_bf16 a[112:115],v[200:203],v[248:251],a[112:115]\n"
"v_mfma_f32_16x16x32_bf16 a[120:123],v[200:203],v[252:255],a[120:123]\n"
"buffer_load_dwordx4 v113,s[20:23],s77 offen offset:2048 lds\n"
"v_mfma_f32_16x16x32_bf16 a[120:123],v[136:139],v[188:191],a[120:123]\n"
"s_mov_b32 s76,s55\n"
"s_waitcnt vmcnt(7)\n"
"v_mfma_f32_16x16x32_bf16 a[68:71],v[140:143],v[160:163],a[68:71]\n"
"v_xor_b32 v116,s80,v74\n"
"v_xor_b32 v117,s81,v75\n"
"v_mfma_f32_16x16x32_bf16 a[68:71],v[204:207],v[224:227],a[68:71]\n"
"v_or_b32 v116,v116,v117\n"
"v_xor_b32 v117,s80,v76\n"
"v_mfma_f32_16x16x32_bf16 a[76:79],v[204:207],v[228:231],a[76:79]\n"
"buffer_load_dwordx4 v114,s[20:23],s77 offen offset:3072 lds\n"
"v_mfma_f32_16x16x32_bf16 a[76:79],v[140:143],v[164:167],a[76:79]\n"
"v_or_b32 v116,v116,v117\n"
"v_xor_b32 v117,s81,v77\n"
"v_mfma_f32_16x16x32_bf16 a[84:87],v[140:143],v[168:171],a[84:87]\n"
"v_or_b32 v116,v116,v117\n"
"v_cmp_ne_u32 vcc,0,v116\n"
"v_mfma_f32_16x16x32_bf16 a[84:87],v[204:207],v[232:235],a[84:87]\n"
"s_cbranch_vccnz .Lfk_xn1_s\n"
".Lfk_xn1_ok:\n"
"v_mfma_f32_16x16x32_bf16 a[92:95],v[204:207],v[236:239],a[92:95]\n"
"s_add_u32 m0,s78,65536\n"
"v_mfma_f32_16x16x32_bf16 a[92:95],v[140:143],v[172:175],a[92:95]\n"
"buffer_load_dwordx4 v112,s[24:27],s76 offen offset:0 sc1 lds\n"
"v_mfma_f32_16x16x32_bf16 a[100:103],v[140:143],v[176:179],a[100:103]\n"
"s_waitcnt lgkmcnt(0)\n"
"s_lshl2_add_u32 s88,s100,s52\n"
"v_mfma_f32_16x16x32_bf16 a[100:103],v[204:207],v[240:243],a[100:103]\n"
"s_mul_i32 s88,s88,7168\n"
"s_add_u32 s88,s88,0\n"
"v_mfma_f32_16x16x32_bf16 a[108:111],v[204:207],v[244:247],a[108:111]\n"
"s_add_u32 s48,s48,s88\n"
"s_addc_u32 s49,s49,0\n"
"s_mov_b32 s50,3584\n"
"v_mfma_f32_16x16x32_bf16 a[108:111],v[140:143],v[180:183],a[108:111]\n"
"s_mov_b32 s51,0x20000\n"
"v_lshlrev_b32 v8,3,v112\n"
"v_mfma_f32_16x16x32_bf16 a[116:119],v[140:143],v[184:187],a[116:119]\n"
"buffer_load_dwordx4 v112,s[24:27],s76 offen offset:1024 sc1 lds\n"
"v_mfma_f32_16x16x32_bf16 a[116:119],v[204:207],v[248:251],a[116:119]\n"
"v_mfma_f32_16x16x32_bf16 a[124:127],v[204:207],v[252:255],a[124:127]\n"
"v_mfma_f32_16x16x32_bf16 a[124:127],v[140:143],v[188:191],a[124:127]\n"
"v_mfma_f32_16x16x32_bf16 a[128:131],v[144:147],v[160:163],a[128:131]\n"
"v_mfma_f32_16x16x32_bf16 a[128:131],v[208:211],v[224:227],a[128:131]\n"
"buffer_load_dwordx4 v112,s[24:27],s76 offen offset:2048 sc1 lds\n"
"v_mfma_f32_16x16x32_bf16 a[136:139],v[208:211],v[228:231],a[136:139]\n"
"v_mfma_f32_16x16x32_bf16 a[136:139],v[144:147],v[164:167],a[136:139]\n"
"v_mfma_f32_16x16x32_bf16 a[144:147],v[144:147],v[168:171],a[144:147]\n"
"v_mfma_f32_16x16x32_bf16 a[144:147],v[208:211],v[232:235],a[144:147]\n"
"buffer_load_dwordx4 v112,s[24:27],s76 offen offset:3072 sc1 lds\n"
"v_mfma_f32_16x16x32_bf16 a[152:155],v[208:211],v[236:239],a[152:155]\n"
"s_xor_b32 s76,s76,16384\n"
"v_mfma_f32_16x16x32_bf16 a[152:155],v[144:147],v[172:175],a[152:155]\n"
"v_mfma_f32_16x16x32_bf16 a[160:163],v[144:147],v[176:179],a[160:163]\n"
"v_mfma_f32_16x16x32_bf16 a[160:163],v[208:211],v[240:243],a[160:163]\n"
"v_mfma_f32_16x16x32_bf16 a[168:171],v[208:211],v[244:247],a[168:171]\n"
"s_add_u32 m0,s78,81920\n"
"v_mfma_f32_16x16x32_bf16 a[168:171],v[144:147],v[180:183],a[168:171]\n"
"buffer_load_dwordx4 v112,s[24:27],s76 offen offset:0 sc1 lds\n"
"v_mfma_f32_16x16x32_bf16 a[176:179],v[144:147],v[184:187],a[176:179]\n"
"v_mfma_f32_16x16x32_bf16 a[176:179],v[208:211],v[248:251],a[176:179]\n"
"v_mfma_f32_16x16x32_bf16 a[184:187],v[208:211],v[252:255],a[184:187]\n"
"v_mfma_f32_16x16x32_bf16 a[184:187],v[144:147],v[188:191],a[184:187]\n"
"v_mfma_f32_16x16x32_bf16 a[132:135],v[148:151],v[160:163],a[132:135]\n"
"buffer_load_dwordx4 v112,s[24:27],s76 offen offset:1024 sc1 lds\n"
"v_mfma_f32_16x16x32_bf16 a[132:135],v[212:215],v[224:227],a[132:135]\n"
"v_mfma_f32_16x16x32_bf16 a[140:143],v[212:215],v[228:231],a[140:143]\n"
"v_mfma_f32_16x16x32_bf16 a[140:143],v[148:151],v[164:167],a[140:143]\n"
"v_mfma_f32_16x16x32_bf16 a[148:151],v[148:151],v[168:171],a[148:151]\n"
"v_mfma_f32_16x16x32_bf16 a[148:151],v[212:215],v[232:235],a[148:151]\n"
"buffer_load_dwordx4 v112,s[24:27],s76 offen offset:2048 sc1 lds\n"
"v_mfma_f32_16x16x32_bf16 a[156:159],v[212:215],v[236:239],a[156:159]\n"
"v_mfma_f32_16x16x32_bf16 a[156:159],v[148:151],v[172:175],a[156:159]\n"
"v_mfma_f32_16x16x32_bf16 a[164:167],v[148:151],v[176:179],a[164:167]\n"
"v_mfma_f32_16x16x32_bf16 a[164:167],v[212:215],v[240:243],a[164:167]\n"
"buffer_load_dwordx4 v112,s[24:27],s76 offen offset:3072 sc1 lds\n"
"v_mfma_f32_16x16x32_bf16 a[172:175],v[212:215],v[244:247],a[172:175]\n"
"s_xor_b32 s76,s76,49152\n"
"v_mfma_f32_16x16x32_bf16 a[172:175],v[148:151],v[180:183],a[172:175]\n"
"v_mfma_f32_16x16x32_bf16 a[180:183],v[148:151],v[184:187],a[180:183]\n"
"buffer_load_dword v115,v8,s[48:51],0 offen\n"
"v_mfma_f32_16x16x32_bf16 a[180:183],v[212:215],v[248:251],a[180:183]\n"
"v_mfma_f32_16x16x32_bf16 a[188:191],v[212:215],v[252:255],a[188:191]\n"
"v_mfma_f32_16x16x32_bf16 a[188:191],v[148:151],v[188:191],a[188:191]\n"
"s_barrier\n"
"v_mfma_f32_16x16x32_bf16 a[192:195],v[216:219],v[224:227],a[192:195]\n"
"ds_read_b128 v[152:155],v80 offset:12288\n"
"v_mfma_f32_16x16x32_bf16 a[196:199],v[220:223],v[224:227],a[196:199]\n"
"ds_read_b128 v[156:159],v80 offset:14336\n"
"v_mfma_f32_16x16x32_bf16 a[204:207],v[220:223],v[228:231],a[204:207]\n"
"ds_read_b64_tr_b16 v[160:161],v98 offset:49152\n"
"v_mfma_f32_16x16x32_bf16 a[200:203],v[216:219],v[228:231],a[200:203]\n"
"ds_read_b64_tr_b16 v[162:163],v98 offset:50176\n"
"v_mfma_f32_16x16x32_bf16 a[208:211],v[216:219],v[232:235],a[208:211]\n"
"ds_read_b64_tr_b16 v[164:165],v99 offset:49152\n"
"v_mfma_f32_16x16x32_bf16 a[212:215],v[220:223],v[232:235],a[212:215]\n"
"ds_read_b64_tr_b16 v[166:167],v99 offset:50176\n"
"v_mfma_f32_16x16x32_bf16 a[220:223],v[220:223],v[236:239],a[220:223]\n"
"ds_read_b64_tr_b16 v[168:169],v100 offset:49152\n"
"v_mfma_f32_16x16x32_bf16 a[216:219],v[216:219],v[236:239],a[216:219]\n"
"ds_read_b64_tr_b16 v[170:171],v100 offset:50176\n"
"v_mfma_f32_16x16x32_bf16 a[224:227],v[216:219],v[240:243],a[224:227]\n"
"ds_read_b64_tr_b16 v[172:173],v101 offset:49152\n"
"v_mfma_f32_16x16x32_bf16 a[228:231],v[220:223],v[240:243],a[228:231]\n"
"ds_read_b64_tr_b16 v[174:175],v101 offset:50176\n"
"v_mfma_f32_16x16x32_bf16 a[236:239],v[220:223],v[244:247],a[236:239]\n"
"ds_read_b64_tr_b16 v[176:177],v102 offset:49152\n"
"v_mfma_f32_16x16x32_bf16 a[232:235],v[216:219],v[244:247],a[232:235]\n"
"ds_read_b64_tr_b16 v[178:179],v102 offset:50176\n"
"v_mfma_f32_16x16x32_bf16 a[240:243],v[216:219],v[248:251],a[240:243]\n"
"ds_read_b64_tr_b16 v[180:181],v103 offset:49152\n"
"v_mfma_f32_16x16x32_bf16 a[244:247],v[220:223],v[248:251],a[244:247]\n"
"ds_read_b64_tr_b16 v[182:183],v103 offset:50176\n"
"ds_read_b64_tr_b16 v[184:185],v104 offset:49152\n"
"v_mfma_f32_16x16x32_bf16 a[252:255],v[220:223],v[252:255],a[252:255]\n"
"v_mov_b32 v110,v72\n"
"v_mov_b32 v111,v73\n"
"v_mfma_f32_16x16x32_bf16 a[248:251],v[216:219],v[252:255],a[248:251]\n"
"ds_read_b64_tr_b16 v[186:187],v104 offset:50176\n"
"s_waitcnt lgkmcnt(10)\n"
"v_mfma_f32_16x16x32_bf16 a[192:195],v[152:155],v[160:163],a[192:195]\n"
"ds_read_b64_tr_b16 v[188:189],v105 offset:49152\n"
"v_mfma_f32_16x16x32_bf16 a[196:199],v[156:159],v[160:163],a[196:199]\n"
"ds_read_b64_tr_b16 v[190:191],v105 offset:50176\n"
"v_mfma_f32_16x16x32_bf16 a[204:207],v[156:159],v[164:167],a[204:207]\n"
"ds_read_b128 v[128:131],v80 offset:0\n"
"v_mfma_f32_16x16x32_bf16 a[200:203],v[152:155],v[164:167],a[200:203]\n"
"ds_read_b128 v[192:195],v81 offset:0\n"
"s_waitcnt lgkmcnt(10)\n"
"v_mfma_f32_16x16x32_bf16 a[208:211],v[152:155],v[168:171],a[208:211]\n"
"ds_read_b64_tr_b16 v[224:225],v98 offset:32768\n"
"v_mfma_f32_16x16x32_bf16 a[212:215],v[156:159],v[168:171],a[212:215]\n"
"ds_read_b64_tr_b16 v[226:227],v98 offset:33792\n"
"v_mfma_f32_16x16x32_bf16 a[220:223],v[156:159],v[172:175],a[220:223]\n"
"ds_read_b64_tr_b16 v[228:229],v99 offset:32768\n"
"v_mfma_f32_16x16x32_bf16 a[216:219],v[152:155],v[172:175],a[216:219]\n"
"ds_read_b64_tr_b16 v[230:231],v99 offset:33792\n"
"s_waitcnt lgkmcnt(10)\n"
"v_mfma_f32_16x16x32_bf16 a[224:227],v[152:155],v[176:179],a[224:227]\n"
"ds_read_b64_tr_b16 v[232:233],v100 offset:32768\n"
"v_mfma_f32_16x16x32_bf16 a[228:231],v[156:159],v[176:179],a[228:231]\n"
"ds_read_b64_tr_b16 v[234:235],v100 offset:33792\n"
"v_mfma_f32_16x16x32_bf16 a[236:239],v[156:159],v[180:183],a[236:239]\n"
"ds_read_b64_tr_b16 v[236:237],v101 offset:32768\n"
"v_mfma_f32_16x16x32_bf16 a[232:235],v[152:155],v[180:183],a[232:235]\n"
"ds_read_b64_tr_b16 v[238:239],v101 offset:33792\n"
"s_waitcnt lgkmcnt(10)\n"
"v_mfma_f32_16x16x32_bf16 a[240:243],v[152:155],v[184:187],a[240:243]\n"
"ds_read_b64_tr_b16 v[240:241],v102 offset:32768\n"
"v_mfma_f32_16x16x32_bf16 a[244:247],v[156:159],v[184:187],a[244:247]\n"
"ds_read_b64_tr_b16 v[242:243],v102 offset:33792\n"
"v_mfma_f32_16x16x32_bf16 a[252:255],v[156:159],v[188:191],a[252:255]\n"
"ds_read_b64_tr_b16 v[244:245],v103 offset:32768\n"
"v_mfma_f32_16x16x32_bf16 a[248:251],v[152:155],v[188:191],a[248:251]\n"
"ds_read_b64_tr_b16 v[246:247],v103 offset:33792\n"
"s_waitcnt lgkmcnt(8)\n"
"v_mfma_f32_16x16x32_bf16 a[0:3],v[128:131],v[160:163],a[0:3]\n"
"ds_read_b64_tr_b16 v[248:249],v104 offset:32768\n"
"v_mfma_f32_16x16x32_bf16 a[0:3],v[192:195],v[224:227],a[0:3]\n"
"ds_read_b64_tr_b16 v[250:251],v104 offset:33792\n"
"v_mfma_f32_16x16x32_bf16 a[8:11],v[192:195],v[228:231],a[8:11]\n"
"ds_read_b64_tr_b16 v[252:253],v105 offset:32768\n"
"v_mfma_f32_16x16x32_bf16 a[8:11],v[128:131],v[164:167],a[8:11]\n"
"ds_read_b64_tr_b16 v[254:255],v105 offset:33792\n"
"s_waitcnt lgkmcnt(8)\n"
"v_mfma_f32_16x16x32_bf16 a[16:19],v[128:131],v[168:171],a[16:19]\n"
"ds_read_b128 v[132:135],v80 offset:2048\n"
"v_mfma_f32_16x16x32_bf16 a[16:19],v[192:195],v[232:235],a[16:19]\n"
"ds_read_b128 v[196:199],v81 offset:2048\n"
"v_mfma_f32_16x16x32_bf16 a[24:27],v[192:195],v[236:239],a[24:27]\n"
"ds_read_b128 v[136:139],v80 offset:4096\n"
"v_mfma_f32_16x16x32_bf16 a[24:27],v[128:131],v[172:175],a[24:27]\n"
"ds_read_b128 v[200:203],v81 offset:4096\n"
"s_waitcnt lgkmcnt(8)\n"
"v_mfma_f32_16x16x32_bf16 a[32:35],v[128:131],v[176:179],a[32:35]\n"
"ds_read_b128 v[140:143],v80 offset:6144\n"
"v_mfma_f32_16x16x32_bf16 a[32:35],v[192:195],v[240:243],a[32:35]\n"
"ds_read_b128 v[204:207],v81 offset:6144\n"
"v_mfma_f32_16x16x32_bf16 a[40:43],v[192:195],v[244:247],a[40:43]\n"
"ds_read_b128 v[144:147],v80 offset:8192\n"
"v_mfma_f32_16x16x32_bf16 a[40:43],v[128:131],v[180:183],a[40:43]\n"
"ds_read_b128 v[208:211],v81 offset:8192\n"
"s_waitcnt lgkmcnt(8)\n"
"v_mfma_f32_16x16x32_bf16 a[48:51],v[128:131],v[184:187],a[48:51]\n"
"ds_read_b128 v[148:151],v80 offset:10240\n"
"v_mfma_f32_16x16x32_bf16 a[48:51],v[192:195],v[248:251],a[48:51]\n"
"ds_read_b128 v[212:215],v81 offset:10240\n"
"v_mfma_f32_16x16x32_bf16 a[56:59],v[192:195],v[252:255],a[56:59]\n"
"ds_read_b128 v[216:219],v81 offset:12288\n"
"v_mfma_f32_16x16x32_bf16 a[56:59],v[128:131],v[188:191],a[56:59]\n"
"ds_read_b128 v[220:223],v81 offset:14336\n"
"s_waitcnt lgkmcnt(10)\n"
"v_mfma_f32_16x16x32_bf16 a[4:7],v[132:135],v[160:163],a[4:7]\n"
"v_mfma_f32_16x16x32_bf16 a[4:7],v[196:199],v[224:227],a[4:7]\n"
"v_mfma_f32_16x16x32_bf16 a[12:15],v[196:199],v[228:231],a[12:15]\n"
"v_mfma_f32_16x16x32_bf16 a[12:15],v[132:135],v[164:167],a[12:15]\n"
"v_mfma_f32_16x16x32_bf16 a[20:23],v[132:135],v[168:171],a[20:23]\n"
"s_waitcnt lgkmcnt(0)\n"
"s_barrier\n"
"v_mfma_f32_16x16x32_bf16 a[20:23],v[196:199],v[232:235],a[20:23]\n"
"s_add_u32 m0,s85,147456\n"
"v_mfma_f32_16x16x32_bf16 a[28:31],v[196:199],v[236:239],a[28:31]\n"
"buffer_load_dwordx4 v110,s[28:31],s86 offen lds\n"
"v_mfma_f32_16x16x32_bf16 a[28:31],v[132:135],v[172:175],a[28:31]\n"
"v_mfma_f32_16x16x32_bf16 a[36:39],v[132:135],v[176:179],a[36:39]\n"
"s_load_dwordx2 s[48:49],s[0:1],56\n"
"v_mfma_f32_16x16x32_bf16 a[36:39],v[196:199],v[240:243],a[36:39]\n"
"s_add_u32 m0,s85,148480\n"
"v_mfma_f32_16x16x32_bf16 a[44:47],v[196:199],v[244:247],a[44:47]\n"
"buffer_load_dwordx4 v111,s[28:31],s86 offen lds\n"
"v_mfma_f32_16x16x32_bf16 a[44:47],v[132:135],v[180:183],a[44:47]\n"
"s_xor_b32 s86,s86,262144\n"
"v_mfma_f32_16x16x32_bf16 a[52:55],v[132:135],v[184:187],a[52:55]\n"
"v_mfma_f32_16x16x32_bf16 a[52:55],v[196:199],v[248:251],a[52:55]\n"
"s_add_u32 m0,s85,155648\n"
"v_mfma_f32_16x16x32_bf16 a[60:63],v[196:199],v[252:255],a[60:63]\n"
"buffer_load_dwordx4 v110,s[28:31],s86 offen lds\n"
"v_mfma_f32_16x16x32_bf16 a[60:63],v[132:135],v[188:191],a[60:63]\n"
"v_mfma_f32_16x16x32_bf16 a[64:67],v[136:139],v[160:163],a[64:67]\n"
"v_mfma_f32_16x16x32_bf16 a[64:67],v[200:203],v[224:227],a[64:67]\n"
"s_add_u32 m0,s85,156672\n"
"v_mfma_f32_16x16x32_bf16 a[72:75],v[200:203],v[228:231],a[72:75]\n"
"buffer_load_dwordx4 v111,s[28:31],s86 offen lds\n"
"v_mfma_f32_16x16x32_bf16 a[72:75],v[136:139],v[164:167],a[72:75]\n"
"s_xor_b32 s86,s86,786432\n"
"v_mfma_f32_16x16x32_bf16 a[80:83],v[136:139],v[168:171],a[80:83]\n"
"v_mfma_f32_16x16x32_bf16 a[80:83],v[200:203],v[232:235],a[80:83]\n"
"s_add_u32 m0,s85,131072\n"
"v_mfma_f32_16x16x32_bf16 a[88:91],v[200:203],v[236:239],a[88:91]\n"
"buffer_load_dwordx4 v110,s[28:31],s86 offen lds\n"
"v_mfma_f32_16x16x32_bf16 a[88:91],v[136:139],v[172:175],a[88:91]\n"
"v_mfma_f32_16x16x32_bf16 a[96:99],v[136:139],v[176:179],a[96:99]\n"
"v_mfma_f32_16x16x32_bf16 a[96:99],v[200:203],v[240:243],a[96:99]\n"
"s_add_u32 m0,s85,132096\n"
"v_mfma_f32_16x16x32_bf16 a[104:107],v[200:203],v[244:247],a[104:107]\n"
"buffer_load_dwordx4 v111,s[28:31],s86 offen lds\n"
"v_mfma_f32_16x16x32_bf16 a[104:107],v[136:139],v[180:183],a[104:107]\n"
"s_xor_b32 s86,s86,262144\n"
"v_mfma_f32_16x16x32_bf16 a[112:115],v[136:139],v[184:187],a[112:115]\n"
"v_mfma_f32_16x16x32_bf16 a[112:115],v[200:203],v[248:251],a[112:115]\n"
"v_mfma_f32_16x16x32_bf16 a[120:123],v[200:203],v[252:255],a[120:123]\n"
"v_mfma_f32_16x16x32_bf16 a[120:123],v[136:139],v[188:191],a[120:123]\n"
"v_mfma_f32_16x16x32_bf16 a[68:71],v[140:143],v[160:163],a[68:71]\n"
"v_mfma_f32_16x16x32_bf16 a[68:71],v[204:207],v[224:227],a[68:71]\n"
"s_add_u32 m0,s85,139264\n"
"v_mfma_f32_16x16x32_bf16 a[76:79],v[204:207],v[228:231],a[76:79]\n"
"buffer_load_dwordx4 v110,s[28:31],s86 offen lds\n"
"v_mfma_f32_16x16x32_bf16 a[76:79],v[140:143],v[164:167],a[76:79]\n"
"v_mfma_f32_16x16x32_bf16 a[84:87],v[140:143],v[168:171],a[84:87]\n"
"v_mfma_f32_16x16x32_bf16 a[84:87],v[204:207],v[232:235],a[84:87]\n"
"s_add_u32 m0,s85,140288\n"
"v_mfma_f32_16x16x32_bf16 a[92:95],v[204:207],v[236:239],a[92:95]\n"
"buffer_load_dwordx4 v111,s[28:31],s86 offen lds\n"
"v_mfma_f32_16x16x32_bf16 a[92:95],v[140:143],v[172:175],a[92:95]\n"
"s_xor_b32 s86,s86,1835008\n"
"v_mfma_f32_16x16x32_bf16 a[100:103],v[140:143],v[176:179],a[100:103]\n"
"s_waitcnt lgkmcnt(0)\n"
"s_lshl2_add_u32 s88,s100,s52\n"
"v_mfma_f32_16x16x32_bf16 a[100:103],v[204:207],v[240:243],a[100:103]\n"
"s_mul_i32 s88,s88,7168\n"
"s_add_u32 m0,s78,0\n"
"v_mfma_f32_16x16x32_bf16 a[108:111],v[204:207],v[244:247],a[108:111]\n"
"buffer_load_dwordx4 v112,s[24:27],s76 offen offset:0 sc1 lds\n"
"v_mfma_f32_16x16x32_bf16 a[108:111],v[140:143],v[180:183],a[108:111]\n"
"s_add_u32 s88,s88,3584\n"
"s_add_u32 s48,s48,s88\n"
"s_addc_u32 s49,s49,0\n"
"v_mfma_f32_16x16x32_bf16 a[116:119],v[140:143],v[184:187],a[116:119]\n"
"s_mov_b32 s50,3584\n"
"s_mov_b32 s51,0x20000\n"
"v_mfma_f32_16x16x32_bf16 a[116:119],v[204:207],v[248:251],a[116:119]\n"
"v_lshlrev_b32 v8,3,v112\n"
"v_mfma_f32_16x16x32_bf16 a[124:127],v[204:207],v[252:255],a[124:127]\n"
"buffer_load_dwordx4 v112,s[24:27],s76 offen offset:1024 sc1 lds\n"
"v_mfma_f32_16x16x32_bf16 a[124:127],v[140:143],v[188:191],a[124:127]\n"
"v_mfma_f32_16x16x32_bf16 a[128:131],v[144:147],v[160:163],a[128:131]\n"
"v_mfma_f32_16x16x32_bf16 a[128:131],v[208:211],v[224:227],a[128:131]\n"
"v_mfma_f32_16x16x32_bf16 a[136:139],v[208:211],v[228:231],a[136:139]\n"
"buffer_load_dwordx4 v112,s[24:27],s76 offen offset:2048 sc1 lds\n"
"v_mfma_f32_16x16x32_bf16 a[136:139],v[144:147],v[164:167],a[136:139]\n"
"v_mfma_f32_16x16x32_bf16 a[144:147],v[144:147],v[168:171],a[144:147]\n"
"v_mfma_f32_16x16x32_bf16 a[144:147],v[208:211],v[232:235],a[144:147]\n"
"buffer_load_dwordx4 v112,s[24:27],s76 offen offset:3072 sc1 lds\n"
"v_mfma_f32_16x16x32_bf16 a[152:155],v[208:211],v[236:239],a[152:155]\n"
"s_xor_b32 s76,s76,16384\n"
"v_mfma_f32_16x16x32_bf16 a[152:155],v[144:147],v[172:175],a[152:155]\n"
"v_mfma_f32_16x16x32_bf16 a[160:163],v[144:147],v[176:179],a[160:163]\n"
"v_mfma_f32_16x16x32_bf16 a[160:163],v[208:211],v[240:243],a[160:163]\n"
"s_add_u32 m0,s78,16384\n"
"v_mfma_f32_16x16x32_bf16 a[168:171],v[208:211],v[244:247],a[168:171]\n"
"buffer_load_dwordx4 v112,s[24:27],s76 offen offset:0 sc1 lds\n"
"v_mfma_f32_16x16x32_bf16 a[168:171],v[144:147],v[180:183],a[168:171]\n"
"v_mfma_f32_16x16x32_bf16 a[176:179],v[144:147],v[184:187],a[176:179]\n"
"v_mfma_f32_16x16x32_bf16 a[176:179],v[208:211],v[248:251],a[176:179]\n"
"v_mfma_f32_16x16x32_bf16 a[184:187],v[208:211],v[252:255],a[184:187]\n"
"buffer_load_dwordx4 v112,s[24:27],s76 offen offset:1024 sc1 lds\n"
"v_mfma_f32_16x16x32_bf16 a[184:187],v[144:147],v[188:191],a[184:187]\n"
"v_mfma_f32_16x16x32_bf16 a[132:135],v[148:151],v[160:163],a[132:135]\n"
"v_mfma_f32_16x16x32_bf16 a[132:135],v[212:215],v[224:227],a[132:135]\n"
"v_mfma_f32_16x16x32_bf16 a[140:143],v[212:215],v[228:231],a[140:143]\n"
"buffer_load_dwordx4 v112,s[24:27],s76 offen offset:2048 sc1 lds\n"
"v_mfma_f32_16x16x32_bf16 a[140:143],v[148:151],v[164:167],a[140:143]\n"
"v_mfma_f32_16x16x32_bf16 a[148:151],v[148:151],v[168:171],a[148:151]\n"
"v_mfma_f32_16x16x32_bf16 a[148:151],v[212:215],v[232:235],a[148:151]\n"
"buffer_load_dwordx4 v112,s[24:27],s76 offen offset:3072 sc1 lds\n"
"v_mfma_f32_16x16x32_bf16 a[156:159],v[212:215],v[236:239],a[156:159]\n"
"s_xor_b32 s76,s76,114688\n"
"v_mfma_f32_16x16x32_bf16 a[156:159],v[148:151],v[172:175],a[156:159]\n"
"v_mfma_f32_16x16x32_bf16 a[164:167],v[148:151],v[176:179],a[164:167]\n"
"buffer_load_dword v115,v8,s[48:51],0 offen\n"
"v_mfma_f32_16x16x32_bf16 a[164:167],v[212:215],v[240:243],a[164:167]\n"
"v_mfma_f32_16x16x32_bf16 a[172:175],v[212:215],v[244:247],a[172:175]\n"
"v_mfma_f32_16x16x32_bf16 a[172:175],v[148:151],v[180:183],a[172:175]\n"
"v_mfma_f32_16x16x32_bf16 a[180:183],v[148:151],v[184:187],a[180:183]\n"
"v_mfma_f32_16x16x32_bf16 a[180:183],v[212:215],v[248:251],a[180:183]\n"
"v_mfma_f32_16x16x32_bf16 a[188:191],v[212:215],v[252:255],a[188:191]\n"
"v_mfma_f32_16x16x32_bf16 a[188:191],v[148:151],v[188:191],a[188:191]\n"
"s_waitcnt vmcnt(26)\n"
"s_barrier\n"
"v_mfma_f32_16x16x32_bf16 a[192:195],v[216:219],v[224:227],a[192:195]\n"
"ds_read_b128 v[152:155],v80 offset:45056\n"
"v_mfma_f32_16x16x32_bf16 a[196:199],v[220:223],v[224:227],a[196:199]\n"
"ds_read_b128 v[156:159],v80 offset:47104\n"
"v_mfma_f32_16x16x32_bf16 a[204:207],v[220:223],v[228:231],a[204:207]\n"
"ds_read_b64_tr_b16 v[160:161],v98 offset:0\n"
"v_mfma_f32_16x16x32_bf16 a[200:203],v[216:219],v[228:231],a[200:203]\n"
"ds_read_b64_tr_b16 v[162:163],v98 offset:1024\n"
"v_mfma_f32_16x16x32_bf16 a[208:211],v[216:219],v[232:235],a[208:211]\n"
"ds_read_b64_tr_b16 v[164:165],v99 offset:0\n"
"v_mfma_f32_16x16x32_bf16 a[212:215],v[220:223],v[232:235],a[212:215]\n"
"ds_read_b64_tr_b16 v[166:167],v99 offset:1024\n"
"v_mfma_f32_16x16x32_bf16 a[220:223],v[220:223],v[236:239],a[220:223]\n"
"ds_read_b64_tr_b16 v[168:169],v100 offset:0\n"
"v_mfma_f32_16x16x32_bf16 a[216:219],v[216:219],v[236:239],a[216:219]\n"
"ds_read_b64_tr_b16 v[170:171],v100 offset:1024\n"
"v_mfma_f32_16x16x32_bf16 a[224:227],v[216:219],v[240:243],a[224:227]\n"
"ds_read_b64_tr_b16 v[172:173],v101 offset:0\n"
"v_mfma_f32_16x16x32_bf16 a[228:231],v[220:223],v[240:243],a[228:231]\n"
"ds_read_b64_tr_b16 v[174:175],v101 offset:1024\n"
"v_mfma_f32_16x16x32_bf16 a[236:239],v[220:223],v[244:247],a[236:239]\n"
"ds_read_b64_tr_b16 v[176:177],v102 offset:0\n"
"v_mfma_f32_16x16x32_bf16 a[232:235],v[216:219],v[244:247],a[232:235]\n"
"ds_read_b64_tr_b16 v[178:179],v102 offset:1024\n"
"v_mfma_f32_16x16x32_bf16 a[240:243],v[216:219],v[248:251],a[240:243]\n"
"ds_read_b64_tr_b16 v[180:181],v103 offset:0\n"
"v_mfma_f32_16x16x32_bf16 a[244:247],v[220:223],v[248:251],a[244:247]\n"
"ds_read_b64_tr_b16 v[182:183],v103 offset:1024\n"
"v_mfma_f32_16x16x32_bf16 a[252:255],v[220:223],v[252:255],a[252:255]\n"
"ds_read_b64_tr_b16 v[184:185],v104 offset:0\n"
"v_mfma_f32_16x16x32_bf16 a[248:251],v[216:219],v[252:255],a[248:251]\n"
"ds_read_b64_tr_b16 v[186:187],v104 offset:1024\n"
"s_waitcnt lgkmcnt(10)\n"
"v_mfma_f32_16x16x32_bf16 a[192:195],v[152:155],v[160:163],a[192:195]\n"
"ds_read_b64_tr_b16 v[188:189],v105 offset:0\n"
"v_mfma_f32_16x16x32_bf16 a[196:199],v[156:159],v[160:163],a[196:199]\n"
"ds_read_b64_tr_b16 v[190:191],v105 offset:1024\n"
"v_mfma_f32_16x16x32_bf16 a[204:207],v[156:159],v[164:167],a[204:207]\n"
"ds_read_b128 v[128:131],v80 offset:32768\n"
"v_mfma_f32_16x16x32_bf16 a[200:203],v[152:155],v[164:167],a[200:203]\n"
"ds_read_b128 v[192:195],v81 offset:32768\n"
"s_waitcnt lgkmcnt(10)\n"
"v_mfma_f32_16x16x32_bf16 a[208:211],v[152:155],v[168:171],a[208:211]\n"
"ds_read_b64_tr_b16 v[224:225],v98 offset:16384\n"
"v_mfma_f32_16x16x32_bf16 a[212:215],v[156:159],v[168:171],a[212:215]\n"
"ds_read_b64_tr_b16 v[226:227],v98 offset:17408\n"
"v_mfma_f32_16x16x32_bf16 a[220:223],v[156:159],v[172:175],a[220:223]\n"
"ds_read_b64_tr_b16 v[228:229],v99 offset:16384\n"
"v_mfma_f32_16x16x32_bf16 a[216:219],v[152:155],v[172:175],a[216:219]\n"
"ds_read_b64_tr_b16 v[230:231],v99 offset:17408\n"
"s_waitcnt lgkmcnt(10)\n"
"v_mfma_f32_16x16x32_bf16 a[224:227],v[152:155],v[176:179],a[224:227]\n"
"ds_read_b64_tr_b16 v[232:233],v100 offset:16384\n"
"v_mfma_f32_16x16x32_bf16 a[228:231],v[156:159],v[176:179],a[228:231]\n"
"ds_read_b64_tr_b16 v[234:235],v100 offset:17408\n"
"v_mfma_f32_16x16x32_bf16 a[236:239],v[156:159],v[180:183],a[236:239]\n"
"ds_read_b64_tr_b16 v[236:237],v101 offset:16384\n"
"v_mfma_f32_16x16x32_bf16 a[232:235],v[152:155],v[180:183],a[232:235]\n"
"ds_read_b64_tr_b16 v[238:239],v101 offset:17408\n"
"s_waitcnt lgkmcnt(10)\n"
"v_mfma_f32_16x16x32_bf16 a[240:243],v[152:155],v[184:187],a[240:243]\n"
"ds_read_b64_tr_b16 v[240:241],v102 offset:16384\n"
"v_mfma_f32_16x16x32_bf16 a[244:247],v[156:159],v[184:187],a[244:247]\n"
"ds_read_b64_tr_b16 v[242:243],v102 offset:17408\n"
"v_mfma_f32_16x16x32_bf16 a[252:255],v[156:159],v[188:191],a[252:255]\n"
"ds_read_b64_tr_b16 v[244:245],v103 offset:16384\n"
"v_mfma_f32_16x16x32_bf16 a[248:251],v[152:155],v[188:191],a[248:251]\n"
"ds_read_b64_tr_b16 v[246:247],v103 offset:17408\n"
"s_waitcnt lgkmcnt(8)\n"
"v_mfma_f32_16x16x32_bf16 a[0:3],v[128:131],v[160:163],a[0:3]\n"
"ds_read_b64_tr_b16 v[248:249],v104 offset:16384\n"
"v_mfma_f32_16x16x32_bf16 a[0:3],v[192:195],v[224:227],a[0:3]\n"
"ds_read_b64_tr_b16 v[250:251],v104 offset:17408\n"
"v_mfma_f32_16x16x32_bf16 a[8:11],v[192:195],v[228:231],a[8:11]\n"
"ds_read_b64_tr_b16 v[252:253],v105 offset:16384\n"
"v_mfma_f32_16x16x32_bf16 a[8:11],v[128:131],v[164:167],a[8:11]\n"
"ds_read_b64_tr_b16 v[254:255],v105 offset:17408\n"
"s_waitcnt lgkmcnt(8)\n"
"v_mfma_f32_16x16x32_bf16 a[16:19],v[128:131],v[168:171],a[16:19]\n"
"ds_read_b128 v[132:135],v80 offset:34816\n"
"v_mfma_f32_16x16x32_bf16 a[16:19],v[192:195],v[232:235],a[16:19]\n"
"ds_read_b128 v[196:199],v81 offset:34816\n"
"v_mfma_f32_16x16x32_bf16 a[24:27],v[192:195],v[236:239],a[24:27]\n"
"ds_read_b128 v[136:139],v80 offset:36864\n"
"v_mfma_f32_16x16x32_bf16 a[24:27],v[128:131],v[172:175],a[24:27]\n"
"ds_read_b128 v[200:203],v81 offset:36864\n"
"s_waitcnt lgkmcnt(8)\n"
"v_mfma_f32_16x16x32_bf16 a[32:35],v[128:131],v[176:179],a[32:35]\n"
"s_and_b32 s82,s52,1\n"
"ds_read_b128 v[140:143],v80 offset:38912\n"
"s_lshl_b32 s82,s82,13\n"
"v_mfma_f32_16x16x32_bf16 a[32:35],v[192:195],v[240:243],a[32:35]\n"
"ds_read_b128 v[204:207],v81 offset:38912\n"
"v_mfma_f32_16x16x32_bf16 a[40:43],v[192:195],v[244:247],a[40:43]\n"
"ds_read_b128 v[144:147],v80 offset:40960\n"
"v_mfma_f32_16x16x32_bf16 a[40:43],v[128:131],v[180:183],a[40:43]\n"
"ds_read_b128 v[208:211],v81 offset:40960\n"
"s_waitcnt lgkmcnt(8)\n"
"v_mfma_f32_16x16x32_bf16 a[48:51],v[128:131],v[184:187],a[48:51]\n"
"ds_read_b128 v[148:151],v80 offset:43008\n"
"v_mfma_f32_16x16x32_bf16 a[48:51],v[192:195],v[248:251],a[48:51]\n"
"ds_read_b128 v[212:215],v81 offset:43008\n"
"v_subrev_u32 v98,s82,v98\n"
"v_mfma_f32_16x16x32_bf16 a[56:59],v[192:195],v[252:255],a[56:59]\n"
"ds_read_b128 v[216:219],v81 offset:45056\n"
"v_add_u32 v96,s78,v97\n"
"v_mfma_f32_16x16x32_bf16 a[56:59],v[128:131],v[188:191],a[56:59]\n"
"ds_read_b128 v[220:223],v81 offset:47104\n"
"s_waitcnt lgkmcnt(10)\n"
"v_mfma_f32_16x16x32_bf16 a[4:7],v[132:135],v[160:163],a[4:7]\n"
"v_add_u32 v97,65536,v96\n"
"v_mfma_f32_16x16x32_bf16 a[4:7],v[196:199],v[224:227],a[4:7]\n"
"v_subrev_u32 v99,s82,v99\n"
"v_mfma_f32_16x16x32_bf16 a[12:15],v[196:199],v[228:231],a[12:15]\n"
"v_mfma_f32_16x16x32_bf16 a[12:15],v[132:135],v[164:167],a[12:15]\n"
"v_mfma_f32_16x16x32_bf16 a[20:23],v[132:135],v[168:171],a[20:23]\n"
"s_waitcnt lgkmcnt(0)\n"
"s_barrier\n"
"v_mfma_f32_16x16x32_bf16 a[20:23],v[196:199],v[232:235],a[20:23]\n"
"v_subrev_u32 v100,s82,v100\n"
"s_add_u32 m0,s85,98304\n"
"v_mfma_f32_16x16x32_bf16 a[28:31],v[196:199],v[236:239],a[28:31]\n"
"buffer_load_dwordx4 v110,s[28:31],s86 offen lds\n"
"v_mfma_f32_16x16x32_bf16 a[28:31],v[132:135],v[172:175],a[28:31]\n"
"v_mfma_f32_16x16x32_bf16 a[36:39],v[132:135],v[176:179],a[36:39]\n"
"v_mfma_f32_16x16x32_bf16 a[36:39],v[196:199],v[240:243],a[36:39]\n"
"v_subrev_u32 v101,s82,v101\n"
"s_add_u32 m0,s85,99328\n"
"v_mfma_f32_16x16x32_bf16 a[44:47],v[196:199],v[244:247],a[44:47]\n"
"buffer_load_dwordx4 v111,s[28:31],s86 offen lds\n"
"v_mfma_f32_16x16x32_bf16 a[44:47],v[132:135],v[180:183],a[44:47]\n"
"s_xor_b32 s86,s86,262144\n"
"v_mfma_f32_16x16x32_bf16 a[52:55],v[132:135],v[184:187],a[52:55]\n"
"v_mfma_f32_16x16x32_bf16 a[52:55],v[196:199],v[248:251],a[52:55]\n"
"v_subrev_u32 v102,s82,v102\n"
"s_add_u32 m0,s78,32768\n"
"v_mfma_f32_16x16x32_bf16 a[60:63],v[196:199],v[252:255],a[60:63]\n"
"buffer_load_dwordx4 v112,s[24:27],s76 offen offset:0 sc1 lds\n"
"v_mfma_f32_16x16x32_bf16 a[60:63],v[132:135],v[188:191],a[60:63]\n"
"v_mfma_f32_16x16x32_bf16 a[64:67],v[136:139],v[160:163],a[64:67]\n"
"v_mfma_f32_16x16x32_bf16 a[64:67],v[200:203],v[224:227],a[64:67]\n"
"v_subrev_u32 v103,s82,v103\n"
"v_mfma_f32_16x16x32_bf16 a[72:75],v[200:203],v[228:231],a[72:75]\n"
"buffer_load_dwordx4 v112,s[24:27],s76 offen offset:1024 sc1 lds\n"
"v_mfma_f32_16x16x32_bf16 a[72:75],v[136:139],v[164:167],a[72:75]\n"
"v_mfma_f32_16x16x32_bf16 a[80:83],v[136:139],v[168:171],a[80:83]\n"
"v_accvgpr_read_b32 v116,a0\n"
"v_accvgpr_read_b32 v117,a1\n"
"v_mfma_f32_16x16x32_bf16 a[80:83],v[200:203],v[232:235],a[80:83]\n"
"v_subrev_u32 v104,s82,v104\n"
"v_accvgpr_read_b32 v118,a2\n"
"v_mfma_f32_16x16x32_bf16 a[88:91],v[200:203],v[236:239],a[88:91]\n"
"buffer_load_dwordx4 v112,s[24:27],s76 offen offset:2048 sc1 lds\n"
"v_mfma_f32_16x16x32_bf16 a[88:91],v[136:139],v[172:175],a[88:91]\n"
"v_accvgpr_read_b32 v119,a3\n"
"v_accvgpr_read_b32 v120,a4\n"
"v_mfma_f32_16x16x32_bf16 a[96:99],v[136:139],v[176:179],a[96:99]\n"
"v_accvgpr_read_b32 v121,a5\n"
"v_accvgpr_read_b32 v122,a6\n"
"v_mfma_f32_16x16x32_bf16 a[96:99],v[200:203],v[240:243],a[96:99]\n"
"v_subrev_u32 v105,s82,v105\n"
"v_accvgpr_read_b32 v123,a7\n"
"v_mfma_f32_16x16x32_bf16 a[104:107],v[200:203],v[244:247],a[104:107]\n"
"buffer_load_dwordx4 v112,s[24:27],s76 offen offset:3072 sc1 lds\n"
"v_mfma_f32_16x16x32_bf16 a[104:107],v[136:139],v[180:183],a[104:107]\n"
"s_xor_b32 s76,s76,16384\n"
"v_cvt_pk_bf16_f32 v116,v116,v117\n"
"v_cvt_pk_bf16_f32 v117,v118,v119\n"
"v_mfma_f32_16x16x32_bf16 a[112:115],v[136:139],v[184:187],a[112:115]\n"
"v_cvt_pk_bf16_f32 v118,v120,v121\n"
"v_cvt_pk_bf16_f32 v119,v122,v123\n"
"v_mfma_f32_16x16x32_bf16 a[112:115],v[200:203],v[248:251],a[112:115]\n"
"buffer_store_dwordx4 v[116:119],v112,s[24:27],s56 offen offset:0 sc1\n"
"v_mfma_f32_16x16x32_bf16 a[120:123],v[200:203],v[252:255],a[120:123]\n"
"s_nop 0\n"
"v_accvgpr_read_b32 v116,a8\n"
"v_accvgpr_read_b32 v117,a9\n"
"v_accvgpr_read_b32 v118,a10\n"
"v_mfma_f32_16x16x32_bf16 a[120:123],v[136:139],v[188:191],a[120:123]\n"
"s_waitcnt vmcnt(20)\n"
"v_accvgpr_read_b32 v119,a11\n"
"v_accvgpr_read_b32 v120,a12\n"
"s_barrier\n"
"v_mfma_f32_16x16x32_bf16 a[68:71],v[140:143],v[160:163],a[68:71]\n"
"ds_read_b128 v[0:3],v97 offset:0\n"
"v_accvgpr_read_b32 v121,a13\n"
"v_mfma_f32_16x16x32_bf16 a[68:71],v[204:207],v[224:227],a[68:71]\n"
"ds_read_b128 v[8:11],v97 offset:1024\n"
"v_accvgpr_read_b32 v122,a14\n"
"v_mfma_f32_16x16x32_bf16 a[76:79],v[204:207],v[228:231],a[76:79]\n"
"ds_read_b128 v[16:19],v97 offset:2048\n"
"v_accvgpr_read_b32 v123,a15\n"
"v_mfma_f32_16x16x32_bf16 a[76:79],v[140:143],v[164:167],a[76:79]\n"
"ds_read_b128 v[24:27],v97 offset:3072\n"
"v_cvt_pk_bf16_f32 v116,v116,v117\n"
"v_mfma_f32_16x16x32_bf16 a[84:87],v[140:143],v[168:171],a[84:87]\n"
"ds_read_b128 v[4:7],v97 offset:16384\n"
"v_cvt_pk_bf16_f32 v117,v118,v119\n"
"v_mfma_f32_16x16x32_bf16 a[84:87],v[204:207],v[232:235],a[84:87]\n"
"ds_read_b128 v[12:15],v97 offset:17408\n"
"v_cvt_pk_bf16_f32 v118,v120,v121\n"
"v_mfma_f32_16x16x32_bf16 a[92:95],v[204:207],v[236:239],a[92:95]\n"
"ds_read_b128 v[20:23],v97 offset:18432\n"
"v_cvt_pk_bf16_f32 v119,v122,v123\n"
"v_mfma_f32_16x16x32_bf16 a[92:95],v[140:143],v[172:175],a[92:95]\n"
"ds_read_b128 v[28:31],v97 offset:19456\n"
"buffer_store_dwordx4 v[116:119],v112,s[24:27],s56 offen offset:1024 sc1\n"
"v_mfma_f32_16x16x32_bf16 a[100:103],v[140:143],v[176:179],a[100:103]\n"
"ds_read_b64_tr_b16 v[32:33],v98 offset:49152\n"
"v_accvgpr_read_b32 v116,a16\n"
"v_mfma_f32_16x16x32_bf16 a[100:103],v[204:207],v[240:243],a[100:103]\n"
"ds_read_b64_tr_b16 v[34:35],v98 offset:50176\n"
"v_accvgpr_read_b32 v117,a17\n"
"v_mfma_f32_16x16x32_bf16 a[108:111],v[204:207],v[244:247],a[108:111]\n"
"ds_read_b64_tr_b16 v[36:37],v98 offset:57344\n"
"v_accvgpr_read_b32 v118,a18\n"
"v_mfma_f32_16x16x32_bf16 a[108:111],v[140:143],v[180:183],a[108:111]\n"
"ds_read_b64_tr_b16 v[38:39],v98 offset:58368\n"
"v_accvgpr_read_b32 v119,a19\n"
"v_mfma_f32_16x16x32_bf16 a[116:119],v[140:143],v[184:187],a[116:119]\n"
"ds_read_b64_tr_b16 v[40:41],v99 offset:49152\n"
"v_accvgpr_read_b32 v120,a20\n"
"v_mfma_f32_16x16x32_bf16 a[116:119],v[204:207],v[248:251],a[116:119]\n"
"ds_read_b64_tr_b16 v[42:43],v99 offset:50176\n"
"v_accvgpr_read_b32 v121,a21\n"
"v_mfma_f32_16x16x32_bf16 a[124:127],v[204:207],v[252:255],a[124:127]\n"
"ds_read_b64_tr_b16 v[44:45],v99 offset:57344\n"
"v_accvgpr_read_b32 v122,a22\n"
"v_mfma_f32_16x16x32_bf16 a[124:127],v[140:143],v[188:191],a[124:127]\n"
"ds_read_b64_tr_b16 v[46:47],v99 offset:58368\n"
"v_accvgpr_read_b32 v123,a23\n"
"v_mfma_f32_16x16x32_bf16 a[128:131],v[144:147],v[160:163],a[128:131]\n"
"ds_read_b64_tr_b16 v[48:49],v100 offset:49152\n"
"v_cvt_pk_bf16_f32 v116,v116,v117\n"
"v_mfma_f32_16x16x32_bf16 a[128:131],v[208:211],v[224:227],a[128:131]\n"
"ds_read_b64_tr_b16 v[50:51],v100 offset:50176\n"
"v_cvt_pk_bf16_f32 v117,v118,v119\n"
"v_mfma_f32_16x16x32_bf16 a[136:139],v[208:211],v[228:231],a[136:139]\n"
"ds_read_b64_tr_b16 v[52:53],v100 offset:57344\n"
"v_cvt_pk_bf16_f32 v118,v120,v121\n"
"v_mfma_f32_16x16x32_bf16 a[136:139],v[144:147],v[164:167],a[136:139]\n"
"ds_read_b64_tr_b16 v[54:55],v100 offset:58368\n"
"v_cvt_pk_bf16_f32 v119,v122,v123\n"
"v_mfma_f32_16x16x32_bf16 a[144:147],v[144:147],v[168:171],a[144:147]\n"
"ds_read_b64_tr_b16 v[56:57],v101 offset:49152\n"
"buffer_store_dwordx4 v[116:119],v112,s[24:27],s56 offen offset:2048 sc1\n"
"v_mfma_f32_16x16x32_bf16 a[144:147],v[208:211],v[232:235],a[144:147]\n"
"ds_read_b64_tr_b16 v[58:59],v101 offset:50176\n"
"v_accvgpr_read_b32 v116,a24\n"
"v_mfma_f32_16x16x32_bf16 a[152:155],v[208:211],v[236:239],a[152:155]\n"
"ds_read_b64_tr_b16 v[60:61],v101 offset:57344\n"
"v_accvgpr_read_b32 v117,a25\n"
"v_mfma_f32_16x16x32_bf16 a[152:155],v[144:147],v[172:175],a[152:155]\n"
"ds_read_b64_tr_b16 v[62:63],v101 offset:58368\n"
"v_accvgpr_read_b32 v118,a26\n"
"v_mfma_f32_16x16x32_bf16 a[160:163],v[144:147],v[176:179],a[160:163]\n"
"ds_read_b64_tr_b16 v[64:65],v102 offset:49152\n"
"v_accvgpr_read_b32 v119,a27\n"
"v_mfma_f32_16x16x32_bf16 a[160:163],v[208:211],v[240:243],a[160:163]\n"
"ds_read_b64_tr_b16 v[66:67],v102 offset:50176\n"
"v_accvgpr_read_b32 v120,a28\n"
"v_mfma_f32_16x16x32_bf16 a[168:171],v[208:211],v[244:247],a[168:171]\n"
"ds_read_b64_tr_b16 v[68:69],v102 offset:57344\n"
"v_accvgpr_read_b32 v121,a29\n"
"v_mfma_f32_16x16x32_bf16 a[168:171],v[144:147],v[180:183],a[168:171]\n"
"ds_read_b64_tr_b16 v[70:71],v102 offset:58368\n"
"v_accvgpr_read_b32 v122,a30\n"
"v_mfma_f32_16x16x32_bf16 a[176:179],v[144:147],v[184:187],a[176:179]\n"
"ds_read_b64_tr_b16 v[72:73],v103 offset:49152\n"
"v_accvgpr_read_b32 v123,a31\n"
"v_mfma_f32_16x16x32_bf16 a[176:179],v[208:211],v[248:251],a[176:179]\n"
"ds_read_b64_tr_b16 v[74:75],v103 offset:50176\n"
"v_cvt_pk_bf16_f32 v116,v116,v117\n"
"v_mfma_f32_16x16x32_bf16 a[184:187],v[208:211],v[252:255],a[184:187]\n"
"ds_read_b64_tr_b16 v[76:77],v103 offset:57344\n"
"v_cvt_pk_bf16_f32 v117,v118,v119\n"
"v_mfma_f32_16x16x32_bf16 a[184:187],v[144:147],v[188:191],a[184:187]\n"
"ds_read_b64_tr_b16 v[78:79],v103 offset:58368\n"
"v_cvt_pk_bf16_f32 v118,v120,v121\n"
"v_mfma_f32_16x16x32_bf16 a[132:135],v[148:151],v[160:163],a[132:135]\n"
"ds_read_b64_tr_b16 v[80:81],v104 offset:49152\n"
"v_cvt_pk_bf16_f32 v119,v122,v123\n"
"v_mfma_f32_16x16x32_bf16 a[132:135],v[212:215],v[224:227],a[132:135]\n"
"ds_read_b64_tr_b16 v[82:83],v104 offset:50176\n"
"buffer_store_dwordx4 v[116:119],v112,s[24:27],s56 offen offset:3072 sc1\n"
"v_mfma_f32_16x16x32_bf16 a[140:143],v[212:215],v[228:231],a[140:143]\n"
"ds_read_b64_tr_b16 v[84:85],v104 offset:57344\n"
"v_mfma_f32_16x16x32_bf16 a[140:143],v[148:151],v[164:167],a[140:143]\n"
"ds_read_b64_tr_b16 v[86:87],v104 offset:58368\n"
"v_mfma_f32_16x16x32_bf16 a[148:151],v[148:151],v[168:171],a[148:151]\n"
"ds_read_b64_tr_b16 v[88:89],v105 offset:49152\n"
"v_mfma_f32_16x16x32_bf16 a[148:151],v[212:215],v[232:235],a[148:151]\n"
"ds_read_b64_tr_b16 v[90:91],v105 offset:50176\n"
"v_mfma_f32_16x16x32_bf16 a[156:159],v[212:215],v[236:239],a[156:159]\n"
"ds_read_b64_tr_b16 v[92:93],v105 offset:57344\n"
"v_mfma_f32_16x16x32_bf16 a[156:159],v[148:151],v[172:175],a[156:159]\n"
"ds_read_b64_tr_b16 v[94:95],v105 offset:58368\n"
"v_mfma_f32_16x16x32_bf16 a[164:167],v[148:151],v[176:179],a[164:167]\n"
"s_add_u32 m0,s85,106496\n"
"v_mfma_f32_16x16x32_bf16 a[164:167],v[212:215],v[240:243],a[164:167]\n"
"buffer_load_dwordx4 v110,s[28:31],s86 offen lds\n"
"v_mfma_f32_16x16x32_bf16 a[172:175],v[212:215],v[244:247],a[172:175]\n"
"v_mfma_f32_16x16x32_bf16 a[172:175],v[148:151],v[180:183],a[172:175]\n"
"s_add_u32 m0,s85,107520\n"
"v_mfma_f32_16x16x32_bf16 a[180:183],v[148:151],v[184:187],a[180:183]\n"
"buffer_load_dwordx4 v111,s[28:31],s86 offen lds\n"
"v_mfma_f32_16x16x32_bf16 a[180:183],v[212:215],v[248:251],a[180:183]\n"
"s_xor_b32 s86,s86,786432\n"
"v_mfma_f32_16x16x32_bf16 a[188:191],v[212:215],v[252:255],a[188:191]\n"
"s_add_u32 m0,s78,49152\n"
"v_mfma_f32_16x16x32_bf16 a[188:191],v[148:151],v[188:191],a[188:191]\n"
"buffer_load_dwordx4 v112,s[24:27],s76 offen offset:0 sc1 lds\n"
"v_mfma_f32_16x16x32_bf16 a[192:195],v[216:219],v[224:227],a[192:195]\n"
"v_mfma_f32_16x16x32_bf16 a[196:199],v[220:223],v[224:227],a[196:199]\n"
"v_mfma_f32_16x16x32_bf16 a[204:207],v[220:223],v[228:231],a[204:207]\n"
"buffer_load_dwordx4 v112,s[24:27],s76 offen offset:1024 sc1 lds\n"
"v_mfma_f32_16x16x32_bf16 a[200:203],v[216:219],v[228:231],a[200:203]\n"
"v_mfma_f32_16x16x32_bf16 a[208:211],v[216:219],v[232:235],a[208:211]\n"
"v_mfma_f32_16x16x32_bf16 a[212:215],v[220:223],v[232:235],a[212:215]\n"
"buffer_load_dwordx4 v112,s[24:27],s76 offen offset:2048 sc1 lds\n"
"v_mfma_f32_16x16x32_bf16 a[220:223],v[220:223],v[236:239],a[220:223]\n"
"v_mfma_f32_16x16x32_bf16 a[216:219],v[216:219],v[236:239],a[216:219]\n"
"buffer_load_dwordx4 v112,s[24:27],s76 offen offset:3072 sc1 lds\n"
"v_mfma_f32_16x16x32_bf16 a[224:227],v[216:219],v[240:243],a[224:227]\n"
"s_xor_b32 s76,s76,49152\n"
"v_mfma_f32_16x16x32_bf16 a[228:231],v[220:223],v[240:243],a[228:231]\n"
"v_mfma_f32_16x16x32_bf16 a[236:239],v[220:223],v[244:247],a[236:239]\n"
"v_mfma_f32_16x16x32_bf16 a[232:235],v[216:219],v[244:247],a[232:235]\n"
"v_mfma_f32_16x16x32_bf16 a[240:243],v[216:219],v[248:251],a[240:243]\n"
"v_mfma_f32_16x16x32_bf16 a[244:247],v[220:223],v[248:251],a[244:247]\n"
"v_mfma_f32_16x16x32_bf16 a[252:255],v[220:223],v[252:255],a[252:255]\n"
"v_mfma_f32_16x16x32_bf16 a[248:251],v[216:219],v[252:255],a[248:251]\n"
".Lfk_ph2:\n"
"s_waitcnt vmcnt(25) lgkmcnt(0)\n"
"s_barrier\n"
"v_mfma_f32_16x16x32_bf16 v[128:131],v[32:35],v[0:3],0\n"
"v_accvgpr_read_b32 v116,a32\n"
"v_accvgpr_read_b32 v117,a33\n"
"v_mfma_f32_16x16x32_bf16 v[128:131],v[36:39],v[4:7],v[128:131]\n"
"v_accvgpr_read_b32 v118,a34\n"
"v_accvgpr_read_b32 v119,a35\n"
"v_mfma_f32_16x16x32_bf16 v[160:163],v[36:39],v[12:15],0\n"
"v_accvgpr_read_b32 v120,a36\n"
"v_accvgpr_read_b32 v121,a37\n"
"v_mfma_f32_16x16x32_bf16 v[160:163],v[32:35],v[8:11],v[160:163]\n"
"s_add_u32 m0,s85,147456\n"
"v_accvgpr_read_b32 v122,a38\n"
"v_mfma_f32_16x16x32_bf16 v[192:195],v[32:35],v[16:19],0\n"
"buffer_load_dwordx4 v110,s[28:31],s86 offen lds\n"
"v_mfma_f32_16x16x32_bf16 v[192:195],v[36:39],v[20:23],v[192:195]\n"
"v_accvgpr_read_b32 v123,a39\n"
"v_cvt_pk_bf16_f32 v116,v116,v117\n"
"v_mfma_f32_16x16x32_bf16 v[224:227],v[36:39],v[28:31],0\n"
"v_cvt_pk_bf16_f32 v117,v118,v119\n"
"v_cvt_pk_bf16_f32 v118,v120,v121\n"
"v_mfma_f32_16x16x32_bf16 v[224:227],v[32:35],v[24:27],v[224:227]\n"
"v_cvt_pk_bf16_f32 v119,v122,v123\n"
"s_add_u32 s56,s56,4096\n"
"v_mfma_f32_16x16x32_bf16 v[228:231],v[40:43],v[24:27],0\n"
"ds_read_b64_tr_b16 v[32:33],v100 offset:32768\n"
"buffer_store_dwordx4 v[116:119],v112,s[24:27],s56 offen offset:0 sc1\n"
"v_mfma_f32_16x16x32_bf16 v[228:231],v[44:47],v[28:31],v[228:231]\n"
"ds_read_b64_tr_b16 v[34:35],v100 offset:33792\n"
"v_accvgpr_read_b32 v116,a40\n"
"v_mfma_f32_16x16x32_bf16 v[196:199],v[44:47],v[20:23],0\n"
"ds_read_b64_tr_b16 v[36:37],v100 offset:40960\n"
"v_accvgpr_read_b32 v117,a41\n"
"v_mfma_f32_16x16x32_bf16 v[196:199],v[40:43],v[16:19],v[196:199]\n"
"s_add_u32 m0,s85,148480\n"
"ds_read_b64_tr_b16 v[38:39],v100 offset:41984\n"
"v_mfma_f32_16x16x32_bf16 v[164:167],v[40:43],v[8:11],0\n"
"buffer_load_dwordx4 v111,s[28:31],s86 offen lds\n"
"v_mfma_f32_16x16x32_bf16 v[164:167],v[44:47],v[12:15],v[164:167]\n"
"s_add_u32 m0,s85,155648\n"
"s_xor_b32 s86,s86,262144\n"
"v_mfma_f32_16x16x32_bf16 v[132:135],v[44:47],v[4:7],0\n"
"buffer_load_dwordx4 v110,s[28:31],s86 offen lds\n"
"v_mfma_f32_16x16x32_bf16 v[132:135],v[40:43],v[0:3],v[132:135]\n"
"v_accvgpr_read_b32 v118,a42\n"
"v_accvgpr_read_b32 v119,a43\n"
"v_mfma_f32_16x16x32_bf16 v[136:139],v[48:51],v[0:3],0\n"
"ds_read_b64_tr_b16 v[40:41],v101 offset:32768\n"
"v_accvgpr_read_b32 v120,a44\n"
"v_mfma_f32_16x16x32_bf16 v[136:139],v[52:55],v[4:7],v[136:139]\n"
"ds_read_b64_tr_b16 v[42:43],v101 offset:33792\n"
"v_accvgpr_read_b32 v121,a45\n"
"v_mfma_f32_16x16x32_bf16 v[168:171],v[52:55],v[12:15],0\n"
"ds_read_b64_tr_b16 v[44:45],v101 offset:40960\n"
"v_accvgpr_read_b32 v122,a46\n"
"v_mfma_f32_16x16x32_bf16 v[168:171],v[48:51],v[8:11],v[168:171]\n"
"s_add_u32 m0,s85,156672\n"
"ds_read_b64_tr_b16 v[46:47],v101 offset:41984\n"
"v_mfma_f32_16x16x32_bf16 v[200:203],v[48:51],v[16:19],0\n"
"buffer_load_dwordx4 v111,s[28:31],s86 offen lds\n"
"v_mfma_f32_16x16x32_bf16 v[200:203],v[52:55],v[20:23],v[200:203]\n"
"s_xor_b32 s86,s86,3932160\n"
"v_accvgpr_read_b32 v123,a47\n"
"v_mfma_f32_16x16x32_bf16 v[232:235],v[52:55],v[28:31],0\n"
"v_cvt_pk_bf16_f32 v116,v116,v117\n"
"v_cvt_pk_bf16_f32 v117,v118,v119\n"
"v_mfma_f32_16x16x32_bf16 v[232:235],v[48:51],v[24:27],v[232:235]\n"
"v_cvt_pk_bf16_f32 v118,v120,v121\n"
"v_cvt_pk_bf16_f32 v119,v122,v123\n"
"v_mfma_f32_16x16x32_bf16 v[236:239],v[56:59],v[24:27],0\n"
"ds_read_b64_tr_b16 v[48:49],v102 offset:32768\n"
"buffer_store_dwordx4 v[116:119],v112,s[24:27],s56 offen offset:1024 sc1\n"
"v_mfma_f32_16x16x32_bf16 v[236:239],v[60:63],v[28:31],v[236:239]\n"
"ds_read_b64_tr_b16 v[50:51],v102 offset:33792\n"
"v_accvgpr_read_b32 v116,a48\n"
"v_mfma_f32_16x16x32_bf16 v[204:207],v[60:63],v[20:23],0\n"
"ds_read_b64_tr_b16 v[52:53],v102 offset:40960\n"
"v_accvgpr_read_b32 v117,a49\n"
"v_mfma_f32_16x16x32_bf16 v[204:207],v[56:59],v[16:19],v[204:207]\n"
"s_add_u32 m0,s78,65536\n"
"ds_read_b64_tr_b16 v[54:55],v102 offset:41984\n"
"v_mfma_f32_16x16x32_bf16 v[172:175],v[56:59],v[8:11],0\n"
"buffer_load_dwordx4 v112,s[24:27],s76 offen offset:0 sc1 lds\n"
"v_mfma_f32_16x16x32_bf16 v[172:175],v[60:63],v[12:15],v[172:175]\n"
"v_accvgpr_read_b32 v118,a50\n"
"v_mfma_f32_16x16x32_bf16 v[140:143],v[60:63],v[4:7],0\n"
"buffer_load_dwordx4 v112,s[24:27],s76 offen offset:1024 sc1 lds\n"
"v_mfma_f32_16x16x32_bf16 v[140:143],v[56:59],v[0:3],v[140:143]\n"
"s_waitcnt vmcnt(25)\n"
"v_mfma_f32_16x16x32_bf16 v[144:147],v[64:67],v[0:3],0\n"
"ds_read_b64_tr_b16 a[32:33],v98 offset:32768\n"
"ds_read_b64_tr_b16 v[56:57],v103 offset:32768\n"
"v_mfma_f32_16x16x32_bf16 v[144:147],v[68:71],v[4:7],v[144:147]\n"
"ds_read_b64_tr_b16 a[34:35],v98 offset:33792\n"
"ds_read_b64_tr_b16 v[58:59],v103 offset:33792\n"
"v_mfma_f32_16x16x32_bf16 v[176:179],v[68:71],v[12:15],0\n"
"ds_read_b64_tr_b16 a[36:37],v98 offset:40960\n"
"ds_read_b64_tr_b16 v[60:61],v103 offset:40960\n"
"v_mfma_f32_16x16x32_bf16 v[176:179],v[64:67],v[8:11],v[176:179]\n"
"ds_read_b64_tr_b16 a[38:39],v98 offset:41984\n"
"ds_read_b64_tr_b16 v[62:63],v103 offset:41984\n"
"v_mfma_f32_16x16x32_bf16 v[208:211],v[64:67],v[16:19],0\n"
"buffer_load_dwordx4 v112,s[24:27],s76 offen offset:2048 sc1 lds\n"
"v_mfma_f32_16x16x32_bf16 v[208:211],v[68:71],v[20:23],v[208:211]\n"
"ds_read_b128 a[0:3],v96 offset:0\n"
"v_accvgpr_read_b32 v119,a51\n"
"v_mfma_f32_16x16x32_bf16 v[240:243],v[68:71],v[28:31],0\n"
"v_accvgpr_read_b32 v120,a52\n"
"v_accvgpr_read_b32 v121,a53\n"
"v_mfma_f32_16x16x32_bf16 v[240:243],v[64:67],v[24:27],v[240:243]\n"
"ds_read_b128 a[8:11],v96 offset:1024\n"
"v_accvgpr_read_b32 v122,a54\n"
"v_mfma_f32_16x16x32_bf16 v[244:247],v[72:75],v[24:27],0\n"
"ds_read_b64_tr_b16 a[40:41],v99 offset:32768\n"
"ds_read_b64_tr_b16 v[64:65],v104 offset:32768\n"
"v_mfma_f32_16x16x32_bf16 v[244:247],v[76:79],v[28:31],v[244:247]\n"
"ds_read_b64_tr_b16 a[42:43],v99 offset:33792\n"
"ds_read_b64_tr_b16 v[66:67],v104 offset:33792\n"
"v_mfma_f32_16x16x32_bf16 v[212:215],v[76:79],v[20:23],0\n"
"ds_read_b64_tr_b16 a[44:45],v99 offset:40960\n"
"ds_read_b64_tr_b16 v[68:69],v104 offset:40960\n"
"v_mfma_f32_16x16x32_bf16 v[212:215],v[72:75],v[16:19],v[212:215]\n"
"ds_read_b64_tr_b16 a[46:47],v99 offset:41984\n"
"ds_read_b64_tr_b16 v[70:71],v104 offset:41984\n"
"v_mfma_f32_16x16x32_bf16 v[180:183],v[72:75],v[8:11],0\n"
"buffer_load_dwordx4 v112,s[24:27],s76 offen offset:3072 sc1 lds\n"
"v_mfma_f32_16x16x32_bf16 v[180:183],v[76:79],v[12:15],v[180:183]\n"
"s_add_u32 m0,s78,81920\n"
"s_xor_b32 s76,s76,16384\n"
"ds_read_b128 a[16:19],v96 offset:2048\n"
"v_mfma_f32_16x16x32_bf16 v[148:151],v[76:79],v[4:7],0\n"
"buffer_load_dwordx4 v112,s[24:27],s76 offen offset:0 sc1 lds\n"
"v_mfma_f32_16x16x32_bf16 v[148:151],v[72:75],v[0:3],v[148:151]\n"
"ds_read_b128 a[24:27],v96 offset:3072\n"
"v_accvgpr_read_b32 v123,a55\n"
"v_mfma_f32_16x16x32_bf16 v[152:155],v[80:83],v[0:3],0\n"
"ds_read_b64_tr_b16 v[72:73],v105 offset:32768\n"
"v_cvt_pk_bf16_f32 v116,v116,v117\n"
"v_mfma_f32_16x16x32_bf16 v[152:155],v[84:87],v[4:7],v[152:155]\n"
"ds_read_b64_tr_b16 v[74:75],v105 offset:33792\n"
"v_cvt_pk_bf16_f32 v117,v118,v119\n"
"v_mfma_f32_16x16x32_bf16 v[184:187],v[84:87],v[12:15],0\n"
"ds_read_b64_tr_b16 v[76:77],v105 offset:40960\n"
"v_cvt_pk_bf16_f32 v118,v120,v121\n"
"v_mfma_f32_16x16x32_bf16 v[184:187],v[80:83],v[8:11],v[184:187]\n"
"ds_read_b64_tr_b16 v[78:79],v105 offset:41984\n"
"v_cvt_pk_bf16_f32 v119,v122,v123\n"
"v_mfma_f32_16x16x32_bf16 v[216:219],v[80:83],v[16:19],0\n"
"ds_read_b128 a[4:7],v96 offset:16384\n"
"buffer_store_dwordx4 v[116:119],v112,s[24:27],s56 offen offset:2048 sc1\n"
"v_mfma_f32_16x16x32_bf16 v[216:219],v[84:87],v[20:23],v[216:219]\n"
"ds_read_b128 a[12:15],v96 offset:17408\n"
"v_accvgpr_read_b32 v116,a56\n"
"v_mfma_f32_16x16x32_bf16 v[248:251],v[84:87],v[28:31],0\n"
"ds_read_b128 a[20:23],v96 offset:18432\n"
"v_accvgpr_read_b32 v117,a57\n"
"v_mfma_f32_16x16x32_bf16 v[248:251],v[80:83],v[24:27],v[248:251]\n"
"ds_read_b128 a[28:31],v96 offset:19456\n"
"v_accvgpr_read_b32 v118,a58\n"
"v_mfma_f32_16x16x32_bf16 v[252:255],v[88:91],v[24:27],0\n"
"buffer_load_dwordx4 v112,s[24:27],s76 offen offset:1024 sc1 lds\n"
"v_mfma_f32_16x16x32_bf16 v[252:255],v[92:95],v[28:31],v[252:255]\n"
"v_accvgpr_read_b32 v119,a59\n"
"v_accvgpr_read_b32 v120,a60\n"
"v_mfma_f32_16x16x32_bf16 v[220:223],v[92:95],v[20:23],0\n"
"v_accvgpr_read_b32 v121,a61\n"
"v_accvgpr_read_b32 v122,a62\n"
"v_mfma_f32_16x16x32_bf16 v[220:223],v[88:91],v[16:19],v[220:223]\n"
"buffer_load_dwordx4 v112,s[24:27],s76 offen offset:2048 sc1 lds\n"
"v_mfma_f32_16x16x32_bf16 v[188:191],v[88:91],v[8:11],0\n"
"v_accvgpr_read_b32 v123,a63\n"
"v_cvt_pk_bf16_f32 v116,v116,v117\n"
"v_mfma_f32_16x16x32_bf16 v[188:191],v[92:95],v[12:15],v[188:191]\n"
"v_cvt_pk_bf16_f32 v117,v118,v119\n"
"v_cvt_pk_bf16_f32 v118,v120,v121\n"
"v_mfma_f32_16x16x32_bf16 v[156:159],v[92:95],v[4:7],0\n"
"buffer_load_dwordx4 v112,s[24:27],s76 offen offset:3072 sc1 lds\n"
"v_mfma_f32_16x16x32_bf16 v[156:159],v[88:91],v[0:3],v[156:159]\n"
"s_xor_b32 s76,s76,245760\n"
"v_cvt_pk_bf16_f32 v119,v122,v123\n"
"s_waitcnt vmcnt(19) lgkmcnt(0)\n"
"s_barrier\n"
"v_mfma_f32_16x16x32_bf16 v[128:131],a[32:35],a[0:3],v[128:131]\n"
"ds_read_b64_tr_b16 v[80:81],v98 offset:0\n"
"ds_read_b64_tr_b16 v[88:89],v99 offset:0\n"
"v_mfma_f32_16x16x32_bf16 v[128:131],a[36:39],a[4:7],v[128:131]\n"
"ds_read_b64_tr_b16 v[82:83],v98 offset:1024\n"
"ds_read_b64_tr_b16 v[90:91],v99 offset:1024\n"
"v_mfma_f32_16x16x32_bf16 v[160:163],a[36:39],a[12:15],v[160:163]\n"
"ds_read_b64_tr_b16 v[84:85],v98 offset:8192\n"
"ds_read_b64_tr_b16 v[92:93],v99 offset:8192\n"
"v_mfma_f32_16x16x32_bf16 v[160:163],a[32:35],a[8:11],v[160:163]\n"
"s_add_u32 m0,s85,131072\n"
"ds_read_b64_tr_b16 v[86:87],v98 offset:9216\n"
"ds_read_b64_tr_b16 v[94:95],v99 offset:9216\n"
"v_mfma_f32_16x16x32_bf16 v[192:195],a[32:35],a[16:19],v[192:195]\n"
"buffer_load_dwordx4 v110,s[28:31],s86 offen lds\n"
"buffer_store_dwordx4 v[116:119],v112,s[24:27],s56 offen offset:3072 sc1\n"
"v_mfma_f32_16x16x32_bf16 v[192:195],a[36:39],a[20:23],v[192:195]\n"
"s_nop 0\n"
"v_accvgpr_read_b32 v116,a64\n"
"v_mfma_f32_16x16x32_bf16 v[224:227],a[36:39],a[28:31],v[224:227]\n"
"v_accvgpr_read_b32 v117,a65\n"
"v_accvgpr_read_b32 v118,a66\n"
"v_mfma_f32_16x16x32_bf16 v[224:227],a[32:35],a[24:27],v[224:227]\n"
"v_accvgpr_read_b32 v119,a67\n"
"v_accvgpr_read_b32 v120,a68\n"
"v_mfma_f32_16x16x32_bf16 v[228:231],a[40:43],a[24:27],v[228:231]\n"
"ds_read_b64_tr_b16 a[32:33],v100 offset:0\n"
"v_accvgpr_read_b32 v121,a69\n"
"v_mfma_f32_16x16x32_bf16 v[228:231],a[44:47],a[28:31],v[228:231]\n"
"ds_read_b64_tr_b16 a[34:35],v100 offset:1024\n"
"v_accvgpr_read_b32 v122,a70\n"
"v_mfma_f32_16x16x32_bf16 v[196:199],a[44:47],a[20:23],v[196:199]\n"
"ds_read_b64_tr_b16 a[36:37],v100 offset:8192\n"
"v_accvgpr_read_b32 v123,a71\n"
"v_mfma_f32_16x16x32_bf16 v[196:199],a[40:43],a[16:19],v[196:199]\n"
"s_add_u32 m0,s85,132096\n"
"ds_read_b64_tr_b16 a[38:39],v100 offset:9216\n"
"v_mfma_f32_16x16x32_bf16 v[164:167],a[40:43],a[8:11],v[164:167]\n"
"buffer_load_dwordx4 v111,s[28:31],s86 offen lds\n"
"v_mfma_f32_16x16x32_bf16 v[164:167],a[44:47],a[12:15],v[164:167]\n"
"s_add_u32 m0,s85,139264\n"
"s_xor_b32 s86,s86,262144\n"
"v_mfma_f32_16x16x32_bf16 v[132:135],a[44:47],a[4:7],v[132:135]\n"
"buffer_load_dwordx4 v110,s[28:31],s86 offen lds\n"
"v_mfma_f32_16x16x32_bf16 v[132:135],a[40:43],a[0:3],v[132:135]\n"
"v_cvt_pk_bf16_f32 v116,v116,v117\n"
"v_cvt_pk_bf16_f32 v117,v118,v119\n"
"v_mfma_f32_16x16x32_bf16 v[136:139],v[32:35],a[0:3],v[136:139]\n"
"ds_read_b64_tr_b16 a[40:41],v101 offset:0\n"
"v_cvt_pk_bf16_f32 v118,v120,v121\n"
"v_mfma_f32_16x16x32_bf16 v[136:139],v[36:39],a[4:7],v[136:139]\n"
"ds_read_b64_tr_b16 a[42:43],v101 offset:1024\n"
"v_cvt_pk_bf16_f32 v119,v122,v123\n"
"v_mfma_f32_16x16x32_bf16 v[168:171],v[36:39],a[12:15],v[168:171]\n"
"ds_read_b64_tr_b16 a[44:45],v101 offset:8192\n"
"s_add_u32 s56,s56,126976\n"
"v_mfma_f32_16x16x32_bf16 v[168:171],v[32:35],a[8:11],v[168:171]\n"
"s_add_u32 m0,s85,140288\n"
"ds_read_b64_tr_b16 a[46:47],v101 offset:9216\n"
"v_mfma_f32_16x16x32_bf16 v[200:203],v[32:35],a[16:19],v[200:203]\n"
"buffer_load_dwordx4 v111,s[28:31],s86 offen lds\n"
"v_mfma_f32_16x16x32_bf16 v[200:203],v[36:39],a[20:23],v[200:203]\n"
"buffer_store_dwordx4 v[116:119],v112,s[24:27],s56 offen offset:0 sc1\n"
"v_mfma_f32_16x16x32_bf16 v[232:235],v[36:39],a[28:31],v[232:235]\n"
"s_xor_b32 s86,s86,786432\n"
"v_accvgpr_read_b32 v116,a72\n"
"v_accvgpr_read_b32 v117,a73\n"
"v_mfma_f32_16x16x32_bf16 v[232:235],v[32:35],a[24:27],v[232:235]\n"
"v_accvgpr_read_b32 v118,a74\n"
"v_accvgpr_read_b32 v119,a75\n"
"v_mfma_f32_16x16x32_bf16 v[236:239],v[40:43],a[24:27],v[236:239]\n"
"ds_read_b64_tr_b16 v[32:33],v102 offset:0\n"
"v_accvgpr_read_b32 v120,a76\n"
"v_mfma_f32_16x16x32_bf16 v[236:239],v[44:47],a[28:31],v[236:239]\n"
"ds_read_b64_tr_b16 v[34:35],v102 offset:1024\n"
"v_accvgpr_read_b32 v121,a77\n"
"v_mfma_f32_16x16x32_bf16 v[204:207],v[44:47],a[20:23],v[204:207]\n"
"ds_read_b64_tr_b16 v[36:37],v102 offset:8192\n"
"v_accvgpr_read_b32 v122,a78\n"
"v_mfma_f32_16x16x32_bf16 v[204:207],v[40:43],a[16:19],v[204:207]\n"
"s_add_u32 m0,s78,0\n"
"ds_read_b64_tr_b16 v[38:39],v102 offset:9216\n"
"v_mfma_f32_16x16x32_bf16 v[172:175],v[40:43],a[8:11],v[172:175]\n"
"buffer_load_dwordx4 v112,s[24:27],s76 offen offset:0 sc1 lds\n"
"v_mfma_f32_16x16x32_bf16 v[172:175],v[44:47],a[12:15],v[172:175]\n"
"v_accvgpr_read_b32 v123,a79\n"
"v_cvt_pk_bf16_f32 v116,v116,v117\n"
"v_mfma_f32_16x16x32_bf16 v[140:143],v[44:47],a[4:7],v[140:143]\n"
"buffer_load_dwordx4 v112,s[24:27],s76 offen offset:1024 sc1 lds\n"
"v_mfma_f32_16x16x32_bf16 v[140:143],v[40:43],a[0:3],v[140:143]\n"
"s_waitcnt vmcnt(23)\n"
"v_cvt_pk_bf16_f32 v117,v118,v119\n"
"v_cvt_pk_bf16_f32 v118,v120,v121\n"
"v_mfma_f32_16x16x32_bf16 v[144:147],v[48:51],a[0:3],v[144:147]\n"
"ds_read_b64_tr_b16 v[40:41],v103 offset:0\n"
"v_cvt_pk_bf16_f32 v119,v122,v123\n"
"v_mfma_f32_16x16x32_bf16 v[144:147],v[52:55],a[4:7],v[144:147]\n"
"ds_read_b64_tr_b16 v[42:43],v103 offset:1024\n"
"buffer_store_dwordx4 v[116:119],v112,s[24:27],s56 offen offset:1024 sc1\n"
"v_mfma_f32_16x16x32_bf16 v[176:179],v[52:55],a[12:15],v[176:179]\n"
"ds_read_b64_tr_b16 v[44:45],v103 offset:8192\n"
"v_accvgpr_read_b32 v116,a80\n"
"v_mfma_f32_16x16x32_bf16 v[176:179],v[48:51],a[8:11],v[176:179]\n"
"ds_read_b64_tr_b16 v[46:47],v103 offset:9216\n"
"v_accvgpr_read_b32 v117,a81\n"
"v_mfma_f32_16x16x32_bf16 v[208:211],v[48:51],a[16:19],v[208:211]\n"
"buffer_load_dwordx4 v112,s[24:27],s76 offen offset:2048 sc1 lds\n"
"v_mfma_f32_16x16x32_bf16 v[208:211],v[52:55],a[20:23],v[208:211]\n"
"ds_read_b128 v[0:3],v96 offset:32768\n"
"v_accvgpr_read_b32 v118,a82\n"
"v_mfma_f32_16x16x32_bf16 v[240:243],v[52:55],a[28:31],v[240:243]\n"
"v_accvgpr_read_b32 v119,a83\n"
"v_accvgpr_read_b32 v120,a84\n"
"v_mfma_f32_16x16x32_bf16 v[240:243],v[48:51],a[24:27],v[240:243]\n"
"ds_read_b128 v[8:11],v96 offset:33792\n"
"v_accvgpr_read_b32 v121,a85\n"
"v_mfma_f32_16x16x32_bf16 v[244:247],v[56:59],a[24:27],v[244:247]\n"
"ds_read_b64_tr_b16 v[48:49],v104 offset:0\n"
"v_accvgpr_read_b32 v122,a86\n"
"v_mfma_f32_16x16x32_bf16 v[244:247],v[60:63],a[28:31],v[244:247]\n"
"ds_read_b64_tr_b16 v[50:51],v104 offset:1024\n"
"v_accvgpr_read_b32 v123,a87\n"
"v_mfma_f32_16x16x32_bf16 v[212:215],v[60:63],a[20:23],v[212:215]\n"
"ds_read_b64_tr_b16 v[52:53],v104 offset:8192\n"
"v_cvt_pk_bf16_f32 v116,v116,v117\n"
"v_mfma_f32_16x16x32_bf16 v[212:215],v[56:59],a[16:19],v[212:215]\n"
"ds_read_b64_tr_b16 v[54:55],v104 offset:9216\n"
"v_cvt_pk_bf16_f32 v117,v118,v119\n"
"v_mfma_f32_16x16x32_bf16 v[180:183],v[56:59],a[8:11],v[180:183]\n"
"buffer_load_dwordx4 v112,s[24:27],s76 offen offset:3072 sc1 lds\n"
"v_mfma_f32_16x16x32_bf16 v[180:183],v[60:63],a[12:15],v[180:183]\n"
"s_add_u32 m0,s78,16384\n"
"s_xor_b32 s76,s76,16384\n"
"ds_read_b128 v[16:19],v96 offset:34816\n"
"v_mfma_f32_16x16x32_bf16 v[148:151],v[60:63],a[4:7],v[148:151]\n"
"buffer_load_dwordx4 v112,s[24:27],s76 offen offset:0 sc1 lds\n"
"v_mfma_f32_16x16x32_bf16 v[148:151],v[56:59],a[0:3],v[148:151]\n"
"ds_read_b128 v[24:27],v96 offset:35840\n"
"v_cvt_pk_bf16_f32 v118,v120,v121\n"
"v_mfma_f32_16x16x32_bf16 v[152:155],v[64:67],a[0:3],v[152:155]\n"
"ds_read_b64_tr_b16 v[56:57],v105 offset:0\n"
"v_cvt_pk_bf16_f32 v119,v122,v123\n"
"v_mfma_f32_16x16x32_bf16 v[152:155],v[68:71],a[4:7],v[152:155]\n"
"ds_read_b64_tr_b16 v[58:59],v105 offset:1024\n"
"buffer_store_dwordx4 v[116:119],v112,s[24:27],s56 offen offset:2048 sc1\n"
"v_mfma_f32_16x16x32_bf16 v[184:187],v[68:71],a[12:15],v[184:187]\n"
"ds_read_b64_tr_b16 v[60:61],v105 offset:8192\n"
"v_accvgpr_read_b32 v116,a88\n"
"v_mfma_f32_16x16x32_bf16 v[184:187],v[64:67],a[8:11],v[184:187]\n"
"ds_read_b64_tr_b16 v[62:63],v105 offset:9216\n"
"v_accvgpr_read_b32 v117,a89\n"
"v_mfma_f32_16x16x32_bf16 v[216:219],v[64:67],a[16:19],v[216:219]\n"
"ds_read_b128 v[4:7],v96 offset:49152\n"
"v_accvgpr_read_b32 v118,a90\n"
"v_mfma_f32_16x16x32_bf16 v[216:219],v[68:71],a[20:23],v[216:219]\n"
"ds_read_b128 v[12:15],v96 offset:50176\n"
"v_accvgpr_read_b32 v119,a91\n"
"v_mfma_f32_16x16x32_bf16 v[248:251],v[68:71],a[28:31],v[248:251]\n"
"ds_read_b128 v[20:23],v96 offset:51200\n"
"v_accvgpr_read_b32 v120,a92\n"
"v_mfma_f32_16x16x32_bf16 v[248:251],v[64:67],a[24:27],v[248:251]\n"
"ds_read_b128 v[28:31],v96 offset:52224\n"
"v_accvgpr_read_b32 v121,a93\n"
"v_mfma_f32_16x16x32_bf16 v[252:255],v[72:75],a[24:27],v[252:255]\n"
"buffer_load_dwordx4 v112,s[24:27],s76 offen offset:1024 sc1 lds\n"
"v_mfma_f32_16x16x32_bf16 v[252:255],v[76:79],a[28:31],v[252:255]\n"
"v_accvgpr_read_b32 v122,a94\n"
"v_accvgpr_read_b32 v123,a95\n"
"v_mfma_f32_16x16x32_bf16 v[220:223],v[76:79],a[20:23],v[220:223]\n"
"v_cvt_pk_bf16_f32 v116,v116,v117\n"
"v_cvt_pk_bf16_f32 v117,v118,v119\n"
"v_mfma_f32_16x16x32_bf16 v[220:223],v[72:75],a[16:19],v[220:223]\n"
"buffer_load_dwordx4 v112,s[24:27],s76 offen offset:2048 sc1 lds\n"
"v_mfma_f32_16x16x32_bf16 v[188:191],v[72:75],a[8:11],v[188:191]\n"
"v_cvt_pk_bf16_f32 v118,v120,v121\n"
"v_cvt_pk_bf16_f32 v119,v122,v123\n"
"v_mfma_f32_16x16x32_bf16 v[188:191],v[76:79],a[12:15],v[188:191]\n"
"buffer_store_dwordx4 v[116:119],v112,s[24:27],s56 offen offset:3072 sc1\n"
"v_mfma_f32_16x16x32_bf16 v[156:159],v[76:79],a[4:7],v[156:159]\n"
"buffer_load_dwordx4 v112,s[24:27],s76 offen offset:3072 sc1 lds\n"
"v_accvgpr_read_b32 v116,a96\n"
"v_mfma_f32_16x16x32_bf16 v[156:159],v[72:75],a[0:3],v[156:159]\n"
"s_xor_b32 s76,s76,49152\n"
"v_accvgpr_read_b32 v117,a97\n"
"s_waitcnt vmcnt(27) lgkmcnt(0)\n"
"s_barrier\n"
"v_mfma_f32_16x16x32_bf16 v[128:131],v[80:83],v[0:3],v[128:131]\n"
"ds_read_b64_tr_b16 v[64:65],v98 offset:49152\n"
"ds_read_b64_tr_b16 v[72:73],v99 offset:49152\n"
"v_mfma_f32_16x16x32_bf16 v[128:131],v[84:87],v[4:7],v[128:131]\n"
"ds_read_b64_tr_b16 v[66:67],v98 offset:50176\n"
"ds_read_b64_tr_b16 v[74:75],v99 offset:50176\n"
"v_mfma_f32_16x16x32_bf16 v[160:163],v[84:87],v[12:15],v[160:163]\n"
"ds_read_b64_tr_b16 v[68:69],v98 offset:57344\n"
"ds_read_b64_tr_b16 v[76:77],v99 offset:57344\n"
"v_mfma_f32_16x16x32_bf16 v[160:163],v[80:83],v[8:11],v[160:163]\n"
"s_add_u32 m0,s85,98304\n"
"ds_read_b64_tr_b16 v[70:71],v98 offset:58368\n"
"ds_read_b64_tr_b16 v[78:79],v99 offset:58368\n"
"v_mfma_f32_16x16x32_bf16 v[192:195],v[80:83],v[16:19],v[192:195]\n"
"buffer_load_dwordx4 v110,s[28:31],s86 offen lds\n"
"v_mfma_f32_16x16x32_bf16 v[192:195],v[84:87],v[20:23],v[192:195]\n"
"v_accvgpr_read_b32 v118,a98\n"
"v_accvgpr_read_b32 v119,a99\n"
"v_mfma_f32_16x16x32_bf16 v[224:227],v[84:87],v[28:31],v[224:227]\n"
"v_accvgpr_read_b32 v120,a100\n"
"v_accvgpr_read_b32 v121,a101\n"
"v_mfma_f32_16x16x32_bf16 v[224:227],v[80:83],v[24:27],v[224:227]\n"
"v_accvgpr_read_b32 v122,a102\n"
"v_accvgpr_read_b32 v123,a103\n"
"v_mfma_f32_16x16x32_bf16 v[228:231],v[88:91],v[24:27],v[228:231]\n"
"ds_read_b64_tr_b16 v[80:81],v100 offset:49152\n"
"v_cvt_pk_bf16_f32 v116,v116,v117\n"
"v_mfma_f32_16x16x32_bf16 v[228:231],v[92:95],v[28:31],v[228:231]\n"
"ds_read_b64_tr_b16 v[82:83],v100 offset:50176\n"
"v_cvt_pk_bf16_f32 v117,v118,v119\n"
"v_mfma_f32_16x16x32_bf16 v[196:199],v[92:95],v[20:23],v[196:199]\n"
"ds_read_b64_tr_b16 v[84:85],v100 offset:57344\n"
"v_cvt_pk_bf16_f32 v118,v120,v121\n"
"v_mfma_f32_16x16x32_bf16 v[196:199],v[88:91],v[16:19],v[196:199]\n"
"s_add_u32 m0,s85,99328\n"
"ds_read_b64_tr_b16 v[86:87],v100 offset:58368\n"
"v_mfma_f32_16x16x32_bf16 v[164:167],v[88:91],v[8:11],v[164:167]\n"
"buffer_load_dwordx4 v111,s[28:31],s86 offen lds\n"
"v_mfma_f32_16x16x32_bf16 v[164:167],v[92:95],v[12:15],v[164:167]\n"
"s_add_u32 m0,s85,106496\n"
"s_xor_b32 s86,s86,262144\n"
"v_mfma_f32_16x16x32_bf16 v[132:135],v[92:95],v[4:7],v[132:135]\n"
"buffer_load_dwordx4 v110,s[28:31],s86 offen lds\n"
"v_mfma_f32_16x16x32_bf16 v[132:135],v[88:91],v[0:3],v[132:135]\n"
"v_cvt_pk_bf16_f32 v119,v122,v123\n"
"s_add_u32 s56,s56,4096\n"
"v_mfma_f32_16x16x32_bf16 v[136:139],a[32:35],v[0:3],v[136:139]\n"
"ds_read_b64_tr_b16 v[88:89],v101 offset:49152\n"
"buffer_store_dwordx4 v[116:119],v112,s[24:27],s56 offen offset:0 sc1\n"
"v_mfma_f32_16x16x32_bf16 v[136:139],a[36:39],v[4:7],v[136:139]\n"
"ds_read_b64_tr_b16 v[90:91],v101 offset:50176\n"
"v_accvgpr_read_b32 v116,a104\n"
"v_mfma_f32_16x16x32_bf16 v[168:171],a[36:39],v[12:15],v[168:171]\n"
"ds_read_b64_tr_b16 v[92:93],v101 offset:57344\n"
"v_accvgpr_read_b32 v117,a105\n"
"v_mfma_f32_16x16x32_bf16 v[168:171],a[32:35],v[8:11],v[168:171]\n"
"s_add_u32 m0,s85,107520\n"
"ds_read_b64_tr_b16 v[94:95],v101 offset:58368\n"
"v_mfma_f32_16x16x32_bf16 v[200:203],a[32:35],v[16:19],v[200:203]\n"
"buffer_load_dwordx4 v111,s[28:31],s86 offen lds\n"
"v_mfma_f32_16x16x32_bf16 v[200:203],a[36:39],v[20:23],v[200:203]\n"
"s_xor_b32 s86,s86,1835008\n"
"v_accvgpr_read_b32 v118,a106\n"
"v_mfma_f32_16x16x32_bf16 v[232:235],a[36:39],v[28:31],v[232:235]\n"
"v_accvgpr_read_b32 v119,a107\n"
"v_accvgpr_read_b32 v120,a108\n"
"v_mfma_f32_16x16x32_bf16 v[232:235],a[32:35],v[24:27],v[232:235]\n"
"v_accvgpr_read_b32 v121,a109\n"
"v_accvgpr_read_b32 v122,a110\n"
"v_mfma_f32_16x16x32_bf16 v[236:239],a[40:43],v[24:27],v[236:239]\n"
"ds_read_b64_tr_b16 a[32:33],v102 offset:49152\n"
"v_accvgpr_read_b32 v123,a111\n"
"v_mfma_f32_16x16x32_bf16 v[236:239],a[44:47],v[28:31],v[236:239]\n"
"ds_read_b64_tr_b16 a[34:35],v102 offset:50176\n"
"v_cvt_pk_bf16_f32 v116,v116,v117\n"
"v_mfma_f32_16x16x32_bf16 v[204:207],a[44:47],v[20:23],v[204:207]\n"
"ds_read_b64_tr_b16 a[36:37],v102 offset:57344\n"
"v_cvt_pk_bf16_f32 v117,v118,v119\n"
"v_mfma_f32_16x16x32_bf16 v[204:207],a[40:43],v[16:19],v[204:207]\n"
"s_add_u32 m0,s78,32768\n"
"ds_read_b64_tr_b16 a[38:39],v102 offset:58368\n"
"v_mfma_f32_16x16x32_bf16 v[172:175],a[40:43],v[8:11],v[172:175]\n"
"buffer_load_dwordx4 v112,s[24:27],s76 offen offset:0 sc1 lds\n"
"v_mfma_f32_16x16x32_bf16 v[172:175],a[44:47],v[12:15],v[172:175]\n"
"v_cvt_pk_bf16_f32 v118,v120,v121\n"
"v_cvt_pk_bf16_f32 v119,v122,v123\n"
"v_mfma_f32_16x16x32_bf16 v[140:143],a[44:47],v[4:7],v[140:143]\n"
"buffer_load_dwordx4 v112,s[24:27],s76 offen offset:1024 sc1 lds\n"
"buffer_store_dwordx4 v[116:119],v112,s[24:27],s56 offen offset:1024 sc1\n"
"v_mfma_f32_16x16x32_bf16 v[140:143],a[40:43],v[0:3],v[140:143]\n"
"s_nop 0\n"
"v_accvgpr_read_b32 v116,a112\n"
"s_waitcnt vmcnt(25)\n"
"v_mfma_f32_16x16x32_bf16 v[144:147],v[32:35],v[0:3],v[144:147]\n"
"ds_read_b64_tr_b16 a[40:41],v103 offset:49152\n"
"v_accvgpr_read_b32 v117,a113\n"
"v_mfma_f32_16x16x32_bf16 v[144:147],v[36:39],v[4:7],v[144:147]\n"
"ds_read_b64_tr_b16 a[42:43],v103 offset:50176\n"
"v_accvgpr_read_b32 v118,a114\n"
"v_mfma_f32_16x16x32_bf16 v[176:179],v[36:39],v[12:15],v[176:179]\n"
"ds_read_b64_tr_b16 a[44:45],v103 offset:57344\n"
"v_accvgpr_read_b32 v119,a115\n"
"v_mfma_f32_16x16x32_bf16 v[176:179],v[32:35],v[8:11],v[176:179]\n"
"ds_read_b64_tr_b16 a[46:47],v103 offset:58368\n"
"v_accvgpr_read_b32 v120,a116\n"
"v_mfma_f32_16x16x32_bf16 v[208:211],v[32:35],v[16:19],v[208:211]\n"
"buffer_load_dwordx4 v112,s[24:27],s76 offen offset:2048 sc1 lds\n"
"v_mfma_f32_16x16x32_bf16 v[208:211],v[36:39],v[20:23],v[208:211]\n"
"ds_read_b128 a[0:3],v97 offset:0\n"
"v_accvgpr_read_b32 v121,a117\n"
"v_mfma_f32_16x16x32_bf16 v[240:243],v[36:39],v[28:31],v[240:243]\n"
"v_accvgpr_read_b32 v122,a118\n"
"v_accvgpr_read_b32 v123,a119\n"
"v_mfma_f32_16x16x32_bf16 v[240:243],v[32:35],v[24:27],v[240:243]\n"
"ds_read_b128 a[8:11],v97 offset:1024\n"
"v_cvt_pk_bf16_f32 v116,v116,v117\n"
"v_mfma_f32_16x16x32_bf16 v[244:247],v[40:43],v[24:27],v[244:247]\n"
"ds_read_b64_tr_b16 v[32:33],v104 offset:49152\n"
"v_cvt_pk_bf16_f32 v117,v118,v119\n"
"v_mfma_f32_16x16x32_bf16 v[244:247],v[44:47],v[28:31],v[244:247]\n"
"ds_read_b64_tr_b16 v[34:35],v104 offset:50176\n"
"v_cvt_pk_bf16_f32 v118,v120,v121\n"
"v_mfma_f32_16x16x32_bf16 v[212:215],v[44:47],v[20:23],v[212:215]\n"
"ds_read_b64_tr_b16 v[36:37],v104 offset:57344\n"
"v_cvt_pk_bf16_f32 v119,v122,v123\n"
"v_mfma_f32_16x16x32_bf16 v[212:215],v[40:43],v[16:19],v[212:215]\n"
"ds_read_b64_tr_b16 v[38:39],v104 offset:58368\n"
"buffer_store_dwordx4 v[116:119],v112,s[24:27],s56 offen offset:2048 sc1\n"
"v_mfma_f32_16x16x32_bf16 v[180:183],v[40:43],v[8:11],v[180:183]\n"
"buffer_load_dwordx4 v112,s[24:27],s76 offen offset:3072 sc1 lds\n"
"v_mfma_f32_16x16x32_bf16 v[180:183],v[44:47],v[12:15],v[180:183]\n"
"s_add_u32 m0,s78,49152\n"
"s_xor_b32 s76,s76,16384\n"
"ds_read_b128 a[16:19],v97 offset:2048\n"
"v_mfma_f32_16x16x32_bf16 v[148:151],v[44:47],v[4:7],v[148:151]\n"
"buffer_load_dwordx4 v112,s[24:27],s76 offen offset:0 sc1 lds\n"
"v_mfma_f32_16x16x32_bf16 v[148:151],v[40:43],v[0:3],v[148:151]\n"
"ds_read_b128 a[24:27],v97 offset:3072\n"
"v_accvgpr_read_b32 v116,a120\n"
"v_mfma_f32_16x16x32_bf16 v[152:155],v[48:51],v[0:3],v[152:155]\n"
"ds_read_b64_tr_b16 v[40:41],v105 offset:49152\n"
"v_accvgpr_read_b32 v117,a121\n"
"v_mfma_f32_16x16x32_bf16 v[152:155],v[52:55],v[4:7],v[152:155]\n"
"ds_read_b64_tr_b16 v[42:43],v105 offset:50176\n"
"v_accvgpr_read_b32 v118,a122\n"
"v_mfma_f32_16x16x32_bf16 v[184:187],v[52:55],v[12:15],v[184:187]\n"
"ds_read_b64_tr_b16 v[44:45],v105 offset:57344\n"
"v_accvgpr_read_b32 v119,a123\n"
"v_mfma_f32_16x16x32_bf16 v[184:187],v[48:51],v[8:11],v[184:187]\n"
"ds_read_b64_tr_b16 v[46:47],v105 offset:58368\n"
"v_accvgpr_read_b32 v120,a124\n"
"v_mfma_f32_16x16x32_bf16 v[216:219],v[48:51],v[16:19],v[216:219]\n"
"ds_read_b128 a[4:7],v97 offset:16384\n"
"v_accvgpr_read_b32 v121,a125\n"
"v_mfma_f32_16x16x32_bf16 v[216:219],v[52:55],v[20:23],v[216:219]\n"
"ds_read_b128 a[12:15],v97 offset:17408\n"
"v_accvgpr_read_b32 v122,a126\n"
"v_mfma_f32_16x16x32_bf16 v[248:251],v[52:55],v[28:31],v[248:251]\n"
"ds_read_b128 a[20:23],v97 offset:18432\n"
"v_accvgpr_read_b32 v123,a127\n"
"v_mfma_f32_16x16x32_bf16 v[248:251],v[48:51],v[24:27],v[248:251]\n"
"ds_read_b128 a[28:31],v97 offset:19456\n"
"v_cvt_pk_bf16_f32 v116,v116,v117\n"
"v_mfma_f32_16x16x32_bf16 v[252:255],v[56:59],v[24:27],v[252:255]\n"
"buffer_load_dwordx4 v112,s[24:27],s76 offen offset:1024 sc1 lds\n"
"v_mfma_f32_16x16x32_bf16 v[252:255],v[60:63],v[28:31],v[252:255]\n"
"v_cvt_pk_bf16_f32 v117,v118,v119\n"
"v_cvt_pk_bf16_f32 v118,v120,v121\n"
"v_cvt_pk_bf16_f32 v119,v122,v123\n"
"v_mfma_f32_16x16x32_bf16 v[220:223],v[60:63],v[20:23],v[220:223]\n"
"buffer_store_dwordx4 v[116:119],v112,s[24:27],s56 offen offset:3072 sc1\n"
"v_mfma_f32_16x16x32_bf16 v[220:223],v[56:59],v[16:19],v[220:223]\n"
"buffer_load_dwordx4 v112,s[24:27],s76 offen offset:2048 sc1 lds\n"
"v_mfma_f32_16x16x32_bf16 v[188:191],v[56:59],v[8:11],v[188:191]\n"
"v_accvgpr_read_b32 v116,a128\n"
"v_accvgpr_read_b32 v117,a129\n"
"v_mfma_f32_16x16x32_bf16 v[188:191],v[60:63],v[12:15],v[188:191]\n"
"v_accvgpr_read_b32 v118,a130\n"
"v_accvgpr_read_b32 v119,a131\n"
"v_mfma_f32_16x16x32_bf16 v[156:159],v[60:63],v[4:7],v[156:159]\n"
"buffer_load_dwordx4 v112,s[24:27],s76 offen offset:3072 sc1 lds\n"
"v_mfma_f32_16x16x32_bf16 v[156:159],v[56:59],v[0:3],v[156:159]\n"
"s_xor_b32 s76,s76,114688\n"
"v_accvgpr_read_b32 v120,a132\n"
"s_waitcnt vmcnt(28) lgkmcnt(0)\n"
"s_barrier\n"
"v_mfma_f32_16x16x32_bf16 v[128:131],v[64:67],a[0:3],v[128:131]\n"
"ds_read_b64_tr_b16 v[48:49],v98 offset:32768\n"
"ds_read_b64_tr_b16 v[56:57],v99 offset:32768\n"
"v_mfma_f32_16x16x32_bf16 v[128:131],v[68:71],a[4:7],v[128:131]\n"
"ds_read_b64_tr_b16 v[50:51],v98 offset:33792\n"
"ds_read_b64_tr_b16 v[58:59],v99 offset:33792\n"
"v_mfma_f32_16x16x32_bf16 v[160:163],v[68:71],a[12:15],v[160:163]\n"
"ds_read_b64_tr_b16 v[52:53],v98 offset:40960\n"
"ds_read_b64_tr_b16 v[60:61],v99 offset:40960\n"
"v_mfma_f32_16x16x32_bf16 v[160:163],v[64:67],a[8:11],v[160:163]\n"
"s_add_u32 m0,s85,147456\n"
"ds_read_b64_tr_b16 v[54:55],v98 offset:41984\n"
"ds_read_b64_tr_b16 v[62:63],v99 offset:41984\n"
"v_mfma_f32_16x16x32_bf16 v[192:195],v[64:67],a[16:19],v[192:195]\n"
"buffer_load_dwordx4 v110,s[28:31],s86 offen lds\n"
"v_mfma_f32_16x16x32_bf16 v[192:195],v[68:71],a[20:23],v[192:195]\n"
"v_accvgpr_read_b32 v121,a133\n"
"v_accvgpr_read_b32 v122,a134\n"
"v_mfma_f32_16x16x32_bf16 v[224:227],v[68:71],a[28:31],v[224:227]\n"
"v_accvgpr_read_b32 v123,a135\n"
"v_cvt_pk_bf16_f32 v116,v116,v117\n"
"v_mfma_f32_16x16x32_bf16 v[224:227],v[64:67],a[24:27],v[224:227]\n"
"v_cvt_pk_bf16_f32 v117,v118,v119\n"
"v_cvt_pk_bf16_f32 v118,v120,v121\n"
"v_mfma_f32_16x16x32_bf16 v[228:231],v[72:75],a[24:27],v[228:231]\n"
"ds_read_b64_tr_b16 v[64:65],v100 offset:32768\n"
"v_cvt_pk_bf16_f32 v119,v122,v123\n"
"v_mfma_f32_16x16x32_bf16 v[228:231],v[76:79],a[28:31],v[228:231]\n"
"ds_read_b64_tr_b16 v[66:67],v100 offset:33792\n"
"s_add_u32 s56,s56,126976\n"
"v_mfma_f32_16x16x32_bf16 v[196:199],v[76:79],a[20:23],v[196:199]\n"
"ds_read_b64_tr_b16 v[68:69],v100 offset:40960\n"
"buffer_store_dwordx4 v[116:119],v112,s[24:27],s56 offen offset:0 sc1\n"
"v_mfma_f32_16x16x32_bf16 v[196:199],v[72:75],a[16:19],v[196:199]\n"
"s_add_u32 m0,s85,148480\n"
"ds_read_b64_tr_b16 v[70:71],v100 offset:41984\n"
"v_mfma_f32_16x16x32_bf16 v[164:167],v[72:75],a[8:11],v[164:167]\n"
"buffer_load_dwordx4 v111,s[28:31],s86 offen lds\n"
"v_mfma_f32_16x16x32_bf16 v[164:167],v[76:79],a[12:15],v[164:167]\n"
"s_add_u32 m0,s85,155648\n"
"s_xor_b32 s86,s86,262144\n"
"v_mfma_f32_16x16x32_bf16 v[132:135],v[76:79],a[4:7],v[132:135]\n"
"buffer_load_dwordx4 v110,s[28:31],s86 offen lds\n"
"v_mfma_f32_16x16x32_bf16 v[132:135],v[72:75],a[0:3],v[132:135]\n"
"v_accvgpr_read_b32 v116,a136\n"
"v_accvgpr_read_b32 v117,a137\n"
"v_mfma_f32_16x16x32_bf16 v[136:139],v[80:83],a[0:3],v[136:139]\n"
"ds_read_b64_tr_b16 v[72:73],v101 offset:32768\n"
"v_accvgpr_read_b32 v118,a138\n"
"v_mfma_f32_16x16x32_bf16 v[136:139],v[84:87],a[4:7],v[136:139]\n"
"ds_read_b64_tr_b16 v[74:75],v101 offset:33792\n"
"v_accvgpr_read_b32 v119,a139\n"
"v_mfma_f32_16x16x32_bf16 v[168:171],v[84:87],a[12:15],v[168:171]\n"
"ds_read_b64_tr_b16 v[76:77],v101 offset:40960\n"
"v_accvgpr_read_b32 v120,a140\n"
"v_mfma_f32_16x16x32_bf16 v[168:171],v[80:83],a[8:11],v[168:171]\n"
"s_add_u32 m0,s85,156672\n"
"ds_read_b64_tr_b16 v[78:79],v101 offset:41984\n"
"v_mfma_f32_16x16x32_bf16 v[200:203],v[80:83],a[16:19],v[200:203]\n"
"buffer_load_dwordx4 v111,s[28:31],s86 offen lds\n"
"v_mfma_f32_16x16x32_bf16 v[200:203],v[84:87],a[20:23],v[200:203]\n"
"s_xor_b32 s86,s86,786432\n"
"v_accvgpr_read_b32 v121,a141\n"
"v_mfma_f32_16x16x32_bf16 v[232:235],v[84:87],a[28:31],v[232:235]\n"
"v_accvgpr_read_b32 v122,a142\n"
"v_accvgpr_read_b32 v123,a143\n"
"v_mfma_f32_16x16x32_bf16 v[232:235],v[80:83],a[24:27],v[232:235]\n"
"v_cvt_pk_bf16_f32 v116,v116,v117\n"
"v_cvt_pk_bf16_f32 v117,v118,v119\n"
"v_mfma_f32_16x16x32_bf16 v[236:239],v[88:91],a[24:27],v[236:239]\n"
"ds_read_b64_tr_b16 v[80:81],v102 offset:32768\n"
"v_cvt_pk_bf16_f32 v118,v120,v121\n"
"v_mfma_f32_16x16x32_bf16 v[236:239],v[92:95],a[28:31],v[236:239]\n"
"ds_read_b64_tr_b16 v[82:83],v102 offset:33792\n"
"v_cvt_pk_bf16_f32 v119,v122,v123\n"
"v_mfma_f32_16x16x32_bf16 v[204:207],v[92:95],a[20:23],v[204:207]\n"
"ds_read_b64_tr_b16 v[84:85],v102 offset:40960\n"
"buffer_store_dwordx4 v[116:119],v112,s[24:27],s56 offen offset:1024 sc1\n"
"v_mfma_f32_16x16x32_bf16 v[204:207],v[88:91],a[16:19],v[204:207]\n"
"s_add_u32 m0,s78,65536\n"
"ds_read_b64_tr_b16 v[86:87],v102 offset:41984\n"
"v_accvgpr_read_b32 v118,a146\n"
"v_mfma_f32_16x16x32_bf16 v[172:175],v[88:91],a[8:11],v[172:175]\n"
"buffer_load_dwordx4 v112,s[24:27],s76 offen offset:0 sc1 lds\n"
"v_mfma_f32_16x16x32_bf16 v[172:175],v[92:95],a[12:15],v[172:175]\n"
"v_accvgpr_read_b32 v116,a144\n"
"v_accvgpr_read_b32 v117,a145\n"
"v_mfma_f32_16x16x32_bf16 v[140:143],v[92:95],a[4:7],v[140:143]\n"
"buffer_load_dwordx4 v112,s[24:27],s76 offen offset:1024 sc1 lds\n"
"v_mfma_f32_16x16x32_bf16 v[140:143],v[88:91],a[0:3],v[140:143]\n"
"v_accvgpr_read_b32 v119,a147\n"
"s_waitcnt vmcnt(24)\n"
"v_mfma_f32_16x16x32_bf16 v[144:147],a[32:35],a[0:3],v[144:147]\n"
"ds_read_b64_tr_b16 v[88:89],v103 offset:32768\n"
"v_accvgpr_read_b32 v120,a148\n"
"v_mfma_f32_16x16x32_bf16 v[144:147],a[36:39],a[4:7],v[144:147]\n"
"ds_read_b64_tr_b16 v[90:91],v103 offset:33792\n"
"v_accvgpr_read_b32 v121,a149\n"
"v_mfma_f32_16x16x32_bf16 v[176:179],a[36:39],a[12:15],v[176:179]\n"
"ds_read_b64_tr_b16 v[92:93],v103 offset:40960\n"
"v_accvgpr_read_b32 v122,a150\n"
"v_mfma_f32_16x16x32_bf16 v[176:179],a[32:35],a[8:11],v[176:179]\n"
"ds_read_b64_tr_b16 v[94:95],v103 offset:41984\n"
"v_accvgpr_read_b32 v123,a151\n"
"v_mfma_f32_16x16x32_bf16 v[208:211],a[32:35],a[16:19],v[208:211]\n"
"buffer_load_dwordx4 v112,s[24:27],s76 offen offset:2048 sc1 lds\n"
"v_mfma_f32_16x16x32_bf16 v[208:211],a[36:39],a[20:23],v[208:211]\n"
"ds_read_b128 v[0:3],v96 offset:0\n"
"v_cvt_pk_bf16_f32 v116,v116,v117\n"
"v_mfma_f32_16x16x32_bf16 v[240:243],a[36:39],a[28:31],v[240:243]\n"
"v_cvt_pk_bf16_f32 v117,v118,v119\n"
"v_cvt_pk_bf16_f32 v118,v120,v121\n"
"v_mfma_f32_16x16x32_bf16 v[240:243],a[32:35],a[24:27],v[240:243]\n"
"ds_read_b128 v[8:11],v96 offset:1024\n"
"v_cvt_pk_bf16_f32 v119,v122,v123\n"
"v_mfma_f32_16x16x32_bf16 v[244:247],a[40:43],a[24:27],v[244:247]\n"
"ds_read_b64_tr_b16 a[32:33],v104 offset:32768\n"
"buffer_store_dwordx4 v[116:119],v112,s[24:27],s56 offen offset:2048 sc1\n"
"v_mfma_f32_16x16x32_bf16 v[244:247],a[44:47],a[28:31],v[244:247]\n"
"ds_read_b64_tr_b16 a[34:35],v104 offset:33792\n"
"v_accvgpr_read_b32 v116,a152\n"
"v_mfma_f32_16x16x32_bf16 v[212:215],a[44:47],a[20:23],v[212:215]\n"
"ds_read_b64_tr_b16 a[36:37],v104 offset:40960\n"
"v_accvgpr_read_b32 v117,a153\n"
"v_mfma_f32_16x16x32_bf16 v[212:215],a[40:43],a[16:19],v[212:215]\n"
"ds_read_b64_tr_b16 a[38:39],v104 offset:41984\n"
"v_accvgpr_read_b32 v118,a154\n"
"v_mfma_f32_16x16x32_bf16 v[180:183],a[40:43],a[8:11],v[180:183]\n"
"buffer_load_dwordx4 v112,s[24:27],s76 offen offset:3072 sc1 lds\n"
"v_mfma_f32_16x16x32_bf16 v[180:183],a[44:47],a[12:15],v[180:183]\n"
"s_add_u32 m0,s78,81920\n"
"s_xor_b32 s76,s76,16384\n"
"ds_read_b128 v[16:19],v96 offset:2048\n"
"v_mfma_f32_16x16x32_bf16 v[148:151],a[44:47],a[4:7],v[148:151]\n"
"buffer_load_dwordx4 v112,s[24:27],s76 offen offset:0 sc1 lds\n"
"v_mfma_f32_16x16x32_bf16 v[148:151],a[40:43],a[0:3],v[148:151]\n"
"ds_read_b128 v[24:27],v96 offset:3072\n"
"v_accvgpr_read_b32 v119,a155\n"
"v_mfma_f32_16x16x32_bf16 v[152:155],v[32:35],a[0:3],v[152:155]\n"
"ds_read_b64_tr_b16 a[40:41],v105 offset:32768\n"
"v_accvgpr_read_b32 v120,a156\n"
"v_mfma_f32_16x16x32_bf16 v[152:155],v[36:39],a[4:7],v[152:155]\n"
"ds_read_b64_tr_b16 a[42:43],v105 offset:33792\n"
"v_accvgpr_read_b32 v121,a157\n"
"v_mfma_f32_16x16x32_bf16 v[184:187],v[36:39],a[12:15],v[184:187]\n"
"ds_read_b64_tr_b16 a[44:45],v105 offset:40960\n"
"v_accvgpr_read_b32 v122,a158\n"
"v_mfma_f32_16x16x32_bf16 v[184:187],v[32:35],a[8:11],v[184:187]\n"
"ds_read_b64_tr_b16 a[46:47],v105 offset:41984\n"
"v_accvgpr_read_b32 v123,a159\n"
"v_mfma_f32_16x16x32_bf16 v[216:219],v[32:35],a[16:19],v[216:219]\n"
"ds_read_b128 v[4:7],v96 offset:16384\n"
"v_cvt_pk_bf16_f32 v116,v116,v117\n"
"v_mfma_f32_16x16x32_bf16 v[216:219],v[36:39],a[20:23],v[216:219]\n"
"ds_read_b128 v[12:15],v96 offset:17408\n"
"v_cvt_pk_bf16_f32 v117,v118,v119\n"
"v_mfma_f32_16x16x32_bf16 v[248:251],v[36:39],a[28:31],v[248:251]\n"
"ds_read_b128 v[20:23],v96 offset:18432\n"
"v_cvt_pk_bf16_f32 v118,v120,v121\n"
"v_mfma_f32_16x16x32_bf16 v[248:251],v[32:35],a[24:27],v[248:251]\n"
"ds_read_b128 v[28:31],v96 offset:19456\n"
"v_cvt_pk_bf16_f32 v119,v122,v123\n"
"v_mfma_f32_16x16x32_bf16 v[252:255],v[40:43],a[24:27],v[252:255]\n"
"buffer_load_dwordx4 v112,s[24:27],s76 offen offset:1024 sc1 lds\n"
"buffer_store_dwordx4 v[116:119],v112,s[24:27],s56 offen offset:3072 sc1\n"
"v_mfma_f32_16x16x32_bf16 v[252:255],v[44:47],a[28:31],v[252:255]\n"
"s_nop 0\n"
"v_accvgpr_read_b32 v116,a160\n"
"v_mfma_f32_16x16x32_bf16 v[220:223],v[44:47],a[20:23],v[220:223]\n"
"v_accvgpr_read_b32 v117,a161\n"
"v_accvgpr_read_b32 v118,a162\n"
"v_mfma_f32_16x16x32_bf16 v[220:223],v[40:43],a[16:19],v[220:223]\n"
"buffer_load_dwordx4 v112,s[24:27],s76 offen offset:2048 sc1 lds\n"
"v_mfma_f32_16x16x32_bf16 v[188:191],v[40:43],a[8:11],v[188:191]\n"
"v_accvgpr_read_b32 v119,a163\n"
"v_accvgpr_read_b32 v120,a164\n"
"v_mfma_f32_16x16x32_bf16 v[188:191],v[44:47],a[12:15],v[188:191]\n"
"v_accvgpr_read_b32 v121,a165\n"
"v_accvgpr_read_b32 v122,a166\n"
"v_mfma_f32_16x16x32_bf16 v[156:159],v[44:47],a[4:7],v[156:159]\n"
"buffer_load_dwordx4 v112,s[24:27],s76 offen offset:3072 sc1 lds\n"
"v_mfma_f32_16x16x32_bf16 v[156:159],v[40:43],a[0:3],v[156:159]\n"
"s_xor_b32 s76,s76,49152\n"
"v_accvgpr_read_b32 v123,a167\n"
"s_waitcnt vmcnt(27) lgkmcnt(0)\n"
"s_barrier\n"
"v_mfma_f32_16x16x32_bf16 v[128:131],v[48:51],v[0:3],v[128:131]\n"
"ds_read_b64_tr_b16 v[32:33],v98 offset:0\n"
"ds_read_b64_tr_b16 v[40:41],v99 offset:0\n"
"v_mfma_f32_16x16x32_bf16 v[128:131],v[52:55],v[4:7],v[128:131]\n"
"ds_read_b64_tr_b16 v[34:35],v98 offset:1024\n"
"ds_read_b64_tr_b16 v[42:43],v99 offset:1024\n"
"v_mfma_f32_16x16x32_bf16 v[160:163],v[52:55],v[12:15],v[160:163]\n"
"ds_read_b64_tr_b16 v[36:37],v98 offset:8192\n"
"ds_read_b64_tr_b16 v[44:45],v99 offset:8192\n"
"v_mfma_f32_16x16x32_bf16 v[160:163],v[48:51],v[8:11],v[160:163]\n"
"s_add_u32 m0,s85,131072\n"
"ds_read_b64_tr_b16 v[38:39],v98 offset:9216\n"
"ds_read_b64_tr_b16 v[46:47],v99 offset:9216\n"
"v_mfma_f32_16x16x32_bf16 v[192:195],v[48:51],v[16:19],v[192:195]\n"
"buffer_load_dwordx4 v110,s[28:31],s86 offen lds\n"
"v_mfma_f32_16x16x32_bf16 v[192:195],v[52:55],v[20:23],v[192:195]\n"
"v_cvt_pk_bf16_f32 v116,v116,v117\n"
"v_cvt_pk_bf16_f32 v117,v118,v119\n"
"v_mfma_f32_16x16x32_bf16 v[224:227],v[52:55],v[28:31],v[224:227]\n"
"s_add_u32 s56,s56,4096\n"
"v_cvt_pk_bf16_f32 v118,v120,v121\n"
"v_cvt_pk_bf16_f32 v119,v122,v123\n"
"v_mfma_f32_16x16x32_bf16 v[224:227],v[48:51],v[24:27],v[224:227]\n"
"buffer_store_dwordx4 v[116:119],v112,s[24:27],s56 offen offset:0 sc1\n"
"v_mfma_f32_16x16x32_bf16 v[228:231],v[56:59],v[24:27],v[228:231]\n"
"ds_read_b64_tr_b16 v[48:49],v100 offset:0\n"
"v_accvgpr_read_b32 v116,a168\n"
"v_mfma_f32_16x16x32_bf16 v[228:231],v[60:63],v[28:31],v[228:231]\n"
"ds_read_b64_tr_b16 v[50:51],v100 offset:1024\n"
"v_accvgpr_read_b32 v117,a169\n"
"v_mfma_f32_16x16x32_bf16 v[196:199],v[60:63],v[20:23],v[196:199]\n"
"ds_read_b64_tr_b16 v[52:53],v100 offset:8192\n"
"v_accvgpr_read_b32 v118,a170\n"
"v_mfma_f32_16x16x32_bf16 v[196:199],v[56:59],v[16:19],v[196:199]\n"
"s_add_u32 m0,s85,132096\n"
"ds_read_b64_tr_b16 v[54:55],v100 offset:9216\n"
"v_mfma_f32_16x16x32_bf16 v[164:167],v[56:59],v[8:11],v[164:167]\n"
"buffer_load_dwordx4 v111,s[28:31],s86 offen lds\n"
"v_mfma_f32_16x16x32_bf16 v[164:167],v[60:63],v[12:15],v[164:167]\n"
"s_add_u32 m0,s85,139264\n"
"s_xor_b32 s86,s86,262144\n"
"v_mfma_f32_16x16x32_bf16 v[132:135],v[60:63],v[4:7],v[132:135]\n"
"buffer_load_dwordx4 v110,s[28:31],s86 offen lds\n"
"v_mfma_f32_16x16x32_bf16 v[132:135],v[56:59],v[0:3],v[132:135]\n"
"v_accvgpr_read_b32 v119,a171\n"
"v_accvgpr_read_b32 v120,a172\n"
"v_mfma_f32_16x16x32_bf16 v[136:139],v[64:67],v[0:3],v[136:139]\n"
"ds_read_b64_tr_b16 v[56:57],v101 offset:0\n"
"v_accvgpr_read_b32 v121,a173\n"
"v_mfma_f32_16x16x32_bf16 v[136:139],v[68:71],v[4:7],v[136:139]\n"
"ds_read_b64_tr_b16 v[58:59],v101 offset:1024\n"
"v_accvgpr_read_b32 v122,a174\n"
"v_mfma_f32_16x16x32_bf16 v[168:171],v[68:71],v[12:15],v[168:171]\n"
"ds_read_b64_tr_b16 v[60:61],v101 offset:8192\n"
"v_accvgpr_read_b32 v123,a175\n"
"v_mfma_f32_16x16x32_bf16 v[168:171],v[64:67],v[8:11],v[168:171]\n"
"s_add_u32 m0,s85,140288\n"
"ds_read_b64_tr_b16 v[62:63],v101 offset:9216\n"
"v_mfma_f32_16x16x32_bf16 v[200:203],v[64:67],v[16:19],v[200:203]\n"
"buffer_load_dwordx4 v111,s[28:31],s86 offen lds\n"
"v_mfma_f32_16x16x32_bf16 v[200:203],v[68:71],v[20:23],v[200:203]\n"
"s_xor_b32 s86,s86,8126464\n"
"v_cvt_pk_bf16_f32 v116,v116,v117\n"
"v_cvt_pk_bf16_f32 v117,v118,v119\n"
"v_mfma_f32_16x16x32_bf16 v[232:235],v[68:71],v[28:31],v[232:235]\n"
"v_cvt_pk_bf16_f32 v118,v120,v121\n"
"v_cvt_pk_bf16_f32 v119,v122,v123\n"
"v_mfma_f32_16x16x32_bf16 v[232:235],v[64:67],v[24:27],v[232:235]\n"
"buffer_store_dwordx4 v[116:119],v112,s[24:27],s56 offen offset:1024 sc1\n"
"v_mfma_f32_16x16x32_bf16 v[236:239],v[72:75],v[24:27],v[236:239]\n"
"ds_read_b64_tr_b16 v[64:65],v102 offset:0\n"
"v_accvgpr_read_b32 v116,a176\n"
"v_mfma_f32_16x16x32_bf16 v[236:239],v[76:79],v[28:31],v[236:239]\n"
"ds_read_b64_tr_b16 v[66:67],v102 offset:1024\n"
"v_accvgpr_read_b32 v117,a177\n"
"v_mfma_f32_16x16x32_bf16 v[204:207],v[76:79],v[20:23],v[204:207]\n"
"ds_read_b64_tr_b16 v[68:69],v102 offset:8192\n"
"v_accvgpr_read_b32 v118,a178\n"
"v_mfma_f32_16x16x32_bf16 v[204:207],v[72:75],v[16:19],v[204:207]\n"
"s_add_u32 m0,s78,0\n"
"ds_read_b64_tr_b16 v[70:71],v102 offset:9216\n"
"v_accvgpr_read_b32 v121,a181\n"
"v_mfma_f32_16x16x32_bf16 v[172:175],v[72:75],v[8:11],v[172:175]\n"
"buffer_load_dwordx4 v112,s[24:27],s76 offen offset:0 sc1 lds\n"
"v_mfma_f32_16x16x32_bf16 v[172:175],v[76:79],v[12:15],v[172:175]\n"
"v_accvgpr_read_b32 v119,a179\n"
"v_accvgpr_read_b32 v120,a180\n"
"v_mfma_f32_16x16x32_bf16 v[140:143],v[76:79],v[4:7],v[140:143]\n"
"buffer_load_dwordx4 v112,s[24:27],s76 offen offset:1024 sc1 lds\n"
"v_mfma_f32_16x16x32_bf16 v[140:143],v[72:75],v[0:3],v[140:143]\n"
"v_accvgpr_read_b32 v122,a182\n"
"s_waitcnt vmcnt(24)\n"
"v_mfma_f32_16x16x32_bf16 v[144:147],v[80:83],v[0:3],v[144:147]\n"
"ds_read_b64_tr_b16 v[72:73],v103 offset:0\n"
"v_accvgpr_read_b32 v123,a183\n"
"v_mfma_f32_16x16x32_bf16 v[144:147],v[84:87],v[4:7],v[144:147]\n"
"ds_read_b64_tr_b16 v[74:75],v103 offset:1024\n"
"v_cvt_pk_bf16_f32 v116,v116,v117\n"
"v_mfma_f32_16x16x32_bf16 v[176:179],v[84:87],v[12:15],v[176:179]\n"
"ds_read_b64_tr_b16 v[76:77],v103 offset:8192\n"
"v_cvt_pk_bf16_f32 v117,v118,v119\n"
"v_mfma_f32_16x16x32_bf16 v[176:179],v[80:83],v[8:11],v[176:179]\n"
"ds_read_b64_tr_b16 v[78:79],v103 offset:9216\n"
"v_cvt_pk_bf16_f32 v118,v120,v121\n"
"v_mfma_f32_16x16x32_bf16 v[208:211],v[80:83],v[16:19],v[208:211]\n"
"buffer_load_dwordx4 v112,s[24:27],s76 offen offset:2048 sc1 lds\n"
"v_mfma_f32_16x16x32_bf16 v[208:211],v[84:87],v[20:23],v[208:211]\n"
"ds_read_b128 a[0:3],v96 offset:32768\n"
"v_cvt_pk_bf16_f32 v119,v122,v123\n"
"v_mfma_f32_16x16x32_bf16 v[240:243],v[84:87],v[28:31],v[240:243]\n"
"buffer_store_dwordx4 v[116:119],v112,s[24:27],s56 offen offset:2048 sc1\n"
"v_mfma_f32_16x16x32_bf16 v[240:243],v[80:83],v[24:27],v[240:243]\n"
"ds_read_b128 a[8:11],v96 offset:33792\n"
"v_accvgpr_read_b32 v116,a184\n"
"v_accvgpr_read_b32 v117,a185\n"
"v_mfma_f32_16x16x32_bf16 v[244:247],v[88:91],v[24:27],v[244:247]\n"
"ds_read_b64_tr_b16 v[80:81],v104 offset:0\n"
"v_accvgpr_read_b32 v118,a186\n"
"v_mfma_f32_16x16x32_bf16 v[244:247],v[92:95],v[28:31],v[244:247]\n"
"ds_read_b64_tr_b16 v[82:83],v104 offset:1024\n"
"v_accvgpr_read_b32 v119,a187\n"
"v_mfma_f32_16x16x32_bf16 v[212:215],v[92:95],v[20:23],v[212:215]\n"
"ds_read_b64_tr_b16 v[84:85],v104 offset:8192\n"
"v_accvgpr_read_b32 v120,a188\n"
"v_mfma_f32_16x16x32_bf16 v[212:215],v[88:91],v[16:19],v[212:215]\n"
"ds_read_b64_tr_b16 v[86:87],v104 offset:9216\n"
"v_accvgpr_read_b32 v121,a189\n"
"v_mfma_f32_16x16x32_bf16 v[180:183],v[88:91],v[8:11],v[180:183]\n"
"buffer_load_dwordx4 v112,s[24:27],s76 offen offset:3072 sc1 lds\n"
"v_mfma_f32_16x16x32_bf16 v[180:183],v[92:95],v[12:15],v[180:183]\n"
"s_add_u32 m0,s78,16384\n"
"s_xor_b32 s76,s76,16384\n"
"ds_read_b128 a[16:19],v96 offset:34816\n"
"v_mfma_f32_16x16x32_bf16 v[148:151],v[92:95],v[4:7],v[148:151]\n"
"buffer_load_dwordx4 v112,s[24:27],s76 offen offset:0 sc1 lds\n"
"v_mfma_f32_16x16x32_bf16 v[148:151],v[88:91],v[0:3],v[148:151]\n"
"ds_read_b128 a[24:27],v96 offset:35840\n"
"v_accvgpr_read_b32 v122,a190\n"
"v_mfma_f32_16x16x32_bf16 v[152:155],a[32:35],v[0:3],v[152:155]\n"
"ds_read_b64_tr_b16 v[88:89],v105 offset:0\n"
"v_accvgpr_read_b32 v123,a191\n"
"v_mfma_f32_16x16x32_bf16 v[152:155],a[36:39],v[4:7],v[152:155]\n"
"ds_read_b64_tr_b16 v[90:91],v105 offset:1024\n"
"v_cvt_pk_bf16_f32 v116,v116,v117\n"
"v_mfma_f32_16x16x32_bf16 v[184:187],a[36:39],v[12:15],v[184:187]\n"
"ds_read_b64_tr_b16 v[92:93],v105 offset:8192\n"
"v_cvt_pk_bf16_f32 v117,v118,v119\n"
"v_mfma_f32_16x16x32_bf16 v[184:187],a[32:35],v[8:11],v[184:187]\n"
"ds_read_b64_tr_b16 v[94:95],v105 offset:9216\n"
"v_cvt_pk_bf16_f32 v118,v120,v121\n"
"v_mfma_f32_16x16x32_bf16 v[216:219],a[32:35],v[16:19],v[216:219]\n"
"ds_read_b128 a[4:7],v96 offset:49152\n"
"v_cvt_pk_bf16_f32 v119,v122,v123\n"
"v_mfma_f32_16x16x32_bf16 v[216:219],a[36:39],v[20:23],v[216:219]\n"
"ds_read_b128 a[12:15],v96 offset:50176\n"
"buffer_store_dwordx4 v[116:119],v112,s[24:27],s56 offen offset:3072 sc1\n"
"v_mfma_f32_16x16x32_bf16 v[248:251],a[36:39],v[28:31],v[248:251]\n"
"ds_read_b128 a[20:23],v96 offset:51200\n"
"v_accvgpr_read_b32 v116,a248\n"
"v_mfma_f32_16x16x32_bf16 v[248:251],a[32:35],v[24:27],v[248:251]\n"
"ds_read_b128 a[28:31],v96 offset:52224\n"
"v_accvgpr_read_b32 v117,a249\n"
"v_mfma_f32_16x16x32_bf16 v[252:255],a[40:43],v[24:27],v[252:255]\n"
"buffer_load_dwordx4 v112,s[24:27],s76 offen offset:1024 sc1 lds\n"
"v_mfma_f32_16x16x32_bf16 v[252:255],a[44:47],v[28:31],v[252:255]\n"
"v_accvgpr_read_b32 v118,a250\n"
"v_accvgpr_read_b32 v119,a251\n"
"v_mfma_f32_16x16x32_bf16 v[220:223],a[44:47],v[20:23],v[220:223]\n"
"v_accvgpr_read_b32 v120,a252\n"
"v_accvgpr_read_b32 v121,a253\n"
"v_mfma_f32_16x16x32_bf16 v[220:223],a[40:43],v[16:19],v[220:223]\n"
"buffer_load_dwordx4 v112,s[24:27],s76 offen offset:2048 sc1 lds\n"
"v_mfma_f32_16x16x32_bf16 v[188:191],a[40:43],v[8:11],v[188:191]\n"
"v_accvgpr_read_b32 v122,a254\n"
"v_accvgpr_read_b32 v123,a255\n"
"v_mfma_f32_16x16x32_bf16 v[188:191],a[44:47],v[12:15],v[188:191]\n"
"v_cvt_pk_bf16_f32 v116,v116,v117\n"
"v_cvt_pk_bf16_f32 v117,v118,v119\n"
"v_mfma_f32_16x16x32_bf16 v[156:159],a[44:47],v[4:7],v[156:159]\n"
"buffer_load_dwordx4 v112,s[24:27],s76 offen offset:3072 sc1 lds\n"
"v_mfma_f32_16x16x32_bf16 v[156:159],a[40:43],v[0:3],v[156:159]\n"
"s_xor_b32 s76,s76,507904\n"
"v_cvt_pk_bf16_f32 v118,v120,v121\n"
"s_waitcnt vmcnt(27) lgkmcnt(0)\n"
"s_barrier\n"
"v_mfma_f32_16x16x32_bf16 v[128:131],v[32:35],a[0:3],v[128:131]\n"
"ds_read_b64_tr_b16 a[32:33],v98 offset:49152\n"
"ds_read_b64_tr_b16 a[40:41],v99 offset:49152\n"
"v_mfma_f32_16x16x32_bf16 v[128:131],v[36:39],a[4:7],v[128:131]\n"
"ds_read_b64_tr_b16 a[34:35],v98 offset:50176\n"
"ds_read_b64_tr_b16 a[42:43],v99 offset:50176\n"
"v_mfma_f32_16x16x32_bf16 v[160:163],v[36:39],a[12:15],v[160:163]\n"
"ds_read_b64_tr_b16 a[36:37],v98 offset:57344\n"
"ds_read_b64_tr_b16 a[44:45],v99 offset:57344\n"
"v_mfma_f32_16x16x32_bf16 v[160:163],v[32:35],a[8:11],v[160:163]\n"
"s_add_u32 m0,s85,98304\n"
"ds_read_b64_tr_b16 a[38:39],v98 offset:58368\n"
"ds_read_b64_tr_b16 a[46:47],v99 offset:58368\n"
"v_mfma_f32_16x16x32_bf16 v[192:195],v[32:35],a[16:19],v[192:195]\n"
"buffer_load_dwordx4 v110,s[28:31],s86 offen lds\n"
"v_mfma_f32_16x16x32_bf16 v[192:195],v[36:39],a[20:23],v[192:195]\n"
"v_cvt_pk_bf16_f32 v119,v122,v123\n"
"v_accvgpr_write_b32 a252,v116\n"
"v_mfma_f32_16x16x32_bf16 v[224:227],v[36:39],a[28:31],v[224:227]\n"
"v_accvgpr_write_b32 a253,v117\n"
"v_accvgpr_write_b32 a254,v118\n"
"v_mfma_f32_16x16x32_bf16 v[224:227],v[32:35],a[24:27],v[224:227]\n"
"v_accvgpr_write_b32 a255,v119\n"
"v_accvgpr_read_b32 v116,a240\n"
"v_mfma_f32_16x16x32_bf16 v[228:231],v[40:43],a[24:27],v[228:231]\n"
"ds_read_b64_tr_b16 v[32:33],v100 offset:49152\n"
"v_accvgpr_read_b32 v117,a241\n"
"v_mfma_f32_16x16x32_bf16 v[228:231],v[44:47],a[28:31],v[228:231]\n"
"ds_read_b64_tr_b16 v[34:35],v100 offset:50176\n"
"v_accvgpr_read_b32 v118,a242\n"
"v_mfma_f32_16x16x32_bf16 v[196:199],v[44:47],a[20:23],v[196:199]\n"
"ds_read_b64_tr_b16 v[36:37],v100 offset:57344\n"
"v_accvgpr_read_b32 v119,a243\n"
"v_mfma_f32_16x16x32_bf16 v[196:199],v[40:43],a[16:19],v[196:199]\n"
"s_add_u32 m0,s85,99328\n"
"ds_read_b64_tr_b16 v[38:39],v100 offset:58368\n"
"v_mfma_f32_16x16x32_bf16 v[164:167],v[40:43],a[8:11],v[164:167]\n"
"buffer_load_dwordx4 v111,s[28:31],s86 offen lds\n"
"v_mfma_f32_16x16x32_bf16 v[164:167],v[44:47],a[12:15],v[164:167]\n"
"s_add_u32 m0,s85,106496\n"
"s_xor_b32 s86,s86,262144\n"
"v_mfma_f32_16x16x32_bf16 v[132:135],v[44:47],a[4:7],v[132:135]\n"
"buffer_load_dwordx4 v110,s[28:31],s86 offen lds\n"
"v_mfma_f32_16x16x32_bf16 v[132:135],v[40:43],a[0:3],v[132:135]\n"
"v_accvgpr_read_b32 v120,a244\n"
"v_accvgpr_read_b32 v121,a245\n"
"v_mfma_f32_16x16x32_bf16 v[136:139],v[48:51],a[0:3],v[136:139]\n"
"ds_read_b64_tr_b16 v[40:41],v101 offset:49152\n"
"v_accvgpr_read_b32 v122,a246\n"
"v_mfma_f32_16x16x32_bf16 v[136:139],v[52:55],a[4:7],v[136:139]\n"
"ds_read_b64_tr_b16 v[42:43],v101 offset:50176\n"
"v_accvgpr_read_b32 v123,a247\n"
"v_mfma_f32_16x16x32_bf16 v[168:171],v[52:55],a[12:15],v[168:171]\n"
"ds_read_b64_tr_b16 v[44:45],v101 offset:57344\n"
"v_cvt_pk_bf16_f32 v116,v116,v117\n"
"v_mfma_f32_16x16x32_bf16 v[168:171],v[48:51],a[8:11],v[168:171]\n"
"s_add_u32 m0,s85,107520\n"
"ds_read_b64_tr_b16 v[46:47],v101 offset:58368\n"
"v_mfma_f32_16x16x32_bf16 v[200:203],v[48:51],a[16:19],v[200:203]\n"
"buffer_load_dwordx4 v111,s[28:31],s86 offen lds\n"
"v_mfma_f32_16x16x32_bf16 v[200:203],v[52:55],a[20:23],v[200:203]\n"
"s_xor_b32 s86,s86,786432\n"
"v_cvt_pk_bf16_f32 v117,v118,v119\n"
"v_mfma_f32_16x16x32_bf16 v[232:235],v[52:55],a[28:31],v[232:235]\n"
"v_cvt_pk_bf16_f32 v118,v120,v121\n"
"v_cvt_pk_bf16_f32 v119,v122,v123\n"
"v_mfma_f32_16x16x32_bf16 v[232:235],v[48:51],a[24:27],v[232:235]\n"
"v_accvgpr_write_b32 a248,v116\n"
"v_accvgpr_write_b32 a249,v117\n"
"v_mfma_f32_16x16x32_bf16 v[236:239],v[56:59],a[24:27],v[236:239]\n"
"ds_read_b64_tr_b16 v[48:49],v102 offset:49152\n"
"v_accvgpr_write_b32 a250,v118\n"
"v_mfma_f32_16x16x32_bf16 v[236:239],v[60:63],a[28:31],v[236:239]\n"
"ds_read_b64_tr_b16 v[50:51],v102 offset:50176\n"
"v_accvgpr_write_b32 a251,v119\n"
"v_mfma_f32_16x16x32_bf16 v[204:207],v[60:63],a[20:23],v[204:207]\n"
"ds_read_b64_tr_b16 v[52:53],v102 offset:57344\n"
"v_accvgpr_read_b32 v116,a232\n"
"v_mfma_f32_16x16x32_bf16 v[204:207],v[56:59],a[16:19],v[204:207]\n"
"s_add_u32 m0,s78,32768\n"
"ds_read_b64_tr_b16 v[54:55],v102 offset:58368\n"
"v_accvgpr_read_b32 v119,a235\n"
"v_mfma_f32_16x16x32_bf16 v[172:175],v[56:59],a[8:11],v[172:175]\n"
"buffer_load_dwordx4 v112,s[24:27],s76 offen offset:0 sc1 lds\n"
"v_mfma_f32_16x16x32_bf16 v[172:175],v[60:63],a[12:15],v[172:175]\n"
"v_accvgpr_read_b32 v117,a233\n"
"v_accvgpr_read_b32 v118,a234\n"
"v_mfma_f32_16x16x32_bf16 v[140:143],v[60:63],a[4:7],v[140:143]\n"
"buffer_load_dwordx4 v112,s[24:27],s76 offen offset:1024 sc1 lds\n"
"v_mfma_f32_16x16x32_bf16 v[140:143],v[56:59],a[0:3],v[140:143]\n"
"v_accvgpr_read_b32 v120,a236\n"
"s_waitcnt vmcnt(22)\n"
"v_mfma_f32_16x16x32_bf16 v[144:147],v[64:67],a[0:3],v[144:147]\n"
"ds_read_b64_tr_b16 v[56:57],v103 offset:49152\n"
"v_accvgpr_read_b32 v121,a237\n"
"v_mfma_f32_16x16x32_bf16 v[144:147],v[68:71],a[4:7],v[144:147]\n"
"ds_read_b64_tr_b16 v[58:59],v103 offset:50176\n"
"v_accvgpr_read_b32 v122,a238\n"
"v_mfma_f32_16x16x32_bf16 v[176:179],v[68:71],a[12:15],v[176:179]\n"
"ds_read_b64_tr_b16 v[60:61],v103 offset:57344\n"
"v_accvgpr_read_b32 v123,a239\n"
"v_mfma_f32_16x16x32_bf16 v[176:179],v[64:67],a[8:11],v[176:179]\n"
"ds_read_b64_tr_b16 v[62:63],v103 offset:58368\n"
"v_cvt_pk_bf16_f32 v116,v116,v117\n"
"v_mfma_f32_16x16x32_bf16 v[208:211],v[64:67],a[16:19],v[208:211]\n"
"buffer_load_dwordx4 v112,s[24:27],s76 offen offset:2048 sc1 lds\n"
"v_mfma_f32_16x16x32_bf16 v[208:211],v[68:71],a[20:23],v[208:211]\n"
"ds_read_b128 v[0:3],v97 offset:0\n"
"v_cvt_pk_bf16_f32 v117,v118,v119\n"
"v_mfma_f32_16x16x32_bf16 v[240:243],v[68:71],a[28:31],v[240:243]\n"
"v_cvt_pk_bf16_f32 v118,v120,v121\n"
"v_cvt_pk_bf16_f32 v119,v122,v123\n"
"v_mfma_f32_16x16x32_bf16 v[240:243],v[64:67],a[24:27],v[240:243]\n"
"ds_read_b128 v[8:11],v97 offset:1024\n"
"v_accvgpr_write_b32 a244,v116\n"
"v_mfma_f32_16x16x32_bf16 v[244:247],v[72:75],a[24:27],v[244:247]\n"
"ds_read_b64_tr_b16 v[64:65],v104 offset:49152\n"
"v_accvgpr_write_b32 a245,v117\n"
"v_mfma_f32_16x16x32_bf16 v[244:247],v[76:79],a[28:31],v[244:247]\n"
"ds_read_b64_tr_b16 v[66:67],v104 offset:50176\n"
"v_accvgpr_write_b32 a246,v118\n"
"v_mfma_f32_16x16x32_bf16 v[212:215],v[76:79],a[20:23],v[212:215]\n"
"ds_read_b64_tr_b16 v[68:69],v104 offset:57344\n"
"v_accvgpr_write_b32 a247,v119\n"
"v_mfma_f32_16x16x32_bf16 v[212:215],v[72:75],a[16:19],v[212:215]\n"
"ds_read_b64_tr_b16 v[70:71],v104 offset:58368\n"
"v_accvgpr_read_b32 v116,a224\n"
"v_mfma_f32_16x16x32_bf16 v[180:183],v[72:75],a[8:11],v[180:183]\n"
"buffer_load_dwordx4 v112,s[24:27],s76 offen offset:3072 sc1 lds\n"
"v_mfma_f32_16x16x32_bf16 v[180:183],v[76:79],a[12:15],v[180:183]\n"
"s_add_u32 m0,s78,49152\n"
"s_xor_b32 s76,s76,16384\n"
"ds_read_b128 v[16:19],v97 offset:2048\n"
"v_mfma_f32_16x16x32_bf16 v[148:151],v[76:79],a[4:7],v[148:151]\n"
"buffer_load_dwordx4 v112,s[24:27],s76 offen offset:0 sc1 lds\n"
"v_mfma_f32_16x16x32_bf16 v[148:151],v[72:75],a[0:3],v[148:151]\n"
"ds_read_b128 v[24:27],v97 offset:3072\n"
"v_accvgpr_read_b32 v117,a225\n"
"v_mfma_f32_16x16x32_bf16 v[152:155],v[80:83],a[0:3],v[152:155]\n"
"ds_read_b64_tr_b16 v[72:73],v105 offset:49152\n"
"v_accvgpr_read_b32 v118,a226\n"
"v_mfma_f32_16x16x32_bf16 v[152:155],v[84:87],a[4:7],v[152:155]\n"
"ds_read_b64_tr_b16 v[74:75],v105 offset:50176\n"
"v_accvgpr_read_b32 v119,a227\n"
"v_mfma_f32_16x16x32_bf16 v[184:187],v[84:87],a[12:15],v[184:187]\n"
"ds_read_b64_tr_b16 v[76:77],v105 offset:57344\n"
"v_accvgpr_read_b32 v120,a228\n"
"v_mfma_f32_16x16x32_bf16 v[184:187],v[80:83],a[8:11],v[184:187]\n"
"ds_read_b64_tr_b16 v[78:79],v105 offset:58368\n"
"v_accvgpr_read_b32 v121,a229\n"
"v_mfma_f32_16x16x32_bf16 v[216:219],v[80:83],a[16:19],v[216:219]\n"
"ds_read_b128 v[4:7],v97 offset:16384\n"
"v_accvgpr_read_b32 v122,a230\n"
"v_mfma_f32_16x16x32_bf16 v[216:219],v[84:87],a[20:23],v[216:219]\n"
"ds_read_b128 v[12:15],v97 offset:17408\n"
"v_accvgpr_read_b32 v123,a231\n"
"v_mfma_f32_16x16x32_bf16 v[248:251],v[84:87],a[28:31],v[248:251]\n"
"ds_read_b128 v[20:23],v97 offset:18432\n"
"v_cvt_pk_bf16_f32 v116,v116,v117\n"
"v_mfma_f32_16x16x32_bf16 v[248:251],v[80:83],a[24:27],v[248:251]\n"
"ds_read_b128 v[28:31],v97 offset:19456\n"
"v_cvt_pk_bf16_f32 v117,v118,v119\n"
"v_mfma_f32_16x16x32_bf16 v[252:255],v[88:91],a[24:27],v[252:255]\n"
"buffer_load_dwordx4 v112,s[24:27],s76 offen offset:1024 sc1 lds\n"
"v_mfma_f32_16x16x32_bf16 v[252:255],v[92:95],a[28:31],v[252:255]\n"
"v_cvt_pk_bf16_f32 v118,v120,v121\n"
"v_cvt_pk_bf16_f32 v119,v122,v123\n"
"v_mfma_f32_16x16x32_bf16 v[220:223],v[92:95],a[20:23],v[220:223]\n"
"v_accvgpr_write_b32 a240,v116\n"
"v_accvgpr_write_b32 a241,v117\n"
"v_mfma_f32_16x16x32_bf16 v[220:223],v[88:91],a[16:19],v[220:223]\n"
"buffer_load_dwordx4 v112,s[24:27],s76 offen offset:2048 sc1 lds\n"
"v_mfma_f32_16x16x32_bf16 v[188:191],v[88:91],a[8:11],v[188:191]\n"
"v_accvgpr_write_b32 a242,v118\n"
"v_accvgpr_write_b32 a243,v119\n"
"v_mfma_f32_16x16x32_bf16 v[188:191],v[92:95],a[12:15],v[188:191]\n"
"v_accvgpr_read_b32 v116,a216\n"
"v_accvgpr_read_b32 v117,a217\n"
"v_mfma_f32_16x16x32_bf16 v[156:159],v[92:95],a[4:7],v[156:159]\n"
"buffer_load_dwordx4 v112,s[24:27],s76 offen offset:3072 sc1 lds\n"
"v_mfma_f32_16x16x32_bf16 v[156:159],v[88:91],a[0:3],v[156:159]\n"
"s_xor_b32 s76,s76,49152\n"
"v_accvgpr_read_b32 v118,a218\n"
"s_waitcnt vmcnt(23) lgkmcnt(0)\n"
"s_barrier\n"
"v_mfma_f32_16x16x32_bf16 v[128:131],a[32:35],v[0:3],v[128:131]\n"
"ds_read_b64_tr_b16 v[80:81],v98 offset:32768\n"
"ds_read_b64_tr_b16 v[88:89],v99 offset:32768\n"
"v_mfma_f32_16x16x32_bf16 v[128:131],a[36:39],v[4:7],v[128:131]\n"
"ds_read_b64_tr_b16 v[82:83],v98 offset:33792\n"
"ds_read_b64_tr_b16 v[90:91],v99 offset:33792\n"
"v_mfma_f32_16x16x32_bf16 v[160:163],a[36:39],v[12:15],v[160:163]\n"
"ds_read_b64_tr_b16 v[84:85],v98 offset:40960\n"
"ds_read_b64_tr_b16 v[92:93],v99 offset:40960\n"
"v_mfma_f32_16x16x32_bf16 v[160:163],a[32:35],v[8:11],v[160:163]\n"
"s_add_u32 m0,s85,147456\n"
"ds_read_b64_tr_b16 v[86:87],v98 offset:41984\n"
"ds_read_b64_tr_b16 v[94:95],v99 offset:41984\n"
"v_mfma_f32_16x16x32_bf16 v[192:195],a[32:35],v[16:19],v[192:195]\n"
"buffer_load_dwordx4 v110,s[28:31],s86 offen lds\n"
"v_mfma_f32_16x16x32_bf16 v[192:195],a[36:39],v[20:23],v[192:195]\n"
"v_accvgpr_read_b32 v119,a219\n"
"v_accvgpr_read_b32 v120,a220\n"
"v_mfma_f32_16x16x32_bf16 v[224:227],a[36:39],v[28:31],v[224:227]\n"
"v_accvgpr_read_b32 v121,a221\n"
"v_accvgpr_read_b32 v122,a222\n"
"v_mfma_f32_16x16x32_bf16 v[224:227],a[32:35],v[24:27],v[224:227]\n"
"v_accvgpr_read_b32 v123,a223\n"
"v_cvt_pk_bf16_f32 v116,v116,v117\n"
"v_mfma_f32_16x16x32_bf16 v[228:231],a[40:43],v[24:27],v[228:231]\n"
"ds_read_b64_tr_b16 a[32:33],v100 offset:32768\n"
"v_cvt_pk_bf16_f32 v117,v118,v119\n"
"v_mfma_f32_16x16x32_bf16 v[228:231],a[44:47],v[28:31],v[228:231]\n"
"ds_read_b64_tr_b16 a[34:35],v100 offset:33792\n"
"v_cvt_pk_bf16_f32 v118,v120,v121\n"
"v_mfma_f32_16x16x32_bf16 v[196:199],a[44:47],v[20:23],v[196:199]\n"
"ds_read_b64_tr_b16 a[36:37],v100 offset:40960\n"
"v_cvt_pk_bf16_f32 v119,v122,v123\n"
"v_mfma_f32_16x16x32_bf16 v[196:199],a[40:43],v[16:19],v[196:199]\n"
"s_add_u32 m0,s85,148480\n"
"ds_read_b64_tr_b16 a[38:39],v100 offset:41984\n"
"v_mfma_f32_16x16x32_bf16 v[164:167],a[40:43],v[8:11],v[164:167]\n"
"buffer_load_dwordx4 v111,s[28:31],s86 offen lds\n"
"v_mfma_f32_16x16x32_bf16 v[164:167],a[44:47],v[12:15],v[164:167]\n"
"s_add_u32 m0,s85,155648\n"
"s_xor_b32 s86,s86,262144\n"
"v_mfma_f32_16x16x32_bf16 v[132:135],a[44:47],v[4:7],v[132:135]\n"
"buffer_load_dwordx4 v110,s[28:31],s86 offen lds\n"
"v_mfma_f32_16x16x32_bf16 v[132:135],a[40:43],v[0:3],v[132:135]\n"
"v_accvgpr_write_b32 a236,v116\n"
"v_accvgpr_write_b32 a237,v117\n"
"v_mfma_f32_16x16x32_bf16 v[136:139],v[32:35],v[0:3],v[136:139]\n"
"ds_read_b64_tr_b16 a[40:41],v101 offset:32768\n"
"v_accvgpr_write_b32 a238,v118\n"
"v_mfma_f32_16x16x32_bf16 v[136:139],v[36:39],v[4:7],v[136:139]\n"
"ds_read_b64_tr_b16 a[42:43],v101 offset:33792\n"
"v_accvgpr_write_b32 a239,v119\n"
"v_mfma_f32_16x16x32_bf16 v[168:171],v[36:39],v[12:15],v[168:171]\n"
"ds_read_b64_tr_b16 a[44:45],v101 offset:40960\n"
"v_accvgpr_read_b32 v116,a208\n"
"v_mfma_f32_16x16x32_bf16 v[168:171],v[32:35],v[8:11],v[168:171]\n"
"s_add_u32 m0,s85,156672\n"
"ds_read_b64_tr_b16 a[46:47],v101 offset:41984\n"
"v_mfma_f32_16x16x32_bf16 v[200:203],v[32:35],v[16:19],v[200:203]\n"
"buffer_load_dwordx4 v111,s[28:31],s86 offen lds\n"
"v_mfma_f32_16x16x32_bf16 v[200:203],v[36:39],v[20:23],v[200:203]\n"
"s_xor_b32 s86,s86,1835008\n"
"v_accvgpr_read_b32 v117,a209\n"
"v_mfma_f32_16x16x32_bf16 v[232:235],v[36:39],v[28:31],v[232:235]\n"
"v_accvgpr_read_b32 v118,a210\n"
"v_accvgpr_read_b32 v119,a211\n"
"v_mfma_f32_16x16x32_bf16 v[232:235],v[32:35],v[24:27],v[232:235]\n"
"v_accvgpr_read_b32 v120,a212\n"
"v_accvgpr_read_b32 v121,a213\n"
"v_mfma_f32_16x16x32_bf16 v[236:239],v[40:43],v[24:27],v[236:239]\n"
"ds_read_b64_tr_b16 v[32:33],v102 offset:32768\n"
"v_accvgpr_read_b32 v122,a214\n"
"v_mfma_f32_16x16x32_bf16 v[236:239],v[44:47],v[28:31],v[236:239]\n"
"ds_read_b64_tr_b16 v[34:35],v102 offset:33792\n"
"v_accvgpr_read_b32 v123,a215\n"
"v_mfma_f32_16x16x32_bf16 v[204:207],v[44:47],v[20:23],v[204:207]\n"
"ds_read_b64_tr_b16 v[36:37],v102 offset:40960\n"
"v_cvt_pk_bf16_f32 v116,v116,v117\n"
"v_mfma_f32_16x16x32_bf16 v[204:207],v[40:43],v[16:19],v[204:207]\n"
"s_add_u32 m0,s78,65536\n"
"ds_read_b64_tr_b16 v[38:39],v102 offset:41984\n"
"v_accvgpr_write_b32 a232,v116\n"
"v_mfma_f32_16x16x32_bf16 v[172:175],v[40:43],v[8:11],v[172:175]\n"
"buffer_load_dwordx4 v112,s[24:27],s76 offen offset:0 sc1 lds\n"
"v_mfma_f32_16x16x32_bf16 v[172:175],v[44:47],v[12:15],v[172:175]\n"
"v_cvt_pk_bf16_f32 v117,v118,v119\n"
"v_cvt_pk_bf16_f32 v118,v120,v121\n"
"v_mfma_f32_16x16x32_bf16 v[140:143],v[44:47],v[4:7],v[140:143]\n"
"buffer_load_dwordx4 v112,s[24:27],s76 offen offset:1024 sc1 lds\n"
"v_mfma_f32_16x16x32_bf16 v[140:143],v[40:43],v[0:3],v[140:143]\n"
"v_cvt_pk_bf16_f32 v119,v122,v123\n"
"s_waitcnt vmcnt(18)\n"
"v_mfma_f32_16x16x32_bf16 v[144:147],v[48:51],v[0:3],v[144:147]\n"
"ds_read_b64_tr_b16 v[40:41],v103 offset:32768\n"
"v_accvgpr_write_b32 a233,v117\n"
"v_lshrrev_b32 v106,8,v112\n"
"v_mfma_f32_16x16x32_bf16 v[144:147],v[52:55],v[4:7],v[144:147]\n"
"ds_read_b64_tr_b16 v[42:43],v103 offset:33792\n"
"v_accvgpr_write_b32 a234,v118\n"
"v_lshlrev_b32 v107,6,v106\n"
"v_mfma_f32_16x16x32_bf16 v[176:179],v[52:55],v[12:15],v[176:179]\n"
"ds_read_b64_tr_b16 v[44:45],v103 offset:40960\n"
"v_accvgpr_write_b32 a235,v119\n"
"v_and_b32 v108,0xf0,v112\n"
"v_mfma_f32_16x16x32_bf16 v[176:179],v[48:51],v[8:11],v[176:179]\n"
"ds_read_b64_tr_b16 v[46:47],v103 offset:41984\n"
"v_accvgpr_read_b32 v116,a200\n"
"v_xor_b32 v107,v107,v108\n"
"v_mfma_f32_16x16x32_bf16 v[208:211],v[48:51],v[16:19],v[208:211]\n"
"buffer_load_dwordx4 v112,s[24:27],s76 offen offset:2048 sc1 lds\n"
"v_lshl_add_u32 v106,v106,11,v107\n"
"v_mfma_f32_16x16x32_bf16 v[208:211],v[52:55],v[20:23],v[208:211]\n"
"ds_read_b128 a[0:3],v96 offset:0\n"
"v_accvgpr_read_b32 v117,a201\n"
"v_lshrrev_b32 v107,6,v112\n"
"v_mfma_f32_16x16x32_bf16 v[240:243],v[52:55],v[28:31],v[240:243]\n"
"v_accvgpr_read_b32 v118,a202\n"
"v_accvgpr_read_b32 v119,a203\n"
"v_and_b32 v108,3,v107\n"
"v_mfma_f32_16x16x32_bf16 v[240:243],v[48:51],v[24:27],v[240:243]\n"
"ds_read_b128 a[8:11],v96 offset:1024\n"
"v_accvgpr_read_b32 v120,a204\n"
"v_lshlrev_b32 v108,6,v108\n"
"v_mfma_f32_16x16x32_bf16 v[244:247],v[56:59],v[24:27],v[244:247]\n"
"ds_read_b64_tr_b16 v[48:49],v104 offset:32768\n"
"v_accvgpr_read_b32 v121,a205\n"
"v_mfma_f32_16x16x32_bf16 v[244:247],v[60:63],v[28:31],v[244:247]\n"
"s_lshl_b32 s82,s52,6\n"
"ds_read_b64_tr_b16 v[50:51],v104 offset:33792\n"
"v_accvgpr_read_b32 v122,a206\n"
"v_xor_b32 v108,s82,v108\n"
"v_mfma_f32_16x16x32_bf16 v[212:215],v[60:63],v[20:23],v[212:215]\n"
"ds_read_b64_tr_b16 v[52:53],v104 offset:40960\n"
"v_accvgpr_read_b32 v123,a207\n"
"v_lshl_add_u32 v107,v107,8,v108\n"
"v_mfma_f32_16x16x32_bf16 v[212:215],v[56:59],v[16:19],v[212:215]\n"
"ds_read_b64_tr_b16 v[54:55],v104 offset:41984\n"
"v_cvt_pk_bf16_f32 v116,v116,v117\n"
"v_and_b32 v108,0x30,v112\n"
"v_mfma_f32_16x16x32_bf16 v[180:183],v[56:59],v[8:11],v[180:183]\n"
"buffer_load_dwordx4 v112,s[24:27],s76 offen offset:3072 sc1 lds\n"
"v_add_u32 v107,v107,v108\n"
"v_mfma_f32_16x16x32_bf16 v[180:183],v[60:63],v[12:15],v[180:183]\n"
"s_add_u32 m0,s78,81920\n"
"s_xor_b32 s76,s76,16384\n"
"ds_read_b128 a[16:19],v96 offset:2048\n"
"v_mfma_f32_16x16x32_bf16 v[148:151],v[60:63],v[4:7],v[148:151]\n"
"buffer_load_dwordx4 v112,s[24:27],s76 offen offset:0 sc1 lds\n"
"v_add_u32 v107,114688,v107\n"
"v_mfma_f32_16x16x32_bf16 v[148:151],v[56:59],v[0:3],v[148:151]\n"
"ds_read_b128 a[24:27],v96 offset:3072\n"
"v_cvt_pk_bf16_f32 v117,v118,v119\n"
"v_mfma_f32_16x16x32_bf16 v[152:155],v[64:67],v[0:3],v[152:155]\n"
"ds_read_b64_tr_b16 v[56:57],v105 offset:32768\n"
"v_cvt_pk_bf16_f32 v118,v120,v121\n"
"v_mfma_f32_16x16x32_bf16 v[152:155],v[68:71],v[4:7],v[152:155]\n"
"ds_read_b64_tr_b16 v[58:59],v105 offset:33792\n"
"v_cvt_pk_bf16_f32 v119,v122,v123\n"
"v_mfma_f32_16x16x32_bf16 v[184:187],v[68:71],v[12:15],v[184:187]\n"
"ds_read_b64_tr_b16 v[60:61],v105 offset:40960\n"
"v_accvgpr_write_b32 a228,v116\n"
"v_mfma_f32_16x16x32_bf16 v[184:187],v[64:67],v[8:11],v[184:187]\n"
"ds_read_b64_tr_b16 v[62:63],v105 offset:41984\n"
"v_accvgpr_write_b32 a229,v117\n"
"v_mfma_f32_16x16x32_bf16 v[216:219],v[64:67],v[16:19],v[216:219]\n"
"ds_read_b128 a[4:7],v96 offset:16384\n"
"v_accvgpr_write_b32 a230,v118\n"
"v_mfma_f32_16x16x32_bf16 v[216:219],v[68:71],v[20:23],v[216:219]\n"
"ds_read_b128 a[12:15],v96 offset:17408\n"
"v_accvgpr_write_b32 a231,v119\n"
"v_mfma_f32_16x16x32_bf16 v[248:251],v[68:71],v[28:31],v[248:251]\n"
"ds_read_b128 a[20:23],v96 offset:18432\n"
"v_accvgpr_read_b32 v116,a192\n"
"v_mfma_f32_16x16x32_bf16 v[248:251],v[64:67],v[24:27],v[248:251]\n"
"ds_read_b128 a[28:31],v96 offset:19456\n"
"v_accvgpr_read_b32 v117,a193\n"
"v_mfma_f32_16x16x32_bf16 v[252:255],v[72:75],v[24:27],v[252:255]\n"
"buffer_load_dwordx4 v112,s[24:27],s76 offen offset:1024 sc1 lds\n"
"v_mfma_f32_16x16x32_bf16 v[252:255],v[76:79],v[28:31],v[252:255]\n"
"v_accvgpr_read_b32 v118,a194\n"
"v_accvgpr_read_b32 v119,a195\n"
"v_mfma_f32_16x16x32_bf16 v[220:223],v[76:79],v[20:23],v[220:223]\n"
"v_accvgpr_read_b32 v120,a196\n"
"v_accvgpr_read_b32 v121,a197\n"
"v_mfma_f32_16x16x32_bf16 v[220:223],v[72:75],v[16:19],v[220:223]\n"
"buffer_load_dwordx4 v112,s[24:27],s76 offen offset:2048 sc1 lds\n"
"v_mfma_f32_16x16x32_bf16 v[188:191],v[72:75],v[8:11],v[188:191]\n"
"v_accvgpr_read_b32 v122,a198\n"
"v_accvgpr_read_b32 v123,a199\n"
"v_mfma_f32_16x16x32_bf16 v[188:191],v[76:79],v[12:15],v[188:191]\n"
"v_cvt_pk_bf16_f32 v116,v116,v117\n"
"v_cvt_pk_bf16_f32 v117,v118,v119\n"
"v_mfma_f32_16x16x32_bf16 v[156:159],v[76:79],v[4:7],v[156:159]\n"
"buffer_load_dwordx4 v112,s[24:27],s76 offen offset:3072 sc1 lds\n"
"v_mfma_f32_16x16x32_bf16 v[156:159],v[72:75],v[0:3],v[156:159]\n"
"s_xor_b32 s76,s76,114688\n"
"v_cvt_pk_bf16_f32 v118,v120,v121\n"
"s_waitcnt vmcnt(20) lgkmcnt(0)\n"
"s_barrier\n"
"v_mfma_f32_16x16x32_bf16 v[128:131],v[80:83],a[0:3],v[128:131]\n"
"ds_read_b64_tr_b16 v[64:65],v98 offset:0\n"
"ds_read_b64_tr_b16 v[72:73],v99 offset:0\n"
"v_mfma_f32_16x16x32_bf16 v[128:131],v[84:87],a[4:7],v[128:131]\n"
"ds_read_b64_tr_b16 v[66:67],v98 offset:1024\n"
"ds_read_b64_tr_b16 v[74:75],v99 offset:1024\n"
"v_mfma_f32_16x16x32_bf16 v[160:163],v[84:87],a[12:15],v[160:163]\n"
"ds_read_b64_tr_b16 v[68:69],v98 offset:8192\n"
"ds_read_b64_tr_b16 v[76:77],v99 offset:8192\n"
"v_mfma_f32_16x16x32_bf16 v[160:163],v[80:83],a[8:11],v[160:163]\n"
"s_add_u32 m0,s85,131072\n"
"ds_read_b64_tr_b16 v[70:71],v98 offset:9216\n"
"ds_read_b64_tr_b16 v[78:79],v99 offset:9216\n"
"v_mfma_f32_16x16x32_bf16 v[192:195],v[80:83],a[16:19],v[192:195]\n"
"buffer_load_dwordx4 v110,s[28:31],s86 offen lds\n"
"v_mfma_f32_16x16x32_bf16 v[192:195],v[84:87],a[20:23],v[192:195]\n"
"s_add_u32 m0,s85,132096\n"
"v_cvt_pk_bf16_f32 v119,v122,v123\n"
"v_mfma_f32_16x16x32_bf16 v[224:227],v[84:87],a[28:31],v[224:227]\n"
"buffer_load_dwordx4 v111,s[28:31],s86 offen lds\n"
"v_mfma_f32_16x16x32_bf16 v[224:227],v[80:83],a[24:27],v[224:227]\n"
"s_xor_b32 s86,s86,262144\n"
"v_accvgpr_write_b32 a224,v116\n"
"v_mfma_f32_16x16x32_bf16 v[228:231],v[88:91],a[24:27],v[228:231]\n"
"ds_read_b64_tr_b16 v[80:81],v100 offset:0\n"
"v_accvgpr_write_b32 a225,v117\n"
"v_mfma_f32_16x16x32_bf16 v[228:231],v[92:95],a[28:31],v[228:231]\n"
"ds_read_b64_tr_b16 v[82:83],v100 offset:1024\n"
"v_accvgpr_write_b32 a226,v118\n"
"v_mfma_f32_16x16x32_bf16 v[196:199],v[92:95],a[20:23],v[196:199]\n"
"ds_read_b64_tr_b16 v[84:85],v100 offset:8192\n"
"v_accvgpr_write_b32 a227,v119\n"
"v_mfma_f32_16x16x32_bf16 v[196:199],v[88:91],a[16:19],v[196:199]\n"
"s_add_u32 m0,s85,139264\n"
"ds_read_b64_tr_b16 v[86:87],v100 offset:9216\n"
"v_mfma_f32_16x16x32_bf16 v[164:167],v[88:91],a[8:11],v[164:167]\n"
"buffer_load_dwordx4 v110,s[28:31],s86 offen lds\n"
"v_mfma_f32_16x16x32_bf16 v[164:167],v[92:95],a[12:15],v[164:167]\n"
"s_add_u32 m0,s85,140288\n"
"s_add_u32 s56,s56,126976\n"
"v_mfma_f32_16x16x32_bf16 v[132:135],v[92:95],a[4:7],v[132:135]\n"
"buffer_load_dwordx4 v111,s[28:31],s86 offen lds\n"
"v_mfma_f32_16x16x32_bf16 v[132:135],v[88:91],a[0:3],v[132:135]\n"
"s_xor_b32 s86,s86,786432\n"
"v_mfma_f32_16x16x32_bf16 v[136:139],a[32:35],a[0:3],v[136:139]\n"
"ds_read_b64_tr_b16 v[88:89],v101 offset:0\n"
"v_mfma_f32_16x16x32_bf16 v[136:139],a[36:39],a[4:7],v[136:139]\n"
"ds_read_b64_tr_b16 v[90:91],v101 offset:1024\n"
"v_mfma_f32_16x16x32_bf16 v[168:171],a[36:39],a[12:15],v[168:171]\n"
"ds_read_b64_tr_b16 v[92:93],v101 offset:8192\n"
"v_mfma_f32_16x16x32_bf16 v[168:171],a[32:35],a[8:11],v[168:171]\n"
"s_add_u32 m0,s78,114688\n"
"ds_read_b64_tr_b16 v[94:95],v101 offset:9216\n"
"v_mfma_f32_16x16x32_bf16 v[200:203],a[32:35],a[16:19],v[200:203]\n"
"buffer_load_dwordx4 v106,s[4:7],s13 offen offset:0 nt lds\n"
"v_mfma_f32_16x16x32_bf16 v[200:203],a[36:39],a[20:23],v[200:203]\n"
"s_add_u32 s13,s13,7168\n"
"v_mfma_f32_16x16x32_bf16 v[232:235],a[36:39],a[28:31],v[232:235]\n"
"buffer_load_dwordx4 v106,s[4:7],s13 offen offset:1024 nt lds\n"
"v_mfma_f32_16x16x32_bf16 v[232:235],a[32:35],a[24:27],v[232:235]\n"
"s_add_u32 s13,s13,7168\n"
"v_mfma_f32_16x16x32_bf16 v[236:239],a[40:43],a[24:27],v[236:239]\n"
"ds_read_b64_tr_b16 a[32:33],v102 offset:0\n"
"v_mfma_f32_16x16x32_bf16 v[236:239],a[44:47],a[28:31],v[236:239]\n"
"ds_read_b64_tr_b16 a[34:35],v102 offset:1024\n"
"v_mfma_f32_16x16x32_bf16 v[204:207],a[44:47],a[20:23],v[204:207]\n"
"ds_read_b64_tr_b16 a[36:37],v102 offset:8192\n"
"v_mfma_f32_16x16x32_bf16 v[204:207],a[40:43],a[16:19],v[204:207]\n"
"ds_read_b64_tr_b16 a[38:39],v102 offset:9216\n"
"v_mfma_f32_16x16x32_bf16 v[172:175],a[40:43],a[8:11],v[172:175]\n"
"buffer_load_dwordx4 v106,s[4:7],s13 offen offset:2048 nt lds\n"
"v_mfma_f32_16x16x32_bf16 v[172:175],a[44:47],a[12:15],v[172:175]\n"
"s_add_u32 s13,s13,7168\n"
"v_mfma_f32_16x16x32_bf16 v[140:143],a[44:47],a[4:7],v[140:143]\n"
"buffer_load_dwordx4 v106,s[4:7],s13 offen offset:3072 nt lds\n"
"v_mfma_f32_16x16x32_bf16 v[140:143],a[40:43],a[0:3],v[140:143]\n"
"s_add_u32 s13,s13,109568\n"
"s_waitcnt vmcnt(20)\n"
"v_mfma_f32_16x16x32_bf16 v[144:147],v[32:35],a[0:3],v[144:147]\n"
"ds_read_b64_tr_b16 a[40:41],v103 offset:0\n"
"v_mfma_f32_16x16x32_bf16 v[144:147],v[36:39],a[4:7],v[144:147]\n"
"ds_read_b64_tr_b16 a[42:43],v103 offset:1024\n"
"v_mfma_f32_16x16x32_bf16 v[176:179],v[36:39],a[12:15],v[176:179]\n"
"ds_read_b64_tr_b16 a[44:45],v103 offset:8192\n"
"v_mfma_f32_16x16x32_bf16 v[176:179],v[32:35],a[8:11],v[176:179]\n"
"s_add_u32 m0,s78,0\n"
"ds_read_b64_tr_b16 a[46:47],v103 offset:9216\n"
"v_mfma_f32_16x16x32_bf16 v[208:211],v[32:35],a[16:19],v[208:211]\n"
"buffer_load_dwordx4 v112,s[24:27],s76 offen offset:0 sc1 lds\n"
"v_mfma_f32_16x16x32_bf16 v[208:211],v[36:39],a[20:23],v[208:211]\n"
"ds_read_b128 v[0:3],v96 offset:32768\n"
"v_mfma_f32_16x16x32_bf16 v[240:243],v[36:39],a[28:31],v[240:243]\n"
"buffer_load_dwordx4 v112,s[24:27],s76 offen offset:1024 sc1 lds\n"
"v_mfma_f32_16x16x32_bf16 v[240:243],v[32:35],a[24:27],v[240:243]\n"
"ds_read_b128 v[8:11],v96 offset:33792\n"
"v_mfma_f32_16x16x32_bf16 v[244:247],v[40:43],a[24:27],v[244:247]\n"
"ds_read_b64_tr_b16 v[32:33],v104 offset:0\n"
"v_mfma_f32_16x16x32_bf16 v[244:247],v[44:47],a[28:31],v[244:247]\n"
"ds_read_b64_tr_b16 v[34:35],v104 offset:1024\n"
"v_mfma_f32_16x16x32_bf16 v[212:215],v[44:47],a[20:23],v[212:215]\n"
"ds_read_b64_tr_b16 v[36:37],v104 offset:8192\n"
"v_mfma_f32_16x16x32_bf16 v[212:215],v[40:43],a[16:19],v[212:215]\n"
"ds_read_b64_tr_b16 v[38:39],v104 offset:9216\n"
"v_mfma_f32_16x16x32_bf16 v[180:183],v[40:43],a[8:11],v[180:183]\n"
"buffer_load_dwordx4 v112,s[24:27],s76 offen offset:2048 sc1 lds\n"
"v_mfma_f32_16x16x32_bf16 v[180:183],v[44:47],a[12:15],v[180:183]\n"
"ds_read_b128 v[16:19],v96 offset:34816\n"
"v_mfma_f32_16x16x32_bf16 v[148:151],v[44:47],a[4:7],v[148:151]\n"
"buffer_load_dwordx4 v112,s[24:27],s76 offen offset:3072 sc1 lds\n"
"v_mfma_f32_16x16x32_bf16 v[148:151],v[40:43],a[0:3],v[148:151]\n"
"s_xor_b32 s76,s76,16384\n"
"ds_read_b128 v[24:27],v96 offset:35840\n"
"v_mfma_f32_16x16x32_bf16 v[152:155],v[48:51],a[0:3],v[152:155]\n"
"ds_read_b64_tr_b16 v[40:41],v105 offset:0\n"
"v_mfma_f32_16x16x32_bf16 v[152:155],v[52:55],a[4:7],v[152:155]\n"
"ds_read_b64_tr_b16 v[42:43],v105 offset:1024\n"
"v_mfma_f32_16x16x32_bf16 v[184:187],v[52:55],a[12:15],v[184:187]\n"
"ds_read_b64_tr_b16 v[44:45],v105 offset:8192\n"
"v_mfma_f32_16x16x32_bf16 v[184:187],v[48:51],a[8:11],v[184:187]\n"
"ds_read_b64_tr_b16 v[46:47],v105 offset:9216\n"
"v_mfma_f32_16x16x32_bf16 v[216:219],v[48:51],a[16:19],v[216:219]\n"
"ds_read_b128 v[4:7],v96 offset:49152\n"
"v_mfma_f32_16x16x32_bf16 v[216:219],v[52:55],a[20:23],v[216:219]\n"
"ds_read_b128 v[12:15],v96 offset:50176\n"
"ds_read_b128 v[20:23],v96 offset:51200\n"
"v_mfma_f32_16x16x32_bf16 v[248:251],v[52:55],a[28:31],v[248:251]\n"
"buffer_store_dwordx2 v[124:125],off,s[24:27],s59 sc1\n"
"v_mfma_f32_16x16x32_bf16 v[248:251],v[48:51],a[24:27],v[248:251]\n"
"s_add_u32 m0,s78,16384\n"
"ds_read_b128 v[28:31],v96 offset:52224\n"
"v_mfma_f32_16x16x32_bf16 v[252:255],v[56:59],a[24:27],v[252:255]\n"
"buffer_load_dwordx4 v112,s[24:27],s76 offen offset:0 sc1 lds\n"
"v_mfma_f32_16x16x32_bf16 v[252:255],v[60:63],a[28:31],v[252:255]\n"
"v_mfma_f32_16x16x32_bf16 v[220:223],v[60:63],a[20:23],v[220:223]\n"
"buffer_load_dwordx4 v112,s[24:27],s76 offen offset:1024 sc1 lds\n"
"v_mfma_f32_16x16x32_bf16 v[220:223],v[56:59],a[16:19],v[220:223]\n"
"v_mfma_f32_16x16x32_bf16 v[188:191],v[56:59],a[8:11],v[188:191]\n"
"buffer_load_dwordx4 v112,s[24:27],s76 offen offset:2048 sc1 lds\n"
"v_mfma_f32_16x16x32_bf16 v[188:191],v[60:63],a[12:15],v[188:191]\n"
"v_mfma_f32_16x16x32_bf16 v[156:159],v[60:63],a[4:7],v[156:159]\n"
"buffer_load_dwordx4 v112,s[24:27],s76 offen offset:3072 sc1 lds\n"
"v_mfma_f32_16x16x32_bf16 v[156:159],v[56:59],a[0:3],v[156:159]\n"
"s_xor_b32 s76,s76,49152\n"
"s_waitcnt vmcnt(25) lgkmcnt(0)\n"
"s_barrier\n"
"v_mfma_f32_16x16x32_bf16 v[128:131],v[64:67],v[0:3],v[128:131]\n"
"ds_read_b64_tr_b16 v[48:49],v98 offset:49152\n"
"ds_read_b64_tr_b16 v[56:57],v99 offset:49152\n"
"v_mfma_f32_16x16x32_bf16 v[128:131],v[68:71],v[4:7],v[128:131]\n"
"ds_read_b64_tr_b16 v[50:51],v98 offset:50176\n"
"ds_read_b64_tr_b16 v[58:59],v99 offset:50176\n"
"v_mfma_f32_16x16x32_bf16 v[160:163],v[68:71],v[12:15],v[160:163]\n"
"ds_read_b64_tr_b16 v[52:53],v98 offset:57344\n"
"ds_read_b64_tr_b16 v[60:61],v99 offset:57344\n"
"v_mfma_f32_16x16x32_bf16 v[160:163],v[64:67],v[8:11],v[160:163]\n"
"s_add_u32 m0,s85,98304\n"
"ds_read_b64_tr_b16 v[54:55],v98 offset:58368\n"
"ds_read_b64_tr_b16 v[62:63],v99 offset:58368\n"
"v_mfma_f32_16x16x32_bf16 v[192:195],v[64:67],v[16:19],v[192:195]\n"
"buffer_load_dwordx4 v110,s[28:31],s86 offen lds\n"
"v_mfma_f32_16x16x32_bf16 v[192:195],v[68:71],v[20:23],v[192:195]\n"
"v_mfma_f32_16x16x32_bf16 v[224:227],v[68:71],v[28:31],v[224:227]\n"
"v_mfma_f32_16x16x32_bf16 v[224:227],v[64:67],v[24:27],v[224:227]\n"
"v_mfma_f32_16x16x32_bf16 v[228:231],v[72:75],v[24:27],v[228:231]\n"
"ds_read_b64_tr_b16 v[64:65],v100 offset:49152\n"
"v_mfma_f32_16x16x32_bf16 v[228:231],v[76:79],v[28:31],v[228:231]\n"
"ds_read_b64_tr_b16 v[66:67],v100 offset:50176\n"
"v_mfma_f32_16x16x32_bf16 v[196:199],v[76:79],v[20:23],v[196:199]\n"
"ds_read_b64_tr_b16 v[68:69],v100 offset:57344\n"
"v_mfma_f32_16x16x32_bf16 v[196:199],v[72:75],v[16:19],v[196:199]\n"
"s_add_u32 m0,s85,99328\n"
"ds_read_b64_tr_b16 v[70:71],v100 offset:58368\n"
"v_mfma_f32_16x16x32_bf16 v[164:167],v[72:75],v[8:11],v[164:167]\n"
"buffer_load_dwordx4 v111,s[28:31],s86 offen lds\n"
"v_mfma_f32_16x16x32_bf16 v[164:167],v[76:79],v[12:15],v[164:167]\n"
"s_add_u32 m0,s85,106496\n"
"s_xor_b32 s86,s86,262144\n"
"v_mfma_f32_16x16x32_bf16 v[132:135],v[76:79],v[4:7],v[132:135]\n"
"buffer_load_dwordx4 v110,s[28:31],s86 offen lds\n"
"v_mfma_f32_16x16x32_bf16 v[132:135],v[72:75],v[0:3],v[132:135]\n"
"v_mfma_f32_16x16x32_bf16 v[136:139],v[80:83],v[0:3],v[136:139]\n"
"ds_read_b64_tr_b16 v[72:73],v101 offset:49152\n"
"v_mfma_f32_16x16x32_bf16 v[136:139],v[84:87],v[4:7],v[136:139]\n"
"ds_read_b64_tr_b16 v[74:75],v101 offset:50176\n"
"v_mfma_f32_16x16x32_bf16 v[168:171],v[84:87],v[12:15],v[168:171]\n"
"ds_read_b64_tr_b16 v[76:77],v101 offset:57344\n"
"v_mfma_f32_16x16x32_bf16 v[168:171],v[80:83],v[8:11],v[168:171]\n"
"s_add_u32 m0,s85,107520\n"
"ds_read_b64_tr_b16 v[78:79],v101 offset:58368\n"
"v_mfma_f32_16x16x32_bf16 v[200:203],v[80:83],v[16:19],v[200:203]\n"
"buffer_load_dwordx4 v111,s[28:31],s86 offen lds\n"
"v_mfma_f32_16x16x32_bf16 v[200:203],v[84:87],v[20:23],v[200:203]\n"
"s_xor_b32 s86,s86,3932160\n"
"v_mfma_f32_16x16x32_bf16 v[232:235],v[84:87],v[28:31],v[232:235]\n"
"v_mfma_f32_16x16x32_bf16 v[232:235],v[80:83],v[24:27],v[232:235]\n"
"v_mfma_f32_16x16x32_bf16 v[236:239],v[88:91],v[24:27],v[236:239]\n"
"ds_read_b64_tr_b16 v[80:81],v102 offset:49152\n"
"v_mfma_f32_16x16x32_bf16 v[236:239],v[92:95],v[28:31],v[236:239]\n"
"ds_read_b64_tr_b16 v[82:83],v102 offset:50176\n"
"v_mfma_f32_16x16x32_bf16 v[204:207],v[92:95],v[20:23],v[204:207]\n"
"ds_read_b64_tr_b16 v[84:85],v102 offset:57344\n"
"v_mfma_f32_16x16x32_bf16 v[204:207],v[88:91],v[16:19],v[204:207]\n"
"s_add_u32 m0,s78,32768\n"
"ds_read_b64_tr_b16 v[86:87],v102 offset:58368\n"
"v_mfma_f32_16x16x32_bf16 v[172:175],v[88:91],v[8:11],v[172:175]\n"
"buffer_load_dwordx4 v112,s[24:27],s76 offen offset:0 sc1 lds\n"
"v_mfma_f32_16x16x32_bf16 v[172:175],v[92:95],v[12:15],v[172:175]\n"
"v_mfma_f32_16x16x32_bf16 v[140:143],v[92:95],v[4:7],v[140:143]\n"
"buffer_load_dwordx4 v112,s[24:27],s76 offen offset:1024 sc1 lds\n"
"v_mfma_f32_16x16x32_bf16 v[140:143],v[88:91],v[0:3],v[140:143]\n"
"s_waitcnt vmcnt(23)\n"
"ds_read_b64_tr_b16 v[88:89],v103 offset:49152\n"
"ds_read_b64_tr_b16 v[90:91],v103 offset:50176\n"
"v_mfma_f32_16x16x32_bf16 v[144:147],a[32:35],v[0:3],v[144:147]\n"
"v_lshrrev_b32 v120,4,v112\n"
"v_and_b32 v121,15,v120\n"
"v_mfma_f32_16x16x32_bf16 v[144:147],a[36:39],v[4:7],v[144:147]\n"
"v_lshrrev_b32 v122,4,v120\n"
"v_lshlrev_b32 v122,2,v122\n"
"v_mfma_f32_16x16x32_bf16 v[176:179],a[36:39],v[12:15],v[176:179]\n"
"ds_read_b64_tr_b16 v[92:93],v103 offset:57344\n"
"v_sub_u32 v121,v121,v122\n"
"v_mfma_f32_16x16x32_bf16 v[176:179],a[32:35],v[8:11],v[176:179]\n"
"s_and_b32 s82,s52,1\n"
"ds_read_b64_tr_b16 v[94:95],v103 offset:58368\n"
"s_lshl_b32 s82,s82,4\n"
"v_add_u32 v121,s82,v121\n"
"v_mfma_f32_16x16x32_bf16 v[208:211],a[32:35],v[16:19],v[208:211]\n"
"buffer_load_dwordx4 v112,s[24:27],s76 offen offset:2048 sc1 lds\n"
"v_cvt_f32_i32 v121,v121\n"
"v_mfma_f32_16x16x32_bf16 v[208:211],a[36:39],v[20:23],v[208:211]\n"
"ds_read_b128 a[96:99],v97 offset:0\n"
"v_add_f32 v121,0.5,v121\n"
"v_mul_f32 v126,0x7149f2ca,v121\n"
"v_mfma_f32_16x16x32_bf16 v[240:243],a[36:39],v[28:31],v[240:243]\n"
"v_and_b32 v120,31,v120\n"
"v_mov_b32 v121,0x3f803f80\n"
"v_mfma_f32_16x16x32_bf16 v[240:243],a[32:35],v[24:27],v[240:243]\n"
"s_mov_b32 s93,0x3f800000\n"
"ds_read_b128 a[104:107],v97 offset:1024\n"
"v_accvgpr_write_b32 a144,v121\n"
"v_mfma_f32_16x16x32_bf16 v[244:247],a[40:43],v[24:27],v[244:247]\n"
"ds_read_b64_tr_b16 a[128:129],v104 offset:49152\n"
"v_accvgpr_write_b32 a146,v121\n"
"v_mfma_f32_16x16x32_bf16 v[244:247],a[44:47],v[28:31],v[244:247]\n"
"ds_read_b64_tr_b16 a[130:131],v104 offset:50176\n"
"v_accvgpr_write_b32 a147,v121\n"
"v_mfma_f32_16x16x32_bf16 v[212:215],a[44:47],v[20:23],v[212:215]\n"
"ds_read_b64_tr_b16 a[132:133],v104 offset:57344\n"
"v_lshlrev_b32 v120,3,v120\n"
"v_mfma_f32_16x16x32_bf16 v[212:215],a[40:43],v[16:19],v[212:215]\n"
"ds_read_b64_tr_b16 a[134:135],v104 offset:58368\n"
"v_accvgpr_write_b32 a145,v121\n"
"v_mfma_f32_16x16x32_bf16 v[180:183],a[40:43],v[8:11],v[180:183]\n"
"buffer_load_dwordx4 v112,s[24:27],s76 offen offset:3072 sc1 lds\n"
"v_mfma_f32_16x16x32_bf16 v[180:183],a[44:47],v[12:15],v[180:183]\n"
"s_add_u32 m0,s78,49152\n"
"s_xor_b32 s76,s76,16384\n"
"ds_read_b128 a[112:115],v97 offset:2048\n"
"v_mfma_f32_16x16x32_bf16 v[148:151],a[44:47],v[4:7],v[148:151]\n"
"buffer_load_dwordx4 v112,s[24:27],s76 offen offset:0 sc1 lds\n"
"v_mfma_f32_16x16x32_bf16 v[148:151],a[40:43],v[0:3],v[148:151]\n"
"ds_read_b128 a[120:123],v97 offset:3072\n"
"v_mfma_f32_16x16x32_bf16 v[152:155],v[32:35],v[0:3],v[152:155]\n"
"ds_read_b64_tr_b16 a[136:137],v105 offset:49152\n"
"v_mfma_f32_16x16x32_bf16 v[152:155],v[36:39],v[4:7],v[152:155]\n"
"ds_read_b64_tr_b16 a[138:139],v105 offset:50176\n"
"v_mfma_f32_16x16x32_bf16 v[184:187],v[36:39],v[12:15],v[184:187]\n"
"ds_read_b64_tr_b16 a[140:141],v105 offset:57344\n"
"v_mfma_f32_16x16x32_bf16 v[184:187],v[32:35],v[8:11],v[184:187]\n"
"ds_read_b64_tr_b16 a[142:143],v105 offset:58368\n"
"v_mfma_f32_16x16x32_bf16 v[216:219],v[32:35],v[16:19],v[216:219]\n"
"ds_read_b128 a[100:103],v97 offset:16384\n"
"v_mfma_f32_16x16x32_bf16 v[216:219],v[36:39],v[20:23],v[216:219]\n"
"ds_read_b128 a[108:111],v97 offset:17408\n"
"v_mfma_f32_16x16x32_bf16 v[248:251],v[36:39],v[28:31],v[248:251]\n"
"ds_read_b128 a[116:119],v97 offset:18432\n"
"v_mfma_f32_16x16x32_bf16 v[248:251],v[32:35],v[24:27],v[248:251]\n"
"ds_read_b128 a[124:127],v97 offset:19456\n"
"v_mfma_f32_16x16x32_bf16 v[252:255],v[40:43],v[24:27],v[252:255]\n"
"buffer_load_dwordx4 v112,s[24:27],s76 offen offset:1024 sc1 lds\n"
"v_mfma_f32_16x16x32_bf16 v[252:255],v[44:47],v[28:31],v[252:255]\n"
"v_mfma_f32_16x16x32_bf16 v[220:223],v[44:47],v[20:23],v[220:223]\n"
"v_mfma_f32_16x16x32_bf16 v[220:223],v[40:43],v[16:19],v[220:223]\n"
"buffer_load_dwordx4 v112,s[24:27],s76 offen offset:2048 sc1 lds\n"
"v_mfma_f32_16x16x32_bf16 v[188:191],v[40:43],v[8:11],v[188:191]\n"
"v_mfma_f32_16x16x32_bf16 v[188:191],v[44:47],v[12:15],v[188:191]\n"
"v_mfma_f32_16x16x32_bf16 v[156:159],v[44:47],v[4:7],v[156:159]\n"
"buffer_load_dwordx4 v112,s[24:27],s76 offen offset:3072 sc1 lds\n"
"v_mfma_f32_16x16x32_bf16 v[156:159],v[40:43],v[0:3],v[156:159]\n"
"s_xor_b32 s76,s76,245760\n"
"s_waitcnt vmcnt(21) lgkmcnt(0)\n"
"s_barrier\n"
"v_mfma_f32_16x16x32_bf16 v[128:131],v[48:51],a[96:99],v[128:131]\n"
"ds_read_b64_tr_b16 v[32:33],v98 offset:32768\n"
"ds_read_b64_tr_b16 v[40:41],v99 offset:32768\n"
"v_mfma_f32_16x16x32_bf16 v[128:131],v[52:55],a[100:103],v[128:131]\n"
"ds_read_b64_tr_b16 v[34:35],v98 offset:33792\n"
"ds_read_b64_tr_b16 v[42:43],v99 offset:33792\n"
"v_mfma_f32_16x16x32_bf16 v[160:163],v[52:55],a[108:111],v[160:163]\n"
"ds_read_b64_tr_b16 v[36:37],v98 offset:40960\n"
"ds_read_b64_tr_b16 v[44:45],v99 offset:40960\n"
"v_mfma_f32_16x16x32_bf16 v[160:163],v[48:51],a[104:107],v[160:163]\n"
"s_add_u32 m0,s85,147456\n"
"ds_read_b64_tr_b16 v[38:39],v98 offset:41984\n"
"ds_read_b64_tr_b16 v[46:47],v99 offset:41984\n"
"v_mfma_f32_16x16x32_bf16 v[192:195],v[48:51],a[112:115],v[192:195]\n"
"buffer_load_dwordx4 v110,s[28:31],s86 offen lds\n"
"ds_read_b64_tr_b16 a[160:161],v107 offset:0\n"
"v_mfma_f32_16x16x32_bf16 v[192:195],v[52:55],a[116:119],v[192:195]\n"
"ds_read_b64_tr_b16 a[162:163],v107 offset:4096\n"
"ds_read_b64_tr_b16 a[164:165],v107 offset:8\n"
"ds_read_b64_tr_b16 a[166:167],v107 offset:4104\n"
"v_mfma_f32_16x16x32_bf16 v[224:227],v[52:55],a[124:127],v[224:227]\n"
"ds_read_b64_tr_b16 a[168:169],v107 offset:8192\n"
"ds_read_b64_tr_b16 a[170:171],v107 offset:12288\n"
"v_mfma_f32_16x16x32_bf16 v[224:227],v[48:51],a[120:123],v[224:227]\n"
"ds_read_b64_tr_b16 a[172:173],v107 offset:8200\n"
"ds_read_b64_tr_b16 a[174:175],v107 offset:12296\n"
"v_mfma_f32_16x16x32_bf16 v[228:231],v[56:59],a[120:123],v[228:231]\n"
"ds_read_b64_tr_b16 v[48:49],v100 offset:32768\n"
"v_mfma_f32_16x16x32_bf16 v[228:231],v[60:63],a[124:127],v[228:231]\n"
"ds_read_b64_tr_b16 v[50:51],v100 offset:33792\n"
"v_mfma_f32_16x16x32_bf16 v[196:199],v[60:63],a[116:119],v[196:199]\n"
"ds_read_b64_tr_b16 v[52:53],v100 offset:40960\n"
"v_mfma_f32_16x16x32_bf16 v[196:199],v[56:59],a[112:115],v[196:199]\n"
"s_add_u32 m0,s85,148480\n"
"ds_read_b64_tr_b16 v[54:55],v100 offset:41984\n"
"v_mfma_f32_16x16x32_bf16 v[164:167],v[56:59],a[104:107],v[164:167]\n"
"buffer_load_dwordx4 v111,s[28:31],s86 offen lds\n"
"v_mfma_f32_16x16x32_bf16 v[164:167],v[60:63],a[108:111],v[164:167]\n"
"s_add_u32 m0,s85,155648\n"
"s_xor_b32 s86,s86,262144\n"
"v_mfma_f32_16x16x32_bf16 v[132:135],v[60:63],a[100:103],v[132:135]\n"
"buffer_load_dwordx4 v110,s[28:31],s86 offen lds\n"
"v_mfma_f32_16x16x32_bf16 v[132:135],v[56:59],a[96:99],v[132:135]\n"
"v_mfma_f32_16x16x32_bf16 v[136:139],v[64:67],a[96:99],v[136:139]\n"
"ds_read_b64_tr_b16 v[56:57],v101 offset:32768\n"
"v_mfma_f32_16x16x32_bf16 v[136:139],v[68:71],a[100:103],v[136:139]\n"
"ds_read_b64_tr_b16 v[58:59],v101 offset:33792\n"
"v_mfma_f32_16x16x32_bf16 v[168:171],v[68:71],a[108:111],v[168:171]\n"
"ds_read_b64_tr_b16 v[60:61],v101 offset:40960\n"
"v_mfma_f32_16x16x32_bf16 v[168:171],v[64:67],a[104:107],v[168:171]\n"
"s_add_u32 m0,s85,156672\n"
"ds_read_b64_tr_b16 v[62:63],v101 offset:41984\n"
"v_mfma_f32_16x16x32_bf16 v[200:203],v[64:67],a[112:115],v[200:203]\n"
"buffer_load_dwordx4 v111,s[28:31],s86 offen lds\n"
"v_mfma_f32_16x16x32_bf16 v[200:203],v[68:71],a[116:119],v[200:203]\n"
"s_xor_b32 s86,s86,786432\n"
"v_mfma_f32_16x16x32_bf16 v[232:235],v[68:71],a[124:127],v[232:235]\n"
"v_mfma_f32_16x16x32_bf16 v[232:235],v[64:67],a[120:123],v[232:235]\n"
"v_mfma_f32_16x16x32_bf16 v[236:239],v[72:75],a[120:123],v[236:239]\n"
"ds_read_b64_tr_b16 v[64:65],v102 offset:32768\n"
"v_mfma_f32_16x16x32_bf16 v[236:239],v[76:79],a[124:127],v[236:239]\n"
"ds_read_b64_tr_b16 v[66:67],v102 offset:33792\n"
"v_mfma_f32_16x16x32_bf16 v[204:207],v[76:79],a[116:119],v[204:207]\n"
"ds_read_b64_tr_b16 v[68:69],v102 offset:40960\n"
"v_mfma_f32_16x16x32_bf16 v[204:207],v[72:75],a[112:115],v[204:207]\n"
"s_add_u32 m0,s78,65536\n"
"ds_read_b64_tr_b16 v[70:71],v102 offset:41984\n"
"v_mfma_f32_16x16x32_bf16 v[172:175],v[72:75],a[104:107],v[172:175]\n"
"buffer_load_dwordx4 v112,s[24:27],s76 offen offset:0 sc1 lds\n"
"v_mfma_f32_16x16x32_bf16 v[172:175],v[76:79],a[108:111],v[172:175]\n"
"v_mfma_f32_16x16x32_bf16 v[140:143],v[76:79],a[100:103],v[140:143]\n"
"buffer_load_dwordx4 v112,s[24:27],s76 offen offset:1024 sc1 lds\n"
"v_mfma_f32_16x16x32_bf16 v[140:143],v[72:75],a[96:99],v[140:143]\n"
"s_waitcnt vmcnt(18)\n"
"v_mfma_f32_16x16x32_bf16 v[144:147],v[80:83],a[96:99],v[144:147]\n"
"ds_read_b64_tr_b16 v[72:73],v103 offset:32768\n"
"v_accvgpr_write_b32 a156,0\n"
"v_mfma_f32_16x16x32_bf16 v[144:147],v[84:87],a[100:103],v[144:147]\n"
"ds_read_b64_tr_b16 v[74:75],v103 offset:33792\n"
"v_accvgpr_write_b32 a157,0\n"
"v_mfma_f32_16x16x32_bf16 v[176:179],v[84:87],a[108:111],v[176:179]\n"
"ds_read_b64_tr_b16 v[76:77],v103 offset:40960\n"
"v_accvgpr_write_b32 a158,0\n"
"v_mfma_f32_16x16x32_bf16 v[176:179],v[80:83],a[104:107],v[176:179]\n"
"ds_read_b64_tr_b16 v[78:79],v103 offset:41984\n"
"v_accvgpr_write_b32 a159,0\n"
"v_mfma_f32_16x16x32_bf16 v[208:211],v[80:83],a[112:115],v[208:211]\n"
"buffer_load_dwordx4 v112,s[24:27],s76 offen offset:2048 sc1 lds\n"
"v_mfma_f32_16x16x32_bf16 v[208:211],v[84:87],a[116:119],v[208:211]\n"
"ds_read_b128 v[0:3],v96 offset:0\n"
"v_mfma_f32_16x16x32_bf16 v[240:243],v[84:87],a[124:127],v[240:243]\n"
"v_mfma_f32_16x16x32_bf16 v[240:243],v[80:83],a[120:123],v[240:243]\n"
"ds_read_b128 v[8:11],v96 offset:1024\n"
"v_mfma_f32_16x16x32_bf16 v[244:247],v[88:91],a[120:123],v[244:247]\n"
"ds_read_b64_tr_b16 v[80:81],v104 offset:32768\n"
"v_mfma_f32_16x16x32_bf16 v[244:247],v[92:95],a[124:127],v[244:247]\n"
"ds_read_b64_tr_b16 v[82:83],v104 offset:33792\n"
"v_mfma_f32_16x16x32_bf16 v[212:215],v[92:95],a[116:119],v[212:215]\n"
"ds_read_b64_tr_b16 v[84:85],v104 offset:40960\n"
"v_mfma_f32_16x16x32_bf16 v[212:215],v[88:91],a[112:115],v[212:215]\n"
"ds_read_b64_tr_b16 v[86:87],v104 offset:41984\n"
"v_mfma_f32_16x16x32_bf16 v[180:183],v[88:91],a[104:107],v[180:183]\n"
"buffer_load_dwordx4 v112,s[24:27],s76 offen offset:3072 sc1 lds\n"
"v_mfma_f32_16x16x32_bf16 v[180:183],v[92:95],a[108:111],v[180:183]\n"
"s_add_u32 m0,s78,81920\n"
"s_xor_b32 s76,s76,16384\n"
"ds_read_b128 v[16:19],v96 offset:2048\n"
"v_mfma_f32_16x16x32_bf16 v[148:151],v[92:95],a[100:103],v[148:151]\n"
"buffer_load_dwordx4 v112,s[24:27],s76 offen offset:0 sc1 lds\n"
"v_mfma_f32_16x16x32_bf16 v[148:151],v[88:91],a[96:99],v[148:151]\n"
"ds_read_b128 v[24:27],v96 offset:3072\n"
"v_mfma_f32_16x16x32_bf16 v[152:155],a[128:131],a[96:99],v[152:155]\n"
"ds_read_b64_tr_b16 v[88:89],v105 offset:32768\n"
"v_mfma_f32_16x16x32_bf16 v[152:155],a[132:135],a[100:103],v[152:155]\n"
"ds_read_b64_tr_b16 v[90:91],v105 offset:33792\n"
"v_mfma_f32_16x16x32_bf16 v[184:187],a[132:135],a[108:111],v[184:187]\n"
"ds_read_b64_tr_b16 v[92:93],v105 offset:40960\n"
"v_mfma_f32_16x16x32_bf16 v[184:187],a[128:131],a[104:107],v[184:187]\n"
"ds_read_b64_tr_b16 v[94:95],v105 offset:41984\n"
"v_mfma_f32_16x16x32_bf16 v[216:219],a[128:131],a[112:115],v[216:219]\n"
"ds_read_b128 v[4:7],v96 offset:16384\n"
"v_mfma_f32_16x16x32_bf16 v[216:219],a[132:135],a[116:119],v[216:219]\n"
"ds_read_b128 v[12:15],v96 offset:17408\n"
"v_mfma_f32_16x16x32_bf16 v[248:251],a[132:135],a[124:127],v[248:251]\n"
"ds_read_b128 v[20:23],v96 offset:18432\n"
"v_mfma_f32_16x16x32_bf16 v[248:251],a[128:131],a[120:123],v[248:251]\n"
"ds_read_b128 v[28:31],v96 offset:19456\n"
"v_mfma_f32_16x16x32_bf16 v[252:255],a[136:139],a[120:123],v[252:255]\n"
"buffer_load_dwordx4 v112,s[24:27],s76 offen offset:1024 sc1 lds\n"
"v_mfma_f32_16x16x32_bf16 v[252:255],a[140:143],a[124:127],v[252:255]\n"
"v_mfma_f32_16x16x32_bf16 v[220:223],a[140:143],a[116:119],v[220:223]\n"
"v_mfma_f32_16x16x32_bf16 v[220:223],a[136:139],a[112:115],v[220:223]\n"
"buffer_load_dwordx4 v112,s[24:27],s76 offen offset:2048 sc1 lds\n"
"v_mfma_f32_16x16x32_bf16 v[188:191],a[136:139],a[104:107],v[188:191]\n"
"v_mfma_f32_16x16x32_bf16 v[188:191],a[140:143],a[108:111],v[188:191]\n"
"v_mfma_f32_16x16x32_bf16 v[156:159],a[140:143],a[100:103],v[156:159]\n"
"buffer_load_dwordx4 v112,s[24:27],s76 offen offset:3072 sc1 lds\n"
"v_mfma_f32_16x16x32_bf16 v[156:159],a[136:139],a[96:99],v[156:159]\n"
"s_xor_b32 s76,s76,49152\n"
"s_waitcnt vmcnt(20) lgkmcnt(0)\n"
"s_barrier\n"
"v_mfma_f32_16x16x32_bf16 v[128:131],v[32:35],v[0:3],v[128:131]\n"
"ds_read_b64_tr_b16 a[128:129],v98 offset:0\n"
"ds_read_b64_tr_b16 a[136:137],v99 offset:0\n"
"v_mfma_f32_16x16x32_bf16 v[128:131],v[36:39],v[4:7],v[128:131]\n"
"ds_read_b64_tr_b16 a[130:131],v98 offset:1024\n"
"ds_read_b64_tr_b16 a[138:139],v99 offset:1024\n"
"v_mfma_f32_16x16x32_bf16 v[160:163],v[36:39],v[12:15],v[160:163]\n"
"ds_read_b64_tr_b16 a[132:133],v98 offset:8192\n"
"ds_read_b64_tr_b16 a[140:141],v99 offset:8192\n"
"v_mfma_f32_16x16x32_bf16 v[160:163],v[32:35],v[8:11],v[160:163]\n"
"s_add_u32 m0,s85,131072\n"
"ds_read_b64_tr_b16 a[134:135],v98 offset:9216\n"
"ds_read_b64_tr_b16 a[142:143],v99 offset:9216\n"
"v_mfma_f32_16x16x32_bf16 v[192:195],v[32:35],v[16:19],v[192:195]\n"
"buffer_load_dwordx4 v110,s[28:31],s86 offen lds\n"
"v_mfma_f32_16x16x32_bf16 v[192:195],v[36:39],v[20:23],v[192:195]\n"
"s_add_u32 m0,s85,132096\n"
"v_mfma_f32_16x16x32_bf16 v[224:227],v[36:39],v[28:31],v[224:227]\n"
"buffer_load_dwordx4 v111,s[28:31],s86 offen lds\n"
"v_mfma_f32_16x16x32_bf16 v[224:227],v[32:35],v[24:27],v[224:227]\n"
"s_xor_b32 s86,s86,262144\n"
"v_mfma_f32_16x16x32_bf16 v[228:231],v[40:43],v[24:27],v[228:231]\n"
"ds_read_b64_tr_b16 v[32:33],v100 offset:0\n"
"v_mfma_f32_16x16x32_bf16 v[228:231],v[44:47],v[28:31],v[228:231]\n"
"ds_read_b64_tr_b16 v[34:35],v100 offset:1024\n"
"v_mfma_f32_16x16x32_bf16 v[196:199],v[44:47],v[20:23],v[196:199]\n"
"ds_read_b64_tr_b16 v[36:37],v100 offset:8192\n"
"v_mfma_f32_16x16x32_bf16 v[196:199],v[40:43],v[16:19],v[196:199]\n"
"s_add_u32 m0,s85,139264\n"
"ds_read_b64_tr_b16 v[38:39],v100 offset:9216\n"
"v_mfma_f32_16x16x32_bf16 v[164:167],v[40:43],v[8:11],v[164:167]\n"
"buffer_load_dwordx4 v110,s[28:31],s86 offen lds\n"
"v_mfma_f32_16x16x32_bf16 v[164:167],v[44:47],v[12:15],v[164:167]\n"
"s_add_u32 m0,s85,140288\n"
"v_mfma_f32_16x16x32_bf16 v[132:135],v[44:47],v[4:7],v[132:135]\n"
"buffer_load_dwordx4 v111,s[28:31],s86 offen lds\n"
"v_mfma_f32_16x16x32_bf16 v[132:135],v[40:43],v[0:3],v[132:135]\n"
"s_xor_b32 s86,s86,1835008\n"
"v_mfma_f32_16x16x32_bf16 v[136:139],v[48:51],v[0:3],v[136:139]\n"
"ds_read_b64_tr_b16 v[40:41],v101 offset:0\n"
"v_mfma_f32_16x16x32_bf16 v[136:139],v[52:55],v[4:7],v[136:139]\n"
"ds_read_b64_tr_b16 v[42:43],v101 offset:1024\n"
"v_mfma_f32_16x16x32_bf16 v[168:171],v[52:55],v[12:15],v[168:171]\n"
"ds_read_b64_tr_b16 v[44:45],v101 offset:8192\n"
"v_mfma_f32_16x16x32_bf16 v[168:171],v[48:51],v[8:11],v[168:171]\n"
"s_add_u32 m0,s78,114688\n"
"ds_read_b64_tr_b16 v[46:47],v101 offset:9216\n"
"v_mfma_f32_16x16x32_bf16 v[200:203],v[48:51],v[16:19],v[200:203]\n"
"buffer_load_dwordx4 v106,s[4:7],s13 offen offset:0 nt lds\n"
"v_mfma_f32_16x16x32_bf16 v[200:203],v[52:55],v[20:23],v[200:203]\n"
"s_add_u32 s13,s13,7168\n"
"v_mfma_f32_16x16x32_bf16 v[232:235],v[52:55],v[28:31],v[232:235]\n"
"buffer_load_dwordx4 v106,s[4:7],s13 offen offset:1024 nt lds\n"
"v_mfma_f32_16x16x32_bf16 v[232:235],v[48:51],v[24:27],v[232:235]\n"
"s_add_u32 s13,s13,7168\n"
"v_mfma_f32_16x16x32_bf16 v[236:239],v[56:59],v[24:27],v[236:239]\n"
"ds_read_b64_tr_b16 v[48:49],v102 offset:0\n"
"v_mfma_f32_16x16x32_bf16 v[236:239],v[60:63],v[28:31],v[236:239]\n"
"ds_read_b64_tr_b16 v[50:51],v102 offset:1024\n"
"v_mfma_f32_16x16x32_bf16 v[204:207],v[60:63],v[20:23],v[204:207]\n"
"ds_read_b64_tr_b16 v[52:53],v102 offset:8192\n"
"v_mfma_f32_16x16x32_bf16 v[204:207],v[56:59],v[16:19],v[204:207]\n"
"ds_read_b64_tr_b16 v[54:55],v102 offset:9216\n"
"v_mfma_f32_16x16x32_bf16 v[172:175],v[56:59],v[8:11],v[172:175]\n"
"buffer_load_dwordx4 v106,s[4:7],s13 offen offset:2048 nt lds\n"
"v_mfma_f32_16x16x32_bf16 v[172:175],v[60:63],v[12:15],v[172:175]\n"
"s_add_u32 s13,s13,7168\n"
"v_mfma_f32_16x16x32_bf16 v[140:143],v[60:63],v[4:7],v[140:143]\n"
"buffer_load_dwordx4 v106,s[4:7],s13 offen offset:3072 nt lds\n"
"v_mfma_f32_16x16x32_bf16 v[140:143],v[56:59],v[0:3],v[140:143]\n"
"s_add_u32 s13,s13,109568\n"
"s_waitcnt vmcnt(20)\n"
"v_mfma_f32_16x16x32_bf16 v[144:147],v[64:67],v[0:3],v[144:147]\n"
"ds_read_b64_tr_b16 v[56:57],v103 offset:0\n"
"v_mfma_f32_16x16x32_bf16 v[144:147],v[68:71],v[4:7],v[144:147]\n"
"ds_read_b64_tr_b16 v[58:59],v103 offset:1024\n"
"v_mfma_f32_16x16x32_bf16 v[176:179],v[68:71],v[12:15],v[176:179]\n"
"ds_read_b64_tr_b16 v[60:61],v103 offset:8192\n"
"v_mfma_f32_16x16x32_bf16 v[176:179],v[64:67],v[8:11],v[176:179]\n"
"s_add_u32 m0,s78,0\n"
"ds_read_b64_tr_b16 v[62:63],v103 offset:9216\n"
"v_mfma_f32_16x16x32_bf16 v[208:211],v[64:67],v[16:19],v[208:211]\n"
"buffer_load_dwordx4 v112,s[24:27],s76 offen offset:0 sc1 lds\n"
"v_mfma_f32_16x16x32_bf16 v[208:211],v[68:71],v[20:23],v[208:211]\n"
"ds_read_b128 a[96:99],v96 offset:32768\n"
"v_mfma_f32_16x16x32_bf16 v[240:243],v[68:71],v[28:31],v[240:243]\n"
"buffer_load_dwordx4 v112,s[24:27],s76 offen offset:1024 sc1 lds\n"
"v_mfma_f32_16x16x32_bf16 v[240:243],v[64:67],v[24:27],v[240:243]\n"
"ds_read_b128 a[104:107],v96 offset:33792\n"
"v_mfma_f32_16x16x32_bf16 v[244:247],v[72:75],v[24:27],v[244:247]\n"
"ds_read_b64_tr_b16 v[64:65],v104 offset:0\n"
"v_mfma_f32_16x16x32_bf16 v[244:247],v[76:79],v[28:31],v[244:247]\n"
"ds_read_b64_tr_b16 v[66:67],v104 offset:1024\n"
"v_mfma_f32_16x16x32_bf16 v[212:215],v[76:79],v[20:23],v[212:215]\n"
"ds_read_b64_tr_b16 v[68:69],v104 offset:8192\n"
"v_mfma_f32_16x16x32_bf16 v[212:215],v[72:75],v[16:19],v[212:215]\n"
"ds_read_b64_tr_b16 v[70:71],v104 offset:9216\n"
"v_mfma_f32_16x16x32_bf16 v[180:183],v[72:75],v[8:11],v[180:183]\n"
"buffer_load_dwordx4 v112,s[24:27],s76 offen offset:2048 sc1 lds\n"
"v_mfma_f32_16x16x32_bf16 v[180:183],v[76:79],v[12:15],v[180:183]\n"
"ds_read_b128 a[112:115],v96 offset:34816\n"
"v_mfma_f32_16x16x32_bf16 v[148:151],v[76:79],v[4:7],v[148:151]\n"
"buffer_load_dwordx4 v112,s[24:27],s76 offen offset:3072 sc1 lds\n"
"v_mfma_f32_16x16x32_bf16 v[148:151],v[72:75],v[0:3],v[148:151]\n"
"s_xor_b32 s76,s76,16384\n"
"ds_read_b128 a[120:123],v96 offset:35840\n"
"v_mfma_f32_16x16x32_bf16 v[152:155],v[80:83],v[0:3],v[152:155]\n"
"ds_read_b64_tr_b16 v[72:73],v105 offset:0\n"
"v_mfma_f32_16x16x32_bf16 v[152:155],v[84:87],v[4:7],v[152:155]\n"
"ds_read_b64_tr_b16 v[74:75],v105 offset:1024\n"
"v_mfma_f32_16x16x32_bf16 v[184:187],v[84:87],v[12:15],v[184:187]\n"
"ds_read_b64_tr_b16 v[76:77],v105 offset:8192\n"
"v_mfma_f32_16x16x32_bf16 v[184:187],v[80:83],v[8:11],v[184:187]\n"
"ds_read_b64_tr_b16 v[78:79],v105 offset:9216\n"
"v_mfma_f32_16x16x32_bf16 v[216:219],v[80:83],v[16:19],v[216:219]\n"
"ds_read_b128 a[100:103],v96 offset:49152\n"
"v_mfma_f32_16x16x32_bf16 v[216:219],v[84:87],v[20:23],v[216:219]\n"
"ds_read_b128 a[108:111],v96 offset:50176\n"
"ds_read_b128 a[116:119],v96 offset:51200\n"
"v_mfma_f32_16x16x32_bf16 v[248:251],v[84:87],v[28:31],v[248:251]\n"
"s_load_dwordx2 s[32:33],s[0:1],56\n"
"s_load_dwordx2 s[36:37],s[0:1],64\n"
"s_load_dwordx2 s[40:41],s[0:1],72\n"
"v_mfma_f32_16x16x32_bf16 v[248:251],v[80:83],v[24:27],v[248:251]\n"
"s_add_u32 m0,s78,16384\n"
"ds_read_b128 a[124:127],v96 offset:52224\n"
"v_mfma_f32_16x16x32_bf16 v[252:255],v[88:91],v[24:27],v[252:255]\n"
"buffer_load_dwordx4 v112,s[24:27],s76 offen offset:0 sc1 lds\n"
"v_mfma_f32_16x16x32_bf16 v[252:255],v[92:95],v[28:31],v[252:255]\n"
"v_mfma_f32_16x16x32_bf16 v[220:223],v[92:95],v[20:23],v[220:223]\n"
"buffer_load_dwordx4 v112,s[24:27],s76 offen offset:1024 sc1 lds\n"
"v_mfma_f32_16x16x32_bf16 v[220:223],v[88:91],v[16:19],v[220:223]\n"
"v_mfma_f32_16x16x32_bf16 v[188:191],v[88:91],v[8:11],v[188:191]\n"
"buffer_load_dwordx4 v112,s[24:27],s76 offen offset:2048 sc1 lds\n"
"v_mfma_f32_16x16x32_bf16 v[188:191],v[92:95],v[12:15],v[188:191]\n"
"v_mfma_f32_16x16x32_bf16 v[156:159],v[92:95],v[4:7],v[156:159]\n"
"buffer_load_dwordx4 v112,s[24:27],s76 offen offset:3072 sc1 lds\n"
"v_mfma_f32_16x16x32_bf16 v[156:159],v[88:91],v[0:3],v[156:159]\n"
"s_xor_b32 s76,s76,114688\n"
"s_waitcnt vmcnt(24) lgkmcnt(0)\n"
"s_barrier\n"
"v_mfma_f32_16x16x32_bf16 v[128:131],a[128:131],a[96:99],v[128:131]\n"
"ds_read_b64_tr_b16 v[80:81],v98 offset:49152\n"
"ds_read_b64_tr_b16 v[88:89],v99 offset:49152\n"
"v_mfma_f32_16x16x32_bf16 v[128:131],a[132:135],a[100:103],v[128:131]\n"
"ds_read_b64_tr_b16 v[82:83],v98 offset:50176\n"
"ds_read_b64_tr_b16 v[90:91],v99 offset:50176\n"
"v_mfma_f32_16x16x32_bf16 v[160:163],a[132:135],a[108:111],v[160:163]\n"
"ds_read_b64_tr_b16 v[84:85],v98 offset:57344\n"
"ds_read_b64_tr_b16 v[92:93],v99 offset:57344\n"
"v_mfma_f32_16x16x32_bf16 v[160:163],a[128:131],a[104:107],v[160:163]\n"
"s_add_u32 m0,s85,98304\n"
"ds_read_b64_tr_b16 v[86:87],v98 offset:58368\n"
"ds_read_b64_tr_b16 v[94:95],v99 offset:58368\n"
"v_mfma_f32_16x16x32_bf16 v[192:195],a[128:131],a[112:115],v[192:195]\n"
"buffer_load_dwordx4 v110,s[28:31],s86 offen lds\n"
"v_mfma_f32_16x16x32_bf16 v[192:195],a[132:135],a[116:119],v[192:195]\n"
"v_accvgpr_write_b32 a154,v124\n"
"v_accvgpr_write_b32 a155,v125\n"
"v_mfma_f32_16x16x32_bf16 v[224:227],a[132:135],a[124:127],v[224:227]\n"
"v_accvgpr_write_b32 a152,1\n"
"v_lshrrev_b32 v124,2,v112\n"
"v_mfma_f32_16x16x32_bf16 v[224:227],a[128:131],a[120:123],v[224:227]\n"
"v_add_u32 v124,147456,v124\n"
"v_mov_b32 v125,0\n"
"v_mfma_f32_16x16x32_bf16 v[228:231],a[136:139],a[120:123],v[228:231]\n"
"ds_read_b64_tr_b16 a[128:129],v100 offset:49152\n"
"s_mov_b32 s101,0\n"
"v_mfma_f32_16x16x32_bf16 v[228:231],a[140:143],a[124:127],v[228:231]\n"
"ds_read_b64_tr_b16 a[130:131],v100 offset:50176\n"
"v_mfma_f32_16x16x32_bf16 v[196:199],a[140:143],a[116:119],v[196:199]\n"
"ds_read_b64_tr_b16 a[132:133],v100 offset:57344\n"
"v_mfma_f32_16x16x32_bf16 v[196:199],a[136:139],a[112:115],v[196:199]\n"
"s_add_u32 m0,s85,99328\n"
"ds_read_b64_tr_b16 a[134:135],v100 offset:58368\n"
"v_mfma_f32_16x16x32_bf16 v[164:167],a[136:139],a[104:107],v[164:167]\n"
"buffer_load_dwordx4 v111,s[28:31],s86 offen lds\n"
"v_mfma_f32_16x16x32_bf16 v[164:167],a[140:143],a[108:111],v[164:167]\n"
"s_add_u32 m0,s85,106496\n"
"s_xor_b32 s86,s86,262144\n"
"v_mfma_f32_16x16x32_bf16 v[132:135],a[140:143],a[100:103],v[132:135]\n"
"buffer_load_dwordx4 v110,s[28:31],s86 offen lds\n"
"v_mfma_f32_16x16x32_bf16 v[132:135],a[136:139],a[96:99],v[132:135]\n"
"v_mfma_f32_16x16x32_bf16 v[136:139],v[32:35],a[96:99],v[136:139]\n"
"ds_read_b64_tr_b16 a[136:137],v101 offset:49152\n"
"v_mfma_f32_16x16x32_bf16 v[136:139],v[36:39],a[100:103],v[136:139]\n"
"ds_read_b64_tr_b16 a[138:139],v101 offset:50176\n"
"v_mfma_f32_16x16x32_bf16 v[168:171],v[36:39],a[108:111],v[168:171]\n"
"ds_read_b64_tr_b16 a[140:141],v101 offset:57344\n"
"v_mfma_f32_16x16x32_bf16 v[168:171],v[32:35],a[104:107],v[168:171]\n"
"s_add_u32 m0,s85,107520\n"
"ds_read_b64_tr_b16 a[142:143],v101 offset:58368\n"
"v_mfma_f32_16x16x32_bf16 v[200:203],v[32:35],a[112:115],v[200:203]\n"
"buffer_load_dwordx4 v111,s[28:31],s86 offen lds\n"
"v_mfma_f32_16x16x32_bf16 v[200:203],v[36:39],a[116:119],v[200:203]\n"
"s_xor_b32 s86,s86,786432\n"
"v_mfma_f32_16x16x32_bf16 v[232:235],v[36:39],a[124:127],v[232:235]\n"
"v_mfma_f32_16x16x32_bf16 v[232:235],v[32:35],a[120:123],v[232:235]\n"
"v_mfma_f32_16x16x32_bf16 v[236:239],v[40:43],a[120:123],v[236:239]\n"
"ds_read_b64_tr_b16 v[32:33],v102 offset:49152\n"
"v_mfma_f32_16x16x32_bf16 v[236:239],v[44:47],a[124:127],v[236:239]\n"
"ds_read_b64_tr_b16 v[34:35],v102 offset:50176\n"
"v_mfma_f32_16x16x32_bf16 v[204:207],v[44:47],a[116:119],v[204:207]\n"
"ds_read_b64_tr_b16 v[36:37],v102 offset:57344\n"
"v_mfma_f32_16x16x32_bf16 v[204:207],v[40:43],a[112:115],v[204:207]\n"
"s_add_u32 m0,s78,32768\n"
"ds_read_b64_tr_b16 v[38:39],v102 offset:58368\n"
"buffer_load_dwordx4 v112,s[24:27],s76 offen offset:0 sc1 lds\n"
"v_mfma_f32_16x16x32_bf16 v[172:175],v[40:43],a[104:107],v[172:175]\n"
"buffer_load_dwordx2 v[118:119],v120,s[24:27],s63 offen sc1\n"
"v_mfma_f32_16x16x32_bf16 v[172:175],v[44:47],a[108:111],v[172:175]\n"
"v_mfma_f32_16x16x32_bf16 v[140:143],v[44:47],a[100:103],v[140:143]\n"
"buffer_load_dwordx4 v112,s[24:27],s76 offen offset:1024 sc1 lds\n"
"v_mfma_f32_16x16x32_bf16 v[140:143],v[40:43],a[96:99],v[140:143]\n"
"s_waitcnt vmcnt(23)\n"
"v_mfma_f32_16x16x32_bf16 v[144:147],v[48:51],a[96:99],v[144:147]\n"
"ds_read_b64_tr_b16 v[40:41],v103 offset:49152\n"
"s_and_b32 s82,s100,7\n"
"v_mfma_f32_16x16x32_bf16 v[144:147],v[52:55],a[100:103],v[144:147]\n"
"ds_read_b64_tr_b16 v[42:43],v103 offset:50176\n"
"s_mul_i32 s82,s82,28\n"
"v_mfma_f32_16x16x32_bf16 v[176:179],v[52:55],a[108:111],v[176:179]\n"
"ds_read_b64_tr_b16 v[44:45],v103 offset:57344\n"
"s_lshr_b32 s83,s100,3\n"
"v_mfma_f32_16x16x32_bf16 v[176:179],v[48:51],a[104:107],v[176:179]\n"
"ds_read_b64_tr_b16 v[46:47],v103 offset:58368\n"
"s_add_u32 s23,s82,s83\n"
"v_mfma_f32_16x16x32_bf16 v[208:211],v[48:51],a[112:115],v[208:211]\n"
"buffer_load_dwordx4 v112,s[24:27],s76 offen offset:2048 sc1 lds\n"
"v_mfma_f32_16x16x32_bf16 v[208:211],v[52:55],a[116:119],v[208:211]\n"
"s_cmp_lt_u32 s83,28\n"
"s_cselect_b32 s34,1827872,0\n"
"s_cselect_b32 s42,8192,0\n"
"s_cselect_b32 s23,s23,0\n"
"v_mfma_f32_16x16x32_bf16 v[240:243],v[52:55],a[124:127],v[240:243]\n"
"s_mov_b32 s35,0x20000\n"
"ds_read_b128 v[0:3],v97 offset:0\n"
"s_mov_b32 s43,0x20000\n"
"v_mfma_f32_16x16x32_bf16 v[240:243],v[48:51],a[120:123],v[240:243]\n"
"ds_read_b128 v[8:11],v97 offset:1024\n"
"s_lshl2_add_u32 s82,s83,s52\n"
"v_mfma_f32_16x16x32_bf16 v[244:247],v[56:59],a[120:123],v[244:247]\n"
"ds_read_b64_tr_b16 v[48:49],v104 offset:49152\n"
"s_mul_i32 s82,s82,57344\n"
"v_mfma_f32_16x16x32_bf16 v[244:247],v[60:63],a[124:127],v[244:247]\n"
"ds_read_b64_tr_b16 v[50:51],v104 offset:50176\n"
"s_and_b32 s83,s100,7\n"
"v_mfma_f32_16x16x32_bf16 v[212:215],v[60:63],a[116:119],v[212:215]\n"
"ds_read_b64_tr_b16 v[52:53],v104 offset:57344\n"
"s_mul_i32 s83,s83,896\n"
"v_mfma_f32_16x16x32_bf16 v[212:215],v[56:59],a[112:115],v[212:215]\n"
"ds_read_b64_tr_b16 v[54:55],v104 offset:58368\n"
"s_add_u32 s82,s82,s83\n"
"v_mfma_f32_16x16x32_bf16 v[180:183],v[56:59],a[104:107],v[180:183]\n"
"buffer_load_dwordx4 v112,s[24:27],s76 offen offset:3072 sc1 lds\n"
"v_mfma_f32_16x16x32_bf16 v[180:183],v[60:63],a[108:111],v[180:183]\n"
"s_add_u32 m0,s78,49152\n"
"s_xor_b32 s76,s76,16384\n"
"ds_read_b128 v[16:19],v97 offset:2048\n"
"v_mfma_f32_16x16x32_bf16 v[148:151],v[60:63],a[100:103],v[148:151]\n"
"buffer_load_dwordx4 v112,s[24:27],s76 offen offset:0 sc1 lds\n"
"v_mfma_f32_16x16x32_bf16 v[148:151],v[56:59],a[96:99],v[148:151]\n"
"s_add_u32 s44,s32,s82\n"
"ds_read_b128 v[24:27],v97 offset:3072\n"
"s_addc_u32 s45,s33,0\n"
"v_mfma_f32_16x16x32_bf16 v[152:155],v[64:67],a[96:99],v[152:155]\n"
"s_add_u32 s48,s36,s82\n"
"ds_read_b64_tr_b16 v[56:57],v105 offset:49152\n"
"s_addc_u32 s49,s37,0\n"
"v_mfma_f32_16x16x32_bf16 v[152:155],v[68:71],a[100:103],v[152:155]\n"
"ds_read_b64_tr_b16 v[58:59],v105 offset:50176\n"
"s_mov_b32 s46,51072\n"
"v_mfma_f32_16x16x32_bf16 v[184:187],v[68:71],a[108:111],v[184:187]\n"
"ds_read_b64_tr_b16 v[60:61],v105 offset:57344\n"
"s_mov_b32 s50,s46\n"
"v_mfma_f32_16x16x32_bf16 v[184:187],v[64:67],a[104:107],v[184:187]\n"
"ds_read_b64_tr_b16 v[62:63],v105 offset:58368\n"
"s_mov_b32 s47,0x20000\n"
"v_mfma_f32_16x16x32_bf16 v[216:219],v[64:67],a[112:115],v[216:219]\n"
"ds_read_b128 v[4:7],v97 offset:16384\n"
"s_mov_b32 s51,0x20000\n"
"v_mfma_f32_16x16x32_bf16 v[216:219],v[68:71],a[116:119],v[216:219]\n"
"ds_read_b128 v[12:15],v97 offset:17408\n"
"s_mul_i32 s82,s52,1835008\n"
"v_mfma_f32_16x16x32_bf16 v[248:251],v[68:71],a[124:127],v[248:251]\n"
"ds_read_b128 v[20:23],v97 offset:18432\n"
"s_lshl_b32 s83,s23,5\n"
"v_mfma_f32_16x16x32_bf16 v[248:251],v[64:67],a[120:123],v[248:251]\n"
"ds_read_b128 v[28:31],v97 offset:19456\n"
"s_add_u32 s82,s82,s83\n"
"v_mfma_f32_16x16x32_bf16 v[252:255],v[72:75],a[120:123],v[252:255]\n"
"buffer_load_dwordx4 v112,s[24:27],s76 offen offset:1024 sc1 lds\n"
"v_mfma_f32_16x16x32_bf16 v[252:255],v[76:79],a[124:127],v[252:255]\n"
"s_add_u32 s32,s32,s82\n"
"s_addc_u32 s33,s33,0\n"
"s_add_u32 s36,s36,s82\n"
"v_mfma_f32_16x16x32_bf16 v[220:223],v[76:79],a[116:119],v[220:223]\n"
"s_addc_u32 s37,s37,0\n"
"s_lshl2_add_u32 s82,s23,s52\n"
"s_lshl_b32 s82,s82,13\n"
"v_mfma_f32_16x16x32_bf16 v[220:223],v[72:75],a[112:115],v[220:223]\n"
"buffer_load_dwordx4 v112,s[24:27],s76 offen offset:2048 sc1 lds\n"
"v_mfma_f32_16x16x32_bf16 v[188:191],v[72:75],a[104:107],v[188:191]\n"
"s_add_u32 s40,s40,s82\n"
"s_addc_u32 s41,s41,0\n"
"v_lshrrev_b32 v121,5,v112\n"
"v_mfma_f32_16x16x32_bf16 v[188:191],v[76:79],a[108:111],v[188:191]\n"
"v_mul_u32_u24 v121,7168,v121\n"
"v_and_b32 v122,16,v112\n"
"v_mfma_f32_16x16x32_bf16 v[156:159],v[76:79],a[100:103],v[156:159]\n"
"buffer_load_dwordx4 v112,s[24:27],s76 offen offset:3072 sc1 lds\n"
"v_mfma_f32_16x16x32_bf16 v[156:159],v[72:75],a[96:99],v[156:159]\n"
"s_xor_b32 s76,s76,49152\n"
"v_add_u32 v115,v121,v122\n"
"s_waitcnt vmcnt(21) lgkmcnt(0)\n"
"s_barrier\n"
"v_mfma_f32_16x16x32_bf16 v[128:131],v[80:83],v[0:3],v[128:131]\n"
"ds_read_b64_tr_b16 v[64:65],v98 offset:32768\n"
"ds_read_b64_tr_b16 v[72:73],v99 offset:32768\n"
"v_mfma_f32_16x16x32_bf16 v[128:131],v[84:87],v[4:7],v[128:131]\n"
"ds_read_b64_tr_b16 v[66:67],v98 offset:33792\n"
"ds_read_b64_tr_b16 v[74:75],v99 offset:33792\n"
"v_mfma_f32_16x16x32_bf16 v[160:163],v[84:87],v[12:15],v[160:163]\n"
"ds_read_b64_tr_b16 v[68:69],v98 offset:40960\n"
"ds_read_b64_tr_b16 v[76:77],v99 offset:40960\n"
"v_mfma_f32_16x16x32_bf16 v[160:163],v[80:83],v[8:11],v[160:163]\n"
"s_add_u32 m0,s85,147456\n"
"ds_read_b64_tr_b16 v[70:71],v98 offset:41984\n"
"ds_read_b64_tr_b16 v[78:79],v99 offset:41984\n"
"v_mfma_f32_16x16x32_bf16 v[192:195],v[80:83],v[16:19],v[192:195]\n"
"buffer_load_dwordx4 v110,s[28:31],s86 offen lds\n"
"ds_read_b64_tr_b16 a[176:177],v107 offset:0\n"
"v_mfma_f32_16x16x32_bf16 v[192:195],v[84:87],v[20:23],v[192:195]\n"
"s_add_u32 m0,s85,148480\n"
"ds_read_b64_tr_b16 a[178:179],v107 offset:4096\n"
"ds_read_b64_tr_b16 a[180:181],v107 offset:8\n"
"v_mfma_f32_16x16x32_bf16 v[224:227],v[84:87],v[28:31],v[224:227]\n"
"buffer_load_dwordx4 v111,s[28:31],s86 offen lds\n"
"ds_read_b64_tr_b16 a[182:183],v107 offset:4104\n"
"v_mfma_f32_16x16x32_bf16 v[224:227],v[80:83],v[24:27],v[224:227]\n"
"s_xor_b32 s86,s86,262144\n"
"ds_read_b64_tr_b16 a[184:185],v107 offset:8192\n"
"ds_read_b64_tr_b16 a[186:187],v107 offset:12288\n"
"v_mfma_f32_16x16x32_bf16 v[228:231],v[88:91],v[24:27],v[228:231]\n"
"ds_read_b64_tr_b16 v[80:81],v100 offset:32768\n"
"s_waitcnt vmcnt(9)\n"
"v_xor_b32 v116,s80,v118\n"
"v_mfma_f32_16x16x32_bf16 v[228:231],v[92:95],v[28:31],v[228:231]\n"
"ds_read_b64_tr_b16 v[82:83],v100 offset:33792\n"
"ds_read_b64_tr_b16 a[188:189],v107 offset:8200\n"
"v_xor_b32 v117,s81,v119\n"
"v_mfma_f32_16x16x32_bf16 v[196:199],v[92:95],v[20:23],v[196:199]\n"
"ds_read_b64_tr_b16 v[84:85],v100 offset:40960\n"
"ds_read_b64_tr_b16 a[190:191],v107 offset:12296\n"
"v_mfma_f32_16x16x32_bf16 v[196:199],v[88:91],v[16:19],v[196:199]\n"
"s_add_u32 m0,s85,155648\n"
"ds_read_b64_tr_b16 v[86:87],v100 offset:41984\n"
"v_or_b32 v116,v116,v117\n"
"v_mfma_f32_16x16x32_bf16 v[164:167],v[88:91],v[8:11],v[164:167]\n"
"buffer_load_dwordx4 v110,s[28:31],s86 offen lds\n"
"v_mfma_f32_16x16x32_bf16 v[164:167],v[92:95],v[12:15],v[164:167]\n"
"s_add_u32 m0,s85,156672\n"
"v_cmp_ne_u32 vcc,0,v116\n"
"v_mfma_f32_16x16x32_bf16 v[132:135],v[92:95],v[4:7],v[132:135]\n"
"buffer_load_dwordx4 v111,s[28:31],s86 offen lds\n"
"v_mfma_f32_16x16x32_bf16 v[132:135],v[88:91],v[0:3],v[132:135]\n"
"s_xor_b32 s86,s86,16515072\n"
"s_cbranch_vccnz .Lfk_kv_s\n"
".Lfk_kv_ok:\n"
"v_mfma_f32_16x16x32_bf16 v[136:139],a[128:131],v[0:3],v[136:139]\n"
"ds_read_b64_tr_b16 v[88:89],v101 offset:32768\n"
"s_mov_b32 s54,-229376\n"
"v_mfma_f32_16x16x32_bf16 v[136:139],a[132:135],v[4:7],v[136:139]\n"
"ds_read_b64_tr_b16 v[90:91],v101 offset:33792\n"
"s_lshl_b32 s55,s52,10\n"
"v_mfma_f32_16x16x32_bf16 v[168:171],a[132:135],v[12:15],v[168:171]\n"
"ds_read_b64_tr_b16 v[92:93],v101 offset:40960\n"
"s_add_u32 s55,s55,131072\n"
"v_mfma_f32_16x16x32_bf16 v[168:171],a[128:131],v[8:11],v[168:171]\n"
"s_add_u32 m0,s85,81920\n"
"s_add_u32 s77,s73,16384\n"
"ds_read_b64_tr_b16 v[94:95],v101 offset:41984\n"
"v_mfma_f32_16x16x32_bf16 v[200:203],a[128:131],v[16:19],v[200:203]\n"
"buffer_load_dwordx4 v112,s[24:27],s77 offen offset:0 sc1 lds\n"
"v_mfma_f32_16x16x32_bf16 v[200:203],a[132:135],v[20:23],v[200:203]\n"
"s_mov_b32 s22,229376\n"
"v_lshrrev_b32 v121,7,v112\n"
"v_mfma_f32_16x16x32_bf16 v[232:235],a[132:135],v[28:31],v[232:235]\n"
"buffer_load_dwordx4 v112,s[24:27],s77 offen offset:1024 sc1 lds\n"
"v_mfma_f32_16x16x32_bf16 v[232:235],a[128:131],v[24:27],v[232:235]\n"
"v_mul_u32_u24 v121,7168,v121\n"
"v_lshlrev_b32 v123,3,v112\n"
"v_mfma_f32_16x16x32_bf16 v[236:239],a[136:139],v[24:27],v[236:239]\n"
"ds_read_b64_tr_b16 a[128:129],v102 offset:32768\n"
"v_and_b32 v122,0x380,v123\n"
"v_mfma_f32_16x16x32_bf16 v[236:239],a[140:143],v[28:31],v[236:239]\n"
"ds_read_b64_tr_b16 a[130:131],v102 offset:33792\n"
"v_min_u32 v122,0x300,v122\n"
"v_mfma_f32_16x16x32_bf16 v[204:207],a[140:143],v[20:23],v[204:207]\n"
"ds_read_b64_tr_b16 a[132:133],v102 offset:40960\n"
"v_add_u32 v122,v121,v122\n"
"v_mfma_f32_16x16x32_bf16 v[204:207],a[136:139],v[16:19],v[204:207]\n"
"s_add_u32 m0,s85,65536\n"
"s_add_u32 s77,s73,65536\n"
"ds_read_b64_tr_b16 a[134:135],v102 offset:41984\n"
"v_mfma_f32_16x16x32_bf16 v[172:175],a[136:139],v[8:11],v[172:175]\n"
"buffer_load_dwordx4 v112,s[24:27],s77 offen offset:0 sc1 lds\n"
"v_mfma_f32_16x16x32_bf16 v[172:175],a[140:143],v[12:15],v[172:175]\n"
"v_mfma_f32_16x16x32_bf16 v[140:143],a[140:143],v[4:7],v[140:143]\n"
"buffer_load_dwordx4 v112,s[24:27],s77 offen offset:1024 sc1 lds\n"
"v_mfma_f32_16x16x32_bf16 v[140:143],a[136:139],v[0:3],v[140:143]\n"
"v_mfma_f32_16x16x32_bf16 v[144:147],v[32:35],v[0:3],v[144:147]\n"
"ds_read_b64_tr_b16 a[136:137],v103 offset:32768\n"
"v_mfma_f32_16x16x32_bf16 v[144:147],v[36:39],v[4:7],v[144:147]\n"
"ds_read_b64_tr_b16 a[138:139],v103 offset:33792\n"
"v_mfma_f32_16x16x32_bf16 v[176:179],v[36:39],v[12:15],v[176:179]\n"
"ds_read_b64_tr_b16 a[140:141],v103 offset:40960\n"
"v_mfma_f32_16x16x32_bf16 v[176:179],v[32:35],v[8:11],v[176:179]\n"
"s_add_u32 m0,s85,73728\n"
"s_add_u32 s77,s73,24576\n"
"ds_read_b64_tr_b16 a[142:143],v103 offset:41984\n"
"v_mfma_f32_16x16x32_bf16 v[208:211],v[32:35],v[16:19],v[208:211]\n"
"buffer_load_dwordx4 v112,s[24:27],s77 offen offset:0 sc1 lds\n"
"v_mfma_f32_16x16x32_bf16 v[208:211],v[36:39],v[20:23],v[208:211]\n"
"ds_read_b128 a[96:99],v96 offset:0\n"
"v_mfma_f32_16x16x32_bf16 v[240:243],v[36:39],v[28:31],v[240:243]\n"
"buffer_load_dwordx4 v112,s[24:27],s77 offen offset:1024 sc1 lds\n"
"v_mfma_f32_16x16x32_bf16 v[240:243],v[32:35],v[24:27],v[240:243]\n"
"ds_read_b128 a[104:107],v96 offset:1024\n"
"v_mfma_f32_16x16x32_bf16 v[244:247],v[40:43],v[24:27],v[244:247]\n"
"ds_read_b64_tr_b16 v[32:33],v104 offset:32768\n"
"v_mfma_f32_16x16x32_bf16 v[244:247],v[44:47],v[28:31],v[244:247]\n"
"ds_read_b64_tr_b16 v[34:35],v104 offset:33792\n"
"v_mfma_f32_16x16x32_bf16 v[212:215],v[44:47],v[20:23],v[212:215]\n"
"ds_read_b64_tr_b16 v[36:37],v104 offset:40960\n"
"v_mfma_f32_16x16x32_bf16 v[212:215],v[40:43],v[16:19],v[212:215]\n"
"ds_read_b64_tr_b16 v[38:39],v104 offset:41984\n"
"v_mfma_f32_16x16x32_bf16 v[180:183],v[40:43],v[8:11],v[180:183]\n"
"v_mfma_f32_16x16x32_bf16 v[180:183],v[44:47],v[12:15],v[180:183]\n"
"ds_read_b128 a[112:115],v96 offset:2048\n"
"v_mfma_f32_16x16x32_bf16 v[148:151],v[44:47],v[4:7],v[148:151]\n"
"v_mfma_f32_16x16x32_bf16 v[148:151],v[40:43],v[0:3],v[148:151]\n"
"ds_read_b128 a[120:123],v96 offset:3072\n"
"v_mfma_f32_16x16x32_bf16 v[152:155],v[48:51],v[0:3],v[152:155]\n"
"ds_read_b64_tr_b16 v[40:41],v105 offset:32768\n"
"v_mfma_f32_16x16x32_bf16 v[152:155],v[52:55],v[4:7],v[152:155]\n"
"ds_read_b64_tr_b16 v[42:43],v105 offset:33792\n"
"v_mfma_f32_16x16x32_bf16 v[184:187],v[52:55],v[12:15],v[184:187]\n"
"ds_read_b64_tr_b16 v[44:45],v105 offset:40960\n"
"v_mfma_f32_16x16x32_bf16 v[184:187],v[48:51],v[8:11],v[184:187]\n"
"ds_read_b64_tr_b16 v[46:47],v105 offset:41984\n"
"v_mfma_f32_16x16x32_bf16 v[216:219],v[48:51],v[16:19],v[216:219]\n"
"ds_read_b128 a[100:103],v96 offset:16384\n"
"v_mfma_f32_16x16x32_bf16 v[216:219],v[52:55],v[20:23],v[216:219]\n"
"ds_read_b128 a[108:111],v96 offset:17408\n"
"v_mfma_f32_16x16x32_bf16 v[248:251],v[52:55],v[28:31],v[248:251]\n"
"ds_read_b128 a[116:119],v96 offset:18432\n"
"v_mfma_f32_16x16x32_bf16 v[248:251],v[48:51],v[24:27],v[248:251]\n"
"ds_read_b128 a[124:127],v96 offset:19456\n"
"v_mfma_f32_16x16x32_bf16 v[252:255],v[56:59],v[24:27],v[252:255]\n"
"v_mfma_f32_16x16x32_bf16 v[252:255],v[60:63],v[28:31],v[252:255]\n"
"v_mfma_f32_16x16x32_bf16 v[220:223],v[60:63],v[20:23],v[220:223]\n"
"v_mfma_f32_16x16x32_bf16 v[220:223],v[56:59],v[16:19],v[220:223]\n"
"v_mfma_f32_16x16x32_bf16 v[188:191],v[56:59],v[8:11],v[188:191]\n"
"v_mfma_f32_16x16x32_bf16 v[188:191],v[60:63],v[12:15],v[188:191]\n"
"v_mfma_f32_16x16x32_bf16 v[156:159],v[60:63],v[4:7],v[156:159]\n"
"v_mfma_f32_16x16x32_bf16 v[156:159],v[56:59],v[0:3],v[156:159]\n"
"s_waitcnt lgkmcnt(0)\n"
"s_barrier\n"
"v_mfma_f32_16x16x32_bf16 v[128:131],v[64:67],a[96:99],v[128:131]\n"
"ds_read_b64_tr_b16 v[48:49],v98 offset:0\n"
"ds_read_b64_tr_b16 v[56:57],v99 offset:0\n"
"v_mfma_f32_16x16x32_bf16 v[128:131],v[68:71],a[100:103],v[128:131]\n"
"ds_read_b64_tr_b16 v[50:51],v98 offset:1024\n"
"ds_read_b64_tr_b16 v[58:59],v99 offset:1024\n"
"v_mfma_f32_16x16x32_bf16 v[160:163],v[68:71],a[108:111],v[160:163]\n"
"ds_read_b64_tr_b16 v[52:53],v98 offset:8192\n"
"ds_read_b64_tr_b16 v[60:61],v99 offset:8192\n"
"v_mfma_f32_16x16x32_bf16 v[160:163],v[64:67],a[104:107],v[160:163]\n"
"s_add_u32 m0,s78,0\n"
"ds_read_b64_tr_b16 v[54:55],v98 offset:9216\n"
"ds_read_b64_tr_b16 v[62:63],v99 offset:9216\n"
"v_mfma_f32_16x16x32_bf16 v[192:195],v[64:67],a[112:115],v[192:195]\n"
"buffer_load_dwordx4 v112,s[24:27],s76 offen offset:0 sc1 lds\n"
"v_mfma_f32_16x16x32_bf16 v[192:195],v[68:71],a[116:119],v[192:195]\n"
"v_mfma_f32_16x16x32_bf16 v[224:227],v[68:71],a[124:127],v[224:227]\n"
"v_mfma_f32_16x16x32_bf16 v[224:227],v[64:67],a[120:123],v[224:227]\n"
"v_mfma_f32_16x16x32_bf16 v[228:231],v[72:75],a[120:123],v[228:231]\n"
"ds_read_b64_tr_b16 v[64:65],v100 offset:0\n"
"v_mfma_f32_16x16x32_bf16 v[228:231],v[76:79],a[124:127],v[228:231]\n"
"ds_read_b64_tr_b16 v[66:67],v100 offset:1024\n"
"v_mfma_f32_16x16x32_bf16 v[196:199],v[76:79],a[116:119],v[196:199]\n"
"ds_read_b64_tr_b16 v[68:69],v100 offset:8192\n"
"v_mfma_f32_16x16x32_bf16 v[196:199],v[72:75],a[112:115],v[196:199]\n"
"ds_read_b64_tr_b16 v[70:71],v100 offset:9216\n"
"v_mfma_f32_16x16x32_bf16 v[164:167],v[72:75],a[104:107],v[164:167]\n"
"buffer_load_dwordx4 v112,s[24:27],s76 offen offset:1024 sc1 lds\n"
"v_mfma_f32_16x16x32_bf16 v[164:167],v[76:79],a[108:111],v[164:167]\n"
"v_mfma_f32_16x16x32_bf16 v[132:135],v[76:79],a[100:103],v[132:135]\n"
"buffer_load_dwordx4 v112,s[24:27],s76 offen offset:2048 sc1 lds\n"
"v_mfma_f32_16x16x32_bf16 v[132:135],v[72:75],a[96:99],v[132:135]\n"
"v_mfma_f32_16x16x32_bf16 v[136:139],v[80:83],a[96:99],v[136:139]\n"
"ds_read_b64_tr_b16 v[72:73],v101 offset:0\n"
"v_mfma_f32_16x16x32_bf16 v[136:139],v[84:87],a[100:103],v[136:139]\n"
"ds_read_b64_tr_b16 v[74:75],v101 offset:1024\n"
"v_mfma_f32_16x16x32_bf16 v[168:171],v[84:87],a[108:111],v[168:171]\n"
"ds_read_b64_tr_b16 v[76:77],v101 offset:8192\n"
"v_mfma_f32_16x16x32_bf16 v[168:171],v[80:83],a[104:107],v[168:171]\n"
"ds_read_b64_tr_b16 v[78:79],v101 offset:9216\n"
"v_mfma_f32_16x16x32_bf16 v[200:203],v[80:83],a[112:115],v[200:203]\n"
"buffer_load_dwordx4 v112,s[24:27],s76 offen offset:3072 sc1 lds\n"
"v_mfma_f32_16x16x32_bf16 v[200:203],v[84:87],a[116:119],v[200:203]\n"
"s_xor_b32 s76,s76,16384\n"
"v_mfma_f32_16x16x32_bf16 v[232:235],v[84:87],a[124:127],v[232:235]\n"
"v_mfma_f32_16x16x32_bf16 v[232:235],v[80:83],a[120:123],v[232:235]\n"
"v_mfma_f32_16x16x32_bf16 v[236:239],v[88:91],a[120:123],v[236:239]\n"
"ds_read_b64_tr_b16 v[80:81],v102 offset:0\n"
"v_mfma_f32_16x16x32_bf16 v[236:239],v[92:95],a[124:127],v[236:239]\n"
"ds_read_b64_tr_b16 v[82:83],v102 offset:1024\n"
"v_mfma_f32_16x16x32_bf16 v[204:207],v[92:95],a[116:119],v[204:207]\n"
"ds_read_b64_tr_b16 v[84:85],v102 offset:8192\n"
"v_mfma_f32_16x16x32_bf16 v[204:207],v[88:91],a[112:115],v[204:207]\n"
"s_add_u32 m0,s78,16384\n"
"ds_read_b64_tr_b16 v[86:87],v102 offset:9216\n"
"v_mfma_f32_16x16x32_bf16 v[172:175],v[88:91],a[104:107],v[172:175]\n"
"buffer_load_dwordx4 v112,s[24:27],s76 offen offset:0 sc1 lds\n"
"v_mfma_f32_16x16x32_bf16 v[172:175],v[92:95],a[108:111],v[172:175]\n"
"v_mfma_f32_16x16x32_bf16 v[140:143],v[92:95],a[100:103],v[140:143]\n"
"buffer_load_dwordx4 v112,s[24:27],s76 offen offset:1024 sc1 lds\n"
"v_mfma_f32_16x16x32_bf16 v[140:143],v[88:91],a[96:99],v[140:143]\n"
".Lfk_q27:\n"
"s_waitcnt vmcnt(16)\n"
"v_mfma_f32_16x16x32_bf16 v[144:147],a[128:131],a[96:99],v[144:147]\n"
"ds_read_b64_tr_b16 v[88:89],v103 offset:0\n"
"v_mfma_f32_16x16x32_bf16 v[144:147],a[132:135],a[100:103],v[144:147]\n"
"ds_read_b64_tr_b16 v[90:91],v103 offset:1024\n"
"v_mfma_f32_16x16x32_bf16 v[176:179],a[132:135],a[108:111],v[176:179]\n"
"ds_read_b64_tr_b16 v[92:93],v103 offset:8192\n"
"v_mfma_f32_16x16x32_bf16 v[176:179],a[128:131],a[104:107],v[176:179]\n"
"ds_read_b64_tr_b16 v[94:95],v103 offset:9216\n"
"v_mfma_f32_16x16x32_bf16 v[208:211],a[128:131],a[112:115],v[208:211]\n"
"buffer_load_dwordx4 v112,s[24:27],s76 offen offset:2048 sc1 lds\n"
"v_mfma_f32_16x16x32_bf16 v[208:211],a[132:135],a[116:119],v[208:211]\n"
"ds_read_b128 v[0:3],v96 offset:32768\n"
"v_mfma_f32_16x16x32_bf16 v[240:243],a[132:135],a[124:127],v[240:243]\n"
"v_mfma_f32_16x16x32_bf16 v[240:243],a[128:131],a[120:123],v[240:243]\n"
"ds_read_b128 v[8:11],v96 offset:33792\n"
"v_mfma_f32_16x16x32_bf16 v[244:247],a[136:139],a[120:123],v[244:247]\n"
"ds_read_b64_tr_b16 a[128:129],v104 offset:0\n"
"v_mfma_f32_16x16x32_bf16 v[244:247],a[140:143],a[124:127],v[244:247]\n"
"ds_read_b64_tr_b16 a[130:131],v104 offset:1024\n"
"v_mfma_f32_16x16x32_bf16 v[212:215],a[140:143],a[116:119],v[212:215]\n"
"ds_read_b64_tr_b16 a[132:133],v104 offset:8192\n"
"v_mfma_f32_16x16x32_bf16 v[212:215],a[136:139],a[112:115],v[212:215]\n"
"ds_read_b64_tr_b16 a[134:135],v104 offset:9216\n"
"v_mfma_f32_16x16x32_bf16 v[180:183],a[136:139],a[104:107],v[180:183]\n"
"buffer_load_dwordx4 v112,s[24:27],s76 offen offset:3072 sc1 lds\n"
"v_mfma_f32_16x16x32_bf16 v[180:183],a[140:143],a[108:111],v[180:183]\n"
"s_add_u32 m0,s85,122880\n"
"s_add_u32 s77,s73,0\n"
"ds_read_b128 v[16:19],v96 offset:34816\n"
"v_mfma_f32_16x16x32_bf16 v[148:151],a[140:143],a[100:103],v[148:151]\n"
"buffer_load_dwordx4 v112,s[24:27],s77 offen offset:0 sc1 lds\n"
"v_mfma_f32_16x16x32_bf16 v[148:151],a[136:139],a[96:99],v[148:151]\n"
"s_xor_b32 s76,s76,1032192\n"
"ds_read_b128 v[24:27],v96 offset:35840\n"
"v_mfma_f32_16x16x32_bf16 v[152:155],v[32:35],a[96:99],v[152:155]\n"
"ds_read_b64_tr_b16 a[136:137],v105 offset:0\n"
"v_mfma_f32_16x16x32_bf16 v[152:155],v[36:39],a[100:103],v[152:155]\n"
"ds_read_b64_tr_b16 a[138:139],v105 offset:1024\n"
"v_mfma_f32_16x16x32_bf16 v[184:187],v[36:39],a[108:111],v[184:187]\n"
"ds_read_b64_tr_b16 a[140:141],v105 offset:8192\n"
"v_mfma_f32_16x16x32_bf16 v[184:187],v[32:35],a[104:107],v[184:187]\n"
"ds_read_b64_tr_b16 a[142:143],v105 offset:9216\n"
"v_mfma_f32_16x16x32_bf16 v[216:219],v[32:35],a[112:115],v[216:219]\n"
"ds_read_b128 v[4:7],v96 offset:49152\n"
"v_mfma_f32_16x16x32_bf16 v[216:219],v[36:39],a[116:119],v[216:219]\n"
"ds_read_b128 v[12:15],v96 offset:50176\n"
"v_mfma_f32_16x16x32_bf16 v[248:251],v[36:39],a[124:127],v[248:251]\n"
"ds_read_b128 v[20:23],v96 offset:51200\n"
"v_mfma_f32_16x16x32_bf16 v[248:251],v[32:35],a[120:123],v[248:251]\n"
"ds_read_b128 v[28:31],v96 offset:52224\n"
"v_mfma_f32_16x16x32_bf16 v[252:255],v[40:43],a[120:123],v[252:255]\n"
"buffer_load_dwordx4 v112,s[24:27],s77 offen offset:1024 sc1 lds\n"
"v_mfma_f32_16x16x32_bf16 v[252:255],v[44:47],a[124:127],v[252:255]\n"
"v_mfma_f32_16x16x32_bf16 v[220:223],v[44:47],a[116:119],v[220:223]\n"
"s_add_u32 m0,s85,114688\n"
"s_add_u32 s77,s73,32768\n"
"v_mfma_f32_16x16x32_bf16 v[220:223],v[40:43],a[112:115],v[220:223]\n"
"buffer_load_dwordx4 v112,s[24:27],s77 offen offset:0 sc1 lds\n"
"v_mfma_f32_16x16x32_bf16 v[188:191],v[40:43],a[104:107],v[188:191]\n"
"v_mfma_f32_16x16x32_bf16 v[188:191],v[44:47],a[108:111],v[188:191]\n"
"v_mfma_f32_16x16x32_bf16 v[156:159],v[44:47],a[100:103],v[156:159]\n"
"buffer_load_dwordx4 v112,s[24:27],s77 offen offset:1024 sc1 lds\n"
"v_mfma_f32_16x16x32_bf16 v[156:159],v[40:43],a[96:99],v[156:159]\n"
"s_waitcnt vmcnt(18) lgkmcnt(0)\n"
"s_barrier\n"
"v_mfma_f32_16x16x32_bf16 v[128:131],v[48:51],v[0:3],v[128:131]\n"
"ds_read_b64_tr_b16 v[32:33],v98 offset:49152\n"
"ds_read_b64_tr_b16 v[40:41],v99 offset:49152\n"
"v_mfma_f32_16x16x32_bf16 v[128:131],v[52:55],v[4:7],v[128:131]\n"
"ds_read_b64_tr_b16 v[34:35],v98 offset:50176\n"
"ds_read_b64_tr_b16 v[42:43],v99 offset:50176\n"
"v_mfma_f32_16x16x32_bf16 v[160:163],v[52:55],v[12:15],v[160:163]\n"
"s_add_u32 m0,s85,98304\n"
"ds_read_b64_tr_b16 v[36:37],v98 offset:57344\n"
"ds_read_b64_tr_b16 v[44:45],v99 offset:57344\n"
"v_mfma_f32_16x16x32_bf16 v[160:163],v[48:51],v[8:11],v[160:163]\n"
"s_add_u32 s77,s73,49152\n"
"ds_read_b64_tr_b16 v[38:39],v98 offset:58368\n"
"ds_read_b64_tr_b16 v[46:47],v99 offset:58368\n"
"v_mfma_f32_16x16x32_bf16 v[192:195],v[48:51],v[16:19],v[192:195]\n"
"buffer_load_dwordx4 v112,s[24:27],s77 offen offset:0 sc1 lds\n"
"v_mfma_f32_16x16x32_bf16 v[192:195],v[52:55],v[20:23],v[192:195]\n"
"v_mfma_f32_16x16x32_bf16 v[224:227],v[52:55],v[28:31],v[224:227]\n"
"buffer_load_dwordx4 v112,s[24:27],s77 offen offset:1024 sc1 lds\n"
"v_mfma_f32_16x16x32_bf16 v[224:227],v[48:51],v[24:27],v[224:227]\n"
"v_mfma_f32_16x16x32_bf16 v[228:231],v[56:59],v[24:27],v[228:231]\n"
"ds_read_b64_tr_b16 v[48:49],v100 offset:49152\n"
"v_mfma_f32_16x16x32_bf16 v[228:231],v[60:63],v[28:31],v[228:231]\n"
"ds_read_b64_tr_b16 v[50:51],v100 offset:50176\n"
"v_mfma_f32_16x16x32_bf16 v[196:199],v[60:63],v[20:23],v[196:199]\n"
"ds_read_b64_tr_b16 v[52:53],v100 offset:57344\n"
"v_mfma_f32_16x16x32_bf16 v[196:199],v[56:59],v[16:19],v[196:199]\n"
"s_add_u32 m0,s85,106496\n"
"s_add_u32 s77,s73,8192\n"
"ds_read_b64_tr_b16 v[54:55],v100 offset:58368\n"
"v_mfma_f32_16x16x32_bf16 v[164:167],v[56:59],v[8:11],v[164:167]\n"
"buffer_load_dwordx4 v112,s[24:27],s77 offen offset:0 sc1 lds\n"
"v_mfma_f32_16x16x32_bf16 v[164:167],v[60:63],v[12:15],v[164:167]\n"
"v_mfma_f32_16x16x32_bf16 v[132:135],v[60:63],v[4:7],v[132:135]\n"
"buffer_load_dwordx4 v112,s[24:27],s77 offen offset:1024 sc1 lds\n"
"v_mfma_f32_16x16x32_bf16 v[132:135],v[56:59],v[0:3],v[132:135]\n"
"v_mfma_f32_16x16x32_bf16 v[136:139],v[64:67],v[0:3],v[136:139]\n"
"ds_read_b64_tr_b16 v[56:57],v101 offset:49152\n"
"v_mfma_f32_16x16x32_bf16 v[136:139],v[68:71],v[4:7],v[136:139]\n"
"ds_read_b64_tr_b16 v[58:59],v101 offset:50176\n"
"v_mfma_f32_16x16x32_bf16 v[168:171],v[68:71],v[12:15],v[168:171]\n"
"ds_read_b64_tr_b16 v[60:61],v101 offset:57344\n"
"v_mfma_f32_16x16x32_bf16 v[168:171],v[64:67],v[8:11],v[168:171]\n"
"ds_read_b64_tr_b16 v[62:63],v101 offset:58368\n"
"v_mfma_f32_16x16x32_bf16 v[200:203],v[64:67],v[16:19],v[200:203]\n"
"buffer_load_dword a148,v122,s[48:51],0 offen\n"
"v_mfma_f32_16x16x32_bf16 v[200:203],v[68:71],v[20:23],v[200:203]\n"
"v_mfma_f32_16x16x32_bf16 v[232:235],v[68:71],v[28:31],v[232:235]\n"
"v_mfma_f32_16x16x32_bf16 v[232:235],v[64:67],v[24:27],v[232:235]\n"
"v_mfma_f32_16x16x32_bf16 v[236:239],v[72:75],v[24:27],v[236:239]\n"
"ds_read_b64_tr_b16 v[64:65],v102 offset:49152\n"
"v_mfma_f32_16x16x32_bf16 v[236:239],v[76:79],v[28:31],v[236:239]\n"
"ds_read_b64_tr_b16 v[66:67],v102 offset:50176\n"
"v_mfma_f32_16x16x32_bf16 v[204:207],v[76:79],v[20:23],v[204:207]\n"
"ds_read_b64_tr_b16 v[68:69],v102 offset:57344\n"
"v_mfma_f32_16x16x32_bf16 v[204:207],v[72:75],v[16:19],v[204:207]\n"
"ds_read_b64_tr_b16 v[70:71],v102 offset:58368\n"
"v_mfma_f32_16x16x32_bf16 v[172:175],v[72:75],v[8:11],v[172:175]\n"
"v_mfma_f32_16x16x32_bf16 v[172:175],v[76:79],v[12:15],v[172:175]\n"
"v_mfma_f32_16x16x32_bf16 v[140:143],v[76:79],v[4:7],v[140:143]\n"
"v_mfma_f32_16x16x32_bf16 v[140:143],v[72:75],v[0:3],v[140:143]\n"
"s_waitcnt vmcnt(9)\n"
"v_mfma_f32_16x16x32_bf16 v[144:147],v[80:83],v[0:3],v[144:147]\n"
"ds_read_b64_tr_b16 v[72:73],v103 offset:49152\n"
"s_mov_b32 s90,0\n"
"v_mfma_f32_16x16x32_bf16 v[144:147],v[84:87],v[4:7],v[144:147]\n"
"ds_read_b64_tr_b16 v[74:75],v103 offset:50176\n"
"s_mov_b32 s16,0xc21c0000\n"
"v_mfma_f32_16x16x32_bf16 v[176:179],v[84:87],v[12:15],v[176:179]\n"
"ds_read_b64_tr_b16 v[76:77],v103 offset:57344\n"
"s_mov_b32 s17,0x41a00000\n"
"v_mfma_f32_16x16x32_bf16 v[176:179],v[80:83],v[8:11],v[176:179]\n"
"ds_read_b64_tr_b16 v[78:79],v103 offset:58368\n"
"s_mov_b32 s89,0\n"
"v_mfma_f32_16x16x32_bf16 v[208:211],v[80:83],v[16:19],v[208:211]\n"
"s_xor_b32 s82,s78,8192\n"
"v_add_u32 v116,s82,v112\n"
"v_mfma_f32_16x16x32_bf16 v[208:211],v[84:87],v[20:23],v[208:211]\n"
"ds_read_b128 a[96:99],v96 offset:0\n"
"v_add_u32 v117,4096,v112\n"
"v_mfma_f32_16x16x32_bf16 v[240:243],v[84:87],v[28:31],v[240:243]\n"
"s_getpc_b64 s[20:21]\n"
".Lfk_evp:\n"
"s_add_u32 s20,s20,.Lfk_ev-.Lfk_evp\n"
"s_addc_u32 s21,s21,0\n"
"v_mfma_f32_16x16x32_bf16 v[240:243],v[80:83],v[24:27],v[240:243]\n"
"ds_read_b128 a[104:107],v96 offset:1024\n"
"v_mfma_f32_16x16x32_bf16 v[244:247],v[88:91],v[24:27],v[244:247]\n"
"ds_read_b64_tr_b16 v[80:81],v104 offset:49152\n"
"v_mfma_f32_16x16x32_bf16 v[244:247],v[92:95],v[28:31],v[244:247]\n"
"ds_read_b64_tr_b16 v[82:83],v104 offset:50176\n"
"v_mfma_f32_16x16x32_bf16 v[212:215],v[92:95],v[20:23],v[212:215]\n"
"ds_read_b64_tr_b16 v[84:85],v104 offset:57344\n"
"v_mfma_f32_16x16x32_bf16 v[212:215],v[88:91],v[16:19],v[212:215]\n"
"ds_read_b64_tr_b16 v[86:87],v104 offset:58368\n"
"v_mfma_f32_16x16x32_bf16 v[180:183],v[88:91],v[8:11],v[180:183]\n"
"buffer_store_dwordx4 a[156:159],v112,s[24:27],s74 offen offset:0 sc1\n"
"v_mfma_f32_16x16x32_bf16 v[180:183],v[92:95],v[12:15],v[180:183]\n"
"ds_read_b128 a[112:115],v96 offset:2048\n"
"v_mfma_f32_16x16x32_bf16 v[148:151],v[92:95],v[4:7],v[148:151]\n"
"v_mfma_f32_16x16x32_bf16 v[148:151],v[88:91],v[0:3],v[148:151]\n"
"ds_read_b128 a[120:123],v96 offset:3072\n"
"v_mfma_f32_16x16x32_bf16 v[152:155],a[128:131],v[0:3],v[152:155]\n"
"ds_read_b64_tr_b16 v[88:89],v105 offset:49152\n"
"v_mfma_f32_16x16x32_bf16 v[152:155],a[132:135],v[4:7],v[152:155]\n"
"ds_read_b64_tr_b16 v[90:91],v105 offset:50176\n"
"v_mfma_f32_16x16x32_bf16 v[184:187],a[132:135],v[12:15],v[184:187]\n"
"ds_read_b64_tr_b16 v[92:93],v105 offset:57344\n"
"v_mfma_f32_16x16x32_bf16 v[184:187],a[128:131],v[8:11],v[184:187]\n"
"ds_read_b64_tr_b16 v[94:95],v105 offset:58368\n"
"v_mfma_f32_16x16x32_bf16 v[216:219],a[128:131],v[16:19],v[216:219]\n"
"ds_read_b128 a[100:103],v96 offset:16384\n"
"v_mfma_f32_16x16x32_bf16 v[216:219],a[132:135],v[20:23],v[216:219]\n"
"ds_read_b128 a[108:111],v96 offset:17408\n"
"v_add_u32 v113,65536,v112\n"
"v_mfma_f32_16x16x32_bf16 v[248:251],a[132:135],v[28:31],v[248:251]\n"
"ds_read_b128 a[116:119],v96 offset:18432\n"
"v_add_u32 v114,65536,v112\n"
"v_mfma_f32_16x16x32_bf16 v[248:251],a[128:131],v[24:27],v[248:251]\n"
"ds_read_b128 a[124:127],v96 offset:19456\n"
"v_mfma_f32_16x16x32_bf16 v[252:255],a[136:139],v[24:27],v[252:255]\n"
"v_mfma_f32_16x16x32_bf16 v[252:255],a[140:143],v[28:31],v[252:255]\n"
"buffer_store_dwordx4 a[156:159],v112,s[24:27],s74 offen offset:1024 sc1\n"
"v_mfma_f32_16x16x32_bf16 v[220:223],a[140:143],v[20:23],v[220:223]\n"
"v_mfma_f32_16x16x32_bf16 v[220:223],a[136:139],v[16:19],v[220:223]\n"
"v_mfma_f32_16x16x32_bf16 v[188:191],a[136:139],v[8:11],v[188:191]\n"
"v_mfma_f32_16x16x32_bf16 v[188:191],a[140:143],v[12:15],v[188:191]\n"
"buffer_store_dword a156,off,s[24:27],s72 sc1\n"
"v_mfma_f32_16x16x32_bf16 v[156:159],a[140:143],v[4:7],v[156:159]\n"
"v_mfma_f32_16x16x32_bf16 v[156:159],a[136:139],v[0:3],v[156:159]\n"
"s_waitcnt vmcnt(10) lgkmcnt(0)\n"
"s_barrier\n"
"v_mfma_f32_16x16x32_bf16 v[224:227],v[32:35],a[120:123],v[224:227]\n"
"ds_read_b128 a[64:67],v113 offset:57344\n"
"v_mov_b32 v96,0xc1980000\n"
"v_mfma_f32_16x16x32_bf16 v[224:227],v[36:39],a[124:127],v[224:227]\n"
"ds_read_b128 a[68:71],v113 offset:58368\n"
"v_mov_b32 v97,0xc1980000\n"
"v_mfma_f32_16x16x32_bf16 v[228:231],v[44:47],a[124:127],v[228:231]\n"
"ds_read_b128 a[72:75],v113 offset:59392\n"
"v_mov_b32 v98,0xc1980000\n"
"v_mfma_f32_16x16x32_bf16 v[228:231],v[40:43],a[120:123],v[228:231]\n"
"ds_read_b128 a[76:79],v113 offset:60416\n"
"v_mov_b32 v99,0xc1980000\n"
"v_mfma_f32_16x16x32_bf16 v[232:235],v[48:51],a[120:123],v[232:235]\n"
"ds_read_b128 a[80:83],v113 offset:61440\n"
"v_mov_b32 v100,0xc1980000\n"
"v_mfma_f32_16x16x32_bf16 v[232:235],v[52:55],a[124:127],v[232:235]\n"
"ds_read_b128 a[84:87],v113 offset:62464\n"
"v_mov_b32 v101,0xc1980000\n"
"v_mfma_f32_16x16x32_bf16 v[236:239],v[60:63],a[124:127],v[236:239]\n"
"ds_read_b128 a[88:91],v113 offset:63488\n"
"v_mov_b32 v102,0xc1980000\n"
"v_mfma_f32_16x16x32_bf16 v[236:239],v[56:59],a[120:123],v[236:239]\n"
"ds_read_b128 a[92:95],v113 offset:64512\n"
"v_mov_b32 v103,0xc1980000\n"
"v_mfma_f32_16x16x32_bf16 v[240:243],v[64:67],a[120:123],v[240:243]\n"
"v_mov_b32 v104,0xc1980000\n"
"v_mov_b32 v105,0xc1980000\n"
"v_mfma_f32_16x16x32_bf16 v[240:243],v[68:71],a[124:127],v[240:243]\n"
"ds_write_b32 v124,v125\n"
"v_mfma_f32_16x16x32_bf16 v[244:247],v[76:79],a[124:127],v[244:247]\n"
"v_mov_b32 v107,0xc1980000\n"
"v_mov_b32 v108,0xc1980000\n"
"v_mfma_f32_16x16x32_bf16 v[244:247],v[72:75],a[120:123],v[244:247]\n"
"v_mov_b32 v109,0xc1980000\n"
"v_mov_b32 v110,0xc1980000\n"
"v_mfma_f32_16x16x32_bf16 v[248:251],v[80:83],a[120:123],v[248:251]\n"
"v_mov_b32 v106,0xc1980000\n"
"v_mov_b32 v111,0xc1980000\n"
"v_mfma_f32_16x16x32_bf16 v[248:251],v[84:87],a[124:127],v[248:251]\n"
"v_mfma_f32_16x16x32_bf16 v[252:255],v[92:95],a[124:127],v[252:255]\n"
"v_mfma_f32_16x16x32_bf16 v[252:255],v[88:91],a[120:123],v[252:255]\n"
"v_mfma_f32_16x16x32_bf16 v[128:131],v[32:35],a[96:99],v[128:131]\n"
"v_mfma_f32_16x16x32_bf16 v[128:131],v[36:39],a[100:103],v[128:131]\n"
"v_cvt_pk_bf16_f32 v232,v232,v233\n"
"v_cvt_pk_bf16_f32 v233,v234,v235\n"
"v_mfma_f32_16x16x32_bf16 v[132:135],v[44:47],a[100:103],v[132:135]\n"
"v_cvt_pk_bf16_f32 v224,v224,v225\n"
"v_cvt_pk_bf16_f32 v225,v226,v227\n"
"v_mfma_f32_16x16x32_bf16 v[132:135],v[40:43],a[96:99],v[132:135]\n"
"v_cvt_pk_bf16_f32 v226,v228,v229\n"
"v_cvt_pk_bf16_f32 v227,v230,v231\n"
"v_mfma_f32_16x16x32_bf16 v[136:139],v[48:51],a[96:99],v[136:139]\n"
"ds_write_b128 v116,v[224:227] offset:0\n"
"v_mfma_f32_16x16x32_bf16 v[136:139],v[52:55],a[100:103],v[136:139]\n"
"v_cvt_pk_bf16_f32 v234,v236,v237\n"
"v_cvt_pk_bf16_f32 v235,v238,v239\n"
"v_mfma_f32_16x16x32_bf16 v[140:143],v[60:63],a[100:103],v[140:143]\n"
"ds_write_b128 v116,v[232:235] offset:1024\n"
"v_mfma_f32_16x16x32_bf16 v[140:143],v[56:59],a[96:99],v[140:143]\n"
"v_cvt_pk_bf16_f32 v240,v240,v241\n"
"v_cvt_pk_bf16_f32 v241,v242,v243\n"
"v_mfma_f32_16x16x32_bf16 v[144:147],v[64:67],a[96:99],v[144:147]\n"
"v_cvt_pk_bf16_f32 v242,v244,v245\n"
"v_cvt_pk_bf16_f32 v243,v246,v247\n"
"v_mfma_f32_16x16x32_bf16 v[144:147],v[68:71],a[100:103],v[144:147]\n"
"ds_write_b128 v116,v[240:243] offset:2048\n"
"v_cvt_pk_bf16_f32 v248,v248,v249\n"
"v_mfma_f32_16x16x32_bf16 v[148:151],v[76:79],a[100:103],v[148:151]\n"
"v_cvt_pk_bf16_f32 v249,v250,v251\n"
"v_cvt_pk_bf16_f32 v250,v252,v253\n"
"v_cvt_pk_bf16_f32 v251,v254,v255\n"
"v_mfma_f32_16x16x32_bf16 v[148:151],v[72:75],a[96:99],v[148:151]\n"
"ds_write_b128 v116,v[248:251] offset:3072\n"
"v_mfma_f32_16x16x32_bf16 v[152:155],v[80:83],a[96:99],v[152:155]\n"
"ds_read_b128 a[48:51],v116 offset:0\n"
"ds_read_b128 a[52:55],v116 offset:1024\n"
"v_mfma_f32_16x16x32_bf16 v[152:155],v[84:87],a[100:103],v[152:155]\n"
"ds_read_b128 a[56:59],v116 offset:2048\n"
"ds_read_b128 a[60:63],v116 offset:3072\n"
"v_mfma_f32_16x16x32_bf16 v[156:159],v[92:95],a[100:103],v[156:159]\n"
"v_mfma_f32_16x16x32_bf16 v[156:159],v[88:91],a[96:99],v[156:159]\n"
"v_mfma_f32_16x16x32_bf16 v[160:163],v[32:35],a[104:107],v[160:163]\n"
"v_mfma_f32_16x16x32_bf16 v[160:163],v[36:39],a[108:111],v[160:163]\n"
"v_cvt_pk_bf16_f32 v136,v136,v137\n"
"v_cvt_pk_bf16_f32 v137,v138,v139\n"
"v_mfma_f32_16x16x32_bf16 v[164:167],v[44:47],a[108:111],v[164:167]\n"
"v_cvt_pk_bf16_f32 v128,v128,v129\n"
"v_cvt_pk_bf16_f32 v129,v130,v131\n"
"v_mfma_f32_16x16x32_bf16 v[164:167],v[40:43],a[104:107],v[164:167]\n"
"v_cvt_pk_bf16_f32 v130,v132,v133\n"
"v_cvt_pk_bf16_f32 v131,v134,v135\n"
"v_mfma_f32_16x16x32_bf16 v[168:171],v[48:51],a[104:107],v[168:171]\n"
"ds_write_b128 v116,v[128:131] offset:16384\n"
"v_mfma_f32_16x16x32_bf16 v[168:171],v[52:55],a[108:111],v[168:171]\n"
"v_cvt_pk_bf16_f32 v138,v140,v141\n"
"v_cvt_pk_bf16_f32 v139,v142,v143\n"
"v_mfma_f32_16x16x32_bf16 v[172:175],v[60:63],a[108:111],v[172:175]\n"
"ds_write_b128 v116,v[136:139] offset:17408\n"
"v_mfma_f32_16x16x32_bf16 v[172:175],v[56:59],a[104:107],v[172:175]\n"
"v_cvt_pk_bf16_f32 v144,v144,v145\n"
"v_cvt_pk_bf16_f32 v145,v146,v147\n"
"v_mfma_f32_16x16x32_bf16 v[176:179],v[64:67],a[104:107],v[176:179]\n"
"v_cvt_pk_bf16_f32 v146,v148,v149\n"
"v_cvt_pk_bf16_f32 v147,v150,v151\n"
"v_mfma_f32_16x16x32_bf16 v[176:179],v[68:71],a[108:111],v[176:179]\n"
"ds_write_b128 v116,v[144:147] offset:18432\n"
"v_cvt_pk_bf16_f32 v152,v152,v153\n"
"v_mfma_f32_16x16x32_bf16 v[180:183],v[76:79],a[108:111],v[180:183]\n"
"v_cvt_pk_bf16_f32 v153,v154,v155\n"
"v_cvt_pk_bf16_f32 v154,v156,v157\n"
"v_cvt_pk_bf16_f32 v155,v158,v159\n"
"v_mfma_f32_16x16x32_bf16 v[180:183],v[72:75],a[104:107],v[180:183]\n"
"ds_write_b128 v116,v[152:155] offset:19456\n"
"v_mfma_f32_16x16x32_bf16 v[184:187],v[80:83],a[104:107],v[184:187]\n"
"ds_read_b128 a[0:3],v116 offset:16384\n"
"ds_read_b128 a[4:7],v116 offset:17408\n"
"v_mfma_f32_16x16x32_bf16 v[184:187],v[84:87],a[108:111],v[184:187]\n"
"ds_read_b128 a[8:11],v116 offset:18432\n"
"ds_read_b128 a[12:15],v116 offset:19456\n"
"v_mfma_f32_16x16x32_bf16 v[188:191],v[92:95],a[108:111],v[188:191]\n"
"v_mfma_f32_16x16x32_bf16 v[188:191],v[88:91],a[104:107],v[188:191]\n"
"v_mfma_f32_16x16x32_bf16 v[192:195],v[32:35],a[112:115],v[192:195]\n"
"v_mfma_f32_16x16x32_bf16 v[192:195],v[36:39],a[116:119],v[192:195]\n"
"v_cvt_pk_bf16_f32 v168,v168,v169\n"
"v_cvt_pk_bf16_f32 v169,v170,v171\n"
"v_mfma_f32_16x16x32_bf16 v[196:199],v[44:47],a[116:119],v[196:199]\n"
"v_cvt_pk_bf16_f32 v160,v160,v161\n"
"v_cvt_pk_bf16_f32 v161,v162,v163\n"
"v_mfma_f32_16x16x32_bf16 v[196:199],v[40:43],a[112:115],v[196:199]\n"
"v_cvt_pk_bf16_f32 v162,v164,v165\n"
"v_cvt_pk_bf16_f32 v163,v166,v167\n"
"v_mfma_f32_16x16x32_bf16 v[200:203],v[48:51],a[112:115],v[200:203]\n"
"ds_write_b128 v116,v[160:163] offset:49152\n"
"v_mfma_f32_16x16x32_bf16 v[200:203],v[52:55],a[116:119],v[200:203]\n"
"v_cvt_pk_bf16_f32 v170,v172,v173\n"
"v_cvt_pk_bf16_f32 v171,v174,v175\n"
"v_mfma_f32_16x16x32_bf16 v[204:207],v[60:63],a[116:119],v[204:207]\n"
"ds_write_b128 v116,v[168:171] offset:50176\n"
"v_mfma_f32_16x16x32_bf16 v[204:207],v[56:59],a[112:115],v[204:207]\n"
"v_cvt_pk_bf16_f32 v176,v176,v177\n"
"v_cvt_pk_bf16_f32 v177,v178,v179\n"
"v_mfma_f32_16x16x32_bf16 v[208:211],v[64:67],a[112:115],v[208:211]\n"
"v_cvt_pk_bf16_f32 v178,v180,v181\n"
"v_cvt_pk_bf16_f32 v179,v182,v183\n"
"v_mfma_f32_16x16x32_bf16 v[208:211],v[68:71],a[116:119],v[208:211]\n"
"ds_write_b128 v116,v[176:179] offset:51200\n"
"v_cvt_pk_bf16_f32 v184,v184,v185\n"
"v_mfma_f32_16x16x32_bf16 v[212:215],v[76:79],a[116:119],v[212:215]\n"
"v_cvt_pk_bf16_f32 v185,v186,v187\n"
"v_cvt_pk_bf16_f32 v186,v188,v189\n"
"v_cvt_pk_bf16_f32 v187,v190,v191\n"
"v_mfma_f32_16x16x32_bf16 v[212:215],v[72:75],a[112:115],v[212:215]\n"
"ds_write_b128 v116,v[184:187] offset:52224\n"
"v_mfma_f32_16x16x32_bf16 v[216:219],v[80:83],a[112:115],v[216:219]\n"
"ds_read_b128 a[16:19],v116 offset:49152\n"
"ds_read_b128 a[20:23],v116 offset:50176\n"
"v_mfma_f32_16x16x32_bf16 v[216:219],v[84:87],a[116:119],v[216:219]\n"
"ds_read_b128 a[24:27],v116 offset:51200\n"
"ds_read_b128 a[28:31],v116 offset:52224\n"
"v_mfma_f32_16x16x32_bf16 v[220:223],v[92:95],a[116:119],v[220:223]\n"
"s_lshl2_add_u32 s82,s60,s52\n"
"s_lshr_b32 s82,s82,1\n"
"s_cmp_eq_u32 s82,1\n"
"v_mfma_f32_16x16x32_bf16 v[220:223],v[88:91],a[112:115],v[220:223]\n"
"s_cbranch_scc1 .Lfk_head0\n"
"s_waitcnt lgkmcnt(0)\n"
"s_barrier\n"
"v_mfma_f32_16x16x32_bf16 v[24:27],a[64:67],a[48:51],v[108:111]\n"
"s_andn2_b32 m0,s55,16384\n"
"s_add_u32 s54,s54,s22\n"
"v_cvt_pk_bf16_f32 v200,v200,v201\n"
"v_mfma_f32_16x16x32_bf16 v[24:27],a[68:71],a[52:55],v[24:27]\n"
"buffer_load_dwordx4 v115,s[32:35],s54 offen sc1 lds\n"
"v_mfma_f32_16x16x32_bf16 v[24:27],a[72:75],a[56:59],v[24:27]\n"
"v_cvt_pk_bf16_f32 v192,v192,v193\n"
"v_cvt_pk_bf16_f32 v193,v194,v195\n"
"v_mfma_f32_16x16x32_bf16 v[24:27],a[76:79],a[60:63],v[24:27]\n"
"v_cvt_pk_bf16_f32 v194,v196,v197\n"
"v_cvt_pk_bf16_f32 v195,v198,v199\n"
"v_mfma_f32_16x16x32_bf16 v[28:31],a[92:95],a[60:63],v[108:111]\n"
"ds_write_b128 v116,v[192:195] offset:32768\n"
"v_mfma_f32_16x16x32_bf16 v[28:31],a[88:91],a[56:59],v[28:31]\n"
"buffer_store_dwordx4 a[160:163],v112,s[24:27],s3 offen offset:0 sc1\n"
"v_cvt_pk_bf16_f32 v201,v202,v203\n"
"v_mfma_f32_16x16x32_bf16 v[28:31],a[84:87],a[52:55],v[28:31]\n"
"v_cvt_pk_bf16_f32 v202,v204,v205\n"
"v_cvt_pk_bf16_f32 v203,v206,v207\n"
"v_mfma_f32_16x16x32_bf16 v[28:31],a[80:83],a[48:51],v[28:31]\n"
"ds_write_b128 v116,v[200:203] offset:33792\n"
"v_cvt_pk_bf16_f32 v208,v208,v209\n"
"v_mfma_f32_16x16x32_bf16 v[4:7],a[80:83],a[0:3],v[96:99]\n"
"v_cvt_pk_bf16_f32 v209,v210,v211\n"
"v_cvt_pk_bf16_f32 v210,v212,v213\n"
"v_mfma_f32_16x16x32_bf16 v[4:7],a[84:87],a[4:7],v[4:7]\n"
"ds_read_b128 a[96:99],v113 offset:16384\n"
"ds_read_b128 a[100:103],v113 offset:17408\n"
"v_mfma_f32_16x16x32_bf16 v[4:7],a[88:91],a[8:11],v[4:7]\n"
"ds_read_b128 a[104:107],v113 offset:18432\n"
"ds_read_b128 a[108:111],v113 offset:19456\n"
"v_mfma_f32_16x16x32_bf16 v[4:7],a[92:95],a[12:15],v[4:7]\n"
"ds_read_b128 a[112:115],v113 offset:20480\n"
"ds_read_b128 a[116:119],v113 offset:21504\n"
"v_mfma_f32_16x16x32_bf16 v[0:3],a[76:79],a[12:15],v[96:99]\n"
"ds_read_b128 a[120:123],v113 offset:22528\n"
"ds_read_b128 a[124:127],v113 offset:23552\n"
"v_mfma_f32_16x16x32_bf16 v[0:3],a[72:75],a[8:11],v[0:3]\n"
"v_max3_f32 v118,v24,v25,v26\n"
"v_max3_f32 v118,v118,v27,v28\n"
"v_mfma_f32_16x16x32_bf16 v[0:3],a[68:71],a[4:7],v[0:3]\n"
"v_max3_f32 v118,v118,v29,v30\n"
"v_max_f32 v118,v118,v31\n"
"v_mfma_f32_16x16x32_bf16 v[0:3],a[64:67],a[0:3],v[0:3]\n"
"v_mov_b32 v120,v118\n"
"v_cvt_pk_bf16_f32 v211,v214,v215\n"
"v_mfma_f32_16x16x32_bf16 v[8:11],a[64:67],a[16:19],v[100:103]\n"
"buffer_store_dwordx4 a[164:167],v112,s[24:27],s3 offen offset:1024 sc1\n"
"ds_write_b128 v116,v[208:211] offset:34816\n"
"v_mfma_f32_16x16x32_bf16 v[8:11],a[68:71],a[20:23],v[8:11]\n"
"v_cvt_pk_bf16_f32 v216,v216,v217\n"
"v_cvt_pk_bf16_f32 v217,v218,v219\n"
"v_mfma_f32_16x16x32_bf16 v[8:11],a[72:75],a[24:27],v[8:11]\n"
"v_cvt_pk_bf16_f32 v218,v220,v221\n"
"v_cvt_pk_bf16_f32 v219,v222,v223\n"
"v_mfma_f32_16x16x32_bf16 v[8:11],a[76:79],a[28:31],v[8:11]\n"
"v_max3_f32 v119,v0,v1,v2\n"
"v_max3_f32 v119,v119,v3,v4\n"
"v_mfma_f32_16x16x32_bf16 v[12:15],a[92:95],a[28:31],v[100:103]\n"
"v_max3_f32 v119,v119,v5,v6\n"
"v_max_f32 v119,v119,v7\n"
"v_mfma_f32_16x16x32_bf16 v[12:15],a[88:91],a[24:27],v[12:15]\n"
"v_min_f32 v120,v120,v119\n"
"v_max_f32 v118,v118,v119\n"
"v_mfma_f32_16x16x32_bf16 v[12:15],a[84:87],a[20:23],v[12:15]\n"
"ds_write_b128 v116,v[216:219] offset:35840\n"
"ds_read_b128 a[32:35],v116 offset:32768\n"
"v_mfma_f32_16x16x32_bf16 v[12:15],a[80:83],a[16:19],v[12:15]\n"
"ds_read_b128 a[36:39],v116 offset:33792\n"
"ds_read_b128 a[40:43],v116 offset:34816\n"
"ds_read_b128 a[44:47],v116 offset:35840\n"
"s_waitcnt lgkmcnt(0)\n"
"v_mfma_f32_16x16x32_bf16 v[20:23],a[80:83],a[32:35],v[104:107]\n"
"v_mfma_f32_16x16x32_bf16 v[20:23],a[84:87],a[36:39],v[20:23]\n"
"v_mfma_f32_16x16x32_bf16 v[20:23],a[88:91],a[40:43],v[20:23]\n"
"buffer_store_dwordx4 a[224:227],v112,s[24:27],s56 offen offset:0 sc1\n"
"v_mfma_f32_16x16x32_bf16 v[20:23],a[92:95],a[44:47],v[20:23]\n"
"v_max3_f32 v119,v8,v9,v10\n"
"v_mfma_f32_16x16x32_bf16 v[16:19],a[76:79],a[44:47],v[104:107]\n"
"v_max3_f32 v119,v119,v11,v12\n"
"v_max3_f32 v119,v119,v13,v14\n"
"v_mfma_f32_16x16x32_bf16 v[16:19],a[72:75],a[40:43],v[16:19]\n"
"v_max_f32 v119,v119,v15\n"
"v_min_f32 v120,v120,v119\n"
"v_mfma_f32_16x16x32_bf16 v[16:19],a[68:71],a[36:39],v[16:19]\n"
"v_max_f32 v118,v118,v119\n"
"v_mfma_f32_16x16x32_bf16 v[16:19],a[64:67],a[32:35],v[16:19]\n"
"s_nop 7\n"
"v_max3_f32 v119,v16,v17,v18\n"
"v_max3_f32 v119,v119,v19,v20\n"
"v_max3_f32 v119,v119,v21,v22\n"
"v_max_f32 v119,v119,v23\n"
"v_min_f32 v120,v120,v119\n"
"v_max_f32 v118,v118,v119\n"
"v_cmp_gt_f32 s[96:97],s16,v120\n"
"v_cmp_lt_f32 vcc,s93,v118\n"
"s_nop 0\n"
"s_or_b64 s[96:97],s[96:97],vcc\n"
"s_cbranch_scc1 .Lfk_cs_h3\n"
".Lfk_cr_h3:\n"
"s_cmp_ge_u32 s52,2\n"
"s_cbranch_scc1 .Lfk_g0\n"
"s_waitcnt vmcnt(12) lgkmcnt(0)\n"
"s_barrier\n"
"v_mfma_f32_16x16x32_bf16 v[32:35],a[96:99],a[0:3],v[96:99]\n"
"s_add_u32 m0,s55,4096\n"
"v_exp_f32 v0,v0\n"
"v_mfma_f32_16x16x32_bf16 v[32:35],a[100:103],a[4:7],v[32:35]\n"
"s_add_u32 s54,s54,s22\n"
"v_exp_f32 v1,v1\n"
"v_mfma_f32_16x16x32_bf16 v[32:35],a[104:107],a[8:11],v[32:35]\n"
"buffer_load_dwordx4 v115,s[32:35],s54 offen sc1 lds\n"
"v_exp_f32 v2,v2\n"
"v_cvt_pk_bf16_f32 v64,v0,v1\n"
"v_mfma_f32_16x16x32_bf16 v[32:35],a[108:111],a[12:15],v[32:35]\n"
"s_add_u32 m0,s67,16384\n"
"v_exp_f32 v3,v3\n"
"v_mfma_f32_16x16x32_bf16 v[36:39],a[124:127],a[12:15],v[96:99]\n"
"v_exp_f32 v4,v4\n"
"v_cvt_pk_bf16_f32 v65,v2,v3\n"
"v_exp_f32 v5,v5\n"
"v_mfma_f32_16x16x32_bf16 v[36:39],a[120:123],a[8:11],v[36:39]\n"
"buffer_load_dwordx4 v112,s[24:27],s64 offen offset:0 sc1 lds\n"
"v_mfma_f32_16x16x32_bf16 v[36:39],a[116:119],a[4:7],v[36:39]\n"
"v_exp_f32 v6,v6\n"
"v_cvt_pk_bf16_f32 v66,v4,v5\n"
"v_exp_f32 v7,v7\n"
"v_mfma_f32_16x16x32_bf16 v[36:39],a[112:115],a[0:3],v[36:39]\n"
"buffer_load_dwordx4 v112,s[24:27],s64 offen offset:1024 sc1 lds\n"
"v_mfma_f32_16x16x32_bf16 v[44:47],a[112:115],a[16:19],v[100:103]\n"
"v_exp_f32 v8,v8\n"
"v_cvt_pk_bf16_f32 v67,v6,v7\n"
"v_exp_f32 v9,v9\n"
"v_mfma_f32_16x16x32_bf16 v[44:47],a[116:119],a[20:23],v[44:47]\n"
"ds_read_b128 a[64:67],v113 offset:49152\n"
"ds_read_b128 a[68:71],v113 offset:50176\n"
"v_exp_f32 v10,v10\n"
"v_mfma_f32_16x16x32_bf16 v[44:47],a[120:123],a[24:27],v[44:47]\n"
"ds_read_b128 a[72:75],v113 offset:51200\n"
"ds_read_b128 a[76:79],v113 offset:52224\n"
"v_exp_f32 v11,v11\n"
"v_mfma_f32_16x16x32_bf16 v[44:47],a[124:127],a[28:31],v[44:47]\n"
"ds_read_b128 a[80:83],v113 offset:53248\n"
"ds_read_b128 a[84:87],v113 offset:54272\n"
"v_exp_f32 v12,v12\n"
"v_mfma_f32_16x16x32_bf16 v[40:43],a[108:111],a[28:31],v[100:103]\n"
"ds_read_b128 a[88:91],v113 offset:55296\n"
"ds_read_b128 a[92:95],v113 offset:56320\n"
"v_exp_f32 v13,v13\n"
"v_mfma_f32_16x16x32_bf16 v[40:43],a[104:107],a[24:27],v[40:43]\n"
"buffer_load_dwordx4 v112,s[24:27],s64 offen offset:2048 sc1 lds\n"
"v_exp_f32 v14,v14\n"
"v_mfma_f32_16x16x32_bf16 v[40:43],a[100:103],a[20:23],v[40:43]\n"
"buffer_load_dwordx4 v112,s[24:27],s64 offen offset:3072 sc1 lds\n"
"v_mfma_f32_16x16x32_bf16 v[40:43],a[96:99],a[16:19],v[40:43]\n"
"s_add_u32 m0,s67,20480\n"
"v_exp_f32 v15,v15\n"
"v_mfma_f32_16x16x32_bf16 v[48:51],a[96:99],a[32:35],v[104:107]\n"
"buffer_load_dwordx4 v117,s[24:27],s64 offen offset:0 sc1 lds\n"
"buffer_load_dwordx4 v117,s[24:27],s64 offen offset:1024 sc1 lds\n"
"v_exp_f32 v16,v16\n"
"v_exp_f32 v17,v17\n"
"v_mfma_f32_16x16x32_bf16 v[48:51],a[100:103],a[36:39],v[48:51]\n"
"buffer_load_dwordx4 v117,s[24:27],s64 offen offset:2048 sc1 lds\n"
"v_mfma_f32_16x16x32_bf16 v[48:51],a[104:107],a[40:43],v[48:51]\n"
"v_exp_f32 v18,v18\n"
"v_cvt_pk_bf16_f32 v68,v8,v9\n"
"v_cvt_pk_bf16_f32 v69,v10,v11\n"
"v_exp_f32 v19,v19\n"
"v_mfma_f32_16x16x32_bf16 v[48:51],a[108:111],a[44:47],v[48:51]\n"
"buffer_load_dwordx4 v117,s[24:27],s64 offen offset:3072 sc1 lds\n"
"v_mfma_f32_16x16x32_bf16 v[52:55],a[124:127],a[44:47],v[104:107]\n"
"s_add_u32 s64,s64,16384\n"
"v_exp_f32 v20,v20\n"
"v_cvt_pk_bf16_f32 v70,v12,v13\n"
"v_cvt_pk_bf16_f32 v71,v14,v15\n"
"v_mfma_f32_16x16x32_bf16 v[52:55],a[120:123],a[40:43],v[52:55]\n"
"v_exp_f32 v21,v21\n"
"v_cvt_pk_bf16_f32 v72,v16,v17\n"
"v_cvt_pk_bf16_f32 v73,v18,v19\n"
"v_mfma_f32_16x16x32_bf16 v[52:55],a[116:119],a[36:39],v[52:55]\n"
"v_exp_f32 v22,v22\n"
"v_cvt_pk_bf16_f32 v74,v20,v21\n"
"v_mfma_f32_16x16x32_bf16 v[52:55],a[112:115],a[32:35],v[52:55]\n"
"v_exp_f32 v23,v23\n"
"v_mfma_f32_16x16x32_bf16 v[60:63],a[112:115],a[48:51],v[108:111]\n"
"v_exp_f32 v24,v24\n"
"v_cvt_pk_bf16_f32 v75,v22,v23\n"
"v_mfma_f32_16x16x32_bf16 v[60:63],a[116:119],a[52:55],v[60:63]\n"
"v_exp_f32 v25,v25\n"
"v_mfma_f32_16x16x32_bf16 v[60:63],a[120:123],a[56:59],v[60:63]\n"
"v_exp_f32 v26,v26\n"
"v_exp_f32 v27,v27\n"
"v_mfma_f32_16x16x32_bf16 v[60:63],a[124:127],a[60:63],v[60:63]\n"
"buffer_store_dwordx4 a[228:231],v112,s[24:27],s56 offen offset:1024 sc1\n"
"v_mfma_f32_16x16x32_bf16 v[56:59],a[108:111],a[60:63],v[108:111]\n"
"v_exp_f32 v28,v28\n"
"v_cvt_pk_bf16_f32 v77,v26,v27\n"
"v_mfma_f32_16x16x32_bf16 v[56:59],a[104:107],a[56:59],v[56:59]\n"
"v_exp_f32 v29,v29\n"
"v_mfma_f32_16x16x32_bf16 v[56:59],a[100:103],a[52:55],v[56:59]\n"
"v_exp_f32 v30,v30\n"
"v_cvt_pk_bf16_f32 v78,v28,v29\n"
"v_mfma_f32_16x16x32_bf16 v[56:59],a[96:99],a[48:51],v[56:59]\n"
"s_waitcnt vmcnt(18) lgkmcnt(0)\n"
"v_exp_f32 v31,v31\n"
"v_cvt_pk_bf16_f32 v76,v24,v25\n"
"v_cvt_pk_bf16_f32 v79,v30,v31\n"
"s_barrier\n"
"v_mfma_f32_16x16x32_bf16 v[0:3],a[64:67],a[0:3],v[96:99]\n"
"s_add_u32 m0,s55,8192\n"
"v_exp_f32 v122,v32\n"
"v_mfma_f32_16x16x32_bf16 v[0:3],a[68:71],a[4:7],v[0:3]\n"
"s_add_u32 s54,s54,s22\n"
"v_exp_f32 v123,v33\n"
"v_mfma_f32_16x16x32_bf16 v[0:3],a[72:75],a[8:11],v[0:3]\n"
"ds_read_b128 a[96:99],v114 offset:40960\n"
"ds_read_b128 a[100:103],v114 offset:41984\n"
"v_mfma_f32_16x16x32_bf16 v[0:3],a[76:79],a[12:15],v[0:3]\n"
"v_exp_f32 v119,v34\n"
"v_mfma_f32_16x16x32_bf16 v[4:7],a[92:95],a[12:15],v[96:99]\n"
"ds_read_b128 a[104:107],v114 offset:43008\n"
"v_cvt_pk_bf16_f32 v80,v122,v123\n"
"v_mfma_f32_16x16x32_bf16 v[4:7],a[88:91],a[8:11],v[4:7]\n"
"v_exp_f32 v120,v35\n"
"v_mfma_f32_16x16x32_bf16 v[4:7],a[84:87],a[4:7],v[4:7]\n"
"ds_read_b128 a[108:111],v114 offset:44032\n"
"ds_read_b128 a[112:115],v114 offset:45056\n"
"v_mfma_f32_16x16x32_bf16 v[4:7],a[80:83],a[0:3],v[4:7]\n"
"v_exp_f32 v122,v36\n"
"v_mfma_f32_16x16x32_bf16 v[12:15],a[80:83],a[16:19],v[100:103]\n"
"ds_read_b128 a[116:119],v114 offset:46080\n"
"v_cvt_pk_bf16_f32 v81,v119,v120\n"
"v_mfma_f32_16x16x32_bf16 v[12:15],a[84:87],a[20:23],v[12:15]\n"
"v_exp_f32 v123,v37\n"
"v_mfma_f32_16x16x32_bf16 v[12:15],a[88:91],a[24:27],v[12:15]\n"
"ds_read_b128 a[120:123],v114 offset:47104\n"
"ds_read_b128 a[124:127],v114 offset:48128\n"
"v_mfma_f32_16x16x32_bf16 v[12:15],a[92:95],a[28:31],v[12:15]\n"
"v_exp_f32 v119,v38\n"
"v_cvt_pk_bf16_f32 v82,v122,v123\n"
"v_mfma_f32_16x16x32_bf16 v[8:11],a[76:79],a[28:31],v[100:103]\n"
"buffer_store_dwordx4 a[168:171],v112,s[24:27],s3 offen offset:2048 sc1\n"
"v_mfma_f32_16x16x32_bf16 v[8:11],a[72:75],a[24:27],v[8:11]\n"
"v_exp_f32 v120,v39\n"
"v_mfma_f32_16x16x32_bf16 v[8:11],a[68:71],a[20:23],v[8:11]\n"
"buffer_load_dwordx4 v115,s[32:35],s54 offen sc1 lds\n"
"v_mfma_f32_16x16x32_bf16 v[8:11],a[64:67],a[16:19],v[8:11]\n"
"s_add_u32 m0,s67,49152\n"
"v_exp_f32 v122,v40\n"
"v_mfma_f32_16x16x32_bf16 v[16:19],a[64:67],a[32:35],v[104:107]\n"
"v_cvt_pk_bf16_f32 v83,v119,v120\n"
"v_or3_b32 v118,v80,v81,v82\n"
"v_mfma_f32_16x16x32_bf16 v[16:19],a[68:71],a[36:39],v[16:19]\n"
"v_exp_f32 v123,v41\n"
"v_mfma_f32_16x16x32_bf16 v[16:19],a[72:75],a[40:43],v[16:19]\n"
"buffer_load_dwordx4 v112,s[24:27],s64 offen offset:0 sc1 lds\n"
"v_mfma_f32_16x16x32_bf16 v[16:19],a[76:79],a[44:47],v[16:19]\n"
"v_exp_f32 v119,v42\n"
"v_mfma_f32_16x16x32_bf16 v[20:23],a[92:95],a[44:47],v[104:107]\n"
"v_cvt_pk_bf16_f32 v84,v122,v123\n"
"v_or3_b32 v118,v118,v83,v84\n"
"v_mfma_f32_16x16x32_bf16 v[20:23],a[88:91],a[40:43],v[20:23]\n"
"v_exp_f32 v120,v43\n"
"v_mfma_f32_16x16x32_bf16 v[20:23],a[84:87],a[36:39],v[20:23]\n"
"buffer_load_dwordx4 v112,s[24:27],s64 offen offset:1024 sc1 lds\n"
"v_mfma_f32_16x16x32_bf16 v[20:23],a[80:83],a[32:35],v[20:23]\n"
"v_exp_f32 v122,v44\n"
"v_mfma_f32_16x16x32_bf16 v[28:31],a[80:83],a[48:51],v[108:111]\n"
"v_cvt_pk_bf16_f32 v85,v119,v120\n"
"v_mfma_f32_16x16x32_bf16 v[28:31],a[84:87],a[52:55],v[28:31]\n"
"v_exp_f32 v123,v45\n"
"v_mfma_f32_16x16x32_bf16 v[28:31],a[88:91],a[56:59],v[28:31]\n"
"buffer_load_dwordx4 v112,s[24:27],s64 offen offset:2048 sc1 lds\n"
"v_mfma_f32_16x16x32_bf16 v[28:31],a[92:95],a[60:63],v[28:31]\n"
"v_exp_f32 v119,v46\n"
"v_cvt_pk_bf16_f32 v86,v122,v123\n"
"v_mfma_f32_16x16x32_bf16 v[24:27],a[76:79],a[60:63],v[108:111]\n"
"buffer_store_dwordx4 a[172:175],v112,s[24:27],s3 offen offset:3072 sc1\n"
"v_mfma_f32_16x16x32_bf16 v[24:27],a[72:75],a[56:59],v[24:27]\n"
"v_exp_f32 v120,v47\n"
"v_mfma_f32_16x16x32_bf16 v[24:27],a[68:71],a[52:55],v[24:27]\n"
"buffer_load_dwordx4 v112,s[24:27],s64 offen offset:3072 sc1 lds\n"
"v_mfma_f32_16x16x32_bf16 v[24:27],a[64:67],a[48:51],v[24:27]\n"
"s_waitcnt lgkmcnt(0)\n"
"v_exp_f32 v122,v48\n"
"v_mfma_f32_16x16x32_bf16 v[128:131],a[96:99],v[64:67],0\n"
"v_exp_f32 v123,v49\n"
"v_mfma_f32_16x16x32_bf16 v[160:163],a[96:99],v[68:71],0\n"
"ds_read_b128 a[64:67],v113 offset:32768\n"
"v_cvt_pk_bf16_f32 v87,v119,v120\n"
"v_mfma_f32_16x16x32_bf16 v[192:195],a[96:99],v[72:75],0\n"
"s_add_u32 m0,s67,53248\n"
"v_exp_f32 v119,v50\n"
"v_mfma_f32_16x16x32_bf16 v[224:227],a[96:99],v[76:79],0\n"
"ds_read_b128 a[68:71],v113 offset:33792\n"
"v_cvt_pk_bf16_f32 v88,v122,v123\n"
"v_mfma_f32_16x16x32_bf16 a[140:143],a[144:147],v[76:79],0\n"
"v_exp_f32 v120,v51\n"
"v_mfma_f32_16x16x32_bf16 v[228:231],a[100:103],v[76:79],0\n"
"ds_read_b128 a[72:75],v113 offset:34816\n"
"ds_read_b128 a[76:79],v113 offset:35840\n"
"v_mfma_f32_16x16x32_bf16 v[132:135],a[100:103],v[64:67],0\n"
"v_exp_f32 v122,v52\n"
"v_mfma_f32_16x16x32_bf16 v[164:167],a[100:103],v[68:71],0\n"
"ds_read_b128 a[80:83],v113 offset:36864\n"
"v_cvt_pk_bf16_f32 v89,v119,v120\n"
"v_mfma_f32_16x16x32_bf16 v[196:199],a[100:103],v[72:75],0\n"
"v_exp_f32 v123,v53\n"
"v_mfma_f32_16x16x32_bf16 a[136:139],a[144:147],v[72:75],0\n"
"ds_read_b128 a[84:87],v113 offset:37888\n"
"ds_read_b128 a[88:91],v113 offset:38912\n"
"v_mfma_f32_16x16x32_bf16 v[200:203],a[104:107],v[72:75],0\n"
"v_exp_f32 v119,v54\n"
"v_mfma_f32_16x16x32_bf16 v[232:235],a[104:107],v[76:79],0\n"
"ds_read_b128 a[92:95],v113 offset:39936\n"
"v_cvt_pk_bf16_f32 v90,v122,v123\n"
"v_mfma_f32_16x16x32_bf16 v[136:139],a[104:107],v[64:67],0\n"
"v_exp_f32 v120,v55\n"
"v_mfma_f32_16x16x32_bf16 v[168:171],a[104:107],v[68:71],0\n"
"buffer_load_dwordx4 v117,s[24:27],s64 offen offset:0 sc1 lds\n"
"v_mfma_f32_16x16x32_bf16 a[132:135],a[144:147],v[68:71],0\n"
"v_exp_f32 v122,v56\n"
"v_mfma_f32_16x16x32_bf16 v[172:175],a[108:111],v[68:71],0\n"
"v_cvt_pk_bf16_f32 v91,v119,v120\n"
"s_sub_u32 s3,s3,16384\n"
"v_mfma_f32_16x16x32_bf16 v[204:207],a[108:111],v[72:75],0\n"
"v_exp_f32 v123,v57\n"
"v_mfma_f32_16x16x32_bf16 v[236:239],a[108:111],v[76:79],0\n"
"buffer_load_dwordx4 v117,s[24:27],s64 offen offset:1024 sc1 lds\n"
"v_mfma_f32_16x16x32_bf16 v[140:143],a[108:111],v[64:67],0\n"
"v_exp_f32 v119,v58\n"
"v_cvt_pk_bf16_f32 v92,v122,v123\n"
"v_mfma_f32_16x16x32_bf16 a[128:131],a[144:147],v[64:67],0\n"
"buffer_store_dwordx4 a[176:179],v112,s[24:27],s3 offen offset:0 sc1\n"
"v_mfma_f32_16x16x32_bf16 v[144:147],a[112:115],v[64:67],0\n"
"v_exp_f32 v120,v59\n"
"v_mfma_f32_16x16x32_bf16 v[176:179],a[112:115],v[68:71],0\n"
"buffer_load_dwordx4 v117,s[24:27],s64 offen offset:2048 sc1 lds\n"
"v_mfma_f32_16x16x32_bf16 v[208:211],a[112:115],v[72:75],0\n"
"v_exp_f32 v122,v60\n"
"v_mfma_f32_16x16x32_bf16 v[240:243],a[112:115],v[76:79],0\n"
"v_cvt_pk_bf16_f32 v93,v119,v120\n"
"v_or3_b32 v118,v118,v85,v86\n"
"v_mfma_f32_16x16x32_bf16 v[244:247],a[116:119],v[76:79],0\n"
"v_exp_f32 v123,v61\n"
"v_mfma_f32_16x16x32_bf16 v[148:151],a[116:119],v[64:67],0\n"
"buffer_load_dwordx4 v117,s[24:27],s64 offen offset:3072 sc1 lds\n"
"v_mfma_f32_16x16x32_bf16 v[180:183],a[116:119],v[68:71],0\n"
"s_add_u32 s64,s64,16384\n"
"v_exp_f32 v119,v62\n"
"v_cvt_pk_bf16_f32 v94,v122,v123\n"
"v_mfma_f32_16x16x32_bf16 v[212:215],a[116:119],v[72:75],0\n"
"buffer_store_dwordx4 a[232:235],v112,s[24:27],s56 offen offset:2048 sc1\n"
"v_mfma_f32_16x16x32_bf16 v[216:219],a[120:123],v[72:75],0\n"
"v_exp_f32 v120,v63\n"
"v_mfma_f32_16x16x32_bf16 v[248:251],a[120:123],v[76:79],0\n"
"v_cvt_pk_bf16_f32 v95,v119,v120\n"
"v_or3_b32 v118,v118,v87,v88\n"
"v_mfma_f32_16x16x32_bf16 v[152:155],a[120:123],v[64:67],0\n"
"v_or3_b32 v118,v118,v89,v90\n"
"v_or3_b32 v118,v118,v91,v92\n"
"v_mfma_f32_16x16x32_bf16 v[184:187],a[120:123],v[68:71],0\n"
"v_or3_b32 v118,v118,v93,v94\n"
"v_or_b32 v118,v118,v95\n"
"v_mfma_f32_16x16x32_bf16 v[188:191],a[124:127],v[68:71],0\n"
"v_and_b32 v118,0x40004000,v118\n"
"v_cmp_ne_u32 vcc,0,v118\n"
"v_mfma_f32_16x16x32_bf16 v[220:223],a[124:127],v[72:75],0\n"
"v_mfma_f32_16x16x32_bf16 v[252:255],a[124:127],v[76:79],0\n"
"v_mfma_f32_16x16x32_bf16 v[156:159],a[124:127],v[64:67],0\n"
"s_cbranch_vccnz .Lfk_cs_h1\n"
".Lfk_cr_h1:\n"
"s_waitcnt lgkmcnt(0)\n"
"s_barrier\n"
"v_mfma_f32_16x16x32_bf16 v[32:35],a[64:67],a[0:3],v[96:99]\n"
"s_add_u32 m0,s55,12288\n"
"v_exp_f32 v122,v0\n"
"v_mfma_f32_16x16x32_bf16 v[32:35],a[68:71],a[4:7],v[32:35]\n"
"s_add_u32 s54,s54,s22\n"
"v_exp_f32 v123,v1\n"
"v_mfma_f32_16x16x32_bf16 v[32:35],a[72:75],a[8:11],v[32:35]\n"
"ds_read_b128 a[96:99],v114 offset:8192\n"
"ds_read_b128 a[100:103],v114 offset:9216\n"
"v_mfma_f32_16x16x32_bf16 v[32:35],a[76:79],a[12:15],v[32:35]\n"
"v_exp_f32 v119,v2\n"
"v_mfma_f32_16x16x32_bf16 v[36:39],a[92:95],a[12:15],v[96:99]\n"
"ds_read_b128 a[104:107],v114 offset:10240\n"
"v_cvt_pk_bf16_f32 v64,v122,v123\n"
"v_mfma_f32_16x16x32_bf16 v[36:39],a[88:91],a[8:11],v[36:39]\n"
"v_exp_f32 v120,v3\n"
"v_mfma_f32_16x16x32_bf16 v[36:39],a[84:87],a[4:7],v[36:39]\n"
"ds_read_b128 a[108:111],v114 offset:11264\n"
"ds_read_b128 a[112:115],v114 offset:12288\n"
"v_mfma_f32_16x16x32_bf16 v[36:39],a[80:83],a[0:3],v[36:39]\n"
"v_exp_f32 v122,v4\n"
"v_mfma_f32_16x16x32_bf16 v[44:47],a[80:83],a[16:19],v[100:103]\n"
"ds_read_b128 a[116:119],v114 offset:13312\n"
"v_cvt_pk_bf16_f32 v65,v119,v120\n"
"v_mfma_f32_16x16x32_bf16 v[44:47],a[84:87],a[20:23],v[44:47]\n"
"v_exp_f32 v123,v5\n"
"v_mfma_f32_16x16x32_bf16 v[44:47],a[88:91],a[24:27],v[44:47]\n"
"ds_read_b128 a[120:123],v114 offset:14336\n"
"ds_read_b128 a[124:127],v114 offset:15360\n"
"v_mfma_f32_16x16x32_bf16 v[44:47],a[92:95],a[28:31],v[44:47]\n"
"v_exp_f32 v119,v6\n"
"v_cvt_pk_bf16_f32 v66,v122,v123\n"
"v_mfma_f32_16x16x32_bf16 v[40:43],a[76:79],a[28:31],v[100:103]\n"
"buffer_store_dwordx4 a[180:183],v112,s[24:27],s3 offen offset:1024 sc1\n"
"v_mfma_f32_16x16x32_bf16 v[40:43],a[72:75],a[24:27],v[40:43]\n"
"v_exp_f32 v120,v7\n"
"v_mfma_f32_16x16x32_bf16 v[40:43],a[68:71],a[20:23],v[40:43]\n"
"buffer_load_dwordx4 v115,s[32:35],s54 offen sc1 lds\n"
"v_mfma_f32_16x16x32_bf16 v[40:43],a[64:67],a[16:19],v[40:43]\n"
"s_add_u32 m0,s67,32768\n"
"v_exp_f32 v122,v8\n"
"v_mfma_f32_16x16x32_bf16 v[48:51],a[64:67],a[32:35],v[104:107]\n"
"v_cvt_pk_bf16_f32 v67,v119,v120\n"
"v_or3_b32 v118,v64,v65,v66\n"
"v_mfma_f32_16x16x32_bf16 v[48:51],a[68:71],a[36:39],v[48:51]\n"
"v_exp_f32 v123,v9\n"
"v_mfma_f32_16x16x32_bf16 v[48:51],a[72:75],a[40:43],v[48:51]\n"
"buffer_load_dwordx4 v112,s[24:27],s64 offen offset:0 sc1 lds\n"
"v_mfma_f32_16x16x32_bf16 v[48:51],a[76:79],a[44:47],v[48:51]\n"
"v_exp_f32 v119,v10\n"
"v_mfma_f32_16x16x32_bf16 v[52:55],a[92:95],a[44:47],v[104:107]\n"
"v_cvt_pk_bf16_f32 v68,v122,v123\n"
"v_or3_b32 v118,v118,v67,v68\n"
"v_mfma_f32_16x16x32_bf16 v[52:55],a[88:91],a[40:43],v[52:55]\n"
"v_exp_f32 v120,v11\n"
"v_mfma_f32_16x16x32_bf16 v[52:55],a[84:87],a[36:39],v[52:55]\n"
"buffer_load_dwordx4 v112,s[24:27],s64 offen offset:1024 sc1 lds\n"
"v_mfma_f32_16x16x32_bf16 v[52:55],a[80:83],a[32:35],v[52:55]\n"
"v_exp_f32 v122,v12\n"
"v_mfma_f32_16x16x32_bf16 v[60:63],a[80:83],a[48:51],v[108:111]\n"
"v_cvt_pk_bf16_f32 v69,v119,v120\n"
"v_mfma_f32_16x16x32_bf16 v[60:63],a[84:87],a[52:55],v[60:63]\n"
"v_exp_f32 v123,v13\n"
"v_mfma_f32_16x16x32_bf16 v[60:63],a[88:91],a[56:59],v[60:63]\n"
"buffer_load_dwordx4 v112,s[24:27],s64 offen offset:2048 sc1 lds\n"
"v_mfma_f32_16x16x32_bf16 v[60:63],a[92:95],a[60:63],v[60:63]\n"
"v_exp_f32 v119,v14\n"
"v_cvt_pk_bf16_f32 v70,v122,v123\n"
"v_mfma_f32_16x16x32_bf16 v[56:59],a[76:79],a[60:63],v[108:111]\n"
"buffer_store_dwordx4 a[184:187],v112,s[24:27],s3 offen offset:2048 sc1\n"
"v_mfma_f32_16x16x32_bf16 v[56:59],a[72:75],a[56:59],v[56:59]\n"
"v_exp_f32 v120,v15\n"
"v_mfma_f32_16x16x32_bf16 v[56:59],a[68:71],a[52:55],v[56:59]\n"
"buffer_load_dwordx4 v112,s[24:27],s64 offen offset:3072 sc1 lds\n"
"v_mfma_f32_16x16x32_bf16 v[56:59],a[64:67],a[48:51],v[56:59]\n"
"s_waitcnt lgkmcnt(0)\n"
"v_exp_f32 v122,v16\n"
"v_mfma_f32_16x16x32_bf16 v[128:131],a[96:99],v[80:83],v[128:131]\n"
"v_exp_f32 v123,v17\n"
"v_mfma_f32_16x16x32_bf16 v[160:163],a[96:99],v[84:87],v[160:163]\n"
"ds_read_b128 a[64:67],v113 offset:0\n"
"v_cvt_pk_bf16_f32 v71,v119,v120\n"
"v_mfma_f32_16x16x32_bf16 v[192:195],a[96:99],v[88:91],v[192:195]\n"
"s_add_u32 m0,s67,36864\n"
"v_exp_f32 v119,v18\n"
"v_mfma_f32_16x16x32_bf16 v[224:227],a[96:99],v[92:95],v[224:227]\n"
"ds_read_b128 a[68:71],v113 offset:1024\n"
"v_cvt_pk_bf16_f32 v72,v122,v123\n"
"v_mfma_f32_16x16x32_bf16 a[140:143],a[144:147],v[92:95],a[140:143]\n"
"v_exp_f32 v120,v19\n"
"v_mfma_f32_16x16x32_bf16 v[228:231],a[100:103],v[92:95],v[228:231]\n"
"ds_read_b128 a[72:75],v113 offset:2048\n"
"ds_read_b128 a[76:79],v113 offset:3072\n"
"v_mfma_f32_16x16x32_bf16 v[132:135],a[100:103],v[80:83],v[132:135]\n"
"v_exp_f32 v122,v20\n"
"v_mfma_f32_16x16x32_bf16 v[164:167],a[100:103],v[84:87],v[164:167]\n"
"ds_read_b128 a[80:83],v113 offset:4096\n"
"v_cvt_pk_bf16_f32 v73,v119,v120\n"
"v_mfma_f32_16x16x32_bf16 v[196:199],a[100:103],v[88:91],v[196:199]\n"
"v_exp_f32 v123,v21\n"
"v_mfma_f32_16x16x32_bf16 a[136:139],a[144:147],v[88:91],a[136:139]\n"
"ds_read_b128 a[84:87],v113 offset:5120\n"
"ds_read_b128 a[88:91],v113 offset:6144\n"
"v_mfma_f32_16x16x32_bf16 v[200:203],a[104:107],v[88:91],v[200:203]\n"
"v_exp_f32 v119,v22\n"
"v_mfma_f32_16x16x32_bf16 v[232:235],a[104:107],v[92:95],v[232:235]\n"
"ds_read_b128 a[92:95],v113 offset:7168\n"
"v_cvt_pk_bf16_f32 v74,v122,v123\n"
"v_mfma_f32_16x16x32_bf16 v[136:139],a[104:107],v[80:83],v[136:139]\n"
"v_exp_f32 v120,v23\n"
"v_mfma_f32_16x16x32_bf16 v[168:171],a[104:107],v[84:87],v[168:171]\n"
"buffer_load_dwordx4 v117,s[24:27],s64 offen offset:0 sc1 lds\n"
"v_mfma_f32_16x16x32_bf16 a[132:135],a[144:147],v[84:87],a[132:135]\n"
"v_exp_f32 v122,v24\n"
"v_cvt_pk_bf16_f32 v75,v119,v120\n"
"v_mfma_f32_16x16x32_bf16 v[172:175],a[108:111],v[84:87],v[172:175]\n"
"buffer_store_dwordx4 a[188:191],v112,s[24:27],s3 offen offset:3072 sc1\n"
"v_mfma_f32_16x16x32_bf16 v[204:207],a[108:111],v[88:91],v[204:207]\n"
"v_exp_f32 v123,v25\n"
"v_mfma_f32_16x16x32_bf16 v[236:239],a[108:111],v[92:95],v[236:239]\n"
"buffer_load_dwordx4 v117,s[24:27],s64 offen offset:1024 sc1 lds\n"
"v_mfma_f32_16x16x32_bf16 v[140:143],a[108:111],v[80:83],v[140:143]\n"
"v_exp_f32 v119,v26\n"
"v_mfma_f32_16x16x32_bf16 a[128:131],a[144:147],v[80:83],a[128:131]\n"
"v_cvt_pk_bf16_f32 v76,v122,v123\n"
"s_or_b32 s3,s3,0x3c000\n"
"v_mfma_f32_16x16x32_bf16 v[144:147],a[112:115],v[80:83],v[144:147]\n"
"v_exp_f32 v120,v27\n"
"v_mfma_f32_16x16x32_bf16 v[176:179],a[112:115],v[84:87],v[176:179]\n"
"buffer_load_dwordx4 v117,s[24:27],s64 offen offset:2048 sc1 lds\n"
"v_mfma_f32_16x16x32_bf16 v[208:211],a[112:115],v[88:91],v[208:211]\n"
"v_exp_f32 v122,v28\n"
"v_mfma_f32_16x16x32_bf16 v[240:243],a[112:115],v[92:95],v[240:243]\n"
"v_cvt_pk_bf16_f32 v77,v119,v120\n"
"v_or3_b32 v118,v118,v69,v70\n"
"v_mfma_f32_16x16x32_bf16 v[244:247],a[116:119],v[92:95],v[244:247]\n"
"v_exp_f32 v123,v29\n"
"v_mfma_f32_16x16x32_bf16 v[148:151],a[116:119],v[80:83],v[148:151]\n"
"buffer_load_dwordx4 v117,s[24:27],s64 offen offset:3072 sc1 lds\n"
"v_mfma_f32_16x16x32_bf16 v[180:183],a[116:119],v[84:87],v[180:183]\n"
"s_add_u32 s64,s64,16384\n"
"v_exp_f32 v119,v30\n"
"v_cvt_pk_bf16_f32 v78,v122,v123\n"
"v_mfma_f32_16x16x32_bf16 v[212:215],a[116:119],v[88:91],v[212:215]\n"
"buffer_store_dwordx4 a[236:239],v112,s[24:27],s56 offen offset:3072 sc1\n"
"v_mfma_f32_16x16x32_bf16 v[216:219],a[120:123],v[88:91],v[216:219]\n"
"v_exp_f32 v120,v31\n"
"v_mfma_f32_16x16x32_bf16 v[248:251],a[120:123],v[92:95],v[248:251]\n"
"v_cvt_pk_bf16_f32 v79,v119,v120\n"
"v_or3_b32 v118,v118,v71,v72\n"
"v_mfma_f32_16x16x32_bf16 v[152:155],a[120:123],v[80:83],v[152:155]\n"
"v_or3_b32 v118,v118,v73,v74\n"
"v_or3_b32 v118,v118,v75,v76\n"
"v_mfma_f32_16x16x32_bf16 v[184:187],a[120:123],v[84:87],v[184:187]\n"
"v_or3_b32 v118,v118,v77,v78\n"
"v_or_b32 v118,v118,v79\n"
"v_mfma_f32_16x16x32_bf16 v[188:191],a[124:127],v[84:87],v[188:191]\n"
"v_and_b32 v118,0x40004000,v118\n"
"v_cmp_ne_u32 vcc,0,v118\n"
"v_mfma_f32_16x16x32_bf16 v[220:223],a[124:127],v[88:91],v[220:223]\n"
"v_mfma_f32_16x16x32_bf16 v[252:255],a[124:127],v[92:95],v[252:255]\n"
"v_mfma_f32_16x16x32_bf16 v[156:159],a[124:127],v[80:83],v[156:159]\n"
"s_cbranch_vccnz .Lfk_cs_h2\n"
".Lfk_cr_h2:\n"
"s_waitcnt vmcnt(27) lgkmcnt(0)\n"
"s_barrier\n"
".Lfk_g1:\n"
"s_and_b32 s82,s78,4096\n"
"s_lshl1_add_u32 s82,s82,69632\n"
"s_cmp_lt_u32 s52,2\n"
"s_cselect_b32 s64,s64,s62\n"
"s_cselect_b32 s67,s67,s82\n"
"s_add_u32 s89,s89,1\n"
"s_cmp_ge_u32 s89,s60\n"
"s_cbranch_scc1 .Lfk_Fx\n"
".Lfk_F:\n"
"v_mfma_f32_16x16x32_bf16 v[0:3],a[64:67],a[0:3],v[96:99]\n"
"s_swappc_b64 s[94:95],s[20:21]\n"
"v_exp_f32 v122,v32\n"
"v_mfma_f32_16x16x32_bf16 v[0:3],a[68:71],a[4:7],v[0:3]\n"
"v_exp_f32 v123,v33\n"
"v_mfma_f32_16x16x32_bf16 v[0:3],a[72:75],a[8:11],v[0:3]\n"
"ds_read_b128 a[96:99],v114 offset:24576\n"
"ds_read_b128 a[100:103],v114 offset:25600\n"
"v_mfma_f32_16x16x32_bf16 v[0:3],a[76:79],a[12:15],v[0:3]\n"
"v_exp_f32 v119,v34\n"
"v_mfma_f32_16x16x32_bf16 v[4:7],a[92:95],a[12:15],v[96:99]\n"
"s_add_u32 s101,s101,4\n"
"ds_read_b128 a[104:107],v114 offset:26624\n"
"v_cvt_pk_bf16_f32 v80,v122,v123\n"
"v_mfma_f32_16x16x32_bf16 v[4:7],a[88:91],a[8:11],v[4:7]\n"
"v_exp_f32 v120,v35\n"
"v_mfma_f32_16x16x32_bf16 v[4:7],a[84:87],a[4:7],v[4:7]\n"
"ds_read_b128 a[108:111],v114 offset:27648\n"
"ds_read_b128 a[112:115],v114 offset:28672\n"
"v_mfma_f32_16x16x32_bf16 v[4:7],a[80:83],a[0:3],v[4:7]\n"
"v_exp_f32 v122,v36\n"
"v_mfma_f32_16x16x32_bf16 v[12:15],a[80:83],a[16:19],v[100:103]\n"
"ds_read_b128 a[116:119],v114 offset:29696\n"
"v_cvt_pk_bf16_f32 v81,v119,v120\n"
"v_mfma_f32_16x16x32_bf16 v[12:15],a[84:87],a[20:23],v[12:15]\n"
"v_exp_f32 v123,v37\n"
"v_mfma_f32_16x16x32_bf16 v[12:15],a[88:91],a[24:27],v[12:15]\n"
"ds_read_b128 a[120:123],v114 offset:30720\n"
"ds_read_b128 a[124:127],v114 offset:31744\n"
"v_mfma_f32_16x16x32_bf16 v[12:15],a[92:95],a[28:31],v[12:15]\n"
"s_add_u32 m0,s67,0\n"
"v_exp_f32 v119,v38\n"
"v_mfma_f32_16x16x32_bf16 v[8:11],a[76:79],a[28:31],v[100:103]\n"
"v_cvt_pk_bf16_f32 v82,v122,v123\n"
"v_or3_b32 v118,v80,v81,v82\n"
"v_mfma_f32_16x16x32_bf16 v[8:11],a[72:75],a[24:27],v[8:11]\n"
"v_exp_f32 v120,v39\n"
"v_mfma_f32_16x16x32_bf16 v[8:11],a[68:71],a[20:23],v[8:11]\n"
"buffer_load_dwordx4 v112,s[24:27],s64 offen offset:0 sc1 lds\n"
"v_mfma_f32_16x16x32_bf16 v[8:11],a[64:67],a[16:19],v[8:11]\n"
"v_exp_f32 v122,v40\n"
"v_mfma_f32_16x16x32_bf16 v[16:19],a[64:67],a[32:35],v[104:107]\n"
"v_cvt_pk_bf16_f32 v83,v119,v120\n"
"v_mfma_f32_16x16x32_bf16 v[16:19],a[68:71],a[36:39],v[16:19]\n"
"v_exp_f32 v123,v41\n"
"v_mfma_f32_16x16x32_bf16 v[16:19],a[72:75],a[40:43],v[16:19]\n"
"buffer_load_dwordx4 v112,s[24:27],s64 offen offset:1024 sc1 lds\n"
"v_mfma_f32_16x16x32_bf16 v[16:19],a[76:79],a[44:47],v[16:19]\n"
"v_exp_f32 v119,v42\n"
"v_mfma_f32_16x16x32_bf16 v[20:23],a[92:95],a[44:47],v[104:107]\n"
"v_cvt_pk_bf16_f32 v84,v122,v123\n"
"v_or3_b32 v118,v118,v83,v84\n"
"v_mfma_f32_16x16x32_bf16 v[20:23],a[88:91],a[40:43],v[20:23]\n"
"v_exp_f32 v120,v43\n"
"v_mfma_f32_16x16x32_bf16 v[20:23],a[84:87],a[36:39],v[20:23]\n"
"v_cvt_pk_bf16_f32 v85,v119,v120\n"
"v_mfma_f32_16x16x32_bf16 v[20:23],a[80:83],a[32:35],v[20:23]\n"
"v_exp_f32 v122,v44\n"
"v_mfma_f32_16x16x32_bf16 v[28:31],a[80:83],a[48:51],v[108:111]\n"
"buffer_load_dwordx4 v112,s[24:27],s64 offen offset:2048 sc1 lds\n"
"v_mfma_f32_16x16x32_bf16 v[28:31],a[84:87],a[52:55],v[28:31]\n"
"v_exp_f32 v123,v45\n"
"v_mfma_f32_16x16x32_bf16 v[28:31],a[88:91],a[56:59],v[28:31]\n"
"v_cvt_pk_bf16_f32 v86,v122,v123\n"
"v_or3_b32 v118,v118,v85,v86\n"
"v_mfma_f32_16x16x32_bf16 v[28:31],a[92:95],a[60:63],v[28:31]\n"
"v_exp_f32 v119,v46\n"
"v_mfma_f32_16x16x32_bf16 v[24:27],a[76:79],a[60:63],v[108:111]\n"
"v_exp_f32 v122,v48\n"
"v_mfma_f32_16x16x32_bf16 v[24:27],a[72:75],a[56:59],v[24:27]\n"
"v_exp_f32 v120,v47\n"
"v_mfma_f32_16x16x32_bf16 v[24:27],a[68:71],a[52:55],v[24:27]\n"
"buffer_load_dwordx4 v112,s[24:27],s64 offen offset:3072 sc1 lds\n"
"v_mfma_f32_16x16x32_bf16 v[24:27],a[64:67],a[48:51],v[24:27]\n"
"s_add_u32 s64,s64,16384\n"
"s_waitcnt lgkmcnt(0)\n"
"v_mfma_f32_16x16x32_bf16 v[128:131],a[96:99],v[64:67],v[128:131]\n"
"s_andn2_b32 m0,s55,16384\n"
"v_exp_f32 v123,v49\n"
"v_mfma_f32_16x16x32_bf16 v[160:163],a[96:99],v[68:71],v[160:163]\n"
"ds_read_b128 a[64:67],v113 offset:16384\n"
"v_cvt_pk_bf16_f32 v87,v119,v120\n"
"v_mfma_f32_16x16x32_bf16 v[192:195],a[96:99],v[72:75],v[192:195]\n"
"s_add_u32 s54,s54,s22\n"
"v_exp_f32 v119,v50\n"
"v_mfma_f32_16x16x32_bf16 v[224:227],a[96:99],v[76:79],v[224:227]\n"
"ds_read_b128 a[68:71],v113 offset:17408\n"
"v_cvt_pk_bf16_f32 v88,v122,v123\n"
"v_mfma_f32_16x16x32_bf16 a[140:143],a[144:147],v[76:79],a[140:143]\n"
"v_exp_f32 v120,v51\n"
"v_mfma_f32_16x16x32_bf16 v[228:231],a[100:103],v[76:79],v[228:231]\n"
"ds_read_b128 a[72:75],v113 offset:18432\n"
"ds_read_b128 a[76:79],v113 offset:19456\n"
"v_mfma_f32_16x16x32_bf16 v[132:135],a[100:103],v[64:67],v[132:135]\n"
"v_exp_f32 v122,v52\n"
"v_mfma_f32_16x16x32_bf16 v[164:167],a[100:103],v[68:71],v[164:167]\n"
"ds_read_b128 a[80:83],v113 offset:20480\n"
"v_cvt_pk_bf16_f32 v89,v119,v120\n"
"v_mfma_f32_16x16x32_bf16 v[196:199],a[100:103],v[72:75],v[196:199]\n"
"v_exp_f32 v123,v53\n"
"v_mfma_f32_16x16x32_bf16 a[136:139],a[144:147],v[72:75],a[136:139]\n"
"ds_read_b128 a[84:87],v113 offset:21504\n"
"ds_read_b128 a[88:91],v113 offset:22528\n"
"v_mfma_f32_16x16x32_bf16 v[200:203],a[104:107],v[72:75],v[200:203]\n"
"v_exp_f32 v119,v54\n"
"v_mfma_f32_16x16x32_bf16 v[232:235],a[104:107],v[76:79],v[232:235]\n"
"ds_read_b128 a[92:95],v113 offset:23552\n"
"v_cvt_pk_bf16_f32 v90,v122,v123\n"
"v_mfma_f32_16x16x32_bf16 v[136:139],a[104:107],v[64:67],v[136:139]\n"
"v_exp_f32 v120,v55\n"
"v_mfma_f32_16x16x32_bf16 v[168:171],a[104:107],v[68:71],v[168:171]\n"
"s_waitcnt vmcnt(11)\n"
"ds_add_u32 v124,a152\n"
"v_mfma_f32_16x16x32_bf16 a[132:135],a[144:147],v[68:71],a[132:135]\n"
"v_exp_f32 v122,v56\n"
"v_mfma_f32_16x16x32_bf16 v[172:175],a[108:111],v[68:71],v[172:175]\n"
"v_cvt_pk_bf16_f32 v91,v119,v120\n"
"v_or3_b32 v118,v118,v87,v88\n"
"v_mfma_f32_16x16x32_bf16 v[204:207],a[108:111],v[72:75],v[204:207]\n"
"v_exp_f32 v123,v57\n"
"v_mfma_f32_16x16x32_bf16 v[236:239],a[108:111],v[76:79],v[236:239]\n"
"buffer_load_dwordx4 v115,s[32:35],s54 offen sc1 lds\n"
"v_mfma_f32_16x16x32_bf16 v[140:143],a[108:111],v[64:67],v[140:143]\n"
"v_exp_f32 v119,v58\n"
"v_mfma_f32_16x16x32_bf16 a[128:131],a[144:147],v[64:67],a[128:131]\n"
"v_cvt_pk_bf16_f32 v92,v122,v123\n"
"v_or3_b32 v118,v118,v89,v90\n"
"v_mfma_f32_16x16x32_bf16 v[144:147],a[112:115],v[64:67],v[144:147]\n"
"v_exp_f32 v120,v59\n"
"v_mfma_f32_16x16x32_bf16 v[176:179],a[112:115],v[68:71],v[176:179]\n"
"v_cvt_pk_bf16_f32 v93,v119,v120\n"
"v_or3_b32 v118,v118,v91,v92\n"
"v_mfma_f32_16x16x32_bf16 v[208:211],a[112:115],v[72:75],v[208:211]\n"
"v_exp_f32 v122,v60\n"
"v_mfma_f32_16x16x32_bf16 v[240:243],a[112:115],v[76:79],v[240:243]\n"
"v_mfma_f32_16x16x32_bf16 v[244:247],a[116:119],v[76:79],v[244:247]\n"
"v_exp_f32 v123,v61\n"
"v_mfma_f32_16x16x32_bf16 v[148:151],a[116:119],v[64:67],v[148:151]\n"
"v_cvt_pk_bf16_f32 v94,v122,v123\n"
"v_or3_b32 v118,v118,v93,v94\n"
"v_mfma_f32_16x16x32_bf16 v[180:183],a[116:119],v[68:71],v[180:183]\n"
"v_exp_f32 v119,v62\n"
"v_mfma_f32_16x16x32_bf16 v[212:215],a[116:119],v[72:75],v[212:215]\n"
"ds_read_b32 v125,v124\n"
"buffer_store_dwordx4 a[240:243],v112,s[24:27],s56 offen offset:0 sc1\n"
"v_mfma_f32_16x16x32_bf16 v[216:219],a[120:123],v[72:75],v[216:219]\n"
"v_exp_f32 v120,v63\n"
"v_mfma_f32_16x16x32_bf16 v[248:251],a[120:123],v[76:79],v[248:251]\n"
"v_cvt_pk_bf16_f32 v95,v119,v120\n"
"v_mfma_f32_16x16x32_bf16 v[152:155],a[120:123],v[64:67],v[152:155]\n"
"v_mfma_f32_16x16x32_bf16 v[184:187],a[120:123],v[68:71],v[184:187]\n"
"v_mfma_f32_16x16x32_bf16 v[188:191],a[124:127],v[68:71],v[188:191]\n"
"s_waitcnt lgkmcnt(0)\n"
"v_subrev_u32 v125,s101,v125\n"
"v_or3_b32 v125,v118,v95,v125\n"
"v_mfma_f32_16x16x32_bf16 v[220:223],a[124:127],v[72:75],v[220:223]\n"
"v_and_b32 v125,0x40004000,v125\n"
"v_cmp_ne_u32 vcc,0,v125\n"
"v_mfma_f32_16x16x32_bf16 v[252:255],a[124:127],v[76:79],v[252:255]\n"
"v_mfma_f32_16x16x32_bf16 v[156:159],a[124:127],v[64:67],v[156:159]\n"
"s_cbranch_vccnz .Lfk_cs_f3\n"
".Lfk_cr_f3:\n"
"v_mfma_f32_16x16x32_bf16 v[32:35],a[64:67],a[0:3],v[96:99]\n"
"s_add_u32 s101,s101,4\n"
"v_exp_f32 v122,v0\n"
"v_mfma_f32_16x16x32_bf16 v[32:35],a[68:71],a[4:7],v[32:35]\n"
"s_add_u32 m0,s55,4096\n"
"v_exp_f32 v123,v1\n"
"v_mfma_f32_16x16x32_bf16 v[32:35],a[72:75],a[8:11],v[32:35]\n"
"ds_read_b128 a[96:99],v114 offset:57344\n"
"ds_read_b128 a[100:103],v114 offset:58368\n"
"v_mfma_f32_16x16x32_bf16 v[32:35],a[76:79],a[12:15],v[32:35]\n"
"s_add_u32 s54,s54,s22\n"
"v_exp_f32 v119,v2\n"
"v_mfma_f32_16x16x32_bf16 v[36:39],a[92:95],a[12:15],v[96:99]\n"
"ds_read_b128 a[104:107],v114 offset:59392\n"
"v_cvt_pk_bf16_f32 v64,v122,v123\n"
"v_mfma_f32_16x16x32_bf16 v[36:39],a[88:91],a[8:11],v[36:39]\n"
"v_exp_f32 v120,v3\n"
"v_mfma_f32_16x16x32_bf16 v[36:39],a[84:87],a[4:7],v[36:39]\n"
"ds_read_b128 a[108:111],v114 offset:60416\n"
"ds_read_b128 a[112:115],v114 offset:61440\n"
"v_mfma_f32_16x16x32_bf16 v[36:39],a[80:83],a[0:3],v[36:39]\n"
"v_exp_f32 v122,v4\n"
"v_mfma_f32_16x16x32_bf16 v[44:47],a[80:83],a[16:19],v[100:103]\n"
"ds_read_b128 a[116:119],v114 offset:62464\n"
"v_cvt_pk_bf16_f32 v65,v119,v120\n"
"v_mfma_f32_16x16x32_bf16 v[44:47],a[84:87],a[20:23],v[44:47]\n"
"v_exp_f32 v123,v5\n"
"v_mfma_f32_16x16x32_bf16 v[44:47],a[88:91],a[24:27],v[44:47]\n"
"ds_read_b128 a[120:123],v114 offset:63488\n"
"ds_read_b128 a[124:127],v114 offset:64512\n"
"v_mfma_f32_16x16x32_bf16 v[44:47],a[92:95],a[28:31],v[44:47]\n"
"v_exp_f32 v119,v6\n"
"v_mfma_f32_16x16x32_bf16 v[40:43],a[76:79],a[28:31],v[100:103]\n"
"v_cvt_pk_bf16_f32 v66,v122,v123\n"
"v_or3_b32 v118,v64,v65,v66\n"
"v_mfma_f32_16x16x32_bf16 v[40:43],a[72:75],a[24:27],v[40:43]\n"
"v_exp_f32 v120,v7\n"
"v_mfma_f32_16x16x32_bf16 v[40:43],a[68:71],a[20:23],v[40:43]\n"
"buffer_load_dwordx4 v115,s[32:35],s54 offen sc1 lds\n"
"v_mfma_f32_16x16x32_bf16 v[40:43],a[64:67],a[16:19],v[40:43]\n"
"s_add_u32 m0,s67,16384\n"
"v_exp_f32 v122,v8\n"
"v_mfma_f32_16x16x32_bf16 v[48:51],a[64:67],a[32:35],v[104:107]\n"
"v_cvt_pk_bf16_f32 v67,v119,v120\n"
"v_mfma_f32_16x16x32_bf16 v[48:51],a[68:71],a[36:39],v[48:51]\n"
"v_exp_f32 v123,v9\n"
"v_mfma_f32_16x16x32_bf16 v[48:51],a[72:75],a[40:43],v[48:51]\n"
"buffer_load_dwordx4 v112,s[24:27],s64 offen offset:0 sc1 lds\n"
"v_mfma_f32_16x16x32_bf16 v[48:51],a[76:79],a[44:47],v[48:51]\n"
"v_exp_f32 v119,v10\n"
"v_mfma_f32_16x16x32_bf16 v[52:55],a[92:95],a[44:47],v[104:107]\n"
"v_cvt_pk_bf16_f32 v68,v122,v123\n"
"v_or3_b32 v118,v118,v67,v68\n"
"v_mfma_f32_16x16x32_bf16 v[52:55],a[88:91],a[40:43],v[52:55]\n"
"v_exp_f32 v120,v11\n"
"v_mfma_f32_16x16x32_bf16 v[52:55],a[84:87],a[36:39],v[52:55]\n"
"buffer_load_dwordx4 v112,s[24:27],s64 offen offset:1024 sc1 lds\n"
"v_mfma_f32_16x16x32_bf16 v[52:55],a[80:83],a[32:35],v[52:55]\n"
"v_exp_f32 v122,v12\n"
"v_mfma_f32_16x16x32_bf16 v[60:63],a[80:83],a[48:51],v[108:111]\n"
"v_cvt_pk_bf16_f32 v69,v119,v120\n"
"v_mfma_f32_16x16x32_bf16 v[60:63],a[84:87],a[52:55],v[60:63]\n"
"v_exp_f32 v123,v13\n"
"v_mfma_f32_16x16x32_bf16 v[60:63],a[88:91],a[56:59],v[60:63]\n"
"buffer_load_dwordx4 v112,s[24:27],s64 offen offset:2048 sc1 lds\n"
"v_mfma_f32_16x16x32_bf16 v[60:63],a[92:95],a[60:63],v[60:63]\n"
"v_exp_f32 v119,v14\n"
"v_mfma_f32_16x16x32_bf16 v[56:59],a[76:79],a[60:63],v[108:111]\n"
"v_cvt_pk_bf16_f32 v70,v122,v123\n"
"v_or3_b32 v118,v118,v69,v70\n"
"v_mfma_f32_16x16x32_bf16 v[56:59],a[72:75],a[56:59],v[56:59]\n"
"v_exp_f32 v120,v15\n"
"v_mfma_f32_16x16x32_bf16 v[56:59],a[68:71],a[52:55],v[56:59]\n"
"buffer_load_dwordx4 v112,s[24:27],s64 offen offset:3072 sc1 lds\n"
"v_mfma_f32_16x16x32_bf16 v[56:59],a[64:67],a[48:51],v[56:59]\n"
"s_waitcnt lgkmcnt(0)\n"
"v_exp_f32 v122,v16\n"
"v_mfma_f32_16x16x32_bf16 v[128:131],a[96:99],v[80:83],v[128:131]\n"
"v_exp_f32 v123,v17\n"
"v_mfma_f32_16x16x32_bf16 v[160:163],a[96:99],v[84:87],v[160:163]\n"
"s_add_u32 s64,s64,16384\n"
"ds_read_b128 a[64:67],v113 offset:49152\n"
"v_cvt_pk_bf16_f32 v71,v119,v120\n"
"v_mfma_f32_16x16x32_bf16 v[192:195],a[96:99],v[88:91],v[192:195]\n"
"v_exp_f32 v119,v18\n"
"v_mfma_f32_16x16x32_bf16 v[224:227],a[96:99],v[92:95],v[224:227]\n"
"ds_read_b128 a[68:71],v113 offset:50176\n"
"v_cvt_pk_bf16_f32 v72,v122,v123\n"
"v_mfma_f32_16x16x32_bf16 a[140:143],a[144:147],v[92:95],a[140:143]\n"
"v_exp_f32 v120,v19\n"
"v_mfma_f32_16x16x32_bf16 v[228:231],a[100:103],v[92:95],v[228:231]\n"
"ds_read_b128 a[72:75],v113 offset:51200\n"
"ds_read_b128 a[76:79],v113 offset:52224\n"
"v_mfma_f32_16x16x32_bf16 v[132:135],a[100:103],v[80:83],v[132:135]\n"
"v_exp_f32 v122,v20\n"
"v_mfma_f32_16x16x32_bf16 v[164:167],a[100:103],v[84:87],v[164:167]\n"
"ds_read_b128 a[80:83],v113 offset:53248\n"
"v_cvt_pk_bf16_f32 v73,v119,v120\n"
"v_mfma_f32_16x16x32_bf16 v[196:199],a[100:103],v[88:91],v[196:199]\n"
"v_exp_f32 v123,v21\n"
"v_mfma_f32_16x16x32_bf16 a[136:139],a[144:147],v[88:91],a[136:139]\n"
"ds_read_b128 a[84:87],v113 offset:54272\n"
"ds_read_b128 a[88:91],v113 offset:55296\n"
"v_mfma_f32_16x16x32_bf16 v[200:203],a[104:107],v[88:91],v[200:203]\n"
"v_exp_f32 v119,v22\n"
"v_mfma_f32_16x16x32_bf16 v[232:235],a[104:107],v[92:95],v[232:235]\n"
"ds_read_b128 a[92:95],v113 offset:56320\n"
"v_cvt_pk_bf16_f32 v74,v122,v123\n"
"v_mfma_f32_16x16x32_bf16 v[136:139],a[104:107],v[80:83],v[136:139]\n"
"v_exp_f32 v120,v23\n"
"v_mfma_f32_16x16x32_bf16 v[168:171],a[104:107],v[84:87],v[168:171]\n"
"s_waitcnt vmcnt(12)\n"
"ds_add_u32 v124,a152\n"
"v_mfma_f32_16x16x32_bf16 a[132:135],a[144:147],v[84:87],a[132:135]\n"
"v_exp_f32 v122,v24\n"
"v_mfma_f32_16x16x32_bf16 v[172:175],a[108:111],v[84:87],v[172:175]\n"
"v_cvt_pk_bf16_f32 v75,v119,v120\n"
"v_or3_b32 v118,v118,v71,v72\n"
"v_mfma_f32_16x16x32_bf16 v[204:207],a[108:111],v[88:91],v[204:207]\n"
"v_exp_f32 v123,v25\n"
"v_mfma_f32_16x16x32_bf16 v[236:239],a[108:111],v[92:95],v[236:239]\n"
"v_cvt_pk_bf16_f32 v76,v122,v123\n"
"v_or3_b32 v118,v118,v73,v74\n"
"v_mfma_f32_16x16x32_bf16 v[140:143],a[108:111],v[80:83],v[140:143]\n"
"v_exp_f32 v119,v26\n"
"v_mfma_f32_16x16x32_bf16 a[128:131],a[144:147],v[80:83],a[128:131]\n"
"v_or3_b32 v118,v118,v75,v76\n"
"v_mfma_f32_16x16x32_bf16 v[144:147],a[112:115],v[80:83],v[144:147]\n"
"v_exp_f32 v120,v27\n"
"v_mfma_f32_16x16x32_bf16 v[176:179],a[112:115],v[84:87],v[176:179]\n"
"v_cvt_pk_bf16_f32 v77,v119,v120\n"
"v_mfma_f32_16x16x32_bf16 v[208:211],a[112:115],v[88:91],v[208:211]\n"
"v_exp_f32 v122,v28\n"
"v_mfma_f32_16x16x32_bf16 v[240:243],a[112:115],v[92:95],v[240:243]\n"
"v_mfma_f32_16x16x32_bf16 v[244:247],a[116:119],v[92:95],v[244:247]\n"
"v_exp_f32 v123,v29\n"
"v_mfma_f32_16x16x32_bf16 v[148:151],a[116:119],v[80:83],v[148:151]\n"
"v_cvt_pk_bf16_f32 v78,v122,v123\n"
"v_or3_b32 v118,v118,v77,v78\n"
"v_mfma_f32_16x16x32_bf16 v[180:183],a[116:119],v[84:87],v[180:183]\n"
"v_exp_f32 v119,v30\n"
"v_mfma_f32_16x16x32_bf16 v[212:215],a[116:119],v[88:91],v[212:215]\n"
"ds_read_b32 v125,v124\n"
"buffer_store_dwordx4 a[244:247],v112,s[24:27],s56 offen offset:1024 sc1\n"
"v_mfma_f32_16x16x32_bf16 v[216:219],a[120:123],v[88:91],v[216:219]\n"
"v_exp_f32 v120,v31\n"
"v_mfma_f32_16x16x32_bf16 v[248:251],a[120:123],v[92:95],v[248:251]\n"
"v_cvt_pk_bf16_f32 v79,v119,v120\n"
"v_mfma_f32_16x16x32_bf16 v[152:155],a[120:123],v[80:83],v[152:155]\n"
"v_mfma_f32_16x16x32_bf16 v[184:187],a[120:123],v[84:87],v[184:187]\n"
"v_mfma_f32_16x16x32_bf16 v[188:191],a[124:127],v[84:87],v[188:191]\n"
"s_waitcnt lgkmcnt(0)\n"
"v_subrev_u32 v125,s101,v125\n"
"v_or3_b32 v125,v118,v79,v125\n"
"v_mfma_f32_16x16x32_bf16 v[220:223],a[124:127],v[88:91],v[220:223]\n"
"v_and_b32 v125,0x40004000,v125\n"
"v_cmp_ne_u32 vcc,0,v125\n"
"v_mfma_f32_16x16x32_bf16 v[252:255],a[124:127],v[92:95],v[252:255]\n"
"v_mfma_f32_16x16x32_bf16 v[156:159],a[124:127],v[80:83],v[156:159]\n"
"s_cbranch_vccnz .Lfk_cs_f0\n"
".Lfk_cr_f0:\n"
"v_mfma_f32_16x16x32_bf16 v[0:3],a[64:67],a[0:3],v[96:99]\n"
"s_add_u32 s101,s101,4\n"
"v_exp_f32 v122,v32\n"
"v_mfma_f32_16x16x32_bf16 v[0:3],a[68:71],a[4:7],v[0:3]\n"
"s_add_u32 m0,s55,8192\n"
"v_exp_f32 v123,v33\n"
"v_mfma_f32_16x16x32_bf16 v[0:3],a[72:75],a[8:11],v[0:3]\n"
"ds_read_b128 a[96:99],v114 offset:40960\n"
"ds_read_b128 a[100:103],v114 offset:41984\n"
"v_mfma_f32_16x16x32_bf16 v[0:3],a[76:79],a[12:15],v[0:3]\n"
"s_add_u32 s54,s54,s22\n"
"v_exp_f32 v119,v34\n"
"v_mfma_f32_16x16x32_bf16 v[4:7],a[92:95],a[12:15],v[96:99]\n"
"ds_read_b128 a[104:107],v114 offset:43008\n"
"v_cvt_pk_bf16_f32 v80,v122,v123\n"
"v_mfma_f32_16x16x32_bf16 v[4:7],a[88:91],a[8:11],v[4:7]\n"
"v_exp_f32 v120,v35\n"
"v_mfma_f32_16x16x32_bf16 v[4:7],a[84:87],a[4:7],v[4:7]\n"
"ds_read_b128 a[108:111],v114 offset:44032\n"
"ds_read_b128 a[112:115],v114 offset:45056\n"
"v_mfma_f32_16x16x32_bf16 v[4:7],a[80:83],a[0:3],v[4:7]\n"
"v_exp_f32 v122,v36\n"
"v_mfma_f32_16x16x32_bf16 v[12:15],a[80:83],a[16:19],v[100:103]\n"
"ds_read_b128 a[116:119],v114 offset:46080\n"
"v_cvt_pk_bf16_f32 v81,v119,v120\n"
"v_mfma_f32_16x16x32_bf16 v[12:15],a[84:87],a[20:23],v[12:15]\n"
"v_exp_f32 v123,v37\n"
"v_mfma_f32_16x16x32_bf16 v[12:15],a[88:91],a[24:27],v[12:15]\n"
"ds_read_b128 a[120:123],v114 offset:47104\n"
"ds_read_b128 a[124:127],v114 offset:48128\n"
"v_mfma_f32_16x16x32_bf16 v[12:15],a[92:95],a[28:31],v[12:15]\n"
"v_exp_f32 v119,v38\n"
"v_mfma_f32_16x16x32_bf16 v[8:11],a[76:79],a[28:31],v[100:103]\n"
"v_cvt_pk_bf16_f32 v82,v122,v123\n"
"v_or3_b32 v118,v80,v81,v82\n"
"v_mfma_f32_16x16x32_bf16 v[8:11],a[72:75],a[24:27],v[8:11]\n"
"v_exp_f32 v120,v39\n"
"v_mfma_f32_16x16x32_bf16 v[8:11],a[68:71],a[20:23],v[8:11]\n"
"buffer_load_dwordx4 v115,s[32:35],s54 offen sc1 lds\n"
"v_mfma_f32_16x16x32_bf16 v[8:11],a[64:67],a[16:19],v[8:11]\n"
"s_add_u32 m0,s67,49152\n"
"v_exp_f32 v122,v40\n"
"v_mfma_f32_16x16x32_bf16 v[16:19],a[64:67],a[32:35],v[104:107]\n"
"v_cvt_pk_bf16_f32 v83,v119,v120\n"
"v_mfma_f32_16x16x32_bf16 v[16:19],a[68:71],a[36:39],v[16:19]\n"
"v_exp_f32 v123,v41\n"
"v_mfma_f32_16x16x32_bf16 v[16:19],a[72:75],a[40:43],v[16:19]\n"
"buffer_load_dwordx4 v112,s[24:27],s64 offen offset:0 sc1 lds\n"
"v_mfma_f32_16x16x32_bf16 v[16:19],a[76:79],a[44:47],v[16:19]\n"
"v_exp_f32 v119,v42\n"
"v_mfma_f32_16x16x32_bf16 v[20:23],a[92:95],a[44:47],v[104:107]\n"
"v_cvt_pk_bf16_f32 v84,v122,v123\n"
"v_or3_b32 v118,v118,v83,v84\n"
"v_mfma_f32_16x16x32_bf16 v[20:23],a[88:91],a[40:43],v[20:23]\n"
"v_exp_f32 v120,v43\n"
"v_mfma_f32_16x16x32_bf16 v[20:23],a[84:87],a[36:39],v[20:23]\n"
"buffer_load_dwordx4 v112,s[24:27],s64 offen offset:1024 sc1 lds\n"
"v_mfma_f32_16x16x32_bf16 v[20:23],a[80:83],a[32:35],v[20:23]\n"
"v_exp_f32 v122,v44\n"
"v_mfma_f32_16x16x32_bf16 v[28:31],a[80:83],a[48:51],v[108:111]\n"
"v_cvt_pk_bf16_f32 v85,v119,v120\n"
"v_mfma_f32_16x16x32_bf16 v[28:31],a[84:87],a[52:55],v[28:31]\n"
"v_exp_f32 v123,v45\n"
"v_mfma_f32_16x16x32_bf16 v[28:31],a[88:91],a[56:59],v[28:31]\n"
"buffer_load_dwordx4 v112,s[24:27],s64 offen offset:2048 sc1 lds\n"
"v_mfma_f32_16x16x32_bf16 v[28:31],a[92:95],a[60:63],v[28:31]\n"
"v_exp_f32 v119,v46\n"
"v_mfma_f32_16x16x32_bf16 v[24:27],a[76:79],a[60:63],v[108:111]\n"
"v_cvt_pk_bf16_f32 v86,v122,v123\n"
"v_or3_b32 v118,v118,v85,v86\n"
"v_mfma_f32_16x16x32_bf16 v[24:27],a[72:75],a[56:59],v[24:27]\n"
"v_exp_f32 v120,v47\n"
"v_mfma_f32_16x16x32_bf16 v[24:27],a[68:71],a[52:55],v[24:27]\n"
"buffer_load_dwordx4 v112,s[24:27],s64 offen offset:3072 sc1 lds\n"
"v_mfma_f32_16x16x32_bf16 v[24:27],a[64:67],a[48:51],v[24:27]\n"
"s_waitcnt lgkmcnt(0)\n"
"v_exp_f32 v122,v48\n"
"v_mfma_f32_16x16x32_bf16 v[128:131],a[96:99],v[64:67],v[128:131]\n"
"v_exp_f32 v123,v49\n"
"v_mfma_f32_16x16x32_bf16 v[160:163],a[96:99],v[68:71],v[160:163]\n"
"s_add_u32 s64,s64,16384\n"
"ds_read_b128 a[64:67],v113 offset:32768\n"
"v_cvt_pk_bf16_f32 v87,v119,v120\n"
"v_mfma_f32_16x16x32_bf16 v[192:195],a[96:99],v[72:75],v[192:195]\n"
"v_exp_f32 v119,v50\n"
"v_mfma_f32_16x16x32_bf16 v[224:227],a[96:99],v[76:79],v[224:227]\n"
"ds_read_b128 a[68:71],v113 offset:33792\n"
"v_cvt_pk_bf16_f32 v88,v122,v123\n"
"v_mfma_f32_16x16x32_bf16 a[140:143],a[144:147],v[76:79],a[140:143]\n"
"v_exp_f32 v120,v51\n"
"v_mfma_f32_16x16x32_bf16 v[228:231],a[100:103],v[76:79],v[228:231]\n"
"ds_read_b128 a[72:75],v113 offset:34816\n"
"ds_read_b128 a[76:79],v113 offset:35840\n"
"v_mfma_f32_16x16x32_bf16 v[132:135],a[100:103],v[64:67],v[132:135]\n"
"v_exp_f32 v122,v52\n"
"v_mfma_f32_16x16x32_bf16 v[164:167],a[100:103],v[68:71],v[164:167]\n"
"ds_read_b128 a[80:83],v113 offset:36864\n"
"v_cvt_pk_bf16_f32 v89,v119,v120\n"
"v_mfma_f32_16x16x32_bf16 v[196:199],a[100:103],v[72:75],v[196:199]\n"
"v_exp_f32 v123,v53\n"
"v_mfma_f32_16x16x32_bf16 a[136:139],a[144:147],v[72:75],a[136:139]\n"
"ds_read_b128 a[84:87],v113 offset:37888\n"
"ds_read_b128 a[88:91],v113 offset:38912\n"
"v_mfma_f32_16x16x32_bf16 v[200:203],a[104:107],v[72:75],v[200:203]\n"
"v_exp_f32 v119,v54\n"
"v_mfma_f32_16x16x32_bf16 v[232:235],a[104:107],v[76:79],v[232:235]\n"
"ds_read_b128 a[92:95],v113 offset:39936\n"
"v_cvt_pk_bf16_f32 v90,v122,v123\n"
"v_mfma_f32_16x16x32_bf16 v[136:139],a[104:107],v[64:67],v[136:139]\n"
"v_exp_f32 v120,v55\n"
"v_mfma_f32_16x16x32_bf16 v[168:171],a[104:107],v[68:71],v[168:171]\n"
"s_waitcnt vmcnt(12)\n"
"ds_add_u32 v124,a152\n"
"v_mfma_f32_16x16x32_bf16 a[132:135],a[144:147],v[68:71],a[132:135]\n"
"v_exp_f32 v122,v56\n"
"v_mfma_f32_16x16x32_bf16 v[172:175],a[108:111],v[68:71],v[172:175]\n"
"v_cvt_pk_bf16_f32 v91,v119,v120\n"
"v_or3_b32 v118,v118,v87,v88\n"
"v_mfma_f32_16x16x32_bf16 v[204:207],a[108:111],v[72:75],v[204:207]\n"
"v_exp_f32 v123,v57\n"
"v_mfma_f32_16x16x32_bf16 v[236:239],a[108:111],v[76:79],v[236:239]\n"
"v_cvt_pk_bf16_f32 v92,v122,v123\n"
"v_or3_b32 v118,v118,v89,v90\n"
"v_mfma_f32_16x16x32_bf16 v[140:143],a[108:111],v[64:67],v[140:143]\n"
"v_exp_f32 v119,v58\n"
"v_mfma_f32_16x16x32_bf16 a[128:131],a[144:147],v[64:67],a[128:131]\n"
"v_or3_b32 v118,v118,v91,v92\n"
"v_mfma_f32_16x16x32_bf16 v[144:147],a[112:115],v[64:67],v[144:147]\n"
"v_exp_f32 v120,v59\n"
"v_mfma_f32_16x16x32_bf16 v[176:179],a[112:115],v[68:71],v[176:179]\n"
"v_cvt_pk_bf16_f32 v93,v119,v120\n"
"v_mfma_f32_16x16x32_bf16 v[208:211],a[112:115],v[72:75],v[208:211]\n"
"v_exp_f32 v122,v60\n"
"v_mfma_f32_16x16x32_bf16 v[240:243],a[112:115],v[76:79],v[240:243]\n"
"v_mfma_f32_16x16x32_bf16 v[244:247],a[116:119],v[76:79],v[244:247]\n"
"v_exp_f32 v123,v61\n"
"v_mfma_f32_16x16x32_bf16 v[148:151],a[116:119],v[64:67],v[148:151]\n"
"v_cvt_pk_bf16_f32 v94,v122,v123\n"
"v_or3_b32 v118,v118,v93,v94\n"
"v_mfma_f32_16x16x32_bf16 v[180:183],a[116:119],v[68:71],v[180:183]\n"
"v_exp_f32 v119,v62\n"
"v_mfma_f32_16x16x32_bf16 v[212:215],a[116:119],v[72:75],v[212:215]\n"
"ds_read_b32 v125,v124\n"
"buffer_store_dwordx4 a[248:251],v112,s[24:27],s56 offen offset:2048 sc1\n"
"v_mfma_f32_16x16x32_bf16 v[216:219],a[120:123],v[72:75],v[216:219]\n"
"v_exp_f32 v120,v63\n"
"v_mfma_f32_16x16x32_bf16 v[248:251],a[120:123],v[76:79],v[248:251]\n"
"v_cvt_pk_bf16_f32 v95,v119,v120\n"
"v_mfma_f32_16x16x32_bf16 v[152:155],a[120:123],v[64:67],v[152:155]\n"
"v_mfma_f32_16x16x32_bf16 v[184:187],a[120:123],v[68:71],v[184:187]\n"
"v_mfma_f32_16x16x32_bf16 v[188:191],a[124:127],v[68:71],v[188:191]\n"
"s_waitcnt lgkmcnt(0)\n"
"v_subrev_u32 v125,s101,v125\n"
"v_or3_b32 v125,v118,v95,v125\n"
"v_mfma_f32_16x16x32_bf16 v[220:223],a[124:127],v[72:75],v[220:223]\n"
"v_and_b32 v125,0x40004000,v125\n"
"v_cmp_ne_u32 vcc,0,v125\n"
"v_mfma_f32_16x16x32_bf16 v[252:255],a[124:127],v[76:79],v[252:255]\n"
"v_mfma_f32_16x16x32_bf16 v[156:159],a[124:127],v[64:67],v[156:159]\n"
"s_cbranch_vccnz .Lfk_cs_f1\n"
".Lfk_cr_f1:\n"
"v_mfma_f32_16x16x32_bf16 v[32:35],a[64:67],a[0:3],v[96:99]\n"
"s_add_u32 s101,s101,4\n"
"v_exp_f32 v122,v0\n"
"v_mfma_f32_16x16x32_bf16 v[32:35],a[68:71],a[4:7],v[32:35]\n"
"s_add_u32 m0,s55,12288\n"
"v_exp_f32 v123,v1\n"
"v_mfma_f32_16x16x32_bf16 v[32:35],a[72:75],a[8:11],v[32:35]\n"
"ds_read_b128 a[96:99],v114 offset:8192\n"
"ds_read_b128 a[100:103],v114 offset:9216\n"
"v_mfma_f32_16x16x32_bf16 v[32:35],a[76:79],a[12:15],v[32:35]\n"
"s_add_u32 s54,s54,s22\n"
"v_exp_f32 v119,v2\n"
"v_mfma_f32_16x16x32_bf16 v[36:39],a[92:95],a[12:15],v[96:99]\n"
"ds_read_b128 a[104:107],v114 offset:10240\n"
"v_cvt_pk_bf16_f32 v64,v122,v123\n"
"v_mfma_f32_16x16x32_bf16 v[36:39],a[88:91],a[8:11],v[36:39]\n"
"v_exp_f32 v120,v3\n"
"v_mfma_f32_16x16x32_bf16 v[36:39],a[84:87],a[4:7],v[36:39]\n"
"ds_read_b128 a[108:111],v114 offset:11264\n"
"ds_read_b128 a[112:115],v114 offset:12288\n"
"v_mfma_f32_16x16x32_bf16 v[36:39],a[80:83],a[0:3],v[36:39]\n"
"v_exp_f32 v122,v4\n"
"v_mfma_f32_16x16x32_bf16 v[44:47],a[80:83],a[16:19],v[100:103]\n"
"ds_read_b128 a[116:119],v114 offset:13312\n"
"v_cvt_pk_bf16_f32 v65,v119,v120\n"
"v_mfma_f32_16x16x32_bf16 v[44:47],a[84:87],a[20:23],v[44:47]\n"
"v_exp_f32 v123,v5\n"
"v_mfma_f32_16x16x32_bf16 v[44:47],a[88:91],a[24:27],v[44:47]\n"
"ds_read_b128 a[120:123],v114 offset:14336\n"
"ds_read_b128 a[124:127],v114 offset:15360\n"
"v_mfma_f32_16x16x32_bf16 v[44:47],a[92:95],a[28:31],v[44:47]\n"
"v_exp_f32 v119,v6\n"
"v_mfma_f32_16x16x32_bf16 v[40:43],a[76:79],a[28:31],v[100:103]\n"
"v_cvt_pk_bf16_f32 v66,v122,v123\n"
"v_or3_b32 v118,v64,v65,v66\n"
"v_mfma_f32_16x16x32_bf16 v[40:43],a[72:75],a[24:27],v[40:43]\n"
"v_exp_f32 v120,v7\n"
"v_mfma_f32_16x16x32_bf16 v[40:43],a[68:71],a[20:23],v[40:43]\n"
"buffer_load_dwordx4 v115,s[32:35],s54 offen sc1 lds\n"
"v_mfma_f32_16x16x32_bf16 v[40:43],a[64:67],a[16:19],v[40:43]\n"
"s_add_u32 m0,s67,32768\n"
"v_exp_f32 v122,v8\n"
"v_mfma_f32_16x16x32_bf16 v[48:51],a[64:67],a[32:35],v[104:107]\n"
"v_cvt_pk_bf16_f32 v67,v119,v120\n"
"v_mfma_f32_16x16x32_bf16 v[48:51],a[68:71],a[36:39],v[48:51]\n"
"v_exp_f32 v123,v9\n"
"v_mfma_f32_16x16x32_bf16 v[48:51],a[72:75],a[40:43],v[48:51]\n"
"buffer_load_dwordx4 v112,s[24:27],s64 offen offset:0 sc1 lds\n"
"v_mfma_f32_16x16x32_bf16 v[48:51],a[76:79],a[44:47],v[48:51]\n"
"v_exp_f32 v119,v10\n"
"v_mfma_f32_16x16x32_bf16 v[52:55],a[92:95],a[44:47],v[104:107]\n"
"v_cvt_pk_bf16_f32 v68,v122,v123\n"
"v_or3_b32 v118,v118,v67,v68\n"
"v_mfma_f32_16x16x32_bf16 v[52:55],a[88:91],a[40:43],v[52:55]\n"
"v_exp_f32 v120,v11\n"
"v_mfma_f32_16x16x32_bf16 v[52:55],a[84:87],a[36:39],v[52:55]\n"
"buffer_load_dwordx4 v112,s[24:27],s64 offen offset:1024 sc1 lds\n"
"v_mfma_f32_16x16x32_bf16 v[52:55],a[80:83],a[32:35],v[52:55]\n"
"v_exp_f32 v122,v12\n"
"v_mfma_f32_16x16x32_bf16 v[60:63],a[80:83],a[48:51],v[108:111]\n"
"v_cvt_pk_bf16_f32 v69,v119,v120\n"
"v_mfma_f32_16x16x32_bf16 v[60:63],a[84:87],a[52:55],v[60:63]\n"
"v_exp_f32 v123,v13\n"
"v_mfma_f32_16x16x32_bf16 v[60:63],a[88:91],a[56:59],v[60:63]\n"
"buffer_load_dwordx4 v112,s[24:27],s64 offen offset:2048 sc1 lds\n"
"v_mfma_f32_16x16x32_bf16 v[60:63],a[92:95],a[60:63],v[60:63]\n"
"v_exp_f32 v119,v14\n"
"v_mfma_f32_16x16x32_bf16 v[56:59],a[76:79],a[60:63],v[108:111]\n"
"v_cvt_pk_bf16_f32 v70,v122,v123\n"
"v_or3_b32 v118,v118,v69,v70\n"
"v_mfma_f32_16x16x32_bf16 v[56:59],a[72:75],a[56:59],v[56:59]\n"
"v_exp_f32 v120,v15\n"
"v_mfma_f32_16x16x32_bf16 v[56:59],a[68:71],a[52:55],v[56:59]\n"
"buffer_load_dwordx4 v112,s[24:27],s64 offen offset:3072 sc1 lds\n"
"v_mfma_f32_16x16x32_bf16 v[56:59],a[64:67],a[48:51],v[56:59]\n"
"s_waitcnt lgkmcnt(0)\n"
"v_exp_f32 v122,v16\n"
"v_mfma_f32_16x16x32_bf16 v[128:131],a[96:99],v[80:83],v[128:131]\n"
"v_exp_f32 v123,v17\n"
"v_mfma_f32_16x16x32_bf16 v[160:163],a[96:99],v[84:87],v[160:163]\n"
"s_add_u32 s64,s64,16384\n"
"ds_read_b128 a[64:67],v113 offset:0\n"
"v_cvt_pk_bf16_f32 v71,v119,v120\n"
"v_mfma_f32_16x16x32_bf16 v[192:195],a[96:99],v[88:91],v[192:195]\n"
"v_exp_f32 v119,v18\n"
"v_mfma_f32_16x16x32_bf16 v[224:227],a[96:99],v[92:95],v[224:227]\n"
"ds_read_b128 a[68:71],v113 offset:1024\n"
"v_cvt_pk_bf16_f32 v72,v122,v123\n"
"v_mfma_f32_16x16x32_bf16 a[140:143],a[144:147],v[92:95],a[140:143]\n"
"v_exp_f32 v120,v19\n"
"v_mfma_f32_16x16x32_bf16 v[228:231],a[100:103],v[92:95],v[228:231]\n"
"ds_read_b128 a[72:75],v113 offset:2048\n"
"ds_read_b128 a[76:79],v113 offset:3072\n"
"v_mfma_f32_16x16x32_bf16 v[132:135],a[100:103],v[80:83],v[132:135]\n"
"v_exp_f32 v122,v20\n"
"v_mfma_f32_16x16x32_bf16 v[164:167],a[100:103],v[84:87],v[164:167]\n"
"ds_read_b128 a[80:83],v113 offset:4096\n"
"v_cvt_pk_bf16_f32 v73,v119,v120\n"
"v_mfma_f32_16x16x32_bf16 v[196:199],a[100:103],v[88:91],v[196:199]\n"
"v_exp_f32 v123,v21\n"
"v_mfma_f32_16x16x32_bf16 a[136:139],a[144:147],v[88:91],a[136:139]\n"
"ds_read_b128 a[84:87],v113 offset:5120\n"
"ds_read_b128 a[88:91],v113 offset:6144\n"
"v_mfma_f32_16x16x32_bf16 v[200:203],a[104:107],v[88:91],v[200:203]\n"
"v_exp_f32 v119,v22\n"
"v_mfma_f32_16x16x32_bf16 v[232:235],a[104:107],v[92:95],v[232:235]\n"
"ds_read_b128 a[92:95],v113 offset:7168\n"
"v_cvt_pk_bf16_f32 v74,v122,v123\n"
"v_mfma_f32_16x16x32_bf16 v[136:139],a[104:107],v[80:83],v[136:139]\n"
"v_exp_f32 v120,v23\n"
"v_mfma_f32_16x16x32_bf16 v[168:171],a[104:107],v[84:87],v[168:171]\n"
"s_waitcnt vmcnt(12)\n"
"ds_add_u32 v124,a152\n"
"v_mfma_f32_16x16x32_bf16 a[132:135],a[144:147],v[84:87],a[132:135]\n"
"v_exp_f32 v122,v24\n"
"v_mfma_f32_16x16x32_bf16 v[172:175],a[108:111],v[84:87],v[172:175]\n"
"v_cvt_pk_bf16_f32 v75,v119,v120\n"
"v_or3_b32 v118,v118,v71,v72\n"
"v_mfma_f32_16x16x32_bf16 v[204:207],a[108:111],v[88:91],v[204:207]\n"
"v_exp_f32 v123,v25\n"
"v_mfma_f32_16x16x32_bf16 v[236:239],a[108:111],v[92:95],v[236:239]\n"
"v_cvt_pk_bf16_f32 v76,v122,v123\n"
"v_or3_b32 v118,v118,v73,v74\n"
"v_mfma_f32_16x16x32_bf16 v[140:143],a[108:111],v[80:83],v[140:143]\n"
"v_exp_f32 v119,v26\n"
"v_mfma_f32_16x16x32_bf16 a[128:131],a[144:147],v[80:83],a[128:131]\n"
"v_or3_b32 v118,v118,v75,v76\n"
"v_mfma_f32_16x16x32_bf16 v[144:147],a[112:115],v[80:83],v[144:147]\n"
"v_exp_f32 v120,v27\n"
"v_mfma_f32_16x16x32_bf16 v[176:179],a[112:115],v[84:87],v[176:179]\n"
"v_cvt_pk_bf16_f32 v77,v119,v120\n"
"v_mfma_f32_16x16x32_bf16 v[208:211],a[112:115],v[88:91],v[208:211]\n"
"v_exp_f32 v122,v28\n"
"v_mfma_f32_16x16x32_bf16 v[240:243],a[112:115],v[92:95],v[240:243]\n"
"v_mfma_f32_16x16x32_bf16 v[244:247],a[116:119],v[92:95],v[244:247]\n"
"v_exp_f32 v123,v29\n"
"v_mfma_f32_16x16x32_bf16 v[148:151],a[116:119],v[80:83],v[148:151]\n"
"v_cvt_pk_bf16_f32 v78,v122,v123\n"
"v_or3_b32 v118,v118,v77,v78\n"
"v_mfma_f32_16x16x32_bf16 v[180:183],a[116:119],v[84:87],v[180:183]\n"
"v_exp_f32 v119,v30\n"
"v_mfma_f32_16x16x32_bf16 v[212:215],a[116:119],v[88:91],v[212:215]\n"
"ds_read_b32 v125,v124\n"
"buffer_store_dwordx4 a[252:255],v112,s[24:27],s56 offen offset:3072 sc1\n"
"v_mfma_f32_16x16x32_bf16 v[216:219],a[120:123],v[88:91],v[216:219]\n"
"s_add_u32 s89,s89,1\n"
"v_exp_f32 v120,v31\n"
"v_mfma_f32_16x16x32_bf16 v[248:251],a[120:123],v[92:95],v[248:251]\n"
"v_cvt_pk_bf16_f32 v79,v119,v120\n"
"v_mfma_f32_16x16x32_bf16 v[152:155],a[120:123],v[80:83],v[152:155]\n"
"v_mfma_f32_16x16x32_bf16 v[184:187],a[120:123],v[84:87],v[184:187]\n"
"v_mfma_f32_16x16x32_bf16 v[188:191],a[124:127],v[84:87],v[188:191]\n"
"s_waitcnt lgkmcnt(0)\n"
"v_subrev_u32 v125,s101,v125\n"
"v_or3_b32 v125,v118,v79,v125\n"
"v_mfma_f32_16x16x32_bf16 v[220:223],a[124:127],v[88:91],v[220:223]\n"
"v_and_b32 v125,0x40004000,v125\n"
"v_cmp_ne_u32 vcc,0,v125\n"
"v_mfma_f32_16x16x32_bf16 v[252:255],a[124:127],v[92:95],v[252:255]\n"
"v_mfma_f32_16x16x32_bf16 v[156:159],a[124:127],v[80:83],v[156:159]\n"
"s_cbranch_vccnz .Lfk_cs_f2\n"
".Lfk_cr_f2:\n"
"s_cmp_lt_u32 s89,s60\n"
"s_cbranch_scc1 .Lfk_F\n"
"s_branch .Lfk_Fx\n"
".Lfk_cs_f3:\n"
"s_call_b64 s[94:95],.Lfk_meet3\n"
"v_or_b32 v118,v118,v95\n"
"v_and_b32 v118,0x40004000,v118\n"
"v_cmp_ne_u32 vcc,0,v118\n"
"s_nop 0\n"
"s_cbranch_vccz .Lfk_cr_f3\n"
"s_mov_b32 s91,15\n"
"s_call_b64 s[94:95],.Lfk_rare3\n"
"s_branch .Lfk_cr_f3\n"
".Lfk_cs_f0:\n"
"s_call_b64 s[94:95],.Lfk_meet3\n"
"v_or_b32 v118,v118,v79\n"
"v_and_b32 v118,0x40004000,v118\n"
"v_cmp_ne_u32 vcc,0,v118\n"
"s_nop 0\n"
"s_cbranch_vccz .Lfk_cr_f0\n"
"s_mov_b32 s91,15\n"
"s_call_b64 s[94:95],.Lfk_rare2\n"
"s_branch .Lfk_cr_f0\n"
".Lfk_cs_f1:\n"
"s_call_b64 s[94:95],.Lfk_meet3\n"
"v_or_b32 v118,v118,v95\n"
"v_and_b32 v118,0x40004000,v118\n"
"v_cmp_ne_u32 vcc,0,v118\n"
"s_nop 0\n"
"s_cbranch_vccz .Lfk_cr_f1\n"
"s_mov_b32 s91,15\n"
"s_call_b64 s[94:95],.Lfk_rare3\n"
"s_branch .Lfk_cr_f1\n"
".Lfk_cs_f2:\n"
"s_call_b64 s[94:95],.Lfk_meet3\n"
"v_or_b32 v118,v118,v79\n"
"v_and_b32 v118,0x40004000,v118\n"
"v_cmp_ne_u32 vcc,0,v118\n"
"s_nop 0\n"
"s_cbranch_vccz .Lfk_cr_f2\n"
"s_mov_b32 s91,15\n"
"s_call_b64 s[94:95],.Lfk_rare2\n"
"s_branch .Lfk_cr_f2\n"
".Lfk_cs_g3:\n"
"s_call_b64 s[94:95],.Lfk_meet3\n"
"v_or_b32 v118,v118,v95\n"
"v_and_b32 v118,0x40004000,v118\n"
"v_cmp_ne_u32 vcc,0,v118\n"
"s_nop 0\n"
"s_cbranch_vccz .Lfk_cr_g3\n"
"s_mov_b32 s91,15\n"
"s_call_b64 s[94:95],.Lfk_rare3\n"
"s_branch .Lfk_cr_g3\n"
".Lfk_cs_g0:\n"
"s_call_b64 s[94:95],.Lfk_meet3\n"
"v_or_b32 v118,v118,v79\n"
"v_and_b32 v118,0x40004000,v118\n"
"v_cmp_ne_u32 vcc,0,v118\n"
"s_nop 0\n"
"s_cbranch_vccz .Lfk_cr_g0\n"
"s_mov_b32 s91,15\n"
"s_call_b64 s[94:95],.Lfk_rare2\n"
"s_branch .Lfk_cr_g0\n"
".Lfk_cs_g1:\n"
"s_call_b64 s[94:95],.Lfk_meet3\n"
"v_or_b32 v118,v118,v95\n"
"v_and_b32 v118,0x40004000,v118\n"
"v_cmp_ne_u32 vcc,0,v118\n"
"s_nop 0\n"
"s_cbranch_vccz .Lfk_cr_g1\n"
"s_mov_b32 s91,15\n"
"s_call_b64 s[94:95],.Lfk_rare3\n"
"s_branch .Lfk_cr_g1\n"
".Lfk_cs_g2:\n"
"s_call_b64 s[94:95],.Lfk_meet3\n"
"v_or_b32 v118,v118,v79\n"
"v_and_b32 v118,0x40004000,v118\n"
"v_cmp_ne_u32 vcc,0,v118\n"
"s_nop 0\n"
"s_cbranch_vccz .Lfk_cr_g2\n"
"s_mov_b32 s91,15\n"
"s_call_b64 s[94:95],.Lfk_rare2\n"
"s_branch .Lfk_cr_g2\n"
".Lfk_meet3:\n"
"s_sub_u32 s82,s101,0\n"
"s_mov_b32 s84,0x1000000\n"
".Lfk_meetl3:\n"
"ds_read_b32 v125,v124\n"
"s_sub_u32 s84,s84,1\n"
"s_cbranch_scc1 .Lfk_meetx3\n"
"s_waitcnt lgkmcnt(0)\n"
"v_cmp_gt_u32 vcc,s82,v125\n"
"s_nop 0\n"
"s_cbranch_vccnz .Lfk_meetl3\n"
".Lfk_meetx3:\n"
"s_setpc_b64 s[94:95]\n"
".Lfk_Fx:\n"
"s_mov_b32 s82,2146435072\n"
"buffer_load_dword a148,off,s[24:27],s82\n"
"buffer_load_dword a148,off,s[24:27],s82\n"
"buffer_load_dword a148,off,s[24:27],s82\n"
"buffer_load_dword a148,off,s[24:27],s82\n"
"s_cmp_ge_u32 s52,2\n"
"s_cbranch_scc1 .Lfk_winA\n"
".Lfk_H:\n"
"v_mfma_f32_16x16x32_bf16 v[0:3],a[64:67],a[0:3],v[96:99]\n"
"s_swappc_b64 s[94:95],s[20:21]\n"
"s_add_u32 s101,s101,4\n"
"v_exp_f32 v122,v32\n"
"v_mfma_f32_16x16x32_bf16 v[0:3],a[68:71],a[4:7],v[0:3]\n"
"s_cmp_eq_u32 s64,s65\n"
"v_exp_f32 v123,v33\n"
"v_mfma_f32_16x16x32_bf16 v[0:3],a[72:75],a[8:11],v[0:3]\n"
"ds_read_b128 a[96:99],v114 offset:24576\n"
"ds_read_b128 a[100:103],v114 offset:25600\n"
"v_mfma_f32_16x16x32_bf16 v[0:3],a[76:79],a[12:15],v[0:3]\n"
"s_cselect_b32 s64,s66,s64\n"
"v_exp_f32 v119,v34\n"
"v_mfma_f32_16x16x32_bf16 v[4:7],a[92:95],a[12:15],v[96:99]\n"
"ds_read_b128 a[104:107],v114 offset:26624\n"
"v_cvt_pk_bf16_f32 v80,v122,v123\n"
"v_mfma_f32_16x16x32_bf16 v[4:7],a[88:91],a[8:11],v[4:7]\n"
"v_exp_f32 v120,v35\n"
"v_mfma_f32_16x16x32_bf16 v[4:7],a[84:87],a[4:7],v[4:7]\n"
"ds_read_b128 a[108:111],v114 offset:27648\n"
"ds_read_b128 a[112:115],v114 offset:28672\n"
"v_mfma_f32_16x16x32_bf16 v[4:7],a[80:83],a[0:3],v[4:7]\n"
"v_exp_f32 v122,v36\n"
"v_mfma_f32_16x16x32_bf16 v[12:15],a[80:83],a[16:19],v[100:103]\n"
"ds_read_b128 a[116:119],v114 offset:29696\n"
"v_cvt_pk_bf16_f32 v81,v119,v120\n"
"v_mfma_f32_16x16x32_bf16 v[12:15],a[84:87],a[20:23],v[12:15]\n"
"v_exp_f32 v123,v37\n"
"v_mfma_f32_16x16x32_bf16 v[12:15],a[88:91],a[24:27],v[12:15]\n"
"ds_read_b128 a[120:123],v114 offset:30720\n"
"ds_read_b128 a[124:127],v114 offset:31744\n"
"v_mfma_f32_16x16x32_bf16 v[12:15],a[92:95],a[28:31],v[12:15]\n"
"s_add_u32 m0,s67,0\n"
"v_exp_f32 v119,v38\n"
"v_mfma_f32_16x16x32_bf16 v[8:11],a[76:79],a[28:31],v[100:103]\n"
"v_cvt_pk_bf16_f32 v82,v122,v123\n"
"v_or3_b32 v118,v80,v81,v82\n"
"v_mfma_f32_16x16x32_bf16 v[8:11],a[72:75],a[24:27],v[8:11]\n"
"v_exp_f32 v120,v39\n"
"v_mfma_f32_16x16x32_bf16 v[8:11],a[68:71],a[20:23],v[8:11]\n"
"buffer_load_dwordx4 v112,s[24:27],s64 offen offset:0 sc1 lds\n"
"v_mfma_f32_16x16x32_bf16 v[8:11],a[64:67],a[16:19],v[8:11]\n"
"v_exp_f32 v122,v40\n"
"v_mfma_f32_16x16x32_bf16 v[16:19],a[64:67],a[32:35],v[104:107]\n"
"v_cvt_pk_bf16_f32 v83,v119,v120\n"
"v_mfma_f32_16x16x32_bf16 v[16:19],a[68:71],a[36:39],v[16:19]\n"
"v_exp_f32 v123,v41\n"
"v_mfma_f32_16x16x32_bf16 v[16:19],a[72:75],a[40:43],v[16:19]\n"
"buffer_load_dwordx4 v112,s[24:27],s64 offen offset:1024 sc1 lds\n"
"v_mfma_f32_16x16x32_bf16 v[16:19],a[76:79],a[44:47],v[16:19]\n"
"v_exp_f32 v119,v42\n"
"v_mfma_f32_16x16x32_bf16 v[20:23],a[92:95],a[44:47],v[104:107]\n"
"v_cvt_pk_bf16_f32 v84,v122,v123\n"
"v_or3_b32 v118,v118,v83,v84\n"
"v_mfma_f32_16x16x32_bf16 v[20:23],a[88:91],a[40:43],v[20:23]\n"
"v_exp_f32 v120,v43\n"
"v_mfma_f32_16x16x32_bf16 v[20:23],a[84:87],a[36:39],v[20:23]\n"
"v_cvt_pk_bf16_f32 v85,v119,v120\n"
"v_mfma_f32_16x16x32_bf16 v[20:23],a[80:83],a[32:35],v[20:23]\n"
"v_exp_f32 v122,v44\n"
"v_mfma_f32_16x16x32_bf16 v[28:31],a[80:83],a[48:51],v[108:111]\n"
"buffer_load_dwordx4 v112,s[24:27],s64 offen offset:2048 sc1 lds\n"
"v_mfma_f32_16x16x32_bf16 v[28:31],a[84:87],a[52:55],v[28:31]\n"
"v_exp_f32 v123,v45\n"
"v_mfma_f32_16x16x32_bf16 v[28:31],a[88:91],a[56:59],v[28:31]\n"
"v_cvt_pk_bf16_f32 v86,v122,v123\n"
"v_or3_b32 v118,v118,v85,v86\n"
"v_mfma_f32_16x16x32_bf16 v[28:31],a[92:95],a[60:63],v[28:31]\n"
"v_exp_f32 v119,v46\n"
"v_mfma_f32_16x16x32_bf16 v[24:27],a[76:79],a[60:63],v[108:111]\n"
"v_mfma_f32_16x16x32_bf16 v[24:27],a[72:75],a[56:59],v[24:27]\n"
"v_exp_f32 v120,v47\n"
"v_mfma_f32_16x16x32_bf16 v[24:27],a[68:71],a[52:55],v[24:27]\n"
"buffer_load_dwordx4 v112,s[24:27],s64 offen offset:3072 sc1 lds\n"
"v_mfma_f32_16x16x32_bf16 v[24:27],a[64:67],a[48:51],v[24:27]\n"
"s_waitcnt lgkmcnt(0)\n"
"v_exp_f32 v122,v48\n"
"v_mfma_f32_16x16x32_bf16 v[128:131],a[96:99],v[64:67],v[128:131]\n"
"v_exp_f32 v123,v49\n"
"v_mfma_f32_16x16x32_bf16 v[160:163],a[96:99],v[68:71],v[160:163]\n"
"ds_read_b128 a[64:67],v113 offset:16384\n"
"v_cvt_pk_bf16_f32 v87,v119,v120\n"
"v_mfma_f32_16x16x32_bf16 v[192:195],a[96:99],v[72:75],v[192:195]\n"
"s_add_u32 m0,s67,4096\n"
"v_exp_f32 v119,v50\n"
"v_mfma_f32_16x16x32_bf16 v[224:227],a[96:99],v[76:79],v[224:227]\n"
"ds_read_b128 a[68:71],v113 offset:17408\n"
"v_cvt_pk_bf16_f32 v88,v122,v123\n"
"v_mfma_f32_16x16x32_bf16 a[140:143],a[144:147],v[76:79],a[140:143]\n"
"v_exp_f32 v120,v51\n"
"v_mfma_f32_16x16x32_bf16 v[228:231],a[100:103],v[76:79],v[228:231]\n"
"ds_read_b128 a[72:75],v113 offset:18432\n"
"ds_read_b128 a[76:79],v113 offset:19456\n"
"v_mfma_f32_16x16x32_bf16 v[132:135],a[100:103],v[64:67],v[132:135]\n"
"v_exp_f32 v122,v52\n"
"v_mfma_f32_16x16x32_bf16 v[164:167],a[100:103],v[68:71],v[164:167]\n"
"ds_read_b128 a[80:83],v113 offset:20480\n"
"v_cvt_pk_bf16_f32 v89,v119,v120\n"
"v_mfma_f32_16x16x32_bf16 v[196:199],a[100:103],v[72:75],v[196:199]\n"
"v_exp_f32 v123,v53\n"
"v_mfma_f32_16x16x32_bf16 a[136:139],a[144:147],v[72:75],a[136:139]\n"
"ds_read_b128 a[84:87],v113 offset:21504\n"
"ds_read_b128 a[88:91],v113 offset:22528\n"
"v_mfma_f32_16x16x32_bf16 v[200:203],a[104:107],v[72:75],v[200:203]\n"
"v_exp_f32 v119,v54\n"
"v_mfma_f32_16x16x32_bf16 v[232:235],a[104:107],v[76:79],v[232:235]\n"
"ds_read_b128 a[92:95],v113 offset:23552\n"
"v_cvt_pk_bf16_f32 v90,v122,v123\n"
"v_mfma_f32_16x16x32_bf16 v[136:139],a[104:107],v[64:67],v[136:139]\n"
"v_exp_f32 v120,v55\n"
"v_mfma_f32_16x16x32_bf16 v[168:171],a[104:107],v[68:71],v[168:171]\n"
"s_waitcnt vmcnt(15)\n"
"ds_add_u32 v124,a152\n"
"v_mfma_f32_16x16x32_bf16 a[132:135],a[144:147],v[68:71],a[132:135]\n"
"v_exp_f32 v122,v56\n"
"v_mfma_f32_16x16x32_bf16 v[172:175],a[108:111],v[68:71],v[172:175]\n"
"v_cvt_pk_bf16_f32 v91,v119,v120\n"
"v_or3_b32 v118,v118,v87,v88\n"
"v_mfma_f32_16x16x32_bf16 v[204:207],a[108:111],v[72:75],v[204:207]\n"
"v_exp_f32 v123,v57\n"
"v_mfma_f32_16x16x32_bf16 v[236:239],a[108:111],v[76:79],v[236:239]\n"
"buffer_load_dwordx4 v117,s[24:27],s64 offen offset:0 sc1 lds\n"
"v_mfma_f32_16x16x32_bf16 v[140:143],a[108:111],v[64:67],v[140:143]\n"
"v_exp_f32 v119,v58\n"
"v_mfma_f32_16x16x32_bf16 a[128:131],a[144:147],v[64:67],a[128:131]\n"
"v_cvt_pk_bf16_f32 v92,v122,v123\n"
"v_or3_b32 v118,v118,v89,v90\n"
"v_mfma_f32_16x16x32_bf16 v[144:147],a[112:115],v[64:67],v[144:147]\n"
"v_exp_f32 v120,v59\n"
"v_mfma_f32_16x16x32_bf16 v[176:179],a[112:115],v[68:71],v[176:179]\n"
"buffer_load_dwordx4 v117,s[24:27],s64 offen offset:1024 sc1 lds\n"
"v_mfma_f32_16x16x32_bf16 v[208:211],a[112:115],v[72:75],v[208:211]\n"
"v_exp_f32 v122,v60\n"
"v_mfma_f32_16x16x32_bf16 v[240:243],a[112:115],v[76:79],v[240:243]\n"
"v_cvt_pk_bf16_f32 v93,v119,v120\n"
"v_or3_b32 v118,v118,v91,v92\n"
"v_mfma_f32_16x16x32_bf16 v[244:247],a[116:119],v[76:79],v[244:247]\n"
"v_exp_f32 v123,v61\n"
"v_mfma_f32_16x16x32_bf16 v[148:151],a[116:119],v[64:67],v[148:151]\n"
"buffer_load_dwordx4 v117,s[24:27],s64 offen offset:2048 sc1 lds\n"
"v_mfma_f32_16x16x32_bf16 v[180:183],a[116:119],v[68:71],v[180:183]\n"
"v_exp_f32 v119,v62\n"
"v_mfma_f32_16x16x32_bf16 v[212:215],a[116:119],v[72:75],v[212:215]\n"
"ds_read_b32 v125,v124\n"
"v_cvt_pk_bf16_f32 v94,v122,v123\n"
"v_mfma_f32_16x16x32_bf16 v[216:219],a[120:123],v[72:75],v[216:219]\n"
"v_exp_f32 v120,v63\n"
"v_mfma_f32_16x16x32_bf16 v[248:251],a[120:123],v[76:79],v[248:251]\n"
"buffer_load_dwordx4 v117,s[24:27],s64 offen offset:3072 sc1 lds\n"
"v_mfma_f32_16x16x32_bf16 v[152:155],a[120:123],v[64:67],v[152:155]\n"
"s_add_u32 s64,s64,16384\n"
"s_andn2_b32 m0,s55,16384\n"
"v_mfma_f32_16x16x32_bf16 v[184:187],a[120:123],v[68:71],v[184:187]\n"
"s_add_u32 s54,s54,s22\n"
"v_cvt_pk_bf16_f32 v95,v119,v120\n"
"v_mfma_f32_16x16x32_bf16 v[188:191],a[124:127],v[68:71],v[188:191]\n"
"s_waitcnt lgkmcnt(0)\n"
"v_subrev_u32 v125,s101,v125\n"
"v_or3_b32 v118,v118,v93,v94\n"
"v_mfma_f32_16x16x32_bf16 v[220:223],a[124:127],v[72:75],v[220:223]\n"
"v_or3_b32 v125,v118,v95,v125\n"
"v_and_b32 v125,0x40004000,v125\n"
"v_mfma_f32_16x16x32_bf16 v[252:255],a[124:127],v[76:79],v[252:255]\n"
"buffer_load_dwordx4 v115,s[32:35],s54 offen sc1 lds\n"
"buffer_store_dwordx4 a[240:243],v112,s[24:27],s56 offen offset:0 sc1\n"
"v_cmp_ne_u32 vcc,0,v125\n"
"v_mfma_f32_16x16x32_bf16 v[156:159],a[124:127],v[64:67],v[156:159]\n"
"s_cbranch_vccnz .Lfk_cs_g3\n"
".Lfk_cr_g3:\n"
"v_mfma_f32_16x16x32_bf16 v[32:35],a[64:67],a[0:3],v[96:99]\n"
"s_add_u32 s101,s101,4\n"
"v_exp_f32 v122,v0\n"
"v_mfma_f32_16x16x32_bf16 v[32:35],a[68:71],a[4:7],v[32:35]\n"
"s_add_u32 m0,s55,4096\n"
"v_exp_f32 v123,v1\n"
"v_mfma_f32_16x16x32_bf16 v[32:35],a[72:75],a[8:11],v[32:35]\n"
"ds_read_b128 a[96:99],v114 offset:57344\n"
"ds_read_b128 a[100:103],v114 offset:58368\n"
"v_mfma_f32_16x16x32_bf16 v[32:35],a[76:79],a[12:15],v[32:35]\n"
"s_add_u32 s54,s54,s22\n"
"v_exp_f32 v119,v2\n"
"v_mfma_f32_16x16x32_bf16 v[36:39],a[92:95],a[12:15],v[96:99]\n"
"ds_read_b128 a[104:107],v114 offset:59392\n"
"v_cvt_pk_bf16_f32 v64,v122,v123\n"
"v_mfma_f32_16x16x32_bf16 v[36:39],a[88:91],a[8:11],v[36:39]\n"
"v_exp_f32 v120,v3\n"
"v_mfma_f32_16x16x32_bf16 v[36:39],a[84:87],a[4:7],v[36:39]\n"
"ds_read_b128 a[108:111],v114 offset:60416\n"
"ds_read_b128 a[112:115],v114 offset:61440\n"
"v_mfma_f32_16x16x32_bf16 v[36:39],a[80:83],a[0:3],v[36:39]\n"
"v_exp_f32 v122,v4\n"
"v_mfma_f32_16x16x32_bf16 v[44:47],a[80:83],a[16:19],v[100:103]\n"
"ds_read_b128 a[116:119],v114 offset:62464\n"
"v_cvt_pk_bf16_f32 v65,v119,v120\n"
"v_mfma_f32_16x16x32_bf16 v[44:47],a[84:87],a[20:23],v[44:47]\n"
"v_exp_f32 v123,v5\n"
"v_mfma_f32_16x16x32_bf16 v[44:47],a[88:91],a[24:27],v[44:47]\n"
"ds_read_b128 a[120:123],v114 offset:63488\n"
"ds_read_b128 a[124:127],v114 offset:64512\n"
"v_mfma_f32_16x16x32_bf16 v[44:47],a[92:95],a[28:31],v[44:47]\n"
"v_exp_f32 v119,v6\n"
"v_mfma_f32_16x16x32_bf16 v[40:43],a[76:79],a[28:31],v[100:103]\n"
"v_cvt_pk_bf16_f32 v66,v122,v123\n"
"v_or3_b32 v118,v64,v65,v66\n"
"v_mfma_f32_16x16x32_bf16 v[40:43],a[72:75],a[24:27],v[40:43]\n"
"v_exp_f32 v120,v7\n"
"v_mfma_f32_16x16x32_bf16 v[40:43],a[68:71],a[20:23],v[40:43]\n"
"buffer_load_dwordx4 v115,s[32:35],s54 offen sc1 lds\n"
"v_mfma_f32_16x16x32_bf16 v[40:43],a[64:67],a[16:19],v[40:43]\n"
"s_add_u32 m0,s67,16384\n"
"v_exp_f32 v122,v8\n"
"v_mfma_f32_16x16x32_bf16 v[48:51],a[64:67],a[32:35],v[104:107]\n"
"v_cvt_pk_bf16_f32 v67,v119,v120\n"
"v_mfma_f32_16x16x32_bf16 v[48:51],a[68:71],a[36:39],v[48:51]\n"
"v_exp_f32 v123,v9\n"
"v_mfma_f32_16x16x32_bf16 v[48:51],a[72:75],a[40:43],v[48:51]\n"
"buffer_load_dwordx4 v112,s[24:27],s64 offen offset:0 sc1 lds\n"
"v_mfma_f32_16x16x32_bf16 v[48:51],a[76:79],a[44:47],v[48:51]\n"
"v_exp_f32 v119,v10\n"
"v_mfma_f32_16x16x32_bf16 v[52:55],a[92:95],a[44:47],v[104:107]\n"
"v_cvt_pk_bf16_f32 v68,v122,v123\n"
"v_or3_b32 v118,v118,v67,v68\n"
"v_mfma_f32_16x16x32_bf16 v[52:55],a[88:91],a[40:43],v[52:55]\n"
"v_exp_f32 v120,v11\n"
"v_mfma_f32_16x16x32_bf16 v[52:55],a[84:87],a[36:39],v[52:55]\n"
"buffer_load_dwordx4 v112,s[24:27],s64 offen offset:1024 sc1 lds\n"
"v_mfma_f32_16x16x32_bf16 v[52:55],a[80:83],a[32:35],v[52:55]\n"
"v_exp_f32 v122,v12\n"
"v_mfma_f32_16x16x32_bf16 v[60:63],a[80:83],a[48:51],v[108:111]\n"
"v_cvt_pk_bf16_f32 v69,v119,v120\n"
"v_mfma_f32_16x16x32_bf16 v[60:63],a[84:87],a[52:55],v[60:63]\n"
"v_exp_f32 v123,v13\n"
"v_mfma_f32_16x16x32_bf16 v[60:63],a[88:91],a[56:59],v[60:63]\n"
"buffer_load_dwordx4 v112,s[24:27],s64 offen offset:2048 sc1 lds\n"
"v_mfma_f32_16x16x32_bf16 v[60:63],a[92:95],a[60:63],v[60:63]\n"
"v_exp_f32 v119,v14\n"
"v_mfma_f32_16x16x32_bf16 v[56:59],a[76:79],a[60:63],v[108:111]\n"
"v_cvt_pk_bf16_f32 v70,v122,v123\n"
"v_or3_b32 v118,v118,v69,v70\n"
"v_mfma_f32_16x16x32_bf16 v[56:59],a[72:75],a[56:59],v[56:59]\n"
"v_exp_f32 v120,v15\n"
"v_mfma_f32_16x16x32_bf16 v[56:59],a[68:71],a[52:55],v[56:59]\n"
"buffer_load_dwordx4 v112,s[24:27],s64 offen offset:3072 sc1 lds\n"
"v_mfma_f32_16x16x32_bf16 v[56:59],a[64:67],a[48:51],v[56:59]\n"
"s_waitcnt lgkmcnt(0)\n"
"v_exp_f32 v122,v16\n"
"v_mfma_f32_16x16x32_bf16 v[128:131],a[96:99],v[80:83],v[128:131]\n"
"v_exp_f32 v123,v17\n"
"v_mfma_f32_16x16x32_bf16 v[160:163],a[96:99],v[84:87],v[160:163]\n"
"ds_read_b128 a[64:67],v113 offset:49152\n"
"v_cvt_pk_bf16_f32 v71,v119,v120\n"
"v_mfma_f32_16x16x32_bf16 v[192:195],a[96:99],v[88:91],v[192:195]\n"
"s_add_u32 m0,s67,20480\n"
"v_exp_f32 v119,v18\n"
"v_mfma_f32_16x16x32_bf16 v[224:227],a[96:99],v[92:95],v[224:227]\n"
"ds_read_b128 a[68:71],v113 offset:50176\n"
"v_cvt_pk_bf16_f32 v72,v122,v123\n"
"v_mfma_f32_16x16x32_bf16 a[140:143],a[144:147],v[92:95],a[140:143]\n"
"v_exp_f32 v120,v19\n"
"v_mfma_f32_16x16x32_bf16 v[228:231],a[100:103],v[92:95],v[228:231]\n"
"ds_read_b128 a[72:75],v113 offset:51200\n"
"ds_read_b128 a[76:79],v113 offset:52224\n"
"v_mfma_f32_16x16x32_bf16 v[132:135],a[100:103],v[80:83],v[132:135]\n"
"v_exp_f32 v122,v20\n"
"v_mfma_f32_16x16x32_bf16 v[164:167],a[100:103],v[84:87],v[164:167]\n"
"ds_read_b128 a[80:83],v113 offset:53248\n"
"v_cvt_pk_bf16_f32 v73,v119,v120\n"
"v_mfma_f32_16x16x32_bf16 v[196:199],a[100:103],v[88:91],v[196:199]\n"
"v_exp_f32 v123,v21\n"
"v_mfma_f32_16x16x32_bf16 a[136:139],a[144:147],v[88:91],a[136:139]\n"
"ds_read_b128 a[84:87],v113 offset:54272\n"
"ds_read_b128 a[88:91],v113 offset:55296\n"
"v_mfma_f32_16x16x32_bf16 v[200:203],a[104:107],v[88:91],v[200:203]\n"
"v_exp_f32 v119,v22\n"
"v_mfma_f32_16x16x32_bf16 v[232:235],a[104:107],v[92:95],v[232:235]\n"
"ds_read_b128 a[92:95],v113 offset:56320\n"
"v_cvt_pk_bf16_f32 v74,v122,v123\n"
"v_mfma_f32_16x16x32_bf16 v[136:139],a[104:107],v[80:83],v[136:139]\n"
"v_exp_f32 v120,v23\n"
"v_mfma_f32_16x16x32_bf16 v[168:171],a[104:107],v[84:87],v[168:171]\n"
"s_waitcnt vmcnt(16)\n"
"ds_add_u32 v124,a152\n"
"v_mfma_f32_16x16x32_bf16 a[132:135],a[144:147],v[84:87],a[132:135]\n"
"v_exp_f32 v122,v24\n"
"v_mfma_f32_16x16x32_bf16 v[172:175],a[108:111],v[84:87],v[172:175]\n"
"v_cvt_pk_bf16_f32 v75,v119,v120\n"
"v_or3_b32 v118,v118,v71,v72\n"
"v_mfma_f32_16x16x32_bf16 v[204:207],a[108:111],v[88:91],v[204:207]\n"
"v_exp_f32 v123,v25\n"
"v_mfma_f32_16x16x32_bf16 v[236:239],a[108:111],v[92:95],v[236:239]\n"
"buffer_load_dwordx4 v117,s[24:27],s64 offen offset:0 sc1 lds\n"
"v_mfma_f32_16x16x32_bf16 v[140:143],a[108:111],v[80:83],v[140:143]\n"
"v_exp_f32 v119,v26\n"
"v_mfma_f32_16x16x32_bf16 a[128:131],a[144:147],v[80:83],a[128:131]\n"
"v_cvt_pk_bf16_f32 v76,v122,v123\n"
"v_or3_b32 v118,v118,v73,v74\n"
"v_mfma_f32_16x16x32_bf16 v[144:147],a[112:115],v[80:83],v[144:147]\n"
"v_exp_f32 v120,v27\n"
"v_mfma_f32_16x16x32_bf16 v[176:179],a[112:115],v[84:87],v[176:179]\n"
"buffer_load_dwordx4 v117,s[24:27],s64 offen offset:1024 sc1 lds\n"
"v_mfma_f32_16x16x32_bf16 v[208:211],a[112:115],v[88:91],v[208:211]\n"
"v_exp_f32 v122,v28\n"
"v_mfma_f32_16x16x32_bf16 v[240:243],a[112:115],v[92:95],v[240:243]\n"
"v_cvt_pk_bf16_f32 v77,v119,v120\n"
"v_or3_b32 v118,v118,v75,v76\n"
"v_mfma_f32_16x16x32_bf16 v[244:247],a[116:119],v[92:95],v[244:247]\n"
"v_exp_f32 v123,v29\n"
"v_mfma_f32_16x16x32_bf16 v[148:151],a[116:119],v[80:83],v[148:151]\n"
"buffer_load_dwordx4 v117,s[24:27],s64 offen offset:2048 sc1 lds\n"
"v_mfma_f32_16x16x32_bf16 v[180:183],a[116:119],v[84:87],v[180:183]\n"
"v_exp_f32 v119,v30\n"
"v_mfma_f32_16x16x32_bf16 v[212:215],a[116:119],v[88:91],v[212:215]\n"
"ds_read_b32 v125,v124\n"
"v_cvt_pk_bf16_f32 v78,v122,v123\n"
"v_mfma_f32_16x16x32_bf16 v[216:219],a[120:123],v[88:91],v[216:219]\n"
"v_exp_f32 v120,v31\n"
"v_mfma_f32_16x16x32_bf16 v[248:251],a[120:123],v[92:95],v[248:251]\n"
"buffer_load_dwordx4 v117,s[24:27],s64 offen offset:3072 sc1 lds\n"
"v_mfma_f32_16x16x32_bf16 v[152:155],a[120:123],v[80:83],v[152:155]\n"
"buffer_store_dwordx4 a[244:247],v112,s[24:27],s56 offen offset:1024 sc1\n"
"v_mfma_f32_16x16x32_bf16 v[184:187],a[120:123],v[84:87],v[184:187]\n"
"s_add_u32 s64,s64,16384\n"
"v_cvt_pk_bf16_f32 v79,v119,v120\n"
"v_or3_b32 v118,v118,v77,v78\n"
"v_mfma_f32_16x16x32_bf16 v[188:191],a[124:127],v[84:87],v[188:191]\n"
"s_waitcnt lgkmcnt(0)\n"
"v_subrev_u32 v125,s101,v125\n"
"v_or3_b32 v125,v118,v79,v125\n"
"v_mfma_f32_16x16x32_bf16 v[220:223],a[124:127],v[88:91],v[220:223]\n"
"v_and_b32 v125,0x40004000,v125\n"
"v_cmp_ne_u32 vcc,0,v125\n"
"v_mfma_f32_16x16x32_bf16 v[252:255],a[124:127],v[92:95],v[252:255]\n"
"v_mfma_f32_16x16x32_bf16 v[156:159],a[124:127],v[80:83],v[156:159]\n"
"s_cbranch_vccnz .Lfk_cs_g0\n"
".Lfk_cr_g0:\n"
"v_mfma_f32_16x16x32_bf16 v[0:3],a[64:67],a[0:3],v[96:99]\n"
"s_add_u32 s101,s101,4\n"
"v_exp_f32 v122,v32\n"
"v_mfma_f32_16x16x32_bf16 v[0:3],a[68:71],a[4:7],v[0:3]\n"
"s_add_u32 m0,s55,8192\n"
"v_exp_f32 v123,v33\n"
"v_mfma_f32_16x16x32_bf16 v[0:3],a[72:75],a[8:11],v[0:3]\n"
"ds_read_b128 a[96:99],v114 offset:40960\n"
"ds_read_b128 a[100:103],v114 offset:41984\n"
"v_mfma_f32_16x16x32_bf16 v[0:3],a[76:79],a[12:15],v[0:3]\n"
"s_add_u32 s54,s54,s22\n"
"v_exp_f32 v119,v34\n"
"v_mfma_f32_16x16x32_bf16 v[4:7],a[92:95],a[12:15],v[96:99]\n"
"ds_read_b128 a[104:107],v114 offset:43008\n"
"v_cvt_pk_bf16_f32 v80,v122,v123\n"
"v_mfma_f32_16x16x32_bf16 v[4:7],a[88:91],a[8:11],v[4:7]\n"
"v_exp_f32 v120,v35\n"
"v_mfma_f32_16x16x32_bf16 v[4:7],a[84:87],a[4:7],v[4:7]\n"
"ds_read_b128 a[108:111],v114 offset:44032\n"
"ds_read_b128 a[112:115],v114 offset:45056\n"
"v_mfma_f32_16x16x32_bf16 v[4:7],a[80:83],a[0:3],v[4:7]\n"
"v_exp_f32 v122,v36\n"
"v_mfma_f32_16x16x32_bf16 v[12:15],a[80:83],a[16:19],v[100:103]\n"
"ds_read_b128 a[116:119],v114 offset:46080\n"
"v_cvt_pk_bf16_f32 v81,v119,v120\n"
"v_mfma_f32_16x16x32_bf16 v[12:15],a[84:87],a[20:23],v[12:15]\n"
"v_exp_f32 v123,v37\n"
"v_mfma_f32_16x16x32_bf16 v[12:15],a[88:91],a[24:27],v[12:15]\n"
"ds_read_b128 a[120:123],v114 offset:47104\n"
"ds_read_b128 a[124:127],v114 offset:48128\n"
"v_mfma_f32_16x16x32_bf16 v[12:15],a[92:95],a[28:31],v[12:15]\n"
"v_exp_f32 v119,v38\n"
"v_mfma_f32_16x16x32_bf16 v[8:11],a[76:79],a[28:31],v[100:103]\n"
"v_cvt_pk_bf16_f32 v82,v122,v123\n"
"v_or3_b32 v118,v80,v81,v82\n"
"v_mfma_f32_16x16x32_bf16 v[8:11],a[72:75],a[24:27],v[8:11]\n"
"v_exp_f32 v120,v39\n"
"v_mfma_f32_16x16x32_bf16 v[8:11],a[68:71],a[20:23],v[8:11]\n"
"buffer_load_dwordx4 v115,s[32:35],s54 offen sc1 lds\n"
"v_mfma_f32_16x16x32_bf16 v[8:11],a[64:67],a[16:19],v[8:11]\n"
"s_add_u32 m0,s67,49152\n"
"v_exp_f32 v122,v40\n"
"v_mfma_f32_16x16x32_bf16 v[16:19],a[64:67],a[32:35],v[104:107]\n"
"v_cvt_pk_bf16_f32 v83,v119,v120\n"
"v_mfma_f32_16x16x32_bf16 v[16:19],a[68:71],a[36:39],v[16:19]\n"
"v_exp_f32 v123,v41\n"
"v_mfma_f32_16x16x32_bf16 v[16:19],a[72:75],a[40:43],v[16:19]\n"
"buffer_load_dwordx4 v112,s[24:27],s64 offen offset:0 sc1 lds\n"
"v_mfma_f32_16x16x32_bf16 v[16:19],a[76:79],a[44:47],v[16:19]\n"
"v_exp_f32 v119,v42\n"
"v_mfma_f32_16x16x32_bf16 v[20:23],a[92:95],a[44:47],v[104:107]\n"
"v_cvt_pk_bf16_f32 v84,v122,v123\n"
"v_or3_b32 v118,v118,v83,v84\n"
"v_mfma_f32_16x16x32_bf16 v[20:23],a[88:91],a[40:43],v[20:23]\n"
"v_exp_f32 v120,v43\n"
"v_mfma_f32_16x16x32_bf16 v[20:23],a[84:87],a[36:39],v[20:23]\n"
"buffer_load_dwordx4 v112,s[24:27],s64 offen offset:1024 sc1 lds\n"
"v_mfma_f32_16x16x32_bf16 v[20:23],a[80:83],a[32:35],v[20:23]\n"
"v_exp_f32 v122,v44\n"
"v_mfma_f32_16x16x32_bf16 v[28:31],a[80:83],a[48:51],v[108:111]\n"
"v_cvt_pk_bf16_f32 v85,v119,v120\n"
"v_mfma_f32_16x16x32_bf16 v[28:31],a[84:87],a[52:55],v[28:31]\n"
"v_exp_f32 v123,v45\n"
"v_mfma_f32_16x16x32_bf16 v[28:31],a[88:91],a[56:59],v[28:31]\n"
"buffer_load_dwordx4 v112,s[24:27],s64 offen offset:2048 sc1 lds\n"
"v_mfma_f32_16x16x32_bf16 v[28:31],a[92:95],a[60:63],v[28:31]\n"
"v_exp_f32 v119,v46\n"
"v_mfma_f32_16x16x32_bf16 v[24:27],a[76:79],a[60:63],v[108:111]\n"
"v_cvt_pk_bf16_f32 v86,v122,v123\n"
"v_or3_b32 v118,v118,v85,v86\n"
"v_mfma_f32_16x16x32_bf16 v[24:27],a[72:75],a[56:59],v[24:27]\n"
"v_exp_f32 v120,v47\n"
"v_mfma_f32_16x16x32_bf16 v[24:27],a[68:71],a[52:55],v[24:27]\n"
"buffer_load_dwordx4 v112,s[24:27],s64 offen offset:3072 sc1 lds\n"
"v_mfma_f32_16x16x32_bf16 v[24:27],a[64:67],a[48:51],v[24:27]\n"
"s_waitcnt lgkmcnt(0)\n"
"v_exp_f32 v122,v48\n"
"v_mfma_f32_16x16x32_bf16 v[128:131],a[96:99],v[64:67],v[128:131]\n"
"v_exp_f32 v123,v49\n"
"v_mfma_f32_16x16x32_bf16 v[160:163],a[96:99],v[68:71],v[160:163]\n"
"ds_read_b128 a[64:67],v113 offset:32768\n"
"v_cvt_pk_bf16_f32 v87,v119,v120\n"
"v_mfma_f32_16x16x32_bf16 v[192:195],a[96:99],v[72:75],v[192:195]\n"
"s_add_u32 m0,s67,53248\n"
"v_exp_f32 v119,v50\n"
"v_mfma_f32_16x16x32_bf16 v[224:227],a[96:99],v[76:79],v[224:227]\n"
"ds_read_b128 a[68:71],v113 offset:33792\n"
"v_cvt_pk_bf16_f32 v88,v122,v123\n"
"v_mfma_f32_16x16x32_bf16 a[140:143],a[144:147],v[76:79],a[140:143]\n"
"v_exp_f32 v120,v51\n"
"v_mfma_f32_16x16x32_bf16 v[228:231],a[100:103],v[76:79],v[228:231]\n"
"ds_read_b128 a[72:75],v113 offset:34816\n"
"ds_read_b128 a[76:79],v113 offset:35840\n"
"v_mfma_f32_16x16x32_bf16 v[132:135],a[100:103],v[64:67],v[132:135]\n"
"v_exp_f32 v122,v52\n"
"v_mfma_f32_16x16x32_bf16 v[164:167],a[100:103],v[68:71],v[164:167]\n"
"ds_read_b128 a[80:83],v113 offset:36864\n"
"v_cvt_pk_bf16_f32 v89,v119,v120\n"
"v_mfma_f32_16x16x32_bf16 v[196:199],a[100:103],v[72:75],v[196:199]\n"
"v_exp_f32 v123,v53\n"
"v_mfma_f32_16x16x32_bf16 a[136:139],a[144:147],v[72:75],a[136:139]\n"
"ds_read_b128 a[84:87],v113 offset:37888\n"
"ds_read_b128 a[88:91],v113 offset:38912\n"
"v_mfma_f32_16x16x32_bf16 v[200:203],a[104:107],v[72:75],v[200:203]\n"
"v_exp_f32 v119,v54\n"
"v_mfma_f32_16x16x32_bf16 v[232:235],a[104:107],v[76:79],v[232:235]\n"
"ds_read_b128 a[92:95],v113 offset:39936\n"
"v_cvt_pk_bf16_f32 v90,v122,v123\n"
"v_mfma_f32_16x16x32_bf16 v[136:139],a[104:107],v[64:67],v[136:139]\n"
"v_exp_f32 v120,v55\n"
"v_mfma_f32_16x16x32_bf16 v[168:171],a[104:107],v[68:71],v[168:171]\n"
"s_waitcnt vmcnt(16)\n"
"ds_add_u32 v124,a152\n"
"v_mfma_f32_16x16x32_bf16 a[132:135],a[144:147],v[68:71],a[132:135]\n"
"v_exp_f32 v122,v56\n"
"v_mfma_f32_16x16x32_bf16 v[172:175],a[108:111],v[68:71],v[172:175]\n"
"v_cvt_pk_bf16_f32 v91,v119,v120\n"
"v_or3_b32 v118,v118,v87,v88\n"
"v_mfma_f32_16x16x32_bf16 v[204:207],a[108:111],v[72:75],v[204:207]\n"
"v_exp_f32 v123,v57\n"
"v_mfma_f32_16x16x32_bf16 v[236:239],a[108:111],v[76:79],v[236:239]\n"
"buffer_load_dwordx4 v117,s[24:27],s64 offen offset:0 sc1 lds\n"
"v_mfma_f32_16x16x32_bf16 v[140:143],a[108:111],v[64:67],v[140:143]\n"
"v_exp_f32 v119,v58\n"
"v_mfma_f32_16x16x32_bf16 a[128:131],a[144:147],v[64:67],a[128:131]\n"
"v_cvt_pk_bf16_f32 v92,v122,v123\n"
"v_or3_b32 v118,v118,v89,v90\n"
"v_mfma_f32_16x16x32_bf16 v[144:147],a[112:115],v[64:67],v[144:147]\n"
"v_exp_f32 v120,v59\n"
"v_mfma_f32_16x16x32_bf16 v[176:179],a[112:115],v[68:71],v[176:179]\n"
"buffer_load_dwordx4 v117,s[24:27],s64 offen offset:1024 sc1 lds\n"
"v_mfma_f32_16x16x32_bf16 v[208:211],a[112:115],v[72:75],v[208:211]\n"
"v_exp_f32 v122,v60\n"
"v_mfma_f32_16x16x32_bf16 v[240:243],a[112:115],v[76:79],v[240:243]\n"
"v_cvt_pk_bf16_f32 v93,v119,v120\n"
"v_or3_b32 v118,v118,v91,v92\n"
"v_mfma_f32_16x16x32_bf16 v[244:247],a[116:119],v[76:79],v[244:247]\n"
"v_exp_f32 v123,v61\n"
"v_mfma_f32_16x16x32_bf16 v[148:151],a[116:119],v[64:67],v[148:151]\n"
"buffer_load_dwordx4 v117,s[24:27],s64 offen offset:2048 sc1 lds\n"
"v_mfma_f32_16x16x32_bf16 v[180:183],a[116:119],v[68:71],v[180:183]\n"
"v_exp_f32 v119,v62\n"
"v_mfma_f32_16x16x32_bf16 v[212:215],a[116:119],v[72:75],v[212:215]\n"
"ds_read_b32 v125,v124\n"
"v_cvt_pk_bf16_f32 v94,v122,v123\n"
"v_mfma_f32_16x16x32_bf16 v[216:219],a[120:123],v[72:75],v[216:219]\n"
"v_exp_f32 v120,v63\n"
"v_mfma_f32_16x16x32_bf16 v[248:251],a[120:123],v[76:79],v[248:251]\n"
"buffer_load_dwordx4 v117,s[24:27],s64 offen offset:3072 sc1 lds\n"
"v_mfma_f32_16x16x32_bf16 v[152:155],a[120:123],v[64:67],v[152:155]\n"
"buffer_store_dwordx4 a[248:251],v112,s[24:27],s56 offen offset:2048 sc1\n"
"v_mfma_f32_16x16x32_bf16 v[184:187],a[120:123],v[68:71],v[184:187]\n"
"s_add_u32 s64,s64,16384\n"
"v_cvt_pk_bf16_f32 v95,v119,v120\n"
"v_or3_b32 v118,v118,v93,v94\n"
"v_mfma_f32_16x16x32_bf16 v[188:191],a[124:127],v[68:71],v[188:191]\n"
"s_waitcnt lgkmcnt(0)\n"
"v_subrev_u32 v125,s101,v125\n"
"v_or3_b32 v125,v118,v95,v125\n"
"v_mfma_f32_16x16x32_bf16 v[220:223],a[124:127],v[72:75],v[220:223]\n"
"v_and_b32 v125,0x40004000,v125\n"
"v_cmp_ne_u32 vcc,0,v125\n"
"v_mfma_f32_16x16x32_bf16 v[252:255],a[124:127],v[76:79],v[252:255]\n"
"v_mfma_f32_16x16x32_bf16 v[156:159],a[124:127],v[64:67],v[156:159]\n"
"s_cbranch_vccnz .Lfk_cs_g1\n"
".Lfk_cr_g1:\n"
"v_mfma_f32_16x16x32_bf16 v[32:35],a[64:67],a[0:3],v[96:99]\n"
"s_add_u32 s101,s101,4\n"
"v_exp_f32 v122,v0\n"
"v_mfma_f32_16x16x32_bf16 v[32:35],a[68:71],a[4:7],v[32:35]\n"
"s_add_u32 m0,s55,12288\n"
"v_exp_f32 v123,v1\n"
"v_mfma_f32_16x16x32_bf16 v[32:35],a[72:75],a[8:11],v[32:35]\n"
"ds_read_b128 a[96:99],v114 offset:8192\n"
"ds_read_b128 a[100:103],v114 offset:9216\n"
"v_mfma_f32_16x16x32_bf16 v[32:35],a[76:79],a[12:15],v[32:35]\n"
"s_add_u32 s54,s54,s22\n"
"v_exp_f32 v119,v2\n"
"v_mfma_f32_16x16x32_bf16 v[36:39],a[92:95],a[12:15],v[96:99]\n"
"ds_read_b128 a[104:107],v114 offset:10240\n"
"v_cvt_pk_bf16_f32 v64,v122,v123\n"
"v_mfma_f32_16x16x32_bf16 v[36:39],a[88:91],a[8:11],v[36:39]\n"
"v_exp_f32 v120,v3\n"
"v_mfma_f32_16x16x32_bf16 v[36:39],a[84:87],a[4:7],v[36:39]\n"
"ds_read_b128 a[108:111],v114 offset:11264\n"
"ds_read_b128 a[112:115],v114 offset:12288\n"
"v_mfma_f32_16x16x32_bf16 v[36:39],a[80:83],a[0:3],v[36:39]\n"
"v_exp_f32 v122,v4\n"
"v_mfma_f32_16x16x32_bf16 v[44:47],a[80:83],a[16:19],v[100:103]\n"
"ds_read_b128 a[116:119],v114 offset:13312\n"
"v_cvt_pk_bf16_f32 v65,v119,v120\n"
"v_mfma_f32_16x16x32_bf16 v[44:47],a[84:87],a[20:23],v[44:47]\n"
"v_exp_f32 v123,v5\n"
"v_mfma_f32_16x16x32_bf16 v[44:47],a[88:91],a[24:27],v[44:47]\n"
"ds_read_b128 a[120:123],v114 offset:14336\n"
"ds_read_b128 a[124:127],v114 offset:15360\n"
"v_mfma_f32_16x16x32_bf16 v[44:47],a[92:95],a[28:31],v[44:47]\n"
"v_exp_f32 v119,v6\n"
"v_mfma_f32_16x16x32_bf16 v[40:43],a[76:79],a[28:31],v[100:103]\n"
"v_cvt_pk_bf16_f32 v66,v122,v123\n"
"v_or3_b32 v118,v64,v65,v66\n"
"v_mfma_f32_16x16x32_bf16 v[40:43],a[72:75],a[24:27],v[40:43]\n"
"v_exp_f32 v120,v7\n"
"v_mfma_f32_16x16x32_bf16 v[40:43],a[68:71],a[20:23],v[40:43]\n"
"buffer_load_dwordx4 v115,s[32:35],s54 offen sc1 lds\n"
"v_mfma_f32_16x16x32_bf16 v[40:43],a[64:67],a[16:19],v[40:43]\n"
"s_cmp_eq_u32 s64,s65\n"
"v_exp_f32 v122,v8\n"
"v_mfma_f32_16x16x32_bf16 v[48:51],a[64:67],a[32:35],v[104:107]\n"
"v_cvt_pk_bf16_f32 v67,v119,v120\n"
"s_cselect_b32 s64,s66,s64\n"
"v_mfma_f32_16x16x32_bf16 v[48:51],a[68:71],a[36:39],v[48:51]\n"
"s_add_u32 m0,s67,32768\n"
"v_exp_f32 v123,v9\n"
"v_mfma_f32_16x16x32_bf16 v[48:51],a[72:75],a[40:43],v[48:51]\n"
"buffer_load_dwordx4 v112,s[24:27],s64 offen offset:0 sc1 lds\n"
"v_mfma_f32_16x16x32_bf16 v[48:51],a[76:79],a[44:47],v[48:51]\n"
"v_exp_f32 v119,v10\n"
"v_mfma_f32_16x16x32_bf16 v[52:55],a[92:95],a[44:47],v[104:107]\n"
"v_cvt_pk_bf16_f32 v68,v122,v123\n"
"v_or3_b32 v118,v118,v67,v68\n"
"v_mfma_f32_16x16x32_bf16 v[52:55],a[88:91],a[40:43],v[52:55]\n"
"v_exp_f32 v120,v11\n"
"v_mfma_f32_16x16x32_bf16 v[52:55],a[84:87],a[36:39],v[52:55]\n"
"buffer_load_dwordx4 v112,s[24:27],s64 offen offset:1024 sc1 lds\n"
"v_mfma_f32_16x16x32_bf16 v[52:55],a[80:83],a[32:35],v[52:55]\n"
"v_exp_f32 v122,v12\n"
"v_mfma_f32_16x16x32_bf16 v[60:63],a[80:83],a[48:51],v[108:111]\n"
"v_cvt_pk_bf16_f32 v69,v119,v120\n"
"v_mfma_f32_16x16x32_bf16 v[60:63],a[84:87],a[52:55],v[60:63]\n"
"v_exp_f32 v123,v13\n"
"v_mfma_f32_16x16x32_bf16 v[60:63],a[88:91],a[56:59],v[60:63]\n"
"buffer_load_dwordx4 v112,s[24:27],s64 offen offset:2048 sc1 lds\n"
"v_mfma_f32_16x16x32_bf16 v[60:63],a[92:95],a[60:63],v[60:63]\n"
"v_exp_f32 v119,v14\n"
"v_mfma_f32_16x16x32_bf16 v[56:59],a[76:79],a[60:63],v[108:111]\n"
"v_cvt_pk_bf16_f32 v70,v122,v123\n"
"v_or3_b32 v118,v118,v69,v70\n"
"v_mfma_f32_16x16x32_bf16 v[56:59],a[72:75],a[56:59],v[56:59]\n"
"v_exp_f32 v120,v15\n"
"v_mfma_f32_16x16x32_bf16 v[56:59],a[68:71],a[52:55],v[56:59]\n"
"buffer_load_dwordx4 v112,s[24:27],s64 offen offset:3072 sc1 lds\n"
"v_mfma_f32_16x16x32_bf16 v[56:59],a[64:67],a[48:51],v[56:59]\n"
"s_waitcnt lgkmcnt(0)\n"
"v_exp_f32 v122,v16\n"
"v_mfma_f32_16x16x32_bf16 v[128:131],a[96:99],v[80:83],v[128:131]\n"
"v_exp_f32 v123,v17\n"
"v_mfma_f32_16x16x32_bf16 v[160:163],a[96:99],v[84:87],v[160:163]\n"
"ds_read_b128 a[64:67],v113 offset:0\n"
"v_cvt_pk_bf16_f32 v71,v119,v120\n"
"v_mfma_f32_16x16x32_bf16 v[192:195],a[96:99],v[88:91],v[192:195]\n"
"s_add_u32 m0,s67,36864\n"
"v_exp_f32 v119,v18\n"
"v_mfma_f32_16x16x32_bf16 v[224:227],a[96:99],v[92:95],v[224:227]\n"
"ds_read_b128 a[68:71],v113 offset:1024\n"
"v_cvt_pk_bf16_f32 v72,v122,v123\n"
"v_mfma_f32_16x16x32_bf16 a[140:143],a[144:147],v[92:95],a[140:143]\n"
"v_exp_f32 v120,v19\n"
"v_mfma_f32_16x16x32_bf16 v[228:231],a[100:103],v[92:95],v[228:231]\n"
"ds_read_b128 a[72:75],v113 offset:2048\n"
"ds_read_b128 a[76:79],v113 offset:3072\n"
"v_mfma_f32_16x16x32_bf16 v[132:135],a[100:103],v[80:83],v[132:135]\n"
"v_exp_f32 v122,v20\n"
"v_mfma_f32_16x16x32_bf16 v[164:167],a[100:103],v[84:87],v[164:167]\n"
"ds_read_b128 a[80:83],v113 offset:4096\n"
"v_cvt_pk_bf16_f32 v73,v119,v120\n"
"v_mfma_f32_16x16x32_bf16 v[196:199],a[100:103],v[88:91],v[196:199]\n"
"v_exp_f32 v123,v21\n"
"v_mfma_f32_16x16x32_bf16 a[136:139],a[144:147],v[88:91],a[136:139]\n"
"ds_read_b128 a[84:87],v113 offset:5120\n"
"ds_read_b128 a[88:91],v113 offset:6144\n"
"v_mfma_f32_16x16x32_bf16 v[200:203],a[104:107],v[88:91],v[200:203]\n"
"v_exp_f32 v119,v22\n"
"v_mfma_f32_16x16x32_bf16 v[232:235],a[104:107],v[92:95],v[232:235]\n"
"ds_read_b128 a[92:95],v113 offset:7168\n"
"v_cvt_pk_bf16_f32 v74,v122,v123\n"
"v_mfma_f32_16x16x32_bf16 v[136:139],a[104:107],v[80:83],v[136:139]\n"
"v_exp_f32 v120,v23\n"
"v_mfma_f32_16x16x32_bf16 v[168:171],a[104:107],v[84:87],v[168:171]\n"
"s_waitcnt vmcnt(16)\n"
"ds_add_u32 v124,a152\n"
"v_mfma_f32_16x16x32_bf16 a[132:135],a[144:147],v[84:87],a[132:135]\n"
"v_exp_f32 v122,v24\n"
"v_mfma_f32_16x16x32_bf16 v[172:175],a[108:111],v[84:87],v[172:175]\n"
"v_cvt_pk_bf16_f32 v75,v119,v120\n"
"v_or3_b32 v118,v118,v71,v72\n"
"v_mfma_f32_16x16x32_bf16 v[204:207],a[108:111],v[88:91],v[204:207]\n"
"v_exp_f32 v123,v25\n"
"v_mfma_f32_16x16x32_bf16 v[236:239],a[108:111],v[92:95],v[236:239]\n"
"buffer_load_dwordx4 v117,s[24:27],s64 offen offset:0 sc1 lds\n"
"v_mfma_f32_16x16x32_bf16 v[140:143],a[108:111],v[80:83],v[140:143]\n"
"v_exp_f32 v119,v26\n"
"v_mfma_f32_16x16x32_bf16 a[128:131],a[144:147],v[80:83],a[128:131]\n"
"v_cvt_pk_bf16_f32 v76,v122,v123\n"
"v_or3_b32 v118,v118,v73,v74\n"
"v_mfma_f32_16x16x32_bf16 v[144:147],a[112:115],v[80:83],v[144:147]\n"
"v_exp_f32 v120,v27\n"
"v_mfma_f32_16x16x32_bf16 v[176:179],a[112:115],v[84:87],v[176:179]\n"
"buffer_load_dwordx4 v117,s[24:27],s64 offen offset:1024 sc1 lds\n"
"v_mfma_f32_16x16x32_bf16 v[208:211],a[112:115],v[88:91],v[208:211]\n"
"v_exp_f32 v122,v28\n"
"v_mfma_f32_16x16x32_bf16 v[240:243],a[112:115],v[92:95],v[240:243]\n"
"v_cvt_pk_bf16_f32 v77,v119,v120\n"
"v_or3_b32 v118,v118,v75,v76\n"
"v_mfma_f32_16x16x32_bf16 v[244:247],a[116:119],v[92:95],v[244:247]\n"
"v_exp_f32 v123,v29\n"
"v_mfma_f32_16x16x32_bf16 v[148:151],a[116:119],v[80:83],v[148:151]\n"
"buffer_load_dwordx4 v117,s[24:27],s64 offen offset:2048 sc1 lds\n"
"v_mfma_f32_16x16x32_bf16 v[180:183],a[116:119],v[84:87],v[180:183]\n"
"v_exp_f32 v119,v30\n"
"v_mfma_f32_16x16x32_bf16 v[212:215],a[116:119],v[88:91],v[212:215]\n"
"ds_read_b32 v125,v124\n"
"v_cvt_pk_bf16_f32 v78,v122,v123\n"
"v_mfma_f32_16x16x32_bf16 v[216:219],a[120:123],v[88:91],v[216:219]\n"
"v_exp_f32 v120,v31\n"
"v_mfma_f32_16x16x32_bf16 v[248:251],a[120:123],v[92:95],v[248:251]\n"
"buffer_load_dwordx4 v117,s[24:27],s64 offen offset:3072 sc1 lds\n"
"v_mfma_f32_16x16x32_bf16 v[152:155],a[120:123],v[80:83],v[152:155]\n"
"buffer_store_dwordx4 a[252:255],v112,s[24:27],s56 offen offset:3072 sc1\n"
"v_mfma_f32_16x16x32_bf16 v[184:187],a[120:123],v[84:87],v[184:187]\n"
"s_add_u32 s64,s64,16384\n"
"s_add_u32 s89,s89,1\n"
"v_cvt_pk_bf16_f32 v79,v119,v120\n"
"v_mfma_f32_16x16x32_bf16 v[188:191],a[124:127],v[84:87],v[188:191]\n"
"s_waitcnt lgkmcnt(0)\n"
"v_subrev_u32 v125,s101,v125\n"
"v_or3_b32 v118,v118,v77,v78\n"
"v_mfma_f32_16x16x32_bf16 v[220:223],a[124:127],v[88:91],v[220:223]\n"
"v_or3_b32 v125,v118,v79,v125\n"
"v_and_b32 v125,0x40004000,v125\n"
"v_mfma_f32_16x16x32_bf16 v[252:255],a[124:127],v[92:95],v[252:255]\n"
"v_cmp_ne_u32 vcc,0,v125\n"
"v_mfma_f32_16x16x32_bf16 v[156:159],a[124:127],v[80:83],v[156:159]\n"
"s_cbranch_vccnz .Lfk_cs_g2\n"
".Lfk_cr_g2:\n"
".Lfk_Hx:\n"
"s_cmp_lt_u32 s89,8\n"
"s_cbranch_scc1 .Lfk_H\n"
"v_max3_f32 v118,v32,v33,v34\n"
"v_max3_f32 v119,v35,v36,v37\n"
"v_max3_f32 v119,v119,v38,v39\n"
"v_max3_f32 v118,v118,v40,v41\n"
"v_max3_f32 v119,v119,v42,v43\n"
"v_max3_f32 v118,v118,v44,v45\n"
"v_max3_f32 v119,v119,v46,v47\n"
"v_max3_f32 v118,v118,v48,v49\n"
"v_max3_f32 v119,v119,v50,v51\n"
"v_max3_f32 v118,v118,v52,v53\n"
"v_max3_f32 v119,v119,v54,v55\n"
"v_max3_f32 v118,v118,v56,v57\n"
"v_max3_f32 v119,v119,v58,v59\n"
"v_max3_f32 v118,v118,v60,v61\n"
"v_max3_f32 v119,v119,v62,v63\n"
"v_max_f32 v118,v118,v119\n"
"v_cmp_lt_f32 vcc,s93,v118\n"
"s_nop 0\n"
"s_cbranch_vccnz .Lfk_cs_x1\n"
".Lfk_cr_x1:\n"
"s_cmp_ge_u32 s52,2\n"
"s_cbranch_scc1 .Lfk_tailB\n"
".Lfk_tailA:\n"
"s_add_u32 s82,s66,28672\n"
"s_cmp_eq_u32 s52,0\n"
"s_cselect_b32 s67,77824,s67\n"
"s_cselect_b32 s64,s82,s64\n"
"s_waitcnt vmcnt(11) lgkmcnt(0)\n"
"s_barrier\n"
"v_mfma_f32_16x16x32_bf16 v[16:19],a[64:67],a[32:35],v[104:107]\n"
"ds_read_b128 a[96:99],v114 offset:24576\n"
"ds_read_b128 a[100:103],v114 offset:25600\n"
"ds_read_b128 a[104:107],v114 offset:26624\n"
"v_mfma_f32_16x16x32_bf16 v[16:19],a[68:71],a[36:39],v[16:19]\n"
"ds_read_b128 a[108:111],v114 offset:27648\n"
"ds_read_b128 a[112:115],v114 offset:28672\n"
"ds_read_b128 a[116:119],v114 offset:29696\n"
"v_mfma_f32_16x16x32_bf16 v[16:19],a[72:75],a[40:43],v[16:19]\n"
"ds_read_b128 a[120:123],v114 offset:30720\n"
"ds_read_b128 a[124:127],v114 offset:31744\n"
"v_add_f32 v8,0x80000000,v126\n"
"v_mfma_f32_16x16x32_bf16 v[16:19],a[76:79],a[44:47],v[16:19]\n"
"v_min_f32 v8,v8,v100\n"
"v_add_f32 v9,0xf149f2ca,v126\n"
"v_min_f32 v9,v9,v101\n"
"v_mfma_f32_16x16x32_bf16 v[20:23],a[92:95],a[44:47],v[104:107]\n"
"v_add_f32 v10,0xf1c9f2ca,v126\n"
"v_min_f32 v10,v10,v102\n"
"v_add_f32 v11,0xf2177617,v126\n"
"v_mfma_f32_16x16x32_bf16 v[20:23],a[88:91],a[40:43],v[20:23]\n"
"v_min_f32 v11,v11,v103\n"
"v_add_f32 v12,0xf349f2ca,v126\n"
"v_min_f32 v12,v12,v100\n"
"v_mfma_f32_16x16x32_bf16 v[20:23],a[84:87],a[36:39],v[20:23]\n"
"v_add_f32 v13,0xf35691f6,v126\n"
"v_min_f32 v13,v13,v101\n"
"v_add_f32 v14,0xf3633123,v126\n"
"v_mfma_f32_16x16x32_bf16 v[20:23],a[80:83],a[32:35],v[20:23]\n"
"v_min_f32 v14,v14,v102\n"
"v_add_f32 v15,0xf36fd050,v126\n"
"v_min_f32 v15,v15,v103\n"
"v_mfma_f32_16x16x32_bf16 v[28:31],a[80:83],a[48:51],v[108:111]\n"
"s_add_u32 m0,s67,0\n"
"v_exp_f32 v32,v32\n"
"v_mfma_f32_16x16x32_bf16 v[28:31],a[84:87],a[52:55],v[28:31]\n"
"buffer_load_dwordx4 v112,s[24:27],s64 offen offset:0 sc1 lds\n"
"buffer_load_dwordx4 v112,s[24:27],s64 offen offset:1024 sc1 lds\n"
"v_exp_f32 v33,v33\n"
"v_mfma_f32_16x16x32_bf16 v[28:31],a[88:91],a[56:59],v[28:31]\n"
"v_exp_f32 v34,v34\n"
"v_cvt_pk_bf16_f32 v80,v32,v33\n"
"v_mfma_f32_16x16x32_bf16 v[28:31],a[92:95],a[60:63],v[28:31]\n"
"v_exp_f32 v35,v35\n"
"v_max3_f32 v118,v16,v17,v18\n"
"v_exp_f32 v36,v36\n"
"v_mfma_f32_16x16x32_bf16 v[24:27],a[76:79],a[60:63],v[108:111]\n"
"buffer_load_dwordx4 v112,s[24:27],s64 offen offset:2048 sc1 lds\n"
"v_mfma_f32_16x16x32_bf16 v[24:27],a[72:75],a[56:59],v[24:27]\n"
"v_exp_f32 v37,v37\n"
"v_cvt_pk_bf16_f32 v81,v34,v35\n"
"v_mfma_f32_16x16x32_bf16 v[24:27],a[68:71],a[52:55],v[24:27]\n"
"v_exp_f32 v38,v38\n"
"v_cvt_pk_bf16_f32 v82,v36,v37\n"
"v_exp_f32 v39,v39\n"
"v_mfma_f32_16x16x32_bf16 v[24:27],a[64:67],a[48:51],v[24:27]\n"
"buffer_load_dwordx4 v112,s[24:27],s64 offen offset:3072 sc1 lds\n"
"v_mfma_f32_16x16x32_bf16 v[8:11],a[64:67],a[16:19],v[8:11]\n"
"s_add_u32 s64,s64,16384\n"
"v_exp_f32 v40,v40\n"
"v_accvgpr_read_b32 v119,a150\n"
"v_mfma_f32_16x16x32_bf16 v[8:11],a[68:71],a[20:23],v[8:11]\n"
"v_exp_f32 v41,v41\n"
"v_accvgpr_read_b32 v120,a151\n"
"v_mfma_f32_16x16x32_bf16 v[8:11],a[72:75],a[24:27],v[8:11]\n"
"v_exp_f32 v42,v42\n"
"v_xor_b32 v119,s80,v119\n"
"v_mfma_f32_16x16x32_bf16 v[8:11],a[76:79],a[28:31],v[8:11]\n"
"v_exp_f32 v43,v43\n"
"v_xor_b32 v120,s81,v120\n"
"v_mfma_f32_16x16x32_bf16 v[12:15],a[92:95],a[28:31],v[12:15]\n"
"v_exp_f32 v44,v44\n"
"v_or_b32 v119,v119,v120\n"
"v_mfma_f32_16x16x32_bf16 v[12:15],a[88:91],a[24:27],v[12:15]\n"
"s_add_u32 s18,s8,0\n"
"v_exp_f32 v45,v45\n"
"v_cmp_ne_u32 s[14:15],0,v119\n"
"v_mfma_f32_16x16x32_bf16 v[12:15],a[84:87],a[20:23],v[12:15]\n"
"s_cmp_lg_u32 s14,0\n"
"s_cselect_b32 s18,2146435072,s18\n"
"v_exp_f32 v46,v46\n"
"v_mfma_f32_16x16x32_bf16 v[12:15],a[80:83],a[16:19],v[12:15]\n"
"s_add_u32 m0,s30,0\n"
"ds_read_b128 a[64:67],v113 offset:16384\n"
"s_waitcnt lgkmcnt(1)\n"
"v_mfma_f32_16x16x32_bf16 v[128:131],a[96:99],v[64:67],v[128:131]\n"
"ds_read_b128 a[68:71],v113 offset:17408\n"
"ds_read_b128 a[72:75],v113 offset:18432\n"
"ds_read_b128 a[76:79],v113 offset:19456\n"
"v_mfma_f32_16x16x32_bf16 v[160:163],a[96:99],v[68:71],v[160:163]\n"
"ds_read_b128 a[80:83],v113 offset:20480\n"
"v_exp_f32 v47,v47\n"
"v_mfma_f32_16x16x32_bf16 v[192:195],a[96:99],v[72:75],v[192:195]\n"
"ds_read_b128 a[84:87],v113 offset:21504\n"
"ds_read_b128 a[88:91],v113 offset:22528\n"
"ds_read_b128 a[92:95],v113 offset:23552\n"
"v_exp_f32 v48,v48\n"
"v_mfma_f32_16x16x32_bf16 v[224:227],a[96:99],v[76:79],v[224:227]\n"
"buffer_load_dwordx4 v112,s[24:27],s18 offen offset:0 sc1 lds\n"
"v_exp_f32 v49,v49\n"
"v_mfma_f32_16x16x32_bf16 a[140:143],a[144:147],v[76:79],a[140:143]\n"
"buffer_load_dwordx4 v112,s[24:27],s18 offen offset:1024 sc1 lds\n"
"v_mfma_f32_16x16x32_bf16 v[228:231],a[100:103],v[76:79],v[228:231]\n"
"buffer_load_dwordx4 v112,s[24:27],s18 offen offset:2048 sc1 lds\n"
"buffer_load_dwordx4 v112,s[24:27],s18 offen offset:3072 sc1 lds\n"
"v_mfma_f32_16x16x32_bf16 v[132:135],a[100:103],v[64:67],v[132:135]\n"
"s_add_u32 m0,m0,4096\n"
"v_exp_f32 v50,v50\n"
"v_cvt_pk_bf16_f32 v83,v38,v39\n"
"v_mfma_f32_16x16x32_bf16 v[164:167],a[100:103],v[68:71],v[164:167]\n"
"buffer_load_dwordx4 v117,s[24:27],s18 offen offset:0 sc1 lds\n"
"v_cvt_pk_bf16_f32 v84,v40,v41\n"
"v_cvt_pk_bf16_f32 v85,v42,v43\n"
"v_mfma_f32_16x16x32_bf16 v[196:199],a[100:103],v[72:75],v[196:199]\n"
"v_exp_f32 v51,v51\n"
"v_cvt_pk_bf16_f32 v86,v44,v45\n"
"v_mfma_f32_16x16x32_bf16 a[136:139],a[144:147],v[72:75],a[136:139]\n"
"v_exp_f32 v52,v52\n"
"v_cvt_pk_bf16_f32 v87,v46,v47\n"
"v_mfma_f32_16x16x32_bf16 v[200:203],a[104:107],v[72:75],v[200:203]\n"
"buffer_load_dwordx4 v117,s[24:27],s18 offen offset:1024 sc1 lds\n"
"v_cvt_pk_bf16_f32 v88,v48,v49\n"
"v_cvt_pk_bf16_f32 v89,v50,v51\n"
"v_mfma_f32_16x16x32_bf16 v[232:235],a[104:107],v[76:79],v[232:235]\n"
"v_exp_f32 v53,v53\n"
"v_max3_f32 v118,v118,v19,v20\n"
"v_mfma_f32_16x16x32_bf16 v[136:139],a[104:107],v[64:67],v[136:139]\n"
"v_cvt_pk_bf16_f32 v90,v52,v53\n"
"v_max3_f32 v118,v118,v21,v22\n"
"v_max3_f32 v118,v118,v23,v28\n"
"v_exp_f32 v54,v54\n"
"v_mfma_f32_16x16x32_bf16 v[168:171],a[104:107],v[68:71],v[168:171]\n"
"buffer_load_dwordx4 v117,s[24:27],s18 offen offset:2048 sc1 lds\n"
"v_mfma_f32_16x16x32_bf16 a[132:135],a[144:147],v[68:71],a[132:135]\n"
"v_exp_f32 v55,v55\n"
"v_max3_f32 v118,v118,v29,v30\n"
"v_mfma_f32_16x16x32_bf16 v[172:175],a[108:111],v[68:71],v[172:175]\n"
"v_cvt_pk_bf16_f32 v91,v54,v55\n"
"v_max3_f32 v118,v118,v31,v24\n"
"v_max3_f32 v118,v118,v25,v26\n"
"v_exp_f32 v56,v56\n"
"v_mfma_f32_16x16x32_bf16 v[204:207],a[108:111],v[72:75],v[204:207]\n"
"buffer_load_dwordx4 v117,s[24:27],s18 offen offset:3072 sc1 lds\n"
"v_mfma_f32_16x16x32_bf16 v[236:239],a[108:111],v[76:79],v[236:239]\n"
"s_sub_u32 s82,s3,32768\n"
"v_max3_f32 v118,v118,v27,v8\n"
"v_max3_f32 v118,v118,v9,v10\n"
"v_mfma_f32_16x16x32_bf16 v[140:143],a[108:111],v[64:67],v[140:143]\n"
"v_exp_f32 v57,v57\n"
"v_max3_f32 v118,v118,v11,v12\n"
"v_exp_f32 v58,v58\n"
"v_mfma_f32_16x16x32_bf16 a[128:131],a[144:147],v[64:67],a[128:131]\n"
"buffer_load_dwordx4 a[0:3],v112,s[24:27],s82 offen offset:0 sc1\n"
"v_mfma_f32_16x16x32_bf16 v[144:147],a[112:115],v[64:67],v[144:147]\n"
"v_max3_f32 v118,v118,v13,v14\n"
"v_max_f32 v118,v118,v15\n"
"v_mfma_f32_16x16x32_bf16 v[176:179],a[112:115],v[68:71],v[176:179]\n"
"v_exp_f32 v59,v59\n"
"v_mfma_f32_16x16x32_bf16 v[208:211],a[112:115],v[72:75],v[208:211]\n"
"v_cvt_pk_bf16_f32 v92,v56,v57\n"
"v_cvt_pk_bf16_f32 v93,v58,v59\n"
"v_exp_f32 v60,v60\n"
"v_mfma_f32_16x16x32_bf16 v[240:243],a[112:115],v[76:79],v[240:243]\n"
"buffer_load_dwordx4 a[4:7],v112,s[24:27],s82 offen offset:1024 sc1\n"
"v_mfma_f32_16x16x32_bf16 v[244:247],a[116:119],v[76:79],v[244:247]\n"
"v_exp_f32 v61,v61\n"
"v_mfma_f32_16x16x32_bf16 v[148:151],a[116:119],v[64:67],v[148:151]\n"
"v_cmp_lt_f32 vcc,s93,v118\n"
"v_cvt_pk_bf16_f32 v94,v60,v61\n"
"v_mfma_f32_16x16x32_bf16 v[180:183],a[116:119],v[68:71],v[180:183]\n"
"buffer_load_dwordx4 a[8:11],v112,s[24:27],s82 offen offset:2048 sc1\n"
"v_mfma_f32_16x16x32_bf16 v[212:215],a[116:119],v[72:75],v[212:215]\n"
"v_exp_f32 v62,v62\n"
"v_mfma_f32_16x16x32_bf16 v[216:219],a[120:123],v[72:75],v[216:219]\n"
"v_exp_f32 v63,v63\n"
"v_mfma_f32_16x16x32_bf16 v[248:251],a[120:123],v[76:79],v[248:251]\n"
"v_cvt_pk_bf16_f32 v95,v62,v63\n"
"v_mfma_f32_16x16x32_bf16 v[152:155],a[120:123],v[64:67],v[152:155]\n"
"buffer_load_dwordx4 a[12:15],v112,s[24:27],s82 offen offset:3072 sc1\n"
"v_mfma_f32_16x16x32_bf16 v[184:187],a[120:123],v[68:71],v[184:187]\n"
"v_mfma_f32_16x16x32_bf16 v[188:191],a[124:127],v[68:71],v[188:191]\n"
"v_mfma_f32_16x16x32_bf16 v[220:223],a[124:127],v[72:75],v[220:223]\n"
"v_mfma_f32_16x16x32_bf16 v[252:255],a[124:127],v[76:79],v[252:255]\n"
"v_mfma_f32_16x16x32_bf16 v[156:159],a[124:127],v[64:67],v[156:159]\n"
"s_cbranch_vccnz .Lfk_cs_a3\n"
".Lfk_cr_a3:\n"
"s_waitcnt vmcnt(17) lgkmcnt(0)\n"
"s_barrier\n"
"v_mfma_f32_16x16x32_bf16 v[56:59],a[64:67],a[48:51],v[108:111]\n"
"ds_read_b128 a[96:99],v114 offset:57344\n"
"ds_read_b128 a[100:103],v114 offset:58368\n"
"ds_read_b128 a[104:107],v114 offset:59392\n"
"v_mfma_f32_16x16x32_bf16 v[56:59],a[68:71],a[52:55],v[56:59]\n"
"ds_read_b128 a[108:111],v114 offset:60416\n"
"ds_read_b128 a[112:115],v114 offset:61440\n"
"ds_read_b128 a[116:119],v114 offset:62464\n"
"v_mfma_f32_16x16x32_bf16 v[56:59],a[72:75],a[56:59],v[56:59]\n"
"ds_read_b128 a[120:123],v114 offset:63488\n"
"ds_read_b128 a[124:127],v114 offset:64512\n"
"v_add_f32 v48,0x80000000,v126\n"
"v_mfma_f32_16x16x32_bf16 v[56:59],a[76:79],a[60:63],v[56:59]\n"
"v_min_f32 v48,v48,v104\n"
"v_add_f32 v49,0xf149f2ca,v126\n"
"v_min_f32 v49,v49,v105\n"
"v_mfma_f32_16x16x32_bf16 v[60:63],a[92:95],a[60:63],v[108:111]\n"
"v_add_f32 v50,0xf1c9f2ca,v126\n"
"v_add_f32 v51,0xf2177617,v126\n"
"v_mfma_f32_16x16x32_bf16 v[60:63],a[88:91],a[56:59],v[60:63]\n"
"v_add_f32 v52,0xf349f2ca,v126\n"
"v_min_f32 v52,v52,v104\n"
"v_mfma_f32_16x16x32_bf16 v[60:63],a[84:87],a[52:55],v[60:63]\n"
"v_add_f32 v53,0xf35691f6,v126\n"
"v_min_f32 v53,v53,v105\n"
"v_add_f32 v54,0xf3633123,v126\n"
"v_mfma_f32_16x16x32_bf16 v[60:63],a[80:83],a[48:51],v[60:63]\n"
"v_min_f32 v54,v54,v106\n"
"v_add_f32 v55,0xf36fd050,v126\n"
"v_min_f32 v55,v55,v107\n"
"v_min_f32 v51,v51,v107\n"
"v_min_f32 v50,v50,v106\n"
"v_mfma_f32_16x16x32_bf16 v[52:55],a[80:83],a[32:35],v[52:55]\n"
"s_add_u32 m0,s67,16384\n"
"v_exp_f32 v8,v8\n"
"v_mfma_f32_16x16x32_bf16 v[52:55],a[84:87],a[36:39],v[52:55]\n"
"buffer_load_dwordx4 v112,s[24:27],s64 offen offset:0 sc1 lds\n"
"buffer_load_dwordx4 v112,s[24:27],s64 offen offset:1024 sc1 lds\n"
"v_exp_f32 v9,v9\n"
"v_mfma_f32_16x16x32_bf16 v[52:55],a[88:91],a[40:43],v[52:55]\n"
"v_exp_f32 v10,v10\n"
"v_cvt_pk_bf16_f32 v68,v8,v9\n"
"v_mfma_f32_16x16x32_bf16 v[52:55],a[92:95],a[44:47],v[52:55]\n"
"v_exp_f32 v11,v11\n"
"v_max3_f32 v118,v56,v57,v58\n"
"v_exp_f32 v12,v12\n"
"v_mfma_f32_16x16x32_bf16 v[48:51],a[76:79],a[44:47],v[48:51]\n"
"buffer_load_dwordx4 v112,s[24:27],s64 offen offset:2048 sc1 lds\n"
"v_mfma_f32_16x16x32_bf16 v[48:51],a[72:75],a[40:43],v[48:51]\n"
"v_exp_f32 v13,v13\n"
"v_cvt_pk_bf16_f32 v69,v10,v11\n"
"v_mfma_f32_16x16x32_bf16 v[48:51],a[68:71],a[36:39],v[48:51]\n"
"v_exp_f32 v14,v14\n"
"v_cvt_pk_bf16_f32 v70,v12,v13\n"
"v_exp_f32 v15,v15\n"
"v_mfma_f32_16x16x32_bf16 v[48:51],a[64:67],a[32:35],v[48:51]\n"
"ds_read_b128 a[64:67],v113 offset:49152\n"
"s_waitcnt lgkmcnt(1)\n"
"v_mfma_f32_16x16x32_bf16 v[128:131],a[96:99],v[80:83],v[128:131]\n"
"ds_read_b128 a[68:71],v113 offset:50176\n"
"v_exp_f32 v16,v16\n"
"v_mfma_f32_16x16x32_bf16 v[160:163],a[96:99],v[84:87],v[160:163]\n"
"ds_read_b128 a[72:75],v113 offset:51200\n"
"v_exp_f32 v17,v17\n"
"v_mfma_f32_16x16x32_bf16 v[192:195],a[96:99],v[88:91],v[192:195]\n"
"ds_read_b128 a[76:79],v113 offset:52224\n"
"ds_read_b128 a[80:83],v113 offset:53248\n"
"ds_read_b128 a[84:87],v113 offset:54272\n"
"v_mfma_f32_16x16x32_bf16 v[224:227],a[96:99],v[92:95],v[224:227]\n"
"ds_read_b128 a[88:91],v113 offset:55296\n"
"v_exp_f32 v18,v18\n"
"v_mfma_f32_16x16x32_bf16 a[140:143],a[144:147],v[92:95],a[140:143]\n"
"ds_read_b128 a[92:95],v113 offset:56320\n"
"buffer_load_dwordx4 v112,s[24:27],s64 offen offset:3072 sc1 lds\n"
"s_add_u32 s64,s64,16384\n"
"v_mfma_f32_16x16x32_bf16 v[228:231],a[100:103],v[92:95],v[228:231]\n"
"s_sub_u32 s82,s3,49152\n"
"v_exp_f32 v19,v19\n"
"v_mfma_f32_16x16x32_bf16 v[132:135],a[100:103],v[80:83],v[132:135]\n"
"buffer_load_dwordx4 a[16:19],v112,s[24:27],s82 offen offset:0 sc1\n"
"v_cvt_pk_bf16_f32 v71,v14,v15\n"
"v_cvt_pk_bf16_f32 v72,v16,v17\n"
"v_cvt_pk_bf16_f32 v73,v18,v19\n"
"v_exp_f32 v20,v20\n"
"v_mfma_f32_16x16x32_bf16 v[164:167],a[100:103],v[84:87],v[164:167]\n"
"buffer_load_dwordx4 a[20:23],v112,s[24:27],s82 offen offset:1024 sc1\n"
"v_mfma_f32_16x16x32_bf16 v[196:199],a[100:103],v[88:91],v[196:199]\n"
"v_exp_f32 v21,v21\n"
"v_max3_f32 v118,v118,v59,v60\n"
"v_mfma_f32_16x16x32_bf16 a[136:139],a[144:147],v[88:91],a[136:139]\n"
"v_cvt_pk_bf16_f32 v74,v20,v21\n"
"v_max3_f32 v118,v118,v61,v62\n"
"v_max3_f32 v118,v118,v63,v52\n"
"v_mfma_f32_16x16x32_bf16 v[200:203],a[104:107],v[88:91],v[200:203]\n"
"v_exp_f32 v22,v22\n"
"v_max3_f32 v118,v118,v53,v54\n"
"v_mfma_f32_16x16x32_bf16 v[232:235],a[104:107],v[92:95],v[232:235]\n"
"buffer_load_dwordx4 a[24:27],v112,s[24:27],s82 offen offset:2048 sc1\n"
"v_max3_f32 v118,v118,v55,v48\n"
"v_max3_f32 v118,v118,v49,v50\n"
"v_mfma_f32_16x16x32_bf16 v[136:139],a[104:107],v[80:83],v[136:139]\n"
"v_exp_f32 v23,v23\n"
"v_max_f32 v118,v118,v51\n"
"v_mfma_f32_16x16x32_bf16 v[168:171],a[104:107],v[84:87],v[168:171]\n"
"v_cvt_pk_bf16_f32 v75,v22,v23\n"
"v_cmp_lt_f32 vcc,s93,v118\n"
"v_mfma_f32_16x16x32_bf16 a[132:135],a[144:147],v[84:87],a[132:135]\n"
"buffer_load_dwordx4 a[28:31],v112,s[24:27],s82 offen offset:3072 sc1\n"
"v_mfma_f32_16x16x32_bf16 v[172:175],a[108:111],v[84:87],v[172:175]\n"
"v_exp_f32 v24,v24\n"
"v_mfma_f32_16x16x32_bf16 v[204:207],a[108:111],v[88:91],v[204:207]\n"
"s_sub_u32 s82,s3,65536\n"
"v_exp_f32 v25,v25\n"
"v_mfma_f32_16x16x32_bf16 v[236:239],a[108:111],v[92:95],v[236:239]\n"
"v_cvt_pk_bf16_f32 v76,v24,v25\n"
"v_mfma_f32_16x16x32_bf16 v[140:143],a[108:111],v[80:83],v[140:143]\n"
"buffer_load_dwordx4 v[32:35],v112,s[24:27],s82 offen offset:0 sc1\n"
"v_mfma_f32_16x16x32_bf16 a[128:131],a[144:147],v[80:83],a[128:131]\n"
"v_exp_f32 v26,v26\n"
"v_mfma_f32_16x16x32_bf16 v[144:147],a[112:115],v[80:83],v[144:147]\n"
"v_exp_f32 v27,v27\n"
"v_mfma_f32_16x16x32_bf16 v[176:179],a[112:115],v[84:87],v[176:179]\n"
"buffer_load_dwordx4 v[36:39],v112,s[24:27],s82 offen offset:1024 sc1\n"
"v_mfma_f32_16x16x32_bf16 v[208:211],a[112:115],v[88:91],v[208:211]\n"
"v_exp_f32 v28,v28\n"
"v_mfma_f32_16x16x32_bf16 v[240:243],a[112:115],v[92:95],v[240:243]\n"
"v_cvt_pk_bf16_f32 v77,v26,v27\n"
"v_mfma_f32_16x16x32_bf16 v[244:247],a[116:119],v[92:95],v[244:247]\n"
"v_exp_f32 v29,v29\n"
"v_mfma_f32_16x16x32_bf16 v[148:151],a[116:119],v[80:83],v[148:151]\n"
"buffer_load_dwordx4 v[40:43],v112,s[24:27],s82 offen offset:2048 sc1\n"
"v_mfma_f32_16x16x32_bf16 v[180:183],a[116:119],v[84:87],v[180:183]\n"
"v_exp_f32 v30,v30\n"
"v_mfma_f32_16x16x32_bf16 v[212:215],a[116:119],v[88:91],v[212:215]\n"
"v_cvt_pk_bf16_f32 v78,v28,v29\n"
"v_mfma_f32_16x16x32_bf16 v[216:219],a[120:123],v[88:91],v[216:219]\n"
"v_exp_f32 v31,v31\n"
"v_mfma_f32_16x16x32_bf16 v[248:251],a[120:123],v[92:95],v[248:251]\n"
"v_cvt_pk_bf16_f32 v79,v30,v31\n"
"v_mfma_f32_16x16x32_bf16 v[152:155],a[120:123],v[80:83],v[152:155]\n"
"buffer_load_dwordx4 v[44:47],v112,s[24:27],s82 offen offset:3072 sc1\n"
"v_mfma_f32_16x16x32_bf16 v[184:187],a[120:123],v[84:87],v[184:187]\n"
"v_mfma_f32_16x16x32_bf16 v[188:191],a[124:127],v[84:87],v[188:191]\n"
"v_mfma_f32_16x16x32_bf16 v[220:223],a[124:127],v[88:91],v[220:223]\n"
"v_mfma_f32_16x16x32_bf16 v[252:255],a[124:127],v[92:95],v[252:255]\n"
"v_mfma_f32_16x16x32_bf16 v[156:159],a[124:127],v[80:83],v[156:159]\n"
"s_cbranch_vccnz .Lfk_cs_a0\n"
".Lfk_cr_a0:\n"
"s_load_dwordx2 s[36:37],s[0:1],0\n"
"s_load_dwordx2 s[44:45],s[0:1],80\n"
"s_load_dwordx2 s[48:49],s[0:1],88\n"
"s_lshl_b32 s82,s78,2\n"
"s_add_u32 s82,s82,65536\n"
"v_add_u32 v113,s82,v112\n"
"s_lshr_b32 s82,s78,2\n"
"s_add_u32 s82,s82,122880\n"
"v_lshrrev_b32 v115,1,v112\n"
"v_add_u32 v116,s82,v115\n"
"s_waitcnt lgkmcnt(0)\n"
"s_barrier\n"
"v_add_f32 v24,0x80000000,v126\n"
"v_min_f32 v24,v24,v108\n"
"v_add_f32 v25,0xf149f2ca,v126\n"
"v_min_f32 v25,v25,v109\n"
"v_add_f32 v26,0xf1c9f2ca,v126\n"
"v_min_f32 v26,v26,v110\n"
"v_add_f32 v27,0xf2177617,v126\n"
"v_min_f32 v27,v27,v111\n"
"v_add_f32 v28,0xf349f2ca,v126\n"
"v_min_f32 v28,v28,v108\n"
"v_add_f32 v29,0xf35691f6,v126\n"
"v_min_f32 v29,v29,v109\n"
"v_add_f32 v30,0xf3633123,v126\n"
"v_min_f32 v30,v30,v110\n"
"v_add_f32 v31,0xf36fd050,v126\n"
"v_min_f32 v31,v31,v111\n"
"v_exp_f32 v48,v48\n"
"v_mfma_f32_16x16x32_bf16 v[24:27],a[64:67],a[48:51],v[24:27]\n"
"s_add_u32 s83,s18,2097152\n"
"ds_read_b128 a[96:99],v114 offset:40960\n"
"ds_read_b128 a[100:103],v114 offset:41984\n"
"v_exp_f32 v49,v49\n"
"v_mfma_f32_16x16x32_bf16 v[24:27],a[68:71],a[52:55],v[24:27]\n"
"s_add_u32 m0,s30,32768\n"
"ds_read_b128 a[104:107],v114 offset:43008\n"
"ds_read_b128 a[108:111],v114 offset:44032\n"
"v_mfma_f32_16x16x32_bf16 v[24:27],a[72:75],a[56:59],v[24:27]\n"
"ds_read_b128 a[112:115],v114 offset:45056\n"
"ds_read_b128 a[116:119],v114 offset:46080\n"
"ds_read_b128 a[120:123],v114 offset:47104\n"
"ds_read_b128 a[124:127],v114 offset:48128\n"
"v_exp_f32 v50,v50\n"
"v_mfma_f32_16x16x32_bf16 v[24:27],a[76:79],a[60:63],v[24:27]\n"
"buffer_load_dwordx4 v112,s[24:27],s83 offen offset:0 sc1 lds\n"
"v_cvt_pk_bf16_f32 v88,v48,v49\n"
"v_mfma_f32_16x16x32_bf16 v[28:31],a[92:95],a[60:63],v[28:31]\n"
"buffer_load_dwordx4 v112,s[24:27],s83 offen offset:1024 sc1 lds\n"
"v_accvgpr_read_b32 v97,a128\n"
"v_cvt_pk_bf16_f32 v128,v128,v129\n"
"v_cvt_pk_bf16_f32 v129,v130,v131\n"
"v_mfma_f32_16x16x32_bf16 v[28:31],a[88:91],a[56:59],v[28:31]\n"
"v_exp_f32 v51,v51\n"
"v_cvt_pk_bf16_f32 v130,v132,v133\n"
"v_cvt_pk_bf16_f32 v131,v134,v135\n"
"v_cvt_pk_bf16_f32 v89,v50,v51\n"
"v_mfma_f32_16x16x32_bf16 v[28:31],a[84:87],a[52:55],v[28:31]\n"
"ds_write_b128 v113,v[128:131] offset:0\n"
"v_cvt_pk_bf16_f32 v136,v136,v137\n"
"v_cvt_pk_bf16_f32 v137,v138,v139\n"
"v_exp_f32 v52,v52\n"
"v_mfma_f32_16x16x32_bf16 v[28:31],a[80:83],a[48:51],v[28:31]\n"
"buffer_load_dwordx4 v112,s[24:27],s83 offen offset:2048 sc1 lds\n"
"v_max3_f32 v118,v24,v25,v26\n"
"s_waitcnt lgkmcnt(1)\n"
"v_mfma_f32_16x16x32_bf16 v[160:163],a[96:99],v[68:71],v[160:163]\n"
"v_cvt_pk_bf16_f32 v138,v140,v141\n"
"v_cvt_pk_bf16_f32 v139,v142,v143\n"
"v_cvt_pk_bf16_f32 v144,v144,v145\n"
"v_mfma_f32_16x16x32_bf16 a[132:135],a[144:147],v[68:71],a[132:135]\n"
"ds_write_b128 v113,v[136:139] offset:1024\n"
"buffer_load_dwordx4 v112,s[24:27],s83 offen offset:3072 sc1 lds\n"
"v_exp_f32 v53,v53\n"
"v_cvt_pk_bf16_f32 v145,v146,v147\n"
"v_mfma_f32_16x16x32_bf16 v[164:167],a[100:103],v[68:71],v[164:167]\n"
"s_add_u32 m0,m0,4096\n"
"v_exp_f32 v54,v54\n"
"v_cvt_pk_bf16_f32 v90,v52,v53\n"
"v_cvt_pk_bf16_f32 v146,v148,v149\n"
"v_mfma_f32_16x16x32_bf16 v[168:171],a[104:107],v[68:71],v[168:171]\n"
"v_max3_f32 v118,v118,v27,v28\n"
"v_max3_f32 v118,v118,v29,v30\n"
"v_max_f32 v118,v118,v31\n"
"v_cmp_lt_f32 vcc,s93,v118\n"
"v_exp_f32 v55,v55\n"
"v_mfma_f32_16x16x32_bf16 v[172:175],a[108:111],v[68:71],v[172:175]\n"
"buffer_load_dwordx4 v117,s[24:27],s83 offen offset:0 sc1 lds\n"
"v_cvt_pk_bf16_f32 v147,v150,v151\n"
"v_cvt_pk_bf16_f32 v91,v54,v55\n"
"v_mfma_f32_16x16x32_bf16 v[176:179],a[112:115],v[68:71],v[176:179]\n"
"ds_write_b128 v113,v[144:147] offset:2048\n"
"v_cvt_pk_bf16_f32 v152,v152,v153\n"
"v_cvt_pk_bf16_f32 v153,v154,v155\n"
"v_exp_f32 v56,v56\n"
"v_mfma_f32_16x16x32_bf16 v[180:183],a[116:119],v[68:71],v[180:183]\n"
"buffer_load_dwordx4 v117,s[24:27],s83 offen offset:1024 sc1 lds\n"
"v_cvt_pk_bf16_f32 v154,v156,v157\n"
"v_cvt_pk_bf16_f32 v155,v158,v159\n"
"v_mfma_f32_16x16x32_bf16 v[184:187],a[120:123],v[68:71],v[184:187]\n"
"ds_write_b128 v113,v[152:155] offset:3072\n"
"ds_write_b64 v116,v[96:97] offset:0\n"
"v_mfma_f32_16x16x32_bf16 v[188:191],a[124:127],v[68:71],v[188:191]\n"
"v_exp_f32 v57,v57\n"
"v_mfma_f32_16x16x32_bf16 v[192:195],a[96:99],v[72:75],v[192:195]\n"
"buffer_load_dwordx4 v117,s[24:27],s83 offen offset:2048 sc1 lds\n"
"v_cvt_pk_bf16_f32 v92,v56,v57\n"
"v_mfma_f32_16x16x32_bf16 a[136:139],a[144:147],v[72:75],a[136:139]\n"
"v_exp_f32 v58,v58\n"
"v_exp_f32 v59,v59\n"
"v_mfma_f32_16x16x32_bf16 v[196:199],a[100:103],v[72:75],v[196:199]\n"
"buffer_load_dwordx4 v117,s[24:27],s83 offen offset:3072 sc1 lds\n"
"v_accvgpr_read_b32 v101,a132\n"
"v_mfma_f32_16x16x32_bf16 v[200:203],a[104:107],v[72:75],v[200:203]\n"
"s_sub_u32 s82,s3,81920\n"
"v_cvt_pk_bf16_f32 v93,v58,v59\n"
"v_cvt_pk_bf16_f32 v160,v160,v161\n"
"v_cvt_pk_bf16_f32 v161,v162,v163\n"
"v_mfma_f32_16x16x32_bf16 v[204:207],a[108:111],v[72:75],v[204:207]\n"
"v_exp_f32 v60,v60\n"
"v_cvt_pk_bf16_f32 v162,v164,v165\n"
"v_cvt_pk_bf16_f32 v163,v166,v167\n"
"v_mfma_f32_16x16x32_bf16 v[208:211],a[112:115],v[72:75],v[208:211]\n"
"buffer_load_dwordx4 a[32:35],v112,s[24:27],s82 offen offset:0 sc1\n"
"ds_write_b128 v113,v[160:163] offset:4096\n"
"v_cvt_pk_bf16_f32 v168,v168,v169\n"
"v_cvt_pk_bf16_f32 v169,v170,v171\n"
"v_mfma_f32_16x16x32_bf16 v[212:215],a[116:119],v[72:75],v[212:215]\n"
"v_exp_f32 v61,v61\n"
"v_cvt_pk_bf16_f32 v170,v172,v173\n"
"v_cvt_pk_bf16_f32 v171,v174,v175\n"
"v_cvt_pk_bf16_f32 v94,v60,v61\n"
"v_mfma_f32_16x16x32_bf16 v[216:219],a[120:123],v[72:75],v[216:219]\n"
"ds_write_b128 v113,v[168:171] offset:5120\n"
"v_cvt_pk_bf16_f32 v176,v176,v177\n"
"v_cvt_pk_bf16_f32 v177,v178,v179\n"
"v_exp_f32 v62,v62\n"
"v_mfma_f32_16x16x32_bf16 v[220:223],a[124:127],v[72:75],v[220:223]\n"
"buffer_load_dwordx4 a[36:39],v112,s[24:27],s82 offen offset:1024 sc1\n"
"v_cvt_pk_bf16_f32 v178,v180,v181\n"
"v_cvt_pk_bf16_f32 v179,v182,v183\n"
"v_mfma_f32_16x16x32_bf16 v[224:227],a[96:99],v[76:79],v[224:227]\n"
"ds_write_b128 v113,v[176:179] offset:6144\n"
"v_cvt_pk_bf16_f32 v184,v184,v185\n"
"v_cvt_pk_bf16_f32 v185,v186,v187\n"
"v_mfma_f32_16x16x32_bf16 a[140:143],a[144:147],v[76:79],a[140:143]\n"
"v_exp_f32 v63,v63\n"
"v_cvt_pk_bf16_f32 v186,v188,v189\n"
"v_cvt_pk_bf16_f32 v187,v190,v191\n"
"v_mfma_f32_16x16x32_bf16 v[228:231],a[100:103],v[76:79],v[228:231]\n"
"buffer_load_dwordx4 a[40:43],v112,s[24:27],s82 offen offset:2048 sc1\n"
"ds_write_b128 v113,v[184:187] offset:7168\n"
"v_mfma_f32_16x16x32_bf16 v[232:235],a[104:107],v[76:79],v[232:235]\n"
"ds_write_b64 v116,v[100:101] offset:512\n"
"v_cvt_pk_bf16_f32 v95,v62,v63\n"
"v_mfma_f32_16x16x32_bf16 v[236:239],a[108:111],v[76:79],v[236:239]\n"
"buffer_load_dwordx4 a[44:47],v112,s[24:27],s82 offen offset:3072 sc1\n"
"v_mfma_f32_16x16x32_bf16 v[240:243],a[112:115],v[76:79],v[240:243]\n"
"s_sub_u32 s82,s3,131072\n"
"buffer_load_dwordx4 v[128:131],v112,s[24:27],s82 offen offset:0 sc1\n"
"buffer_load_dwordx4 v[132:135],v112,s[24:27],s82 offen offset:1024 sc1\n"
"buffer_load_dwordx4 v[136:139],v112,s[24:27],s82 offen offset:2048 sc1\n"
"buffer_load_dwordx4 v[140:143],v112,s[24:27],s82 offen offset:3072 sc1\n"
"v_mfma_f32_16x16x32_bf16 v[244:247],a[116:119],v[76:79],v[244:247]\n"
"s_sub_u32 s82,s3,147456\n"
"buffer_load_dwordx4 v[144:147],v112,s[24:27],s82 offen offset:0 sc1\n"
"buffer_load_dwordx4 v[148:151],v112,s[24:27],s82 offen offset:1024 sc1\n"
"v_mfma_f32_16x16x32_bf16 v[248:251],a[120:123],v[76:79],v[248:251]\n"
"buffer_load_dwordx4 v[152:155],v112,s[24:27],s82 offen offset:2048 sc1\n"
"buffer_load_dwordx4 v[156:159],v112,s[24:27],s82 offen offset:3072 sc1\n"
"v_mfma_f32_16x16x32_bf16 v[252:255],a[124:127],v[76:79],v[252:255]\n"
"s_cbranch_vccnz .Lfk_cs_a1\n"
".Lfk_cr_a1:\n"
"s_waitcnt vmcnt(28) lgkmcnt(0)\n"
"s_barrier\n"
"ds_read_b128 a[96:99],v114 offset:8192\n"
"ds_read_b128 a[100:103],v114 offset:9216\n"
"ds_read_b128 a[104:107],v114 offset:10240\n"
"ds_read_b128 a[108:111],v114 offset:11264\n"
"ds_read_b128 a[112:115],v114 offset:12288\n"
"ds_read_b128 a[116:119],v114 offset:13312\n"
"ds_read_b128 a[120:123],v114 offset:14336\n"
"ds_read_b128 a[124:127],v114 offset:15360\n"
"v_xor_b32 v113,32768,v113\n"
"v_xor_b32 v116,2048,v116\n"
"ds_read_b64 v[98:99],v116 offset:0\n"
"ds_read_b128 a[64:67],v113 offset:0\n"
"ds_read_b128 a[68:71],v113 offset:1024\n"
"ds_read_b128 a[72:75],v113 offset:2048\n"
"ds_read_b128 a[76:79],v113 offset:3072\n"
"ds_read_b64 v[102:103],v116 offset:512\n"
"ds_read_b128 a[80:83],v113 offset:4096\n"
"ds_read_b128 a[84:87],v113 offset:5120\n"
"ds_read_b128 a[88:91],v113 offset:6144\n"
"ds_read_b128 a[92:95],v113 offset:7168\n"
"v_exp_f32 v24,v24\n"
"v_exp_f32 v25,v25\n"
"v_exp_f32 v26,v26\n"
"v_exp_f32 v27,v27\n"
"v_exp_f32 v28,v28\n"
"v_exp_f32 v29,v29\n"
"v_exp_f32 v30,v30\n"
"v_exp_f32 v31,v31\n"
"v_cvt_pk_bf16_f32 v76,v24,v25\n"
"v_cvt_pk_bf16_f32 v77,v26,v27\n"
"v_cvt_pk_bf16_f32 v78,v28,v29\n"
"v_cvt_pk_bf16_f32 v79,v30,v31\n"
"s_waitcnt lgkmcnt(10)\n"
"v_mfma_f32_16x16x32_bf16 a[136:139],a[144:147],v[88:91],a[136:139]\n"
"s_sub_u32 s82,s3,163840\n"
"v_lshrrev_b32 v70,4,v112\n"
"v_and_b32 v71,15,v70\n"
"v_mfma_f32_16x16x32_bf16 a[140:143],a[144:147],v[92:95],a[140:143]\n"
"buffer_load_dwordx4 v[0:3],v112,s[24:27],s82 offen offset:0 sc1\n"
"v_lshrrev_b32 v70,4,v70\n"
"v_lshlrev_b32 v70,2,v70\n"
"v_mfma_f32_16x16x32_bf16 v[192:195],a[96:99],v[88:91],v[192:195]\n"
"buffer_load_dwordx4 v[4:7],v112,s[24:27],s82 offen offset:1024 sc1\n"
"v_sub_u32 v70,v71,v70\n"
"v_mov_b32 v64,0\n"
"v_mfma_f32_16x16x32_bf16 v[224:227],a[96:99],v[92:95],v[224:227]\n"
"ds_read_b128 a[96:99],v114 offset:24576\n"
"buffer_load_dwordx4 v[8:11],v112,s[24:27],s82 offen offset:2048 sc1\n"
"v_mfma_f32_16x16x32_bf16 v[196:199],a[100:103],v[88:91],v[196:199]\n"
"buffer_load_dwordx4 v[12:15],v112,s[24:27],s82 offen offset:3072 sc1\n"
"v_mov_b32 v69,0\n"
"v_mfma_f32_16x16x32_bf16 v[228:231],a[100:103],v[92:95],v[228:231]\n"
"s_sub_u32 s82,s3,180224\n"
"ds_read_b128 a[100:103],v114 offset:25600\n"
"v_mov_b32 v71,0x3f80\n"
"v_mfma_f32_16x16x32_bf16 v[200:203],a[104:107],v[88:91],v[200:203]\n"
"buffer_load_dwordx4 v[160:163],v112,s[24:27],s82 offen offset:0 sc1\n"
"v_cmp_eq_u32 vcc,0,v70\n"
"v_mov_b32 v68,0\n"
"v_mov_b32 v65,0\n"
"v_cndmask_b32 v66,0,v71,vcc\n"
"v_cmp_eq_u32 vcc,1,v70\n"
"v_mfma_f32_16x16x32_bf16 v[232:235],a[104:107],v[92:95],v[232:235]\n"
"ds_read_b128 a[104:107],v114 offset:26624\n"
"buffer_load_dwordx4 v[164:167],v112,s[24:27],s82 offen offset:1024 sc1\n"
"v_mov_b32 v72,0x3f800000\n"
"v_cndmask_b32 v66,v66,v72,vcc\n"
"v_cmp_eq_u32 vcc,2,v70\n"
"v_mfma_f32_16x16x32_bf16 v[204:207],a[108:111],v[88:91],v[204:207]\n"
"buffer_load_dwordx4 v[168:171],v112,s[24:27],s82 offen offset:2048 sc1\n"
"v_cndmask_b32 v67,0,v71,vcc\n"
"v_cmp_eq_u32 vcc,3,v70\n"
"v_mfma_f32_16x16x32_bf16 v[236:239],a[108:111],v[92:95],v[236:239]\n"
"ds_read_b128 a[108:111],v114 offset:27648\n"
"buffer_load_dwordx4 v[172:175],v112,s[24:27],s82 offen offset:3072 sc1\n"
"v_mfma_f32_16x16x32_bf16 v[208:211],a[112:115],v[88:91],v[208:211]\n"
"v_cndmask_b32 v67,v67,v72,vcc\n"
"s_sub_u32 s82,s3,196608\n"
"v_mfma_f32_16x16x32_bf16 v[240:243],a[112:115],v[92:95],v[240:243]\n"
"ds_read_b128 a[112:115],v114 offset:28672\n"
"buffer_load_dwordx4 v[176:179],v112,s[24:27],s82 offen offset:0 sc1\n"
"v_mfma_f32_16x16x32_bf16 v[212:215],a[116:119],v[88:91],v[212:215]\n"
"buffer_load_dwordx4 v[180:183],v112,s[24:27],s82 offen offset:1024 sc1\n"
"v_mfma_f32_16x16x32_bf16 v[244:247],a[116:119],v[92:95],v[244:247]\n"
"ds_read_b128 a[116:119],v114 offset:29696\n"
"buffer_load_dwordx4 v[184:187],v112,s[24:27],s82 offen offset:2048 sc1\n"
"v_mfma_f32_16x16x32_bf16 v[216:219],a[120:123],v[88:91],v[216:219]\n"
"buffer_load_dwordx4 v[188:191],v112,s[24:27],s82 offen offset:3072 sc1\n"
"v_mfma_f32_16x16x32_bf16 v[248:251],a[120:123],v[92:95],v[248:251]\n"
"ds_read_b128 a[120:123],v114 offset:30720\n"
"v_mfma_f32_16x16x32_bf16 v[220:223],a[124:127],v[88:91],v[220:223]\n"
"v_mfma_f32_16x16x32_bf16 v[252:255],a[124:127],v[92:95],v[252:255]\n"
"ds_read_b128 a[124:127],v114 offset:31744\n"
"s_waitcnt lgkmcnt(13)\n"
"v_cmp_neq_f32 vcc,v98,v104\n"
"s_nop 0\n"
"s_cbranch_vccnz .Lfk_ms_a0\n"
".Lfk_mr_a0:\n"
"v_mfma_f32_16x16x32_bf16 v[192:195],v[66:69],a[64:67],v[192:195]\n"
"v_mfma_f32_16x16x32_bf16 v[196:199],v[64:67],a[64:67],v[196:199]\n"
"v_mfma_f32_16x16x32_bf16 v[200:203],v[66:69],a[68:71],v[200:203]\n"
"v_mfma_f32_16x16x32_bf16 v[204:207],v[64:67],a[68:71],v[204:207]\n"
"v_mfma_f32_16x16x32_bf16 v[208:211],v[66:69],a[72:75],v[208:211]\n"
"v_mfma_f32_16x16x32_bf16 v[212:215],v[64:67],a[72:75],v[212:215]\n"
"v_mfma_f32_16x16x32_bf16 v[216:219],v[66:69],a[76:79],v[216:219]\n"
"v_mfma_f32_16x16x32_bf16 v[220:223],v[64:67],a[76:79],v[220:223]\n"
"s_waitcnt lgkmcnt(0)\n"
"v_mfma_f32_16x16x32_bf16 a[140:143],a[144:147],v[76:79],a[140:143]\n"
"v_mfma_f32_16x16x32_bf16 v[224:227],a[96:99],v[76:79],v[224:227]\n"
"v_mfma_f32_16x16x32_bf16 v[228:231],a[100:103],v[76:79],v[228:231]\n"
"v_accvgpr_read_b32 v122,a136\n"
"v_add_f32 v122,v122,v99\n"
"v_mfma_f32_16x16x32_bf16 v[232:235],a[104:107],v[76:79],v[232:235]\n"
"v_rcp_f32 v122,v122\n"
"v_mfma_f32_16x16x32_bf16 v[236:239],a[108:111],v[76:79],v[236:239]\n"
"v_pk_mul_f32 v[192:193],v[192:193],v[122:123] op_sel_hi:[1,0]\n"
"v_pk_mul_f32 v[194:195],v[194:195],v[122:123] op_sel_hi:[1,0]\n"
"v_pk_mul_f32 v[196:197],v[196:197],v[122:123] op_sel_hi:[1,0]\n"
"v_pk_mul_f32 v[198:199],v[198:199],v[122:123] op_sel_hi:[1,0]\n"
"v_mfma_f32_16x16x32_bf16 v[240:243],a[112:115],v[76:79],v[240:243]\n"
"v_cvt_pk_bf16_f32 v192,v192,v193\n"
"v_cvt_pk_bf16_f32 v193,v194,v195\n"
"v_mfma_f32_16x16x32_bf16 v[244:247],a[116:119],v[76:79],v[244:247]\n"
"v_cvt_pk_bf16_f32 v194,v196,v197\n"
"v_cvt_pk_bf16_f32 v195,v198,v199\n"
"v_mfma_f32_16x16x32_bf16 v[248:251],a[120:123],v[76:79],v[248:251]\n"
"buffer_store_dwordx4 v[192:195],v112,s[24:27],s70 offen offset:0 sc1\n"
"v_pk_mul_f32 v[200:201],v[200:201],v[122:123] op_sel_hi:[1,0]\n"
"v_pk_mul_f32 v[202:203],v[202:203],v[122:123] op_sel_hi:[1,0]\n"
"v_pk_mul_f32 v[204:205],v[204:205],v[122:123] op_sel_hi:[1,0]\n"
"v_pk_mul_f32 v[206:207],v[206:207],v[122:123] op_sel_hi:[1,0]\n"
"v_mfma_f32_16x16x32_bf16 v[252:255],a[124:127],v[76:79],v[252:255]\n"
"v_cvt_pk_bf16_f32 v200,v200,v201\n"
"v_cvt_pk_bf16_f32 v201,v202,v203\n"
"v_cmp_neq_f32 vcc,v102,v108\n"
"s_nop 0\n"
"s_cbranch_vccnz .Lfk_ms_a1\n"
".Lfk_mr_a1:\n"
"v_mfma_f32_16x16x32_bf16 v[224:227],v[66:69],a[80:83],v[224:227]\n"
"v_cvt_pk_bf16_f32 v202,v204,v205\n"
"v_cvt_pk_bf16_f32 v203,v206,v207\n"
"v_mfma_f32_16x16x32_bf16 v[228:231],v[64:67],a[80:83],v[228:231]\n"
"buffer_store_dwordx4 v[200:203],v112,s[24:27],s70 offen offset:1024 sc1\n"
"v_pk_mul_f32 v[208:209],v[208:209],v[122:123] op_sel_hi:[1,0]\n"
"v_pk_mul_f32 v[210:211],v[210:211],v[122:123] op_sel_hi:[1,0]\n"
"v_pk_mul_f32 v[212:213],v[212:213],v[122:123] op_sel_hi:[1,0]\n"
"v_pk_mul_f32 v[214:215],v[214:215],v[122:123] op_sel_hi:[1,0]\n"
"v_mfma_f32_16x16x32_bf16 v[232:235],v[66:69],a[84:87],v[232:235]\n"
"v_cvt_pk_bf16_f32 v208,v208,v209\n"
"v_cvt_pk_bf16_f32 v209,v210,v211\n"
"v_mfma_f32_16x16x32_bf16 v[236:239],v[64:67],a[84:87],v[236:239]\n"
"v_cvt_pk_bf16_f32 v210,v212,v213\n"
"v_cvt_pk_bf16_f32 v211,v214,v215\n"
"v_mfma_f32_16x16x32_bf16 v[240:243],v[66:69],a[88:91],v[240:243]\n"
"buffer_store_dwordx4 v[208:211],v112,s[24:27],s70 offen offset:2048 sc1\n"
"v_pk_mul_f32 v[216:217],v[216:217],v[122:123] op_sel_hi:[1,0]\n"
"v_pk_mul_f32 v[218:219],v[218:219],v[122:123] op_sel_hi:[1,0]\n"
"v_pk_mul_f32 v[220:221],v[220:221],v[122:123] op_sel_hi:[1,0]\n"
"v_pk_mul_f32 v[222:223],v[222:223],v[122:123] op_sel_hi:[1,0]\n"
"v_mfma_f32_16x16x32_bf16 v[244:247],v[64:67],a[88:91],v[244:247]\n"
"v_cvt_pk_bf16_f32 v216,v216,v217\n"
"v_cvt_pk_bf16_f32 v217,v218,v219\n"
"v_mfma_f32_16x16x32_bf16 v[248:251],v[66:69],a[92:95],v[248:251]\n"
"v_cvt_pk_bf16_f32 v218,v220,v221\n"
"v_cvt_pk_bf16_f32 v219,v222,v223\n"
"v_mfma_f32_16x16x32_bf16 v[252:255],v[64:67],a[92:95],v[252:255]\n"
"buffer_store_dwordx4 v[216:219],v112,s[24:27],s70 offen offset:3072 sc1\n"
"s_add_u32 s70,s70,262144\n"
"s_branch .Lfk_p1\n"
".Lfk_look:\n"
"s_lshr_b64 s[28:29],s[14:15],s21\n"
"s_and_b32 s83,s28,s19\n"
"s_cbranch_scc0 .Lfk_lk1\n"
"s_mov_b32 s84,0x40000\n"
"s_waitcnt vmcnt(10)\n"
".Lfk_lk0:\n"
"v_xor_b32 v119,s80,v122\n"
"v_xor_b32 v120,s81,v123\n"
"v_or_b32 v119,v119,v120\n"
"v_cmp_ne_u32 s[14:15],0,v119\n"
"s_nop 0\n"
"s_lshr_b64 s[28:29],s[14:15],s21\n"
"s_and_b32 s83,s28,s19\n"
"s_cbranch_scc0 .Lfk_lk1\n"
"s_sleep 2\n"
"buffer_load_dwordx2 v[122:123],v115,s[24:27],s11 offen sc1\n"
"s_waitcnt vmcnt(0)\n"
"s_sub_u32 s84,s84,1\n"
"s_cbranch_scc0 .Lfk_lk0\n"
".Lfk_lk1:\n"
"s_andn2_b32 s28,s28,s19\n"
"s_lshl_b64 s[14:15],s[28:29],s21\n"
"s_lshl_b32 s83,s19,8\n"
"s_and_b32 s83,s28,s83\n"
"s_cbranch_scc0 .Lfk_lk2\n"
"buffer_load_dwordx2 v[122:123],v115,s[24:27],s11 offen sc1\n"
".Lfk_lk2:\n"
"s_nop 1\n"
"s_setpc_b64 s[94:95]\n"
".Lfk_meet4:\n"
"s_mov_b32 s84,0x1000000\n"
".Lfk_meetl4:\n"
"ds_read_b32 v125,v114\n"
"s_sub_u32 s84,s84,1\n"
"s_cbranch_scc1 .Lfk_meetx4\n"
"s_waitcnt lgkmcnt(0)\n"
"v_cmp_gt_u32 vcc,s16,v125\n"
"s_nop 0\n"
"s_cbranch_vccnz .Lfk_meetl4\n"
".Lfk_meetx4:\n"
"s_setpc_b64 s[94:95]\n"
".Lfk_es_lkone:\n"
"s_call_b64 s[94:95],.Lfk_look\n"
"s_branch .Lfk_er_lkone\n"
".Lfk_z0:\n"
"s_call_b64 s[94:95],.Lfk_meet4\n"
"v_xor_b32 v119,s80,v104\n"
"v_xor_b32 v120,s81,v105\n"
"v_or_b32 v119,v119,v120\n"
"v_xor_b32 v120,s80,v106\n"
"v_or_b32 v119,v119,v120\n"
"v_xor_b32 v120,s81,v107\n"
"v_or_b32 v119,v119,v120\n"
"v_xor_b32 v120,s80,v108\n"
"v_or_b32 v119,v119,v120\n"
"v_xor_b32 v120,s81,v109\n"
"v_or_b32 v119,v119,v120\n"
"v_xor_b32 v120,s80,v110\n"
"v_or_b32 v119,v119,v120\n"
"v_xor_b32 v120,s81,v111\n"
"v_or_b32 v119,v119,v120\n"
"v_cmp_ne_u32 vcc,0,v119\n"
"s_nop 0\n"
"s_cbranch_vccz .Lfk_z1\n"
"s_mov_b32 s84,0x40000\n"
"s_mov_b32 s83,148946944\n"
"buffer_load_dwordx4 v[104:107],v112,s[24:27],s83 offen offset:0 sc1\n"
"buffer_load_dwordx4 v[108:111],v112,s[24:27],s83 offen offset:1024 sc1\n"
"s_waitcnt vmcnt(0)\n"
".Lfk_w0:\n"
"v_xor_b32 v119,s80,v104\n"
"v_xor_b32 v120,s81,v105\n"
"v_or_b32 v119,v119,v120\n"
"v_xor_b32 v120,s80,v106\n"
"v_or_b32 v119,v119,v120\n"
"v_xor_b32 v120,s81,v107\n"
"v_or_b32 v119,v119,v120\n"
"v_xor_b32 v120,s80,v108\n"
"v_or_b32 v119,v119,v120\n"
"v_xor_b32 v120,s81,v109\n"
"v_or_b32 v119,v119,v120\n"
"v_xor_b32 v120,s80,v110\n"
"v_or_b32 v119,v119,v120\n"
"v_xor_b32 v120,s81,v111\n"
"v_or_b32 v119,v119,v120\n"
"v_cmp_ne_u32 vcc,0,v119\n"
"s_nop 0\n"
"s_cbranch_vccz .Lfk_w0_x\n"
"s_sleep 8\n"
"s_mov_b64 exec,vcc\n"
"s_mov_b32 s83,148946944\n"
"buffer_load_dwordx4 v[104:107],v112,s[24:27],s83 offen offset:0 sc1\n"
"buffer_load_dwordx4 v[108:111],v112,s[24:27],s83 offen offset:1024 sc1\n"
"s_mov_b64 exec,-1\n"
"s_sub_u32 s84,s84,1\n"
"s_waitcnt vmcnt(0)\n"
"s_cbranch_scc0 .Lfk_w0\n"
".Lfk_w0_x:\n"
"s_nop 1\n"
"s_branch .Lfk_z1\n"
".Lfk_es_lkonem:\n"
"s_call_b64 s[94:95],.Lfk_look\n"
"s_branch .Lfk_er_lkonem\n"
".Lfk_es_mone:\n"
"s_call_b64 s[94:95],.Lfk_meet4\n"
"s_branch .Lfk_er_mone\n"
".Lfk_es_lktwom:\n"
"s_call_b64 s[94:95],.Lfk_look\n"
"s_branch .Lfk_er_lktwom\n"
".Lfk_es_mtwo:\n"
"s_call_b64 s[94:95],.Lfk_meet4\n"
"s_branch .Lfk_er_mtwo\n"
".Lfk_es_lkloopm:\n"
"s_call_b64 s[94:95],.Lfk_look\n"
"s_branch .Lfk_er_lkloopm\n"
".Lfk_es_me3:\n"
"s_call_b64 s[94:95],.Lfk_meet4\n"
"s_branch .Lfk_er_me3\n"
".Lfk_es_me2:\n"
"s_call_b64 s[94:95],.Lfk_meet4\n"
"s_branch .Lfk_er_me2\n"
".Lfk_p1:\n"
"s_cmp_eq_u32 s18,2146435072\n"
"s_cbranch_scc1 .Lfk_p2\n"
".Lfk_p3:\n"
"s_lshr_b32 s22,s8,2\n"
"s_and_b32 s31,s8,0xfff1ffff\n"
"s_add_u32 s18,s18,16384\n"
"s_andn2_b32 s91,s55,16384\n"
"v_add_u32 v116,s91,v112\n"
"v_accvgpr_read_b32 v122,a140\n"
"v_add_f32 v122,v122,v103\n"
"v_rcp_f32 v122,v122\n"
"s_nop 0\n"
"v_pk_mul_f32 v[224:225],v[224:225],v[122:123] op_sel_hi:[1,0]\n"
"v_pk_mul_f32 v[226:227],v[226:227],v[122:123] op_sel_hi:[1,0]\n"
"v_pk_mul_f32 v[228:229],v[228:229],v[122:123] op_sel_hi:[1,0]\n"
"v_pk_mul_f32 v[230:231],v[230:231],v[122:123] op_sel_hi:[1,0]\n"
"v_pk_mul_f32 v[232:233],v[232:233],v[122:123] op_sel_hi:[1,0]\n"
"v_pk_mul_f32 v[234:235],v[234:235],v[122:123] op_sel_hi:[1,0]\n"
"v_pk_mul_f32 v[236:237],v[236:237],v[122:123] op_sel_hi:[1,0]\n"
"v_pk_mul_f32 v[238:239],v[238:239],v[122:123] op_sel_hi:[1,0]\n"
"v_pk_mul_f32 v[240:241],v[240:241],v[122:123] op_sel_hi:[1,0]\n"
"v_pk_mul_f32 v[242:243],v[242:243],v[122:123] op_sel_hi:[1,0]\n"
"v_pk_mul_f32 v[244:245],v[244:245],v[122:123] op_sel_hi:[1,0]\n"
"v_pk_mul_f32 v[246:247],v[246:247],v[122:123] op_sel_hi:[1,0]\n"
"v_pk_mul_f32 v[248:249],v[248:249],v[122:123] op_sel_hi:[1,0]\n"
"v_pk_mul_f32 v[250:251],v[250:251],v[122:123] op_sel_hi:[1,0]\n"
"v_pk_mul_f32 v[252:253],v[252:253],v[122:123] op_sel_hi:[1,0]\n"
"v_pk_mul_f32 v[254:255],v[254:255],v[122:123] op_sel_hi:[1,0]\n"
"s_waitcnt vmcnt(24) lgkmcnt(0)\n"
"s_barrier\n"
"ds_read_b128 v[192:195],v112 offset:0\n"
"ds_read_b128 v[196:199],v112 offset:1024\n"
"ds_read_b128 v[200:203],v112 offset:2048\n"
"ds_read_b128 v[204:207],v112 offset:3072\n"
"ds_read_b128 v[208:211],v112 offset:4096\n"
"ds_read_b128 v[212:215],v112 offset:5120\n"
"ds_read_b128 v[216:219],v112 offset:6144\n"
"ds_read_b128 v[220:223],v112 offset:7168\n"
"v_cvt_pk_bf16_f32 v224,v224,v225\n"
"v_cvt_pk_bf16_f32 v225,v226,v227\n"
"v_cvt_pk_bf16_f32 v226,v228,v229\n"
"v_cvt_pk_bf16_f32 v227,v230,v231\n"
"v_cvt_pk_bf16_f32 v232,v232,v233\n"
"v_cvt_pk_bf16_f32 v233,v234,v235\n"
"v_cvt_pk_bf16_f32 v234,v236,v237\n"
"v_cvt_pk_bf16_f32 v235,v238,v239\n"
"v_cvt_pk_bf16_f32 v240,v240,v241\n"
"v_cvt_pk_bf16_f32 v241,v242,v243\n"
"v_cvt_pk_bf16_f32 v242,v244,v245\n"
"v_cvt_pk_bf16_f32 v243,v246,v247\n"
"v_cvt_pk_bf16_f32 v248,v248,v249\n"
"v_cvt_pk_bf16_f32 v249,v250,v251\n"
"v_cvt_pk_bf16_f32 v250,v252,v253\n"
"v_cvt_pk_bf16_f32 v251,v254,v255\n"
"buffer_store_dwordx4 v[224:227],v112,s[24:27],s70 offen offset:0 sc1\n"
"buffer_store_dwordx4 v[232:235],v112,s[24:27],s70 offen offset:1024 sc1\n"
"buffer_store_dwordx4 v[240:243],v112,s[24:27],s70 offen offset:2048 sc1\n"
"buffer_store_dwordx4 v[248:251],v112,s[24:27],s70 offen offset:3072 sc1\n"
"s_waitcnt lgkmcnt(4)\n"
"v_mfma_f32_16x16x32_bf16 v[80:83],a[224:227],v[192:195],0\n"
"v_mfma_f32_16x16x32_bf16 v[80:83],a[232:235],v[196:199],v[80:83]\n"
"ds_read_b128 v[224:227],v112 offset:8192\n"
"ds_read_b128 v[228:231],v112 offset:9216\n"
"v_mfma_f32_16x16x32_bf16 v[80:83],a[240:243],v[200:203],v[80:83]\n"
"ds_read_b128 v[232:235],v112 offset:10240\n"
"ds_read_b128 v[236:239],v112 offset:11264\n"
"v_mfma_f32_16x16x32_bf16 v[80:83],a[248:251],v[204:207],v[80:83]\n"
"ds_read_b128 v[240:243],v112 offset:12288\n"
"ds_read_b128 v[244:247],v112 offset:13312\n"
"v_mfma_f32_16x16x32_bf16 v[84:87],a[252:255],v[204:207],0\n"
"ds_read_b128 v[248:251],v112 offset:14336\n"
"ds_read_b128 v[252:255],v112 offset:15360\n"
"v_mfma_f32_16x16x32_bf16 v[84:87],a[244:247],v[200:203],v[84:87]\n"
"ds_read_b128 a[68:71],v116 offset:4096\n"
"ds_read_b128 a[72:75],v116 offset:8192\n"
"v_mfma_f32_16x16x32_bf16 v[84:87],a[236:239],v[196:199],v[84:87]\n"
"ds_read_b128 a[76:79],v116 offset:12288\n"
"ds_read_b128 a[80:83],v116 offset:0\n"
"v_mfma_f32_16x16x32_bf16 v[84:87],a[228:231],v[192:195],v[84:87]\n"
"ds_read_b128 a[84:87],v116 offset:20480\n"
"ds_read_b128 a[88:91],v116 offset:24576\n"
"s_waitcnt lgkmcnt(14)\n"
"v_mfma_f32_16x16x32_bf16 v[84:87],a[4:7],v[208:211],v[84:87]\n"
"ds_read_b128 a[92:95],v116 offset:28672\n"
"v_mfma_f32_16x16x32_bf16 v[84:87],a[12:15],v[212:215],v[84:87]\n"
"ds_read_b128 v[204:207],v112 offset:19456\n"
"s_lshl_b32 s91,s78,3\n"
"v_mfma_f32_16x16x32_bf16 v[84:87],a[20:23],v[216:219],v[84:87]\n"
"ds_read_b128 v[200:203],v112 offset:18432\n"
"s_add_u32 s91,s91,131072\n"
"v_mfma_f32_16x16x32_bf16 v[84:87],a[28:31],v[220:223],v[84:87]\n"
"ds_read_b128 v[196:199],v112 offset:17408\n"
"v_lshrrev_b32 v101,2,v112\n"
"v_mfma_f32_16x16x32_bf16 v[80:83],a[24:27],v[220:223],v[80:83]\n"
"ds_read_b128 v[192:195],v112 offset:16384\n"
"v_add_u32 v101,s91,v101\n"
"v_mfma_f32_16x16x32_bf16 v[80:83],a[16:19],v[216:219],v[80:83]\n"
"ds_read_b128 v[220:223],v112 offset:23552\n"
"v_lshrrev_b32 v114,2,v112\n"
"v_mfma_f32_16x16x32_bf16 v[80:83],a[8:11],v[212:215],v[80:83]\n"
"ds_read_b128 v[216:219],v112 offset:22528\n"
"v_add_u32 v114,131328,v114\n"
"v_mfma_f32_16x16x32_bf16 v[80:83],a[0:3],v[208:211],v[80:83]\n"
"ds_read_b128 v[212:215],v112 offset:21504\n"
"s_waitcnt lgkmcnt(15)\n"
"v_mfma_f32_16x16x32_bf16 v[80:83],v[32:35],v[224:227],v[80:83]\n"
"ds_read_b128 v[208:211],v112 offset:20480\n"
"s_mov_b32 s83,148946944\n"
"v_mfma_f32_16x16x32_bf16 v[80:83],v[40:43],v[228:231],v[80:83]\n"
"buffer_load_dwordx4 v[104:107],v112,s[24:27],s83 offen offset:0 sc1\n"
"v_mov_b32 v116,0\n"
"v_mfma_f32_16x16x32_bf16 v[80:83],a[32:35],v[232:235],v[80:83]\n"
"ds_write_b32 v101,v116 offset:256\n"
"v_mov_b32 v116,1\n"
"v_mfma_f32_16x16x32_bf16 v[80:83],a[40:43],v[236:239],v[80:83]\n"
"buffer_load_dwordx4 v[108:111],v112,s[24:27],s83 offen offset:1024 sc1\n"
"v_mfma_f32_16x16x32_bf16 v[84:87],a[44:47],v[236:239],v[84:87]\n"
"s_mov_b32 s16,4\n"
"ds_read_b128 v[236:239],v112 offset:27648\n"
"v_add_u32 v113,65536,v112\n"
"v_mfma_f32_16x16x32_bf16 v[84:87],a[36:39],v[232:235],v[84:87]\n"
"ds_read_b128 v[232:235],v112 offset:26624\n"
"s_add_u32 s31,s31,8388608\n"
"v_mfma_f32_16x16x32_bf16 v[84:87],v[44:47],v[228:231],v[84:87]\n"
"ds_read_b128 v[228:231],v112 offset:25600\n"
"s_add_u32 s53,s22,786432\n"
"v_mfma_f32_16x16x32_bf16 v[84:87],v[36:39],v[224:227],v[84:87]\n"
"s_add_u32 m0,s30,65536\n"
"ds_read_b128 v[224:227],v112 offset:24576\n"
"s_waitcnt lgkmcnt(13)\n"
"v_mfma_f32_16x16x32_bf16 v[84:87],a[68:71],v[240:243],v[84:87]\n"
"buffer_load_dwordx4 v112,s[24:27],s18 offen offset:0 sc1 lds\n"
"v_mfma_f32_16x16x32_bf16 v[84:87],a[76:79],v[244:247],v[84:87]\n"
"buffer_load_dwordx4 v112,s[24:27],s18 offen offset:1024 sc1 lds\n"
"v_mfma_f32_16x16x32_bf16 v[84:87],a[84:87],v[248:251],v[84:87]\n"
"s_add_u32 s56,s22,1048576\n"
"s_add_u32 s58,s22,1310720\n"
"v_mfma_f32_16x16x32_bf16 v[84:87],a[92:95],v[252:255],v[84:87]\n"
"buffer_load_dwordx4 v112,s[24:27],s18 offen offset:2048 sc1 lds\n"
"v_mfma_f32_16x16x32_bf16 v[80:83],a[88:91],v[252:255],v[80:83]\n"
"ds_read_b128 v[252:255],v112 offset:31744\n"
"s_add_u32 s59,s22,1572864\n"
"v_mfma_f32_16x16x32_bf16 v[80:83],a[80:83],v[248:251],v[80:83]\n"
"ds_read_b128 v[248:251],v112 offset:30720\n"
"s_bfe_u32 s10,s3,0x20015\n"
"v_mfma_f32_16x16x32_bf16 v[80:83],a[72:75],v[244:247],v[80:83]\n"
"ds_read_b128 v[244:247],v112 offset:29696\n"
"s_mul_i32 s10,s10,4186112\n"
"v_mfma_f32_16x16x32_bf16 v[80:83],a[156:159],v[240:243],v[80:83]\n"
"ds_read_b128 v[240:243],v112 offset:28672\n"
"s_waitcnt lgkmcnt(13)\n"
"v_mfma_f32_16x16x32_bf16 v[64:67],a[224:227],v[192:195],0\n"
"buffer_load_dwordx4 v112,s[24:27],s18 offen offset:3072 sc1 lds\n"
"s_add_u32 m0,m0,4096\n"
"v_mfma_f32_16x16x32_bf16 v[64:67],a[232:235],v[196:199],v[64:67]\n"
"buffer_load_dwordx4 v117,s[24:27],s18 offen offset:0 sc1 lds\n"
"v_mfma_f32_16x16x32_bf16 v[64:67],a[240:243],v[200:203],v[64:67]\n"
"s_add_u32 s12,s12,s10\n"
"s_bfe_u32 s10,s3,0x30012\n"
"v_mfma_f32_16x16x32_bf16 v[64:67],a[248:251],v[204:207],v[64:67]\n"
"buffer_load_dwordx4 v117,s[24:27],s18 offen offset:1024 sc1 lds\n"
"v_mfma_f32_16x16x32_bf16 v[68:71],a[252:255],v[204:207],0\n"
"ds_read_b128 v[204:207],v112 offset:35840\n"
"s_lshl_b32 s10,s10,12\n"
"v_mfma_f32_16x16x32_bf16 v[68:71],a[244:247],v[200:203],v[68:71]\n"
"ds_read_b128 v[200:203],v112 offset:34816\n"
"s_add_u32 s12,s12,s10\n"
"v_mfma_f32_16x16x32_bf16 v[68:71],a[236:239],v[196:199],v[68:71]\n"
"ds_read_b128 v[196:199],v112 offset:33792\n"
"s_bfe_u32 s10,s3,0x50012\n"
"v_mfma_f32_16x16x32_bf16 v[68:71],a[228:231],v[192:195],v[68:71]\n"
"ds_read_b128 v[192:195],v112 offset:32768\n"
"s_waitcnt lgkmcnt(13)\n"
"s_waitcnt vmcnt(8) lgkmcnt(12)\n"
"s_barrier\n"
"v_mfma_f32_16x16x32_bf16 v[68:71],a[4:7],v[208:211],v[68:71]\n"
"buffer_store_dwordx2 a[154:155],off,s[24:27],s71 sc1\n"
"buffer_load_dwordx4 v117,s[24:27],s18 offen offset:2048 sc1 lds\n"
"v_mfma_f32_16x16x32_bf16 v[68:71],a[12:15],v[212:215],v[68:71]\n"
"buffer_load_dwordx4 v117,s[24:27],s18 offen offset:3072 sc1 lds\n"
"v_mfma_f32_16x16x32_bf16 v[68:71],a[20:23],v[216:219],v[68:71]\n"
"s_sub_u32 s82,s3,212992\n"
"s_lshl_b32 s10,s10,8\n"
"v_mfma_f32_16x16x32_bf16 v[68:71],a[28:31],v[220:223],v[68:71]\n"
"buffer_load_dwordx4 a[48:51],v112,s[24:27],s82 offen offset:0 sc1\n"
"v_mfma_f32_16x16x32_bf16 v[64:67],a[24:27],v[220:223],v[64:67]\n"
"s_add_u32 s10,s10,149061632\n"
"ds_read_b128 v[220:223],v112 offset:39936\n"
"s_mov_b32 s14,0\n"
"v_mfma_f32_16x16x32_bf16 v[64:67],a[16:19],v[216:219],v[64:67]\n"
"ds_read_b128 v[216:219],v112 offset:38912\n"
"v_not_b32 v122,s80\n"
"v_mfma_f32_16x16x32_bf16 v[64:67],a[8:11],v[212:215],v[64:67]\n"
"s_cmp_eq_u32 s52,0\n"
"ds_read_b128 v[212:215],v112 offset:37888\n"
"s_cselect_b64 s[14:15],s[14:15],0\n"
"v_mfma_f32_16x16x32_bf16 v[64:67],a[0:3],v[208:211],v[64:67]\n"
"s_cselect_b32 s10,s10,2146435072\n"
"ds_read_b128 v[208:211],v112 offset:36864\n"
"s_waitcnt lgkmcnt(12)\n"
"v_mfma_f32_16x16x32_bf16 v[64:67],v[32:35],v[224:227],v[64:67]\n"
"buffer_load_dwordx4 a[52:55],v112,s[24:27],s82 offen offset:1024 sc1\n"
"s_cmp_lg_u32 s15,0\n"
"s_cselect_b32 s91,s11,2146435072\n"
"v_mfma_f32_16x16x32_bf16 v[64:67],v[40:43],v[228:231],v[64:67]\n"
"buffer_load_dwordx4 a[56:59],v112,s[24:27],s82 offen offset:2048 sc1\n"
"buffer_load_dwordx2 v[122:123],v115,s[24:27],s91 offen sc1\n"
"v_mfma_f32_16x16x32_bf16 v[64:67],a[32:35],v[232:235],v[64:67]\n"
"s_mov_b32 s89,0\n"
"s_mov_b32 s20,32768\n"
"v_mfma_f32_16x16x32_bf16 v[64:67],a[40:43],v[236:239],v[64:67]\n"
"buffer_load_dwordx4 a[60:63],v112,s[24:27],s82 offen offset:3072 sc1\n"
"v_mfma_f32_16x16x32_bf16 v[68:71],a[44:47],v[236:239],v[68:71]\n"
"s_mov_b32 s19,0xffff\n"
"ds_read_b128 v[236:239],v112 offset:44032\n"
"s_mov_b32 s21,32\n"
"v_mfma_f32_16x16x32_bf16 v[68:71],a[36:39],v[232:235],v[68:71]\n"
"ds_read_b128 v[232:235],v112 offset:43008\n"
"v_mfma_f32_16x16x32_bf16 v[68:71],v[44:47],v[228:231],v[68:71]\n"
"ds_read_b128 v[228:231],v112 offset:41984\n"
"v_mfma_f32_16x16x32_bf16 v[68:71],v[36:39],v[224:227],v[68:71]\n"
"s_sub_u32 s82,s3,229376\n"
"ds_read_b128 v[224:227],v112 offset:40960\n"
"s_waitcnt lgkmcnt(12)\n"
"v_mfma_f32_16x16x32_bf16 v[68:71],a[68:71],v[240:243],v[68:71]\n"
"buffer_load_dwordx4 v[48:51],v112,s[24:27],s82 offen offset:0 sc1\n"
"v_mfma_f32_16x16x32_bf16 v[68:71],a[76:79],v[244:247],v[68:71]\n"
"buffer_load_dwordx4 v[52:55],v112,s[24:27],s82 offen offset:1024 sc1\n"
"v_mfma_f32_16x16x32_bf16 v[68:71],a[84:87],v[248:251],v[68:71]\n"
"buffer_load_dwordx4 v[56:59],v112,s[24:27],s82 offen offset:2048 sc1\n"
"v_mfma_f32_16x16x32_bf16 v[68:71],a[92:95],v[252:255],v[68:71]\n"
"s_waitcnt vmcnt(17)\n"
"v_mfma_f32_16x16x32_bf16 v[64:67],a[88:91],v[252:255],v[64:67]\n"
"ds_read_b128 v[252:255],v112 offset:48128\n"
"v_xor_b32 v119,s80,v104\n"
"v_mfma_f32_16x16x32_bf16 v[64:67],a[80:83],v[248:251],v[64:67]\n"
"ds_read_b128 v[248:251],v112 offset:47104\n"
"v_xor_b32 v120,s81,v105\n"
"v_mfma_f32_16x16x32_bf16 v[64:67],a[72:75],v[244:247],v[64:67]\n"
"ds_read_b128 v[244:247],v112 offset:46080\n"
"v_or_b32 v119,v119,v120\n"
"v_mfma_f32_16x16x32_bf16 v[64:67],a[156:159],v[240:243],v[64:67]\n"
"ds_read_b128 v[240:243],v112 offset:45056\n"
"s_waitcnt lgkmcnt(12)\n"
"v_mfma_f32_16x16x32_bf16 v[72:75],a[224:227],v[192:195],0\n"
"buffer_load_dwordx4 v[60:63],v112,s[24:27],s82 offen offset:3072 sc1\n"
"s_sub_u32 s82,s3,245760\n"
"v_mfma_f32_16x16x32_bf16 v[72:75],a[232:235],v[196:199],v[72:75]\n"
"buffer_load_dwordx4 v[16:19],v112,s[24:27],s82 offen offset:0 sc1\n"
"v_xor_b32 v120,s80,v106\n"
"v_mfma_f32_16x16x32_bf16 v[72:75],a[240:243],v[200:203],v[72:75]\n"
"v_or_b32 v119,v119,v120\n"
"v_xor_b32 v120,s81,v107\n"
"v_mfma_f32_16x16x32_bf16 v[72:75],a[248:251],v[204:207],v[72:75]\n"
"buffer_load_dwordx4 v[20:23],v112,s[24:27],s82 offen offset:1024 sc1\n"
"v_or_b32 v119,v119,v120\n"
"v_mfma_f32_16x16x32_bf16 v[76:79],a[252:255],v[204:207],0\n"
"ds_read_b128 v[204:207],v112 offset:52224\n"
"v_xor_b32 v120,s80,v108\n"
"v_mfma_f32_16x16x32_bf16 v[76:79],a[244:247],v[200:203],v[76:79]\n"
"ds_read_b128 v[200:203],v112 offset:51200\n"
"v_or_b32 v119,v119,v120\n"
"v_mfma_f32_16x16x32_bf16 v[76:79],a[236:239],v[196:199],v[76:79]\n"
"ds_read_b128 v[196:199],v112 offset:50176\n"
"v_xor_b32 v120,s81,v109\n"
"v_mfma_f32_16x16x32_bf16 v[76:79],a[228:231],v[192:195],v[76:79]\n"
"ds_read_b128 v[192:195],v112 offset:49152\n"
"s_waitcnt lgkmcnt(12)\n"
"v_mfma_f32_16x16x32_bf16 v[76:79],a[4:7],v[208:211],v[76:79]\n"
"buffer_load_dwordx4 v[24:27],v112,s[24:27],s82 offen offset:2048 sc1\n"
"v_or_b32 v119,v119,v120\n"
"v_mfma_f32_16x16x32_bf16 v[76:79],a[12:15],v[212:215],v[76:79]\n"
"buffer_load_dwordx4 v[28:31],v112,s[24:27],s82 offen offset:3072 sc1\n"
"v_mfma_f32_16x16x32_bf16 v[76:79],a[20:23],v[216:219],v[76:79]\n"
"s_add_u32 s18,s18,2097152\n"
"v_xor_b32 v120,s80,v110\n"
"s_add_u32 m0,s30,98304\n"
"v_mfma_f32_16x16x32_bf16 v[76:79],a[28:31],v[220:223],v[76:79]\n"
"buffer_load_dwordx4 v112,s[24:27],s18 offen offset:0 sc1 lds\n"
"v_mfma_f32_16x16x32_bf16 v[72:75],a[24:27],v[220:223],v[72:75]\n"
"ds_read_b128 v[220:223],v112 offset:56320\n"
"v_or_b32 v119,v119,v120\n"
"v_mfma_f32_16x16x32_bf16 v[72:75],a[16:19],v[216:219],v[72:75]\n"
"ds_read_b128 v[216:219],v112 offset:55296\n"
"v_xor_b32 v120,s81,v111\n"
"v_mfma_f32_16x16x32_bf16 v[72:75],a[8:11],v[212:215],v[72:75]\n"
"ds_read_b128 v[212:215],v112 offset:54272\n"
"v_or_b32 v119,v119,v120\n"
"v_mfma_f32_16x16x32_bf16 v[72:75],a[0:3],v[208:211],v[72:75]\n"
"ds_read_b128 v[208:211],v112 offset:53248\n"
"s_waitcnt lgkmcnt(12)\n"
"v_mfma_f32_16x16x32_bf16 v[72:75],v[32:35],v[224:227],v[72:75]\n"
"buffer_load_dwordx4 v112,s[24:27],s18 offen offset:1024 sc1 lds\n"
"v_mfma_f32_16x16x32_bf16 v[72:75],v[40:43],v[228:231],v[72:75]\n"
"buffer_load_dwordx4 v112,s[24:27],s18 offen offset:2048 sc1 lds\n"
"v_mfma_f32_16x16x32_bf16 v[72:75],a[32:35],v[232:235],v[72:75]\n"
"v_cmp_ne_u32 vcc,0,v119\n"
"v_mfma_f32_16x16x32_bf16 v[72:75],a[40:43],v[236:239],v[72:75]\n"
"buffer_load_dwordx4 v112,s[24:27],s18 offen offset:3072 sc1 lds\n"
"v_mfma_f32_16x16x32_bf16 v[76:79],a[44:47],v[236:239],v[76:79]\n"
"ds_read_b128 v[236:239],v112 offset:60416\n"
"v_mfma_f32_16x16x32_bf16 v[76:79],a[36:39],v[232:235],v[76:79]\n"
"ds_read_b128 v[232:235],v112 offset:59392\n"
"v_xor_b32 v119,s80,v122\n"
"v_mfma_f32_16x16x32_bf16 v[76:79],v[44:47],v[228:231],v[76:79]\n"
"ds_read_b128 v[228:231],v112 offset:58368\n"
"v_xor_b32 v120,s81,v123\n"
"v_mfma_f32_16x16x32_bf16 v[76:79],v[36:39],v[224:227],v[76:79]\n"
"s_add_u32 m0,m0,4096\n"
"ds_read_b128 v[224:227],v112 offset:57344\n"
"s_waitcnt lgkmcnt(12)\n"
"v_mfma_f32_16x16x32_bf16 v[76:79],a[68:71],v[240:243],v[76:79]\n"
"buffer_load_dwordx4 v117,s[24:27],s18 offen offset:0 sc1 lds\n"
"v_or_b32 v119,v119,v120\n"
"v_mfma_f32_16x16x32_bf16 v[76:79],a[76:79],v[244:247],v[76:79]\n"
"buffer_load_dwordx4 v117,s[24:27],s18 offen offset:1024 sc1 lds\n"
"v_cmp_ne_u32 s[28:29],0,v119\n"
"v_mfma_f32_16x16x32_bf16 v[76:79],a[84:87],v[248:251],v[76:79]\n"
"s_and_b64 s[14:15],s[14:15],s[28:29]\n"
"s_cselect_b32 s91,s11,2146435072\n"
"buffer_load_dwordx2 v[122:123],v115,s[24:27],s91 offen sc1\n"
"v_mfma_f32_16x16x32_bf16 v[76:79],a[92:95],v[252:255],v[76:79]\n"
"buffer_load_dwordx4 v117,s[24:27],s18 offen offset:2048 sc1 lds\n"
"v_mfma_f32_16x16x32_bf16 v[72:75],a[88:91],v[252:255],v[72:75]\n"
"ds_read_b128 v[252:255],v112 offset:64512\n"
"v_mfma_f32_16x16x32_bf16 v[72:75],a[80:83],v[248:251],v[72:75]\n"
"ds_read_b128 v[248:251],v112 offset:63488\n"
"v_mfma_f32_16x16x32_bf16 v[72:75],a[72:75],v[244:247],v[72:75]\n"
"ds_read_b128 v[244:247],v112 offset:62464\n"
"v_mfma_f32_16x16x32_bf16 v[72:75],a[156:159],v[240:243],v[72:75]\n"
"s_and_b32 s83,s15,0xffff\n"
"ds_read_b128 v[240:243],v112 offset:61440\n"
"s_waitcnt lgkmcnt(12)\n"
"v_mfma_f32_16x16x32_bf16 v[88:91],a[224:227],v[192:195],0\n"
"buffer_load_dwordx4 v117,s[24:27],s18 offen offset:3072 sc1 lds\n"
"v_mfma_f32_16x16x32_bf16 v[88:91],a[232:235],v[196:199],v[88:91]\n"
"s_cbranch_scc1 .Lfk_es_lkone\n"
".Lfk_er_lkone:\n"
"v_mfma_f32_16x16x32_bf16 v[88:91],a[240:243],v[200:203],v[88:91]\n"
"s_waitcnt vmcnt(22)\n"
"ds_add_u32 v114,v116\n"
"v_mfma_f32_16x16x32_bf16 v[88:91],a[248:251],v[204:207],v[88:91]\n"
"v_and_b32 v118,0xf0,v112\n"
"v_lshrrev_b32 v120,8,v112\n"
"v_mfma_f32_16x16x32_bf16 v[92:95],a[252:255],v[204:207],0\n"
"v_lshlrev_b32 v120,4,v120\n"
"v_lshl_add_u32 v118,v118,2,v120\n"
"v_mfma_f32_16x16x32_bf16 v[92:95],a[244:247],v[200:203],v[92:95]\n"
"s_lshr_b32 s82,s78,20\n"
"v_xor_b32 v118,s82,v118\n"
"v_mov_b32 v100,1\n"
"v_mfma_f32_16x16x32_bf16 v[92:95],a[236:239],v[196:199],v[92:95]\n"
"v_add_u32 v118,s12,v118\n"
"v_mov_b32 v102,131076\n"
"v_mfma_f32_16x16x32_bf16 v[92:95],a[228:231],v[192:195],v[92:95]\n"
"s_waitcnt lgkmcnt(9)\n"
"v_mfma_f32_16x16x32_bf16 v[92:95],a[4:7],v[208:211],v[92:95]\n"
"v_mfma_f32_16x16x32_bf16 v[92:95],a[12:15],v[212:215],v[92:95]\n"
"ds_read_b32 v125,v114\n"
"v_mfma_f32_16x16x32_bf16 v[92:95],a[20:23],v[216:219],v[92:95]\n"
"v_mfma_f32_16x16x32_bf16 v[92:95],a[28:31],v[220:223],v[92:95]\n"
"v_mfma_f32_16x16x32_bf16 v[88:91],a[24:27],v[220:223],v[88:91]\n"
"v_mfma_f32_16x16x32_bf16 v[88:91],a[16:19],v[216:219],v[88:91]\n"
"s_waitcnt lgkmcnt(0)\n"
"v_cmp_gt_u32 s[28:29],s16,v125\n"
"v_mfma_f32_16x16x32_bf16 v[88:91],a[8:11],v[212:215],v[88:91]\n"
"s_add_u32 s18,s8,4194304\n"
"s_add_u32 s8,s8,5242880\n"
"s_or_b64 vcc,vcc,s[28:29]\n"
"v_mfma_f32_16x16x32_bf16 v[88:91],a[0:3],v[208:211],v[88:91]\n"
"s_cbranch_vccnz .Lfk_z0\n"
".Lfk_z1:\n"
"ds_read_b128 v[192:195],v113 offset:0\n"
"v_mfma_f32_16x16x32_bf16 v[88:91],v[32:35],v[224:227],v[88:91]\n"
"s_mov_b32 s89,1\n"
"s_mov_b32 s19,0xff\n"
"s_lshl_b64 exec,1,s89\n"
"buffer_atomic_add v100,off,s[24:27],s10 sc0\n"
"s_mov_b64 exec,-1\n"
"v_mfma_f32_16x16x32_bf16 v[88:91],v[40:43],v[228:231],v[88:91]\n"
"ds_read_b128 v[196:199],v113 offset:1024\n"
"s_add_u32 m0,s30,0\n"
"v_mfma_f32_16x16x32_bf16 v[88:91],a[32:35],v[232:235],v[88:91]\n"
"ds_read_b128 v[200:203],v113 offset:2048\n"
"buffer_load_dwordx4 v112,s[24:27],s18 offen offset:0 sc1 lds\n"
"v_mfma_f32_16x16x32_bf16 v[88:91],a[40:43],v[236:239],v[88:91]\n"
"s_add_u32 s89,s89,1\n"
"ds_read_b128 v[204:207],v113 offset:3072\n"
"s_add_u32 s21,s21,16\n"
"v_mfma_f32_16x16x32_bf16 v[92:95],a[44:47],v[236:239],v[92:95]\n"
"ds_read_b128 v[208:211],v113 offset:16384\n"
"v_mfma_f32_16x16x32_bf16 v[92:95],a[36:39],v[232:235],v[92:95]\n"
"ds_read_b128 v[212:215],v113 offset:17408\n"
"s_add_u32 s16,s16,4\n"
"v_mfma_f32_16x16x32_bf16 v[92:95],v[44:47],v[228:231],v[92:95]\n"
"ds_read_b128 v[216:219],v113 offset:18432\n"
"buffer_load_dwordx4 v112,s[24:27],s18 offen offset:1024 sc1 lds\n"
"v_mfma_f32_16x16x32_bf16 v[92:95],v[36:39],v[224:227],v[92:95]\n"
"ds_read_b128 v[220:223],v113 offset:19456\n"
"v_mfma_f32_16x16x32_bf16 v[92:95],a[68:71],v[240:243],v[92:95]\n"
"ds_read_b128 v[236:239],v113 offset:7168\n"
"v_mfma_f32_16x16x32_bf16 v[92:95],a[76:79],v[244:247],v[92:95]\n"
"ds_read_b128 v[232:235],v113 offset:6144\n"
"ds_read_b128 v[228:231],v113 offset:5120\n"
"v_mfma_f32_16x16x32_bf16 v[92:95],a[84:87],v[248:251],v[92:95]\n"
"buffer_load_dwordx4 v112,s[24:27],s18 offen offset:2048 sc1 lds\n"
"v_mfma_f32_16x16x32_bf16 v[92:95],a[92:95],v[252:255],v[92:95]\n"
"ds_read_b128 v[224:227],v113 offset:4096\n"
"v_mfma_f32_16x16x32_bf16 v[88:91],a[88:91],v[252:255],v[88:91]\n"
"ds_read_b128 v[252:255],v113 offset:23552\n"
"v_mfma_f32_16x16x32_bf16 v[88:91],a[80:83],v[248:251],v[88:91]\n"
"ds_read_b128 v[248:251],v113 offset:22528\n"
"v_mfma_f32_16x16x32_bf16 v[88:91],a[72:75],v[244:247],v[88:91]\n"
"ds_read_b128 v[244:247],v113 offset:21504\n"
"v_mfma_f32_16x16x32_bf16 v[88:91],a[156:159],v[240:243],v[88:91]\n"
"ds_read_b128 v[240:243],v113 offset:20480\n"
"s_waitcnt lgkmcnt(12)\n"
"v_mfma_f32_16x16x32_bf16 v[80:83],v[128:131],v[192:195],v[80:83]\n"
"buffer_load_dwordx4 v112,s[24:27],s18 offen offset:3072 sc1 lds\n"
"v_mfma_f32_16x16x32_bf16 v[80:83],v[136:139],v[196:199],v[80:83]\n"
"v_mfma_f32_16x16x32_bf16 v[80:83],v[144:147],v[200:203],v[80:83]\n"
"s_add_u32 m0,m0,4096\n"
"v_mfma_f32_16x16x32_bf16 v[80:83],v[152:155],v[204:207],v[80:83]\n"
"buffer_load_dwordx4 v117,s[24:27],s18 offen offset:0 sc1 lds\n"
"v_mfma_f32_16x16x32_bf16 v[84:87],v[156:159],v[204:207],v[84:87]\n"
"ds_read_b128 v[204:207],v113 offset:11264\n"
"s_add_u32 s20,s20,32768\n"
"v_mfma_f32_16x16x32_bf16 v[84:87],v[148:151],v[200:203],v[84:87]\n"
"ds_read_b128 v[200:203],v113 offset:10240\n"
"s_and_b32 s20,s20,0x1ffff\n"
"v_mfma_f32_16x16x32_bf16 v[84:87],v[140:143],v[196:199],v[84:87]\n"
"ds_read_b128 v[196:199],v113 offset:9216\n"
"v_mfma_f32_16x16x32_bf16 v[84:87],v[132:135],v[192:195],v[84:87]\n"
"ds_read_b128 v[192:195],v113 offset:8192\n"
"s_waitcnt lgkmcnt(12)\n"
"v_mfma_f32_16x16x32_bf16 v[68:71],v[132:135],v[208:211],v[68:71]\n"
"buffer_load_dwordx4 v117,s[24:27],s18 offen offset:1024 sc1 lds\n"
"v_mfma_f32_16x16x32_bf16 v[68:71],v[140:143],v[212:215],v[68:71]\n"
"v_mfma_f32_16x16x32_bf16 v[68:71],v[148:151],v[216:219],v[68:71]\n"
"v_mfma_f32_16x16x32_bf16 v[68:71],v[156:159],v[220:223],v[68:71]\n"
"buffer_load_dwordx4 v117,s[24:27],s18 offen offset:2048 sc1 lds\n"
"v_mfma_f32_16x16x32_bf16 v[64:67],v[152:155],v[220:223],v[64:67]\n"
"ds_read_b128 v[220:223],v113 offset:27648\n"
"v_xor_b32 v119,s80,v122\n"
"v_mfma_f32_16x16x32_bf16 v[64:67],v[144:147],v[216:219],v[64:67]\n"
"ds_read_b128 v[216:219],v113 offset:26624\n"
"v_xor_b32 v120,s81,v123\n"
"v_mfma_f32_16x16x32_bf16 v[64:67],v[136:139],v[212:215],v[64:67]\n"
"ds_read_b128 v[212:215],v113 offset:25600\n"
"v_or_b32 v119,v119,v120\n"
"v_mfma_f32_16x16x32_bf16 v[64:67],v[128:131],v[208:211],v[64:67]\n"
"ds_read_b128 v[208:211],v113 offset:24576\n"
"s_waitcnt lgkmcnt(12)\n"
"v_mfma_f32_16x16x32_bf16 v[80:83],v[0:3],v[224:227],v[80:83]\n"
"buffer_load_dwordx4 v117,s[24:27],s18 offen offset:3072 sc1 lds\n"
"v_cmp_ne_u32 s[28:29],0,v119\n"
"v_mfma_f32_16x16x32_bf16 v[80:83],v[8:11],v[228:231],v[80:83]\n"
"s_and_b64 s[14:15],s[14:15],s[28:29]\n"
"s_cselect_b32 s91,s11,2146435072\n"
"s_add_u32 m0,s30,32768\n"
"v_mfma_f32_16x16x32_bf16 v[80:83],v[160:163],v[232:235],v[80:83]\n"
"buffer_load_dwordx2 v[122:123],v115,s[24:27],s91 offen sc1\n"
"v_mfma_f32_16x16x32_bf16 v[80:83],v[168:171],v[236:239],v[80:83]\n"
"buffer_load_dwordx4 v112,s[24:27],s8 offen offset:0 sc1 lds\n"
"v_mfma_f32_16x16x32_bf16 v[84:87],v[172:175],v[236:239],v[84:87]\n"
"ds_read_b128 v[236:239],v113 offset:15360\n"
"v_mfma_f32_16x16x32_bf16 v[84:87],v[164:167],v[232:235],v[84:87]\n"
"ds_read_b128 v[232:235],v113 offset:14336\n"
"v_mfma_f32_16x16x32_bf16 v[84:87],v[12:15],v[228:231],v[84:87]\n"
"ds_read_b128 v[228:231],v113 offset:13312\n"
"v_mfma_f32_16x16x32_bf16 v[84:87],v[4:7],v[224:227],v[84:87]\n"
"s_and_b32 s83,s15,0xff0000\n"
"ds_read_b128 v[224:227],v113 offset:12288\n"
"s_waitcnt lgkmcnt(12)\n"
"v_mfma_f32_16x16x32_bf16 v[68:71],v[4:7],v[240:243],v[68:71]\n"
"buffer_load_dwordx4 v112,s[24:27],s8 offen offset:1024 sc1 lds\n"
"v_mfma_f32_16x16x32_bf16 v[68:71],v[12:15],v[244:247],v[68:71]\n"
"s_cbranch_scc1 .Lfk_es_lkonem\n"
".Lfk_er_lkonem:\n"
"v_mfma_f32_16x16x32_bf16 v[68:71],v[164:167],v[248:251],v[68:71]\n"
"v_mfma_f32_16x16x32_bf16 v[68:71],v[172:175],v[252:255],v[68:71]\n"
"buffer_load_dwordx4 v112,s[24:27],s8 offen offset:2048 sc1 lds\n"
"v_mfma_f32_16x16x32_bf16 v[64:67],v[168:171],v[252:255],v[64:67]\n"
"ds_read_b128 v[252:255],v113 offset:31744\n"
"v_mfma_f32_16x16x32_bf16 v[64:67],v[160:163],v[248:251],v[64:67]\n"
"ds_read_b128 v[248:251],v113 offset:30720\n"
"v_mfma_f32_16x16x32_bf16 v[64:67],v[8:11],v[244:247],v[64:67]\n"
"ds_read_b128 v[244:247],v113 offset:29696\n"
"v_mfma_f32_16x16x32_bf16 v[64:67],v[0:3],v[240:243],v[64:67]\n"
"ds_read_b128 v[240:243],v113 offset:28672\n"
"s_waitcnt lgkmcnt(12)\n"
"v_mfma_f32_16x16x32_bf16 v[80:83],v[176:179],v[192:195],v[80:83]\n"
"s_waitcnt vmcnt(13)\n"
"ds_add_u32 v114,v116\n"
"v_mfma_f32_16x16x32_bf16 v[80:83],v[184:187],v[196:199],v[80:83]\n"
"buffer_load_dwordx4 v112,s[24:27],s8 offen offset:3072 sc1 lds\n"
"v_mfma_f32_16x16x32_bf16 v[80:83],a[48:51],v[200:203],v[80:83]\n"
"s_add_u32 m0,m0,4096\n"
"v_mfma_f32_16x16x32_bf16 v[80:83],a[56:59],v[204:207],v[80:83]\n"
"s_add_u32 s83,s20,32768\n"
"s_and_b32 s83,s83,0x1ffff\n"
"v_add_u32 v113,s83,v112\n"
"v_mfma_f32_16x16x32_bf16 v[84:87],a[60:63],v[204:207],v[84:87]\n"
"buffer_load_dwordx4 v117,s[24:27],s8 offen offset:0 sc1 lds\n"
"v_mfma_f32_16x16x32_bf16 v[84:87],a[52:55],v[200:203],v[84:87]\n"
"s_cmp_eq_u32 s52,0\n"
"v_not_b32 v104,s80\n"
"s_cselect_b32 s9,s9,2146435072\n"
"v_mfma_f32_16x16x32_bf16 v[84:87],v[188:191],v[196:199],v[84:87]\n"
"buffer_load_dwordx2 v[104:105],v115,s[24:27],s9 offen sc1\n"
"v_mfma_f32_16x16x32_bf16 v[84:87],v[180:183],v[192:195],v[84:87]\n"
"buffer_load_dwordx4 v117,s[24:27],s8 offen offset:1024 sc1 lds\n"
"s_waitcnt lgkmcnt(9)\n"
"v_mfma_f32_16x16x32_bf16 v[68:71],v[180:183],v[208:211],v[68:71]\n"
"v_mfma_f32_16x16x32_bf16 v[68:71],v[188:191],v[212:215],v[68:71]\n"
"ds_read_b32 v125,v114\n"
"v_mfma_f32_16x16x32_bf16 v[68:71],a[52:55],v[216:219],v[68:71]\n"
"buffer_load_dwordx4 v117,s[24:27],s8 offen offset:2048 sc1 lds\n"
"v_mfma_f32_16x16x32_bf16 v[68:71],a[60:63],v[220:223],v[68:71]\n"
"v_mfma_f32_16x16x32_bf16 v[64:67],a[56:59],v[220:223],v[64:67]\n"
"v_mfma_f32_16x16x32_bf16 v[64:67],a[48:51],v[216:219],v[64:67]\n"
"s_waitcnt lgkmcnt(0)\n"
"v_cmp_gt_u32 vcc,s16,v125\n"
"v_mfma_f32_16x16x32_bf16 v[64:67],v[184:187],v[212:215],v[64:67]\n"
"buffer_load_dwordx4 v117,s[24:27],s8 offen offset:3072 sc1 lds\n"
"v_mfma_f32_16x16x32_bf16 v[64:67],v[176:179],v[208:211],v[64:67]\n"
"s_lshl_b64 exec,1,s89\n"
"buffer_atomic_add v100,off,s[24:27],s10 sc0\n"
"s_mov_b64 exec,-1\n"
"s_add_u32 s8,s8,1048576\n"
"s_cbranch_vccnz .Lfk_es_mone\n"
".Lfk_er_mone:\n"
"v_mfma_f32_16x16x32_bf16 v[80:83],v[48:51],v[224:227],v[80:83]\n"
"ds_read_b128 v[192:195],v113 offset:0\n"
"v_mfma_f32_16x16x32_bf16 v[80:83],v[56:59],v[228:231],v[80:83]\n"
"ds_read_b128 v[196:199],v113 offset:1024\n"
"v_mfma_f32_16x16x32_bf16 v[80:83],v[16:19],v[232:235],v[80:83]\n"
"ds_read_b128 v[200:203],v113 offset:2048\n"
"s_add_u32 s89,s89,1\n"
"v_mfma_f32_16x16x32_bf16 v[80:83],v[24:27],v[236:239],v[80:83]\n"
"ds_read_b128 v[204:207],v113 offset:3072\n"
"s_add_u32 s21,s21,8\n"
"v_mfma_f32_16x16x32_bf16 v[84:87],v[28:31],v[236:239],v[84:87]\n"
"ds_read_b128 v[208:211],v113 offset:4096\n"
"v_mfma_f32_16x16x32_bf16 v[84:87],v[20:23],v[232:235],v[84:87]\n"
"ds_read_b128 v[212:215],v113 offset:5120\n"
"s_add_u32 s16,s16,4\n"
"v_mfma_f32_16x16x32_bf16 v[84:87],v[60:63],v[228:231],v[84:87]\n"
"ds_read_b128 v[216:219],v113 offset:6144\n"
"v_mfma_f32_16x16x32_bf16 v[84:87],v[52:55],v[224:227],v[84:87]\n"
"ds_read_b128 v[220:223],v113 offset:7168\n"
"v_mfma_f32_16x16x32_bf16 v[68:71],v[52:55],v[240:243],v[68:71]\n"
"ds_read_b128 v[236:239],v113 offset:11264\n"
"v_mfma_f32_16x16x32_bf16 v[68:71],v[60:63],v[244:247],v[68:71]\n"
"ds_read_b128 v[232:235],v113 offset:10240\n"
"v_mfma_f32_16x16x32_bf16 v[68:71],v[20:23],v[248:251],v[68:71]\n"
"ds_read_b128 v[228:231],v113 offset:9216\n"
"ds_read_b128 v[224:227],v113 offset:8192\n"
"v_mfma_f32_16x16x32_bf16 v[68:71],v[28:31],v[252:255],v[68:71]\n"
"v_cvt_pk_bf16_f32 v80,v80,v81\n"
"v_cvt_pk_bf16_f32 v81,v82,v83\n"
"v_mfma_f32_16x16x32_bf16 v[64:67],v[24:27],v[252:255],v[64:67]\n"
"ds_read_b128 v[252:255],v113 offset:15360\n"
"v_cvt_pk_bf16_f32 v82,v84,v85\n"
"v_cvt_pk_bf16_f32 v83,v86,v87\n"
"v_mfma_f32_16x16x32_bf16 v[64:67],v[16:19],v[248:251],v[64:67]\n"
"ds_read_b128 v[248:251],v113 offset:14336\n"
"v_max_u32 v80,1,v80\n"
"v_max_u32 v81,1,v81\n"
"v_mfma_f32_16x16x32_bf16 v[64:67],v[56:59],v[244:247],v[64:67]\n"
"ds_read_b128 v[244:247],v113 offset:13312\n"
"v_max_u32 v82,1,v82\n"
"v_max_u32 v83,1,v83\n"
"v_mfma_f32_16x16x32_bf16 v[64:67],v[48:51],v[240:243],v[64:67]\n"
"ds_read_b128 v[240:243],v113 offset:12288\n"
"buffer_store_dwordx4 v[80:83],v118,s[24:27],s22 offen sc1\n"
"s_waitcnt lgkmcnt(12)\n"
"v_mfma_f32_16x16x32_bf16 v[72:75],v[128:131],v[192:195],v[72:75]\n"
"s_add_u32 m0,s30,s20\n"
"s_add_u32 s22,s22,262144\n"
"v_mfma_f32_16x16x32_bf16 v[72:75],v[136:139],v[196:199],v[72:75]\n"
"buffer_load_dwordx4 v112,s[24:27],s8 offen offset:0 sc1 lds\n"
"v_mfma_f32_16x16x32_bf16 v[72:75],v[144:147],v[200:203],v[72:75]\n"
"v_mfma_f32_16x16x32_bf16 v[72:75],v[152:155],v[204:207],v[72:75]\n"
"v_cvt_pk_bf16_f32 v64,v64,v65\n"
"v_mfma_f32_16x16x32_bf16 v[76:79],v[156:159],v[204:207],v[76:79]\n"
"ds_read_b128 v[204:207],v113 offset:19456\n"
"s_add_u32 s20,s20,32768\n"
"v_mfma_f32_16x16x32_bf16 v[76:79],v[148:151],v[200:203],v[76:79]\n"
"ds_read_b128 v[200:203],v113 offset:18432\n"
"s_and_b32 s20,s20,0x1ffff\n"
"v_mfma_f32_16x16x32_bf16 v[76:79],v[140:143],v[196:199],v[76:79]\n"
"ds_read_b128 v[196:199],v113 offset:17408\n"
"v_cvt_pk_bf16_f32 v65,v66,v67\n"
"v_mfma_f32_16x16x32_bf16 v[76:79],v[132:135],v[192:195],v[76:79]\n"
"ds_read_b128 v[192:195],v113 offset:16384\n"
"v_cvt_pk_bf16_f32 v66,v68,v69\n"
"s_waitcnt lgkmcnt(12)\n"
"v_mfma_f32_16x16x32_bf16 v[76:79],v[4:7],v[208:211],v[76:79]\n"
"buffer_load_dwordx4 v112,s[24:27],s8 offen offset:1024 sc1 lds\n"
"v_cvt_pk_bf16_f32 v67,v70,v71\n"
"v_mfma_f32_16x16x32_bf16 v[76:79],v[12:15],v[212:215],v[76:79]\n"
"v_max_u32 v64,1,v64\n"
"v_max_u32 v65,1,v65\n"
"v_max_u32 v66,1,v66\n"
"v_max_u32 v67,1,v67\n"
"v_mfma_f32_16x16x32_bf16 v[76:79],v[164:167],v[216:219],v[76:79]\n"
"buffer_store_dwordx4 v[64:67],v118,s[24:27],s22 offen sc1\n"
"v_mfma_f32_16x16x32_bf16 v[76:79],v[172:175],v[220:223],v[76:79]\n"
"buffer_load_dwordx4 v112,s[24:27],s8 offen offset:2048 sc1 lds\n"
"v_mfma_f32_16x16x32_bf16 v[72:75],v[168:171],v[220:223],v[72:75]\n"
"s_add_u32 s22,s22,262144\n"
"ds_read_b128 v[220:223],v113 offset:23552\n"
"v_mfma_f32_16x16x32_bf16 v[72:75],v[160:163],v[216:219],v[72:75]\n"
"ds_read_b128 v[216:219],v113 offset:22528\n"
"v_mfma_f32_16x16x32_bf16 v[72:75],v[8:11],v[212:215],v[72:75]\n"
"ds_read_b128 v[212:215],v113 offset:21504\n"
"v_mfma_f32_16x16x32_bf16 v[72:75],v[0:3],v[208:211],v[72:75]\n"
"ds_read_b128 v[208:211],v113 offset:20480\n"
"s_waitcnt lgkmcnt(12)\n"
"v_mfma_f32_16x16x32_bf16 v[72:75],v[176:179],v[224:227],v[72:75]\n"
"buffer_load_dwordx4 v112,s[24:27],s8 offen offset:3072 sc1 lds\n"
"v_mfma_f32_16x16x32_bf16 v[72:75],v[184:187],v[228:231],v[72:75]\n"
"v_xor_b32 v119,s80,v122\n"
"v_xor_b32 v120,s81,v123\n"
"v_mfma_f32_16x16x32_bf16 v[72:75],a[48:51],v[232:235],v[72:75]\n"
"v_or_b32 v119,v119,v120\n"
"v_cmp_ne_u32 s[28:29],0,v119\n"
"v_mfma_f32_16x16x32_bf16 v[72:75],a[56:59],v[236:239],v[72:75]\n"
"s_and_b64 s[14:15],s[14:15],s[28:29]\n"
"v_mfma_f32_16x16x32_bf16 v[76:79],a[60:63],v[236:239],v[76:79]\n"
"ds_read_b128 v[236:239],v113 offset:27648\n"
"v_mfma_f32_16x16x32_bf16 v[76:79],a[52:55],v[232:235],v[76:79]\n"
"ds_read_b128 v[232:235],v113 offset:26624\n"
"v_mfma_f32_16x16x32_bf16 v[76:79],v[188:191],v[228:231],v[76:79]\n"
"ds_read_b128 v[228:231],v113 offset:25600\n"
"v_mfma_f32_16x16x32_bf16 v[76:79],v[180:183],v[224:227],v[76:79]\n"
"s_add_u32 m0,m0,4096\n"
"ds_read_b128 v[224:227],v113 offset:24576\n"
"s_waitcnt lgkmcnt(12)\n"
"v_mfma_f32_16x16x32_bf16 v[76:79],v[52:55],v[240:243],v[76:79]\n"
"buffer_load_dwordx4 v117,s[24:27],s8 offen offset:0 sc1 lds\n"
"v_mfma_f32_16x16x32_bf16 v[76:79],v[60:63],v[244:247],v[76:79]\n"
"s_and_b32 s83,s15,0xff000000\n"
"s_cbranch_scc1 .Lfk_es_lktwom\n"
".Lfk_er_lktwom:\n"
"v_mfma_f32_16x16x32_bf16 v[76:79],v[20:23],v[248:251],v[76:79]\n"
"v_xor_b32 v119,s80,v104\n"
"v_xor_b32 v120,s81,v105\n"
"v_or_b32 v119,v119,v120\n"
"v_mfma_f32_16x16x32_bf16 v[76:79],v[28:31],v[252:255],v[76:79]\n"
"buffer_load_dwordx4 v117,s[24:27],s8 offen offset:1024 sc1 lds\n"
"v_cmp_ne_u32 s[28:29],0,v119\n"
"v_mfma_f32_16x16x32_bf16 v[72:75],v[24:27],v[252:255],v[72:75]\n"
"s_cmp_eq_u32 s52,0\n"
"ds_read_b128 v[252:255],v113 offset:31744\n"
"s_cselect_b64 s[14:15],-1,0\n"
"v_mfma_f32_16x16x32_bf16 v[72:75],v[16:19],v[248:251],v[72:75]\n"
"s_mov_b32 s11,s9\n"
"ds_read_b128 v[248:251],v113 offset:30720\n"
"s_waitcnt vmcnt(18)\n"
"v_not_b32 v122,s80\n"
"v_mfma_f32_16x16x32_bf16 v[72:75],v[56:59],v[244:247],v[72:75]\n"
"s_and_b64 s[14:15],s[14:15],s[28:29]\n"
"s_cselect_b32 s91,s11,2146435072\n"
"ds_read_b128 v[244:247],v113 offset:29696\n"
"buffer_load_dwordx2 v[122:123],v115,s[24:27],s91 offen sc1\n"
"v_mfma_f32_16x16x32_bf16 v[72:75],v[48:51],v[240:243],v[72:75]\n"
"ds_read_b128 v[240:243],v113 offset:28672\n"
"s_waitcnt lgkmcnt(12)\n"
"v_mfma_f32_16x16x32_bf16 v[88:91],v[128:131],v[192:195],v[88:91]\n"
"ds_write_b32 v101,v100\n"
"v_mfma_f32_16x16x32_bf16 v[88:91],v[136:139],v[196:199],v[88:91]\n"
"ds_add_u32 v114,v116\n"
"v_mfma_f32_16x16x32_bf16 v[88:91],v[144:147],v[200:203],v[88:91]\n"
"buffer_load_dwordx4 v117,s[24:27],s8 offen offset:2048 sc1 lds\n"
"v_mfma_f32_16x16x32_bf16 v[88:91],v[152:155],v[204:207],v[88:91]\n"
"s_add_u32 s83,s20,32768\n"
"s_and_b32 s83,s83,0x1ffff\n"
"v_add_u32 v113,s83,v112\n"
"v_mfma_f32_16x16x32_bf16 v[92:95],v[156:159],v[204:207],v[92:95]\n"
"v_cvt_pk_bf16_f32 v72,v72,v73\n"
"v_cvt_pk_bf16_f32 v73,v74,v75\n"
"v_cvt_pk_bf16_f32 v74,v76,v77\n"
"v_mfma_f32_16x16x32_bf16 v[92:95],v[148:151],v[200:203],v[92:95]\n"
"v_cvt_pk_bf16_f32 v75,v78,v79\n"
"v_max_u32 v72,1,v72\n"
"v_max_u32 v73,1,v73\n"
"v_mfma_f32_16x16x32_bf16 v[92:95],v[140:143],v[196:199],v[92:95]\n"
"v_max_u32 v74,1,v74\n"
"v_max_u32 v75,1,v75\n"
"v_mfma_f32_16x16x32_bf16 v[92:95],v[132:135],v[192:195],v[92:95]\n"
"buffer_store_dwordx4 v[72:75],v118,s[24:27],s22 offen sc1\n"
"s_waitcnt lgkmcnt(10)\n"
"v_mfma_f32_16x16x32_bf16 v[92:95],v[4:7],v[208:211],v[92:95]\n"
"buffer_load_dwordx4 v117,s[24:27],s8 offen offset:3072 sc1 lds\n"
"v_mfma_f32_16x16x32_bf16 v[92:95],v[12:15],v[212:215],v[92:95]\n"
"s_add_u32 s22,s22,262144\n"
"ds_read_b32 v125,v114\n"
"v_mfma_f32_16x16x32_bf16 v[92:95],v[164:167],v[216:219],v[92:95]\n"
"v_mfma_f32_16x16x32_bf16 v[92:95],v[172:175],v[220:223],v[92:95]\n"
"v_mfma_f32_16x16x32_bf16 v[88:91],v[168:171],v[220:223],v[88:91]\n"
"s_lshl_b64 exec,1,s89\n"
"buffer_atomic_add v100,off,s[24:27],s10 sc0\n"
"s_mov_b64 exec,-1\n"
"v_mfma_f32_16x16x32_bf16 v[88:91],v[160:163],v[216:219],v[88:91]\n"
"s_waitcnt lgkmcnt(0)\n"
"v_cmp_gt_u32 vcc,s16,v125\n"
"v_mfma_f32_16x16x32_bf16 v[88:91],v[8:11],v[212:215],v[88:91]\n"
"s_add_u32 s8,s8,1048576\n"
"v_mfma_f32_16x16x32_bf16 v[88:91],v[0:3],v[208:211],v[88:91]\n"
"s_cbranch_vccnz .Lfk_es_mtwo\n"
".Lfk_er_mtwo:\n"
".Lfk_l4:\n"
"v_mfma_f32_16x16x32_bf16 v[88:91],v[176:179],v[224:227],v[88:91]\n"
"ds_read_b128 v[192:195],v113 offset:0\n"
"v_mfma_f32_16x16x32_bf16 v[88:91],v[184:187],v[228:231],v[88:91]\n"
"ds_read_b128 v[196:199],v113 offset:1024\n"
"v_mfma_f32_16x16x32_bf16 v[88:91],a[48:51],v[232:235],v[88:91]\n"
"ds_read_b128 v[200:203],v113 offset:2048\n"
"s_add_u32 s89,s89,1\n"
"v_mfma_f32_16x16x32_bf16 v[88:91],a[56:59],v[236:239],v[88:91]\n"
"ds_read_b128 v[204:207],v113 offset:3072\n"
"v_mfma_f32_16x16x32_bf16 v[92:95],a[60:63],v[236:239],v[92:95]\n"
"ds_read_b128 v[208:211],v113 offset:4096\n"
"v_mfma_f32_16x16x32_bf16 v[92:95],a[52:55],v[232:235],v[92:95]\n"
"ds_read_b128 v[212:215],v113 offset:5120\n"
"s_add_u32 s16,s16,4\n"
"v_mfma_f32_16x16x32_bf16 v[92:95],v[188:191],v[228:231],v[92:95]\n"
"ds_read_b128 v[216:219],v113 offset:6144\n"
"v_mfma_f32_16x16x32_bf16 v[92:95],v[180:183],v[224:227],v[92:95]\n"
"ds_read_b128 v[220:223],v113 offset:7168\n"
"v_mfma_f32_16x16x32_bf16 v[92:95],v[52:55],v[240:243],v[92:95]\n"
"ds_read_b128 v[236:239],v113 offset:11264\n"
"v_mfma_f32_16x16x32_bf16 v[92:95],v[60:63],v[244:247],v[92:95]\n"
"ds_read_b128 v[232:235],v113 offset:10240\n"
"v_mfma_f32_16x16x32_bf16 v[92:95],v[20:23],v[248:251],v[92:95]\n"
"ds_read_b128 v[228:231],v113 offset:9216\n"
"v_mfma_f32_16x16x32_bf16 v[92:95],v[28:31],v[252:255],v[92:95]\n"
"ds_read_b128 v[224:227],v113 offset:8192\n"
"v_mfma_f32_16x16x32_bf16 v[88:91],v[24:27],v[252:255],v[88:91]\n"
"ds_read_b128 v[252:255],v113 offset:15360\n"
"v_mfma_f32_16x16x32_bf16 v[88:91],v[16:19],v[248:251],v[88:91]\n"
"ds_read_b128 v[248:251],v113 offset:14336\n"
"v_mfma_f32_16x16x32_bf16 v[88:91],v[56:59],v[244:247],v[88:91]\n"
"ds_read_b128 v[244:247],v113 offset:13312\n"
"v_mfma_f32_16x16x32_bf16 v[88:91],v[48:51],v[240:243],v[88:91]\n"
"ds_read_b128 v[240:243],v113 offset:12288\n"
"s_waitcnt lgkmcnt(12)\n"
"v_mfma_f32_16x16x32_bf16 v[80:83],a[224:227],v[192:195],0\n"
"s_add_u32 m0,s30,s20\n"
"ds_read_b32 v103,v102\n"
"v_mfma_f32_16x16x32_bf16 v[80:83],a[232:235],v[196:199],v[80:83]\n"
"buffer_load_dwordx4 v112,s[24:27],s8 offen offset:0 sc1 lds\n"
"v_mfma_f32_16x16x32_bf16 v[80:83],a[240:243],v[200:203],v[80:83]\n"
"v_add_u32 v102,4,v102\n"
"v_and_b32 v102,0x200ff,v102\n"
"v_mfma_f32_16x16x32_bf16 v[80:83],a[248:251],v[204:207],v[80:83]\n"
"v_cvt_pk_bf16_f32 v88,v88,v89\n"
"v_cvt_pk_bf16_f32 v89,v90,v91\n"
"v_mfma_f32_16x16x32_bf16 v[84:87],a[252:255],v[204:207],0\n"
"ds_read_b128 v[204:207],v113 offset:19456\n"
"s_add_u32 s20,s20,32768\n"
"v_mfma_f32_16x16x32_bf16 v[84:87],a[244:247],v[200:203],v[84:87]\n"
"ds_read_b128 v[200:203],v113 offset:18432\n"
"s_and_b32 s20,s20,0x1ffff\n"
"v_mfma_f32_16x16x32_bf16 v[84:87],a[236:239],v[196:199],v[84:87]\n"
"ds_read_b128 v[196:199],v113 offset:17408\n"
"v_cvt_pk_bf16_f32 v90,v92,v93\n"
"v_mfma_f32_16x16x32_bf16 v[84:87],a[228:231],v[192:195],v[84:87]\n"
"ds_read_b128 v[192:195],v113 offset:16384\n"
"s_waitcnt lgkmcnt(13)\n"
"v_mfma_f32_16x16x32_bf16 v[84:87],a[4:7],v[208:211],v[84:87]\n"
"buffer_load_dwordx4 v112,s[24:27],s8 offen offset:1024 sc1 lds\n"
"v_mfma_f32_16x16x32_bf16 v[84:87],a[12:15],v[212:215],v[84:87]\n"
"v_cvt_pk_bf16_f32 v91,v94,v95\n"
"v_max_u32 v88,1,v88\n"
"v_mfma_f32_16x16x32_bf16 v[84:87],a[20:23],v[216:219],v[84:87]\n"
"v_max_u32 v89,1,v89\n"
"v_max_u32 v90,1,v90\n"
"v_mfma_f32_16x16x32_bf16 v[84:87],a[28:31],v[220:223],v[84:87]\n"
"buffer_load_dwordx4 v112,s[24:27],s8 offen offset:2048 sc1 lds\n"
"v_mfma_f32_16x16x32_bf16 v[80:83],a[24:27],v[220:223],v[80:83]\n"
"ds_read_b128 v[220:223],v113 offset:23552\n"
"v_max_u32 v91,1,v91\n"
"v_mfma_f32_16x16x32_bf16 v[80:83],a[16:19],v[216:219],v[80:83]\n"
"ds_read_b128 v[216:219],v113 offset:22528\n"
"buffer_store_dwordx4 v[88:91],v118,s[24:27],s53 offen sc1\n"
"v_mfma_f32_16x16x32_bf16 v[80:83],a[8:11],v[212:215],v[80:83]\n"
"ds_read_b128 v[212:215],v113 offset:21504\n"
"v_mfma_f32_16x16x32_bf16 v[80:83],a[0:3],v[208:211],v[80:83]\n"
"ds_read_b128 v[208:211],v113 offset:20480\n"
"s_waitcnt lgkmcnt(13)\n"
"v_mfma_f32_16x16x32_bf16 v[80:83],v[32:35],v[224:227],v[80:83]\n"
"v_mfma_f32_16x16x32_bf16 v[80:83],v[40:43],v[228:231],v[80:83]\n"
"buffer_load_dwordx4 v112,s[24:27],s8 offen offset:3072 sc1 lds\n"
"v_mfma_f32_16x16x32_bf16 v[80:83],a[32:35],v[232:235],v[80:83]\n"
"s_waitcnt lgkmcnt(8)\n"
"v_readfirstlane_b32 s22,v103\n"
"v_mfma_f32_16x16x32_bf16 v[80:83],a[40:43],v[236:239],v[80:83]\n"
"v_mfma_f32_16x16x32_bf16 v[84:87],a[44:47],v[236:239],v[84:87]\n"
"ds_read_b128 v[236:239],v113 offset:27648\n"
"s_and_b32 s21,s22,0x38\n"
"v_mfma_f32_16x16x32_bf16 v[84:87],a[36:39],v[232:235],v[84:87]\n"
"ds_read_b128 v[232:235],v113 offset:26624\n"
"v_mfma_f32_16x16x32_bf16 v[84:87],v[44:47],v[228:231],v[84:87]\n"
"ds_read_b128 v[228:231],v113 offset:25600\n"
"v_mfma_f32_16x16x32_bf16 v[84:87],v[36:39],v[224:227],v[84:87]\n"
"ds_read_b128 v[224:227],v113 offset:24576\n"
"s_add_u32 m0,m0,4096\n"
"v_mfma_f32_16x16x32_bf16 v[84:87],a[68:71],v[240:243],v[84:87]\n"
"buffer_load_dwordx4 v117,s[24:27],s8 offen offset:0 sc1 lds\n"
"v_mfma_f32_16x16x32_bf16 v[84:87],a[76:79],v[244:247],v[84:87]\n"
"s_cmp_lg_u64 s[14:15],0\n"
"s_cbranch_scc1 .Lfk_es_lkloopm\n"
".Lfk_er_lkloopm:\n"
"v_mfma_f32_16x16x32_bf16 v[84:87],a[84:87],v[248:251],v[84:87]\n"
"v_mfma_f32_16x16x32_bf16 v[84:87],a[92:95],v[252:255],v[84:87]\n"
"buffer_load_dwordx4 v117,s[24:27],s8 offen offset:1024 sc1 lds\n"
"v_mfma_f32_16x16x32_bf16 v[80:83],a[88:91],v[252:255],v[80:83]\n"
"ds_read_b128 v[252:255],v113 offset:31744\n"
"s_mov_b32 s53,s56\n"
"v_mfma_f32_16x16x32_bf16 v[80:83],a[80:83],v[248:251],v[80:83]\n"
"ds_read_b128 v[248:251],v113 offset:30720\n"
"s_mov_b32 s56,s58\n"
"v_mfma_f32_16x16x32_bf16 v[80:83],a[72:75],v[244:247],v[80:83]\n"
"ds_read_b128 v[244:247],v113 offset:29696\n"
"s_mov_b32 s58,s59\n"
"v_mfma_f32_16x16x32_bf16 v[80:83],a[156:159],v[240:243],v[80:83]\n"
"ds_read_b128 v[240:243],v113 offset:28672\n"
"s_waitcnt lgkmcnt(12)\n"
"v_mfma_f32_16x16x32_bf16 v[88:91],v[128:131],v[192:195],v[80:83]\n"
"s_waitcnt vmcnt(17)\n"
"ds_write_b32 v101,v100\n"
"v_mfma_f32_16x16x32_bf16 v[88:91],v[136:139],v[196:199],v[88:91]\n"
"ds_add_u32 v114,v116\n"
"v_mfma_f32_16x16x32_bf16 v[88:91],v[144:147],v[200:203],v[88:91]\n"
"buffer_load_dwordx4 v117,s[24:27],s8 offen offset:2048 sc1 lds\n"
"v_mfma_f32_16x16x32_bf16 v[88:91],v[152:155],v[204:207],v[88:91]\n"
"s_add_u32 s83,s20,32768\n"
"s_and_b32 s83,s83,0x1ffff\n"
"v_add_u32 v113,s83,v112\n"
"v_mfma_f32_16x16x32_bf16 v[92:95],v[156:159],v[204:207],v[84:87]\n"
"s_cmp_lt_u32 s22,64\n"
"s_cselect_b32 s17,s16,-1\n"
"v_mfma_f32_16x16x32_bf16 v[92:95],v[148:151],v[200:203],v[92:95]\n"
"v_mfma_f32_16x16x32_bf16 v[92:95],v[140:143],v[196:199],v[92:95]\n"
"v_mfma_f32_16x16x32_bf16 v[92:95],v[132:135],v[192:195],v[92:95]\n"
"s_waitcnt lgkmcnt(10)\n"
"v_mfma_f32_16x16x32_bf16 v[92:95],v[4:7],v[208:211],v[92:95]\n"
"buffer_load_dwordx4 v117,s[24:27],s8 offen offset:3072 sc1 lds\n"
"v_mfma_f32_16x16x32_bf16 v[92:95],v[12:15],v[212:215],v[92:95]\n"
"s_lshr_b32 s59,s8,2\n"
"ds_read_b32 v125,v114\n"
"s_lshl_b32 s8,s22,17\n"
"v_mfma_f32_16x16x32_bf16 v[92:95],v[164:167],v[216:219],v[92:95]\n"
"s_add_u32 s8,s8,s31\n"
"v_mfma_f32_16x16x32_bf16 v[92:95],v[172:175],v[220:223],v[92:95]\n"
"v_mfma_f32_16x16x32_bf16 v[88:91],v[168:171],v[220:223],v[88:91]\n"
"s_lshl_b64 exec,1,s89\n"
"buffer_atomic_add v100,off,s[24:27],s10 sc0\n"
"s_mov_b64 exec,-1\n"
"v_mfma_f32_16x16x32_bf16 v[88:91],v[160:163],v[216:219],v[88:91]\n"
"s_waitcnt lgkmcnt(0)\n"
"v_cmp_gt_u32 vcc,s17,v125\n"
"v_mfma_f32_16x16x32_bf16 v[88:91],v[8:11],v[212:215],v[88:91]\n"
"v_mfma_f32_16x16x32_bf16 v[88:91],v[0:3],v[208:211],v[88:91]\n"
"s_cbranch_vccz .Lfk_l4\n"
"s_call_b64 s[94:95],.Lfk_meet4\n"
"s_cmp_lt_u32 s22,64\n"
"s_cbranch_scc1 .Lfk_l4\n"
"ds_read_b128 v[192:195],v113 offset:0\n"
"v_mfma_f32_16x16x32_bf16 v[88:91],v[176:179],v[224:227],v[88:91]\n"
"s_getpc_b64 s[96:97]\n"
".Lfk_rt2:\n"
"s_add_u32 s96,s96,.Lfk_tail-.Lfk_rt2\n"
"s_addc_u32 s97,s97,0\n"
"s_mov_b32 s98,.Lfk_cold-.Lfk_tail\n"
"s_mov_b32 s99,0x20000\n"
"s_lshr_b32 s82,s100,3\n"
"s_lshl2_add_u32 s82,s82,s52\n"
"v_lshrrev_b32 v121,2,v112\n"
"s_lshl_b32 s82,s82,8\n"
"v_mfma_f32_16x16x32_bf16 v[88:91],v[184:187],v[228:231],v[88:91]\n"
"buffer_load_dword v120,v121,s[96:99],s82 offen\n"
"ds_read_b128 v[196:199],v113 offset:1024\n"
"v_mfma_f32_16x16x32_bf16 v[88:91],a[48:51],v[232:235],v[88:91]\n"
"s_mov_b32 s77,0\n"
"ds_read_b128 v[200:203],v113 offset:2048\n"
"s_and_b32 s91,s52,1\n"
"v_mfma_f32_16x16x32_bf16 v[88:91],a[56:59],v[236:239],v[88:91]\n"
"ds_read_b128 v[204:207],v113 offset:3072\n"
"s_lshl_b32 s91,s91,7\n"
"v_mfma_f32_16x16x32_bf16 v[92:95],a[60:63],v[236:239],v[92:95]\n"
"ds_read_b128 v[208:211],v113 offset:4096\n"
"v_xor_b32 v96,s91,v112\n"
"v_mfma_f32_16x16x32_bf16 v[92:95],a[52:55],v[232:235],v[92:95]\n"
"ds_read_b128 v[212:215],v113 offset:5120\n"
"s_add_u32 s16,s16,4\n"
"v_mfma_f32_16x16x32_bf16 v[92:95],v[188:191],v[228:231],v[92:95]\n"
"ds_read_b128 v[216:219],v113 offset:6144\n"
"v_xor_b32 v97,32,v96\n"
"v_mfma_f32_16x16x32_bf16 v[92:95],v[180:183],v[224:227],v[92:95]\n"
"ds_read_b128 v[220:223],v113 offset:7168\n"
"v_xor_b32 v98,64,v96\n"
"v_mfma_f32_16x16x32_bf16 v[92:95],v[52:55],v[240:243],v[92:95]\n"
"ds_read_b128 v[236:239],v113 offset:11264\n"
"v_xor_b32 v99,96,v96\n"
"v_mfma_f32_16x16x32_bf16 v[92:95],v[60:63],v[244:247],v[92:95]\n"
"ds_read_b128 v[232:235],v113 offset:10240\n"
"s_mov_b32 s38,4194304\n"
"v_mfma_f32_16x16x32_bf16 v[92:95],v[20:23],v[248:251],v[92:95]\n"
"ds_read_b128 v[228:231],v113 offset:9216\n"
"s_mov_b32 s39,0x20000\n"
"v_mfma_f32_16x16x32_bf16 v[92:95],v[28:31],v[252:255],v[92:95]\n"
"ds_read_b128 v[224:227],v113 offset:8192\n"
"s_mov_b32 s46,2048\n"
"v_mfma_f32_16x16x32_bf16 v[88:91],v[24:27],v[252:255],v[88:91]\n"
"ds_read_b128 v[252:255],v113 offset:15360\n"
"s_mov_b32 s47,0x20000\n"
"v_mfma_f32_16x16x32_bf16 v[88:91],v[16:19],v[248:251],v[88:91]\n"
"ds_read_b128 v[248:251],v113 offset:14336\n"
"s_mov_b32 s50,4194304\n"
"v_mfma_f32_16x16x32_bf16 v[88:91],v[56:59],v[244:247],v[88:91]\n"
"ds_read_b128 v[244:247],v113 offset:13312\n"
"s_mov_b32 s51,0x20000\n"
"v_mfma_f32_16x16x32_bf16 v[88:91],v[48:51],v[240:243],v[88:91]\n"
"ds_read_b128 v[240:243],v113 offset:12288\n"
"s_waitcnt lgkmcnt(12)\n"
"v_mfma_f32_16x16x32_bf16 v[80:83],a[224:227],v[192:195],0\n"
"s_and_b32 s60,s100,7\n"
"s_lshl_b32 s60,s60,5\n"
"v_mfma_f32_16x16x32_bf16 v[80:83],a[232:235],v[196:199],v[80:83]\n"
"s_lshr_b32 s73,s100,3\n"
"s_add_u32 s73,s73,s60\n"
"v_mfma_f32_16x16x32_bf16 v[80:83],a[240:243],v[200:203],v[80:83]\n"
"s_bfe_u32 s60,s100,0x00003\n"
"s_xor_b32 s60,s60,s100\n"
"v_mfma_f32_16x16x32_bf16 v[80:83],a[248:251],v[204:207],v[80:83]\n"
"v_cvt_pk_bf16_f32 v88,v88,v89\n"
"v_cvt_pk_bf16_f32 v89,v90,v91\n"
"v_mfma_f32_16x16x32_bf16 v[84:87],a[252:255],v[204:207],0\n"
"ds_read_b128 v[204:207],v113 offset:19456\n"
"s_add_u32 s20,s20,32768\n"
"v_mfma_f32_16x16x32_bf16 v[84:87],a[244:247],v[200:203],v[84:87]\n"
"ds_read_b128 v[200:203],v113 offset:18432\n"
"s_and_b32 s20,s20,0x1ffff\n"
"v_mfma_f32_16x16x32_bf16 v[84:87],a[236:239],v[196:199],v[84:87]\n"
"ds_read_b128 v[196:199],v113 offset:17408\n"
"v_cvt_pk_bf16_f32 v90,v92,v93\n"
"v_mfma_f32_16x16x32_bf16 v[84:87],a[228:231],v[192:195],v[84:87]\n"
"ds_read_b128 v[192:195],v113 offset:16384\n"
"s_waitcnt lgkmcnt(12)\n"
"v_mfma_f32_16x16x32_bf16 v[84:87],a[4:7],v[208:211],v[84:87]\n"
"v_cvt_pk_bf16_f32 v91,v94,v95\n"
"v_max_u32 v88,1,v88\n"
"v_mfma_f32_16x16x32_bf16 v[84:87],a[12:15],v[212:215],v[84:87]\n"
"v_max_u32 v89,1,v89\n"
"v_max_u32 v90,1,v90\n"
"v_max_u32 v91,1,v91\n"
"v_mfma_f32_16x16x32_bf16 v[84:87],a[20:23],v[216:219],v[84:87]\n"
"buffer_store_dwordx4 v[88:91],v118,s[24:27],s53 offen sc1\n"
"v_mfma_f32_16x16x32_bf16 v[84:87],a[28:31],v[220:223],v[84:87]\n"
"v_mfma_f32_16x16x32_bf16 v[80:83],a[24:27],v[220:223],v[80:83]\n"
"ds_read_b128 v[220:223],v113 offset:23552\n"
"v_mfma_f32_16x16x32_bf16 v[80:83],a[16:19],v[216:219],v[80:83]\n"
"ds_read_b128 v[216:219],v113 offset:22528\n"
"v_mfma_f32_16x16x32_bf16 v[80:83],a[8:11],v[212:215],v[80:83]\n"
"ds_read_b128 v[212:215],v113 offset:21504\n"
"v_mfma_f32_16x16x32_bf16 v[80:83],a[0:3],v[208:211],v[80:83]\n"
"ds_read_b128 v[208:211],v113 offset:20480\n"
"s_waitcnt lgkmcnt(12)\n"
"v_mfma_f32_16x16x32_bf16 v[80:83],v[32:35],v[224:227],v[80:83]\n"
"v_mfma_f32_16x16x32_bf16 v[80:83],v[40:43],v[228:231],v[80:83]\n"
"v_mfma_f32_16x16x32_bf16 v[80:83],a[32:35],v[232:235],v[80:83]\n"
"v_mfma_f32_16x16x32_bf16 v[80:83],a[40:43],v[236:239],v[80:83]\n"
"v_mfma_f32_16x16x32_bf16 v[84:87],a[44:47],v[236:239],v[84:87]\n"
"ds_read_b128 v[236:239],v113 offset:27648\n"
"v_mfma_f32_16x16x32_bf16 v[84:87],a[36:39],v[232:235],v[84:87]\n"
"ds_read_b128 v[232:235],v113 offset:26624\n"
"v_mfma_f32_16x16x32_bf16 v[84:87],v[44:47],v[228:231],v[84:87]\n"
"ds_read_b128 v[228:231],v113 offset:25600\n"
"v_mfma_f32_16x16x32_bf16 v[84:87],v[36:39],v[224:227],v[84:87]\n"
"ds_read_b128 v[224:227],v113 offset:24576\n"
"s_waitcnt lgkmcnt(12)\n"
"v_mfma_f32_16x16x32_bf16 v[84:87],a[68:71],v[240:243],v[84:87]\n"
"s_lshl2_add_u32 s60,s60,s52\n"
"s_lshl_b32 s61,s60,11\n"
"s_lshr_b32 s62,s60,4\n"
"v_mfma_f32_16x16x32_bf16 v[84:87],a[76:79],v[244:247],v[84:87]\n"
"s_lshl_b32 s62,s62,15\n"
"s_and_b32 s91,s60,15\n"
"s_lshl_b32 s91,s91,6\n"
"v_mfma_f32_16x16x32_bf16 v[84:87],a[84:87],v[248:251],v[84:87]\n"
"s_add_u32 s62,s62,71303168\n"
"v_lshlrev_b32 v102,2,v112\n"
"v_and_b32 v102,0x0,v102\n"
"v_xor_b32 v102,s91,v102\n"
"v_mfma_f32_16x16x32_bf16 v[84:87],a[92:95],v[252:255],v[84:87]\n"
"s_cmp_eq_u32 s42,0\n"
"s_cselect_b32 s72,0x3ff80,s23\n"
"s_add_u32 s91,s61,2097152\n"
"v_mfma_f32_16x16x32_bf16 v[80:83],a[88:91],v[252:255],v[80:83]\n"
"s_mov_b32 s53,s56\n"
"ds_read_b128 v[252:255],v113 offset:31744\n"
"v_lshrrev_b32 v126,6,v112\n"
"v_mfma_f32_16x16x32_bf16 v[80:83],a[80:83],v[248:251],v[80:83]\n"
"s_mov_b32 s56,s58\n"
"ds_read_b128 v[248:251],v113 offset:30720\n"
"v_and_or_b32 v103,v112,48,v102\n"
"v_mfma_f32_16x16x32_bf16 v[80:83],a[72:75],v[244:247],v[80:83]\n"
"s_mov_b32 s58,s59\n"
"ds_read_b128 v[244:247],v113 offset:29696\n"
"v_lshl_add_u32 v126,v126,10,v103\n"
"v_mfma_f32_16x16x32_bf16 v[80:83],a[156:159],v[240:243],v[80:83]\n"
"ds_read_b128 v[240:243],v113 offset:28672\n"
"s_waitcnt lgkmcnt(12)\n"
"v_mfma_f32_16x16x32_bf16 v[88:91],v[128:131],v[192:195],v[80:83]\n"
"s_waitcnt vmcnt(13)\n"
"ds_add_u32 v114,v116\n"
"v_mfma_f32_16x16x32_bf16 v[88:91],v[136:139],v[196:199],v[88:91]\n"
"v_add_u32 v117,16384,v126\n"
"v_mfma_f32_16x16x32_bf16 v[88:91],v[144:147],v[200:203],v[88:91]\n"
"v_mfma_f32_16x16x32_bf16 v[88:91],v[152:155],v[204:207],v[88:91]\n"
"s_add_u32 s83,s20,32768\n"
"s_and_b32 s83,s83,0x1ffff\n"
"v_add_u32 v113,s83,v112\n"
"v_mfma_f32_16x16x32_bf16 v[92:95],v[156:159],v[204:207],v[84:87]\n"
"v_mfma_f32_16x16x32_bf16 v[92:95],v[148:151],v[200:203],v[92:95]\n"
"v_mfma_f32_16x16x32_bf16 v[92:95],v[140:143],v[196:199],v[92:95]\n"
"v_mfma_f32_16x16x32_bf16 v[92:95],v[132:135],v[192:195],v[92:95]\n"
"s_waitcnt lgkmcnt(9)\n"
"v_mfma_f32_16x16x32_bf16 v[92:95],v[4:7],v[208:211],v[92:95]\n"
"v_mfma_f32_16x16x32_bf16 v[92:95],v[12:15],v[212:215],v[92:95]\n"
"ds_read_b32 v125,v114\n"
"v_mfma_f32_16x16x32_bf16 v[92:95],v[164:167],v[216:219],v[92:95]\n"
"v_mfma_f32_16x16x32_bf16 v[92:95],v[172:175],v[220:223],v[92:95]\n"
"v_mfma_f32_16x16x32_bf16 v[88:91],v[168:171],v[220:223],v[88:91]\n"
"v_mfma_f32_16x16x32_bf16 v[88:91],v[160:163],v[216:219],v[88:91]\n"
"s_waitcnt lgkmcnt(0)\n"
"v_cmp_gt_u32 vcc,s16,v125\n"
"v_mfma_f32_16x16x32_bf16 v[88:91],v[8:11],v[212:215],v[88:91]\n"
"v_mfma_f32_16x16x32_bf16 v[88:91],v[0:3],v[208:211],v[88:91]\n"
"s_cbranch_vccnz .Lfk_es_me3\n"
".Lfk_er_me3:\n"
"ds_read_b128 v[192:195],v113 offset:0\n"
"v_mfma_f32_16x16x32_bf16 v[88:91],v[176:179],v[224:227],v[88:91]\n"
"buffer_load_dwordx4 a[96:99],v112,s[36:39],s61 offen offset:0\n"
"v_mfma_f32_16x16x32_bf16 v[88:91],v[184:187],v[228:231],v[88:91]\n"
"ds_read_b128 v[196:199],v113 offset:1024\n"
"v_lshrrev_b32 v101,4,v112\n"
"v_mfma_f32_16x16x32_bf16 v[88:91],a[48:51],v[232:235],v[88:91]\n"
"ds_read_b128 v[200:203],v113 offset:2048\n"
"v_and_b32 v102,15,v101\n"
"v_mfma_f32_16x16x32_bf16 v[88:91],a[56:59],v[236:239],v[88:91]\n"
"ds_read_b128 v[204:207],v113 offset:3072\n"
"buffer_load_dwordx4 a[100:103],v112,s[36:39],s61 offen offset:1024\n"
"v_mfma_f32_16x16x32_bf16 v[92:95],a[60:63],v[236:239],v[92:95]\n"
"ds_read_b128 v[208:211],v113 offset:4096\n"
"v_lshrrev_b32 v101,4,v101\n"
"v_mfma_f32_16x16x32_bf16 v[92:95],a[52:55],v[232:235],v[92:95]\n"
"ds_read_b128 v[212:215],v113 offset:5120\n"
"s_add_u32 s16,s16,4\n"
"v_mfma_f32_16x16x32_bf16 v[92:95],v[188:191],v[228:231],v[92:95]\n"
"ds_read_b128 v[216:219],v113 offset:6144\n"
"buffer_load_dwordx4 a[104:107],v112,s[36:39],s91 offen offset:0\n"
"v_mfma_f32_16x16x32_bf16 v[92:95],v[180:183],v[224:227],v[92:95]\n"
"ds_read_b128 v[220:223],v113 offset:7168\n"
"v_lshlrev_b32 v101,2,v101\n"
"v_mfma_f32_16x16x32_bf16 v[92:95],v[52:55],v[240:243],v[92:95]\n"
"ds_read_b128 v[236:239],v113 offset:11264\n"
"v_sub_u32 v101,v102,v101\n"
"v_mfma_f32_16x16x32_bf16 v[92:95],v[60:63],v[244:247],v[92:95]\n"
"ds_read_b128 v[232:235],v113 offset:10240\n"
"buffer_load_dwordx4 a[108:111],v112,s[36:39],s91 offen offset:1024\n"
"v_mfma_f32_16x16x32_bf16 v[92:95],v[20:23],v[248:251],v[92:95]\n"
"ds_read_b128 v[228:231],v113 offset:9216\n"
"v_mov_b32 v64,0\n"
"v_mfma_f32_16x16x32_bf16 v[92:95],v[28:31],v[252:255],v[92:95]\n"
"ds_read_b128 v[224:227],v113 offset:8192\n"
"v_mov_b32 v65,0\n"
"v_mfma_f32_16x16x32_bf16 v[88:91],v[24:27],v[252:255],v[88:91]\n"
"ds_read_b128 v[252:255],v113 offset:15360\n"
"buffer_load_dwordx4 v[72:75],v112,s[44:47],0 offen offset:0\n"
"v_mfma_f32_16x16x32_bf16 v[88:91],v[16:19],v[248:251],v[88:91]\n"
"ds_read_b128 v[248:251],v113 offset:14336\n"
"v_mov_b32 v68,0\n"
"v_mfma_f32_16x16x32_bf16 v[88:91],v[56:59],v[244:247],v[88:91]\n"
"ds_read_b128 v[244:247],v113 offset:13312\n"
"v_mov_b32 v69,0\n"
"v_mfma_f32_16x16x32_bf16 v[88:91],v[48:51],v[240:243],v[88:91]\n"
"ds_read_b128 v[240:243],v113 offset:12288\n"
"buffer_load_dwordx4 v[76:79],v112,s[44:47],0 offen offset:1024\n"
"s_waitcnt lgkmcnt(12)\n"
"v_mfma_f32_16x16x32_bf16 v[80:83],a[224:227],v[192:195],0\n"
"v_mov_b32 v102,0x3f80\n"
"v_mov_b32 v103,0x3f800000\n"
"v_cmp_eq_u32 vcc,0,v101\n"
"v_mfma_f32_16x16x32_bf16 v[80:83],a[232:235],v[196:199],v[80:83]\n"
"s_nop 0\n"
"v_cndmask_b32 v66,0,v102,vcc\n"
"v_cmp_eq_u32 vcc,1,v101\n"
"v_mfma_f32_16x16x32_bf16 v[80:83],a[240:243],v[200:203],v[80:83]\n"
"s_mov_b32 s91,s62\n"
"v_cndmask_b32 v66,v66,v103,vcc\n"
"v_cmp_eq_u32 vcc,2,v101\n"
"v_mfma_f32_16x16x32_bf16 v[80:83],a[248:251],v[204:207],v[80:83]\n"
"s_nop 0\n"
"v_cndmask_b32 v67,0,v102,vcc\n"
"v_cvt_pk_bf16_f32 v88,v88,v89\n"
"v_cvt_pk_bf16_f32 v89,v90,v91\n"
"v_mfma_f32_16x16x32_bf16 v[84:87],a[252:255],v[204:207],0\n"
"ds_read_b128 v[204:207],v113 offset:19456\n"
"s_add_u32 s20,s20,32768\n"
"v_mfma_f32_16x16x32_bf16 v[84:87],a[244:247],v[200:203],v[84:87]\n"
"s_and_b32 s20,s20,0x1ffff\n"
"ds_read_b128 v[200:203],v113 offset:18432\n"
"buffer_load_dwordx4 a[112:115],v126,s[24:27],s91 offen sc1\n"
"v_mfma_f32_16x16x32_bf16 v[84:87],a[236:239],v[196:199],v[84:87]\n"
"ds_read_b128 v[196:199],v113 offset:17408\n"
"v_cvt_pk_bf16_f32 v90,v92,v93\n"
"v_mfma_f32_16x16x32_bf16 v[84:87],a[228:231],v[192:195],v[84:87]\n"
"ds_read_b128 v[192:195],v113 offset:16384\n"
"s_waitcnt lgkmcnt(12)\n"
"v_mfma_f32_16x16x32_bf16 v[84:87],a[4:7],v[208:211],v[84:87]\n"
"v_cvt_pk_bf16_f32 v91,v94,v95\n"
"v_max_u32 v88,1,v88\n"
"v_mfma_f32_16x16x32_bf16 v[84:87],a[12:15],v[212:215],v[84:87]\n"
"buffer_load_dwordx4 a[116:119],v117,s[24:27],s91 offen sc1\n"
"v_max_u32 v89,1,v89\n"
"v_max_u32 v90,1,v90\n"
"v_max_u32 v91,1,v91\n"
"v_mfma_f32_16x16x32_bf16 v[84:87],a[20:23],v[216:219],v[84:87]\n"
"buffer_store_dwordx4 v[88:91],v118,s[24:27],s53 offen sc1\n"
"v_cmp_eq_u32 vcc,3,v101\n"
"v_mfma_f32_16x16x32_bf16 v[84:87],a[28:31],v[220:223],v[84:87]\n"
"s_add_u32 s91,s91,4194304\n"
"v_cndmask_b32 v67,v67,v103,vcc\n"
"v_mfma_f32_16x16x32_bf16 v[80:83],a[24:27],v[220:223],v[80:83]\n"
"ds_read_b128 v[220:223],v113 offset:23552\n"
"v_mfma_f32_16x16x32_bf16 v[80:83],a[16:19],v[216:219],v[80:83]\n"
"ds_read_b128 v[216:219],v113 offset:22528\n"
"v_mfma_f32_16x16x32_bf16 v[80:83],a[8:11],v[212:215],v[80:83]\n"
"ds_read_b128 v[212:215],v113 offset:21504\n"
"buffer_load_dwordx4 a[120:123],v126,s[24:27],s91 offen sc1\n"
"v_mfma_f32_16x16x32_bf16 v[80:83],a[0:3],v[208:211],v[80:83]\n"
"ds_read_b128 v[208:211],v113 offset:20480\n"
"s_waitcnt lgkmcnt(12)\n"
"v_mfma_f32_16x16x32_bf16 v[80:83],v[32:35],v[224:227],v[80:83]\n"
"v_mfma_f32_16x16x32_bf16 v[80:83],v[40:43],v[228:231],v[80:83]\n"
"buffer_load_dwordx4 a[124:127],v117,s[24:27],s91 offen sc1\n"
"v_mfma_f32_16x16x32_bf16 v[80:83],a[32:35],v[232:235],v[80:83]\n"
"v_mfma_f32_16x16x32_bf16 v[80:83],a[40:43],v[236:239],v[80:83]\n"
"v_mfma_f32_16x16x32_bf16 v[84:87],a[44:47],v[236:239],v[84:87]\n"
"ds_read_b128 v[236:239],v113 offset:27648\n"
"s_add_u32 s91,s91,4194304\n"
"v_mfma_f32_16x16x32_bf16 v[84:87],a[36:39],v[232:235],v[84:87]\n"
"ds_read_b128 v[232:235],v113 offset:26624\n"
"v_mfma_f32_16x16x32_bf16 v[84:87],v[44:47],v[228:231],v[84:87]\n"
"ds_read_b128 v[228:231],v113 offset:25600\n"
"v_mfma_f32_16x16x32_bf16 v[84:87],v[36:39],v[224:227],v[84:87]\n"
"ds_read_b128 v[224:227],v113 offset:24576\n"
"buffer_load_dwordx4 a[128:131],v126,s[24:27],s91 offen sc1\n"
"s_waitcnt lgkmcnt(12)\n"
"v_mfma_f32_16x16x32_bf16 v[84:87],a[68:71],v[240:243],v[84:87]\n"
"v_mfma_f32_16x16x32_bf16 v[84:87],a[76:79],v[244:247],v[84:87]\n"
"v_mfma_f32_16x16x32_bf16 v[84:87],a[84:87],v[248:251],v[84:87]\n"
"buffer_load_dwordx4 a[132:135],v117,s[24:27],s91 offen sc1\n"
"v_mfma_f32_16x16x32_bf16 v[84:87],a[92:95],v[252:255],v[84:87]\n"
"v_mfma_f32_16x16x32_bf16 v[80:83],a[88:91],v[252:255],v[80:83]\n"
"ds_read_b128 v[252:255],v113 offset:31744\n"
"s_mov_b32 s53,s56\n"
"v_mfma_f32_16x16x32_bf16 v[80:83],a[80:83],v[248:251],v[80:83]\n"
"s_mov_b32 s56,s58\n"
"ds_read_b128 v[248:251],v113 offset:30720\n"
"s_add_u32 s91,s91,4194304\n"
"v_mfma_f32_16x16x32_bf16 v[80:83],a[72:75],v[244:247],v[80:83]\n"
"ds_read_b128 v[244:247],v113 offset:29696\n"
"s_mov_b32 s58,s59\n"
"v_mfma_f32_16x16x32_bf16 v[80:83],a[156:159],v[240:243],v[80:83]\n"
"ds_read_b128 v[240:243],v113 offset:28672\n"
"s_waitcnt lgkmcnt(12)\n"
"v_mfma_f32_16x16x32_bf16 v[88:91],v[128:131],v[192:195],v[80:83]\n"
"s_waitcnt vmcnt(16)\n"
"ds_add_u32 v114,v116\n"
"v_mfma_f32_16x16x32_bf16 v[88:91],v[136:139],v[196:199],v[88:91]\n"
"buffer_load_dwordx4 a[136:139],v126,s[24:27],s91 offen sc1\n"
"v_mfma_f32_16x16x32_bf16 v[88:91],v[144:147],v[200:203],v[88:91]\n"
"s_add_u32 s83,s20,32768\n"
"s_and_b32 s83,s83,0x1ffff\n"
"v_mfma_f32_16x16x32_bf16 v[88:91],v[152:155],v[204:207],v[88:91]\n"
"buffer_load_dwordx4 a[140:143],v117,s[24:27],s91 offen sc1\n"
"v_mfma_f32_16x16x32_bf16 v[92:95],v[156:159],v[204:207],v[84:87]\n"
"v_add_u32 v113,s83,v112\n"
"v_mfma_f32_16x16x32_bf16 v[92:95],v[148:151],v[200:203],v[92:95]\n"
"v_mfma_f32_16x16x32_bf16 v[92:95],v[140:143],v[196:199],v[92:95]\n"
"v_mfma_f32_16x16x32_bf16 v[92:95],v[132:135],v[192:195],v[92:95]\n"
"s_waitcnt lgkmcnt(9)\n"
"v_mfma_f32_16x16x32_bf16 v[92:95],v[4:7],v[208:211],v[92:95]\n"
"v_mfma_f32_16x16x32_bf16 v[92:95],v[12:15],v[212:215],v[92:95]\n"
"ds_read_b32 v125,v114\n"
"v_mfma_f32_16x16x32_bf16 v[92:95],v[164:167],v[216:219],v[92:95]\n"
"v_mfma_f32_16x16x32_bf16 v[92:95],v[172:175],v[220:223],v[92:95]\n"
"v_mfma_f32_16x16x32_bf16 v[88:91],v[168:171],v[220:223],v[88:91]\n"
"v_mfma_f32_16x16x32_bf16 v[88:91],v[160:163],v[216:219],v[88:91]\n"
"s_waitcnt lgkmcnt(0)\n"
"v_cmp_gt_u32 vcc,s16,v125\n"
"v_mfma_f32_16x16x32_bf16 v[88:91],v[8:11],v[212:215],v[88:91]\n"
"v_mfma_f32_16x16x32_bf16 v[88:91],v[0:3],v[208:211],v[88:91]\n"
"s_cbranch_vccnz .Lfk_es_me2\n"
".Lfk_er_me2:\n"
"v_mfma_f32_16x16x32_bf16 v[88:91],v[176:179],v[224:227],v[88:91]\n"
"ds_read_b128 v[192:195],v113 offset:0\n"
"v_mfma_f32_16x16x32_bf16 v[88:91],v[184:187],v[228:231],v[88:91]\n"
"ds_read_b128 v[196:199],v113 offset:1024\n"
"v_mfma_f32_16x16x32_bf16 v[88:91],a[48:51],v[232:235],v[88:91]\n"
"ds_read_b128 v[200:203],v113 offset:2048\n"
"v_mfma_f32_16x16x32_bf16 v[88:91],a[56:59],v[236:239],v[88:91]\n"
"ds_read_b128 v[204:207],v113 offset:3072\n"
"v_mfma_f32_16x16x32_bf16 v[92:95],a[60:63],v[236:239],v[92:95]\n"
"ds_read_b128 v[208:211],v113 offset:4096\n"
"v_mfma_f32_16x16x32_bf16 v[92:95],a[52:55],v[232:235],v[92:95]\n"
"ds_read_b128 v[212:215],v113 offset:5120\n"
"v_mfma_f32_16x16x32_bf16 v[92:95],v[188:191],v[228:231],v[92:95]\n"
"ds_read_b128 v[216:219],v113 offset:6144\n"
"v_mfma_f32_16x16x32_bf16 v[92:95],v[180:183],v[224:227],v[92:95]\n"
"ds_read_b128 v[220:223],v113 offset:7168\n"
"v_mfma_f32_16x16x32_bf16 v[92:95],v[52:55],v[240:243],v[92:95]\n"
"ds_read_b128 v[236:239],v113 offset:11264\n"
"v_mfma_f32_16x16x32_bf16 v[92:95],v[60:63],v[244:247],v[92:95]\n"
"ds_read_b128 v[232:235],v113 offset:10240\n"
"v_mfma_f32_16x16x32_bf16 v[92:95],v[20:23],v[248:251],v[92:95]\n"
"ds_read_b128 v[228:231],v113 offset:9216\n"
"v_mfma_f32_16x16x32_bf16 v[92:95],v[28:31],v[252:255],v[92:95]\n"
"ds_read_b128 v[224:227],v113 offset:8192\n"
"v_mfma_f32_16x16x32_bf16 v[88:91],v[24:27],v[252:255],v[88:91]\n"
"ds_read_b128 v[252:255],v113 offset:15360\n"
"v_mfma_f32_16x16x32_bf16 v[88:91],v[16:19],v[248:251],v[88:91]\n"
"ds_read_b128 v[248:251],v113 offset:14336\n"
"v_mfma_f32_16x16x32_bf16 v[88:91],v[56:59],v[244:247],v[88:91]\n"
"ds_read_b128 v[244:247],v113 offset:13312\n"
"v_mfma_f32_16x16x32_bf16 v[88:91],v[48:51],v[240:243],v[88:91]\n"
"ds_read_b128 v[240:243],v113 offset:12288\n"
"s_waitcnt lgkmcnt(12)\n"
"v_mfma_f32_16x16x32_bf16 v[80:83],a[224:227],v[192:195],0\n"
"v_mfma_f32_16x16x32_bf16 v[80:83],a[232:235],v[196:199],v[80:83]\n"
"v_mfma_f32_16x16x32_bf16 v[80:83],a[240:243],v[200:203],v[80:83]\n"
"s_nop 2\n"
"v_cvt_pk_bf16_f32 v88,v88,v89\n"
"v_mfma_f32_16x16x32_bf16 v[80:83],a[248:251],v[204:207],v[80:83]\n"
"v_cvt_pk_bf16_f32 v89,v90,v91\n"
"v_mfma_f32_16x16x32_bf16 v[84:87],a[252:255],v[204:207],0\n"
"ds_read_b128 v[204:207],v113 offset:19456\n"
"v_cvt_pk_bf16_f32 v90,v92,v93\n"
"v_mfma_f32_16x16x32_bf16 v[84:87],a[244:247],v[200:203],v[84:87]\n"
"ds_read_b128 v[200:203],v113 offset:18432\n"
"v_cvt_pk_bf16_f32 v91,v94,v95\n"
"v_mfma_f32_16x16x32_bf16 v[84:87],a[236:239],v[196:199],v[84:87]\n"
"ds_read_b128 v[196:199],v113 offset:17408\n"
"v_max_u32 v88,1,v88\n"
"v_mfma_f32_16x16x32_bf16 v[84:87],a[228:231],v[192:195],v[84:87]\n"
"ds_read_b128 v[192:195],v113 offset:16384\n"
"s_waitcnt lgkmcnt(12)\n"
"v_mfma_f32_16x16x32_bf16 v[84:87],a[4:7],v[208:211],v[84:87]\n"
"v_max_u32 v89,1,v89\n"
"v_max_u32 v90,1,v90\n"
"v_max_u32 v91,1,v91\n"
"v_mfma_f32_16x16x32_bf16 v[84:87],a[12:15],v[212:215],v[84:87]\n"
"buffer_store_dwordx4 v[88:91],v118,s[24:27],s53 offen sc1\n"
"v_mfma_f32_16x16x32_bf16 v[84:87],a[20:23],v[216:219],v[84:87]\n"
"v_mfma_f32_16x16x32_bf16 v[84:87],a[28:31],v[220:223],v[84:87]\n"
"v_mfma_f32_16x16x32_bf16 v[80:83],a[24:27],v[220:223],v[80:83]\n"
"ds_read_b128 v[220:223],v113 offset:23552\n"
"v_mfma_f32_16x16x32_bf16 v[80:83],a[16:19],v[216:219],v[80:83]\n"
"ds_read_b128 v[216:219],v113 offset:22528\n"
"v_mfma_f32_16x16x32_bf16 v[80:83],a[8:11],v[212:215],v[80:83]\n"
"ds_read_b128 v[212:215],v113 offset:21504\n"
"v_mfma_f32_16x16x32_bf16 v[80:83],a[0:3],v[208:211],v[80:83]\n"
"ds_read_b128 v[208:211],v113 offset:20480\n"
"s_waitcnt lgkmcnt(12)\n"
"v_mfma_f32_16x16x32_bf16 v[80:83],v[32:35],v[224:227],v[80:83]\n"
"v_mfma_f32_16x16x32_bf16 v[80:83],v[40:43],v[228:231],v[80:83]\n"
"v_mfma_f32_16x16x32_bf16 v[80:83],a[32:35],v[232:235],v[80:83]\n"
"v_mfma_f32_16x16x32_bf16 v[80:83],a[40:43],v[236:239],v[80:83]\n"
"v_mfma_f32_16x16x32_bf16 v[84:87],a[44:47],v[236:239],v[84:87]\n"
"ds_read_b128 v[236:239],v113 offset:27648\n"
"v_mfma_f32_16x16x32_bf16 v[84:87],a[36:39],v[232:235],v[84:87]\n"
"ds_read_b128 v[232:235],v113 offset:26624\n"
"v_mfma_f32_16x16x32_bf16 v[84:87],v[44:47],v[228:231],v[84:87]\n"
"ds_read_b128 v[228:231],v113 offset:25600\n"
"v_mfma_f32_16x16x32_bf16 v[84:87],v[36:39],v[224:227],v[84:87]\n"
"ds_read_b128 v[224:227],v113 offset:24576\n"
"s_waitcnt lgkmcnt(12)\n"
"v_mfma_f32_16x16x32_bf16 v[84:87],a[68:71],v[240:243],v[84:87]\n"
"v_mfma_f32_16x16x32_bf16 v[84:87],a[76:79],v[244:247],v[84:87]\n"
"v_mfma_f32_16x16x32_bf16 v[84:87],a[84:87],v[248:251],v[84:87]\n"
"v_mfma_f32_16x16x32_bf16 v[84:87],a[92:95],v[252:255],v[84:87]\n"
"v_mfma_f32_16x16x32_bf16 v[80:83],a[88:91],v[252:255],v[80:83]\n"
"ds_read_b128 v[252:255],v113 offset:31744\n"
"s_mov_b32 s53,s56\n"
"v_mfma_f32_16x16x32_bf16 v[80:83],a[80:83],v[248:251],v[80:83]\n"
"ds_read_b128 v[248:251],v113 offset:30720\n"
"s_mov_b32 s56,s58\n"
"v_mfma_f32_16x16x32_bf16 v[80:83],a[72:75],v[244:247],v[80:83]\n"
"ds_read_b128 v[244:247],v113 offset:29696\n"
"s_mov_b32 s58,s59\n"
"v_mfma_f32_16x16x32_bf16 v[80:83],a[156:159],v[240:243],v[80:83]\n"
"ds_read_b128 v[240:243],v113 offset:28672\n"
"s_waitcnt lgkmcnt(12)\n"
"v_mfma_f32_16x16x32_bf16 v[88:91],v[128:131],v[192:195],v[80:83]\n"
"v_mfma_f32_16x16x32_bf16 v[88:91],v[136:139],v[196:199],v[88:91]\n"
"v_mfma_f32_16x16x32_bf16 v[88:91],v[144:147],v[200:203],v[88:91]\n"
"v_mfma_f32_16x16x32_bf16 v[88:91],v[152:155],v[204:207],v[88:91]\n"
"v_mfma_f32_16x16x32_bf16 v[92:95],v[156:159],v[204:207],v[84:87]\n"
"v_mfma_f32_16x16x32_bf16 v[92:95],v[148:151],v[200:203],v[92:95]\n"
"v_mfma_f32_16x16x32_bf16 v[92:95],v[140:143],v[196:199],v[92:95]\n"
"v_mfma_f32_16x16x32_bf16 v[92:95],v[132:135],v[192:195],v[92:95]\n"
"s_waitcnt lgkmcnt(8)\n"
"v_mfma_f32_16x16x32_bf16 v[92:95],v[4:7],v[208:211],v[92:95]\n"
"v_mfma_f32_16x16x32_bf16 v[92:95],v[12:15],v[212:215],v[92:95]\n"
"v_mfma_f32_16x16x32_bf16 v[92:95],v[164:167],v[216:219],v[92:95]\n"
"v_mfma_f32_16x16x32_bf16 v[92:95],v[172:175],v[220:223],v[92:95]\n"
"v_mfma_f32_16x16x32_bf16 v[88:91],v[168:171],v[220:223],v[88:91]\n"
"s_mov_b32 s91,s62\n"
"v_mfma_f32_16x16x32_bf16 v[88:91],v[160:163],v[216:219],v[88:91]\n"
"v_mfma_f32_16x16x32_bf16 v[88:91],v[8:11],v[212:215],v[88:91]\n"
"v_mfma_f32_16x16x32_bf16 v[88:91],v[0:3],v[208:211],v[88:91]\n"
"s_waitcnt lgkmcnt(0)\n"
"s_waitcnt vmcnt(1)\n"
"v_mfma_f32_16x16x32_bf16 v[88:91],v[176:179],v[224:227],v[88:91]\n"
"v_accvgpr_read_b32 v128,a112\n"
"v_accvgpr_read_b32 v129,a113\n"
"v_mfma_f32_16x16x32_bf16 v[88:91],v[184:187],v[228:231],v[88:91]\n"
"v_accvgpr_read_b32 v130,a114\n"
"v_accvgpr_read_b32 v131,a115\n"
"v_mfma_f32_16x16x32_bf16 v[88:91],a[48:51],v[232:235],v[88:91]\n"
"v_accvgpr_read_b32 v132,a116\n"
"v_accvgpr_read_b32 v133,a117\n"
"v_mfma_f32_16x16x32_bf16 v[88:91],a[56:59],v[236:239],v[88:91]\n"
"v_accvgpr_read_b32 v134,a118\n"
"v_accvgpr_read_b32 v135,a119\n"
"v_mfma_f32_16x16x32_bf16 v[92:95],a[60:63],v[236:239],v[92:95]\n"
"v_accvgpr_read_b32 v136,a120\n"
"v_accvgpr_read_b32 v137,a121\n"
"v_mfma_f32_16x16x32_bf16 v[92:95],a[52:55],v[232:235],v[92:95]\n"
"v_accvgpr_read_b32 v138,a122\n"
"v_accvgpr_read_b32 v139,a123\n"
"v_mfma_f32_16x16x32_bf16 v[92:95],v[188:191],v[228:231],v[92:95]\n"
"v_accvgpr_read_b32 v140,a124\n"
"v_accvgpr_read_b32 v141,a125\n"
"v_mfma_f32_16x16x32_bf16 v[92:95],v[180:183],v[224:227],v[92:95]\n"
"v_accvgpr_read_b32 v142,a126\n"
"v_accvgpr_read_b32 v143,a127\n"
"v_mfma_f32_16x16x32_bf16 v[92:95],v[52:55],v[240:243],v[92:95]\n"
"v_accvgpr_read_b32 v144,a128\n"
"v_accvgpr_read_b32 v145,a129\n"
"v_mfma_f32_16x16x32_bf16 v[92:95],v[60:63],v[244:247],v[92:95]\n"
"v_accvgpr_read_b32 v146,a130\n"
"v_accvgpr_read_b32 v147,a131\n"
"v_mfma_f32_16x16x32_bf16 v[92:95],v[20:23],v[248:251],v[92:95]\n"
"v_accvgpr_read_b32 v148,a132\n"
"v_accvgpr_read_b32 v149,a133\n"
"v_mfma_f32_16x16x32_bf16 v[92:95],v[28:31],v[252:255],v[92:95]\n"
"v_accvgpr_read_b32 v150,a134\n"
"v_accvgpr_read_b32 v151,a135\n"
"v_mfma_f32_16x16x32_bf16 v[88:91],v[24:27],v[252:255],v[88:91]\n"
"v_accvgpr_read_b32 v152,a136\n"
"v_accvgpr_read_b32 v153,a137\n"
"v_mfma_f32_16x16x32_bf16 v[88:91],v[16:19],v[248:251],v[88:91]\n"
"v_accvgpr_read_b32 v154,a138\n"
"v_accvgpr_read_b32 v155,a139\n"
"v_mfma_f32_16x16x32_bf16 v[88:91],v[56:59],v[244:247],v[88:91]\n"
"v_accvgpr_read_b32 v156,a140\n"
"v_accvgpr_read_b32 v157,a141\n"
"v_mfma_f32_16x16x32_bf16 v[88:91],v[48:51],v[240:243],v[88:91]\n"
"v_accvgpr_read_b32 v158,a142\n"
"v_accvgpr_read_b32 v159,a143\n"
".Lfk_tail:\n"
"v_lshlrev_b32 v224,16,v72\n"
"v_and_b32 v225,0xffff0000,v72\n"
"v_lshlrev_b32 v226,16,v73\n"
"v_and_b32 v227,0xffff0000,v73\n"
"v_lshlrev_b32 v228,16,v74\n"
"s_nop 0\n"
"v_cvt_pk_bf16_f32 v88,v88,v89\n"
"v_cvt_pk_bf16_f32 v89,v90,v91\n"
"v_cvt_pk_bf16_f32 v90,v92,v93\n"
"v_cvt_pk_bf16_f32 v91,v94,v95\n"
"v_max_u32 v88,1,v88\n"
"v_max_u32 v89,1,v89\n"
"v_max_u32 v90,1,v90\n"
"v_max_u32 v91,1,v91\n"
"v_mfma_f32_16x16x32_bf16 v[0:3],v[66:69],a[96:99],0\n"
"buffer_store_dwordx4 v[88:91],v118,s[24:27],s53 offen sc1\n"
"v_mfma_f32_16x16x32_bf16 v[4:7],v[64:67],a[96:99],0\n"
"s_mov_b32 s101,s22\n"
"v_and_b32 v231,0xffff0000,v75\n"
"v_lshlrev_b32 v232,16,v76\n"
"v_mfma_f32_16x16x32_bf16 v[8:11],v[66:69],a[100:103],0\n"
"v_and_b32 v229,0xffff0000,v74\n"
"v_lshlrev_b32 v230,16,v75\n"
"v_mfma_f32_16x16x32_bf16 v[12:15],v[64:67],a[100:103],0\n"
"s_mov_b32 s67,0x3a800000\n"
"v_and_b32 v233,0xffff0000,v76\n"
"v_lshlrev_b32 v234,16,v77\n"
"v_and_b32 v235,0xffff0000,v77\n"
"v_lshlrev_b32 v236,16,v78\n"
"v_and_b32 v237,0xffff0000,v78\n"
"v_lshlrev_b32 v238,16,v79\n"
"v_and_b32 v239,0xffff0000,v79\n"
"v_pk_add_f32 v[70:71],v[224:225],v[226:227]\n"
"v_pk_add_f32 v[70:71],v[70:71],v[228:229]\n"
"v_pk_add_f32 v[70:71],v[70:71],v[230:231]\n"
"v_pk_add_f32 v[70:71],v[70:71],v[232:233]\n"
"v_pk_add_f32 v[70:71],v[70:71],v[234:235]\n"
"v_pk_add_f32 v[70:71],v[70:71],v[236:237]\n"
"v_pk_add_f32 v[70:71],v[70:71],v[238:239]\n"
"v_add_f32 v70,v70,v71\n"
"s_nop 1\n"
"v_add_f32_dpp v70,v70,v70 quad_perm:[1,0,3,2] row_mask:0xf bank_mask:0xf\n"
"s_nop 1\n"
"v_add_f32_dpp v70,v70,v70 quad_perm:[2,3,0,1] row_mask:0xf bank_mask:0xf\n"
"s_nop 1\n"
"v_add_f32_dpp v70,v70,v70 row_half_mirror row_mask:0xf bank_mask:0xf\n"
"s_nop 1\n"
"v_add_f32_dpp v70,v70,v70 row_mirror row_mask:0xf bank_mask:0xf\n"
"v_mov_b32 v71,v70\n"
"s_nop 1\n"
"v_permlane16_swap_b32 v71,v70\n"
"v_add_f32 v70,v70,v71\n"
"v_mov_b32 v71,v70\n"
"s_nop 1\n"
"v_permlane32_swap_b32 v71,v70\n"
"v_add_f32 v70,v70,v71\n"
"v_min3_u32 v36,v128,v129,v130\n"
"v_min3_u32 v37,v131,v132,v133\n"
"v_min3_u32 v36,v36,v134,v135\n"
"v_min3_u32 v37,v37,v136,v137\n"
"v_min3_u32 v36,v36,v138,v139\n"
"v_min3_u32 v37,v37,v140,v141\n"
"v_min3_u32 v36,v36,v142,v143\n"
"v_min3_u32 v37,v37,v144,v145\n"
"v_min3_u32 v36,v36,v146,v147\n"
"v_min3_u32 v37,v37,v148,v149\n"
"v_min3_u32 v36,v36,v150,v151\n"
"v_min3_u32 v37,v37,v152,v153\n"
"v_min3_u32 v36,v36,v154,v155\n"
"v_min3_u32 v37,v37,v156,v157\n"
"v_min3_u32 v36,v36,v158,v159\n"
"v_min_u32 v36,v36,v37\n"
"v_cmp_eq_u32 vcc,0,v36\n"
"s_nop 0\n"
"s_cbranch_vccz .Lfk_t0\n"
"s_mov_b32 s84,0x40000\n"
"s_mov_b32 s91,s62\n"
"buffer_load_dwordx4 v[128:131],v126,s[24:27],s91 offen sc1\n"
"buffer_load_dwordx4 v[132:135],v117,s[24:27],s91 offen sc1\n"
"s_add_u32 s91,s91,4194304\n"
"buffer_load_dwordx4 v[136:139],v126,s[24:27],s91 offen sc1\n"
"buffer_load_dwordx4 v[140:143],v117,s[24:27],s91 offen sc1\n"
"s_add_u32 s91,s91,4194304\n"
"buffer_load_dwordx4 v[144:147],v126,s[24:27],s91 offen sc1\n"
"buffer_load_dwordx4 v[148:151],v117,s[24:27],s91 offen sc1\n"
"s_add_u32 s91,s91,4194304\n"
"buffer_load_dwordx4 v[152:155],v126,s[24:27],s91 offen sc1\n"
"buffer_load_dwordx4 v[156:159],v117,s[24:27],s91 offen sc1\n"
"s_waitcnt vmcnt(0)\n"
".Lfk_wa:\n"
"v_min3_u32 v36,v128,v129,v130\n"
"v_min3_u32 v37,v131,v132,v133\n"
"v_min3_u32 v36,v36,v134,v135\n"
"v_min3_u32 v37,v37,v136,v137\n"
"v_min3_u32 v36,v36,v138,v139\n"
"v_min3_u32 v37,v37,v140,v141\n"
"v_min3_u32 v36,v36,v142,v143\n"
"v_min3_u32 v37,v37,v144,v145\n"
"v_min3_u32 v36,v36,v146,v147\n"
"v_min3_u32 v37,v37,v148,v149\n"
"v_min3_u32 v36,v36,v150,v151\n"
"v_min3_u32 v37,v37,v152,v153\n"
"v_min3_u32 v36,v36,v154,v155\n"
"v_min3_u32 v37,v37,v156,v157\n"
"v_min3_u32 v36,v36,v158,v159\n"
"v_min_u32 v36,v36,v37\n"
"v_cmp_eq_u32 vcc,0,v36\n"
"s_nop 0\n"
"s_cbranch_vccz .Lfk_wa_x\n"
"s_sleep 2\n"
"s_mov_b64 exec,vcc\n"
"s_mov_b32 s91,s62\n"
"buffer_load_dwordx4 v[128:131],v126,s[24:27],s91 offen sc1\n"
"buffer_load_dwordx4 v[132:135],v117,s[24:27],s91 offen sc1\n"
"s_add_u32 s91,s91,4194304\n"
"buffer_load_dwordx4 v[136:139],v126,s[24:27],s91 offen sc1\n"
"buffer_load_dwordx4 v[140:143],v117,s[24:27],s91 offen sc1\n"
"s_add_u32 s91,s91,4194304\n"
"buffer_load_dwordx4 v[144:147],v126,s[24:27],s91 offen sc1\n"
"buffer_load_dwordx4 v[148:151],v117,s[24:27],s91 offen sc1\n"
"s_add_u32 s91,s91,4194304\n"
"buffer_load_dwordx4 v[152:155],v126,s[24:27],s91 offen sc1\n"
"buffer_load_dwordx4 v[156:159],v117,s[24:27],s91 offen sc1\n"
"s_mov_b64 exec,-1\n"
"s_sub_u32 s84,s84,1\n"
"s_waitcnt vmcnt(0)\n"
"s_cbranch_scc0 .Lfk_wa\n"
".Lfk_wa_x:\n"
".Lfk_t0:\n"
"s_getpc_b64 s[94:95]\n"
".Lfk_rm0:\n"
"s_add_u32 s94,s94,.Lfk_rm1-.Lfk_rm0\n"
"v_mfma_f32_16x16x32_bf16 v[0:3],v[66:69],v[128:131],v[0:3]\n"
"s_addc_u32 s95,s95,0\n"
"v_mfma_f32_16x16x32_bf16 v[4:7],v[64:67],v[128:131],v[4:7]\n"
"v_mfma_f32_16x16x32_bf16 v[8:11],v[66:69],v[132:135],v[8:11]\n"
"v_mfma_f32_16x16x32_bf16 v[12:15],v[64:67],v[132:135],v[12:15]\n"
"v_mfma_f32_16x16x32_bf16 v[0:3],v[66:69],v[136:139],v[0:3]\n"
"v_mfma_f32_16x16x32_bf16 v[4:7],v[64:67],v[136:139],v[4:7]\n"
"v_mfma_f32_16x16x32_bf16 v[8:11],v[66:69],v[140:143],v[8:11]\n"
"v_mfma_f32_16x16x32_bf16 v[12:15],v[64:67],v[140:143],v[12:15]\n"
"v_mfma_f32_16x16x32_bf16 v[0:3],v[66:69],v[144:147],v[0:3]\n"
"v_mfma_f32_16x16x32_bf16 v[4:7],v[64:67],v[144:147],v[4:7]\n"
"v_mfma_f32_16x16x32_bf16 v[8:11],v[66:69],v[148:151],v[8:11]\n"
"v_mfma_f32_16x16x32_bf16 v[12:15],v[64:67],v[148:151],v[12:15]\n"
"v_mfma_f32_16x16x32_bf16 v[0:3],v[66:69],v[152:155],v[0:3]\n"
"v_mfma_f32_16x16x32_bf16 v[4:7],v[64:67],v[152:155],v[4:7]\n"
"v_mfma_f32_16x16x32_bf16 v[8:11],v[66:69],v[156:159],v[8:11]\n"
"v_mfma_f32_16x16x32_bf16 v[12:15],v[64:67],v[156:159],v[12:15]\n"
"s_nop 7\n"
".Lfk_rm:\n"
"v_readfirstlane_b32 s64,v0\n"
"s_nop 1\n"
"v_mov_b32 v38,s64\n"
"v_pk_add_f32 v[16:17],v[0:1],v[38:39] op_sel_hi:[1,0] neg_lo:[0,1] neg_hi:[0,1]\n"
"v_pk_add_f32 v[18:19],v[2:3],v[38:39] op_sel_hi:[1,0] neg_lo:[0,1] neg_hi:[0,1]\n"
"v_pk_add_f32 v[20:21],v[4:5],v[38:39] op_sel_hi:[1,0] neg_lo:[0,1] neg_hi:[0,1]\n"
"v_pk_add_f32 v[22:23],v[6:7],v[38:39] op_sel_hi:[1,0] neg_lo:[0,1] neg_hi:[0,1]\n"
"v_pk_add_f32 v[24:25],v[8:9],v[38:39] op_sel_hi:[1,0] neg_lo:[0,1] neg_hi:[0,1]\n"
"v_pk_add_f32 v[26:27],v[10:11],v[38:39] op_sel_hi:[1,0] neg_lo:[0,1] neg_hi:[0,1]\n"
"v_pk_add_f32 v[28:29],v[12:13],v[38:39] op_sel_hi:[1,0] neg_lo:[0,1] neg_hi:[0,1]\n"
"v_pk_add_f32 v[30:31],v[14:15],v[38:39] op_sel_hi:[1,0] neg_lo:[0,1] neg_hi:[0,1]\n"
"v_pk_add_f32 v[32:33],v[16:17],v[18:19]\n"
"v_pk_mul_f32 v[34:35],v[16:17],v[16:17]\n"
"v_pk_mul_f32 v[36:37],v[16:17],v[224:225]\n"
"v_pk_fma_f32 v[34:35],v[18:19],v[18:19],v[34:35]\n"
"v_pk_fma_f32 v[36:37],v[18:19],v[226:227],v[36:37]\n"
"v_pk_add_f32 v[32:33],v[32:33],v[20:21]\n"
"v_pk_fma_f32 v[34:35],v[20:21],v[20:21],v[34:35]\n"
"v_pk_fma_f32 v[36:37],v[20:21],v[228:229],v[36:37]\n"
"v_pk_add_f32 v[32:33],v[32:33],v[22:23]\n"
"v_pk_fma_f32 v[34:35],v[22:23],v[22:23],v[34:35]\n"
"v_pk_fma_f32 v[36:37],v[22:23],v[230:231],v[36:37]\n"
"v_pk_add_f32 v[32:33],v[32:33],v[24:25]\n"
"v_pk_fma_f32 v[34:35],v[24:25],v[24:25],v[34:35]\n"
"v_pk_fma_f32 v[36:37],v[24:25],v[232:233],v[36:37]\n"
"v_pk_add_f32 v[32:33],v[32:33],v[26:27]\n"
"v_pk_fma_f32 v[34:35],v[26:27],v[26:27],v[34:35]\n"
"v_pk_fma_f32 v[36:37],v[26:27],v[234:235],v[36:37]\n"
"v_pk_add_f32 v[32:33],v[32:33],v[28:29]\n"
"v_pk_fma_f32 v[34:35],v[28:29],v[28:29],v[34:35]\n"
"v_pk_fma_f32 v[36:37],v[28:29],v[236:237],v[36:37]\n"
"v_pk_add_f32 v[32:33],v[32:33],v[30:31]\n"
"v_pk_fma_f32 v[34:35],v[30:31],v[30:31],v[34:35]\n"
"v_pk_fma_f32 v[36:37],v[30:31],v[238:239],v[36:37]\n"
"v_add_f32 v32,v32,v33\n"
"v_add_f32 v33,v34,v35\n"
"v_add_f32 v34,v36,v37\n"
"v_add_f32_dpp v32,v32,v32 quad_perm:[1,0,3,2] row_mask:0xf bank_mask:0xf\n"
"v_add_f32_dpp v33,v33,v33 quad_perm:[1,0,3,2] row_mask:0xf bank_mask:0xf\n"
"v_add_f32_dpp v34,v34,v34 quad_perm:[1,0,3,2] row_mask:0xf bank_mask:0xf\n"
"v_add_f32_dpp v32,v32,v32 quad_perm:[2,3,0,1] row_mask:0xf bank_mask:0xf\n"
"v_add_f32_dpp v33,v33,v33 quad_perm:[2,3,0,1] row_mask:0xf bank_mask:0xf\n"
"v_add_f32_dpp v34,v34,v34 quad_perm:[2,3,0,1] row_mask:0xf bank_mask:0xf\n"
"v_add_f32_dpp v32,v32,v32 row_half_mirror row_mask:0xf bank_mask:0xf\n"
"v_add_f32_dpp v33,v33,v33 row_half_mirror row_mask:0xf bank_mask:0xf\n"
"v_add_f32_dpp v34,v34,v34 row_half_mirror row_mask:0xf bank_mask:0xf\n"
"v_add_f32_dpp v32,v32,v32 row_mirror row_mask:0xf bank_mask:0xf\n"
"v_add_f32_dpp v33,v33,v33 row_mirror row_mask:0xf bank_mask:0xf\n"
"v_add_f32_dpp v34,v34,v34 row_mirror row_mask:0xf bank_mask:0xf\n"
"v_add_f32_dpp v32,v32,v32 row_bcast:15 row_mask:0xa bank_mask:0xf\n"
"v_add_f32_dpp v33,v33,v33 row_bcast:15 row_mask:0xa bank_mask:0xf\n"
"v_add_f32_dpp v34,v34,v34 row_bcast:15 row_mask:0xa bank_mask:0xf\n"
"v_add_f32_dpp v32,v32,v32 row_bcast:31 row_mask:0xc bank_mask:0xf\n"
"v_add_f32_dpp v33,v33,v33 row_bcast:31 row_mask:0xc bank_mask:0xf\n"
"v_add_f32_dpp v34,v34,v34 row_bcast:31 row_mask:0xc bank_mask:0xf\n"
"v_mul_f32 v32,s67,v32\n"
"v_add_f32 v35,v32,v38\n"
"v_mul_f32 v32,v32,v32\n"
"v_cvt_pk_bf16_f32 v35,v35,v35\n"
"v_lshlrev_b32 v35,16,v35\n"
"v_fma_f32 v33,v33,s67,-v32\n"
"v_sub_f32 v36,v38,v35\n"
"v_max_f32 v33,0,v33\n"
"v_fmac_f32 v34,v36,v70\n"
"v_cvt_pk_bf16_f32 v33,v33,v33\n"
"v_lshlrev_b32 v33,16,v33\n"
"v_add_f32 v33,0x3727c5ac,v33\n"
"v_cvt_pk_bf16_f32 v33,v33,v33\n"
"v_lshlrev_b32 v33,16,v33\n"
"v_sqrt_f32 v33,v33\n"
"s_nop 0\n"
"v_cvt_pk_bf16_f32 v33,v33,v33\n"
"v_lshlrev_b32 v33,16,v33\n"
"v_rcp_f32 v36,v33\n"
"v_lshrrev_b32 v33,16,v33\n"
"v_mul_f32 v37,v34,v36\n"
"v_or_b32 v33,v33,v35\n"
"v_cvt_pk_bf16_f32 v37,v37,v37\n"
"v_ashrrev_i32 v36,31,v37\n"
"v_or_b32 v36,0x80000000,v36\n"
"v_xor_b32 v37,v37,v36\n"
"v_and_b32 v37,0xffff0000,v37\n"
"v_readlane_b32 s64,v33,63\n"
"v_readlane_b32 s63,v37,63\n"
"s_nop 1\n"
"s_setpc_b64 s[94:95]\n"
".Lfk_rm1:\n"
"s_add_u32 s91,s62,2097152\n"
"buffer_load_dwordx4 v[160:163],v126,s[24:27],s91 offen sc1\n"
"buffer_load_dwordx4 v[164:167],v117,s[24:27],s91 offen sc1\n"
"s_add_u32 s91,s91,4194304\n"
"buffer_load_dwordx4 v[168:171],v126,s[24:27],s91 offen sc1\n"
"buffer_load_dwordx4 v[172:175],v117,s[24:27],s91 offen sc1\n"
"s_add_u32 s91,s91,4194304\n"
"buffer_load_dwordx4 v[176:179],v126,s[24:27],s91 offen sc1\n"
"buffer_load_dwordx4 v[180:183],v117,s[24:27],s91 offen sc1\n"
"s_add_u32 s91,s91,4194304\n"
"buffer_load_dwordx4 v[184:187],v126,s[24:27],s91 offen sc1\n"
"buffer_load_dwordx4 v[188:191],v117,s[24:27],s91 offen sc1\n"
"s_sub_u32 s65,0xffff,s60\n"
"s_or_b32 s65,s65,s63\n"
"s_mov_b32 s66,s64\n"
"v_lshlrev_b32 v34,16,s64\n"
"v_mov_b32 v35,s64\n"
"v_rcp_f32 v34,v34\n"
"v_and_b32 v35,0xffff0000,v35\n"
"v_cvt_pk_bf16_f32 v240,v0,v1\n"
"v_cvt_pk_bf16_f32 v241,v2,v3\n"
"v_cvt_pk_bf16_f32 v242,v4,v5\n"
"v_cvt_pk_bf16_f32 v243,v6,v7\n"
"v_cvt_pk_bf16_f32 v244,v8,v9\n"
"v_cvt_pk_bf16_f32 v245,v10,v11\n"
"v_cvt_pk_bf16_f32 v246,v12,v13\n"
"v_cvt_pk_bf16_f32 v247,v14,v15\n"
"v_lshlrev_b32 v0,16,v240\n"
"v_and_b32 v1,0xffff0000,v240\n"
"v_lshlrev_b32 v2,16,v241\n"
"v_and_b32 v3,0xffff0000,v241\n"
"v_lshlrev_b32 v4,16,v242\n"
"v_and_b32 v5,0xffff0000,v242\n"
"v_lshlrev_b32 v6,16,v243\n"
"v_and_b32 v7,0xffff0000,v243\n"
"v_lshlrev_b32 v8,16,v244\n"
"v_and_b32 v9,0xffff0000,v244\n"
"v_lshlrev_b32 v10,16,v245\n"
"v_and_b32 v11,0xffff0000,v245\n"
"v_lshlrev_b32 v12,16,v246\n"
"v_and_b32 v13,0xffff0000,v246\n"
"v_lshlrev_b32 v14,16,v247\n"
"v_and_b32 v15,0xffff0000,v247\n"
"v_pk_add_f32 v[16:17],v[0:1],v[34:35] op_sel:[0,1] neg_lo:[0,1] neg_hi:[0,1]\n"
"v_pk_add_f32 v[18:19],v[2:3],v[34:35] op_sel:[0,1] neg_lo:[0,1] neg_hi:[0,1]\n"
"v_pk_add_f32 v[20:21],v[4:5],v[34:35] op_sel:[0,1] neg_lo:[0,1] neg_hi:[0,1]\n"
"v_pk_add_f32 v[22:23],v[6:7],v[34:35] op_sel:[0,1] neg_lo:[0,1] neg_hi:[0,1]\n"
"v_pk_add_f32 v[24:25],v[8:9],v[34:35] op_sel:[0,1] neg_lo:[0,1] neg_hi:[0,1]\n"
"v_pk_add_f32 v[26:27],v[10:11],v[34:35] op_sel:[0,1] neg_lo:[0,1] neg_hi:[0,1]\n"
"v_pk_add_f32 v[28:29],v[12:13],v[34:35] op_sel:[0,1] neg_lo:[0,1] neg_hi:[0,1]\n"
"v_pk_add_f32 v[30:31],v[14:15],v[34:35] op_sel:[0,1] neg_lo:[0,1] neg_hi:[0,1]\n"
"v_cvt_pk_bf16_f32 v240,v16,v17\n"
"v_cvt_pk_bf16_f32 v241,v18,v19\n"
"v_cvt_pk_bf16_f32 v242,v20,v21\n"
"v_cvt_pk_bf16_f32 v243,v22,v23\n"
"v_cvt_pk_bf16_f32 v244,v24,v25\n"
"v_cvt_pk_bf16_f32 v245,v26,v27\n"
"v_cvt_pk_bf16_f32 v246,v28,v29\n"
"v_cvt_pk_bf16_f32 v247,v30,v31\n"
"v_lshlrev_b32 v16,16,v240\n"
"v_and_b32 v17,0xffff0000,v240\n"
"v_lshlrev_b32 v18,16,v241\n"
"v_and_b32 v19,0xffff0000,v241\n"
"v_lshlrev_b32 v20,16,v242\n"
"v_and_b32 v21,0xffff0000,v242\n"
"v_lshlrev_b32 v22,16,v243\n"
"v_and_b32 v23,0xffff0000,v243\n"
"v_lshlrev_b32 v24,16,v244\n"
"v_and_b32 v25,0xffff0000,v244\n"
"v_lshlrev_b32 v26,16,v245\n"
"v_and_b32 v27,0xffff0000,v245\n"
"v_lshlrev_b32 v28,16,v246\n"
"v_and_b32 v29,0xffff0000,v246\n"
"v_lshlrev_b32 v30,16,v247\n"
"v_and_b32 v31,0xffff0000,v247\n"
"v_pk_mul_f32 v[16:17],v[16:17],v[34:35] op_sel_hi:[1,0]\n"
"v_pk_mul_f32 v[18:19],v[18:19],v[34:35] op_sel_hi:[1,0]\n"
"v_pk_mul_f32 v[20:21],v[20:21],v[34:35] op_sel_hi:[1,0]\n"
"v_pk_mul_f32 v[22:23],v[22:23],v[34:35] op_sel_hi:[1,0]\n"
"v_pk_mul_f32 v[24:25],v[24:25],v[34:35] op_sel_hi:[1,0]\n"
"v_pk_mul_f32 v[26:27],v[26:27],v[34:35] op_sel_hi:[1,0]\n"
"v_pk_mul_f32 v[28:29],v[28:29],v[34:35] op_sel_hi:[1,0]\n"
"v_pk_mul_f32 v[30:31],v[30:31],v[34:35] op_sel_hi:[1,0]\n"
"v_cvt_pk_bf16_f32 v240,v16,v17\n"
"v_cvt_pk_bf16_f32 v241,v18,v19\n"
"v_cvt_pk_bf16_f32 v242,v20,v21\n"
"v_cvt_pk_bf16_f32 v243,v22,v23\n"
"v_cvt_pk_bf16_f32 v244,v24,v25\n"
"v_cvt_pk_bf16_f32 v245,v26,v27\n"
"v_cvt_pk_bf16_f32 v246,v28,v29\n"
"v_cvt_pk_bf16_f32 v247,v30,v31\n"
"v_lshlrev_b32 v16,16,v240\n"
"v_and_b32 v17,0xffff0000,v240\n"
"v_lshlrev_b32 v18,16,v241\n"
"v_and_b32 v19,0xffff0000,v241\n"
"v_lshlrev_b32 v20,16,v242\n"
"v_and_b32 v21,0xffff0000,v242\n"
"v_lshlrev_b32 v22,16,v243\n"
"v_and_b32 v23,0xffff0000,v243\n"
"v_lshlrev_b32 v24,16,v244\n"
"v_and_b32 v25,0xffff0000,v244\n"
"v_lshlrev_b32 v26,16,v245\n"
"v_and_b32 v27,0xffff0000,v245\n"
"v_lshlrev_b32 v28,16,v246\n"
"v_and_b32 v29,0xffff0000,v246\n"
"v_lshlrev_b32 v30,16,v247\n"
"v_and_b32 v31,0xffff0000,v247\n"
"v_pk_add_f32 v[16:17],v[16:17],v[0:1]\n"
"v_pk_add_f32 v[18:19],v[18:19],v[2:3]\n"
"v_pk_add_f32 v[20:21],v[20:21],v[4:5]\n"
"v_pk_add_f32 v[22:23],v[22:23],v[6:7]\n"
"v_pk_add_f32 v[24:25],v[24:25],v[8:9]\n"
"v_pk_add_f32 v[26:27],v[26:27],v[10:11]\n"
"v_pk_add_f32 v[28:29],v[28:29],v[12:13]\n"
"v_pk_add_f32 v[30:31],v[30:31],v[14:15]\n"
"v_cvt_pk_bf16_f32 v240,v16,v17\n"
"v_cvt_pk_bf16_f32 v241,v18,v19\n"
"v_cvt_pk_bf16_f32 v242,v20,v21\n"
"v_cvt_pk_bf16_f32 v243,v22,v23\n"
"v_cvt_pk_bf16_f32 v244,v24,v25\n"
"v_cvt_pk_bf16_f32 v245,v26,v27\n"
"v_cvt_pk_bf16_f32 v246,v28,v29\n"
"v_cvt_pk_bf16_f32 v247,v30,v31\n"
"buffer_store_dwordx4 v[240:243],v112,s[48:51],s61 offen offset:0 sc1\n"
"buffer_store_dwordx4 v[244:247],v112,s[48:51],s61 offen offset:1024 sc1\n"
"s_barrier\n"
"s_lshl_b32 s82,s52,13\n"
"s_add_u32 s82,s82,0\n"
"v_bfe_u32 v41,v112,5,2\n"
"v_lshlrev_b32 v41,13,v41\n"
"v_bfe_u32 v42,v112,4,1\n"
"v_lshl_add_u32 v41,v42,3,v41\n"
"s_lshl_b32 s83,s60,2\n"
"v_mov_b32 v49,s80\n"
"v_mfma_f32_16x16x32_bf16 v[200:203],v[66:69],a[104:107],0\n"
"s_add_u32 s83,s83,149946368\n"
"v_mov_b32 v45,s66\n"
"v_add_u32 v41,0,v41\n"
"v_mfma_f32_16x16x32_bf16 v[204:207],v[64:67],a[104:107],0\n"
"v_mov_b32 v40,s82\n"
"v_mov_b32 v51,s80\n"
"v_mfma_f32_16x16x32_bf16 v[208:211],v[66:69],a[108:111],0\n"
"v_lshlrev_b32 v43,8,v112\n"
"v_mov_b32 v50,0\n"
"v_mfma_f32_16x16x32_bf16 v[212:215],v[64:67],a[108:111],0\n"
"s_mov_b32 s84,0x40000\n"
"v_mov_b32 v44,s65\n"
"s_waitcnt vmcnt(2)\n"
".Lfk_w1:\n"
"v_mfma_f32_16x16x32_bf16 v[0:3],v[66:69],v[160:163],v[200:203]\n"
"v_min3_u32 v36,v160,v161,v162\n"
"v_min3_u32 v37,v163,v164,v165\n"
"v_mfma_f32_16x16x32_bf16 v[4:7],v[64:67],v[160:163],v[204:207]\n"
"v_min3_u32 v36,v36,v166,v167\n"
"v_min3_u32 v37,v37,v168,v169\n"
"v_mfma_f32_16x16x32_bf16 v[8:11],v[66:69],v[164:167],v[208:211]\n"
"v_min3_u32 v36,v36,v170,v171\n"
"v_min3_u32 v37,v37,v172,v173\n"
"v_mfma_f32_16x16x32_bf16 v[12:15],v[64:67],v[164:167],v[212:215]\n"
"v_min3_u32 v36,v36,v174,v175\n"
"v_min3_u32 v37,v37,v176,v177\n"
"v_mfma_f32_16x16x32_bf16 v[0:3],v[66:69],v[168:171],v[0:3]\n"
"v_min3_u32 v36,v36,v178,v179\n"
"v_min3_u32 v37,v37,v180,v181\n"
"v_mfma_f32_16x16x32_bf16 v[4:7],v[64:67],v[168:171],v[4:7]\n"
"v_min3_u32 v36,v36,v182,v183\n"
"v_min3_u32 v37,v37,v184,v185\n"
"v_mfma_f32_16x16x32_bf16 v[8:11],v[66:69],v[172:175],v[8:11]\n"
"v_min3_u32 v36,v36,v186,v187\n"
"v_min3_u32 v37,v37,v188,v189\n"
"v_mfma_f32_16x16x32_bf16 v[12:15],v[64:67],v[172:175],v[12:15]\n"
"v_min3_u32 v36,v36,v190,v191\n"
"v_min_u32 v36,v36,v37\n"
"v_cmp_eq_u32 vcc,0,v36\n"
"v_mfma_f32_16x16x32_bf16 v[0:3],v[66:69],v[176:179],v[0:3]\n"
"s_cbranch_vccz .Lfk_w1_x\n"
"s_mov_b64 exec,vcc\n"
"s_add_u32 s91,s62,2097152\n"
"buffer_load_dwordx4 v[160:163],v126,s[24:27],s91 offen sc1\n"
"buffer_load_dwordx4 v[164:167],v117,s[24:27],s91 offen sc1\n"
"s_add_u32 s91,s91,4194304\n"
"buffer_load_dwordx4 v[168:171],v126,s[24:27],s91 offen sc1\n"
"buffer_load_dwordx4 v[172:175],v117,s[24:27],s91 offen sc1\n"
"s_add_u32 s91,s91,4194304\n"
"buffer_load_dwordx4 v[176:179],v126,s[24:27],s91 offen sc1\n"
"buffer_load_dwordx4 v[180:183],v117,s[24:27],s91 offen sc1\n"
"s_add_u32 s91,s91,4194304\n"
"buffer_load_dwordx4 v[184:187],v126,s[24:27],s91 offen sc1\n"
"buffer_load_dwordx4 v[188:191],v117,s[24:27],s91 offen sc1\n"
"s_mov_b64 exec,-1\n"
"s_sub_u32 s84,s84,1\n"
"s_waitcnt vmcnt(0)\n"
"s_cbranch_scc0 .Lfk_w1\n"
".Lfk_w1_x:\n"
"v_mfma_f32_16x16x32_bf16 v[4:7],v[64:67],v[176:179],v[4:7]\n"
"v_mfma_f32_16x16x32_bf16 v[8:11],v[66:69],v[180:183],v[8:11]\n"
"v_mfma_f32_16x16x32_bf16 v[12:15],v[64:67],v[180:183],v[12:15]\n"
"v_mfma_f32_16x16x32_bf16 v[0:3],v[66:69],v[184:187],v[0:3]\n"
"v_mfma_f32_16x16x32_bf16 v[4:7],v[64:67],v[184:187],v[4:7]\n"
"v_mfma_f32_16x16x32_bf16 v[8:11],v[66:69],v[188:191],v[8:11]\n"
"v_mfma_f32_16x16x32_bf16 v[12:15],v[64:67],v[188:191],v[12:15]\n"
"s_nop 6\n"
"s_call_b64 s[94:95],.Lfk_rm\n"
"s_sub_u32 s82,64511,s60\n"
"s_or_b32 s63,s63,s82\n"
"v_mov_b32 v46,s63\n"
"v_mov_b32 v47,s64\n"
"ds_write_b128 v40,v[44:47]\n"
"s_waitcnt vmcnt(0) lgkmcnt(0)\n"
"s_barrier\n"
"ds_read_b64 v[52:53],v41\n"
"s_waitcnt lgkmcnt(0)\n"
"v_mov_b32 v48,v52\n"
"s_nop 1\n"
"v_min_u32_dpp v54,v48,v48 quad_perm:[1,0,3,2] row_mask:0xf bank_mask:0xf\n"
"v_max_u32_dpp v48,v48,v48 quad_perm:[1,0,3,2] row_mask:0xf bank_mask:0xf\n"
"v_max_u32_dpp v50,v50,v50 quad_perm:[1,0,3,2] row_mask:0xf bank_mask:0xf\n"
"v_max_u32 v50,v50,v54\n"
"v_min_u32_dpp v54,v48,v48 quad_perm:[2,3,0,1] row_mask:0xf bank_mask:0xf\n"
"v_max_u32_dpp v48,v48,v48 quad_perm:[2,3,0,1] row_mask:0xf bank_mask:0xf\n"
"v_max_u32_dpp v50,v50,v50 quad_perm:[2,3,0,1] row_mask:0xf bank_mask:0xf\n"
"v_max_u32 v50,v50,v54\n"
"v_min_u32_dpp v54,v48,v48 row_half_mirror row_mask:0xf bank_mask:0xf\n"
"v_max_u32_dpp v48,v48,v48 row_half_mirror row_mask:0xf bank_mask:0xf\n"
"v_max_u32_dpp v50,v50,v50 row_half_mirror row_mask:0xf bank_mask:0xf\n"
"v_max_u32 v50,v50,v54\n"
"s_cmp_lg_u32 s52,0\n"
"s_cbranch_scc1 .Lfk_t1\n"
"s_mov_b64 exec,0xff\n"
"buffer_store_dwordx4 v[48:51],v43,s[24:27],s83 offen sc1\n"
"s_mov_b64 exec,-1\n"
"v_cmp_eq_u32 vcc,v48,v52\n"
"s_nop 0\n"
"s_ff1_i32_b64 s82,vcc\n"
"v_readlane_b32 s90,v53,s82\n"
"s_nop 1\n"
"v_mov_b32 v44,s90\n"
"v_mov_b32 v45,s80\n"
"v_cmp_eq_u32 vcc,v50,v52\n"
"s_nop 0\n"
"s_ff1_i32_b64 s82,vcc\n"
"v_readlane_b32 s90,v53,s82\n"
"s_nop 1\n"
"v_mov_b32 v46,s90\n"
"v_mov_b32 v47,s80\n"
"s_mov_b64 exec,0x100\n"
"buffer_store_dwordx4 v[44:47],v43,s[24:27],s83 offen sc1\n"
"s_mov_b64 exec,-1\n"
".Lfk_t1:\n"
"v_readfirstlane_b32 s89,v50\n"
"s_add_u32 m0,s30,131072\n"
"s_nop 0\n"
"buffer_load_dwordx4 v96,s[40:43],s77 offen offset:0 nt lds\n"
"buffer_load_dwordx4 v96,s[40:43],s77 offen offset:1024 nt lds\n"
"buffer_load_dwordx4 v97,s[40:43],s77 offen offset:2048 nt lds\n"
"buffer_load_dwordx4 v97,s[40:43],s77 offen offset:3072 nt lds\n"
"s_add_u32 s77,s77,4096\n"
"s_add_u32 m0,s30,135168\n"
"s_nop 0\n"
"buffer_load_dwordx4 v98,s[40:43],s77 offen offset:0 nt lds\n"
"buffer_load_dwordx4 v98,s[40:43],s77 offen offset:1024 nt lds\n"
"buffer_load_dwordx4 v99,s[40:43],s77 offen offset:2048 nt lds\n"
"buffer_load_dwordx4 v99,s[40:43],s77 offen offset:3072 nt lds\n"
"v_lshlrev_b32 v34,16,s64\n"
"v_mov_b32 v35,s64\n"
"v_rcp_f32 v34,v34\n"
"v_and_b32 v35,0xffff0000,v35\n"
"v_cvt_pk_bf16_f32 v248,v0,v1\n"
"v_cvt_pk_bf16_f32 v249,v2,v3\n"
"v_cvt_pk_bf16_f32 v250,v4,v5\n"
"v_cvt_pk_bf16_f32 v251,v6,v7\n"
"v_cvt_pk_bf16_f32 v252,v8,v9\n"
"v_cvt_pk_bf16_f32 v253,v10,v11\n"
"v_cvt_pk_bf16_f32 v254,v12,v13\n"
"v_cvt_pk_bf16_f32 v255,v14,v15\n"
"v_lshlrev_b32 v0,16,v248\n"
"v_and_b32 v1,0xffff0000,v248\n"
"v_lshlrev_b32 v2,16,v249\n"
"v_and_b32 v3,0xffff0000,v249\n"
"v_lshlrev_b32 v4,16,v250\n"
"v_and_b32 v5,0xffff0000,v250\n"
"v_lshlrev_b32 v6,16,v251\n"
"v_and_b32 v7,0xffff0000,v251\n"
"v_lshlrev_b32 v8,16,v252\n"
"v_and_b32 v9,0xffff0000,v252\n"
"v_lshlrev_b32 v10,16,v253\n"
"v_and_b32 v11,0xffff0000,v253\n"
"v_lshlrev_b32 v12,16,v254\n"
"v_and_b32 v13,0xffff0000,v254\n"
"v_lshlrev_b32 v14,16,v255\n"
"v_and_b32 v15,0xffff0000,v255\n"
"v_pk_add_f32 v[16:17],v[0:1],v[34:35] op_sel:[0,1] neg_lo:[0,1] neg_hi:[0,1]\n"
"v_pk_add_f32 v[18:19],v[2:3],v[34:35] op_sel:[0,1] neg_lo:[0,1] neg_hi:[0,1]\n"
"v_pk_add_f32 v[20:21],v[4:5],v[34:35] op_sel:[0,1] neg_lo:[0,1] neg_hi:[0,1]\n"
"v_pk_add_f32 v[22:23],v[6:7],v[34:35] op_sel:[0,1] neg_lo:[0,1] neg_hi:[0,1]\n"
"v_pk_add_f32 v[24:25],v[8:9],v[34:35] op_sel:[0,1] neg_lo:[0,1] neg_hi:[0,1]\n"
"v_pk_add_f32 v[26:27],v[10:11],v[34:35] op_sel:[0,1] neg_lo:[0,1] neg_hi:[0,1]\n"
"v_pk_add_f32 v[28:29],v[12:13],v[34:35] op_sel:[0,1] neg_lo:[0,1] neg_hi:[0,1]\n"
"v_pk_add_f32 v[30:31],v[14:15],v[34:35] op_sel:[0,1] neg_lo:[0,1] neg_hi:[0,1]\n"
"v_cvt_pk_bf16_f32 v248,v16,v17\n"
"v_cvt_pk_bf16_f32 v249,v18,v19\n"
"v_cvt_pk_bf16_f32 v250,v20,v21\n"
"v_cvt_pk_bf16_f32 v251,v22,v23\n"
"v_cvt_pk_bf16_f32 v252,v24,v25\n"
"v_cvt_pk_bf16_f32 v253,v26,v27\n"
"v_cvt_pk_bf16_f32 v254,v28,v29\n"
"v_cvt_pk_bf16_f32 v255,v30,v31\n"
"v_lshlrev_b32 v16,16,v248\n"
"v_and_b32 v17,0xffff0000,v248\n"
"v_lshlrev_b32 v18,16,v249\n"
"v_and_b32 v19,0xffff0000,v249\n"
"v_lshlrev_b32 v20,16,v250\n"
"v_and_b32 v21,0xffff0000,v250\n"
"v_lshlrev_b32 v22,16,v251\n"
"v_and_b32 v23,0xffff0000,v251\n"
"v_lshlrev_b32 v24,16,v252\n"
"v_and_b32 v25,0xffff0000,v252\n"
"v_lshlrev_b32 v26,16,v253\n"
"v_and_b32 v27,0xffff0000,v253\n"
"v_lshlrev_b32 v28,16,v254\n"
"v_and_b32 v29,0xffff0000,v254\n"
"v_lshlrev_b32 v30,16,v255\n"
"v_and_b32 v31,0xffff0000,v255\n"
"v_pk_mul_f32 v[16:17],v[16:17],v[34:35] op_sel_hi:[1,0]\n"
"v_pk_mul_f32 v[18:19],v[18:19],v[34:35] op_sel_hi:[1,0]\n"
"v_pk_mul_f32 v[20:21],v[20:21],v[34:35] op_sel_hi:[1,0]\n"
"v_pk_mul_f32 v[22:23],v[22:23],v[34:35] op_sel_hi:[1,0]\n"
"v_pk_mul_f32 v[24:25],v[24:25],v[34:35] op_sel_hi:[1,0]\n"
"v_pk_mul_f32 v[26:27],v[26:27],v[34:35] op_sel_hi:[1,0]\n"
"v_pk_mul_f32 v[28:29],v[28:29],v[34:35] op_sel_hi:[1,0]\n"
"v_pk_mul_f32 v[30:31],v[30:31],v[34:35] op_sel_hi:[1,0]\n"
"v_cvt_pk_bf16_f32 v248,v16,v17\n"
"v_cvt_pk_bf16_f32 v249,v18,v19\n"
"v_cvt_pk_bf16_f32 v250,v20,v21\n"
"v_cvt_pk_bf16_f32 v251,v22,v23\n"
"v_cvt_pk_bf16_f32 v252,v24,v25\n"
"v_cvt_pk_bf16_f32 v253,v26,v27\n"
"v_cvt_pk_bf16_f32 v254,v28,v29\n"
"v_cvt_pk_bf16_f32 v255,v30,v31\n"
"v_lshlrev_b32 v16,16,v248\n"
"v_and_b32 v17,0xffff0000,v248\n"
"v_lshlrev_b32 v18,16,v249\n"
"v_and_b32 v19,0xffff0000,v249\n"
"v_lshlrev_b32 v20,16,v250\n"
"v_and_b32 v21,0xffff0000,v250\n"
"v_lshlrev_b32 v22,16,v251\n"
"v_and_b32 v23,0xffff0000,v251\n"
"v_lshlrev_b32 v24,16,v252\n"
"v_and_b32 v25,0xffff0000,v252\n"
"v_lshlrev_b32 v26,16,v253\n"
"v_and_b32 v27,0xffff0000,v253\n"
"v_lshlrev_b32 v28,16,v254\n"
"v_and_b32 v29,0xffff0000,v254\n"
"v_lshlrev_b32 v30,16,v255\n"
"v_and_b32 v31,0xffff0000,v255\n"
"v_pk_add_f32 v[16:17],v[16:17],v[0:1]\n"
"v_pk_add_f32 v[18:19],v[18:19],v[2:3]\n"
"v_pk_add_f32 v[20:21],v[20:21],v[4:5]\n"
"v_pk_add_f32 v[22:23],v[22:23],v[6:7]\n"
"v_pk_add_f32 v[24:25],v[24:25],v[8:9]\n"
"v_pk_add_f32 v[26:27],v[26:27],v[10:11]\n"
"v_pk_add_f32 v[28:29],v[28:29],v[12:13]\n"
"v_pk_add_f32 v[30:31],v[30:31],v[14:15]\n"
"v_cvt_pk_bf16_f32 v248,v16,v17\n"
"v_cvt_pk_bf16_f32 v249,v18,v19\n"
"v_cvt_pk_bf16_f32 v250,v20,v21\n"
"v_cvt_pk_bf16_f32 v251,v22,v23\n"
"v_cvt_pk_bf16_f32 v252,v24,v25\n"
"v_cvt_pk_bf16_f32 v253,v26,v27\n"
"v_cvt_pk_bf16_f32 v254,v28,v29\n"
"v_cvt_pk_bf16_f32 v255,v30,v31\n"
"s_add_u32 s68,s61,2097152\n"
"s_cmp_lt_u32 s63,s89\n"
"s_cselect_b32 s83,s68,2146435072\n"
"s_cselect_b32 s68,2146435072,s68\n"
"buffer_store_dwordx4 v[248:251],v112,s[48:51],s83 offen offset:0 sc1\n"
"buffer_store_dwordx4 v[252:255],v112,s[48:51],s83 offen offset:1024 sc1\n"
"v_lshrrev_b32 v40,6,v112\n"
"v_and_b32 v41,7,v40\n"
"v_lshlrev_b32 v41,5,v41\n"
"v_bfe_u32 v42,v112,4,2\n"
"v_lshlrev_b32 v42,3,v42\n"
"v_lshl_add_u32 v40,v40,11,v42\n"
"s_lshl_b32 s82,s52,9\n"
"v_add_u32 v40,s82,v40\n"
"v_add_u32 v40,131072,v40\n"
"v_xor_b32 v42,0,v41\n"
"v_add_u32 v192,v40,v42\n"
"v_xor_b32 v42,32,v41\n"
"v_add_u32 v193,v40,v42\n"
"v_xor_b32 v42,64,v41\n"
"v_add_u32 v194,v40,v42\n"
"v_xor_b32 v42,96,v41\n"
"v_add_u32 v195,v40,v42\n"
"v_xor_b32 v42,128,v41\n"
"v_add_u32 v196,v40,v42\n"
"v_xor_b32 v42,160,v41\n"
"v_add_u32 v197,v40,v42\n"
"v_xor_b32 v42,192,v41\n"
"v_add_u32 v198,v40,v42\n"
"v_xor_b32 v42,224,v41\n"
"v_add_u32 v199,v40,v42\n"
"v_bfe_u32 v56,v112,4,1\n"
"v_lshrrev_b32 v57,1,v112\n"
"v_and_b32 v57,0x1f0,v57\n"
"s_lshl_b32 s83,s52,13\n"
"s_add_u32 s83,s83,1024\n"
"v_lshl_add_u32 v58,v56,9,s83\n"
"v_lshrrev_b32 v59,8,v112\n"
"v_lshl_add_u32 v59,v59,3,v58\n"
"v_add_u32 v58,v58,v57\n"
"v_add_u32 v57,s82,v57\n"
"v_and_b32 v55,0x7c0,v57\n"
"v_and_b32 v54,0x30,v57\n"
"v_lshl_add_u32 v55,v55,4,v54\n"
"v_lshrrev_b32 v54,2,v55\n"
"v_and_b32 v54,0x0,v54\n"
"v_or_b32 v55,v55,v54\n"
"v_add_u32 v60,s83,v112\n"
"v_add_u32 v61,1024,v112\n"
"v_bfe_u32 v62,v112,5,3\n"
"v_cmp_eq_u32 s[8:9],0,v62\n"
"v_cmp_eq_u32 s[10:11],1,v62\n"
"v_cmp_eq_u32 s[12:13],2,v62\n"
"v_cmp_eq_u32 s[14:15],3,v62\n"
"v_cmp_eq_u32 s[16:17],4,v62\n"
"v_cmp_eq_u32 s[18:19],5,v62\n"
"v_cmp_eq_u32 s[20:21],6,v62\n"
"v_cmp_eq_u32 s[22:23],7,v62\n"
"v_lshlrev_b32 v62,6,v62\n"
"v_lshl_add_u32 v62,v56,12,v62\n"
"v_lshrrev_b32 v63,8,v112\n"
"v_lshl_add_u32 v62,v63,4,v62\n"
"s_lshl_b32 s69,s72,13\n"
"s_lshl_b32 s82,s52,10\n"
"s_add_u32 s69,s69,s82\n"
"s_add_u32 s69,s69,171966464\n"
"v_lshrrev_b32 v63,5,v112\n"
"v_min_u32 v63,27,v63\n"
"v_lshlrev_b32 v63,13,v63\n"
"v_lshl_add_u32 v63,v56,4,v63\n"
"s_mul_i32 s70,s52,458752\n"
"s_lshl_b32 s82,s73,5\n"
"s_add_u32 s70,s70,s82\n"
"s_add_u32 s70,s70,171966464\n"
"s_add_u32 s71,s70,229376\n"
"s_and_b32 s83,s100,7\n"
"s_lshl_b32 s83,s83,12\n"
"s_add_u32 s83,s83,149946368\n"
"v_add_u32 v20,0,v112\n"
"v_lshlrev_b32 v20,0,v20\n"
"v_add_u32 v21,1024,v112\n"
"v_lshlrev_b32 v21,0,v21\n"
"v_add_u32 v22,2048,v112\n"
"v_lshlrev_b32 v22,0,v22\n"
"v_add_u32 v23,3072,v112\n"
"v_lshlrev_b32 v23,0,v23\n"
"s_sleep 16\n"
".Lfk_nn:\n"
"s_mov_b32 s84,0x40000\n"
"buffer_load_dwordx4 v[0:3],v20,s[24:27],s83 offen sc1\n"
"buffer_load_dwordx4 v[4:7],v21,s[24:27],s83 offen sc1\n"
"buffer_load_dwordx4 v[8:11],v22,s[24:27],s83 offen sc1\n"
"buffer_load_dwordx4 v[12:15],v23,s[24:27],s83 offen sc1\n"
"s_waitcnt vmcnt(0)\n"
".Lfk_wc:\n"
"v_xor_b32 v24,s80,v1\n"
"v_xor_b32 v25,s80,v3\n"
"v_xor_b32 v26,s80,v5\n"
"v_xor_b32 v27,s80,v7\n"
"v_xor_b32 v28,s80,v9\n"
"v_xor_b32 v29,s80,v11\n"
"v_xor_b32 v30,s80,v13\n"
"v_xor_b32 v31,s80,v15\n"
"v_or3_b32 v16,v24,v25,v26\n"
"v_or3_b32 v17,v27,v28,v29\n"
"v_or3_b32 v16,v16,v30,v31\n"
"v_or_b32 v16,v16,v17\n"
"v_cmp_ne_u32 vcc,0,v16\n"
"s_nop 0\n"
"s_cbranch_vccz .Lfk_wc_x\n"
"s_sleep 2\n"
"s_mov_b64 exec,vcc\n"
"buffer_load_dwordx4 v[0:3],v20,s[24:27],s83 offen sc1\n"
"buffer_load_dwordx4 v[4:7],v21,s[24:27],s83 offen sc1\n"
"buffer_load_dwordx4 v[8:11],v22,s[24:27],s83 offen sc1\n"
"buffer_load_dwordx4 v[12:15],v23,s[24:27],s83 offen sc1\n"
"s_mov_b64 exec,-1\n"
"s_sub_u32 s84,s84,1\n"
"s_waitcnt vmcnt(0)\n"
"s_cbranch_scc0 .Lfk_wc\n"
".Lfk_wc_x:\n"
"v_min_u32 v16,v0,v4\n"
"v_max_u32 v0,v0,v4\n"
"v_max3_u32 v2,v2,v6,v16\n"
"v_min_u32 v16,v8,v12\n"
"v_max_u32 v8,v8,v12\n"
"v_max3_u32 v10,v10,v14,v16\n"
"v_min_u32 v16,v0,v8\n"
"v_max_u32 v0,v0,v8\n"
"v_max3_u32 v2,v2,v10,v16\n"
"v_cmp_eq_u32 vcc,0,v56\n"
"v_min_u32_dpp v16,v0,v0 quad_perm:[1,0,3,2] row_mask:0xf bank_mask:0xf\n"
"v_max_u32_dpp v0,v0,v0 quad_perm:[1,0,3,2] row_mask:0xf bank_mask:0xf\n"
"v_max_u32_dpp v2,v2,v2 quad_perm:[1,0,3,2] row_mask:0xf bank_mask:0xf\n"
"v_max_u32 v2,v2,v16\n"
"v_min_u32_dpp v16,v0,v0 quad_perm:[2,3,0,1] row_mask:0xf bank_mask:0xf\n"
"v_max_u32_dpp v0,v0,v0 quad_perm:[2,3,0,1] row_mask:0xf bank_mask:0xf\n"
"v_max_u32_dpp v2,v2,v2 quad_perm:[2,3,0,1] row_mask:0xf bank_mask:0xf\n"
"v_max_u32 v2,v2,v16\n"
"v_min_u32_dpp v16,v0,v0 row_half_mirror row_mask:0xf bank_mask:0xf\n"
"v_max_u32_dpp v0,v0,v0 row_half_mirror row_mask:0xf bank_mask:0xf\n"
"v_max_u32_dpp v2,v2,v2 row_half_mirror row_mask:0xf bank_mask:0xf\n"
"v_max_u32 v2,v2,v16\n"
"v_min_u32_dpp v16,v0,v0 row_mirror row_mask:0xf bank_mask:0xf\n"
"v_max_u32_dpp v0,v0,v0 row_mirror row_mask:0xf bank_mask:0xf\n"
"v_max_u32_dpp v2,v2,v2 row_mirror row_mask:0xf bank_mask:0xf\n"
"v_max_u32 v2,v2,v16\n"
"v_min_u32_dpp v16,v0,v0 row_bcast:15 row_mask:0xa bank_mask:0xf\n"
"v_max_u32_dpp v0,v0,v0 row_bcast:15 row_mask:0xa bank_mask:0xf\n"
"v_max_u32_dpp v2,v2,v2 row_bcast:15 row_mask:0xa bank_mask:0xf\n"
"v_max_u32 v2,v2,v16\n"
"v_min_u32_dpp v16,v0,v0 row_bcast:31 row_mask:0xc bank_mask:0xf\n"
"v_max_u32_dpp v0,v0,v0 row_bcast:31 row_mask:0xc bank_mask:0xf\n"
"v_max_u32_dpp v2,v2,v2 row_bcast:31 row_mask:0xc bank_mask:0xf\n"
"v_max_u32 v2,v2,v16\n"
"v_readlane_b32 s90,v0,63\n"
"v_readlane_b32 s92,v2,63\n"
"s_nop 0\n"
"v_mov_b32 v4,s90\n"
"v_mov_b32 v5,s92\n"
"v_cndmask_b32 v6,v5,v4,vcc\n"
"v_not_b32 v7,v6\n"
"v_and_b32 v7,0x7ff,v7\n"
"v_lshl_add_u32 v8,v7,11,v57\n"
"v_and_b32 v9,15,v7\n"
"v_lshlrev_b32 v9,6,v9\n"
"v_xor_b32 v9,v9,v55\n"
"v_and_b32 v10,0x7f0,v7\n"
"v_lshl_add_u32 v10,v10,11,v9\n"
"s_mov_b32 s82,71303168\n"
"buffer_load_dwordx4 a[0:3],v10,s[24:27],s82 offen sc1\n"
"s_add_u32 s82,s82,4194304\n"
"buffer_load_dwordx4 a[4:7],v10,s[24:27],s82 offen sc1\n"
"s_add_u32 s82,s82,4194304\n"
"buffer_load_dwordx4 a[8:11],v10,s[24:27],s82 offen sc1\n"
"s_add_u32 s82,s82,4194304\n"
"buffer_load_dwordx4 a[12:15],v10,s[24:27],s82 offen sc1\n"
"buffer_load_dwordx4 a[16:19],v8,s[36:39],0 offen\n"
"v_bfe_u32 v9,v7,2,8\n"
"v_lshlrev_b32 v9,4,v9\n"
"buffer_load_dwordx4 v[12:15],v9,s[24:27],s83 offen sc1\n"
"s_mov_b32 s82,149979136\n"
"buffer_load_dwordx4 v[16:19],v9,s[24:27],s82 offen sc1\n"
"s_barrier\n"
"ds_read_b64_tr_b16 a[224:225],v192 offset:0\n"
"ds_read_b64_tr_b16 a[240:241],v192 offset:256\n"
"ds_read_b64_tr_b16 a[226:227],v193 offset:0\n"
"ds_read_b64_tr_b16 a[242:243],v193 offset:256\n"
"ds_read_b64_tr_b16 a[228:229],v194 offset:0\n"
"ds_read_b64_tr_b16 a[244:245],v194 offset:256\n"
"ds_read_b64_tr_b16 a[230:231],v195 offset:0\n"
"ds_read_b64_tr_b16 a[246:247],v195 offset:256\n"
"ds_read_b64_tr_b16 a[232:233],v196 offset:0\n"
"ds_read_b64_tr_b16 a[248:249],v196 offset:256\n"
"ds_read_b64_tr_b16 a[234:235],v197 offset:0\n"
"ds_read_b64_tr_b16 a[250:251],v197 offset:256\n"
"ds_read_b64_tr_b16 a[236:237],v198 offset:0\n"
"ds_read_b64_tr_b16 a[252:253],v198 offset:256\n"
"ds_read_b64_tr_b16 a[238:239],v199 offset:0\n"
"ds_read_b64_tr_b16 a[254:255],v199 offset:256\n"
"s_mov_b32 s84,0x40000\n"
"s_waitcnt vmcnt(0)\n"
".Lfk_ws:\n"
"v_xor_b32 v20,s80,v17\n"
"v_xor_b32 v21,s80,v19\n"
"v_or_b32 v20,v20,v21\n"
"v_cmp_ne_u32 vcc,0,v20\n"
"s_nop 0\n"
"s_cbranch_vccz .Lfk_ws_x\n"
"s_mov_b64 exec,vcc\n"
"s_mov_b32 s82,149979136\n"
"buffer_load_dwordx4 v[12:15],v9,s[24:27],s83 offen sc1\n"
"buffer_load_dwordx4 v[16:19],v9,s[24:27],s82 offen sc1\n"
"s_mov_b64 exec,-1\n"
"s_sub_u32 s84,s84,1\n"
"s_waitcnt vmcnt(0)\n"
"s_cbranch_scc0 .Lfk_ws\n"
".Lfk_ws_x:\n"
"v_mfma_f32_16x16x32_bf16 v[24:27],v[66:69],a[0:3],0\n"
"v_mfma_f32_16x16x32_bf16 v[28:31],v[64:67],a[0:3],0\n"
"v_mfma_f32_16x16x32_bf16 v[24:27],v[66:69],a[4:7],v[24:27]\n"
"v_mfma_f32_16x16x32_bf16 v[28:31],v[64:67],a[4:7],v[28:31]\n"
"v_mfma_f32_16x16x32_bf16 v[24:27],v[66:69],a[8:11],v[24:27]\n"
"v_mfma_f32_16x16x32_bf16 v[28:31],v[64:67],a[8:11],v[28:31]\n"
"v_mfma_f32_16x16x32_bf16 v[24:27],v[66:69],a[12:15],v[24:27]\n"
"v_mfma_f32_16x16x32_bf16 v[28:31],v[64:67],a[12:15],v[28:31]\n"
"v_mfma_f32_16x16x32_bf16 v[24:27],v[66:69],a[16:19],v[24:27]\n"
"v_cmp_eq_u32 vcc,v12,v6\n"
"v_mfma_f32_16x16x32_bf16 v[28:31],v[64:67],a[16:19],v[28:31]\n"
"s_nop 0\n"
"v_cndmask_b32 v10,v18,v16,vcc\n"
"v_lshlrev_b32 v11,16,v10\n"
"v_rcp_f32 v11,v11\n"
"v_and_b32 v10,0xffff0000,v10\n"
"s_nop 0\n"
"v_cvt_pk_bf16_f32 v20,v24,v25\n"
"v_cvt_pk_bf16_f32 v21,v26,v27\n"
"v_lshlrev_b32 v24,16,v20\n"
"v_and_b32 v25,0xffff0000,v20\n"
"v_cvt_pk_bf16_f32 v23,v30,v31\n"
"v_cvt_pk_bf16_f32 v22,v28,v29\n"
"v_lshlrev_b32 v26,16,v21\n"
"v_and_b32 v27,0xffff0000,v21\n"
"v_lshlrev_b32 v28,16,v22\n"
"v_and_b32 v29,0xffff0000,v22\n"
"v_lshlrev_b32 v30,16,v23\n"
"v_and_b32 v31,0xffff0000,v23\n"
"v_pk_add_f32 v[32:33],v[24:25],v[10:11] op_sel_hi:[1,0] neg_lo:[0,1] neg_hi:[0,1]\n"
"v_pk_add_f32 v[34:35],v[26:27],v[10:11] op_sel_hi:[1,0] neg_lo:[0,1] neg_hi:[0,1]\n"
"v_pk_add_f32 v[36:37],v[28:29],v[10:11] op_sel_hi:[1,0] neg_lo:[0,1] neg_hi:[0,1]\n"
"v_pk_add_f32 v[38:39],v[30:31],v[10:11] op_sel_hi:[1,0] neg_lo:[0,1] neg_hi:[0,1]\n"
"v_cvt_pk_bf16_f32 v20,v32,v33\n"
"v_cvt_pk_bf16_f32 v21,v34,v35\n"
"v_cvt_pk_bf16_f32 v22,v36,v37\n"
"v_cvt_pk_bf16_f32 v23,v38,v39\n"
"v_lshlrev_b32 v32,16,v20\n"
"v_and_b32 v33,0xffff0000,v20\n"
"v_lshlrev_b32 v34,16,v21\n"
"v_and_b32 v35,0xffff0000,v21\n"
"v_lshlrev_b32 v36,16,v22\n"
"v_and_b32 v37,0xffff0000,v22\n"
"v_lshlrev_b32 v38,16,v23\n"
"v_and_b32 v39,0xffff0000,v23\n"
"v_pk_mul_f32 v[32:33],v[32:33],v[10:11] op_sel:[0,1]\n"
"v_pk_mul_f32 v[34:35],v[34:35],v[10:11] op_sel:[0,1]\n"
"v_pk_mul_f32 v[36:37],v[36:37],v[10:11] op_sel:[0,1]\n"
"v_pk_mul_f32 v[38:39],v[38:39],v[10:11] op_sel:[0,1]\n"
"v_cvt_pk_bf16_f32 v20,v32,v33\n"
"v_cvt_pk_bf16_f32 v21,v34,v35\n"
"v_cvt_pk_bf16_f32 v22,v36,v37\n"
"v_cvt_pk_bf16_f32 v23,v38,v39\n"
"ds_write_b128 v58,v[20:23]\n"
"ds_read2_b64 v[128:131],v59 offset0:0 offset1:4\n"
"ds_read2_b64 v[132:135],v59 offset0:8 offset1:12\n"
"ds_read2_b64 v[136:139],v59 offset0:16 offset1:20\n"
"ds_read2_b64 v[140:143],v59 offset0:24 offset1:28\n"
"ds_read2_b64 v[144:147],v59 offset0:32 offset1:36\n"
"ds_read2_b64 v[148:151],v59 offset0:40 offset1:44\n"
"ds_read2_b64 v[152:155],v59 offset0:48 offset1:52\n"
"ds_read2_b64 v[156:159],v59 offset0:56 offset1:60\n"
"ds_write_b128 v60,v[24:27] offset:3072\n"
"ds_write_b128 v60,v[28:31] offset:4096\n"
"s_waitcnt lgkmcnt(9)\n"
"v_mfma_f32_16x16x32_bf16 v[40:43],a[160:163],v[128:131],0\n"
"v_mfma_f32_16x16x32_bf16 v[44:47],a[192:195],v[128:131],0\n"
"s_waitcnt lgkmcnt(8)\n"
"v_mfma_f32_16x16x32_bf16 v[40:43],a[164:167],v[132:135],v[40:43]\n"
"v_mfma_f32_16x16x32_bf16 v[44:47],a[196:199],v[132:135],v[44:47]\n"
"s_waitcnt lgkmcnt(7)\n"
"v_mfma_f32_16x16x32_bf16 v[40:43],a[168:171],v[136:139],v[40:43]\n"
"v_mfma_f32_16x16x32_bf16 v[44:47],a[200:203],v[136:139],v[44:47]\n"
"s_waitcnt lgkmcnt(6)\n"
"v_mfma_f32_16x16x32_bf16 v[40:43],a[172:175],v[140:143],v[40:43]\n"
"v_mfma_f32_16x16x32_bf16 v[44:47],a[204:207],v[140:143],v[44:47]\n"
"s_waitcnt lgkmcnt(5)\n"
"v_mfma_f32_16x16x32_bf16 v[40:43],a[176:179],v[144:147],v[40:43]\n"
"v_mfma_f32_16x16x32_bf16 v[44:47],a[208:211],v[144:147],v[44:47]\n"
"s_waitcnt lgkmcnt(4)\n"
"v_mfma_f32_16x16x32_bf16 v[40:43],a[180:183],v[148:151],v[40:43]\n"
"v_mfma_f32_16x16x32_bf16 v[44:47],a[212:215],v[148:151],v[44:47]\n"
"s_waitcnt lgkmcnt(3)\n"
"v_mfma_f32_16x16x32_bf16 v[40:43],a[184:187],v[152:155],v[40:43]\n"
"v_mfma_f32_16x16x32_bf16 v[44:47],a[216:219],v[152:155],v[44:47]\n"
"s_waitcnt lgkmcnt(2)\n"
"v_mfma_f32_16x16x32_bf16 v[40:43],a[188:191],v[156:159],v[40:43]\n"
"v_mfma_f32_16x16x32_bf16 v[44:47],a[220:223],v[156:159],v[44:47]\n"
"s_nop 6\n"
"ds_write_b128 v60,v[40:43] offset:1024\n"
"ds_write_b128 v60,v[44:47] offset:2048\n"
"s_waitcnt lgkmcnt(0)\n"
"s_barrier\n"
"ds_read_b128 v[128:131],v61 offset:1024\n"
"ds_read_b128 v[144:147],v61 offset:2048\n"
"ds_read_b128 v[132:135],v61 offset:9216\n"
"ds_read_b128 v[148:151],v61 offset:10240\n"
"ds_read_b128 v[136:139],v61 offset:17408\n"
"ds_read_b128 v[152:155],v61 offset:18432\n"
"ds_read_b128 v[140:143],v61 offset:25600\n"
"ds_read_b128 v[156:159],v61 offset:26624\n"
"s_waitcnt lgkmcnt(0)\n"
"v_pk_add_f32 v[128:129],v[128:129],v[132:133]\n"
"v_pk_add_f32 v[136:137],v[136:137],v[140:141]\n"
"v_pk_add_f32 v[130:131],v[130:131],v[134:135]\n"
"v_pk_add_f32 v[138:139],v[138:139],v[142:143]\n"
"v_pk_add_f32 v[128:129],v[128:129],v[136:137]\n"
"v_pk_add_f32 v[130:131],v[130:131],v[138:139]\n"
"v_pk_add_f32 v[144:145],v[144:145],v[148:149]\n"
"v_pk_add_f32 v[152:153],v[152:153],v[156:157]\n"
"v_pk_add_f32 v[146:147],v[146:147],v[150:151]\n"
"v_pk_add_f32 v[154:155],v[154:155],v[158:159]\n"
"v_pk_add_f32 v[144:145],v[144:145],v[152:153]\n"
"v_pk_add_f32 v[146:147],v[146:147],v[154:155]\n"
"v_mul_f32 v132,0xbfb8aa3b,v128\n"
"v_mul_f32 v133,0xbfb8aa3b,v129\n"
"v_mul_f32 v134,0xbfb8aa3b,v130\n"
"v_mul_f32 v135,0xbfb8aa3b,v131\n"
"v_exp_f32 v132,v132\n"
"v_exp_f32 v133,v133\n"
"v_exp_f32 v134,v134\n"
"v_exp_f32 v135,v135\n"
"v_add_f32 v132,1.0,v132\n"
"v_add_f32 v133,1.0,v133\n"
"v_add_f32 v134,1.0,v134\n"
"v_add_f32 v135,1.0,v135\n"
"v_rcp_f32 v132,v132\n"
"v_rcp_f32 v133,v133\n"
"v_rcp_f32 v134,v134\n"
"v_rcp_f32 v135,v135\n"
"v_pk_mul_f32 v[128:129],v[128:129],v[132:133]\n"
"v_pk_mul_f32 v[130:131],v[130:131],v[134:135]\n"
"v_pk_mul_f32 v[128:129],v[128:129],v[144:145]\n"
"v_pk_mul_f32 v[130:131],v[130:131],v[146:147]\n"
"v_cvt_pk_bf16_f32 v132,v128,v129\n"
"v_cvt_pk_bf16_f32 v133,v130,v131\n"
"v_cndmask_b32 v136,0,v132,s[8:9]\n"
"v_cndmask_b32 v137,0,v133,s[8:9]\n"
"v_cndmask_b32 v138,0,v132,s[10:11]\n"
"v_cndmask_b32 v139,0,v133,s[10:11]\n"
"v_cndmask_b32 v141,0,v133,s[12:13]\n"
"v_cndmask_b32 v143,0,v133,s[14:15]\n"
"v_mfma_f32_16x16x32_bf16 v[40:43],a[224:227],v[136:139],0\n"
"v_cndmask_b32 v142,0,v132,s[14:15]\n"
"v_cndmask_b32 v140,0,v132,s[12:13]\n"
"v_mfma_f32_16x16x32_bf16 v[44:47],a[240:243],v[136:139],0\n"
"v_cndmask_b32 v145,0,v133,s[16:17]\n"
"v_cndmask_b32 v147,0,v133,s[18:19]\n"
"v_mfma_f32_16x16x32_bf16 v[40:43],a[228:231],v[140:143],v[40:43]\n"
"v_cndmask_b32 v146,0,v132,s[18:19]\n"
"v_cndmask_b32 v144,0,v132,s[16:17]\n"
"v_mfma_f32_16x16x32_bf16 v[44:47],a[244:247],v[140:143],v[44:47]\n"
"v_cndmask_b32 v149,0,v133,s[20:21]\n"
"v_cndmask_b32 v151,0,v133,s[22:23]\n"
"v_mfma_f32_16x16x32_bf16 v[40:43],a[232:235],v[144:147],v[40:43]\n"
"v_cndmask_b32 v150,0,v132,s[22:23]\n"
"v_cndmask_b32 v148,0,v132,s[20:21]\n"
"v_mfma_f32_16x16x32_bf16 v[44:47],a[248:251],v[144:147],v[44:47]\n"
"s_nop 0\n"
"v_mfma_f32_16x16x32_bf16 v[40:43],a[236:239],v[148:151],v[40:43]\n"
"v_mfma_f32_16x16x32_bf16 v[44:47],a[252:255],v[148:151],v[44:47]\n"
"s_cmp_ge_u32 s63,s92\n"
"s_cselect_b32 s68,2146435072,s68\n"
"s_nop 4\n"
"v_or_b32 v40,1,v40\n"
"v_or_b32 v41,1,v41\n"
"v_or_b32 v42,1,v42\n"
"v_or_b32 v43,1,v43\n"
"v_or_b32 v44,1,v44\n"
"v_or_b32 v45,1,v45\n"
"v_or_b32 v46,1,v46\n"
"v_or_b32 v47,1,v47\n"
"buffer_store_dwordx4 v[40:43],v62,s[24:27],s69 offen offset:0 sc1\n"
"buffer_store_dwordx4 v[44:47],v62,s[24:27],s69 offen offset:512 sc1\n"
"buffer_store_dwordx4 v[248:251],v112,s[48:51],s68 offen offset:0 sc1\n"
"buffer_store_dwordx4 v[252:255],v112,s[48:51],s68 offen offset:1024 sc1\n"
"s_cmp_eq_u32 s60,0\n"
"s_cselect_b32 s82,s75,2146435072\n"
"buffer_store_dwordx2 a[154:155],off,s[24:27],s82 sc1\n"
"s_bfe_u32 s82,s73,0x20005\n"
"s_lshl_b32 s82,s82,13\n"
"s_and_b32 s83,s73,31\n"
"s_lshr_b32 s91,s73,7\n"
"s_lshl_b32 s89,s91,9\n"
"s_lshl4_add_u32 s89,s83,s89\n"
"s_add_u32 s89,s89,s82\n"
"s_add_u32 s89,s89,1024\n"
"s_lshl1_add_u32 s83,s83,s91\n"
"s_lshl4_add_u32 s82,s83,s82\n"
"s_add_u32 s82,s82,4096\n"
"v_bfe_u32 v0,v112,4,3\n"
"v_lshl_add_u32 v1,v0,1,s89\n"
"ds_read_u16 v2,v1\n"
"v_and_b32 v3,3,v0\n"
"v_lshl_add_u32 v3,v3,2,s82\n"
"v_lshrrev_b32 v4,2,v0\n"
"v_lshl_add_u32 v3,v4,10,v3\n"
"ds_read_b32 v3,v3\n"
"s_cmp_eq_u32 s91,0\n"
"s_cselect_b32 s82,s90,s92\n"
"s_not_b32 s83,s82\n"
"s_and_b32 s89,s83,0x7ff\n"
"s_lshl_b32 s89,s89,11\n"
"s_and_b32 s91,s73,127\n"
"s_lshl4_add_u32 s89,s91,s89\n"
"s_ashr_i32 s83,s83,31\n"
"s_or_b32 s83,s83,0x80000000\n"
"s_xor_b32 s82,s82,s83\n"
"s_and_b32 s82,s82,0xffff0000\n"
"v_mov_b32 v5,0xbfb8aa3b\n"
"v_mul_f32 v5,s82,v5\n"
"v_exp_f32 v5,v5\n"
"s_nop 0\n"
"v_add_f32 v5,1.0,v5\n"
"v_rcp_f32 v5,v5\n"
"s_nop 0\n"
"v_cvt_pk_bf16_f32 v5,v5,v5\n"
"v_lshlrev_b32 v5,16,v5\n"
"s_lshl_b32 s82,s52,7\n"
"s_add_u32 s82,s82,64\n"
"v_lshrrev_b32 v6,3,v112\n"
"v_and_b32 v6,0x60,v6\n"
"v_and_b32 v7,16,v112\n"
"v_add3_u32 v6,v6,v7,s82\n"
"v_lshrrev_b32 v7,2,v112\n"
"v_add_u32 v7,64,v7\n"
"v_lshlrev_b32 v8,1,v0\n"
"s_waitcnt lgkmcnt(0)\n"
"v_lshlrev_b32 v2,16,v2\n"
"s_sleep 6\n"
"s_mov_b32 s84,0x40000\n"
"buffer_load_dwordx4 v[16:19],v63,s[24:27],s70 offen sc1\n"
"buffer_load_dwordx4 v[20:23],v63,s[24:27],s71 offen sc1\n"
"s_waitcnt vmcnt(0)\n"
".Lfk_wy:\n"
"v_min3_u32 v24,v16,v17,v18\n"
"v_min3_u32 v25,v19,v20,v21\n"
"v_min3_u32 v24,v24,v22,v23\n"
"v_min_u32 v24,v24,v25\n"
"v_cmp_eq_u32 vcc,0,v24\n"
"s_nop 0\n"
"s_cbranch_vccz .Lfk_wy_x\n"
"s_sleep 1\n"
"s_mov_b64 exec,vcc\n"
"buffer_load_dwordx4 v[16:19],v63,s[24:27],s70 offen sc1\n"
"buffer_load_dwordx4 v[20:23],v63,s[24:27],s71 offen sc1\n"
"s_mov_b64 exec,-1\n"
"s_sub_u32 s84,s84,1\n"
"s_waitcnt vmcnt(0)\n"
"s_cbranch_scc0 .Lfk_wy\n"
".Lfk_wy_x:\n"
"s_mov_b32 vcc_lo,-1\n"
"s_mov_b32 vcc_hi,0x00ffffff\n"
"v_add_f32 v16,v16,v20\n"
"v_add_f32 v17,v17,v21\n"
"v_add_f32 v18,v18,v22\n"
"v_add_f32 v19,v19,v23\n"
"v_cndmask_b32 v16,0,v16,vcc\n"
"v_cndmask_b32 v17,0,v17,vcc\n"
"v_cndmask_b32 v18,0,v18,vcc\n"
"v_cndmask_b32 v19,0,v19,vcc\n"
"v_add_f32_dpp v16,v16,v16 quad_perm:[2,3,0,1] row_mask:0xf bank_mask:0xf\n"
"v_add_f32_dpp v17,v17,v17 quad_perm:[2,3,0,1] row_mask:0xf bank_mask:0xf\n"
"v_add_f32_dpp v18,v18,v18 quad_perm:[2,3,0,1] row_mask:0xf bank_mask:0xf\n"
"v_add_f32_dpp v19,v19,v19 quad_perm:[2,3,0,1] row_mask:0xf bank_mask:0xf\n"
"v_add_f32_dpp v16,v16,v16 row_ror:4 row_mask:0xf bank_mask:0xf\n"
"v_add_f32_dpp v17,v17,v17 row_ror:4 row_mask:0xf bank_mask:0xf\n"
"v_add_f32_dpp v18,v18,v18 row_ror:4 row_mask:0xf bank_mask:0xf\n"
"v_add_f32_dpp v19,v19,v19 row_ror:4 row_mask:0xf bank_mask:0xf\n"
"v_add_f32_dpp v16,v16,v16 row_ror:8 row_mask:0xf bank_mask:0xf\n"
"v_add_f32_dpp v17,v17,v17 row_ror:8 row_mask:0xf bank_mask:0xf\n"
"v_add_f32_dpp v18,v18,v18 row_ror:8 row_mask:0xf bank_mask:0xf\n"
"v_add_f32_dpp v19,v19,v19 row_ror:8 row_mask:0xf bank_mask:0xf\n"
"s_mov_b32 exec_lo,0x30003\n"
"s_mov_b32 exec_hi,0x30003\n"
"ds_write_b128 v6,v[16:19]\n"
"s_mov_b64 exec,-1\n"
"s_waitcnt lgkmcnt(0)\n"
"s_barrier\n"
"s_cmp_lg_u32 s52,0\n"
"s_cbranch_scc1 .Lfk_t2\n"
"ds_read2_b32 v[16:17],v7 offset0:0 offset1:64\n"
"s_waitcnt lgkmcnt(0)\n"
"v_add_f32 v16,v16,v17\n"
"s_nop 1\n"
"v_add_f32_dpp v16,v16,v16 row_ror:8 row_mask:0xf bank_mask:0xf\n"
"v_mov_b32 v17,v16\n"
"s_nop 1\n"
"v_permlane16_swap_b32 v17,v16\n"
"v_add_f32 v16,v16,v17\n"
"v_mov_b32 v17,v16\n"
"s_nop 1\n"
"v_permlane32_swap_b32 v17,v16\n"
"v_add_f32 v16,v16,v17\n"
"v_cvt_pk_bf16_f32 v16,v16,v16\n"
"v_lshlrev_b32 v16,16,v16\n"
"v_mul_f32 v16,v16,v5\n"
"v_cvt_pk_bf16_f32 v16,v16,v16\n"
"v_lshlrev_b32 v16,16,v16\n"
"v_add_f32 v16,v16,v2\n"
"v_cvt_pk_bf16_f32 v16,v16,v16\n"
"v_lshlrev_b32 v16,16,v16\n"
"v_add_f32 v16,v16,v3\n"
"s_nop 1\n"
"v_mov_b32_dpp v17,v16 quad_perm:[1,0,3,2] row_mask:0xf bank_mask:0xf\n"
"v_cvt_pk_bf16_f32 v16,v16,v17\n"
"s_mov_b64 exec,0x55\n"
"buffer_store_dword v16,v8,s[48:51],s89 offen sc1\n"
"s_mov_b64 exec,-1\n"
".Lfk_t2:\n"
"s_branch .Lfk_end\n"
".Lfk_g0:\n"
"s_waitcnt vmcnt(12) lgkmcnt(0)\n"
"s_barrier\n"
"v_mfma_f32_16x16x32_bf16 v[32:35],a[96:99],a[0:3],v[96:99]\n"
"s_add_u32 m0,s55,4096\n"
"v_exp_f32 v0,v0\n"
"v_mfma_f32_16x16x32_bf16 v[32:35],a[100:103],a[4:7],v[32:35]\n"
"s_add_u32 s54,s54,s22\n"
"v_exp_f32 v1,v1\n"
"v_mfma_f32_16x16x32_bf16 v[32:35],a[104:107],a[8:11],v[32:35]\n"
"buffer_load_dwordx4 v115,s[32:35],s54 offen sc1 lds\n"
"v_exp_f32 v2,v2\n"
"v_cvt_pk_bf16_f32 v64,v0,v1\n"
"v_mfma_f32_16x16x32_bf16 v[32:35],a[108:111],a[12:15],v[32:35]\n"
"v_exp_f32 v3,v3\n"
"v_mfma_f32_16x16x32_bf16 v[36:39],a[124:127],a[12:15],v[96:99]\n"
"v_exp_f32 v4,v4\n"
"v_cvt_pk_bf16_f32 v65,v2,v3\n"
"v_mfma_f32_16x16x32_bf16 v[36:39],a[120:123],a[8:11],v[36:39]\n"
"v_exp_f32 v5,v5\n"
"v_mfma_f32_16x16x32_bf16 v[36:39],a[116:119],a[4:7],v[36:39]\n"
"v_exp_f32 v6,v6\n"
"v_cvt_pk_bf16_f32 v66,v4,v5\n"
"v_mfma_f32_16x16x32_bf16 v[36:39],a[112:115],a[0:3],v[36:39]\n"
"v_exp_f32 v7,v7\n"
"v_mfma_f32_16x16x32_bf16 v[44:47],a[112:115],a[16:19],v[100:103]\n"
"v_exp_f32 v8,v8\n"
"v_cvt_pk_bf16_f32 v67,v6,v7\n"
"v_exp_f32 v9,v9\n"
"v_mfma_f32_16x16x32_bf16 v[44:47],a[116:119],a[20:23],v[44:47]\n"
"ds_read_b128 a[64:67],v113 offset:49152\n"
"ds_read_b128 a[68:71],v113 offset:50176\n"
"v_exp_f32 v10,v10\n"
"v_mfma_f32_16x16x32_bf16 v[44:47],a[120:123],a[24:27],v[44:47]\n"
"ds_read_b128 a[72:75],v113 offset:51200\n"
"ds_read_b128 a[76:79],v113 offset:52224\n"
"v_exp_f32 v11,v11\n"
"v_mfma_f32_16x16x32_bf16 v[44:47],a[124:127],a[28:31],v[44:47]\n"
"ds_read_b128 a[80:83],v113 offset:53248\n"
"ds_read_b128 a[84:87],v113 offset:54272\n"
"v_exp_f32 v12,v12\n"
"v_mfma_f32_16x16x32_bf16 v[40:43],a[108:111],a[28:31],v[100:103]\n"
"ds_read_b128 a[88:91],v113 offset:55296\n"
"ds_read_b128 a[92:95],v113 offset:56320\n"
"v_mfma_f32_16x16x32_bf16 v[40:43],a[104:107],a[24:27],v[40:43]\n"
"v_exp_f32 v13,v13\n"
"v_cvt_pk_bf16_f32 v68,v8,v9\n"
"v_cvt_pk_bf16_f32 v69,v10,v11\n"
"v_mfma_f32_16x16x32_bf16 v[40:43],a[100:103],a[20:23],v[40:43]\n"
"v_exp_f32 v14,v14\n"
"v_cvt_pk_bf16_f32 v70,v12,v13\n"
"v_mfma_f32_16x16x32_bf16 v[40:43],a[96:99],a[16:19],v[40:43]\n"
"v_exp_f32 v15,v15\n"
"v_mfma_f32_16x16x32_bf16 v[48:51],a[96:99],a[32:35],v[104:107]\n"
"v_exp_f32 v16,v16\n"
"v_cvt_pk_bf16_f32 v71,v14,v15\n"
"v_mfma_f32_16x16x32_bf16 v[48:51],a[100:103],a[36:39],v[48:51]\n"
"v_exp_f32 v17,v17\n"
"v_mfma_f32_16x16x32_bf16 v[48:51],a[104:107],a[40:43],v[48:51]\n"
"v_exp_f32 v18,v18\n"
"v_cvt_pk_bf16_f32 v72,v16,v17\n"
"v_mfma_f32_16x16x32_bf16 v[48:51],a[108:111],a[44:47],v[48:51]\n"
"v_exp_f32 v19,v19\n"
"v_mfma_f32_16x16x32_bf16 v[52:55],a[124:127],a[44:47],v[104:107]\n"
"v_exp_f32 v20,v20\n"
"v_cvt_pk_bf16_f32 v73,v18,v19\n"
"v_mfma_f32_16x16x32_bf16 v[52:55],a[120:123],a[40:43],v[52:55]\n"
"v_exp_f32 v21,v21\n"
"v_mfma_f32_16x16x32_bf16 v[52:55],a[116:119],a[36:39],v[52:55]\n"
"v_exp_f32 v22,v22\n"
"v_cvt_pk_bf16_f32 v74,v20,v21\n"
"v_mfma_f32_16x16x32_bf16 v[52:55],a[112:115],a[32:35],v[52:55]\n"
"v_exp_f32 v23,v23\n"
"v_mfma_f32_16x16x32_bf16 v[60:63],a[112:115],a[48:51],v[108:111]\n"
"v_exp_f32 v24,v24\n"
"v_cvt_pk_bf16_f32 v75,v22,v23\n"
"v_mfma_f32_16x16x32_bf16 v[60:63],a[116:119],a[52:55],v[60:63]\n"
"v_exp_f32 v25,v25\n"
"v_mfma_f32_16x16x32_bf16 v[60:63],a[120:123],a[56:59],v[60:63]\n"
"v_exp_f32 v26,v26\n"
"v_exp_f32 v27,v27\n"
"v_mfma_f32_16x16x32_bf16 v[60:63],a[124:127],a[60:63],v[60:63]\n"
"buffer_store_dwordx4 a[228:231],v112,s[24:27],s56 offen offset:1024 sc1\n"
"v_mfma_f32_16x16x32_bf16 v[56:59],a[108:111],a[60:63],v[108:111]\n"
"v_exp_f32 v28,v28\n"
"v_cvt_pk_bf16_f32 v77,v26,v27\n"
"v_mfma_f32_16x16x32_bf16 v[56:59],a[104:107],a[56:59],v[56:59]\n"
"v_exp_f32 v29,v29\n"
"v_mfma_f32_16x16x32_bf16 v[56:59],a[100:103],a[52:55],v[56:59]\n"
"v_exp_f32 v30,v30\n"
"v_cvt_pk_bf16_f32 v78,v28,v29\n"
"v_mfma_f32_16x16x32_bf16 v[56:59],a[96:99],a[48:51],v[56:59]\n"
"s_waitcnt vmcnt(10) lgkmcnt(0)\n"
"v_exp_f32 v31,v31\n"
"v_cvt_pk_bf16_f32 v76,v24,v25\n"
"v_cvt_pk_bf16_f32 v79,v30,v31\n"
"s_barrier\n"
"v_mfma_f32_16x16x32_bf16 v[0:3],a[64:67],a[0:3],v[96:99]\n"
"s_add_u32 m0,s55,8192\n"
"v_exp_f32 v122,v32\n"
"v_mfma_f32_16x16x32_bf16 v[0:3],a[68:71],a[4:7],v[0:3]\n"
"s_add_u32 s54,s54,s22\n"
"v_exp_f32 v123,v33\n"
"v_mfma_f32_16x16x32_bf16 v[0:3],a[72:75],a[8:11],v[0:3]\n"
"ds_read_b128 a[96:99],v114 offset:40960\n"
"ds_read_b128 a[100:103],v114 offset:41984\n"
"v_mfma_f32_16x16x32_bf16 v[0:3],a[76:79],a[12:15],v[0:3]\n"
"v_exp_f32 v119,v34\n"
"v_mfma_f32_16x16x32_bf16 v[4:7],a[92:95],a[12:15],v[96:99]\n"
"ds_read_b128 a[104:107],v114 offset:43008\n"
"v_cvt_pk_bf16_f32 v80,v122,v123\n"
"v_mfma_f32_16x16x32_bf16 v[4:7],a[88:91],a[8:11],v[4:7]\n"
"v_exp_f32 v120,v35\n"
"v_mfma_f32_16x16x32_bf16 v[4:7],a[84:87],a[4:7],v[4:7]\n"
"ds_read_b128 a[108:111],v114 offset:44032\n"
"ds_read_b128 a[112:115],v114 offset:45056\n"
"v_mfma_f32_16x16x32_bf16 v[4:7],a[80:83],a[0:3],v[4:7]\n"
"v_exp_f32 v122,v36\n"
"v_mfma_f32_16x16x32_bf16 v[12:15],a[80:83],a[16:19],v[100:103]\n"
"ds_read_b128 a[116:119],v114 offset:46080\n"
"v_cvt_pk_bf16_f32 v81,v119,v120\n"
"v_mfma_f32_16x16x32_bf16 v[12:15],a[84:87],a[20:23],v[12:15]\n"
"v_exp_f32 v123,v37\n"
"v_mfma_f32_16x16x32_bf16 v[12:15],a[88:91],a[24:27],v[12:15]\n"
"ds_read_b128 a[120:123],v114 offset:47104\n"
"ds_read_b128 a[124:127],v114 offset:48128\n"
"v_mfma_f32_16x16x32_bf16 v[12:15],a[92:95],a[28:31],v[12:15]\n"
"v_exp_f32 v119,v38\n"
"v_cvt_pk_bf16_f32 v82,v122,v123\n"
"v_mfma_f32_16x16x32_bf16 v[8:11],a[76:79],a[28:31],v[100:103]\n"
"buffer_store_dwordx4 a[168:171],v112,s[24:27],s3 offen offset:2048 sc1\n"
"v_mfma_f32_16x16x32_bf16 v[8:11],a[72:75],a[24:27],v[8:11]\n"
"v_exp_f32 v120,v39\n"
"v_mfma_f32_16x16x32_bf16 v[8:11],a[68:71],a[20:23],v[8:11]\n"
"buffer_load_dwordx4 v115,s[32:35],s54 offen sc1 lds\n"
"v_mfma_f32_16x16x32_bf16 v[8:11],a[64:67],a[16:19],v[8:11]\n"
"v_exp_f32 v122,v40\n"
"v_mfma_f32_16x16x32_bf16 v[16:19],a[64:67],a[32:35],v[104:107]\n"
"v_cvt_pk_bf16_f32 v83,v119,v120\n"
"v_or3_b32 v118,v80,v81,v82\n"
"v_mfma_f32_16x16x32_bf16 v[16:19],a[68:71],a[36:39],v[16:19]\n"
"v_exp_f32 v123,v41\n"
"v_mfma_f32_16x16x32_bf16 v[16:19],a[72:75],a[40:43],v[16:19]\n"
"v_cvt_pk_bf16_f32 v84,v122,v123\n"
"v_or3_b32 v118,v118,v83,v84\n"
"v_mfma_f32_16x16x32_bf16 v[16:19],a[76:79],a[44:47],v[16:19]\n"
"v_exp_f32 v119,v42\n"
"v_mfma_f32_16x16x32_bf16 v[20:23],a[92:95],a[44:47],v[104:107]\n"
"v_mfma_f32_16x16x32_bf16 v[20:23],a[88:91],a[40:43],v[20:23]\n"
"v_exp_f32 v120,v43\n"
"v_mfma_f32_16x16x32_bf16 v[20:23],a[84:87],a[36:39],v[20:23]\n"
"v_cvt_pk_bf16_f32 v85,v119,v120\n"
"v_mfma_f32_16x16x32_bf16 v[20:23],a[80:83],a[32:35],v[20:23]\n"
"v_exp_f32 v122,v44\n"
"v_mfma_f32_16x16x32_bf16 v[28:31],a[80:83],a[48:51],v[108:111]\n"
"buffer_store_dwordx4 a[172:175],v112,s[24:27],s3 offen offset:3072 sc1\n"
"v_mfma_f32_16x16x32_bf16 v[28:31],a[84:87],a[52:55],v[28:31]\n"
"v_exp_f32 v123,v45\n"
"v_mfma_f32_16x16x32_bf16 v[28:31],a[88:91],a[56:59],v[28:31]\n"
"s_sub_u32 s3,s3,16384\n"
"v_mfma_f32_16x16x32_bf16 v[28:31],a[92:95],a[60:63],v[28:31]\n"
"v_exp_f32 v119,v46\n"
"v_mfma_f32_16x16x32_bf16 v[24:27],a[76:79],a[60:63],v[108:111]\n"
"v_cvt_pk_bf16_f32 v86,v122,v123\n"
"v_or3_b32 v118,v118,v85,v86\n"
"v_mfma_f32_16x16x32_bf16 v[24:27],a[72:75],a[56:59],v[24:27]\n"
"v_exp_f32 v120,v47\n"
"v_mfma_f32_16x16x32_bf16 v[24:27],a[68:71],a[52:55],v[24:27]\n"
"v_cvt_pk_bf16_f32 v87,v119,v120\n"
"v_mfma_f32_16x16x32_bf16 v[24:27],a[64:67],a[48:51],v[24:27]\n"
"s_waitcnt lgkmcnt(0)\n"
"v_exp_f32 v122,v48\n"
"v_mfma_f32_16x16x32_bf16 v[128:131],a[96:99],v[64:67],0\n"
"v_exp_f32 v123,v49\n"
"v_mfma_f32_16x16x32_bf16 v[160:163],a[96:99],v[68:71],0\n"
"ds_read_b128 a[64:67],v113 offset:32768\n"
"ds_read_b128 a[68:71],v113 offset:33792\n"
"v_mfma_f32_16x16x32_bf16 v[192:195],a[96:99],v[72:75],0\n"
"v_exp_f32 v119,v50\n"
"v_mfma_f32_16x16x32_bf16 v[224:227],a[96:99],v[76:79],0\n"
"ds_read_b128 a[72:75],v113 offset:34816\n"
"v_cvt_pk_bf16_f32 v88,v122,v123\n"
"v_mfma_f32_16x16x32_bf16 a[140:143],a[144:147],v[76:79],0\n"
"v_exp_f32 v120,v51\n"
"v_mfma_f32_16x16x32_bf16 v[228:231],a[100:103],v[76:79],0\n"
"ds_read_b128 a[76:79],v113 offset:35840\n"
"ds_read_b128 a[80:83],v113 offset:36864\n"
"v_mfma_f32_16x16x32_bf16 v[132:135],a[100:103],v[64:67],0\n"
"v_exp_f32 v122,v52\n"
"v_mfma_f32_16x16x32_bf16 v[164:167],a[100:103],v[68:71],0\n"
"ds_read_b128 a[84:87],v113 offset:37888\n"
"v_cvt_pk_bf16_f32 v89,v119,v120\n"
"v_mfma_f32_16x16x32_bf16 v[196:199],a[100:103],v[72:75],0\n"
"v_exp_f32 v123,v53\n"
"v_mfma_f32_16x16x32_bf16 a[136:139],a[144:147],v[72:75],0\n"
"ds_read_b128 a[88:91],v113 offset:38912\n"
"ds_read_b128 a[92:95],v113 offset:39936\n"
"v_mfma_f32_16x16x32_bf16 v[200:203],a[104:107],v[72:75],0\n"
"v_exp_f32 v119,v54\n"
"v_cvt_pk_bf16_f32 v90,v122,v123\n"
"v_mfma_f32_16x16x32_bf16 v[232:235],a[104:107],v[76:79],0\n"
"buffer_store_dwordx4 a[176:179],v112,s[24:27],s3 offen offset:0 sc1\n"
"v_mfma_f32_16x16x32_bf16 v[136:139],a[104:107],v[64:67],0\n"
"v_exp_f32 v120,v55\n"
"v_mfma_f32_16x16x32_bf16 v[168:171],a[104:107],v[68:71],0\n"
"v_cvt_pk_bf16_f32 v91,v119,v120\n"
"v_or3_b32 v118,v118,v87,v88\n"
"v_mfma_f32_16x16x32_bf16 a[132:135],a[144:147],v[68:71],0\n"
"v_exp_f32 v122,v56\n"
"v_mfma_f32_16x16x32_bf16 v[172:175],a[108:111],v[68:71],0\n"
"v_or3_b32 v118,v118,v89,v90\n"
"v_mfma_f32_16x16x32_bf16 v[204:207],a[108:111],v[72:75],0\n"
"v_exp_f32 v123,v57\n"
"v_mfma_f32_16x16x32_bf16 v[236:239],a[108:111],v[76:79],0\n"
"v_cvt_pk_bf16_f32 v92,v122,v123\n"
"v_or3_b32 v118,v118,v91,v92\n"
"v_mfma_f32_16x16x32_bf16 v[140:143],a[108:111],v[64:67],0\n"
"v_exp_f32 v119,v58\n"
"v_mfma_f32_16x16x32_bf16 a[128:131],a[144:147],v[64:67],0\n"
"v_mfma_f32_16x16x32_bf16 v[144:147],a[112:115],v[64:67],0\n"
"v_exp_f32 v120,v59\n"
"v_mfma_f32_16x16x32_bf16 v[176:179],a[112:115],v[68:71],0\n"
"v_cvt_pk_bf16_f32 v93,v119,v120\n"
"v_mfma_f32_16x16x32_bf16 v[208:211],a[112:115],v[72:75],0\n"
"v_exp_f32 v122,v60\n"
"v_mfma_f32_16x16x32_bf16 v[240:243],a[112:115],v[76:79],0\n"
"v_mfma_f32_16x16x32_bf16 v[244:247],a[116:119],v[76:79],0\n"
"v_exp_f32 v123,v61\n"
"v_mfma_f32_16x16x32_bf16 v[148:151],a[116:119],v[64:67],0\n"
"v_cvt_pk_bf16_f32 v94,v122,v123\n"
"v_or3_b32 v118,v118,v93,v94\n"
"v_mfma_f32_16x16x32_bf16 v[180:183],a[116:119],v[68:71],0\n"
"v_exp_f32 v119,v62\n"
"v_mfma_f32_16x16x32_bf16 v[212:215],a[116:119],v[72:75],0\n"
"buffer_store_dwordx4 a[232:235],v112,s[24:27],s56 offen offset:2048 sc1\n"
"v_mfma_f32_16x16x32_bf16 v[216:219],a[120:123],v[72:75],0\n"
"v_exp_f32 v120,v63\n"
"v_mfma_f32_16x16x32_bf16 v[248:251],a[120:123],v[76:79],0\n"
"v_cvt_pk_bf16_f32 v95,v119,v120\n"
"v_or_b32 v118,v118,v95\n"
"v_mfma_f32_16x16x32_bf16 v[152:155],a[120:123],v[64:67],0\n"
"v_and_b32 v118,0x40004000,v118\n"
"v_cmp_ne_u32 vcc,0,v118\n"
"v_mfma_f32_16x16x32_bf16 v[184:187],a[120:123],v[68:71],0\n"
"v_mfma_f32_16x16x32_bf16 v[188:191],a[124:127],v[68:71],0\n"
"v_mfma_f32_16x16x32_bf16 v[220:223],a[124:127],v[72:75],0\n"
"v_mfma_f32_16x16x32_bf16 v[252:255],a[124:127],v[76:79],0\n"
"v_mfma_f32_16x16x32_bf16 v[156:159],a[124:127],v[64:67],0\n"
"s_cbranch_vccnz .Lfk_cs_k1\n"
".Lfk_cr_k1:\n"
"s_waitcnt lgkmcnt(0)\n"
"s_barrier\n"
"v_mfma_f32_16x16x32_bf16 v[32:35],a[64:67],a[0:3],v[96:99]\n"
"s_add_u32 m0,s55,12288\n"
"v_exp_f32 v122,v0\n"
"v_mfma_f32_16x16x32_bf16 v[32:35],a[68:71],a[4:7],v[32:35]\n"
"s_add_u32 s54,s54,s22\n"
"v_exp_f32 v123,v1\n"
"v_mfma_f32_16x16x32_bf16 v[32:35],a[72:75],a[8:11],v[32:35]\n"
"ds_read_b128 a[96:99],v114 offset:8192\n"
"ds_read_b128 a[100:103],v114 offset:9216\n"
"v_mfma_f32_16x16x32_bf16 v[32:35],a[76:79],a[12:15],v[32:35]\n"
"v_exp_f32 v119,v2\n"
"v_mfma_f32_16x16x32_bf16 v[36:39],a[92:95],a[12:15],v[96:99]\n"
"ds_read_b128 a[104:107],v114 offset:10240\n"
"v_cvt_pk_bf16_f32 v64,v122,v123\n"
"v_mfma_f32_16x16x32_bf16 v[36:39],a[88:91],a[8:11],v[36:39]\n"
"v_exp_f32 v120,v3\n"
"v_mfma_f32_16x16x32_bf16 v[36:39],a[84:87],a[4:7],v[36:39]\n"
"ds_read_b128 a[108:111],v114 offset:11264\n"
"ds_read_b128 a[112:115],v114 offset:12288\n"
"v_mfma_f32_16x16x32_bf16 v[36:39],a[80:83],a[0:3],v[36:39]\n"
"v_exp_f32 v122,v4\n"
"v_mfma_f32_16x16x32_bf16 v[44:47],a[80:83],a[16:19],v[100:103]\n"
"ds_read_b128 a[116:119],v114 offset:13312\n"
"v_cvt_pk_bf16_f32 v65,v119,v120\n"
"v_mfma_f32_16x16x32_bf16 v[44:47],a[84:87],a[20:23],v[44:47]\n"
"v_exp_f32 v123,v5\n"
"v_mfma_f32_16x16x32_bf16 v[44:47],a[88:91],a[24:27],v[44:47]\n"
"ds_read_b128 a[120:123],v114 offset:14336\n"
"ds_read_b128 a[124:127],v114 offset:15360\n"
"v_mfma_f32_16x16x32_bf16 v[44:47],a[92:95],a[28:31],v[44:47]\n"
"v_exp_f32 v119,v6\n"
"v_cvt_pk_bf16_f32 v66,v122,v123\n"
"v_mfma_f32_16x16x32_bf16 v[40:43],a[76:79],a[28:31],v[100:103]\n"
"buffer_store_dwordx4 a[180:183],v112,s[24:27],s3 offen offset:1024 sc1\n"
"v_mfma_f32_16x16x32_bf16 v[40:43],a[72:75],a[24:27],v[40:43]\n"
"v_exp_f32 v120,v7\n"
"v_mfma_f32_16x16x32_bf16 v[40:43],a[68:71],a[20:23],v[40:43]\n"
"buffer_load_dwordx4 v115,s[32:35],s54 offen sc1 lds\n"
"v_mfma_f32_16x16x32_bf16 v[40:43],a[64:67],a[16:19],v[40:43]\n"
"v_exp_f32 v122,v8\n"
"v_mfma_f32_16x16x32_bf16 v[48:51],a[64:67],a[32:35],v[104:107]\n"
"v_cvt_pk_bf16_f32 v67,v119,v120\n"
"v_or3_b32 v118,v64,v65,v66\n"
"v_mfma_f32_16x16x32_bf16 v[48:51],a[68:71],a[36:39],v[48:51]\n"
"v_exp_f32 v123,v9\n"
"v_mfma_f32_16x16x32_bf16 v[48:51],a[72:75],a[40:43],v[48:51]\n"
"v_cvt_pk_bf16_f32 v68,v122,v123\n"
"v_or3_b32 v118,v118,v67,v68\n"
"v_mfma_f32_16x16x32_bf16 v[48:51],a[76:79],a[44:47],v[48:51]\n"
"v_exp_f32 v119,v10\n"
"v_mfma_f32_16x16x32_bf16 v[52:55],a[92:95],a[44:47],v[104:107]\n"
"v_mfma_f32_16x16x32_bf16 v[52:55],a[88:91],a[40:43],v[52:55]\n"
"v_exp_f32 v120,v11\n"
"v_mfma_f32_16x16x32_bf16 v[52:55],a[84:87],a[36:39],v[52:55]\n"
"v_cvt_pk_bf16_f32 v69,v119,v120\n"
"v_mfma_f32_16x16x32_bf16 v[52:55],a[80:83],a[32:35],v[52:55]\n"
"v_exp_f32 v122,v12\n"
"v_mfma_f32_16x16x32_bf16 v[60:63],a[80:83],a[48:51],v[108:111]\n"
"buffer_store_dwordx4 a[184:187],v112,s[24:27],s3 offen offset:2048 sc1\n"
"v_mfma_f32_16x16x32_bf16 v[60:63],a[84:87],a[52:55],v[60:63]\n"
"v_exp_f32 v123,v13\n"
"v_mfma_f32_16x16x32_bf16 v[60:63],a[88:91],a[56:59],v[60:63]\n"
"v_cvt_pk_bf16_f32 v70,v122,v123\n"
"v_mfma_f32_16x16x32_bf16 v[60:63],a[92:95],a[60:63],v[60:63]\n"
"v_exp_f32 v119,v14\n"
"v_mfma_f32_16x16x32_bf16 v[56:59],a[76:79],a[60:63],v[108:111]\n"
"v_or3_b32 v118,v118,v69,v70\n"
"v_mfma_f32_16x16x32_bf16 v[56:59],a[72:75],a[56:59],v[56:59]\n"
"v_exp_f32 v120,v15\n"
"v_mfma_f32_16x16x32_bf16 v[56:59],a[68:71],a[52:55],v[56:59]\n"
"v_cvt_pk_bf16_f32 v71,v119,v120\n"
"v_mfma_f32_16x16x32_bf16 v[56:59],a[64:67],a[48:51],v[56:59]\n"
"s_waitcnt lgkmcnt(0)\n"
"v_exp_f32 v122,v16\n"
"v_mfma_f32_16x16x32_bf16 v[128:131],a[96:99],v[80:83],v[128:131]\n"
"v_exp_f32 v123,v17\n"
"v_mfma_f32_16x16x32_bf16 v[160:163],a[96:99],v[84:87],v[160:163]\n"
"ds_read_b128 a[64:67],v113 offset:0\n"
"ds_read_b128 a[68:71],v113 offset:1024\n"
"v_mfma_f32_16x16x32_bf16 v[192:195],a[96:99],v[88:91],v[192:195]\n"
"v_exp_f32 v119,v18\n"
"v_mfma_f32_16x16x32_bf16 v[224:227],a[96:99],v[92:95],v[224:227]\n"
"ds_read_b128 a[72:75],v113 offset:2048\n"
"v_cvt_pk_bf16_f32 v72,v122,v123\n"
"v_mfma_f32_16x16x32_bf16 a[140:143],a[144:147],v[92:95],a[140:143]\n"
"v_exp_f32 v120,v19\n"
"v_mfma_f32_16x16x32_bf16 v[228:231],a[100:103],v[92:95],v[228:231]\n"
"ds_read_b128 a[76:79],v113 offset:3072\n"
"ds_read_b128 a[80:83],v113 offset:4096\n"
"v_mfma_f32_16x16x32_bf16 v[132:135],a[100:103],v[80:83],v[132:135]\n"
"v_exp_f32 v122,v20\n"
"v_mfma_f32_16x16x32_bf16 v[164:167],a[100:103],v[84:87],v[164:167]\n"
"ds_read_b128 a[84:87],v113 offset:5120\n"
"v_cvt_pk_bf16_f32 v73,v119,v120\n"
"v_mfma_f32_16x16x32_bf16 v[196:199],a[100:103],v[88:91],v[196:199]\n"
"v_exp_f32 v123,v21\n"
"v_mfma_f32_16x16x32_bf16 a[136:139],a[144:147],v[88:91],a[136:139]\n"
"ds_read_b128 a[88:91],v113 offset:6144\n"
"ds_read_b128 a[92:95],v113 offset:7168\n"
"v_mfma_f32_16x16x32_bf16 v[200:203],a[104:107],v[88:91],v[200:203]\n"
"v_exp_f32 v119,v22\n"
"v_mfma_f32_16x16x32_bf16 v[232:235],a[104:107],v[92:95],v[232:235]\n"
"buffer_store_dwordx4 a[188:191],v112,s[24:27],s3 offen offset:3072 sc1\n"
"v_mfma_f32_16x16x32_bf16 v[136:139],a[104:107],v[80:83],v[136:139]\n"
"v_exp_f32 v120,v23\n"
"v_mfma_f32_16x16x32_bf16 v[168:171],a[104:107],v[84:87],v[168:171]\n"
"s_or_b32 s3,s3,0x3c000\n"
"v_cvt_pk_bf16_f32 v74,v122,v123\n"
"v_cvt_pk_bf16_f32 v75,v119,v120\n"
"v_mfma_f32_16x16x32_bf16 a[132:135],a[144:147],v[84:87],a[132:135]\n"
"v_exp_f32 v122,v24\n"
"v_mfma_f32_16x16x32_bf16 v[172:175],a[108:111],v[84:87],v[172:175]\n"
"v_or3_b32 v118,v118,v71,v72\n"
"v_or3_b32 v118,v118,v73,v74\n"
"v_mfma_f32_16x16x32_bf16 v[204:207],a[108:111],v[88:91],v[204:207]\n"
"v_exp_f32 v123,v25\n"
"v_mfma_f32_16x16x32_bf16 v[236:239],a[108:111],v[92:95],v[236:239]\n"
"v_cvt_pk_bf16_f32 v76,v122,v123\n"
"v_or3_b32 v118,v118,v75,v76\n"
"v_mfma_f32_16x16x32_bf16 v[140:143],a[108:111],v[80:83],v[140:143]\n"
"v_exp_f32 v119,v26\n"
"v_mfma_f32_16x16x32_bf16 a[128:131],a[144:147],v[80:83],a[128:131]\n"
"v_mfma_f32_16x16x32_bf16 v[144:147],a[112:115],v[80:83],v[144:147]\n"
"v_exp_f32 v120,v27\n"
"v_mfma_f32_16x16x32_bf16 v[176:179],a[112:115],v[84:87],v[176:179]\n"
"v_cvt_pk_bf16_f32 v77,v119,v120\n"
"v_mfma_f32_16x16x32_bf16 v[208:211],a[112:115],v[88:91],v[208:211]\n"
"v_exp_f32 v122,v28\n"
"v_mfma_f32_16x16x32_bf16 v[240:243],a[112:115],v[92:95],v[240:243]\n"
"v_mfma_f32_16x16x32_bf16 v[244:247],a[116:119],v[92:95],v[244:247]\n"
"v_exp_f32 v123,v29\n"
"v_mfma_f32_16x16x32_bf16 v[148:151],a[116:119],v[80:83],v[148:151]\n"
"v_cvt_pk_bf16_f32 v78,v122,v123\n"
"v_or3_b32 v118,v118,v77,v78\n"
"v_mfma_f32_16x16x32_bf16 v[180:183],a[116:119],v[84:87],v[180:183]\n"
"v_exp_f32 v119,v30\n"
"v_mfma_f32_16x16x32_bf16 v[212:215],a[116:119],v[88:91],v[212:215]\n"
"buffer_store_dwordx4 a[236:239],v112,s[24:27],s56 offen offset:3072 sc1\n"
"v_mfma_f32_16x16x32_bf16 v[216:219],a[120:123],v[88:91],v[216:219]\n"
"v_exp_f32 v120,v31\n"
"v_mfma_f32_16x16x32_bf16 v[248:251],a[120:123],v[92:95],v[248:251]\n"
"v_cvt_pk_bf16_f32 v79,v119,v120\n"
"v_or_b32 v118,v118,v79\n"
"v_mfma_f32_16x16x32_bf16 v[152:155],a[120:123],v[80:83],v[152:155]\n"
"v_and_b32 v118,0x40004000,v118\n"
"v_cmp_ne_u32 vcc,0,v118\n"
"v_mfma_f32_16x16x32_bf16 v[184:187],a[120:123],v[84:87],v[184:187]\n"
"v_mfma_f32_16x16x32_bf16 v[188:191],a[124:127],v[84:87],v[188:191]\n"
"v_mfma_f32_16x16x32_bf16 v[220:223],a[124:127],v[88:91],v[220:223]\n"
"v_mfma_f32_16x16x32_bf16 v[252:255],a[124:127],v[92:95],v[252:255]\n"
"v_mfma_f32_16x16x32_bf16 v[156:159],a[124:127],v[80:83],v[156:159]\n"
"s_cbranch_vccnz .Lfk_cs_k2\n"
".Lfk_cr_k2:\n"
"s_waitcnt vmcnt(15) lgkmcnt(0)\n"
"s_barrier\n"
"s_branch .Lfk_g1\n"
".Lfk_winA:\n"
"v_max3_f32 v118,v32,v33,v34\n"
"v_max3_f32 v119,v35,v36,v37\n"
"v_max3_f32 v119,v119,v38,v39\n"
"v_max3_f32 v118,v118,v40,v41\n"
"v_max3_f32 v119,v119,v42,v43\n"
"v_max3_f32 v118,v118,v44,v45\n"
"v_max3_f32 v119,v119,v46,v47\n"
"v_max3_f32 v118,v118,v48,v49\n"
"v_max3_f32 v119,v119,v50,v51\n"
"v_max3_f32 v118,v118,v52,v53\n"
"v_max3_f32 v119,v119,v54,v55\n"
"v_max3_f32 v118,v118,v56,v57\n"
"v_max3_f32 v119,v119,v58,v59\n"
"v_max3_f32 v118,v118,v60,v61\n"
"v_max3_f32 v119,v119,v62,v63\n"
"v_max_f32 v118,v118,v119\n"
"v_cmp_lt_f32 vcc,s93,v118\n"
"s_nop 0\n"
"s_cbranch_vccnz .Lfk_cs_x2\n"
".Lfk_cr_x2:\n"
"s_mov_b32 s64,s61\n"
"s_cmp_eq_u32 s52,3\n"
"s_cselect_b32 s67,172032,0\n"
"v_mfma_f32_16x16x32_bf16 v[8:11],a[64:67],a[16:19],v[100:103]\n"
"s_swappc_b64 s[94:95],s[20:21]\n"
"ds_read_b128 a[96:99],v114 offset:24576\n"
"ds_read_b128 a[100:103],v114 offset:25600\n"
"v_mfma_f32_16x16x32_bf16 v[8:11],a[68:71],a[20:23],v[8:11]\n"
"ds_read_b128 a[104:107],v114 offset:26624\n"
"ds_read_b128 a[108:111],v114 offset:27648\n"
"v_mfma_f32_16x16x32_bf16 v[8:11],a[72:75],a[24:27],v[8:11]\n"
"ds_read_b128 a[112:115],v114 offset:28672\n"
"ds_read_b128 a[116:119],v114 offset:29696\n"
"v_mfma_f32_16x16x32_bf16 v[8:11],a[76:79],a[28:31],v[8:11]\n"
"ds_read_b128 a[120:123],v114 offset:30720\n"
"ds_read_b128 a[124:127],v114 offset:31744\n"
"v_mfma_f32_16x16x32_bf16 v[12:15],a[92:95],a[28:31],v[100:103]\n"
"v_add_f32 v0,0x80000000,v126\n"
"v_min_f32 v0,v0,v96\n"
"v_mfma_f32_16x16x32_bf16 v[12:15],a[88:91],a[24:27],v[12:15]\n"
"v_add_f32 v1,0xf149f2ca,v126\n"
"v_min_f32 v1,v1,v97\n"
"v_mfma_f32_16x16x32_bf16 v[12:15],a[84:87],a[20:23],v[12:15]\n"
"v_add_f32 v2,0xf1c9f2ca,v126\n"
"v_min_f32 v2,v2,v98\n"
"v_mfma_f32_16x16x32_bf16 v[12:15],a[80:83],a[16:19],v[12:15]\n"
"v_add_f32 v3,0xf2177617,v126\n"
"v_min_f32 v3,v3,v99\n"
"v_mfma_f32_16x16x32_bf16 v[20:23],a[80:83],a[32:35],v[104:107]\n"
"v_add_f32 v4,0xf349f2ca,v126\n"
"v_min_f32 v4,v4,v96\n"
"v_mfma_f32_16x16x32_bf16 v[20:23],a[84:87],a[36:39],v[20:23]\n"
"v_add_f32 v5,0xf35691f6,v126\n"
"v_min_f32 v5,v5,v97\n"
"v_mfma_f32_16x16x32_bf16 v[20:23],a[88:91],a[40:43],v[20:23]\n"
"v_add_f32 v6,0xf3633123,v126\n"
"v_min_f32 v6,v6,v98\n"
"v_mfma_f32_16x16x32_bf16 v[20:23],a[92:95],a[44:47],v[20:23]\n"
"v_add_f32 v7,0xf36fd050,v126\n"
"v_min_f32 v7,v7,v99\n"
"v_mfma_f32_16x16x32_bf16 v[16:19],a[76:79],a[44:47],v[104:107]\n"
"s_add_u32 s101,s101,4\n"
"v_exp_f32 v32,v32\n"
"v_mfma_f32_16x16x32_bf16 v[16:19],a[72:75],a[40:43],v[16:19]\n"
"s_add_u32 m0,s67,0\n"
"v_exp_f32 v33,v33\n"
"v_mfma_f32_16x16x32_bf16 v[16:19],a[68:71],a[36:39],v[16:19]\n"
"v_exp_f32 v34,v34\n"
"v_mfma_f32_16x16x32_bf16 v[16:19],a[64:67],a[32:35],v[16:19]\n"
"v_exp_f32 v35,v35\n"
"v_mfma_f32_16x16x32_bf16 v[24:27],a[64:67],a[48:51],v[108:111]\n"
"v_exp_f32 v36,v36\n"
"v_mfma_f32_16x16x32_bf16 v[24:27],a[68:71],a[52:55],v[24:27]\n"
"v_exp_f32 v37,v37\n"
"v_mfma_f32_16x16x32_bf16 v[24:27],a[72:75],a[56:59],v[24:27]\n"
"v_exp_f32 v38,v38\n"
"v_mfma_f32_16x16x32_bf16 v[24:27],a[76:79],a[60:63],v[24:27]\n"
"v_exp_f32 v39,v39\n"
"v_mfma_f32_16x16x32_bf16 v[28:31],a[92:95],a[60:63],v[108:111]\n"
"v_exp_f32 v40,v40\n"
"v_mfma_f32_16x16x32_bf16 v[28:31],a[88:91],a[56:59],v[28:31]\n"
"v_exp_f32 v41,v41\n"
"v_mfma_f32_16x16x32_bf16 v[28:31],a[84:87],a[52:55],v[28:31]\n"
"v_exp_f32 v42,v42\n"
"v_mfma_f32_16x16x32_bf16 v[28:31],a[80:83],a[48:51],v[28:31]\n"
"v_exp_f32 v43,v43\n"
"v_mfma_f32_16x16x32_bf16 v[4:7],a[80:83],a[0:3],v[4:7]\n"
"v_exp_f32 v44,v44\n"
"v_mfma_f32_16x16x32_bf16 v[4:7],a[84:87],a[4:7],v[4:7]\n"
"v_exp_f32 v45,v45\n"
"v_mfma_f32_16x16x32_bf16 v[4:7],a[88:91],a[8:11],v[4:7]\n"
"buffer_load_dwordx4 v112,s[24:27],s64 offen offset:0 sc1 lds\n"
"v_mfma_f32_16x16x32_bf16 v[4:7],a[92:95],a[12:15],v[4:7]\n"
"v_exp_f32 v46,v46\n"
"v_mfma_f32_16x16x32_bf16 v[0:3],a[76:79],a[12:15],v[0:3]\n"
"buffer_load_dwordx4 v112,s[24:27],s64 offen offset:1024 sc1 lds\n"
"v_mfma_f32_16x16x32_bf16 v[0:3],a[72:75],a[8:11],v[0:3]\n"
"v_exp_f32 v47,v47\n"
"v_mfma_f32_16x16x32_bf16 v[0:3],a[68:71],a[4:7],v[0:3]\n"
"buffer_load_dwordx4 v112,s[24:27],s64 offen offset:2048 sc1 lds\n"
"v_mfma_f32_16x16x32_bf16 v[0:3],a[64:67],a[0:3],v[0:3]\n"
"s_waitcnt lgkmcnt(0)\n"
"v_exp_f32 v48,v48\n"
"v_mfma_f32_16x16x32_bf16 v[128:131],a[96:99],v[64:67],v[128:131]\n"
"v_exp_f32 v49,v49\n"
"v_mfma_f32_16x16x32_bf16 v[160:163],a[96:99],v[68:71],v[160:163]\n"
"ds_read_b128 a[64:67],v113 offset:16384\n"
"ds_read_b128 a[68:71],v113 offset:17408\n"
"v_mfma_f32_16x16x32_bf16 v[192:195],a[96:99],v[72:75],v[192:195]\n"
"v_exp_f32 v50,v50\n"
"v_mfma_f32_16x16x32_bf16 v[224:227],a[96:99],v[76:79],v[224:227]\n"
"ds_read_b128 a[72:75],v113 offset:18432\n"
"ds_read_b128 a[76:79],v113 offset:19456\n"
"v_mfma_f32_16x16x32_bf16 a[140:143],a[144:147],v[76:79],a[140:143]\n"
"v_exp_f32 v51,v51\n"
"v_mfma_f32_16x16x32_bf16 v[228:231],a[100:103],v[76:79],v[228:231]\n"
"ds_read_b128 a[80:83],v113 offset:20480\n"
"ds_read_b128 a[84:87],v113 offset:21504\n"
"v_mfma_f32_16x16x32_bf16 v[132:135],a[100:103],v[64:67],v[132:135]\n"
"v_exp_f32 v52,v52\n"
"v_mfma_f32_16x16x32_bf16 v[164:167],a[100:103],v[68:71],v[164:167]\n"
"ds_read_b128 a[88:91],v113 offset:22528\n"
"ds_read_b128 a[92:95],v113 offset:23552\n"
"v_mfma_f32_16x16x32_bf16 v[196:199],a[100:103],v[72:75],v[196:199]\n"
"v_exp_f32 v53,v53\n"
"v_mfma_f32_16x16x32_bf16 a[136:139],a[144:147],v[72:75],a[136:139]\n"
"s_waitcnt vmcnt(14)\n"
"ds_add_u32 v124,a152\n"
"v_mfma_f32_16x16x32_bf16 v[200:203],a[104:107],v[72:75],v[200:203]\n"
"v_exp_f32 v54,v54\n"
"v_mfma_f32_16x16x32_bf16 v[232:235],a[104:107],v[76:79],v[232:235]\n"
"buffer_load_dwordx4 v112,s[24:27],s64 offen offset:3072 sc1 lds\n"
"v_mfma_f32_16x16x32_bf16 v[136:139],a[104:107],v[64:67],v[136:139]\n"
"s_add_u32 m0,s67,4096\n"
"v_exp_f32 v55,v55\n"
"v_mfma_f32_16x16x32_bf16 v[168:171],a[104:107],v[68:71],v[168:171]\n"
"buffer_load_dwordx4 v117,s[24:27],s64 offen offset:0 sc1 lds\n"
"v_mfma_f32_16x16x32_bf16 a[132:135],a[144:147],v[68:71],a[132:135]\n"
"v_exp_f32 v56,v56\n"
"v_mfma_f32_16x16x32_bf16 v[172:175],a[108:111],v[68:71],v[172:175]\n"
"buffer_load_dwordx4 v117,s[24:27],s64 offen offset:1024 sc1 lds\n"
"v_mfma_f32_16x16x32_bf16 v[204:207],a[108:111],v[72:75],v[204:207]\n"
"v_exp_f32 v57,v57\n"
"v_mfma_f32_16x16x32_bf16 v[236:239],a[108:111],v[76:79],v[236:239]\n"
"buffer_load_dwordx4 v117,s[24:27],s64 offen offset:2048 sc1 lds\n"
"v_mfma_f32_16x16x32_bf16 v[140:143],a[108:111],v[64:67],v[140:143]\n"
"v_exp_f32 v58,v58\n"
"v_mfma_f32_16x16x32_bf16 a[128:131],a[144:147],v[64:67],a[128:131]\n"
"v_cvt_pk_bf16_f32 v80,v32,v33\n"
"v_cvt_pk_bf16_f32 v81,v34,v35\n"
"v_mfma_f32_16x16x32_bf16 v[144:147],a[112:115],v[64:67],v[144:147]\n"
"v_exp_f32 v59,v59\n"
"v_mfma_f32_16x16x32_bf16 v[176:179],a[112:115],v[68:71],v[176:179]\n"
"v_cvt_pk_bf16_f32 v82,v36,v37\n"
"v_cvt_pk_bf16_f32 v83,v38,v39\n"
"v_mfma_f32_16x16x32_bf16 v[208:211],a[112:115],v[72:75],v[208:211]\n"
"v_exp_f32 v60,v60\n"
"v_mfma_f32_16x16x32_bf16 v[240:243],a[112:115],v[76:79],v[240:243]\n"
"buffer_load_dwordx4 v117,s[24:27],s64 offen offset:3072 sc1 lds\n"
"v_mfma_f32_16x16x32_bf16 v[244:247],a[116:119],v[76:79],v[244:247]\n"
"s_add_u32 s64,s64,16384\n"
"v_exp_f32 v61,v61\n"
"v_mfma_f32_16x16x32_bf16 v[148:151],a[116:119],v[64:67],v[148:151]\n"
"s_andn2_b32 m0,s55,16384\n"
"s_add_u32 s54,s54,s22\n"
"v_cvt_pk_bf16_f32 v85,v42,v43\n"
"v_mfma_f32_16x16x32_bf16 v[180:183],a[116:119],v[68:71],v[180:183]\n"
"v_exp_f32 v62,v62\n"
"v_mfma_f32_16x16x32_bf16 v[212:215],a[116:119],v[72:75],v[212:215]\n"
"ds_read_b32 v125,v124\n"
"v_cvt_pk_bf16_f32 v84,v40,v41\n"
"v_mfma_f32_16x16x32_bf16 v[216:219],a[120:123],v[72:75],v[216:219]\n"
"v_exp_f32 v63,v63\n"
"v_mfma_f32_16x16x32_bf16 v[248:251],a[120:123],v[76:79],v[248:251]\n"
"buffer_load_dwordx4 v115,s[32:35],s54 offen sc1 lds\n"
"v_mfma_f32_16x16x32_bf16 v[152:155],a[120:123],v[64:67],v[152:155]\n"
"buffer_store_dwordx4 a[240:243],v112,s[24:27],s56 offen offset:0 sc1\n"
"v_mfma_f32_16x16x32_bf16 v[184:187],a[120:123],v[68:71],v[184:187]\n"
"v_cvt_pk_bf16_f32 v86,v44,v45\n"
"v_cvt_pk_bf16_f32 v87,v46,v47\n"
"v_mfma_f32_16x16x32_bf16 v[188:191],a[124:127],v[68:71],v[188:191]\n"
"s_waitcnt lgkmcnt(0)\n"
"v_cvt_pk_bf16_f32 v88,v48,v49\n"
"v_cvt_pk_bf16_f32 v89,v50,v51\n"
"v_mfma_f32_16x16x32_bf16 v[220:223],a[124:127],v[72:75],v[220:223]\n"
"v_cvt_pk_bf16_f32 v90,v52,v53\n"
"v_cvt_pk_bf16_f32 v91,v54,v55\n"
"v_mfma_f32_16x16x32_bf16 v[252:255],a[124:127],v[76:79],v[252:255]\n"
"v_cvt_pk_bf16_f32 v92,v56,v57\n"
"v_cvt_pk_bf16_f32 v93,v58,v59\n"
"v_mfma_f32_16x16x32_bf16 v[156:159],a[124:127],v[64:67],v[156:159]\n"
"v_cvt_pk_bf16_f32 v94,v60,v61\n"
"v_cvt_pk_bf16_f32 v95,v62,v63\n"
"v_max3_f32 v118,v8,v9,v10\n"
"v_max3_f32 v118,v118,v11,v12\n"
"v_max3_f32 v118,v118,v13,v14\n"
"v_max3_f32 v118,v118,v15,v20\n"
"v_max3_f32 v118,v118,v21,v22\n"
"v_max3_f32 v118,v118,v23,v16\n"
"v_max3_f32 v118,v118,v17,v18\n"
"v_max3_f32 v118,v118,v19,v24\n"
"v_max3_f32 v118,v118,v25,v26\n"
"v_max3_f32 v118,v118,v27,v28\n"
"v_max3_f32 v118,v118,v29,v30\n"
"v_max3_f32 v118,v118,v31,v4\n"
"v_max3_f32 v118,v118,v5,v6\n"
"v_max3_f32 v118,v118,v7,v0\n"
"v_max3_f32 v118,v118,v1,v2\n"
"v_max_f32 v118,v118,v3\n"
"v_cmp_lt_f32 vcc,s93,v118\n"
"s_nop 0\n"
"s_cbranch_vccnz .Lfk_cs_u3\n"
".Lfk_cr_u3:\n"
"v_cmp_gt_u32 vcc,s101,v125\n"
"s_nop 0\n"
"s_cbranch_vccnz .Lfk_cs_mu3\n"
".Lfk_cr_mu3:\n"
"v_mfma_f32_16x16x32_bf16 v[48:51],a[64:67],a[32:35],v[104:107]\n"
"ds_read_b128 a[96:99],v114 offset:57344\n"
"ds_read_b128 a[100:103],v114 offset:58368\n"
"v_mfma_f32_16x16x32_bf16 v[48:51],a[68:71],a[36:39],v[48:51]\n"
"ds_read_b128 a[104:107],v114 offset:59392\n"
"ds_read_b128 a[108:111],v114 offset:60416\n"
"v_mfma_f32_16x16x32_bf16 v[48:51],a[72:75],a[40:43],v[48:51]\n"
"ds_read_b128 a[112:115],v114 offset:61440\n"
"ds_read_b128 a[116:119],v114 offset:62464\n"
"v_mfma_f32_16x16x32_bf16 v[48:51],a[76:79],a[44:47],v[48:51]\n"
"ds_read_b128 a[120:123],v114 offset:63488\n"
"ds_read_b128 a[124:127],v114 offset:64512\n"
"v_mfma_f32_16x16x32_bf16 v[52:55],a[92:95],a[44:47],v[104:107]\n"
"ds_read_b128 a[0:3],v116 offset:40960\n"
"ds_read_b128 a[4:7],v116 offset:41984\n"
"v_mfma_f32_16x16x32_bf16 v[52:55],a[88:91],a[40:43],v[52:55]\n"
"ds_read_b128 a[8:11],v116 offset:43008\n"
"ds_read_b128 a[12:15],v116 offset:44032\n"
"v_mfma_f32_16x16x32_bf16 v[52:55],a[84:87],a[36:39],v[52:55]\n"
"v_add_f32 v40,0x80000000,v126\n"
"v_min_f32 v40,v40,v100\n"
"v_mfma_f32_16x16x32_bf16 v[52:55],a[80:83],a[32:35],v[52:55]\n"
"v_add_f32 v41,0xf149f2ca,v126\n"
"v_min_f32 v41,v41,v101\n"
"v_mfma_f32_16x16x32_bf16 v[60:63],a[80:83],a[48:51],v[108:111]\n"
"v_add_f32 v42,0xf1c9f2ca,v126\n"
"v_min_f32 v42,v42,v102\n"
"v_mfma_f32_16x16x32_bf16 v[60:63],a[84:87],a[52:55],v[60:63]\n"
"v_add_f32 v43,0xf2177617,v126\n"
"v_min_f32 v43,v43,v103\n"
"v_mfma_f32_16x16x32_bf16 v[60:63],a[88:91],a[56:59],v[60:63]\n"
"v_add_f32 v44,0xf349f2ca,v126\n"
"v_min_f32 v44,v44,v100\n"
"v_mfma_f32_16x16x32_bf16 v[60:63],a[92:95],a[60:63],v[60:63]\n"
"v_add_f32 v45,0xf35691f6,v126\n"
"v_min_f32 v45,v45,v101\n"
"v_mfma_f32_16x16x32_bf16 v[56:59],a[76:79],a[60:63],v[108:111]\n"
"v_add_f32 v46,0xf3633123,v126\n"
"v_min_f32 v46,v46,v102\n"
"v_mfma_f32_16x16x32_bf16 v[56:59],a[72:75],a[56:59],v[56:59]\n"
"v_add_f32 v47,0xf36fd050,v126\n"
"v_min_f32 v47,v47,v103\n"
"v_mfma_f32_16x16x32_bf16 v[56:59],a[68:71],a[52:55],v[56:59]\n"
"s_add_u32 s101,s101,4\n"
"v_exp_f32 v0,v0\n"
"v_mfma_f32_16x16x32_bf16 v[56:59],a[64:67],a[48:51],v[56:59]\n"
"s_add_u32 m0,s55,4096\n"
"v_exp_f32 v1,v1\n"
"v_mfma_f32_16x16x32_bf16 v[40:43],a[64:67],a[16:19],v[40:43]\n"
"s_add_u32 s54,s54,s22\n"
"v_exp_f32 v2,v2\n"
"v_mfma_f32_16x16x32_bf16 v[40:43],a[68:71],a[20:23],v[40:43]\n"
"v_exp_f32 v3,v3\n"
"v_mfma_f32_16x16x32_bf16 v[40:43],a[72:75],a[24:27],v[40:43]\n"
"v_exp_f32 v4,v4\n"
"v_mfma_f32_16x16x32_bf16 v[40:43],a[76:79],a[28:31],v[40:43]\n"
"v_exp_f32 v5,v5\n"
"v_mfma_f32_16x16x32_bf16 v[44:47],a[92:95],a[28:31],v[44:47]\n"
"v_exp_f32 v6,v6\n"
"v_mfma_f32_16x16x32_bf16 v[44:47],a[88:91],a[24:27],v[44:47]\n"
"v_exp_f32 v7,v7\n"
"v_mfma_f32_16x16x32_bf16 v[44:47],a[84:87],a[20:23],v[44:47]\n"
"v_exp_f32 v8,v8\n"
"v_mfma_f32_16x16x32_bf16 v[44:47],a[80:83],a[16:19],v[44:47]\n"
"s_waitcnt lgkmcnt(4)\n"
"v_exp_f32 v9,v9\n"
"v_mfma_f32_16x16x32_bf16 v[128:131],a[96:99],v[80:83],v[128:131]\n"
"v_exp_f32 v10,v10\n"
"v_mfma_f32_16x16x32_bf16 v[160:163],a[96:99],v[84:87],v[160:163]\n"
"v_exp_f32 v11,v11\n"
"v_mfma_f32_16x16x32_bf16 v[192:195],a[96:99],v[88:91],v[192:195]\n"
"v_exp_f32 v12,v12\n"
"v_mfma_f32_16x16x32_bf16 v[224:227],a[96:99],v[92:95],v[224:227]\n"
"v_exp_f32 v13,v13\n"
"v_mfma_f32_16x16x32_bf16 a[140:143],a[144:147],v[92:95],a[140:143]\n"
"v_exp_f32 v14,v14\n"
"v_mfma_f32_16x16x32_bf16 v[228:231],a[100:103],v[92:95],v[228:231]\n"
"v_exp_f32 v15,v15\n"
"v_mfma_f32_16x16x32_bf16 v[132:135],a[100:103],v[80:83],v[132:135]\n"
"v_exp_f32 v16,v16\n"
"v_mfma_f32_16x16x32_bf16 v[164:167],a[100:103],v[84:87],v[164:167]\n"
"v_exp_f32 v17,v17\n"
"v_mfma_f32_16x16x32_bf16 v[196:199],a[100:103],v[88:91],v[196:199]\n"
"v_exp_f32 v18,v18\n"
"v_mfma_f32_16x16x32_bf16 a[136:139],a[144:147],v[88:91],a[136:139]\n"
"v_exp_f32 v19,v19\n"
"v_mfma_f32_16x16x32_bf16 v[200:203],a[104:107],v[88:91],v[200:203]\n"
"v_exp_f32 v20,v20\n"
"v_mfma_f32_16x16x32_bf16 v[232:235],a[104:107],v[92:95],v[232:235]\n"
"v_exp_f32 v21,v21\n"
"v_mfma_f32_16x16x32_bf16 v[136:139],a[104:107],v[80:83],v[136:139]\n"
"ds_read_b128 a[64:67],v113 offset:49152\n"
"ds_read_b128 a[68:71],v113 offset:50176\n"
"v_mfma_f32_16x16x32_bf16 v[168:171],a[104:107],v[84:87],v[168:171]\n"
"v_exp_f32 v22,v22\n"
"v_mfma_f32_16x16x32_bf16 a[132:135],a[144:147],v[84:87],a[132:135]\n"
"v_exp_f32 v23,v23\n"
"v_mfma_f32_16x16x32_bf16 v[172:175],a[108:111],v[84:87],v[172:175]\n"
"ds_read_b128 a[72:75],v113 offset:51200\n"
"ds_read_b128 a[76:79],v113 offset:52224\n"
"v_mfma_f32_16x16x32_bf16 v[204:207],a[108:111],v[88:91],v[204:207]\n"
"v_exp_f32 v24,v24\n"
"v_mfma_f32_16x16x32_bf16 v[236:239],a[108:111],v[92:95],v[236:239]\n"
"ds_read_b128 a[80:83],v113 offset:53248\n"
"ds_read_b128 a[84:87],v113 offset:54272\n"
"v_mfma_f32_16x16x32_bf16 v[140:143],a[108:111],v[80:83],v[140:143]\n"
"v_exp_f32 v25,v25\n"
"v_mfma_f32_16x16x32_bf16 a[128:131],a[144:147],v[80:83],a[128:131]\n"
"v_exp_f32 v26,v26\n"
"v_mfma_f32_16x16x32_bf16 v[144:147],a[112:115],v[80:83],v[144:147]\n"
"ds_read_b128 a[88:91],v113 offset:55296\n"
"ds_read_b128 a[92:95],v113 offset:56320\n"
"v_mfma_f32_16x16x32_bf16 v[176:179],a[112:115],v[84:87],v[176:179]\n"
"v_exp_f32 v27,v27\n"
"v_mfma_f32_16x16x32_bf16 v[208:211],a[112:115],v[88:91],v[208:211]\n"
"s_waitcnt vmcnt(11)\n"
"ds_add_u32 v124,a152\n"
"v_mfma_f32_16x16x32_bf16 v[240:243],a[112:115],v[92:95],v[240:243]\n"
"v_exp_f32 v28,v28\n"
"v_mfma_f32_16x16x32_bf16 v[244:247],a[116:119],v[92:95],v[244:247]\n"
"v_exp_f32 v29,v29\n"
"v_mfma_f32_16x16x32_bf16 v[148:151],a[116:119],v[80:83],v[148:151]\n"
"buffer_load_dwordx4 v115,s[32:35],s54 offen sc1 lds\n"
"v_mfma_f32_16x16x32_bf16 v[180:183],a[116:119],v[84:87],v[180:183]\n"
"s_add_u32 m0,s67,16384\n"
"v_exp_f32 v30,v30\n"
"v_mfma_f32_16x16x32_bf16 v[212:215],a[116:119],v[88:91],v[212:215]\n"
"ds_read_b32 v125,v124\n"
"v_cvt_pk_bf16_f32 v64,v0,v1\n"
"v_mfma_f32_16x16x32_bf16 v[216:219],a[120:123],v[88:91],v[216:219]\n"
"v_exp_f32 v31,v31\n"
"v_mfma_f32_16x16x32_bf16 v[248:251],a[120:123],v[92:95],v[248:251]\n"
"buffer_load_dwordx4 v112,s[24:27],s64 offen offset:0 sc1 lds\n"
"v_mfma_f32_16x16x32_bf16 v[152:155],a[120:123],v[80:83],v[152:155]\n"
"buffer_load_dwordx4 v112,s[24:27],s64 offen offset:1024 sc1 lds\n"
"v_mfma_f32_16x16x32_bf16 v[184:187],a[120:123],v[84:87],v[184:187]\n"
"buffer_load_dwordx4 v112,s[24:27],s64 offen offset:2048 sc1 lds\n"
"v_mfma_f32_16x16x32_bf16 v[188:191],a[124:127],v[84:87],v[188:191]\n"
"s_waitcnt lgkmcnt(0)\n"
"buffer_load_dwordx4 v112,s[24:27],s64 offen offset:3072 sc1 lds\n"
"v_mfma_f32_16x16x32_bf16 v[220:223],a[124:127],v[88:91],v[220:223]\n"
"s_add_u32 m0,s67,20480\n"
"v_cvt_pk_bf16_f32 v65,v2,v3\n"
"v_cvt_pk_bf16_f32 v66,v4,v5\n"
"v_mfma_f32_16x16x32_bf16 v[252:255],a[124:127],v[92:95],v[252:255]\n"
"buffer_load_dwordx4 v117,s[24:27],s64 offen offset:0 sc1 lds\n"
"v_mfma_f32_16x16x32_bf16 v[156:159],a[124:127],v[80:83],v[156:159]\n"
"buffer_load_dwordx4 v117,s[24:27],s64 offen offset:1024 sc1 lds\n"
"buffer_load_dwordx4 v117,s[24:27],s64 offen offset:2048 sc1 lds\n"
"buffer_load_dwordx4 v117,s[24:27],s64 offen offset:3072 sc1 lds\n"
"buffer_store_dwordx4 a[244:247],v112,s[24:27],s56 offen offset:1024 sc1\n"
"s_add_u32 s64,s64,16384\n"
"v_cvt_pk_bf16_f32 v67,v6,v7\n"
"v_cvt_pk_bf16_f32 v68,v8,v9\n"
"v_cvt_pk_bf16_f32 v69,v10,v11\n"
"v_cvt_pk_bf16_f32 v70,v12,v13\n"
"v_cvt_pk_bf16_f32 v71,v14,v15\n"
"v_cvt_pk_bf16_f32 v72,v16,v17\n"
"v_cvt_pk_bf16_f32 v73,v18,v19\n"
"v_cvt_pk_bf16_f32 v74,v20,v21\n"
"v_cvt_pk_bf16_f32 v75,v22,v23\n"
"v_cvt_pk_bf16_f32 v76,v24,v25\n"
"v_cvt_pk_bf16_f32 v77,v26,v27\n"
"v_cvt_pk_bf16_f32 v78,v28,v29\n"
"v_cvt_pk_bf16_f32 v79,v30,v31\n"
"v_max3_f32 v118,v48,v49,v50\n"
"v_max3_f32 v118,v118,v51,v52\n"
"v_max3_f32 v118,v118,v53,v54\n"
"v_max3_f32 v118,v118,v55,v60\n"
"v_max3_f32 v118,v118,v61,v62\n"
"v_max3_f32 v118,v118,v63,v56\n"
"v_max3_f32 v118,v118,v57,v58\n"
"v_max3_f32 v118,v118,v59,v40\n"
"v_max3_f32 v118,v118,v41,v42\n"
"v_max3_f32 v118,v118,v43,v44\n"
"v_max3_f32 v118,v118,v45,v46\n"
"v_max_f32 v118,v118,v47\n"
"v_cmp_lt_f32 vcc,s93,v118\n"
"s_nop 0\n"
"s_cbranch_vccnz .Lfk_cs_u0\n"
".Lfk_cr_u0:\n"
"v_cmp_gt_u32 vcc,s101,v125\n"
"s_nop 0\n"
"s_cbranch_vccnz .Lfk_cs_mu0\n"
".Lfk_cr_mu0:\n"
"v_mfma_f32_16x16x32_bf16 v[24:27],a[64:67],a[48:51],v[108:111]\n"
"ds_read_b128 a[96:99],v114 offset:40960\n"
"ds_read_b128 a[100:103],v114 offset:41984\n"
"v_mfma_f32_16x16x32_bf16 v[24:27],a[68:71],a[52:55],v[24:27]\n"
"ds_read_b128 a[104:107],v114 offset:43008\n"
"ds_read_b128 a[108:111],v114 offset:44032\n"
"v_mfma_f32_16x16x32_bf16 v[24:27],a[72:75],a[56:59],v[24:27]\n"
"ds_read_b128 a[112:115],v114 offset:45056\n"
"ds_read_b128 a[116:119],v114 offset:46080\n"
"v_mfma_f32_16x16x32_bf16 v[24:27],a[76:79],a[60:63],v[24:27]\n"
"ds_read_b128 a[120:123],v114 offset:47104\n"
"ds_read_b128 a[124:127],v114 offset:48128\n"
"v_mfma_f32_16x16x32_bf16 v[28:31],a[92:95],a[60:63],v[108:111]\n"
"ds_read_b128 a[16:19],v116 offset:8192\n"
"ds_read_b128 a[20:23],v116 offset:9216\n"
"v_mfma_f32_16x16x32_bf16 v[28:31],a[88:91],a[56:59],v[28:31]\n"
"ds_read_b128 a[24:27],v116 offset:10240\n"
"ds_read_b128 a[28:31],v116 offset:11264\n"
"v_mfma_f32_16x16x32_bf16 v[28:31],a[84:87],a[52:55],v[28:31]\n"
"v_add_f32 v16,0x80000000,v126\n"
"v_min_f32 v16,v16,v104\n"
"v_mfma_f32_16x16x32_bf16 v[28:31],a[80:83],a[48:51],v[28:31]\n"
"v_add_f32 v17,0xf149f2ca,v126\n"
"v_add_f32 v18,0xf1c9f2ca,v126\n"
"v_add_f32 v19,0xf2177617,v126\n"
"v_min_f32 v19,v19,v107\n"
"v_add_f32 v20,0xf349f2ca,v126\n"
"v_min_f32 v20,v20,v104\n"
"v_add_f32 v21,0xf35691f6,v126\n"
"v_min_f32 v21,v21,v105\n"
"v_add_f32 v22,0xf3633123,v126\n"
"v_min_f32 v22,v22,v106\n"
"v_add_f32 v23,0xf36fd050,v126\n"
"v_min_f32 v23,v23,v107\n"
"v_min_f32 v18,v18,v106\n"
"v_min_f32 v17,v17,v105\n"
"v_mfma_f32_16x16x32_bf16 v[20:23],a[80:83],a[32:35],v[20:23]\n"
"s_add_u32 s101,s101,4\n"
"v_exp_f32 v40,v40\n"
"v_mfma_f32_16x16x32_bf16 v[20:23],a[84:87],a[36:39],v[20:23]\n"
"s_add_u32 m0,s55,8192\n"
"v_exp_f32 v41,v41\n"
"v_mfma_f32_16x16x32_bf16 v[20:23],a[88:91],a[40:43],v[20:23]\n"
"s_add_u32 s54,s54,s22\n"
"v_exp_f32 v42,v42\n"
"v_mfma_f32_16x16x32_bf16 v[20:23],a[92:95],a[44:47],v[20:23]\n"
"v_exp_f32 v43,v43\n"
"v_mfma_f32_16x16x32_bf16 v[16:19],a[76:79],a[44:47],v[16:19]\n"
"v_exp_f32 v44,v44\n"
"v_mfma_f32_16x16x32_bf16 v[16:19],a[72:75],a[40:43],v[16:19]\n"
"v_exp_f32 v45,v45\n"
"v_mfma_f32_16x16x32_bf16 v[16:19],a[68:71],a[36:39],v[16:19]\n"
"v_exp_f32 v46,v46\n"
"v_mfma_f32_16x16x32_bf16 v[16:19],a[64:67],a[32:35],v[16:19]\n"
"s_waitcnt lgkmcnt(4)\n"
"v_exp_f32 v47,v47\n"
"v_mfma_f32_16x16x32_bf16 v[128:131],a[96:99],v[64:67],v[128:131]\n"
"v_exp_f32 v48,v48\n"
"v_mfma_f32_16x16x32_bf16 v[160:163],a[96:99],v[68:71],v[160:163]\n"
"v_exp_f32 v49,v49\n"
"v_mfma_f32_16x16x32_bf16 v[192:195],a[96:99],v[72:75],v[192:195]\n"
"ds_read_b128 a[64:67],v113 offset:32768\n"
"ds_read_b128 a[68:71],v113 offset:33792\n"
"v_mfma_f32_16x16x32_bf16 v[224:227],a[96:99],v[76:79],v[224:227]\n"
"v_exp_f32 v50,v50\n"
"v_mfma_f32_16x16x32_bf16 a[140:143],a[144:147],v[76:79],a[140:143]\n"
"ds_read_b128 a[72:75],v113 offset:34816\n"
"ds_read_b128 a[76:79],v113 offset:35840\n"
"v_mfma_f32_16x16x32_bf16 v[228:231],a[100:103],v[76:79],v[228:231]\n"
"v_exp_f32 v51,v51\n"
"v_mfma_f32_16x16x32_bf16 v[132:135],a[100:103],v[64:67],v[132:135]\n"
"ds_read_b128 a[80:83],v113 offset:36864\n"
"ds_read_b128 a[84:87],v113 offset:37888\n"
"v_mfma_f32_16x16x32_bf16 v[164:167],a[100:103],v[68:71],v[164:167]\n"
"v_exp_f32 v52,v52\n"
"v_mfma_f32_16x16x32_bf16 v[196:199],a[100:103],v[72:75],v[196:199]\n"
"v_exp_f32 v53,v53\n"
"v_mfma_f32_16x16x32_bf16 a[136:139],a[144:147],v[72:75],a[136:139]\n"
"ds_read_b128 a[88:91],v113 offset:38912\n"
"ds_read_b128 a[92:95],v113 offset:39936\n"
"v_mfma_f32_16x16x32_bf16 v[200:203],a[104:107],v[72:75],v[200:203]\n"
"v_exp_f32 v54,v54\n"
"v_mfma_f32_16x16x32_bf16 v[232:235],a[104:107],v[76:79],v[232:235]\n"
"s_waitcnt vmcnt(11)\n"
"ds_add_u32 v124,a152\n"
"v_mfma_f32_16x16x32_bf16 v[136:139],a[104:107],v[64:67],v[136:139]\n"
"v_exp_f32 v55,v55\n"
"v_mfma_f32_16x16x32_bf16 v[168:171],a[104:107],v[68:71],v[168:171]\n"
"buffer_load_dwordx4 v115,s[32:35],s54 offen sc1 lds\n"
"v_mfma_f32_16x16x32_bf16 a[132:135],a[144:147],v[68:71],a[132:135]\n"
"s_add_u32 m0,s67,49152\n"
"v_exp_f32 v56,v56\n"
"v_mfma_f32_16x16x32_bf16 v[172:175],a[108:111],v[68:71],v[172:175]\n"
"buffer_load_dwordx4 v112,s[24:27],s64 offen offset:0 sc1 lds\n"
"v_mfma_f32_16x16x32_bf16 v[204:207],a[108:111],v[72:75],v[204:207]\n"
"v_exp_f32 v57,v57\n"
"v_mfma_f32_16x16x32_bf16 v[236:239],a[108:111],v[76:79],v[236:239]\n"
"buffer_load_dwordx4 v112,s[24:27],s64 offen offset:1024 sc1 lds\n"
"v_mfma_f32_16x16x32_bf16 v[140:143],a[108:111],v[64:67],v[140:143]\n"
"v_exp_f32 v58,v58\n"
"v_mfma_f32_16x16x32_bf16 a[128:131],a[144:147],v[64:67],a[128:131]\n"
"buffer_load_dwordx4 v112,s[24:27],s64 offen offset:2048 sc1 lds\n"
"v_mfma_f32_16x16x32_bf16 v[144:147],a[112:115],v[64:67],v[144:147]\n"
"v_exp_f32 v59,v59\n"
"v_mfma_f32_16x16x32_bf16 v[176:179],a[112:115],v[68:71],v[176:179]\n"
"buffer_load_dwordx4 v112,s[24:27],s64 offen offset:3072 sc1 lds\n"
"v_mfma_f32_16x16x32_bf16 v[208:211],a[112:115],v[72:75],v[208:211]\n"
"s_add_u32 m0,s67,53248\n"
"v_exp_f32 v60,v60\n"
"v_mfma_f32_16x16x32_bf16 v[240:243],a[112:115],v[76:79],v[240:243]\n"
"buffer_load_dwordx4 v117,s[24:27],s64 offen offset:0 sc1 lds\n"
"v_mfma_f32_16x16x32_bf16 v[244:247],a[116:119],v[76:79],v[244:247]\n"
"v_exp_f32 v61,v61\n"
"v_mfma_f32_16x16x32_bf16 v[148:151],a[116:119],v[64:67],v[148:151]\n"
"buffer_load_dwordx4 v117,s[24:27],s64 offen offset:1024 sc1 lds\n"
"v_mfma_f32_16x16x32_bf16 v[180:183],a[116:119],v[68:71],v[180:183]\n"
"v_exp_f32 v62,v62\n"
"v_mfma_f32_16x16x32_bf16 v[212:215],a[116:119],v[72:75],v[212:215]\n"
"ds_read_b32 v125,v124\n"
"v_cvt_pk_bf16_f32 v84,v40,v41\n"
"v_mfma_f32_16x16x32_bf16 v[216:219],a[120:123],v[72:75],v[216:219]\n"
"v_exp_f32 v63,v63\n"
"v_mfma_f32_16x16x32_bf16 v[248:251],a[120:123],v[76:79],v[248:251]\n"
"buffer_load_dwordx4 v117,s[24:27],s64 offen offset:2048 sc1 lds\n"
"v_mfma_f32_16x16x32_bf16 v[152:155],a[120:123],v[64:67],v[152:155]\n"
"buffer_load_dwordx4 v117,s[24:27],s64 offen offset:3072 sc1 lds\n"
"v_mfma_f32_16x16x32_bf16 v[184:187],a[120:123],v[68:71],v[184:187]\n"
"s_add_u32 s64,s64,16384\n"
"buffer_store_dwordx4 a[248:251],v112,s[24:27],s56 offen offset:2048 sc1\n"
"v_mfma_f32_16x16x32_bf16 v[188:191],a[124:127],v[68:71],v[188:191]\n"
"s_waitcnt lgkmcnt(0)\n"
"s_and_b32 s67,s67,8192\n"
"v_cvt_pk_bf16_f32 v85,v42,v43\n"
"v_mfma_f32_16x16x32_bf16 v[220:223],a[124:127],v[72:75],v[220:223]\n"
"v_cvt_pk_bf16_f32 v86,v44,v45\n"
"v_cvt_pk_bf16_f32 v87,v46,v47\n"
"v_mfma_f32_16x16x32_bf16 v[252:255],a[124:127],v[76:79],v[252:255]\n"
"v_cvt_pk_bf16_f32 v88,v48,v49\n"
"v_cvt_pk_bf16_f32 v89,v50,v51\n"
"v_mfma_f32_16x16x32_bf16 v[156:159],a[124:127],v[64:67],v[156:159]\n"
"v_cvt_pk_bf16_f32 v90,v52,v53\n"
"v_cvt_pk_bf16_f32 v91,v54,v55\n"
"v_cvt_pk_bf16_f32 v92,v56,v57\n"
"v_cvt_pk_bf16_f32 v93,v58,v59\n"
"v_cvt_pk_bf16_f32 v94,v60,v61\n"
"v_cvt_pk_bf16_f32 v95,v62,v63\n"
"v_max3_f32 v118,v24,v25,v26\n"
"v_max3_f32 v118,v118,v27,v28\n"
"v_max3_f32 v118,v118,v29,v30\n"
"v_max3_f32 v118,v118,v31,v20\n"
"v_max3_f32 v118,v118,v21,v22\n"
"v_max3_f32 v118,v118,v23,v16\n"
"v_max3_f32 v118,v118,v17,v18\n"
"v_max_f32 v118,v118,v19\n"
"v_cmp_lt_f32 vcc,s93,v118\n"
"s_nop 0\n"
"s_cbranch_vccnz .Lfk_cs_u1\n"
".Lfk_cr_u1:\n"
"v_cmp_gt_u32 vcc,s101,v125\n"
"s_nop 0\n"
"s_cbranch_vccnz .Lfk_cs_mu1\n"
".Lfk_cr_mu1:\n"
"v_mov_b32 v113,v112\n"
"v_add_f32 v56,0x80000000,v126\n"
"v_min_f32 v56,v56,v108\n"
"v_add_f32 v57,0xf149f2ca,v126\n"
"v_min_f32 v57,v57,v109\n"
"v_add_f32 v58,0xf1c9f2ca,v126\n"
"v_min_f32 v58,v58,v110\n"
"v_add_f32 v59,0xf2177617,v126\n"
"v_min_f32 v59,v59,v111\n"
"v_add_f32 v60,0xf349f2ca,v126\n"
"v_min_f32 v60,v60,v108\n"
"v_add_f32 v61,0xf35691f6,v126\n"
"v_min_f32 v61,v61,v109\n"
"v_add_f32 v62,0xf3633123,v126\n"
"v_min_f32 v62,v62,v110\n"
"v_add_f32 v63,0xf36fd050,v126\n"
"v_min_f32 v63,v63,v111\n"
"v_exp_f32 v16,v16\n"
"v_mfma_f32_16x16x32_bf16 v[56:59],a[64:67],a[48:51],v[56:59]\n"
"s_add_u32 s101,s101,4\n"
"ds_read_b128 a[96:99],v114 offset:8192\n"
"ds_read_b128 a[100:103],v114 offset:9216\n"
"ds_read_b128 a[104:107],v114 offset:10240\n"
"v_exp_f32 v17,v17\n"
"v_mfma_f32_16x16x32_bf16 v[56:59],a[68:71],a[52:55],v[56:59]\n"
"s_add_u32 m0,s55,12288\n"
"ds_read_b128 a[108:111],v114 offset:11264\n"
"ds_read_b128 a[112:115],v114 offset:12288\n"
"ds_read_b128 a[116:119],v114 offset:13312\n"
"v_mfma_f32_16x16x32_bf16 v[56:59],a[72:75],a[56:59],v[56:59]\n"
"ds_read_b128 a[120:123],v114 offset:14336\n"
"ds_read_b128 a[124:127],v114 offset:15360\n"
"ds_read_b128 a[32:35],v116 offset:24576\n"
"ds_read_b128 a[36:39],v116 offset:25600\n"
"ds_read_b128 a[40:43],v116 offset:26624\n"
"v_mfma_f32_16x16x32_bf16 v[56:59],a[76:79],a[60:63],v[56:59]\n"
"s_add_u32 s54,s54,s22\n"
"ds_read_b128 a[44:47],v116 offset:27648\n"
"v_exp_f32 v18,v18\n"
"v_mfma_f32_16x16x32_bf16 v[60:63],a[92:95],a[60:63],v[60:63]\n"
"buffer_load_dwordx4 v115,s[32:35],s54 offen sc1 lds\n"
"v_cvt_pk_bf16_f32 v72,v16,v17\n"
"v_mfma_f32_16x16x32_bf16 v[60:63],a[88:91],a[56:59],v[60:63]\n"
"s_add_u32 m0,s67,32768\n"
"v_exp_f32 v19,v19\n"
"v_mfma_f32_16x16x32_bf16 v[60:63],a[84:87],a[52:55],v[60:63]\n"
"buffer_load_dwordx4 v112,s[24:27],s64 offen offset:0 sc1 lds\n"
"v_cvt_pk_bf16_f32 v73,v18,v19\n"
"v_exp_f32 v20,v20\n"
"v_mfma_f32_16x16x32_bf16 v[60:63],a[80:83],a[48:51],v[60:63]\n"
"ds_read_b128 a[64:67],v113 offset:0\n"
"ds_read_b128 a[68:71],v113 offset:1024\n"
"ds_read_b128 a[72:75],v113 offset:2048\n"
"s_waitcnt lgkmcnt(7)\n"
"v_mfma_f32_16x16x32_bf16 v[160:163],a[96:99],v[84:87],v[160:163]\n"
"ds_read_b128 a[76:79],v113 offset:3072\n"
"ds_read_b128 a[80:83],v113 offset:4096\n"
"ds_read_b128 a[84:87],v113 offset:5120\n"
"ds_read_b128 a[88:91],v113 offset:6144\n"
"ds_read_b128 a[92:95],v113 offset:7168\n"
"v_exp_f32 v21,v21\n"
"v_mfma_f32_16x16x32_bf16 a[132:135],a[144:147],v[84:87],a[132:135]\n"
"buffer_load_dwordx4 v112,s[24:27],s64 offen offset:1024 sc1 lds\n"
"v_max3_f32 v118,v56,v57,v58\n"
"v_mfma_f32_16x16x32_bf16 v[164:167],a[100:103],v[84:87],v[164:167]\n"
"v_exp_f32 v22,v22\n"
"v_cvt_pk_bf16_f32 v74,v20,v21\n"
"ds_read_b128 a[48:51],v116 offset:57344\n"
"ds_read_b128 a[52:55],v116 offset:58368\n"
"v_mfma_f32_16x16x32_bf16 v[168:171],a[104:107],v[84:87],v[168:171]\n"
"v_max3_f32 v118,v118,v59,v60\n"
"v_max3_f32 v118,v118,v61,v62\n"
"v_max_f32 v118,v118,v63\n"
"v_exp_f32 v23,v23\n"
"v_mfma_f32_16x16x32_bf16 v[172:175],a[108:111],v[84:87],v[172:175]\n"
"ds_read_b128 a[56:59],v116 offset:59392\n"
"buffer_load_dwordx4 v112,s[24:27],s64 offen offset:2048 sc1 lds\n"
"ds_read_b128 a[60:63],v116 offset:60416\n"
"v_mfma_f32_16x16x32_bf16 v[176:179],a[112:115],v[84:87],v[176:179]\n"
"s_waitcnt vmcnt(15)\n"
"v_cvt_pk_bf16_f32 v75,v22,v23\n"
"v_accvgpr_read_b32 v122,a128\n"
"v_rcp_f32 v122,v122\n"
"v_mfma_f32_16x16x32_bf16 v[180:183],a[116:119],v[84:87],v[180:183]\n"
"ds_add_u32 v124,a152\n"
"v_cmp_lt_f32 vcc,s93,v118\n"
"v_exp_f32 v24,v24\n"
"v_pk_mul_f32 v[128:129],v[128:129],v[122:123] op_sel_hi:[1,0]\n"
"v_pk_mul_f32 v[130:131],v[130:131],v[122:123] op_sel_hi:[1,0]\n"
"v_pk_mul_f32 v[132:133],v[132:133],v[122:123] op_sel_hi:[1,0]\n"
"v_pk_mul_f32 v[134:135],v[134:135],v[122:123] op_sel_hi:[1,0]\n"
"v_mfma_f32_16x16x32_bf16 v[184:187],a[120:123],v[84:87],v[184:187]\n"
"buffer_load_dwordx4 v112,s[24:27],s64 offen offset:3072 sc1 lds\n"
"v_cvt_pk_bf16_f32 v128,v128,v129\n"
"v_cvt_pk_bf16_f32 v129,v130,v131\n"
"v_mfma_f32_16x16x32_bf16 v[188:191],a[124:127],v[84:87],v[188:191]\n"
"s_add_u32 m0,s67,36864\n"
"v_cvt_pk_bf16_f32 v130,v132,v133\n"
"v_cvt_pk_bf16_f32 v131,v134,v135\n"
"v_exp_f32 v25,v25\n"
"v_mfma_f32_16x16x32_bf16 v[192:195],a[96:99],v[88:91],v[192:195]\n"
"buffer_store_dwordx4 v[128:131],v112,s[24:27],s70 offen offset:0 sc1\n"
"v_cvt_pk_bf16_f32 v76,v24,v25\n"
"v_pk_mul_f32 v[136:137],v[136:137],v[122:123] op_sel_hi:[1,0]\n"
"v_pk_mul_f32 v[138:139],v[138:139],v[122:123] op_sel_hi:[1,0]\n"
"v_pk_mul_f32 v[140:141],v[140:141],v[122:123] op_sel_hi:[1,0]\n"
"v_pk_mul_f32 v[142:143],v[142:143],v[122:123] op_sel_hi:[1,0]\n"
"v_exp_f32 v26,v26\n"
"v_mfma_f32_16x16x32_bf16 a[136:139],a[144:147],v[88:91],a[136:139]\n"
"buffer_load_dwordx4 v117,s[24:27],s64 offen offset:0 sc1 lds\n"
"v_cvt_pk_bf16_f32 v136,v136,v137\n"
"v_mfma_f32_16x16x32_bf16 v[196:199],a[100:103],v[88:91],v[196:199]\n"
"v_exp_f32 v27,v27\n"
"v_cvt_pk_bf16_f32 v137,v138,v139\n"
"v_cvt_pk_bf16_f32 v138,v140,v141\n"
"v_cvt_pk_bf16_f32 v139,v142,v143\n"
"v_cvt_pk_bf16_f32 v77,v26,v27\n"
"v_mfma_f32_16x16x32_bf16 v[200:203],a[104:107],v[88:91],v[200:203]\n"
"buffer_store_dwordx4 v[136:139],v112,s[24:27],s70 offen offset:1024 sc1\n"
"v_pk_mul_f32 v[144:145],v[144:145],v[122:123] op_sel_hi:[1,0]\n"
"v_pk_mul_f32 v[146:147],v[146:147],v[122:123] op_sel_hi:[1,0]\n"
"v_pk_mul_f32 v[148:149],v[148:149],v[122:123] op_sel_hi:[1,0]\n"
"v_pk_mul_f32 v[150:151],v[150:151],v[122:123] op_sel_hi:[1,0]\n"
"v_mfma_f32_16x16x32_bf16 v[204:207],a[108:111],v[88:91],v[204:207]\n"
"buffer_load_dwordx4 v117,s[24:27],s64 offen offset:1024 sc1 lds\n"
"v_exp_f32 v28,v28\n"
"v_mfma_f32_16x16x32_bf16 v[208:211],a[112:115],v[88:91],v[208:211]\n"
"v_cvt_pk_bf16_f32 v144,v144,v145\n"
"v_cvt_pk_bf16_f32 v145,v146,v147\n"
"v_cvt_pk_bf16_f32 v146,v148,v149\n"
"v_cvt_pk_bf16_f32 v147,v150,v151\n"
"v_mfma_f32_16x16x32_bf16 v[212:215],a[116:119],v[88:91],v[212:215]\n"
"buffer_store_dwordx4 v[144:147],v112,s[24:27],s70 offen offset:2048 sc1\n"
"v_pk_mul_f32 v[152:153],v[152:153],v[122:123] op_sel_hi:[1,0]\n"
"v_pk_mul_f32 v[154:155],v[154:155],v[122:123] op_sel_hi:[1,0]\n"
"v_pk_mul_f32 v[156:157],v[156:157],v[122:123] op_sel_hi:[1,0]\n"
"v_mfma_f32_16x16x32_bf16 v[216:219],a[120:123],v[88:91],v[216:219]\n"
"buffer_load_dwordx4 v117,s[24:27],s64 offen offset:2048 sc1 lds\n"
"v_exp_f32 v29,v29\n"
"v_cvt_pk_bf16_f32 v152,v152,v153\n"
"v_cvt_pk_bf16_f32 v78,v28,v29\n"
"v_pk_mul_f32 v[158:159],v[158:159],v[122:123] op_sel_hi:[1,0]\n"
"v_mfma_f32_16x16x32_bf16 v[220:223],a[124:127],v[88:91],v[220:223]\n"
"v_cvt_pk_bf16_f32 v153,v154,v155\n"
"v_cvt_pk_bf16_f32 v154,v156,v157\n"
"v_cvt_pk_bf16_f32 v155,v158,v159\n"
"v_mfma_f32_16x16x32_bf16 v[224:227],a[96:99],v[92:95],v[224:227]\n"
"ds_read_b32 v125,v124\n"
"buffer_store_dwordx4 v[152:155],v112,s[24:27],s70 offen offset:3072 sc1\n"
"buffer_load_dwordx4 v117,s[24:27],s64 offen offset:3072 sc1 lds\n"
"v_accvgpr_read_b32 v122,a132\n"
"v_mfma_f32_16x16x32_bf16 a[140:143],a[144:147],v[92:95],a[140:143]\n"
"s_add_u32 s70,s70,262144\n"
"v_exp_f32 v31,v31\n"
"v_mfma_f32_16x16x32_bf16 v[228:231],a[100:103],v[92:95],v[228:231]\n"
"s_add_u32 s64,s64,16384\n"
"v_exp_f32 v30,v30\n"
"v_rcp_f32 v122,v122\n"
"v_cvt_pk_bf16_f32 v79,v30,v31\n"
"v_pk_mul_f32 v[160:161],v[160:161],v[122:123] op_sel_hi:[1,0]\n"
"v_pk_mul_f32 v[162:163],v[162:163],v[122:123] op_sel_hi:[1,0]\n"
"v_pk_mul_f32 v[164:165],v[164:165],v[122:123] op_sel_hi:[1,0]\n"
"v_pk_mul_f32 v[166:167],v[166:167],v[122:123] op_sel_hi:[1,0]\n"
"v_mfma_f32_16x16x32_bf16 v[232:235],a[104:107],v[92:95],v[232:235]\n"
"buffer_store_dwordx4 a[252:255],v112,s[24:27],s56 offen offset:3072 sc1\n"
"v_cvt_pk_bf16_f32 v160,v160,v161\n"
"v_cvt_pk_bf16_f32 v161,v162,v163\n"
"v_cvt_pk_bf16_f32 v162,v164,v165\n"
"v_cvt_pk_bf16_f32 v163,v166,v167\n"
"v_mfma_f32_16x16x32_bf16 v[236:239],a[108:111],v[92:95],v[236:239]\n"
"buffer_store_dwordx4 v[160:163],v112,s[24:27],s70 offen offset:0 sc1\n"
"v_pk_mul_f32 v[168:169],v[168:169],v[122:123] op_sel_hi:[1,0]\n"
"v_pk_mul_f32 v[170:171],v[170:171],v[122:123] op_sel_hi:[1,0]\n"
"v_pk_mul_f32 v[172:173],v[172:173],v[122:123] op_sel_hi:[1,0]\n"
"v_pk_mul_f32 v[174:175],v[174:175],v[122:123] op_sel_hi:[1,0]\n"
"v_mfma_f32_16x16x32_bf16 v[240:243],a[112:115],v[92:95],v[240:243]\n"
"s_waitcnt lgkmcnt(0)\n"
"v_cvt_pk_bf16_f32 v168,v168,v169\n"
"v_cvt_pk_bf16_f32 v169,v170,v171\n"
"v_cvt_pk_bf16_f32 v170,v172,v173\n"
"v_cvt_pk_bf16_f32 v171,v174,v175\n"
"v_mfma_f32_16x16x32_bf16 v[244:247],a[116:119],v[92:95],v[244:247]\n"
"buffer_store_dwordx4 v[168:171],v112,s[24:27],s70 offen offset:1024 sc1\n"
"v_pk_mul_f32 v[176:177],v[176:177],v[122:123] op_sel_hi:[1,0]\n"
"v_pk_mul_f32 v[178:179],v[178:179],v[122:123] op_sel_hi:[1,0]\n"
"v_pk_mul_f32 v[180:181],v[180:181],v[122:123] op_sel_hi:[1,0]\n"
"v_pk_mul_f32 v[182:183],v[182:183],v[122:123] op_sel_hi:[1,0]\n"
"v_mfma_f32_16x16x32_bf16 v[248:251],a[120:123],v[92:95],v[248:251]\n"
"v_cvt_pk_bf16_f32 v176,v176,v177\n"
"v_cvt_pk_bf16_f32 v177,v178,v179\n"
"v_cvt_pk_bf16_f32 v178,v180,v181\n"
"v_cvt_pk_bf16_f32 v179,v182,v183\n"
"v_mfma_f32_16x16x32_bf16 v[252:255],a[124:127],v[92:95],v[252:255]\n"
"buffer_store_dwordx4 v[176:179],v112,s[24:27],s70 offen offset:2048 sc1\n"
"v_pk_mul_f32 v[184:185],v[184:185],v[122:123] op_sel_hi:[1,0]\n"
"v_pk_mul_f32 v[186:187],v[186:187],v[122:123] op_sel_hi:[1,0]\n"
"v_pk_mul_f32 v[188:189],v[188:189],v[122:123] op_sel_hi:[1,0]\n"
"v_pk_mul_f32 v[190:191],v[190:191],v[122:123] op_sel_hi:[1,0]\n"
"v_cvt_pk_bf16_f32 v184,v184,v185\n"
"v_cvt_pk_bf16_f32 v185,v186,v187\n"
"v_cvt_pk_bf16_f32 v186,v188,v189\n"
"v_cvt_pk_bf16_f32 v187,v190,v191\n"
"buffer_store_dwordx4 v[184:187],v112,s[24:27],s70 offen offset:3072 sc1\n"
"s_add_u32 s70,s70,262144\n"
"s_cbranch_vccnz .Lfk_cs_u2\n"
".Lfk_cr_u2:\n"
"v_cmp_gt_u32 vcc,s101,v125\n"
"s_nop 0\n"
"s_cbranch_vccnz .Lfk_cs_mu2\n"
".Lfk_cr_mu2:\n"
"s_add_u32 s89,s89,1\n"
"s_cmp_eq_u32 s89,8\n"
"s_cbranch_scc1 .Lfk_tailB7\n"
".Lfk_winB:\n"
"v_mfma_f32_16x16x32_bf16 v[0:3],a[64:67],a[0:3],v[96:99]\n"
"s_swappc_b64 s[94:95],s[20:21]\n"
"ds_read_b128 a[96:99],v114 offset:24576\n"
"v_mfma_f32_16x16x32_bf16 v[0:3],a[68:71],a[4:7],v[0:3]\n"
"s_add_u32 s101,s101,4\n"
"ds_read_b128 a[100:103],v114 offset:25600\n"
"ds_read_b128 a[104:107],v114 offset:26624\n"
"ds_read_b128 a[108:111],v114 offset:27648\n"
"v_mfma_f32_16x16x32_bf16 v[0:3],a[72:75],a[8:11],v[0:3]\n"
"ds_read_b128 a[112:115],v114 offset:28672\n"
"ds_read_b128 a[116:119],v114 offset:29696\n"
"v_mfma_f32_16x16x32_bf16 v[0:3],a[76:79],a[12:15],v[0:3]\n"
"ds_read_b128 a[120:123],v114 offset:30720\n"
"ds_read_b128 a[124:127],v114 offset:31744\n"
"v_mfma_f32_16x16x32_bf16 v[4:7],a[92:95],a[12:15],v[96:99]\n"
"v_exp_f32 v56,v56\n"
"v_mfma_f32_16x16x32_bf16 v[4:7],a[88:91],a[8:11],v[4:7]\n"
"v_exp_f32 v57,v57\n"
"v_mfma_f32_16x16x32_bf16 v[4:7],a[84:87],a[4:7],v[4:7]\n"
"v_cvt_pk_bf16_f32 v92,v56,v57\n"
"v_mfma_f32_16x16x32_bf16 v[4:7],a[80:83],a[0:3],v[4:7]\n"
"s_add_u32 m0,s67,0\n"
"s_waitcnt lgkmcnt(0)\n"
"v_mfma_f32_16x16x32_bf16 v[192:195],a[96:99],v[72:75],v[192:195]\n"
"buffer_load_dwordx4 v112,s[24:27],s64 offen offset:0 sc1 lds\n"
"v_mfma_f32_16x16x32_bf16 a[136:139],a[144:147],v[72:75],a[136:139]\n"
"buffer_load_dwordx4 v112,s[24:27],s64 offen offset:1024 sc1 lds\n"
"v_mfma_f32_16x16x32_bf16 v[196:199],a[100:103],v[72:75],v[196:199]\n"
"v_exp_f32 v58,v58\n"
"v_mfma_f32_16x16x32_bf16 v[200:203],a[104:107],v[72:75],v[200:203]\n"
"v_max3_f32 v118,v0,v1,v2\n"
"v_mfma_f32_16x16x32_bf16 v[204:207],a[108:111],v[72:75],v[204:207]\n"
"v_max3_f32 v118,v118,v3,v4\n"
"v_max3_f32 v118,v118,v5,v6\n"
"v_mfma_f32_16x16x32_bf16 v[208:211],a[112:115],v[72:75],v[208:211]\n"
"v_max_f32 v118,v118,v7\n"
"v_mov_b32 v120,v118\n"
"v_mfma_f32_16x16x32_bf16 v[212:215],a[116:119],v[72:75],v[212:215]\n"
"v_mfma_f32_16x16x32_bf16 v[216:219],a[120:123],v[72:75],v[216:219]\n"
"v_mfma_f32_16x16x32_bf16 v[220:223],a[124:127],v[72:75],v[220:223]\n"
"v_exp_f32 v59,v59\n"
"v_mfma_f32_16x16x32_bf16 v[224:227],a[96:99],v[76:79],v[224:227]\n"
"v_cvt_pk_bf16_f32 v93,v58,v59\n"
"v_accvgpr_read_b32 v122,a136\n"
"v_mfma_f32_16x16x32_bf16 a[140:143],a[144:147],v[76:79],a[140:143]\n"
"buffer_load_dwordx4 v112,s[24:27],s64 offen offset:2048 sc1 lds\n"
"v_mfma_f32_16x16x32_bf16 v[228:231],a[100:103],v[76:79],v[228:231]\n"
"v_rcp_f32 v122,v122\n"
"v_mfma_f32_16x16x32_bf16 v[232:235],a[104:107],v[76:79],v[232:235]\n"
"buffer_load_dwordx4 v112,s[24:27],s64 offen offset:3072 sc1 lds\n"
"v_pk_mul_f32 v[192:193],v[192:193],v[122:123] op_sel_hi:[1,0]\n"
"v_pk_mul_f32 v[194:195],v[194:195],v[122:123] op_sel_hi:[1,0]\n"
"v_mfma_f32_16x16x32_bf16 v[236:239],a[108:111],v[76:79],v[236:239]\n"
"v_cvt_pk_bf16_f32 v192,v192,v193\n"
"v_exp_f32 v60,v60\n"
"v_cvt_pk_bf16_f32 v193,v194,v195\n"
"v_pk_mul_f32 v[196:197],v[196:197],v[122:123] op_sel_hi:[1,0]\n"
"v_pk_mul_f32 v[198:199],v[198:199],v[122:123] op_sel_hi:[1,0]\n"
"v_mfma_f32_16x16x32_bf16 v[240:243],a[112:115],v[76:79],v[240:243]\n"
"s_add_u32 m0,s67,4096\n"
"v_cvt_pk_bf16_f32 v194,v196,v197\n"
"v_cvt_pk_bf16_f32 v195,v198,v199\n"
"v_mfma_f32_16x16x32_bf16 v[244:247],a[116:119],v[76:79],v[244:247]\n"
"v_mfma_f32_16x16x32_bf16 v[248:251],a[120:123],v[76:79],v[248:251]\n"
"buffer_store_dwordx4 v[192:195],v112,s[24:27],s70 offen offset:0 sc1\n"
"v_pk_mul_f32 v[200:201],v[200:201],v[122:123] op_sel_hi:[1,0]\n"
"v_mfma_f32_16x16x32_bf16 v[252:255],a[124:127],v[76:79],v[252:255]\n"
"buffer_load_dwordx4 v117,s[24:27],s64 offen offset:0 sc1 lds\n"
"v_cvt_pk_bf16_f32 v200,v200,v201\n"
"v_exp_f32 v61,v61\n"
"v_pk_mul_f32 v[202:203],v[202:203],v[122:123] op_sel_hi:[1,0]\n"
"v_pk_mul_f32 v[204:205],v[204:205],v[122:123] op_sel_hi:[1,0]\n"
"v_pk_mul_f32 v[206:207],v[206:207],v[122:123] op_sel_hi:[1,0]\n"
"v_mfma_f32_16x16x32_bf16 v[12:15],a[80:83],a[16:19],v[100:103]\n"
"v_cvt_pk_bf16_f32 v201,v202,v203\n"
"v_cvt_pk_bf16_f32 v202,v204,v205\n"
"v_mfma_f32_16x16x32_bf16 v[12:15],a[84:87],a[20:23],v[12:15]\n"
"v_cvt_pk_bf16_f32 v203,v206,v207\n"
"v_cvt_pk_bf16_f32 v94,v60,v61\n"
"v_mfma_f32_16x16x32_bf16 v[12:15],a[88:91],a[24:27],v[12:15]\n"
"buffer_load_dwordx4 v117,s[24:27],s64 offen offset:1024 sc1 lds\n"
"v_mfma_f32_16x16x32_bf16 v[12:15],a[92:95],a[28:31],v[12:15]\n"
"buffer_store_dwordx4 v[200:203],v112,s[24:27],s70 offen offset:1024 sc1\n"
"v_pk_mul_f32 v[208:209],v[208:209],v[122:123] op_sel_hi:[1,0]\n"
"v_pk_mul_f32 v[210:211],v[210:211],v[122:123] op_sel_hi:[1,0]\n"
"v_mfma_f32_16x16x32_bf16 v[8:11],a[76:79],a[28:31],v[100:103]\n"
"buffer_load_dwordx4 v117,s[24:27],s64 offen offset:2048 sc1 lds\n"
"v_cvt_pk_bf16_f32 v208,v208,v209\n"
"v_cvt_pk_bf16_f32 v209,v210,v211\n"
"v_pk_mul_f32 v[212:213],v[212:213],v[122:123] op_sel_hi:[1,0]\n"
"v_pk_mul_f32 v[214:215],v[214:215],v[122:123] op_sel_hi:[1,0]\n"
"v_mfma_f32_16x16x32_bf16 v[8:11],a[72:75],a[24:27],v[8:11]\n"
"v_cvt_pk_bf16_f32 v210,v212,v213\n"
"v_cvt_pk_bf16_f32 v211,v214,v215\n"
"v_mfma_f32_16x16x32_bf16 v[8:11],a[68:71],a[20:23],v[8:11]\n"
"v_exp_f32 v62,v62\n"
"v_mfma_f32_16x16x32_bf16 v[8:11],a[64:67],a[16:19],v[8:11]\n"
"buffer_store_dwordx4 v[208:211],v112,s[24:27],s70 offen offset:2048 sc1\n"
"v_pk_mul_f32 v[216:217],v[216:217],v[122:123] op_sel_hi:[1,0]\n"
"v_mfma_f32_16x16x32_bf16 v[16:19],a[64:67],a[32:35],v[104:107]\n"
"v_cvt_pk_bf16_f32 v216,v216,v217\n"
"buffer_load_dwordx4 v117,s[24:27],s64 offen offset:3072 sc1 lds\n"
"v_pk_mul_f32 v[218:219],v[218:219],v[122:123] op_sel_hi:[1,0]\n"
"v_pk_mul_f32 v[220:221],v[220:221],v[122:123] op_sel_hi:[1,0]\n"
"v_pk_mul_f32 v[222:223],v[222:223],v[122:123] op_sel_hi:[1,0]\n"
"v_mfma_f32_16x16x32_bf16 v[16:19],a[68:71],a[36:39],v[16:19]\n"
"v_cvt_pk_bf16_f32 v217,v218,v219\n"
"v_cvt_pk_bf16_f32 v218,v220,v221\n"
"v_mfma_f32_16x16x32_bf16 v[16:19],a[72:75],a[40:43],v[16:19]\n"
"s_add_u32 s64,s64,16384\n"
"v_exp_f32 v63,v63\n"
"v_mfma_f32_16x16x32_bf16 v[16:19],a[76:79],a[44:47],v[16:19]\n"
"s_andn2_b32 m0,s55,16384\n"
"s_add_u32 s54,s54,s22\n"
"v_max3_f32 v119,v8,v9,v10\n"
"v_mfma_f32_16x16x32_bf16 v[20:23],a[92:95],a[44:47],v[104:107]\n"
"v_max3_f32 v119,v119,v11,v12\n"
"v_cvt_pk_bf16_f32 v95,v62,v63\n"
"v_mfma_f32_16x16x32_bf16 v[20:23],a[88:91],a[40:43],v[20:23]\n"
"buffer_load_dwordx4 v115,s[32:35],s54 offen sc1 lds\n"
"v_mfma_f32_16x16x32_bf16 v[20:23],a[84:87],a[36:39],v[20:23]\n"
"v_max3_f32 v119,v119,v13,v14\n"
"v_max_f32 v119,v119,v15\n"
"v_mfma_f32_16x16x32_bf16 v[20:23],a[80:83],a[32:35],v[20:23]\n"
"v_cvt_pk_bf16_f32 v219,v222,v223\n"
"v_mfma_f32_16x16x32_bf16 v[28:31],a[80:83],a[48:51],v[108:111]\n"
"buffer_store_dwordx4 v[216:219],v112,s[24:27],s70 offen offset:3072 sc1\n"
"buffer_store_dwordx4 a[240:243],v112,s[24:27],s56 offen offset:0 sc1\n"
"v_mfma_f32_16x16x32_bf16 v[28:31],a[84:87],a[52:55],v[28:31]\n"
"s_add_u32 s70,s70,262144\n"
"v_max_f32 v118,v118,v119\n"
"v_min_f32 v120,v120,v119\n"
"v_mfma_f32_16x16x32_bf16 v[28:31],a[88:91],a[56:59],v[28:31]\n"
"v_max3_f32 v119,v16,v17,v18\n"
"v_mfma_f32_16x16x32_bf16 v[28:31],a[92:95],a[60:63],v[28:31]\n"
"v_max3_f32 v119,v119,v19,v20\n"
"v_max3_f32 v119,v119,v21,v22\n"
"v_mfma_f32_16x16x32_bf16 v[24:27],a[76:79],a[60:63],v[108:111]\n"
"v_max_f32 v119,v119,v23\n"
"v_max_f32 v118,v118,v119\n"
"v_mfma_f32_16x16x32_bf16 v[24:27],a[72:75],a[56:59],v[24:27]\n"
"v_min_f32 v120,v120,v119\n"
"v_mfma_f32_16x16x32_bf16 v[24:27],a[68:71],a[52:55],v[24:27]\n"
"v_mfma_f32_16x16x32_bf16 v[24:27],a[64:67],a[48:51],v[24:27]\n"
"ds_read_b128 a[64:67],v113 offset:16384\n"
"ds_read_b128 a[68:71],v113 offset:17408\n"
"ds_read_b128 a[72:75],v113 offset:18432\n"
"ds_read_b128 a[76:79],v113 offset:19456\n"
"ds_read_b128 a[80:83],v113 offset:20480\n"
"ds_read_b128 a[84:87],v113 offset:21504\n"
"ds_read_b128 a[88:91],v113 offset:22528\n"
"ds_read_b128 a[92:95],v113 offset:23552\n"
"v_max3_f32 v119,v24,v25,v26\n"
"v_max3_f32 v119,v119,v27,v28\n"
"v_max3_f32 v119,v119,v29,v30\n"
"v_max_f32 v119,v119,v31\n"
"v_min_f32 v120,v120,v119\n"
"v_max_f32 v118,v118,v119\n"
"v_cmp_gt_f32 s[96:97],s16,v120\n"
"v_cmp_lt_f32 vcc,s93,v118\n"
"s_waitcnt vmcnt(25)\n"
"ds_add_u32 v124,a152\n"
"ds_read_b32 v125,v124\n"
"s_waitcnt lgkmcnt(0)\n"
"s_or_b64 s[96:97],s[96:97],vcc\n"
"s_cbranch_scc1 .Lfk_cs_v3\n"
".Lfk_cr_v3:\n"
"v_cmp_gt_u32 vcc,s101,v125\n"
"s_nop 0\n"
"s_cbranch_vccnz .Lfk_cs_mv3\n"
".Lfk_cr_mv3:\n"
"v_exp_f32 v0,v0\n"
"v_mfma_f32_16x16x32_bf16 v[32:35],a[64:67],a[0:3],v[96:99]\n"
"s_add_u32 s101,s101,4\n"
"ds_read_b128 a[96:99],v114 offset:57344\n"
"ds_read_b128 a[100:103],v114 offset:58368\n"
"v_exp_f32 v1,v1\n"
"v_mfma_f32_16x16x32_bf16 v[32:35],a[68:71],a[4:7],v[32:35]\n"
"s_add_u32 m0,s55,4096\n"
"ds_read_b128 a[104:107],v114 offset:59392\n"
"ds_read_b128 a[108:111],v114 offset:60416\n"
"v_exp_f32 v2,v2\n"
"v_mfma_f32_16x16x32_bf16 v[32:35],a[72:75],a[8:11],v[32:35]\n"
"s_add_u32 s54,s54,s22\n"
"ds_read_b128 a[112:115],v114 offset:61440\n"
"ds_read_b128 a[116:119],v114 offset:62464\n"
"v_exp_f32 v3,v3\n"
"v_mfma_f32_16x16x32_bf16 v[32:35],a[76:79],a[12:15],v[32:35]\n"
"ds_read_b128 a[120:123],v114 offset:63488\n"
"ds_read_b128 a[124:127],v114 offset:64512\n"
"v_exp_f32 v4,v4\n"
"v_mfma_f32_16x16x32_bf16 v[36:39],a[92:95],a[12:15],v[96:99]\n"
"buffer_load_dwordx4 v115,s[32:35],s54 offen sc1 lds\n"
"v_mfma_f32_16x16x32_bf16 v[36:39],a[88:91],a[8:11],v[36:39]\n"
"s_add_u32 m0,s67,16384\n"
"v_exp_f32 v5,v5\n"
"v_cvt_pk_bf16_f32 v64,v0,v1\n"
"v_cvt_pk_bf16_f32 v65,v2,v3\n"
"v_mfma_f32_16x16x32_bf16 v[36:39],a[84:87],a[4:7],v[36:39]\n"
"v_exp_f32 v6,v6\n"
"v_exp_f32 v7,v7\n"
"v_mfma_f32_16x16x32_bf16 v[36:39],a[80:83],a[0:3],v[36:39]\n"
"buffer_load_dwordx4 v112,s[24:27],s64 offen offset:0 sc1 lds\n"
"s_waitcnt lgkmcnt(0)\n"
"v_mfma_f32_16x16x32_bf16 v[224:227],a[96:99],v[92:95],v[224:227]\n"
"v_exp_f32 v8,v8\n"
"v_cvt_pk_bf16_f32 v67,v6,v7\n"
"v_mfma_f32_16x16x32_bf16 a[140:143],a[144:147],v[92:95],a[140:143]\n"
"v_exp_f32 v9,v9\n"
"v_exp_f32 v10,v10\n"
"v_mfma_f32_16x16x32_bf16 v[228:231],a[100:103],v[92:95],v[228:231]\n"
"buffer_load_dwordx4 v112,s[24:27],s64 offen offset:1024 sc1 lds\n"
"v_mfma_f32_16x16x32_bf16 v[232:235],a[104:107],v[92:95],v[232:235]\n"
"v_cvt_pk_bf16_f32 v66,v4,v5\n"
"v_cvt_pk_bf16_f32 v68,v8,v9\n"
"v_mfma_f32_16x16x32_bf16 v[236:239],a[108:111],v[92:95],v[236:239]\n"
"v_exp_f32 v11,v11\n"
"v_exp_f32 v12,v12\n"
"v_mfma_f32_16x16x32_bf16 v[240:243],a[112:115],v[92:95],v[240:243]\n"
"buffer_load_dwordx4 v112,s[24:27],s64 offen offset:2048 sc1 lds\n"
"v_mfma_f32_16x16x32_bf16 v[244:247],a[116:119],v[92:95],v[244:247]\n"
"v_exp_f32 v13,v13\n"
"v_cvt_pk_bf16_f32 v69,v10,v11\n"
"v_mfma_f32_16x16x32_bf16 v[248:251],a[120:123],v[92:95],v[248:251]\n"
"v_exp_f32 v14,v14\n"
"v_cvt_pk_bf16_f32 v70,v12,v13\n"
"v_exp_f32 v15,v15\n"
"v_mfma_f32_16x16x32_bf16 v[252:255],a[124:127],v[92:95],v[252:255]\n"
"buffer_load_dwordx4 v112,s[24:27],s64 offen offset:3072 sc1 lds\n"
"v_mfma_f32_16x16x32_bf16 v[44:47],a[80:83],a[16:19],v[100:103]\n"
"s_add_u32 m0,s67,20480\n"
"v_exp_f32 v16,v16\n"
"v_cvt_pk_bf16_f32 v71,v14,v15\n"
"v_mfma_f32_16x16x32_bf16 v[44:47],a[84:87],a[20:23],v[44:47]\n"
"v_exp_f32 v17,v17\n"
"v_exp_f32 v18,v18\n"
"v_mfma_f32_16x16x32_bf16 v[44:47],a[88:91],a[24:27],v[44:47]\n"
"buffer_load_dwordx4 v117,s[24:27],s64 offen offset:0 sc1 lds\n"
"v_mfma_f32_16x16x32_bf16 v[44:47],a[92:95],a[28:31],v[44:47]\n"
"v_exp_f32 v19,v19\n"
"v_accvgpr_read_b32 v122,a140\n"
"v_mfma_f32_16x16x32_bf16 v[40:43],a[76:79],a[28:31],v[100:103]\n"
"v_cvt_pk_bf16_f32 v73,v18,v19\n"
"v_rcp_f32 v122,v122\n"
"v_mfma_f32_16x16x32_bf16 v[40:43],a[72:75],a[24:27],v[40:43]\n"
"buffer_load_dwordx4 v117,s[24:27],s64 offen offset:1024 sc1 lds\n"
"v_mfma_f32_16x16x32_bf16 v[40:43],a[68:71],a[20:23],v[40:43]\n"
"v_exp_f32 v21,v21\n"
"v_exp_f32 v20,v20\n"
"v_cvt_pk_bf16_f32 v72,v16,v17\n"
"v_pk_mul_f32 v[224:225],v[224:225],v[122:123] op_sel_hi:[1,0]\n"
"v_pk_mul_f32 v[226:227],v[226:227],v[122:123] op_sel_hi:[1,0]\n"
"v_pk_mul_f32 v[228:229],v[228:229],v[122:123] op_sel_hi:[1,0]\n"
"v_pk_mul_f32 v[230:231],v[230:231],v[122:123] op_sel_hi:[1,0]\n"
"v_mfma_f32_16x16x32_bf16 v[40:43],a[64:67],a[16:19],v[40:43]\n"
"v_cvt_pk_bf16_f32 v74,v20,v21\n"
"v_exp_f32 v22,v22\n"
"v_cvt_pk_bf16_f32 v224,v224,v225\n"
"v_exp_f32 v23,v23\n"
"v_mfma_f32_16x16x32_bf16 v[48:51],a[64:67],a[32:35],v[104:107]\n"
"buffer_load_dwordx4 v117,s[24:27],s64 offen offset:2048 sc1 lds\n"
"v_mfma_f32_16x16x32_bf16 v[48:51],a[68:71],a[36:39],v[48:51]\n"
"v_exp_f32 v24,v24\n"
"v_cvt_pk_bf16_f32 v225,v226,v227\n"
"v_mfma_f32_16x16x32_bf16 v[48:51],a[72:75],a[40:43],v[48:51]\n"
"v_exp_f32 v25,v25\n"
"v_cvt_pk_bf16_f32 v226,v228,v229\n"
"v_cvt_pk_bf16_f32 v227,v230,v231\n"
"v_mfma_f32_16x16x32_bf16 v[48:51],a[76:79],a[44:47],v[48:51]\n"
"buffer_load_dwordx4 v117,s[24:27],s64 offen offset:3072 sc1 lds\n"
"v_mfma_f32_16x16x32_bf16 v[52:55],a[92:95],a[44:47],v[104:107]\n"
"buffer_store_dwordx4 v[224:227],v112,s[24:27],s70 offen offset:0 sc1\n"
"v_exp_f32 v27,v27\n"
"v_mfma_f32_16x16x32_bf16 v[52:55],a[88:91],a[40:43],v[52:55]\n"
"s_add_u32 s64,s64,16384\n"
"v_exp_f32 v26,v26\n"
"v_exp_f32 v28,v28\n"
"v_cvt_pk_bf16_f32 v77,v26,v27\n"
"v_pk_mul_f32 v[232:233],v[232:233],v[122:123] op_sel_hi:[1,0]\n"
"v_mfma_f32_16x16x32_bf16 v[52:55],a[84:87],a[36:39],v[52:55]\n"
"v_cvt_pk_bf16_f32 v75,v22,v23\n"
"v_cvt_pk_bf16_f32 v76,v24,v25\n"
"v_exp_f32 v29,v29\n"
"v_exp_f32 v31,v31\n"
"v_pk_mul_f32 v[234:235],v[234:235],v[122:123] op_sel_hi:[1,0]\n"
"v_pk_mul_f32 v[236:237],v[236:237],v[122:123] op_sel_hi:[1,0]\n"
"v_pk_mul_f32 v[238:239],v[238:239],v[122:123] op_sel_hi:[1,0]\n"
"v_mfma_f32_16x16x32_bf16 v[52:55],a[80:83],a[32:35],v[52:55]\n"
"v_exp_f32 v30,v30\n"
"v_cvt_pk_bf16_f32 v78,v28,v29\n"
"v_mfma_f32_16x16x32_bf16 v[60:63],a[80:83],a[48:51],v[108:111]\n"
"v_cvt_pk_bf16_f32 v232,v232,v233\n"
"v_cvt_pk_bf16_f32 v233,v234,v235\n"
"v_mfma_f32_16x16x32_bf16 v[60:63],a[84:87],a[52:55],v[60:63]\n"
"buffer_store_dwordx4 a[244:247],v112,s[24:27],s56 offen offset:1024 sc1\n"
"v_cvt_pk_bf16_f32 v234,v236,v237\n"
"v_cvt_pk_bf16_f32 v235,v238,v239\n"
"v_mfma_f32_16x16x32_bf16 v[60:63],a[88:91],a[56:59],v[60:63]\n"
"buffer_store_dwordx4 v[232:235],v112,s[24:27],s70 offen offset:1024 sc1\n"
"v_pk_mul_f32 v[240:241],v[240:241],v[122:123] op_sel_hi:[1,0]\n"
"v_pk_mul_f32 v[242:243],v[242:243],v[122:123] op_sel_hi:[1,0]\n"
"v_pk_mul_f32 v[244:245],v[244:245],v[122:123] op_sel_hi:[1,0]\n"
"v_pk_mul_f32 v[246:247],v[246:247],v[122:123] op_sel_hi:[1,0]\n"
"v_mfma_f32_16x16x32_bf16 v[60:63],a[92:95],a[60:63],v[60:63]\n"
"v_cvt_pk_bf16_f32 v240,v240,v241\n"
"v_cvt_pk_bf16_f32 v241,v242,v243\n"
"v_cvt_pk_bf16_f32 v242,v244,v245\n"
"v_cvt_pk_bf16_f32 v243,v246,v247\n"
"v_mfma_f32_16x16x32_bf16 v[56:59],a[76:79],a[60:63],v[108:111]\n"
"buffer_store_dwordx4 v[240:243],v112,s[24:27],s70 offen offset:2048 sc1\n"
"v_pk_mul_f32 v[248:249],v[248:249],v[122:123] op_sel_hi:[1,0]\n"
"v_pk_mul_f32 v[250:251],v[250:251],v[122:123] op_sel_hi:[1,0]\n"
"v_mfma_f32_16x16x32_bf16 v[56:59],a[72:75],a[56:59],v[56:59]\n"
"v_cvt_pk_bf16_f32 v79,v30,v31\n"
"v_cvt_pk_bf16_f32 v248,v248,v249\n"
"v_cvt_pk_bf16_f32 v249,v250,v251\n"
"v_pk_mul_f32 v[252:253],v[252:253],v[122:123] op_sel_hi:[1,0]\n"
"v_pk_mul_f32 v[254:255],v[254:255],v[122:123] op_sel_hi:[1,0]\n"
"v_mfma_f32_16x16x32_bf16 v[56:59],a[68:71],a[52:55],v[56:59]\n"
"v_cvt_pk_bf16_f32 v250,v252,v253\n"
"v_cvt_pk_bf16_f32 v251,v254,v255\n"
"v_mfma_f32_16x16x32_bf16 v[56:59],a[64:67],a[48:51],v[56:59]\n"
"buffer_store_dwordx4 v[248:251],v112,s[24:27],s70 offen offset:3072 sc1\n"
"s_add_u32 s70,s70,262144\n"
"ds_read_b128 a[64:67],v113 offset:49152\n"
"ds_read_b128 a[68:71],v113 offset:50176\n"
"ds_read_b128 a[72:75],v113 offset:51200\n"
"ds_read_b128 a[76:79],v113 offset:52224\n"
"ds_read_b128 a[80:83],v113 offset:53248\n"
"ds_read_b128 a[84:87],v113 offset:54272\n"
"ds_read_b128 a[88:91],v113 offset:55296\n"
"ds_read_b128 a[92:95],v113 offset:56320\n"
"v_mov_b32 v114,v112\n"
"s_mov_b32 s70,s68\n"
"s_waitcnt vmcnt(25)\n"
"ds_add_u32 v124,a152\n"
"ds_read_b32 v125,v124\n"
"s_waitcnt lgkmcnt(0)\n"
"v_cmp_gt_u32 vcc,s101,v125\n"
"s_nop 0\n"
"s_cbranch_vccnz .Lfk_cs_mv0\n"
".Lfk_cr_mv0:\n"
"v_mfma_f32_16x16x32_bf16 v[0:3],a[64:67],a[0:3],v[96:99]\n"
"s_add_u32 s101,s101,4\n"
"v_exp_f32 v122,v32\n"
"v_mfma_f32_16x16x32_bf16 v[0:3],a[68:71],a[4:7],v[0:3]\n"
"s_add_u32 m0,s55,8192\n"
"v_exp_f32 v123,v33\n"
"v_mfma_f32_16x16x32_bf16 v[0:3],a[72:75],a[8:11],v[0:3]\n"
"ds_read_b128 a[96:99],v114 offset:40960\n"
"ds_read_b128 a[100:103],v114 offset:41984\n"
"v_mfma_f32_16x16x32_bf16 v[0:3],a[76:79],a[12:15],v[0:3]\n"
"s_add_u32 s54,s54,s22\n"
"v_exp_f32 v119,v34\n"
"v_mfma_f32_16x16x32_bf16 v[4:7],a[92:95],a[12:15],v[96:99]\n"
"ds_read_b128 a[104:107],v114 offset:43008\n"
"v_cvt_pk_bf16_f32 v80,v122,v123\n"
"v_mfma_f32_16x16x32_bf16 v[4:7],a[88:91],a[8:11],v[4:7]\n"
"v_exp_f32 v120,v35\n"
"v_mfma_f32_16x16x32_bf16 v[4:7],a[84:87],a[4:7],v[4:7]\n"
"ds_read_b128 a[108:111],v114 offset:44032\n"
"ds_read_b128 a[112:115],v114 offset:45056\n"
"v_mfma_f32_16x16x32_bf16 v[4:7],a[80:83],a[0:3],v[4:7]\n"
"v_exp_f32 v122,v36\n"
"v_mfma_f32_16x16x32_bf16 v[12:15],a[80:83],a[16:19],v[100:103]\n"
"ds_read_b128 a[116:119],v114 offset:46080\n"
"v_cvt_pk_bf16_f32 v81,v119,v120\n"
"v_mfma_f32_16x16x32_bf16 v[12:15],a[84:87],a[20:23],v[12:15]\n"
"v_exp_f32 v123,v37\n"
"v_mfma_f32_16x16x32_bf16 v[12:15],a[88:91],a[24:27],v[12:15]\n"
"ds_read_b128 a[120:123],v114 offset:47104\n"
"ds_read_b128 a[124:127],v114 offset:48128\n"
"v_mfma_f32_16x16x32_bf16 v[12:15],a[92:95],a[28:31],v[12:15]\n"
"v_exp_f32 v119,v38\n"
"v_mfma_f32_16x16x32_bf16 v[8:11],a[76:79],a[28:31],v[100:103]\n"
"v_cvt_pk_bf16_f32 v82,v122,v123\n"
"v_or3_b32 v118,v80,v81,v82\n"
"v_mfma_f32_16x16x32_bf16 v[8:11],a[72:75],a[24:27],v[8:11]\n"
"v_exp_f32 v120,v39\n"
"v_mfma_f32_16x16x32_bf16 v[8:11],a[68:71],a[20:23],v[8:11]\n"
"buffer_load_dwordx4 v115,s[32:35],s54 offen sc1 lds\n"
"v_mfma_f32_16x16x32_bf16 v[8:11],a[64:67],a[16:19],v[8:11]\n"
"s_add_u32 m0,s67,49152\n"
"v_exp_f32 v122,v40\n"
"v_mfma_f32_16x16x32_bf16 v[16:19],a[64:67],a[32:35],v[104:107]\n"
"v_cvt_pk_bf16_f32 v83,v119,v120\n"
"v_mfma_f32_16x16x32_bf16 v[16:19],a[68:71],a[36:39],v[16:19]\n"
"v_exp_f32 v123,v41\n"
"v_mfma_f32_16x16x32_bf16 v[16:19],a[72:75],a[40:43],v[16:19]\n"
"buffer_load_dwordx4 v112,s[24:27],s64 offen offset:0 sc1 lds\n"
"v_mfma_f32_16x16x32_bf16 v[16:19],a[76:79],a[44:47],v[16:19]\n"
"v_exp_f32 v119,v42\n"
"v_mfma_f32_16x16x32_bf16 v[20:23],a[92:95],a[44:47],v[104:107]\n"
"v_cvt_pk_bf16_f32 v84,v122,v123\n"
"v_or3_b32 v118,v118,v83,v84\n"
"v_mfma_f32_16x16x32_bf16 v[20:23],a[88:91],a[40:43],v[20:23]\n"
"v_exp_f32 v120,v43\n"
"v_mfma_f32_16x16x32_bf16 v[20:23],a[84:87],a[36:39],v[20:23]\n"
"buffer_load_dwordx4 v112,s[24:27],s64 offen offset:1024 sc1 lds\n"
"v_mfma_f32_16x16x32_bf16 v[20:23],a[80:83],a[32:35],v[20:23]\n"
"v_exp_f32 v122,v44\n"
"v_mfma_f32_16x16x32_bf16 v[28:31],a[80:83],a[48:51],v[108:111]\n"
"v_cvt_pk_bf16_f32 v85,v119,v120\n"
"v_mfma_f32_16x16x32_bf16 v[28:31],a[84:87],a[52:55],v[28:31]\n"
"v_exp_f32 v123,v45\n"
"v_mfma_f32_16x16x32_bf16 v[28:31],a[88:91],a[56:59],v[28:31]\n"
"buffer_load_dwordx4 v112,s[24:27],s64 offen offset:2048 sc1 lds\n"
"v_mfma_f32_16x16x32_bf16 v[28:31],a[92:95],a[60:63],v[28:31]\n"
"v_exp_f32 v119,v46\n"
"v_mfma_f32_16x16x32_bf16 v[24:27],a[76:79],a[60:63],v[108:111]\n"
"v_cvt_pk_bf16_f32 v86,v122,v123\n"
"v_or3_b32 v118,v118,v85,v86\n"
"v_mfma_f32_16x16x32_bf16 v[24:27],a[72:75],a[56:59],v[24:27]\n"
"v_exp_f32 v120,v47\n"
"v_mfma_f32_16x16x32_bf16 v[24:27],a[68:71],a[52:55],v[24:27]\n"
"buffer_load_dwordx4 v112,s[24:27],s64 offen offset:3072 sc1 lds\n"
"v_mfma_f32_16x16x32_bf16 v[24:27],a[64:67],a[48:51],v[24:27]\n"
"s_waitcnt lgkmcnt(0)\n"
"v_exp_f32 v122,v48\n"
"v_mfma_f32_16x16x32_bf16 v[128:131],a[96:99],v[64:67],0\n"
"v_exp_f32 v123,v49\n"
"v_mfma_f32_16x16x32_bf16 v[160:163],a[96:99],v[68:71],0\n"
"ds_read_b128 a[64:67],v113 offset:32768\n"
"v_cvt_pk_bf16_f32 v87,v119,v120\n"
"v_mfma_f32_16x16x32_bf16 v[192:195],a[96:99],v[72:75],0\n"
"s_add_u32 m0,s67,53248\n"
"v_exp_f32 v119,v50\n"
"v_mfma_f32_16x16x32_bf16 v[224:227],a[96:99],v[76:79],0\n"
"ds_read_b128 a[68:71],v113 offset:33792\n"
"v_cvt_pk_bf16_f32 v88,v122,v123\n"
"v_mfma_f32_16x16x32_bf16 a[140:143],a[144:147],v[76:79],0\n"
"v_exp_f32 v120,v51\n"
"v_mfma_f32_16x16x32_bf16 v[228:231],a[100:103],v[76:79],0\n"
"ds_read_b128 a[72:75],v113 offset:34816\n"
"ds_read_b128 a[76:79],v113 offset:35840\n"
"v_mfma_f32_16x16x32_bf16 v[132:135],a[100:103],v[64:67],0\n"
"v_exp_f32 v122,v52\n"
"v_mfma_f32_16x16x32_bf16 v[164:167],a[100:103],v[68:71],0\n"
"ds_read_b128 a[80:83],v113 offset:36864\n"
"v_cvt_pk_bf16_f32 v89,v119,v120\n"
"v_mfma_f32_16x16x32_bf16 v[196:199],a[100:103],v[72:75],0\n"
"v_exp_f32 v123,v53\n"
"v_mfma_f32_16x16x32_bf16 a[136:139],a[144:147],v[72:75],0\n"
"ds_read_b128 a[84:87],v113 offset:37888\n"
"ds_read_b128 a[88:91],v113 offset:38912\n"
"v_mfma_f32_16x16x32_bf16 v[200:203],a[104:107],v[72:75],0\n"
"v_exp_f32 v119,v54\n"
"v_mfma_f32_16x16x32_bf16 v[232:235],a[104:107],v[76:79],0\n"
"ds_read_b128 a[92:95],v113 offset:39936\n"
"v_cvt_pk_bf16_f32 v90,v122,v123\n"
"v_mfma_f32_16x16x32_bf16 v[136:139],a[104:107],v[64:67],0\n"
"v_exp_f32 v120,v55\n"
"v_mfma_f32_16x16x32_bf16 v[168:171],a[104:107],v[68:71],0\n"
"s_waitcnt vmcnt(16)\n"
"ds_add_u32 v124,a152\n"
"v_mfma_f32_16x16x32_bf16 a[132:135],a[144:147],v[68:71],0\n"
"v_exp_f32 v122,v56\n"
"v_mfma_f32_16x16x32_bf16 v[172:175],a[108:111],v[68:71],0\n"
"v_cvt_pk_bf16_f32 v91,v119,v120\n"
"v_or3_b32 v118,v118,v87,v88\n"
"v_mfma_f32_16x16x32_bf16 v[204:207],a[108:111],v[72:75],0\n"
"v_exp_f32 v123,v57\n"
"v_mfma_f32_16x16x32_bf16 v[236:239],a[108:111],v[76:79],0\n"
"buffer_load_dwordx4 v117,s[24:27],s64 offen offset:0 sc1 lds\n"
"v_mfma_f32_16x16x32_bf16 v[140:143],a[108:111],v[64:67],0\n"
"v_exp_f32 v119,v58\n"
"v_mfma_f32_16x16x32_bf16 a[128:131],a[144:147],v[64:67],0\n"
"v_cvt_pk_bf16_f32 v92,v122,v123\n"
"v_or3_b32 v118,v118,v89,v90\n"
"v_mfma_f32_16x16x32_bf16 v[144:147],a[112:115],v[64:67],0\n"
"v_exp_f32 v120,v59\n"
"v_mfma_f32_16x16x32_bf16 v[176:179],a[112:115],v[68:71],0\n"
"buffer_load_dwordx4 v117,s[24:27],s64 offen offset:1024 sc1 lds\n"
"v_mfma_f32_16x16x32_bf16 v[208:211],a[112:115],v[72:75],0\n"
"v_exp_f32 v122,v60\n"
"v_mfma_f32_16x16x32_bf16 v[240:243],a[112:115],v[76:79],0\n"
"v_cvt_pk_bf16_f32 v93,v119,v120\n"
"v_or3_b32 v118,v118,v91,v92\n"
"v_mfma_f32_16x16x32_bf16 v[244:247],a[116:119],v[76:79],0\n"
"v_exp_f32 v123,v61\n"
"v_mfma_f32_16x16x32_bf16 v[148:151],a[116:119],v[64:67],0\n"
"buffer_load_dwordx4 v117,s[24:27],s64 offen offset:2048 sc1 lds\n"
"v_mfma_f32_16x16x32_bf16 v[180:183],a[116:119],v[68:71],0\n"
"v_exp_f32 v119,v62\n"
"v_mfma_f32_16x16x32_bf16 v[212:215],a[116:119],v[72:75],0\n"
"ds_read_b32 v125,v124\n"
"v_cvt_pk_bf16_f32 v94,v122,v123\n"
"v_mfma_f32_16x16x32_bf16 v[216:219],a[120:123],v[72:75],0\n"
"v_exp_f32 v120,v63\n"
"v_mfma_f32_16x16x32_bf16 v[248:251],a[120:123],v[76:79],0\n"
"buffer_load_dwordx4 v117,s[24:27],s64 offen offset:3072 sc1 lds\n"
"v_mfma_f32_16x16x32_bf16 v[152:155],a[120:123],v[64:67],0\n"
"buffer_store_dwordx4 a[248:251],v112,s[24:27],s56 offen offset:2048 sc1\n"
"v_mfma_f32_16x16x32_bf16 v[184:187],a[120:123],v[68:71],0\n"
"s_add_u32 s64,s64,16384\n"
"v_cvt_pk_bf16_f32 v95,v119,v120\n"
"v_or3_b32 v118,v118,v93,v94\n"
"v_mfma_f32_16x16x32_bf16 v[188:191],a[124:127],v[68:71],0\n"
"s_waitcnt lgkmcnt(0)\n"
"v_subrev_u32 v125,s101,v125\n"
"v_or3_b32 v125,v118,v95,v125\n"
"v_mfma_f32_16x16x32_bf16 v[220:223],a[124:127],v[72:75],0\n"
"v_and_b32 v125,0x40004000,v125\n"
"v_cmp_ne_u32 vcc,0,v125\n"
"v_mfma_f32_16x16x32_bf16 v[252:255],a[124:127],v[76:79],0\n"
"v_mfma_f32_16x16x32_bf16 v[156:159],a[124:127],v[64:67],0\n"
"s_cbranch_vccnz .Lfk_cs_v1\n"
".Lfk_cr_v1:\n"
"v_mfma_f32_16x16x32_bf16 v[32:35],a[64:67],a[0:3],v[96:99]\n"
"s_add_u32 s101,s101,4\n"
"v_exp_f32 v122,v0\n"
"v_mfma_f32_16x16x32_bf16 v[32:35],a[68:71],a[4:7],v[32:35]\n"
"s_add_u32 m0,s55,12288\n"
"v_exp_f32 v123,v1\n"
"v_mfma_f32_16x16x32_bf16 v[32:35],a[72:75],a[8:11],v[32:35]\n"
"ds_read_b128 a[96:99],v114 offset:8192\n"
"ds_read_b128 a[100:103],v114 offset:9216\n"
"v_mfma_f32_16x16x32_bf16 v[32:35],a[76:79],a[12:15],v[32:35]\n"
"s_add_u32 s54,s54,s22\n"
"v_exp_f32 v119,v2\n"
"v_mfma_f32_16x16x32_bf16 v[36:39],a[92:95],a[12:15],v[96:99]\n"
"ds_read_b128 a[104:107],v114 offset:10240\n"
"v_cvt_pk_bf16_f32 v64,v122,v123\n"
"v_mfma_f32_16x16x32_bf16 v[36:39],a[88:91],a[8:11],v[36:39]\n"
"v_exp_f32 v120,v3\n"
"v_mfma_f32_16x16x32_bf16 v[36:39],a[84:87],a[4:7],v[36:39]\n"
"ds_read_b128 a[108:111],v114 offset:11264\n"
"ds_read_b128 a[112:115],v114 offset:12288\n"
"v_mfma_f32_16x16x32_bf16 v[36:39],a[80:83],a[0:3],v[36:39]\n"
"v_exp_f32 v122,v4\n"
"v_mfma_f32_16x16x32_bf16 v[44:47],a[80:83],a[16:19],v[100:103]\n"
"ds_read_b128 a[116:119],v114 offset:13312\n"
"v_cvt_pk_bf16_f32 v65,v119,v120\n"
"v_mfma_f32_16x16x32_bf16 v[44:47],a[84:87],a[20:23],v[44:47]\n"
"v_exp_f32 v123,v5\n"
"v_mfma_f32_16x16x32_bf16 v[44:47],a[88:91],a[24:27],v[44:47]\n"
"ds_read_b128 a[120:123],v114 offset:14336\n"
"ds_read_b128 a[124:127],v114 offset:15360\n"
"v_mfma_f32_16x16x32_bf16 v[44:47],a[92:95],a[28:31],v[44:47]\n"
"v_exp_f32 v119,v6\n"
"v_mfma_f32_16x16x32_bf16 v[40:43],a[76:79],a[28:31],v[100:103]\n"
"v_cvt_pk_bf16_f32 v66,v122,v123\n"
"v_or3_b32 v118,v64,v65,v66\n"
"v_mfma_f32_16x16x32_bf16 v[40:43],a[72:75],a[24:27],v[40:43]\n"
"v_exp_f32 v120,v7\n"
"v_mfma_f32_16x16x32_bf16 v[40:43],a[68:71],a[20:23],v[40:43]\n"
"buffer_load_dwordx4 v115,s[32:35],s54 offen sc1 lds\n"
"v_mfma_f32_16x16x32_bf16 v[40:43],a[64:67],a[16:19],v[40:43]\n"
"s_add_u32 m0,s67,32768\n"
"v_exp_f32 v122,v8\n"
"v_mfma_f32_16x16x32_bf16 v[48:51],a[64:67],a[32:35],v[104:107]\n"
"v_cvt_pk_bf16_f32 v67,v119,v120\n"
"v_mfma_f32_16x16x32_bf16 v[48:51],a[68:71],a[36:39],v[48:51]\n"
"v_exp_f32 v123,v9\n"
"v_mfma_f32_16x16x32_bf16 v[48:51],a[72:75],a[40:43],v[48:51]\n"
"buffer_load_dwordx4 v112,s[24:27],s64 offen offset:0 sc1 lds\n"
"v_mfma_f32_16x16x32_bf16 v[48:51],a[76:79],a[44:47],v[48:51]\n"
"v_exp_f32 v119,v10\n"
"v_mfma_f32_16x16x32_bf16 v[52:55],a[92:95],a[44:47],v[104:107]\n"
"v_cvt_pk_bf16_f32 v68,v122,v123\n"
"v_or3_b32 v118,v118,v67,v68\n"
"v_mfma_f32_16x16x32_bf16 v[52:55],a[88:91],a[40:43],v[52:55]\n"
"v_exp_f32 v120,v11\n"
"v_mfma_f32_16x16x32_bf16 v[52:55],a[84:87],a[36:39],v[52:55]\n"
"buffer_load_dwordx4 v112,s[24:27],s64 offen offset:1024 sc1 lds\n"
"v_mfma_f32_16x16x32_bf16 v[52:55],a[80:83],a[32:35],v[52:55]\n"
"v_exp_f32 v122,v12\n"
"v_mfma_f32_16x16x32_bf16 v[60:63],a[80:83],a[48:51],v[108:111]\n"
"v_cvt_pk_bf16_f32 v69,v119,v120\n"
"v_mfma_f32_16x16x32_bf16 v[60:63],a[84:87],a[52:55],v[60:63]\n"
"v_exp_f32 v123,v13\n"
"v_mfma_f32_16x16x32_bf16 v[60:63],a[88:91],a[56:59],v[60:63]\n"
"buffer_load_dwordx4 v112,s[24:27],s64 offen offset:2048 sc1 lds\n"
"v_mfma_f32_16x16x32_bf16 v[60:63],a[92:95],a[60:63],v[60:63]\n"
"v_exp_f32 v119,v14\n"
"v_mfma_f32_16x16x32_bf16 v[56:59],a[76:79],a[60:63],v[108:111]\n"
"v_cvt_pk_bf16_f32 v70,v122,v123\n"
"v_or3_b32 v118,v118,v69,v70\n"
"v_mfma_f32_16x16x32_bf16 v[56:59],a[72:75],a[56:59],v[56:59]\n"
"v_exp_f32 v120,v15\n"
"v_mfma_f32_16x16x32_bf16 v[56:59],a[68:71],a[52:55],v[56:59]\n"
"buffer_load_dwordx4 v112,s[24:27],s64 offen offset:3072 sc1 lds\n"
"v_mfma_f32_16x16x32_bf16 v[56:59],a[64:67],a[48:51],v[56:59]\n"
"s_waitcnt lgkmcnt(0)\n"
"v_exp_f32 v122,v16\n"
"v_mfma_f32_16x16x32_bf16 v[128:131],a[96:99],v[80:83],v[128:131]\n"
"v_exp_f32 v123,v17\n"
"v_mfma_f32_16x16x32_bf16 v[160:163],a[96:99],v[84:87],v[160:163]\n"
"ds_read_b128 a[64:67],v113 offset:0\n"
"v_cvt_pk_bf16_f32 v71,v119,v120\n"
"v_mfma_f32_16x16x32_bf16 v[192:195],a[96:99],v[88:91],v[192:195]\n"
"s_add_u32 m0,s67,36864\n"
"v_exp_f32 v119,v18\n"
"v_mfma_f32_16x16x32_bf16 v[224:227],a[96:99],v[92:95],v[224:227]\n"
"ds_read_b128 a[68:71],v113 offset:1024\n"
"v_cvt_pk_bf16_f32 v72,v122,v123\n"
"v_mfma_f32_16x16x32_bf16 a[140:143],a[144:147],v[92:95],a[140:143]\n"
"v_exp_f32 v120,v19\n"
"v_mfma_f32_16x16x32_bf16 v[228:231],a[100:103],v[92:95],v[228:231]\n"
"ds_read_b128 a[72:75],v113 offset:2048\n"
"ds_read_b128 a[76:79],v113 offset:3072\n"
"v_mfma_f32_16x16x32_bf16 v[132:135],a[100:103],v[80:83],v[132:135]\n"
"v_exp_f32 v122,v20\n"
"v_mfma_f32_16x16x32_bf16 v[164:167],a[100:103],v[84:87],v[164:167]\n"
"ds_read_b128 a[80:83],v113 offset:4096\n"
"v_cvt_pk_bf16_f32 v73,v119,v120\n"
"v_mfma_f32_16x16x32_bf16 v[196:199],a[100:103],v[88:91],v[196:199]\n"
"v_exp_f32 v123,v21\n"
"v_mfma_f32_16x16x32_bf16 a[136:139],a[144:147],v[88:91],a[136:139]\n"
"ds_read_b128 a[84:87],v113 offset:5120\n"
"ds_read_b128 a[88:91],v113 offset:6144\n"
"v_mfma_f32_16x16x32_bf16 v[200:203],a[104:107],v[88:91],v[200:203]\n"
"v_exp_f32 v119,v22\n"
"v_mfma_f32_16x16x32_bf16 v[232:235],a[104:107],v[92:95],v[232:235]\n"
"ds_read_b128 a[92:95],v113 offset:7168\n"
"v_cvt_pk_bf16_f32 v74,v122,v123\n"
"v_mfma_f32_16x16x32_bf16 v[136:139],a[104:107],v[80:83],v[136:139]\n"
"v_exp_f32 v120,v23\n"
"v_mfma_f32_16x16x32_bf16 v[168:171],a[104:107],v[84:87],v[168:171]\n"
"s_waitcnt vmcnt(16)\n"
"ds_add_u32 v124,a152\n"
"v_mfma_f32_16x16x32_bf16 a[132:135],a[144:147],v[84:87],a[132:135]\n"
"v_exp_f32 v122,v24\n"
"v_mfma_f32_16x16x32_bf16 v[172:175],a[108:111],v[84:87],v[172:175]\n"
"v_cvt_pk_bf16_f32 v75,v119,v120\n"
"v_or3_b32 v118,v118,v71,v72\n"
"v_mfma_f32_16x16x32_bf16 v[204:207],a[108:111],v[88:91],v[204:207]\n"
"v_exp_f32 v123,v25\n"
"v_mfma_f32_16x16x32_bf16 v[236:239],a[108:111],v[92:95],v[236:239]\n"
"buffer_load_dwordx4 v117,s[24:27],s64 offen offset:0 sc1 lds\n"
"v_mfma_f32_16x16x32_bf16 v[140:143],a[108:111],v[80:83],v[140:143]\n"
"v_exp_f32 v119,v26\n"
"v_mfma_f32_16x16x32_bf16 a[128:131],a[144:147],v[80:83],a[128:131]\n"
"v_cvt_pk_bf16_f32 v76,v122,v123\n"
"v_or3_b32 v118,v118,v73,v74\n"
"v_mfma_f32_16x16x32_bf16 v[144:147],a[112:115],v[80:83],v[144:147]\n"
"v_exp_f32 v120,v27\n"
"v_mfma_f32_16x16x32_bf16 v[176:179],a[112:115],v[84:87],v[176:179]\n"
"buffer_load_dwordx4 v117,s[24:27],s64 offen offset:1024 sc1 lds\n"
"v_mfma_f32_16x16x32_bf16 v[208:211],a[112:115],v[88:91],v[208:211]\n"
"v_exp_f32 v122,v28\n"
"v_mfma_f32_16x16x32_bf16 v[240:243],a[112:115],v[92:95],v[240:243]\n"
"v_cvt_pk_bf16_f32 v77,v119,v120\n"
"v_or3_b32 v118,v118,v75,v76\n"
"v_mfma_f32_16x16x32_bf16 v[244:247],a[116:119],v[92:95],v[244:247]\n"
"v_exp_f32 v123,v29\n"
"v_mfma_f32_16x16x32_bf16 v[148:151],a[116:119],v[80:83],v[148:151]\n"
"buffer_load_dwordx4 v117,s[24:27],s64 offen offset:2048 sc1 lds\n"
"v_mfma_f32_16x16x32_bf16 v[180:183],a[116:119],v[84:87],v[180:183]\n"
"v_exp_f32 v119,v30\n"
"v_mfma_f32_16x16x32_bf16 v[212:215],a[116:119],v[88:91],v[212:215]\n"
"ds_read_b32 v125,v124\n"
"v_cvt_pk_bf16_f32 v78,v122,v123\n"
"v_mfma_f32_16x16x32_bf16 v[216:219],a[120:123],v[88:91],v[216:219]\n"
"v_exp_f32 v120,v31\n"
"v_mfma_f32_16x16x32_bf16 v[248:251],a[120:123],v[92:95],v[248:251]\n"
"buffer_load_dwordx4 v117,s[24:27],s64 offen offset:3072 sc1 lds\n"
"v_mfma_f32_16x16x32_bf16 v[152:155],a[120:123],v[80:83],v[152:155]\n"
"buffer_store_dwordx4 a[252:255],v112,s[24:27],s56 offen offset:3072 sc1\n"
"v_mfma_f32_16x16x32_bf16 v[184:187],a[120:123],v[84:87],v[184:187]\n"
"s_add_u32 s64,s64,16384\n"
"s_add_u32 s89,s89,1\n"
"v_cvt_pk_bf16_f32 v79,v119,v120\n"
"v_mfma_f32_16x16x32_bf16 v[188:191],a[124:127],v[84:87],v[188:191]\n"
"s_waitcnt lgkmcnt(0)\n"
"v_subrev_u32 v125,s101,v125\n"
"v_or3_b32 v118,v118,v77,v78\n"
"v_mfma_f32_16x16x32_bf16 v[220:223],a[124:127],v[88:91],v[220:223]\n"
"v_or3_b32 v125,v118,v79,v125\n"
"v_and_b32 v125,0x40004000,v125\n"
"v_mfma_f32_16x16x32_bf16 v[252:255],a[124:127],v[92:95],v[252:255]\n"
"v_cmp_ne_u32 vcc,0,v125\n"
"v_mfma_f32_16x16x32_bf16 v[156:159],a[124:127],v[80:83],v[156:159]\n"
"s_cbranch_vccnz .Lfk_cs_v2\n"
".Lfk_cr_v2:\n"
"s_waitcnt vmcnt(20)\n"
"buffer_store_dwordx2 a[154:155],off,s[24:27],s69 sc1\n"
"s_branch .Lfk_Hx\n"
".Lfk_tailB:\n"
"s_waitcnt vmcnt(11) lgkmcnt(0)\n"
"s_barrier\n"
"v_mfma_f32_16x16x32_bf16 v[0:3],a[64:67],a[0:3],v[96:99]\n"
"ds_read_b128 a[96:99],v114 offset:24576\n"
"ds_read_b128 a[100:103],v114 offset:25600\n"
"ds_read_b128 a[104:107],v114 offset:26624\n"
"v_mfma_f32_16x16x32_bf16 v[0:3],a[68:71],a[4:7],v[0:3]\n"
"ds_read_b128 a[108:111],v114 offset:27648\n"
"ds_read_b128 a[112:115],v114 offset:28672\n"
"ds_read_b128 a[116:119],v114 offset:29696\n"
"v_mfma_f32_16x16x32_bf16 v[0:3],a[72:75],a[8:11],v[0:3]\n"
"ds_read_b128 a[120:123],v114 offset:30720\n"
"ds_read_b128 a[124:127],v114 offset:31744\n"
"v_add_f32 v16,0x80000000,v126\n"
"v_mfma_f32_16x16x32_bf16 v[0:3],a[76:79],a[12:15],v[0:3]\n"
"v_min_f32 v16,v16,v104\n"
"v_add_f32 v17,0xf149f2ca,v126\n"
"v_min_f32 v17,v17,v105\n"
"v_mfma_f32_16x16x32_bf16 v[4:7],a[92:95],a[12:15],v[96:99]\n"
"v_add_f32 v18,0xf1c9f2ca,v126\n"
"v_min_f32 v18,v18,v106\n"
"v_add_f32 v19,0xf2177617,v126\n"
"v_mfma_f32_16x16x32_bf16 v[4:7],a[88:91],a[8:11],v[4:7]\n"
"v_min_f32 v19,v19,v107\n"
"v_add_f32 v20,0xf349f2ca,v126\n"
"v_min_f32 v20,v20,v104\n"
"v_mfma_f32_16x16x32_bf16 v[4:7],a[84:87],a[4:7],v[4:7]\n"
"v_add_f32 v21,0xf35691f6,v126\n"
"v_min_f32 v21,v21,v105\n"
"v_add_f32 v22,0xf3633123,v126\n"
"v_mfma_f32_16x16x32_bf16 v[4:7],a[80:83],a[0:3],v[4:7]\n"
"v_min_f32 v22,v22,v106\n"
"v_add_f32 v23,0xf36fd050,v126\n"
"v_min_f32 v23,v23,v107\n"
"v_mfma_f32_16x16x32_bf16 v[12:15],a[80:83],a[16:19],v[100:103]\n"
"v_exp_f32 v32,v32\n"
"v_max3_f32 v118,v0,v1,v2\n"
"v_mfma_f32_16x16x32_bf16 v[12:15],a[84:87],a[20:23],v[12:15]\n"
"v_exp_f32 v33,v33\n"
"v_mfma_f32_16x16x32_bf16 v[12:15],a[88:91],a[24:27],v[12:15]\n"
"v_exp_f32 v34,v34\n"
"v_cvt_pk_bf16_f32 v80,v32,v33\n"
"v_mfma_f32_16x16x32_bf16 v[12:15],a[92:95],a[28:31],v[12:15]\n"
"v_exp_f32 v35,v35\n"
"v_max3_f32 v118,v118,v3,v4\n"
"v_mfma_f32_16x16x32_bf16 v[8:11],a[76:79],a[28:31],v[100:103]\n"
"v_exp_f32 v36,v36\n"
"v_cvt_pk_bf16_f32 v81,v34,v35\n"
"v_mfma_f32_16x16x32_bf16 v[8:11],a[72:75],a[24:27],v[8:11]\n"
"v_exp_f32 v37,v37\n"
"v_max3_f32 v118,v118,v5,v6\n"
"v_mfma_f32_16x16x32_bf16 v[8:11],a[68:71],a[20:23],v[8:11]\n"
"v_exp_f32 v38,v38\n"
"v_mfma_f32_16x16x32_bf16 v[8:11],a[64:67],a[16:19],v[8:11]\n"
"v_exp_f32 v39,v39\n"
"v_max3_f32 v118,v118,v7,v12\n"
"v_mfma_f32_16x16x32_bf16 v[24:27],a[64:67],a[48:51],v[108:111]\n"
"v_exp_f32 v40,v40\n"
"v_mfma_f32_16x16x32_bf16 v[24:27],a[68:71],a[52:55],v[24:27]\n"
"v_exp_f32 v41,v41\n"
"v_max3_f32 v118,v118,v13,v14\n"
"v_mfma_f32_16x16x32_bf16 v[24:27],a[72:75],a[56:59],v[24:27]\n"
"v_cvt_pk_bf16_f32 v82,v36,v37\n"
"v_cvt_pk_bf16_f32 v84,v40,v41\n"
"v_mfma_f32_16x16x32_bf16 v[24:27],a[76:79],a[60:63],v[24:27]\n"
"v_exp_f32 v42,v42\n"
"v_max3_f32 v118,v118,v15,v8\n"
"v_mfma_f32_16x16x32_bf16 v[28:31],a[92:95],a[60:63],v[108:111]\n"
"v_cvt_pk_bf16_f32 v83,v38,v39\n"
"v_max3_f32 v118,v118,v9,v10\n"
"v_mfma_f32_16x16x32_bf16 v[28:31],a[88:91],a[56:59],v[28:31]\n"
"v_exp_f32 v43,v43\n"
"v_mfma_f32_16x16x32_bf16 v[28:31],a[84:87],a[52:55],v[28:31]\n"
"v_cvt_pk_bf16_f32 v85,v42,v43\n"
"v_mfma_f32_16x16x32_bf16 v[28:31],a[80:83],a[48:51],v[28:31]\n"
"v_exp_f32 v44,v44\n"
"v_mfma_f32_16x16x32_bf16 v[20:23],a[80:83],a[32:35],v[20:23]\n"
"v_max3_f32 v118,v118,v11,v24\n"
"v_max3_f32 v118,v118,v25,v26\n"
"v_mfma_f32_16x16x32_bf16 v[20:23],a[84:87],a[36:39],v[20:23]\n"
"v_exp_f32 v45,v45\n"
"v_mfma_f32_16x16x32_bf16 v[20:23],a[88:91],a[40:43],v[20:23]\n"
"v_cvt_pk_bf16_f32 v86,v44,v45\n"
"v_mfma_f32_16x16x32_bf16 v[20:23],a[92:95],a[44:47],v[20:23]\n"
"v_exp_f32 v46,v46\n"
"v_mfma_f32_16x16x32_bf16 v[16:19],a[76:79],a[44:47],v[16:19]\n"
"v_max3_f32 v118,v118,v27,v28\n"
"v_max3_f32 v118,v118,v29,v30\n"
"v_mfma_f32_16x16x32_bf16 v[16:19],a[72:75],a[40:43],v[16:19]\n"
"v_exp_f32 v47,v47\n"
"v_mfma_f32_16x16x32_bf16 v[16:19],a[68:71],a[36:39],v[16:19]\n"
"v_cvt_pk_bf16_f32 v87,v46,v47\n"
"v_exp_f32 v48,v48\n"
"v_mfma_f32_16x16x32_bf16 v[16:19],a[64:67],a[32:35],v[16:19]\n"
"v_max3_f32 v118,v118,v31,v20\n"
"s_waitcnt lgkmcnt(0)\n"
"v_mfma_f32_16x16x32_bf16 v[128:131],a[96:99],v[64:67],v[128:131]\n"
"v_exp_f32 v49,v49\n"
"v_max3_f32 v118,v118,v21,v22\n"
"v_cvt_pk_bf16_f32 v88,v48,v49\n"
"v_mfma_f32_16x16x32_bf16 v[160:163],a[96:99],v[68:71],v[160:163]\n"
"ds_read_b128 a[64:67],v114 offset:57344\n"
"ds_read_b128 a[68:71],v114 offset:58368\n"
"v_mfma_f32_16x16x32_bf16 v[192:195],a[96:99],v[72:75],v[192:195]\n"
"ds_read_b128 a[72:75],v114 offset:59392\n"
"v_exp_f32 v50,v50\n"
"v_mfma_f32_16x16x32_bf16 v[224:227],a[96:99],v[76:79],v[224:227]\n"
"v_max3_f32 v118,v118,v23,v16\n"
"v_max3_f32 v118,v118,v17,v18\n"
"v_max_f32 v118,v118,v19\n"
"v_mfma_f32_16x16x32_bf16 a[140:143],a[144:147],v[76:79],a[140:143]\n"
"v_exp_f32 v51,v51\n"
"v_cmp_lt_f32 vcc,s93,v118\n"
"v_cvt_pk_bf16_f32 v89,v50,v51\n"
"v_mfma_f32_16x16x32_bf16 v[228:231],a[100:103],v[76:79],v[228:231]\n"
"ds_read_b128 a[76:79],v114 offset:60416\n"
"ds_read_b128 a[80:83],v114 offset:61440\n"
"v_mfma_f32_16x16x32_bf16 v[132:135],a[100:103],v[64:67],v[132:135]\n"
"ds_read_b128 a[84:87],v114 offset:62464\n"
"v_exp_f32 v52,v52\n"
"v_mfma_f32_16x16x32_bf16 v[164:167],a[100:103],v[68:71],v[164:167]\n"
"s_sub_u32 s82,s3,32768\n"
"ds_read_b128 a[88:91],v114 offset:63488\n"
"ds_read_b128 a[92:95],v114 offset:64512\n"
"v_mfma_f32_16x16x32_bf16 v[196:199],a[100:103],v[72:75],v[196:199]\n"
"buffer_load_dwordx4 a[0:3],v112,s[24:27],s82 offen offset:0 sc1\n"
"v_exp_f32 v53,v53\n"
"v_mfma_f32_16x16x32_bf16 a[136:139],a[144:147],v[72:75],a[136:139]\n"
"buffer_load_dwordx4 a[4:7],v112,s[24:27],s82 offen offset:1024 sc1\n"
"buffer_load_dwordx4 a[8:11],v112,s[24:27],s82 offen offset:2048 sc1\n"
"v_cvt_pk_bf16_f32 v90,v52,v53\n"
"v_exp_f32 v54,v54\n"
"v_mfma_f32_16x16x32_bf16 v[200:203],a[104:107],v[72:75],v[200:203]\n"
"buffer_load_dwordx4 a[12:15],v112,s[24:27],s82 offen offset:3072 sc1\n"
"v_mfma_f32_16x16x32_bf16 v[232:235],a[104:107],v[76:79],v[232:235]\n"
"s_sub_u32 s82,s3,49152\n"
"buffer_load_dwordx4 a[16:19],v112,s[24:27],s82 offen offset:0 sc1\n"
"buffer_load_dwordx4 a[20:23],v112,s[24:27],s82 offen offset:1024 sc1\n"
"v_exp_f32 v55,v55\n"
"v_mfma_f32_16x16x32_bf16 v[136:139],a[104:107],v[64:67],v[136:139]\n"
"buffer_load_dwordx4 a[24:27],v112,s[24:27],s82 offen offset:2048 sc1\n"
"v_mfma_f32_16x16x32_bf16 v[168:171],a[104:107],v[68:71],v[168:171]\n"
"buffer_load_dwordx4 a[28:31],v112,s[24:27],s82 offen offset:3072 sc1\n"
"v_mfma_f32_16x16x32_bf16 a[132:135],a[144:147],v[68:71],a[132:135]\n"
"v_exp_f32 v56,v56\n"
"v_mfma_f32_16x16x32_bf16 v[172:175],a[108:111],v[68:71],v[172:175]\n"
"v_cvt_pk_bf16_f32 v91,v54,v55\n"
"v_mfma_f32_16x16x32_bf16 v[204:207],a[108:111],v[72:75],v[204:207]\n"
"v_exp_f32 v57,v57\n"
"v_mfma_f32_16x16x32_bf16 v[236:239],a[108:111],v[76:79],v[236:239]\n"
"v_cvt_pk_bf16_f32 v92,v56,v57\n"
"v_mfma_f32_16x16x32_bf16 v[140:143],a[108:111],v[64:67],v[140:143]\n"
"v_exp_f32 v58,v58\n"
"v_mfma_f32_16x16x32_bf16 a[128:131],a[144:147],v[64:67],a[128:131]\n"
"v_mfma_f32_16x16x32_bf16 v[144:147],a[112:115],v[64:67],v[144:147]\n"
"v_exp_f32 v59,v59\n"
"v_mfma_f32_16x16x32_bf16 v[176:179],a[112:115],v[68:71],v[176:179]\n"
"v_cvt_pk_bf16_f32 v93,v58,v59\n"
"v_mfma_f32_16x16x32_bf16 v[208:211],a[112:115],v[72:75],v[208:211]\n"
"v_exp_f32 v60,v60\n"
"v_mfma_f32_16x16x32_bf16 v[240:243],a[112:115],v[76:79],v[240:243]\n"
"v_mfma_f32_16x16x32_bf16 v[244:247],a[116:119],v[76:79],v[244:247]\n"
"v_exp_f32 v61,v61\n"
"v_mfma_f32_16x16x32_bf16 v[148:151],a[116:119],v[64:67],v[148:151]\n"
"v_cvt_pk_bf16_f32 v94,v60,v61\n"
"v_mfma_f32_16x16x32_bf16 v[180:183],a[116:119],v[68:71],v[180:183]\n"
"v_exp_f32 v62,v62\n"
"v_mfma_f32_16x16x32_bf16 v[212:215],a[116:119],v[72:75],v[212:215]\n"
"v_mfma_f32_16x16x32_bf16 v[216:219],a[120:123],v[72:75],v[216:219]\n"
"v_exp_f32 v63,v63\n"
"v_mfma_f32_16x16x32_bf16 v[248:251],a[120:123],v[76:79],v[248:251]\n"
"v_cvt_pk_bf16_f32 v95,v62,v63\n"
"v_mfma_f32_16x16x32_bf16 v[152:155],a[120:123],v[64:67],v[152:155]\n"
"v_mfma_f32_16x16x32_bf16 v[184:187],a[120:123],v[68:71],v[184:187]\n"
"v_mfma_f32_16x16x32_bf16 v[188:191],a[124:127],v[68:71],v[188:191]\n"
"v_mfma_f32_16x16x32_bf16 v[220:223],a[124:127],v[72:75],v[220:223]\n"
"v_mfma_f32_16x16x32_bf16 v[252:255],a[124:127],v[76:79],v[252:255]\n"
"v_mfma_f32_16x16x32_bf16 v[156:159],a[124:127],v[64:67],v[156:159]\n"
"s_cbranch_vccnz .Lfk_cs_b3\n"
".Lfk_cr_b3:\n"
"s_waitcnt vmcnt(9) lgkmcnt(0)\n"
"s_barrier\n"
"v_mfma_f32_16x16x32_bf16 v[128:131],a[64:67],v[80:83],v[128:131]\n"
"v_exp_f32 v0,v0\n"
"v_mfma_f32_16x16x32_bf16 v[160:163],a[64:67],v[84:87],v[160:163]\n"
"v_exp_f32 v1,v1\n"
"v_mfma_f32_16x16x32_bf16 v[192:195],a[64:67],v[88:91],v[192:195]\n"
"v_exp_f32 v2,v2\n"
"v_cvt_pk_bf16_f32 v64,v0,v1\n"
"v_mfma_f32_16x16x32_bf16 v[224:227],a[64:67],v[92:95],v[224:227]\n"
"v_exp_f32 v3,v3\n"
"v_mfma_f32_16x16x32_bf16 a[140:143],a[144:147],v[92:95],a[140:143]\n"
"v_exp_f32 v4,v4\n"
"v_cvt_pk_bf16_f32 v65,v2,v3\n"
"v_mfma_f32_16x16x32_bf16 v[228:231],a[68:71],v[92:95],v[228:231]\n"
"v_exp_f32 v5,v5\n"
"v_mfma_f32_16x16x32_bf16 v[132:135],a[68:71],v[80:83],v[132:135]\n"
"v_exp_f32 v6,v6\n"
"v_cvt_pk_bf16_f32 v66,v4,v5\n"
"v_exp_f32 v7,v7\n"
"v_mfma_f32_16x16x32_bf16 v[164:167],a[68:71],v[84:87],v[164:167]\n"
"ds_read_b128 a[96:99],v114 offset:40960\n"
"ds_read_b128 a[100:103],v114 offset:41984\n"
"v_exp_f32 v8,v8\n"
"v_mfma_f32_16x16x32_bf16 v[196:199],a[68:71],v[88:91],v[196:199]\n"
"ds_read_b128 a[104:107],v114 offset:43008\n"
"ds_read_b128 a[108:111],v114 offset:44032\n"
"v_exp_f32 v9,v9\n"
"v_mfma_f32_16x16x32_bf16 a[136:139],a[144:147],v[88:91],a[136:139]\n"
"ds_read_b128 a[112:115],v114 offset:45056\n"
"ds_read_b128 a[116:119],v114 offset:46080\n"
"v_exp_f32 v10,v10\n"
"v_mfma_f32_16x16x32_bf16 v[200:203],a[72:75],v[88:91],v[200:203]\n"
"ds_read_b128 a[120:123],v114 offset:47104\n"
"ds_read_b128 a[124:127],v114 offset:48128\n"
"v_mfma_f32_16x16x32_bf16 v[232:235],a[72:75],v[92:95],v[232:235]\n"
"v_exp_f32 v11,v11\n"
"v_accvgpr_read_b32 v119,a150\n"
"v_accvgpr_read_b32 v120,a151\n"
"v_mfma_f32_16x16x32_bf16 v[136:139],a[72:75],v[80:83],v[136:139]\n"
"v_exp_f32 v12,v12\n"
"v_xor_b32 v119,s80,v119\n"
"v_xor_b32 v120,s81,v120\n"
"v_mfma_f32_16x16x32_bf16 v[168:171],a[72:75],v[84:87],v[168:171]\n"
"v_exp_f32 v13,v13\n"
"v_or_b32 v119,v119,v120\n"
"v_cmp_ne_u32 s[14:15],0,v119\n"
"v_mfma_f32_16x16x32_bf16 a[132:135],a[144:147],v[84:87],a[132:135]\n"
"s_add_u32 s18,s8,1032192\n"
"v_exp_f32 v14,v14\n"
"s_cmp_lg_u32 s14,0\n"
"s_cselect_b32 s18,2146435072,s18\n"
"v_mfma_f32_16x16x32_bf16 v[172:175],a[76:79],v[84:87],v[172:175]\n"
"s_add_u32 m0,s30,0\n"
"v_exp_f32 v15,v15\n"
"buffer_load_dwordx4 v112,s[24:27],s18 offen offset:0 sc1 lds\n"
"buffer_load_dwordx4 v112,s[24:27],s18 offen offset:1024 sc1 lds\n"
"v_exp_f32 v16,v16\n"
"v_mfma_f32_16x16x32_bf16 v[204:207],a[76:79],v[88:91],v[204:207]\n"
"buffer_load_dwordx4 v112,s[24:27],s18 offen offset:2048 sc1 lds\n"
"buffer_load_dwordx4 v112,s[24:27],s18 offen offset:3072 sc1 lds\n"
"v_mfma_f32_16x16x32_bf16 v[236:239],a[76:79],v[92:95],v[236:239]\n"
"s_add_u32 m0,m0,4096\n"
"v_exp_f32 v17,v17\n"
"v_cvt_pk_bf16_f32 v67,v6,v7\n"
"v_mfma_f32_16x16x32_bf16 v[140:143],a[76:79],v[80:83],v[140:143]\n"
"buffer_load_dwordx4 v117,s[24:27],s18 offen offset:0 sc1 lds\n"
"v_exp_f32 v18,v18\n"
"v_cvt_pk_bf16_f32 v68,v8,v9\n"
"v_cvt_pk_bf16_f32 v69,v10,v11\n"
"v_exp_f32 v19,v19\n"
"v_mfma_f32_16x16x32_bf16 a[128:131],a[144:147],v[80:83],a[128:131]\n"
"buffer_load_dwordx4 v117,s[24:27],s18 offen offset:1024 sc1 lds\n"
"v_cvt_pk_bf16_f32 v70,v12,v13\n"
"v_mfma_f32_16x16x32_bf16 v[144:147],a[80:83],v[80:83],v[144:147]\n"
"v_exp_f32 v20,v20\n"
"v_cvt_pk_bf16_f32 v71,v14,v15\n"
"v_cvt_pk_bf16_f32 v72,v16,v17\n"
"v_exp_f32 v21,v21\n"
"v_mfma_f32_16x16x32_bf16 v[176:179],a[80:83],v[84:87],v[176:179]\n"
"buffer_load_dwordx4 v117,s[24:27],s18 offen offset:2048 sc1 lds\n"
"v_cvt_pk_bf16_f32 v73,v18,v19\n"
"v_mfma_f32_16x16x32_bf16 v[208:211],a[80:83],v[88:91],v[208:211]\n"
"v_exp_f32 v22,v22\n"
"v_cvt_pk_bf16_f32 v74,v20,v21\n"
"v_exp_f32 v23,v23\n"
"v_mfma_f32_16x16x32_bf16 v[240:243],a[80:83],v[92:95],v[240:243]\n"
"buffer_load_dwordx4 v117,s[24:27],s18 offen offset:3072 sc1 lds\n"
"v_mfma_f32_16x16x32_bf16 v[244:247],a[84:87],v[92:95],v[244:247]\n"
"s_sub_u32 s82,s3,65536\n"
"v_exp_f32 v24,v24\n"
"v_cvt_pk_bf16_f32 v75,v22,v23\n"
"v_exp_f32 v25,v25\n"
"v_mfma_f32_16x16x32_bf16 v[148:151],a[84:87],v[80:83],v[148:151]\n"
"buffer_load_dwordx4 v[32:35],v112,s[24:27],s82 offen offset:0 sc1\n"
"v_mfma_f32_16x16x32_bf16 v[180:183],a[84:87],v[84:87],v[180:183]\n"
"v_exp_f32 v26,v26\n"
"v_cvt_pk_bf16_f32 v76,v24,v25\n"
"v_exp_f32 v27,v27\n"
"v_mfma_f32_16x16x32_bf16 v[212:215],a[84:87],v[88:91],v[212:215]\n"
"buffer_load_dwordx4 v[36:39],v112,s[24:27],s82 offen offset:1024 sc1\n"
"v_mfma_f32_16x16x32_bf16 v[216:219],a[88:91],v[88:91],v[216:219]\n"
"v_exp_f32 v28,v28\n"
"v_exp_f32 v29,v29\n"
"v_mfma_f32_16x16x32_bf16 v[248:251],a[88:91],v[92:95],v[248:251]\n"
"buffer_load_dwordx4 v[40:43],v112,s[24:27],s82 offen offset:2048 sc1\n"
"v_mfma_f32_16x16x32_bf16 v[152:155],a[88:91],v[80:83],v[152:155]\n"
"v_exp_f32 v30,v30\n"
"v_mfma_f32_16x16x32_bf16 v[184:187],a[88:91],v[84:87],v[184:187]\n"
"v_exp_f32 v31,v31\n"
"v_mfma_f32_16x16x32_bf16 v[188:191],a[92:95],v[84:87],v[188:191]\n"
"buffer_load_dwordx4 v[44:47],v112,s[24:27],s82 offen offset:3072 sc1\n"
"v_mfma_f32_16x16x32_bf16 v[220:223],a[92:95],v[88:91],v[220:223]\n"
"v_cvt_pk_bf16_f32 v78,v28,v29\n"
"v_cvt_pk_bf16_f32 v77,v26,v27\n"
"v_mfma_f32_16x16x32_bf16 v[252:255],a[92:95],v[92:95],v[252:255]\n"
"v_cvt_pk_bf16_f32 v79,v30,v31\n"
"v_mfma_f32_16x16x32_bf16 v[156:159],a[92:95],v[80:83],v[156:159]\n"
"s_load_dwordx2 s[36:37],s[0:1],0\n"
"s_load_dwordx2 s[44:45],s[0:1],80\n"
"s_load_dwordx2 s[48:49],s[0:1],88\n"
"s_lshl_b32 s82,s78,2\n"
"s_add_u32 s82,s82,65536\n"
"v_add_u32 v113,s82,v112\n"
"s_lshr_b32 s82,s78,2\n"
"s_add_u32 s82,s82,122880\n"
"v_lshrrev_b32 v115,1,v112\n"
"v_add_u32 v116,s82,v115\n"
"s_waitcnt lgkmcnt(0)\n"
"s_barrier\n"
"v_mfma_f32_16x16x32_bf16 v[128:131],a[96:99],v[64:67],v[128:131]\n"
"s_add_u32 s83,s18,2097152\n"
"s_add_u32 m0,s30,32768\n"
"v_mfma_f32_16x16x32_bf16 a[128:131],a[144:147],v[64:67],a[128:131]\n"
"buffer_load_dwordx4 v112,s[24:27],s83 offen offset:0 sc1 lds\n"
"v_mfma_f32_16x16x32_bf16 v[132:135],a[100:103],v[64:67],v[132:135]\n"
"buffer_load_dwordx4 v112,s[24:27],s83 offen offset:1024 sc1 lds\n"
"v_mfma_f32_16x16x32_bf16 v[136:139],a[104:107],v[64:67],v[136:139]\n"
"buffer_load_dwordx4 v112,s[24:27],s83 offen offset:2048 sc1 lds\n"
"v_mfma_f32_16x16x32_bf16 v[140:143],a[108:111],v[64:67],v[140:143]\n"
"v_mfma_f32_16x16x32_bf16 v[144:147],a[112:115],v[64:67],v[144:147]\n"
"buffer_load_dwordx4 v112,s[24:27],s83 offen offset:3072 sc1 lds\n"
"v_mfma_f32_16x16x32_bf16 v[148:151],a[116:119],v[64:67],v[148:151]\n"
"s_add_u32 m0,m0,4096\n"
"v_mfma_f32_16x16x32_bf16 v[152:155],a[120:123],v[64:67],v[152:155]\n"
"v_mfma_f32_16x16x32_bf16 v[156:159],a[124:127],v[64:67],v[156:159]\n"
"buffer_load_dwordx4 v117,s[24:27],s83 offen offset:0 sc1 lds\n"
"v_mfma_f32_16x16x32_bf16 v[160:163],a[96:99],v[68:71],v[160:163]\n"
"v_accvgpr_read_b32 v97,a128\n"
"v_cvt_pk_bf16_f32 v136,v136,v137\n"
"v_mfma_f32_16x16x32_bf16 a[132:135],a[144:147],v[68:71],a[132:135]\n"
"buffer_load_dwordx4 v117,s[24:27],s83 offen offset:1024 sc1 lds\n"
"v_mfma_f32_16x16x32_bf16 v[164:167],a[100:103],v[68:71],v[164:167]\n"
"v_cvt_pk_bf16_f32 v128,v128,v129\n"
"v_cvt_pk_bf16_f32 v129,v130,v131\n"
"v_mfma_f32_16x16x32_bf16 v[168:171],a[104:107],v[68:71],v[168:171]\n"
"buffer_load_dwordx4 v117,s[24:27],s83 offen offset:2048 sc1 lds\n"
"v_cvt_pk_bf16_f32 v130,v132,v133\n"
"v_cvt_pk_bf16_f32 v131,v134,v135\n"
"v_mfma_f32_16x16x32_bf16 v[172:175],a[108:111],v[68:71],v[172:175]\n"
"ds_write_b128 v113,v[128:131] offset:0\n"
"v_cvt_pk_bf16_f32 v137,v138,v139\n"
"v_mfma_f32_16x16x32_bf16 v[176:179],a[112:115],v[68:71],v[176:179]\n"
"buffer_load_dwordx4 v117,s[24:27],s83 offen offset:3072 sc1 lds\n"
"v_cvt_pk_bf16_f32 v138,v140,v141\n"
"v_cvt_pk_bf16_f32 v139,v142,v143\n"
"v_mfma_f32_16x16x32_bf16 v[180:183],a[116:119],v[68:71],v[180:183]\n"
"s_sub_u32 s82,s3,81920\n"
"ds_write_b128 v113,v[136:139] offset:1024\n"
"v_cvt_pk_bf16_f32 v144,v144,v145\n"
"v_mfma_f32_16x16x32_bf16 v[184:187],a[120:123],v[68:71],v[184:187]\n"
"buffer_load_dwordx4 a[32:35],v112,s[24:27],s82 offen offset:0 sc1\n"
"v_cvt_pk_bf16_f32 v145,v146,v147\n"
"v_cvt_pk_bf16_f32 v146,v148,v149\n"
"v_cvt_pk_bf16_f32 v147,v150,v151\n"
"v_mfma_f32_16x16x32_bf16 v[188:191],a[124:127],v[68:71],v[188:191]\n"
"ds_write_b128 v113,v[144:147] offset:2048\n"
"v_cvt_pk_bf16_f32 v152,v152,v153\n"
"v_mfma_f32_16x16x32_bf16 v[192:195],a[96:99],v[72:75],v[192:195]\n"
"buffer_load_dwordx4 a[36:39],v112,s[24:27],s82 offen offset:1024 sc1\n"
"v_cvt_pk_bf16_f32 v153,v154,v155\n"
"v_cvt_pk_bf16_f32 v154,v156,v157\n"
"v_cvt_pk_bf16_f32 v155,v158,v159\n"
"v_mfma_f32_16x16x32_bf16 a[136:139],a[144:147],v[72:75],a[136:139]\n"
"ds_write_b128 v113,v[152:155] offset:3072\n"
"ds_write_b64 v116,v[96:97] offset:0\n"
"v_mfma_f32_16x16x32_bf16 v[196:199],a[100:103],v[72:75],v[196:199]\n"
"buffer_load_dwordx4 a[40:43],v112,s[24:27],s82 offen offset:2048 sc1\n"
"v_accvgpr_read_b32 v101,a132\n"
"v_cvt_pk_bf16_f32 v160,v160,v161\n"
"v_mfma_f32_16x16x32_bf16 v[200:203],a[104:107],v[72:75],v[200:203]\n"
"v_cvt_pk_bf16_f32 v161,v162,v163\n"
"v_cvt_pk_bf16_f32 v162,v164,v165\n"
"v_cvt_pk_bf16_f32 v163,v166,v167\n"
"v_mfma_f32_16x16x32_bf16 v[204:207],a[108:111],v[72:75],v[204:207]\n"
"ds_write_b128 v113,v[160:163] offset:4096\n"
"v_cvt_pk_bf16_f32 v168,v168,v169\n"
"v_cvt_pk_bf16_f32 v169,v170,v171\n"
"v_mfma_f32_16x16x32_bf16 v[208:211],a[112:115],v[72:75],v[208:211]\n"
"buffer_load_dwordx4 a[44:47],v112,s[24:27],s82 offen offset:3072 sc1\n"
"v_cvt_pk_bf16_f32 v170,v172,v173\n"
"v_cvt_pk_bf16_f32 v171,v174,v175\n"
"v_mfma_f32_16x16x32_bf16 v[212:215],a[116:119],v[72:75],v[212:215]\n"
"ds_write_b128 v113,v[168:171] offset:5120\n"
"v_cvt_pk_bf16_f32 v176,v176,v177\n"
"v_cvt_pk_bf16_f32 v177,v178,v179\n"
"v_mfma_f32_16x16x32_bf16 v[216:219],a[120:123],v[72:75],v[216:219]\n"
"v_cvt_pk_bf16_f32 v178,v180,v181\n"
"v_cvt_pk_bf16_f32 v179,v182,v183\n"
"v_mfma_f32_16x16x32_bf16 v[220:223],a[124:127],v[72:75],v[220:223]\n"
"ds_write_b128 v113,v[176:179] offset:6144\n"
"v_cvt_pk_bf16_f32 v184,v184,v185\n"
"v_cvt_pk_bf16_f32 v185,v186,v187\n"
"v_cvt_pk_bf16_f32 v186,v188,v189\n"
"v_cvt_pk_bf16_f32 v187,v190,v191\n"
"v_mfma_f32_16x16x32_bf16 v[224:227],a[96:99],v[76:79],v[224:227]\n"
"ds_write_b128 v113,v[184:187] offset:7168\n"
"v_mfma_f32_16x16x32_bf16 a[140:143],a[144:147],v[76:79],a[140:143]\n"
"s_sub_u32 s82,s3,131072\n"
"ds_write_b64 v116,v[100:101] offset:512\n"
"buffer_load_dwordx4 v[128:131],v112,s[24:27],s82 offen offset:0 sc1\n"
"buffer_load_dwordx4 v[132:135],v112,s[24:27],s82 offen offset:1024 sc1\n"
"v_mfma_f32_16x16x32_bf16 v[228:231],a[100:103],v[76:79],v[228:231]\n"
"buffer_load_dwordx4 v[136:139],v112,s[24:27],s82 offen offset:2048 sc1\n"
"buffer_load_dwordx4 v[140:143],v112,s[24:27],s82 offen offset:3072 sc1\n"
"s_sub_u32 s82,s3,147456\n"
"v_mfma_f32_16x16x32_bf16 v[232:235],a[104:107],v[76:79],v[232:235]\n"
"buffer_load_dwordx4 v[144:147],v112,s[24:27],s82 offen offset:0 sc1\n"
"buffer_load_dwordx4 v[148:151],v112,s[24:27],s82 offen offset:1024 sc1\n"
"buffer_load_dwordx4 v[152:155],v112,s[24:27],s82 offen offset:2048 sc1\n"
"v_mfma_f32_16x16x32_bf16 v[236:239],a[108:111],v[76:79],v[236:239]\n"
"buffer_load_dwordx4 v[156:159],v112,s[24:27],s82 offen offset:3072 sc1\n"
"v_mfma_f32_16x16x32_bf16 v[240:243],a[112:115],v[76:79],v[240:243]\n"
"v_mfma_f32_16x16x32_bf16 v[244:247],a[116:119],v[76:79],v[244:247]\n"
"v_mfma_f32_16x16x32_bf16 v[248:251],a[120:123],v[76:79],v[248:251]\n"
"v_mfma_f32_16x16x32_bf16 v[252:255],a[124:127],v[76:79],v[252:255]\n"
".Lfk_endB:\n"
"v_lshrrev_b32 v70,4,v112\n"
"v_and_b32 v71,15,v70\n"
"v_lshrrev_b32 v70,4,v70\n"
"v_lshlrev_b32 v70,2,v70\n"
"v_sub_u32 v70,v71,v70\n"
"v_mov_b32 v71,0x3f80\n"
"v_cmp_eq_u32 vcc,0,v70\n"
"v_mov_b32 v68,0\n"
"v_mov_b32 v64,0\n"
"v_cndmask_b32 v66,0,v71,vcc\n"
"v_cmp_eq_u32 vcc,1,v70\n"
"v_mov_b32 v65,0\n"
"v_mov_b32 v72,0x3f800000\n"
"v_cndmask_b32 v66,v66,v72,vcc\n"
"v_cmp_eq_u32 vcc,2,v70\n"
"v_mov_b32 v69,0\n"
"s_nop 0\n"
"v_cndmask_b32 v67,0,v71,vcc\n"
"v_cmp_eq_u32 vcc,3,v70\n"
"s_nop 1\n"
"v_cndmask_b32 v67,v67,v72,vcc\n"
"s_sub_u32 s82,s3,163840\n"
"buffer_load_dwordx4 v[0:3],v112,s[24:27],s82 offen offset:0 sc1\n"
"buffer_load_dwordx4 v[4:7],v112,s[24:27],s82 offen offset:1024 sc1\n"
"buffer_load_dwordx4 v[8:11],v112,s[24:27],s82 offen offset:2048 sc1\n"
"buffer_load_dwordx4 v[12:15],v112,s[24:27],s82 offen offset:3072 sc1\n"
"s_sub_u32 s82,s3,180224\n"
"buffer_load_dwordx4 v[160:163],v112,s[24:27],s82 offen offset:0 sc1\n"
"buffer_load_dwordx4 v[164:167],v112,s[24:27],s82 offen offset:1024 sc1\n"
"buffer_load_dwordx4 v[168:171],v112,s[24:27],s82 offen offset:2048 sc1\n"
"buffer_load_dwordx4 v[172:175],v112,s[24:27],s82 offen offset:3072 sc1\n"
"s_sub_u32 s82,s3,196608\n"
"buffer_load_dwordx4 v[176:179],v112,s[24:27],s82 offen offset:0 sc1\n"
"buffer_load_dwordx4 v[180:183],v112,s[24:27],s82 offen offset:1024 sc1\n"
"buffer_load_dwordx4 v[184:187],v112,s[24:27],s82 offen offset:2048 sc1\n"
"buffer_load_dwordx4 v[188:191],v112,s[24:27],s82 offen offset:3072 sc1\n"
"s_waitcnt lgkmcnt(0)\n"
"s_barrier\n"
"v_xor_b32 v113,32768,v113\n"
"v_xor_b32 v116,2048,v116\n"
"ds_read_b64 v[98:99],v116 offset:0\n"
"ds_read_b128 a[64:67],v113 offset:0\n"
"ds_read_b128 a[68:71],v113 offset:1024\n"
"ds_read_b128 a[72:75],v113 offset:2048\n"
"ds_read_b128 a[76:79],v113 offset:3072\n"
"ds_read_b64 v[102:103],v116 offset:512\n"
"ds_read_b128 a[80:83],v113 offset:4096\n"
"ds_read_b128 a[84:87],v113 offset:5120\n"
"ds_read_b128 a[88:91],v113 offset:6144\n"
"ds_read_b128 a[92:95],v113 offset:7168\n"
"s_waitcnt lgkmcnt(5)\n"
"v_cmp_neq_f32 vcc,v98,v104\n"
"s_nop 0\n"
"s_cbranch_vccnz .Lfk_ms_b0\n"
".Lfk_mr_b0:\n"
"v_mfma_f32_16x16x32_bf16 v[192:195],v[66:69],a[64:67],v[192:195]\n"
"v_mfma_f32_16x16x32_bf16 v[196:199],v[64:67],a[64:67],v[196:199]\n"
"v_mfma_f32_16x16x32_bf16 v[200:203],v[66:69],a[68:71],v[200:203]\n"
"v_mfma_f32_16x16x32_bf16 v[204:207],v[64:67],a[68:71],v[204:207]\n"
"v_mfma_f32_16x16x32_bf16 v[208:211],v[66:69],a[72:75],v[208:211]\n"
"v_mfma_f32_16x16x32_bf16 v[212:215],v[64:67],a[72:75],v[212:215]\n"
"v_mfma_f32_16x16x32_bf16 v[216:219],v[66:69],a[76:79],v[216:219]\n"
"v_mfma_f32_16x16x32_bf16 v[220:223],v[64:67],a[76:79],v[220:223]\n"
"s_waitcnt lgkmcnt(0)\n"
"v_cmp_neq_f32 vcc,v102,v108\n"
"s_nop 0\n"
"s_cbranch_vccnz .Lfk_ms_b1\n"
".Lfk_mr_b1:\n"
"v_mfma_f32_16x16x32_bf16 v[224:227],v[66:69],a[80:83],v[224:227]\n"
"v_mfma_f32_16x16x32_bf16 v[228:231],v[64:67],a[80:83],v[228:231]\n"
"v_accvgpr_read_b32 v122,a136\n"
"v_add_f32 v122,v122,v99\n"
"v_mfma_f32_16x16x32_bf16 v[232:235],v[66:69],a[84:87],v[232:235]\n"
"v_rcp_f32 v122,v122\n"
"v_mfma_f32_16x16x32_bf16 v[236:239],v[64:67],a[84:87],v[236:239]\n"
"v_pk_mul_f32 v[192:193],v[192:193],v[122:123] op_sel_hi:[1,0]\n"
"v_pk_mul_f32 v[194:195],v[194:195],v[122:123] op_sel_hi:[1,0]\n"
"v_pk_mul_f32 v[196:197],v[196:197],v[122:123] op_sel_hi:[1,0]\n"
"v_pk_mul_f32 v[198:199],v[198:199],v[122:123] op_sel_hi:[1,0]\n"
"v_mfma_f32_16x16x32_bf16 v[240:243],v[66:69],a[88:91],v[240:243]\n"
"v_cvt_pk_bf16_f32 v192,v192,v193\n"
"v_cvt_pk_bf16_f32 v193,v194,v195\n"
"v_mfma_f32_16x16x32_bf16 v[244:247],v[64:67],a[88:91],v[244:247]\n"
"v_cvt_pk_bf16_f32 v194,v196,v197\n"
"v_cvt_pk_bf16_f32 v195,v198,v199\n"
"v_mfma_f32_16x16x32_bf16 v[248:251],v[66:69],a[92:95],v[248:251]\n"
"buffer_store_dwordx4 v[192:195],v112,s[24:27],s70 offen offset:0 sc1\n"
"v_pk_mul_f32 v[200:201],v[200:201],v[122:123] op_sel_hi:[1,0]\n"
"v_pk_mul_f32 v[202:203],v[202:203],v[122:123] op_sel_hi:[1,0]\n"
"v_pk_mul_f32 v[204:205],v[204:205],v[122:123] op_sel_hi:[1,0]\n"
"v_pk_mul_f32 v[206:207],v[206:207],v[122:123] op_sel_hi:[1,0]\n"
"v_mfma_f32_16x16x32_bf16 v[252:255],v[64:67],a[92:95],v[252:255]\n"
"v_cvt_pk_bf16_f32 v200,v200,v201\n"
"v_cvt_pk_bf16_f32 v201,v202,v203\n"
"v_cvt_pk_bf16_f32 v202,v204,v205\n"
"v_cvt_pk_bf16_f32 v203,v206,v207\n"
"v_pk_mul_f32 v[208:209],v[208:209],v[122:123] op_sel_hi:[1,0]\n"
"v_pk_mul_f32 v[210:211],v[210:211],v[122:123] op_sel_hi:[1,0]\n"
"v_pk_mul_f32 v[212:213],v[212:213],v[122:123] op_sel_hi:[1,0]\n"
"v_pk_mul_f32 v[214:215],v[214:215],v[122:123] op_sel_hi:[1,0]\n"
"v_cvt_pk_bf16_f32 v208,v208,v209\n"
"v_cvt_pk_bf16_f32 v209,v210,v211\n"
"v_cvt_pk_bf16_f32 v210,v212,v213\n"
"v_cvt_pk_bf16_f32 v211,v214,v215\n"
"buffer_store_dwordx4 v[200:203],v112,s[24:27],s70 offen offset:1024 sc1\n"
"buffer_store_dwordx4 v[208:211],v112,s[24:27],s70 offen offset:2048 sc1\n"
"v_pk_mul_f32 v[216:217],v[216:217],v[122:123] op_sel_hi:[1,0]\n"
"v_pk_mul_f32 v[218:219],v[218:219],v[122:123] op_sel_hi:[1,0]\n"
"v_pk_mul_f32 v[220:221],v[220:221],v[122:123] op_sel_hi:[1,0]\n"
"v_pk_mul_f32 v[222:223],v[222:223],v[122:123] op_sel_hi:[1,0]\n"
"v_cvt_pk_bf16_f32 v216,v216,v217\n"
"v_cvt_pk_bf16_f32 v217,v218,v219\n"
"v_cvt_pk_bf16_f32 v218,v220,v221\n"
"v_cvt_pk_bf16_f32 v219,v222,v223\n"
"buffer_store_dwordx4 v[216:219],v112,s[24:27],s70 offen offset:3072 sc1\n"
"s_add_u32 s70,s70,262144\n"
"s_branch .Lfk_p1\n"
".Lfk_tailB7:\n"
"s_waitcnt vmcnt(11) lgkmcnt(0)\n"
"s_barrier\n"
"v_mfma_f32_16x16x32_bf16 v[0:3],a[64:67],a[0:3],v[96:99]\n"
"ds_read_b128 a[96:99],v114 offset:24576\n"
"ds_read_b128 a[100:103],v114 offset:25600\n"
"ds_read_b128 a[104:107],v114 offset:26624\n"
"v_mfma_f32_16x16x32_bf16 v[0:3],a[68:71],a[4:7],v[0:3]\n"
"ds_read_b128 a[108:111],v114 offset:27648\n"
"ds_read_b128 a[112:115],v114 offset:28672\n"
"ds_read_b128 a[116:119],v114 offset:29696\n"
"v_mfma_f32_16x16x32_bf16 v[0:3],a[72:75],a[8:11],v[0:3]\n"
"ds_read_b128 a[120:123],v114 offset:30720\n"
"ds_read_b128 a[124:127],v114 offset:31744\n"
"v_add_f32 v16,0x80000000,v126\n"
"v_mfma_f32_16x16x32_bf16 v[0:3],a[76:79],a[12:15],v[0:3]\n"
"v_min_f32 v16,v16,v104\n"
"v_add_f32 v17,0xf149f2ca,v126\n"
"v_min_f32 v17,v17,v105\n"
"v_mfma_f32_16x16x32_bf16 v[4:7],a[92:95],a[12:15],v[96:99]\n"
"v_add_f32 v18,0xf1c9f2ca,v126\n"
"v_min_f32 v18,v18,v106\n"
"v_add_f32 v19,0xf2177617,v126\n"
"v_mfma_f32_16x16x32_bf16 v[4:7],a[88:91],a[8:11],v[4:7]\n"
"v_min_f32 v19,v19,v107\n"
"v_add_f32 v20,0xf349f2ca,v126\n"
"v_mfma_f32_16x16x32_bf16 v[4:7],a[84:87],a[4:7],v[4:7]\n"
"v_add_f32 v21,0xf35691f6,v126\n"
"v_min_f32 v21,v21,v105\n"
"v_add_f32 v22,0xf3633123,v126\n"
"v_mfma_f32_16x16x32_bf16 v[4:7],a[80:83],a[0:3],v[4:7]\n"
"s_waitcnt lgkmcnt(0)\n"
"v_min_f32 v22,v22,v106\n"
"v_add_f32 v23,0xf36fd050,v126\n"
"v_mfma_f32_16x16x32_bf16 v[192:195],a[96:99],v[72:75],v[192:195]\n"
"v_exp_f32 v56,v56\n"
"v_mfma_f32_16x16x32_bf16 a[136:139],a[144:147],v[72:75],a[136:139]\n"
"v_exp_f32 v57,v57\n"
"v_mfma_f32_16x16x32_bf16 v[196:199],a[100:103],v[72:75],v[196:199]\n"
"v_min_f32 v20,v20,v104\n"
"v_cvt_pk_bf16_f32 v92,v56,v57\n"
"v_mfma_f32_16x16x32_bf16 v[200:203],a[104:107],v[72:75],v[200:203]\n"
"v_max3_f32 v118,v0,v1,v2\n"
"v_max3_f32 v118,v118,v3,v4\n"
"v_mfma_f32_16x16x32_bf16 v[204:207],a[108:111],v[72:75],v[204:207]\n"
"v_max3_f32 v118,v118,v5,v6\n"
"v_max_f32 v118,v118,v7\n"
"v_mfma_f32_16x16x32_bf16 v[208:211],a[112:115],v[72:75],v[208:211]\n"
"v_min_f32 v23,v23,v107\n"
"v_mov_b32 v120,v118\n"
"v_mfma_f32_16x16x32_bf16 v[212:215],a[116:119],v[72:75],v[212:215]\n"
"v_exp_f32 v58,v58\n"
"v_mfma_f32_16x16x32_bf16 v[216:219],a[120:123],v[72:75],v[216:219]\n"
"v_mfma_f32_16x16x32_bf16 v[220:223],a[124:127],v[72:75],v[220:223]\n"
"v_exp_f32 v59,v59\n"
"v_mfma_f32_16x16x32_bf16 v[224:227],a[96:99],v[76:79],v[224:227]\n"
"v_cvt_pk_bf16_f32 v93,v58,v59\n"
"v_mfma_f32_16x16x32_bf16 a[140:143],a[144:147],v[76:79],a[140:143]\n"
"v_accvgpr_read_b32 v122,a136\n"
"v_mfma_f32_16x16x32_bf16 v[228:231],a[100:103],v[76:79],v[228:231]\n"
"v_rcp_f32 v122,v122\n"
"v_mfma_f32_16x16x32_bf16 v[232:235],a[104:107],v[76:79],v[232:235]\n"
"v_pk_mul_f32 v[192:193],v[192:193],v[122:123] op_sel_hi:[1,0]\n"
"v_pk_mul_f32 v[194:195],v[194:195],v[122:123] op_sel_hi:[1,0]\n"
"v_pk_mul_f32 v[196:197],v[196:197],v[122:123] op_sel_hi:[1,0]\n"
"v_pk_mul_f32 v[198:199],v[198:199],v[122:123] op_sel_hi:[1,0]\n"
"v_mfma_f32_16x16x32_bf16 v[236:239],a[108:111],v[76:79],v[236:239]\n"
"v_cvt_pk_bf16_f32 v192,v192,v193\n"
"v_cvt_pk_bf16_f32 v193,v194,v195\n"
"v_mfma_f32_16x16x32_bf16 v[240:243],a[112:115],v[76:79],v[240:243]\n"
"v_cvt_pk_bf16_f32 v194,v196,v197\n"
"v_cvt_pk_bf16_f32 v195,v198,v199\n"
"v_mfma_f32_16x16x32_bf16 v[244:247],a[116:119],v[76:79],v[244:247]\n"
"buffer_store_dwordx4 v[192:195],v112,s[24:27],s70 offen offset:0 sc1\n"
"v_pk_mul_f32 v[200:201],v[200:201],v[122:123] op_sel_hi:[1,0]\n"
"v_pk_mul_f32 v[202:203],v[202:203],v[122:123] op_sel_hi:[1,0]\n"
"v_mfma_f32_16x16x32_bf16 v[248:251],a[120:123],v[76:79],v[248:251]\n"
"v_exp_f32 v60,v60\n"
"v_cvt_pk_bf16_f32 v200,v200,v201\n"
"v_cvt_pk_bf16_f32 v201,v202,v203\n"
"v_pk_mul_f32 v[204:205],v[204:205],v[122:123] op_sel_hi:[1,0]\n"
"v_pk_mul_f32 v[206:207],v[206:207],v[122:123] op_sel_hi:[1,0]\n"
"v_mfma_f32_16x16x32_bf16 v[252:255],a[124:127],v[76:79],v[252:255]\n"
"v_cvt_pk_bf16_f32 v202,v204,v205\n"
"v_cvt_pk_bf16_f32 v203,v206,v207\n"
"v_mfma_f32_16x16x32_bf16 v[12:15],a[80:83],a[16:19],v[100:103]\n"
"buffer_store_dwordx4 v[200:203],v112,s[24:27],s70 offen offset:1024 sc1\n"
"v_pk_mul_f32 v[208:209],v[208:209],v[122:123] op_sel_hi:[1,0]\n"
"v_pk_mul_f32 v[210:211],v[210:211],v[122:123] op_sel_hi:[1,0]\n"
"v_pk_mul_f32 v[212:213],v[212:213],v[122:123] op_sel_hi:[1,0]\n"
"v_pk_mul_f32 v[214:215],v[214:215],v[122:123] op_sel_hi:[1,0]\n"
"v_mfma_f32_16x16x32_bf16 v[12:15],a[84:87],a[20:23],v[12:15]\n"
"v_exp_f32 v61,v61\n"
"v_mfma_f32_16x16x32_bf16 v[12:15],a[88:91],a[24:27],v[12:15]\n"
"v_cvt_pk_bf16_f32 v94,v60,v61\n"
"v_cvt_pk_bf16_f32 v208,v208,v209\n"
"v_mfma_f32_16x16x32_bf16 v[12:15],a[92:95],a[28:31],v[12:15]\n"
"v_cvt_pk_bf16_f32 v209,v210,v211\n"
"v_cvt_pk_bf16_f32 v210,v212,v213\n"
"v_cvt_pk_bf16_f32 v211,v214,v215\n"
"v_mfma_f32_16x16x32_bf16 v[8:11],a[76:79],a[28:31],v[100:103]\n"
"buffer_store_dwordx4 v[208:211],v112,s[24:27],s70 offen offset:2048 sc1\n"
"v_pk_mul_f32 v[216:217],v[216:217],v[122:123] op_sel_hi:[1,0]\n"
"v_pk_mul_f32 v[218:219],v[218:219],v[122:123] op_sel_hi:[1,0]\n"
"v_mfma_f32_16x16x32_bf16 v[8:11],a[72:75],a[24:27],v[8:11]\n"
"v_cvt_pk_bf16_f32 v216,v216,v217\n"
"v_cvt_pk_bf16_f32 v217,v218,v219\n"
"v_pk_mul_f32 v[220:221],v[220:221],v[122:123] op_sel_hi:[1,0]\n"
"v_pk_mul_f32 v[222:223],v[222:223],v[122:123] op_sel_hi:[1,0]\n"
"v_mfma_f32_16x16x32_bf16 v[8:11],a[68:71],a[20:23],v[8:11]\n"
"v_cvt_pk_bf16_f32 v218,v220,v221\n"
"v_cvt_pk_bf16_f32 v219,v222,v223\n"
"v_mfma_f32_16x16x32_bf16 v[8:11],a[64:67],a[16:19],v[8:11]\n"
"buffer_store_dwordx4 v[216:219],v112,s[24:27],s70 offen offset:3072 sc1\n"
"v_mfma_f32_16x16x32_bf16 v[24:27],a[64:67],a[48:51],v[108:111]\n"
"s_add_u32 s70,s70,262144\n"
"v_exp_f32 v62,v62\n"
"v_mfma_f32_16x16x32_bf16 v[24:27],a[68:71],a[52:55],v[24:27]\n"
"v_exp_f32 v63,v63\n"
"v_mfma_f32_16x16x32_bf16 v[24:27],a[72:75],a[56:59],v[24:27]\n"
"s_nop 0\n"
"v_max3_f32 v119,v8,v9,v10\n"
"v_mfma_f32_16x16x32_bf16 v[24:27],a[76:79],a[60:63],v[24:27]\n"
"v_max3_f32 v119,v119,v11,v12\n"
"v_max3_f32 v119,v119,v13,v14\n"
"v_mfma_f32_16x16x32_bf16 v[28:31],a[92:95],a[60:63],v[108:111]\n"
"v_max_f32 v119,v119,v15\n"
"v_mfma_f32_16x16x32_bf16 v[28:31],a[88:91],a[56:59],v[28:31]\n"
"v_min_f32 v120,v120,v119\n"
"v_max_f32 v118,v118,v119\n"
"v_mfma_f32_16x16x32_bf16 v[28:31],a[84:87],a[52:55],v[28:31]\n"
"v_mfma_f32_16x16x32_bf16 v[28:31],a[80:83],a[48:51],v[28:31]\n"
"v_cvt_pk_bf16_f32 v95,v62,v63\n"
"v_mfma_f32_16x16x32_bf16 v[20:23],a[80:83],a[32:35],v[20:23]\n"
"v_mfma_f32_16x16x32_bf16 v[20:23],a[84:87],a[36:39],v[20:23]\n"
"v_mfma_f32_16x16x32_bf16 v[20:23],a[88:91],a[40:43],v[20:23]\n"
"v_max3_f32 v119,v24,v25,v26\n"
"v_mfma_f32_16x16x32_bf16 v[20:23],a[92:95],a[44:47],v[20:23]\n"
"s_nop 1\n"
"v_max3_f32 v119,v119,v27,v28\n"
"v_mfma_f32_16x16x32_bf16 v[16:19],a[76:79],a[44:47],v[16:19]\n"
"v_max3_f32 v119,v119,v29,v30\n"
"v_max_f32 v119,v119,v31\n"
"v_mfma_f32_16x16x32_bf16 v[16:19],a[72:75],a[40:43],v[16:19]\n"
"v_mfma_f32_16x16x32_bf16 v[16:19],a[68:71],a[36:39],v[16:19]\n"
"v_mfma_f32_16x16x32_bf16 v[16:19],a[64:67],a[32:35],v[16:19]\n"
"ds_read_b128 a[64:67],v114 offset:57344\n"
"ds_read_b128 a[68:71],v114 offset:58368\n"
"ds_read_b128 a[72:75],v114 offset:59392\n"
"v_max_f32 v118,v118,v119\n"
"v_min_f32 v120,v120,v119\n"
"v_cmp_gt_f32 s[96:97],s16,v120\n"
"s_nop 1\n"
"v_max3_f32 v119,v16,v17,v18\n"
"v_max3_f32 v119,v119,v19,v20\n"
"v_max3_f32 v119,v119,v21,v22\n"
"v_max_f32 v119,v119,v23\n"
"v_cmp_gt_f32 s[28:29],s16,v119\n"
"v_max_f32 v118,v118,v119\n"
"v_cmp_lt_f32 vcc,s93,v118\n"
"ds_read_b128 a[76:79],v114 offset:60416\n"
"ds_read_b128 a[80:83],v114 offset:61440\n"
"ds_read_b128 a[84:87],v114 offset:62464\n"
"ds_read_b128 a[88:91],v114 offset:63488\n"
"ds_read_b128 a[92:95],v114 offset:64512\n"
"v_mov_b32 v114,v112\n"
"s_sub_u32 s82,s3,32768\n"
"buffer_load_dwordx4 a[0:3],v112,s[24:27],s82 offen offset:0 sc1\n"
"buffer_load_dwordx4 a[4:7],v112,s[24:27],s82 offen offset:1024 sc1\n"
"buffer_load_dwordx4 a[8:11],v112,s[24:27],s82 offen offset:2048 sc1\n"
"buffer_load_dwordx4 a[12:15],v112,s[24:27],s82 offen offset:3072 sc1\n"
"s_sub_u32 s82,s3,49152\n"
"buffer_load_dwordx4 a[16:19],v112,s[24:27],s82 offen offset:0 sc1\n"
"buffer_load_dwordx4 a[20:23],v112,s[24:27],s82 offen offset:1024 sc1\n"
"buffer_load_dwordx4 a[24:27],v112,s[24:27],s82 offen offset:2048 sc1\n"
"buffer_load_dwordx4 a[28:31],v112,s[24:27],s82 offen offset:3072 sc1\n"
"s_and_b32 s28,s28,0xffff\n"
"s_or_b32 s96,s96,s28\n"
"s_or_b64 s[96:97],s[96:97],vcc\n"
"s_cbranch_scc1 .Lfk_cs_c3\n"
".Lfk_cr_c3:\n"
"s_waitcnt vmcnt(13) lgkmcnt(0)\n"
"s_barrier\n"
"v_mfma_f32_16x16x32_bf16 v[224:227],a[64:67],v[92:95],v[224:227]\n"
"ds_read_b128 a[96:99],v114 offset:40960\n"
"v_exp_f32 v0,v0\n"
"v_mfma_f32_16x16x32_bf16 a[140:143],a[144:147],v[92:95],a[140:143]\n"
"ds_read_b128 a[100:103],v114 offset:41984\n"
"v_exp_f32 v1,v1\n"
"v_mfma_f32_16x16x32_bf16 v[228:231],a[68:71],v[92:95],v[228:231]\n"
"ds_read_b128 a[104:107],v114 offset:43008\n"
"v_exp_f32 v2,v2\n"
"v_mfma_f32_16x16x32_bf16 v[232:235],a[72:75],v[92:95],v[232:235]\n"
"ds_read_b128 a[108:111],v114 offset:44032\n"
"v_exp_f32 v3,v3\n"
"v_mfma_f32_16x16x32_bf16 v[236:239],a[76:79],v[92:95],v[236:239]\n"
"ds_read_b128 a[112:115],v114 offset:45056\n"
"v_exp_f32 v4,v4\n"
"v_mfma_f32_16x16x32_bf16 v[240:243],a[80:83],v[92:95],v[240:243]\n"
"ds_read_b128 a[116:119],v114 offset:46080\n"
"v_exp_f32 v5,v5\n"
"v_mfma_f32_16x16x32_bf16 v[244:247],a[84:87],v[92:95],v[244:247]\n"
"ds_read_b128 a[120:123],v114 offset:47104\n"
"v_exp_f32 v6,v6\n"
"v_mfma_f32_16x16x32_bf16 v[248:251],a[88:91],v[92:95],v[248:251]\n"
"v_exp_f32 v7,v7\n"
"v_mfma_f32_16x16x32_bf16 v[252:255],a[92:95],v[92:95],v[252:255]\n"
"v_exp_f32 v8,v8\n"
"v_accvgpr_read_b32 v119,a150\n"
"v_accvgpr_read_b32 v120,a151\n"
"v_xor_b32 v119,s80,v119\n"
"v_xor_b32 v120,s81,v120\n"
"v_or_b32 v119,v119,v120\n"
"v_cmp_ne_u32 s[14:15],0,v119\n"
"s_add_u32 s18,s8,1032192\n"
"s_cmp_lg_u32 s14,0\n"
"s_cselect_b32 s18,2146435072,s18\n"
"s_add_u32 m0,s30,0\n"
"ds_read_b128 a[124:127],v114 offset:48128\n"
"buffer_load_dwordx4 v112,s[24:27],s18 offen offset:0 sc1 lds\n"
"buffer_load_dwordx4 v112,s[24:27],s18 offen offset:1024 sc1 lds\n"
"buffer_load_dwordx4 v112,s[24:27],s18 offen offset:2048 sc1 lds\n"
"buffer_load_dwordx4 v112,s[24:27],s18 offen offset:3072 sc1 lds\n"
"s_add_u32 m0,m0,4096\n"
"s_nop 0\n"
"buffer_load_dwordx4 v117,s[24:27],s18 offen offset:0 sc1 lds\n"
"buffer_load_dwordx4 v117,s[24:27],s18 offen offset:1024 sc1 lds\n"
"buffer_load_dwordx4 v117,s[24:27],s18 offen offset:2048 sc1 lds\n"
"buffer_load_dwordx4 v117,s[24:27],s18 offen offset:3072 sc1 lds\n"
"s_sub_u32 s82,s3,65536\n"
"buffer_load_dwordx4 v[32:35],v112,s[24:27],s82 offen offset:0 sc1\n"
"buffer_load_dwordx4 v[36:39],v112,s[24:27],s82 offen offset:1024 sc1\n"
"buffer_load_dwordx4 v[40:43],v112,s[24:27],s82 offen offset:2048 sc1\n"
"buffer_load_dwordx4 v[44:47],v112,s[24:27],s82 offen offset:3072 sc1\n"
"v_exp_f32 v9,v9\n"
"v_cvt_pk_bf16_f32 v64,v0,v1\n"
"v_exp_f32 v10,v10\n"
"v_cvt_pk_bf16_f32 v65,v2,v3\n"
"v_exp_f32 v11,v11\n"
"v_cvt_pk_bf16_f32 v66,v4,v5\n"
"v_exp_f32 v12,v12\n"
"v_cvt_pk_bf16_f32 v67,v6,v7\n"
"v_exp_f32 v13,v13\n"
"v_cvt_pk_bf16_f32 v68,v8,v9\n"
"v_exp_f32 v14,v14\n"
"v_cvt_pk_bf16_f32 v69,v10,v11\n"
"v_exp_f32 v15,v15\n"
"v_cvt_pk_bf16_f32 v70,v12,v13\n"
"v_exp_f32 v16,v16\n"
"v_cvt_pk_bf16_f32 v71,v14,v15\n"
"v_exp_f32 v17,v17\n"
"v_exp_f32 v18,v18\n"
"v_cvt_pk_bf16_f32 v72,v16,v17\n"
"v_exp_f32 v19,v19\n"
"v_exp_f32 v20,v20\n"
"v_cvt_pk_bf16_f32 v73,v18,v19\n"
"v_exp_f32 v21,v21\n"
"v_exp_f32 v22,v22\n"
"v_cvt_pk_bf16_f32 v74,v20,v21\n"
"v_exp_f32 v23,v23\n"
"v_exp_f32 v24,v24\n"
"v_cvt_pk_bf16_f32 v75,v22,v23\n"
"v_exp_f32 v25,v25\n"
"v_exp_f32 v26,v26\n"
"v_cvt_pk_bf16_f32 v76,v24,v25\n"
"v_exp_f32 v27,v27\n"
"v_exp_f32 v28,v28\n"
"v_cvt_pk_bf16_f32 v77,v26,v27\n"
"v_exp_f32 v29,v29\n"
"v_exp_f32 v30,v30\n"
"v_cvt_pk_bf16_f32 v78,v28,v29\n"
"v_exp_f32 v31,v31\n"
"s_nop 0\n"
"v_cvt_pk_bf16_f32 v79,v30,v31\n"
"v_accvgpr_read_b32 v122,a140\n"
"v_rcp_f32 v122,v122\n"
"s_nop 0\n"
"v_pk_mul_f32 v[224:225],v[224:225],v[122:123] op_sel_hi:[1,0]\n"
"v_pk_mul_f32 v[226:227],v[226:227],v[122:123] op_sel_hi:[1,0]\n"
"v_pk_mul_f32 v[228:229],v[228:229],v[122:123] op_sel_hi:[1,0]\n"
"v_pk_mul_f32 v[230:231],v[230:231],v[122:123] op_sel_hi:[1,0]\n"
"v_cvt_pk_bf16_f32 v224,v224,v225\n"
"v_cvt_pk_bf16_f32 v225,v226,v227\n"
"v_cvt_pk_bf16_f32 v226,v228,v229\n"
"v_cvt_pk_bf16_f32 v227,v230,v231\n"
"buffer_store_dwordx4 v[224:227],v112,s[24:27],s70 offen offset:0 sc1\n"
"v_pk_mul_f32 v[232:233],v[232:233],v[122:123] op_sel_hi:[1,0]\n"
"v_pk_mul_f32 v[234:235],v[234:235],v[122:123] op_sel_hi:[1,0]\n"
"v_pk_mul_f32 v[236:237],v[236:237],v[122:123] op_sel_hi:[1,0]\n"
"v_pk_mul_f32 v[238:239],v[238:239],v[122:123] op_sel_hi:[1,0]\n"
"v_cvt_pk_bf16_f32 v232,v232,v233\n"
"v_cvt_pk_bf16_f32 v233,v234,v235\n"
"v_cvt_pk_bf16_f32 v234,v236,v237\n"
"v_cvt_pk_bf16_f32 v235,v238,v239\n"
"buffer_store_dwordx4 v[232:235],v112,s[24:27],s70 offen offset:1024 sc1\n"
"v_pk_mul_f32 v[240:241],v[240:241],v[122:123] op_sel_hi:[1,0]\n"
"v_pk_mul_f32 v[242:243],v[242:243],v[122:123] op_sel_hi:[1,0]\n"
"v_pk_mul_f32 v[244:245],v[244:245],v[122:123] op_sel_hi:[1,0]\n"
"v_pk_mul_f32 v[246:247],v[246:247],v[122:123] op_sel_hi:[1,0]\n"
"v_cvt_pk_bf16_f32 v240,v240,v241\n"
"v_cvt_pk_bf16_f32 v241,v242,v243\n"
"v_cvt_pk_bf16_f32 v242,v244,v245\n"
"v_cvt_pk_bf16_f32 v243,v246,v247\n"
"buffer_store_dwordx4 v[240:243],v112,s[24:27],s70 offen offset:2048 sc1\n"
"v_pk_mul_f32 v[248:249],v[248:249],v[122:123] op_sel_hi:[1,0]\n"
"v_pk_mul_f32 v[250:251],v[250:251],v[122:123] op_sel_hi:[1,0]\n"
"v_pk_mul_f32 v[252:253],v[252:253],v[122:123] op_sel_hi:[1,0]\n"
"v_pk_mul_f32 v[254:255],v[254:255],v[122:123] op_sel_hi:[1,0]\n"
"v_cvt_pk_bf16_f32 v248,v248,v249\n"
"v_cvt_pk_bf16_f32 v249,v250,v251\n"
"v_cvt_pk_bf16_f32 v250,v252,v253\n"
"v_cvt_pk_bf16_f32 v251,v254,v255\n"
"buffer_store_dwordx4 v[248:251],v112,s[24:27],s70 offen offset:3072 sc1\n"
"s_add_u32 s70,s70,262144\n"
"s_mov_b32 s70,s68\n"
"s_load_dwordx2 s[36:37],s[0:1],0\n"
"s_load_dwordx2 s[44:45],s[0:1],80\n"
"s_load_dwordx2 s[48:49],s[0:1],88\n"
"s_lshl_b32 s82,s78,2\n"
"s_add_u32 s82,s82,65536\n"
"v_add_u32 v113,s82,v112\n"
"s_lshr_b32 s82,s78,2\n"
"s_add_u32 s82,s82,122880\n"
"v_lshrrev_b32 v115,1,v112\n"
"v_add_u32 v116,s82,v115\n"
"s_waitcnt lgkmcnt(0)\n"
"s_barrier\n"
"v_mfma_f32_16x16x32_bf16 v[128:131],a[96:99],v[64:67],0\n"
"s_add_u32 s83,s18,2097152\n"
"s_add_u32 m0,s30,32768\n"
"v_mfma_f32_16x16x32_bf16 a[128:131],a[144:147],v[64:67],0\n"
"buffer_load_dwordx4 v112,s[24:27],s83 offen offset:0 sc1 lds\n"
"v_mfma_f32_16x16x32_bf16 v[132:135],a[100:103],v[64:67],0\n"
"buffer_load_dwordx4 v112,s[24:27],s83 offen offset:1024 sc1 lds\n"
"v_mfma_f32_16x16x32_bf16 v[136:139],a[104:107],v[64:67],0\n"
"buffer_load_dwordx4 v112,s[24:27],s83 offen offset:2048 sc1 lds\n"
"v_mfma_f32_16x16x32_bf16 v[140:143],a[108:111],v[64:67],0\n"
"v_mfma_f32_16x16x32_bf16 v[144:147],a[112:115],v[64:67],0\n"
"buffer_load_dwordx4 v112,s[24:27],s83 offen offset:3072 sc1 lds\n"
"v_mfma_f32_16x16x32_bf16 v[148:151],a[116:119],v[64:67],0\n"
"s_add_u32 m0,m0,4096\n"
"v_mfma_f32_16x16x32_bf16 v[152:155],a[120:123],v[64:67],0\n"
"v_mfma_f32_16x16x32_bf16 v[156:159],a[124:127],v[64:67],0\n"
"buffer_load_dwordx4 v117,s[24:27],s83 offen offset:0 sc1 lds\n"
"v_mfma_f32_16x16x32_bf16 v[160:163],a[96:99],v[68:71],0\n"
"v_accvgpr_read_b32 v97,a128\n"
"v_cvt_pk_bf16_f32 v136,v136,v137\n"
"v_mfma_f32_16x16x32_bf16 a[132:135],a[144:147],v[68:71],0\n"
"buffer_load_dwordx4 v117,s[24:27],s83 offen offset:1024 sc1 lds\n"
"v_mfma_f32_16x16x32_bf16 v[164:167],a[100:103],v[68:71],0\n"
"v_cvt_pk_bf16_f32 v128,v128,v129\n"
"v_cvt_pk_bf16_f32 v129,v130,v131\n"
"v_mfma_f32_16x16x32_bf16 v[168:171],a[104:107],v[68:71],0\n"
"buffer_load_dwordx4 v117,s[24:27],s83 offen offset:2048 sc1 lds\n"
"v_cvt_pk_bf16_f32 v130,v132,v133\n"
"v_cvt_pk_bf16_f32 v131,v134,v135\n"
"v_mfma_f32_16x16x32_bf16 v[172:175],a[108:111],v[68:71],0\n"
"ds_write_b128 v113,v[128:131] offset:0\n"
"v_cvt_pk_bf16_f32 v137,v138,v139\n"
"v_mfma_f32_16x16x32_bf16 v[176:179],a[112:115],v[68:71],0\n"
"buffer_load_dwordx4 v117,s[24:27],s83 offen offset:3072 sc1 lds\n"
"v_cvt_pk_bf16_f32 v138,v140,v141\n"
"v_cvt_pk_bf16_f32 v139,v142,v143\n"
"v_mfma_f32_16x16x32_bf16 v[180:183],a[116:119],v[68:71],0\n"
"s_sub_u32 s82,s3,81920\n"
"ds_write_b128 v113,v[136:139] offset:1024\n"
"v_cvt_pk_bf16_f32 v144,v144,v145\n"
"v_mfma_f32_16x16x32_bf16 v[184:187],a[120:123],v[68:71],0\n"
"buffer_load_dwordx4 a[32:35],v112,s[24:27],s82 offen offset:0 sc1\n"
"v_cvt_pk_bf16_f32 v145,v146,v147\n"
"v_cvt_pk_bf16_f32 v146,v148,v149\n"
"v_cvt_pk_bf16_f32 v147,v150,v151\n"
"v_mfma_f32_16x16x32_bf16 v[188:191],a[124:127],v[68:71],0\n"
"ds_write_b128 v113,v[144:147] offset:2048\n"
"v_cvt_pk_bf16_f32 v152,v152,v153\n"
"v_mfma_f32_16x16x32_bf16 v[192:195],a[96:99],v[72:75],0\n"
"buffer_load_dwordx4 a[36:39],v112,s[24:27],s82 offen offset:1024 sc1\n"
"v_cvt_pk_bf16_f32 v153,v154,v155\n"
"v_cvt_pk_bf16_f32 v154,v156,v157\n"
"v_cvt_pk_bf16_f32 v155,v158,v159\n"
"v_mfma_f32_16x16x32_bf16 a[136:139],a[144:147],v[72:75],0\n"
"ds_write_b128 v113,v[152:155] offset:3072\n"
"ds_write_b64 v116,v[96:97] offset:0\n"
"v_mfma_f32_16x16x32_bf16 v[196:199],a[100:103],v[72:75],0\n"
"buffer_load_dwordx4 a[40:43],v112,s[24:27],s82 offen offset:2048 sc1\n"
"v_accvgpr_read_b32 v101,a132\n"
"v_cvt_pk_bf16_f32 v160,v160,v161\n"
"v_mfma_f32_16x16x32_bf16 v[200:203],a[104:107],v[72:75],0\n"
"v_cvt_pk_bf16_f32 v161,v162,v163\n"
"v_cvt_pk_bf16_f32 v162,v164,v165\n"
"v_cvt_pk_bf16_f32 v163,v166,v167\n"
"v_mfma_f32_16x16x32_bf16 v[204:207],a[108:111],v[72:75],0\n"
"ds_write_b128 v113,v[160:163] offset:4096\n"
"v_cvt_pk_bf16_f32 v168,v168,v169\n"
"v_cvt_pk_bf16_f32 v169,v170,v171\n"
"v_mfma_f32_16x16x32_bf16 v[208:211],a[112:115],v[72:75],0\n"
"buffer_load_dwordx4 a[44:47],v112,s[24:27],s82 offen offset:3072 sc1\n"
"v_cvt_pk_bf16_f32 v170,v172,v173\n"
"v_cvt_pk_bf16_f32 v171,v174,v175\n"
"v_mfma_f32_16x16x32_bf16 v[212:215],a[116:119],v[72:75],0\n"
"ds_write_b128 v113,v[168:171] offset:5120\n"
"v_cvt_pk_bf16_f32 v176,v176,v177\n"
"v_cvt_pk_bf16_f32 v177,v178,v179\n"
"v_mfma_f32_16x16x32_bf16 v[216:219],a[120:123],v[72:75],0\n"
"v_cvt_pk_bf16_f32 v178,v180,v181\n"
"v_cvt_pk_bf16_f32 v179,v182,v183\n"
"v_mfma_f32_16x16x32_bf16 v[220:223],a[124:127],v[72:75],0\n"
"ds_write_b128 v113,v[176:179] offset:6144\n"
"v_cvt_pk_bf16_f32 v184,v184,v185\n"
"v_cvt_pk_bf16_f32 v185,v186,v187\n"
"v_cvt_pk_bf16_f32 v186,v188,v189\n"
"v_cvt_pk_bf16_f32 v187,v190,v191\n"
"v_mfma_f32_16x16x32_bf16 v[224:227],a[96:99],v[76:79],0\n"
"ds_write_b128 v113,v[184:187] offset:7168\n"
"v_mfma_f32_16x16x32_bf16 a[140:143],a[144:147],v[76:79],0\n"
"s_sub_u32 s82,s3,131072\n"
"ds_write_b64 v116,v[100:101] offset:512\n"
"buffer_load_dwordx4 v[128:131],v112,s[24:27],s82 offen offset:0 sc1\n"
"buffer_load_dwordx4 v[132:135],v112,s[24:27],s82 offen offset:1024 sc1\n"
"v_mfma_f32_16x16x32_bf16 v[228:231],a[100:103],v[76:79],0\n"
"buffer_load_dwordx4 v[136:139],v112,s[24:27],s82 offen offset:2048 sc1\n"
"buffer_load_dwordx4 v[140:143],v112,s[24:27],s82 offen offset:3072 sc1\n"
"s_sub_u32 s82,s3,147456\n"
"v_mfma_f32_16x16x32_bf16 v[232:235],a[104:107],v[76:79],0\n"
"buffer_load_dwordx4 v[144:147],v112,s[24:27],s82 offen offset:0 sc1\n"
"buffer_load_dwordx4 v[148:151],v112,s[24:27],s82 offen offset:1024 sc1\n"
"buffer_load_dwordx4 v[152:155],v112,s[24:27],s82 offen offset:2048 sc1\n"
"v_mfma_f32_16x16x32_bf16 v[236:239],a[108:111],v[76:79],0\n"
"buffer_load_dwordx4 v[156:159],v112,s[24:27],s82 offen offset:3072 sc1\n"
"v_mfma_f32_16x16x32_bf16 v[240:243],a[112:115],v[76:79],0\n"
"v_mfma_f32_16x16x32_bf16 v[244:247],a[116:119],v[76:79],0\n"
"v_mfma_f32_16x16x32_bf16 v[248:251],a[120:123],v[76:79],0\n"
"v_mfma_f32_16x16x32_bf16 v[252:255],a[124:127],v[76:79],0\n"
"s_waitcnt vmcnt(20)\n"
"buffer_store_dwordx2 a[154:155],off,s[24:27],s69 sc1\n"
"s_branch .Lfk_endB\n"
".Lfk_head0:\n"
"s_mov_b32 s64,s61\n"
"s_cmp_eq_u32 s52,3\n"
"s_cselect_b32 s67,172032,0\n"
"s_waitcnt lgkmcnt(0)\n"
"s_barrier\n"
"v_mfma_f32_16x16x32_bf16 v[24:27],a[64:67],a[48:51],v[108:111]\n"
"v_add_f32 v0,0x80000000,v126\n"
"v_min_f32 v0,v0,v96\n"
"v_mfma_f32_16x16x32_bf16 v[24:27],a[68:71],a[52:55],v[24:27]\n"
"v_add_f32 v1,0xf149f2ca,v126\n"
"v_min_f32 v1,v1,v97\n"
"v_mfma_f32_16x16x32_bf16 v[24:27],a[72:75],a[56:59],v[24:27]\n"
"v_add_f32 v2,0xf1c9f2ca,v126\n"
"v_min_f32 v2,v2,v98\n"
"v_mfma_f32_16x16x32_bf16 v[24:27],a[76:79],a[60:63],v[24:27]\n"
"v_add_f32 v3,0xf2177617,v126\n"
"v_min_f32 v3,v3,v99\n"
"v_mfma_f32_16x16x32_bf16 v[28:31],a[92:95],a[60:63],v[108:111]\n"
"v_add_f32 v4,0xf349f2ca,v126\n"
"v_min_f32 v4,v4,v96\n"
"v_mfma_f32_16x16x32_bf16 v[28:31],a[88:91],a[56:59],v[28:31]\n"
"v_add_f32 v5,0xf35691f6,v126\n"
"v_min_f32 v5,v5,v97\n"
"v_mfma_f32_16x16x32_bf16 v[28:31],a[84:87],a[52:55],v[28:31]\n"
"v_add_f32 v6,0xf3633123,v126\n"
"v_min_f32 v6,v6,v98\n"
"v_mfma_f32_16x16x32_bf16 v[28:31],a[80:83],a[48:51],v[28:31]\n"
"v_add_f32 v7,0xf36fd050,v126\n"
"v_min_f32 v7,v7,v99\n"
"s_nop 1\n"
"v_mfma_f32_16x16x32_bf16 v[4:7],a[80:83],a[0:3],v[4:7]\n"
"s_andn2_b32 m0,s55,16384\n"
"s_add_u32 s54,s54,s22\n"
"v_mfma_f32_16x16x32_bf16 v[4:7],a[84:87],a[4:7],v[4:7]\n"
"ds_read_b128 a[96:99],v113 offset:16384\n"
"ds_read_b128 a[100:103],v113 offset:17408\n"
"v_mfma_f32_16x16x32_bf16 v[4:7],a[88:91],a[8:11],v[4:7]\n"
"ds_read_b128 a[104:107],v113 offset:18432\n"
"ds_read_b128 a[108:111],v113 offset:19456\n"
"v_mfma_f32_16x16x32_bf16 v[4:7],a[92:95],a[12:15],v[4:7]\n"
"ds_read_b128 a[112:115],v113 offset:20480\n"
"ds_read_b128 a[116:119],v113 offset:21504\n"
"v_mfma_f32_16x16x32_bf16 v[0:3],a[76:79],a[12:15],v[0:3]\n"
"ds_read_b128 a[120:123],v113 offset:22528\n"
"ds_read_b128 a[124:127],v113 offset:23552\n"
"v_mfma_f32_16x16x32_bf16 v[0:3],a[72:75],a[8:11],v[0:3]\n"
"buffer_load_dwordx4 v115,s[32:35],s54 offen sc1 lds\n"
"v_mfma_f32_16x16x32_bf16 v[0:3],a[68:71],a[4:7],v[0:3]\n"
"s_add_u32 m0,s67,0\n"
"v_max3_f32 v118,v24,v25,v26\n"
"v_mfma_f32_16x16x32_bf16 v[0:3],a[64:67],a[0:3],v[0:3]\n"
"buffer_load_dwordx4 v112,s[24:27],s64 offen offset:0 sc1 lds\n"
"v_mfma_f32_16x16x32_bf16 v[8:11],a[64:67],a[16:19],v[100:103]\n"
"buffer_load_dwordx4 v112,s[24:27],s64 offen offset:1024 sc1 lds\n"
"v_mfma_f32_16x16x32_bf16 v[8:11],a[68:71],a[20:23],v[8:11]\n"
"buffer_load_dwordx4 v112,s[24:27],s64 offen offset:2048 sc1 lds\n"
"v_mfma_f32_16x16x32_bf16 v[8:11],a[72:75],a[24:27],v[8:11]\n"
"buffer_load_dwordx4 v112,s[24:27],s64 offen offset:3072 sc1 lds\n"
"v_mfma_f32_16x16x32_bf16 v[8:11],a[76:79],a[28:31],v[8:11]\n"
"s_add_u32 m0,s67,4096\n"
"v_max3_f32 v118,v118,v27,v28\n"
"v_cvt_pk_bf16_f32 v192,v192,v193\n"
"v_mfma_f32_16x16x32_bf16 v[12:15],a[92:95],a[28:31],v[100:103]\n"
"buffer_load_dwordx4 v117,s[24:27],s64 offen offset:0 sc1 lds\n"
"v_mfma_f32_16x16x32_bf16 v[12:15],a[88:91],a[24:27],v[12:15]\n"
"buffer_load_dwordx4 v117,s[24:27],s64 offen offset:1024 sc1 lds\n"
"v_mfma_f32_16x16x32_bf16 v[12:15],a[84:87],a[20:23],v[12:15]\n"
"buffer_load_dwordx4 v117,s[24:27],s64 offen offset:2048 sc1 lds\n"
"v_mfma_f32_16x16x32_bf16 v[12:15],a[80:83],a[16:19],v[12:15]\n"
"buffer_load_dwordx4 v117,s[24:27],s64 offen offset:3072 sc1 lds\n"
"v_cvt_pk_bf16_f32 v193,v194,v195\n"
"v_cvt_pk_bf16_f32 v194,v196,v197\n"
"v_cvt_pk_bf16_f32 v195,v198,v199\n"
"v_cvt_pk_bf16_f32 v200,v200,v201\n"
"v_cvt_pk_bf16_f32 v201,v202,v203\n"
"v_cvt_pk_bf16_f32 v202,v204,v205\n"
"v_cvt_pk_bf16_f32 v203,v206,v207\n"
"ds_write_b128 v116,v[192:195] offset:32768\n"
"ds_write_b128 v116,v[200:203] offset:33792\n"
"v_cvt_pk_bf16_f32 v208,v208,v209\n"
"v_cvt_pk_bf16_f32 v209,v210,v211\n"
"v_cvt_pk_bf16_f32 v210,v212,v213\n"
"v_cvt_pk_bf16_f32 v211,v214,v215\n"
"v_cvt_pk_bf16_f32 v216,v216,v217\n"
"v_cvt_pk_bf16_f32 v217,v218,v219\n"
"v_cvt_pk_bf16_f32 v218,v220,v221\n"
"v_cvt_pk_bf16_f32 v219,v222,v223\n"
"ds_write_b128 v116,v[208:211] offset:34816\n"
"ds_write_b128 v116,v[216:219] offset:35840\n"
"ds_read_b128 a[32:35],v116 offset:32768\n"
"ds_read_b128 a[36:39],v116 offset:33792\n"
"ds_read_b128 a[40:43],v116 offset:34816\n"
"ds_read_b128 a[44:47],v116 offset:35840\n"
"s_waitcnt lgkmcnt(0)\n"
"v_mfma_f32_16x16x32_bf16 v[20:23],a[80:83],a[32:35],v[104:107]\n"
"s_add_u32 s64,s64,16384\n"
"v_max3_f32 v118,v118,v29,v30\n"
"v_mfma_f32_16x16x32_bf16 v[20:23],a[84:87],a[36:39],v[20:23]\n"
"v_max_f32 v118,v118,v31\n"
"v_mov_b32 v120,v118\n"
"v_mfma_f32_16x16x32_bf16 v[20:23],a[88:91],a[40:43],v[20:23]\n"
"v_max3_f32 v119,v0,v1,v2\n"
"v_max3_f32 v119,v119,v3,v4\n"
"v_mfma_f32_16x16x32_bf16 v[20:23],a[92:95],a[44:47],v[20:23]\n"
"buffer_store_dwordx4 a[224:227],v112,s[24:27],s56 offen offset:0 sc1\n"
"v_max3_f32 v119,v119,v5,v6\n"
"v_mfma_f32_16x16x32_bf16 v[16:19],a[76:79],a[44:47],v[104:107]\n"
"v_max_f32 v119,v119,v7\n"
"v_cmp_gt_f32 s[28:29],s16,v119\n"
"v_mfma_f32_16x16x32_bf16 v[16:19],a[72:75],a[40:43],v[16:19]\n"
"v_max_f32 v118,v118,v119\n"
"v_max3_f32 v119,v8,v9,v10\n"
"v_mfma_f32_16x16x32_bf16 v[16:19],a[68:71],a[36:39],v[16:19]\n"
"v_max3_f32 v119,v119,v11,v12\n"
"v_max3_f32 v119,v119,v13,v14\n"
"v_mfma_f32_16x16x32_bf16 v[16:19],a[64:67],a[32:35],v[16:19]\n"
"s_and_b32 s28,s28,0xffff\n"
"v_max_f32 v119,v119,v15\n"
"v_min_f32 v120,v120,v119\n"
"v_max_f32 v118,v118,v119\n"
"s_nop 3\n"
"v_max3_f32 v119,v16,v17,v18\n"
"v_max3_f32 v119,v119,v19,v20\n"
"v_max3_f32 v119,v119,v21,v22\n"
"v_max_f32 v119,v119,v23\n"
"v_min_f32 v120,v120,v119\n"
"v_max_f32 v118,v118,v119\n"
"v_cmp_gt_f32 s[96:97],s16,v120\n"
"v_cmp_lt_f32 vcc,s93,v118\n"
"s_or_b32 s96,s96,s28\n"
"s_or_b64 s[96:97],s[96:97],vcc\n"
"s_cbranch_scc1 .Lfk_cs_z3\n"
".Lfk_cr_z3:\n"
"s_waitcnt vmcnt(18) lgkmcnt(0)\n"
"s_barrier\n"
"v_mfma_f32_16x16x32_bf16 v[48:51],a[96:99],a[32:35],v[104:107]\n"
"v_add_f32 v40,0x80000000,v126\n"
"v_min_f32 v40,v40,v100\n"
"v_add_f32 v41,0xf149f2ca,v126\n"
"v_min_f32 v41,v41,v101\n"
"v_mfma_f32_16x16x32_bf16 v[48:51],a[100:103],a[36:39],v[48:51]\n"
"v_add_f32 v42,0xf1c9f2ca,v126\n"
"v_min_f32 v42,v42,v102\n"
"v_add_f32 v43,0xf2177617,v126\n"
"v_min_f32 v43,v43,v103\n"
"v_mfma_f32_16x16x32_bf16 v[48:51],a[104:107],a[40:43],v[48:51]\n"
"v_add_f32 v44,0xf349f2ca,v126\n"
"v_min_f32 v44,v44,v100\n"
"v_add_f32 v45,0xf35691f6,v126\n"
"v_min_f32 v45,v45,v101\n"
"v_mfma_f32_16x16x32_bf16 v[48:51],a[108:111],a[44:47],v[48:51]\n"
"v_add_f32 v46,0xf3633123,v126\n"
"v_min_f32 v46,v46,v102\n"
"v_add_f32 v47,0xf36fd050,v126\n"
"v_min_f32 v47,v47,v103\n"
"v_mfma_f32_16x16x32_bf16 v[52:55],a[124:127],a[44:47],v[104:107]\n"
"s_add_u32 m0,s55,4096\n"
"v_exp_f32 v0,v0\n"
"s_add_u32 s54,s54,s22\n"
"v_mfma_f32_16x16x32_bf16 v[52:55],a[120:123],a[40:43],v[52:55]\n"
"buffer_store_dwordx4 a[160:163],v112,s[24:27],s3 offen offset:0 sc1\n"
"buffer_load_dwordx4 v115,s[32:35],s54 offen sc1 lds\n"
"v_exp_f32 v1,v1\n"
"v_mfma_f32_16x16x32_bf16 v[52:55],a[116:119],a[36:39],v[52:55]\n"
"s_add_u32 m0,s67,16384\n"
"v_exp_f32 v2,v2\n"
"v_mfma_f32_16x16x32_bf16 v[52:55],a[112:115],a[32:35],v[52:55]\n"
"buffer_load_dwordx4 v112,s[24:27],s64 offen offset:0 sc1 lds\n"
"buffer_load_dwordx4 v112,s[24:27],s64 offen offset:1024 sc1 lds\n"
"v_exp_f32 v3,v3\n"
"v_exp_f32 v4,v4\n"
"v_mfma_f32_16x16x32_bf16 v[60:63],a[112:115],a[48:51],v[108:111]\n"
"buffer_load_dwordx4 v112,s[24:27],s64 offen offset:2048 sc1 lds\n"
"v_exp_f32 v5,v5\n"
"v_mfma_f32_16x16x32_bf16 v[60:63],a[116:119],a[52:55],v[60:63]\n"
"ds_read_b128 a[64:67],v113 offset:49152\n"
"ds_read_b128 a[68:71],v113 offset:50176\n"
"v_exp_f32 v6,v6\n"
"v_mfma_f32_16x16x32_bf16 v[60:63],a[120:123],a[56:59],v[60:63]\n"
"ds_read_b128 a[72:75],v113 offset:51200\n"
"ds_read_b128 a[76:79],v113 offset:52224\n"
"v_exp_f32 v7,v7\n"
"v_mfma_f32_16x16x32_bf16 v[60:63],a[124:127],a[60:63],v[60:63]\n"
"ds_read_b128 a[80:83],v113 offset:53248\n"
"ds_read_b128 a[84:87],v113 offset:54272\n"
"v_exp_f32 v8,v8\n"
"v_mfma_f32_16x16x32_bf16 v[56:59],a[108:111],a[60:63],v[108:111]\n"
"ds_read_b128 a[88:91],v113 offset:55296\n"
"ds_read_b128 a[92:95],v113 offset:56320\n"
"v_exp_f32 v9,v9\n"
"v_mfma_f32_16x16x32_bf16 v[56:59],a[104:107],a[56:59],v[56:59]\n"
"buffer_load_dwordx4 v112,s[24:27],s64 offen offset:3072 sc1 lds\n"
"v_mfma_f32_16x16x32_bf16 v[56:59],a[100:103],a[52:55],v[56:59]\n"
"s_add_u32 m0,s67,20480\n"
"v_exp_f32 v10,v10\n"
"v_mfma_f32_16x16x32_bf16 v[56:59],a[96:99],a[48:51],v[56:59]\n"
"buffer_load_dwordx4 v117,s[24:27],s64 offen offset:0 sc1 lds\n"
"buffer_load_dwordx4 v117,s[24:27],s64 offen offset:1024 sc1 lds\n"
"v_exp_f32 v11,v11\n"
"v_exp_f32 v12,v12\n"
"v_mfma_f32_16x16x32_bf16 v[40:43],a[96:99],a[16:19],v[40:43]\n"
"buffer_load_dwordx4 v117,s[24:27],s64 offen offset:2048 sc1 lds\n"
"v_exp_f32 v13,v13\n"
"v_mfma_f32_16x16x32_bf16 v[40:43],a[100:103],a[20:23],v[40:43]\n"
"buffer_store_dwordx4 a[164:167],v112,s[24:27],s3 offen offset:1024 sc1\n"
"v_cvt_pk_bf16_f32 v64,v0,v1\n"
"v_exp_f32 v14,v14\n"
"v_mfma_f32_16x16x32_bf16 v[40:43],a[104:107],a[24:27],v[40:43]\n"
"buffer_load_dwordx4 v117,s[24:27],s64 offen offset:3072 sc1 lds\n"
"v_mfma_f32_16x16x32_bf16 v[40:43],a[108:111],a[28:31],v[40:43]\n"
"s_add_u32 s64,s64,16384\n"
"v_exp_f32 v15,v15\n"
"v_cvt_pk_bf16_f32 v65,v2,v3\n"
"v_cvt_pk_bf16_f32 v66,v4,v5\n"
"v_exp_f32 v16,v16\n"
"v_mfma_f32_16x16x32_bf16 v[44:47],a[124:127],a[28:31],v[44:47]\n"
"buffer_store_dwordx4 a[228:231],v112,s[24:27],s56 offen offset:1024 sc1\n"
"v_cvt_pk_bf16_f32 v67,v6,v7\n"
"v_mfma_f32_16x16x32_bf16 v[44:47],a[120:123],a[24:27],v[44:47]\n"
"v_exp_f32 v17,v17\n"
"v_cvt_pk_bf16_f32 v68,v8,v9\n"
"v_cvt_pk_bf16_f32 v69,v10,v11\n"
"v_mfma_f32_16x16x32_bf16 v[44:47],a[116:119],a[20:23],v[44:47]\n"
"v_exp_f32 v18,v18\n"
"v_cvt_pk_bf16_f32 v70,v12,v13\n"
"v_cvt_pk_bf16_f32 v71,v14,v15\n"
"v_mfma_f32_16x16x32_bf16 v[44:47],a[112:115],a[16:19],v[44:47]\n"
"v_exp_f32 v19,v19\n"
"v_cvt_pk_bf16_f32 v72,v16,v17\n"
"v_max3_f32 v118,v48,v49,v50\n"
"v_exp_f32 v20,v20\n"
"v_cvt_pk_bf16_f32 v73,v18,v19\n"
"v_exp_f32 v21,v21\n"
"ds_read_b128 a[0:3],v116 offset:40960\n"
"ds_read_b128 a[4:7],v116 offset:41984\n"
"ds_read_b128 a[8:11],v116 offset:43008\n"
"ds_read_b128 a[12:15],v116 offset:44032\n"
"v_exp_f32 v22,v22\n"
"v_exp_f32 v23,v23\n"
"v_exp_f32 v24,v24\n"
"v_cvt_pk_bf16_f32 v75,v22,v23\n"
"v_exp_f32 v25,v25\n"
"v_exp_f32 v26,v26\n"
"v_cvt_pk_bf16_f32 v76,v24,v25\n"
"v_exp_f32 v27,v27\n"
"v_exp_f32 v28,v28\n"
"v_cvt_pk_bf16_f32 v77,v26,v27\n"
"v_exp_f32 v29,v29\n"
"v_exp_f32 v30,v30\n"
"v_cvt_pk_bf16_f32 v78,v28,v29\n"
"v_exp_f32 v31,v31\n"
"v_cvt_pk_bf16_f32 v74,v20,v21\n"
"v_cvt_pk_bf16_f32 v79,v30,v31\n"
"v_max3_f32 v118,v118,v51,v52\n"
"v_max3_f32 v118,v118,v53,v54\n"
"v_max3_f32 v118,v118,v55,v60\n"
"v_max3_f32 v118,v118,v61,v62\n"
"v_max3_f32 v118,v118,v63,v56\n"
"v_max3_f32 v118,v118,v57,v58\n"
"v_max3_f32 v118,v118,v59,v40\n"
"v_max3_f32 v118,v118,v41,v42\n"
"v_max3_f32 v118,v118,v43,v44\n"
"v_max3_f32 v118,v118,v45,v46\n"
"v_max_f32 v118,v118,v47\n"
"v_cmp_lt_f32 vcc,s93,v118\n"
"s_nop 0\n"
"s_cbranch_vccnz .Lfk_cs_z0\n"
".Lfk_cr_z0:\n"
"s_waitcnt vmcnt(26) lgkmcnt(0)\n"
"s_barrier\n"
"v_mfma_f32_16x16x32_bf16 v[24:27],a[64:67],a[48:51],v[108:111]\n"
"ds_read_b128 a[96:99],v114 offset:40960\n"
"ds_read_b128 a[100:103],v114 offset:41984\n"
"v_mfma_f32_16x16x32_bf16 v[24:27],a[68:71],a[52:55],v[24:27]\n"
"ds_read_b128 a[104:107],v114 offset:43008\n"
"ds_read_b128 a[108:111],v114 offset:44032\n"
"v_mfma_f32_16x16x32_bf16 v[24:27],a[72:75],a[56:59],v[24:27]\n"
"ds_read_b128 a[112:115],v114 offset:45056\n"
"ds_read_b128 a[116:119],v114 offset:46080\n"
"v_mfma_f32_16x16x32_bf16 v[24:27],a[76:79],a[60:63],v[24:27]\n"
"ds_read_b128 a[120:123],v114 offset:47104\n"
"ds_read_b128 a[124:127],v114 offset:48128\n"
"v_mfma_f32_16x16x32_bf16 v[28:31],a[92:95],a[60:63],v[108:111]\n"
"v_add_f32 v16,0x80000000,v126\n"
"v_min_f32 v16,v16,v104\n"
"v_mfma_f32_16x16x32_bf16 v[28:31],a[88:91],a[56:59],v[28:31]\n"
"v_add_f32 v17,0xf149f2ca,v126\n"
"v_min_f32 v17,v17,v105\n"
"v_mfma_f32_16x16x32_bf16 v[28:31],a[84:87],a[52:55],v[28:31]\n"
"v_add_f32 v18,0xf1c9f2ca,v126\n"
"v_min_f32 v18,v18,v106\n"
"v_mfma_f32_16x16x32_bf16 v[28:31],a[80:83],a[48:51],v[28:31]\n"
"v_add_f32 v20,0xf349f2ca,v126\n"
"v_min_f32 v20,v20,v104\n"
"v_add_f32 v21,0xf35691f6,v126\n"
"v_min_f32 v21,v21,v105\n"
"v_add_f32 v22,0xf3633123,v126\n"
"v_min_f32 v22,v22,v106\n"
"v_add_f32 v23,0xf36fd050,v126\n"
"v_min_f32 v23,v23,v107\n"
"v_add_f32 v19,0xf2177617,v126\n"
"v_min_f32 v19,v19,v107\n"
"v_mfma_f32_16x16x32_bf16 v[20:23],a[80:83],a[32:35],v[20:23]\n"
"s_add_u32 m0,s55,8192\n"
"v_exp_f32 v40,v40\n"
"v_mfma_f32_16x16x32_bf16 v[20:23],a[84:87],a[36:39],v[20:23]\n"
"s_add_u32 s54,s54,s22\n"
"v_exp_f32 v41,v41\n"
"v_mfma_f32_16x16x32_bf16 v[20:23],a[88:91],a[40:43],v[20:23]\n"
"v_exp_f32 v42,v42\n"
"v_mfma_f32_16x16x32_bf16 v[20:23],a[92:95],a[44:47],v[20:23]\n"
"v_exp_f32 v43,v43\n"
"v_mfma_f32_16x16x32_bf16 v[16:19],a[76:79],a[44:47],v[16:19]\n"
"v_exp_f32 v44,v44\n"
"v_mfma_f32_16x16x32_bf16 v[16:19],a[72:75],a[40:43],v[16:19]\n"
"v_exp_f32 v45,v45\n"
"v_mfma_f32_16x16x32_bf16 v[16:19],a[68:71],a[36:39],v[16:19]\n"
"v_exp_f32 v46,v46\n"
"v_mfma_f32_16x16x32_bf16 v[16:19],a[64:67],a[32:35],v[16:19]\n"
"s_waitcnt lgkmcnt(0)\n"
"v_exp_f32 v47,v47\n"
"v_mfma_f32_16x16x32_bf16 v[128:131],a[96:99],v[64:67],0\n"
"v_exp_f32 v48,v48\n"
"v_mfma_f32_16x16x32_bf16 v[160:163],a[96:99],v[68:71],0\n"
"v_exp_f32 v49,v49\n"
"v_mfma_f32_16x16x32_bf16 v[192:195],a[96:99],v[72:75],0\n"
"ds_read_b128 a[64:67],v113 offset:32768\n"
"ds_read_b128 a[68:71],v113 offset:33792\n"
"v_mfma_f32_16x16x32_bf16 v[224:227],a[96:99],v[76:79],0\n"
"v_exp_f32 v50,v50\n"
"v_mfma_f32_16x16x32_bf16 a[140:143],a[144:147],v[76:79],0\n"
"ds_read_b128 a[72:75],v113 offset:34816\n"
"ds_read_b128 a[76:79],v113 offset:35840\n"
"v_mfma_f32_16x16x32_bf16 v[228:231],a[100:103],v[76:79],0\n"
"v_exp_f32 v51,v51\n"
"v_mfma_f32_16x16x32_bf16 v[132:135],a[100:103],v[64:67],0\n"
"ds_read_b128 a[80:83],v113 offset:36864\n"
"ds_read_b128 a[84:87],v113 offset:37888\n"
"v_mfma_f32_16x16x32_bf16 v[164:167],a[100:103],v[68:71],0\n"
"v_exp_f32 v52,v52\n"
"v_mfma_f32_16x16x32_bf16 v[196:199],a[100:103],v[72:75],0\n"
"v_exp_f32 v53,v53\n"
"v_mfma_f32_16x16x32_bf16 a[136:139],a[144:147],v[72:75],0\n"
"ds_read_b128 a[88:91],v113 offset:38912\n"
"ds_read_b128 a[92:95],v113 offset:39936\n"
"v_mfma_f32_16x16x32_bf16 v[200:203],a[104:107],v[72:75],0\n"
"v_exp_f32 v54,v54\n"
"v_mfma_f32_16x16x32_bf16 v[232:235],a[104:107],v[76:79],0\n"
"buffer_load_dwordx4 v115,s[32:35],s54 offen sc1 lds\n"
"v_mfma_f32_16x16x32_bf16 v[136:139],a[104:107],v[64:67],0\n"
"s_add_u32 m0,s67,49152\n"
"v_exp_f32 v55,v55\n"
"v_mfma_f32_16x16x32_bf16 v[168:171],a[104:107],v[68:71],0\n"
"buffer_load_dwordx4 v112,s[24:27],s64 offen offset:0 sc1 lds\n"
"v_mfma_f32_16x16x32_bf16 a[132:135],a[144:147],v[68:71],0\n"
"v_exp_f32 v56,v56\n"
"v_mfma_f32_16x16x32_bf16 v[172:175],a[108:111],v[68:71],0\n"
"buffer_load_dwordx4 v112,s[24:27],s64 offen offset:1024 sc1 lds\n"
"v_mfma_f32_16x16x32_bf16 v[204:207],a[108:111],v[72:75],0\n"
"v_exp_f32 v57,v57\n"
"v_mfma_f32_16x16x32_bf16 v[236:239],a[108:111],v[76:79],0\n"
"buffer_load_dwordx4 v112,s[24:27],s64 offen offset:2048 sc1 lds\n"
"v_mfma_f32_16x16x32_bf16 v[140:143],a[108:111],v[64:67],0\n"
"v_exp_f32 v58,v58\n"
"v_mfma_f32_16x16x32_bf16 a[128:131],a[144:147],v[64:67],0\n"
"buffer_load_dwordx4 v112,s[24:27],s64 offen offset:3072 sc1 lds\n"
"v_mfma_f32_16x16x32_bf16 v[144:147],a[112:115],v[64:67],0\n"
"s_add_u32 m0,s67,53248\n"
"v_exp_f32 v59,v59\n"
"v_mfma_f32_16x16x32_bf16 v[176:179],a[112:115],v[68:71],0\n"
"buffer_load_dwordx4 v117,s[24:27],s64 offen offset:0 sc1 lds\n"
"v_mfma_f32_16x16x32_bf16 v[208:211],a[112:115],v[72:75],0\n"
"v_exp_f32 v60,v60\n"
"v_mfma_f32_16x16x32_bf16 v[240:243],a[112:115],v[76:79],0\n"
"buffer_load_dwordx4 v117,s[24:27],s64 offen offset:1024 sc1 lds\n"
"v_mfma_f32_16x16x32_bf16 v[244:247],a[116:119],v[76:79],0\n"
"v_exp_f32 v61,v61\n"
"v_mfma_f32_16x16x32_bf16 v[148:151],a[116:119],v[64:67],0\n"
"buffer_load_dwordx4 v117,s[24:27],s64 offen offset:2048 sc1 lds\n"
"v_mfma_f32_16x16x32_bf16 v[180:183],a[116:119],v[68:71],0\n"
"v_exp_f32 v62,v62\n"
"v_mfma_f32_16x16x32_bf16 v[212:215],a[116:119],v[72:75],0\n"
"buffer_load_dwordx4 v117,s[24:27],s64 offen offset:3072 sc1 lds\n"
"v_mfma_f32_16x16x32_bf16 v[216:219],a[120:123],v[72:75],0\n"
"s_add_u32 s64,s64,16384\n"
"v_exp_f32 v63,v63\n"
"v_mfma_f32_16x16x32_bf16 v[248:251],a[120:123],v[76:79],0\n"
"buffer_store_dwordx4 a[232:235],v112,s[24:27],s56 offen offset:2048 sc1\n"
"s_and_b32 s67,s67,8192\n"
"v_mfma_f32_16x16x32_bf16 v[152:155],a[120:123],v[64:67],0\n"
"buffer_store_dwordx4 a[168:171],v112,s[24:27],s3 offen offset:2048 sc1\n"
"buffer_store_dwordx4 a[172:175],v112,s[24:27],s3 offen offset:3072 sc1\n"
"v_mfma_f32_16x16x32_bf16 v[184:187],a[120:123],v[68:71],0\n"
"s_sub_u32 s3,s3,16384\n"
"buffer_store_dwordx4 a[176:179],v112,s[24:27],s3 offen offset:0 sc1\n"
"v_mfma_f32_16x16x32_bf16 v[188:191],a[124:127],v[68:71],0\n"
"v_cvt_pk_bf16_f32 v84,v40,v41\n"
"v_cvt_pk_bf16_f32 v85,v42,v43\n"
"v_mfma_f32_16x16x32_bf16 v[220:223],a[124:127],v[72:75],0\n"
"v_cvt_pk_bf16_f32 v86,v44,v45\n"
"v_cvt_pk_bf16_f32 v87,v46,v47\n"
"v_mfma_f32_16x16x32_bf16 v[252:255],a[124:127],v[76:79],0\n"
"v_cvt_pk_bf16_f32 v88,v48,v49\n"
"v_cvt_pk_bf16_f32 v89,v50,v51\n"
"v_mfma_f32_16x16x32_bf16 v[156:159],a[124:127],v[64:67],0\n"
"v_cvt_pk_bf16_f32 v90,v52,v53\n"
"v_cvt_pk_bf16_f32 v91,v54,v55\n"
"v_cvt_pk_bf16_f32 v92,v56,v57\n"
"ds_read_b128 a[16:19],v116 offset:8192\n"
"ds_read_b128 a[20:23],v116 offset:9216\n"
"ds_read_b128 a[24:27],v116 offset:10240\n"
"ds_read_b128 a[28:31],v116 offset:11264\n"
"v_cvt_pk_bf16_f32 v93,v58,v59\n"
"v_cvt_pk_bf16_f32 v94,v60,v61\n"
"v_cvt_pk_bf16_f32 v95,v62,v63\n"
"v_max3_f32 v118,v24,v25,v26\n"
"v_max3_f32 v118,v118,v27,v28\n"
"v_max3_f32 v118,v118,v29,v30\n"
"v_max3_f32 v118,v118,v31,v20\n"
"v_max3_f32 v118,v118,v21,v22\n"
"v_max3_f32 v118,v118,v23,v16\n"
"v_max3_f32 v118,v118,v17,v18\n"
"v_max_f32 v118,v118,v19\n"
"v_cmp_lt_f32 vcc,s93,v118\n"
"s_nop 0\n"
"s_cbranch_vccnz .Lfk_cs_z1\n"
".Lfk_cr_z1:\n"
"v_mov_b32 v113,v112\n"
"s_waitcnt vmcnt(21) lgkmcnt(0)\n"
"s_barrier\n"
"v_add_f32 v56,0x80000000,v126\n"
"v_min_f32 v56,v56,v108\n"
"v_add_f32 v57,0xf149f2ca,v126\n"
"v_min_f32 v57,v57,v109\n"
"v_add_f32 v58,0xf1c9f2ca,v126\n"
"v_min_f32 v58,v58,v110\n"
"v_add_f32 v59,0xf2177617,v126\n"
"v_min_f32 v59,v59,v111\n"
"v_add_f32 v60,0xf349f2ca,v126\n"
"v_min_f32 v60,v60,v108\n"
"v_add_f32 v61,0xf35691f6,v126\n"
"v_min_f32 v61,v61,v109\n"
"v_add_f32 v62,0xf3633123,v126\n"
"v_min_f32 v62,v62,v110\n"
"v_add_f32 v63,0xf36fd050,v126\n"
"v_min_f32 v63,v63,v111\n"
"v_exp_f32 v16,v16\n"
"v_mfma_f32_16x16x32_bf16 v[56:59],a[64:67],a[48:51],v[56:59]\n"
"s_add_u32 m0,s55,12288\n"
"ds_read_b128 a[96:99],v114 offset:8192\n"
"ds_read_b128 a[100:103],v114 offset:9216\n"
"ds_read_b128 a[104:107],v114 offset:10240\n"
"v_exp_f32 v17,v17\n"
"v_mfma_f32_16x16x32_bf16 v[56:59],a[68:71],a[52:55],v[56:59]\n"
"s_add_u32 s54,s54,s22\n"
"ds_read_b128 a[108:111],v114 offset:11264\n"
"ds_read_b128 a[112:115],v114 offset:12288\n"
"ds_read_b128 a[116:119],v114 offset:13312\n"
"v_mfma_f32_16x16x32_bf16 v[56:59],a[72:75],a[56:59],v[56:59]\n"
"ds_read_b128 a[120:123],v114 offset:14336\n"
"ds_read_b128 a[124:127],v114 offset:15360\n"
"buffer_load_dwordx4 v115,s[32:35],s54 offen sc1 lds\n"
"v_cvt_pk_bf16_f32 v72,v16,v17\n"
"v_exp_f32 v18,v18\n"
"v_mfma_f32_16x16x32_bf16 v[56:59],a[76:79],a[60:63],v[56:59]\n"
"ds_read_b128 a[32:35],v116 offset:24576\n"
"ds_read_b128 a[36:39],v116 offset:25600\n"
"ds_read_b128 a[40:43],v116 offset:26624\n"
"v_mfma_f32_16x16x32_bf16 v[60:63],a[92:95],a[60:63],v[60:63]\n"
"buffer_store_dwordx4 a[180:183],v112,s[24:27],s3 offen offset:1024 sc1\n"
"ds_read_b128 a[44:47],v116 offset:27648\n"
"v_mfma_f32_16x16x32_bf16 v[60:63],a[88:91],a[56:59],v[60:63]\n"
"s_add_u32 m0,s67,32768\n"
"v_exp_f32 v19,v19\n"
"v_mfma_f32_16x16x32_bf16 v[60:63],a[84:87],a[52:55],v[60:63]\n"
"buffer_load_dwordx4 v112,s[24:27],s64 offen offset:0 sc1 lds\n"
"v_cvt_pk_bf16_f32 v73,v18,v19\n"
"v_exp_f32 v20,v20\n"
"v_mfma_f32_16x16x32_bf16 v[60:63],a[80:83],a[48:51],v[60:63]\n"
"ds_read_b128 a[64:67],v113 offset:0\n"
"ds_read_b128 a[68:71],v113 offset:1024\n"
"ds_read_b128 a[72:75],v113 offset:2048\n"
"s_waitcnt lgkmcnt(7)\n"
"v_mfma_f32_16x16x32_bf16 v[160:163],a[96:99],v[84:87],v[160:163]\n"
"ds_read_b128 a[76:79],v113 offset:3072\n"
"ds_read_b128 a[80:83],v113 offset:4096\n"
"ds_read_b128 a[84:87],v113 offset:5120\n"
"ds_read_b128 a[88:91],v113 offset:6144\n"
"ds_read_b128 a[92:95],v113 offset:7168\n"
"v_exp_f32 v21,v21\n"
"v_mfma_f32_16x16x32_bf16 a[132:135],a[144:147],v[84:87],a[132:135]\n"
"buffer_load_dwordx4 v112,s[24:27],s64 offen offset:1024 sc1 lds\n"
"v_max3_f32 v118,v56,v57,v58\n"
"v_mfma_f32_16x16x32_bf16 v[164:167],a[100:103],v[84:87],v[164:167]\n"
"v_exp_f32 v22,v22\n"
"v_cvt_pk_bf16_f32 v74,v20,v21\n"
"ds_read_b128 a[48:51],v116 offset:57344\n"
"ds_read_b128 a[52:55],v116 offset:58368\n"
"v_mfma_f32_16x16x32_bf16 v[168:171],a[104:107],v[84:87],v[168:171]\n"
"v_max3_f32 v118,v118,v59,v60\n"
"v_max3_f32 v118,v118,v61,v62\n"
"v_max_f32 v118,v118,v63\n"
"v_exp_f32 v23,v23\n"
"v_mfma_f32_16x16x32_bf16 v[172:175],a[108:111],v[84:87],v[172:175]\n"
"ds_read_b128 a[56:59],v116 offset:59392\n"
"buffer_load_dwordx4 v112,s[24:27],s64 offen offset:2048 sc1 lds\n"
"ds_read_b128 a[60:63],v116 offset:60416\n"
"v_mfma_f32_16x16x32_bf16 v[176:179],a[112:115],v[84:87],v[176:179]\n"
"v_cvt_pk_bf16_f32 v75,v22,v23\n"
"v_accvgpr_read_b32 v122,a128\n"
"v_rcp_f32 v122,v122\n"
"v_cmp_lt_f32 vcc,s93,v118\n"
"v_pk_mul_f32 v[128:129],v[128:129],v[122:123] op_sel_hi:[1,0]\n"
"v_mfma_f32_16x16x32_bf16 v[180:183],a[116:119],v[84:87],v[180:183]\n"
"buffer_store_dwordx4 a[184:187],v112,s[24:27],s3 offen offset:2048 sc1\n"
"v_exp_f32 v24,v24\n"
"v_pk_mul_f32 v[130:131],v[130:131],v[122:123] op_sel_hi:[1,0]\n"
"v_pk_mul_f32 v[132:133],v[132:133],v[122:123] op_sel_hi:[1,0]\n"
"v_pk_mul_f32 v[134:135],v[134:135],v[122:123] op_sel_hi:[1,0]\n"
"v_mfma_f32_16x16x32_bf16 v[184:187],a[120:123],v[84:87],v[184:187]\n"
"buffer_load_dwordx4 v112,s[24:27],s64 offen offset:3072 sc1 lds\n"
"v_cvt_pk_bf16_f32 v128,v128,v129\n"
"v_cvt_pk_bf16_f32 v129,v130,v131\n"
"v_cvt_pk_bf16_f32 v130,v132,v133\n"
"v_cvt_pk_bf16_f32 v131,v134,v135\n"
"v_mfma_f32_16x16x32_bf16 v[188:191],a[124:127],v[84:87],v[188:191]\n"
"buffer_store_dwordx4 v[128:131],v112,s[24:27],s70 offen offset:0 sc1\n"
"v_pk_mul_f32 v[136:137],v[136:137],v[122:123] op_sel_hi:[1,0]\n"
"v_mfma_f32_16x16x32_bf16 v[192:195],a[96:99],v[88:91],v[192:195]\n"
"s_add_u32 m0,s67,36864\n"
"v_exp_f32 v25,v25\n"
"v_exp_f32 v27,v27\n"
"v_cvt_pk_bf16_f32 v76,v24,v25\n"
"v_cvt_pk_bf16_f32 v136,v136,v137\n"
"v_pk_mul_f32 v[138:139],v[138:139],v[122:123] op_sel_hi:[1,0]\n"
"v_pk_mul_f32 v[140:141],v[140:141],v[122:123] op_sel_hi:[1,0]\n"
"v_pk_mul_f32 v[142:143],v[142:143],v[122:123] op_sel_hi:[1,0]\n"
"v_mfma_f32_16x16x32_bf16 a[136:139],a[144:147],v[88:91],a[136:139]\n"
"buffer_load_dwordx4 v117,s[24:27],s64 offen offset:0 sc1 lds\n"
"v_cvt_pk_bf16_f32 v137,v138,v139\n"
"v_mfma_f32_16x16x32_bf16 v[196:199],a[100:103],v[88:91],v[196:199]\n"
"v_cvt_pk_bf16_f32 v138,v140,v141\n"
"v_cvt_pk_bf16_f32 v139,v142,v143\n"
"v_exp_f32 v26,v26\n"
"v_mfma_f32_16x16x32_bf16 v[200:203],a[104:107],v[88:91],v[200:203]\n"
"buffer_store_dwordx4 v[136:139],v112,s[24:27],s70 offen offset:1024 sc1\n"
"v_cvt_pk_bf16_f32 v77,v26,v27\n"
"v_pk_mul_f32 v[144:145],v[144:145],v[122:123] op_sel_hi:[1,0]\n"
"v_pk_mul_f32 v[146:147],v[146:147],v[122:123] op_sel_hi:[1,0]\n"
"v_pk_mul_f32 v[148:149],v[148:149],v[122:123] op_sel_hi:[1,0]\n"
"v_pk_mul_f32 v[150:151],v[150:151],v[122:123] op_sel_hi:[1,0]\n"
"v_exp_f32 v28,v28\n"
"v_mfma_f32_16x16x32_bf16 v[204:207],a[108:111],v[88:91],v[204:207]\n"
"buffer_load_dwordx4 v117,s[24:27],s64 offen offset:1024 sc1 lds\n"
"buffer_store_dwordx4 a[188:191],v112,s[24:27],s3 offen offset:3072 sc1\n"
"v_mfma_f32_16x16x32_bf16 v[208:211],a[112:115],v[88:91],v[208:211]\n"
"s_or_b32 s3,s3,0x3c000\n"
"v_cvt_pk_bf16_f32 v144,v144,v145\n"
"v_cvt_pk_bf16_f32 v145,v146,v147\n"
"v_cvt_pk_bf16_f32 v146,v148,v149\n"
"v_cvt_pk_bf16_f32 v147,v150,v151\n"
"v_mfma_f32_16x16x32_bf16 v[212:215],a[116:119],v[88:91],v[212:215]\n"
"buffer_store_dwordx4 v[144:147],v112,s[24:27],s70 offen offset:2048 sc1\n"
"v_pk_mul_f32 v[152:153],v[152:153],v[122:123] op_sel_hi:[1,0]\n"
"v_pk_mul_f32 v[154:155],v[154:155],v[122:123] op_sel_hi:[1,0]\n"
"v_mfma_f32_16x16x32_bf16 v[216:219],a[120:123],v[88:91],v[216:219]\n"
"buffer_load_dwordx4 v117,s[24:27],s64 offen offset:2048 sc1 lds\n"
"v_exp_f32 v29,v29\n"
"v_exp_f32 v31,v31\n"
"v_cvt_pk_bf16_f32 v78,v28,v29\n"
"v_pk_mul_f32 v[156:157],v[156:157],v[122:123] op_sel_hi:[1,0]\n"
"v_pk_mul_f32 v[158:159],v[158:159],v[122:123] op_sel_hi:[1,0]\n"
"v_mfma_f32_16x16x32_bf16 v[220:223],a[124:127],v[88:91],v[220:223]\n"
"v_cvt_pk_bf16_f32 v152,v152,v153\n"
"v_cvt_pk_bf16_f32 v153,v154,v155\n"
"v_cvt_pk_bf16_f32 v154,v156,v157\n"
"v_cvt_pk_bf16_f32 v155,v158,v159\n"
"v_mfma_f32_16x16x32_bf16 v[224:227],a[96:99],v[92:95],v[224:227]\n"
"buffer_store_dwordx4 v[152:155],v112,s[24:27],s70 offen offset:3072 sc1\n"
"v_accvgpr_read_b32 v122,a132\n"
"v_mfma_f32_16x16x32_bf16 a[140:143],a[144:147],v[92:95],a[140:143]\n"
"s_add_u32 s70,s70,262144\n"
"buffer_load_dwordx4 v117,s[24:27],s64 offen offset:3072 sc1 lds\n"
"v_mfma_f32_16x16x32_bf16 v[228:231],a[100:103],v[92:95],v[228:231]\n"
"s_add_u32 s64,s64,16384\n"
"v_exp_f32 v30,v30\n"
"v_rcp_f32 v122,v122\n"
"v_cvt_pk_bf16_f32 v79,v30,v31\n"
"v_pk_mul_f32 v[160:161],v[160:161],v[122:123] op_sel_hi:[1,0]\n"
"v_pk_mul_f32 v[162:163],v[162:163],v[122:123] op_sel_hi:[1,0]\n"
"v_pk_mul_f32 v[164:165],v[164:165],v[122:123] op_sel_hi:[1,0]\n"
"v_pk_mul_f32 v[166:167],v[166:167],v[122:123] op_sel_hi:[1,0]\n"
"v_mfma_f32_16x16x32_bf16 v[232:235],a[104:107],v[92:95],v[232:235]\n"
"buffer_store_dwordx4 a[236:239],v112,s[24:27],s56 offen offset:3072 sc1\n"
"v_cvt_pk_bf16_f32 v160,v160,v161\n"
"v_cvt_pk_bf16_f32 v161,v162,v163\n"
"v_cvt_pk_bf16_f32 v162,v164,v165\n"
"v_cvt_pk_bf16_f32 v163,v166,v167\n"
"v_mfma_f32_16x16x32_bf16 v[236:239],a[108:111],v[92:95],v[236:239]\n"
"buffer_store_dwordx4 v[160:163],v112,s[24:27],s70 offen offset:0 sc1\n"
"v_pk_mul_f32 v[168:169],v[168:169],v[122:123] op_sel_hi:[1,0]\n"
"v_pk_mul_f32 v[170:171],v[170:171],v[122:123] op_sel_hi:[1,0]\n"
"v_pk_mul_f32 v[172:173],v[172:173],v[122:123] op_sel_hi:[1,0]\n"
"v_pk_mul_f32 v[174:175],v[174:175],v[122:123] op_sel_hi:[1,0]\n"
"v_mfma_f32_16x16x32_bf16 v[240:243],a[112:115],v[92:95],v[240:243]\n"
"v_cvt_pk_bf16_f32 v168,v168,v169\n"
"v_cvt_pk_bf16_f32 v169,v170,v171\n"
"v_cvt_pk_bf16_f32 v170,v172,v173\n"
"v_cvt_pk_bf16_f32 v171,v174,v175\n"
"v_mfma_f32_16x16x32_bf16 v[244:247],a[116:119],v[92:95],v[244:247]\n"
"buffer_store_dwordx4 v[168:171],v112,s[24:27],s70 offen offset:1024 sc1\n"
"v_pk_mul_f32 v[176:177],v[176:177],v[122:123] op_sel_hi:[1,0]\n"
"v_pk_mul_f32 v[178:179],v[178:179],v[122:123] op_sel_hi:[1,0]\n"
"v_pk_mul_f32 v[180:181],v[180:181],v[122:123] op_sel_hi:[1,0]\n"
"v_pk_mul_f32 v[182:183],v[182:183],v[122:123] op_sel_hi:[1,0]\n"
"v_mfma_f32_16x16x32_bf16 v[248:251],a[120:123],v[92:95],v[248:251]\n"
"v_cvt_pk_bf16_f32 v176,v176,v177\n"
"v_cvt_pk_bf16_f32 v177,v178,v179\n"
"v_cvt_pk_bf16_f32 v178,v180,v181\n"
"v_cvt_pk_bf16_f32 v179,v182,v183\n"
"v_mfma_f32_16x16x32_bf16 v[252:255],a[124:127],v[92:95],v[252:255]\n"
"buffer_store_dwordx4 v[176:179],v112,s[24:27],s70 offen offset:2048 sc1\n"
"v_pk_mul_f32 v[184:185],v[184:185],v[122:123] op_sel_hi:[1,0]\n"
"v_pk_mul_f32 v[186:187],v[186:187],v[122:123] op_sel_hi:[1,0]\n"
"v_pk_mul_f32 v[188:189],v[188:189],v[122:123] op_sel_hi:[1,0]\n"
"v_pk_mul_f32 v[190:191],v[190:191],v[122:123] op_sel_hi:[1,0]\n"
"v_cvt_pk_bf16_f32 v184,v184,v185\n"
"v_cvt_pk_bf16_f32 v185,v186,v187\n"
"v_cvt_pk_bf16_f32 v186,v188,v189\n"
"v_cvt_pk_bf16_f32 v187,v190,v191\n"
"buffer_store_dwordx4 v[184:187],v112,s[24:27],s70 offen offset:3072 sc1\n"
"s_add_u32 s70,s70,262144\n"
"s_cbranch_vccnz .Lfk_cs_z2\n"
".Lfk_cr_z2:\n"
"s_waitcnt vmcnt(35) lgkmcnt(0)\n"
"s_barrier\n"
"s_add_u32 s89,s89,1\n"
"s_branch .Lfk_winB\n"
".Lfk_ev:\n"
"s_xor_b32 s55,s55,16384\n"
"s_andn2_b32 s91,s55,16384\n"
"v_lshrrev_b32 v121,1,v112\n"
"v_add_u32 v121,s91,v121\n"
"ds_read_b64_tr_b16 a[160:161],v121 offset:0\n"
"ds_read_b64_tr_b16 a[162:163],v121 offset:512\n"
"s_add_u32 s56,s56,4096\n"
"s_swappc_b64 s[20:21],s[94:95]\n"
"s_xor_b32 s55,s55,16384\n"
"s_andn2_b32 s91,s55,16384\n"
"v_lshrrev_b32 v121,1,v112\n"
"v_add_u32 v121,s91,v121\n"
"ds_read_b64_tr_b16 a[164:165],v121 offset:4096\n"
"ds_read_b64_tr_b16 a[166:167],v121 offset:4608\n"
"ds_read_b64_tr_b16 a[168:169],v121 offset:8192\n"
"ds_read_b64_tr_b16 a[170:171],v121 offset:8704\n"
"ds_read_b64_tr_b16 a[172:173],v121 offset:12288\n"
"ds_read_b64_tr_b16 a[174:175],v121 offset:12800\n"
"ds_read_b64_tr_b16 a[176:177],v121 offset:0\n"
"ds_read_b64_tr_b16 a[178:179],v121 offset:512\n"
"buffer_store_dwordx2 a[154:155],off,s[24:27],s2 sc1\n"
"s_and_b32 s2,s2,0xfffffc00\n"
"s_mov_b32 s56,s10\n"
"v_accvgpr_write_b32 a240,0\n"
"v_accvgpr_write_b32 a241,0\n"
"v_accvgpr_write_b32 a242,0\n"
"v_accvgpr_write_b32 a243,0\n"
"v_accvgpr_write_b32 a244,0\n"
"v_accvgpr_write_b32 a245,0\n"
"v_accvgpr_write_b32 a246,0\n"
"v_accvgpr_write_b32 a247,0\n"
"v_accvgpr_write_b32 a248,0\n"
"v_accvgpr_write_b32 a249,0\n"
"v_accvgpr_write_b32 a250,0\n"
"v_accvgpr_write_b32 a251,0\n"
"v_accvgpr_write_b32 a252,0\n"
"v_accvgpr_write_b32 a253,0\n"
"v_accvgpr_write_b32 a254,0\n"
"v_accvgpr_write_b32 a255,0\n"
"s_mov_b64 s[32:33],s[36:37]\n"
"s_mov_b32 s54,-229376\n"
"s_swappc_b64 s[20:21],s[94:95]\n"
"s_xor_b32 s55,s55,16384\n"
"s_add_u32 s56,s56,131072\n"
"s_andn2_b32 s91,s55,16384\n"
"v_lshrrev_b32 v121,1,v112\n"
"v_add_u32 v121,s91,v121\n"
"ds_read_b64_tr_b16 a[180:181],v121 offset:20480\n"
"ds_read_b64_tr_b16 a[182:183],v121 offset:20992\n"
"ds_read_b64_tr_b16 a[184:185],v121 offset:24576\n"
"ds_read_b64_tr_b16 a[186:187],v121 offset:25088\n"
"ds_read_b64_tr_b16 a[188:189],v121 offset:28672\n"
"ds_read_b64_tr_b16 a[190:191],v121 offset:29184\n"
"ds_read_b64_tr_b16 a[192:193],v121 offset:0\n"
"ds_read_b64_tr_b16 a[194:195],v121 offset:512\n"
"buffer_load_dwordx2 a[150:151],v112,s[24:27],s2 offen sc1\n"
"s_add_u32 s82,s59,8192\n"
"buffer_store_dwordx2 a[154:155],off,s[24:27],s82 sc1\n"
"s_swappc_b64 s[20:21],s[94:95]\n"
"s_xor_b32 s55,s55,16384\n"
"s_add_u32 s56,s56,131072\n"
"s_andn2_b32 s91,s55,16384\n"
"v_lshrrev_b32 v121,1,v112\n"
"v_add_u32 v121,s91,v121\n"
"ds_read_b64_tr_b16 a[196:197],v121 offset:4096\n"
"ds_read_b64_tr_b16 a[198:199],v121 offset:4608\n"
"ds_read_b64_tr_b16 a[200:201],v121 offset:8192\n"
"ds_read_b64_tr_b16 a[202:203],v121 offset:8704\n"
"ds_read_b64_tr_b16 a[204:205],v121 offset:12288\n"
"ds_read_b64_tr_b16 a[206:207],v121 offset:12800\n"
"ds_read_b64_tr_b16 a[208:209],v121 offset:0\n"
"ds_read_b64_tr_b16 a[210:211],v121 offset:512\n"
"s_mov_b32 s84,0x40000\n"
".Lfk_w_chk:\n"
"v_accvgpr_read_b32 v119,a150\n"
"v_accvgpr_read_b32 v120,a151\n"
"v_xor_b32 v119,s80,v119\n"
"v_xor_b32 v120,s81,v120\n"
"v_or_b32 v119,v119,v120\n"
"v_cmp_ne_u32 vcc,0,v119\n"
"s_nop 0\n"
"s_cbranch_vccz .Lfk_w_ok\n"
"s_sub_u32 s84,s84,1\n"
"s_cbranch_scc1 .Lfk_w_ok\n"
"s_sleep 4\n"
"buffer_load_dwordx2 a[150:151],v112,s[24:27],s2 offen sc1\n"
"s_waitcnt vmcnt(0)\n"
"s_branch .Lfk_w_chk\n"
".Lfk_w_ok:\n"
"v_lshrrev_b32 v121,1,v112\n"
"v_and_b32 v121,0xf8,v121\n"
"s_add_u32 s82,s63,8192\n"
"buffer_load_dwordx2 a[150:151],v121,s[24:27],s82 offen sc1\n"
"s_mov_b64 s[32:33],s[24:25]\n"
"s_mov_b32 s34,s26\n"
"s_mov_b32 s22,1024\n"
"v_mov_b32 v115,v112\n"
"s_sub_u32 s54,s3,1024\n"
"s_swappc_b64 s[20:21],s[94:95]\n"
"s_xor_b32 s55,s55,16384\n"
"s_add_u32 s56,s56,131072\n"
"s_andn2_b32 s91,s55,16384\n"
"v_lshrrev_b32 v121,1,v112\n"
"v_add_u32 v121,s91,v121\n"
"ds_read_b64_tr_b16 a[212:213],v121 offset:20480\n"
"ds_read_b64_tr_b16 a[214:215],v121 offset:20992\n"
"ds_read_b64_tr_b16 a[216:217],v121 offset:24576\n"
"ds_read_b64_tr_b16 a[218:219],v121 offset:25088\n"
"ds_read_b64_tr_b16 a[220:221],v121 offset:28672\n"
"ds_read_b64_tr_b16 a[222:223],v121 offset:29184\n"
"v_add_u32 v121,s91,v112\n"
"ds_read_b128 a[224:227],v121 offset:0\n"
"s_sub_u32 s54,s3,17408\n"
"s_mov_b32 s84,0x40000\n"
".Lfk_k_chk:\n"
"v_accvgpr_read_b32 v119,a150\n"
"v_accvgpr_read_b32 v120,a151\n"
"v_xor_b32 v119,s80,v119\n"
"v_xor_b32 v120,s81,v120\n"
"v_or_b32 v119,v119,v120\n"
"v_cmp_ne_u32 vcc,0,v119\n"
"s_nop 0\n"
"s_cbranch_vccz .Lfk_k_ok\n"
"s_sub_u32 s84,s84,1\n"
"s_cbranch_scc1 .Lfk_k_ok\n"
"s_sleep 4\n"
"v_lshrrev_b32 v121,1,v112\n"
"v_and_b32 v121,0xf8,v121\n"
"s_add_u32 s82,s63,8192\n"
"buffer_load_dwordx2 a[150:151],v121,s[24:27],s82 offen sc1\n"
"s_waitcnt vmcnt(0)\n"
"s_branch .Lfk_k_chk\n"
".Lfk_k_ok:\n"
"s_swappc_b64 s[20:21],s[94:95]\n"
"s_xor_b32 s55,s55,16384\n"
"s_andn2_b32 s91,s55,16384\n"
"v_add_u32 v121,s91,v112\n"
"ds_read_b128 a[228:231],v121 offset:4096\n"
"ds_read_b128 a[232:235],v121 offset:8192\n"
"ds_read_b128 a[236:239],v121 offset:12288\n"
"ds_read_b128 a[240:243],v121 offset:0\n"
"s_sub_u32 s54,s3,99328\n"
"s_mov_b32 s56,2146435072\n"
"s_getpc_b64 s[96:97]\n"
".Lfk_rt1:\n"
"s_sub_u32 s96,s96,.Lfk_rt1-.Lfk_tailA\n"
"s_subb_u32 s97,s97,0\n"
"s_mov_b32 s98,.Lfk_cold-.Lfk_tailA\n"
"s_mov_b32 s99,0x20000\n"
"s_lshr_b32 s82,s100,3\n"
"s_lshl2_add_u32 s82,s82,s52\n"
"s_lshl_b32 s82,s82,9\n"
"v_lshrrev_b32 v121,1,v112\n"
"buffer_load_dword a148,v121,s[96:99],s82 offen\n"
"s_swappc_b64 s[20:21],s[94:95]\n"
"s_xor_b32 s55,s55,16384\n"
"s_andn2_b32 s91,s55,16384\n"
"v_add_u32 v121,s91,v112\n"
"ds_read_b128 a[244:247],v121 offset:20480\n"
"ds_read_b128 a[248:251],v121 offset:24576\n"
"ds_read_b128 a[252:255],v121 offset:28672\n"
"ds_read_b128 a[156:159],v121 offset:0\n"
"s_sub_u32 s54,s3,115712\n"
"s_lshl_b32 s82,s100,3\n"
"s_add_u32 s82,s82,148946944\n"
"s_cmp_eq_u32 s52,0\n"
"s_cselect_b32 s82,s82,2146435072\n"
"buffer_store_dwordx2 a[154:155],off,s[24:27],s82 sc1\n"
"s_lshr_b32 s82,s3,9\n"
"s_and_b32 s82,s82,0x3000\n"
"s_add_u32 s11,s11,s82\n"
"s_add_u32 s9,s9,s82\n"
"s_lshl3_add_u32 s8,s82,s8\n"
"s_lshr_b32 s82,s3,18\n"
"s_sub_u32 s82,s82,s60\n"
"s_and_b32 s82,s82,7\n"
"s_lshl_b32 s82,s82,17\n"
"s_add_u32 s8,s8,s82\n"
"v_lshrrev_b32 v120,1,v112\n"
"buffer_load_dwordx2 a[150:151],v120,s[24:27],s11 offen sc1\n"
"s_lshl_b32 s30,s78,1\n"
"s_swappc_b64 s[20:21],s[94:95]\n"
"s_nop 1\n"
".Lfk_xn0_s:\n"
"s_mov_b32 s84,0x40000\n"
"buffer_load_dwordx4 v[74:77],v112,s[24:27],s58 offen sc1\n"
"s_waitcnt vmcnt(0)\n"
".Lfk_xn0:\n"
"v_xor_b32 v116,s80,v74\n"
"v_xor_b32 v117,s81,v75\n"
"v_or_b32 v116,v116,v117\n"
"v_xor_b32 v117,s80,v76\n"
"v_or_b32 v116,v116,v117\n"
"v_xor_b32 v117,s81,v77\n"
"v_or_b32 v116,v116,v117\n"
"v_cmp_ne_u32 vcc,0,v116\n"
"s_nop 0\n"
"s_cbranch_vccz .Lfk_xn0_x\n"
"s_sleep 4\n"
"s_mov_b64 exec,vcc\n"
"buffer_load_dwordx4 v[74:77],v112,s[24:27],s58 offen sc1\n"
"s_mov_b64 exec,-1\n"
"s_sub_u32 s84,s84,1\n"
"s_waitcnt vmcnt(0)\n"
"s_cbranch_scc0 .Lfk_xn0\n"
".Lfk_xn0_x:\n"
"s_nop 1\n"
"s_branch .Lfk_xn0_ok\n"
".Lfk_xn1_s:\n"
"s_mov_b32 s84,0x40000\n"
"buffer_load_dwordx4 v[74:77],v112,s[24:27],s58 offen sc1\n"
"s_waitcnt vmcnt(0)\n"
".Lfk_xn1:\n"
"v_xor_b32 v116,s80,v74\n"
"v_xor_b32 v117,s81,v75\n"
"v_or_b32 v116,v116,v117\n"
"v_xor_b32 v117,s80,v76\n"
"v_or_b32 v116,v116,v117\n"
"v_xor_b32 v117,s81,v77\n"
"v_or_b32 v116,v116,v117\n"
"v_cmp_ne_u32 vcc,0,v116\n"
"s_nop 0\n"
"s_cbranch_vccz .Lfk_xn1_x\n"
"s_sleep 4\n"
"s_mov_b64 exec,vcc\n"
"buffer_load_dwordx4 v[74:77],v112,s[24:27],s58 offen sc1\n"
"s_mov_b64 exec,-1\n"
"s_sub_u32 s84,s84,1\n"
"s_waitcnt vmcnt(0)\n"
"s_cbranch_scc0 .Lfk_xn1\n"
".Lfk_xn1_x:\n"
"s_nop 1\n"
"s_branch .Lfk_xn1_ok\n"
".Lfk_kv_s:\n"
"s_mov_b32 s84,0x40000\n"
"buffer_load_dwordx2 v[118:119],v120,s[24:27],s63 offen sc1\n"
"s_waitcnt vmcnt(0)\n"
".Lfk_kv:\n"
"v_xor_b32 v116,s80,v118\n"
"v_xor_b32 v117,s81,v119\n"
"v_or_b32 v116,v116,v117\n"
"v_cmp_ne_u32 vcc,0,v116\n"
"s_nop 0\n"
"s_cbranch_vccz .Lfk_kv_x\n"
"s_sleep 4\n"
"s_mov_b64 exec,vcc\n"
"buffer_load_dwordx2 v[118:119],v120,s[24:27],s63 offen sc1\n"
"s_mov_b64 exec,-1\n"
"s_sub_u32 s84,s84,1\n"
"s_waitcnt vmcnt(0)\n"
"s_cbranch_scc0 .Lfk_kv\n"
".Lfk_kv_x:\n"
"s_nop 1\n"
"s_branch .Lfk_kv_ok\n"
".Lfk_cs_h3:\n"
"s_mov_b32 s91,15\n"
"s_mov_b32 s90,15\n"
"s_call_b64 s[94:95],.Lfk_rare0\n"
"s_branch .Lfk_cr_h3\n"
".Lfk_cs_h1:\n"
"s_mov_b32 s91,15\n"
"s_call_b64 s[94:95],.Lfk_rare3\n"
"s_branch .Lfk_cr_h1\n"
".Lfk_cs_h2:\n"
"s_mov_b32 s91,15\n"
"s_call_b64 s[94:95],.Lfk_rare2\n"
"s_branch .Lfk_cr_h2\n"
".Lfk_cs_x1:\n"
"s_mov_b32 s91,15\n"
"s_call_b64 s[94:95],.Lfk_rare1\n"
"s_branch .Lfk_cr_x1\n"
".Lfk_cs_a3:\n"
"s_mov_b32 s91,14\n"
"s_call_b64 s[94:95],.Lfk_rare0\n"
"s_branch .Lfk_cr_a3\n"
".Lfk_cs_a0:\n"
"s_mov_b32 s91,12\n"
"s_call_b64 s[94:95],.Lfk_rare1\n"
"s_branch .Lfk_cr_a0\n"
".Lfk_cs_a1:\n"
"s_mov_b32 s91,8\n"
"s_call_b64 s[94:95],.Lfk_rare0\n"
"s_branch .Lfk_cr_a1\n"
".Lfk_ms_a0:\n"
"s_call_b64 s[94:95],.Lfk_mslow0\n"
"s_branch .Lfk_mr_a0\n"
".Lfk_ms_a1:\n"
"s_call_b64 s[94:95],.Lfk_mslow1\n"
"s_branch .Lfk_mr_a1\n"
".Lfk_p2:\n"
"s_nop 7\n"
"s_mov_b32 s84,0x40000\n"
"buffer_load_dwordx2 v[122:123],v115,s[24:27],s11 offen sc1\n"
"s_waitcnt vmcnt(0)\n"
".Lfk_pj:\n"
"v_xor_b32 v119,s80,v122\n"
"v_xor_b32 v120,s81,v123\n"
"v_or_b32 v119,v119,v120\n"
"v_cmp_ne_u32 s[14:15],0,v119\n"
"s_nop 0\n"
"s_mov_b32 vcc_lo,s14\n"
"s_mov_b32 vcc_hi,0\n"
"s_cbranch_vccz .Lfk_pj_x\n"
"s_sleep 4\n"
"s_mov_b64 exec,vcc\n"
"buffer_load_dwordx2 v[122:123],v115,s[24:27],s11 offen sc1\n"
"s_mov_b64 exec,-1\n"
"s_sub_u32 s84,s84,1\n"
"s_waitcnt vmcnt(0)\n"
"s_cbranch_scc0 .Lfk_pj\n"
".Lfk_pj_x:\n"
"s_cmp_ge_u32 s52,2\n"
"s_cselect_b32 s18,1032192,0\n"
"s_add_u32 s18,s18,s8\n"
"s_add_u32 m0,s30,0\n"
"s_nop 0\n"
"buffer_load_dwordx4 v112,s[24:27],s18 offen offset:0 sc1 lds\n"
"buffer_load_dwordx4 v112,s[24:27],s18 offen offset:1024 sc1 lds\n"
"buffer_load_dwordx4 v112,s[24:27],s18 offen offset:2048 sc1 lds\n"
"buffer_load_dwordx4 v112,s[24:27],s18 offen offset:3072 sc1 lds\n"
"s_add_u32 m0,m0,4096\n"
"s_nop 0\n"
"buffer_load_dwordx4 v117,s[24:27],s18 offen offset:0 sc1 lds\n"
"buffer_load_dwordx4 v117,s[24:27],s18 offen offset:1024 sc1 lds\n"
"buffer_load_dwordx4 v117,s[24:27],s18 offen offset:2048 sc1 lds\n"
"buffer_load_dwordx4 v117,s[24:27],s18 offen offset:3072 sc1 lds\n"
"s_add_u32 s83,s18,2097152\n"
"s_add_u32 m0,s30,32768\n"
"s_nop 0\n"
"buffer_load_dwordx4 v112,s[24:27],s83 offen offset:0 sc1 lds\n"
"buffer_load_dwordx4 v112,s[24:27],s83 offen offset:1024 sc1 lds\n"
"buffer_load_dwordx4 v112,s[24:27],s83 offen offset:2048 sc1 lds\n"
"buffer_load_dwordx4 v112,s[24:27],s83 offen offset:3072 sc1 lds\n"
"s_add_u32 m0,m0,4096\n"
"s_nop 0\n"
"buffer_load_dwordx4 v117,s[24:27],s83 offen offset:0 sc1 lds\n"
"buffer_load_dwordx4 v117,s[24:27],s83 offen offset:1024 sc1 lds\n"
"buffer_load_dwordx4 v117,s[24:27],s83 offen offset:2048 sc1 lds\n"
"buffer_load_dwordx4 v117,s[24:27],s83 offen offset:3072 sc1 lds\n"
"s_waitcnt vmcnt(0)\n"
"s_nop 1\n"
"s_branch .Lfk_p3\n"
".Lfk_cs_k1:\n"
"s_mov_b32 s91,15\n"
"s_call_b64 s[94:95],.Lfk_rare3\n"
"s_branch .Lfk_cr_k1\n"
".Lfk_cs_k2:\n"
"s_mov_b32 s91,15\n"
"s_call_b64 s[94:95],.Lfk_rare2\n"
"s_branch .Lfk_cr_k2\n"
".Lfk_cs_x2:\n"
"s_mov_b32 s91,15\n"
"s_call_b64 s[94:95],.Lfk_rare1\n"
"s_branch .Lfk_cr_x2\n"
".Lfk_cs_u3:\n"
"s_mov_b32 s91,15\n"
"s_call_b64 s[94:95],.Lfk_rare0\n"
"s_branch .Lfk_cr_u3\n"
".Lfk_cs_mu3:\n"
"s_call_b64 s[94:95],.Lfk_meet3\n"
"s_branch .Lfk_cr_mu3\n"
".Lfk_cs_u0:\n"
"s_mov_b32 s91,14\n"
"s_call_b64 s[94:95],.Lfk_rare1\n"
"s_branch .Lfk_cr_u0\n"
".Lfk_cs_mu0:\n"
"s_call_b64 s[94:95],.Lfk_meet3\n"
"s_branch .Lfk_cr_mu0\n"
".Lfk_cs_u1:\n"
"s_mov_b32 s91,12\n"
"s_call_b64 s[94:95],.Lfk_rare0\n"
"s_branch .Lfk_cr_u1\n"
".Lfk_cs_mu1:\n"
"s_call_b64 s[94:95],.Lfk_meet3\n"
"s_branch .Lfk_cr_mu1\n"
".Lfk_cs_u2:\n"
"s_mov_b32 s91,8\n"
"s_call_b64 s[94:95],.Lfk_rare1\n"
"s_branch .Lfk_cr_u2\n"
".Lfk_cs_mu2:\n"
"s_call_b64 s[94:95],.Lfk_meet3\n"
"s_branch .Lfk_cr_mu2\n"
".Lfk_cs_v3:\n"
"s_mov_b32 s91,15\n"
"s_mov_b32 s90,15\n"
"s_call_b64 s[94:95],.Lfk_rare0\n"
"s_branch .Lfk_cr_v3\n"
".Lfk_cs_mv3:\n"
"s_call_b64 s[94:95],.Lfk_meet3\n"
"s_branch .Lfk_cr_mv3\n"
".Lfk_cs_mv0:\n"
"s_call_b64 s[94:95],.Lfk_meet3\n"
"s_branch .Lfk_cr_mv0\n"
".Lfk_cs_v1:\n"
"s_call_b64 s[94:95],.Lfk_meet3\n"
"v_or_b32 v118,v118,v95\n"
"v_and_b32 v118,0x40004000,v118\n"
"v_cmp_ne_u32 vcc,0,v118\n"
"s_nop 0\n"
"s_cbranch_vccz .Lfk_cr_v1\n"
"s_mov_b32 s91,15\n"
"s_call_b64 s[94:95],.Lfk_rare3\n"
"s_branch .Lfk_cr_v1\n"
".Lfk_cs_v2:\n"
"s_call_b64 s[94:95],.Lfk_meet3\n"
"v_or_b32 v118,v118,v79\n"
"v_and_b32 v118,0x40004000,v118\n"
"v_cmp_ne_u32 vcc,0,v118\n"
"s_nop 0\n"
"s_cbranch_vccz .Lfk_cr_v2\n"
"s_mov_b32 s91,15\n"
"s_call_b64 s[94:95],.Lfk_rare2\n"
"s_branch .Lfk_cr_v2\n"
".Lfk_cs_b3:\n"
"s_mov_b32 s91,15\n"
"s_call_b64 s[94:95],.Lfk_rare0\n"
"s_branch .Lfk_cr_b3\n"
".Lfk_ms_b0:\n"
"s_call_b64 s[94:95],.Lfk_mslow0\n"
"s_branch .Lfk_mr_b0\n"
".Lfk_ms_b1:\n"
"s_call_b64 s[94:95],.Lfk_mslow1\n"
"s_branch .Lfk_mr_b1\n"
".Lfk_cs_c3:\n"
"s_mov_b32 s91,15\n"
"s_mov_b32 s90,15\n"
"s_call_b64 s[94:95],.Lfk_rare0\n"
"s_branch .Lfk_cr_c3\n"
".Lfk_cs_z3:\n"
"s_mov_b32 s91,15\n"
"s_mov_b32 s90,15\n"
"s_call_b64 s[94:95],.Lfk_rare0\n"
"s_branch .Lfk_cr_z3\n"
".Lfk_cs_z0:\n"
"s_mov_b32 s91,14\n"
"s_call_b64 s[94:95],.Lfk_rare1\n"
"s_branch .Lfk_cr_z0\n"
".Lfk_cs_z1:\n"
"s_mov_b32 s91,12\n"
"s_call_b64 s[94:95],.Lfk_rare0\n"
"s_branch .Lfk_cr_z1\n"
".Lfk_cs_z2:\n"
"s_mov_b32 s91,8\n"
"s_call_b64 s[94:95],.Lfk_rare1\n"
"s_branch .Lfk_cr_z2\n"
".Lfk_cold:\n"
".Lfk_rare0:\n"
"s_nop 7\n"
"s_bitcmp1_b32 s91,0\n"
"s_cbranch_scc0 .Lfk_rare0_0\n"
"v_max3_f32 v119,v0,v1,v2\n"
"v_max3_f32 v120,v3,v4,v5\n"
"v_max3_f32 v119,v119,v6,v7\n"
"v_max_f32 v119,v119,v120\n"
"v_mov_b32 v120,v119\n"
"s_nop 1\n"
"v_permlane16_swap_b32 v120,v119\n"
"v_max_f32 v119,v119,v120\n"
"v_mov_b32 v120,v119\n"
"s_nop 1\n"
"v_permlane32_swap_b32 v120,v119\n"
"v_max_f32 v119,v119,v120\n"
"s_bitcmp1_b32 s90,0\n"
"s_cselect_b32 s82,0xff800000,0\n"
"v_add_f32 v119,0x41980000,v119\n"
"v_max_f32 v119,s82,v119\n"
"v_cmp_lt_f32 vcc,s17,|v119|\n"
"s_nop 0\n"
"s_cbranch_vccz .Lfk_rare0_0\n"
"v_ceil_f32 v119,v119\n"
"v_sub_f32 v0,v0,v119\n"
"v_sub_f32 v1,v1,v119\n"
"v_sub_f32 v2,v2,v119\n"
"v_sub_f32 v3,v3,v119\n"
"v_sub_f32 v4,v4,v119\n"
"v_sub_f32 v5,v5,v119\n"
"v_sub_f32 v6,v6,v119\n"
"v_sub_f32 v7,v7,v119\n"
"v_sub_f32 v96,v96,v119\n"
"v_sub_f32 v97,v97,v119\n"
"v_sub_f32 v98,v98,v119\n"
"v_sub_f32 v99,v99,v119\n"
"s_cbranch_scc1 .Lfk_rare0_0\n"
"v_exp_f32 v119,-v119\n"
"s_nop 0\n"
"v_mul_f32 v128,v128,v119\n"
"v_mul_f32 v129,v129,v119\n"
"v_mul_f32 v130,v130,v119\n"
"v_mul_f32 v131,v131,v119\n"
"v_mul_f32 v132,v132,v119\n"
"v_mul_f32 v133,v133,v119\n"
"v_mul_f32 v134,v134,v119\n"
"v_mul_f32 v135,v135,v119\n"
"v_mul_f32 v136,v136,v119\n"
"v_mul_f32 v137,v137,v119\n"
"v_mul_f32 v138,v138,v119\n"
"v_mul_f32 v139,v139,v119\n"
"v_mul_f32 v140,v140,v119\n"
"v_mul_f32 v141,v141,v119\n"
"v_mul_f32 v142,v142,v119\n"
"v_mul_f32 v143,v143,v119\n"
"v_mul_f32 v144,v144,v119\n"
"v_mul_f32 v145,v145,v119\n"
"v_mul_f32 v146,v146,v119\n"
"v_mul_f32 v147,v147,v119\n"
"v_mul_f32 v148,v148,v119\n"
"v_mul_f32 v149,v149,v119\n"
"v_mul_f32 v150,v150,v119\n"
"v_mul_f32 v151,v151,v119\n"
"v_mul_f32 v152,v152,v119\n"
"v_mul_f32 v153,v153,v119\n"
"v_mul_f32 v154,v154,v119\n"
"v_mul_f32 v155,v155,v119\n"
"v_mul_f32 v156,v156,v119\n"
"v_mul_f32 v157,v157,v119\n"
"v_mul_f32 v158,v158,v119\n"
"v_mul_f32 v159,v159,v119\n"
"v_accvgpr_read_b32 v120,a128\n"
"v_mul_f32 v120,v120,v119\n"
"v_accvgpr_write_b32 a128,v120\n"
"v_lshlrev_b32 v120,16,v80\n"
"v_and_b32 v121,0xffff0000,v80\n"
"v_mul_f32 v120,v120,v119\n"
"v_mul_f32 v121,v121,v119\n"
"v_cvt_pk_bf16_f32 v80,v120,v121\n"
"v_accvgpr_read_b32 v120,a129\n"
"v_mul_f32 v120,v120,v119\n"
"v_accvgpr_write_b32 a129,v120\n"
"v_lshlrev_b32 v120,16,v81\n"
"v_and_b32 v121,0xffff0000,v81\n"
"v_mul_f32 v120,v120,v119\n"
"v_mul_f32 v121,v121,v119\n"
"v_cvt_pk_bf16_f32 v81,v120,v121\n"
"v_accvgpr_read_b32 v120,a130\n"
"v_mul_f32 v120,v120,v119\n"
"v_accvgpr_write_b32 a130,v120\n"
"v_lshlrev_b32 v120,16,v82\n"
"v_and_b32 v121,0xffff0000,v82\n"
"v_mul_f32 v120,v120,v119\n"
"v_mul_f32 v121,v121,v119\n"
"v_cvt_pk_bf16_f32 v82,v120,v121\n"
"v_accvgpr_read_b32 v120,a131\n"
"v_mul_f32 v120,v120,v119\n"
"v_accvgpr_write_b32 a131,v120\n"
"v_lshlrev_b32 v120,16,v83\n"
"v_and_b32 v121,0xffff0000,v83\n"
"v_mul_f32 v120,v120,v119\n"
"v_mul_f32 v121,v121,v119\n"
"v_cvt_pk_bf16_f32 v83,v120,v121\n"
".Lfk_rare0_0:\n"
"s_bitcmp1_b32 s91,1\n"
"s_cbranch_scc0 .Lfk_rare0_1\n"
"v_max3_f32 v119,v8,v9,v10\n"
"v_max3_f32 v120,v11,v12,v13\n"
"v_max3_f32 v119,v119,v14,v15\n"
"v_max_f32 v119,v119,v120\n"
"v_mov_b32 v120,v119\n"
"s_nop 1\n"
"v_permlane16_swap_b32 v120,v119\n"
"v_max_f32 v119,v119,v120\n"
"v_mov_b32 v120,v119\n"
"s_nop 1\n"
"v_permlane32_swap_b32 v120,v119\n"
"v_max_f32 v119,v119,v120\n"
"s_bitcmp1_b32 s90,1\n"
"s_cselect_b32 s82,0xff800000,0\n"
"v_add_f32 v119,0x41980000,v119\n"
"v_max_f32 v119,s82,v119\n"
"v_cmp_lt_f32 vcc,s17,|v119|\n"
"s_nop 0\n"
"s_cbranch_vccz .Lfk_rare0_1\n"
"v_ceil_f32 v119,v119\n"
"v_sub_f32 v8,v8,v119\n"
"v_sub_f32 v9,v9,v119\n"
"v_sub_f32 v10,v10,v119\n"
"v_sub_f32 v11,v11,v119\n"
"v_sub_f32 v12,v12,v119\n"
"v_sub_f32 v13,v13,v119\n"
"v_sub_f32 v14,v14,v119\n"
"v_sub_f32 v15,v15,v119\n"
"v_sub_f32 v100,v100,v119\n"
"v_sub_f32 v101,v101,v119\n"
"v_sub_f32 v102,v102,v119\n"
"v_sub_f32 v103,v103,v119\n"
"s_cbranch_scc1 .Lfk_rare0_1\n"
"v_exp_f32 v119,-v119\n"
"s_nop 0\n"
"v_mul_f32 v160,v160,v119\n"
"v_mul_f32 v161,v161,v119\n"
"v_mul_f32 v162,v162,v119\n"
"v_mul_f32 v163,v163,v119\n"
"v_mul_f32 v164,v164,v119\n"
"v_mul_f32 v165,v165,v119\n"
"v_mul_f32 v166,v166,v119\n"
"v_mul_f32 v167,v167,v119\n"
"v_mul_f32 v168,v168,v119\n"
"v_mul_f32 v169,v169,v119\n"
"v_mul_f32 v170,v170,v119\n"
"v_mul_f32 v171,v171,v119\n"
"v_mul_f32 v172,v172,v119\n"
"v_mul_f32 v173,v173,v119\n"
"v_mul_f32 v174,v174,v119\n"
"v_mul_f32 v175,v175,v119\n"
"v_mul_f32 v176,v176,v119\n"
"v_mul_f32 v177,v177,v119\n"
"v_mul_f32 v178,v178,v119\n"
"v_mul_f32 v179,v179,v119\n"
"v_mul_f32 v180,v180,v119\n"
"v_mul_f32 v181,v181,v119\n"
"v_mul_f32 v182,v182,v119\n"
"v_mul_f32 v183,v183,v119\n"
"v_mul_f32 v184,v184,v119\n"
"v_mul_f32 v185,v185,v119\n"
"v_mul_f32 v186,v186,v119\n"
"v_mul_f32 v187,v187,v119\n"
"v_mul_f32 v188,v188,v119\n"
"v_mul_f32 v189,v189,v119\n"
"v_mul_f32 v190,v190,v119\n"
"v_mul_f32 v191,v191,v119\n"
"v_accvgpr_read_b32 v120,a132\n"
"v_mul_f32 v120,v120,v119\n"
"v_accvgpr_write_b32 a132,v120\n"
"v_lshlrev_b32 v120,16,v84\n"
"v_and_b32 v121,0xffff0000,v84\n"
"v_mul_f32 v120,v120,v119\n"
"v_mul_f32 v121,v121,v119\n"
"v_cvt_pk_bf16_f32 v84,v120,v121\n"
"v_accvgpr_read_b32 v120,a133\n"
"v_mul_f32 v120,v120,v119\n"
"v_accvgpr_write_b32 a133,v120\n"
"v_lshlrev_b32 v120,16,v85\n"
"v_and_b32 v121,0xffff0000,v85\n"
"v_mul_f32 v120,v120,v119\n"
"v_mul_f32 v121,v121,v119\n"
"v_cvt_pk_bf16_f32 v85,v120,v121\n"
"v_accvgpr_read_b32 v120,a134\n"
"v_mul_f32 v120,v120,v119\n"
"v_accvgpr_write_b32 a134,v120\n"
"v_lshlrev_b32 v120,16,v86\n"
"v_and_b32 v121,0xffff0000,v86\n"
"v_mul_f32 v120,v120,v119\n"
"v_mul_f32 v121,v121,v119\n"
"v_cvt_pk_bf16_f32 v86,v120,v121\n"
"v_accvgpr_read_b32 v120,a135\n"
"v_mul_f32 v120,v120,v119\n"
"v_accvgpr_write_b32 a135,v120\n"
"v_lshlrev_b32 v120,16,v87\n"
"v_and_b32 v121,0xffff0000,v87\n"
"v_mul_f32 v120,v120,v119\n"
"v_mul_f32 v121,v121,v119\n"
"v_cvt_pk_bf16_f32 v87,v120,v121\n"
".Lfk_rare0_1:\n"
"s_bitcmp1_b32 s91,2\n"
"s_cbranch_scc0 .Lfk_rare0_2\n"
"v_max3_f32 v119,v16,v17,v18\n"
"v_max3_f32 v120,v19,v20,v21\n"
"v_max3_f32 v119,v119,v22,v23\n"
"v_max_f32 v119,v119,v120\n"
"v_mov_b32 v120,v119\n"
"s_nop 1\n"
"v_permlane16_swap_b32 v120,v119\n"
"v_max_f32 v119,v119,v120\n"
"v_mov_b32 v120,v119\n"
"s_nop 1\n"
"v_permlane32_swap_b32 v120,v119\n"
"v_max_f32 v119,v119,v120\n"
"s_bitcmp1_b32 s90,2\n"
"s_cselect_b32 s82,0xff800000,0\n"
"v_add_f32 v119,0x41980000,v119\n"
"v_max_f32 v119,s82,v119\n"
"v_cmp_lt_f32 vcc,s17,|v119|\n"
"s_nop 0\n"
"s_cbranch_vccz .Lfk_rare0_2\n"
"v_ceil_f32 v119,v119\n"
"v_sub_f32 v16,v16,v119\n"
"v_sub_f32 v17,v17,v119\n"
"v_sub_f32 v18,v18,v119\n"
"v_sub_f32 v19,v19,v119\n"
"v_sub_f32 v20,v20,v119\n"
"v_sub_f32 v21,v21,v119\n"
"v_sub_f32 v22,v22,v119\n"
"v_sub_f32 v23,v23,v119\n"
"v_sub_f32 v104,v104,v119\n"
"v_sub_f32 v105,v105,v119\n"
"v_sub_f32 v106,v106,v119\n"
"v_sub_f32 v107,v107,v119\n"
"s_cbranch_scc1 .Lfk_rare0_2\n"
"v_exp_f32 v119,-v119\n"
"s_nop 0\n"
"v_mul_f32 v192,v192,v119\n"
"v_mul_f32 v193,v193,v119\n"
"v_mul_f32 v194,v194,v119\n"
"v_mul_f32 v195,v195,v119\n"
"v_mul_f32 v196,v196,v119\n"
"v_mul_f32 v197,v197,v119\n"
"v_mul_f32 v198,v198,v119\n"
"v_mul_f32 v199,v199,v119\n"
"v_mul_f32 v200,v200,v119\n"
"v_mul_f32 v201,v201,v119\n"
"v_mul_f32 v202,v202,v119\n"
"v_mul_f32 v203,v203,v119\n"
"v_mul_f32 v204,v204,v119\n"
"v_mul_f32 v205,v205,v119\n"
"v_mul_f32 v206,v206,v119\n"
"v_mul_f32 v207,v207,v119\n"
"v_mul_f32 v208,v208,v119\n"
"v_mul_f32 v209,v209,v119\n"
"v_mul_f32 v210,v210,v119\n"
"v_mul_f32 v211,v211,v119\n"
"v_mul_f32 v212,v212,v119\n"
"v_mul_f32 v213,v213,v119\n"
"v_mul_f32 v214,v214,v119\n"
"v_mul_f32 v215,v215,v119\n"
"v_mul_f32 v216,v216,v119\n"
"v_mul_f32 v217,v217,v119\n"
"v_mul_f32 v218,v218,v119\n"
"v_mul_f32 v219,v219,v119\n"
"v_mul_f32 v220,v220,v119\n"
"v_mul_f32 v221,v221,v119\n"
"v_mul_f32 v222,v222,v119\n"
"v_mul_f32 v223,v223,v119\n"
"v_accvgpr_read_b32 v120,a136\n"
"v_mul_f32 v120,v120,v119\n"
"v_accvgpr_write_b32 a136,v120\n"
"v_lshlrev_b32 v120,16,v88\n"
"v_and_b32 v121,0xffff0000,v88\n"
"v_mul_f32 v120,v120,v119\n"
"v_mul_f32 v121,v121,v119\n"
"v_cvt_pk_bf16_f32 v88,v120,v121\n"
"v_accvgpr_read_b32 v120,a137\n"
"v_mul_f32 v120,v120,v119\n"
"v_accvgpr_write_b32 a137,v120\n"
"v_lshlrev_b32 v120,16,v89\n"
"v_and_b32 v121,0xffff0000,v89\n"
"v_mul_f32 v120,v120,v119\n"
"v_mul_f32 v121,v121,v119\n"
"v_cvt_pk_bf16_f32 v89,v120,v121\n"
"v_accvgpr_read_b32 v120,a138\n"
"v_mul_f32 v120,v120,v119\n"
"v_accvgpr_write_b32 a138,v120\n"
"v_lshlrev_b32 v120,16,v90\n"
"v_and_b32 v121,0xffff0000,v90\n"
"v_mul_f32 v120,v120,v119\n"
"v_mul_f32 v121,v121,v119\n"
"v_cvt_pk_bf16_f32 v90,v120,v121\n"
"v_accvgpr_read_b32 v120,a139\n"
"v_mul_f32 v120,v120,v119\n"
"v_accvgpr_write_b32 a139,v120\n"
"v_lshlrev_b32 v120,16,v91\n"
"v_and_b32 v121,0xffff0000,v91\n"
"v_mul_f32 v120,v120,v119\n"
"v_mul_f32 v121,v121,v119\n"
"v_cvt_pk_bf16_f32 v91,v120,v121\n"
".Lfk_rare0_2:\n"
"s_bitcmp1_b32 s91,3\n"
"s_cbranch_scc0 .Lfk_rare0_3\n"
"v_max3_f32 v119,v24,v25,v26\n"
"v_max3_f32 v120,v27,v28,v29\n"
"v_max3_f32 v119,v119,v30,v31\n"
"v_max_f32 v119,v119,v120\n"
"v_mov_b32 v120,v119\n"
"s_nop 1\n"
"v_permlane16_swap_b32 v120,v119\n"
"v_max_f32 v119,v119,v120\n"
"v_mov_b32 v120,v119\n"
"s_nop 1\n"
"v_permlane32_swap_b32 v120,v119\n"
"v_max_f32 v119,v119,v120\n"
"s_bitcmp1_b32 s90,3\n"
"s_cselect_b32 s82,0xff800000,0\n"
"v_add_f32 v119,0x41980000,v119\n"
"v_max_f32 v119,s82,v119\n"
"v_cmp_lt_f32 vcc,s17,|v119|\n"
"s_nop 0\n"
"s_cbranch_vccz .Lfk_rare0_3\n"
"v_ceil_f32 v119,v119\n"
"v_sub_f32 v24,v24,v119\n"
"v_sub_f32 v25,v25,v119\n"
"v_sub_f32 v26,v26,v119\n"
"v_sub_f32 v27,v27,v119\n"
"v_sub_f32 v28,v28,v119\n"
"v_sub_f32 v29,v29,v119\n"
"v_sub_f32 v30,v30,v119\n"
"v_sub_f32 v31,v31,v119\n"
"v_sub_f32 v108,v108,v119\n"
"v_sub_f32 v109,v109,v119\n"
"v_sub_f32 v110,v110,v119\n"
"v_sub_f32 v111,v111,v119\n"
"s_cbranch_scc1 .Lfk_rare0_3\n"
"v_exp_f32 v119,-v119\n"
"s_nop 0\n"
"v_mul_f32 v224,v224,v119\n"
"v_mul_f32 v225,v225,v119\n"
"v_mul_f32 v226,v226,v119\n"
"v_mul_f32 v227,v227,v119\n"
"v_mul_f32 v228,v228,v119\n"
"v_mul_f32 v229,v229,v119\n"
"v_mul_f32 v230,v230,v119\n"
"v_mul_f32 v231,v231,v119\n"
"v_mul_f32 v232,v232,v119\n"
"v_mul_f32 v233,v233,v119\n"
"v_mul_f32 v234,v234,v119\n"
"v_mul_f32 v235,v235,v119\n"
"v_mul_f32 v236,v236,v119\n"
"v_mul_f32 v237,v237,v119\n"
"v_mul_f32 v238,v238,v119\n"
"v_mul_f32 v239,v239,v119\n"
"v_mul_f32 v240,v240,v119\n"
"v_mul_f32 v241,v241,v119\n"
"v_mul_f32 v242,v242,v119\n"
"v_mul_f32 v243,v243,v119\n"
"v_mul_f32 v244,v244,v119\n"
"v_mul_f32 v245,v245,v119\n"
"v_mul_f32 v246,v246,v119\n"
"v_mul_f32 v247,v247,v119\n"
"v_mul_f32 v248,v248,v119\n"
"v_mul_f32 v249,v249,v119\n"
"v_mul_f32 v250,v250,v119\n"
"v_mul_f32 v251,v251,v119\n"
"v_mul_f32 v252,v252,v119\n"
"v_mul_f32 v253,v253,v119\n"
"v_mul_f32 v254,v254,v119\n"
"v_mul_f32 v255,v255,v119\n"
"v_accvgpr_read_b32 v120,a140\n"
"v_mul_f32 v120,v120,v119\n"
"v_accvgpr_write_b32 a140,v120\n"
"v_lshlrev_b32 v120,16,v92\n"
"v_and_b32 v121,0xffff0000,v92\n"
"v_mul_f32 v120,v120,v119\n"
"v_mul_f32 v121,v121,v119\n"
"v_cvt_pk_bf16_f32 v92,v120,v121\n"
"v_accvgpr_read_b32 v120,a141\n"
"v_mul_f32 v120,v120,v119\n"
"v_accvgpr_write_b32 a141,v120\n"
"v_lshlrev_b32 v120,16,v93\n"
"v_and_b32 v121,0xffff0000,v93\n"
"v_mul_f32 v120,v120,v119\n"
"v_mul_f32 v121,v121,v119\n"
"v_cvt_pk_bf16_f32 v93,v120,v121\n"
"v_accvgpr_read_b32 v120,a142\n"
"v_mul_f32 v120,v120,v119\n"
"v_accvgpr_write_b32 a142,v120\n"
"v_lshlrev_b32 v120,16,v94\n"
"v_and_b32 v121,0xffff0000,v94\n"
"v_mul_f32 v120,v120,v119\n"
"v_mul_f32 v121,v121,v119\n"
"v_cvt_pk_bf16_f32 v94,v120,v121\n"
"v_accvgpr_read_b32 v120,a143\n"
"v_mul_f32 v120,v120,v119\n"
"v_accvgpr_write_b32 a143,v120\n"
"v_lshlrev_b32 v120,16,v95\n"
"v_and_b32 v121,0xffff0000,v95\n"
"v_mul_f32 v120,v120,v119\n"
"v_mul_f32 v121,v121,v119\n"
"v_cvt_pk_bf16_f32 v95,v120,v121\n"
".Lfk_rare0_3:\n"
"s_mov_b32 s90,0\n"
"s_nop 1\n"
"s_setpc_b64 s[94:95]\n"
".Lfk_rare2:\n"
"s_nop 7\n"
"v_max3_f32 v119,v0,v1,v2\n"
"v_max3_f32 v120,v3,v4,v5\n"
"v_max3_f32 v119,v119,v6,v7\n"
"v_max_f32 v119,v119,v120\n"
"v_mov_b32 v120,v119\n"
"s_nop 1\n"
"v_permlane16_swap_b32 v120,v119\n"
"v_max_f32 v119,v119,v120\n"
"v_mov_b32 v120,v119\n"
"s_nop 1\n"
"v_permlane32_swap_b32 v120,v119\n"
"v_max_f32 v119,v119,v120\n"
"s_bitcmp1_b32 s90,0\n"
"s_cselect_b32 s82,0xff800000,0\n"
"v_add_f32 v119,0x41980000,v119\n"
"v_max_f32 v119,s82,v119\n"
"v_cmp_lt_f32 vcc,s17,|v119|\n"
"s_nop 0\n"
"s_cbranch_vccz .Lfk_rare2_0\n"
"v_ceil_f32 v119,v119\n"
"v_sub_f32 v0,v0,v119\n"
"v_sub_f32 v1,v1,v119\n"
"v_sub_f32 v2,v2,v119\n"
"v_sub_f32 v3,v3,v119\n"
"v_sub_f32 v4,v4,v119\n"
"v_sub_f32 v5,v5,v119\n"
"v_sub_f32 v6,v6,v119\n"
"v_sub_f32 v7,v7,v119\n"
"v_sub_f32 v96,v96,v119\n"
"v_sub_f32 v97,v97,v119\n"
"v_sub_f32 v98,v98,v119\n"
"v_sub_f32 v99,v99,v119\n"
"v_sub_f32 v32,v32,v119\n"
"v_sub_f32 v33,v33,v119\n"
"v_sub_f32 v34,v34,v119\n"
"v_sub_f32 v35,v35,v119\n"
"v_sub_f32 v36,v36,v119\n"
"v_sub_f32 v37,v37,v119\n"
"v_sub_f32 v38,v38,v119\n"
"v_sub_f32 v39,v39,v119\n"
"v_exp_f32 v0,v0\n"
"v_exp_f32 v1,v1\n"
"v_exp_f32 v2,v2\n"
"v_exp_f32 v3,v3\n"
"v_exp_f32 v4,v4\n"
"v_exp_f32 v5,v5\n"
"v_exp_f32 v6,v6\n"
"v_exp_f32 v7,v7\n"
"v_cvt_pk_bf16_f32 v64,v0,v1\n"
"v_cvt_pk_bf16_f32 v65,v2,v3\n"
"v_cvt_pk_bf16_f32 v66,v4,v5\n"
"v_cvt_pk_bf16_f32 v67,v6,v7\n"
"s_cbranch_scc1 .Lfk_rare2_0\n"
"v_exp_f32 v119,-v119\n"
"s_nop 0\n"
"v_mul_f32 v128,v128,v119\n"
"v_mul_f32 v129,v129,v119\n"
"v_mul_f32 v130,v130,v119\n"
"v_mul_f32 v131,v131,v119\n"
"v_mul_f32 v132,v132,v119\n"
"v_mul_f32 v133,v133,v119\n"
"v_mul_f32 v134,v134,v119\n"
"v_mul_f32 v135,v135,v119\n"
"v_mul_f32 v136,v136,v119\n"
"v_mul_f32 v137,v137,v119\n"
"v_mul_f32 v138,v138,v119\n"
"v_mul_f32 v139,v139,v119\n"
"v_mul_f32 v140,v140,v119\n"
"v_mul_f32 v141,v141,v119\n"
"v_mul_f32 v142,v142,v119\n"
"v_mul_f32 v143,v143,v119\n"
"v_mul_f32 v144,v144,v119\n"
"v_mul_f32 v145,v145,v119\n"
"v_mul_f32 v146,v146,v119\n"
"v_mul_f32 v147,v147,v119\n"
"v_mul_f32 v148,v148,v119\n"
"v_mul_f32 v149,v149,v119\n"
"v_mul_f32 v150,v150,v119\n"
"v_mul_f32 v151,v151,v119\n"
"v_mul_f32 v152,v152,v119\n"
"v_mul_f32 v153,v153,v119\n"
"v_mul_f32 v154,v154,v119\n"
"v_mul_f32 v155,v155,v119\n"
"v_mul_f32 v156,v156,v119\n"
"v_mul_f32 v157,v157,v119\n"
"v_mul_f32 v158,v158,v119\n"
"v_mul_f32 v159,v159,v119\n"
"v_accvgpr_read_b32 v120,a128\n"
"v_mul_f32 v120,v120,v119\n"
"v_accvgpr_write_b32 a128,v120\n"
"v_accvgpr_read_b32 v120,a129\n"
"v_mul_f32 v120,v120,v119\n"
"v_accvgpr_write_b32 a129,v120\n"
"v_accvgpr_read_b32 v120,a130\n"
"v_mul_f32 v120,v120,v119\n"
"v_accvgpr_write_b32 a130,v120\n"
"v_accvgpr_read_b32 v120,a131\n"
"v_mul_f32 v120,v120,v119\n"
"v_accvgpr_write_b32 a131,v120\n"
".Lfk_rare2_0:\n"
"v_max3_f32 v119,v8,v9,v10\n"
"v_max3_f32 v120,v11,v12,v13\n"
"v_max3_f32 v119,v119,v14,v15\n"
"v_max_f32 v119,v119,v120\n"
"v_mov_b32 v120,v119\n"
"s_nop 1\n"
"v_permlane16_swap_b32 v120,v119\n"
"v_max_f32 v119,v119,v120\n"
"v_mov_b32 v120,v119\n"
"s_nop 1\n"
"v_permlane32_swap_b32 v120,v119\n"
"v_max_f32 v119,v119,v120\n"
"s_bitcmp1_b32 s90,1\n"
"s_cselect_b32 s82,0xff800000,0\n"
"v_add_f32 v119,0x41980000,v119\n"
"v_max_f32 v119,s82,v119\n"
"v_cmp_lt_f32 vcc,s17,|v119|\n"
"s_nop 0\n"
"s_cbranch_vccz .Lfk_rare2_1\n"
"v_ceil_f32 v119,v119\n"
"v_sub_f32 v8,v8,v119\n"
"v_sub_f32 v9,v9,v119\n"
"v_sub_f32 v10,v10,v119\n"
"v_sub_f32 v11,v11,v119\n"
"v_sub_f32 v12,v12,v119\n"
"v_sub_f32 v13,v13,v119\n"
"v_sub_f32 v14,v14,v119\n"
"v_sub_f32 v15,v15,v119\n"
"v_sub_f32 v100,v100,v119\n"
"v_sub_f32 v101,v101,v119\n"
"v_sub_f32 v102,v102,v119\n"
"v_sub_f32 v103,v103,v119\n"
"v_sub_f32 v40,v40,v119\n"
"v_sub_f32 v41,v41,v119\n"
"v_sub_f32 v42,v42,v119\n"
"v_sub_f32 v43,v43,v119\n"
"v_sub_f32 v44,v44,v119\n"
"v_sub_f32 v45,v45,v119\n"
"v_sub_f32 v46,v46,v119\n"
"v_sub_f32 v47,v47,v119\n"
"v_exp_f32 v8,v8\n"
"v_exp_f32 v9,v9\n"
"v_exp_f32 v10,v10\n"
"v_exp_f32 v11,v11\n"
"v_exp_f32 v12,v12\n"
"v_exp_f32 v13,v13\n"
"v_exp_f32 v14,v14\n"
"v_exp_f32 v15,v15\n"
"v_cvt_pk_bf16_f32 v68,v8,v9\n"
"v_cvt_pk_bf16_f32 v69,v10,v11\n"
"v_cvt_pk_bf16_f32 v70,v12,v13\n"
"v_cvt_pk_bf16_f32 v71,v14,v15\n"
"s_cbranch_scc1 .Lfk_rare2_1\n"
"v_exp_f32 v119,-v119\n"
"s_nop 0\n"
"v_mul_f32 v160,v160,v119\n"
"v_mul_f32 v161,v161,v119\n"
"v_mul_f32 v162,v162,v119\n"
"v_mul_f32 v163,v163,v119\n"
"v_mul_f32 v164,v164,v119\n"
"v_mul_f32 v165,v165,v119\n"
"v_mul_f32 v166,v166,v119\n"
"v_mul_f32 v167,v167,v119\n"
"v_mul_f32 v168,v168,v119\n"
"v_mul_f32 v169,v169,v119\n"
"v_mul_f32 v170,v170,v119\n"
"v_mul_f32 v171,v171,v119\n"
"v_mul_f32 v172,v172,v119\n"
"v_mul_f32 v173,v173,v119\n"
"v_mul_f32 v174,v174,v119\n"
"v_mul_f32 v175,v175,v119\n"
"v_mul_f32 v176,v176,v119\n"
"v_mul_f32 v177,v177,v119\n"
"v_mul_f32 v178,v178,v119\n"
"v_mul_f32 v179,v179,v119\n"
"v_mul_f32 v180,v180,v119\n"
"v_mul_f32 v181,v181,v119\n"
"v_mul_f32 v182,v182,v119\n"
"v_mul_f32 v183,v183,v119\n"
"v_mul_f32 v184,v184,v119\n"
"v_mul_f32 v185,v185,v119\n"
"v_mul_f32 v186,v186,v119\n"
"v_mul_f32 v187,v187,v119\n"
"v_mul_f32 v188,v188,v119\n"
"v_mul_f32 v189,v189,v119\n"
"v_mul_f32 v190,v190,v119\n"
"v_mul_f32 v191,v191,v119\n"
"v_accvgpr_read_b32 v120,a132\n"
"v_mul_f32 v120,v120,v119\n"
"v_accvgpr_write_b32 a132,v120\n"
"v_accvgpr_read_b32 v120,a133\n"
"v_mul_f32 v120,v120,v119\n"
"v_accvgpr_write_b32 a133,v120\n"
"v_accvgpr_read_b32 v120,a134\n"
"v_mul_f32 v120,v120,v119\n"
"v_accvgpr_write_b32 a134,v120\n"
"v_accvgpr_read_b32 v120,a135\n"
"v_mul_f32 v120,v120,v119\n"
"v_accvgpr_write_b32 a135,v120\n"
".Lfk_rare2_1:\n"
"v_max3_f32 v119,v16,v17,v18\n"
"v_max3_f32 v120,v19,v20,v21\n"
"v_max3_f32 v119,v119,v22,v23\n"
"v_max_f32 v119,v119,v120\n"
"v_mov_b32 v120,v119\n"
"s_nop 1\n"
"v_permlane16_swap_b32 v120,v119\n"
"v_max_f32 v119,v119,v120\n"
"v_mov_b32 v120,v119\n"
"s_nop 1\n"
"v_permlane32_swap_b32 v120,v119\n"
"v_max_f32 v119,v119,v120\n"
"s_bitcmp1_b32 s90,2\n"
"s_cselect_b32 s82,0xff800000,0\n"
"v_add_f32 v119,0x41980000,v119\n"
"v_max_f32 v119,s82,v119\n"
"v_cmp_lt_f32 vcc,s17,|v119|\n"
"s_nop 0\n"
"s_cbranch_vccz .Lfk_rare2_2\n"
"v_ceil_f32 v119,v119\n"
"v_sub_f32 v16,v16,v119\n"
"v_sub_f32 v17,v17,v119\n"
"v_sub_f32 v18,v18,v119\n"
"v_sub_f32 v19,v19,v119\n"
"v_sub_f32 v20,v20,v119\n"
"v_sub_f32 v21,v21,v119\n"
"v_sub_f32 v22,v22,v119\n"
"v_sub_f32 v23,v23,v119\n"
"v_sub_f32 v104,v104,v119\n"
"v_sub_f32 v105,v105,v119\n"
"v_sub_f32 v106,v106,v119\n"
"v_sub_f32 v107,v107,v119\n"
"v_sub_f32 v48,v48,v119\n"
"v_sub_f32 v49,v49,v119\n"
"v_sub_f32 v50,v50,v119\n"
"v_sub_f32 v51,v51,v119\n"
"v_sub_f32 v52,v52,v119\n"
"v_sub_f32 v53,v53,v119\n"
"v_sub_f32 v54,v54,v119\n"
"v_sub_f32 v55,v55,v119\n"
"v_exp_f32 v16,v16\n"
"v_exp_f32 v17,v17\n"
"v_exp_f32 v18,v18\n"
"v_exp_f32 v19,v19\n"
"v_exp_f32 v20,v20\n"
"v_exp_f32 v21,v21\n"
"v_exp_f32 v22,v22\n"
"v_exp_f32 v23,v23\n"
"v_cvt_pk_bf16_f32 v72,v16,v17\n"
"v_cvt_pk_bf16_f32 v73,v18,v19\n"
"v_cvt_pk_bf16_f32 v74,v20,v21\n"
"v_cvt_pk_bf16_f32 v75,v22,v23\n"
"s_cbranch_scc1 .Lfk_rare2_2\n"
"v_exp_f32 v119,-v119\n"
"s_nop 0\n"
"v_mul_f32 v192,v192,v119\n"
"v_mul_f32 v193,v193,v119\n"
"v_mul_f32 v194,v194,v119\n"
"v_mul_f32 v195,v195,v119\n"
"v_mul_f32 v196,v196,v119\n"
"v_mul_f32 v197,v197,v119\n"
"v_mul_f32 v198,v198,v119\n"
"v_mul_f32 v199,v199,v119\n"
"v_mul_f32 v200,v200,v119\n"
"v_mul_f32 v201,v201,v119\n"
"v_mul_f32 v202,v202,v119\n"
"v_mul_f32 v203,v203,v119\n"
"v_mul_f32 v204,v204,v119\n"
"v_mul_f32 v205,v205,v119\n"
"v_mul_f32 v206,v206,v119\n"
"v_mul_f32 v207,v207,v119\n"
"v_mul_f32 v208,v208,v119\n"
"v_mul_f32 v209,v209,v119\n"
"v_mul_f32 v210,v210,v119\n"
"v_mul_f32 v211,v211,v119\n"
"v_mul_f32 v212,v212,v119\n"
"v_mul_f32 v213,v213,v119\n"
"v_mul_f32 v214,v214,v119\n"
"v_mul_f32 v215,v215,v119\n"
"v_mul_f32 v216,v216,v119\n"
"v_mul_f32 v217,v217,v119\n"
"v_mul_f32 v218,v218,v119\n"
"v_mul_f32 v219,v219,v119\n"
"v_mul_f32 v220,v220,v119\n"
"v_mul_f32 v221,v221,v119\n"
"v_mul_f32 v222,v222,v119\n"
"v_mul_f32 v223,v223,v119\n"
"v_accvgpr_read_b32 v120,a136\n"
"v_mul_f32 v120,v120,v119\n"
"v_accvgpr_write_b32 a136,v120\n"
"v_accvgpr_read_b32 v120,a137\n"
"v_mul_f32 v120,v120,v119\n"
"v_accvgpr_write_b32 a137,v120\n"
"v_accvgpr_read_b32 v120,a138\n"
"v_mul_f32 v120,v120,v119\n"
"v_accvgpr_write_b32 a138,v120\n"
"v_accvgpr_read_b32 v120,a139\n"
"v_mul_f32 v120,v120,v119\n"
"v_accvgpr_write_b32 a139,v120\n"
".Lfk_rare2_2:\n"
"v_max3_f32 v119,v24,v25,v26\n"
"v_max3_f32 v120,v27,v28,v29\n"
"v_max3_f32 v119,v119,v30,v31\n"
"v_max_f32 v119,v119,v120\n"
"v_mov_b32 v120,v119\n"
"s_nop 1\n"
"v_permlane16_swap_b32 v120,v119\n"
"v_max_f32 v119,v119,v120\n"
"v_mov_b32 v120,v119\n"
"s_nop 1\n"
"v_permlane32_swap_b32 v120,v119\n"
"v_max_f32 v119,v119,v120\n"
"s_bitcmp1_b32 s90,3\n"
"s_cselect_b32 s82,0xff800000,0\n"
"v_add_f32 v119,0x41980000,v119\n"
"v_max_f32 v119,s82,v119\n"
"v_cmp_lt_f32 vcc,s17,|v119|\n"
"s_nop 0\n"
"s_cbranch_vccz .Lfk_rare2_3\n"
"v_ceil_f32 v119,v119\n"
"v_sub_f32 v24,v24,v119\n"
"v_sub_f32 v25,v25,v119\n"
"v_sub_f32 v26,v26,v119\n"
"v_sub_f32 v27,v27,v119\n"
"v_sub_f32 v28,v28,v119\n"
"v_sub_f32 v29,v29,v119\n"
"v_sub_f32 v30,v30,v119\n"
"v_sub_f32 v31,v31,v119\n"
"v_sub_f32 v108,v108,v119\n"
"v_sub_f32 v109,v109,v119\n"
"v_sub_f32 v110,v110,v119\n"
"v_sub_f32 v111,v111,v119\n"
"v_sub_f32 v56,v56,v119\n"
"v_sub_f32 v57,v57,v119\n"
"v_sub_f32 v58,v58,v119\n"
"v_sub_f32 v59,v59,v119\n"
"v_sub_f32 v60,v60,v119\n"
"v_sub_f32 v61,v61,v119\n"
"v_sub_f32 v62,v62,v119\n"
"v_sub_f32 v63,v63,v119\n"
"v_exp_f32 v24,v24\n"
"v_exp_f32 v25,v25\n"
"v_exp_f32 v26,v26\n"
"v_exp_f32 v27,v27\n"
"v_exp_f32 v28,v28\n"
"v_exp_f32 v29,v29\n"
"v_exp_f32 v30,v30\n"
"v_exp_f32 v31,v31\n"
"v_cvt_pk_bf16_f32 v76,v24,v25\n"
"v_cvt_pk_bf16_f32 v77,v26,v27\n"
"v_cvt_pk_bf16_f32 v78,v28,v29\n"
"v_cvt_pk_bf16_f32 v79,v30,v31\n"
"s_cbranch_scc1 .Lfk_rare2_3\n"
"v_exp_f32 v119,-v119\n"
"s_nop 0\n"
"v_mul_f32 v224,v224,v119\n"
"v_mul_f32 v225,v225,v119\n"
"v_mul_f32 v226,v226,v119\n"
"v_mul_f32 v227,v227,v119\n"
"v_mul_f32 v228,v228,v119\n"
"v_mul_f32 v229,v229,v119\n"
"v_mul_f32 v230,v230,v119\n"
"v_mul_f32 v231,v231,v119\n"
"v_mul_f32 v232,v232,v119\n"
"v_mul_f32 v233,v233,v119\n"
"v_mul_f32 v234,v234,v119\n"
"v_mul_f32 v235,v235,v119\n"
"v_mul_f32 v236,v236,v119\n"
"v_mul_f32 v237,v237,v119\n"
"v_mul_f32 v238,v238,v119\n"
"v_mul_f32 v239,v239,v119\n"
"v_mul_f32 v240,v240,v119\n"
"v_mul_f32 v241,v241,v119\n"
"v_mul_f32 v242,v242,v119\n"
"v_mul_f32 v243,v243,v119\n"
"v_mul_f32 v244,v244,v119\n"
"v_mul_f32 v245,v245,v119\n"
"v_mul_f32 v246,v246,v119\n"
"v_mul_f32 v247,v247,v119\n"
"v_mul_f32 v248,v248,v119\n"
"v_mul_f32 v249,v249,v119\n"
"v_mul_f32 v250,v250,v119\n"
"v_mul_f32 v251,v251,v119\n"
"v_mul_f32 v252,v252,v119\n"
"v_mul_f32 v253,v253,v119\n"
"v_mul_f32 v254,v254,v119\n"
"v_mul_f32 v255,v255,v119\n"
"v_accvgpr_read_b32 v120,a140\n"
"v_mul_f32 v120,v120,v119\n"
"v_accvgpr_write_b32 a140,v120\n"
"v_accvgpr_read_b32 v120,a141\n"
"v_mul_f32 v120,v120,v119\n"
"v_accvgpr_write_b32 a141,v120\n"
"v_accvgpr_read_b32 v120,a142\n"
"v_mul_f32 v120,v120,v119\n"
"v_accvgpr_write_b32 a142,v120\n"
"v_accvgpr_read_b32 v120,a143\n"
"v_mul_f32 v120,v120,v119\n"
"v_accvgpr_write_b32 a143,v120\n"
".Lfk_rare2_3:\n"
"s_mov_b32 s90,0\n"
"s_nop 1\n"
"s_setpc_b64 s[94:95]\n"
".Lfk_mslow0:\n"
"s_nop 7\n"
"v_min_f32 v119,v98,v104\n"
"v_sub_f32 v120,v119,v104\n"
"v_sub_f32 v119,v119,v98\n"
"v_exp_f32 v120,v120\n"
"v_exp_f32 v119,v119\n"
"v_mul_f32 v192,v192,v120\n"
"v_mul_f32 v193,v193,v120\n"
"v_mul_f32 v194,v194,v120\n"
"v_mul_f32 v195,v195,v120\n"
"v_mul_f32 v196,v196,v120\n"
"v_mul_f32 v197,v197,v120\n"
"v_mul_f32 v198,v198,v120\n"
"v_mul_f32 v199,v199,v120\n"
"v_mul_f32 v200,v200,v120\n"
"v_mul_f32 v201,v201,v120\n"
"v_mul_f32 v202,v202,v120\n"
"v_mul_f32 v203,v203,v120\n"
"v_mul_f32 v204,v204,v120\n"
"v_mul_f32 v205,v205,v120\n"
"v_mul_f32 v206,v206,v120\n"
"v_mul_f32 v207,v207,v120\n"
"v_mul_f32 v208,v208,v120\n"
"v_mul_f32 v209,v209,v120\n"
"v_mul_f32 v210,v210,v120\n"
"v_mul_f32 v211,v211,v120\n"
"v_mul_f32 v212,v212,v120\n"
"v_mul_f32 v213,v213,v120\n"
"v_mul_f32 v214,v214,v120\n"
"v_mul_f32 v215,v215,v120\n"
"v_mul_f32 v216,v216,v120\n"
"v_mul_f32 v217,v217,v120\n"
"v_mul_f32 v218,v218,v120\n"
"v_mul_f32 v219,v219,v120\n"
"v_mul_f32 v220,v220,v120\n"
"v_mul_f32 v221,v221,v120\n"
"v_mul_f32 v222,v222,v120\n"
"v_mul_f32 v223,v223,v120\n"
"v_accvgpr_read_b32 v121,a136\n"
"v_mul_f32 v121,v121,v120\n"
"v_accvgpr_write_b32 a136,v121\n"
"v_mul_f32 v99,v99,v119\n"
"v_accvgpr_read_b32 v121,a64\n"
"v_lshlrev_b32 v120,16,v121\n"
"v_and_b32 v121,0xffff0000,v121\n"
"v_mul_f32 v120,v120,v119\n"
"v_mul_f32 v121,v121,v119\n"
"v_cvt_pk_bf16_f32 v121,v120,v121\n"
"v_accvgpr_write_b32 a64,v121\n"
"v_accvgpr_read_b32 v121,a65\n"
"v_lshlrev_b32 v120,16,v121\n"
"v_and_b32 v121,0xffff0000,v121\n"
"v_mul_f32 v120,v120,v119\n"
"v_mul_f32 v121,v121,v119\n"
"v_cvt_pk_bf16_f32 v121,v120,v121\n"
"v_accvgpr_write_b32 a65,v121\n"
"v_accvgpr_read_b32 v121,a66\n"
"v_lshlrev_b32 v120,16,v121\n"
"v_and_b32 v121,0xffff0000,v121\n"
"v_mul_f32 v120,v120,v119\n"
"v_mul_f32 v121,v121,v119\n"
"v_cvt_pk_bf16_f32 v121,v120,v121\n"
"v_accvgpr_write_b32 a66,v121\n"
"v_accvgpr_read_b32 v121,a67\n"
"v_lshlrev_b32 v120,16,v121\n"
"v_and_b32 v121,0xffff0000,v121\n"
"v_mul_f32 v120,v120,v119\n"
"v_mul_f32 v121,v121,v119\n"
"v_cvt_pk_bf16_f32 v121,v120,v121\n"
"v_accvgpr_write_b32 a67,v121\n"
"v_accvgpr_read_b32 v121,a68\n"
"v_lshlrev_b32 v120,16,v121\n"
"v_and_b32 v121,0xffff0000,v121\n"
"v_mul_f32 v120,v120,v119\n"
"v_mul_f32 v121,v121,v119\n"
"v_cvt_pk_bf16_f32 v121,v120,v121\n"
"v_accvgpr_write_b32 a68,v121\n"
"v_accvgpr_read_b32 v121,a69\n"
"v_lshlrev_b32 v120,16,v121\n"
"v_and_b32 v121,0xffff0000,v121\n"
"v_mul_f32 v120,v120,v119\n"
"v_mul_f32 v121,v121,v119\n"
"v_cvt_pk_bf16_f32 v121,v120,v121\n"
"v_accvgpr_write_b32 a69,v121\n"
"v_accvgpr_read_b32 v121,a70\n"
"v_lshlrev_b32 v120,16,v121\n"
"v_and_b32 v121,0xffff0000,v121\n"
"v_mul_f32 v120,v120,v119\n"
"v_mul_f32 v121,v121,v119\n"
"v_cvt_pk_bf16_f32 v121,v120,v121\n"
"v_accvgpr_write_b32 a70,v121\n"
"v_accvgpr_read_b32 v121,a71\n"
"v_lshlrev_b32 v120,16,v121\n"
"v_and_b32 v121,0xffff0000,v121\n"
"v_mul_f32 v120,v120,v119\n"
"v_mul_f32 v121,v121,v119\n"
"v_cvt_pk_bf16_f32 v121,v120,v121\n"
"v_accvgpr_write_b32 a71,v121\n"
"v_accvgpr_read_b32 v121,a72\n"
"v_lshlrev_b32 v120,16,v121\n"
"v_and_b32 v121,0xffff0000,v121\n"
"v_mul_f32 v120,v120,v119\n"
"v_mul_f32 v121,v121,v119\n"
"v_cvt_pk_bf16_f32 v121,v120,v121\n"
"v_accvgpr_write_b32 a72,v121\n"
"v_accvgpr_read_b32 v121,a73\n"
"v_lshlrev_b32 v120,16,v121\n"
"v_and_b32 v121,0xffff0000,v121\n"
"v_mul_f32 v120,v120,v119\n"
"v_mul_f32 v121,v121,v119\n"
"v_cvt_pk_bf16_f32 v121,v120,v121\n"
"v_accvgpr_write_b32 a73,v121\n"
"v_accvgpr_read_b32 v121,a74\n"
"v_lshlrev_b32 v120,16,v121\n"
"v_and_b32 v121,0xffff0000,v121\n"
"v_mul_f32 v120,v120,v119\n"
"v_mul_f32 v121,v121,v119\n"
"v_cvt_pk_bf16_f32 v121,v120,v121\n"
"v_accvgpr_write_b32 a74,v121\n"
"v_accvgpr_read_b32 v121,a75\n"
"v_lshlrev_b32 v120,16,v121\n"
"v_and_b32 v121,0xffff0000,v121\n"
"v_mul_f32 v120,v120,v119\n"
"v_mul_f32 v121,v121,v119\n"
"v_cvt_pk_bf16_f32 v121,v120,v121\n"
"v_accvgpr_write_b32 a75,v121\n"
"v_accvgpr_read_b32 v121,a76\n"
"v_lshlrev_b32 v120,16,v121\n"
"v_and_b32 v121,0xffff0000,v121\n"
"v_mul_f32 v120,v120,v119\n"
"v_mul_f32 v121,v121,v119\n"
"v_cvt_pk_bf16_f32 v121,v120,v121\n"
"v_accvgpr_write_b32 a76,v121\n"
"v_accvgpr_read_b32 v121,a77\n"
"v_lshlrev_b32 v120,16,v121\n"
"v_and_b32 v121,0xffff0000,v121\n"
"v_mul_f32 v120,v120,v119\n"
"v_mul_f32 v121,v121,v119\n"
"v_cvt_pk_bf16_f32 v121,v120,v121\n"
"v_accvgpr_write_b32 a77,v121\n"
"v_accvgpr_read_b32 v121,a78\n"
"v_lshlrev_b32 v120,16,v121\n"
"v_and_b32 v121,0xffff0000,v121\n"
"v_mul_f32 v120,v120,v119\n"
"v_mul_f32 v121,v121,v119\n"
"v_cvt_pk_bf16_f32 v121,v120,v121\n"
"v_accvgpr_write_b32 a78,v121\n"
"v_accvgpr_read_b32 v121,a79\n"
"v_lshlrev_b32 v120,16,v121\n"
"v_and_b32 v121,0xffff0000,v121\n"
"v_mul_f32 v120,v120,v119\n"
"v_mul_f32 v121,v121,v119\n"
"v_cvt_pk_bf16_f32 v121,v120,v121\n"
"v_accvgpr_write_b32 a79,v121\n"
"s_nop 1\n"
"s_setpc_b64 s[94:95]\n"
".Lfk_rare1:\n"
"s_nop 7\n"
"s_bitcmp1_b32 s91,0\n"
"s_cbranch_scc0 .Lfk_rare1_0\n"
"v_max3_f32 v119,v32,v33,v34\n"
"v_max3_f32 v120,v35,v36,v37\n"
"v_max3_f32 v119,v119,v38,v39\n"
"v_max_f32 v119,v119,v120\n"
"v_mov_b32 v120,v119\n"
"s_nop 1\n"
"v_permlane16_swap_b32 v120,v119\n"
"v_max_f32 v119,v119,v120\n"
"v_mov_b32 v120,v119\n"
"s_nop 1\n"
"v_permlane32_swap_b32 v120,v119\n"
"v_max_f32 v119,v119,v120\n"
"s_bitcmp1_b32 s90,0\n"
"s_cselect_b32 s82,0xff800000,0\n"
"v_add_f32 v119,0x41980000,v119\n"
"v_max_f32 v119,s82,v119\n"
"v_cmp_lt_f32 vcc,s17,|v119|\n"
"s_nop 0\n"
"s_cbranch_vccz .Lfk_rare1_0\n"
"v_ceil_f32 v119,v119\n"
"v_sub_f32 v32,v32,v119\n"
"v_sub_f32 v33,v33,v119\n"
"v_sub_f32 v34,v34,v119\n"
"v_sub_f32 v35,v35,v119\n"
"v_sub_f32 v36,v36,v119\n"
"v_sub_f32 v37,v37,v119\n"
"v_sub_f32 v38,v38,v119\n"
"v_sub_f32 v39,v39,v119\n"
"v_sub_f32 v96,v96,v119\n"
"v_sub_f32 v97,v97,v119\n"
"v_sub_f32 v98,v98,v119\n"
"v_sub_f32 v99,v99,v119\n"
"s_cbranch_scc1 .Lfk_rare1_0\n"
"v_exp_f32 v119,-v119\n"
"s_nop 0\n"
"v_mul_f32 v128,v128,v119\n"
"v_mul_f32 v129,v129,v119\n"
"v_mul_f32 v130,v130,v119\n"
"v_mul_f32 v131,v131,v119\n"
"v_mul_f32 v132,v132,v119\n"
"v_mul_f32 v133,v133,v119\n"
"v_mul_f32 v134,v134,v119\n"
"v_mul_f32 v135,v135,v119\n"
"v_mul_f32 v136,v136,v119\n"
"v_mul_f32 v137,v137,v119\n"
"v_mul_f32 v138,v138,v119\n"
"v_mul_f32 v139,v139,v119\n"
"v_mul_f32 v140,v140,v119\n"
"v_mul_f32 v141,v141,v119\n"
"v_mul_f32 v142,v142,v119\n"
"v_mul_f32 v143,v143,v119\n"
"v_mul_f32 v144,v144,v119\n"
"v_mul_f32 v145,v145,v119\n"
"v_mul_f32 v146,v146,v119\n"
"v_mul_f32 v147,v147,v119\n"
"v_mul_f32 v148,v148,v119\n"
"v_mul_f32 v149,v149,v119\n"
"v_mul_f32 v150,v150,v119\n"
"v_mul_f32 v151,v151,v119\n"
"v_mul_f32 v152,v152,v119\n"
"v_mul_f32 v153,v153,v119\n"
"v_mul_f32 v154,v154,v119\n"
"v_mul_f32 v155,v155,v119\n"
"v_mul_f32 v156,v156,v119\n"
"v_mul_f32 v157,v157,v119\n"
"v_mul_f32 v158,v158,v119\n"
"v_mul_f32 v159,v159,v119\n"
"v_accvgpr_read_b32 v120,a128\n"
"v_mul_f32 v120,v120,v119\n"
"v_accvgpr_write_b32 a128,v120\n"
"v_lshlrev_b32 v120,16,v64\n"
"v_and_b32 v121,0xffff0000,v64\n"
"v_mul_f32 v120,v120,v119\n"
"v_mul_f32 v121,v121,v119\n"
"v_cvt_pk_bf16_f32 v64,v120,v121\n"
"v_accvgpr_read_b32 v120,a129\n"
"v_mul_f32 v120,v120,v119\n"
"v_accvgpr_write_b32 a129,v120\n"
"v_lshlrev_b32 v120,16,v65\n"
"v_and_b32 v121,0xffff0000,v65\n"
"v_mul_f32 v120,v120,v119\n"
"v_mul_f32 v121,v121,v119\n"
"v_cvt_pk_bf16_f32 v65,v120,v121\n"
"v_accvgpr_read_b32 v120,a130\n"
"v_mul_f32 v120,v120,v119\n"
"v_accvgpr_write_b32 a130,v120\n"
"v_lshlrev_b32 v120,16,v66\n"
"v_and_b32 v121,0xffff0000,v66\n"
"v_mul_f32 v120,v120,v119\n"
"v_mul_f32 v121,v121,v119\n"
"v_cvt_pk_bf16_f32 v66,v120,v121\n"
"v_accvgpr_read_b32 v120,a131\n"
"v_mul_f32 v120,v120,v119\n"
"v_accvgpr_write_b32 a131,v120\n"
"v_lshlrev_b32 v120,16,v67\n"
"v_and_b32 v121,0xffff0000,v67\n"
"v_mul_f32 v120,v120,v119\n"
"v_mul_f32 v121,v121,v119\n"
"v_cvt_pk_bf16_f32 v67,v120,v121\n"
".Lfk_rare1_0:\n"
"s_bitcmp1_b32 s91,1\n"
"s_cbranch_scc0 .Lfk_rare1_1\n"
"v_max3_f32 v119,v40,v41,v42\n"
"v_max3_f32 v120,v43,v44,v45\n"
"v_max3_f32 v119,v119,v46,v47\n"
"v_max_f32 v119,v119,v120\n"
"v_mov_b32 v120,v119\n"
"s_nop 1\n"
"v_permlane16_swap_b32 v120,v119\n"
"v_max_f32 v119,v119,v120\n"
"v_mov_b32 v120,v119\n"
"s_nop 1\n"
"v_permlane32_swap_b32 v120,v119\n"
"v_max_f32 v119,v119,v120\n"
"s_bitcmp1_b32 s90,1\n"
"s_cselect_b32 s82,0xff800000,0\n"
"v_add_f32 v119,0x41980000,v119\n"
"v_max_f32 v119,s82,v119\n"
"v_cmp_lt_f32 vcc,s17,|v119|\n"
"s_nop 0\n"
"s_cbranch_vccz .Lfk_rare1_1\n"
"v_ceil_f32 v119,v119\n"
"v_sub_f32 v40,v40,v119\n"
"v_sub_f32 v41,v41,v119\n"
"v_sub_f32 v42,v42,v119\n"
"v_sub_f32 v43,v43,v119\n"
"v_sub_f32 v44,v44,v119\n"
"v_sub_f32 v45,v45,v119\n"
"v_sub_f32 v46,v46,v119\n"
"v_sub_f32 v47,v47,v119\n"
"v_sub_f32 v100,v100,v119\n"
"v_sub_f32 v101,v101,v119\n"
"v_sub_f32 v102,v102,v119\n"
"v_sub_f32 v103,v103,v119\n"
"s_cbranch_scc1 .Lfk_rare1_1\n"
"v_exp_f32 v119,-v119\n"
"s_nop 0\n"
"v_mul_f32 v160,v160,v119\n"
"v_mul_f32 v161,v161,v119\n"
"v_mul_f32 v162,v162,v119\n"
"v_mul_f32 v163,v163,v119\n"
"v_mul_f32 v164,v164,v119\n"
"v_mul_f32 v165,v165,v119\n"
"v_mul_f32 v166,v166,v119\n"
"v_mul_f32 v167,v167,v119\n"
"v_mul_f32 v168,v168,v119\n"
"v_mul_f32 v169,v169,v119\n"
"v_mul_f32 v170,v170,v119\n"
"v_mul_f32 v171,v171,v119\n"
"v_mul_f32 v172,v172,v119\n"
"v_mul_f32 v173,v173,v119\n"
"v_mul_f32 v174,v174,v119\n"
"v_mul_f32 v175,v175,v119\n"
"v_mul_f32 v176,v176,v119\n"
"v_mul_f32 v177,v177,v119\n"
"v_mul_f32 v178,v178,v119\n"
"v_mul_f32 v179,v179,v119\n"
"v_mul_f32 v180,v180,v119\n"
"v_mul_f32 v181,v181,v119\n"
"v_mul_f32 v182,v182,v119\n"
"v_mul_f32 v183,v183,v119\n"
"v_mul_f32 v184,v184,v119\n"
"v_mul_f32 v185,v185,v119\n"
"v_mul_f32 v186,v186,v119\n"
"v_mul_f32 v187,v187,v119\n"
"v_mul_f32 v188,v188,v119\n"
"v_mul_f32 v189,v189,v119\n"
"v_mul_f32 v190,v190,v119\n"
"v_mul_f32 v191,v191,v119\n"
"v_accvgpr_read_b32 v120,a132\n"
"v_mul_f32 v120,v120,v119\n"
"v_accvgpr_write_b32 a132,v120\n"
"v_lshlrev_b32 v120,16,v68\n"
"v_and_b32 v121,0xffff0000,v68\n"
"v_mul_f32 v120,v120,v119\n"
"v_mul_f32 v121,v121,v119\n"
"v_cvt_pk_bf16_f32 v68,v120,v121\n"
"v_accvgpr_read_b32 v120,a133\n"
"v_mul_f32 v120,v120,v119\n"
"v_accvgpr_write_b32 a133,v120\n"
"v_lshlrev_b32 v120,16,v69\n"
"v_and_b32 v121,0xffff0000,v69\n"
"v_mul_f32 v120,v120,v119\n"
"v_mul_f32 v121,v121,v119\n"
"v_cvt_pk_bf16_f32 v69,v120,v121\n"
"v_accvgpr_read_b32 v120,a134\n"
"v_mul_f32 v120,v120,v119\n"
"v_accvgpr_write_b32 a134,v120\n"
"v_lshlrev_b32 v120,16,v70\n"
"v_and_b32 v121,0xffff0000,v70\n"
"v_mul_f32 v120,v120,v119\n"
"v_mul_f32 v121,v121,v119\n"
"v_cvt_pk_bf16_f32 v70,v120,v121\n"
"v_accvgpr_read_b32 v120,a135\n"
"v_mul_f32 v120,v120,v119\n"
"v_accvgpr_write_b32 a135,v120\n"
"v_lshlrev_b32 v120,16,v71\n"
"v_and_b32 v121,0xffff0000,v71\n"
"v_mul_f32 v120,v120,v119\n"
"v_mul_f32 v121,v121,v119\n"
"v_cvt_pk_bf16_f32 v71,v120,v121\n"
".Lfk_rare1_1:\n"
"s_bitcmp1_b32 s91,2\n"
"s_cbranch_scc0 .Lfk_rare1_2\n"
"v_max3_f32 v119,v48,v49,v50\n"
"v_max3_f32 v120,v51,v52,v53\n"
"v_max3_f32 v119,v119,v54,v55\n"
"v_max_f32 v119,v119,v120\n"
"v_mov_b32 v120,v119\n"
"s_nop 1\n"
"v_permlane16_swap_b32 v120,v119\n"
"v_max_f32 v119,v119,v120\n"
"v_mov_b32 v120,v119\n"
"s_nop 1\n"
"v_permlane32_swap_b32 v120,v119\n"
"v_max_f32 v119,v119,v120\n"
"s_bitcmp1_b32 s90,2\n"
"s_cselect_b32 s82,0xff800000,0\n"
"v_add_f32 v119,0x41980000,v119\n"
"v_max_f32 v119,s82,v119\n"
"v_cmp_lt_f32 vcc,s17,|v119|\n"
"s_nop 0\n"
"s_cbranch_vccz .Lfk_rare1_2\n"
"v_ceil_f32 v119,v119\n"
"v_sub_f32 v48,v48,v119\n"
"v_sub_f32 v49,v49,v119\n"
"v_sub_f32 v50,v50,v119\n"
"v_sub_f32 v51,v51,v119\n"
"v_sub_f32 v52,v52,v119\n"
"v_sub_f32 v53,v53,v119\n"
"v_sub_f32 v54,v54,v119\n"
"v_sub_f32 v55,v55,v119\n"
"v_sub_f32 v104,v104,v119\n"
"v_sub_f32 v105,v105,v119\n"
"v_sub_f32 v106,v106,v119\n"
"v_sub_f32 v107,v107,v119\n"
"s_cbranch_scc1 .Lfk_rare1_2\n"
"v_exp_f32 v119,-v119\n"
"s_nop 0\n"
"v_mul_f32 v192,v192,v119\n"
"v_mul_f32 v193,v193,v119\n"
"v_mul_f32 v194,v194,v119\n"
"v_mul_f32 v195,v195,v119\n"
"v_mul_f32 v196,v196,v119\n"
"v_mul_f32 v197,v197,v119\n"
"v_mul_f32 v198,v198,v119\n"
"v_mul_f32 v199,v199,v119\n"
"v_mul_f32 v200,v200,v119\n"
"v_mul_f32 v201,v201,v119\n"
"v_mul_f32 v202,v202,v119\n"
"v_mul_f32 v203,v203,v119\n"
"v_mul_f32 v204,v204,v119\n"
"v_mul_f32 v205,v205,v119\n"
"v_mul_f32 v206,v206,v119\n"
"v_mul_f32 v207,v207,v119\n"
"v_mul_f32 v208,v208,v119\n"
"v_mul_f32 v209,v209,v119\n"
"v_mul_f32 v210,v210,v119\n"
"v_mul_f32 v211,v211,v119\n"
"v_mul_f32 v212,v212,v119\n"
"v_mul_f32 v213,v213,v119\n"
"v_mul_f32 v214,v214,v119\n"
"v_mul_f32 v215,v215,v119\n"
"v_mul_f32 v216,v216,v119\n"
"v_mul_f32 v217,v217,v119\n"
"v_mul_f32 v218,v218,v119\n"
"v_mul_f32 v219,v219,v119\n"
"v_mul_f32 v220,v220,v119\n"
"v_mul_f32 v221,v221,v119\n"
"v_mul_f32 v222,v222,v119\n"
"v_mul_f32 v223,v223,v119\n"
"v_accvgpr_read_b32 v120,a136\n"
"v_mul_f32 v120,v120,v119\n"
"v_accvgpr_write_b32 a136,v120\n"
"v_lshlrev_b32 v120,16,v72\n"
"v_and_b32 v121,0xffff0000,v72\n"
"v_mul_f32 v120,v120,v119\n"
"v_mul_f32 v121,v121,v119\n"
"v_cvt_pk_bf16_f32 v72,v120,v121\n"
"v_accvgpr_read_b32 v120,a137\n"
"v_mul_f32 v120,v120,v119\n"
"v_accvgpr_write_b32 a137,v120\n"
"v_lshlrev_b32 v120,16,v73\n"
"v_and_b32 v121,0xffff0000,v73\n"
"v_mul_f32 v120,v120,v119\n"
"v_mul_f32 v121,v121,v119\n"
"v_cvt_pk_bf16_f32 v73,v120,v121\n"
"v_accvgpr_read_b32 v120,a138\n"
"v_mul_f32 v120,v120,v119\n"
"v_accvgpr_write_b32 a138,v120\n"
"v_lshlrev_b32 v120,16,v74\n"
"v_and_b32 v121,0xffff0000,v74\n"
"v_mul_f32 v120,v120,v119\n"
"v_mul_f32 v121,v121,v119\n"
"v_cvt_pk_bf16_f32 v74,v120,v121\n"
"v_accvgpr_read_b32 v120,a139\n"
"v_mul_f32 v120,v120,v119\n"
"v_accvgpr_write_b32 a139,v120\n"
"v_lshlrev_b32 v120,16,v75\n"
"v_and_b32 v121,0xffff0000,v75\n"
"v_mul_f32 v120,v120,v119\n"
"v_mul_f32 v121,v121,v119\n"
"v_cvt_pk_bf16_f32 v75,v120,v121\n"
".Lfk_rare1_2:\n"
"s_bitcmp1_b32 s91,3\n"
"s_cbranch_scc0 .Lfk_rare1_3\n"
"v_max3_f32 v119,v56,v57,v58\n"
"v_max3_f32 v120,v59,v60,v61\n"
"v_max3_f32 v119,v119,v62,v63\n"
"v_max_f32 v119,v119,v120\n"
"v_mov_b32 v120,v119\n"
"s_nop 1\n"
"v_permlane16_swap_b32 v120,v119\n"
"v_max_f32 v119,v119,v120\n"
"v_mov_b32 v120,v119\n"
"s_nop 1\n"
"v_permlane32_swap_b32 v120,v119\n"
"v_max_f32 v119,v119,v120\n"
"s_bitcmp1_b32 s90,3\n"
"s_cselect_b32 s82,0xff800000,0\n"
"v_add_f32 v119,0x41980000,v119\n"
"v_max_f32 v119,s82,v119\n"
"v_cmp_lt_f32 vcc,s17,|v119|\n"
"s_nop 0\n"
"s_cbranch_vccz .Lfk_rare1_3\n"
"v_ceil_f32 v119,v119\n"
"v_sub_f32 v56,v56,v119\n"
"v_sub_f32 v57,v57,v119\n"
"v_sub_f32 v58,v58,v119\n"
"v_sub_f32 v59,v59,v119\n"
"v_sub_f32 v60,v60,v119\n"
"v_sub_f32 v61,v61,v119\n"
"v_sub_f32 v62,v62,v119\n"
"v_sub_f32 v63,v63,v119\n"
"v_sub_f32 v108,v108,v119\n"
"v_sub_f32 v109,v109,v119\n"
"v_sub_f32 v110,v110,v119\n"
"v_sub_f32 v111,v111,v119\n"
"s_cbranch_scc1 .Lfk_rare1_3\n"
"v_exp_f32 v119,-v119\n"
"s_nop 0\n"
"v_mul_f32 v224,v224,v119\n"
"v_mul_f32 v225,v225,v119\n"
"v_mul_f32 v226,v226,v119\n"
"v_mul_f32 v227,v227,v119\n"
"v_mul_f32 v228,v228,v119\n"
"v_mul_f32 v229,v229,v119\n"
"v_mul_f32 v230,v230,v119\n"
"v_mul_f32 v231,v231,v119\n"
"v_mul_f32 v232,v232,v119\n"
"v_mul_f32 v233,v233,v119\n"
"v_mul_f32 v234,v234,v119\n"
"v_mul_f32 v235,v235,v119\n"
"v_mul_f32 v236,v236,v119\n"
"v_mul_f32 v237,v237,v119\n"
"v_mul_f32 v238,v238,v119\n"
"v_mul_f32 v239,v239,v119\n"
"v_mul_f32 v240,v240,v119\n"
"v_mul_f32 v241,v241,v119\n"
"v_mul_f32 v242,v242,v119\n"
"v_mul_f32 v243,v243,v119\n"
"v_mul_f32 v244,v244,v119\n"
"v_mul_f32 v245,v245,v119\n"
"v_mul_f32 v246,v246,v119\n"
"v_mul_f32 v247,v247,v119\n"
"v_mul_f32 v248,v248,v119\n"
"v_mul_f32 v249,v249,v119\n"
"v_mul_f32 v250,v250,v119\n"
"v_mul_f32 v251,v251,v119\n"
"v_mul_f32 v252,v252,v119\n"
"v_mul_f32 v253,v253,v119\n"
"v_mul_f32 v254,v254,v119\n"
"v_mul_f32 v255,v255,v119\n"
"v_accvgpr_read_b32 v120,a140\n"
"v_mul_f32 v120,v120,v119\n"
"v_accvgpr_write_b32 a140,v120\n"
"v_lshlrev_b32 v120,16,v76\n"
"v_and_b32 v121,0xffff0000,v76\n"
"v_mul_f32 v120,v120,v119\n"
"v_mul_f32 v121,v121,v119\n"
"v_cvt_pk_bf16_f32 v76,v120,v121\n"
"v_accvgpr_read_b32 v120,a141\n"
"v_mul_f32 v120,v120,v119\n"
"v_accvgpr_write_b32 a141,v120\n"
"v_lshlrev_b32 v120,16,v77\n"
"v_and_b32 v121,0xffff0000,v77\n"
"v_mul_f32 v120,v120,v119\n"
"v_mul_f32 v121,v121,v119\n"
"v_cvt_pk_bf16_f32 v77,v120,v121\n"
"v_accvgpr_read_b32 v120,a142\n"
"v_mul_f32 v120,v120,v119\n"
"v_accvgpr_write_b32 a142,v120\n"
"v_lshlrev_b32 v120,16,v78\n"
"v_and_b32 v121,0xffff0000,v78\n"
"v_mul_f32 v120,v120,v119\n"
"v_mul_f32 v121,v121,v119\n"
"v_cvt_pk_bf16_f32 v78,v120,v121\n"
"v_accvgpr_read_b32 v120,a143\n"
"v_mul_f32 v120,v120,v119\n"
"v_accvgpr_write_b32 a143,v120\n"
"v_lshlrev_b32 v120,16,v79\n"
"v_and_b32 v121,0xffff0000,v79\n"
"v_mul_f32 v120,v120,v119\n"
"v_mul_f32 v121,v121,v119\n"
"v_cvt_pk_bf16_f32 v79,v120,v121\n"
".Lfk_rare1_3:\n"
"s_mov_b32 s90,0\n"
"s_nop 1\n"
"s_setpc_b64 s[94:95]\n"
".Lfk_rare3:\n"
"s_nop 7\n"
"v_max3_f32 v119,v32,v33,v34\n"
"v_max3_f32 v120,v35,v36,v37\n"
"v_max3_f32 v119,v119,v38,v39\n"
"v_max_f32 v119,v119,v120\n"
"v_mov_b32 v120,v119\n"
"s_nop 1\n"
"v_permlane16_swap_b32 v120,v119\n"
"v_max_f32 v119,v119,v120\n"
"v_mov_b32 v120,v119\n"
"s_nop 1\n"
"v_permlane32_swap_b32 v120,v119\n"
"v_max_f32 v119,v119,v120\n"
"s_bitcmp1_b32 s90,0\n"
"s_cselect_b32 s82,0xff800000,0\n"
"v_add_f32 v119,0x41980000,v119\n"
"v_max_f32 v119,s82,v119\n"
"v_cmp_lt_f32 vcc,s17,|v119|\n"
"s_nop 0\n"
"s_cbranch_vccz .Lfk_rare3_0\n"
"v_ceil_f32 v119,v119\n"
"v_sub_f32 v32,v32,v119\n"
"v_sub_f32 v33,v33,v119\n"
"v_sub_f32 v34,v34,v119\n"
"v_sub_f32 v35,v35,v119\n"
"v_sub_f32 v36,v36,v119\n"
"v_sub_f32 v37,v37,v119\n"
"v_sub_f32 v38,v38,v119\n"
"v_sub_f32 v39,v39,v119\n"
"v_sub_f32 v96,v96,v119\n"
"v_sub_f32 v97,v97,v119\n"
"v_sub_f32 v98,v98,v119\n"
"v_sub_f32 v99,v99,v119\n"
"v_sub_f32 v0,v0,v119\n"
"v_sub_f32 v1,v1,v119\n"
"v_sub_f32 v2,v2,v119\n"
"v_sub_f32 v3,v3,v119\n"
"v_sub_f32 v4,v4,v119\n"
"v_sub_f32 v5,v5,v119\n"
"v_sub_f32 v6,v6,v119\n"
"v_sub_f32 v7,v7,v119\n"
"v_exp_f32 v32,v32\n"
"v_exp_f32 v33,v33\n"
"v_exp_f32 v34,v34\n"
"v_exp_f32 v35,v35\n"
"v_exp_f32 v36,v36\n"
"v_exp_f32 v37,v37\n"
"v_exp_f32 v38,v38\n"
"v_exp_f32 v39,v39\n"
"v_cvt_pk_bf16_f32 v80,v32,v33\n"
"v_cvt_pk_bf16_f32 v81,v34,v35\n"
"v_cvt_pk_bf16_f32 v82,v36,v37\n"
"v_cvt_pk_bf16_f32 v83,v38,v39\n"
"s_cbranch_scc1 .Lfk_rare3_0\n"
"v_exp_f32 v119,-v119\n"
"s_nop 0\n"
"v_mul_f32 v128,v128,v119\n"
"v_mul_f32 v129,v129,v119\n"
"v_mul_f32 v130,v130,v119\n"
"v_mul_f32 v131,v131,v119\n"
"v_mul_f32 v132,v132,v119\n"
"v_mul_f32 v133,v133,v119\n"
"v_mul_f32 v134,v134,v119\n"
"v_mul_f32 v135,v135,v119\n"
"v_mul_f32 v136,v136,v119\n"
"v_mul_f32 v137,v137,v119\n"
"v_mul_f32 v138,v138,v119\n"
"v_mul_f32 v139,v139,v119\n"
"v_mul_f32 v140,v140,v119\n"
"v_mul_f32 v141,v141,v119\n"
"v_mul_f32 v142,v142,v119\n"
"v_mul_f32 v143,v143,v119\n"
"v_mul_f32 v144,v144,v119\n"
"v_mul_f32 v145,v145,v119\n"
"v_mul_f32 v146,v146,v119\n"
"v_mul_f32 v147,v147,v119\n"
"v_mul_f32 v148,v148,v119\n"
"v_mul_f32 v149,v149,v119\n"
"v_mul_f32 v150,v150,v119\n"
"v_mul_f32 v151,v151,v119\n"
"v_mul_f32 v152,v152,v119\n"
"v_mul_f32 v153,v153,v119\n"
"v_mul_f32 v154,v154,v119\n"
"v_mul_f32 v155,v155,v119\n"
"v_mul_f32 v156,v156,v119\n"
"v_mul_f32 v157,v157,v119\n"
"v_mul_f32 v158,v158,v119\n"
"v_mul_f32 v159,v159,v119\n"
"v_accvgpr_read_b32 v120,a128\n"
"v_mul_f32 v120,v120,v119\n"
"v_accvgpr_write_b32 a128,v120\n"
"v_accvgpr_read_b32 v120,a129\n"
"v_mul_f32 v120,v120,v119\n"
"v_accvgpr_write_b32 a129,v120\n"
"v_accvgpr_read_b32 v120,a130\n"
"v_mul_f32 v120,v120,v119\n"
"v_accvgpr_write_b32 a130,v120\n"
"v_accvgpr_read_b32 v120,a131\n"
"v_mul_f32 v120,v120,v119\n"
"v_accvgpr_write_b32 a131,v120\n"
".Lfk_rare3_0:\n"
"v_max3_f32 v119,v40,v41,v42\n"
"v_max3_f32 v120,v43,v44,v45\n"
"v_max3_f32 v119,v119,v46,v47\n"
"v_max_f32 v119,v119,v120\n"
"v_mov_b32 v120,v119\n"
"s_nop 1\n"
"v_permlane16_swap_b32 v120,v119\n"
"v_max_f32 v119,v119,v120\n"
"v_mov_b32 v120,v119\n"
"s_nop 1\n"
"v_permlane32_swap_b32 v120,v119\n"
"v_max_f32 v119,v119,v120\n"
"s_bitcmp1_b32 s90,1\n"
"s_cselect_b32 s82,0xff800000,0\n"
"v_add_f32 v119,0x41980000,v119\n"
"v_max_f32 v119,s82,v119\n"
"v_cmp_lt_f32 vcc,s17,|v119|\n"
"s_nop 0\n"
"s_cbranch_vccz .Lfk_rare3_1\n"
"v_ceil_f32 v119,v119\n"
"v_sub_f32 v40,v40,v119\n"
"v_sub_f32 v41,v41,v119\n"
"v_sub_f32 v42,v42,v119\n"
"v_sub_f32 v43,v43,v119\n"
"v_sub_f32 v44,v44,v119\n"
"v_sub_f32 v45,v45,v119\n"
"v_sub_f32 v46,v46,v119\n"
"v_sub_f32 v47,v47,v119\n"
"v_sub_f32 v100,v100,v119\n"
"v_sub_f32 v101,v101,v119\n"
"v_sub_f32 v102,v102,v119\n"
"v_sub_f32 v103,v103,v119\n"
"v_sub_f32 v8,v8,v119\n"
"v_sub_f32 v9,v9,v119\n"
"v_sub_f32 v10,v10,v119\n"
"v_sub_f32 v11,v11,v119\n"
"v_sub_f32 v12,v12,v119\n"
"v_sub_f32 v13,v13,v119\n"
"v_sub_f32 v14,v14,v119\n"
"v_sub_f32 v15,v15,v119\n"
"v_exp_f32 v40,v40\n"
"v_exp_f32 v41,v41\n"
"v_exp_f32 v42,v42\n"
"v_exp_f32 v43,v43\n"
"v_exp_f32 v44,v44\n"
"v_exp_f32 v45,v45\n"
"v_exp_f32 v46,v46\n"
"v_exp_f32 v47,v47\n"
"v_cvt_pk_bf16_f32 v84,v40,v41\n"
"v_cvt_pk_bf16_f32 v85,v42,v43\n"
"v_cvt_pk_bf16_f32 v86,v44,v45\n"
"v_cvt_pk_bf16_f32 v87,v46,v47\n"
"s_cbranch_scc1 .Lfk_rare3_1\n"
"v_exp_f32 v119,-v119\n"
"s_nop 0\n"
"v_mul_f32 v160,v160,v119\n"
"v_mul_f32 v161,v161,v119\n"
"v_mul_f32 v162,v162,v119\n"
"v_mul_f32 v163,v163,v119\n"
"v_mul_f32 v164,v164,v119\n"
"v_mul_f32 v165,v165,v119\n"
"v_mul_f32 v166,v166,v119\n"
"v_mul_f32 v167,v167,v119\n"
"v_mul_f32 v168,v168,v119\n"
"v_mul_f32 v169,v169,v119\n"
"v_mul_f32 v170,v170,v119\n"
"v_mul_f32 v171,v171,v119\n"
"v_mul_f32 v172,v172,v119\n"
"v_mul_f32 v173,v173,v119\n"
"v_mul_f32 v174,v174,v119\n"
"v_mul_f32 v175,v175,v119\n"
"v_mul_f32 v176,v176,v119\n"
"v_mul_f32 v177,v177,v119\n"
"v_mul_f32 v178,v178,v119\n"
"v_mul_f32 v179,v179,v119\n"
"v_mul_f32 v180,v180,v119\n"
"v_mul_f32 v181,v181,v119\n"
"v_mul_f32 v182,v182,v119\n"
"v_mul_f32 v183,v183,v119\n"
"v_mul_f32 v184,v184,v119\n"
"v_mul_f32 v185,v185,v119\n"
"v_mul_f32 v186,v186,v119\n"
"v_mul_f32 v187,v187,v119\n"
"v_mul_f32 v188,v188,v119\n"
"v_mul_f32 v189,v189,v119\n"
"v_mul_f32 v190,v190,v119\n"
"v_mul_f32 v191,v191,v119\n"
"v_accvgpr_read_b32 v120,a132\n"
"v_mul_f32 v120,v120,v119\n"
"v_accvgpr_write_b32 a132,v120\n"
"v_accvgpr_read_b32 v120,a133\n"
"v_mul_f32 v120,v120,v119\n"
"v_accvgpr_write_b32 a133,v120\n"
"v_accvgpr_read_b32 v120,a134\n"
"v_mul_f32 v120,v120,v119\n"
"v_accvgpr_write_b32 a134,v120\n"
"v_accvgpr_read_b32 v120,a135\n"
"v_mul_f32 v120,v120,v119\n"
"v_accvgpr_write_b32 a135,v120\n"
".Lfk_rare3_1:\n"
"v_max3_f32 v119,v48,v49,v50\n"
"v_max3_f32 v120,v51,v52,v53\n"
"v_max3_f32 v119,v119,v54,v55\n"
"v_max_f32 v119,v119,v120\n"
"v_mov_b32 v120,v119\n"
"s_nop 1\n"
"v_permlane16_swap_b32 v120,v119\n"
"v_max_f32 v119,v119,v120\n"
"v_mov_b32 v120,v119\n"
"s_nop 1\n"
"v_permlane32_swap_b32 v120,v119\n"
"v_max_f32 v119,v119,v120\n"
"s_bitcmp1_b32 s90,2\n"
"s_cselect_b32 s82,0xff800000,0\n"
"v_add_f32 v119,0x41980000,v119\n"
"v_max_f32 v119,s82,v119\n"
"v_cmp_lt_f32 vcc,s17,|v119|\n"
"s_nop 0\n"
"s_cbranch_vccz .Lfk_rare3_2\n"
"v_ceil_f32 v119,v119\n"
"v_sub_f32 v48,v48,v119\n"
"v_sub_f32 v49,v49,v119\n"
"v_sub_f32 v50,v50,v119\n"
"v_sub_f32 v51,v51,v119\n"
"v_sub_f32 v52,v52,v119\n"
"v_sub_f32 v53,v53,v119\n"
"v_sub_f32 v54,v54,v119\n"
"v_sub_f32 v55,v55,v119\n"
"v_sub_f32 v104,v104,v119\n"
"v_sub_f32 v105,v105,v119\n"
"v_sub_f32 v106,v106,v119\n"
"v_sub_f32 v107,v107,v119\n"
"v_sub_f32 v16,v16,v119\n"
"v_sub_f32 v17,v17,v119\n"
"v_sub_f32 v18,v18,v119\n"
"v_sub_f32 v19,v19,v119\n"
"v_sub_f32 v20,v20,v119\n"
"v_sub_f32 v21,v21,v119\n"
"v_sub_f32 v22,v22,v119\n"
"v_sub_f32 v23,v23,v119\n"
"v_exp_f32 v48,v48\n"
"v_exp_f32 v49,v49\n"
"v_exp_f32 v50,v50\n"
"v_exp_f32 v51,v51\n"
"v_exp_f32 v52,v52\n"
"v_exp_f32 v53,v53\n"
"v_exp_f32 v54,v54\n"
"v_exp_f32 v55,v55\n"
"v_cvt_pk_bf16_f32 v88,v48,v49\n"
"v_cvt_pk_bf16_f32 v89,v50,v51\n"
"v_cvt_pk_bf16_f32 v90,v52,v53\n"
"v_cvt_pk_bf16_f32 v91,v54,v55\n"
"s_cbranch_scc1 .Lfk_rare3_2\n"
"v_exp_f32 v119,-v119\n"
"s_nop 0\n"
"v_mul_f32 v192,v192,v119\n"
"v_mul_f32 v193,v193,v119\n"
"v_mul_f32 v194,v194,v119\n"
"v_mul_f32 v195,v195,v119\n"
"v_mul_f32 v196,v196,v119\n"
"v_mul_f32 v197,v197,v119\n"
"v_mul_f32 v198,v198,v119\n"
"v_mul_f32 v199,v199,v119\n"
"v_mul_f32 v200,v200,v119\n"
"v_mul_f32 v201,v201,v119\n"
"v_mul_f32 v202,v202,v119\n"
"v_mul_f32 v203,v203,v119\n"
"v_mul_f32 v204,v204,v119\n"
"v_mul_f32 v205,v205,v119\n"
"v_mul_f32 v206,v206,v119\n"
"v_mul_f32 v207,v207,v119\n"
"v_mul_f32 v208,v208,v119\n"
"v_mul_f32 v209,v209,v119\n"
"v_mul_f32 v210,v210,v119\n"
"v_mul_f32 v211,v211,v119\n"
"v_mul_f32 v212,v212,v119\n"
"v_mul_f32 v213,v213,v119\n"
"v_mul_f32 v214,v214,v119\n"
"v_mul_f32 v215,v215,v119\n"
"v_mul_f32 v216,v216,v119\n"
"v_mul_f32 v217,v217,v119\n"
"v_mul_f32 v218,v218,v119\n"
"v_mul_f32 v219,v219,v119\n"
"v_mul_f32 v220,v220,v119\n"
"v_mul_f32 v221,v221,v119\n"
"v_mul_f32 v222,v222,v119\n"
"v_mul_f32 v223,v223,v119\n"
"v_accvgpr_read_b32 v120,a136\n"
"v_mul_f32 v120,v120,v119\n"
"v_accvgpr_write_b32 a136,v120\n"
"v_accvgpr_read_b32 v120,a137\n"
"v_mul_f32 v120,v120,v119\n"
"v_accvgpr_write_b32 a137,v120\n"
"v_accvgpr_read_b32 v120,a138\n"
"v_mul_f32 v120,v120,v119\n"
"v_accvgpr_write_b32 a138,v120\n"
"v_accvgpr_read_b32 v120,a139\n"
"v_mul_f32 v120,v120,v119\n"
"v_accvgpr_write_b32 a139,v120\n"
".Lfk_rare3_2:\n"
"v_max3_f32 v119,v56,v57,v58\n"
"v_max3_f32 v120,v59,v60,v61\n"
"v_max3_f32 v119,v119,v62,v63\n"
"v_max_f32 v119,v119,v120\n"
"v_mov_b32 v120,v119\n"
"s_nop 1\n"
"v_permlane16_swap_b32 v120,v119\n"
"v_max_f32 v119,v119,v120\n"
"v_mov_b32 v120,v119\n"
"s_nop 1\n"
"v_permlane32_swap_b32 v120,v119\n"
"v_max_f32 v119,v119,v120\n"
"s_bitcmp1_b32 s90,3\n"
"s_cselect_b32 s82,0xff800000,0\n"
"v_add_f32 v119,0x41980000,v119\n"
"v_max_f32 v119,s82,v119\n"
"v_cmp_lt_f32 vcc,s17,|v119|\n"
"s_nop 0\n"
"s_cbranch_vccz .Lfk_rare3_3\n"
"v_ceil_f32 v119,v119\n"
"v_sub_f32 v56,v56,v119\n"
"v_sub_f32 v57,v57,v119\n"
"v_sub_f32 v58,v58,v119\n"
"v_sub_f32 v59,v59,v119\n"
"v_sub_f32 v60,v60,v119\n"
"v_sub_f32 v61,v61,v119\n"
"v_sub_f32 v62,v62,v119\n"
"v_sub_f32 v63,v63,v119\n"
"v_sub_f32 v108,v108,v119\n"
"v_sub_f32 v109,v109,v119\n"
"v_sub_f32 v110,v110,v119\n"
"v_sub_f32 v111,v111,v119\n"
"v_sub_f32 v24,v24,v119\n"
"v_sub_f32 v25,v25,v119\n"
"v_sub_f32 v26,v26,v119\n"
"v_sub_f32 v27,v27,v119\n"
"v_sub_f32 v28,v28,v119\n"
"v_sub_f32 v29,v29,v119\n"
"v_sub_f32 v30,v30,v119\n"
"v_sub_f32 v31,v31,v119\n"
"v_exp_f32 v56,v56\n"
"v_exp_f32 v57,v57\n"
"v_exp_f32 v58,v58\n"
"v_exp_f32 v59,v59\n"
"v_exp_f32 v60,v60\n"
"v_exp_f32 v61,v61\n"
"v_exp_f32 v62,v62\n"
"v_exp_f32 v63,v63\n"
"v_cvt_pk_bf16_f32 v92,v56,v57\n"
"v_cvt_pk_bf16_f32 v93,v58,v59\n"
"v_cvt_pk_bf16_f32 v94,v60,v61\n"
"v_cvt_pk_bf16_f32 v95,v62,v63\n"
"s_cbranch_scc1 .Lfk_rare3_3\n"
"v_exp_f32 v119,-v119\n"
"s_nop 0\n"
"v_mul_f32 v224,v224,v119\n"
"v_mul_f32 v225,v225,v119\n"
"v_mul_f32 v226,v226,v119\n"
"v_mul_f32 v227,v227,v119\n"
"v_mul_f32 v228,v228,v119\n"
"v_mul_f32 v229,v229,v119\n"
"v_mul_f32 v230,v230,v119\n"
"v_mul_f32 v231,v231,v119\n"
"v_mul_f32 v232,v232,v119\n"
"v_mul_f32 v233,v233,v119\n"
"v_mul_f32 v234,v234,v119\n"
"v_mul_f32 v235,v235,v119\n"
"v_mul_f32 v236,v236,v119\n"
"v_mul_f32 v237,v237,v119\n"
"v_mul_f32 v238,v238,v119\n"
"v_mul_f32 v239,v239,v119\n"
"v_mul_f32 v240,v240,v119\n"
"v_mul_f32 v241,v241,v119\n"
"v_mul_f32 v242,v242,v119\n"
"v_mul_f32 v243,v243,v119\n"
"v_mul_f32 v244,v244,v119\n"
"v_mul_f32 v245,v245,v119\n"
"v_mul_f32 v246,v246,v119\n"
"v_mul_f32 v247,v247,v119\n"
"v_mul_f32 v248,v248,v119\n"
"v_mul_f32 v249,v249,v119\n"
"v_mul_f32 v250,v250,v119\n"
"v_mul_f32 v251,v251,v119\n"
"v_mul_f32 v252,v252,v119\n"
"v_mul_f32 v253,v253,v119\n"
"v_mul_f32 v254,v254,v119\n"
"v_mul_f32 v255,v255,v119\n"
"v_accvgpr_read_b32 v120,a140\n"
"v_mul_f32 v120,v120,v119\n"
"v_accvgpr_write_b32 a140,v120\n"
"v_accvgpr_read_b32 v120,a141\n"
"v_mul_f32 v120,v120,v119\n"
"v_accvgpr_write_b32 a141,v120\n"
"v_accvgpr_read_b32 v120,a142\n"
"v_mul_f32 v120,v120,v119\n"
"v_accvgpr_write_b32 a142,v120\n"
"v_accvgpr_read_b32 v120,a143\n"
"v_mul_f32 v120,v120,v119\n"
"v_accvgpr_write_b32 a143,v120\n"
".Lfk_rare3_3:\n"
"s_mov_b32 s90,0\n"
"s_nop 1\n"
"s_setpc_b64 s[94:95]\n"
".Lfk_mslow1:\n"
"s_nop 7\n"
"v_min_f32 v119,v102,v108\n"
"v_sub_f32 v120,v119,v108\n"
"v_sub_f32 v119,v119,v102\n"
"v_exp_f32 v120,v120\n"
"v_exp_f32 v119,v119\n"
"v_mul_f32 v224,v224,v120\n"
"v_mul_f32 v225,v225,v120\n"
"v_mul_f32 v226,v226,v120\n"
"v_mul_f32 v227,v227,v120\n"
"v_mul_f32 v228,v228,v120\n"
"v_mul_f32 v229,v229,v120\n"
"v_mul_f32 v230,v230,v120\n"
"v_mul_f32 v231,v231,v120\n"
"v_mul_f32 v232,v232,v120\n"
"v_mul_f32 v233,v233,v120\n"
"v_mul_f32 v234,v234,v120\n"
"v_mul_f32 v235,v235,v120\n"
"v_mul_f32 v236,v236,v120\n"
"v_mul_f32 v237,v237,v120\n"
"v_mul_f32 v238,v238,v120\n"
"v_mul_f32 v239,v239,v120\n"
"v_mul_f32 v240,v240,v120\n"
"v_mul_f32 v241,v241,v120\n"
"v_mul_f32 v242,v242,v120\n"
"v_mul_f32 v243,v243,v120\n"
"v_mul_f32 v244,v244,v120\n"
"v_mul_f32 v245,v245,v120\n"
"v_mul_f32 v246,v246,v120\n"
"v_mul_f32 v247,v247,v120\n"
"v_mul_f32 v248,v248,v120\n"
"v_mul_f32 v249,v249,v120\n"
"v_mul_f32 v250,v250,v120\n"
"v_mul_f32 v251,v251,v120\n"
"v_mul_f32 v252,v252,v120\n"
"v_mul_f32 v253,v253,v120\n"
"v_mul_f32 v254,v254,v120\n"
"v_mul_f32 v255,v255,v120\n"
"v_accvgpr_read_b32 v121,a140\n"
"v_mul_f32 v121,v121,v120\n"
"v_accvgpr_write_b32 a140,v121\n"
"v_mul_f32 v103,v103,v119\n"
"v_accvgpr_read_b32 v121,a80\n"
"v_lshlrev_b32 v120,16,v121\n"
"v_and_b32 v121,0xffff0000,v121\n"
"v_mul_f32 v120,v120,v119\n"
"v_mul_f32 v121,v121,v119\n"
"v_cvt_pk_bf16_f32 v121,v120,v121\n"
"v_accvgpr_write_b32 a80,v121\n"
"v_accvgpr_read_b32 v121,a81\n"
"v_lshlrev_b32 v120,16,v121\n"
"v_and_b32 v121,0xffff0000,v121\n"
"v_mul_f32 v120,v120,v119\n"
"v_mul_f32 v121,v121,v119\n"
"v_cvt_pk_bf16_f32 v121,v120,v121\n"
"v_accvgpr_write_b32 a81,v121\n"
"v_accvgpr_read_b32 v121,a82\n"
"v_lshlrev_b32 v120,16,v121\n"
"v_and_b32 v121,0xffff0000,v121\n"
"v_mul_f32 v120,v120,v119\n"
"v_mul_f32 v121,v121,v119\n"
"v_cvt_pk_bf16_f32 v121,v120,v121\n"
"v_accvgpr_write_b32 a82,v121\n"
"v_accvgpr_read_b32 v121,a83\n"
"v_lshlrev_b32 v120,16,v121\n"
"v_and_b32 v121,0xffff0000,v121\n"
"v_mul_f32 v120,v120,v119\n"
"v_mul_f32 v121,v121,v119\n"
"v_cvt_pk_bf16_f32 v121,v120,v121\n"
"v_accvgpr_write_b32 a83,v121\n"
"v_accvgpr_read_b32 v121,a84\n"
"v_lshlrev_b32 v120,16,v121\n"
"v_and_b32 v121,0xffff0000,v121\n"
"v_mul_f32 v120,v120,v119\n"
"v_mul_f32 v121,v121,v119\n"
"v_cvt_pk_bf16_f32 v121,v120,v121\n"
"v_accvgpr_write_b32 a84,v121\n"
"v_accvgpr_read_b32 v121,a85\n"
"v_lshlrev_b32 v120,16,v121\n"
"v_and_b32 v121,0xffff0000,v121\n"
"v_mul_f32 v120,v120,v119\n"
"v_mul_f32 v121,v121,v119\n"
"v_cvt_pk_bf16_f32 v121,v120,v121\n"
"v_accvgpr_write_b32 a85,v121\n"
"v_accvgpr_read_b32 v121,a86\n"
"v_lshlrev_b32 v120,16,v121\n"
"v_and_b32 v121,0xffff0000,v121\n"
"v_mul_f32 v120,v120,v119\n"
"v_mul_f32 v121,v121,v119\n"
"v_cvt_pk_bf16_f32 v121,v120,v121\n"
"v_accvgpr_write_b32 a86,v121\n"
"v_accvgpr_read_b32 v121,a87\n"
"v_lshlrev_b32 v120,16,v121\n"
"v_and_b32 v121,0xffff0000,v121\n"
"v_mul_f32 v120,v120,v119\n"
"v_mul_f32 v121,v121,v119\n"
"v_cvt_pk_bf16_f32 v121,v120,v121\n"
"v_accvgpr_write_b32 a87,v121\n"
"v_accvgpr_read_b32 v121,a88\n"
"v_lshlrev_b32 v120,16,v121\n"
"v_and_b32 v121,0xffff0000,v121\n"
"v_mul_f32 v120,v120,v119\n"
"v_mul_f32 v121,v121,v119\n"
"v_cvt_pk_bf16_f32 v121,v120,v121\n"
"v_accvgpr_write_b32 a88,v121\n"
"v_accvgpr_read_b32 v121,a89\n"
"v_lshlrev_b32 v120,16,v121\n"
"v_and_b32 v121,0xffff0000,v121\n"
"v_mul_f32 v120,v120,v119\n"
"v_mul_f32 v121,v121,v119\n"
"v_cvt_pk_bf16_f32 v121,v120,v121\n"
"v_accvgpr_write_b32 a89,v121\n"
"v_accvgpr_read_b32 v121,a90\n"
"v_lshlrev_b32 v120,16,v121\n"
"v_and_b32 v121,0xffff0000,v121\n"
"v_mul_f32 v120,v120,v119\n"
"v_mul_f32 v121,v121,v119\n"
"v_cvt_pk_bf16_f32 v121,v120,v121\n"
"v_accvgpr_write_b32 a90,v121\n"
"v_accvgpr_read_b32 v121,a91\n"
"v_lshlrev_b32 v120,16,v121\n"
"v_and_b32 v121,0xffff0000,v121\n"
"v_mul_f32 v120,v120,v119\n"
"v_mul_f32 v121,v121,v119\n"
"v_cvt_pk_bf16_f32 v121,v120,v121\n"
"v_accvgpr_write_b32 a91,v121\n"
"v_accvgpr_read_b32 v121,a92\n"
"v_lshlrev_b32 v120,16,v121\n"
"v_and_b32 v121,0xffff0000,v121\n"
"v_mul_f32 v120,v120,v119\n"
"v_mul_f32 v121,v121,v119\n"
"v_cvt_pk_bf16_f32 v121,v120,v121\n"
"v_accvgpr_write_b32 a92,v121\n"
"v_accvgpr_read_b32 v121,a93\n"
"v_lshlrev_b32 v120,16,v121\n"
"v_and_b32 v121,0xffff0000,v121\n"
"v_mul_f32 v120,v120,v119\n"
"v_mul_f32 v121,v121,v119\n"
"v_cvt_pk_bf16_f32 v121,v120,v121\n"
"v_accvgpr_write_b32 a93,v121\n"
"v_accvgpr_read_b32 v121,a94\n"
"v_lshlrev_b32 v120,16,v121\n"
"v_and_b32 v121,0xffff0000,v121\n"
"v_mul_f32 v120,v120,v119\n"
"v_mul_f32 v121,v121,v119\n"
"v_cvt_pk_bf16_f32 v121,v120,v121\n"
"v_accvgpr_write_b32 a94,v121\n"
"v_accvgpr_read_b32 v121,a95\n"
"v_lshlrev_b32 v120,16,v121\n"
"v_and_b32 v121,0xffff0000,v121\n"
"v_mul_f32 v120,v120,v119\n"
"v_mul_f32 v121,v121,v119\n"
"v_cvt_pk_bf16_f32 v121,v120,v121\n"
"v_accvgpr_write_b32 a95,v121\n"
"s_nop 1\n"
"s_setpc_b64 s[94:95]\n"
".Lfk_end:\n"
        : "+{v0}"(tid), "+{s2}"(bid)
        : "{s[0:1]}"(__builtin_amdgcn_kernarg_segment_ptr())
        : "v1","v2","v3","v4","v5","v6","v7","v8","v9","v10","v11","v12","v13","v14","v15","v16","v17","v18","v19","v20","v21","v22","v23","v24","v25","v26","v27","v28","v29","v30","v31","v32","v33","v34","v35","v36","v37","v38","v39","v40","v41","v42","v43","v44","v45","v46","v47","v48","v49","v50","v51","v52","v53","v54","v55","v56","v57","v58","v59","v60","v61","v62","v63","v64","v65","v66","v67","v68","v69","v70","v71","v72","v73","v74","v75","v76","v77","v78","v79","v80","v81","v82","v83","v84","v85","v86","v87","v88","v89","v90","v91","v92","v93","v94","v95","v96","v97","v98","v99","v100","v101","v102","v103","v104","v105","v106","v107","v108","v109","v110","v111","v112","v113","v114","v115","v116","v117","v118","v119","v120","v121","v122","v123","v124","v125","v126","v127","v128","v129","v130","v131","v132","v133","v134","v135","v136","v137","v138","v139","v140","v141","v142","v143","v144","v145","v146","v147","v148","v149","v150","v151","v152","v153","v154","v155","v156","v157","v158","v159","v160","v161","v162","v163","v164","v165","v166","v167","v168","v169","v170","v171","v172","v173","v174","v175","v176","v177","v178","v179","v180","v181","v182","v183","v184","v185","v186","v187","v188","v189","v190","v191","v192","v193","v194","v195","v196","v197","v198","v199","v200","v201","v202","v203","v204","v205","v206","v207","v208","v209","v210","v211","v212","v213","v214","v215","v216","v217","v218","v219","v220","v221","v222","v223","v224","v225","v226","v227","v228","v229","v230","v231","v232","v233","v234","v235","v236","v237","v238","v239","v240","v241","v242","v243","v244","v245","v246","v247","v248","v249","v250","v251","v252","v253","v254","v255",
          "a0","a1","a2","a3","a4","a5","a6","a7","a8","a9","a10","a11","a12","a13","a14","a15","a16","a17","a18","a19","a20","a21","a22","a23","a24","a25","a26","a27","a28","a29","a30","a31","a32","a33","a34","a35","a36","a37","a38","a39","a40","a41","a42","a43","a44","a45","a46","a47","a48","a49","a50","a51","a52","a53","a54","a55","a56","a57","a58","a59","a60","a61","a62","a63","a64","a65","a66","a67","a68","a69","a70","a71","a72","a73","a74","a75","a76","a77","a78","a79","a80","a81","a82","a83","a84","a85","a86","a87","a88","a89","a90","a91","a92","a93","a94","a95","a96","a97","a98","a99","a100","a101","a102","a103","a104","a105","a106","a107","a108","a109","a110","a111","a112","a113","a114","a115","a116","a117","a118","a119","a120","a121","a122","a123","a124","a125","a126","a127","a128","a129","a130","a131","a132","a133","a134","a135","a136","a137","a138","a139","a140","a141","a142","a143","a144","a145","a146","a147","a148","a149","a150","a151","a152","a153","a154","a155","a156","a157","a158","a159","a160","a161","a162","a163","a164","a165","a166","a167","a168","a169","a170","a171","a172","a173","a174","a175","a176","a177","a178","a179","a180","a181","a182","a183","a184","a185","a186","a187","a188","a189","a190","a191","a192","a193","a194","a195","a196","a197","a198","a199","a200","a201","a202","a203","a204","a205","a206","a207","a208","a209","a210","a211","a212","a213","a214","a215","a216","a217","a218","a219","a220","a221","a222","a223","a224","a225","a226","a227","a228","a229","a230","a231","a232","a233","a234","a235","a236","a237","a238","a239","a240","a241","a242","a243","a244","a245","a246","a247","a248","a249","a250","a251","a252","a253","a254","a255",
          "s3","s4","s5","s6","s7","s8","s9","s10","s11","s12","s13","s14","s15","s16","s17","s18","s19","s20","s21","s22","s23","s24","s25","s26","s27","s28","s29","s30","s31","s32","s33","s34","s35","s36","s37","s38","s39","s40","s41","s42","s43","s44","s45","s46","s47","s48","s49","s50","s51","s52","s53","s54","s55","s56","s57","s58","s59","s60","s61","s62","s63","s64","s65","s66","s67","s68","s69","s70","s71","s72","s73","s74","s75","s76","s77","s78","s79","s80","s81","s82","s83","s84","s85","s86","s87","s88","s89","s90","s91","s92","s93","s94","s95","s96","s97","s98","s99","s100","s101","vcc","scc","m0","memory");
#endif
}

extern "C" void kernel_launch(void* const* d_in, const int* in_sizes, int n_in, void* d_out, int out_size, void* d_ws, size_t ws_size,
                              hipStream_t stream) {
    (void)hipFuncSetAttribute((const void*)fused_kernel, hipFuncAttributeMaxDynamicSharedMemorySize, 163840);
    fused_kernel<<<256, 256, 163840, stream>>>((const char*)d_in[0], (const char*)d_in[1], (const char*)d_in[2], (const char*)d_in[3], (const char*)d_in[4], (char*)d_ws,
                                               (const char*)d_in[5], (const char*)d_in[8], (const char*)d_in[9], (const char*)d_in[10], (const char*)d_in[7], (char*)d_out);
}
```

```cpp
#include <hip/hip_runtime.h>

extern "C" __global__ __launch_bounds__(256, 1) void fused_kernel(const char* __restrict__ x, const char* __restrict__ ckv, const char* __restrict__ wq,
                                                       const char* __restrict__ wuk, const char* __restrict__ wuv, char* ws,
                                                       const char* __restrict__ pwo, const char* __restrict__ pwg, const char* __restrict__ pwu, const char* __restrict__ pwd,
                                                       const char* __restrict__ pwr, char* pout) {
    unsigned tid = threadIdx.x, bid = blockIdx.x;
#if defined(__HIP_DEVICE_COMPILE__)
    asm volatile(
"s_load_dwordx2 s[16:17],s[0:1],8\n"
"s_load_dword s3,s[0:1],72\n"
"s_getpc_b64 s[8:9]\n"
".Lfk_pc:\n"
"s_lshr_b32 s82,s2,3\n"
"s_lshl_b32 s82,s82,12\n"
"v_lshlrev_b32 v1,4,v0\n"
"s_mov_b32 s10,.Lfk_cold-.Lfk_pc\n"
"s_mov_b32 s11,0x20000\n"
"buffer_load_dword v115,v1,s[8:11],s82 offen\n"
"v_readfirstlane_b32 s52,v0\n"
"s_load_dwordx2 s[32:33],s[0:1],0\n"
"s_load_dwordx2 s[24:25],s[0:1],40\n"
"s_load_dwordx2 s[44:45],s[0:1],8\n"
"s_load_dwordx2 s[28:29],s[0:1],16\n"
"s_load_dwordx2 s[40:41],s[0:1],16\n"
"s_load_dwordx2 s[4:5],s[0:1],48\n"
"s_and_b32 s82,s2,8\n"
"s_add_u32 s82,s82,24\n"
"s_load_dwordx2 s[20:21],s[0:1],s82\n"
"s_lshr_b32 s52,s52,6\n"
"s_lshr_b32 s83,s2,7\n"
"s_lshl_b32 s83,s83,3\n"
"s_add_u32 s83,s83,24\n"
"s_load_dwordx2 s[36:37],s[0:1],s83\n"
"s_mov_b32 s100,s2\n"
"s_lshl_b32 s92,s2,2\n"
"s_add_u32 s92,s92,s52\n"
"v_and_b32 v0,63,v0\n"
"v_lshlrev_b32 v112,4,v0\n"
"s_and_b32 s88,s2,7\n"
"s_lshr_b32 s89,s2,3\n"
"s_lshl_b32 s93,s89,2\n"
"s_add_u32 s93,s93,s52\n"
"v_lshrrev_b32 v2,3,v0\n"
"v_xor_b32 v3,v2,v0\n"
"v_and_b32 v3,7,v3\n"
"v_lshlrev_b32 v3,4,v3\n"
"s_lshl_b32 s82,s52,9\n"
"v_add_u32 v2,s82,v2\n"
"v_lshl_add_u32 v106,v2,10,v3\n"
"v_add_u32 v107,7168,v106\n"
"v_add_u32 v108,14336,v106\n"
"v_add_u32 v109,21504,v106\n"
"v_lshrrev_b32 v4,4,v0\n"
"v_and_b32 v5,15,v0\n"
"v_lshlrev_b32 v6,1,v4\n"
"v_xor_b32 v5,v5,v6\n"
"v_lshlrev_b32 v5,4,v5\n"
"v_xor_b32 v6,0x80,v5\n"
"s_and_b32 s82,s52,1\n"
"s_lshl_b32 s82,s82,17\n"
"s_lshr_b32 s83,s52,1\n"
"s_lshl_b32 s83,s83,8\n"
"s_add_u32 s82,s82,s83\n"
"v_lshl_add_u32 v7,v4,13,s82\n"
"v_add_u32 v110,v7,v5\n"
"v_add_u32 v111,31744,v110\n"
"v_add_u32 v113,v7,v6\n"
"v_add_u32 v113,63488,v113\n"
"v_add_u32 v114,31744,v113\n"
"s_lshl_b32 s78,s52,12\n"
"s_lshl_b32 s79,s52,13\n"
"s_lshl_b32 s85,s52,11\n"
"s_lshr_b32 s82,s2,5\n"
"s_lshl_b32 s82,s82,15\n"
"s_lshl_b32 s83,s88,10\n"
"s_bfe_u32 s97,s2,0x10004\n"
"s_lshl_b32 s97,s97,9\n"
"s_add_u32 s83,s83,s97\n"
"v_lshrrev_b32 v60,3,v0\n"
"v_lshlrev_b32 v60,19,v60\n"
"v_and_b32 v61,7,v0\n"
"v_lshl_add_u32 v60,v61,7,v60\n"
"s_and_b32 s12,s93,63\n"
"s_lshl3_add_u32 s12,s12,s88\n"
"s_lshl_b32 s12,s12,10\n"
"s_add_u32 s38,s12,1049600\n"
"s_mov_b32 s39,0x20000\n"
"s_and_b32 s14,s52,1\n"
"s_mul_i32 s13,s14,507904\n"
"v_subrev_u32 v62,s13,v106\n"
"v_subrev_u32 v63,s13,v107\n"
"s_mul_i32 s14,s14,6144\n"
"s_sub_u32 s13,s79,s14\n"
"s_waitcnt lgkmcnt(0)\n"
"buffer_load_dword v115,v60,s[36:39],s12 offen\n"
"s_lshl_b32 s87,s92,11\n"
"s_add_u32 s44,s44,s87\n"
"s_addc_u32 s45,s45,0\n"
"s_mov_b32 s46,2048\n"
"s_mov_b32 s47,0x20000\n"
"v_lshlrev_b32 v1,5,v0\n"
"buffer_load_dword v115,v1,s[44:47],0 offen\n"
"s_add_u32 s16,s16,s82\n"
"s_addc_u32 s17,s17,0\n"
"s_sub_u32 s18,2097152,s82\n"
"s_mov_b32 s19,0x20000\n"
"s_add_u32 s20,s20,s83\n"
"s_addc_u32 s21,s21,0\n"
"s_sub_u32 s22,4194304,s83\n"
"s_mov_b32 s23,0x20000\n"
"s_mov_b32 s77,0\n"
"s_add_u32 m0,s78,147456\n"
"s_nop 0\n"
"buffer_load_dwordx4 v110,s[20:23],s77 offen offset:0 lds\n"
"buffer_load_dwordx4 v111,s[20:23],s77 offen offset:1024 lds\n"
"buffer_load_dwordx4 v113,s[20:23],s77 offen offset:2048 lds\n"
"buffer_load_dwordx4 v114,s[20:23],s77 offen offset:3072 lds\n"
"s_mov_b32 s76,786432\n"
"s_add_u32 m0,s13,12288\n"
"s_nop 0\n"
"buffer_load_dwordx4 v62,s[16:19],s76 offen offset:0 lds\n"
"buffer_load_dwordx4 v63,s[16:19],s76 offen offset:1024 lds\n"
"s_mov_b32 s77,262144\n"
"s_add_u32 m0,s78,131072\n"
"s_nop 0\n"
"buffer_load_dwordx4 v110,s[20:23],s77 offen offset:0 lds\n"
"buffer_load_dwordx4 v111,s[20:23],s77 offen offset:1024 lds\n"
"buffer_load_dwordx4 v113,s[20:23],s77 offen offset:2048 lds\n"
"buffer_load_dwordx4 v114,s[20:23],s77 offen offset:3072 lds\n"
"s_mov_b32 s76,0\n"
"s_add_u32 m0,s13,0\n"
"s_nop 0\n"
"buffer_load_dwordx4 v62,s[16:19],s76 offen offset:0 lds\n"
"buffer_load_dwordx4 v63,s[16:19],s76 offen offset:1024 lds\n"
"s_mov_b32 s76,262144\n"
"s_add_u32 m0,s13,4096\n"
"s_nop 0\n"
"buffer_load_dwordx4 v62,s[16:19],s76 offen offset:0 lds\n"
"buffer_load_dwordx4 v63,s[16:19],s76 offen offset:1024 lds\n"
"s_mov_b32 s76,524288\n"
"s_add_u32 m0,s13,8192\n"
"s_nop 0\n"
"buffer_load_dwordx4 v62,s[16:19],s76 offen offset:0 lds\n"
"buffer_load_dwordx4 v63,s[16:19],s76 offen offset:1024 lds\n"
"s_lshl_b32 s82,s92,12\n"
"s_add_u32 s32,s32,s82\n"
"s_addc_u32 s33,s33,0\n"
"s_mov_b32 s34,4096\n"
"s_mov_b32 s35,0x20000\n"
"s_mov_b32 s26,176160768\n"
"s_mov_b32 s27,0x20000\n"
"s_mov_b32 s75,148897792\n"
"s_lshr_b32 s54,s2,5\n"
"s_lshl_b32 s54,s54,19\n"
"s_and_b32 s82,s92,0x47\n"
"s_bfe_u32 s83,s92,0x10003\n"
"s_lshl_b32 s83,s83,5\n"
"s_or_b32 s82,s82,s83\n"
"s_bfe_u32 s83,s92,0x20004\n"
"s_lshl3_add_u32 s82,s83,s82\n"
"s_lshl_b32 s82,s82,7\n"
"s_add_u32 s54,s54,s82\n"
"v_lshrrev_b32 v1,3,v0\n"
"v_bfe_u32 v2,v0,2,1\n"
"v_and_b32 v3,3,v0\n"
"v_lshlrev_b32 v3,4,v3\n"
"v_lshl_add_u32 v70,v1,6,v3\n"
"v_lshl_add_u32 v70,v2,11,v70\n"
"s_bfe_u32 s82,s54,0x10009\n"
"s_lshl_b32 s82,s82,5\n"
"v_xor_b32 v3,s82,v3\n"
"v_lshl_add_u32 v71,v2,6,v3\n"
"v_lshl_add_u32 v71,v1,14,v71\n"
"buffer_load_dwordx2 v[78:79],off,s[24:27],s75 sc1\n"
"s_lshl_b32 s82,s52,16\n"
"v_lshl_add_u32 v7,v4,13,s82\n"
"s_bitcmp1_b32 s52,0\n"
"s_cselect_b32 s83,0x80,0\n"
"v_xor_b32 v5,s83,v5\n"
"v_add_u32 v72,v7,v5\n"
"v_add_u32 v73,32768,v72\n"
"s_mov_b32 s86,0\n"
"v_and_b32 v1,15,v0\n"
"v_lshrrev_b32 v2,4,v0\n"
"v_lshlrev_b32 v3,6,v1\n"
"v_lshl_add_u32 v3,v2,4,v3\n"
"v_lshrrev_b32 v4,4,v3\n"
"v_and_b32 v4,32,v4\n"
"v_xor_b32 v97,v3,v4\n"
"s_lshr_b32 s82,s52,1\n"
"s_lshl_b32 s82,s82,14\n"
"v_and_b32 v3,7,v1\n"
"v_xor_b32 v3,v3,v2\n"
"v_lshlrev_b32 v3,4,v3\n"
"v_lshl_add_u32 v3,v1,7,v3\n"
"v_add_u32 v80,s82,v3\n"
"v_xor_b32 v81,64,v80\n"
"v_add_u32 v82,65536,v80\n"
"v_add_u32 v83,65536,v81\n"
"v_bfe_u32 v3,v0,2,2\n"
"v_lshl_add_u32 v4,v2,3,v3\n"
"v_lshlrev_b32 v4,8,v4\n"
"v_and_b32 v5,1,v2\n"
"v_lshl_add_u32 v5,v5,2,v3\n"
"v_lshlrev_b32 v5,5,v5\n"
"v_and_b32 v6,3,v0\n"
"v_lshl_add_u32 v4,v6,3,v4\n"
"s_and_b32 s82,s52,1\n"
"s_lshl_b32 s82,s82,13\n"
"s_add_u32 s82,s82,98304\n"
"v_add_u32 v4,s82,v4\n"
"v_xor_b32 v6,0,v5\n"
"v_add_u32 v98,v4,v6\n"
"v_xor_b32 v6,32,v5\n"
"v_add_u32 v99,v4,v6\n"
"v_xor_b32 v6,64,v5\n"
"v_add_u32 v100,v4,v6\n"
"v_xor_b32 v6,96,v5\n"
"v_add_u32 v101,v4,v6\n"
"v_xor_b32 v6,128,v5\n"
"v_add_u32 v102,v4,v6\n"
"v_xor_b32 v6,160,v5\n"
"v_add_u32 v103,v4,v6\n"
"v_xor_b32 v6,192,v5\n"
"v_add_u32 v104,v4,v6\n"
"v_xor_b32 v6,224,v5\n"
"v_add_u32 v105,v4,v6\n"
"s_and_b32 s53,s89,1\n"
"s_lshl_b32 s83,s88,10\n"
"s_and_b32 s82,s93,63\n"
"s_add_u32 s82,s82,128\n"
"s_lshl_b32 s82,s82,13\n"
"s_add_u32 s82,s82,s83\n"
"s_add_u32 s36,s36,s82\n"
"s_addc_u32 s37,s37,0\n"
"s_mov_b32 s38,2622464\n"
"s_mov_b32 s39,0x20000\n"
"s_lshl_b32 s82,s93,13\n"
"s_add_u32 s82,s82,s83\n"
"s_add_u32 s40,s40,s82\n"
"s_addc_u32 s41,s41,0\n"
"s_mov_b32 s42,7341056\n"
"s_mov_b32 s43,0x20000\n"
"s_cmp_lg_u32 s53,0\n"
"s_cbranch_scc1 .Lfk_v\n"
"s_waitcnt vmcnt(11)\n"
"s_barrier\n"
"ds_read_b128 v[152:155],v80 offset:12288\n"
"ds_read_b128 v[156:159],v80 offset:14336\n"
"ds_read_b64_tr_b16 v[160:161],v98 offset:49152\n"
"ds_read_b64_tr_b16 v[162:163],v98 offset:50176\n"
"ds_read_b64_tr_b16 v[164:165],v99 offset:49152\n"
"ds_read_b64_tr_b16 v[166:167],v99 offset:50176\n"
"ds_read_b64_tr_b16 v[168:169],v100 offset:49152\n"
"ds_read_b64_tr_b16 v[170:171],v100 offset:50176\n"
"ds_read_b64_tr_b16 v[172:173],v101 offset:49152\n"
"ds_read_b64_tr_b16 v[174:175],v101 offset:50176\n"
"ds_read_b64_tr_b16 v[176:177],v102 offset:49152\n"
"ds_read_b64_tr_b16 v[178:179],v102 offset:50176\n"
"ds_read_b64_tr_b16 v[180:181],v103 offset:49152\n"
"ds_read_b64_tr_b16 v[182:183],v103 offset:50176\n"
"ds_read_b64_tr_b16 v[184:185],v104 offset:49152\n"
"ds_read_b64_tr_b16 v[186:187],v104 offset:50176\n"
"ds_read_b64_tr_b16 v[188:189],v105 offset:49152\n"
"ds_read_b64_tr_b16 v[190:191],v105 offset:50176\n"
"s_waitcnt lgkmcnt(12)\n"
"v_mfma_f32_16x16x32_bf16 a[192:195],v[160:163],v[152:155],0\n"
"s_and_b32 s91,s89,7\n"
"s_mov_b32 s76,128\n"
"s_add_u32 m0,s79,32768\n"
"v_mfma_f32_16x16x32_bf16 a[224:227],v[160:163],v[156:159],0\n"
"buffer_load_dwordx4 v106,s[16:19],s76 offen offset:0 lds\n"
"v_mfma_f32_16x16x32_bf16 a[228:231],v[164:167],v[156:159],0\n"
"s_lshr_b32 s97,s89,3\n"
"s_lshl2_add_u32 s90,s88,s97\n"
"v_mfma_f32_16x16x32_bf16 a[196:199],v[164:167],v[152:155],0\n"
"buffer_load_dwordx4 v107,s[16:19],s76 offen offset:1024 lds\n"
"s_waitcnt lgkmcnt(8)\n"
"v_mfma_f32_16x16x32_bf16 a[200:203],v[168:171],v[152:155],0\n"
"s_lshl_b32 vcc_lo,s90,8\n"
"s_add_u32 s28,s28,vcc_lo\n"
"s_addc_u32 s29,s29,0\n"
"v_mfma_f32_16x16x32_bf16 a[232:235],v[168:171],v[156:159],0\n"
"buffer_load_dwordx4 v108,s[16:19],s76 offen offset:2048 lds\n"
"v_mfma_f32_16x16x32_bf16 a[236:239],v[172:175],v[156:159],0\n"
"s_sub_u32 s30,8388608,vcc_lo\n"
"s_mov_b32 s31,0x20000\n"
"buffer_load_dwordx4 v109,s[16:19],s76 offen offset:3072 lds\n"
"v_mfma_f32_16x16x32_bf16 a[204:207],v[172:175],v[152:155],0\n"
"s_waitcnt vmcnt(9)\n"
"s_barrier\n"
"s_waitcnt lgkmcnt(4)\n"
"v_mfma_f32_16x16x32_bf16 a[208:211],v[176:179],v[152:155],0\n"
"ds_read_b128 v[128:131],v80 offset:0\n"
"ds_read_b128 v[192:195],v81 offset:0\n"
"v_mfma_f32_16x16x32_bf16 a[240:243],v[176:179],v[156:159],0\n"
"ds_read_b64_tr_b16 v[224:225],v98 offset:32768\n"
"ds_read_b64_tr_b16 v[226:227],v98 offset:33792\n"
"v_mfma_f32_16x16x32_bf16 a[244:247],v[180:183],v[156:159],0\n"
"ds_read_b64_tr_b16 v[228:229],v99 offset:32768\n"
"ds_read_b64_tr_b16 v[230:231],v99 offset:33792\n"
"v_mfma_f32_16x16x32_bf16 a[212:215],v[180:183],v[152:155],0\n"
"ds_read_b64_tr_b16 v[232:233],v100 offset:32768\n"
"ds_read_b64_tr_b16 v[234:235],v100 offset:33792\n"
"s_waitcnt lgkmcnt(8)\n"
"v_mfma_f32_16x16x32_bf16 a[216:219],v[184:187],v[152:155],0\n"
"ds_read_b64_tr_b16 v[236:237],v101 offset:32768\n"
"ds_read_b64_tr_b16 v[238:239],v101 offset:33792\n"
"v_mfma_f32_16x16x32_bf16 a[248:251],v[184:187],v[156:159],0\n"
"ds_read_b64_tr_b16 v[240:241],v102 offset:32768\n"
"ds_read_b64_tr_b16 v[242:243],v102 offset:33792\n"
"v_mfma_f32_16x16x32_bf16 a[252:255],v[188:191],v[156:159],0\n"
"ds_read_b64_tr_b16 v[244:245],v103 offset:32768\n"
"ds_read_b64_tr_b16 v[246:247],v103 offset:33792\n"
"v_mfma_f32_16x16x32_bf16 a[220:223],v[188:191],v[152:155],0\n"
"ds_read_b64_tr_b16 v[248:249],v104 offset:32768\n"
"ds_read_b64_tr_b16 v[250:251],v104 offset:33792\n"
"s_waitcnt lgkmcnt(10)\n"
"v_mfma_f32_16x16x32_bf16 a[0:3],v[160:163],v[128:131],0\n"
"ds_read_b64_tr_b16 v[252:253],v105 offset:32768\n"
"ds_read_b64_tr_b16 v[254:255],v105 offset:33792\n"
"v_mfma_f32_16x16x32_bf16 a[0:3],v[224:227],v[192:195],a[0:3]\n"
"ds_read_b128 v[132:135],v80 offset:2048\n"
"ds_read_b128 v[196:199],v81 offset:2048\n"
"v_mfma_f32_16x16x32_bf16 a[4:7],v[228:231],v[192:195],0\n"
"ds_read_b128 v[216:219],v81 offset:12288\n"
"ds_read_b128 v[220:223],v81 offset:14336\n"
"v_mfma_f32_16x16x32_bf16 a[4:7],v[164:167],v[128:131],a[4:7]\n"
"s_waitcnt lgkmcnt(12)\n"
"s_mov_b32 s76,262272\n"
"s_add_u32 m0,s79,36864\n"
"v_mfma_f32_16x16x32_bf16 a[8:11],v[168:171],v[128:131],0\n"
"buffer_load_dwordx4 v106,s[16:19],s76 offen offset:0 lds\n"
"v_mfma_f32_16x16x32_bf16 a[8:11],v[232:235],v[192:195],a[8:11]\n"
"buffer_load_dwordx4 v107,s[16:19],s76 offen offset:1024 lds\n"
"v_mfma_f32_16x16x32_bf16 a[12:15],v[236:239],v[192:195],0\n"
"buffer_load_dwordx4 v108,s[16:19],s76 offen offset:2048 lds\n"
"buffer_load_dwordx4 v109,s[16:19],s76 offen offset:3072 lds\n"
"v_mfma_f32_16x16x32_bf16 a[12:15],v[172:175],v[128:131],a[12:15]\n"
"s_waitcnt lgkmcnt(8)\n"
"s_mov_b32 s77,524288\n"
"s_add_u32 m0,s78,98304\n"
"v_mfma_f32_16x16x32_bf16 a[16:19],v[176:179],v[128:131],0\n"
"buffer_load_dwordx4 v110,s[20:23],s77 offen offset:0 lds\n"
"v_mfma_f32_16x16x32_bf16 a[16:19],v[240:243],v[192:195],a[16:19]\n"
"buffer_load_dwordx4 v111,s[20:23],s77 offen offset:1024 lds\n"
"v_mfma_f32_16x16x32_bf16 a[20:23],v[244:247],v[192:195],0\n"
"buffer_load_dwordx4 v113,s[20:23],s77 offen offset:2048 lds\n"
"buffer_load_dwordx4 v114,s[20:23],s77 offen offset:3072 lds\n"
"v_mfma_f32_16x16x32_bf16 a[20:23],v[180:183],v[128:131],a[20:23]\n"
"s_waitcnt lgkmcnt(4)\n"
"s_mov_b32 s77,786432\n"
"s_add_u32 m0,s78,114688\n"
"v_mfma_f32_16x16x32_bf16 a[24:27],v[184:187],v[128:131],0\n"
"buffer_load_dwordx4 v110,s[20:23],s77 offen offset:0 lds\n"
"v_mfma_f32_16x16x32_bf16 a[24:27],v[248:251],v[192:195],a[24:27]\n"
"buffer_load_dwordx4 v111,s[20:23],s77 offen offset:1024 lds\n"
"v_mfma_f32_16x16x32_bf16 a[28:31],v[252:255],v[192:195],0\n"
"buffer_load_dwordx4 v113,s[20:23],s77 offen offset:2048 lds\n"
"buffer_load_dwordx4 v114,s[20:23],s77 offen offset:3072 lds\n"
"v_mfma_f32_16x16x32_bf16 a[28:31],v[188:191],v[128:131],a[28:31]\n"
"s_waitcnt lgkmcnt(2)\n"
"s_mov_b32 s76,256\n"
"s_add_u32 m0,s79,65536\n"
"v_mfma_f32_16x16x32_bf16 a[32:35],v[160:163],v[132:135],0\n"
"buffer_load_dwordx4 v106,s[16:19],s76 offen offset:0 lds\n"
"v_mfma_f32_16x16x32_bf16 a[32:35],v[224:227],v[196:199],a[32:35]\n"
"buffer_load_dwordx4 v107,s[16:19],s76 offen offset:1024 lds\n"
"v_mfma_f32_16x16x32_bf16 a[36:39],v[228:231],v[196:199],0\n"
"buffer_load_dwordx4 v108,s[16:19],s76 offen offset:2048 lds\n"
"buffer_load_dwordx4 v109,s[16:19],s76 offen offset:3072 lds\n"
"v_mfma_f32_16x16x32_bf16 a[36:39],v[164:167],v[132:135],a[36:39]\n"
"s_waitcnt vmcnt(21)\n"
"s_barrier\n"
"v_mfma_f32_16x16x32_bf16 a[40:43],v[168:171],v[132:135],0\n"
"ds_read_b128 v[136:139],v80 offset:4096\n"
"ds_read_b128 v[200:203],v81 offset:4096\n"
"v_mfma_f32_16x16x32_bf16 a[40:43],v[232:235],v[196:199],a[40:43]\n"
"ds_read_b128 v[140:143],v80 offset:6144\n"
"ds_read_b128 v[204:207],v81 offset:6144\n"
"v_mfma_f32_16x16x32_bf16 a[44:47],v[236:239],v[196:199],0\n"
"ds_read_b128 v[144:147],v80 offset:8192\n"
"ds_read_b128 v[208:211],v81 offset:8192\n"
"v_mfma_f32_16x16x32_bf16 a[44:47],v[172:175],v[132:135],a[44:47]\n"
"ds_read_b128 v[148:151],v80 offset:10240\n"
"ds_read_b128 v[212:215],v81 offset:10240\n"
"v_mfma_f32_16x16x32_bf16 a[48:51],v[176:179],v[132:135],0\n"
"s_mov_b32 s60,s91\n"
"s_mov_b32 s76,262400\n"
"s_add_u32 m0,s79,69632\n"
"v_mfma_f32_16x16x32_bf16 a[48:51],v[240:243],v[196:199],a[48:51]\n"
"buffer_load_dwordx4 v106,s[16:19],s76 offen offset:0 lds\n"
"v_mfma_f32_16x16x32_bf16 a[52:55],v[244:247],v[196:199],0\n"
"buffer_load_dwordx4 v107,s[16:19],s76 offen offset:1024 lds\n"
"v_mfma_f32_16x16x32_bf16 a[52:55],v[180:183],v[132:135],a[52:55]\n"
"buffer_load_dwordx4 v108,s[16:19],s76 offen offset:2048 lds\n"
"v_mfma_f32_16x16x32_bf16 a[56:59],v[184:187],v[132:135],0\n"
"buffer_load_dwordx4 v109,s[16:19],s76 offen offset:3072 lds\n"
"v_mfma_f32_16x16x32_bf16 a[56:59],v[248:251],v[196:199],a[56:59]\n"
"s_sub_u32 s94,15,s91\n"
"s_waitcnt lgkmcnt(0)\n"
"s_barrier\n"
"v_mfma_f32_16x16x32_bf16 a[60:63],v[252:255],v[196:199],0\n"
"s_cmp_lt_u32 s52,2\n"
"s_cselect_b32 s95,s94,s91\n"
"s_mov_b32 s77,1048576\n"
"s_add_u32 m0,s78,147456\n"
"v_mfma_f32_16x16x32_bf16 a[60:63],v[188:191],v[132:135],a[60:63]\n"
"buffer_load_dwordx4 v110,s[20:23],s77 offen offset:0 lds\n"
"v_mfma_f32_16x16x32_bf16 a[64:67],v[160:163],v[136:139],0\n"
"s_lshr_b32 vcc_lo,s95,1\n"
"s_lshl_b32 s55,vcc_lo,19\n"
"v_mfma_f32_16x16x32_bf16 a[64:67],v[224:227],v[200:203],a[64:67]\n"
"s_lshl_b32 s58,vcc_lo,10\n"
"s_add_u32 s58,s58,148898816\n"
"v_mfma_f32_16x16x32_bf16 a[68:71],v[228:231],v[200:203],0\n"
"s_and_b32 vcc_lo,s95,1\n"
"s_lshl_b32 vcc_lo,vcc_lo,13\n"
"v_mfma_f32_16x16x32_bf16 a[68:71],v[164:167],v[136:139],a[68:71]\n"
"buffer_load_dwordx4 v111,s[20:23],s77 offen offset:1024 lds\n"
"v_mfma_f32_16x16x32_bf16 a[72:75],v[168:171],v[136:139],0\n"
"s_and_b32 vcc_hi,s78,4096\n"
"s_add_u32 s55,s55,vcc_lo\n"
"v_mfma_f32_16x16x32_bf16 a[72:75],v[232:235],v[200:203],a[72:75]\n"
"s_add_u32 s55,s55,vcc_hi\n"
"s_and_b32 vcc_lo,s89,2\n"
"v_mfma_f32_16x16x32_bf16 a[76:79],v[236:239],v[200:203],0\n"
"s_lshl2_add_u32 vcc_lo,s88,vcc_lo\n"
"s_and_b32 vcc_hi,s52,1\n"
"v_mfma_f32_16x16x32_bf16 a[76:79],v[172:175],v[136:139],a[76:79]\n"
"buffer_load_dwordx4 v113,s[20:23],s77 offen offset:2048 lds\n"
"v_mfma_f32_16x16x32_bf16 a[80:83],v[176:179],v[136:139],0\n"
"s_add_u32 vcc_lo,vcc_lo,vcc_hi\n"
"s_lshr_b32 vcc_hi,s89,2\n"
"v_mfma_f32_16x16x32_bf16 a[80:83],v[240:243],v[200:203],a[80:83]\n"
"s_lshr_b32 s84,s52,1\n"
"s_lshl1_add_u32 vcc_hi,vcc_hi,s84\n"
"v_mfma_f32_16x16x32_bf16 a[84:87],v[244:247],v[200:203],0\n"
"s_mul_i32 s56,vcc_lo,1179648\n"
"s_lshl_b32 s84,s84,19\n"
"v_mfma_f32_16x16x32_bf16 a[84:87],v[180:183],v[136:139],a[84:87]\n"
"buffer_load_dwordx4 v114,s[20:23],s77 offen offset:3072 lds\n"
"v_mfma_f32_16x16x32_bf16 a[88:91],v[184:187],v[136:139],0\n"
"s_add_u32 s56,s56,s84\n"
"s_lshr_b32 s84,s89,2\n"
"v_mfma_f32_16x16x32_bf16 a[88:91],v[248:251],v[200:203],a[88:91]\n"
"s_lshl_b32 s84,s84,14\n"
"s_add_u32 s56,s56,s84\n"
"v_mfma_f32_16x16x32_bf16 a[92:95],v[252:255],v[200:203],0\n"
"s_lshl_b32 s84,s53,13\n"
"s_mov_b32 s77,1310720\n"
"s_add_u32 m0,s78,131072\n"
"v_mfma_f32_16x16x32_bf16 a[92:95],v[188:191],v[136:139],a[92:95]\n"
"buffer_load_dwordx4 v110,s[20:23],s77 offen offset:0 lds\n"
"v_mfma_f32_16x16x32_bf16 a[96:99],v[160:163],v[140:143],0\n"
"s_add_u32 s56,s56,s84\n"
"s_add_u32 s56,s56,4194304\n"
"v_mfma_f32_16x16x32_bf16 a[96:99],v[224:227],v[204:207],a[96:99]\n"
"s_lshl4_add_u32 vcc_hi,s53,vcc_hi\n"
"s_lshl_b32 vcc_lo,vcc_lo,8\n"
"v_mfma_f32_16x16x32_bf16 a[100:103],v[228:231],v[204:207],0\n"
"buffer_load_dwordx4 v111,s[20:23],s77 offen offset:1024 lds\n"
"v_mfma_f32_16x16x32_bf16 a[100:103],v[164:167],v[140:143],a[100:103]\n"
"s_lshl3_add_u32 s59,vcc_hi,vcc_lo\n"
"s_add_u32 s59,s59,148914176\n"
"v_mfma_f32_16x16x32_bf16 a[104:107],v[168:171],v[140:143],0\n"
"s_lshl_b32 s57,s92,3\n"
"s_add_u32 s57,s57,148898816\n"
"v_mfma_f32_16x16x32_bf16 a[104:107],v[232:235],v[204:207],a[104:107]\n"
"s_lshl_b32 s63,s90,8\n"
"s_add_u32 s63,s63,148914176\n"
"v_mfma_f32_16x16x32_bf16 a[108:111],v[236:239],v[204:207],0\n"
"buffer_load_dwordx4 v113,s[20:23],s77 offen offset:2048 lds\n"
"v_mfma_f32_16x16x32_bf16 a[108:111],v[172:175],v[140:143],a[108:111]\n"
"s_lshl_b32 s74,s92,11\n"
"s_add_u32 s74,s74,171966464\n"
"v_mfma_f32_16x16x32_bf16 a[112:115],v[176:179],v[140:143],0\n"
"s_mul_i32 s96,s90,1179648\n"
"s_add_u32 s96,s96,4194304\n"
"v_mfma_f32_16x16x32_bf16 a[112:115],v[240:243],v[204:207],a[112:115]\n"
"s_add_u32 s73,s96,s85\n"
"s_and_b32 vcc_hi,s78,4096\n"
"v_mfma_f32_16x16x32_bf16 a[116:119],v[244:247],v[204:207],0\n"
"buffer_load_dwordx4 v114,s[20:23],s77 offen offset:3072 lds\n"
"v_mfma_f32_16x16x32_bf16 a[116:119],v[180:183],v[140:143],a[116:119]\n"
"s_lshl_b32 vcc_hi,vcc_hi,1\n"
"s_add_u32 vcc_lo,s96,vcc_hi\n"
"v_mfma_f32_16x16x32_bf16 a[120:123],v[184:187],v[140:143],0\n"
"s_add_u32 s65,vcc_lo,524288\n"
"s_lshl_b32 s66,s91,16\n"
"v_mfma_f32_16x16x32_bf16 a[120:123],v[248:251],v[204:207],a[120:123]\n"
"s_sub_u32 s66,vcc_lo,s66\n"
"s_mov_b32 s76,384\n"
"s_add_u32 m0,s79,0\n"
"v_mfma_f32_16x16x32_bf16 a[124:127],v[252:255],v[204:207],0\n"
"buffer_load_dwordx4 v106,s[16:19],s76 offen offset:0 lds\n"
"v_mfma_f32_16x16x32_bf16 a[124:127],v[188:191],v[140:143],a[124:127]\n"
"s_add_u32 s66,s66,999424\n"
"s_add_u32 s67,vcc_hi,65536\n"
"v_mfma_f32_16x16x32_bf16 a[128:131],v[160:163],v[144:147],0\n"
"s_mul_i32 vcc_lo,vcc_hi,5\n"
"s_sub_u32 vcc_lo,s96,vcc_lo\n"
"v_mfma_f32_16x16x32_bf16 a[128:131],v[224:227],v[208:211],a[128:131]\n"
"s_add_u32 s64,vcc_lo,81920\n"
"s_add_u32 s62,vcc_lo,135168\n"
"s_add_u32 s61,vcc_lo,524288\n"
"v_mfma_f32_16x16x32_bf16 a[132:135],v[228:231],v[208:211],0\n"
"buffer_load_dwordx4 v107,s[16:19],s76 offen offset:1024 lds\n"
"v_mfma_f32_16x16x32_bf16 a[132:135],v[164:167],v[144:147],a[132:135]\n"
"s_cmp_lt_u32 s52,2\n"
"s_cselect_b32 s64,s64,2146435072\n"
"s_cselect_b32 s67,s67,163840\n"
"s_cselect_b32 s65,s65,1\n"
"v_mfma_f32_16x16x32_bf16 a[136:139],v[168:171],v[144:147],0\n"
"s_cselect_b32 vcc_lo,4,0\n"
"s_and_b32 vcc_hi,s52,1\n"
"s_add_u32 vcc_lo,vcc_lo,vcc_hi\n"
"v_mfma_f32_16x16x32_bf16 a[136:139],v[232:235],v[208:211],a[136:139]\n"
"s_lshl_b32 vcc_hi,s90,12\n"
"s_add_u32 vcc_hi,vcc_hi,54525952\n"
"v_mfma_f32_16x16x32_bf16 a[140:143],v[236:239],v[208:211],0\n"
"buffer_load_dwordx4 v108,s[16:19],s76 offen offset:2048 lds\n"
"v_mfma_f32_16x16x32_bf16 a[140:143],v[172:175],v[144:147],a[140:143]\n"
"s_lshl3_add_u32 s70,s95,vcc_lo\n"
"s_lshl_b32 s70,s70,17\n"
"v_mfma_f32_16x16x32_bf16 a[144:147],v[176:179],v[144:147],0\n"
"s_add_u32 s70,s70,vcc_hi\n"
"s_lshl3_add_u32 s68,s94,vcc_lo\n"
"v_mfma_f32_16x16x32_bf16 a[144:147],v[240:243],v[208:211],a[144:147]\n"
"s_lshl_b32 s68,s68,17\n"
"s_add_u32 s68,s68,vcc_hi\n"
"v_mfma_f32_16x16x32_bf16 a[148:151],v[244:247],v[208:211],0\n"
"buffer_load_dwordx4 v109,s[16:19],s76 offen offset:3072 lds\n"
"v_mfma_f32_16x16x32_bf16 a[148:151],v[180:183],v[144:147],a[148:151]\n"
"s_lshr_b32 vcc_lo,s90,3\n"
"s_lshl2_add_u32 vcc_lo,vcc_lo,s52\n"
"v_mfma_f32_16x16x32_bf16 a[152:155],v[184:187],v[144:147],0\n"
"s_lshl_b32 vcc_lo,vcc_lo,7\n"
"s_and_b32 vcc_hi,s90,7\n"
"v_mfma_f32_16x16x32_bf16 a[152:155],v[248:251],v[208:211],a[152:155]\n"
"s_add_u32 vcc_lo,vcc_lo,vcc_hi\n"
"s_mov_b32 s76,262528\n"
"s_add_u32 m0,s79,4096\n"
"v_mfma_f32_16x16x32_bf16 a[156:159],v[252:255],v[208:211],0\n"
"buffer_load_dwordx4 v106,s[16:19],s76 offen offset:0 lds\n"
"v_mfma_f32_16x16x32_bf16 a[156:159],v[188:191],v[144:147],a[156:159]\n"
"s_lshl3_add_u32 s69,s91,vcc_lo\n"
"s_lshl_b32 s69,s69,3\n"
"v_mfma_f32_16x16x32_bf16 a[160:163],v[160:163],v[148:151],0\n"
"s_add_u32 s69,s69,148930560\n"
"s_lshl3_add_u32 s71,s94,vcc_lo\n"
"v_mfma_f32_16x16x32_bf16 a[160:163],v[224:227],v[212:215],a[160:163]\n"
"s_lshl_b32 s71,s71,3\n"
"s_add_u32 s71,s71,148930560\n"
"v_mfma_f32_16x16x32_bf16 a[164:167],v[228:231],v[212:215],0\n"
"buffer_load_dwordx4 v107,s[16:19],s76 offen offset:1024 lds\n"
"v_mfma_f32_16x16x32_bf16 a[164:167],v[164:167],v[148:151],a[164:167]\n"
"s_mov_b32 s6,8388608\n"
"s_mov_b32 s7,0x20000\n"
"v_mfma_f32_16x16x32_bf16 a[168:171],v[168:171],v[148:151],0\n"
"s_lshr_b32 s94,s88,1\n"
"s_and_b32 vcc_lo,s91,1\n"
"v_mfma_f32_16x16x32_bf16 a[168:171],v[232:235],v[212:215],a[168:171]\n"
"s_lshl1_add_u32 s95,s97,vcc_lo\n"
"s_and_b32 vcc_lo,s88,1\n"
"v_mfma_f32_16x16x32_bf16 a[172:175],v[236:239],v[212:215],0\n"
"buffer_load_dwordx4 v108,s[16:19],s76 offen offset:2048 lds\n"
"v_mfma_f32_16x16x32_bf16 a[172:175],v[172:175],v[148:151],a[172:175]\n"
"s_lshr_b32 vcc_hi,s91,1\n"
"s_lshl2_add_u32 s96,vcc_lo,vcc_hi\n"
"v_mfma_f32_16x16x32_bf16 a[176:179],v[176:179],v[148:151],0\n"
"s_lshr_b32 vcc_lo,s88,2\n"
"s_lshl2_add_u32 s96,vcc_lo,vcc_hi\n"
"v_mfma_f32_16x16x32_bf16 a[176:179],v[240:243],v[212:215],a[176:179]\n"
"s_sub_u32 vcc_lo,7,s88\n"
"s_min_u32 s94,vcc_lo,s88\n"
"v_mfma_f32_16x16x32_bf16 a[180:183],v[244:247],v[212:215],0\n"
"buffer_load_dwordx4 v109,s[16:19],s76 offen offset:3072 lds\n"
"v_mfma_f32_16x16x32_bf16 a[180:183],v[180:183],v[148:151],a[180:183]\n"
"s_and_b32 s96,s88,4\n"
"s_add_u32 s96,s96,s97\n"
"s_add_u32 s95,s91,s96\n"
"v_mfma_f32_16x16x32_bf16 a[184:187],v[184:187],v[148:151],0\n"
"buffer_load_dwordx4 v[8:11],v70,s[32:35],0 offen offset:0\n"
"v_mfma_f32_16x16x32_bf16 a[184:187],v[248:251],v[212:215],a[184:187]\n"
"buffer_load_dwordx4 v[12:15],v70,s[32:35],0 offen offset:512\n"
"s_and_b32 s95,s95,7\n"
"v_mfma_f32_16x16x32_bf16 a[188:191],v[252:255],v[212:215],0\n"
"buffer_load_dwordx4 v[16:19],v70,s[32:35],0 offen offset:1024\n"
"buffer_load_dwordx4 v[20:23],v70,s[32:35],0 offen offset:1536\n"
"s_lshl3_add_u32 vcc_lo,s94,s96\n"
"v_mfma_f32_16x16x32_bf16 a[188:191],v[188:191],v[148:151],a[188:191]\n"
"s_waitcnt vmcnt(28)\n"
"s_barrier\n"
"v_mfma_f32_16x16x32_bf16 a[192:195],v[224:227],v[216:219],a[192:195]\n"
"ds_read_b128 v[152:155],v80 offset:45056\n"
"s_mov_b32 s87,524288\n"
"v_mfma_f32_16x16x32_bf16 a[224:227],v[224:227],v[220:223],a[224:227]\n"
"ds_read_b128 v[156:159],v80 offset:47104\n"
"buffer_load_dword v115,v112,s[36:39],s87 offen\n"
"v_mfma_f32_16x16x32_bf16 a[228:231],v[228:231],v[220:223],a[228:231]\n"
"ds_read_b64_tr_b16 v[160:161],v98 offset:0\n"
"s_mov_b32 s87,1048576\n"
"v_mfma_f32_16x16x32_bf16 a[196:199],v[228:231],v[216:219],a[196:199]\n"
"ds_read_b64_tr_b16 v[162:163],v98 offset:1024\n"
"buffer_load_dword v115,v112,s[36:39],s87 offen\n"
"v_mfma_f32_16x16x32_bf16 a[200:203],v[232:235],v[216:219],a[200:203]\n"
"ds_read_b64_tr_b16 v[164:165],v99 offset:0\n"
"s_mov_b32 s87,1572864\n"
"v_mfma_f32_16x16x32_bf16 a[232:235],v[232:235],v[220:223],a[232:235]\n"
"ds_read_b64_tr_b16 v[166:167],v99 offset:1024\n"
"buffer_load_dword v115,v112,s[36:39],s87 offen\n"
"v_mfma_f32_16x16x32_bf16 a[236:239],v[236:239],v[220:223],a[236:239]\n"
"ds_read_b64_tr_b16 v[168:169],v100 offset:0\n"
"s_lshl3_add_u32 vcc_lo,vcc_lo,s52\n"
"v_mfma_f32_16x16x32_bf16 a[204:207],v[236:239],v[216:219],a[204:207]\n"
"ds_read_b64_tr_b16 v[170:171],v100 offset:1024\n"
"s_lshl_b32 s13,vcc_lo,15\n"
"v_mfma_f32_16x16x32_bf16 a[208:211],v[240:243],v[216:219],a[208:211]\n"
"ds_read_b64_tr_b16 v[172:173],v101 offset:0\n"
"s_lshl_b32 vcc_lo,s95,8\n"
"v_mfma_f32_16x16x32_bf16 a[240:243],v[240:243],v[220:223],a[240:243]\n"
"ds_read_b64_tr_b16 v[174:175],v101 offset:1024\n"
"s_add_u32 s13,s13,vcc_lo\n"
"v_mfma_f32_16x16x32_bf16 a[244:247],v[244:247],v[220:223],a[244:247]\n"
"ds_read_b64_tr_b16 v[176:177],v102 offset:0\n"
"s_lshl3_add_u32 vcc_hi,s94,s95\n"
"v_mfma_f32_16x16x32_bf16 a[212:215],v[244:247],v[216:219],a[212:215]\n"
"ds_read_b64_tr_b16 v[178:179],v102 offset:1024\n"
"s_lshl_b32 s72,vcc_hi,8\n"
"v_mfma_f32_16x16x32_bf16 a[216:219],v[248:251],v[216:219],a[216:219]\n"
"ds_read_b64_tr_b16 v[180:181],v103 offset:0\n"
"s_add_u32 s72,s72,149061632\n"
"v_mfma_f32_16x16x32_bf16 a[248:251],v[248:251],v[220:223],a[248:251]\n"
"s_or_b32 vcc_lo,s96,s52\n"
"ds_read_b64_tr_b16 v[182:183],v103 offset:1024\n"
"s_cselect_b32 s72,2146435072,s72\n"
"v_mfma_f32_16x16x32_bf16 a[252:255],v[252:255],v[220:223],a[252:255]\n"
"ds_read_b64_tr_b16 v[184:185],v104 offset:0\n"
"s_lshl_b32 s3,vcc_hi,18\n"
"v_mfma_f32_16x16x32_bf16 a[220:223],v[252:255],v[216:219],a[220:223]\n"
"s_lshl_b32 vcc_lo,s96,15\n"
"ds_read_b64_tr_b16 v[186:187],v104 offset:1024\n"
"s_waitcnt lgkmcnt(10)\n"
"v_mfma_f32_16x16x32_bf16 a[192:195],v[160:163],v[152:155],a[192:195]\n"
"ds_read_b64_tr_b16 v[188:189],v105 offset:0\n"
"s_sub_u32 s3,s3,vcc_lo\n"
"v_mfma_f32_16x16x32_bf16 a[224:227],v[160:163],v[156:159],a[224:227]\n"
"ds_read_b64_tr_b16 v[190:191],v105 offset:1024\n"
"s_add_u32 s3,s3,s78\n"
"v_mfma_f32_16x16x32_bf16 a[228:231],v[164:167],v[156:159],a[228:231]\n"
"ds_read_b128 v[128:131],v80 offset:32768\n"
"s_add_u32 s3,s3,100909056\n"
"v_mfma_f32_16x16x32_bf16 a[196:199],v[164:167],v[152:155],a[196:199]\n"
"ds_read_b128 v[192:195],v81 offset:32768\n"
"s_waitcnt lgkmcnt(10)\n"
"v_mfma_f32_16x16x32_bf16 a[200:203],v[168:171],v[152:155],a[200:203]\n"
"ds_read_b64_tr_b16 v[224:225],v98 offset:16384\n"
"s_lshl3_add_u32 vcc_hi,vcc_hi,s96\n"
"v_mfma_f32_16x16x32_bf16 a[232:235],v[168:171],v[156:159],a[232:235]\n"
"ds_read_b64_tr_b16 v[226:227],v98 offset:17408\n"
"s_lshl2_add_u32 vcc_hi,vcc_hi,s52\n"
"v_mfma_f32_16x16x32_bf16 a[236:239],v[172:175],v[156:159],a[236:239]\n"
"ds_read_b64_tr_b16 v[228:229],v99 offset:16384\n"
"s_lshl_b32 s2,vcc_hi,4\n"
"v_mfma_f32_16x16x32_bf16 a[204:207],v[172:175],v[152:155],a[204:207]\n"
"ds_read_b64_tr_b16 v[230:231],v99 offset:17408\n"
"s_waitcnt lgkmcnt(10)\n"
"v_mfma_f32_16x16x32_bf16 a[208:211],v[176:179],v[152:155],a[208:211]\n"
"ds_read_b64_tr_b16 v[232:233],v100 offset:16384\n"
"s_add_u32 s2,s2,149028864\n"
"v_mfma_f32_16x16x32_bf16 a[240:243],v[176:179],v[156:159],a[240:243]\n"
"ds_read_b64_tr_b16 v[234:235],v100 offset:17408\n"
"s_lshl2_add_u32 vcc_lo,s95,s52\n"
"v_mfma_f32_16x16x32_bf16 a[244:247],v[180:183],v[156:159],a[244:247]\n"
"ds_read_b64_tr_b16 v[236:237],v101 offset:16384\n"
"s_lshl_b32 vcc_lo,vcc_lo,10\n"
"v_mfma_f32_16x16x32_bf16 a[212:215],v[180:183],v[152:155],a[212:215]\n"
"ds_read_b64_tr_b16 v[238:239],v101 offset:17408\n"
"s_waitcnt lgkmcnt(10)\n"
"v_mfma_f32_16x16x32_bf16 a[216:219],v[184:187],v[152:155],a[216:219]\n"
"ds_read_b64_tr_b16 v[240:241],v102 offset:16384\n"
"s_lshl_b32 vcc_hi,s94,22\n"
"v_mfma_f32_16x16x32_bf16 a[248:251],v[184:187],v[156:159],a[248:251]\n"
"ds_read_b64_tr_b16 v[242:243],v102 offset:17408\n"
"s_add_u32 s12,vcc_lo,vcc_hi\n"
"v_mfma_f32_16x16x32_bf16 a[252:255],v[188:191],v[156:159],a[252:255]\n"
"ds_read_b64_tr_b16 v[244:245],v103 offset:16384\n"
"s_lshl_b32 vcc_lo,s96,19\n"
"v_mfma_f32_16x16x32_bf16 a[220:223],v[188:191],v[152:155],a[220:223]\n"
"ds_read_b64_tr_b16 v[246:247],v103 offset:17408\n"
"s_waitcnt lgkmcnt(8)\n"
"v_mfma_f32_16x16x32_bf16 a[0:3],v[160:163],v[128:131],a[0:3]\n"
"ds_read_b64_tr_b16 v[248:249],v104 offset:16384\n"
"s_add_u32 s10,s12,vcc_lo\n"
"v_mfma_f32_16x16x32_bf16 a[0:3],v[224:227],v[192:195],a[0:3]\n"
"ds_read_b64_tr_b16 v[250:251],v104 offset:17408\n"
"s_add_u32 s10,s10,71303168\n"
"v_mfma_f32_16x16x32_bf16 a[4:7],v[228:231],v[192:195],a[4:7]\n"
"ds_read_b64_tr_b16 v[252:253],v105 offset:16384\n"
"s_mul_i32 vcc_lo,s52,31744\n"
"v_mfma_f32_16x16x32_bf16 a[4:7],v[164:167],v[128:131],a[4:7]\n"
"ds_read_b64_tr_b16 v[254:255],v105 offset:17408\n"
"s_waitcnt lgkmcnt(8)\n"
"v_mfma_f32_16x16x32_bf16 a[8:11],v[168:171],v[128:131],a[8:11]\n"
"ds_read_b128 v[132:135],v80 offset:34816\n"
"s_add_u32 s10,s10,vcc_lo\n"
"v_mfma_f32_16x16x32_bf16 a[8:11],v[232:235],v[192:195],a[8:11]\n"
"ds_read_b128 v[196:199],v81 offset:34816\n"
"s_lshl_b32 vcc_lo,s52,10\n"
"v_mfma_f32_16x16x32_bf16 a[12:15],v[236:239],v[192:195],a[12:15]\n"
"ds_read_b128 v[136:139],v80 offset:36864\n"
"s_sub_u32 s12,57671680,vcc_lo\n"
"v_mfma_f32_16x16x32_bf16 a[12:15],v[172:175],v[128:131],a[12:15]\n"
"ds_read_b128 v[200:203],v81 offset:36864\n"
"s_waitcnt lgkmcnt(8)\n"
"v_mfma_f32_16x16x32_bf16 a[16:19],v[176:179],v[128:131],a[16:19]\n"
"ds_read_b128 v[140:143],v80 offset:38912\n"
"s_add_u32 s8,s79,54525952\n"
"v_mfma_f32_16x16x32_bf16 a[16:19],v[240:243],v[192:195],a[16:19]\n"
"ds_read_b128 v[204:207],v81 offset:38912\n"
"s_and_b32 vcc_lo,s96,1\n"
"v_mfma_f32_16x16x32_bf16 a[20:23],v[244:247],v[192:195],a[20:23]\n"
"ds_read_b128 v[144:147],v80 offset:40960\n"
"s_lshl_b32 s11,vcc_lo,10\n"
"v_mfma_f32_16x16x32_bf16 a[20:23],v[180:183],v[128:131],a[20:23]\n"
"ds_read_b128 v[208:211],v81 offset:40960\n"
"s_waitcnt lgkmcnt(8)\n"
"v_mfma_f32_16x16x32_bf16 a[24:27],v[184:187],v[128:131],a[24:27]\n"
"ds_read_b128 v[148:151],v80 offset:43008\n"
"s_add_u32 s11,s11,148932608\n"
"v_mfma_f32_16x16x32_bf16 a[24:27],v[248:251],v[192:195],a[24:27]\n"
"ds_read_b128 v[212:215],v81 offset:43008\n"
"s_mov_b32 s9,148931072\n"
"v_mfma_f32_16x16x32_bf16 a[28:31],v[252:255],v[192:195],a[28:31]\n"
"s_cmp_eq_u32 s52,0\n"
"ds_read_b128 v[216:219],v81 offset:45056\n"
"s_cselect_b32 s71,s71,2146435072\n"
"v_mfma_f32_16x16x32_bf16 a[28:31],v[188:191],v[128:131],a[28:31]\n"
"ds_read_b128 v[220:223],v81 offset:47104\n"
"s_waitcnt lgkmcnt(10)\n"
"v_mfma_f32_16x16x32_bf16 a[32:35],v[160:163],v[132:135],a[32:35]\n"
"v_mfma_f32_16x16x32_bf16 a[32:35],v[224:227],v[196:199],a[32:35]\n"
"v_mfma_f32_16x16x32_bf16 a[36:39],v[228:231],v[196:199],a[36:39]\n"
"v_mfma_f32_16x16x32_bf16 a[36:39],v[164:167],v[132:135],a[36:39]\n"
"v_mfma_f32_16x16x32_bf16 a[40:43],v[168:171],v[132:135],a[40:43]\n"
"s_waitcnt lgkmcnt(0)\n"
"s_barrier\n"
"v_mfma_f32_16x16x32_bf16 a[40:43],v[232:235],v[196:199],a[40:43]\n"
"s_mov_b32 s77,1572864\n"
"s_add_u32 m0,s78,98304\n"
"v_mfma_f32_16x16x32_bf16 a[44:47],v[236:239],v[196:199],a[44:47]\n"
"buffer_load_dwordx4 v110,s[20:23],s77 offen offset:0 lds\n"
"v_mfma_f32_16x16x32_bf16 a[44:47],v[172:175],v[132:135],a[44:47]\n"
"v_mfma_f32_16x16x32_bf16 a[48:51],v[176:179],v[132:135],a[48:51]\n"
"v_mfma_f32_16x16x32_bf16 a[48:51],v[240:243],v[196:199],a[48:51]\n"
"v_mfma_f32_16x16x32_bf16 a[52:55],v[244:247],v[196:199],a[52:55]\n"
"buffer_load_dwordx4 v111,s[20:23],s77 offen offset:1024 lds\n"
"v_mfma_f32_16x16x32_bf16 a[52:55],v[180:183],v[132:135],a[52:55]\n"
"v_mfma_f32_16x16x32_bf16 a[56:59],v[184:187],v[132:135],a[56:59]\n"
"v_mfma_f32_16x16x32_bf16 a[56:59],v[248:251],v[196:199],a[56:59]\n"
"v_mfma_f32_16x16x32_bf16 a[60:63],v[252:255],v[196:199],a[60:63]\n"
"buffer_load_dwordx4 v113,s[20:23],s77 offen offset:2048 lds\n"
"v_mfma_f32_16x16x32_bf16 a[60:63],v[188:191],v[132:135],a[60:63]\n"
"v_mfma_f32_16x16x32_bf16 a[64:67],v[160:163],v[136:139],a[64:67]\n"
"v_mfma_f32_16x16x32_bf16 a[64:67],v[224:227],v[200:203],a[64:67]\n"
"v_mfma_f32_16x16x32_bf16 a[68:71],v[228:231],v[200:203],a[68:71]\n"
"buffer_load_dwordx4 v114,s[20:23],s77 offen offset:3072 lds\n"
"v_mfma_f32_16x16x32_bf16 a[68:71],v[164:167],v[136:139],a[68:71]\n"
"v_mfma_f32_16x16x32_bf16 a[72:75],v[168:171],v[136:139],a[72:75]\n"
"v_mfma_f32_16x16x32_bf16 a[72:75],v[232:235],v[200:203],a[72:75]\n"
"s_mov_b32 s77,1835008\n"
"s_add_u32 m0,s78,114688\n"
"v_mfma_f32_16x16x32_bf16 a[76:79],v[236:239],v[200:203],a[76:79]\n"
"buffer_load_dwordx4 v110,s[20:23],s77 offen offset:0 lds\n"
"v_mfma_f32_16x16x32_bf16 a[76:79],v[172:175],v[136:139],a[76:79]\n"
"v_mfma_f32_16x16x32_bf16 a[80:83],v[176:179],v[136:139],a[80:83]\n"
"v_mfma_f32_16x16x32_bf16 a[80:83],v[240:243],v[200:203],a[80:83]\n"
"v_mfma_f32_16x16x32_bf16 a[84:87],v[244:247],v[200:203],a[84:87]\n"
"buffer_load_dwordx4 v111,s[20:23],s77 offen offset:1024 lds\n"
"v_mfma_f32_16x16x32_bf16 a[84:87],v[180:183],v[136:139],a[84:87]\n"
"v_mfma_f32_16x16x32_bf16 a[88:91],v[184:187],v[136:139],a[88:91]\n"
"v_mfma_f32_16x16x32_bf16 a[88:91],v[248:251],v[200:203],a[88:91]\n"
"v_mfma_f32_16x16x32_bf16 a[92:95],v[252:255],v[200:203],a[92:95]\n"
"buffer_load_dwordx4 v113,s[20:23],s77 offen offset:2048 lds\n"
"v_mfma_f32_16x16x32_bf16 a[92:95],v[188:191],v[136:139],a[92:95]\n"
"s_waitcnt vmcnt(10)\n"
"v_mfma_f32_16x16x32_bf16 a[96:99],v[160:163],v[140:143],a[96:99]\n"
"v_lshlrev_b32 v24,16,v8\n"
"v_and_b32 v25,0xffff0000,v8\n"
"v_mfma_f32_16x16x32_bf16 a[96:99],v[224:227],v[204:207],a[96:99]\n"
"v_lshlrev_b32 v26,16,v9\n"
"v_and_b32 v27,0xffff0000,v9\n"
"v_mfma_f32_16x16x32_bf16 a[100:103],v[228:231],v[204:207],a[100:103]\n"
"buffer_load_dwordx4 v114,s[20:23],s77 offen offset:3072 lds\n"
"v_mfma_f32_16x16x32_bf16 a[100:103],v[164:167],v[140:143],a[100:103]\n"
"v_lshlrev_b32 v28,16,v10\n"
"v_and_b32 v29,0xffff0000,v10\n"
"v_mfma_f32_16x16x32_bf16 a[104:107],v[168:171],v[140:143],a[104:107]\n"
"v_lshlrev_b32 v30,16,v11\n"
"v_and_b32 v31,0xffff0000,v11\n"
"v_mfma_f32_16x16x32_bf16 a[104:107],v[232:235],v[204:207],a[104:107]\n"
"v_lshlrev_b32 v32,16,v12\n"
"v_and_b32 v33,0xffff0000,v12\n"
"v_mfma_f32_16x16x32_bf16 a[108:111],v[236:239],v[204:207],a[108:111]\n"
"s_mov_b32 s76,512\n"
"v_lshlrev_b32 v34,16,v13\n"
"s_add_u32 m0,s79,32768\n"
"v_mfma_f32_16x16x32_bf16 a[108:111],v[172:175],v[140:143],a[108:111]\n"
"buffer_load_dwordx4 v106,s[16:19],s76 offen offset:0 lds\n"
"v_mfma_f32_16x16x32_bf16 a[112:115],v[176:179],v[140:143],a[112:115]\n"
"v_and_b32 v35,0xffff0000,v13\n"
"v_lshlrev_b32 v36,16,v14\n"
"v_mfma_f32_16x16x32_bf16 a[112:115],v[240:243],v[204:207],a[112:115]\n"
"v_and_b32 v37,0xffff0000,v14\n"
"v_lshlrev_b32 v38,16,v15\n"
"v_mfma_f32_16x16x32_bf16 a[116:119],v[244:247],v[204:207],a[116:119]\n"
"v_and_b32 v39,0xffff0000,v15\n"
"v_lshlrev_b32 v40,16,v16\n"
"v_mfma_f32_16x16x32_bf16 a[116:119],v[180:183],v[140:143],a[116:119]\n"
"v_and_b32 v41,0xffff0000,v16\n"
"v_lshlrev_b32 v42,16,v17\n"
"v_mfma_f32_16x16x32_bf16 a[120:123],v[184:187],v[140:143],a[120:123]\n"
"buffer_load_dwordx4 v107,s[16:19],s76 offen offset:1024 lds\n"
"v_mfma_f32_16x16x32_bf16 a[120:123],v[248:251],v[204:207],a[120:123]\n"
"v_and_b32 v43,0xffff0000,v17\n"
"v_lshlrev_b32 v44,16,v18\n"
"v_mfma_f32_16x16x32_bf16 a[124:127],v[252:255],v[204:207],a[124:127]\n"
"v_and_b32 v45,0xffff0000,v18\n"
"v_lshlrev_b32 v46,16,v19\n"
"v_mfma_f32_16x16x32_bf16 a[124:127],v[188:191],v[140:143],a[124:127]\n"
"v_and_b32 v47,0xffff0000,v19\n"
"v_lshlrev_b32 v48,16,v20\n"
"v_mfma_f32_16x16x32_bf16 a[128:131],v[160:163],v[144:147],a[128:131]\n"
"v_and_b32 v49,0xffff0000,v20\n"
"v_lshlrev_b32 v50,16,v21\n"
"v_mfma_f32_16x16x32_bf16 a[128:131],v[224:227],v[208:211],a[128:131]\n"
"buffer_load_dwordx4 v108,s[16:19],s76 offen offset:2048 lds\n"
"v_mfma_f32_16x16x32_bf16 a[132:135],v[228:231],v[208:211],a[132:135]\n"
"v_and_b32 v51,0xffff0000,v21\n"
"v_lshlrev_b32 v52,16,v22\n"
"v_mfma_f32_16x16x32_bf16 a[132:135],v[164:167],v[144:147],a[132:135]\n"
"v_and_b32 v53,0xffff0000,v22\n"
"v_lshlrev_b32 v54,16,v23\n"
"v_mfma_f32_16x16x32_bf16 a[136:139],v[168:171],v[144:147],a[136:139]\n"
"v_and_b32 v55,0xffff0000,v23\n"
"v_add_f32 v56,v24,v25\n"
"v_mfma_f32_16x16x32_bf16 a[136:139],v[232:235],v[208:211],a[136:139]\n"
"v_add_f32 v57,v26,v27\n"
"v_add_f32 v56,v56,v28\n"
"v_mfma_f32_16x16x32_bf16 a[140:143],v[236:239],v[208:211],a[140:143]\n"
"buffer_load_dwordx4 v109,s[16:19],s76 offen offset:3072 lds\n"
"v_mfma_f32_16x16x32_bf16 a[140:143],v[172:175],v[144:147],a[140:143]\n"
"v_add_f32 v57,v57,v29\n"
"v_add_f32 v56,v56,v30\n"
"v_mfma_f32_16x16x32_bf16 a[144:147],v[176:179],v[144:147],a[144:147]\n"
"v_add_f32 v57,v57,v31\n"
"v_add_f32 v56,v56,v32\n"
"v_mfma_f32_16x16x32_bf16 a[144:147],v[240:243],v[208:211],a[144:147]\n"
"v_readfirstlane_b32 s82,v78\n"
"v_readfirstlane_b32 s83,v79\n"
"v_mfma_f32_16x16x32_bf16 a[148:151],v[244:247],v[208:211],a[148:151]\n"
"s_mov_b32 s76,262656\n"
"v_add_f32 v57,v57,v33\n"
"s_add_u32 m0,s79,36864\n"
"v_mfma_f32_16x16x32_bf16 a[148:151],v[180:183],v[144:147],a[148:151]\n"
"buffer_load_dwordx4 v106,s[16:19],s76 offen offset:0 lds\n"
"s_mul_i32 s80,s82,0x9e3779b1\n"
"v_mfma_f32_16x16x32_bf16 a[152:155],v[184:187],v[144:147],a[152:155]\n"
"s_add_u32 s80,s80,0x7f4a7c15\n"
"v_add_f32 v56,v56,v34\n"
"v_add_f32 v57,v57,v35\n"
"v_mfma_f32_16x16x32_bf16 a[152:155],v[248:251],v[208:211],a[152:155]\n"
"s_add_u32 s81,s83,s80\n"
"s_add_u32 s81,s81,1\n"
"v_mfma_f32_16x16x32_bf16 a[156:159],v[252:255],v[208:211],a[156:159]\n"
"v_mov_b32 v124,s80\n"
"v_mov_b32 v125,s81\n"
"v_mfma_f32_16x16x32_bf16 a[156:159],v[188:191],v[144:147],a[156:159]\n"
"v_add_f32 v56,v56,v36\n"
"v_add_f32 v57,v57,v37\n"
"v_mfma_f32_16x16x32_bf16 a[160:163],v[160:163],v[148:151],a[160:163]\n"
"buffer_load_dwordx4 v107,s[16:19],s76 offen offset:1024 lds\n"
"v_mfma_f32_16x16x32_bf16 a[160:163],v[224:227],v[212:215],a[160:163]\n"
"v_add_f32 v56,v56,v38\n"
"v_add_f32 v57,v57,v39\n"
"v_mfma_f32_16x16x32_bf16 a[164:167],v[228:231],v[212:215],a[164:167]\n"
"v_add_f32 v56,v56,v40\n"
"v_add_f32 v57,v57,v41\n"
"v_mfma_f32_16x16x32_bf16 a[164:167],v[164:167],v[148:151],a[164:167]\n"
"v_add_f32 v56,v56,v42\n"
"v_add_f32 v57,v57,v43\n"
"v_mfma_f32_16x16x32_bf16 a[168:171],v[168:171],v[148:151],a[168:171]\n"
"v_add_f32 v56,v56,v44\n"
"v_add_f32 v57,v57,v45\n"
"v_mfma_f32_16x16x32_bf16 a[168:171],v[232:235],v[212:215],a[168:171]\n"
"buffer_load_dwordx4 v108,s[16:19],s76 offen offset:2048 lds\n"
"v_mfma_f32_16x16x32_bf16 a[172:175],v[236:239],v[212:215],a[172:175]\n"
"v_add_f32 v56,v56,v46\n"
"v_add_f32 v57,v57,v47\n"
"v_mfma_f32_16x16x32_bf16 a[172:175],v[172:175],v[148:151],a[172:175]\n"
"v_add_f32 v56,v56,v48\n"
"v_add_f32 v57,v57,v49\n"
"v_mfma_f32_16x16x32_bf16 a[176:179],v[176:179],v[148:151],a[176:179]\n"
"v_add_f32 v56,v56,v50\n"
"v_add_f32 v57,v57,v51\n"
"v_mfma_f32_16x16x32_bf16 a[176:179],v[240:243],v[212:215],a[176:179]\n"
"v_add_f32 v56,v56,v52\n"
"v_add_f32 v57,v57,v53\n"
"v_mfma_f32_16x16x32_bf16 a[180:183],v[244:247],v[212:215],a[180:183]\n"
"buffer_load_dwordx4 v109,s[16:19],s76 offen offset:3072 lds\n"
"v_mfma_f32_16x16x32_bf16 a[180:183],v[180:183],v[148:151],a[180:183]\n"
"s_mov_b32 s87,2097152\n"
"v_add_f32 v56,v56,v54\n"
"v_add_f32 v57,v57,v55\n"
"v_mfma_f32_16x16x32_bf16 a[184:187],v[184:187],v[148:151],a[184:187]\n"
"buffer_load_dword v115,v112,s[36:39],s87 offen\n"
"v_mfma_f32_16x16x32_bf16 a[184:187],v[248:251],v[212:215],a[184:187]\n"
"s_mov_b32 s87,2621440\n"
"v_add_f32 v56,v56,v57\n"
"v_mfma_f32_16x16x32_bf16 a[188:191],v[252:255],v[212:215],a[188:191]\n"
"buffer_load_dword v115,v112,s[36:39],s87 offen\n"
"v_add_f32_dpp v56,v56,v56 quad_perm:[1,0,3,2] row_mask:0xf bank_mask:0xf\n"
"v_mfma_f32_16x16x32_bf16 a[188:191],v[188:191],v[148:151],a[188:191]\n"
"s_barrier\n"
"v_mfma_f32_16x16x32_bf16 a[192:195],v[224:227],v[216:219],a[192:195]\n"
"ds_read_b128 v[152:155],v82 offset:12288\n"
"v_mfma_f32_16x16x32_bf16 a[224:227],v[224:227],v[220:223],a[224:227]\n"
"ds_read_b128 v[156:159],v82 offset:14336\n"
"v_add_f32_dpp v56,v56,v56 quad_perm:[2,3,0,1] row_mask:0xf bank_mask:0xf\n"
"v_mfma_f32_16x16x32_bf16 a[228:231],v[228:231],v[220:223],a[228:231]\n"
"ds_read_b64_tr_b16 v[160:161],v98 offset:49152\n"
"v_mfma_f32_16x16x32_bf16 a[196:199],v[228:231],v[216:219],a[196:199]\n"
"ds_read_b64_tr_b16 v[162:163],v98 offset:50176\n"
"v_add_f32_dpp v56,v56,v56 row_ror:8 row_mask:0xf bank_mask:0xf\n"
"v_mfma_f32_16x16x32_bf16 a[200:203],v[232:235],v[216:219],a[200:203]\n"
"ds_read_b64_tr_b16 v[164:165],v99 offset:49152\n"
"v_mov_b32 v57,v56\n"
"v_mfma_f32_16x16x32_bf16 a[232:235],v[232:235],v[220:223],a[232:235]\n"
"ds_read_b64_tr_b16 v[166:167],v99 offset:50176\n"
"v_mfma_f32_16x16x32_bf16 a[236:239],v[236:239],v[220:223],a[236:239]\n"
"ds_read_b64_tr_b16 v[168:169],v100 offset:49152\n"
"v_permlane16_swap_b32 v57,v56\n"
"v_mfma_f32_16x16x32_bf16 a[204:207],v[236:239],v[216:219],a[204:207]\n"
"ds_read_b64_tr_b16 v[170:171],v100 offset:50176\n"
"v_add_f32 v56,v56,v57\n"
"v_mfma_f32_16x16x32_bf16 a[208:211],v[240:243],v[216:219],a[208:211]\n"
"ds_read_b64_tr_b16 v[172:173],v101 offset:49152\n"
"v_mov_b32 v57,v56\n"
"v_mfma_f32_16x16x32_bf16 a[240:243],v[240:243],v[220:223],a[240:243]\n"
"ds_read_b64_tr_b16 v[174:175],v101 offset:50176\n"
"v_mfma_f32_16x16x32_bf16 a[244:247],v[244:247],v[220:223],a[244:247]\n"
"ds_read_b64_tr_b16 v[176:177],v102 offset:49152\n"
"v_permlane32_swap_b32 v57,v56\n"
"v_mfma_f32_16x16x32_bf16 a[212:215],v[244:247],v[216:219],a[212:215]\n"
"ds_read_b64_tr_b16 v[178:179],v102 offset:50176\n"
"v_add_f32 v56,v56,v57\n"
"v_mfma_f32_16x16x32_bf16 a[216:219],v[248:251],v[216:219],a[216:219]\n"
"ds_read_b64_tr_b16 v[180:181],v103 offset:49152\n"
"v_mul_f32 v58,0x3a800000,v56\n"
"v_mfma_f32_16x16x32_bf16 a[248:251],v[248:251],v[220:223],a[248:251]\n"
"ds_read_b64_tr_b16 v[182:183],v103 offset:50176\n"
"v_sub_f32 v62,v24,v58\n"
"v_mfma_f32_16x16x32_bf16 a[252:255],v[252:255],v[220:223],a[252:255]\n"
"ds_read_b64_tr_b16 v[184:185],v104 offset:49152\n"
"v_mul_f32 v59,v62,v62\n"
"v_mfma_f32_16x16x32_bf16 a[220:223],v[252:255],v[216:219],a[220:223]\n"
"ds_read_b64_tr_b16 v[186:187],v104 offset:50176\n"
"v_sub_f32 v63,v25,v58\n"
"s_waitcnt lgkmcnt(10)\n"
"v_mfma_f32_16x16x32_bf16 a[192:195],v[160:163],v[152:155],a[192:195]\n"
"ds_read_b64_tr_b16 v[188:189],v105 offset:49152\n"
"v_mul_f32 v60,v63,v63\n"
"v_mfma_f32_16x16x32_bf16 a[224:227],v[160:163],v[156:159],a[224:227]\n"
"ds_read_b64_tr_b16 v[190:191],v105 offset:50176\n"
"v_sub_f32 v64,v26,v58\n"
"v_mfma_f32_16x16x32_bf16 a[228:231],v[164:167],v[156:159],a[228:231]\n"
"ds_read_b128 v[128:131],v82 offset:0\n"
"v_fmac_f32 v59,v64,v64\n"
"v_mfma_f32_16x16x32_bf16 a[196:199],v[164:167],v[152:155],a[196:199]\n"
"ds_read_b128 v[192:195],v83 offset:0\n"
"s_waitcnt lgkmcnt(10)\n"
"v_mfma_f32_16x16x32_bf16 a[200:203],v[168:171],v[152:155],a[200:203]\n"
"ds_read_b64_tr_b16 v[224:225],v98 offset:32768\n"
"v_sub_f32 v65,v27,v58\n"
"v_mfma_f32_16x16x32_bf16 a[232:235],v[168:171],v[156:159],a[232:235]\n"
"ds_read_b64_tr_b16 v[226:227],v98 offset:33792\n"
"v_fmac_f32 v60,v65,v65\n"
"v_mfma_f32_16x16x32_bf16 a[236:239],v[172:175],v[156:159],a[236:239]\n"
"ds_read_b64_tr_b16 v[228:229],v99 offset:32768\n"
"v_sub_f32 v62,v28,v58\n"
"v_mfma_f32_16x16x32_bf16 a[204:207],v[172:175],v[152:155],a[204:207]\n"
"ds_read_b64_tr_b16 v[230:231],v99 offset:33792\n"
"s_waitcnt lgkmcnt(10)\n"
"v_mfma_f32_16x16x32_bf16 a[208:211],v[176:179],v[152:155],a[208:211]\n"
"ds_read_b64_tr_b16 v[232:233],v100 offset:32768\n"
"v_fmac_f32 v59,v62,v62\n"
"v_mfma_f32_16x16x32_bf16 a[240:243],v[176:179],v[156:159],a[240:243]\n"
"ds_read_b64_tr_b16 v[234:235],v100 offset:33792\n"
"v_sub_f32 v63,v29,v58\n"
"v_mfma_f32_16x16x32_bf16 a[244:247],v[180:183],v[156:159],a[244:247]\n"
"ds_read_b64_tr_b16 v[236:237],v101 offset:32768\n"
"v_fmac_f32 v60,v63,v63\n"
"v_mfma_f32_16x16x32_bf16 a[212:215],v[180:183],v[152:155],a[212:215]\n"
"ds_read_b64_tr_b16 v[238:239],v101 offset:33792\n"
"s_waitcnt lgkmcnt(10)\n"
"v_mfma_f32_16x16x32_bf16 a[216:219],v[184:187],v[152:155],a[216:219]\n"
"ds_read_b64_tr_b16 v[240:241],v102 offset:32768\n"
"v_sub_f32 v64,v30,v58\n"
"v_mfma_f32_16x16x32_bf16 a[248:251],v[184:187],v[156:159],a[248:251]\n"
"ds_read_b64_tr_b16 v[242:243],v102 offset:33792\n"
"v_fmac_f32 v59,v64,v64\n"
"v_mfma_f32_16x16x32_bf16 a[252:255],v[188:191],v[156:159],a[252:255]\n"
"ds_read_b64_tr_b16 v[244:245],v103 offset:32768\n"
"v_sub_f32 v65,v31,v58\n"
"v_mfma_f32_16x16x32_bf16 a[220:223],v[188:191],v[152:155],a[220:223]\n"
"ds_read_b64_tr_b16 v[246:247],v103 offset:33792\n"
"s_waitcnt lgkmcnt(8)\n"
"v_mfma_f32_16x16x32_bf16 a[0:3],v[160:163],v[128:131],a[0:3]\n"
"ds_read_b64_tr_b16 v[248:249],v104 offset:32768\n"
"v_fmac_f32 v60,v65,v65\n"
"v_mfma_f32_16x16x32_bf16 a[0:3],v[224:227],v[192:195],a[0:3]\n"
"ds_read_b64_tr_b16 v[250:251],v104 offset:33792\n"
"v_sub_f32 v62,v32,v58\n"
"v_mfma_f32_16x16x32_bf16 a[4:7],v[228:231],v[192:195],a[4:7]\n"
"ds_read_b64_tr_b16 v[252:253],v105 offset:32768\n"
"v_fmac_f32 v59,v62,v62\n"
"v_mfma_f32_16x16x32_bf16 a[4:7],v[164:167],v[128:131],a[4:7]\n"
"ds_read_b64_tr_b16 v[254:255],v105 offset:33792\n"
"s_waitcnt lgkmcnt(8)\n"
"v_mfma_f32_16x16x32_bf16 a[8:11],v[168:171],v[128:131],a[8:11]\n"
"ds_read_b128 v[132:135],v82 offset:2048\n"
"v_sub_f32 v63,v33,v58\n"
"v_mfma_f32_16x16x32_bf16 a[8:11],v[232:235],v[192:195],a[8:11]\n"
"ds_read_b128 v[196:199],v83 offset:2048\n"
"v_fmac_f32 v60,v63,v63\n"
"v_mfma_f32_16x16x32_bf16 a[12:15],v[236:239],v[192:195],a[12:15]\n"
"ds_read_b128 v[136:139],v82 offset:4096\n"
"v_sub_f32 v64,v34,v58\n"
"v_mfma_f32_16x16x32_bf16 a[12:15],v[172:175],v[128:131],a[12:15]\n"
"ds_read_b128 v[200:203],v83 offset:4096\n"
"s_waitcnt lgkmcnt(8)\n"
"v_mfma_f32_16x16x32_bf16 a[16:19],v[176:179],v[128:131],a[16:19]\n"
"ds_read_b128 v[140:143],v82 offset:6144\n"
"v_fmac_f32 v59,v64,v64\n"
"v_mfma_f32_16x16x32_bf16 a[16:19],v[240:243],v[192:195],a[16:19]\n"
"ds_read_b128 v[204:207],v83 offset:6144\n"
"v_sub_f32 v65,v35,v58\n"
"v_mfma_f32_16x16x32_bf16 a[20:23],v[244:247],v[192:195],a[20:23]\n"
"ds_read_b128 v[144:147],v82 offset:8192\n"
"v_fmac_f32 v60,v65,v65\n"
"v_mfma_f32_16x16x32_bf16 a[20:23],v[180:183],v[128:131],a[20:23]\n"
"ds_read_b128 v[208:211],v83 offset:8192\n"
"s_waitcnt lgkmcnt(8)\n"
"v_mfma_f32_16x16x32_bf16 a[24:27],v[184:187],v[128:131],a[24:27]\n"
"ds_read_b128 v[148:151],v82 offset:10240\n"
"v_sub_f32 v62,v36,v58\n"
"v_mfma_f32_16x16x32_bf16 a[24:27],v[248:251],v[192:195],a[24:27]\n"
"ds_read_b128 v[212:215],v83 offset:10240\n"
"v_fmac_f32 v59,v62,v62\n"
"v_mfma_f32_16x16x32_bf16 a[28:31],v[252:255],v[192:195],a[28:31]\n"
"ds_read_b128 v[216:219],v83 offset:12288\n"
"v_sub_f32 v63,v37,v58\n"
"v_mfma_f32_16x16x32_bf16 a[28:31],v[188:191],v[128:131],a[28:31]\n"
"ds_read_b128 v[220:223],v83 offset:14336\n"
"s_waitcnt lgkmcnt(10)\n"
"v_mfma_f32_16x16x32_bf16 a[32:35],v[160:163],v[132:135],a[32:35]\n"
"v_fmac_f32 v60,v63,v63\n"
"v_sub_f32 v64,v38,v58\n"
"v_mfma_f32_16x16x32_bf16 a[32:35],v[224:227],v[196:199],a[32:35]\n"
"v_fmac_f32 v59,v64,v64\n"
"v_sub_f32 v65,v39,v58\n"
"v_mfma_f32_16x16x32_bf16 a[36:39],v[228:231],v[196:199],a[36:39]\n"
"v_fmac_f32 v60,v65,v65\n"
"v_sub_f32 v62,v40,v58\n"
"v_mfma_f32_16x16x32_bf16 a[36:39],v[164:167],v[132:135],a[36:39]\n"
"v_fmac_f32 v59,v62,v62\n"
"v_sub_f32 v63,v41,v58\n"
"v_mfma_f32_16x16x32_bf16 a[40:43],v[168:171],v[132:135],a[40:43]\n"
"s_waitcnt lgkmcnt(0)\n"
"s_barrier\n"
"v_mfma_f32_16x16x32_bf16 a[40:43],v[232:235],v[196:199],a[40:43]\n"
"s_mov_b32 s77,2097152\n"
"v_fmac_f32 v60,v63,v63\n"
"s_add_u32 m0,s78,147456\n"
"v_mfma_f32_16x16x32_bf16 a[44:47],v[236:239],v[196:199],a[44:47]\n"
"buffer_load_dwordx4 v110,s[20:23],s77 offen offset:0 lds\n"
"v_mfma_f32_16x16x32_bf16 a[44:47],v[172:175],v[132:135],a[44:47]\n"
"v_sub_f32 v64,v42,v58\n"
"v_fmac_f32 v59,v64,v64\n"
"v_mfma_f32_16x16x32_bf16 a[48:51],v[176:179],v[132:135],a[48:51]\n"
"v_sub_f32 v65,v43,v58\n"
"v_fmac_f32 v60,v65,v65\n"
"v_mfma_f32_16x16x32_bf16 a[48:51],v[240:243],v[196:199],a[48:51]\n"
"v_sub_f32 v62,v44,v58\n"
"v_fmac_f32 v59,v62,v62\n"
"v_mfma_f32_16x16x32_bf16 a[52:55],v[244:247],v[196:199],a[52:55]\n"
"buffer_load_dwordx4 v111,s[20:23],s77 offen offset:1024 lds\n"
"v_mfma_f32_16x16x32_bf16 a[52:55],v[180:183],v[132:135],a[52:55]\n"
"v_sub_f32 v63,v45,v58\n"
"v_fmac_f32 v60,v63,v63\n"
"v_mfma_f32_16x16x32_bf16 a[56:59],v[184:187],v[132:135],a[56:59]\n"
"v_sub_f32 v64,v46,v58\n"
"v_fmac_f32 v59,v64,v64\n"
"v_mfma_f32_16x16x32_bf16 a[56:59],v[248:251],v[196:199],a[56:59]\n"
"v_sub_f32 v65,v47,v58\n"
"v_fmac_f32 v60,v65,v65\n"
"v_mfma_f32_16x16x32_bf16 a[60:63],v[252:255],v[196:199],a[60:63]\n"
"buffer_load_dwordx4 v113,s[20:23],s77 offen offset:2048 lds\n"
"v_mfma_f32_16x16x32_bf16 a[60:63],v[188:191],v[132:135],a[60:63]\n"
"v_sub_f32 v62,v48,v58\n"
"v_fmac_f32 v59,v62,v62\n"
"v_mfma_f32_16x16x32_bf16 a[64:67],v[160:163],v[136:139],a[64:67]\n"
"v_sub_f32 v63,v49,v58\n"
"v_fmac_f32 v60,v63,v63\n"
"v_mfma_f32_16x16x32_bf16 a[64:67],v[224:227],v[200:203],a[64:67]\n"
"v_sub_f32 v64,v50,v58\n"
"v_fmac_f32 v59,v64,v64\n"
"v_mfma_f32_16x16x32_bf16 a[68:71],v[228:231],v[200:203],a[68:71]\n"
"buffer_load_dwordx4 v114,s[20:23],s77 offen offset:3072 lds\n"
"v_mfma_f32_16x16x32_bf16 a[68:71],v[164:167],v[136:139],a[68:71]\n"
"v_sub_f32 v65,v51,v58\n"
"v_fmac_f32 v60,v65,v65\n"
"v_mfma_f32_16x16x32_bf16 a[72:75],v[168:171],v[136:139],a[72:75]\n"
"v_sub_f32 v62,v52,v58\n"
"v_fmac_f32 v59,v62,v62\n"
"v_mfma_f32_16x16x32_bf16 a[72:75],v[232:235],v[200:203],a[72:75]\n"
"s_mov_b32 s77,2359296\n"
"v_sub_f32 v63,v53,v58\n"
"s_add_u32 m0,s78,131072\n"
"v_mfma_f32_16x16x32_bf16 a[76:79],v[236:239],v[200:203],a[76:79]\n"
"buffer_load_dwordx4 v110,s[20:23],s77 offen offset:0 lds\n"
"v_mfma_f32_16x16x32_bf16 a[76:79],v[172:175],v[136:139],a[76:79]\n"
"v_fmac_f32 v60,v63,v63\n"
"v_sub_f32 v64,v54,v58\n"
"v_mfma_f32_16x16x32_bf16 a[80:83],v[176:179],v[136:139],a[80:83]\n"
"v_fmac_f32 v59,v64,v64\n"
"v_sub_f32 v65,v55,v58\n"
"v_mfma_f32_16x16x32_bf16 a[80:83],v[240:243],v[200:203],a[80:83]\n"
"v_fmac_f32 v60,v65,v65\n"
"v_add_f32 v59,v59,v60\n"
"v_mfma_f32_16x16x32_bf16 a[84:87],v[244:247],v[200:203],a[84:87]\n"
"buffer_load_dwordx4 v111,s[20:23],s77 offen offset:1024 lds\n"
"v_mfma_f32_16x16x32_bf16 a[84:87],v[180:183],v[136:139],a[84:87]\n"
"v_mfma_f32_16x16x32_bf16 a[88:91],v[184:187],v[136:139],a[88:91]\n"
"v_add_f32_dpp v59,v59,v59 quad_perm:[1,0,3,2] row_mask:0xf bank_mask:0xf\n"
"v_mfma_f32_16x16x32_bf16 a[88:91],v[248:251],v[200:203],a[88:91]\n"
"s_nop 0\n"
"v_add_f32_dpp v59,v59,v59 quad_perm:[2,3,0,1] row_mask:0xf bank_mask:0xf\n"
"v_mfma_f32_16x16x32_bf16 a[92:95],v[252:255],v[200:203],a[92:95]\n"
"buffer_load_dwordx4 v113,s[20:23],s77 offen offset:2048 lds\n"
"v_mfma_f32_16x16x32_bf16 a[92:95],v[188:191],v[136:139],a[92:95]\n"
"v_add_f32_dpp v59,v59,v59 row_ror:8 row_mask:0xf bank_mask:0xf\n"
"v_mov_b32 v57,v59\n"
"v_mfma_f32_16x16x32_bf16 a[96:99],v[160:163],v[140:143],a[96:99]\n"
"v_mfma_f32_16x16x32_bf16 a[96:99],v[224:227],v[204:207],a[96:99]\n"
"v_permlane16_swap_b32 v57,v59\n"
"v_add_f32 v59,v59,v57\n"
"v_mfma_f32_16x16x32_bf16 a[100:103],v[228:231],v[204:207],a[100:103]\n"
"buffer_load_dwordx4 v114,s[20:23],s77 offen offset:3072 lds\n"
"v_mfma_f32_16x16x32_bf16 a[100:103],v[164:167],v[140:143],a[100:103]\n"
"v_mov_b32 v57,v59\n"
"v_mfma_f32_16x16x32_bf16 a[104:107],v[168:171],v[140:143],a[104:107]\n"
"s_nop 0\n"
"v_permlane32_swap_b32 v57,v59\n"
"v_add_f32 v59,v59,v57\n"
"v_mfma_f32_16x16x32_bf16 a[104:107],v[232:235],v[204:207],a[104:107]\n"
"v_mul_f32 v59,0x3a800000,v59\n"
"v_cvt_pk_bf16_f32 v59,v59,v59\n"
"v_mfma_f32_16x16x32_bf16 a[108:111],v[236:239],v[204:207],a[108:111]\n"
"s_mov_b32 s76,640\n"
"v_lshlrev_b32 v59,16,v59\n"
"s_add_u32 m0,s79,65536\n"
"v_mfma_f32_16x16x32_bf16 a[108:111],v[172:175],v[140:143],a[108:111]\n"
"buffer_load_dwordx4 v106,s[16:19],s76 offen offset:0 lds\n"
"v_mfma_f32_16x16x32_bf16 a[112:115],v[176:179],v[140:143],a[112:115]\n"
"v_add_f32 v59,0x3727c5ac,v59\n"
"v_cvt_pk_bf16_f32 v59,v59,v59\n"
"v_mfma_f32_16x16x32_bf16 a[112:115],v[240:243],v[204:207],a[112:115]\n"
"v_lshlrev_b32 v59,16,v59\n"
"v_sqrt_f32 v59,v59\n"
"v_mfma_f32_16x16x32_bf16 a[116:119],v[244:247],v[204:207],a[116:119]\n"
"v_cvt_pk_bf16_f32 v58,v58,v58\n"
"v_lshlrev_b32 v58,16,v58\n"
"v_mfma_f32_16x16x32_bf16 a[116:119],v[180:183],v[140:143],a[116:119]\n"
"v_cvt_pk_bf16_f32 v59,v59,v59\n"
"v_lshlrev_b32 v59,16,v59\n"
"v_mfma_f32_16x16x32_bf16 a[120:123],v[184:187],v[140:143],a[120:123]\n"
"buffer_load_dwordx4 v107,s[16:19],s76 offen offset:1024 lds\n"
"v_mfma_f32_16x16x32_bf16 a[120:123],v[248:251],v[204:207],a[120:123]\n"
"v_rcp_f32 v66,v59\n"
"v_mfma_f32_16x16x32_bf16 a[124:127],v[252:255],v[204:207],a[124:127]\n"
"v_mul_f32 v66,0x3e0293ee,v66\n"
"v_mul_f32 v58,v58,v66\n"
"v_mfma_f32_16x16x32_bf16 a[124:127],v[188:191],v[140:143],a[124:127]\n"
"v_fma_f32 v24,v24,v66,-v58\n"
"v_fma_f32 v25,v25,v66,-v58\n"
"v_mfma_f32_16x16x32_bf16 a[128:131],v[160:163],v[144:147],a[128:131]\n"
"v_cvt_pk_bf16_f32 v8,v24,v25\n"
"v_fma_f32 v26,v26,v66,-v58\n"
"v_mfma_f32_16x16x32_bf16 a[128:131],v[224:227],v[208:211],a[128:131]\n"
"buffer_load_dwordx4 v108,s[16:19],s76 offen offset:2048 lds\n"
"v_mfma_f32_16x16x32_bf16 a[132:135],v[228:231],v[208:211],a[132:135]\n"
"v_fma_f32 v27,v27,v66,-v58\n"
"v_cvt_pk_bf16_f32 v9,v26,v27\n"
"v_mfma_f32_16x16x32_bf16 a[132:135],v[164:167],v[144:147],a[132:135]\n"
"v_fma_f32 v28,v28,v66,-v58\n"
"v_fma_f32 v29,v29,v66,-v58\n"
"v_mfma_f32_16x16x32_bf16 a[136:139],v[168:171],v[144:147],a[136:139]\n"
"v_cvt_pk_bf16_f32 v10,v28,v29\n"
"v_fma_f32 v30,v30,v66,-v58\n"
"v_mfma_f32_16x16x32_bf16 a[136:139],v[232:235],v[208:211],a[136:139]\n"
"v_fma_f32 v31,v31,v66,-v58\n"
"v_cvt_pk_bf16_f32 v11,v30,v31\n"
"v_mfma_f32_16x16x32_bf16 a[140:143],v[236:239],v[208:211],a[140:143]\n"
"buffer_load_dwordx4 v109,s[16:19],s76 offen offset:3072 lds\n"
"v_mfma_f32_16x16x32_bf16 a[140:143],v[172:175],v[144:147],a[140:143]\n"
"buffer_store_dwordx4 v[8:11],v71,s[24:27],s54 offen sc1\n"
"v_mfma_f32_16x16x32_bf16 a[144:147],v[176:179],v[144:147],a[144:147]\n"
"s_add_u32 s54,s54,131072\n"
"v_fma_f32 v32,v32,v66,-v58\n"
"v_fma_f32 v33,v33,v66,-v58\n"
"v_mfma_f32_16x16x32_bf16 a[144:147],v[240:243],v[208:211],a[144:147]\n"
"v_cvt_pk_bf16_f32 v12,v32,v33\n"
"v_fma_f32 v34,v34,v66,-v58\n"
"v_mfma_f32_16x16x32_bf16 a[148:151],v[244:247],v[208:211],a[148:151]\n"
"s_mov_b32 s76,262784\n"
"v_fma_f32 v35,v35,v66,-v58\n"
"s_add_u32 m0,s79,69632\n"
"v_mfma_f32_16x16x32_bf16 a[148:151],v[180:183],v[144:147],a[148:151]\n"
"buffer_load_dwordx4 v106,s[16:19],s76 offen offset:0 lds\n"
"v_mfma_f32_16x16x32_bf16 a[152:155],v[184:187],v[144:147],a[152:155]\n"
"v_cvt_pk_bf16_f32 v13,v34,v35\n"
"v_fma_f32 v36,v36,v66,-v58\n"
"v_mfma_f32_16x16x32_bf16 a[152:155],v[248:251],v[208:211],a[152:155]\n"
"v_fma_f32 v37,v37,v66,-v58\n"
"v_cvt_pk_bf16_f32 v14,v36,v37\n"
"v_mfma_f32_16x16x32_bf16 a[156:159],v[252:255],v[208:211],a[156:159]\n"
"v_fma_f32 v38,v38,v66,-v58\n"
"v_fma_f32 v39,v39,v66,-v58\n"
"v_cvt_pk_bf16_f32 v15,v38,v39\n"
"v_mfma_f32_16x16x32_bf16 a[156:159],v[188:191],v[144:147],a[156:159]\n"
"buffer_store_dwordx4 v[12:15],v71,s[24:27],s54 offen sc1\n"
"v_mfma_f32_16x16x32_bf16 a[160:163],v[160:163],v[148:151],a[160:163]\n"
"buffer_load_dwordx4 v107,s[16:19],s76 offen offset:1024 lds\n"
"v_mfma_f32_16x16x32_bf16 a[160:163],v[224:227],v[212:215],a[160:163]\n"
"s_add_u32 s54,s54,131072\n"
"v_fma_f32 v40,v40,v66,-v58\n"
"v_mfma_f32_16x16x32_bf16 a[164:167],v[228:231],v[212:215],a[164:167]\n"
"v_fma_f32 v41,v41,v66,-v58\n"
"v_cvt_pk_bf16_f32 v16,v40,v41\n"
"v_mfma_f32_16x16x32_bf16 a[164:167],v[164:167],v[148:151],a[164:167]\n"
"v_fma_f32 v42,v42,v66,-v58\n"
"v_fma_f32 v43,v43,v66,-v58\n"
"v_mfma_f32_16x16x32_bf16 a[168:171],v[168:171],v[148:151],a[168:171]\n"
"v_cvt_pk_bf16_f32 v17,v42,v43\n"
"v_fma_f32 v44,v44,v66,-v58\n"
"v_mfma_f32_16x16x32_bf16 a[168:171],v[232:235],v[212:215],a[168:171]\n"
"buffer_load_dwordx4 v108,s[16:19],s76 offen offset:2048 lds\n"
"v_mfma_f32_16x16x32_bf16 a[172:175],v[236:239],v[212:215],a[172:175]\n"
"v_fma_f32 v45,v45,v66,-v58\n"
"v_cvt_pk_bf16_f32 v18,v44,v45\n"
"v_mfma_f32_16x16x32_bf16 a[172:175],v[172:175],v[148:151],a[172:175]\n"
"v_fma_f32 v46,v46,v66,-v58\n"
"v_fma_f32 v47,v47,v66,-v58\n"
"v_cvt_pk_bf16_f32 v19,v46,v47\n"
"v_mfma_f32_16x16x32_bf16 a[176:179],v[176:179],v[148:151],a[176:179]\n"
"buffer_store_dwordx4 v[16:19],v71,s[24:27],s54 offen sc1\n"
"v_mfma_f32_16x16x32_bf16 a[176:179],v[240:243],v[212:215],a[176:179]\n"
"s_add_u32 s54,s54,131072\n"
"v_fma_f32 v48,v48,v66,-v58\n"
"v_mfma_f32_16x16x32_bf16 a[180:183],v[244:247],v[212:215],a[180:183]\n"
"buffer_load_dwordx4 v109,s[16:19],s76 offen offset:3072 lds\n"
"v_mfma_f32_16x16x32_bf16 a[180:183],v[180:183],v[148:151],a[180:183]\n"
"s_mov_b32 s87,0\n"
"v_fma_f32 v49,v49,v66,-v58\n"
"v_cvt_pk_bf16_f32 v20,v48,v49\n"
"v_mfma_f32_16x16x32_bf16 a[184:187],v[184:187],v[148:151],a[184:187]\n"
"buffer_load_dword v115,v112,s[40:43],s87 offen\n"
"v_mfma_f32_16x16x32_bf16 a[184:187],v[248:251],v[212:215],a[184:187]\n"
"s_mov_b32 s87,1048576\n"
"v_fma_f32 v50,v50,v66,-v58\n"
"v_fma_f32 v51,v51,v66,-v58\n"
"v_mfma_f32_16x16x32_bf16 a[188:191],v[252:255],v[212:215],a[188:191]\n"
"buffer_load_dword v115,v112,s[40:43],s87 offen\n"
"v_mfma_f32_16x16x32_bf16 a[188:191],v[188:191],v[148:151],a[188:191]\n"
"s_mov_b32 s87,2097152\n"
"s_waitcnt vmcnt(31)\n"
"s_barrier\n"
"v_mfma_f32_16x16x32_bf16 a[192:195],v[224:227],v[216:219],a[192:195]\n"
"ds_read_b128 v[152:155],v80 offset:12288\n"
"buffer_load_dword v115,v112,s[40:43],s87 offen\n"
"v_mfma_f32_16x16x32_bf16 a[224:227],v[224:227],v[220:223],a[224:227]\n"
"ds_read_b128 v[156:159],v80 offset:14336\n"
"v_cvt_pk_bf16_f32 v21,v50,v51\n"
"v_mfma_f32_16x16x32_bf16 a[228:231],v[228:231],v[220:223],a[228:231]\n"
"ds_read_b64_tr_b16 v[160:161],v98 offset:0\n"
"v_fma_f32 v52,v52,v66,-v58\n"
"v_mfma_f32_16x16x32_bf16 a[196:199],v[228:231],v[216:219],a[196:199]\n"
"ds_read_b64_tr_b16 v[162:163],v98 offset:1024\n"
"v_fma_f32 v53,v53,v66,-v58\n"
"v_mfma_f32_16x16x32_bf16 a[200:203],v[232:235],v[216:219],a[200:203]\n"
"ds_read_b64_tr_b16 v[164:165],v99 offset:0\n"
"v_cvt_pk_bf16_f32 v22,v52,v53\n"
"v_mfma_f32_16x16x32_bf16 a[232:235],v[232:235],v[220:223],a[232:235]\n"
"ds_read_b64_tr_b16 v[166:167],v99 offset:1024\n"
"v_fma_f32 v54,v54,v66,-v58\n"
"v_mfma_f32_16x16x32_bf16 a[236:239],v[236:239],v[220:223],a[236:239]\n"
"ds_read_b64_tr_b16 v[168:169],v100 offset:0\n"
"v_fma_f32 v55,v55,v66,-v58\n"
"v_mfma_f32_16x16x32_bf16 a[204:207],v[236:239],v[216:219],a[204:207]\n"
"ds_read_b64_tr_b16 v[170:171],v100 offset:1024\n"
"v_cvt_pk_bf16_f32 v23,v54,v55\n"
"v_mfma_f32_16x16x32_bf16 a[208:211],v[240:243],v[216:219],a[208:211]\n"
"ds_read_b64_tr_b16 v[172:173],v101 offset:0\n"
"buffer_store_dwordx4 v[20:23],v71,s[24:27],s54 offen sc1\n"
"v_mfma_f32_16x16x32_bf16 a[240:243],v[240:243],v[220:223],a[240:243]\n"
"ds_read_b64_tr_b16 v[174:175],v101 offset:1024\n"
"v_mfma_f32_16x16x32_bf16 a[244:247],v[244:247],v[220:223],a[244:247]\n"
"ds_read_b64_tr_b16 v[176:177],v102 offset:0\n"
"v_mfma_f32_16x16x32_bf16 a[212:215],v[244:247],v[216:219],a[212:215]\n"
"ds_read_b64_tr_b16 v[178:179],v102 offset:1024\n"
"v_mfma_f32_16x16x32_bf16 a[216:219],v[248:251],v[216:219],a[216:219]\n"
"ds_read_b64_tr_b16 v[180:181],v103 offset:0\n"
"v_mfma_f32_16x16x32_bf16 a[248:251],v[248:251],v[220:223],a[248:251]\n"
"ds_read_b64_tr_b16 v[182:183],v103 offset:1024\n"
"v_mfma_f32_16x16x32_bf16 a[252:255],v[252:255],v[220:223],a[252:255]\n"
"ds_read_b64_tr_b16 v[184:185],v104 offset:0\n"
"v_mfma_f32_16x16x32_bf16 a[220:223],v[252:255],v[216:219],a[220:223]\n"
"ds_read_b64_tr_b16 v[186:187],v104 offset:1024\n"
"s_waitcnt lgkmcnt(10)\n"
"v_mfma_f32_16x16x32_bf16 a[192:195],v[160:163],v[152:155],a[192:195]\n"
"ds_read_b64_tr_b16 v[188:189],v105 offset:0\n"
"v_mfma_f32_16x16x32_bf16 a[224:227],v[160:163],v[156:159],a[224:227]\n"
"ds_read_b64_tr_b16 v[190:191],v105 offset:1024\n"
"v_mfma_f32_16x16x32_bf16 a[228:231],v[164:167],v[156:159],a[228:231]\n"
"ds_read_b128 v[128:131],v80 offset:0\n"
"v_mfma_f32_16x16x32_bf16 a[196:199],v[164:167],v[152:155],a[196:199]\n"
"ds_read_b128 v[192:195],v81 offset:0\n"
"s_waitcnt lgkmcnt(10)\n"
"v_mfma_f32_16x16x32_bf16 a[200:203],v[168:171],v[152:155],a[200:203]\n"
"ds_read_b64_tr_b16 v[224:225],v98 offset:16384\n"
"v_mfma_f32_16x16x32_bf16 a[232:235],v[168:171],v[156:159],a[232:235]\n"
"ds_read_b64_tr_b16 v[226:227],v98 offset:17408\n"
"v_mfma_f32_16x16x32_bf16 a[236:239],v[172:175],v[156:159],a[236:239]\n"
"ds_read_b64_tr_b16 v[228:229],v99 offset:16384\n"
"v_mfma_f32_16x16x32_bf16 a[204:207],v[172:175],v[152:155],a[204:207]\n"
"ds_read_b64_tr_b16 v[230:231],v99 offset:17408\n"
"s_waitcnt lgkmcnt(10)\n"
"v_mfma_f32_16x16x32_bf16 a[208:211],v[176:179],v[152:155],a[208:211]\n"
"ds_read_b64_tr_b16 v[232:233],v100 offset:16384\n"
"v_mfma_f32_16x16x32_bf16 a[240:243],v[176:179],v[156:159],a[240:243]\n"
"ds_read_b64_tr_b16 v[234:235],v100 offset:17408\n"
"v_mfma_f32_16x16x32_bf16 a[244:247],v[180:183],v[156:159],a[244:247]\n"
"ds_read_b64_tr_b16 v[236:237],v101 offset:16384\n"
"v_mfma_f32_16x16x32_bf16 a[212:215],v[180:183],v[152:155],a[212:215]\n"
"ds_read_b64_tr_b16 v[238:239],v101 offset:17408\n"
"s_waitcnt lgkmcnt(10)\n"
"v_mfma_f32_16x16x32_bf16 a[216:219],v[184:187],v[152:155],a[216:219]\n"
"ds_read_b64_tr_b16 v[240:241],v102 offset:16384\n"
"v_mfma_f32_16x16x32_bf16 a[248:251],v[184:187],v[156:159],a[248:251]\n"
"ds_read_b64_tr_b16 v[242:243],v102 offset:17408\n"
"v_mfma_f32_16x16x32_bf16 a[252:255],v[188:191],v[156:159],a[252:255]\n"
"ds_read_b64_tr_b16 v[244:245],v103 offset:16384\n"
"v_mfma_f32_16x16x32_bf16 a[220:223],v[188:191],v[152:155],a[220:223]\n"
"ds_read_b64_tr_b16 v[246:247],v103 offset:17408\n"
"s_waitcnt lgkmcnt(8)\n"
"v_mfma_f32_16x16x32_bf16 a[0:3],v[160:163],v[128:131],a[0:3]\n"
"ds_read_b64_tr_b16 v[248:249],v104 offset:16384\n"
"v_mfma_f32_16x16x32_bf16 a[0:3],v[224:227],v[192:195],a[0:3]\n"
"ds_read_b64_tr_b16 v[250:251],v104 offset:17408\n"
"v_mfma_f32_16x16x32_bf16 a[4:7],v[228:231],v[192:195],a[4:7]\n"
"ds_read_b64_tr_b16 v[252:253],v105 offset:16384\n"
"v_mfma_f32_16x16x32_bf16 a[4:7],v[164:167],v[128:131],a[4:7]\n"
"ds_read_b64_tr_b16 v[254:255],v105 offset:17408\n"
"s_waitcnt lgkmcnt(8)\n"
"v_mfma_f32_16x16x32_bf16 a[8:11],v[168:171],v[128:131],a[8:11]\n"
"ds_read_b128 v[132:135],v80 offset:2048\n"
"v_mfma_f32_16x16x32_bf16 a[8:11],v[232:235],v[192:195],a[8:11]\n"
"ds_read_b128 v[196:199],v81 offset:2048\n"
"v_mfma_f32_16x16x32_bf16 a[12:15],v[236:239],v[192:195],a[12:15]\n"
"ds_read_b128 v[136:139],v80 offset:4096\n"
"v_mfma_f32_16x16x32_bf16 a[12:15],v[172:175],v[128:131],a[12:15]\n"
"ds_read_b128 v[200:203],v81 offset:4096\n"
"s_waitcnt lgkmcnt(8)\n"
"v_mfma_f32_16x16x32_bf16 a[16:19],v[176:179],v[128:131],a[16:19]\n"
"ds_read_b128 v[140:143],v80 offset:6144\n"
"v_mfma_f32_16x16x32_bf16 a[16:19],v[240:243],v[192:195],a[16:19]\n"
"ds_read_b128 v[204:207],v81 offset:6144\n"
"v_mfma_f32_16x16x32_bf16 a[20:23],v[244:247],v[192:195],a[20:23]\n"
"ds_read_b128 v[144:147],v80 offset:8192\n"
"v_mfma_f32_16x16x32_bf16 a[20:23],v[180:183],v[128:131],a[20:23]\n"
"ds_read_b128 v[208:211],v81 offset:8192\n"
"s_waitcnt lgkmcnt(8)\n"
"v_mfma_f32_16x16x32_bf16 a[24:27],v[184:187],v[128:131],a[24:27]\n"
"ds_read_b128 v[148:151],v80 offset:10240\n"
"v_mfma_f32_16x16x32_bf16 a[24:27],v[248:251],v[192:195],a[24:27]\n"
"ds_read_b128 v[212:215],v81 offset:10240\n"
"v_mfma_f32_16x16x32_bf16 a[28:31],v[252:255],v[192:195],a[28:31]\n"
"ds_read_b128 v[216:219],v81 offset:12288\n"
"v_mfma_f32_16x16x32_bf16 a[28:31],v[188:191],v[128:131],a[28:31]\n"
"ds_read_b128 v[220:223],v81 offset:14336\n"
"s_waitcnt lgkmcnt(10)\n"
"v_mfma_f32_16x16x32_bf16 a[32:35],v[160:163],v[132:135],a[32:35]\n"
"v_mfma_f32_16x16x32_bf16 a[32:35],v[224:227],v[196:199],a[32:35]\n"
"v_mfma_f32_16x16x32_bf16 a[36:39],v[228:231],v[196:199],a[36:39]\n"
"v_mfma_f32_16x16x32_bf16 a[36:39],v[164:167],v[132:135],a[36:39]\n"
"v_mfma_f32_16x16x32_bf16 a[40:43],v[168:171],v[132:135],a[40:43]\n"
"s_waitcnt lgkmcnt(0)\n"
"s_barrier\n"
"v_mfma_f32_16x16x32_bf16 a[40:43],v[232:235],v[196:199],a[40:43]\n"
"s_mov_b32 s77,2621440\n"
"s_add_u32 m0,s78,98304\n"
"v_mfma_f32_16x16x32_bf16 a[44:47],v[236:239],v[196:199],a[44:47]\n"
"buffer_load_dwordx4 v110,s[20:23],s77 offen offset:0 lds\n"
"v_mfma_f32_16x16x32_bf16 a[44:47],v[172:175],v[132:135],a[44:47]\n"
"v_mfma_f32_16x16x32_bf16 a[48:51],v[176:179],v[132:135],a[48:51]\n"
"v_mfma_f32_16x16x32_bf16 a[48:51],v[240:243],v[196:199],a[48:51]\n"
"v_mfma_f32_16x16x32_bf16 a[52:55],v[244:247],v[196:199],a[52:55]\n"
"buffer_load_dwordx4 v111,s[20:23],s77 offen offset:1024 lds\n"
"v_mfma_f32_16x16x32_bf16 a[52:55],v[180:183],v[132:135],a[52:55]\n"
"v_mfma_f32_16x16x32_bf16 a[56:59],v[184:187],v[132:135],a[56:59]\n"
"v_mfma_f32_16x16x32_bf16 a[56:59],v[248:251],v[196:199],a[56:59]\n"
"v_mfma_f32_16x16x32_bf16 a[60:63],v[252:255],v[196:199],a[60:63]\n"
"buffer_load_dwordx4 v113,s[20:23],s77 offen offset:2048 lds\n"
"v_mfma_f32_16x16x32_bf16 a[60:63],v[188:191],v[132:135],a[60:63]\n"
"v_mfma_f32_16x16x32_bf16 a[64:67],v[160:163],v[136:139],a[64:67]\n"
"v_mfma_f32_16x16x32_bf16 a[64:67],v[224:227],v[200:203],a[64:67]\n"
"v_mfma_f32_16x16x32_bf16 a[68:71],v[228:231],v[200:203],a[68:71]\n"
"buffer_load_dwordx4 v114,s[20:23],s77 offen offset:3072 lds\n"
"v_mfma_f32_16x16x32_bf16 a[68:71],v[164:167],v[136:139],a[68:71]\n"
"v_mfma_f32_16x16x32_bf16 a[72:75],v[168:171],v[136:139],a[72:75]\n"
"v_mfma_f32_16x16x32_bf16 a[72:75],v[232:235],v[200:203],a[72:75]\n"
"s_mov_b32 s77,2883584\n"
"s_add_u32 m0,s78,114688\n"
"v_mfma_f32_16x16x32_bf16 a[76:79],v[236:239],v[200:203],a[76:79]\n"
"buffer_load_dwordx4 v110,s[20:23],s77 offen offset:0 lds\n"
"v_mfma_f32_16x16x32_bf16 a[76:79],v[172:175],v[136:139],a[76:79]\n"
"v_mfma_f32_16x16x32_bf16 a[80:83],v[176:179],v[136:139],a[80:83]\n"
"v_mfma_f32_16x16x32_bf16 a[80:83],v[240:243],v[200:203],a[80:83]\n"
"v_mfma_f32_16x16x32_bf16 a[84:87],v[244:247],v[200:203],a[84:87]\n"
"buffer_load_dwordx4 v111,s[20:23],s77 offen offset:1024 lds\n"
"v_mfma_f32_16x16x32_bf16 a[84:87],v[180:183],v[136:139],a[84:87]\n"
"v_mfma_f32_16x16x32_bf16 a[88:91],v[184:187],v[136:139],a[88:91]\n"
"v_mfma_f32_16x16x32_bf16 a[88:91],v[248:251],v[200:203],a[88:91]\n"
"v_mfma_f32_16x16x32_bf16 a[92:95],v[252:255],v[200:203],a[92:95]\n"
"buffer_load_dwordx4 v113,s[20:23],s77 offen offset:2048 lds\n"
"v_mfma_f32_16x16x32_bf16 a[92:95],v[188:191],v[136:139],a[92:95]\n"
"v_mfma_f32_16x16x32_bf16 a[96:99],v[160:163],v[140:143],a[96:99]\n"
"v_mfma_f32_16x16x32_bf16 a[96:99],v[224:227],v[204:207],a[96:99]\n"
"v_mfma_f32_16x16x32_bf16 a[100:103],v[228:231],v[204:207],a[100:103]\n"
"buffer_load_dwordx4 v114,s[20:23],s77 offen offset:3072 lds\n"
"v_mfma_f32_16x16x32_bf16 a[100:103],v[164:167],v[140:143],a[100:103]\n"
"v_mfma_f32_16x16x32_bf16 a[104:107],v[168:171],v[140:143],a[104:107]\n"
"v_mfma_f32_16x16x32_bf16 a[104:107],v[232:235],v[204:207],a[104:107]\n"
"v_mfma_f32_16x16x32_bf16 a[108:111],v[236:239],v[204:207],a[108:111]\n"
"s_mov_b32 s76,768\n"
"s_add_u32 m0,s79,0\n"
"v_mfma_f32_16x16x32_bf16 a[108:111],v[172:175],v[140:143],a[108:111]\n"
"buffer_load_dwordx4 v106,s[16:19],s76 offen offset:0 lds\n"
"v_mfma_f32_16x16x32_bf16 a[112:115],v[176:179],v[140:143],a[112:115]\n"
"v_mfma_f32_16x16x32_bf16 a[112:115],v[240:243],v[204:207],a[112:115]\n"
"v_mfma_f32_16x16x32_bf16 a[116:119],v[244:247],v[204:207],a[116:119]\n"
"v_mfma_f32_16x16x32_bf16 a[116:119],v[180:183],v[140:143],a[116:119]\n"
"v_mfma_f32_16x16x32_bf16 a[120:123],v[184:187],v[140:143],a[120:123]\n"
"buffer_load_dwordx4 v107,s[16:19],s76 offen offset:1024 lds\n"
"v_mfma_f32_16x16x32_bf16 a[120:123],v[248:251],v[204:207],a[120:123]\n"
"v_mfma_f32_16x16x32_bf16 a[124:127],v[252:255],v[204:207],a[124:127]\n"
"v_mfma_f32_16x16x32_bf16 a[124:127],v[188:191],v[140:143],a[124:127]\n"
"v_mfma_f32_16x16x32_bf16 a[128:131],v[160:163],v[144:147],a[128:131]\n"
"v_mfma_f32_16x16x32_bf16 a[128:131],v[224:227],v[208:211],a[128:131]\n"
"buffer_load_dwordx4 v108,s[16:19],s76 offen offset:2048 lds\n"
"v_mfma_f32_16x16x32_bf16 a[132:135],v[228:231],v[208:211],a[132:135]\n"
"v_mfma_f32_16x16x32_bf16 a[132:135],v[164:167],v[144:147],a[132:135]\n"
"v_mfma_f32_16x16x32_bf16 a[136:139],v[168:171],v[144:147],a[136:139]\n"
"v_mfma_f32_16x16x32_bf16 a[136:139],v[232:235],v[208:211],a[136:139]\n"
"v_mfma_f32_16x16x32_bf16 a[140:143],v[236:239],v[208:211],a[140:143]\n"
"buffer_load_dwordx4 v109,s[16:19],s76 offen offset:3072 lds\n"
"v_mfma_f32_16x16x32_bf16 a[140:143],v[172:175],v[144:147],a[140:143]\n"
"v_mfma_f32_16x16x32_bf16 a[144:147],v[176:179],v[144:147],a[144:147]\n"
"v_mfma_f32_16x16x32_bf16 a[144:147],v[240:243],v[208:211],a[144:147]\n"
"v_mfma_f32_16x16x32_bf16 a[148:151],v[244:247],v[208:211],a[148:151]\n"
"s_mov_b32 s76,262912\n"
"s_add_u32 m0,s79,4096\n"
"v_mfma_f32_16x16x32_bf16 a[148:151],v[180:183],v[144:147],a[148:151]\n"
"buffer_load_dwordx4 v106,s[16:19],s76 offen offset:0 lds\n"
"v_mfma_f32_16x16x32_bf16 a[152:155],v[184:187],v[144:147],a[152:155]\n"
"v_mfma_f32_16x16x32_bf16 a[152:155],v[248:251],v[208:211],a[152:155]\n"
"v_mfma_f32_16x16x32_bf16 a[156:159],v[252:255],v[208:211],a[156:159]\n"
"v_mfma_f32_16x16x32_bf16 a[156:159],v[188:191],v[144:147],a[156:159]\n"
"v_mfma_f32_16x16x32_bf16 a[160:163],v[160:163],v[148:151],a[160:163]\n"
"buffer_load_dwordx4 v107,s[16:19],s76 offen offset:1024 lds\n"
"v_mfma_f32_16x16x32_bf16 a[160:163],v[224:227],v[212:215],a[160:163]\n"
"v_mfma_f32_16x16x32_bf16 a[164:167],v[228:231],v[212:215],a[164:167]\n"
"v_mfma_f32_16x16x32_bf16 a[164:167],v[164:167],v[148:151],a[164:167]\n"
"v_mfma_f32_16x16x32_bf16 a[168:171],v[168:171],v[148:151],a[168:171]\n"
"v_mfma_f32_16x16x32_bf16 a[168:171],v[232:235],v[212:215],a[168:171]\n"
"buffer_load_dwordx4 v108,s[16:19],s76 offen offset:2048 lds\n"
"v_mfma_f32_16x16x32_bf16 a[172:175],v[236:239],v[212:215],a[172:175]\n"
"v_mfma_f32_16x16x32_bf16 a[172:175],v[172:175],v[148:151],a[172:175]\n"
"v_mfma_f32_16x16x32_bf16 a[176:179],v[176:179],v[148:151],a[176:179]\n"
"v_mfma_f32_16x16x32_bf16 a[176:179],v[240:243],v[212:215],a[176:179]\n"
"v_mfma_f32_16x16x32_bf16 a[180:183],v[244:247],v[212:215],a[180:183]\n"
"buffer_load_dwordx4 v109,s[16:19],s76 offen offset:3072 lds\n"
"v_mfma_f32_16x16x32_bf16 a[180:183],v[180:183],v[148:151],a[180:183]\n"
"s_mov_b32 s87,3145728\n"
"v_mfma_f32_16x16x32_bf16 a[184:187],v[184:187],v[148:151],a[184:187]\n"
"buffer_load_dword v115,v112,s[40:43],s87 offen\n"
"v_mfma_f32_16x16x32_bf16 a[184:187],v[248:251],v[212:215],a[184:187]\n"
"s_mov_b32 s87,4194304\n"
"v_mfma_f32_16x16x32_bf16 a[188:191],v[252:255],v[212:215],a[188:191]\n"
"buffer_load_dword v115,v112,s[40:43],s87 offen\n"
"v_mfma_f32_16x16x32_bf16 a[188:191],v[188:191],v[148:151],a[188:191]\n"
"s_mov_b32 s87,5242880\n"
"s_waitcnt vmcnt(33)\n"
"s_barrier\n"
"v_mfma_f32_16x16x32_bf16 a[192:195],v[224:227],v[216:219],a[192:195]\n"
"ds_read_b128 v[152:155],v80 offset:45056\n"
"buffer_load_dword v115,v112,s[40:43],s87 offen\n"
"v_mfma_f32_16x16x32_bf16 a[224:227],v[224:227],v[220:223],a[224:227]\n"
"ds_read_b128 v[156:159],v80 offset:47104\n"
"v_mfma_f32_16x16x32_bf16 a[228:231],v[228:231],v[220:223],a[228:231]\n"
"ds_read_b64_tr_b16 v[160:161],v98 offset:49152\n"
"v_mfma_f32_16x16x32_bf16 a[196:199],v[228:231],v[216:219],a[196:199]\n"
"ds_read_b64_tr_b16 v[162:163],v98 offset:50176\n"
"v_mfma_f32_16x16x32_bf16 a[200:203],v[232:235],v[216:219],a[200:203]\n"
"ds_read_b64_tr_b16 v[164:165],v99 offset:49152\n"
"v_mfma_f32_16x16x32_bf16 a[232:235],v[232:235],v[220:223],a[232:235]\n"
"ds_read_b64_tr_b16 v[166:167],v99 offset:50176\n"
"v_mfma_f32_16x16x32_bf16 a[236:239],v[236:239],v[220:223],a[236:239]\n"
"ds_read_b64_tr_b16 v[168:169],v100 offset:49152\n"
"v_mfma_f32_16x16x32_bf16 a[204:207],v[236:239],v[216:219],a[204:207]\n"
"ds_read_b64_tr_b16 v[170:171],v100 offset:50176\n"
"v_mfma_f32_16x16x32_bf16 a[208:211],v[240:243],v[216:219],a[208:211]\n"
"ds_read_b64_tr_b16 v[172:173],v101 offset:49152\n"
"v_mfma_f32_16x16x32_bf16 a[240:243],v[240:243],v[220:223],a[240:243]\n"
"ds_read_b64_tr_b16 v[174:175],v101 offset:50176\n"
"v_mfma_f32_16x16x32_bf16 a[244:247],v[244:247],v[220:223],a[244:247]\n"
"ds_read_b64_tr_b16 v[176:177],v102 offset:49152\n"
"v_mfma_f32_16x16x32_bf16 a[212:215],v[244:247],v[216:219],a[212:215]\n"
"ds_read_b64_tr_b16 v[178:179],v102 offset:50176\n"
"v_mfma_f32_16x16x32_bf16 a[216:219],v[248:251],v[216:219],a[216:219]\n"
"ds_read_b64_tr_b16 v[180:181],v103 offset:49152\n"
"v_mfma_f32_16x16x32_bf16 a[248:251],v[248:251],v[220:223],a[248:251]\n"
"ds_read_b64_tr_b16 v[182:183],v103 offset:50176\n"
"v_mfma_f32_16x16x32_bf16 a[252:255],v[252:255],v[220:223],a[252:255]\n"
"ds_read_b64_tr_b16 v[184:185],v104 offset:49152\n"
"ds_read_b64_tr_b16 v[186:187],v104 offset:50176\n"
"v_mfma_f32_16x16x32_bf16 a[220:223],v[252:255],v[216:219],a[220:223]\n"
"s_waitcnt vmcnt(19)\n"
"s_waitcnt lgkmcnt(10)\n"
"v_mfma_f32_16x16x32_bf16 a[192:195],v[160:163],v[152:155],a[192:195]\n"
"ds_read_b64_tr_b16 v[188:189],v105 offset:49152\n"
"ds_read_b64_tr_b16 v[190:191],v105 offset:50176\n"
"v_mfma_f32_16x16x32_bf16 a[224:227],v[160:163],v[156:159],a[224:227]\n"
"buffer_store_dwordx2 v[124:125],off,s[24:27],s57 sc1\n"
"v_mfma_f32_16x16x32_bf16 a[228:231],v[164:167],v[156:159],a[228:231]\n"
"ds_read_b128 v[128:131],v80 offset:32768\n"
"v_mfma_f32_16x16x32_bf16 a[196:199],v[164:167],v[152:155],a[196:199]\n"
"ds_read_b128 v[192:195],v81 offset:32768\n"
"s_waitcnt lgkmcnt(10)\n"
"v_mfma_f32_16x16x32_bf16 a[200:203],v[168:171],v[152:155],a[200:203]\n"
"ds_read_b64_tr_b16 v[224:225],v98 offset:32768\n"
"v_mfma_f32_16x16x32_bf16 a[232:235],v[168:171],v[156:159],a[232:235]\n"
"ds_read_b64_tr_b16 v[226:227],v98 offset:33792\n"
"v_mfma_f32_16x16x32_bf16 a[236:239],v[172:175],v[156:159],a[236:239]\n"
"ds_read_b64_tr_b16 v[228:229],v99 offset:32768\n"
"v_mfma_f32_16x16x32_bf16 a[204:207],v[172:175],v[152:155],a[204:207]\n"
"ds_read_b64_tr_b16 v[230:231],v99 offset:33792\n"
"s_waitcnt lgkmcnt(10)\n"
"v_mfma_f32_16x16x32_bf16 a[208:211],v[176:179],v[152:155],a[208:211]\n"
"ds_read_b64_tr_b16 v[232:233],v100 offset:32768\n"
"v_mfma_f32_16x16x32_bf16 a[240:243],v[176:179],v[156:159],a[240:243]\n"
"ds_read_b64_tr_b16 v[234:235],v100 offset:33792\n"
"v_mfma_f32_16x16x32_bf16 a[244:247],v[180:183],v[156:159],a[244:247]\n"
"ds_read_b64_tr_b16 v[236:237],v101 offset:32768\n"
"v_mfma_f32_16x16x32_bf16 a[212:215],v[180:183],v[152:155],a[212:215]\n"
"ds_read_b64_tr_b16 v[238:239],v101 offset:33792\n"
"s_waitcnt lgkmcnt(10)\n"
"v_mfma_f32_16x16x32_bf16 a[216:219],v[184:187],v[152:155],a[216:219]\n"
"ds_read_b64_tr_b16 v[240:241],v102 offset:32768\n"
"v_mfma_f32_16x16x32_bf16 a[248:251],v[184:187],v[156:159],a[248:251]\n"
"ds_read_b64_tr_b16 v[242:243],v102 offset:33792\n"
"v_mfma_f32_16x16x32_bf16 a[252:255],v[188:191],v[156:159],a[252:255]\n"
"ds_read_b64_tr_b16 v[244:245],v103 offset:32768\n"
"v_mfma_f32_16x16x32_bf16 a[220:223],v[188:191],v[152:155],a[220:223]\n"
"ds_read_b64_tr_b16 v[246:247],v103 offset:33792\n"
"s_waitcnt lgkmcnt(8)\n"
"v_mfma_f32_16x16x32_bf16 a[0:3],v[160:163],v[128:131],a[0:3]\n"
"ds_read_b64_tr_b16 v[248:249],v104 offset:32768\n"
"v_mfma_f32_16x16x32_bf16 a[0:3],v[224:227],v[192:195],a[0:3]\n"
"ds_read_b64_tr_b16 v[250:251],v104 offset:33792\n"
"v_mfma_f32_16x16x32_bf16 a[4:7],v[228:231],v[192:195],a[4:7]\n"
"ds_read_b64_tr_b16 v[252:253],v105 offset:32768\n"
"v_mfma_f32_16x16x32_bf16 a[4:7],v[164:167],v[128:131],a[4:7]\n"
"ds_read_b64_tr_b16 v[254:255],v105 offset:33792\n"
"s_waitcnt lgkmcnt(8)\n"
"v_mfma_f32_16x16x32_bf16 a[8:11],v[168:171],v[128:131],a[8:11]\n"
"ds_read_b128 v[132:135],v80 offset:34816\n"
"v_mfma_f32_16x16x32_bf16 a[8:11],v[232:235],v[192:195],a[8:11]\n"
"ds_read_b128 v[196:199],v81 offset:34816\n"
"v_mfma_f32_16x16x32_bf16 a[12:15],v[236:239],v[192:195],a[12:15]\n"
"ds_read_b128 v[136:139],v80 offset:36864\n"
"v_mfma_f32_16x16x32_bf16 a[12:15],v[172:175],v[128:131],a[12:15]\n"
"ds_read_b128 v[200:203],v81 offset:36864\n"
"s_waitcnt lgkmcnt(8)\n"
"v_mfma_f32_16x16x32_bf16 a[16:19],v[176:179],v[128:131],a[16:19]\n"
"ds_read_b128 v[140:143],v80 offset:38912\n"
"v_mfma_f32_16x16x32_bf16 a[16:19],v[240:243],v[192:195],a[16:19]\n"
"ds_read_b128 v[204:207],v81 offset:38912\n"
"v_mfma_f32_16x16x32_bf16 a[20:23],v[244:247],v[192:195],a[20:23]\n"
"ds_read_b128 v[144:147],v80 offset:40960\n"
"v_mfma_f32_16x16x32_bf16 a[20:23],v[180:183],v[128:131],a[20:23]\n"
"ds_read_b128 v[208:211],v81 offset:40960\n"
"s_waitcnt lgkmcnt(8)\n"
"v_mfma_f32_16x16x32_bf16 a[24:27],v[184:187],v[128:131],a[24:27]\n"
"ds_read_b128 v[148:151],v80 offset:43008\n"
"v_mfma_f32_16x16x32_bf16 a[24:27],v[248:251],v[192:195],a[24:27]\n"
"ds_read_b128 v[212:215],v81 offset:43008\n"
"v_mfma_f32_16x16x32_bf16 a[28:31],v[252:255],v[192:195],a[28:31]\n"
"ds_read_b128 v[216:219],v81 offset:45056\n"
"v_mfma_f32_16x16x32_bf16 a[28:31],v[188:191],v[128:131],a[28:31]\n"
"ds_read_b128 v[220:223],v81 offset:47104\n"
"s_waitcnt lgkmcnt(10)\n"
"v_mfma_f32_16x16x32_bf16 a[32:35],v[160:163],v[132:135],a[32:35]\n"
"v_mfma_f32_16x16x32_bf16 a[32:35],v[224:227],v[196:199],a[32:35]\n"
"v_mfma_f32_16x16x32_bf16 a[36:39],v[228:231],v[196:199],a[36:39]\n"
"v_mfma_f32_16x16x32_bf16 a[36:39],v[164:167],v[132:135],a[36:39]\n"
"v_mfma_f32_16x16x32_bf16 a[40:43],v[168:171],v[132:135],a[40:43]\n"
"s_waitcnt lgkmcnt(0)\n"
"s_barrier\n"
"v_mfma_f32_16x16x32_bf16 a[40:43],v[232:235],v[196:199],a[40:43]\n"
"s_mov_b32 s77,3145728\n"
"s_add_u32 m0,s78,147456\n"
"v_mfma_f32_16x16x32_bf16 a[44:47],v[236:239],v[196:199],a[44:47]\n"
"buffer_load_dwordx4 v110,s[20:23],s77 offen offset:0 lds\n"
"v_mfma_f32_16x16x32_bf16 a[44:47],v[172:175],v[132:135],a[44:47]\n"
"v_mfma_f32_16x16x32_bf16 a[48:51],v[176:179],v[132:135],a[48:51]\n"
"v_mfma_f32_16x16x32_bf16 a[48:51],v[240:243],v[196:199],a[48:51]\n"
"v_mfma_f32_16x16x32_bf16 a[52:55],v[244:247],v[196:199],a[52:55]\n"
"buffer_load_dwordx4 v111,s[20:23],s77 offen offset:1024 lds\n"
"v_mfma_f32_16x16x32_bf16 a[52:55],v[180:183],v[132:135],a[52:55]\n"
"v_mfma_f32_16x16x32_bf16 a[56:59],v[184:187],v[132:135],a[56:59]\n"
"v_mfma_f32_16x16x32_bf16 a[56:59],v[248:251],v[196:199],a[56:59]\n"
"v_mfma_f32_16x16x32_bf16 a[60:63],v[252:255],v[196:199],a[60:63]\n"
"buffer_load_dwordx4 v113,s[20:23],s77 offen offset:2048 lds\n"
"v_mfma_f32_16x16x32_bf16 a[60:63],v[188:191],v[132:135],a[60:63]\n"
"v_mfma_f32_16x16x32_bf16 a[64:67],v[160:163],v[136:139],a[64:67]\n"
"v_mfma_f32_16x16x32_bf16 a[64:67],v[224:227],v[200:203],a[64:67]\n"
"v_mfma_f32_16x16x32_bf16 a[68:71],v[228:231],v[200:203],a[68:71]\n"
"buffer_load_dwordx4 v114,s[20:23],s77 offen offset:3072 lds\n"
"v_mfma_f32_16x16x32_bf16 a[68:71],v[164:167],v[136:139],a[68:71]\n"
"v_mfma_f32_16x16x32_bf16 a[72:75],v[168:171],v[136:139],a[72:75]\n"
"v_mfma_f32_16x16x32_bf16 a[72:75],v[232:235],v[200:203],a[72:75]\n"
"s_mov_b32 s77,3407872\n"
"s_add_u32 m0,s78,131072\n"
"v_mfma_f32_16x16x32_bf16 a[76:79],v[236:239],v[200:203],a[76:79]\n"
"buffer_load_dwordx4 v110,s[20:23],s77 offen offset:0 lds\n"
"v_mfma_f32_16x16x32_bf16 a[76:79],v[172:175],v[136:139],a[76:79]\n"
"v_mfma_f32_16x16x32_bf16 a[80:83],v[176:179],v[136:139],a[80:83]\n"
"v_mfma_f32_16x16x32_bf16 a[80:83],v[240:243],v[200:203],a[80:83]\n"
"v_mfma_f32_16x16x32_bf16 a[84:87],v[244:247],v[200:203],a[84:87]\n"
"buffer_load_dwordx4 v111,s[20:23],s77 offen offset:1024 lds\n"
"v_mfma_f32_16x16x32_bf16 a[84:87],v[180:183],v[136:139],a[84:87]\n"
"v_mfma_f32_16x16x32_bf16 a[88:91],v[184:187],v[136:139],a[88:91]\n"
"v_mfma_f32_16x16x32_bf16 a[88:91],v[248:251],v[200:203],a[88:91]\n"
"v_mfma_f32_16x16x32_bf16 a[92:95],v[252:255],v[200:203],a[92:95]\n"
"buffer_load_dwordx4 v113,s[20:23],s77 offen offset:2048 lds\n"
"v_mfma_f32_16x16x32_bf16 a[92:95],v[188:191],v[136:139],a[92:95]\n"
"v_mfma_f32_16x16x32_bf16 a[96:99],v[160:163],v[140:143],a[96:99]\n"
"v_mfma_f32_16x16x32_bf16 a[96:99],v[224:227],v[204:207],a[96:99]\n"
"v_mfma_f32_16x16x32_bf16 a[100:103],v[228:231],v[204:207],a[100:103]\n"
"buffer_load_dwordx4 v114,s[20:23],s77 offen offset:3072 lds\n"
"v_mfma_f32_16x16x32_bf16 a[100:103],v[164:167],v[140:143],a[100:103]\n"
"v_mfma_f32_16x16x32_bf16 a[104:107],v[168:171],v[140:143],a[104:107]\n"
"v_mfma_f32_16x16x32_bf16 a[104:107],v[232:235],v[204:207],a[104:107]\n"
"v_mfma_f32_16x16x32_bf16 a[108:111],v[236:239],v[204:207],a[108:111]\n"
"s_mov_b32 s76,896\n"
"s_add_u32 m0,s79,32768\n"
"v_mfma_f32_16x16x32_bf16 a[108:111],v[172:175],v[140:143],a[108:111]\n"
"buffer_load_dwordx4 v106,s[16:19],s76 offen offset:0 lds\n"
"v_mfma_f32_16x16x32_bf16 a[112:115],v[176:179],v[140:143],a[112:115]\n"
"v_mfma_f32_16x16x32_bf16 a[112:115],v[240:243],v[204:207],a[112:115]\n"
"v_mfma_f32_16x16x32_bf16 a[116:119],v[244:247],v[204:207],a[116:119]\n"
"v_mfma_f32_16x16x32_bf16 a[116:119],v[180:183],v[140:143],a[116:119]\n"
"v_mfma_f32_16x16x32_bf16 a[120:123],v[184:187],v[140:143],a[120:123]\n"
"buffer_load_dwordx4 v107,s[16:19],s76 offen offset:1024 lds\n"
"v_mfma_f32_16x16x32_bf16 a[120:123],v[248:251],v[204:207],a[120:123]\n"
"v_mfma_f32_16x16x32_bf16 a[124:127],v[252:255],v[204:207],a[124:127]\n"
"v_mfma_f32_16x16x32_bf16 a[124:127],v[188:191],v[140:143],a[124:127]\n"
"v_mfma_f32_16x16x32_bf16 a[128:131],v[160:163],v[144:147],a[128:131]\n"
"v_mfma_f32_16x16x32_bf16 a[128:131],v[224:227],v[208:211],a[128:131]\n"
"buffer_load_dwordx4 v108,s[16:19],s76 offen offset:2048 lds\n"
"v_mfma_f32_16x16x32_bf16 a[132:135],v[228:231],v[208:211],a[132:135]\n"
"v_mfma_f32_16x16x32_bf16 a[132:135],v[164:167],v[144:147],a[132:135]\n"
"v_mfma_f32_16x16x32_bf16 a[136:139],v[168:171],v[144:147],a[136:139]\n"
"v_mfma_f32_16x16x32_bf16 a[136:139],v[232:235],v[208:211],a[136:139]\n"
"v_mfma_f32_16x16x32_bf16 a[140:143],v[236:239],v[208:211],a[140:143]\n"
"buffer_load_dwordx4 v109,s[16:19],s76 offen offset:3072 lds\n"
"v_mfma_f32_16x16x32_bf16 a[140:143],v[172:175],v[144:147],a[140:143]\n"
"v_mfma_f32_16x16x32_bf16 a[144:147],v[176:179],v[144:147],a[144:147]\n"
"v_mfma_f32_16x16x32_bf16 a[144:147],v[240:243],v[208:211],a[144:147]\n"
"v_mfma_f32_16x16x32_bf16 a[148:151],v[244:247],v[208:211],a[148:151]\n"
"s_mov_b32 s76,263040\n"
"s_add_u32 m0,s79,36864\n"
"v_mfma_f32_16x16x32_bf16 a[148:151],v[180:183],v[144:147],a[148:151]\n"
"buffer_load_dwordx4 v106,s[16:19],s76 offen offset:0 lds\n"
"v_mfma_f32_16x16x32_bf16 a[152:155],v[184:187],v[144:147],a[152:155]\n"
"v_mfma_f32_16x16x32_bf16 a[152:155],v[248:251],v[208:211],a[152:155]\n"
"v_mfma_f32_16x16x32_bf16 a[156:159],v[252:255],v[208:211],a[156:159]\n"
"v_mfma_f32_16x16x32_bf16 a[156:159],v[188:191],v[144:147],a[156:159]\n"
"v_mfma_f32_16x16x32_bf16 a[160:163],v[160:163],v[148:151],a[160:163]\n"
"buffer_load_dwordx4 v107,s[16:19],s76 offen offset:1024 lds\n"
"v_mfma_f32_16x16x32_bf16 a[160:163],v[224:227],v[212:215],a[160:163]\n"
"v_mfma_f32_16x16x32_bf16 a[164:167],v[228:231],v[212:215],a[164:167]\n"
"v_mfma_f32_16x16x32_bf16 a[164:167],v[164:167],v[148:151],a[164:167]\n"
"v_mfma_f32_16x16x32_bf16 a[168:171],v[168:171],v[148:151],a[168:171]\n"
"v_mfma_f32_16x16x32_bf16 a[168:171],v[232:235],v[212:215],a[168:171]\n"
"buffer_load_dwordx4 v108,s[16:19],s76 offen offset:2048 lds\n"
"v_mfma_f32_16x16x32_bf16 a[172:175],v[236:239],v[212:215],a[172:175]\n"
"v_mfma_f32_16x16x32_bf16 a[172:175],v[172:175],v[148:151],a[172:175]\n"
"v_mfma_f32_16x16x32_bf16 a[176:179],v[176:179],v[148:151],a[176:179]\n"
"v_mfma_f32_16x16x32_bf16 a[176:179],v[240:243],v[212:215],a[176:179]\n"
"v_mfma_f32_16x16x32_bf16 a[180:183],v[244:247],v[212:215],a[180:183]\n"
"buffer_load_dwordx4 v109,s[16:19],s76 offen offset:3072 lds\n"
"v_mfma_f32_16x16x32_bf16 a[180:183],v[180:183],v[148:151],a[180:183]\n"
"s_mov_b32 s87,6291456\n"
"v_mfma_f32_16x16x32_bf16 a[184:187],v[184:187],v[148:151],a[184:187]\n"
"buffer_load_dword v115,v112,s[40:43],s87 offen\n"
"v_mfma_f32_16x16x32_bf16 a[184:187],v[248:251],v[212:215],a[184:187]\n"
"s_mov_b32 s87,7340032\n"
"v_mfma_f32_16x16x32_bf16 a[188:191],v[252:255],v[212:215],a[188:191]\n"
"buffer_load_dword v115,v112,s[40:43],s87 offen\n"
"v_mfma_f32_16x16x32_bf16 a[188:191],v[188:191],v[148:151],a[188:191]\n"
"s_waitcnt vmcnt(30)\n"
"s_barrier\n"
"v_mfma_f32_16x16x32_bf16 a[192:195],v[224:227],v[216:219],a[192:195]\n"
"ds_read_b128 v[152:155],v82 offset:12288\n"
"v_mfma_f32_16x16x32_bf16 a[224:227],v[224:227],v[220:223],a[224:227]\n"
"ds_read_b128 v[156:159],v82 offset:14336\n"
"v_mfma_f32_16x16x32_bf16 a[228:231],v[228:231],v[220:223],a[228:231]\n"
"ds_read_b64_tr_b16 v[160:161],v98 offset:0\n"
"v_mfma_f32_16x16x32_bf16 a[196:199],v[228:231],v[216:219],a[196:199]\n"
"ds_read_b64_tr_b16 v[162:163],v98 offset:1024\n"
"v_mfma_f32_16x16x32_bf16 a[200:203],v[232:235],v[216:219],a[200:203]\n"
"ds_read_b64_tr_b16 v[164:165],v99 offset:0\n"
"v_mfma_f32_16x16x32_bf16 a[232:235],v[232:235],v[220:223],a[232:235]\n"
"ds_read_b64_tr_b16 v[166:167],v99 offset:1024\n"
"v_mfma_f32_16x16x32_bf16 a[236:239],v[236:239],v[220:223],a[236:239]\n"
"ds_read_b64_tr_b16 v[168:169],v100 offset:0\n"
"v_mfma_f32_16x16x32_bf16 a[204:207],v[236:239],v[216:219],a[204:207]\n"
"ds_read_b64_tr_b16 v[170:171],v100 offset:1024\n"
"v_mfma_f32_16x16x32_bf16 a[208:211],v[240:243],v[216:219],a[208:211]\n"
"ds_read_b64_tr_b16 v[172:173],v101 offset:0\n"
"v_mfma_f32_16x16x32_bf16 a[240:243],v[240:243],v[220:223],a[240:243]\n"
"ds_read_b64_tr_b16 v[174:175],v101 offset:1024\n"
"v_mfma_f32_16x16x32_bf16 a[244:247],v[244:247],v[220:223],a[244:247]\n"
"ds_read_b64_tr_b16 v[176:177],v102 offset:0\n"
"v_mfma_f32_16x16x32_bf16 a[212:215],v[244:247],v[216:219],a[212:215]\n"
"ds_read_b64_tr_b16 v[178:179],v102 offset:1024\n"
"v_mfma_f32_16x16x32_bf16 a[216:219],v[248:251],v[216:219],a[216:219]\n"
"ds_read_b64_tr_b16 v[180:181],v103 offset:0\n"
"v_mfma_f32_16x16x32_bf16 a[248:251],v[248:251],v[220:223],a[248:251]\n"
"ds_read_b64_tr_b16 v[182:183],v103 offset:1024\n"
"v_mfma_f32_16x16x32_bf16 a[252:255],v[252:255],v[220:223],a[252:255]\n"
"ds_read_b64_tr_b16 v[184:185],v104 offset:0\n"
"v_mfma_f32_16x16x32_bf16 a[220:223],v[252:255],v[216:219],a[220:223]\n"
"ds_read_b64_tr_b16 v[186:187],v104 offset:1024\n"
"s_waitcnt lgkmcnt(10)\n"
"v_mfma_f32_16x16x32_bf16 a[192:195],v[160:163],v[152:155],a[192:195]\n"
"ds_read_b64_tr_b16 v[188:189],v105 offset:0\n"
"v_mfma_f32_16x16x32_bf16 a[224:227],v[160:163],v[156:159],a[224:227]\n"
"ds_read_b64_tr_b16 v[190:191],v105 offset:1024\n"
"v_mfma_f32_16x16x32_bf16 a[228:231],v[164:167],v[156:159],a[228:231]\n"
"ds_read_b128 v[128:131],v82 offset:0\n"
"v_mfma_f32_16x16x32_bf16 a[196:199],v[164:167],v[152:155],a[196:199]\n"
"ds_read_b128 v[192:195],v83 offset:0\n"
"s_waitcnt lgkmcnt(10)\n"
"v_mfma_f32_16x16x32_bf16 a[200:203],v[168:171],v[152:155],a[200:203]\n"
"ds_read_b64_tr_b16 v[224:225],v98 offset:16384\n"
"v_mfma_f32_16x16x32_bf16 a[232:235],v[168:171],v[156:159],a[232:235]\n"
"ds_read_b64_tr_b16 v[226:227],v98 offset:17408\n"
"v_mfma_f32_16x16x32_bf16 a[236:239],v[172:175],v[156:159],a[236:239]\n"
"ds_read_b64_tr_b16 v[228:229],v99 offset:16384\n"
"v_mfma_f32_16x16x32_bf16 a[204:207],v[172:175],v[152:155],a[204:207]\n"
"ds_read_b64_tr_b16 v[230:231],v99 offset:17408\n"
"s_waitcnt lgkmcnt(10)\n"
"v_mfma_f32_16x16x32_bf16 a[208:211],v[176:179],v[152:155],a[208:211]\n"
"ds_read_b64_tr_b16 v[232:233],v100 offset:16384\n"
"v_mfma_f32_16x16x32_bf16 a[240:243],v[176:179],v[156:159],a[240:243]\n"
"ds_read_b64_tr_b16 v[234:235],v100 offset:17408\n"
"v_mfma_f32_16x16x32_bf16 a[244:247],v[180:183],v[156:159],a[244:247]\n"
"ds_read_b64_tr_b16 v[236:237],v101 offset:16384\n"
"v_mfma_f32_16x16x32_bf16 a[212:215],v[180:183],v[152:155],a[212:215]\n"
"ds_read_b64_tr_b16 v[238:239],v101 offset:17408\n"
"s_waitcnt lgkmcnt(10)\n"
"v_mfma_f32_16x16x32_bf16 a[216:219],v[184:187],v[152:155],a[216:219]\n"
"ds_read_b64_tr_b16 v[240:241],v102 offset:16384\n"
"v_mfma_f32_16x16x32_bf16 a[248:251],v[184:187],v[156:159],a[248:251]\n"
"ds_read_b64_tr_b16 v[242:243],v102 offset:17408\n"
"v_mfma_f32_16x16x32_bf16 a[252:255],v[188:191],v[156:159],a[252:255]\n"
"ds_read_b64_tr_b16 v[244:245],v103 offset:16384\n"
"v_mfma_f32_16x16x32_bf16 a[220:223],v[188:191],v[152:155],a[220:223]\n"
"ds_read_b64_tr_b16 v[246:247],v103 offset:17408\n"
"s_waitcnt lgkmcnt(8)\n"
"v_mfma_f32_16x16x32_bf16 a[0:3],v[160:163],v[128:131],a[0:3]\n"
"ds_read_b64_tr_b16 v[248:249],v104 offset:16384\n"
"v_mfma_f32_16x16x32_bf16 a[0:3],v[224:227],v[192:195],a[0:3]\n"
"ds_read_b64_tr_b16 v[250:251],v104 offset:17408\n"
"v_mfma_f32_16x16x32_bf16 a[4:7],v[228:231],v[192:195],a[4:7]\n"
"ds_read_b64_tr_b16 v[252:253],v105 offset:16384\n"
"v_mfma_f32_16x16x32_bf16 a[4:7],v[164:167],v[128:131],a[4:7]\n"
"ds_read_b64_tr_b16 v[254:255],v105 offset:17408\n"
"s_waitcnt lgkmcnt(8)\n"
"v_mfma_f32_16x16x32_bf16 a[8:11],v[168:171],v[128:131],a[8:11]\n"
"ds_read_b128 v[132:135],v82 offset:2048\n"
"v_mfma_f32_16x16x32_bf16 a[8:11],v[232:235],v[192:195],a[8:11]\n"
"ds_read_b128 v[196:199],v83 offset:2048\n"
"v_mfma_f32_16x16x32_bf16 a[12:15],v[236:239],v[192:195],a[12:15]\n"
"ds_read_b128 v[136:139],v82 offset:4096\n"
"v_mfma_f32_16x16x32_bf16 a[12:15],v[172:175],v[128:131],a[12:15]\n"
"ds_read_b128 v[200:203],v83 offset:4096\n"
"s_waitcnt lgkmcnt(8)\n"
"v_mfma_f32_16x16x32_bf16 a[16:19],v[176:179],v[128:131],a[16:19]\n"
"ds_read_b128 v[140:143],v82 offset:6144\n"
"v_mfma_f32_16x16x32_bf16 a[16:19],v[240:243],v[192:195],a[16:19]\n"
"ds_read_b128 v[204:207],v83 offset:6144\n"
"v_mfma_f32_16x16x32_bf16 a[20:23],v[244:247],v[192:195],a[20:23]\n"
"ds_read_b128 v[144:147],v82 offset:8192\n"
"v_mfma_f32_16x16x32_bf16 a[20:23],v[180:183],v[128:131],a[20:23]\n"
"ds_read_b128 v[208:211],v83 offset:8192\n"
"s_waitcnt lgkmcnt(8)\n"
"v_mfma_f32_16x16x32_bf16 a[24:27],v[184:187],v[128:131],a[24:27]\n"
"ds_read_b128 v[148:151],v82 offset:10240\n"
"v_mfma_f32_16x16x32_bf16 a[24:27],v[248:251],v[192:195],a[24:27]\n"
"ds_read_b128 v[212:215],v83 offset:10240\n"
"v_mfma_f32_16x16x32_bf16 a[28:31],v[252:255],v[192:195],a[28:31]\n"
"ds_read_b128 v[216:219],v83 offset:12288\n"
"v_mfma_f32_16x16x32_bf16 a[28:31],v[188:191],v[128:131],a[28:31]\n"
"ds_read_b128 v[220:223],v83 offset:14336\n"
"s_waitcnt lgkmcnt(10)\n"
"v_mfma_f32_16x16x32_bf16 a[32:35],v[160:163],v[132:135],a[32:35]\n"
"v_mfma_f32_16x16x32_bf16 a[32:35],v[224:227],v[196:199],a[32:35]\n"
"buffer_load_dwordx4 v[74:77],v112,s[24:27],s58 offen sc1\n"
"v_mfma_f32_16x16x32_bf16 a[36:39],v[228:231],v[196:199],a[36:39]\n"
"v_mfma_f32_16x16x32_bf16 a[36:39],v[164:167],v[132:135],a[36:39]\n"
"v_mfma_f32_16x16x32_bf16 a[40:43],v[168:171],v[132:135],a[40:43]\n"
"s_waitcnt lgkmcnt(0)\n"
"s_barrier\n"
"v_mfma_f32_16x16x32_bf16 a[40:43],v[232:235],v[196:199],a[40:43]\n"
"s_mov_b32 s77,3670016\n"
"s_add_u32 m0,s78,98304\n"
"v_mfma_f32_16x16x32_bf16 a[44:47],v[236:239],v[196:199],a[44:47]\n"
"buffer_load_dwordx4 v110,s[20:23],s77 offen offset:0 lds\n"
"v_mfma_f32_16x16x32_bf16 a[44:47],v[172:175],v[132:135],a[44:47]\n"
"v_mfma_f32_16x16x32_bf16 a[48:51],v[176:179],v[132:135],a[48:51]\n"
"s_load_dwordx2 s[48:49],s[0:1],56\n"
"v_mfma_f32_16x16x32_bf16 a[48:51],v[240:243],v[196:199],a[48:51]\n"
"v_mfma_f32_16x16x32_bf16 a[52:55],v[244:247],v[196:199],a[52:55]\n"
"buffer_load_dwordx4 v111,s[20:23],s77 offen offset:1024 lds\n"
"v_mfma_f32_16x16x32_bf16 a[52:55],v[180:183],v[132:135],a[52:55]\n"
"v_mfma_f32_16x16x32_bf16 a[56:59],v[184:187],v[132:135],a[56:59]\n"
"v_mfma_f32_16x16x32_bf16 a[56:59],v[248:251],v[196:199],a[56:59]\n"
"v_mfma_f32_16x16x32_bf16 a[60:63],v[252:255],v[196:199],a[60:63]\n"
"buffer_load_dwordx4 v113,s[20:23],s77 offen offset:2048 lds\n"
"v_mfma_f32_16x16x32_bf16 a[60:63],v[188:191],v[132:135],a[60:63]\n"
"v_mfma_f32_16x16x32_bf16 a[64:67],v[160:163],v[136:139],a[64:67]\n"
"v_mfma_f32_16x16x32_bf16 a[64:67],v[224:227],v[200:203],a[64:67]\n"
"v_mfma_f32_16x16x32_bf16 a[68:71],v[228:231],v[200:203],a[68:71]\n"
"buffer_load_dwordx4 v114,s[20:23],s77 offen offset:3072 lds\n"
"v_mfma_f32_16x16x32_bf16 a[68:71],v[164:167],v[136:139],a[68:71]\n"
"v_mfma_f32_16x16x32_bf16 a[72:75],v[168:171],v[136:139],a[72:75]\n"
"v_mfma_f32_16x16x32_bf16 a[72:75],v[232:235],v[200:203],a[72:75]\n"
"s_mov_b32 s77,3932160\n"
"s_add_u32 m0,s78,114688\n"
"v_mfma_f32_16x16x32_bf16 a[76:79],v[236:239],v[200:203],a[76:79]\n"
"buffer_load_dwordx4 v110,s[20:23],s77 offen offset:0 lds\n"
"v_mfma_f32_16x16x32_bf16 a[76:79],v[172:175],v[136:139],a[76:79]\n"
"v_mfma_f32_16x16x32_bf16 a[80:83],v[176:179],v[136:139],a[80:83]\n"
"v_mfma_f32_16x16x32_bf16 a[80:83],v[240:243],v[200:203],a[80:83]\n"
"v_mfma_f32_16x16x32_bf16 a[84:87],v[244:247],v[200:203],a[84:87]\n"
"buffer_load_dwordx4 v111,s[20:23],s77 offen offset:1024 lds\n"
"v_mfma_f32_16x16x32_bf16 a[84:87],v[180:183],v[136:139],a[84:87]\n"
"v_mfma_f32_16x16x32_bf16 a[88:91],v[184:187],v[136:139],a[88:91]\n"
"v_mfma_f32_16x16x32_bf16 a[88:91],v[248:251],v[200:203],a[88:91]\n"
"v_mfma_f32_16x16x32_bf16 a[92:95],v[252:255],v[200:203],a[92:95]\n"
"buffer_load_dwordx4 v113,s[20:23],s77 offen offset:2048 lds\n"
"v_mfma_f32_16x16x32_bf16 a[92:95],v[188:191],v[136:139],a[92:95]\n"
"s_mov_b32 s76,s55\n"
"s_waitcnt vmcnt(7)\n"
"v_mfma_f32_16x16x32_bf16 a[96:99],v[160:163],v[140:143],a[96:99]\n"
"v_xor_b32 v116,s80,v74\n"
"v_xor_b32 v117,s81,v75\n"
"v_mfma_f32_16x16x32_bf16 a[96:99],v[224:227],v[204:207],a[96:99]\n"
"v_or_b32 v116,v116,v117\n"
"v_xor_b32 v117,s80,v76\n"
"v_mfma_f32_16x16x32_bf16 a[100:103],v[228:231],v[204:207],a[100:103]\n"
"buffer_load_dwordx4 v114,s[20:23],s77 offen offset:3072 lds\n"
"v_mfma_f32_16x16x32_bf16 a[100:103],v[164:167],v[140:143],a[100:103]\n"
"v_or_b32 v116,v116,v117\n"
"v_xor_b32 v117,s81,v77\n"
"v_mfma_f32_16x16x32_bf16 a[104:107],v[168:171],v[140:143],a[104:107]\n"
"v_or_b32 v116,v116,v117\n"
"v_cmp_ne_u32 vcc,0,v116\n"
"v_mfma_f32_16x16x32_bf16 a[104:107],v[232:235],v[204:207],a[104:107]\n"
"s_cbranch_vccnz .Lfk_xn0_s\n"
".Lfk_xn0_ok:\n"
"v_mfma_f32_16x16x32_bf16 a[108:111],v[236:239],v[204:207],a[108:111]\n"
"s_add_u32 m0,s78,65536\n"
"v_mfma_f32_16x16x32_bf16 a[108:111],v[172:175],v[140:143],a[108:111]\n"
"buffer_load_dwordx4 v112,s[24:27],s76 offen offset:0 sc1 lds\n"
"v_mfma_f32_16x16x32_bf16 a[112:115],v[176:179],v[140:143],a[112:115]\n"
"s_waitcnt lgkmcnt(0)\n"
"s_lshl2_add_u32 s88,s100,s52\n"
"v_mfma_f32_16x16x32_bf16 a[112:115],v[240:243],v[204:207],a[112:115]\n"
"s_mul_i32 s88,s88,7168\n"
"s_add_u32 s88,s88,0\n"
"v_mfma_f32_16x16x32_bf16 a[116:119],v[244:247],v[204:207],a[116:119]\n"
"s_add_u32 s48,s48,s88\n"
"s_addc_u32 s49,s49,0\n"
"s_mov_b32 s50,3584\n"
"v_mfma_f32_16x16x32_bf16 a[116:119],v[180:183],v[140:143],a[116:119]\n"
"s_mov_b32 s51,0x20000\n"
"v_lshlrev_b32 v8,3,v112\n"
"v_mfma_f32_16x16x32_bf16 a[120:123],v[184:187],v[140:143],a[120:123]\n"
"buffer_load_dwordx4 v112,s[24:27],s76 offen offset:1024 sc1 lds\n"
"v_mfma_f32_16x16x32_bf16 a[120:123],v[248:251],v[204:207],a[120:123]\n"
"v_mfma_f32_16x16x32_bf16 a[124:127],v[252:255],v[204:207],a[124:127]\n"
"v_mfma_f32_16x16x32_bf16 a[124:127],v[188:191],v[140:143],a[124:127]\n"
"v_mfma_f32_16x16x32_bf16 a[128:131],v[160:163],v[144:147],a[128:131]\n"
"v_mfma_f32_16x16x32_bf16 a[128:131],v[224:227],v[208:211],a[128:131]\n"
"buffer_load_dwordx4 v112,s[24:27],s76 offen offset:2048 sc1 lds\n"
"v_mfma_f32_16x16x32_bf16 a[132:135],v[228:231],v[208:211],a[132:135]\n"
"v_mfma_f32_16x16x32_bf16 a[132:135],v[164:167],v[144:147],a[132:135]\n"
"v_mfma_f32_16x16x32_bf16 a[136:139],v[168:171],v[144:147],a[136:139]\n"
"v_mfma_f32_16x16x32_bf16 a[136:139],v[232:235],v[208:211],a[136:139]\n"
"buffer_load_dwordx4 v112,s[24:27],s76 offen offset:3072 sc1 lds\n"
"v_mfma_f32_16x16x32_bf16 a[140:143],v[236:239],v[208:211],a[140:143]\n"
"s_xor_b32 s76,s76,16384\n"
"v_mfma_f32_16x16x32_bf16 a[140:143],v[172:175],v[144:147],a[140:143]\n"
"v_mfma_f32_16x16x32_bf16 a[144:147],v[176:179],v[144:147],a[144:147]\n"
"v_mfma_f32_16x16x32_bf16 a[144:147],v[240:243],v[208:211],a[144:147]\n"
"v_mfma_f32_16x16x32_bf16 a[148:151],v[244:247],v[208:211],a[148:151]\n"
"s_add_u32 m0,s78,81920\n"
"v_mfma_f32_16x16x32_bf16 a[148:151],v[180:183],v[144:147],a[148:151]\n"
"buffer_load_dwordx4 v112,s[24:27],s76 offen offset:0 sc1 lds\n"
"v_mfma_f32_16x16x32_bf16 a[152:155],v[184:187],v[144:147],a[152:155]\n"
"v_mfma_f32_16x16x32_bf16 a[152:155],v[248:251],v[208:211],a[152:155]\n"
"v_mfma_f32_16x16x32_bf16 a[156:159],v[252:255],v[208:211],a[156:159]\n"
"v_mfma_f32_16x16x32_bf16 a[156:159],v[188:191],v[144:147],a[156:159]\n"
"v_mfma_f32_16x16x32_bf16 a[160:163],v[160:163],v[148:151],a[160:163]\n"
"buffer_load_dwordx4 v112,s[24:27],s76 offen offset:1024 sc1 lds\n"
"v_mfma_f32_16x16x32_bf16 a[160:163],v[224:227],v[212:215],a[160:163]\n"
"v_mfma_f32_16x16x32_bf16 a[164:167],v[228:231],v[212:215],a[164:167]\n"
"v_mfma_f32_16x16x32_bf16 a[164:167],v[164:167],v[148:151],a[164:167]\n"
"v_mfma_f32_16x16x32_bf16 a[168:171],v[168:171],v[148:151],a[168:171]\n"
"v_mfma_f32_16x16x32_bf16 a[168:171],v[232:235],v[212:215],a[168:171]\n"
"buffer_load_dwordx4 v112,s[24:27],s76 offen offset:2048 sc1 lds\n"
"v_mfma_f32_16x16x32_bf16 a[172:175],v[236:239],v[212:215],a[172:175]\n"
"v_mfma_f32_16x16x32_bf16 a[172:175],v[172:175],v[148:151],a[172:175]\n"
"v_mfma_f32_16x16x32_bf16 a[176:179],v[176:179],v[148:151],a[176:179]\n"
"v_mfma_f32_16x16x32_bf16 a[176:179],v[240:243],v[212:215],a[176:179]\n"
"buffer_load_dwordx4 v112,s[24:27],s76 offen offset:3072 sc1 lds\n"
"v_mfma_f32_16x16x32_bf16 a[180:183],v[244:247],v[212:215],a[180:183]\n"
"s_xor_b32 s76,s76,49152\n"
"v_mfma_f32_16x16x32_bf16 a[180:183],v[180:183],v[148:151],a[180:183]\n"
"v_mfma_f32_16x16x32_bf16 a[184:187],v[184:187],v[148:151],a[184:187]\n"
"buffer_load_dword v115,v8,s[48:51],0 offen\n"
"v_mfma_f32_16x16x32_bf16 a[184:187],v[248:251],v[212:215],a[184:187]\n"
"v_mfma_f32_16x16x32_bf16 a[188:191],v[252:255],v[212:215],a[188:191]\n"
"v_mfma_f32_16x16x32_bf16 a[188:191],v[188:191],v[148:151],a[188:191]\n"
"s_barrier\n"
"v_mfma_f32_16x16x32_bf16 a[192:195],v[224:227],v[216:219],a[192:195]\n"
"ds_read_b128 v[152:155],v80 offset:12288\n"
"v_mfma_f32_16x16x32_bf16 a[224:227],v[224:227],v[220:223],a[224:227]\n"
"ds_read_b128 v[156:159],v80 offset:14336\n"
"v_mfma_f32_16x16x32_bf16 a[228:231],v[228:231],v[220:223],a[228:231]\n"
"ds_read_b64_tr_b16 v[160:161],v98 offset:49152\n"
"v_mfma_f32_16x16x32_bf16 a[196:199],v[228:231],v[216:219],a[196:199]\n"
"ds_read_b64_tr_b16 v[162:163],v98 offset:50176\n"
"v_mfma_f32_16x16x32_bf16 a[200:203],v[232:235],v[216:219],a[200:203]\n"
"ds_read_b64_tr_b16 v[164:165],v99 offset:49152\n"
"v_mfma_f32_16x16x32_bf16 a[232:235],v[232:235],v[220:223],a[232:235]\n"
"ds_read_b64_tr_b16 v[166:167],v99 offset:50176\n"
"v_mfma_f32_16x16x32_bf16 a[236:239],v[236:239],v[220:223],a[236:239]\n"
"ds_read_b64_tr_b16 v[168:169],v100 offset:49152\n"
"v_mfma_f32_16x16x32_bf16 a[204:207],v[236:239],v[216:219],a[204:207]\n"
"ds_read_b64_tr_b16 v[170:171],v100 offset:50176\n"
"v_mfma_f32_16x16x32_bf16 a[208:211],v[240:243],v[216:219],a[208:211]\n"
"ds_read_b64_tr_b16 v[172:173],v101 offset:49152\n"
"v_mfma_f32_16x16x32_bf16 a[240:243],v[240:243],v[220:223],a[240:243]\n"
"ds_read_b64_tr_b16 v[174:175],v101 offset:50176\n"
"v_mfma_f32_16x16x32_bf16 a[244:247],v[244:247],v[220:223],a[244:247]\n"
"ds_read_b64_tr_b16 v[176:177],v102 offset:49152\n"
"v_mfma_f32_16x16x32_bf16 a[212:215],v[244:247],v[216:219],a[212:215]\n"
"ds_read_b64_tr_b16 v[178:179],v102 offset:50176\n"
"v_mfma_f32_16x16x32_bf16 a[216:219],v[248:251],v[216:219],a[216:219]\n"
"ds_read_b64_tr_b16 v[180:181],v103 offset:49152\n"
"v_mfma_f32_16x16x32_bf16 a[248:251],v[248:251],v[220:223],a[248:251]\n"
"ds_read_b64_tr_b16 v[182:183],v103 offset:50176\n"
"ds_read_b64_tr_b16 v[184:185],v104 offset:49152\n"
"v_mfma_f32_16x16x32_bf16 a[252:255],v[252:255],v[220:223],a[252:255]\n"
"v_mov_b32 v110,v72\n"
"v_mov_b32 v111,v73\n"
"v_mfma_f32_16x16x32_bf16 a[220:223],v[252:255],v[216:219],a[220:223]\n"
"ds_read_b64_tr_b16 v[186:187],v104 offset:50176\n"
"s_waitcnt lgkmcnt(10)\n"
"v_mfma_f32_16x16x32_bf16 a[192:195],v[160:163],v[152:155],a[192:195]\n"
"ds_read_b64_tr_b16 v[188:189],v105 offset:49152\n"
"v_mfma_f32_16x16x32_bf16 a[224:227],v[160:163],v[156:159],a[224:227]\n"
"ds_read_b64_tr_b16 v[190:191],v105 offset:50176\n"
"v_mfma_f32_16x16x32_bf16 a[228:231],v[164:167],v[156:159],a[228:231]\n"
"ds_read_b128 v[128:131],v80 offset:0\n"
"v_mfma_f32_16x16x32_bf16 a[196:199],v[164:167],v[152:155],a[196:199]\n"
"ds_read_b128 v[192:195],v81 offset:0\n"
"s_waitcnt lgkmcnt(10)\n"
"v_mfma_f32_16x16x32_bf16 a[200:203],v[168:171],v[152:155],a[200:203]\n"
"ds_read_b64_tr_b16 v[224:225],v98 offset:32768\n"
"v_mfma_f32_16x16x32_bf16 a[232:235],v[168:171],v[156:159],a[232:235]\n"
"ds_read_b64_tr_b16 v[226:227],v98 offset:33792\n"
"v_mfma_f32_16x16x32_bf16 a[236:239],v[172:175],v[156:159],a[236:239]\n"
"ds_read_b64_tr_b16 v[228:229],v99 offset:32768\n"
"v_mfma_f32_16x16x32_bf16 a[204:207],v[172:175],v[152:155],a[204:207]\n"
"ds_read_b64_tr_b16 v[230:231],v99 offset:33792\n"
"s_waitcnt lgkmcnt(10)\n"
"v_mfma_f32_16x16x32_bf16 a[208:211],v[176:179],v[152:155],a[208:211]\n"
"ds_read_b64_tr_b16 v[232:233],v100 offset:32768\n"
"v_mfma_f32_16x16x32_bf16 a[240:243],v[176:179],v[156:159],a[240:243]\n"
"ds_read_b64_tr_b16 v[234:235],v100 offset:33792\n"
"v_mfma_f32_16x16x32_bf16 a[244:247],v[180:183],v[156:159],a[244:247]\n"
"ds_read_b64_tr_b16 v[236:237],v101 offset:32768\n"
"v_mfma_f32_16x16x32_bf16 a[212:215],v[180:183],v[152:155],a[212:215]\n"
"ds_read_b64_tr_b16 v[238:239],v101 offset:33792\n"
"s_waitcnt lgkmcnt(10)\n"
"v_mfma_f32_16x16x32_bf16 a[216:219],v[184:187],v[152:155],a[216:219]\n"
"ds_read_b64_tr_b16 v[240:241],v102 offset:32768\n"
"v_mfma_f32_16x16x32_bf16 a[248:251],v[184:187],v[156:159],a[248:251]\n"
"ds_read_b64_tr_b16 v[242:243],v102 offset:33792\n"
"v_mfma_f32_16x16x32_bf16 a[252:255],v[188:191],v[156:159],a[252:255]\n"
"ds_read_b64_tr_b16 v[244:245],v103 offset:32768\n"
"v_mfma_f32_16x16x32_bf16 a[220:223],v[188:191],v[152:155],a[220:223]\n"
"ds_read_b64_tr_b16 v[246:247],v103 offset:33792\n"
"s_waitcnt lgkmcnt(8)\n"
"v_mfma_f32_16x16x32_bf16 a[0:3],v[160:163],v[128:131],a[0:3]\n"
"ds_read_b64_tr_b16 v[248:249],v104 offset:32768\n"
"v_mfma_f32_16x16x32_bf16 a[0:3],v[224:227],v[192:195],a[0:3]\n"
"ds_read_b64_tr_b16 v[250:251],v104 offset:33792\n"
"v_mfma_f32_16x16x32_bf16 a[4:7],v[228:231],v[192:195],a[4:7]\n"
"ds_read_b64_tr_b16 v[252:253],v105 offset:32768\n"
"v_mfma_f32_16x16x32_bf16 a[4:7],v[164:167],v[128:131],a[4:7]\n"
"ds_read_b64_tr_b16 v[254:255],v105 offset:33792\n"
"s_waitcnt lgkmcnt(8)\n"
"v_mfma_f32_16x16x32_bf16 a[8:11],v[168:171],v[128:131],a[8:11]\n"
"ds_read_b128 v[132:135],v80 offset:2048\n"
"v_mfma_f32_16x16x32_bf16 a[8:11],v[232:235],v[192:195],a[8:11]\n"
"ds_read_b128 v[196:199],v81 offset:2048\n"
"v_mfma_f32_16x16x32_bf16 a[12:15],v[236:239],v[192:195],a[12:15]\n"
"ds_read_b128 v[136:139],v80 offset:4096\n"
"v_mfma_f32_16x16x32_bf16 a[12:15],v[172:175],v[128:131],a[12:15]\n"
"ds_read_b128 v[200:203],v81 offset:4096\n"
"s_waitcnt lgkmcnt(8)\n"
"v_mfma_f32_16x16x32_bf16 a[16:19],v[176:179],v[128:131],a[16:19]\n"
"ds_read_b128 v[140:143],v80 offset:6144\n"
"v_mfma_f32_16x16x32_bf16 a[16:19],v[240:243],v[192:195],a[16:19]\n"
"ds_read_b128 v[204:207],v81 offset:6144\n"
"v_mfma_f32_16x16x32_bf16 a[20:23],v[244:247],v[192:195],a[20:23]\n"
"ds_read_b128 v[144:147],v80 offset:8192\n"
"v_mfma_f32_16x16x32_bf16 a[20:23],v[180:183],v[128:131],a[20:23]\n"
"ds_read_b128 v[208:211],v81 offset:8192\n"
"s_waitcnt lgkmcnt(8)\n"
"v_mfma_f32_16x16x32_bf16 a[24:27],v[184:187],v[128:131],a[24:27]\n"
"ds_read_b128 v[148:151],v80 offset:10240\n"
"v_mfma_f32_16x16x32_bf16 a[24:27],v[248:251],v[192:195],a[24:27]\n"
"ds_read_b128 v[212:215],v81 offset:10240\n"
"v_mfma_f32_16x16x32_bf16 a[28:31],v[252:255],v[192:195],a[28:31]\n"
"ds_read_b128 v[216:219],v81 offset:12288\n"
"v_mfma_f32_16x16x32_bf16 a[28:31],v[188:191],v[128:131],a[28:31]\n"
"ds_read_b128 v[220:223],v81 offset:14336\n"
"s_waitcnt lgkmcnt(10)\n"
"v_mfma_f32_16x16x32_bf16 a[32:35],v[160:163],v[132:135],a[32:35]\n"
"v_mfma_f32_16x16x32_bf16 a[32:35],v[224:227],v[196:199],a[32:35]\n"
"v_mfma_f32_16x16x32_bf16 a[36:39],v[228:231],v[196:199],a[36:39]\n"
"v_mfma_f32_16x16x32_bf16 a[36:39],v[164:167],v[132:135],a[36:39]\n"
"v_mfma_f32_16x16x32_bf16 a[40:43],v[168:171],v[132:135],a[40:43]\n"
"s_waitcnt lgkmcnt(0)\n"
"s_barrier\n"
"v_mfma_f32_16x16x32_bf16 a[40:43],v[232:235],v[196:199],a[40:43]\n"
"s_add_u32 m0,s85,147456\n"
"v_mfma_f32_16x16x32_bf16 a[44:47],v[236:239],v[196:199],a[44:47]\n"
"buffer_load_dwordx4 v110,s[28:31],s86 offen lds\n"
"v_mfma_f32_16x16x32_bf16 a[44:47],v[172:175],v[132:135],a[44:47]\n"
"v_mfma_f32_16x16x32_bf16 a[48:51],v[176:179],v[132:135],a[48:51]\n"
"s_load_dwordx2 s[48:49],s[0:1],56\n"
"v_mfma_f32_16x16x32_bf16 a[48:51],v[240:243],v[196:199],a[48:51]\n"
"s_add_u32 m0,s85,148480\n"
"v_mfma_f32_16x16x32_bf16 a[52:55],v[244:247],v[196:199],a[52:55]\n"
"buffer_load_dwordx4 v111,s[28:31],s86 offen lds\n"
"v_mfma_f32_16x16x32_bf16 a[52:55],v[180:183],v[132:135],a[52:55]\n"
"s_xor_b32 s86,s86,262144\n"
"v_mfma_f32_16x16x32_bf16 a[56:59],v[184:187],v[132:135],a[56:59]\n"
"v_mfma_f32_16x16x32_bf16 a[56:59],v[248:251],v[196:199],a[56:59]\n"
"s_add_u32 m0,s85,155648\n"
"v_mfma_f32_16x16x32_bf16 a[60:63],v[252:255],v[196:199],a[60:63]\n"
"buffer_load_dwordx4 v110,s[28:31],s86 offen lds\n"
"v_mfma_f32_16x16x32_bf16 a[60:63],v[188:191],v[132:135],a[60:63]\n"
"v_mfma_f32_16x16x32_bf16 a[64:67],v[160:163],v[136:139],a[64:67]\n"
"v_mfma_f32_16x16x32_bf16 a[64:67],v[224:227],v[200:203],a[64:67]\n"
"s_add_u32 m0,s85,156672\n"
"v_mfma_f32_16x16x32_bf16 a[68:71],v[228:231],v[200:203],a[68:71]\n"
"buffer_load_dwordx4 v111,s[28:31],s86 offen lds\n"
"v_mfma_f32_16x16x32_bf16 a[68:71],v[164:167],v[136:139],a[68:71]\n"
"s_xor_b32 s86,s86,786432\n"
"v_mfma_f32_16x16x32_bf16 a[72:75],v[168:171],v[136:139],a[72:75]\n"
"v_mfma_f32_16x16x32_bf16 a[72:75],v[232:235],v[200:203],a[72:75]\n"
"s_add_u32 m0,s85,131072\n"
"v_mfma_f32_16x16x32_bf16 a[76:79],v[236:239],v[200:203],a[76:79]\n"
"buffer_load_dwordx4 v110,s[28:31],s86 offen lds\n"
"v_mfma_f32_16x16x32_bf16 a[76:79],v[172:175],v[136:139],a[76:79]\n"
"v_mfma_f32_16x16x32_bf16 a[80:83],v[176:179],v[136:139],a[80:83]\n"
"v_mfma_f32_16x16x32_bf16 a[80:83],v[240:243],v[200:203],a[80:83]\n"
"s_add_u32 m0,s85,132096\n"
"v_mfma_f32_16x16x32_bf16 a[84:87],v[244:247],v[200:203],a[84:87]\n"
"buffer_load_dwordx4 v111,s[28:31],s86 offen lds\n"
"v_mfma_f32_16x16x32_bf16 a[84:87],v[180:183],v[136:139],a[84:87]\n"
"s_xor_b32 s86,s86,262144\n"
"v_mfma_f32_16x16x32_bf16 a[88:91],v[184:187],v[136:139],a[88:91]\n"
"v_mfma_f32_16x16x32_bf16 a[88:91],v[248:251],v[200:203],a[88:91]\n"
"v_mfma_f32_16x16x32_bf16 a[92:95],v[252:255],v[200:203],a[92:95]\n"
"v_mfma_f32_16x16x32_bf16 a[92:95],v[188:191],v[136:139],a[92:95]\n"
"v_mfma_f32_16x16x32_bf16 a[96:99],v[160:163],v[140:143],a[96:99]\n"
"v_mfma_f32_16x16x32_bf16 a[96:99],v[224:227],v[204:207],a[96:99]\n"
"s_add_u32 m0,s85,139264\n"
"v_mfma_f32_16x16x32_bf16 a[100:103],v[228:231],v[204:207],a[100:103]\n"
"buffer_load_dwordx4 v110,s[28:31],s86 offen lds\n"
"v_mfma_f32_16x16x32_bf16 a[100:103],v[164:167],v[140:143],a[100:103]\n"
"v_mfma_f32_16x16x32_bf16 a[104:107],v[168:171],v[140:143],a[104:107]\n"
"v_mfma_f32_16x16x32_bf16 a[104:107],v[232:235],v[204:207],a[104:107]\n"
"s_add_u32 m0,s85,140288\n"
"v_mfma_f32_16x16x32_bf16 a[108:111],v[236:239],v[204:207],a[108:111]\n"
"buffer_load_dwordx4 v111,s[28:31],s86 offen lds\n"
"v_mfma_f32_16x16x32_bf16 a[108:111],v[172:175],v[140:143],a[108:111]\n"
"s_xor_b32 s86,s86,1835008\n"
"v_mfma_f32_16x16x32_bf16 a[112:115],v[176:179],v[140:143],a[112:115]\n"
"s_waitcnt lgkmcnt(0)\n"
"s_lshl2_add_u32 s88,s100,s52\n"
"v_mfma_f32_16x16x32_bf16 a[112:115],v[240:243],v[204:207],a[112:115]\n"
"s_mul_i32 s88,s88,7168\n"
"s_add_u32 m0,s78,0\n"
"v_mfma_f32_16x16x32_bf16 a[116:119],v[244:247],v[204:207],a[116:119]\n"
"buffer_load_dwordx4 v112,s[24:27],s76 offen offset:0 sc1 lds\n"
"v_mfma_f32_16x16x32_bf16 a[116:119],v[180:183],v[140:143],a[116:119]\n"
"s_add_u32 s88,s88,3584\n"
"s_add_u32 s48,s48,s88\n"
"s_addc_u32 s49,s49,0\n"
"v_mfma_f32_16x16x32_bf16 a[120:123],v[184:187],v[140:143],a[120:123]\n"
"s_mov_b32 s50,3584\n"
"s_mov_b32 s51,0x20000\n"
"v_mfma_f32_16x16x32_bf16 a[120:123],v[248:251],v[204:207],a[120:123]\n"
"v_lshlrev_b32 v8,3,v112\n"
"v_mfma_f32_16x16x32_bf16 a[124:127],v[252:255],v[204:207],a[124:127]\n"
"buffer_load_dwordx4 v112,s[24:27],s76 offen offset:1024 sc1 lds\n"
"v_mfma_f32_16x16x32_bf16 a[124:127],v[188:191],v[140:143],a[124:127]\n"
"v_mfma_f32_16x16x32_bf16 a[128:131],v[160:163],v[144:147],a[128:131]\n"
"v_mfma_f32_16x16x32_bf16 a[128:131],v[224:227],v[208:211],a[128:131]\n"
"v_mfma_f32_16x16x32_bf16 a[132:135],v[228:231],v[208:211],a[132:135]\n"
"buffer_load_dwordx4 v112,s[24:27],s76 offen offset:2048 sc1 lds\n"
"v_mfma_f32_16x16x32_bf16 a[132:135],v[164:167],v[144:147],a[132:135]\n"
"v_mfma_f32_16x16x32_bf16 a[136:139],v[168:171],v[144:147],a[136:139]\n"
"v_mfma_f32_16x16x32_bf16 a[136:139],v[232:235],v[208:211],a[136:139]\n"
"buffer_load_dwordx4 v112,s[24:27],s76 offen offset:3072 sc1 lds\n"
"v_mfma_f32_16x16x32_bf16 a[140:143],v[236:239],v[208:211],a[140:143]\n"
"s_xor_b32 s76,s76,16384\n"
"v_mfma_f32_16x16x32_bf16 a[140:143],v[172:175],v[144:147],a[140:143]\n"
"v_mfma_f32_16x16x32_bf16 a[144:147],v[176:179],v[144:147],a[144:147]\n"
"v_mfma_f32_16x16x32_bf16 a[144:147],v[240:243],v[208:211],a[144:147]\n"
"s_add_u32 m0,s78,16384\n"
"v_mfma_f32_16x16x32_bf16 a[148:151],v[244:247],v[208:211],a[148:151]\n"
"buffer_load_dwordx4 v112,s[24:27],s76 offen offset:0 sc1 lds\n"
"v_mfma_f32_16x16x32_bf16 a[148:151],v[180:183],v[144:147],a[148:151]\n"
"v_mfma_f32_16x16x32_bf16 a[152:155],v[184:187],v[144:147],a[152:155]\n"
"v_mfma_f32_16x16x32_bf16 a[152:155],v[248:251],v[208:211],a[152:155]\n"
"v_mfma_f32_16x16x32_bf16 a[156:159],v[252:255],v[208:211],a[156:159]\n"
"buffer_load_dwordx4 v112,s[24:27],s76 offen offset:1024 sc1 lds\n"
"v_mfma_f32_16x16x32_bf16 a[156:159],v[188:191],v[144:147],a[156:159]\n"
"v_mfma_f32_16x16x32_bf16 a[160:163],v[160:163],v[148:151],a[160:163]\n"
"v_mfma_f32_16x16x32_bf16 a[160:163],v[224:227],v[212:215],a[160:163]\n"
"v_mfma_f32_16x16x32_bf16 a[164:167],v[228:231],v[212:215],a[164:167]\n"
"buffer_load_dwordx4 v112,s[24:27],s76 offen offset:2048 sc1 lds\n"
"v_mfma_f32_16x16x32_bf16 a[164:167],v[164:167],v[148:151],a[164:167]\n"
"v_mfma_f32_16x16x32_bf16 a[168:171],v[168:171],v[148:151],a[168:171]\n"
"v_mfma_f32_16x16x32_bf16 a[168:171],v[232:235],v[212:215],a[168:171]\n"
"buffer_load_dwordx4 v112,s[24:27],s76 offen offset:3072 sc1 lds\n"
"v_mfma_f32_16x16x32_bf16 a[172:175],v[236:239],v[212:215],a[172:175]\n"
"s_xor_b32 s76,s76,114688\n"
"v_mfma_f32_16x16x32_bf16 a[172:175],v[172:175],v[148:151],a[172:175]\n"
"v_mfma_f32_16x16x32_bf16 a[176:179],v[176:179],v[148:151],a[176:179]\n"
"buffer_load_dword v115,v8,s[48:51],0 offen\n"
"v_mfma_f32_16x16x32_bf16 a[176:179],v[240:243],v[212:215],a[176:179]\n"
"v_mfma_f32_16x16x32_bf16 a[180:183],v[244:247],v[212:215],a[180:183]\n"
"v_mfma_f32_16x16x32_bf16 a[180:183],v[180:183],v[148:151],a[180:183]\n"
"v_mfma_f32_16x16x32_bf16 a[184:187],v[184:187],v[148:151],a[184:187]\n"
"v_mfma_f32_16x16x32_bf16 a[184:187],v[248:251],v[212:215],a[184:187]\n"
"v_mfma_f32_16x16x32_bf16 a[188:191],v[252:255],v[212:215],a[188:191]\n"
"v_mfma_f32_16x16x32_bf16 a[188:191],v[188:191],v[148:151],a[188:191]\n"
"s_waitcnt vmcnt(26)\n"
"s_barrier\n"
"v_mfma_f32_16x16x32_bf16 a[192:195],v[224:227],v[216:219],a[192:195]\n"
"ds_read_b128 v[152:155],v80 offset:45056\n"
"v_mfma_f32_16x16x32_bf16 a[224:227],v[224:227],v[220:223],a[224:227]\n"
"ds_read_b128 v[156:159],v80 offset:47104\n"
"v_mfma_f32_16x16x32_bf16 a[228:231],v[228:231],v[220:223],a[228:231]\n"
"ds_read_b64_tr_b16 v[160:161],v98 offset:0\n"
"v_mfma_f32_16x16x32_bf16 a[196:199],v[228:231],v[216:219],a[196:199]\n"
"ds_read_b64_tr_b16 v[162:163],v98 offset:1024\n"
"v_mfma_f32_16x16x32_bf16 a[200:203],v[232:235],v[216:219],a[200:203]\n"
"ds_read_b64_tr_b16 v[164:165],v99 offset:0\n"
"v_mfma_f32_16x16x32_bf16 a[232:235],v[232:235],v[220:223],a[232:235]\n"
"ds_read_b64_tr_b16 v[166:167],v99 offset:1024\n"
"v_mfma_f32_16x16x32_bf16 a[236:239],v[236:239],v[220:223],a[236:239]\n"
"ds_read_b64_tr_b16 v[168:169],v100 offset:0\n"
"v_mfma_f32_16x16x32_bf16 a[204:207],v[236:239],v[216:219],a[204:207]\n"
"ds_read_b64_tr_b16 v[170:171],v100 offset:1024\n"
"v_mfma_f32_16x16x32_bf16 a[208:211],v[240:243],v[216:219],a[208:211]\n"
"ds_read_b64_tr_b16 v[172:173],v101 offset:0\n"
"v_mfma_f32_16x16x32_bf16 a[240:243],v[240:243],v[220:223],a[240:243]\n"
"ds_read_b64_tr_b16 v[174:175],v101 offset:1024\n"
"v_mfma_f32_16x16x32_bf16 a[244:247],v[244:247],v[220:223],a[244:247]\n"
"ds_read_b64_tr_b16 v[176:177],v102 offset:0\n"
"v_mfma_f32_16x16x32_bf16 a[212:215],v[244:247],v[216:219],a[212:215]\n"
"ds_read_b64_tr_b16 v[178:179],v102 offset:1024\n"
"v_mfma_f32_16x16x32_bf16 a[216:219],v[248:251],v[216:219],a[216:219]\n"
"ds_read_b64_tr_b16 v[180:181],v103 offset:0\n"
"v_mfma_f32_16x16x32_bf16 a[248:251],v[248:251],v[220:223],a[248:251]\n"
"ds_read_b64_tr_b16 v[182:183],v103 offset:1024\n"
"v_mfma_f32_16x16x32_bf16 a[252:255],v[252:255],v[220:223],a[252:255]\n"
"ds_read_b64_tr_b16 v[184:185],v104 offset:0\n"
"v_mfma_f32_16x16x32_bf16 a[220:223],v[252:255],v[216:219],a[220:223]\n"
"ds_read_b64_tr_b16 v[186:187],v104 offset:1024\n"
"s_waitcnt lgkmcnt(10)\n"
"v_mfma_f32_16x16x32_bf16 a[192:195],v[160:163],v[152:155],a[192:195]\n"
"ds_read_b64_tr_b16 v[188:189],v105 offset:0\n"
"v_mfma_f32_16x16x32_bf16 a[224:227],v[160:163],v[156:159],a[224:227]\n"
"ds_read_b64_tr_b16 v[190:191],v105 offset:1024\n"
"v_mfma_f32_16x16x32_bf16 a[228:231],v[164:167],v[156:159],a[228:231]\n"
"ds_read_b128 v[128:131],v80 offset:32768\n"
"v_mfma_f32_16x16x32_bf16 a[196:199],v[164:167],v[152:155],a[196:199]\n"
"ds_read_b128 v[192:195],v81 offset:32768\n"
"s_waitcnt lgkmcnt(10)\n"
"v_mfma_f32_16x16x32_bf16 a[200:203],v[168:171],v[152:155],a[200:203]\n"
"ds_read_b64_tr_b16 v[224:225],v98 offset:16384\n"
"v_mfma_f32_16x16x32_bf16 a[232:235],v[168:171],v[156:159],a[232:235]\n"
"ds_read_b64_tr_b16 v[226:227],v98 offset:17408\n"
"v_mfma_f32_16x16x32_bf16 a[236:239],v[172:175],v[156:159],a[236:239]\n"
"ds_read_b64_tr_b16 v[228:229],v99 offset:16384\n"
"v_mfma_f32_16x16x32_bf16 a[204:207],v[172:175],v[152:155],a[204:207]\n"
"ds_read_b64_tr_b16 v[230:231],v99 offset:17408\n"
"s_waitcnt lgkmcnt(10)\n"
"v_mfma_f32_16x16x32_bf16 a[208:211],v[176:179],v[152:155],a[208:211]\n"
"ds_read_b64_tr_b16 v[232:233],v100 offset:16384\n"
"v_mfma_f32_16x16x32_bf16 a[240:243],v[176:179],v[156:159],a[240:243]\n"
"ds_read_b64_tr_b16 v[234:235],v100 offset:17408\n"
"v_mfma_f32_16x16x32_bf16 a[244:247],v[180:183],v[156:159],a[244:247]\n"
"ds_read_b64_tr_b16 v[236:237],v101 offset:16384\n"
"v_mfma_f32_16x16x32_bf16 a[212:215],v[180:183],v[152:155],a[212:215]\n"
"ds_read_b64_tr_b16 v[238:239],v101 offset:17408\n"
"s_waitcnt lgkmcnt(10)\n"
"v_mfma_f32_16x16x32_bf16 a[216:219],v[184:187],v[152:155],a[216:219]\n"
"ds_read_b64_tr_b16 v[240:241],v102 offset:16384\n"
"v_mfma_f32_16x16x32_bf16 a[248:251],v[184:187],v[156:159],a[248:251]\n"
"ds_read_b64_tr_b16 v[242:243],v102 offset:17408\n"
"v_mfma_f32_16x16x32_bf16 a[252:255],v[188:191],v[156:159],a[252:255]\n"
"ds_read_b64_tr_b16 v[244:245],v103 offset:16384\n"
"v_mfma_f32_16x16x32_bf16 a[220:223],v[188:191],v[152:155],a[220:223]\n"
"ds_read_b64_tr_b16 v[246:247],v103 offset:17408\n"
"s_waitcnt lgkmcnt(8)\n"
"v_mfma_f32_16x16x32_bf16 a[0:3],v[160:163],v[128:131],a[0:3]\n"
"ds_read_b64_tr_b16 v[248:249],v104 offset:16384\n"
"v_mfma_f32_16x16x32_bf16 a[0:3],v[224:227],v[192:195],a[0:3]\n"
"ds_read_b64_tr_b16 v[250:251],v104 offset:17408\n"
"v_mfma_f32_16x16x32_bf16 a[4:7],v[228:231],v[192:195],a[4:7]\n"
"ds_read_b64_tr_b16 v[252:253],v105 offset:16384\n"
"v_mfma_f32_16x16x32_bf16 a[4:7],v[164:167],v[128:131],a[4:7]\n"
"ds_read_b64_tr_b16 v[254:255],v105 offset:17408\n"
"s_waitcnt lgkmcnt(8)\n"
"v_mfma_f32_16x16x32_bf16 a[8:11],v[168:171],v[128:131],a[8:11]\n"
"ds_read_b128 v[132:135],v80 offset:34816\n"
"v_mfma_f32_16x16x32_bf16 a[8:11],v[232:235],v[192:195],a[8:11]\n"
"ds_read_b128 v[196:199],v81 offset:34816\n"
"v_mfma_f32_16x16x32_bf16 a[12:15],v[236:239],v[192:195],a[12:15]\n"
"ds_read_b128 v[136:139],v80 offset:36864\n"
"v_mfma_f32_16x16x32_bf16 a[12:15],v[172:175],v[128:131],a[12:15]\n"
"ds_read_b128 v[200:203],v81 offset:36864\n"
"s_waitcnt lgkmcnt(8)\n"
"v_mfma_f32_16x16x32_bf16 a[16:19],v[176:179],v[128:131],a[16:19]\n"
"s_and_b32 s82,s52,1\n"
"ds_read_b128 v[140:143],v80 offset:38912\n"
"s_lshl_b32 s82,s82,13\n"
"v_mfma_f32_16x16x32_bf16 a[16:19],v[240:243],v[192:195],a[16:19]\n"
"ds_read_b128 v[204:207],v81 offset:38912\n"
"v_mfma_f32_16x16x32_bf16 a[20:23],v[244:247],v[192:195],a[20:23]\n"
"ds_read_b128 v[144:147],v80 offset:40960\n"
"v_mfma_f32_16x16x32_bf16 a[20:23],v[180:183],v[128:131],a[20:23]\n"
"ds_read_b128 v[208:211],v81 offset:40960\n"
"s_waitcnt lgkmcnt(8)\n"
"v_mfma_f32_16x16x32_bf16 a[24:27],v[184:187],v[128:131],a[24:27]\n"
"ds_read_b128 v[148:151],v80 offset:43008\n"
"v_mfma_f32_16x16x32_bf16 a[24:27],v[248:251],v[192:195],a[24:27]\n"
"ds_read_b128 v[212:215],v81 offset:43008\n"
"v_subrev_u32 v98,s82,v98\n"
"v_mfma_f32_16x16x32_bf16 a[28:31],v[252:255],v[192:195],a[28:31]\n"
"ds_read_b128 v[216:219],v81 offset:45056\n"
"v_add_u32 v96,s78,v97\n"
"v_mfma_f32_16x16x32_bf16 a[28:31],v[188:191],v[128:131],a[28:31]\n"
"ds_read_b128 v[220:223],v81 offset:47104\n"
"s_waitcnt lgkmcnt(10)\n"
"v_mfma_f32_16x16x32_bf16 a[32:35],v[160:163],v[132:135],a[32:35]\n"
"v_add_u32 v97,65536,v96\n"
"v_mfma_f32_16x16x32_bf16 a[32:35],v[224:227],v[196:199],a[32:35]\n"
"v_subrev_u32 v99,s82,v99\n"
"v_mfma_f32_16x16x32_bf16 a[36:39],v[228:231],v[196:199],a[36:39]\n"
"v_mfma_f32_16x16x32_bf16 a[36:39],v[164:167],v[132:135],a[36:39]\n"
"v_mfma_f32_16x16x32_bf16 a[40:43],v[168:171],v[132:135],a[40:43]\n"
"s_waitcnt lgkmcnt(0)\n"
"s_barrier\n"
"v_mfma_f32_16x16x32_bf16 a[40:43],v[232:235],v[196:199],a[40:43]\n"
"v_subrev_u32 v100,s82,v100\n"
"s_add_u32 m0,s85,98304\n"
"v_mfma_f32_16x16x32_bf16 a[44:47],v[236:239],v[196:199],a[44:47]\n"
"buffer_load_dwordx4 v110,s[28:31],s86 offen lds\n"
"v_mfma_f32_16x16x32_bf16 a[44:47],v[172:175],v[132:135],a[44:47]\n"
"v_mfma_f32_16x16x32_bf16 a[48:51],v[176:179],v[132:135],a[48:51]\n"
"v_mfma_f32_16x16x32_bf16 a[48:51],v[240:243],v[196:199],a[48:51]\n"
"v_subrev_u32 v101,s82,v101\n"
"s_add_u32 m0,s85,99328\n"
"v_mfma_f32_16x16x32_bf16 a[52:55],v[244:247],v[196:199],a[52:55]\n"
"buffer_load_dwordx4 v111,s[28:31],s86 offen lds\n"
"v_mfma_f32_16x16x32_bf16 a[52:55],v[180:183],v[132:135],a[52:55]\n"
"s_xor_b32 s86,s86,262144\n"
"v_mfma_f32_16x16x32_bf16 a[56:59],v[184:187],v[132:135],a[56:59]\n"
"v_mfma_f32_16x16x32_bf16 a[56:59],v[248:251],v[196:199],a[56:59]\n"
"v_subrev_u32 v102,s82,v102\n"
"s_add_u32 m0,s78,32768\n"
"v_mfma_f32_16x16x32_bf16 a[60:63],v[252:255],v[196:199],a[60:63]\n"
"buffer_load_dwordx4 v112,s[24:27],s76 offen offset:0 sc1 lds\n"
"v_mfma_f32_16x16x32_bf16 a[60:63],v[188:191],v[132:135],a[60:63]\n"
"v_mfma_f32_16x16x32_bf16 a[64:67],v[160:163],v[136:139],a[64:67]\n"
"v_mfma_f32_16x16x32_bf16 a[64:67],v[224:227],v[200:203],a[64:67]\n"
"v_subrev_u32 v103,s82,v103\n"
"v_mfma_f32_16x16x32_bf16 a[68:71],v[228:231],v[200:203],a[68:71]\n"
"buffer_load_dwordx4 v112,s[24:27],s76 offen offset:1024 sc1 lds\n"
"v_mfma_f32_16x16x32_bf16 a[68:71],v[164:167],v[136:139],a[68:71]\n"
"v_mfma_f32_16x16x32_bf16 a[72:75],v[168:171],v[136:139],a[72:75]\n"
"v_accvgpr_read_b32 v116,a0\n"
"v_accvgpr_read_b32 v117,a1\n"
"v_mfma_f32_16x16x32_bf16 a[72:75],v[232:235],v[200:203],a[72:75]\n"
"v_subrev_u32 v104,s82,v104\n"
"v_accvgpr_read_b32 v118,a2\n"
"v_mfma_f32_16x16x32_bf16 a[76:79],v[236:239],v[200:203],a[76:79]\n"
"buffer_load_dwordx4 v112,s[24:27],s76 offen offset:2048 sc1 lds\n"
"v_mfma_f32_16x16x32_bf16 a[76:79],v[172:175],v[136:139],a[76:79]\n"
"v_accvgpr_read_b32 v119,a3\n"
"v_accvgpr_read_b32 v120,a4\n"
"v_mfma_f32_16x16x32_bf16 a[80:83],v[176:179],v[136:139],a[80:83]\n"
"v_accvgpr_read_b32 v121,a5\n"
"v_accvgpr_read_b32 v122,a6\n"
"v_mfma_f32_16x16x32_bf16 a[80:83],v[240:243],v[200:203],a[80:83]\n"
"v_subrev_u32 v105,s82,v105\n"
"v_accvgpr_read_b32 v123,a7\n"
"v_mfma_f32_16x16x32_bf16 a[84:87],v[244:247],v[200:203],a[84:87]\n"
"buffer_load_dwordx4 v112,s[24:27],s76 offen offset:3072 sc1 lds\n"
"v_mfma_f32_16x16x32_bf16 a[84:87],v[180:183],v[136:139],a[84:87]\n"
"s_xor_b32 s76,s76,16384\n"
"v_cvt_pk_bf16_f32 v116,v116,v117\n"
"v_cvt_pk_bf16_f32 v117,v118,v119\n"
"v_mfma_f32_16x16x32_bf16 a[88:91],v[184:187],v[136:139],a[88:91]\n"
"v_cvt_pk_bf16_f32 v118,v120,v121\n"
"v_cvt_pk_bf16_f32 v119,v122,v123\n"
"v_mfma_f32_16x16x32_bf16 a[88:91],v[248:251],v[200:203],a[88:91]\n"
"buffer_store_dwordx4 v[116:119],v112,s[24:27],s56 offen offset:0 sc1\n"
"v_mfma_f32_16x16x32_bf16 a[92:95],v[252:255],v[200:203],a[92:95]\n"
"s_nop 0\n"
"v_accvgpr_read_b32 v116,a8\n"
"v_accvgpr_read_b32 v117,a9\n"
"v_accvgpr_read_b32 v118,a10\n"
"v_mfma_f32_16x16x32_bf16 a[92:95],v[188:191],v[136:139],a[92:95]\n"
"s_waitcnt vmcnt(20)\n"
"v_accvgpr_read_b32 v119,a11\n"
"v_accvgpr_read_b32 v120,a12\n"
"s_barrier\n"
"v_mfma_f32_16x16x32_bf16 a[96:99],v[160:163],v[140:143],a[96:99]\n"
"ds_read_b128 v[0:3],v97 offset:0\n"
"v_accvgpr_read_b32 v121,a13\n"
"v_mfma_f32_16x16x32_bf16 a[96:99],v[224:227],v[204:207],a[96:99]\n"
"ds_read_b128 v[8:11],v97 offset:1024\n"
"v_accvgpr_read_b32 v122,a14\n"
"v_mfma_f32_16x16x32_bf16 a[100:103],v[228:231],v[204:207],a[100:103]\n"
"ds_read_b128 v[16:19],v97 offset:2048\n"
"v_accvgpr_read_b32 v123,a15\n"
"v_mfma_f32_16x16x32_bf16 a[100:103],v[164:167],v[140:143],a[100:103]\n"
"ds_read_b128 v[24:27],v97 offset:3072\n"
"v_cvt_pk_bf16_f32 v116,v116,v117\n"
"v_mfma_f32_16x16x32_bf16 a[104:107],v[168:171],v[140:143],a[104:107]\n"
"ds_read_b128 v[4:7],v97 offset:16384\n"
"v_cvt_pk_bf16_f32 v117,v118,v119\n"
"v_mfma_f32_16x16x32_bf16 a[104:107],v[232:235],v[204:207],a[104:107]\n"
"ds_read_b128 v[12:15],v97 offset:17408\n"
"v_cvt_pk_bf16_f32 v118,v120,v121\n"
"v_mfma_f32_16x16x32_bf16 a[108:111],v[236:239],v[204:207],a[108:111]\n"
"ds_read_b128 v[20:23],v97 offset:18432\n"
"v_cvt_pk_bf16_f32 v119,v122,v123\n"
"v_mfma_f32_16x16x32_bf16 a[108:111],v[172:175],v[140:143],a[108:111]\n"
"ds_read_b128 v[28:31],v97 offset:19456\n"
"buffer_store_dwordx4 v[116:119],v112,s[24:27],s56 offen offset:1024 sc1\n"
"v_mfma_f32_16x16x32_bf16 a[112:115],v[176:179],v[140:143],a[112:115]\n"
"ds_read_b64_tr_b16 v[32:33],v98 offset:49152\n"
"v_accvgpr_read_b32 v116,a16\n"
"v_mfma_f32_16x16x32_bf16 a[112:115],v[240:243],v[204:207],a[112:115]\n"
"ds_read_b64_tr_b16 v[34:35],v98 offset:50176\n"
"v_accvgpr_read_b32 v117,a17\n"
"v_mfma_f32_16x16x32_bf16 a[116:119],v[244:247],v[204:207],a[116:119]\n"
"ds_read_b64_tr_b16 v[36:37],v98 offset:57344\n"
"v_accvgpr_read_b32 v118,a18\n"
"v_mfma_f32_16x16x32_bf16 a[116:119],v[180:183],v[140:143],a[116:119]\n"
"ds_read_b64_tr_b16 v[38:39],v98 offset:58368\n"
"v_accvgpr_read_b32 v119,a19\n"
"v_mfma_f32_16x16x32_bf16 a[120:123],v[184:187],v[140:143],a[120:123]\n"
"ds_read_b64_tr_b16 v[40:41],v99 offset:49152\n"
"v_accvgpr_read_b32 v120,a20\n"
"v_mfma_f32_16x16x32_bf16 a[120:123],v[248:251],v[204:207],a[120:123]\n"
"ds_read_b64_tr_b16 v[42:43],v99 offset:50176\n"
"v_accvgpr_read_b32 v121,a21\n"
"v_mfma_f32_16x16x32_bf16 a[124:127],v[252:255],v[204:207],a[124:127]\n"
"ds_read_b64_tr_b16 v[44:45],v99 offset:57344\n"
"v_accvgpr_read_b32 v122,a22\n"
"v_mfma_f32_16x16x32_bf16 a[124:127],v[188:191],v[140:143],a[124:127]\n"
"ds_read_b64_tr_b16 v[46:47],v99 offset:58368\n"
"v_accvgpr_read_b32 v123,a23\n"
"v_mfma_f32_16x16x32_bf16 a[128:131],v[160:163],v[144:147],a[128:131]\n"
"ds_read_b64_tr_b16 v[48:49],v100 offset:49152\n"
"v_cvt_pk_bf16_f32 v116,v116,v117\n"
"v_mfma_f32_16x16x32_bf16 a[128:131],v[224:227],v[208:211],a[128:131]\n"
"ds_read_b64_tr_b16 v[50:51],v100 offset:50176\n"
"v_cvt_pk_bf16_f32 v117,v118,v119\n"
"v_mfma_f32_16x16x32_bf16 a[132:135],v[228:231],v[208:211],a[132:135]\n"
"ds_read_b64_tr_b16 v[52:53],v100 offset:57344\n"
"v_cvt_pk_bf16_f32 v118,v120,v121\n"
"v_mfma_f32_16x16x32_bf16 a[132:135],v[164:167],v[144:147],a[132:135]\n"
"ds_read_b64_tr_b16 v[54:55],v100 offset:58368\n"
"v_cvt_pk_bf16_f32 v119,v122,v123\n"
"v_mfma_f32_16x16x32_bf16 a[136:139],v[168:171],v[144:147],a[136:139]\n"
"ds_read_b64_tr_b16 v[56:57],v101 offset:49152\n"
"buffer_store_dwordx4 v[116:119],v112,s[24:27],s56 offen offset:2048 sc1\n"
"v_mfma_f32_16x16x32_bf16 a[136:139],v[232:235],v[208:211],a[136:139]\n"
"ds_read_b64_tr_b16 v[58:59],v101 offset:50176\n"
"v_accvgpr_read_b32 v116,a24\n"
"v_mfma_f32_16x16x32_bf16 a[140:143],v[236:239],v[208:211],a[140:143]\n"
"ds_read_b64_tr_b16 v[60:61],v101 offset:57344\n"
"v_accvgpr_read_b32 v117,a25\n"
"v_mfma_f32_16x16x32_bf16 a[140:143],v[172:175],v[144:147],a[140:143]\n"
"ds_read_b64_tr_b16 v[62:63],v101 offset:58368\n"
"v_accvgpr_read_b32 v118,a26\n"
"v_mfma_f32_16x16x32_bf16 a[144:147],v[176:179],v[144:147],a[144:147]\n"
"ds_read_b64_tr_b16 v[64:65],v102 offset:49152\n"
"v_accvgpr_read_b32 v119,a27\n"
"v_mfma_f32_16x16x32_bf16 a[144:147],v[240:243],v[208:211],a[144:147]\n"
"ds_read_b64_tr_b16 v[66:67],v102 offset:50176\n"
"v_accvgpr_read_b32 v120,a28\n"
"v_mfma_f32_16x16x32_bf16 a[148:151],v[244:247],v[208:211],a[148:151]\n"
"ds_read_b64_tr_b16 v[68:69],v102 offset:57344\n"
"v_accvgpr_read_b32 v121,a29\n"
"v_mfma_f32_16x16x32_bf16 a[148:151],v[180:183],v[144:147],a[148:151]\n"
"ds_read_b64_tr_b16 v[70:71],v102 offset:58368\n"
"v_accvgpr_read_b32 v122,a30\n"
"v_mfma_f32_16x16x32_bf16 a[152:155],v[184:187],v[144:147],a[152:155]\n"
"ds_read_b64_tr_b16 v[72:73],v103 offset:49152\n"
"v_accvgpr_read_b32 v123,a31\n"
"v_mfma_f32_16x16x32_bf16 a[152:155],v[248:251],v[208:211],a[152:155]\n"
"ds_read_b64_tr_b16 v[74:75],v103 offset:50176\n"
"v_cvt_pk_bf16_f32 v116,v116,v117\n"
"v_mfma_f32_16x16x32_bf16 a[156:159],v[252:255],v[208:211],a[156:159]\n"
"ds_read_b64_tr_b16 v[76:77],v103 offset:57344\n"
"v_cvt_pk_bf16_f32 v117,v118,v119\n"
"v_mfma_f32_16x16x32_bf16 a[156:159],v[188:191],v[144:147],a[156:159]\n"
"ds_read_b64_tr_b16 v[78:79],v103 offset:58368\n"
"v_cvt_pk_bf16_f32 v118,v120,v121\n"
"v_mfma_f32_16x16x32_bf16 a[160:163],v[160:163],v[148:151],a[160:163]\n"
"ds_read_b64_tr_b16 v[80:81],v104 offset:49152\n"
"v_cvt_pk_bf16_f32 v119,v122,v123\n"
"v_mfma_f32_16x16x32_bf16 a[160:163],v[224:227],v[212:215],a[160:163]\n"
"ds_read_b64_tr_b16 v[82:83],v104 offset:50176\n"
"buffer_store_dwordx4 v[116:119],v112,s[24:27],s56 offen offset:3072 sc1\n"
"v_mfma_f32_16x16x32_bf16 a[164:167],v[228:231],v[212:215],a[164:167]\n"
"ds_read_b64_tr_b16 v[84:85],v104 offset:57344\n"
"v_mfma_f32_16x16x32_bf16 a[164:167],v[164:167],v[148:151],a[164:167]\n"
"ds_read_b64_tr_b16 v[86:87],v104 offset:58368\n"
"v_mfma_f32_16x16x32_bf16 a[168:171],v[168:171],v[148:151],a[168:171]\n"
"ds_read_b64_tr_b16 v[88:89],v105 offset:49152\n"
"v_mfma_f32_16x16x32_bf16 a[168:171],v[232:235],v[212:215],a[168:171]\n"
"ds_read_b64_tr_b16 v[90:91],v105 offset:50176\n"
"v_mfma_f32_16x16x32_bf16 a[172:175],v[236:239],v[212:215],a[172:175]\n"
"ds_read_b64_tr_b16 v[92:93],v105 offset:57344\n"
"v_mfma_f32_16x16x32_bf16 a[172:175],v[172:175],v[148:151],a[172:175]\n"
"ds_read_b64_tr_b16 v[94:95],v105 offset:58368\n"
"v_mfma_f32_16x16x32_bf16 a[176:179],v[176:179],v[148:151],a[176:179]\n"
"s_add_u32 m0,s85,106496\n"
"v_mfma_f32_16x16x32_bf16 a[176:179],v[240:243],v[212:215],a[176:179]\n"
"buffer_load_dwordx4 v110,s[28:31],s86 offen lds\n"
"v_mfma_f32_16x16x32_bf16 a[180:183],v[244:247],v[212:215],a[180:183]\n"
"v_mfma_f32_16x16x32_bf16 a[180:183],v[180:183],v[148:151],a[180:183]\n"
"s_add_u32 m0,s85,107520\n"
"v_mfma_f32_16x16x32_bf16 a[184:187],v[184:187],v[148:151],a[184:187]\n"
"buffer_load_dwordx4 v111,s[28:31],s86 offen lds\n"
"v_mfma_f32_16x16x32_bf16 a[184:187],v[248:251],v[212:215],a[184:187]\n"
"s_xor_b32 s86,s86,786432\n"
"v_mfma_f32_16x16x32_bf16 a[188:191],v[252:255],v[212:215],a[188:191]\n"
"s_add_u32 m0,s78,49152\n"
"v_mfma_f32_16x16x32_bf16 a[188:191],v[188:191],v[148:151],a[188:191]\n"
"buffer_load_dwordx4 v112,s[24:27],s76 offen offset:0 sc1 lds\n"
"v_mfma_f32_16x16x32_bf16 a[192:195],v[224:227],v[216:219],a[192:195]\n"
"v_mfma_f32_16x16x32_bf16 a[224:227],v[224:227],v[220:223],a[224:227]\n"
"v_mfma_f32_16x16x32_bf16 a[228:231],v[228:231],v[220:223],a[228:231]\n"
"buffer_load_dwordx4 v112,s[24:27],s76 offen offset:1024 sc1 lds\n"
"v_mfma_f32_16x16x32_bf16 a[196:199],v[228:231],v[216:219],a[196:199]\n"
"v_mfma_f32_16x16x32_bf16 a[200:203],v[232:235],v[216:219],a[200:203]\n"
"v_mfma_f32_16x16x32_bf16 a[232:235],v[232:235],v[220:223],a[232:235]\n"
"buffer_load_dwordx4 v112,s[24:27],s76 offen offset:2048 sc1 lds\n"
"v_mfma_f32_16x16x32_bf16 a[236:239],v[236:239],v[220:223],a[236:239]\n"
"v_mfma_f32_16x16x32_bf16 a[204:207],v[236:239],v[216:219],a[204:207]\n"
"buffer_load_dwordx4 v112,s[24:27],s76 offen offset:3072 sc1 lds\n"
"v_mfma_f32_16x16x32_bf16 a[208:211],v[240:243],v[216:219],a[208:211]\n"
"s_xor_b32 s76,s76,49152\n"
"v_mfma_f32_16x16x32_bf16 a[240:243],v[240:243],v[220:223],a[240:243]\n"
"v_mfma_f32_16x16x32_bf16 a[244:247],v[244:247],v[220:223],a[244:247]\n"
"v_mfma_f32_16x16x32_bf16 a[212:215],v[244:247],v[216:219],a[212:215]\n"
"v_mfma_f32_16x16x32_bf16 a[216:219],v[248:251],v[216:219],a[216:219]\n"
"v_mfma_f32_16x16x32_bf16 a[248:251],v[248:251],v[220:223],a[248:251]\n"
"v_mfma_f32_16x16x32_bf16 a[252:255],v[252:255],v[220:223],a[252:255]\n"
"v_mfma_f32_16x16x32_bf16 a[220:223],v[252:255],v[216:219],a[220:223]\n"
"s_branch .Lfk_ph2\n"
".Lfk_v:\n"
"s_waitcnt vmcnt(11)\n"
"s_barrier\n"
"ds_read_b128 v[152:155],v80 offset:12288\n"
"ds_read_b128 v[156:159],v80 offset:14336\n"
"ds_read_b64_tr_b16 v[160:161],v98 offset:49152\n"
"ds_read_b64_tr_b16 v[162:163],v98 offset:50176\n"
"ds_read_b64_tr_b16 v[164:165],v99 offset:49152\n"
"ds_read_b64_tr_b16 v[166:167],v99 offset:50176\n"
"ds_read_b64_tr_b16 v[168:169],v100 offset:49152\n"
"ds_read_b64_tr_b16 v[170:171],v100 offset:50176\n"
"ds_read_b64_tr_b16 v[172:173],v101 offset:49152\n"
"ds_read_b64_tr_b16 v[174:175],v101 offset:50176\n"
"ds_read_b64_tr_b16 v[176:177],v102 offset:49152\n"
"ds_read_b64_tr_b16 v[178:179],v102 offset:50176\n"
"ds_read_b64_tr_b16 v[180:181],v103 offset:49152\n"
"ds_read_b64_tr_b16 v[182:183],v103 offset:50176\n"
"ds_read_b64_tr_b16 v[184:185],v104 offset:49152\n"
"ds_read_b64_tr_b16 v[186:187],v104 offset:50176\n"
"ds_read_b64_tr_b16 v[188:189],v105 offset:49152\n"
"ds_read_b64_tr_b16 v[190:191],v105 offset:50176\n"
"s_waitcnt lgkmcnt(12)\n"
"v_mfma_f32_16x16x32_bf16 a[192:195],v[152:155],v[160:163],0\n"
"s_and_b32 s91,s89,7\n"
"s_mov_b32 s76,128\n"
"s_add_u32 m0,s79,32768\n"
"v_mfma_f32_16x16x32_bf16 a[196:199],v[156:159],v[160:163],0\n"
"buffer_load_dwordx4 v106,s[16:19],s76 offen offset:0 lds\n"
"v_mfma_f32_16x16x32_bf16 a[204:207],v[156:159],v[164:167],0\n"
"s_lshr_b32 s97,s89,3\n"
"s_lshl2_add_u32 s90,s88,s97\n"
"v_mfma_f32_16x16x32_bf16 a[200:203],v[152:155],v[164:167],0\n"
"buffer_load_dwordx4 v107,s[16:19],s76 offen offset:1024 lds\n"
"s_waitcnt lgkmcnt(8)\n"
"v_mfma_f32_16x16x32_bf16 a[208:211],v[152:155],v[168:171],0\n"
"s_lshl_b32 vcc_lo,s90,8\n"
"s_add_u32 s28,s28,vcc_lo\n"
"s_addc_u32 s29,s29,0\n"
"v_mfma_f32_16x16x32_bf16 a[212:215],v[156:159],v[168:171],0\n"
"buffer_load_dwordx4 v108,s[16:19],s76 offen offset:2048 lds\n"
"v_mfma_f32_16x16x32_bf16 a[220:223],v[156:159],v[172:175],0\n"
"s_sub_u32 s30,8388608,vcc_lo\n"
"s_mov_b32 s31,0x20000\n"
"buffer_load_dwordx4 v109,s[16:19],s76 offen offset:3072 lds\n"
"v_mfma_f32_16x16x32_bf16 a[216:219],v[152:155],v[172:175],0\n"
"s_waitcnt vmcnt(9)\n"
"s_barrier\n"
"s_waitcnt lgkmcnt(4)\n"
"v_mfma_f32_16x16x32_bf16 a[224:227],v[152:155],v[176:179],0\n"
"ds_read_b128 v[128:131],v80 offset:0\n"
"ds_read_b128 v[192:195],v81 offset:0\n"
"v_mfma_f32_16x16x32_bf16 a[228:231],v[156:159],v[176:179],0\n"
"ds_read_b64_tr_b16 v[224:225],v98 offset:32768\n"
"ds_read_b64_tr_b16 v[226:227],v98 offset:33792\n"
"v_mfma_f32_16x16x32_bf16 a[236:239],v[156:159],v[180:183],0\n"
"ds_read_b64_tr_b16 v[228:229],v99 offset:32768\n"
"ds_read_b64_tr_b16 v[230:231],v99 offset:33792\n"
"v_mfma_f32_16x16x32_bf16 a[232:235],v[152:155],v[180:183],0\n"
"ds_read_b64_tr_b16 v[232:233],v100 offset:32768\n"
"ds_read_b64_tr_b16 v[234:235],v100 offset:33792\n"
"s_waitcnt lgkmcnt(8)\n"
"v_mfma_f32_16x16x32_bf16 a[240:243],v[152:155],v[184:187],0\n"
"ds_read_b64_tr_b16 v[236:237],v101 offset:32768\n"
"ds_read_b64_tr_b16 v[238:239],v101 offset:33792\n"
"v_mfma_f32_16x16x32_bf16 a[244:247],v[156:159],v[184:187],0\n"
"ds_read_b64_tr_b16 v[240:241],v102 offset:32768\n"
"ds_read_b64_tr_b16 v[242:243],v102 offset:33792\n"
"v_mfma_f32_16x16x32_bf16 a[252:255],v[156:159],v[188:191],0\n"
"ds_read_b64_tr_b16 v[244:245],v103 offset:32768\n"
"ds_read_b64_tr_b16 v[246:247],v103 offset:33792\n"
"v_mfma_f32_16x16x32_bf16 a[248:251],v[152:155],v[188:191],0\n"
"ds_read_b64_tr_b16 v[248:249],v104 offset:32768\n"
"ds_read_b64_tr_b16 v[250:251],v104 offset:33792\n"
"s_waitcnt lgkmcnt(10)\n"
"v_mfma_f32_16x16x32_bf16 a[0:3],v[128:131],v[160:163],0\n"
"ds_read_b64_tr_b16 v[252:253],v105 offset:32768\n"
"ds_read_b64_tr_b16 v[254:255],v105 offset:33792\n"
"v_mfma_f32_16x16x32_bf16 a[0:3],v[192:195],v[224:227],a[0:3]\n"
"ds_read_b128 v[132:135],v80 offset:2048\n"
"ds_read_b128 v[196:199],v81 offset:2048\n"
"v_mfma_f32_16x16x32_bf16 a[8:11],v[192:195],v[228:231],0\n"
"ds_read_b128 v[216:219],v81 offset:12288\n"
"ds_read_b128 v[220:223],v81 offset:14336\n"
"v_mfma_f32_16x16x32_bf16 a[8:11],v[128:131],v[164:167],a[8:11]\n"
"s_waitcnt lgkmcnt(12)\n"
"s_mov_b32 s76,262272\n"
"s_add_u32 m0,s79,36864\n"
"v_mfma_f32_16x16x32_bf16 a[16:19],v[128:131],v[168:171],0\n"
"buffer_load_dwordx4 v106,s[16:19],s76 offen offset:0 lds\n"
"v_mfma_f32_16x16x32_bf16 a[16:19],v[192:195],v[232:235],a[16:19]\n"
"buffer_load_dwordx4 v107,s[16:19],s76 offen offset:1024 lds\n"
"v_mfma_f32_16x16x32_bf16 a[24:27],v[192:195],v[236:239],0\n"
"buffer_load_dwordx4 v108,s[16:19],s76 offen offset:2048 lds\n"
"buffer_load_dwordx4 v109,s[16:19],s76 offen offset:3072 lds\n"
"v_mfma_f32_16x16x32_bf16 a[24:27],v[128:131],v[172:175],a[24:27]\n"
"s_waitcnt lgkmcnt(8)\n"
"s_mov_b32 s77,524288\n"
"s_add_u32 m0,s78,98304\n"
"v_mfma_f32_16x16x32_bf16 a[32:35],v[128:131],v[176:179],0\n"
"buffer_load_dwordx4 v110,s[20:23],s77 offen offset:0 lds\n"
"v_mfma_f32_16x16x32_bf16 a[32:35],v[192:195],v[240:243],a[32:35]\n"
"buffer_load_dwordx4 v111,s[20:23],s77 offen offset:1024 lds\n"
"v_mfma_f32_16x16x32_bf16 a[40:43],v[192:195],v[244:247],0\n"
"buffer_load_dwordx4 v113,s[20:23],s77 offen offset:2048 lds\n"
"buffer_load_dwordx4 v114,s[20:23],s77 offen offset:3072 lds\n"
"v_mfma_f32_16x16x32_bf16 a[40:43],v[128:131],v[180:183],a[40:43]\n"
"s_waitcnt lgkmcnt(4)\n"
"s_mov_b32 s77,786432\n"
"s_add_u32 m0,s78,114688\n"
"v_mfma_f32_16x16x32_bf16 a[48:51],v[128:131],v[184:187],0\n"
"buffer_load_dwordx4 v110,s[20:23],s77 offen offset:0 lds\n"
"v_mfma_f32_16x16x32_bf16 a[48:51],v[192:195],v[248:251],a[48:51]\n"
"buffer_load_dwordx4 v111,s[20:23],s77 offen offset:1024 lds\n"
"v_mfma_f32_16x16x32_bf16 a[56:59],v[192:195],v[252:255],0\n"
"buffer_load_dwordx4 v113,s[20:23],s77 offen offset:2048 lds\n"
"buffer_load_dwordx4 v114,s[20:23],s77 offen offset:3072 lds\n"
"v_mfma_f32_16x16x32_bf16 a[56:59],v[128:131],v[188:191],a[56:59]\n"
"s_waitcnt lgkmcnt(2)\n"
"s_mov_b32 s76,256\n"
"s_add_u32 m0,s79,65536\n"
"v_mfma_f32_16x16x32_bf16 a[4:7],v[132:135],v[160:163],0\n"
"buffer_load_dwordx4 v106,s[16:19],s76 offen offset:0 lds\n"
"v_mfma_f32_16x16x32_bf16 a[4:7],v[196:199],v[224:227],a[4:7]\n"
"buffer_load_dwordx4 v107,s[16:19],s76 offen offset:1024 lds\n"
"v_mfma_f32_16x16x32_bf16 a[12:15],v[196:199],v[228:231],0\n"
"buffer_load_dwordx4 v108,s[16:19],s76 offen offset:2048 lds\n"
"buffer_load_dwordx4 v109,s[16:19],s76 offen offset:3072 lds\n"
"v_mfma_f32_16x16x32_bf16 a[12:15],v[132:135],v[164:167],a[12:15]\n"
"s_waitcnt vmcnt(21)\n"
"s_barrier\n"
"v_mfma_f32_16x16x32_bf16 a[20:23],v[132:135],v[168:171],0\n"
"ds_read_b128 v[136:139],v80 offset:4096\n"
"ds_read_b128 v[200:203],v81 offset:4096\n"
"v_mfma_f32_16x16x32_bf16 a[20:23],v[196:199],v[232:235],a[20:23]\n"
"ds_read_b128 v[140:143],v80 offset:6144\n"
"ds_read_b128 v[204:207],v81 offset:6144\n"
"v_mfma_f32_16x16x32_bf16 a[28:31],v[196:199],v[236:239],0\n"
"ds_read_b128 v[144:147],v80 offset:8192\n"
"ds_read_b128 v[208:211],v81 offset:8192\n"
"v_mfma_f32_16x16x32_bf16 a[28:31],v[132:135],v[172:175],a[28:31]\n"
"ds_read_b128 v[148:151],v80 offset:10240\n"
"ds_read_b128 v[212:215],v81 offset:10240\n"
"v_mfma_f32_16x16x32_bf16 a[36:39],v[132:135],v[176:179],0\n"
"s_mov_b32 s60,s91\n"
"s_mov_b32 s76,262400\n"
"s_add_u32 m0,s79,69632\n"
"v_mfma_f32_16x16x32_bf16 a[36:39],v[196:199],v[240:243],a[36:39]\n"
"buffer_load_dwordx4 v106,s[16:19],s76 offen offset:0 lds\n"
"v_mfma_f32_16x16x32_bf16 a[44:47],v[196:199],v[244:247],0\n"
"buffer_load_dwordx4 v107,s[16:19],s76 offen offset:1024 lds\n"
"v_mfma_f32_16x16x32_bf16 a[44:47],v[132:135],v[180:183],a[44:47]\n"
"buffer_load_dwordx4 v108,s[16:19],s76 offen offset:2048 lds\n"
"v_mfma_f32_16x16x32_bf16 a[52:55],v[132:135],v[184:187],0\n"
"buffer_load_dwordx4 v109,s[16:19],s76 offen offset:3072 lds\n"
"v_mfma_f32_16x16x32_bf16 a[52:55],v[196:199],v[248:251],a[52:55]\n"
"s_sub_u32 s94,15,s91\n"
"s_waitcnt lgkmcnt(0)\n"
"s_barrier\n"
"v_mfma_f32_16x16x32_bf16 a[60:63],v[196:199],v[252:255],0\n"
"s_cmp_lt_u32 s52,2\n"
"s_cselect_b32 s95,s94,s91\n"
"s_mov_b32 s77,1048576\n"
"s_add_u32 m0,s78,147456\n"
"v_mfma_f32_16x16x32_bf16 a[60:63],v[132:135],v[188:191],a[60:63]\n"
"buffer_load_dwordx4 v110,s[20:23],s77 offen offset:0 lds\n"
"v_mfma_f32_16x16x32_bf16 a[64:67],v[136:139],v[160:163],0\n"
"s_lshr_b32 vcc_lo,s95,1\n"
"s_lshl_b32 s55,vcc_lo,19\n"
"v_mfma_f32_16x16x32_bf16 a[64:67],v[200:203],v[224:227],a[64:67]\n"
"s_lshl_b32 s58,vcc_lo,10\n"
"s_add_u32 s58,s58,148898816\n"
"v_mfma_f32_16x16x32_bf16 a[72:75],v[200:203],v[228:231],0\n"
"s_and_b32 vcc_lo,s95,1\n"
"s_lshl_b32 vcc_lo,vcc_lo,13\n"
"v_mfma_f32_16x16x32_bf16 a[72:75],v[136:139],v[164:167],a[72:75]\n"
"buffer_load_dwordx4 v111,s[20:23],s77 offen offset:1024 lds\n"
"v_mfma_f32_16x16x32_bf16 a[80:83],v[136:139],v[168:171],0\n"
"s_and_b32 vcc_hi,s78,4096\n"
"s_add_u32 s55,s55,vcc_lo\n"
"v_mfma_f32_16x16x32_bf16 a[80:83],v[200:203],v[232:235],a[80:83]\n"
"s_add_u32 s55,s55,vcc_hi\n"
"s_and_b32 vcc_lo,s89,2\n"
"v_mfma_f32_16x16x32_bf16 a[88:91],v[200:203],v[236:239],0\n"
"s_lshl2_add_u32 vcc_lo,s88,vcc_lo\n"
"s_and_b32 vcc_hi,s52,1\n"
"v_mfma_f32_16x16x32_bf16 a[88:91],v[136:139],v[172:175],a[88:91]\n"
"buffer_load_dwordx4 v113,s[20:23],s77 offen offset:2048 lds\n"
"v_mfma_f32_16x16x32_bf16 a[96:99],v[136:139],v[176:179],0\n"
"s_add_u32 vcc_lo,vcc_lo,vcc_hi\n"
"s_lshr_b32 vcc_hi,s89,2\n"
"v_mfma_f32_16x16x32_bf16 a[96:99],v[200:203],v[240:243],a[96:99]\n"
"s_lshr_b32 s84,s52,1\n"
"s_lshl1_add_u32 vcc_hi,vcc_hi,s84\n"
"v_mfma_f32_16x16x32_bf16 a[104:107],v[200:203],v[244:247],0\n"
"s_mul_i32 s56,vcc_lo,1179648\n"
"s_lshl_b32 s84,s84,19\n"
"v_mfma_f32_16x16x32_bf16 a[104:107],v[136:139],v[180:183],a[104:107]\n"
"buffer_load_dwordx4 v114,s[20:23],s77 offen offset:3072 lds\n"
"v_mfma_f32_16x16x32_bf16 a[112:115],v[136:139],v[184:187],0\n"
"s_add_u32 s56,s56,s84\n"
"s_lshr_b32 s84,s89,2\n"
"v_mfma_f32_16x16x32_bf16 a[112:115],v[200:203],v[248:251],a[112:115]\n"
"s_lshl_b32 s84,s84,14\n"
"s_add_u32 s56,s56,s84\n"
"v_mfma_f32_16x16x32_bf16 a[120:123],v[200:203],v[252:255],0\n"
"s_lshl_b32 s84,s53,13\n"
"s_mov_b32 s77,1310720\n"
"s_add_u32 m0,s78,131072\n"
"v_mfma_f32_16x16x32_bf16 a[120:123],v[136:139],v[188:191],a[120:123]\n"
"buffer_load_dwordx4 v110,s[20:23],s77 offen offset:0 lds\n"
"v_mfma_f32_16x16x32_bf16 a[68:71],v[140:143],v[160:163],0\n"
"s_add_u32 s56,s56,s84\n"
"s_add_u32 s56,s56,4194304\n"
"v_mfma_f32_16x16x32_bf16 a[68:71],v[204:207],v[224:227],a[68:71]\n"
"s_lshl4_add_u32 vcc_hi,s53,vcc_hi\n"
"s_lshl_b32 vcc_lo,vcc_lo,8\n"
"v_mfma_f32_16x16x32_bf16 a[76:79],v[204:207],v[228:231],0\n"
"buffer_load_dwordx4 v111,s[20:23],s77 offen offset:1024 lds\n"
"v_mfma_f32_16x16x32_bf16 a[76:79],v[140:143],v[164:167],a[76:79]\n"
"s_lshl3_add_u32 s59,vcc_hi,vcc_lo\n"
"s_add_u32 s59,s59,148914176\n"
"v_mfma_f32_16x16x32_bf16 a[84:87],v[140:143],v[168:171],0\n"
"s_lshl_b32 s57,s92,3\n"
"s_add_u32 s57,s57,148898816\n"
"v_mfma_f32_16x16x32_bf16 a[84:87],v[204:207],v[232:235],a[84:87]\n"
"s_lshl_b32 s63,s90,8\n"
"s_add_u32 s63,s63,148914176\n"
"v_mfma_f32_16x16x32_bf16 a[92:95],v[204:207],v[236:239],0\n"
"buffer_load_dwordx4 v113,s[20:23],s77 offen offset:2048 lds\n"
"v_mfma_f32_16x16x32_bf16 a[92:95],v[140:143],v[172:175],a[92:95]\n"
"s_lshl_b32 s74,s92,11\n"
"s_add_u32 s74,s74,171966464\n"
"v_mfma_f32_16x16x32_bf16 a[100:103],v[140:143],v[176:179],0\n"
"s_mul_i32 s96,s90,1179648\n"
"s_add_u32 s96,s96,4194304\n"
"v_mfma_f32_16x16x32_bf16 a[100:103],v[204:207],v[240:243],a[100:103]\n"
"s_add_u32 s73,s96,s85\n"
"s_and_b32 vcc_hi,s78,4096\n"
"v_mfma_f32_16x16x32_bf16 a[108:111],v[204:207],v[244:247],0\n"
"buffer_load_dwordx4 v114,s[20:23],s77 offen offset:3072 lds\n"
"v_mfma_f32_16x16x32_bf16 a[108:111],v[140:143],v[180:183],a[108:111]\n"
"s_lshl_b32 vcc_hi,vcc_hi,1\n"
"s_add_u32 vcc_lo,s96,vcc_hi\n"
"v_mfma_f32_16x16x32_bf16 a[116:119],v[140:143],v[184:187],0\n"
"s_add_u32 s65,vcc_lo,524288\n"
"s_lshl_b32 s66,s91,16\n"
"v_mfma_f32_16x16x32_bf16 a[116:119],v[204:207],v[248:251],a[116:119]\n"
"s_sub_u32 s66,vcc_lo,s66\n"
"s_mov_b32 s76,384\n"
"s_add_u32 m0,s79,0\n"
"v_mfma_f32_16x16x32_bf16 a[124:127],v[204:207],v[252:255],0\n"
"buffer_load_dwordx4 v106,s[16:19],s76 offen offset:0 lds\n"
"v_mfma_f32_16x16x32_bf16 a[124:127],v[140:143],v[188:191],a[124:127]\n"
"s_add_u32 s66,s66,999424\n"
"s_add_u32 s67,vcc_hi,65536\n"
"v_mfma_f32_16x16x32_bf16 a[128:131],v[144:147],v[160:163],0\n"
"s_mul_i32 vcc_lo,vcc_hi,5\n"
"s_sub_u32 vcc_lo,s96,vcc_lo\n"
"v_mfma_f32_16x16x32_bf16 a[128:131],v[208:211],v[224:227],a[128:131]\n"
"s_add_u32 s64,vcc_lo,81920\n"
"s_add_u32 s62,vcc_lo,135168\n"
"s_add_u32 s61,vcc_lo,524288\n"
"v_mfma_f32_16x16x32_bf16 a[136:139],v[208:211],v[228:231],0\n"
"buffer_load_dwordx4 v107,s[16:19],s76 offen offset:1024 lds\n"
"v_mfma_f32_16x16x32_bf16 a[136:139],v[144:147],v[164:167],a[136:139]\n"
"s_cmp_lt_u32 s52,2\n"
"s_cselect_b32 s64,s64,2146435072\n"
"s_cselect_b32 s67,s67,163840\n"
"s_cselect_b32 s65,s65,1\n"
"v_mfma_f32_16x16x32_bf16 a[144:147],v[144:147],v[168:171],0\n"
"s_cselect_b32 vcc_lo,4,0\n"
"s_and_b32 vcc_hi,s52,1\n"
"s_add_u32 vcc_lo,vcc_lo,vcc_hi\n"
"v_mfma_f32_16x16x32_bf16 a[144:147],v[208:211],v[232:235],a[144:147]\n"
"s_lshl_b32 vcc_hi,s90,12\n"
"s_add_u32 vcc_hi,vcc_hi,54525952\n"
"v_mfma_f32_16x16x32_bf16 a[152:155],v[208:211],v[236:239],0\n"
"buffer_load_dwordx4 v108,s[16:19],s76 offen offset:2048 lds\n"
"v_mfma_f32_16x16x32_bf16 a[152:155],v[144:147],v[172:175],a[152:155]\n"
"s_lshl3_add_u32 s70,s95,vcc_lo\n"
"s_lshl_b32 s70,s70,17\n"
"v_mfma_f32_16x16x32_bf16 a[160:163],v[144:147],v[176:179],0\n"
"s_add_u32 s70,s70,vcc_hi\n"
"s_lshl3_add_u32 s68,s94,vcc_lo\n"
"v_mfma_f32_16x16x32_bf16 a[160:163],v[208:211],v[240:243],a[160:163]\n"
"s_lshl_b32 s68,s68,17\n"
"s_add_u32 s68,s68,vcc_hi\n"
"v_mfma_f32_16x16x32_bf16 a[168:171],v[208:211],v[244:247],0\n"
"buffer_load_dwordx4 v109,s[16:19],s76 offen offset:3072 lds\n"
"v_mfma_f32_16x16x32_bf16 a[168:171],v[144:147],v[180:183],a[168:171]\n"
"s_lshr_b32 vcc_lo,s90,3\n"
"s_lshl2_add_u32 vcc_lo,vcc_lo,s52\n"
"v_mfma_f32_16x16x32_bf16 a[176:179],v[144:147],v[184:187],0\n"
"s_lshl_b32 vcc_lo,vcc_lo,7\n"
"s_and_b32 vcc_hi,s90,7\n"
"v_mfma_f32_16x16x32_bf16 a[176:179],v[208:211],v[248:251],a[176:179]\n"
"s_add_u32 vcc_lo,vcc_lo,vcc_hi\n"
"s_mov_b32 s76,262528\n"
"s_add_u32 m0,s79,4096\n"
"v_mfma_f32_16x16x32_bf16 a[184:187],v[208:211],v[252:255],0\n"
"buffer_load_dwordx4 v106,s[16:19],s76 offen offset:0 lds\n"
"v_mfma_f32_16x16x32_bf16 a[184:187],v[144:147],v[188:191],a[184:187]\n"
"s_lshl3_add_u32 s69,s91,vcc_lo\n"
"s_lshl_b32 s69,s69,3\n"
"v_mfma_f32_16x16x32_bf16 a[132:135],v[148:151],v[160:163],0\n"
"s_add_u32 s69,s69,148930560\n"
"s_lshl3_add_u32 s71,s94,vcc_lo\n"
"v_mfma_f32_16x16x32_bf16 a[132:135],v[212:215],v[224:227],a[132:135]\n"
"s_lshl_b32 s71,s71,3\n"
"s_add_u32 s71,s71,148930560\n"
"v_mfma_f32_16x16x32_bf16 a[140:143],v[212:215],v[228:231],0\n"
"buffer_load_dwordx4 v107,s[16:19],s76 offen offset:1024 lds\n"
"v_mfma_f32_16x16x32_bf16 a[140:143],v[148:151],v[164:167],a[140:143]\n"
"s_mov_b32 s6,8388608\n"
"s_mov_b32 s7,0x20000\n"
"v_mfma_f32_16x16x32_bf16 a[148:151],v[148:151],v[168:171],0\n"
"s_lshr_b32 s94,s88,1\n"
"s_and_b32 vcc_lo,s91,1\n"
"v_mfma_f32_16x16x32_bf16 a[148:151],v[212:215],v[232:235],a[148:151]\n"
"s_lshl1_add_u32 s95,s97,vcc_lo\n"
"s_and_b32 vcc_lo,s88,1\n"
"v_mfma_f32_16x16x32_bf16 a[156:159],v[212:215],v[236:239],0\n"
"buffer_load_dwordx4 v108,s[16:19],s76 offen offset:2048 lds\n"
"v_mfma_f32_16x16x32_bf16 a[156:159],v[148:151],v[172:175],a[156:159]\n"
"s_lshr_b32 vcc_hi,s91,1\n"
"s_lshl2_add_u32 s96,vcc_lo,vcc_hi\n"
"v_mfma_f32_16x16x32_bf16 a[164:167],v[148:151],v[176:179],0\n"
"s_lshr_b32 vcc_lo,s88,2\n"
"s_lshl2_add_u32 s96,vcc_lo,vcc_hi\n"
"v_mfma_f32_16x16x32_bf16 a[164:167],v[212:215],v[240:243],a[164:167]\n"
"s_sub_u32 vcc_lo,7,s88\n"
"s_min_u32 s94,vcc_lo,s88\n"
"v_mfma_f32_16x16x32_bf16 a[172:175],v[212:215],v[244:247],0\n"
"buffer_load_dwordx4 v109,s[16:19],s76 offen offset:3072 lds\n"
"v_mfma_f32_16x16x32_bf16 a[172:175],v[148:151],v[180:183],a[172:175]\n"
"s_and_b32 s96,s88,4\n"
"s_add_u32 s96,s96,s97\n"
"s_add_u32 s95,s91,s96\n"
"v_mfma_f32_16x16x32_bf16 a[180:183],v[148:151],v[184:187],0\n"
"buffer_load_dwordx4 v[8:11],v70,s[32:35],0 offen offset:0\n"
"v_mfma_f32_16x16x32_bf16 a[180:183],v[212:215],v[248:251],a[180:183]\n"
"buffer_load_dwordx4 v[12:15],v70,s[32:35],0 offen offset:512\n"
"s_and_b32 s95,s95,7\n"
"v_mfma_f32_16x16x32_bf16 a[188:191],v[212:215],v[252:255],0\n"
"buffer_load_dwordx4 v[16:19],v70,s[32:35],0 offen offset:1024\n"
"buffer_load_dwordx4 v[20:23],v70,s[32:35],0 offen offset:1536\n"
"s_lshl3_add_u32 vcc_lo,s94,s96\n"
"v_mfma_f32_16x16x32_bf16 a[188:191],v[148:151],v[188:191],a[188:191]\n"
"s_waitcnt vmcnt(28)\n"
"s_barrier\n"
"v_mfma_f32_16x16x32_bf16 a[192:195],v[216:219],v[224:227],a[192:195]\n"
"ds_read_b128 v[152:155],v80 offset:45056\n"
"s_mov_b32 s87,524288\n"
"v_mfma_f32_16x16x32_bf16 a[196:199],v[220:223],v[224:227],a[196:199]\n"
"ds_read_b128 v[156:159],v80 offset:47104\n"
"buffer_load_dword v115,v112,s[36:39],s87 offen\n"
"v_mfma_f32_16x16x32_bf16 a[204:207],v[220:223],v[228:231],a[204:207]\n"
"ds_read_b64_tr_b16 v[160:161],v98 offset:0\n"
"s_mov_b32 s87,1048576\n"
"v_mfma_f32_16x16x32_bf16 a[200:203],v[216:219],v[228:231],a[200:203]\n"
"ds_read_b64_tr_b16 v[162:163],v98 offset:1024\n"
"buffer_load_dword v115,v112,s[36:39],s87 offen\n"
"v_mfma_f32_16x16x32_bf16 a[208:211],v[216:219],v[232:235],a[208:211]\n"
"ds_read_b64_tr_b16 v[164:165],v99 offset:0\n"
"s_mov_b32 s87,1572864\n"
"v_mfma_f32_16x16x32_bf16 a[212:215],v[220:223],v[232:235],a[212:215]\n"
"ds_read_b64_tr_b16 v[166:167],v99 offset:1024\n"
"buffer_load_dword v115,v112,s[36:39],s87 offen\n"
"v_mfma_f32_16x16x32_bf16 a[220:223],v[220:223],v[236:239],a[220:223]\n"
"ds_read_b64_tr_b16 v[168:169],v100 offset:0\n"
"s_lshl3_add_u32 vcc_lo,vcc_lo,s52\n"
"v_mfma_f32_16x16x32_bf16 a[216:219],v[216:219],v[236:239],a[216:219]\n"
"ds_read_b64_tr_b16 v[170:171],v100 offset:1024\n"
"s_lshl_b32 s13,vcc_lo,15\n"
"v_mfma_f32_16x16x32_bf16 a[224:227],v[216:219],v[240:243],a[224:227]\n"
"ds_read_b64_tr_b16 v[172:173],v101 offset:0\n"
"s_lshl_b32 vcc_lo,s95,8\n"
"v_mfma_f32_16x16x32_bf16 a[228:231],v[220:223],v[240:243],a[228:231]\n"
"ds_read_b64_tr_b16 v[174:175],v101 offset:1024\n"
"s_add_u32 s13,s13,vcc_lo\n"
"v_mfma_f32_16x16x32_bf16 a[236:239],v[220:223],v[244:247],a[236:239]\n"
"ds_read_b64_tr_b16 v[176:177],v102 offset:0\n"
"s_lshl3_add_u32 vcc_hi,s94,s95\n"
"v_mfma_f32_16x16x32_bf16 a[232:235],v[216:219],v[244:247],a[232:235]\n"
"ds_read_b64_tr_b16 v[178:179],v102 offset:1024\n"
"s_lshl_b32 s72,vcc_hi,8\n"
"v_mfma_f32_16x16x32_bf16 a[240:243],v[216:219],v[248:251],a[240:243]\n"
"ds_read_b64_tr_b16 v[180:181],v103 offset:0\n"
"s_add_u32 s72,s72,149061632\n"
"v_mfma_f32_16x16x32_bf16 a[244:247],v[220:223],v[248:251],a[244:247]\n"
"s_or_b32 vcc_lo,s96,s52\n"
"ds_read_b64_tr_b16 v[182:183],v103 offset:1024\n"
"s_cselect_b32 s72,2146435072,s72\n"
"v_mfma_f32_16x16x32_bf16 a[252:255],v[220:223],v[252:255],a[252:255]\n"
"ds_read_b64_tr_b16 v[184:185],v104 offset:0\n"
"s_lshl_b32 s3,vcc_hi,18\n"
"v_mfma_f32_16x16x32_bf16 a[248:251],v[216:219],v[252:255],a[248:251]\n"
"s_lshl_b32 vcc_lo,s96,15\n"
"ds_read_b64_tr_b16 v[186:187],v104 offset:1024\n"
"s_waitcnt lgkmcnt(10)\n"
"v_mfma_f32_16x16x32_bf16 a[192:195],v[152:155],v[160:163],a[192:195]\n"
"ds_read_b64_tr_b16 v[188:189],v105 offset:0\n"
"s_sub_u32 s3,s3,vcc_lo\n"
"v_mfma_f32_16x16x32_bf16 a[196:199],v[156:159],v[160:163],a[196:199]\n"
"ds_read_b64_tr_b16 v[190:191],v105 offset:1024\n"
"s_add_u32 s3,s3,s78\n"
"v_mfma_f32_16x16x32_bf16 a[204:207],v[156:159],v[164:167],a[204:207]\n"
"ds_read_b128 v[128:131],v80 offset:32768\n"
"s_add_u32 s3,s3,100909056\n"
"v_mfma_f32_16x16x32_bf16 a[200:203],v[152:155],v[164:167],a[200:203]\n"
"ds_read_b128 v[192:195],v81 offset:32768\n"
"s_waitcnt lgkmcnt(10)\n"
"v_mfma_f32_16x16x32_bf16 a[208:211],v[152:155],v[168:171],a[208:211]\n"
"ds_read_b64_tr_b16 v[224:225],v98 offset:16384\n"
"s_lshl3_add_u32 vcc_hi,vcc_hi,s96\n"
"v_mfma_f32_16x16x32_bf16 a[212:215],v[156:159],v[168:171],a[212:215]\n"
"ds_read_b64_tr_b16 v[226:227],v98 offset:17408\n"
"s_lshl2_add_u32 vcc_hi,vcc_hi,s52\n"
"v_mfma_f32_16x16x32_bf16 a[220:223],v[156:159],v[172:175],a[220:223]\n"
"ds_read_b64_tr_b16 v[228:229],v99 offset:16384\n"
"s_lshl_b32 s2,vcc_hi,4\n"
"v_mfma_f32_16x16x32_bf16 a[216:219],v[152:155],v[172:175],a[216:219]\n"
"ds_read_b64_tr_b16 v[230:231],v99 offset:17408\n"
"s_waitcnt lgkmcnt(10)\n"
"v_mfma_f32_16x16x32_bf16 a[224:227],v[152:155],v[176:179],a[224:227]\n"
"ds_read_b64_tr_b16 v[232:233],v100 offset:16384\n"
"s_add_u32 s2,s2,149028864\n"
"v_mfma_f32_16x16x32_bf16 a[228:231],v[156:159],v[176:179],a[228:231]\n"
"ds_read_b64_tr_b16 v[234:235],v100 offset:17408\n"
"s_lshl2_add_u32 vcc_lo,s95,s52\n"
"v_mfma_f32_16x16x32_bf16 a[236:239],v[156:159],v[180:183],a[236:239]\n"
"ds_read_b64_tr_b16 v[236:237],v101 offset:16384\n"
"s_lshl_b32 vcc_lo,vcc_lo,10\n"
"v_mfma_f32_16x16x32_bf16 a[232:235],v[152:155],v[180:183],a[232:235]\n"
"ds_read_b64_tr_b16 v[238:239],v101 offset:17408\n"
"s_waitcnt lgkmcnt(10)\n"
"v_mfma_f32_16x16x32_bf16 a[240:243],v[152:155],v[184:187],a[240:243]\n"
"ds_read_b64_tr_b16 v[240:241],v102 offset:16384\n"
"s_lshl_b32 vcc_hi,s94,22\n"
"v_mfma_f32_16x16x32_bf16 a[244:247],v[156:159],v[184:187],a[244:247]\n"
"ds_read_b64_tr_b16 v[242:243],v102 offset:17408\n"
"s_add_u32 s12,vcc_lo,vcc_hi\n"
"v_mfma_f32_16x16x32_bf16 a[252:255],v[156:159],v[188:191],a[252:255]\n"
"ds_read_b64_tr_b16 v[244:245],v103 offset:16384\n"
"s_lshl_b32 vcc_lo,s96,19\n"
"v_mfma_f32_16x16x32_bf16 a[248:251],v[152:155],v[188:191],a[248:251]\n"
"ds_read_b64_tr_b16 v[246:247],v103 offset:17408\n"
"s_waitcnt lgkmcnt(8)\n"
"v_mfma_f32_16x16x32_bf16 a[0:3],v[128:131],v[160:163],a[0:3]\n"
"ds_read_b64_tr_b16 v[248:249],v104 offset:16384\n"
"s_add_u32 s10,s12,vcc_lo\n"
"v_mfma_f32_16x16x32_bf16 a[0:3],v[192:195],v[224:227],a[0:3]\n"
"ds_read_b64_tr_b16 v[250:251],v104 offset:17408\n"
"s_add_u32 s10,s10,71303168\n"
"v_mfma_f32_16x16x32_bf16 a[8:11],v[192:195],v[228:231],a[8:11]\n"
"ds_read_b64_tr_b16 v[252:253],v105 offset:16384\n"
"s_mul_i32 vcc_lo,s52,31744\n"
"v_mfma_f32_16x16x32_bf16 a[8:11],v[128:131],v[164:167],a[8:11]\n"
"ds_read_b64_tr_b16 v[254:255],v105 offset:17408\n"
"s_waitcnt lgkmcnt(8)\n"
"v_mfma_f32_16x16x32_bf16 a[16:19],v[128:131],v[168:171],a[16:19]\n"
"ds_read_b128 v[132:135],v80 offset:34816\n"
"s_add_u32 s10,s10,vcc_lo\n"
"v_mfma_f32_16x16x32_bf16 a[16:19],v[192:195],v[232:235],a[16:19]\n"
"ds_read_b128 v[196:199],v81 offset:34816\n"
"s_lshl_b32 vcc_lo,s52,10\n"
"v_mfma_f32_16x16x32_bf16 a[24:27],v[192:195],v[236:239],a[24:27]\n"
"ds_read_b128 v[136:139],v80 offset:36864\n"
"s_sub_u32 s12,57671680,vcc_lo\n"
"v_mfma_f32_16x16x32_bf16 a[24:27],v[128:131],v[172:175],a[24:27]\n"
"ds_read_b128 v[200:203],v81 offset:36864\n"
"s_waitcnt lgkmcnt(8)\n"
"v_mfma_f32_16x16x32_bf16 a[32:35],v[128:131],v[176:179],a[32:35]\n"
"ds_read_b128 v[140:143],v80 offset:38912\n"
"s_add_u32 s8,s79,54525952\n"
"v_mfma_f32_16x16x32_bf16 a[32:35],v[192:195],v[240:243],a[32:35]\n"
"ds_read_b128 v[204:207],v81 offset:38912\n"
"s_and_b32 vcc_lo,s96,1\n"
"v_mfma_f32_16x16x32_bf16 a[40:43],v[192:195],v[244:247],a[40:43]\n"
"ds_read_b128 v[144:147],v80 offset:40960\n"
"s_lshl_b32 s11,vcc_lo,10\n"
"v_mfma_f32_16x16x32_bf16 a[40:43],v[128:131],v[180:183],a[40:43]\n"
"ds_read_b128 v[208:211],v81 offset:40960\n"
"s_waitcnt lgkmcnt(8)\n"
"v_mfma_f32_16x16x32_bf16 a[48:51],v[128:131],v[184:187],a[48:51]\n"
"ds_read_b128 v[148:151],v80 offset:43008\n"
"s_add_u32 s11,s11,148932608\n"
"v_mfma_f32_16x16x32_bf16 a[48:51],v[192:195],v[248:251],a[48:51]\n"
"ds_read_b128 v[212:215],v81 offset:43008\n"
"s_mov_b32 s9,148931072\n"
"v_mfma_f32_16x16x32_bf16 a[56:59],v[192:195],v[252:255],a[56:59]\n"
"s_cmp_eq_u32 s52,0\n"
"ds_read_b128 v[216:219],v81 offset:45056\n"
"s_cselect_b32 s71,s71,2146435072\n"
"v_mfma_f32_16x16x32_bf16 a[56:59],v[128:131],v[188:191],a[56:59]\n"
"ds_read_b128 v[220:223],v81 offset:47104\n"
"s_waitcnt lgkmcnt(10)\n"
"v_mfma_f32_16x16x32_bf16 a[4:7],v[132:135],v[160:163],a[4:7]\n"
"v_mfma_f32_16x16x32_bf16 a[4:7],v[196:199],v[224:227],a[4:7]\n"
"v_mfma_f32_16x16x32_bf16 a[12:15],v[196:199],v[228:231],a[12:15]\n"
"v_mfma_f32_16x16x32_bf16 a[12:15],v[132:135],v[164:167],a[12:15]\n"
"v_mfma_f32_16x16x32_bf16 a[20:23],v[132:135],v[168:171],a[20:23]\n"
"s_waitcnt lgkmcnt(0)\n"
"s_barrier\n"
"v_mfma_f32_16x16x32_bf16 a[20:23],v[196:199],v[232:235],a[20:23]\n"
"s_mov_b32 s77,1572864\n"
"s_add_u32 m0,s78,98304\n"
"v_mfma_f32_16x16x32_bf16 a[28:31],v[196:199],v[236:239],a[28:31]\n"
"buffer_load_dwordx4 v110,s[20:23],s77 offen offset:0 lds\n"
"v_mfma_f32_16x16x32_bf16 a[28:31],v[132:135],v[172:175],a[28:31]\n"
"v_mfma_f32_16x16x32_bf16 a[36:39],v[132:135],v[176:179],a[36:39]\n"
"v_mfma_f32_16x16x32_bf16 a[36:39],v[196:199],v[240:243],a[36:39]\n"
"v_mfma_f32_16x16x32_bf16 a[44:47],v[196:199],v[244:247],a[44:47]\n"
"buffer_load_dwordx4 v111,s[20:23],s77 offen offset:1024 lds\n"
"v_mfma_f32_16x16x32_bf16 a[44:47],v[132:135],v[180:183],a[44:47]\n"
"v_mfma_f32_16x16x32_bf16 a[52:55],v[132:135],v[184:187],a[52:55]\n"
"v_mfma_f32_16x16x32_bf16 a[52:55],v[196:199],v[248:251],a[52:55]\n"
"v_mfma_f32_16x16x32_bf16 a[60:63],v[196:199],v[252:255],a[60:63]\n"
"buffer_load_dwordx4 v113,s[20:23],s77 offen offset:2048 lds\n"
"v_mfma_f32_16x16x32_bf16 a[60:63],v[132:135],v[188:191],a[60:63]\n"
"v_mfma_f32_16x16x32_bf16 a[64:67],v[136:139],v[160:163],a[64:67]\n"
"v_mfma_f32_16x16x32_bf16 a[64:67],v[200:203],v[224:227],a[64:67]\n"
"v_mfma_f32_16x16x32_bf16 a[72:75],v[200:203],v[228:231],a[72:75]\n"
"buffer_load_dwordx4 v114,s[20:23],s77 offen offset:3072 lds\n"
"v_mfma_f32_16x16x32_bf16 a[72:75],v[136:139],v[164:167],a[72:75]\n"
"v_mfma_f32_16x16x32_bf16 a[80:83],v[136:139],v[168:171],a[80:83]\n"
"v_mfma_f32_16x16x32_bf16 a[80:83],v[200:203],v[232:235],a[80:83]\n"
"s_mov_b32 s77,1835008\n"
"s_add_u32 m0,s78,114688\n"
"v_mfma_f32_16x16x32_bf16 a[88:91],v[200:203],v[236:239],a[88:91]\n"
"buffer_load_dwordx4 v110,s[20:23],s77 offen offset:0 lds\n"
"v_mfma_f32_16x16x32_bf16 a[88:91],v[136:139],v[172:175],a[88:91]\n"
"v_mfma_f32_16x16x32_bf16 a[96:99],v[136:139],v[176:179],a[96:99]\n"
"v_mfma_f32_16x16x32_bf16 a[96:99],v[200:203],v[240:243],a[96:99]\n"
"v_mfma_f32_16x16x32_bf16 a[104:107],v[200:203],v[244:247],a[104:107]\n"
"buffer_load_dwordx4 v111,s[20:23],s77 offen offset:1024 lds\n"
"v_mfma_f32_16x16x32_bf16 a[104:107],v[136:139],v[180:183],a[104:107]\n"
"v_mfma_f32_16x16x32_bf16 a[112:115],v[136:139],v[184:187],a[112:115]\n"
"v_mfma_f32_16x16x32_bf16 a[112:115],v[200:203],v[248:251],a[112:115]\n"
"v_mfma_f32_16x16x32_bf16 a[120:123],v[200:203],v[252:255],a[120:123]\n"
"buffer_load_dwordx4 v113,s[20:23],s77 offen offset:2048 lds\n"
"v_mfma_f32_16x16x32_bf16 a[120:123],v[136:139],v[188:191],a[120:123]\n"
"s_waitcnt vmcnt(10)\n"
"v_mfma_f32_16x16x32_bf16 a[68:71],v[140:143],v[160:163],a[68:71]\n"
"v_lshlrev_b32 v24,16,v8\n"
"v_and_b32 v25,0xffff0000,v8\n"
"v_mfma_f32_16x16x32_bf16 a[68:71],v[204:207],v[224:227],a[68:71]\n"
"v_lshlrev_b32 v26,16,v9\n"
"v_and_b32 v27,0xffff0000,v9\n"
"v_mfma_f32_16x16x32_bf16 a[76:79],v[204:207],v[228:231],a[76:79]\n"
"buffer_load_dwordx4 v114,s[20:23],s77 offen offset:3072 lds\n"
"v_mfma_f32_16x16x32_bf16 a[76:79],v[140:143],v[164:167],a[76:79]\n"
"v_lshlrev_b32 v28,16,v10\n"
"v_and_b32 v29,0xffff0000,v10\n"
"v_mfma_f32_16x16x32_bf16 a[84:87],v[140:143],v[168:171],a[84:87]\n"
"v_lshlrev_b32 v30,16,v11\n"
"v_and_b32 v31,0xffff0000,v11\n"
"v_mfma_f32_16x16x32_bf16 a[84:87],v[204:207],v[232:235],a[84:87]\n"
"v_lshlrev_b32 v32,16,v12\n"
"v_and_b32 v33,0xffff0000,v12\n"
"v_mfma_f32_16x16x32_bf16 a[92:95],v[204:207],v[236:239],a[92:95]\n"
"s_mov_b32 s76,512\n"
"v_lshlrev_b32 v34,16,v13\n"
"s_add_u32 m0,s79,32768\n"
"v_mfma_f32_16x16x32_bf16 a[92:95],v[140:143],v[172:175],a[92:95]\n"
"buffer_load_dwordx4 v106,s[16:19],s76 offen offset:0 lds\n"
"v_mfma_f32_16x16x32_bf16 a[100:103],v[140:143],v[176:179],a[100:103]\n"
"v_and_b32 v35,0xffff0000,v13\n"
"v_lshlrev_b32 v36,16,v14\n"
"v_mfma_f32_16x16x32_bf16 a[100:103],v[204:207],v[240:243],a[100:103]\n"
"v_and_b32 v37,0xffff0000,v14\n"
"v_lshlrev_b32 v38,16,v15\n"
"v_mfma_f32_16x16x32_bf16 a[108:111],v[204:207],v[244:247],a[108:111]\n"
"v_and_b32 v39,0xffff0000,v15\n"
"v_lshlrev_b32 v40,16,v16\n"
"v_mfma_f32_16x16x32_bf16 a[108:111],v[140:143],v[180:183],a[108:111]\n"
"v_and_b32 v41,0xffff0000,v16\n"
"v_lshlrev_b32 v42,16,v17\n"
"v_mfma_f32_16x16x32_bf16 a[116:119],v[140:143],v[184:187],a[116:119]\n"
"buffer_load_dwordx4 v107,s[16:19],s76 offen offset:1024 lds\n"
"v_mfma_f32_16x16x32_bf16 a[116:119],v[204:207],v[248:251],a[116:119]\n"
"v_and_b32 v43,0xffff0000,v17\n"
"v_lshlrev_b32 v44,16,v18\n"
"v_mfma_f32_16x16x32_bf16 a[124:127],v[204:207],v[252:255],a[124:127]\n"
"v_and_b32 v45,0xffff0000,v18\n"
"v_lshlrev_b32 v46,16,v19\n"
"v_mfma_f32_16x16x32_bf16 a[124:127],v[140:143],v[188:191],a[124:127]\n"
"v_and_b32 v47,0xffff0000,v19\n"
"v_lshlrev_b32 v48,16,v20\n"
"v_mfma_f32_16x16x32_bf16 a[128:131],v[144:147],v[160:163],a[128:131]\n"
"v_and_b32 v49,0xffff0000,v20\n"
"v_lshlrev_b32 v50,16,v21\n"
"v_mfma_f32_16x16x32_bf16 a[128:131],v[208:211],v[224:227],a[128:131]\n"
"buffer_load_dwordx4 v108,s[16:19],s76 offen offset:2048 lds\n"
"v_mfma_f32_16x16x32_bf16 a[136:139],v[208:211],v[228:231],a[136:139]\n"
"v_and_b32 v51,0xffff0000,v21\n"
"v_lshlrev_b32 v52,16,v22\n"
"v_mfma_f32_16x16x32_bf16 a[136:139],v[144:147],v[164:167],a[136:139]\n"
"v_and_b32 v53,0xffff0000,v22\n"
"v_lshlrev_b32 v54,16,v23\n"
"v_mfma_f32_16x16x32_bf16 a[144:147],v[144:147],v[168:171],a[144:147]\n"
"v_and_b32 v55,0xffff0000,v23\n"
"v_add_f32 v56,v24,v25\n"
"v_mfma_f32_16x16x32_bf16 a[144:147],v[208:211],v[232:235],a[144:147]\n"
"v_add_f32 v57,v26,v27\n"
"v_add_f32 v56,v56,v28\n"
"v_mfma_f32_16x16x32_bf16 a[152:155],v[208:211],v[236:239],a[152:155]\n"
"buffer_load_dwordx4 v109,s[16:19],s76 offen offset:3072 lds\n"
"v_mfma_f32_16x16x32_bf16 a[152:155],v[144:147],v[172:175],a[152:155]\n"
"v_add_f32 v57,v57,v29\n"
"v_add_f32 v56,v56,v30\n"
"v_mfma_f32_16x16x32_bf16 a[160:163],v[144:147],v[176:179],a[160:163]\n"
"v_add_f32 v57,v57,v31\n"
"v_add_f32 v56,v56,v32\n"
"v_mfma_f32_16x16x32_bf16 a[160:163],v[208:211],v[240:243],a[160:163]\n"
"v_readfirstlane_b32 s82,v78\n"
"v_readfirstlane_b32 s83,v79\n"
"v_mfma_f32_16x16x32_bf16 a[168:171],v[208:211],v[244:247],a[168:171]\n"
"s_mov_b32 s76,262656\n"
"v_add_f32 v57,v57,v33\n"
"s_add_u32 m0,s79,36864\n"
"v_mfma_f32_16x16x32_bf16 a[168:171],v[144:147],v[180:183],a[168:171]\n"
"buffer_load_dwordx4 v106,s[16:19],s76 offen offset:0 lds\n"
"s_mul_i32 s80,s82,0x9e3779b1\n"
"v_mfma_f32_16x16x32_bf16 a[176:179],v[144:147],v[184:187],a[176:179]\n"
"s_add_u32 s80,s80,0x7f4a7c15\n"
"v_add_f32 v56,v56,v34\n"
"v_add_f32 v57,v57,v35\n"
"v_mfma_f32_16x16x32_bf16 a[176:179],v[208:211],v[248:251],a[176:179]\n"
"s_add_u32 s81,s83,s80\n"
"s_add_u32 s81,s81,1\n"
"v_mfma_f32_16x16x32_bf16 a[184:187],v[208:211],v[252:255],a[184:187]\n"
"v_mov_b32 v124,s80\n"
"v_mov_b32 v125,s81\n"
"v_mfma_f32_16x16x32_bf16 a[184:187],v[144:147],v[188:191],a[184:187]\n"
"v_add_f32 v56,v56,v36\n"
"v_add_f32 v57,v57,v37\n"
"v_mfma_f32_16x16x32_bf16 a[132:135],v[148:151],v[160:163],a[132:135]\n"
"buffer_load_dwordx4 v107,s[16:19],s76 offen offset:1024 lds\n"
"v_mfma_f32_16x16x32_bf16 a[132:135],v[212:215],v[224:227],a[132:135]\n"
"v_add_f32 v56,v56,v38\n"
"v_add_f32 v57,v57,v39\n"
"v_mfma_f32_16x16x32_bf16 a[140:143],v[212:215],v[228:231],a[140:143]\n"
"v_add_f32 v56,v56,v40\n"
"v_add_f32 v57,v57,v41\n"
"v_mfma_f32_16x16x32_bf16 a[140:143],v[148:151],v[164:167],a[140:143]\n"
"v_add_f32 v56,v56,v42\n"
"v_add_f32 v57,v57,v43\n"
"v_mfma_f32_16x16x32_bf16 a[148:151],v[148:151],v[168:171],a[148:151]\n"
"v_add_f32 v56,v56,v44\n"
"v_add_f32 v57,v57,v45\n"
"v_mfma_f32_16x16x32_bf16 a[148:151],v[212:215],v[232:235],a[148:151]\n"
"buffer_load_dwordx4 v108,s[16:19],s76 offen offset:2048 lds\n"
"v_mfma_f32_16x16x32_bf16 a[156:159],v[212:215],v[236:239],a[156:159]\n"
"v_add_f32 v56,v56,v46\n"
"v_add_f32 v57,v57,v47\n"
"v_mfma_f32_16x16x32_bf16 a[156:159],v[148:151],v[172:175],a[156:159]\n"
"v_add_f32 v56,v56,v48\n"
"v_add_f32 v57,v57,v49\n"
"v_mfma_f32_16x16x32_bf16 a[164:167],v[148:151],v[176:179],a[164:167]\n"
"v_add_f32 v56,v56,v50\n"
"v_add_f32 v57,v57,v51\n"
"v_mfma_f32_16x16x32_bf16 a[164:167],v[212:215],v[240:243],a[164:167]\n"
"v_add_f32 v56,v56,v52\n"
"v_add_f32 v57,v57,v53\n"
"v_mfma_f32_16x16x32_bf16 a[172:175],v[212:215],v[244:247],a[172:175]\n"
"buffer_load_dwordx4 v109,s[16:19],s76 offen offset:3072 lds\n"
"v_mfma_f32_16x16x32_bf16 a[172:175],v[148:151],v[180:183],a[172:175]\n"
"s_mov_b32 s87,2097152\n"
"v_add_f32 v56,v56,v54\n"
"v_add_f32 v57,v57,v55\n"
"v_mfma_f32_16x16x32_bf16 a[180:183],v[148:151],v[184:187],a[180:183]\n"
"buffer_load_dword v115,v112,s[36:39],s87 offen\n"
"v_mfma_f32_16x16x32_bf16 a[180:183],v[212:215],v[248:251],a[180:183]\n"
"s_mov_b32 s87,2621440\n"
"v_add_f32 v56,v56,v57\n"
"v_mfma_f32_16x16x32_bf16 a[188:191],v[212:215],v[252:255],a[188:191]\n"
"buffer_load_dword v115,v112,s[36:39],s87 offen\n"
"v_add_f32_dpp v56,v56,v56 quad_perm:[1,0,3,2] row_mask:0xf bank_mask:0xf\n"
"v_mfma_f32_16x16x32_bf16 a[188:191],v[148:151],v[188:191],a[188:191]\n"
"s_barrier\n"
"v_mfma_f32_16x16x32_bf16 a[192:195],v[216:219],v[224:227],a[192:195]\n"
"ds_read_b128 v[152:155],v82 offset:12288\n"
"v_mfma_f32_16x16x32_bf16 a[196:199],v[220:223],v[224:227],a[196:199]\n"
"ds_read_b128 v[156:159],v82 offset:14336\n"
"v_add_f32_dpp v56,v56,v56 quad_perm:[2,3,0,1] row_mask:0xf bank_mask:0xf\n"
"v_mfma_f32_16x16x32_bf16 a[204:207],v[220:223],v[228:231],a[204:207]\n"
"ds_read_b64_tr_b16 v[160:161],v98 offset:49152\n"
"v_mfma_f32_16x16x32_bf16 a[200:203],v[216:219],v[228:231],a[200:203]\n"
"ds_read_b64_tr_b16 v[162:163],v98 offset:50176\n"
"v_add_f32_dpp v56,v56,v56 row_ror:8 row_mask:0xf bank_mask:0xf\n"
"v_mfma_f32_16x16x32_bf16 a[208:211],v[216:219],v[232:235],a[208:211]\n"
"ds_read_b64_tr_b16 v[164:165],v99 offset:49152\n"
"v_mov_b32 v57,v56\n"
"v_mfma_f32_16x16x32_bf16 a[212:215],v[220:223],v[232:235],a[212:215]\n"
"ds_read_b64_tr_b16 v[166:167],v99 offset:50176\n"
"v_mfma_f32_16x16x32_bf16 a[220:223],v[220:223],v[236:239],a[220:223]\n"
"ds_read_b64_tr_b16 v[168:169],v100 offset:49152\n"
"v_permlane16_swap_b32 v57,v56\n"
"v_mfma_f32_16x16x32_bf16 a[216:219],v[216:219],v[236:239],a[216:219]\n"
"ds_read_b64_tr_b16 v[170:171],v100 offset:50176\n"
"v_add_f32 v56,v56,v57\n"
"v_mfma_f32_16x16x32_bf16 a[224:227],v[216:219],v[240:243],a[224:227]\n"
"ds_read_b64_tr_b16 v[172:173],v101 offset:49152\n"
"v_mov_b32 v57,v56\n"
"v_mfma_f32_16x16x32_bf16 a[228:231],v[220:223],v[240:243],a[228:231]\n"
"ds_read_b64_tr_b16 v[174:175],v101 offset:50176\n"
"v_mfma_f32_16x16x32_bf16 a[236:239],v[220:223],v[244:247],a[236:239]\n"
"ds_read_b64_tr_b16 v[176:177],v102 offset:49152\n"
"v_permlane32_swap_b32 v57,v56\n"
"v_mfma_f32_16x16x32_bf16 a[232:235],v[216:219],v[244:247],a[232:235]\n"
"ds_read_b64_tr_b16 v[178:179],v102 offset:50176\n"
"v_add_f32 v56,v56,v57\n"
"v_mfma_f32_16x16x32_bf16 a[240:243],v[216:219],v[248:251],a[240:243]\n"
"ds_read_b64_tr_b16 v[180:181],v103 offset:49152\n"
"v_mul_f32 v58,0x3a800000,v56\n"
"v_mfma_f32_16x16x32_bf16 a[244:247],v[220:223],v[248:251],a[244:247]\n"
"ds_read_b64_tr_b16 v[182:183],v103 offset:50176\n"
"v_sub_f32 v62,v24,v58\n"
"v_mfma_f32_16x16x32_bf16 a[252:255],v[220:223],v[252:255],a[252:255]\n"
"ds_read_b64_tr_b16 v[184:185],v104 offset:49152\n"
"v_mul_f32 v59,v62,v62\n"
"v_mfma_f32_16x16x32_bf16 a[248:251],v[216:219],v[252:255],a[248:251]\n"
"ds_read_b64_tr_b16 v[186:187],v104 offset:50176\n"
"v_sub_f32 v63,v25,v58\n"
"s_waitcnt lgkmcnt(10)\n"
"v_mfma_f32_16x16x32_bf16 a[192:195],v[152:155],v[160:163],a[192:195]\n"
"ds_read_b64_tr_b16 v[188:189],v105 offset:49152\n"
"v_mul_f32 v60,v63,v63\n"
"v_mfma_f32_16x16x32_bf16 a[196:199],v[156:159],v[160:163],a[196:199]\n"
"ds_read_b64_tr_b16 v[190:191],v105 offset:50176\n"
"v_sub_f32 v64,v26,v58\n"
"v_mfma_f32_16x16x32_bf16 a[204:207],v[156:159],v[164:167],a[204:207]\n"
"ds_read_b128 v[128:131],v82 offset:0\n"
"v_fmac_f32 v59,v64,v64\n"
"v_mfma_f32_16x16x32_bf16 a[200:203],v[152:155],v[164:167],a[200:203]\n"
"ds_read_b128 v[192:195],v83 offset:0\n"
"s_waitcnt lgkmcnt(10)\n"
"v_mfma_f32_16x16x32_bf16 a[208:211],v[152:155],v[168:171],a[208:211]\n"
"ds_read_b64_tr_b16 v[224:225],v98 offset:32768\n"
"v_sub_f32 v65,v27,v58\n"
"v_mfma_f32_16x16x32_bf16 a[212:215],v[156:159],v[168:171],a[212:215]\n"
"ds_read_b64_tr_b16 v[226:227],v98 offset:33792\n"
"v_fmac_f32 v60,v65,v65\n"
"v_mfma_f32_16x16x32_bf16 a[220:223],v[156:159],v[172:175],a[220:223]\n"
"ds_read_b64_tr_b16 v[228:229],v99 offset:32768\n"
"v_sub_f32 v62,v28,v58\n"
"v_mfma_f32_16x16x32_bf16 a[216:219],v[152:155],v[172:175],a[216:219]\n"
"ds_read_b64_tr_b16 v[230:231],v99 offset:33792\n"
"s_waitcnt lgkmcnt(10)\n"
"v_mfma_f32_16x16x32_bf16 a[224:227],v[152:155],v[176:179],a[224:227]\n"
"ds_read_b64_tr_b16 v[232:233],v100 offset:32768\n"
"v_fmac_f32 v59,v62,v62\n"
"v_mfma_f32_16x16x32_bf16 a[228:231],v[156:159],v[176:179],a[228:231]\n"
"ds_read_b64_tr_b16 v[234:235],v100 offset:33792\n"
"v_sub_f32 v63,v29,v58\n"
"v_mfma_f32_16x16x32_bf16 a[236:239],v[156:159],v[180:183],a[236:239]\n"
"ds_read_b64_tr_b16 v[236:237],v101 offset:32768\n"
"v_fmac_f32 v60,v63,v63\n"
"v_mfma_f32_16x16x32_bf16 a[232:235],v[152:155],v[180:183],a[232:235]\n"
"ds_read_b64_tr_b16 v[238:239],v101 offset:33792\n"
"s_waitcnt lgkmcnt(10)\n"
"v_mfma_f32_16x16x32_bf16 a[240:243],v[152:155],v[184:187],a[240:243]\n"
"ds_read_b64_tr_b16 v[240:241],v102 offset:32768\n"
"v_sub_f32 v64,v30,v58\n"
"v_mfma_f32_16x16x32_bf16 a[244:247],v[156:159],v[184:187],a[244:247]\n"
"ds_read_b64_tr_b16 v[242:243],v102 offset:33792\n"
"v_fmac_f32 v59,v64,v64\n"
"v_mfma_f32_16x16x32_bf16 a[252:255],v[156:159],v[188:191],a[252:255]\n"
"ds_read_b64_tr_b16 v[244:245],v103 offset:32768\n"
"v_sub_f32 v65,v31,v58\n"
"v_mfma_f32_16x16x32_bf16 a[248:251],v[152:155],v[188:191],a[248:251]\n"
"ds_read_b64_tr_b16 v[246:247],v103 offset:33792\n"
"s_waitcnt lgkmcnt(8)\n"
"v_mfma_f32_16x16x32_bf16 a[0:3],v[128:131],v[160:163],a[0:3]\n"
"ds_read_b64_tr_b16 v[248:249],v104 offset:32768\n"
"v_fmac_f32 v60,v65,v65\n"
"v_mfma_f32_16x16x32_bf16 a[0:3],v[192:195],v[224:227],a[0:3]\n"
"ds_read_b64_tr_b16 v[250:251],v104 offset:33792\n"
"v_sub_f32 v62,v32,v58\n"
"v_mfma_f32_16x16x32_bf16 a[8:11],v[192:195],v[228:231],a[8:11]\n"
"ds_read_b64_tr_b16 v[252:253],v105 offset:32768\n"
"v_fmac_f32 v59,v62,v62\n"
"v_mfma_f32_16x16x32_bf16 a[8:11],v[128:131],v[164:167],a[8:11]\n"
"ds_read_b64_tr_b16 v[254:255],v105 offset:33792\n"
"s_waitcnt lgkmcnt(8)\n"
"v_mfma_f32_16x16x32_bf16 a[16:19],v[128:131],v[168:171],a[16:19]\n"
"ds_read_b128 v[132:135],v82 offset:2048\n"
"v_sub_f32 v63,v33,v58\n"
"v_mfma_f32_16x16x32_bf16 a[16:19],v[192:195],v[232:235],a[16:19]\n"
"ds_read_b128 v[196:199],v83 offset:2048\n"
"v_fmac_f32 v60,v63,v63\n"
"v_mfma_f32_16x16x32_bf16 a[24:27],v[192:195],v[236:239],a[24:27]\n"
"ds_read_b128 v[136:139],v82 offset:4096\n"
"v_sub_f32 v64,v34,v58\n"
"v_mfma_f32_16x16x32_bf16 a[24:27],v[128:131],v[172:175],a[24:27]\n"
"ds_read_b128 v[200:203],v83 offset:4096\n"
"s_waitcnt lgkmcnt(8)\n"
"v_mfma_f32_16x16x32_bf16 a[32:35],v[128:131],v[176:179],a[32:35]\n"
"ds_read_b128 v[140:143],v82 offset:6144\n"
"v_fmac_f32 v59,v64,v64\n"
"v_mfma_f32_16x16x32_bf16 a[32:35],v[192:195],v[240:243],a[32:35]\n"
"ds_read_b128 v[204:207],v83 offset:6144\n"
"v_sub_f32 v65,v35,v58\n"
"v_mfma_f32_16x16x32_bf16 a[40:43],v[192:195],v[244:247],a[40:43]\n"
"ds_read_b128 v[144:147],v82 offset:8192\n"
"v_fmac_f32 v60,v65,v65\n"
"v_mfma_f32_16x16x32_bf16 a[40:43],v[128:131],v[180:183],a[40:43]\n"
"ds_read_b128 v[208:211],v83 offset:8192\n"
"s_waitcnt lgkmcnt(8)\n"
"v_mfma_f32_16x16x32_bf16 a[48:51],v[128:131],v[184:187],a[48:51]\n"
"ds_read_b128 v[148:151],v82 offset:10240\n"
"v_sub_f32 v62,v36,v58\n"
"v_mfma_f32_16x16x32_bf16 a[48:51],v[192:195],v[248:251],a[48:51]\n"
"ds_read_b128 v[212:215],v83 offset:10240\n"
"v_fmac_f32 v59,v62,v62\n"
"v_mfma_f32_16x16x32_bf16 a[56:59],v[192:195],v[252:255],a[56:59]\n"
"ds_read_b128 v[216:219],v83 offset:12288\n"
"v_sub_f32 v63,v37,v58\n"
"v_mfma_f32_16x16x32_bf16 a[56:59],v[128:131],v[188:191],a[56:59]\n"
"ds_read_b128 v[220:223],v83 offset:14336\n"
"s_waitcnt lgkmcnt(10)\n"
"v_mfma_f32_16x16x32_bf16 a[4:7],v[132:135],v[160:163],a[4:7]\n"
"v_fmac_f32 v60,v63,v63\n"
"v_sub_f32 v64,v38,v58\n"
"v_mfma_f32_16x16x32_bf16 a[4:7],v[196:199],v[224:227],a[4:7]\n"
"v_fmac_f32 v59,v64,v64\n"
"v_sub_f32 v65,v39,v58\n"
"v_mfma_f32_16x16x32_bf16 a[12:15],v[196:199],v[228:231],a[12:15]\n"
"v_fmac_f32 v60,v65,v65\n"
"v_sub_f32 v62,v40,v58\n"
"v_mfma_f32_16x16x32_bf16 a[12:15],v[132:135],v[164:167],a[12:15]\n"
"v_fmac_f32 v59,v62,v62\n"
"v_sub_f32 v63,v41,v58\n"
"v_mfma_f32_16x16x32_bf16 a[20:23],v[132:135],v[168:171],a[20:23]\n"
"s_waitcnt lgkmcnt(0)\n"
"s_barrier\n"
"v_mfma_f32_16x16x32_bf16 a[20:23],v[196:199],v[232:235],a[20:23]\n"
"s_mov_b32 s77,2097152\n"
"v_fmac_f32 v60,v63,v63\n"
"s_add_u32 m0,s78,147456\n"
"v_mfma_f32_16x16x32_bf16 a[28:31],v[196:199],v[236:239],a[28:31]\n"
"buffer_load_dwordx4 v110,s[20:23],s77 offen offset:0 lds\n"
"v_mfma_f32_16x16x32_bf16 a[28:31],v[132:135],v[172:175],a[28:31]\n"
"v_sub_f32 v64,v42,v58\n"
"v_fmac_f32 v59,v64,v64\n"
"v_mfma_f32_16x16x32_bf16 a[36:39],v[132:135],v[176:179],a[36:39]\n"
"v_sub_f32 v65,v43,v58\n"
"v_fmac_f32 v60,v65,v65\n"
"v_mfma_f32_16x16x32_bf16 a[36:39],v[196:199],v[240:243],a[36:39]\n"
"v_sub_f32 v62,v44,v58\n"
"v_fmac_f32 v59,v62,v62\n"
"v_mfma_f32_16x16x32_bf16 a[44:47],v[196:199],v[244:247],a[44:47]\n"
"buffer_load_dwordx4 v111,s[20:23],s77 offen offset:1024 lds\n"
"v_mfma_f32_16x16x32_bf16 a[44:47],v[132:135],v[180:183],a[44:47]\n"
"v_sub_f32 v63,v45,v58\n"
"v_fmac_f32 v60,v63,v63\n"
"v_mfma_f32_16x16x32_bf16 a[52:55],v[132:135],v[184:187],a[52:55]\n"
"v_sub_f32 v64,v46,v58\n"
"v_fmac_f32 v59,v64,v64\n"
"v_mfma_f32_16x16x32_bf16 a[52:55],v[196:199],v[248:251],a[52:55]\n"
"v_sub_f32 v65,v47,v58\n"
"v_fmac_f32 v60,v65,v65\n"
"v_mfma_f32_16x16x32_bf16 a[60:63],v[196:199],v[252:255],a[60:63]\n"
"buffer_load_dwordx4 v113,s[20:23],s77 offen offset:2048 lds\n"
"v_mfma_f32_16x16x32_bf16 a[60:63],v[132:135],v[188:191],a[60:63]\n"
"v_sub_f32 v62,v48,v58\n"
"v_fmac_f32 v59,v62,v62\n"
"v_mfma_f32_16x16x32_bf16 a[64:67],v[136:139],v[160:163],a[64:67]\n"
"v_sub_f32 v63,v49,v58\n"
"v_fmac_f32 v60,v63,v63\n"
"v_mfma_f32_16x16x32_bf16 a[64:67],v[200:203],v[224:227],a[64:67]\n"
"v_sub_f32 v64,v50,v58\n"
"v_fmac_f32 v59,v64,v64\n"
"v_mfma_f32_16x16x32_bf16 a[72:75],v[200:203],v[228:231],a[72:75]\n"
"buffer_load_dwordx4 v114,s[20:23],s77 offen offset:3072 lds\n"
"v_mfma_f32_16x16x32_bf16 a[72:75],v[136:139],v[164:167],a[72:75]\n"
"v_sub_f32 v65,v51,v58\n"
"v_fmac_f32 v60,v65,v65\n"
"v_mfma_f32_16x16x32_bf16 a[80:83],v[136:139],v[168:171],a[80:83]\n"
"v_sub_f32 v62,v52,v58\n"
"v_fmac_f32 v59,v62,v62\n"
"v_mfma_f32_16x16x32_bf16 a[80:83],v[200:203],v[232:235],a[80:83]\n"
"s_mov_b32 s77,2359296\n"
"v_sub_f32 v63,v53,v58\n"
"s_add_u32 m0,s78,131072\n"
"v_mfma_f32_16x16x32_bf16 a[88:91],v[200:203],v[236:239],a[88:91]\n"
"buffer_load_dwordx4 v110,s[20:23],s77 offen offset:0 lds\n"
"v_mfma_f32_16x16x32_bf16 a[88:91],v[136:139],v[172:175],a[88:91]\n"
"v_fmac_f32 v60,v63,v63\n"
"v_sub_f32 v64,v54,v58\n"
"v_mfma_f32_16x16x32_bf16 a[96:99],v[136:139],v[176:179],a[96:99]\n"
"v_fmac_f32 v59,v64,v64\n"
"v_sub_f32 v65,v55,v58\n"
"v_mfma_f32_16x16x32_bf16 a[96:99],v[200:203],v[240:243],a[96:99]\n"
"v_fmac_f32 v60,v65,v65\n"
"v_add_f32 v59,v59,v60\n"
"v_mfma_f32_16x16x32_bf16 a[104:107],v[200:203],v[244:247],a[104:107]\n"
"buffer_load_dwordx4 v111,s[20:23],s77 offen offset:1024 lds\n"
"v_mfma_f32_16x16x32_bf16 a[104:107],v[136:139],v[180:183],a[104:107]\n"
"v_mfma_f32_16x16x32_bf16 a[112:115],v[136:139],v[184:187],a[112:115]\n"
"v_add_f32_dpp v59,v59,v59 quad_perm:[1,0,3,2] row_mask:0xf bank_mask:0xf\n"
"v_mfma_f32_16x16x32_bf16 a[112:115],v[200:203],v[248:251],a[112:115]\n"
"s_nop 0\n"
"v_add_f32_dpp v59,v59,v59 quad_perm:[2,3,0,1] row_mask:0xf bank_mask:0xf\n"
"v_mfma_f32_16x16x32_bf16 a[120:123],v[200:203],v[252:255],a[120:123]\n"
"buffer_load_dwordx4 v113,s[20:23],s77 offen offset:2048 lds\n"
"v_mfma_f32_16x16x32_bf16 a[120:123],v[136:139],v[188:191],a[120:123]\n"
"v_add_f32_dpp v59,v59,v59 row_ror:8 row_mask:0xf bank_mask:0xf\n"
"v_mov_b32 v57,v59\n"
"v_mfma_f32_16x16x32_bf16 a[68:71],v[140:143],v[160:163],a[68:71]\n"
"v_mfma_f32_16x16x32_bf16 a[68:71],v[204:207],v[224:227],a[68:71]\n"
"v_permlane16_swap_b32 v57,v59\n"
"v_add_f32 v59,v59,v57\n"
"v_mfma_f32_16x16x32_bf16 a[76:79],v[204:207],v[228:231],a[76:79]\n"
"buffer_load_dwordx4 v114,s[20:23],s77 offen offset:3072 lds\n"
"v_mfma_f32_16x16x32_bf16 a[76:79],v[140:143],v[164:167],a[76:79]\n"
"v_mov_b32 v57,v59\n"
"v_mfma_f32_16x16x32_bf16 a[84:87],v[140:143],v[168:171],a[84:87]\n"
"s_nop 0\n"
"v_permlane32_swap_b32 v57,v59\n"
"v_add_f32 v59,v59,v57\n"
"v_mfma_f32_16x16x32_bf16 a[84:87],v[204:207],v[232:235],a[84:87]\n"
"v_mul_f32 v59,0x3a800000,v59\n"
"v_cvt_pk_bf16_f32 v59,v59,v59\n"
"v_mfma_f32_16x16x32_bf16 a[92:95],v[204:207],v[236:239],a[92:95]\n"
"s_mov_b32 s76,640\n"
"v_lshlrev_b32 v59,16,v59\n"
"s_add_u32 m0,s79,65536\n"
"v_mfma_f32_16x16x32_bf16 a[92:95],v[140:143],v[172:175],a[92:95]\n"
"buffer_load_dwordx4 v106,s[16:19],s76 offen offset:0 lds\n"
"v_mfma_f32_16x16x32_bf16 a[100:103],v[140:143],v[176:179],a[100:103]\n"
"v_add_f32 v59,0x3727c5ac,v59\n"
"v_cvt_pk_bf16_f32 v59,v59,v59\n"
"v_mfma_f32_16x16x32_bf16 a[100:103],v[204:207],v[240:243],a[100:103]\n"
"v_lshlrev_b32 v59,16,v59\n"
"v_sqrt_f32 v59,v59\n"
"v_mfma_f32_16x16x32_bf16 a[108:111],v[204:207],v[244:247],a[108:111]\n"
"v_cvt_pk_bf16_f32 v58,v58,v58\n"
"v_lshlrev_b32 v58,16,v58\n"
"v_mfma_f32_16x16x32_bf16 a[108:111],v[140:143],v[180:183],a[108:111]\n"
"v_cvt_pk_bf16_f32 v59,v59,v59\n"
"v_lshlrev_b32 v59,16,v59\n"
"v_mfma_f32_16x16x32_bf16 a[116:119],v[140:143],v[184:187],a[116:119]\n"
"buffer_load_dwordx4 v107,s[16:19],s76 offen offset:1024 lds\n"
"v_mfma_f32_16x16x32_bf16 a[116:119],v[204:207],v[248:251],a[116:119]\n"
"v_rcp_f32 v66,v59\n"
"v_mfma_f32_16x16x32_bf16 a[124:127],v[204:207],v[252:255],a[124:127]\n"
"v_mul_f32 v66,0x3e0293ee,v66\n"
"v_mul_f32 v58,v58,v66\n"
"v_mfma_f32_16x16x32_bf16 a[124:127],v[140:143],v[188:191],a[124:127]\n"
"v_fma_f32 v24,v24,v66,-v58\n"
"v_fma_f32 v25,v25,v66,-v58\n"
"v_mfma_f32_16x16x32_bf16 a[128:131],v[144:147],v[160:163],a[128:131]\n"
"v_cvt_pk_bf16_f32 v8,v24,v25\n"
"v_fma_f32 v26,v26,v66,-v58\n"
"v_mfma_f32_16x16x32_bf16 a[128:131],v[208:211],v[224:227],a[128:131]\n"
"buffer_load_dwordx4 v108,s[16:19],s76 offen offset:2048 lds\n"
"v_mfma_f32_16x16x32_bf16 a[136:139],v[208:211],v[228:231],a[136:139]\n"
"v_fma_f32 v27,v27,v66,-v58\n"
"v_cvt_pk_bf16_f32 v9,v26,v27\n"
"v_mfma_f32_16x16x32_bf16 a[136:139],v[144:147],v[164:167],a[136:139]\n"
"v_fma_f32 v28,v28,v66,-v58\n"
"v_fma_f32 v29,v29,v66,-v58\n"
"v_mfma_f32_16x16x32_bf16 a[144:147],v[144:147],v[168:171],a[144:147]\n"
"v_cvt_pk_bf16_f32 v10,v28,v29\n"
"v_fma_f32 v30,v30,v66,-v58\n"
"v_mfma_f32_16x16x32_bf16 a[144:147],v[208:211],v[232:235],a[144:147]\n"
"v_fma_f32 v31,v31,v66,-v58\n"
"v_cvt_pk_bf16_f32 v11,v30,v31\n"
"v_mfma_f32_16x16x32_bf16 a[152:155],v[208:211],v[236:239],a[152:155]\n"
"buffer_load_dwordx4 v109,s[16:19],s76 offen offset:3072 lds\n"
"v_mfma_f32_16x16x32_bf16 a[152:155],v[144:147],v[172:175],a[152:155]\n"
"buffer_store_dwordx4 v[8:11],v71,s[24:27],s54 offen sc1\n"
"v_mfma_f32_16x16x32_bf16 a[160:163],v[144:147],v[176:179],a[160:163]\n"
"s_add_u32 s54,s54,131072\n"
"v_fma_f32 v32,v32,v66,-v58\n"
"v_fma_f32 v33,v33,v66,-v58\n"
"v_mfma_f32_16x16x32_bf16 a[160:163],v[208:211],v[240:243],a[160:163]\n"
"v_cvt_pk_bf16_f32 v12,v32,v33\n"
"v_fma_f32 v34,v34,v66,-v58\n"
"v_mfma_f32_16x16x32_bf16 a[168:171],v[208:211],v[244:247],a[168:171]\n"
"s_mov_b32 s76,262784\n"
"v_fma_f32 v35,v35,v66,-v58\n"
"s_add_u32 m0,s79,69632\n"
"v_mfma_f32_16x16x32_bf16 a[168:171],v[144:147],v[180:183],a[168:171]\n"
"buffer_load_dwordx4 v106,s[16:19],s76 offen offset:0 lds\n"
"v_mfma_f32_16x16x32_bf16 a[176:179],v[144:147],v[184:187],a[176:179]\n"
"v_cvt_pk_bf16_f32 v13,v34,v35\n"
"v_fma_f32 v36,v36,v66,-v58\n"
"v_mfma_f32_16x16x32_bf16 a[176:179],v[208:211],v[248:251],a[176:179]\n"
"v_fma_f32 v37,v37,v66,-v58\n"
"v_cvt_pk_bf16_f32 v14,v36,v37\n"
"v_mfma_f32_16x16x32_bf16 a[184:187],v[208:211],v[252:255],a[184:187]\n"
"v_fma_f32 v38,v38,v66,-v58\n"
"v_fma_f32 v39,v39,v66,-v58\n"
"v_cvt_pk_bf16_f32 v15,v38,v39\n"
"v_mfma_f32_16x16x32_bf16 a[184:187],v[144:147],v[188:191],a[184:187]\n"
"buffer_store_dwordx4 v[12:15],v71,s[24:27],s54 offen sc1\n"
"v_mfma_f32_16x16x32_bf16 a[132:135],v[148:151],v[160:163],a[132:135]\n"
"buffer_load_dwordx4 v107,s[16:19],s76 offen offset:1024 lds\n"
"v_mfma_f32_16x16x32_bf16 a[132:135],v[212:215],v[224:227],a[132:135]\n"
"s_add_u32 s54,s54,131072\n"
"v_fma_f32 v40,v40,v66,-v58\n"
"v_mfma_f32_16x16x32_bf16 a[140:143],v[212:215],v[228:231],a[140:143]\n"
"v_fma_f32 v41,v41,v66,-v58\n"
"v_cvt_pk_bf16_f32 v16,v40,v41\n"
"v_mfma_f32_16x16x32_bf16 a[140:143],v[148:151],v[164:167],a[140:143]\n"
"v_fma_f32 v42,v42,v66,-v58\n"
"v_fma_f32 v43,v43,v66,-v58\n"
"v_mfma_f32_16x16x32_bf16 a[148:151],v[148:151],v[168:171],a[148:151]\n"
"v_cvt_pk_bf16_f32 v17,v42,v43\n"
"v_fma_f32 v44,v44,v66,-v58\n"
"v_mfma_f32_16x16x32_bf16 a[148:151],v[212:215],v[232:235],a[148:151]\n"
"buffer_load_dwordx4 v108,s[16:19],s76 offen offset:2048 lds\n"
"v_mfma_f32_16x16x32_bf16 a[156:159],v[212:215],v[236:239],a[156:159]\n"
"v_fma_f32 v45,v45,v66,-v58\n"
"v_cvt_pk_bf16_f32 v18,v44,v45\n"
"v_mfma_f32_16x16x32_bf16 a[156:159],v[148:151],v[172:175],a[156:159]\n"
"v_fma_f32 v46,v46,v66,-v58\n"
"v_fma_f32 v47,v47,v66,-v58\n"
"v_cvt_pk_bf16_f32 v19,v46,v47\n"
"v_mfma_f32_16x16x32_bf16 a[164:167],v[148:151],v[176:179],a[164:167]\n"
"buffer_store_dwordx4 v[16:19],v71,s[24:27],s54 offen sc1\n"
"v_mfma_f32_16x16x32_bf16 a[164:167],v[212:215],v[240:243],a[164:167]\n"
"s_add_u32 s54,s54,131072\n"
"v_fma_f32 v48,v48,v66,-v58\n"
"v_mfma_f32_16x16x32_bf16 a[172:175],v[212:215],v[244:247],a[172:175]\n"
"buffer_load_dwordx4 v109,s[16:19],s76 offen offset:3072 lds\n"
"v_mfma_f32_16x16x32_bf16 a[172:175],v[148:151],v[180:183],a[172:175]\n"
"s_mov_b32 s87,0\n"
"v_fma_f32 v49,v49,v66,-v58\n"
"v_cvt_pk_bf16_f32 v20,v48,v49\n"
"v_mfma_f32_16x16x32_bf16 a[180:183],v[148:151],v[184:187],a[180:183]\n"
"buffer_load_dword v115,v112,s[40:43],s87 offen\n"
"v_mfma_f32_16x16x32_bf16 a[180:183],v[212:215],v[248:251],a[180:183]\n"
"s_mov_b32 s87,1048576\n"
"v_fma_f32 v50,v50,v66,-v58\n"
"v_fma_f32 v51,v51,v66,-v58\n"
"v_mfma_f32_16x16x32_bf16 a[188:191],v[212:215],v[252:255],a[188:191]\n"
"buffer_load_dword v115,v112,s[40:43],s87 offen\n"
"v_mfma_f32_16x16x32_bf16 a[188:191],v[148:151],v[188:191],a[188:191]\n"
"s_mov_b32 s87,2097152\n"
"s_waitcnt vmcnt(31)\n"
"s_barrier\n"
"v_mfma_f32_16x16x32_bf16 a[192:195],v[216:219],v[224:227],a[192:195]\n"
"ds_read_b128 v[152:155],v80 offset:12288\n"
"buffer_load_dword v115,v112,s[40:43],s87 offen\n"
"v_mfma_f32_16x16x32_bf16 a[196:199],v[220:223],v[224:227],a[196:199]\n"
"ds_read_b128 v[156:159],v80 offset:14336\n"
"v_cvt_pk_bf16_f32 v21,v50,v51\n"
"v_mfma_f32_16x16x32_bf16 a[204:207],v[220:223],v[228:231],a[204:207]\n"
"ds_read_b64_tr_b16 v[160:161],v98 offset:0\n"
"v_fma_f32 v52,v52,v66,-v58\n"
"v_mfma_f32_16x16x32_bf16 a[200:203],v[216:219],v[228:231],a[200:203]\n"
"ds_read_b64_tr_b16 v[162:163],v98 offset:1024\n"
"v_fma_f32 v53,v53,v66,-v58\n"
"v_mfma_f32_16x16x32_bf16 a[208:211],v[216:219],v[232:235],a[208:211]\n"
"ds_read_b64_tr_b16 v[164:165],v99 offset:0\n"
"v_cvt_pk_bf16_f32 v22,v52,v53\n"
"v_mfma_f32_16x16x32_bf16 a[212:215],v[220:223],v[232:235],a[212:215]\n"
"ds_read_b64_tr_b16 v[166:167],v99 offset:1024\n"
"v_fma_f32 v54,v54,v66,-v58\n"
"v_mfma_f32_16x16x32_bf16 a[220:223],v[220:223],v[236:239],a[220:223]\n"
"ds_read_b64_tr_b16 v[168:169],v100 offset:0\n"
"v_fma_f32 v55,v55,v66,-v58\n"
"v_mfma_f32_16x16x32_bf16 a[216:219],v[216:219],v[236:239],a[216:219]\n"
"ds_read_b64_tr_b16 v[170:171],v100 offset:1024\n"
"v_cvt_pk_bf16_f32 v23,v54,v55\n"
"v_mfma_f32_16x16x32_bf16 a[224:227],v[216:219],v[240:243],a[224:227]\n"
"ds_read_b64_tr_b16 v[172:173],v101 offset:0\n"
"buffer_store_dwordx4 v[20:23],v71,s[24:27],s54 offen sc1\n"
"v_mfma_f32_16x16x32_bf16 a[228:231],v[220:223],v[240:243],a[228:231]\n"
"ds_read_b64_tr_b16 v[174:175],v101 offset:1024\n"
"v_mfma_f32_16x16x32_bf16 a[236:239],v[220:223],v[244:247],a[236:239]\n"
"ds_read_b64_tr_b16 v[176:177],v102 offset:0\n"
"v_mfma_f32_16x16x32_bf16 a[232:235],v[216:219],v[244:247],a[232:235]\n"
"ds_read_b64_tr_b16 v[178:179],v102 offset:1024\n"
"v_mfma_f32_16x16x32_bf16 a[240:243],v[216:219],v[248:251],a[240:243]\n"
"ds_read_b64_tr_b16 v[180:181],v103 offset:0\n"
"v_mfma_f32_16x16x32_bf16 a[244:247],v[220:223],v[248:251],a[244:247]\n"
"ds_read_b64_tr_b16 v[182:183],v103 offset:1024\n"
"v_mfma_f32_16x16x32_bf16 a[252:255],v[220:223],v[252:255],a[252:255]\n"
"ds_read_b64_tr_b16 v[184:185],v104 offset:0\n"
"v_mfma_f32_16x16x32_bf16 a[248:251],v[216:219],v[252:255],a[248:251]\n"
"ds_read_b64_tr_b16 v[186:187],v104 offset:1024\n"
"s_waitcnt lgkmcnt(10)\n"
"v_mfma_f32_16x16x32_bf16 a[192:195],v[152:155],v[160:163],a[192:195]\n"
"ds_read_b64_tr_b16 v[188:189],v105 offset:0\n"
"v_mfma_f32_16x16x32_bf16 a[196:199],v[156:159],v[160:163],a[196:199]\n"
"ds_read_b64_tr_b16 v[190:191],v105 offset:1024\n"
"v_mfma_f32_16x16x32_bf16 a[204:207],v[156:159],v[164:167],a[204:207]\n"
"ds_read_b128 v[128:131],v80 offset:0\n"
"v_mfma_f32_16x16x32_bf16 a[200:203],v[152:155],v[164:167],a[200:203]\n"
"ds_read_b128 v[192:195],v81 offset:0\n"
"s_waitcnt lgkmcnt(10)\n"
"v_mfma_f32_16x16x32_bf16 a[208:211],v[152:155],v[168:171],a[208:211]\n"
"ds_read_b64_tr_b16 v[224:225],v98 offset:16384\n"
"v_mfma_f32_16x16x32_bf16 a[212:215],v[156:159],v[168:171],a[212:215]\n"
"ds_read_b64_tr_b16 v[226:227],v98 offset:17408\n"
"v_mfma_f32_16x16x32_bf16 a[220:223],v[156:159],v[172:175],a[220:223]\n"
"ds_read_b64_tr_b16 v[228:229],v99 offset:16384\n"
"v_mfma_f32_16x16x32_bf16 a[216:219],v[152:155],v[172:175],a[216:219]\n"
"ds_read_b64_tr_b16 v[230:231],v99 offset:17408\n"
"s_waitcnt lgkmcnt(10)\n"
"v_mfma_f32_16x16x32_bf16 a[224:227],v[152:155],v[176:179],a[224:227]\n"
"ds_read_b64_tr_b16 v[232:233],v100 offset:16384\n"
"v_mfma_f32_16x16x32_bf16 a[228:231],v[156:159],v[176:179],a[228:231]\n"
"ds_read_b64_tr_b16 v[234:235],v100 offset:17408\n"
"v_mfma_f32_16x16x32_bf16 a[236:239],v[156:159],v[180:183],a[236:239]\n"
"ds_read_b64_tr_b16 v[236:237],v101 offset:16384\n"
"v_mfma_f32_16x16x32_bf16 a[232:235],v[152:155],v[180:183],a[232:235]\n"
"ds_read_b64_tr_b16 v[238:239],v101 offset:17408\n"
"s_waitcnt lgkmcnt(10)\n"
"v_mfma_f32_16x16x32_bf16 a[240:243],v[152:155],v[184:187],a[240:243]\n"
"ds_read_b64_tr_b16 v[240:241],v102 offset:16384\n"
"v_mfma_f32_16x16x32_bf16 a[244:247],v[156:159],v[184:187],a[244:247]\n"
"ds_read_b64_tr_b16 v[242:243],v102 offset:17408\n"
"v_mfma_f32_16x16x32_bf16 a[252:255],v[156:159],v[188:191],a[252:255]\n"
"ds_read_b64_tr_b16 v[244:245],v103 offset:16384\n"
"v_mfma_f32_16x16x32_bf16 a[248:251],v[152:155],v[188:191],a[248:251]\n"
"ds_read_b64_tr_b16 v[246:247],v103 offset:17408\n"
"s_waitcnt lgkmcnt(8)\n"
"v_mfma_f32_16x16x32_bf16 a[0:3],v[128:131],v[160:163],a[0:3]\n"
"ds_read_b64_tr_b16 v[248:249],v104 offset:16384\n"
"v_mfma_f32_16x16x32_bf16 a[0:3],v[192:195],v[224:227],a[0:3]\n"
"ds_read_b64_tr_b16 v[250:251],v104 offset:17408\n"
"v_mfma_f32_16x16x32_bf16 a[8:11],v[192:195],v[228:231],a[8:11]\n"
"ds_read_b64_tr_b16 v[252:253],v105 offset:16384\n"
"v_mfma_f32_16x16x32_bf16 a[8:11],v[128:131],v[164:167],a[8:11]\n"
"ds_read_b64_tr_b16 v[254:255],v105 offset:17408\n"
"s_waitcnt lgkmcnt(8)\n"
"v_mfma_f32_16x16x32_bf16 a[16:19],v[128:131],v[168:171],a[16:19]\n"
"ds_read_b128 v[132:135],v80 offset:2048\n"
"v_mfma_f32_16x16x32_bf16 a[16:19],v[192:195],v[232:235],a[16:19]\n"
"ds_read_b128 v[196:199],v81 offset:2048\n"
"v_mfma_f32_16x16x32_bf16 a[24:27],v[192:195],v[236:239],a[24:27]\n"
"ds_read_b128 v[136:139],v80 offset:4096\n"
"v_mfma_f32_16x16x32_bf16 a[24:27],v[128:131],v[172:175],a[24:27]\n"
"ds_read_b128 v[200:203],v81 offset:4096\n"
"s_waitcnt lgkmcnt(8)\n"
"v_mfma_f32_16x16x32_bf16 a[32:35],v[128:131],v[176:179],a[32:35]\n"
"ds_read_b128 v[140:143],v80 offset:6144\n"
"v_mfma_f32_16x16x32_bf16 a[32:35],v[192:195],v[240:243],a[32:35]\n"
"ds_read_b128 v[204:207],v81 offset:6144\n"
"v_mfma_f32_16x16x32_bf16 a[40:43],v[192:195],v[244:247],a[40:43]\n"
"ds_read_b128 v[144:147],v80 offset:8192\n"
"v_mfma_f32_16x16x32_bf16 a[40:43],v[128:131],v[180:183],a[40:43]\n"
"ds_read_b128 v[208:211],v81 offset:8192\n"
"s_waitcnt lgkmcnt(8)\n"
"v_mfma_f32_16x16x32_bf16 a[48:51],v[128:131],v[184:187],a[48:51]\n"
"ds_read_b128 v[148:151],v80 offset:10240\n"
"v_mfma_f32_16x16x32_bf16 a[48:51],v[192:195],v[248:251],a[48:51]\n"
"ds_read_b128 v[212:215],v81 offset:10240\n"
"v_mfma_f32_16x16x32_bf16 a[56:59],v[192:195],v[252:255],a[56:59]\n"
"ds_read_b128 v[216:219],v81 offset:12288\n"
"v_mfma_f32_16x16x32_bf16 a[56:59],v[128:131],v[188:191],a[56:59]\n"
"ds_read_b128 v[220:223],v81 offset:14336\n"
"s_waitcnt lgkmcnt(10)\n"
"v_mfma_f32_16x16x32_bf16 a[4:7],v[132:135],v[160:163],a[4:7]\n"
"v_mfma_f32_16x16x32_bf16 a[4:7],v[196:199],v[224:227],a[4:7]\n"
"v_mfma_f32_16x16x32_bf16 a[12:15],v[196:199],v[228:231],a[12:15]\n"
"v_mfma_f32_16x16x32_bf16 a[12:15],v[132:135],v[164:167],a[12:15]\n"
"v_mfma_f32_16x16x32_bf16 a[20:23],v[132:135],v[168:171],a[20:23]\n"
"s_waitcnt lgkmcnt(0)\n"
"s_barrier\n"
"v_mfma_f32_16x16x32_bf16 a[20:23],v[196:199],v[232:235],a[20:23]\n"
"s_mov_b32 s77,2621440\n"
"s_add_u32 m0,s78,98304\n"
"v_mfma_f32_16x16x32_bf16 a[28:31],v[196:199],v[236:239],a[28:31]\n"
"buffer_load_dwordx4 v110,s[20:23],s77 offen offset:0 lds\n"
"v_mfma_f32_16x16x32_bf16 a[28:31],v[132:135],v[172:175],a[28:31]\n"
"v_mfma_f32_16x16x32_bf16 a[36:39],v[132:135],v[176:179],a[36:39]\n"
"v_mfma_f32_16x16x32_bf16 a[36:39],v[196:199],v[240:243],a[36:39]\n"
"v_mfma_f32_16x16x32_bf16 a[44:47],v[196:199],v[244:247],a[44:47]\n"
"buffer_load_dwordx4 v111,s[20:23],s77 offen offset:1024 lds\n"
"v_mfma_f32_16x16x32_bf16 a[44:47],v[132:135],v[180:183],a[44:47]\n"
"v_mfma_f32_16x16x32_bf16 a[52:55],v[132:135],v[184:187],a[52:55]\n"
"v_mfma_f32_16x16x32_bf16 a[52:55],v[196:199],v[248:251],a[52:55]\n"
"v_mfma_f32_16x16x32_bf16 a[60:63],v[196:199],v[252:255],a[60:63]\n"
"buffer_load_dwordx4 v113,s[20:23],s77 offen offset:2048 lds\n"
"v_mfma_f32_16x16x32_bf16 a[60:63],v[132:135],v[188:191],a[60:63]\n"
"v_mfma_f32_16x16x32_bf16 a[64:67],v[136:139],v[160:163],a[64:67]\n"
"v_mfma_f32_16x16x32_bf16 a[64:67],v[200:203],v[224:227],a[64:67]\n"
"v_mfma_f32_16x16x32_bf16 a[72:75],v[200:203],v[228:231],a[72:75]\n"
"buffer_load_dwordx4 v114,s[20:23],s77 offen offset:3072 lds\n"
"v_mfma_f32_16x16x32_bf16 a[72:75],v[136:139],v[164:167],a[72:75]\n"
"v_mfma_f32_16x16x32_bf16 a[80:83],v[136:139],v[168:171],a[80:83]\n"
"v_mfma_f32_16x16x32_bf16 a[80:83],v[200:203],v[232:235],a[80:83]\n"
"s_mov_b32 s77,2883584\n"
"s_add_u32 m0,s78,114688\n"
"v_mfma_f32_16x16x32_bf16 a[88:91],v[200:203],v[236:239],a[88:91]\n"
"buffer_load_dwordx4 v110,s[20:23],s77 offen offset:0 lds\n"
"v_mfma_f32_16x16x32_bf16 a[88:91],v[136:139],v[172:175],a[88:91]\n"
"v_mfma_f32_16x16x32_bf16 a[96:99],v[136:139],v[176:179],a[96:99]\n"
"v_mfma_f32_16x16x32_bf16 a[96:99],v[200:203],v[240:243],a[96:99]\n"
"v_mfma_f32_16x16x32_bf16 a[104:107],v[200:203],v[244:247],a[104:107]\n"
"buffer_load_dwordx4 v111,s[20:23],s77 offen offset:1024 lds\n"
"v_mfma_f32_16x16x32_bf16 a[104:107],v[136:139],v[180:183],a[104:107]\n"
"v_mfma_f32_16x16x32_bf16 a[112:115],v[136:139],v[184:187],a[112:115]\n"
"v_mfma_f32_16x16x32_bf16 a[112:115],v[200:203],v[248:251],a[112:115]\n"
"v_mfma_f32_16x16x32_bf16 a[120:123],v[200:203],v[252:255],a[120:123]\n"
"buffer_load_dwordx4 v113,s[20:23],s77 offen offset:2048 lds\n"
"v_mfma_f32_16x16x32_bf16 a[120:123],v[136:139],v[188:191],a[120:123]\n"
"v_mfma_f32_16x16x32_bf16 a[68:71],v[140:143],v[160:163],a[68:71]\n"
"v_mfma_f32_16x16x32_bf16 a[68:71],v[204:207],v[224:227],a[68:71]\n"
"v_mfma_f32_16x16x32_bf16 a[76:79],v[204:207],v[228:231],a[76:79]\n"
"buffer_load_dwordx4 v114,s[20:23],s77 offen offset:3072 lds\n"
"v_mfma_f32_16x16x32_bf16 a[76:79],v[140:143],v[164:167],a[76:79]\n"
"v_mfma_f32_16x16x32_bf16 a[84:87],v[140:143],v[168:171],a[84:87]\n"
"v_mfma_f32_16x16x32_bf16 a[84:87],v[204:207],v[232:235],a[84:87]\n"
"v_mfma_f32_16x16x32_bf16 a[92:95],v[204:207],v[236:239],a[92:95]\n"
"s_mov_b32 s76,768\n"
"s_add_u32 m0,s79,0\n"
"v_mfma_f32_16x16x32_bf16 a[92:95],v[140:143],v[172:175],a[92:95]\n"
"buffer_load_dwordx4 v106,s[16:19],s76 offen offset:0 lds\n"
"v_mfma_f32_16x16x32_bf16 a[100:103],v[140:143],v[176:179],a[100:103]\n"
"v_mfma_f32_16x16x32_bf16 a[100:103],v[204:207],v[240:243],a[100:103]\n"
"v_mfma_f32_16x16x32_bf16 a[108:111],v[204:207],v[244:247],a[108:111]\n"
"v_mfma_f32_16x16x32_bf16 a[108:111],v[140:143],v[180:183],a[108:111]\n"
"v_mfma_f32_16x16x32_bf16 a[116:119],v[140:143],v[184:187],a[116:119]\n"
"buffer_load_dwordx4 v107,s[16:19],s76 offen offset:1024 lds\n"
"v_mfma_f32_16x16x32_bf16 a[116:119],v[204:207],v[248:251],a[116:119]\n"
"v_mfma_f32_16x16x32_bf16 a[124:127],v[204:207],v[252:255],a[124:127]\n"
"v_mfma_f32_16x16x32_bf16 a[124:127],v[140:143],v[188:191],a[124:127]\n"
"v_mfma_f32_16x16x32_bf16 a[128:131],v[144:147],v[160:163],a[128:131]\n"
"v_mfma_f32_16x16x32_bf16 a[128:131],v[208:211],v[224:227],a[128:131]\n"
"buffer_load_dwordx4 v108,s[16:19],s76 offen offset:2048 lds\n"
"v_mfma_f32_16x16x32_bf16 a[136:139],v[208:211],v[228:231],a[136:139]\n"
"v_mfma_f32_16x16x32_bf16 a[136:139],v[144:147],v[164:167],a[136:139]\n"
"v_mfma_f32_16x16x32_bf16 a[144:147],v[144:147],v[168:171],a[144:147]\n"
"v_mfma_f32_16x16x32_bf16 a[144:147],v[208:211],v[232:235],a[144:147]\n"
"v_mfma_f32_16x16x32_bf16 a[152:155],v[208:211],v[236:239],a[152:155]\n"
"buffer_load_dwordx4 v109,s[16:19],s76 offen offset:3072 lds\n"
"v_mfma_f32_16x16x32_bf16 a[152:155],v[144:147],v[172:175],a[152:155]\n"
"v_mfma_f32_16x16x32_bf16 a[160:163],v[144:147],v[176:179],a[160:163]\n"
"v_mfma_f32_16x16x32_bf16 a[160:163],v[208:211],v[240:243],a[160:163]\n"
"v_mfma_f32_16x16x32_bf16 a[168:171],v[208:211],v[244:247],a[168:171]\n"
"s_mov_b32 s76,262912\n"
"s_add_u32 m0,s79,4096\n"
"v_mfma_f32_16x16x32_bf16 a[168:171],v[144:147],v[180:183],a[168:171]\n"
"buffer_load_dwordx4 v106,s[16:19],s76 offen offset:0 lds\n"
"v_mfma_f32_16x16x32_bf16 a[176:179],v[144:147],v[184:187],a[176:179]\n"
"v_mfma_f32_16x16x32_bf16 a[176:179],v[208:211],v[248:251],a[176:179]\n"
"v_mfma_f32_16x16x32_bf16 a[184:187],v[208:211],v[252:255],a[184:187]\n"
"v_mfma_f32_16x16x32_bf16 a[184:187],v[144:147],v[188:191],a[184:187]\n"
"v_mfma_f32_16x16x32_bf16 a[132:135],v[148:151],v[160:163],a[132:135]\n"
"buffer_load_dwordx4 v107,s[16:19],s76 offen offset:1024 lds\n"
"v_mfma_f32_16x16x32_bf16 a[132:135],v[212:215],v[224:227],a[132:135]\n"
"v_mfma_f32_16x16x32_bf16 a[140:143],v[212:215],v[228:231],a[140:143]\n"
"v_mfma_f32_16x16x32_bf16 a[140:143],v[148:151],v[164:167],a[140:143]\n"
"v_mfma_f32_16x16x32_bf16 a[148:151],v[148:151],v[168:171],a[148:151]\n"
"v_mfma_f32_16x16x32_bf16 a[148:151],v[212:215],v[232:235],a[148:151]\n"
"buffer_load_dwordx4 v108,s[16:19],s76 offen offset:2048 lds\n"
"v_mfma_f32_16x16x32_bf16 a[156:159],v[212:215],v[236:239],a[156:159]\n"
"v_mfma_f32_16x16x32_bf16 a[156:159],v[148:151],v[172:175],a[156:159]\n"
"v_mfma_f32_16x16x32_bf16 a[164:167],v[148:151],v[176:179],a[164:167]\n"
"v_mfma_f32_16x16x32_bf16 a[164:167],v[212:215],v[240:243],a[164:167]\n"
"v_mfma_f32_16x16x32_bf16 a[172:175],v[212:215],v[244:247],a[172:175]\n"
"buffer_load_dwordx4 v109,s[16:19],s76 offen offset:3072 lds\n"
"v_mfma_f32_16x16x32_bf16 a[172:175],v[148:151],v[180:183],a[172:175]\n"
"s_mov_b32 s87,3145728\n"
"v_mfma_f32_16x16x32_bf16 a[180:183],v[148:151],v[184:187],a[180:183]\n"
"buffer_load_dword v115,v112,s[40:43],s87 offen\n"
"v_mfma_f32_16x16x32_bf16 a[180:183],v[212:215],v[248:251],a[180:183]\n"
"s_mov_b32 s87,4194304\n"
"v_mfma_f32_16x16x32_bf16 a[188:191],v[212:215],v[252:255],a[188:191]\n"
"buffer_load_dword v115,v112,s[40:43],s87 offen\n"
"v_mfma_f32_16x16x32_bf16 a[188:191],v[148:151],v[188:191],a[188:191]\n"
"s_mov_b32 s87,5242880\n"
"s_waitcnt vmcnt(33)\n"
"s_barrier\n"
"v_mfma_f32_16x16x32_bf16 a[192:195],v[216:219],v[224:227],a[192:195]\n"
"ds_read_b128 v[152:155],v80 offset:45056\n"
"buffer_load_dword v115,v112,s[40:43],s87 offen\n"
"v_mfma_f32_16x16x32_bf16 a[196:199],v[220:223],v[224:227],a[196:199]\n"
"ds_read_b128 v[156:159],v80 offset:47104\n"
"v_mfma_f32_16x16x32_bf16 a[204:207],v[220:223],v[228:231],a[204:207]\n"
"ds_read_b64_tr_b16 v[160:161],v98 offset:49152\n"
"v_mfma_f32_16x16x32_bf16 a[200:203],v[216:219],v[228:231],a[200:203]\n"
"ds_read_b64_tr_b16 v[162:163],v98 offset:50176\n"
"v_mfma_f32_16x16x32_bf16 a[208:211],v[216:219],v[232:235],a[208:211]\n"
"ds_read_b64_tr_b16 v[164:165],v99 offset:49152\n"
"v_mfma_f32_16x16x32_bf16 a[212:215],v[220:223],v[232:235],a[212:215]\n"
"ds_read_b64_tr_b16 v[166:167],v99 offset:50176\n"
"v_mfma_f32_16x16x32_bf16 a[220:223],v[220:223],v[236:239],a[220:223]\n"
"ds_read_b64_tr_b16 v[168:169],v100 offset:49152\n"
"v_mfma_f32_16x16x32_bf16 a[216:219],v[216:219],v[236:239],a[216:219]\n"
"ds_read_b64_tr_b16 v[170:171],v100 offset:50176\n"
"v_mfma_f32_16x16x32_bf16 a[224:227],v[216:219],v[240:243],a[224:227]\n"
"ds_read_b64_tr_b16 v[172:173],v101 offset:49152\n"
"v_mfma_f32_16x16x32_bf16 a[228:231],v[220:223],v[240:243],a[228:231]\n"
"ds_read_b64_tr_b16 v[174:175],v101 offset:50176\n"
"v_mfma_f32_16x16x32_bf16 a[236:239],v[220:223],v[244:247],a[236:239]\n"
"ds_read_b64_tr_b16 v[176:177],v102 offset:49152\n"
"v_mfma_f32_16x16x32_bf16 a[232:235],v[216:219],v[244:247],a[232:235]\n"
"ds_read_b64_tr_b16 v[178:179],v102 offset:50176\n"
"v_mfma_f32_16x16x32_bf16 a[240:243],v[216:219],v[248:251],a[240:243]\n"
"ds_read_b64_tr_b16 v[180:181],v103 offset:49152\n"
"v_mfma_f32_16x16x32_bf16 a[244:247],v[220:223],v[248:251],a[244:247]\n"
"ds_read_b64_tr_b16 v[182:183],v103 offset:50176\n"
"v_mfma_f32_16x16x32_bf16 a[252:255],v[220:223],v[252:255],a[252:255]\n"
"ds_read_b64_tr_b16 v[184:185],v104 offset:49152\n"
"ds_read_b64_tr_b16 v[186:187],v104 offset:50176\n"
"v_mfma_f32_16x16x32_bf16 a[248:251],v[216:219],v[252:255],a[248:251]\n"
"s_waitcnt vmcnt(19)\n"
"s_waitcnt lgkmcnt(10)\n"
"v_mfma_f32_16x16x32_bf16 a[192:195],v[152:155],v[160:163],a[192:195]\n"
"ds_read_b64_tr_b16 v[188:189],v105 offset:49152\n"
"ds_read_b64_tr_b16 v[190:191],v105 offset:50176\n"
"v_mfma_f32_16x16x32_bf16 a[196:199],v[156:159],v[160:163],a[196:199]\n"
"buffer_store_dwordx2 v[124:125],off,s[24:27],s57 sc1\n"
"v_mfma_f32_16x16x32_bf16 a[204:207],v[156:159],v[164:167],a[204:207]\n"
"ds_read_b128 v[128:131],v80 offset:32768\n"
"v_mfma_f32_16x16x32_bf16 a[200:203],v[152:155],v[164:167],a[200:203]\n"
"ds_read_b128 v[192:195],v81 offset:32768\n"
"s_waitcnt lgkmcnt(10)\n"
"v_mfma_f32_16x16x32_bf16 a[208:211],v[152:155],v[168:171],a[208:211]\n"
"ds_read_b64_tr_b16 v[224:225],v98 offset:32768\n"
"v_mfma_f32_16x16x32_bf16 a[212:215],v[156:159],v[168:171],a[212:215]\n"
"ds_read_b64_tr_b16 v[226:227],v98 offset:33792\n"
"v_mfma_f32_16x16x32_bf16 a[220:223],v[156:159],v[172:175],a[220:223]\n"
"ds_read_b64_tr_b16 v[228:229],v99 offset:32768\n"
"v_mfma_f32_16x16x32_bf16 a[216:219],v[152:155],v[172:175],a[216:219]\n"
"ds_read_b64_tr_b16 v[230:231],v99 offset:33792\n"
"s_waitcnt lgkmcnt(10)\n"
"v_mfma_f32_16x16x32_bf16 a[224:227],v[152:155],v[176:179],a[224:227]\n"
"ds_read_b64_tr_b16 v[232:233],v100 offset:32768\n"
"v_mfma_f32_16x16x32_bf16 a[228:231],v[156:159],v[176:179],a[228:231]\n"
"ds_read_b64_tr_b16 v[234:235],v100 offset:33792\n"
"v_mfma_f32_16x16x32_bf16 a[236:239],v[156:159],v[180:183],a[236:239]\n"
"ds_read_b64_tr_b16 v[236:237],v101 offset:32768\n"
"v_mfma_f32_16x16x32_bf16 a[232:235],v[152:155],v[180:183],a[232:235]\n"
"ds_read_b64_tr_b16 v[238:239],v101 offset:33792\n"
"s_waitcnt lgkmcnt(10)\n"
"v_mfma_f32_16x16x32_bf16 a[240:243],v[152:155],v[184:187],a[240:243]\n"
"ds_read_b64_tr_b16 v[240:241],v102 offset:32768\n"
"v_mfma_f32_16x16x32_bf16 a[244:247],v[156:159],v[184:187],a[244:247]\n"
"ds_read_b64_tr_b16 v[242:243],v102 offset:33792\n"
"v_mfma_f32_16x16x32_bf16 a[252:255],v[156:159],v[188:191],a[252:255]\n"
"ds_read_b64_tr_b16 v[244:245],v103 offset:32768\n"
"v_mfma_f32_16x16x32_bf16 a[248:251],v[152:155],v[188:191],a[248:251]\n"
"ds_read_b64_tr_b16 v[246:247],v103 offset:33792\n"
"s_waitcnt lgkmcnt(8)\n"
"v_mfma_f32_16x16x32_bf16 a[0:3],v[128:131],v[160:163],a[0:3]\n"
"ds_read_b64_tr_b16 v[248:249],v104 offset:32768\n"
"v_mfma_f32_16x16x32_bf16 a[0:3],v[192:195],v[224:227],a[0:3]\n"
"ds_read_b64_tr_b16 v[250:251],v104 offset:33792\n"
"v_mfma_f32_16x16x32_bf16 a[8:11],v[192:195],v[228:231],a[8:11]\n"
"ds_read_b64_tr_b16 v[252:253],v105 offset:32768\n"
"v_mfma_f32_16x16x32_bf16 a[8:11],v[128:131],v[164:167],a[8:11]\n"
"ds_read_b64_tr_b16 v[254:255],v105 offset:33792\n"
"s_waitcnt lgkmcnt(8)\n"
"v_mfma_f32_16x16x32_bf16 a[16:19],v[128:131],v[168:171],a[16:19]\n"
"ds_read_b128 v[132:135],v80 offset:34816\n"
"v_mfma_f32_16x16x32_bf16 a[16:19],v[192:195],v[232:235],a[16:19]\n"
"ds_read_b128 v[196:199],v81 offset:34816\n"
"v_mfma_f32_16x16x32_bf16 a[24:27],v[192:195],v[236:239],a[24:27]\n"
"ds_read_b128 v[136:139],v80 offset:36864\n"
"v_mfma_f32_16x16x32_bf16 a[24:27],v[128:131],v[172:175],a[24:27]\n"
"ds_read_b128 v[200:203],v81 offset:36864\n"
"s_waitcnt lgkmcnt(8)\n"
"v_mfma_f32_16x16x32_bf16 a[32:35],v[128:131],v[176:179],a[32:35]\n"
"ds_read_b128 v[140:143],v80 offset:38912\n"
"v_mfma_f32_16x16x32_bf16 a[32:35],v[192:195],v[240:243],a[32:35]\n"
"ds_read_b128 v[204:207],v81 offset:38912\n"
"v_mfma_f32_16x16x32_bf16 a[40:43],v[192:195],v[244:247],a[40:43]\n"
"ds_read_b128 v[144:147],v80 offset:40960\n"
"v_mfma_f32_16x16x32_bf16 a[40:43],v[128:131],v[180:183],a[40:43]\n"
"ds_read_b128 v[208:211],v81 offset:40960\n"
"s_waitcnt lgkmcnt(8)\n"
"v_mfma_f32_16x16x32_bf16 a[48:51],v[128:131],v[184:187],a[48:51]\n"
"ds_read_b128 v[148:151],v80 offset:43008\n"
"v_mfma_f32_16x16x32_bf16 a[48:51],v[192:195],v[248:251],a[48:51]\n"
"ds_read_b128 v[212:215],v81 offset:43008\n"
"v_mfma_f32_16x16x32_bf16 a[56:59],v[192:195],v[252:255],a[56:59]\n"
"ds_read_b128 v[216:219],v81 offset:45056\n"
"v_mfma_f32_16x16x32_bf16 a[56:59],v[128:131],v[188:191],a[56:59]\n"
"ds_read_b128 v[220:223],v81 offset:47104\n"
"s_waitcnt lgkmcnt(10)\n"
"v_mfma_f32_16x16x32_bf16 a[4:7],v[132:135],v[160:163],a[4:7]\n"
"v_mfma_f32_16x16x32_bf16 a[4:7],v[196:199],v[224:227],a[4:7]\n"
"v_mfma_f32_16x16x32_bf16 a[12:15],v[196:199],v[228:231],a[12:15]\n"
"v_mfma_f32_16x16x32_bf16 a[12:15],v[132:135],v[164:167],a[12:15]\n"
"v_mfma_f32_16x16x32_bf16 a[20:23],v[132:135],v[168:171],a[20:23]\n"
"s_waitcnt lgkmcnt(0)\n"
"s_barrier\n"
"v_mfma_f32_16x16x32_bf16 a[20:23],v[196:199],v[232:235],a[20:23]\n"
"s_mov_b32 s77,3145728\n"
"s_add_u32 m0,s78,147456\n"
"v_mfma_f32_16x16x32_bf16 a[28:31],v[196:199],v[236:239],a[28:31]\n"
"buffer_load_dwordx4 v110,s[20:23],s77 offen offset:0 lds\n"
"v_mfma_f32_16x16x32_bf16 a[28:31],v[132:135],v[172:175],a[28:31]\n"
"v_mfma_f32_16x16x32_bf16 a[36:39],v[132:135],v[176:179],a[36:39]\n"
"v_mfma_f32_16x16x32_bf16 a[36:39],v[196:199],v[240:243],a[36:39]\n"
"v_mfma_f32_16x16x32_bf16 a[44:47],v[196:199],v[244:247],a[44:47]\n"
"buffer_load_dwordx4 v111,s[20:23],s77 offen offset:1024 lds\n"
"v_mfma_f32_16x16x32_bf16 a[44:47],v[132:135],v[180:183],a[44:47]\n"
"v_mfma_f32_16x16x32_bf16 a[52:55],v[132:135],v[184:187],a[52:55]\n"
"v_mfma_f32_16x16x32_bf16 a[52:55],v[196:199],v[248:251],a[52:55]\n"
"v_mfma_f32_16x16x32_bf16 a[60:63],v[196:199],v[252:255],a[60:63]\n"
"buffer_load_dwordx4 v113,s[20:23],s77 offen offset:2048 lds\n"
"v_mfma_f32_16x16x32_bf16 a[60:63],v[132:135],v[188:191],a[60:63]\n"
"v_mfma_f32_16x16x32_bf16 a[64:67],v[136:139],v[160:163],a[64:67]\n"
"v_mfma_f32_16x16x32_bf16 a[64:67],v[200:203],v[224:227],a[64:67]\n"
"v_mfma_f32_16x16x32_bf16 a[72:75],v[200:203],v[228:231],a[72:75]\n"
"buffer_load_dwordx4 v114,s[20:23],s77 offen offset:3072 lds\n"
"v_mfma_f32_16x16x32_bf16 a[72:75],v[136:139],v[164:167],a[72:75]\n"
"v_mfma_f32_16x16x32_bf16 a[80:83],v[136:139],v[168:171],a[80:83]\n"
"v_mfma_f32_16x16x32_bf16 a[80:83],v[200:203],v[232:235],a[80:83]\n"
"s_mov_b32 s77,3407872\n"
"s_add_u32 m0,s78,131072\n"
"v_mfma_f32_16x16x32_bf16 a[88:91],v[200:203],v[236:239],a[88:91]\n"
"buffer_load_dwordx4 v110,s[20:23],s77 offen offset:0 lds\n"
"v_mfma_f32_16x16x32_bf16 a[88:91],v[136:139],v[172:175],a[88:91]\n"
"v_mfma_f32_16x16x32_bf16 a[96:99],v[136:139],v[176:179],a[96:99]\n"
"v_mfma_f32_16x16x32_bf16 a[96:99],v[200:203],v[240:243],a[96:99]\n"
"v_mfma_f32_16x16x32_bf16 a[104:107],v[200:203],v[244:247],a[104:107]\n"
"buffer_load_dwordx4 v111,s[20:23],s77 offen offset:1024 lds\n"
"v_mfma_f32_16x16x32_bf16 a[104:107],v[136:139],v[180:183],a[104:107]\n"
"v_mfma_f32_16x16x32_bf16 a[112:115],v[136:139],v[184:187],a[112:115]\n"
"v_mfma_f32_16x16x32_bf16 a[112:115],v[200:203],v[248:251],a[112:115]\n"
"v_mfma_f32_16x16x32_bf16 a[120:123],v[200:203],v[252:255],a[120:123]\n"
"buffer_load_dwordx4 v113,s[20:23],s77 offen offset:2048 lds\n"
"v_mfma_f32_16x16x32_bf16 a[120:123],v[136:139],v[188:191],a[120:123]\n"
"v_mfma_f32_16x16x32_bf16 a[68:71],v[140:143],v[160:163],a[68:71]\n"
"v_mfma_f32_16x16x32_bf16 a[68:71],v[204:207],v[224:227],a[68:71]\n"
"v_mfma_f32_16x16x32_bf16 a[76:79],v[204:207],v[228:231],a[76:79]\n"
"buffer_load_dwordx4 v114,s[20:23],s77 offen offset:3072 lds\n"
"v_mfma_f32_16x16x32_bf16 a[76:79],v[140:143],v[164:167],a[76:79]\n"
"v_mfma_f32_16x16x32_bf16 a[84:87],v[140:143],v[168:171],a[84:87]\n"
"v_mfma_f32_16x16x32_bf16 a[84:87],v[204:207],v[232:235],a[84:87]\n"
"v_mfma_f32_16x16x32_bf16 a[92:95],v[204:207],v[236:239],a[92:95]\n"
"s_mov_b32 s76,896\n"
"s_add_u32 m0,s79,32768\n"
"v_mfma_f32_16x16x32_bf16 a[92:95],v[140:143],v[172:175],a[92:95]\n"
"buffer_load_dwordx4 v106,s[16:19],s76 offen offset:0 lds\n"
"v_mfma_f32_16x16x32_bf16 a[100:103],v[140:143],v[176:179],a[100:103]\n"
"v_mfma_f32_16x16x32_bf16 a[100:103],v[204:207],v[240:243],a[100:103]\n"
"v_mfma_f32_16x16x32_bf16 a[108:111],v[204:207],v[244:247],a[108:111]\n"
"v_mfma_f32_16x16x32_bf16 a[108:111],v[140:143],v[180:183],a[108:111]\n"
"v_mfma_f32_16x16x32_bf16 a[116:119],v[140:143],v[184:187],a[116:119]\n"
"buffer_load_dwordx4 v107,s[16:19],s76 offen offset:1024 lds\n"
"v_mfma_f32_16x16x32_bf16 a[116:119],v[204:207],v[248:251],a[116:119]\n"
"v_mfma_f32_16x16x32_bf16 a[124:127],v[204:207],v[252:255],a[124:127]\n"
"v_mfma_f32_16x16x32_bf16 a[124:127],v[140:143],v[188:191],a[124:127]\n"
"v_mfma_f32_16x16x32_bf16 a[128:131],v[144:147],v[160:163],a[128:131]\n"
"v_mfma_f32_16x16x32_bf16 a[128:131],v[208:211],v[224:227],a[128:131]\n"
"buffer_load_dwordx4 v108,s[16:19],s76 offen offset:2048 lds\n"
"v_mfma_f32_16x16x32_bf16 a[136:139],v[208:211],v[228:231],a[136:139]\n"
"v_mfma_f32_16x16x32_bf16 a[136:139],v[144:147],v[164:167],a[136:139]\n"
"v_mfma_f32_16x16x32_bf16 a[144:147],v[144:147],v[168:171],a[144:147]\n"
"v_mfma_f32_16x16x32_bf16 a[144:147],v[208:211],v[232:235],a[144:147]\n"
"v_mfma_f32_16x16x32_bf16 a[152:155],v[208:211],v[236:239],a[152:155]\n"
"buffer_load_dwordx4 v109,s[16:19],s76 offen offset:3072 lds\n"
"v_mfma_f32_16x16x32_bf16 a[152:155],v[144:147],v[172:175],a[152:155]\n"
"v_mfma_f32_16x16x32_bf16 a[160:163],v[144:147],v[176:179],a[160:163]\n"
"v_mfma_f32_16x16x32_bf16 a[160:163],v[208:211],v[240:243],a[160:163]\n"
"v_mfma_f32_16x16x32_bf16 a[168:171],v[208:211],v[244:247],a[168:171]\n"
"s_mov_b32 s76,263040\n"
"s_add_u32 m0,s79,36864\n"
"v_mfma_f32_16x16x32_bf16 a[168:171],v[144:147],v[180:183],a[168:171]\n"
"buffer_load_dwordx4 v106,s[16:19],s76 offen offset:0 lds\n"
"v_mfma_f32_16x16x32_bf16 a[176:179],v[144:147],v[184:187],a[176:179]\n"
"v_mfma_f32_16x16x32_bf16 a[176:179],v[208:211],v[248:251],a[176:179]\n"
"v_mfma_f32_16x16x32_bf16 a[184:187],v[208:211],v[252:255],a[184:187]\n"
"v_mfma_f32_16x16x32_bf16 a[184:187],v[144:147],v[188:191],a[184:187]\n"
"v_mfma_f32_16x16x32_bf16 a[132:135],v[148:151],v[160:163],a[132:135]\n"
"buffer_load_dwordx4 v107,s[16:19],s76 offen offset:1024 lds\n"
"v_mfma_f32_16x16x32_bf16 a[132:135],v[212:215],v[224:227],a[132:135]\n"
"v_mfma_f32_16x16x32_bf16 a[140:143],v[212:215],v[228:231],a[140:143]\n"
"v_mfma_f32_16x16x32_bf16 a[140:143],v[148:151],v[164:167],a[140:143]\n"
"v_mfma_f32_16x16x32_bf16 a[148:151],v[148:151],v[168:171],a[148:151]\n"
"v_mfma_f32_16x16x32_bf16 a[148:151],v[212:215],v[232:235],a[148:151]\n"
"buffer_load_dwordx4 v108,s[16:19],s76 offen offset:2048 lds\n"
"v_mfma_f32_16x16x32_bf16 a[156:159],v[212:215],v[236:239],a[156:159]\n"
"v_mfma_f32_16x16x32_bf16 a[156:159],v[148:151],v[172:175],a[156:159]\n"
"v_mfma_f32_16x16x32_bf16 a[164:167],v[148:151],v[176:179],a[164:167]\n"
"v_mfma_f32_16x16x32_bf16 a[164:167],v[212:215],v[240:243],a[164:167]\n"
"v_mfma_f32_16x16x32_bf16 a[172:175],v[212:215],v[244:247],a[172:175]\n"
"buffer_load_dwordx4 v109,s[16:19],s76 offen offset:3072 lds\n"
"v_mfma_f32_16x16x32_bf16 a[172:175],v[148:151],v[180:183],a[172:175]\n"
"s_mov_b32 s87,6291456\n"
"v_mfma_f32_16x16x32_bf16 a[180:183],v[148:151],v[184:187],a[180:183]\n"
"buffer_load_dword v115,v112,s[40:43],s87 offen\n"
"v_mfma_f32_16x16x32_bf16 a[180:183],v[212:215],v[248:251],a[180:183]\n"
"s_mov_b32 s87,7340032\n"
"v_mfma_f32_16x16x32_bf16 a[188:191],v[212:215],v[252:255],a[188:191]\n"
"buffer_load_dword v115,v112,s[40:43],s87 offen\n"
"v_mfma_f32_16x16x32_bf16 a[188:191],v[148:151],v[188:191],a[188:191]\n"
"s_waitcnt vmcnt(30)\n"
"s_barrier\n"
"v_mfma_f32_16x16x32_bf16 a[192:195],v[216:219],v[224:227],a[192:195]\n"
"ds_read_b128 v[152:155],v82 offset:12288\n"
"v_mfma_f32_16x16x32_bf16 a[196:199],v[220:223],v[224:227],a[196:199]\n"
"ds_read_b128 v[156:159],v82 offset:14336\n"
"v_mfma_f32_16x16x32_bf16 a[204:207],v[220:223],v[228:231],a[204:207]\n"
"ds_read_b64_tr_b16 v[160:161],v98 offset:0\n"
"v_mfma_f32_16x16x32_bf16 a[200:203],v[216:219],v[228:231],a[200:203]\n"
"ds_read_b64_tr_b16 v[162:163],v98 offset:1024\n"
"v_mfma_f32_16x16x32_bf16 a[208:211],v[216:219],v[232:235],a[208:211]\n"
"ds_read_b64_tr_b16 v[164:165],v99 offset:0\n"
"v_mfma_f32_16x16x32_bf16 a[212:215],v[220:223],v[232:235],a[212:215]\n"
"ds_read_b64_tr_b16 v[166:167],v99 offset:1024\n"
"v_mfma_f32_16x16x32_bf16 a[220:223],v[220:223],v[236:239],a[220:223]\n"
"ds_read_b64_tr_b16 v[168:169],v100 offset:0\n"
"v_mfma_f32_16x16x32_bf16 a[216:219],v[216:219],v[236:239],a[216:219]\n"
"ds_read_b64_tr_b16 v[170:171],v100 offset:1024\n"
"v_mfma_f32_16x16x32_bf16 a[224:227],v[216:219],v[240:243],a[224:227]\n"
"ds_read_b64_tr_b16 v[172:173],v101 offset:0\n"
"v_mfma_f32_16x16x32_bf16 a[228:231],v[220:223],v[240:243],a[228:231]\n"
"ds_read_b64_tr_b16 v[174:175],v101 offset:1024\n"
"v_mfma_f32_16x16x32_bf16 a[236:239],v[220:223],v[244:247],a[236:239]\n"
"ds_read_b64_tr_b16 v[176:177],v102 offset:0\n"
"v_mfma_f32_16x16x32_bf16 a[232:235],v[216:219],v[244:247],a[232:235]\n"
"ds_read_b64_tr_b16 v[178:179],v102 offset:1024\n"
"v_mfma_f32_16x16x32_bf16 a[240:243],v[216:219],v[248:251],a[240:243]\n"
"ds_read_b64_tr_b16 v[180:181],v103 offset:0\n"
"v_mfma_f32_16x16x32_bf16 a[244:247],v[220:223],v[248:251],a[244:247]\n"
"ds_read_b64_tr_b16 v[182:183],v103 offset:1024\n"
"v_mfma_f32_16x16x32_bf16 a[252:255],v[220:223],v[252:255],a[252:255]\n"
"ds_read_b64_tr_b16 v[184:185],v104 offset:0\n"
"v_mfma_f32_16x16x32_bf16 a[248:251],v[216:219],v[252:255],a[248:251]\n"
"ds_read_b64_tr_b16 v[186:187],v104 offset:1024\n"
"s_waitcnt lgkmcnt(10)\n"
"v_mfma_f32_16x16x32_bf16 a[192:195],v[152:155],v[160:163],a[192:195]\n"
"ds_read_b64_tr_b16 v[188:189],v105 offset:0\n"
"v_mfma_f32_16x16x32_bf16 a[196:199],v[156:159],v[160:163],a[196:199]\n"
"ds_read_b64_tr_b16 v[190:191],v105 offset:1024\n"
"v_mfma_f32_16x16x32_bf16 a[204:207],v[156:159],v[164:167],a[204:207]\n"
"ds_read_b128 v[128:131],v82 offset:0\n"
"v_mfma_f32_16x16x32_bf16 a[200:203],v[152:155],v[164:167],a[200:203]\n"
"ds_read_b128 v[192:195],v83 offset:0\n"
"s_waitcnt lgkmcnt(10)\n"
"v_mfma_f32_16x16x32_bf16 a[208:211],v[152:155],v[168:171],a[208:211]\n"
"ds_read_b64_tr_b16 v[224:225],v98 offset:16384\n"
"v_mfma_f32_16x16x32_bf16 a[212:215],v[156:159],v[168:171],a[212:215]\n"
"ds_read_b64_tr_b16 v[226:227],v98 offset:17408\n"
"v_mfma_f32_16x16x32_bf16 a[220:223],v[156:159],v[172:175],a[220:223]\n"
"ds_read_b64_tr_b16 v[228:229],v99 offset:16384\n"
"v_mfma_f32_16x16x32_bf16 a[216:219],v[152:155],v[172:175],a[216:219]\n"
"ds_read_b64_tr_b16 v[230:231],v99 offset:17408\n"
"s_waitcnt lgkmcnt(10)\n"
"v_mfma_f32_16x16x32_bf16 a[224:227],v[152:155],v[176:179],a[224:227]\n"
"ds_read_b64_tr_b16 v[232:233],v100 offset:16384\n"
"v_mfma_f32_16x16x32_bf16 a[228:231],v[156:159],v[176:179],a[228:231]\n"
"ds_read_b64_tr_b16 v[234:235],v100 offset:17408\n"
"v_mfma_f32_16x16x32_bf16 a[236:239],v[156:159],v[180:183],a[236:239]\n"
"ds_read_b64_tr_b16 v[236:237],v101 offset:16384\n"
"v_mfma_f32_16x16x32_bf16 a[232:235],v[152:155],v[180:183],a[232:235]\n"
"ds_read_b64_tr_b16 v[238:239],v101 offset:17408\n"
"s_waitcnt lgkmcnt(10)\n"
"v_mfma_f32_16x16x32_bf16 a[240:243],v[152:155],v[184:187],a[240:243]\n"
"ds_read_b64_tr_b16 v[240:241],v102 offset:16384\n"
"v_mfma_f32_16x16x32_bf16 a[244:247],v[156:159],v[184:187],a[244:247]\n"
"ds_read_b64_tr_b16 v[242:243],v102 offset:17408\n"
"v_mfma_f32_16x16x32_bf16 a[252:255],v[156:159],v[188:191],a[252:255]\n"
"ds_read_b64_tr_b16 v[244:245],v103 offset:16384\n"
"v_mfma_f32_16x16x32_bf16 a[248:251],v[152:155],v[188:191],a[248:251]\n"
"ds_read_b64_tr_b16 v[246:247],v103 offset:17408\n"
"s_waitcnt lgkmcnt(8)\n"
"v_mfma_f32_16x16x32_bf16 a[0:3],v[128:131],v[160:163],a[0:3]\n"
"ds_read_b64_tr_b16 v[248:249],v104 offset:16384\n"
"v_mfma_f32_16x16x32_bf16 a[0:3],v[192:195],v[224:227],a[0:3]\n"
"ds_read_b64_tr_b16 v[250:251],v104 offset:17408\n"
"v_mfma_f32_16x16x32_bf16 a[8:11],v[192:195],v[228:231],a[8:11]\n"
"ds_read_b64_tr_b16 v[252:253],v105 offset:16384\n"
"v_mfma_f32_16x16x32_bf16 a[8:11],v[128:131],v[164:167],a[8:11]\n"
"ds_read_b64_tr_b16 v[254:255],v105 offset:17408\n"
"s_waitcnt lgkmcnt(8)\n"
"v_mfma_f32_16x16x32_bf16 a[16:19],v[128:131],v[168:171],a[16:19]\n"
"ds_read_b128 v[132:135],v82 offset:2048\n"
"v_mfma_f32_16x16x32_bf16 a[16:19],v[192:195],v[232:235],a[16:19]\n"
"ds_read_b128 v[196:199],v83 offset:2048\n"
"v_mfma_f32_16x16x32_bf16 a[24:27],v[192:195],v[236:239],a[24:27]\n"
"ds_read_b128 v[136:139],v82 offset:4096\n"
"v_mfma_f32_16x16x32_bf16 a[24:27],v[128:131],v[172:175],a[24:27]\n"
"ds_read_b128 v[200:203],v83 offset:4096\n"
"s_waitcnt lgkmcnt(8)\n"
"v_mfma_f32_16x16x32_bf16 a[32:35],v[128:131],v[176:179],a[32:35]\n"
"ds_read_b128 v[140:143],v82 offset:6144\n"
"v_mfma_f32_16x16x32_bf16 a[32:35],v[192:195],v[240:243],a[32:35]\n"
"ds_read_b128 v[204:207],v83 offset:6144\n"
"v_mfma_f32_16x16x32_bf16 a[40:43],v[192:195],v[244:247],a[40:43]\n"
"ds_read_b128 v[144:147],v82 offset:8192\n"
"v_mfma_f32_16x16x32_bf16 a[40:43],v[128:131],v[180:183],a[40:43]\n"
"ds_read_b128 v[208:211],v83 offset:8192\n"
"s_waitcnt lgkmcnt(8)\n"
"v_mfma_f32_16x16x32_bf16 a[48:51],v[128:131],v[184:187],a[48:51]\n"
"ds_read_b128 v[148:151],v82 offset:10240\n"
"v_mfma_f32_16x16x32_bf16 a[48:51],v[192:195],v[248:251],a[48:51]\n"
"ds_read_b128 v[212:215],v83 offset:10240\n"
"v_mfma_f32_16x16x32_bf16 a[56:59],v[192:195],v[252:255],a[56:59]\n"
"ds_read_b128 v[216:219],v83 offset:12288\n"
"v_mfma_f32_16x16x32_bf16 a[56:59],v[128:131],v[188:191],a[56:59]\n"
"ds_read_b128 v[220:223],v83 offset:14336\n"
"s_waitcnt lgkmcnt(10)\n"
"v_mfma_f32_16x16x32_bf16 a[4:7],v[132:135],v[160:163],a[4:7]\n"
"v_mfma_f32_16x16x32_bf16 a[4:7],v[196:199],v[224:227],a[4:7]\n"
"buffer_load_dwordx4 v[74:77],v112,s[24:27],s58 offen sc1\n"
"v_mfma_f32_16x16x32_bf16 a[12:15],v[196:199],v[228:231],a[12:15]\n"
"v_mfma_f32_16x16x32_bf16 a[12:15],v[132:135],v[164:167],a[12:15]\n"
"v_mfma_f32_16x16x32_bf16 a[20:23],v[132:135],v[168:171],a[20:23]\n"
"s_waitcnt lgkmcnt(0)\n"
"s_barrier\n"
"v_mfma_f32_16x16x32_bf16 a[20:23],v[196:199],v[232:235],a[20:23]\n"
"s_mov_b32 s77,3670016\n"
"s_add_u32 m0,s78,98304\n"
"v_mfma_f32_16x16x32_bf16 a[28:31],v[196:199],v[236:239],a[28:31]\n"
"buffer_load_dwordx4 v110,s[20:23],s77 offen offset:0 lds\n"
"v_mfma_f32_16x16x32_bf16 a[28:31],v[132:135],v[172:175],a[28:31]\n"
"v_mfma_f32_16x16x32_bf16 a[36:39],v[132:135],v[176:179],a[36:39]\n"
"s_load_dwordx2 s[48:49],s[0:1],56\n"
"v_mfma_f32_16x16x32_bf16 a[36:39],v[196:199],v[240:243],a[36:39]\n"
"v_mfma_f32_16x16x32_bf16 a[44:47],v[196:199],v[244:247],a[44:47]\n"
"buffer_load_dwordx4 v111,s[20:23],s77 offen offset:1024 lds\n"
"v_mfma_f32_16x16x32_bf16 a[44:47],v[132:135],v[180:183],a[44:47]\n"
"v_mfma_f32_16x16x32_bf16 a[52:55],v[132:135],v[184:187],a[52:55]\n"
"v_mfma_f32_16x16x32_bf16 a[52:55],v[196:199],v[248:251],a[52:55]\n"
"v_mfma_f32_16x16x32_bf16 a[60:63],v[196:199],v[252:255],a[60:63]\n"
"buffer_load_dwordx4 v113,s[20:23],s77 offen offset:2048 lds\n"
"v_mfma_f32_16x16x32_bf16 a[60:63],v[132:135],v[188:191],a[60:63]\n"
"v_mfma_f32_16x16x32_bf16 a[64:67],v[136:139],v[160:163],a[64:67]\n"
"v_mfma_f32_16x16x32_bf16 a[64:67],v[200:203],v[224:227],a[64:67]\n"
"v_mfma_f32_16x16x32_bf16 a[72:75],v[200:203],v[228:231],a[72:75]\n"
"buffer_load_dwordx4 v114,s[20:23],s77 offen offset:3072 lds\n"
"v_mfma_f32_16x16x32_bf16 a[72:75],v[136:139],v[164:167],a[72:75]\n"
"v_mfma_f32_16x16x32_bf16 a[80:83],v[136:139],v[168:171],a[80:83]\n"
"v_mfma_f32_16x16x32_bf16 a[80:83],v[200:203],v[232:235],a[80:83]\n"
"s_mov_b32 s77,3932160\n"
"s_add_u32 m0,s78,114688\n"
"v_mfma_f32_16x16x32_bf16 a[88:91],v[200:203],v[236:239],a[88:91]\n"
"buffer_load_dwordx4 v110,s[20:23],s77 offen offset:0 lds\n"
"v_mfma_f32_16x16x32_bf16 a[88:91],v[136:139],v[172:175],a[88:91]\n"
"v_mfma_f32_16x16x32_bf16 a[96:99],v[136:139],v[176:179],a[96:99]\n"
"v_mfma_f32_16x16x32_bf16 a[96:99],v[200:203],v[240:243],a[96:99]\n"
"v_mfma_f32_16x16x32_bf16 a[104:107],v[200:203],v[244:247],a[104:107]\n"
"buffer_load_dwordx4 v111,s[20:23],s77 offen offset:1024 lds\n"
"v_mfma_f32_16x16x32_bf16 a[104:107],v[136:139],v[180:183],a[104:107]\n"
"v_mfma_f32_16x16x32_bf16 a[112:115],v[136:139],v[184:187],a[112:115]\n"
"v_mfma_f32_16x16x32_bf16 a[112:115],v[200:203],v[248:251],a[112:115]\n"
"v_mfma_f32_16x16x32_bf16 a[120:123],v[200:203],v[252:255],a[120:123]\n"
"buffer_load_dwordx4 v113,s[20:23],s77 offen offset:2048 lds\n"
"v_mfma_f32_16x16x32_bf16 a[120:123],v[136:139],v[188:191],a[120:123]\n"
"s_mov_b32 s76,s55\n"
"s_waitcnt vmcnt(7)\n"
"v_mfma_f32_16x16x32_bf16 a[68:71],v[140:143],v[160:163],a[68:71]\n"
"v_xor_b32 v116,s80,v74\n"
"v_xor_b32 v117,s81,v75\n"
"v_mfma_f32_16x16x32_bf16 a[68:71],v[204:207],v[224:227],a[68:71]\n"
"v_or_b32 v116,v116,v117\n"
"v_xor_b32 v117,s80,v76\n"
"v_mfma_f32_16x16x32_bf16 a[76:79],v[204:207],v[228:231],a[76:79]\n"
"buffer_load_dwordx4 v114,s[20:23],s77 offen offset:3072 lds\n"
"v_mfma_f32_16x16x32_bf16 a[76:79],v[140:143],v[164:167],a[76:79]\n"
"v_or_b32 v116,v116,v117\n"
"v_xor_b32 v117,s81,v77\n"
"v_mfma_f32_16x16x32_bf16 a[84:87],v[140:143],v[168:171],a[84:87]\n"
"v_or_b32 v116,v116,v117\n"
"v_cmp_ne_u32 vcc,0,v116\n"
"v_mfma_f32_16x16x32_bf16 a[84:87],v[204:207],v[232:235],a[84:87]\n"
"s_cbranch_vccnz .Lfk_xn1_s\n"
".Lfk_xn1_ok:\n"
"v_mfma_f32_16x16x32_bf16 a[92:95],v[204:207],v[236:239],a[92:95]\n"
"s_add_u32 m0,s78,65536\n"
"v_mfma_f32_16x16x32_bf16 a[92:95],v[140:143],v[172:175],a[92:95]\n"
"buffer_load_dwordx4 v112,s[24:27],s76 offen offset:0 sc1 lds\n"
"v_mfma_f32_16x16x32_bf16 a[100:103],v[140:143],v[176:179],a[100:103]\n"
"s_waitcnt lgkmcnt(0)\n"
"s_lshl2_add_u32 s88,s100,s52\n"
"v_mfma_f32_16x16x32_bf16 a[100:103],v[204:207],v[240:243],a[100:103]\n"
"s_mul_i32 s88,s88,7168\n"
"s_add_u32 s88,s88,0\n"
"v_mfma_f32_16x16x32_bf16 a[108:111],v[204:207],v[244:247],a[108:111]\n"
"s_add_u32 s48,s48,s88\n"
"s_addc_u32 s49,s49,0\n"
"s_mov_b32 s50,3584\n"
"v_mfma_f32_16x16x32_bf16 a[108:111],v[140:143],v[180:183],a[108:111]\n"
"s_mov_b32 s51,0x20000\n"
"v_lshlrev_b32 v8,3,v112\n"
"v_mfma_f32_16x16x32_bf16 a[116:119],v[140:143],v[184:187],a[116:119]\n"
"buffer_load_dwordx4 v112,s[24:27],s76 offen offset:1024 sc1 lds\n"
"v_mfma_f32_16x16x32_bf16 a[116:119],v[204:207],v[248:251],a[116:119]\n"
"v_mfma_f32_16x16x32_bf16 a[124:127],v[204:207],v[252:255],a[124:127]\n"
"v_mfma_f32_16x16x32_bf16 a[124:127],v[140:143],v[188:191],a[124:127]\n"
"v_mfma_f32_16x16x32_bf16 a[128:131],v[144:147],v[160:163],a[128:131]\n"
"v_mfma_f32_16x16x32_bf16 a[128:131],v[208:211],v[224:227],a[128:131]\n"
"buffer_load_dwordx4 v112,s[24:27],s76 offen offset:2048 sc1 lds\n"
"v_mfma_f32_16x16x32_bf16 a[136:139],v[208:211],v[228:231],a[136:139]\n"
"v_mfma_f32_16x16x32_bf16 a[136:139],v[144:147],v[164:167],a[136:139]\n"
"v_mfma_f32_16x16x32_bf16 a[144:147],v[144:147],v[168:171],a[144:147]\n"
"v_mfma_f32_16x16x32_bf16 a[144:147],v[208:211],v[232:235],a[144:147]\n"
"buffer_load_dwordx4 v112,s[24:27],s76 offen offset:3072 sc1 lds\n"
"v_mfma_f32_16x16x32_bf16 a[152:155],v[208:211],v[236:239],a[152:155]\n"
"s_xor_b32 s76,s76,16384\n"
"v_mfma_f32_16x16x32_bf16 a[152:155],v[144:147],v[172:175],a[152:155]\n"
"v_mfma_f32_16x16x32_bf16 a[160:163],v[144:147],v[176:179],a[160:163]\n"
"v_mfma_f32_16x16x32_bf16 a[160:163],v[208:211],v[240:243],a[160:163]\n"
"v_mfma_f32_16x16x32_bf16 a[168:171],v[208:211],v[244:247],a[168:171]\n"
"s_add_u32 m0,s78,81920\n"
"v_mfma_f32_16x16x32_bf16 a[168:171],v[144:147],v[180:183],a[168:171]\n"
"buffer_load_dwordx4 v112,s[24:27],s76 offen offset:0 sc1 lds\n"
"v_mfma_f32_16x16x32_bf16 a[176:179],v[144:147],v[184:187],a[176:179]\n"
"v_mfma_f32_16x16x32_bf16 a[176:179],v[208:211],v[248:251],a[176:179]\n"
"v_mfma_f32_16x16x32_bf16 a[184:187],v[208:211],v[252:255],a[184:187]\n"
"v_mfma_f32_16x16x32_bf16 a[184:187],v[144:147],v[188:191],a[184:187]\n"
"v_mfma_f32_16x16x32_bf16 a[132:135],v[148:151],v[160:163],a[132:135]\n"
"buffer_load_dwordx4 v112,s[24:27],s76 offen offset:1024 sc1 lds\n"
"v_mfma_f32_16x16x32_bf16 a[132:135],v[212:215],v[224:227],a[132:135]\n"
"v_mfma_f32_16x16x32_bf16 a[140:143],v[212:215],v[228:231],a[140:143]\n"
"v_mfma_f32_16x16x32_bf16 a[140:143],v[148:151],v[164:167],a[140:143]\n"
"v_mfma_f32_16x16x32_bf16 a[148:151],v[148:151],v[168:171],a[148:151]\n"
"v_mfma_f32_16x16x32_bf16 a[148:151],v[212:215],v[232:235],a[148:151]\n"
"buffer_load_dwordx4 v112,s[24:27],s76 offen offset:2048 sc1 lds\n"
"v_mfma_f32_16x16x32_bf16 a[156:159],v[212:215],v[236:239],a[156:159]\n"
"v_mfma_f32_16x16x32_bf16 a[156:159],v[148:151],v[172:175],a[156:159]\n"
"v_mfma_f32_16x16x32_bf16 a[164:167],v[148:151],v[176:179],a[164:167]\n"
"v_mfma_f32_16x16x32_bf16 a[164:167],v[212:215],v[240:243],a[164:167]\n"
"buffer_load_dwordx4 v112,s[24:27],s76 offen offset:3072 sc1 lds\n"
"v_mfma_f32_16x16x32_bf16 a[172:175],v[212:215],v[244:247],a[172:175]\n"
"s_xor_b32 s76,s76,49152\n"
"v_mfma_f32_16x16x32_bf16 a[172:175],v[148:151],v[180:183],a[172:175]\n"
"v_mfma_f32_16x16x32_bf16 a[180:183],v[148:151],v[184:187],a[180:183]\n"
"buffer_load_dword v115,v8,s[48:51],0 offen\n"
"v_mfma_f32_16x16x32_bf16 a[180:183],v[212:215],v[248:251],a[180:183]\n"
"v_mfma_f32_16x16x32_bf16 a[188:191],v[212:215],v[252:255],a[188:191]\n"
"v_mfma_f32_16x16x32_bf16 a[188:191],v[148:151],v[188:191],a[188:191]\n"
"s_barrier\n"
"v_mfma_f32_16x16x32_bf16 a[192:195],v[216:219],v[224:227],a[192:195]\n"
"ds_read_b128 v[152:155],v80 offset:12288\n"
"v_mfma_f32_16x16x32_bf16 a[196:199],v[220:223],v[224:227],a[196:199]\n"
"ds_read_b128 v[156:159],v80 offset:14336\n"
"v_mfma_f32_16x16x32_bf16 a[204:207],v[220:223],v[228:231],a[204:207]\n"
"ds_read_b64_tr_b16 v[160:161],v98 offset:49152\n"
"v_mfma_f32_16x16x32_bf16 a[200:203],v[216:219],v[228:231],a[200:203]\n"
"ds_read_b64_tr_b16 v[162:163],v98 offset:50176\n"
"v_mfma_f32_16x16x32_bf16 a[208:211],v[216:219],v[232:235],a[208:211]\n"
"ds_read_b64_tr_b16 v[164:165],v99 offset:49152\n"
"v_mfma_f32_16x16x32_bf16 a[212:215],v[220:223],v[232:235],a[212:215]\n"
"ds_read_b64_tr_b16 v[166:167],v99 offset:50176\n"
"v_mfma_f32_16x16x32_bf16 a[220:223],v[220:223],v[236:239],a[220:223]\n"
"ds_read_b64_tr_b16 v[168:169],v100 offset:49152\n"
"v_mfma_f32_16x16x32_bf16 a[216:219],v[216:219],v[236:239],a[216:219]\n"
"ds_read_b64_tr_b16 v[170:171],v100 offset:50176\n"
"v_mfma_f32_16x16x32_bf16 a[224:227],v[216:219],v[240:243],a[224:227]\n"
"ds_read_b64_tr_b16 v[172:173],v101 offset:49152\n"
"v_mfma_f32_16x16x32_bf16 a[228:231],v[220:223],v[240:243],a[228:231]\n"
"ds_read_b64_tr_b16 v[174:175],v101 offset:50176\n"
"v_mfma_f32_16x16x32_bf16 a[236:239],v[220:223],v[244:247],a[236:239]\n"
"ds_read_b64_tr_b16 v[176:177],v102 offset:49152\n"
"v_mfma_f32_16x16x32_bf16 a[232:235],v[216:219],v[244:247],a[232:235]\n"
"ds_read_b64_tr_b16 v[178:179],v102 offset:50176\n"
"v_mfma_f32_16x16x32_bf16 a[240:243],v[216:219],v[248:251],a[240:243]\n"
"ds_read_b64_tr_b16 v[180:181],v103 offset:49152\n"
"v_mfma_f32_16x16x32_bf16 a[244:247],v[220:223],v[248:251],a[244:247]\n"
"ds_read_b64_tr_b16 v[182:183],v103 offset:50176\n"
"ds_read_b64_tr_b16 v[184:185],v104 offset:49152\n"
"v_mfma_f32_16x16x32_bf16 a[252:255],v[220:223],v[252:255],a[252:255]\n"
"v_mov_b32 v110,v72\n"
"v_mov_b32 v111,v73\n"
"v_mfma_f32_16x16x32_bf16 a[248:251],v[216:219],v[252:255],a[248:251]\n"
"ds_read_b64_tr_b16 v[186:187],v104 offset:50176\n"
"s_waitcnt lgkmcnt(10)\n"
"v_mfma_f32_16x16x32_bf16 a[192:195],v[152:155],v[160:163],a[192:195]\n"
"ds_read_b64_tr_b16 v[188:189],v105 offset:49152\n"
"v_mfma_f32_16x16x32_bf16 a[196:199],v[156:159],v[160:163],a[196:199]\n"
"ds_read_b64_tr_b16 v[190:191],v105 offset:50176\n"
"v_mfma_f32_16x16x32_bf16 a[204:207],v[156:159],v[164:167],a[204:207]\n"
"ds_read_b128 v[128:131],v80 offset:0\n"
"v_mfma_f32_16x16x32_bf16 a[200:203],v[152:155],v[164:167],a[200:203]\n"
"ds_read_b128 v[192:195],v81 offset:0\n"
"s_waitcnt lgkmcnt(10)\n"
"v_mfma_f32_16x16x32_bf16 a[208:211],v[152:155],v[168:171],a[208:211]\n"
"ds_read_b64_tr_b16 v[224:225],v98 offset:32768\n"
"v_mfma_f32_16x16x32_bf16 a[212:215],v[156:159],v[168:171],a[212:215]\n"
"ds_read_b64_tr_b16 v[226:227],v98 offset:33792\n"
"v_mfma_f32_16x16x32_bf16 a[220:223],v[156:159],v[172:175],a[220:223]\n"
"ds_read_b64_tr_b16 v[228:229],v99 offset:32768\n"
"v_mfma_f32_16x16x32_bf16 a[216:219],v[152:155],v[172:175],a[216:219]\n"
"ds_read_b64_tr_b16 v[230:231],v99 offset:33792\n"
"s_waitcnt lgkmcnt(10)\n"
"v_mfma_f32_16x16x32_bf16 a[224:227],v[152:155],v[176:179],a[224:227]\n"
"ds_read_b64_tr_b16 v[232:233],v100 offset:32768\n"
"v_mfma_f32_16x16x32_bf16 a[228:231],v[156:159],v[176:179],a[228:231]\n"
"ds_read_b64_tr_b16 v[234:235],v100 offset:33792\n"
"v_mfma_f32_16x16x32_bf16 a[236:239],v[156:159],v[180:183],a[236:239]\n"
"ds_read_b64_tr_b16 v[236:237],v101 offset:32768\n"
"v_mfma_f32_16x16x32_bf16 a[232:235],v[152:155],v[180:183],a[232:235]\n"
"ds_read_b64_tr_b16 v[238:239],v101 offset:33792\n"
"s_waitcnt lgkmcnt(10)\n"
"v_mfma_f32_16x16x32_bf16 a[240:243],v[152:155],v[184:187],a[240:243]\n"
"ds_read_b64_tr_b16 v[240:241],v102 offset:32768\n"
"v_mfma_f32_16x16x32_bf16 a[244:247],v[156:159],v[184:187],a[244:247]\n"
"ds_read_b64_tr_b16 v[242:243],v102 offset:33792\n"
"v_mfma_f32_16x16x32_bf16 a[252:255],v[156:159],v[188:191],a[252:255]\n"
"ds_read_b64_tr_b16 v[244:245],v103 offset:32768\n"
"v_mfma_f32_16x16x32_bf16 a[248:251],v[152:155],v[188:191],a[248:251]\n"
"ds_read_b64_tr_b16 v[246:247],v103 offset:33792\n"
"s_waitcnt lgkmcnt(8)\n"
"v_mfma_f32_16x16x32_bf16 a[0:3],v[128:131],v[160:163],a[0:3]\n"
"ds_read_b64_tr_b16 v[248:249],v104 offset:32768\n"
"v_mfma_f32_16x16x32_bf16 a[0:3],v[192:195],v[224:227],a[0:3]\n"
"ds_read_b64_tr_b16 v[250:251],v104 offset:33792\n"
"v_mfma_f32_16x16x32_bf16 a[8:11],v[192:195],v[228:231],a[8:11]\n"
"ds_read_b64_tr_b16 v[252:253],v105 offset:32768\n"
"v_mfma_f32_16x16x32_bf16 a[8:11],v[128:131],v[164:167],a[8:11]\n"
"ds_read_b64_tr_b16 v[254:255],v105 offset:33792\n"
"s_waitcnt lgkmcnt(8)\n"
"v_mfma_f32_16x16x32_bf16 a[16:19],v[128:131],v[168:171],a[16:19]\n"
"ds_read_b128 v[132:135],v80 offset:2048\n"
"v_mfma_f32_16x16x32_bf16 a[16:19],v[192:195],v[232:235],a[16:19]\n"
"ds_read_b128 v[196:199],v81 offset:2048\n"
"v_mfma_f32_16x16x32_bf16 a[24:27],v[192:195],v[236:239],a[24:27]\n"
"ds_read_b128 v[136:139],v80 offset:4096\n"
"v_mfma_f32_16x16x32_bf16 a[24:27],v[128:131],v[172:175],a[24:27]\n"
"ds_read_b128 v[200:203],v81 offset:4096\n"
"s_waitcnt lgkmcnt(8)\n"
"v_mfma_f32_16x16x32_bf16 a[32:35],v[128:131],v[176:179],a[32:35]\n"
"ds_read_b128 v[140:143],v80 offset:6144\n"
"v_mfma_f32_16x16x32_bf16 a[32:35],v[192:195],v[240:243],a[32:35]\n"
"ds_read_b128 v[204:207],v81 offset:6144\n"
"v_mfma_f32_16x16x32_bf16 a[40:43],v[192:195],v[244:247],a[40:43]\n"
"ds_read_b128 v[144:147],v80 offset:8192\n"
"v_mfma_f32_16x16x32_bf16 a[40:43],v[128:131],v[180:183],a[40:43]\n"
"ds_read_b128 v[208:211],v81 offset:8192\n"
"s_waitcnt lgkmcnt(8)\n"
"v_mfma_f32_16x16x32_bf16 a[48:51],v[128:131],v[184:187],a[48:51]\n"
"ds_read_b128 v[148:151],v80 offset:10240\n"
"v_mfma_f32_16x16x32_bf16 a[48:51],v[192:195],v[248:251],a[48:51]\n"
"ds_read_b128 v[212:215],v81 offset:10240\n"
"v_mfma_f32_16x16x32_bf16 a[56:59],v[192:195],v[252:255],a[56:59]\n"
"ds_read_b128 v[216:219],v81 offset:12288\n"
"v_mfma_f32_16x16x32_bf16 a[56:59],v[128:131],v[188:191],a[56:59]\n"
"ds_read_b128 v[220:223],v81 offset:14336\n"
"s_waitcnt lgkmcnt(10)\n"
"v_mfma_f32_16x16x32_bf16 a[4:7],v[132:135],v[160:163],a[4:7]\n"
"v_mfma_f32_16x16x32_bf16 a[4:7],v[196:199],v[224:227],a[4:7]\n"
"v_mfma_f32_16x16x32_bf16 a[12:15],v[196:199],v[228:231],a[12:15]\n"
"v_mfma_f32_16x16x32_bf16 a[12:15],v[132:135],v[164:167],a[12:15]\n"
"v_mfma_f32_16x16x32_bf16 a[20:23],v[132:135],v[168:171],a[20:23]\n"
"s_waitcnt lgkmcnt(0)\n"
"s_barrier\n"
"v_mfma_f32_16x16x32_bf16 a[20:23],v[196:199],v[232:235],a[20:23]\n"
"s_add_u32 m0,s85,147456\n"
"v_mfma_f32_16x16x32_bf16 a[28:31],v[196:199],v[236:239],a[28:31]\n"
"buffer_load_dwordx4 v110,s[28:31],s86 offen lds\n"
"v_mfma_f32_16x16x32_bf16 a[28:31],v[132:135],v[172:175],a[28:31]\n"
"v_mfma_f32_16x16x32_bf16 a[36:39],v[132:135],v[176:179],a[36:39]\n"
"s_load_dwordx2 s[48:49],s[0:1],56\n"
"v_mfma_f32_16x16x32_bf16 a[36:39],v[196:199],v[240:243],a[36:39]\n"
"s_add_u32 m0,s85,148480\n"
"v_mfma_f32_16x16x32_bf16 a[44:47],v[196:199],v[244:247],a[44:47]\n"
"buffer_load_dwordx4 v111,s[28:31],s86 offen lds\n"
"v_mfma_f32_16x16x32_bf16 a[44:47],v[132:135],v[180:183],a[44:47]\n"
"s_xor_b32 s86,s86,262144\n"
"v_mfma_f32_16x16x32_bf16 a[52:55],v[132:135],v[184:187],a[52:55]\n"
"v_mfma_f32_16x16x32_bf16 a[52:55],v[196:199],v[248:251],a[52:55]\n"
"s_add_u32 m0,s85,155648\n"
"v_mfma_f32_16x16x32_bf16 a[60:63],v[196:199],v[252:255],a[60:63]\n"
"buffer_load_dwordx4 v110,s[28:31],s86 offen lds\n"
"v_mfma_f32_16x16x32_bf16 a[60:63],v[132:135],v[188:191],a[60:63]\n"
"v_mfma_f32_16x16x32_bf16 a[64:67],v[136:139],v[160:163],a[64:67]\n"
"v_mfma_f32_16x16x32_bf16 a[64:67],v[200:203],v[224:227],a[64:67]\n"
"s_add_u32 m0,s85,156672\n"
"v_mfma_f32_16x16x32_bf16 a[72:75],v[200:203],v[228:231],a[72:75]\n"
"buffer_load_dwordx4 v111,s[28:31],s86 offen lds\n"
"v_mfma_f32_16x16x32_bf16 a[72:75],v[136:139],v[164:167],a[72:75]\n"
"s_xor_b32 s86,s86,786432\n"
"v_mfma_f32_16x16x32_bf16 a[80:83],v[136:139],v[168:171],a[80:83]\n"
"v_mfma_f32_16x16x32_bf16 a[80:83],v[200:203],v[232:235],a[80:83]\n"
"s_add_u32 m0,s85,131072\n"
"v_mfma_f32_16x16x32_bf16 a[88:91],v[200:203],v[236:239],a[88:91]\n"
"buffer_load_dwordx4 v110,s[28:31],s86 offen lds\n"
"v_mfma_f32_16x16x32_bf16 a[88:91],v[136:139],v[172:175],a[88:91]\n"
"v_mfma_f32_16x16x32_bf16 a[96:99],v[136:139],v[176:179],a[96:99]\n"
"v_mfma_f32_16x16x32_bf16 a[96:99],v[200:203],v[240:243],a[96:99]\n"
"s_add_u32 m0,s85,132096\n"
"v_mfma_f32_16x16x32_bf16 a[104:107],v[200:203],v[244:247],a[104:107]\n"
"buffer_load_dwordx4 v111,s[28:31],s86 offen lds\n"
"v_mfma_f32_16x16x32_bf16 a[104:107],v[136:139],v[180:183],a[104:107]\n"
"s_xor_b32 s86,s86,262144\n"
"v_mfma_f32_16x16x32_bf16 a[112:115],v[136:139],v[184:187],a[112:115]\n"
"v_mfma_f32_16x16x32_bf16 a[112:115],v[200:203],v[248:251],a[112:115]\n"
"v_mfma_f32_16x16x32_bf16 a[120:123],v[200:203],v[252:255],a[120:123]\n"
"v_mfma_f32_16x16x32_bf16 a[120:123],v[136:139],v[188:191],a[120:123]\n"
"v_mfma_f32_16x16x32_bf16 a[68:71],v[140:143],v[160:163],a[68:71]\n"
"v_mfma_f32_16x16x32_bf16 a[68:71],v[204:207],v[224:227],a[68:71]\n"
"s_add_u32 m0,s85,139264\n"
"v_mfma_f32_16x16x32_bf16 a[76:79],v[204:207],v[228:231],a[76:79]\n"
"buffer_load_dwordx4 v110,s[28:31],s86 offen lds\n"
"v_mfma_f32_16x16x32_bf16 a[76:79],v[140:143],v[164:167],a[76:79]\n"
"v_mfma_f32_16x16x32_bf16 a[84:87],v[140:143],v[168:171],a[84:87]\n"
"v_mfma_f32_16x16x32_bf16 a[84:87],v[204:207],v[232:235],a[84:87]\n"
"s_add_u32 m0,s85,140288\n"
"v_mfma_f32_16x16x32_bf16 a[92:95],v[204:207],v[236:239],a[92:95]\n"
"buffer_load_dwordx4 v111,s[28:31],s86 offen lds\n"
"v_mfma_f32_16x16x32_bf16 a[92:95],v[140:143],v[172:175],a[92:95]\n"
"s_xor_b32 s86,s86,1835008\n"
"v_mfma_f32_16x16x32_bf16 a[100:103],v[140:143],v[176:179],a[100:103]\n"
"s_waitcnt lgkmcnt(0)\n"
"s_lshl2_add_u32 s88,s100,s52\n"
"v_mfma_f32_16x16x32_bf16 a[100:103],v[204:207],v[240:243],a[100:103]\n"
"s_mul_i32 s88,s88,7168\n"
"s_add_u32 m0,s78,0\n"
"v_mfma_f32_16x16x32_bf16 a[108:111],v[204:207],v[244:247],a[108:111]\n"
"buffer_load_dwordx4 v112,s[24:27],s76 offen offset:0 sc1 lds\n"
"v_mfma_f32_16x16x32_bf16 a[108:111],v[140:143],v[180:183],a[108:111]\n"
"s_add_u32 s88,s88,3584\n"
"s_add_u32 s48,s48,s88\n"
"s_addc_u32 s49,s49,0\n"
"v_mfma_f32_16x16x32_bf16 a[116:119],v[140:143],v[184:187],a[116:119]\n"
"s_mov_b32 s50,3584\n"
"s_mov_b32 s51,0x20000\n"
"v_mfma_f32_16x16x32_bf16 a[116:119],v[204:207],v[248:251],a[116:119]\n"
"v_lshlrev_b32 v8,3,v112\n"
"v_mfma_f32_16x16x32_bf16 a[124:127],v[204:207],v[252:255],a[124:127]\n"
"buffer_load_dwordx4 v112,s[24:27],s76 offen offset:1024 sc1 lds\n"
"v_mfma_f32_16x16x32_bf16 a[124:127],v[140:143],v[188:191],a[124:127]\n"
"v_mfma_f32_16x16x32_bf16 a[128:131],v[144:147],v[160:163],a[128:131]\n"
"v_mfma_f32_16x16x32_bf16 a[128:131],v[208:211],v[224:227],a[128:131]\n"
"v_mfma_f32_16x16x32_bf16 a[136:139],v[208:211],v[228:231],a[136:139]\n"
"buffer_load_dwordx4 v112,s[24:27],s76 offen offset:2048 sc1 lds\n"
"v_mfma_f32_16x16x32_bf16 a[136:139],v[144:147],v[164:167],a[136:139]\n"
"v_mfma_f32_16x16x32_bf16 a[144:147],v[144:147],v[168:171],a[144:147]\n"
"v_mfma_f32_16x16x32_bf16 a[144:147],v[208:211],v[232:235],a[144:147]\n"
"buffer_load_dwordx4 v112,s[24:27],s76 offen offset:3072 sc1 lds\n"
"v_mfma_f32_16x16x32_bf16 a[152:155],v[208:211],v[236:239],a[152:155]\n"
"s_xor_b32 s76,s76,16384\n"
"v_mfma_f32_16x16x32_bf16 a[152:155],v[144:147],v[172:175],a[152:155]\n"
"v_mfma_f32_16x16x32_bf16 a[160:163],v[144:147],v[176:179],a[160:163]\n"
"v_mfma_f32_16x16x32_bf16 a[160:163],v[208:211],v[240:243],a[160:163]\n"
"s_add_u32 m0,s78,16384\n"
"v_mfma_f32_16x16x32_bf16 a[168:171],v[208:211],v[244:247],a[168:171]\n"
"buffer_load_dwordx4 v112,s[24:27],s76 offen offset:0 sc1 lds\n"
"v_mfma_f32_16x16x32_bf16 a[168:171],v[144:147],v[180:183],a[168:171]\n"
"v_mfma_f32_16x16x32_bf16 a[176:179],v[144:147],v[184:187],a[176:179]\n"
"v_mfma_f32_16x16x32_bf16 a[176:179],v[208:211],v[248:251],a[176:179]\n"
"v_mfma_f32_16x16x32_bf16 a[184:187],v[208:211],v[252:255],a[184:187]\n"
"buffer_load_dwordx4 v112,s[24:27],s76 offen offset:1024 sc1 lds\n"
"v_mfma_f32_16x16x32_bf16 a[184:187],v[144:147],v[188:191],a[184:187]\n"
"v_mfma_f32_16x16x32_bf16 a[132:135],v[148:151],v[160:163],a[132:135]\n"
"v_mfma_f32_16x16x32_bf16 a[132:135],v[212:215],v[224:227],a[132:135]\n"
"v_mfma_f32_16x16x32_bf16 a[140:143],v[212:215],v[228:231],a[140:143]\n"
"buffer_load_dwordx4 v112,s[24:27],s76 offen offset:2048 sc1 lds\n"
"v_mfma_f32_16x16x32_bf16 a[140:143],v[148:151],v[164:167],a[140:143]\n"
"v_mfma_f32_16x16x32_bf16 a[148:151],v[148:151],v[168:171],a[148:151]\n"
"v_mfma_f32_16x16x32_bf16 a[148:151],v[212:215],v[232:235],a[148:151]\n"
"buffer_load_dwordx4 v112,s[24:27],s76 offen offset:3072 sc1 lds\n"
"v_mfma_f32_16x16x32_bf16 a[156:159],v[212:215],v[236:239],a[156:159]\n"
"s_xor_b32 s76,s76,114688\n"
"v_mfma_f32_16x16x32_bf16 a[156:159],v[148:151],v[172:175],a[156:159]\n"
"v_mfma_f32_16x16x32_bf16 a[164:167],v[148:151],v[176:179],a[164:167]\n"
"buffer_load_dword v115,v8,s[48:51],0 offen\n"
"v_mfma_f32_16x16x32_bf16 a[164:167],v[212:215],v[240:243],a[164:167]\n"
"v_mfma_f32_16x16x32_bf16 a[172:175],v[212:215],v[244:247],a[172:175]\n"
"v_mfma_f32_16x16x32_bf16 a[172:175],v[148:151],v[180:183],a[172:175]\n"
"v_mfma_f32_16x16x32_bf16 a[180:183],v[148:151],v[184:187],a[180:183]\n"
"v_mfma_f32_16x16x32_bf16 a[180:183],v[212:215],v[248:251],a[180:183]\n"
"v_mfma_f32_16x16x32_bf16 a[188:191],v[212:215],v[252:255],a[188:191]\n"
"v_mfma_f32_16x16x32_bf16 a[188:191],v[148:151],v[188:191],a[188:191]\n"
"s_waitcnt vmcnt(26)\n"
"s_barrier\n"
"v_mfma_f32_16x16x32_bf16 a[192:195],v[216:219],v[224:227],a[192:195]\n"
"ds_read_b128 v[152:155],v80 offset:45056\n"
"v_mfma_f32_16x16x32_bf16 a[196:199],v[220:223],v[224:227],a[196:199]\n"
"ds_read_b128 v[156:159],v80 offset:47104\n"
"v_mfma_f32_16x16x32_bf16 a[204:207],v[220:223],v[228:231],a[204:207]\n"
"ds_read_b64_tr_b16 v[160:161],v98 offset:0\n"
"v_mfma_f32_16x16x32_bf16 a[200:203],v[216:219],v[228:231],a[200:203]\n"
"ds_read_b64_tr_b16 v[162:163],v98 offset:1024\n"
"v_mfma_f32_16x16x32_bf16 a[208:211],v[216:219],v[232:235],a[208:211]\n"
"ds_read_b64_tr_b16 v[164:165],v99 offset:0\n"
"v_mfma_f32_16x16x32_bf16 a[212:215],v[220:223],v[232:235],a[212:215]\n"
"ds_read_b64_tr_b16 v[166:167],v99 offset:1024\n"
"v_mfma_f32_16x16x32_bf16 a[220:223],v[220:223],v[236:239],a[220:223]\n"
"ds_read_b64_tr_b16 v[168:169],v100 offset:0\n"
"v_mfma_f32_16x16x32_bf16 a[216:219],v[216:219],v[236:239],a[216:219]\n"
"ds_read_b64_tr_b16 v[170:171],v100 offset:1024\n"
"v_mfma_f32_16x16x32_bf16 a[224:227],v[216:219],v[240:243],a[224:227]\n"
"ds_read_b64_tr_b16 v[172:173],v101 offset:0\n"
"v_mfma_f32_16x16x32_bf16 a[228:231],v[220:223],v[240:243],a[228:231]\n"
"ds_read_b64_tr_b16 v[174:175],v101 offset:1024\n"
"v_mfma_f32_16x16x32_bf16 a[236:239],v[220:223],v[244:247],a[236:239]\n"
"ds_read_b64_tr_b16 v[176:177],v102 offset:0\n"
"v_mfma_f32_16x16x32_bf16 a[232:235],v[216:219],v[244:247],a[232:235]\n"
"ds_read_b64_tr_b16 v[178:179],v102 offset:1024\n"
"v_mfma_f32_16x16x32_bf16 a[240:243],v[216:219],v[248:251],a[240:243]\n"
"ds_read_b64_tr_b16 v[180:181],v103 offset:0\n"
"v_mfma_f32_16x16x32_bf16 a[244:247],v[220:223],v[248:251],a[244:247]\n"
"ds_read_b64_tr_b16 v[182:183],v103 offset:1024\n"
"v_mfma_f32_16x16x32_bf16 a[252:255],v[220:223],v[252:255],a[252:255]\n"
"ds_read_b64_tr_b16 v[184:185],v104 offset:0\n"
"v_mfma_f32_16x16x32_bf16 a[248:251],v[216:219],v[252:255],a[248:251]\n"
"ds_read_b64_tr_b16 v[186:187],v104 offset:1024\n"
"s_waitcnt lgkmcnt(10)\n"
"v_mfma_f32_16x16x32_bf16 a[192:195],v[152:155],v[160:163],a[192:195]\n"
"ds_read_b64_tr_b16 v[188:189],v105 offset:0\n"
"v_mfma_f32_16x16x32_bf16 a[196:199],v[156:159],v[160:163],a[196:199]\n"
"ds_read_b64_tr_b16 v[190:191],v105 offset:1024\n"
"v_mfma_f32_16x16x32_bf16 a[204:207],v[156:159],v[164:167],a[204:207]\n"
"ds_read_b128 v[128:131],v80 offset:32768\n"
"v_mfma_f32_16x16x32_bf16 a[200:203],v[152:155],v[164:167],a[200:203]\n"
"ds_read_b128 v[192:195],v81 offset:32768\n"
"s_waitcnt lgkmcnt(10)\n"
"v_mfma_f32_16x16x32_bf16 a[208:211],v[152:155],v[168:171],a[208:211]\n"
"ds_read_b64_tr_b16 v[224:225],v98 offset:16384\n"
"v_mfma_f32_16x16x32_bf16 a[212:215],v[156:159],v[168:171],a[212:215]\n"
"ds_read_b64_tr_b16 v[226:227],v98 offset:17408\n"
"v_mfma_f32_16x16x32_bf16 a[220:223],v[156:159],v[172:175],a[220:223]\n"
"ds_read_b64_tr_b16 v[228:229],v99 offset:16384\n"
"v_mfma_f32_16x16x32_bf16 a[216:219],v[152:155],v[172:175],a[216:219]\n"
"ds_read_b64_tr_b16 v[230:231],v99 offset:17408\n"
"s_waitcnt lgkmcnt(10)\n"
"v_mfma_f32_16x16x32_bf16 a[224:227],v[152:155],v[176:179],a[224:227]\n"
"ds_read_b64_tr_b16 v[232:233],v100 offset:16384\n"
"v_mfma_f32_16x16x32_bf16 a[228:231],v[156:159],v[176:179],a[228:231]\n"
"ds_read_b64_tr_b16 v[234:235],v100 offset:17408\n"
"v_mfma_f32_16x16x32_bf16 a[236:239],v[156:159],v[180:183],a[236:239]\n"
"ds_read_b64_tr_b16 v[236:237],v101 offset:16384\n"
"v_mfma_f32_16x16x32_bf16 a[232:235],v[152:155],v[180:183],a[232:235]\n"
"ds_read_b64_tr_b16 v[238:239],v101 offset:17408\n"
"s_waitcnt lgkmcnt(10)\n"
"v_mfma_f32_16x16x32_bf16 a[240:243],v[152:155],v[184:187],a[240:243]\n"
"ds_read_b64_tr_b16 v[240:241],v102 offset:16384\n"
"v_mfma_f32_16x16x32_bf16 a[244:247],v[156:159],v[184:187],a[244:247]\n"
"ds_read_b64_tr_b16 v[242:243],v102 offset:17408\n"
"v_mfma_f32_16x16x32_bf16 a[252:255],v[156:159],v[188:191],a[252:255]\n"
"ds_read_b64_tr_b16 v[244:245],v103 offset:16384\n"
"v_mfma_f32_16x16x32_bf16 a[248:251],v[152:155],v[188:191],a[248:251]\n"
"ds_read_b64_tr_b16 v[246:247],v103 offset:17408\n"
"s_waitcnt lgkmcnt(8)\n"
"v_mfma_f32_16x16x32_bf16 a[0:3],v[128:131],v[160:163],a[0:3]\n"
"ds_read_b64_tr_b16 v[248:249],v104 offset:16384\n"
"v_mfma_f32_16x16x32_bf16 a[0:3],v[192:195],v[224:227],a[0:3]\n"
"ds_read_b64_tr_b16 v[250:251],v104 offset:17408\n"
"v_mfma_f32_16x16x32_bf16 a[8:11],v[192:195],v[228:231],a[8:11]\n"
"ds_read_b64_tr_b16 v[252:253],v105 offset:16384\n"
"v_mfma_f32_16x16x32_bf16 a[8:11],v[128:131],v[164:167],a[8:11]\n"
"ds_read_b64_tr_b16 v[254:255],v105 offset:17408\n"
"s_waitcnt lgkmcnt(8)\n"
"v_mfma_f32_16x16x32_bf16 a[16:19],v[128:131],v[168:171],a[16:19]\n"
"ds_read_b128 v[132:135],v80 offset:34816\n"
"v_mfma_f32_16x16x32_bf16 a[16:19],v[192:195],v[232:235],a[16:19]\n"
"ds_read_b128 v[196:199],v81 offset:34816\n"
"v_mfma_f32_16x16x32_bf16 a[24:27],v[192:195],v[236:239],a[24:27]\n"
"ds_read_b128 v[136:139],v80 offset:36864\n"
"v_mfma_f32_16x16x32_bf16 a[24:27],v[128:131],v[172:175],a[24:27]\n"
"ds_read_b128 v[200:203],v81 offset:36864\n"
"s_waitcnt lgkmcnt(8)\n"
"v_mfma_f32_16x16x32_bf16 a[32:35],v[128:131],v[176:179],a[32:35]\n"
"s_and_b32 s82,s52,1\n"
"ds_read_b128 v[140:143],v80 offset:38912\n"
"s_lshl_b32 s82,s82,13\n"
"v_mfma_f32_16x16x32_bf16 a[32:35],v[192:195],v[240:243],a[32:35]\n"
"ds_read_b128 v[204:207],v81 offset:38912\n"
"v_mfma_f32_16x16x32_bf16 a[40:43],v[192:195],v[244:247],a[40:43]\n"
"ds_read_b128 v[144:147],v80 offset:40960\n"
"v_mfma_f32_16x16x32_bf16 a[40:43],v[128:131],v[180:183],a[40:43]\n"
"ds_read_b128 v[208:211],v81 offset:40960\n"
"s_waitcnt lgkmcnt(8)\n"
"v_mfma_f32_16x16x32_bf16 a[48:51],v[128:131],v[184:187],a[48:51]\n"
"ds_read_b128 v[148:151],v80 offset:43008\n"
"v_mfma_f32_16x16x32_bf16 a[48:51],v[192:195],v[248:251],a[48:51]\n"
"ds_read_b128 v[212:215],v81 offset:43008\n"
"v_subrev_u32 v98,s82,v98\n"
"v_mfma_f32_16x16x32_bf16 a[56:59],v[192:195],v[252:255],a[56:59]\n"
"ds_read_b128 v[216:219],v81 offset:45056\n"
"v_add_u32 v96,s78,v97\n"
"v_mfma_f32_16x16x32_bf16 a[56:59],v[128:131],v[188:191],a[56:59]\n"
"ds_read_b128 v[220:223],v81 offset:47104\n"
"s_waitcnt lgkmcnt(10)\n"
"v_mfma_f32_16x16x32_bf16 a[4:7],v[132:135],v[160:163],a[4:7]\n"
"v_add_u32 v97,65536,v96\n"
"v_mfma_f32_16x16x32_bf16 a[4:7],v[196:199],v[224:227],a[4:7]\n"
"v_subrev_u32 v99,s82,v99\n"
"v_mfma_f32_16x16x32_bf16 a[12:15],v[196:199],v[228:231],a[12:15]\n"
"v_mfma_f32_16x16x32_bf16 a[12:15],v[132:135],v[164:167],a[12:15]\n"
"v_mfma_f32_16x16x32_bf16 a[20:23],v[132:135],v[168:171],a[20:23]\n"
"s_waitcnt lgkmcnt(0)\n"
"s_barrier\n"
"v_mfma_f32_16x16x32_bf16 a[20:23],v[196:199],v[232:235],a[20:23]\n"
"v_subrev_u32 v100,s82,v100\n"
"s_add_u32 m0,s85,98304\n"
"v_mfma_f32_16x16x32_bf16 a[28:31],v[196:199],v[236:239],a[28:31]\n"
"buffer_load_dwordx4 v110,s[28:31],s86 offen lds\n"
"v_mfma_f32_16x16x32_bf16 a[28:31],v[132:135],v[172:175],a[28:31]\n"
"v_mfma_f32_16x16x32_bf16 a[36:39],v[132:135],v[176:179],a[36:39]\n"
"v_mfma_f32_16x16x32_bf16 a[36:39],v[196:199],v[240:243],a[36:39]\n"
"v_subrev_u32 v101,s82,v101\n"
"s_add_u32 m0,s85,99328\n"
"v_mfma_f32_16x16x32_bf16 a[44:47],v[196:199],v[244:247],a[44:47]\n"
"buffer_load_dwordx4 v111,s[28:31],s86 offen lds\n"
"v_mfma_f32_16x16x32_bf16 a[44:47],v[132:135],v[180:183],a[44:47]\n"
"s_xor_b32 s86,s86,262144\n"
"v_mfma_f32_16x16x32_bf16 a[52:55],v[132:135],v[184:187],a[52:55]\n"
"v_mfma_f32_16x16x32_bf16 a[52:55],v[196:199],v[248:251],a[52:55]\n"
"v_subrev_u32 v102,s82,v102\n"
"s_add_u32 m0,s78,32768\n"
"v_mfma_f32_16x16x32_bf16 a[60:63],v[196:199],v[252:255],a[60:63]\n"
"buffer_load_dwordx4 v112,s[24:27],s76 offen offset:0 sc1 lds\n"
"v_mfma_f32_16x16x32_bf16 a[60:63],v[132:135],v[188:191],a[60:63]\n"
"v_mfma_f32_16x16x32_bf16 a[64:67],v[136:139],v[160:163],a[64:67]\n"
"v_mfma_f32_16x16x32_bf16 a[64:67],v[200:203],v[224:227],a[64:67]\n"
"v_subrev_u32 v103,s82,v103\n"
"v_mfma_f32_16x16x32_bf16 a[72:75],v[200:203],v[228:231],a[72:75]\n"
"buffer_load_dwordx4 v112,s[24:27],s76 offen offset:1024 sc1 lds\n"
"v_mfma_f32_16x16x32_bf16 a[72:75],v[136:139],v[164:167],a[72:75]\n"
"v_mfma_f32_16x16x32_bf16 a[80:83],v[136:139],v[168:171],a[80:83]\n"
"v_accvgpr_read_b32 v116,a0\n"
"v_accvgpr_read_b32 v117,a1\n"
"v_mfma_f32_16x16x32_bf16 a[80:83],v[200:203],v[232:235],a[80:83]\n"
"v_subrev_u32 v104,s82,v104\n"
"v_accvgpr_read_b32 v118,a2\n"
"v_mfma_f32_16x16x32_bf16 a[88:91],v[200:203],v[236:239],a[88:91]\n"
"buffer_load_dwordx4 v112,s[24:27],s76 offen offset:2048 sc1 lds\n"
"v_mfma_f32_16x16x32_bf16 a[88:91],v[136:139],v[172:175],a[88:91]\n"
"v_accvgpr_read_b32 v119,a3\n"
"v_accvgpr_read_b32 v120,a4\n"
"v_mfma_f32_16x16x32_bf16 a[96:99],v[136:139],v[176:179],a[96:99]\n"
"v_accvgpr_read_b32 v121,a5\n"
"v_accvgpr_read_b32 v122,a6\n"
"v_mfma_f32_16x16x32_bf16 a[96:99],v[200:203],v[240:243],a[96:99]\n"
"v_subrev_u32 v105,s82,v105\n"
"v_accvgpr_read_b32 v123,a7\n"
"v_mfma_f32_16x16x32_bf16 a[104:107],v[200:203],v[244:247],a[104:107]\n"
"buffer_load_dwordx4 v112,s[24:27],s76 offen offset:3072 sc1 lds\n"
"v_mfma_f32_16x16x32_bf16 a[104:107],v[136:139],v[180:183],a[104:107]\n"
"s_xor_b32 s76,s76,16384\n"
"v_cvt_pk_bf16_f32 v116,v116,v117\n"
"v_cvt_pk_bf16_f32 v117,v118,v119\n"
"v_mfma_f32_16x16x32_bf16 a[112:115],v[136:139],v[184:187],a[112:115]\n"
"v_cvt_pk_bf16_f32 v118,v120,v121\n"
"v_cvt_pk_bf16_f32 v119,v122,v123\n"
"v_mfma_f32_16x16x32_bf16 a[112:115],v[200:203],v[248:251],a[112:115]\n"
"buffer_store_dwordx4 v[116:119],v112,s[24:27],s56 offen offset:0 sc1\n"
"v_mfma_f32_16x16x32_bf16 a[120:123],v[200:203],v[252:255],a[120:123]\n"
"s_nop 0\n"
"v_accvgpr_read_b32 v116,a8\n"
"v_accvgpr_read_b32 v117,a9\n"
"v_accvgpr_read_b32 v118,a10\n"
"v_mfma_f32_16x16x32_bf16 a[120:123],v[136:139],v[188:191],a[120:123]\n"
"s_waitcnt vmcnt(20)\n"
"v_accvgpr_read_b32 v119,a11\n"
"v_accvgpr_read_b32 v120,a12\n"
"s_barrier\n"
"v_mfma_f32_16x16x32_bf16 a[68:71],v[140:143],v[160:163],a[68:71]\n"
"ds_read_b128 v[0:3],v97 offset:0\n"
"v_accvgpr_read_b32 v121,a13\n"
"v_mfma_f32_16x16x32_bf16 a[68:71],v[204:207],v[224:227],a[68:71]\n"
"ds_read_b128 v[8:11],v97 offset:1024\n"
"v_accvgpr_read_b32 v122,a14\n"
"v_mfma_f32_16x16x32_bf16 a[76:79],v[204:207],v[228:231],a[76:79]\n"
"ds_read_b128 v[16:19],v97 offset:2048\n"
"v_accvgpr_read_b32 v123,a15\n"
"v_mfma_f32_16x16x32_bf16 a[76:79],v[140:143],v[164:167],a[76:79]\n"
"ds_read_b128 v[24:27],v97 offset:3072\n"
"v_cvt_pk_bf16_f32 v116,v116,v117\n"
"v_mfma_f32_16x16x32_bf16 a[84:87],v[140:143],v[168:171],a[84:87]\n"
"ds_read_b128 v[4:7],v97 offset:16384\n"
"v_cvt_pk_bf16_f32 v117,v118,v119\n"
"v_mfma_f32_16x16x32_bf16 a[84:87],v[204:207],v[232:235],a[84:87]\n"
"ds_read_b128 v[12:15],v97 offset:17408\n"
"v_cvt_pk_bf16_f32 v118,v120,v121\n"
"v_mfma_f32_16x16x32_bf16 a[92:95],v[204:207],v[236:239],a[92:95]\n"
"ds_read_b128 v[20:23],v97 offset:18432\n"
"v_cvt_pk_bf16_f32 v119,v122,v123\n"
"v_mfma_f32_16x16x32_bf16 a[92:95],v[140:143],v[172:175],a[92:95]\n"
"ds_read_b128 v[28:31],v97 offset:19456\n"
"buffer_store_dwordx4 v[116:119],v112,s[24:27],s56 offen offset:1024 sc1\n"
"v_mfma_f32_16x16x32_bf16 a[100:103],v[140:143],v[176:179],a[100:103]\n"
"ds_read_b64_tr_b16 v[32:33],v98 offset:49152\n"
"v_accvgpr_read_b32 v116,a16\n"
"v_mfma_f32_16x16x32_bf16 a[100:103],v[204:207],v[240:243],a[100:103]\n"
"ds_read_b64_tr_b16 v[34:35],v98 offset:50176\n"
"v_accvgpr_read_b32 v117,a17\n"
"v_mfma_f32_16x16x32_bf16 a[108:111],v[204:207],v[244:247],a[108:111]\n"
"ds_read_b64_tr_b16 v[36:37],v98 offset:57344\n"
"v_accvgpr_read_b32 v118,a18\n"
"v_mfma_f32_16x16x32_bf16 a[108:111],v[140:143],v[180:183],a[108:111]\n"
"ds_read_b64_tr_b16 v[38:39],v98 offset:58368\n"
"v_accvgpr_read_b32 v119,a19\n"
"v_mfma_f32_16x16x32_bf16 a[116:119],v[140:143],v[184:187],a[116:119]\n"
"ds_read_b64_tr_b16 v[40:41],v99 offset:49152\n"
"v_accvgpr_read_b32 v120,a20\n"
"v_mfma_f32_16x16x32_bf16 a[116:119],v[204:207],v[248:251],a[116:119]\n"
"ds_read_b64_tr_b16 v[42:43],v99 offset:50176\n"
"v_accvgpr_read_b32 v121,a21\n"
"v_mfma_f32_16x16x32_bf16 a[124:127],v[204:207],v[252:255],a[124:127]\n"
"ds_read_b64_tr_b16 v[44:45],v99 offset:57344\n"
"v_accvgpr_read_b32 v122,a22\n"
"v_mfma_f32_16x16x32_bf16 a[124:127],v[140:143],v[188:191],a[124:127]\n"
"ds_read_b64_tr_b16 v[46:47],v99 offset:58368\n"
"v_accvgpr_read_b32 v123,a23\n"
"v_mfma_f32_16x16x32_bf16 a[128:131],v[144:147],v[160:163],a[128:131]\n"
"ds_read_b64_tr_b16 v[48:49],v100 offset:49152\n"
"v_cvt_pk_bf16_f32 v116,v116,v117\n"
"v_mfma_f32_16x16x32_bf16 a[128:131],v[208:211],v[224:227],a[128:131]\n"
"ds_read_b64_tr_b16 v[50:51],v100 offset:50176\n"
"v_cvt_pk_bf16_f32 v117,v118,v119\n"
"v_mfma_f32_16x16x32_bf16 a[136:139],v[208:211],v[228:231],a[136:139]\n"
"ds_read_b64_tr_b16 v[52:53],v100 offset:57344\n"
"v_cvt_pk_bf16_f32 v118,v120,v121\n"
"v_mfma_f32_16x16x32_bf16 a[136:139],v[144:147],v[164:167],a[136:139]\n"
"ds_read_b64_tr_b16 v[54:55],v100 offset:58368\n"
"v_cvt_pk_bf16_f32 v119,v122,v123\n"
"v_mfma_f32_16x16x32_bf16 a[144:147],v[144:147],v[168:171],a[144:147]\n"
"ds_read_b64_tr_b16 v[56:57],v101 offset:49152\n"
"buffer_store_dwordx4 v[116:119],v112,s[24:27],s56 offen offset:2048 sc1\n"
"v_mfma_f32_16x16x32_bf16 a[144:147],v[208:211],v[232:235],a[144:147]\n"
"ds_read_b64_tr_b16 v[58:59],v101 offset:50176\n"
"v_accvgpr_read_b32 v116,a24\n"
"v_mfma_f32_16x16x32_bf16 a[152:155],v[208:211],v[236:239],a[152:155]\n"
"ds_read_b64_tr_b16 v[60:61],v101 offset:57344\n"
"v_accvgpr_read_b32 v117,a25\n"
"v_mfma_f32_16x16x32_bf16 a[152:155],v[144:147],v[172:175],a[152:155]\n"
"ds_read_b64_tr_b16 v[62:63],v101 offset:58368\n"
"v_accvgpr_read_b32 v118,a26\n"
"v_mfma_f32_16x16x32_bf16 a[160:163],v[144:147],v[176:179],a[160:163]\n"
"ds_read_b64_tr_b16 v[64:65],v102 offset:49152\n"
"v_accvgpr_read_b32 v119,a27\n"
"v_mfma_f32_16x16x32_bf16 a[160:163],v[208:211],v[240:243],a[160:163]\n"
"ds_read_b64_tr_b16 v[66:67],v102 offset:50176\n"
"v_accvgpr_read_b32 v120,a28\n"
"v_mfma_f32_16x16x32_bf16 a[168:171],v[208:211],v[244:247],a[168:171]\n"
"ds_read_b64_tr_b16 v[68:69],v102 offset:57344\n"
"v_accvgpr_read_b32 v121,a29\n"
"v_mfma_f32_16x16x32_bf16 a[168:171],v[144:147],v[180:183],a[168:171]\n"
"ds_read_b64_tr_b16 v[70:71],v102 offset:58368\n"
"v_accvgpr_read_b32 v122,a30\n"
"v_mfma_f32_16x16x32_bf16 a[176:179],v[144:147],v[184:187],a[176:179]\n"
"ds_read_b64_tr_b16 v[72:73],v103 offset:49152\n"
"v_accvgpr_read_b32 v123,a31\n"
"v_mfma_f32_16x16x32_bf16 a[176:179],v[208:211],v[248:251],a[176:179]\n"
"ds_read_b64_tr_b16 v[74:75],v103 offset:50176\n"
"v_cvt_pk_bf16_f32 v116,v116,v117\n"
"v_mfma_f32_16x16x32_bf16 a[184:187],v[208:211],v[252:255],a[184:187]\n"
"ds_read_b64_tr_b16 v[76:77],v103 offset:57344\n"
"v_cvt_pk_bf16_f32 v117,v118,v119\n"
"v_mfma_f32_16x16x32_bf16 a[184:187],v[144:147],v[188:191],a[184:187]\n"
"ds_read_b64_tr_b16 v[78:79],v103 offset:58368\n"
"v_cvt_pk_bf16_f32 v118,v120,v121\n"
"v_mfma_f32_16x16x32_bf16 a[132:135],v[148:151],v[160:163],a[132:135]\n"
"ds_read_b64_tr_b16 v[80:81],v104 offset:49152\n"
"v_cvt_pk_bf16_f32 v119,v122,v123\n"
"v_mfma_f32_16x16x32_bf16 a[132:135],v[212:215],v[224:227],a[132:135]\n"
"ds_read_b64_tr_b16 v[82:83],v104 offset:50176\n"
"buffer_store_dwordx4 v[116:119],v112,s[24:27],s56 offen offset:3072 sc1\n"
"v_mfma_f32_16x16x32_bf16 a[140:143],v[212:215],v[228:231],a[140:143]\n"
"ds_read_b64_tr_b16 v[84:85],v104 offset:57344\n"
"v_mfma_f32_16x16x32_bf16 a[140:143],v[148:151],v[164:167],a[140:143]\n"
"ds_read_b64_tr_b16 v[86:87],v104 offset:58368\n"
"v_mfma_f32_16x16x32_bf16 a[148:151],v[148:151],v[168:171],a[148:151]\n"
"ds_read_b64_tr_b16 v[88:89],v105 offset:49152\n"
"v_mfma_f32_16x16x32_bf16 a[148:151],v[212:215],v[232:235],a[148:151]\n"
"ds_read_b64_tr_b16 v[90:91],v105 offset:50176\n"
"v_mfma_f32_16x16x32_bf16 a[156:159],v[212:215],v[236:239],a[156:159]\n"
"ds_read_b64_tr_b16 v[92:93],v105 offset:57344\n"
"v_mfma_f32_16x16x32_bf16 a[156:159],v[148:151],v[172:175],a[156:159]\n"
"ds_read_b64_tr_b16 v[94:95],v105 offset:58368\n"
"v_mfma_f32_16x16x32_bf16 a[164:167],v[148:151],v[176:179],a[164:167]\n"
"s_add_u32 m0,s85,106496\n"
"v_mfma_f32_16x16x32_bf16 a[164:167],v[212:215],v[240:243],a[164:167]\n"
"buffer_load_dwordx4 v110,s[28:31],s86 offen lds\n"
"v_mfma_f32_16x16x32_bf16 a[172:175],v[212:215],v[244:247],a[172:175]\n"
"v_mfma_f32_16x16x32_bf16 a[172:175],v[148:151],v[180:183],a[172:175]\n"
"s_add_u32 m0,s85,107520\n"
"v_mfma_f32_16x16x32_bf16 a[180:183],v[148:151],v[184:187],a[180:183]\n"
"buffer_load_dwordx4 v111,s[28:31],s86 offen lds\n"
"v_mfma_f32_16x16x32_bf16 a[180:183],v[212:215],v[248:251],a[180:183]\n"
"s_xor_b32 s86,s86,786432\n"
"v_mfma_f32_16x16x32_bf16 a[188:191],v[212:215],v[252:255],a[188:191]\n"
"s_add_u32 m0,s78,49152\n"
"v_mfma_f32_16x16x32_bf16 a[188:191],v[148:151],v[188:191],a[188:191]\n"
"buffer_load_dwordx4 v112,s[24:27],s76 offen offset:0 sc1 lds\n"
"v_mfma_f32_16x16x32_bf16 a[192:195],v[216:219],v[224:227],a[192:195]\n"
"v_mfma_f32_16x16x32_bf16 a[196:199],v[220:223],v[224:227],a[196:199]\n"
"v_mfma_f32_16x16x32_bf16 a[204:207],v[220:223],v[228:231],a[204:207]\n"
"buffer_load_dwordx4 v112,s[24:27],s76 offen offset:1024 sc1 lds\n"
"v_mfma_f32_16x16x32_bf16 a[200:203],v[216:219],v[228:231],a[200:203]\n"
"v_mfma_f32_16x16x32_bf16 a[208:211],v[216:219],v[232:235],a[208:211]\n"
"v_mfma_f32_16x16x32_bf16 a[212:215],v[220:223],v[232:235],a[212:215]\n"
"buffer_load_dwordx4 v112,s[24:27],s76 offen offset:2048 sc1 lds\n"
"v_mfma_f32_16x16x32_bf16 a[220:223],v[220:223],v[236:239],a[220:223]\n"
"v_mfma_f32_16x16x32_bf16 a[216:219],v[216:219],v[236:239],a[216:219]\n"
"buffer_load_dwordx4 v112,s[24:27],s76 offen offset:3072 sc1 lds\n"
"v_mfma_f32_16x16x32_bf16 a[224:227],v[216:219],v[240:243],a[224:227]\n"
"s_xor_b32 s76,s76,49152\n"
"v_mfma_f32_16x16x32_bf16 a[228:231],v[220:223],v[240:243],a[228:231]\n"
"v_mfma_f32_16x16x32_bf16 a[236:239],v[220:223],v[244:247],a[236:239]\n"
"v_mfma_f32_16x16x32_bf16 a[232:235],v[216:219],v[244:247],a[232:235]\n"
"v_mfma_f32_16x16x32_bf16 a[240:243],v[216:219],v[248:251],a[240:243]\n"
"v_mfma_f32_16x16x32_bf16 a[244:247],v[220:223],v[248:251],a[244:247]\n"
"v_mfma_f32_16x16x32_bf16 a[252:255],v[220:223],v[252:255],a[252:255]\n"
"v_mfma_f32_16x16x32_bf16 a[248:251],v[216:219],v[252:255],a[248:251]\n"
".Lfk_ph2:\n"
"s_waitcnt vmcnt(25) lgkmcnt(0)\n"
"s_barrier\n"
"v_mfma_f32_16x16x32_bf16 v[128:131],v[32:35],v[0:3],0\n"
"v_accvgpr_read_b32 v116,a32\n"
"v_accvgpr_read_b32 v117,a33\n"
"v_mfma_f32_16x16x32_bf16 v[128:131],v[36:39],v[4:7],v[128:131]\n"
"v_accvgpr_read_b32 v118,a34\n"
"v_accvgpr_read_b32 v119,a35\n"
"v_mfma_f32_16x16x32_bf16 v[160:163],v[36:39],v[12:15],0\n"
"v_accvgpr_read_b32 v120,a36\n"
"v_accvgpr_read_b32 v121,a37\n"
"v_mfma_f32_16x16x32_bf16 v[160:163],v[32:35],v[8:11],v[160:163]\n"
"s_add_u32 m0,s85,147456\n"
"v_accvgpr_read_b32 v122,a38\n"
"v_mfma_f32_16x16x32_bf16 v[192:195],v[32:35],v[16:19],0\n"
"buffer_load_dwordx4 v110,s[28:31],s86 offen lds\n"
"v_mfma_f32_16x16x32_bf16 v[192:195],v[36:39],v[20:23],v[192:195]\n"
"v_accvgpr_read_b32 v123,a39\n"
"v_cvt_pk_bf16_f32 v116,v116,v117\n"
"v_mfma_f32_16x16x32_bf16 v[224:227],v[36:39],v[28:31],0\n"
"v_cvt_pk_bf16_f32 v117,v118,v119\n"
"v_cvt_pk_bf16_f32 v118,v120,v121\n"
"v_mfma_f32_16x16x32_bf16 v[224:227],v[32:35],v[24:27],v[224:227]\n"
"v_cvt_pk_bf16_f32 v119,v122,v123\n"
"s_add_u32 s56,s56,4096\n"
"v_mfma_f32_16x16x32_bf16 v[228:231],v[40:43],v[24:27],0\n"
"ds_read_b64_tr_b16 v[32:33],v100 offset:32768\n"
"buffer_store_dwordx4 v[116:119],v112,s[24:27],s56 offen offset:0 sc1\n"
"v_mfma_f32_16x16x32_bf16 v[228:231],v[44:47],v[28:31],v[228:231]\n"
"ds_read_b64_tr_b16 v[34:35],v100 offset:33792\n"
"v_accvgpr_read_b32 v116,a40\n"
"v_mfma_f32_16x16x32_bf16 v[196:199],v[44:47],v[20:23],0\n"
"ds_read_b64_tr_b16 v[36:37],v100 offset:40960\n"
"v_accvgpr_read_b32 v117,a41\n"
"v_mfma_f32_16x16x32_bf16 v[196:199],v[40:43],v[16:19],v[196:199]\n"
"s_add_u32 m0,s85,148480\n"
"ds_read_b64_tr_b16 v[38:39],v100 offset:41984\n"
"v_mfma_f32_16x16x32_bf16 v[164:167],v[40:43],v[8:11],0\n"
"buffer_load_dwordx4 v111,s[28:31],s86 offen lds\n"
"v_mfma_f32_16x16x32_bf16 v[164:167],v[44:47],v[12:15],v[164:167]\n"
"s_add_u32 m0,s85,155648\n"
"s_xor_b32 s86,s86,262144\n"
"v_mfma_f32_16x16x32_bf16 v[132:135],v[44:47],v[4:7],0\n"
"buffer_load_dwordx4 v110,s[28:31],s86 offen lds\n"
"v_mfma_f32_16x16x32_bf16 v[132:135],v[40:43],v[0:3],v[132:135]\n"
"v_accvgpr_read_b32 v118,a42\n"
"v_accvgpr_read_b32 v119,a43\n"
"v_mfma_f32_16x16x32_bf16 v[136:139],v[48:51],v[0:3],0\n"
"ds_read_b64_tr_b16 v[40:41],v101 offset:32768\n"
"v_accvgpr_read_b32 v120,a44\n"
"v_mfma_f32_16x16x32_bf16 v[136:139],v[52:55],v[4:7],v[136:139]\n"
"ds_read_b64_tr_b16 v[42:43],v101 offset:33792\n"
"v_accvgpr_read_b32 v121,a45\n"
"v_mfma_f32_16x16x32_bf16 v[168:171],v[52:55],v[12:15],0\n"
"ds_read_b64_tr_b16 v[44:45],v101 offset:40960\n"
"v_accvgpr_read_b32 v122,a46\n"
"v_mfma_f32_16x16x32_bf16 v[168:171],v[48:51],v[8:11],v[168:171]\n"
"s_add_u32 m0,s85,156672\n"
"ds_read_b64_tr_b16 v[46:47],v101 offset:41984\n"
"v_mfma_f32_16x16x32_bf16 v[200:203],v[48:51],v[16:19],0\n"
"buffer_load_dwordx4 v111,s[28:31],s86 offen lds\n"
"v_mfma_f32_16x16x32_bf16 v[200:203],v[52:55],v[20:23],v[200:203]\n"
"s_xor_b32 s86,s86,3932160\n"
"v_accvgpr_read_b32 v123,a47\n"
"v_mfma_f32_16x16x32_bf16 v[232:235],v[52:55],v[28:31],0\n"
"v_cvt_pk_bf16_f32 v116,v116,v117\n"
"v_cvt_pk_bf16_f32 v117,v118,v119\n"
"v_mfma_f32_16x16x32_bf16 v[232:235],v[48:51],v[24:27],v[232:235]\n"
"v_cvt_pk_bf16_f32 v118,v120,v121\n"
"v_cvt_pk_bf16_f32 v119,v122,v123\n"
"v_mfma_f32_16x16x32_bf16 v[236:239],v[56:59],v[24:27],0\n"
"ds_read_b64_tr_b16 v[48:49],v102 offset:32768\n"
"buffer_store_dwordx4 v[116:119],v112,s[24:27],s56 offen offset:1024 sc1\n"
"v_mfma_f32_16x16x32_bf16 v[236:239],v[60:63],v[28:31],v[236:239]\n"
"ds_read_b64_tr_b16 v[50:51],v102 offset:33792\n"
"v_accvgpr_read_b32 v116,a48\n"
"v_mfma_f32_16x16x32_bf16 v[204:207],v[60:63],v[20:23],0\n"
"ds_read_b64_tr_b16 v[52:53],v102 offset:40960\n"
"v_accvgpr_read_b32 v117,a49\n"
"v_mfma_f32_16x16x32_bf16 v[204:207],v[56:59],v[16:19],v[204:207]\n"
"s_add_u32 m0,s78,65536\n"
"ds_read_b64_tr_b16 v[54:55],v102 offset:41984\n"
"v_mfma_f32_16x16x32_bf16 v[172:175],v[56:59],v[8:11],0\n"
"buffer_load_dwordx4 v112,s[24:27],s76 offen offset:0 sc1 lds\n"
"v_mfma_f32_16x16x32_bf16 v[172:175],v[60:63],v[12:15],v[172:175]\n"
"v_accvgpr_read_b32 v118,a50\n"
"v_mfma_f32_16x16x32_bf16 v[140:143],v[60:63],v[4:7],0\n"
"buffer_load_dwordx4 v112,s[24:27],s76 offen offset:1024 sc1 lds\n"
"v_mfma_f32_16x16x32_bf16 v[140:143],v[56:59],v[0:3],v[140:143]\n"
"s_waitcnt vmcnt(25)\n"
"v_mfma_f32_16x16x32_bf16 v[144:147],v[64:67],v[0:3],0\n"
"ds_read_b64_tr_b16 a[32:33],v98 offset:32768\n"
"ds_read_b64_tr_b16 v[56:57],v103 offset:32768\n"
"v_mfma_f32_16x16x32_bf16 v[144:147],v[68:71],v[4:7],v[144:147]\n"
"ds_read_b64_tr_b16 a[34:35],v98 offset:33792\n"
"ds_read_b64_tr_b16 v[58:59],v103 offset:33792\n"
"v_mfma_f32_16x16x32_bf16 v[176:179],v[68:71],v[12:15],0\n"
"ds_read_b64_tr_b16 a[36:37],v98 offset:40960\n"
"ds_read_b64_tr_b16 v[60:61],v103 offset:40960\n"
"v_mfma_f32_16x16x32_bf16 v[176:179],v[64:67],v[8:11],v[176:179]\n"
"ds_read_b64_tr_b16 a[38:39],v98 offset:41984\n"
"ds_read_b64_tr_b16 v[62:63],v103 offset:41984\n"
"v_mfma_f32_16x16x32_bf16 v[208:211],v[64:67],v[16:19],0\n"
"buffer_load_dwordx4 v112,s[24:27],s76 offen offset:2048 sc1 lds\n"
"v_mfma_f32_16x16x32_bf16 v[208:211],v[68:71],v[20:23],v[208:211]\n"
"ds_read_b128 a[0:3],v96 offset:0\n"
"v_accvgpr_read_b32 v119,a51\n"
"v_mfma_f32_16x16x32_bf16 v[240:243],v[68:71],v[28:31],0\n"
"v_accvgpr_read_b32 v120,a52\n"
"v_accvgpr_read_b32 v121,a53\n"
"v_mfma_f32_16x16x32_bf16 v[240:243],v[64:67],v[24:27],v[240:243]\n"
"ds_read_b128 a[8:11],v96 offset:1024\n"
"v_accvgpr_read_b32 v122,a54\n"
"v_mfma_f32_16x16x32_bf16 v[244:247],v[72:75],v[24:27],0\n"
"ds_read_b64_tr_b16 a[40:41],v99 offset:32768\n"
"ds_read_b64_tr_b16 v[64:65],v104 offset:32768\n"
"v_mfma_f32_16x16x32_bf16 v[244:247],v[76:79],v[28:31],v[244:247]\n"
"ds_read_b64_tr_b16 a[42:43],v99 offset:33792\n"
"ds_read_b64_tr_b16 v[66:67],v104 offset:33792\n"
"v_mfma_f32_16x16x32_bf16 v[212:215],v[76:79],v[20:23],0\n"
"ds_read_b64_tr_b16 a[44:45],v99 offset:40960\n"
"ds_read_b64_tr_b16 v[68:69],v104 offset:40960\n"
"v_mfma_f32_16x16x32_bf16 v[212:215],v[72:75],v[16:19],v[212:215]\n"
"ds_read_b64_tr_b16 a[46:47],v99 offset:41984\n"
"ds_read_b64_tr_b16 v[70:71],v104 offset:41984\n"
"v_mfma_f32_16x16x32_bf16 v[180:183],v[72:75],v[8:11],0\n"
"buffer_load_dwordx4 v112,s[24:27],s76 offen offset:3072 sc1 lds\n"
"v_mfma_f32_16x16x32_bf16 v[180:183],v[76:79],v[12:15],v[180:183]\n"
"s_add_u32 m0,s78,81920\n"
"s_xor_b32 s76,s76,16384\n"
"ds_read_b128 a[16:19],v96 offset:2048\n"
"v_mfma_f32_16x16x32_bf16 v[148:151],v[76:79],v[4:7],0\n"
"buffer_load_dwordx4 v112,s[24:27],s76 offen offset:0 sc1 lds\n"
"v_mfma_f32_16x16x32_bf16 v[148:151],v[72:75],v[0:3],v[148:151]\n"
"ds_read_b128 a[24:27],v96 offset:3072\n"
"v_accvgpr_read_b32 v123,a55\n"
"v_mfma_f32_16x16x32_bf16 v[152:155],v[80:83],v[0:3],0\n"
"ds_read_b64_tr_b16 v[72:73],v105 offset:32768\n"
"v_cvt_pk_bf16_f32 v116,v116,v117\n"
"v_mfma_f32_16x16x32_bf16 v[152:155],v[84:87],v[4:7],v[152:155]\n"
"ds_read_b64_tr_b16 v[74:75],v105 offset:33792\n"
"v_cvt_pk_bf16_f32 v117,v118,v119\n"
"v_mfma_f32_16x16x32_bf16 v[184:187],v[84:87],v[12:15],0\n"
"ds_read_b64_tr_b16 v[76:77],v105 offset:40960\n"
"v_cvt_pk_bf16_f32 v118,v120,v121\n"
"v_mfma_f32_16x16x32_bf16 v[184:187],v[80:83],v[8:11],v[184:187]\n"
"ds_read_b64_tr_b16 v[78:79],v105 offset:41984\n"
"v_cvt_pk_bf16_f32 v119,v122,v123\n"
"v_mfma_f32_16x16x32_bf16 v[216:219],v[80:83],v[16:19],0\n"
"ds_read_b128 a[4:7],v96 offset:16384\n"
"buffer_store_dwordx4 v[116:119],v112,s[24:27],s56 offen offset:2048 sc1\n"
"v_mfma_f32_16x16x32_bf16 v[216:219],v[84:87],v[20:23],v[216:219]\n"
"ds_read_b128 a[12:15],v96 offset:17408\n"
"v_accvgpr_read_b32 v116,a56\n"
"v_mfma_f32_16x16x32_bf16 v[248:251],v[84:87],v[28:31],0\n"
"ds_read_b128 a[20:23],v96 offset:18432\n"
"v_accvgpr_read_b32 v117,a57\n"
"v_mfma_f32_16x16x32_bf16 v[248:251],v[80:83],v[24:27],v[248:251]\n"
"ds_read_b128 a[28:31],v96 offset:19456\n"
"v_accvgpr_read_b32 v118,a58\n"
"v_mfma_f32_16x16x32_bf16 v[252:255],v[88:91],v[24:27],0\n"
"buffer_load_dwordx4 v112,s[24:27],s76 offen offset:1024 sc1 lds\n"
"v_mfma_f32_16x16x32_bf16 v[252:255],v[92:95],v[28:31],v[252:255]\n"
"v_accvgpr_read_b32 v119,a59\n"
"v_accvgpr_read_b32 v120,a60\n"
"v_mfma_f32_16x16x32_bf16 v[220:223],v[92:95],v[20:23],0\n"
"v_accvgpr_read_b32 v121,a61\n"
"v_accvgpr_read_b32 v122,a62\n"
"v_mfma_f32_16x16x32_bf16 v[220:223],v[88:91],v[16:19],v[220:223]\n"
"buffer_load_dwordx4 v112,s[24:27],s76 offen offset:2048 sc1 lds\n"
"v_mfma_f32_16x16x32_bf16 v[188:191],v[88:91],v[8:11],0\n"
"v_accvgpr_read_b32 v123,a63\n"
"v_cvt_pk_bf16_f32 v116,v116,v117\n"
"v_mfma_f32_16x16x32_bf16 v[188:191],v[92:95],v[12:15],v[188:191]\n"
"v_cvt_pk_bf16_f32 v117,v118,v119\n"
"v_cvt_pk_bf16_f32 v118,v120,v121\n"
"v_mfma_f32_16x16x32_bf16 v[156:159],v[92:95],v[4:7],0\n"
"buffer_load_dwordx4 v112,s[24:27],s76 offen offset:3072 sc1 lds\n"
"v_mfma_f32_16x16x32_bf16 v[156:159],v[88:91],v[0:3],v[156:159]\n"
"s_xor_b32 s76,s76,245760\n"
"v_cvt_pk_bf16_f32 v119,v122,v123\n"
"s_waitcnt vmcnt(19) lgkmcnt(0)\n"
"s_barrier\n"
"v_mfma_f32_16x16x32_bf16 v[128:131],a[32:35],a[0:3],v[128:131]\n"
"ds_read_b64_tr_b16 v[80:81],v98 offset:0\n"
"ds_read_b64_tr_b16 v[88:89],v99 offset:0\n"
"v_mfma_f32_16x16x32_bf16 v[128:131],a[36:39],a[4:7],v[128:131]\n"
"ds_read_b64_tr_b16 v[82:83],v98 offset:1024\n"
"ds_read_b64_tr_b16 v[90:91],v99 offset:1024\n"
"v_mfma_f32_16x16x32_bf16 v[160:163],a[36:39],a[12:15],v[160:163]\n"
"ds_read_b64_tr_b16 v[84:85],v98 offset:8192\n"
"ds_read_b64_tr_b16 v[92:93],v99 offset:8192\n"
"v_mfma_f32_16x16x32_bf16 v[160:163],a[32:35],a[8:11],v[160:163]\n"
"s_add_u32 m0,s85,131072\n"
"ds_read_b64_tr_b16 v[86:87],v98 offset:9216\n"
"ds_read_b64_tr_b16 v[94:95],v99 offset:9216\n"
"v_mfma_f32_16x16x32_bf16 v[192:195],a[32:35],a[16:19],v[192:195]\n"
"buffer_load_dwordx4 v110,s[28:31],s86 offen lds\n"
"buffer_store_dwordx4 v[116:119],v112,s[24:27],s56 offen offset:3072 sc1\n"
"v_mfma_f32_16x16x32_bf16 v[192:195],a[36:39],a[20:23],v[192:195]\n"
"s_nop 0\n"
"v_accvgpr_read_b32 v116,a64\n"
"v_mfma_f32_16x16x32_bf16 v[224:227],a[36:39],a[28:31],v[224:227]\n"
"v_accvgpr_read_b32 v117,a65\n"
"v_accvgpr_read_b32 v118,a66\n"
"v_mfma_f32_16x16x32_bf16 v[224:227],a[32:35],a[24:27],v[224:227]\n"
"v_accvgpr_read_b32 v119,a67\n"
"v_accvgpr_read_b32 v120,a68\n"
"v_mfma_f32_16x16x32_bf16 v[228:231],a[40:43],a[24:27],v[228:231]\n"
"ds_read_b64_tr_b16 a[32:33],v100 offset:0\n"
"v_accvgpr_read_b32 v121,a69\n"
"v_mfma_f32_16x16x32_bf16 v[228:231],a[44:47],a[28:31],v[228:231]\n"
"ds_read_b64_tr_b16 a[34:35],v100 offset:1024\n"
"v_accvgpr_read_b32 v122,a70\n"
"v_mfma_f32_16x16x32_bf16 v[196:199],a[44:47],a[20:23],v[196:199]\n"
"ds_read_b64_tr_b16 a[36:37],v100 offset:8192\n"
"v_accvgpr_read_b32 v123,a71\n"
"v_mfma_f32_16x16x32_bf16 v[196:199],a[40:43],a[16:19],v[196:199]\n"
"s_add_u32 m0,s85,132096\n"
"ds_read_b64_tr_b16 a[38:39],v100 offset:9216\n"
"v_mfma_f32_16x16x32_bf16 v[164:167],a[40:43],a[8:11],v[164:167]\n"
"buffer_load_dwordx4 v111,s[28:31],s86 offen lds\n"
"v_mfma_f32_16x16x32_bf16 v[164:167],a[44:47],a[12:15],v[164:167]\n"
"s_add_u32 m0,s85,139264\n"
"s_xor_b32 s86,s86,262144\n"
"v_mfma_f32_16x16x32_bf16 v[132:135],a[44:47],a[4:7],v[132:135]\n"
"buffer_load_dwordx4 v110,s[28:31],s86 offen lds\n"
"v_mfma_f32_16x16x32_bf16 v[132:135],a[40:43],a[0:3],v[132:135]\n"
"v_cvt_pk_bf16_f32 v116,v116,v117\n"
"v_cvt_pk_bf16_f32 v117,v118,v119\n"
"v_mfma_f32_16x16x32_bf16 v[136:139],v[32:35],a[0:3],v[136:139]\n"
"ds_read_b64_tr_b16 a[40:41],v101 offset:0\n"
"v_cvt_pk_bf16_f32 v118,v120,v121\n"
"v_mfma_f32_16x16x32_bf16 v[136:139],v[36:39],a[4:7],v[136:139]\n"
"ds_read_b64_tr_b16 a[42:43],v101 offset:1024\n"
"v_cvt_pk_bf16_f32 v119,v122,v123\n"
"v_mfma_f32_16x16x32_bf16 v[168:171],v[36:39],a[12:15],v[168:171]\n"
"ds_read_b64_tr_b16 a[44:45],v101 offset:8192\n"
"s_add_u32 s56,s56,126976\n"
"v_mfma_f32_16x16x32_bf16 v[168:171],v[32:35],a[8:11],v[168:171]\n"
"s_add_u32 m0,s85,140288\n"
"ds_read_b64_tr_b16 a[46:47],v101 offset:9216\n"
"v_mfma_f32_16x16x32_bf16 v[200:203],v[32:35],a[16:19],v[200:203]\n"
"buffer_load_dwordx4 v111,s[28:31],s86 offen lds\n"
"v_mfma_f32_16x16x32_bf16 v[200:203],v[36:39],a[20:23],v[200:203]\n"
"buffer_store_dwordx4 v[116:119],v112,s[24:27],s56 offen offset:0 sc1\n"
"v_mfma_f32_16x16x32_bf16 v[232:235],v[36:39],a[28:31],v[232:235]\n"
"s_xor_b32 s86,s86,786432\n"
"v_accvgpr_read_b32 v116,a72\n"
"v_accvgpr_read_b32 v117,a73\n"
"v_mfma_f32_16x16x32_bf16 v[232:235],v[32:35],a[24:27],v[232:235]\n"
"v_accvgpr_read_b32 v118,a74\n"
"v_accvgpr_read_b32 v119,a75\n"
"v_mfma_f32_16x16x32_bf16 v[236:239],v[40:43],a[24:27],v[236:239]\n"
"ds_read_b64_tr_b16 v[32:33],v102 offset:0\n"
"v_accvgpr_read_b32 v120,a76\n"
"v_mfma_f32_16x16x32_bf16 v[236:239],v[44:47],a[28:31],v[236:239]\n"
"ds_read_b64_tr_b16 v[34:35],v102 offset:1024\n"
"v_accvgpr_read_b32 v121,a77\n"
"v_mfma_f32_16x16x32_bf16 v[204:207],v[44:47],a[20:23],v[204:207]\n"
"ds_read_b64_tr_b16 v[36:37],v102 offset:8192\n"
"v_accvgpr_read_b32 v122,a78\n"
"v_mfma_f32_16x16x32_bf16 v[204:207],v[40:43],a[16:19],v[204:207]\n"
"s_add_u32 m0,s78,0\n"
"ds_read_b64_tr_b16 v[38:39],v102 offset:9216\n"
"v_mfma_f32_16x16x32_bf16 v[172:175],v[40:43],a[8:11],v[172:175]\n"
"buffer_load_dwordx4 v112,s[24:27],s76 offen offset:0 sc1 lds\n"
"v_mfma_f32_16x16x32_bf16 v[172:175],v[44:47],a[12:15],v[172:175]\n"
"v_accvgpr_read_b32 v123,a79\n"
"v_cvt_pk_bf16_f32 v116,v116,v117\n"
"v_mfma_f32_16x16x32_bf16 v[140:143],v[44:47],a[4:7],v[140:143]\n"
"buffer_load_dwordx4 v112,s[24:27],s76 offen offset:1024 sc1 lds\n"
"v_mfma_f32_16x16x32_bf16 v[140:143],v[40:43],a[0:3],v[140:143]\n"
"s_waitcnt vmcnt(23)\n"
"v_cvt_pk_bf16_f32 v117,v118,v119\n"
"v_cvt_pk_bf16_f32 v118,v120,v121\n"
"v_mfma_f32_16x16x32_bf16 v[144:147],v[48:51],a[0:3],v[144:147]\n"
"ds_read_b64_tr_b16 v[40:41],v103 offset:0\n"
"v_cvt_pk_bf16_f32 v119,v122,v123\n"
"v_mfma_f32_16x16x32_bf16 v[144:147],v[52:55],a[4:7],v[144:147]\n"
"ds_read_b64_tr_b16 v[42:43],v103 offset:1024\n"
"buffer_store_dwordx4 v[116:119],v112,s[24:27],s56 offen offset:1024 sc1\n"
"v_mfma_f32_16x16x32_bf16 v[176:179],v[52:55],a[12:15],v[176:179]\n"
"ds_read_b64_tr_b16 v[44:45],v103 offset:8192\n"
"v_accvgpr_read_b32 v116,a80\n"
"v_mfma_f32_16x16x32_bf16 v[176:179],v[48:51],a[8:11],v[176:179]\n"
"ds_read_b64_tr_b16 v[46:47],v103 offset:9216\n"
"v_accvgpr_read_b32 v117,a81\n"
"v_mfma_f32_16x16x32_bf16 v[208:211],v[48:51],a[16:19],v[208:211]\n"
"buffer_load_dwordx4 v112,s[24:27],s76 offen offset:2048 sc1 lds\n"
"v_mfma_f32_16x16x32_bf16 v[208:211],v[52:55],a[20:23],v[208:211]\n"
"ds_read_b128 v[0:3],v96 offset:32768\n"
"v_accvgpr_read_b32 v118,a82\n"
"v_mfma_f32_16x16x32_bf16 v[240:243],v[52:55],a[28:31],v[240:243]\n"
"v_accvgpr_read_b32 v119,a83\n"
"v_accvgpr_read_b32 v120,a84\n"
"v_mfma_f32_16x16x32_bf16 v[240:243],v[48:51],a[24:27],v[240:243]\n"
"ds_read_b128 v[8:11],v96 offset:33792\n"
"v_accvgpr_read_b32 v121,a85\n"
"v_mfma_f32_16x16x32_bf16 v[244:247],v[56:59],a[24:27],v[244:247]\n"
"ds_read_b64_tr_b16 v[48:49],v104 offset:0\n"
"v_accvgpr_read_b32 v122,a86\n"
"v_mfma_f32_16x16x32_bf16 v[244:247],v[60:63],a[28:31],v[244:247]\n"
"ds_read_b64_tr_b16 v[50:51],v104 offset:1024\n"
"v_accvgpr_read_b32 v123,a87\n"
"v_mfma_f32_16x16x32_bf16 v[212:215],v[60:63],a[20:23],v[212:215]\n"
"ds_read_b64_tr_b16 v[52:53],v104 offset:8192\n"
"v_cvt_pk_bf16_f32 v116,v116,v117\n"
"v_mfma_f32_16x16x32_bf16 v[212:215],v[56:59],a[16:19],v[212:215]\n"
"ds_read_b64_tr_b16 v[54:55],v104 offset:9216\n"
"v_cvt_pk_bf16_f32 v117,v118,v119\n"
"v_mfma_f32_16x16x32_bf16 v[180:183],v[56:59],a[8:11],v[180:183]\n"
"buffer_load_dwordx4 v112,s[24:27],s76 offen offset:3072 sc1 lds\n"
"v_mfma_f32_16x16x32_bf16 v[180:183],v[60:63],a[12:15],v[180:183]\n"
"s_add_u32 m0,s78,16384\n"
"s_xor_b32 s76,s76,16384\n"
"ds_read_b128 v[16:19],v96 offset:34816\n"
"v_mfma_f32_16x16x32_bf16 v[148:151],v[60:63],a[4:7],v[148:151]\n"
"buffer_load_dwordx4 v112,s[24:27],s76 offen offset:0 sc1 lds\n"
"v_mfma_f32_16x16x32_bf16 v[148:151],v[56:59],a[0:3],v[148:151]\n"
"ds_read_b128 v[24:27],v96 offset:35840\n"
"v_cvt_pk_bf16_f32 v118,v120,v121\n"
"v_mfma_f32_16x16x32_bf16 v[152:155],v[64:67],a[0:3],v[152:155]\n"
"ds_read_b64_tr_b16 v[56:57],v105 offset:0\n"
"v_cvt_pk_bf16_f32 v119,v122,v123\n"
"v_mfma_f32_16x16x32_bf16 v[152:155],v[68:71],a[4:7],v[152:155]\n"
"ds_read_b64_tr_b16 v[58:59],v105 offset:1024\n"
"buffer_store_dwordx4 v[116:119],v112,s[24:27],s56 offen offset:2048 sc1\n"
"v_mfma_f32_16x16x32_bf16 v[184:187],v[68:71],a[12:15],v[184:187]\n"
"ds_read_b64_tr_b16 v[60:61],v105 offset:8192\n"
"v_accvgpr_read_b32 v116,a88\n"
"v_mfma_f32_16x16x32_bf16 v[184:187],v[64:67],a[8:11],v[184:187]\n"
"ds_read_b64_tr_b16 v[62:63],v105 offset:9216\n"
"v_accvgpr_read_b32 v117,a89\n"
"v_mfma_f32_16x16x32_bf16 v[216:219],v[64:67],a[16:19],v[216:219]\n"
"ds_read_b128 v[4:7],v96 offset:49152\n"
"v_accvgpr_read_b32 v118,a90\n"
"v_mfma_f32_16x16x32_bf16 v[216:219],v[68:71],a[20:23],v[216:219]\n"
"ds_read_b128 v[12:15],v96 offset:50176\n"
"v_accvgpr_read_b32 v119,a91\n"
"v_mfma_f32_16x16x32_bf16 v[248:251],v[68:71],a[28:31],v[248:251]\n"
"ds_read_b128 v[20:23],v96 offset:51200\n"
"v_accvgpr_read_b32 v120,a92\n"
"v_mfma_f32_16x16x32_bf16 v[248:251],v[64:67],a[24:27],v[248:251]\n"
"ds_read_b128 v[28:31],v96 offset:52224\n"
"v_accvgpr_read_b32 v121,a93\n"
"v_mfma_f32_16x16x32_bf16 v[252:255],v[72:75],a[24:27],v[252:255]\n"
"buffer_load_dwordx4 v112,s[24:27],s76 offen offset:1024 sc1 lds\n"
"v_mfma_f32_16x16x32_bf16 v[252:255],v[76:79],a[28:31],v[252:255]\n"
"v_accvgpr_read_b32 v122,a94\n"
"v_accvgpr_read_b32 v123,a95\n"
"v_mfma_f32_16x16x32_bf16 v[220:223],v[76:79],a[20:23],v[220:223]\n"
"v_cvt_pk_bf16_f32 v116,v116,v117\n"
"v_cvt_pk_bf16_f32 v117,v118,v119\n"
"v_mfma_f32_16x16x32_bf16 v[220:223],v[72:75],a[16:19],v[220:223]\n"
"buffer_load_dwordx4 v112,s[24:27],s76 offen offset:2048 sc1 lds\n"
"v_mfma_f32_16x16x32_bf16 v[188:191],v[72:75],a[8:11],v[188:191]\n"
"v_cvt_pk_bf16_f32 v118,v120,v121\n"
"v_cvt_pk_bf16_f32 v119,v122,v123\n"
"v_mfma_f32_16x16x32_bf16 v[188:191],v[76:79],a[12:15],v[188:191]\n"
"buffer_store_dwordx4 v[116:119],v112,s[24:27],s56 offen offset:3072 sc1\n"
"v_mfma_f32_16x16x32_bf16 v[156:159],v[76:79],a[4:7],v[156:159]\n"
"buffer_load_dwordx4 v112,s[24:27],s76 offen offset:3072 sc1 lds\n"
"v_accvgpr_read_b32 v116,a96\n"
"v_mfma_f32_16x16x32_bf16 v[156:159],v[72:75],a[0:3],v[156:159]\n"
"s_xor_b32 s76,s76,49152\n"
"v_accvgpr_read_b32 v117,a97\n"
"s_waitcnt vmcnt(27) lgkmcnt(0)\n"
"s_barrier\n"
"v_mfma_f32_16x16x32_bf16 v[128:131],v[80:83],v[0:3],v[128:131]\n"
"ds_read_b64_tr_b16 v[64:65],v98 offset:49152\n"
"ds_read_b64_tr_b16 v[72:73],v99 offset:49152\n"
"v_mfma_f32_16x16x32_bf16 v[128:131],v[84:87],v[4:7],v[128:131]\n"
"ds_read_b64_tr_b16 v[66:67],v98 offset:50176\n"
"ds_read_b64_tr_b16 v[74:75],v99 offset:50176\n"
"v_mfma_f32_16x16x32_bf16 v[160:163],v[84:87],v[12:15],v[160:163]\n"
"ds_read_b64_tr_b16 v[68:69],v98 offset:57344\n"
"ds_read_b64_tr_b16 v[76:77],v99 offset:57344\n"
"v_mfma_f32_16x16x32_bf16 v[160:163],v[80:83],v[8:11],v[160:163]\n"
"s_add_u32 m0,s85,98304\n"
"ds_read_b64_tr_b16 v[70:71],v98 offset:58368\n"
"ds_read_b64_tr_b16 v[78:79],v99 offset:58368\n"
"v_mfma_f32_16x16x32_bf16 v[192:195],v[80:83],v[16:19],v[192:195]\n"
"buffer_load_dwordx4 v110,s[28:31],s86 offen lds\n"
"v_mfma_f32_16x16x32_bf16 v[192:195],v[84:87],v[20:23],v[192:195]\n"
"v_accvgpr_read_b32 v118,a98\n"
"v_accvgpr_read_b32 v119,a99\n"
"v_mfma_f32_16x16x32_bf16 v[224:227],v[84:87],v[28:31],v[224:227]\n"
"v_accvgpr_read_b32 v120,a100\n"
"v_accvgpr_read_b32 v121,a101\n"
"v_mfma_f32_16x16x32_bf16 v[224:227],v[80:83],v[24:27],v[224:227]\n"
"v_accvgpr_read_b32 v122,a102\n"
"v_accvgpr_read_b32 v123,a103\n"
"v_mfma_f32_16x16x32_bf16 v[228:231],v[88:91],v[24:27],v[228:231]\n"
"ds_read_b64_tr_b16 v[80:81],v100 offset:49152\n"
"v_cvt_pk_bf16_f32 v116,v116,v117\n"
"v_mfma_f32_16x16x32_bf16 v[228:231],v[92:95],v[28:31],v[228:231]\n"
"ds_read_b64_tr_b16 v[82:83],v100 offset:50176\n"
"v_cvt_pk_bf16_f32 v117,v118,v119\n"
"v_mfma_f32_16x16x32_bf16 v[196:199],v[92:95],v[20:23],v[196:199]\n"
"ds_read_b64_tr_b16 v[84:85],v100 offset:57344\n"
"v_cvt_pk_bf16_f32 v118,v120,v121\n"
"v_mfma_f32_16x16x32_bf16 v[196:199],v[88:91],v[16:19],v[196:199]\n"
"s_add_u32 m0,s85,99328\n"
"ds_read_b64_tr_b16 v[86:87],v100 offset:58368\n"
"v_mfma_f32_16x16x32_bf16 v[164:167],v[88:91],v[8:11],v[164:167]\n"
"buffer_load_dwordx4 v111,s[28:31],s86 offen lds\n"
"v_mfma_f32_16x16x32_bf16 v[164:167],v[92:95],v[12:15],v[164:167]\n"
"s_add_u32 m0,s85,106496\n"
"s_xor_b32 s86,s86,262144\n"
"v_mfma_f32_16x16x32_bf16 v[132:135],v[92:95],v[4:7],v[132:135]\n"
"buffer_load_dwordx4 v110,s[28:31],s86 offen lds\n"
"v_mfma_f32_16x16x32_bf16 v[132:135],v[88:91],v[0:3],v[132:135]\n"
"v_cvt_pk_bf16_f32 v119,v122,v123\n"
"s_add_u32 s56,s56,4096\n"
"v_mfma_f32_16x16x32_bf16 v[136:139],a[32:35],v[0:3],v[136:139]\n"
"ds_read_b64_tr_b16 v[88:89],v101 offset:49152\n"
"buffer_store_dwordx4 v[116:119],v112,s[24:27],s56 offen offset:0 sc1\n"
"v_mfma_f32_16x16x32_bf16 v[136:139],a[36:39],v[4:7],v[136:139]\n"
"ds_read_b64_tr_b16 v[90:91],v101 offset:50176\n"
"v_accvgpr_read_b32 v116,a104\n"
"v_mfma_f32_16x16x32_bf16 v[168:171],a[36:39],v[12:15],v[168:171]\n"
"ds_read_b64_tr_b16 v[92:93],v101 offset:57344\n"
"v_accvgpr_read_b32 v117,a105\n"
"v_mfma_f32_16x16x32_bf16 v[168:171],a[32:35],v[8:11],v[168:171]\n"
"s_add_u32 m0,s85,107520\n"
"ds_read_b64_tr_b16 v[94:95],v101 offset:58368\n"
"v_mfma_f32_16x16x32_bf16 v[200:203],a[32:35],v[16:19],v[200:203]\n"
"buffer_load_dwordx4 v111,s[28:31],s86 offen lds\n"
"v_mfma_f32_16x16x32_bf16 v[200:203],a[36:39],v[20:23],v[200:203]\n"
"s_xor_b32 s86,s86,1835008\n"
"v_accvgpr_read_b32 v118,a106\n"
"v_mfma_f32_16x16x32_bf16 v[232:235],a[36:39],v[28:31],v[232:235]\n"
"v_accvgpr_read_b32 v119,a107\n"
"v_accvgpr_read_b32 v120,a108\n"
"v_mfma_f32_16x16x32_bf16 v[232:235],a[32:35],v[24:27],v[232:235]\n"
"v_accvgpr_read_b32 v121,a109\n"
"v_accvgpr_read_b32 v122,a110\n"
"v_mfma_f32_16x16x32_bf16 v[236:239],a[40:43],v[24:27],v[236:239]\n"
"ds_read_b64_tr_b16 a[32:33],v102 offset:49152\n"
"v_accvgpr_read_b32 v123,a111\n"
"v_mfma_f32_16x16x32_bf16 v[236:239],a[44:47],v[28:31],v[236:239]\n"
"ds_read_b64_tr_b16 a[34:35],v102 offset:50176\n"
"v_cvt_pk_bf16_f32 v116,v116,v117\n"
"v_mfma_f32_16x16x32_bf16 v[204:207],a[44:47],v[20:23],v[204:207]\n"
"ds_read_b64_tr_b16 a[36:37],v102 offset:57344\n"
"v_cvt_pk_bf16_f32 v117,v118,v119\n"
"v_mfma_f32_16x16x32_bf16 v[204:207],a[40:43],v[16:19],v[204:207]\n"
"s_add_u32 m0,s78,32768\n"
"ds_read_b64_tr_b16 a[38:39],v102 offset:58368\n"
"v_mfma_f32_16x16x32_bf16 v[172:175],a[40:43],v[8:11],v[172:175]\n"
"buffer_load_dwordx4 v112,s[24:27],s76 offen offset:0 sc1 lds\n"
"v_mfma_f32_16x16x32_bf16 v[172:175],a[44:47],v[12:15],v[172:175]\n"
"v_cvt_pk_bf16_f32 v118,v120,v121\n"
"v_cvt_pk_bf16_f32 v119,v122,v123\n"
"v_mfma_f32_16x16x32_bf16 v[140:143],a[44:47],v[4:7],v[140:143]\n"
"buffer_load_dwordx4 v112,s[24:27],s76 offen offset:1024 sc1 lds\n"
"buffer_store_dwordx4 v[116:119],v112,s[24:27],s56 offen offset:1024 sc1\n"
"v_mfma_f32_16x16x32_bf16 v[140:143],a[40:43],v[0:3],v[140:143]\n"
"s_nop 0\n"
"v_accvgpr_read_b32 v116,a112\n"
"s_waitcnt vmcnt(25)\n"
"v_mfma_f32_16x16x32_bf16 v[144:147],v[32:35],v[0:3],v[144:147]\n"
"ds_read_b64_tr_b16 a[40:41],v103 offset:49152\n"
"v_accvgpr_read_b32 v117,a113\n"
"v_mfma_f32_16x16x32_bf16 v[144:147],v[36:39],v[4:7],v[144:147]\n"
"ds_read_b64_tr_b16 a[42:43],v103 offset:50176\n"
"v_accvgpr_read_b32 v118,a114\n"
"v_mfma_f32_16x16x32_bf16 v[176:179],v[36:39],v[12:15],v[176:179]\n"
"ds_read_b64_tr_b16 a[44:45],v103 offset:57344\n"
"v_accvgpr_read_b32 v119,a115\n"
"v_mfma_f32_16x16x32_bf16 v[176:179],v[32:35],v[8:11],v[176:179]\n"
"ds_read_b64_tr_b16 a[46:47],v103 offset:58368\n"
"v_accvgpr_read_b32 v120,a116\n"
"v_mfma_f32_16x16x32_bf16 v[208:211],v[32:35],v[16:19],v[208:211]\n"
"buffer_load_dwordx4 v112,s[24:27],s76 offen offset:2048 sc1 lds\n"
"v_mfma_f32_16x16x32_bf16 v[208:211],v[36:39],v[20:23],v[208:211]\n"
"ds_read_b128 a[0:3],v97 offset:0\n"
"v_accvgpr_read_b32 v121,a117\n"
"v_mfma_f32_16x16x32_bf16 v[240:243],v[36:39],v[28:31],v[240:243]\n"
"v_accvgpr_read_b32 v122,a118\n"
"v_accvgpr_read_b32 v123,a119\n"
"v_mfma_f32_16x16x32_bf16 v[240:243],v[32:35],v[24:27],v[240:243]\n"
"ds_read_b128 a[8:11],v97 offset:1024\n"
"v_cvt_pk_bf16_f32 v116,v116,v117\n"
"v_mfma_f32_16x16x32_bf16 v[244:247],v[40:43],v[24:27],v[244:247]\n"
"ds_read_b64_tr_b16 v[32:33],v104 offset:49152\n"
"v_cvt_pk_bf16_f32 v117,v118,v119\n"
"v_mfma_f32_16x16x32_bf16 v[244:247],v[44:47],v[28:31],v[244:247]\n"
"ds_read_b64_tr_b16 v[34:35],v104 offset:50176\n"
"v_cvt_pk_bf16_f32 v118,v120,v121\n"
"v_mfma_f32_16x16x32_bf16 v[212:215],v[44:47],v[20:23],v[212:215]\n"
"ds_read_b64_tr_b16 v[36:37],v104 offset:57344\n"
"v_cvt_pk_bf16_f32 v119,v122,v123\n"
"v_mfma_f32_16x16x32_bf16 v[212:215],v[40:43],v[16:19],v[212:215]\n"
"ds_read_b64_tr_b16 v[38:39],v104 offset:58368\n"
"buffer_store_dwordx4 v[116:119],v112,s[24:27],s56 offen offset:2048 sc1\n"
"v_mfma_f32_16x16x32_bf16 v[180:183],v[40:43],v[8:11],v[180:183]\n"
"buffer_load_dwordx4 v112,s[24:27],s76 offen offset:3072 sc1 lds\n"
"v_mfma_f32_16x16x32_bf16 v[180:183],v[44:47],v[12:15],v[180:183]\n"
"s_add_u32 m0,s78,49152\n"
"s_xor_b32 s76,s76,16384\n"
"ds_read_b128 a[16:19],v97 offset:2048\n"
"v_mfma_f32_16x16x32_bf16 v[148:151],v[44:47],v[4:7],v[148:151]\n"
"buffer_load_dwordx4 v112,s[24:27],s76 offen offset:0 sc1 lds\n"
"v_mfma_f32_16x16x32_bf16 v[148:151],v[40:43],v[0:3],v[148:151]\n"
"ds_read_b128 a[24:27],v97 offset:3072\n"
"v_accvgpr_read_b32 v116,a120\n"
"v_mfma_f32_16x16x32_bf16 v[152:155],v[48:51],v[0:3],v[152:155]\n"
"ds_read_b64_tr_b16 v[40:41],v105 offset:49152\n"
"v_accvgpr_read_b32 v117,a121\n"
"v_mfma_f32_16x16x32_bf16 v[152:155],v[52:55],v[4:7],v[152:155]\n"
"ds_read_b64_tr_b16 v[42:43],v105 offset:50176\n"
"v_accvgpr_read_b32 v118,a122\n"
"v_mfma_f32_16x16x32_bf16 v[184:187],v[52:55],v[12:15],v[184:187]\n"
"ds_read_b64_tr_b16 v[44:45],v105 offset:57344\n"
"v_accvgpr_read_b32 v119,a123\n"
"v_mfma_f32_16x16x32_bf16 v[184:187],v[48:51],v[8:11],v[184:187]\n"
"ds_read_b64_tr_b16 v[46:47],v105 offset:58368\n"
"v_accvgpr_read_b32 v120,a124\n"
"v_mfma_f32_16x16x32_bf16 v[216:219],v[48:51],v[16:19],v[216:219]\n"
"ds_read_b128 a[4:7],v97 offset:16384\n"
"v_accvgpr_read_b32 v121,a125\n"
"v_mfma_f32_16x16x32_bf16 v[216:219],v[52:55],v[20:23],v[216:219]\n"
"ds_read_b128 a[12:15],v97 offset:17408\n"
"v_accvgpr_read_b32 v122,a126\n"
"v_mfma_f32_16x16x32_bf16 v[248:251],v[52:55],v[28:31],v[248:251]\n"
"ds_read_b128 a[20:23],v97 offset:18432\n"
"v_accvgpr_read_b32 v123,a127\n"
"v_mfma_f32_16x16x32_bf16 v[248:251],v[48:51],v[24:27],v[248:251]\n"
"ds_read_b128 a[28:31],v97 offset:19456\n"
"v_cvt_pk_bf16_f32 v116,v116,v117\n"
"v_mfma_f32_16x16x32_bf16 v[252:255],v[56:59],v[24:27],v[252:255]\n"
"buffer_load_dwordx4 v112,s[24:27],s76 offen offset:1024 sc1 lds\n"
"v_mfma_f32_16x16x32_bf16 v[252:255],v[60:63],v[28:31],v[252:255]\n"
"v_cvt_pk_bf16_f32 v117,v118,v119\n"
"v_cvt_pk_bf16_f32 v118,v120,v121\n"
"v_cvt_pk_bf16_f32 v119,v122,v123\n"
"v_mfma_f32_16x16x32_bf16 v[220:223],v[60:63],v[20:23],v[220:223]\n"
"buffer_store_dwordx4 v[116:119],v112,s[24:27],s56 offen offset:3072 sc1\n"
"v_mfma_f32_16x16x32_bf16 v[220:223],v[56:59],v[16:19],v[220:223]\n"
"buffer_load_dwordx4 v112,s[24:27],s76 offen offset:2048 sc1 lds\n"
"v_mfma_f32_16x16x32_bf16 v[188:191],v[56:59],v[8:11],v[188:191]\n"
"v_accvgpr_read_b32 v116,a128\n"
"v_accvgpr_read_b32 v117,a129\n"
"v_mfma_f32_16x16x32_bf16 v[188:191],v[60:63],v[12:15],v[188:191]\n"
"v_accvgpr_read_b32 v118,a130\n"
"v_accvgpr_read_b32 v119,a131\n"
"v_mfma_f32_16x16x32_bf16 v[156:159],v[60:63],v[4:7],v[156:159]\n"
"buffer_load_dwordx4 v112,s[24:27],s76 offen offset:3072 sc1 lds\n"
"v_mfma_f32_16x16x32_bf16 v[156:159],v[56:59],v[0:3],v[156:159]\n"
"s_xor_b32 s76,s76,114688\n"
"v_accvgpr_read_b32 v120,a132\n"
"s_waitcnt vmcnt(28) lgkmcnt(0)\n"
"s_barrier\n"
"v_mfma_f32_16x16x32_bf16 v[128:131],v[64:67],a[0:3],v[128:131]\n"
"ds_read_b64_tr_b16 v[48:49],v98 offset:32768\n"
"ds_read_b64_tr_b16 v[56:57],v99 offset:32768\n"
"v_mfma_f32_16x16x32_bf16 v[128:131],v[68:71],a[4:7],v[128:131]\n"
"ds_read_b64_tr_b16 v[50:51],v98 offset:33792\n"
"ds_read_b64_tr_b16 v[58:59],v99 offset:33792\n"
"v_mfma_f32_16x16x32_bf16 v[160:163],v[68:71],a[12:15],v[160:163]\n"
"ds_read_b64_tr_b16 v[52:53],v98 offset:40960\n"
"ds_read_b64_tr_b16 v[60:61],v99 offset:40960\n"
"v_mfma_f32_16x16x32_bf16 v[160:163],v[64:67],a[8:11],v[160:163]\n"
"s_add_u32 m0,s85,147456\n"
"ds_read_b64_tr_b16 v[54:55],v98 offset:41984\n"
"ds_read_b64_tr_b16 v[62:63],v99 offset:41984\n"
"v_mfma_f32_16x16x32_bf16 v[192:195],v[64:67],a[16:19],v[192:195]\n"
"buffer_load_dwordx4 v110,s[28:31],s86 offen lds\n"
"v_mfma_f32_16x16x32_bf16 v[192:195],v[68:71],a[20:23],v[192:195]\n"
"v_accvgpr_read_b32 v121,a133\n"
"v_accvgpr_read_b32 v122,a134\n"
"v_mfma_f32_16x16x32_bf16 v[224:227],v[68:71],a[28:31],v[224:227]\n"
"v_accvgpr_read_b32 v123,a135\n"
"v_cvt_pk_bf16_f32 v116,v116,v117\n"
"v_mfma_f32_16x16x32_bf16 v[224:227],v[64:67],a[24:27],v[224:227]\n"
"v_cvt_pk_bf16_f32 v117,v118,v119\n"
"v_cvt_pk_bf16_f32 v118,v120,v121\n"
"v_mfma_f32_16x16x32_bf16 v[228:231],v[72:75],a[24:27],v[228:231]\n"
"ds_read_b64_tr_b16 v[64:65],v100 offset:32768\n"
"v_cvt_pk_bf16_f32 v119,v122,v123\n"
"v_mfma_f32_16x16x32_bf16 v[228:231],v[76:79],a[28:31],v[228:231]\n"
"ds_read_b64_tr_b16 v[66:67],v100 offset:33792\n"
"s_add_u32 s56,s56,126976\n"
"v_mfma_f32_16x16x32_bf16 v[196:199],v[76:79],a[20:23],v[196:199]\n"
"ds_read_b64_tr_b16 v[68:69],v100 offset:40960\n"
"buffer_store_dwordx4 v[116:119],v112,s[24:27],s56 offen offset:0 sc1\n"
"v_mfma_f32_16x16x32_bf16 v[196:199],v[72:75],a[16:19],v[196:199]\n"
"s_add_u32 m0,s85,148480\n"
"ds_read_b64_tr_b16 v[70:71],v100 offset:41984\n"
"v_mfma_f32_16x16x32_bf16 v[164:167],v[72:75],a[8:11],v[164:167]\n"
"buffer_load_dwordx4 v111,s[28:31],s86 offen lds\n"
"v_mfma_f32_16x16x32_bf16 v[164:167],v[76:79],a[12:15],v[164:167]\n"
"s_add_u32 m0,s85,155648\n"
"s_xor_b32 s86,s86,262144\n"
"v_mfma_f32_16x16x32_bf16 v[132:135],v[76:79],a[4:7],v[132:135]\n"
"buffer_load_dwordx4 v110,s[28:31],s86 offen lds\n"
"v_mfma_f32_16x16x32_bf16 v[132:135],v[72:75],a[0:3],v[132:135]\n"
"v_accvgpr_read_b32 v116,a136\n"
"v_accvgpr_read_b32 v117,a137\n"
"v_mfma_f32_16x16x32_bf16 v[136:139],v[80:83],a[0:3],v[136:139]\n"
"ds_read_b64_tr_b16 v[72:73],v101 offset:32768\n"
"v_accvgpr_read_b32 v118,a138\n"
"v_mfma_f32_16x16x32_bf16 v[136:139],v[84:87],a[4:7],v[136:139]\n"
"ds_read_b64_tr_b16 v[74:75],v101 offset:33792\n"
"v_accvgpr_read_b32 v119,a139\n"
"v_mfma_f32_16x16x32_bf16 v[168:171],v[84:87],a[12:15],v[168:171]\n"
"ds_read_b64_tr_b16 v[76:77],v101 offset:40960\n"
"v_accvgpr_read_b32 v120,a140\n"
"v_mfma_f32_16x16x32_bf16 v[168:171],v[80:83],a[8:11],v[168:171]\n"
"s_add_u32 m0,s85,156672\n"
"ds_read_b64_tr_b16 v[78:79],v101 offset:41984\n"
"v_mfma_f32_16x16x32_bf16 v[200:203],v[80:83],a[16:19],v[200:203]\n"
"buffer_load_dwordx4 v111,s[28:31],s86 offen lds\n"
"v_mfma_f32_16x16x32_bf16 v[200:203],v[84:87],a[20:23],v[200:203]\n"
"s_xor_b32 s86,s86,786432\n"
"v_accvgpr_read_b32 v121,a141\n"
"v_mfma_f32_16x16x32_bf16 v[232:235],v[84:87],a[28:31],v[232:235]\n"
"v_accvgpr_read_b32 v122,a142\n"
"v_accvgpr_read_b32 v123,a143\n"
"v_mfma_f32_16x16x32_bf16 v[232:235],v[80:83],a[24:27],v[232:235]\n"
"v_cvt_pk_bf16_f32 v116,v116,v117\n"
"v_cvt_pk_bf16_f32 v117,v118,v119\n"
"v_mfma_f32_16x16x32_bf16 v[236:239],v[88:91],a[24:27],v[236:239]\n"
"ds_read_b64_tr_b16 v[80:81],v102 offset:32768\n"
"v_cvt_pk_bf16_f32 v118,v120,v121\n"
"v_mfma_f32_16x16x32_bf16 v[236:239],v[92:95],a[28:31],v[236:239]\n"
"ds_read_b64_tr_b16 v[82:83],v102 offset:33792\n"
"v_cvt_pk_bf16_f32 v119,v122,v123\n"
"v_mfma_f32_16x16x32_bf16 v[204:207],v[92:95],a[20:23],v[204:207]\n"
"ds_read_b64_tr_b16 v[84:85],v102 offset:40960\n"
"buffer_store_dwordx4 v[116:119],v112,s[24:27],s56 offen offset:1024 sc1\n"
"v_mfma_f32_16x16x32_bf16 v[204:207],v[88:91],a[16:19],v[204:207]\n"
"s_add_u32 m0,s78,65536\n"
"ds_read_b64_tr_b16 v[86:87],v102 offset:41984\n"
"v_accvgpr_read_b32 v118,a146\n"
"v_mfma_f32_16x16x32_bf16 v[172:175],v[88:91],a[8:11],v[172:175]\n"
"buffer_load_dwordx4 v112,s[24:27],s76 offen offset:0 sc1 lds\n"
"v_mfma_f32_16x16x32_bf16 v[172:175],v[92:95],a[12:15],v[172:175]\n"
"v_accvgpr_read_b32 v116,a144\n"
"v_accvgpr_read_b32 v117,a145\n"
"v_mfma_f32_16x16x32_bf16 v[140:143],v[92:95],a[4:7],v[140:143]\n"
"buffer_load_dwordx4 v112,s[24:27],s76 offen offset:1024 sc1 lds\n"
"v_mfma_f32_16x16x32_bf16 v[140:143],v[88:91],a[0:3],v[140:143]\n"
"v_accvgpr_read_b32 v119,a147\n"
"s_waitcnt vmcnt(24)\n"
"v_mfma_f32_16x16x32_bf16 v[144:147],a[32:35],a[0:3],v[144:147]\n"
"ds_read_b64_tr_b16 v[88:89],v103 offset:32768\n"
"v_accvgpr_read_b32 v120,a148\n"
"v_mfma_f32_16x16x32_bf16 v[144:147],a[36:39],a[4:7],v[144:147]\n"
"ds_read_b64_tr_b16 v[90:91],v103 offset:33792\n"
"v_accvgpr_read_b32 v121,a149\n"
"v_mfma_f32_16x16x32_bf16 v[176:179],a[36:39],a[12:15],v[176:179]\n"
"ds_read_b64_tr_b16 v[92:93],v103 offset:40960\n"
"v_accvgpr_read_b32 v122,a150\n"
"v_mfma_f32_16x16x32_bf16 v[176:179],a[32:35],a[8:11],v[176:179]\n"
"ds_read_b64_tr_b16 v[94:95],v103 offset:41984\n"
"v_accvgpr_read_b32 v123,a151\n"
"v_mfma_f32_16x16x32_bf16 v[208:211],a[32:35],a[16:19],v[208:211]\n"
"buffer_load_dwordx4 v112,s[24:27],s76 offen offset:2048 sc1 lds\n"
"v_mfma_f32_16x16x32_bf16 v[208:211],a[36:39],a[20:23],v[208:211]\n"
"ds_read_b128 v[0:3],v96 offset:0\n"
"v_cvt_pk_bf16_f32 v116,v116,v117\n"
"v_mfma_f32_16x16x32_bf16 v[240:243],a[36:39],a[28:31],v[240:243]\n"
"v_cvt_pk_bf16_f32 v117,v118,v119\n"
"v_cvt_pk_bf16_f32 v118,v120,v121\n"
"v_mfma_f32_16x16x32_bf16 v[240:243],a[32:35],a[24:27],v[240:243]\n"
"ds_read_b128 v[8:11],v96 offset:1024\n"
"v_cvt_pk_bf16_f32 v119,v122,v123\n"
"v_mfma_f32_16x16x32_bf16 v[244:247],a[40:43],a[24:27],v[244:247]\n"
"ds_read_b64_tr_b16 a[32:33],v104 offset:32768\n"
"buffer_store_dwordx4 v[116:119],v112,s[24:27],s56 offen offset:2048 sc1\n"
"v_mfma_f32_16x16x32_bf16 v[244:247],a[44:47],a[28:31],v[244:247]\n"
"ds_read_b64_tr_b16 a[34:35],v104 offset:33792\n"
"v_accvgpr_read_b32 v116,a152\n"
"v_mfma_f32_16x16x32_bf16 v[212:215],a[44:47],a[20:23],v[212:215]\n"
"ds_read_b64_tr_b16 a[36:37],v104 offset:40960\n"
"v_accvgpr_read_b32 v117,a153\n"
"v_mfma_f32_16x16x32_bf16 v[212:215],a[40:43],a[16:19],v[212:215]\n"
"ds_read_b64_tr_b16 a[38:39],v104 offset:41984\n"
"v_accvgpr_read_b32 v118,a154\n"
"v_mfma_f32_16x16x32_bf16 v[180:183],a[40:43],a[8:11],v[180:183]\n"
"buffer_load_dwordx4 v112,s[24:27],s76 offen offset:3072 sc1 lds\n"
"v_mfma_f32_16x16x32_bf16 v[180:183],a[44:47],a[12:15],v[180:183]\n"
"s_add_u32 m0,s78,81920\n"
"s_xor_b32 s76,s76,16384\n"
"ds_read_b128 v[16:19],v96 offset:2048\n"
"v_mfma_f32_16x16x32_bf16 v[148:151],a[44:47],a[4:7],v[148:151]\n"
"buffer_load_dwordx4 v112,s[24:27],s76 offen offset:0 sc1 lds\n"
"v_mfma_f32_16x16x32_bf16 v[148:151],a[40:43],a[0:3],v[148:151]\n"
"ds_read_b128 v[24:27],v96 offset:3072\n"
"v_accvgpr_read_b32 v119,a155\n"
"v_mfma_f32_16x16x32_bf16 v[152:155],v[32:35],a[0:3],v[152:155]\n"
"ds_read_b64_tr_b16 a[40:41],v105 offset:32768\n"
"v_accvgpr_read_b32 v120,a156\n"
"v_mfma_f32_16x16x32_bf16 v[152:155],v[36:39],a[4:7],v[152:155]\n"
"ds_read_b64_tr_b16 a[42:43],v105 offset:33792\n"
"v_accvgpr_read_b32 v121,a157\n"
"v_mfma_f32_16x16x32_bf16 v[184:187],v[36:39],a[12:15],v[184:187]\n"
"ds_read_b64_tr_b16 a[44:45],v105 offset:40960\n"
"v_accvgpr_read_b32 v122,a158\n"
"v_mfma_f32_16x16x32_bf16 v[184:187],v[32:35],a[8:11],v[184:187]\n"
"ds_read_b64_tr_b16 a[46:47],v105 offset:41984\n"
"v_accvgpr_read_b32 v123,a159\n"
"v_mfma_f32_16x16x32_bf16 v[216:219],v[32:35],a[16:19],v[216:219]\n"
"ds_read_b128 v[4:7],v96 offset:16384\n"
"v_cvt_pk_bf16_f32 v116,v116,v117\n"
"v_mfma_f32_16x16x32_bf16 v[216:219],v[36:39],a[20:23],v[216:219]\n"
"ds_read_b128 v[12:15],v96 offset:17408\n"
"v_cvt_pk_bf16_f32 v117,v118,v119\n"
"v_mfma_f32_16x16x32_bf16 v[248:251],v[36:39],a[28:31],v[248:251]\n"
"ds_read_b128 v[20:23],v96 offset:18432\n"
"v_cvt_pk_bf16_f32 v118,v120,v121\n"
"v_mfma_f32_16x16x32_bf16 v[248:251],v[32:35],a[24:27],v[248:251]\n"
"ds_read_b128 v[28:31],v96 offset:19456\n"
"v_cvt_pk_bf16_f32 v119,v122,v123\n"
"v_mfma_f32_16x16x32_bf16 v[252:255],v[40:43],a[24:27],v[252:255]\n"
"buffer_load_dwordx4 v112,s[24:27],s76 offen offset:1024 sc1 lds\n"
"buffer_store_dwordx4 v[116:119],v112,s[24:27],s56 offen offset:3072 sc1\n"
"v_mfma_f32_16x16x32_bf16 v[252:255],v[44:47],a[28:31],v[252:255]\n"
"s_nop 0\n"
"v_accvgpr_read_b32 v116,a160\n"
"v_mfma_f32_16x16x32_bf16 v[220:223],v[44:47],a[20:23],v[220:223]\n"
"v_accvgpr_read_b32 v117,a161\n"
"v_accvgpr_read_b32 v118,a162\n"
"v_mfma_f32_16x16x32_bf16 v[220:223],v[40:43],a[16:19],v[220:223]\n"
"buffer_load_dwordx4 v112,s[24:27],s76 offen offset:2048 sc1 lds\n"
"v_mfma_f32_16x16x32_bf16 v[188:191],v[40:43],a[8:11],v[188:191]\n"
"v_accvgpr_read_b32 v119,a163\n"
"v_accvgpr_read_b32 v120,a164\n"
"v_mfma_f32_16x16x32_bf16 v[188:191],v[44:47],a[12:15],v[188:191]\n"
"v_accvgpr_read_b32 v121,a165\n"
"v_accvgpr_read_b32 v122,a166\n"
"v_mfma_f32_16x16x32_bf16 v[156:159],v[44:47],a[4:7],v[156:159]\n"
"buffer_load_dwordx4 v112,s[24:27],s76 offen offset:3072 sc1 lds\n"
"v_mfma_f32_16x16x32_bf16 v[156:159],v[40:43],a[0:3],v[156:159]\n"
"s_xor_b32 s76,s76,49152\n"
"v_accvgpr_read_b32 v123,a167\n"
"s_waitcnt vmcnt(27) lgkmcnt(0)\n"
"s_barrier\n"
"v_mfma_f32_16x16x32_bf16 v[128:131],v[48:51],v[0:3],v[128:131]\n"
"ds_read_b64_tr_b16 v[32:33],v98 offset:0\n"
"ds_read_b64_tr_b16 v[40:41],v99 offset:0\n"
"v_mfma_f32_16x16x32_bf16 v[128:131],v[52:55],v[4:7],v[128:131]\n"
"ds_read_b64_tr_b16 v[34:35],v98 offset:1024\n"
"ds_read_b64_tr_b16 v[42:43],v99 offset:1024\n"
"v_mfma_f32_16x16x32_bf16 v[160:163],v[52:55],v[12:15],v[160:163]\n"
"ds_read_b64_tr_b16 v[36:37],v98 offset:8192\n"
"ds_read_b64_tr_b16 v[44:45],v99 offset:8192\n"
"v_mfma_f32_16x16x32_bf16 v[160:163],v[48:51],v[8:11],v[160:163]\n"
"s_add_u32 m0,s85,131072\n"
"ds_read_b64_tr_b16 v[38:39],v98 offset:9216\n"
"ds_read_b64_tr_b16 v[46:47],v99 offset:9216\n"
"v_mfma_f32_16x16x32_bf16 v[192:195],v[48:51],v[16:19],v[192:195]\n"
"buffer_load_dwordx4 v110,s[28:31],s86 offen lds\n"
"v_mfma_f32_16x16x32_bf16 v[192:195],v[52:55],v[20:23],v[192:195]\n"
"v_cvt_pk_bf16_f32 v116,v116,v117\n"
"v_cvt_pk_bf16_f32 v117,v118,v119\n"
"v_mfma_f32_16x16x32_bf16 v[224:227],v[52:55],v[28:31],v[224:227]\n"
"s_add_u32 s56,s56,4096\n"
"v_cvt_pk_bf16_f32 v118,v120,v121\n"
"v_cvt_pk_bf16_f32 v119,v122,v123\n"
"v_mfma_f32_16x16x32_bf16 v[224:227],v[48:51],v[24:27],v[224:227]\n"
"buffer_store_dwordx4 v[116:119],v112,s[24:27],s56 offen offset:0 sc1\n"
"v_mfma_f32_16x16x32_bf16 v[228:231],v[56:59],v[24:27],v[228:231]\n"
"ds_read_b64_tr_b16 v[48:49],v100 offset:0\n"
"v_accvgpr_read_b32 v116,a168\n"
"v_mfma_f32_16x16x32_bf16 v[228:231],v[60:63],v[28:31],v[228:231]\n"
"ds_read_b64_tr_b16 v[50:51],v100 offset:1024\n"
"v_accvgpr_read_b32 v117,a169\n"
"v_mfma_f32_16x16x32_bf16 v[196:199],v[60:63],v[20:23],v[196:199]\n"
"ds_read_b64_tr_b16 v[52:53],v100 offset:8192\n"
"v_accvgpr_read_b32 v118,a170\n"
"v_mfma_f32_16x16x32_bf16 v[196:199],v[56:59],v[16:19],v[196:199]\n"
"s_add_u32 m0,s85,132096\n"
"ds_read_b64_tr_b16 v[54:55],v100 offset:9216\n"
"v_mfma_f32_16x16x32_bf16 v[164:167],v[56:59],v[8:11],v[164:167]\n"
"buffer_load_dwordx4 v111,s[28:31],s86 offen lds\n"
"v_mfma_f32_16x16x32_bf16 v[164:167],v[60:63],v[12:15],v[164:167]\n"
"s_add_u32 m0,s85,139264\n"
"s_xor_b32 s86,s86,262144\n"
"v_mfma_f32_16x16x32_bf16 v[132:135],v[60:63],v[4:7],v[132:135]\n"
"buffer_load_dwordx4 v110,s[28:31],s86 offen lds\n"
"v_mfma_f32_16x16x32_bf16 v[132:135],v[56:59],v[0:3],v[132:135]\n"
"v_accvgpr_read_b32 v119,a171\n"
"v_accvgpr_read_b32 v120,a172\n"
"v_mfma_f32_16x16x32_bf16 v[136:139],v[64:67],v[0:3],v[136:139]\n"
"ds_read_b64_tr_b16 v[56:57],v101 offset:0\n"
"v_accvgpr_read_b32 v121,a173\n"
"v_mfma_f32_16x16x32_bf16 v[136:139],v[68:71],v[4:7],v[136:139]\n"
"ds_read_b64_tr_b16 v[58:59],v101 offset:1024\n"
"v_accvgpr_read_b32 v122,a174\n"
"v_mfma_f32_16x16x32_bf16 v[168:171],v[68:71],v[12:15],v[168:171]\n"
"ds_read_b64_tr_b16 v[60:61],v101 offset:8192\n"
"v_accvgpr_read_b32 v123,a175\n"
"v_mfma_f32_16x16x32_bf16 v[168:171],v[64:67],v[8:11],v[168:171]\n"
"s_add_u32 m0,s85,140288\n"
"ds_read_b64_tr_b16 v[62:63],v101 offset:9216\n"
"v_mfma_f32_16x16x32_bf16 v[200:203],v[64:67],v[16:19],v[200:203]\n"
"buffer_load_dwordx4 v111,s[28:31],s86 offen lds\n"
"v_mfma_f32_16x16x32_bf16 v[200:203],v[68:71],v[20:23],v[200:203]\n"
"s_xor_b32 s86,s86,8126464\n"
"v_cvt_pk_bf16_f32 v116,v116,v117\n"
"v_cvt_pk_bf16_f32 v117,v118,v119\n"
"v_mfma_f32_16x16x32_bf16 v[232:235],v[68:71],v[28:31],v[232:235]\n"
"v_cvt_pk_bf16_f32 v118,v120,v121\n"
"v_cvt_pk_bf16_f32 v119,v122,v123\n"
"v_mfma_f32_16x16x32_bf16 v[232:235],v[64:67],v[24:27],v[232:235]\n"
"buffer_store_dwordx4 v[116:119],v112,s[24:27],s56 offen offset:1024 sc1\n"
"v_mfma_f32_16x16x32_bf16 v[236:239],v[72:75],v[24:27],v[236:239]\n"
"ds_read_b64_tr_b16 v[64:65],v102 offset:0\n"
"v_accvgpr_read_b32 v116,a176\n"
"v_mfma_f32_16x16x32_bf16 v[236:239],v[76:79],v[28:31],v[236:239]\n"
"ds_read_b64_tr_b16 v[66:67],v102 offset:1024\n"
"v_accvgpr_read_b32 v117,a177\n"
"v_mfma_f32_16x16x32_bf16 v[204:207],v[76:79],v[20:23],v[204:207]\n"
"ds_read_b64_tr_b16 v[68:69],v102 offset:8192\n"
"v_accvgpr_read_b32 v118,a178\n"
"v_mfma_f32_16x16x32_bf16 v[204:207],v[72:75],v[16:19],v[204:207]\n"
"s_add_u32 m0,s78,0\n"
"ds_read_b64_tr_b16 v[70:71],v102 offset:9216\n"
"v_accvgpr_read_b32 v121,a181\n"
"v_mfma_f32_16x16x32_bf16 v[172:175],v[72:75],v[8:11],v[172:175]\n"
"buffer_load_dwordx4 v112,s[24:27],s76 offen offset:0 sc1 lds\n"
"v_mfma_f32_16x16x32_bf16 v[172:175],v[76:79],v[12:15],v[172:175]\n"
"v_accvgpr_read_b32 v119,a179\n"
"v_accvgpr_read_b32 v120,a180\n"
"v_mfma_f32_16x16x32_bf16 v[140:143],v[76:79],v[4:7],v[140:143]\n"
"buffer_load_dwordx4 v112,s[24:27],s76 offen offset:1024 sc1 lds\n"
"v_mfma_f32_16x16x32_bf16 v[140:143],v[72:75],v[0:3],v[140:143]\n"
"v_accvgpr_read_b32 v122,a182\n"
"s_waitcnt vmcnt(24)\n"
"v_mfma_f32_16x16x32_bf16 v[144:147],v[80:83],v[0:3],v[144:147]\n"
"ds_read_b64_tr_b16 v[72:73],v103 offset:0\n"
"v_accvgpr_read_b32 v123,a183\n"
"v_mfma_f32_16x16x32_bf16 v[144:147],v[84:87],v[4:7],v[144:147]\n"
"ds_read_b64_tr_b16 v[74:75],v103 offset:1024\n"
"v_cvt_pk_bf16_f32 v116,v116,v117\n"
"v_mfma_f32_16x16x32_bf16 v[176:179],v[84:87],v[12:15],v[176:179]\n"
"ds_read_b64_tr_b16 v[76:77],v103 offset:8192\n"
"v_cvt_pk_bf16_f32 v117,v118,v119\n"
"v_mfma_f32_16x16x32_bf16 v[176:179],v[80:83],v[8:11],v[176:179]\n"
"ds_read_b64_tr_b16 v[78:79],v103 offset:9216\n"
"v_cvt_pk_bf16_f32 v118,v120,v121\n"
"v_mfma_f32_16x16x32_bf16 v[208:211],v[80:83],v[16:19],v[208:211]\n"
"buffer_load_dwordx4 v112,s[24:27],s76 offen offset:2048 sc1 lds\n"
"v_mfma_f32_16x16x32_bf16 v[208:211],v[84:87],v[20:23],v[208:211]\n"
"ds_read_b128 a[0:3],v96 offset:32768\n"
"v_cvt_pk_bf16_f32 v119,v122,v123\n"
"v_mfma_f32_16x16x32_bf16 v[240:243],v[84:87],v[28:31],v[240:243]\n"
"buffer_store_dwordx4 v[116:119],v112,s[24:27],s56 offen offset:2048 sc1\n"
"v_mfma_f32_16x16x32_bf16 v[240:243],v[80:83],v[24:27],v[240:243]\n"
"ds_read_b128 a[8:11],v96 offset:33792\n"
"v_accvgpr_read_b32 v116,a184\n"
"v_accvgpr_read_b32 v117,a185\n"
"v_mfma_f32_16x16x32_bf16 v[244:247],v[88:91],v[24:27],v[244:247]\n"
"ds_read_b64_tr_b16 v[80:81],v104 offset:0\n"
"v_accvgpr_read_b32 v118,a186\n"
"v_mfma_f32_16x16x32_bf16 v[244:247],v[92:95],v[28:31],v[244:247]\n"
"ds_read_b64_tr_b16 v[82:83],v104 offset:1024\n"
"v_accvgpr_read_b32 v119,a187\n"
"v_mfma_f32_16x16x32_bf16 v[212:215],v[92:95],v[20:23],v[212:215]\n"
"ds_read_b64_tr_b16 v[84:85],v104 offset:8192\n"
"v_accvgpr_read_b32 v120,a188\n"
"v_mfma_f32_16x16x32_bf16 v[212:215],v[88:91],v[16:19],v[212:215]\n"
"ds_read_b64_tr_b16 v[86:87],v104 offset:9216\n"
"v_accvgpr_read_b32 v121,a189\n"
"v_mfma_f32_16x16x32_bf16 v[180:183],v[88:91],v[8:11],v[180:183]\n"
"buffer_load_dwordx4 v112,s[24:27],s76 offen offset:3072 sc1 lds\n"
"v_mfma_f32_16x16x32_bf16 v[180:183],v[92:95],v[12:15],v[180:183]\n"
"s_add_u32 m0,s78,16384\n"
"s_xor_b32 s76,s76,16384\n"
"ds_read_b128 a[16:19],v96 offset:34816\n"
"v_mfma_f32_16x16x32_bf16 v[148:151],v[92:95],v[4:7],v[148:151]\n"
"buffer_load_dwordx4 v112,s[24:27],s76 offen offset:0 sc1 lds\n"
"v_mfma_f32_16x16x32_bf16 v[148:151],v[88:91],v[0:3],v[148:151]\n"
"ds_read_b128 a[24:27],v96 offset:35840\n"
"v_accvgpr_read_b32 v122,a190\n"
"v_mfma_f32_16x16x32_bf16 v[152:155],a[32:35],v[0:3],v[152:155]\n"
"ds_read_b64_tr_b16 v[88:89],v105 offset:0\n"
"v_accvgpr_read_b32 v123,a191\n"
"v_mfma_f32_16x16x32_bf16 v[152:155],a[36:39],v[4:7],v[152:155]\n"
"ds_read_b64_tr_b16 v[90:91],v105 offset:1024\n"
"v_cvt_pk_bf16_f32 v116,v116,v117\n"
"v_mfma_f32_16x16x32_bf16 v[184:187],a[36:39],v[12:15],v[184:187]\n"
"ds_read_b64_tr_b16 v[92:93],v105 offset:8192\n"
"v_cvt_pk_bf16_f32 v117,v118,v119\n"
"v_mfma_f32_16x16x32_bf16 v[184:187],a[32:35],v[8:11],v[184:187]\n"
"ds_read_b64_tr_b16 v[94:95],v105 offset:9216\n"
"v_cvt_pk_bf16_f32 v118,v120,v121\n"
"v_mfma_f32_16x16x32_bf16 v[216:219],a[32:35],v[16:19],v[216:219]\n"
"ds_read_b128 a[4:7],v96 offset:49152\n"
"v_cvt_pk_bf16_f32 v119,v122,v123\n"
"v_mfma_f32_16x16x32_bf16 v[216:219],a[36:39],v[20:23],v[216:219]\n"
"ds_read_b128 a[12:15],v96 offset:50176\n"
"buffer_store_dwordx4 v[116:119],v112,s[24:27],s56 offen offset:3072 sc1\n"
"v_mfma_f32_16x16x32_bf16 v[248:251],a[36:39],v[28:31],v[248:251]\n"
"ds_read_b128 a[20:23],v96 offset:51200\n"
"v_accvgpr_read_b32 v116,a248\n"
"v_mfma_f32_16x16x32_bf16 v[248:251],a[32:35],v[24:27],v[248:251]\n"
"ds_read_b128 a[28:31],v96 offset:52224\n"
"v_accvgpr_read_b32 v117,a249\n"
"v_mfma_f32_16x16x32_bf16 v[252:255],a[40:43],v[24:27],v[252:255]\n"
"buffer_load_dwordx4 v112,s[24:27],s76 offen offset:1024 sc1 lds\n"
"v_mfma_f32_16x16x32_bf16 v[252:255],a[44:47],v[28:31],v[252:255]\n"
"v_accvgpr_read_b32 v118,a250\n"
"v_accvgpr_read_b32 v119,a251\n"
"v_mfma_f32_16x16x32_bf16 v[220:223],a[44:47],v[20:23],v[220:223]\n"
"v_accvgpr_read_b32 v120,a252\n"
"v_accvgpr_read_b32 v121,a253\n"
"v_mfma_f32_16x16x32_bf16 v[220:223],a[40:43],v[16:19],v[220:223]\n"
"buffer_load_dwordx4 v112,s[24:27],s76 offen offset:2048 sc1 lds\n"
"v_mfma_f32_16x16x32_bf16 v[188:191],a[40:43],v[8:11],v[188:191]\n"
"v_accvgpr_read_b32 v122,a254\n"
"v_accvgpr_read_b32 v123,a255\n"
"v_mfma_f32_16x16x32_bf16 v[188:191],a[44:47],v[12:15],v[188:191]\n"
"v_cvt_pk_bf16_f32 v116,v116,v117\n"
"v_cvt_pk_bf16_f32 v117,v118,v119\n"
"v_mfma_f32_16x16x32_bf16 v[156:159],a[44:47],v[4:7],v[156:159]\n"
"buffer_load_dwordx4 v112,s[24:27],s76 offen offset:3072 sc1 lds\n"
"v_mfma_f32_16x16x32_bf16 v[156:159],a[40:43],v[0:3],v[156:159]\n"
"s_xor_b32 s76,s76,507904\n"
"v_cvt_pk_bf16_f32 v118,v120,v121\n"
"s_waitcnt vmcnt(27) lgkmcnt(0)\n"
"s_barrier\n"
"v_mfma_f32_16x16x32_bf16 v[128:131],v[32:35],a[0:3],v[128:131]\n"
"ds_read_b64_tr_b16 a[32:33],v98 offset:49152\n"
"ds_read_b64_tr_b16 a[40:41],v99 offset:49152\n"
"v_mfma_f32_16x16x32_bf16 v[128:131],v[36:39],a[4:7],v[128:131]\n"
"ds_read_b64_tr_b16 a[34:35],v98 offset:50176\n"
"ds_read_b64_tr_b16 a[42:43],v99 offset:50176\n"
"v_mfma_f32_16x16x32_bf16 v[160:163],v[36:39],a[12:15],v[160:163]\n"
"ds_read_b64_tr_b16 a[36:37],v98 offset:57344\n"
"ds_read_b64_tr_b16 a[44:45],v99 offset:57344\n"
"v_mfma_f32_16x16x32_bf16 v[160:163],v[32:35],a[8:11],v[160:163]\n"
"s_add_u32 m0,s85,98304\n"
"ds_read_b64_tr_b16 a[38:39],v98 offset:58368\n"
"ds_read_b64_tr_b16 a[46:47],v99 offset:58368\n"
"v_mfma_f32_16x16x32_bf16 v[192:195],v[32:35],a[16:19],v[192:195]\n"
"buffer_load_dwordx4 v110,s[28:31],s86 offen lds\n"
"v_mfma_f32_16x16x32_bf16 v[192:195],v[36:39],a[20:23],v[192:195]\n"
"v_cvt_pk_bf16_f32 v119,v122,v123\n"
"v_accvgpr_write_b32 a252,v116\n"
"v_mfma_f32_16x16x32_bf16 v[224:227],v[36:39],a[28:31],v[224:227]\n"
"v_accvgpr_write_b32 a253,v117\n"
"v_accvgpr_write_b32 a254,v118\n"
"v_mfma_f32_16x16x32_bf16 v[224:227],v[32:35],a[24:27],v[224:227]\n"
"v_accvgpr_write_b32 a255,v119\n"
"v_accvgpr_read_b32 v116,a240\n"
"v_mfma_f32_16x16x32_bf16 v[228:231],v[40:43],a[24:27],v[228:231]\n"
"ds_read_b64_tr_b16 v[32:33],v100 offset:49152\n"
"v_accvgpr_read_b32 v117,a241\n"
"v_mfma_f32_16x16x32_bf16 v[228:231],v[44:47],a[28:31],v[228:231]\n"
"ds_read_b64_tr_b16 v[34:35],v100 offset:50176\n"
"v_accvgpr_read_b32 v118,a242\n"
"v_mfma_f32_16x16x32_bf16 v[196:199],v[44:47],a[20:23],v[196:199]\n"
"ds_read_b64_tr_b16 v[36:37],v100 offset:57344\n"
"v_accvgpr_read_b32 v119,a243\n"
"v_mfma_f32_16x16x32_bf16 v[196:199],v[40:43],a[16:19],v[196:199]\n"
"s_add_u32 m0,s85,99328\n"
"ds_read_b64_tr_b16 v[38:39],v100 offset:58368\n"
"v_mfma_f32_16x16x32_bf16 v[164:167],v[40:43],a[8:11],v[164:167]\n"
"buffer_load_dwordx4 v111,s[28:31],s86 offen lds\n"
"v_mfma_f32_16x16x32_bf16 v[164:167],v[44:47],a[12:15],v[164:167]\n"
"s_add_u32 m0,s85,106496\n"
"s_xor_b32 s86,s86,262144\n"
"v_mfma_f32_16x16x32_bf16 v[132:135],v[44:47],a[4:7],v[132:135]\n"
"buffer_load_dwordx4 v110,s[28:31],s86 offen lds\n"
"v_mfma_f32_16x16x32_bf16 v[132:135],v[40:43],a[0:3],v[132:135]\n"
"v_accvgpr_read_b32 v120,a244\n"
"v_accvgpr_read_b32 v121,a245\n"
"v_mfma_f32_16x16x32_bf16 v[136:139],v[48:51],a[0:3],v[136:139]\n"
"ds_read_b64_tr_b16 v[40:41],v101 offset:49152\n"
"v_accvgpr_read_b32 v122,a246\n"
"v_mfma_f32_16x16x32_bf16 v[136:139],v[52:55],a[4:7],v[136:139]\n"
"ds_read_b64_tr_b16 v[42:43],v101 offset:50176\n"
"v_accvgpr_read_b32 v123,a247\n"
"v_mfma_f32_16x16x32_bf16 v[168:171],v[52:55],a[12:15],v[168:171]\n"
"ds_read_b64_tr_b16 v[44:45],v101 offset:57344\n"
"v_cvt_pk_bf16_f32 v116,v116,v117\n"
"v_mfma_f32_16x16x32_bf16 v[168:171],v[48:51],a[8:11],v[168:171]\n"
"s_add_u32 m0,s85,107520\n"
"ds_read_b64_tr_b16 v[46:47],v101 offset:58368\n"
"v_mfma_f32_16x16x32_bf16 v[200:203],v[48:51],a[16:19],v[200:203]\n"
"buffer_load_dwordx4 v111,s[28:31],s86 offen lds\n"
"v_mfma_f32_16x16x32_bf16 v[200:203],v[52:55],a[20:23],v[200:203]\n"
"s_xor_b32 s86,s86,786432\n"
"v_cvt_pk_bf16_f32 v117,v118,v119\n"
"v_mfma_f32_16x16x32_bf16 v[232:235],v[52:55],a[28:31],v[232:235]\n"
"v_cvt_pk_bf16_f32 v118,v120,v121\n"
"v_cvt_pk_bf16_f32 v119,v122,v123\n"
"v_mfma_f32_16x16x32_bf16 v[232:235],v[48:51],a[24:27],v[232:235]\n"
"v_accvgpr_write_b32 a248,v116\n"
"v_accvgpr_write_b32 a249,v117\n"
"v_mfma_f32_16x16x32_bf16 v[236:239],v[56:59],a[24:27],v[236:239]\n"
"ds_read_b64_tr_b16 v[48:49],v102 offset:49152\n"
"v_accvgpr_write_b32 a250,v118\n"
"v_mfma_f32_16x16x32_bf16 v[236:239],v[60:63],a[28:31],v[236:239]\n"
"ds_read_b64_tr_b16 v[50:51],v102 offset:50176\n"
"v_accvgpr_write_b32 a251,v119\n"
"v_mfma_f32_16x16x32_bf16 v[204:207],v[60:63],a[20:23],v[204:207]\n"
"ds_read_b64_tr_b16 v[52:53],v102 offset:57344\n"
"v_accvgpr_read_b32 v116,a232\n"
"v_mfma_f32_16x16x32_bf16 v[204:207],v[56:59],a[16:19],v[204:207]\n"
"s_add_u32 m0,s78,32768\n"
"ds_read_b64_tr_b16 v[54:55],v102 offset:58368\n"
"v_accvgpr_read_b32 v119,a235\n"
"v_mfma_f32_16x16x32_bf16 v[172:175],v[56:59],a[8:11],v[172:175]\n"
"buffer_load_dwordx4 v112,s[24:27],s76 offen offset:0 sc1 lds\n"
"v_mfma_f32_16x16x32_bf16 v[172:175],v[60:63],a[12:15],v[172:175]\n"
"v_accvgpr_read_b32 v117,a233\n"
"v_accvgpr_read_b32 v118,a234\n"
"v_mfma_f32_16x16x32_bf16 v[140:143],v[60:63],a[4:7],v[140:143]\n"
"buffer_load_dwordx4 v112,s[24:27],s76 offen offset:1024 sc1 lds\n"
"v_mfma_f32_16x16x32_bf16 v[140:143],v[56:59],a[0:3],v[140:143]\n"
"v_accvgpr_read_b32 v120,a236\n"
"s_waitcnt vmcnt(22)\n"
"v_mfma_f32_16x16x32_bf16 v[144:147],v[64:67],a[0:3],v[144:147]\n"
"ds_read_b64_tr_b16 v[56:57],v103 offset:49152\n"
"v_accvgpr_read_b32 v121,a237\n"
"v_mfma_f32_16x16x32_bf16 v[144:147],v[68:71],a[4:7],v[144:147]\n"
"ds_read_b64_tr_b16 v[58:59],v103 offset:50176\n"
"v_accvgpr_read_b32 v122,a238\n"
"v_mfma_f32_16x16x32_bf16 v[176:179],v[68:71],a[12:15],v[176:179]\n"
"ds_read_b64_tr_b16 v[60:61],v103 offset:57344\n"
"v_accvgpr_read_b32 v123,a239\n"
"v_mfma_f32_16x16x32_bf16 v[176:179],v[64:67],a[8:11],v[176:179]\n"
"ds_read_b64_tr_b16 v[62:63],v103 offset:58368\n"
"v_cvt_pk_bf16_f32 v116,v116,v117\n"
"v_mfma_f32_16x16x32_bf16 v[208:211],v[64:67],a[16:19],v[208:211]\n"
"buffer_load_dwordx4 v112,s[24:27],s76 offen offset:2048 sc1 lds\n"
"v_mfma_f32_16x16x32_bf16 v[208:211],v[68:71],a[20:23],v[208:211]\n"
"ds_read_b128 v[0:3],v97 offset:0\n"
"v_cvt_pk_bf16_f32 v117,v118,v119\n"
"v_mfma_f32_16x16x32_bf16 v[240:243],v[68:71],a[28:31],v[240:243]\n"
"v_cvt_pk_bf16_f32 v118,v120,v121\n"
"v_cvt_pk_bf16_f32 v119,v122,v123\n"
"v_mfma_f32_16x16x32_bf16 v[240:243],v[64:67],a[24:27],v[240:243]\n"
"ds_read_b128 v[8:11],v97 offset:1024\n"
"v_accvgpr_write_b32 a244,v116\n"
"v_mfma_f32_16x16x32_bf16 v[244:247],v[72:75],a[24:27],v[244:247]\n"
"ds_read_b64_tr_b16 v[64:65],v104 offset:49152\n"
"v_accvgpr_write_b32 a245,v117\n"
"v_mfma_f32_16x16x32_bf16 v[244:247],v[76:79],a[28:31],v[244:247]\n"
"ds_read_b64_tr_b16 v[66:67],v104 offset:50176\n"
"v_accvgpr_write_b32 a246,v118\n"
"v_mfma_f32_16x16x32_bf16 v[212:215],v[76:79],a[20:23],v[212:215]\n"
"ds_read_b64_tr_b16 v[68:69],v104 offset:57344\n"
"v_accvgpr_write_b32 a247,v119\n"
"v_mfma_f32_16x16x32_bf16 v[212:215],v[72:75],a[16:19],v[212:215]\n"
"ds_read_b64_tr_b16 v[70:71],v104 offset:58368\n"
"v_accvgpr_read_b32 v116,a224\n"
"v_mfma_f32_16x16x32_bf16 v[180:183],v[72:75],a[8:11],v[180:183]\n"
"buffer_load_dwordx4 v112,s[24:27],s76 offen offset:3072 sc1 lds\n"
"v_mfma_f32_16x16x32_bf16 v[180:183],v[76:79],a[12:15],v[180:183]\n"
"s_add_u32 m0,s78,49152\n"
"s_xor_b32 s76,s76,16384\n"
"ds_read_b128 v[16:19],v97 offset:2048\n"
"v_mfma_f32_16x16x32_bf16 v[148:151],v[76:79],a[4:7],v[148:151]\n"
"buffer_load_dwordx4 v112,s[24:27],s76 offen offset:0 sc1 lds\n"
"v_mfma_f32_16x16x32_bf16 v[148:151],v[72:75],a[0:3],v[148:151]\n"
"ds_read_b128 v[24:27],v97 offset:3072\n"
"v_accvgpr_read_b32 v117,a225\n"
"v_mfma_f32_16x16x32_bf16 v[152:155],v[80:83],a[0:3],v[152:155]\n"
"ds_read_b64_tr_b16 v[72:73],v105 offset:49152\n"
"v_accvgpr_read_b32 v118,a226\n"
"v_mfma_f32_16x16x32_bf16 v[152:155],v[84:87],a[4:7],v[152:155]\n"
"ds_read_b64_tr_b16 v[74:75],v105 offset:50176\n"
"v_accvgpr_read_b32 v119,a227\n"
"v_mfma_f32_16x16x32_bf16 v[184:187],v[84:87],a[12:15],v[184:187]\n"
"ds_read_b64_tr_b16 v[76:77],v105 offset:57344\n"
"v_accvgpr_read_b32 v120,a228\n"
"v_mfma_f32_16x16x32_bf16 v[184:187],v[80:83],a[8:11],v[184:187]\n"
"ds_read_b64_tr_b16 v[78:79],v105 offset:58368\n"
"v_accvgpr_read_b32 v121,a229\n"
"v_mfma_f32_16x16x32_bf16 v[216:219],v[80:83],a[16:19],v[216:219]\n"
"ds_read_b128 v[4:7],v97 offset:16384\n"
"v_accvgpr_read_b32 v122,a230\n"
"v_mfma_f32_16x16x32_bf16 v[216:219],v[84:87],a[20:23],v[216:219]\n"
"ds_read_b128 v[12:15],v97 offset:17408\n"
"v_accvgpr_read_b32 v123,a231\n"
"v_mfma_f32_16x16x32_bf16 v[248:251],v[84:87],a[28:31],v[248:251]\n"
"ds_read_b128 v[20:23],v97 offset:18432\n"
"v_cvt_pk_bf16_f32 v116,v116,v117\n"
"v_mfma_f32_16x16x32_bf16 v[248:251],v[80:83],a[24:27],v[248:251]\n"
"ds_read_b128 v[28:31],v97 offset:19456\n"
"v_cvt_pk_bf16_f32 v117,v118,v119\n"
"v_mfma_f32_16x16x32_bf16 v[252:255],v[88:91],a[24:27],v[252:255]\n"
"buffer_load_dwordx4 v112,s[24:27],s76 offen offset:1024 sc1 lds\n"
"v_mfma_f32_16x16x32_bf16 v[252:255],v[92:95],a[28:31],v[252:255]\n"
"v_cvt_pk_bf16_f32 v118,v120,v121\n"
"v_cvt_pk_bf16_f32 v119,v122,v123\n"
"v_mfma_f32_16x16x32_bf16 v[220:223],v[92:95],a[20:23],v[220:223]\n"
"v_accvgpr_write_b32 a240,v116\n"
"v_accvgpr_write_b32 a241,v117\n"
"v_mfma_f32_16x16x32_bf16 v[220:223],v[88:91],a[16:19],v[220:223]\n"
"buffer_load_dwordx4 v112,s[24:27],s76 offen offset:2048 sc1 lds\n"
"v_mfma_f32_16x16x32_bf16 v[188:191],v[88:91],a[8:11],v[188:191]\n"
"v_accvgpr_write_b32 a242,v118\n"
"v_accvgpr_write_b32 a243,v119\n"
"v_mfma_f32_16x16x32_bf16 v[188:191],v[92:95],a[12:15],v[188:191]\n"
"v_accvgpr_read_b32 v116,a216\n"
"v_accvgpr_read_b32 v117,a217\n"
"v_mfma_f32_16x16x32_bf16 v[156:159],v[92:95],a[4:7],v[156:159]\n"
"buffer_load_dwordx4 v112,s[24:27],s76 offen offset:3072 sc1 lds\n"
"v_mfma_f32_16x16x32_bf16 v[156:159],v[88:91],a[0:3],v[156:159]\n"
"s_xor_b32 s76,s76,49152\n"
"v_accvgpr_read_b32 v118,a218\n"
"s_waitcnt vmcnt(23) lgkmcnt(0)\n"
"s_barrier\n"
"v_mfma_f32_16x16x32_bf16 v[128:131],a[32:35],v[0:3],v[128:131]\n"
"ds_read_b64_tr_b16 v[80:81],v98 offset:32768\n"
"ds_read_b64_tr_b16 v[88:89],v99 offset:32768\n"
"v_mfma_f32_16x16x32_bf16 v[128:131],a[36:39],v[4:7],v[128:131]\n"
"ds_read_b64_tr_b16 v[82:83],v98 offset:33792\n"
"ds_read_b64_tr_b16 v[90:91],v99 offset:33792\n"
"v_mfma_f32_16x16x32_bf16 v[160:163],a[36:39],v[12:15],v[160:163]\n"
"ds_read_b64_tr_b16 v[84:85],v98 offset:40960\n"
"ds_read_b64_tr_b16 v[92:93],v99 offset:40960\n"
"v_mfma_f32_16x16x32_bf16 v[160:163],a[32:35],v[8:11],v[160:163]\n"
"s_add_u32 m0,s85,147456\n"
"ds_read_b64_tr_b16 v[86:87],v98 offset:41984\n"
"ds_read_b64_tr_b16 v[94:95],v99 offset:41984\n"
"v_mfma_f32_16x16x32_bf16 v[192:195],a[32:35],v[16:19],v[192:195]\n"
"buffer_load_dwordx4 v110,s[28:31],s86 offen lds\n"
"v_mfma_f32_16x16x32_bf16 v[192:195],a[36:39],v[20:23],v[192:195]\n"
"v_accvgpr_read_b32 v119,a219\n"
"v_accvgpr_read_b32 v120,a220\n"
"v_mfma_f32_16x16x32_bf16 v[224:227],a[36:39],v[28:31],v[224:227]\n"
"v_accvgpr_read_b32 v121,a221\n"
"v_accvgpr_read_b32 v122,a222\n"
"v_mfma_f32_16x16x32_bf16 v[224:227],a[32:35],v[24:27],v[224:227]\n"
"v_accvgpr_read_b32 v123,a223\n"
"v_cvt_pk_bf16_f32 v116,v116,v117\n"
"v_mfma_f32_16x16x32_bf16 v[228:231],a[40:43],v[24:27],v[228:231]\n"
"ds_read_b64_tr_b16 a[32:33],v100 offset:32768\n"
"v_cvt_pk_bf16_f32 v117,v118,v119\n"
"v_mfma_f32_16x16x32_bf16 v[228:231],a[44:47],v[28:31],v[228:231]\n"
"ds_read_b64_tr_b16 a[34:35],v100 offset:33792\n"
"v_cvt_pk_bf16_f32 v118,v120,v121\n"
"v_mfma_f32_16x16x32_bf16 v[196:199],a[44:47],v[20:23],v[196:199]\n"
"ds_read_b64_tr_b16 a[36:37],v100 offset:40960\n"
"v_cvt_pk_bf16_f32 v119,v122,v123\n"
"v_mfma_f32_16x16x32_bf16 v[196:199],a[40:43],v[16:19],v[196:199]\n"
"s_add_u32 m0,s85,148480\n"
"ds_read_b64_tr_b16 a[38:39],v100 offset:41984\n"
"v_mfma_f32_16x16x32_bf16 v[164:167],a[40:43],v[8:11],v[164:167]\n"
"buffer_load_dwordx4 v111,s[28:31],s86 offen lds\n"
"v_mfma_f32_16x16x32_bf16 v[164:167],a[44:47],v[12:15],v[164:167]\n"
"s_add_u32 m0,s85,155648\n"
"s_xor_b32 s86,s86,262144\n"
"v_mfma_f32_16x16x32_bf16 v[132:135],a[44:47],v[4:7],v[132:135]\n"
"buffer_load_dwordx4 v110,s[28:31],s86 offen lds\n"
"v_mfma_f32_16x16x32_bf16 v[132:135],a[40:43],v[0:3],v[132:135]\n"
"v_accvgpr_write_b32 a236,v116\n"
"v_accvgpr_write_b32 a237,v117\n"
"v_mfma_f32_16x16x32_bf16 v[136:139],v[32:35],v[0:3],v[136:139]\n"
"ds_read_b64_tr_b16 a[40:41],v101 offset:32768\n"
"v_accvgpr_write_b32 a238,v118\n"
"v_mfma_f32_16x16x32_bf16 v[136:139],v[36:39],v[4:7],v[136:139]\n"
"ds_read_b64_tr_b16 a[42:43],v101 offset:33792\n"
"v_accvgpr_write_b32 a239,v119\n"
"v_mfma_f32_16x16x32_bf16 v[168:171],v[36:39],v[12:15],v[168:171]\n"
"ds_read_b64_tr_b16 a[44:45],v101 offset:40960\n"
"v_accvgpr_read_b32 v116,a208\n"
"v_mfma_f32_16x16x32_bf16 v[168:171],v[32:35],v[8:11],v[168:171]\n"
"s_add_u32 m0,s85,156672\n"
"ds_read_b64_tr_b16 a[46:47],v101 offset:41984\n"
"v_mfma_f32_16x16x32_bf16 v[200:203],v[32:35],v[16:19],v[200:203]\n"
"buffer_load_dwordx4 v111,s[28:31],s86 offen lds\n"
"v_mfma_f32_16x16x32_bf16 v[200:203],v[36:39],v[20:23],v[200:203]\n"
"s_xor_b32 s86,s86,1835008\n"
"v_accvgpr_read_b32 v117,a209\n"
"v_mfma_f32_16x16x32_bf16 v[232:235],v[36:39],v[28:31],v[232:235]\n"
"v_accvgpr_read_b32 v118,a210\n"
"v_accvgpr_read_b32 v119,a211\n"
"v_mfma_f32_16x16x32_bf16 v[232:235],v[32:35],v[24:27],v[232:235]\n"
"v_accvgpr_read_b32 v120,a212\n"
"v_accvgpr_read_b32 v121,a213\n"
"v_mfma_f32_16x16x32_bf16 v[236:239],v[40:43],v[24:27],v[236:239]\n"
"ds_read_b64_tr_b16 v[32:33],v102 offset:32768\n"
"v_accvgpr_read_b32 v122,a214\n"
"v_mfma_f32_16x16x32_bf16 v[236:239],v[44:47],v[28:31],v[236:239]\n"
"ds_read_b64_tr_b16 v[34:35],v102 offset:33792\n"
"v_accvgpr_read_b32 v123,a215\n"
"v_mfma_f32_16x16x32_bf16 v[204:207],v[44:47],v[20:23],v[204:207]\n"
"ds_read_b64_tr_b16 v[36:37],v102 offset:40960\n"
"v_cvt_pk_bf16_f32 v116,v116,v117\n"
"v_mfma_f32_16x16x32_bf16 v[204:207],v[40:43],v[16:19],v[204:207]\n"
"s_add_u32 m0,s78,65536\n"
"ds_read_b64_tr_b16 v[38:39],v102 offset:41984\n"
"v_accvgpr_write_b32 a232,v116\n"
"v_mfma_f32_16x16x32_bf16 v[172:175],v[40:43],v[8:11],v[172:175]\n"
"buffer_load_dwordx4 v112,s[24:27],s76 offen offset:0 sc1 lds\n"
"v_mfma_f32_16x16x32_bf16 v[172:175],v[44:47],v[12:15],v[172:175]\n"
"v_cvt_pk_bf16_f32 v117,v118,v119\n"
"v_cvt_pk_bf16_f32 v118,v120,v121\n"
"v_mfma_f32_16x16x32_bf16 v[140:143],v[44:47],v[4:7],v[140:143]\n"
"buffer_load_dwordx4 v112,s[24:27],s76 offen offset:1024 sc1 lds\n"
"v_mfma_f32_16x16x32_bf16 v[140:143],v[40:43],v[0:3],v[140:143]\n"
"v_cvt_pk_bf16_f32 v119,v122,v123\n"
"s_waitcnt vmcnt(18)\n"
"v_mfma_f32_16x16x32_bf16 v[144:147],v[48:51],v[0:3],v[144:147]\n"
"ds_read_b64_tr_b16 v[40:41],v103 offset:32768\n"
"v_accvgpr_write_b32 a233,v117\n"
"v_lshrrev_b32 v106,8,v112\n"
"v_mfma_f32_16x16x32_bf16 v[144:147],v[52:55],v[4:7],v[144:147]\n"
"ds_read_b64_tr_b16 v[42:43],v103 offset:33792\n"
"v_accvgpr_write_b32 a234,v118\n"
"v_lshlrev_b32 v107,6,v106\n"
"v_mfma_f32_16x16x32_bf16 v[176:179],v[52:55],v[12:15],v[176:179]\n"
"ds_read_b64_tr_b16 v[44:45],v103 offset:40960\n"
"v_accvgpr_write_b32 a235,v119\n"
"v_and_b32 v108,0xf0,v112\n"
"v_mfma_f32_16x16x32_bf16 v[176:179],v[48:51],v[8:11],v[176:179]\n"
"ds_read_b64_tr_b16 v[46:47],v103 offset:41984\n"
"v_accvgpr_read_b32 v116,a200\n"
"v_xor_b32 v107,v107,v108\n"
"v_mfma_f32_16x16x32_bf16 v[208:211],v[48:51],v[16:19],v[208:211]\n"
"buffer_load_dwordx4 v112,s[24:27],s76 offen offset:2048 sc1 lds\n"
"v_lshl_add_u32 v106,v106,11,v107\n"
"v_mfma_f32_16x16x32_bf16 v[208:211],v[52:55],v[20:23],v[208:211]\n"
"ds_read_b128 a[0:3],v96 offset:0\n"
"v_accvgpr_read_b32 v117,a201\n"
"v_lshrrev_b32 v107,6,v112\n"
"v_mfma_f32_16x16x32_bf16 v[240:243],v[52:55],v[28:31],v[240:243]\n"
"v_accvgpr_read_b32 v118,a202\n"
"v_accvgpr_read_b32 v119,a203\n"
"v_and_b32 v108,3,v107\n"
"v_mfma_f32_16x16x32_bf16 v[240:243],v[48:51],v[24:27],v[240:243]\n"
"ds_read_b128 a[8:11],v96 offset:1024\n"
"v_accvgpr_read_b32 v120,a204\n"
"v_lshlrev_b32 v108,6,v108\n"
"v_mfma_f32_16x16x32_bf16 v[244:247],v[56:59],v[24:27],v[244:247]\n"
"ds_read_b64_tr_b16 v[48:49],v104 offset:32768\n"
"v_accvgpr_read_b32 v121,a205\n"
"v_mfma_f32_16x16x32_bf16 v[244:247],v[60:63],v[28:31],v[244:247]\n"
"s_lshl_b32 s82,s52,6\n"
"ds_read_b64_tr_b16 v[50:51],v104 offset:33792\n"
"v_accvgpr_read_b32 v122,a206\n"
"v_xor_b32 v108,s82,v108\n"
"v_mfma_f32_16x16x32_bf16 v[212:215],v[60:63],v[20:23],v[212:215]\n"
"ds_read_b64_tr_b16 v[52:53],v104 offset:40960\n"
"v_accvgpr_read_b32 v123,a207\n"
"v_lshl_add_u32 v107,v107,8,v108\n"
"v_mfma_f32_16x16x32_bf16 v[212:215],v[56:59],v[16:19],v[212:215]\n"
"ds_read_b64_tr_b16 v[54:55],v104 offset:41984\n"
"v_cvt_pk_bf16_f32 v116,v116,v117\n"
"v_and_b32 v108,0x30,v112\n"
"v_mfma_f32_16x16x32_bf16 v[180:183],v[56:59],v[8:11],v[180:183]\n"
"buffer_load_dwordx4 v112,s[24:27],s76 offen offset:3072 sc1 lds\n"
"v_add_u32 v107,v107,v108\n"
"v_mfma_f32_16x16x32_bf16 v[180:183],v[60:63],v[12:15],v[180:183]\n"
"s_add_u32 m0,s78,81920\n"
"s_xor_b32 s76,s76,16384\n"
"ds_read_b128 a[16:19],v96 offset:2048\n"
"v_mfma_f32_16x16x32_bf16 v[148:151],v[60:63],v[4:7],v[148:151]\n"
"buffer_load_dwordx4 v112,s[24:27],s76 offen offset:0 sc1 lds\n"
"v_add_u32 v107,114688,v107\n"
"v_mfma_f32_16x16x32_bf16 v[148:151],v[56:59],v[0:3],v[148:151]\n"
"ds_read_b128 a[24:27],v96 offset:3072\n"
"v_cvt_pk_bf16_f32 v117,v118,v119\n"
"v_mfma_f32_16x16x32_bf16 v[152:155],v[64:67],v[0:3],v[152:155]\n"
"ds_read_b64_tr_b16 v[56:57],v105 offset:32768\n"
"v_cvt_pk_bf16_f32 v118,v120,v121\n"
"v_mfma_f32_16x16x32_bf16 v[152:155],v[68:71],v[4:7],v[152:155]\n"
"ds_read_b64_tr_b16 v[58:59],v105 offset:33792\n"
"v_cvt_pk_bf16_f32 v119,v122,v123\n"
"v_mfma_f32_16x16x32_bf16 v[184:187],v[68:71],v[12:15],v[184:187]\n"
"ds_read_b64_tr_b16 v[60:61],v105 offset:40960\n"
"v_accvgpr_write_b32 a228,v116\n"
"v_mfma_f32_16x16x32_bf16 v[184:187],v[64:67],v[8:11],v[184:187]\n"
"ds_read_b64_tr_b16 v[62:63],v105 offset:41984\n"
"v_accvgpr_write_b32 a229,v117\n"
"v_mfma_f32_16x16x32_bf16 v[216:219],v[64:67],v[16:19],v[216:219]\n"
"ds_read_b128 a[4:7],v96 offset:16384\n"
"v_accvgpr_write_b32 a230,v118\n"
"v_mfma_f32_16x16x32_bf16 v[216:219],v[68:71],v[20:23],v[216:219]\n"
"ds_read_b128 a[12:15],v96 offset:17408\n"
"v_accvgpr_write_b32 a231,v119\n"
"v_mfma_f32_16x16x32_bf16 v[248:251],v[68:71],v[28:31],v[248:251]\n"
"ds_read_b128 a[20:23],v96 offset:18432\n"
"v_accvgpr_read_b32 v116,a192\n"
"v_mfma_f32_16x16x32_bf16 v[248:251],v[64:67],v[24:27],v[248:251]\n"
"ds_read_b128 a[28:31],v96 offset:19456\n"
"v_accvgpr_read_b32 v117,a193\n"
"v_mfma_f32_16x16x32_bf16 v[252:255],v[72:75],v[24:27],v[252:255]\n"
"buffer_load_dwordx4 v112,s[24:27],s76 offen offset:1024 sc1 lds\n"
"v_mfma_f32_16x16x32_bf16 v[252:255],v[76:79],v[28:31],v[252:255]\n"
"v_accvgpr_read_b32 v118,a194\n"
"v_accvgpr_read_b32 v119,a195\n"
"v_mfma_f32_16x16x32_bf16 v[220:223],v[76:79],v[20:23],v[220:223]\n"
"v_accvgpr_read_b32 v120,a196\n"
"v_accvgpr_read_b32 v121,a197\n"
"v_mfma_f32_16x16x32_bf16 v[220:223],v[72:75],v[16:19],v[220:223]\n"
"buffer_load_dwordx4 v112,s[24:27],s76 offen offset:2048 sc1 lds\n"
"v_mfma_f32_16x16x32_bf16 v[188:191],v[72:75],v[8:11],v[188:191]\n"
"v_accvgpr_read_b32 v122,a198\n"
"v_accvgpr_read_b32 v123,a199\n"
"v_mfma_f32_16x16x32_bf16 v[188:191],v[76:79],v[12:15],v[188:191]\n"
"v_cvt_pk_bf16_f32 v116,v116,v117\n"
"v_cvt_pk_bf16_f32 v117,v118,v119\n"
"v_mfma_f32_16x16x32_bf16 v[156:159],v[76:79],v[4:7],v[156:159]\n"
"buffer_load_dwordx4 v112,s[24:27],s76 offen offset:3072 sc1 lds\n"
"v_mfma_f32_16x16x32_bf16 v[156:159],v[72:75],v[0:3],v[156:159]\n"
"s_xor_b32 s76,s76,114688\n"
"v_cvt_pk_bf16_f32 v118,v120,v121\n"
"s_waitcnt vmcnt(20) lgkmcnt(0)\n"
"s_barrier\n"
"v_mfma_f32_16x16x32_bf16 v[128:131],v[80:83],a[0:3],v[128:131]\n"
"ds_read_b64_tr_b16 v[64:65],v98 offset:0\n"
"ds_read_b64_tr_b16 v[72:73],v99 offset:0\n"
"v_mfma_f32_16x16x32_bf16 v[128:131],v[84:87],a[4:7],v[128:131]\n"
"ds_read_b64_tr_b16 v[66:67],v98 offset:1024\n"
"ds_read_b64_tr_b16 v[74:75],v99 offset:1024\n"
"v_mfma_f32_16x16x32_bf16 v[160:163],v[84:87],a[12:15],v[160:163]\n"
"ds_read_b64_tr_b16 v[68:69],v98 offset:8192\n"
"ds_read_b64_tr_b16 v[76:77],v99 offset:8192\n"
"v_mfma_f32_16x16x32_bf16 v[160:163],v[80:83],a[8:11],v[160:163]\n"
"s_add_u32 m0,s85,131072\n"
"ds_read_b64_tr_b16 v[70:71],v98 offset:9216\n"
"ds_read_b64_tr_b16 v[78:79],v99 offset:9216\n"
"v_mfma_f32_16x16x32_bf16 v[192:195],v[80:83],a[16:19],v[192:195]\n"
"buffer_load_dwordx4 v110,s[28:31],s86 offen lds\n"
"v_mfma_f32_16x16x32_bf16 v[192:195],v[84:87],a[20:23],v[192:195]\n"
"s_add_u32 m0,s85,132096\n"
"v_cvt_pk_bf16_f32 v119,v122,v123\n"
"v_mfma_f32_16x16x32_bf16 v[224:227],v[84:87],a[28:31],v[224:227]\n"
"buffer_load_dwordx4 v111,s[28:31],s86 offen lds\n"
"v_mfma_f32_16x16x32_bf16 v[224:227],v[80:83],a[24:27],v[224:227]\n"
"s_xor_b32 s86,s86,262144\n"
"v_accvgpr_write_b32 a224,v116\n"
"v_mfma_f32_16x16x32_bf16 v[228:231],v[88:91],a[24:27],v[228:231]\n"
"ds_read_b64_tr_b16 v[80:81],v100 offset:0\n"
"v_accvgpr_write_b32 a225,v117\n"
"v_mfma_f32_16x16x32_bf16 v[228:231],v[92:95],a[28:31],v[228:231]\n"
"ds_read_b64_tr_b16 v[82:83],v100 offset:1024\n"
"v_accvgpr_write_b32 a226,v118\n"
"v_mfma_f32_16x16x32_bf16 v[196:199],v[92:95],a[20:23],v[196:199]\n"
"ds_read_b64_tr_b16 v[84:85],v100 offset:8192\n"
"v_accvgpr_write_b32 a227,v119\n"
"v_mfma_f32_16x16x32_bf16 v[196:199],v[88:91],a[16:19],v[196:199]\n"
"s_add_u32 m0,s85,139264\n"
"ds_read_b64_tr_b16 v[86:87],v100 offset:9216\n"
"v_mfma_f32_16x16x32_bf16 v[164:167],v[88:91],a[8:11],v[164:167]\n"
"buffer_load_dwordx4 v110,s[28:31],s86 offen lds\n"
"v_mfma_f32_16x16x32_bf16 v[164:167],v[92:95],a[12:15],v[164:167]\n"
"s_add_u32 m0,s85,140288\n"
"s_add_u32 s56,s56,126976\n"
"v_mfma_f32_16x16x32_bf16 v[132:135],v[92:95],a[4:7],v[132:135]\n"
"buffer_load_dwordx4 v111,s[28:31],s86 offen lds\n"
"v_mfma_f32_16x16x32_bf16 v[132:135],v[88:91],a[0:3],v[132:135]\n"
"s_xor_b32 s86,s86,786432\n"
"v_mfma_f32_16x16x32_bf16 v[136:139],a[32:35],a[0:3],v[136:139]\n"
"ds_read_b64_tr_b16 v[88:89],v101 offset:0\n"
"v_mfma_f32_16x16x32_bf16 v[136:139],a[36:39],a[4:7],v[136:139]\n"
"ds_read_b64_tr_b16 v[90:91],v101 offset:1024\n"
"v_mfma_f32_16x16x32_bf16 v[168:171],a[36:39],a[12:15],v[168:171]\n"
"ds_read_b64_tr_b16 v[92:93],v101 offset:8192\n"
"v_mfma_f32_16x16x32_bf16 v[168:171],a[32:35],a[8:11],v[168:171]\n"
"s_add_u32 m0,s78,0\n"
"ds_read_b64_tr_b16 v[94:95],v101 offset:9216\n"
"v_mfma_f32_16x16x32_bf16 v[200:203],a[32:35],a[16:19],v[200:203]\n"
"buffer_load_dwordx4 v112,s[24:27],s76 offen offset:0 sc1 lds\n"
"v_mfma_f32_16x16x32_bf16 v[200:203],a[36:39],a[20:23],v[200:203]\n"
"v_mfma_f32_16x16x32_bf16 v[232:235],a[36:39],a[28:31],v[232:235]\n"
"buffer_load_dwordx4 v112,s[24:27],s76 offen offset:1024 sc1 lds\n"
"v_mfma_f32_16x16x32_bf16 v[232:235],a[32:35],a[24:27],v[232:235]\n"
"v_mfma_f32_16x16x32_bf16 v[236:239],a[40:43],a[24:27],v[236:239]\n"
"ds_read_b64_tr_b16 a[32:33],v102 offset:0\n"
"v_mfma_f32_16x16x32_bf16 v[236:239],a[44:47],a[28:31],v[236:239]\n"
"ds_read_b64_tr_b16 a[34:35],v102 offset:1024\n"
"v_mfma_f32_16x16x32_bf16 v[204:207],a[44:47],a[20:23],v[204:207]\n"
"ds_read_b64_tr_b16 a[36:37],v102 offset:8192\n"
"v_mfma_f32_16x16x32_bf16 v[204:207],a[40:43],a[16:19],v[204:207]\n"
"ds_read_b64_tr_b16 a[38:39],v102 offset:9216\n"
"v_mfma_f32_16x16x32_bf16 v[172:175],a[40:43],a[8:11],v[172:175]\n"
"buffer_load_dwordx4 v112,s[24:27],s76 offen offset:2048 sc1 lds\n"
"v_mfma_f32_16x16x32_bf16 v[172:175],a[44:47],a[12:15],v[172:175]\n"
"v_mfma_f32_16x16x32_bf16 v[140:143],a[44:47],a[4:7],v[140:143]\n"
"buffer_load_dwordx4 v112,s[24:27],s76 offen offset:3072 sc1 lds\n"
"v_mfma_f32_16x16x32_bf16 v[140:143],a[40:43],a[0:3],v[140:143]\n"
"s_xor_b32 s76,s76,16384\n"
"s_waitcnt vmcnt(20)\n"
"v_mfma_f32_16x16x32_bf16 v[144:147],v[32:35],a[0:3],v[144:147]\n"
"ds_read_b64_tr_b16 a[40:41],v103 offset:0\n"
"v_mfma_f32_16x16x32_bf16 v[144:147],v[36:39],a[4:7],v[144:147]\n"
"ds_read_b64_tr_b16 a[42:43],v103 offset:1024\n"
"v_mfma_f32_16x16x32_bf16 v[176:179],v[36:39],a[12:15],v[176:179]\n"
"ds_read_b64_tr_b16 a[44:45],v103 offset:8192\n"
"v_mfma_f32_16x16x32_bf16 v[176:179],v[32:35],a[8:11],v[176:179]\n"
"s_add_u32 m0,s78,16384\n"
"ds_read_b64_tr_b16 a[46:47],v103 offset:9216\n"
"v_mfma_f32_16x16x32_bf16 v[208:211],v[32:35],a[16:19],v[208:211]\n"
"buffer_load_dwordx4 v112,s[24:27],s76 offen offset:0 sc1 lds\n"
"v_mfma_f32_16x16x32_bf16 v[208:211],v[36:39],a[20:23],v[208:211]\n"
"ds_read_b128 v[0:3],v96 offset:32768\n"
"v_mfma_f32_16x16x32_bf16 v[240:243],v[36:39],a[28:31],v[240:243]\n"
"buffer_load_dwordx4 v112,s[24:27],s76 offen offset:1024 sc1 lds\n"
"v_mfma_f32_16x16x32_bf16 v[240:243],v[32:35],a[24:27],v[240:243]\n"
"ds_read_b128 v[8:11],v96 offset:33792\n"
"v_mfma_f32_16x16x32_bf16 v[244:247],v[40:43],a[24:27],v[244:247]\n"
"ds_read_b64_tr_b16 v[32:33],v104 offset:0\n"
"v_mfma_f32_16x16x32_bf16 v[244:247],v[44:47],a[28:31],v[244:247]\n"
"ds_read_b64_tr_b16 v[34:35],v104 offset:1024\n"
"v_mfma_f32_16x16x32_bf16 v[212:215],v[44:47],a[20:23],v[212:215]\n"
"ds_read_b64_tr_b16 v[36:37],v104 offset:8192\n"
"v_mfma_f32_16x16x32_bf16 v[212:215],v[40:43],a[16:19],v[212:215]\n"
"ds_read_b64_tr_b16 v[38:39],v104 offset:9216\n"
"v_mfma_f32_16x16x32_bf16 v[180:183],v[40:43],a[8:11],v[180:183]\n"
"buffer_load_dwordx4 v112,s[24:27],s76 offen offset:2048 sc1 lds\n"
"v_mfma_f32_16x16x32_bf16 v[180:183],v[44:47],a[12:15],v[180:183]\n"
"ds_read_b128 v[16:19],v96 offset:34816\n"
"v_mfma_f32_16x16x32_bf16 v[148:151],v[44:47],a[4:7],v[148:151]\n"
"buffer_load_dwordx4 v112,s[24:27],s76 offen offset:3072 sc1 lds\n"
"v_mfma_f32_16x16x32_bf16 v[148:151],v[40:43],a[0:3],v[148:151]\n"
"s_xor_b32 s76,s76,49152\n"
"ds_read_b128 v[24:27],v96 offset:35840\n"
"v_mfma_f32_16x16x32_bf16 v[152:155],v[48:51],a[0:3],v[152:155]\n"
"ds_read_b64_tr_b16 v[40:41],v105 offset:0\n"
"v_mfma_f32_16x16x32_bf16 v[152:155],v[52:55],a[4:7],v[152:155]\n"
"ds_read_b64_tr_b16 v[42:43],v105 offset:1024\n"
"v_mfma_f32_16x16x32_bf16 v[184:187],v[52:55],a[12:15],v[184:187]\n"
"ds_read_b64_tr_b16 v[44:45],v105 offset:8192\n"
"v_mfma_f32_16x16x32_bf16 v[184:187],v[48:51],a[8:11],v[184:187]\n"
"ds_read_b64_tr_b16 v[46:47],v105 offset:9216\n"
"v_mfma_f32_16x16x32_bf16 v[216:219],v[48:51],a[16:19],v[216:219]\n"
"ds_read_b128 v[4:7],v96 offset:49152\n"
"v_mfma_f32_16x16x32_bf16 v[216:219],v[52:55],a[20:23],v[216:219]\n"
"ds_read_b128 v[12:15],v96 offset:50176\n"
"ds_read_b128 v[20:23],v96 offset:51200\n"
"v_mfma_f32_16x16x32_bf16 v[248:251],v[52:55],a[28:31],v[248:251]\n"
"buffer_store_dwordx2 v[124:125],off,s[24:27],s59 sc1\n"
"v_mfma_f32_16x16x32_bf16 v[248:251],v[48:51],a[24:27],v[248:251]\n"
"s_add_u32 m0,s78,114688\n"
"ds_read_b128 v[28:31],v96 offset:52224\n"
"v_mfma_f32_16x16x32_bf16 v[252:255],v[56:59],a[24:27],v[252:255]\n"
"buffer_load_dwordx4 v106,s[4:7],s13 offen offset:0 nt lds\n"
"v_mfma_f32_16x16x32_bf16 v[252:255],v[60:63],a[28:31],v[252:255]\n"
"s_add_u32 s13,s13,7168\n"
"v_mfma_f32_16x16x32_bf16 v[220:223],v[60:63],a[20:23],v[220:223]\n"
"buffer_load_dwordx4 v106,s[4:7],s13 offen offset:1024 nt lds\n"
"v_mfma_f32_16x16x32_bf16 v[220:223],v[56:59],a[16:19],v[220:223]\n"
"s_add_u32 s13,s13,7168\n"
"v_mfma_f32_16x16x32_bf16 v[188:191],v[56:59],a[8:11],v[188:191]\n"
"buffer_load_dwordx4 v106,s[4:7],s13 offen offset:2048 nt lds\n"
"v_mfma_f32_16x16x32_bf16 v[188:191],v[60:63],a[12:15],v[188:191]\n"
"s_add_u32 s13,s13,7168\n"
"v_mfma_f32_16x16x32_bf16 v[156:159],v[60:63],a[4:7],v[156:159]\n"
"buffer_load_dwordx4 v106,s[4:7],s13 offen offset:3072 nt lds\n"
"v_mfma_f32_16x16x32_bf16 v[156:159],v[56:59],a[0:3],v[156:159]\n"
"s_add_u32 s13,s13,109568\n"
"s_waitcnt vmcnt(25) lgkmcnt(0)\n"
"s_barrier\n"
"v_mfma_f32_16x16x32_bf16 v[128:131],v[64:67],v[0:3],v[128:131]\n"
"ds_read_b64_tr_b16 v[48:49],v98 offset:49152\n"
"ds_read_b64_tr_b16 v[56:57],v99 offset:49152\n"
"v_mfma_f32_16x16x32_bf16 v[128:131],v[68:71],v[4:7],v[128:131]\n"
"ds_read_b64_tr_b16 v[50:51],v98 offset:50176\n"
"ds_read_b64_tr_b16 v[58:59],v99 offset:50176\n"
"v_mfma_f32_16x16x32_bf16 v[160:163],v[68:71],v[12:15],v[160:163]\n"
"ds_read_b64_tr_b16 v[52:53],v98 offset:57344\n"
"ds_read_b64_tr_b16 v[60:61],v99 offset:57344\n"
"v_mfma_f32_16x16x32_bf16 v[160:163],v[64:67],v[8:11],v[160:163]\n"
"s_add_u32 m0,s85,98304\n"
"ds_read_b64_tr_b16 v[54:55],v98 offset:58368\n"
"ds_read_b64_tr_b16 v[62:63],v99 offset:58368\n"
"v_mfma_f32_16x16x32_bf16 v[192:195],v[64:67],v[16:19],v[192:195]\n"
"buffer_load_dwordx4 v110,s[28:31],s86 offen lds\n"
"v_mfma_f32_16x16x32_bf16 v[192:195],v[68:71],v[20:23],v[192:195]\n"
"v_mfma_f32_16x16x32_bf16 v[224:227],v[68:71],v[28:31],v[224:227]\n"
"v_mfma_f32_16x16x32_bf16 v[224:227],v[64:67],v[24:27],v[224:227]\n"
"v_mfma_f32_16x16x32_bf16 v[228:231],v[72:75],v[24:27],v[228:231]\n"
"ds_read_b64_tr_b16 v[64:65],v100 offset:49152\n"
"v_mfma_f32_16x16x32_bf16 v[228:231],v[76:79],v[28:31],v[228:231]\n"
"ds_read_b64_tr_b16 v[66:67],v100 offset:50176\n"
"v_mfma_f32_16x16x32_bf16 v[196:199],v[76:79],v[20:23],v[196:199]\n"
"ds_read_b64_tr_b16 v[68:69],v100 offset:57344\n"
"v_mfma_f32_16x16x32_bf16 v[196:199],v[72:75],v[16:19],v[196:199]\n"
"s_add_u32 m0,s85,99328\n"
"ds_read_b64_tr_b16 v[70:71],v100 offset:58368\n"
"v_mfma_f32_16x16x32_bf16 v[164:167],v[72:75],v[8:11],v[164:167]\n"
"buffer_load_dwordx4 v111,s[28:31],s86 offen lds\n"
"v_mfma_f32_16x16x32_bf16 v[164:167],v[76:79],v[12:15],v[164:167]\n"
"s_add_u32 m0,s85,106496\n"
"s_xor_b32 s86,s86,262144\n"
"v_mfma_f32_16x16x32_bf16 v[132:135],v[76:79],v[4:7],v[132:135]\n"
"buffer_load_dwordx4 v110,s[28:31],s86 offen lds\n"
"v_mfma_f32_16x16x32_bf16 v[132:135],v[72:75],v[0:3],v[132:135]\n"
"v_mfma_f32_16x16x32_bf16 v[136:139],v[80:83],v[0:3],v[136:139]\n"
"ds_read_b64_tr_b16 v[72:73],v101 offset:49152\n"
"v_mfma_f32_16x16x32_bf16 v[136:139],v[84:87],v[4:7],v[136:139]\n"
"ds_read_b64_tr_b16 v[74:75],v101 offset:50176\n"
"v_mfma_f32_16x16x32_bf16 v[168:171],v[84:87],v[12:15],v[168:171]\n"
"ds_read_b64_tr_b16 v[76:77],v101 offset:57344\n"
"v_mfma_f32_16x16x32_bf16 v[168:171],v[80:83],v[8:11],v[168:171]\n"
"s_add_u32 m0,s85,107520\n"
"ds_read_b64_tr_b16 v[78:79],v101 offset:58368\n"
"v_mfma_f32_16x16x32_bf16 v[200:203],v[80:83],v[16:19],v[200:203]\n"
"buffer_load_dwordx4 v111,s[28:31],s86 offen lds\n"
"v_mfma_f32_16x16x32_bf16 v[200:203],v[84:87],v[20:23],v[200:203]\n"
"s_xor_b32 s86,s86,3932160\n"
"v_mfma_f32_16x16x32_bf16 v[232:235],v[84:87],v[28:31],v[232:235]\n"
"v_mfma_f32_16x16x32_bf16 v[232:235],v[80:83],v[24:27],v[232:235]\n"
"v_mfma_f32_16x16x32_bf16 v[236:239],v[88:91],v[24:27],v[236:239]\n"
"ds_read_b64_tr_b16 v[80:81],v102 offset:49152\n"
"v_mfma_f32_16x16x32_bf16 v[236:239],v[92:95],v[28:31],v[236:239]\n"
"ds_read_b64_tr_b16 v[82:83],v102 offset:50176\n"
"v_mfma_f32_16x16x32_bf16 v[204:207],v[92:95],v[20:23],v[204:207]\n"
"ds_read_b64_tr_b16 v[84:85],v102 offset:57344\n"
"v_mfma_f32_16x16x32_bf16 v[204:207],v[88:91],v[16:19],v[204:207]\n"
"s_add_u32 m0,s78,32768\n"
"ds_read_b64_tr_b16 v[86:87],v102 offset:58368\n"
"v_mfma_f32_16x16x32_bf16 v[172:175],v[88:91],v[8:11],v[172:175]\n"
"buffer_load_dwordx4 v112,s[24:27],s76 offen offset:0 sc1 lds\n"
"v_mfma_f32_16x16x32_bf16 v[172:175],v[92:95],v[12:15],v[172:175]\n"
"v_mfma_f32_16x16x32_bf16 v[140:143],v[92:95],v[4:7],v[140:143]\n"
"buffer_load_dwordx4 v112,s[24:27],s76 offen offset:1024 sc1 lds\n"
"v_mfma_f32_16x16x32_bf16 v[140:143],v[88:91],v[0:3],v[140:143]\n"
"s_waitcnt vmcnt(23)\n"
"ds_read_b64_tr_b16 v[88:89],v103 offset:49152\n"
"ds_read_b64_tr_b16 v[90:91],v103 offset:50176\n"
"v_mfma_f32_16x16x32_bf16 v[144:147],a[32:35],v[0:3],v[144:147]\n"
"v_lshrrev_b32 v120,4,v112\n"
"v_and_b32 v121,15,v120\n"
"v_mfma_f32_16x16x32_bf16 v[144:147],a[36:39],v[4:7],v[144:147]\n"
"v_lshrrev_b32 v122,4,v120\n"
"v_lshlrev_b32 v122,2,v122\n"
"v_mfma_f32_16x16x32_bf16 v[176:179],a[36:39],v[12:15],v[176:179]\n"
"ds_read_b64_tr_b16 v[92:93],v103 offset:57344\n"
"v_sub_u32 v121,v121,v122\n"
"v_mfma_f32_16x16x32_bf16 v[176:179],a[32:35],v[8:11],v[176:179]\n"
"s_and_b32 s82,s52,1\n"
"ds_read_b64_tr_b16 v[94:95],v103 offset:58368\n"
"s_lshl_b32 s82,s82,4\n"
"v_add_u32 v121,s82,v121\n"
"v_mfma_f32_16x16x32_bf16 v[208:211],a[32:35],v[16:19],v[208:211]\n"
"buffer_load_dwordx4 v112,s[24:27],s76 offen offset:2048 sc1 lds\n"
"v_cvt_f32_i32 v121,v121\n"
"v_mfma_f32_16x16x32_bf16 v[208:211],a[36:39],v[20:23],v[208:211]\n"
"ds_read_b128 a[96:99],v97 offset:0\n"
"v_add_f32 v121,0.5,v121\n"
"v_mul_f32 v126,0x7149f2ca,v121\n"
"v_mfma_f32_16x16x32_bf16 v[240:243],a[36:39],v[28:31],v[240:243]\n"
"v_and_b32 v120,31,v120\n"
"v_mov_b32 v121,0x3f803f80\n"
"v_mfma_f32_16x16x32_bf16 v[240:243],a[32:35],v[24:27],v[240:243]\n"
"s_mov_b32 s93,0x3f800000\n"
"ds_read_b128 a[104:107],v97 offset:1024\n"
"v_accvgpr_write_b32 a144,v121\n"
"v_mfma_f32_16x16x32_bf16 v[244:247],a[40:43],v[24:27],v[244:247]\n"
"ds_read_b64_tr_b16 a[128:129],v104 offset:49152\n"
"v_accvgpr_write_b32 a146,v121\n"
"v_mfma_f32_16x16x32_bf16 v[244:247],a[44:47],v[28:31],v[244:247]\n"
"ds_read_b64_tr_b16 a[130:131],v104 offset:50176\n"
"v_accvgpr_write_b32 a147,v121\n"
"v_mfma_f32_16x16x32_bf16 v[212:215],a[44:47],v[20:23],v[212:215]\n"
"ds_read_b64_tr_b16 a[132:133],v104 offset:57344\n"
"v_lshlrev_b32 v120,3,v120\n"
"v_mfma_f32_16x16x32_bf16 v[212:215],a[40:43],v[16:19],v[212:215]\n"
"ds_read_b64_tr_b16 a[134:135],v104 offset:58368\n"
"v_accvgpr_write_b32 a145,v121\n"
"v_mfma_f32_16x16x32_bf16 v[180:183],a[40:43],v[8:11],v[180:183]\n"
"buffer_load_dwordx4 v112,s[24:27],s76 offen offset:3072 sc1 lds\n"
"v_mfma_f32_16x16x32_bf16 v[180:183],a[44:47],v[12:15],v[180:183]\n"
"s_add_u32 m0,s78,49152\n"
"s_xor_b32 s76,s76,16384\n"
"ds_read_b128 a[112:115],v97 offset:2048\n"
"v_mfma_f32_16x16x32_bf16 v[148:151],a[44:47],v[4:7],v[148:151]\n"
"buffer_load_dwordx4 v112,s[24:27],s76 offen offset:0 sc1 lds\n"
"v_mfma_f32_16x16x32_bf16 v[148:151],a[40:43],v[0:3],v[148:151]\n"
"ds_read_b128 a[120:123],v97 offset:3072\n"
"v_mfma_f32_16x16x32_bf16 v[152:155],v[32:35],v[0:3],v[152:155]\n"
"ds_read_b64_tr_b16 a[136:137],v105 offset:49152\n"
"v_mfma_f32_16x16x32_bf16 v[152:155],v[36:39],v[4:7],v[152:155]\n"
"ds_read_b64_tr_b16 a[138:139],v105 offset:50176\n"
"v_mfma_f32_16x16x32_bf16 v[184:187],v[36:39],v[12:15],v[184:187]\n"
"ds_read_b64_tr_b16 a[140:141],v105 offset:57344\n"
"v_mfma_f32_16x16x32_bf16 v[184:187],v[32:35],v[8:11],v[184:187]\n"
"ds_read_b64_tr_b16 a[142:143],v105 offset:58368\n"
"v_mfma_f32_16x16x32_bf16 v[216:219],v[32:35],v[16:19],v[216:219]\n"
"ds_read_b128 a[100:103],v97 offset:16384\n"
"v_mfma_f32_16x16x32_bf16 v[216:219],v[36:39],v[20:23],v[216:219]\n"
"ds_read_b128 a[108:111],v97 offset:17408\n"
"v_mfma_f32_16x16x32_bf16 v[248:251],v[36:39],v[28:31],v[248:251]\n"
"ds_read_b128 a[116:119],v97 offset:18432\n"
"v_mfma_f32_16x16x32_bf16 v[248:251],v[32:35],v[24:27],v[248:251]\n"
"ds_read_b128 a[124:127],v97 offset:19456\n"
"v_mfma_f32_16x16x32_bf16 v[252:255],v[40:43],v[24:27],v[252:255]\n"
"buffer_load_dwordx4 v112,s[24:27],s76 offen offset:1024 sc1 lds\n"
"v_mfma_f32_16x16x32_bf16 v[252:255],v[44:47],v[28:31],v[252:255]\n"
"v_mfma_f32_16x16x32_bf16 v[220:223],v[44:47],v[20:23],v[220:223]\n"
"v_mfma_f32_16x16x32_bf16 v[220:223],v[40:43],v[16:19],v[220:223]\n"
"buffer_load_dwordx4 v112,s[24:27],s76 offen offset:2048 sc1 lds\n"
"v_mfma_f32_16x16x32_bf16 v[188:191],v[40:43],v[8:11],v[188:191]\n"
"v_mfma_f32_16x16x32_bf16 v[188:191],v[44:47],v[12:15],v[188:191]\n"
"v_mfma_f32_16x16x32_bf16 v[156:159],v[44:47],v[4:7],v[156:159]\n"
"buffer_load_dwordx4 v112,s[24:27],s76 offen offset:3072 sc1 lds\n"
"v_mfma_f32_16x16x32_bf16 v[156:159],v[40:43],v[0:3],v[156:159]\n"
"s_xor_b32 s76,s76,245760\n"
"s_waitcnt vmcnt(12) lgkmcnt(0)\n"
"s_barrier\n"
"v_mfma_f32_16x16x32_bf16 v[128:131],v[48:51],a[96:99],v[128:131]\n"
"ds_read_b64_tr_b16 v[32:33],v98 offset:32768\n"
"ds_read_b64_tr_b16 v[40:41],v99 offset:32768\n"
"v_mfma_f32_16x16x32_bf16 v[128:131],v[52:55],a[100:103],v[128:131]\n"
"ds_read_b64_tr_b16 v[34:35],v98 offset:33792\n"
"ds_read_b64_tr_b16 v[42:43],v99 offset:33792\n"
"v_mfma_f32_16x16x32_bf16 v[160:163],v[52:55],a[108:111],v[160:163]\n"
"ds_read_b64_tr_b16 v[36:37],v98 offset:40960\n"
"ds_read_b64_tr_b16 v[44:45],v99 offset:40960\n"
"v_mfma_f32_16x16x32_bf16 v[160:163],v[48:51],a[104:107],v[160:163]\n"
"s_add_u32 m0,s85,147456\n"
"ds_read_b64_tr_b16 v[38:39],v98 offset:41984\n"
"ds_read_b64_tr_b16 v[46:47],v99 offset:41984\n"
"v_mfma_f32_16x16x32_bf16 v[192:195],v[48:51],a[112:115],v[192:195]\n"
"buffer_load_dwordx4 v110,s[28:31],s86 offen lds\n"
"ds_read_b64_tr_b16 a[160:161],v107 offset:0\n"
"v_mfma_f32_16x16x32_bf16 v[192:195],v[52:55],a[116:119],v[192:195]\n"
"ds_read_b64_tr_b16 a[162:163],v107 offset:4096\n"
"ds_read_b64_tr_b16 a[164:165],v107 offset:8\n"
"ds_read_b64_tr_b16 a[166:167],v107 offset:4104\n"
"v_mfma_f32_16x16x32_bf16 v[224:227],v[52:55],a[124:127],v[224:227]\n"
"ds_read_b64_tr_b16 a[168:169],v107 offset:8192\n"
"ds_read_b64_tr_b16 a[170:171],v107 offset:12288\n"
"v_mfma_f32_16x16x32_bf16 v[224:227],v[48:51],a[120:123],v[224:227]\n"
"ds_read_b64_tr_b16 a[172:173],v107 offset:8200\n"
"ds_read_b64_tr_b16 a[174:175],v107 offset:12296\n"
"v_mfma_f32_16x16x32_bf16 v[228:231],v[56:59],a[120:123],v[228:231]\n"
"ds_read_b64_tr_b16 v[48:49],v100 offset:32768\n"
"v_mfma_f32_16x16x32_bf16 v[228:231],v[60:63],a[124:127],v[228:231]\n"
"ds_read_b64_tr_b16 v[50:51],v100 offset:33792\n"
"v_mfma_f32_16x16x32_bf16 v[196:199],v[60:63],a[116:119],v[196:199]\n"
"ds_read_b64_tr_b16 v[52:53],v100 offset:40960\n"
"v_mfma_f32_16x16x32_bf16 v[196:199],v[56:59],a[112:115],v[196:199]\n"
"s_add_u32 m0,s85,148480\n"
"ds_read_b64_tr_b16 v[54:55],v100 offset:41984\n"
"v_mfma_f32_16x16x32_bf16 v[164:167],v[56:59],a[104:107],v[164:167]\n"
"buffer_load_dwordx4 v111,s[28:31],s86 offen lds\n"
"v_mfma_f32_16x16x32_bf16 v[164:167],v[60:63],a[108:111],v[164:167]\n"
"s_add_u32 m0,s85,155648\n"
"s_xor_b32 s86,s86,262144\n"
"v_mfma_f32_16x16x32_bf16 v[132:135],v[60:63],a[100:103],v[132:135]\n"
"buffer_load_dwordx4 v110,s[28:31],s86 offen lds\n"
"v_mfma_f32_16x16x32_bf16 v[132:135],v[56:59],a[96:99],v[132:135]\n"
"v_mfma_f32_16x16x32_bf16 v[136:139],v[64:67],a[96:99],v[136:139]\n"
"ds_read_b64_tr_b16 v[56:57],v101 offset:32768\n"
"v_mfma_f32_16x16x32_bf16 v[136:139],v[68:71],a[100:103],v[136:139]\n"
"ds_read_b64_tr_b16 v[58:59],v101 offset:33792\n"
"v_mfma_f32_16x16x32_bf16 v[168:171],v[68:71],a[108:111],v[168:171]\n"
"ds_read_b64_tr_b16 v[60:61],v101 offset:40960\n"
"v_mfma_f32_16x16x32_bf16 v[168:171],v[64:67],a[104:107],v[168:171]\n"
"s_add_u32 m0,s85,156672\n"
"ds_read_b64_tr_b16 v[62:63],v101 offset:41984\n"
"v_mfma_f32_16x16x32_bf16 v[200:203],v[64:67],a[112:115],v[200:203]\n"
"buffer_load_dwordx4 v111,s[28:31],s86 offen lds\n"
"v_mfma_f32_16x16x32_bf16 v[200:203],v[68:71],a[116:119],v[200:203]\n"
"s_xor_b32 s86,s86,786432\n"
"v_mfma_f32_16x16x32_bf16 v[232:235],v[68:71],a[124:127],v[232:235]\n"
"v_mfma_f32_16x16x32_bf16 v[232:235],v[64:67],a[120:123],v[232:235]\n"
"v_mfma_f32_16x16x32_bf16 v[236:239],v[72:75],a[120:123],v[236:239]\n"
"ds_read_b64_tr_b16 v[64:65],v102 offset:32768\n"
"v_mfma_f32_16x16x32_bf16 v[236:239],v[76:79],a[124:127],v[236:239]\n"
"ds_read_b64_tr_b16 v[66:67],v102 offset:33792\n"
"v_mfma_f32_16x16x32_bf16 v[204:207],v[76:79],a[116:119],v[204:207]\n"
"ds_read_b64_tr_b16 v[68:69],v102 offset:40960\n"
"v_mfma_f32_16x16x32_bf16 v[204:207],v[72:75],a[112:115],v[204:207]\n"
"s_add_u32 m0,s78,65536\n"
"ds_read_b64_tr_b16 v[70:71],v102 offset:41984\n"
"v_mfma_f32_16x16x32_bf16 v[172:175],v[72:75],a[104:107],v[172:175]\n"
"buffer_load_dwordx4 v112,s[24:27],s76 offen offset:0 sc1 lds\n"
"v_mfma_f32_16x16x32_bf16 v[172:175],v[76:79],a[108:111],v[172:175]\n"
"v_mfma_f32_16x16x32_bf16 v[140:143],v[76:79],a[100:103],v[140:143]\n"
"buffer_load_dwordx4 v112,s[24:27],s76 offen offset:1024 sc1 lds\n"
"v_mfma_f32_16x16x32_bf16 v[140:143],v[72:75],a[96:99],v[140:143]\n"
"v_mfma_f32_16x16x32_bf16 v[144:147],v[80:83],a[96:99],v[144:147]\n"
"ds_read_b64_tr_b16 v[72:73],v103 offset:32768\n"
"v_accvgpr_write_b32 a156,0\n"
"v_mfma_f32_16x16x32_bf16 v[144:147],v[84:87],a[100:103],v[144:147]\n"
"ds_read_b64_tr_b16 v[74:75],v103 offset:33792\n"
"v_accvgpr_write_b32 a157,0\n"
"v_mfma_f32_16x16x32_bf16 v[176:179],v[84:87],a[108:111],v[176:179]\n"
"ds_read_b64_tr_b16 v[76:77],v103 offset:40960\n"
"v_accvgpr_write_b32 a158,0\n"
"v_mfma_f32_16x16x32_bf16 v[176:179],v[80:83],a[104:107],v[176:179]\n"
"ds_read_b64_tr_b16 v[78:79],v103 offset:41984\n"
"v_accvgpr_write_b32 a159,0\n"
"v_mfma_f32_16x16x32_bf16 v[208:211],v[80:83],a[112:115],v[208:211]\n"
"buffer_load_dwordx4 v112,s[24:27],s76 offen offset:2048 sc1 lds\n"
"v_mfma_f32_16x16x32_bf16 v[208:211],v[84:87],a[116:119],v[208:211]\n"
"ds_read_b128 v[0:3],v96 offset:0\n"
"v_mfma_f32_16x16x32_bf16 v[240:243],v[84:87],a[124:127],v[240:243]\n"
"v_mfma_f32_16x16x32_bf16 v[240:243],v[80:83],a[120:123],v[240:243]\n"
"ds_read_b128 v[8:11],v96 offset:1024\n"
"v_mfma_f32_16x16x32_bf16 v[244:247],v[88:91],a[120:123],v[244:247]\n"
"ds_read_b64_tr_b16 v[80:81],v104 offset:32768\n"
"v_mfma_f32_16x16x32_bf16 v[244:247],v[92:95],a[124:127],v[244:247]\n"
"ds_read_b64_tr_b16 v[82:83],v104 offset:33792\n"
"v_mfma_f32_16x16x32_bf16 v[212:215],v[92:95],a[116:119],v[212:215]\n"
"ds_read_b64_tr_b16 v[84:85],v104 offset:40960\n"
"v_mfma_f32_16x16x32_bf16 v[212:215],v[88:91],a[112:115],v[212:215]\n"
"ds_read_b64_tr_b16 v[86:87],v104 offset:41984\n"
"v_mfma_f32_16x16x32_bf16 v[180:183],v[88:91],a[104:107],v[180:183]\n"
"buffer_load_dwordx4 v112,s[24:27],s76 offen offset:3072 sc1 lds\n"
"v_mfma_f32_16x16x32_bf16 v[180:183],v[92:95],a[108:111],v[180:183]\n"
"s_add_u32 m0,s78,81920\n"
"s_xor_b32 s76,s76,16384\n"
"ds_read_b128 v[16:19],v96 offset:2048\n"
"v_mfma_f32_16x16x32_bf16 v[148:151],v[92:95],a[100:103],v[148:151]\n"
"buffer_load_dwordx4 v112,s[24:27],s76 offen offset:0 sc1 lds\n"
"v_mfma_f32_16x16x32_bf16 v[148:151],v[88:91],a[96:99],v[148:151]\n"
"ds_read_b128 v[24:27],v96 offset:3072\n"
"v_mfma_f32_16x16x32_bf16 v[152:155],a[128:131],a[96:99],v[152:155]\n"
"ds_read_b64_tr_b16 v[88:89],v105 offset:32768\n"
"v_mfma_f32_16x16x32_bf16 v[152:155],a[132:135],a[100:103],v[152:155]\n"
"ds_read_b64_tr_b16 v[90:91],v105 offset:33792\n"
"v_mfma_f32_16x16x32_bf16 v[184:187],a[132:135],a[108:111],v[184:187]\n"
"ds_read_b64_tr_b16 v[92:93],v105 offset:40960\n"
"v_mfma_f32_16x16x32_bf16 v[184:187],a[128:131],a[104:107],v[184:187]\n"
"ds_read_b64_tr_b16 v[94:95],v105 offset:41984\n"
"v_mfma_f32_16x16x32_bf16 v[216:219],a[128:131],a[112:115],v[216:219]\n"
"ds_read_b128 v[4:7],v96 offset:16384\n"
"v_mfma_f32_16x16x32_bf16 v[216:219],a[132:135],a[116:119],v[216:219]\n"
"ds_read_b128 v[12:15],v96 offset:17408\n"
"v_mfma_f32_16x16x32_bf16 v[248:251],a[132:135],a[124:127],v[248:251]\n"
"ds_read_b128 v[20:23],v96 offset:18432\n"
"v_mfma_f32_16x16x32_bf16 v[248:251],a[128:131],a[120:123],v[248:251]\n"
"ds_read_b128 v[28:31],v96 offset:19456\n"
"v_mfma_f32_16x16x32_bf16 v[252:255],a[136:139],a[120:123],v[252:255]\n"
"buffer_load_dwordx4 v112,s[24:27],s76 offen offset:1024 sc1 lds\n"
"v_mfma_f32_16x16x32_bf16 v[252:255],a[140:143],a[124:127],v[252:255]\n"
"v_mfma_f32_16x16x32_bf16 v[220:223],a[140:143],a[116:119],v[220:223]\n"
"v_mfma_f32_16x16x32_bf16 v[220:223],a[136:139],a[112:115],v[220:223]\n"
"buffer_load_dwordx4 v112,s[24:27],s76 offen offset:2048 sc1 lds\n"
"v_mfma_f32_16x16x32_bf16 v[188:191],a[136:139],a[104:107],v[188:191]\n"
"v_mfma_f32_16x16x32_bf16 v[188:191],a[140:143],a[108:111],v[188:191]\n"
"v_mfma_f32_16x16x32_bf16 v[156:159],a[140:143],a[100:103],v[156:159]\n"
"buffer_load_dwordx4 v112,s[24:27],s76 offen offset:3072 sc1 lds\n"
"v_mfma_f32_16x16x32_bf16 v[156:159],a[136:139],a[96:99],v[156:159]\n"
"s_xor_b32 s76,s76,49152\n"
"s_waitcnt vmcnt(20) lgkmcnt(0)\n"
"s_barrier\n"
"v_mfma_f32_16x16x32_bf16 v[128:131],v[32:35],v[0:3],v[128:131]\n"
"ds_read_b64_tr_b16 a[128:129],v98 offset:0\n"
"ds_read_b64_tr_b16 a[136:137],v99 offset:0\n"
"v_mfma_f32_16x16x32_bf16 v[128:131],v[36:39],v[4:7],v[128:131]\n"
"ds_read_b64_tr_b16 a[130:131],v98 offset:1024\n"
"ds_read_b64_tr_b16 a[138:139],v99 offset:1024\n"
"v_mfma_f32_16x16x32_bf16 v[160:163],v[36:39],v[12:15],v[160:163]\n"
"ds_read_b64_tr_b16 a[132:133],v98 offset:8192\n"
"ds_read_b64_tr_b16 a[140:141],v99 offset:8192\n"
"v_mfma_f32_16x16x32_bf16 v[160:163],v[32:35],v[8:11],v[160:163]\n"
"s_add_u32 m0,s85,131072\n"
"ds_read_b64_tr_b16 a[134:135],v98 offset:9216\n"
"ds_read_b64_tr_b16 a[142:143],v99 offset:9216\n"
"v_mfma_f32_16x16x32_bf16 v[192:195],v[32:35],v[16:19],v[192:195]\n"
"buffer_load_dwordx4 v110,s[28:31],s86 offen lds\n"
"v_mfma_f32_16x16x32_bf16 v[192:195],v[36:39],v[20:23],v[192:195]\n"
"s_add_u32 m0,s85,132096\n"
"v_mfma_f32_16x16x32_bf16 v[224:227],v[36:39],v[28:31],v[224:227]\n"
"buffer_load_dwordx4 v111,s[28:31],s86 offen lds\n"
"v_mfma_f32_16x16x32_bf16 v[224:227],v[32:35],v[24:27],v[224:227]\n"
"s_xor_b32 s86,s86,262144\n"
"v_mfma_f32_16x16x32_bf16 v[228:231],v[40:43],v[24:27],v[228:231]\n"
"ds_read_b64_tr_b16 v[32:33],v100 offset:0\n"
"v_mfma_f32_16x16x32_bf16 v[228:231],v[44:47],v[28:31],v[228:231]\n"
"ds_read_b64_tr_b16 v[34:35],v100 offset:1024\n"
"v_mfma_f32_16x16x32_bf16 v[196:199],v[44:47],v[20:23],v[196:199]\n"
"ds_read_b64_tr_b16 v[36:37],v100 offset:8192\n"
"v_mfma_f32_16x16x32_bf16 v[196:199],v[40:43],v[16:19],v[196:199]\n"
"s_add_u32 m0,s85,139264\n"
"ds_read_b64_tr_b16 v[38:39],v100 offset:9216\n"
"v_mfma_f32_16x16x32_bf16 v[164:167],v[40:43],v[8:11],v[164:167]\n"
"buffer_load_dwordx4 v110,s[28:31],s86 offen lds\n"
"v_mfma_f32_16x16x32_bf16 v[164:167],v[44:47],v[12:15],v[164:167]\n"
"s_add_u32 m0,s85,140288\n"
"v_mfma_f32_16x16x32_bf16 v[132:135],v[44:47],v[4:7],v[132:135]\n"
"buffer_load_dwordx4 v111,s[28:31],s86 offen lds\n"
"v_mfma_f32_16x16x32_bf16 v[132:135],v[40:43],v[0:3],v[132:135]\n"
"s_xor_b32 s86,s86,1835008\n"
"v_mfma_f32_16x16x32_bf16 v[136:139],v[48:51],v[0:3],v[136:139]\n"
"ds_read_b64_tr_b16 v[40:41],v101 offset:0\n"
"v_mfma_f32_16x16x32_bf16 v[136:139],v[52:55],v[4:7],v[136:139]\n"
"ds_read_b64_tr_b16 v[42:43],v101 offset:1024\n"
"v_mfma_f32_16x16x32_bf16 v[168:171],v[52:55],v[12:15],v[168:171]\n"
"ds_read_b64_tr_b16 v[44:45],v101 offset:8192\n"
"v_mfma_f32_16x16x32_bf16 v[168:171],v[48:51],v[8:11],v[168:171]\n"
"s_add_u32 m0,s78,0\n"
"ds_read_b64_tr_b16 v[46:47],v101 offset:9216\n"
"v_mfma_f32_16x16x32_bf16 v[200:203],v[48:51],v[16:19],v[200:203]\n"
"buffer_load_dwordx4 v112,s[24:27],s76 offen offset:0 sc1 lds\n"
"v_mfma_f32_16x16x32_bf16 v[200:203],v[52:55],v[20:23],v[200:203]\n"
"v_mfma_f32_16x16x32_bf16 v[232:235],v[52:55],v[28:31],v[232:235]\n"
"buffer_load_dwordx4 v112,s[24:27],s76 offen offset:1024 sc1 lds\n"
"v_mfma_f32_16x16x32_bf16 v[232:235],v[48:51],v[24:27],v[232:235]\n"
"v_mfma_f32_16x16x32_bf16 v[236:239],v[56:59],v[24:27],v[236:239]\n"
"ds_read_b64_tr_b16 v[48:49],v102 offset:0\n"
"v_mfma_f32_16x16x32_bf16 v[236:239],v[60:63],v[28:31],v[236:239]\n"
"ds_read_b64_tr_b16 v[50:51],v102 offset:1024\n"
"v_mfma_f32_16x16x32_bf16 v[204:207],v[60:63],v[20:23],v[204:207]\n"
"ds_read_b64_tr_b16 v[52:53],v102 offset:8192\n"
"v_mfma_f32_16x16x32_bf16 v[204:207],v[56:59],v[16:19],v[204:207]\n"
"ds_read_b64_tr_b16 v[54:55],v102 offset:9216\n"
"v_mfma_f32_16x16x32_bf16 v[172:175],v[56:59],v[8:11],v[172:175]\n"
"buffer_load_dwordx4 v112,s[24:27],s76 offen offset:2048 sc1 lds\n"
"v_mfma_f32_16x16x32_bf16 v[172:175],v[60:63],v[12:15],v[172:175]\n"
"v_mfma_f32_16x16x32_bf16 v[140:143],v[60:63],v[4:7],v[140:143]\n"
"buffer_load_dwordx4 v112,s[24:27],s76 offen offset:3072 sc1 lds\n"
"v_mfma_f32_16x16x32_bf16 v[140:143],v[56:59],v[0:3],v[140:143]\n"
"s_xor_b32 s76,s76,16384\n"
"s_waitcnt vmcnt(20)\n"
"v_mfma_f32_16x16x32_bf16 v[144:147],v[64:67],v[0:3],v[144:147]\n"
"ds_read_b64_tr_b16 v[56:57],v103 offset:0\n"
"v_mfma_f32_16x16x32_bf16 v[144:147],v[68:71],v[4:7],v[144:147]\n"
"ds_read_b64_tr_b16 v[58:59],v103 offset:1024\n"
"v_mfma_f32_16x16x32_bf16 v[176:179],v[68:71],v[12:15],v[176:179]\n"
"ds_read_b64_tr_b16 v[60:61],v103 offset:8192\n"
"v_mfma_f32_16x16x32_bf16 v[176:179],v[64:67],v[8:11],v[176:179]\n"
"s_add_u32 m0,s78,16384\n"
"ds_read_b64_tr_b16 v[62:63],v103 offset:9216\n"
"v_mfma_f32_16x16x32_bf16 v[208:211],v[64:67],v[16:19],v[208:211]\n"
"buffer_load_dwordx4 v112,s[24:27],s76 offen offset:0 sc1 lds\n"
"v_mfma_f32_16x16x32_bf16 v[208:211],v[68:71],v[20:23],v[208:211]\n"
"ds_read_b128 a[96:99],v96 offset:32768\n"
"v_mfma_f32_16x16x32_bf16 v[240:243],v[68:71],v[28:31],v[240:243]\n"
"buffer_load_dwordx4 v112,s[24:27],s76 offen offset:1024 sc1 lds\n"
"v_mfma_f32_16x16x32_bf16 v[240:243],v[64:67],v[24:27],v[240:243]\n"
"ds_read_b128 a[104:107],v96 offset:33792\n"
"v_mfma_f32_16x16x32_bf16 v[244:247],v[72:75],v[24:27],v[244:247]\n"
"ds_read_b64_tr_b16 v[64:65],v104 offset:0\n"
"v_mfma_f32_16x16x32_bf16 v[244:247],v[76:79],v[28:31],v[244:247]\n"
"ds_read_b64_tr_b16 v[66:67],v104 offset:1024\n"
"v_mfma_f32_16x16x32_bf16 v[212:215],v[76:79],v[20:23],v[212:215]\n"
"ds_read_b64_tr_b16 v[68:69],v104 offset:8192\n"
"v_mfma_f32_16x16x32_bf16 v[212:215],v[72:75],v[16:19],v[212:215]\n"
"ds_read_b64_tr_b16 v[70:71],v104 offset:9216\n"
"v_mfma_f32_16x16x32_bf16 v[180:183],v[72:75],v[8:11],v[180:183]\n"
"buffer_load_dwordx4 v112,s[24:27],s76 offen offset:2048 sc1 lds\n"
"v_mfma_f32_16x16x32_bf16 v[180:183],v[76:79],v[12:15],v[180:183]\n"
"ds_read_b128 a[112:115],v96 offset:34816\n"
"v_mfma_f32_16x16x32_bf16 v[148:151],v[76:79],v[4:7],v[148:151]\n"
"buffer_load_dwordx4 v112,s[24:27],s76 offen offset:3072 sc1 lds\n"
"v_mfma_f32_16x16x32_bf16 v[148:151],v[72:75],v[0:3],v[148:151]\n"
"s_xor_b32 s76,s76,114688\n"
"ds_read_b128 a[120:123],v96 offset:35840\n"
"v_mfma_f32_16x16x32_bf16 v[152:155],v[80:83],v[0:3],v[152:155]\n"
"ds_read_b64_tr_b16 v[72:73],v105 offset:0\n"
"v_mfma_f32_16x16x32_bf16 v[152:155],v[84:87],v[4:7],v[152:155]\n"
"ds_read_b64_tr_b16 v[74:75],v105 offset:1024\n"
"v_mfma_f32_16x16x32_bf16 v[184:187],v[84:87],v[12:15],v[184:187]\n"
"ds_read_b64_tr_b16 v[76:77],v105 offset:8192\n"
"v_mfma_f32_16x16x32_bf16 v[184:187],v[80:83],v[8:11],v[184:187]\n"
"ds_read_b64_tr_b16 v[78:79],v105 offset:9216\n"
"v_mfma_f32_16x16x32_bf16 v[216:219],v[80:83],v[16:19],v[216:219]\n"
"ds_read_b128 a[100:103],v96 offset:49152\n"
"v_mfma_f32_16x16x32_bf16 v[216:219],v[84:87],v[20:23],v[216:219]\n"
"ds_read_b128 a[108:111],v96 offset:50176\n"
"ds_read_b128 a[116:119],v96 offset:51200\n"
"v_mfma_f32_16x16x32_bf16 v[248:251],v[84:87],v[28:31],v[248:251]\n"
"s_load_dwordx2 s[32:33],s[0:1],56\n"
"s_load_dwordx2 s[36:37],s[0:1],64\n"
"s_load_dwordx2 s[40:41],s[0:1],72\n"
"v_mfma_f32_16x16x32_bf16 v[248:251],v[80:83],v[24:27],v[248:251]\n"
"s_add_u32 m0,s78,114688\n"
"ds_read_b128 a[124:127],v96 offset:52224\n"
"v_mfma_f32_16x16x32_bf16 v[252:255],v[88:91],v[24:27],v[252:255]\n"
"buffer_load_dwordx4 v106,s[4:7],s13 offen offset:0 nt lds\n"
"v_mfma_f32_16x16x32_bf16 v[252:255],v[92:95],v[28:31],v[252:255]\n"
"s_add_u32 s13,s13,7168\n"
"v_mfma_f32_16x16x32_bf16 v[220:223],v[92:95],v[20:23],v[220:223]\n"
"buffer_load_dwordx4 v106,s[4:7],s13 offen offset:1024 nt lds\n"
"v_mfma_f32_16x16x32_bf16 v[220:223],v[88:91],v[16:19],v[220:223]\n"
"s_add_u32 s13,s13,7168\n"
"v_mfma_f32_16x16x32_bf16 v[188:191],v[88:91],v[8:11],v[188:191]\n"
"buffer_load_dwordx4 v106,s[4:7],s13 offen offset:2048 nt lds\n"
"v_mfma_f32_16x16x32_bf16 v[188:191],v[92:95],v[12:15],v[188:191]\n"
"s_add_u32 s13,s13,7168\n"
"v_mfma_f32_16x16x32_bf16 v[156:159],v[92:95],v[4:7],v[156:159]\n"
"buffer_load_dwordx4 v106,s[4:7],s13 offen offset:3072 nt lds\n"
"v_mfma_f32_16x16x32_bf16 v[156:159],v[88:91],v[0:3],v[156:159]\n"
"s_add_u32 s13,s13,109568\n"
"s_waitcnt vmcnt(24) lgkmcnt(0)\n"
"s_barrier\n"
"v_mfma_f32_16x16x32_bf16 v[128:131],a[128:131],a[96:99],v[128:131]\n"
"ds_read_b64_tr_b16 v[80:81],v98 offset:49152\n"
"ds_read_b64_tr_b16 v[88:89],v99 offset:49152\n"
"v_mfma_f32_16x16x32_bf16 v[128:131],a[132:135],a[100:103],v[128:131]\n"
"ds_read_b64_tr_b16 v[82:83],v98 offset:50176\n"
"ds_read_b64_tr_b16 v[90:91],v99 offset:50176\n"
"v_mfma_f32_16x16x32_bf16 v[160:163],a[132:135],a[108:111],v[160:163]\n"
"ds_read_b64_tr_b16 v[84:85],v98 offset:57344\n"
"ds_read_b64_tr_b16 v[92:93],v99 offset:57344\n"
"v_mfma_f32_16x16x32_bf16 v[160:163],a[128:131],a[104:107],v[160:163]\n"
"s_add_u32 m0,s85,98304\n"
"ds_read_b64_tr_b16 v[86:87],v98 offset:58368\n"
"ds_read_b64_tr_b16 v[94:95],v99 offset:58368\n"
"v_mfma_f32_16x16x32_bf16 v[192:195],a[128:131],a[112:115],v[192:195]\n"
"buffer_load_dwordx4 v110,s[28:31],s86 offen lds\n"
"v_mfma_f32_16x16x32_bf16 v[192:195],a[132:135],a[116:119],v[192:195]\n"
"v_accvgpr_write_b32 a154,v124\n"
"v_accvgpr_write_b32 a155,v125\n"
"v_mfma_f32_16x16x32_bf16 v[224:227],a[132:135],a[124:127],v[224:227]\n"
"v_accvgpr_write_b32 a152,1\n"
"v_lshrrev_b32 v124,2,v112\n"
"v_mfma_f32_16x16x32_bf16 v[224:227],a[128:131],a[120:123],v[224:227]\n"
"v_add_u32 v124,147456,v124\n"
"v_mov_b32 v125,0\n"
"v_mfma_f32_16x16x32_bf16 v[228:231],a[136:139],a[120:123],v[228:231]\n"
"ds_read_b64_tr_b16 a[128:129],v100 offset:49152\n"
"s_mov_b32 s101,0\n"
"v_mfma_f32_16x16x32_bf16 v[228:231],a[140:143],a[124:127],v[228:231]\n"
"ds_read_b64_tr_b16 a[130:131],v100 offset:50176\n"
"v_mfma_f32_16x16x32_bf16 v[196:199],a[140:143],a[116:119],v[196:199]\n"
"ds_read_b64_tr_b16 a[132:133],v100 offset:57344\n"
"v_mfma_f32_16x16x32_bf16 v[196:199],a[136:139],a[112:115],v[196:199]\n"
"s_add_u32 m0,s85,99328\n"
"ds_read_b64_tr_b16 a[134:135],v100 offset:58368\n"
"v_mfma_f32_16x16x32_bf16 v[164:167],a[136:139],a[104:107],v[164:167]\n"
"buffer_load_dwordx4 v111,s[28:31],s86 offen lds\n"
"v_mfma_f32_16x16x32_bf16 v[164:167],a[140:143],a[108:111],v[164:167]\n"
"s_add_u32 m0,s85,106496\n"
"s_xor_b32 s86,s86,262144\n"
"v_mfma_f32_16x16x32_bf16 v[132:135],a[140:143],a[100:103],v[132:135]\n"
"buffer_load_dwordx4 v110,s[28:31],s86 offen lds\n"
"v_mfma_f32_16x16x32_bf16 v[132:135],a[136:139],a[96:99],v[132:135]\n"
"v_mfma_f32_16x16x32_bf16 v[136:139],v[32:35],a[96:99],v[136:139]\n"
"ds_read_b64_tr_b16 a[136:137],v101 offset:49152\n"
"v_mfma_f32_16x16x32_bf16 v[136:139],v[36:39],a[100:103],v[136:139]\n"
"ds_read_b64_tr_b16 a[138:139],v101 offset:50176\n"
"v_mfma_f32_16x16x32_bf16 v[168:171],v[36:39],a[108:111],v[168:171]\n"
"ds_read_b64_tr_b16 a[140:141],v101 offset:57344\n"
"v_mfma_f32_16x16x32_bf16 v[168:171],v[32:35],a[104:107],v[168:171]\n"
"s_add_u32 m0,s85,107520\n"
"ds_read_b64_tr_b16 a[142:143],v101 offset:58368\n"
"v_mfma_f32_16x16x32_bf16 v[200:203],v[32:35],a[112:115],v[200:203]\n"
"buffer_load_dwordx4 v111,s[28:31],s86 offen lds\n"
"v_mfma_f32_16x16x32_bf16 v[200:203],v[36:39],a[116:119],v[200:203]\n"
"s_xor_b32 s86,s86,786432\n"
"v_mfma_f32_16x16x32_bf16 v[232:235],v[36:39],a[124:127],v[232:235]\n"
"v_mfma_f32_16x16x32_bf16 v[232:235],v[32:35],a[120:123],v[232:235]\n"
"v_mfma_f32_16x16x32_bf16 v[236:239],v[40:43],a[120:123],v[236:239]\n"
"ds_read_b64_tr_b16 v[32:33],v102 offset:49152\n"
"v_mfma_f32_16x16x32_bf16 v[236:239],v[44:47],a[124:127],v[236:239]\n"
"ds_read_b64_tr_b16 v[34:35],v102 offset:50176\n"
"v_mfma_f32_16x16x32_bf16 v[204:207],v[44:47],a[116:119],v[204:207]\n"
"ds_read_b64_tr_b16 v[36:37],v102 offset:57344\n"
"v_mfma_f32_16x16x32_bf16 v[204:207],v[40:43],a[112:115],v[204:207]\n"
"s_add_u32 m0,s78,32768\n"
"ds_read_b64_tr_b16 v[38:39],v102 offset:58368\n"
"buffer_load_dwordx4 v112,s[24:27],s76 offen offset:0 sc1 lds\n"
"v_mfma_f32_16x16x32_bf16 v[172:175],v[40:43],a[104:107],v[172:175]\n"
"buffer_load_dwordx2 v[118:119],v120,s[24:27],s63 offen sc1\n"
"v_mfma_f32_16x16x32_bf16 v[172:175],v[44:47],a[108:111],v[172:175]\n"
"v_mfma_f32_16x16x32_bf16 v[140:143],v[44:47],a[100:103],v[140:143]\n"
"buffer_load_dwordx4 v112,s[24:27],s76 offen offset:1024 sc1 lds\n"
"v_mfma_f32_16x16x32_bf16 v[140:143],v[40:43],a[96:99],v[140:143]\n"
"s_waitcnt vmcnt(23)\n"
"v_mfma_f32_16x16x32_bf16 v[144:147],v[48:51],a[96:99],v[144:147]\n"
"ds_read_b64_tr_b16 v[40:41],v103 offset:49152\n"
"s_and_b32 s82,s100,7\n"
"v_mfma_f32_16x16x32_bf16 v[144:147],v[52:55],a[100:103],v[144:147]\n"
"ds_read_b64_tr_b16 v[42:43],v103 offset:50176\n"
"s_mul_i32 s82,s82,28\n"
"v_mfma_f32_16x16x32_bf16 v[176:179],v[52:55],a[108:111],v[176:179]\n"
"ds_read_b64_tr_b16 v[44:45],v103 offset:57344\n"
"s_lshr_b32 s83,s100,3\n"
"v_mfma_f32_16x16x32_bf16 v[176:179],v[48:51],a[104:107],v[176:179]\n"
"ds_read_b64_tr_b16 v[46:47],v103 offset:58368\n"
"s_add_u32 s23,s82,s83\n"
"v_mfma_f32_16x16x32_bf16 v[208:211],v[48:51],a[112:115],v[208:211]\n"
"buffer_load_dwordx4 v112,s[24:27],s76 offen offset:2048 sc1 lds\n"
"v_mfma_f32_16x16x32_bf16 v[208:211],v[52:55],a[116:119],v[208:211]\n"
"s_cmp_lt_u32 s83,28\n"
"s_cselect_b32 s34,1827872,0\n"
"s_cselect_b32 s42,8192,0\n"
"s_cselect_b32 s23,s23,0\n"
"v_mfma_f32_16x16x32_bf16 v[240:243],v[52:55],a[124:127],v[240:243]\n"
"s_mov_b32 s35,0x20000\n"
"ds_read_b128 v[0:3],v97 offset:0\n"
"s_mov_b32 s43,0x20000\n"
"v_mfma_f32_16x16x32_bf16 v[240:243],v[48:51],a[120:123],v[240:243]\n"
"ds_read_b128 v[8:11],v97 offset:1024\n"
"s_lshl2_add_u32 s82,s83,s52\n"
"v_mfma_f32_16x16x32_bf16 v[244:247],v[56:59],a[120:123],v[244:247]\n"
"ds_read_b64_tr_b16 v[48:49],v104 offset:49152\n"
"s_mul_i32 s82,s82,57344\n"
"v_mfma_f32_16x16x32_bf16 v[244:247],v[60:63],a[124:127],v[244:247]\n"
"ds_read_b64_tr_b16 v[50:51],v104 offset:50176\n"
"s_and_b32 s83,s100,7\n"
"v_mfma_f32_16x16x32_bf16 v[212:215],v[60:63],a[116:119],v[212:215]\n"
"ds_read_b64_tr_b16 v[52:53],v104 offset:57344\n"
"s_mul_i32 s83,s83,896\n"
"v_mfma_f32_16x16x32_bf16 v[212:215],v[56:59],a[112:115],v[212:215]\n"
"ds_read_b64_tr_b16 v[54:55],v104 offset:58368\n"
"s_add_u32 s82,s82,s83\n"
"v_mfma_f32_16x16x32_bf16 v[180:183],v[56:59],a[104:107],v[180:183]\n"
"buffer_load_dwordx4 v112,s[24:27],s76 offen offset:3072 sc1 lds\n"
"v_mfma_f32_16x16x32_bf16 v[180:183],v[60:63],a[108:111],v[180:183]\n"
"s_add_u32 m0,s78,49152\n"
"s_xor_b32 s76,s76,16384\n"
"ds_read_b128 v[16:19],v97 offset:2048\n"
"v_mfma_f32_16x16x32_bf16 v[148:151],v[60:63],a[100:103],v[148:151]\n"
"buffer_load_dwordx4 v112,s[24:27],s76 offen offset:0 sc1 lds\n"
"v_mfma_f32_16x16x32_bf16 v[148:151],v[56:59],a[96:99],v[148:151]\n"
"s_add_u32 s44,s32,s82\n"
"ds_read_b128 v[24:27],v97 offset:3072\n"
"s_addc_u32 s45,s33,0\n"
"v_mfma_f32_16x16x32_bf16 v[152:155],v[64:67],a[96:99],v[152:155]\n"
"s_add_u32 s48,s36,s82\n"
"ds_read_b64_tr_b16 v[56:57],v105 offset:49152\n"
"s_addc_u32 s49,s37,0\n"
"v_mfma_f32_16x16x32_bf16 v[152:155],v[68:71],a[100:103],v[152:155]\n"
"ds_read_b64_tr_b16 v[58:59],v105 offset:50176\n"
"s_mov_b32 s46,51072\n"
"v_mfma_f32_16x16x32_bf16 v[184:187],v[68:71],a[108:111],v[184:187]\n"
"ds_read_b64_tr_b16 v[60:61],v105 offset:57344\n"
"s_mov_b32 s50,s46\n"
"v_mfma_f32_16x16x32_bf16 v[184:187],v[64:67],a[104:107],v[184:187]\n"
"ds_read_b64_tr_b16 v[62:63],v105 offset:58368\n"
"s_mov_b32 s47,0x20000\n"
"v_mfma_f32_16x16x32_bf16 v[216:219],v[64:67],a[112:115],v[216:219]\n"
"ds_read_b128 v[4:7],v97 offset:16384\n"
"s_mov_b32 s51,0x20000\n"
"v_mfma_f32_16x16x32_bf16 v[216:219],v[68:71],a[116:119],v[216:219]\n"
"ds_read_b128 v[12:15],v97 offset:17408\n"
"s_mul_i32 s82,s52,1835008\n"
"v_mfma_f32_16x16x32_bf16 v[248:251],v[68:71],a[124:127],v[248:251]\n"
"ds_read_b128 v[20:23],v97 offset:18432\n"
"s_lshl_b32 s83,s23,5\n"
"v_mfma_f32_16x16x32_bf16 v[248:251],v[64:67],a[120:123],v[248:251]\n"
"ds_read_b128 v[28:31],v97 offset:19456\n"
"s_add_u32 s82,s82,s83\n"
"v_mfma_f32_16x16x32_bf16 v[252:255],v[72:75],a[120:123],v[252:255]\n"
"buffer_load_dwordx4 v112,s[24:27],s76 offen offset:1024 sc1 lds\n"
"v_mfma_f32_16x16x32_bf16 v[252:255],v[76:79],a[124:127],v[252:255]\n"
"s_add_u32 s32,s32,s82\n"
"s_addc_u32 s33,s33,0\n"
"s_add_u32 s36,s36,s82\n"
"v_mfma_f32_16x16x32_bf16 v[220:223],v[76:79],a[116:119],v[220:223]\n"
"s_addc_u32 s37,s37,0\n"
"s_lshl2_add_u32 s82,s23,s52\n"
"s_lshl_b32 s82,s82,13\n"
"v_mfma_f32_16x16x32_bf16 v[220:223],v[72:75],a[112:115],v[220:223]\n"
"buffer_load_dwordx4 v112,s[24:27],s76 offen offset:2048 sc1 lds\n"
"v_mfma_f32_16x16x32_bf16 v[188:191],v[72:75],a[104:107],v[188:191]\n"
"s_add_u32 s40,s40,s82\n"
"s_addc_u32 s41,s41,0\n"
"v_bfe_u32 v121,v112,5,2\n"
"v_mfma_f32_16x16x32_bf16 v[188:191],v[76:79],a[108:111],v[188:191]\n"
"v_bfe_u32 v122,v112,7,2\n"
"v_lshl_add_u32 v121,v122,3,v121\n"
"v_mfma_f32_16x16x32_bf16 v[156:159],v[76:79],a[100:103],v[156:159]\n"
"buffer_load_dwordx4 v112,s[24:27],s76 offen offset:3072 sc1 lds\n"
"v_mfma_f32_16x16x32_bf16 v[156:159],v[72:75],a[96:99],v[156:159]\n"
"s_xor_b32 s76,s76,49152\n"
"v_bfe_u32 v122,v112,9,1\n"
"s_waitcnt vmcnt(13) lgkmcnt(0)\n"
"s_barrier\n"
"v_mfma_f32_16x16x32_bf16 v[128:131],v[80:83],v[0:3],v[128:131]\n"
"ds_read_b64_tr_b16 v[64:65],v98 offset:32768\n"
"ds_read_b64_tr_b16 v[72:73],v99 offset:32768\n"
"v_mfma_f32_16x16x32_bf16 v[128:131],v[84:87],v[4:7],v[128:131]\n"
"ds_read_b64_tr_b16 v[66:67],v98 offset:33792\n"
"ds_read_b64_tr_b16 v[74:75],v99 offset:33792\n"
"v_mfma_f32_16x16x32_bf16 v[160:163],v[84:87],v[12:15],v[160:163]\n"
"ds_read_b64_tr_b16 v[68:69],v98 offset:40960\n"
"ds_read_b64_tr_b16 v[76:77],v99 offset:40960\n"
"v_mfma_f32_16x16x32_bf16 v[160:163],v[80:83],v[8:11],v[160:163]\n"
"s_add_u32 m0,s85,147456\n"
"ds_read_b64_tr_b16 v[70:71],v98 offset:41984\n"
"ds_read_b64_tr_b16 v[78:79],v99 offset:41984\n"
"v_mfma_f32_16x16x32_bf16 v[192:195],v[80:83],v[16:19],v[192:195]\n"
"buffer_load_dwordx4 v110,s[28:31],s86 offen lds\n"
"ds_read_b64_tr_b16 a[176:177],v107 offset:0\n"
"v_mfma_f32_16x16x32_bf16 v[192:195],v[84:87],v[20:23],v[192:195]\n"
"s_add_u32 m0,s85,148480\n"
"ds_read_b64_tr_b16 a[178:179],v107 offset:4096\n"
"ds_read_b64_tr_b16 a[180:181],v107 offset:8\n"
"v_mfma_f32_16x16x32_bf16 v[224:227],v[84:87],v[28:31],v[224:227]\n"
"buffer_load_dwordx4 v111,s[28:31],s86 offen lds\n"
"ds_read_b64_tr_b16 a[182:183],v107 offset:4104\n"
"v_mfma_f32_16x16x32_bf16 v[224:227],v[80:83],v[24:27],v[224:227]\n"
"s_xor_b32 s86,s86,262144\n"
"ds_read_b64_tr_b16 a[184:185],v107 offset:8192\n"
"ds_read_b64_tr_b16 a[186:187],v107 offset:12288\n"
"v_mfma_f32_16x16x32_bf16 v[228:231],v[88:91],v[24:27],v[228:231]\n"
"ds_read_b64_tr_b16 v[80:81],v100 offset:32768\n"
"s_waitcnt vmcnt(9)\n"
"v_xor_b32 v116,s80,v118\n"
"v_mfma_f32_16x16x32_bf16 v[228:231],v[92:95],v[28:31],v[228:231]\n"
"ds_read_b64_tr_b16 v[82:83],v100 offset:33792\n"
"ds_read_b64_tr_b16 a[188:189],v107 offset:8200\n"
"v_xor_b32 v117,s81,v119\n"
"v_mfma_f32_16x16x32_bf16 v[196:199],v[92:95],v[20:23],v[196:199]\n"
"ds_read_b64_tr_b16 v[84:85],v100 offset:40960\n"
"ds_read_b64_tr_b16 a[190:191],v107 offset:12296\n"
"v_mfma_f32_16x16x32_bf16 v[196:199],v[88:91],v[16:19],v[196:199]\n"
"s_add_u32 m0,s85,155648\n"
"ds_read_b64_tr_b16 v[86:87],v100 offset:41984\n"
"v_or_b32 v116,v116,v117\n"
"v_mfma_f32_16x16x32_bf16 v[164:167],v[88:91],v[8:11],v[164:167]\n"
"buffer_load_dwordx4 v110,s[28:31],s86 offen lds\n"
"v_mfma_f32_16x16x32_bf16 v[164:167],v[92:95],v[12:15],v[164:167]\n"
"s_add_u32 m0,s85,156672\n"
"v_cmp_ne_u32 vcc,0,v116\n"
"v_mfma_f32_16x16x32_bf16 v[132:135],v[92:95],v[4:7],v[132:135]\n"
"buffer_load_dwordx4 v111,s[28:31],s86 offen lds\n"
"v_mfma_f32_16x16x32_bf16 v[132:135],v[88:91],v[0:3],v[132:135]\n"
"s_xor_b32 s86,s86,16515072\n"
"s_cbranch_vccnz .Lfk_kv_s\n"
".Lfk_kv_ok:\n"
"v_mfma_f32_16x16x32_bf16 v[136:139],a[128:131],v[0:3],v[136:139]\n"
"ds_read_b64_tr_b16 v[88:89],v101 offset:32768\n"
"v_lshl_add_u32 v121,v122,2,v121\n"
"v_mfma_f32_16x16x32_bf16 v[136:139],a[132:135],v[4:7],v[136:139]\n"
"ds_read_b64_tr_b16 v[90:91],v101 offset:33792\n"
"v_mul_u32_u24 v121,7168,v121\n"
"v_mfma_f32_16x16x32_bf16 v[168:171],a[132:135],v[12:15],v[168:171]\n"
"ds_read_b64_tr_b16 v[92:93],v101 offset:40960\n"
"v_and_b32 v122,16,v112\n"
"v_mfma_f32_16x16x32_bf16 v[168:171],a[128:131],v[8:11],v[168:171]\n"
"s_add_u32 m0,s85,81920\n"
"s_add_u32 s77,s73,16384\n"
"ds_read_b64_tr_b16 v[94:95],v101 offset:41984\n"
"v_mfma_f32_16x16x32_bf16 v[200:203],a[128:131],v[16:19],v[200:203]\n"
"buffer_load_dwordx4 v112,s[24:27],s77 offen offset:0 sc1 lds\n"
"v_mfma_f32_16x16x32_bf16 v[200:203],a[132:135],v[20:23],v[200:203]\n"
"v_add_u32 v115,v121,v122\n"
"s_mov_b32 s54,-229376\n"
"v_mfma_f32_16x16x32_bf16 v[232:235],a[132:135],v[28:31],v[232:235]\n"
"buffer_load_dwordx4 v112,s[24:27],s77 offen offset:1024 sc1 lds\n"
"v_mfma_f32_16x16x32_bf16 v[232:235],a[128:131],v[24:27],v[232:235]\n"
"s_lshl_b32 s55,s52,10\n"
"s_add_u32 s55,s55,131072\n"
"v_mfma_f32_16x16x32_bf16 v[236:239],a[136:139],v[24:27],v[236:239]\n"
"ds_read_b64_tr_b16 a[128:129],v102 offset:32768\n"
"s_mov_b32 s22,229376\n"
"v_mfma_f32_16x16x32_bf16 v[236:239],a[140:143],v[28:31],v[236:239]\n"
"ds_read_b64_tr_b16 a[130:131],v102 offset:33792\n"
"v_lshrrev_b32 v121,7,v112\n"
"v_mfma_f32_16x16x32_bf16 v[204:207],a[140:143],v[20:23],v[204:207]\n"
"ds_read_b64_tr_b16 a[132:133],v102 offset:40960\n"
"v_mul_u32_u24 v121,7168,v121\n"
"v_mfma_f32_16x16x32_bf16 v[204:207],a[136:139],v[16:19],v[204:207]\n"
"s_add_u32 m0,s85,65536\n"
"s_add_u32 s77,s73,65536\n"
"ds_read_b64_tr_b16 a[134:135],v102 offset:41984\n"
"v_mfma_f32_16x16x32_bf16 v[172:175],a[136:139],v[8:11],v[172:175]\n"
"buffer_load_dwordx4 v112,s[24:27],s77 offen offset:0 sc1 lds\n"
"v_mfma_f32_16x16x32_bf16 v[172:175],a[140:143],v[12:15],v[172:175]\n"
"v_lshlrev_b32 v123,3,v112\n"
"v_and_b32 v122,0x380,v123\n"
"v_mfma_f32_16x16x32_bf16 v[140:143],a[140:143],v[4:7],v[140:143]\n"
"buffer_load_dwordx4 v112,s[24:27],s77 offen offset:1024 sc1 lds\n"
"v_mfma_f32_16x16x32_bf16 v[140:143],a[136:139],v[0:3],v[140:143]\n"
"v_min_u32 v122,0x300,v122\n"
"v_add_u32 v122,v121,v122\n"
"v_mfma_f32_16x16x32_bf16 v[144:147],v[32:35],v[0:3],v[144:147]\n"
"ds_read_b64_tr_b16 a[136:137],v103 offset:32768\n"
"v_mfma_f32_16x16x32_bf16 v[144:147],v[36:39],v[4:7],v[144:147]\n"
"ds_read_b64_tr_b16 a[138:139],v103 offset:33792\n"
"v_mfma_f32_16x16x32_bf16 v[176:179],v[36:39],v[12:15],v[176:179]\n"
"ds_read_b64_tr_b16 a[140:141],v103 offset:40960\n"
"v_mfma_f32_16x16x32_bf16 v[176:179],v[32:35],v[8:11],v[176:179]\n"
"s_add_u32 m0,s85,73728\n"
"s_add_u32 s77,s73,24576\n"
"ds_read_b64_tr_b16 a[142:143],v103 offset:41984\n"
"v_mfma_f32_16x16x32_bf16 v[208:211],v[32:35],v[16:19],v[208:211]\n"
"buffer_load_dwordx4 v112,s[24:27],s77 offen offset:0 sc1 lds\n"
"v_mfma_f32_16x16x32_bf16 v[208:211],v[36:39],v[20:23],v[208:211]\n"
"ds_read_b128 a[96:99],v96 offset:0\n"
"v_mfma_f32_16x16x32_bf16 v[240:243],v[36:39],v[28:31],v[240:243]\n"
"buffer_load_dwordx4 v112,s[24:27],s77 offen offset:1024 sc1 lds\n"
"v_mfma_f32_16x16x32_bf16 v[240:243],v[32:35],v[24:27],v[240:243]\n"
"ds_read_b128 a[104:107],v96 offset:1024\n"
"v_mfma_f32_16x16x32_bf16 v[244:247],v[40:43],v[24:27],v[244:247]\n"
"ds_read_b64_tr_b16 v[32:33],v104 offset:32768\n"
"v_mfma_f32_16x16x32_bf16 v[244:247],v[44:47],v[28:31],v[244:247]\n"
"ds_read_b64_tr_b16 v[34:35],v104 offset:33792\n"
"v_mfma_f32_16x16x32_bf16 v[212:215],v[44:47],v[20:23],v[212:215]\n"
"ds_read_b64_tr_b16 v[36:37],v104 offset:40960\n"
"v_mfma_f32_16x16x32_bf16 v[212:215],v[40:43],v[16:19],v[212:215]\n"
"ds_read_b64_tr_b16 v[38:39],v104 offset:41984\n"
"s_mov_b32 s50,22400\n"
"v_mfma_f32_16x16x32_bf16 v[180:183],v[40:43],v[8:11],v[180:183]\n"
"buffer_load_dword a148,v122,s[48:51],0 offen\n"
"v_mfma_f32_16x16x32_bf16 v[180:183],v[44:47],v[12:15],v[180:183]\n"
"ds_read_b128 a[112:115],v96 offset:2048\n"
"v_mfma_f32_16x16x32_bf16 v[148:151],v[44:47],v[4:7],v[148:151]\n"
"v_mfma_f32_16x16x32_bf16 v[148:151],v[40:43],v[0:3],v[148:151]\n"
"ds_read_b128 a[120:123],v96 offset:3072\n"
"v_mfma_f32_16x16x32_bf16 v[152:155],v[48:51],v[0:3],v[152:155]\n"
"ds_read_b64_tr_b16 v[40:41],v105 offset:32768\n"
"v_mfma_f32_16x16x32_bf16 v[152:155],v[52:55],v[4:7],v[152:155]\n"
"ds_read_b64_tr_b16 v[42:43],v105 offset:33792\n"
"v_mfma_f32_16x16x32_bf16 v[184:187],v[52:55],v[12:15],v[184:187]\n"
"ds_read_b64_tr_b16 v[44:45],v105 offset:40960\n"
"v_mfma_f32_16x16x32_bf16 v[184:187],v[48:51],v[8:11],v[184:187]\n"
"ds_read_b64_tr_b16 v[46:47],v105 offset:41984\n"
"v_mfma_f32_16x16x32_bf16 v[216:219],v[48:51],v[16:19],v[216:219]\n"
"ds_read_b128 a[100:103],v96 offset:16384\n"
"v_mfma_f32_16x16x32_bf16 v[216:219],v[52:55],v[20:23],v[216:219]\n"
"ds_read_b128 a[108:111],v96 offset:17408\n"
"v_mfma_f32_16x16x32_bf16 v[248:251],v[52:55],v[28:31],v[248:251]\n"
"ds_read_b128 a[116:119],v96 offset:18432\n"
"v_mfma_f32_16x16x32_bf16 v[248:251],v[48:51],v[24:27],v[248:251]\n"
"ds_read_b128 a[124:127],v96 offset:19456\n"
"v_mfma_f32_16x16x32_bf16 v[252:255],v[56:59],v[24:27],v[252:255]\n"
"v_mfma_f32_16x16x32_bf16 v[252:255],v[60:63],v[28:31],v[252:255]\n"
"buffer_store_dwordx4 a[160:163],v112,s[24:27],s3 offen offset:0 sc1\n"
"v_mfma_f32_16x16x32_bf16 v[220:223],v[60:63],v[20:23],v[220:223]\n"
"v_mfma_f32_16x16x32_bf16 v[220:223],v[56:59],v[16:19],v[220:223]\n"
"v_mfma_f32_16x16x32_bf16 v[188:191],v[56:59],v[8:11],v[188:191]\n"
"v_mfma_f32_16x16x32_bf16 v[188:191],v[60:63],v[12:15],v[188:191]\n"
"buffer_store_dwordx4 a[164:167],v112,s[24:27],s3 offen offset:1024 sc1\n"
"v_mfma_f32_16x16x32_bf16 v[156:159],v[60:63],v[4:7],v[156:159]\n"
"v_mfma_f32_16x16x32_bf16 v[156:159],v[56:59],v[0:3],v[156:159]\n"
"s_waitcnt lgkmcnt(0)\n"
"s_barrier\n"
"v_mfma_f32_16x16x32_bf16 v[128:131],v[64:67],a[96:99],v[128:131]\n"
"ds_read_b64_tr_b16 v[48:49],v98 offset:0\n"
"ds_read_b64_tr_b16 v[56:57],v99 offset:0\n"
"v_mfma_f32_16x16x32_bf16 v[128:131],v[68:71],a[100:103],v[128:131]\n"
"ds_read_b64_tr_b16 v[50:51],v98 offset:1024\n"
"ds_read_b64_tr_b16 v[58:59],v99 offset:1024\n"
"v_mfma_f32_16x16x32_bf16 v[160:163],v[68:71],a[108:111],v[160:163]\n"
"ds_read_b64_tr_b16 v[52:53],v98 offset:8192\n"
"ds_read_b64_tr_b16 v[60:61],v99 offset:8192\n"
"v_mfma_f32_16x16x32_bf16 v[160:163],v[64:67],a[104:107],v[160:163]\n"
"s_add_u32 m0,s78,0\n"
"ds_read_b64_tr_b16 v[54:55],v98 offset:9216\n"
"ds_read_b64_tr_b16 v[62:63],v99 offset:9216\n"
"v_mfma_f32_16x16x32_bf16 v[192:195],v[64:67],a[112:115],v[192:195]\n"
"buffer_load_dwordx4 v112,s[24:27],s76 offen offset:0 sc1 lds\n"
"v_mfma_f32_16x16x32_bf16 v[192:195],v[68:71],a[116:119],v[192:195]\n"
"v_mfma_f32_16x16x32_bf16 v[224:227],v[68:71],a[124:127],v[224:227]\n"
"v_mfma_f32_16x16x32_bf16 v[224:227],v[64:67],a[120:123],v[224:227]\n"
"v_mfma_f32_16x16x32_bf16 v[228:231],v[72:75],a[120:123],v[228:231]\n"
"ds_read_b64_tr_b16 v[64:65],v100 offset:0\n"
"v_mfma_f32_16x16x32_bf16 v[228:231],v[76:79],a[124:127],v[228:231]\n"
"ds_read_b64_tr_b16 v[66:67],v100 offset:1024\n"
"v_mfma_f32_16x16x32_bf16 v[196:199],v[76:79],a[116:119],v[196:199]\n"
"ds_read_b64_tr_b16 v[68:69],v100 offset:8192\n"
"v_mfma_f32_16x16x32_bf16 v[196:199],v[72:75],a[112:115],v[196:199]\n"
"ds_read_b64_tr_b16 v[70:71],v100 offset:9216\n"
"v_mfma_f32_16x16x32_bf16 v[164:167],v[72:75],a[104:107],v[164:167]\n"
"buffer_load_dwordx4 v112,s[24:27],s76 offen offset:1024 sc1 lds\n"
"v_mfma_f32_16x16x32_bf16 v[164:167],v[76:79],a[108:111],v[164:167]\n"
"v_mfma_f32_16x16x32_bf16 v[132:135],v[76:79],a[100:103],v[132:135]\n"
"buffer_load_dwordx4 v112,s[24:27],s76 offen offset:2048 sc1 lds\n"
"v_mfma_f32_16x16x32_bf16 v[132:135],v[72:75],a[96:99],v[132:135]\n"
"v_mfma_f32_16x16x32_bf16 v[136:139],v[80:83],a[96:99],v[136:139]\n"
"ds_read_b64_tr_b16 v[72:73],v101 offset:0\n"
"v_mfma_f32_16x16x32_bf16 v[136:139],v[84:87],a[100:103],v[136:139]\n"
"ds_read_b64_tr_b16 v[74:75],v101 offset:1024\n"
"v_mfma_f32_16x16x32_bf16 v[168:171],v[84:87],a[108:111],v[168:171]\n"
"ds_read_b64_tr_b16 v[76:77],v101 offset:8192\n"
"v_mfma_f32_16x16x32_bf16 v[168:171],v[80:83],a[104:107],v[168:171]\n"
"ds_read_b64_tr_b16 v[78:79],v101 offset:9216\n"
"v_mfma_f32_16x16x32_bf16 v[200:203],v[80:83],a[112:115],v[200:203]\n"
"buffer_load_dwordx4 v112,s[24:27],s76 offen offset:3072 sc1 lds\n"
"v_mfma_f32_16x16x32_bf16 v[200:203],v[84:87],a[116:119],v[200:203]\n"
"s_xor_b32 s76,s76,16384\n"
"v_mfma_f32_16x16x32_bf16 v[232:235],v[84:87],a[124:127],v[232:235]\n"
"v_mfma_f32_16x16x32_bf16 v[232:235],v[80:83],a[120:123],v[232:235]\n"
"v_mfma_f32_16x16x32_bf16 v[236:239],v[88:91],a[120:123],v[236:239]\n"
"ds_read_b64_tr_b16 v[80:81],v102 offset:0\n"
"v_mfma_f32_16x16x32_bf16 v[236:239],v[92:95],a[124:127],v[236:239]\n"
"ds_read_b64_tr_b16 v[82:83],v102 offset:1024\n"
"v_mfma_f32_16x16x32_bf16 v[204:207],v[92:95],a[116:119],v[204:207]\n"
"ds_read_b64_tr_b16 v[84:85],v102 offset:8192\n"
"v_mfma_f32_16x16x32_bf16 v[204:207],v[88:91],a[112:115],v[204:207]\n"
"s_add_u32 m0,s78,16384\n"
"ds_read_b64_tr_b16 v[86:87],v102 offset:9216\n"
"v_mfma_f32_16x16x32_bf16 v[172:175],v[88:91],a[104:107],v[172:175]\n"
"buffer_load_dwordx4 v112,s[24:27],s76 offen offset:0 sc1 lds\n"
"v_mfma_f32_16x16x32_bf16 v[172:175],v[92:95],a[108:111],v[172:175]\n"
"v_mfma_f32_16x16x32_bf16 v[140:143],v[92:95],a[100:103],v[140:143]\n"
"buffer_load_dwordx4 v112,s[24:27],s76 offen offset:1024 sc1 lds\n"
"v_mfma_f32_16x16x32_bf16 v[140:143],v[88:91],a[96:99],v[140:143]\n"
".Lfk_q27:\n"
"s_waitcnt vmcnt(19)\n"
"v_mfma_f32_16x16x32_bf16 v[144:147],a[128:131],a[96:99],v[144:147]\n"
"ds_read_b64_tr_b16 v[88:89],v103 offset:0\n"
"v_mfma_f32_16x16x32_bf16 v[144:147],a[132:135],a[100:103],v[144:147]\n"
"ds_read_b64_tr_b16 v[90:91],v103 offset:1024\n"
"v_mfma_f32_16x16x32_bf16 v[176:179],a[132:135],a[108:111],v[176:179]\n"
"ds_read_b64_tr_b16 v[92:93],v103 offset:8192\n"
"v_mfma_f32_16x16x32_bf16 v[176:179],a[128:131],a[104:107],v[176:179]\n"
"ds_read_b64_tr_b16 v[94:95],v103 offset:9216\n"
"v_mfma_f32_16x16x32_bf16 v[208:211],a[128:131],a[112:115],v[208:211]\n"
"buffer_load_dwordx4 v112,s[24:27],s76 offen offset:2048 sc1 lds\n"
"v_mfma_f32_16x16x32_bf16 v[208:211],a[132:135],a[116:119],v[208:211]\n"
"ds_read_b128 v[0:3],v96 offset:32768\n"
"v_mfma_f32_16x16x32_bf16 v[240:243],a[132:135],a[124:127],v[240:243]\n"
"v_mfma_f32_16x16x32_bf16 v[240:243],a[128:131],a[120:123],v[240:243]\n"
"ds_read_b128 v[8:11],v96 offset:33792\n"
"v_mfma_f32_16x16x32_bf16 v[244:247],a[136:139],a[120:123],v[244:247]\n"
"ds_read_b64_tr_b16 a[128:129],v104 offset:0\n"
"v_mfma_f32_16x16x32_bf16 v[244:247],a[140:143],a[124:127],v[244:247]\n"
"ds_read_b64_tr_b16 a[130:131],v104 offset:1024\n"
"v_mfma_f32_16x16x32_bf16 v[212:215],a[140:143],a[116:119],v[212:215]\n"
"ds_read_b64_tr_b16 a[132:133],v104 offset:8192\n"
"v_mfma_f32_16x16x32_bf16 v[212:215],a[136:139],a[112:115],v[212:215]\n"
"ds_read_b64_tr_b16 a[134:135],v104 offset:9216\n"
"v_mfma_f32_16x16x32_bf16 v[180:183],a[136:139],a[104:107],v[180:183]\n"
"buffer_load_dwordx4 v112,s[24:27],s76 offen offset:3072 sc1 lds\n"
"v_mfma_f32_16x16x32_bf16 v[180:183],a[140:143],a[108:111],v[180:183]\n"
"s_add_u32 m0,s85,122880\n"
"s_add_u32 s77,s73,0\n"
"ds_read_b128 v[16:19],v96 offset:34816\n"
"v_mfma_f32_16x16x32_bf16 v[148:151],a[140:143],a[100:103],v[148:151]\n"
"buffer_load_dwordx4 v112,s[24:27],s77 offen offset:0 sc1 lds\n"
"v_mfma_f32_16x16x32_bf16 v[148:151],a[136:139],a[96:99],v[148:151]\n"
"s_xor_b32 s76,s76,1032192\n"
"ds_read_b128 v[24:27],v96 offset:35840\n"
"v_mfma_f32_16x16x32_bf16 v[152:155],v[32:35],a[96:99],v[152:155]\n"
"ds_read_b64_tr_b16 a[136:137],v105 offset:0\n"
"v_mfma_f32_16x16x32_bf16 v[152:155],v[36:39],a[100:103],v[152:155]\n"
"ds_read_b64_tr_b16 a[138:139],v105 offset:1024\n"
"v_mfma_f32_16x16x32_bf16 v[184:187],v[36:39],a[108:111],v[184:187]\n"
"ds_read_b64_tr_b16 a[140:141],v105 offset:8192\n"
"v_mfma_f32_16x16x32_bf16 v[184:187],v[32:35],a[104:107],v[184:187]\n"
"ds_read_b64_tr_b16 a[142:143],v105 offset:9216\n"
"v_mfma_f32_16x16x32_bf16 v[216:219],v[32:35],a[112:115],v[216:219]\n"
"ds_read_b128 v[4:7],v96 offset:49152\n"
"v_mfma_f32_16x16x32_bf16 v[216:219],v[36:39],a[116:119],v[216:219]\n"
"ds_read_b128 v[12:15],v96 offset:50176\n"
"v_mfma_f32_16x16x32_bf16 v[248:251],v[36:39],a[124:127],v[248:251]\n"
"ds_read_b128 v[20:23],v96 offset:51200\n"
"v_mfma_f32_16x16x32_bf16 v[248:251],v[32:35],a[120:123],v[248:251]\n"
"ds_read_b128 v[28:31],v96 offset:52224\n"
"v_mfma_f32_16x16x32_bf16 v[252:255],v[40:43],a[120:123],v[252:255]\n"
"buffer_load_dwordx4 v112,s[24:27],s77 offen offset:1024 sc1 lds\n"
"v_mfma_f32_16x16x32_bf16 v[252:255],v[44:47],a[124:127],v[252:255]\n"
"v_mfma_f32_16x16x32_bf16 v[220:223],v[44:47],a[116:119],v[220:223]\n"
"s_add_u32 m0,s85,114688\n"
"s_add_u32 s77,s73,32768\n"
"v_mfma_f32_16x16x32_bf16 v[220:223],v[40:43],a[112:115],v[220:223]\n"
"buffer_load_dwordx4 v112,s[24:27],s77 offen offset:0 sc1 lds\n"
"v_mfma_f32_16x16x32_bf16 v[188:191],v[40:43],a[104:107],v[188:191]\n"
"v_mfma_f32_16x16x32_bf16 v[188:191],v[44:47],a[108:111],v[188:191]\n"
"v_mfma_f32_16x16x32_bf16 v[156:159],v[44:47],a[100:103],v[156:159]\n"
"buffer_load_dwordx4 v112,s[24:27],s77 offen offset:1024 sc1 lds\n"
"v_mfma_f32_16x16x32_bf16 v[156:159],v[40:43],a[96:99],v[156:159]\n"
"s_waitcnt vmcnt(21) lgkmcnt(0)\n"
"s_barrier\n"
"v_mfma_f32_16x16x32_bf16 v[128:131],v[48:51],v[0:3],v[128:131]\n"
"ds_read_b64_tr_b16 v[32:33],v98 offset:49152\n"
"ds_read_b64_tr_b16 v[40:41],v99 offset:49152\n"
"v_mfma_f32_16x16x32_bf16 v[128:131],v[52:55],v[4:7],v[128:131]\n"
"ds_read_b64_tr_b16 v[34:35],v98 offset:50176\n"
"ds_read_b64_tr_b16 v[42:43],v99 offset:50176\n"
"v_mfma_f32_16x16x32_bf16 v[160:163],v[52:55],v[12:15],v[160:163]\n"
"s_add_u32 m0,s85,98304\n"
"ds_read_b64_tr_b16 v[36:37],v98 offset:57344\n"
"ds_read_b64_tr_b16 v[44:45],v99 offset:57344\n"
"v_mfma_f32_16x16x32_bf16 v[160:163],v[48:51],v[8:11],v[160:163]\n"
"s_add_u32 s77,s73,49152\n"
"ds_read_b64_tr_b16 v[38:39],v98 offset:58368\n"
"ds_read_b64_tr_b16 v[46:47],v99 offset:58368\n"
"v_mfma_f32_16x16x32_bf16 v[192:195],v[48:51],v[16:19],v[192:195]\n"
"buffer_load_dwordx4 v112,s[24:27],s77 offen offset:0 sc1 lds\n"
"v_mfma_f32_16x16x32_bf16 v[192:195],v[52:55],v[20:23],v[192:195]\n"
"v_mfma_f32_16x16x32_bf16 v[224:227],v[52:55],v[28:31],v[224:227]\n"
"buffer_load_dwordx4 v112,s[24:27],s77 offen offset:1024 sc1 lds\n"
"v_mfma_f32_16x16x32_bf16 v[224:227],v[48:51],v[24:27],v[224:227]\n"
"v_mfma_f32_16x16x32_bf16 v[228:231],v[56:59],v[24:27],v[228:231]\n"
"ds_read_b64_tr_b16 v[48:49],v100 offset:49152\n"
"v_mfma_f32_16x16x32_bf16 v[228:231],v[60:63],v[28:31],v[228:231]\n"
"ds_read_b64_tr_b16 v[50:51],v100 offset:50176\n"
"v_mfma_f32_16x16x32_bf16 v[196:199],v[60:63],v[20:23],v[196:199]\n"
"ds_read_b64_tr_b16 v[52:53],v100 offset:57344\n"
"v_mfma_f32_16x16x32_bf16 v[196:199],v[56:59],v[16:19],v[196:199]\n"
"s_add_u32 m0,s85,106496\n"
"s_add_u32 s77,s73,8192\n"
"ds_read_b64_tr_b16 v[54:55],v100 offset:58368\n"
"v_mfma_f32_16x16x32_bf16 v[164:167],v[56:59],v[8:11],v[164:167]\n"
"buffer_load_dwordx4 v112,s[24:27],s77 offen offset:0 sc1 lds\n"
"v_mfma_f32_16x16x32_bf16 v[164:167],v[60:63],v[12:15],v[164:167]\n"
"v_mfma_f32_16x16x32_bf16 v[132:135],v[60:63],v[4:7],v[132:135]\n"
"buffer_load_dwordx4 v112,s[24:27],s77 offen offset:1024 sc1 lds\n"
"v_mfma_f32_16x16x32_bf16 v[132:135],v[56:59],v[0:3],v[132:135]\n"
"v_mfma_f32_16x16x32_bf16 v[136:139],v[64:67],v[0:3],v[136:139]\n"
"ds_read_b64_tr_b16 v[56:57],v101 offset:49152\n"
"v_mfma_f32_16x16x32_bf16 v[136:139],v[68:71],v[4:7],v[136:139]\n"
"ds_read_b64_tr_b16 v[58:59],v101 offset:50176\n"
"v_mfma_f32_16x16x32_bf16 v[168:171],v[68:71],v[12:15],v[168:171]\n"
"ds_read_b64_tr_b16 v[60:61],v101 offset:57344\n"
"ds_read_b64_tr_b16 v[62:63],v101 offset:58368\n"
"v_mfma_f32_16x16x32_bf16 v[168:171],v[64:67],v[8:11],v[168:171]\n"
"s_add_u32 s48,s48,28672\n"
"s_addc_u32 s49,s49,0\n"
"v_mfma_f32_16x16x32_bf16 v[200:203],v[64:67],v[16:19],v[200:203]\n"
"buffer_load_dword a148,v122,s[48:51],0 offen\n"
"v_mfma_f32_16x16x32_bf16 v[200:203],v[68:71],v[20:23],v[200:203]\n"
"v_mfma_f32_16x16x32_bf16 v[232:235],v[68:71],v[28:31],v[232:235]\n"
"v_mfma_f32_16x16x32_bf16 v[232:235],v[64:67],v[24:27],v[232:235]\n"
"v_mfma_f32_16x16x32_bf16 v[236:239],v[72:75],v[24:27],v[236:239]\n"
"ds_read_b64_tr_b16 v[64:65],v102 offset:49152\n"
"v_mfma_f32_16x16x32_bf16 v[236:239],v[76:79],v[28:31],v[236:239]\n"
"ds_read_b64_tr_b16 v[66:67],v102 offset:50176\n"
"v_mfma_f32_16x16x32_bf16 v[204:207],v[76:79],v[20:23],v[204:207]\n"
"ds_read_b64_tr_b16 v[68:69],v102 offset:57344\n"
"v_mfma_f32_16x16x32_bf16 v[204:207],v[72:75],v[16:19],v[204:207]\n"
"ds_read_b64_tr_b16 v[70:71],v102 offset:58368\n"
"v_mfma_f32_16x16x32_bf16 v[172:175],v[72:75],v[8:11],v[172:175]\n"
"v_mfma_f32_16x16x32_bf16 v[172:175],v[76:79],v[12:15],v[172:175]\n"
"v_mfma_f32_16x16x32_bf16 v[140:143],v[76:79],v[4:7],v[140:143]\n"
"v_mfma_f32_16x16x32_bf16 v[140:143],v[72:75],v[0:3],v[140:143]\n"
"s_waitcnt vmcnt(9)\n"
"v_mfma_f32_16x16x32_bf16 v[144:147],v[80:83],v[0:3],v[144:147]\n"
"ds_read_b64_tr_b16 v[72:73],v103 offset:49152\n"
"s_mov_b32 s90,0\n"
"v_mfma_f32_16x16x32_bf16 v[144:147],v[84:87],v[4:7],v[144:147]\n"
"ds_read_b64_tr_b16 v[74:75],v103 offset:50176\n"
"s_mov_b32 s16,0xc21c0000\n"
"v_mfma_f32_16x16x32_bf16 v[176:179],v[84:87],v[12:15],v[176:179]\n"
"ds_read_b64_tr_b16 v[76:77],v103 offset:57344\n"
"s_mov_b32 s17,0x41a00000\n"
"v_mfma_f32_16x16x32_bf16 v[176:179],v[80:83],v[8:11],v[176:179]\n"
"ds_read_b64_tr_b16 v[78:79],v103 offset:58368\n"
"s_mov_b32 s89,0\n"
"v_mfma_f32_16x16x32_bf16 v[208:211],v[80:83],v[16:19],v[208:211]\n"
"s_xor_b32 s82,s78,8192\n"
"v_add_u32 v116,s82,v112\n"
"v_mfma_f32_16x16x32_bf16 v[208:211],v[84:87],v[20:23],v[208:211]\n"
"ds_read_b128 a[96:99],v96 offset:0\n"
"v_add_u32 v117,4096,v112\n"
"v_mfma_f32_16x16x32_bf16 v[240:243],v[84:87],v[28:31],v[240:243]\n"
"s_getpc_b64 s[20:21]\n"
".Lfk_evp:\n"
"s_add_u32 s20,s20,.Lfk_ev-.Lfk_evp\n"
"s_addc_u32 s21,s21,0\n"
"v_mfma_f32_16x16x32_bf16 v[240:243],v[80:83],v[24:27],v[240:243]\n"
"ds_read_b128 a[104:107],v96 offset:1024\n"
"v_mfma_f32_16x16x32_bf16 v[244:247],v[88:91],v[24:27],v[244:247]\n"
"ds_read_b64_tr_b16 v[80:81],v104 offset:49152\n"
"v_mfma_f32_16x16x32_bf16 v[244:247],v[92:95],v[28:31],v[244:247]\n"
"ds_read_b64_tr_b16 v[82:83],v104 offset:50176\n"
"v_mfma_f32_16x16x32_bf16 v[212:215],v[92:95],v[20:23],v[212:215]\n"
"ds_read_b64_tr_b16 v[84:85],v104 offset:57344\n"
"v_mfma_f32_16x16x32_bf16 v[212:215],v[88:91],v[16:19],v[212:215]\n"
"ds_read_b64_tr_b16 v[86:87],v104 offset:58368\n"
"v_mfma_f32_16x16x32_bf16 v[180:183],v[88:91],v[8:11],v[180:183]\n"
"buffer_store_dwordx4 a[156:159],v112,s[24:27],s74 offen offset:0 sc1\n"
"v_mfma_f32_16x16x32_bf16 v[180:183],v[92:95],v[12:15],v[180:183]\n"
"ds_read_b128 a[112:115],v96 offset:2048\n"
"v_mfma_f32_16x16x32_bf16 v[148:151],v[92:95],v[4:7],v[148:151]\n"
"v_mfma_f32_16x16x32_bf16 v[148:151],v[88:91],v[0:3],v[148:151]\n"
"ds_read_b128 a[120:123],v96 offset:3072\n"
"v_mfma_f32_16x16x32_bf16 v[152:155],a[128:131],v[0:3],v[152:155]\n"
"ds_read_b64_tr_b16 v[88:89],v105 offset:49152\n"
"v_mfma_f32_16x16x32_bf16 v[152:155],a[132:135],v[4:7],v[152:155]\n"
"ds_read_b64_tr_b16 v[90:91],v105 offset:50176\n"
"v_mfma_f32_16x16x32_bf16 v[184:187],a[132:135],v[12:15],v[184:187]\n"
"ds_read_b64_tr_b16 v[92:93],v105 offset:57344\n"
"v_mfma_f32_16x16x32_bf16 v[184:187],a[128:131],v[8:11],v[184:187]\n"
"ds_read_b64_tr_b16 v[94:95],v105 offset:58368\n"
"v_mfma_f32_16x16x32_bf16 v[216:219],a[128:131],v[16:19],v[216:219]\n"
"ds_read_b128 a[100:103],v96 offset:16384\n"
"v_mfma_f32_16x16x32_bf16 v[216:219],a[132:135],v[20:23],v[216:219]\n"
"ds_read_b128 a[108:111],v96 offset:17408\n"
"v_add_u32 v113,65536,v112\n"
"v_mfma_f32_16x16x32_bf16 v[248:251],a[132:135],v[28:31],v[248:251]\n"
"ds_read_b128 a[116:119],v96 offset:18432\n"
"v_add_u32 v114,65536,v112\n"
"v_mfma_f32_16x16x32_bf16 v[248:251],a[128:131],v[24:27],v[248:251]\n"
"ds_read_b128 a[124:127],v96 offset:19456\n"
"v_mfma_f32_16x16x32_bf16 v[252:255],a[136:139],v[24:27],v[252:255]\n"
"v_mfma_f32_16x16x32_bf16 v[252:255],a[140:143],v[28:31],v[252:255]\n"
"buffer_store_dwordx4 a[156:159],v112,s[24:27],s74 offen offset:1024 sc1\n"
"v_mfma_f32_16x16x32_bf16 v[220:223],a[140:143],v[20:23],v[220:223]\n"
"s_lshr_b32 s82,s74,1\n"
"s_add_u32 s82,s82,88080384\n"
"v_mfma_f32_16x16x32_bf16 v[220:223],a[136:139],v[16:19],v[220:223]\n"
"buffer_store_dwordx4 a[156:159],v112,s[24:27],s82 offen sc1\n"
"v_mfma_f32_16x16x32_bf16 v[188:191],a[136:139],v[8:11],v[188:191]\n"
"v_mfma_f32_16x16x32_bf16 v[188:191],a[140:143],v[12:15],v[188:191]\n"
"buffer_store_dword a156,off,s[24:27],s72 sc1\n"
"v_mfma_f32_16x16x32_bf16 v[156:159],a[140:143],v[4:7],v[156:159]\n"
"v_mfma_f32_16x16x32_bf16 v[156:159],a[136:139],v[0:3],v[156:159]\n"
"s_waitcnt vmcnt(11) lgkmcnt(0)\n"
"s_barrier\n"
"v_mfma_f32_16x16x32_bf16 v[224:227],v[32:35],a[120:123],v[224:227]\n"
"ds_read_b128 a[64:67],v113 offset:57344\n"
"v_mov_b32 v96,0xc1980000\n"
"v_mfma_f32_16x16x32_bf16 v[224:227],v[36:39],a[124:127],v[224:227]\n"
"ds_read_b128 a[68:71],v113 offset:58368\n"
"v_mov_b32 v97,0xc1980000\n"
"v_mfma_f32_16x16x32_bf16 v[228:231],v[44:47],a[124:127],v[228:231]\n"
"ds_read_b128 a[72:75],v113 offset:59392\n"
"v_mov_b32 v98,0xc1980000\n"
"v_mfma_f32_16x16x32_bf16 v[228:231],v[40:43],a[120:123],v[228:231]\n"
"ds_read_b128 a[76:79],v113 offset:60416\n"
"v_mov_b32 v99,0xc1980000\n"
"v_mfma_f32_16x16x32_bf16 v[232:235],v[48:51],a[120:123],v[232:235]\n"
"ds_read_b128 a[80:83],v113 offset:61440\n"
"v_mov_b32 v100,0xc1980000\n"
"v_mfma_f32_16x16x32_bf16 v[232:235],v[52:55],a[124:127],v[232:235]\n"
"ds_read_b128 a[84:87],v113 offset:62464\n"
"v_mov_b32 v101,0xc1980000\n"
"v_mfma_f32_16x16x32_bf16 v[236:239],v[60:63],a[124:127],v[236:239]\n"
"ds_read_b128 a[88:91],v113 offset:63488\n"
"v_mov_b32 v102,0xc1980000\n"
"v_mfma_f32_16x16x32_bf16 v[236:239],v[56:59],a[120:123],v[236:239]\n"
"ds_read_b128 a[92:95],v113 offset:64512\n"
"v_mov_b32 v103,0xc1980000\n"
"v_mfma_f32_16x16x32_bf16 v[240:243],v[64:67],a[120:123],v[240:243]\n"
"v_mov_b32 v104,0xc1980000\n"
"v_mov_b32 v105,0xc1980000\n"
"v_mfma_f32_16x16x32_bf16 v[240:243],v[68:71],a[124:127],v[240:243]\n"
"ds_write_b32 v124,v125\n"
"v_mfma_f32_16x16x32_bf16 v[244:247],v[76:79],a[124:127],v[244:247]\n"
"v_mov_b32 v107,0xc1980000\n"
"v_mov_b32 v108,0xc1980000\n"
"v_mfma_f32_16x16x32_bf16 v[244:247],v[72:75],a[120:123],v[244:247]\n"
"v_mov_b32 v109,0xc1980000\n"
"v_mov_b32 v110,0xc1980000\n"
"v_mfma_f32_16x16x32_bf16 v[248:251],v[80:83],a[120:123],v[248:251]\n"
"v_mov_b32 v106,0xc1980000\n"
"v_mov_b32 v111,0xc1980000\n"
"v_mfma_f32_16x16x32_bf16 v[248:251],v[84:87],a[124:127],v[248:251]\n"
"v_mfma_f32_16x16x32_bf16 v[252:255],v[92:95],a[124:127],v[252:255]\n"
"v_mfma_f32_16x16x32_bf16 v[252:255],v[88:91],a[120:123],v[252:255]\n"
"v_mfma_f32_16x16x32_bf16 v[128:131],v[32:35],a[96:99],v[128:131]\n"
"v_mfma_f32_16x16x32_bf16 v[128:131],v[36:39],a[100:103],v[128:131]\n"
"v_cvt_pk_bf16_f32 v232,v232,v233\n"
"v_cvt_pk_bf16_f32 v233,v234,v235\n"
"v_mfma_f32_16x16x32_bf16 v[132:135],v[44:47],a[100:103],v[132:135]\n"
"v_cvt_pk_bf16_f32 v224,v224,v225\n"
"v_cvt_pk_bf16_f32 v225,v226,v227\n"
"v_mfma_f32_16x16x32_bf16 v[132:135],v[40:43],a[96:99],v[132:135]\n"
"v_cvt_pk_bf16_f32 v226,v228,v229\n"
"v_cvt_pk_bf16_f32 v227,v230,v231\n"
"v_mfma_f32_16x16x32_bf16 v[136:139],v[48:51],a[96:99],v[136:139]\n"
"ds_write_b128 v116,v[224:227] offset:0\n"
"v_mfma_f32_16x16x32_bf16 v[136:139],v[52:55],a[100:103],v[136:139]\n"
"v_cvt_pk_bf16_f32 v234,v236,v237\n"
"v_cvt_pk_bf16_f32 v235,v238,v239\n"
"v_mfma_f32_16x16x32_bf16 v[140:143],v[60:63],a[100:103],v[140:143]\n"
"ds_write_b128 v116,v[232:235] offset:1024\n"
"v_mfma_f32_16x16x32_bf16 v[140:143],v[56:59],a[96:99],v[140:143]\n"
"v_cvt_pk_bf16_f32 v240,v240,v241\n"
"v_cvt_pk_bf16_f32 v241,v242,v243\n"
"v_mfma_f32_16x16x32_bf16 v[144:147],v[64:67],a[96:99],v[144:147]\n"
"v_cvt_pk_bf16_f32 v242,v244,v245\n"
"v_cvt_pk_bf16_f32 v243,v246,v247\n"
"v_mfma_f32_16x16x32_bf16 v[144:147],v[68:71],a[100:103],v[144:147]\n"
"ds_write_b128 v116,v[240:243] offset:2048\n"
"v_cvt_pk_bf16_f32 v248,v248,v249\n"
"v_mfma_f32_16x16x32_bf16 v[148:151],v[76:79],a[100:103],v[148:151]\n"
"v_cvt_pk_bf16_f32 v249,v250,v251\n"
"v_cvt_pk_bf16_f32 v250,v252,v253\n"
"v_cvt_pk_bf16_f32 v251,v254,v255\n"
"v_mfma_f32_16x16x32_bf16 v[148:151],v[72:75],a[96:99],v[148:151]\n"
"ds_write_b128 v116,v[248:251] offset:3072\n"
"v_mfma_f32_16x16x32_bf16 v[152:155],v[80:83],a[96:99],v[152:155]\n"
"ds_read_b128 a[48:51],v116 offset:0\n"
"ds_read_b128 a[52:55],v116 offset:1024\n"
"v_mfma_f32_16x16x32_bf16 v[152:155],v[84:87],a[100:103],v[152:155]\n"
"ds_read_b128 a[56:59],v116 offset:2048\n"
"ds_read_b128 a[60:63],v116 offset:3072\n"
"v_mfma_f32_16x16x32_bf16 v[156:159],v[92:95],a[100:103],v[156:159]\n"
"v_mfma_f32_16x16x32_bf16 v[156:159],v[88:91],a[96:99],v[156:159]\n"
"v_mfma_f32_16x16x32_bf16 v[160:163],v[32:35],a[104:107],v[160:163]\n"
"v_mfma_f32_16x16x32_bf16 v[160:163],v[36:39],a[108:111],v[160:163]\n"
"v_cvt_pk_bf16_f32 v136,v136,v137\n"
"v_cvt_pk_bf16_f32 v137,v138,v139\n"
"v_mfma_f32_16x16x32_bf16 v[164:167],v[44:47],a[108:111],v[164:167]\n"
"v_cvt_pk_bf16_f32 v128,v128,v129\n"
"v_cvt_pk_bf16_f32 v129,v130,v131\n"
"v_mfma_f32_16x16x32_bf16 v[164:167],v[40:43],a[104:107],v[164:167]\n"
"v_cvt_pk_bf16_f32 v130,v132,v133\n"
"v_cvt_pk_bf16_f32 v131,v134,v135\n"
"v_mfma_f32_16x16x32_bf16 v[168:171],v[48:51],a[104:107],v[168:171]\n"
"ds_write_b128 v116,v[128:131] offset:16384\n"
"v_mfma_f32_16x16x32_bf16 v[168:171],v[52:55],a[108:111],v[168:171]\n"
"v_cvt_pk_bf16_f32 v138,v140,v141\n"
"v_cvt_pk_bf16_f32 v139,v142,v143\n"
"v_mfma_f32_16x16x32_bf16 v[172:175],v[60:63],a[108:111],v[172:175]\n"
"ds_write_b128 v116,v[136:139] offset:17408\n"
"v_mfma_f32_16x16x32_bf16 v[172:175],v[56:59],a[104:107],v[172:175]\n"
"v_cvt_pk_bf16_f32 v144,v144,v145\n"
"v_cvt_pk_bf16_f32 v145,v146,v147\n"
"v_mfma_f32_16x16x32_bf16 v[176:179],v[64:67],a[104:107],v[176:179]\n"
"v_cvt_pk_bf16_f32 v146,v148,v149\n"
"v_cvt_pk_bf16_f32 v147,v150,v151\n"
"v_mfma_f32_16x16x32_bf16 v[176:179],v[68:71],a[108:111],v[176:179]\n"
"ds_write_b128 v116,v[144:147] offset:18432\n"
"v_cvt_pk_bf16_f32 v152,v152,v153\n"
"v_mfma_f32_16x16x32_bf16 v[180:183],v[76:79],a[108:111],v[180:183]\n"
"v_cvt_pk_bf16_f32 v153,v154,v155\n"
"v_cvt_pk_bf16_f32 v154,v156,v157\n"
"v_cvt_pk_bf16_f32 v155,v158,v159\n"
"v_mfma_f32_16x16x32_bf16 v[180:183],v[72:75],a[104:107],v[180:183]\n"
"ds_write_b128 v116,v[152:155] offset:19456\n"
"v_mfma_f32_16x16x32_bf16 v[184:187],v[80:83],a[104:107],v[184:187]\n"
"ds_read_b128 a[0:3],v116 offset:16384\n"
"ds_read_b128 a[4:7],v116 offset:17408\n"
"v_mfma_f32_16x16x32_bf16 v[184:187],v[84:87],a[108:111],v[184:187]\n"
"ds_read_b128 a[8:11],v116 offset:18432\n"
"ds_read_b128 a[12:15],v116 offset:19456\n"
"v_mfma_f32_16x16x32_bf16 v[188:191],v[92:95],a[108:111],v[188:191]\n"
"v_mfma_f32_16x16x32_bf16 v[188:191],v[88:91],a[104:107],v[188:191]\n"
"v_mfma_f32_16x16x32_bf16 v[192:195],v[32:35],a[112:115],v[192:195]\n"
"v_mfma_f32_16x16x32_bf16 v[192:195],v[36:39],a[116:119],v[192:195]\n"
"v_cvt_pk_bf16_f32 v168,v168,v169\n"
"v_cvt_pk_bf16_f32 v169,v170,v171\n"
"v_mfma_f32_16x16x32_bf16 v[196:199],v[44:47],a[116:119],v[196:199]\n"
"v_cvt_pk_bf16_f32 v160,v160,v161\n"
"v_cvt_pk_bf16_f32 v161,v162,v163\n"
"v_mfma_f32_16x16x32_bf16 v[196:199],v[40:43],a[112:115],v[196:199]\n"
"v_cvt_pk_bf16_f32 v162,v164,v165\n"
"v_cvt_pk_bf16_f32 v163,v166,v167\n"
"v_mfma_f32_16x16x32_bf16 v[200:203],v[48:51],a[112:115],v[200:203]\n"
"ds_write_b128 v116,v[160:163] offset:49152\n"
"v_mfma_f32_16x16x32_bf16 v[200:203],v[52:55],a[116:119],v[200:203]\n"
"v_cvt_pk_bf16_f32 v170,v172,v173\n"
"v_cvt_pk_bf16_f32 v171,v174,v175\n"
"v_mfma_f32_16x16x32_bf16 v[204:207],v[60:63],a[116:119],v[204:207]\n"
"ds_write_b128 v116,v[168:171] offset:50176\n"
"v_mfma_f32_16x16x32_bf16 v[204:207],v[56:59],a[112:115],v[204:207]\n"
"v_cvt_pk_bf16_f32 v176,v176,v177\n"
"v_cvt_pk_bf16_f32 v177,v178,v179\n"
"v_mfma_f32_16x16x32_bf16 v[208:211],v[64:67],a[112:115],v[208:211]\n"
"v_cvt_pk_bf16_f32 v178,v180,v181\n"
"v_cvt_pk_bf16_f32 v179,v182,v183\n"
"v_mfma_f32_16x16x32_bf16 v[208:211],v[68:71],a[116:119],v[208:211]\n"
"ds_write_b128 v116,v[176:179] offset:51200\n"
"v_cvt_pk_bf16_f32 v184,v184,v185\n"
"v_mfma_f32_16x16x32_bf16 v[212:215],v[76:79],a[116:119],v[212:215]\n"
"v_cvt_pk_bf16_f32 v185,v186,v187\n"
"v_cvt_pk_bf16_f32 v186,v188,v189\n"
"v_cvt_pk_bf16_f32 v187,v190,v191\n"
"v_mfma_f32_16x16x32_bf16 v[212:215],v[72:75],a[112:115],v[212:215]\n"
"ds_write_b128 v116,v[184:187] offset:52224\n"
"v_mfma_f32_16x16x32_bf16 v[216:219],v[80:83],a[112:115],v[216:219]\n"
"ds_read_b128 a[16:19],v116 offset:49152\n"
"ds_read_b128 a[20:23],v116 offset:50176\n"
"v_mfma_f32_16x16x32_bf16 v[216:219],v[84:87],a[116:119],v[216:219]\n"
"ds_read_b128 a[24:27],v116 offset:51200\n"
"ds_read_b128 a[28:31],v116 offset:52224\n"
"v_mfma_f32_16x16x32_bf16 v[220:223],v[92:95],a[116:119],v[220:223]\n"
"s_lshl2_add_u32 s82,s60,s52\n"
"s_lshr_b32 s82,s82,1\n"
"s_cmp_eq_u32 s82,1\n"
"v_mfma_f32_16x16x32_bf16 v[220:223],v[88:91],a[112:115],v[220:223]\n"
"s_cbranch_scc1 .Lfk_head0\n"
"s_waitcnt lgkmcnt(0)\n"
"s_barrier\n"
"v_mfma_f32_16x16x32_bf16 v[24:27],a[64:67],a[48:51],v[108:111]\n"
"s_andn2_b32 m0,s55,16384\n"
"s_add_u32 s54,s54,s22\n"
"v_cvt_pk_bf16_f32 v200,v200,v201\n"
"v_mfma_f32_16x16x32_bf16 v[24:27],a[68:71],a[52:55],v[24:27]\n"
"buffer_load_dwordx4 v115,s[32:35],s54 offen sc1 lds\n"
"v_mfma_f32_16x16x32_bf16 v[24:27],a[72:75],a[56:59],v[24:27]\n"
"v_cvt_pk_bf16_f32 v192,v192,v193\n"
"v_cvt_pk_bf16_f32 v193,v194,v195\n"
"v_mfma_f32_16x16x32_bf16 v[24:27],a[76:79],a[60:63],v[24:27]\n"
"v_cvt_pk_bf16_f32 v194,v196,v197\n"
"v_cvt_pk_bf16_f32 v195,v198,v199\n"
"v_mfma_f32_16x16x32_bf16 v[28:31],a[92:95],a[60:63],v[108:111]\n"
"ds_write_b128 v116,v[192:195] offset:32768\n"
"v_mfma_f32_16x16x32_bf16 v[28:31],a[88:91],a[56:59],v[28:31]\n"
"v_cvt_pk_bf16_f32 v201,v202,v203\n"
"v_cvt_pk_bf16_f32 v202,v204,v205\n"
"v_cvt_pk_bf16_f32 v203,v206,v207\n"
"v_mfma_f32_16x16x32_bf16 v[28:31],a[84:87],a[52:55],v[28:31]\n"
"ds_write_b128 v116,v[200:203] offset:33792\n"
"v_mfma_f32_16x16x32_bf16 v[28:31],a[80:83],a[48:51],v[28:31]\n"
"v_cvt_pk_bf16_f32 v208,v208,v209\n"
"v_cvt_pk_bf16_f32 v209,v210,v211\n"
"v_mfma_f32_16x16x32_bf16 v[4:7],a[80:83],a[0:3],v[96:99]\n"
"v_cvt_pk_bf16_f32 v210,v212,v213\n"
"v_cvt_pk_bf16_f32 v211,v214,v215\n"
"v_mfma_f32_16x16x32_bf16 v[4:7],a[84:87],a[4:7],v[4:7]\n"
"ds_read_b128 a[96:99],v113 offset:16384\n"
"ds_read_b128 a[100:103],v113 offset:17408\n"
"v_mfma_f32_16x16x32_bf16 v[4:7],a[88:91],a[8:11],v[4:7]\n"
"ds_read_b128 a[104:107],v113 offset:18432\n"
"ds_read_b128 a[108:111],v113 offset:19456\n"
"v_mfma_f32_16x16x32_bf16 v[4:7],a[92:95],a[12:15],v[4:7]\n"
"ds_read_b128 a[112:115],v113 offset:20480\n"
"ds_read_b128 a[116:119],v113 offset:21504\n"
"v_mfma_f32_16x16x32_bf16 v[0:3],a[76:79],a[12:15],v[96:99]\n"
"ds_read_b128 a[120:123],v113 offset:22528\n"
"ds_read_b128 a[124:127],v113 offset:23552\n"
"v_mfma_f32_16x16x32_bf16 v[0:3],a[72:75],a[8:11],v[0:3]\n"
"v_max3_f32 v118,v24,v25,v26\n"
"v_max3_f32 v118,v118,v27,v28\n"
"v_mfma_f32_16x16x32_bf16 v[0:3],a[68:71],a[4:7],v[0:3]\n"
"v_max3_f32 v118,v118,v29,v30\n"
"v_max_f32 v118,v118,v31\n"
"v_mov_b32 v120,v118\n"
"v_mfma_f32_16x16x32_bf16 v[0:3],a[64:67],a[0:3],v[0:3]\n"
"ds_write_b128 v116,v[208:211] offset:34816\n"
"v_mfma_f32_16x16x32_bf16 v[8:11],a[64:67],a[16:19],v[100:103]\n"
"v_cvt_pk_bf16_f32 v216,v216,v217\n"
"v_cvt_pk_bf16_f32 v217,v218,v219\n"
"v_mfma_f32_16x16x32_bf16 v[8:11],a[68:71],a[20:23],v[8:11]\n"
"v_cvt_pk_bf16_f32 v218,v220,v221\n"
"v_cvt_pk_bf16_f32 v219,v222,v223\n"
"v_mfma_f32_16x16x32_bf16 v[8:11],a[72:75],a[24:27],v[8:11]\n"
"ds_write_b128 v116,v[216:219] offset:35840\n"
"ds_read_b128 a[32:35],v116 offset:32768\n"
"v_mfma_f32_16x16x32_bf16 v[8:11],a[76:79],a[28:31],v[8:11]\n"
"v_max3_f32 v119,v0,v1,v2\n"
"v_max3_f32 v119,v119,v3,v4\n"
"v_mfma_f32_16x16x32_bf16 v[12:15],a[92:95],a[28:31],v[100:103]\n"
"v_max3_f32 v119,v119,v5,v6\n"
"v_max_f32 v119,v119,v7\n"
"v_mfma_f32_16x16x32_bf16 v[12:15],a[88:91],a[24:27],v[12:15]\n"
"v_min_f32 v120,v120,v119\n"
"v_max_f32 v118,v118,v119\n"
"v_mfma_f32_16x16x32_bf16 v[12:15],a[84:87],a[20:23],v[12:15]\n"
"ds_read_b128 a[36:39],v116 offset:33792\n"
"ds_read_b128 a[40:43],v116 offset:34816\n"
"v_mfma_f32_16x16x32_bf16 v[12:15],a[80:83],a[16:19],v[12:15]\n"
"ds_read_b128 a[44:47],v116 offset:35840\n"
"s_waitcnt lgkmcnt(0)\n"
"v_mfma_f32_16x16x32_bf16 v[20:23],a[80:83],a[32:35],v[104:107]\n"
"v_mfma_f32_16x16x32_bf16 v[20:23],a[84:87],a[36:39],v[20:23]\n"
"v_mfma_f32_16x16x32_bf16 v[20:23],a[88:91],a[40:43],v[20:23]\n"
"buffer_store_dwordx4 a[224:227],v112,s[24:27],s56 offen offset:0 sc1\n"
"v_mfma_f32_16x16x32_bf16 v[20:23],a[92:95],a[44:47],v[20:23]\n"
"v_max3_f32 v119,v8,v9,v10\n"
"v_mfma_f32_16x16x32_bf16 v[16:19],a[76:79],a[44:47],v[104:107]\n"
"v_max3_f32 v119,v119,v11,v12\n"
"v_max3_f32 v119,v119,v13,v14\n"
"v_mfma_f32_16x16x32_bf16 v[16:19],a[72:75],a[40:43],v[16:19]\n"
"v_max_f32 v119,v119,v15\n"
"v_min_f32 v120,v120,v119\n"
"v_mfma_f32_16x16x32_bf16 v[16:19],a[68:71],a[36:39],v[16:19]\n"
"v_max_f32 v118,v118,v119\n"
"v_mfma_f32_16x16x32_bf16 v[16:19],a[64:67],a[32:35],v[16:19]\n"
"s_nop 7\n"
"v_max3_f32 v119,v16,v17,v18\n"
"v_max3_f32 v119,v119,v19,v20\n"
"v_max3_f32 v119,v119,v21,v22\n"
"v_max_f32 v119,v119,v23\n"
"v_min_f32 v120,v120,v119\n"
"v_max_f32 v118,v118,v119\n"
"v_cmp_gt_f32 s[96:97],s16,v120\n"
"v_cmp_lt_f32 vcc,s93,v118\n"
"s_nop 0\n"
"s_or_b64 s[96:97],s[96:97],vcc\n"
"s_cbranch_scc1 .Lfk_cs_h3\n"
".Lfk_cr_h3:\n"
"s_cmp_ge_u32 s52,2\n"
"s_cbranch_scc1 .Lfk_g0\n"
"s_waitcnt vmcnt(11) lgkmcnt(0)\n"
"s_barrier\n"
"v_mfma_f32_16x16x32_bf16 v[32:35],a[96:99],a[0:3],v[96:99]\n"
"s_add_u32 m0,s55,4096\n"
"v_exp_f32 v0,v0\n"
"v_mfma_f32_16x16x32_bf16 v[32:35],a[100:103],a[4:7],v[32:35]\n"
"s_add_u32 s54,s54,s22\n"
"v_exp_f32 v1,v1\n"
"v_mfma_f32_16x16x32_bf16 v[32:35],a[104:107],a[8:11],v[32:35]\n"
"buffer_load_dwordx4 v115,s[32:35],s54 offen sc1 lds\n"
"v_exp_f32 v2,v2\n"
"v_cvt_pk_bf16_f32 v64,v0,v1\n"
"v_mfma_f32_16x16x32_bf16 v[32:35],a[108:111],a[12:15],v[32:35]\n"
"s_add_u32 m0,s67,16384\n"
"v_exp_f32 v3,v3\n"
"v_mfma_f32_16x16x32_bf16 v[36:39],a[124:127],a[12:15],v[96:99]\n"
"v_exp_f32 v4,v4\n"
"v_cvt_pk_bf16_f32 v65,v2,v3\n"
"v_exp_f32 v5,v5\n"
"v_mfma_f32_16x16x32_bf16 v[36:39],a[120:123],a[8:11],v[36:39]\n"
"buffer_load_dwordx4 v112,s[24:27],s64 offen offset:0 sc1 lds\n"
"v_mfma_f32_16x16x32_bf16 v[36:39],a[116:119],a[4:7],v[36:39]\n"
"v_exp_f32 v6,v6\n"
"v_cvt_pk_bf16_f32 v66,v4,v5\n"
"v_exp_f32 v7,v7\n"
"v_mfma_f32_16x16x32_bf16 v[36:39],a[112:115],a[0:3],v[36:39]\n"
"buffer_load_dwordx4 v112,s[24:27],s64 offen offset:1024 sc1 lds\n"
"v_mfma_f32_16x16x32_bf16 v[44:47],a[112:115],a[16:19],v[100:103]\n"
"v_exp_f32 v8,v8\n"
"v_cvt_pk_bf16_f32 v67,v6,v7\n"
"v_exp_f32 v9,v9\n"
"v_mfma_f32_16x16x32_bf16 v[44:47],a[116:119],a[20:23],v[44:47]\n"
"ds_read_b128 a[64:67],v113 offset:49152\n"
"ds_read_b128 a[68:71],v113 offset:50176\n"
"v_exp_f32 v10,v10\n"
"v_mfma_f32_16x16x32_bf16 v[44:47],a[120:123],a[24:27],v[44:47]\n"
"ds_read_b128 a[72:75],v113 offset:51200\n"
"ds_read_b128 a[76:79],v113 offset:52224\n"
"v_exp_f32 v11,v11\n"
"v_mfma_f32_16x16x32_bf16 v[44:47],a[124:127],a[28:31],v[44:47]\n"
"ds_read_b128 a[80:83],v113 offset:53248\n"
"ds_read_b128 a[84:87],v113 offset:54272\n"
"v_exp_f32 v12,v12\n"
"v_mfma_f32_16x16x32_bf16 v[40:43],a[108:111],a[28:31],v[100:103]\n"
"ds_read_b128 a[88:91],v113 offset:55296\n"
"ds_read_b128 a[92:95],v113 offset:56320\n"
"v_exp_f32 v13,v13\n"
"v_mfma_f32_16x16x32_bf16 v[40:43],a[104:107],a[24:27],v[40:43]\n"
"buffer_load_dwordx4 v112,s[24:27],s64 offen offset:2048 sc1 lds\n"
"v_exp_f32 v14,v14\n"
"v_mfma_f32_16x16x32_bf16 v[40:43],a[100:103],a[20:23],v[40:43]\n"
"buffer_load_dwordx4 v112,s[24:27],s64 offen offset:3072 sc1 lds\n"
"v_mfma_f32_16x16x32_bf16 v[40:43],a[96:99],a[16:19],v[40:43]\n"
"s_add_u32 m0,s67,20480\n"
"v_exp_f32 v15,v15\n"
"v_mfma_f32_16x16x32_bf16 v[48:51],a[96:99],a[32:35],v[104:107]\n"
"buffer_load_dwordx4 v117,s[24:27],s64 offen offset:0 sc1 lds\n"
"buffer_load_dwordx4 v117,s[24:27],s64 offen offset:1024 sc1 lds\n"
"v_exp_f32 v16,v16\n"
"v_exp_f32 v17,v17\n"
"v_mfma_f32_16x16x32_bf16 v[48:51],a[100:103],a[36:39],v[48:51]\n"
"buffer_load_dwordx4 v117,s[24:27],s64 offen offset:2048 sc1 lds\n"
"v_mfma_f32_16x16x32_bf16 v[48:51],a[104:107],a[40:43],v[48:51]\n"
"v_exp_f32 v18,v18\n"
"v_cvt_pk_bf16_f32 v68,v8,v9\n"
"v_cvt_pk_bf16_f32 v69,v10,v11\n"
"v_exp_f32 v19,v19\n"
"v_mfma_f32_16x16x32_bf16 v[48:51],a[108:111],a[44:47],v[48:51]\n"
"buffer_load_dwordx4 v117,s[24:27],s64 offen offset:3072 sc1 lds\n"
"v_mfma_f32_16x16x32_bf16 v[52:55],a[124:127],a[44:47],v[104:107]\n"
"s_add_u32 s64,s64,16384\n"
"v_exp_f32 v20,v20\n"
"v_cvt_pk_bf16_f32 v70,v12,v13\n"
"v_cvt_pk_bf16_f32 v71,v14,v15\n"
"v_mfma_f32_16x16x32_bf16 v[52:55],a[120:123],a[40:43],v[52:55]\n"
"v_exp_f32 v21,v21\n"
"v_cvt_pk_bf16_f32 v72,v16,v17\n"
"v_cvt_pk_bf16_f32 v73,v18,v19\n"
"v_mfma_f32_16x16x32_bf16 v[52:55],a[116:119],a[36:39],v[52:55]\n"
"v_exp_f32 v22,v22\n"
"v_cvt_pk_bf16_f32 v74,v20,v21\n"
"v_mfma_f32_16x16x32_bf16 v[52:55],a[112:115],a[32:35],v[52:55]\n"
"v_exp_f32 v23,v23\n"
"v_mfma_f32_16x16x32_bf16 v[60:63],a[112:115],a[48:51],v[108:111]\n"
"v_exp_f32 v24,v24\n"
"v_cvt_pk_bf16_f32 v75,v22,v23\n"
"v_mfma_f32_16x16x32_bf16 v[60:63],a[116:119],a[52:55],v[60:63]\n"
"v_exp_f32 v25,v25\n"
"v_mfma_f32_16x16x32_bf16 v[60:63],a[120:123],a[56:59],v[60:63]\n"
"v_exp_f32 v26,v26\n"
"v_exp_f32 v27,v27\n"
"v_mfma_f32_16x16x32_bf16 v[60:63],a[124:127],a[60:63],v[60:63]\n"
"buffer_store_dwordx4 a[228:231],v112,s[24:27],s56 offen offset:1024 sc1\n"
"v_mfma_f32_16x16x32_bf16 v[56:59],a[108:111],a[60:63],v[108:111]\n"
"v_exp_f32 v28,v28\n"
"v_cvt_pk_bf16_f32 v77,v26,v27\n"
"v_mfma_f32_16x16x32_bf16 v[56:59],a[104:107],a[56:59],v[56:59]\n"
"v_exp_f32 v29,v29\n"
"v_mfma_f32_16x16x32_bf16 v[56:59],a[100:103],a[52:55],v[56:59]\n"
"v_exp_f32 v30,v30\n"
"v_cvt_pk_bf16_f32 v78,v28,v29\n"
"v_mfma_f32_16x16x32_bf16 v[56:59],a[96:99],a[48:51],v[56:59]\n"
"s_waitcnt vmcnt(17) lgkmcnt(0)\n"
"v_exp_f32 v31,v31\n"
"v_cvt_pk_bf16_f32 v76,v24,v25\n"
"v_cvt_pk_bf16_f32 v79,v30,v31\n"
"s_barrier\n"
"v_mfma_f32_16x16x32_bf16 v[0:3],a[64:67],a[0:3],v[96:99]\n"
"s_add_u32 m0,s55,8192\n"
"v_exp_f32 v122,v32\n"
"v_mfma_f32_16x16x32_bf16 v[0:3],a[68:71],a[4:7],v[0:3]\n"
"s_add_u32 s54,s54,s22\n"
"v_exp_f32 v123,v33\n"
"v_mfma_f32_16x16x32_bf16 v[0:3],a[72:75],a[8:11],v[0:3]\n"
"ds_read_b128 a[96:99],v114 offset:40960\n"
"ds_read_b128 a[100:103],v114 offset:41984\n"
"v_mfma_f32_16x16x32_bf16 v[0:3],a[76:79],a[12:15],v[0:3]\n"
"v_exp_f32 v119,v34\n"
"v_mfma_f32_16x16x32_bf16 v[4:7],a[92:95],a[12:15],v[96:99]\n"
"ds_read_b128 a[104:107],v114 offset:43008\n"
"v_cvt_pk_bf16_f32 v80,v122,v123\n"
"v_mfma_f32_16x16x32_bf16 v[4:7],a[88:91],a[8:11],v[4:7]\n"
"v_exp_f32 v120,v35\n"
"v_mfma_f32_16x16x32_bf16 v[4:7],a[84:87],a[4:7],v[4:7]\n"
"ds_read_b128 a[108:111],v114 offset:44032\n"
"ds_read_b128 a[112:115],v114 offset:45056\n"
"v_mfma_f32_16x16x32_bf16 v[4:7],a[80:83],a[0:3],v[4:7]\n"
"v_exp_f32 v122,v36\n"
"v_mfma_f32_16x16x32_bf16 v[12:15],a[80:83],a[16:19],v[100:103]\n"
"ds_read_b128 a[116:119],v114 offset:46080\n"
"v_cvt_pk_bf16_f32 v81,v119,v120\n"
"v_mfma_f32_16x16x32_bf16 v[12:15],a[84:87],a[20:23],v[12:15]\n"
"v_exp_f32 v123,v37\n"
"v_mfma_f32_16x16x32_bf16 v[12:15],a[88:91],a[24:27],v[12:15]\n"
"ds_read_b128 a[120:123],v114 offset:47104\n"
"ds_read_b128 a[124:127],v114 offset:48128\n"
"v_mfma_f32_16x16x32_bf16 v[12:15],a[92:95],a[28:31],v[12:15]\n"
"v_exp_f32 v119,v38\n"
"v_cvt_pk_bf16_f32 v82,v122,v123\n"
"v_mfma_f32_16x16x32_bf16 v[8:11],a[76:79],a[28:31],v[100:103]\n"
"buffer_store_dwordx4 a[168:171],v112,s[24:27],s3 offen offset:2048 sc1\n"
"v_mfma_f32_16x16x32_bf16 v[8:11],a[72:75],a[24:27],v[8:11]\n"
"v_exp_f32 v120,v39\n"
"v_mfma_f32_16x16x32_bf16 v[8:11],a[68:71],a[20:23],v[8:11]\n"
"buffer_load_dwordx4 v115,s[32:35],s54 offen sc1 lds\n"
"v_mfma_f32_16x16x32_bf16 v[8:11],a[64:67],a[16:19],v[8:11]\n"
"s_add_u32 m0,s67,49152\n"
"v_exp_f32 v122,v40\n"
"v_mfma_f32_16x16x32_bf16 v[16:19],a[64:67],a[32:35],v[104:107]\n"
"v_cvt_pk_bf16_f32 v83,v119,v120\n"
"v_or3_b32 v118,v80,v81,v82\n"
"v_mfma_f32_16x16x32_bf16 v[16:19],a[68:71],a[36:39],v[16:19]\n"
"v_exp_f32 v123,v41\n"
"v_mfma_f32_16x16x32_bf16 v[16:19],a[72:75],a[40:43],v[16:19]\n"
"buffer_load_dwordx4 v112,s[24:27],s64 offen offset:0 sc1 lds\n"
"v_mfma_f32_16x16x32_bf16 v[16:19],a[76:79],a[44:47],v[16:19]\n"
"v_exp_f32 v119,v42\n"
"v_mfma_f32_16x16x32_bf16 v[20:23],a[92:95],a[44:47],v[104:107]\n"
"v_cvt_pk_bf16_f32 v84,v122,v123\n"
"v_or3_b32 v118,v118,v83,v84\n"
"v_mfma_f32_16x16x32_bf16 v[20:23],a[88:91],a[40:43],v[20:23]\n"
"v_exp_f32 v120,v43\n"
"v_mfma_f32_16x16x32_bf16 v[20:23],a[84:87],a[36:39],v[20:23]\n"
"buffer_load_dwordx4 v112,s[24:27],s64 offen offset:1024 sc1 lds\n"
"v_mfma_f32_16x16x32_bf16 v[20:23],a[80:83],a[32:35],v[20:23]\n"
"v_exp_f32 v122,v44\n"
"v_mfma_f32_16x16x32_bf16 v[28:31],a[80:83],a[48:51],v[108:111]\n"
"v_cvt_pk_bf16_f32 v85,v119,v120\n"
"v_mfma_f32_16x16x32_bf16 v[28:31],a[84:87],a[52:55],v[28:31]\n"
"v_exp_f32 v123,v45\n"
"v_mfma_f32_16x16x32_bf16 v[28:31],a[88:91],a[56:59],v[28:31]\n"
"buffer_load_dwordx4 v112,s[24:27],s64 offen offset:2048 sc1 lds\n"
"v_mfma_f32_16x16x32_bf16 v[28:31],a[92:95],a[60:63],v[28:31]\n"
"v_exp_f32 v119,v46\n"
"v_cvt_pk_bf16_f32 v86,v122,v123\n"
"v_mfma_f32_16x16x32_bf16 v[24:27],a[76:79],a[60:63],v[108:111]\n"
"buffer_store_dwordx4 a[172:175],v112,s[24:27],s3 offen offset:3072 sc1\n"
"v_mfma_f32_16x16x32_bf16 v[24:27],a[72:75],a[56:59],v[24:27]\n"
"v_exp_f32 v120,v47\n"
"v_mfma_f32_16x16x32_bf16 v[24:27],a[68:71],a[52:55],v[24:27]\n"
"buffer_load_dwordx4 v112,s[24:27],s64 offen offset:3072 sc1 lds\n"
"v_mfma_f32_16x16x32_bf16 v[24:27],a[64:67],a[48:51],v[24:27]\n"
"s_waitcnt lgkmcnt(0)\n"
"v_exp_f32 v122,v48\n"
"v_mfma_f32_16x16x32_bf16 v[128:131],a[96:99],v[64:67],0\n"
"v_exp_f32 v123,v49\n"
"v_mfma_f32_16x16x32_bf16 v[160:163],a[96:99],v[68:71],0\n"
"ds_read_b128 a[64:67],v113 offset:32768\n"
"v_cvt_pk_bf16_f32 v87,v119,v120\n"
"v_mfma_f32_16x16x32_bf16 v[192:195],a[96:99],v[72:75],0\n"
"s_add_u32 m0,s67,53248\n"
"v_exp_f32 v119,v50\n"
"v_mfma_f32_16x16x32_bf16 v[224:227],a[96:99],v[76:79],0\n"
"ds_read_b128 a[68:71],v113 offset:33792\n"
"v_cvt_pk_bf16_f32 v88,v122,v123\n"
"v_mfma_f32_16x16x32_bf16 a[140:143],a[144:147],v[76:79],0\n"
"v_exp_f32 v120,v51\n"
"v_mfma_f32_16x16x32_bf16 v[228:231],a[100:103],v[76:79],0\n"
"ds_read_b128 a[72:75],v113 offset:34816\n"
"ds_read_b128 a[76:79],v113 offset:35840\n"
"v_mfma_f32_16x16x32_bf16 v[132:135],a[100:103],v[64:67],0\n"
"v_exp_f32 v122,v52\n"
"v_mfma_f32_16x16x32_bf16 v[164:167],a[100:103],v[68:71],0\n"
"ds_read_b128 a[80:83],v113 offset:36864\n"
"v_cvt_pk_bf16_f32 v89,v119,v120\n"
"v_mfma_f32_16x16x32_bf16 v[196:199],a[100:103],v[72:75],0\n"
"v_exp_f32 v123,v53\n"
"v_mfma_f32_16x16x32_bf16 a[136:139],a[144:147],v[72:75],0\n"
"ds_read_b128 a[84:87],v113 offset:37888\n"
"ds_read_b128 a[88:91],v113 offset:38912\n"
"v_mfma_f32_16x16x32_bf16 v[200:203],a[104:107],v[72:75],0\n"
"v_exp_f32 v119,v54\n"
"v_mfma_f32_16x16x32_bf16 v[232:235],a[104:107],v[76:79],0\n"
"ds_read_b128 a[92:95],v113 offset:39936\n"
"v_cvt_pk_bf16_f32 v90,v122,v123\n"
"v_mfma_f32_16x16x32_bf16 v[136:139],a[104:107],v[64:67],0\n"
"v_exp_f32 v120,v55\n"
"v_mfma_f32_16x16x32_bf16 v[168:171],a[104:107],v[68:71],0\n"
"buffer_load_dwordx4 v117,s[24:27],s64 offen offset:0 sc1 lds\n"
"v_mfma_f32_16x16x32_bf16 a[132:135],a[144:147],v[68:71],0\n"
"v_exp_f32 v122,v56\n"
"v_mfma_f32_16x16x32_bf16 v[172:175],a[108:111],v[68:71],0\n"
"v_cvt_pk_bf16_f32 v91,v119,v120\n"
"s_sub_u32 s3,s3,16384\n"
"v_mfma_f32_16x16x32_bf16 v[204:207],a[108:111],v[72:75],0\n"
"v_exp_f32 v123,v57\n"
"v_mfma_f32_16x16x32_bf16 v[236:239],a[108:111],v[76:79],0\n"
"buffer_load_dwordx4 v117,s[24:27],s64 offen offset:1024 sc1 lds\n"
"v_mfma_f32_16x16x32_bf16 v[140:143],a[108:111],v[64:67],0\n"
"v_exp_f32 v119,v58\n"
"v_cvt_pk_bf16_f32 v92,v122,v123\n"
"v_mfma_f32_16x16x32_bf16 a[128:131],a[144:147],v[64:67],0\n"
"buffer_store_dwordx4 a[176:179],v112,s[24:27],s3 offen offset:0 sc1\n"
"v_mfma_f32_16x16x32_bf16 v[144:147],a[112:115],v[64:67],0\n"
"v_exp_f32 v120,v59\n"
"v_mfma_f32_16x16x32_bf16 v[176:179],a[112:115],v[68:71],0\n"
"buffer_load_dwordx4 v117,s[24:27],s64 offen offset:2048 sc1 lds\n"
"v_mfma_f32_16x16x32_bf16 v[208:211],a[112:115],v[72:75],0\n"
"v_exp_f32 v122,v60\n"
"v_mfma_f32_16x16x32_bf16 v[240:243],a[112:115],v[76:79],0\n"
"v_cvt_pk_bf16_f32 v93,v119,v120\n"
"v_or3_b32 v118,v118,v85,v86\n"
"v_mfma_f32_16x16x32_bf16 v[244:247],a[116:119],v[76:79],0\n"
"v_exp_f32 v123,v61\n"
"v_mfma_f32_16x16x32_bf16 v[148:151],a[116:119],v[64:67],0\n"
"buffer_load_dwordx4 v117,s[24:27],s64 offen offset:3072 sc1 lds\n"
"v_mfma_f32_16x16x32_bf16 v[180:183],a[116:119],v[68:71],0\n"
"s_add_u32 s64,s64,16384\n"
"v_exp_f32 v119,v62\n"
"v_cvt_pk_bf16_f32 v94,v122,v123\n"
"v_mfma_f32_16x16x32_bf16 v[212:215],a[116:119],v[72:75],0\n"
"buffer_store_dwordx4 a[232:235],v112,s[24:27],s56 offen offset:2048 sc1\n"
"v_mfma_f32_16x16x32_bf16 v[216:219],a[120:123],v[72:75],0\n"
"v_exp_f32 v120,v63\n"
"v_mfma_f32_16x16x32_bf16 v[248:251],a[120:123],v[76:79],0\n"
"v_cvt_pk_bf16_f32 v95,v119,v120\n"
"v_or3_b32 v118,v118,v87,v88\n"
"v_mfma_f32_16x16x32_bf16 v[152:155],a[120:123],v[64:67],0\n"
"v_or3_b32 v118,v118,v89,v90\n"
"v_or3_b32 v118,v118,v91,v92\n"
"v_mfma_f32_16x16x32_bf16 v[184:187],a[120:123],v[68:71],0\n"
"v_or3_b32 v118,v118,v93,v94\n"
"v_or_b32 v118,v118,v95\n"
"v_mfma_f32_16x16x32_bf16 v[188:191],a[124:127],v[68:71],0\n"
"v_and_b32 v118,0x40004000,v118\n"
"v_cmp_ne_u32 vcc,0,v118\n"
"v_mfma_f32_16x16x32_bf16 v[220:223],a[124:127],v[72:75],0\n"
"v_mfma_f32_16x16x32_bf16 v[252:255],a[124:127],v[76:79],0\n"
"v_mfma_f32_16x16x32_bf16 v[156:159],a[124:127],v[64:67],0\n"
"s_cbranch_vccnz .Lfk_cs_h1\n"
".Lfk_cr_h1:\n"
"s_waitcnt lgkmcnt(0)\n"
"s_barrier\n"
"v_mfma_f32_16x16x32_bf16 v[32:35],a[64:67],a[0:3],v[96:99]\n"
"s_add_u32 m0,s55,12288\n"
"v_exp_f32 v122,v0\n"
"v_mfma_f32_16x16x32_bf16 v[32:35],a[68:71],a[4:7],v[32:35]\n"
"s_add_u32 s54,s54,s22\n"
"v_exp_f32 v123,v1\n"
"v_mfma_f32_16x16x32_bf16 v[32:35],a[72:75],a[8:11],v[32:35]\n"
"ds_read_b128 a[96:99],v114 offset:8192\n"
"ds_read_b128 a[100:103],v114 offset:9216\n"
"v_mfma_f32_16x16x32_bf16 v[32:35],a[76:79],a[12:15],v[32:35]\n"
"v_exp_f32 v119,v2\n"
"v_mfma_f32_16x16x32_bf16 v[36:39],a[92:95],a[12:15],v[96:99]\n"
"ds_read_b128 a[104:107],v114 offset:10240\n"
"v_cvt_pk_bf16_f32 v64,v122,v123\n"
"v_mfma_f32_16x16x32_bf16 v[36:39],a[88:91],a[8:11],v[36:39]\n"
"v_exp_f32 v120,v3\n"
"v_mfma_f32_16x16x32_bf16 v[36:39],a[84:87],a[4:7],v[36:39]\n"
"ds_read_b128 a[108:111],v114 offset:11264\n"
"ds_read_b128 a[112:115],v114 offset:12288\n"
"v_mfma_f32_16x16x32_bf16 v[36:39],a[80:83],a[0:3],v[36:39]\n"
"v_exp_f32 v122,v4\n"
"v_mfma_f32_16x16x32_bf16 v[44:47],a[80:83],a[16:19],v[100:103]\n"
"ds_read_b128 a[116:119],v114 offset:13312\n"
"v_cvt_pk_bf16_f32 v65,v119,v120\n"
"v_mfma_f32_16x16x32_bf16 v[44:47],a[84:87],a[20:23],v[44:47]\n"
"v_exp_f32 v123,v5\n"
"v_mfma_f32_16x16x32_bf16 v[44:47],a[88:91],a[24:27],v[44:47]\n"
"ds_read_b128 a[120:123],v114 offset:14336\n"
"ds_read_b128 a[124:127],v114 offset:15360\n"
"v_mfma_f32_16x16x32_bf16 v[44:47],a[92:95],a[28:31],v[44:47]\n"
"v_exp_f32 v119,v6\n"
"v_cvt_pk_bf16_f32 v66,v122,v123\n"
"v_mfma_f32_16x16x32_bf16 v[40:43],a[76:79],a[28:31],v[100:103]\n"
"buffer_store_dwordx4 a[180:183],v112,s[24:27],s3 offen offset:1024 sc1\n"
"v_mfma_f32_16x16x32_bf16 v[40:43],a[72:75],a[24:27],v[40:43]\n"
"v_exp_f32 v120,v7\n"
"v_mfma_f32_16x16x32_bf16 v[40:43],a[68:71],a[20:23],v[40:43]\n"
"buffer_load_dwordx4 v115,s[32:35],s54 offen sc1 lds\n"
"v_mfma_f32_16x16x32_bf16 v[40:43],a[64:67],a[16:19],v[40:43]\n"
"s_add_u32 m0,s67,32768\n"
"v_exp_f32 v122,v8\n"
"v_mfma_f32_16x16x32_bf16 v[48:51],a[64:67],a[32:35],v[104:107]\n"
"v_cvt_pk_bf16_f32 v67,v119,v120\n"
"v_or3_b32 v118,v64,v65,v66\n"
"v_mfma_f32_16x16x32_bf16 v[48:51],a[68:71],a[36:39],v[48:51]\n"
"v_exp_f32 v123,v9\n"
"v_mfma_f32_16x16x32_bf16 v[48:51],a[72:75],a[40:43],v[48:51]\n"
"buffer_load_dwordx4 v112,s[24:27],s64 offen offset:0 sc1 lds\n"
"v_mfma_f32_16x16x32_bf16 v[48:51],a[76:79],a[44:47],v[48:51]\n"
"v_exp_f32 v119,v10\n"
"v_mfma_f32_16x16x32_bf16 v[52:55],a[92:95],a[44:47],v[104:107]\n"
"v_cvt_pk_bf16_f32 v68,v122,v123\n"
"v_or3_b32 v118,v118,v67,v68\n"
"v_mfma_f32_16x16x32_bf16 v[52:55],a[88:91],a[40:43],v[52:55]\n"
"v_exp_f32 v120,v11\n"
"v_mfma_f32_16x16x32_bf16 v[52:55],a[84:87],a[36:39],v[52:55]\n"
"buffer_load_dwordx4 v112,s[24:27],s64 offen offset:1024 sc1 lds\n"
"v_mfma_f32_16x16x32_bf16 v[52:55],a[80:83],a[32:35],v[52:55]\n"
"v_exp_f32 v122,v12\n"
"v_mfma_f32_16x16x32_bf16 v[60:63],a[80:83],a[48:51],v[108:111]\n"
"v_cvt_pk_bf16_f32 v69,v119,v120\n"
"v_mfma_f32_16x16x32_bf16 v[60:63],a[84:87],a[52:55],v[60:63]\n"
"v_exp_f32 v123,v13\n"
"v_mfma_f32_16x16x32_bf16 v[60:63],a[88:91],a[56:59],v[60:63]\n"
"buffer_load_dwordx4 v112,s[24:27],s64 offen offset:2048 sc1 lds\n"
"v_mfma_f32_16x16x32_bf16 v[60:63],a[92:95],a[60:63],v[60:63]\n"
"v_exp_f32 v119,v14\n"
"v_cvt_pk_bf16_f32 v70,v122,v123\n"
"v_mfma_f32_16x16x32_bf16 v[56:59],a[76:79],a[60:63],v[108:111]\n"
"buffer_store_dwordx4 a[184:187],v112,s[24:27],s3 offen offset:2048 sc1\n"
"v_mfma_f32_16x16x32_bf16 v[56:59],a[72:75],a[56:59],v[56:59]\n"
"v_exp_f32 v120,v15\n"
"v_mfma_f32_16x16x32_bf16 v[56:59],a[68:71],a[52:55],v[56:59]\n"
"buffer_load_dwordx4 v112,s[24:27],s64 offen offset:3072 sc1 lds\n"
"v_mfma_f32_16x16x32_bf16 v[56:59],a[64:67],a[48:51],v[56:59]\n"
"s_waitcnt lgkmcnt(0)\n"
"v_exp_f32 v122,v16\n"
"v_mfma_f32_16x16x32_bf16 v[128:131],a[96:99],v[80:83],v[128:131]\n"
"v_exp_f32 v123,v17\n"
"v_mfma_f32_16x16x32_bf16 v[160:163],a[96:99],v[84:87],v[160:163]\n"
"ds_read_b128 a[64:67],v113 offset:0\n"
"v_cvt_pk_bf16_f32 v71,v119,v120\n"
"v_mfma_f32_16x16x32_bf16 v[192:195],a[96:99],v[88:91],v[192:195]\n"
"s_add_u32 m0,s67,36864\n"
"v_exp_f32 v119,v18\n"
"v_mfma_f32_16x16x32_bf16 v[224:227],a[96:99],v[92:95],v[224:227]\n"
"ds_read_b128 a[68:71],v113 offset:1024\n"
"v_cvt_pk_bf16_f32 v72,v122,v123\n"
"v_mfma_f32_16x16x32_bf16 a[140:143],a[144:147],v[92:95],a[140:143]\n"
"v_exp_f32 v120,v19\n"
"v_mfma_f32_16x16x32_bf16 v[228:231],a[100:103],v[92:95],v[228:231]\n"
"ds_read_b128 a[72:75],v113 offset:2048\n"
"ds_read_b128 a[76:79],v113 offset:3072\n"
"v_mfma_f32_16x16x32_bf16 v[132:135],a[100:103],v[80:83],v[132:135]\n"
"v_exp_f32 v122,v20\n"
"v_mfma_f32_16x16x32_bf16 v[164:167],a[100:103],v[84:87],v[164:167]\n"
"ds_read_b128 a[80:83],v113 offset:4096\n"
"v_cvt_pk_bf16_f32 v73,v119,v120\n"
"v_mfma_f32_16x16x32_bf16 v[196:199],a[100:103],v[88:91],v[196:199]\n"
"v_exp_f32 v123,v21\n"
"v_mfma_f32_16x16x32_bf16 a[136:139],a[144:147],v[88:91],a[136:139]\n"
"ds_read_b128 a[84:87],v113 offset:5120\n"
"ds_read_b128 a[88:91],v113 offset:6144\n"
"v_mfma_f32_16x16x32_bf16 v[200:203],a[104:107],v[88:91],v[200:203]\n"
"v_exp_f32 v119,v22\n"
"v_mfma_f32_16x16x32_bf16 v[232:235],a[104:107],v[92:95],v[232:235]\n"
"ds_read_b128 a[92:95],v113 offset:7168\n"
"v_cvt_pk_bf16_f32 v74,v122,v123\n"
"v_mfma_f32_16x16x32_bf16 v[136:139],a[104:107],v[80:83],v[136:139]\n"
"v_exp_f32 v120,v23\n"
"v_mfma_f32_16x16x32_bf16 v[168:171],a[104:107],v[84:87],v[168:171]\n"
"buffer_load_dwordx4 v117,s[24:27],s64 offen offset:0 sc1 lds\n"
"v_mfma_f32_16x16x32_bf16 a[132:135],a[144:147],v[84:87],a[132:135]\n"
"v_exp_f32 v122,v24\n"
"v_cvt_pk_bf16_f32 v75,v119,v120\n"
"v_mfma_f32_16x16x32_bf16 v[172:175],a[108:111],v[84:87],v[172:175]\n"
"buffer_store_dwordx4 a[188:191],v112,s[24:27],s3 offen offset:3072 sc1\n"
"v_mfma_f32_16x16x32_bf16 v[204:207],a[108:111],v[88:91],v[204:207]\n"
"v_exp_f32 v123,v25\n"
"v_mfma_f32_16x16x32_bf16 v[236:239],a[108:111],v[92:95],v[236:239]\n"
"buffer_load_dwordx4 v117,s[24:27],s64 offen offset:1024 sc1 lds\n"
"v_mfma_f32_16x16x32_bf16 v[140:143],a[108:111],v[80:83],v[140:143]\n"
"v_exp_f32 v119,v26\n"
"v_mfma_f32_16x16x32_bf16 a[128:131],a[144:147],v[80:83],a[128:131]\n"
"v_cvt_pk_bf16_f32 v76,v122,v123\n"
"s_or_b32 s3,s3,0x3c000\n"
"v_mfma_f32_16x16x32_bf16 v[144:147],a[112:115],v[80:83],v[144:147]\n"
"v_exp_f32 v120,v27\n"
"v_mfma_f32_16x16x32_bf16 v[176:179],a[112:115],v[84:87],v[176:179]\n"
"buffer_load_dwordx4 v117,s[24:27],s64 offen offset:2048 sc1 lds\n"
"v_mfma_f32_16x16x32_bf16 v[208:211],a[112:115],v[88:91],v[208:211]\n"
"v_exp_f32 v122,v28\n"
"v_mfma_f32_16x16x32_bf16 v[240:243],a[112:115],v[92:95],v[240:243]\n"
"v_cvt_pk_bf16_f32 v77,v119,v120\n"
"v_or3_b32 v118,v118,v69,v70\n"
"v_mfma_f32_16x16x32_bf16 v[244:247],a[116:119],v[92:95],v[244:247]\n"
"v_exp_f32 v123,v29\n"
"v_mfma_f32_16x16x32_bf16 v[148:151],a[116:119],v[80:83],v[148:151]\n"
"buffer_load_dwordx4 v117,s[24:27],s64 offen offset:3072 sc1 lds\n"
"v_mfma_f32_16x16x32_bf16 v[180:183],a[116:119],v[84:87],v[180:183]\n"
"s_add_u32 s64,s64,16384\n"
"v_exp_f32 v119,v30\n"
"v_cvt_pk_bf16_f32 v78,v122,v123\n"
"v_mfma_f32_16x16x32_bf16 v[212:215],a[116:119],v[88:91],v[212:215]\n"
"buffer_store_dwordx4 a[236:239],v112,s[24:27],s56 offen offset:3072 sc1\n"
"v_mfma_f32_16x16x32_bf16 v[216:219],a[120:123],v[88:91],v[216:219]\n"
"v_exp_f32 v120,v31\n"
"v_mfma_f32_16x16x32_bf16 v[248:251],a[120:123],v[92:95],v[248:251]\n"
"v_cvt_pk_bf16_f32 v79,v119,v120\n"
"v_or3_b32 v118,v118,v71,v72\n"
"v_mfma_f32_16x16x32_bf16 v[152:155],a[120:123],v[80:83],v[152:155]\n"
"v_or3_b32 v118,v118,v73,v74\n"
"v_or3_b32 v118,v118,v75,v76\n"
"v_mfma_f32_16x16x32_bf16 v[184:187],a[120:123],v[84:87],v[184:187]\n"
"v_or3_b32 v118,v118,v77,v78\n"
"v_or_b32 v118,v118,v79\n"
"v_mfma_f32_16x16x32_bf16 v[188:191],a[124:127],v[84:87],v[188:191]\n"
"v_and_b32 v118,0x40004000,v118\n"
"v_cmp_ne_u32 vcc,0,v118\n"
"v_mfma_f32_16x16x32_bf16 v[220:223],a[124:127],v[88:91],v[220:223]\n"
"v_mfma_f32_16x16x32_bf16 v[252:255],a[124:127],v[92:95],v[252:255]\n"
"v_mfma_f32_16x16x32_bf16 v[156:159],a[124:127],v[80:83],v[156:159]\n"
"s_cbranch_vccnz .Lfk_cs_h2\n"
".Lfk_cr_h2:\n"
"s_waitcnt vmcnt(27) lgkmcnt(0)\n"
"s_barrier\n"
".Lfk_g1:\n"
"s_and_b32 s82,s78,4096\n"
"s_lshl1_add_u32 s82,s82,69632\n"
"s_cmp_lt_u32 s52,2\n"
"s_cselect_b32 s64,s64,s62\n"
"s_cselect_b32 s67,s67,s82\n"
"s_add_u32 s89,s89,1\n"
"s_cmp_ge_u32 s89,s60\n"
"s_cbranch_scc1 .Lfk_Fx\n"
".Lfk_F:\n"
"v_mfma_f32_16x16x32_bf16 v[0:3],a[64:67],a[0:3],v[96:99]\n"
"s_swappc_b64 s[94:95],s[20:21]\n"
"v_exp_f32 v122,v32\n"
"v_mfma_f32_16x16x32_bf16 v[0:3],a[68:71],a[4:7],v[0:3]\n"
"v_exp_f32 v123,v33\n"
"v_mfma_f32_16x16x32_bf16 v[0:3],a[72:75],a[8:11],v[0:3]\n"
"ds_read_b128 a[96:99],v114 offset:24576\n"
"ds_read_b128 a[100:103],v114 offset:25600\n"
"v_mfma_f32_16x16x32_bf16 v[0:3],a[76:79],a[12:15],v[0:3]\n"
"v_exp_f32 v119,v34\n"
"v_mfma_f32_16x16x32_bf16 v[4:7],a[92:95],a[12:15],v[96:99]\n"
"s_add_u32 s101,s101,4\n"
"ds_read_b128 a[104:107],v114 offset:26624\n"
"v_cvt_pk_bf16_f32 v80,v122,v123\n"
"v_mfma_f32_16x16x32_bf16 v[4:7],a[88:91],a[8:11],v[4:7]\n"
"v_exp_f32 v120,v35\n"
"v_mfma_f32_16x16x32_bf16 v[4:7],a[84:87],a[4:7],v[4:7]\n"
"ds_read_b128 a[108:111],v114 offset:27648\n"
"ds_read_b128 a[112:115],v114 offset:28672\n"
"v_mfma_f32_16x16x32_bf16 v[4:7],a[80:83],a[0:3],v[4:7]\n"
"v_exp_f32 v122,v36\n"
"v_mfma_f32_16x16x32_bf16 v[12:15],a[80:83],a[16:19],v[100:103]\n"
"ds_read_b128 a[116:119],v114 offset:29696\n"
"v_cvt_pk_bf16_f32 v81,v119,v120\n"
"v_mfma_f32_16x16x32_bf16 v[12:15],a[84:87],a[20:23],v[12:15]\n"
"v_exp_f32 v123,v37\n"
"v_mfma_f32_16x16x32_bf16 v[12:15],a[88:91],a[24:27],v[12:15]\n"
"ds_read_b128 a[120:123],v114 offset:30720\n"
"ds_read_b128 a[124:127],v114 offset:31744\n"
"v_mfma_f32_16x16x32_bf16 v[12:15],a[92:95],a[28:31],v[12:15]\n"
"s_add_u32 m0,s67,0\n"
"v_exp_f32 v119,v38\n"
"v_mfma_f32_16x16x32_bf16 v[8:11],a[76:79],a[28:31],v[100:103]\n"
"v_cvt_pk_bf16_f32 v82,v122,v123\n"
"v_or3_b32 v118,v80,v81,v82\n"
"v_mfma_f32_16x16x32_bf16 v[8:11],a[72:75],a[24:27],v[8:11]\n"
"v_exp_f32 v120,v39\n"
"v_mfma_f32_16x16x32_bf16 v[8:11],a[68:71],a[20:23],v[8:11]\n"
"buffer_load_dwordx4 v112,s[24:27],s64 offen offset:0 sc1 lds\n"
"v_mfma_f32_16x16x32_bf16 v[8:11],a[64:67],a[16:19],v[8:11]\n"
"v_exp_f32 v122,v40\n"
"v_mfma_f32_16x16x32_bf16 v[16:19],a[64:67],a[32:35],v[104:107]\n"
"v_cvt_pk_bf16_f32 v83,v119,v120\n"
"v_mfma_f32_16x16x32_bf16 v[16:19],a[68:71],a[36:39],v[16:19]\n"
"v_exp_f32 v123,v41\n"
"v_mfma_f32_16x16x32_bf16 v[16:19],a[72:75],a[40:43],v[16:19]\n"
"buffer_load_dwordx4 v112,s[24:27],s64 offen offset:1024 sc1 lds\n"
"v_mfma_f32_16x16x32_bf16 v[16:19],a[76:79],a[44:47],v[16:19]\n"
"v_exp_f32 v119,v42\n"
"v_mfma_f32_16x16x32_bf16 v[20:23],a[92:95],a[44:47],v[104:107]\n"
"v_cvt_pk_bf16_f32 v84,v122,v123\n"
"v_or3_b32 v118,v118,v83,v84\n"
"v_mfma_f32_16x16x32_bf16 v[20:23],a[88:91],a[40:43],v[20:23]\n"
"v_exp_f32 v120,v43\n"
"v_mfma_f32_16x16x32_bf16 v[20:23],a[84:87],a[36:39],v[20:23]\n"
"v_cvt_pk_bf16_f32 v85,v119,v120\n"
"v_mfma_f32_16x16x32_bf16 v[20:23],a[80:83],a[32:35],v[20:23]\n"
"v_exp_f32 v122,v44\n"
"v_mfma_f32_16x16x32_bf16 v[28:31],a[80:83],a[48:51],v[108:111]\n"
"buffer_load_dwordx4 v112,s[24:27],s64 offen offset:2048 sc1 lds\n"
"v_mfma_f32_16x16x32_bf16 v[28:31],a[84:87],a[52:55],v[28:31]\n"
"v_exp_f32 v123,v45\n"
"v_mfma_f32_16x16x32_bf16 v[28:31],a[88:91],a[56:59],v[28:31]\n"
"v_cvt_pk_bf16_f32 v86,v122,v123\n"
"v_or3_b32 v118,v118,v85,v86\n"
"v_mfma_f32_16x16x32_bf16 v[28:31],a[92:95],a[60:63],v[28:31]\n"
"v_exp_f32 v119,v46\n"
"v_mfma_f32_16x16x32_bf16 v[24:27],a[76:79],a[60:63],v[108:111]\n"
"v_exp_f32 v122,v48\n"
"v_mfma_f32_16x16x32_bf16 v[24:27],a[72:75],a[56:59],v[24:27]\n"
"v_exp_f32 v120,v47\n"
"v_mfma_f32_16x16x32_bf16 v[24:27],a[68:71],a[52:55],v[24:27]\n"
"buffer_load_dwordx4 v112,s[24:27],s64 offen offset:3072 sc1 lds\n"
"v_mfma_f32_16x16x32_bf16 v[24:27],a[64:67],a[48:51],v[24:27]\n"
"s_add_u32 s64,s64,16384\n"
"s_waitcnt lgkmcnt(0)\n"
"v_mfma_f32_16x16x32_bf16 v[128:131],a[96:99],v[64:67],v[128:131]\n"
"s_andn2_b32 m0,s55,16384\n"
"v_exp_f32 v123,v49\n"
"v_mfma_f32_16x16x32_bf16 v[160:163],a[96:99],v[68:71],v[160:163]\n"
"ds_read_b128 a[64:67],v113 offset:16384\n"
"v_cvt_pk_bf16_f32 v87,v119,v120\n"
"v_mfma_f32_16x16x32_bf16 v[192:195],a[96:99],v[72:75],v[192:195]\n"
"s_add_u32 s54,s54,s22\n"
"v_exp_f32 v119,v50\n"
"v_mfma_f32_16x16x32_bf16 v[224:227],a[96:99],v[76:79],v[224:227]\n"
"ds_read_b128 a[68:71],v113 offset:17408\n"
"v_cvt_pk_bf16_f32 v88,v122,v123\n"
"v_mfma_f32_16x16x32_bf16 a[140:143],a[144:147],v[76:79],a[140:143]\n"
"v_exp_f32 v120,v51\n"
"v_mfma_f32_16x16x32_bf16 v[228:231],a[100:103],v[76:79],v[228:231]\n"
"ds_read_b128 a[72:75],v113 offset:18432\n"
"ds_read_b128 a[76:79],v113 offset:19456\n"
"v_mfma_f32_16x16x32_bf16 v[132:135],a[100:103],v[64:67],v[132:135]\n"
"v_exp_f32 v122,v52\n"
"v_mfma_f32_16x16x32_bf16 v[164:167],a[100:103],v[68:71],v[164:167]\n"
"ds_read_b128 a[80:83],v113 offset:20480\n"
"v_cvt_pk_bf16_f32 v89,v119,v120\n"
"v_mfma_f32_16x16x32_bf16 v[196:199],a[100:103],v[72:75],v[196:199]\n"
"v_exp_f32 v123,v53\n"
"v_mfma_f32_16x16x32_bf16 a[136:139],a[144:147],v[72:75],a[136:139]\n"
"ds_read_b128 a[84:87],v113 offset:21504\n"
"ds_read_b128 a[88:91],v113 offset:22528\n"
"v_mfma_f32_16x16x32_bf16 v[200:203],a[104:107],v[72:75],v[200:203]\n"
"v_exp_f32 v119,v54\n"
"v_mfma_f32_16x16x32_bf16 v[232:235],a[104:107],v[76:79],v[232:235]\n"
"ds_read_b128 a[92:95],v113 offset:23552\n"
"v_cvt_pk_bf16_f32 v90,v122,v123\n"
"v_mfma_f32_16x16x32_bf16 v[136:139],a[104:107],v[64:67],v[136:139]\n"
"v_exp_f32 v120,v55\n"
"v_mfma_f32_16x16x32_bf16 v[168:171],a[104:107],v[68:71],v[168:171]\n"
"s_waitcnt vmcnt(11)\n"
"ds_add_u32 v124,a152\n"
"v_mfma_f32_16x16x32_bf16 a[132:135],a[144:147],v[68:71],a[132:135]\n"
"v_exp_f32 v122,v56\n"
"v_mfma_f32_16x16x32_bf16 v[172:175],a[108:111],v[68:71],v[172:175]\n"
"v_cvt_pk_bf16_f32 v91,v119,v120\n"
"v_or3_b32 v118,v118,v87,v88\n"
"v_mfma_f32_16x16x32_bf16 v[204:207],a[108:111],v[72:75],v[204:207]\n"
"v_exp_f32 v123,v57\n"
"v_mfma_f32_16x16x32_bf16 v[236:239],a[108:111],v[76:79],v[236:239]\n"
"buffer_load_dwordx4 v115,s[32:35],s54 offen sc1 lds\n"
"v_mfma_f32_16x16x32_bf16 v[140:143],a[108:111],v[64:67],v[140:143]\n"
"v_exp_f32 v119,v58\n"
"v_mfma_f32_16x16x32_bf16 a[128:131],a[144:147],v[64:67],a[128:131]\n"
"v_cvt_pk_bf16_f32 v92,v122,v123\n"
"v_or3_b32 v118,v118,v89,v90\n"
"v_mfma_f32_16x16x32_bf16 v[144:147],a[112:115],v[64:67],v[144:147]\n"
"v_exp_f32 v120,v59\n"
"v_mfma_f32_16x16x32_bf16 v[176:179],a[112:115],v[68:71],v[176:179]\n"
"v_cvt_pk_bf16_f32 v93,v119,v120\n"
"v_or3_b32 v118,v118,v91,v92\n"
"v_mfma_f32_16x16x32_bf16 v[208:211],a[112:115],v[72:75],v[208:211]\n"
"v_exp_f32 v122,v60\n"
"v_mfma_f32_16x16x32_bf16 v[240:243],a[112:115],v[76:79],v[240:243]\n"
"v_mfma_f32_16x16x32_bf16 v[244:247],a[116:119],v[76:79],v[244:247]\n"
"v_exp_f32 v123,v61\n"
"v_mfma_f32_16x16x32_bf16 v[148:151],a[116:119],v[64:67],v[148:151]\n"
"v_cvt_pk_bf16_f32 v94,v122,v123\n"
"v_or3_b32 v118,v118,v93,v94\n"
"v_mfma_f32_16x16x32_bf16 v[180:183],a[116:119],v[68:71],v[180:183]\n"
"v_exp_f32 v119,v62\n"
"v_mfma_f32_16x16x32_bf16 v[212:215],a[116:119],v[72:75],v[212:215]\n"
"ds_read_b32 v125,v124\n"
"buffer_store_dwordx4 a[240:243],v112,s[24:27],s56 offen offset:0 sc1\n"
"v_mfma_f32_16x16x32_bf16 v[216:219],a[120:123],v[72:75],v[216:219]\n"
"v_exp_f32 v120,v63\n"
"v_mfma_f32_16x16x32_bf16 v[248:251],a[120:123],v[76:79],v[248:251]\n"
"v_cvt_pk_bf16_f32 v95,v119,v120\n"
"v_mfma_f32_16x16x32_bf16 v[152:155],a[120:123],v[64:67],v[152:155]\n"
"v_mfma_f32_16x16x32_bf16 v[184:187],a[120:123],v[68:71],v[184:187]\n"
"v_mfma_f32_16x16x32_bf16 v[188:191],a[124:127],v[68:71],v[188:191]\n"
"s_waitcnt lgkmcnt(0)\n"
"v_subrev_u32 v125,s101,v125\n"
"v_or3_b32 v125,v118,v95,v125\n"
"v_mfma_f32_16x16x32_bf16 v[220:223],a[124:127],v[72:75],v[220:223]\n"
"v_and_b32 v125,0x40004000,v125\n"
"v_cmp_ne_u32 vcc,0,v125\n"
"v_mfma_f32_16x16x32_bf16 v[252:255],a[124:127],v[76:79],v[252:255]\n"
"v_mfma_f32_16x16x32_bf16 v[156:159],a[124:127],v[64:67],v[156:159]\n"
"s_cbranch_vccnz .Lfk_cs_f3\n"
".Lfk_cr_f3:\n"
"v_mfma_f32_16x16x32_bf16 v[32:35],a[64:67],a[0:3],v[96:99]\n"
"s_add_u32 s101,s101,4\n"
"v_exp_f32 v122,v0\n"
"v_mfma_f32_16x16x32_bf16 v[32:35],a[68:71],a[4:7],v[32:35]\n"
"s_add_u32 m0,s55,4096\n"
"v_exp_f32 v123,v1\n"
"v_mfma_f32_16x16x32_bf16 v[32:35],a[72:75],a[8:11],v[32:35]\n"
"ds_read_b128 a[96:99],v114 offset:57344\n"
"ds_read_b128 a[100:103],v114 offset:58368\n"
"v_mfma_f32_16x16x32_bf16 v[32:35],a[76:79],a[12:15],v[32:35]\n"
"s_add_u32 s54,s54,s22\n"
"v_exp_f32 v119,v2\n"
"v_mfma_f32_16x16x32_bf16 v[36:39],a[92:95],a[12:15],v[96:99]\n"
"ds_read_b128 a[104:107],v114 offset:59392\n"
"v_cvt_pk_bf16_f32 v64,v122,v123\n"
"v_mfma_f32_16x16x32_bf16 v[36:39],a[88:91],a[8:11],v[36:39]\n"
"v_exp_f32 v120,v3\n"
"v_mfma_f32_16x16x32_bf16 v[36:39],a[84:87],a[4:7],v[36:39]\n"
"ds_read_b128 a[108:111],v114 offset:60416\n"
"ds_read_b128 a[112:115],v114 offset:61440\n"
"v_mfma_f32_16x16x32_bf16 v[36:39],a[80:83],a[0:3],v[36:39]\n"
"v_exp_f32 v122,v4\n"
"v_mfma_f32_16x16x32_bf16 v[44:47],a[80:83],a[16:19],v[100:103]\n"
"ds_read_b128 a[116:119],v114 offset:62464\n"
"v_cvt_pk_bf16_f32 v65,v119,v120\n"
"v_mfma_f32_16x16x32_bf16 v[44:47],a[84:87],a[20:23],v[44:47]\n"
"v_exp_f32 v123,v5\n"
"v_mfma_f32_16x16x32_bf16 v[44:47],a[88:91],a[24:27],v[44:47]\n"
"ds_read_b128 a[120:123],v114 offset:63488\n"
"ds_read_b128 a[124:127],v114 offset:64512\n"
"v_mfma_f32_16x16x32_bf16 v[44:47],a[92:95],a[28:31],v[44:47]\n"
"v_exp_f32 v119,v6\n"
"v_mfma_f32_16x16x32_bf16 v[40:43],a[76:79],a[28:31],v[100:103]\n"
"v_cvt_pk_bf16_f32 v66,v122,v123\n"
"v_or3_b32 v118,v64,v65,v66\n"
"v_mfma_f32_16x16x32_bf16 v[40:43],a[72:75],a[24:27],v[40:43]\n"
"v_exp_f32 v120,v7\n"
"v_mfma_f32_16x16x32_bf16 v[40:43],a[68:71],a[20:23],v[40:43]\n"
"buffer_load_dwordx4 v115,s[32:35],s54 offen sc1 lds\n"
"v_mfma_f32_16x16x32_bf16 v[40:43],a[64:67],a[16:19],v[40:43]\n"
"s_add_u32 m0,s67,16384\n"
"v_exp_f32 v122,v8\n"
"v_mfma_f32_16x16x32_bf16 v[48:51],a[64:67],a[32:35],v[104:107]\n"
"v_cvt_pk_bf16_f32 v67,v119,v120\n"
"v_mfma_f32_16x16x32_bf16 v[48:51],a[68:71],a[36:39],v[48:51]\n"
"v_exp_f32 v123,v9\n"
"v_mfma_f32_16x16x32_bf16 v[48:51],a[72:75],a[40:43],v[48:51]\n"
"buffer_load_dwordx4 v112,s[24:27],s64 offen offset:0 sc1 lds\n"
"v_mfma_f32_16x16x32_bf16 v[48:51],a[76:79],a[44:47],v[48:51]\n"
"v_exp_f32 v119,v10\n"
"v_mfma_f32_16x16x32_bf16 v[52:55],a[92:95],a[44:47],v[104:107]\n"
"v_cvt_pk_bf16_f32 v68,v122,v123\n"
"v_or3_b32 v118,v118,v67,v68\n"
"v_mfma_f32_16x16x32_bf16 v[52:55],a[88:91],a[40:43],v[52:55]\n"
"v_exp_f32 v120,v11\n"
"v_mfma_f32_16x16x32_bf16 v[52:55],a[84:87],a[36:39],v[52:55]\n"
"buffer_load_dwordx4 v112,s[24:27],s64 offen offset:1024 sc1 lds\n"
"v_mfma_f32_16x16x32_bf16 v[52:55],a[80:83],a[32:35],v[52:55]\n"
"v_exp_f32 v122,v12\n"
"v_mfma_f32_16x16x32_bf16 v[60:63],a[80:83],a[48:51],v[108:111]\n"
"v_cvt_pk_bf16_f32 v69,v119,v120\n"
"v_mfma_f32_16x16x32_bf16 v[60:63],a[84:87],a[52:55],v[60:63]\n"
"v_exp_f32 v123,v13\n"
"v_mfma_f32_16x16x32_bf16 v[60:63],a[88:91],a[56:59],v[60:63]\n"
"buffer_load_dwordx4 v112,s[24:27],s64 offen offset:2048 sc1 lds\n"
"v_mfma_f32_16x16x32_bf16 v[60:63],a[92:95],a[60:63],v[60:63]\n"
"v_exp_f32 v119,v14\n"
"v_mfma_f32_16x16x32_bf16 v[56:59],a[76:79],a[60:63],v[108:111]\n"
"v_cvt_pk_bf16_f32 v70,v122,v123\n"
"v_or3_b32 v118,v118,v69,v70\n"
"v_mfma_f32_16x16x32_bf16 v[56:59],a[72:75],a[56:59],v[56:59]\n"
"v_exp_f32 v120,v15\n"
"v_mfma_f32_16x16x32_bf16 v[56:59],a[68:71],a[52:55],v[56:59]\n"
"buffer_load_dwordx4 v112,s[24:27],s64 offen offset:3072 sc1 lds\n"
"v_mfma_f32_16x16x32_bf16 v[56:59],a[64:67],a[48:51],v[56:59]\n"
"s_waitcnt lgkmcnt(0)\n"
"v_exp_f32 v122,v16\n"
"v_mfma_f32_16x16x32_bf16 v[128:131],a[96:99],v[80:83],v[128:131]\n"
"v_exp_f32 v123,v17\n"
"v_mfma_f32_16x16x32_bf16 v[160:163],a[96:99],v[84:87],v[160:163]\n"
"s_add_u32 s64,s64,16384\n"
"ds_read_b128 a[64:67],v113 offset:49152\n"
"v_cvt_pk_bf16_f32 v71,v119,v120\n"
"v_mfma_f32_16x16x32_bf16 v[192:195],a[96:99],v[88:91],v[192:195]\n"
"v_exp_f32 v119,v18\n"
"v_mfma_f32_16x16x32_bf16 v[224:227],a[96:99],v[92:95],v[224:227]\n"
"ds_read_b128 a[68:71],v113 offset:50176\n"
"v_cvt_pk_bf16_f32 v72,v122,v123\n"
"v_mfma_f32_16x16x32_bf16 a[140:143],a[144:147],v[92:95],a[140:143]\n"
"v_exp_f32 v120,v19\n"
"v_mfma_f32_16x16x32_bf16 v[228:231],a[100:103],v[92:95],v[228:231]\n"
"ds_read_b128 a[72:75],v113 offset:51200\n"
"ds_read_b128 a[76:79],v113 offset:52224\n"
"v_mfma_f32_16x16x32_bf16 v[132:135],a[100:103],v[80:83],v[132:135]\n"
"v_exp_f32 v122,v20\n"
"v_mfma_f32_16x16x32_bf16 v[164:167],a[100:103],v[84:87],v[164:167]\n"
"ds_read_b128 a[80:83],v113 offset:53248\n"
"v_cvt_pk_bf16_f32 v73,v119,v120\n"
"v_mfma_f32_16x16x32_bf16 v[196:199],a[100:103],v[88:91],v[196:199]\n"
"v_exp_f32 v123,v21\n"
"v_mfma_f32_16x16x32_bf16 a[136:139],a[144:147],v[88:91],a[136:139]\n"
"ds_read_b128 a[84:87],v113 offset:54272\n"
"ds_read_b128 a[88:91],v113 offset:55296\n"
"v_mfma_f32_16x16x32_bf16 v[200:203],a[104:107],v[88:91],v[200:203]\n"
"v_exp_f32 v119,v22\n"
"v_mfma_f32_16x16x32_bf16 v[232:235],a[104:107],v[92:95],v[232:235]\n"
"ds_read_b128 a[92:95],v113 offset:56320\n"
"v_cvt_pk_bf16_f32 v74,v122,v123\n"
"v_mfma_f32_16x16x32_bf16 v[136:139],a[104:107],v[80:83],v[136:139]\n"
"v_exp_f32 v120,v23\n"
"v_mfma_f32_16x16x32_bf16 v[168:171],a[104:107],v[84:87],v[168:171]\n"
"s_waitcnt vmcnt(12)\n"
"ds_add_u32 v124,a152\n"
"v_mfma_f32_16x16x32_bf16 a[132:135],a[144:147],v[84:87],a[132:135]\n"
"v_exp_f32 v122,v24\n"
"v_mfma_f32_16x16x32_bf16 v[172:175],a[108:111],v[84:87],v[172:175]\n"
"v_cvt_pk_bf16_f32 v75,v119,v120\n"
"v_or3_b32 v118,v118,v71,v72\n"
"v_mfma_f32_16x16x32_bf16 v[204:207],a[108:111],v[88:91],v[204:207]\n"
"v_exp_f32 v123,v25\n"
"v_mfma_f32_16x16x32_bf16 v[236:239],a[108:111],v[92:95],v[236:239]\n"
"v_cvt_pk_bf16_f32 v76,v122,v123\n"
"v_or3_b32 v118,v118,v73,v74\n"
"v_mfma_f32_16x16x32_bf16 v[140:143],a[108:111],v[80:83],v[140:143]\n"
"v_exp_f32 v119,v26\n"
"v_mfma_f32_16x16x32_bf16 a[128:131],a[144:147],v[80:83],a[128:131]\n"
"v_or3_b32 v118,v118,v75,v76\n"
"v_mfma_f32_16x16x32_bf16 v[144:147],a[112:115],v[80:83],v[144:147]\n"
"v_exp_f32 v120,v27\n"
"v_mfma_f32_16x16x32_bf16 v[176:179],a[112:115],v[84:87],v[176:179]\n"
"v_cvt_pk_bf16_f32 v77,v119,v120\n"
"v_mfma_f32_16x16x32_bf16 v[208:211],a[112:115],v[88:91],v[208:211]\n"
"v_exp_f32 v122,v28\n"
"v_mfma_f32_16x16x32_bf16 v[240:243],a[112:115],v[92:95],v[240:243]\n"
"v_mfma_f32_16x16x32_bf16 v[244:247],a[116:119],v[92:95],v[244:247]\n"
"v_exp_f32 v123,v29\n"
"v_mfma_f32_16x16x32_bf16 v[148:151],a[116:119],v[80:83],v[148:151]\n"
"v_cvt_pk_bf16_f32 v78,v122,v123\n"
"v_or3_b32 v118,v118,v77,v78\n"
"v_mfma_f32_16x16x32_bf16 v[180:183],a[116:119],v[84:87],v[180:183]\n"
"v_exp_f32 v119,v30\n"
"v_mfma_f32_16x16x32_bf16 v[212:215],a[116:119],v[88:91],v[212:215]\n"
"ds_read_b32 v125,v124\n"
"buffer_store_dwordx4 a[244:247],v112,s[24:27],s56 offen offset:1024 sc1\n"
"v_mfma_f32_16x16x32_bf16 v[216:219],a[120:123],v[88:91],v[216:219]\n"
"v_exp_f32 v120,v31\n"
"v_mfma_f32_16x16x32_bf16 v[248:251],a[120:123],v[92:95],v[248:251]\n"
"v_cvt_pk_bf16_f32 v79,v119,v120\n"
"v_mfma_f32_16x16x32_bf16 v[152:155],a[120:123],v[80:83],v[152:155]\n"
"v_mfma_f32_16x16x32_bf16 v[184:187],a[120:123],v[84:87],v[184:187]\n"
"v_mfma_f32_16x16x32_bf16 v[188:191],a[124:127],v[84:87],v[188:191]\n"
"s_waitcnt lgkmcnt(0)\n"
"v_subrev_u32 v125,s101,v125\n"
"v_or3_b32 v125,v118,v79,v125\n"
"v_mfma_f32_16x16x32_bf16 v[220:223],a[124:127],v[88:91],v[220:223]\n"
"v_and_b32 v125,0x40004000,v125\n"
"v_cmp_ne_u32 vcc,0,v125\n"
"v_mfma_f32_16x16x32_bf16 v[252:255],a[124:127],v[92:95],v[252:255]\n"
"v_mfma_f32_16x16x32_bf16 v[156:159],a[124:127],v[80:83],v[156:159]\n"
"s_cbranch_vccnz .Lfk_cs_f0\n"
".Lfk_cr_f0:\n"
"v_mfma_f32_16x16x32_bf16 v[0:3],a[64:67],a[0:3],v[96:99]\n"
"s_add_u32 s101,s101,4\n"
"v_exp_f32 v122,v32\n"
"v_mfma_f32_16x16x32_bf16 v[0:3],a[68:71],a[4:7],v[0:3]\n"
"s_add_u32 m0,s55,8192\n"
"v_exp_f32 v123,v33\n"
"v_mfma_f32_16x16x32_bf16 v[0:3],a[72:75],a[8:11],v[0:3]\n"
"ds_read_b128 a[96:99],v114 offset:40960\n"
"ds_read_b128 a[100:103],v114 offset:41984\n"
"v_mfma_f32_16x16x32_bf16 v[0:3],a[76:79],a[12:15],v[0:3]\n"
"s_add_u32 s54,s54,s22\n"
"v_exp_f32 v119,v34\n"
"v_mfma_f32_16x16x32_bf16 v[4:7],a[92:95],a[12:15],v[96:99]\n"
"ds_read_b128 a[104:107],v114 offset:43008\n"
"v_cvt_pk_bf16_f32 v80,v122,v123\n"
"v_mfma_f32_16x16x32_bf16 v[4:7],a[88:91],a[8:11],v[4:7]\n"
"v_exp_f32 v120,v35\n"
"v_mfma_f32_16x16x32_bf16 v[4:7],a[84:87],a[4:7],v[4:7]\n"
"ds_read_b128 a[108:111],v114 offset:44032\n"
"ds_read_b128 a[112:115],v114 offset:45056\n"
"v_mfma_f32_16x16x32_bf16 v[4:7],a[80:83],a[0:3],v[4:7]\n"
"v_exp_f32 v122,v36\n"
"v_mfma_f32_16x16x32_bf16 v[12:15],a[80:83],a[16:19],v[100:103]\n"
"ds_read_b128 a[116:119],v114 offset:46080\n"
"v_cvt_pk_bf16_f32 v81,v119,v120\n"
"v_mfma_f32_16x16x32_bf16 v[12:15],a[84:87],a[20:23],v[12:15]\n"
"v_exp_f32 v123,v37\n"
"v_mfma_f32_16x16x32_bf16 v[12:15],a[88:91],a[24:27],v[12:15]\n"
"ds_read_b128 a[120:123],v114 offset:47104\n"
"ds_read_b128 a[124:127],v114 offset:48128\n"
"v_mfma_f32_16x16x32_bf16 v[12:15],a[92:95],a[28:31],v[12:15]\n"
"v_exp_f32 v119,v38\n"
"v_mfma_f32_16x16x32_bf16 v[8:11],a[76:79],a[28:31],v[100:103]\n"
"v_cvt_pk_bf16_f32 v82,v122,v123\n"
"v_or3_b32 v118,v80,v81,v82\n"
"v_mfma_f32_16x16x32_bf16 v[8:11],a[72:75],a[24:27],v[8:11]\n"
"v_exp_f32 v120,v39\n"
"v_mfma_f32_16x16x32_bf16 v[8:11],a[68:71],a[20:23],v[8:11]\n"
"buffer_load_dwordx4 v115,s[32:35],s54 offen sc1 lds\n"
"v_mfma_f32_16x16x32_bf16 v[8:11],a[64:67],a[16:19],v[8:11]\n"
"s_add_u32 m0,s67,49152\n"
"v_exp_f32 v122,v40\n"
"v_mfma_f32_16x16x32_bf16 v[16:19],a[64:67],a[32:35],v[104:107]\n"
"v_cvt_pk_bf16_f32 v83,v119,v120\n"
"v_mfma_f32_16x16x32_bf16 v[16:19],a[68:71],a[36:39],v[16:19]\n"
"v_exp_f32 v123,v41\n"
"v_mfma_f32_16x16x32_bf16 v[16:19],a[72:75],a[40:43],v[16:19]\n"
"buffer_load_dwordx4 v112,s[24:27],s64 offen offset:0 sc1 lds\n"
"v_mfma_f32_16x16x32_bf16 v[16:19],a[76:79],a[44:47],v[16:19]\n"
"v_exp_f32 v119,v42\n"
"v_mfma_f32_16x16x32_bf16 v[20:23],a[92:95],a[44:47],v[104:107]\n"
"v_cvt_pk_bf16_f32 v84,v122,v123\n"
"v_or3_b32 v118,v118,v83,v84\n"
"v_mfma_f32_16x16x32_bf16 v[20:23],a[88:91],a[40:43],v[20:23]\n"
"v_exp_f32 v120,v43\n"
"v_mfma_f32_16x16x32_bf16 v[20:23],a[84:87],a[36:39],v[20:23]\n"
"buffer_load_dwordx4 v112,s[24:27],s64 offen offset:1024 sc1 lds\n"
"v_mfma_f32_16x16x32_bf16 v[20:23],a[80:83],a[32:35],v[20:23]\n"
"v_exp_f32 v122,v44\n"
"v_mfma_f32_16x16x32_bf16 v[28:31],a[80:83],a[48:51],v[108:111]\n"
"v_cvt_pk_bf16_f32 v85,v119,v120\n"
"v_mfma_f32_16x16x32_bf16 v[28:31],a[84:87],a[52:55],v[28:31]\n"
"v_exp_f32 v123,v45\n"
"v_mfma_f32_16x16x32_bf16 v[28:31],a[88:91],a[56:59],v[28:31]\n"
"buffer_load_dwordx4 v112,s[24:27],s64 offen offset:2048 sc1 lds\n"
"v_mfma_f32_16x16x32_bf16 v[28:31],a[92:95],a[60:63],v[28:31]\n"
"v_exp_f32 v119,v46\n"
"v_mfma_f32_16x16x32_bf16 v[24:27],a[76:79],a[60:63],v[108:111]\n"
"v_cvt_pk_bf16_f32 v86,v122,v123\n"
"v_or3_b32 v118,v118,v85,v86\n"
"v_mfma_f32_16x16x32_bf16 v[24:27],a[72:75],a[56:59],v[24:27]\n"
"v_exp_f32 v120,v47\n"
"v_mfma_f32_16x16x32_bf16 v[24:27],a[68:71],a[52:55],v[24:27]\n"
"buffer_load_dwordx4 v112,s[24:27],s64 offen offset:3072 sc1 lds\n"
"v_mfma_f32_16x16x32_bf16 v[24:27],a[64:67],a[48:51],v[24:27]\n"
"s_waitcnt lgkmcnt(0)\n"
"v_exp_f32 v122,v48\n"
"v_mfma_f32_16x16x32_bf16 v[128:131],a[96:99],v[64:67],v[128:131]\n"
"v_exp_f32 v123,v49\n"
"v_mfma_f32_16x16x32_bf16 v[160:163],a[96:99],v[68:71],v[160:163]\n"
"s_add_u32 s64,s64,16384\n"
"ds_read_b128 a[64:67],v113 offset:32768\n"
"v_cvt_pk_bf16_f32 v87,v119,v120\n"
"v_mfma_f32_16x16x32_bf16 v[192:195],a[96:99],v[72:75],v[192:195]\n"
"v_exp_f32 v119,v50\n"
"v_mfma_f32_16x16x32_bf16 v[224:227],a[96:99],v[76:79],v[224:227]\n"
"ds_read_b128 a[68:71],v113 offset:33792\n"
"v_cvt_pk_bf16_f32 v88,v122,v123\n"
"v_mfma_f32_16x16x32_bf16 a[140:143],a[144:147],v[76:79],a[140:143]\n"
"v_exp_f32 v120,v51\n"
"v_mfma_f32_16x16x32_bf16 v[228:231],a[100:103],v[76:79],v[228:231]\n"
"ds_read_b128 a[72:75],v113 offset:34816\n"
"ds_read_b128 a[76:79],v113 offset:35840\n"
"v_mfma_f32_16x16x32_bf16 v[132:135],a[100:103],v[64:67],v[132:135]\n"
"v_exp_f32 v122,v52\n"
"v_mfma_f32_16x16x32_bf16 v[164:167],a[100:103],v[68:71],v[164:167]\n"
"ds_read_b128 a[80:83],v113 offset:36864\n"
"v_cvt_pk_bf16_f32 v89,v119,v120\n"
"v_mfma_f32_16x16x32_bf16 v[196:199],a[100:103],v[72:75],v[196:199]\n"
"v_exp_f32 v123,v53\n"
"v_mfma_f32_16x16x32_bf16 a[136:139],a[144:147],v[72:75],a[136:139]\n"
"ds_read_b128 a[84:87],v113 offset:37888\n"
"ds_read_b128 a[88:91],v113 offset:38912\n"
"v_mfma_f32_16x16x32_bf16 v[200:203],a[104:107],v[72:75],v[200:203]\n"
"v_exp_f32 v119,v54\n"
"v_mfma_f32_16x16x32_bf16 v[232:235],a[104:107],v[76:79],v[232:235]\n"
"ds_read_b128 a[92:95],v113 offset:39936\n"
"v_cvt_pk_bf16_f32 v90,v122,v123\n"
"v_mfma_f32_16x16x32_bf16 v[136:139],a[104:107],v[64:67],v[136:139]\n"
"v_exp_f32 v120,v55\n"
"v_mfma_f32_16x16x32_bf16 v[168:171],a[104:107],v[68:71],v[168:171]\n"
"s_waitcnt vmcnt(12)\n"
"ds_add_u32 v124,a152\n"
"v_mfma_f32_16x16x32_bf16 a[132:135],a[144:147],v[68:71],a[132:135]\n"
"v_exp_f32 v122,v56\n"
"v_mfma_f32_16x16x32_bf16 v[172:175],a[108:111],v[68:71],v[172:175]\n"
"v_cvt_pk_bf16_f32 v91,v119,v120\n"
"v_or3_b32 v118,v118,v87,v88\n"
"v_mfma_f32_16x16x32_bf16 v[204:207],a[108:111],v[72:75],v[204:207]\n"
"v_exp_f32 v123,v57\n"
"v_mfma_f32_16x16x32_bf16 v[236:239],a[108:111],v[76:79],v[236:239]\n"
"v_cvt_pk_bf16_f32 v92,v122,v123\n"
"v_or3_b32 v118,v118,v89,v90\n"
"v_mfma_f32_16x16x32_bf16 v[140:143],a[108:111],v[64:67],v[140:143]\n"
"v_exp_f32 v119,v58\n"
"v_mfma_f32_16x16x32_bf16 a[128:131],a[144:147],v[64:67],a[128:131]\n"
"v_or3_b32 v118,v118,v91,v92\n"
"v_mfma_f32_16x16x32_bf16 v[144:147],a[112:115],v[64:67],v[144:147]\n"
"v_exp_f32 v120,v59\n"
"v_mfma_f32_16x16x32_bf16 v[176:179],a[112:115],v[68:71],v[176:179]\n"
"v_cvt_pk_bf16_f32 v93,v119,v120\n"
"v_mfma_f32_16x16x32_bf16 v[208:211],a[112:115],v[72:75],v[208:211]\n"
"v_exp_f32 v122,v60\n"
"v_mfma_f32_16x16x32_bf16 v[240:243],a[112:115],v[76:79],v[240:243]\n"
"v_mfma_f32_16x16x32_bf16 v[244:247],a[116:119],v[76:79],v[244:247]\n"
"v_exp_f32 v123,v61\n"
"v_mfma_f32_16x16x32_bf16 v[148:151],a[116:119],v[64:67],v[148:151]\n"
"v_cvt_pk_bf16_f32 v94,v122,v123\n"
"v_or3_b32 v118,v118,v93,v94\n"
"v_mfma_f32_16x16x32_bf16 v[180:183],a[116:119],v[68:71],v[180:183]\n"
"v_exp_f32 v119,v62\n"
"v_mfma_f32_16x16x32_bf16 v[212:215],a[116:119],v[72:75],v[212:215]\n"
"ds_read_b32 v125,v124\n"
"buffer_store_dwordx4 a[248:251],v112,s[24:27],s56 offen offset:2048 sc1\n"
"v_mfma_f32_16x16x32_bf16 v[216:219],a[120:123],v[72:75],v[216:219]\n"
"v_exp_f32 v120,v63\n"
"v_mfma_f32_16x16x32_bf16 v[248:251],a[120:123],v[76:79],v[248:251]\n"
"v_cvt_pk_bf16_f32 v95,v119,v120\n"
"v_mfma_f32_16x16x32_bf16 v[152:155],a[120:123],v[64:67],v[152:155]\n"
"v_mfma_f32_16x16x32_bf16 v[184:187],a[120:123],v[68:71],v[184:187]\n"
"v_mfma_f32_16x16x32_bf16 v[188:191],a[124:127],v[68:71],v[188:191]\n"
"s_waitcnt lgkmcnt(0)\n"
"v_subrev_u32 v125,s101,v125\n"
"v_or3_b32 v125,v118,v95,v125\n"
"v_mfma_f32_16x16x32_bf16 v[220:223],a[124:127],v[72:75],v[220:223]\n"
"v_and_b32 v125,0x40004000,v125\n"
"v_cmp_ne_u32 vcc,0,v125\n"
"v_mfma_f32_16x16x32_bf16 v[252:255],a[124:127],v[76:79],v[252:255]\n"
"v_mfma_f32_16x16x32_bf16 v[156:159],a[124:127],v[64:67],v[156:159]\n"
"s_cbranch_vccnz .Lfk_cs_f1\n"
".Lfk_cr_f1:\n"
"v_mfma_f32_16x16x32_bf16 v[32:35],a[64:67],a[0:3],v[96:99]\n"
"s_add_u32 s101,s101,4\n"
"v_exp_f32 v122,v0\n"
"v_mfma_f32_16x16x32_bf16 v[32:35],a[68:71],a[4:7],v[32:35]\n"
"s_add_u32 m0,s55,12288\n"
"v_exp_f32 v123,v1\n"
"v_mfma_f32_16x16x32_bf16 v[32:35],a[72:75],a[8:11],v[32:35]\n"
"ds_read_b128 a[96:99],v114 offset:8192\n"
"ds_read_b128 a[100:103],v114 offset:9216\n"
"v_mfma_f32_16x16x32_bf16 v[32:35],a[76:79],a[12:15],v[32:35]\n"
"s_add_u32 s54,s54,s22\n"
"v_exp_f32 v119,v2\n"
"v_mfma_f32_16x16x32_bf16 v[36:39],a[92:95],a[12:15],v[96:99]\n"
"ds_read_b128 a[104:107],v114 offset:10240\n"
"v_cvt_pk_bf16_f32 v64,v122,v123\n"
"v_mfma_f32_16x16x32_bf16 v[36:39],a[88:91],a[8:11],v[36:39]\n"
"v_exp_f32 v120,v3\n"
"v_mfma_f32_16x16x32_bf16 v[36:39],a[84:87],a[4:7],v[36:39]\n"
"ds_read_b128 a[108:111],v114 offset:11264\n"
"ds_read_b128 a[112:115],v114 offset:12288\n"
"v_mfma_f32_16x16x32_bf16 v[36:39],a[80:83],a[0:3],v[36:39]\n"
"v_exp_f32 v122,v4\n"
"v_mfma_f32_16x16x32_bf16 v[44:47],a[80:83],a[16:19],v[100:103]\n"
"ds_read_b128 a[116:119],v114 offset:13312\n"
"v_cvt_pk_bf16_f32 v65,v119,v120\n"
"v_mfma_f32_16x16x32_bf16 v[44:47],a[84:87],a[20:23],v[44:47]\n"
"v_exp_f32 v123,v5\n"
"v_mfma_f32_16x16x32_bf16 v[44:47],a[88:91],a[24:27],v[44:47]\n"
"ds_read_b128 a[120:123],v114 offset:14336\n"
"ds_read_b128 a[124:127],v114 offset:15360\n"
"v_mfma_f32_16x16x32_bf16 v[44:47],a[92:95],a[28:31],v[44:47]\n"
"v_exp_f32 v119,v6\n"
"v_mfma_f32_16x16x32_bf16 v[40:43],a[76:79],a[28:31],v[100:103]\n"
"v_cvt_pk_bf16_f32 v66,v122,v123\n"
"v_or3_b32 v118,v64,v65,v66\n"
"v_mfma_f32_16x16x32_bf16 v[40:43],a[72:75],a[24:27],v[40:43]\n"
"v_exp_f32 v120,v7\n"
"v_mfma_f32_16x16x32_bf16 v[40:43],a[68:71],a[20:23],v[40:43]\n"
"buffer_load_dwordx4 v115,s[32:35],s54 offen sc1 lds\n"
"v_mfma_f32_16x16x32_bf16 v[40:43],a[64:67],a[16:19],v[40:43]\n"
"s_add_u32 m0,s67,32768\n"
"v_exp_f32 v122,v8\n"
"v_mfma_f32_16x16x32_bf16 v[48:51],a[64:67],a[32:35],v[104:107]\n"
"v_cvt_pk_bf16_f32 v67,v119,v120\n"
"v_mfma_f32_16x16x32_bf16 v[48:51],a[68:71],a[36:39],v[48:51]\n"
"v_exp_f32 v123,v9\n"
"v_mfma_f32_16x16x32_bf16 v[48:51],a[72:75],a[40:43],v[48:51]\n"
"buffer_load_dwordx4 v112,s[24:27],s64 offen offset:0 sc1 lds\n"
"v_mfma_f32_16x16x32_bf16 v[48:51],a[76:79],a[44:47],v[48:51]\n"
"v_exp_f32 v119,v10\n"
"v_mfma_f32_16x16x32_bf16 v[52:55],a[92:95],a[44:47],v[104:107]\n"
"v_cvt_pk_bf16_f32 v68,v122,v123\n"
"v_or3_b32 v118,v118,v67,v68\n"
"v_mfma_f32_16x16x32_bf16 v[52:55],a[88:91],a[40:43],v[52:55]\n"
"v_exp_f32 v120,v11\n"
"v_mfma_f32_16x16x32_bf16 v[52:55],a[84:87],a[36:39],v[52:55]\n"
"buffer_load_dwordx4 v112,s[24:27],s64 offen offset:1024 sc1 lds\n"
"v_mfma_f32_16x16x32_bf16 v[52:55],a[80:83],a[32:35],v[52:55]\n"
"v_exp_f32 v122,v12\n"
"v_mfma_f32_16x16x32_bf16 v[60:63],a[80:83],a[48:51],v[108:111]\n"
"v_cvt_pk_bf16_f32 v69,v119,v120\n"
"v_mfma_f32_16x16x32_bf16 v[60:63],a[84:87],a[52:55],v[60:63]\n"
"v_exp_f32 v123,v13\n"
"v_mfma_f32_16x16x32_bf16 v[60:63],a[88:91],a[56:59],v[60:63]\n"
"buffer_load_dwordx4 v112,s[24:27],s64 offen offset:2048 sc1 lds\n"
"v_mfma_f32_16x16x32_bf16 v[60:63],a[92:95],a[60:63],v[60:63]\n"
"v_exp_f32 v119,v14\n"
"v_mfma_f32_16x16x32_bf16 v[56:59],a[76:79],a[60:63],v[108:111]\n"
"v_cvt_pk_bf16_f32 v70,v122,v123\n"
"v_or3_b32 v118,v118,v69,v70\n"
"v_mfma_f32_16x16x32_bf16 v[56:59],a[72:75],a[56:59],v[56:59]\n"
"v_exp_f32 v120,v15\n"
"v_mfma_f32_16x16x32_bf16 v[56:59],a[68:71],a[52:55],v[56:59]\n"
"buffer_load_dwordx4 v112,s[24:27],s64 offen offset:3072 sc1 lds\n"
"v_mfma_f32_16x16x32_bf16 v[56:59],a[64:67],a[48:51],v[56:59]\n"
"s_waitcnt lgkmcnt(0)\n"
"v_exp_f32 v122,v16\n"
"v_mfma_f32_16x16x32_bf16 v[128:131],a[96:99],v[80:83],v[128:131]\n"
"v_exp_f32 v123,v17\n"
"v_mfma_f32_16x16x32_bf16 v[160:163],a[96:99],v[84:87],v[160:163]\n"
"s_add_u32 s64,s64,16384\n"
"ds_read_b128 a[64:67],v113 offset:0\n"
"v_cvt_pk_bf16_f32 v71,v119,v120\n"
"v_mfma_f32_16x16x32_bf16 v[192:195],a[96:99],v[88:91],v[192:195]\n"
"v_exp_f32 v119,v18\n"
"v_mfma_f32_16x16x32_bf16 v[224:227],a[96:99],v[92:95],v[224:227]\n"
"ds_read_b128 a[68:71],v113 offset:1024\n"
"v_cvt_pk_bf16_f32 v72,v122,v123\n"
"v_mfma_f32_16x16x32_bf16 a[140:143],a[144:147],v[92:95],a[140:143]\n"
"v_exp_f32 v120,v19\n"
"v_mfma_f32_16x16x32_bf16 v[228:231],a[100:103],v[92:95],v[228:231]\n"
"ds_read_b128 a[72:75],v113 offset:2048\n"
"ds_read_b128 a[76:79],v113 offset:3072\n"
"v_mfma_f32_16x16x32_bf16 v[132:135],a[100:103],v[80:83],v[132:135]\n"
"v_exp_f32 v122,v20\n"
"v_mfma_f32_16x16x32_bf16 v[164:167],a[100:103],v[84:87],v[164:167]\n"
"ds_read_b128 a[80:83],v113 offset:4096\n"
"v_cvt_pk_bf16_f32 v73,v119,v120\n"
"v_mfma_f32_16x16x32_bf16 v[196:199],a[100:103],v[88:91],v[196:199]\n"
"v_exp_f32 v123,v21\n"
"v_mfma_f32_16x16x32_bf16 a[136:139],a[144:147],v[88:91],a[136:139]\n"
"ds_read_b128 a[84:87],v113 offset:5120\n"
"ds_read_b128 a[88:91],v113 offset:6144\n"
"v_mfma_f32_16x16x32_bf16 v[200:203],a[104:107],v[88:91],v[200:203]\n"
"v_exp_f32 v119,v22\n"
"v_mfma_f32_16x16x32_bf16 v[232:235],a[104:107],v[92:95],v[232:235]\n"
"ds_read_b128 a[92:95],v113 offset:7168\n"
"v_cvt_pk_bf16_f32 v74,v122,v123\n"
"v_mfma_f32_16x16x32_bf16 v[136:139],a[104:107],v[80:83],v[136:139]\n"
"v_exp_f32 v120,v23\n"
"v_mfma_f32_16x16x32_bf16 v[168:171],a[104:107],v[84:87],v[168:171]\n"
"s_waitcnt vmcnt(12)\n"
"ds_add_u32 v124,a152\n"
"v_mfma_f32_16x16x32_bf16 a[132:135],a[144:147],v[84:87],a[132:135]\n"
"v_exp_f32 v122,v24\n"
"v_mfma_f32_16x16x32_bf16 v[172:175],a[108:111],v[84:87],v[172:175]\n"
"v_cvt_pk_bf16_f32 v75,v119,v120\n"
"v_or3_b32 v118,v118,v71,v72\n"
"v_mfma_f32_16x16x32_bf16 v[204:207],a[108:111],v[88:91],v[204:207]\n"
"v_exp_f32 v123,v25\n"
"v_mfma_f32_16x16x32_bf16 v[236:239],a[108:111],v[92:95],v[236:239]\n"
"v_cvt_pk_bf16_f32 v76,v122,v123\n"
"v_or3_b32 v118,v118,v73,v74\n"
"v_mfma_f32_16x16x32_bf16 v[140:143],a[108:111],v[80:83],v[140:143]\n"
"v_exp_f32 v119,v26\n"
"v_mfma_f32_16x16x32_bf16 a[128:131],a[144:147],v[80:83],a[128:131]\n"
"v_or3_b32 v118,v118,v75,v76\n"
"v_mfma_f32_16x16x32_bf16 v[144:147],a[112:115],v[80:83],v[144:147]\n"
"v_exp_f32 v120,v27\n"
"v_mfma_f32_16x16x32_bf16 v[176:179],a[112:115],v[84:87],v[176:179]\n"
"v_cvt_pk_bf16_f32 v77,v119,v120\n"
"v_mfma_f32_16x16x32_bf16 v[208:211],a[112:115],v[88:91],v[208:211]\n"
"v_exp_f32 v122,v28\n"
"v_mfma_f32_16x16x32_bf16 v[240:243],a[112:115],v[92:95],v[240:243]\n"
"v_mfma_f32_16x16x32_bf16 v[244:247],a[116:119],v[92:95],v[244:247]\n"
"v_exp_f32 v123,v29\n"
"v_mfma_f32_16x16x32_bf16 v[148:151],a[116:119],v[80:83],v[148:151]\n"
"v_cvt_pk_bf16_f32 v78,v122,v123\n"
"v_or3_b32 v118,v118,v77,v78\n"
"v_mfma_f32_16x16x32_bf16 v[180:183],a[116:119],v[84:87],v[180:183]\n"
"v_exp_f32 v119,v30\n"
"v_mfma_f32_16x16x32_bf16 v[212:215],a[116:119],v[88:91],v[212:215]\n"
"ds_read_b32 v125,v124\n"
"buffer_store_dwordx4 a[252:255],v112,s[24:27],s56 offen offset:3072 sc1\n"
"v_mfma_f32_16x16x32_bf16 v[216:219],a[120:123],v[88:91],v[216:219]\n"
"s_add_u32 s89,s89,1\n"
"v_exp_f32 v120,v31\n"
"v_mfma_f32_16x16x32_bf16 v[248:251],a[120:123],v[92:95],v[248:251]\n"
"v_cvt_pk_bf16_f32 v79,v119,v120\n"
"v_mfma_f32_16x16x32_bf16 v[152:155],a[120:123],v[80:83],v[152:155]\n"
"v_mfma_f32_16x16x32_bf16 v[184:187],a[120:123],v[84:87],v[184:187]\n"
"v_mfma_f32_16x16x32_bf16 v[188:191],a[124:127],v[84:87],v[188:191]\n"
"s_waitcnt lgkmcnt(0)\n"
"v_subrev_u32 v125,s101,v125\n"
"v_or3_b32 v125,v118,v79,v125\n"
"v_mfma_f32_16x16x32_bf16 v[220:223],a[124:127],v[88:91],v[220:223]\n"
"v_and_b32 v125,0x40004000,v125\n"
"v_cmp_ne_u32 vcc,0,v125\n"
"v_mfma_f32_16x16x32_bf16 v[252:255],a[124:127],v[92:95],v[252:255]\n"
"v_mfma_f32_16x16x32_bf16 v[156:159],a[124:127],v[80:83],v[156:159]\n"
"s_cbranch_vccnz .Lfk_cs_f2\n"
".Lfk_cr_f2:\n"
"s_cmp_lt_u32 s89,s60\n"
"s_cbranch_scc1 .Lfk_F\n"
"s_branch .Lfk_Fx\n"
".Lfk_cs_f3:\n"
"s_call_b64 s[94:95],.Lfk_meet3\n"
"v_or_b32 v118,v118,v95\n"
"v_and_b32 v118,0x40004000,v118\n"
"v_cmp_ne_u32 vcc,0,v118\n"
"s_nop 0\n"
"s_cbranch_vccz .Lfk_cr_f3\n"
"s_mov_b32 s91,15\n"
"s_call_b64 s[94:95],.Lfk_rare3\n"
"s_branch .Lfk_cr_f3\n"
".Lfk_cs_f0:\n"
"s_call_b64 s[94:95],.Lfk_meet3\n"
"v_or_b32 v118,v118,v79\n"
"v_and_b32 v118,0x40004000,v118\n"
"v_cmp_ne_u32 vcc,0,v118\n"
"s_nop 0\n"
"s_cbranch_vccz .Lfk_cr_f0\n"
"s_mov_b32 s91,15\n"
"s_call_b64 s[94:95],.Lfk_rare2\n"
"s_branch .Lfk_cr_f0\n"
".Lfk_cs_f1:\n"
"s_call_b64 s[94:95],.Lfk_meet3\n"
"v_or_b32 v118,v118,v95\n"
"v_and_b32 v118,0x40004000,v118\n"
"v_cmp_ne_u32 vcc,0,v118\n"
"s_nop 0\n"
"s_cbranch_vccz .Lfk_cr_f1\n"
"s_mov_b32 s91,15\n"
"s_call_b64 s[94:95],.Lfk_rare3\n"
"s_branch .Lfk_cr_f1\n"
".Lfk_cs_f2:\n"
"s_call_b64 s[94:95],.Lfk_meet3\n"
"v_or_b32 v118,v118,v79\n"
"v_and_b32 v118,0x40004000,v118\n"
"v_cmp_ne_u32 vcc,0,v118\n"
"s_nop 0\n"
"s_cbranch_vccz .Lfk_cr_f2\n"
"s_mov_b32 s91,15\n"
"s_call_b64 s[94:95],.Lfk_rare2\n"
"s_branch .Lfk_cr_f2\n"
".Lfk_cs_g3:\n"
"s_call_b64 s[94:95],.Lfk_meet3\n"
"v_or_b32 v118,v118,v95\n"
"v_and_b32 v118,0x40004000,v118\n"
"v_cmp_ne_u32 vcc,0,v118\n"
"s_nop 0\n"
"s_cbranch_vccz .Lfk_cr_g3\n"
"s_mov_b32 s91,15\n"
"s_call_b64 s[94:95],.Lfk_rare3\n"
"s_branch .Lfk_cr_g3\n"
".Lfk_cs_g0:\n"
"s_call_b64 s[94:95],.Lfk_meet3\n"
"v_or_b32 v118,v118,v79\n"
"v_and_b32 v118,0x40004000,v118\n"
"v_cmp_ne_u32 vcc,0,v118\n"
"s_nop 0\n"
"s_cbranch_vccz .Lfk_cr_g0\n"
"s_mov_b32 s91,15\n"
"s_call_b64 s[94:95],.Lfk_rare2\n"
"s_branch .Lfk_cr_g0\n"
".Lfk_cs_g1:\n"
"s_call_b64 s[94:95],.Lfk_meet3\n"
"v_or_b32 v118,v118,v95\n"
"v_and_b32 v118,0x40004000,v118\n"
"v_cmp_ne_u32 vcc,0,v118\n"
"s_nop 0\n"
"s_cbranch_vccz .Lfk_cr_g1\n"
"s_mov_b32 s91,15\n"
"s_call_b64 s[94:95],.Lfk_rare3\n"
"s_branch .Lfk_cr_g1\n"
".Lfk_cs_g2:\n"
"s_call_b64 s[94:95],.Lfk_meet3\n"
"v_or_b32 v118,v118,v79\n"
"v_and_b32 v118,0x40004000,v118\n"
"v_cmp_ne_u32 vcc,0,v118\n"
"s_nop 0\n"
"s_cbranch_vccz .Lfk_cr_g2\n"
"s_mov_b32 s91,15\n"
"s_call_b64 s[94:95],.Lfk_rare2\n"
"s_branch .Lfk_cr_g2\n"
".Lfk_meet3:\n"
"s_sub_u32 s82,s101,0\n"
"s_mov_b32 s84,0x1000000\n"
".Lfk_meetl3:\n"
"ds_read_b32 v125,v124\n"
"s_sub_u32 s84,s84,1\n"
"s_cbranch_scc1 .Lfk_meetx3\n"
"s_waitcnt lgkmcnt(0)\n"
"v_cmp_gt_u32 vcc,s82,v125\n"
"s_nop 0\n"
"s_cbranch_vccnz .Lfk_meetl3\n"
".Lfk_meetx3:\n"
"s_setpc_b64 s[94:95]\n"
".Lfk_Fx:\n"
"s_mov_b32 s82,2146435072\n"
"buffer_load_dword a148,off,s[24:27],s82\n"
"buffer_load_dword a148,off,s[24:27],s82\n"
"buffer_load_dword a148,off,s[24:27],s82\n"
"buffer_load_dword a148,off,s[24:27],s82\n"
"s_cmp_ge_u32 s52,2\n"
"s_cbranch_scc1 .Lfk_winA\n"
".Lfk_H:\n"
"v_mfma_f32_16x16x32_bf16 v[0:3],a[64:67],a[0:3],v[96:99]\n"
"s_swappc_b64 s[94:95],s[20:21]\n"
"s_add_u32 s101,s101,4\n"
"v_exp_f32 v122,v32\n"
"v_mfma_f32_16x16x32_bf16 v[0:3],a[68:71],a[4:7],v[0:3]\n"
"s_cmp_eq_u32 s64,s65\n"
"v_exp_f32 v123,v33\n"
"v_mfma_f32_16x16x32_bf16 v[0:3],a[72:75],a[8:11],v[0:3]\n"
"ds_read_b128 a[96:99],v114 offset:24576\n"
"ds_read_b128 a[100:103],v114 offset:25600\n"
"v_mfma_f32_16x16x32_bf16 v[0:3],a[76:79],a[12:15],v[0:3]\n"
"s_cselect_b32 s64,s66,s64\n"
"v_exp_f32 v119,v34\n"
"v_mfma_f32_16x16x32_bf16 v[4:7],a[92:95],a[12:15],v[96:99]\n"
"ds_read_b128 a[104:107],v114 offset:26624\n"
"v_cvt_pk_bf16_f32 v80,v122,v123\n"
"v_mfma_f32_16x16x32_bf16 v[4:7],a[88:91],a[8:11],v[4:7]\n"
"v_exp_f32 v120,v35\n"
"v_mfma_f32_16x16x32_bf16 v[4:7],a[84:87],a[4:7],v[4:7]\n"
"ds_read_b128 a[108:111],v114 offset:27648\n"
"ds_read_b128 a[112:115],v114 offset:28672\n"
"v_mfma_f32_16x16x32_bf16 v[4:7],a[80:83],a[0:3],v[4:7]\n"
"v_exp_f32 v122,v36\n"
"v_mfma_f32_16x16x32_bf16 v[12:15],a[80:83],a[16:19],v[100:103]\n"
"ds_read_b128 a[116:119],v114 offset:29696\n"
"v_cvt_pk_bf16_f32 v81,v119,v120\n"
"v_mfma_f32_16x16x32_bf16 v[12:15],a[84:87],a[20:23],v[12:15]\n"
"v_exp_f32 v123,v37\n"
"v_mfma_f32_16x16x32_bf16 v[12:15],a[88:91],a[24:27],v[12:15]\n"
"ds_read_b128 a[120:123],v114 offset:30720\n"
"ds_read_b128 a[124:127],v114 offset:31744\n"
"v_mfma_f32_16x16x32_bf16 v[12:15],a[92:95],a[28:31],v[12:15]\n"
"s_add_u32 m0,s67,0\n"
"v_exp_f32 v119,v38\n"
"v_mfma_f32_16x16x32_bf16 v[8:11],a[76:79],a[28:31],v[100:103]\n"
"v_cvt_pk_bf16_f32 v82,v122,v123\n"
"v_or3_b32 v118,v80,v81,v82\n"
"v_mfma_f32_16x16x32_bf16 v[8:11],a[72:75],a[24:27],v[8:11]\n"
"v_exp_f32 v120,v39\n"
"v_mfma_f32_16x16x32_bf16 v[8:11],a[68:71],a[20:23],v[8:11]\n"
"buffer_load_dwordx4 v112,s[24:27],s64 offen offset:0 sc1 lds\n"
"v_mfma_f32_16x16x32_bf16 v[8:11],a[64:67],a[16:19],v[8:11]\n"
"v_exp_f32 v122,v40\n"
"v_mfma_f32_16x16x32_bf16 v[16:19],a[64:67],a[32:35],v[104:107]\n"
"v_cvt_pk_bf16_f32 v83,v119,v120\n"
"v_mfma_f32_16x16x32_bf16 v[16:19],a[68:71],a[36:39],v[16:19]\n"
"v_exp_f32 v123,v41\n"
"v_mfma_f32_16x16x32_bf16 v[16:19],a[72:75],a[40:43],v[16:19]\n"
"buffer_load_dwordx4 v112,s[24:27],s64 offen offset:1024 sc1 lds\n"
"v_mfma_f32_16x16x32_bf16 v[16:19],a[76:79],a[44:47],v[16:19]\n"
"v_exp_f32 v119,v42\n"
"v_mfma_f32_16x16x32_bf16 v[20:23],a[92:95],a[44:47],v[104:107]\n"
"v_cvt_pk_bf16_f32 v84,v122,v123\n"
"v_or3_b32 v118,v118,v83,v84\n"
"v_mfma_f32_16x16x32_bf16 v[20:23],a[88:91],a[40:43],v[20:23]\n"
"v_exp_f32 v120,v43\n"
"v_mfma_f32_16x16x32_bf16 v[20:23],a[84:87],a[36:39],v[20:23]\n"
"v_cvt_pk_bf16_f32 v85,v119,v120\n"
"v_mfma_f32_16x16x32_bf16 v[20:23],a[80:83],a[32:35],v[20:23]\n"
"v_exp_f32 v122,v44\n"
"v_mfma_f32_16x16x32_bf16 v[28:31],a[80:83],a[48:51],v[108:111]\n"
"buffer_load_dwordx4 v112,s[24:27],s64 offen offset:2048 sc1 lds\n"
"v_mfma_f32_16x16x32_bf16 v[28:31],a[84:87],a[52:55],v[28:31]\n"
"v_exp_f32 v123,v45\n"
"v_mfma_f32_16x16x32_bf16 v[28:31],a[88:91],a[56:59],v[28:31]\n"
"v_cvt_pk_bf16_f32 v86,v122,v123\n"
"v_or3_b32 v118,v118,v85,v86\n"
"v_mfma_f32_16x16x32_bf16 v[28:31],a[92:95],a[60:63],v[28:31]\n"
"v_exp_f32 v119,v46\n"
"v_mfma_f32_16x16x32_bf16 v[24:27],a[76:79],a[60:63],v[108:111]\n"
"v_mfma_f32_16x16x32_bf16 v[24:27],a[72:75],a[56:59],v[24:27]\n"
"v_exp_f32 v120,v47\n"
"v_mfma_f32_16x16x32_bf16 v[24:27],a[68:71],a[52:55],v[24:27]\n"
"buffer_load_dwordx4 v112,s[24:27],s64 offen offset:3072 sc1 lds\n"
"v_mfma_f32_16x16x32_bf16 v[24:27],a[64:67],a[48:51],v[24:27]\n"
"s_waitcnt lgkmcnt(0)\n"
"v_exp_f32 v122,v48\n"
"v_mfma_f32_16x16x32_bf16 v[128:131],a[96:99],v[64:67],v[128:131]\n"
"v_exp_f32 v123,v49\n"
"v_mfma_f32_16x16x32_bf16 v[160:163],a[96:99],v[68:71],v[160:163]\n"
"ds_read_b128 a[64:67],v113 offset:16384\n"
"v_cvt_pk_bf16_f32 v87,v119,v120\n"
"v_mfma_f32_16x16x32_bf16 v[192:195],a[96:99],v[72:75],v[192:195]\n"
"s_add_u32 m0,s67,4096\n"
"v_exp_f32 v119,v50\n"
"v_mfma_f32_16x16x32_bf16 v[224:227],a[96:99],v[76:79],v[224:227]\n"
"ds_read_b128 a[68:71],v113 offset:17408\n"
"v_cvt_pk_bf16_f32 v88,v122,v123\n"
"v_mfma_f32_16x16x32_bf16 a[140:143],a[144:147],v[76:79],a[140:143]\n"
"v_exp_f32 v120,v51\n"
"v_mfma_f32_16x16x32_bf16 v[228:231],a[100:103],v[76:79],v[228:231]\n"
"ds_read_b128 a[72:75],v113 offset:18432\n"
"ds_read_b128 a[76:79],v113 offset:19456\n"
"v_mfma_f32_16x16x32_bf16 v[132:135],a[100:103],v[64:67],v[132:135]\n"
"v_exp_f32 v122,v52\n"
"v_mfma_f32_16x16x32_bf16 v[164:167],a[100:103],v[68:71],v[164:167]\n"
"ds_read_b128 a[80:83],v113 offset:20480\n"
"v_cvt_pk_bf16_f32 v89,v119,v120\n"
"v_mfma_f32_16x16x32_bf16 v[196:199],a[100:103],v[72:75],v[196:199]\n"
"v_exp_f32 v123,v53\n"
"v_mfma_f32_16x16x32_bf16 a[136:139],a[144:147],v[72:75],a[136:139]\n"
"ds_read_b128 a[84:87],v113 offset:21504\n"
"ds_read_b128 a[88:91],v113 offset:22528\n"
"v_mfma_f32_16x16x32_bf16 v[200:203],a[104:107],v[72:75],v[200:203]\n"
"v_exp_f32 v119,v54\n"
"v_mfma_f32_16x16x32_bf16 v[232:235],a[104:107],v[76:79],v[232:235]\n"
"ds_read_b128 a[92:95],v113 offset:23552\n"
"v_cvt_pk_bf16_f32 v90,v122,v123\n"
"v_mfma_f32_16x16x32_bf16 v[136:139],a[104:107],v[64:67],v[136:139]\n"
"v_exp_f32 v120,v55\n"
"v_mfma_f32_16x16x32_bf16 v[168:171],a[104:107],v[68:71],v[168:171]\n"
"s_waitcnt vmcnt(15)\n"
"ds_add_u32 v124,a152\n"
"v_mfma_f32_16x16x32_bf16 a[132:135],a[144:147],v[68:71],a[132:135]\n"
"v_exp_f32 v122,v56\n"
"v_mfma_f32_16x16x32_bf16 v[172:175],a[108:111],v[68:71],v[172:175]\n"
"v_cvt_pk_bf16_f32 v91,v119,v120\n"
"v_or3_b32 v118,v118,v87,v88\n"
"v_mfma_f32_16x16x32_bf16 v[204:207],a[108:111],v[72:75],v[204:207]\n"
"v_exp_f32 v123,v57\n"
"v_mfma_f32_16x16x32_bf16 v[236:239],a[108:111],v[76:79],v[236:239]\n"
"buffer_load_dwordx4 v117,s[24:27],s64 offen offset:0 sc1 lds\n"
"v_mfma_f32_16x16x32_bf16 v[140:143],a[108:111],v[64:67],v[140:143]\n"
"v_exp_f32 v119,v58\n"
"v_mfma_f32_16x16x32_bf16 a[128:131],a[144:147],v[64:67],a[128:131]\n"
"v_cvt_pk_bf16_f32 v92,v122,v123\n"
"v_or3_b32 v118,v118,v89,v90\n"
"v_mfma_f32_16x16x32_bf16 v[144:147],a[112:115],v[64:67],v[144:147]\n"
"v_exp_f32 v120,v59\n"
"v_mfma_f32_16x16x32_bf16 v[176:179],a[112:115],v[68:71],v[176:179]\n"
"buffer_load_dwordx4 v117,s[24:27],s64 offen offset:1024 sc1 lds\n"
"v_mfma_f32_16x16x32_bf16 v[208:211],a[112:115],v[72:75],v[208:211]\n"
"v_exp_f32 v122,v60\n"
"v_mfma_f32_16x16x32_bf16 v[240:243],a[112:115],v[76:79],v[240:243]\n"
"v_cvt_pk_bf16_f32 v93,v119,v120\n"
"v_or3_b32 v118,v118,v91,v92\n"
"v_mfma_f32_16x16x32_bf16 v[244:247],a[116:119],v[76:79],v[244:247]\n"
"v_exp_f32 v123,v61\n"
"v_mfma_f32_16x16x32_bf16 v[148:151],a[116:119],v[64:67],v[148:151]\n"
"buffer_load_dwordx4 v117,s[24:27],s64 offen offset:2048 sc1 lds\n"
"v_mfma_f32_16x16x32_bf16 v[180:183],a[116:119],v[68:71],v[180:183]\n"
"v_exp_f32 v119,v62\n"
"v_mfma_f32_16x16x32_bf16 v[212:215],a[116:119],v[72:75],v[212:215]\n"
"ds_read_b32 v125,v124\n"
"v_cvt_pk_bf16_f32 v94,v122,v123\n"
"v_mfma_f32_16x16x32_bf16 v[216:219],a[120:123],v[72:75],v[216:219]\n"
"v_exp_f32 v120,v63\n"
"v_mfma_f32_16x16x32_bf16 v[248:251],a[120:123],v[76:79],v[248:251]\n"
"buffer_load_dwordx4 v117,s[24:27],s64 offen offset:3072 sc1 lds\n"
"v_mfma_f32_16x16x32_bf16 v[152:155],a[120:123],v[64:67],v[152:155]\n"
"s_add_u32 s64,s64,16384\n"
"s_andn2_b32 m0,s55,16384\n"
"v_mfma_f32_16x16x32_bf16 v[184:187],a[120:123],v[68:71],v[184:187]\n"
"s_add_u32 s54,s54,s22\n"
"v_cvt_pk_bf16_f32 v95,v119,v120\n"
"v_mfma_f32_16x16x32_bf16 v[188:191],a[124:127],v[68:71],v[188:191]\n"
"s_waitcnt lgkmcnt(0)\n"
"v_subrev_u32 v125,s101,v125\n"
"v_or3_b32 v118,v118,v93,v94\n"
"v_mfma_f32_16x16x32_bf16 v[220:223],a[124:127],v[72:75],v[220:223]\n"
"v_or3_b32 v125,v118,v95,v125\n"
"v_and_b32 v125,0x40004000,v125\n"
"v_mfma_f32_16x16x32_bf16 v[252:255],a[124:127],v[76:79],v[252:255]\n"
"buffer_load_dwordx4 v115,s[32:35],s54 offen sc1 lds\n"
"buffer_store_dwordx4 a[240:243],v112,s[24:27],s56 offen offset:0 sc1\n"
"v_cmp_ne_u32 vcc,0,v125\n"
"v_mfma_f32_16x16x32_bf16 v[156:159],a[124:127],v[64:67],v[156:159]\n"
"s_cbranch_vccnz .Lfk_cs_g3\n"
".Lfk_cr_g3:\n"
"v_mfma_f32_16x16x32_bf16 v[32:35],a[64:67],a[0:3],v[96:99]\n"
"s_add_u32 s101,s101,4\n"
"v_exp_f32 v122,v0\n"
"v_mfma_f32_16x16x32_bf16 v[32:35],a[68:71],a[4:7],v[32:35]\n"
"s_add_u32 m0,s55,4096\n"
"v_exp_f32 v123,v1\n"
"v_mfma_f32_16x16x32_bf16 v[32:35],a[72:75],a[8:11],v[32:35]\n"
"ds_read_b128 a[96:99],v114 offset:57344\n"
"ds_read_b128 a[100:103],v114 offset:58368\n"
"v_mfma_f32_16x16x32_bf16 v[32:35],a[76:79],a[12:15],v[32:35]\n"
"s_add_u32 s54,s54,s22\n"
"v_exp_f32 v119,v2\n"
"v_mfma_f32_16x16x32_bf16 v[36:39],a[92:95],a[12:15],v[96:99]\n"
"ds_read_b128 a[104:107],v114 offset:59392\n"
"v_cvt_pk_bf16_f32 v64,v122,v123\n"
"v_mfma_f32_16x16x32_bf16 v[36:39],a[88:91],a[8:11],v[36:39]\n"
"v_exp_f32 v120,v3\n"
"v_mfma_f32_16x16x32_bf16 v[36:39],a[84:87],a[4:7],v[36:39]\n"
"ds_read_b128 a[108:111],v114 offset:60416\n"
"ds_read_b128 a[112:115],v114 offset:61440\n"
"v_mfma_f32_16x16x32_bf16 v[36:39],a[80:83],a[0:3],v[36:39]\n"
"v_exp_f32 v122,v4\n"
"v_mfma_f32_16x16x32_bf16 v[44:47],a[80:83],a[16:19],v[100:103]\n"
"ds_read_b128 a[116:119],v114 offset:62464\n"
"v_cvt_pk_bf16_f32 v65,v119,v120\n"
"v_mfma_f32_16x16x32_bf16 v[44:47],a[84:87],a[20:23],v[44:47]\n"
"v_exp_f32 v123,v5\n"
"v_mfma_f32_16x16x32_bf16 v[44:47],a[88:91],a[24:27],v[44:47]\n"
"ds_read_b128 a[120:123],v114 offset:63488\n"
"ds_read_b128 a[124:127],v114 offset:64512\n"
"v_mfma_f32_16x16x32_bf16 v[44:47],a[92:95],a[28:31],v[44:47]\n"
"v_exp_f32 v119,v6\n"
"v_mfma_f32_16x16x32_bf16 v[40:43],a[76:79],a[28:31],v[100:103]\n"
"v_cvt_pk_bf16_f32 v66,v122,v123\n"
"v_or3_b32 v118,v64,v65,v66\n"
"v_mfma_f32_16x16x32_bf16 v[40:43],a[72:75],a[24:27],v[40:43]\n"
"v_exp_f32 v120,v7\n"
"v_mfma_f32_16x16x32_bf16 v[40:43],a[68:71],a[20:23],v[40:43]\n"
"buffer_load_dwordx4 v115,s[32:35],s54 offen sc1 lds\n"
"v_mfma_f32_16x16x32_bf16 v[40:43],a[64:67],a[16:19],v[40:43]\n"
"s_add_u32 m0,s67,16384\n"
"v_exp_f32 v122,v8\n"
"v_mfma_f32_16x16x32_bf16 v[48:51],a[64:67],a[32:35],v[104:107]\n"
"v_cvt_pk_bf16_f32 v67,v119,v120\n"
"v_mfma_f32_16x16x32_bf16 v[48:51],a[68:71],a[36:39],v[48:51]\n"
"v_exp_f32 v123,v9\n"
"v_mfma_f32_16x16x32_bf16 v[48:51],a[72:75],a[40:43],v[48:51]\n"
"buffer_load_dwordx4 v112,s[24:27],s64 offen offset:0 sc1 lds\n"
"v_mfma_f32_16x16x32_bf16 v[48:51],a[76:79],a[44:47],v[48:51]\n"
"v_exp_f32 v119,v10\n"
"v_mfma_f32_16x16x32_bf16 v[52:55],a[92:95],a[44:47],v[104:107]\n"
"v_cvt_pk_bf16_f32 v68,v122,v123\n"
"v_or3_b32 v118,v118,v67,v68\n"
"v_mfma_f32_16x16x32_bf16 v[52:55],a[88:91],a[40:43],v[52:55]\n"
"v_exp_f32 v120,v11\n"
"v_mfma_f32_16x16x32_bf16 v[52:55],a[84:87],a[36:39],v[52:55]\n"
"buffer_load_dwordx4 v112,s[24:27],s64 offen offset:1024 sc1 lds\n"
"v_mfma_f32_16x16x32_bf16 v[52:55],a[80:83],a[32:35],v[52:55]\n"
"v_exp_f32 v122,v12\n"
"v_mfma_f32_16x16x32_bf16 v[60:63],a[80:83],a[48:51],v[108:111]\n"
"v_cvt_pk_bf16_f32 v69,v119,v120\n"
"v_mfma_f32_16x16x32_bf16 v[60:63],a[84:87],a[52:55],v[60:63]\n"
"v_exp_f32 v123,v13\n"
"v_mfma_f32_16x16x32_bf16 v[60:63],a[88:91],a[56:59],v[60:63]\n"
"buffer_load_dwordx4 v112,s[24:27],s64 offen offset:2048 sc1 lds\n"
"v_mfma_f32_16x16x32_bf16 v[60:63],a[92:95],a[60:63],v[60:63]\n"
"v_exp_f32 v119,v14\n"
"v_mfma_f32_16x16x32_bf16 v[56:59],a[76:79],a[60:63],v[108:111]\n"
"v_cvt_pk_bf16_f32 v70,v122,v123\n"
"v_or3_b32 v118,v118,v69,v70\n"
"v_mfma_f32_16x16x32_bf16 v[56:59],a[72:75],a[56:59],v[56:59]\n"
"v_exp_f32 v120,v15\n"
"v_mfma_f32_16x16x32_bf16 v[56:59],a[68:71],a[52:55],v[56:59]\n"
"buffer_load_dwordx4 v112,s[24:27],s64 offen offset:3072 sc1 lds\n"
"v_mfma_f32_16x16x32_bf16 v[56:59],a[64:67],a[48:51],v[56:59]\n"
"s_waitcnt lgkmcnt(0)\n"
"v_exp_f32 v122,v16\n"
"v_mfma_f32_16x16x32_bf16 v[128:131],a[96:99],v[80:83],v[128:131]\n"
"v_exp_f32 v123,v17\n"
"v_mfma_f32_16x16x32_bf16 v[160:163],a[96:99],v[84:87],v[160:163]\n"
"ds_read_b128 a[64:67],v113 offset:49152\n"
"v_cvt_pk_bf16_f32 v71,v119,v120\n"
"v_mfma_f32_16x16x32_bf16 v[192:195],a[96:99],v[88:91],v[192:195]\n"
"s_add_u32 m0,s67,20480\n"
"v_exp_f32 v119,v18\n"
"v_mfma_f32_16x16x32_bf16 v[224:227],a[96:99],v[92:95],v[224:227]\n"
"ds_read_b128 a[68:71],v113 offset:50176\n"
"v_cvt_pk_bf16_f32 v72,v122,v123\n"
"v_mfma_f32_16x16x32_bf16 a[140:143],a[144:147],v[92:95],a[140:143]\n"
"v_exp_f32 v120,v19\n"
"v_mfma_f32_16x16x32_bf16 v[228:231],a[100:103],v[92:95],v[228:231]\n"
"ds_read_b128 a[72:75],v113 offset:51200\n"
"ds_read_b128 a[76:79],v113 offset:52224\n"
"v_mfma_f32_16x16x32_bf16 v[132:135],a[100:103],v[80:83],v[132:135]\n"
"v_exp_f32 v122,v20\n"
"v_mfma_f32_16x16x32_bf16 v[164:167],a[100:103],v[84:87],v[164:167]\n"
"ds_read_b128 a[80:83],v113 offset:53248\n"
"v_cvt_pk_bf16_f32 v73,v119,v120\n"
"v_mfma_f32_16x16x32_bf16 v[196:199],a[100:103],v[88:91],v[196:199]\n"
"v_exp_f32 v123,v21\n"
"v_mfma_f32_16x16x32_bf16 a[136:139],a[144:147],v[88:91],a[136:139]\n"
"ds_read_b128 a[84:87],v113 offset:54272\n"
"ds_read_b128 a[88:91],v113 offset:55296\n"
"v_mfma_f32_16x16x32_bf16 v[200:203],a[104:107],v[88:91],v[200:203]\n"
"v_exp_f32 v119,v22\n"
"v_mfma_f32_16x16x32_bf16 v[232:235],a[104:107],v[92:95],v[232:235]\n"
"ds_read_b128 a[92:95],v113 offset:56320\n"
"v_cvt_pk_bf16_f32 v74,v122,v123\n"
"v_mfma_f32_16x16x32_bf16 v[136:139],a[104:107],v[80:83],v[136:139]\n"
"v_exp_f32 v120,v23\n"
"v_mfma_f32_16x16x32_bf16 v[168:171],a[104:107],v[84:87],v[168:171]\n"
"s_waitcnt vmcnt(16)\n"
"ds_add_u32 v124,a152\n"
"v_mfma_f32_16x16x32_bf16 a[132:135],a[144:147],v[84:87],a[132:135]\n"
"v_exp_f32 v122,v24\n"
"v_mfma_f32_16x16x32_bf16 v[172:175],a[108:111],v[84:87],v[172:175]\n"
"v_cvt_pk_bf16_f32 v75,v119,v120\n"
"v_or3_b32 v118,v118,v71,v72\n"
"v_mfma_f32_16x16x32_bf16 v[204:207],a[108:111],v[88:91],v[204:207]\n"
"v_exp_f32 v123,v25\n"
"v_mfma_f32_16x16x32_bf16 v[236:239],a[108:111],v[92:95],v[236:239]\n"
"buffer_load_dwordx4 v117,s[24:27],s64 offen offset:0 sc1 lds\n"
"v_mfma_f32_16x16x32_bf16 v[140:143],a[108:111],v[80:83],v[140:143]\n"
"v_exp_f32 v119,v26\n"
"v_mfma_f32_16x16x32_bf16 a[128:131],a[144:147],v[80:83],a[128:131]\n"
"v_cvt_pk_bf16_f32 v76,v122,v123\n"
"v_or3_b32 v118,v118,v73,v74\n"
"v_mfma_f32_16x16x32_bf16 v[144:147],a[112:115],v[80:83],v[144:147]\n"
"v_exp_f32 v120,v27\n"
"v_mfma_f32_16x16x32_bf16 v[176:179],a[112:115],v[84:87],v[176:179]\n"
"buffer_load_dwordx4 v117,s[24:27],s64 offen offset:1024 sc1 lds\n"
"v_mfma_f32_16x16x32_bf16 v[208:211],a[112:115],v[88:91],v[208:211]\n"
"v_exp_f32 v122,v28\n"
"v_mfma_f32_16x16x32_bf16 v[240:243],a[112:115],v[92:95],v[240:243]\n"
"v_cvt_pk_bf16_f32 v77,v119,v120\n"
"v_or3_b32 v118,v118,v75,v76\n"
"v_mfma_f32_16x16x32_bf16 v[244:247],a[116:119],v[92:95],v[244:247]\n"
"v_exp_f32 v123,v29\n"
"v_mfma_f32_16x16x32_bf16 v[148:151],a[116:119],v[80:83],v[148:151]\n"
"buffer_load_dwordx4 v117,s[24:27],s64 offen offset:2048 sc1 lds\n"
"v_mfma_f32_16x16x32_bf16 v[180:183],a[116:119],v[84:87],v[180:183]\n"
"v_exp_f32 v119,v30\n"
"v_mfma_f32_16x16x32_bf16 v[212:215],a[116:119],v[88:91],v[212:215]\n"
"ds_read_b32 v125,v124\n"
"v_cvt_pk_bf16_f32 v78,v122,v123\n"
"v_mfma_f32_16x16x32_bf16 v[216:219],a[120:123],v[88:91],v[216:219]\n"
"v_exp_f32 v120,v31\n"
"v_mfma_f32_16x16x32_bf16 v[248:251],a[120:123],v[92:95],v[248:251]\n"
"buffer_load_dwordx4 v117,s[24:27],s64 offen offset:3072 sc1 lds\n"
"v_mfma_f32_16x16x32_bf16 v[152:155],a[120:123],v[80:83],v[152:155]\n"
"buffer_store_dwordx4 a[244:247],v112,s[24:27],s56 offen offset:1024 sc1\n"
"v_mfma_f32_16x16x32_bf16 v[184:187],a[120:123],v[84:87],v[184:187]\n"
"s_add_u32 s64,s64,16384\n"
"v_cvt_pk_bf16_f32 v79,v119,v120\n"
"v_or3_b32 v118,v118,v77,v78\n"
"v_mfma_f32_16x16x32_bf16 v[188:191],a[124:127],v[84:87],v[188:191]\n"
"s_waitcnt lgkmcnt(0)\n"
"v_subrev_u32 v125,s101,v125\n"
"v_or3_b32 v125,v118,v79,v125\n"
"v_mfma_f32_16x16x32_bf16 v[220:223],a[124:127],v[88:91],v[220:223]\n"
"v_and_b32 v125,0x40004000,v125\n"
"v_cmp_ne_u32 vcc,0,v125\n"
"v_mfma_f32_16x16x32_bf16 v[252:255],a[124:127],v[92:95],v[252:255]\n"
"v_mfma_f32_16x16x32_bf16 v[156:159],a[124:127],v[80:83],v[156:159]\n"
"s_cbranch_vccnz .Lfk_cs_g0\n"
".Lfk_cr_g0:\n"
"v_mfma_f32_16x16x32_bf16 v[0:3],a[64:67],a[0:3],v[96:99]\n"
"s_add_u32 s101,s101,4\n"
"v_exp_f32 v122,v32\n"
"v_mfma_f32_16x16x32_bf16 v[0:3],a[68:71],a[4:7],v[0:3]\n"
"s_add_u32 m0,s55,8192\n"
"v_exp_f32 v123,v33\n"
"v_mfma_f32_16x16x32_bf16 v[0:3],a[72:75],a[8:11],v[0:3]\n"
"ds_read_b128 a[96:99],v114 offset:40960\n"
"ds_read_b128 a[100:103],v114 offset:41984\n"
"v_mfma_f32_16x16x32_bf16 v[0:3],a[76:79],a[12:15],v[0:3]\n"
"s_add_u32 s54,s54,s22\n"
"v_exp_f32 v119,v34\n"
"v_mfma_f32_16x16x32_bf16 v[4:7],a[92:95],a[12:15],v[96:99]\n"
"ds_read_b128 a[104:107],v114 offset:43008\n"
"v_cvt_pk_bf16_f32 v80,v122,v123\n"
"v_mfma_f32_16x16x32_bf16 v[4:7],a[88:91],a[8:11],v[4:7]\n"
"v_exp_f32 v120,v35\n"
"v_mfma_f32_16x16x32_bf16 v[4:7],a[84:87],a[4:7],v[4:7]\n"
"ds_read_b128 a[108:111],v114 offset:44032\n"
"ds_read_b128 a[112:115],v114 offset:45056\n"
"v_mfma_f32_16x16x32_bf16 v[4:7],a[80:83],a[0:3],v[4:7]\n"
"v_exp_f32 v122,v36\n"
"v_mfma_f32_16x16x32_bf16 v[12:15],a[80:83],a[16:19],v[100:103]\n"
"ds_read_b128 a[116:119],v114 offset:46080\n"
"v_cvt_pk_bf16_f32 v81,v119,v120\n"
"v_mfma_f32_16x16x32_bf16 v[12:15],a[84:87],a[20:23],v[12:15]\n"
"v_exp_f32 v123,v37\n"
"v_mfma_f32_16x16x32_bf16 v[12:15],a[88:91],a[24:27],v[12:15]\n"
"ds_read_b128 a[120:123],v114 offset:47104\n"
"ds_read_b128 a[124:127],v114 offset:48128\n"
"v_mfma_f32_16x16x32_bf16 v[12:15],a[92:95],a[28:31],v[12:15]\n"
"v_exp_f32 v119,v38\n"
"v_mfma_f32_16x16x32_bf16 v[8:11],a[76:79],a[28:31],v[100:103]\n"
"v_cvt_pk_bf16_f32 v82,v122,v123\n"
"v_or3_b32 v118,v80,v81,v82\n"
"v_mfma_f32_16x16x32_bf16 v[8:11],a[72:75],a[24:27],v[8:11]\n"
"v_exp_f32 v120,v39\n"
"v_mfma_f32_16x16x32_bf16 v[8:11],a[68:71],a[20:23],v[8:11]\n"
"buffer_load_dwordx4 v115,s[32:35],s54 offen sc1 lds\n"
"v_mfma_f32_16x16x32_bf16 v[8:11],a[64:67],a[16:19],v[8:11]\n"
"s_add_u32 m0,s67,49152\n"
"v_exp_f32 v122,v40\n"
"v_mfma_f32_16x16x32_bf16 v[16:19],a[64:67],a[32:35],v[104:107]\n"
"v_cvt_pk_bf16_f32 v83,v119,v120\n"
"v_mfma_f32_16x16x32_bf16 v[16:19],a[68:71],a[36:39],v[16:19]\n"
"v_exp_f32 v123,v41\n"
"v_mfma_f32_16x16x32_bf16 v[16:19],a[72:75],a[40:43],v[16:19]\n"
"buffer_load_dwordx4 v112,s[24:27],s64 offen offset:0 sc1 lds\n"
"v_mfma_f32_16x16x32_bf16 v[16:19],a[76:79],a[44:47],v[16:19]\n"
"v_exp_f32 v119,v42\n"
"v_mfma_f32_16x16x32_bf16 v[20:23],a[92:95],a[44:47],v[104:107]\n"
"v_cvt_pk_bf16_f32 v84,v122,v123\n"
"v_or3_b32 v118,v118,v83,v84\n"
"v_mfma_f32_16x16x32_bf16 v[20:23],a[88:91],a[40:43],v[20:23]\n"
"v_exp_f32 v120,v43\n"
"v_mfma_f32_16x16x32_bf16 v[20:23],a[84:87],a[36:39],v[20:23]\n"
"buffer_load_dwordx4 v112,s[24:27],s64 offen offset:1024 sc1 lds\n"
"v_mfma_f32_16x16x32_bf16 v[20:23],a[80:83],a[32:35],v[20:23]\n"
"v_exp_f32 v122,v44\n"
"v_mfma_f32_16x16x32_bf16 v[28:31],a[80:83],a[48:51],v[108:111]\n"
"v_cvt_pk_bf16_f32 v85,v119,v120\n"
"v_mfma_f32_16x16x32_bf16 v[28:31],a[84:87],a[52:55],v[28:31]\n"
"v_exp_f32 v123,v45\n"
"v_mfma_f32_16x16x32_bf16 v[28:31],a[88:91],a[56:59],v[28:31]\n"
"buffer_load_dwordx4 v112,s[24:27],s64 offen offset:2048 sc1 lds\n"
"v_mfma_f32_16x16x32_bf16 v[28:31],a[92:95],a[60:63],v[28:31]\n"
"v_exp_f32 v119,v46\n"
"v_mfma_f32_16x16x32_bf16 v[24:27],a[76:79],a[60:63],v[108:111]\n"
"v_cvt_pk_bf16_f32 v86,v122,v123\n"
"v_or3_b32 v118,v118,v85,v86\n"
"v_mfma_f32_16x16x32_bf16 v[24:27],a[72:75],a[56:59],v[24:27]\n"
"v_exp_f32 v120,v47\n"
"v_mfma_f32_16x16x32_bf16 v[24:27],a[68:71],a[52:55],v[24:27]\n"
"buffer_load_dwordx4 v112,s[24:27],s64 offen offset:3072 sc1 lds\n"
"v_mfma_f32_16x16x32_bf16 v[24:27],a[64:67],a[48:51],v[24:27]\n"
"s_waitcnt lgkmcnt(0)\n"
"v_exp_f32 v122,v48\n"
"v_mfma_f32_16x16x32_bf16 v[128:131],a[96:99],v[64:67],v[128:131]\n"
"v_exp_f32 v123,v49\n"
"v_mfma_f32_16x16x32_bf16 v[160:163],a[96:99],v[68:71],v[160:163]\n"
"ds_read_b128 a[64:67],v113 offset:32768\n"
"v_cvt_pk_bf16_f32 v87,v119,v120\n"
"v_mfma_f32_16x16x32_bf16 v[192:195],a[96:99],v[72:75],v[192:195]\n"
"s_add_u32 m0,s67,53248\n"
"v_exp_f32 v119,v50\n"
"v_mfma_f32_16x16x32_bf16 v[224:227],a[96:99],v[76:79],v[224:227]\n"
"ds_read_b128 a[68:71],v113 offset:33792\n"
"v_cvt_pk_bf16_f32 v88,v122,v123\n"
"v_mfma_f32_16x16x32_bf16 a[140:143],a[144:147],v[76:79],a[140:143]\n"
"v_exp_f32 v120,v51\n"
"v_mfma_f32_16x16x32_bf16 v[228:231],a[100:103],v[76:79],v[228:231]\n"
"ds_read_b128 a[72:75],v113 offset:34816\n"
"ds_read_b128 a[76:79],v113 offset:35840\n"
"v_mfma_f32_16x16x32_bf16 v[132:135],a[100:103],v[64:67],v[132:135]\n"
"v_exp_f32 v122,v52\n"
"v_mfma_f32_16x16x32_bf16 v[164:167],a[100:103],v[68:71],v[164:167]\n"
"ds_read_b128 a[80:83],v113 offset:36864\n"
"v_cvt_pk_bf16_f32 v89,v119,v120\n"
"v_mfma_f32_16x16x32_bf16 v[196:199],a[100:103],v[72:75],v[196:199]\n"
"v_exp_f32 v123,v53\n"
"v_mfma_f32_16x16x32_bf16 a[136:139],a[144:147],v[72:75],a[136:139]\n"
"ds_read_b128 a[84:87],v113 offset:37888\n"
"ds_read_b128 a[88:91],v113 offset:38912\n"
"v_mfma_f32_16x16x32_bf16 v[200:203],a[104:107],v[72:75],v[200:203]\n"
"v_exp_f32 v119,v54\n"
"v_mfma_f32_16x16x32_bf16 v[232:235],a[104:107],v[76:79],v[232:235]\n"
"ds_read_b128 a[92:95],v113 offset:39936\n"
"v_cvt_pk_bf16_f32 v90,v122,v123\n"
"v_mfma_f32_16x16x32_bf16 v[136:139],a[104:107],v[64:67],v[136:139]\n"
"v_exp_f32 v120,v55\n"
"v_mfma_f32_16x16x32_bf16 v[168:171],a[104:107],v[68:71],v[168:171]\n"
"s_waitcnt vmcnt(16)\n"
"ds_add_u32 v124,a152\n"
"v_mfma_f32_16x16x32_bf16 a[132:135],a[144:147],v[68:71],a[132:135]\n"
"v_exp_f32 v122,v56\n"
"v_mfma_f32_16x16x32_bf16 v[172:175],a[108:111],v[68:71],v[172:175]\n"
"v_cvt_pk_bf16_f32 v91,v119,v120\n"
"v_or3_b32 v118,v118,v87,v88\n"
"v_mfma_f32_16x16x32_bf16 v[204:207],a[108:111],v[72:75],v[204:207]\n"
"v_exp_f32 v123,v57\n"
"v_mfma_f32_16x16x32_bf16 v[236:239],a[108:111],v[76:79],v[236:239]\n"
"buffer_load_dwordx4 v117,s[24:27],s64 offen offset:0 sc1 lds\n"
"v_mfma_f32_16x16x32_bf16 v[140:143],a[108:111],v[64:67],v[140:143]\n"
"v_exp_f32 v119,v58\n"
"v_mfma_f32_16x16x32_bf16 a[128:131],a[144:147],v[64:67],a[128:131]\n"
"v_cvt_pk_bf16_f32 v92,v122,v123\n"
"v_or3_b32 v118,v118,v89,v90\n"
"v_mfma_f32_16x16x32_bf16 v[144:147],a[112:115],v[64:67],v[144:147]\n"
"v_exp_f32 v120,v59\n"
"v_mfma_f32_16x16x32_bf16 v[176:179],a[112:115],v[68:71],v[176:179]\n"
"buffer_load_dwordx4 v117,s[24:27],s64 offen offset:1024 sc1 lds\n"
"v_mfma_f32_16x16x32_bf16 v[208:211],a[112:115],v[72:75],v[208:211]\n"
"v_exp_f32 v122,v60\n"
"v_mfma_f32_16x16x32_bf16 v[240:243],a[112:115],v[76:79],v[240:243]\n"
"v_cvt_pk_bf16_f32 v93,v119,v120\n"
"v_or3_b32 v118,v118,v91,v92\n"
"v_mfma_f32_16x16x32_bf16 v[244:247],a[116:119],v[76:79],v[244:247]\n"
"v_exp_f32 v123,v61\n"
"v_mfma_f32_16x16x32_bf16 v[148:151],a[116:119],v[64:67],v[148:151]\n"
"buffer_load_dwordx4 v117,s[24:27],s64 offen offset:2048 sc1 lds\n"
"v_mfma_f32_16x16x32_bf16 v[180:183],a[116:119],v[68:71],v[180:183]\n"
"v_exp_f32 v119,v62\n"
"v_mfma_f32_16x16x32_bf16 v[212:215],a[116:119],v[72:75],v[212:215]\n"
"ds_read_b32 v125,v124\n"
"v_cvt_pk_bf16_f32 v94,v122,v123\n"
"v_mfma_f32_16x16x32_bf16 v[216:219],a[120:123],v[72:75],v[216:219]\n"
"v_exp_f32 v120,v63\n"
"v_mfma_f32_16x16x32_bf16 v[248:251],a[120:123],v[76:79],v[248:251]\n"
"buffer_load_dwordx4 v117,s[24:27],s64 offen offset:3072 sc1 lds\n"
"v_mfma_f32_16x16x32_bf16 v[152:155],a[120:123],v[64:67],v[152:155]\n"
"buffer_store_dwordx4 a[248:251],v112,s[24:27],s56 offen offset:2048 sc1\n"
"v_mfma_f32_16x16x32_bf16 v[184:187],a[120:123],v[68:71],v[184:187]\n"
"s_add_u32 s64,s64,16384\n"
"v_cvt_pk_bf16_f32 v95,v119,v120\n"
"v_or3_b32 v118,v118,v93,v94\n"
"v_mfma_f32_16x16x32_bf16 v[188:191],a[124:127],v[68:71],v[188:191]\n"
"s_waitcnt lgkmcnt(0)\n"
"v_subrev_u32 v125,s101,v125\n"
"v_or3_b32 v125,v118,v95,v125\n"
"v_mfma_f32_16x16x32_bf16 v[220:223],a[124:127],v[72:75],v[220:223]\n"
"v_and_b32 v125,0x40004000,v125\n"
"v_cmp_ne_u32 vcc,0,v125\n"
"v_mfma_f32_16x16x32_bf16 v[252:255],a[124:127],v[76:79],v[252:255]\n"
"v_mfma_f32_16x16x32_bf16 v[156:159],a[124:127],v[64:67],v[156:159]\n"
"s_cbranch_vccnz .Lfk_cs_g1\n"
".Lfk_cr_g1:\n"
"v_mfma_f32_16x16x32_bf16 v[32:35],a[64:67],a[0:3],v[96:99]\n"
"s_add_u32 s101,s101,4\n"
"v_exp_f32 v122,v0\n"
"v_mfma_f32_16x16x32_bf16 v[32:35],a[68:71],a[4:7],v[32:35]\n"
"s_add_u32 m0,s55,12288\n"
"v_exp_f32 v123,v1\n"
"v_mfma_f32_16x16x32_bf16 v[32:35],a[72:75],a[8:11],v[32:35]\n"
"ds_read_b128 a[96:99],v114 offset:8192\n"
"ds_read_b128 a[100:103],v114 offset:9216\n"
"v_mfma_f32_16x16x32_bf16 v[32:35],a[76:79],a[12:15],v[32:35]\n"
"s_add_u32 s54,s54,s22\n"
"v_exp_f32 v119,v2\n"
"v_mfma_f32_16x16x32_bf16 v[36:39],a[92:95],a[12:15],v[96:99]\n"
"ds_read_b128 a[104:107],v114 offset:10240\n"
"v_cvt_pk_bf16_f32 v64,v122,v123\n"
"v_mfma_f32_16x16x32_bf16 v[36:39],a[88:91],a[8:11],v[36:39]\n"
"v_exp_f32 v120,v3\n"
"v_mfma_f32_16x16x32_bf16 v[36:39],a[84:87],a[4:7],v[36:39]\n"
"ds_read_b128 a[108:111],v114 offset:11264\n"
"ds_read_b128 a[112:115],v114 offset:12288\n"
"v_mfma_f32_16x16x32_bf16 v[36:39],a[80:83],a[0:3],v[36:39]\n"
"v_exp_f32 v122,v4\n"
"v_mfma_f32_16x16x32_bf16 v[44:47],a[80:83],a[16:19],v[100:103]\n"
"ds_read_b128 a[116:119],v114 offset:13312\n"
"v_cvt_pk_bf16_f32 v65,v119,v120\n"
"v_mfma_f32_16x16x32_bf16 v[44:47],a[84:87],a[20:23],v[44:47]\n"
"v_exp_f32 v123,v5\n"
"v_mfma_f32_16x16x32_bf16 v[44:47],a[88:91],a[24:27],v[44:47]\n"
"ds_read_b128 a[120:123],v114 offset:14336\n"
"ds_read_b128 a[124:127],v114 offset:15360\n"
"v_mfma_f32_16x16x32_bf16 v[44:47],a[92:95],a[28:31],v[44:47]\n"
"v_exp_f32 v119,v6\n"
"v_mfma_f32_16x16x32_bf16 v[40:43],a[76:79],a[28:31],v[100:103]\n"
"v_cvt_pk_bf16_f32 v66,v122,v123\n"
"v_or3_b32 v118,v64,v65,v66\n"
"v_mfma_f32_16x16x32_bf16 v[40:43],a[72:75],a[24:27],v[40:43]\n"
"v_exp_f32 v120,v7\n"
"v_mfma_f32_16x16x32_bf16 v[40:43],a[68:71],a[20:23],v[40:43]\n"
"buffer_load_dwordx4 v115,s[32:35],s54 offen sc1 lds\n"
"v_mfma_f32_16x16x32_bf16 v[40:43],a[64:67],a[16:19],v[40:43]\n"
"s_cmp_eq_u32 s64,s65\n"
"v_exp_f32 v122,v8\n"
"v_mfma_f32_16x16x32_bf16 v[48:51],a[64:67],a[32:35],v[104:107]\n"
"v_cvt_pk_bf16_f32 v67,v119,v120\n"
"s_cselect_b32 s64,s66,s64\n"
"v_mfma_f32_16x16x32_bf16 v[48:51],a[68:71],a[36:39],v[48:51]\n"
"s_add_u32 m0,s67,32768\n"
"v_exp_f32 v123,v9\n"
"v_mfma_f32_16x16x32_bf16 v[48:51],a[72:75],a[40:43],v[48:51]\n"
"buffer_load_dwordx4 v112,s[24:27],s64 offen offset:0 sc1 lds\n"
"v_mfma_f32_16x16x32_bf16 v[48:51],a[76:79],a[44:47],v[48:51]\n"
"v_exp_f32 v119,v10\n"
"v_mfma_f32_16x16x32_bf16 v[52:55],a[92:95],a[44:47],v[104:107]\n"
"v_cvt_pk_bf16_f32 v68,v122,v123\n"
"v_or3_b32 v118,v118,v67,v68\n"
"v_mfma_f32_16x16x32_bf16 v[52:55],a[88:91],a[40:43],v[52:55]\n"
"v_exp_f32 v120,v11\n"
"v_mfma_f32_16x16x32_bf16 v[52:55],a[84:87],a[36:39],v[52:55]\n"
"buffer_load_dwordx4 v112,s[24:27],s64 offen offset:1024 sc1 lds\n"
"v_mfma_f32_16x16x32_bf16 v[52:55],a[80:83],a[32:35],v[52:55]\n"
"v_exp_f32 v122,v12\n"
"v_mfma_f32_16x16x32_bf16 v[60:63],a[80:83],a[48:51],v[108:111]\n"
"v_cvt_pk_bf16_f32 v69,v119,v120\n"
"v_mfma_f32_16x16x32_bf16 v[60:63],a[84:87],a[52:55],v[60:63]\n"
"v_exp_f32 v123,v13\n"
"v_mfma_f32_16x16x32_bf16 v[60:63],a[88:91],a[56:59],v[60:63]\n"
"buffer_load_dwordx4 v112,s[24:27],s64 offen offset:2048 sc1 lds\n"
"v_mfma_f32_16x16x32_bf16 v[60:63],a[92:95],a[60:63],v[60:63]\n"
"v_exp_f32 v119,v14\n"
"v_mfma_f32_16x16x32_bf16 v[56:59],a[76:79],a[60:63],v[108:111]\n"
"v_cvt_pk_bf16_f32 v70,v122,v123\n"
"v_or3_b32 v118,v118,v69,v70\n"
"v_mfma_f32_16x16x32_bf16 v[56:59],a[72:75],a[56:59],v[56:59]\n"
"v_exp_f32 v120,v15\n"
"v_mfma_f32_16x16x32_bf16 v[56:59],a[68:71],a[52:55],v[56:59]\n"
"buffer_load_dwordx4 v112,s[24:27],s64 offen offset:3072 sc1 lds\n"
"v_mfma_f32_16x16x32_bf16 v[56:59],a[64:67],a[48:51],v[56:59]\n"
"s_waitcnt lgkmcnt(0)\n"
"v_exp_f32 v122,v16\n"
"v_mfma_f32_16x16x32_bf16 v[128:131],a[96:99],v[80:83],v[128:131]\n"
"v_exp_f32 v123,v17\n"
"v_mfma_f32_16x16x32_bf16 v[160:163],a[96:99],v[84:87],v[160:163]\n"
"ds_read_b128 a[64:67],v113 offset:0\n"
"v_cvt_pk_bf16_f32 v71,v119,v120\n"
"v_mfma_f32_16x16x32_bf16 v[192:195],a[96:99],v[88:91],v[192:195]\n"
"s_add_u32 m0,s67,36864\n"
"v_exp_f32 v119,v18\n"
"v_mfma_f32_16x16x32_bf16 v[224:227],a[96:99],v[92:95],v[224:227]\n"
"ds_read_b128 a[68:71],v113 offset:1024\n"
"v_cvt_pk_bf16_f32 v72,v122,v123\n"
"v_mfma_f32_16x16x32_bf16 a[140:143],a[144:147],v[92:95],a[140:143]\n"
"v_exp_f32 v120,v19\n"
"v_mfma_f32_16x16x32_bf16 v[228:231],a[100:103],v[92:95],v[228:231]\n"
"ds_read_b128 a[72:75],v113 offset:2048\n"
"ds_read_b128 a[76:79],v113 offset:3072\n"
"v_mfma_f32_16x16x32_bf16 v[132:135],a[100:103],v[80:83],v[132:135]\n"
"v_exp_f32 v122,v20\n"
"v_mfma_f32_16x16x32_bf16 v[164:167],a[100:103],v[84:87],v[164:167]\n"
"ds_read_b128 a[80:83],v113 offset:4096\n"
"v_cvt_pk_bf16_f32 v73,v119,v120\n"
"v_mfma_f32_16x16x32_bf16 v[196:199],a[100:103],v[88:91],v[196:199]\n"
"v_exp_f32 v123,v21\n"
"v_mfma_f32_16x16x32_bf16 a[136:139],a[144:147],v[88:91],a[136:139]\n"
"ds_read_b128 a[84:87],v113 offset:5120\n"
"ds_read_b128 a[88:91],v113 offset:6144\n"
"v_mfma_f32_16x16x32_bf16 v[200:203],a[104:107],v[88:91],v[200:203]\n"
"v_exp_f32 v119,v22\n"
"v_mfma_f32_16x16x32_bf16 v[232:235],a[104:107],v[92:95],v[232:235]\n"
"ds_read_b128 a[92:95],v113 offset:7168\n"
"v_cvt_pk_bf16_f32 v74,v122,v123\n"
"v_mfma_f32_16x16x32_bf16 v[136:139],a[104:107],v[80:83],v[136:139]\n"
"v_exp_f32 v120,v23\n"
"v_mfma_f32_16x16x32_bf16 v[168:171],a[104:107],v[84:87],v[168:171]\n"
"s_waitcnt vmcnt(16)\n"
"ds_add_u32 v124,a152\n"
"v_mfma_f32_16x16x32_bf16 a[132:135],a[144:147],v[84:87],a[132:135]\n"
"v_exp_f32 v122,v24\n"
"v_mfma_f32_16x16x32_bf16 v[172:175],a[108:111],v[84:87],v[172:175]\n"
"v_cvt_pk_bf16_f32 v75,v119,v120\n"
"v_or3_b32 v118,v118,v71,v72\n"
"v_mfma_f32_16x16x32_bf16 v[204:207],a[108:111],v[88:91],v[204:207]\n"
"v_exp_f32 v123,v25\n"
"v_mfma_f32_16x16x32_bf16 v[236:239],a[108:111],v[92:95],v[236:239]\n"
"buffer_load_dwordx4 v117,s[24:27],s64 offen offset:0 sc1 lds\n"
"v_mfma_f32_16x16x32_bf16 v[140:143],a[108:111],v[80:83],v[140:143]\n"
"v_exp_f32 v119,v26\n"
"v_mfma_f32_16x16x32_bf16 a[128:131],a[144:147],v[80:83],a[128:131]\n"
"v_cvt_pk_bf16_f32 v76,v122,v123\n"
"v_or3_b32 v118,v118,v73,v74\n"
"v_mfma_f32_16x16x32_bf16 v[144:147],a[112:115],v[80:83],v[144:147]\n"
"v_exp_f32 v120,v27\n"
"v_mfma_f32_16x16x32_bf16 v[176:179],a[112:115],v[84:87],v[176:179]\n"
"buffer_load_dwordx4 v117,s[24:27],s64 offen offset:1024 sc1 lds\n"
"v_mfma_f32_16x16x32_bf16 v[208:211],a[112:115],v[88:91],v[208:211]\n"
"v_exp_f32 v122,v28\n"
"v_mfma_f32_16x16x32_bf16 v[240:243],a[112:115],v[92:95],v[240:243]\n"
"v_cvt_pk_bf16_f32 v77,v119,v120\n"
"v_or3_b32 v118,v118,v75,v76\n"
"v_mfma_f32_16x16x32_bf16 v[244:247],a[116:119],v[92:95],v[244:247]\n"
"v_exp_f32 v123,v29\n"
"v_mfma_f32_16x16x32_bf16 v[148:151],a[116:119],v[80:83],v[148:151]\n"
"buffer_load_dwordx4 v117,s[24:27],s64 offen offset:2048 sc1 lds\n"
"v_mfma_f32_16x16x32_bf16 v[180:183],a[116:119],v[84:87],v[180:183]\n"
"v_exp_f32 v119,v30\n"
"v_mfma_f32_16x16x32_bf16 v[212:215],a[116:119],v[88:91],v[212:215]\n"
"ds_read_b32 v125,v124\n"
"v_cvt_pk_bf16_f32 v78,v122,v123\n"
"v_mfma_f32_16x16x32_bf16 v[216:219],a[120:123],v[88:91],v[216:219]\n"
"v_exp_f32 v120,v31\n"
"v_mfma_f32_16x16x32_bf16 v[248:251],a[120:123],v[92:95],v[248:251]\n"
"buffer_load_dwordx4 v117,s[24:27],s64 offen offset:3072 sc1 lds\n"
"v_mfma_f32_16x16x32_bf16 v[152:155],a[120:123],v[80:83],v[152:155]\n"
"buffer_store_dwordx4 a[252:255],v112,s[24:27],s56 offen offset:3072 sc1\n"
"v_mfma_f32_16x16x32_bf16 v[184:187],a[120:123],v[84:87],v[184:187]\n"
"s_add_u32 s64,s64,16384\n"
"s_add_u32 s89,s89,1\n"
"v_cvt_pk_bf16_f32 v79,v119,v120\n"
"v_mfma_f32_16x16x32_bf16 v[188:191],a[124:127],v[84:87],v[188:191]\n"
"s_waitcnt lgkmcnt(0)\n"
"v_subrev_u32 v125,s101,v125\n"
"v_or3_b32 v118,v118,v77,v78\n"
"v_mfma_f32_16x16x32_bf16 v[220:223],a[124:127],v[88:91],v[220:223]\n"
"v_or3_b32 v125,v118,v79,v125\n"
"v_and_b32 v125,0x40004000,v125\n"
"v_mfma_f32_16x16x32_bf16 v[252:255],a[124:127],v[92:95],v[252:255]\n"
"v_cmp_ne_u32 vcc,0,v125\n"
"v_mfma_f32_16x16x32_bf16 v[156:159],a[124:127],v[80:83],v[156:159]\n"
"s_cbranch_vccnz .Lfk_cs_g2\n"
".Lfk_cr_g2:\n"
".Lfk_Hx:\n"
"s_cmp_lt_u32 s89,8\n"
"s_cbranch_scc1 .Lfk_H\n"
"v_max3_f32 v118,v32,v33,v34\n"
"v_max3_f32 v119,v35,v36,v37\n"
"v_max3_f32 v119,v119,v38,v39\n"
"v_max3_f32 v118,v118,v40,v41\n"
"v_max3_f32 v119,v119,v42,v43\n"
"v_max3_f32 v118,v118,v44,v45\n"
"v_max3_f32 v119,v119,v46,v47\n"
"v_max3_f32 v118,v118,v48,v49\n"
"v_max3_f32 v119,v119,v50,v51\n"
"v_max3_f32 v118,v118,v52,v53\n"
"v_max3_f32 v119,v119,v54,v55\n"
"v_max3_f32 v118,v118,v56,v57\n"
"v_max3_f32 v119,v119,v58,v59\n"
"v_max3_f32 v118,v118,v60,v61\n"
"v_max3_f32 v119,v119,v62,v63\n"
"v_max_f32 v118,v118,v119\n"
"v_cmp_lt_f32 vcc,s93,v118\n"
"s_nop 0\n"
"s_cbranch_vccnz .Lfk_cs_x1\n"
".Lfk_cr_x1:\n"
"s_cmp_ge_u32 s52,2\n"
"s_cbranch_scc1 .Lfk_tailB\n"
".Lfk_tailA:\n"
"s_add_u32 s82,s66,28672\n"
"s_cmp_eq_u32 s52,0\n"
"s_cselect_b32 s67,77824,s67\n"
"s_cselect_b32 s64,s82,s64\n"
"s_waitcnt vmcnt(11) lgkmcnt(0)\n"
"s_barrier\n"
"v_mfma_f32_16x16x32_bf16 v[16:19],a[64:67],a[32:35],v[104:107]\n"
"ds_read_b128 a[96:99],v114 offset:24576\n"
"ds_read_b128 a[100:103],v114 offset:25600\n"
"ds_read_b128 a[104:107],v114 offset:26624\n"
"v_mfma_f32_16x16x32_bf16 v[16:19],a[68:71],a[36:39],v[16:19]\n"
"ds_read_b128 a[108:111],v114 offset:27648\n"
"ds_read_b128 a[112:115],v114 offset:28672\n"
"ds_read_b128 a[116:119],v114 offset:29696\n"
"v_mfma_f32_16x16x32_bf16 v[16:19],a[72:75],a[40:43],v[16:19]\n"
"ds_read_b128 a[120:123],v114 offset:30720\n"
"ds_read_b128 a[124:127],v114 offset:31744\n"
"v_add_f32 v8,0x80000000,v126\n"
"v_mfma_f32_16x16x32_bf16 v[16:19],a[76:79],a[44:47],v[16:19]\n"
"v_min_f32 v8,v8,v100\n"
"v_add_f32 v9,0xf149f2ca,v126\n"
"v_min_f32 v9,v9,v101\n"
"v_mfma_f32_16x16x32_bf16 v[20:23],a[92:95],a[44:47],v[104:107]\n"
"v_add_f32 v10,0xf1c9f2ca,v126\n"
"v_min_f32 v10,v10,v102\n"
"v_add_f32 v11,0xf2177617,v126\n"
"v_mfma_f32_16x16x32_bf16 v[20:23],a[88:91],a[40:43],v[20:23]\n"
"v_min_f32 v11,v11,v103\n"
"v_add_f32 v12,0xf349f2ca,v126\n"
"v_min_f32 v12,v12,v100\n"
"v_mfma_f32_16x16x32_bf16 v[20:23],a[84:87],a[36:39],v[20:23]\n"
"v_add_f32 v13,0xf35691f6,v126\n"
"v_min_f32 v13,v13,v101\n"
"v_add_f32 v14,0xf3633123,v126\n"
"v_mfma_f32_16x16x32_bf16 v[20:23],a[80:83],a[32:35],v[20:23]\n"
"v_min_f32 v14,v14,v102\n"
"v_add_f32 v15,0xf36fd050,v126\n"
"v_min_f32 v15,v15,v103\n"
"v_mfma_f32_16x16x32_bf16 v[28:31],a[80:83],a[48:51],v[108:111]\n"
"s_add_u32 m0,s67,0\n"
"v_exp_f32 v32,v32\n"
"v_mfma_f32_16x16x32_bf16 v[28:31],a[84:87],a[52:55],v[28:31]\n"
"buffer_load_dwordx4 v112,s[24:27],s64 offen offset:0 sc1 lds\n"
"buffer_load_dwordx4 v112,s[24:27],s64 offen offset:1024 sc1 lds\n"
"v_exp_f32 v33,v33\n"
"v_mfma_f32_16x16x32_bf16 v[28:31],a[88:91],a[56:59],v[28:31]\n"
"v_exp_f32 v34,v34\n"
"v_cvt_pk_bf16_f32 v80,v32,v33\n"
"v_mfma_f32_16x16x32_bf16 v[28:31],a[92:95],a[60:63],v[28:31]\n"
"v_exp_f32 v35,v35\n"
"v_max3_f32 v118,v16,v17,v18\n"
"v_exp_f32 v36,v36\n"
"v_mfma_f32_16x16x32_bf16 v[24:27],a[76:79],a[60:63],v[108:111]\n"
"buffer_load_dwordx4 v112,s[24:27],s64 offen offset:2048 sc1 lds\n"
"v_mfma_f32_16x16x32_bf16 v[24:27],a[72:75],a[56:59],v[24:27]\n"
"v_exp_f32 v37,v37\n"
"v_cvt_pk_bf16_f32 v81,v34,v35\n"
"v_mfma_f32_16x16x32_bf16 v[24:27],a[68:71],a[52:55],v[24:27]\n"
"v_exp_f32 v38,v38\n"
"v_cvt_pk_bf16_f32 v82,v36,v37\n"
"v_exp_f32 v39,v39\n"
"v_mfma_f32_16x16x32_bf16 v[24:27],a[64:67],a[48:51],v[24:27]\n"
"buffer_load_dwordx4 v112,s[24:27],s64 offen offset:3072 sc1 lds\n"
"v_mfma_f32_16x16x32_bf16 v[8:11],a[64:67],a[16:19],v[8:11]\n"
"s_add_u32 s64,s64,16384\n"
"v_exp_f32 v40,v40\n"
"v_accvgpr_read_b32 v119,a150\n"
"v_mfma_f32_16x16x32_bf16 v[8:11],a[68:71],a[20:23],v[8:11]\n"
"v_exp_f32 v41,v41\n"
"v_accvgpr_read_b32 v120,a151\n"
"v_mfma_f32_16x16x32_bf16 v[8:11],a[72:75],a[24:27],v[8:11]\n"
"v_exp_f32 v42,v42\n"
"v_xor_b32 v119,s80,v119\n"
"v_mfma_f32_16x16x32_bf16 v[8:11],a[76:79],a[28:31],v[8:11]\n"
"v_exp_f32 v43,v43\n"
"v_xor_b32 v120,s81,v120\n"
"v_mfma_f32_16x16x32_bf16 v[12:15],a[92:95],a[28:31],v[12:15]\n"
"v_exp_f32 v44,v44\n"
"v_or_b32 v119,v119,v120\n"
"v_mfma_f32_16x16x32_bf16 v[12:15],a[88:91],a[24:27],v[12:15]\n"
"s_add_u32 s18,s8,0\n"
"v_exp_f32 v45,v45\n"
"v_cmp_ne_u32 s[14:15],0,v119\n"
"v_mfma_f32_16x16x32_bf16 v[12:15],a[84:87],a[20:23],v[12:15]\n"
"s_cmp_lg_u32 s14,0\n"
"s_cselect_b32 s18,2146435072,s18\n"
"v_exp_f32 v46,v46\n"
"v_mfma_f32_16x16x32_bf16 v[12:15],a[80:83],a[16:19],v[12:15]\n"
"s_add_u32 m0,s30,0\n"
"ds_read_b128 a[64:67],v113 offset:16384\n"
"s_waitcnt lgkmcnt(1)\n"
"v_mfma_f32_16x16x32_bf16 v[128:131],a[96:99],v[64:67],v[128:131]\n"
"ds_read_b128 a[68:71],v113 offset:17408\n"
"ds_read_b128 a[72:75],v113 offset:18432\n"
"ds_read_b128 a[76:79],v113 offset:19456\n"
"v_mfma_f32_16x16x32_bf16 v[160:163],a[96:99],v[68:71],v[160:163]\n"
"ds_read_b128 a[80:83],v113 offset:20480\n"
"v_exp_f32 v47,v47\n"
"v_mfma_f32_16x16x32_bf16 v[192:195],a[96:99],v[72:75],v[192:195]\n"
"ds_read_b128 a[84:87],v113 offset:21504\n"
"ds_read_b128 a[88:91],v113 offset:22528\n"
"ds_read_b128 a[92:95],v113 offset:23552\n"
"v_exp_f32 v48,v48\n"
"v_mfma_f32_16x16x32_bf16 v[224:227],a[96:99],v[76:79],v[224:227]\n"
"buffer_load_dwordx4 v112,s[24:27],s18 offen offset:0 sc1 lds\n"
"v_exp_f32 v49,v49\n"
"v_mfma_f32_16x16x32_bf16 a[140:143],a[144:147],v[76:79],a[140:143]\n"
"buffer_load_dwordx4 v112,s[24:27],s18 offen offset:1024 sc1 lds\n"
"v_mfma_f32_16x16x32_bf16 v[228:231],a[100:103],v[76:79],v[228:231]\n"
"buffer_load_dwordx4 v112,s[24:27],s18 offen offset:2048 sc1 lds\n"
"buffer_load_dwordx4 v112,s[24:27],s18 offen offset:3072 sc1 lds\n"
"v_mfma_f32_16x16x32_bf16 v[132:135],a[100:103],v[64:67],v[132:135]\n"
"s_add_u32 m0,m0,4096\n"
"v_exp_f32 v50,v50\n"
"v_cvt_pk_bf16_f32 v83,v38,v39\n"
"v_mfma_f32_16x16x32_bf16 v[164:167],a[100:103],v[68:71],v[164:167]\n"
"buffer_load_dwordx4 v117,s[24:27],s18 offen offset:0 sc1 lds\n"
"v_cvt_pk_bf16_f32 v84,v40,v41\n"
"v_cvt_pk_bf16_f32 v85,v42,v43\n"
"v_mfma_f32_16x16x32_bf16 v[196:199],a[100:103],v[72:75],v[196:199]\n"
"v_exp_f32 v51,v51\n"
"v_cvt_pk_bf16_f32 v86,v44,v45\n"
"v_mfma_f32_16x16x32_bf16 a[136:139],a[144:147],v[72:75],a[136:139]\n"
"v_exp_f32 v52,v52\n"
"v_cvt_pk_bf16_f32 v87,v46,v47\n"
"v_mfma_f32_16x16x32_bf16 v[200:203],a[104:107],v[72:75],v[200:203]\n"
"buffer_load_dwordx4 v117,s[24:27],s18 offen offset:1024 sc1 lds\n"
"v_cvt_pk_bf16_f32 v88,v48,v49\n"
"v_cvt_pk_bf16_f32 v89,v50,v51\n"
"v_mfma_f32_16x16x32_bf16 v[232:235],a[104:107],v[76:79],v[232:235]\n"
"v_exp_f32 v53,v53\n"
"v_max3_f32 v118,v118,v19,v20\n"
"v_mfma_f32_16x16x32_bf16 v[136:139],a[104:107],v[64:67],v[136:139]\n"
"v_cvt_pk_bf16_f32 v90,v52,v53\n"
"v_max3_f32 v118,v118,v21,v22\n"
"v_max3_f32 v118,v118,v23,v28\n"
"v_exp_f32 v54,v54\n"
"v_mfma_f32_16x16x32_bf16 v[168:171],a[104:107],v[68:71],v[168:171]\n"
"buffer_load_dwordx4 v117,s[24:27],s18 offen offset:2048 sc1 lds\n"
"v_mfma_f32_16x16x32_bf16 a[132:135],a[144:147],v[68:71],a[132:135]\n"
"v_exp_f32 v55,v55\n"
"v_max3_f32 v118,v118,v29,v30\n"
"v_mfma_f32_16x16x32_bf16 v[172:175],a[108:111],v[68:71],v[172:175]\n"
"v_cvt_pk_bf16_f32 v91,v54,v55\n"
"v_max3_f32 v118,v118,v31,v24\n"
"v_max3_f32 v118,v118,v25,v26\n"
"v_exp_f32 v56,v56\n"
"v_mfma_f32_16x16x32_bf16 v[204:207],a[108:111],v[72:75],v[204:207]\n"
"buffer_load_dwordx4 v117,s[24:27],s18 offen offset:3072 sc1 lds\n"
"v_mfma_f32_16x16x32_bf16 v[236:239],a[108:111],v[76:79],v[236:239]\n"
"s_sub_u32 s82,s3,32768\n"
"v_max3_f32 v118,v118,v27,v8\n"
"v_max3_f32 v118,v118,v9,v10\n"
"v_mfma_f32_16x16x32_bf16 v[140:143],a[108:111],v[64:67],v[140:143]\n"
"v_exp_f32 v57,v57\n"
"v_max3_f32 v118,v118,v11,v12\n"
"v_exp_f32 v58,v58\n"
"v_mfma_f32_16x16x32_bf16 a[128:131],a[144:147],v[64:67],a[128:131]\n"
"buffer_load_dwordx4 a[0:3],v112,s[24:27],s82 offen offset:0 sc1\n"
"v_mfma_f32_16x16x32_bf16 v[144:147],a[112:115],v[64:67],v[144:147]\n"
"v_max3_f32 v118,v118,v13,v14\n"
"v_max_f32 v118,v118,v15\n"
"v_mfma_f32_16x16x32_bf16 v[176:179],a[112:115],v[68:71],v[176:179]\n"
"v_exp_f32 v59,v59\n"
"v_mfma_f32_16x16x32_bf16 v[208:211],a[112:115],v[72:75],v[208:211]\n"
"v_cvt_pk_bf16_f32 v92,v56,v57\n"
"v_cvt_pk_bf16_f32 v93,v58,v59\n"
"v_exp_f32 v60,v60\n"
"v_mfma_f32_16x16x32_bf16 v[240:243],a[112:115],v[76:79],v[240:243]\n"
"buffer_load_dwordx4 a[4:7],v112,s[24:27],s82 offen offset:1024 sc1\n"
"v_mfma_f32_16x16x32_bf16 v[244:247],a[116:119],v[76:79],v[244:247]\n"
"v_exp_f32 v61,v61\n"
"v_mfma_f32_16x16x32_bf16 v[148:151],a[116:119],v[64:67],v[148:151]\n"
"v_cmp_lt_f32 vcc,s93,v118\n"
"v_cvt_pk_bf16_f32 v94,v60,v61\n"
"v_mfma_f32_16x16x32_bf16 v[180:183],a[116:119],v[68:71],v[180:183]\n"
"buffer_load_dwordx4 a[8:11],v112,s[24:27],s82 offen offset:2048 sc1\n"
"v_mfma_f32_16x16x32_bf16 v[212:215],a[116:119],v[72:75],v[212:215]\n"
"v_exp_f32 v62,v62\n"
"v_mfma_f32_16x16x32_bf16 v[216:219],a[120:123],v[72:75],v[216:219]\n"
"v_exp_f32 v63,v63\n"
"v_mfma_f32_16x16x32_bf16 v[248:251],a[120:123],v[76:79],v[248:251]\n"
"v_cvt_pk_bf16_f32 v95,v62,v63\n"
"v_mfma_f32_16x16x32_bf16 v[152:155],a[120:123],v[64:67],v[152:155]\n"
"buffer_load_dwordx4 a[12:15],v112,s[24:27],s82 offen offset:3072 sc1\n"
"v_mfma_f32_16x16x32_bf16 v[184:187],a[120:123],v[68:71],v[184:187]\n"
"v_mfma_f32_16x16x32_bf16 v[188:191],a[124:127],v[68:71],v[188:191]\n"
"v_mfma_f32_16x16x32_bf16 v[220:223],a[124:127],v[72:75],v[220:223]\n"
"v_mfma_f32_16x16x32_bf16 v[252:255],a[124:127],v[76:79],v[252:255]\n"
"v_mfma_f32_16x16x32_bf16 v[156:159],a[124:127],v[64:67],v[156:159]\n"
"s_cbranch_vccnz .Lfk_cs_a3\n"
".Lfk_cr_a3:\n"
"s_waitcnt vmcnt(17) lgkmcnt(0)\n"
"s_barrier\n"
"v_mfma_f32_16x16x32_bf16 v[56:59],a[64:67],a[48:51],v[108:111]\n"
"ds_read_b128 a[96:99],v114 offset:57344\n"
"ds_read_b128 a[100:103],v114 offset:58368\n"
"ds_read_b128 a[104:107],v114 offset:59392\n"
"v_mfma_f32_16x16x32_bf16 v[56:59],a[68:71],a[52:55],v[56:59]\n"
"ds_read_b128 a[108:111],v114 offset:60416\n"
"ds_read_b128 a[112:115],v114 offset:61440\n"
"ds_read_b128 a[116:119],v114 offset:62464\n"
"v_mfma_f32_16x16x32_bf16 v[56:59],a[72:75],a[56:59],v[56:59]\n"
"ds_read_b128 a[120:123],v114 offset:63488\n"
"ds_read_b128 a[124:127],v114 offset:64512\n"
"v_add_f32 v48,0x80000000,v126\n"
"v_mfma_f32_16x16x32_bf16 v[56:59],a[76:79],a[60:63],v[56:59]\n"
"v_min_f32 v48,v48,v104\n"
"v_add_f32 v49,0xf149f2ca,v126\n"
"v_min_f32 v49,v49,v105\n"
"v_mfma_f32_16x16x32_bf16 v[60:63],a[92:95],a[60:63],v[108:111]\n"
"v_add_f32 v50,0xf1c9f2ca,v126\n"
"v_add_f32 v51,0xf2177617,v126\n"
"v_mfma_f32_16x16x32_bf16 v[60:63],a[88:91],a[56:59],v[60:63]\n"
"v_add_f32 v52,0xf349f2ca,v126\n"
"v_min_f32 v52,v52,v104\n"
"v_mfma_f32_16x16x32_bf16 v[60:63],a[84:87],a[52:55],v[60:63]\n"
"v_add_f32 v53,0xf35691f6,v126\n"
"v_min_f32 v53,v53,v105\n"
"v_add_f32 v54,0xf3633123,v126\n"
"v_mfma_f32_16x16x32_bf16 v[60:63],a[80:83],a[48:51],v[60:63]\n"
"v_min_f32 v54,v54,v106\n"
"v_add_f32 v55,0xf36fd050,v126\n"
"v_min_f32 v55,v55,v107\n"
"v_min_f32 v51,v51,v107\n"
"v_min_f32 v50,v50,v106\n"
"v_mfma_f32_16x16x32_bf16 v[52:55],a[80:83],a[32:35],v[52:55]\n"
"s_add_u32 m0,s67,16384\n"
"v_exp_f32 v8,v8\n"
"v_mfma_f32_16x16x32_bf16 v[52:55],a[84:87],a[36:39],v[52:55]\n"
"buffer_load_dwordx4 v112,s[24:27],s64 offen offset:0 sc1 lds\n"
"buffer_load_dwordx4 v112,s[24:27],s64 offen offset:1024 sc1 lds\n"
"v_exp_f32 v9,v9\n"
"v_mfma_f32_16x16x32_bf16 v[52:55],a[88:91],a[40:43],v[52:55]\n"
"v_exp_f32 v10,v10\n"
"v_cvt_pk_bf16_f32 v68,v8,v9\n"
"v_mfma_f32_16x16x32_bf16 v[52:55],a[92:95],a[44:47],v[52:55]\n"
"v_exp_f32 v11,v11\n"
"v_max3_f32 v118,v56,v57,v58\n"
"v_exp_f32 v12,v12\n"
"v_mfma_f32_16x16x32_bf16 v[48:51],a[76:79],a[44:47],v[48:51]\n"
"buffer_load_dwordx4 v112,s[24:27],s64 offen offset:2048 sc1 lds\n"
"v_mfma_f32_16x16x32_bf16 v[48:51],a[72:75],a[40:43],v[48:51]\n"
"v_exp_f32 v13,v13\n"
"v_cvt_pk_bf16_f32 v69,v10,v11\n"
"v_mfma_f32_16x16x32_bf16 v[48:51],a[68:71],a[36:39],v[48:51]\n"
"v_exp_f32 v14,v14\n"
"v_cvt_pk_bf16_f32 v70,v12,v13\n"
"v_exp_f32 v15,v15\n"
"v_mfma_f32_16x16x32_bf16 v[48:51],a[64:67],a[32:35],v[48:51]\n"
"ds_read_b128 a[64:67],v113 offset:49152\n"
"s_waitcnt lgkmcnt(1)\n"
"v_mfma_f32_16x16x32_bf16 v[128:131],a[96:99],v[80:83],v[128:131]\n"
"ds_read_b128 a[68:71],v113 offset:50176\n"
"v_exp_f32 v16,v16\n"
"v_mfma_f32_16x16x32_bf16 v[160:163],a[96:99],v[84:87],v[160:163]\n"
"ds_read_b128 a[72:75],v113 offset:51200\n"
"v_exp_f32 v17,v17\n"
"v_mfma_f32_16x16x32_bf16 v[192:195],a[96:99],v[88:91],v[192:195]\n"
"ds_read_b128 a[76:79],v113 offset:52224\n"
"ds_read_b128 a[80:83],v113 offset:53248\n"
"ds_read_b128 a[84:87],v113 offset:54272\n"
"v_mfma_f32_16x16x32_bf16 v[224:227],a[96:99],v[92:95],v[224:227]\n"
"ds_read_b128 a[88:91],v113 offset:55296\n"
"v_exp_f32 v18,v18\n"
"v_mfma_f32_16x16x32_bf16 a[140:143],a[144:147],v[92:95],a[140:143]\n"
"ds_read_b128 a[92:95],v113 offset:56320\n"
"buffer_load_dwordx4 v112,s[24:27],s64 offen offset:3072 sc1 lds\n"
"s_add_u32 s64,s64,16384\n"
"v_mfma_f32_16x16x32_bf16 v[228:231],a[100:103],v[92:95],v[228:231]\n"
"s_sub_u32 s82,s3,49152\n"
"v_exp_f32 v19,v19\n"
"v_mfma_f32_16x16x32_bf16 v[132:135],a[100:103],v[80:83],v[132:135]\n"
"buffer_load_dwordx4 a[16:19],v112,s[24:27],s82 offen offset:0 sc1\n"
"v_cvt_pk_bf16_f32 v71,v14,v15\n"
"v_cvt_pk_bf16_f32 v72,v16,v17\n"
"v_cvt_pk_bf16_f32 v73,v18,v19\n"
"v_exp_f32 v20,v20\n"
"v_mfma_f32_16x16x32_bf16 v[164:167],a[100:103],v[84:87],v[164:167]\n"
"buffer_load_dwordx4 a[20:23],v112,s[24:27],s82 offen offset:1024 sc1\n"
"v_mfma_f32_16x16x32_bf16 v[196:199],a[100:103],v[88:91],v[196:199]\n"
"v_exp_f32 v21,v21\n"
"v_max3_f32 v118,v118,v59,v60\n"
"v_mfma_f32_16x16x32_bf16 a[136:139],a[144:147],v[88:91],a[136:139]\n"
"v_cvt_pk_bf16_f32 v74,v20,v21\n"
"v_max3_f32 v118,v118,v61,v62\n"
"v_max3_f32 v118,v118,v63,v52\n"
"v_mfma_f32_16x16x32_bf16 v[200:203],a[104:107],v[88:91],v[200:203]\n"
"v_exp_f32 v22,v22\n"
"v_max3_f32 v118,v118,v53,v54\n"
"v_mfma_f32_16x16x32_bf16 v[232:235],a[104:107],v[92:95],v[232:235]\n"
"buffer_load_dwordx4 a[24:27],v112,s[24:27],s82 offen offset:2048 sc1\n"
"v_max3_f32 v118,v118,v55,v48\n"
"v_max3_f32 v118,v118,v49,v50\n"
"v_mfma_f32_16x16x32_bf16 v[136:139],a[104:107],v[80:83],v[136:139]\n"
"v_exp_f32 v23,v23\n"
"v_max_f32 v118,v118,v51\n"
"v_mfma_f32_16x16x32_bf16 v[168:171],a[104:107],v[84:87],v[168:171]\n"
"v_cvt_pk_bf16_f32 v75,v22,v23\n"
"v_cmp_lt_f32 vcc,s93,v118\n"
"v_mfma_f32_16x16x32_bf16 a[132:135],a[144:147],v[84:87],a[132:135]\n"
"buffer_load_dwordx4 a[28:31],v112,s[24:27],s82 offen offset:3072 sc1\n"
"v_mfma_f32_16x16x32_bf16 v[172:175],a[108:111],v[84:87],v[172:175]\n"
"v_exp_f32 v24,v24\n"
"v_mfma_f32_16x16x32_bf16 v[204:207],a[108:111],v[88:91],v[204:207]\n"
"s_sub_u32 s82,s3,65536\n"
"v_exp_f32 v25,v25\n"
"v_mfma_f32_16x16x32_bf16 v[236:239],a[108:111],v[92:95],v[236:239]\n"
"v_cvt_pk_bf16_f32 v76,v24,v25\n"
"v_mfma_f32_16x16x32_bf16 v[140:143],a[108:111],v[80:83],v[140:143]\n"
"buffer_load_dwordx4 v[32:35],v112,s[24:27],s82 offen offset:0 sc1\n"
"v_mfma_f32_16x16x32_bf16 a[128:131],a[144:147],v[80:83],a[128:131]\n"
"v_exp_f32 v26,v26\n"
"v_mfma_f32_16x16x32_bf16 v[144:147],a[112:115],v[80:83],v[144:147]\n"
"v_exp_f32 v27,v27\n"
"v_mfma_f32_16x16x32_bf16 v[176:179],a[112:115],v[84:87],v[176:179]\n"
"buffer_load_dwordx4 v[36:39],v112,s[24:27],s82 offen offset:1024 sc1\n"
"v_mfma_f32_16x16x32_bf16 v[208:211],a[112:115],v[88:91],v[208:211]\n"
"v_exp_f32 v28,v28\n"
"v_mfma_f32_16x16x32_bf16 v[240:243],a[112:115],v[92:95],v[240:243]\n"
"v_cvt_pk_bf16_f32 v77,v26,v27\n"
"v_mfma_f32_16x16x32_bf16 v[244:247],a[116:119],v[92:95],v[244:247]\n"
"v_exp_f32 v29,v29\n"
"v_mfma_f32_16x16x32_bf16 v[148:151],a[116:119],v[80:83],v[148:151]\n"
"buffer_load_dwordx4 v[40:43],v112,s[24:27],s82 offen offset:2048 sc1\n"
"v_mfma_f32_16x16x32_bf16 v[180:183],a[116:119],v[84:87],v[180:183]\n"
"v_exp_f32 v30,v30\n"
"v_mfma_f32_16x16x32_bf16 v[212:215],a[116:119],v[88:91],v[212:215]\n"
"v_cvt_pk_bf16_f32 v78,v28,v29\n"
"v_mfma_f32_16x16x32_bf16 v[216:219],a[120:123],v[88:91],v[216:219]\n"
"v_exp_f32 v31,v31\n"
"v_mfma_f32_16x16x32_bf16 v[248:251],a[120:123],v[92:95],v[248:251]\n"
"v_cvt_pk_bf16_f32 v79,v30,v31\n"
"v_mfma_f32_16x16x32_bf16 v[152:155],a[120:123],v[80:83],v[152:155]\n"
"buffer_load_dwordx4 v[44:47],v112,s[24:27],s82 offen offset:3072 sc1\n"
"v_mfma_f32_16x16x32_bf16 v[184:187],a[120:123],v[84:87],v[184:187]\n"
"v_mfma_f32_16x16x32_bf16 v[188:191],a[124:127],v[84:87],v[188:191]\n"
"v_mfma_f32_16x16x32_bf16 v[220:223],a[124:127],v[88:91],v[220:223]\n"
"v_mfma_f32_16x16x32_bf16 v[252:255],a[124:127],v[92:95],v[252:255]\n"
"v_mfma_f32_16x16x32_bf16 v[156:159],a[124:127],v[80:83],v[156:159]\n"
"s_cbranch_vccnz .Lfk_cs_a0\n"
".Lfk_cr_a0:\n"
"s_load_dwordx2 s[36:37],s[0:1],0\n"
"s_load_dwordx2 s[44:45],s[0:1],80\n"
"s_load_dwordx2 s[48:49],s[0:1],88\n"
"s_lshl_b32 s82,s78,2\n"
"s_add_u32 s82,s82,65536\n"
"v_add_u32 v113,s82,v112\n"
"s_lshr_b32 s82,s78,2\n"
"s_add_u32 s82,s82,122880\n"
"v_lshrrev_b32 v115,1,v112\n"
"v_add_u32 v116,s82,v115\n"
"s_waitcnt lgkmcnt(0)\n"
"s_barrier\n"
"v_add_f32 v24,0x80000000,v126\n"
"v_min_f32 v24,v24,v108\n"
"v_add_f32 v25,0xf149f2ca,v126\n"
"v_min_f32 v25,v25,v109\n"
"v_add_f32 v26,0xf1c9f2ca,v126\n"
"v_min_f32 v26,v26,v110\n"
"v_add_f32 v27,0xf2177617,v126\n"
"v_min_f32 v27,v27,v111\n"
"v_add_f32 v28,0xf349f2ca,v126\n"
"v_min_f32 v28,v28,v108\n"
"v_add_f32 v29,0xf35691f6,v126\n"
"v_min_f32 v29,v29,v109\n"
"v_add_f32 v30,0xf3633123,v126\n"
"v_min_f32 v30,v30,v110\n"
"v_add_f32 v31,0xf36fd050,v126\n"
"v_min_f32 v31,v31,v111\n"
"v_exp_f32 v48,v48\n"
"v_mfma_f32_16x16x32_bf16 v[24:27],a[64:67],a[48:51],v[24:27]\n"
"s_add_u32 s83,s18,2097152\n"
"ds_read_b128 a[96:99],v114 offset:40960\n"
"ds_read_b128 a[100:103],v114 offset:41984\n"
"v_exp_f32 v49,v49\n"
"v_mfma_f32_16x16x32_bf16 v[24:27],a[68:71],a[52:55],v[24:27]\n"
"s_add_u32 m0,s30,32768\n"
"ds_read_b128 a[104:107],v114 offset:43008\n"
"ds_read_b128 a[108:111],v114 offset:44032\n"
"v_mfma_f32_16x16x32_bf16 v[24:27],a[72:75],a[56:59],v[24:27]\n"
"ds_read_b128 a[112:115],v114 offset:45056\n"
"ds_read_b128 a[116:119],v114 offset:46080\n"
"ds_read_b128 a[120:123],v114 offset:47104\n"
"ds_read_b128 a[124:127],v114 offset:48128\n"
"v_exp_f32 v50,v50\n"
"v_mfma_f32_16x16x32_bf16 v[24:27],a[76:79],a[60:63],v[24:27]\n"
"buffer_load_dwordx4 v112,s[24:27],s83 offen offset:0 sc1 lds\n"
"v_cvt_pk_bf16_f32 v88,v48,v49\n"
"v_mfma_f32_16x16x32_bf16 v[28:31],a[92:95],a[60:63],v[28:31]\n"
"buffer_load_dwordx4 v112,s[24:27],s83 offen offset:1024 sc1 lds\n"
"v_accvgpr_read_b32 v97,a128\n"
"v_cvt_pk_bf16_f32 v128,v128,v129\n"
"v_cvt_pk_bf16_f32 v129,v130,v131\n"
"v_mfma_f32_16x16x32_bf16 v[28:31],a[88:91],a[56:59],v[28:31]\n"
"v_exp_f32 v51,v51\n"
"v_cvt_pk_bf16_f32 v130,v132,v133\n"
"v_cvt_pk_bf16_f32 v131,v134,v135\n"
"v_cvt_pk_bf16_f32 v89,v50,v51\n"
"v_mfma_f32_16x16x32_bf16 v[28:31],a[84:87],a[52:55],v[28:31]\n"
"ds_write_b128 v113,v[128:131] offset:0\n"
"v_cvt_pk_bf16_f32 v136,v136,v137\n"
"v_cvt_pk_bf16_f32 v137,v138,v139\n"
"v_exp_f32 v52,v52\n"
"v_mfma_f32_16x16x32_bf16 v[28:31],a[80:83],a[48:51],v[28:31]\n"
"buffer_load_dwordx4 v112,s[24:27],s83 offen offset:2048 sc1 lds\n"
"v_max3_f32 v118,v24,v25,v26\n"
"s_waitcnt lgkmcnt(1)\n"
"v_mfma_f32_16x16x32_bf16 v[160:163],a[96:99],v[68:71],v[160:163]\n"
"v_cvt_pk_bf16_f32 v138,v140,v141\n"
"v_cvt_pk_bf16_f32 v139,v142,v143\n"
"v_cvt_pk_bf16_f32 v144,v144,v145\n"
"v_mfma_f32_16x16x32_bf16 a[132:135],a[144:147],v[68:71],a[132:135]\n"
"ds_write_b128 v113,v[136:139] offset:1024\n"
"buffer_load_dwordx4 v112,s[24:27],s83 offen offset:3072 sc1 lds\n"
"v_exp_f32 v53,v53\n"
"v_cvt_pk_bf16_f32 v145,v146,v147\n"
"v_mfma_f32_16x16x32_bf16 v[164:167],a[100:103],v[68:71],v[164:167]\n"
"s_add_u32 m0,m0,4096\n"
"v_exp_f32 v54,v54\n"
"v_cvt_pk_bf16_f32 v90,v52,v53\n"
"v_cvt_pk_bf16_f32 v146,v148,v149\n"
"v_mfma_f32_16x16x32_bf16 v[168:171],a[104:107],v[68:71],v[168:171]\n"
"v_max3_f32 v118,v118,v27,v28\n"
"v_max3_f32 v118,v118,v29,v30\n"
"v_max_f32 v118,v118,v31\n"
"v_cmp_lt_f32 vcc,s93,v118\n"
"v_exp_f32 v55,v55\n"
"v_mfma_f32_16x16x32_bf16 v[172:175],a[108:111],v[68:71],v[172:175]\n"
"buffer_load_dwordx4 v117,s[24:27],s83 offen offset:0 sc1 lds\n"
"v_cvt_pk_bf16_f32 v147,v150,v151\n"
"v_cvt_pk_bf16_f32 v91,v54,v55\n"
"v_mfma_f32_16x16x32_bf16 v[176:179],a[112:115],v[68:71],v[176:179]\n"
"ds_write_b128 v113,v[144:147] offset:2048\n"
"v_cvt_pk_bf16_f32 v152,v152,v153\n"
"v_cvt_pk_bf16_f32 v153,v154,v155\n"
"v_exp_f32 v56,v56\n"
"v_mfma_f32_16x16x32_bf16 v[180:183],a[116:119],v[68:71],v[180:183]\n"
"buffer_load_dwordx4 v117,s[24:27],s83 offen offset:1024 sc1 lds\n"
"v_cvt_pk_bf16_f32 v154,v156,v157\n"
"v_cvt_pk_bf16_f32 v155,v158,v159\n"
"v_mfma_f32_16x16x32_bf16 v[184:187],a[120:123],v[68:71],v[184:187]\n"
"ds_write_b128 v113,v[152:155] offset:3072\n"
"ds_write_b64 v116,v[96:97] offset:0\n"
"v_mfma_f32_16x16x32_bf16 v[188:191],a[124:127],v[68:71],v[188:191]\n"
"v_exp_f32 v57,v57\n"
"v_mfma_f32_16x16x32_bf16 v[192:195],a[96:99],v[72:75],v[192:195]\n"
"buffer_load_dwordx4 v117,s[24:27],s83 offen offset:2048 sc1 lds\n"
"v_cvt_pk_bf16_f32 v92,v56,v57\n"
"v_mfma_f32_16x16x32_bf16 a[136:139],a[144:147],v[72:75],a[136:139]\n"
"v_exp_f32 v58,v58\n"
"v_exp_f32 v59,v59\n"
"v_mfma_f32_16x16x32_bf16 v[196:199],a[100:103],v[72:75],v[196:199]\n"
"buffer_load_dwordx4 v117,s[24:27],s83 offen offset:3072 sc1 lds\n"
"v_accvgpr_read_b32 v101,a132\n"
"v_mfma_f32_16x16x32_bf16 v[200:203],a[104:107],v[72:75],v[200:203]\n"
"s_sub_u32 s82,s3,81920\n"
"v_cvt_pk_bf16_f32 v93,v58,v59\n"
"v_cvt_pk_bf16_f32 v160,v160,v161\n"
"v_cvt_pk_bf16_f32 v161,v162,v163\n"
"v_mfma_f32_16x16x32_bf16 v[204:207],a[108:111],v[72:75],v[204:207]\n"
"v_exp_f32 v60,v60\n"
"v_cvt_pk_bf16_f32 v162,v164,v165\n"
"v_cvt_pk_bf16_f32 v163,v166,v167\n"
"v_mfma_f32_16x16x32_bf16 v[208:211],a[112:115],v[72:75],v[208:211]\n"
"buffer_load_dwordx4 a[32:35],v112,s[24:27],s82 offen offset:0 sc1\n"
"ds_write_b128 v113,v[160:163] offset:4096\n"
"v_cvt_pk_bf16_f32 v168,v168,v169\n"
"v_cvt_pk_bf16_f32 v169,v170,v171\n"
"v_mfma_f32_16x16x32_bf16 v[212:215],a[116:119],v[72:75],v[212:215]\n"
"v_exp_f32 v61,v61\n"
"v_cvt_pk_bf16_f32 v170,v172,v173\n"
"v_cvt_pk_bf16_f32 v171,v174,v175\n"
"v_cvt_pk_bf16_f32 v94,v60,v61\n"
"v_mfma_f32_16x16x32_bf16 v[216:219],a[120:123],v[72:75],v[216:219]\n"
"ds_write_b128 v113,v[168:171] offset:5120\n"
"v_cvt_pk_bf16_f32 v176,v176,v177\n"
"v_cvt_pk_bf16_f32 v177,v178,v179\n"
"v_exp_f32 v62,v62\n"
"v_mfma_f32_16x16x32_bf16 v[220:223],a[124:127],v[72:75],v[220:223]\n"
"buffer_load_dwordx4 a[36:39],v112,s[24:27],s82 offen offset:1024 sc1\n"
"v_cvt_pk_bf16_f32 v178,v180,v181\n"
"v_cvt_pk_bf16_f32 v179,v182,v183\n"
"v_mfma_f32_16x16x32_bf16 v[224:227],a[96:99],v[76:79],v[224:227]\n"
"ds_write_b128 v113,v[176:179] offset:6144\n"
"v_cvt_pk_bf16_f32 v184,v184,v185\n"
"v_cvt_pk_bf16_f32 v185,v186,v187\n"
"v_mfma_f32_16x16x32_bf16 a[140:143],a[144:147],v[76:79],a[140:143]\n"
"v_exp_f32 v63,v63\n"
"v_cvt_pk_bf16_f32 v186,v188,v189\n"
"v_cvt_pk_bf16_f32 v187,v190,v191\n"
"v_mfma_f32_16x16x32_bf16 v[228:231],a[100:103],v[76:79],v[228:231]\n"
"buffer_load_dwordx4 a[40:43],v112,s[24:27],s82 offen offset:2048 sc1\n"
"ds_write_b128 v113,v[184:187] offset:7168\n"
"v_mfma_f32_16x16x32_bf16 v[232:235],a[104:107],v[76:79],v[232:235]\n"
"ds_write_b64 v116,v[100:101] offset:512\n"
"v_cvt_pk_bf16_f32 v95,v62,v63\n"
"v_mfma_f32_16x16x32_bf16 v[236:239],a[108:111],v[76:79],v[236:239]\n"
"buffer_load_dwordx4 a[44:47],v112,s[24:27],s82 offen offset:3072 sc1\n"
"v_mfma_f32_16x16x32_bf16 v[240:243],a[112:115],v[76:79],v[240:243]\n"
"s_sub_u32 s82,s3,131072\n"
"buffer_load_dwordx4 v[128:131],v112,s[24:27],s82 offen offset:0 sc1\n"
"buffer_load_dwordx4 v[132:135],v112,s[24:27],s82 offen offset:1024 sc1\n"
"buffer_load_dwordx4 v[136:139],v112,s[24:27],s82 offen offset:2048 sc1\n"
"buffer_load_dwordx4 v[140:143],v112,s[24:27],s82 offen offset:3072 sc1\n"
"v_mfma_f32_16x16x32_bf16 v[244:247],a[116:119],v[76:79],v[244:247]\n"
"s_sub_u32 s82,s3,147456\n"
"buffer_load_dwordx4 v[144:147],v112,s[24:27],s82 offen offset:0 sc1\n"
"buffer_load_dwordx4 v[148:151],v112,s[24:27],s82 offen offset:1024 sc1\n"
"v_mfma_f32_16x16x32_bf16 v[248:251],a[120:123],v[76:79],v[248:251]\n"
"buffer_load_dwordx4 v[152:155],v112,s[24:27],s82 offen offset:2048 sc1\n"
"buffer_load_dwordx4 v[156:159],v112,s[24:27],s82 offen offset:3072 sc1\n"
"v_mfma_f32_16x16x32_bf16 v[252:255],a[124:127],v[76:79],v[252:255]\n"
"s_cbranch_vccnz .Lfk_cs_a1\n"
".Lfk_cr_a1:\n"
"s_waitcnt vmcnt(28) lgkmcnt(0)\n"
"s_barrier\n"
"ds_read_b128 a[96:99],v114 offset:8192\n"
"ds_read_b128 a[100:103],v114 offset:9216\n"
"ds_read_b128 a[104:107],v114 offset:10240\n"
"ds_read_b128 a[108:111],v114 offset:11264\n"
"ds_read_b128 a[112:115],v114 offset:12288\n"
"ds_read_b128 a[116:119],v114 offset:13312\n"
"ds_read_b128 a[120:123],v114 offset:14336\n"
"ds_read_b128 a[124:127],v114 offset:15360\n"
"v_xor_b32 v113,32768,v113\n"
"v_xor_b32 v116,2048,v116\n"
"ds_read_b64 v[98:99],v116 offset:0\n"
"ds_read_b128 a[64:67],v113 offset:0\n"
"ds_read_b128 a[68:71],v113 offset:1024\n"
"ds_read_b128 a[72:75],v113 offset:2048\n"
"ds_read_b128 a[76:79],v113 offset:3072\n"
"ds_read_b64 v[102:103],v116 offset:512\n"
"ds_read_b128 a[80:83],v113 offset:4096\n"
"ds_read_b128 a[84:87],v113 offset:5120\n"
"ds_read_b128 a[88:91],v113 offset:6144\n"
"ds_read_b128 a[92:95],v113 offset:7168\n"
"v_exp_f32 v24,v24\n"
"v_exp_f32 v25,v25\n"
"v_exp_f32 v26,v26\n"
"v_exp_f32 v27,v27\n"
"v_exp_f32 v28,v28\n"
"v_exp_f32 v29,v29\n"
"v_exp_f32 v30,v30\n"
"v_exp_f32 v31,v31\n"
"v_cvt_pk_bf16_f32 v76,v24,v25\n"
"v_cvt_pk_bf16_f32 v77,v26,v27\n"
"v_cvt_pk_bf16_f32 v78,v28,v29\n"
"v_cvt_pk_bf16_f32 v79,v30,v31\n"
"s_waitcnt lgkmcnt(10)\n"
"v_mfma_f32_16x16x32_bf16 a[136:139],a[144:147],v[88:91],a[136:139]\n"
"s_sub_u32 s82,s3,163840\n"
"v_lshrrev_b32 v70,4,v112\n"
"v_and_b32 v71,15,v70\n"
"v_mfma_f32_16x16x32_bf16 a[140:143],a[144:147],v[92:95],a[140:143]\n"
"buffer_load_dwordx4 v[0:3],v112,s[24:27],s82 offen offset:0 sc1\n"
"v_lshrrev_b32 v70,4,v70\n"
"v_lshlrev_b32 v70,2,v70\n"
"v_mfma_f32_16x16x32_bf16 v[192:195],a[96:99],v[88:91],v[192:195]\n"
"buffer_load_dwordx4 v[4:7],v112,s[24:27],s82 offen offset:1024 sc1\n"
"v_sub_u32 v70,v71,v70\n"
"v_mov_b32 v64,0\n"
"v_mfma_f32_16x16x32_bf16 v[224:227],a[96:99],v[92:95],v[224:227]\n"
"ds_read_b128 a[96:99],v114 offset:24576\n"
"buffer_load_dwordx4 v[8:11],v112,s[24:27],s82 offen offset:2048 sc1\n"
"v_mfma_f32_16x16x32_bf16 v[196:199],a[100:103],v[88:91],v[196:199]\n"
"buffer_load_dwordx4 v[12:15],v112,s[24:27],s82 offen offset:3072 sc1\n"
"v_mov_b32 v69,0\n"
"v_mfma_f32_16x16x32_bf16 v[228:231],a[100:103],v[92:95],v[228:231]\n"
"s_sub_u32 s82,s3,180224\n"
"ds_read_b128 a[100:103],v114 offset:25600\n"
"v_mov_b32 v71,0x3f80\n"
"v_mfma_f32_16x16x32_bf16 v[200:203],a[104:107],v[88:91],v[200:203]\n"
"buffer_load_dwordx4 v[160:163],v112,s[24:27],s82 offen offset:0 sc1\n"
"v_cmp_eq_u32 vcc,0,v70\n"
"v_mov_b32 v68,0\n"
"v_mov_b32 v65,0\n"
"v_cndmask_b32 v66,0,v71,vcc\n"
"v_cmp_eq_u32 vcc,1,v70\n"
"v_mfma_f32_16x16x32_bf16 v[232:235],a[104:107],v[92:95],v[232:235]\n"
"ds_read_b128 a[104:107],v114 offset:26624\n"
"buffer_load_dwordx4 v[164:167],v112,s[24:27],s82 offen offset:1024 sc1\n"
"v_mov_b32 v72,0x3f800000\n"
"v_cndmask_b32 v66,v66,v72,vcc\n"
"v_cmp_eq_u32 vcc,2,v70\n"
"v_mfma_f32_16x16x32_bf16 v[204:207],a[108:111],v[88:91],v[204:207]\n"
"buffer_load_dwordx4 v[168:171],v112,s[24:27],s82 offen offset:2048 sc1\n"
"v_cndmask_b32 v67,0,v71,vcc\n"
"v_cmp_eq_u32 vcc,3,v70\n"
"v_mfma_f32_16x16x32_bf16 v[236:239],a[108:111],v[92:95],v[236:239]\n"
"ds_read_b128 a[108:111],v114 offset:27648\n"
"buffer_load_dwordx4 v[172:175],v112,s[24:27],s82 offen offset:3072 sc1\n"
"v_mfma_f32_16x16x32_bf16 v[208:211],a[112:115],v[88:91],v[208:211]\n"
"v_cndmask_b32 v67,v67,v72,vcc\n"
"s_sub_u32 s82,s3,196608\n"
"v_mfma_f32_16x16x32_bf16 v[240:243],a[112:115],v[92:95],v[240:243]\n"
"ds_read_b128 a[112:115],v114 offset:28672\n"
"buffer_load_dwordx4 v[176:179],v112,s[24:27],s82 offen offset:0 sc1\n"
"v_mfma_f32_16x16x32_bf16 v[212:215],a[116:119],v[88:91],v[212:215]\n"
"buffer_load_dwordx4 v[180:183],v112,s[24:27],s82 offen offset:1024 sc1\n"
"v_mfma_f32_16x16x32_bf16 v[244:247],a[116:119],v[92:95],v[244:247]\n"
"ds_read_b128 a[116:119],v114 offset:29696\n"
"buffer_load_dwordx4 v[184:187],v112,s[24:27],s82 offen offset:2048 sc1\n"
"v_mfma_f32_16x16x32_bf16 v[216:219],a[120:123],v[88:91],v[216:219]\n"
"buffer_load_dwordx4 v[188:191],v112,s[24:27],s82 offen offset:3072 sc1\n"
"v_mfma_f32_16x16x32_bf16 v[248:251],a[120:123],v[92:95],v[248:251]\n"
"ds_read_b128 a[120:123],v114 offset:30720\n"
"v_mfma_f32_16x16x32_bf16 v[220:223],a[124:127],v[88:91],v[220:223]\n"
"v_mfma_f32_16x16x32_bf16 v[252:255],a[124:127],v[92:95],v[252:255]\n"
"ds_read_b128 a[124:127],v114 offset:31744\n"
"s_waitcnt lgkmcnt(13)\n"
"v_cmp_neq_f32 vcc,v98,v104\n"
"s_nop 0\n"
"s_cbranch_vccnz .Lfk_ms_a0\n"
".Lfk_mr_a0:\n"
"v_mfma_f32_16x16x32_bf16 v[192:195],v[66:69],a[64:67],v[192:195]\n"
"v_mfma_f32_16x16x32_bf16 v[196:199],v[64:67],a[64:67],v[196:199]\n"
"v_mfma_f32_16x16x32_bf16 v[200:203],v[66:69],a[68:71],v[200:203]\n"
"v_mfma_f32_16x16x32_bf16 v[204:207],v[64:67],a[68:71],v[204:207]\n"
"v_mfma_f32_16x16x32_bf16 v[208:211],v[66:69],a[72:75],v[208:211]\n"
"v_mfma_f32_16x16x32_bf16 v[212:215],v[64:67],a[72:75],v[212:215]\n"
"v_mfma_f32_16x16x32_bf16 v[216:219],v[66:69],a[76:79],v[216:219]\n"
"v_mfma_f32_16x16x32_bf16 v[220:223],v[64:67],a[76:79],v[220:223]\n"
"s_waitcnt lgkmcnt(0)\n"
"v_mfma_f32_16x16x32_bf16 a[140:143],a[144:147],v[76:79],a[140:143]\n"
"v_mfma_f32_16x16x32_bf16 v[224:227],a[96:99],v[76:79],v[224:227]\n"
"v_mfma_f32_16x16x32_bf16 v[228:231],a[100:103],v[76:79],v[228:231]\n"
"v_accvgpr_read_b32 v122,a136\n"
"v_add_f32 v122,v122,v99\n"
"v_mfma_f32_16x16x32_bf16 v[232:235],a[104:107],v[76:79],v[232:235]\n"
"v_rcp_f32 v122,v122\n"
"v_mfma_f32_16x16x32_bf16 v[236:239],a[108:111],v[76:79],v[236:239]\n"
"v_pk_mul_f32 v[192:193],v[192:193],v[122:123] op_sel_hi:[1,0]\n"
"v_pk_mul_f32 v[194:195],v[194:195],v[122:123] op_sel_hi:[1,0]\n"
"v_pk_mul_f32 v[196:197],v[196:197],v[122:123] op_sel_hi:[1,0]\n"
"v_pk_mul_f32 v[198:199],v[198:199],v[122:123] op_sel_hi:[1,0]\n"
"v_mfma_f32_16x16x32_bf16 v[240:243],a[112:115],v[76:79],v[240:243]\n"
"v_cvt_pk_bf16_f32 v192,v192,v193\n"
"v_cvt_pk_bf16_f32 v193,v194,v195\n"
"v_mfma_f32_16x16x32_bf16 v[244:247],a[116:119],v[76:79],v[244:247]\n"
"v_cvt_pk_bf16_f32 v194,v196,v197\n"
"v_cvt_pk_bf16_f32 v195,v198,v199\n"
"v_mfma_f32_16x16x32_bf16 v[248:251],a[120:123],v[76:79],v[248:251]\n"
"buffer_store_dwordx4 v[192:195],v112,s[24:27],s70 offen offset:0 sc1\n"
"v_pk_mul_f32 v[200:201],v[200:201],v[122:123] op_sel_hi:[1,0]\n"
"v_pk_mul_f32 v[202:203],v[202:203],v[122:123] op_sel_hi:[1,0]\n"
"v_pk_mul_f32 v[204:205],v[204:205],v[122:123] op_sel_hi:[1,0]\n"
"v_pk_mul_f32 v[206:207],v[206:207],v[122:123] op_sel_hi:[1,0]\n"
"v_mfma_f32_16x16x32_bf16 v[252:255],a[124:127],v[76:79],v[252:255]\n"
"v_cvt_pk_bf16_f32 v200,v200,v201\n"
"v_cvt_pk_bf16_f32 v201,v202,v203\n"
"v_cmp_neq_f32 vcc,v102,v108\n"
"s_nop 0\n"
"s_cbranch_vccnz .Lfk_ms_a1\n"
".Lfk_mr_a1:\n"
"v_mfma_f32_16x16x32_bf16 v[224:227],v[66:69],a[80:83],v[224:227]\n"
"v_cvt_pk_bf16_f32 v202,v204,v205\n"
"v_cvt_pk_bf16_f32 v203,v206,v207\n"
"v_mfma_f32_16x16x32_bf16 v[228:231],v[64:67],a[80:83],v[228:231]\n"
"buffer_store_dwordx4 v[200:203],v112,s[24:27],s70 offen offset:1024 sc1\n"
"v_pk_mul_f32 v[208:209],v[208:209],v[122:123] op_sel_hi:[1,0]\n"
"v_pk_mul_f32 v[210:211],v[210:211],v[122:123] op_sel_hi:[1,0]\n"
"v_pk_mul_f32 v[212:213],v[212:213],v[122:123] op_sel_hi:[1,0]\n"
"v_pk_mul_f32 v[214:215],v[214:215],v[122:123] op_sel_hi:[1,0]\n"
"v_mfma_f32_16x16x32_bf16 v[232:235],v[66:69],a[84:87],v[232:235]\n"
"v_cvt_pk_bf16_f32 v208,v208,v209\n"
"v_cvt_pk_bf16_f32 v209,v210,v211\n"
"v_mfma_f32_16x16x32_bf16 v[236:239],v[64:67],a[84:87],v[236:239]\n"
"v_cvt_pk_bf16_f32 v210,v212,v213\n"
"v_cvt_pk_bf16_f32 v211,v214,v215\n"
"v_mfma_f32_16x16x32_bf16 v[240:243],v[66:69],a[88:91],v[240:243]\n"
"buffer_store_dwordx4 v[208:211],v112,s[24:27],s70 offen offset:2048 sc1\n"
"v_pk_mul_f32 v[216:217],v[216:217],v[122:123] op_sel_hi:[1,0]\n"
"v_pk_mul_f32 v[218:219],v[218:219],v[122:123] op_sel_hi:[1,0]\n"
"v_pk_mul_f32 v[220:221],v[220:221],v[122:123] op_sel_hi:[1,0]\n"
"v_pk_mul_f32 v[222:223],v[222:223],v[122:123] op_sel_hi:[1,0]\n"
"v_mfma_f32_16x16x32_bf16 v[244:247],v[64:67],a[88:91],v[244:247]\n"
"v_cvt_pk_bf16_f32 v216,v216,v217\n"
"v_cvt_pk_bf16_f32 v217,v218,v219\n"
"v_mfma_f32_16x16x32_bf16 v[248:251],v[66:69],a[92:95],v[248:251]\n"
"v_cvt_pk_bf16_f32 v218,v220,v221\n"
"v_cvt_pk_bf16_f32 v219,v222,v223\n"
"v_mfma_f32_16x16x32_bf16 v[252:255],v[64:67],a[92:95],v[252:255]\n"
"buffer_store_dwordx4 v[216:219],v112,s[24:27],s70 offen offset:3072 sc1\n"
"s_add_u32 s70,s70,262144\n"
"s_branch .Lfk_p1\n"
".Lfk_look:\n"
"s_lshr_b64 s[28:29],s[14:15],s21\n"
"s_and_b32 s83,s28,s19\n"
"s_cbranch_scc0 .Lfk_lk1\n"
"s_mov_b32 s84,0x40000\n"
"s_waitcnt vmcnt(10)\n"
".Lfk_lk0:\n"
"v_xor_b32 v119,s80,v122\n"
"v_xor_b32 v120,s81,v123\n"
"v_or_b32 v119,v119,v120\n"
"v_cmp_ne_u32 s[14:15],0,v119\n"
"s_nop 0\n"
"s_lshr_b64 s[28:29],s[14:15],s21\n"
"s_and_b32 s83,s28,s19\n"
"s_cbranch_scc0 .Lfk_lk1\n"
"s_sleep 2\n"
"buffer_load_dwordx2 v[122:123],v115,s[24:27],s11 offen sc1\n"
"s_waitcnt vmcnt(0)\n"
"s_sub_u32 s84,s84,1\n"
"s_cbranch_scc0 .Lfk_lk0\n"
".Lfk_lk1:\n"
"s_andn2_b32 s28,s28,s19\n"
"s_lshl_b64 s[14:15],s[28:29],s21\n"
"s_lshl_b32 s83,s19,8\n"
"s_and_b32 s83,s28,s83\n"
"s_cbranch_scc0 .Lfk_lk2\n"
"buffer_load_dwordx2 v[122:123],v115,s[24:27],s11 offen sc1\n"
".Lfk_lk2:\n"
"s_nop 1\n"
"s_setpc_b64 s[94:95]\n"
".Lfk_meet4:\n"
"s_mov_b32 s84,0x1000000\n"
".Lfk_meetl4:\n"
"ds_read_b32 v125,v114\n"
"s_sub_u32 s84,s84,1\n"
"s_cbranch_scc1 .Lfk_meetx4\n"
"s_waitcnt lgkmcnt(0)\n"
"v_cmp_gt_u32 vcc,s16,v125\n"
"s_nop 0\n"
"s_cbranch_vccnz .Lfk_meetl4\n"
".Lfk_meetx4:\n"
"s_setpc_b64 s[94:95]\n"
".Lfk_es_lkone:\n"
"s_call_b64 s[94:95],.Lfk_look\n"
"s_branch .Lfk_er_lkone\n"
".Lfk_z0:\n"
"s_call_b64 s[94:95],.Lfk_meet4\n"
"v_xor_b32 v119,s80,v104\n"
"v_xor_b32 v120,s81,v105\n"
"v_or_b32 v119,v119,v120\n"
"v_xor_b32 v120,s80,v106\n"
"v_or_b32 v119,v119,v120\n"
"v_xor_b32 v120,s81,v107\n"
"v_or_b32 v119,v119,v120\n"
"v_xor_b32 v120,s80,v108\n"
"v_or_b32 v119,v119,v120\n"
"v_xor_b32 v120,s81,v109\n"
"v_or_b32 v119,v119,v120\n"
"v_xor_b32 v120,s80,v110\n"
"v_or_b32 v119,v119,v120\n"
"v_xor_b32 v120,s81,v111\n"
"v_or_b32 v119,v119,v120\n"
"v_cmp_ne_u32 vcc,0,v119\n"
"s_nop 0\n"
"s_cbranch_vccz .Lfk_z1\n"
"s_mov_b32 s84,0x40000\n"
"s_mov_b32 s83,148946944\n"
"buffer_load_dwordx4 v[104:107],v112,s[24:27],s83 offen offset:0 sc1\n"
"buffer_load_dwordx4 v[108:111],v112,s[24:27],s83 offen offset:1024 sc1\n"
"s_waitcnt vmcnt(0)\n"
".Lfk_w0:\n"
"v_xor_b32 v119,s80,v104\n"
"v_xor_b32 v120,s81,v105\n"
"v_or_b32 v119,v119,v120\n"
"v_xor_b32 v120,s80,v106\n"
"v_or_b32 v119,v119,v120\n"
"v_xor_b32 v120,s81,v107\n"
"v_or_b32 v119,v119,v120\n"
"v_xor_b32 v120,s80,v108\n"
"v_or_b32 v119,v119,v120\n"
"v_xor_b32 v120,s81,v109\n"
"v_or_b32 v119,v119,v120\n"
"v_xor_b32 v120,s80,v110\n"
"v_or_b32 v119,v119,v120\n"
"v_xor_b32 v120,s81,v111\n"
"v_or_b32 v119,v119,v120\n"
"v_cmp_ne_u32 vcc,0,v119\n"
"s_nop 0\n"
"s_cbranch_vccz .Lfk_w0_x\n"
"s_sleep 8\n"
"s_mov_b64 exec,vcc\n"
"s_mov_b32 s83,148946944\n"
"buffer_load_dwordx4 v[104:107],v112,s[24:27],s83 offen offset:0 sc1\n"
"buffer_load_dwordx4 v[108:111],v112,s[24:27],s83 offen offset:1024 sc1\n"
"s_mov_b64 exec,-1\n"
"s_sub_u32 s84,s84,1\n"
"s_waitcnt vmcnt(0)\n"
"s_cbranch_scc0 .Lfk_w0\n"
".Lfk_w0_x:\n"
"s_nop 1\n"
"s_branch .Lfk_z1\n"
".Lfk_es_lkonem:\n"
"s_call_b64 s[94:95],.Lfk_look\n"
"s_branch .Lfk_er_lkonem\n"
".Lfk_es_mone:\n"
"s_call_b64 s[94:95],.Lfk_meet4\n"
"s_branch .Lfk_er_mone\n"
".Lfk_es_lktwom:\n"
"s_call_b64 s[94:95],.Lfk_look\n"
"s_branch .Lfk_er_lktwom\n"
".Lfk_es_mtwo:\n"
"s_call_b64 s[94:95],.Lfk_meet4\n"
"s_branch .Lfk_er_mtwo\n"
".Lfk_es_lkloopm:\n"
"s_call_b64 s[94:95],.Lfk_look\n"
"s_branch .Lfk_er_lkloopm\n"
".Lfk_es_me3:\n"
"s_call_b64 s[94:95],.Lfk_meet4\n"
"s_branch .Lfk_er_me3\n"
".Lfk_es_me2:\n"
"s_call_b64 s[94:95],.Lfk_meet4\n"
"s_branch .Lfk_er_me2\n"
".Lfk_p1:\n"
"s_cmp_eq_u32 s18,2146435072\n"
"s_cbranch_scc1 .Lfk_p2\n"
".Lfk_p3:\n"
"s_lshr_b32 s22,s8,2\n"
"s_and_b32 s31,s8,0xfff1ffff\n"
"s_add_u32 s18,s18,16384\n"
"s_andn2_b32 s91,s55,16384\n"
"v_add_u32 v116,s91,v112\n"
"v_accvgpr_read_b32 v122,a140\n"
"v_add_f32 v122,v122,v103\n"
"v_rcp_f32 v122,v122\n"
"s_nop 0\n"
"v_pk_mul_f32 v[224:225],v[224:225],v[122:123] op_sel_hi:[1,0]\n"
"v_pk_mul_f32 v[226:227],v[226:227],v[122:123] op_sel_hi:[1,0]\n"
"v_pk_mul_f32 v[228:229],v[228:229],v[122:123] op_sel_hi:[1,0]\n"
"v_pk_mul_f32 v[230:231],v[230:231],v[122:123] op_sel_hi:[1,0]\n"
"v_pk_mul_f32 v[232:233],v[232:233],v[122:123] op_sel_hi:[1,0]\n"
"v_pk_mul_f32 v[234:235],v[234:235],v[122:123] op_sel_hi:[1,0]\n"
"v_pk_mul_f32 v[236:237],v[236:237],v[122:123] op_sel_hi:[1,0]\n"
"v_pk_mul_f32 v[238:239],v[238:239],v[122:123] op_sel_hi:[1,0]\n"
"v_pk_mul_f32 v[240:241],v[240:241],v[122:123] op_sel_hi:[1,0]\n"
"v_pk_mul_f32 v[242:243],v[242:243],v[122:123] op_sel_hi:[1,0]\n"
"v_pk_mul_f32 v[244:245],v[244:245],v[122:123] op_sel_hi:[1,0]\n"
"v_pk_mul_f32 v[246:247],v[246:247],v[122:123] op_sel_hi:[1,0]\n"
"v_pk_mul_f32 v[248:249],v[248:249],v[122:123] op_sel_hi:[1,0]\n"
"v_pk_mul_f32 v[250:251],v[250:251],v[122:123] op_sel_hi:[1,0]\n"
"v_pk_mul_f32 v[252:253],v[252:253],v[122:123] op_sel_hi:[1,0]\n"
"v_pk_mul_f32 v[254:255],v[254:255],v[122:123] op_sel_hi:[1,0]\n"
"s_waitcnt vmcnt(24) lgkmcnt(0)\n"
"s_barrier\n"
"ds_read_b128 v[192:195],v112 offset:0\n"
"ds_read_b128 v[196:199],v112 offset:1024\n"
"ds_read_b128 v[200:203],v112 offset:2048\n"
"ds_read_b128 v[204:207],v112 offset:3072\n"
"ds_read_b128 v[208:211],v112 offset:4096\n"
"ds_read_b128 v[212:215],v112 offset:5120\n"
"ds_read_b128 v[216:219],v112 offset:6144\n"
"ds_read_b128 v[220:223],v112 offset:7168\n"
"v_cvt_pk_bf16_f32 v224,v224,v225\n"
"v_cvt_pk_bf16_f32 v225,v226,v227\n"
"v_cvt_pk_bf16_f32 v226,v228,v229\n"
"v_cvt_pk_bf16_f32 v227,v230,v231\n"
"v_cvt_pk_bf16_f32 v232,v232,v233\n"
"v_cvt_pk_bf16_f32 v233,v234,v235\n"
"v_cvt_pk_bf16_f32 v234,v236,v237\n"
"v_cvt_pk_bf16_f32 v235,v238,v239\n"
"v_cvt_pk_bf16_f32 v240,v240,v241\n"
"v_cvt_pk_bf16_f32 v241,v242,v243\n"
"v_cvt_pk_bf16_f32 v242,v244,v245\n"
"v_cvt_pk_bf16_f32 v243,v246,v247\n"
"v_cvt_pk_bf16_f32 v248,v248,v249\n"
"v_cvt_pk_bf16_f32 v249,v250,v251\n"
"v_cvt_pk_bf16_f32 v250,v252,v253\n"
"v_cvt_pk_bf16_f32 v251,v254,v255\n"
"buffer_store_dwordx4 v[224:227],v112,s[24:27],s70 offen offset:0 sc1\n"
"buffer_store_dwordx4 v[232:235],v112,s[24:27],s70 offen offset:1024 sc1\n"
"buffer_store_dwordx4 v[240:243],v112,s[24:27],s70 offen offset:2048 sc1\n"
"buffer_store_dwordx4 v[248:251],v112,s[24:27],s70 offen offset:3072 sc1\n"
"s_waitcnt lgkmcnt(4)\n"
"v_mfma_f32_16x16x32_bf16 v[80:83],a[224:227],v[192:195],0\n"
"v_mfma_f32_16x16x32_bf16 v[80:83],a[232:235],v[196:199],v[80:83]\n"
"ds_read_b128 v[224:227],v112 offset:8192\n"
"ds_read_b128 v[228:231],v112 offset:9216\n"
"v_mfma_f32_16x16x32_bf16 v[80:83],a[240:243],v[200:203],v[80:83]\n"
"ds_read_b128 v[232:235],v112 offset:10240\n"
"ds_read_b128 v[236:239],v112 offset:11264\n"
"v_mfma_f32_16x16x32_bf16 v[80:83],a[248:251],v[204:207],v[80:83]\n"
"ds_read_b128 v[240:243],v112 offset:12288\n"
"ds_read_b128 v[244:247],v112 offset:13312\n"
"v_mfma_f32_16x16x32_bf16 v[84:87],a[252:255],v[204:207],0\n"
"ds_read_b128 v[248:251],v112 offset:14336\n"
"ds_read_b128 v[252:255],v112 offset:15360\n"
"v_mfma_f32_16x16x32_bf16 v[84:87],a[244:247],v[200:203],v[84:87]\n"
"ds_read_b128 a[68:71],v116 offset:4096\n"
"ds_read_b128 a[72:75],v116 offset:8192\n"
"v_mfma_f32_16x16x32_bf16 v[84:87],a[236:239],v[196:199],v[84:87]\n"
"ds_read_b128 a[76:79],v116 offset:12288\n"
"ds_read_b128 a[80:83],v116 offset:0\n"
"v_mfma_f32_16x16x32_bf16 v[84:87],a[228:231],v[192:195],v[84:87]\n"
"ds_read_b128 a[84:87],v116 offset:20480\n"
"ds_read_b128 a[88:91],v116 offset:24576\n"
"s_waitcnt lgkmcnt(14)\n"
"v_mfma_f32_16x16x32_bf16 v[84:87],a[4:7],v[208:211],v[84:87]\n"
"ds_read_b128 a[92:95],v116 offset:28672\n"
"v_mfma_f32_16x16x32_bf16 v[84:87],a[12:15],v[212:215],v[84:87]\n"
"ds_read_b128 v[204:207],v112 offset:19456\n"
"s_lshl_b32 s91,s78,3\n"
"v_mfma_f32_16x16x32_bf16 v[84:87],a[20:23],v[216:219],v[84:87]\n"
"ds_read_b128 v[200:203],v112 offset:18432\n"
"s_add_u32 s91,s91,131072\n"
"v_mfma_f32_16x16x32_bf16 v[84:87],a[28:31],v[220:223],v[84:87]\n"
"ds_read_b128 v[196:199],v112 offset:17408\n"
"v_lshrrev_b32 v101,2,v112\n"
"v_mfma_f32_16x16x32_bf16 v[80:83],a[24:27],v[220:223],v[80:83]\n"
"ds_read_b128 v[192:195],v112 offset:16384\n"
"v_add_u32 v101,s91,v101\n"
"v_mfma_f32_16x16x32_bf16 v[80:83],a[16:19],v[216:219],v[80:83]\n"
"ds_read_b128 v[220:223],v112 offset:23552\n"
"v_lshrrev_b32 v114,2,v112\n"
"v_mfma_f32_16x16x32_bf16 v[80:83],a[8:11],v[212:215],v[80:83]\n"
"ds_read_b128 v[216:219],v112 offset:22528\n"
"v_add_u32 v114,131328,v114\n"
"v_mfma_f32_16x16x32_bf16 v[80:83],a[0:3],v[208:211],v[80:83]\n"
"ds_read_b128 v[212:215],v112 offset:21504\n"
"s_waitcnt lgkmcnt(15)\n"
"v_mfma_f32_16x16x32_bf16 v[80:83],v[32:35],v[224:227],v[80:83]\n"
"ds_read_b128 v[208:211],v112 offset:20480\n"
"s_mov_b32 s83,148946944\n"
"v_mfma_f32_16x16x32_bf16 v[80:83],v[40:43],v[228:231],v[80:83]\n"
"buffer_load_dwordx4 v[104:107],v112,s[24:27],s83 offen offset:0 sc1\n"
"v_mov_b32 v116,0\n"
"v_mfma_f32_16x16x32_bf16 v[80:83],a[32:35],v[232:235],v[80:83]\n"
"ds_write_b32 v101,v116 offset:256\n"
"v_mov_b32 v116,1\n"
"v_mfma_f32_16x16x32_bf16 v[80:83],a[40:43],v[236:239],v[80:83]\n"
"buffer_load_dwordx4 v[108:111],v112,s[24:27],s83 offen offset:1024 sc1\n"
"v_mfma_f32_16x16x32_bf16 v[84:87],a[44:47],v[236:239],v[84:87]\n"
"s_mov_b32 s16,4\n"
"ds_read_b128 v[236:239],v112 offset:27648\n"
"v_add_u32 v113,65536,v112\n"
"v_mfma_f32_16x16x32_bf16 v[84:87],a[36:39],v[232:235],v[84:87]\n"
"ds_read_b128 v[232:235],v112 offset:26624\n"
"s_add_u32 s31,s31,8388608\n"
"v_mfma_f32_16x16x32_bf16 v[84:87],v[44:47],v[228:231],v[84:87]\n"
"ds_read_b128 v[228:231],v112 offset:25600\n"
"s_add_u32 s53,s22,786432\n"
"v_mfma_f32_16x16x32_bf16 v[84:87],v[36:39],v[224:227],v[84:87]\n"
"s_add_u32 m0,s30,65536\n"
"ds_read_b128 v[224:227],v112 offset:24576\n"
"s_waitcnt lgkmcnt(13)\n"
"v_mfma_f32_16x16x32_bf16 v[84:87],a[68:71],v[240:243],v[84:87]\n"
"buffer_load_dwordx4 v112,s[24:27],s18 offen offset:0 sc1 lds\n"
"v_mfma_f32_16x16x32_bf16 v[84:87],a[76:79],v[244:247],v[84:87]\n"
"buffer_load_dwordx4 v112,s[24:27],s18 offen offset:1024 sc1 lds\n"
"v_mfma_f32_16x16x32_bf16 v[84:87],a[84:87],v[248:251],v[84:87]\n"
"s_add_u32 s56,s22,1048576\n"
"s_add_u32 s58,s22,1310720\n"
"v_mfma_f32_16x16x32_bf16 v[84:87],a[92:95],v[252:255],v[84:87]\n"
"buffer_load_dwordx4 v112,s[24:27],s18 offen offset:2048 sc1 lds\n"
"v_mfma_f32_16x16x32_bf16 v[80:83],a[88:91],v[252:255],v[80:83]\n"
"ds_read_b128 v[252:255],v112 offset:31744\n"
"s_add_u32 s59,s22,1572864\n"
"v_mfma_f32_16x16x32_bf16 v[80:83],a[80:83],v[248:251],v[80:83]\n"
"ds_read_b128 v[248:251],v112 offset:30720\n"
"s_bfe_u32 s10,s3,0x20015\n"
"v_mfma_f32_16x16x32_bf16 v[80:83],a[72:75],v[244:247],v[80:83]\n"
"ds_read_b128 v[244:247],v112 offset:29696\n"
"s_mul_i32 s10,s10,4186112\n"
"v_mfma_f32_16x16x32_bf16 v[80:83],a[156:159],v[240:243],v[80:83]\n"
"ds_read_b128 v[240:243],v112 offset:28672\n"
"s_waitcnt lgkmcnt(13)\n"
"v_mfma_f32_16x16x32_bf16 v[64:67],a[224:227],v[192:195],0\n"
"buffer_load_dwordx4 v112,s[24:27],s18 offen offset:3072 sc1 lds\n"
"s_add_u32 m0,m0,4096\n"
"v_mfma_f32_16x16x32_bf16 v[64:67],a[232:235],v[196:199],v[64:67]\n"
"buffer_load_dwordx4 v117,s[24:27],s18 offen offset:0 sc1 lds\n"
"v_mfma_f32_16x16x32_bf16 v[64:67],a[240:243],v[200:203],v[64:67]\n"
"s_add_u32 s12,s12,s10\n"
"s_bfe_u32 s10,s3,0x30012\n"
"v_mfma_f32_16x16x32_bf16 v[64:67],a[248:251],v[204:207],v[64:67]\n"
"buffer_load_dwordx4 v117,s[24:27],s18 offen offset:1024 sc1 lds\n"
"v_mfma_f32_16x16x32_bf16 v[68:71],a[252:255],v[204:207],0\n"
"ds_read_b128 v[204:207],v112 offset:35840\n"
"s_lshl_b32 s10,s10,12\n"
"v_mfma_f32_16x16x32_bf16 v[68:71],a[244:247],v[200:203],v[68:71]\n"
"ds_read_b128 v[200:203],v112 offset:34816\n"
"s_add_u32 s12,s12,s10\n"
"v_mfma_f32_16x16x32_bf16 v[68:71],a[236:239],v[196:199],v[68:71]\n"
"ds_read_b128 v[196:199],v112 offset:33792\n"
"s_bfe_u32 s10,s3,0x50012\n"
"v_mfma_f32_16x16x32_bf16 v[68:71],a[228:231],v[192:195],v[68:71]\n"
"ds_read_b128 v[192:195],v112 offset:32768\n"
"s_waitcnt lgkmcnt(13)\n"
"s_waitcnt vmcnt(8) lgkmcnt(12)\n"
"s_barrier\n"
"v_mfma_f32_16x16x32_bf16 v[68:71],a[4:7],v[208:211],v[68:71]\n"
"buffer_store_dwordx2 a[154:155],off,s[24:27],s71 sc1\n"
"buffer_load_dwordx4 v117,s[24:27],s18 offen offset:2048 sc1 lds\n"
"v_mfma_f32_16x16x32_bf16 v[68:71],a[12:15],v[212:215],v[68:71]\n"
"buffer_load_dwordx4 v117,s[24:27],s18 offen offset:3072 sc1 lds\n"
"v_mfma_f32_16x16x32_bf16 v[68:71],a[20:23],v[216:219],v[68:71]\n"
"s_sub_u32 s82,s3,212992\n"
"s_lshl_b32 s10,s10,8\n"
"v_mfma_f32_16x16x32_bf16 v[68:71],a[28:31],v[220:223],v[68:71]\n"
"buffer_load_dwordx4 a[48:51],v112,s[24:27],s82 offen offset:0 sc1\n"
"v_mfma_f32_16x16x32_bf16 v[64:67],a[24:27],v[220:223],v[64:67]\n"
"s_add_u32 s10,s10,149061632\n"
"ds_read_b128 v[220:223],v112 offset:39936\n"
"s_mov_b32 s14,0\n"
"v_mfma_f32_16x16x32_bf16 v[64:67],a[16:19],v[216:219],v[64:67]\n"
"ds_read_b128 v[216:219],v112 offset:38912\n"
"v_not_b32 v122,s80\n"
"v_mfma_f32_16x16x32_bf16 v[64:67],a[8:11],v[212:215],v[64:67]\n"
"s_cmp_eq_u32 s52,0\n"
"ds_read_b128 v[212:215],v112 offset:37888\n"
"s_cselect_b64 s[14:15],s[14:15],0\n"
"v_mfma_f32_16x16x32_bf16 v[64:67],a[0:3],v[208:211],v[64:67]\n"
"s_cselect_b32 s10,s10,2146435072\n"
"ds_read_b128 v[208:211],v112 offset:36864\n"
"s_waitcnt lgkmcnt(12)\n"
"v_mfma_f32_16x16x32_bf16 v[64:67],v[32:35],v[224:227],v[64:67]\n"
"buffer_load_dwordx4 a[52:55],v112,s[24:27],s82 offen offset:1024 sc1\n"
"s_cmp_lg_u32 s15,0\n"
"s_cselect_b32 s91,s11,2146435072\n"
"v_mfma_f32_16x16x32_bf16 v[64:67],v[40:43],v[228:231],v[64:67]\n"
"buffer_load_dwordx4 a[56:59],v112,s[24:27],s82 offen offset:2048 sc1\n"
"buffer_load_dwordx2 v[122:123],v115,s[24:27],s91 offen sc1\n"
"v_mfma_f32_16x16x32_bf16 v[64:67],a[32:35],v[232:235],v[64:67]\n"
"s_mov_b32 s89,0\n"
"s_mov_b32 s20,32768\n"
"v_mfma_f32_16x16x32_bf16 v[64:67],a[40:43],v[236:239],v[64:67]\n"
"buffer_load_dwordx4 a[60:63],v112,s[24:27],s82 offen offset:3072 sc1\n"
"v_mfma_f32_16x16x32_bf16 v[68:71],a[44:47],v[236:239],v[68:71]\n"
"s_mov_b32 s19,0xffff\n"
"ds_read_b128 v[236:239],v112 offset:44032\n"
"s_mov_b32 s21,32\n"
"v_mfma_f32_16x16x32_bf16 v[68:71],a[36:39],v[232:235],v[68:71]\n"
"ds_read_b128 v[232:235],v112 offset:43008\n"
"v_mfma_f32_16x16x32_bf16 v[68:71],v[44:47],v[228:231],v[68:71]\n"
"ds_read_b128 v[228:231],v112 offset:41984\n"
"v_mfma_f32_16x16x32_bf16 v[68:71],v[36:39],v[224:227],v[68:71]\n"
"s_sub_u32 s82,s3,229376\n"
"ds_read_b128 v[224:227],v112 offset:40960\n"
"s_waitcnt lgkmcnt(12)\n"
"v_mfma_f32_16x16x32_bf16 v[68:71],a[68:71],v[240:243],v[68:71]\n"
"buffer_load_dwordx4 v[48:51],v112,s[24:27],s82 offen offset:0 sc1\n"
"v_mfma_f32_16x16x32_bf16 v[68:71],a[76:79],v[244:247],v[68:71]\n"
"buffer_load_dwordx4 v[52:55],v112,s[24:27],s82 offen offset:1024 sc1\n"
"v_mfma_f32_16x16x32_bf16 v[68:71],a[84:87],v[248:251],v[68:71]\n"
"buffer_load_dwordx4 v[56:59],v112,s[24:27],s82 offen offset:2048 sc1\n"
"v_mfma_f32_16x16x32_bf16 v[68:71],a[92:95],v[252:255],v[68:71]\n"
"s_waitcnt vmcnt(17)\n"
"v_mfma_f32_16x16x32_bf16 v[64:67],a[88:91],v[252:255],v[64:67]\n"
"ds_read_b128 v[252:255],v112 offset:48128\n"
"v_xor_b32 v119,s80,v104\n"
"v_mfma_f32_16x16x32_bf16 v[64:67],a[80:83],v[248:251],v[64:67]\n"
"ds_read_b128 v[248:251],v112 offset:47104\n"
"v_xor_b32 v120,s81,v105\n"
"v_mfma_f32_16x16x32_bf16 v[64:67],a[72:75],v[244:247],v[64:67]\n"
"ds_read_b128 v[244:247],v112 offset:46080\n"
"v_or_b32 v119,v119,v120\n"
"v_mfma_f32_16x16x32_bf16 v[64:67],a[156:159],v[240:243],v[64:67]\n"
"ds_read_b128 v[240:243],v112 offset:45056\n"
"s_waitcnt lgkmcnt(12)\n"
"v_mfma_f32_16x16x32_bf16 v[72:75],a[224:227],v[192:195],0\n"
"buffer_load_dwordx4 v[60:63],v112,s[24:27],s82 offen offset:3072 sc1\n"
"s_sub_u32 s82,s3,245760\n"
"v_mfma_f32_16x16x32_bf16 v[72:75],a[232:235],v[196:199],v[72:75]\n"
"buffer_load_dwordx4 v[16:19],v112,s[24:27],s82 offen offset:0 sc1\n"
"v_xor_b32 v120,s80,v106\n"
"v_mfma_f32_16x16x32_bf16 v[72:75],a[240:243],v[200:203],v[72:75]\n"
"v_or_b32 v119,v119,v120\n"
"v_xor_b32 v120,s81,v107\n"
"v_mfma_f32_16x16x32_bf16 v[72:75],a[248:251],v[204:207],v[72:75]\n"
"buffer_load_dwordx4 v[20:23],v112,s[24:27],s82 offen offset:1024 sc1\n"
"v_or_b32 v119,v119,v120\n"
"v_mfma_f32_16x16x32_bf16 v[76:79],a[252:255],v[204:207],0\n"
"ds_read_b128 v[204:207],v112 offset:52224\n"
"v_xor_b32 v120,s80,v108\n"
"v_mfma_f32_16x16x32_bf16 v[76:79],a[244:247],v[200:203],v[76:79]\n"
"ds_read_b128 v[200:203],v112 offset:51200\n"
"v_or_b32 v119,v119,v120\n"
"v_mfma_f32_16x16x32_bf16 v[76:79],a[236:239],v[196:199],v[76:79]\n"
"ds_read_b128 v[196:199],v112 offset:50176\n"
"v_xor_b32 v120,s81,v109\n"
"v_mfma_f32_16x16x32_bf16 v[76:79],a[228:231],v[192:195],v[76:79]\n"
"ds_read_b128 v[192:195],v112 offset:49152\n"
"s_waitcnt lgkmcnt(12)\n"
"v_mfma_f32_16x16x32_bf16 v[76:79],a[4:7],v[208:211],v[76:79]\n"
"buffer_load_dwordx4 v[24:27],v112,s[24:27],s82 offen offset:2048 sc1\n"
"v_or_b32 v119,v119,v120\n"
"v_mfma_f32_16x16x32_bf16 v[76:79],a[12:15],v[212:215],v[76:79]\n"
"buffer_load_dwordx4 v[28:31],v112,s[24:27],s82 offen offset:3072 sc1\n"
"v_mfma_f32_16x16x32_bf16 v[76:79],a[20:23],v[216:219],v[76:79]\n"
"s_add_u32 s18,s18,2097152\n"
"v_xor_b32 v120,s80,v110\n"
"s_add_u32 m0,s30,98304\n"
"v_mfma_f32_16x16x32_bf16 v[76:79],a[28:31],v[220:223],v[76:79]\n"
"buffer_load_dwordx4 v112,s[24:27],s18 offen offset:0 sc1 lds\n"
"v_mfma_f32_16x16x32_bf16 v[72:75],a[24:27],v[220:223],v[72:75]\n"
"ds_read_b128 v[220:223],v112 offset:56320\n"
"v_or_b32 v119,v119,v120\n"
"v_mfma_f32_16x16x32_bf16 v[72:75],a[16:19],v[216:219],v[72:75]\n"
"ds_read_b128 v[216:219],v112 offset:55296\n"
"v_xor_b32 v120,s81,v111\n"
"v_mfma_f32_16x16x32_bf16 v[72:75],a[8:11],v[212:215],v[72:75]\n"
"ds_read_b128 v[212:215],v112 offset:54272\n"
"v_or_b32 v119,v119,v120\n"
"v_mfma_f32_16x16x32_bf16 v[72:75],a[0:3],v[208:211],v[72:75]\n"
"ds_read_b128 v[208:211],v112 offset:53248\n"
"s_waitcnt lgkmcnt(12)\n"
"v_mfma_f32_16x16x32_bf16 v[72:75],v[32:35],v[224:227],v[72:75]\n"
"buffer_load_dwordx4 v112,s[24:27],s18 offen offset:1024 sc1 lds\n"
"v_mfma_f32_16x16x32_bf16 v[72:75],v[40:43],v[228:231],v[72:75]\n"
"buffer_load_dwordx4 v112,s[24:27],s18 offen offset:2048 sc1 lds\n"
"v_mfma_f32_16x16x32_bf16 v[72:75],a[32:35],v[232:235],v[72:75]\n"
"v_cmp_ne_u32 vcc,0,v119\n"
"v_mfma_f32_16x16x32_bf16 v[72:75],a[40:43],v[236:239],v[72:75]\n"
"buffer_load_dwordx4 v112,s[24:27],s18 offen offset:3072 sc1 lds\n"
"v_mfma_f32_16x16x32_bf16 v[76:79],a[44:47],v[236:239],v[76:79]\n"
"ds_read_b128 v[236:239],v112 offset:60416\n"
"v_mfma_f32_16x16x32_bf16 v[76:79],a[36:39],v[232:235],v[76:79]\n"
"ds_read_b128 v[232:235],v112 offset:59392\n"
"v_xor_b32 v119,s80,v122\n"
"v_mfma_f32_16x16x32_bf16 v[76:79],v[44:47],v[228:231],v[76:79]\n"
"ds_read_b128 v[228:231],v112 offset:58368\n"
"v_xor_b32 v120,s81,v123\n"
"v_mfma_f32_16x16x32_bf16 v[76:79],v[36:39],v[224:227],v[76:79]\n"
"s_add_u32 m0,m0,4096\n"
"ds_read_b128 v[224:227],v112 offset:57344\n"
"s_waitcnt lgkmcnt(12)\n"
"v_mfma_f32_16x16x32_bf16 v[76:79],a[68:71],v[240:243],v[76:79]\n"
"buffer_load_dwordx4 v117,s[24:27],s18 offen offset:0 sc1 lds\n"
"v_or_b32 v119,v119,v120\n"
"v_mfma_f32_16x16x32_bf16 v[76:79],a[76:79],v[244:247],v[76:79]\n"
"buffer_load_dwordx4 v117,s[24:27],s18 offen offset:1024 sc1 lds\n"
"v_cmp_ne_u32 s[28:29],0,v119\n"
"v_mfma_f32_16x16x32_bf16 v[76:79],a[84:87],v[248:251],v[76:79]\n"
"s_and_b64 s[14:15],s[14:15],s[28:29]\n"
"s_cselect_b32 s91,s11,2146435072\n"
"buffer_load_dwordx2 v[122:123],v115,s[24:27],s91 offen sc1\n"
"v_mfma_f32_16x16x32_bf16 v[76:79],a[92:95],v[252:255],v[76:79]\n"
"buffer_load_dwordx4 v117,s[24:27],s18 offen offset:2048 sc1 lds\n"
"v_mfma_f32_16x16x32_bf16 v[72:75],a[88:91],v[252:255],v[72:75]\n"
"ds_read_b128 v[252:255],v112 offset:64512\n"
"v_mfma_f32_16x16x32_bf16 v[72:75],a[80:83],v[248:251],v[72:75]\n"
"ds_read_b128 v[248:251],v112 offset:63488\n"
"v_mfma_f32_16x16x32_bf16 v[72:75],a[72:75],v[244:247],v[72:75]\n"
"ds_read_b128 v[244:247],v112 offset:62464\n"
"v_mfma_f32_16x16x32_bf16 v[72:75],a[156:159],v[240:243],v[72:75]\n"
"s_and_b32 s83,s15,0xffff\n"
"ds_read_b128 v[240:243],v112 offset:61440\n"
"s_waitcnt lgkmcnt(12)\n"
"v_mfma_f32_16x16x32_bf16 v[88:91],a[224:227],v[192:195],0\n"
"buffer_load_dwordx4 v117,s[24:27],s18 offen offset:3072 sc1 lds\n"
"v_mfma_f32_16x16x32_bf16 v[88:91],a[232:235],v[196:199],v[88:91]\n"
"s_cbranch_scc1 .Lfk_es_lkone\n"
".Lfk_er_lkone:\n"
"v_mfma_f32_16x16x32_bf16 v[88:91],a[240:243],v[200:203],v[88:91]\n"
"s_waitcnt vmcnt(22)\n"
"ds_add_u32 v114,v116\n"
"v_mfma_f32_16x16x32_bf16 v[88:91],a[248:251],v[204:207],v[88:91]\n"
"v_and_b32 v118,0xf0,v112\n"
"v_lshrrev_b32 v120,8,v112\n"
"v_lshlrev_b32 v120,4,v120\n"
"v_lshl_add_u32 v118,v118,2,v120\n"
"v_mfma_f32_16x16x32_bf16 v[92:95],a[252:255],v[204:207],0\n"
"s_lshr_b32 s82,s78,20\n"
"v_xor_b32 v118,s82,v118\n"
"v_mov_b32 v100,1\n"
"v_mfma_f32_16x16x32_bf16 v[92:95],a[244:247],v[200:203],v[92:95]\n"
"s_mov_b32 s77,0\n"
"v_mov_b32 v102,131076\n"
"s_and_b32 s91,s52,1\n"
"v_mfma_f32_16x16x32_bf16 v[92:95],a[236:239],v[196:199],v[92:95]\n"
"s_lshl_b32 s91,s91,7\n"
"v_xor_b32 v96,s91,v112\n"
"v_xor_b32 v97,32,v96\n"
"v_mfma_f32_16x16x32_bf16 v[92:95],a[228:231],v[192:195],v[92:95]\n"
"v_xor_b32 v99,96,v96\n"
"s_waitcnt lgkmcnt(9)\n"
"v_mfma_f32_16x16x32_bf16 v[92:95],a[4:7],v[208:211],v[92:95]\n"
"v_xor_b32 v98,64,v96\n"
"v_add_u32 v118,s12,v118\n"
"v_mfma_f32_16x16x32_bf16 v[92:95],a[12:15],v[212:215],v[92:95]\n"
"ds_read_b32 v125,v114\n"
"v_mfma_f32_16x16x32_bf16 v[92:95],a[20:23],v[216:219],v[92:95]\n"
"v_mfma_f32_16x16x32_bf16 v[92:95],a[28:31],v[220:223],v[92:95]\n"
"v_mfma_f32_16x16x32_bf16 v[88:91],a[24:27],v[220:223],v[88:91]\n"
"v_mfma_f32_16x16x32_bf16 v[88:91],a[16:19],v[216:219],v[88:91]\n"
"s_waitcnt lgkmcnt(0)\n"
"v_cmp_gt_u32 s[28:29],s16,v125\n"
"v_mfma_f32_16x16x32_bf16 v[88:91],a[8:11],v[212:215],v[88:91]\n"
"s_add_u32 s18,s8,4194304\n"
"s_add_u32 s8,s8,5242880\n"
"s_or_b64 vcc,vcc,s[28:29]\n"
"v_mfma_f32_16x16x32_bf16 v[88:91],a[0:3],v[208:211],v[88:91]\n"
"s_cbranch_vccnz .Lfk_z0\n"
".Lfk_z1:\n"
"ds_read_b128 v[192:195],v113 offset:0\n"
"v_mfma_f32_16x16x32_bf16 v[88:91],v[32:35],v[224:227],v[88:91]\n"
"s_mov_b32 s89,1\n"
"s_mov_b32 s19,0xff\n"
"s_lshl_b64 exec,1,s89\n"
"buffer_atomic_add v100,off,s[24:27],s10 sc0\n"
"s_mov_b64 exec,-1\n"
"v_mfma_f32_16x16x32_bf16 v[88:91],v[40:43],v[228:231],v[88:91]\n"
"ds_read_b128 v[196:199],v113 offset:1024\n"
"s_add_u32 m0,s30,0\n"
"v_mfma_f32_16x16x32_bf16 v[88:91],a[32:35],v[232:235],v[88:91]\n"
"ds_read_b128 v[200:203],v113 offset:2048\n"
"buffer_load_dwordx4 v112,s[24:27],s18 offen offset:0 sc1 lds\n"
"v_mfma_f32_16x16x32_bf16 v[88:91],a[40:43],v[236:239],v[88:91]\n"
"s_add_u32 s89,s89,1\n"
"ds_read_b128 v[204:207],v113 offset:3072\n"
"s_add_u32 s21,s21,16\n"
"v_mfma_f32_16x16x32_bf16 v[92:95],a[44:47],v[236:239],v[92:95]\n"
"ds_read_b128 v[208:211],v113 offset:16384\n"
"v_mfma_f32_16x16x32_bf16 v[92:95],a[36:39],v[232:235],v[92:95]\n"
"ds_read_b128 v[212:215],v113 offset:17408\n"
"s_add_u32 s16,s16,4\n"
"v_mfma_f32_16x16x32_bf16 v[92:95],v[44:47],v[228:231],v[92:95]\n"
"ds_read_b128 v[216:219],v113 offset:18432\n"
"buffer_load_dwordx4 v112,s[24:27],s18 offen offset:1024 sc1 lds\n"
"v_mfma_f32_16x16x32_bf16 v[92:95],v[36:39],v[224:227],v[92:95]\n"
"ds_read_b128 v[220:223],v113 offset:19456\n"
"v_mfma_f32_16x16x32_bf16 v[92:95],a[68:71],v[240:243],v[92:95]\n"
"ds_read_b128 v[236:239],v113 offset:7168\n"
"v_mfma_f32_16x16x32_bf16 v[92:95],a[76:79],v[244:247],v[92:95]\n"
"ds_read_b128 v[232:235],v113 offset:6144\n"
"ds_read_b128 v[228:231],v113 offset:5120\n"
"v_mfma_f32_16x16x32_bf16 v[92:95],a[84:87],v[248:251],v[92:95]\n"
"buffer_load_dwordx4 v112,s[24:27],s18 offen offset:2048 sc1 lds\n"
"v_mfma_f32_16x16x32_bf16 v[92:95],a[92:95],v[252:255],v[92:95]\n"
"ds_read_b128 v[224:227],v113 offset:4096\n"
"v_mfma_f32_16x16x32_bf16 v[88:91],a[88:91],v[252:255],v[88:91]\n"
"ds_read_b128 v[252:255],v113 offset:23552\n"
"v_mfma_f32_16x16x32_bf16 v[88:91],a[80:83],v[248:251],v[88:91]\n"
"ds_read_b128 v[248:251],v113 offset:22528\n"
"v_mfma_f32_16x16x32_bf16 v[88:91],a[72:75],v[244:247],v[88:91]\n"
"ds_read_b128 v[244:247],v113 offset:21504\n"
"v_mfma_f32_16x16x32_bf16 v[88:91],a[156:159],v[240:243],v[88:91]\n"
"ds_read_b128 v[240:243],v113 offset:20480\n"
"s_waitcnt lgkmcnt(12)\n"
"v_mfma_f32_16x16x32_bf16 v[80:83],v[128:131],v[192:195],v[80:83]\n"
"buffer_load_dwordx4 v112,s[24:27],s18 offen offset:3072 sc1 lds\n"
"v_mfma_f32_16x16x32_bf16 v[80:83],v[136:139],v[196:199],v[80:83]\n"
"v_mfma_f32_16x16x32_bf16 v[80:83],v[144:147],v[200:203],v[80:83]\n"
"s_add_u32 m0,m0,4096\n"
"v_mfma_f32_16x16x32_bf16 v[80:83],v[152:155],v[204:207],v[80:83]\n"
"buffer_load_dwordx4 v117,s[24:27],s18 offen offset:0 sc1 lds\n"
"v_mfma_f32_16x16x32_bf16 v[84:87],v[156:159],v[204:207],v[84:87]\n"
"ds_read_b128 v[204:207],v113 offset:11264\n"
"s_add_u32 s20,s20,32768\n"
"v_mfma_f32_16x16x32_bf16 v[84:87],v[148:151],v[200:203],v[84:87]\n"
"ds_read_b128 v[200:203],v113 offset:10240\n"
"s_and_b32 s20,s20,0x1ffff\n"
"v_mfma_f32_16x16x32_bf16 v[84:87],v[140:143],v[196:199],v[84:87]\n"
"ds_read_b128 v[196:199],v113 offset:9216\n"
"v_mfma_f32_16x16x32_bf16 v[84:87],v[132:135],v[192:195],v[84:87]\n"
"ds_read_b128 v[192:195],v113 offset:8192\n"
"s_waitcnt lgkmcnt(12)\n"
"v_mfma_f32_16x16x32_bf16 v[68:71],v[132:135],v[208:211],v[68:71]\n"
"buffer_load_dwordx4 v117,s[24:27],s18 offen offset:1024 sc1 lds\n"
"v_mfma_f32_16x16x32_bf16 v[68:71],v[140:143],v[212:215],v[68:71]\n"
"v_mfma_f32_16x16x32_bf16 v[68:71],v[148:151],v[216:219],v[68:71]\n"
"v_mfma_f32_16x16x32_bf16 v[68:71],v[156:159],v[220:223],v[68:71]\n"
"buffer_load_dwordx4 v117,s[24:27],s18 offen offset:2048 sc1 lds\n"
"v_mfma_f32_16x16x32_bf16 v[64:67],v[152:155],v[220:223],v[64:67]\n"
"ds_read_b128 v[220:223],v113 offset:27648\n"
"v_xor_b32 v119,s80,v122\n"
"v_mfma_f32_16x16x32_bf16 v[64:67],v[144:147],v[216:219],v[64:67]\n"
"ds_read_b128 v[216:219],v113 offset:26624\n"
"v_xor_b32 v120,s81,v123\n"
"v_mfma_f32_16x16x32_bf16 v[64:67],v[136:139],v[212:215],v[64:67]\n"
"ds_read_b128 v[212:215],v113 offset:25600\n"
"v_or_b32 v119,v119,v120\n"
"v_mfma_f32_16x16x32_bf16 v[64:67],v[128:131],v[208:211],v[64:67]\n"
"ds_read_b128 v[208:211],v113 offset:24576\n"
"s_waitcnt lgkmcnt(12)\n"
"v_mfma_f32_16x16x32_bf16 v[80:83],v[0:3],v[224:227],v[80:83]\n"
"buffer_load_dwordx4 v117,s[24:27],s18 offen offset:3072 sc1 lds\n"
"v_cmp_ne_u32 s[28:29],0,v119\n"
"v_mfma_f32_16x16x32_bf16 v[80:83],v[8:11],v[228:231],v[80:83]\n"
"s_and_b64 s[14:15],s[14:15],s[28:29]\n"
"s_cselect_b32 s91,s11,2146435072\n"
"s_add_u32 m0,s30,32768\n"
"v_mfma_f32_16x16x32_bf16 v[80:83],v[160:163],v[232:235],v[80:83]\n"
"buffer_load_dwordx2 v[122:123],v115,s[24:27],s91 offen sc1\n"
"v_mfma_f32_16x16x32_bf16 v[80:83],v[168:171],v[236:239],v[80:83]\n"
"buffer_load_dwordx4 v112,s[24:27],s8 offen offset:0 sc1 lds\n"
"v_mfma_f32_16x16x32_bf16 v[84:87],v[172:175],v[236:239],v[84:87]\n"
"ds_read_b128 v[236:239],v113 offset:15360\n"
"v_mfma_f32_16x16x32_bf16 v[84:87],v[164:167],v[232:235],v[84:87]\n"
"ds_read_b128 v[232:235],v113 offset:14336\n"
"v_mfma_f32_16x16x32_bf16 v[84:87],v[12:15],v[228:231],v[84:87]\n"
"ds_read_b128 v[228:231],v113 offset:13312\n"
"v_mfma_f32_16x16x32_bf16 v[84:87],v[4:7],v[224:227],v[84:87]\n"
"s_and_b32 s83,s15,0xff0000\n"
"ds_read_b128 v[224:227],v113 offset:12288\n"
"s_waitcnt lgkmcnt(12)\n"
"v_mfma_f32_16x16x32_bf16 v[68:71],v[4:7],v[240:243],v[68:71]\n"
"buffer_load_dwordx4 v112,s[24:27],s8 offen offset:1024 sc1 lds\n"
"v_mfma_f32_16x16x32_bf16 v[68:71],v[12:15],v[244:247],v[68:71]\n"
"s_cbranch_scc1 .Lfk_es_lkonem\n"
".Lfk_er_lkonem:\n"
"v_mfma_f32_16x16x32_bf16 v[68:71],v[164:167],v[248:251],v[68:71]\n"
"v_mfma_f32_16x16x32_bf16 v[68:71],v[172:175],v[252:255],v[68:71]\n"
"buffer_load_dwordx4 v112,s[24:27],s8 offen offset:2048 sc1 lds\n"
"v_mfma_f32_16x16x32_bf16 v[64:67],v[168:171],v[252:255],v[64:67]\n"
"ds_read_b128 v[252:255],v113 offset:31744\n"
"v_mfma_f32_16x16x32_bf16 v[64:67],v[160:163],v[248:251],v[64:67]\n"
"ds_read_b128 v[248:251],v113 offset:30720\n"
"v_mfma_f32_16x16x32_bf16 v[64:67],v[8:11],v[244:247],v[64:67]\n"
"ds_read_b128 v[244:247],v113 offset:29696\n"
"v_mfma_f32_16x16x32_bf16 v[64:67],v[0:3],v[240:243],v[64:67]\n"
"ds_read_b128 v[240:243],v113 offset:28672\n"
"s_waitcnt lgkmcnt(12)\n"
"v_mfma_f32_16x16x32_bf16 v[80:83],v[176:179],v[192:195],v[80:83]\n"
"s_waitcnt vmcnt(13)\n"
"ds_add_u32 v114,v116\n"
"v_mfma_f32_16x16x32_bf16 v[80:83],v[184:187],v[196:199],v[80:83]\n"
"buffer_load_dwordx4 v112,s[24:27],s8 offen offset:3072 sc1 lds\n"
"v_mfma_f32_16x16x32_bf16 v[80:83],a[48:51],v[200:203],v[80:83]\n"
"s_add_u32 m0,m0,4096\n"
"v_mfma_f32_16x16x32_bf16 v[80:83],a[56:59],v[204:207],v[80:83]\n"
"s_add_u32 s83,s20,32768\n"
"s_and_b32 s83,s83,0x1ffff\n"
"v_add_u32 v113,s83,v112\n"
"v_mfma_f32_16x16x32_bf16 v[84:87],a[60:63],v[204:207],v[84:87]\n"
"buffer_load_dwordx4 v117,s[24:27],s8 offen offset:0 sc1 lds\n"
"v_mfma_f32_16x16x32_bf16 v[84:87],a[52:55],v[200:203],v[84:87]\n"
"s_cmp_eq_u32 s52,0\n"
"v_not_b32 v104,s80\n"
"s_cselect_b32 s9,s9,2146435072\n"
"v_mfma_f32_16x16x32_bf16 v[84:87],v[188:191],v[196:199],v[84:87]\n"
"buffer_load_dwordx2 v[104:105],v115,s[24:27],s9 offen sc1\n"
"v_mfma_f32_16x16x32_bf16 v[84:87],v[180:183],v[192:195],v[84:87]\n"
"buffer_load_dwordx4 v117,s[24:27],s8 offen offset:1024 sc1 lds\n"
"s_waitcnt lgkmcnt(9)\n"
"v_mfma_f32_16x16x32_bf16 v[68:71],v[180:183],v[208:211],v[68:71]\n"
"v_mfma_f32_16x16x32_bf16 v[68:71],v[188:191],v[212:215],v[68:71]\n"
"ds_read_b32 v125,v114\n"
"v_mfma_f32_16x16x32_bf16 v[68:71],a[52:55],v[216:219],v[68:71]\n"
"buffer_load_dwordx4 v117,s[24:27],s8 offen offset:2048 sc1 lds\n"
"v_mfma_f32_16x16x32_bf16 v[68:71],a[60:63],v[220:223],v[68:71]\n"
"v_mfma_f32_16x16x32_bf16 v[64:67],a[56:59],v[220:223],v[64:67]\n"
"v_mfma_f32_16x16x32_bf16 v[64:67],a[48:51],v[216:219],v[64:67]\n"
"s_waitcnt lgkmcnt(0)\n"
"v_cmp_gt_u32 vcc,s16,v125\n"
"v_mfma_f32_16x16x32_bf16 v[64:67],v[184:187],v[212:215],v[64:67]\n"
"buffer_load_dwordx4 v117,s[24:27],s8 offen offset:3072 sc1 lds\n"
"v_mfma_f32_16x16x32_bf16 v[64:67],v[176:179],v[208:211],v[64:67]\n"
"s_lshl_b64 exec,1,s89\n"
"buffer_atomic_add v100,off,s[24:27],s10 sc0\n"
"s_mov_b64 exec,-1\n"
"s_add_u32 s8,s8,1048576\n"
"s_cbranch_vccnz .Lfk_es_mone\n"
".Lfk_er_mone:\n"
"v_mfma_f32_16x16x32_bf16 v[80:83],v[48:51],v[224:227],v[80:83]\n"
"ds_read_b128 v[192:195],v113 offset:0\n"
"v_mfma_f32_16x16x32_bf16 v[80:83],v[56:59],v[228:231],v[80:83]\n"
"ds_read_b128 v[196:199],v113 offset:1024\n"
"v_mfma_f32_16x16x32_bf16 v[80:83],v[16:19],v[232:235],v[80:83]\n"
"ds_read_b128 v[200:203],v113 offset:2048\n"
"s_add_u32 s89,s89,1\n"
"v_mfma_f32_16x16x32_bf16 v[80:83],v[24:27],v[236:239],v[80:83]\n"
"ds_read_b128 v[204:207],v113 offset:3072\n"
"s_add_u32 s21,s21,8\n"
"v_mfma_f32_16x16x32_bf16 v[84:87],v[28:31],v[236:239],v[84:87]\n"
"ds_read_b128 v[208:211],v113 offset:4096\n"
"v_mfma_f32_16x16x32_bf16 v[84:87],v[20:23],v[232:235],v[84:87]\n"
"ds_read_b128 v[212:215],v113 offset:5120\n"
"s_add_u32 s16,s16,4\n"
"v_mfma_f32_16x16x32_bf16 v[84:87],v[60:63],v[228:231],v[84:87]\n"
"ds_read_b128 v[216:219],v113 offset:6144\n"
"v_mfma_f32_16x16x32_bf16 v[84:87],v[52:55],v[224:227],v[84:87]\n"
"ds_read_b128 v[220:223],v113 offset:7168\n"
"v_mfma_f32_16x16x32_bf16 v[68:71],v[52:55],v[240:243],v[68:71]\n"
"ds_read_b128 v[236:239],v113 offset:11264\n"
"v_mfma_f32_16x16x32_bf16 v[68:71],v[60:63],v[244:247],v[68:71]\n"
"ds_read_b128 v[232:235],v113 offset:10240\n"
"v_mfma_f32_16x16x32_bf16 v[68:71],v[20:23],v[248:251],v[68:71]\n"
"ds_read_b128 v[228:231],v113 offset:9216\n"
"ds_read_b128 v[224:227],v113 offset:8192\n"
"v_mfma_f32_16x16x32_bf16 v[68:71],v[28:31],v[252:255],v[68:71]\n"
"v_cvt_pk_bf16_f32 v80,v80,v81\n"
"v_cvt_pk_bf16_f32 v81,v82,v83\n"
"v_mfma_f32_16x16x32_bf16 v[64:67],v[24:27],v[252:255],v[64:67]\n"
"ds_read_b128 v[252:255],v113 offset:15360\n"
"v_cvt_pk_bf16_f32 v82,v84,v85\n"
"v_cvt_pk_bf16_f32 v83,v86,v87\n"
"v_mfma_f32_16x16x32_bf16 v[64:67],v[16:19],v[248:251],v[64:67]\n"
"ds_read_b128 v[248:251],v113 offset:14336\n"
"v_max_u32 v80,1,v80\n"
"v_max_u32 v81,1,v81\n"
"v_mfma_f32_16x16x32_bf16 v[64:67],v[56:59],v[244:247],v[64:67]\n"
"ds_read_b128 v[244:247],v113 offset:13312\n"
"v_max_u32 v82,1,v82\n"
"v_max_u32 v83,1,v83\n"
"v_mfma_f32_16x16x32_bf16 v[64:67],v[48:51],v[240:243],v[64:67]\n"
"ds_read_b128 v[240:243],v113 offset:12288\n"
"buffer_store_dwordx4 v[80:83],v118,s[24:27],s22 offen sc1\n"
"s_waitcnt lgkmcnt(12)\n"
"v_mfma_f32_16x16x32_bf16 v[72:75],v[128:131],v[192:195],v[72:75]\n"
"s_add_u32 m0,s30,s20\n"
"s_add_u32 s22,s22,262144\n"
"v_mfma_f32_16x16x32_bf16 v[72:75],v[136:139],v[196:199],v[72:75]\n"
"buffer_load_dwordx4 v112,s[24:27],s8 offen offset:0 sc1 lds\n"
"v_mfma_f32_16x16x32_bf16 v[72:75],v[144:147],v[200:203],v[72:75]\n"
"v_mfma_f32_16x16x32_bf16 v[72:75],v[152:155],v[204:207],v[72:75]\n"
"v_cvt_pk_bf16_f32 v64,v64,v65\n"
"v_mfma_f32_16x16x32_bf16 v[76:79],v[156:159],v[204:207],v[76:79]\n"
"ds_read_b128 v[204:207],v113 offset:19456\n"
"s_add_u32 s20,s20,32768\n"
"v_mfma_f32_16x16x32_bf16 v[76:79],v[148:151],v[200:203],v[76:79]\n"
"ds_read_b128 v[200:203],v113 offset:18432\n"
"s_and_b32 s20,s20,0x1ffff\n"
"v_mfma_f32_16x16x32_bf16 v[76:79],v[140:143],v[196:199],v[76:79]\n"
"ds_read_b128 v[196:199],v113 offset:17408\n"
"v_cvt_pk_bf16_f32 v65,v66,v67\n"
"v_mfma_f32_16x16x32_bf16 v[76:79],v[132:135],v[192:195],v[76:79]\n"
"ds_read_b128 v[192:195],v113 offset:16384\n"
"v_cvt_pk_bf16_f32 v66,v68,v69\n"
"s_waitcnt lgkmcnt(12)\n"
"v_mfma_f32_16x16x32_bf16 v[76:79],v[4:7],v[208:211],v[76:79]\n"
"buffer_load_dwordx4 v112,s[24:27],s8 offen offset:1024 sc1 lds\n"
"v_cvt_pk_bf16_f32 v67,v70,v71\n"
"v_mfma_f32_16x16x32_bf16 v[76:79],v[12:15],v[212:215],v[76:79]\n"
"v_max_u32 v64,1,v64\n"
"v_max_u32 v65,1,v65\n"
"v_max_u32 v66,1,v66\n"
"v_max_u32 v67,1,v67\n"
"v_mfma_f32_16x16x32_bf16 v[76:79],v[164:167],v[216:219],v[76:79]\n"
"buffer_store_dwordx4 v[64:67],v118,s[24:27],s22 offen sc1\n"
"v_mfma_f32_16x16x32_bf16 v[76:79],v[172:175],v[220:223],v[76:79]\n"
"buffer_load_dwordx4 v112,s[24:27],s8 offen offset:2048 sc1 lds\n"
"v_mfma_f32_16x16x32_bf16 v[72:75],v[168:171],v[220:223],v[72:75]\n"
"s_add_u32 s22,s22,262144\n"
"ds_read_b128 v[220:223],v113 offset:23552\n"
"v_mfma_f32_16x16x32_bf16 v[72:75],v[160:163],v[216:219],v[72:75]\n"
"ds_read_b128 v[216:219],v113 offset:22528\n"
"v_mfma_f32_16x16x32_bf16 v[72:75],v[8:11],v[212:215],v[72:75]\n"
"ds_read_b128 v[212:215],v113 offset:21504\n"
"v_mfma_f32_16x16x32_bf16 v[72:75],v[0:3],v[208:211],v[72:75]\n"
"ds_read_b128 v[208:211],v113 offset:20480\n"
"s_waitcnt lgkmcnt(12)\n"
"v_mfma_f32_16x16x32_bf16 v[72:75],v[176:179],v[224:227],v[72:75]\n"
"buffer_load_dwordx4 v112,s[24:27],s8 offen offset:3072 sc1 lds\n"
"v_mfma_f32_16x16x32_bf16 v[72:75],v[184:187],v[228:231],v[72:75]\n"
"v_xor_b32 v119,s80,v122\n"
"v_xor_b32 v120,s81,v123\n"
"v_mfma_f32_16x16x32_bf16 v[72:75],a[48:51],v[232:235],v[72:75]\n"
"v_or_b32 v119,v119,v120\n"
"v_cmp_ne_u32 s[28:29],0,v119\n"
"v_mfma_f32_16x16x32_bf16 v[72:75],a[56:59],v[236:239],v[72:75]\n"
"s_and_b64 s[14:15],s[14:15],s[28:29]\n"
"v_mfma_f32_16x16x32_bf16 v[76:79],a[60:63],v[236:239],v[76:79]\n"
"ds_read_b128 v[236:239],v113 offset:27648\n"
"v_mfma_f32_16x16x32_bf16 v[76:79],a[52:55],v[232:235],v[76:79]\n"
"ds_read_b128 v[232:235],v113 offset:26624\n"
"v_mfma_f32_16x16x32_bf16 v[76:79],v[188:191],v[228:231],v[76:79]\n"
"ds_read_b128 v[228:231],v113 offset:25600\n"
"v_mfma_f32_16x16x32_bf16 v[76:79],v[180:183],v[224:227],v[76:79]\n"
"s_add_u32 m0,m0,4096\n"
"ds_read_b128 v[224:227],v113 offset:24576\n"
"s_waitcnt lgkmcnt(12)\n"
"v_mfma_f32_16x16x32_bf16 v[76:79],v[52:55],v[240:243],v[76:79]\n"
"buffer_load_dwordx4 v117,s[24:27],s8 offen offset:0 sc1 lds\n"
"v_mfma_f32_16x16x32_bf16 v[76:79],v[60:63],v[244:247],v[76:79]\n"
"s_and_b32 s83,s15,0xff000000\n"
"s_cbranch_scc1 .Lfk_es_lktwom\n"
".Lfk_er_lktwom:\n"
"v_mfma_f32_16x16x32_bf16 v[76:79],v[20:23],v[248:251],v[76:79]\n"
"v_xor_b32 v119,s80,v104\n"
"v_xor_b32 v120,s81,v105\n"
"v_or_b32 v119,v119,v120\n"
"v_mfma_f32_16x16x32_bf16 v[76:79],v[28:31],v[252:255],v[76:79]\n"
"buffer_load_dwordx4 v117,s[24:27],s8 offen offset:1024 sc1 lds\n"
"v_cmp_ne_u32 s[28:29],0,v119\n"
"v_mfma_f32_16x16x32_bf16 v[72:75],v[24:27],v[252:255],v[72:75]\n"
"s_cmp_eq_u32 s52,0\n"
"ds_read_b128 v[252:255],v113 offset:31744\n"
"s_cselect_b64 s[14:15],-1,0\n"
"v_mfma_f32_16x16x32_bf16 v[72:75],v[16:19],v[248:251],v[72:75]\n"
"s_mov_b32 s11,s9\n"
"ds_read_b128 v[248:251],v113 offset:30720\n"
"s_waitcnt vmcnt(18)\n"
"v_not_b32 v122,s80\n"
"v_mfma_f32_16x16x32_bf16 v[72:75],v[56:59],v[244:247],v[72:75]\n"
"s_and_b64 s[14:15],s[14:15],s[28:29]\n"
"s_cselect_b32 s91,s11,2146435072\n"
"ds_read_b128 v[244:247],v113 offset:29696\n"
"buffer_load_dwordx2 v[122:123],v115,s[24:27],s91 offen sc1\n"
"v_mfma_f32_16x16x32_bf16 v[72:75],v[48:51],v[240:243],v[72:75]\n"
"ds_read_b128 v[240:243],v113 offset:28672\n"
"s_waitcnt lgkmcnt(12)\n"
"v_mfma_f32_16x16x32_bf16 v[88:91],v[128:131],v[192:195],v[88:91]\n"
"ds_write_b32 v101,v100\n"
"v_mfma_f32_16x16x32_bf16 v[88:91],v[136:139],v[196:199],v[88:91]\n"
"ds_add_u32 v114,v116\n"
"v_mfma_f32_16x16x32_bf16 v[88:91],v[144:147],v[200:203],v[88:91]\n"
"buffer_load_dwordx4 v117,s[24:27],s8 offen offset:2048 sc1 lds\n"
"v_mfma_f32_16x16x32_bf16 v[88:91],v[152:155],v[204:207],v[88:91]\n"
"s_add_u32 s83,s20,32768\n"
"s_and_b32 s83,s83,0x1ffff\n"
"v_add_u32 v113,s83,v112\n"
"v_mfma_f32_16x16x32_bf16 v[92:95],v[156:159],v[204:207],v[92:95]\n"
"v_cvt_pk_bf16_f32 v72,v72,v73\n"
"v_cvt_pk_bf16_f32 v73,v74,v75\n"
"v_cvt_pk_bf16_f32 v74,v76,v77\n"
"v_mfma_f32_16x16x32_bf16 v[92:95],v[148:151],v[200:203],v[92:95]\n"
"v_cvt_pk_bf16_f32 v75,v78,v79\n"
"v_max_u32 v72,1,v72\n"
"v_max_u32 v73,1,v73\n"
"v_mfma_f32_16x16x32_bf16 v[92:95],v[140:143],v[196:199],v[92:95]\n"
"v_max_u32 v74,1,v74\n"
"v_max_u32 v75,1,v75\n"
"v_mfma_f32_16x16x32_bf16 v[92:95],v[132:135],v[192:195],v[92:95]\n"
"buffer_store_dwordx4 v[72:75],v118,s[24:27],s22 offen sc1\n"
"s_waitcnt lgkmcnt(10)\n"
"v_mfma_f32_16x16x32_bf16 v[92:95],v[4:7],v[208:211],v[92:95]\n"
"buffer_load_dwordx4 v117,s[24:27],s8 offen offset:3072 sc1 lds\n"
"v_mfma_f32_16x16x32_bf16 v[92:95],v[12:15],v[212:215],v[92:95]\n"
"s_add_u32 s22,s22,262144\n"
"ds_read_b32 v125,v114\n"
"s_add_u32 s77,s77,4096\n"
"s_add_u32 m0,s30,135168\n"
"v_mfma_f32_16x16x32_bf16 v[92:95],v[164:167],v[216:219],v[92:95]\n"
"buffer_load_dwordx4 v98,s[40:43],s77 offen offset:0 nt lds\n"
"v_mfma_f32_16x16x32_bf16 v[92:95],v[172:175],v[220:223],v[92:95]\n"
"buffer_load_dwordx4 v98,s[40:43],s77 offen offset:1024 nt lds\n"
"v_mfma_f32_16x16x32_bf16 v[88:91],v[168:171],v[220:223],v[88:91]\n"
"s_lshl_b64 exec,1,s89\n"
"buffer_atomic_add v100,off,s[24:27],s10 sc0\n"
"s_mov_b64 exec,-1\n"
"v_mfma_f32_16x16x32_bf16 v[88:91],v[160:163],v[216:219],v[88:91]\n"
"s_waitcnt lgkmcnt(0)\n"
"buffer_load_dwordx4 v99,s[40:43],s77 offen offset:2048 nt lds\n"
"v_cmp_gt_u32 vcc,s16,v125\n"
"v_mfma_f32_16x16x32_bf16 v[88:91],v[8:11],v[212:215],v[88:91]\n"
"buffer_load_dwordx4 v99,s[40:43],s77 offen offset:3072 nt lds\n"
"v_mfma_f32_16x16x32_bf16 v[88:91],v[0:3],v[208:211],v[88:91]\n"
"s_add_u32 s8,s8,1048576\n"
"s_cbranch_vccnz .Lfk_es_mtwo\n"
".Lfk_er_mtwo:\n"
".Lfk_l4:\n"
"v_mfma_f32_16x16x32_bf16 v[88:91],v[176:179],v[224:227],v[88:91]\n"
"ds_read_b128 v[192:195],v113 offset:0\n"
"v_mfma_f32_16x16x32_bf16 v[88:91],v[184:187],v[228:231],v[88:91]\n"
"ds_read_b128 v[196:199],v113 offset:1024\n"
"v_mfma_f32_16x16x32_bf16 v[88:91],a[48:51],v[232:235],v[88:91]\n"
"ds_read_b128 v[200:203],v113 offset:2048\n"
"s_add_u32 s89,s89,1\n"
"v_mfma_f32_16x16x32_bf16 v[88:91],a[56:59],v[236:239],v[88:91]\n"
"ds_read_b128 v[204:207],v113 offset:3072\n"
"v_mfma_f32_16x16x32_bf16 v[92:95],a[60:63],v[236:239],v[92:95]\n"
"ds_read_b128 v[208:211],v113 offset:4096\n"
"v_mfma_f32_16x16x32_bf16 v[92:95],a[52:55],v[232:235],v[92:95]\n"
"ds_read_b128 v[212:215],v113 offset:5120\n"
"s_add_u32 s16,s16,4\n"
"v_mfma_f32_16x16x32_bf16 v[92:95],v[188:191],v[228:231],v[92:95]\n"
"ds_read_b128 v[216:219],v113 offset:6144\n"
"v_mfma_f32_16x16x32_bf16 v[92:95],v[180:183],v[224:227],v[92:95]\n"
"ds_read_b128 v[220:223],v113 offset:7168\n"
"v_mfma_f32_16x16x32_bf16 v[92:95],v[52:55],v[240:243],v[92:95]\n"
"ds_read_b128 v[236:239],v113 offset:11264\n"
"v_mfma_f32_16x16x32_bf16 v[92:95],v[60:63],v[244:247],v[92:95]\n"
"ds_read_b128 v[232:235],v113 offset:10240\n"
"v_mfma_f32_16x16x32_bf16 v[92:95],v[20:23],v[248:251],v[92:95]\n"
"ds_read_b128 v[228:231],v113 offset:9216\n"
"v_mfma_f32_16x16x32_bf16 v[92:95],v[28:31],v[252:255],v[92:95]\n"
"ds_read_b128 v[224:227],v113 offset:8192\n"
"v_mfma_f32_16x16x32_bf16 v[88:91],v[24:27],v[252:255],v[88:91]\n"
"ds_read_b128 v[252:255],v113 offset:15360\n"
"v_mfma_f32_16x16x32_bf16 v[88:91],v[16:19],v[248:251],v[88:91]\n"
"ds_read_b128 v[248:251],v113 offset:14336\n"
"v_mfma_f32_16x16x32_bf16 v[88:91],v[56:59],v[244:247],v[88:91]\n"
"ds_read_b128 v[244:247],v113 offset:13312\n"
"v_mfma_f32_16x16x32_bf16 v[88:91],v[48:51],v[240:243],v[88:91]\n"
"ds_read_b128 v[240:243],v113 offset:12288\n"
"s_waitcnt lgkmcnt(12)\n"
"v_mfma_f32_16x16x32_bf16 v[80:83],a[224:227],v[192:195],0\n"
"s_add_u32 m0,s30,s20\n"
"ds_read_b32 v103,v102\n"
"v_mfma_f32_16x16x32_bf16 v[80:83],a[232:235],v[196:199],v[80:83]\n"
"buffer_load_dwordx4 v112,s[24:27],s8 offen offset:0 sc1 lds\n"
"v_mfma_f32_16x16x32_bf16 v[80:83],a[240:243],v[200:203],v[80:83]\n"
"v_add_u32 v102,4,v102\n"
"v_and_b32 v102,0x200ff,v102\n"
"v_mfma_f32_16x16x32_bf16 v[80:83],a[248:251],v[204:207],v[80:83]\n"
"v_cvt_pk_bf16_f32 v88,v88,v89\n"
"v_cvt_pk_bf16_f32 v89,v90,v91\n"
"v_mfma_f32_16x16x32_bf16 v[84:87],a[252:255],v[204:207],0\n"
"ds_read_b128 v[204:207],v113 offset:19456\n"
"s_add_u32 s20,s20,32768\n"
"v_mfma_f32_16x16x32_bf16 v[84:87],a[244:247],v[200:203],v[84:87]\n"
"ds_read_b128 v[200:203],v113 offset:18432\n"
"s_and_b32 s20,s20,0x1ffff\n"
"v_mfma_f32_16x16x32_bf16 v[84:87],a[236:239],v[196:199],v[84:87]\n"
"ds_read_b128 v[196:199],v113 offset:17408\n"
"v_cvt_pk_bf16_f32 v90,v92,v93\n"
"v_mfma_f32_16x16x32_bf16 v[84:87],a[228:231],v[192:195],v[84:87]\n"
"ds_read_b128 v[192:195],v113 offset:16384\n"
"s_waitcnt lgkmcnt(13)\n"
"v_mfma_f32_16x16x32_bf16 v[84:87],a[4:7],v[208:211],v[84:87]\n"
"buffer_load_dwordx4 v112,s[24:27],s8 offen offset:1024 sc1 lds\n"
"v_mfma_f32_16x16x32_bf16 v[84:87],a[12:15],v[212:215],v[84:87]\n"
"v_cvt_pk_bf16_f32 v91,v94,v95\n"
"v_max_u32 v88,1,v88\n"
"v_mfma_f32_16x16x32_bf16 v[84:87],a[20:23],v[216:219],v[84:87]\n"
"v_max_u32 v89,1,v89\n"
"v_max_u32 v90,1,v90\n"
"v_mfma_f32_16x16x32_bf16 v[84:87],a[28:31],v[220:223],v[84:87]\n"
"buffer_load_dwordx4 v112,s[24:27],s8 offen offset:2048 sc1 lds\n"
"v_mfma_f32_16x16x32_bf16 v[80:83],a[24:27],v[220:223],v[80:83]\n"
"ds_read_b128 v[220:223],v113 offset:23552\n"
"v_max_u32 v91,1,v91\n"
"v_mfma_f32_16x16x32_bf16 v[80:83],a[16:19],v[216:219],v[80:83]\n"
"ds_read_b128 v[216:219],v113 offset:22528\n"
"buffer_store_dwordx4 v[88:91],v118,s[24:27],s53 offen sc1\n"
"v_mfma_f32_16x16x32_bf16 v[80:83],a[8:11],v[212:215],v[80:83]\n"
"ds_read_b128 v[212:215],v113 offset:21504\n"
"v_mfma_f32_16x16x32_bf16 v[80:83],a[0:3],v[208:211],v[80:83]\n"
"ds_read_b128 v[208:211],v113 offset:20480\n"
"s_waitcnt lgkmcnt(13)\n"
"v_mfma_f32_16x16x32_bf16 v[80:83],v[32:35],v[224:227],v[80:83]\n"
"v_mfma_f32_16x16x32_bf16 v[80:83],v[40:43],v[228:231],v[80:83]\n"
"buffer_load_dwordx4 v112,s[24:27],s8 offen offset:3072 sc1 lds\n"
"v_mfma_f32_16x16x32_bf16 v[80:83],a[32:35],v[232:235],v[80:83]\n"
"s_waitcnt lgkmcnt(8)\n"
"v_readfirstlane_b32 s22,v103\n"
"v_mfma_f32_16x16x32_bf16 v[80:83],a[40:43],v[236:239],v[80:83]\n"
"v_mfma_f32_16x16x32_bf16 v[84:87],a[44:47],v[236:239],v[84:87]\n"
"ds_read_b128 v[236:239],v113 offset:27648\n"
"s_and_b32 s21,s22,0x38\n"
"v_mfma_f32_16x16x32_bf16 v[84:87],a[36:39],v[232:235],v[84:87]\n"
"ds_read_b128 v[232:235],v113 offset:26624\n"
"v_mfma_f32_16x16x32_bf16 v[84:87],v[44:47],v[228:231],v[84:87]\n"
"ds_read_b128 v[228:231],v113 offset:25600\n"
"v_mfma_f32_16x16x32_bf16 v[84:87],v[36:39],v[224:227],v[84:87]\n"
"ds_read_b128 v[224:227],v113 offset:24576\n"
"s_add_u32 m0,m0,4096\n"
"v_mfma_f32_16x16x32_bf16 v[84:87],a[68:71],v[240:243],v[84:87]\n"
"buffer_load_dwordx4 v117,s[24:27],s8 offen offset:0 sc1 lds\n"
"v_mfma_f32_16x16x32_bf16 v[84:87],a[76:79],v[244:247],v[84:87]\n"
"s_cmp_lg_u64 s[14:15],0\n"
"s_cbranch_scc1 .Lfk_es_lkloopm\n"
".Lfk_er_lkloopm:\n"
"v_mfma_f32_16x16x32_bf16 v[84:87],a[84:87],v[248:251],v[84:87]\n"
"v_mfma_f32_16x16x32_bf16 v[84:87],a[92:95],v[252:255],v[84:87]\n"
"buffer_load_dwordx4 v117,s[24:27],s8 offen offset:1024 sc1 lds\n"
"v_mfma_f32_16x16x32_bf16 v[80:83],a[88:91],v[252:255],v[80:83]\n"
"ds_read_b128 v[252:255],v113 offset:31744\n"
"s_mov_b32 s53,s56\n"
"v_mfma_f32_16x16x32_bf16 v[80:83],a[80:83],v[248:251],v[80:83]\n"
"ds_read_b128 v[248:251],v113 offset:30720\n"
"s_mov_b32 s56,s58\n"
"v_mfma_f32_16x16x32_bf16 v[80:83],a[72:75],v[244:247],v[80:83]\n"
"ds_read_b128 v[244:247],v113 offset:29696\n"
"s_mov_b32 s58,s59\n"
"v_mfma_f32_16x16x32_bf16 v[80:83],a[156:159],v[240:243],v[80:83]\n"
"ds_read_b128 v[240:243],v113 offset:28672\n"
"s_waitcnt lgkmcnt(12)\n"
"v_mfma_f32_16x16x32_bf16 v[88:91],v[128:131],v[192:195],v[80:83]\n"
"s_waitcnt vmcnt(17)\n"
"ds_write_b32 v101,v100\n"
"v_mfma_f32_16x16x32_bf16 v[88:91],v[136:139],v[196:199],v[88:91]\n"
"ds_add_u32 v114,v116\n"
"v_mfma_f32_16x16x32_bf16 v[88:91],v[144:147],v[200:203],v[88:91]\n"
"buffer_load_dwordx4 v117,s[24:27],s8 offen offset:2048 sc1 lds\n"
"v_mfma_f32_16x16x32_bf16 v[88:91],v[152:155],v[204:207],v[88:91]\n"
"s_add_u32 s83,s20,32768\n"
"s_and_b32 s83,s83,0x1ffff\n"
"v_add_u32 v113,s83,v112\n"
"v_mfma_f32_16x16x32_bf16 v[92:95],v[156:159],v[204:207],v[84:87]\n"
"s_cmp_lt_u32 s22,64\n"
"s_cselect_b32 s17,s16,-1\n"
"v_mfma_f32_16x16x32_bf16 v[92:95],v[148:151],v[200:203],v[92:95]\n"
"v_mfma_f32_16x16x32_bf16 v[92:95],v[140:143],v[196:199],v[92:95]\n"
"v_mfma_f32_16x16x32_bf16 v[92:95],v[132:135],v[192:195],v[92:95]\n"
"s_waitcnt lgkmcnt(10)\n"
"v_mfma_f32_16x16x32_bf16 v[92:95],v[4:7],v[208:211],v[92:95]\n"
"buffer_load_dwordx4 v117,s[24:27],s8 offen offset:3072 sc1 lds\n"
"v_mfma_f32_16x16x32_bf16 v[92:95],v[12:15],v[212:215],v[92:95]\n"
"s_lshr_b32 s59,s8,2\n"
"ds_read_b32 v125,v114\n"
"s_lshl_b32 s8,s22,17\n"
"v_mfma_f32_16x16x32_bf16 v[92:95],v[164:167],v[216:219],v[92:95]\n"
"s_add_u32 s8,s8,s31\n"
"v_mfma_f32_16x16x32_bf16 v[92:95],v[172:175],v[220:223],v[92:95]\n"
"v_mfma_f32_16x16x32_bf16 v[88:91],v[168:171],v[220:223],v[88:91]\n"
"s_lshl_b64 exec,1,s89\n"
"buffer_atomic_add v100,off,s[24:27],s10 sc0\n"
"s_mov_b64 exec,-1\n"
"v_mfma_f32_16x16x32_bf16 v[88:91],v[160:163],v[216:219],v[88:91]\n"
"s_waitcnt lgkmcnt(0)\n"
"v_cmp_gt_u32 vcc,s17,v125\n"
"v_mfma_f32_16x16x32_bf16 v[88:91],v[8:11],v[212:215],v[88:91]\n"
"v_mfma_f32_16x16x32_bf16 v[88:91],v[0:3],v[208:211],v[88:91]\n"
"s_cbranch_vccz .Lfk_l4\n"
"s_call_b64 s[94:95],.Lfk_meet4\n"
"s_cmp_lt_u32 s22,64\n"
"s_cbranch_scc1 .Lfk_l4\n"
"ds_read_b128 v[192:195],v113 offset:0\n"
"v_mfma_f32_16x16x32_bf16 v[88:91],v[176:179],v[224:227],v[88:91]\n"
"s_getpc_b64 s[96:97]\n"
".Lfk_rt2:\n"
"s_add_u32 s96,s96,.Lfk_tail-.Lfk_rt2\n"
"s_addc_u32 s97,s97,0\n"
"s_mov_b32 s98,.Lfk_cold-.Lfk_tail\n"
"s_mov_b32 s99,0x20000\n"
"s_lshr_b32 s82,s100,3\n"
"s_lshl2_add_u32 s82,s82,s52\n"
"v_lshrrev_b32 v121,2,v112\n"
"s_lshl_b32 s82,s82,8\n"
"v_mfma_f32_16x16x32_bf16 v[88:91],v[184:187],v[228:231],v[88:91]\n"
"buffer_load_dword v120,v121,s[96:99],s82 offen\n"
"ds_read_b128 v[196:199],v113 offset:1024\n"
"v_mfma_f32_16x16x32_bf16 v[88:91],a[48:51],v[232:235],v[88:91]\n"
"s_mov_b32 s38,4194304\n"
"ds_read_b128 v[200:203],v113 offset:2048\n"
"s_mov_b32 s39,0x20000\n"
"v_mfma_f32_16x16x32_bf16 v[88:91],a[56:59],v[236:239],v[88:91]\n"
"ds_read_b128 v[204:207],v113 offset:3072\n"
"s_mov_b32 s46,2048\n"
"v_mfma_f32_16x16x32_bf16 v[92:95],a[60:63],v[236:239],v[92:95]\n"
"ds_read_b128 v[208:211],v113 offset:4096\n"
"s_mov_b32 s47,0x20000\n"
"v_mfma_f32_16x16x32_bf16 v[92:95],a[52:55],v[232:235],v[92:95]\n"
"ds_read_b128 v[212:215],v113 offset:5120\n"
"s_add_u32 s16,s16,4\n"
"v_mfma_f32_16x16x32_bf16 v[92:95],v[188:191],v[228:231],v[92:95]\n"
"ds_read_b128 v[216:219],v113 offset:6144\n"
"s_mov_b32 s50,4194304\n"
"v_mfma_f32_16x16x32_bf16 v[92:95],v[180:183],v[224:227],v[92:95]\n"
"ds_read_b128 v[220:223],v113 offset:7168\n"
"s_mov_b32 s51,0x20000\n"
"v_mfma_f32_16x16x32_bf16 v[92:95],v[52:55],v[240:243],v[92:95]\n"
"ds_read_b128 v[236:239],v113 offset:11264\n"
"s_and_b32 s60,s100,7\n"
"v_mfma_f32_16x16x32_bf16 v[92:95],v[60:63],v[244:247],v[92:95]\n"
"ds_read_b128 v[232:235],v113 offset:10240\n"
"s_lshl_b32 s60,s60,5\n"
"v_mfma_f32_16x16x32_bf16 v[92:95],v[20:23],v[248:251],v[92:95]\n"
"ds_read_b128 v[228:231],v113 offset:9216\n"
"s_lshr_b32 s73,s100,3\n"
"v_mfma_f32_16x16x32_bf16 v[92:95],v[28:31],v[252:255],v[92:95]\n"
"ds_read_b128 v[224:227],v113 offset:8192\n"
"s_add_u32 s73,s73,s60\n"
"v_mfma_f32_16x16x32_bf16 v[88:91],v[24:27],v[252:255],v[88:91]\n"
"ds_read_b128 v[252:255],v113 offset:15360\n"
"s_bfe_u32 s60,s100,0x00003\n"
"v_mfma_f32_16x16x32_bf16 v[88:91],v[16:19],v[248:251],v[88:91]\n"
"ds_read_b128 v[248:251],v113 offset:14336\n"
"s_xor_b32 s60,s60,s100\n"
"v_mfma_f32_16x16x32_bf16 v[88:91],v[56:59],v[244:247],v[88:91]\n"
"ds_read_b128 v[244:247],v113 offset:13312\n"
"s_lshl2_add_u32 s60,s60,s52\n"
"v_mfma_f32_16x16x32_bf16 v[88:91],v[48:51],v[240:243],v[88:91]\n"
"ds_read_b128 v[240:243],v113 offset:12288\n"
"s_waitcnt lgkmcnt(12)\n"
"v_mfma_f32_16x16x32_bf16 v[80:83],a[224:227],v[192:195],0\n"
"s_lshl_b32 s61,s60,11\n"
"s_lshr_b32 s62,s60,4\n"
"v_mfma_f32_16x16x32_bf16 v[80:83],a[232:235],v[196:199],v[80:83]\n"
"s_lshl_b32 s62,s62,15\n"
"s_and_b32 s91,s60,15\n"
"v_mfma_f32_16x16x32_bf16 v[80:83],a[240:243],v[200:203],v[80:83]\n"
"s_lshl_b32 s91,s91,6\n"
"v_lshlrev_b32 v102,2,v112\n"
"v_mfma_f32_16x16x32_bf16 v[80:83],a[248:251],v[204:207],v[80:83]\n"
"v_cvt_pk_bf16_f32 v88,v88,v89\n"
"v_cvt_pk_bf16_f32 v89,v90,v91\n"
"v_mfma_f32_16x16x32_bf16 v[84:87],a[252:255],v[204:207],0\n"
"ds_read_b128 v[204:207],v113 offset:19456\n"
"s_add_u32 s20,s20,32768\n"
"v_mfma_f32_16x16x32_bf16 v[84:87],a[244:247],v[200:203],v[84:87]\n"
"ds_read_b128 v[200:203],v113 offset:18432\n"
"s_and_b32 s20,s20,0x1ffff\n"
"v_mfma_f32_16x16x32_bf16 v[84:87],a[236:239],v[196:199],v[84:87]\n"
"ds_read_b128 v[196:199],v113 offset:17408\n"
"v_cvt_pk_bf16_f32 v90,v92,v93\n"
"v_mfma_f32_16x16x32_bf16 v[84:87],a[228:231],v[192:195],v[84:87]\n"
"ds_read_b128 v[192:195],v113 offset:16384\n"
"s_waitcnt lgkmcnt(12)\n"
"v_mfma_f32_16x16x32_bf16 v[84:87],a[4:7],v[208:211],v[84:87]\n"
"v_cvt_pk_bf16_f32 v91,v94,v95\n"
"v_max_u32 v88,1,v88\n"
"v_mfma_f32_16x16x32_bf16 v[84:87],a[12:15],v[212:215],v[84:87]\n"
"v_max_u32 v89,1,v89\n"
"v_max_u32 v90,1,v90\n"
"v_max_u32 v91,1,v91\n"
"v_mfma_f32_16x16x32_bf16 v[84:87],a[20:23],v[216:219],v[84:87]\n"
"buffer_store_dwordx4 v[88:91],v118,s[24:27],s53 offen sc1\n"
"v_mfma_f32_16x16x32_bf16 v[84:87],a[28:31],v[220:223],v[84:87]\n"
"v_mfma_f32_16x16x32_bf16 v[80:83],a[24:27],v[220:223],v[80:83]\n"
"ds_read_b128 v[220:223],v113 offset:23552\n"
"v_mfma_f32_16x16x32_bf16 v[80:83],a[16:19],v[216:219],v[80:83]\n"
"ds_read_b128 v[216:219],v113 offset:22528\n"
"v_mfma_f32_16x16x32_bf16 v[80:83],a[8:11],v[212:215],v[80:83]\n"
"ds_read_b128 v[212:215],v113 offset:21504\n"
"v_mfma_f32_16x16x32_bf16 v[80:83],a[0:3],v[208:211],v[80:83]\n"
"ds_read_b128 v[208:211],v113 offset:20480\n"
"s_waitcnt lgkmcnt(12)\n"
"v_mfma_f32_16x16x32_bf16 v[80:83],v[32:35],v[224:227],v[80:83]\n"
"v_mfma_f32_16x16x32_bf16 v[80:83],v[40:43],v[228:231],v[80:83]\n"
"v_mfma_f32_16x16x32_bf16 v[80:83],a[32:35],v[232:235],v[80:83]\n"
"v_mfma_f32_16x16x32_bf16 v[80:83],a[40:43],v[236:239],v[80:83]\n"
"v_mfma_f32_16x16x32_bf16 v[84:87],a[44:47],v[236:239],v[84:87]\n"
"ds_read_b128 v[236:239],v113 offset:27648\n"
"v_mfma_f32_16x16x32_bf16 v[84:87],a[36:39],v[232:235],v[84:87]\n"
"ds_read_b128 v[232:235],v113 offset:26624\n"
"v_mfma_f32_16x16x32_bf16 v[84:87],v[44:47],v[228:231],v[84:87]\n"
"ds_read_b128 v[228:231],v113 offset:25600\n"
"v_mfma_f32_16x16x32_bf16 v[84:87],v[36:39],v[224:227],v[84:87]\n"
"ds_read_b128 v[224:227],v113 offset:24576\n"
"s_waitcnt lgkmcnt(12)\n"
"v_mfma_f32_16x16x32_bf16 v[84:87],a[68:71],v[240:243],v[84:87]\n"
"s_add_u32 s62,s62,71303168\n"
"v_and_b32 v102,0x0,v102\n"
"v_xor_b32 v102,s91,v102\n"
"v_mfma_f32_16x16x32_bf16 v[84:87],a[76:79],v[244:247],v[84:87]\n"
"s_cmp_eq_u32 s42,0\n"
"s_cselect_b32 s72,0x3ff80,s23\n"
"v_lshrrev_b32 v126,6,v112\n"
"v_mfma_f32_16x16x32_bf16 v[84:87],a[84:87],v[248:251],v[84:87]\n"
"s_add_u32 s91,s61,2097152\n"
"v_and_or_b32 v103,v112,48,v102\n"
"v_lshl_add_u32 v126,v126,10,v103\n"
"v_mfma_f32_16x16x32_bf16 v[84:87],a[92:95],v[252:255],v[84:87]\n"
"v_add_u32 v117,16384,v126\n"
"v_mfma_f32_16x16x32_bf16 v[80:83],a[88:91],v[252:255],v[80:83]\n"
"ds_read_b128 v[252:255],v113 offset:31744\n"
"s_mov_b32 s53,s56\n"
"v_mfma_f32_16x16x32_bf16 v[80:83],a[80:83],v[248:251],v[80:83]\n"
"ds_read_b128 v[248:251],v113 offset:30720\n"
"s_mov_b32 s56,s58\n"
"v_mfma_f32_16x16x32_bf16 v[80:83],a[72:75],v[244:247],v[80:83]\n"
"ds_read_b128 v[244:247],v113 offset:29696\n"
"s_mov_b32 s58,s59\n"
"v_mfma_f32_16x16x32_bf16 v[80:83],a[156:159],v[240:243],v[80:83]\n"
"ds_read_b128 v[240:243],v113 offset:28672\n"
"s_waitcnt lgkmcnt(12)\n"
"v_mfma_f32_16x16x32_bf16 v[88:91],v[128:131],v[192:195],v[80:83]\n"
"s_waitcnt vmcnt(13)\n"
"ds_add_u32 v114,v116\n"
"v_mfma_f32_16x16x32_bf16 v[88:91],v[136:139],v[196:199],v[88:91]\n"
"v_mfma_f32_16x16x32_bf16 v[88:91],v[144:147],v[200:203],v[88:91]\n"
"v_mfma_f32_16x16x32_bf16 v[88:91],v[152:155],v[204:207],v[88:91]\n"
"s_add_u32 s83,s20,32768\n"
"s_and_b32 s83,s83,0x1ffff\n"
"v_add_u32 v113,s83,v112\n"
"v_mfma_f32_16x16x32_bf16 v[92:95],v[156:159],v[204:207],v[84:87]\n"
"v_mfma_f32_16x16x32_bf16 v[92:95],v[148:151],v[200:203],v[92:95]\n"
"v_mfma_f32_16x16x32_bf16 v[92:95],v[140:143],v[196:199],v[92:95]\n"
"v_mfma_f32_16x16x32_bf16 v[92:95],v[132:135],v[192:195],v[92:95]\n"
"s_waitcnt lgkmcnt(9)\n"
"v_mfma_f32_16x16x32_bf16 v[92:95],v[4:7],v[208:211],v[92:95]\n"
"v_mfma_f32_16x16x32_bf16 v[92:95],v[12:15],v[212:215],v[92:95]\n"
"ds_read_b32 v125,v114\n"
"v_mfma_f32_16x16x32_bf16 v[92:95],v[164:167],v[216:219],v[92:95]\n"
"v_mfma_f32_16x16x32_bf16 v[92:95],v[172:175],v[220:223],v[92:95]\n"
"v_mfma_f32_16x16x32_bf16 v[88:91],v[168:171],v[220:223],v[88:91]\n"
"v_mfma_f32_16x16x32_bf16 v[88:91],v[160:163],v[216:219],v[88:91]\n"
"s_waitcnt lgkmcnt(0)\n"
"v_cmp_gt_u32 vcc,s16,v125\n"
"v_mfma_f32_16x16x32_bf16 v[88:91],v[8:11],v[212:215],v[88:91]\n"
"v_mfma_f32_16x16x32_bf16 v[88:91],v[0:3],v[208:211],v[88:91]\n"
"s_cbranch_vccnz .Lfk_es_me3\n"
".Lfk_er_me3:\n"
"ds_read_b128 v[192:195],v113 offset:0\n"
"v_mfma_f32_16x16x32_bf16 v[88:91],v[176:179],v[224:227],v[88:91]\n"
"buffer_load_dwordx4 a[96:99],v112,s[36:39],s61 offen offset:0\n"
"v_mfma_f32_16x16x32_bf16 v[88:91],v[184:187],v[228:231],v[88:91]\n"
"ds_read_b128 v[196:199],v113 offset:1024\n"
"v_lshrrev_b32 v101,4,v112\n"
"v_mfma_f32_16x16x32_bf16 v[88:91],a[48:51],v[232:235],v[88:91]\n"
"ds_read_b128 v[200:203],v113 offset:2048\n"
"v_and_b32 v102,15,v101\n"
"v_mfma_f32_16x16x32_bf16 v[88:91],a[56:59],v[236:239],v[88:91]\n"
"ds_read_b128 v[204:207],v113 offset:3072\n"
"buffer_load_dwordx4 a[100:103],v112,s[36:39],s61 offen offset:1024\n"
"v_mfma_f32_16x16x32_bf16 v[92:95],a[60:63],v[236:239],v[92:95]\n"
"ds_read_b128 v[208:211],v113 offset:4096\n"
"v_lshrrev_b32 v101,4,v101\n"
"v_mfma_f32_16x16x32_bf16 v[92:95],a[52:55],v[232:235],v[92:95]\n"
"ds_read_b128 v[212:215],v113 offset:5120\n"
"s_add_u32 s16,s16,4\n"
"v_mfma_f32_16x16x32_bf16 v[92:95],v[188:191],v[228:231],v[92:95]\n"
"ds_read_b128 v[216:219],v113 offset:6144\n"
"buffer_load_dwordx4 a[104:107],v112,s[36:39],s91 offen offset:0\n"
"v_mfma_f32_16x16x32_bf16 v[92:95],v[180:183],v[224:227],v[92:95]\n"
"ds_read_b128 v[220:223],v113 offset:7168\n"
"v_lshlrev_b32 v101,2,v101\n"
"v_mfma_f32_16x16x32_bf16 v[92:95],v[52:55],v[240:243],v[92:95]\n"
"ds_read_b128 v[236:239],v113 offset:11264\n"
"v_sub_u32 v101,v102,v101\n"
"v_mfma_f32_16x16x32_bf16 v[92:95],v[60:63],v[244:247],v[92:95]\n"
"ds_read_b128 v[232:235],v113 offset:10240\n"
"buffer_load_dwordx4 a[108:111],v112,s[36:39],s91 offen offset:1024\n"
"v_mfma_f32_16x16x32_bf16 v[92:95],v[20:23],v[248:251],v[92:95]\n"
"ds_read_b128 v[228:231],v113 offset:9216\n"
"v_mov_b32 v64,0\n"
"v_mfma_f32_16x16x32_bf16 v[92:95],v[28:31],v[252:255],v[92:95]\n"
"ds_read_b128 v[224:227],v113 offset:8192\n"
"v_mov_b32 v65,0\n"
"v_mfma_f32_16x16x32_bf16 v[88:91],v[24:27],v[252:255],v[88:91]\n"
"ds_read_b128 v[252:255],v113 offset:15360\n"
"buffer_load_dwordx4 v[72:75],v112,s[44:47],0 offen offset:0\n"
"v_mfma_f32_16x16x32_bf16 v[88:91],v[16:19],v[248:251],v[88:91]\n"
"ds_read_b128 v[248:251],v113 offset:14336\n"
"v_mov_b32 v68,0\n"
"v_mfma_f32_16x16x32_bf16 v[88:91],v[56:59],v[244:247],v[88:91]\n"
"ds_read_b128 v[244:247],v113 offset:13312\n"
"v_mov_b32 v69,0\n"
"v_mfma_f32_16x16x32_bf16 v[88:91],v[48:51],v[240:243],v[88:91]\n"
"ds_read_b128 v[240:243],v113 offset:12288\n"
"buffer_load_dwordx4 v[76:79],v112,s[44:47],0 offen offset:1024\n"
"s_waitcnt lgkmcnt(12)\n"
"v_mfma_f32_16x16x32_bf16 v[80:83],a[224:227],v[192:195],0\n"
"v_mov_b32 v102,0x3f80\n"
"v_mov_b32 v103,0x3f800000\n"
"v_cmp_eq_u32 vcc,0,v101\n"
"v_mfma_f32_16x16x32_bf16 v[80:83],a[232:235],v[196:199],v[80:83]\n"
"s_nop 0\n"
"v_cndmask_b32 v66,0,v102,vcc\n"
"v_cmp_eq_u32 vcc,1,v101\n"
"v_mfma_f32_16x16x32_bf16 v[80:83],a[240:243],v[200:203],v[80:83]\n"
"s_mov_b32 s91,s62\n"
"v_cndmask_b32 v66,v66,v103,vcc\n"
"v_cmp_eq_u32 vcc,2,v101\n"
"v_mfma_f32_16x16x32_bf16 v[80:83],a[248:251],v[204:207],v[80:83]\n"
"s_nop 0\n"
"v_cndmask_b32 v67,0,v102,vcc\n"
"v_cvt_pk_bf16_f32 v88,v88,v89\n"
"v_cvt_pk_bf16_f32 v89,v90,v91\n"
"v_mfma_f32_16x16x32_bf16 v[84:87],a[252:255],v[204:207],0\n"
"ds_read_b128 v[204:207],v113 offset:19456\n"
"s_add_u32 s20,s20,32768\n"
"v_mfma_f32_16x16x32_bf16 v[84:87],a[244:247],v[200:203],v[84:87]\n"
"s_and_b32 s20,s20,0x1ffff\n"
"ds_read_b128 v[200:203],v113 offset:18432\n"
"buffer_load_dwordx4 a[112:115],v126,s[24:27],s91 offen sc1\n"
"v_mfma_f32_16x16x32_bf16 v[84:87],a[236:239],v[196:199],v[84:87]\n"
"ds_read_b128 v[196:199],v113 offset:17408\n"
"v_cvt_pk_bf16_f32 v90,v92,v93\n"
"v_mfma_f32_16x16x32_bf16 v[84:87],a[228:231],v[192:195],v[84:87]\n"
"ds_read_b128 v[192:195],v113 offset:16384\n"
"s_waitcnt lgkmcnt(12)\n"
"v_mfma_f32_16x16x32_bf16 v[84:87],a[4:7],v[208:211],v[84:87]\n"
"v_cvt_pk_bf16_f32 v91,v94,v95\n"
"v_max_u32 v88,1,v88\n"
"v_mfma_f32_16x16x32_bf16 v[84:87],a[12:15],v[212:215],v[84:87]\n"
"buffer_load_dwordx4 a[116:119],v117,s[24:27],s91 offen sc1\n"
"v_max_u32 v89,1,v89\n"
"v_max_u32 v90,1,v90\n"
"v_max_u32 v91,1,v91\n"
"v_mfma_f32_16x16x32_bf16 v[84:87],a[20:23],v[216:219],v[84:87]\n"
"buffer_store_dwordx4 v[88:91],v118,s[24:27],s53 offen sc1\n"
"v_cmp_eq_u32 vcc,3,v101\n"
"v_mfma_f32_16x16x32_bf16 v[84:87],a[28:31],v[220:223],v[84:87]\n"
"s_add_u32 s91,s91,4194304\n"
"v_cndmask_b32 v67,v67,v103,vcc\n"
"v_mfma_f32_16x16x32_bf16 v[80:83],a[24:27],v[220:223],v[80:83]\n"
"ds_read_b128 v[220:223],v113 offset:23552\n"
"v_mfma_f32_16x16x32_bf16 v[80:83],a[16:19],v[216:219],v[80:83]\n"
"ds_read_b128 v[216:219],v113 offset:22528\n"
"v_mfma_f32_16x16x32_bf16 v[80:83],a[8:11],v[212:215],v[80:83]\n"
"ds_read_b128 v[212:215],v113 offset:21504\n"
"buffer_load_dwordx4 a[120:123],v126,s[24:27],s91 offen sc1\n"
"v_mfma_f32_16x16x32_bf16 v[80:83],a[0:3],v[208:211],v[80:83]\n"
"ds_read_b128 v[208:211],v113 offset:20480\n"
"s_waitcnt lgkmcnt(12)\n"
"v_mfma_f32_16x16x32_bf16 v[80:83],v[32:35],v[224:227],v[80:83]\n"
"v_mfma_f32_16x16x32_bf16 v[80:83],v[40:43],v[228:231],v[80:83]\n"
"buffer_load_dwordx4 a[124:127],v117,s[24:27],s91 offen sc1\n"
"v_mfma_f32_16x16x32_bf16 v[80:83],a[32:35],v[232:235],v[80:83]\n"
"v_mfma_f32_16x16x32_bf16 v[80:83],a[40:43],v[236:239],v[80:83]\n"
"v_mfma_f32_16x16x32_bf16 v[84:87],a[44:47],v[236:239],v[84:87]\n"
"ds_read_b128 v[236:239],v113 offset:27648\n"
"s_add_u32 s91,s91,4194304\n"
"v_mfma_f32_16x16x32_bf16 v[84:87],a[36:39],v[232:235],v[84:87]\n"
"ds_read_b128 v[232:235],v113 offset:26624\n"
"v_mfma_f32_16x16x32_bf16 v[84:87],v[44:47],v[228:231],v[84:87]\n"
"ds_read_b128 v[228:231],v113 offset:25600\n"
"v_mfma_f32_16x16x32_bf16 v[84:87],v[36:39],v[224:227],v[84:87]\n"
"ds_read_b128 v[224:227],v113 offset:24576\n"
"buffer_load_dwordx4 a[128:131],v126,s[24:27],s91 offen sc1\n"
"s_waitcnt lgkmcnt(12)\n"
"v_mfma_f32_16x16x32_bf16 v[84:87],a[68:71],v[240:243],v[84:87]\n"
"v_mfma_f32_16x16x32_bf16 v[84:87],a[76:79],v[244:247],v[84:87]\n"
"v_mfma_f32_16x16x32_bf16 v[84:87],a[84:87],v[248:251],v[84:87]\n"
"buffer_load_dwordx4 a[132:135],v117,s[24:27],s91 offen sc1\n"
"v_mfma_f32_16x16x32_bf16 v[84:87],a[92:95],v[252:255],v[84:87]\n"
"v_mfma_f32_16x16x32_bf16 v[80:83],a[88:91],v[252:255],v[80:83]\n"
"ds_read_b128 v[252:255],v113 offset:31744\n"
"s_mov_b32 s53,s56\n"
"v_mfma_f32_16x16x32_bf16 v[80:83],a[80:83],v[248:251],v[80:83]\n"
"s_mov_b32 s56,s58\n"
"ds_read_b128 v[248:251],v113 offset:30720\n"
"s_add_u32 s91,s91,4194304\n"
"v_mfma_f32_16x16x32_bf16 v[80:83],a[72:75],v[244:247],v[80:83]\n"
"ds_read_b128 v[244:247],v113 offset:29696\n"
"s_mov_b32 s58,s59\n"
"v_mfma_f32_16x16x32_bf16 v[80:83],a[156:159],v[240:243],v[80:83]\n"
"ds_read_b128 v[240:243],v113 offset:28672\n"
"s_waitcnt lgkmcnt(12)\n"
"v_mfma_f32_16x16x32_bf16 v[88:91],v[128:131],v[192:195],v[80:83]\n"
"s_waitcnt vmcnt(16)\n"
"ds_add_u32 v114,v116\n"
"v_mfma_f32_16x16x32_bf16 v[88:91],v[136:139],v[196:199],v[88:91]\n"
"buffer_load_dwordx4 a[136:139],v126,s[24:27],s91 offen sc1\n"
"v_mfma_f32_16x16x32_bf16 v[88:91],v[144:147],v[200:203],v[88:91]\n"
"s_add_u32 s83,s20,32768\n"
"s_and_b32 s83,s83,0x1ffff\n"
"v_mfma_f32_16x16x32_bf16 v[88:91],v[152:155],v[204:207],v[88:91]\n"
"buffer_load_dwordx4 a[140:143],v117,s[24:27],s91 offen sc1\n"
"v_mfma_f32_16x16x32_bf16 v[92:95],v[156:159],v[204:207],v[84:87]\n"
"v_add_u32 v113,s83,v112\n"
"v_mfma_f32_16x16x32_bf16 v[92:95],v[148:151],v[200:203],v[92:95]\n"
"v_mfma_f32_16x16x32_bf16 v[92:95],v[140:143],v[196:199],v[92:95]\n"
"v_mfma_f32_16x16x32_bf16 v[92:95],v[132:135],v[192:195],v[92:95]\n"
"s_waitcnt lgkmcnt(9)\n"
"v_mfma_f32_16x16x32_bf16 v[92:95],v[4:7],v[208:211],v[92:95]\n"
"v_mfma_f32_16x16x32_bf16 v[92:95],v[12:15],v[212:215],v[92:95]\n"
"ds_read_b32 v125,v114\n"
"v_mfma_f32_16x16x32_bf16 v[92:95],v[164:167],v[216:219],v[92:95]\n"
"v_mfma_f32_16x16x32_bf16 v[92:95],v[172:175],v[220:223],v[92:95]\n"
"v_mfma_f32_16x16x32_bf16 v[88:91],v[168:171],v[220:223],v[88:91]\n"
"v_mfma_f32_16x16x32_bf16 v[88:91],v[160:163],v[216:219],v[88:91]\n"
"s_waitcnt lgkmcnt(0)\n"
"v_cmp_gt_u32 vcc,s16,v125\n"
"v_mfma_f32_16x16x32_bf16 v[88:91],v[8:11],v[212:215],v[88:91]\n"
"v_mfma_f32_16x16x32_bf16 v[88:91],v[0:3],v[208:211],v[88:91]\n"
"s_cbranch_vccnz .Lfk_es_me2\n"
".Lfk_er_me2:\n"
"v_mfma_f32_16x16x32_bf16 v[88:91],v[176:179],v[224:227],v[88:91]\n"
"ds_read_b128 v[192:195],v113 offset:0\n"
"v_mfma_f32_16x16x32_bf16 v[88:91],v[184:187],v[228:231],v[88:91]\n"
"ds_read_b128 v[196:199],v113 offset:1024\n"
"v_mfma_f32_16x16x32_bf16 v[88:91],a[48:51],v[232:235],v[88:91]\n"
"ds_read_b128 v[200:203],v113 offset:2048\n"
"v_mfma_f32_16x16x32_bf16 v[88:91],a[56:59],v[236:239],v[88:91]\n"
"ds_read_b128 v[204:207],v113 offset:3072\n"
"v_mfma_f32_16x16x32_bf16 v[92:95],a[60:63],v[236:239],v[92:95]\n"
"ds_read_b128 v[208:211],v113 offset:4096\n"
"v_mfma_f32_16x16x32_bf16 v[92:95],a[52:55],v[232:235],v[92:95]\n"
"ds_read_b128 v[212:215],v113 offset:5120\n"
"v_mfma_f32_16x16x32_bf16 v[92:95],v[188:191],v[228:231],v[92:95]\n"
"ds_read_b128 v[216:219],v113 offset:6144\n"
"v_mfma_f32_16x16x32_bf16 v[92:95],v[180:183],v[224:227],v[92:95]\n"
"ds_read_b128 v[220:223],v113 offset:7168\n"
"v_mfma_f32_16x16x32_bf16 v[92:95],v[52:55],v[240:243],v[92:95]\n"
"ds_read_b128 v[236:239],v113 offset:11264\n"
"v_mfma_f32_16x16x32_bf16 v[92:95],v[60:63],v[244:247],v[92:95]\n"
"ds_read_b128 v[232:235],v113 offset:10240\n"
"v_mfma_f32_16x16x32_bf16 v[92:95],v[20:23],v[248:251],v[92:95]\n"
"ds_read_b128 v[228:231],v113 offset:9216\n"
"v_mfma_f32_16x16x32_bf16 v[92:95],v[28:31],v[252:255],v[92:95]\n"
"ds_read_b128 v[224:227],v113 offset:8192\n"
"v_mfma_f32_16x16x32_bf16 v[88:91],v[24:27],v[252:255],v[88:91]\n"
"ds_read_b128 v[252:255],v113 offset:15360\n"
"v_mfma_f32_16x16x32_bf16 v[88:91],v[16:19],v[248:251],v[88:91]\n"
"ds_read_b128 v[248:251],v113 offset:14336\n"
"v_mfma_f32_16x16x32_bf16 v[88:91],v[56:59],v[244:247],v[88:91]\n"
"ds_read_b128 v[244:247],v113 offset:13312\n"
"v_mfma_f32_16x16x32_bf16 v[88:91],v[48:51],v[240:243],v[88:91]\n"
"ds_read_b128 v[240:243],v113 offset:12288\n"
"s_waitcnt lgkmcnt(12)\n"
"v_mfma_f32_16x16x32_bf16 v[80:83],a[224:227],v[192:195],0\n"
"v_mfma_f32_16x16x32_bf16 v[80:83],a[232:235],v[196:199],v[80:83]\n"
"v_mfma_f32_16x16x32_bf16 v[80:83],a[240:243],v[200:203],v[80:83]\n"
"s_nop 2\n"
"v_cvt_pk_bf16_f32 v88,v88,v89\n"
"v_mfma_f32_16x16x32_bf16 v[80:83],a[248:251],v[204:207],v[80:83]\n"
"v_cvt_pk_bf16_f32 v89,v90,v91\n"
"v_mfma_f32_16x16x32_bf16 v[84:87],a[252:255],v[204:207],0\n"
"ds_read_b128 v[204:207],v113 offset:19456\n"
"v_cvt_pk_bf16_f32 v90,v92,v93\n"
"v_mfma_f32_16x16x32_bf16 v[84:87],a[244:247],v[200:203],v[84:87]\n"
"ds_read_b128 v[200:203],v113 offset:18432\n"
"v_cvt_pk_bf16_f32 v91,v94,v95\n"
"v_mfma_f32_16x16x32_bf16 v[84:87],a[236:239],v[196:199],v[84:87]\n"
"ds_read_b128 v[196:199],v113 offset:17408\n"
"v_max_u32 v88,1,v88\n"
"v_mfma_f32_16x16x32_bf16 v[84:87],a[228:231],v[192:195],v[84:87]\n"
"ds_read_b128 v[192:195],v113 offset:16384\n"
"s_waitcnt lgkmcnt(12)\n"
"v_mfma_f32_16x16x32_bf16 v[84:87],a[4:7],v[208:211],v[84:87]\n"
"v_max_u32 v89,1,v89\n"
"v_max_u32 v90,1,v90\n"
"v_max_u32 v91,1,v91\n"
"v_mfma_f32_16x16x32_bf16 v[84:87],a[12:15],v[212:215],v[84:87]\n"
"buffer_store_dwordx4 v[88:91],v118,s[24:27],s53 offen sc1\n"
"v_mfma_f32_16x16x32_bf16 v[84:87],a[20:23],v[216:219],v[84:87]\n"
"v_mfma_f32_16x16x32_bf16 v[84:87],a[28:31],v[220:223],v[84:87]\n"
"v_mfma_f32_16x16x32_bf16 v[80:83],a[24:27],v[220:223],v[80:83]\n"
"ds_read_b128 v[220:223],v113 offset:23552\n"
"v_mfma_f32_16x16x32_bf16 v[80:83],a[16:19],v[216:219],v[80:83]\n"
"ds_read_b128 v[216:219],v113 offset:22528\n"
"v_mfma_f32_16x16x32_bf16 v[80:83],a[8:11],v[212:215],v[80:83]\n"
"ds_read_b128 v[212:215],v113 offset:21504\n"
"v_mfma_f32_16x16x32_bf16 v[80:83],a[0:3],v[208:211],v[80:83]\n"
"ds_read_b128 v[208:211],v113 offset:20480\n"
"s_waitcnt lgkmcnt(12)\n"
"v_mfma_f32_16x16x32_bf16 v[80:83],v[32:35],v[224:227],v[80:83]\n"
"v_mfma_f32_16x16x32_bf16 v[80:83],v[40:43],v[228:231],v[80:83]\n"
"v_mfma_f32_16x16x32_bf16 v[80:83],a[32:35],v[232:235],v[80:83]\n"
"v_mfma_f32_16x16x32_bf16 v[80:83],a[40:43],v[236:239],v[80:83]\n"
"v_mfma_f32_16x16x32_bf16 v[84:87],a[44:47],v[236:239],v[84:87]\n"
"ds_read_b128 v[236:239],v113 offset:27648\n"
"v_mfma_f32_16x16x32_bf16 v[84:87],a[36:39],v[232:235],v[84:87]\n"
"ds_read_b128 v[232:235],v113 offset:26624\n"
"v_mfma_f32_16x16x32_bf16 v[84:87],v[44:47],v[228:231],v[84:87]\n"
"ds_read_b128 v[228:231],v113 offset:25600\n"
"v_mfma_f32_16x16x32_bf16 v[84:87],v[36:39],v[224:227],v[84:87]\n"
"ds_read_b128 v[224:227],v113 offset:24576\n"
"s_waitcnt lgkmcnt(12)\n"
"v_mfma_f32_16x16x32_bf16 v[84:87],a[68:71],v[240:243],v[84:87]\n"
"v_mfma_f32_16x16x32_bf16 v[84:87],a[76:79],v[244:247],v[84:87]\n"
"v_mfma_f32_16x16x32_bf16 v[84:87],a[84:87],v[248:251],v[84:87]\n"
"v_mfma_f32_16x16x32_bf16 v[84:87],a[92:95],v[252:255],v[84:87]\n"
"v_mfma_f32_16x16x32_bf16 v[80:83],a[88:91],v[252:255],v[80:83]\n"
"ds_read_b128 v[252:255],v113 offset:31744\n"
"s_mov_b32 s53,s56\n"
"v_mfma_f32_16x16x32_bf16 v[80:83],a[80:83],v[248:251],v[80:83]\n"
"ds_read_b128 v[248:251],v113 offset:30720\n"
"s_mov_b32 s56,s58\n"
"v_mfma_f32_16x16x32_bf16 v[80:83],a[72:75],v[244:247],v[80:83]\n"
"ds_read_b128 v[244:247],v113 offset:29696\n"
"s_mov_b32 s58,s59\n"
"v_mfma_f32_16x16x32_bf16 v[80:83],a[156:159],v[240:243],v[80:83]\n"
"ds_read_b128 v[240:243],v113 offset:28672\n"
"s_waitcnt lgkmcnt(12)\n"
"v_mfma_f32_16x16x32_bf16 v[88:91],v[128:131],v[192:195],v[80:83]\n"
"v_mfma_f32_16x16x32_bf16 v[88:91],v[136:139],v[196:199],v[88:91]\n"
"v_mfma_f32_16x16x32_bf16 v[88:91],v[144:147],v[200:203],v[88:91]\n"
"v_mfma_f32_16x16x32_bf16 v[88:91],v[152:155],v[204:207],v[88:91]\n"
"v_mfma_f32_16x16x32_bf16 v[92:95],v[156:159],v[204:207],v[84:87]\n"
"v_mfma_f32_16x16x32_bf16 v[92:95],v[148:151],v[200:203],v[92:95]\n"
"v_mfma_f32_16x16x32_bf16 v[92:95],v[140:143],v[196:199],v[92:95]\n"
"v_mfma_f32_16x16x32_bf16 v[92:95],v[132:135],v[192:195],v[92:95]\n"
"s_waitcnt lgkmcnt(8)\n"
"v_mfma_f32_16x16x32_bf16 v[92:95],v[4:7],v[208:211],v[92:95]\n"
"v_mfma_f32_16x16x32_bf16 v[92:95],v[12:15],v[212:215],v[92:95]\n"
"v_mfma_f32_16x16x32_bf16 v[92:95],v[164:167],v[216:219],v[92:95]\n"
"v_mfma_f32_16x16x32_bf16 v[92:95],v[172:175],v[220:223],v[92:95]\n"
"v_mfma_f32_16x16x32_bf16 v[88:91],v[168:171],v[220:223],v[88:91]\n"
"s_mov_b32 s91,s62\n"
"v_mfma_f32_16x16x32_bf16 v[88:91],v[160:163],v[216:219],v[88:91]\n"
"v_mfma_f32_16x16x32_bf16 v[88:91],v[8:11],v[212:215],v[88:91]\n"
"v_mfma_f32_16x16x32_bf16 v[88:91],v[0:3],v[208:211],v[88:91]\n"
"s_waitcnt lgkmcnt(0)\n"
"s_waitcnt vmcnt(1)\n"
"v_mfma_f32_16x16x32_bf16 v[88:91],v[176:179],v[224:227],v[88:91]\n"
"v_accvgpr_read_b32 v128,a112\n"
"v_accvgpr_read_b32 v129,a113\n"
"v_mfma_f32_16x16x32_bf16 v[88:91],v[184:187],v[228:231],v[88:91]\n"
"v_accvgpr_read_b32 v130,a114\n"
"v_accvgpr_read_b32 v131,a115\n"
"v_mfma_f32_16x16x32_bf16 v[88:91],a[48:51],v[232:235],v[88:91]\n"
"v_accvgpr_read_b32 v132,a116\n"
"v_accvgpr_read_b32 v133,a117\n"
"v_mfma_f32_16x16x32_bf16 v[88:91],a[56:59],v[236:239],v[88:91]\n"
"v_accvgpr_read_b32 v134,a118\n"
"v_accvgpr_read_b32 v135,a119\n"
"v_mfma_f32_16x16x32_bf16 v[92:95],a[60:63],v[236:239],v[92:95]\n"
"v_accvgpr_read_b32 v136,a120\n"
"v_accvgpr_read_b32 v137,a121\n"
"v_mfma_f32_16x16x32_bf16 v[92:95],a[52:55],v[232:235],v[92:95]\n"
"v_accvgpr_read_b32 v138,a122\n"
"v_accvgpr_read_b32 v139,a123\n"
"v_mfma_f32_16x16x32_bf16 v[92:95],v[188:191],v[228:231],v[92:95]\n"
"v_accvgpr_read_b32 v140,a124\n"
"v_accvgpr_read_b32 v141,a125\n"
"v_mfma_f32_16x16x32_bf16 v[92:95],v[180:183],v[224:227],v[92:95]\n"
"v_accvgpr_read_b32 v142,a126\n"
"v_accvgpr_read_b32 v143,a127\n"
"v_mfma_f32_16x16x32_bf16 v[92:95],v[52:55],v[240:243],v[92:95]\n"
"v_accvgpr_read_b32 v144,a128\n"
"v_accvgpr_read_b32 v145,a129\n"
"v_mfma_f32_16x16x32_bf16 v[92:95],v[60:63],v[244:247],v[92:95]\n"
"v_accvgpr_read_b32 v146,a130\n"
"v_accvgpr_read_b32 v147,a131\n"
"v_mfma_f32_16x16x32_bf16 v[92:95],v[20:23],v[248:251],v[92:95]\n"
"v_accvgpr_read_b32 v148,a132\n"
"v_accvgpr_read_b32 v149,a133\n"
"v_mfma_f32_16x16x32_bf16 v[92:95],v[28:31],v[252:255],v[92:95]\n"
"v_accvgpr_read_b32 v150,a134\n"
"v_accvgpr_read_b32 v151,a135\n"
"v_mfma_f32_16x16x32_bf16 v[88:91],v[24:27],v[252:255],v[88:91]\n"
"v_accvgpr_read_b32 v152,a136\n"
"v_accvgpr_read_b32 v153,a137\n"
"v_mfma_f32_16x16x32_bf16 v[88:91],v[16:19],v[248:251],v[88:91]\n"
"v_accvgpr_read_b32 v154,a138\n"
"v_accvgpr_read_b32 v155,a139\n"
"v_mfma_f32_16x16x32_bf16 v[88:91],v[56:59],v[244:247],v[88:91]\n"
"v_accvgpr_read_b32 v156,a140\n"
"v_accvgpr_read_b32 v157,a141\n"
"v_mfma_f32_16x16x32_bf16 v[88:91],v[48:51],v[240:243],v[88:91]\n"
"v_accvgpr_read_b32 v158,a142\n"
"v_accvgpr_read_b32 v159,a143\n"
".Lfk_tail:\n"
"v_lshlrev_b32 v224,16,v72\n"
"v_and_b32 v225,0xffff0000,v72\n"
"v_lshlrev_b32 v226,16,v73\n"
"v_and_b32 v227,0xffff0000,v73\n"
"v_lshlrev_b32 v228,16,v74\n"
"s_nop 0\n"
"v_cvt_pk_bf16_f32 v88,v88,v89\n"
"v_cvt_pk_bf16_f32 v89,v90,v91\n"
"v_cvt_pk_bf16_f32 v90,v92,v93\n"
"v_cvt_pk_bf16_f32 v91,v94,v95\n"
"v_max_u32 v88,1,v88\n"
"v_max_u32 v89,1,v89\n"
"v_max_u32 v90,1,v90\n"
"v_max_u32 v91,1,v91\n"
"v_mfma_f32_16x16x32_bf16 v[0:3],v[66:69],a[96:99],0\n"
"buffer_store_dwordx4 v[88:91],v118,s[24:27],s53 offen sc1\n"
"v_mfma_f32_16x16x32_bf16 v[4:7],v[64:67],a[96:99],0\n"
"s_mov_b32 s101,s22\n"
"v_and_b32 v231,0xffff0000,v75\n"
"v_lshlrev_b32 v232,16,v76\n"
"v_mfma_f32_16x16x32_bf16 v[8:11],v[66:69],a[100:103],0\n"
"v_and_b32 v229,0xffff0000,v74\n"
"v_lshlrev_b32 v230,16,v75\n"
"v_mfma_f32_16x16x32_bf16 v[12:15],v[64:67],a[100:103],0\n"
"s_mov_b32 s67,0x3a800000\n"
"v_and_b32 v233,0xffff0000,v76\n"
"v_lshlrev_b32 v234,16,v77\n"
"v_and_b32 v235,0xffff0000,v77\n"
"v_lshlrev_b32 v236,16,v78\n"
"v_and_b32 v237,0xffff0000,v78\n"
"v_lshlrev_b32 v238,16,v79\n"
"v_and_b32 v239,0xffff0000,v79\n"
"v_pk_add_f32 v[70:71],v[224:225],v[226:227]\n"
"v_pk_add_f32 v[70:71],v[70:71],v[228:229]\n"
"v_pk_add_f32 v[70:71],v[70:71],v[230:231]\n"
"v_pk_add_f32 v[70:71],v[70:71],v[232:233]\n"
"v_pk_add_f32 v[70:71],v[70:71],v[234:235]\n"
"v_pk_add_f32 v[70:71],v[70:71],v[236:237]\n"
"v_pk_add_f32 v[70:71],v[70:71],v[238:239]\n"
"v_add_f32 v70,v70,v71\n"
"s_nop 1\n"
"v_add_f32_dpp v70,v70,v70 quad_perm:[1,0,3,2] row_mask:0xf bank_mask:0xf\n"
"s_nop 1\n"
"v_add_f32_dpp v70,v70,v70 quad_perm:[2,3,0,1] row_mask:0xf bank_mask:0xf\n"
"s_nop 1\n"
"v_add_f32_dpp v70,v70,v70 row_half_mirror row_mask:0xf bank_mask:0xf\n"
"s_nop 1\n"
"v_add_f32_dpp v70,v70,v70 row_mirror row_mask:0xf bank_mask:0xf\n"
"v_mov_b32 v71,v70\n"
"s_nop 1\n"
"v_permlane16_swap_b32 v71,v70\n"
"v_add_f32 v70,v70,v71\n"
"v_mov_b32 v71,v70\n"
"s_nop 1\n"
"v_permlane32_swap_b32 v71,v70\n"
"v_add_f32 v70,v70,v71\n"
"v_min3_u32 v36,v128,v129,v130\n"
"v_min3_u32 v37,v131,v132,v133\n"
"v_min3_u32 v36,v36,v134,v135\n"
"v_min3_u32 v37,v37,v136,v137\n"
"v_min3_u32 v36,v36,v138,v139\n"
"v_min3_u32 v37,v37,v140,v141\n"
"v_min3_u32 v36,v36,v142,v143\n"
"v_min3_u32 v37,v37,v144,v145\n"
"v_min3_u32 v36,v36,v146,v147\n"
"v_min3_u32 v37,v37,v148,v149\n"
"v_min3_u32 v36,v36,v150,v151\n"
"v_min3_u32 v37,v37,v152,v153\n"
"v_min3_u32 v36,v36,v154,v155\n"
"v_min3_u32 v37,v37,v156,v157\n"
"v_min3_u32 v36,v36,v158,v159\n"
"v_min_u32 v36,v36,v37\n"
"v_cmp_eq_u32 vcc,0,v36\n"
"s_nop 0\n"
"s_cbranch_vccz .Lfk_t0\n"
"s_mov_b32 s84,0x40000\n"
"s_mov_b32 s91,s62\n"
"buffer_load_dwordx4 v[128:131],v126,s[24:27],s91 offen sc1\n"
"buffer_load_dwordx4 v[132:135],v117,s[24:27],s91 offen sc1\n"
"s_add_u32 s91,s91,4194304\n"
"buffer_load_dwordx4 v[136:139],v126,s[24:27],s91 offen sc1\n"
"buffer_load_dwordx4 v[140:143],v117,s[24:27],s91 offen sc1\n"
"s_add_u32 s91,s91,4194304\n"
"buffer_load_dwordx4 v[144:147],v126,s[24:27],s91 offen sc1\n"
"buffer_load_dwordx4 v[148:151],v117,s[24:27],s91 offen sc1\n"
"s_add_u32 s91,s91,4194304\n"
"buffer_load_dwordx4 v[152:155],v126,s[24:27],s91 offen sc1\n"
"buffer_load_dwordx4 v[156:159],v117,s[24:27],s91 offen sc1\n"
"s_waitcnt vmcnt(0)\n"
".Lfk_wa:\n"
"v_min3_u32 v36,v128,v129,v130\n"
"v_min3_u32 v37,v131,v132,v133\n"
"v_min3_u32 v36,v36,v134,v135\n"
"v_min3_u32 v37,v37,v136,v137\n"
"v_min3_u32 v36,v36,v138,v139\n"
"v_min3_u32 v37,v37,v140,v141\n"
"v_min3_u32 v36,v36,v142,v143\n"
"v_min3_u32 v37,v37,v144,v145\n"
"v_min3_u32 v36,v36,v146,v147\n"
"v_min3_u32 v37,v37,v148,v149\n"
"v_min3_u32 v36,v36,v150,v151\n"
"v_min3_u32 v37,v37,v152,v153\n"
"v_min3_u32 v36,v36,v154,v155\n"
"v_min3_u32 v37,v37,v156,v157\n"
"v_min3_u32 v36,v36,v158,v159\n"
"v_min_u32 v36,v36,v37\n"
"v_cmp_eq_u32 vcc,0,v36\n"
"s_nop 0\n"
"s_cbranch_vccz .Lfk_wa_x\n"
"s_sleep 2\n"
"s_mov_b64 exec,vcc\n"
"s_mov_b32 s91,s62\n"
"buffer_load_dwordx4 v[128:131],v126,s[24:27],s91 offen sc1\n"
"buffer_load_dwordx4 v[132:135],v117,s[24:27],s91 offen sc1\n"
"s_add_u32 s91,s91,4194304\n"
"buffer_load_dwordx4 v[136:139],v126,s[24:27],s91 offen sc1\n"
"buffer_load_dwordx4 v[140:143],v117,s[24:27],s91 offen sc1\n"
"s_add_u32 s91,s91,4194304\n"
"buffer_load_dwordx4 v[144:147],v126,s[24:27],s91 offen sc1\n"
"buffer_load_dwordx4 v[148:151],v117,s[24:27],s91 offen sc1\n"
"s_add_u32 s91,s91,4194304\n"
"buffer_load_dwordx4 v[152:155],v126,s[24:27],s91 offen sc1\n"
"buffer_load_dwordx4 v[156:159],v117,s[24:27],s91 offen sc1\n"
"s_mov_b64 exec,-1\n"
"s_sub_u32 s84,s84,1\n"
"s_waitcnt vmcnt(0)\n"
"s_cbranch_scc0 .Lfk_wa\n"
".Lfk_wa_x:\n"
".Lfk_t0:\n"
"s_getpc_b64 s[94:95]\n"
".Lfk_rm0:\n"
"s_add_u32 s94,s94,.Lfk_rm1-.Lfk_rm0\n"
"v_mfma_f32_16x16x32_bf16 v[0:3],v[66:69],v[128:131],v[0:3]\n"
"s_addc_u32 s95,s95,0\n"
"v_mfma_f32_16x16x32_bf16 v[4:7],v[64:67],v[128:131],v[4:7]\n"
"v_mfma_f32_16x16x32_bf16 v[8:11],v[66:69],v[132:135],v[8:11]\n"
"v_mfma_f32_16x16x32_bf16 v[12:15],v[64:67],v[132:135],v[12:15]\n"
"v_mfma_f32_16x16x32_bf16 v[0:3],v[66:69],v[136:139],v[0:3]\n"
"v_mfma_f32_16x16x32_bf16 v[4:7],v[64:67],v[136:139],v[4:7]\n"
"v_mfma_f32_16x16x32_bf16 v[8:11],v[66:69],v[140:143],v[8:11]\n"
"v_mfma_f32_16x16x32_bf16 v[12:15],v[64:67],v[140:143],v[12:15]\n"
"v_mfma_f32_16x16x32_bf16 v[0:3],v[66:69],v[144:147],v[0:3]\n"
"v_mfma_f32_16x16x32_bf16 v[4:7],v[64:67],v[144:147],v[4:7]\n"
"v_mfma_f32_16x16x32_bf16 v[8:11],v[66:69],v[148:151],v[8:11]\n"
"v_mfma_f32_16x16x32_bf16 v[12:15],v[64:67],v[148:151],v[12:15]\n"
"v_mfma_f32_16x16x32_bf16 v[0:3],v[66:69],v[152:155],v[0:3]\n"
"v_mfma_f32_16x16x32_bf16 v[4:7],v[64:67],v[152:155],v[4:7]\n"
"v_mfma_f32_16x16x32_bf16 v[8:11],v[66:69],v[156:159],v[8:11]\n"
"v_mfma_f32_16x16x32_bf16 v[12:15],v[64:67],v[156:159],v[12:15]\n"
"s_nop 7\n"
".Lfk_rm:\n"
"v_readfirstlane_b32 s64,v0\n"
"s_nop 1\n"
"v_mov_b32 v38,s64\n"
"v_pk_add_f32 v[16:17],v[0:1],v[38:39] op_sel_hi:[1,0] neg_lo:[0,1] neg_hi:[0,1]\n"
"v_pk_add_f32 v[18:19],v[2:3],v[38:39] op_sel_hi:[1,0] neg_lo:[0,1] neg_hi:[0,1]\n"
"v_pk_add_f32 v[20:21],v[4:5],v[38:39] op_sel_hi:[1,0] neg_lo:[0,1] neg_hi:[0,1]\n"
"v_pk_add_f32 v[22:23],v[6:7],v[38:39] op_sel_hi:[1,0] neg_lo:[0,1] neg_hi:[0,1]\n"
"v_pk_add_f32 v[24:25],v[8:9],v[38:39] op_sel_hi:[1,0] neg_lo:[0,1] neg_hi:[0,1]\n"
"v_pk_add_f32 v[26:27],v[10:11],v[38:39] op_sel_hi:[1,0] neg_lo:[0,1] neg_hi:[0,1]\n"
"v_pk_add_f32 v[28:29],v[12:13],v[38:39] op_sel_hi:[1,0] neg_lo:[0,1] neg_hi:[0,1]\n"
"v_pk_add_f32 v[30:31],v[14:15],v[38:39] op_sel_hi:[1,0] neg_lo:[0,1] neg_hi:[0,1]\n"
"v_pk_add_f32 v[32:33],v[16:17],v[18:19]\n"
"v_pk_mul_f32 v[34:35],v[16:17],v[16:17]\n"
"v_pk_mul_f32 v[36:37],v[16:17],v[224:225]\n"
"v_pk_fma_f32 v[34:35],v[18:19],v[18:19],v[34:35]\n"
"v_pk_fma_f32 v[36:37],v[18:19],v[226:227],v[36:37]\n"
"v_pk_add_f32 v[32:33],v[32:33],v[20:21]\n"
"v_pk_fma_f32 v[34:35],v[20:21],v[20:21],v[34:35]\n"
"v_pk_fma_f32 v[36:37],v[20:21],v[228:229],v[36:37]\n"
"v_pk_add_f32 v[32:33],v[32:33],v[22:23]\n"
"v_pk_fma_f32 v[34:35],v[22:23],v[22:23],v[34:35]\n"
"v_pk_fma_f32 v[36:37],v[22:23],v[230:231],v[36:37]\n"
"v_pk_add_f32 v[32:33],v[32:33],v[24:25]\n"
"v_pk_fma_f32 v[34:35],v[24:25],v[24:25],v[34:35]\n"
"v_pk_fma_f32 v[36:37],v[24:25],v[232:233],v[36:37]\n"
"v_pk_add_f32 v[32:33],v[32:33],v[26:27]\n"
"v_pk_fma_f32 v[34:35],v[26:27],v[26:27],v[34:35]\n"
"v_pk_fma_f32 v[36:37],v[26:27],v[234:235],v[36:37]\n"
"v_pk_add_f32 v[32:33],v[32:33],v[28:29]\n"
"v_pk_fma_f32 v[34:35],v[28:29],v[28:29],v[34:35]\n"
"v_pk_fma_f32 v[36:37],v[28:29],v[236:237],v[36:37]\n"
"v_pk_add_f32 v[32:33],v[32:33],v[30:31]\n"
"v_pk_fma_f32 v[34:35],v[30:31],v[30:31],v[34:35]\n"
"v_pk_fma_f32 v[36:37],v[30:31],v[238:239],v[36:37]\n"
"v_add_f32 v32,v32,v33\n"
"v_add_f32 v33,v34,v35\n"
"v_add_f32 v34,v36,v37\n"
"v_add_f32_dpp v32,v32,v32 quad_perm:[1,0,3,2] row_mask:0xf bank_mask:0xf\n"
"v_add_f32_dpp v33,v33,v33 quad_perm:[1,0,3,2] row_mask:0xf bank_mask:0xf\n"
"v_add_f32_dpp v34,v34,v34 quad_perm:[1,0,3,2] row_mask:0xf bank_mask:0xf\n"
"v_add_f32_dpp v32,v32,v32 quad_perm:[2,3,0,1] row_mask:0xf bank_mask:0xf\n"
"v_add_f32_dpp v33,v33,v33 quad_perm:[2,3,0,1] row_mask:0xf bank_mask:0xf\n"
"v_add_f32_dpp v34,v34,v34 quad_perm:[2,3,0,1] row_mask:0xf bank_mask:0xf\n"
"v_add_f32_dpp v32,v32,v32 row_half_mirror row_mask:0xf bank_mask:0xf\n"
"v_add_f32_dpp v33,v33,v33 row_half_mirror row_mask:0xf bank_mask:0xf\n"
"v_add_f32_dpp v34,v34,v34 row_half_mirror row_mask:0xf bank_mask:0xf\n"
"v_add_f32_dpp v32,v32,v32 row_mirror row_mask:0xf bank_mask:0xf\n"
"v_add_f32_dpp v33,v33,v33 row_mirror row_mask:0xf bank_mask:0xf\n"
"v_add_f32_dpp v34,v34,v34 row_mirror row_mask:0xf bank_mask:0xf\n"
"v_add_f32_dpp v32,v32,v32 row_bcast:15 row_mask:0xa bank_mask:0xf\n"
"v_add_f32_dpp v33,v33,v33 row_bcast:15 row_mask:0xa bank_mask:0xf\n"
"v_add_f32_dpp v34,v34,v34 row_bcast:15 row_mask:0xa bank_mask:0xf\n"
"v_add_f32_dpp v32,v32,v32 row_bcast:31 row_mask:0xc bank_mask:0xf\n"
"v_add_f32_dpp v33,v33,v33 row_bcast:31 row_mask:0xc bank_mask:0xf\n"
"v_add_f32_dpp v34,v34,v34 row_bcast:31 row_mask:0xc bank_mask:0xf\n"
"v_mul_f32 v32,s67,v32\n"
"v_add_f32 v35,v32,v38\n"
"v_mul_f32 v32,v32,v32\n"
"v_cvt_pk_bf16_f32 v35,v35,v35\n"
"v_lshlrev_b32 v35,16,v35\n"
"v_fma_f32 v33,v33,s67,-v32\n"
"v_sub_f32 v36,v38,v35\n"
"v_max_f32 v33,0,v33\n"
"v_fmac_f32 v34,v36,v70\n"
"v_cvt_pk_bf16_f32 v33,v33,v33\n"
"v_lshlrev_b32 v33,16,v33\n"
"v_add_f32 v33,0x3727c5ac,v33\n"
"v_cvt_pk_bf16_f32 v33,v33,v33\n"
"v_lshlrev_b32 v33,16,v33\n"
"v_sqrt_f32 v33,v33\n"
"s_nop 0\n"
"v_cvt_pk_bf16_f32 v33,v33,v33\n"
"v_lshlrev_b32 v33,16,v33\n"
"v_rcp_f32 v36,v33\n"
"v_lshrrev_b32 v33,16,v33\n"
"v_mul_f32 v37,v34,v36\n"
"v_or_b32 v33,v33,v35\n"
"v_cvt_pk_bf16_f32 v37,v37,v37\n"
"v_ashrrev_i32 v36,31,v37\n"
"v_or_b32 v36,0x80000000,v36\n"
"v_xor_b32 v37,v37,v36\n"
"v_and_b32 v37,0xffff0000,v37\n"
"v_readlane_b32 s64,v33,63\n"
"v_readlane_b32 s63,v37,63\n"
"s_nop 1\n"
"s_setpc_b64 s[94:95]\n"
".Lfk_rm1:\n"
"s_add_u32 s91,s62,2097152\n"
"buffer_load_dwordx4 v[160:163],v126,s[24:27],s91 offen sc1\n"
"buffer_load_dwordx4 v[164:167],v117,s[24:27],s91 offen sc1\n"
"s_add_u32 s91,s91,4194304\n"
"buffer_load_dwordx4 v[168:171],v126,s[24:27],s91 offen sc1\n"
"buffer_load_dwordx4 v[172:175],v117,s[24:27],s91 offen sc1\n"
"s_add_u32 s91,s91,4194304\n"
"buffer_load_dwordx4 v[176:179],v126,s[24:27],s91 offen sc1\n"
"buffer_load_dwordx4 v[180:183],v117,s[24:27],s91 offen sc1\n"
"s_add_u32 s91,s91,4194304\n"
"buffer_load_dwordx4 v[184:187],v126,s[24:27],s91 offen sc1\n"
"buffer_load_dwordx4 v[188:191],v117,s[24:27],s91 offen sc1\n"
"s_sub_u32 s65,0xffff,s60\n"
"s_or_b32 s65,s65,s63\n"
"s_mov_b32 s66,s64\n"
"v_lshlrev_b32 v34,16,s64\n"
"v_mov_b32 v35,s64\n"
"v_rcp_f32 v34,v34\n"
"v_and_b32 v35,0xffff0000,v35\n"
"v_cvt_pk_bf16_f32 v240,v0,v1\n"
"v_cvt_pk_bf16_f32 v241,v2,v3\n"
"v_cvt_pk_bf16_f32 v242,v4,v5\n"
"v_cvt_pk_bf16_f32 v243,v6,v7\n"
"v_cvt_pk_bf16_f32 v244,v8,v9\n"
"v_cvt_pk_bf16_f32 v245,v10,v11\n"
"v_cvt_pk_bf16_f32 v246,v12,v13\n"
"v_cvt_pk_bf16_f32 v247,v14,v15\n"
"v_lshlrev_b32 v0,16,v240\n"
"v_and_b32 v1,0xffff0000,v240\n"
"v_lshlrev_b32 v2,16,v241\n"
"v_and_b32 v3,0xffff0000,v241\n"
"v_lshlrev_b32 v4,16,v242\n"
"v_and_b32 v5,0xffff0000,v242\n"
"v_lshlrev_b32 v6,16,v243\n"
"v_and_b32 v7,0xffff0000,v243\n"
"v_lshlrev_b32 v8,16,v244\n"
"v_and_b32 v9,0xffff0000,v244\n"
"v_lshlrev_b32 v10,16,v245\n"
"v_and_b32 v11,0xffff0000,v245\n"
"v_lshlrev_b32 v12,16,v246\n"
"v_and_b32 v13,0xffff0000,v246\n"
"v_lshlrev_b32 v14,16,v247\n"
"v_and_b32 v15,0xffff0000,v247\n"
"v_pk_add_f32 v[16:17],v[0:1],v[34:35] op_sel:[0,1] neg_lo:[0,1] neg_hi:[0,1]\n"
"v_pk_add_f32 v[18:19],v[2:3],v[34:35] op_sel:[0,1] neg_lo:[0,1] neg_hi:[0,1]\n"
"v_pk_add_f32 v[20:21],v[4:5],v[34:35] op_sel:[0,1] neg_lo:[0,1] neg_hi:[0,1]\n"
"v_pk_add_f32 v[22:23],v[6:7],v[34:35] op_sel:[0,1] neg_lo:[0,1] neg_hi:[0,1]\n"
"v_pk_add_f32 v[24:25],v[8:9],v[34:35] op_sel:[0,1] neg_lo:[0,1] neg_hi:[0,1]\n"
"v_pk_add_f32 v[26:27],v[10:11],v[34:35] op_sel:[0,1] neg_lo:[0,1] neg_hi:[0,1]\n"
"v_pk_add_f32 v[28:29],v[12:13],v[34:35] op_sel:[0,1] neg_lo:[0,1] neg_hi:[0,1]\n"
"v_pk_add_f32 v[30:31],v[14:15],v[34:35] op_sel:[0,1] neg_lo:[0,1] neg_hi:[0,1]\n"
"v_cvt_pk_bf16_f32 v240,v16,v17\n"
"v_cvt_pk_bf16_f32 v241,v18,v19\n"
"v_cvt_pk_bf16_f32 v242,v20,v21\n"
"v_cvt_pk_bf16_f32 v243,v22,v23\n"
"v_cvt_pk_bf16_f32 v244,v24,v25\n"
"v_cvt_pk_bf16_f32 v245,v26,v27\n"
"v_cvt_pk_bf16_f32 v246,v28,v29\n"
"v_cvt_pk_bf16_f32 v247,v30,v31\n"
"v_lshlrev_b32 v16,16,v240\n"
"v_and_b32 v17,0xffff0000,v240\n"
"v_lshlrev_b32 v18,16,v241\n"
"v_and_b32 v19,0xffff0000,v241\n"
"v_lshlrev_b32 v20,16,v242\n"
"v_and_b32 v21,0xffff0000,v242\n"
"v_lshlrev_b32 v22,16,v243\n"
"v_and_b32 v23,0xffff0000,v243\n"
"v_lshlrev_b32 v24,16,v244\n"
"v_and_b32 v25,0xffff0000,v244\n"
"v_lshlrev_b32 v26,16,v245\n"
"v_and_b32 v27,0xffff0000,v245\n"
"v_lshlrev_b32 v28,16,v246\n"
"v_and_b32 v29,0xffff0000,v246\n"
"v_lshlrev_b32 v30,16,v247\n"
"v_and_b32 v31,0xffff0000,v247\n"
"v_pk_mul_f32 v[16:17],v[16:17],v[34:35] op_sel_hi:[1,0]\n"
"v_pk_mul_f32 v[18:19],v[18:19],v[34:35] op_sel_hi:[1,0]\n"
"v_pk_mul_f32 v[20:21],v[20:21],v[34:35] op_sel_hi:[1,0]\n"
"v_pk_mul_f32 v[22:23],v[22:23],v[34:35] op_sel_hi:[1,0]\n"
"v_pk_mul_f32 v[24:25],v[24:25],v[34:35] op_sel_hi:[1,0]\n"
"v_pk_mul_f32 v[26:27],v[26:27],v[34:35] op_sel_hi:[1,0]\n"
"v_pk_mul_f32 v[28:29],v[28:29],v[34:35] op_sel_hi:[1,0]\n"
"v_pk_mul_f32 v[30:31],v[30:31],v[34:35] op_sel_hi:[1,0]\n"
"v_cvt_pk_bf16_f32 v240,v16,v17\n"
"v_cvt_pk_bf16_f32 v241,v18,v19\n"
"v_cvt_pk_bf16_f32 v242,v20,v21\n"
"v_cvt_pk_bf16_f32 v243,v22,v23\n"
"v_cvt_pk_bf16_f32 v244,v24,v25\n"
"v_cvt_pk_bf16_f32 v245,v26,v27\n"
"v_cvt_pk_bf16_f32 v246,v28,v29\n"
"v_cvt_pk_bf16_f32 v247,v30,v31\n"
"v_max_u32 v216,1,v240\n"
"v_max_u32 v217,1,v241\n"
"v_max_u32 v218,1,v242\n"
"v_max_u32 v219,1,v243\n"
"v_max_u32 v220,1,v244\n"
"v_max_u32 v221,1,v245\n"
"v_max_u32 v222,1,v246\n"
"v_max_u32 v223,1,v247\n"
"v_lshlrev_b32 v16,16,v240\n"
"v_and_b32 v17,0xffff0000,v240\n"
"v_lshlrev_b32 v18,16,v241\n"
"v_and_b32 v19,0xffff0000,v241\n"
"v_lshlrev_b32 v20,16,v242\n"
"v_and_b32 v21,0xffff0000,v242\n"
"v_lshlrev_b32 v22,16,v243\n"
"v_and_b32 v23,0xffff0000,v243\n"
"v_lshlrev_b32 v24,16,v244\n"
"v_and_b32 v25,0xffff0000,v244\n"
"v_lshlrev_b32 v26,16,v245\n"
"v_and_b32 v27,0xffff0000,v245\n"
"v_lshlrev_b32 v28,16,v246\n"
"v_and_b32 v29,0xffff0000,v246\n"
"v_lshlrev_b32 v30,16,v247\n"
"v_and_b32 v31,0xffff0000,v247\n"
"v_pk_add_f32 v[16:17],v[16:17],v[0:1]\n"
"v_pk_add_f32 v[18:19],v[18:19],v[2:3]\n"
"v_pk_add_f32 v[20:21],v[20:21],v[4:5]\n"
"v_pk_add_f32 v[22:23],v[22:23],v[6:7]\n"
"v_pk_add_f32 v[24:25],v[24:25],v[8:9]\n"
"v_pk_add_f32 v[26:27],v[26:27],v[10:11]\n"
"v_pk_add_f32 v[28:29],v[28:29],v[12:13]\n"
"v_pk_add_f32 v[30:31],v[30:31],v[14:15]\n"
"v_cvt_pk_bf16_f32 v240,v16,v17\n"
"v_cvt_pk_bf16_f32 v241,v18,v19\n"
"v_cvt_pk_bf16_f32 v242,v20,v21\n"
"v_cvt_pk_bf16_f32 v243,v22,v23\n"
"v_cvt_pk_bf16_f32 v244,v24,v25\n"
"v_cvt_pk_bf16_f32 v245,v26,v27\n"
"v_cvt_pk_bf16_f32 v246,v28,v29\n"
"v_cvt_pk_bf16_f32 v247,v30,v31\n"
"buffer_store_dwordx4 v[240:243],v112,s[48:51],s61 offen offset:0 sc1\n"
"buffer_store_dwordx4 v[244:247],v112,s[48:51],s61 offen offset:1024 sc1\n"
"s_barrier\n"
"s_lshl_b32 s82,s52,13\n"
"s_add_u32 s82,s82,0\n"
"v_bfe_u32 v41,v112,5,2\n"
"v_lshlrev_b32 v41,13,v41\n"
"v_bfe_u32 v42,v112,4,1\n"
"v_lshl_add_u32 v41,v42,3,v41\n"
"s_lshl_b32 s83,s60,2\n"
"v_mfma_f32_16x16x32_bf16 v[200:203],v[66:69],a[104:107],0\n"
"s_add_u32 s83,s83,149946368\n"
"v_mov_b32 v45,s66\n"
"v_add_u32 v41,0,v41\n"
"v_mfma_f32_16x16x32_bf16 v[204:207],v[64:67],a[104:107],0\n"
"v_mov_b32 v40,s82\n"
"v_mov_b32 v51,s80\n"
"v_mfma_f32_16x16x32_bf16 v[208:211],v[66:69],a[108:111],0\n"
"v_mov_b32 v44,s65\n"
"v_lshlrev_b32 v43,8,v112\n"
"v_mfma_f32_16x16x32_bf16 v[212:215],v[64:67],a[108:111],0\n"
"s_mov_b32 s84,0x40000\n"
"v_mov_b32 v49,s80\n"
"s_waitcnt vmcnt(2)\n"
".Lfk_w1:\n"
"v_mfma_f32_16x16x32_bf16 v[0:3],v[66:69],v[160:163],v[200:203]\n"
"v_min3_u32 v36,v160,v161,v162\n"
"v_min3_u32 v37,v163,v164,v165\n"
"v_mfma_f32_16x16x32_bf16 v[4:7],v[64:67],v[160:163],v[204:207]\n"
"v_min3_u32 v36,v36,v166,v167\n"
"v_min3_u32 v37,v37,v168,v169\n"
"v_mfma_f32_16x16x32_bf16 v[8:11],v[66:69],v[164:167],v[208:211]\n"
"v_min3_u32 v36,v36,v170,v171\n"
"v_min3_u32 v37,v37,v172,v173\n"
"v_mfma_f32_16x16x32_bf16 v[12:15],v[64:67],v[164:167],v[212:215]\n"
"v_min3_u32 v36,v36,v174,v175\n"
"v_min3_u32 v37,v37,v176,v177\n"
"v_mfma_f32_16x16x32_bf16 v[0:3],v[66:69],v[168:171],v[0:3]\n"
"v_min3_u32 v36,v36,v178,v179\n"
"v_min3_u32 v37,v37,v180,v181\n"
"v_mfma_f32_16x16x32_bf16 v[4:7],v[64:67],v[168:171],v[4:7]\n"
"v_min3_u32 v36,v36,v182,v183\n"
"v_min3_u32 v37,v37,v184,v185\n"
"v_mfma_f32_16x16x32_bf16 v[8:11],v[66:69],v[172:175],v[8:11]\n"
"v_min3_u32 v36,v36,v186,v187\n"
"v_min3_u32 v37,v37,v188,v189\n"
"v_mfma_f32_16x16x32_bf16 v[12:15],v[64:67],v[172:175],v[12:15]\n"
"v_min3_u32 v36,v36,v190,v191\n"
"v_min_u32 v36,v36,v37\n"
"v_cmp_eq_u32 vcc,0,v36\n"
"v_mfma_f32_16x16x32_bf16 v[0:3],v[66:69],v[176:179],v[0:3]\n"
"s_cbranch_vccz .Lfk_w1_x\n"
"s_mov_b64 exec,vcc\n"
"s_add_u32 s91,s62,2097152\n"
"buffer_load_dwordx4 v[160:163],v126,s[24:27],s91 offen sc1\n"
"buffer_load_dwordx4 v[164:167],v117,s[24:27],s91 offen sc1\n"
"s_add_u32 s91,s91,4194304\n"
"buffer_load_dwordx4 v[168:171],v126,s[24:27],s91 offen sc1\n"
"buffer_load_dwordx4 v[172:175],v117,s[24:27],s91 offen sc1\n"
"s_add_u32 s91,s91,4194304\n"
"buffer_load_dwordx4 v[176:179],v126,s[24:27],s91 offen sc1\n"
"buffer_load_dwordx4 v[180:183],v117,s[24:27],s91 offen sc1\n"
"s_add_u32 s91,s91,4194304\n"
"buffer_load_dwordx4 v[184:187],v126,s[24:27],s91 offen sc1\n"
"buffer_load_dwordx4 v[188:191],v117,s[24:27],s91 offen sc1\n"
"s_mov_b64 exec,-1\n"
"s_sub_u32 s84,s84,1\n"
"s_waitcnt vmcnt(0)\n"
"s_cbranch_scc0 .Lfk_w1\n"
".Lfk_w1_x:\n"
"v_mfma_f32_16x16x32_bf16 v[4:7],v[64:67],v[176:179],v[4:7]\n"
"v_mfma_f32_16x16x32_bf16 v[8:11],v[66:69],v[180:183],v[8:11]\n"
"v_mfma_f32_16x16x32_bf16 v[12:15],v[64:67],v[180:183],v[12:15]\n"
"v_mfma_f32_16x16x32_bf16 v[0:3],v[66:69],v[184:187],v[0:3]\n"
"v_mfma_f32_16x16x32_bf16 v[4:7],v[64:67],v[184:187],v[4:7]\n"
"v_mfma_f32_16x16x32_bf16 v[8:11],v[66:69],v[188:191],v[8:11]\n"
"v_mfma_f32_16x16x32_bf16 v[12:15],v[64:67],v[188:191],v[12:15]\n"
"s_nop 6\n"
"s_call_b64 s[94:95],.Lfk_rm\n"
"s_sub_u32 s82,64511,s60\n"
"s_or_b32 s63,s63,s82\n"
"v_mov_b32 v46,s63\n"
"v_mov_b32 v47,s64\n"
"v_mov_b32 v39,0\n"
"ds_write_b128 v40,v[44:47]\n"
"s_waitcnt vmcnt(0) lgkmcnt(0)\n"
"s_barrier\n"
"ds_read_b64 v[52:53],v41\n"
"s_waitcnt lgkmcnt(0)\n"
"v_mov_b32 v48,v52\n"
"s_nop 1\n"
"v_min_u32_dpp v54,v48,v48 quad_perm:[1,0,3,2] row_mask:0xf bank_mask:0xf\n"
"v_max_u32_dpp v48,v48,v48 quad_perm:[1,0,3,2] row_mask:0xf bank_mask:0xf\n"
"v_max_u32_dpp v39,v39,v39 quad_perm:[1,0,3,2] row_mask:0xf bank_mask:0xf\n"
"v_max_u32 v39,v39,v54\n"
"v_min_u32_dpp v54,v48,v48 quad_perm:[2,3,0,1] row_mask:0xf bank_mask:0xf\n"
"v_max_u32_dpp v48,v48,v48 quad_perm:[2,3,0,1] row_mask:0xf bank_mask:0xf\n"
"v_max_u32_dpp v39,v39,v39 quad_perm:[2,3,0,1] row_mask:0xf bank_mask:0xf\n"
"v_max_u32 v39,v39,v54\n"
"v_min_u32_dpp v54,v48,v48 row_half_mirror row_mask:0xf bank_mask:0xf\n"
"v_max_u32_dpp v48,v48,v48 row_half_mirror row_mask:0xf bank_mask:0xf\n"
"v_max_u32_dpp v39,v39,v39 row_half_mirror row_mask:0xf bank_mask:0xf\n"
"v_max_u32 v39,v39,v54\n"
"v_xor_b32 v50,0x800,v39\n"
"s_cmp_lg_u32 s52,0\n"
"s_cbranch_scc1 .Lfk_t1\n"
"s_mov_b64 exec,0xff\n"
"buffer_store_dwordx4 v[48:51],v43,s[24:27],s83 offen sc1\n"
"s_mov_b64 exec,-1\n"
"v_cmp_eq_u32 vcc,v48,v52\n"
"s_nop 0\n"
"s_ff1_i32_b64 s82,vcc\n"
"v_readlane_b32 s90,v53,s82\n"
"s_nop 1\n"
"v_mov_b32 v44,s90\n"
"v_mov_b32 v45,s80\n"
"v_cmp_eq_u32 vcc,v39,v52\n"
"s_nop 0\n"
"s_ff1_i32_b64 s82,vcc\n"
"v_readlane_b32 s90,v53,s82\n"
"s_nop 1\n"
"v_mov_b32 v46,s90\n"
"v_mov_b32 v47,s80\n"
"s_mov_b64 exec,0x100\n"
"buffer_store_dwordx4 v[44:47],v43,s[24:27],s83 offen sc1\n"
"s_mov_b64 exec,-1\n"
".Lfk_t1:\n"
"v_readfirstlane_b32 s89,v39\n"
"s_add_u32 m0,s30,131072\n"
"s_nop 0\n"
"buffer_load_dwordx4 v96,s[40:43],0 offen offset:0 nt lds\n"
"buffer_load_dwordx4 v96,s[40:43],0 offen offset:1024 nt lds\n"
"buffer_load_dwordx4 v97,s[40:43],0 offen offset:2048 nt lds\n"
"buffer_load_dwordx4 v97,s[40:43],0 offen offset:3072 nt lds\n"
"v_lshlrev_b32 v34,16,s64\n"
"v_mov_b32 v35,s64\n"
"v_rcp_f32 v34,v34\n"
"v_and_b32 v35,0xffff0000,v35\n"
"v_cvt_pk_bf16_f32 v248,v0,v1\n"
"v_cvt_pk_bf16_f32 v249,v2,v3\n"
"v_cvt_pk_bf16_f32 v250,v4,v5\n"
"v_cvt_pk_bf16_f32 v251,v6,v7\n"
"v_cvt_pk_bf16_f32 v252,v8,v9\n"
"v_cvt_pk_bf16_f32 v253,v10,v11\n"
"v_cvt_pk_bf16_f32 v254,v12,v13\n"
"v_cvt_pk_bf16_f32 v255,v14,v15\n"
"v_lshlrev_b32 v0,16,v248\n"
"v_and_b32 v1,0xffff0000,v248\n"
"v_lshlrev_b32 v2,16,v249\n"
"v_and_b32 v3,0xffff0000,v249\n"
"v_lshlrev_b32 v4,16,v250\n"
"v_and_b32 v5,0xffff0000,v250\n"
"v_lshlrev_b32 v6,16,v251\n"
"v_and_b32 v7,0xffff0000,v251\n"
"v_lshlrev_b32 v8,16,v252\n"
"v_and_b32 v9,0xffff0000,v252\n"
"v_lshlrev_b32 v10,16,v253\n"
"v_and_b32 v11,0xffff0000,v253\n"
"v_lshlrev_b32 v12,16,v254\n"
"v_and_b32 v13,0xffff0000,v254\n"
"v_lshlrev_b32 v14,16,v255\n"
"v_and_b32 v15,0xffff0000,v255\n"
"v_pk_add_f32 v[16:17],v[0:1],v[34:35] op_sel:[0,1] neg_lo:[0,1] neg_hi:[0,1]\n"
"v_pk_add_f32 v[18:19],v[2:3],v[34:35] op_sel:[0,1] neg_lo:[0,1] neg_hi:[0,1]\n"
"v_pk_add_f32 v[20:21],v[4:5],v[34:35] op_sel:[0,1] neg_lo:[0,1] neg_hi:[0,1]\n"
"v_pk_add_f32 v[22:23],v[6:7],v[34:35] op_sel:[0,1] neg_lo:[0,1] neg_hi:[0,1]\n"
"v_pk_add_f32 v[24:25],v[8:9],v[34:35] op_sel:[0,1] neg_lo:[0,1] neg_hi:[0,1]\n"
"v_pk_add_f32 v[26:27],v[10:11],v[34:35] op_sel:[0,1] neg_lo:[0,1] neg_hi:[0,1]\n"
"v_pk_add_f32 v[28:29],v[12:13],v[34:35] op_sel:[0,1] neg_lo:[0,1] neg_hi:[0,1]\n"
"v_pk_add_f32 v[30:31],v[14:15],v[34:35] op_sel:[0,1] neg_lo:[0,1] neg_hi:[0,1]\n"
"v_cvt_pk_bf16_f32 v248,v16,v17\n"
"v_cvt_pk_bf16_f32 v249,v18,v19\n"
"v_cvt_pk_bf16_f32 v250,v20,v21\n"
"v_cvt_pk_bf16_f32 v251,v22,v23\n"
"v_cvt_pk_bf16_f32 v252,v24,v25\n"
"v_cvt_pk_bf16_f32 v253,v26,v27\n"
"v_cvt_pk_bf16_f32 v254,v28,v29\n"
"v_cvt_pk_bf16_f32 v255,v30,v31\n"
"v_lshlrev_b32 v16,16,v248\n"
"v_and_b32 v17,0xffff0000,v248\n"
"v_lshlrev_b32 v18,16,v249\n"
"v_and_b32 v19,0xffff0000,v249\n"
"v_lshlrev_b32 v20,16,v250\n"
"v_and_b32 v21,0xffff0000,v250\n"
"v_lshlrev_b32 v22,16,v251\n"
"v_and_b32 v23,0xffff0000,v251\n"
"v_lshlrev_b32 v24,16,v252\n"
"v_and_b32 v25,0xffff0000,v252\n"
"v_lshlrev_b32 v26,16,v253\n"
"v_and_b32 v27,0xffff0000,v253\n"
"v_lshlrev_b32 v28,16,v254\n"
"v_and_b32 v29,0xffff0000,v254\n"
"v_lshlrev_b32 v30,16,v255\n"
"v_and_b32 v31,0xffff0000,v255\n"
"v_pk_mul_f32 v[16:17],v[16:17],v[34:35] op_sel_hi:[1,0]\n"
"v_pk_mul_f32 v[18:19],v[18:19],v[34:35] op_sel_hi:[1,0]\n"
"v_pk_mul_f32 v[20:21],v[20:21],v[34:35] op_sel_hi:[1,0]\n"
"v_pk_mul_f32 v[22:23],v[22:23],v[34:35] op_sel_hi:[1,0]\n"
"v_pk_mul_f32 v[24:25],v[24:25],v[34:35] op_sel_hi:[1,0]\n"
"v_pk_mul_f32 v[26:27],v[26:27],v[34:35] op_sel_hi:[1,0]\n"
"v_pk_mul_f32 v[28:29],v[28:29],v[34:35] op_sel_hi:[1,0]\n"
"v_pk_mul_f32 v[30:31],v[30:31],v[34:35] op_sel_hi:[1,0]\n"
"v_cvt_pk_bf16_f32 v248,v16,v17\n"
"v_cvt_pk_bf16_f32 v249,v18,v19\n"
"v_cvt_pk_bf16_f32 v250,v20,v21\n"
"v_cvt_pk_bf16_f32 v251,v22,v23\n"
"v_cvt_pk_bf16_f32 v252,v24,v25\n"
"v_cvt_pk_bf16_f32 v253,v26,v27\n"
"v_cvt_pk_bf16_f32 v254,v28,v29\n"
"v_cvt_pk_bf16_f32 v255,v30,v31\n"
"v_max_u32 v208,1,v248\n"
"v_max_u32 v209,1,v249\n"
"v_max_u32 v210,1,v250\n"
"v_max_u32 v211,1,v251\n"
"v_max_u32 v212,1,v252\n"
"v_max_u32 v213,1,v253\n"
"v_max_u32 v214,1,v254\n"
"v_max_u32 v215,1,v255\n"
"s_lshr_b32 s82,s60,2\n"
"s_lshl_b32 s82,s82,12\n"
"s_cmp_eq_u32 s63,s89\n"
"s_cselect_b32 s83,2048,0\n"
"s_add_u32 s82,s82,s83\n"
"s_add_u32 s82,s82,174063616\n"
"s_cmp_ge_u32 s63,s89\n"
"s_cselect_b32 s82,s82,2146435072\n"
"buffer_store_dwordx4 v[208:211],v112,s[24:27],s82 offen offset:0 sc1\n"
"buffer_store_dwordx4 v[212:215],v112,s[24:27],s82 offen offset:1024 sc1\n"
"s_lshr_b32 s82,s60,2\n"
"s_lshl_b32 s82,s82,12\n"
"s_cmp_eq_u32 s65,s89\n"
"s_cselect_b32 s83,2048,0\n"
"s_add_u32 s82,s82,s83\n"
"s_add_u32 s82,s82,174063616\n"
"s_cmp_ge_u32 s65,s89\n"
"s_cselect_b32 s82,s82,2146435072\n"
"buffer_store_dwordx4 v[216:219],v112,s[24:27],s82 offen offset:0 sc1\n"
"buffer_store_dwordx4 v[220:223],v112,s[24:27],s82 offen offset:1024 sc1\n"
"v_lshlrev_b32 v16,16,v248\n"
"v_and_b32 v17,0xffff0000,v248\n"
"v_lshlrev_b32 v18,16,v249\n"
"v_and_b32 v19,0xffff0000,v249\n"
"v_lshlrev_b32 v20,16,v250\n"
"v_and_b32 v21,0xffff0000,v250\n"
"v_lshlrev_b32 v22,16,v251\n"
"v_and_b32 v23,0xffff0000,v251\n"
"v_lshlrev_b32 v24,16,v252\n"
"v_and_b32 v25,0xffff0000,v252\n"
"v_lshlrev_b32 v26,16,v253\n"
"v_and_b32 v27,0xffff0000,v253\n"
"v_lshlrev_b32 v28,16,v254\n"
"v_and_b32 v29,0xffff0000,v254\n"
"v_lshlrev_b32 v30,16,v255\n"
"v_and_b32 v31,0xffff0000,v255\n"
"v_pk_add_f32 v[16:17],v[16:17],v[0:1]\n"
"v_pk_add_f32 v[18:19],v[18:19],v[2:3]\n"
"v_pk_add_f32 v[20:21],v[20:21],v[4:5]\n"
"v_pk_add_f32 v[22:23],v[22:23],v[6:7]\n"
"v_pk_add_f32 v[24:25],v[24:25],v[8:9]\n"
"v_pk_add_f32 v[26:27],v[26:27],v[10:11]\n"
"v_pk_add_f32 v[28:29],v[28:29],v[12:13]\n"
"v_pk_add_f32 v[30:31],v[30:31],v[14:15]\n"
"v_cvt_pk_bf16_f32 v248,v16,v17\n"
"v_cvt_pk_bf16_f32 v249,v18,v19\n"
"v_cvt_pk_bf16_f32 v250,v20,v21\n"
"v_cvt_pk_bf16_f32 v251,v22,v23\n"
"v_cvt_pk_bf16_f32 v252,v24,v25\n"
"v_cvt_pk_bf16_f32 v253,v26,v27\n"
"v_cvt_pk_bf16_f32 v254,v28,v29\n"
"v_cvt_pk_bf16_f32 v255,v30,v31\n"
"s_add_u32 s68,s61,2097152\n"
"s_cmp_lt_u32 s63,s89\n"
"s_cselect_b32 s83,s68,2146435072\n"
"s_cselect_b32 s68,2146435072,s68\n"
"buffer_store_dwordx4 v[248:251],v112,s[48:51],s83 offen offset:0 sc1\n"
"buffer_store_dwordx4 v[252:255],v112,s[48:51],s83 offen offset:1024 sc1\n"
"v_lshrrev_b32 v40,6,v112\n"
"v_and_b32 v41,7,v40\n"
"v_lshlrev_b32 v41,5,v41\n"
"v_bfe_u32 v42,v112,4,2\n"
"v_lshlrev_b32 v42,3,v42\n"
"v_lshl_add_u32 v40,v40,11,v42\n"
"s_lshl_b32 s82,s52,9\n"
"v_add_u32 v40,s82,v40\n"
"v_add_u32 v40,131072,v40\n"
"v_xor_b32 v42,0,v41\n"
"v_add_u32 v192,v40,v42\n"
"v_xor_b32 v42,32,v41\n"
"v_add_u32 v193,v40,v42\n"
"v_xor_b32 v42,64,v41\n"
"v_add_u32 v194,v40,v42\n"
"v_xor_b32 v42,96,v41\n"
"v_add_u32 v195,v40,v42\n"
"v_xor_b32 v42,128,v41\n"
"v_add_u32 v196,v40,v42\n"
"v_xor_b32 v42,160,v41\n"
"v_add_u32 v197,v40,v42\n"
"v_xor_b32 v42,192,v41\n"
"v_add_u32 v198,v40,v42\n"
"v_xor_b32 v42,224,v41\n"
"v_add_u32 v199,v40,v42\n"
"v_bfe_u32 v56,v112,4,1\n"
"v_lshrrev_b32 v57,1,v112\n"
"v_and_b32 v57,0x1f0,v57\n"
"s_lshl_b32 s83,s52,13\n"
"s_add_u32 s83,s83,1024\n"
"v_lshl_add_u32 v58,v56,9,s83\n"
"v_lshrrev_b32 v59,8,v112\n"
"v_lshl_add_u32 v59,v59,4,s82\n"
"v_add_u32 v59,174063616,v59\n"
"v_add_u32 v58,v58,v57\n"
"v_add_u32 v57,s82,v57\n"
"v_and_b32 v55,0x7c0,v57\n"
"v_and_b32 v54,0x30,v57\n"
"v_lshl_add_u32 v55,v55,4,v54\n"
"v_lshrrev_b32 v54,2,v55\n"
"v_and_b32 v54,0x0,v54\n"
"v_or_b32 v55,v55,v54\n"
"v_add_u32 v60,s83,v112\n"
"v_add_u32 v61,1024,v112\n"
"v_bfe_u32 v62,v112,5,3\n"
"v_cmp_eq_u32 s[8:9],0,v62\n"
"v_cmp_eq_u32 s[10:11],1,v62\n"
"v_cmp_eq_u32 s[12:13],2,v62\n"
"v_cmp_eq_u32 s[14:15],3,v62\n"
"v_cmp_eq_u32 s[16:17],4,v62\n"
"v_cmp_eq_u32 s[18:19],5,v62\n"
"v_cmp_eq_u32 s[20:21],6,v62\n"
"v_cmp_eq_u32 s[22:23],7,v62\n"
"v_lshlrev_b32 v62,6,v62\n"
"v_lshl_add_u32 v62,v56,12,v62\n"
"v_lshrrev_b32 v63,8,v112\n"
"v_lshl_add_u32 v62,v63,4,v62\n"
"s_lshl_b32 s69,s72,13\n"
"s_lshl_b32 s82,s52,10\n"
"s_add_u32 s69,s69,s82\n"
"s_add_u32 s69,s69,171966464\n"
"v_lshrrev_b32 v63,5,v112\n"
"v_min_u32 v63,27,v63\n"
"v_lshlrev_b32 v63,13,v63\n"
"v_lshl_add_u32 v63,v56,4,v63\n"
"s_mul_i32 s70,s52,458752\n"
"s_lshl_b32 s82,s73,5\n"
"s_add_u32 s70,s70,s82\n"
"s_add_u32 s70,s70,171966464\n"
"s_add_u32 s71,s70,229376\n"
"s_and_b32 s83,s100,7\n"
"s_lshl_b32 s83,s83,12\n"
"s_add_u32 s83,s83,149946368\n"
"v_add_u32 v20,0,v112\n"
"v_lshlrev_b32 v20,0,v20\n"
"v_add_u32 v21,1024,v112\n"
"v_lshlrev_b32 v21,0,v21\n"
"v_add_u32 v22,2048,v112\n"
"v_lshlrev_b32 v22,0,v22\n"
"v_add_u32 v23,3072,v112\n"
"v_lshlrev_b32 v23,0,v23\n"
"s_sleep 16\n"
".Lfk_nn:\n"
"s_mov_b32 s84,0x40000\n"
"buffer_load_dwordx4 v[0:3],v20,s[24:27],s83 offen sc1\n"
"buffer_load_dwordx4 v[4:7],v21,s[24:27],s83 offen sc1\n"
"buffer_load_dwordx4 v[8:11],v22,s[24:27],s83 offen sc1\n"
"buffer_load_dwordx4 v[12:15],v23,s[24:27],s83 offen sc1\n"
"s_waitcnt vmcnt(0)\n"
".Lfk_wc:\n"
"v_xor_b32 v24,s80,v1\n"
"v_xor_b32 v25,s80,v3\n"
"v_xor_b32 v26,s80,v5\n"
"v_xor_b32 v27,s80,v7\n"
"v_xor_b32 v28,s80,v9\n"
"v_xor_b32 v29,s80,v11\n"
"v_xor_b32 v30,s80,v13\n"
"v_xor_b32 v31,s80,v15\n"
"v_or3_b32 v16,v24,v25,v26\n"
"v_or3_b32 v17,v27,v28,v29\n"
"v_or3_b32 v16,v16,v30,v31\n"
"v_or_b32 v16,v16,v17\n"
"v_cmp_ne_u32 vcc,0,v16\n"
"s_nop 0\n"
"s_cbranch_vccz .Lfk_wc_x\n"
"s_sleep 2\n"
"s_mov_b64 exec,vcc\n"
"buffer_load_dwordx4 v[0:3],v20,s[24:27],s83 offen sc1\n"
"buffer_load_dwordx4 v[4:7],v21,s[24:27],s83 offen sc1\n"
"buffer_load_dwordx4 v[8:11],v22,s[24:27],s83 offen sc1\n"
"buffer_load_dwordx4 v[12:15],v23,s[24:27],s83 offen sc1\n"
"s_mov_b64 exec,-1\n"
"s_sub_u32 s84,s84,1\n"
"s_waitcnt vmcnt(0)\n"
"s_cbranch_scc0 .Lfk_wc\n"
".Lfk_wc_x:\n"
"v_min_u32 v16,v0,v4\n"
"v_max_u32 v0,v0,v4\n"
"v_max3_u32 v2,v2,v6,v16\n"
"v_min_u32 v16,v8,v12\n"
"v_max_u32 v8,v8,v12\n"
"v_max3_u32 v10,v10,v14,v16\n"
"v_min_u32 v16,v0,v8\n"
"v_max_u32 v0,v0,v8\n"
"v_max3_u32 v2,v2,v10,v16\n"
"v_cmp_eq_u32 vcc,0,v56\n"
"v_min_u32_dpp v16,v0,v0 quad_perm:[1,0,3,2] row_mask:0xf bank_mask:0xf\n"
"v_max_u32_dpp v0,v0,v0 quad_perm:[1,0,3,2] row_mask:0xf bank_mask:0xf\n"
"v_max_u32_dpp v2,v2,v2 quad_perm:[1,0,3,2] row_mask:0xf bank_mask:0xf\n"
"v_max_u32 v2,v2,v16\n"
"v_min_u32_dpp v16,v0,v0 quad_perm:[2,3,0,1] row_mask:0xf bank_mask:0xf\n"
"v_max_u32_dpp v0,v0,v0 quad_perm:[2,3,0,1] row_mask:0xf bank_mask:0xf\n"
"v_max_u32_dpp v2,v2,v2 quad_perm:[2,3,0,1] row_mask:0xf bank_mask:0xf\n"
"v_max_u32 v2,v2,v16\n"
"v_min_u32_dpp v16,v0,v0 row_half_mirror row_mask:0xf bank_mask:0xf\n"
"v_max_u32_dpp v0,v0,v0 row_half_mirror row_mask:0xf bank_mask:0xf\n"
"v_max_u32_dpp v2,v2,v2 row_half_mirror row_mask:0xf bank_mask:0xf\n"
"v_max_u32 v2,v2,v16\n"
"v_min_u32_dpp v16,v0,v0 row_mirror row_mask:0xf bank_mask:0xf\n"
"v_max_u32_dpp v0,v0,v0 row_mirror row_mask:0xf bank_mask:0xf\n"
"v_max_u32_dpp v2,v2,v2 row_mirror row_mask:0xf bank_mask:0xf\n"
"v_max_u32 v2,v2,v16\n"
"v_min_u32_dpp v16,v0,v0 row_bcast:15 row_mask:0xa bank_mask:0xf\n"
"v_max_u32_dpp v0,v0,v0 row_bcast:15 row_mask:0xa bank_mask:0xf\n"
"v_max_u32_dpp v2,v2,v2 row_bcast:15 row_mask:0xa bank_mask:0xf\n"
"v_max_u32 v2,v2,v16\n"
"v_min_u32_dpp v16,v0,v0 row_bcast:31 row_mask:0xc bank_mask:0xf\n"
"v_max_u32_dpp v0,v0,v0 row_bcast:31 row_mask:0xc bank_mask:0xf\n"
"v_max_u32_dpp v2,v2,v2 row_bcast:31 row_mask:0xc bank_mask:0xf\n"
"v_max_u32 v2,v2,v16\n"
"v_readlane_b32 s90,v0,63\n"
"v_readlane_b32 s92,v2,63\n"
"s_nop 0\n"
"v_mov_b32 v4,s90\n"
"v_mov_b32 v5,s92\n"
"v_cndmask_b32 v6,v5,v4,vcc\n"
"v_not_b32 v7,v6\n"
"v_bfe_u32 v9,v7,2,8\n"
"v_and_b32 v11,0x800,v7\n"
"v_lshl_add_u32 v11,v9,12,v11\n"
"v_add_u32 v11,v11,v59\n"
"buffer_load_dwordx4 v[128:131],v11,s[24:27],0 offen offset:0 sc1\n"
"buffer_load_dwordx4 v[132:135],v11,s[24:27],0 offen offset:64 sc1\n"
"buffer_load_dwordx4 v[136:139],v11,s[24:27],0 offen offset:128 sc1\n"
"buffer_load_dwordx4 v[140:143],v11,s[24:27],0 offen offset:192 sc1\n"
"buffer_load_dwordx4 v[144:147],v11,s[24:27],0 offen offset:256 sc1\n"
"buffer_load_dwordx4 v[148:151],v11,s[24:27],0 offen offset:320 sc1\n"
"buffer_load_dwordx4 v[152:155],v11,s[24:27],0 offen offset:384 sc1\n"
"buffer_load_dwordx4 v[156:159],v11,s[24:27],0 offen offset:448 sc1\n"
"v_and_b32 v7,0x7ff,v7\n"
"v_lshl_add_u32 v8,v7,11,v57\n"
"v_and_b32 v9,15,v7\n"
"v_lshlrev_b32 v9,6,v9\n"
"v_xor_b32 v9,v9,v55\n"
"v_and_b32 v10,0x7f0,v7\n"
"v_lshl_add_u32 v10,v10,11,v9\n"
"s_mov_b32 s82,71303168\n"
"buffer_load_dwordx4 a[0:3],v10,s[24:27],s82 offen sc1\n"
"s_add_u32 s82,s82,4194304\n"
"buffer_load_dwordx4 a[4:7],v10,s[24:27],s82 offen sc1\n"
"s_add_u32 s82,s82,4194304\n"
"buffer_load_dwordx4 a[8:11],v10,s[24:27],s82 offen sc1\n"
"s_add_u32 s82,s82,4194304\n"
"buffer_load_dwordx4 a[12:15],v10,s[24:27],s82 offen sc1\n"
"buffer_load_dwordx4 a[16:19],v8,s[36:39],0 offen\n"
"v_bfe_u32 v9,v7,2,8\n"
"v_lshlrev_b32 v9,4,v9\n"
"buffer_load_dwordx4 v[12:15],v9,s[24:27],s83 offen sc1\n"
"s_mov_b32 s82,149979136\n"
"buffer_load_dwordx4 v[16:19],v9,s[24:27],s82 offen sc1\n"
"s_barrier\n"
"ds_read_b64_tr_b16 a[224:225],v192 offset:0\n"
"ds_read_b64_tr_b16 a[240:241],v192 offset:256\n"
"ds_read_b64_tr_b16 a[226:227],v193 offset:0\n"
"ds_read_b64_tr_b16 a[242:243],v193 offset:256\n"
"ds_read_b64_tr_b16 a[228:229],v194 offset:0\n"
"ds_read_b64_tr_b16 a[244:245],v194 offset:256\n"
"ds_read_b64_tr_b16 a[230:231],v195 offset:0\n"
"ds_read_b64_tr_b16 a[246:247],v195 offset:256\n"
"ds_read_b64_tr_b16 a[232:233],v196 offset:0\n"
"ds_read_b64_tr_b16 a[248:249],v196 offset:256\n"
"ds_read_b64_tr_b16 a[234:235],v197 offset:0\n"
"ds_read_b64_tr_b16 a[250:251],v197 offset:256\n"
"ds_read_b64_tr_b16 a[236:237],v198 offset:0\n"
"ds_read_b64_tr_b16 a[252:253],v198 offset:256\n"
"ds_read_b64_tr_b16 a[238:239],v199 offset:0\n"
"ds_read_b64_tr_b16 a[254:255],v199 offset:256\n"
"s_mov_b32 s84,0x40000\n"
"s_waitcnt vmcnt(7)\n"
".Lfk_wh:\n"
"v_mfma_f32_16x16x32_bf16 v[40:43],a[160:163],v[128:131],0\n"
"v_min3_u32 v36,v128,v129,v130\n"
"v_min3_u32 v37,v131,v132,v133\n"
"v_mfma_f32_16x16x32_bf16 v[40:43],a[164:167],v[132:135],v[40:43]\n"
"v_min3_u32 v36,v36,v134,v135\n"
"v_min3_u32 v37,v37,v136,v137\n"
"v_mfma_f32_16x16x32_bf16 v[40:43],a[168:171],v[136:139],v[40:43]\n"
"v_min3_u32 v36,v36,v138,v139\n"
"v_min3_u32 v37,v37,v140,v141\n"
"v_mfma_f32_16x16x32_bf16 v[40:43],a[172:175],v[140:143],v[40:43]\n"
"v_min3_u32 v36,v36,v142,v143\n"
"v_min3_u32 v37,v37,v144,v145\n"
"v_mfma_f32_16x16x32_bf16 v[40:43],a[176:179],v[144:147],v[40:43]\n"
"v_min3_u32 v36,v36,v146,v147\n"
"v_min3_u32 v37,v37,v148,v149\n"
"v_mfma_f32_16x16x32_bf16 v[40:43],a[180:183],v[148:151],v[40:43]\n"
"v_min3_u32 v36,v36,v150,v151\n"
"v_min3_u32 v37,v37,v152,v153\n"
"v_mfma_f32_16x16x32_bf16 v[40:43],a[184:187],v[152:155],v[40:43]\n"
"v_min3_u32 v36,v36,v154,v155\n"
"v_min3_u32 v37,v37,v156,v157\n"
"v_mfma_f32_16x16x32_bf16 v[40:43],a[188:191],v[156:159],v[40:43]\n"
"v_min3_u32 v36,v36,v158,v159\n"
"v_min_u32 v36,v36,v37\n"
"v_cmp_eq_u32 vcc,0,v36\n"
"v_mfma_f32_16x16x32_bf16 v[44:47],a[192:195],v[128:131],0\n"
"s_cbranch_vccz .Lfk_wh_x\n"
"s_sleep 1\n"
"s_mov_b64 exec,vcc\n"
"buffer_load_dwordx4 v[128:131],v11,s[24:27],0 offen offset:0 sc1\n"
"buffer_load_dwordx4 v[132:135],v11,s[24:27],0 offen offset:64 sc1\n"
"buffer_load_dwordx4 v[136:139],v11,s[24:27],0 offen offset:128 sc1\n"
"buffer_load_dwordx4 v[140:143],v11,s[24:27],0 offen offset:192 sc1\n"
"buffer_load_dwordx4 v[144:147],v11,s[24:27],0 offen offset:256 sc1\n"
"buffer_load_dwordx4 v[148:151],v11,s[24:27],0 offen offset:320 sc1\n"
"buffer_load_dwordx4 v[152:155],v11,s[24:27],0 offen offset:384 sc1\n"
"buffer_load_dwordx4 v[156:159],v11,s[24:27],0 offen offset:448 sc1\n"
"s_mov_b64 exec,-1\n"
"s_sub_u32 s84,s84,1\n"
"s_waitcnt vmcnt(0)\n"
"s_cbranch_scc0 .Lfk_wh\n"
".Lfk_wh_x:\n"
"v_mfma_f32_16x16x32_bf16 v[44:47],a[196:199],v[132:135],v[44:47]\n"
"v_mfma_f32_16x16x32_bf16 v[44:47],a[200:203],v[136:139],v[44:47]\n"
"v_mfma_f32_16x16x32_bf16 v[44:47],a[204:207],v[140:143],v[44:47]\n"
"ds_write_b128 v60,v[40:43] offset:1024\n"
"v_mfma_f32_16x16x32_bf16 v[44:47],a[208:211],v[144:147],v[44:47]\n"
"v_mfma_f32_16x16x32_bf16 v[44:47],a[212:215],v[148:151],v[44:47]\n"
"v_mfma_f32_16x16x32_bf16 v[44:47],a[216:219],v[152:155],v[44:47]\n"
"v_mfma_f32_16x16x32_bf16 v[44:47],a[220:223],v[156:159],v[44:47]\n"
"s_nop 7\n"
"ds_write_b128 v60,v[44:47] offset:2048\n"
"s_waitcnt lgkmcnt(0)\n"
"s_barrier\n"
"ds_read_b128 v[128:131],v61 offset:1024\n"
"ds_read_b128 v[144:147],v61 offset:2048\n"
"ds_read_b128 v[132:135],v61 offset:9216\n"
"ds_read_b128 v[148:151],v61 offset:10240\n"
"ds_read_b128 v[136:139],v61 offset:17408\n"
"ds_read_b128 v[152:155],v61 offset:18432\n"
"ds_read_b128 v[140:143],v61 offset:25600\n"
"ds_read_b128 v[156:159],v61 offset:26624\n"
"s_waitcnt lgkmcnt(0)\n"
"v_pk_add_f32 v[128:129],v[128:129],v[132:133]\n"
"v_pk_add_f32 v[136:137],v[136:137],v[140:141]\n"
"v_pk_add_f32 v[130:131],v[130:131],v[134:135]\n"
"v_pk_add_f32 v[138:139],v[138:139],v[142:143]\n"
"v_pk_add_f32 v[128:129],v[128:129],v[136:137]\n"
"v_pk_add_f32 v[130:131],v[130:131],v[138:139]\n"
"v_pk_add_f32 v[144:145],v[144:145],v[148:149]\n"
"v_pk_add_f32 v[152:153],v[152:153],v[156:157]\n"
"v_pk_add_f32 v[146:147],v[146:147],v[150:151]\n"
"v_pk_add_f32 v[154:155],v[154:155],v[158:159]\n"
"v_pk_add_f32 v[144:145],v[144:145],v[152:153]\n"
"v_pk_add_f32 v[146:147],v[146:147],v[154:155]\n"
"v_mul_f32 v132,0xbfb8aa3b,v128\n"
"v_mul_f32 v133,0xbfb8aa3b,v129\n"
"v_mul_f32 v134,0xbfb8aa3b,v130\n"
"v_mul_f32 v135,0xbfb8aa3b,v131\n"
"v_exp_f32 v132,v132\n"
"v_exp_f32 v133,v133\n"
"v_exp_f32 v134,v134\n"
"v_exp_f32 v135,v135\n"
"v_add_f32 v132,1.0,v132\n"
"v_add_f32 v133,1.0,v133\n"
"v_add_f32 v134,1.0,v134\n"
"v_add_f32 v135,1.0,v135\n"
"v_rcp_f32 v132,v132\n"
"v_rcp_f32 v133,v133\n"
"v_rcp_f32 v134,v134\n"
"v_rcp_f32 v135,v135\n"
"v_pk_mul_f32 v[128:129],v[128:129],v[132:133]\n"
"v_pk_mul_f32 v[130:131],v[130:131],v[134:135]\n"
"v_pk_mul_f32 v[128:129],v[128:129],v[144:145]\n"
"v_pk_mul_f32 v[130:131],v[130:131],v[146:147]\n"
"v_cvt_pk_bf16_f32 v132,v128,v129\n"
"v_cvt_pk_bf16_f32 v133,v130,v131\n"
"v_cndmask_b32 v136,0,v132,s[8:9]\n"
"v_cndmask_b32 v137,0,v133,s[8:9]\n"
"v_cndmask_b32 v138,0,v132,s[10:11]\n"
"v_cndmask_b32 v139,0,v133,s[10:11]\n"
"v_cndmask_b32 v141,0,v133,s[12:13]\n"
"v_cndmask_b32 v143,0,v133,s[14:15]\n"
"v_mfma_f32_16x16x32_bf16 v[40:43],a[224:227],v[136:139],0\n"
"v_cndmask_b32 v142,0,v132,s[14:15]\n"
"v_cndmask_b32 v140,0,v132,s[12:13]\n"
"v_mfma_f32_16x16x32_bf16 v[44:47],a[240:243],v[136:139],0\n"
"v_cndmask_b32 v145,0,v133,s[16:17]\n"
"v_cndmask_b32 v147,0,v133,s[18:19]\n"
"v_mfma_f32_16x16x32_bf16 v[40:43],a[228:231],v[140:143],v[40:43]\n"
"v_cndmask_b32 v146,0,v132,s[18:19]\n"
"v_cndmask_b32 v144,0,v132,s[16:17]\n"
"v_mfma_f32_16x16x32_bf16 v[44:47],a[244:247],v[140:143],v[44:47]\n"
"v_cndmask_b32 v149,0,v133,s[20:21]\n"
"v_cndmask_b32 v151,0,v133,s[22:23]\n"
"v_mfma_f32_16x16x32_bf16 v[40:43],a[232:235],v[144:147],v[40:43]\n"
"v_cndmask_b32 v150,0,v132,s[22:23]\n"
"v_cndmask_b32 v148,0,v132,s[20:21]\n"
"v_mfma_f32_16x16x32_bf16 v[44:47],a[248:251],v[144:147],v[44:47]\n"
"s_nop 0\n"
"v_mfma_f32_16x16x32_bf16 v[40:43],a[236:239],v[148:151],v[40:43]\n"
"v_mfma_f32_16x16x32_bf16 v[44:47],a[252:255],v[148:151],v[44:47]\n"
"s_or_b32 s82,s90,0x800\n"
"s_cmp_eq_u32 s63,s82\n"
"s_nop 4\n"
"v_or_b32 v40,1,v40\n"
"v_or_b32 v41,1,v41\n"
"v_or_b32 v42,1,v42\n"
"v_or_b32 v43,1,v43\n"
"v_or_b32 v44,1,v44\n"
"v_or_b32 v45,1,v45\n"
"v_or_b32 v46,1,v46\n"
"v_or_b32 v47,1,v47\n"
"buffer_store_dwordx4 v[40:43],v62,s[24:27],s69 offen offset:0 sc1\n"
"buffer_store_dwordx4 v[44:47],v62,s[24:27],s69 offen offset:512 sc1\n"
"s_cselect_b32 s68,2146435072,s68\n"
"s_or_b32 s82,s92,0x800\n"
"s_cmp_eq_u32 s63,s82\n"
"s_cselect_b32 s68,2146435072,s68\n"
"buffer_store_dwordx4 v[248:251],v112,s[48:51],s68 offen offset:0 sc1\n"
"buffer_store_dwordx4 v[252:255],v112,s[48:51],s68 offen offset:1024 sc1\n"
"s_cmp_eq_u32 s60,0\n"
"s_cselect_b32 s82,s75,2146435072\n"
"buffer_store_dwordx2 a[154:155],off,s[24:27],s82 sc1\n"
"s_mov_b32 s84,0x40000\n"
"s_waitcnt vmcnt(5)\n"
".Lfk_ws:\n"
"v_xor_b32 v20,s80,v17\n"
"v_xor_b32 v21,s80,v19\n"
"v_or_b32 v20,v20,v21\n"
"v_cmp_ne_u32 vcc,0,v20\n"
"s_nop 0\n"
"s_cbranch_vccz .Lfk_ws_x\n"
"s_mov_b64 exec,vcc\n"
"s_mov_b32 s82,149979136\n"
"buffer_load_dwordx4 v[12:15],v9,s[24:27],s83 offen sc1\n"
"buffer_load_dwordx4 v[16:19],v9,s[24:27],s82 offen sc1\n"
"s_mov_b64 exec,-1\n"
"s_sub_u32 s84,s84,1\n"
"s_waitcnt vmcnt(0)\n"
"s_cbranch_scc0 .Lfk_ws\n"
".Lfk_ws_x:\n"
"v_mfma_f32_16x16x32_bf16 v[24:27],v[66:69],a[0:3],0\n"
"v_mfma_f32_16x16x32_bf16 v[28:31],v[64:67],a[0:3],0\n"
"v_mfma_f32_16x16x32_bf16 v[24:27],v[66:69],a[4:7],v[24:27]\n"
"v_mfma_f32_16x16x32_bf16 v[28:31],v[64:67],a[4:7],v[28:31]\n"
"v_mfma_f32_16x16x32_bf16 v[24:27],v[66:69],a[8:11],v[24:27]\n"
"v_mfma_f32_16x16x32_bf16 v[28:31],v[64:67],a[8:11],v[28:31]\n"
"v_mfma_f32_16x16x32_bf16 v[24:27],v[66:69],a[12:15],v[24:27]\n"
"v_mfma_f32_16x16x32_bf16 v[28:31],v[64:67],a[12:15],v[28:31]\n"
"v_mfma_f32_16x16x32_bf16 v[24:27],v[66:69],a[16:19],v[24:27]\n"
"v_cmp_eq_u32 vcc,v12,v6\n"
"v_mfma_f32_16x16x32_bf16 v[28:31],v[64:67],a[16:19],v[28:31]\n"
"s_nop 0\n"
"v_cndmask_b32 v10,v18,v16,vcc\n"
"v_lshlrev_b32 v11,16,v10\n"
"v_rcp_f32 v11,v11\n"
"v_and_b32 v10,0xffff0000,v10\n"
"s_nop 0\n"
"v_cvt_pk_bf16_f32 v20,v24,v25\n"
"v_cvt_pk_bf16_f32 v21,v26,v27\n"
"v_lshlrev_b32 v24,16,v20\n"
"v_and_b32 v25,0xffff0000,v20\n"
"v_cvt_pk_bf16_f32 v23,v30,v31\n"
"v_cvt_pk_bf16_f32 v22,v28,v29\n"
"v_lshlrev_b32 v26,16,v21\n"
"v_and_b32 v27,0xffff0000,v21\n"
"v_lshlrev_b32 v28,16,v22\n"
"v_and_b32 v29,0xffff0000,v22\n"
"v_lshlrev_b32 v30,16,v23\n"
"v_and_b32 v31,0xffff0000,v23\n"
"v_pk_add_f32 v[32:33],v[24:25],v[10:11] op_sel_hi:[1,0] neg_lo:[0,1] neg_hi:[0,1]\n"
"v_pk_add_f32 v[34:35],v[26:27],v[10:11] op_sel_hi:[1,0] neg_lo:[0,1] neg_hi:[0,1]\n"
"v_pk_add_f32 v[36:37],v[28:29],v[10:11] op_sel_hi:[1,0] neg_lo:[0,1] neg_hi:[0,1]\n"
"v_pk_add_f32 v[38:39],v[30:31],v[10:11] op_sel_hi:[1,0] neg_lo:[0,1] neg_hi:[0,1]\n"
"v_cvt_pk_bf16_f32 v20,v32,v33\n"
"v_cvt_pk_bf16_f32 v21,v34,v35\n"
"v_cvt_pk_bf16_f32 v22,v36,v37\n"
"v_cvt_pk_bf16_f32 v23,v38,v39\n"
"v_lshlrev_b32 v32,16,v20\n"
"v_and_b32 v33,0xffff0000,v20\n"
"v_lshlrev_b32 v34,16,v21\n"
"v_and_b32 v35,0xffff0000,v21\n"
"v_lshlrev_b32 v36,16,v22\n"
"v_and_b32 v37,0xffff0000,v22\n"
"v_lshlrev_b32 v38,16,v23\n"
"v_and_b32 v39,0xffff0000,v23\n"
"v_pk_mul_f32 v[32:33],v[32:33],v[10:11] op_sel:[0,1]\n"
"v_pk_mul_f32 v[34:35],v[34:35],v[10:11] op_sel:[0,1]\n"
"v_pk_mul_f32 v[36:37],v[36:37],v[10:11] op_sel:[0,1]\n"
"v_pk_mul_f32 v[38:39],v[38:39],v[10:11] op_sel:[0,1]\n"
"v_cvt_pk_bf16_f32 v20,v32,v33\n"
"v_cvt_pk_bf16_f32 v21,v34,v35\n"
"v_cvt_pk_bf16_f32 v22,v36,v37\n"
"v_cvt_pk_bf16_f32 v23,v38,v39\n"
"ds_write_b128 v58,v[20:23]\n"
"ds_write_b128 v60,v[24:27] offset:3072\n"
"ds_write_b128 v60,v[28:31] offset:4096\n"
"s_bfe_u32 s82,s73,0x20005\n"
"s_lshl_b32 s82,s82,13\n"
"s_and_b32 s83,s73,31\n"
"s_lshr_b32 s91,s73,7\n"
"s_lshl_b32 s89,s91,9\n"
"s_lshl4_add_u32 s89,s83,s89\n"
"s_add_u32 s89,s89,s82\n"
"s_add_u32 s89,s89,1024\n"
"s_lshl1_add_u32 s83,s83,s91\n"
"s_lshl4_add_u32 s82,s83,s82\n"
"s_add_u32 s82,s82,4096\n"
"v_bfe_u32 v0,v112,4,3\n"
"v_lshl_add_u32 v1,v0,1,s89\n"
"v_and_b32 v3,3,v0\n"
"v_lshl_add_u32 v3,v3,2,s82\n"
"v_lshrrev_b32 v4,2,v0\n"
"v_lshl_add_u32 v3,v4,10,v3\n"
"s_cmp_eq_u32 s91,0\n"
"s_cselect_b32 s82,s90,s92\n"
"s_not_b32 s83,s82\n"
"s_and_b32 s89,s83,0x7ff\n"
"s_lshl_b32 s89,s89,11\n"
"s_and_b32 s91,s73,127\n"
"s_lshl4_add_u32 s89,s91,s89\n"
"s_ashr_i32 s83,s83,31\n"
"s_or_b32 s83,s83,0x80000000\n"
"s_xor_b32 s82,s82,s83\n"
"s_and_b32 s82,s82,0xffff0000\n"
"v_mov_b32 v5,0xbfb8aa3b\n"
"v_mul_f32 v5,s82,v5\n"
"v_exp_f32 v5,v5\n"
"s_nop 0\n"
"v_add_f32 v5,1.0,v5\n"
"v_rcp_f32 v5,v5\n"
"s_nop 0\n"
"v_cvt_pk_bf16_f32 v5,v5,v5\n"
"v_lshlrev_b32 v5,16,v5\n"
"s_lshl_b32 s82,s52,7\n"
"s_add_u32 s82,s82,64\n"
"v_lshrrev_b32 v6,3,v112\n"
"v_and_b32 v6,0x60,v6\n"
"v_and_b32 v7,16,v112\n"
"v_add3_u32 v6,v6,v7,s82\n"
"v_lshrrev_b32 v7,2,v112\n"
"v_add_u32 v7,64,v7\n"
"v_lshlrev_b32 v8,1,v0\n"
"s_sleep 1\n"
"s_mov_b32 s84,0x40000\n"
"buffer_load_dwordx4 v[16:19],v63,s[24:27],s70 offen sc1\n"
"buffer_load_dwordx4 v[20:23],v63,s[24:27],s71 offen sc1\n"
"s_waitcnt vmcnt(0)\n"
".Lfk_wy:\n"
"v_min3_u32 v24,v16,v17,v18\n"
"v_min3_u32 v25,v19,v20,v21\n"
"v_min3_u32 v24,v24,v22,v23\n"
"v_min_u32 v24,v24,v25\n"
"v_cmp_eq_u32 vcc,0,v24\n"
"s_nop 0\n"
"s_cbranch_vccz .Lfk_wy_x\n"
"s_sleep 1\n"
"s_mov_b64 exec,vcc\n"
"buffer_load_dwordx4 v[16:19],v63,s[24:27],s70 offen sc1\n"
"buffer_load_dwordx4 v[20:23],v63,s[24:27],s71 offen sc1\n"
"s_mov_b64 exec,-1\n"
"s_sub_u32 s84,s84,1\n"
"s_waitcnt vmcnt(0)\n"
"s_cbranch_scc0 .Lfk_wy\n"
".Lfk_wy_x:\n"
"s_mov_b32 vcc_lo,-1\n"
"s_mov_b32 vcc_hi,0x00ffffff\n"
"v_add_f32 v16,v16,v20\n"
"v_add_f32 v17,v17,v21\n"
"v_add_f32 v18,v18,v22\n"
"v_add_f32 v19,v19,v23\n"
"v_cndmask_b32 v16,0,v16,vcc\n"
"v_cndmask_b32 v17,0,v17,vcc\n"
"v_cndmask_b32 v18,0,v18,vcc\n"
"v_cndmask_b32 v19,0,v19,vcc\n"
"v_add_f32_dpp v16,v16,v16 quad_perm:[2,3,0,1] row_mask:0xf bank_mask:0xf\n"
"v_add_f32_dpp v17,v17,v17 quad_perm:[2,3,0,1] row_mask:0xf bank_mask:0xf\n"
"v_add_f32_dpp v18,v18,v18 quad_perm:[2,3,0,1] row_mask:0xf bank_mask:0xf\n"
"v_add_f32_dpp v19,v19,v19 quad_perm:[2,3,0,1] row_mask:0xf bank_mask:0xf\n"
"v_add_f32_dpp v16,v16,v16 row_ror:4 row_mask:0xf bank_mask:0xf\n"
"v_add_f32_dpp v17,v17,v17 row_ror:4 row_mask:0xf bank_mask:0xf\n"
"v_add_f32_dpp v18,v18,v18 row_ror:4 row_mask:0xf bank_mask:0xf\n"
"v_add_f32_dpp v19,v19,v19 row_ror:4 row_mask:0xf bank_mask:0xf\n"
"v_add_f32_dpp v16,v16,v16 row_ror:8 row_mask:0xf bank_mask:0xf\n"
"v_add_f32_dpp v17,v17,v17 row_ror:8 row_mask:0xf bank_mask:0xf\n"
"v_add_f32_dpp v18,v18,v18 row_ror:8 row_mask:0xf bank_mask:0xf\n"
"v_add_f32_dpp v19,v19,v19 row_ror:8 row_mask:0xf bank_mask:0xf\n"
"s_mov_b32 exec_lo,0x30003\n"
"s_mov_b32 exec_hi,0x30003\n"
"ds_write_b128 v6,v[16:19]\n"
"s_mov_b64 exec,-1\n"
"s_waitcnt lgkmcnt(0)\n"
"s_barrier\n"
"s_cmp_lg_u32 s52,0\n"
"s_cbranch_scc1 .Lfk_t2\n"
"ds_read2_b32 v[16:17],v7 offset0:0 offset1:64\n"
"ds_read_u16 v2,v1\n"
"ds_read_b32 v3,v3\n"
"s_waitcnt lgkmcnt(0)\n"
"v_lshlrev_b32 v2,16,v2\n"
"v_add_f32 v16,v16,v17\n"
"s_nop 1\n"
"v_add_f32_dpp v16,v16,v16 row_ror:8 row_mask:0xf bank_mask:0xf\n"
"v_mov_b32 v17,v16\n"
"s_nop 1\n"
"v_permlane16_swap_b32 v17,v16\n"
"v_add_f32 v16,v16,v17\n"
"v_mov_b32 v17,v16\n"
"s_nop 1\n"
"v_permlane32_swap_b32 v17,v16\n"
"v_add_f32 v16,v16,v17\n"
"v_cvt_pk_bf16_f32 v16,v16,v16\n"
"v_lshlrev_b32 v16,16,v16\n"
"v_mul_f32 v16,v16,v5\n"
"v_cvt_pk_bf16_f32 v16,v16,v16\n"
"v_lshlrev_b32 v16,16,v16\n"
"v_add_f32 v16,v16,v2\n"
"v_cvt_pk_bf16_f32 v16,v16,v16\n"
"v_lshlrev_b32 v16,16,v16\n"
"v_add_f32 v16,v16,v3\n"
"s_nop 1\n"
"v_mov_b32_dpp v17,v16 quad_perm:[1,0,3,2] row_mask:0xf bank_mask:0xf\n"
"v_cvt_pk_bf16_f32 v16,v16,v17\n"
"s_mov_b64 exec,0x55\n"
"buffer_store_dword v16,v8,s[48:51],s89 offen sc1\n"
"s_mov_b64 exec,-1\n"
".Lfk_t2:\n"
"s_branch .Lfk_end\n"
".Lfk_g0:\n"
"s_waitcnt vmcnt(11) lgkmcnt(0)\n"
"s_barrier\n"
"v_mfma_f32_16x16x32_bf16 v[32:35],a[96:99],a[0:3],v[96:99]\n"
"s_add_u32 m0,s55,4096\n"
"v_exp_f32 v0,v0\n"
"v_mfma_f32_16x16x32_bf16 v[32:35],a[100:103],a[4:7],v[32:35]\n"
"s_add_u32 s54,s54,s22\n"
"v_exp_f32 v1,v1\n"
"v_mfma_f32_16x16x32_bf16 v[32:35],a[104:107],a[8:11],v[32:35]\n"
"buffer_load_dwordx4 v115,s[32:35],s54 offen sc1 lds\n"
"v_exp_f32 v2,v2\n"
"v_cvt_pk_bf16_f32 v64,v0,v1\n"
"v_mfma_f32_16x16x32_bf16 v[32:35],a[108:111],a[12:15],v[32:35]\n"
"v_exp_f32 v3,v3\n"
"v_mfma_f32_16x16x32_bf16 v[36:39],a[124:127],a[12:15],v[96:99]\n"
"v_exp_f32 v4,v4\n"
"v_cvt_pk_bf16_f32 v65,v2,v3\n"
"v_mfma_f32_16x16x32_bf16 v[36:39],a[120:123],a[8:11],v[36:39]\n"
"v_exp_f32 v5,v5\n"
"v_mfma_f32_16x16x32_bf16 v[36:39],a[116:119],a[4:7],v[36:39]\n"
"v_exp_f32 v6,v6\n"
"v_cvt_pk_bf16_f32 v66,v4,v5\n"
"v_mfma_f32_16x16x32_bf16 v[36:39],a[112:115],a[0:3],v[36:39]\n"
"v_exp_f32 v7,v7\n"
"v_mfma_f32_16x16x32_bf16 v[44:47],a[112:115],a[16:19],v[100:103]\n"
"v_exp_f32 v8,v8\n"
"v_cvt_pk_bf16_f32 v67,v6,v7\n"
"v_exp_f32 v9,v9\n"
"v_mfma_f32_16x16x32_bf16 v[44:47],a[116:119],a[20:23],v[44:47]\n"
"ds_read_b128 a[64:67],v113 offset:49152\n"
"ds_read_b128 a[68:71],v113 offset:50176\n"
"v_exp_f32 v10,v10\n"
"v_mfma_f32_16x16x32_bf16 v[44:47],a[120:123],a[24:27],v[44:47]\n"
"ds_read_b128 a[72:75],v113 offset:51200\n"
"ds_read_b128 a[76:79],v113 offset:52224\n"
"v_exp_f32 v11,v11\n"
"v_mfma_f32_16x16x32_bf16 v[44:47],a[124:127],a[28:31],v[44:47]\n"
"ds_read_b128 a[80:83],v113 offset:53248\n"
"ds_read_b128 a[84:87],v113 offset:54272\n"
"v_exp_f32 v12,v12\n"
"v_mfma_f32_16x16x32_bf16 v[40:43],a[108:111],a[28:31],v[100:103]\n"
"ds_read_b128 a[88:91],v113 offset:55296\n"
"ds_read_b128 a[92:95],v113 offset:56320\n"
"v_mfma_f32_16x16x32_bf16 v[40:43],a[104:107],a[24:27],v[40:43]\n"
"v_exp_f32 v13,v13\n"
"v_cvt_pk_bf16_f32 v68,v8,v9\n"
"v_cvt_pk_bf16_f32 v69,v10,v11\n"
"v_mfma_f32_16x16x32_bf16 v[40:43],a[100:103],a[20:23],v[40:43]\n"
"v_exp_f32 v14,v14\n"
"v_cvt_pk_bf16_f32 v70,v12,v13\n"
"v_mfma_f32_16x16x32_bf16 v[40:43],a[96:99],a[16:19],v[40:43]\n"
"v_exp_f32 v15,v15\n"
"v_mfma_f32_16x16x32_bf16 v[48:51],a[96:99],a[32:35],v[104:107]\n"
"v_exp_f32 v16,v16\n"
"v_cvt_pk_bf16_f32 v71,v14,v15\n"
"v_mfma_f32_16x16x32_bf16 v[48:51],a[100:103],a[36:39],v[48:51]\n"
"v_exp_f32 v17,v17\n"
"v_mfma_f32_16x16x32_bf16 v[48:51],a[104:107],a[40:43],v[48:51]\n"
"v_exp_f32 v18,v18\n"
"v_cvt_pk_bf16_f32 v72,v16,v17\n"
"v_mfma_f32_16x16x32_bf16 v[48:51],a[108:111],a[44:47],v[48:51]\n"
"v_exp_f32 v19,v19\n"
"v_mfma_f32_16x16x32_bf16 v[52:55],a[124:127],a[44:47],v[104:107]\n"
"v_exp_f32 v20,v20\n"
"v_cvt_pk_bf16_f32 v73,v18,v19\n"
"v_mfma_f32_16x16x32_bf16 v[52:55],a[120:123],a[40:43],v[52:55]\n"
"v_exp_f32 v21,v21\n"
"v_mfma_f32_16x16x32_bf16 v[52:55],a[116:119],a[36:39],v[52:55]\n"
"v_exp_f32 v22,v22\n"
"v_cvt_pk_bf16_f32 v74,v20,v21\n"
"v_mfma_f32_16x16x32_bf16 v[52:55],a[112:115],a[32:35],v[52:55]\n"
"v_exp_f32 v23,v23\n"
"v_mfma_f32_16x16x32_bf16 v[60:63],a[112:115],a[48:51],v[108:111]\n"
"v_exp_f32 v24,v24\n"
"v_cvt_pk_bf16_f32 v75,v22,v23\n"
"v_mfma_f32_16x16x32_bf16 v[60:63],a[116:119],a[52:55],v[60:63]\n"
"v_exp_f32 v25,v25\n"
"v_mfma_f32_16x16x32_bf16 v[60:63],a[120:123],a[56:59],v[60:63]\n"
"v_exp_f32 v26,v26\n"
"v_exp_f32 v27,v27\n"
"v_mfma_f32_16x16x32_bf16 v[60:63],a[124:127],a[60:63],v[60:63]\n"
"buffer_store_dwordx4 a[228:231],v112,s[24:27],s56 offen offset:1024 sc1\n"
"v_mfma_f32_16x16x32_bf16 v[56:59],a[108:111],a[60:63],v[108:111]\n"
"v_exp_f32 v28,v28\n"
"v_cvt_pk_bf16_f32 v77,v26,v27\n"
"v_mfma_f32_16x16x32_bf16 v[56:59],a[104:107],a[56:59],v[56:59]\n"
"v_exp_f32 v29,v29\n"
"v_mfma_f32_16x16x32_bf16 v[56:59],a[100:103],a[52:55],v[56:59]\n"
"v_exp_f32 v30,v30\n"
"v_cvt_pk_bf16_f32 v78,v28,v29\n"
"v_mfma_f32_16x16x32_bf16 v[56:59],a[96:99],a[48:51],v[56:59]\n"
"s_waitcnt vmcnt(9) lgkmcnt(0)\n"
"v_exp_f32 v31,v31\n"
"v_cvt_pk_bf16_f32 v76,v24,v25\n"
"v_cvt_pk_bf16_f32 v79,v30,v31\n"
"s_barrier\n"
"v_mfma_f32_16x16x32_bf16 v[0:3],a[64:67],a[0:3],v[96:99]\n"
"s_add_u32 m0,s55,8192\n"
"v_exp_f32 v122,v32\n"
"v_mfma_f32_16x16x32_bf16 v[0:3],a[68:71],a[4:7],v[0:3]\n"
"s_add_u32 s54,s54,s22\n"
"v_exp_f32 v123,v33\n"
"v_mfma_f32_16x16x32_bf16 v[0:3],a[72:75],a[8:11],v[0:3]\n"
"ds_read_b128 a[96:99],v114 offset:40960\n"
"ds_read_b128 a[100:103],v114 offset:41984\n"
"v_mfma_f32_16x16x32_bf16 v[0:3],a[76:79],a[12:15],v[0:3]\n"
"v_exp_f32 v119,v34\n"
"v_mfma_f32_16x16x32_bf16 v[4:7],a[92:95],a[12:15],v[96:99]\n"
"ds_read_b128 a[104:107],v114 offset:43008\n"
"v_cvt_pk_bf16_f32 v80,v122,v123\n"
"v_mfma_f32_16x16x32_bf16 v[4:7],a[88:91],a[8:11],v[4:7]\n"
"v_exp_f32 v120,v35\n"
"v_mfma_f32_16x16x32_bf16 v[4:7],a[84:87],a[4:7],v[4:7]\n"
"ds_read_b128 a[108:111],v114 offset:44032\n"
"ds_read_b128 a[112:115],v114 offset:45056\n"
"v_mfma_f32_16x16x32_bf16 v[4:7],a[80:83],a[0:3],v[4:7]\n"
"v_exp_f32 v122,v36\n"
"v_mfma_f32_16x16x32_bf16 v[12:15],a[80:83],a[16:19],v[100:103]\n"
"ds_read_b128 a[116:119],v114 offset:46080\n"
"v_cvt_pk_bf16_f32 v81,v119,v120\n"
"v_mfma_f32_16x16x32_bf16 v[12:15],a[84:87],a[20:23],v[12:15]\n"
"v_exp_f32 v123,v37\n"
"v_mfma_f32_16x16x32_bf16 v[12:15],a[88:91],a[24:27],v[12:15]\n"
"ds_read_b128 a[120:123],v114 offset:47104\n"
"ds_read_b128 a[124:127],v114 offset:48128\n"
"v_mfma_f32_16x16x32_bf16 v[12:15],a[92:95],a[28:31],v[12:15]\n"
"v_exp_f32 v119,v38\n"
"v_cvt_pk_bf16_f32 v82,v122,v123\n"
"v_mfma_f32_16x16x32_bf16 v[8:11],a[76:79],a[28:31],v[100:103]\n"
"buffer_store_dwordx4 a[168:171],v112,s[24:27],s3 offen offset:2048 sc1\n"
"v_mfma_f32_16x16x32_bf16 v[8:11],a[72:75],a[24:27],v[8:11]\n"
"v_exp_f32 v120,v39\n"
"v_mfma_f32_16x16x32_bf16 v[8:11],a[68:71],a[20:23],v[8:11]\n"
"buffer_load_dwordx4 v115,s[32:35],s54 offen sc1 lds\n"
"v_mfma_f32_16x16x32_bf16 v[8:11],a[64:67],a[16:19],v[8:11]\n"
"v_exp_f32 v122,v40\n"
"v_mfma_f32_16x16x32_bf16 v[16:19],a[64:67],a[32:35],v[104:107]\n"
"v_cvt_pk_bf16_f32 v83,v119,v120\n"
"v_or3_b32 v118,v80,v81,v82\n"
"v_mfma_f32_16x16x32_bf16 v[16:19],a[68:71],a[36:39],v[16:19]\n"
"v_exp_f32 v123,v41\n"
"v_mfma_f32_16x16x32_bf16 v[16:19],a[72:75],a[40:43],v[16:19]\n"
"v_cvt_pk_bf16_f32 v84,v122,v123\n"
"v_or3_b32 v118,v118,v83,v84\n"
"v_mfma_f32_16x16x32_bf16 v[16:19],a[76:79],a[44:47],v[16:19]\n"
"v_exp_f32 v119,v42\n"
"v_mfma_f32_16x16x32_bf16 v[20:23],a[92:95],a[44:47],v[104:107]\n"
"v_mfma_f32_16x16x32_bf16 v[20:23],a[88:91],a[40:43],v[20:23]\n"
"v_exp_f32 v120,v43\n"
"v_mfma_f32_16x16x32_bf16 v[20:23],a[84:87],a[36:39],v[20:23]\n"
"v_cvt_pk_bf16_f32 v85,v119,v120\n"
"v_mfma_f32_16x16x32_bf16 v[20:23],a[80:83],a[32:35],v[20:23]\n"
"v_exp_f32 v122,v44\n"
"v_mfma_f32_16x16x32_bf16 v[28:31],a[80:83],a[48:51],v[108:111]\n"
"buffer_store_dwordx4 a[172:175],v112,s[24:27],s3 offen offset:3072 sc1\n"
"v_mfma_f32_16x16x32_bf16 v[28:31],a[84:87],a[52:55],v[28:31]\n"
"v_exp_f32 v123,v45\n"
"v_mfma_f32_16x16x32_bf16 v[28:31],a[88:91],a[56:59],v[28:31]\n"
"s_sub_u32 s3,s3,16384\n"
"v_mfma_f32_16x16x32_bf16 v[28:31],a[92:95],a[60:63],v[28:31]\n"
"v_exp_f32 v119,v46\n"
"v_mfma_f32_16x16x32_bf16 v[24:27],a[76:79],a[60:63],v[108:111]\n"
"v_cvt_pk_bf16_f32 v86,v122,v123\n"
"v_or3_b32 v118,v118,v85,v86\n"
"v_mfma_f32_16x16x32_bf16 v[24:27],a[72:75],a[56:59],v[24:27]\n"
"v_exp_f32 v120,v47\n"
"v_mfma_f32_16x16x32_bf16 v[24:27],a[68:71],a[52:55],v[24:27]\n"
"v_cvt_pk_bf16_f32 v87,v119,v120\n"
"v_mfma_f32_16x16x32_bf16 v[24:27],a[64:67],a[48:51],v[24:27]\n"
"s_waitcnt lgkmcnt(0)\n"
"v_exp_f32 v122,v48\n"
"v_mfma_f32_16x16x32_bf16 v[128:131],a[96:99],v[64:67],0\n"
"v_exp_f32 v123,v49\n"
"v_mfma_f32_16x16x32_bf16 v[160:163],a[96:99],v[68:71],0\n"
"ds_read_b128 a[64:67],v113 offset:32768\n"
"ds_read_b128 a[68:71],v113 offset:33792\n"
"v_mfma_f32_16x16x32_bf16 v[192:195],a[96:99],v[72:75],0\n"
"v_exp_f32 v119,v50\n"
"v_mfma_f32_16x16x32_bf16 v[224:227],a[96:99],v[76:79],0\n"
"ds_read_b128 a[72:75],v113 offset:34816\n"
"v_cvt_pk_bf16_f32 v88,v122,v123\n"
"v_mfma_f32_16x16x32_bf16 a[140:143],a[144:147],v[76:79],0\n"
"v_exp_f32 v120,v51\n"
"v_mfma_f32_16x16x32_bf16 v[228:231],a[100:103],v[76:79],0\n"
"ds_read_b128 a[76:79],v113 offset:35840\n"
"ds_read_b128 a[80:83],v113 offset:36864\n"
"v_mfma_f32_16x16x32_bf16 v[132:135],a[100:103],v[64:67],0\n"
"v_exp_f32 v122,v52\n"
"v_mfma_f32_16x16x32_bf16 v[164:167],a[100:103],v[68:71],0\n"
"ds_read_b128 a[84:87],v113 offset:37888\n"
"v_cvt_pk_bf16_f32 v89,v119,v120\n"
"v_mfma_f32_16x16x32_bf16 v[196:199],a[100:103],v[72:75],0\n"
"v_exp_f32 v123,v53\n"
"v_mfma_f32_16x16x32_bf16 a[136:139],a[144:147],v[72:75],0\n"
"ds_read_b128 a[88:91],v113 offset:38912\n"
"ds_read_b128 a[92:95],v113 offset:39936\n"
"v_mfma_f32_16x16x32_bf16 v[200:203],a[104:107],v[72:75],0\n"
"v_exp_f32 v119,v54\n"
"v_cvt_pk_bf16_f32 v90,v122,v123\n"
"v_mfma_f32_16x16x32_bf16 v[232:235],a[104:107],v[76:79],0\n"
"buffer_store_dwordx4 a[176:179],v112,s[24:27],s3 offen offset:0 sc1\n"
"v_mfma_f32_16x16x32_bf16 v[136:139],a[104:107],v[64:67],0\n"
"v_exp_f32 v120,v55\n"
"v_mfma_f32_16x16x32_bf16 v[168:171],a[104:107],v[68:71],0\n"
"v_cvt_pk_bf16_f32 v91,v119,v120\n"
"v_or3_b32 v118,v118,v87,v88\n"
"v_mfma_f32_16x16x32_bf16 a[132:135],a[144:147],v[68:71],0\n"
"v_exp_f32 v122,v56\n"
"v_mfma_f32_16x16x32_bf16 v[172:175],a[108:111],v[68:71],0\n"
"v_or3_b32 v118,v118,v89,v90\n"
"v_mfma_f32_16x16x32_bf16 v[204:207],a[108:111],v[72:75],0\n"
"v_exp_f32 v123,v57\n"
"v_mfma_f32_16x16x32_bf16 v[236:239],a[108:111],v[76:79],0\n"
"v_cvt_pk_bf16_f32 v92,v122,v123\n"
"v_or3_b32 v118,v118,v91,v92\n"
"v_mfma_f32_16x16x32_bf16 v[140:143],a[108:111],v[64:67],0\n"
"v_exp_f32 v119,v58\n"
"v_mfma_f32_16x16x32_bf16 a[128:131],a[144:147],v[64:67],0\n"
"v_mfma_f32_16x16x32_bf16 v[144:147],a[112:115],v[64:67],0\n"
"v_exp_f32 v120,v59\n"
"v_mfma_f32_16x16x32_bf16 v[176:179],a[112:115],v[68:71],0\n"
"v_cvt_pk_bf16_f32 v93,v119,v120\n"
"v_mfma_f32_16x16x32_bf16 v[208:211],a[112:115],v[72:75],0\n"
"v_exp_f32 v122,v60\n"
"v_mfma_f32_16x16x32_bf16 v[240:243],a[112:115],v[76:79],0\n"
"v_mfma_f32_16x16x32_bf16 v[244:247],a[116:119],v[76:79],0\n"
"v_exp_f32 v123,v61\n"
"v_mfma_f32_16x16x32_bf16 v[148:151],a[116:119],v[64:67],0\n"
"v_cvt_pk_bf16_f32 v94,v122,v123\n"
"v_or3_b32 v118,v118,v93,v94\n"
"v_mfma_f32_16x16x32_bf16 v[180:183],a[116:119],v[68:71],0\n"
"v_exp_f32 v119,v62\n"
"v_mfma_f32_16x16x32_bf16 v[212:215],a[116:119],v[72:75],0\n"
"buffer_store_dwordx4 a[232:235],v112,s[24:27],s56 offen offset:2048 sc1\n"
"v_mfma_f32_16x16x32_bf16 v[216:219],a[120:123],v[72:75],0\n"
"v_exp_f32 v120,v63\n"
"v_mfma_f32_16x16x32_bf16 v[248:251],a[120:123],v[76:79],0\n"
"v_cvt_pk_bf16_f32 v95,v119,v120\n"
"v_or_b32 v118,v118,v95\n"
"v_mfma_f32_16x16x32_bf16 v[152:155],a[120:123],v[64:67],0\n"
"v_and_b32 v118,0x40004000,v118\n"
"v_cmp_ne_u32 vcc,0,v118\n"
"v_mfma_f32_16x16x32_bf16 v[184:187],a[120:123],v[68:71],0\n"
"v_mfma_f32_16x16x32_bf16 v[188:191],a[124:127],v[68:71],0\n"
"v_mfma_f32_16x16x32_bf16 v[220:223],a[124:127],v[72:75],0\n"
"v_mfma_f32_16x16x32_bf16 v[252:255],a[124:127],v[76:79],0\n"
"v_mfma_f32_16x16x32_bf16 v[156:159],a[124:127],v[64:67],0\n"
"s_cbranch_vccnz .Lfk_cs_k1\n"
".Lfk_cr_k1:\n"
"s_waitcnt lgkmcnt(0)\n"
"s_barrier\n"
"v_mfma_f32_16x16x32_bf16 v[32:35],a[64:67],a[0:3],v[96:99]\n"
"s_add_u32 m0,s55,12288\n"
"v_exp_f32 v122,v0\n"
"v_mfma_f32_16x16x32_bf16 v[32:35],a[68:71],a[4:7],v[32:35]\n"
"s_add_u32 s54,s54,s22\n"
"v_exp_f32 v123,v1\n"
"v_mfma_f32_16x16x32_bf16 v[32:35],a[72:75],a[8:11],v[32:35]\n"
"ds_read_b128 a[96:99],v114 offset:8192\n"
"ds_read_b128 a[100:103],v114 offset:9216\n"
"v_mfma_f32_16x16x32_bf16 v[32:35],a[76:79],a[12:15],v[32:35]\n"
"v_exp_f32 v119,v2\n"
"v_mfma_f32_16x16x32_bf16 v[36:39],a[92:95],a[12:15],v[96:99]\n"
"ds_read_b128 a[104:107],v114 offset:10240\n"
"v_cvt_pk_bf16_f32 v64,v122,v123\n"
"v_mfma_f32_16x16x32_bf16 v[36:39],a[88:91],a[8:11],v[36:39]\n"
"v_exp_f32 v120,v3\n"
"v_mfma_f32_16x16x32_bf16 v[36:39],a[84:87],a[4:7],v[36:39]\n"
"ds_read_b128 a[108:111],v114 offset:11264\n"
"ds_read_b128 a[112:115],v114 offset:12288\n"
"v_mfma_f32_16x16x32_bf16 v[36:39],a[80:83],a[0:3],v[36:39]\n"
"v_exp_f32 v122,v4\n"
"v_mfma_f32_16x16x32_bf16 v[44:47],a[80:83],a[16:19],v[100:103]\n"
"ds_read_b128 a[116:119],v114 offset:13312\n"
"v_cvt_pk_bf16_f32 v65,v119,v120\n"
"v_mfma_f32_16x16x32_bf16 v[44:47],a[84:87],a[20:23],v[44:47]\n"
"v_exp_f32 v123,v5\n"
"v_mfma_f32_16x16x32_bf16 v[44:47],a[88:91],a[24:27],v[44:47]\n"
"ds_read_b128 a[120:123],v114 offset:14336\n"
"ds_read_b128 a[124:127],v114 offset:15360\n"
"v_mfma_f32_16x16x32_bf16 v[44:47],a[92:95],a[28:31],v[44:47]\n"
"v_exp_f32 v119,v6\n"
"v_cvt_pk_bf16_f32 v66,v122,v123\n"
"v_mfma_f32_16x16x32_bf16 v[40:43],a[76:79],a[28:31],v[100:103]\n"
"buffer_store_dwordx4 a[180:183],v112,s[24:27],s3 offen offset:1024 sc1\n"
"v_mfma_f32_16x16x32_bf16 v[40:43],a[72:75],a[24:27],v[40:43]\n"
"v_exp_f32 v120,v7\n"
"v_mfma_f32_16x16x32_bf16 v[40:43],a[68:71],a[20:23],v[40:43]\n"
"buffer_load_dwordx4 v115,s[32:35],s54 offen sc1 lds\n"
"v_mfma_f32_16x16x32_bf16 v[40:43],a[64:67],a[16:19],v[40:43]\n"
"v_exp_f32 v122,v8\n"
"v_mfma_f32_16x16x32_bf16 v[48:51],a[64:67],a[32:35],v[104:107]\n"
"v_cvt_pk_bf16_f32 v67,v119,v120\n"
"v_or3_b32 v118,v64,v65,v66\n"
"v_mfma_f32_16x16x32_bf16 v[48:51],a[68:71],a[36:39],v[48:51]\n"
"v_exp_f32 v123,v9\n"
"v_mfma_f32_16x16x32_bf16 v[48:51],a[72:75],a[40:43],v[48:51]\n"
"v_cvt_pk_bf16_f32 v68,v122,v123\n"
"v_or3_b32 v118,v118,v67,v68\n"
"v_mfma_f32_16x16x32_bf16 v[48:51],a[76:79],a[44:47],v[48:51]\n"
"v_exp_f32 v119,v10\n"
"v_mfma_f32_16x16x32_bf16 v[52:55],a[92:95],a[44:47],v[104:107]\n"
"v_mfma_f32_16x16x32_bf16 v[52:55],a[88:91],a[40:43],v[52:55]\n"
"v_exp_f32 v120,v11\n"
"v_mfma_f32_16x16x32_bf16 v[52:55],a[84:87],a[36:39],v[52:55]\n"
"v_cvt_pk_bf16_f32 v69,v119,v120\n"
"v_mfma_f32_16x16x32_bf16 v[52:55],a[80:83],a[32:35],v[52:55]\n"
"v_exp_f32 v122,v12\n"
"v_mfma_f32_16x16x32_bf16 v[60:63],a[80:83],a[48:51],v[108:111]\n"
"buffer_store_dwordx4 a[184:187],v112,s[24:27],s3 offen offset:2048 sc1\n"
"v_mfma_f32_16x16x32_bf16 v[60:63],a[84:87],a[52:55],v[60:63]\n"
"v_exp_f32 v123,v13\n"
"v_mfma_f32_16x16x32_bf16 v[60:63],a[88:91],a[56:59],v[60:63]\n"
"v_cvt_pk_bf16_f32 v70,v122,v123\n"
"v_mfma_f32_16x16x32_bf16 v[60:63],a[92:95],a[60:63],v[60:63]\n"
"v_exp_f32 v119,v14\n"
"v_mfma_f32_16x16x32_bf16 v[56:59],a[76:79],a[60:63],v[108:111]\n"
"v_or3_b32 v118,v118,v69,v70\n"
"v_mfma_f32_16x16x32_bf16 v[56:59],a[72:75],a[56:59],v[56:59]\n"
"v_exp_f32 v120,v15\n"
"v_mfma_f32_16x16x32_bf16 v[56:59],a[68:71],a[52:55],v[56:59]\n"
"v_cvt_pk_bf16_f32 v71,v119,v120\n"
"v_mfma_f32_16x16x32_bf16 v[56:59],a[64:67],a[48:51],v[56:59]\n"
"s_waitcnt lgkmcnt(0)\n"
"v_exp_f32 v122,v16\n"
"v_mfma_f32_16x16x32_bf16 v[128:131],a[96:99],v[80:83],v[128:131]\n"
"v_exp_f32 v123,v17\n"
"v_mfma_f32_16x16x32_bf16 v[160:163],a[96:99],v[84:87],v[160:163]\n"
"ds_read_b128 a[64:67],v113 offset:0\n"
"ds_read_b128 a[68:71],v113 offset:1024\n"
"v_mfma_f32_16x16x32_bf16 v[192:195],a[96:99],v[88:91],v[192:195]\n"
"v_exp_f32 v119,v18\n"
"v_mfma_f32_16x16x32_bf16 v[224:227],a[96:99],v[92:95],v[224:227]\n"
"ds_read_b128 a[72:75],v113 offset:2048\n"
"v_cvt_pk_bf16_f32 v72,v122,v123\n"
"v_mfma_f32_16x16x32_bf16 a[140:143],a[144:147],v[92:95],a[140:143]\n"
"v_exp_f32 v120,v19\n"
"v_mfma_f32_16x16x32_bf16 v[228:231],a[100:103],v[92:95],v[228:231]\n"
"ds_read_b128 a[76:79],v113 offset:3072\n"
"ds_read_b128 a[80:83],v113 offset:4096\n"
"v_mfma_f32_16x16x32_bf16 v[132:135],a[100:103],v[80:83],v[132:135]\n"
"v_exp_f32 v122,v20\n"
"v_mfma_f32_16x16x32_bf16 v[164:167],a[100:103],v[84:87],v[164:167]\n"
"ds_read_b128 a[84:87],v113 offset:5120\n"
"v_cvt_pk_bf16_f32 v73,v119,v120\n"
"v_mfma_f32_16x16x32_bf16 v[196:199],a[100:103],v[88:91],v[196:199]\n"
"v_exp_f32 v123,v21\n"
"v_mfma_f32_16x16x32_bf16 a[136:139],a[144:147],v[88:91],a[136:139]\n"
"ds_read_b128 a[88:91],v113 offset:6144\n"
"ds_read_b128 a[92:95],v113 offset:7168\n"
"v_mfma_f32_16x16x32_bf16 v[200:203],a[104:107],v[88:91],v[200:203]\n"
"v_exp_f32 v119,v22\n"
"v_mfma_f32_16x16x32_bf16 v[232:235],a[104:107],v[92:95],v[232:235]\n"
"buffer_store_dwordx4 a[188:191],v112,s[24:27],s3 offen offset:3072 sc1\n"
"v_mfma_f32_16x16x32_bf16 v[136:139],a[104:107],v[80:83],v[136:139]\n"
"v_exp_f32 v120,v23\n"
"v_mfma_f32_16x16x32_bf16 v[168:171],a[104:107],v[84:87],v[168:171]\n"
"s_or_b32 s3,s3,0x3c000\n"
"v_cvt_pk_bf16_f32 v74,v122,v123\n"
"v_cvt_pk_bf16_f32 v75,v119,v120\n"
"v_mfma_f32_16x16x32_bf16 a[132:135],a[144:147],v[84:87],a[132:135]\n"
"v_exp_f32 v122,v24\n"
"v_mfma_f32_16x16x32_bf16 v[172:175],a[108:111],v[84:87],v[172:175]\n"
"v_or3_b32 v118,v118,v71,v72\n"
"v_or3_b32 v118,v118,v73,v74\n"
"v_mfma_f32_16x16x32_bf16 v[204:207],a[108:111],v[88:91],v[204:207]\n"
"v_exp_f32 v123,v25\n"
"v_mfma_f32_16x16x32_bf16 v[236:239],a[108:111],v[92:95],v[236:239]\n"
"v_cvt_pk_bf16_f32 v76,v122,v123\n"
"v_or3_b32 v118,v118,v75,v76\n"
"v_mfma_f32_16x16x32_bf16 v[140:143],a[108:111],v[80:83],v[140:143]\n"
"v_exp_f32 v119,v26\n"
"v_mfma_f32_16x16x32_bf16 a[128:131],a[144:147],v[80:83],a[128:131]\n"
"v_mfma_f32_16x16x32_bf16 v[144:147],a[112:115],v[80:83],v[144:147]\n"
"v_exp_f32 v120,v27\n"
"v_mfma_f32_16x16x32_bf16 v[176:179],a[112:115],v[84:87],v[176:179]\n"
"v_cvt_pk_bf16_f32 v77,v119,v120\n"
"v_mfma_f32_16x16x32_bf16 v[208:211],a[112:115],v[88:91],v[208:211]\n"
"v_exp_f32 v122,v28\n"
"v_mfma_f32_16x16x32_bf16 v[240:243],a[112:115],v[92:95],v[240:243]\n"
"v_mfma_f32_16x16x32_bf16 v[244:247],a[116:119],v[92:95],v[244:247]\n"
"v_exp_f32 v123,v29\n"
"v_mfma_f32_16x16x32_bf16 v[148:151],a[116:119],v[80:83],v[148:151]\n"
"v_cvt_pk_bf16_f32 v78,v122,v123\n"
"v_or3_b32 v118,v118,v77,v78\n"
"v_mfma_f32_16x16x32_bf16 v[180:183],a[116:119],v[84:87],v[180:183]\n"
"v_exp_f32 v119,v30\n"
"v_mfma_f32_16x16x32_bf16 v[212:215],a[116:119],v[88:91],v[212:215]\n"
"buffer_store_dwordx4 a[236:239],v112,s[24:27],s56 offen offset:3072 sc1\n"
"v_mfma_f32_16x16x32_bf16 v[216:219],a[120:123],v[88:91],v[216:219]\n"
"v_exp_f32 v120,v31\n"
"v_mfma_f32_16x16x32_bf16 v[248:251],a[120:123],v[92:95],v[248:251]\n"
"v_cvt_pk_bf16_f32 v79,v119,v120\n"
"v_or_b32 v118,v118,v79\n"
"v_mfma_f32_16x16x32_bf16 v[152:155],a[120:123],v[80:83],v[152:155]\n"
"v_and_b32 v118,0x40004000,v118\n"
"v_cmp_ne_u32 vcc,0,v118\n"
"v_mfma_f32_16x16x32_bf16 v[184:187],a[120:123],v[84:87],v[184:187]\n"
"v_mfma_f32_16x16x32_bf16 v[188:191],a[124:127],v[84:87],v[188:191]\n"
"v_mfma_f32_16x16x32_bf16 v[220:223],a[124:127],v[88:91],v[220:223]\n"
"v_mfma_f32_16x16x32_bf16 v[252:255],a[124:127],v[92:95],v[252:255]\n"
"v_mfma_f32_16x16x32_bf16 v[156:159],a[124:127],v[80:83],v[156:159]\n"
"s_cbranch_vccnz .Lfk_cs_k2\n"
".Lfk_cr_k2:\n"
"s_waitcnt vmcnt(13) lgkmcnt(0)\n"
"s_barrier\n"
"s_branch .Lfk_g1\n"
".Lfk_winA:\n"
"v_max3_f32 v118,v32,v33,v34\n"
"v_max3_f32 v119,v35,v36,v37\n"
"v_max3_f32 v119,v119,v38,v39\n"
"v_max3_f32 v118,v118,v40,v41\n"
"v_max3_f32 v119,v119,v42,v43\n"
"v_max3_f32 v118,v118,v44,v45\n"
"v_max3_f32 v119,v119,v46,v47\n"
"v_max3_f32 v118,v118,v48,v49\n"
"v_max3_f32 v119,v119,v50,v51\n"
"v_max3_f32 v118,v118,v52,v53\n"
"v_max3_f32 v119,v119,v54,v55\n"
"v_max3_f32 v118,v118,v56,v57\n"
"v_max3_f32 v119,v119,v58,v59\n"
"v_max3_f32 v118,v118,v60,v61\n"
"v_max3_f32 v119,v119,v62,v63\n"
"v_max_f32 v118,v118,v119\n"
"v_cmp_lt_f32 vcc,s93,v118\n"
"s_nop 0\n"
"s_cbranch_vccnz .Lfk_cs_x2\n"
".Lfk_cr_x2:\n"
"s_mov_b32 s64,s61\n"
"s_cmp_eq_u32 s52,3\n"
"s_cselect_b32 s67,172032,0\n"
"v_mfma_f32_16x16x32_bf16 v[8:11],a[64:67],a[16:19],v[100:103]\n"
"s_swappc_b64 s[94:95],s[20:21]\n"
"ds_read_b128 a[96:99],v114 offset:24576\n"
"ds_read_b128 a[100:103],v114 offset:25600\n"
"v_mfma_f32_16x16x32_bf16 v[8:11],a[68:71],a[20:23],v[8:11]\n"
"ds_read_b128 a[104:107],v114 offset:26624\n"
"ds_read_b128 a[108:111],v114 offset:27648\n"
"v_mfma_f32_16x16x32_bf16 v[8:11],a[72:75],a[24:27],v[8:11]\n"
"ds_read_b128 a[112:115],v114 offset:28672\n"
"ds_read_b128 a[116:119],v114 offset:29696\n"
"v_mfma_f32_16x16x32_bf16 v[8:11],a[76:79],a[28:31],v[8:11]\n"
"ds_read_b128 a[120:123],v114 offset:30720\n"
"ds_read_b128 a[124:127],v114 offset:31744\n"
"v_mfma_f32_16x16x32_bf16 v[12:15],a[92:95],a[28:31],v[100:103]\n"
"v_add_f32 v0,0x80000000,v126\n"
"v_min_f32 v0,v0,v96\n"
"v_mfma_f32_16x16x32_bf16 v[12:15],a[88:91],a[24:27],v[12:15]\n"
"v_add_f32 v1,0xf149f2ca,v126\n"
"v_min_f32 v1,v1,v97\n"
"v_mfma_f32_16x16x32_bf16 v[12:15],a[84:87],a[20:23],v[12:15]\n"
"v_add_f32 v2,0xf1c9f2ca,v126\n"
"v_min_f32 v2,v2,v98\n"
"v_mfma_f32_16x16x32_bf16 v[12:15],a[80:83],a[16:19],v[12:15]\n"
"v_add_f32 v3,0xf2177617,v126\n"
"v_min_f32 v3,v3,v99\n"
"v_mfma_f32_16x16x32_bf16 v[20:23],a[80:83],a[32:35],v[104:107]\n"
"v_add_f32 v4,0xf349f2ca,v126\n"
"v_min_f32 v4,v4,v96\n"
"v_mfma_f32_16x16x32_bf16 v[20:23],a[84:87],a[36:39],v[20:23]\n"
"v_add_f32 v5,0xf35691f6,v126\n"
"v_min_f32 v5,v5,v97\n"
"v_mfma_f32_16x16x32_bf16 v[20:23],a[88:91],a[40:43],v[20:23]\n"
"v_add_f32 v6,0xf3633123,v126\n"
"v_min_f32 v6,v6,v98\n"
"v_mfma_f32_16x16x32_bf16 v[20:23],a[92:95],a[44:47],v[20:23]\n"
"v_add_f32 v7,0xf36fd050,v126\n"
"v_min_f32 v7,v7,v99\n"
"v_mfma_f32_16x16x32_bf16 v[16:19],a[76:79],a[44:47],v[104:107]\n"
"s_add_u32 s101,s101,4\n"
"v_exp_f32 v32,v32\n"
"v_mfma_f32_16x16x32_bf16 v[16:19],a[72:75],a[40:43],v[16:19]\n"
"s_add_u32 m0,s67,0\n"
"v_exp_f32 v33,v33\n"
"v_mfma_f32_16x16x32_bf16 v[16:19],a[68:71],a[36:39],v[16:19]\n"
"v_exp_f32 v34,v34\n"
"v_mfma_f32_16x16x32_bf16 v[16:19],a[64:67],a[32:35],v[16:19]\n"
"v_exp_f32 v35,v35\n"
"v_mfma_f32_16x16x32_bf16 v[24:27],a[64:67],a[48:51],v[108:111]\n"
"v_exp_f32 v36,v36\n"
"v_mfma_f32_16x16x32_bf16 v[24:27],a[68:71],a[52:55],v[24:27]\n"
"v_exp_f32 v37,v37\n"
"v_mfma_f32_16x16x32_bf16 v[24:27],a[72:75],a[56:59],v[24:27]\n"
"v_exp_f32 v38,v38\n"
"v_mfma_f32_16x16x32_bf16 v[24:27],a[76:79],a[60:63],v[24:27]\n"
"v_exp_f32 v39,v39\n"
"v_mfma_f32_16x16x32_bf16 v[28:31],a[92:95],a[60:63],v[108:111]\n"
"v_exp_f32 v40,v40\n"
"v_mfma_f32_16x16x32_bf16 v[28:31],a[88:91],a[56:59],v[28:31]\n"
"v_exp_f32 v41,v41\n"
"v_mfma_f32_16x16x32_bf16 v[28:31],a[84:87],a[52:55],v[28:31]\n"
"v_exp_f32 v42,v42\n"
"v_mfma_f32_16x16x32_bf16 v[28:31],a[80:83],a[48:51],v[28:31]\n"
"v_exp_f32 v43,v43\n"
"v_mfma_f32_16x16x32_bf16 v[4:7],a[80:83],a[0:3],v[4:7]\n"
"v_exp_f32 v44,v44\n"
"v_mfma_f32_16x16x32_bf16 v[4:7],a[84:87],a[4:7],v[4:7]\n"
"v_exp_f32 v45,v45\n"
"v_mfma_f32_16x16x32_bf16 v[4:7],a[88:91],a[8:11],v[4:7]\n"
"buffer_load_dwordx4 v112,s[24:27],s64 offen offset:0 sc1 lds\n"
"v_mfma_f32_16x16x32_bf16 v[4:7],a[92:95],a[12:15],v[4:7]\n"
"v_exp_f32 v46,v46\n"
"v_mfma_f32_16x16x32_bf16 v[0:3],a[76:79],a[12:15],v[0:3]\n"
"buffer_load_dwordx4 v112,s[24:27],s64 offen offset:1024 sc1 lds\n"
"v_mfma_f32_16x16x32_bf16 v[0:3],a[72:75],a[8:11],v[0:3]\n"
"v_exp_f32 v47,v47\n"
"v_mfma_f32_16x16x32_bf16 v[0:3],a[68:71],a[4:7],v[0:3]\n"
"buffer_load_dwordx4 v112,s[24:27],s64 offen offset:2048 sc1 lds\n"
"v_mfma_f32_16x16x32_bf16 v[0:3],a[64:67],a[0:3],v[0:3]\n"
"s_waitcnt lgkmcnt(0)\n"
"v_exp_f32 v48,v48\n"
"v_mfma_f32_16x16x32_bf16 v[128:131],a[96:99],v[64:67],v[128:131]\n"
"v_exp_f32 v49,v49\n"
"v_mfma_f32_16x16x32_bf16 v[160:163],a[96:99],v[68:71],v[160:163]\n"
"ds_read_b128 a[64:67],v113 offset:16384\n"
"ds_read_b128 a[68:71],v113 offset:17408\n"
"v_mfma_f32_16x16x32_bf16 v[192:195],a[96:99],v[72:75],v[192:195]\n"
"v_exp_f32 v50,v50\n"
"v_mfma_f32_16x16x32_bf16 v[224:227],a[96:99],v[76:79],v[224:227]\n"
"ds_read_b128 a[72:75],v113 offset:18432\n"
"ds_read_b128 a[76:79],v113 offset:19456\n"
"v_mfma_f32_16x16x32_bf16 a[140:143],a[144:147],v[76:79],a[140:143]\n"
"v_exp_f32 v51,v51\n"
"v_mfma_f32_16x16x32_bf16 v[228:231],a[100:103],v[76:79],v[228:231]\n"
"ds_read_b128 a[80:83],v113 offset:20480\n"
"ds_read_b128 a[84:87],v113 offset:21504\n"
"v_mfma_f32_16x16x32_bf16 v[132:135],a[100:103],v[64:67],v[132:135]\n"
"v_exp_f32 v52,v52\n"
"v_mfma_f32_16x16x32_bf16 v[164:167],a[100:103],v[68:71],v[164:167]\n"
"ds_read_b128 a[88:91],v113 offset:22528\n"
"ds_read_b128 a[92:95],v113 offset:23552\n"
"v_mfma_f32_16x16x32_bf16 v[196:199],a[100:103],v[72:75],v[196:199]\n"
"v_exp_f32 v53,v53\n"
"v_mfma_f32_16x16x32_bf16 a[136:139],a[144:147],v[72:75],a[136:139]\n"
"s_waitcnt vmcnt(14)\n"
"ds_add_u32 v124,a152\n"
"v_mfma_f32_16x16x32_bf16 v[200:203],a[104:107],v[72:75],v[200:203]\n"
"v_exp_f32 v54,v54\n"
"v_mfma_f32_16x16x32_bf16 v[232:235],a[104:107],v[76:79],v[232:235]\n"
"buffer_load_dwordx4 v112,s[24:27],s64 offen offset:3072 sc1 lds\n"
"v_mfma_f32_16x16x32_bf16 v[136:139],a[104:107],v[64:67],v[136:139]\n"
"s_add_u32 m0,s67,4096\n"
"v_exp_f32 v55,v55\n"
"v_mfma_f32_16x16x32_bf16 v[168:171],a[104:107],v[68:71],v[168:171]\n"
"buffer_load_dwordx4 v117,s[24:27],s64 offen offset:0 sc1 lds\n"
"v_mfma_f32_16x16x32_bf16 a[132:135],a[144:147],v[68:71],a[132:135]\n"
"v_exp_f32 v56,v56\n"
"v_mfma_f32_16x16x32_bf16 v[172:175],a[108:111],v[68:71],v[172:175]\n"
"buffer_load_dwordx4 v117,s[24:27],s64 offen offset:1024 sc1 lds\n"
"v_mfma_f32_16x16x32_bf16 v[204:207],a[108:111],v[72:75],v[204:207]\n"
"v_exp_f32 v57,v57\n"
"v_mfma_f32_16x16x32_bf16 v[236:239],a[108:111],v[76:79],v[236:239]\n"
"buffer_load_dwordx4 v117,s[24:27],s64 offen offset:2048 sc1 lds\n"
"v_mfma_f32_16x16x32_bf16 v[140:143],a[108:111],v[64:67],v[140:143]\n"
"v_exp_f32 v58,v58\n"
"v_mfma_f32_16x16x32_bf16 a[128:131],a[144:147],v[64:67],a[128:131]\n"
"v_cvt_pk_bf16_f32 v80,v32,v33\n"
"v_cvt_pk_bf16_f32 v81,v34,v35\n"
"v_mfma_f32_16x16x32_bf16 v[144:147],a[112:115],v[64:67],v[144:147]\n"
"v_exp_f32 v59,v59\n"
"v_mfma_f32_16x16x32_bf16 v[176:179],a[112:115],v[68:71],v[176:179]\n"
"v_cvt_pk_bf16_f32 v82,v36,v37\n"
"v_cvt_pk_bf16_f32 v83,v38,v39\n"
"v_mfma_f32_16x16x32_bf16 v[208:211],a[112:115],v[72:75],v[208:211]\n"
"v_exp_f32 v60,v60\n"
"v_mfma_f32_16x16x32_bf16 v[240:243],a[112:115],v[76:79],v[240:243]\n"
"buffer_load_dwordx4 v117,s[24:27],s64 offen offset:3072 sc1 lds\n"
"v_mfma_f32_16x16x32_bf16 v[244:247],a[116:119],v[76:79],v[244:247]\n"
"s_add_u32 s64,s64,16384\n"
"v_exp_f32 v61,v61\n"
"v_mfma_f32_16x16x32_bf16 v[148:151],a[116:119],v[64:67],v[148:151]\n"
"s_andn2_b32 m0,s55,16384\n"
"s_add_u32 s54,s54,s22\n"
"v_cvt_pk_bf16_f32 v85,v42,v43\n"
"v_mfma_f32_16x16x32_bf16 v[180:183],a[116:119],v[68:71],v[180:183]\n"
"v_exp_f32 v62,v62\n"
"v_mfma_f32_16x16x32_bf16 v[212:215],a[116:119],v[72:75],v[212:215]\n"
"ds_read_b32 v125,v124\n"
"v_cvt_pk_bf16_f32 v84,v40,v41\n"
"v_mfma_f32_16x16x32_bf16 v[216:219],a[120:123],v[72:75],v[216:219]\n"
"v_exp_f32 v63,v63\n"
"v_mfma_f32_16x16x32_bf16 v[248:251],a[120:123],v[76:79],v[248:251]\n"
"buffer_load_dwordx4 v115,s[32:35],s54 offen sc1 lds\n"
"v_mfma_f32_16x16x32_bf16 v[152:155],a[120:123],v[64:67],v[152:155]\n"
"buffer_store_dwordx4 a[240:243],v112,s[24:27],s56 offen offset:0 sc1\n"
"v_mfma_f32_16x16x32_bf16 v[184:187],a[120:123],v[68:71],v[184:187]\n"
"v_cvt_pk_bf16_f32 v86,v44,v45\n"
"v_cvt_pk_bf16_f32 v87,v46,v47\n"
"v_mfma_f32_16x16x32_bf16 v[188:191],a[124:127],v[68:71],v[188:191]\n"
"s_waitcnt lgkmcnt(0)\n"
"v_cvt_pk_bf16_f32 v88,v48,v49\n"
"v_cvt_pk_bf16_f32 v89,v50,v51\n"
"v_mfma_f32_16x16x32_bf16 v[220:223],a[124:127],v[72:75],v[220:223]\n"
"v_cvt_pk_bf16_f32 v90,v52,v53\n"
"v_cvt_pk_bf16_f32 v91,v54,v55\n"
"v_mfma_f32_16x16x32_bf16 v[252:255],a[124:127],v[76:79],v[252:255]\n"
"v_cvt_pk_bf16_f32 v92,v56,v57\n"
"v_cvt_pk_bf16_f32 v93,v58,v59\n"
"v_mfma_f32_16x16x32_bf16 v[156:159],a[124:127],v[64:67],v[156:159]\n"
"v_cvt_pk_bf16_f32 v94,v60,v61\n"
"v_cvt_pk_bf16_f32 v95,v62,v63\n"
"v_max3_f32 v118,v8,v9,v10\n"
"v_max3_f32 v118,v118,v11,v12\n"
"v_max3_f32 v118,v118,v13,v14\n"
"v_max3_f32 v118,v118,v15,v20\n"
"v_max3_f32 v118,v118,v21,v22\n"
"v_max3_f32 v118,v118,v23,v16\n"
"v_max3_f32 v118,v118,v17,v18\n"
"v_max3_f32 v118,v118,v19,v24\n"
"v_max3_f32 v118,v118,v25,v26\n"
"v_max3_f32 v118,v118,v27,v28\n"
"v_max3_f32 v118,v118,v29,v30\n"
"v_max3_f32 v118,v118,v31,v4\n"
"v_max3_f32 v118,v118,v5,v6\n"
"v_max3_f32 v118,v118,v7,v0\n"
"v_max3_f32 v118,v118,v1,v2\n"
"v_max_f32 v118,v118,v3\n"
"v_cmp_lt_f32 vcc,s93,v118\n"
"s_nop 0\n"
"s_cbranch_vccnz .Lfk_cs_u3\n"
".Lfk_cr_u3:\n"
"v_cmp_gt_u32 vcc,s101,v125\n"
"s_nop 0\n"
"s_cbranch_vccnz .Lfk_cs_mu3\n"
".Lfk_cr_mu3:\n"
"v_mfma_f32_16x16x32_bf16 v[48:51],a[64:67],a[32:35],v[104:107]\n"
"ds_read_b128 a[96:99],v114 offset:57344\n"
"ds_read_b128 a[100:103],v114 offset:58368\n"
"v_mfma_f32_16x16x32_bf16 v[48:51],a[68:71],a[36:39],v[48:51]\n"
"ds_read_b128 a[104:107],v114 offset:59392\n"
"ds_read_b128 a[108:111],v114 offset:60416\n"
"v_mfma_f32_16x16x32_bf16 v[48:51],a[72:75],a[40:43],v[48:51]\n"
"ds_read_b128 a[112:115],v114 offset:61440\n"
"ds_read_b128 a[116:119],v114 offset:62464\n"
"v_mfma_f32_16x16x32_bf16 v[48:51],a[76:79],a[44:47],v[48:51]\n"
"ds_read_b128 a[120:123],v114 offset:63488\n"
"ds_read_b128 a[124:127],v114 offset:64512\n"
"v_mfma_f32_16x16x32_bf16 v[52:55],a[92:95],a[44:47],v[104:107]\n"
"ds_read_b128 a[0:3],v116 offset:40960\n"
"ds_read_b128 a[4:7],v116 offset:41984\n"
"v_mfma_f32_16x16x32_bf16 v[52:55],a[88:91],a[40:43],v[52:55]\n"
"ds_read_b128 a[8:11],v116 offset:43008\n"
"ds_read_b128 a[12:15],v116 offset:44032\n"
"v_mfma_f32_16x16x32_bf16 v[52:55],a[84:87],a[36:39],v[52:55]\n"
"v_add_f32 v40,0x80000000,v126\n"
"v_min_f32 v40,v40,v100\n"
"v_mfma_f32_16x16x32_bf16 v[52:55],a[80:83],a[32:35],v[52:55]\n"
"v_add_f32 v41,0xf149f2ca,v126\n"
"v_min_f32 v41,v41,v101\n"
"v_mfma_f32_16x16x32_bf16 v[60:63],a[80:83],a[48:51],v[108:111]\n"
"v_add_f32 v42,0xf1c9f2ca,v126\n"
"v_min_f32 v42,v42,v102\n"
"v_mfma_f32_16x16x32_bf16 v[60:63],a[84:87],a[52:55],v[60:63]\n"
"v_add_f32 v43,0xf2177617,v126\n"
"v_min_f32 v43,v43,v103\n"
"v_mfma_f32_16x16x32_bf16 v[60:63],a[88:91],a[56:59],v[60:63]\n"
"v_add_f32 v44,0xf349f2ca,v126\n"
"v_min_f32 v44,v44,v100\n"
"v_mfma_f32_16x16x32_bf16 v[60:63],a[92:95],a[60:63],v[60:63]\n"
"v_add_f32 v45,0xf35691f6,v126\n"
"v_min_f32 v45,v45,v101\n"
"v_mfma_f32_16x16x32_bf16 v[56:59],a[76:79],a[60:63],v[108:111]\n"
"v_add_f32 v46,0xf3633123,v126\n"
"v_min_f32 v46,v46,v102\n"
"v_mfma_f32_16x16x32_bf16 v[56:59],a[72:75],a[56:59],v[56:59]\n"
"v_add_f32 v47,0xf36fd050,v126\n"
"v_min_f32 v47,v47,v103\n"
"v_mfma_f32_16x16x32_bf16 v[56:59],a[68:71],a[52:55],v[56:59]\n"
"s_add_u32 s101,s101,4\n"
"v_exp_f32 v0,v0\n"
"v_mfma_f32_16x16x32_bf16 v[56:59],a[64:67],a[48:51],v[56:59]\n"
"s_add_u32 m0,s55,4096\n"
"v_exp_f32 v1,v1\n"
"v_mfma_f32_16x16x32_bf16 v[40:43],a[64:67],a[16:19],v[40:43]\n"
"s_add_u32 s54,s54,s22\n"
"v_exp_f32 v2,v2\n"
"v_mfma_f32_16x16x32_bf16 v[40:43],a[68:71],a[20:23],v[40:43]\n"
"v_exp_f32 v3,v3\n"
"v_mfma_f32_16x16x32_bf16 v[40:43],a[72:75],a[24:27],v[40:43]\n"
"v_exp_f32 v4,v4\n"
"v_mfma_f32_16x16x32_bf16 v[40:43],a[76:79],a[28:31],v[40:43]\n"
"v_exp_f32 v5,v5\n"
"v_mfma_f32_16x16x32_bf16 v[44:47],a[92:95],a[28:31],v[44:47]\n"
"v_exp_f32 v6,v6\n"
"v_mfma_f32_16x16x32_bf16 v[44:47],a[88:91],a[24:27],v[44:47]\n"
"v_exp_f32 v7,v7\n"
"v_mfma_f32_16x16x32_bf16 v[44:47],a[84:87],a[20:23],v[44:47]\n"
"v_exp_f32 v8,v8\n"
"v_mfma_f32_16x16x32_bf16 v[44:47],a[80:83],a[16:19],v[44:47]\n"
"s_waitcnt lgkmcnt(4)\n"
"v_exp_f32 v9,v9\n"
"v_mfma_f32_16x16x32_bf16 v[128:131],a[96:99],v[80:83],v[128:131]\n"
"v_exp_f32 v10,v10\n"
"v_mfma_f32_16x16x32_bf16 v[160:163],a[96:99],v[84:87],v[160:163]\n"
"v_exp_f32 v11,v11\n"
"v_mfma_f32_16x16x32_bf16 v[192:195],a[96:99],v[88:91],v[192:195]\n"
"v_exp_f32 v12,v12\n"
"v_mfma_f32_16x16x32_bf16 v[224:227],a[96:99],v[92:95],v[224:227]\n"
"v_exp_f32 v13,v13\n"
"v_mfma_f32_16x16x32_bf16 a[140:143],a[144:147],v[92:95],a[140:143]\n"
"v_exp_f32 v14,v14\n"
"v_mfma_f32_16x16x32_bf16 v[228:231],a[100:103],v[92:95],v[228:231]\n"
"v_exp_f32 v15,v15\n"
"v_mfma_f32_16x16x32_bf16 v[132:135],a[100:103],v[80:83],v[132:135]\n"
"v_exp_f32 v16,v16\n"
"v_mfma_f32_16x16x32_bf16 v[164:167],a[100:103],v[84:87],v[164:167]\n"
"v_exp_f32 v17,v17\n"
"v_mfma_f32_16x16x32_bf16 v[196:199],a[100:103],v[88:91],v[196:199]\n"
"v_exp_f32 v18,v18\n"
"v_mfma_f32_16x16x32_bf16 a[136:139],a[144:147],v[88:91],a[136:139]\n"
"v_exp_f32 v19,v19\n"
"v_mfma_f32_16x16x32_bf16 v[200:203],a[104:107],v[88:91],v[200:203]\n"
"v_exp_f32 v20,v20\n"
"v_mfma_f32_16x16x32_bf16 v[232:235],a[104:107],v[92:95],v[232:235]\n"
"v_exp_f32 v21,v21\n"
"v_mfma_f32_16x16x32_bf16 v[136:139],a[104:107],v[80:83],v[136:139]\n"
"ds_read_b128 a[64:67],v113 offset:49152\n"
"ds_read_b128 a[68:71],v113 offset:50176\n"
"v_mfma_f32_16x16x32_bf16 v[168:171],a[104:107],v[84:87],v[168:171]\n"
"v_exp_f32 v22,v22\n"
"v_mfma_f32_16x16x32_bf16 a[132:135],a[144:147],v[84:87],a[132:135]\n"
"v_exp_f32 v23,v23\n"
"v_mfma_f32_16x16x32_bf16 v[172:175],a[108:111],v[84:87],v[172:175]\n"
"ds_read_b128 a[72:75],v113 offset:51200\n"
"ds_read_b128 a[76:79],v113 offset:52224\n"
"v_mfma_f32_16x16x32_bf16 v[204:207],a[108:111],v[88:91],v[204:207]\n"
"v_exp_f32 v24,v24\n"
"v_mfma_f32_16x16x32_bf16 v[236:239],a[108:111],v[92:95],v[236:239]\n"
"ds_read_b128 a[80:83],v113 offset:53248\n"
"ds_read_b128 a[84:87],v113 offset:54272\n"
"v_mfma_f32_16x16x32_bf16 v[140:143],a[108:111],v[80:83],v[140:143]\n"
"v_exp_f32 v25,v25\n"
"v_mfma_f32_16x16x32_bf16 a[128:131],a[144:147],v[80:83],a[128:131]\n"
"v_exp_f32 v26,v26\n"
"v_mfma_f32_16x16x32_bf16 v[144:147],a[112:115],v[80:83],v[144:147]\n"
"ds_read_b128 a[88:91],v113 offset:55296\n"
"ds_read_b128 a[92:95],v113 offset:56320\n"
"v_mfma_f32_16x16x32_bf16 v[176:179],a[112:115],v[84:87],v[176:179]\n"
"v_exp_f32 v27,v27\n"
"v_mfma_f32_16x16x32_bf16 v[208:211],a[112:115],v[88:91],v[208:211]\n"
"s_waitcnt vmcnt(11)\n"
"ds_add_u32 v124,a152\n"
"v_mfma_f32_16x16x32_bf16 v[240:243],a[112:115],v[92:95],v[240:243]\n"
"v_exp_f32 v28,v28\n"
"v_mfma_f32_16x16x32_bf16 v[244:247],a[116:119],v[92:95],v[244:247]\n"
"v_exp_f32 v29,v29\n"
"v_mfma_f32_16x16x32_bf16 v[148:151],a[116:119],v[80:83],v[148:151]\n"
"buffer_load_dwordx4 v115,s[32:35],s54 offen sc1 lds\n"
"v_mfma_f32_16x16x32_bf16 v[180:183],a[116:119],v[84:87],v[180:183]\n"
"s_add_u32 m0,s67,16384\n"
"v_exp_f32 v30,v30\n"
"v_mfma_f32_16x16x32_bf16 v[212:215],a[116:119],v[88:91],v[212:215]\n"
"ds_read_b32 v125,v124\n"
"v_cvt_pk_bf16_f32 v64,v0,v1\n"
"v_mfma_f32_16x16x32_bf16 v[216:219],a[120:123],v[88:91],v[216:219]\n"
"v_exp_f32 v31,v31\n"
"v_mfma_f32_16x16x32_bf16 v[248:251],a[120:123],v[92:95],v[248:251]\n"
"buffer_load_dwordx4 v112,s[24:27],s64 offen offset:0 sc1 lds\n"
"v_mfma_f32_16x16x32_bf16 v[152:155],a[120:123],v[80:83],v[152:155]\n"
"buffer_load_dwordx4 v112,s[24:27],s64 offen offset:1024 sc1 lds\n"
"v_mfma_f32_16x16x32_bf16 v[184:187],a[120:123],v[84:87],v[184:187]\n"
"buffer_load_dwordx4 v112,s[24:27],s64 offen offset:2048 sc1 lds\n"
"v_mfma_f32_16x16x32_bf16 v[188:191],a[124:127],v[84:87],v[188:191]\n"
"s_waitcnt lgkmcnt(0)\n"
"buffer_load_dwordx4 v112,s[24:27],s64 offen offset:3072 sc1 lds\n"
"v_mfma_f32_16x16x32_bf16 v[220:223],a[124:127],v[88:91],v[220:223]\n"
"s_add_u32 m0,s67,20480\n"
"v_cvt_pk_bf16_f32 v65,v2,v3\n"
"v_cvt_pk_bf16_f32 v66,v4,v5\n"
"v_mfma_f32_16x16x32_bf16 v[252:255],a[124:127],v[92:95],v[252:255]\n"
"buffer_load_dwordx4 v117,s[24:27],s64 offen offset:0 sc1 lds\n"
"v_mfma_f32_16x16x32_bf16 v[156:159],a[124:127],v[80:83],v[156:159]\n"
"buffer_load_dwordx4 v117,s[24:27],s64 offen offset:1024 sc1 lds\n"
"buffer_load_dwordx4 v117,s[24:27],s64 offen offset:2048 sc1 lds\n"
"buffer_load_dwordx4 v117,s[24:27],s64 offen offset:3072 sc1 lds\n"
"buffer_store_dwordx4 a[244:247],v112,s[24:27],s56 offen offset:1024 sc1\n"
"s_add_u32 s64,s64,16384\n"
"v_cvt_pk_bf16_f32 v67,v6,v7\n"
"v_cvt_pk_bf16_f32 v68,v8,v9\n"
"v_cvt_pk_bf16_f32 v69,v10,v11\n"
"v_cvt_pk_bf16_f32 v70,v12,v13\n"
"v_cvt_pk_bf16_f32 v71,v14,v15\n"
"v_cvt_pk_bf16_f32 v72,v16,v17\n"
"v_cvt_pk_bf16_f32 v73,v18,v19\n"
"v_cvt_pk_bf16_f32 v74,v20,v21\n"
"v_cvt_pk_bf16_f32 v75,v22,v23\n"
"v_cvt_pk_bf16_f32 v76,v24,v25\n"
"v_cvt_pk_bf16_f32 v77,v26,v27\n"
"v_cvt_pk_bf16_f32 v78,v28,v29\n"
"v_cvt_pk_bf16_f32 v79,v30,v31\n"
"v_max3_f32 v118,v48,v49,v50\n"
"v_max3_f32 v118,v118,v51,v52\n"
"v_max3_f32 v118,v118,v53,v54\n"
"v_max3_f32 v118,v118,v55,v60\n"
"v_max3_f32 v118,v118,v61,v62\n"
"v_max3_f32 v118,v118,v63,v56\n"
"v_max3_f32 v118,v118,v57,v58\n"
"v_max3_f32 v118,v118,v59,v40\n"
"v_max3_f32 v118,v118,v41,v42\n"
"v_max3_f32 v118,v118,v43,v44\n"
"v_max3_f32 v118,v118,v45,v46\n"
"v_max_f32 v118,v118,v47\n"
"v_cmp_lt_f32 vcc,s93,v118\n"
"s_nop 0\n"
"s_cbranch_vccnz .Lfk_cs_u0\n"
".Lfk_cr_u0:\n"
"v_cmp_gt_u32 vcc,s101,v125\n"
"s_nop 0\n"
"s_cbranch_vccnz .Lfk_cs_mu0\n"
".Lfk_cr_mu0:\n"
"v_mfma_f32_16x16x32_bf16 v[24:27],a[64:67],a[48:51],v[108:111]\n"
"ds_read_b128 a[96:99],v114 offset:40960\n"
"ds_read_b128 a[100:103],v114 offset:41984\n"
"v_mfma_f32_16x16x32_bf16 v[24:27],a[68:71],a[52:55],v[24:27]\n"
"ds_read_b128 a[104:107],v114 offset:43008\n"
"ds_read_b128 a[108:111],v114 offset:44032\n"
"v_mfma_f32_16x16x32_bf16 v[24:27],a[72:75],a[56:59],v[24:27]\n"
"ds_read_b128 a[112:115],v114 offset:45056\n"
"ds_read_b128 a[116:119],v114 offset:46080\n"
"v_mfma_f32_16x16x32_bf16 v[24:27],a[76:79],a[60:63],v[24:27]\n"
"ds_read_b128 a[120:123],v114 offset:47104\n"
"ds_read_b128 a[124:127],v114 offset:48128\n"
"v_mfma_f32_16x16x32_bf16 v[28:31],a[92:95],a[60:63],v[108:111]\n"
"ds_read_b128 a[16:19],v116 offset:8192\n"
"ds_read_b128 a[20:23],v116 offset:9216\n"
"v_mfma_f32_16x16x32_bf16 v[28:31],a[88:91],a[56:59],v[28:31]\n"
"ds_read_b128 a[24:27],v116 offset:10240\n"
"ds_read_b128 a[28:31],v116 offset:11264\n"
"v_mfma_f32_16x16x32_bf16 v[28:31],a[84:87],a[52:55],v[28:31]\n"
"v_add_f32 v16,0x80000000,v126\n"
"v_min_f32 v16,v16,v104\n"
"v_mfma_f32_16x16x32_bf16 v[28:31],a[80:83],a[48:51],v[28:31]\n"
"v_add_f32 v17,0xf149f2ca,v126\n"
"v_add_f32 v18,0xf1c9f2ca,v126\n"
"v_add_f32 v19,0xf2177617,v126\n"
"v_min_f32 v19,v19,v107\n"
"v_add_f32 v20,0xf349f2ca,v126\n"
"v_min_f32 v20,v20,v104\n"
"v_add_f32 v21,0xf35691f6,v126\n"
"v_min_f32 v21,v21,v105\n"
"v_add_f32 v22,0xf3633123,v126\n"
"v_min_f32 v22,v22,v106\n"
"v_add_f32 v23,0xf36fd050,v126\n"
"v_min_f32 v23,v23,v107\n"
"v_min_f32 v18,v18,v106\n"
"v_min_f32 v17,v17,v105\n"
"v_mfma_f32_16x16x32_bf16 v[20:23],a[80:83],a[32:35],v[20:23]\n"
"s_add_u32 s101,s101,4\n"
"v_exp_f32 v40,v40\n"
"v_mfma_f32_16x16x32_bf16 v[20:23],a[84:87],a[36:39],v[20:23]\n"
"s_add_u32 m0,s55,8192\n"
"v_exp_f32 v41,v41\n"
"v_mfma_f32_16x16x32_bf16 v[20:23],a[88:91],a[40:43],v[20:23]\n"
"s_add_u32 s54,s54,s22\n"
"v_exp_f32 v42,v42\n"
"v_mfma_f32_16x16x32_bf16 v[20:23],a[92:95],a[44:47],v[20:23]\n"
"v_exp_f32 v43,v43\n"
"v_mfma_f32_16x16x32_bf16 v[16:19],a[76:79],a[44:47],v[16:19]\n"
"v_exp_f32 v44,v44\n"
"v_mfma_f32_16x16x32_bf16 v[16:19],a[72:75],a[40:43],v[16:19]\n"
"v_exp_f32 v45,v45\n"
"v_mfma_f32_16x16x32_bf16 v[16:19],a[68:71],a[36:39],v[16:19]\n"
"v_exp_f32 v46,v46\n"
"v_mfma_f32_16x16x32_bf16 v[16:19],a[64:67],a[32:35],v[16:19]\n"
"s_waitcnt lgkmcnt(4)\n"
"v_exp_f32 v47,v47\n"
"v_mfma_f32_16x16x32_bf16 v[128:131],a[96:99],v[64:67],v[128:131]\n"
"v_exp_f32 v48,v48\n"
"v_mfma_f32_16x16x32_bf16 v[160:163],a[96:99],v[68:71],v[160:163]\n"
"v_exp_f32 v49,v49\n"
"v_mfma_f32_16x16x32_bf16 v[192:195],a[96:99],v[72:75],v[192:195]\n"
"ds_read_b128 a[64:67],v113 offset:32768\n"
"ds_read_b128 a[68:71],v113 offset:33792\n"
"v_mfma_f32_16x16x32_bf16 v[224:227],a[96:99],v[76:79],v[224:227]\n"
"v_exp_f32 v50,v50\n"
"v_mfma_f32_16x16x32_bf16 a[140:143],a[144:147],v[76:79],a[140:143]\n"
"ds_read_b128 a[72:75],v113 offset:34816\n"
"ds_read_b128 a[76:79],v113 offset:35840\n"
"v_mfma_f32_16x16x32_bf16 v[228:231],a[100:103],v[76:79],v[228:231]\n"
"v_exp_f32 v51,v51\n"
"v_mfma_f32_16x16x32_bf16 v[132:135],a[100:103],v[64:67],v[132:135]\n"
"ds_read_b128 a[80:83],v113 offset:36864\n"
"ds_read_b128 a[84:87],v113 offset:37888\n"
"v_mfma_f32_16x16x32_bf16 v[164:167],a[100:103],v[68:71],v[164:167]\n"
"v_exp_f32 v52,v52\n"
"v_mfma_f32_16x16x32_bf16 v[196:199],a[100:103],v[72:75],v[196:199]\n"
"v_exp_f32 v53,v53\n"
"v_mfma_f32_16x16x32_bf16 a[136:139],a[144:147],v[72:75],a[136:139]\n"
"ds_read_b128 a[88:91],v113 offset:38912\n"
"ds_read_b128 a[92:95],v113 offset:39936\n"
"v_mfma_f32_16x16x32_bf16 v[200:203],a[104:107],v[72:75],v[200:203]\n"
"v_exp_f32 v54,v54\n"
"v_mfma_f32_16x16x32_bf16 v[232:235],a[104:107],v[76:79],v[232:235]\n"
"s_waitcnt vmcnt(11)\n"
"ds_add_u32 v124,a152\n"
"v_mfma_f32_16x16x32_bf16 v[136:139],a[104:107],v[64:67],v[136:139]\n"
"v_exp_f32 v55,v55\n"
"v_mfma_f32_16x16x32_bf16 v[168:171],a[104:107],v[68:71],v[168:171]\n"
"buffer_load_dwordx4 v115,s[32:35],s54 offen sc1 lds\n"
"v_mfma_f32_16x16x32_bf16 a[132:135],a[144:147],v[68:71],a[132:135]\n"
"s_add_u32 m0,s67,49152\n"
"v_exp_f32 v56,v56\n"
"v_mfma_f32_16x16x32_bf16 v[172:175],a[108:111],v[68:71],v[172:175]\n"
"buffer_load_dwordx4 v112,s[24:27],s64 offen offset:0 sc1 lds\n"
"v_mfma_f32_16x16x32_bf16 v[204:207],a[108:111],v[72:75],v[204:207]\n"
"v_exp_f32 v57,v57\n"
"v_mfma_f32_16x16x32_bf16 v[236:239],a[108:111],v[76:79],v[236:239]\n"
"buffer_load_dwordx4 v112,s[24:27],s64 offen offset:1024 sc1 lds\n"
"v_mfma_f32_16x16x32_bf16 v[140:143],a[108:111],v[64:67],v[140:143]\n"
"v_exp_f32 v58,v58\n"
"v_mfma_f32_16x16x32_bf16 a[128:131],a[144:147],v[64:67],a[128:131]\n"
"buffer_load_dwordx4 v112,s[24:27],s64 offen offset:2048 sc1 lds\n"
"v_mfma_f32_16x16x32_bf16 v[144:147],a[112:115],v[64:67],v[144:147]\n"
"v_exp_f32 v59,v59\n"
"v_mfma_f32_16x16x32_bf16 v[176:179],a[112:115],v[68:71],v[176:179]\n"
"buffer_load_dwordx4 v112,s[24:27],s64 offen offset:3072 sc1 lds\n"
"v_mfma_f32_16x16x32_bf16 v[208:211],a[112:115],v[72:75],v[208:211]\n"
"s_add_u32 m0,s67,53248\n"
"v_exp_f32 v60,v60\n"
"v_mfma_f32_16x16x32_bf16 v[240:243],a[112:115],v[76:79],v[240:243]\n"
"buffer_load_dwordx4 v117,s[24:27],s64 offen offset:0 sc1 lds\n"
"v_mfma_f32_16x16x32_bf16 v[244:247],a[116:119],v[76:79],v[244:247]\n"
"v_exp_f32 v61,v61\n"
"v_mfma_f32_16x16x32_bf16 v[148:151],a[116:119],v[64:67],v[148:151]\n"
"buffer_load_dwordx4 v117,s[24:27],s64 offen offset:1024 sc1 lds\n"
"v_mfma_f32_16x16x32_bf16 v[180:183],a[116:119],v[68:71],v[180:183]\n"
"v_exp_f32 v62,v62\n"
"v_mfma_f32_16x16x32_bf16 v[212:215],a[116:119],v[72:75],v[212:215]\n"
"ds_read_b32 v125,v124\n"
"v_cvt_pk_bf16_f32 v84,v40,v41\n"
"v_mfma_f32_16x16x32_bf16 v[216:219],a[120:123],v[72:75],v[216:219]\n"
"v_exp_f32 v63,v63\n"
"v_mfma_f32_16x16x32_bf16 v[248:251],a[120:123],v[76:79],v[248:251]\n"
"buffer_load_dwordx4 v117,s[24:27],s64 offen offset:2048 sc1 lds\n"
"v_mfma_f32_16x16x32_bf16 v[152:155],a[120:123],v[64:67],v[152:155]\n"
"buffer_load_dwordx4 v117,s[24:27],s64 offen offset:3072 sc1 lds\n"
"v_mfma_f32_16x16x32_bf16 v[184:187],a[120:123],v[68:71],v[184:187]\n"
"s_add_u32 s64,s64,16384\n"
"buffer_store_dwordx4 a[248:251],v112,s[24:27],s56 offen offset:2048 sc1\n"
"v_mfma_f32_16x16x32_bf16 v[188:191],a[124:127],v[68:71],v[188:191]\n"
"s_waitcnt lgkmcnt(0)\n"
"s_and_b32 s67,s67,8192\n"
"v_cvt_pk_bf16_f32 v85,v42,v43\n"
"v_mfma_f32_16x16x32_bf16 v[220:223],a[124:127],v[72:75],v[220:223]\n"
"v_cvt_pk_bf16_f32 v86,v44,v45\n"
"v_cvt_pk_bf16_f32 v87,v46,v47\n"
"v_mfma_f32_16x16x32_bf16 v[252:255],a[124:127],v[76:79],v[252:255]\n"
"v_cvt_pk_bf16_f32 v88,v48,v49\n"
"v_cvt_pk_bf16_f32 v89,v50,v51\n"
"v_mfma_f32_16x16x32_bf16 v[156:159],a[124:127],v[64:67],v[156:159]\n"
"v_cvt_pk_bf16_f32 v90,v52,v53\n"
"v_cvt_pk_bf16_f32 v91,v54,v55\n"
"v_cvt_pk_bf16_f32 v92,v56,v57\n"
"v_cvt_pk_bf16_f32 v93,v58,v59\n"
"v_cvt_pk_bf16_f32 v94,v60,v61\n"
"v_cvt_pk_bf16_f32 v95,v62,v63\n"
"v_max3_f32 v118,v24,v25,v26\n"
"v_max3_f32 v118,v118,v27,v28\n"
"v_max3_f32 v118,v118,v29,v30\n"
"v_max3_f32 v118,v118,v31,v20\n"
"v_max3_f32 v118,v118,v21,v22\n"
"v_max3_f32 v118,v118,v23,v16\n"
"v_max3_f32 v118,v118,v17,v18\n"
"v_max_f32 v118,v118,v19\n"
"v_cmp_lt_f32 vcc,s93,v118\n"
"s_nop 0\n"
"s_cbranch_vccnz .Lfk_cs_u1\n"
".Lfk_cr_u1:\n"
"v_cmp_gt_u32 vcc,s101,v125\n"
"s_nop 0\n"
"s_cbranch_vccnz .Lfk_cs_mu1\n"
".Lfk_cr_mu1:\n"
"v_mov_b32 v113,v112\n"
"v_add_f32 v56,0x80000000,v126\n"
"v_min_f32 v56,v56,v108\n"
"v_add_f32 v57,0xf149f2ca,v126\n"
"v_min_f32 v57,v57,v109\n"
"v_add_f32 v58,0xf1c9f2ca,v126\n"
"v_min_f32 v58,v58,v110\n"
"v_add_f32 v59,0xf2177617,v126\n"
"v_min_f32 v59,v59,v111\n"
"v_add_f32 v60,0xf349f2ca,v126\n"
"v_min_f32 v60,v60,v108\n"
"v_add_f32 v61,0xf35691f6,v126\n"
"v_min_f32 v61,v61,v109\n"
"v_add_f32 v62,0xf3633123,v126\n"
"v_min_f32 v62,v62,v110\n"
"v_add_f32 v63,0xf36fd050,v126\n"
"v_min_f32 v63,v63,v111\n"
"v_exp_f32 v16,v16\n"
"v_mfma_f32_16x16x32_bf16 v[56:59],a[64:67],a[48:51],v[56:59]\n"
"s_add_u32 s101,s101,4\n"
"ds_read_b128 a[96:99],v114 offset:8192\n"
"ds_read_b128 a[100:103],v114 offset:9216\n"
"ds_read_b128 a[104:107],v114 offset:10240\n"
"v_exp_f32 v17,v17\n"
"v_mfma_f32_16x16x32_bf16 v[56:59],a[68:71],a[52:55],v[56:59]\n"
"s_add_u32 m0,s55,12288\n"
"ds_read_b128 a[108:111],v114 offset:11264\n"
"ds_read_b128 a[112:115],v114 offset:12288\n"
"ds_read_b128 a[116:119],v114 offset:13312\n"
"v_mfma_f32_16x16x32_bf16 v[56:59],a[72:75],a[56:59],v[56:59]\n"
"ds_read_b128 a[120:123],v114 offset:14336\n"
"ds_read_b128 a[124:127],v114 offset:15360\n"
"ds_read_b128 a[32:35],v116 offset:24576\n"
"ds_read_b128 a[36:39],v116 offset:25600\n"
"ds_read_b128 a[40:43],v116 offset:26624\n"
"v_mfma_f32_16x16x32_bf16 v[56:59],a[76:79],a[60:63],v[56:59]\n"
"s_add_u32 s54,s54,s22\n"
"ds_read_b128 a[44:47],v116 offset:27648\n"
"v_exp_f32 v18,v18\n"
"v_mfma_f32_16x16x32_bf16 v[60:63],a[92:95],a[60:63],v[60:63]\n"
"buffer_load_dwordx4 v115,s[32:35],s54 offen sc1 lds\n"
"v_cvt_pk_bf16_f32 v72,v16,v17\n"
"v_mfma_f32_16x16x32_bf16 v[60:63],a[88:91],a[56:59],v[60:63]\n"
"s_add_u32 m0,s67,32768\n"
"v_exp_f32 v19,v19\n"
"v_mfma_f32_16x16x32_bf16 v[60:63],a[84:87],a[52:55],v[60:63]\n"
"buffer_load_dwordx4 v112,s[24:27],s64 offen offset:0 sc1 lds\n"
"v_cvt_pk_bf16_f32 v73,v18,v19\n"
"v_exp_f32 v20,v20\n"
"v_mfma_f32_16x16x32_bf16 v[60:63],a[80:83],a[48:51],v[60:63]\n"
"ds_read_b128 a[64:67],v113 offset:0\n"
"ds_read_b128 a[68:71],v113 offset:1024\n"
"ds_read_b128 a[72:75],v113 offset:2048\n"
"s_waitcnt lgkmcnt(7)\n"
"v_mfma_f32_16x16x32_bf16 v[160:163],a[96:99],v[84:87],v[160:163]\n"
"ds_read_b128 a[76:79],v113 offset:3072\n"
"ds_read_b128 a[80:83],v113 offset:4096\n"
"ds_read_b128 a[84:87],v113 offset:5120\n"
"ds_read_b128 a[88:91],v113 offset:6144\n"
"ds_read_b128 a[92:95],v113 offset:7168\n"
"v_exp_f32 v21,v21\n"
"v_mfma_f32_16x16x32_bf16 a[132:135],a[144:147],v[84:87],a[132:135]\n"
"buffer_load_dwordx4 v112,s[24:27],s64 offen offset:1024 sc1 lds\n"
"v_max3_f32 v118,v56,v57,v58\n"
"v_mfma_f32_16x16x32_bf16 v[164:167],a[100:103],v[84:87],v[164:167]\n"
"v_exp_f32 v22,v22\n"
"v_cvt_pk_bf16_f32 v74,v20,v21\n"
"ds_read_b128 a[48:51],v116 offset:57344\n"
"ds_read_b128 a[52:55],v116 offset:58368\n"
"v_mfma_f32_16x16x32_bf16 v[168:171],a[104:107],v[84:87],v[168:171]\n"
"v_max3_f32 v118,v118,v59,v60\n"
"v_max3_f32 v118,v118,v61,v62\n"
"v_max_f32 v118,v118,v63\n"
"v_exp_f32 v23,v23\n"
"v_mfma_f32_16x16x32_bf16 v[172:175],a[108:111],v[84:87],v[172:175]\n"
"ds_read_b128 a[56:59],v116 offset:59392\n"
"buffer_load_dwordx4 v112,s[24:27],s64 offen offset:2048 sc1 lds\n"
"ds_read_b128 a[60:63],v116 offset:60416\n"
"v_mfma_f32_16x16x32_bf16 v[176:179],a[112:115],v[84:87],v[176:179]\n"
"s_waitcnt vmcnt(15)\n"
"v_cvt_pk_bf16_f32 v75,v22,v23\n"
"v_accvgpr_read_b32 v122,a128\n"
"v_rcp_f32 v122,v122\n"
"v_mfma_f32_16x16x32_bf16 v[180:183],a[116:119],v[84:87],v[180:183]\n"
"ds_add_u32 v124,a152\n"
"v_cmp_lt_f32 vcc,s93,v118\n"
"v_exp_f32 v24,v24\n"
"v_pk_mul_f32 v[128:129],v[128:129],v[122:123] op_sel_hi:[1,0]\n"
"v_pk_mul_f32 v[130:131],v[130:131],v[122:123] op_sel_hi:[1,0]\n"
"v_pk_mul_f32 v[132:133],v[132:133],v[122:123] op_sel_hi:[1,0]\n"
"v_pk_mul_f32 v[134:135],v[134:135],v[122:123] op_sel_hi:[1,0]\n"
"v_mfma_f32_16x16x32_bf16 v[184:187],a[120:123],v[84:87],v[184:187]\n"
"buffer_load_dwordx4 v112,s[24:27],s64 offen offset:3072 sc1 lds\n"
"v_cvt_pk_bf16_f32 v128,v128,v129\n"
"v_cvt_pk_bf16_f32 v129,v130,v131\n"
"v_mfma_f32_16x16x32_bf16 v[188:191],a[124:127],v[84:87],v[188:191]\n"
"s_add_u32 m0,s67,36864\n"
"v_cvt_pk_bf16_f32 v130,v132,v133\n"
"v_cvt_pk_bf16_f32 v131,v134,v135\n"
"v_exp_f32 v25,v25\n"
"v_mfma_f32_16x16x32_bf16 v[192:195],a[96:99],v[88:91],v[192:195]\n"
"buffer_store_dwordx4 v[128:131],v112,s[24:27],s70 offen offset:0 sc1\n"
"v_cvt_pk_bf16_f32 v76,v24,v25\n"
"v_pk_mul_f32 v[136:137],v[136:137],v[122:123] op_sel_hi:[1,0]\n"
"v_pk_mul_f32 v[138:139],v[138:139],v[122:123] op_sel_hi:[1,0]\n"
"v_pk_mul_f32 v[140:141],v[140:141],v[122:123] op_sel_hi:[1,0]\n"
"v_pk_mul_f32 v[142:143],v[142:143],v[122:123] op_sel_hi:[1,0]\n"
"v_exp_f32 v26,v26\n"
"v_mfma_f32_16x16x32_bf16 a[136:139],a[144:147],v[88:91],a[136:139]\n"
"buffer_load_dwordx4 v117,s[24:27],s64 offen offset:0 sc1 lds\n"
"v_cvt_pk_bf16_f32 v136,v136,v137\n"
"v_mfma_f32_16x16x32_bf16 v[196:199],a[100:103],v[88:91],v[196:199]\n"
"v_exp_f32 v27,v27\n"
"v_cvt_pk_bf16_f32 v137,v138,v139\n"
"v_cvt_pk_bf16_f32 v138,v140,v141\n"
"v_cvt_pk_bf16_f32 v139,v142,v143\n"
"v_cvt_pk_bf16_f32 v77,v26,v27\n"
"v_mfma_f32_16x16x32_bf16 v[200:203],a[104:107],v[88:91],v[200:203]\n"
"buffer_store_dwordx4 v[136:139],v112,s[24:27],s70 offen offset:1024 sc1\n"
"v_pk_mul_f32 v[144:145],v[144:145],v[122:123] op_sel_hi:[1,0]\n"
"v_pk_mul_f32 v[146:147],v[146:147],v[122:123] op_sel_hi:[1,0]\n"
"v_pk_mul_f32 v[148:149],v[148:149],v[122:123] op_sel_hi:[1,0]\n"
"v_pk_mul_f32 v[150:151],v[150:151],v[122:123] op_sel_hi:[1,0]\n"
"v_mfma_f32_16x16x32_bf16 v[204:207],a[108:111],v[88:91],v[204:207]\n"
"buffer_load_dwordx4 v117,s[24:27],s64 offen offset:1024 sc1 lds\n"
"v_exp_f32 v28,v28\n"
"v_mfma_f32_16x16x32_bf16 v[208:211],a[112:115],v[88:91],v[208:211]\n"
"v_cvt_pk_bf16_f32 v144,v144,v145\n"
"v_cvt_pk_bf16_f32 v145,v146,v147\n"
"v_cvt_pk_bf16_f32 v146,v148,v149\n"
"v_cvt_pk_bf16_f32 v147,v150,v151\n"
"v_mfma_f32_16x16x32_bf16 v[212:215],a[116:119],v[88:91],v[212:215]\n"
"buffer_store_dwordx4 v[144:147],v112,s[24:27],s70 offen offset:2048 sc1\n"
"v_pk_mul_f32 v[152:153],v[152:153],v[122:123] op_sel_hi:[1,0]\n"
"v_pk_mul_f32 v[154:155],v[154:155],v[122:123] op_sel_hi:[1,0]\n"
"v_pk_mul_f32 v[156:157],v[156:157],v[122:123] op_sel_hi:[1,0]\n"
"v_mfma_f32_16x16x32_bf16 v[216:219],a[120:123],v[88:91],v[216:219]\n"
"buffer_load_dwordx4 v117,s[24:27],s64 offen offset:2048 sc1 lds\n"
"v_exp_f32 v29,v29\n"
"v_cvt_pk_bf16_f32 v152,v152,v153\n"
"v_cvt_pk_bf16_f32 v78,v28,v29\n"
"v_pk_mul_f32 v[158:159],v[158:159],v[122:123] op_sel_hi:[1,0]\n"
"v_mfma_f32_16x16x32_bf16 v[220:223],a[124:127],v[88:91],v[220:223]\n"
"v_cvt_pk_bf16_f32 v153,v154,v155\n"
"v_cvt_pk_bf16_f32 v154,v156,v157\n"
"v_cvt_pk_bf16_f32 v155,v158,v159\n"
"v_mfma_f32_16x16x32_bf16 v[224:227],a[96:99],v[92:95],v[224:227]\n"
"ds_read_b32 v125,v124\n"
"buffer_store_dwordx4 v[152:155],v112,s[24:27],s70 offen offset:3072 sc1\n"
"buffer_load_dwordx4 v117,s[24:27],s64 offen offset:3072 sc1 lds\n"
"v_accvgpr_read_b32 v122,a132\n"
"v_mfma_f32_16x16x32_bf16 a[140:143],a[144:147],v[92:95],a[140:143]\n"
"s_add_u32 s70,s70,262144\n"
"v_exp_f32 v31,v31\n"
"v_mfma_f32_16x16x32_bf16 v[228:231],a[100:103],v[92:95],v[228:231]\n"
"s_add_u32 s64,s64,16384\n"
"v_exp_f32 v30,v30\n"
"v_rcp_f32 v122,v122\n"
"v_cvt_pk_bf16_f32 v79,v30,v31\n"
"v_pk_mul_f32 v[160:161],v[160:161],v[122:123] op_sel_hi:[1,0]\n"
"v_pk_mul_f32 v[162:163],v[162:163],v[122:123] op_sel_hi:[1,0]\n"
"v_pk_mul_f32 v[164:165],v[164:165],v[122:123] op_sel_hi:[1,0]\n"
"v_pk_mul_f32 v[166:167],v[166:167],v[122:123] op_sel_hi:[1,0]\n"
"v_mfma_f32_16x16x32_bf16 v[232:235],a[104:107],v[92:95],v[232:235]\n"
"buffer_store_dwordx4 a[252:255],v112,s[24:27],s56 offen offset:3072 sc1\n"
"v_cvt_pk_bf16_f32 v160,v160,v161\n"
"v_cvt_pk_bf16_f32 v161,v162,v163\n"
"v_cvt_pk_bf16_f32 v162,v164,v165\n"
"v_cvt_pk_bf16_f32 v163,v166,v167\n"
"v_mfma_f32_16x16x32_bf16 v[236:239],a[108:111],v[92:95],v[236:239]\n"
"buffer_store_dwordx4 v[160:163],v112,s[24:27],s70 offen offset:0 sc1\n"
"v_pk_mul_f32 v[168:169],v[168:169],v[122:123] op_sel_hi:[1,0]\n"
"v_pk_mul_f32 v[170:171],v[170:171],v[122:123] op_sel_hi:[1,0]\n"
"v_pk_mul_f32 v[172:173],v[172:173],v[122:123] op_sel_hi:[1,0]\n"
"v_pk_mul_f32 v[174:175],v[174:175],v[122:123] op_sel_hi:[1,0]\n"
"v_mfma_f32_16x16x32_bf16 v[240:243],a[112:115],v[92:95],v[240:243]\n"
"s_waitcnt lgkmcnt(0)\n"
"v_cvt_pk_bf16_f32 v168,v168,v169\n"
"v_cvt_pk_bf16_f32 v169,v170,v171\n"
"v_cvt_pk_bf16_f32 v170,v172,v173\n"
"v_cvt_pk_bf16_f32 v171,v174,v175\n"
"v_mfma_f32_16x16x32_bf16 v[244:247],a[116:119],v[92:95],v[244:247]\n"
"buffer_store_dwordx4 v[168:171],v112,s[24:27],s70 offen offset:1024 sc1\n"
"v_pk_mul_f32 v[176:177],v[176:177],v[122:123] op_sel_hi:[1,0]\n"
"v_pk_mul_f32 v[178:179],v[178:179],v[122:123] op_sel_hi:[1,0]\n"
"v_pk_mul_f32 v[180:181],v[180:181],v[122:123] op_sel_hi:[1,0]\n"
"v_pk_mul_f32 v[182:183],v[182:183],v[122:123] op_sel_hi:[1,0]\n"
"v_mfma_f32_16x16x32_bf16 v[248:251],a[120:123],v[92:95],v[248:251]\n"
"v_cvt_pk_bf16_f32 v176,v176,v177\n"
"v_cvt_pk_bf16_f32 v177,v178,v179\n"
"v_cvt_pk_bf16_f32 v178,v180,v181\n"
"v_cvt_pk_bf16_f32 v179,v182,v183\n"
"v_mfma_f32_16x16x32_bf16 v[252:255],a[124:127],v[92:95],v[252:255]\n"
"buffer_store_dwordx4 v[176:179],v112,s[24:27],s70 offen offset:2048 sc1\n"
"v_pk_mul_f32 v[184:185],v[184:185],v[122:123] op_sel_hi:[1,0]\n"
"v_pk_mul_f32 v[186:187],v[186:187],v[122:123] op_sel_hi:[1,0]\n"
"v_pk_mul_f32 v[188:189],v[188:189],v[122:123] op_sel_hi:[1,0]\n"
"v_pk_mul_f32 v[190:191],v[190:191],v[122:123] op_sel_hi:[1,0]\n"
"v_cvt_pk_bf16_f32 v184,v184,v185\n"
"v_cvt_pk_bf16_f32 v185,v186,v187\n"
"v_cvt_pk_bf16_f32 v186,v188,v189\n"
"v_cvt_pk_bf16_f32 v187,v190,v191\n"
"buffer_store_dwordx4 v[184:187],v112,s[24:27],s70 offen offset:3072 sc1\n"
"s_add_u32 s70,s70,262144\n"
"s_cbranch_vccnz .Lfk_cs_u2\n"
".Lfk_cr_u2:\n"
"v_cmp_gt_u32 vcc,s101,v125\n"
"s_nop 0\n"
"s_cbranch_vccnz .Lfk_cs_mu2\n"
".Lfk_cr_mu2:\n"
"s_add_u32 s89,s89,1\n"
"s_cmp_eq_u32 s89,8\n"
"s_cbranch_scc1 .Lfk_tailB7\n"
".Lfk_winB:\n"
"v_mfma_f32_16x16x32_bf16 v[0:3],a[64:67],a[0:3],v[96:99]\n"
"s_swappc_b64 s[94:95],s[20:21]\n"
"ds_read_b128 a[96:99],v114 offset:24576\n"
"v_mfma_f32_16x16x32_bf16 v[0:3],a[68:71],a[4:7],v[0:3]\n"
"s_add_u32 s101,s101,4\n"
"ds_read_b128 a[100:103],v114 offset:25600\n"
"ds_read_b128 a[104:107],v114 offset:26624\n"
"ds_read_b128 a[108:111],v114 offset:27648\n"
"v_mfma_f32_16x16x32_bf16 v[0:3],a[72:75],a[8:11],v[0:3]\n"
"ds_read_b128 a[112:115],v114 offset:28672\n"
"ds_read_b128 a[116:119],v114 offset:29696\n"
"v_mfma_f32_16x16x32_bf16 v[0:3],a[76:79],a[12:15],v[0:3]\n"
"ds_read_b128 a[120:123],v114 offset:30720\n"
"ds_read_b128 a[124:127],v114 offset:31744\n"
"v_mfma_f32_16x16x32_bf16 v[4:7],a[92:95],a[12:15],v[96:99]\n"
"v_exp_f32 v56,v56\n"
"v_mfma_f32_16x16x32_bf16 v[4:7],a[88:91],a[8:11],v[4:7]\n"
"v_exp_f32 v57,v57\n"
"v_mfma_f32_16x16x32_bf16 v[4:7],a[84:87],a[4:7],v[4:7]\n"
"v_cvt_pk_bf16_f32 v92,v56,v57\n"
"v_mfma_f32_16x16x32_bf16 v[4:7],a[80:83],a[0:3],v[4:7]\n"
"s_add_u32 m0,s67,0\n"
"s_waitcnt lgkmcnt(0)\n"
"v_mfma_f32_16x16x32_bf16 v[192:195],a[96:99],v[72:75],v[192:195]\n"
"buffer_load_dwordx4 v112,s[24:27],s64 offen offset:0 sc1 lds\n"
"v_mfma_f32_16x16x32_bf16 a[136:139],a[144:147],v[72:75],a[136:139]\n"
"buffer_load_dwordx4 v112,s[24:27],s64 offen offset:1024 sc1 lds\n"
"v_mfma_f32_16x16x32_bf16 v[196:199],a[100:103],v[72:75],v[196:199]\n"
"v_exp_f32 v58,v58\n"
"v_mfma_f32_16x16x32_bf16 v[200:203],a[104:107],v[72:75],v[200:203]\n"
"v_max3_f32 v118,v0,v1,v2\n"
"v_mfma_f32_16x16x32_bf16 v[204:207],a[108:111],v[72:75],v[204:207]\n"
"v_max3_f32 v118,v118,v3,v4\n"
"v_max3_f32 v118,v118,v5,v6\n"
"v_mfma_f32_16x16x32_bf16 v[208:211],a[112:115],v[72:75],v[208:211]\n"
"v_max_f32 v118,v118,v7\n"
"v_mov_b32 v120,v118\n"
"v_mfma_f32_16x16x32_bf16 v[212:215],a[116:119],v[72:75],v[212:215]\n"
"v_mfma_f32_16x16x32_bf16 v[216:219],a[120:123],v[72:75],v[216:219]\n"
"v_mfma_f32_16x16x32_bf16 v[220:223],a[124:127],v[72:75],v[220:223]\n"
"v_exp_f32 v59,v59\n"
"v_mfma_f32_16x16x32_bf16 v[224:227],a[96:99],v[76:79],v[224:227]\n"
"v_cvt_pk_bf16_f32 v93,v58,v59\n"
"v_accvgpr_read_b32 v122,a136\n"
"v_mfma_f32_16x16x32_bf16 a[140:143],a[144:147],v[76:79],a[140:143]\n"
"buffer_load_dwordx4 v112,s[24:27],s64 offen offset:2048 sc1 lds\n"
"v_mfma_f32_16x16x32_bf16 v[228:231],a[100:103],v[76:79],v[228:231]\n"
"v_rcp_f32 v122,v122\n"
"v_mfma_f32_16x16x32_bf16 v[232:235],a[104:107],v[76:79],v[232:235]\n"
"buffer_load_dwordx4 v112,s[24:27],s64 offen offset:3072 sc1 lds\n"
"v_pk_mul_f32 v[192:193],v[192:193],v[122:123] op_sel_hi:[1,0]\n"
"v_pk_mul_f32 v[194:195],v[194:195],v[122:123] op_sel_hi:[1,0]\n"
"v_mfma_f32_16x16x32_bf16 v[236:239],a[108:111],v[76:79],v[236:239]\n"
"v_cvt_pk_bf16_f32 v192,v192,v193\n"
"v_exp_f32 v60,v60\n"
"v_cvt_pk_bf16_f32 v193,v194,v195\n"
"v_pk_mul_f32 v[196:197],v[196:197],v[122:123] op_sel_hi:[1,0]\n"
"v_pk_mul_f32 v[198:199],v[198:199],v[122:123] op_sel_hi:[1,0]\n"
"v_mfma_f32_16x16x32_bf16 v[240:243],a[112:115],v[76:79],v[240:243]\n"
"s_add_u32 m0,s67,4096\n"
"v_cvt_pk_bf16_f32 v194,v196,v197\n"
"v_cvt_pk_bf16_f32 v195,v198,v199\n"
"v_mfma_f32_16x16x32_bf16 v[244:247],a[116:119],v[76:79],v[244:247]\n"
"v_mfma_f32_16x16x32_bf16 v[248:251],a[120:123],v[76:79],v[248:251]\n"
"buffer_store_dwordx4 v[192:195],v112,s[24:27],s70 offen offset:0 sc1\n"
"v_pk_mul_f32 v[200:201],v[200:201],v[122:123] op_sel_hi:[1,0]\n"
"v_mfma_f32_16x16x32_bf16 v[252:255],a[124:127],v[76:79],v[252:255]\n"
"buffer_load_dwordx4 v117,s[24:27],s64 offen offset:0 sc1 lds\n"
"v_cvt_pk_bf16_f32 v200,v200,v201\n"
"v_exp_f32 v61,v61\n"
"v_pk_mul_f32 v[202:203],v[202:203],v[122:123] op_sel_hi:[1,0]\n"
"v_pk_mul_f32 v[204:205],v[204:205],v[122:123] op_sel_hi:[1,0]\n"
"v_pk_mul_f32 v[206:207],v[206:207],v[122:123] op_sel_hi:[1,0]\n"
"v_mfma_f32_16x16x32_bf16 v[12:15],a[80:83],a[16:19],v[100:103]\n"
"v_cvt_pk_bf16_f32 v201,v202,v203\n"
"v_cvt_pk_bf16_f32 v202,v204,v205\n"
"v_mfma_f32_16x16x32_bf16 v[12:15],a[84:87],a[20:23],v[12:15]\n"
"v_cvt_pk_bf16_f32 v203,v206,v207\n"
"v_cvt_pk_bf16_f32 v94,v60,v61\n"
"v_mfma_f32_16x16x32_bf16 v[12:15],a[88:91],a[24:27],v[12:15]\n"
"buffer_load_dwordx4 v117,s[24:27],s64 offen offset:1024 sc1 lds\n"
"v_mfma_f32_16x16x32_bf16 v[12:15],a[92:95],a[28:31],v[12:15]\n"
"buffer_store_dwordx4 v[200:203],v112,s[24:27],s70 offen offset:1024 sc1\n"
"v_pk_mul_f32 v[208:209],v[208:209],v[122:123] op_sel_hi:[1,0]\n"
"v_pk_mul_f32 v[210:211],v[210:211],v[122:123] op_sel_hi:[1,0]\n"
"v_mfma_f32_16x16x32_bf16 v[8:11],a[76:79],a[28:31],v[100:103]\n"
"buffer_load_dwordx4 v117,s[24:27],s64 offen offset:2048 sc1 lds\n"
"v_cvt_pk_bf16_f32 v208,v208,v209\n"
"v_cvt_pk_bf16_f32 v209,v210,v211\n"
"v_pk_mul_f32 v[212:213],v[212:213],v[122:123] op_sel_hi:[1,0]\n"
"v_pk_mul_f32 v[214:215],v[214:215],v[122:123] op_sel_hi:[1,0]\n"
"v_mfma_f32_16x16x32_bf16 v[8:11],a[72:75],a[24:27],v[8:11]\n"
"v_cvt_pk_bf16_f32 v210,v212,v213\n"
"v_cvt_pk_bf16_f32 v211,v214,v215\n"
"v_mfma_f32_16x16x32_bf16 v[8:11],a[68:71],a[20:23],v[8:11]\n"
"v_exp_f32 v62,v62\n"
"v_mfma_f32_16x16x32_bf16 v[8:11],a[64:67],a[16:19],v[8:11]\n"
"buffer_store_dwordx4 v[208:211],v112,s[24:27],s70 offen offset:2048 sc1\n"
"v_pk_mul_f32 v[216:217],v[216:217],v[122:123] op_sel_hi:[1,0]\n"
"v_mfma_f32_16x16x32_bf16 v[16:19],a[64:67],a[32:35],v[104:107]\n"
"v_cvt_pk_bf16_f32 v216,v216,v217\n"
"buffer_load_dwordx4 v117,s[24:27],s64 offen offset:3072 sc1 lds\n"
"v_pk_mul_f32 v[218:219],v[218:219],v[122:123] op_sel_hi:[1,0]\n"
"v_pk_mul_f32 v[220:221],v[220:221],v[122:123] op_sel_hi:[1,0]\n"
"v_pk_mul_f32 v[222:223],v[222:223],v[122:123] op_sel_hi:[1,0]\n"
"v_mfma_f32_16x16x32_bf16 v[16:19],a[68:71],a[36:39],v[16:19]\n"
"v_cvt_pk_bf16_f32 v217,v218,v219\n"
"v_cvt_pk_bf16_f32 v218,v220,v221\n"
"v_mfma_f32_16x16x32_bf16 v[16:19],a[72:75],a[40:43],v[16:19]\n"
"s_add_u32 s64,s64,16384\n"
"v_exp_f32 v63,v63\n"
"v_mfma_f32_16x16x32_bf16 v[16:19],a[76:79],a[44:47],v[16:19]\n"
"s_andn2_b32 m0,s55,16384\n"
"s_add_u32 s54,s54,s22\n"
"v_max3_f32 v119,v8,v9,v10\n"
"v_mfma_f32_16x16x32_bf16 v[20:23],a[92:95],a[44:47],v[104:107]\n"
"v_max3_f32 v119,v119,v11,v12\n"
"v_cvt_pk_bf16_f32 v95,v62,v63\n"
"v_mfma_f32_16x16x32_bf16 v[20:23],a[88:91],a[40:43],v[20:23]\n"
"buffer_load_dwordx4 v115,s[32:35],s54 offen sc1 lds\n"
"v_mfma_f32_16x16x32_bf16 v[20:23],a[84:87],a[36:39],v[20:23]\n"
"v_max3_f32 v119,v119,v13,v14\n"
"v_max_f32 v119,v119,v15\n"
"v_mfma_f32_16x16x32_bf16 v[20:23],a[80:83],a[32:35],v[20:23]\n"
"v_cvt_pk_bf16_f32 v219,v222,v223\n"
"v_mfma_f32_16x16x32_bf16 v[28:31],a[80:83],a[48:51],v[108:111]\n"
"buffer_store_dwordx4 v[216:219],v112,s[24:27],s70 offen offset:3072 sc1\n"
"buffer_store_dwordx4 a[240:243],v112,s[24:27],s56 offen offset:0 sc1\n"
"v_mfma_f32_16x16x32_bf16 v[28:31],a[84:87],a[52:55],v[28:31]\n"
"s_add_u32 s70,s70,262144\n"
"v_max_f32 v118,v118,v119\n"
"v_min_f32 v120,v120,v119\n"
"v_mfma_f32_16x16x32_bf16 v[28:31],a[88:91],a[56:59],v[28:31]\n"
"v_max3_f32 v119,v16,v17,v18\n"
"v_mfma_f32_16x16x32_bf16 v[28:31],a[92:95],a[60:63],v[28:31]\n"
"v_max3_f32 v119,v119,v19,v20\n"
"v_max3_f32 v119,v119,v21,v22\n"
"v_mfma_f32_16x16x32_bf16 v[24:27],a[76:79],a[60:63],v[108:111]\n"
"v_max_f32 v119,v119,v23\n"
"v_max_f32 v118,v118,v119\n"
"v_mfma_f32_16x16x32_bf16 v[24:27],a[72:75],a[56:59],v[24:27]\n"
"v_min_f32 v120,v120,v119\n"
"v_mfma_f32_16x16x32_bf16 v[24:27],a[68:71],a[52:55],v[24:27]\n"
"v_mfma_f32_16x16x32_bf16 v[24:27],a[64:67],a[48:51],v[24:27]\n"
"ds_read_b128 a[64:67],v113 offset:16384\n"
"ds_read_b128 a[68:71],v113 offset:17408\n"
"ds_read_b128 a[72:75],v113 offset:18432\n"
"ds_read_b128 a[76:79],v113 offset:19456\n"
"ds_read_b128 a[80:83],v113 offset:20480\n"
"ds_read_b128 a[84:87],v113 offset:21504\n"
"ds_read_b128 a[88:91],v113 offset:22528\n"
"ds_read_b128 a[92:95],v113 offset:23552\n"
"v_max3_f32 v119,v24,v25,v26\n"
"v_max3_f32 v119,v119,v27,v28\n"
"v_max3_f32 v119,v119,v29,v30\n"
"v_max_f32 v119,v119,v31\n"
"v_min_f32 v120,v120,v119\n"
"v_max_f32 v118,v118,v119\n"
"v_cmp_gt_f32 s[96:97],s16,v120\n"
"v_cmp_lt_f32 vcc,s93,v118\n"
"s_waitcnt vmcnt(25)\n"
"ds_add_u32 v124,a152\n"
"ds_read_b32 v125,v124\n"
"s_waitcnt lgkmcnt(0)\n"
"s_or_b64 s[96:97],s[96:97],vcc\n"
"s_cbranch_scc1 .Lfk_cs_v3\n"
".Lfk_cr_v3:\n"
"v_cmp_gt_u32 vcc,s101,v125\n"
"s_nop 0\n"
"s_cbranch_vccnz .Lfk_cs_mv3\n"
".Lfk_cr_mv3:\n"
"v_exp_f32 v0,v0\n"
"v_mfma_f32_16x16x32_bf16 v[32:35],a[64:67],a[0:3],v[96:99]\n"
"s_add_u32 s101,s101,4\n"
"ds_read_b128 a[96:99],v114 offset:57344\n"
"ds_read_b128 a[100:103],v114 offset:58368\n"
"v_exp_f32 v1,v1\n"
"v_mfma_f32_16x16x32_bf16 v[32:35],a[68:71],a[4:7],v[32:35]\n"
"s_add_u32 m0,s55,4096\n"
"ds_read_b128 a[104:107],v114 offset:59392\n"
"ds_read_b128 a[108:111],v114 offset:60416\n"
"v_exp_f32 v2,v2\n"
"v_mfma_f32_16x16x32_bf16 v[32:35],a[72:75],a[8:11],v[32:35]\n"
"s_add_u32 s54,s54,s22\n"
"ds_read_b128 a[112:115],v114 offset:61440\n"
"ds_read_b128 a[116:119],v114 offset:62464\n"
"v_exp_f32 v3,v3\n"
"v_mfma_f32_16x16x32_bf16 v[32:35],a[76:79],a[12:15],v[32:35]\n"
"ds_read_b128 a[120:123],v114 offset:63488\n"
"ds_read_b128 a[124:127],v114 offset:64512\n"
"v_exp_f32 v4,v4\n"
"v_mfma_f32_16x16x32_bf16 v[36:39],a[92:95],a[12:15],v[96:99]\n"
"buffer_load_dwordx4 v115,s[32:35],s54 offen sc1 lds\n"
"v_mfma_f32_16x16x32_bf16 v[36:39],a[88:91],a[8:11],v[36:39]\n"
"s_add_u32 m0,s67,16384\n"
"v_exp_f32 v5,v5\n"
"v_cvt_pk_bf16_f32 v64,v0,v1\n"
"v_cvt_pk_bf16_f32 v65,v2,v3\n"
"v_mfma_f32_16x16x32_bf16 v[36:39],a[84:87],a[4:7],v[36:39]\n"
"v_exp_f32 v6,v6\n"
"v_exp_f32 v7,v7\n"
"v_mfma_f32_16x16x32_bf16 v[36:39],a[80:83],a[0:3],v[36:39]\n"
"buffer_load_dwordx4 v112,s[24:27],s64 offen offset:0 sc1 lds\n"
"s_waitcnt lgkmcnt(0)\n"
"v_mfma_f32_16x16x32_bf16 v[224:227],a[96:99],v[92:95],v[224:227]\n"
"v_exp_f32 v8,v8\n"
"v_cvt_pk_bf16_f32 v67,v6,v7\n"
"v_mfma_f32_16x16x32_bf16 a[140:143],a[144:147],v[92:95],a[140:143]\n"
"v_exp_f32 v9,v9\n"
"v_exp_f32 v10,v10\n"
"v_mfma_f32_16x16x32_bf16 v[228:231],a[100:103],v[92:95],v[228:231]\n"
"buffer_load_dwordx4 v112,s[24:27],s64 offen offset:1024 sc1 lds\n"
"v_mfma_f32_16x16x32_bf16 v[232:235],a[104:107],v[92:95],v[232:235]\n"
"v_cvt_pk_bf16_f32 v66,v4,v5\n"
"v_cvt_pk_bf16_f32 v68,v8,v9\n"
"v_mfma_f32_16x16x32_bf16 v[236:239],a[108:111],v[92:95],v[236:239]\n"
"v_exp_f32 v11,v11\n"
"v_exp_f32 v12,v12\n"
"v_mfma_f32_16x16x32_bf16 v[240:243],a[112:115],v[92:95],v[240:243]\n"
"buffer_load_dwordx4 v112,s[24:27],s64 offen offset:2048 sc1 lds\n"
"v_mfma_f32_16x16x32_bf16 v[244:247],a[116:119],v[92:95],v[244:247]\n"
"v_exp_f32 v13,v13\n"
"v_cvt_pk_bf16_f32 v69,v10,v11\n"
"v_mfma_f32_16x16x32_bf16 v[248:251],a[120:123],v[92:95],v[248:251]\n"
"v_exp_f32 v14,v14\n"
"v_cvt_pk_bf16_f32 v70,v12,v13\n"
"v_exp_f32 v15,v15\n"
"v_mfma_f32_16x16x32_bf16 v[252:255],a[124:127],v[92:95],v[252:255]\n"
"buffer_load_dwordx4 v112,s[24:27],s64 offen offset:3072 sc1 lds\n"
"v_mfma_f32_16x16x32_bf16 v[44:47],a[80:83],a[16:19],v[100:103]\n"
"s_add_u32 m0,s67,20480\n"
"v_exp_f32 v16,v16\n"
"v_cvt_pk_bf16_f32 v71,v14,v15\n"
"v_mfma_f32_16x16x32_bf16 v[44:47],a[84:87],a[20:23],v[44:47]\n"
"v_exp_f32 v17,v17\n"
"v_exp_f32 v18,v18\n"
"v_mfma_f32_16x16x32_bf16 v[44:47],a[88:91],a[24:27],v[44:47]\n"
"buffer_load_dwordx4 v117,s[24:27],s64 offen offset:0 sc1 lds\n"
"v_mfma_f32_16x16x32_bf16 v[44:47],a[92:95],a[28:31],v[44:47]\n"
"v_exp_f32 v19,v19\n"
"v_accvgpr_read_b32 v122,a140\n"
"v_mfma_f32_16x16x32_bf16 v[40:43],a[76:79],a[28:31],v[100:103]\n"
"v_cvt_pk_bf16_f32 v73,v18,v19\n"
"v_rcp_f32 v122,v122\n"
"v_mfma_f32_16x16x32_bf16 v[40:43],a[72:75],a[24:27],v[40:43]\n"
"buffer_load_dwordx4 v117,s[24:27],s64 offen offset:1024 sc1 lds\n"
"v_mfma_f32_16x16x32_bf16 v[40:43],a[68:71],a[20:23],v[40:43]\n"
"v_exp_f32 v21,v21\n"
"v_exp_f32 v20,v20\n"
"v_cvt_pk_bf16_f32 v72,v16,v17\n"
"v_pk_mul_f32 v[224:225],v[224:225],v[122:123] op_sel_hi:[1,0]\n"
"v_pk_mul_f32 v[226:227],v[226:227],v[122:123] op_sel_hi:[1,0]\n"
"v_pk_mul_f32 v[228:229],v[228:229],v[122:123] op_sel_hi:[1,0]\n"
"v_pk_mul_f32 v[230:231],v[230:231],v[122:123] op_sel_hi:[1,0]\n"
"v_mfma_f32_16x16x32_bf16 v[40:43],a[64:67],a[16:19],v[40:43]\n"
"v_cvt_pk_bf16_f32 v74,v20,v21\n"
"v_exp_f32 v22,v22\n"
"v_cvt_pk_bf16_f32 v224,v224,v225\n"
"v_exp_f32 v23,v23\n"
"v_mfma_f32_16x16x32_bf16 v[48:51],a[64:67],a[32:35],v[104:107]\n"
"buffer_load_dwordx4 v117,s[24:27],s64 offen offset:2048 sc1 lds\n"
"v_mfma_f32_16x16x32_bf16 v[48:51],a[68:71],a[36:39],v[48:51]\n"
"v_exp_f32 v24,v24\n"
"v_cvt_pk_bf16_f32 v225,v226,v227\n"
"v_mfma_f32_16x16x32_bf16 v[48:51],a[72:75],a[40:43],v[48:51]\n"
"v_exp_f32 v25,v25\n"
"v_cvt_pk_bf16_f32 v226,v228,v229\n"
"v_cvt_pk_bf16_f32 v227,v230,v231\n"
"v_mfma_f32_16x16x32_bf16 v[48:51],a[76:79],a[44:47],v[48:51]\n"
"buffer_load_dwordx4 v117,s[24:27],s64 offen offset:3072 sc1 lds\n"
"v_mfma_f32_16x16x32_bf16 v[52:55],a[92:95],a[44:47],v[104:107]\n"
"buffer_store_dwordx4 v[224:227],v112,s[24:27],s70 offen offset:0 sc1\n"
"v_exp_f32 v27,v27\n"
"v_mfma_f32_16x16x32_bf16 v[52:55],a[88:91],a[40:43],v[52:55]\n"
"s_add_u32 s64,s64,16384\n"
"v_exp_f32 v26,v26\n"
"v_exp_f32 v28,v28\n"
"v_cvt_pk_bf16_f32 v77,v26,v27\n"
"v_pk_mul_f32 v[232:233],v[232:233],v[122:123] op_sel_hi:[1,0]\n"
"v_mfma_f32_16x16x32_bf16 v[52:55],a[84:87],a[36:39],v[52:55]\n"
"v_cvt_pk_bf16_f32 v75,v22,v23\n"
"v_cvt_pk_bf16_f32 v76,v24,v25\n"
"v_exp_f32 v29,v29\n"
"v_exp_f32 v31,v31\n"
"v_pk_mul_f32 v[234:235],v[234:235],v[122:123] op_sel_hi:[1,0]\n"
"v_pk_mul_f32 v[236:237],v[236:237],v[122:123] op_sel_hi:[1,0]\n"
"v_pk_mul_f32 v[238:239],v[238:239],v[122:123] op_sel_hi:[1,0]\n"
"v_mfma_f32_16x16x32_bf16 v[52:55],a[80:83],a[32:35],v[52:55]\n"
"v_exp_f32 v30,v30\n"
"v_cvt_pk_bf16_f32 v78,v28,v29\n"
"v_mfma_f32_16x16x32_bf16 v[60:63],a[80:83],a[48:51],v[108:111]\n"
"v_cvt_pk_bf16_f32 v232,v232,v233\n"
"v_cvt_pk_bf16_f32 v233,v234,v235\n"
"v_mfma_f32_16x16x32_bf16 v[60:63],a[84:87],a[52:55],v[60:63]\n"
"buffer_store_dwordx4 a[244:247],v112,s[24:27],s56 offen offset:1024 sc1\n"
"v_cvt_pk_bf16_f32 v234,v236,v237\n"
"v_cvt_pk_bf16_f32 v235,v238,v239\n"
"v_mfma_f32_16x16x32_bf16 v[60:63],a[88:91],a[56:59],v[60:63]\n"
"buffer_store_dwordx4 v[232:235],v112,s[24:27],s70 offen offset:1024 sc1\n"
"v_pk_mul_f32 v[240:241],v[240:241],v[122:123] op_sel_hi:[1,0]\n"
"v_pk_mul_f32 v[242:243],v[242:243],v[122:123] op_sel_hi:[1,0]\n"
"v_pk_mul_f32 v[244:245],v[244:245],v[122:123] op_sel_hi:[1,0]\n"
"v_pk_mul_f32 v[246:247],v[246:247],v[122:123] op_sel_hi:[1,0]\n"
"v_mfma_f32_16x16x32_bf16 v[60:63],a[92:95],a[60:63],v[60:63]\n"
"v_cvt_pk_bf16_f32 v240,v240,v241\n"
"v_cvt_pk_bf16_f32 v241,v242,v243\n"
"v_cvt_pk_bf16_f32 v242,v244,v245\n"
"v_cvt_pk_bf16_f32 v243,v246,v247\n"
"v_mfma_f32_16x16x32_bf16 v[56:59],a[76:79],a[60:63],v[108:111]\n"
"buffer_store_dwordx4 v[240:243],v112,s[24:27],s70 offen offset:2048 sc1\n"
"v_pk_mul_f32 v[248:249],v[248:249],v[122:123] op_sel_hi:[1,0]\n"
"v_pk_mul_f32 v[250:251],v[250:251],v[122:123] op_sel_hi:[1,0]\n"
"v_mfma_f32_16x16x32_bf16 v[56:59],a[72:75],a[56:59],v[56:59]\n"
"v_cvt_pk_bf16_f32 v79,v30,v31\n"
"v_cvt_pk_bf16_f32 v248,v248,v249\n"
"v_cvt_pk_bf16_f32 v249,v250,v251\n"
"v_pk_mul_f32 v[252:253],v[252:253],v[122:123] op_sel_hi:[1,0]\n"
"v_pk_mul_f32 v[254:255],v[254:255],v[122:123] op_sel_hi:[1,0]\n"
"v_mfma_f32_16x16x32_bf16 v[56:59],a[68:71],a[52:55],v[56:59]\n"
"v_cvt_pk_bf16_f32 v250,v252,v253\n"
"v_cvt_pk_bf16_f32 v251,v254,v255\n"
"v_mfma_f32_16x16x32_bf16 v[56:59],a[64:67],a[48:51],v[56:59]\n"
"buffer_store_dwordx4 v[248:251],v112,s[24:27],s70 offen offset:3072 sc1\n"
"s_add_u32 s70,s70,262144\n"
"ds_read_b128 a[64:67],v113 offset:49152\n"
"ds_read_b128 a[68:71],v113 offset:50176\n"
"ds_read_b128 a[72:75],v113 offset:51200\n"
"ds_read_b128 a[76:79],v113 offset:52224\n"
"ds_read_b128 a[80:83],v113 offset:53248\n"
"ds_read_b128 a[84:87],v113 offset:54272\n"
"ds_read_b128 a[88:91],v113 offset:55296\n"
"ds_read_b128 a[92:95],v113 offset:56320\n"
"v_mov_b32 v114,v112\n"
"s_mov_b32 s70,s68\n"
"s_waitcnt vmcnt(25)\n"
"ds_add_u32 v124,a152\n"
"ds_read_b32 v125,v124\n"
"s_waitcnt lgkmcnt(0)\n"
"v_cmp_gt_u32 vcc,s101,v125\n"
"s_nop 0\n"
"s_cbranch_vccnz .Lfk_cs_mv0\n"
".Lfk_cr_mv0:\n"
"v_mfma_f32_16x16x32_bf16 v[0:3],a[64:67],a[0:3],v[96:99]\n"
"s_add_u32 s101,s101,4\n"
"v_exp_f32 v122,v32\n"
"v_mfma_f32_16x16x32_bf16 v[0:3],a[68:71],a[4:7],v[0:3]\n"
"s_add_u32 m0,s55,8192\n"
"v_exp_f32 v123,v33\n"
"v_mfma_f32_16x16x32_bf16 v[0:3],a[72:75],a[8:11],v[0:3]\n"
"ds_read_b128 a[96:99],v114 offset:40960\n"
"ds_read_b128 a[100:103],v114 offset:41984\n"
"v_mfma_f32_16x16x32_bf16 v[0:3],a[76:79],a[12:15],v[0:3]\n"
"s_add_u32 s54,s54,s22\n"
"v_exp_f32 v119,v34\n"
"v_mfma_f32_16x16x32_bf16 v[4:7],a[92:95],a[12:15],v[96:99]\n"
"ds_read_b128 a[104:107],v114 offset:43008\n"
"v_cvt_pk_bf16_f32 v80,v122,v123\n"
"v_mfma_f32_16x16x32_bf16 v[4:7],a[88:91],a[8:11],v[4:7]\n"
"v_exp_f32 v120,v35\n"
"v_mfma_f32_16x16x32_bf16 v[4:7],a[84:87],a[4:7],v[4:7]\n"
"ds_read_b128 a[108:111],v114 offset:44032\n"
"ds_read_b128 a[112:115],v114 offset:45056\n"
"v_mfma_f32_16x16x32_bf16 v[4:7],a[80:83],a[0:3],v[4:7]\n"
"v_exp_f32 v122,v36\n"
"v_mfma_f32_16x16x32_bf16 v[12:15],a[80:83],a[16:19],v[100:103]\n"
"ds_read_b128 a[116:119],v114 offset:46080\n"
"v_cvt_pk_bf16_f32 v81,v119,v120\n"
"v_mfma_f32_16x16x32_bf16 v[12:15],a[84:87],a[20:23],v[12:15]\n"
"v_exp_f32 v123,v37\n"
"v_mfma_f32_16x16x32_bf16 v[12:15],a[88:91],a[24:27],v[12:15]\n"
"ds_read_b128 a[120:123],v114 offset:47104\n"
"ds_read_b128 a[124:127],v114 offset:48128\n"
"v_mfma_f32_16x16x32_bf16 v[12:15],a[92:95],a[28:31],v[12:15]\n"
"v_exp_f32 v119,v38\n"
"v_mfma_f32_16x16x32_bf16 v[8:11],a[76:79],a[28:31],v[100:103]\n"
"v_cvt_pk_bf16_f32 v82,v122,v123\n"
"v_or3_b32 v118,v80,v81,v82\n"
"v_mfma_f32_16x16x32_bf16 v[8:11],a[72:75],a[24:27],v[8:11]\n"
"v_exp_f32 v120,v39\n"
"v_mfma_f32_16x16x32_bf16 v[8:11],a[68:71],a[20:23],v[8:11]\n"
"buffer_load_dwordx4 v115,s[32:35],s54 offen sc1 lds\n"
"v_mfma_f32_16x16x32_bf16 v[8:11],a[64:67],a[16:19],v[8:11]\n"
"s_add_u32 m0,s67,49152\n"
"v_exp_f32 v122,v40\n"
"v_mfma_f32_16x16x32_bf16 v[16:19],a[64:67],a[32:35],v[104:107]\n"
"v_cvt_pk_bf16_f32 v83,v119,v120\n"
"v_mfma_f32_16x16x32_bf16 v[16:19],a[68:71],a[36:39],v[16:19]\n"
"v_exp_f32 v123,v41\n"
"v_mfma_f32_16x16x32_bf16 v[16:19],a[72:75],a[40:43],v[16:19]\n"
"buffer_load_dwordx4 v112,s[24:27],s64 offen offset:0 sc1 lds\n"
"v_mfma_f32_16x16x32_bf16 v[16:19],a[76:79],a[44:47],v[16:19]\n"
"v_exp_f32 v119,v42\n"
"v_mfma_f32_16x16x32_bf16 v[20:23],a[92:95],a[44:47],v[104:107]\n"
"v_cvt_pk_bf16_f32 v84,v122,v123\n"
"v_or3_b32 v118,v118,v83,v84\n"
"v_mfma_f32_16x16x32_bf16 v[20:23],a[88:91],a[40:43],v[20:23]\n"
"v_exp_f32 v120,v43\n"
"v_mfma_f32_16x16x32_bf16 v[20:23],a[84:87],a[36:39],v[20:23]\n"
"buffer_load_dwordx4 v112,s[24:27],s64 offen offset:1024 sc1 lds\n"
"v_mfma_f32_16x16x32_bf16 v[20:23],a[80:83],a[32:35],v[20:23]\n"
"v_exp_f32 v122,v44\n"
"v_mfma_f32_16x16x32_bf16 v[28:31],a[80:83],a[48:51],v[108:111]\n"
"v_cvt_pk_bf16_f32 v85,v119,v120\n"
"v_mfma_f32_16x16x32_bf16 v[28:31],a[84:87],a[52:55],v[28:31]\n"
"v_exp_f32 v123,v45\n"
"v_mfma_f32_16x16x32_bf16 v[28:31],a[88:91],a[56:59],v[28:31]\n"
"buffer_load_dwordx4 v112,s[24:27],s64 offen offset:2048 sc1 lds\n"
"v_mfma_f32_16x16x32_bf16 v[28:31],a[92:95],a[60:63],v[28:31]\n"
"v_exp_f32 v119,v46\n"
"v_mfma_f32_16x16x32_bf16 v[24:27],a[76:79],a[60:63],v[108:111]\n"
"v_cvt_pk_bf16_f32 v86,v122,v123\n"
"v_or3_b32 v118,v118,v85,v86\n"
"v_mfma_f32_16x16x32_bf16 v[24:27],a[72:75],a[56:59],v[24:27]\n"
"v_exp_f32 v120,v47\n"
"v_mfma_f32_16x16x32_bf16 v[24:27],a[68:71],a[52:55],v[24:27]\n"
"buffer_load_dwordx4 v112,s[24:27],s64 offen offset:3072 sc1 lds\n"
"v_mfma_f32_16x16x32_bf16 v[24:27],a[64:67],a[48:51],v[24:27]\n"
"s_waitcnt lgkmcnt(0)\n"
"v_exp_f32 v122,v48\n"
"v_mfma_f32_16x16x32_bf16 v[128:131],a[96:99],v[64:67],0\n"
"v_exp_f32 v123,v49\n"
"v_mfma_f32_16x16x32_bf16 v[160:163],a[96:99],v[68:71],0\n"
"ds_read_b128 a[64:67],v113 offset:32768\n"
"v_cvt_pk_bf16_f32 v87,v119,v120\n"
"v_mfma_f32_16x16x32_bf16 v[192:195],a[96:99],v[72:75],0\n"
"s_add_u32 m0,s67,53248\n"
"v_exp_f32 v119,v50\n"
"v_mfma_f32_16x16x32_bf16 v[224:227],a[96:99],v[76:79],0\n"
"ds_read_b128 a[68:71],v113 offset:33792\n"
"v_cvt_pk_bf16_f32 v88,v122,v123\n"
"v_mfma_f32_16x16x32_bf16 a[140:143],a[144:147],v[76:79],0\n"
"v_exp_f32 v120,v51\n"
"v_mfma_f32_16x16x32_bf16 v[228:231],a[100:103],v[76:79],0\n"
"ds_read_b128 a[72:75],v113 offset:34816\n"
"ds_read_b128 a[76:79],v113 offset:35840\n"
"v_mfma_f32_16x16x32_bf16 v[132:135],a[100:103],v[64:67],0\n"
"v_exp_f32 v122,v52\n"
"v_mfma_f32_16x16x32_bf16 v[164:167],a[100:103],v[68:71],0\n"
"ds_read_b128 a[80:83],v113 offset:36864\n"
"v_cvt_pk_bf16_f32 v89,v119,v120\n"
"v_mfma_f32_16x16x32_bf16 v[196:199],a[100:103],v[72:75],0\n"
"v_exp_f32 v123,v53\n"
"v_mfma_f32_16x16x32_bf16 a[136:139],a[144:147],v[72:75],0\n"
"ds_read_b128 a[84:87],v113 offset:37888\n"
"ds_read_b128 a[88:91],v113 offset:38912\n"
"v_mfma_f32_16x16x32_bf16 v[200:203],a[104:107],v[72:75],0\n"
"v_exp_f32 v119,v54\n"
"v_mfma_f32_16x16x32_bf16 v[232:235],a[104:107],v[76:79],0\n"
"ds_read_b128 a[92:95],v113 offset:39936\n"
"v_cvt_pk_bf16_f32 v90,v122,v123\n"
"v_mfma_f32_16x16x32_bf16 v[136:139],a[104:107],v[64:67],0\n"
"v_exp_f32 v120,v55\n"
"v_mfma_f32_16x16x32_bf16 v[168:171],a[104:107],v[68:71],0\n"
"s_waitcnt vmcnt(16)\n"
"ds_add_u32 v124,a152\n"
"v_mfma_f32_16x16x32_bf16 a[132:135],a[144:147],v[68:71],0\n"
"v_exp_f32 v122,v56\n"
"v_mfma_f32_16x16x32_bf16 v[172:175],a[108:111],v[68:71],0\n"
"v_cvt_pk_bf16_f32 v91,v119,v120\n"
"v_or3_b32 v118,v118,v87,v88\n"
"v_mfma_f32_16x16x32_bf16 v[204:207],a[108:111],v[72:75],0\n"
"v_exp_f32 v123,v57\n"
"v_mfma_f32_16x16x32_bf16 v[236:239],a[108:111],v[76:79],0\n"
"buffer_load_dwordx4 v117,s[24:27],s64 offen offset:0 sc1 lds\n"
"v_mfma_f32_16x16x32_bf16 v[140:143],a[108:111],v[64:67],0\n"
"v_exp_f32 v119,v58\n"
"v_mfma_f32_16x16x32_bf16 a[128:131],a[144:147],v[64:67],0\n"
"v_cvt_pk_bf16_f32 v92,v122,v123\n"
"v_or3_b32 v118,v118,v89,v90\n"
"v_mfma_f32_16x16x32_bf16 v[144:147],a[112:115],v[64:67],0\n"
"v_exp_f32 v120,v59\n"
"v_mfma_f32_16x16x32_bf16 v[176:179],a[112:115],v[68:71],0\n"
"buffer_load_dwordx4 v117,s[24:27],s64 offen offset:1024 sc1 lds\n"
"v_mfma_f32_16x16x32_bf16 v[208:211],a[112:115],v[72:75],0\n"
"v_exp_f32 v122,v60\n"
"v_mfma_f32_16x16x32_bf16 v[240:243],a[112:115],v[76:79],0\n"
"v_cvt_pk_bf16_f32 v93,v119,v120\n"
"v_or3_b32 v118,v118,v91,v92\n"
"v_mfma_f32_16x16x32_bf16 v[244:247],a[116:119],v[76:79],0\n"
"v_exp_f32 v123,v61\n"
"v_mfma_f32_16x16x32_bf16 v[148:151],a[116:119],v[64:67],0\n"
"buffer_load_dwordx4 v117,s[24:27],s64 offen offset:2048 sc1 lds\n"
"v_mfma_f32_16x16x32_bf16 v[180:183],a[116:119],v[68:71],0\n"
"v_exp_f32 v119,v62\n"
"v_mfma_f32_16x16x32_bf16 v[212:215],a[116:119],v[72:75],0\n"
"ds_read_b32 v125,v124\n"
"v_cvt_pk_bf16_f32 v94,v122,v123\n"
"v_mfma_f32_16x16x32_bf16 v[216:219],a[120:123],v[72:75],0\n"
"v_exp_f32 v120,v63\n"
"v_mfma_f32_16x16x32_bf16 v[248:251],a[120:123],v[76:79],0\n"
"buffer_load_dwordx4 v117,s[24:27],s64 offen offset:3072 sc1 lds\n"
"v_mfma_f32_16x16x32_bf16 v[152:155],a[120:123],v[64:67],0\n"
"buffer_store_dwordx4 a[248:251],v112,s[24:27],s56 offen offset:2048 sc1\n"
"v_mfma_f32_16x16x32_bf16 v[184:187],a[120:123],v[68:71],0\n"
"s_add_u32 s64,s64,16384\n"
"v_cvt_pk_bf16_f32 v95,v119,v120\n"
"v_or3_b32 v118,v118,v93,v94\n"
"v_mfma_f32_16x16x32_bf16 v[188:191],a[124:127],v[68:71],0\n"
"s_waitcnt lgkmcnt(0)\n"
"v_subrev_u32 v125,s101,v125\n"
"v_or3_b32 v125,v118,v95,v125\n"
"v_mfma_f32_16x16x32_bf16 v[220:223],a[124:127],v[72:75],0\n"
"v_and_b32 v125,0x40004000,v125\n"
"v_cmp_ne_u32 vcc,0,v125\n"
"v_mfma_f32_16x16x32_bf16 v[252:255],a[124:127],v[76:79],0\n"
"v_mfma_f32_16x16x32_bf16 v[156:159],a[124:127],v[64:67],0\n"
"s_cbranch_vccnz .Lfk_cs_v1\n"
".Lfk_cr_v1:\n"
"v_mfma_f32_16x16x32_bf16 v[32:35],a[64:67],a[0:3],v[96:99]\n"
"s_add_u32 s101,s101,4\n"
"v_exp_f32 v122,v0\n"
"v_mfma_f32_16x16x32_bf16 v[32:35],a[68:71],a[4:7],v[32:35]\n"
"s_add_u32 m0,s55,12288\n"
"v_exp_f32 v123,v1\n"
"v_mfma_f32_16x16x32_bf16 v[32:35],a[72:75],a[8:11],v[32:35]\n"
"ds_read_b128 a[96:99],v114 offset:8192\n"
"ds_read_b128 a[100:103],v114 offset:9216\n"
"v_mfma_f32_16x16x32_bf16 v[32:35],a[76:79],a[12:15],v[32:35]\n"
"s_add_u32 s54,s54,s22\n"
"v_exp_f32 v119,v2\n"
"v_mfma_f32_16x16x32_bf16 v[36:39],a[92:95],a[12:15],v[96:99]\n"
"ds_read_b128 a[104:107],v114 offset:10240\n"
"v_cvt_pk_bf16_f32 v64,v122,v123\n"
"v_mfma_f32_16x16x32_bf16 v[36:39],a[88:91],a[8:11],v[36:39]\n"
"v_exp_f32 v120,v3\n"
"v_mfma_f32_16x16x32_bf16 v[36:39],a[84:87],a[4:7],v[36:39]\n"
"ds_read_b128 a[108:111],v114 offset:11264\n"
"ds_read_b128 a[112:115],v114 offset:12288\n"
"v_mfma_f32_16x16x32_bf16 v[36:39],a[80:83],a[0:3],v[36:39]\n"
"v_exp_f32 v122,v4\n"
"v_mfma_f32_16x16x32_bf16 v[44:47],a[80:83],a[16:19],v[100:103]\n"
"ds_read_b128 a[116:119],v114 offset:13312\n"
"v_cvt_pk_bf16_f32 v65,v119,v120\n"
"v_mfma_f32_16x16x32_bf16 v[44:47],a[84:87],a[20:23],v[44:47]\n"
"v_exp_f32 v123,v5\n"
"v_mfma_f32_16x16x32_bf16 v[44:47],a[88:91],a[24:27],v[44:47]\n"
"ds_read_b128 a[120:123],v114 offset:14336\n"
"ds_read_b128 a[124:127],v114 offset:15360\n"
"v_mfma_f32_16x16x32_bf16 v[44:47],a[92:95],a[28:31],v[44:47]\n"
"v_exp_f32 v119,v6\n"
"v_mfma_f32_16x16x32_bf16 v[40:43],a[76:79],a[28:31],v[100:103]\n"
"v_cvt_pk_bf16_f32 v66,v122,v123\n"
"v_or3_b32 v118,v64,v65,v66\n"
"v_mfma_f32_16x16x32_bf16 v[40:43],a[72:75],a[24:27],v[40:43]\n"
"v_exp_f32 v120,v7\n"
"v_mfma_f32_16x16x32_bf16 v[40:43],a[68:71],a[20:23],v[40:43]\n"
"buffer_load_dwordx4 v115,s[32:35],s54 offen sc1 lds\n"
"v_mfma_f32_16x16x32_bf16 v[40:43],a[64:67],a[16:19],v[40:43]\n"
"s_add_u32 m0,s67,32768\n"
"v_exp_f32 v122,v8\n"
"v_mfma_f32_16x16x32_bf16 v[48:51],a[64:67],a[32:35],v[104:107]\n"
"v_cvt_pk_bf16_f32 v67,v119,v120\n"
"v_mfma_f32_16x16x32_bf16 v[48:51],a[68:71],a[36:39],v[48:51]\n"
"v_exp_f32 v123,v9\n"
"v_mfma_f32_16x16x32_bf16 v[48:51],a[72:75],a[40:43],v[48:51]\n"
"buffer_load_dwordx4 v112,s[24:27],s64 offen offset:0 sc1 lds\n"
"v_mfma_f32_16x16x32_bf16 v[48:51],a[76:79],a[44:47],v[48:51]\n"
"v_exp_f32 v119,v10\n"
"v_mfma_f32_16x16x32_bf16 v[52:55],a[92:95],a[44:47],v[104:107]\n"
"v_cvt_pk_bf16_f32 v68,v122,v123\n"
"v_or3_b32 v118,v118,v67,v68\n"
"v_mfma_f32_16x16x32_bf16 v[52:55],a[88:91],a[40:43],v[52:55]\n"
"v_exp_f32 v120,v11\n"
"v_mfma_f32_16x16x32_bf16 v[52:55],a[84:87],a[36:39],v[52:55]\n"
"buffer_load_dwordx4 v112,s[24:27],s64 offen offset:1024 sc1 lds\n"
"v_mfma_f32_16x16x32_bf16 v[52:55],a[80:83],a[32:35],v[52:55]\n"
"v_exp_f32 v122,v12\n"
"v_mfma_f32_16x16x32_bf16 v[60:63],a[80:83],a[48:51],v[108:111]\n"
"v_cvt_pk_bf16_f32 v69,v119,v120\n"
"v_mfma_f32_16x16x32_bf16 v[60:63],a[84:87],a[52:55],v[60:63]\n"
"v_exp_f32 v123,v13\n"
"v_mfma_f32_16x16x32_bf16 v[60:63],a[88:91],a[56:59],v[60:63]\n"
"buffer_load_dwordx4 v112,s[24:27],s64 offen offset:2048 sc1 lds\n"
"v_mfma_f32_16x16x32_bf16 v[60:63],a[92:95],a[60:63],v[60:63]\n"
"v_exp_f32 v119,v14\n"
"v_mfma_f32_16x16x32_bf16 v[56:59],a[76:79],a[60:63],v[108:111]\n"
"v_cvt_pk_bf16_f32 v70,v122,v123\n"
"v_or3_b32 v118,v118,v69,v70\n"
"v_mfma_f32_16x16x32_bf16 v[56:59],a[72:75],a[56:59],v[56:59]\n"
"v_exp_f32 v120,v15\n"
"v_mfma_f32_16x16x32_bf16 v[56:59],a[68:71],a[52:55],v[56:59]\n"
"buffer_load_dwordx4 v112,s[24:27],s64 offen offset:3072 sc1 lds\n"
"v_mfma_f32_16x16x32_bf16 v[56:59],a[64:67],a[48:51],v[56:59]\n"
"s_waitcnt lgkmcnt(0)\n"
"v_exp_f32 v122,v16\n"
"v_mfma_f32_16x16x32_bf16 v[128:131],a[96:99],v[80:83],v[128:131]\n"
"v_exp_f32 v123,v17\n"
"v_mfma_f32_16x16x32_bf16 v[160:163],a[96:99],v[84:87],v[160:163]\n"
"ds_read_b128 a[64:67],v113 offset:0\n"
"v_cvt_pk_bf16_f32 v71,v119,v120\n"
"v_mfma_f32_16x16x32_bf16 v[192:195],a[96:99],v[88:91],v[192:195]\n"
"s_add_u32 m0,s67,36864\n"
"v_exp_f32 v119,v18\n"
"v_mfma_f32_16x16x32_bf16 v[224:227],a[96:99],v[92:95],v[224:227]\n"
"ds_read_b128 a[68:71],v113 offset:1024\n"
"v_cvt_pk_bf16_f32 v72,v122,v123\n"
"v_mfma_f32_16x16x32_bf16 a[140:143],a[144:147],v[92:95],a[140:143]\n"
"v_exp_f32 v120,v19\n"
"v_mfma_f32_16x16x32_bf16 v[228:231],a[100:103],v[92:95],v[228:231]\n"
"ds_read_b128 a[72:75],v113 offset:2048\n"
"ds_read_b128 a[76:79],v113 offset:3072\n"
"v_mfma_f32_16x16x32_bf16 v[132:135],a[100:103],v[80:83],v[132:135]\n"
"v_exp_f32 v122,v20\n"
"v_mfma_f32_16x16x32_bf16 v[164:167],a[100:103],v[84:87],v[164:167]\n"
"ds_read_b128 a[80:83],v113 offset:4096\n"
"v_cvt_pk_bf16_f32 v73,v119,v120\n"
"v_mfma_f32_16x16x32_bf16 v[196:199],a[100:103],v[88:91],v[196:199]\n"
"v_exp_f32 v123,v21\n"
"v_mfma_f32_16x16x32_bf16 a[136:139],a[144:147],v[88:91],a[136:139]\n"
"ds_read_b128 a[84:87],v113 offset:5120\n"
"ds_read_b128 a[88:91],v113 offset:6144\n"
"v_mfma_f32_16x16x32_bf16 v[200:203],a[104:107],v[88:91],v[200:203]\n"
"v_exp_f32 v119,v22\n"
"v_mfma_f32_16x16x32_bf16 v[232:235],a[104:107],v[92:95],v[232:235]\n"
"ds_read_b128 a[92:95],v113 offset:7168\n"
"v_cvt_pk_bf16_f32 v74,v122,v123\n"
"v_mfma_f32_16x16x32_bf16 v[136:139],a[104:107],v[80:83],v[136:139]\n"
"v_exp_f32 v120,v23\n"
"v_mfma_f32_16x16x32_bf16 v[168:171],a[104:107],v[84:87],v[168:171]\n"
"s_waitcnt vmcnt(16)\n"
"ds_add_u32 v124,a152\n"
"v_mfma_f32_16x16x32_bf16 a[132:135],a[144:147],v[84:87],a[132:135]\n"
"v_exp_f32 v122,v24\n"
"v_mfma_f32_16x16x32_bf16 v[172:175],a[108:111],v[84:87],v[172:175]\n"
"v_cvt_pk_bf16_f32 v75,v119,v120\n"
"v_or3_b32 v118,v118,v71,v72\n"
"v_mfma_f32_16x16x32_bf16 v[204:207],a[108:111],v[88:91],v[204:207]\n"
"v_exp_f32 v123,v25\n"
"v_mfma_f32_16x16x32_bf16 v[236:239],a[108:111],v[92:95],v[236:239]\n"
"buffer_load_dwordx4 v117,s[24:27],s64 offen offset:0 sc1 lds\n"
"v_mfma_f32_16x16x32_bf16 v[140:143],a[108:111],v[80:83],v[140:143]\n"
"v_exp_f32 v119,v26\n"
"v_mfma_f32_16x16x32_bf16 a[128:131],a[144:147],v[80:83],a[128:131]\n"
"v_cvt_pk_bf16_f32 v76,v122,v123\n"
"v_or3_b32 v118,v118,v73,v74\n"
"v_mfma_f32_16x16x32_bf16 v[144:147],a[112:115],v[80:83],v[144:147]\n"
"v_exp_f32 v120,v27\n"
"v_mfma_f32_16x16x32_bf16 v[176:179],a[112:115],v[84:87],v[176:179]\n"
"buffer_load_dwordx4 v117,s[24:27],s64 offen offset:1024 sc1 lds\n"
"v_mfma_f32_16x16x32_bf16 v[208:211],a[112:115],v[88:91],v[208:211]\n"
"v_exp_f32 v122,v28\n"
"v_mfma_f32_16x16x32_bf16 v[240:243],a[112:115],v[92:95],v[240:243]\n"
"v_cvt_pk_bf16_f32 v77,v119,v120\n"
"v_or3_b32 v118,v118,v75,v76\n"
"v_mfma_f32_16x16x32_bf16 v[244:247],a[116:119],v[92:95],v[244:247]\n"
"v_exp_f32 v123,v29\n"
"v_mfma_f32_16x16x32_bf16 v[148:151],a[116:119],v[80:83],v[148:151]\n"
"buffer_load_dwordx4 v117,s[24:27],s64 offen offset:2048 sc1 lds\n"
"v_mfma_f32_16x16x32_bf16 v[180:183],a[116:119],v[84:87],v[180:183]\n"
"v_exp_f32 v119,v30\n"
"v_mfma_f32_16x16x32_bf16 v[212:215],a[116:119],v[88:91],v[212:215]\n"
"ds_read_b32 v125,v124\n"
"v_cvt_pk_bf16_f32 v78,v122,v123\n"
"v_mfma_f32_16x16x32_bf16 v[216:219],a[120:123],v[88:91],v[216:219]\n"
"v_exp_f32 v120,v31\n"
"v_mfma_f32_16x16x32_bf16 v[248:251],a[120:123],v[92:95],v[248:251]\n"
"buffer_load_dwordx4 v117,s[24:27],s64 offen offset:3072 sc1 lds\n"
"v_mfma_f32_16x16x32_bf16 v[152:155],a[120:123],v[80:83],v[152:155]\n"
"buffer_store_dwordx4 a[252:255],v112,s[24:27],s56 offen offset:3072 sc1\n"
"v_mfma_f32_16x16x32_bf16 v[184:187],a[120:123],v[84:87],v[184:187]\n"
"s_add_u32 s64,s64,16384\n"
"s_add_u32 s89,s89,1\n"
"v_cvt_pk_bf16_f32 v79,v119,v120\n"
"v_mfma_f32_16x16x32_bf16 v[188:191],a[124:127],v[84:87],v[188:191]\n"
"s_waitcnt lgkmcnt(0)\n"
"v_subrev_u32 v125,s101,v125\n"
"v_or3_b32 v118,v118,v77,v78\n"
"v_mfma_f32_16x16x32_bf16 v[220:223],a[124:127],v[88:91],v[220:223]\n"
"v_or3_b32 v125,v118,v79,v125\n"
"v_and_b32 v125,0x40004000,v125\n"
"v_mfma_f32_16x16x32_bf16 v[252:255],a[124:127],v[92:95],v[252:255]\n"
"v_cmp_ne_u32 vcc,0,v125\n"
"v_mfma_f32_16x16x32_bf16 v[156:159],a[124:127],v[80:83],v[156:159]\n"
"s_cbranch_vccnz .Lfk_cs_v2\n"
".Lfk_cr_v2:\n"
"s_waitcnt vmcnt(20)\n"
"buffer_store_dwordx2 a[154:155],off,s[24:27],s69 sc1\n"
"s_branch .Lfk_Hx\n"
".Lfk_tailB:\n"
"s_waitcnt vmcnt(11) lgkmcnt(0)\n"
"s_barrier\n"
"v_mfma_f32_16x16x32_bf16 v[0:3],a[64:67],a[0:3],v[96:99]\n"
"ds_read_b128 a[96:99],v114 offset:24576\n"
"ds_read_b128 a[100:103],v114 offset:25600\n"
"ds_read_b128 a[104:107],v114 offset:26624\n"
"v_mfma_f32_16x16x32_bf16 v[0:3],a[68:71],a[4:7],v[0:3]\n"
"ds_read_b128 a[108:111],v114 offset:27648\n"
"ds_read_b128 a[112:115],v114 offset:28672\n"
"ds_read_b128 a[116:119],v114 offset:29696\n"
"v_mfma_f32_16x16x32_bf16 v[0:3],a[72:75],a[8:11],v[0:3]\n"
"ds_read_b128 a[120:123],v114 offset:30720\n"
"ds_read_b128 a[124:127],v114 offset:31744\n"
"v_add_f32 v16,0x80000000,v126\n"
"v_mfma_f32_16x16x32_bf16 v[0:3],a[76:79],a[12:15],v[0:3]\n"
"v_min_f32 v16,v16,v104\n"
"v_add_f32 v17,0xf149f2ca,v126\n"
"v_min_f32 v17,v17,v105\n"
"v_mfma_f32_16x16x32_bf16 v[4:7],a[92:95],a[12:15],v[96:99]\n"
"v_add_f32 v18,0xf1c9f2ca,v126\n"
"v_min_f32 v18,v18,v106\n"
"v_add_f32 v19,0xf2177617,v126\n"
"v_mfma_f32_16x16x32_bf16 v[4:7],a[88:91],a[8:11],v[4:7]\n"
"v_min_f32 v19,v19,v107\n"
"v_add_f32 v20,0xf349f2ca,v126\n"
"v_min_f32 v20,v20,v104\n"
"v_mfma_f32_16x16x32_bf16 v[4:7],a[84:87],a[4:7],v[4:7]\n"
"v_add_f32 v21,0xf35691f6,v126\n"
"v_min_f32 v21,v21,v105\n"
"v_add_f32 v22,0xf3633123,v126\n"
"v_mfma_f32_16x16x32_bf16 v[4:7],a[80:83],a[0:3],v[4:7]\n"
"v_min_f32 v22,v22,v106\n"
"v_add_f32 v23,0xf36fd050,v126\n"
"v_min_f32 v23,v23,v107\n"
"v_mfma_f32_16x16x32_bf16 v[12:15],a[80:83],a[16:19],v[100:103]\n"
"v_exp_f32 v32,v32\n"
"v_max3_f32 v118,v0,v1,v2\n"
"v_mfma_f32_16x16x32_bf16 v[12:15],a[84:87],a[20:23],v[12:15]\n"
"v_exp_f32 v33,v33\n"
"v_mfma_f32_16x16x32_bf16 v[12:15],a[88:91],a[24:27],v[12:15]\n"
"v_exp_f32 v34,v34\n"
"v_cvt_pk_bf16_f32 v80,v32,v33\n"
"v_mfma_f32_16x16x32_bf16 v[12:15],a[92:95],a[28:31],v[12:15]\n"
"v_exp_f32 v35,v35\n"
"v_max3_f32 v118,v118,v3,v4\n"
"v_mfma_f32_16x16x32_bf16 v[8:11],a[76:79],a[28:31],v[100:103]\n"
"v_exp_f32 v36,v36\n"
"v_cvt_pk_bf16_f32 v81,v34,v35\n"
"v_mfma_f32_16x16x32_bf16 v[8:11],a[72:75],a[24:27],v[8:11]\n"
"v_exp_f32 v37,v37\n"
"v_max3_f32 v118,v118,v5,v6\n"
"v_mfma_f32_16x16x32_bf16 v[8:11],a[68:71],a[20:23],v[8:11]\n"
"v_exp_f32 v38,v38\n"
"v_mfma_f32_16x16x32_bf16 v[8:11],a[64:67],a[16:19],v[8:11]\n"
"v_exp_f32 v39,v39\n"
"v_max3_f32 v118,v118,v7,v12\n"
"v_mfma_f32_16x16x32_bf16 v[24:27],a[64:67],a[48:51],v[108:111]\n"
"v_exp_f32 v40,v40\n"
"v_mfma_f32_16x16x32_bf16 v[24:27],a[68:71],a[52:55],v[24:27]\n"
"v_exp_f32 v41,v41\n"
"v_max3_f32 v118,v118,v13,v14\n"
"v_mfma_f32_16x16x32_bf16 v[24:27],a[72:75],a[56:59],v[24:27]\n"
"v_cvt_pk_bf16_f32 v82,v36,v37\n"
"v_cvt_pk_bf16_f32 v84,v40,v41\n"
"v_mfma_f32_16x16x32_bf16 v[24:27],a[76:79],a[60:63],v[24:27]\n"
"v_exp_f32 v42,v42\n"
"v_max3_f32 v118,v118,v15,v8\n"
"v_mfma_f32_16x16x32_bf16 v[28:31],a[92:95],a[60:63],v[108:111]\n"
"v_cvt_pk_bf16_f32 v83,v38,v39\n"
"v_max3_f32 v118,v118,v9,v10\n"
"v_mfma_f32_16x16x32_bf16 v[28:31],a[88:91],a[56:59],v[28:31]\n"
"v_exp_f32 v43,v43\n"
"v_mfma_f32_16x16x32_bf16 v[28:31],a[84:87],a[52:55],v[28:31]\n"
"v_cvt_pk_bf16_f32 v85,v42,v43\n"
"v_mfma_f32_16x16x32_bf16 v[28:31],a[80:83],a[48:51],v[28:31]\n"
"v_exp_f32 v44,v44\n"
"v_mfma_f32_16x16x32_bf16 v[20:23],a[80:83],a[32:35],v[20:23]\n"
"v_max3_f32 v118,v118,v11,v24\n"
"v_max3_f32 v118,v118,v25,v26\n"
"v_mfma_f32_16x16x32_bf16 v[20:23],a[84:87],a[36:39],v[20:23]\n"
"v_exp_f32 v45,v45\n"
"v_mfma_f32_16x16x32_bf16 v[20:23],a[88:91],a[40:43],v[20:23]\n"
"v_cvt_pk_bf16_f32 v86,v44,v45\n"
"v_mfma_f32_16x16x32_bf16 v[20:23],a[92:95],a[44:47],v[20:23]\n"
"v_exp_f32 v46,v46\n"
"v_mfma_f32_16x16x32_bf16 v[16:19],a[76:79],a[44:47],v[16:19]\n"
"v_max3_f32 v118,v118,v27,v28\n"
"v_max3_f32 v118,v118,v29,v30\n"
"v_mfma_f32_16x16x32_bf16 v[16:19],a[72:75],a[40:43],v[16:19]\n"
"v_exp_f32 v47,v47\n"
"v_mfma_f32_16x16x32_bf16 v[16:19],a[68:71],a[36:39],v[16:19]\n"
"v_cvt_pk_bf16_f32 v87,v46,v47\n"
"v_exp_f32 v48,v48\n"
"v_mfma_f32_16x16x32_bf16 v[16:19],a[64:67],a[32:35],v[16:19]\n"
"v_max3_f32 v118,v118,v31,v20\n"
"s_waitcnt lgkmcnt(0)\n"
"v_mfma_f32_16x16x32_bf16 v[128:131],a[96:99],v[64:67],v[128:131]\n"
"v_exp_f32 v49,v49\n"
"v_max3_f32 v118,v118,v21,v22\n"
"v_cvt_pk_bf16_f32 v88,v48,v49\n"
"v_mfma_f32_16x16x32_bf16 v[160:163],a[96:99],v[68:71],v[160:163]\n"
"ds_read_b128 a[64:67],v114 offset:57344\n"
"ds_read_b128 a[68:71],v114 offset:58368\n"
"v_mfma_f32_16x16x32_bf16 v[192:195],a[96:99],v[72:75],v[192:195]\n"
"ds_read_b128 a[72:75],v114 offset:59392\n"
"v_exp_f32 v50,v50\n"
"v_mfma_f32_16x16x32_bf16 v[224:227],a[96:99],v[76:79],v[224:227]\n"
"v_max3_f32 v118,v118,v23,v16\n"
"v_max3_f32 v118,v118,v17,v18\n"
"v_max_f32 v118,v118,v19\n"
"v_mfma_f32_16x16x32_bf16 a[140:143],a[144:147],v[76:79],a[140:143]\n"
"v_exp_f32 v51,v51\n"
"v_cmp_lt_f32 vcc,s93,v118\n"
"v_cvt_pk_bf16_f32 v89,v50,v51\n"
"v_mfma_f32_16x16x32_bf16 v[228:231],a[100:103],v[76:79],v[228:231]\n"
"ds_read_b128 a[76:79],v114 offset:60416\n"
"ds_read_b128 a[80:83],v114 offset:61440\n"
"v_mfma_f32_16x16x32_bf16 v[132:135],a[100:103],v[64:67],v[132:135]\n"
"ds_read_b128 a[84:87],v114 offset:62464\n"
"v_exp_f32 v52,v52\n"
"v_mfma_f32_16x16x32_bf16 v[164:167],a[100:103],v[68:71],v[164:167]\n"
"s_sub_u32 s82,s3,32768\n"
"ds_read_b128 a[88:91],v114 offset:63488\n"
"ds_read_b128 a[92:95],v114 offset:64512\n"
"v_mfma_f32_16x16x32_bf16 v[196:199],a[100:103],v[72:75],v[196:199]\n"
"buffer_load_dwordx4 a[0:3],v112,s[24:27],s82 offen offset:0 sc1\n"
"v_exp_f32 v53,v53\n"
"v_mfma_f32_16x16x32_bf16 a[136:139],a[144:147],v[72:75],a[136:139]\n"
"buffer_load_dwordx4 a[4:7],v112,s[24:27],s82 offen offset:1024 sc1\n"
"buffer_load_dwordx4 a[8:11],v112,s[24:27],s82 offen offset:2048 sc1\n"
"v_cvt_pk_bf16_f32 v90,v52,v53\n"
"v_exp_f32 v54,v54\n"
"v_mfma_f32_16x16x32_bf16 v[200:203],a[104:107],v[72:75],v[200:203]\n"
"buffer_load_dwordx4 a[12:15],v112,s[24:27],s82 offen offset:3072 sc1\n"
"v_mfma_f32_16x16x32_bf16 v[232:235],a[104:107],v[76:79],v[232:235]\n"
"s_sub_u32 s82,s3,49152\n"
"buffer_load_dwordx4 a[16:19],v112,s[24:27],s82 offen offset:0 sc1\n"
"buffer_load_dwordx4 a[20:23],v112,s[24:27],s82 offen offset:1024 sc1\n"
"v_exp_f32 v55,v55\n"
"v_mfma_f32_16x16x32_bf16 v[136:139],a[104:107],v[64:67],v[136:139]\n"
"buffer_load_dwordx4 a[24:27],v112,s[24:27],s82 offen offset:2048 sc1\n"
"v_mfma_f32_16x16x32_bf16 v[168:171],a[104:107],v[68:71],v[168:171]\n"
"buffer_load_dwordx4 a[28:31],v112,s[24:27],s82 offen offset:3072 sc1\n"
"v_mfma_f32_16x16x32_bf16 a[132:135],a[144:147],v[68:71],a[132:135]\n"
"v_exp_f32 v56,v56\n"
"v_mfma_f32_16x16x32_bf16 v[172:175],a[108:111],v[68:71],v[172:175]\n"
"v_cvt_pk_bf16_f32 v91,v54,v55\n"
"v_mfma_f32_16x16x32_bf16 v[204:207],a[108:111],v[72:75],v[204:207]\n"
"v_exp_f32 v57,v57\n"
"v_mfma_f32_16x16x32_bf16 v[236:239],a[108:111],v[76:79],v[236:239]\n"
"v_cvt_pk_bf16_f32 v92,v56,v57\n"
"v_mfma_f32_16x16x32_bf16 v[140:143],a[108:111],v[64:67],v[140:143]\n"
"v_exp_f32 v58,v58\n"
"v_mfma_f32_16x16x32_bf16 a[128:131],a[144:147],v[64:67],a[128:131]\n"
"v_mfma_f32_16x16x32_bf16 v[144:147],a[112:115],v[64:67],v[144:147]\n"
"v_exp_f32 v59,v59\n"
"v_mfma_f32_16x16x32_bf16 v[176:179],a[112:115],v[68:71],v[176:179]\n"
"v_cvt_pk_bf16_f32 v93,v58,v59\n"
"v_mfma_f32_16x16x32_bf16 v[208:211],a[112:115],v[72:75],v[208:211]\n"
"v_exp_f32 v60,v60\n"
"v_mfma_f32_16x16x32_bf16 v[240:243],a[112:115],v[76:79],v[240:243]\n"
"v_mfma_f32_16x16x32_bf16 v[244:247],a[116:119],v[76:79],v[244:247]\n"
"v_exp_f32 v61,v61\n"
"v_mfma_f32_16x16x32_bf16 v[148:151],a[116:119],v[64:67],v[148:151]\n"
"v_cvt_pk_bf16_f32 v94,v60,v61\n"
"v_mfma_f32_16x16x32_bf16 v[180:183],a[116:119],v[68:71],v[180:183]\n"
"v_exp_f32 v62,v62\n"
"v_mfma_f32_16x16x32_bf16 v[212:215],a[116:119],v[72:75],v[212:215]\n"
"v_mfma_f32_16x16x32_bf16 v[216:219],a[120:123],v[72:75],v[216:219]\n"
"v_exp_f32 v63,v63\n"
"v_mfma_f32_16x16x32_bf16 v[248:251],a[120:123],v[76:79],v[248:251]\n"
"v_cvt_pk_bf16_f32 v95,v62,v63\n"
"v_mfma_f32_16x16x32_bf16 v[152:155],a[120:123],v[64:67],v[152:155]\n"
"v_mfma_f32_16x16x32_bf16 v[184:187],a[120:123],v[68:71],v[184:187]\n"
"v_mfma_f32_16x16x32_bf16 v[188:191],a[124:127],v[68:71],v[188:191]\n"
"v_mfma_f32_16x16x32_bf16 v[220:223],a[124:127],v[72:75],v[220:223]\n"
"v_mfma_f32_16x16x32_bf16 v[252:255],a[124:127],v[76:79],v[252:255]\n"
"v_mfma_f32_16x16x32_bf16 v[156:159],a[124:127],v[64:67],v[156:159]\n"
"s_cbranch_vccnz .Lfk_cs_b3\n"
".Lfk_cr_b3:\n"
"s_waitcnt vmcnt(9) lgkmcnt(0)\n"
"s_barrier\n"
"v_mfma_f32_16x16x32_bf16 v[128:131],a[64:67],v[80:83],v[128:131]\n"
"v_exp_f32 v0,v0\n"
"v_mfma_f32_16x16x32_bf16 v[160:163],a[64:67],v[84:87],v[160:163]\n"
"v_exp_f32 v1,v1\n"
"v_mfma_f32_16x16x32_bf16 v[192:195],a[64:67],v[88:91],v[192:195]\n"
"v_exp_f32 v2,v2\n"
"v_cvt_pk_bf16_f32 v64,v0,v1\n"
"v_mfma_f32_16x16x32_bf16 v[224:227],a[64:67],v[92:95],v[224:227]\n"
"v_exp_f32 v3,v3\n"
"v_mfma_f32_16x16x32_bf16 a[140:143],a[144:147],v[92:95],a[140:143]\n"
"v_exp_f32 v4,v4\n"
"v_cvt_pk_bf16_f32 v65,v2,v3\n"
"v_mfma_f32_16x16x32_bf16 v[228:231],a[68:71],v[92:95],v[228:231]\n"
"v_exp_f32 v5,v5\n"
"v_mfma_f32_16x16x32_bf16 v[132:135],a[68:71],v[80:83],v[132:135]\n"
"v_exp_f32 v6,v6\n"
"v_cvt_pk_bf16_f32 v66,v4,v5\n"
"v_exp_f32 v7,v7\n"
"v_mfma_f32_16x16x32_bf16 v[164:167],a[68:71],v[84:87],v[164:167]\n"
"ds_read_b128 a[96:99],v114 offset:40960\n"
"ds_read_b128 a[100:103],v114 offset:41984\n"
"v_exp_f32 v8,v8\n"
"v_mfma_f32_16x16x32_bf16 v[196:199],a[68:71],v[88:91],v[196:199]\n"
"ds_read_b128 a[104:107],v114 offset:43008\n"
"ds_read_b128 a[108:111],v114 offset:44032\n"
"v_exp_f32 v9,v9\n"
"v_mfma_f32_16x16x32_bf16 a[136:139],a[144:147],v[88:91],a[136:139]\n"
"ds_read_b128 a[112:115],v114 offset:45056\n"
"ds_read_b128 a[116:119],v114 offset:46080\n"
"v_exp_f32 v10,v10\n"
"v_mfma_f32_16x16x32_bf16 v[200:203],a[72:75],v[88:91],v[200:203]\n"
"ds_read_b128 a[120:123],v114 offset:47104\n"
"ds_read_b128 a[124:127],v114 offset:48128\n"
"v_mfma_f32_16x16x32_bf16 v[232:235],a[72:75],v[92:95],v[232:235]\n"
"v_exp_f32 v11,v11\n"
"v_accvgpr_read_b32 v119,a150\n"
"v_accvgpr_read_b32 v120,a151\n"
"v_mfma_f32_16x16x32_bf16 v[136:139],a[72:75],v[80:83],v[136:139]\n"
"v_exp_f32 v12,v12\n"
"v_xor_b32 v119,s80,v119\n"
"v_xor_b32 v120,s81,v120\n"
"v_mfma_f32_16x16x32_bf16 v[168:171],a[72:75],v[84:87],v[168:171]\n"
"v_exp_f32 v13,v13\n"
"v_or_b32 v119,v119,v120\n"
"v_cmp_ne_u32 s[14:15],0,v119\n"
"v_mfma_f32_16x16x32_bf16 a[132:135],a[144:147],v[84:87],a[132:135]\n"
"s_add_u32 s18,s8,1032192\n"
"v_exp_f32 v14,v14\n"
"s_cmp_lg_u32 s14,0\n"
"s_cselect_b32 s18,2146435072,s18\n"
"v_mfma_f32_16x16x32_bf16 v[172:175],a[76:79],v[84:87],v[172:175]\n"
"s_add_u32 m0,s30,0\n"
"v_exp_f32 v15,v15\n"
"buffer_load_dwordx4 v112,s[24:27],s18 offen offset:0 sc1 lds\n"
"buffer_load_dwordx4 v112,s[24:27],s18 offen offset:1024 sc1 lds\n"
"v_exp_f32 v16,v16\n"
"v_mfma_f32_16x16x32_bf16 v[204:207],a[76:79],v[88:91],v[204:207]\n"
"buffer_load_dwordx4 v112,s[24:27],s18 offen offset:2048 sc1 lds\n"
"buffer_load_dwordx4 v112,s[24:27],s18 offen offset:3072 sc1 lds\n"
"v_mfma_f32_16x16x32_bf16 v[236:239],a[76:79],v[92:95],v[236:239]\n"
"s_add_u32 m0,m0,4096\n"
"v_exp_f32 v17,v17\n"
"v_cvt_pk_bf16_f32 v67,v6,v7\n"
"v_mfma_f32_16x16x32_bf16 v[140:143],a[76:79],v[80:83],v[140:143]\n"
"buffer_load_dwordx4 v117,s[24:27],s18 offen offset:0 sc1 lds\n"
"v_exp_f32 v18,v18\n"
"v_cvt_pk_bf16_f32 v68,v8,v9\n"
"v_cvt_pk_bf16_f32 v69,v10,v11\n"
"v_exp_f32 v19,v19\n"
"v_mfma_f32_16x16x32_bf16 a[128:131],a[144:147],v[80:83],a[128:131]\n"
"buffer_load_dwordx4 v117,s[24:27],s18 offen offset:1024 sc1 lds\n"
"v_cvt_pk_bf16_f32 v70,v12,v13\n"
"v_mfma_f32_16x16x32_bf16 v[144:147],a[80:83],v[80:83],v[144:147]\n"
"v_exp_f32 v20,v20\n"
"v_cvt_pk_bf16_f32 v71,v14,v15\n"
"v_cvt_pk_bf16_f32 v72,v16,v17\n"
"v_exp_f32 v21,v21\n"
"v_mfma_f32_16x16x32_bf16 v[176:179],a[80:83],v[84:87],v[176:179]\n"
"buffer_load_dwordx4 v117,s[24:27],s18 offen offset:2048 sc1 lds\n"
"v_cvt_pk_bf16_f32 v73,v18,v19\n"
"v_mfma_f32_16x16x32_bf16 v[208:211],a[80:83],v[88:91],v[208:211]\n"
"v_exp_f32 v22,v22\n"
"v_cvt_pk_bf16_f32 v74,v20,v21\n"
"v_exp_f32 v23,v23\n"
"v_mfma_f32_16x16x32_bf16 v[240:243],a[80:83],v[92:95],v[240:243]\n"
"buffer_load_dwordx4 v117,s[24:27],s18 offen offset:3072 sc1 lds\n"
"v_mfma_f32_16x16x32_bf16 v[244:247],a[84:87],v[92:95],v[244:247]\n"
"s_sub_u32 s82,s3,65536\n"
"v_exp_f32 v24,v24\n"
"v_cvt_pk_bf16_f32 v75,v22,v23\n"
"v_exp_f32 v25,v25\n"
"v_mfma_f32_16x16x32_bf16 v[148:151],a[84:87],v[80:83],v[148:151]\n"
"buffer_load_dwordx4 v[32:35],v112,s[24:27],s82 offen offset:0 sc1\n"
"v_mfma_f32_16x16x32_bf16 v[180:183],a[84:87],v[84:87],v[180:183]\n"
"v_exp_f32 v26,v26\n"
"v_cvt_pk_bf16_f32 v76,v24,v25\n"
"v_exp_f32 v27,v27\n"
"v_mfma_f32_16x16x32_bf16 v[212:215],a[84:87],v[88:91],v[212:215]\n"
"buffer_load_dwordx4 v[36:39],v112,s[24:27],s82 offen offset:1024 sc1\n"
"v_mfma_f32_16x16x32_bf16 v[216:219],a[88:91],v[88:91],v[216:219]\n"
"v_exp_f32 v28,v28\n"
"v_exp_f32 v29,v29\n"
"v_mfma_f32_16x16x32_bf16 v[248:251],a[88:91],v[92:95],v[248:251]\n"
"buffer_load_dwordx4 v[40:43],v112,s[24:27],s82 offen offset:2048 sc1\n"
"v_mfma_f32_16x16x32_bf16 v[152:155],a[88:91],v[80:83],v[152:155]\n"
"v_exp_f32 v30,v30\n"
"v_mfma_f32_16x16x32_bf16 v[184:187],a[88:91],v[84:87],v[184:187]\n"
"v_exp_f32 v31,v31\n"
"v_mfma_f32_16x16x32_bf16 v[188:191],a[92:95],v[84:87],v[188:191]\n"
"buffer_load_dwordx4 v[44:47],v112,s[24:27],s82 offen offset:3072 sc1\n"
"v_mfma_f32_16x16x32_bf16 v[220:223],a[92:95],v[88:91],v[220:223]\n"
"v_cvt_pk_bf16_f32 v78,v28,v29\n"
"v_cvt_pk_bf16_f32 v77,v26,v27\n"
"v_mfma_f32_16x16x32_bf16 v[252:255],a[92:95],v[92:95],v[252:255]\n"
"v_cvt_pk_bf16_f32 v79,v30,v31\n"
"v_mfma_f32_16x16x32_bf16 v[156:159],a[92:95],v[80:83],v[156:159]\n"
"s_load_dwordx2 s[36:37],s[0:1],0\n"
"s_load_dwordx2 s[44:45],s[0:1],80\n"
"s_load_dwordx2 s[48:49],s[0:1],88\n"
"s_lshl_b32 s82,s78,2\n"
"s_add_u32 s82,s82,65536\n"
"v_add_u32 v113,s82,v112\n"
"s_lshr_b32 s82,s78,2\n"
"s_add_u32 s82,s82,122880\n"
"v_lshrrev_b32 v115,1,v112\n"
"v_add_u32 v116,s82,v115\n"
"s_waitcnt lgkmcnt(0)\n"
"s_barrier\n"
"v_mfma_f32_16x16x32_bf16 v[128:131],a[96:99],v[64:67],v[128:131]\n"
"s_add_u32 s83,s18,2097152\n"
"s_add_u32 m0,s30,32768\n"
"v_mfma_f32_16x16x32_bf16 a[128:131],a[144:147],v[64:67],a[128:131]\n"
"buffer_load_dwordx4 v112,s[24:27],s83 offen offset:0 sc1 lds\n"
"v_mfma_f32_16x16x32_bf16 v[132:135],a[100:103],v[64:67],v[132:135]\n"
"buffer_load_dwordx4 v112,s[24:27],s83 offen offset:1024 sc1 lds\n"
"v_mfma_f32_16x16x32_bf16 v[136:139],a[104:107],v[64:67],v[136:139]\n"
"buffer_load_dwordx4 v112,s[24:27],s83 offen offset:2048 sc1 lds\n"
"v_mfma_f32_16x16x32_bf16 v[140:143],a[108:111],v[64:67],v[140:143]\n"
"v_mfma_f32_16x16x32_bf16 v[144:147],a[112:115],v[64:67],v[144:147]\n"
"buffer_load_dwordx4 v112,s[24:27],s83 offen offset:3072 sc1 lds\n"
"v_mfma_f32_16x16x32_bf16 v[148:151],a[116:119],v[64:67],v[148:151]\n"
"s_add_u32 m0,m0,4096\n"
"v_mfma_f32_16x16x32_bf16 v[152:155],a[120:123],v[64:67],v[152:155]\n"
"v_mfma_f32_16x16x32_bf16 v[156:159],a[124:127],v[64:67],v[156:159]\n"
"buffer_load_dwordx4 v117,s[24:27],s83 offen offset:0 sc1 lds\n"
"v_mfma_f32_16x16x32_bf16 v[160:163],a[96:99],v[68:71],v[160:163]\n"
"v_accvgpr_read_b32 v97,a128\n"
"v_cvt_pk_bf16_f32 v136,v136,v137\n"
"v_mfma_f32_16x16x32_bf16 a[132:135],a[144:147],v[68:71],a[132:135]\n"
"buffer_load_dwordx4 v117,s[24:27],s83 offen offset:1024 sc1 lds\n"
"v_mfma_f32_16x16x32_bf16 v[164:167],a[100:103],v[68:71],v[164:167]\n"
"v_cvt_pk_bf16_f32 v128,v128,v129\n"
"v_cvt_pk_bf16_f32 v129,v130,v131\n"
"v_mfma_f32_16x16x32_bf16 v[168:171],a[104:107],v[68:71],v[168:171]\n"
"buffer_load_dwordx4 v117,s[24:27],s83 offen offset:2048 sc1 lds\n"
"v_cvt_pk_bf16_f32 v130,v132,v133\n"
"v_cvt_pk_bf16_f32 v131,v134,v135\n"
"v_mfma_f32_16x16x32_bf16 v[172:175],a[108:111],v[68:71],v[172:175]\n"
"ds_write_b128 v113,v[128:131] offset:0\n"
"v_cvt_pk_bf16_f32 v137,v138,v139\n"
"v_mfma_f32_16x16x32_bf16 v[176:179],a[112:115],v[68:71],v[176:179]\n"
"buffer_load_dwordx4 v117,s[24:27],s83 offen offset:3072 sc1 lds\n"
"v_cvt_pk_bf16_f32 v138,v140,v141\n"
"v_cvt_pk_bf16_f32 v139,v142,v143\n"
"v_mfma_f32_16x16x32_bf16 v[180:183],a[116:119],v[68:71],v[180:183]\n"
"s_sub_u32 s82,s3,81920\n"
"ds_write_b128 v113,v[136:139] offset:1024\n"
"v_cvt_pk_bf16_f32 v144,v144,v145\n"
"v_mfma_f32_16x16x32_bf16 v[184:187],a[120:123],v[68:71],v[184:187]\n"
"buffer_load_dwordx4 a[32:35],v112,s[24:27],s82 offen offset:0 sc1\n"
"v_cvt_pk_bf16_f32 v145,v146,v147\n"
"v_cvt_pk_bf16_f32 v146,v148,v149\n"
"v_cvt_pk_bf16_f32 v147,v150,v151\n"
"v_mfma_f32_16x16x32_bf16 v[188:191],a[124:127],v[68:71],v[188:191]\n"
"ds_write_b128 v113,v[144:147] offset:2048\n"
"v_cvt_pk_bf16_f32 v152,v152,v153\n"
"v_mfma_f32_16x16x32_bf16 v[192:195],a[96:99],v[72:75],v[192:195]\n"
"buffer_load_dwordx4 a[36:39],v112,s[24:27],s82 offen offset:1024 sc1\n"
"v_cvt_pk_bf16_f32 v153,v154,v155\n"
"v_cvt_pk_bf16_f32 v154,v156,v157\n"
"v_cvt_pk_bf16_f32 v155,v158,v159\n"
"v_mfma_f32_16x16x32_bf16 a[136:139],a[144:147],v[72:75],a[136:139]\n"
"ds_write_b128 v113,v[152:155] offset:3072\n"
"ds_write_b64 v116,v[96:97] offset:0\n"
"v_mfma_f32_16x16x32_bf16 v[196:199],a[100:103],v[72:75],v[196:199]\n"
"buffer_load_dwordx4 a[40:43],v112,s[24:27],s82 offen offset:2048 sc1\n"
"v_accvgpr_read_b32 v101,a132\n"
"v_cvt_pk_bf16_f32 v160,v160,v161\n"
"v_mfma_f32_16x16x32_bf16 v[200:203],a[104:107],v[72:75],v[200:203]\n"
"v_cvt_pk_bf16_f32 v161,v162,v163\n"
"v_cvt_pk_bf16_f32 v162,v164,v165\n"
"v_cvt_pk_bf16_f32 v163,v166,v167\n"
"v_mfma_f32_16x16x32_bf16 v[204:207],a[108:111],v[72:75],v[204:207]\n"
"ds_write_b128 v113,v[160:163] offset:4096\n"
"v_cvt_pk_bf16_f32 v168,v168,v169\n"
"v_cvt_pk_bf16_f32 v169,v170,v171\n"
"v_mfma_f32_16x16x32_bf16 v[208:211],a[112:115],v[72:75],v[208:211]\n"
"buffer_load_dwordx4 a[44:47],v112,s[24:27],s82 offen offset:3072 sc1\n"
"v_cvt_pk_bf16_f32 v170,v172,v173\n"
"v_cvt_pk_bf16_f32 v171,v174,v175\n"
"v_mfma_f32_16x16x32_bf16 v[212:215],a[116:119],v[72:75],v[212:215]\n"
"ds_write_b128 v113,v[168:171] offset:5120\n"
"v_cvt_pk_bf16_f32 v176,v176,v177\n"
"v_cvt_pk_bf16_f32 v177,v178,v179\n"
"v_mfma_f32_16x16x32_bf16 v[216:219],a[120:123],v[72:75],v[216:219]\n"
"v_cvt_pk_bf16_f32 v178,v180,v181\n"
"v_cvt_pk_bf16_f32 v179,v182,v183\n"
"v_mfma_f32_16x16x32_bf16 v[220:223],a[124:127],v[72:75],v[220:223]\n"
"ds_write_b128 v113,v[176:179] offset:6144\n"
"v_cvt_pk_bf16_f32 v184,v184,v185\n"
"v_cvt_pk_bf16_f32 v185,v186,v187\n"
"v_cvt_pk_bf16_f32 v186,v188,v189\n"
"v_cvt_pk_bf16_f32 v187,v190,v191\n"
"v_mfma_f32_16x16x32_bf16 v[224:227],a[96:99],v[76:79],v[224:227]\n"
"ds_write_b128 v113,v[184:187] offset:7168\n"
"v_mfma_f32_16x16x32_bf16 a[140:143],a[144:147],v[76:79],a[140:143]\n"
"s_sub_u32 s82,s3,131072\n"
"ds_write_b64 v116,v[100:101] offset:512\n"
"buffer_load_dwordx4 v[128:131],v112,s[24:27],s82 offen offset:0 sc1\n"
"buffer_load_dwordx4 v[132:135],v112,s[24:27],s82 offen offset:1024 sc1\n"
"v_mfma_f32_16x16x32_bf16 v[228:231],a[100:103],v[76:79],v[228:231]\n"
"buffer_load_dwordx4 v[136:139],v112,s[24:27],s82 offen offset:2048 sc1\n"
"buffer_load_dwordx4 v[140:143],v112,s[24:27],s82 offen offset:3072 sc1\n"
"s_sub_u32 s82,s3,147456\n"
"v_mfma_f32_16x16x32_bf16 v[232:235],a[104:107],v[76:79],v[232:235]\n"
"buffer_load_dwordx4 v[144:147],v112,s[24:27],s82 offen offset:0 sc1\n"
"buffer_load_dwordx4 v[148:151],v112,s[24:27],s82 offen offset:1024 sc1\n"
"buffer_load_dwordx4 v[152:155],v112,s[24:27],s82 offen offset:2048 sc1\n"
"v_mfma_f32_16x16x32_bf16 v[236:239],a[108:111],v[76:79],v[236:239]\n"
"buffer_load_dwordx4 v[156:159],v112,s[24:27],s82 offen offset:3072 sc1\n"
"v_mfma_f32_16x16x32_bf16 v[240:243],a[112:115],v[76:79],v[240:243]\n"
"v_mfma_f32_16x16x32_bf16 v[244:247],a[116:119],v[76:79],v[244:247]\n"
"v_mfma_f32_16x16x32_bf16 v[248:251],a[120:123],v[76:79],v[248:251]\n"
"v_mfma_f32_16x16x32_bf16 v[252:255],a[124:127],v[76:79],v[252:255]\n"
".Lfk_endB:\n"
"v_lshrrev_b32 v70,4,v112\n"
"v_and_b32 v71,15,v70\n"
"v_lshrrev_b32 v70,4,v70\n"
"v_lshlrev_b32 v70,2,v70\n"
"v_sub_u32 v70,v71,v70\n"
"v_mov_b32 v71,0x3f80\n"
"v_cmp_eq_u32 vcc,0,v70\n"
"v_mov_b32 v68,0\n"
"v_mov_b32 v64,0\n"
"v_cndmask_b32 v66,0,v71,vcc\n"
"v_cmp_eq_u32 vcc,1,v70\n"
"v_mov_b32 v65,0\n"
"v_mov_b32 v72,0x3f800000\n"
"v_cndmask_b32 v66,v66,v72,vcc\n"
"v_cmp_eq_u32 vcc,2,v70\n"
"v_mov_b32 v69,0\n"
"s_nop 0\n"
"v_cndmask_b32 v67,0,v71,vcc\n"
"v_cmp_eq_u32 vcc,3,v70\n"
"s_nop 1\n"
"v_cndmask_b32 v67,v67,v72,vcc\n"
"s_sub_u32 s82,s3,163840\n"
"buffer_load_dwordx4 v[0:3],v112,s[24:27],s82 offen offset:0 sc1\n"
"buffer_load_dwordx4 v[4:7],v112,s[24:27],s82 offen offset:1024 sc1\n"
"buffer_load_dwordx4 v[8:11],v112,s[24:27],s82 offen offset:2048 sc1\n"
"buffer_load_dwordx4 v[12:15],v112,s[24:27],s82 offen offset:3072 sc1\n"
"s_sub_u32 s82,s3,180224\n"
"buffer_load_dwordx4 v[160:163],v112,s[24:27],s82 offen offset:0 sc1\n"
"buffer_load_dwordx4 v[164:167],v112,s[24:27],s82 offen offset:1024 sc1\n"
"buffer_load_dwordx4 v[168:171],v112,s[24:27],s82 offen offset:2048 sc1\n"
"buffer_load_dwordx4 v[172:175],v112,s[24:27],s82 offen offset:3072 sc1\n"
"s_sub_u32 s82,s3,196608\n"
"buffer_load_dwordx4 v[176:179],v112,s[24:27],s82 offen offset:0 sc1\n"
"buffer_load_dwordx4 v[180:183],v112,s[24:27],s82 offen offset:1024 sc1\n"
"buffer_load_dwordx4 v[184:187],v112,s[24:27],s82 offen offset:2048 sc1\n"
"buffer_load_dwordx4 v[188:191],v112,s[24:27],s82 offen offset:3072 sc1\n"
"s_waitcnt lgkmcnt(0)\n"
"s_barrier\n"
"v_xor_b32 v113,32768,v113\n"
"v_xor_b32 v116,2048,v116\n"
"ds_read_b64 v[98:99],v116 offset:0\n"
"ds_read_b128 a[64:67],v113 offset:0\n"
"ds_read_b128 a[68:71],v113 offset:1024\n"
"ds_read_b128 a[72:75],v113 offset:2048\n"
"ds_read_b128 a[76:79],v113 offset:3072\n"
"ds_read_b64 v[102:103],v116 offset:512\n"
"ds_read_b128 a[80:83],v113 offset:4096\n"
"ds_read_b128 a[84:87],v113 offset:5120\n"
"ds_read_b128 a[88:91],v113 offset:6144\n"
"ds_read_b128 a[92:95],v113 offset:7168\n"
"s_waitcnt lgkmcnt(5)\n"
"v_cmp_neq_f32 vcc,v98,v104\n"
"s_nop 0\n"
"s_cbranch_vccnz .Lfk_ms_b0\n"
".Lfk_mr_b0:\n"
"v_mfma_f32_16x16x32_bf16 v[192:195],v[66:69],a[64:67],v[192:195]\n"
"v_mfma_f32_16x16x32_bf16 v[196:199],v[64:67],a[64:67],v[196:199]\n"
"v_mfma_f32_16x16x32_bf16 v[200:203],v[66:69],a[68:71],v[200:203]\n"
"v_mfma_f32_16x16x32_bf16 v[204:207],v[64:67],a[68:71],v[204:207]\n"
"v_mfma_f32_16x16x32_bf16 v[208:211],v[66:69],a[72:75],v[208:211]\n"
"v_mfma_f32_16x16x32_bf16 v[212:215],v[64:67],a[72:75],v[212:215]\n"
"v_mfma_f32_16x16x32_bf16 v[216:219],v[66:69],a[76:79],v[216:219]\n"
"v_mfma_f32_16x16x32_bf16 v[220:223],v[64:67],a[76:79],v[220:223]\n"
"s_waitcnt lgkmcnt(0)\n"
"v_cmp_neq_f32 vcc,v102,v108\n"
"s_nop 0\n"
"s_cbranch_vccnz .Lfk_ms_b1\n"
".Lfk_mr_b1:\n"
"v_mfma_f32_16x16x32_bf16 v[224:227],v[66:69],a[80:83],v[224:227]\n"
"v_mfma_f32_16x16x32_bf16 v[228:231],v[64:67],a[80:83],v[228:231]\n"
"v_accvgpr_read_b32 v122,a136\n"
"v_add_f32 v122,v122,v99\n"
"v_mfma_f32_16x16x32_bf16 v[232:235],v[66:69],a[84:87],v[232:235]\n"
"v_rcp_f32 v122,v122\n"
"v_mfma_f32_16x16x32_bf16 v[236:239],v[64:67],a[84:87],v[236:239]\n"
"v_pk_mul_f32 v[192:193],v[192:193],v[122:123] op_sel_hi:[1,0]\n"
"v_pk_mul_f32 v[194:195],v[194:195],v[122:123] op_sel_hi:[1,0]\n"
"v_pk_mul_f32 v[196:197],v[196:197],v[122:123] op_sel_hi:[1,0]\n"
"v_pk_mul_f32 v[198:199],v[198:199],v[122:123] op_sel_hi:[1,0]\n"
"v_mfma_f32_16x16x32_bf16 v[240:243],v[66:69],a[88:91],v[240:243]\n"
"v_cvt_pk_bf16_f32 v192,v192,v193\n"
"v_cvt_pk_bf16_f32 v193,v194,v195\n"
"v_mfma_f32_16x16x32_bf16 v[244:247],v[64:67],a[88:91],v[244:247]\n"
"v_cvt_pk_bf16_f32 v194,v196,v197\n"
"v_cvt_pk_bf16_f32 v195,v198,v199\n"
"v_mfma_f32_16x16x32_bf16 v[248:251],v[66:69],a[92:95],v[248:251]\n"
"buffer_store_dwordx4 v[192:195],v112,s[24:27],s70 offen offset:0 sc1\n"
"v_pk_mul_f32 v[200:201],v[200:201],v[122:123] op_sel_hi:[1,0]\n"
"v_pk_mul_f32 v[202:203],v[202:203],v[122:123] op_sel_hi:[1,0]\n"
"v_pk_mul_f32 v[204:205],v[204:205],v[122:123] op_sel_hi:[1,0]\n"
"v_pk_mul_f32 v[206:207],v[206:207],v[122:123] op_sel_hi:[1,0]\n"
"v_mfma_f32_16x16x32_bf16 v[252:255],v[64:67],a[92:95],v[252:255]\n"
"v_cvt_pk_bf16_f32 v200,v200,v201\n"
"v_cvt_pk_bf16_f32 v201,v202,v203\n"
"v_cvt_pk_bf16_f32 v202,v204,v205\n"
"v_cvt_pk_bf16_f32 v203,v206,v207\n"
"v_pk_mul_f32 v[208:209],v[208:209],v[122:123] op_sel_hi:[1,0]\n"
"v_pk_mul_f32 v[210:211],v[210:211],v[122:123] op_sel_hi:[1,0]\n"
"v_pk_mul_f32 v[212:213],v[212:213],v[122:123] op_sel_hi:[1,0]\n"
"v_pk_mul_f32 v[214:215],v[214:215],v[122:123] op_sel_hi:[1,0]\n"
"v_cvt_pk_bf16_f32 v208,v208,v209\n"
"v_cvt_pk_bf16_f32 v209,v210,v211\n"
"v_cvt_pk_bf16_f32 v210,v212,v213\n"
"v_cvt_pk_bf16_f32 v211,v214,v215\n"
"buffer_store_dwordx4 v[200:203],v112,s[24:27],s70 offen offset:1024 sc1\n"
"buffer_store_dwordx4 v[208:211],v112,s[24:27],s70 offen offset:2048 sc1\n"
"v_pk_mul_f32 v[216:217],v[216:217],v[122:123] op_sel_hi:[1,0]\n"
"v_pk_mul_f32 v[218:219],v[218:219],v[122:123] op_sel_hi:[1,0]\n"
"v_pk_mul_f32 v[220:221],v[220:221],v[122:123] op_sel_hi:[1,0]\n"
"v_pk_mul_f32 v[222:223],v[222:223],v[122:123] op_sel_hi:[1,0]\n"
"v_cvt_pk_bf16_f32 v216,v216,v217\n"
"v_cvt_pk_bf16_f32 v217,v218,v219\n"
"v_cvt_pk_bf16_f32 v218,v220,v221\n"
"v_cvt_pk_bf16_f32 v219,v222,v223\n"
"buffer_store_dwordx4 v[216:219],v112,s[24:27],s70 offen offset:3072 sc1\n"
"s_add_u32 s70,s70,262144\n"
"s_branch .Lfk_p1\n"
".Lfk_tailB7:\n"
"s_waitcnt vmcnt(11) lgkmcnt(0)\n"
"s_barrier\n"
"v_mfma_f32_16x16x32_bf16 v[0:3],a[64:67],a[0:3],v[96:99]\n"
"ds_read_b128 a[96:99],v114 offset:24576\n"
"ds_read_b128 a[100:103],v114 offset:25600\n"
"ds_read_b128 a[104:107],v114 offset:26624\n"
"v_mfma_f32_16x16x32_bf16 v[0:3],a[68:71],a[4:7],v[0:3]\n"
"ds_read_b128 a[108:111],v114 offset:27648\n"
"ds_read_b128 a[112:115],v114 offset:28672\n"
"ds_read_b128 a[116:119],v114 offset:29696\n"
"v_mfma_f32_16x16x32_bf16 v[0:3],a[72:75],a[8:11],v[0:3]\n"
"ds_read_b128 a[120:123],v114 offset:30720\n"
"ds_read_b128 a[124:127],v114 offset:31744\n"
"v_add_f32 v16,0x80000000,v126\n"
"v_mfma_f32_16x16x32_bf16 v[0:3],a[76:79],a[12:15],v[0:3]\n"
"v_min_f32 v16,v16,v104\n"
"v_add_f32 v17,0xf149f2ca,v126\n"
"v_min_f32 v17,v17,v105\n"
"v_mfma_f32_16x16x32_bf16 v[4:7],a[92:95],a[12:15],v[96:99]\n"
"v_add_f32 v18,0xf1c9f2ca,v126\n"
"v_min_f32 v18,v18,v106\n"
"v_add_f32 v19,0xf2177617,v126\n"
"v_mfma_f32_16x16x32_bf16 v[4:7],a[88:91],a[8:11],v[4:7]\n"
"v_min_f32 v19,v19,v107\n"
"v_add_f32 v20,0xf349f2ca,v126\n"
"v_mfma_f32_16x16x32_bf16 v[4:7],a[84:87],a[4:7],v[4:7]\n"
"v_add_f32 v21,0xf35691f6,v126\n"
"v_min_f32 v21,v21,v105\n"
"v_add_f32 v22,0xf3633123,v126\n"
"v_mfma_f32_16x16x32_bf16 v[4:7],a[80:83],a[0:3],v[4:7]\n"
"s_waitcnt lgkmcnt(0)\n"
"v_min_f32 v22,v22,v106\n"
"v_add_f32 v23,0xf36fd050,v126\n"
"v_mfma_f32_16x16x32_bf16 v[192:195],a[96:99],v[72:75],v[192:195]\n"
"v_exp_f32 v56,v56\n"
"v_mfma_f32_16x16x32_bf16 a[136:139],a[144:147],v[72:75],a[136:139]\n"
"v_exp_f32 v57,v57\n"
"v_mfma_f32_16x16x32_bf16 v[196:199],a[100:103],v[72:75],v[196:199]\n"
"v_min_f32 v20,v20,v104\n"
"v_cvt_pk_bf16_f32 v92,v56,v57\n"
"v_mfma_f32_16x16x32_bf16 v[200:203],a[104:107],v[72:75],v[200:203]\n"
"v_max3_f32 v118,v0,v1,v2\n"
"v_max3_f32 v118,v118,v3,v4\n"
"v_mfma_f32_16x16x32_bf16 v[204:207],a[108:111],v[72:75],v[204:207]\n"
"v_max3_f32 v118,v118,v5,v6\n"
"v_max_f32 v118,v118,v7\n"
"v_mfma_f32_16x16x32_bf16 v[208:211],a[112:115],v[72:75],v[208:211]\n"
"v_min_f32 v23,v23,v107\n"
"v_mov_b32 v120,v118\n"
"v_mfma_f32_16x16x32_bf16 v[212:215],a[116:119],v[72:75],v[212:215]\n"
"v_exp_f32 v58,v58\n"
"v_mfma_f32_16x16x32_bf16 v[216:219],a[120:123],v[72:75],v[216:219]\n"
"v_mfma_f32_16x16x32_bf16 v[220:223],a[124:127],v[72:75],v[220:223]\n"
"v_exp_f32 v59,v59\n"
"v_mfma_f32_16x16x32_bf16 v[224:227],a[96:99],v[76:79],v[224:227]\n"
"v_cvt_pk_bf16_f32 v93,v58,v59\n"
"v_mfma_f32_16x16x32_bf16 a[140:143],a[144:147],v[76:79],a[140:143]\n"
"v_accvgpr_read_b32 v122,a136\n"
"v_mfma_f32_16x16x32_bf16 v[228:231],a[100:103],v[76:79],v[228:231]\n"
"v_rcp_f32 v122,v122\n"
"v_mfma_f32_16x16x32_bf16 v[232:235],a[104:107],v[76:79],v[232:235]\n"
"v_pk_mul_f32 v[192:193],v[192:193],v[122:123] op_sel_hi:[1,0]\n"
"v_pk_mul_f32 v[194:195],v[194:195],v[122:123] op_sel_hi:[1,0]\n"
"v_pk_mul_f32 v[196:197],v[196:197],v[122:123] op_sel_hi:[1,0]\n"
"v_pk_mul_f32 v[198:199],v[198:199],v[122:123] op_sel_hi:[1,0]\n"
"v_mfma_f32_16x16x32_bf16 v[236:239],a[108:111],v[76:79],v[236:239]\n"
"v_cvt_pk_bf16_f32 v192,v192,v193\n"
"v_cvt_pk_bf16_f32 v193,v194,v195\n"
"v_mfma_f32_16x16x32_bf16 v[240:243],a[112:115],v[76:79],v[240:243]\n"
"v_cvt_pk_bf16_f32 v194,v196,v197\n"
"v_cvt_pk_bf16_f32 v195,v198,v199\n"
"v_mfma_f32_16x16x32_bf16 v[244:247],a[116:119],v[76:79],v[244:247]\n"
"buffer_store_dwordx4 v[192:195],v112,s[24:27],s70 offen offset:0 sc1\n"
"v_pk_mul_f32 v[200:201],v[200:201],v[122:123] op_sel_hi:[1,0]\n"
"v_pk_mul_f32 v[202:203],v[202:203],v[122:123] op_sel_hi:[1,0]\n"
"v_mfma_f32_16x16x32_bf16 v[248:251],a[120:123],v[76:79],v[248:251]\n"
"v_exp_f32 v60,v60\n"
"v_cvt_pk_bf16_f32 v200,v200,v201\n"
"v_cvt_pk_bf16_f32 v201,v202,v203\n"
"v_pk_mul_f32 v[204:205],v[204:205],v[122:123] op_sel_hi:[1,0]\n"
"v_pk_mul_f32 v[206:207],v[206:207],v[122:123] op_sel_hi:[1,0]\n"
"v_mfma_f32_16x16x32_bf16 v[252:255],a[124:127],v[76:79],v[252:255]\n"
"v_cvt_pk_bf16_f32 v202,v204,v205\n"
"v_cvt_pk_bf16_f32 v203,v206,v207\n"
"v_mfma_f32_16x16x32_bf16 v[12:15],a[80:83],a[16:19],v[100:103]\n"
"buffer_store_dwordx4 v[200:203],v112,s[24:27],s70 offen offset:1024 sc1\n"
"v_pk_mul_f32 v[208:209],v[208:209],v[122:123] op_sel_hi:[1,0]\n"
"v_pk_mul_f32 v[210:211],v[210:211],v[122:123] op_sel_hi:[1,0]\n"
"v_pk_mul_f32 v[212:213],v[212:213],v[122:123] op_sel_hi:[1,0]\n"
"v_pk_mul_f32 v[214:215],v[214:215],v[122:123] op_sel_hi:[1,0]\n"
"v_mfma_f32_16x16x32_bf16 v[12:15],a[84:87],a[20:23],v[12:15]\n"
"v_exp_f32 v61,v61\n"
"v_mfma_f32_16x16x32_bf16 v[12:15],a[88:91],a[24:27],v[12:15]\n"
"v_cvt_pk_bf16_f32 v94,v60,v61\n"
"v_cvt_pk_bf16_f32 v208,v208,v209\n"
"v_mfma_f32_16x16x32_bf16 v[12:15],a[92:95],a[28:31],v[12:15]\n"
"v_cvt_pk_bf16_f32 v209,v210,v211\n"
"v_cvt_pk_bf16_f32 v210,v212,v213\n"
"v_cvt_pk_bf16_f32 v211,v214,v215\n"
"v_mfma_f32_16x16x32_bf16 v[8:11],a[76:79],a[28:31],v[100:103]\n"
"buffer_store_dwordx4 v[208:211],v112,s[24:27],s70 offen offset:2048 sc1\n"
"v_pk_mul_f32 v[216:217],v[216:217],v[122:123] op_sel_hi:[1,0]\n"
"v_pk_mul_f32 v[218:219],v[218:219],v[122:123] op_sel_hi:[1,0]\n"
"v_mfma_f32_16x16x32_bf16 v[8:11],a[72:75],a[24:27],v[8:11]\n"
"v_cvt_pk_bf16_f32 v216,v216,v217\n"
"v_cvt_pk_bf16_f32 v217,v218,v219\n"
"v_pk_mul_f32 v[220:221],v[220:221],v[122:123] op_sel_hi:[1,0]\n"
"v_pk_mul_f32 v[222:223],v[222:223],v[122:123] op_sel_hi:[1,0]\n"
"v_mfma_f32_16x16x32_bf16 v[8:11],a[68:71],a[20:23],v[8:11]\n"
"v_cvt_pk_bf16_f32 v218,v220,v221\n"
"v_cvt_pk_bf16_f32 v219,v222,v223\n"
"v_mfma_f32_16x16x32_bf16 v[8:11],a[64:67],a[16:19],v[8:11]\n"
"buffer_store_dwordx4 v[216:219],v112,s[24:27],s70 offen offset:3072 sc1\n"
"v_mfma_f32_16x16x32_bf16 v[24:27],a[64:67],a[48:51],v[108:111]\n"
"s_add_u32 s70,s70,262144\n"
"v_exp_f32 v62,v62\n"
"v_mfma_f32_16x16x32_bf16 v[24:27],a[68:71],a[52:55],v[24:27]\n"
"v_exp_f32 v63,v63\n"
"v_mfma_f32_16x16x32_bf16 v[24:27],a[72:75],a[56:59],v[24:27]\n"
"s_nop 0\n"
"v_max3_f32 v119,v8,v9,v10\n"
"v_mfma_f32_16x16x32_bf16 v[24:27],a[76:79],a[60:63],v[24:27]\n"
"v_max3_f32 v119,v119,v11,v12\n"
"v_max3_f32 v119,v119,v13,v14\n"
"v_mfma_f32_16x16x32_bf16 v[28:31],a[92:95],a[60:63],v[108:111]\n"
"v_max_f32 v119,v119,v15\n"
"v_mfma_f32_16x16x32_bf16 v[28:31],a[88:91],a[56:59],v[28:31]\n"
"v_min_f32 v120,v120,v119\n"
"v_max_f32 v118,v118,v119\n"
"v_mfma_f32_16x16x32_bf16 v[28:31],a[84:87],a[52:55],v[28:31]\n"
"v_mfma_f32_16x16x32_bf16 v[28:31],a[80:83],a[48:51],v[28:31]\n"
"v_cvt_pk_bf16_f32 v95,v62,v63\n"
"v_mfma_f32_16x16x32_bf16 v[20:23],a[80:83],a[32:35],v[20:23]\n"
"v_mfma_f32_16x16x32_bf16 v[20:23],a[84:87],a[36:39],v[20:23]\n"
"v_mfma_f32_16x16x32_bf16 v[20:23],a[88:91],a[40:43],v[20:23]\n"
"v_max3_f32 v119,v24,v25,v26\n"
"v_mfma_f32_16x16x32_bf16 v[20:23],a[92:95],a[44:47],v[20:23]\n"
"s_nop 1\n"
"v_max3_f32 v119,v119,v27,v28\n"
"v_mfma_f32_16x16x32_bf16 v[16:19],a[76:79],a[44:47],v[16:19]\n"
"v_max3_f32 v119,v119,v29,v30\n"
"v_max_f32 v119,v119,v31\n"
"v_mfma_f32_16x16x32_bf16 v[16:19],a[72:75],a[40:43],v[16:19]\n"
"v_mfma_f32_16x16x32_bf16 v[16:19],a[68:71],a[36:39],v[16:19]\n"
"v_mfma_f32_16x16x32_bf16 v[16:19],a[64:67],a[32:35],v[16:19]\n"
"ds_read_b128 a[64:67],v114 offset:57344\n"
"ds_read_b128 a[68:71],v114 offset:58368\n"
"ds_read_b128 a[72:75],v114 offset:59392\n"
"v_max_f32 v118,v118,v119\n"
"v_min_f32 v120,v120,v119\n"
"v_cmp_gt_f32 s[96:97],s16,v120\n"
"s_nop 1\n"
"v_max3_f32 v119,v16,v17,v18\n"
"v_max3_f32 v119,v119,v19,v20\n"
"v_max3_f32 v119,v119,v21,v22\n"
"v_max_f32 v119,v119,v23\n"
"v_cmp_gt_f32 s[28:29],s16,v119\n"
"v_max_f32 v118,v118,v119\n"
"v_cmp_lt_f32 vcc,s93,v118\n"
"ds_read_b128 a[76:79],v114 offset:60416\n"
"ds_read_b128 a[80:83],v114 offset:61440\n"
"ds_read_b128 a[84:87],v114 offset:62464\n"
"ds_read_b128 a[88:91],v114 offset:63488\n"
"ds_read_b128 a[92:95],v114 offset:64512\n"
"v_mov_b32 v114,v112\n"
"s_sub_u32 s82,s3,32768\n"
"buffer_load_dwordx4 a[0:3],v112,s[24:27],s82 offen offset:0 sc1\n"
"buffer_load_dwordx4 a[4:7],v112,s[24:27],s82 offen offset:1024 sc1\n"
"buffer_load_dwordx4 a[8:11],v112,s[24:27],s82 offen offset:2048 sc1\n"
"buffer_load_dwordx4 a[12:15],v112,s[24:27],s82 offen offset:3072 sc1\n"
"s_sub_u32 s82,s3,49152\n"
"buffer_load_dwordx4 a[16:19],v112,s[24:27],s82 offen offset:0 sc1\n"
"buffer_load_dwordx4 a[20:23],v112,s[24:27],s82 offen offset:1024 sc1\n"
"buffer_load_dwordx4 a[24:27],v112,s[24:27],s82 offen offset:2048 sc1\n"
"buffer_load_dwordx4 a[28:31],v112,s[24:27],s82 offen offset:3072 sc1\n"
"s_and_b32 s28,s28,0xffff\n"
"s_or_b32 s96,s96,s28\n"
"s_or_b64 s[96:97],s[96:97],vcc\n"
"s_cbranch_scc1 .Lfk_cs_c3\n"
".Lfk_cr_c3:\n"
"s_waitcnt vmcnt(13) lgkmcnt(0)\n"
"s_barrier\n"
"v_mfma_f32_16x16x32_bf16 v[224:227],a[64:67],v[92:95],v[224:227]\n"
"ds_read_b128 a[96:99],v114 offset:40960\n"
"v_exp_f32 v0,v0\n"
"v_mfma_f32_16x16x32_bf16 a[140:143],a[144:147],v[92:95],a[140:143]\n"
"ds_read_b128 a[100:103],v114 offset:41984\n"
"v_exp_f32 v1,v1\n"
"v_mfma_f32_16x16x32_bf16 v[228:231],a[68:71],v[92:95],v[228:231]\n"
"ds_read_b128 a[104:107],v114 offset:43008\n"
"v_exp_f32 v2,v2\n"
"v_mfma_f32_16x16x32_bf16 v[232:235],a[72:75],v[92:95],v[232:235]\n"
"ds_read_b128 a[108:111],v114 offset:44032\n"
"v_exp_f32 v3,v3\n"
"v_mfma_f32_16x16x32_bf16 v[236:239],a[76:79],v[92:95],v[236:239]\n"
"ds_read_b128 a[112:115],v114 offset:45056\n"
"v_exp_f32 v4,v4\n"
"v_mfma_f32_16x16x32_bf16 v[240:243],a[80:83],v[92:95],v[240:243]\n"
"ds_read_b128 a[116:119],v114 offset:46080\n"
"v_exp_f32 v5,v5\n"
"v_mfma_f32_16x16x32_bf16 v[244:247],a[84:87],v[92:95],v[244:247]\n"
"ds_read_b128 a[120:123],v114 offset:47104\n"
"v_exp_f32 v6,v6\n"
"v_mfma_f32_16x16x32_bf16 v[248:251],a[88:91],v[92:95],v[248:251]\n"
"v_exp_f32 v7,v7\n"
"v_mfma_f32_16x16x32_bf16 v[252:255],a[92:95],v[92:95],v[252:255]\n"
"v_exp_f32 v8,v8\n"
"v_accvgpr_read_b32 v119,a150\n"
"v_accvgpr_read_b32 v120,a151\n"
"v_xor_b32 v119,s80,v119\n"
"v_xor_b32 v120,s81,v120\n"
"v_or_b32 v119,v119,v120\n"
"v_cmp_ne_u32 s[14:15],0,v119\n"
"s_add_u32 s18,s8,1032192\n"
"s_cmp_lg_u32 s14,0\n"
"s_cselect_b32 s18,2146435072,s18\n"
"s_add_u32 m0,s30,0\n"
"ds_read_b128 a[124:127],v114 offset:48128\n"
"buffer_load_dwordx4 v112,s[24:27],s18 offen offset:0 sc1 lds\n"
"buffer_load_dwordx4 v112,s[24:27],s18 offen offset:1024 sc1 lds\n"
"buffer_load_dwordx4 v112,s[24:27],s18 offen offset:2048 sc1 lds\n"
"buffer_load_dwordx4 v112,s[24:27],s18 offen offset:3072 sc1 lds\n"
"s_add_u32 m0,m0,4096\n"
"s_nop 0\n"
"buffer_load_dwordx4 v117,s[24:27],s18 offen offset:0 sc1 lds\n"
"buffer_load_dwordx4 v117,s[24:27],s18 offen offset:1024 sc1 lds\n"
"buffer_load_dwordx4 v117,s[24:27],s18 offen offset:2048 sc1 lds\n"
"buffer_load_dwordx4 v117,s[24:27],s18 offen offset:3072 sc1 lds\n"
"s_sub_u32 s82,s3,65536\n"
"buffer_load_dwordx4 v[32:35],v112,s[24:27],s82 offen offset:0 sc1\n"
"buffer_load_dwordx4 v[36:39],v112,s[24:27],s82 offen offset:1024 sc1\n"
"buffer_load_dwordx4 v[40:43],v112,s[24:27],s82 offen offset:2048 sc1\n"
"buffer_load_dwordx4 v[44:47],v112,s[24:27],s82 offen offset:3072 sc1\n"
"v_exp_f32 v9,v9\n"
"v_cvt_pk_bf16_f32 v64,v0,v1\n"
"v_exp_f32 v10,v10\n"
"v_cvt_pk_bf16_f32 v65,v2,v3\n"
"v_exp_f32 v11,v11\n"
"v_cvt_pk_bf16_f32 v66,v4,v5\n"
"v_exp_f32 v12,v12\n"
"v_cvt_pk_bf16_f32 v67,v6,v7\n"
"v_exp_f32 v13,v13\n"
"v_cvt_pk_bf16_f32 v68,v8,v9\n"
"v_exp_f32 v14,v14\n"
"v_cvt_pk_bf16_f32 v69,v10,v11\n"
"v_exp_f32 v15,v15\n"
"v_cvt_pk_bf16_f32 v70,v12,v13\n"
"v_exp_f32 v16,v16\n"
"v_cvt_pk_bf16_f32 v71,v14,v15\n"
"v_exp_f32 v17,v17\n"
"v_exp_f32 v18,v18\n"
"v_cvt_pk_bf16_f32 v72,v16,v17\n"
"v_exp_f32 v19,v19\n"
"v_exp_f32 v20,v20\n"
"v_cvt_pk_bf16_f32 v73,v18,v19\n"
"v_exp_f32 v21,v21\n"
"v_exp_f32 v22,v22\n"
"v_cvt_pk_bf16_f32 v74,v20,v21\n"
"v_exp_f32 v23,v23\n"
"v_exp_f32 v24,v24\n"
"v_cvt_pk_bf16_f32 v75,v22,v23\n"
"v_exp_f32 v25,v25\n"
"v_exp_f32 v26,v26\n"
"v_cvt_pk_bf16_f32 v76,v24,v25\n"
"v_exp_f32 v27,v27\n"
"v_exp_f32 v28,v28\n"
"v_cvt_pk_bf16_f32 v77,v26,v27\n"
"v_exp_f32 v29,v29\n"
"v_exp_f32 v30,v30\n"
"v_cvt_pk_bf16_f32 v78,v28,v29\n"
"v_exp_f32 v31,v31\n"
"s_nop 0\n"
"v_cvt_pk_bf16_f32 v79,v30,v31\n"
"v_accvgpr_read_b32 v122,a140\n"
"v_rcp_f32 v122,v122\n"
"s_nop 0\n"
"v_pk_mul_f32 v[224:225],v[224:225],v[122:123] op_sel_hi:[1,0]\n"
"v_pk_mul_f32 v[226:227],v[226:227],v[122:123] op_sel_hi:[1,0]\n"
"v_pk_mul_f32 v[228:229],v[228:229],v[122:123] op_sel_hi:[1,0]\n"
"v_pk_mul_f32 v[230:231],v[230:231],v[122:123] op_sel_hi:[1,0]\n"
"v_cvt_pk_bf16_f32 v224,v224,v225\n"
"v_cvt_pk_bf16_f32 v225,v226,v227\n"
"v_cvt_pk_bf16_f32 v226,v228,v229\n"
"v_cvt_pk_bf16_f32 v227,v230,v231\n"
"buffer_store_dwordx4 v[224:227],v112,s[24:27],s70 offen offset:0 sc1\n"
"v_pk_mul_f32 v[232:233],v[232:233],v[122:123] op_sel_hi:[1,0]\n"
"v_pk_mul_f32 v[234:235],v[234:235],v[122:123] op_sel_hi:[1,0]\n"
"v_pk_mul_f32 v[236:237],v[236:237],v[122:123] op_sel_hi:[1,0]\n"
"v_pk_mul_f32 v[238:239],v[238:239],v[122:123] op_sel_hi:[1,0]\n"
"v_cvt_pk_bf16_f32 v232,v232,v233\n"
"v_cvt_pk_bf16_f32 v233,v234,v235\n"
"v_cvt_pk_bf16_f32 v234,v236,v237\n"
"v_cvt_pk_bf16_f32 v235,v238,v239\n"
"buffer_store_dwordx4 v[232:235],v112,s[24:27],s70 offen offset:1024 sc1\n"
"v_pk_mul_f32 v[240:241],v[240:241],v[122:123] op_sel_hi:[1,0]\n"
"v_pk_mul_f32 v[242:243],v[242:243],v[122:123] op_sel_hi:[1,0]\n"
"v_pk_mul_f32 v[244:245],v[244:245],v[122:123] op_sel_hi:[1,0]\n"
"v_pk_mul_f32 v[246:247],v[246:247],v[122:123] op_sel_hi:[1,0]\n"
"v_cvt_pk_bf16_f32 v240,v240,v241\n"
"v_cvt_pk_bf16_f32 v241,v242,v243\n"
"v_cvt_pk_bf16_f32 v242,v244,v245\n"
"v_cvt_pk_bf16_f32 v243,v246,v247\n"
"buffer_store_dwordx4 v[240:243],v112,s[24:27],s70 offen offset:2048 sc1\n"
"v_pk_mul_f32 v[248:249],v[248:249],v[122:123] op_sel_hi:[1,0]\n"
"v_pk_mul_f32 v[250:251],v[250:251],v[122:123] op_sel_hi:[1,0]\n"
"v_pk_mul_f32 v[252:253],v[252:253],v[122:123] op_sel_hi:[1,0]\n"
"v_pk_mul_f32 v[254:255],v[254:255],v[122:123] op_sel_hi:[1,0]\n"
"v_cvt_pk_bf16_f32 v248,v248,v249\n"
"v_cvt_pk_bf16_f32 v249,v250,v251\n"
"v_cvt_pk_bf16_f32 v250,v252,v253\n"
"v_cvt_pk_bf16_f32 v251,v254,v255\n"
"buffer_store_dwordx4 v[248:251],v112,s[24:27],s70 offen offset:3072 sc1\n"
"s_add_u32 s70,s70,262144\n"
"s_mov_b32 s70,s68\n"
"s_load_dwordx2 s[36:37],s[0:1],0\n"
"s_load_dwordx2 s[44:45],s[0:1],80\n"
"s_load_dwordx2 s[48:49],s[0:1],88\n"
"s_lshl_b32 s82,s78,2\n"
"s_add_u32 s82,s82,65536\n"
"v_add_u32 v113,s82,v112\n"
"s_lshr_b32 s82,s78,2\n"
"s_add_u32 s82,s82,122880\n"
"v_lshrrev_b32 v115,1,v112\n"
"v_add_u32 v116,s82,v115\n"
"s_waitcnt lgkmcnt(0)\n"
"s_barrier\n"
"v_mfma_f32_16x16x32_bf16 v[128:131],a[96:99],v[64:67],0\n"
"s_add_u32 s83,s18,2097152\n"
"s_add_u32 m0,s30,32768\n"
"v_mfma_f32_16x16x32_bf16 a[128:131],a[144:147],v[64:67],0\n"
"buffer_load_dwordx4 v112,s[24:27],s83 offen offset:0 sc1 lds\n"
"v_mfma_f32_16x16x32_bf16 v[132:135],a[100:103],v[64:67],0\n"
"buffer_load_dwordx4 v112,s[24:27],s83 offen offset:1024 sc1 lds\n"
"v_mfma_f32_16x16x32_bf16 v[136:139],a[104:107],v[64:67],0\n"
"buffer_load_dwordx4 v112,s[24:27],s83 offen offset:2048 sc1 lds\n"
"v_mfma_f32_16x16x32_bf16 v[140:143],a[108:111],v[64:67],0\n"
"v_mfma_f32_16x16x32_bf16 v[144:147],a[112:115],v[64:67],0\n"
"buffer_load_dwordx4 v112,s[24:27],s83 offen offset:3072 sc1 lds\n"
"v_mfma_f32_16x16x32_bf16 v[148:151],a[116:119],v[64:67],0\n"
"s_add_u32 m0,m0,4096\n"
"v_mfma_f32_16x16x32_bf16 v[152:155],a[120:123],v[64:67],0\n"
"v_mfma_f32_16x16x32_bf16 v[156:159],a[124:127],v[64:67],0\n"
"buffer_load_dwordx4 v117,s[24:27],s83 offen offset:0 sc1 lds\n"
"v_mfma_f32_16x16x32_bf16 v[160:163],a[96:99],v[68:71],0\n"
"v_accvgpr_read_b32 v97,a128\n"
"v_cvt_pk_bf16_f32 v136,v136,v137\n"
"v_mfma_f32_16x16x32_bf16 a[132:135],a[144:147],v[68:71],0\n"
"buffer_load_dwordx4 v117,s[24:27],s83 offen offset:1024 sc1 lds\n"
"v_mfma_f32_16x16x32_bf16 v[164:167],a[100:103],v[68:71],0\n"
"v_cvt_pk_bf16_f32 v128,v128,v129\n"
"v_cvt_pk_bf16_f32 v129,v130,v131\n"
"v_mfma_f32_16x16x32_bf16 v[168:171],a[104:107],v[68:71],0\n"
"buffer_load_dwordx4 v117,s[24:27],s83 offen offset:2048 sc1 lds\n"
"v_cvt_pk_bf16_f32 v130,v132,v133\n"
"v_cvt_pk_bf16_f32 v131,v134,v135\n"
"v_mfma_f32_16x16x32_bf16 v[172:175],a[108:111],v[68:71],0\n"
"ds_write_b128 v113,v[128:131] offset:0\n"
"v_cvt_pk_bf16_f32 v137,v138,v139\n"
"v_mfma_f32_16x16x32_bf16 v[176:179],a[112:115],v[68:71],0\n"
"buffer_load_dwordx4 v117,s[24:27],s83 offen offset:3072 sc1 lds\n"
"v_cvt_pk_bf16_f32 v138,v140,v141\n"
"v_cvt_pk_bf16_f32 v139,v142,v143\n"
"v_mfma_f32_16x16x32_bf16 v[180:183],a[116:119],v[68:71],0\n"
"s_sub_u32 s82,s3,81920\n"
"ds_write_b128 v113,v[136:139] offset:1024\n"
"v_cvt_pk_bf16_f32 v144,v144,v145\n"
"v_mfma_f32_16x16x32_bf16 v[184:187],a[120:123],v[68:71],0\n"
"buffer_load_dwordx4 a[32:35],v112,s[24:27],s82 offen offset:0 sc1\n"
"v_cvt_pk_bf16_f32 v145,v146,v147\n"
"v_cvt_pk_bf16_f32 v146,v148,v149\n"
"v_cvt_pk_bf16_f32 v147,v150,v151\n"
"v_mfma_f32_16x16x32_bf16 v[188:191],a[124:127],v[68:71],0\n"
"ds_write_b128 v113,v[144:147] offset:2048\n"
"v_cvt_pk_bf16_f32 v152,v152,v153\n"
"v_mfma_f32_16x16x32_bf16 v[192:195],a[96:99],v[72:75],0\n"
"buffer_load_dwordx4 a[36:39],v112,s[24:27],s82 offen offset:1024 sc1\n"
"v_cvt_pk_bf16_f32 v153,v154,v155\n"
"v_cvt_pk_bf16_f32 v154,v156,v157\n"
"v_cvt_pk_bf16_f32 v155,v158,v159\n"
"v_mfma_f32_16x16x32_bf16 a[136:139],a[144:147],v[72:75],0\n"
"ds_write_b128 v113,v[152:155] offset:3072\n"
"ds_write_b64 v116,v[96:97] offset:0\n"
"v_mfma_f32_16x16x32_bf16 v[196:199],a[100:103],v[72:75],0\n"
"buffer_load_dwordx4 a[40:43],v112,s[24:27],s82 offen offset:2048 sc1\n"
"v_accvgpr_read_b32 v101,a132\n"
"v_cvt_pk_bf16_f32 v160,v160,v161\n"
"v_mfma_f32_16x16x32_bf16 v[200:203],a[104:107],v[72:75],0\n"
"v_cvt_pk_bf16_f32 v161,v162,v163\n"
"v_cvt_pk_bf16_f32 v162,v164,v165\n"
"v_cvt_pk_bf16_f32 v163,v166,v167\n"
"v_mfma_f32_16x16x32_bf16 v[204:207],a[108:111],v[72:75],0\n"
"ds_write_b128 v113,v[160:163] offset:4096\n"
"v_cvt_pk_bf16_f32 v168,v168,v169\n"
"v_cvt_pk_bf16_f32 v169,v170,v171\n"
"v_mfma_f32_16x16x32_bf16 v[208:211],a[112:115],v[72:75],0\n"
"buffer_load_dwordx4 a[44:47],v112,s[24:27],s82 offen offset:3072 sc1\n"
"v_cvt_pk_bf16_f32 v170,v172,v173\n"
"v_cvt_pk_bf16_f32 v171,v174,v175\n"
"v_mfma_f32_16x16x32_bf16 v[212:215],a[116:119],v[72:75],0\n"
"ds_write_b128 v113,v[168:171] offset:5120\n"
"v_cvt_pk_bf16_f32 v176,v176,v177\n"
"v_cvt_pk_bf16_f32 v177,v178,v179\n"
"v_mfma_f32_16x16x32_bf16 v[216:219],a[120:123],v[72:75],0\n"
"v_cvt_pk_bf16_f32 v178,v180,v181\n"
"v_cvt_pk_bf16_f32 v179,v182,v183\n"
"v_mfma_f32_16x16x32_bf16 v[220:223],a[124:127],v[72:75],0\n"
"ds_write_b128 v113,v[176:179] offset:6144\n"
"v_cvt_pk_bf16_f32 v184,v184,v185\n"
"v_cvt_pk_bf16_f32 v185,v186,v187\n"
"v_cvt_pk_bf16_f32 v186,v188,v189\n"
"v_cvt_pk_bf16_f32 v187,v190,v191\n"
"v_mfma_f32_16x16x32_bf16 v[224:227],a[96:99],v[76:79],0\n"
"ds_write_b128 v113,v[184:187] offset:7168\n"
"v_mfma_f32_16x16x32_bf16 a[140:143],a[144:147],v[76:79],0\n"
"s_sub_u32 s82,s3,131072\n"
"ds_write_b64 v116,v[100:101] offset:512\n"
"buffer_load_dwordx4 v[128:131],v112,s[24:27],s82 offen offset:0 sc1\n"
"buffer_load_dwordx4 v[132:135],v112,s[24:27],s82 offen offset:1024 sc1\n"
"v_mfma_f32_16x16x32_bf16 v[228:231],a[100:103],v[76:79],0\n"
"buffer_load_dwordx4 v[136:139],v112,s[24:27],s82 offen offset:2048 sc1\n"
"buffer_load_dwordx4 v[140:143],v112,s[24:27],s82 offen offset:3072 sc1\n"
"s_sub_u32 s82,s3,147456\n"
"v_mfma_f32_16x16x32_bf16 v[232:235],a[104:107],v[76:79],0\n"
"buffer_load_dwordx4 v[144:147],v112,s[24:27],s82 offen offset:0 sc1\n"
"buffer_load_dwordx4 v[148:151],v112,s[24:27],s82 offen offset:1024 sc1\n"
"buffer_load_dwordx4 v[152:155],v112,s[24:27],s82 offen offset:2048 sc1\n"
"v_mfma_f32_16x16x32_bf16 v[236:239],a[108:111],v[76:79],0\n"
"buffer_load_dwordx4 v[156:159],v112,s[24:27],s82 offen offset:3072 sc1\n"
"v_mfma_f32_16x16x32_bf16 v[240:243],a[112:115],v[76:79],0\n"
"v_mfma_f32_16x16x32_bf16 v[244:247],a[116:119],v[76:79],0\n"
"v_mfma_f32_16x16x32_bf16 v[248:251],a[120:123],v[76:79],0\n"
"v_mfma_f32_16x16x32_bf16 v[252:255],a[124:127],v[76:79],0\n"
"s_waitcnt vmcnt(20)\n"
"buffer_store_dwordx2 a[154:155],off,s[24:27],s69 sc1\n"
"s_branch .Lfk_endB\n"
".Lfk_head0:\n"
"s_mov_b32 s64,s61\n"
"s_cmp_eq_u32 s52,3\n"
"s_cselect_b32 s67,172032,0\n"
"s_waitcnt lgkmcnt(0)\n"
"s_barrier\n"
"v_mfma_f32_16x16x32_bf16 v[24:27],a[64:67],a[48:51],v[108:111]\n"
"v_add_f32 v0,0x80000000,v126\n"
"v_min_f32 v0,v0,v96\n"
"v_mfma_f32_16x16x32_bf16 v[24:27],a[68:71],a[52:55],v[24:27]\n"
"v_add_f32 v1,0xf149f2ca,v126\n"
"v_min_f32 v1,v1,v97\n"
"v_mfma_f32_16x16x32_bf16 v[24:27],a[72:75],a[56:59],v[24:27]\n"
"v_add_f32 v2,0xf1c9f2ca,v126\n"
"v_min_f32 v2,v2,v98\n"
"v_mfma_f32_16x16x32_bf16 v[24:27],a[76:79],a[60:63],v[24:27]\n"
"v_add_f32 v3,0xf2177617,v126\n"
"v_min_f32 v3,v3,v99\n"
"v_mfma_f32_16x16x32_bf16 v[28:31],a[92:95],a[60:63],v[108:111]\n"
"v_add_f32 v4,0xf349f2ca,v126\n"
"v_min_f32 v4,v4,v96\n"
"v_mfma_f32_16x16x32_bf16 v[28:31],a[88:91],a[56:59],v[28:31]\n"
"v_add_f32 v5,0xf35691f6,v126\n"
"v_min_f32 v5,v5,v97\n"
"v_mfma_f32_16x16x32_bf16 v[28:31],a[84:87],a[52:55],v[28:31]\n"
"v_add_f32 v6,0xf3633123,v126\n"
"v_min_f32 v6,v6,v98\n"
"v_mfma_f32_16x16x32_bf16 v[28:31],a[80:83],a[48:51],v[28:31]\n"
"v_add_f32 v7,0xf36fd050,v126\n"
"v_min_f32 v7,v7,v99\n"
"s_nop 1\n"
"v_mfma_f32_16x16x32_bf16 v[4:7],a[80:83],a[0:3],v[4:7]\n"
"s_andn2_b32 m0,s55,16384\n"
"s_add_u32 s54,s54,s22\n"
"v_mfma_f32_16x16x32_bf16 v[4:7],a[84:87],a[4:7],v[4:7]\n"
"ds_read_b128 a[96:99],v113 offset:16384\n"
"ds_read_b128 a[100:103],v113 offset:17408\n"
"v_mfma_f32_16x16x32_bf16 v[4:7],a[88:91],a[8:11],v[4:7]\n"
"ds_read_b128 a[104:107],v113 offset:18432\n"
"ds_read_b128 a[108:111],v113 offset:19456\n"
"v_mfma_f32_16x16x32_bf16 v[4:7],a[92:95],a[12:15],v[4:7]\n"
"ds_read_b128 a[112:115],v113 offset:20480\n"
"ds_read_b128 a[116:119],v113 offset:21504\n"
"v_mfma_f32_16x16x32_bf16 v[0:3],a[76:79],a[12:15],v[0:3]\n"
"ds_read_b128 a[120:123],v113 offset:22528\n"
"ds_read_b128 a[124:127],v113 offset:23552\n"
"v_mfma_f32_16x16x32_bf16 v[0:3],a[72:75],a[8:11],v[0:3]\n"
"buffer_load_dwordx4 v115,s[32:35],s54 offen sc1 lds\n"
"v_mfma_f32_16x16x32_bf16 v[0:3],a[68:71],a[4:7],v[0:3]\n"
"s_add_u32 m0,s67,0\n"
"v_max3_f32 v118,v24,v25,v26\n"
"v_mfma_f32_16x16x32_bf16 v[0:3],a[64:67],a[0:3],v[0:3]\n"
"buffer_load_dwordx4 v112,s[24:27],s64 offen offset:0 sc1 lds\n"
"v_mfma_f32_16x16x32_bf16 v[8:11],a[64:67],a[16:19],v[100:103]\n"
"buffer_load_dwordx4 v112,s[24:27],s64 offen offset:1024 sc1 lds\n"
"v_mfma_f32_16x16x32_bf16 v[8:11],a[68:71],a[20:23],v[8:11]\n"
"buffer_load_dwordx4 v112,s[24:27],s64 offen offset:2048 sc1 lds\n"
"v_mfma_f32_16x16x32_bf16 v[8:11],a[72:75],a[24:27],v[8:11]\n"
"buffer_load_dwordx4 v112,s[24:27],s64 offen offset:3072 sc1 lds\n"
"v_mfma_f32_16x16x32_bf16 v[8:11],a[76:79],a[28:31],v[8:11]\n"
"s_add_u32 m0,s67,4096\n"
"v_max3_f32 v118,v118,v27,v28\n"
"v_cvt_pk_bf16_f32 v192,v192,v193\n"
"v_mfma_f32_16x16x32_bf16 v[12:15],a[92:95],a[28:31],v[100:103]\n"
"buffer_load_dwordx4 v117,s[24:27],s64 offen offset:0 sc1 lds\n"
"v_mfma_f32_16x16x32_bf16 v[12:15],a[88:91],a[24:27],v[12:15]\n"
"buffer_load_dwordx4 v117,s[24:27],s64 offen offset:1024 sc1 lds\n"
"v_mfma_f32_16x16x32_bf16 v[12:15],a[84:87],a[20:23],v[12:15]\n"
"buffer_load_dwordx4 v117,s[24:27],s64 offen offset:2048 sc1 lds\n"
"v_mfma_f32_16x16x32_bf16 v[12:15],a[80:83],a[16:19],v[12:15]\n"
"buffer_load_dwordx4 v117,s[24:27],s64 offen offset:3072 sc1 lds\n"
"v_cvt_pk_bf16_f32 v193,v194,v195\n"
"v_cvt_pk_bf16_f32 v194,v196,v197\n"
"v_cvt_pk_bf16_f32 v195,v198,v199\n"
"v_cvt_pk_bf16_f32 v200,v200,v201\n"
"v_cvt_pk_bf16_f32 v201,v202,v203\n"
"v_cvt_pk_bf16_f32 v202,v204,v205\n"
"v_cvt_pk_bf16_f32 v203,v206,v207\n"
"ds_write_b128 v116,v[192:195] offset:32768\n"
"ds_write_b128 v116,v[200:203] offset:33792\n"
"v_cvt_pk_bf16_f32 v208,v208,v209\n"
"v_cvt_pk_bf16_f32 v209,v210,v211\n"
"v_cvt_pk_bf16_f32 v210,v212,v213\n"
"v_cvt_pk_bf16_f32 v211,v214,v215\n"
"v_cvt_pk_bf16_f32 v216,v216,v217\n"
"v_cvt_pk_bf16_f32 v217,v218,v219\n"
"v_cvt_pk_bf16_f32 v218,v220,v221\n"
"v_cvt_pk_bf16_f32 v219,v222,v223\n"
"ds_write_b128 v116,v[208:211] offset:34816\n"
"ds_write_b128 v116,v[216:219] offset:35840\n"
"ds_read_b128 a[32:35],v116 offset:32768\n"
"ds_read_b128 a[36:39],v116 offset:33792\n"
"ds_read_b128 a[40:43],v116 offset:34816\n"
"ds_read_b128 a[44:47],v116 offset:35840\n"
"s_waitcnt lgkmcnt(0)\n"
"v_mfma_f32_16x16x32_bf16 v[20:23],a[80:83],a[32:35],v[104:107]\n"
"s_add_u32 s64,s64,16384\n"
"v_max3_f32 v118,v118,v29,v30\n"
"v_mfma_f32_16x16x32_bf16 v[20:23],a[84:87],a[36:39],v[20:23]\n"
"v_max_f32 v118,v118,v31\n"
"v_mov_b32 v120,v118\n"
"v_mfma_f32_16x16x32_bf16 v[20:23],a[88:91],a[40:43],v[20:23]\n"
"v_max3_f32 v119,v0,v1,v2\n"
"v_max3_f32 v119,v119,v3,v4\n"
"v_mfma_f32_16x16x32_bf16 v[20:23],a[92:95],a[44:47],v[20:23]\n"
"buffer_store_dwordx4 a[224:227],v112,s[24:27],s56 offen offset:0 sc1\n"
"v_max3_f32 v119,v119,v5,v6\n"
"v_mfma_f32_16x16x32_bf16 v[16:19],a[76:79],a[44:47],v[104:107]\n"
"v_max_f32 v119,v119,v7\n"
"v_cmp_gt_f32 s[28:29],s16,v119\n"
"v_mfma_f32_16x16x32_bf16 v[16:19],a[72:75],a[40:43],v[16:19]\n"
"v_max_f32 v118,v118,v119\n"
"v_max3_f32 v119,v8,v9,v10\n"
"v_mfma_f32_16x16x32_bf16 v[16:19],a[68:71],a[36:39],v[16:19]\n"
"v_max3_f32 v119,v119,v11,v12\n"
"v_max3_f32 v119,v119,v13,v14\n"
"v_mfma_f32_16x16x32_bf16 v[16:19],a[64:67],a[32:35],v[16:19]\n"
"s_and_b32 s28,s28,0xffff\n"
"v_max_f32 v119,v119,v15\n"
"v_min_f32 v120,v120,v119\n"
"v_max_f32 v118,v118,v119\n"
"s_nop 3\n"
"v_max3_f32 v119,v16,v17,v18\n"
"v_max3_f32 v119,v119,v19,v20\n"
"v_max3_f32 v119,v119,v21,v22\n"
"v_max_f32 v119,v119,v23\n"
"v_min_f32 v120,v120,v119\n"
"v_max_f32 v118,v118,v119\n"
"v_cmp_gt_f32 s[96:97],s16,v120\n"
"v_cmp_lt_f32 vcc,s93,v118\n"
"s_or_b32 s96,s96,s28\n"
"s_or_b64 s[96:97],s[96:97],vcc\n"
"s_cbranch_scc1 .Lfk_cs_z3\n"
".Lfk_cr_z3:\n"
"s_waitcnt vmcnt(19) lgkmcnt(0)\n"
"s_barrier\n"
"v_mfma_f32_16x16x32_bf16 v[48:51],a[96:99],a[32:35],v[104:107]\n"
"v_add_f32 v40,0x80000000,v126\n"
"v_min_f32 v40,v40,v100\n"
"v_add_f32 v41,0xf149f2ca,v126\n"
"v_min_f32 v41,v41,v101\n"
"v_mfma_f32_16x16x32_bf16 v[48:51],a[100:103],a[36:39],v[48:51]\n"
"v_add_f32 v42,0xf1c9f2ca,v126\n"
"v_min_f32 v42,v42,v102\n"
"v_add_f32 v43,0xf2177617,v126\n"
"v_min_f32 v43,v43,v103\n"
"v_mfma_f32_16x16x32_bf16 v[48:51],a[104:107],a[40:43],v[48:51]\n"
"v_add_f32 v44,0xf349f2ca,v126\n"
"v_min_f32 v44,v44,v100\n"
"v_add_f32 v45,0xf35691f6,v126\n"
"v_min_f32 v45,v45,v101\n"
"v_mfma_f32_16x16x32_bf16 v[48:51],a[108:111],a[44:47],v[48:51]\n"
"v_add_f32 v46,0xf3633123,v126\n"
"v_min_f32 v46,v46,v102\n"
"v_add_f32 v47,0xf36fd050,v126\n"
"v_min_f32 v47,v47,v103\n"
"v_mfma_f32_16x16x32_bf16 v[52:55],a[124:127],a[44:47],v[104:107]\n"
"s_add_u32 m0,s55,4096\n"
"v_exp_f32 v0,v0\n"
"s_add_u32 s54,s54,s22\n"
"ds_read_b128 a[0:3],v116 offset:40960\n"
"v_exp_f32 v1,v1\n"
"v_mfma_f32_16x16x32_bf16 v[52:55],a[120:123],a[40:43],v[52:55]\n"
"buffer_load_dwordx4 v115,s[32:35],s54 offen sc1 lds\n"
"v_mfma_f32_16x16x32_bf16 v[52:55],a[116:119],a[36:39],v[52:55]\n"
"s_add_u32 m0,s67,16384\n"
"v_exp_f32 v2,v2\n"
"v_mfma_f32_16x16x32_bf16 v[52:55],a[112:115],a[32:35],v[52:55]\n"
"buffer_load_dwordx4 v112,s[24:27],s64 offen offset:0 sc1 lds\n"
"buffer_load_dwordx4 v112,s[24:27],s64 offen offset:1024 sc1 lds\n"
"v_exp_f32 v3,v3\n"
"v_exp_f32 v4,v4\n"
"v_mfma_f32_16x16x32_bf16 v[60:63],a[112:115],a[48:51],v[108:111]\n"
"buffer_load_dwordx4 v112,s[24:27],s64 offen offset:2048 sc1 lds\n"
"v_exp_f32 v5,v5\n"
"v_mfma_f32_16x16x32_bf16 v[60:63],a[116:119],a[52:55],v[60:63]\n"
"ds_read_b128 a[64:67],v113 offset:49152\n"
"ds_read_b128 a[68:71],v113 offset:50176\n"
"v_exp_f32 v6,v6\n"
"v_mfma_f32_16x16x32_bf16 v[60:63],a[120:123],a[56:59],v[60:63]\n"
"ds_read_b128 a[72:75],v113 offset:51200\n"
"ds_read_b128 a[76:79],v113 offset:52224\n"
"v_exp_f32 v7,v7\n"
"v_mfma_f32_16x16x32_bf16 v[60:63],a[124:127],a[60:63],v[60:63]\n"
"ds_read_b128 a[80:83],v113 offset:53248\n"
"ds_read_b128 a[84:87],v113 offset:54272\n"
"v_exp_f32 v8,v8\n"
"v_mfma_f32_16x16x32_bf16 v[56:59],a[108:111],a[60:63],v[108:111]\n"
"ds_read_b128 a[88:91],v113 offset:55296\n"
"ds_read_b128 a[92:95],v113 offset:56320\n"
"v_exp_f32 v9,v9\n"
"v_mfma_f32_16x16x32_bf16 v[56:59],a[104:107],a[56:59],v[56:59]\n"
"buffer_load_dwordx4 v112,s[24:27],s64 offen offset:3072 sc1 lds\n"
"v_mfma_f32_16x16x32_bf16 v[56:59],a[100:103],a[52:55],v[56:59]\n"
"s_add_u32 m0,s67,20480\n"
"v_exp_f32 v10,v10\n"
"v_mfma_f32_16x16x32_bf16 v[56:59],a[96:99],a[48:51],v[56:59]\n"
"buffer_load_dwordx4 v117,s[24:27],s64 offen offset:0 sc1 lds\n"
"buffer_load_dwordx4 v117,s[24:27],s64 offen offset:1024 sc1 lds\n"
"v_exp_f32 v11,v11\n"
"v_exp_f32 v12,v12\n"
"v_mfma_f32_16x16x32_bf16 v[40:43],a[96:99],a[16:19],v[40:43]\n"
"buffer_load_dwordx4 v117,s[24:27],s64 offen offset:2048 sc1 lds\n"
"v_exp_f32 v13,v13\n"
"v_mfma_f32_16x16x32_bf16 v[40:43],a[100:103],a[20:23],v[40:43]\n"
"buffer_store_dwordx4 a[168:171],v112,s[24:27],s3 offen offset:2048 sc1\n"
"v_cvt_pk_bf16_f32 v64,v0,v1\n"
"v_exp_f32 v14,v14\n"
"v_mfma_f32_16x16x32_bf16 v[40:43],a[104:107],a[24:27],v[40:43]\n"
"buffer_load_dwordx4 v117,s[24:27],s64 offen offset:3072 sc1 lds\n"
"v_mfma_f32_16x16x32_bf16 v[40:43],a[108:111],a[28:31],v[40:43]\n"
"s_add_u32 s64,s64,16384\n"
"v_exp_f32 v15,v15\n"
"v_cvt_pk_bf16_f32 v65,v2,v3\n"
"v_cvt_pk_bf16_f32 v66,v4,v5\n"
"v_exp_f32 v16,v16\n"
"v_mfma_f32_16x16x32_bf16 v[44:47],a[124:127],a[28:31],v[44:47]\n"
"buffer_store_dwordx4 a[228:231],v112,s[24:27],s56 offen offset:1024 sc1\n"
"v_cvt_pk_bf16_f32 v67,v6,v7\n"
"v_mfma_f32_16x16x32_bf16 v[44:47],a[120:123],a[24:27],v[44:47]\n"
"v_exp_f32 v17,v17\n"
"v_cvt_pk_bf16_f32 v68,v8,v9\n"
"v_cvt_pk_bf16_f32 v69,v10,v11\n"
"v_mfma_f32_16x16x32_bf16 v[44:47],a[116:119],a[20:23],v[44:47]\n"
"v_exp_f32 v18,v18\n"
"v_cvt_pk_bf16_f32 v70,v12,v13\n"
"v_cvt_pk_bf16_f32 v71,v14,v15\n"
"v_mfma_f32_16x16x32_bf16 v[44:47],a[112:115],a[16:19],v[44:47]\n"
"v_exp_f32 v19,v19\n"
"v_cvt_pk_bf16_f32 v72,v16,v17\n"
"v_max3_f32 v118,v48,v49,v50\n"
"v_exp_f32 v20,v20\n"
"v_cvt_pk_bf16_f32 v73,v18,v19\n"
"v_exp_f32 v21,v21\n"
"ds_read_b128 a[4:7],v116 offset:41984\n"
"ds_read_b128 a[8:11],v116 offset:43008\n"
"ds_read_b128 a[12:15],v116 offset:44032\n"
"v_exp_f32 v22,v22\n"
"v_exp_f32 v23,v23\n"
"v_exp_f32 v24,v24\n"
"v_cvt_pk_bf16_f32 v75,v22,v23\n"
"v_exp_f32 v25,v25\n"
"v_exp_f32 v26,v26\n"
"v_cvt_pk_bf16_f32 v76,v24,v25\n"
"v_exp_f32 v27,v27\n"
"v_exp_f32 v28,v28\n"
"v_cvt_pk_bf16_f32 v77,v26,v27\n"
"v_exp_f32 v29,v29\n"
"v_exp_f32 v30,v30\n"
"v_cvt_pk_bf16_f32 v78,v28,v29\n"
"v_exp_f32 v31,v31\n"
"v_cvt_pk_bf16_f32 v74,v20,v21\n"
"v_cvt_pk_bf16_f32 v79,v30,v31\n"
"v_max3_f32 v118,v118,v51,v52\n"
"v_max3_f32 v118,v118,v53,v54\n"
"v_max3_f32 v118,v118,v55,v60\n"
"v_max3_f32 v118,v118,v61,v62\n"
"v_max3_f32 v118,v118,v63,v56\n"
"v_max3_f32 v118,v118,v57,v58\n"
"v_max3_f32 v118,v118,v59,v40\n"
"v_max3_f32 v118,v118,v41,v42\n"
"v_max3_f32 v118,v118,v43,v44\n"
"v_max3_f32 v118,v118,v45,v46\n"
"v_max_f32 v118,v118,v47\n"
"v_cmp_lt_f32 vcc,s93,v118\n"
"s_nop 0\n"
"s_cbranch_vccnz .Lfk_cs_z0\n"
".Lfk_cr_z0:\n"
"s_waitcnt vmcnt(26) lgkmcnt(0)\n"
"s_barrier\n"
"v_mfma_f32_16x16x32_bf16 v[24:27],a[64:67],a[48:51],v[108:111]\n"
"ds_read_b128 a[96:99],v114 offset:40960\n"
"ds_read_b128 a[100:103],v114 offset:41984\n"
"v_mfma_f32_16x16x32_bf16 v[24:27],a[68:71],a[52:55],v[24:27]\n"
"ds_read_b128 a[104:107],v114 offset:43008\n"
"ds_read_b128 a[108:111],v114 offset:44032\n"
"v_mfma_f32_16x16x32_bf16 v[24:27],a[72:75],a[56:59],v[24:27]\n"
"ds_read_b128 a[112:115],v114 offset:45056\n"
"ds_read_b128 a[116:119],v114 offset:46080\n"
"v_mfma_f32_16x16x32_bf16 v[24:27],a[76:79],a[60:63],v[24:27]\n"
"ds_read_b128 a[120:123],v114 offset:47104\n"
"ds_read_b128 a[124:127],v114 offset:48128\n"
"v_mfma_f32_16x16x32_bf16 v[28:31],a[92:95],a[60:63],v[108:111]\n"
"v_add_f32 v16,0x80000000,v126\n"
"v_min_f32 v16,v16,v104\n"
"v_mfma_f32_16x16x32_bf16 v[28:31],a[88:91],a[56:59],v[28:31]\n"
"v_add_f32 v17,0xf149f2ca,v126\n"
"v_min_f32 v17,v17,v105\n"
"v_mfma_f32_16x16x32_bf16 v[28:31],a[84:87],a[52:55],v[28:31]\n"
"v_add_f32 v18,0xf1c9f2ca,v126\n"
"v_min_f32 v18,v18,v106\n"
"v_mfma_f32_16x16x32_bf16 v[28:31],a[80:83],a[48:51],v[28:31]\n"
"v_add_f32 v20,0xf349f2ca,v126\n"
"v_min_f32 v20,v20,v104\n"
"v_add_f32 v21,0xf35691f6,v126\n"
"v_min_f32 v21,v21,v105\n"
"v_add_f32 v22,0xf3633123,v126\n"
"v_min_f32 v22,v22,v106\n"
"v_add_f32 v23,0xf36fd050,v126\n"
"v_min_f32 v23,v23,v107\n"
"v_add_f32 v19,0xf2177617,v126\n"
"v_min_f32 v19,v19,v107\n"
"v_mfma_f32_16x16x32_bf16 v[20:23],a[80:83],a[32:35],v[20:23]\n"
"s_add_u32 m0,s55,8192\n"
"v_exp_f32 v40,v40\n"
"v_mfma_f32_16x16x32_bf16 v[20:23],a[84:87],a[36:39],v[20:23]\n"
"s_add_u32 s54,s54,s22\n"
"v_exp_f32 v41,v41\n"
"v_mfma_f32_16x16x32_bf16 v[20:23],a[88:91],a[40:43],v[20:23]\n"
"v_exp_f32 v42,v42\n"
"v_mfma_f32_16x16x32_bf16 v[20:23],a[92:95],a[44:47],v[20:23]\n"
"v_exp_f32 v43,v43\n"
"v_mfma_f32_16x16x32_bf16 v[16:19],a[76:79],a[44:47],v[16:19]\n"
"v_exp_f32 v44,v44\n"
"v_mfma_f32_16x16x32_bf16 v[16:19],a[72:75],a[40:43],v[16:19]\n"
"v_exp_f32 v45,v45\n"
"v_mfma_f32_16x16x32_bf16 v[16:19],a[68:71],a[36:39],v[16:19]\n"
"v_exp_f32 v46,v46\n"
"v_mfma_f32_16x16x32_bf16 v[16:19],a[64:67],a[32:35],v[16:19]\n"
"s_waitcnt lgkmcnt(0)\n"
"v_exp_f32 v47,v47\n"
"v_mfma_f32_16x16x32_bf16 v[128:131],a[96:99],v[64:67],0\n"
"v_exp_f32 v48,v48\n"
"v_mfma_f32_16x16x32_bf16 v[160:163],a[96:99],v[68:71],0\n"
"v_exp_f32 v49,v49\n"
"v_mfma_f32_16x16x32_bf16 v[192:195],a[96:99],v[72:75],0\n"
"ds_read_b128 a[64:67],v113 offset:32768\n"
"ds_read_b128 a[68:71],v113 offset:33792\n"
"v_mfma_f32_16x16x32_bf16 v[224:227],a[96:99],v[76:79],0\n"
"v_exp_f32 v50,v50\n"
"v_mfma_f32_16x16x32_bf16 a[140:143],a[144:147],v[76:79],0\n"
"ds_read_b128 a[72:75],v113 offset:34816\n"
"ds_read_b128 a[76:79],v113 offset:35840\n"
"v_mfma_f32_16x16x32_bf16 v[228:231],a[100:103],v[76:79],0\n"
"v_exp_f32 v51,v51\n"
"v_mfma_f32_16x16x32_bf16 v[132:135],a[100:103],v[64:67],0\n"
"ds_read_b128 a[80:83],v113 offset:36864\n"
"ds_read_b128 a[84:87],v113 offset:37888\n"
"v_mfma_f32_16x16x32_bf16 v[164:167],a[100:103],v[68:71],0\n"
"v_exp_f32 v52,v52\n"
"v_mfma_f32_16x16x32_bf16 v[196:199],a[100:103],v[72:75],0\n"
"v_exp_f32 v53,v53\n"
"v_mfma_f32_16x16x32_bf16 a[136:139],a[144:147],v[72:75],0\n"
"ds_read_b128 a[88:91],v113 offset:38912\n"
"ds_read_b128 a[92:95],v113 offset:39936\n"
"v_mfma_f32_16x16x32_bf16 v[200:203],a[104:107],v[72:75],0\n"
"v_exp_f32 v54,v54\n"
"v_mfma_f32_16x16x32_bf16 v[232:235],a[104:107],v[76:79],0\n"
"buffer_load_dwordx4 v115,s[32:35],s54 offen sc1 lds\n"
"v_mfma_f32_16x16x32_bf16 v[136:139],a[104:107],v[64:67],0\n"
"s_add_u32 m0,s67,49152\n"
"v_exp_f32 v55,v55\n"
"v_mfma_f32_16x16x32_bf16 v[168:171],a[104:107],v[68:71],0\n"
"buffer_load_dwordx4 v112,s[24:27],s64 offen offset:0 sc1 lds\n"
"v_mfma_f32_16x16x32_bf16 a[132:135],a[144:147],v[68:71],0\n"
"v_exp_f32 v56,v56\n"
"v_mfma_f32_16x16x32_bf16 v[172:175],a[108:111],v[68:71],0\n"
"buffer_load_dwordx4 v112,s[24:27],s64 offen offset:1024 sc1 lds\n"
"v_mfma_f32_16x16x32_bf16 v[204:207],a[108:111],v[72:75],0\n"
"v_exp_f32 v57,v57\n"
"v_mfma_f32_16x16x32_bf16 v[236:239],a[108:111],v[76:79],0\n"
"buffer_load_dwordx4 v112,s[24:27],s64 offen offset:2048 sc1 lds\n"
"v_mfma_f32_16x16x32_bf16 v[140:143],a[108:111],v[64:67],0\n"
"v_exp_f32 v58,v58\n"
"v_mfma_f32_16x16x32_bf16 a[128:131],a[144:147],v[64:67],0\n"
"buffer_load_dwordx4 v112,s[24:27],s64 offen offset:3072 sc1 lds\n"
"v_mfma_f32_16x16x32_bf16 v[144:147],a[112:115],v[64:67],0\n"
"s_add_u32 m0,s67,53248\n"
"v_exp_f32 v59,v59\n"
"v_mfma_f32_16x16x32_bf16 v[176:179],a[112:115],v[68:71],0\n"
"buffer_load_dwordx4 v117,s[24:27],s64 offen offset:0 sc1 lds\n"
"v_mfma_f32_16x16x32_bf16 v[208:211],a[112:115],v[72:75],0\n"
"v_exp_f32 v60,v60\n"
"v_mfma_f32_16x16x32_bf16 v[240:243],a[112:115],v[76:79],0\n"
"buffer_load_dwordx4 v117,s[24:27],s64 offen offset:1024 sc1 lds\n"
"v_mfma_f32_16x16x32_bf16 v[244:247],a[116:119],v[76:79],0\n"
"v_exp_f32 v61,v61\n"
"v_mfma_f32_16x16x32_bf16 v[148:151],a[116:119],v[64:67],0\n"
"buffer_load_dwordx4 v117,s[24:27],s64 offen offset:2048 sc1 lds\n"
"v_mfma_f32_16x16x32_bf16 v[180:183],a[116:119],v[68:71],0\n"
"v_exp_f32 v62,v62\n"
"v_mfma_f32_16x16x32_bf16 v[212:215],a[116:119],v[72:75],0\n"
"buffer_load_dwordx4 v117,s[24:27],s64 offen offset:3072 sc1 lds\n"
"v_mfma_f32_16x16x32_bf16 v[216:219],a[120:123],v[72:75],0\n"
"s_add_u32 s64,s64,16384\n"
"v_exp_f32 v63,v63\n"
"v_mfma_f32_16x16x32_bf16 v[248:251],a[120:123],v[76:79],0\n"
"buffer_store_dwordx4 a[232:235],v112,s[24:27],s56 offen offset:2048 sc1\n"
"s_and_b32 s67,s67,8192\n"
"v_mfma_f32_16x16x32_bf16 v[152:155],a[120:123],v[64:67],0\n"
"buffer_store_dwordx4 a[172:175],v112,s[24:27],s3 offen offset:3072 sc1\n"
"s_sub_u32 s3,s3,16384\n"
"v_mfma_f32_16x16x32_bf16 v[184:187],a[120:123],v[68:71],0\n"
"buffer_store_dwordx4 a[176:179],v112,s[24:27],s3 offen offset:0 sc1\n"
"v_cvt_pk_bf16_f32 v84,v40,v41\n"
"v_mfma_f32_16x16x32_bf16 v[188:191],a[124:127],v[68:71],0\n"
"v_cvt_pk_bf16_f32 v85,v42,v43\n"
"v_cvt_pk_bf16_f32 v86,v44,v45\n"
"v_mfma_f32_16x16x32_bf16 v[220:223],a[124:127],v[72:75],0\n"
"v_cvt_pk_bf16_f32 v87,v46,v47\n"
"v_cvt_pk_bf16_f32 v88,v48,v49\n"
"v_mfma_f32_16x16x32_bf16 v[252:255],a[124:127],v[76:79],0\n"
"v_cvt_pk_bf16_f32 v89,v50,v51\n"
"v_cvt_pk_bf16_f32 v90,v52,v53\n"
"v_mfma_f32_16x16x32_bf16 v[156:159],a[124:127],v[64:67],0\n"
"v_cvt_pk_bf16_f32 v91,v54,v55\n"
"v_cvt_pk_bf16_f32 v92,v56,v57\n"
"v_cvt_pk_bf16_f32 v93,v58,v59\n"
"ds_read_b128 a[16:19],v116 offset:8192\n"
"ds_read_b128 a[20:23],v116 offset:9216\n"
"ds_read_b128 a[24:27],v116 offset:10240\n"
"ds_read_b128 a[28:31],v116 offset:11264\n"
"v_cvt_pk_bf16_f32 v94,v60,v61\n"
"v_cvt_pk_bf16_f32 v95,v62,v63\n"
"v_max3_f32 v118,v24,v25,v26\n"
"v_max3_f32 v118,v118,v27,v28\n"
"v_max3_f32 v118,v118,v29,v30\n"
"v_max3_f32 v118,v118,v31,v20\n"
"v_max3_f32 v118,v118,v21,v22\n"
"v_max3_f32 v118,v118,v23,v16\n"
"v_max3_f32 v118,v118,v17,v18\n"
"v_max_f32 v118,v118,v19\n"
"v_cmp_lt_f32 vcc,s93,v118\n"
"s_nop 0\n"
"s_cbranch_vccnz .Lfk_cs_z1\n"
".Lfk_cr_z1:\n"
"v_mov_b32 v113,v112\n"
"s_waitcnt vmcnt(21) lgkmcnt(0)\n"
"s_barrier\n"
"v_add_f32 v56,0x80000000,v126\n"
"v_min_f32 v56,v56,v108\n"
"v_add_f32 v57,0xf149f2ca,v126\n"
"v_min_f32 v57,v57,v109\n"
"v_add_f32 v58,0xf1c9f2ca,v126\n"
"v_min_f32 v58,v58,v110\n"
"v_add_f32 v59,0xf2177617,v126\n"
"v_min_f32 v59,v59,v111\n"
"v_add_f32 v60,0xf349f2ca,v126\n"
"v_min_f32 v60,v60,v108\n"
"v_add_f32 v61,0xf35691f6,v126\n"
"v_min_f32 v61,v61,v109\n"
"v_add_f32 v62,0xf3633123,v126\n"
"v_min_f32 v62,v62,v110\n"
"v_add_f32 v63,0xf36fd050,v126\n"
"v_min_f32 v63,v63,v111\n"
"v_exp_f32 v16,v16\n"
"v_mfma_f32_16x16x32_bf16 v[56:59],a[64:67],a[48:51],v[56:59]\n"
"s_add_u32 m0,s55,12288\n"
"ds_read_b128 a[96:99],v114 offset:8192\n"
"ds_read_b128 a[100:103],v114 offset:9216\n"
"ds_read_b128 a[104:107],v114 offset:10240\n"
"v_exp_f32 v17,v17\n"
"v_mfma_f32_16x16x32_bf16 v[56:59],a[68:71],a[52:55],v[56:59]\n"
"s_add_u32 s54,s54,s22\n"
"ds_read_b128 a[108:111],v114 offset:11264\n"
"ds_read_b128 a[112:115],v114 offset:12288\n"
"ds_read_b128 a[116:119],v114 offset:13312\n"
"v_mfma_f32_16x16x32_bf16 v[56:59],a[72:75],a[56:59],v[56:59]\n"
"ds_read_b128 a[120:123],v114 offset:14336\n"
"ds_read_b128 a[124:127],v114 offset:15360\n"
"buffer_load_dwordx4 v115,s[32:35],s54 offen sc1 lds\n"
"v_cvt_pk_bf16_f32 v72,v16,v17\n"
"v_exp_f32 v18,v18\n"
"v_mfma_f32_16x16x32_bf16 v[56:59],a[76:79],a[60:63],v[56:59]\n"
"ds_read_b128 a[32:35],v116 offset:24576\n"
"ds_read_b128 a[36:39],v116 offset:25600\n"
"ds_read_b128 a[40:43],v116 offset:26624\n"
"v_mfma_f32_16x16x32_bf16 v[60:63],a[92:95],a[60:63],v[60:63]\n"
"buffer_store_dwordx4 a[180:183],v112,s[24:27],s3 offen offset:1024 sc1\n"
"ds_read_b128 a[44:47],v116 offset:27648\n"
"v_mfma_f32_16x16x32_bf16 v[60:63],a[88:91],a[56:59],v[60:63]\n"
"s_add_u32 m0,s67,32768\n"
"v_exp_f32 v19,v19\n"
"v_mfma_f32_16x16x32_bf16 v[60:63],a[84:87],a[52:55],v[60:63]\n"
"buffer_load_dwordx4 v112,s[24:27],s64 offen offset:0 sc1 lds\n"
"v_cvt_pk_bf16_f32 v73,v18,v19\n"
"v_exp_f32 v20,v20\n"
"v_mfma_f32_16x16x32_bf16 v[60:63],a[80:83],a[48:51],v[60:63]\n"
"ds_read_b128 a[64:67],v113 offset:0\n"
"ds_read_b128 a[68:71],v113 offset:1024\n"
"ds_read_b128 a[72:75],v113 offset:2048\n"
"s_waitcnt lgkmcnt(7)\n"
"v_mfma_f32_16x16x32_bf16 v[160:163],a[96:99],v[84:87],v[160:163]\n"
"ds_read_b128 a[76:79],v113 offset:3072\n"
"ds_read_b128 a[80:83],v113 offset:4096\n"
"ds_read_b128 a[84:87],v113 offset:5120\n"
"ds_read_b128 a[88:91],v113 offset:6144\n"
"ds_read_b128 a[92:95],v113 offset:7168\n"
"v_exp_f32 v21,v21\n"
"v_mfma_f32_16x16x32_bf16 a[132:135],a[144:147],v[84:87],a[132:135]\n"
"buffer_load_dwordx4 v112,s[24:27],s64 offen offset:1024 sc1 lds\n"
"v_max3_f32 v118,v56,v57,v58\n"
"v_mfma_f32_16x16x32_bf16 v[164:167],a[100:103],v[84:87],v[164:167]\n"
"v_exp_f32 v22,v22\n"
"v_cvt_pk_bf16_f32 v74,v20,v21\n"
"ds_read_b128 a[48:51],v116 offset:57344\n"
"ds_read_b128 a[52:55],v116 offset:58368\n"
"v_mfma_f32_16x16x32_bf16 v[168:171],a[104:107],v[84:87],v[168:171]\n"
"v_max3_f32 v118,v118,v59,v60\n"
"v_max3_f32 v118,v118,v61,v62\n"
"v_max_f32 v118,v118,v63\n"
"v_exp_f32 v23,v23\n"
"v_mfma_f32_16x16x32_bf16 v[172:175],a[108:111],v[84:87],v[172:175]\n"
"ds_read_b128 a[56:59],v116 offset:59392\n"
"buffer_load_dwordx4 v112,s[24:27],s64 offen offset:2048 sc1 lds\n"
"ds_read_b128 a[60:63],v116 offset:60416\n"
"v_mfma_f32_16x16x32_bf16 v[176:179],a[112:115],v[84:87],v[176:179]\n"
"v_cvt_pk_bf16_f32 v75,v22,v23\n"
"v_accvgpr_read_b32 v122,a128\n"
"v_rcp_f32 v122,v122\n"
"v_cmp_lt_f32 vcc,s93,v118\n"
"v_pk_mul_f32 v[128:129],v[128:129],v[122:123] op_sel_hi:[1,0]\n"
"v_mfma_f32_16x16x32_bf16 v[180:183],a[116:119],v[84:87],v[180:183]\n"
"buffer_store_dwordx4 a[184:187],v112,s[24:27],s3 offen offset:2048 sc1\n"
"v_exp_f32 v24,v24\n"
"v_pk_mul_f32 v[130:131],v[130:131],v[122:123] op_sel_hi:[1,0]\n"
"v_pk_mul_f32 v[132:133],v[132:133],v[122:123] op_sel_hi:[1,0]\n"
"v_pk_mul_f32 v[134:135],v[134:135],v[122:123] op_sel_hi:[1,0]\n"
"v_mfma_f32_16x16x32_bf16 v[184:187],a[120:123],v[84:87],v[184:187]\n"
"buffer_load_dwordx4 v112,s[24:27],s64 offen offset:3072 sc1 lds\n"
"v_cvt_pk_bf16_f32 v128,v128,v129\n"
"v_cvt_pk_bf16_f32 v129,v130,v131\n"
"v_cvt_pk_bf16_f32 v130,v132,v133\n"
"v_cvt_pk_bf16_f32 v131,v134,v135\n"
"v_mfma_f32_16x16x32_bf16 v[188:191],a[124:127],v[84:87],v[188:191]\n"
"buffer_store_dwordx4 v[128:131],v112,s[24:27],s70 offen offset:0 sc1\n"
"v_pk_mul_f32 v[136:137],v[136:137],v[122:123] op_sel_hi:[1,0]\n"
"v_mfma_f32_16x16x32_bf16 v[192:195],a[96:99],v[88:91],v[192:195]\n"
"s_add_u32 m0,s67,36864\n"
"v_exp_f32 v25,v25\n"
"v_exp_f32 v27,v27\n"
"v_cvt_pk_bf16_f32 v76,v24,v25\n"
"v_cvt_pk_bf16_f32 v136,v136,v137\n"
"v_pk_mul_f32 v[138:139],v[138:139],v[122:123] op_sel_hi:[1,0]\n"
"v_pk_mul_f32 v[140:141],v[140:141],v[122:123] op_sel_hi:[1,0]\n"
"v_pk_mul_f32 v[142:143],v[142:143],v[122:123] op_sel_hi:[1,0]\n"
"v_mfma_f32_16x16x32_bf16 a[136:139],a[144:147],v[88:91],a[136:139]\n"
"buffer_load_dwordx4 v117,s[24:27],s64 offen offset:0 sc1 lds\n"
"v_cvt_pk_bf16_f32 v137,v138,v139\n"
"v_mfma_f32_16x16x32_bf16 v[196:199],a[100:103],v[88:91],v[196:199]\n"
"v_cvt_pk_bf16_f32 v138,v140,v141\n"
"v_cvt_pk_bf16_f32 v139,v142,v143\n"
"v_exp_f32 v26,v26\n"
"v_mfma_f32_16x16x32_bf16 v[200:203],a[104:107],v[88:91],v[200:203]\n"
"buffer_store_dwordx4 v[136:139],v112,s[24:27],s70 offen offset:1024 sc1\n"
"v_cvt_pk_bf16_f32 v77,v26,v27\n"
"v_pk_mul_f32 v[144:145],v[144:145],v[122:123] op_sel_hi:[1,0]\n"
"v_pk_mul_f32 v[146:147],v[146:147],v[122:123] op_sel_hi:[1,0]\n"
"v_pk_mul_f32 v[148:149],v[148:149],v[122:123] op_sel_hi:[1,0]\n"
"v_pk_mul_f32 v[150:151],v[150:151],v[122:123] op_sel_hi:[1,0]\n"
"v_exp_f32 v28,v28\n"
"v_mfma_f32_16x16x32_bf16 v[204:207],a[108:111],v[88:91],v[204:207]\n"
"buffer_load_dwordx4 v117,s[24:27],s64 offen offset:1024 sc1 lds\n"
"buffer_store_dwordx4 a[188:191],v112,s[24:27],s3 offen offset:3072 sc1\n"
"v_mfma_f32_16x16x32_bf16 v[208:211],a[112:115],v[88:91],v[208:211]\n"
"s_or_b32 s3,s3,0x3c000\n"
"v_cvt_pk_bf16_f32 v144,v144,v145\n"
"v_cvt_pk_bf16_f32 v145,v146,v147\n"
"v_cvt_pk_bf16_f32 v146,v148,v149\n"
"v_cvt_pk_bf16_f32 v147,v150,v151\n"
"v_mfma_f32_16x16x32_bf16 v[212:215],a[116:119],v[88:91],v[212:215]\n"
"buffer_store_dwordx4 v[144:147],v112,s[24:27],s70 offen offset:2048 sc1\n"
"v_pk_mul_f32 v[152:153],v[152:153],v[122:123] op_sel_hi:[1,0]\n"
"v_pk_mul_f32 v[154:155],v[154:155],v[122:123] op_sel_hi:[1,0]\n"
"v_mfma_f32_16x16x32_bf16 v[216:219],a[120:123],v[88:91],v[216:219]\n"
"buffer_load_dwordx4 v117,s[24:27],s64 offen offset:2048 sc1 lds\n"
"v_exp_f32 v29,v29\n"
"v_exp_f32 v31,v31\n"
"v_cvt_pk_bf16_f32 v78,v28,v29\n"
"v_pk_mul_f32 v[156:157],v[156:157],v[122:123] op_sel_hi:[1,0]\n"
"v_pk_mul_f32 v[158:159],v[158:159],v[122:123] op_sel_hi:[1,0]\n"
"v_mfma_f32_16x16x32_bf16 v[220:223],a[124:127],v[88:91],v[220:223]\n"
"v_cvt_pk_bf16_f32 v152,v152,v153\n"
"v_cvt_pk_bf16_f32 v153,v154,v155\n"
"v_cvt_pk_bf16_f32 v154,v156,v157\n"
"v_cvt_pk_bf16_f32 v155,v158,v159\n"
"v_mfma_f32_16x16x32_bf16 v[224:227],a[96:99],v[92:95],v[224:227]\n"
"buffer_store_dwordx4 v[152:155],v112,s[24:27],s70 offen offset:3072 sc1\n"
"v_accvgpr_read_b32 v122,a132\n"
"v_mfma_f32_16x16x32_bf16 a[140:143],a[144:147],v[92:95],a[140:143]\n"
"s_add_u32 s70,s70,262144\n"
"buffer_load_dwordx4 v117,s[24:27],s64 offen offset:3072 sc1 lds\n"
"v_mfma_f32_16x16x32_bf16 v[228:231],a[100:103],v[92:95],v[228:231]\n"
"s_add_u32 s64,s64,16384\n"
"v_exp_f32 v30,v30\n"
"v_rcp_f32 v122,v122\n"
"v_cvt_pk_bf16_f32 v79,v30,v31\n"
"v_pk_mul_f32 v[160:161],v[160:161],v[122:123] op_sel_hi:[1,0]\n"
"v_pk_mul_f32 v[162:163],v[162:163],v[122:123] op_sel_hi:[1,0]\n"
"v_pk_mul_f32 v[164:165],v[164:165],v[122:123] op_sel_hi:[1,0]\n"
"v_pk_mul_f32 v[166:167],v[166:167],v[122:123] op_sel_hi:[1,0]\n"
"v_mfma_f32_16x16x32_bf16 v[232:235],a[104:107],v[92:95],v[232:235]\n"
"buffer_store_dwordx4 a[236:239],v112,s[24:27],s56 offen offset:3072 sc1\n"
"v_cvt_pk_bf16_f32 v160,v160,v161\n"
"v_cvt_pk_bf16_f32 v161,v162,v163\n"
"v_cvt_pk_bf16_f32 v162,v164,v165\n"
"v_cvt_pk_bf16_f32 v163,v166,v167\n"
"v_mfma_f32_16x16x32_bf16 v[236:239],a[108:111],v[92:95],v[236:239]\n"
"buffer_store_dwordx4 v[160:163],v112,s[24:27],s70 offen offset:0 sc1\n"
"v_pk_mul_f32 v[168:169],v[168:169],v[122:123] op_sel_hi:[1,0]\n"
"v_pk_mul_f32 v[170:171],v[170:171],v[122:123] op_sel_hi:[1,0]\n"
"v_pk_mul_f32 v[172:173],v[172:173],v[122:123] op_sel_hi:[1,0]\n"
"v_pk_mul_f32 v[174:175],v[174:175],v[122:123] op_sel_hi:[1,0]\n"
"v_mfma_f32_16x16x32_bf16 v[240:243],a[112:115],v[92:95],v[240:243]\n"
"v_cvt_pk_bf16_f32 v168,v168,v169\n"
"v_cvt_pk_bf16_f32 v169,v170,v171\n"
"v_cvt_pk_bf16_f32 v170,v172,v173\n"
"v_cvt_pk_bf16_f32 v171,v174,v175\n"
"v_mfma_f32_16x16x32_bf16 v[244:247],a[116:119],v[92:95],v[244:247]\n"
"buffer_store_dwordx4 v[168:171],v112,s[24:27],s70 offen offset:1024 sc1\n"
"v_pk_mul_f32 v[176:177],v[176:177],v[122:123] op_sel_hi:[1,0]\n"
"v_pk_mul_f32 v[178:179],v[178:179],v[122:123] op_sel_hi:[1,0]\n"
"v_pk_mul_f32 v[180:181],v[180:181],v[122:123] op_sel_hi:[1,0]\n"
"v_pk_mul_f32 v[182:183],v[182:183],v[122:123] op_sel_hi:[1,0]\n"
"v_mfma_f32_16x16x32_bf16 v[248:251],a[120:123],v[92:95],v[248:251]\n"
"v_cvt_pk_bf16_f32 v176,v176,v177\n"
"v_cvt_pk_bf16_f32 v177,v178,v179\n"
"v_cvt_pk_bf16_f32 v178,v180,v181\n"
"v_cvt_pk_bf16_f32 v179,v182,v183\n"
"v_mfma_f32_16x16x32_bf16 v[252:255],a[124:127],v[92:95],v[252:255]\n"
"buffer_store_dwordx4 v[176:179],v112,s[24:27],s70 offen offset:2048 sc1\n"
"v_pk_mul_f32 v[184:185],v[184:185],v[122:123] op_sel_hi:[1,0]\n"
"v_pk_mul_f32 v[186:187],v[186:187],v[122:123] op_sel_hi:[1,0]\n"
"v_pk_mul_f32 v[188:189],v[188:189],v[122:123] op_sel_hi:[1,0]\n"
"v_pk_mul_f32 v[190:191],v[190:191],v[122:123] op_sel_hi:[1,0]\n"
"v_cvt_pk_bf16_f32 v184,v184,v185\n"
"v_cvt_pk_bf16_f32 v185,v186,v187\n"
"v_cvt_pk_bf16_f32 v186,v188,v189\n"
"v_cvt_pk_bf16_f32 v187,v190,v191\n"
"buffer_store_dwordx4 v[184:187],v112,s[24:27],s70 offen offset:3072 sc1\n"
"s_add_u32 s70,s70,262144\n"
"s_cbranch_vccnz .Lfk_cs_z2\n"
".Lfk_cr_z2:\n"
"s_waitcnt vmcnt(34) lgkmcnt(0)\n"
"s_barrier\n"
"s_add_u32 s89,s89,1\n"
"s_branch .Lfk_winB\n"
".Lfk_ev:\n"
"s_xor_b32 s55,s55,16384\n"
"s_andn2_b32 s91,s55,16384\n"
"v_lshrrev_b32 v121,1,v112\n"
"v_add_u32 v121,s91,v121\n"
"ds_read_b64_tr_b16 a[160:161],v121 offset:0\n"
"ds_read_b64_tr_b16 a[162:163],v121 offset:512\n"
"s_add_u32 s56,s56,4096\n"
"s_swappc_b64 s[20:21],s[94:95]\n"
"s_xor_b32 s55,s55,16384\n"
"s_andn2_b32 s91,s55,16384\n"
"v_lshrrev_b32 v121,1,v112\n"
"v_add_u32 v121,s91,v121\n"
"ds_read_b64_tr_b16 a[164:165],v121 offset:4096\n"
"ds_read_b64_tr_b16 a[166:167],v121 offset:4608\n"
"ds_read_b64_tr_b16 a[168:169],v121 offset:8192\n"
"ds_read_b64_tr_b16 a[170:171],v121 offset:8704\n"
"ds_read_b64_tr_b16 a[172:173],v121 offset:12288\n"
"ds_read_b64_tr_b16 a[174:175],v121 offset:12800\n"
"ds_read_b64_tr_b16 a[176:177],v121 offset:0\n"
"ds_read_b64_tr_b16 a[178:179],v121 offset:512\n"
"buffer_store_dwordx2 a[154:155],off,s[24:27],s2 sc1\n"
"s_and_b32 s2,s2,0xfffffc00\n"
"s_mov_b32 s56,s10\n"
"v_accvgpr_write_b32 a240,0\n"
"v_accvgpr_write_b32 a241,0\n"
"v_accvgpr_write_b32 a242,0\n"
"v_accvgpr_write_b32 a243,0\n"
"v_accvgpr_write_b32 a244,0\n"
"v_accvgpr_write_b32 a245,0\n"
"v_accvgpr_write_b32 a246,0\n"
"v_accvgpr_write_b32 a247,0\n"
"v_accvgpr_write_b32 a248,0\n"
"v_accvgpr_write_b32 a249,0\n"
"v_accvgpr_write_b32 a250,0\n"
"v_accvgpr_write_b32 a251,0\n"
"v_accvgpr_write_b32 a252,0\n"
"v_accvgpr_write_b32 a253,0\n"
"v_accvgpr_write_b32 a254,0\n"
"v_accvgpr_write_b32 a255,0\n"
"s_mov_b64 s[32:33],s[36:37]\n"
"s_mov_b32 s54,-229376\n"
"s_swappc_b64 s[20:21],s[94:95]\n"
"s_xor_b32 s55,s55,16384\n"
"s_add_u32 s56,s56,131072\n"
"s_andn2_b32 s91,s55,16384\n"
"v_lshrrev_b32 v121,1,v112\n"
"v_add_u32 v121,s91,v121\n"
"ds_read_b64_tr_b16 a[180:181],v121 offset:20480\n"
"ds_read_b64_tr_b16 a[182:183],v121 offset:20992\n"
"ds_read_b64_tr_b16 a[184:185],v121 offset:24576\n"
"ds_read_b64_tr_b16 a[186:187],v121 offset:25088\n"
"ds_read_b64_tr_b16 a[188:189],v121 offset:28672\n"
"ds_read_b64_tr_b16 a[190:191],v121 offset:29184\n"
"ds_read_b64_tr_b16 a[192:193],v121 offset:0\n"
"ds_read_b64_tr_b16 a[194:195],v121 offset:512\n"
"buffer_load_dwordx2 a[150:151],v112,s[24:27],s2 offen sc1\n"
"s_add_u32 s82,s59,8192\n"
"buffer_store_dwordx2 a[154:155],off,s[24:27],s82 sc1\n"
"s_swappc_b64 s[20:21],s[94:95]\n"
"s_xor_b32 s55,s55,16384\n"
"s_add_u32 s56,s56,131072\n"
"s_andn2_b32 s91,s55,16384\n"
"v_lshrrev_b32 v121,1,v112\n"
"v_add_u32 v121,s91,v121\n"
"ds_read_b64_tr_b16 a[196:197],v121 offset:4096\n"
"ds_read_b64_tr_b16 a[198:199],v121 offset:4608\n"
"ds_read_b64_tr_b16 a[200:201],v121 offset:8192\n"
"ds_read_b64_tr_b16 a[202:203],v121 offset:8704\n"
"ds_read_b64_tr_b16 a[204:205],v121 offset:12288\n"
"ds_read_b64_tr_b16 a[206:207],v121 offset:12800\n"
"ds_read_b64_tr_b16 a[208:209],v121 offset:0\n"
"ds_read_b64_tr_b16 a[210:211],v121 offset:512\n"
"s_mov_b32 s84,0x40000\n"
".Lfk_w_chk:\n"
"v_accvgpr_read_b32 v119,a150\n"
"v_accvgpr_read_b32 v120,a151\n"
"v_xor_b32 v119,s80,v119\n"
"v_xor_b32 v120,s81,v120\n"
"v_or_b32 v119,v119,v120\n"
"v_cmp_ne_u32 vcc,0,v119\n"
"s_nop 0\n"
"s_cbranch_vccz .Lfk_w_ok\n"
"s_sub_u32 s84,s84,1\n"
"s_cbranch_scc1 .Lfk_w_ok\n"
"s_sleep 4\n"
"buffer_load_dwordx2 a[150:151],v112,s[24:27],s2 offen sc1\n"
"s_waitcnt vmcnt(0)\n"
"s_branch .Lfk_w_chk\n"
".Lfk_w_ok:\n"
"v_lshrrev_b32 v121,1,v112\n"
"v_and_b32 v121,0xf8,v121\n"
"s_add_u32 s82,s63,8192\n"
"buffer_load_dwordx2 a[150:151],v121,s[24:27],s82 offen sc1\n"
"s_mov_b64 s[32:33],s[24:25]\n"
"s_mov_b32 s34,s26\n"
"s_mov_b32 s22,1024\n"
"v_mov_b32 v115,v112\n"
"s_sub_u32 s54,s3,1024\n"
"s_swappc_b64 s[20:21],s[94:95]\n"
"s_xor_b32 s55,s55,16384\n"
"s_add_u32 s56,s56,131072\n"
"s_andn2_b32 s91,s55,16384\n"
"v_lshrrev_b32 v121,1,v112\n"
"v_add_u32 v121,s91,v121\n"
"ds_read_b64_tr_b16 a[212:213],v121 offset:20480\n"
"ds_read_b64_tr_b16 a[214:215],v121 offset:20992\n"
"ds_read_b64_tr_b16 a[216:217],v121 offset:24576\n"
"ds_read_b64_tr_b16 a[218:219],v121 offset:25088\n"
"ds_read_b64_tr_b16 a[220:221],v121 offset:28672\n"
"ds_read_b64_tr_b16 a[222:223],v121 offset:29184\n"
"v_add_u32 v121,s91,v112\n"
"ds_read_b128 a[224:227],v121 offset:0\n"
"s_sub_u32 s54,s3,17408\n"
"s_mov_b32 s84,0x40000\n"
".Lfk_k_chk:\n"
"v_accvgpr_read_b32 v119,a150\n"
"v_accvgpr_read_b32 v120,a151\n"
"v_xor_b32 v119,s80,v119\n"
"v_xor_b32 v120,s81,v120\n"
"v_or_b32 v119,v119,v120\n"
"v_cmp_ne_u32 vcc,0,v119\n"
"s_nop 0\n"
"s_cbranch_vccz .Lfk_k_ok\n"
"s_sub_u32 s84,s84,1\n"
"s_cbranch_scc1 .Lfk_k_ok\n"
"s_sleep 4\n"
"v_lshrrev_b32 v121,1,v112\n"
"v_and_b32 v121,0xf8,v121\n"
"s_add_u32 s82,s63,8192\n"
"buffer_load_dwordx2 a[150:151],v121,s[24:27],s82 offen sc1\n"
"s_waitcnt vmcnt(0)\n"
"s_branch .Lfk_k_chk\n"
".Lfk_k_ok:\n"
"s_swappc_b64 s[20:21],s[94:95]\n"
"s_xor_b32 s55,s55,16384\n"
"s_andn2_b32 s91,s55,16384\n"
"v_add_u32 v121,s91,v112\n"
"ds_read_b128 a[228:231],v121 offset:4096\n"
"ds_read_b128 a[232:235],v121 offset:8192\n"
"ds_read_b128 a[236:239],v121 offset:12288\n"
"ds_read_b128 a[240:243],v121 offset:0\n"
"s_sub_u32 s54,s3,99328\n"
"s_mov_b32 s56,2146435072\n"
"s_getpc_b64 s[96:97]\n"
".Lfk_rt1:\n"
"s_sub_u32 s96,s96,.Lfk_rt1-.Lfk_tailA\n"
"s_subb_u32 s97,s97,0\n"
"s_mov_b32 s98,.Lfk_cold-.Lfk_tailA\n"
"s_mov_b32 s99,0x20000\n"
"s_lshr_b32 s82,s100,3\n"
"s_lshl2_add_u32 s82,s82,s52\n"
"s_lshl_b32 s82,s82,9\n"
"v_lshrrev_b32 v121,1,v112\n"
"buffer_load_dword a148,v121,s[96:99],s82 offen\n"
"s_swappc_b64 s[20:21],s[94:95]\n"
"s_xor_b32 s55,s55,16384\n"
"s_andn2_b32 s91,s55,16384\n"
"v_add_u32 v121,s91,v112\n"
"ds_read_b128 a[244:247],v121 offset:20480\n"
"ds_read_b128 a[248:251],v121 offset:24576\n"
"ds_read_b128 a[252:255],v121 offset:28672\n"
"ds_read_b128 a[156:159],v121 offset:0\n"
"s_sub_u32 s54,s3,115712\n"
"s_lshl_b32 s82,s100,3\n"
"s_add_u32 s82,s82,148946944\n"
"s_cmp_eq_u32 s52,0\n"
"s_cselect_b32 s82,s82,2146435072\n"
"buffer_store_dwordx2 a[154:155],off,s[24:27],s82 sc1\n"
"s_lshr_b32 s82,s3,9\n"
"s_and_b32 s82,s82,0x3000\n"
"s_add_u32 s11,s11,s82\n"
"s_add_u32 s9,s9,s82\n"
"s_lshl3_add_u32 s8,s82,s8\n"
"s_lshr_b32 s82,s3,18\n"
"s_sub_u32 s82,s82,s60\n"
"s_and_b32 s82,s82,7\n"
"s_lshl_b32 s82,s82,17\n"
"s_add_u32 s8,s8,s82\n"
"v_lshrrev_b32 v120,1,v112\n"
"buffer_load_dwordx2 a[150:151],v120,s[24:27],s11 offen sc1\n"
"s_lshl_b32 s30,s78,1\n"
"s_swappc_b64 s[20:21],s[94:95]\n"
"s_nop 1\n"
".Lfk_xn0_s:\n"
"s_mov_b32 s84,0x40000\n"
"buffer_load_dwordx4 v[74:77],v112,s[24:27],s58 offen sc1\n"
"s_waitcnt vmcnt(0)\n"
".Lfk_xn0:\n"
"v_xor_b32 v116,s80,v74\n"
"v_xor_b32 v117,s81,v75\n"
"v_or_b32 v116,v116,v117\n"
"v_xor_b32 v117,s80,v76\n"
"v_or_b32 v116,v116,v117\n"
"v_xor_b32 v117,s81,v77\n"
"v_or_b32 v116,v116,v117\n"
"v_cmp_ne_u32 vcc,0,v116\n"
"s_nop 0\n"
"s_cbranch_vccz .Lfk_xn0_x\n"
"s_sleep 4\n"
"s_mov_b64 exec,vcc\n"
"buffer_load_dwordx4 v[74:77],v112,s[24:27],s58 offen sc1\n"
"s_mov_b64 exec,-1\n"
"s_sub_u32 s84,s84,1\n"
"s_waitcnt vmcnt(0)\n"
"s_cbranch_scc0 .Lfk_xn0\n"
".Lfk_xn0_x:\n"
"s_nop 1\n"
"s_branch .Lfk_xn0_ok\n"
".Lfk_xn1_s:\n"
"s_mov_b32 s84,0x40000\n"
"buffer_load_dwordx4 v[74:77],v112,s[24:27],s58 offen sc1\n"
"s_waitcnt vmcnt(0)\n"
".Lfk_xn1:\n"
"v_xor_b32 v116,s80,v74\n"
"v_xor_b32 v117,s81,v75\n"
"v_or_b32 v116,v116,v117\n"
"v_xor_b32 v117,s80,v76\n"
"v_or_b32 v116,v116,v117\n"
"v_xor_b32 v117,s81,v77\n"
"v_or_b32 v116,v116,v117\n"
"v_cmp_ne_u32 vcc,0,v116\n"
"s_nop 0\n"
"s_cbranch_vccz .Lfk_xn1_x\n"
"s_sleep 4\n"
"s_mov_b64 exec,vcc\n"
"buffer_load_dwordx4 v[74:77],v112,s[24:27],s58 offen sc1\n"
"s_mov_b64 exec,-1\n"
"s_sub_u32 s84,s84,1\n"
"s_waitcnt vmcnt(0)\n"
"s_cbranch_scc0 .Lfk_xn1\n"
".Lfk_xn1_x:\n"
"s_nop 1\n"
"s_branch .Lfk_xn1_ok\n"
".Lfk_kv_s:\n"
"s_mov_b32 s84,0x40000\n"
"buffer_load_dwordx2 v[118:119],v120,s[24:27],s63 offen sc1\n"
"s_waitcnt vmcnt(0)\n"
".Lfk_kv:\n"
"v_xor_b32 v116,s80,v118\n"
"v_xor_b32 v117,s81,v119\n"
"v_or_b32 v116,v116,v117\n"
"v_cmp_ne_u32 vcc,0,v116\n"
"s_nop 0\n"
"s_cbranch_vccz .Lfk_kv_x\n"
"s_sleep 4\n"
"s_mov_b64 exec,vcc\n"
"buffer_load_dwordx2 v[118:119],v120,s[24:27],s63 offen sc1\n"
"s_mov_b64 exec,-1\n"
"s_sub_u32 s84,s84,1\n"
"s_waitcnt vmcnt(0)\n"
"s_cbranch_scc0 .Lfk_kv\n"
".Lfk_kv_x:\n"
"s_nop 1\n"
"s_branch .Lfk_kv_ok\n"
".Lfk_cs_h3:\n"
"s_mov_b32 s91,15\n"
"s_mov_b32 s90,15\n"
"s_call_b64 s[94:95],.Lfk_rare0\n"
"s_branch .Lfk_cr_h3\n"
".Lfk_cs_h1:\n"
"s_mov_b32 s91,15\n"
"s_call_b64 s[94:95],.Lfk_rare3\n"
"s_branch .Lfk_cr_h1\n"
".Lfk_cs_h2:\n"
"s_mov_b32 s91,15\n"
"s_call_b64 s[94:95],.Lfk_rare2\n"
"s_branch .Lfk_cr_h2\n"
".Lfk_cs_x1:\n"
"s_mov_b32 s91,15\n"
"s_call_b64 s[94:95],.Lfk_rare1\n"
"s_branch .Lfk_cr_x1\n"
".Lfk_cs_a3:\n"
"s_mov_b32 s91,14\n"
"s_call_b64 s[94:95],.Lfk_rare0\n"
"s_branch .Lfk_cr_a3\n"
".Lfk_cs_a0:\n"
"s_mov_b32 s91,12\n"
"s_call_b64 s[94:95],.Lfk_rare1\n"
"s_branch .Lfk_cr_a0\n"
".Lfk_cs_a1:\n"
"s_mov_b32 s91,8\n"
"s_call_b64 s[94:95],.Lfk_rare0\n"
"s_branch .Lfk_cr_a1\n"
".Lfk_ms_a0:\n"
"s_call_b64 s[94:95],.Lfk_mslow0\n"
"s_branch .Lfk_mr_a0\n"
".Lfk_ms_a1:\n"
"s_call_b64 s[94:95],.Lfk_mslow1\n"
"s_branch .Lfk_mr_a1\n"
".Lfk_p2:\n"
"s_nop 7\n"
"s_mov_b32 s84,0x40000\n"
"buffer_load_dwordx2 v[122:123],v115,s[24:27],s11 offen sc1\n"
"s_waitcnt vmcnt(0)\n"
".Lfk_pj:\n"
"v_xor_b32 v119,s80,v122\n"
"v_xor_b32 v120,s81,v123\n"
"v_or_b32 v119,v119,v120\n"
"v_cmp_ne_u32 s[14:15],0,v119\n"
"s_nop 0\n"
"s_mov_b32 vcc_lo,s14\n"
"s_mov_b32 vcc_hi,0\n"
"s_cbranch_vccz .Lfk_pj_x\n"
"s_sleep 4\n"
"s_mov_b64 exec,vcc\n"
"buffer_load_dwordx2 v[122:123],v115,s[24:27],s11 offen sc1\n"
"s_mov_b64 exec,-1\n"
"s_sub_u32 s84,s84,1\n"
"s_waitcnt vmcnt(0)\n"
"s_cbranch_scc0 .Lfk_pj\n"
".Lfk_pj_x:\n"
"s_cmp_ge_u32 s52,2\n"
"s_cselect_b32 s18,1032192,0\n"
"s_add_u32 s18,s18,s8\n"
"s_add_u32 m0,s30,0\n"
"s_nop 0\n"
"buffer_load_dwordx4 v112,s[24:27],s18 offen offset:0 sc1 lds\n"
"buffer_load_dwordx4 v112,s[24:27],s18 offen offset:1024 sc1 lds\n"
"buffer_load_dwordx4 v112,s[24:27],s18 offen offset:2048 sc1 lds\n"
"buffer_load_dwordx4 v112,s[24:27],s18 offen offset:3072 sc1 lds\n"
"s_add_u32 m0,m0,4096\n"
"s_nop 0\n"
"buffer_load_dwordx4 v117,s[24:27],s18 offen offset:0 sc1 lds\n"
"buffer_load_dwordx4 v117,s[24:27],s18 offen offset:1024 sc1 lds\n"
"buffer_load_dwordx4 v117,s[24:27],s18 offen offset:2048 sc1 lds\n"
"buffer_load_dwordx4 v117,s[24:27],s18 offen offset:3072 sc1 lds\n"
"s_add_u32 s83,s18,2097152\n"
"s_add_u32 m0,s30,32768\n"
"s_nop 0\n"
"buffer_load_dwordx4 v112,s[24:27],s83 offen offset:0 sc1 lds\n"
"buffer_load_dwordx4 v112,s[24:27],s83 offen offset:1024 sc1 lds\n"
"buffer_load_dwordx4 v112,s[24:27],s83 offen offset:2048 sc1 lds\n"
"buffer_load_dwordx4 v112,s[24:27],s83 offen offset:3072 sc1 lds\n"
"s_add_u32 m0,m0,4096\n"
"s_nop 0\n"
"buffer_load_dwordx4 v117,s[24:27],s83 offen offset:0 sc1 lds\n"
"buffer_load_dwordx4 v117,s[24:27],s83 offen offset:1024 sc1 lds\n"
"buffer_load_dwordx4 v117,s[24:27],s83 offen offset:2048 sc1 lds\n"
"buffer_load_dwordx4 v117,s[24:27],s83 offen offset:3072 sc1 lds\n"
"s_waitcnt vmcnt(0)\n"
"s_nop 1\n"
"s_branch .Lfk_p3\n"
".Lfk_cs_k1:\n"
"s_mov_b32 s91,15\n"
"s_call_b64 s[94:95],.Lfk_rare3\n"
"s_branch .Lfk_cr_k1\n"
".Lfk_cs_k2:\n"
"s_mov_b32 s91,15\n"
"s_call_b64 s[94:95],.Lfk_rare2\n"
"s_branch .Lfk_cr_k2\n"
".Lfk_cs_x2:\n"
"s_mov_b32 s91,15\n"
"s_call_b64 s[94:95],.Lfk_rare1\n"
"s_branch .Lfk_cr_x2\n"
".Lfk_cs_u3:\n"
"s_mov_b32 s91,15\n"
"s_call_b64 s[94:95],.Lfk_rare0\n"
"s_branch .Lfk_cr_u3\n"
".Lfk_cs_mu3:\n"
"s_call_b64 s[94:95],.Lfk_meet3\n"
"s_branch .Lfk_cr_mu3\n"
".Lfk_cs_u0:\n"
"s_mov_b32 s91,14\n"
"s_call_b64 s[94:95],.Lfk_rare1\n"
"s_branch .Lfk_cr_u0\n"
".Lfk_cs_mu0:\n"
"s_call_b64 s[94:95],.Lfk_meet3\n"
"s_branch .Lfk_cr_mu0\n"
".Lfk_cs_u1:\n"
"s_mov_b32 s91,12\n"
"s_call_b64 s[94:95],.Lfk_rare0\n"
"s_branch .Lfk_cr_u1\n"
".Lfk_cs_mu1:\n"
"s_call_b64 s[94:95],.Lfk_meet3\n"
"s_branch .Lfk_cr_mu1\n"
".Lfk_cs_u2:\n"
"s_mov_b32 s91,8\n"
"s_call_b64 s[94:95],.Lfk_rare1\n"
"s_branch .Lfk_cr_u2\n"
".Lfk_cs_mu2:\n"
"s_call_b64 s[94:95],.Lfk_meet3\n"
"s_branch .Lfk_cr_mu2\n"
".Lfk_cs_v3:\n"
"s_mov_b32 s91,15\n"
"s_mov_b32 s90,15\n"
"s_call_b64 s[94:95],.Lfk_rare0\n"
"s_branch .Lfk_cr_v3\n"
".Lfk_cs_mv3:\n"
"s_call_b64 s[94:95],.Lfk_meet3\n"
"s_branch .Lfk_cr_mv3\n"
".Lfk_cs_mv0:\n"
"s_call_b64 s[94:95],.Lfk_meet3\n"
"s_branch .Lfk_cr_mv0\n"
".Lfk_cs_v1:\n"
"s_call_b64 s[94:95],.Lfk_meet3\n"
"v_or_b32 v118,v118,v95\n"
"v_and_b32 v118,0x40004000,v118\n"
"v_cmp_ne_u32 vcc,0,v118\n"
"s_nop 0\n"
"s_cbranch_vccz .Lfk_cr_v1\n"
"s_mov_b32 s91,15\n"
"s_call_b64 s[94:95],.Lfk_rare3\n"
"s_branch .Lfk_cr_v1\n"
".Lfk_cs_v2:\n"
"s_call_b64 s[94:95],.Lfk_meet3\n"
"v_or_b32 v118,v118,v79\n"
"v_and_b32 v118,0x40004000,v118\n"
"v_cmp_ne_u32 vcc,0,v118\n"
"s_nop 0\n"
"s_cbranch_vccz .Lfk_cr_v2\n"
"s_mov_b32 s91,15\n"
"s_call_b64 s[94:95],.Lfk_rare2\n"
"s_branch .Lfk_cr_v2\n"
".Lfk_cs_b3:\n"
"s_mov_b32 s91,15\n"
"s_call_b64 s[94:95],.Lfk_rare0\n"
"s_branch .Lfk_cr_b3\n"
".Lfk_ms_b0:\n"
"s_call_b64 s[94:95],.Lfk_mslow0\n"
"s_branch .Lfk_mr_b0\n"
".Lfk_ms_b1:\n"
"s_call_b64 s[94:95],.Lfk_mslow1\n"
"s_branch .Lfk_mr_b1\n"
".Lfk_cs_c3:\n"
"s_mov_b32 s91,15\n"
"s_mov_b32 s90,15\n"
"s_call_b64 s[94:95],.Lfk_rare0\n"
"s_branch .Lfk_cr_c3\n"
".Lfk_cs_z3:\n"
"s_mov_b32 s91,15\n"
"s_mov_b32 s90,15\n"
"s_call_b64 s[94:95],.Lfk_rare0\n"
"s_branch .Lfk_cr_z3\n"
".Lfk_cs_z0:\n"
"s_mov_b32 s91,14\n"
"s_call_b64 s[94:95],.Lfk_rare1\n"
"s_branch .Lfk_cr_z0\n"
".Lfk_cs_z1:\n"
"s_mov_b32 s91,12\n"
"s_call_b64 s[94:95],.Lfk_rare0\n"
"s_branch .Lfk_cr_z1\n"
".Lfk_cs_z2:\n"
"s_mov_b32 s91,8\n"
"s_call_b64 s[94:95],.Lfk_rare1\n"
"s_branch .Lfk_cr_z2\n"
".Lfk_cold:\n"
".Lfk_rare0:\n"
"s_nop 7\n"
"s_bitcmp1_b32 s91,0\n"
"s_cbranch_scc0 .Lfk_rare0_0\n"
"v_max3_f32 v119,v0,v1,v2\n"
"v_max3_f32 v120,v3,v4,v5\n"
"v_max3_f32 v119,v119,v6,v7\n"
"v_max_f32 v119,v119,v120\n"
"v_mov_b32 v120,v119\n"
"s_nop 1\n"
"v_permlane16_swap_b32 v120,v119\n"
"v_max_f32 v119,v119,v120\n"
"v_mov_b32 v120,v119\n"
"s_nop 1\n"
"v_permlane32_swap_b32 v120,v119\n"
"v_max_f32 v119,v119,v120\n"
"s_bitcmp1_b32 s90,0\n"
"s_cselect_b32 s82,0xff800000,0\n"
"v_add_f32 v119,0x41980000,v119\n"
"v_max_f32 v119,s82,v119\n"
"v_cmp_lt_f32 vcc,s17,|v119|\n"
"s_nop 0\n"
"s_cbranch_vccz .Lfk_rare0_0\n"
"v_ceil_f32 v119,v119\n"
"v_sub_f32 v0,v0,v119\n"
"v_sub_f32 v1,v1,v119\n"
"v_sub_f32 v2,v2,v119\n"
"v_sub_f32 v3,v3,v119\n"
"v_sub_f32 v4,v4,v119\n"
"v_sub_f32 v5,v5,v119\n"
"v_sub_f32 v6,v6,v119\n"
"v_sub_f32 v7,v7,v119\n"
"v_sub_f32 v96,v96,v119\n"
"v_sub_f32 v97,v97,v119\n"
"v_sub_f32 v98,v98,v119\n"
"v_sub_f32 v99,v99,v119\n"
"s_cbranch_scc1 .Lfk_rare0_0\n"
"v_exp_f32 v119,-v119\n"
"s_nop 0\n"
"v_mul_f32 v128,v128,v119\n"
"v_mul_f32 v129,v129,v119\n"
"v_mul_f32 v130,v130,v119\n"
"v_mul_f32 v131,v131,v119\n"
"v_mul_f32 v132,v132,v119\n"
"v_mul_f32 v133,v133,v119\n"
"v_mul_f32 v134,v134,v119\n"
"v_mul_f32 v135,v135,v119\n"
"v_mul_f32 v136,v136,v119\n"
"v_mul_f32 v137,v137,v119\n"
"v_mul_f32 v138,v138,v119\n"
"v_mul_f32 v139,v139,v119\n"
"v_mul_f32 v140,v140,v119\n"
"v_mul_f32 v141,v141,v119\n"
"v_mul_f32 v142,v142,v119\n"
"v_mul_f32 v143,v143,v119\n"
"v_mul_f32 v144,v144,v119\n"
"v_mul_f32 v145,v145,v119\n"
"v_mul_f32 v146,v146,v119\n"
"v_mul_f32 v147,v147,v119\n"
"v_mul_f32 v148,v148,v119\n"
"v_mul_f32 v149,v149,v119\n"
"v_mul_f32 v150,v150,v119\n"
"v_mul_f32 v151,v151,v119\n"
"v_mul_f32 v152,v152,v119\n"
"v_mul_f32 v153,v153,v119\n"
"v_mul_f32 v154,v154,v119\n"
"v_mul_f32 v155,v155,v119\n"
"v_mul_f32 v156,v156,v119\n"
"v_mul_f32 v157,v157,v119\n"
"v_mul_f32 v158,v158,v119\n"
"v_mul_f32 v159,v159,v119\n"
"v_accvgpr_read_b32 v120,a128\n"
"v_mul_f32 v120,v120,v119\n"
"v_accvgpr_write_b32 a128,v120\n"
"v_lshlrev_b32 v120,16,v80\n"
"v_and_b32 v121,0xffff0000,v80\n"
"v_mul_f32 v120,v120,v119\n"
"v_mul_f32 v121,v121,v119\n"
"v_cvt_pk_bf16_f32 v80,v120,v121\n"
"v_accvgpr_read_b32 v120,a129\n"
"v_mul_f32 v120,v120,v119\n"
"v_accvgpr_write_b32 a129,v120\n"
"v_lshlrev_b32 v120,16,v81\n"
"v_and_b32 v121,0xffff0000,v81\n"
"v_mul_f32 v120,v120,v119\n"
"v_mul_f32 v121,v121,v119\n"
"v_cvt_pk_bf16_f32 v81,v120,v121\n"
"v_accvgpr_read_b32 v120,a130\n"
"v_mul_f32 v120,v120,v119\n"
"v_accvgpr_write_b32 a130,v120\n"
"v_lshlrev_b32 v120,16,v82\n"
"v_and_b32 v121,0xffff0000,v82\n"
"v_mul_f32 v120,v120,v119\n"
"v_mul_f32 v121,v121,v119\n"
"v_cvt_pk_bf16_f32 v82,v120,v121\n"
"v_accvgpr_read_b32 v120,a131\n"
"v_mul_f32 v120,v120,v119\n"
"v_accvgpr_write_b32 a131,v120\n"
"v_lshlrev_b32 v120,16,v83\n"
"v_and_b32 v121,0xffff0000,v83\n"
"v_mul_f32 v120,v120,v119\n"
"v_mul_f32 v121,v121,v119\n"
"v_cvt_pk_bf16_f32 v83,v120,v121\n"
".Lfk_rare0_0:\n"
"s_bitcmp1_b32 s91,1\n"
"s_cbranch_scc0 .Lfk_rare0_1\n"
"v_max3_f32 v119,v8,v9,v10\n"
"v_max3_f32 v120,v11,v12,v13\n"
"v_max3_f32 v119,v119,v14,v15\n"
"v_max_f32 v119,v119,v120\n"
"v_mov_b32 v120,v119\n"
"s_nop 1\n"
"v_permlane16_swap_b32 v120,v119\n"
"v_max_f32 v119,v119,v120\n"
"v_mov_b32 v120,v119\n"
"s_nop 1\n"
"v_permlane32_swap_b32 v120,v119\n"
"v_max_f32 v119,v119,v120\n"
"s_bitcmp1_b32 s90,1\n"
"s_cselect_b32 s82,0xff800000,0\n"
"v_add_f32 v119,0x41980000,v119\n"
"v_max_f32 v119,s82,v119\n"
"v_cmp_lt_f32 vcc,s17,|v119|\n"
"s_nop 0\n"
"s_cbranch_vccz .Lfk_rare0_1\n"
"v_ceil_f32 v119,v119\n"
"v_sub_f32 v8,v8,v119\n"
"v_sub_f32 v9,v9,v119\n"
"v_sub_f32 v10,v10,v119\n"
"v_sub_f32 v11,v11,v119\n"
"v_sub_f32 v12,v12,v119\n"
"v_sub_f32 v13,v13,v119\n"
"v_sub_f32 v14,v14,v119\n"
"v_sub_f32 v15,v15,v119\n"
"v_sub_f32 v100,v100,v119\n"
"v_sub_f32 v101,v101,v119\n"
"v_sub_f32 v102,v102,v119\n"
"v_sub_f32 v103,v103,v119\n"
"s_cbranch_scc1 .Lfk_rare0_1\n"
"v_exp_f32 v119,-v119\n"
"s_nop 0\n"
"v_mul_f32 v160,v160,v119\n"
"v_mul_f32 v161,v161,v119\n"
"v_mul_f32 v162,v162,v119\n"
"v_mul_f32 v163,v163,v119\n"
"v_mul_f32 v164,v164,v119\n"
"v_mul_f32 v165,v165,v119\n"
"v_mul_f32 v166,v166,v119\n"
"v_mul_f32 v167,v167,v119\n"
"v_mul_f32 v168,v168,v119\n"
"v_mul_f32 v169,v169,v119\n"
"v_mul_f32 v170,v170,v119\n"
"v_mul_f32 v171,v171,v119\n"
"v_mul_f32 v172,v172,v119\n"
"v_mul_f32 v173,v173,v119\n"
"v_mul_f32 v174,v174,v119\n"
"v_mul_f32 v175,v175,v119\n"
"v_mul_f32 v176,v176,v119\n"
"v_mul_f32 v177,v177,v119\n"
"v_mul_f32 v178,v178,v119\n"
"v_mul_f32 v179,v179,v119\n"
"v_mul_f32 v180,v180,v119\n"
"v_mul_f32 v181,v181,v119\n"
"v_mul_f32 v182,v182,v119\n"
"v_mul_f32 v183,v183,v119\n"
"v_mul_f32 v184,v184,v119\n"
"v_mul_f32 v185,v185,v119\n"
"v_mul_f32 v186,v186,v119\n"
"v_mul_f32 v187,v187,v119\n"
"v_mul_f32 v188,v188,v119\n"
"v_mul_f32 v189,v189,v119\n"
"v_mul_f32 v190,v190,v119\n"
"v_mul_f32 v191,v191,v119\n"
"v_accvgpr_read_b32 v120,a132\n"
"v_mul_f32 v120,v120,v119\n"
"v_accvgpr_write_b32 a132,v120\n"
"v_lshlrev_b32 v120,16,v84\n"
"v_and_b32 v121,0xffff0000,v84\n"
"v_mul_f32 v120,v120,v119\n"
"v_mul_f32 v121,v121,v119\n"
"v_cvt_pk_bf16_f32 v84,v120,v121\n"
"v_accvgpr_read_b32 v120,a133\n"
"v_mul_f32 v120,v120,v119\n"
"v_accvgpr_write_b32 a133,v120\n"
"v_lshlrev_b32 v120,16,v85\n"
"v_and_b32 v121,0xffff0000,v85\n"
"v_mul_f32 v120,v120,v119\n"
"v_mul_f32 v121,v121,v119\n"
"v_cvt_pk_bf16_f32 v85,v120,v121\n"
"v_accvgpr_read_b32 v120,a134\n"
"v_mul_f32 v120,v120,v119\n"
"v_accvgpr_write_b32 a134,v120\n"
"v_lshlrev_b32 v120,16,v86\n"
"v_and_b32 v121,0xffff0000,v86\n"
"v_mul_f32 v120,v120,v119\n"
"v_mul_f32 v121,v121,v119\n"
"v_cvt_pk_bf16_f32 v86,v120,v121\n"
"v_accvgpr_read_b32 v120,a135\n"
"v_mul_f32 v120,v120,v119\n"
"v_accvgpr_write_b32 a135,v120\n"
"v_lshlrev_b32 v120,16,v87\n"
"v_and_b32 v121,0xffff0000,v87\n"
"v_mul_f32 v120,v120,v119\n"
"v_mul_f32 v121,v121,v119\n"
"v_cvt_pk_bf16_f32 v87,v120,v121\n"
".Lfk_rare0_1:\n"
"s_bitcmp1_b32 s91,2\n"
"s_cbranch_scc0 .Lfk_rare0_2\n"
"v_max3_f32 v119,v16,v17,v18\n"
"v_max3_f32 v120,v19,v20,v21\n"
"v_max3_f32 v119,v119,v22,v23\n"
"v_max_f32 v119,v119,v120\n"
"v_mov_b32 v120,v119\n"
"s_nop 1\n"
"v_permlane16_swap_b32 v120,v119\n"
"v_max_f32 v119,v119,v120\n"
"v_mov_b32 v120,v119\n"
"s_nop 1\n"
"v_permlane32_swap_b32 v120,v119\n"
"v_max_f32 v119,v119,v120\n"
"s_bitcmp1_b32 s90,2\n"
"s_cselect_b32 s82,0xff800000,0\n"
"v_add_f32 v119,0x41980000,v119\n"
"v_max_f32 v119,s82,v119\n"
"v_cmp_lt_f32 vcc,s17,|v119|\n"
"s_nop 0\n"
"s_cbranch_vccz .Lfk_rare0_2\n"
"v_ceil_f32 v119,v119\n"
"v_sub_f32 v16,v16,v119\n"
"v_sub_f32 v17,v17,v119\n"
"v_sub_f32 v18,v18,v119\n"
"v_sub_f32 v19,v19,v119\n"
"v_sub_f32 v20,v20,v119\n"
"v_sub_f32 v21,v21,v119\n"
"v_sub_f32 v22,v22,v119\n"
"v_sub_f32 v23,v23,v119\n"
"v_sub_f32 v104,v104,v119\n"
"v_sub_f32 v105,v105,v119\n"
"v_sub_f32 v106,v106,v119\n"
"v_sub_f32 v107,v107,v119\n"
"s_cbranch_scc1 .Lfk_rare0_2\n"
"v_exp_f32 v119,-v119\n"
"s_nop 0\n"
"v_mul_f32 v192,v192,v119\n"
"v_mul_f32 v193,v193,v119\n"
"v_mul_f32 v194,v194,v119\n"
"v_mul_f32 v195,v195,v119\n"
"v_mul_f32 v196,v196,v119\n"
"v_mul_f32 v197,v197,v119\n"
"v_mul_f32 v198,v198,v119\n"
"v_mul_f32 v199,v199,v119\n"
"v_mul_f32 v200,v200,v119\n"
"v_mul_f32 v201,v201,v119\n"
"v_mul_f32 v202,v202,v119\n"
"v_mul_f32 v203,v203,v119\n"
"v_mul_f32 v204,v204,v119\n"
"v_mul_f32 v205,v205,v119\n"
"v_mul_f32 v206,v206,v119\n"
"v_mul_f32 v207,v207,v119\n"
"v_mul_f32 v208,v208,v119\n"
"v_mul_f32 v209,v209,v119\n"
"v_mul_f32 v210,v210,v119\n"
"v_mul_f32 v211,v211,v119\n"
"v_mul_f32 v212,v212,v119\n"
"v_mul_f32 v213,v213,v119\n"
"v_mul_f32 v214,v214,v119\n"
"v_mul_f32 v215,v215,v119\n"
"v_mul_f32 v216,v216,v119\n"
"v_mul_f32 v217,v217,v119\n"
"v_mul_f32 v218,v218,v119\n"
"v_mul_f32 v219,v219,v119\n"
"v_mul_f32 v220,v220,v119\n"
"v_mul_f32 v221,v221,v119\n"
"v_mul_f32 v222,v222,v119\n"
"v_mul_f32 v223,v223,v119\n"
"v_accvgpr_read_b32 v120,a136\n"
"v_mul_f32 v120,v120,v119\n"
"v_accvgpr_write_b32 a136,v120\n"
"v_lshlrev_b32 v120,16,v88\n"
"v_and_b32 v121,0xffff0000,v88\n"
"v_mul_f32 v120,v120,v119\n"
"v_mul_f32 v121,v121,v119\n"
"v_cvt_pk_bf16_f32 v88,v120,v121\n"
"v_accvgpr_read_b32 v120,a137\n"
"v_mul_f32 v120,v120,v119\n"
"v_accvgpr_write_b32 a137,v120\n"
"v_lshlrev_b32 v120,16,v89\n"
"v_and_b32 v121,0xffff0000,v89\n"
"v_mul_f32 v120,v120,v119\n"
"v_mul_f32 v121,v121,v119\n"
"v_cvt_pk_bf16_f32 v89,v120,v121\n"
"v_accvgpr_read_b32 v120,a138\n"
"v_mul_f32 v120,v120,v119\n"
"v_accvgpr_write_b32 a138,v120\n"
"v_lshlrev_b32 v120,16,v90\n"
"v_and_b32 v121,0xffff0000,v90\n"
"v_mul_f32 v120,v120,v119\n"
"v_mul_f32 v121,v121,v119\n"
"v_cvt_pk_bf16_f32 v90,v120,v121\n"
"v_accvgpr_read_b32 v120,a139\n"
"v_mul_f32 v120,v120,v119\n"
"v_accvgpr_write_b32 a139,v120\n"
"v_lshlrev_b32 v120,16,v91\n"
"v_and_b32 v121,0xffff0000,v91\n"
"v_mul_f32 v120,v120,v119\n"
"v_mul_f32 v121,v121,v119\n"
"v_cvt_pk_bf16_f32 v91,v120,v121\n"
".Lfk_rare0_2:\n"
"s_bitcmp1_b32 s91,3\n"
"s_cbranch_scc0 .Lfk_rare0_3\n"
"v_max3_f32 v119,v24,v25,v26\n"
"v_max3_f32 v120,v27,v28,v29\n"
"v_max3_f32 v119,v119,v30,v31\n"
"v_max_f32 v119,v119,v120\n"
"v_mov_b32 v120,v119\n"
"s_nop 1\n"
"v_permlane16_swap_b32 v120,v119\n"
"v_max_f32 v119,v119,v120\n"
"v_mov_b32 v120,v119\n"
"s_nop 1\n"
"v_permlane32_swap_b32 v120,v119\n"
"v_max_f32 v119,v119,v120\n"
"s_bitcmp1_b32 s90,3\n"
"s_cselect_b32 s82,0xff800000,0\n"
"v_add_f32 v119,0x41980000,v119\n"
"v_max_f32 v119,s82,v119\n"
"v_cmp_lt_f32 vcc,s17,|v119|\n"
"s_nop 0\n"
"s_cbranch_vccz .Lfk_rare0_3\n"
"v_ceil_f32 v119,v119\n"
"v_sub_f32 v24,v24,v119\n"
"v_sub_f32 v25,v25,v119\n"
"v_sub_f32 v26,v26,v119\n"
"v_sub_f32 v27,v27,v119\n"
"v_sub_f32 v28,v28,v119\n"
"v_sub_f32 v29,v29,v119\n"
"v_sub_f32 v30,v30,v119\n"
"v_sub_f32 v31,v31,v119\n"
"v_sub_f32 v108,v108,v119\n"
"v_sub_f32 v109,v109,v119\n"
"v_sub_f32 v110,v110,v119\n"
"v_sub_f32 v111,v111,v119\n"
"s_cbranch_scc1 .Lfk_rare0_3\n"
"v_exp_f32 v119,-v119\n"
"s_nop 0\n"
"v_mul_f32 v224,v224,v119\n"
"v_mul_f32 v225,v225,v119\n"
"v_mul_f32 v226,v226,v119\n"
"v_mul_f32 v227,v227,v119\n"
"v_mul_f32 v228,v228,v119\n"
"v_mul_f32 v229,v229,v119\n"
"v_mul_f32 v230,v230,v119\n"
"v_mul_f32 v231,v231,v119\n"
"v_mul_f32 v232,v232,v119\n"
"v_mul_f32 v233,v233,v119\n"
"v_mul_f32 v234,v234,v119\n"
"v_mul_f32 v235,v235,v119\n"
"v_mul_f32 v236,v236,v119\n"
"v_mul_f32 v237,v237,v119\n"
"v_mul_f32 v238,v238,v119\n"
"v_mul_f32 v239,v239,v119\n"
"v_mul_f32 v240,v240,v119\n"
"v_mul_f32 v241,v241,v119\n"
"v_mul_f32 v242,v242,v119\n"
"v_mul_f32 v243,v243,v119\n"
"v_mul_f32 v244,v244,v119\n"
"v_mul_f32 v245,v245,v119\n"
"v_mul_f32 v246,v246,v119\n"
"v_mul_f32 v247,v247,v119\n"
"v_mul_f32 v248,v248,v119\n"
"v_mul_f32 v249,v249,v119\n"
"v_mul_f32 v250,v250,v119\n"
"v_mul_f32 v251,v251,v119\n"
"v_mul_f32 v252,v252,v119\n"
"v_mul_f32 v253,v253,v119\n"
"v_mul_f32 v254,v254,v119\n"
"v_mul_f32 v255,v255,v119\n"
"v_accvgpr_read_b32 v120,a140\n"
"v_mul_f32 v120,v120,v119\n"
"v_accvgpr_write_b32 a140,v120\n"
"v_lshlrev_b32 v120,16,v92\n"
"v_and_b32 v121,0xffff0000,v92\n"
"v_mul_f32 v120,v120,v119\n"
"v_mul_f32 v121,v121,v119\n"
"v_cvt_pk_bf16_f32 v92,v120,v121\n"
"v_accvgpr_read_b32 v120,a141\n"
"v_mul_f32 v120,v120,v119\n"
"v_accvgpr_write_b32 a141,v120\n"
"v_lshlrev_b32 v120,16,v93\n"
"v_and_b32 v121,0xffff0000,v93\n"
"v_mul_f32 v120,v120,v119\n"
"v_mul_f32 v121,v121,v119\n"
"v_cvt_pk_bf16_f32 v93,v120,v121\n"
"v_accvgpr_read_b32 v120,a142\n"
"v_mul_f32 v120,v120,v119\n"
"v_accvgpr_write_b32 a142,v120\n"
"v_lshlrev_b32 v120,16,v94\n"
"v_and_b32 v121,0xffff0000,v94\n"
"v_mul_f32 v120,v120,v119\n"
"v_mul_f32 v121,v121,v119\n"
"v_cvt_pk_bf16_f32 v94,v120,v121\n"
"v_accvgpr_read_b32 v120,a143\n"
"v_mul_f32 v120,v120,v119\n"
"v_accvgpr_write_b32 a143,v120\n"
"v_lshlrev_b32 v120,16,v95\n"
"v_and_b32 v121,0xffff0000,v95\n"
"v_mul_f32 v120,v120,v119\n"
"v_mul_f32 v121,v121,v119\n"
"v_cvt_pk_bf16_f32 v95,v120,v121\n"
".Lfk_rare0_3:\n"
"s_mov_b32 s90,0\n"
"s_nop 1\n"
"s_setpc_b64 s[94:95]\n"
".Lfk_rare2:\n"
"s_nop 7\n"
"v_max3_f32 v119,v0,v1,v2\n"
"v_max3_f32 v120,v3,v4,v5\n"
"v_max3_f32 v119,v119,v6,v7\n"
"v_max_f32 v119,v119,v120\n"
"v_mov_b32 v120,v119\n"
"s_nop 1\n"
"v_permlane16_swap_b32 v120,v119\n"
"v_max_f32 v119,v119,v120\n"
"v_mov_b32 v120,v119\n"
"s_nop 1\n"
"v_permlane32_swap_b32 v120,v119\n"
"v_max_f32 v119,v119,v120\n"
"s_bitcmp1_b32 s90,0\n"
"s_cselect_b32 s82,0xff800000,0\n"
"v_add_f32 v119,0x41980000,v119\n"
"v_max_f32 v119,s82,v119\n"
"v_cmp_lt_f32 vcc,s17,|v119|\n"
"s_nop 0\n"
"s_cbranch_vccz .Lfk_rare2_0\n"
"v_ceil_f32 v119,v119\n"
"v_sub_f32 v0,v0,v119\n"
"v_sub_f32 v1,v1,v119\n"
"v_sub_f32 v2,v2,v119\n"
"v_sub_f32 v3,v3,v119\n"
"v_sub_f32 v4,v4,v119\n"
"v_sub_f32 v5,v5,v119\n"
"v_sub_f32 v6,v6,v119\n"
"v_sub_f32 v7,v7,v119\n"
"v_sub_f32 v96,v96,v119\n"
"v_sub_f32 v97,v97,v119\n"
"v_sub_f32 v98,v98,v119\n"
"v_sub_f32 v99,v99,v119\n"
"v_sub_f32 v32,v32,v119\n"
"v_sub_f32 v33,v33,v119\n"
"v_sub_f32 v34,v34,v119\n"
"v_sub_f32 v35,v35,v119\n"
"v_sub_f32 v36,v36,v119\n"
"v_sub_f32 v37,v37,v119\n"
"v_sub_f32 v38,v38,v119\n"
"v_sub_f32 v39,v39,v119\n"
"v_exp_f32 v0,v0\n"
"v_exp_f32 v1,v1\n"
"v_exp_f32 v2,v2\n"
"v_exp_f32 v3,v3\n"
"v_exp_f32 v4,v4\n"
"v_exp_f32 v5,v5\n"
"v_exp_f32 v6,v6\n"
"v_exp_f32 v7,v7\n"
"v_cvt_pk_bf16_f32 v64,v0,v1\n"
"v_cvt_pk_bf16_f32 v65,v2,v3\n"
"v_cvt_pk_bf16_f32 v66,v4,v5\n"
"v_cvt_pk_bf16_f32 v67,v6,v7\n"
"s_cbranch_scc1 .Lfk_rare2_0\n"
"v_exp_f32 v119,-v119\n"
"s_nop 0\n"
"v_mul_f32 v128,v128,v119\n"
"v_mul_f32 v129,v129,v119\n"
"v_mul_f32 v130,v130,v119\n"
"v_mul_f32 v131,v131,v119\n"
"v_mul_f32 v132,v132,v119\n"
"v_mul_f32 v133,v133,v119\n"
"v_mul_f32 v134,v134,v119\n"
"v_mul_f32 v135,v135,v119\n"
"v_mul_f32 v136,v136,v119\n"
"v_mul_f32 v137,v137,v119\n"
"v_mul_f32 v138,v138,v119\n"
"v_mul_f32 v139,v139,v119\n"
"v_mul_f32 v140,v140,v119\n"
"v_mul_f32 v141,v141,v119\n"
"v_mul_f32 v142,v142,v119\n"
"v_mul_f32 v143,v143,v119\n"
"v_mul_f32 v144,v144,v119\n"
"v_mul_f32 v145,v145,v119\n"
"v_mul_f32 v146,v146,v119\n"
"v_mul_f32 v147,v147,v119\n"
"v_mul_f32 v148,v148,v119\n"
"v_mul_f32 v149,v149,v119\n"
"v_mul_f32 v150,v150,v119\n"
"v_mul_f32 v151,v151,v119\n"
"v_mul_f32 v152,v152,v119\n"
"v_mul_f32 v153,v153,v119\n"
"v_mul_f32 v154,v154,v119\n"
"v_mul_f32 v155,v155,v119\n"
"v_mul_f32 v156,v156,v119\n"
"v_mul_f32 v157,v157,v119\n"
"v_mul_f32 v158,v158,v119\n"
"v_mul_f32 v159,v159,v119\n"
"v_accvgpr_read_b32 v120,a128\n"
"v_mul_f32 v120,v120,v119\n"
"v_accvgpr_write_b32 a128,v120\n"
"v_accvgpr_read_b32 v120,a129\n"
"v_mul_f32 v120,v120,v119\n"
"v_accvgpr_write_b32 a129,v120\n"
"v_accvgpr_read_b32 v120,a130\n"
"v_mul_f32 v120,v120,v119\n"
"v_accvgpr_write_b32 a130,v120\n"
"v_accvgpr_read_b32 v120,a131\n"
"v_mul_f32 v120,v120,v119\n"
"v_accvgpr_write_b32 a131,v120\n"
".Lfk_rare2_0:\n"
"v_max3_f32 v119,v8,v9,v10\n"
"v_max3_f32 v120,v11,v12,v13\n"
"v_max3_f32 v119,v119,v14,v15\n"
"v_max_f32 v119,v119,v120\n"
"v_mov_b32 v120,v119\n"
"s_nop 1\n"
"v_permlane16_swap_b32 v120,v119\n"
"v_max_f32 v119,v119,v120\n"
"v_mov_b32 v120,v119\n"
"s_nop 1\n"
"v_permlane32_swap_b32 v120,v119\n"
"v_max_f32 v119,v119,v120\n"
"s_bitcmp1_b32 s90,1\n"
"s_cselect_b32 s82,0xff800000,0\n"
"v_add_f32 v119,0x41980000,v119\n"
"v_max_f32 v119,s82,v119\n"
"v_cmp_lt_f32 vcc,s17,|v119|\n"
"s_nop 0\n"
"s_cbranch_vccz .Lfk_rare2_1\n"
"v_ceil_f32 v119,v119\n"
"v_sub_f32 v8,v8,v119\n"
"v_sub_f32 v9,v9,v119\n"
"v_sub_f32 v10,v10,v119\n"
"v_sub_f32 v11,v11,v119\n"
"v_sub_f32 v12,v12,v119\n"
"v_sub_f32 v13,v13,v119\n"
"v_sub_f32 v14,v14,v119\n"
"v_sub_f32 v15,v15,v119\n"
"v_sub_f32 v100,v100,v119\n"
"v_sub_f32 v101,v101,v119\n"
"v_sub_f32 v102,v102,v119\n"
"v_sub_f32 v103,v103,v119\n"
"v_sub_f32 v40,v40,v119\n"
"v_sub_f32 v41,v41,v119\n"
"v_sub_f32 v42,v42,v119\n"
"v_sub_f32 v43,v43,v119\n"
"v_sub_f32 v44,v44,v119\n"
"v_sub_f32 v45,v45,v119\n"
"v_sub_f32 v46,v46,v119\n"
"v_sub_f32 v47,v47,v119\n"
"v_exp_f32 v8,v8\n"
"v_exp_f32 v9,v9\n"
"v_exp_f32 v10,v10\n"
"v_exp_f32 v11,v11\n"
"v_exp_f32 v12,v12\n"
"v_exp_f32 v13,v13\n"
"v_exp_f32 v14,v14\n"
"v_exp_f32 v15,v15\n"
"v_cvt_pk_bf16_f32 v68,v8,v9\n"
"v_cvt_pk_bf16_f32 v69,v10,v11\n"
"v_cvt_pk_bf16_f32 v70,v12,v13\n"
"v_cvt_pk_bf16_f32 v71,v14,v15\n"
"s_cbranch_scc1 .Lfk_rare2_1\n"
"v_exp_f32 v119,-v119\n"
"s_nop 0\n"
"v_mul_f32 v160,v160,v119\n"
"v_mul_f32 v161,v161,v119\n"
"v_mul_f32 v162,v162,v119\n"
"v_mul_f32 v163,v163,v119\n"
"v_mul_f32 v164,v164,v119\n"
"v_mul_f32 v165,v165,v119\n"
"v_mul_f32 v166,v166,v119\n"
"v_mul_f32 v167,v167,v119\n"
"v_mul_f32 v168,v168,v119\n"
"v_mul_f32 v169,v169,v119\n"
"v_mul_f32 v170,v170,v119\n"
"v_mul_f32 v171,v171,v119\n"
"v_mul_f32 v172,v172,v119\n"
"v_mul_f32 v173,v173,v119\n"
"v_mul_f32 v174,v174,v119\n"
"v_mul_f32 v175,v175,v119\n"
"v_mul_f32 v176,v176,v119\n"
"v_mul_f32 v177,v177,v119\n"
"v_mul_f32 v178,v178,v119\n"
"v_mul_f32 v179,v179,v119\n"
"v_mul_f32 v180,v180,v119\n"
"v_mul_f32 v181,v181,v119\n"
"v_mul_f32 v182,v182,v119\n"
"v_mul_f32 v183,v183,v119\n"
"v_mul_f32 v184,v184,v119\n"
"v_mul_f32 v185,v185,v119\n"
"v_mul_f32 v186,v186,v119\n"
"v_mul_f32 v187,v187,v119\n"
"v_mul_f32 v188,v188,v119\n"
"v_mul_f32 v189,v189,v119\n"
"v_mul_f32 v190,v190,v119\n"
"v_mul_f32 v191,v191,v119\n"
"v_accvgpr_read_b32 v120,a132\n"
"v_mul_f32 v120,v120,v119\n"
"v_accvgpr_write_b32 a132,v120\n"
"v_accvgpr_read_b32 v120,a133\n"
"v_mul_f32 v120,v120,v119\n"
"v_accvgpr_write_b32 a133,v120\n"
"v_accvgpr_read_b32 v120,a134\n"
"v_mul_f32 v120,v120,v119\n"
"v_accvgpr_write_b32 a134,v120\n"
"v_accvgpr_read_b32 v120,a135\n"
"v_mul_f32 v120,v120,v119\n"
"v_accvgpr_write_b32 a135,v120\n"
".Lfk_rare2_1:\n"
"v_max3_f32 v119,v16,v17,v18\n"
"v_max3_f32 v120,v19,v20,v21\n"
"v_max3_f32 v119,v119,v22,v23\n"
"v_max_f32 v119,v119,v120\n"
"v_mov_b32 v120,v119\n"
"s_nop 1\n"
"v_permlane16_swap_b32 v120,v119\n"
"v_max_f32 v119,v119,v120\n"
"v_mov_b32 v120,v119\n"
"s_nop 1\n"
"v_permlane32_swap_b32 v120,v119\n"
"v_max_f32 v119,v119,v120\n"
"s_bitcmp1_b32 s90,2\n"
"s_cselect_b32 s82,0xff800000,0\n"
"v_add_f32 v119,0x41980000,v119\n"
"v_max_f32 v119,s82,v119\n"
"v_cmp_lt_f32 vcc,s17,|v119|\n"
"s_nop 0\n"
"s_cbranch_vccz .Lfk_rare2_2\n"
"v_ceil_f32 v119,v119\n"
"v_sub_f32 v16,v16,v119\n"
"v_sub_f32 v17,v17,v119\n"
"v_sub_f32 v18,v18,v119\n"
"v_sub_f32 v19,v19,v119\n"
"v_sub_f32 v20,v20,v119\n"
"v_sub_f32 v21,v21,v119\n"
"v_sub_f32 v22,v22,v119\n"
"v_sub_f32 v23,v23,v119\n"
"v_sub_f32 v104,v104,v119\n"
"v_sub_f32 v105,v105,v119\n"
"v_sub_f32 v106,v106,v119\n"
"v_sub_f32 v107,v107,v119\n"
"v_sub_f32 v48,v48,v119\n"
"v_sub_f32 v49,v49,v119\n"
"v_sub_f32 v50,v50,v119\n"
"v_sub_f32 v51,v51,v119\n"
"v_sub_f32 v52,v52,v119\n"
"v_sub_f32 v53,v53,v119\n"
"v_sub_f32 v54,v54,v119\n"
"v_sub_f32 v55,v55,v119\n"
"v_exp_f32 v16,v16\n"
"v_exp_f32 v17,v17\n"
"v_exp_f32 v18,v18\n"
"v_exp_f32 v19,v19\n"
"v_exp_f32 v20,v20\n"
"v_exp_f32 v21,v21\n"
"v_exp_f32 v22,v22\n"
"v_exp_f32 v23,v23\n"
"v_cvt_pk_bf16_f32 v72,v16,v17\n"
"v_cvt_pk_bf16_f32 v73,v18,v19\n"
"v_cvt_pk_bf16_f32 v74,v20,v21\n"
"v_cvt_pk_bf16_f32 v75,v22,v23\n"
"s_cbranch_scc1 .Lfk_rare2_2\n"
"v_exp_f32 v119,-v119\n"
"s_nop 0\n"
"v_mul_f32 v192,v192,v119\n"
"v_mul_f32 v193,v193,v119\n"
"v_mul_f32 v194,v194,v119\n"
"v_mul_f32 v195,v195,v119\n"
"v_mul_f32 v196,v196,v119\n"
"v_mul_f32 v197,v197,v119\n"
"v_mul_f32 v198,v198,v119\n"
"v_mul_f32 v199,v199,v119\n"
"v_mul_f32 v200,v200,v119\n"
"v_mul_f32 v201,v201,v119\n"
"v_mul_f32 v202,v202,v119\n"
"v_mul_f32 v203,v203,v119\n"
"v_mul_f32 v204,v204,v119\n"
"v_mul_f32 v205,v205,v119\n"
"v_mul_f32 v206,v206,v119\n"
"v_mul_f32 v207,v207,v119\n"
"v_mul_f32 v208,v208,v119\n"
"v_mul_f32 v209,v209,v119\n"
"v_mul_f32 v210,v210,v119\n"
"v_mul_f32 v211,v211,v119\n"
"v_mul_f32 v212,v212,v119\n"
"v_mul_f32 v213,v213,v119\n"
"v_mul_f32 v214,v214,v119\n"
"v_mul_f32 v215,v215,v119\n"
"v_mul_f32 v216,v216,v119\n"
"v_mul_f32 v217,v217,v119\n"
"v_mul_f32 v218,v218,v119\n"
"v_mul_f32 v219,v219,v119\n"
"v_mul_f32 v220,v220,v119\n"
"v_mul_f32 v221,v221,v119\n"
"v_mul_f32 v222,v222,v119\n"
"v_mul_f32 v223,v223,v119\n"
"v_accvgpr_read_b32 v120,a136\n"
"v_mul_f32 v120,v120,v119\n"
"v_accvgpr_write_b32 a136,v120\n"
"v_accvgpr_read_b32 v120,a137\n"
"v_mul_f32 v120,v120,v119\n"
"v_accvgpr_write_b32 a137,v120\n"
"v_accvgpr_read_b32 v120,a138\n"
"v_mul_f32 v120,v120,v119\n"
"v_accvgpr_write_b32 a138,v120\n"
"v_accvgpr_read_b32 v120,a139\n"
"v_mul_f32 v120,v120,v119\n"
"v_accvgpr_write_b32 a139,v120\n"
".Lfk_rare2_2:\n"
"v_max3_f32 v119,v24,v25,v26\n"
"v_max3_f32 v120,v27,v28,v29\n"
"v_max3_f32 v119,v119,v30,v31\n"
"v_max_f32 v119,v119,v120\n"
"v_mov_b32 v120,v119\n"
"s_nop 1\n"
"v_permlane16_swap_b32 v120,v119\n"
"v_max_f32 v119,v119,v120\n"
"v_mov_b32 v120,v119\n"
"s_nop 1\n"
"v_permlane32_swap_b32 v120,v119\n"
"v_max_f32 v119,v119,v120\n"
"s_bitcmp1_b32 s90,3\n"
"s_cselect_b32 s82,0xff800000,0\n"
"v_add_f32 v119,0x41980000,v119\n"
"v_max_f32 v119,s82,v119\n"
"v_cmp_lt_f32 vcc,s17,|v119|\n"
"s_nop 0\n"
"s_cbranch_vccz .Lfk_rare2_3\n"
"v_ceil_f32 v119,v119\n"
"v_sub_f32 v24,v24,v119\n"
"v_sub_f32 v25,v25,v119\n"
"v_sub_f32 v26,v26,v119\n"
"v_sub_f32 v27,v27,v119\n"
"v_sub_f32 v28,v28,v119\n"
"v_sub_f32 v29,v29,v119\n"
"v_sub_f32 v30,v30,v119\n"
"v_sub_f32 v31,v31,v119\n"
"v_sub_f32 v108,v108,v119\n"
"v_sub_f32 v109,v109,v119\n"
"v_sub_f32 v110,v110,v119\n"
"v_sub_f32 v111,v111,v119\n"
"v_sub_f32 v56,v56,v119\n"
"v_sub_f32 v57,v57,v119\n"
"v_sub_f32 v58,v58,v119\n"
"v_sub_f32 v59,v59,v119\n"
"v_sub_f32 v60,v60,v119\n"
"v_sub_f32 v61,v61,v119\n"
"v_sub_f32 v62,v62,v119\n"
"v_sub_f32 v63,v63,v119\n"
"v_exp_f32 v24,v24\n"
"v_exp_f32 v25,v25\n"
"v_exp_f32 v26,v26\n"
"v_exp_f32 v27,v27\n"
"v_exp_f32 v28,v28\n"
"v_exp_f32 v29,v29\n"
"v_exp_f32 v30,v30\n"
"v_exp_f32 v31,v31\n"
"v_cvt_pk_bf16_f32 v76,v24,v25\n"
"v_cvt_pk_bf16_f32 v77,v26,v27\n"
"v_cvt_pk_bf16_f32 v78,v28,v29\n"
"v_cvt_pk_bf16_f32 v79,v30,v31\n"
"s_cbranch_scc1 .Lfk_rare2_3\n"
"v_exp_f32 v119,-v119\n"
"s_nop 0\n"
"v_mul_f32 v224,v224,v119\n"
"v_mul_f32 v225,v225,v119\n"
"v_mul_f32 v226,v226,v119\n"
"v_mul_f32 v227,v227,v119\n"
"v_mul_f32 v228,v228,v119\n"
"v_mul_f32 v229,v229,v119\n"
"v_mul_f32 v230,v230,v119\n"
"v_mul_f32 v231,v231,v119\n"
"v_mul_f32 v232,v232,v119\n"
"v_mul_f32 v233,v233,v119\n"
"v_mul_f32 v234,v234,v119\n"
"v_mul_f32 v235,v235,v119\n"
"v_mul_f32 v236,v236,v119\n"
"v_mul_f32 v237,v237,v119\n"
"v_mul_f32 v238,v238,v119\n"
"v_mul_f32 v239,v239,v119\n"
"v_mul_f32 v240,v240,v119\n"
"v_mul_f32 v241,v241,v119\n"
"v_mul_f32 v242,v242,v119\n"
"v_mul_f32 v243,v243,v119\n"
"v_mul_f32 v244,v244,v119\n"
"v_mul_f32 v245,v245,v119\n"
"v_mul_f32 v246,v246,v119\n"
"v_mul_f32 v247,v247,v119\n"
"v_mul_f32 v248,v248,v119\n"
"v_mul_f32 v249,v249,v119\n"
"v_mul_f32 v250,v250,v119\n"
"v_mul_f32 v251,v251,v119\n"
"v_mul_f32 v252,v252,v119\n"
"v_mul_f32 v253,v253,v119\n"
"v_mul_f32 v254,v254,v119\n"
"v_mul_f32 v255,v255,v119\n"
"v_accvgpr_read_b32 v120,a140\n"
"v_mul_f32 v120,v120,v119\n"
"v_accvgpr_write_b32 a140,v120\n"
"v_accvgpr_read_b32 v120,a141\n"
"v_mul_f32 v120,v120,v119\n"
"v_accvgpr_write_b32 a141,v120\n"
"v_accvgpr_read_b32 v120,a142\n"
"v_mul_f32 v120,v120,v119\n"
"v_accvgpr_write_b32 a142,v120\n"
"v_accvgpr_read_b32 v120,a143\n"
"v_mul_f32 v120,v120,v119\n"
"v_accvgpr_write_b32 a143,v120\n"
".Lfk_rare2_3:\n"
"s_mov_b32 s90,0\n"
"s_nop 1\n"
"s_setpc_b64 s[94:95]\n"
".Lfk_mslow0:\n"
"s_nop 7\n"
"v_min_f32 v119,v98,v104\n"
"v_sub_f32 v120,v119,v104\n"
"v_sub_f32 v119,v119,v98\n"
"v_exp_f32 v120,v120\n"
"v_exp_f32 v119,v119\n"
"v_mul_f32 v192,v192,v120\n"
"v_mul_f32 v193,v193,v120\n"
"v_mul_f32 v194,v194,v120\n"
"v_mul_f32 v195,v195,v120\n"
"v_mul_f32 v196,v196,v120\n"
"v_mul_f32 v197,v197,v120\n"
"v_mul_f32 v198,v198,v120\n"
"v_mul_f32 v199,v199,v120\n"
"v_mul_f32 v200,v200,v120\n"
"v_mul_f32 v201,v201,v120\n"
"v_mul_f32 v202,v202,v120\n"
"v_mul_f32 v203,v203,v120\n"
"v_mul_f32 v204,v204,v120\n"
"v_mul_f32 v205,v205,v120\n"
"v_mul_f32 v206,v206,v120\n"
"v_mul_f32 v207,v207,v120\n"
"v_mul_f32 v208,v208,v120\n"
"v_mul_f32 v209,v209,v120\n"
"v_mul_f32 v210,v210,v120\n"
"v_mul_f32 v211,v211,v120\n"
"v_mul_f32 v212,v212,v120\n"
"v_mul_f32 v213,v213,v120\n"
"v_mul_f32 v214,v214,v120\n"
"v_mul_f32 v215,v215,v120\n"
"v_mul_f32 v216,v216,v120\n"
"v_mul_f32 v217,v217,v120\n"
"v_mul_f32 v218,v218,v120\n"
"v_mul_f32 v219,v219,v120\n"
"v_mul_f32 v220,v220,v120\n"
"v_mul_f32 v221,v221,v120\n"
"v_mul_f32 v222,v222,v120\n"
"v_mul_f32 v223,v223,v120\n"
"v_accvgpr_read_b32 v121,a136\n"
"v_mul_f32 v121,v121,v120\n"
"v_accvgpr_write_b32 a136,v121\n"
"v_mul_f32 v99,v99,v119\n"
"v_accvgpr_read_b32 v121,a64\n"
"v_lshlrev_b32 v120,16,v121\n"
"v_and_b32 v121,0xffff0000,v121\n"
"v_mul_f32 v120,v120,v119\n"
"v_mul_f32 v121,v121,v119\n"
"v_cvt_pk_bf16_f32 v121,v120,v121\n"
"v_accvgpr_write_b32 a64,v121\n"
"v_accvgpr_read_b32 v121,a65\n"
"v_lshlrev_b32 v120,16,v121\n"
"v_and_b32 v121,0xffff0000,v121\n"
"v_mul_f32 v120,v120,v119\n"
"v_mul_f32 v121,v121,v119\n"
"v_cvt_pk_bf16_f32 v121,v120,v121\n"
"v_accvgpr_write_b32 a65,v121\n"
"v_accvgpr_read_b32 v121,a66\n"
"v_lshlrev_b32 v120,16,v121\n"
"v_and_b32 v121,0xffff0000,v121\n"
"v_mul_f32 v120,v120,v119\n"
"v_mul_f32 v121,v121,v119\n"
"v_cvt_pk_bf16_f32 v121,v120,v121\n"
"v_accvgpr_write_b32 a66,v121\n"
"v_accvgpr_read_b32 v121,a67\n"
"v_lshlrev_b32 v120,16,v121\n"
"v_and_b32 v121,0xffff0000,v121\n"
"v_mul_f32 v120,v120,v119\n"
"v_mul_f32 v121,v121,v119\n"
"v_cvt_pk_bf16_f32 v121,v120,v121\n"
"v_accvgpr_write_b32 a67,v121\n"
"v_accvgpr_read_b32 v121,a68\n"
"v_lshlrev_b32 v120,16,v121\n"
"v_and_b32 v121,0xffff0000,v121\n"
"v_mul_f32 v120,v120,v119\n"
"v_mul_f32 v121,v121,v119\n"
"v_cvt_pk_bf16_f32 v121,v120,v121\n"
"v_accvgpr_write_b32 a68,v121\n"
"v_accvgpr_read_b32 v121,a69\n"
"v_lshlrev_b32 v120,16,v121\n"
"v_and_b32 v121,0xffff0000,v121\n"
"v_mul_f32 v120,v120,v119\n"
"v_mul_f32 v121,v121,v119\n"
"v_cvt_pk_bf16_f32 v121,v120,v121\n"
"v_accvgpr_write_b32 a69,v121\n"
"v_accvgpr_read_b32 v121,a70\n"
"v_lshlrev_b32 v120,16,v121\n"
"v_and_b32 v121,0xffff0000,v121\n"
"v_mul_f32 v120,v120,v119\n"
"v_mul_f32 v121,v121,v119\n"
"v_cvt_pk_bf16_f32 v121,v120,v121\n"
"v_accvgpr_write_b32 a70,v121\n"
"v_accvgpr_read_b32 v121,a71\n"
"v_lshlrev_b32 v120,16,v121\n"
"v_and_b32 v121,0xffff0000,v121\n"
"v_mul_f32 v120,v120,v119\n"
"v_mul_f32 v121,v121,v119\n"
"v_cvt_pk_bf16_f32 v121,v120,v121\n"
"v_accvgpr_write_b32 a71,v121\n"
"v_accvgpr_read_b32 v121,a72\n"
"v_lshlrev_b32 v120,16,v121\n"
"v_and_b32 v121,0xffff0000,v121\n"
"v_mul_f32 v120,v120,v119\n"
"v_mul_f32 v121,v121,v119\n"
"v_cvt_pk_bf16_f32 v121,v120,v121\n"
"v_accvgpr_write_b32 a72,v121\n"
"v_accvgpr_read_b32 v121,a73\n"
"v_lshlrev_b32 v120,16,v121\n"
"v_and_b32 v121,0xffff0000,v121\n"
"v_mul_f32 v120,v120,v119\n"
"v_mul_f32 v121,v121,v119\n"
"v_cvt_pk_bf16_f32 v121,v120,v121\n"
"v_accvgpr_write_b32 a73,v121\n"
"v_accvgpr_read_b32 v121,a74\n"
"v_lshlrev_b32 v120,16,v121\n"
"v_and_b32 v121,0xffff0000,v121\n"
"v_mul_f32 v120,v120,v119\n"
"v_mul_f32 v121,v121,v119\n"
"v_cvt_pk_bf16_f32 v121,v120,v121\n"
"v_accvgpr_write_b32 a74,v121\n"
"v_accvgpr_read_b32 v121,a75\n"
"v_lshlrev_b32 v120,16,v121\n"
"v_and_b32 v121,0xffff0000,v121\n"
"v_mul_f32 v120,v120,v119\n"
"v_mul_f32 v121,v121,v119\n"
"v_cvt_pk_bf16_f32 v121,v120,v121\n"
"v_accvgpr_write_b32 a75,v121\n"
"v_accvgpr_read_b32 v121,a76\n"
"v_lshlrev_b32 v120,16,v121\n"
"v_and_b32 v121,0xffff0000,v121\n"
"v_mul_f32 v120,v120,v119\n"
"v_mul_f32 v121,v121,v119\n"
"v_cvt_pk_bf16_f32 v121,v120,v121\n"
"v_accvgpr_write_b32 a76,v121\n"
"v_accvgpr_read_b32 v121,a77\n"
"v_lshlrev_b32 v120,16,v121\n"
"v_and_b32 v121,0xffff0000,v121\n"
"v_mul_f32 v120,v120,v119\n"
"v_mul_f32 v121,v121,v119\n"
"v_cvt_pk_bf16_f32 v121,v120,v121\n"
"v_accvgpr_write_b32 a77,v121\n"
"v_accvgpr_read_b32 v121,a78\n"
"v_lshlrev_b32 v120,16,v121\n"
"v_and_b32 v121,0xffff0000,v121\n"
"v_mul_f32 v120,v120,v119\n"
"v_mul_f32 v121,v121,v119\n"
"v_cvt_pk_bf16_f32 v121,v120,v121\n"
"v_accvgpr_write_b32 a78,v121\n"
"v_accvgpr_read_b32 v121,a79\n"
"v_lshlrev_b32 v120,16,v121\n"
"v_and_b32 v121,0xffff0000,v121\n"
"v_mul_f32 v120,v120,v119\n"
"v_mul_f32 v121,v121,v119\n"
"v_cvt_pk_bf16_f32 v121,v120,v121\n"
"v_accvgpr_write_b32 a79,v121\n"
"s_nop 1\n"
"s_setpc_b64 s[94:95]\n"
".Lfk_rare1:\n"
"s_nop 7\n"
"s_bitcmp1_b32 s91,0\n"
"s_cbranch_scc0 .Lfk_rare1_0\n"
"v_max3_f32 v119,v32,v33,v34\n"
"v_max3_f32 v120,v35,v36,v37\n"
"v_max3_f32 v119,v119,v38,v39\n"
"v_max_f32 v119,v119,v120\n"
"v_mov_b32 v120,v119\n"
"s_nop 1\n"
"v_permlane16_swap_b32 v120,v119\n"
"v_max_f32 v119,v119,v120\n"
"v_mov_b32 v120,v119\n"
"s_nop 1\n"
"v_permlane32_swap_b32 v120,v119\n"
"v_max_f32 v119,v119,v120\n"
"s_bitcmp1_b32 s90,0\n"
"s_cselect_b32 s82,0xff800000,0\n"
"v_add_f32 v119,0x41980000,v119\n"
"v_max_f32 v119,s82,v119\n"
"v_cmp_lt_f32 vcc,s17,|v119|\n"
"s_nop 0\n"
"s_cbranch_vccz .Lfk_rare1_0\n"
"v_ceil_f32 v119,v119\n"
"v_sub_f32 v32,v32,v119\n"
"v_sub_f32 v33,v33,v119\n"
"v_sub_f32 v34,v34,v119\n"
"v_sub_f32 v35,v35,v119\n"
"v_sub_f32 v36,v36,v119\n"
"v_sub_f32 v37,v37,v119\n"
"v_sub_f32 v38,v38,v119\n"
"v_sub_f32 v39,v39,v119\n"
"v_sub_f32 v96,v96,v119\n"
"v_sub_f32 v97,v97,v119\n"
"v_sub_f32 v98,v98,v119\n"
"v_sub_f32 v99,v99,v119\n"
"s_cbranch_scc1 .Lfk_rare1_0\n"
"v_exp_f32 v119,-v119\n"
"s_nop 0\n"
"v_mul_f32 v128,v128,v119\n"
"v_mul_f32 v129,v129,v119\n"
"v_mul_f32 v130,v130,v119\n"
"v_mul_f32 v131,v131,v119\n"
"v_mul_f32 v132,v132,v119\n"
"v_mul_f32 v133,v133,v119\n"
"v_mul_f32 v134,v134,v119\n"
"v_mul_f32 v135,v135,v119\n"
"v_mul_f32 v136,v136,v119\n"
"v_mul_f32 v137,v137,v119\n"
"v_mul_f32 v138,v138,v119\n"
"v_mul_f32 v139,v139,v119\n"
"v_mul_f32 v140,v140,v119\n"
"v_mul_f32 v141,v141,v119\n"
"v_mul_f32 v142,v142,v119\n"
"v_mul_f32 v143,v143,v119\n"
"v_mul_f32 v144,v144,v119\n"
"v_mul_f32 v145,v145,v119\n"
"v_mul_f32 v146,v146,v119\n"
"v_mul_f32 v147,v147,v119\n"
"v_mul_f32 v148,v148,v119\n"
"v_mul_f32 v149,v149,v119\n"
"v_mul_f32 v150,v150,v119\n"
"v_mul_f32 v151,v151,v119\n"
"v_mul_f32 v152,v152,v119\n"
"v_mul_f32 v153,v153,v119\n"
"v_mul_f32 v154,v154,v119\n"
"v_mul_f32 v155,v155,v119\n"
"v_mul_f32 v156,v156,v119\n"
"v_mul_f32 v157,v157,v119\n"
"v_mul_f32 v158,v158,v119\n"
"v_mul_f32 v159,v159,v119\n"
"v_accvgpr_read_b32 v120,a128\n"
"v_mul_f32 v120,v120,v119\n"
"v_accvgpr_write_b32 a128,v120\n"
"v_lshlrev_b32 v120,16,v64\n"
"v_and_b32 v121,0xffff0000,v64\n"
"v_mul_f32 v120,v120,v119\n"
"v_mul_f32 v121,v121,v119\n"
"v_cvt_pk_bf16_f32 v64,v120,v121\n"
"v_accvgpr_read_b32 v120,a129\n"
"v_mul_f32 v120,v120,v119\n"
"v_accvgpr_write_b32 a129,v120\n"
"v_lshlrev_b32 v120,16,v65\n"
"v_and_b32 v121,0xffff0000,v65\n"
"v_mul_f32 v120,v120,v119\n"
"v_mul_f32 v121,v121,v119\n"
"v_cvt_pk_bf16_f32 v65,v120,v121\n"
"v_accvgpr_read_b32 v120,a130\n"
"v_mul_f32 v120,v120,v119\n"
"v_accvgpr_write_b32 a130,v120\n"
"v_lshlrev_b32 v120,16,v66\n"
"v_and_b32 v121,0xffff0000,v66\n"
"v_mul_f32 v120,v120,v119\n"
"v_mul_f32 v121,v121,v119\n"
"v_cvt_pk_bf16_f32 v66,v120,v121\n"
"v_accvgpr_read_b32 v120,a131\n"
"v_mul_f32 v120,v120,v119\n"
"v_accvgpr_write_b32 a131,v120\n"
"v_lshlrev_b32 v120,16,v67\n"
"v_and_b32 v121,0xffff0000,v67\n"
"v_mul_f32 v120,v120,v119\n"
"v_mul_f32 v121,v121,v119\n"
"v_cvt_pk_bf16_f32 v67,v120,v121\n"
".Lfk_rare1_0:\n"
"s_bitcmp1_b32 s91,1\n"
"s_cbranch_scc0 .Lfk_rare1_1\n"
"v_max3_f32 v119,v40,v41,v42\n"
"v_max3_f32 v120,v43,v44,v45\n"
"v_max3_f32 v119,v119,v46,v47\n"
"v_max_f32 v119,v119,v120\n"
"v_mov_b32 v120,v119\n"
"s_nop 1\n"
"v_permlane16_swap_b32 v120,v119\n"
"v_max_f32 v119,v119,v120\n"
"v_mov_b32 v120,v119\n"
"s_nop 1\n"
"v_permlane32_swap_b32 v120,v119\n"
"v_max_f32 v119,v119,v120\n"
"s_bitcmp1_b32 s90,1\n"
"s_cselect_b32 s82,0xff800000,0\n"
"v_add_f32 v119,0x41980000,v119\n"
"v_max_f32 v119,s82,v119\n"
"v_cmp_lt_f32 vcc,s17,|v119|\n"
"s_nop 0\n"
"s_cbranch_vccz .Lfk_rare1_1\n"
"v_ceil_f32 v119,v119\n"
"v_sub_f32 v40,v40,v119\n"
"v_sub_f32 v41,v41,v119\n"
"v_sub_f32 v42,v42,v119\n"
"v_sub_f32 v43,v43,v119\n"
"v_sub_f32 v44,v44,v119\n"
"v_sub_f32 v45,v45,v119\n"
"v_sub_f32 v46,v46,v119\n"
"v_sub_f32 v47,v47,v119\n"
"v_sub_f32 v100,v100,v119\n"
"v_sub_f32 v101,v101,v119\n"
"v_sub_f32 v102,v102,v119\n"
"v_sub_f32 v103,v103,v119\n"
"s_cbranch_scc1 .Lfk_rare1_1\n"
"v_exp_f32 v119,-v119\n"
"s_nop 0\n"
"v_mul_f32 v160,v160,v119\n"
"v_mul_f32 v161,v161,v119\n"
"v_mul_f32 v162,v162,v119\n"
"v_mul_f32 v163,v163,v119\n"
"v_mul_f32 v164,v164,v119\n"
"v_mul_f32 v165,v165,v119\n"
"v_mul_f32 v166,v166,v119\n"
"v_mul_f32 v167,v167,v119\n"
"v_mul_f32 v168,v168,v119\n"
"v_mul_f32 v169,v169,v119\n"
"v_mul_f32 v170,v170,v119\n"
"v_mul_f32 v171,v171,v119\n"
"v_mul_f32 v172,v172,v119\n"
"v_mul_f32 v173,v173,v119\n"
"v_mul_f32 v174,v174,v119\n"
"v_mul_f32 v175,v175,v119\n"
"v_mul_f32 v176,v176,v119\n"
"v_mul_f32 v177,v177,v119\n"
"v_mul_f32 v178,v178,v119\n"
"v_mul_f32 v179,v179,v119\n"
"v_mul_f32 v180,v180,v119\n"
"v_mul_f32 v181,v181,v119\n"
"v_mul_f32 v182,v182,v119\n"
"v_mul_f32 v183,v183,v119\n"
"v_mul_f32 v184,v184,v119\n"
"v_mul_f32 v185,v185,v119\n"
"v_mul_f32 v186,v186,v119\n"
"v_mul_f32 v187,v187,v119\n"
"v_mul_f32 v188,v188,v119\n"
"v_mul_f32 v189,v189,v119\n"
"v_mul_f32 v190,v190,v119\n"
"v_mul_f32 v191,v191,v119\n"
"v_accvgpr_read_b32 v120,a132\n"
"v_mul_f32 v120,v120,v119\n"
"v_accvgpr_write_b32 a132,v120\n"
"v_lshlrev_b32 v120,16,v68\n"
"v_and_b32 v121,0xffff0000,v68\n"
"v_mul_f32 v120,v120,v119\n"
"v_mul_f32 v121,v121,v119\n"
"v_cvt_pk_bf16_f32 v68,v120,v121\n"
"v_accvgpr_read_b32 v120,a133\n"
"v_mul_f32 v120,v120,v119\n"
"v_accvgpr_write_b32 a133,v120\n"
"v_lshlrev_b32 v120,16,v69\n"
"v_and_b32 v121,0xffff0000,v69\n"
"v_mul_f32 v120,v120,v119\n"
"v_mul_f32 v121,v121,v119\n"
"v_cvt_pk_bf16_f32 v69,v120,v121\n"
"v_accvgpr_read_b32 v120,a134\n"
"v_mul_f32 v120,v120,v119\n"
"v_accvgpr_write_b32 a134,v120\n"
"v_lshlrev_b32 v120,16,v70\n"
"v_and_b32 v121,0xffff0000,v70\n"
"v_mul_f32 v120,v120,v119\n"
"v_mul_f32 v121,v121,v119\n"
"v_cvt_pk_bf16_f32 v70,v120,v121\n"
"v_accvgpr_read_b32 v120,a135\n"
"v_mul_f32 v120,v120,v119\n"
"v_accvgpr_write_b32 a135,v120\n"
"v_lshlrev_b32 v120,16,v71\n"
"v_and_b32 v121,0xffff0000,v71\n"
"v_mul_f32 v120,v120,v119\n"
"v_mul_f32 v121,v121,v119\n"
"v_cvt_pk_bf16_f32 v71,v120,v121\n"
".Lfk_rare1_1:\n"
"s_bitcmp1_b32 s91,2\n"
"s_cbranch_scc0 .Lfk_rare1_2\n"
"v_max3_f32 v119,v48,v49,v50\n"
"v_max3_f32 v120,v51,v52,v53\n"
"v_max3_f32 v119,v119,v54,v55\n"
"v_max_f32 v119,v119,v120\n"
"v_mov_b32 v120,v119\n"
"s_nop 1\n"
"v_permlane16_swap_b32 v120,v119\n"
"v_max_f32 v119,v119,v120\n"
"v_mov_b32 v120,v119\n"
"s_nop 1\n"
"v_permlane32_swap_b32 v120,v119\n"
"v_max_f32 v119,v119,v120\n"
"s_bitcmp1_b32 s90,2\n"
"s_cselect_b32 s82,0xff800000,0\n"
"v_add_f32 v119,0x41980000,v119\n"
"v_max_f32 v119,s82,v119\n"
"v_cmp_lt_f32 vcc,s17,|v119|\n"
"s_nop 0\n"
"s_cbranch_vccz .Lfk_rare1_2\n"
"v_ceil_f32 v119,v119\n"
"v_sub_f32 v48,v48,v119\n"
"v_sub_f32 v49,v49,v119\n"
"v_sub_f32 v50,v50,v119\n"
"v_sub_f32 v51,v51,v119\n"
"v_sub_f32 v52,v52,v119\n"
"v_sub_f32 v53,v53,v119\n"
"v_sub_f32 v54,v54,v119\n"
"v_sub_f32 v55,v55,v119\n"
"v_sub_f32 v104,v104,v119\n"
"v_sub_f32 v105,v105,v119\n"
"v_sub_f32 v106,v106,v119\n"
"v_sub_f32 v107,v107,v119\n"
"s_cbranch_scc1 .Lfk_rare1_2\n"
"v_exp_f32 v119,-v119\n"
"s_nop 0\n"
"v_mul_f32 v192,v192,v119\n"
"v_mul_f32 v193,v193,v119\n"
"v_mul_f32 v194,v194,v119\n"
"v_mul_f32 v195,v195,v119\n"
"v_mul_f32 v196,v196,v119\n"
"v_mul_f32 v197,v197,v119\n"
"v_mul_f32 v198,v198,v119\n"
"v_mul_f32 v199,v199,v119\n"
"v_mul_f32 v200,v200,v119\n"
"v_mul_f32 v201,v201,v119\n"
"v_mul_f32 v202,v202,v119\n"
"v_mul_f32 v203,v203,v119\n"
"v_mul_f32 v204,v204,v119\n"
"v_mul_f32 v205,v205,v119\n"
"v_mul_f32 v206,v206,v119\n"
"v_mul_f32 v207,v207,v119\n"
"v_mul_f32 v208,v208,v119\n"
"v_mul_f32 v209,v209,v119\n"
"v_mul_f32 v210,v210,v119\n"
"v_mul_f32 v211,v211,v119\n"
"v_mul_f32 v212,v212,v119\n"
"v_mul_f32 v213,v213,v119\n"
"v_mul_f32 v214,v214,v119\n"
"v_mul_f32 v215,v215,v119\n"
"v_mul_f32 v216,v216,v119\n"
"v_mul_f32 v217,v217,v119\n"
"v_mul_f32 v218,v218,v119\n"
"v_mul_f32 v219,v219,v119\n"
"v_mul_f32 v220,v220,v119\n"
"v_mul_f32 v221,v221,v119\n"
"v_mul_f32 v222,v222,v119\n"
"v_mul_f32 v223,v223,v119\n"
"v_accvgpr_read_b32 v120,a136\n"
"v_mul_f32 v120,v120,v119\n"
"v_accvgpr_write_b32 a136,v120\n"
"v_lshlrev_b32 v120,16,v72\n"
"v_and_b32 v121,0xffff0000,v72\n"
"v_mul_f32 v120,v120,v119\n"
"v_mul_f32 v121,v121,v119\n"
"v_cvt_pk_bf16_f32 v72,v120,v121\n"
"v_accvgpr_read_b32 v120,a137\n"
"v_mul_f32 v120,v120,v119\n"
"v_accvgpr_write_b32 a137,v120\n"
"v_lshlrev_b32 v120,16,v73\n"
"v_and_b32 v121,0xffff0000,v73\n"
"v_mul_f32 v120,v120,v119\n"
"v_mul_f32 v121,v121,v119\n"
"v_cvt_pk_bf16_f32 v73,v120,v121\n"
"v_accvgpr_read_b32 v120,a138\n"
"v_mul_f32 v120,v120,v119\n"
"v_accvgpr_write_b32 a138,v120\n"
"v_lshlrev_b32 v120,16,v74\n"
"v_and_b32 v121,0xffff0000,v74\n"
"v_mul_f32 v120,v120,v119\n"
"v_mul_f32 v121,v121,v119\n"
"v_cvt_pk_bf16_f32 v74,v120,v121\n"
"v_accvgpr_read_b32 v120,a139\n"
"v_mul_f32 v120,v120,v119\n"
"v_accvgpr_write_b32 a139,v120\n"
"v_lshlrev_b32 v120,16,v75\n"
"v_and_b32 v121,0xffff0000,v75\n"
"v_mul_f32 v120,v120,v119\n"
"v_mul_f32 v121,v121,v119\n"
"v_cvt_pk_bf16_f32 v75,v120,v121\n"
".Lfk_rare1_2:\n"
"s_bitcmp1_b32 s91,3\n"
"s_cbranch_scc0 .Lfk_rare1_3\n"
"v_max3_f32 v119,v56,v57,v58\n"
"v_max3_f32 v120,v59,v60,v61\n"
"v_max3_f32 v119,v119,v62,v63\n"
"v_max_f32 v119,v119,v120\n"
"v_mov_b32 v120,v119\n"
"s_nop 1\n"
"v_permlane16_swap_b32 v120,v119\n"
"v_max_f32 v119,v119,v120\n"
"v_mov_b32 v120,v119\n"
"s_nop 1\n"
"v_permlane32_swap_b32 v120,v119\n"
"v_max_f32 v119,v119,v120\n"
"s_bitcmp1_b32 s90,3\n"
"s_cselect_b32 s82,0xff800000,0\n"
"v_add_f32 v119,0x41980000,v119\n"
"v_max_f32 v119,s82,v119\n"
"v_cmp_lt_f32 vcc,s17,|v119|\n"
"s_nop 0\n"
"s_cbranch_vccz .Lfk_rare1_3\n"
"v_ceil_f32 v119,v119\n"
"v_sub_f32 v56,v56,v119\n"
"v_sub_f32 v57,v57,v119\n"
"v_sub_f32 v58,v58,v119\n"
"v_sub_f32 v59,v59,v119\n"
"v_sub_f32 v60,v60,v119\n"
"v_sub_f32 v61,v61,v119\n"
"v_sub_f32 v62,v62,v119\n"
"v_sub_f32 v63,v63,v119\n"
"v_sub_f32 v108,v108,v119\n"
"v_sub_f32 v109,v109,v119\n"
"v_sub_f32 v110,v110,v119\n"
"v_sub_f32 v111,v111,v119\n"
"s_cbranch_scc1 .Lfk_rare1_3\n"
"v_exp_f32 v119,-v119\n"
"s_nop 0\n"
"v_mul_f32 v224,v224,v119\n"
"v_mul_f32 v225,v225,v119\n"
"v_mul_f32 v226,v226,v119\n"
"v_mul_f32 v227,v227,v119\n"
"v_mul_f32 v228,v228,v119\n"
"v_mul_f32 v229,v229,v119\n"
"v_mul_f32 v230,v230,v119\n"
"v_mul_f32 v231,v231,v119\n"
"v_mul_f32 v232,v232,v119\n"
"v_mul_f32 v233,v233,v119\n"
"v_mul_f32 v234,v234,v119\n"
"v_mul_f32 v235,v235,v119\n"
"v_mul_f32 v236,v236,v119\n"
"v_mul_f32 v237,v237,v119\n"
"v_mul_f32 v238,v238,v119\n"
"v_mul_f32 v239,v239,v119\n"
"v_mul_f32 v240,v240,v119\n"
"v_mul_f32 v241,v241,v119\n"
"v_mul_f32 v242,v242,v119\n"
"v_mul_f32 v243,v243,v119\n"
"v_mul_f32 v244,v244,v119\n"
"v_mul_f32 v245,v245,v119\n"
"v_mul_f32 v246,v246,v119\n"
"v_mul_f32 v247,v247,v119\n"
"v_mul_f32 v248,v248,v119\n"
"v_mul_f32 v249,v249,v119\n"
"v_mul_f32 v250,v250,v119\n"
"v_mul_f32 v251,v251,v119\n"
"v_mul_f32 v252,v252,v119\n"
"v_mul_f32 v253,v253,v119\n"
"v_mul_f32 v254,v254,v119\n"
"v_mul_f32 v255,v255,v119\n"
"v_accvgpr_read_b32 v120,a140\n"
"v_mul_f32 v120,v120,v119\n"
"v_accvgpr_write_b32 a140,v120\n"
"v_lshlrev_b32 v120,16,v76\n"
"v_and_b32 v121,0xffff0000,v76\n"
"v_mul_f32 v120,v120,v119\n"
"v_mul_f32 v121,v121,v119\n"
"v_cvt_pk_bf16_f32 v76,v120,v121\n"
"v_accvgpr_read_b32 v120,a141\n"
"v_mul_f32 v120,v120,v119\n"
"v_accvgpr_write_b32 a141,v120\n"
"v_lshlrev_b32 v120,16,v77\n"
"v_and_b32 v121,0xffff0000,v77\n"
"v_mul_f32 v120,v120,v119\n"
"v_mul_f32 v121,v121,v119\n"
"v_cvt_pk_bf16_f32 v77,v120,v121\n"
"v_accvgpr_read_b32 v120,a142\n"
"v_mul_f32 v120,v120,v119\n"
"v_accvgpr_write_b32 a142,v120\n"
"v_lshlrev_b32 v120,16,v78\n"
"v_and_b32 v121,0xffff0000,v78\n"
"v_mul_f32 v120,v120,v119\n"
"v_mul_f32 v121,v121,v119\n"
"v_cvt_pk_bf16_f32 v78,v120,v121\n"
"v_accvgpr_read_b32 v120,a143\n"
"v_mul_f32 v120,v120,v119\n"
"v_accvgpr_write_b32 a143,v120\n"
"v_lshlrev_b32 v120,16,v79\n"
"v_and_b32 v121,0xffff0000,v79\n"
"v_mul_f32 v120,v120,v119\n"
"v_mul_f32 v121,v121,v119\n"
"v_cvt_pk_bf16_f32 v79,v120,v121\n"
".Lfk_rare1_3:\n"
"s_mov_b32 s90,0\n"
"s_nop 1\n"
"s_setpc_b64 s[94:95]\n"
".Lfk_rare3:\n"
"s_nop 7\n"
"v_max3_f32 v119,v32,v33,v34\n"
"v_max3_f32 v120,v35,v36,v37\n"
"v_max3_f32 v119,v119,v38,v39\n"
"v_max_f32 v119,v119,v120\n"
"v_mov_b32 v120,v119\n"
"s_nop 1\n"
"v_permlane16_swap_b32 v120,v119\n"
"v_max_f32 v119,v119,v120\n"
"v_mov_b32 v120,v119\n"
"s_nop 1\n"
"v_permlane32_swap_b32 v120,v119\n"
"v_max_f32 v119,v119,v120\n"
"s_bitcmp1_b32 s90,0\n"
"s_cselect_b32 s82,0xff800000,0\n"
"v_add_f32 v119,0x41980000,v119\n"
"v_max_f32 v119,s82,v119\n"
"v_cmp_lt_f32 vcc,s17,|v119|\n"
"s_nop 0\n"
"s_cbranch_vccz .Lfk_rare3_0\n"
"v_ceil_f32 v119,v119\n"
"v_sub_f32 v32,v32,v119\n"
"v_sub_f32 v33,v33,v119\n"
"v_sub_f32 v34,v34,v119\n"
"v_sub_f32 v35,v35,v119\n"
"v_sub_f32 v36,v36,v119\n"
"v_sub_f32 v37,v37,v119\n"
"v_sub_f32 v38,v38,v119\n"
"v_sub_f32 v39,v39,v119\n"
"v_sub_f32 v96,v96,v119\n"
"v_sub_f32 v97,v97,v119\n"
"v_sub_f32 v98,v98,v119\n"
"v_sub_f32 v99,v99,v119\n"
"v_sub_f32 v0,v0,v119\n"
"v_sub_f32 v1,v1,v119\n"
"v_sub_f32 v2,v2,v119\n"
"v_sub_f32 v3,v3,v119\n"
"v_sub_f32 v4,v4,v119\n"
"v_sub_f32 v5,v5,v119\n"
"v_sub_f32 v6,v6,v119\n"
"v_sub_f32 v7,v7,v119\n"
"v_exp_f32 v32,v32\n"
"v_exp_f32 v33,v33\n"
"v_exp_f32 v34,v34\n"
"v_exp_f32 v35,v35\n"
"v_exp_f32 v36,v36\n"
"v_exp_f32 v37,v37\n"
"v_exp_f32 v38,v38\n"
"v_exp_f32 v39,v39\n"
"v_cvt_pk_bf16_f32 v80,v32,v33\n"
"v_cvt_pk_bf16_f32 v81,v34,v35\n"
"v_cvt_pk_bf16_f32 v82,v36,v37\n"
"v_cvt_pk_bf16_f32 v83,v38,v39\n"
"s_cbranch_scc1 .Lfk_rare3_0\n"
"v_exp_f32 v119,-v119\n"
"s_nop 0\n"
"v_mul_f32 v128,v128,v119\n"
"v_mul_f32 v129,v129,v119\n"
"v_mul_f32 v130,v130,v119\n"
"v_mul_f32 v131,v131,v119\n"
"v_mul_f32 v132,v132,v119\n"
"v_mul_f32 v133,v133,v119\n"
"v_mul_f32 v134,v134,v119\n"
"v_mul_f32 v135,v135,v119\n"
"v_mul_f32 v136,v136,v119\n"
"v_mul_f32 v137,v137,v119\n"
"v_mul_f32 v138,v138,v119\n"
"v_mul_f32 v139,v139,v119\n"
"v_mul_f32 v140,v140,v119\n"
"v_mul_f32 v141,v141,v119\n"
"v_mul_f32 v142,v142,v119\n"
"v_mul_f32 v143,v143,v119\n"
"v_mul_f32 v144,v144,v119\n"
"v_mul_f32 v145,v145,v119\n"
"v_mul_f32 v146,v146,v119\n"
"v_mul_f32 v147,v147,v119\n"
"v_mul_f32 v148,v148,v119\n"
"v_mul_f32 v149,v149,v119\n"
"v_mul_f32 v150,v150,v119\n"
"v_mul_f32 v151,v151,v119\n"
"v_mul_f32 v152,v152,v119\n"
"v_mul_f32 v153,v153,v119\n"
"v_mul_f32 v154,v154,v119\n"
"v_mul_f32 v155,v155,v119\n"
"v_mul_f32 v156,v156,v119\n"
"v_mul_f32 v157,v157,v119\n"
"v_mul_f32 v158,v158,v119\n"
"v_mul_f32 v159,v159,v119\n"
"v_accvgpr_read_b32 v120,a128\n"
"v_mul_f32 v120,v120,v119\n"
"v_accvgpr_write_b32 a128,v120\n"
"v_accvgpr_read_b32 v120,a129\n"
"v_mul_f32 v120,v120,v119\n"
"v_accvgpr_write_b32 a129,v120\n"
"v_accvgpr_read_b32 v120,a130\n"
"v_mul_f32 v120,v120,v119\n"
"v_accvgpr_write_b32 a130,v120\n"
"v_accvgpr_read_b32 v120,a131\n"
"v_mul_f32 v120,v120,v119\n"
"v_accvgpr_write_b32 a131,v120\n"
".Lfk_rare3_0:\n"
"v_max3_f32 v119,v40,v41,v42\n"
"v_max3_f32 v120,v43,v44,v45\n"
"v_max3_f32 v119,v119,v46,v47\n"
"v_max_f32 v119,v119,v120\n"
"v_mov_b32 v120,v119\n"
"s_nop 1\n"
"v_permlane16_swap_b32 v120,v119\n"
"v_max_f32 v119,v119,v120\n"
"v_mov_b32 v120,v119\n"
"s_nop 1\n"
"v_permlane32_swap_b32 v120,v119\n"
"v_max_f32 v119,v119,v120\n"
"s_bitcmp1_b32 s90,1\n"
"s_cselect_b32 s82,0xff800000,0\n"
"v_add_f32 v119,0x41980000,v119\n"
"v_max_f32 v119,s82,v119\n"
"v_cmp_lt_f32 vcc,s17,|v119|\n"
"s_nop 0\n"
"s_cbranch_vccz .Lfk_rare3_1\n"
"v_ceil_f32 v119,v119\n"
"v_sub_f32 v40,v40,v119\n"
"v_sub_f32 v41,v41,v119\n"
"v_sub_f32 v42,v42,v119\n"
"v_sub_f32 v43,v43,v119\n"
"v_sub_f32 v44,v44,v119\n"
"v_sub_f32 v45,v45,v119\n"
"v_sub_f32 v46,v46,v119\n"
"v_sub_f32 v47,v47,v119\n"
"v_sub_f32 v100,v100,v119\n"
"v_sub_f32 v101,v101,v119\n"
"v_sub_f32 v102,v102,v119\n"
"v_sub_f32 v103,v103,v119\n"
"v_sub_f32 v8,v8,v119\n"
"v_sub_f32 v9,v9,v119\n"
"v_sub_f32 v10,v10,v119\n"
"v_sub_f32 v11,v11,v119\n"
"v_sub_f32 v12,v12,v119\n"
"v_sub_f32 v13,v13,v119\n"
"v_sub_f32 v14,v14,v119\n"
"v_sub_f32 v15,v15,v119\n"
"v_exp_f32 v40,v40\n"
"v_exp_f32 v41,v41\n"
"v_exp_f32 v42,v42\n"
"v_exp_f32 v43,v43\n"
"v_exp_f32 v44,v44\n"
"v_exp_f32 v45,v45\n"
"v_exp_f32 v46,v46\n"
"v_exp_f32 v47,v47\n"
"v_cvt_pk_bf16_f32 v84,v40,v41\n"
"v_cvt_pk_bf16_f32 v85,v42,v43\n"
"v_cvt_pk_bf16_f32 v86,v44,v45\n"
"v_cvt_pk_bf16_f32 v87,v46,v47\n"
"s_cbranch_scc1 .Lfk_rare3_1\n"
"v_exp_f32 v119,-v119\n"
"s_nop 0\n"
"v_mul_f32 v160,v160,v119\n"
"v_mul_f32 v161,v161,v119\n"
"v_mul_f32 v162,v162,v119\n"
"v_mul_f32 v163,v163,v119\n"
"v_mul_f32 v164,v164,v119\n"
"v_mul_f32 v165,v165,v119\n"
"v_mul_f32 v166,v166,v119\n"
"v_mul_f32 v167,v167,v119\n"
"v_mul_f32 v168,v168,v119\n"
"v_mul_f32 v169,v169,v119\n"
"v_mul_f32 v170,v170,v119\n"
"v_mul_f32 v171,v171,v119\n"
"v_mul_f32 v172,v172,v119\n"
"v_mul_f32 v173,v173,v119\n"
"v_mul_f32 v174,v174,v119\n"
"v_mul_f32 v175,v175,v119\n"
"v_mul_f32 v176,v176,v119\n"
"v_mul_f32 v177,v177,v119\n"
"v_mul_f32 v178,v178,v119\n"
"v_mul_f32 v179,v179,v119\n"
"v_mul_f32 v180,v180,v119\n"
"v_mul_f32 v181,v181,v119\n"
"v_mul_f32 v182,v182,v119\n"
"v_mul_f32 v183,v183,v119\n"
"v_mul_f32 v184,v184,v119\n"
"v_mul_f32 v185,v185,v119\n"
"v_mul_f32 v186,v186,v119\n"
"v_mul_f32 v187,v187,v119\n"
"v_mul_f32 v188,v188,v119\n"
"v_mul_f32 v189,v189,v119\n"
"v_mul_f32 v190,v190,v119\n"
"v_mul_f32 v191,v191,v119\n"
"v_accvgpr_read_b32 v120,a132\n"
"v_mul_f32 v120,v120,v119\n"
"v_accvgpr_write_b32 a132,v120\n"
"v_accvgpr_read_b32 v120,a133\n"
"v_mul_f32 v120,v120,v119\n"
"v_accvgpr_write_b32 a133,v120\n"
"v_accvgpr_read_b32 v120,a134\n"
"v_mul_f32 v120,v120,v119\n"
"v_accvgpr_write_b32 a134,v120\n"
"v_accvgpr_read_b32 v120,a135\n"
"v_mul_f32 v120,v120,v119\n"
"v_accvgpr_write_b32 a135,v120\n"
".Lfk_rare3_1:\n"
"v_max3_f32 v119,v48,v49,v50\n"
"v_max3_f32 v120,v51,v52,v53\n"
"v_max3_f32 v119,v119,v54,v55\n"
"v_max_f32 v119,v119,v120\n"
"v_mov_b32 v120,v119\n"
"s_nop 1\n"
"v_permlane16_swap_b32 v120,v119\n"
"v_max_f32 v119,v119,v120\n"
"v_mov_b32 v120,v119\n"
"s_nop 1\n"
"v_permlane32_swap_b32 v120,v119\n"
"v_max_f32 v119,v119,v120\n"
"s_bitcmp1_b32 s90,2\n"
"s_cselect_b32 s82,0xff800000,0\n"
"v_add_f32 v119,0x41980000,v119\n"
"v_max_f32 v119,s82,v119\n"
"v_cmp_lt_f32 vcc,s17,|v119|\n"
"s_nop 0\n"
"s_cbranch_vccz .Lfk_rare3_2\n"
"v_ceil_f32 v119,v119\n"
"v_sub_f32 v48,v48,v119\n"
"v_sub_f32 v49,v49,v119\n"
"v_sub_f32 v50,v50,v119\n"
"v_sub_f32 v51,v51,v119\n"
"v_sub_f32 v52,v52,v119\n"
"v_sub_f32 v53,v53,v119\n"
"v_sub_f32 v54,v54,v119\n"
"v_sub_f32 v55,v55,v119\n"
"v_sub_f32 v104,v104,v119\n"
"v_sub_f32 v105,v105,v119\n"
"v_sub_f32 v106,v106,v119\n"
"v_sub_f32 v107,v107,v119\n"
"v_sub_f32 v16,v16,v119\n"
"v_sub_f32 v17,v17,v119\n"
"v_sub_f32 v18,v18,v119\n"
"v_sub_f32 v19,v19,v119\n"
"v_sub_f32 v20,v20,v119\n"
"v_sub_f32 v21,v21,v119\n"
"v_sub_f32 v22,v22,v119\n"
"v_sub_f32 v23,v23,v119\n"
"v_exp_f32 v48,v48\n"
"v_exp_f32 v49,v49\n"
"v_exp_f32 v50,v50\n"
"v_exp_f32 v51,v51\n"
"v_exp_f32 v52,v52\n"
"v_exp_f32 v53,v53\n"
"v_exp_f32 v54,v54\n"
"v_exp_f32 v55,v55\n"
"v_cvt_pk_bf16_f32 v88,v48,v49\n"
"v_cvt_pk_bf16_f32 v89,v50,v51\n"
"v_cvt_pk_bf16_f32 v90,v52,v53\n"
"v_cvt_pk_bf16_f32 v91,v54,v55\n"
"s_cbranch_scc1 .Lfk_rare3_2\n"
"v_exp_f32 v119,-v119\n"
"s_nop 0\n"
"v_mul_f32 v192,v192,v119\n"
"v_mul_f32 v193,v193,v119\n"
"v_mul_f32 v194,v194,v119\n"
"v_mul_f32 v195,v195,v119\n"
"v_mul_f32 v196,v196,v119\n"
"v_mul_f32 v197,v197,v119\n"
"v_mul_f32 v198,v198,v119\n"
"v_mul_f32 v199,v199,v119\n"
"v_mul_f32 v200,v200,v119\n"
"v_mul_f32 v201,v201,v119\n"
"v_mul_f32 v202,v202,v119\n"
"v_mul_f32 v203,v203,v119\n"
"v_mul_f32 v204,v204,v119\n"
"v_mul_f32 v205,v205,v119\n"
"v_mul_f32 v206,v206,v119\n"
"v_mul_f32 v207,v207,v119\n"
"v_mul_f32 v208,v208,v119\n"
"v_mul_f32 v209,v209,v119\n"
"v_mul_f32 v210,v210,v119\n"
"v_mul_f32 v211,v211,v119\n"
"v_mul_f32 v212,v212,v119\n"
"v_mul_f32 v213,v213,v119\n"
"v_mul_f32 v214,v214,v119\n"
"v_mul_f32 v215,v215,v119\n"
"v_mul_f32 v216,v216,v119\n"
"v_mul_f32 v217,v217,v119\n"
"v_mul_f32 v218,v218,v119\n"
"v_mul_f32 v219,v219,v119\n"
"v_mul_f32 v220,v220,v119\n"
"v_mul_f32 v221,v221,v119\n"
"v_mul_f32 v222,v222,v119\n"
"v_mul_f32 v223,v223,v119\n"
"v_accvgpr_read_b32 v120,a136\n"
"v_mul_f32 v120,v120,v119\n"
"v_accvgpr_write_b32 a136,v120\n"
"v_accvgpr_read_b32 v120,a137\n"
"v_mul_f32 v120,v120,v119\n"
"v_accvgpr_write_b32 a137,v120\n"
"v_accvgpr_read_b32 v120,a138\n"
"v_mul_f32 v120,v120,v119\n"
"v_accvgpr_write_b32 a138,v120\n"
"v_accvgpr_read_b32 v120,a139\n"
"v_mul_f32 v120,v120,v119\n"
"v_accvgpr_write_b32 a139,v120\n"
".Lfk_rare3_2:\n"
"v_max3_f32 v119,v56,v57,v58\n"
"v_max3_f32 v120,v59,v60,v61\n"
"v_max3_f32 v119,v119,v62,v63\n"
"v_max_f32 v119,v119,v120\n"
"v_mov_b32 v120,v119\n"
"s_nop 1\n"
"v_permlane16_swap_b32 v120,v119\n"
"v_max_f32 v119,v119,v120\n"
"v_mov_b32 v120,v119\n"
"s_nop 1\n"
"v_permlane32_swap_b32 v120,v119\n"
"v_max_f32 v119,v119,v120\n"
"s_bitcmp1_b32 s90,3\n"
"s_cselect_b32 s82,0xff800000,0\n"
"v_add_f32 v119,0x41980000,v119\n"
"v_max_f32 v119,s82,v119\n"
"v_cmp_lt_f32 vcc,s17,|v119|\n"
"s_nop 0\n"
"s_cbranch_vccz .Lfk_rare3_3\n"
"v_ceil_f32 v119,v119\n"
"v_sub_f32 v56,v56,v119\n"
"v_sub_f32 v57,v57,v119\n"
"v_sub_f32 v58,v58,v119\n"
"v_sub_f32 v59,v59,v119\n"
"v_sub_f32 v60,v60,v119\n"
"v_sub_f32 v61,v61,v119\n"
"v_sub_f32 v62,v62,v119\n"
"v_sub_f32 v63,v63,v119\n"
"v_sub_f32 v108,v108,v119\n"
"v_sub_f32 v109,v109,v119\n"
"v_sub_f32 v110,v110,v119\n"
"v_sub_f32 v111,v111,v119\n"
"v_sub_f32 v24,v24,v119\n"
"v_sub_f32 v25,v25,v119\n"
"v_sub_f32 v26,v26,v119\n"
"v_sub_f32 v27,v27,v119\n"
"v_sub_f32 v28,v28,v119\n"
"v_sub_f32 v29,v29,v119\n"
"v_sub_f32 v30,v30,v119\n"
"v_sub_f32 v31,v31,v119\n"
"v_exp_f32 v56,v56\n"
"v_exp_f32 v57,v57\n"
"v_exp_f32 v58,v58\n"
"v_exp_f32 v59,v59\n"
"v_exp_f32 v60,v60\n"
"v_exp_f32 v61,v61\n"
"v_exp_f32 v62,v62\n"
"v_exp_f32 v63,v63\n"
"v_cvt_pk_bf16_f32 v92,v56,v57\n"
"v_cvt_pk_bf16_f32 v93,v58,v59\n"
"v_cvt_pk_bf16_f32 v94,v60,v61\n"
"v_cvt_pk_bf16_f32 v95,v62,v63\n"
"s_cbranch_scc1 .Lfk_rare3_3\n"
"v_exp_f32 v119,-v119\n"
"s_nop 0\n"
"v_mul_f32 v224,v224,v119\n"
"v_mul_f32 v225,v225,v119\n"
"v_mul_f32 v226,v226,v119\n"
"v_mul_f32 v227,v227,v119\n"
"v_mul_f32 v228,v228,v119\n"
"v_mul_f32 v229,v229,v119\n"
"v_mul_f32 v230,v230,v119\n"
"v_mul_f32 v231,v231,v119\n"
"v_mul_f32 v232,v232,v119\n"
"v_mul_f32 v233,v233,v119\n"
"v_mul_f32 v234,v234,v119\n"
"v_mul_f32 v235,v235,v119\n"
"v_mul_f32 v236,v236,v119\n"
"v_mul_f32 v237,v237,v119\n"
"v_mul_f32 v238,v238,v119\n"
"v_mul_f32 v239,v239,v119\n"
"v_mul_f32 v240,v240,v119\n"
"v_mul_f32 v241,v241,v119\n"
"v_mul_f32 v242,v242,v119\n"
"v_mul_f32 v243,v243,v119\n"
"v_mul_f32 v244,v244,v119\n"
"v_mul_f32 v245,v245,v119\n"
"v_mul_f32 v246,v246,v119\n"
"v_mul_f32 v247,v247,v119\n"
"v_mul_f32 v248,v248,v119\n"
"v_mul_f32 v249,v249,v119\n"
"v_mul_f32 v250,v250,v119\n"
"v_mul_f32 v251,v251,v119\n"
"v_mul_f32 v252,v252,v119\n"
"v_mul_f32 v253,v253,v119\n"
"v_mul_f32 v254,v254,v119\n"
"v_mul_f32 v255,v255,v119\n"
"v_accvgpr_read_b32 v120,a140\n"
"v_mul_f32 v120,v120,v119\n"
"v_accvgpr_write_b32 a140,v120\n"
"v_accvgpr_read_b32 v120,a141\n"
"v_mul_f32 v120,v120,v119\n"
"v_accvgpr_write_b32 a141,v120\n"
"v_accvgpr_read_b32 v120,a142\n"
"v_mul_f32 v120,v120,v119\n"
"v_accvgpr_write_b32 a142,v120\n"
"v_accvgpr_read_b32 v120,a143\n"
"v_mul_f32 v120,v120,v119\n"
"v_accvgpr_write_b32 a143,v120\n"
".Lfk_rare3_3:\n"
"s_mov_b32 s90,0\n"
"s_nop 1\n"
"s_setpc_b64 s[94:95]\n"
".Lfk_mslow1:\n"
"s_nop 7\n"
"v_min_f32 v119,v102,v108\n"
"v_sub_f32 v120,v119,v108\n"
"v_sub_f32 v119,v119,v102\n"
"v_exp_f32 v120,v120\n"
"v_exp_f32 v119,v119\n"
"v_mul_f32 v224,v224,v120\n"
"v_mul_f32 v225,v225,v120\n"
"v_mul_f32 v226,v226,v120\n"
"v_mul_f32 v227,v227,v120\n"
"v_mul_f32 v228,v228,v120\n"
"v_mul_f32 v229,v229,v120\n"
"v_mul_f32 v230,v230,v120\n"
"v_mul_f32 v231,v231,v120\n"
"v_mul_f32 v232,v232,v120\n"
"v_mul_f32 v233,v233,v120\n"
"v_mul_f32 v234,v234,v120\n"
"v_mul_f32 v235,v235,v120\n"
"v_mul_f32 v236,v236,v120\n"
"v_mul_f32 v237,v237,v120\n"
"v_mul_f32 v238,v238,v120\n"
"v_mul_f32 v239,v239,v120\n"
"v_mul_f32 v240,v240,v120\n"
"v_mul_f32 v241,v241,v120\n"
"v_mul_f32 v242,v242,v120\n"
"v_mul_f32 v243,v243,v120\n"
"v_mul_f32 v244,v244,v120\n"
"v_mul_f32 v245,v245,v120\n"
"v_mul_f32 v246,v246,v120\n"
"v_mul_f32 v247,v247,v120\n"
"v_mul_f32 v248,v248,v120\n"
"v_mul_f32 v249,v249,v120\n"
"v_mul_f32 v250,v250,v120\n"
"v_mul_f32 v251,v251,v120\n"
"v_mul_f32 v252,v252,v120\n"
"v_mul_f32 v253,v253,v120\n"
"v_mul_f32 v254,v254,v120\n"
"v_mul_f32 v255,v255,v120\n"
"v_accvgpr_read_b32 v121,a140\n"
"v_mul_f32 v121,v121,v120\n"
"v_accvgpr_write_b32 a140,v121\n"
"v_mul_f32 v103,v103,v119\n"
"v_accvgpr_read_b32 v121,a80\n"
"v_lshlrev_b32 v120,16,v121\n"
"v_and_b32 v121,0xffff0000,v121\n"
"v_mul_f32 v120,v120,v119\n"
"v_mul_f32 v121,v121,v119\n"
"v_cvt_pk_bf16_f32 v121,v120,v121\n"
"v_accvgpr_write_b32 a80,v121\n"
"v_accvgpr_read_b32 v121,a81\n"
"v_lshlrev_b32 v120,16,v121\n"
"v_and_b32 v121,0xffff0000,v121\n"
"v_mul_f32 v120,v120,v119\n"
"v_mul_f32 v121,v121,v119\n"
"v_cvt_pk_bf16_f32 v121,v120,v121\n"
"v_accvgpr_write_b32 a81,v121\n"
"v_accvgpr_read_b32 v121,a82\n"
"v_lshlrev_b32 v120,16,v121\n"
"v_and_b32 v121,0xffff0000,v121\n"
"v_mul_f32 v120,v120,v119\n"
"v_mul_f32 v121,v121,v119\n"
"v_cvt_pk_bf16_f32 v121,v120,v121\n"
"v_accvgpr_write_b32 a82,v121\n"
"v_accvgpr_read_b32 v121,a83\n"
"v_lshlrev_b32 v120,16,v121\n"
"v_and_b32 v121,0xffff0000,v121\n"
"v_mul_f32 v120,v120,v119\n"
"v_mul_f32 v121,v121,v119\n"
"v_cvt_pk_bf16_f32 v121,v120,v121\n"
"v_accvgpr_write_b32 a83,v121\n"
"v_accvgpr_read_b32 v121,a84\n"
"v_lshlrev_b32 v120,16,v121\n"
"v_and_b32 v121,0xffff0000,v121\n"
"v_mul_f32 v120,v120,v119\n"
"v_mul_f32 v121,v121,v119\n"
"v_cvt_pk_bf16_f32 v121,v120,v121\n"
"v_accvgpr_write_b32 a84,v121\n"
"v_accvgpr_read_b32 v121,a85\n"
"v_lshlrev_b32 v120,16,v121\n"
"v_and_b32 v121,0xffff0000,v121\n"
"v_mul_f32 v120,v120,v119\n"
"v_mul_f32 v121,v121,v119\n"
"v_cvt_pk_bf16_f32 v121,v120,v121\n"
"v_accvgpr_write_b32 a85,v121\n"
"v_accvgpr_read_b32 v121,a86\n"
"v_lshlrev_b32 v120,16,v121\n"
"v_and_b32 v121,0xffff0000,v121\n"
"v_mul_f32 v120,v120,v119\n"
"v_mul_f32 v121,v121,v119\n"
"v_cvt_pk_bf16_f32 v121,v120,v121\n"
"v_accvgpr_write_b32 a86,v121\n"
"v_accvgpr_read_b32 v121,a87\n"
"v_lshlrev_b32 v120,16,v121\n"
"v_and_b32 v121,0xffff0000,v121\n"
"v_mul_f32 v120,v120,v119\n"
"v_mul_f32 v121,v121,v119\n"
"v_cvt_pk_bf16_f32 v121,v120,v121\n"
"v_accvgpr_write_b32 a87,v121\n"
"v_accvgpr_read_b32 v121,a88\n"
"v_lshlrev_b32 v120,16,v121\n"
"v_and_b32 v121,0xffff0000,v121\n"
"v_mul_f32 v120,v120,v119\n"
"v_mul_f32 v121,v121,v119\n"
"v_cvt_pk_bf16_f32 v121,v120,v121\n"
"v_accvgpr_write_b32 a88,v121\n"
"v_accvgpr_read_b32 v121,a89\n"
"v_lshlrev_b32 v120,16,v121\n"
"v_and_b32 v121,0xffff0000,v121\n"
"v_mul_f32 v120,v120,v119\n"
"v_mul_f32 v121,v121,v119\n"
"v_cvt_pk_bf16_f32 v121,v120,v121\n"
"v_accvgpr_write_b32 a89,v121\n"
"v_accvgpr_read_b32 v121,a90\n"
"v_lshlrev_b32 v120,16,v121\n"
"v_and_b32 v121,0xffff0000,v121\n"
"v_mul_f32 v120,v120,v119\n"
"v_mul_f32 v121,v121,v119\n"
"v_cvt_pk_bf16_f32 v121,v120,v121\n"
"v_accvgpr_write_b32 a90,v121\n"
"v_accvgpr_read_b32 v121,a91\n"
"v_lshlrev_b32 v120,16,v121\n"
"v_and_b32 v121,0xffff0000,v121\n"
"v_mul_f32 v120,v120,v119\n"
"v_mul_f32 v121,v121,v119\n"
"v_cvt_pk_bf16_f32 v121,v120,v121\n"
"v_accvgpr_write_b32 a91,v121\n"
"v_accvgpr_read_b32 v121,a92\n"
"v_lshlrev_b32 v120,16,v121\n"
"v_and_b32 v121,0xffff0000,v121\n"
"v_mul_f32 v120,v120,v119\n"
"v_mul_f32 v121,v121,v119\n"
"v_cvt_pk_bf16_f32 v121,v120,v121\n"
"v_accvgpr_write_b32 a92,v121\n"
"v_accvgpr_read_b32 v121,a93\n"
"v_lshlrev_b32 v120,16,v121\n"
"v_and_b32 v121,0xffff0000,v121\n"
"v_mul_f32 v120,v120,v119\n"
"v_mul_f32 v121,v121,v119\n"
"v_cvt_pk_bf16_f32 v121,v120,v121\n"
"v_accvgpr_write_b32 a93,v121\n"
"v_accvgpr_read_b32 v121,a94\n"
"v_lshlrev_b32 v120,16,v121\n"
"v_and_b32 v121,0xffff0000,v121\n"
"v_mul_f32 v120,v120,v119\n"
"v_mul_f32 v121,v121,v119\n"
"v_cvt_pk_bf16_f32 v121,v120,v121\n"
"v_accvgpr_write_b32 a94,v121\n"
"v_accvgpr_read_b32 v121,a95\n"
"v_lshlrev_b32 v120,16,v121\n"
"v_and_b32 v121,0xffff0000,v121\n"
"v_mul_f32 v120,v120,v119\n"
"v_mul_f32 v121,v121,v119\n"
"v_cvt_pk_bf16_f32 v121,v120,v121\n"
"v_accvgpr_write_b32 a95,v121\n"
"s_nop 1\n"
"s_setpc_b64 s[94:95]\n"
".Lfk_end:\n"
        : "+{v0}"(tid), "+{s2}"(bid)
        : "{s[0:1]}"(__builtin_amdgcn_kernarg_segment_ptr())
        : "v1","v2","v3","v4","v5","v6","v7","v8","v9","v10","v11","v12","v13","v14","v15","v16","v17","v18","v19","v20","v21","v22","v23","v24","v25","v26","v27","v28","v29","v30","v31","v32","v33","v34","v35","v36","v37","v38","v39","v40","v41","v42","v43","v44","v45","v46","v47","v48","v49","v50","v51","v52","v53","v54","v55","v56","v57","v58","v59","v60","v61","v62","v63","v64","v65","v66","v67","v68","v69","v70","v71","v72","v73","v74","v75","v76","v77","v78","v79","v80","v81","v82","v83","v84","v85","v86","v87","v88","v89","v90","v91","v92","v93","v94","v95","v96","v97","v98","v99","v100","v101","v102","v103","v104","v105","v106","v107","v108","v109","v110","v111","v112","v113","v114","v115","v116","v117","v118","v119","v120","v121","v122","v123","v124","v125","v126","v127","v128","v129","v130","v131","v132","v133","v134","v135","v136","v137","v138","v139","v140","v141","v142","v143","v144","v145","v146","v147","v148","v149","v150","v151","v152","v153","v154","v155","v156","v157","v158","v159","v160","v161","v162","v163","v164","v165","v166","v167","v168","v169","v170","v171","v172","v173","v174","v175","v176","v177","v178","v179","v180","v181","v182","v183","v184","v185","v186","v187","v188","v189","v190","v191","v192","v193","v194","v195","v196","v197","v198","v199","v200","v201","v202","v203","v204","v205","v206","v207","v208","v209","v210","v211","v212","v213","v214","v215","v216","v217","v218","v219","v220","v221","v222","v223","v224","v225","v226","v227","v228","v229","v230","v231","v232","v233","v234","v235","v236","v237","v238","v239","v240","v241","v242","v243","v244","v245","v246","v247","v248","v249","v250","v251","v252","v253","v254","v255",
          "a0","a1","a2","a3","a4","a5","a6","a7","a8","a9","a10","a11","a12","a13","a14","a15","a16","a17","a18","a19","a20","a21","a22","a23","a24","a25","a26","a27","a28","a29","a30","a31","a32","a33","a34","a35","a36","a37","a38","a39","a40","a41","a42","a43","a44","a45","a46","a47","a48","a49","a50","a51","a52","a53","a54","a55","a56","a57","a58","a59","a60","a61","a62","a63","a64","a65","a66","a67","a68","a69","a70","a71","a72","a73","a74","a75","a76","a77","a78","a79","a80","a81","a82","a83","a84","a85","a86","a87","a88","a89","a90","a91","a92","a93","a94","a95","a96","a97","a98","a99","a100","a101","a102","a103","a104","a105","a106","a107","a108","a109","a110","a111","a112","a113","a114","a115","a116","a117","a118","a119","a120","a121","a122","a123","a124","a125","a126","a127","a128","a129","a130","a131","a132","a133","a134","a135","a136","a137","a138","a139","a140","a141","a142","a143","a144","a145","a146","a147","a148","a149","a150","a151","a152","a153","a154","a155","a156","a157","a158","a159","a160","a161","a162","a163","a164","a165","a166","a167","a168","a169","a170","a171","a172","a173","a174","a175","a176","a177","a178","a179","a180","a181","a182","a183","a184","a185","a186","a187","a188","a189","a190","a191","a192","a193","a194","a195","a196","a197","a198","a199","a200","a201","a202","a203","a204","a205","a206","a207","a208","a209","a210","a211","a212","a213","a214","a215","a216","a217","a218","a219","a220","a221","a222","a223","a224","a225","a226","a227","a228","a229","a230","a231","a232","a233","a234","a235","a236","a237","a238","a239","a240","a241","a242","a243","a244","a245","a246","a247","a248","a249","a250","a251","a252","a253","a254","a255",
          "s3","s4","s5","s6","s7","s8","s9","s10","s11","s12","s13","s14","s15","s16","s17","s18","s19","s20","s21","s22","s23","s24","s25","s26","s27","s28","s29","s30","s31","s32","s33","s34","s35","s36","s37","s38","s39","s40","s41","s42","s43","s44","s45","s46","s47","s48","s49","s50","s51","s52","s53","s54","s55","s56","s57","s58","s59","s60","s61","s62","s63","s64","s65","s66","s67","s68","s69","s70","s71","s72","s73","s74","s75","s76","s77","s78","s79","s80","s81","s82","s83","s84","s85","s86","s87","s88","s89","s90","s91","s92","s93","s94","s95","s96","s97","s98","s99","s100","s101","vcc","scc","m0","memory");
#endif
}

extern "C" void kernel_launch(void* const* d_in, const int* in_sizes, int n_in, void* d_out, int out_size, void* d_ws, size_t ws_size,
                              hipStream_t stream) {
    (void)hipFuncSetAttribute((const void*)fused_kernel, hipFuncAttributeMaxDynamicSharedMemorySize, 163840);
    fused_kernel<<<256, 256, 163840, stream>>>((const char*)d_in[0], (const char*)d_in[1], (const char*)d_in[2], (const char*)d_in[3], (const char*)d_in[4], (char*)d_ws,
                                               (const char*)d_in[5], (const char*)d_in[8], (const char*)d_in[9], (const char*)d_in[10], (const char*)d_in[7], (char*)d_out);
}
```

```cpp
#include <hip/hip_runtime.h>

extern "C" __global__ __launch_bounds__(256, 1) void fused_kernel(const char* __restrict__ x, const char* __restrict__ ckv, const char* __restrict__ wq,
                                                       const char* __restrict__ wuk, const char* __restrict__ wuv, char* ws,
                                                       const char* __restrict__ pwo, const char* __restrict__ pwg, const char* __restrict__ pwu, const char* __restrict__ pwd,
                                                       const char* __restrict__ pwr, char* pout) {
    unsigned tid = threadIdx.x, bid = blockIdx.x;
#if defined(__HIP_DEVICE_COMPILE__)
    asm volatile(
"s_load_dwordx2 s[16:17],s[0:1],8\n"
"s_load_dword s3,s[0:1],72\n"
"s_getpc_b64 s[8:9]\n"
".Lfk_pc:\n"
"s_lshr_b32 s82,s2,3\n"
"s_lshl_b32 s82,s82,12\n"
"v_lshlrev_b32 v1,4,v0\n"
"s_mov_b32 s10,.Lfk_cold-.Lfk_pc\n"
"s_mov_b32 s11,0x20000\n"
"buffer_load_dword v115,v1,s[8:11],s82 offen\n"
"v_readfirstlane_b32 s52,v0\n"
"s_load_dwordx2 s[32:33],s[0:1],0\n"
"s_load_dwordx2 s[24:25],s[0:1],40\n"
"s_load_dwordx2 s[44:45],s[0:1],8\n"
"s_load_dwordx2 s[28:29],s[0:1],16\n"
"s_load_dwordx2 s[40:41],s[0:1],16\n"
"s_load_dwordx2 s[4:5],s[0:1],48\n"
"s_and_b32 s82,s2,8\n"
"s_add_u32 s82,s82,24\n"
"s_load_dwordx2 s[20:21],s[0:1],s82\n"
"s_lshr_b32 s52,s52,6\n"
"s_lshr_b32 s83,s2,7\n"
"s_lshl_b32 s83,s83,3\n"
"s_add_u32 s83,s83,24\n"
"s_load_dwordx2 s[36:37],s[0:1],s83\n"
"s_mov_b32 s100,s2\n"
"s_lshl_b32 s92,s2,2\n"
"s_add_u32 s92,s92,s52\n"
"v_and_b32 v0,63,v0\n"
"v_lshlrev_b32 v112,4,v0\n"
"s_and_b32 s88,s2,7\n"
"s_lshr_b32 s89,s2,3\n"
"s_lshl_b32 s93,s89,2\n"
"s_add_u32 s93,s93,s52\n"
"v_lshrrev_b32 v2,3,v0\n"
"v_xor_b32 v3,v2,v0\n"
"v_and_b32 v3,7,v3\n"
"v_lshlrev_b32 v3,4,v3\n"
"s_lshl_b32 s82,s52,9\n"
"v_add_u32 v2,s82,v2\n"
"v_lshl_add_u32 v106,v2,10,v3\n"
"v_add_u32 v107,7168,v106\n"
"v_add_u32 v108,14336,v106\n"
"v_add_u32 v109,21504,v106\n"
"v_lshrrev_b32 v4,4,v0\n"
"v_and_b32 v5,15,v0\n"
"v_lshlrev_b32 v6,1,v4\n"
"v_xor_b32 v5,v5,v6\n"
"v_lshlrev_b32 v5,4,v5\n"
"v_xor_b32 v6,0x80,v5\n"
"s_and_b32 s82,s52,1\n"
"s_lshl_b32 s82,s82,17\n"
"s_lshr_b32 s83,s52,1\n"
"s_lshl_b32 s83,s83,8\n"
"s_add_u32 s82,s82,s83\n"
"v_lshl_add_u32 v7,v4,13,s82\n"
"v_add_u32 v110,v7,v5\n"
"v_add_u32 v111,31744,v110\n"
"v_add_u32 v113,v7,v6\n"
"v_add_u32 v113,63488,v113\n"
"v_add_u32 v114,31744,v113\n"
"s_lshl_b32 s78,s52,12\n"
"s_lshl_b32 s79,s52,13\n"
"s_lshl_b32 s85,s52,11\n"
"s_lshr_b32 s82,s2,5\n"
"s_lshl_b32 s82,s82,15\n"
"s_lshl_b32 s83,s88,10\n"
"s_bfe_u32 s97,s2,0x10004\n"
"s_lshl_b32 s97,s97,9\n"
"s_add_u32 s83,s83,s97\n"
"v_lshrrev_b32 v60,3,v0\n"
"v_lshlrev_b32 v60,19,v60\n"
"v_and_b32 v61,7,v0\n"
"v_lshl_add_u32 v60,v61,7,v60\n"
"s_and_b32 s12,s93,63\n"
"s_lshl3_add_u32 s12,s12,s88\n"
"s_lshl_b32 s12,s12,10\n"
"s_add_u32 s38,s12,1049600\n"
"s_mov_b32 s39,0x20000\n"
"s_and_b32 s14,s52,1\n"
"s_mul_i32 s13,s14,507904\n"
"v_subrev_u32 v62,s13,v106\n"
"v_subrev_u32 v63,s13,v107\n"
"s_mul_i32 s14,s14,6144\n"
"s_sub_u32 s13,s79,s14\n"
"s_waitcnt lgkmcnt(0)\n"
"buffer_load_dword v115,v60,s[36:39],s12 offen\n"
"s_lshl_b32 s87,s92,11\n"
"s_add_u32 s44,s44,s87\n"
"s_addc_u32 s45,s45,0\n"
"s_mov_b32 s46,2048\n"
"s_mov_b32 s47,0x20000\n"
"v_lshlrev_b32 v1,5,v0\n"
"buffer_load_dword v115,v1,s[44:47],0 offen\n"
"s_add_u32 s16,s16,s82\n"
"s_addc_u32 s17,s17,0\n"
"s_sub_u32 s18,2097152,s82\n"
"s_mov_b32 s19,0x20000\n"
"s_add_u32 s20,s20,s83\n"
"s_addc_u32 s21,s21,0\n"
"s_sub_u32 s22,4194304,s83\n"
"s_mov_b32 s23,0x20000\n"
"s_mov_b32 s77,0\n"
"s_add_u32 m0,s78,147456\n"
"s_nop 0\n"
"buffer_load_dwordx4 v110,s[20:23],s77 offen offset:0 lds\n"
"buffer_load_dwordx4 v111,s[20:23],s77 offen offset:1024 lds\n"
"buffer_load_dwordx4 v113,s[20:23],s77 offen offset:2048 lds\n"
"buffer_load_dwordx4 v114,s[20:23],s77 offen offset:3072 lds\n"
"s_mov_b32 s76,786432\n"
"s_add_u32 m0,s13,12288\n"
"s_nop 0\n"
"buffer_load_dwordx4 v62,s[16:19],s76 offen offset:0 lds\n"
"buffer_load_dwordx4 v63,s[16:19],s76 offen offset:1024 lds\n"
"s_mov_b32 s77,262144\n"
"s_add_u32 m0,s78,131072\n"
"s_nop 0\n"
"buffer_load_dwordx4 v110,s[20:23],s77 offen offset:0 lds\n"
"buffer_load_dwordx4 v111,s[20:23],s77 offen offset:1024 lds\n"
"buffer_load_dwordx4 v113,s[20:23],s77 offen offset:2048 lds\n"
"buffer_load_dwordx4 v114,s[20:23],s77 offen offset:3072 lds\n"
"s_mov_b32 s76,0\n"
"s_add_u32 m0,s13,0\n"
"s_nop 0\n"
"buffer_load_dwordx4 v62,s[16:19],s76 offen offset:0 lds\n"
"buffer_load_dwordx4 v63,s[16:19],s76 offen offset:1024 lds\n"
"s_mov_b32 s76,262144\n"
"s_add_u32 m0,s13,4096\n"
"s_nop 0\n"
"buffer_load_dwordx4 v62,s[16:19],s76 offen offset:0 lds\n"
"buffer_load_dwordx4 v63,s[16:19],s76 offen offset:1024 lds\n"
"s_mov_b32 s76,524288\n"
"s_add_u32 m0,s13,8192\n"
"s_nop 0\n"
"buffer_load_dwordx4 v62,s[16:19],s76 offen offset:0 lds\n"
"buffer_load_dwordx4 v63,s[16:19],s76 offen offset:1024 lds\n"
"s_lshl_b32 s82,s92,12\n"
"s_add_u32 s32,s32,s82\n"
"s_addc_u32 s33,s33,0\n"
"s_mov_b32 s34,4096\n"
"s_mov_b32 s35,0x20000\n"
"s_mov_b32 s26,176160768\n"
"s_mov_b32 s27,0x20000\n"
"s_mov_b32 s75,148897792\n"
"s_lshr_b32 s54,s2,5\n"
"s_lshl_b32 s54,s54,19\n"
"s_and_b32 s82,s92,0x47\n"
"s_bfe_u32 s83,s92,0x10003\n"
"s_lshl_b32 s83,s83,5\n"
"s_or_b32 s82,s82,s83\n"
"s_bfe_u32 s83,s92,0x20004\n"
"s_lshl3_add_u32 s82,s83,s82\n"
"s_lshl_b32 s82,s82,7\n"
"s_add_u32 s54,s54,s82\n"
"v_lshrrev_b32 v1,3,v0\n"
"v_bfe_u32 v2,v0,2,1\n"
"v_and_b32 v3,3,v0\n"
"v_lshlrev_b32 v3,4,v3\n"
"v_lshl_add_u32 v70,v1,6,v3\n"
"v_lshl_add_u32 v70,v2,11,v70\n"
"s_bfe_u32 s82,s54,0x10009\n"
"s_lshl_b32 s82,s82,5\n"
"v_xor_b32 v3,s82,v3\n"
"v_lshl_add_u32 v71,v2,6,v3\n"
"v_lshl_add_u32 v71,v1,14,v71\n"
"buffer_load_dwordx2 v[78:79],off,s[24:27],s75 sc1\n"
"s_lshl_b32 s82,s52,16\n"
"v_lshl_add_u32 v7,v4,13,s82\n"
"s_bitcmp1_b32 s52,0\n"
"s_cselect_b32 s83,0x80,0\n"
"v_xor_b32 v5,s83,v5\n"
"v_add_u32 v72,v7,v5\n"
"v_add_u32 v73,32768,v72\n"
"s_mov_b32 s86,0\n"
"v_and_b32 v1,15,v0\n"
"v_lshrrev_b32 v2,4,v0\n"
"v_lshlrev_b32 v3,6,v1\n"
"v_lshl_add_u32 v3,v2,4,v3\n"
"v_lshrrev_b32 v4,4,v3\n"
"v_and_b32 v4,32,v4\n"
"v_xor_b32 v97,v3,v4\n"
"s_lshr_b32 s82,s52,1\n"
"s_lshl_b32 s82,s82,14\n"
"v_and_b32 v3,7,v1\n"
"v_xor_b32 v3,v3,v2\n"
"v_lshlrev_b32 v3,4,v3\n"
"v_lshl_add_u32 v3,v1,7,v3\n"
"v_add_u32 v80,s82,v3\n"
"v_xor_b32 v81,64,v80\n"
"v_add_u32 v82,65536,v80\n"
"v_add_u32 v83,65536,v81\n"
"v_bfe_u32 v3,v0,2,2\n"
"v_lshl_add_u32 v4,v2,3,v3\n"
"v_lshlrev_b32 v4,8,v4\n"
"v_and_b32 v5,1,v2\n"
"v_lshl_add_u32 v5,v5,2,v3\n"
"v_lshlrev_b32 v5,5,v5\n"
"v_and_b32 v6,3,v0\n"
"v_lshl_add_u32 v4,v6,3,v4\n"
"s_and_b32 s82,s52,1\n"
"s_lshl_b32 s82,s82,13\n"
"s_add_u32 s82,s82,98304\n"
"v_add_u32 v4,s82,v4\n"
"v_xor_b32 v6,0,v5\n"
"v_add_u32 v98,v4,v6\n"
"v_xor_b32 v6,32,v5\n"
"v_add_u32 v99,v4,v6\n"
"v_xor_b32 v6,64,v5\n"
"v_add_u32 v100,v4,v6\n"
"v_xor_b32 v6,96,v5\n"
"v_add_u32 v101,v4,v6\n"
"v_xor_b32 v6,128,v5\n"
"v_add_u32 v102,v4,v6\n"
"v_xor_b32 v6,160,v5\n"
"v_add_u32 v103,v4,v6\n"
"v_xor_b32 v6,192,v5\n"
"v_add_u32 v104,v4,v6\n"
"v_xor_b32 v6,224,v5\n"
"v_add_u32 v105,v4,v6\n"
"s_and_b32 s53,s89,1\n"
"s_lshl_b32 s83,s88,10\n"
"s_and_b32 s82,s93,63\n"
"s_add_u32 s82,s82,128\n"
"s_lshl_b32 s82,s82,13\n"
"s_add_u32 s82,s82,s83\n"
"s_add_u32 s36,s36,s82\n"
"s_addc_u32 s37,s37,0\n"
"s_mov_b32 s38,2622464\n"
"s_mov_b32 s39,0x20000\n"
"s_lshl_b32 s82,s93,13\n"
"s_add_u32 s82,s82,s83\n"
"s_add_u32 s40,s40,s82\n"
"s_addc_u32 s41,s41,0\n"
"s_mov_b32 s42,7341056\n"
"s_mov_b32 s43,0x20000\n"
"s_cmp_lg_u32 s53,0\n"
"s_cbranch_scc1 .Lfk_v\n"
"s_waitcnt vmcnt(11)\n"
"s_barrier\n"
"ds_read_b128 v[152:155],v80 offset:12288\n"
"ds_read_b128 v[156:159],v80 offset:14336\n"
"ds_read_b64_tr_b16 v[160:161],v98 offset:49152\n"
"ds_read_b64_tr_b16 v[162:163],v98 offset:50176\n"
"ds_read_b64_tr_b16 v[164:165],v99 offset:49152\n"
"ds_read_b64_tr_b16 v[166:167],v99 offset:50176\n"
"ds_read_b64_tr_b16 v[168:169],v100 offset:49152\n"
"ds_read_b64_tr_b16 v[170:171],v100 offset:50176\n"
"ds_read_b64_tr_b16 v[172:173],v101 offset:49152\n"
"ds_read_b64_tr_b16 v[174:175],v101 offset:50176\n"
"ds_read_b64_tr_b16 v[176:177],v102 offset:49152\n"
"ds_read_b64_tr_b16 v[178:179],v102 offset:50176\n"
"ds_read_b64_tr_b16 v[180:181],v103 offset:49152\n"
"ds_read_b64_tr_b16 v[182:183],v103 offset:50176\n"
"ds_read_b64_tr_b16 v[184:185],v104 offset:49152\n"
"ds_read_b64_tr_b16 v[186:187],v104 offset:50176\n"
"ds_read_b64_tr_b16 v[188:189],v105 offset:49152\n"
"ds_read_b64_tr_b16 v[190:191],v105 offset:50176\n"
"s_waitcnt lgkmcnt(12)\n"
"v_mfma_f32_16x16x32_bf16 a[192:195],v[160:163],v[152:155],0\n"
"s_and_b32 s91,s89,7\n"
"s_mov_b32 s76,128\n"
"s_add_u32 m0,s79,32768\n"
"v_mfma_f32_16x16x32_bf16 a[224:227],v[160:163],v[156:159],0\n"
"buffer_load_dwordx4 v106,s[16:19],s76 offen offset:0 lds\n"
"v_mfma_f32_16x16x32_bf16 a[228:231],v[164:167],v[156:159],0\n"
"s_lshr_b32 s97,s89,3\n"
"s_lshl2_add_u32 s90,s88,s97\n"
"v_mfma_f32_16x16x32_bf16 a[196:199],v[164:167],v[152:155],0\n"
"buffer_load_dwordx4 v107,s[16:19],s76 offen offset:1024 lds\n"
"s_waitcnt lgkmcnt(8)\n"
"v_mfma_f32_16x16x32_bf16 a[200:203],v[168:171],v[152:155],0\n"
"s_lshl_b32 vcc_lo,s90,8\n"
"s_add_u32 s28,s28,vcc_lo\n"
"s_addc_u32 s29,s29,0\n"
"v_mfma_f32_16x16x32_bf16 a[232:235],v[168:171],v[156:159],0\n"
"buffer_load_dwordx4 v108,s[16:19],s76 offen offset:2048 lds\n"
"v_mfma_f32_16x16x32_bf16 a[236:239],v[172:175],v[156:159],0\n"
"s_sub_u32 s30,8388608,vcc_lo\n"
"s_mov_b32 s31,0x20000\n"
"buffer_load_dwordx4 v109,s[16:19],s76 offen offset:3072 lds\n"
"v_mfma_f32_16x16x32_bf16 a[204:207],v[172:175],v[152:155],0\n"
"s_waitcnt vmcnt(9)\n"
"s_barrier\n"
"s_waitcnt lgkmcnt(4)\n"
"v_mfma_f32_16x16x32_bf16 a[208:211],v[176:179],v[152:155],0\n"
"ds_read_b128 v[128:131],v80 offset:0\n"
"ds_read_b128 v[192:195],v81 offset:0\n"
"v_mfma_f32_16x16x32_bf16 a[240:243],v[176:179],v[156:159],0\n"
"ds_read_b64_tr_b16 v[224:225],v98 offset:32768\n"
"ds_read_b64_tr_b16 v[226:227],v98 offset:33792\n"
"v_mfma_f32_16x16x32_bf16 a[244:247],v[180:183],v[156:159],0\n"
"ds_read_b64_tr_b16 v[228:229],v99 offset:32768\n"
"ds_read_b64_tr_b16 v[230:231],v99 offset:33792\n"
"v_mfma_f32_16x16x32_bf16 a[212:215],v[180:183],v[152:155],0\n"
"ds_read_b64_tr_b16 v[232:233],v100 offset:32768\n"
"ds_read_b64_tr_b16 v[234:235],v100 offset:33792\n"
"s_waitcnt lgkmcnt(8)\n"
"v_mfma_f32_16x16x32_bf16 a[216:219],v[184:187],v[152:155],0\n"
"ds_read_b64_tr_b16 v[236:237],v101 offset:32768\n"
"ds_read_b64_tr_b16 v[238:239],v101 offset:33792\n"
"v_mfma_f32_16x16x32_bf16 a[248:251],v[184:187],v[156:159],0\n"
"ds_read_b64_tr_b16 v[240:241],v102 offset:32768\n"
"ds_read_b64_tr_b16 v[242:243],v102 offset:33792\n"
"v_mfma_f32_16x16x32_bf16 a[252:255],v[188:191],v[156:159],0\n"
"ds_read_b64_tr_b16 v[244:245],v103 offset:32768\n"
"ds_read_b64_tr_b16 v[246:247],v103 offset:33792\n"
"v_mfma_f32_16x16x32_bf16 a[220:223],v[188:191],v[152:155],0\n"
"ds_read_b64_tr_b16 v[248:249],v104 offset:32768\n"
"ds_read_b64_tr_b16 v[250:251],v104 offset:33792\n"
"s_waitcnt lgkmcnt(10)\n"
"v_mfma_f32_16x16x32_bf16 a[0:3],v[160:163],v[128:131],0\n"
"ds_read_b64_tr_b16 v[252:253],v105 offset:32768\n"
"ds_read_b64_tr_b16 v[254:255],v105 offset:33792\n"
"v_mfma_f32_16x16x32_bf16 a[0:3],v[224:227],v[192:195],a[0:3]\n"
"ds_read_b128 v[132:135],v80 offset:2048\n"
"ds_read_b128 v[196:199],v81 offset:2048\n"
"v_mfma_f32_16x16x32_bf16 a[4:7],v[228:231],v[192:195],0\n"
"ds_read_b128 v[216:219],v81 offset:12288\n"
"ds_read_b128 v[220:223],v81 offset:14336\n"
"v_mfma_f32_16x16x32_bf16 a[4:7],v[164:167],v[128:131],a[4:7]\n"
"s_waitcnt lgkmcnt(12)\n"
"s_mov_b32 s76,262272\n"
"s_add_u32 m0,s79,36864\n"
"v_mfma_f32_16x16x32_bf16 a[8:11],v[168:171],v[128:131],0\n"
"buffer_load_dwordx4 v106,s[16:19],s76 offen offset:0 lds\n"
"v_mfma_f32_16x16x32_bf16 a[8:11],v[232:235],v[192:195],a[8:11]\n"
"buffer_load_dwordx4 v107,s[16:19],s76 offen offset:1024 lds\n"
"v_mfma_f32_16x16x32_bf16 a[12:15],v[236:239],v[192:195],0\n"
"buffer_load_dwordx4 v108,s[16:19],s76 offen offset:2048 lds\n"
"buffer_load_dwordx4 v109,s[16:19],s76 offen offset:3072 lds\n"
"v_mfma_f32_16x16x32_bf16 a[12:15],v[172:175],v[128:131],a[12:15]\n"
"s_waitcnt lgkmcnt(8)\n"
"s_mov_b32 s77,524288\n"
"s_add_u32 m0,s78,98304\n"
"v_mfma_f32_16x16x32_bf16 a[16:19],v[176:179],v[128:131],0\n"
"buffer_load_dwordx4 v110,s[20:23],s77 offen offset:0 lds\n"
"v_mfma_f32_16x16x32_bf16 a[16:19],v[240:243],v[192:195],a[16:19]\n"
"buffer_load_dwordx4 v111,s[20:23],s77 offen offset:1024 lds\n"
"v_mfma_f32_16x16x32_bf16 a[20:23],v[244:247],v[192:195],0\n"
"buffer_load_dwordx4 v113,s[20:23],s77 offen offset:2048 lds\n"
"buffer_load_dwordx4 v114,s[20:23],s77 offen offset:3072 lds\n"
"v_mfma_f32_16x16x32_bf16 a[20:23],v[180:183],v[128:131],a[20:23]\n"
"s_waitcnt lgkmcnt(4)\n"
"s_mov_b32 s77,786432\n"
"s_add_u32 m0,s78,114688\n"
"v_mfma_f32_16x16x32_bf16 a[24:27],v[184:187],v[128:131],0\n"
"buffer_load_dwordx4 v110,s[20:23],s77 offen offset:0 lds\n"
"v_mfma_f32_16x16x32_bf16 a[24:27],v[248:251],v[192:195],a[24:27]\n"
"buffer_load_dwordx4 v111,s[20:23],s77 offen offset:1024 lds\n"
"v_mfma_f32_16x16x32_bf16 a[28:31],v[252:255],v[192:195],0\n"
"buffer_load_dwordx4 v113,s[20:23],s77 offen offset:2048 lds\n"
"buffer_load_dwordx4 v114,s[20:23],s77 offen offset:3072 lds\n"
"v_mfma_f32_16x16x32_bf16 a[28:31],v[188:191],v[128:131],a[28:31]\n"
"s_waitcnt lgkmcnt(2)\n"
"s_mov_b32 s76,256\n"
"s_add_u32 m0,s79,65536\n"
"v_mfma_f32_16x16x32_bf16 a[32:35],v[160:163],v[132:135],0\n"
"buffer_load_dwordx4 v106,s[16:19],s76 offen offset:0 lds\n"
"v_mfma_f32_16x16x32_bf16 a[32:35],v[224:227],v[196:199],a[32:35]\n"
"buffer_load_dwordx4 v107,s[16:19],s76 offen offset:1024 lds\n"
"v_mfma_f32_16x16x32_bf16 a[36:39],v[228:231],v[196:199],0\n"
"buffer_load_dwordx4 v108,s[16:19],s76 offen offset:2048 lds\n"
"buffer_load_dwordx4 v109,s[16:19],s76 offen offset:3072 lds\n"
"v_mfma_f32_16x16x32_bf16 a[36:39],v[164:167],v[132:135],a[36:39]\n"
"s_waitcnt vmcnt(21)\n"
"s_barrier\n"
"v_mfma_f32_16x16x32_bf16 a[40:43],v[168:171],v[132:135],0\n"
"ds_read_b128 v[136:139],v80 offset:4096\n"
"ds_read_b128 v[200:203],v81 offset:4096\n"
"v_mfma_f32_16x16x32_bf16 a[40:43],v[232:235],v[196:199],a[40:43]\n"
"ds_read_b128 v[140:143],v80 offset:6144\n"
"ds_read_b128 v[204:207],v81 offset:6144\n"
"v_mfma_f32_16x16x32_bf16 a[44:47],v[236:239],v[196:199],0\n"
"ds_read_b128 v[144:147],v80 offset:8192\n"
"ds_read_b128 v[208:211],v81 offset:8192\n"
"v_mfma_f32_16x16x32_bf16 a[44:47],v[172:175],v[132:135],a[44:47]\n"
"ds_read_b128 v[148:151],v80 offset:10240\n"
"ds_read_b128 v[212:215],v81 offset:10240\n"
"v_mfma_f32_16x16x32_bf16 a[48:51],v[176:179],v[132:135],0\n"
"s_mov_b32 s60,s91\n"
"s_mov_b32 s76,262400\n"
"s_add_u32 m0,s79,69632\n"
"v_mfma_f32_16x16x32_bf16 a[48:51],v[240:243],v[196:199],a[48:51]\n"
"buffer_load_dwordx4 v106,s[16:19],s76 offen offset:0 lds\n"
"v_mfma_f32_16x16x32_bf16 a[52:55],v[244:247],v[196:199],0\n"
"buffer_load_dwordx4 v107,s[16:19],s76 offen offset:1024 lds\n"
"v_mfma_f32_16x16x32_bf16 a[52:55],v[180:183],v[132:135],a[52:55]\n"
"buffer_load_dwordx4 v108,s[16:19],s76 offen offset:2048 lds\n"
"v_mfma_f32_16x16x32_bf16 a[56:59],v[184:187],v[132:135],0\n"
"buffer_load_dwordx4 v109,s[16:19],s76 offen offset:3072 lds\n"
"v_mfma_f32_16x16x32_bf16 a[56:59],v[248:251],v[196:199],a[56:59]\n"
"s_sub_u32 s94,15,s91\n"
"s_waitcnt lgkmcnt(0)\n"
"s_barrier\n"
"v_mfma_f32_16x16x32_bf16 a[60:63],v[252:255],v[196:199],0\n"
"s_cmp_lt_u32 s52,2\n"
"s_cselect_b32 s95,s94,s91\n"
"s_mov_b32 s77,1048576\n"
"s_add_u32 m0,s78,147456\n"
"v_mfma_f32_16x16x32_bf16 a[60:63],v[188:191],v[132:135],a[60:63]\n"
"buffer_load_dwordx4 v110,s[20:23],s77 offen offset:0 lds\n"
"v_mfma_f32_16x16x32_bf16 a[64:67],v[160:163],v[136:139],0\n"
"s_lshr_b32 vcc_lo,s95,1\n"
"s_lshl_b32 s55,vcc_lo,19\n"
"v_mfma_f32_16x16x32_bf16 a[64:67],v[224:227],v[200:203],a[64:67]\n"
"s_lshl_b32 s58,vcc_lo,10\n"
"s_add_u32 s58,s58,148898816\n"
"v_mfma_f32_16x16x32_bf16 a[68:71],v[228:231],v[200:203],0\n"
"s_and_b32 vcc_lo,s95,1\n"
"s_lshl_b32 vcc_lo,vcc_lo,13\n"
"v_mfma_f32_16x16x32_bf16 a[68:71],v[164:167],v[136:139],a[68:71]\n"
"buffer_load_dwordx4 v111,s[20:23],s77 offen offset:1024 lds\n"
"v_mfma_f32_16x16x32_bf16 a[72:75],v[168:171],v[136:139],0\n"
"s_and_b32 vcc_hi,s78,4096\n"
"s_add_u32 s55,s55,vcc_lo\n"
"v_mfma_f32_16x16x32_bf16 a[72:75],v[232:235],v[200:203],a[72:75]\n"
"s_add_u32 s55,s55,vcc_hi\n"
"s_and_b32 vcc_lo,s89,2\n"
"v_mfma_f32_16x16x32_bf16 a[76:79],v[236:239],v[200:203],0\n"
"s_lshl2_add_u32 vcc_lo,s88,vcc_lo\n"
"s_and_b32 vcc_hi,s52,1\n"
"v_mfma_f32_16x16x32_bf16 a[76:79],v[172:175],v[136:139],a[76:79]\n"
"buffer_load_dwordx4 v113,s[20:23],s77 offen offset:2048 lds\n"
"v_mfma_f32_16x16x32_bf16 a[80:83],v[176:179],v[136:139],0\n"
"s_add_u32 vcc_lo,vcc_lo,vcc_hi\n"
"s_lshr_b32 vcc_hi,s89,2\n"
"v_mfma_f32_16x16x32_bf16 a[80:83],v[240:243],v[200:203],a[80:83]\n"
"s_lshr_b32 s84,s52,1\n"
"s_lshl1_add_u32 vcc_hi,vcc_hi,s84\n"
"v_mfma_f32_16x16x32_bf16 a[84:87],v[244:247],v[200:203],0\n"
"s_mul_i32 s56,vcc_lo,1179648\n"
"s_lshl_b32 s84,s84,19\n"
"v_mfma_f32_16x16x32_bf16 a[84:87],v[180:183],v[136:139],a[84:87]\n"
"buffer_load_dwordx4 v114,s[20:23],s77 offen offset:3072 lds\n"
"v_mfma_f32_16x16x32_bf16 a[88:91],v[184:187],v[136:139],0\n"
"s_add_u32 s56,s56,s84\n"
"s_lshr_b32 s84,s89,2\n"
"v_mfma_f32_16x16x32_bf16 a[88:91],v[248:251],v[200:203],a[88:91]\n"
"s_lshl_b32 s84,s84,14\n"
"s_add_u32 s56,s56,s84\n"
"v_mfma_f32_16x16x32_bf16 a[92:95],v[252:255],v[200:203],0\n"
"s_lshl_b32 s84,s53,13\n"
"s_mov_b32 s77,1310720\n"
"s_add_u32 m0,s78,131072\n"
"v_mfma_f32_16x16x32_bf16 a[92:95],v[188:191],v[136:139],a[92:95]\n"
"buffer_load_dwordx4 v110,s[20:23],s77 offen offset:0 lds\n"
"v_mfma_f32_16x16x32_bf16 a[96:99],v[160:163],v[140:143],0\n"
"s_add_u32 s56,s56,s84\n"
"s_add_u32 s56,s56,4194304\n"
"v_mfma_f32_16x16x32_bf16 a[96:99],v[224:227],v[204:207],a[96:99]\n"
"s_lshl4_add_u32 vcc_hi,s53,vcc_hi\n"
"s_lshl_b32 vcc_lo,vcc_lo,8\n"
"v_mfma_f32_16x16x32_bf16 a[100:103],v[228:231],v[204:207],0\n"
"buffer_load_dwordx4 v111,s[20:23],s77 offen offset:1024 lds\n"
"v_mfma_f32_16x16x32_bf16 a[100:103],v[164:167],v[140:143],a[100:103]\n"
"s_lshl3_add_u32 s59,vcc_hi,vcc_lo\n"
"s_add_u32 s59,s59,148914176\n"
"v_mfma_f32_16x16x32_bf16 a[104:107],v[168:171],v[140:143],0\n"
"s_lshl_b32 s57,s92,3\n"
"s_add_u32 s57,s57,148898816\n"
"v_mfma_f32_16x16x32_bf16 a[104:107],v[232:235],v[204:207],a[104:107]\n"
"s_lshl_b32 s63,s90,8\n"
"s_add_u32 s63,s63,148914176\n"
"v_mfma_f32_16x16x32_bf16 a[108:111],v[236:239],v[204:207],0\n"
"buffer_load_dwordx4 v113,s[20:23],s77 offen offset:2048 lds\n"
"v_mfma_f32_16x16x32_bf16 a[108:111],v[172:175],v[140:143],a[108:111]\n"
"s_lshl_b32 s74,s92,11\n"
"s_add_u32 s74,s74,171966464\n"
"v_mfma_f32_16x16x32_bf16 a[112:115],v[176:179],v[140:143],0\n"
"s_mul_i32 s96,s90,1179648\n"
"s_add_u32 s96,s96,4194304\n"
"v_mfma_f32_16x16x32_bf16 a[112:115],v[240:243],v[204:207],a[112:115]\n"
"s_add_u32 s73,s96,s85\n"
"s_and_b32 vcc_hi,s78,4096\n"
"v_mfma_f32_16x16x32_bf16 a[116:119],v[244:247],v[204:207],0\n"
"buffer_load_dwordx4 v114,s[20:23],s77 offen offset:3072 lds\n"
"v_mfma_f32_16x16x32_bf16 a[116:119],v[180:183],v[140:143],a[116:119]\n"
"s_lshl_b32 vcc_hi,vcc_hi,1\n"
"s_add_u32 vcc_lo,s96,vcc_hi\n"
"v_mfma_f32_16x16x32_bf16 a[120:123],v[184:187],v[140:143],0\n"
"s_add_u32 s65,vcc_lo,524288\n"
"s_lshl_b32 s66,s91,16\n"
"v_mfma_f32_16x16x32_bf16 a[120:123],v[248:251],v[204:207],a[120:123]\n"
"s_sub_u32 s66,vcc_lo,s66\n"
"s_mov_b32 s76,384\n"
"s_add_u32 m0,s79,0\n"
"v_mfma_f32_16x16x32_bf16 a[124:127],v[252:255],v[204:207],0\n"
"buffer_load_dwordx4 v106,s[16:19],s76 offen offset:0 lds\n"
"v_mfma_f32_16x16x32_bf16 a[124:127],v[188:191],v[140:143],a[124:127]\n"
"s_add_u32 s66,s66,999424\n"
"s_add_u32 s67,vcc_hi,65536\n"
"v_mfma_f32_16x16x32_bf16 a[128:131],v[160:163],v[144:147],0\n"
"s_mul_i32 vcc_lo,vcc_hi,5\n"
"s_sub_u32 vcc_lo,s96,vcc_lo\n"
"v_mfma_f32_16x16x32_bf16 a[128:131],v[224:227],v[208:211],a[128:131]\n"
"s_add_u32 s64,vcc_lo,81920\n"
"s_add_u32 s62,vcc_lo,135168\n"
"s_add_u32 s61,vcc_lo,524288\n"
"v_mfma_f32_16x16x32_bf16 a[132:135],v[228:231],v[208:211],0\n"
"buffer_load_dwordx4 v107,s[16:19],s76 offen offset:1024 lds\n"
"v_mfma_f32_16x16x32_bf16 a[132:135],v[164:167],v[144:147],a[132:135]\n"
"s_cmp_lt_u32 s52,2\n"
"s_cselect_b32 s64,s64,2146435072\n"
"s_cselect_b32 s67,s67,163840\n"
"s_cselect_b32 s65,s65,1\n"
"v_mfma_f32_16x16x32_bf16 a[136:139],v[168:171],v[144:147],0\n"
"s_cselect_b32 vcc_lo,4,0\n"
"s_and_b32 vcc_hi,s52,1\n"
"s_add_u32 vcc_lo,vcc_lo,vcc_hi\n"
"v_mfma_f32_16x16x32_bf16 a[136:139],v[232:235],v[208:211],a[136:139]\n"
"s_lshl_b32 vcc_hi,s90,12\n"
"s_add_u32 vcc_hi,vcc_hi,54525952\n"
"v_mfma_f32_16x16x32_bf16 a[140:143],v[236:239],v[208:211],0\n"
"buffer_load_dwordx4 v108,s[16:19],s76 offen offset:2048 lds\n"
"v_mfma_f32_16x16x32_bf16 a[140:143],v[172:175],v[144:147],a[140:143]\n"
"s_lshl3_add_u32 s70,s95,vcc_lo\n"
"s_lshl_b32 s70,s70,17\n"
"v_mfma_f32_16x16x32_bf16 a[144:147],v[176:179],v[144:147],0\n"
"s_add_u32 s70,s70,vcc_hi\n"
"s_lshl3_add_u32 s68,s94,vcc_lo\n"
"v_mfma_f32_16x16x32_bf16 a[144:147],v[240:243],v[208:211],a[144:147]\n"
"s_lshl_b32 s68,s68,17\n"
"s_add_u32 s68,s68,vcc_hi\n"
"v_mfma_f32_16x16x32_bf16 a[148:151],v[244:247],v[208:211],0\n"
"buffer_load_dwordx4 v109,s[16:19],s76 offen offset:3072 lds\n"
"v_mfma_f32_16x16x32_bf16 a[148:151],v[180:183],v[144:147],a[148:151]\n"
"s_lshr_b32 vcc_lo,s90,3\n"
"s_lshl2_add_u32 vcc_lo,vcc_lo,s52\n"
"v_mfma_f32_16x16x32_bf16 a[152:155],v[184:187],v[144:147],0\n"
"s_lshl_b32 vcc_lo,vcc_lo,7\n"
"s_and_b32 vcc_hi,s90,7\n"
"v_mfma_f32_16x16x32_bf16 a[152:155],v[248:251],v[208:211],a[152:155]\n"
"s_add_u32 vcc_lo,vcc_lo,vcc_hi\n"
"s_mov_b32 s76,262528\n"
"s_add_u32 m0,s79,4096\n"
"v_mfma_f32_16x16x32_bf16 a[156:159],v[252:255],v[208:211],0\n"
"buffer_load_dwordx4 v106,s[16:19],s76 offen offset:0 lds\n"
"v_mfma_f32_16x16x32_bf16 a[156:159],v[188:191],v[144:147],a[156:159]\n"
"s_lshl3_add_u32 s69,s91,vcc_lo\n"
"s_lshl_b32 s69,s69,3\n"
"v_mfma_f32_16x16x32_bf16 a[160:163],v[160:163],v[148:151],0\n"
"s_add_u32 s69,s69,148930560\n"
"s_lshl3_add_u32 s71,s94,vcc_lo\n"
"v_mfma_f32_16x16x32_bf16 a[160:163],v[224:227],v[212:215],a[160:163]\n"
"s_lshl_b32 s71,s71,3\n"
"s_add_u32 s71,s71,148930560\n"
"v_mfma_f32_16x16x32_bf16 a[164:167],v[228:231],v[212:215],0\n"
"buffer_load_dwordx4 v107,s[16:19],s76 offen offset:1024 lds\n"
"v_mfma_f32_16x16x32_bf16 a[164:167],v[164:167],v[148:151],a[164:167]\n"
"s_mov_b32 s6,8388608\n"
"s_mov_b32 s7,0x20000\n"
"v_mfma_f32_16x16x32_bf16 a[168:171],v[168:171],v[148:151],0\n"
"s_lshr_b32 s94,s88,1\n"
"s_and_b32 vcc_lo,s91,1\n"
"v_mfma_f32_16x16x32_bf16 a[168:171],v[232:235],v[212:215],a[168:171]\n"
"s_lshl1_add_u32 s95,s97,vcc_lo\n"
"s_and_b32 vcc_lo,s88,1\n"
"v_mfma_f32_16x16x32_bf16 a[172:175],v[236:239],v[212:215],0\n"
"buffer_load_dwordx4 v108,s[16:19],s76 offen offset:2048 lds\n"
"v_mfma_f32_16x16x32_bf16 a[172:175],v[172:175],v[148:151],a[172:175]\n"
"s_lshr_b32 vcc_hi,s91,1\n"
"s_lshl2_add_u32 s96,vcc_lo,vcc_hi\n"
"v_mfma_f32_16x16x32_bf16 a[176:179],v[176:179],v[148:151],0\n"
"s_lshr_b32 vcc_lo,s88,2\n"
"s_lshl2_add_u32 s96,vcc_lo,vcc_hi\n"
"v_mfma_f32_16x16x32_bf16 a[176:179],v[240:243],v[212:215],a[176:179]\n"
"s_sub_u32 vcc_lo,7,s88\n"
"s_min_u32 s94,vcc_lo,s88\n"
"v_mfma_f32_16x16x32_bf16 a[180:183],v[244:247],v[212:215],0\n"
"buffer_load_dwordx4 v109,s[16:19],s76 offen offset:3072 lds\n"
"v_mfma_f32_16x16x32_bf16 a[180:183],v[180:183],v[148:151],a[180:183]\n"
"s_and_b32 s96,s88,4\n"
"s_add_u32 s96,s96,s97\n"
"s_add_u32 s95,s91,s96\n"
"v_mfma_f32_16x16x32_bf16 a[184:187],v[184:187],v[148:151],0\n"
"buffer_load_dwordx4 v[8:11],v70,s[32:35],0 offen offset:0\n"
"v_mfma_f32_16x16x32_bf16 a[184:187],v[248:251],v[212:215],a[184:187]\n"
"buffer_load_dwordx4 v[12:15],v70,s[32:35],0 offen offset:512\n"
"s_and_b32 s95,s95,7\n"
"v_mfma_f32_16x16x32_bf16 a[188:191],v[252:255],v[212:215],0\n"
"buffer_load_dwordx4 v[16:19],v70,s[32:35],0 offen offset:1024\n"
"buffer_load_dwordx4 v[20:23],v70,s[32:35],0 offen offset:1536\n"
"s_lshl3_add_u32 vcc_lo,s94,s96\n"
"v_mfma_f32_16x16x32_bf16 a[188:191],v[188:191],v[148:151],a[188:191]\n"
"s_waitcnt vmcnt(28)\n"
"s_barrier\n"
"v_mfma_f32_16x16x32_bf16 a[192:195],v[224:227],v[216:219],a[192:195]\n"
"ds_read_b128 v[152:155],v80 offset:45056\n"
"s_mov_b32 s87,524288\n"
"v_mfma_f32_16x16x32_bf16 a[224:227],v[224:227],v[220:223],a[224:227]\n"
"ds_read_b128 v[156:159],v80 offset:47104\n"
"buffer_load_dword v115,v112,s[36:39],s87 offen\n"
"v_mfma_f32_16x16x32_bf16 a[228:231],v[228:231],v[220:223],a[228:231]\n"
"ds_read_b64_tr_b16 v[160:161],v98 offset:0\n"
"s_mov_b32 s87,1048576\n"
"v_mfma_f32_16x16x32_bf16 a[196:199],v[228:231],v[216:219],a[196:199]\n"
"ds_read_b64_tr_b16 v[162:163],v98 offset:1024\n"
"buffer_load_dword v115,v112,s[36:39],s87 offen\n"
"v_mfma_f32_16x16x32_bf16 a[200:203],v[232:235],v[216:219],a[200:203]\n"
"ds_read_b64_tr_b16 v[164:165],v99 offset:0\n"
"s_mov_b32 s87,1572864\n"
"v_mfma_f32_16x16x32_bf16 a[232:235],v[232:235],v[220:223],a[232:235]\n"
"ds_read_b64_tr_b16 v[166:167],v99 offset:1024\n"
"buffer_load_dword v115,v112,s[36:39],s87 offen\n"
"v_mfma_f32_16x16x32_bf16 a[236:239],v[236:239],v[220:223],a[236:239]\n"
"ds_read_b64_tr_b16 v[168:169],v100 offset:0\n"
"s_lshl3_add_u32 vcc_lo,vcc_lo,s52\n"
"v_mfma_f32_16x16x32_bf16 a[204:207],v[236:239],v[216:219],a[204:207]\n"
"ds_read_b64_tr_b16 v[170:171],v100 offset:1024\n"
"s_lshl_b32 s13,vcc_lo,15\n"
"v_mfma_f32_16x16x32_bf16 a[208:211],v[240:243],v[216:219],a[208:211]\n"
"ds_read_b64_tr_b16 v[172:173],v101 offset:0\n"
"s_lshl_b32 vcc_lo,s95,8\n"
"v_mfma_f32_16x16x32_bf16 a[240:243],v[240:243],v[220:223],a[240:243]\n"
"ds_read_b64_tr_b16 v[174:175],v101 offset:1024\n"
"s_add_u32 s13,s13,vcc_lo\n"
"v_mfma_f32_16x16x32_bf16 a[244:247],v[244:247],v[220:223],a[244:247]\n"
"ds_read_b64_tr_b16 v[176:177],v102 offset:0\n"
"s_lshl3_add_u32 vcc_hi,s94,s95\n"
"v_mfma_f32_16x16x32_bf16 a[212:215],v[244:247],v[216:219],a[212:215]\n"
"ds_read_b64_tr_b16 v[178:179],v102 offset:1024\n"
"s_lshl_b32 s72,vcc_hi,8\n"
"v_mfma_f32_16x16x32_bf16 a[216:219],v[248:251],v[216:219],a[216:219]\n"
"ds_read_b64_tr_b16 v[180:181],v103 offset:0\n"
"s_add_u32 s72,s72,149061632\n"
"v_mfma_f32_16x16x32_bf16 a[248:251],v[248:251],v[220:223],a[248:251]\n"
"s_or_b32 vcc_lo,s96,s52\n"
"ds_read_b64_tr_b16 v[182:183],v103 offset:1024\n"
"s_cselect_b32 s72,2146435072,s72\n"
"v_mfma_f32_16x16x32_bf16 a[252:255],v[252:255],v[220:223],a[252:255]\n"
"ds_read_b64_tr_b16 v[184:185],v104 offset:0\n"
"s_lshl_b32 s3,vcc_hi,18\n"
"v_mfma_f32_16x16x32_bf16 a[220:223],v[252:255],v[216:219],a[220:223]\n"
"s_lshl_b32 vcc_lo,s96,15\n"
"ds_read_b64_tr_b16 v[186:187],v104 offset:1024\n"
"s_waitcnt lgkmcnt(10)\n"
"v_mfma_f32_16x16x32_bf16 a[192:195],v[160:163],v[152:155],a[192:195]\n"
"ds_read_b64_tr_b16 v[188:189],v105 offset:0\n"
"s_sub_u32 s3,s3,vcc_lo\n"
"v_mfma_f32_16x16x32_bf16 a[224:227],v[160:163],v[156:159],a[224:227]\n"
"ds_read_b64_tr_b16 v[190:191],v105 offset:1024\n"
"s_add_u32 s3,s3,s78\n"
"v_mfma_f32_16x16x32_bf16 a[228:231],v[164:167],v[156:159],a[228:231]\n"
"ds_read_b128 v[128:131],v80 offset:32768\n"
"s_add_u32 s3,s3,100909056\n"
"v_mfma_f32_16x16x32_bf16 a[196:199],v[164:167],v[152:155],a[196:199]\n"
"ds_read_b128 v[192:195],v81 offset:32768\n"
"s_waitcnt lgkmcnt(10)\n"
"v_mfma_f32_16x16x32_bf16 a[200:203],v[168:171],v[152:155],a[200:203]\n"
"ds_read_b64_tr_b16 v[224:225],v98 offset:16384\n"
"s_lshl3_add_u32 vcc_hi,vcc_hi,s96\n"
"v_mfma_f32_16x16x32_bf16 a[232:235],v[168:171],v[156:159],a[232:235]\n"
"ds_read_b64_tr_b16 v[226:227],v98 offset:17408\n"
"s_lshl2_add_u32 vcc_hi,vcc_hi,s52\n"
"v_mfma_f32_16x16x32_bf16 a[236:239],v[172:175],v[156:159],a[236:239]\n"
"ds_read_b64_tr_b16 v[228:229],v99 offset:16384\n"
"s_lshl_b32 s2,vcc_hi,4\n"
"v_mfma_f32_16x16x32_bf16 a[204:207],v[172:175],v[152:155],a[204:207]\n"
"ds_read_b64_tr_b16 v[230:231],v99 offset:17408\n"
"s_waitcnt lgkmcnt(10)\n"
"v_mfma_f32_16x16x32_bf16 a[208:211],v[176:179],v[152:155],a[208:211]\n"
"ds_read_b64_tr_b16 v[232:233],v100 offset:16384\n"
"s_add_u32 s2,s2,149028864\n"
"v_mfma_f32_16x16x32_bf16 a[240:243],v[176:179],v[156:159],a[240:243]\n"
"ds_read_b64_tr_b16 v[234:235],v100 offset:17408\n"
"s_lshl2_add_u32 vcc_lo,s95,s52\n"
"v_mfma_f32_16x16x32_bf16 a[244:247],v[180:183],v[156:159],a[244:247]\n"
"ds_read_b64_tr_b16 v[236:237],v101 offset:16384\n"
"s_lshl_b32 vcc_lo,vcc_lo,10\n"
"v_mfma_f32_16x16x32_bf16 a[212:215],v[180:183],v[152:155],a[212:215]\n"
"ds_read_b64_tr_b16 v[238:239],v101 offset:17408\n"
"s_waitcnt lgkmcnt(10)\n"
"v_mfma_f32_16x16x32_bf16 a[216:219],v[184:187],v[152:155],a[216:219]\n"
"ds_read_b64_tr_b16 v[240:241],v102 offset:16384\n"
"s_lshl_b32 vcc_hi,s94,22\n"
"v_mfma_f32_16x16x32_bf16 a[248:251],v[184:187],v[156:159],a[248:251]\n"
"ds_read_b64_tr_b16 v[242:243],v102 offset:17408\n"
"s_add_u32 s12,vcc_lo,vcc_hi\n"
"v_mfma_f32_16x16x32_bf16 a[252:255],v[188:191],v[156:159],a[252:255]\n"
"ds_read_b64_tr_b16 v[244:245],v103 offset:16384\n"
"s_lshl_b32 vcc_lo,s96,19\n"
"v_mfma_f32_16x16x32_bf16 a[220:223],v[188:191],v[152:155],a[220:223]\n"
"ds_read_b64_tr_b16 v[246:247],v103 offset:17408\n"
"s_waitcnt lgkmcnt(8)\n"
"v_mfma_f32_16x16x32_bf16 a[0:3],v[160:163],v[128:131],a[0:3]\n"
"ds_read_b64_tr_b16 v[248:249],v104 offset:16384\n"
"s_add_u32 s10,s12,vcc_lo\n"
"v_mfma_f32_16x16x32_bf16 a[0:3],v[224:227],v[192:195],a[0:3]\n"
"ds_read_b64_tr_b16 v[250:251],v104 offset:17408\n"
"s_add_u32 s10,s10,71303168\n"
"v_mfma_f32_16x16x32_bf16 a[4:7],v[228:231],v[192:195],a[4:7]\n"
"ds_read_b64_tr_b16 v[252:253],v105 offset:16384\n"
"s_mul_i32 vcc_lo,s52,31744\n"
"v_mfma_f32_16x16x32_bf16 a[4:7],v[164:167],v[128:131],a[4:7]\n"
"ds_read_b64_tr_b16 v[254:255],v105 offset:17408\n"
"s_waitcnt lgkmcnt(8)\n"
"v_mfma_f32_16x16x32_bf16 a[8:11],v[168:171],v[128:131],a[8:11]\n"
"ds_read_b128 v[132:135],v80 offset:34816\n"
"s_add_u32 s10,s10,vcc_lo\n"
"v_mfma_f32_16x16x32_bf16 a[8:11],v[232:235],v[192:195],a[8:11]\n"
"ds_read_b128 v[196:199],v81 offset:34816\n"
"s_lshl_b32 vcc_lo,s52,10\n"
"v_mfma_f32_16x16x32_bf16 a[12:15],v[236:239],v[192:195],a[12:15]\n"
"ds_read_b128 v[136:139],v80 offset:36864\n"
"s_sub_u32 s12,57671680,vcc_lo\n"
"v_mfma_f32_16x16x32_bf16 a[12:15],v[172:175],v[128:131],a[12:15]\n"
"ds_read_b128 v[200:203],v81 offset:36864\n"
"s_waitcnt lgkmcnt(8)\n"
"v_mfma_f32_16x16x32_bf16 a[16:19],v[176:179],v[128:131],a[16:19]\n"
"ds_read_b128 v[140:143],v80 offset:38912\n"
"s_add_u32 s8,s79,54525952\n"
"v_mfma_f32_16x16x32_bf16 a[16:19],v[240:243],v[192:195],a[16:19]\n"
"ds_read_b128 v[204:207],v81 offset:38912\n"
"s_and_b32 vcc_lo,s96,1\n"
"v_mfma_f32_16x16x32_bf16 a[20:23],v[244:247],v[192:195],a[20:23]\n"
"ds_read_b128 v[144:147],v80 offset:40960\n"
"s_lshl_b32 s11,vcc_lo,10\n"
"v_mfma_f32_16x16x32_bf16 a[20:23],v[180:183],v[128:131],a[20:23]\n"
"ds_read_b128 v[208:211],v81 offset:40960\n"
"s_waitcnt lgkmcnt(8)\n"
"v_mfma_f32_16x16x32_bf16 a[24:27],v[184:187],v[128:131],a[24:27]\n"
"ds_read_b128 v[148:151],v80 offset:43008\n"
"s_add_u32 s11,s11,148932608\n"
"v_mfma_f32_16x16x32_bf16 a[24:27],v[248:251],v[192:195],a[24:27]\n"
"ds_read_b128 v[212:215],v81 offset:43008\n"
"s_mov_b32 s9,148931072\n"
"v_mfma_f32_16x16x32_bf16 a[28:31],v[252:255],v[192:195],a[28:31]\n"
"s_cmp_eq_u32 s52,0\n"
"ds_read_b128 v[216:219],v81 offset:45056\n"
"s_cselect_b32 s71,s71,2146435072\n"
"v_mfma_f32_16x16x32_bf16 a[28:31],v[188:191],v[128:131],a[28:31]\n"
"ds_read_b128 v[220:223],v81 offset:47104\n"
"s_waitcnt lgkmcnt(10)\n"
"v_mfma_f32_16x16x32_bf16 a[32:35],v[160:163],v[132:135],a[32:35]\n"
"v_mfma_f32_16x16x32_bf16 a[32:35],v[224:227],v[196:199],a[32:35]\n"
"v_mfma_f32_16x16x32_bf16 a[36:39],v[228:231],v[196:199],a[36:39]\n"
"v_mfma_f32_16x16x32_bf16 a[36:39],v[164:167],v[132:135],a[36:39]\n"
"v_mfma_f32_16x16x32_bf16 a[40:43],v[168:171],v[132:135],a[40:43]\n"
"s_waitcnt lgkmcnt(0)\n"
"s_barrier\n"
"v_mfma_f32_16x16x32_bf16 a[40:43],v[232:235],v[196:199],a[40:43]\n"
"s_mov_b32 s77,1572864\n"
"s_add_u32 m0,s78,98304\n"
"v_mfma_f32_16x16x32_bf16 a[44:47],v[236:239],v[196:199],a[44:47]\n"
"buffer_load_dwordx4 v110,s[20:23],s77 offen offset:0 lds\n"
"v_mfma_f32_16x16x32_bf16 a[44:47],v[172:175],v[132:135],a[44:47]\n"
"v_mfma_f32_16x16x32_bf16 a[48:51],v[176:179],v[132:135],a[48:51]\n"
"v_mfma_f32_16x16x32_bf16 a[48:51],v[240:243],v[196:199],a[48:51]\n"
"v_mfma_f32_16x16x32_bf16 a[52:55],v[244:247],v[196:199],a[52:55]\n"
"buffer_load_dwordx4 v111,s[20:23],s77 offen offset:1024 lds\n"
"v_mfma_f32_16x16x32_bf16 a[52:55],v[180:183],v[132:135],a[52:55]\n"
"v_mfma_f32_16x16x32_bf16 a[56:59],v[184:187],v[132:135],a[56:59]\n"
"v_mfma_f32_16x16x32_bf16 a[56:59],v[248:251],v[196:199],a[56:59]\n"
"v_mfma_f32_16x16x32_bf16 a[60:63],v[252:255],v[196:199],a[60:63]\n"
"buffer_load_dwordx4 v113,s[20:23],s77 offen offset:2048 lds\n"
"v_mfma_f32_16x16x32_bf16 a[60:63],v[188:191],v[132:135],a[60:63]\n"
"v_mfma_f32_16x16x32_bf16 a[64:67],v[160:163],v[136:139],a[64:67]\n"
"v_mfma_f32_16x16x32_bf16 a[64:67],v[224:227],v[200:203],a[64:67]\n"
"v_mfma_f32_16x16x32_bf16 a[68:71],v[228:231],v[200:203],a[68:71]\n"
"buffer_load_dwordx4 v114,s[20:23],s77 offen offset:3072 lds\n"
"v_mfma_f32_16x16x32_bf16 a[68:71],v[164:167],v[136:139],a[68:71]\n"
"v_mfma_f32_16x16x32_bf16 a[72:75],v[168:171],v[136:139],a[72:75]\n"
"v_mfma_f32_16x16x32_bf16 a[72:75],v[232:235],v[200:203],a[72:75]\n"
"s_mov_b32 s77,1835008\n"
"s_add_u32 m0,s78,114688\n"
"v_mfma_f32_16x16x32_bf16 a[76:79],v[236:239],v[200:203],a[76:79]\n"
"buffer_load_dwordx4 v110,s[20:23],s77 offen offset:0 lds\n"
"v_mfma_f32_16x16x32_bf16 a[76:79],v[172:175],v[136:139],a[76:79]\n"
"v_mfma_f32_16x16x32_bf16 a[80:83],v[176:179],v[136:139],a[80:83]\n"
"v_mfma_f32_16x16x32_bf16 a[80:83],v[240:243],v[200:203],a[80:83]\n"
"v_mfma_f32_16x16x32_bf16 a[84:87],v[244:247],v[200:203],a[84:87]\n"
"buffer_load_dwordx4 v111,s[20:23],s77 offen offset:1024 lds\n"
"v_mfma_f32_16x16x32_bf16 a[84:87],v[180:183],v[136:139],a[84:87]\n"
"v_mfma_f32_16x16x32_bf16 a[88:91],v[184:187],v[136:139],a[88:91]\n"
"v_mfma_f32_16x16x32_bf16 a[88:91],v[248:251],v[200:203],a[88:91]\n"
"v_mfma_f32_16x16x32_bf16 a[92:95],v[252:255],v[200:203],a[92:95]\n"
"buffer_load_dwordx4 v113,s[20:23],s77 offen offset:2048 lds\n"
"v_mfma_f32_16x16x32_bf16 a[92:95],v[188:191],v[136:139],a[92:95]\n"
"s_waitcnt vmcnt(10)\n"
"v_mfma_f32_16x16x32_bf16 a[96:99],v[160:163],v[140:143],a[96:99]\n"
"v_lshlrev_b32 v24,16,v8\n"
"v_and_b32 v25,0xffff0000,v8\n"
"v_mfma_f32_16x16x32_bf16 a[96:99],v[224:227],v[204:207],a[96:99]\n"
"v_lshlrev_b32 v26,16,v9\n"
"v_and_b32 v27,0xffff0000,v9\n"
"v_mfma_f32_16x16x32_bf16 a[100:103],v[228:231],v[204:207],a[100:103]\n"
"buffer_load_dwordx4 v114,s[20:23],s77 offen offset:3072 lds\n"
"v_mfma_f32_16x16x32_bf16 a[100:103],v[164:167],v[140:143],a[100:103]\n"
"v_lshlrev_b32 v28,16,v10\n"
"v_and_b32 v29,0xffff0000,v10\n"
"v_mfma_f32_16x16x32_bf16 a[104:107],v[168:171],v[140:143],a[104:107]\n"
"v_lshlrev_b32 v30,16,v11\n"
"v_and_b32 v31,0xffff0000,v11\n"
"v_mfma_f32_16x16x32_bf16 a[104:107],v[232:235],v[204:207],a[104:107]\n"
"v_lshlrev_b32 v32,16,v12\n"
"v_and_b32 v33,0xffff0000,v12\n"
"v_mfma_f32_16x16x32_bf16 a[108:111],v[236:239],v[204:207],a[108:111]\n"
"s_mov_b32 s76,512\n"
"v_lshlrev_b32 v34,16,v13\n"
"s_add_u32 m0,s79,32768\n"
"v_mfma_f32_16x16x32_bf16 a[108:111],v[172:175],v[140:143],a[108:111]\n"
"buffer_load_dwordx4 v106,s[16:19],s76 offen offset:0 lds\n"
"v_mfma_f32_16x16x32_bf16 a[112:115],v[176:179],v[140:143],a[112:115]\n"
"v_and_b32 v35,0xffff0000,v13\n"
"v_lshlrev_b32 v36,16,v14\n"
"v_mfma_f32_16x16x32_bf16 a[112:115],v[240:243],v[204:207],a[112:115]\n"
"v_and_b32 v37,0xffff0000,v14\n"
"v_lshlrev_b32 v38,16,v15\n"
"v_mfma_f32_16x16x32_bf16 a[116:119],v[244:247],v[204:207],a[116:119]\n"
"v_and_b32 v39,0xffff0000,v15\n"
"v_lshlrev_b32 v40,16,v16\n"
"v_mfma_f32_16x16x32_bf16 a[116:119],v[180:183],v[140:143],a[116:119]\n"
"v_and_b32 v41,0xffff0000,v16\n"
"v_lshlrev_b32 v42,16,v17\n"
"v_mfma_f32_16x16x32_bf16 a[120:123],v[184:187],v[140:143],a[120:123]\n"
"buffer_load_dwordx4 v107,s[16:19],s76 offen offset:1024 lds\n"
"v_mfma_f32_16x16x32_bf16 a[120:123],v[248:251],v[204:207],a[120:123]\n"
"v_and_b32 v43,0xffff0000,v17\n"
"v_lshlrev_b32 v44,16,v18\n"
"v_mfma_f32_16x16x32_bf16 a[124:127],v[252:255],v[204:207],a[124:127]\n"
"v_and_b32 v45,0xffff0000,v18\n"
"v_lshlrev_b32 v46,16,v19\n"
"v_mfma_f32_16x16x32_bf16 a[124:127],v[188:191],v[140:143],a[124:127]\n"
"v_and_b32 v47,0xffff0000,v19\n"
"v_lshlrev_b32 v48,16,v20\n"
"v_mfma_f32_16x16x32_bf16 a[128:131],v[160:163],v[144:147],a[128:131]\n"
"v_and_b32 v49,0xffff0000,v20\n"
"v_lshlrev_b32 v50,16,v21\n"
"v_mfma_f32_16x16x32_bf16 a[128:131],v[224:227],v[208:211],a[128:131]\n"
"buffer_load_dwordx4 v108,s[16:19],s76 offen offset:2048 lds\n"
"v_mfma_f32_16x16x32_bf16 a[132:135],v[228:231],v[208:211],a[132:135]\n"
"v_and_b32 v51,0xffff0000,v21\n"
"v_lshlrev_b32 v52,16,v22\n"
"v_mfma_f32_16x16x32_bf16 a[132:135],v[164:167],v[144:147],a[132:135]\n"
"v_and_b32 v53,0xffff0000,v22\n"
"v_lshlrev_b32 v54,16,v23\n"
"v_mfma_f32_16x16x32_bf16 a[136:139],v[168:171],v[144:147],a[136:139]\n"
"v_and_b32 v55,0xffff0000,v23\n"
"v_add_f32 v56,v24,v25\n"
"v_mfma_f32_16x16x32_bf16 a[136:139],v[232:235],v[208:211],a[136:139]\n"
"v_add_f32 v57,v26,v27\n"
"v_add_f32 v56,v56,v28\n"
"v_mfma_f32_16x16x32_bf16 a[140:143],v[236:239],v[208:211],a[140:143]\n"
"buffer_load_dwordx4 v109,s[16:19],s76 offen offset:3072 lds\n"
"v_mfma_f32_16x16x32_bf16 a[140:143],v[172:175],v[144:147],a[140:143]\n"
"v_add_f32 v57,v57,v29\n"
"v_add_f32 v56,v56,v30\n"
"v_mfma_f32_16x16x32_bf16 a[144:147],v[176:179],v[144:147],a[144:147]\n"
"v_add_f32 v57,v57,v31\n"
"v_add_f32 v56,v56,v32\n"
"v_mfma_f32_16x16x32_bf16 a[144:147],v[240:243],v[208:211],a[144:147]\n"
"v_readfirstlane_b32 s82,v78\n"
"v_readfirstlane_b32 s83,v79\n"
"v_mfma_f32_16x16x32_bf16 a[148:151],v[244:247],v[208:211],a[148:151]\n"
"s_mov_b32 s76,262656\n"
"v_add_f32 v57,v57,v33\n"
"s_add_u32 m0,s79,36864\n"
"v_mfma_f32_16x16x32_bf16 a[148:151],v[180:183],v[144:147],a[148:151]\n"
"buffer_load_dwordx4 v106,s[16:19],s76 offen offset:0 lds\n"
"s_mul_i32 s80,s82,0x9e3779b1\n"
"v_mfma_f32_16x16x32_bf16 a[152:155],v[184:187],v[144:147],a[152:155]\n"
"s_add_u32 s80,s80,0x7f4a7c15\n"
"v_add_f32 v56,v56,v34\n"
"v_add_f32 v57,v57,v35\n"
"v_mfma_f32_16x16x32_bf16 a[152:155],v[248:251],v[208:211],a[152:155]\n"
"s_add_u32 s81,s83,s80\n"
"s_add_u32 s81,s81,1\n"
"v_mfma_f32_16x16x32_bf16 a[156:159],v[252:255],v[208:211],a[156:159]\n"
"v_mov_b32 v124,s80\n"
"v_mov_b32 v125,s81\n"
"v_mfma_f32_16x16x32_bf16 a[156:159],v[188:191],v[144:147],a[156:159]\n"
"v_add_f32 v56,v56,v36\n"
"v_add_f32 v57,v57,v37\n"
"v_mfma_f32_16x16x32_bf16 a[160:163],v[160:163],v[148:151],a[160:163]\n"
"buffer_load_dwordx4 v107,s[16:19],s76 offen offset:1024 lds\n"
"v_mfma_f32_16x16x32_bf16 a[160:163],v[224:227],v[212:215],a[160:163]\n"
"v_add_f32 v56,v56,v38\n"
"v_add_f32 v57,v57,v39\n"
"v_mfma_f32_16x16x32_bf16 a[164:167],v[228:231],v[212:215],a[164:167]\n"
"v_add_f32 v56,v56,v40\n"
"v_add_f32 v57,v57,v41\n"
"v_mfma_f32_16x16x32_bf16 a[164:167],v[164:167],v[148:151],a[164:167]\n"
"v_add_f32 v56,v56,v42\n"
"v_add_f32 v57,v57,v43\n"
"v_mfma_f32_16x16x32_bf16 a[168:171],v[168:171],v[148:151],a[168:171]\n"
"v_add_f32 v56,v56,v44\n"
"v_add_f32 v57,v57,v45\n"
"v_mfma_f32_16x16x32_bf16 a[168:171],v[232:235],v[212:215],a[168:171]\n"
"buffer_load_dwordx4 v108,s[16:19],s76 offen offset:2048 lds\n"
"v_mfma_f32_16x16x32_bf16 a[172:175],v[236:239],v[212:215],a[172:175]\n"
"v_add_f32 v56,v56,v46\n"
"v_add_f32 v57,v57,v47\n"
"v_mfma_f32_16x16x32_bf16 a[172:175],v[172:175],v[148:151],a[172:175]\n"
"v_add_f32 v56,v56,v48\n"
"v_add_f32 v57,v57,v49\n"
"v_mfma_f32_16x16x32_bf16 a[176:179],v[176:179],v[148:151],a[176:179]\n"
"v_add_f32 v56,v56,v50\n"
"v_add_f32 v57,v57,v51\n"
"v_mfma_f32_16x16x32_bf16 a[176:179],v[240:243],v[212:215],a[176:179]\n"
"v_add_f32 v56,v56,v52\n"
"v_add_f32 v57,v57,v53\n"
"v_mfma_f32_16x16x32_bf16 a[180:183],v[244:247],v[212:215],a[180:183]\n"
"buffer_load_dwordx4 v109,s[16:19],s76 offen offset:3072 lds\n"
"v_mfma_f32_16x16x32_bf16 a[180:183],v[180:183],v[148:151],a[180:183]\n"
"s_mov_b32 s87,2097152\n"
"v_add_f32 v56,v56,v54\n"
"v_add_f32 v57,v57,v55\n"
"v_mfma_f32_16x16x32_bf16 a[184:187],v[184:187],v[148:151],a[184:187]\n"
"buffer_load_dword v115,v112,s[36:39],s87 offen\n"
"v_mfma_f32_16x16x32_bf16 a[184:187],v[248:251],v[212:215],a[184:187]\n"
"s_mov_b32 s87,2621440\n"
"v_add_f32 v56,v56,v57\n"
"v_mfma_f32_16x16x32_bf16 a[188:191],v[252:255],v[212:215],a[188:191]\n"
"buffer_load_dword v115,v112,s[36:39],s87 offen\n"
"v_add_f32_dpp v56,v56,v56 quad_perm:[1,0,3,2] row_mask:0xf bank_mask:0xf\n"
"v_mfma_f32_16x16x32_bf16 a[188:191],v[188:191],v[148:151],a[188:191]\n"
"s_barrier\n"
"v_mfma_f32_16x16x32_bf16 a[192:195],v[224:227],v[216:219],a[192:195]\n"
"ds_read_b128 v[152:155],v82 offset:12288\n"
"v_mfma_f32_16x16x32_bf16 a[224:227],v[224:227],v[220:223],a[224:227]\n"
"ds_read_b128 v[156:159],v82 offset:14336\n"
"v_add_f32_dpp v56,v56,v56 quad_perm:[2,3,0,1] row_mask:0xf bank_mask:0xf\n"
"v_mfma_f32_16x16x32_bf16 a[228:231],v[228:231],v[220:223],a[228:231]\n"
"ds_read_b64_tr_b16 v[160:161],v98 offset:49152\n"
"v_mfma_f32_16x16x32_bf16 a[196:199],v[228:231],v[216:219],a[196:199]\n"
"ds_read_b64_tr_b16 v[162:163],v98 offset:50176\n"
"v_add_f32_dpp v56,v56,v56 row_ror:8 row_mask:0xf bank_mask:0xf\n"
"v_mfma_f32_16x16x32_bf16 a[200:203],v[232:235],v[216:219],a[200:203]\n"
"ds_read_b64_tr_b16 v[164:165],v99 offset:49152\n"
"v_mov_b32 v57,v56\n"
"v_mfma_f32_16x16x32_bf16 a[232:235],v[232:235],v[220:223],a[232:235]\n"
"ds_read_b64_tr_b16 v[166:167],v99 offset:50176\n"
"v_mfma_f32_16x16x32_bf16 a[236:239],v[236:239],v[220:223],a[236:239]\n"
"ds_read_b64_tr_b16 v[168:169],v100 offset:49152\n"
"v_permlane16_swap_b32 v57,v56\n"
"v_mfma_f32_16x16x32_bf16 a[204:207],v[236:239],v[216:219],a[204:207]\n"
"ds_read_b64_tr_b16 v[170:171],v100 offset:50176\n"
"v_add_f32 v56,v56,v57\n"
"v_mfma_f32_16x16x32_bf16 a[208:211],v[240:243],v[216:219],a[208:211]\n"
"ds_read_b64_tr_b16 v[172:173],v101 offset:49152\n"
"v_mov_b32 v57,v56\n"
"v_mfma_f32_16x16x32_bf16 a[240:243],v[240:243],v[220:223],a[240:243]\n"
"ds_read_b64_tr_b16 v[174:175],v101 offset:50176\n"
"v_mfma_f32_16x16x32_bf16 a[244:247],v[244:247],v[220:223],a[244:247]\n"
"ds_read_b64_tr_b16 v[176:177],v102 offset:49152\n"
"v_permlane32_swap_b32 v57,v56\n"
"v_mfma_f32_16x16x32_bf16 a[212:215],v[244:247],v[216:219],a[212:215]\n"
"ds_read_b64_tr_b16 v[178:179],v102 offset:50176\n"
"v_add_f32 v56,v56,v57\n"
"v_mfma_f32_16x16x32_bf16 a[216:219],v[248:251],v[216:219],a[216:219]\n"
"ds_read_b64_tr_b16 v[180:181],v103 offset:49152\n"
"v_mul_f32 v58,0x3a800000,v56\n"
"v_mfma_f32_16x16x32_bf16 a[248:251],v[248:251],v[220:223],a[248:251]\n"
"ds_read_b64_tr_b16 v[182:183],v103 offset:50176\n"
"v_sub_f32 v62,v24,v58\n"
"v_mfma_f32_16x16x32_bf16 a[252:255],v[252:255],v[220:223],a[252:255]\n"
"ds_read_b64_tr_b16 v[184:185],v104 offset:49152\n"
"v_mul_f32 v59,v62,v62\n"
"v_mfma_f32_16x16x32_bf16 a[220:223],v[252:255],v[216:219],a[220:223]\n"
"ds_read_b64_tr_b16 v[186:187],v104 offset:50176\n"
"v_sub_f32 v63,v25,v58\n"
"s_waitcnt lgkmcnt(10)\n"
"v_mfma_f32_16x16x32_bf16 a[192:195],v[160:163],v[152:155],a[192:195]\n"
"ds_read_b64_tr_b16 v[188:189],v105 offset:49152\n"
"v_mul_f32 v60,v63,v63\n"
"v_mfma_f32_16x16x32_bf16 a[224:227],v[160:163],v[156:159],a[224:227]\n"
"ds_read_b64_tr_b16 v[190:191],v105 offset:50176\n"
"v_sub_f32 v64,v26,v58\n"
"v_mfma_f32_16x16x32_bf16 a[228:231],v[164:167],v[156:159],a[228:231]\n"
"ds_read_b128 v[128:131],v82 offset:0\n"
"v_fmac_f32 v59,v64,v64\n"
"v_mfma_f32_16x16x32_bf16 a[196:199],v[164:167],v[152:155],a[196:199]\n"
"ds_read_b128 v[192:195],v83 offset:0\n"
"s_waitcnt lgkmcnt(10)\n"
"v_mfma_f32_16x16x32_bf16 a[200:203],v[168:171],v[152:155],a[200:203]\n"
"ds_read_b64_tr_b16 v[224:225],v98 offset:32768\n"
"v_sub_f32 v65,v27,v58\n"
"v_mfma_f32_16x16x32_bf16 a[232:235],v[168:171],v[156:159],a[232:235]\n"
"ds_read_b64_tr_b16 v[226:227],v98 offset:33792\n"
"v_fmac_f32 v60,v65,v65\n"
"v_mfma_f32_16x16x32_bf16 a[236:239],v[172:175],v[156:159],a[236:239]\n"
"ds_read_b64_tr_b16 v[228:229],v99 offset:32768\n"
"v_sub_f32 v62,v28,v58\n"
"v_mfma_f32_16x16x32_bf16 a[204:207],v[172:175],v[152:155],a[204:207]\n"
"ds_read_b64_tr_b16 v[230:231],v99 offset:33792\n"
"s_waitcnt lgkmcnt(10)\n"
"v_mfma_f32_16x16x32_bf16 a[208:211],v[176:179],v[152:155],a[208:211]\n"
"ds_read_b64_tr_b16 v[232:233],v100 offset:32768\n"
"v_fmac_f32 v59,v62,v62\n"
"v_mfma_f32_16x16x32_bf16 a[240:243],v[176:179],v[156:159],a[240:243]\n"
"ds_read_b64_tr_b16 v[234:235],v100 offset:33792\n"
"v_sub_f32 v63,v29,v58\n"
"v_mfma_f32_16x16x32_bf16 a[244:247],v[180:183],v[156:159],a[244:247]\n"
"ds_read_b64_tr_b16 v[236:237],v101 offset:32768\n"
"v_fmac_f32 v60,v63,v63\n"
"v_mfma_f32_16x16x32_bf16 a[212:215],v[180:183],v[152:155],a[212:215]\n"
"ds_read_b64_tr_b16 v[238:239],v101 offset:33792\n"
"s_waitcnt lgkmcnt(10)\n"
"v_mfma_f32_16x16x32_bf16 a[216:219],v[184:187],v[152:155],a[216:219]\n"
"ds_read_b64_tr_b16 v[240:241],v102 offset:32768\n"
"v_sub_f32 v64,v30,v58\n"
"v_mfma_f32_16x16x32_bf16 a[248:251],v[184:187],v[156:159],a[248:251]\n"
"ds_read_b64_tr_b16 v[242:243],v102 offset:33792\n"
"v_fmac_f32 v59,v64,v64\n"
"v_mfma_f32_16x16x32_bf16 a[252:255],v[188:191],v[156:159],a[252:255]\n"
"ds_read_b64_tr_b16 v[244:245],v103 offset:32768\n"
"v_sub_f32 v65,v31,v58\n"
"v_mfma_f32_16x16x32_bf16 a[220:223],v[188:191],v[152:155],a[220:223]\n"
"ds_read_b64_tr_b16 v[246:247],v103 offset:33792\n"
"s_waitcnt lgkmcnt(8)\n"
"v_mfma_f32_16x16x32_bf16 a[0:3],v[160:163],v[128:131],a[0:3]\n"
"ds_read_b64_tr_b16 v[248:249],v104 offset:32768\n"
"v_fmac_f32 v60,v65,v65\n"
"v_mfma_f32_16x16x32_bf16 a[0:3],v[224:227],v[192:195],a[0:3]\n"
"ds_read_b64_tr_b16 v[250:251],v104 offset:33792\n"
"v_sub_f32 v62,v32,v58\n"
"v_mfma_f32_16x16x32_bf16 a[4:7],v[228:231],v[192:195],a[4:7]\n"
"ds_read_b64_tr_b16 v[252:253],v105 offset:32768\n"
"v_fmac_f32 v59,v62,v62\n"
"v_mfma_f32_16x16x32_bf16 a[4:7],v[164:167],v[128:131],a[4:7]\n"
"ds_read_b64_tr_b16 v[254:255],v105 offset:33792\n"
"s_waitcnt lgkmcnt(8)\n"
"v_mfma_f32_16x16x32_bf16 a[8:11],v[168:171],v[128:131],a[8:11]\n"
"ds_read_b128 v[132:135],v82 offset:2048\n"
"v_sub_f32 v63,v33,v58\n"
"v_mfma_f32_16x16x32_bf16 a[8:11],v[232:235],v[192:195],a[8:11]\n"
"ds_read_b128 v[196:199],v83 offset:2048\n"
"v_fmac_f32 v60,v63,v63\n"
"v_mfma_f32_16x16x32_bf16 a[12:15],v[236:239],v[192:195],a[12:15]\n"
"ds_read_b128 v[136:139],v82 offset:4096\n"
"v_sub_f32 v64,v34,v58\n"
"v_mfma_f32_16x16x32_bf16 a[12:15],v[172:175],v[128:131],a[12:15]\n"
"ds_read_b128 v[200:203],v83 offset:4096\n"
"s_waitcnt lgkmcnt(8)\n"
"v_mfma_f32_16x16x32_bf16 a[16:19],v[176:179],v[128:131],a[16:19]\n"
"ds_read_b128 v[140:143],v82 offset:6144\n"
"v_fmac_f32 v59,v64,v64\n"
"v_mfma_f32_16x16x32_bf16 a[16:19],v[240:243],v[192:195],a[16:19]\n"
"ds_read_b128 v[204:207],v83 offset:6144\n"
"v_sub_f32 v65,v35,v58\n"
"v_mfma_f32_16x16x32_bf16 a[20:23],v[244:247],v[192:195],a[20:23]\n"
"ds_read_b128 v[144:147],v82 offset:8192\n"
"v_fmac_f32 v60,v65,v65\n"
"v_mfma_f32_16x16x32_bf16 a[20:23],v[180:183],v[128:131],a[20:23]\n"
"ds_read_b128 v[208:211],v83 offset:8192\n"
"s_waitcnt lgkmcnt(8)\n"
"v_mfma_f32_16x16x32_bf16 a[24:27],v[184:187],v[128:131],a[24:27]\n"
"ds_read_b128 v[148:151],v82 offset:10240\n"
"v_sub_f32 v62,v36,v58\n"
"v_mfma_f32_16x16x32_bf16 a[24:27],v[248:251],v[192:195],a[24:27]\n"
"ds_read_b128 v[212:215],v83 offset:10240\n"
"v_fmac_f32 v59,v62,v62\n"
"v_mfma_f32_16x16x32_bf16 a[28:31],v[252:255],v[192:195],a[28:31]\n"
"ds_read_b128 v[216:219],v83 offset:12288\n"
"v_sub_f32 v63,v37,v58\n"
"v_mfma_f32_16x16x32_bf16 a[28:31],v[188:191],v[128:131],a[28:31]\n"
"ds_read_b128 v[220:223],v83 offset:14336\n"
"s_waitcnt lgkmcnt(10)\n"
"v_mfma_f32_16x16x32_bf16 a[32:35],v[160:163],v[132:135],a[32:35]\n"
"v_fmac_f32 v60,v63,v63\n"
"v_sub_f32 v64,v38,v58\n"
"v_mfma_f32_16x16x32_bf16 a[32:35],v[224:227],v[196:199],a[32:35]\n"
"v_fmac_f32 v59,v64,v64\n"
"v_sub_f32 v65,v39,v58\n"
"v_mfma_f32_16x16x32_bf16 a[36:39],v[228:231],v[196:199],a[36:39]\n"
"v_fmac_f32 v60,v65,v65\n"
"v_sub_f32 v62,v40,v58\n"
"v_mfma_f32_16x16x32_bf16 a[36:39],v[164:167],v[132:135],a[36:39]\n"
"v_fmac_f32 v59,v62,v62\n"
"v_sub_f32 v63,v41,v58\n"
"v_mfma_f32_16x16x32_bf16 a[40:43],v[168:171],v[132:135],a[40:43]\n"
"s_waitcnt lgkmcnt(0)\n"
"s_barrier\n"
"v_mfma_f32_16x16x32_bf16 a[40:43],v[232:235],v[196:199],a[40:43]\n"
"s_mov_b32 s77,2097152\n"
"v_fmac_f32 v60,v63,v63\n"
"s_add_u32 m0,s78,147456\n"
"v_mfma_f32_16x16x32_bf16 a[44:47],v[236:239],v[196:199],a[44:47]\n"
"buffer_load_dwordx4 v110,s[20:23],s77 offen offset:0 lds\n"
"v_mfma_f32_16x16x32_bf16 a[44:47],v[172:175],v[132:135],a[44:47]\n"
"v_sub_f32 v64,v42,v58\n"
"v_fmac_f32 v59,v64,v64\n"
"v_mfma_f32_16x16x32_bf16 a[48:51],v[176:179],v[132:135],a[48:51]\n"
"v_sub_f32 v65,v43,v58\n"
"v_fmac_f32 v60,v65,v65\n"
"v_mfma_f32_16x16x32_bf16 a[48:51],v[240:243],v[196:199],a[48:51]\n"
"v_sub_f32 v62,v44,v58\n"
"v_fmac_f32 v59,v62,v62\n"
"v_mfma_f32_16x16x32_bf16 a[52:55],v[244:247],v[196:199],a[52:55]\n"
"buffer_load_dwordx4 v111,s[20:23],s77 offen offset:1024 lds\n"
"v_mfma_f32_16x16x32_bf16 a[52:55],v[180:183],v[132:135],a[52:55]\n"
"v_sub_f32 v63,v45,v58\n"
"v_fmac_f32 v60,v63,v63\n"
"v_mfma_f32_16x16x32_bf16 a[56:59],v[184:187],v[132:135],a[56:59]\n"
"v_sub_f32 v64,v46,v58\n"
"v_fmac_f32 v59,v64,v64\n"
"v_mfma_f32_16x16x32_bf16 a[56:59],v[248:251],v[196:199],a[56:59]\n"
"v_sub_f32 v65,v47,v58\n"
"v_fmac_f32 v60,v65,v65\n"
"v_mfma_f32_16x16x32_bf16 a[60:63],v[252:255],v[196:199],a[60:63]\n"
"buffer_load_dwordx4 v113,s[20:23],s77 offen offset:2048 lds\n"
"v_mfma_f32_16x16x32_bf16 a[60:63],v[188:191],v[132:135],a[60:63]\n"
"v_sub_f32 v62,v48,v58\n"
"v_fmac_f32 v59,v62,v62\n"
"v_mfma_f32_16x16x32_bf16 a[64:67],v[160:163],v[136:139],a[64:67]\n"
"v_sub_f32 v63,v49,v58\n"
"v_fmac_f32 v60,v63,v63\n"
"v_mfma_f32_16x16x32_bf16 a[64:67],v[224:227],v[200:203],a[64:67]\n"
"v_sub_f32 v64,v50,v58\n"
"v_fmac_f32 v59,v64,v64\n"
"v_mfma_f32_16x16x32_bf16 a[68:71],v[228:231],v[200:203],a[68:71]\n"
"buffer_load_dwordx4 v114,s[20:23],s77 offen offset:3072 lds\n"
"v_mfma_f32_16x16x32_bf16 a[68:71],v[164:167],v[136:139],a[68:71]\n"
"v_sub_f32 v65,v51,v58\n"
"v_fmac_f32 v60,v65,v65\n"
"v_mfma_f32_16x16x32_bf16 a[72:75],v[168:171],v[136:139],a[72:75]\n"
"v_sub_f32 v62,v52,v58\n"
"v_fmac_f32 v59,v62,v62\n"
"v_mfma_f32_16x16x32_bf16 a[72:75],v[232:235],v[200:203],a[72:75]\n"
"s_mov_b32 s77,2359296\n"
"v_sub_f32 v63,v53,v58\n"
"s_add_u32 m0,s78,131072\n"
"v_mfma_f32_16x16x32_bf16 a[76:79],v[236:239],v[200:203],a[76:79]\n"
"buffer_load_dwordx4 v110,s[20:23],s77 offen offset:0 lds\n"
"v_mfma_f32_16x16x32_bf16 a[76:79],v[172:175],v[136:139],a[76:79]\n"
"v_fmac_f32 v60,v63,v63\n"
"v_sub_f32 v64,v54,v58\n"
"v_mfma_f32_16x16x32_bf16 a[80:83],v[176:179],v[136:139],a[80:83]\n"
"v_fmac_f32 v59,v64,v64\n"
"v_sub_f32 v65,v55,v58\n"
"v_mfma_f32_16x16x32_bf16 a[80:83],v[240:243],v[200:203],a[80:83]\n"
"v_fmac_f32 v60,v65,v65\n"
"v_add_f32 v59,v59,v60\n"
"v_mfma_f32_16x16x32_bf16 a[84:87],v[244:247],v[200:203],a[84:87]\n"
"buffer_load_dwordx4 v111,s[20:23],s77 offen offset:1024 lds\n"
"v_mfma_f32_16x16x32_bf16 a[84:87],v[180:183],v[136:139],a[84:87]\n"
"v_mfma_f32_16x16x32_bf16 a[88:91],v[184:187],v[136:139],a[88:91]\n"
"v_add_f32_dpp v59,v59,v59 quad_perm:[1,0,3,2] row_mask:0xf bank_mask:0xf\n"
"v_mfma_f32_16x16x32_bf16 a[88:91],v[248:251],v[200:203],a[88:91]\n"
"s_nop 0\n"
"v_add_f32_dpp v59,v59,v59 quad_perm:[2,3,0,1] row_mask:0xf bank_mask:0xf\n"
"v_mfma_f32_16x16x32_bf16 a[92:95],v[252:255],v[200:203],a[92:95]\n"
"buffer_load_dwordx4 v113,s[20:23],s77 offen offset:2048 lds\n"
"v_mfma_f32_16x16x32_bf16 a[92:95],v[188:191],v[136:139],a[92:95]\n"
"v_add_f32_dpp v59,v59,v59 row_ror:8 row_mask:0xf bank_mask:0xf\n"
"v_mov_b32 v57,v59\n"
"v_mfma_f32_16x16x32_bf16 a[96:99],v[160:163],v[140:143],a[96:99]\n"
"v_mfma_f32_16x16x32_bf16 a[96:99],v[224:227],v[204:207],a[96:99]\n"
"v_permlane16_swap_b32 v57,v59\n"
"v_add_f32 v59,v59,v57\n"
"v_mfma_f32_16x16x32_bf16 a[100:103],v[228:231],v[204:207],a[100:103]\n"
"buffer_load_dwordx4 v114,s[20:23],s77 offen offset:3072 lds\n"
"v_mfma_f32_16x16x32_bf16 a[100:103],v[164:167],v[140:143],a[100:103]\n"
"v_mov_b32 v57,v59\n"
"v_mfma_f32_16x16x32_bf16 a[104:107],v[168:171],v[140:143],a[104:107]\n"
"s_nop 0\n"
"v_permlane32_swap_b32 v57,v59\n"
"v_add_f32 v59,v59,v57\n"
"v_mfma_f32_16x16x32_bf16 a[104:107],v[232:235],v[204:207],a[104:107]\n"
"v_mul_f32 v59,0x3a800000,v59\n"
"v_cvt_pk_bf16_f32 v59,v59,v59\n"
"v_mfma_f32_16x16x32_bf16 a[108:111],v[236:239],v[204:207],a[108:111]\n"
"s_mov_b32 s76,640\n"
"v_lshlrev_b32 v59,16,v59\n"
"s_add_u32 m0,s79,65536\n"
"v_mfma_f32_16x16x32_bf16 a[108:111],v[172:175],v[140:143],a[108:111]\n"
"buffer_load_dwordx4 v106,s[16:19],s76 offen offset:0 lds\n"
"v_mfma_f32_16x16x32_bf16 a[112:115],v[176:179],v[140:143],a[112:115]\n"
"v_add_f32 v59,0x3727c5ac,v59\n"
"v_cvt_pk_bf16_f32 v59,v59,v59\n"
"v_mfma_f32_16x16x32_bf16 a[112:115],v[240:243],v[204:207],a[112:115]\n"
"v_lshlrev_b32 v59,16,v59\n"
"v_sqrt_f32 v59,v59\n"
"v_mfma_f32_16x16x32_bf16 a[116:119],v[244:247],v[204:207],a[116:119]\n"
"v_cvt_pk_bf16_f32 v58,v58,v58\n"
"v_lshlrev_b32 v58,16,v58\n"
"v_mfma_f32_16x16x32_bf16 a[116:119],v[180:183],v[140:143],a[116:119]\n"
"v_cvt_pk_bf16_f32 v59,v59,v59\n"
"v_lshlrev_b32 v59,16,v59\n"
"v_mfma_f32_16x16x32_bf16 a[120:123],v[184:187],v[140:143],a[120:123]\n"
"buffer_load_dwordx4 v107,s[16:19],s76 offen offset:1024 lds\n"
"v_mfma_f32_16x16x32_bf16 a[120:123],v[248:251],v[204:207],a[120:123]\n"
"v_rcp_f32 v66,v59\n"
"v_mfma_f32_16x16x32_bf16 a[124:127],v[252:255],v[204:207],a[124:127]\n"
"v_mul_f32 v66,0x3e0293ee,v66\n"
"v_mul_f32 v58,v58,v66\n"
"v_mfma_f32_16x16x32_bf16 a[124:127],v[188:191],v[140:143],a[124:127]\n"
"v_fma_f32 v24,v24,v66,-v58\n"
"v_fma_f32 v25,v25,v66,-v58\n"
"v_mfma_f32_16x16x32_bf16 a[128:131],v[160:163],v[144:147],a[128:131]\n"
"v_cvt_pk_bf16_f32 v8,v24,v25\n"
"v_fma_f32 v26,v26,v66,-v58\n"
"v_mfma_f32_16x16x32_bf16 a[128:131],v[224:227],v[208:211],a[128:131]\n"
"buffer_load_dwordx4 v108,s[16:19],s76 offen offset:2048 lds\n"
"v_mfma_f32_16x16x32_bf16 a[132:135],v[228:231],v[208:211],a[132:135]\n"
"v_fma_f32 v27,v27,v66,-v58\n"
"v_cvt_pk_bf16_f32 v9,v26,v27\n"
"v_mfma_f32_16x16x32_bf16 a[132:135],v[164:167],v[144:147],a[132:135]\n"
"v_fma_f32 v28,v28,v66,-v58\n"
"v_fma_f32 v29,v29,v66,-v58\n"
"v_mfma_f32_16x16x32_bf16 a[136:139],v[168:171],v[144:147],a[136:139]\n"
"v_cvt_pk_bf16_f32 v10,v28,v29\n"
"v_fma_f32 v30,v30,v66,-v58\n"
"v_mfma_f32_16x16x32_bf16 a[136:139],v[232:235],v[208:211],a[136:139]\n"
"v_fma_f32 v31,v31,v66,-v58\n"
"v_cvt_pk_bf16_f32 v11,v30,v31\n"
"v_mfma_f32_16x16x32_bf16 a[140:143],v[236:239],v[208:211],a[140:143]\n"
"buffer_load_dwordx4 v109,s[16:19],s76 offen offset:3072 lds\n"
"v_mfma_f32_16x16x32_bf16 a[140:143],v[172:175],v[144:147],a[140:143]\n"
"buffer_store_dwordx4 v[8:11],v71,s[24:27],s54 offen sc1\n"
"v_mfma_f32_16x16x32_bf16 a[144:147],v[176:179],v[144:147],a[144:147]\n"
"s_add_u32 s54,s54,131072\n"
"v_fma_f32 v32,v32,v66,-v58\n"
"v_fma_f32 v33,v33,v66,-v58\n"
"v_mfma_f32_16x16x32_bf16 a[144:147],v[240:243],v[208:211],a[144:147]\n"
"v_cvt_pk_bf16_f32 v12,v32,v33\n"
"v_fma_f32 v34,v34,v66,-v58\n"
"v_mfma_f32_16x16x32_bf16 a[148:151],v[244:247],v[208:211],a[148:151]\n"
"s_mov_b32 s76,262784\n"
"v_fma_f32 v35,v35,v66,-v58\n"
"s_add_u32 m0,s79,69632\n"
"v_mfma_f32_16x16x32_bf16 a[148:151],v[180:183],v[144:147],a[148:151]\n"
"buffer_load_dwordx4 v106,s[16:19],s76 offen offset:0 lds\n"
"v_mfma_f32_16x16x32_bf16 a[152:155],v[184:187],v[144:147],a[152:155]\n"
"v_cvt_pk_bf16_f32 v13,v34,v35\n"
"v_fma_f32 v36,v36,v66,-v58\n"
"v_mfma_f32_16x16x32_bf16 a[152:155],v[248:251],v[208:211],a[152:155]\n"
"v_fma_f32 v37,v37,v66,-v58\n"
"v_cvt_pk_bf16_f32 v14,v36,v37\n"
"v_mfma_f32_16x16x32_bf16 a[156:159],v[252:255],v[208:211],a[156:159]\n"
"v_fma_f32 v38,v38,v66,-v58\n"
"v_fma_f32 v39,v39,v66,-v58\n"
"v_cvt_pk_bf16_f32 v15,v38,v39\n"
"v_mfma_f32_16x16x32_bf16 a[156:159],v[188:191],v[144:147],a[156:159]\n"
"buffer_store_dwordx4 v[12:15],v71,s[24:27],s54 offen sc1\n"
"v_mfma_f32_16x16x32_bf16 a[160:163],v[160:163],v[148:151],a[160:163]\n"
"buffer_load_dwordx4 v107,s[16:19],s76 offen offset:1024 lds\n"
"v_mfma_f32_16x16x32_bf16 a[160:163],v[224:227],v[212:215],a[160:163]\n"
"s_add_u32 s54,s54,131072\n"
"v_fma_f32 v40,v40,v66,-v58\n"
"v_mfma_f32_16x16x32_bf16 a[164:167],v[228:231],v[212:215],a[164:167]\n"
"v_fma_f32 v41,v41,v66,-v58\n"
"v_cvt_pk_bf16_f32 v16,v40,v41\n"
"v_mfma_f32_16x16x32_bf16 a[164:167],v[164:167],v[148:151],a[164:167]\n"
"v_fma_f32 v42,v42,v66,-v58\n"
"v_fma_f32 v43,v43,v66,-v58\n"
"v_mfma_f32_16x16x32_bf16 a[168:171],v[168:171],v[148:151],a[168:171]\n"
"v_cvt_pk_bf16_f32 v17,v42,v43\n"
"v_fma_f32 v44,v44,v66,-v58\n"
"v_mfma_f32_16x16x32_bf16 a[168:171],v[232:235],v[212:215],a[168:171]\n"
"buffer_load_dwordx4 v108,s[16:19],s76 offen offset:2048 lds\n"
"v_mfma_f32_16x16x32_bf16 a[172:175],v[236:239],v[212:215],a[172:175]\n"
"v_fma_f32 v45,v45,v66,-v58\n"
"v_cvt_pk_bf16_f32 v18,v44,v45\n"
"v_mfma_f32_16x16x32_bf16 a[172:175],v[172:175],v[148:151],a[172:175]\n"
"v_fma_f32 v46,v46,v66,-v58\n"
"v_fma_f32 v47,v47,v66,-v58\n"
"v_cvt_pk_bf16_f32 v19,v46,v47\n"
"v_mfma_f32_16x16x32_bf16 a[176:179],v[176:179],v[148:151],a[176:179]\n"
"buffer_store_dwordx4 v[16:19],v71,s[24:27],s54 offen sc1\n"
"v_mfma_f32_16x16x32_bf16 a[176:179],v[240:243],v[212:215],a[176:179]\n"
"s_add_u32 s54,s54,131072\n"
"v_fma_f32 v48,v48,v66,-v58\n"
"v_mfma_f32_16x16x32_bf16 a[180:183],v[244:247],v[212:215],a[180:183]\n"
"buffer_load_dwordx4 v109,s[16:19],s76 offen offset:3072 lds\n"
"v_mfma_f32_16x16x32_bf16 a[180:183],v[180:183],v[148:151],a[180:183]\n"
"s_mov_b32 s87,0\n"
"v_fma_f32 v49,v49,v66,-v58\n"
"v_cvt_pk_bf16_f32 v20,v48,v49\n"
"v_mfma_f32_16x16x32_bf16 a[184:187],v[184:187],v[148:151],a[184:187]\n"
"buffer_load_dword v115,v112,s[40:43],s87 offen\n"
"v_mfma_f32_16x16x32_bf16 a[184:187],v[248:251],v[212:215],a[184:187]\n"
"s_mov_b32 s87,1048576\n"
"v_fma_f32 v50,v50,v66,-v58\n"
"v_fma_f32 v51,v51,v66,-v58\n"
"v_mfma_f32_16x16x32_bf16 a[188:191],v[252:255],v[212:215],a[188:191]\n"
"buffer_load_dword v115,v112,s[40:43],s87 offen\n"
"v_mfma_f32_16x16x32_bf16 a[188:191],v[188:191],v[148:151],a[188:191]\n"
"s_mov_b32 s87,2097152\n"
"s_waitcnt vmcnt(31)\n"
"s_barrier\n"
"v_mfma_f32_16x16x32_bf16 a[192:195],v[224:227],v[216:219],a[192:195]\n"
"ds_read_b128 v[152:155],v80 offset:12288\n"
"buffer_load_dword v115,v112,s[40:43],s87 offen\n"
"v_mfma_f32_16x16x32_bf16 a[224:227],v[224:227],v[220:223],a[224:227]\n"
"ds_read_b128 v[156:159],v80 offset:14336\n"
"v_cvt_pk_bf16_f32 v21,v50,v51\n"
"v_mfma_f32_16x16x32_bf16 a[228:231],v[228:231],v[220:223],a[228:231]\n"
"ds_read_b64_tr_b16 v[160:161],v98 offset:0\n"
"v_fma_f32 v52,v52,v66,-v58\n"
"v_mfma_f32_16x16x32_bf16 a[196:199],v[228:231],v[216:219],a[196:199]\n"
"ds_read_b64_tr_b16 v[162:163],v98 offset:1024\n"
"v_fma_f32 v53,v53,v66,-v58\n"
"v_mfma_f32_16x16x32_bf16 a[200:203],v[232:235],v[216:219],a[200:203]\n"
"ds_read_b64_tr_b16 v[164:165],v99 offset:0\n"
"v_cvt_pk_bf16_f32 v22,v52,v53\n"
"v_mfma_f32_16x16x32_bf16 a[232:235],v[232:235],v[220:223],a[232:235]\n"
"ds_read_b64_tr_b16 v[166:167],v99 offset:1024\n"
"v_fma_f32 v54,v54,v66,-v58\n"
"v_mfma_f32_16x16x32_bf16 a[236:239],v[236:239],v[220:223],a[236:239]\n"
"ds_read_b64_tr_b16 v[168:169],v100 offset:0\n"
"v_fma_f32 v55,v55,v66,-v58\n"
"v_mfma_f32_16x16x32_bf16 a[204:207],v[236:239],v[216:219],a[204:207]\n"
"ds_read_b64_tr_b16 v[170:171],v100 offset:1024\n"
"v_cvt_pk_bf16_f32 v23,v54,v55\n"
"v_mfma_f32_16x16x32_bf16 a[208:211],v[240:243],v[216:219],a[208:211]\n"
"ds_read_b64_tr_b16 v[172:173],v101 offset:0\n"
"buffer_store_dwordx4 v[20:23],v71,s[24:27],s54 offen sc1\n"
"v_mfma_f32_16x16x32_bf16 a[240:243],v[240:243],v[220:223],a[240:243]\n"
"ds_read_b64_tr_b16 v[174:175],v101 offset:1024\n"
"v_mfma_f32_16x16x32_bf16 a[244:247],v[244:247],v[220:223],a[244:247]\n"
"ds_read_b64_tr_b16 v[176:177],v102 offset:0\n"
"v_mfma_f32_16x16x32_bf16 a[212:215],v[244:247],v[216:219],a[212:215]\n"
"ds_read_b64_tr_b16 v[178:179],v102 offset:1024\n"
"v_mfma_f32_16x16x32_bf16 a[216:219],v[248:251],v[216:219],a[216:219]\n"
"ds_read_b64_tr_b16 v[180:181],v103 offset:0\n"
"v_mfma_f32_16x16x32_bf16 a[248:251],v[248:251],v[220:223],a[248:251]\n"
"ds_read_b64_tr_b16 v[182:183],v103 offset:1024\n"
"v_mfma_f32_16x16x32_bf16 a[252:255],v[252:255],v[220:223],a[252:255]\n"
"ds_read_b64_tr_b16 v[184:185],v104 offset:0\n"
"v_mfma_f32_16x16x32_bf16 a[220:223],v[252:255],v[216:219],a[220:223]\n"
"ds_read_b64_tr_b16 v[186:187],v104 offset:1024\n"
"s_waitcnt lgkmcnt(10)\n"
"v_mfma_f32_16x16x32_bf16 a[192:195],v[160:163],v[152:155],a[192:195]\n"
"ds_read_b64_tr_b16 v[188:189],v105 offset:0\n"
"v_mfma_f32_16x16x32_bf16 a[224:227],v[160:163],v[156:159],a[224:227]\n"
"ds_read_b64_tr_b16 v[190:191],v105 offset:1024\n"
"v_mfma_f32_16x16x32_bf16 a[228:231],v[164:167],v[156:159],a[228:231]\n"
"ds_read_b128 v[128:131],v80 offset:0\n"
"v_mfma_f32_16x16x32_bf16 a[196:199],v[164:167],v[152:155],a[196:199]\n"
"ds_read_b128 v[192:195],v81 offset:0\n"
"s_waitcnt lgkmcnt(10)\n"
"v_mfma_f32_16x16x32_bf16 a[200:203],v[168:171],v[152:155],a[200:203]\n"
"ds_read_b64_tr_b16 v[224:225],v98 offset:16384\n"
"v_mfma_f32_16x16x32_bf16 a[232:235],v[168:171],v[156:159],a[232:235]\n"
"ds_read_b64_tr_b16 v[226:227],v98 offset:17408\n"
"v_mfma_f32_16x16x32_bf16 a[236:239],v[172:175],v[156:159],a[236:239]\n"
"ds_read_b64_tr_b16 v[228:229],v99 offset:16384\n"
"v_mfma_f32_16x16x32_bf16 a[204:207],v[172:175],v[152:155],a[204:207]\n"
"ds_read_b64_tr_b16 v[230:231],v99 offset:17408\n"
"s_waitcnt lgkmcnt(10)\n"
"v_mfma_f32_16x16x32_bf16 a[208:211],v[176:179],v[152:155],a[208:211]\n"
"ds_read_b64_tr_b16 v[232:233],v100 offset:16384\n"
"v_mfma_f32_16x16x32_bf16 a[240:243],v[176:179],v[156:159],a[240:243]\n"
"ds_read_b64_tr_b16 v[234:235],v100 offset:17408\n"
"v_mfma_f32_16x16x32_bf16 a[244:247],v[180:183],v[156:159],a[244:247]\n"
"ds_read_b64_tr_b16 v[236:237],v101 offset:16384\n"
"v_mfma_f32_16x16x32_bf16 a[212:215],v[180:183],v[152:155],a[212:215]\n"
"ds_read_b64_tr_b16 v[238:239],v101 offset:17408\n"
"s_waitcnt lgkmcnt(10)\n"
"v_mfma_f32_16x16x32_bf16 a[216:219],v[184:187],v[152:155],a[216:219]\n"
"ds_read_b64_tr_b16 v[240:241],v102 offset:16384\n"
"v_mfma_f32_16x16x32_bf16 a[248:251],v[184:187],v[156:159],a[248:251]\n"
"ds_read_b64_tr_b16 v[242:243],v102 offset:17408\n"
"v_mfma_f32_16x16x32_bf16 a[252:255],v[188:191],v[156:159],a[252:255]\n"
"ds_read_b64_tr_b16 v[244:245],v103 offset:16384\n"
"v_mfma_f32_16x16x32_bf16 a[220:223],v[188:191],v[152:155],a[220:223]\n"
"ds_read_b64_tr_b16 v[246:247],v103 offset:17408\n"
"s_waitcnt lgkmcnt(8)\n"
"v_mfma_f32_16x16x32_bf16 a[0:3],v[160:163],v[128:131],a[0:3]\n"
"ds_read_b64_tr_b16 v[248:249],v104 offset:16384\n"
"v_mfma_f32_16x16x32_bf16 a[0:3],v[224:227],v[192:195],a[0:3]\n"
"ds_read_b64_tr_b16 v[250:251],v104 offset:17408\n"
"v_mfma_f32_16x16x32_bf16 a[4:7],v[228:231],v[192:195],a[4:7]\n"
"ds_read_b64_tr_b16 v[252:253],v105 offset:16384\n"
"v_mfma_f32_16x16x32_bf16 a[4:7],v[164:167],v[128:131],a[4:7]\n"
"ds_read_b64_tr_b16 v[254:255],v105 offset:17408\n"
"s_waitcnt lgkmcnt(8)\n"
"v_mfma_f32_16x16x32_bf16 a[8:11],v[168:171],v[128:131],a[8:11]\n"
"ds_read_b128 v[132:135],v80 offset:2048\n"
"v_mfma_f32_16x16x32_bf16 a[8:11],v[232:235],v[192:195],a[8:11]\n"
"ds_read_b128 v[196:199],v81 offset:2048\n"
"v_mfma_f32_16x16x32_bf16 a[12:15],v[236:239],v[192:195],a[12:15]\n"
"ds_read_b128 v[136:139],v80 offset:4096\n"
"v_mfma_f32_16x16x32_bf16 a[12:15],v[172:175],v[128:131],a[12:15]\n"
"ds_read_b128 v[200:203],v81 offset:4096\n"
"s_waitcnt lgkmcnt(8)\n"
"v_mfma_f32_16x16x32_bf16 a[16:19],v[176:179],v[128:131],a[16:19]\n"
"ds_read_b128 v[140:143],v80 offset:6144\n"
"v_mfma_f32_16x16x32_bf16 a[16:19],v[240:243],v[192:195],a[16:19]\n"
"ds_read_b128 v[204:207],v81 offset:6144\n"
"v_mfma_f32_16x16x32_bf16 a[20:23],v[244:247],v[192:195],a[20:23]\n"
"ds_read_b128 v[144:147],v80 offset:8192\n"
"v_mfma_f32_16x16x32_bf16 a[20:23],v[180:183],v[128:131],a[20:23]\n"
"ds_read_b128 v[208:211],v81 offset:8192\n"
"s_waitcnt lgkmcnt(8)\n"
"v_mfma_f32_16x16x32_bf16 a[24:27],v[184:187],v[128:131],a[24:27]\n"
"ds_read_b128 v[148:151],v80 offset:10240\n"
"v_mfma_f32_16x16x32_bf16 a[24:27],v[248:251],v[192:195],a[24:27]\n"
"ds_read_b128 v[212:215],v81 offset:10240\n"
"v_mfma_f32_16x16x32_bf16 a[28:31],v[252:255],v[192:195],a[28:31]\n"
"ds_read_b128 v[216:219],v81 offset:12288\n"
"v_mfma_f32_16x16x32_bf16 a[28:31],v[188:191],v[128:131],a[28:31]\n"
"ds_read_b128 v[220:223],v81 offset:14336\n"
"s_waitcnt lgkmcnt(10)\n"
"v_mfma_f32_16x16x32_bf16 a[32:35],v[160:163],v[132:135],a[32:35]\n"
"v_mfma_f32_16x16x32_bf16 a[32:35],v[224:227],v[196:199],a[32:35]\n"
"v_mfma_f32_16x16x32_bf16 a[36:39],v[228:231],v[196:199],a[36:39]\n"
"v_mfma_f32_16x16x32_bf16 a[36:39],v[164:167],v[132:135],a[36:39]\n"
"v_mfma_f32_16x16x32_bf16 a[40:43],v[168:171],v[132:135],a[40:43]\n"
"s_waitcnt lgkmcnt(0)\n"
"s_barrier\n"
"v_mfma_f32_16x16x32_bf16 a[40:43],v[232:235],v[196:199],a[40:43]\n"
"s_mov_b32 s77,2621440\n"
"s_add_u32 m0,s78,98304\n"
"v_mfma_f32_16x16x32_bf16 a[44:47],v[236:239],v[196:199],a[44:47]\n"
"buffer_load_dwordx4 v110,s[20:23],s77 offen offset:0 lds\n"
"v_mfma_f32_16x16x32_bf16 a[44:47],v[172:175],v[132:135],a[44:47]\n"
"v_mfma_f32_16x16x32_bf16 a[48:51],v[176:179],v[132:135],a[48:51]\n"
"v_mfma_f32_16x16x32_bf16 a[48:51],v[240:243],v[196:199],a[48:51]\n"
"v_mfma_f32_16x16x32_bf16 a[52:55],v[244:247],v[196:199],a[52:55]\n"
"buffer_load_dwordx4 v111,s[20:23],s77 offen offset:1024 lds\n"
"v_mfma_f32_16x16x32_bf16 a[52:55],v[180:183],v[132:135],a[52:55]\n"
"v_mfma_f32_16x16x32_bf16 a[56:59],v[184:187],v[132:135],a[56:59]\n"
"v_mfma_f32_16x16x32_bf16 a[56:59],v[248:251],v[196:199],a[56:59]\n"
"v_mfma_f32_16x16x32_bf16 a[60:63],v[252:255],v[196:199],a[60:63]\n"
"buffer_load_dwordx4 v113,s[20:23],s77 offen offset:2048 lds\n"
"v_mfma_f32_16x16x32_bf16 a[60:63],v[188:191],v[132:135],a[60:63]\n"
"v_mfma_f32_16x16x32_bf16 a[64:67],v[160:163],v[136:139],a[64:67]\n"
"v_mfma_f32_16x16x32_bf16 a[64:67],v[224:227],v[200:203],a[64:67]\n"
"v_mfma_f32_16x16x32_bf16 a[68:71],v[228:231],v[200:203],a[68:71]\n"
"buffer_load_dwordx4 v114,s[20:23],s77 offen offset:3072 lds\n"
"v_mfma_f32_16x16x32_bf16 a[68:71],v[164:167],v[136:139],a[68:71]\n"
"v_mfma_f32_16x16x32_bf16 a[72:75],v[168:171],v[136:139],a[72:75]\n"
"v_mfma_f32_16x16x32_bf16 a[72:75],v[232:235],v[200:203],a[72:75]\n"
"s_mov_b32 s77,2883584\n"
"s_add_u32 m0,s78,114688\n"
"v_mfma_f32_16x16x32_bf16 a[76:79],v[236:239],v[200:203],a[76:79]\n"
"buffer_load_dwordx4 v110,s[20:23],s77 offen offset:0 lds\n"
"v_mfma_f32_16x16x32_bf16 a[76:79],v[172:175],v[136:139],a[76:79]\n"
"v_mfma_f32_16x16x32_bf16 a[80:83],v[176:179],v[136:139],a[80:83]\n"
"v_mfma_f32_16x16x32_bf16 a[80:83],v[240:243],v[200:203],a[80:83]\n"
"v_mfma_f32_16x16x32_bf16 a[84:87],v[244:247],v[200:203],a[84:87]\n"
"buffer_load_dwordx4 v111,s[20:23],s77 offen offset:1024 lds\n"
"v_mfma_f32_16x16x32_bf16 a[84:87],v[180:183],v[136:139],a[84:87]\n"
"v_mfma_f32_16x16x32_bf16 a[88:91],v[184:187],v[136:139],a[88:91]\n"
"v_mfma_f32_16x16x32_bf16 a[88:91],v[248:251],v[200:203],a[88:91]\n"
"v_mfma_f32_16x16x32_bf16 a[92:95],v[252:255],v[200:203],a[92:95]\n"
"buffer_load_dwordx4 v113,s[20:23],s77 offen offset:2048 lds\n"
"v_mfma_f32_16x16x32_bf16 a[92:95],v[188:191],v[136:139],a[92:95]\n"
"v_mfma_f32_16x16x32_bf16 a[96:99],v[160:163],v[140:143],a[96:99]\n"
"v_mfma_f32_16x16x32_bf16 a[96:99],v[224:227],v[204:207],a[96:99]\n"
"v_mfma_f32_16x16x32_bf16 a[100:103],v[228:231],v[204:207],a[100:103]\n"
"buffer_load_dwordx4 v114,s[20:23],s77 offen offset:3072 lds\n"
"v_mfma_f32_16x16x32_bf16 a[100:103],v[164:167],v[140:143],a[100:103]\n"
"v_mfma_f32_16x16x32_bf16 a[104:107],v[168:171],v[140:143],a[104:107]\n"
"v_mfma_f32_16x16x32_bf16 a[104:107],v[232:235],v[204:207],a[104:107]\n"
"v_mfma_f32_16x16x32_bf16 a[108:111],v[236:239],v[204:207],a[108:111]\n"
"s_mov_b32 s76,768\n"
"s_add_u32 m0,s79,0\n"
"v_mfma_f32_16x16x32_bf16 a[108:111],v[172:175],v[140:143],a[108:111]\n"
"buffer_load_dwordx4 v106,s[16:19],s76 offen offset:0 lds\n"
"v_mfma_f32_16x16x32_bf16 a[112:115],v[176:179],v[140:143],a[112:115]\n"
"v_mfma_f32_16x16x32_bf16 a[112:115],v[240:243],v[204:207],a[112:115]\n"
"v_mfma_f32_16x16x32_bf16 a[116:119],v[244:247],v[204:207],a[116:119]\n"
"v_mfma_f32_16x16x32_bf16 a[116:119],v[180:183],v[140:143],a[116:119]\n"
"v_mfma_f32_16x16x32_bf16 a[120:123],v[184:187],v[140:143],a[120:123]\n"
"buffer_load_dwordx4 v107,s[16:19],s76 offen offset:1024 lds\n"
"v_mfma_f32_16x16x32_bf16 a[120:123],v[248:251],v[204:207],a[120:123]\n"
"v_mfma_f32_16x16x32_bf16 a[124:127],v[252:255],v[204:207],a[124:127]\n"
"v_mfma_f32_16x16x32_bf16 a[124:127],v[188:191],v[140:143],a[124:127]\n"
"v_mfma_f32_16x16x32_bf16 a[128:131],v[160:163],v[144:147],a[128:131]\n"
"v_mfma_f32_16x16x32_bf16 a[128:131],v[224:227],v[208:211],a[128:131]\n"
"buffer_load_dwordx4 v108,s[16:19],s76 offen offset:2048 lds\n"
"v_mfma_f32_16x16x32_bf16 a[132:135],v[228:231],v[208:211],a[132:135]\n"
"v_mfma_f32_16x16x32_bf16 a[132:135],v[164:167],v[144:147],a[132:135]\n"
"v_mfma_f32_16x16x32_bf16 a[136:139],v[168:171],v[144:147],a[136:139]\n"
"v_mfma_f32_16x16x32_bf16 a[136:139],v[232:235],v[208:211],a[136:139]\n"
"v_mfma_f32_16x16x32_bf16 a[140:143],v[236:239],v[208:211],a[140:143]\n"
"buffer_load_dwordx4 v109,s[16:19],s76 offen offset:3072 lds\n"
"v_mfma_f32_16x16x32_bf16 a[140:143],v[172:175],v[144:147],a[140:143]\n"
"v_mfma_f32_16x16x32_bf16 a[144:147],v[176:179],v[144:147],a[144:147]\n"
"v_mfma_f32_16x16x32_bf16 a[144:147],v[240:243],v[208:211],a[144:147]\n"
"v_mfma_f32_16x16x32_bf16 a[148:151],v[244:247],v[208:211],a[148:151]\n"
"s_mov_b32 s76,262912\n"
"s_add_u32 m0,s79,4096\n"
"v_mfma_f32_16x16x32_bf16 a[148:151],v[180:183],v[144:147],a[148:151]\n"
"buffer_load_dwordx4 v106,s[16:19],s76 offen offset:0 lds\n"
"v_mfma_f32_16x16x32_bf16 a[152:155],v[184:187],v[144:147],a[152:155]\n"
"v_mfma_f32_16x16x32_bf16 a[152:155],v[248:251],v[208:211],a[152:155]\n"
"v_mfma_f32_16x16x32_bf16 a[156:159],v[252:255],v[208:211],a[156:159]\n"
"v_mfma_f32_16x16x32_bf16 a[156:159],v[188:191],v[144:147],a[156:159]\n"
"v_mfma_f32_16x16x32_bf16 a[160:163],v[160:163],v[148:151],a[160:163]\n"
"buffer_load_dwordx4 v107,s[16:19],s76 offen offset:1024 lds\n"
"v_mfma_f32_16x16x32_bf16 a[160:163],v[224:227],v[212:215],a[160:163]\n"
"v_mfma_f32_16x16x32_bf16 a[164:167],v[228:231],v[212:215],a[164:167]\n"
"v_mfma_f32_16x16x32_bf16 a[164:167],v[164:167],v[148:151],a[164:167]\n"
"v_mfma_f32_16x16x32_bf16 a[168:171],v[168:171],v[148:151],a[168:171]\n"
"v_mfma_f32_16x16x32_bf16 a[168:171],v[232:235],v[212:215],a[168:171]\n"
"buffer_load_dwordx4 v108,s[16:19],s76 offen offset:2048 lds\n"
"v_mfma_f32_16x16x32_bf16 a[172:175],v[236:239],v[212:215],a[172:175]\n"
"v_mfma_f32_16x16x32_bf16 a[172:175],v[172:175],v[148:151],a[172:175]\n"
"v_mfma_f32_16x16x32_bf16 a[176:179],v[176:179],v[148:151],a[176:179]\n"
"v_mfma_f32_16x16x32_bf16 a[176:179],v[240:243],v[212:215],a[176:179]\n"
"v_mfma_f32_16x16x32_bf16 a[180:183],v[244:247],v[212:215],a[180:183]\n"
"buffer_load_dwordx4 v109,s[16:19],s76 offen offset:3072 lds\n"
"v_mfma_f32_16x16x32_bf16 a[180:183],v[180:183],v[148:151],a[180:183]\n"
"s_mov_b32 s87,3145728\n"
"v_mfma_f32_16x16x32_bf16 a[184:187],v[184:187],v[148:151],a[184:187]\n"
"buffer_load_dword v115,v112,s[40:43],s87 offen\n"
"v_mfma_f32_16x16x32_bf16 a[184:187],v[248:251],v[212:215],a[184:187]\n"
"s_mov_b32 s87,4194304\n"
"v_mfma_f32_16x16x32_bf16 a[188:191],v[252:255],v[212:215],a[188:191]\n"
"buffer_load_dword v115,v112,s[40:43],s87 offen\n"
"v_mfma_f32_16x16x32_bf16 a[188:191],v[188:191],v[148:151],a[188:191]\n"
"s_mov_b32 s87,5242880\n"
"s_waitcnt vmcnt(33)\n"
"s_barrier\n"
"v_mfma_f32_16x16x32_bf16 a[192:195],v[224:227],v[216:219],a[192:195]\n"
"ds_read_b128 v[152:155],v80 offset:45056\n"
"buffer_load_dword v115,v112,s[40:43],s87 offen\n"
"v_mfma_f32_16x16x32_bf16 a[224:227],v[224:227],v[220:223],a[224:227]\n"
"ds_read_b128 v[156:159],v80 offset:47104\n"
"v_mfma_f32_16x16x32_bf16 a[228:231],v[228:231],v[220:223],a[228:231]\n"
"ds_read_b64_tr_b16 v[160:161],v98 offset:49152\n"
"v_mfma_f32_16x16x32_bf16 a[196:199],v[228:231],v[216:219],a[196:199]\n"
"ds_read_b64_tr_b16 v[162:163],v98 offset:50176\n"
"v_mfma_f32_16x16x32_bf16 a[200:203],v[232:235],v[216:219],a[200:203]\n"
"ds_read_b64_tr_b16 v[164:165],v99 offset:49152\n"
"v_mfma_f32_16x16x32_bf16 a[232:235],v[232:235],v[220:223],a[232:235]\n"
"ds_read_b64_tr_b16 v[166:167],v99 offset:50176\n"
"v_mfma_f32_16x16x32_bf16 a[236:239],v[236:239],v[220:223],a[236:239]\n"
"ds_read_b64_tr_b16 v[168:169],v100 offset:49152\n"
"v_mfma_f32_16x16x32_bf16 a[204:207],v[236:239],v[216:219],a[204:207]\n"
"ds_read_b64_tr_b16 v[170:171],v100 offset:50176\n"
"v_mfma_f32_16x16x32_bf16 a[208:211],v[240:243],v[216:219],a[208:211]\n"
"ds_read_b64_tr_b16 v[172:173],v101 offset:49152\n"
"v_mfma_f32_16x16x32_bf16 a[240:243],v[240:243],v[220:223],a[240:243]\n"
"ds_read_b64_tr_b16 v[174:175],v101 offset:50176\n"
"v_mfma_f32_16x16x32_bf16 a[244:247],v[244:247],v[220:223],a[244:247]\n"
"ds_read_b64_tr_b16 v[176:177],v102 offset:49152\n"
"v_mfma_f32_16x16x32_bf16 a[212:215],v[244:247],v[216:219],a[212:215]\n"
"ds_read_b64_tr_b16 v[178:179],v102 offset:50176\n"
"v_mfma_f32_16x16x32_bf16 a[216:219],v[248:251],v[216:219],a[216:219]\n"
"ds_read_b64_tr_b16 v[180:181],v103 offset:49152\n"
"v_mfma_f32_16x16x32_bf16 a[248:251],v[248:251],v[220:223],a[248:251]\n"
"ds_read_b64_tr_b16 v[182:183],v103 offset:50176\n"
"v_mfma_f32_16x16x32_bf16 a[252:255],v[252:255],v[220:223],a[252:255]\n"
"ds_read_b64_tr_b16 v[184:185],v104 offset:49152\n"
"ds_read_b64_tr_b16 v[186:187],v104 offset:50176\n"
"v_mfma_f32_16x16x32_bf16 a[220:223],v[252:255],v[216:219],a[220:223]\n"
"s_waitcnt vmcnt(19)\n"
"s_waitcnt lgkmcnt(10)\n"
"v_mfma_f32_16x16x32_bf16 a[192:195],v[160:163],v[152:155],a[192:195]\n"
"ds_read_b64_tr_b16 v[188:189],v105 offset:49152\n"
"ds_read_b64_tr_b16 v[190:191],v105 offset:50176\n"
"v_mfma_f32_16x16x32_bf16 a[224:227],v[160:163],v[156:159],a[224:227]\n"
"buffer_store_dwordx2 v[124:125],off,s[24:27],s57 sc1\n"
"v_mfma_f32_16x16x32_bf16 a[228:231],v[164:167],v[156:159],a[228:231]\n"
"ds_read_b128 v[128:131],v80 offset:32768\n"
"v_mfma_f32_16x16x32_bf16 a[196:199],v[164:167],v[152:155],a[196:199]\n"
"ds_read_b128 v[192:195],v81 offset:32768\n"
"s_waitcnt lgkmcnt(10)\n"
"v_mfma_f32_16x16x32_bf16 a[200:203],v[168:171],v[152:155],a[200:203]\n"
"ds_read_b64_tr_b16 v[224:225],v98 offset:32768\n"
"v_mfma_f32_16x16x32_bf16 a[232:235],v[168:171],v[156:159],a[232:235]\n"
"ds_read_b64_tr_b16 v[226:227],v98 offset:33792\n"
"v_mfma_f32_16x16x32_bf16 a[236:239],v[172:175],v[156:159],a[236:239]\n"
"ds_read_b64_tr_b16 v[228:229],v99 offset:32768\n"
"v_mfma_f32_16x16x32_bf16 a[204:207],v[172:175],v[152:155],a[204:207]\n"
"ds_read_b64_tr_b16 v[230:231],v99 offset:33792\n"
"s_waitcnt lgkmcnt(10)\n"
"v_mfma_f32_16x16x32_bf16 a[208:211],v[176:179],v[152:155],a[208:211]\n"
"ds_read_b64_tr_b16 v[232:233],v100 offset:32768\n"
"v_mfma_f32_16x16x32_bf16 a[240:243],v[176:179],v[156:159],a[240:243]\n"
"ds_read_b64_tr_b16 v[234:235],v100 offset:33792\n"
"v_mfma_f32_16x16x32_bf16 a[244:247],v[180:183],v[156:159],a[244:247]\n"
"ds_read_b64_tr_b16 v[236:237],v101 offset:32768\n"
"v_mfma_f32_16x16x32_bf16 a[212:215],v[180:183],v[152:155],a[212:215]\n"
"ds_read_b64_tr_b16 v[238:239],v101 offset:33792\n"
"s_waitcnt lgkmcnt(10)\n"
"v_mfma_f32_16x16x32_bf16 a[216:219],v[184:187],v[152:155],a[216:219]\n"
"ds_read_b64_tr_b16 v[240:241],v102 offset:32768\n"
"v_mfma_f32_16x16x32_bf16 a[248:251],v[184:187],v[156:159],a[248:251]\n"
"ds_read_b64_tr_b16 v[242:243],v102 offset:33792\n"
"v_mfma_f32_16x16x32_bf16 a[252:255],v[188:191],v[156:159],a[252:255]\n"
"ds_read_b64_tr_b16 v[244:245],v103 offset:32768\n"
"v_mfma_f32_16x16x32_bf16 a[220:223],v[188:191],v[152:155],a[220:223]\n"
"ds_read_b64_tr_b16 v[246:247],v103 offset:33792\n"
"s_waitcnt lgkmcnt(8)\n"
"v_mfma_f32_16x16x32_bf16 a[0:3],v[160:163],v[128:131],a[0:3]\n"
"ds_read_b64_tr_b16 v[248:249],v104 offset:32768\n"
"v_mfma_f32_16x16x32_bf16 a[0:3],v[224:227],v[192:195],a[0:3]\n"
"ds_read_b64_tr_b16 v[250:251],v104 offset:33792\n"
"v_mfma_f32_16x16x32_bf16 a[4:7],v[228:231],v[192:195],a[4:7]\n"
"ds_read_b64_tr_b16 v[252:253],v105 offset:32768\n"
"v_mfma_f32_16x16x32_bf16 a[4:7],v[164:167],v[128:131],a[4:7]\n"
"ds_read_b64_tr_b16 v[254:255],v105 offset:33792\n"
"s_waitcnt lgkmcnt(8)\n"
"v_mfma_f32_16x16x32_bf16 a[8:11],v[168:171],v[128:131],a[8:11]\n"
"ds_read_b128 v[132:135],v80 offset:34816\n"
"v_mfma_f32_16x16x32_bf16 a[8:11],v[232:235],v[192:195],a[8:11]\n"
"ds_read_b128 v[196:199],v81 offset:34816\n"
"v_mfma_f32_16x16x32_bf16 a[12:15],v[236:239],v[192:195],a[12:15]\n"
"ds_read_b128 v[136:139],v80 offset:36864\n"
"v_mfma_f32_16x16x32_bf16 a[12:15],v[172:175],v[128:131],a[12:15]\n"
"ds_read_b128 v[200:203],v81 offset:36864\n"
"s_waitcnt lgkmcnt(8)\n"
"v_mfma_f32_16x16x32_bf16 a[16:19],v[176:179],v[128:131],a[16:19]\n"
"ds_read_b128 v[140:143],v80 offset:38912\n"
"v_mfma_f32_16x16x32_bf16 a[16:19],v[240:243],v[192:195],a[16:19]\n"
"ds_read_b128 v[204:207],v81 offset:38912\n"
"v_mfma_f32_16x16x32_bf16 a[20:23],v[244:247],v[192:195],a[20:23]\n"
"ds_read_b128 v[144:147],v80 offset:40960\n"
"v_mfma_f32_16x16x32_bf16 a[20:23],v[180:183],v[128:131],a[20:23]\n"
"ds_read_b128 v[208:211],v81 offset:40960\n"
"s_waitcnt lgkmcnt(8)\n"
"v_mfma_f32_16x16x32_bf16 a[24:27],v[184:187],v[128:131],a[24:27]\n"
"ds_read_b128 v[148:151],v80 offset:43008\n"
"v_mfma_f32_16x16x32_bf16 a[24:27],v[248:251],v[192:195],a[24:27]\n"
"ds_read_b128 v[212:215],v81 offset:43008\n"
"v_mfma_f32_16x16x32_bf16 a[28:31],v[252:255],v[192:195],a[28:31]\n"
"ds_read_b128 v[216:219],v81 offset:45056\n"
"v_mfma_f32_16x16x32_bf16 a[28:31],v[188:191],v[128:131],a[28:31]\n"
"ds_read_b128 v[220:223],v81 offset:47104\n"
"s_waitcnt lgkmcnt(10)\n"
"v_mfma_f32_16x16x32_bf16 a[32:35],v[160:163],v[132:135],a[32:35]\n"
"v_mfma_f32_16x16x32_bf16 a[32:35],v[224:227],v[196:199],a[32:35]\n"
"v_mfma_f32_16x16x32_bf16 a[36:39],v[228:231],v[196:199],a[36:39]\n"
"v_mfma_f32_16x16x32_bf16 a[36:39],v[164:167],v[132:135],a[36:39]\n"
"v_mfma_f32_16x16x32_bf16 a[40:43],v[168:171],v[132:135],a[40:43]\n"
"s_waitcnt lgkmcnt(0)\n"
"s_barrier\n"
"v_mfma_f32_16x16x32_bf16 a[40:43],v[232:235],v[196:199],a[40:43]\n"
"s_mov_b32 s77,3145728\n"
"s_add_u32 m0,s78,147456\n"
"v_mfma_f32_16x16x32_bf16 a[44:47],v[236:239],v[196:199],a[44:47]\n"
"buffer_load_dwordx4 v110,s[20:23],s77 offen offset:0 lds\n"
"v_mfma_f32_16x16x32_bf16 a[44:47],v[172:175],v[132:135],a[44:47]\n"
"v_mfma_f32_16x16x32_bf16 a[48:51],v[176:179],v[132:135],a[48:51]\n"
"v_mfma_f32_16x16x32_bf16 a[48:51],v[240:243],v[196:199],a[48:51]\n"
"v_mfma_f32_16x16x32_bf16 a[52:55],v[244:247],v[196:199],a[52:55]\n"
"buffer_load_dwordx4 v111,s[20:23],s77 offen offset:1024 lds\n"
"v_mfma_f32_16x16x32_bf16 a[52:55],v[180:183],v[132:135],a[52:55]\n"
"v_mfma_f32_16x16x32_bf16 a[56:59],v[184:187],v[132:135],a[56:59]\n"
"v_mfma_f32_16x16x32_bf16 a[56:59],v[248:251],v[196:199],a[56:59]\n"
"v_mfma_f32_16x16x32_bf16 a[60:63],v[252:255],v[196:199],a[60:63]\n"
"buffer_load_dwordx4 v113,s[20:23],s77 offen offset:2048 lds\n"
"v_mfma_f32_16x16x32_bf16 a[60:63],v[188:191],v[132:135],a[60:63]\n"
"v_mfma_f32_16x16x32_bf16 a[64:67],v[160:163],v[136:139],a[64:67]\n"
"v_mfma_f32_16x16x32_bf16 a[64:67],v[224:227],v[200:203],a[64:67]\n"
"v_mfma_f32_16x16x32_bf16 a[68:71],v[228:231],v[200:203],a[68:71]\n"
"buffer_load_dwordx4 v114,s[20:23],s77 offen offset:3072 lds\n"
"v_mfma_f32_16x16x32_bf16 a[68:71],v[164:167],v[136:139],a[68:71]\n"
"v_mfma_f32_16x16x32_bf16 a[72:75],v[168:171],v[136:139],a[72:75]\n"
"v_mfma_f32_16x16x32_bf16 a[72:75],v[232:235],v[200:203],a[72:75]\n"
"s_mov_b32 s77,3407872\n"
"s_add_u32 m0,s78,131072\n"
"v_mfma_f32_16x16x32_bf16 a[76:79],v[236:239],v[200:203],a[76:79]\n"
"buffer_load_dwordx4 v110,s[20:23],s77 offen offset:0 lds\n"
"v_mfma_f32_16x16x32_bf16 a[76:79],v[172:175],v[136:139],a[76:79]\n"
"v_mfma_f32_16x16x32_bf16 a[80:83],v[176:179],v[136:139],a[80:83]\n"
"v_mfma_f32_16x16x32_bf16 a[80:83],v[240:243],v[200:203],a[80:83]\n"
"v_mfma_f32_16x16x32_bf16 a[84:87],v[244:247],v[200:203],a[84:87]\n"
"buffer_load_dwordx4 v111,s[20:23],s77 offen offset:1024 lds\n"
"v_mfma_f32_16x16x32_bf16 a[84:87],v[180:183],v[136:139],a[84:87]\n"
"v_mfma_f32_16x16x32_bf16 a[88:91],v[184:187],v[136:139],a[88:91]\n"
"v_mfma_f32_16x16x32_bf16 a[88:91],v[248:251],v[200:203],a[88:91]\n"
"v_mfma_f32_16x16x32_bf16 a[92:95],v[252:255],v[200:203],a[92:95]\n"
"buffer_load_dwordx4 v113,s[20:23],s77 offen offset:2048 lds\n"
"v_mfma_f32_16x16x32_bf16 a[92:95],v[188:191],v[136:139],a[92:95]\n"
"v_mfma_f32_16x16x32_bf16 a[96:99],v[160:163],v[140:143],a[96:99]\n"
"v_mfma_f32_16x16x32_bf16 a[96:99],v[224:227],v[204:207],a[96:99]\n"
"v_mfma_f32_16x16x32_bf16 a[100:103],v[228:231],v[204:207],a[100:103]\n"
"buffer_load_dwordx4 v114,s[20:23],s77 offen offset:3072 lds\n"
"v_mfma_f32_16x16x32_bf16 a[100:103],v[164:167],v[140:143],a[100:103]\n"
"v_mfma_f32_16x16x32_bf16 a[104:107],v[168:171],v[140:143],a[104:107]\n"
"v_mfma_f32_16x16x32_bf16 a[104:107],v[232:235],v[204:207],a[104:107]\n"
"v_mfma_f32_16x16x32_bf16 a[108:111],v[236:239],v[204:207],a[108:111]\n"
"s_mov_b32 s76,896\n"
"s_add_u32 m0,s79,32768\n"
"v_mfma_f32_16x16x32_bf16 a[108:111],v[172:175],v[140:143],a[108:111]\n"
"buffer_load_dwordx4 v106,s[16:19],s76 offen offset:0 lds\n"
"v_mfma_f32_16x16x32_bf16 a[112:115],v[176:179],v[140:143],a[112:115]\n"
"v_mfma_f32_16x16x32_bf16 a[112:115],v[240:243],v[204:207],a[112:115]\n"
"v_mfma_f32_16x16x32_bf16 a[116:119],v[244:247],v[204:207],a[116:119]\n"
"v_mfma_f32_16x16x32_bf16 a[116:119],v[180:183],v[140:143],a[116:119]\n"
"v_mfma_f32_16x16x32_bf16 a[120:123],v[184:187],v[140:143],a[120:123]\n"
"buffer_load_dwordx4 v107,s[16:19],s76 offen offset:1024 lds\n"
"v_mfma_f32_16x16x32_bf16 a[120:123],v[248:251],v[204:207],a[120:123]\n"
"v_mfma_f32_16x16x32_bf16 a[124:127],v[252:255],v[204:207],a[124:127]\n"
"v_mfma_f32_16x16x32_bf16 a[124:127],v[188:191],v[140:143],a[124:127]\n"
"v_mfma_f32_16x16x32_bf16 a[128:131],v[160:163],v[144:147],a[128:131]\n"
"v_mfma_f32_16x16x32_bf16 a[128:131],v[224:227],v[208:211],a[128:131]\n"
"buffer_load_dwordx4 v108,s[16:19],s76 offen offset:2048 lds\n"
"v_mfma_f32_16x16x32_bf16 a[132:135],v[228:231],v[208:211],a[132:135]\n"
"v_mfma_f32_16x16x32_bf16 a[132:135],v[164:167],v[144:147],a[132:135]\n"
"v_mfma_f32_16x16x32_bf16 a[136:139],v[168:171],v[144:147],a[136:139]\n"
"v_mfma_f32_16x16x32_bf16 a[136:139],v[232:235],v[208:211],a[136:139]\n"
"v_mfma_f32_16x16x32_bf16 a[140:143],v[236:239],v[208:211],a[140:143]\n"
"buffer_load_dwordx4 v109,s[16:19],s76 offen offset:3072 lds\n"
"v_mfma_f32_16x16x32_bf16 a[140:143],v[172:175],v[144:147],a[140:143]\n"
"v_mfma_f32_16x16x32_bf16 a[144:147],v[176:179],v[144:147],a[144:147]\n"
"v_mfma_f32_16x16x32_bf16 a[144:147],v[240:243],v[208:211],a[144:147]\n"
"v_mfma_f32_16x16x32_bf16 a[148:151],v[244:247],v[208:211],a[148:151]\n"
"s_mov_b32 s76,263040\n"
"s_add_u32 m0,s79,36864\n"
"v_mfma_f32_16x16x32_bf16 a[148:151],v[180:183],v[144:147],a[148:151]\n"
"buffer_load_dwordx4 v106,s[16:19],s76 offen offset:0 lds\n"
"v_mfma_f32_16x16x32_bf16 a[152:155],v[184:187],v[144:147],a[152:155]\n"
"v_mfma_f32_16x16x32_bf16 a[152:155],v[248:251],v[208:211],a[152:155]\n"
"v_mfma_f32_16x16x32_bf16 a[156:159],v[252:255],v[208:211],a[156:159]\n"
"v_mfma_f32_16x16x32_bf16 a[156:159],v[188:191],v[144:147],a[156:159]\n"
"v_mfma_f32_16x16x32_bf16 a[160:163],v[160:163],v[148:151],a[160:163]\n"
"buffer_load_dwordx4 v107,s[16:19],s76 offen offset:1024 lds\n"
"v_mfma_f32_16x16x32_bf16 a[160:163],v[224:227],v[212:215],a[160:163]\n"
"v_mfma_f32_16x16x32_bf16 a[164:167],v[228:231],v[212:215],a[164:167]\n"
"v_mfma_f32_16x16x32_bf16 a[164:167],v[164:167],v[148:151],a[164:167]\n"
"v_mfma_f32_16x16x32_bf16 a[168:171],v[168:171],v[148:151],a[168:171]\n"
"v_mfma_f32_16x16x32_bf16 a[168:171],v[232:235],v[212:215],a[168:171]\n"
"buffer_load_dwordx4 v108,s[16:19],s76 offen offset:2048 lds\n"
"v_mfma_f32_16x16x32_bf16 a[172:175],v[236:239],v[212:215],a[172:175]\n"
"v_mfma_f32_16x16x32_bf16 a[172:175],v[172:175],v[148:151],a[172:175]\n"
"v_mfma_f32_16x16x32_bf16 a[176:179],v[176:179],v[148:151],a[176:179]\n"
"v_mfma_f32_16x16x32_bf16 a[176:179],v[240:243],v[212:215],a[176:179]\n"
"v_mfma_f32_16x16x32_bf16 a[180:183],v[244:247],v[212:215],a[180:183]\n"
"buffer_load_dwordx4 v109,s[16:19],s76 offen offset:3072 lds\n"
"v_mfma_f32_16x16x32_bf16 a[180:183],v[180:183],v[148:151],a[180:183]\n"
"s_mov_b32 s87,6291456\n"
"v_mfma_f32_16x16x32_bf16 a[184:187],v[184:187],v[148:151],a[184:187]\n"
"buffer_load_dword v115,v112,s[40:43],s87 offen\n"
"v_mfma_f32_16x16x32_bf16 a[184:187],v[248:251],v[212:215],a[184:187]\n"
"s_mov_b32 s87,7340032\n"
"v_mfma_f32_16x16x32_bf16 a[188:191],v[252:255],v[212:215],a[188:191]\n"
"buffer_load_dword v115,v112,s[40:43],s87 offen\n"
"v_mfma_f32_16x16x32_bf16 a[188:191],v[188:191],v[148:151],a[188:191]\n"
"s_waitcnt vmcnt(30)\n"
"s_barrier\n"
"v_mfma_f32_16x16x32_bf16 a[192:195],v[224:227],v[216:219],a[192:195]\n"
"ds_read_b128 v[152:155],v82 offset:12288\n"
"v_mfma_f32_16x16x32_bf16 a[224:227],v[224:227],v[220:223],a[224:227]\n"
"ds_read_b128 v[156:159],v82 offset:14336\n"
"v_mfma_f32_16x16x32_bf16 a[228:231],v[228:231],v[220:223],a[228:231]\n"
"ds_read_b64_tr_b16 v[160:161],v98 offset:0\n"
"v_mfma_f32_16x16x32_bf16 a[196:199],v[228:231],v[216:219],a[196:199]\n"
"ds_read_b64_tr_b16 v[162:163],v98 offset:1024\n"
"v_mfma_f32_16x16x32_bf16 a[200:203],v[232:235],v[216:219],a[200:203]\n"
"ds_read_b64_tr_b16 v[164:165],v99 offset:0\n"
"v_mfma_f32_16x16x32_bf16 a[232:235],v[232:235],v[220:223],a[232:235]\n"
"ds_read_b64_tr_b16 v[166:167],v99 offset:1024\n"
"v_mfma_f32_16x16x32_bf16 a[236:239],v[236:239],v[220:223],a[236:239]\n"
"ds_read_b64_tr_b16 v[168:169],v100 offset:0\n"
"v_mfma_f32_16x16x32_bf16 a[204:207],v[236:239],v[216:219],a[204:207]\n"
"ds_read_b64_tr_b16 v[170:171],v100 offset:1024\n"
"v_mfma_f32_16x16x32_bf16 a[208:211],v[240:243],v[216:219],a[208:211]\n"
"ds_read_b64_tr_b16 v[172:173],v101 offset:0\n"
"v_mfma_f32_16x16x32_bf16 a[240:243],v[240:243],v[220:223],a[240:243]\n"
"ds_read_b64_tr_b16 v[174:175],v101 offset:1024\n"
"v_mfma_f32_16x16x32_bf16 a[244:247],v[244:247],v[220:223],a[244:247]\n"
"ds_read_b64_tr_b16 v[176:177],v102 offset:0\n"
"v_mfma_f32_16x16x32_bf16 a[212:215],v[244:247],v[216:219],a[212:215]\n"
"ds_read_b64_tr_b16 v[178:179],v102 offset:1024\n"
"v_mfma_f32_16x16x32_bf16 a[216:219],v[248:251],v[216:219],a[216:219]\n"
"ds_read_b64_tr_b16 v[180:181],v103 offset:0\n"
"v_mfma_f32_16x16x32_bf16 a[248:251],v[248:251],v[220:223],a[248:251]\n"
"ds_read_b64_tr_b16 v[182:183],v103 offset:1024\n"
"v_mfma_f32_16x16x32_bf16 a[252:255],v[252:255],v[220:223],a[252:255]\n"
"ds_read_b64_tr_b16 v[184:185],v104 offset:0\n"
"v_mfma_f32_16x16x32_bf16 a[220:223],v[252:255],v[216:219],a[220:223]\n"
"ds_read_b64_tr_b16 v[186:187],v104 offset:1024\n"
"s_waitcnt lgkmcnt(10)\n"
"v_mfma_f32_16x16x32_bf16 a[192:195],v[160:163],v[152:155],a[192:195]\n"
"ds_read_b64_tr_b16 v[188:189],v105 offset:0\n"
"v_mfma_f32_16x16x32_bf16 a[224:227],v[160:163],v[156:159],a[224:227]\n"
"ds_read_b64_tr_b16 v[190:191],v105 offset:1024\n"
"v_mfma_f32_16x16x32_bf16 a[228:231],v[164:167],v[156:159],a[228:231]\n"
"ds_read_b128 v[128:131],v82 offset:0\n"
"v_mfma_f32_16x16x32_bf16 a[196:199],v[164:167],v[152:155],a[196:199]\n"
"ds_read_b128 v[192:195],v83 offset:0\n"
"s_waitcnt lgkmcnt(10)\n"
"v_mfma_f32_16x16x32_bf16 a[200:203],v[168:171],v[152:155],a[200:203]\n"
"ds_read_b64_tr_b16 v[224:225],v98 offset:16384\n"
"v_mfma_f32_16x16x32_bf16 a[232:235],v[168:171],v[156:159],a[232:235]\n"
"ds_read_b64_tr_b16 v[226:227],v98 offset:17408\n"
"v_mfma_f32_16x16x32_bf16 a[236:239],v[172:175],v[156:159],a[236:239]\n"
"ds_read_b64_tr_b16 v[228:229],v99 offset:16384\n"
"v_mfma_f32_16x16x32_bf16 a[204:207],v[172:175],v[152:155],a[204:207]\n"
"ds_read_b64_tr_b16 v[230:231],v99 offset:17408\n"
"s_waitcnt lgkmcnt(10)\n"
"v_mfma_f32_16x16x32_bf16 a[208:211],v[176:179],v[152:155],a[208:211]\n"
"ds_read_b64_tr_b16 v[232:233],v100 offset:16384\n"
"v_mfma_f32_16x16x32_bf16 a[240:243],v[176:179],v[156:159],a[240:243]\n"
"ds_read_b64_tr_b16 v[234:235],v100 offset:17408\n"
"v_mfma_f32_16x16x32_bf16 a[244:247],v[180:183],v[156:159],a[244:247]\n"
"ds_read_b64_tr_b16 v[236:237],v101 offset:16384\n"
"v_mfma_f32_16x16x32_bf16 a[212:215],v[180:183],v[152:155],a[212:215]\n"
"ds_read_b64_tr_b16 v[238:239],v101 offset:17408\n"
"s_waitcnt lgkmcnt(10)\n"
"v_mfma_f32_16x16x32_bf16 a[216:219],v[184:187],v[152:155],a[216:219]\n"
"ds_read_b64_tr_b16 v[240:241],v102 offset:16384\n"
"v_mfma_f32_16x16x32_bf16 a[248:251],v[184:187],v[156:159],a[248:251]\n"
"ds_read_b64_tr_b16 v[242:243],v102 offset:17408\n"
"v_mfma_f32_16x16x32_bf16 a[252:255],v[188:191],v[156:159],a[252:255]\n"
"ds_read_b64_tr_b16 v[244:245],v103 offset:16384\n"
"v_mfma_f32_16x16x32_bf16 a[220:223],v[188:191],v[152:155],a[220:223]\n"
"ds_read_b64_tr_b16 v[246:247],v103 offset:17408\n"
"s_waitcnt lgkmcnt(8)\n"
"v_mfma_f32_16x16x32_bf16 a[0:3],v[160:163],v[128:131],a[0:3]\n"
"ds_read_b64_tr_b16 v[248:249],v104 offset:16384\n"
"v_mfma_f32_16x16x32_bf16 a[0:3],v[224:227],v[192:195],a[0:3]\n"
"ds_read_b64_tr_b16 v[250:251],v104 offset:17408\n"
"v_mfma_f32_16x16x32_bf16 a[4:7],v[228:231],v[192:195],a[4:7]\n"
"ds_read_b64_tr_b16 v[252:253],v105 offset:16384\n"
"v_mfma_f32_16x16x32_bf16 a[4:7],v[164:167],v[128:131],a[4:7]\n"
"ds_read_b64_tr_b16 v[254:255],v105 offset:17408\n"
"s_waitcnt lgkmcnt(8)\n"
"v_mfma_f32_16x16x32_bf16 a[8:11],v[168:171],v[128:131],a[8:11]\n"
"ds_read_b128 v[132:135],v82 offset:2048\n"
"v_mfma_f32_16x16x32_bf16 a[8:11],v[232:235],v[192:195],a[8:11]\n"
"ds_read_b128 v[196:199],v83 offset:2048\n"
"v_mfma_f32_16x16x32_bf16 a[12:15],v[236:239],v[192:195],a[12:15]\n"
"ds_read_b128 v[136:139],v82 offset:4096\n"
"v_mfma_f32_16x16x32_bf16 a[12:15],v[172:175],v[128:131],a[12:15]\n"
"ds_read_b128 v[200:203],v83 offset:4096\n"
"s_waitcnt lgkmcnt(8)\n"
"v_mfma_f32_16x16x32_bf16 a[16:19],v[176:179],v[128:131],a[16:19]\n"
"ds_read_b128 v[140:143],v82 offset:6144\n"
"v_mfma_f32_16x16x32_bf16 a[16:19],v[240:243],v[192:195],a[16:19]\n"
"ds_read_b128 v[204:207],v83 offset:6144\n"
"v_mfma_f32_16x16x32_bf16 a[20:23],v[244:247],v[192:195],a[20:23]\n"
"ds_read_b128 v[144:147],v82 offset:8192\n"
"v_mfma_f32_16x16x32_bf16 a[20:23],v[180:183],v[128:131],a[20:23]\n"
"ds_read_b128 v[208:211],v83 offset:8192\n"
"s_waitcnt lgkmcnt(8)\n"
"v_mfma_f32_16x16x32_bf16 a[24:27],v[184:187],v[128:131],a[24:27]\n"
"ds_read_b128 v[148:151],v82 offset:10240\n"
"v_mfma_f32_16x16x32_bf16 a[24:27],v[248:251],v[192:195],a[24:27]\n"
"ds_read_b128 v[212:215],v83 offset:10240\n"
"v_mfma_f32_16x16x32_bf16 a[28:31],v[252:255],v[192:195],a[28:31]\n"
"ds_read_b128 v[216:219],v83 offset:12288\n"
"v_mfma_f32_16x16x32_bf16 a[28:31],v[188:191],v[128:131],a[28:31]\n"
"ds_read_b128 v[220:223],v83 offset:14336\n"
"s_waitcnt lgkmcnt(10)\n"
"v_mfma_f32_16x16x32_bf16 a[32:35],v[160:163],v[132:135],a[32:35]\n"
"v_mfma_f32_16x16x32_bf16 a[32:35],v[224:227],v[196:199],a[32:35]\n"
"buffer_load_dwordx4 v[74:77],v112,s[24:27],s58 offen sc1\n"
"v_mfma_f32_16x16x32_bf16 a[36:39],v[228:231],v[196:199],a[36:39]\n"
"v_mfma_f32_16x16x32_bf16 a[36:39],v[164:167],v[132:135],a[36:39]\n"
"v_mfma_f32_16x16x32_bf16 a[40:43],v[168:171],v[132:135],a[40:43]\n"
"s_waitcnt lgkmcnt(0)\n"
"s_barrier\n"
"v_mfma_f32_16x16x32_bf16 a[40:43],v[232:235],v[196:199],a[40:43]\n"
"s_mov_b32 s77,3670016\n"
"s_add_u32 m0,s78,98304\n"
"v_mfma_f32_16x16x32_bf16 a[44:47],v[236:239],v[196:199],a[44:47]\n"
"buffer_load_dwordx4 v110,s[20:23],s77 offen offset:0 lds\n"
"v_mfma_f32_16x16x32_bf16 a[44:47],v[172:175],v[132:135],a[44:47]\n"
"v_mfma_f32_16x16x32_bf16 a[48:51],v[176:179],v[132:135],a[48:51]\n"
"s_load_dwordx2 s[48:49],s[0:1],56\n"
"v_mfma_f32_16x16x32_bf16 a[48:51],v[240:243],v[196:199],a[48:51]\n"
"v_mfma_f32_16x16x32_bf16 a[52:55],v[244:247],v[196:199],a[52:55]\n"
"buffer_load_dwordx4 v111,s[20:23],s77 offen offset:1024 lds\n"
"v_mfma_f32_16x16x32_bf16 a[52:55],v[180:183],v[132:135],a[52:55]\n"
"v_mfma_f32_16x16x32_bf16 a[56:59],v[184:187],v[132:135],a[56:59]\n"
"v_mfma_f32_16x16x32_bf16 a[56:59],v[248:251],v[196:199],a[56:59]\n"
"v_mfma_f32_16x16x32_bf16 a[60:63],v[252:255],v[196:199],a[60:63]\n"
"buffer_load_dwordx4 v113,s[20:23],s77 offen offset:2048 lds\n"
"v_mfma_f32_16x16x32_bf16 a[60:63],v[188:191],v[132:135],a[60:63]\n"
"v_mfma_f32_16x16x32_bf16 a[64:67],v[160:163],v[136:139],a[64:67]\n"
"v_mfma_f32_16x16x32_bf16 a[64:67],v[224:227],v[200:203],a[64:67]\n"
"v_mfma_f32_16x16x32_bf16 a[68:71],v[228:231],v[200:203],a[68:71]\n"
"buffer_load_dwordx4 v114,s[20:23],s77 offen offset:3072 lds\n"
"v_mfma_f32_16x16x32_bf16 a[68:71],v[164:167],v[136:139],a[68:71]\n"
"v_mfma_f32_16x16x32_bf16 a[72:75],v[168:171],v[136:139],a[72:75]\n"
"v_mfma_f32_16x16x32_bf16 a[72:75],v[232:235],v[200:203],a[72:75]\n"
"s_mov_b32 s77,3932160\n"
"s_add_u32 m0,s78,114688\n"
"v_mfma_f32_16x16x32_bf16 a[76:79],v[236:239],v[200:203],a[76:79]\n"
"buffer_load_dwordx4 v110,s[20:23],s77 offen offset:0 lds\n"
"v_mfma_f32_16x16x32_bf16 a[76:79],v[172:175],v[136:139],a[76:79]\n"
"v_mfma_f32_16x16x32_bf16 a[80:83],v[176:179],v[136:139],a[80:83]\n"
"v_mfma_f32_16x16x32_bf16 a[80:83],v[240:243],v[200:203],a[80:83]\n"
"v_mfma_f32_16x16x32_bf16 a[84:87],v[244:247],v[200:203],a[84:87]\n"
"buffer_load_dwordx4 v111,s[20:23],s77 offen offset:1024 lds\n"
"v_mfma_f32_16x16x32_bf16 a[84:87],v[180:183],v[136:139],a[84:87]\n"
"v_mfma_f32_16x16x32_bf16 a[88:91],v[184:187],v[136:139],a[88:91]\n"
"v_mfma_f32_16x16x32_bf16 a[88:91],v[248:251],v[200:203],a[88:91]\n"
"v_mfma_f32_16x16x32_bf16 a[92:95],v[252:255],v[200:203],a[92:95]\n"
"buffer_load_dwordx4 v113,s[20:23],s77 offen offset:2048 lds\n"
"v_mfma_f32_16x16x32_bf16 a[92:95],v[188:191],v[136:139],a[92:95]\n"
"s_mov_b32 s76,s55\n"
"s_waitcnt vmcnt(7)\n"
"v_mfma_f32_16x16x32_bf16 a[96:99],v[160:163],v[140:143],a[96:99]\n"
"v_xor_b32 v116,s80,v74\n"
"v_xor_b32 v117,s81,v75\n"
"v_mfma_f32_16x16x32_bf16 a[96:99],v[224:227],v[204:207],a[96:99]\n"
"v_or_b32 v116,v116,v117\n"
"v_xor_b32 v117,s80,v76\n"
"v_mfma_f32_16x16x32_bf16 a[100:103],v[228:231],v[204:207],a[100:103]\n"
"buffer_load_dwordx4 v114,s[20:23],s77 offen offset:3072 lds\n"
"v_mfma_f32_16x16x32_bf16 a[100:103],v[164:167],v[140:143],a[100:103]\n"
"v_or_b32 v116,v116,v117\n"
"v_xor_b32 v117,s81,v77\n"
"v_mfma_f32_16x16x32_bf16 a[104:107],v[168:171],v[140:143],a[104:107]\n"
"v_or_b32 v116,v116,v117\n"
"v_cmp_ne_u32 vcc,0,v116\n"
"v_mfma_f32_16x16x32_bf16 a[104:107],v[232:235],v[204:207],a[104:107]\n"
"s_cbranch_vccnz .Lfk_xn0_s\n"
".Lfk_xn0_ok:\n"
"v_mfma_f32_16x16x32_bf16 a[108:111],v[236:239],v[204:207],a[108:111]\n"
"s_add_u32 m0,s78,65536\n"
"v_mfma_f32_16x16x32_bf16 a[108:111],v[172:175],v[140:143],a[108:111]\n"
"buffer_load_dwordx4 v112,s[24:27],s76 offen offset:0 sc1 lds\n"
"v_mfma_f32_16x16x32_bf16 a[112:115],v[176:179],v[140:143],a[112:115]\n"
"s_waitcnt lgkmcnt(0)\n"
"s_lshl2_add_u32 s88,s100,s52\n"
"v_mfma_f32_16x16x32_bf16 a[112:115],v[240:243],v[204:207],a[112:115]\n"
"s_mul_i32 s88,s88,7168\n"
"s_add_u32 s88,s88,0\n"
"v_mfma_f32_16x16x32_bf16 a[116:119],v[244:247],v[204:207],a[116:119]\n"
"s_add_u32 s48,s48,s88\n"
"s_addc_u32 s49,s49,0\n"
"s_mov_b32 s50,3584\n"
"v_mfma_f32_16x16x32_bf16 a[116:119],v[180:183],v[140:143],a[116:119]\n"
"s_mov_b32 s51,0x20000\n"
"v_lshlrev_b32 v8,3,v112\n"
"v_mfma_f32_16x16x32_bf16 a[120:123],v[184:187],v[140:143],a[120:123]\n"
"buffer_load_dwordx4 v112,s[24:27],s76 offen offset:1024 sc1 lds\n"
"v_mfma_f32_16x16x32_bf16 a[120:123],v[248:251],v[204:207],a[120:123]\n"
"v_mfma_f32_16x16x32_bf16 a[124:127],v[252:255],v[204:207],a[124:127]\n"
"v_mfma_f32_16x16x32_bf16 a[124:127],v[188:191],v[140:143],a[124:127]\n"
"v_mfma_f32_16x16x32_bf16 a[128:131],v[160:163],v[144:147],a[128:131]\n"
"v_mfma_f32_16x16x32_bf16 a[128:131],v[224:227],v[208:211],a[128:131]\n"
"buffer_load_dwordx4 v112,s[24:27],s76 offen offset:2048 sc1 lds\n"
"v_mfma_f32_16x16x32_bf16 a[132:135],v[228:231],v[208:211],a[132:135]\n"
"v_mfma_f32_16x16x32_bf16 a[132:135],v[164:167],v[144:147],a[132:135]\n"
"v_mfma_f32_16x16x32_bf16 a[136:139],v[168:171],v[144:147],a[136:139]\n"
"v_mfma_f32_16x16x32_bf16 a[136:139],v[232:235],v[208:211],a[136:139]\n"
"buffer_load_dwordx4 v112,s[24:27],s76 offen offset:3072 sc1 lds\n"
"v_mfma_f32_16x16x32_bf16 a[140:143],v[236:239],v[208:211],a[140:143]\n"
"s_xor_b32 s76,s76,16384\n"
"v_mfma_f32_16x16x32_bf16 a[140:143],v[172:175],v[144:147],a[140:143]\n"
"v_mfma_f32_16x16x32_bf16 a[144:147],v[176:179],v[144:147],a[144:147]\n"
"v_mfma_f32_16x16x32_bf16 a[144:147],v[240:243],v[208:211],a[144:147]\n"
"v_mfma_f32_16x16x32_bf16 a[148:151],v[244:247],v[208:211],a[148:151]\n"
"s_add_u32 m0,s78,81920\n"
"v_mfma_f32_16x16x32_bf16 a[148:151],v[180:183],v[144:147],a[148:151]\n"
"buffer_load_dwordx4 v112,s[24:27],s76 offen offset:0 sc1 lds\n"
"v_mfma_f32_16x16x32_bf16 a[152:155],v[184:187],v[144:147],a[152:155]\n"
"v_mfma_f32_16x16x32_bf16 a[152:155],v[248:251],v[208:211],a[152:155]\n"
"v_mfma_f32_16x16x32_bf16 a[156:159],v[252:255],v[208:211],a[156:159]\n"
"v_mfma_f32_16x16x32_bf16 a[156:159],v[188:191],v[144:147],a[156:159]\n"
"v_mfma_f32_16x16x32_bf16 a[160:163],v[160:163],v[148:151],a[160:163]\n"
"buffer_load_dwordx4 v112,s[24:27],s76 offen offset:1024 sc1 lds\n"
"v_mfma_f32_16x16x32_bf16 a[160:163],v[224:227],v[212:215],a[160:163]\n"
"v_mfma_f32_16x16x32_bf16 a[164:167],v[228:231],v[212:215],a[164:167]\n"
"v_mfma_f32_16x16x32_bf16 a[164:167],v[164:167],v[148:151],a[164:167]\n"
"v_mfma_f32_16x16x32_bf16 a[168:171],v[168:171],v[148:151],a[168:171]\n"
"v_mfma_f32_16x16x32_bf16 a[168:171],v[232:235],v[212:215],a[168:171]\n"
"buffer_load_dwordx4 v112,s[24:27],s76 offen offset:2048 sc1 lds\n"
"v_mfma_f32_16x16x32_bf16 a[172:175],v[236:239],v[212:215],a[172:175]\n"
"v_mfma_f32_16x16x32_bf16 a[172:175],v[172:175],v[148:151],a[172:175]\n"
"v_mfma_f32_16x16x32_bf16 a[176:179],v[176:179],v[148:151],a[176:179]\n"
"v_mfma_f32_16x16x32_bf16 a[176:179],v[240:243],v[212:215],a[176:179]\n"
"buffer_load_dwordx4 v112,s[24:27],s76 offen offset:3072 sc1 lds\n"
"v_mfma_f32_16x16x32_bf16 a[180:183],v[244:247],v[212:215],a[180:183]\n"
"s_xor_b32 s76,s76,49152\n"
"v_mfma_f32_16x16x32_bf16 a[180:183],v[180:183],v[148:151],a[180:183]\n"
"v_mfma_f32_16x16x32_bf16 a[184:187],v[184:187],v[148:151],a[184:187]\n"
"buffer_load_dword v115,v8,s[48:51],0 offen\n"
"v_mfma_f32_16x16x32_bf16 a[184:187],v[248:251],v[212:215],a[184:187]\n"
"v_mfma_f32_16x16x32_bf16 a[188:191],v[252:255],v[212:215],a[188:191]\n"
"v_mfma_f32_16x16x32_bf16 a[188:191],v[188:191],v[148:151],a[188:191]\n"
"s_barrier\n"
"v_mfma_f32_16x16x32_bf16 a[192:195],v[224:227],v[216:219],a[192:195]\n"
"ds_read_b128 v[152:155],v80 offset:12288\n"
"v_mfma_f32_16x16x32_bf16 a[224:227],v[224:227],v[220:223],a[224:227]\n"
"ds_read_b128 v[156:159],v80 offset:14336\n"
"v_mfma_f32_16x16x32_bf16 a[228:231],v[228:231],v[220:223],a[228:231]\n"
"ds_read_b64_tr_b16 v[160:161],v98 offset:49152\n"
"v_mfma_f32_16x16x32_bf16 a[196:199],v[228:231],v[216:219],a[196:199]\n"
"ds_read_b64_tr_b16 v[162:163],v98 offset:50176\n"
"v_mfma_f32_16x16x32_bf16 a[200:203],v[232:235],v[216:219],a[200:203]\n"
"ds_read_b64_tr_b16 v[164:165],v99 offset:49152\n"
"v_mfma_f32_16x16x32_bf16 a[232:235],v[232:235],v[220:223],a[232:235]\n"
"ds_read_b64_tr_b16 v[166:167],v99 offset:50176\n"
"v_mfma_f32_16x16x32_bf16 a[236:239],v[236:239],v[220:223],a[236:239]\n"
"ds_read_b64_tr_b16 v[168:169],v100 offset:49152\n"
"v_mfma_f32_16x16x32_bf16 a[204:207],v[236:239],v[216:219],a[204:207]\n"
"ds_read_b64_tr_b16 v[170:171],v100 offset:50176\n"
"v_mfma_f32_16x16x32_bf16 a[208:211],v[240:243],v[216:219],a[208:211]\n"
"ds_read_b64_tr_b16 v[172:173],v101 offset:49152\n"
"v_mfma_f32_16x16x32_bf16 a[240:243],v[240:243],v[220:223],a[240:243]\n"
"ds_read_b64_tr_b16 v[174:175],v101 offset:50176\n"
"v_mfma_f32_16x16x32_bf16 a[244:247],v[244:247],v[220:223],a[244:247]\n"
"ds_read_b64_tr_b16 v[176:177],v102 offset:49152\n"
"v_mfma_f32_16x16x32_bf16 a[212:215],v[244:247],v[216:219],a[212:215]\n"
"ds_read_b64_tr_b16 v[178:179],v102 offset:50176\n"
"v_mfma_f32_16x16x32_bf16 a[216:219],v[248:251],v[216:219],a[216:219]\n"
"ds_read_b64_tr_b16 v[180:181],v103 offset:49152\n"
"v_mfma_f32_16x16x32_bf16 a[248:251],v[248:251],v[220:223],a[248:251]\n"
"ds_read_b64_tr_b16 v[182:183],v103 offset:50176\n"
"ds_read_b64_tr_b16 v[184:185],v104 offset:49152\n"
"v_mfma_f32_16x16x32_bf16 a[252:255],v[252:255],v[220:223],a[252:255]\n"
"v_mov_b32 v110,v72\n"
"v_mov_b32 v111,v73\n"
"v_mfma_f32_16x16x32_bf16 a[220:223],v[252:255],v[216:219],a[220:223]\n"
"ds_read_b64_tr_b16 v[186:187],v104 offset:50176\n"
"s_waitcnt lgkmcnt(10)\n"
"v_mfma_f32_16x16x32_bf16 a[192:195],v[160:163],v[152:155],a[192:195]\n"
"ds_read_b64_tr_b16 v[188:189],v105 offset:49152\n"
"v_mfma_f32_16x16x32_bf16 a[224:227],v[160:163],v[156:159],a[224:227]\n"
"ds_read_b64_tr_b16 v[190:191],v105 offset:50176\n"
"v_mfma_f32_16x16x32_bf16 a[228:231],v[164:167],v[156:159],a[228:231]\n"
"ds_read_b128 v[128:131],v80 offset:0\n"
"v_mfma_f32_16x16x32_bf16 a[196:199],v[164:167],v[152:155],a[196:199]\n"
"ds_read_b128 v[192:195],v81 offset:0\n"
"s_waitcnt lgkmcnt(10)\n"
"v_mfma_f32_16x16x32_bf16 a[200:203],v[168:171],v[152:155],a[200:203]\n"
"ds_read_b64_tr_b16 v[224:225],v98 offset:32768\n"
"v_mfma_f32_16x16x32_bf16 a[232:235],v[168:171],v[156:159],a[232:235]\n"
"ds_read_b64_tr_b16 v[226:227],v98 offset:33792\n"
"v_mfma_f32_16x16x32_bf16 a[236:239],v[172:175],v[156:159],a[236:239]\n"
"ds_read_b64_tr_b16 v[228:229],v99 offset:32768\n"
"v_mfma_f32_16x16x32_bf16 a[204:207],v[172:175],v[152:155],a[204:207]\n"
"ds_read_b64_tr_b16 v[230:231],v99 offset:33792\n"
"s_waitcnt lgkmcnt(10)\n"
"v_mfma_f32_16x16x32_bf16 a[208:211],v[176:179],v[152:155],a[208:211]\n"
"ds_read_b64_tr_b16 v[232:233],v100 offset:32768\n"
"v_mfma_f32_16x16x32_bf16 a[240:243],v[176:179],v[156:159],a[240:243]\n"
"ds_read_b64_tr_b16 v[234:235],v100 offset:33792\n"
"v_mfma_f32_16x16x32_bf16 a[244:247],v[180:183],v[156:159],a[244:247]\n"
"ds_read_b64_tr_b16 v[236:237],v101 offset:32768\n"
"v_mfma_f32_16x16x32_bf16 a[212:215],v[180:183],v[152:155],a[212:215]\n"
"ds_read_b64_tr_b16 v[238:239],v101 offset:33792\n"
"s_waitcnt lgkmcnt(10)\n"
"v_mfma_f32_16x16x32_bf16 a[216:219],v[184:187],v[152:155],a[216:219]\n"
"ds_read_b64_tr_b16 v[240:241],v102 offset:32768\n"
"v_mfma_f32_16x16x32_bf16 a[248:251],v[184:187],v[156:159],a[248:251]\n"
"ds_read_b64_tr_b16 v[242:243],v102 offset:33792\n"
"v_mfma_f32_16x16x32_bf16 a[252:255],v[188:191],v[156:159],a[252:255]\n"
"ds_read_b64_tr_b16 v[244:245],v103 offset:32768\n"
"v_mfma_f32_16x16x32_bf16 a[220:223],v[188:191],v[152:155],a[220:223]\n"
"ds_read_b64_tr_b16 v[246:247],v103 offset:33792\n"
"s_waitcnt lgkmcnt(8)\n"
"v_mfma_f32_16x16x32_bf16 a[0:3],v[160:163],v[128:131],a[0:3]\n"
"ds_read_b64_tr_b16 v[248:249],v104 offset:32768\n"
"v_mfma_f32_16x16x32_bf16 a[0:3],v[224:227],v[192:195],a[0:3]\n"
"ds_read_b64_tr_b16 v[250:251],v104 offset:33792\n"
"v_mfma_f32_16x16x32_bf16 a[4:7],v[228:231],v[192:195],a[4:7]\n"
"ds_read_b64_tr_b16 v[252:253],v105 offset:32768\n"
"v_mfma_f32_16x16x32_bf16 a[4:7],v[164:167],v[128:131],a[4:7]\n"
"ds_read_b64_tr_b16 v[254:255],v105 offset:33792\n"
"s_waitcnt lgkmcnt(8)\n"
"v_mfma_f32_16x16x32_bf16 a[8:11],v[168:171],v[128:131],a[8:11]\n"
"ds_read_b128 v[132:135],v80 offset:2048\n"
"v_mfma_f32_16x16x32_bf16 a[8:11],v[232:235],v[192:195],a[8:11]\n"
"ds_read_b128 v[196:199],v81 offset:2048\n"
"v_mfma_f32_16x16x32_bf16 a[12:15],v[236:239],v[192:195],a[12:15]\n"
"ds_read_b128 v[136:139],v80 offset:4096\n"
"v_mfma_f32_16x16x32_bf16 a[12:15],v[172:175],v[128:131],a[12:15]\n"
"ds_read_b128 v[200:203],v81 offset:4096\n"
"s_waitcnt lgkmcnt(8)\n"
"v_mfma_f32_16x16x32_bf16 a[16:19],v[176:179],v[128:131],a[16:19]\n"
"ds_read_b128 v[140:143],v80 offset:6144\n"
"v_mfma_f32_16x16x32_bf16 a[16:19],v[240:243],v[192:195],a[16:19]\n"
"ds_read_b128 v[204:207],v81 offset:6144\n"
"v_mfma_f32_16x16x32_bf16 a[20:23],v[244:247],v[192:195],a[20:23]\n"
"ds_read_b128 v[144:147],v80 offset:8192\n"
"v_mfma_f32_16x16x32_bf16 a[20:23],v[180:183],v[128:131],a[20:23]\n"
"ds_read_b128 v[208:211],v81 offset:8192\n"
"s_waitcnt lgkmcnt(8)\n"
"v_mfma_f32_16x16x32_bf16 a[24:27],v[184:187],v[128:131],a[24:27]\n"
"ds_read_b128 v[148:151],v80 offset:10240\n"
"v_mfma_f32_16x16x32_bf16 a[24:27],v[248:251],v[192:195],a[24:27]\n"
"ds_read_b128 v[212:215],v81 offset:10240\n"
"v_mfma_f32_16x16x32_bf16 a[28:31],v[252:255],v[192:195],a[28:31]\n"
"ds_read_b128 v[216:219],v81 offset:12288\n"
"v_mfma_f32_16x16x32_bf16 a[28:31],v[188:191],v[128:131],a[28:31]\n"
"ds_read_b128 v[220:223],v81 offset:14336\n"
"s_waitcnt lgkmcnt(10)\n"
"v_mfma_f32_16x16x32_bf16 a[32:35],v[160:163],v[132:135],a[32:35]\n"
"v_mfma_f32_16x16x32_bf16 a[32:35],v[224:227],v[196:199],a[32:35]\n"
"v_mfma_f32_16x16x32_bf16 a[36:39],v[228:231],v[196:199],a[36:39]\n"
"v_mfma_f32_16x16x32_bf16 a[36:39],v[164:167],v[132:135],a[36:39]\n"
"v_mfma_f32_16x16x32_bf16 a[40:43],v[168:171],v[132:135],a[40:43]\n"
"s_waitcnt lgkmcnt(0)\n"
"s_barrier\n"
"v_mfma_f32_16x16x32_bf16 a[40:43],v[232:235],v[196:199],a[40:43]\n"
"s_add_u32 m0,s85,147456\n"
"v_mfma_f32_16x16x32_bf16 a[44:47],v[236:239],v[196:199],a[44:47]\n"
"buffer_load_dwordx4 v110,s[28:31],s86 offen lds\n"
"v_mfma_f32_16x16x32_bf16 a[44:47],v[172:175],v[132:135],a[44:47]\n"
"v_mfma_f32_16x16x32_bf16 a[48:51],v[176:179],v[132:135],a[48:51]\n"
"s_load_dwordx2 s[48:49],s[0:1],56\n"
"v_mfma_f32_16x16x32_bf16 a[48:51],v[240:243],v[196:199],a[48:51]\n"
"s_add_u32 m0,s85,148480\n"
"v_mfma_f32_16x16x32_bf16 a[52:55],v[244:247],v[196:199],a[52:55]\n"
"buffer_load_dwordx4 v111,s[28:31],s86 offen lds\n"
"v_mfma_f32_16x16x32_bf16 a[52:55],v[180:183],v[132:135],a[52:55]\n"
"s_xor_b32 s86,s86,262144\n"
"v_mfma_f32_16x16x32_bf16 a[56:59],v[184:187],v[132:135],a[56:59]\n"
"v_mfma_f32_16x16x32_bf16 a[56:59],v[248:251],v[196:199],a[56:59]\n"
"s_add_u32 m0,s85,155648\n"
"v_mfma_f32_16x16x32_bf16 a[60:63],v[252:255],v[196:199],a[60:63]\n"
"buffer_load_dwordx4 v110,s[28:31],s86 offen lds\n"
"v_mfma_f32_16x16x32_bf16 a[60:63],v[188:191],v[132:135],a[60:63]\n"
"v_mfma_f32_16x16x32_bf16 a[64:67],v[160:163],v[136:139],a[64:67]\n"
"v_mfma_f32_16x16x32_bf16 a[64:67],v[224:227],v[200:203],a[64:67]\n"
"s_add_u32 m0,s85,156672\n"
"v_mfma_f32_16x16x32_bf16 a[68:71],v[228:231],v[200:203],a[68:71]\n"
"buffer_load_dwordx4 v111,s[28:31],s86 offen lds\n"
"v_mfma_f32_16x16x32_bf16 a[68:71],v[164:167],v[136:139],a[68:71]\n"
"s_xor_b32 s86,s86,786432\n"
"v_mfma_f32_16x16x32_bf16 a[72:75],v[168:171],v[136:139],a[72:75]\n"
"v_mfma_f32_16x16x32_bf16 a[72:75],v[232:235],v[200:203],a[72:75]\n"
"s_add_u32 m0,s85,131072\n"
"v_mfma_f32_16x16x32_bf16 a[76:79],v[236:239],v[200:203],a[76:79]\n"
"buffer_load_dwordx4 v110,s[28:31],s86 offen lds\n"
"v_mfma_f32_16x16x32_bf16 a[76:79],v[172:175],v[136:139],a[76:79]\n"
"v_mfma_f32_16x16x32_bf16 a[80:83],v[176:179],v[136:139],a[80:83]\n"
"v_mfma_f32_16x16x32_bf16 a[80:83],v[240:243],v[200:203],a[80:83]\n"
"s_add_u32 m0,s85,132096\n"
"v_mfma_f32_16x16x32_bf16 a[84:87],v[244:247],v[200:203],a[84:87]\n"
"buffer_load_dwordx4 v111,s[28:31],s86 offen lds\n"
"v_mfma_f32_16x16x32_bf16 a[84:87],v[180:183],v[136:139],a[84:87]\n"
"s_xor_b32 s86,s86,262144\n"
"v_mfma_f32_16x16x32_bf16 a[88:91],v[184:187],v[136:139],a[88:91]\n"
"v_mfma_f32_16x16x32_bf16 a[88:91],v[248:251],v[200:203],a[88:91]\n"
"v_mfma_f32_16x16x32_bf16 a[92:95],v[252:255],v[200:203],a[92:95]\n"
"v_mfma_f32_16x16x32_bf16 a[92:95],v[188:191],v[136:139],a[92:95]\n"
"v_mfma_f32_16x16x32_bf16 a[96:99],v[160:163],v[140:143],a[96:99]\n"
"v_mfma_f32_16x16x32_bf16 a[96:99],v[224:227],v[204:207],a[96:99]\n"
"s_add_u32 m0,s85,139264\n"
"v_mfma_f32_16x16x32_bf16 a[100:103],v[228:231],v[204:207],a[100:103]\n"
"buffer_load_dwordx4 v110,s[28:31],s86 offen lds\n"
"v_mfma_f32_16x16x32_bf16 a[100:103],v[164:167],v[140:143],a[100:103]\n"
"v_mfma_f32_16x16x32_bf16 a[104:107],v[168:171],v[140:143],a[104:107]\n"
"v_mfma_f32_16x16x32_bf16 a[104:107],v[232:235],v[204:207],a[104:107]\n"
"s_add_u32 m0,s85,140288\n"
"v_mfma_f32_16x16x32_bf16 a[108:111],v[236:239],v[204:207],a[108:111]\n"
"buffer_load_dwordx4 v111,s[28:31],s86 offen lds\n"
"v_mfma_f32_16x16x32_bf16 a[108:111],v[172:175],v[140:143],a[108:111]\n"
"s_xor_b32 s86,s86,1835008\n"
"v_mfma_f32_16x16x32_bf16 a[112:115],v[176:179],v[140:143],a[112:115]\n"
"s_waitcnt lgkmcnt(0)\n"
"s_lshl2_add_u32 s88,s100,s52\n"
"v_mfma_f32_16x16x32_bf16 a[112:115],v[240:243],v[204:207],a[112:115]\n"
"s_mul_i32 s88,s88,7168\n"
"s_add_u32 m0,s78,0\n"
"v_mfma_f32_16x16x32_bf16 a[116:119],v[244:247],v[204:207],a[116:119]\n"
"buffer_load_dwordx4 v112,s[24:27],s76 offen offset:0 sc1 lds\n"
"v_mfma_f32_16x16x32_bf16 a[116:119],v[180:183],v[140:143],a[116:119]\n"
"s_add_u32 s88,s88,3584\n"
"s_add_u32 s48,s48,s88\n"
"s_addc_u32 s49,s49,0\n"
"v_mfma_f32_16x16x32_bf16 a[120:123],v[184:187],v[140:143],a[120:123]\n"
"s_mov_b32 s50,3584\n"
"s_mov_b32 s51,0x20000\n"
"v_mfma_f32_16x16x32_bf16 a[120:123],v[248:251],v[204:207],a[120:123]\n"
"v_lshlrev_b32 v8,3,v112\n"
"v_mfma_f32_16x16x32_bf16 a[124:127],v[252:255],v[204:207],a[124:127]\n"
"buffer_load_dwordx4 v112,s[24:27],s76 offen offset:1024 sc1 lds\n"
"v_mfma_f32_16x16x32_bf16 a[124:127],v[188:191],v[140:143],a[124:127]\n"
"v_mfma_f32_16x16x32_bf16 a[128:131],v[160:163],v[144:147],a[128:131]\n"
"v_mfma_f32_16x16x32_bf16 a[128:131],v[224:227],v[208:211],a[128:131]\n"
"v_mfma_f32_16x16x32_bf16 a[132:135],v[228:231],v[208:211],a[132:135]\n"
"buffer_load_dwordx4 v112,s[24:27],s76 offen offset:2048 sc1 lds\n"
"v_mfma_f32_16x16x32_bf16 a[132:135],v[164:167],v[144:147],a[132:135]\n"
"v_mfma_f32_16x16x32_bf16 a[136:139],v[168:171],v[144:147],a[136:139]\n"
"v_mfma_f32_16x16x32_bf16 a[136:139],v[232:235],v[208:211],a[136:139]\n"
"buffer_load_dwordx4 v112,s[24:27],s76 offen offset:3072 sc1 lds\n"
"v_mfma_f32_16x16x32_bf16 a[140:143],v[236:239],v[208:211],a[140:143]\n"
"s_xor_b32 s76,s76,16384\n"
"v_mfma_f32_16x16x32_bf16 a[140:143],v[172:175],v[144:147],a[140:143]\n"
"v_mfma_f32_16x16x32_bf16 a[144:147],v[176:179],v[144:147],a[144:147]\n"
"v_mfma_f32_16x16x32_bf16 a[144:147],v[240:243],v[208:211],a[144:147]\n"
"s_add_u32 m0,s78,16384\n"
"v_mfma_f32_16x16x32_bf16 a[148:151],v[244:247],v[208:211],a[148:151]\n"
"buffer_load_dwordx4 v112,s[24:27],s76 offen offset:0 sc1 lds\n"
"v_mfma_f32_16x16x32_bf16 a[148:151],v[180:183],v[144:147],a[148:151]\n"
"v_mfma_f32_16x16x32_bf16 a[152:155],v[184:187],v[144:147],a[152:155]\n"
"v_mfma_f32_16x16x32_bf16 a[152:155],v[248:251],v[208:211],a[152:155]\n"
"v_mfma_f32_16x16x32_bf16 a[156:159],v[252:255],v[208:211],a[156:159]\n"
"buffer_load_dwordx4 v112,s[24:27],s76 offen offset:1024 sc1 lds\n"
"v_mfma_f32_16x16x32_bf16 a[156:159],v[188:191],v[144:147],a[156:159]\n"
"v_mfma_f32_16x16x32_bf16 a[160:163],v[160:163],v[148:151],a[160:163]\n"
"v_mfma_f32_16x16x32_bf16 a[160:163],v[224:227],v[212:215],a[160:163]\n"
"v_mfma_f32_16x16x32_bf16 a[164:167],v[228:231],v[212:215],a[164:167]\n"
"buffer_load_dwordx4 v112,s[24:27],s76 offen offset:2048 sc1 lds\n"
"v_mfma_f32_16x16x32_bf16 a[164:167],v[164:167],v[148:151],a[164:167]\n"
"v_mfma_f32_16x16x32_bf16 a[168:171],v[168:171],v[148:151],a[168:171]\n"
"v_mfma_f32_16x16x32_bf16 a[168:171],v[232:235],v[212:215],a[168:171]\n"
"buffer_load_dwordx4 v112,s[24:27],s76 offen offset:3072 sc1 lds\n"
"v_mfma_f32_16x16x32_bf16 a[172:175],v[236:239],v[212:215],a[172:175]\n"
"s_xor_b32 s76,s76,114688\n"
"v_mfma_f32_16x16x32_bf16 a[172:175],v[172:175],v[148:151],a[172:175]\n"
"v_mfma_f32_16x16x32_bf16 a[176:179],v[176:179],v[148:151],a[176:179]\n"
"buffer_load_dword v115,v8,s[48:51],0 offen\n"
"v_mfma_f32_16x16x32_bf16 a[176:179],v[240:243],v[212:215],a[176:179]\n"
"v_mfma_f32_16x16x32_bf16 a[180:183],v[244:247],v[212:215],a[180:183]\n"
"v_mfma_f32_16x16x32_bf16 a[180:183],v[180:183],v[148:151],a[180:183]\n"
"v_mfma_f32_16x16x32_bf16 a[184:187],v[184:187],v[148:151],a[184:187]\n"
"v_mfma_f32_16x16x32_bf16 a[184:187],v[248:251],v[212:215],a[184:187]\n"
"v_mfma_f32_16x16x32_bf16 a[188:191],v[252:255],v[212:215],a[188:191]\n"
"v_mfma_f32_16x16x32_bf16 a[188:191],v[188:191],v[148:151],a[188:191]\n"
"s_waitcnt vmcnt(26)\n"
"s_barrier\n"
"v_mfma_f32_16x16x32_bf16 a[192:195],v[224:227],v[216:219],a[192:195]\n"
"ds_read_b128 v[152:155],v80 offset:45056\n"
"v_mfma_f32_16x16x32_bf16 a[224:227],v[224:227],v[220:223],a[224:227]\n"
"ds_read_b128 v[156:159],v80 offset:47104\n"
"v_mfma_f32_16x16x32_bf16 a[228:231],v[228:231],v[220:223],a[228:231]\n"
"ds_read_b64_tr_b16 v[160:161],v98 offset:0\n"
"v_mfma_f32_16x16x32_bf16 a[196:199],v[228:231],v[216:219],a[196:199]\n"
"ds_read_b64_tr_b16 v[162:163],v98 offset:1024\n"
"v_mfma_f32_16x16x32_bf16 a[200:203],v[232:235],v[216:219],a[200:203]\n"
"ds_read_b64_tr_b16 v[164:165],v99 offset:0\n"
"v_mfma_f32_16x16x32_bf16 a[232:235],v[232:235],v[220:223],a[232:235]\n"
"ds_read_b64_tr_b16 v[166:167],v99 offset:1024\n"
"v_mfma_f32_16x16x32_bf16 a[236:239],v[236:239],v[220:223],a[236:239]\n"
"ds_read_b64_tr_b16 v[168:169],v100 offset:0\n"
"v_mfma_f32_16x16x32_bf16 a[204:207],v[236:239],v[216:219],a[204:207]\n"
"ds_read_b64_tr_b16 v[170:171],v100 offset:1024\n"
"v_mfma_f32_16x16x32_bf16 a[208:211],v[240:243],v[216:219],a[208:211]\n"
"ds_read_b64_tr_b16 v[172:173],v101 offset:0\n"
"v_mfma_f32_16x16x32_bf16 a[240:243],v[240:243],v[220:223],a[240:243]\n"
"ds_read_b64_tr_b16 v[174:175],v101 offset:1024\n"
"v_mfma_f32_16x16x32_bf16 a[244:247],v[244:247],v[220:223],a[244:247]\n"
"ds_read_b64_tr_b16 v[176:177],v102 offset:0\n"
"v_mfma_f32_16x16x32_bf16 a[212:215],v[244:247],v[216:219],a[212:215]\n"
"ds_read_b64_tr_b16 v[178:179],v102 offset:1024\n"
"v_mfma_f32_16x16x32_bf16 a[216:219],v[248:251],v[216:219],a[216:219]\n"
"ds_read_b64_tr_b16 v[180:181],v103 offset:0\n"
"v_mfma_f32_16x16x32_bf16 a[248:251],v[248:251],v[220:223],a[248:251]\n"
"ds_read_b64_tr_b16 v[182:183],v103 offset:1024\n"
"v_mfma_f32_16x16x32_bf16 a[252:255],v[252:255],v[220:223],a[252:255]\n"
"ds_read_b64_tr_b16 v[184:185],v104 offset:0\n"
"v_mfma_f32_16x16x32_bf16 a[220:223],v[252:255],v[216:219],a[220:223]\n"
"ds_read_b64_tr_b16 v[186:187],v104 offset:1024\n"
"s_waitcnt lgkmcnt(10)\n"
"v_mfma_f32_16x16x32_bf16 a[192:195],v[160:163],v[152:155],a[192:195]\n"
"ds_read_b64_tr_b16 v[188:189],v105 offset:0\n"
"v_mfma_f32_16x16x32_bf16 a[224:227],v[160:163],v[156:159],a[224:227]\n"
"ds_read_b64_tr_b16 v[190:191],v105 offset:1024\n"
"v_mfma_f32_16x16x32_bf16 a[228:231],v[164:167],v[156:159],a[228:231]\n"
"ds_read_b128 v[128:131],v80 offset:32768\n"
"v_mfma_f32_16x16x32_bf16 a[196:199],v[164:167],v[152:155],a[196:199]\n"
"ds_read_b128 v[192:195],v81 offset:32768\n"
"s_waitcnt lgkmcnt(10)\n"
"v_mfma_f32_16x16x32_bf16 a[200:203],v[168:171],v[152:155],a[200:203]\n"
"ds_read_b64_tr_b16 v[224:225],v98 offset:16384\n"
"v_mfma_f32_16x16x32_bf16 a[232:235],v[168:171],v[156:159],a[232:235]\n"
"ds_read_b64_tr_b16 v[226:227],v98 offset:17408\n"
"v_mfma_f32_16x16x32_bf16 a[236:239],v[172:175],v[156:159],a[236:239]\n"
"ds_read_b64_tr_b16 v[228:229],v99 offset:16384\n"
"v_mfma_f32_16x16x32_bf16 a[204:207],v[172:175],v[152:155],a[204:207]\n"
"ds_read_b64_tr_b16 v[230:231],v99 offset:17408\n"
"s_waitcnt lgkmcnt(10)\n"
"v_mfma_f32_16x16x32_bf16 a[208:211],v[176:179],v[152:155],a[208:211]\n"
"ds_read_b64_tr_b16 v[232:233],v100 offset:16384\n"
"v_mfma_f32_16x16x32_bf16 a[240:243],v[176:179],v[156:159],a[240:243]\n"
"ds_read_b64_tr_b16 v[234:235],v100 offset:17408\n"
"v_mfma_f32_16x16x32_bf16 a[244:247],v[180:183],v[156:159],a[244:247]\n"
"ds_read_b64_tr_b16 v[236:237],v101 offset:16384\n"
"v_mfma_f32_16x16x32_bf16 a[212:215],v[180:183],v[152:155],a[212:215]\n"
"ds_read_b64_tr_b16 v[238:239],v101 offset:17408\n"
"s_waitcnt lgkmcnt(10)\n"
"v_mfma_f32_16x16x32_bf16 a[216:219],v[184:187],v[152:155],a[216:219]\n"
"ds_read_b64_tr_b16 v[240:241],v102 offset:16384\n"
"v_mfma_f32_16x16x32_bf16 a[248:251],v[184:187],v[156:159],a[248:251]\n"
"ds_read_b64_tr_b16 v[242:243],v102 offset:17408\n"
"v_mfma_f32_16x16x32_bf16 a[252:255],v[188:191],v[156:159],a[252:255]\n"
"ds_read_b64_tr_b16 v[244:245],v103 offset:16384\n"
"v_mfma_f32_16x16x32_bf16 a[220:223],v[188:191],v[152:155],a[220:223]\n"
"ds_read_b64_tr_b16 v[246:247],v103 offset:17408\n"
"s_waitcnt lgkmcnt(8)\n"
"v_mfma_f32_16x16x32_bf16 a[0:3],v[160:163],v[128:131],a[0:3]\n"
"ds_read_b64_tr_b16 v[248:249],v104 offset:16384\n"
"v_mfma_f32_16x16x32_bf16 a[0:3],v[224:227],v[192:195],a[0:3]\n"
"ds_read_b64_tr_b16 v[250:251],v104 offset:17408\n"
"v_mfma_f32_16x16x32_bf16 a[4:7],v[228:231],v[192:195],a[4:7]\n"
"ds_read_b64_tr_b16 v[252:253],v105 offset:16384\n"
"v_mfma_f32_16x16x32_bf16 a[4:7],v[164:167],v[128:131],a[4:7]\n"
"ds_read_b64_tr_b16 v[254:255],v105 offset:17408\n"
"s_waitcnt lgkmcnt(8)\n"
"v_mfma_f32_16x16x32_bf16 a[8:11],v[168:171],v[128:131],a[8:11]\n"
"ds_read_b128 v[132:135],v80 offset:34816\n"
"v_mfma_f32_16x16x32_bf16 a[8:11],v[232:235],v[192:195],a[8:11]\n"
"ds_read_b128 v[196:199],v81 offset:34816\n"
"v_mfma_f32_16x16x32_bf16 a[12:15],v[236:239],v[192:195],a[12:15]\n"
"ds_read_b128 v[136:139],v80 offset:36864\n"
"v_mfma_f32_16x16x32_bf16 a[12:15],v[172:175],v[128:131],a[12:15]\n"
"ds_read_b128 v[200:203],v81 offset:36864\n"
"s_waitcnt lgkmcnt(8)\n"
"v_mfma_f32_16x16x32_bf16 a[16:19],v[176:179],v[128:131],a[16:19]\n"
"s_and_b32 s82,s52,1\n"
"ds_read_b128 v[140:143],v80 offset:38912\n"
"s_lshl_b32 s82,s82,13\n"
"v_mfma_f32_16x16x32_bf16 a[16:19],v[240:243],v[192:195],a[16:19]\n"
"ds_read_b128 v[204:207],v81 offset:38912\n"
"v_mfma_f32_16x16x32_bf16 a[20:23],v[244:247],v[192:195],a[20:23]\n"
"ds_read_b128 v[144:147],v80 offset:40960\n"
"v_mfma_f32_16x16x32_bf16 a[20:23],v[180:183],v[128:131],a[20:23]\n"
"ds_read_b128 v[208:211],v81 offset:40960\n"
"s_waitcnt lgkmcnt(8)\n"
"v_mfma_f32_16x16x32_bf16 a[24:27],v[184:187],v[128:131],a[24:27]\n"
"ds_read_b128 v[148:151],v80 offset:43008\n"
"v_mfma_f32_16x16x32_bf16 a[24:27],v[248:251],v[192:195],a[24:27]\n"
"ds_read_b128 v[212:215],v81 offset:43008\n"
"v_subrev_u32 v98,s82,v98\n"
"v_mfma_f32_16x16x32_bf16 a[28:31],v[252:255],v[192:195],a[28:31]\n"
"ds_read_b128 v[216:219],v81 offset:45056\n"
"v_add_u32 v96,s78,v97\n"
"v_mfma_f32_16x16x32_bf16 a[28:31],v[188:191],v[128:131],a[28:31]\n"
"ds_read_b128 v[220:223],v81 offset:47104\n"
"s_waitcnt lgkmcnt(10)\n"
"v_mfma_f32_16x16x32_bf16 a[32:35],v[160:163],v[132:135],a[32:35]\n"
"v_add_u32 v97,65536,v96\n"
"v_mfma_f32_16x16x32_bf16 a[32:35],v[224:227],v[196:199],a[32:35]\n"
"v_subrev_u32 v99,s82,v99\n"
"v_mfma_f32_16x16x32_bf16 a[36:39],v[228:231],v[196:199],a[36:39]\n"
"v_mfma_f32_16x16x32_bf16 a[36:39],v[164:167],v[132:135],a[36:39]\n"
"v_mfma_f32_16x16x32_bf16 a[40:43],v[168:171],v[132:135],a[40:43]\n"
"s_waitcnt lgkmcnt(0)\n"
"s_barrier\n"
"v_mfma_f32_16x16x32_bf16 a[40:43],v[232:235],v[196:199],a[40:43]\n"
"v_subrev_u32 v100,s82,v100\n"
"s_add_u32 m0,s85,98304\n"
"v_mfma_f32_16x16x32_bf16 a[44:47],v[236:239],v[196:199],a[44:47]\n"
"buffer_load_dwordx4 v110,s[28:31],s86 offen lds\n"
"v_mfma_f32_16x16x32_bf16 a[44:47],v[172:175],v[132:135],a[44:47]\n"
"v_mfma_f32_16x16x32_bf16 a[48:51],v[176:179],v[132:135],a[48:51]\n"
"v_mfma_f32_16x16x32_bf16 a[48:51],v[240:243],v[196:199],a[48:51]\n"
"v_subrev_u32 v101,s82,v101\n"
"s_add_u32 m0,s85,99328\n"
"v_mfma_f32_16x16x32_bf16 a[52:55],v[244:247],v[196:199],a[52:55]\n"
"buffer_load_dwordx4 v111,s[28:31],s86 offen lds\n"
"v_mfma_f32_16x16x32_bf16 a[52:55],v[180:183],v[132:135],a[52:55]\n"
"s_xor_b32 s86,s86,262144\n"
"v_mfma_f32_16x16x32_bf16 a[56:59],v[184:187],v[132:135],a[56:59]\n"
"v_mfma_f32_16x16x32_bf16 a[56:59],v[248:251],v[196:199],a[56:59]\n"
"v_subrev_u32 v102,s82,v102\n"
"s_add_u32 m0,s78,32768\n"
"v_mfma_f32_16x16x32_bf16 a[60:63],v[252:255],v[196:199],a[60:63]\n"
"buffer_load_dwordx4 v112,s[24:27],s76 offen offset:0 sc1 lds\n"
"v_mfma_f32_16x16x32_bf16 a[60:63],v[188:191],v[132:135],a[60:63]\n"
"v_mfma_f32_16x16x32_bf16 a[64:67],v[160:163],v[136:139],a[64:67]\n"
"v_mfma_f32_16x16x32_bf16 a[64:67],v[224:227],v[200:203],a[64:67]\n"
"v_subrev_u32 v103,s82,v103\n"
"v_mfma_f32_16x16x32_bf16 a[68:71],v[228:231],v[200:203],a[68:71]\n"
"buffer_load_dwordx4 v112,s[24:27],s76 offen offset:1024 sc1 lds\n"
"v_mfma_f32_16x16x32_bf16 a[68:71],v[164:167],v[136:139],a[68:71]\n"
"v_mfma_f32_16x16x32_bf16 a[72:75],v[168:171],v[136:139],a[72:75]\n"
"v_accvgpr_read_b32 v116,a0\n"
"v_accvgpr_read_b32 v117,a1\n"
"v_mfma_f32_16x16x32_bf16 a[72:75],v[232:235],v[200:203],a[72:75]\n"
"v_subrev_u32 v104,s82,v104\n"
"v_accvgpr_read_b32 v118,a2\n"
"v_mfma_f32_16x16x32_bf16 a[76:79],v[236:239],v[200:203],a[76:79]\n"
"buffer_load_dwordx4 v112,s[24:27],s76 offen offset:2048 sc1 lds\n"
"v_mfma_f32_16x16x32_bf16 a[76:79],v[172:175],v[136:139],a[76:79]\n"
"v_accvgpr_read_b32 v119,a3\n"
"v_accvgpr_read_b32 v120,a4\n"
"v_mfma_f32_16x16x32_bf16 a[80:83],v[176:179],v[136:139],a[80:83]\n"
"v_accvgpr_read_b32 v121,a5\n"
"v_accvgpr_read_b32 v122,a6\n"
"v_mfma_f32_16x16x32_bf16 a[80:83],v[240:243],v[200:203],a[80:83]\n"
"v_subrev_u32 v105,s82,v105\n"
"v_accvgpr_read_b32 v123,a7\n"
"v_mfma_f32_16x16x32_bf16 a[84:87],v[244:247],v[200:203],a[84:87]\n"
"buffer_load_dwordx4 v112,s[24:27],s76 offen offset:3072 sc1 lds\n"
"v_mfma_f32_16x16x32_bf16 a[84:87],v[180:183],v[136:139],a[84:87]\n"
"s_xor_b32 s76,s76,16384\n"
"v_cvt_pk_bf16_f32 v116,v116,v117\n"
"v_cvt_pk_bf16_f32 v117,v118,v119\n"
"v_mfma_f32_16x16x32_bf16 a[88:91],v[184:187],v[136:139],a[88:91]\n"
"v_cvt_pk_bf16_f32 v118,v120,v121\n"
"v_cvt_pk_bf16_f32 v119,v122,v123\n"
"v_mfma_f32_16x16x32_bf16 a[88:91],v[248:251],v[200:203],a[88:91]\n"
"buffer_store_dwordx4 v[116:119],v112,s[24:27],s56 offen offset:0 sc1\n"
"v_mfma_f32_16x16x32_bf16 a[92:95],v[252:255],v[200:203],a[92:95]\n"
"s_nop 0\n"
"v_accvgpr_read_b32 v116,a8\n"
"v_accvgpr_read_b32 v117,a9\n"
"v_accvgpr_read_b32 v118,a10\n"
"v_mfma_f32_16x16x32_bf16 a[92:95],v[188:191],v[136:139],a[92:95]\n"
"s_waitcnt vmcnt(20)\n"
"v_accvgpr_read_b32 v119,a11\n"
"v_accvgpr_read_b32 v120,a12\n"
"s_barrier\n"
"v_mfma_f32_16x16x32_bf16 a[96:99],v[160:163],v[140:143],a[96:99]\n"
"ds_read_b128 v[0:3],v97 offset:0\n"
"v_accvgpr_read_b32 v121,a13\n"
"v_mfma_f32_16x16x32_bf16 a[96:99],v[224:227],v[204:207],a[96:99]\n"
"ds_read_b128 v[8:11],v97 offset:1024\n"
"v_accvgpr_read_b32 v122,a14\n"
"v_mfma_f32_16x16x32_bf16 a[100:103],v[228:231],v[204:207],a[100:103]\n"
"ds_read_b128 v[16:19],v97 offset:2048\n"
"v_accvgpr_read_b32 v123,a15\n"
"v_mfma_f32_16x16x32_bf16 a[100:103],v[164:167],v[140:143],a[100:103]\n"
"ds_read_b128 v[24:27],v97 offset:3072\n"
"v_cvt_pk_bf16_f32 v116,v116,v117\n"
"v_mfma_f32_16x16x32_bf16 a[104:107],v[168:171],v[140:143],a[104:107]\n"
"ds_read_b128 v[4:7],v97 offset:16384\n"
"v_cvt_pk_bf16_f32 v117,v118,v119\n"
"v_mfma_f32_16x16x32_bf16 a[104:107],v[232:235],v[204:207],a[104:107]\n"
"ds_read_b128 v[12:15],v97 offset:17408\n"
"v_cvt_pk_bf16_f32 v118,v120,v121\n"
"v_mfma_f32_16x16x32_bf16 a[108:111],v[236:239],v[204:207],a[108:111]\n"
"ds_read_b128 v[20:23],v97 offset:18432\n"
"v_cvt_pk_bf16_f32 v119,v122,v123\n"
"v_mfma_f32_16x16x32_bf16 a[108:111],v[172:175],v[140:143],a[108:111]\n"
"ds_read_b128 v[28:31],v97 offset:19456\n"
"buffer_store_dwordx4 v[116:119],v112,s[24:27],s56 offen offset:1024 sc1\n"
"v_mfma_f32_16x16x32_bf16 a[112:115],v[176:179],v[140:143],a[112:115]\n"
"ds_read_b64_tr_b16 v[32:33],v98 offset:49152\n"
"v_accvgpr_read_b32 v116,a16\n"
"v_mfma_f32_16x16x32_bf16 a[112:115],v[240:243],v[204:207],a[112:115]\n"
"ds_read_b64_tr_b16 v[34:35],v98 offset:50176\n"
"v_accvgpr_read_b32 v117,a17\n"
"v_mfma_f32_16x16x32_bf16 a[116:119],v[244:247],v[204:207],a[116:119]\n"
"ds_read_b64_tr_b16 v[36:37],v98 offset:57344\n"
"v_accvgpr_read_b32 v118,a18\n"
"v_mfma_f32_16x16x32_bf16 a[116:119],v[180:183],v[140:143],a[116:119]\n"
"ds_read_b64_tr_b16 v[38:39],v98 offset:58368\n"
"v_accvgpr_read_b32 v119,a19\n"
"v_mfma_f32_16x16x32_bf16 a[120:123],v[184:187],v[140:143],a[120:123]\n"
"ds_read_b64_tr_b16 v[40:41],v99 offset:49152\n"
"v_accvgpr_read_b32 v120,a20\n"
"v_mfma_f32_16x16x32_bf16 a[120:123],v[248:251],v[204:207],a[120:123]\n"
"ds_read_b64_tr_b16 v[42:43],v99 offset:50176\n"
"v_accvgpr_read_b32 v121,a21\n"
"v_mfma_f32_16x16x32_bf16 a[124:127],v[252:255],v[204:207],a[124:127]\n"
"ds_read_b64_tr_b16 v[44:45],v99 offset:57344\n"
"v_accvgpr_read_b32 v122,a22\n"
"v_mfma_f32_16x16x32_bf16 a[124:127],v[188:191],v[140:143],a[124:127]\n"
"ds_read_b64_tr_b16 v[46:47],v99 offset:58368\n"
"v_accvgpr_read_b32 v123,a23\n"
"v_mfma_f32_16x16x32_bf16 a[128:131],v[160:163],v[144:147],a[128:131]\n"
"ds_read_b64_tr_b16 v[48:49],v100 offset:49152\n"
"v_cvt_pk_bf16_f32 v116,v116,v117\n"
"v_mfma_f32_16x16x32_bf16 a[128:131],v[224:227],v[208:211],a[128:131]\n"
"ds_read_b64_tr_b16 v[50:51],v100 offset:50176\n"
"v_cvt_pk_bf16_f32 v117,v118,v119\n"
"v_mfma_f32_16x16x32_bf16 a[132:135],v[228:231],v[208:211],a[132:135]\n"
"ds_read_b64_tr_b16 v[52:53],v100 offset:57344\n"
"v_cvt_pk_bf16_f32 v118,v120,v121\n"
"v_mfma_f32_16x16x32_bf16 a[132:135],v[164:167],v[144:147],a[132:135]\n"
"ds_read_b64_tr_b16 v[54:55],v100 offset:58368\n"
"v_cvt_pk_bf16_f32 v119,v122,v123\n"
"v_mfma_f32_16x16x32_bf16 a[136:139],v[168:171],v[144:147],a[136:139]\n"
"ds_read_b64_tr_b16 v[56:57],v101 offset:49152\n"
"buffer_store_dwordx4 v[116:119],v112,s[24:27],s56 offen offset:2048 sc1\n"
"v_mfma_f32_16x16x32_bf16 a[136:139],v[232:235],v[208:211],a[136:139]\n"
"ds_read_b64_tr_b16 v[58:59],v101 offset:50176\n"
"v_accvgpr_read_b32 v116,a24\n"
"v_mfma_f32_16x16x32_bf16 a[140:143],v[236:239],v[208:211],a[140:143]\n"
"ds_read_b64_tr_b16 v[60:61],v101 offset:57344\n"
"v_accvgpr_read_b32 v117,a25\n"
"v_mfma_f32_16x16x32_bf16 a[140:143],v[172:175],v[144:147],a[140:143]\n"
"ds_read_b64_tr_b16 v[62:63],v101 offset:58368\n"
"v_accvgpr_read_b32 v118,a26\n"
"v_mfma_f32_16x16x32_bf16 a[144:147],v[176:179],v[144:147],a[144:147]\n"
"ds_read_b64_tr_b16 v[64:65],v102 offset:49152\n"
"v_accvgpr_read_b32 v119,a27\n"
"v_mfma_f32_16x16x32_bf16 a[144:147],v[240:243],v[208:211],a[144:147]\n"
"ds_read_b64_tr_b16 v[66:67],v102 offset:50176\n"
"v_accvgpr_read_b32 v120,a28\n"
"v_mfma_f32_16x16x32_bf16 a[148:151],v[244:247],v[208:211],a[148:151]\n"
"ds_read_b64_tr_b16 v[68:69],v102 offset:57344\n"
"v_accvgpr_read_b32 v121,a29\n"
"v_mfma_f32_16x16x32_bf16 a[148:151],v[180:183],v[144:147],a[148:151]\n"
"ds_read_b64_tr_b16 v[70:71],v102 offset:58368\n"
"v_accvgpr_read_b32 v122,a30\n"
"v_mfma_f32_16x16x32_bf16 a[152:155],v[184:187],v[144:147],a[152:155]\n"
"ds_read_b64_tr_b16 v[72:73],v103 offset:49152\n"
"v_accvgpr_read_b32 v123,a31\n"
"v_mfma_f32_16x16x32_bf16 a[152:155],v[248:251],v[208:211],a[152:155]\n"
"ds_read_b64_tr_b16 v[74:75],v103 offset:50176\n"
"v_cvt_pk_bf16_f32 v116,v116,v117\n"
"v_mfma_f32_16x16x32_bf16 a[156:159],v[252:255],v[208:211],a[156:159]\n"
"ds_read_b64_tr_b16 v[76:77],v103 offset:57344\n"
"v_cvt_pk_bf16_f32 v117,v118,v119\n"
"v_mfma_f32_16x16x32_bf16 a[156:159],v[188:191],v[144:147],a[156:159]\n"
"ds_read_b64_tr_b16 v[78:79],v103 offset:58368\n"
"v_cvt_pk_bf16_f32 v118,v120,v121\n"
"v_mfma_f32_16x16x32_bf16 a[160:163],v[160:163],v[148:151],a[160:163]\n"
"ds_read_b64_tr_b16 v[80:81],v104 offset:49152\n"
"v_cvt_pk_bf16_f32 v119,v122,v123\n"
"v_mfma_f32_16x16x32_bf16 a[160:163],v[224:227],v[212:215],a[160:163]\n"
"ds_read_b64_tr_b16 v[82:83],v104 offset:50176\n"
"buffer_store_dwordx4 v[116:119],v112,s[24:27],s56 offen offset:3072 sc1\n"
"v_mfma_f32_16x16x32_bf16 a[164:167],v[228:231],v[212:215],a[164:167]\n"
"ds_read_b64_tr_b16 v[84:85],v104 offset:57344\n"
"v_mfma_f32_16x16x32_bf16 a[164:167],v[164:167],v[148:151],a[164:167]\n"
"ds_read_b64_tr_b16 v[86:87],v104 offset:58368\n"
"v_mfma_f32_16x16x32_bf16 a[168:171],v[168:171],v[148:151],a[168:171]\n"
"ds_read_b64_tr_b16 v[88:89],v105 offset:49152\n"
"v_mfma_f32_16x16x32_bf16 a[168:171],v[232:235],v[212:215],a[168:171]\n"
"ds_read_b64_tr_b16 v[90:91],v105 offset:50176\n"
"v_mfma_f32_16x16x32_bf16 a[172:175],v[236:239],v[212:215],a[172:175]\n"
"ds_read_b64_tr_b16 v[92:93],v105 offset:57344\n"
"v_mfma_f32_16x16x32_bf16 a[172:175],v[172:175],v[148:151],a[172:175]\n"
"ds_read_b64_tr_b16 v[94:95],v105 offset:58368\n"
"v_mfma_f32_16x16x32_bf16 a[176:179],v[176:179],v[148:151],a[176:179]\n"
"s_add_u32 m0,s85,106496\n"
"v_mfma_f32_16x16x32_bf16 a[176:179],v[240:243],v[212:215],a[176:179]\n"
"buffer_load_dwordx4 v110,s[28:31],s86 offen lds\n"
"v_mfma_f32_16x16x32_bf16 a[180:183],v[244:247],v[212:215],a[180:183]\n"
"v_mfma_f32_16x16x32_bf16 a[180:183],v[180:183],v[148:151],a[180:183]\n"
"s_add_u32 m0,s85,107520\n"
"v_mfma_f32_16x16x32_bf16 a[184:187],v[184:187],v[148:151],a[184:187]\n"
"buffer_load_dwordx4 v111,s[28:31],s86 offen lds\n"
"v_mfma_f32_16x16x32_bf16 a[184:187],v[248:251],v[212:215],a[184:187]\n"
"s_xor_b32 s86,s86,786432\n"
"v_mfma_f32_16x16x32_bf16 a[188:191],v[252:255],v[212:215],a[188:191]\n"
"s_add_u32 m0,s78,49152\n"
"v_mfma_f32_16x16x32_bf16 a[188:191],v[188:191],v[148:151],a[188:191]\n"
"buffer_load_dwordx4 v112,s[24:27],s76 offen offset:0 sc1 lds\n"
"v_mfma_f32_16x16x32_bf16 a[192:195],v[224:227],v[216:219],a[192:195]\n"
"v_mfma_f32_16x16x32_bf16 a[224:227],v[224:227],v[220:223],a[224:227]\n"
"v_mfma_f32_16x16x32_bf16 a[228:231],v[228:231],v[220:223],a[228:231]\n"
"buffer_load_dwordx4 v112,s[24:27],s76 offen offset:1024 sc1 lds\n"
"v_mfma_f32_16x16x32_bf16 a[196:199],v[228:231],v[216:219],a[196:199]\n"
"v_mfma_f32_16x16x32_bf16 a[200:203],v[232:235],v[216:219],a[200:203]\n"
"v_mfma_f32_16x16x32_bf16 a[232:235],v[232:235],v[220:223],a[232:235]\n"
"buffer_load_dwordx4 v112,s[24:27],s76 offen offset:2048 sc1 lds\n"
"v_mfma_f32_16x16x32_bf16 a[236:239],v[236:239],v[220:223],a[236:239]\n"
"v_mfma_f32_16x16x32_bf16 a[204:207],v[236:239],v[216:219],a[204:207]\n"
"buffer_load_dwordx4 v112,s[24:27],s76 offen offset:3072 sc1 lds\n"
"v_mfma_f32_16x16x32_bf16 a[208:211],v[240:243],v[216:219],a[208:211]\n"
"s_xor_b32 s76,s76,49152\n"
"v_mfma_f32_16x16x32_bf16 a[240:243],v[240:243],v[220:223],a[240:243]\n"
"v_mfma_f32_16x16x32_bf16 a[244:247],v[244:247],v[220:223],a[244:247]\n"
"v_mfma_f32_16x16x32_bf16 a[212:215],v[244:247],v[216:219],a[212:215]\n"
"v_mfma_f32_16x16x32_bf16 a[216:219],v[248:251],v[216:219],a[216:219]\n"
"v_mfma_f32_16x16x32_bf16 a[248:251],v[248:251],v[220:223],a[248:251]\n"
"v_mfma_f32_16x16x32_bf16 a[252:255],v[252:255],v[220:223],a[252:255]\n"
"v_mfma_f32_16x16x32_bf16 a[220:223],v[252:255],v[216:219],a[220:223]\n"
"s_branch .Lfk_ph2\n"
".Lfk_v:\n"
"s_waitcnt vmcnt(11)\n"
"s_barrier\n"
"ds_read_b128 v[152:155],v80 offset:12288\n"
"ds_read_b128 v[156:159],v80 offset:14336\n"
"ds_read_b64_tr_b16 v[160:161],v98 offset:49152\n"
"ds_read_b64_tr_b16 v[162:163],v98 offset:50176\n"
"ds_read_b64_tr_b16 v[164:165],v99 offset:49152\n"
"ds_read_b64_tr_b16 v[166:167],v99 offset:50176\n"
"ds_read_b64_tr_b16 v[168:169],v100 offset:49152\n"
"ds_read_b64_tr_b16 v[170:171],v100 offset:50176\n"
"ds_read_b64_tr_b16 v[172:173],v101 offset:49152\n"
"ds_read_b64_tr_b16 v[174:175],v101 offset:50176\n"
"ds_read_b64_tr_b16 v[176:177],v102 offset:49152\n"
"ds_read_b64_tr_b16 v[178:179],v102 offset:50176\n"
"ds_read_b64_tr_b16 v[180:181],v103 offset:49152\n"
"ds_read_b64_tr_b16 v[182:183],v103 offset:50176\n"
"ds_read_b64_tr_b16 v[184:185],v104 offset:49152\n"
"ds_read_b64_tr_b16 v[186:187],v104 offset:50176\n"
"ds_read_b64_tr_b16 v[188:189],v105 offset:49152\n"
"ds_read_b64_tr_b16 v[190:191],v105 offset:50176\n"
"s_waitcnt lgkmcnt(12)\n"
"v_mfma_f32_16x16x32_bf16 a[192:195],v[152:155],v[160:163],0\n"
"s_and_b32 s91,s89,7\n"
"s_mov_b32 s76,128\n"
"s_add_u32 m0,s79,32768\n"
"v_mfma_f32_16x16x32_bf16 a[196:199],v[156:159],v[160:163],0\n"
"buffer_load_dwordx4 v106,s[16:19],s76 offen offset:0 lds\n"
"v_mfma_f32_16x16x32_bf16 a[204:207],v[156:159],v[164:167],0\n"
"s_lshr_b32 s97,s89,3\n"
"s_lshl2_add_u32 s90,s88,s97\n"
"v_mfma_f32_16x16x32_bf16 a[200:203],v[152:155],v[164:167],0\n"
"buffer_load_dwordx4 v107,s[16:19],s76 offen offset:1024 lds\n"
"s_waitcnt lgkmcnt(8)\n"
"v_mfma_f32_16x16x32_bf16 a[208:211],v[152:155],v[168:171],0\n"
"s_lshl_b32 vcc_lo,s90,8\n"
"s_add_u32 s28,s28,vcc_lo\n"
"s_addc_u32 s29,s29,0\n"
"v_mfma_f32_16x16x32_bf16 a[212:215],v[156:159],v[168:171],0\n"
"buffer_load_dwordx4 v108,s[16:19],s76 offen offset:2048 lds\n"
"v_mfma_f32_16x16x32_bf16 a[220:223],v[156:159],v[172:175],0\n"
"s_sub_u32 s30,8388608,vcc_lo\n"
"s_mov_b32 s31,0x20000\n"
"buffer_load_dwordx4 v109,s[16:19],s76 offen offset:3072 lds\n"
"v_mfma_f32_16x16x32_bf16 a[216:219],v[152:155],v[172:175],0\n"
"s_waitcnt vmcnt(9)\n"
"s_barrier\n"
"s_waitcnt lgkmcnt(4)\n"
"v_mfma_f32_16x16x32_bf16 a[224:227],v[152:155],v[176:179],0\n"
"ds_read_b128 v[128:131],v80 offset:0\n"
"ds_read_b128 v[192:195],v81 offset:0\n"
"v_mfma_f32_16x16x32_bf16 a[228:231],v[156:159],v[176:179],0\n"
"ds_read_b64_tr_b16 v[224:225],v98 offset:32768\n"
"ds_read_b64_tr_b16 v[226:227],v98 offset:33792\n"
"v_mfma_f32_16x16x32_bf16 a[236:239],v[156:159],v[180:183],0\n"
"ds_read_b64_tr_b16 v[228:229],v99 offset:32768\n"
"ds_read_b64_tr_b16 v[230:231],v99 offset:33792\n"
"v_mfma_f32_16x16x32_bf16 a[232:235],v[152:155],v[180:183],0\n"
"ds_read_b64_tr_b16 v[232:233],v100 offset:32768\n"
"ds_read_b64_tr_b16 v[234:235],v100 offset:33792\n"
"s_waitcnt lgkmcnt(8)\n"
"v_mfma_f32_16x16x32_bf16 a[240:243],v[152:155],v[184:187],0\n"
"ds_read_b64_tr_b16 v[236:237],v101 offset:32768\n"
"ds_read_b64_tr_b16 v[238:239],v101 offset:33792\n"
"v_mfma_f32_16x16x32_bf16 a[244:247],v[156:159],v[184:187],0\n"
"ds_read_b64_tr_b16 v[240:241],v102 offset:32768\n"
"ds_read_b64_tr_b16 v[242:243],v102 offset:33792\n"
"v_mfma_f32_16x16x32_bf16 a[252:255],v[156:159],v[188:191],0\n"
"ds_read_b64_tr_b16 v[244:245],v103 offset:32768\n"
"ds_read_b64_tr_b16 v[246:247],v103 offset:33792\n"
"v_mfma_f32_16x16x32_bf16 a[248:251],v[152:155],v[188:191],0\n"
"ds_read_b64_tr_b16 v[248:249],v104 offset:32768\n"
"ds_read_b64_tr_b16 v[250:251],v104 offset:33792\n"
"s_waitcnt lgkmcnt(10)\n"
"v_mfma_f32_16x16x32_bf16 a[0:3],v[128:131],v[160:163],0\n"
"ds_read_b64_tr_b16 v[252:253],v105 offset:32768\n"
"ds_read_b64_tr_b16 v[254:255],v105 offset:33792\n"
"v_mfma_f32_16x16x32_bf16 a[0:3],v[192:195],v[224:227],a[0:3]\n"
"ds_read_b128 v[132:135],v80 offset:2048\n"
"ds_read_b128 v[196:199],v81 offset:2048\n"
"v_mfma_f32_16x16x32_bf16 a[8:11],v[192:195],v[228:231],0\n"
"ds_read_b128 v[216:219],v81 offset:12288\n"
"ds_read_b128 v[220:223],v81 offset:14336\n"
"v_mfma_f32_16x16x32_bf16 a[8:11],v[128:131],v[164:167],a[8:11]\n"
"s_waitcnt lgkmcnt(12)\n"
"s_mov_b32 s76,262272\n"
"s_add_u32 m0,s79,36864\n"
"v_mfma_f32_16x16x32_bf16 a[16:19],v[128:131],v[168:171],0\n"
"buffer_load_dwordx4 v106,s[16:19],s76 offen offset:0 lds\n"
"v_mfma_f32_16x16x32_bf16 a[16:19],v[192:195],v[232:235],a[16:19]\n"
"buffer_load_dwordx4 v107,s[16:19],s76 offen offset:1024 lds\n"
"v_mfma_f32_16x16x32_bf16 a[24:27],v[192:195],v[236:239],0\n"
"buffer_load_dwordx4 v108,s[16:19],s76 offen offset:2048 lds\n"
"buffer_load_dwordx4 v109,s[16:19],s76 offen offset:3072 lds\n"
"v_mfma_f32_16x16x32_bf16 a[24:27],v[128:131],v[172:175],a[24:27]\n"
"s_waitcnt lgkmcnt(8)\n"
"s_mov_b32 s77,524288\n"
"s_add_u32 m0,s78,98304\n"
"v_mfma_f32_16x16x32_bf16 a[32:35],v[128:131],v[176:179],0\n"
"buffer_load_dwordx4 v110,s[20:23],s77 offen offset:0 lds\n"
"v_mfma_f32_16x16x32_bf16 a[32:35],v[192:195],v[240:243],a[32:35]\n"
"buffer_load_dwordx4 v111,s[20:23],s77 offen offset:1024 lds\n"
"v_mfma_f32_16x16x32_bf16 a[40:43],v[192:195],v[244:247],0\n"
"buffer_load_dwordx4 v113,s[20:23],s77 offen offset:2048 lds\n"
"buffer_load_dwordx4 v114,s[20:23],s77 offen offset:3072 lds\n"
"v_mfma_f32_16x16x32_bf16 a[40:43],v[128:131],v[180:183],a[40:43]\n"
"s_waitcnt lgkmcnt(4)\n"
"s_mov_b32 s77,786432\n"
"s_add_u32 m0,s78,114688\n"
"v_mfma_f32_16x16x32_bf16 a[48:51],v[128:131],v[184:187],0\n"
"buffer_load_dwordx4 v110,s[20:23],s77 offen offset:0 lds\n"
"v_mfma_f32_16x16x32_bf16 a[48:51],v[192:195],v[248:251],a[48:51]\n"
"buffer_load_dwordx4 v111,s[20:23],s77 offen offset:1024 lds\n"
"v_mfma_f32_16x16x32_bf16 a[56:59],v[192:195],v[252:255],0\n"
"buffer_load_dwordx4 v113,s[20:23],s77 offen offset:2048 lds\n"
"buffer_load_dwordx4 v114,s[20:23],s77 offen offset:3072 lds\n"
"v_mfma_f32_16x16x32_bf16 a[56:59],v[128:131],v[188:191],a[56:59]\n"
"s_waitcnt lgkmcnt(2)\n"
"s_mov_b32 s76,256\n"
"s_add_u32 m0,s79,65536\n"
"v_mfma_f32_16x16x32_bf16 a[4:7],v[132:135],v[160:163],0\n"
"buffer_load_dwordx4 v106,s[16:19],s76 offen offset:0 lds\n"
"v_mfma_f32_16x16x32_bf16 a[4:7],v[196:199],v[224:227],a[4:7]\n"
"buffer_load_dwordx4 v107,s[16:19],s76 offen offset:1024 lds\n"
"v_mfma_f32_16x16x32_bf16 a[12:15],v[196:199],v[228:231],0\n"
"buffer_load_dwordx4 v108,s[16:19],s76 offen offset:2048 lds\n"
"buffer_load_dwordx4 v109,s[16:19],s76 offen offset:3072 lds\n"
"v_mfma_f32_16x16x32_bf16 a[12:15],v[132:135],v[164:167],a[12:15]\n"
"s_waitcnt vmcnt(21)\n"
"s_barrier\n"
"v_mfma_f32_16x16x32_bf16 a[20:23],v[132:135],v[168:171],0\n"
"ds_read_b128 v[136:139],v80 offset:4096\n"
"ds_read_b128 v[200:203],v81 offset:4096\n"
"v_mfma_f32_16x16x32_bf16 a[20:23],v[196:199],v[232:235],a[20:23]\n"
"ds_read_b128 v[140:143],v80 offset:6144\n"
"ds_read_b128 v[204:207],v81 offset:6144\n"
"v_mfma_f32_16x16x32_bf16 a[28:31],v[196:199],v[236:239],0\n"
"ds_read_b128 v[144:147],v80 offset:8192\n"
"ds_read_b128 v[208:211],v81 offset:8192\n"
"v_mfma_f32_16x16x32_bf16 a[28:31],v[132:135],v[172:175],a[28:31]\n"
"ds_read_b128 v[148:151],v80 offset:10240\n"
"ds_read_b128 v[212:215],v81 offset:10240\n"
"v_mfma_f32_16x16x32_bf16 a[36:39],v[132:135],v[176:179],0\n"
"s_mov_b32 s60,s91\n"
"s_mov_b32 s76,262400\n"
"s_add_u32 m0,s79,69632\n"
"v_mfma_f32_16x16x32_bf16 a[36:39],v[196:199],v[240:243],a[36:39]\n"
"buffer_load_dwordx4 v106,s[16:19],s76 offen offset:0 lds\n"
"v_mfma_f32_16x16x32_bf16 a[44:47],v[196:199],v[244:247],0\n"
"buffer_load_dwordx4 v107,s[16:19],s76 offen offset:1024 lds\n"
"v_mfma_f32_16x16x32_bf16 a[44:47],v[132:135],v[180:183],a[44:47]\n"
"buffer_load_dwordx4 v108,s[16:19],s76 offen offset:2048 lds\n"
"v_mfma_f32_16x16x32_bf16 a[52:55],v[132:135],v[184:187],0\n"
"buffer_load_dwordx4 v109,s[16:19],s76 offen offset:3072 lds\n"
"v_mfma_f32_16x16x32_bf16 a[52:55],v[196:199],v[248:251],a[52:55]\n"
"s_sub_u32 s94,15,s91\n"
"s_waitcnt lgkmcnt(0)\n"
"s_barrier\n"
"v_mfma_f32_16x16x32_bf16 a[60:63],v[196:199],v[252:255],0\n"
"s_cmp_lt_u32 s52,2\n"
"s_cselect_b32 s95,s94,s91\n"
"s_mov_b32 s77,1048576\n"
"s_add_u32 m0,s78,147456\n"
"v_mfma_f32_16x16x32_bf16 a[60:63],v[132:135],v[188:191],a[60:63]\n"
"buffer_load_dwordx4 v110,s[20:23],s77 offen offset:0 lds\n"
"v_mfma_f32_16x16x32_bf16 a[64:67],v[136:139],v[160:163],0\n"
"s_lshr_b32 vcc_lo,s95,1\n"
"s_lshl_b32 s55,vcc_lo,19\n"
"v_mfma_f32_16x16x32_bf16 a[64:67],v[200:203],v[224:227],a[64:67]\n"
"s_lshl_b32 s58,vcc_lo,10\n"
"s_add_u32 s58,s58,148898816\n"
"v_mfma_f32_16x16x32_bf16 a[72:75],v[200:203],v[228:231],0\n"
"s_and_b32 vcc_lo,s95,1\n"
"s_lshl_b32 vcc_lo,vcc_lo,13\n"
"v_mfma_f32_16x16x32_bf16 a[72:75],v[136:139],v[164:167],a[72:75]\n"
"buffer_load_dwordx4 v111,s[20:23],s77 offen offset:1024 lds\n"
"v_mfma_f32_16x16x32_bf16 a[80:83],v[136:139],v[168:171],0\n"
"s_and_b32 vcc_hi,s78,4096\n"
"s_add_u32 s55,s55,vcc_lo\n"
"v_mfma_f32_16x16x32_bf16 a[80:83],v[200:203],v[232:235],a[80:83]\n"
"s_add_u32 s55,s55,vcc_hi\n"
"s_and_b32 vcc_lo,s89,2\n"
"v_mfma_f32_16x16x32_bf16 a[88:91],v[200:203],v[236:239],0\n"
"s_lshl2_add_u32 vcc_lo,s88,vcc_lo\n"
"s_and_b32 vcc_hi,s52,1\n"
"v_mfma_f32_16x16x32_bf16 a[88:91],v[136:139],v[172:175],a[88:91]\n"
"buffer_load_dwordx4 v113,s[20:23],s77 offen offset:2048 lds\n"
"v_mfma_f32_16x16x32_bf16 a[96:99],v[136:139],v[176:179],0\n"
"s_add_u32 vcc_lo,vcc_lo,vcc_hi\n"
"s_lshr_b32 vcc_hi,s89,2\n"
"v_mfma_f32_16x16x32_bf16 a[96:99],v[200:203],v[240:243],a[96:99]\n"
"s_lshr_b32 s84,s52,1\n"
"s_lshl1_add_u32 vcc_hi,vcc_hi,s84\n"
"v_mfma_f32_16x16x32_bf16 a[104:107],v[200:203],v[244:247],0\n"
"s_mul_i32 s56,vcc_lo,1179648\n"
"s_lshl_b32 s84,s84,19\n"
"v_mfma_f32_16x16x32_bf16 a[104:107],v[136:139],v[180:183],a[104:107]\n"
"buffer_load_dwordx4 v114,s[20:23],s77 offen offset:3072 lds\n"
"v_mfma_f32_16x16x32_bf16 a[112:115],v[136:139],v[184:187],0\n"
"s_add_u32 s56,s56,s84\n"
"s_lshr_b32 s84,s89,2\n"
"v_mfma_f32_16x16x32_bf16 a[112:115],v[200:203],v[248:251],a[112:115]\n"
"s_lshl_b32 s84,s84,14\n"
"s_add_u32 s56,s56,s84\n"
"v_mfma_f32_16x16x32_bf16 a[120:123],v[200:203],v[252:255],0\n"
"s_lshl_b32 s84,s53,13\n"
"s_mov_b32 s77,1310720\n"
"s_add_u32 m0,s78,131072\n"
"v_mfma_f32_16x16x32_bf16 a[120:123],v[136:139],v[188:191],a[120:123]\n"
"buffer_load_dwordx4 v110,s[20:23],s77 offen offset:0 lds\n"
"v_mfma_f32_16x16x32_bf16 a[68:71],v[140:143],v[160:163],0\n"
"s_add_u32 s56,s56,s84\n"
"s_add_u32 s56,s56,4194304\n"
"v_mfma_f32_16x16x32_bf16 a[68:71],v[204:207],v[224:227],a[68:71]\n"
"s_lshl4_add_u32 vcc_hi,s53,vcc_hi\n"
"s_lshl_b32 vcc_lo,vcc_lo,8\n"
"v_mfma_f32_16x16x32_bf16 a[76:79],v[204:207],v[228:231],0\n"
"buffer_load_dwordx4 v111,s[20:23],s77 offen offset:1024 lds\n"
"v_mfma_f32_16x16x32_bf16 a[76:79],v[140:143],v[164:167],a[76:79]\n"
"s_lshl3_add_u32 s59,vcc_hi,vcc_lo\n"
"s_add_u32 s59,s59,148914176\n"
"v_mfma_f32_16x16x32_bf16 a[84:87],v[140:143],v[168:171],0\n"
"s_lshl_b32 s57,s92,3\n"
"s_add_u32 s57,s57,148898816\n"
"v_mfma_f32_16x16x32_bf16 a[84:87],v[204:207],v[232:235],a[84:87]\n"
"s_lshl_b32 s63,s90,8\n"
"s_add_u32 s63,s63,148914176\n"
"v_mfma_f32_16x16x32_bf16 a[92:95],v[204:207],v[236:239],0\n"
"buffer_load_dwordx4 v113,s[20:23],s77 offen offset:2048 lds\n"
"v_mfma_f32_16x16x32_bf16 a[92:95],v[140:143],v[172:175],a[92:95]\n"
"s_lshl_b32 s74,s92,11\n"
"s_add_u32 s74,s74,171966464\n"
"v_mfma_f32_16x16x32_bf16 a[100:103],v[140:143],v[176:179],0\n"
"s_mul_i32 s96,s90,1179648\n"
"s_add_u32 s96,s96,4194304\n"
"v_mfma_f32_16x16x32_bf16 a[100:103],v[204:207],v[240:243],a[100:103]\n"
"s_add_u32 s73,s96,s85\n"
"s_and_b32 vcc_hi,s78,4096\n"
"v_mfma_f32_16x16x32_bf16 a[108:111],v[204:207],v[244:247],0\n"
"buffer_load_dwordx4 v114,s[20:23],s77 offen offset:3072 lds\n"
"v_mfma_f32_16x16x32_bf16 a[108:111],v[140:143],v[180:183],a[108:111]\n"
"s_lshl_b32 vcc_hi,vcc_hi,1\n"
"s_add_u32 vcc_lo,s96,vcc_hi\n"
"v_mfma_f32_16x16x32_bf16 a[116:119],v[140:143],v[184:187],0\n"
"s_add_u32 s65,vcc_lo,524288\n"
"s_lshl_b32 s66,s91,16\n"
"v_mfma_f32_16x16x32_bf16 a[116:119],v[204:207],v[248:251],a[116:119]\n"
"s_sub_u32 s66,vcc_lo,s66\n"
"s_mov_b32 s76,384\n"
"s_add_u32 m0,s79,0\n"
"v_mfma_f32_16x16x32_bf16 a[124:127],v[204:207],v[252:255],0\n"
"buffer_load_dwordx4 v106,s[16:19],s76 offen offset:0 lds\n"
"v_mfma_f32_16x16x32_bf16 a[124:127],v[140:143],v[188:191],a[124:127]\n"
"s_add_u32 s66,s66,999424\n"
"s_add_u32 s67,vcc_hi,65536\n"
"v_mfma_f32_16x16x32_bf16 a[128:131],v[144:147],v[160:163],0\n"
"s_mul_i32 vcc_lo,vcc_hi,5\n"
"s_sub_u32 vcc_lo,s96,vcc_lo\n"
"v_mfma_f32_16x16x32_bf16 a[128:131],v[208:211],v[224:227],a[128:131]\n"
"s_add_u32 s64,vcc_lo,81920\n"
"s_add_u32 s62,vcc_lo,135168\n"
"s_add_u32 s61,vcc_lo,524288\n"
"v_mfma_f32_16x16x32_bf16 a[136:139],v[208:211],v[228:231],0\n"
"buffer_load_dwordx4 v107,s[16:19],s76 offen offset:1024 lds\n"
"v_mfma_f32_16x16x32_bf16 a[136:139],v[144:147],v[164:167],a[136:139]\n"
"s_cmp_lt_u32 s52,2\n"
"s_cselect_b32 s64,s64,2146435072\n"
"s_cselect_b32 s67,s67,163840\n"
"s_cselect_b32 s65,s65,1\n"
"v_mfma_f32_16x16x32_bf16 a[144:147],v[144:147],v[168:171],0\n"
"s_cselect_b32 vcc_lo,4,0\n"
"s_and_b32 vcc_hi,s52,1\n"
"s_add_u32 vcc_lo,vcc_lo,vcc_hi\n"
"v_mfma_f32_16x16x32_bf16 a[144:147],v[208:211],v[232:235],a[144:147]\n"
"s_lshl_b32 vcc_hi,s90,12\n"
"s_add_u32 vcc_hi,vcc_hi,54525952\n"
"v_mfma_f32_16x16x32_bf16 a[152:155],v[208:211],v[236:239],0\n"
"buffer_load_dwordx4 v108,s[16:19],s76 offen offset:2048 lds\n"
"v_mfma_f32_16x16x32_bf16 a[152:155],v[144:147],v[172:175],a[152:155]\n"
"s_lshl3_add_u32 s70,s95,vcc_lo\n"
"s_lshl_b32 s70,s70,17\n"
"v_mfma_f32_16x16x32_bf16 a[160:163],v[144:147],v[176:179],0\n"
"s_add_u32 s70,s70,vcc_hi\n"
"s_lshl3_add_u32 s68,s94,vcc_lo\n"
"v_mfma_f32_16x16x32_bf16 a[160:163],v[208:211],v[240:243],a[160:163]\n"
"s_lshl_b32 s68,s68,17\n"
"s_add_u32 s68,s68,vcc_hi\n"
"v_mfma_f32_16x16x32_bf16 a[168:171],v[208:211],v[244:247],0\n"
"buffer_load_dwordx4 v109,s[16:19],s76 offen offset:3072 lds\n"
"v_mfma_f32_16x16x32_bf16 a[168:171],v[144:147],v[180:183],a[168:171]\n"
"s_lshr_b32 vcc_lo,s90,3\n"
"s_lshl2_add_u32 vcc_lo,vcc_lo,s52\n"
"v_mfma_f32_16x16x32_bf16 a[176:179],v[144:147],v[184:187],0\n"
"s_lshl_b32 vcc_lo,vcc_lo,7\n"
"s_and_b32 vcc_hi,s90,7\n"
"v_mfma_f32_16x16x32_bf16 a[176:179],v[208:211],v[248:251],a[176:179]\n"
"s_add_u32 vcc_lo,vcc_lo,vcc_hi\n"
"s_mov_b32 s76,262528\n"
"s_add_u32 m0,s79,4096\n"
"v_mfma_f32_16x16x32_bf16 a[184:187],v[208:211],v[252:255],0\n"
"buffer_load_dwordx4 v106,s[16:19],s76 offen offset:0 lds\n"
"v_mfma_f32_16x16x32_bf16 a[184:187],v[144:147],v[188:191],a[184:187]\n"
"s_lshl3_add_u32 s69,s91,vcc_lo\n"
"s_lshl_b32 s69,s69,3\n"
"v_mfma_f32_16x16x32_bf16 a[132:135],v[148:151],v[160:163],0\n"
"s_add_u32 s69,s69,148930560\n"
"s_lshl3_add_u32 s71,s94,vcc_lo\n"
"v_mfma_f32_16x16x32_bf16 a[132:135],v[212:215],v[224:227],a[132:135]\n"
"s_lshl_b32 s71,s71,3\n"
"s_add_u32 s71,s71,148930560\n"
"v_mfma_f32_16x16x32_bf16 a[140:143],v[212:215],v[228:231],0\n"
"buffer_load_dwordx4 v107,s[16:19],s76 offen offset:1024 lds\n"
"v_mfma_f32_16x16x32_bf16 a[140:143],v[148:151],v[164:167],a[140:143]\n"
"s_mov_b32 s6,8388608\n"
"s_mov_b32 s7,0x20000\n"
"v_mfma_f32_16x16x32_bf16 a[148:151],v[148:151],v[168:171],0\n"
"s_lshr_b32 s94,s88,1\n"
"s_and_b32 vcc_lo,s91,1\n"
"v_mfma_f32_16x16x32_bf16 a[148:151],v[212:215],v[232:235],a[148:151]\n"
"s_lshl1_add_u32 s95,s97,vcc_lo\n"
"s_and_b32 vcc_lo,s88,1\n"
"v_mfma_f32_16x16x32_bf16 a[156:159],v[212:215],v[236:239],0\n"
"buffer_load_dwordx4 v108,s[16:19],s76 offen offset:2048 lds\n"
"v_mfma_f32_16x16x32_bf16 a[156:159],v[148:151],v[172:175],a[156:159]\n"
"s_lshr_b32 vcc_hi,s91,1\n"
"s_lshl2_add_u32 s96,vcc_lo,vcc_hi\n"
"v_mfma_f32_16x16x32_bf16 a[164:167],v[148:151],v[176:179],0\n"
"s_lshr_b32 vcc_lo,s88,2\n"
"s_lshl2_add_u32 s96,vcc_lo,vcc_hi\n"
"v_mfma_f32_16x16x32_bf16 a[164:167],v[212:215],v[240:243],a[164:167]\n"
"s_sub_u32 vcc_lo,7,s88\n"
"s_min_u32 s94,vcc_lo,s88\n"
"v_mfma_f32_16x16x32_bf16 a[172:175],v[212:215],v[244:247],0\n"
"buffer_load_dwordx4 v109,s[16:19],s76 offen offset:3072 lds\n"
"v_mfma_f32_16x16x32_bf16 a[172:175],v[148:151],v[180:183],a[172:175]\n"
"s_and_b32 s96,s88,4\n"
"s_add_u32 s96,s96,s97\n"
"s_add_u32 s95,s91,s96\n"
"v_mfma_f32_16x16x32_bf16 a[180:183],v[148:151],v[184:187],0\n"
"buffer_load_dwordx4 v[8:11],v70,s[32:35],0 offen offset:0\n"
"v_mfma_f32_16x16x32_bf16 a[180:183],v[212:215],v[248:251],a[180:183]\n"
"buffer_load_dwordx4 v[12:15],v70,s[32:35],0 offen offset:512\n"
"s_and_b32 s95,s95,7\n"
"v_mfma_f32_16x16x32_bf16 a[188:191],v[212:215],v[252:255],0\n"
"buffer_load_dwordx4 v[16:19],v70,s[32:35],0 offen offset:1024\n"
"buffer_load_dwordx4 v[20:23],v70,s[32:35],0 offen offset:1536\n"
"s_lshl3_add_u32 vcc_lo,s94,s96\n"
"v_mfma_f32_16x16x32_bf16 a[188:191],v[148:151],v[188:191],a[188:191]\n"
"s_waitcnt vmcnt(28)\n"
"s_barrier\n"
"v_mfma_f32_16x16x32_bf16 a[192:195],v[216:219],v[224:227],a[192:195]\n"
"ds_read_b128 v[152:155],v80 offset:45056\n"
"s_mov_b32 s87,524288\n"
"v_mfma_f32_16x16x32_bf16 a[196:199],v[220:223],v[224:227],a[196:199]\n"
"ds_read_b128 v[156:159],v80 offset:47104\n"
"buffer_load_dword v115,v112,s[36:39],s87 offen\n"
"v_mfma_f32_16x16x32_bf16 a[204:207],v[220:223],v[228:231],a[204:207]\n"
"ds_read_b64_tr_b16 v[160:161],v98 offset:0\n"
"s_mov_b32 s87,1048576\n"
"v_mfma_f32_16x16x32_bf16 a[200:203],v[216:219],v[228:231],a[200:203]\n"
"ds_read_b64_tr_b16 v[162:163],v98 offset:1024\n"
"buffer_load_dword v115,v112,s[36:39],s87 offen\n"
"v_mfma_f32_16x16x32_bf16 a[208:211],v[216:219],v[232:235],a[208:211]\n"
"ds_read_b64_tr_b16 v[164:165],v99 offset:0\n"
"s_mov_b32 s87,1572864\n"
"v_mfma_f32_16x16x32_bf16 a[212:215],v[220:223],v[232:235],a[212:215]\n"
"ds_read_b64_tr_b16 v[166:167],v99 offset:1024\n"
"buffer_load_dword v115,v112,s[36:39],s87 offen\n"
"v_mfma_f32_16x16x32_bf16 a[220:223],v[220:223],v[236:239],a[220:223]\n"
"ds_read_b64_tr_b16 v[168:169],v100 offset:0\n"
"s_lshl3_add_u32 vcc_lo,vcc_lo,s52\n"
"v_mfma_f32_16x16x32_bf16 a[216:219],v[216:219],v[236:239],a[216:219]\n"
"ds_read_b64_tr_b16 v[170:171],v100 offset:1024\n"
"s_lshl_b32 s13,vcc_lo,15\n"
"v_mfma_f32_16x16x32_bf16 a[224:227],v[216:219],v[240:243],a[224:227]\n"
"ds_read_b64_tr_b16 v[172:173],v101 offset:0\n"
"s_lshl_b32 vcc_lo,s95,8\n"
"v_mfma_f32_16x16x32_bf16 a[228:231],v[220:223],v[240:243],a[228:231]\n"
"ds_read_b64_tr_b16 v[174:175],v101 offset:1024\n"
"s_add_u32 s13,s13,vcc_lo\n"
"v_mfma_f32_16x16x32_bf16 a[236:239],v[220:223],v[244:247],a[236:239]\n"
"ds_read_b64_tr_b16 v[176:177],v102 offset:0\n"
"s_lshl3_add_u32 vcc_hi,s94,s95\n"
"v_mfma_f32_16x16x32_bf16 a[232:235],v[216:219],v[244:247],a[232:235]\n"
"ds_read_b64_tr_b16 v[178:179],v102 offset:1024\n"
"s_lshl_b32 s72,vcc_hi,8\n"
"v_mfma_f32_16x16x32_bf16 a[240:243],v[216:219],v[248:251],a[240:243]\n"
"ds_read_b64_tr_b16 v[180:181],v103 offset:0\n"
"s_add_u32 s72,s72,149061632\n"
"v_mfma_f32_16x16x32_bf16 a[244:247],v[220:223],v[248:251],a[244:247]\n"
"s_or_b32 vcc_lo,s96,s52\n"
"ds_read_b64_tr_b16 v[182:183],v103 offset:1024\n"
"s_cselect_b32 s72,2146435072,s72\n"
"v_mfma_f32_16x16x32_bf16 a[252:255],v[220:223],v[252:255],a[252:255]\n"
"ds_read_b64_tr_b16 v[184:185],v104 offset:0\n"
"s_lshl_b32 s3,vcc_hi,18\n"
"v_mfma_f32_16x16x32_bf16 a[248:251],v[216:219],v[252:255],a[248:251]\n"
"s_lshl_b32 vcc_lo,s96,15\n"
"ds_read_b64_tr_b16 v[186:187],v104 offset:1024\n"
"s_waitcnt lgkmcnt(10)\n"
"v_mfma_f32_16x16x32_bf16 a[192:195],v[152:155],v[160:163],a[192:195]\n"
"ds_read_b64_tr_b16 v[188:189],v105 offset:0\n"
"s_sub_u32 s3,s3,vcc_lo\n"
"v_mfma_f32_16x16x32_bf16 a[196:199],v[156:159],v[160:163],a[196:199]\n"
"ds_read_b64_tr_b16 v[190:191],v105 offset:1024\n"
"s_add_u32 s3,s3,s78\n"
"v_mfma_f32_16x16x32_bf16 a[204:207],v[156:159],v[164:167],a[204:207]\n"
"ds_read_b128 v[128:131],v80 offset:32768\n"
"s_add_u32 s3,s3,100909056\n"
"v_mfma_f32_16x16x32_bf16 a[200:203],v[152:155],v[164:167],a[200:203]\n"
"ds_read_b128 v[192:195],v81 offset:32768\n"
"s_waitcnt lgkmcnt(10)\n"
"v_mfma_f32_16x16x32_bf16 a[208:211],v[152:155],v[168:171],a[208:211]\n"
"ds_read_b64_tr_b16 v[224:225],v98 offset:16384\n"
"s_lshl3_add_u32 vcc_hi,vcc_hi,s96\n"
"v_mfma_f32_16x16x32_bf16 a[212:215],v[156:159],v[168:171],a[212:215]\n"
"ds_read_b64_tr_b16 v[226:227],v98 offset:17408\n"
"s_lshl2_add_u32 vcc_hi,vcc_hi,s52\n"
"v_mfma_f32_16x16x32_bf16 a[220:223],v[156:159],v[172:175],a[220:223]\n"
"ds_read_b64_tr_b16 v[228:229],v99 offset:16384\n"
"s_lshl_b32 s2,vcc_hi,4\n"
"v_mfma_f32_16x16x32_bf16 a[216:219],v[152:155],v[172:175],a[216:219]\n"
"ds_read_b64_tr_b16 v[230:231],v99 offset:17408\n"
"s_waitcnt lgkmcnt(10)\n"
"v_mfma_f32_16x16x32_bf16 a[224:227],v[152:155],v[176:179],a[224:227]\n"
"ds_read_b64_tr_b16 v[232:233],v100 offset:16384\n"
"s_add_u32 s2,s2,149028864\n"
"v_mfma_f32_16x16x32_bf16 a[228:231],v[156:159],v[176:179],a[228:231]\n"
"ds_read_b64_tr_b16 v[234:235],v100 offset:17408\n"
"s_lshl2_add_u32 vcc_lo,s95,s52\n"
"v_mfma_f32_16x16x32_bf16 a[236:239],v[156:159],v[180:183],a[236:239]\n"
"ds_read_b64_tr_b16 v[236:237],v101 offset:16384\n"
"s_lshl_b32 vcc_lo,vcc_lo,10\n"
"v_mfma_f32_16x16x32_bf16 a[232:235],v[152:155],v[180:183],a[232:235]\n"
"ds_read_b64_tr_b16 v[238:239],v101 offset:17408\n"
"s_waitcnt lgkmcnt(10)\n"
"v_mfma_f32_16x16x32_bf16 a[240:243],v[152:155],v[184:187],a[240:243]\n"
"ds_read_b64_tr_b16 v[240:241],v102 offset:16384\n"
"s_lshl_b32 vcc_hi,s94,22\n"
"v_mfma_f32_16x16x32_bf16 a[244:247],v[156:159],v[184:187],a[244:247]\n"
"ds_read_b64_tr_b16 v[242:243],v102 offset:17408\n"
"s_add_u32 s12,vcc_lo,vcc_hi\n"
"v_mfma_f32_16x16x32_bf16 a[252:255],v[156:159],v[188:191],a[252:255]\n"
"ds_read_b64_tr_b16 v[244:245],v103 offset:16384\n"
"s_lshl_b32 vcc_lo,s96,19\n"
"v_mfma_f32_16x16x32_bf16 a[248:251],v[152:155],v[188:191],a[248:251]\n"
"ds_read_b64_tr_b16 v[246:247],v103 offset:17408\n"
"s_waitcnt lgkmcnt(8)\n"
"v_mfma_f32_16x16x32_bf16 a[0:3],v[128:131],v[160:163],a[0:3]\n"
"ds_read_b64_tr_b16 v[248:249],v104 offset:16384\n"
"s_add_u32 s10,s12,vcc_lo\n"
"v_mfma_f32_16x16x32_bf16 a[0:3],v[192:195],v[224:227],a[0:3]\n"
"ds_read_b64_tr_b16 v[250:251],v104 offset:17408\n"
"s_add_u32 s10,s10,71303168\n"
"v_mfma_f32_16x16x32_bf16 a[8:11],v[192:195],v[228:231],a[8:11]\n"
"ds_read_b64_tr_b16 v[252:253],v105 offset:16384\n"
"s_mul_i32 vcc_lo,s52,31744\n"
"v_mfma_f32_16x16x32_bf16 a[8:11],v[128:131],v[164:167],a[8:11]\n"
"ds_read_b64_tr_b16 v[254:255],v105 offset:17408\n"
"s_waitcnt lgkmcnt(8)\n"
"v_mfma_f32_16x16x32_bf16 a[16:19],v[128:131],v[168:171],a[16:19]\n"
"ds_read_b128 v[132:135],v80 offset:34816\n"
"s_add_u32 s10,s10,vcc_lo\n"
"v_mfma_f32_16x16x32_bf16 a[16:19],v[192:195],v[232:235],a[16:19]\n"
"ds_read_b128 v[196:199],v81 offset:34816\n"
"s_lshl_b32 vcc_lo,s52,10\n"
"v_mfma_f32_16x16x32_bf16 a[24:27],v[192:195],v[236:239],a[24:27]\n"
"ds_read_b128 v[136:139],v80 offset:36864\n"
"s_sub_u32 s12,57671680,vcc_lo\n"
"v_mfma_f32_16x16x32_bf16 a[24:27],v[128:131],v[172:175],a[24:27]\n"
"ds_read_b128 v[200:203],v81 offset:36864\n"
"s_waitcnt lgkmcnt(8)\n"
"v_mfma_f32_16x16x32_bf16 a[32:35],v[128:131],v[176:179],a[32:35]\n"
"ds_read_b128 v[140:143],v80 offset:38912\n"
"s_add_u32 s8,s79,54525952\n"
"v_mfma_f32_16x16x32_bf16 a[32:35],v[192:195],v[240:243],a[32:35]\n"
"ds_read_b128 v[204:207],v81 offset:38912\n"
"s_and_b32 vcc_lo,s96,1\n"
"v_mfma_f32_16x16x32_bf16 a[40:43],v[192:195],v[244:247],a[40:43]\n"
"ds_read_b128 v[144:147],v80 offset:40960\n"
"s_lshl_b32 s11,vcc_lo,10\n"
"v_mfma_f32_16x16x32_bf16 a[40:43],v[128:131],v[180:183],a[40:43]\n"
"ds_read_b128 v[208:211],v81 offset:40960\n"
"s_waitcnt lgkmcnt(8)\n"
"v_mfma_f32_16x16x32_bf16 a[48:51],v[128:131],v[184:187],a[48:51]\n"
"ds_read_b128 v[148:151],v80 offset:43008\n"
"s_add_u32 s11,s11,148932608\n"
"v_mfma_f32_16x16x32_bf16 a[48:51],v[192:195],v[248:251],a[48:51]\n"
"ds_read_b128 v[212:215],v81 offset:43008\n"
"s_mov_b32 s9,148931072\n"
"v_mfma_f32_16x16x32_bf16 a[56:59],v[192:195],v[252:255],a[56:59]\n"
"s_cmp_eq_u32 s52,0\n"
"ds_read_b128 v[216:219],v81 offset:45056\n"
"s_cselect_b32 s71,s71,2146435072\n"
"v_mfma_f32_16x16x32_bf16 a[56:59],v[128:131],v[188:191],a[56:59]\n"
"ds_read_b128 v[220:223],v81 offset:47104\n"
"s_waitcnt lgkmcnt(10)\n"
"v_mfma_f32_16x16x32_bf16 a[4:7],v[132:135],v[160:163],a[4:7]\n"
"v_mfma_f32_16x16x32_bf16 a[4:7],v[196:199],v[224:227],a[4:7]\n"
"v_mfma_f32_16x16x32_bf16 a[12:15],v[196:199],v[228:231],a[12:15]\n"
"v_mfma_f32_16x16x32_bf16 a[12:15],v[132:135],v[164:167],a[12:15]\n"
"v_mfma_f32_16x16x32_bf16 a[20:23],v[132:135],v[168:171],a[20:23]\n"
"s_waitcnt lgkmcnt(0)\n"
"s_barrier\n"
"v_mfma_f32_16x16x32_bf16 a[20:23],v[196:199],v[232:235],a[20:23]\n"
"s_mov_b32 s77,1572864\n"
"s_add_u32 m0,s78,98304\n"
"v_mfma_f32_16x16x32_bf16 a[28:31],v[196:199],v[236:239],a[28:31]\n"
"buffer_load_dwordx4 v110,s[20:23],s77 offen offset:0 lds\n"
"v_mfma_f32_16x16x32_bf16 a[28:31],v[132:135],v[172:175],a[28:31]\n"
"v_mfma_f32_16x16x32_bf16 a[36:39],v[132:135],v[176:179],a[36:39]\n"
"v_mfma_f32_16x16x32_bf16 a[36:39],v[196:199],v[240:243],a[36:39]\n"
"v_mfma_f32_16x16x32_bf16 a[44:47],v[196:199],v[244:247],a[44:47]\n"
"buffer_load_dwordx4 v111,s[20:23],s77 offen offset:1024 lds\n"
"v_mfma_f32_16x16x32_bf16 a[44:47],v[132:135],v[180:183],a[44:47]\n"
"v_mfma_f32_16x16x32_bf16 a[52:55],v[132:135],v[184:187],a[52:55]\n"
"v_mfma_f32_16x16x32_bf16 a[52:55],v[196:199],v[248:251],a[52:55]\n"
"v_mfma_f32_16x16x32_bf16 a[60:63],v[196:199],v[252:255],a[60:63]\n"
"buffer_load_dwordx4 v113,s[20:23],s77 offen offset:2048 lds\n"
"v_mfma_f32_16x16x32_bf16 a[60:63],v[132:135],v[188:191],a[60:63]\n"
"v_mfma_f32_16x16x32_bf16 a[64:67],v[136:139],v[160:163],a[64:67]\n"
"v_mfma_f32_16x16x32_bf16 a[64:67],v[200:203],v[224:227],a[64:67]\n"
"v_mfma_f32_16x16x32_bf16 a[72:75],v[200:203],v[228:231],a[72:75]\n"
"buffer_load_dwordx4 v114,s[20:23],s77 offen offset:3072 lds\n"
"v_mfma_f32_16x16x32_bf16 a[72:75],v[136:139],v[164:167],a[72:75]\n"
"v_mfma_f32_16x16x32_bf16 a[80:83],v[136:139],v[168:171],a[80:83]\n"
"v_mfma_f32_16x16x32_bf16 a[80:83],v[200:203],v[232:235],a[80:83]\n"
"s_mov_b32 s77,1835008\n"
"s_add_u32 m0,s78,114688\n"
"v_mfma_f32_16x16x32_bf16 a[88:91],v[200:203],v[236:239],a[88:91]\n"
"buffer_load_dwordx4 v110,s[20:23],s77 offen offset:0 lds\n"
"v_mfma_f32_16x16x32_bf16 a[88:91],v[136:139],v[172:175],a[88:91]\n"
"v_mfma_f32_16x16x32_bf16 a[96:99],v[136:139],v[176:179],a[96:99]\n"
"v_mfma_f32_16x16x32_bf16 a[96:99],v[200:203],v[240:243],a[96:99]\n"
"v_mfma_f32_16x16x32_bf16 a[104:107],v[200:203],v[244:247],a[104:107]\n"
"buffer_load_dwordx4 v111,s[20:23],s77 offen offset:1024 lds\n"
"v_mfma_f32_16x16x32_bf16 a[104:107],v[136:139],v[180:183],a[104:107]\n"
"v_mfma_f32_16x16x32_bf16 a[112:115],v[136:139],v[184:187],a[112:115]\n"
"v_mfma_f32_16x16x32_bf16 a[112:115],v[200:203],v[248:251],a[112:115]\n"
"v_mfma_f32_16x16x32_bf16 a[120:123],v[200:203],v[252:255],a[120:123]\n"
"buffer_load_dwordx4 v113,s[20:23],s77 offen offset:2048 lds\n"
"v_mfma_f32_16x16x32_bf16 a[120:123],v[136:139],v[188:191],a[120:123]\n"
"s_waitcnt vmcnt(10)\n"
"v_mfma_f32_16x16x32_bf16 a[68:71],v[140:143],v[160:163],a[68:71]\n"
"v_lshlrev_b32 v24,16,v8\n"
"v_and_b32 v25,0xffff0000,v8\n"
"v_mfma_f32_16x16x32_bf16 a[68:71],v[204:207],v[224:227],a[68:71]\n"
"v_lshlrev_b32 v26,16,v9\n"
"v_and_b32 v27,0xffff0000,v9\n"
"v_mfma_f32_16x16x32_bf16 a[76:79],v[204:207],v[228:231],a[76:79]\n"
"buffer_load_dwordx4 v114,s[20:23],s77 offen offset:3072 lds\n"
"v_mfma_f32_16x16x32_bf16 a[76:79],v[140:143],v[164:167],a[76:79]\n"
"v_lshlrev_b32 v28,16,v10\n"
"v_and_b32 v29,0xffff0000,v10\n"
"v_mfma_f32_16x16x32_bf16 a[84:87],v[140:143],v[168:171],a[84:87]\n"
"v_lshlrev_b32 v30,16,v11\n"
"v_and_b32 v31,0xffff0000,v11\n"
"v_mfma_f32_16x16x32_bf16 a[84:87],v[204:207],v[232:235],a[84:87]\n"
"v_lshlrev_b32 v32,16,v12\n"
"v_and_b32 v33,0xffff0000,v12\n"
"v_mfma_f32_16x16x32_bf16 a[92:95],v[204:207],v[236:239],a[92:95]\n"
"s_mov_b32 s76,512\n"
"v_lshlrev_b32 v34,16,v13\n"
"s_add_u32 m0,s79,32768\n"
"v_mfma_f32_16x16x32_bf16 a[92:95],v[140:143],v[172:175],a[92:95]\n"
"buffer_load_dwordx4 v106,s[16:19],s76 offen offset:0 lds\n"
"v_mfma_f32_16x16x32_bf16 a[100:103],v[140:143],v[176:179],a[100:103]\n"
"v_and_b32 v35,0xffff0000,v13\n"
"v_lshlrev_b32 v36,16,v14\n"
"v_mfma_f32_16x16x32_bf16 a[100:103],v[204:207],v[240:243],a[100:103]\n"
"v_and_b32 v37,0xffff0000,v14\n"
"v_lshlrev_b32 v38,16,v15\n"
"v_mfma_f32_16x16x32_bf16 a[108:111],v[204:207],v[244:247],a[108:111]\n"
"v_and_b32 v39,0xffff0000,v15\n"
"v_lshlrev_b32 v40,16,v16\n"
"v_mfma_f32_16x16x32_bf16 a[108:111],v[140:143],v[180:183],a[108:111]\n"
"v_and_b32 v41,0xffff0000,v16\n"
"v_lshlrev_b32 v42,16,v17\n"
"v_mfma_f32_16x16x32_bf16 a[116:119],v[140:143],v[184:187],a[116:119]\n"
"buffer_load_dwordx4 v107,s[16:19],s76 offen offset:1024 lds\n"
"v_mfma_f32_16x16x32_bf16 a[116:119],v[204:207],v[248:251],a[116:119]\n"
"v_and_b32 v43,0xffff0000,v17\n"
"v_lshlrev_b32 v44,16,v18\n"
"v_mfma_f32_16x16x32_bf16 a[124:127],v[204:207],v[252:255],a[124:127]\n"
"v_and_b32 v45,0xffff0000,v18\n"
"v_lshlrev_b32 v46,16,v19\n"
"v_mfma_f32_16x16x32_bf16 a[124:127],v[140:143],v[188:191],a[124:127]\n"
"v_and_b32 v47,0xffff0000,v19\n"
"v_lshlrev_b32 v48,16,v20\n"
"v_mfma_f32_16x16x32_bf16 a[128:131],v[144:147],v[160:163],a[128:131]\n"
"v_and_b32 v49,0xffff0000,v20\n"
"v_lshlrev_b32 v50,16,v21\n"
"v_mfma_f32_16x16x32_bf16 a[128:131],v[208:211],v[224:227],a[128:131]\n"
"buffer_load_dwordx4 v108,s[16:19],s76 offen offset:2048 lds\n"
"v_mfma_f32_16x16x32_bf16 a[136:139],v[208:211],v[228:231],a[136:139]\n"
"v_and_b32 v51,0xffff0000,v21\n"
"v_lshlrev_b32 v52,16,v22\n"
"v_mfma_f32_16x16x32_bf16 a[136:139],v[144:147],v[164:167],a[136:139]\n"
"v_and_b32 v53,0xffff0000,v22\n"
"v_lshlrev_b32 v54,16,v23\n"
"v_mfma_f32_16x16x32_bf16 a[144:147],v[144:147],v[168:171],a[144:147]\n"
"v_and_b32 v55,0xffff0000,v23\n"
"v_add_f32 v56,v24,v25\n"
"v_mfma_f32_16x16x32_bf16 a[144:147],v[208:211],v[232:235],a[144:147]\n"
"v_add_f32 v57,v26,v27\n"
"v_add_f32 v56,v56,v28\n"
"v_mfma_f32_16x16x32_bf16 a[152:155],v[208:211],v[236:239],a[152:155]\n"
"buffer_load_dwordx4 v109,s[16:19],s76 offen offset:3072 lds\n"
"v_mfma_f32_16x16x32_bf16 a[152:155],v[144:147],v[172:175],a[152:155]\n"
"v_add_f32 v57,v57,v29\n"
"v_add_f32 v56,v56,v30\n"
"v_mfma_f32_16x16x32_bf16 a[160:163],v[144:147],v[176:179],a[160:163]\n"
"v_add_f32 v57,v57,v31\n"
"v_add_f32 v56,v56,v32\n"
"v_mfma_f32_16x16x32_bf16 a[160:163],v[208:211],v[240:243],a[160:163]\n"
"v_readfirstlane_b32 s82,v78\n"
"v_readfirstlane_b32 s83,v79\n"
"v_mfma_f32_16x16x32_bf16 a[168:171],v[208:211],v[244:247],a[168:171]\n"
"s_mov_b32 s76,262656\n"
"v_add_f32 v57,v57,v33\n"
"s_add_u32 m0,s79,36864\n"
"v_mfma_f32_16x16x32_bf16 a[168:171],v[144:147],v[180:183],a[168:171]\n"
"buffer_load_dwordx4 v106,s[16:19],s76 offen offset:0 lds\n"
"s_mul_i32 s80,s82,0x9e3779b1\n"
"v_mfma_f32_16x16x32_bf16 a[176:179],v[144:147],v[184:187],a[176:179]\n"
"s_add_u32 s80,s80,0x7f4a7c15\n"
"v_add_f32 v56,v56,v34\n"
"v_add_f32 v57,v57,v35\n"
"v_mfma_f32_16x16x32_bf16 a[176:179],v[208:211],v[248:251],a[176:179]\n"
"s_add_u32 s81,s83,s80\n"
"s_add_u32 s81,s81,1\n"
"v_mfma_f32_16x16x32_bf16 a[184:187],v[208:211],v[252:255],a[184:187]\n"
"v_mov_b32 v124,s80\n"
"v_mov_b32 v125,s81\n"
"v_mfma_f32_16x16x32_bf16 a[184:187],v[144:147],v[188:191],a[184:187]\n"
"v_add_f32 v56,v56,v36\n"
"v_add_f32 v57,v57,v37\n"
"v_mfma_f32_16x16x32_bf16 a[132:135],v[148:151],v[160:163],a[132:135]\n"
"buffer_load_dwordx4 v107,s[16:19],s76 offen offset:1024 lds\n"
"v_mfma_f32_16x16x32_bf16 a[132:135],v[212:215],v[224:227],a[132:135]\n"
"v_add_f32 v56,v56,v38\n"
"v_add_f32 v57,v57,v39\n"
"v_mfma_f32_16x16x32_bf16 a[140:143],v[212:215],v[228:231],a[140:143]\n"
"v_add_f32 v56,v56,v40\n"
"v_add_f32 v57,v57,v41\n"
"v_mfma_f32_16x16x32_bf16 a[140:143],v[148:151],v[164:167],a[140:143]\n"
"v_add_f32 v56,v56,v42\n"
"v_add_f32 v57,v57,v43\n"
"v_mfma_f32_16x16x32_bf16 a[148:151],v[148:151],v[168:171],a[148:151]\n"
"v_add_f32 v56,v56,v44\n"
"v_add_f32 v57,v57,v45\n"
"v_mfma_f32_16x16x32_bf16 a[148:151],v[212:215],v[232:235],a[148:151]\n"
"buffer_load_dwordx4 v108,s[16:19],s76 offen offset:2048 lds\n"
"v_mfma_f32_16x16x32_bf16 a[156:159],v[212:215],v[236:239],a[156:159]\n"
"v_add_f32 v56,v56,v46\n"
"v_add_f32 v57,v57,v47\n"
"v_mfma_f32_16x16x32_bf16 a[156:159],v[148:151],v[172:175],a[156:159]\n"
"v_add_f32 v56,v56,v48\n"
"v_add_f32 v57,v57,v49\n"
"v_mfma_f32_16x16x32_bf16 a[164:167],v[148:151],v[176:179],a[164:167]\n"
"v_add_f32 v56,v56,v50\n"
"v_add_f32 v57,v57,v51\n"
"v_mfma_f32_16x16x32_bf16 a[164:167],v[212:215],v[240:243],a[164:167]\n"
"v_add_f32 v56,v56,v52\n"
"v_add_f32 v57,v57,v53\n"
"v_mfma_f32_16x16x32_bf16 a[172:175],v[212:215],v[244:247],a[172:175]\n"
"buffer_load_dwordx4 v109,s[16:19],s76 offen offset:3072 lds\n"
"v_mfma_f32_16x16x32_bf16 a[172:175],v[148:151],v[180:183],a[172:175]\n"
"s_mov_b32 s87,2097152\n"
"v_add_f32 v56,v56,v54\n"
"v_add_f32 v57,v57,v55\n"
"v_mfma_f32_16x16x32_bf16 a[180:183],v[148:151],v[184:187],a[180:183]\n"
"buffer_load_dword v115,v112,s[36:39],s87 offen\n"
"v_mfma_f32_16x16x32_bf16 a[180:183],v[212:215],v[248:251],a[180:183]\n"
"s_mov_b32 s87,2621440\n"
"v_add_f32 v56,v56,v57\n"
"v_mfma_f32_16x16x32_bf16 a[188:191],v[212:215],v[252:255],a[188:191]\n"
"buffer_load_dword v115,v112,s[36:39],s87 offen\n"
"v_add_f32_dpp v56,v56,v56 quad_perm:[1,0,3,2] row_mask:0xf bank_mask:0xf\n"
"v_mfma_f32_16x16x32_bf16 a[188:191],v[148:151],v[188:191],a[188:191]\n"
"s_barrier\n"
"v_mfma_f32_16x16x32_bf16 a[192:195],v[216:219],v[224:227],a[192:195]\n"
"ds_read_b128 v[152:155],v82 offset:12288\n"
"v_mfma_f32_16x16x32_bf16 a[196:199],v[220:223],v[224:227],a[196:199]\n"
"ds_read_b128 v[156:159],v82 offset:14336\n"
"v_add_f32_dpp v56,v56,v56 quad_perm:[2,3,0,1] row_mask:0xf bank_mask:0xf\n"
"v_mfma_f32_16x16x32_bf16 a[204:207],v[220:223],v[228:231],a[204:207]\n"
"ds_read_b64_tr_b16 v[160:161],v98 offset:49152\n"
"v_mfma_f32_16x16x32_bf16 a[200:203],v[216:219],v[228:231],a[200:203]\n"
"ds_read_b64_tr_b16 v[162:163],v98 offset:50176\n"
"v_add_f32_dpp v56,v56,v56 row_ror:8 row_mask:0xf bank_mask:0xf\n"
"v_mfma_f32_16x16x32_bf16 a[208:211],v[216:219],v[232:235],a[208:211]\n"
"ds_read_b64_tr_b16 v[164:165],v99 offset:49152\n"
"v_mov_b32 v57,v56\n"
"v_mfma_f32_16x16x32_bf16 a[212:215],v[220:223],v[232:235],a[212:215]\n"
"ds_read_b64_tr_b16 v[166:167],v99 offset:50176\n"
"v_mfma_f32_16x16x32_bf16 a[220:223],v[220:223],v[236:239],a[220:223]\n"
"ds_read_b64_tr_b16 v[168:169],v100 offset:49152\n"
"v_permlane16_swap_b32 v57,v56\n"
"v_mfma_f32_16x16x32_bf16 a[216:219],v[216:219],v[236:239],a[216:219]\n"
"ds_read_b64_tr_b16 v[170:171],v100 offset:50176\n"
"v_add_f32 v56,v56,v57\n"
"v_mfma_f32_16x16x32_bf16 a[224:227],v[216:219],v[240:243],a[224:227]\n"
"ds_read_b64_tr_b16 v[172:173],v101 offset:49152\n"
"v_mov_b32 v57,v56\n"
"v_mfma_f32_16x16x32_bf16 a[228:231],v[220:223],v[240:243],a[228:231]\n"
"ds_read_b64_tr_b16 v[174:175],v101 offset:50176\n"
"v_mfma_f32_16x16x32_bf16 a[236:239],v[220:223],v[244:247],a[236:239]\n"
"ds_read_b64_tr_b16 v[176:177],v102 offset:49152\n"
"v_permlane32_swap_b32 v57,v56\n"
"v_mfma_f32_16x16x32_bf16 a[232:235],v[216:219],v[244:247],a[232:235]\n"
"ds_read_b64_tr_b16 v[178:179],v102 offset:50176\n"
"v_add_f32 v56,v56,v57\n"
"v_mfma_f32_16x16x32_bf16 a[240:243],v[216:219],v[248:251],a[240:243]\n"
"ds_read_b64_tr_b16 v[180:181],v103 offset:49152\n"
"v_mul_f32 v58,0x3a800000,v56\n"
"v_mfma_f32_16x16x32_bf16 a[244:247],v[220:223],v[248:251],a[244:247]\n"
"ds_read_b64_tr_b16 v[182:183],v103 offset:50176\n"
"v_sub_f32 v62,v24,v58\n"
"v_mfma_f32_16x16x32_bf16 a[252:255],v[220:223],v[252:255],a[252:255]\n"
"ds_read_b64_tr_b16 v[184:185],v104 offset:49152\n"
"v_mul_f32 v59,v62,v62\n"
"v_mfma_f32_16x16x32_bf16 a[248:251],v[216:219],v[252:255],a[248:251]\n"
"ds_read_b64_tr_b16 v[186:187],v104 offset:50176\n"
"v_sub_f32 v63,v25,v58\n"
"s_waitcnt lgkmcnt(10)\n"
"v_mfma_f32_16x16x32_bf16 a[192:195],v[152:155],v[160:163],a[192:195]\n"
"ds_read_b64_tr_b16 v[188:189],v105 offset:49152\n"
"v_mul_f32 v60,v63,v63\n"
"v_mfma_f32_16x16x32_bf16 a[196:199],v[156:159],v[160:163],a[196:199]\n"
"ds_read_b64_tr_b16 v[190:191],v105 offset:50176\n"
"v_sub_f32 v64,v26,v58\n"
"v_mfma_f32_16x16x32_bf16 a[204:207],v[156:159],v[164:167],a[204:207]\n"
"ds_read_b128 v[128:131],v82 offset:0\n"
"v_fmac_f32 v59,v64,v64\n"
"v_mfma_f32_16x16x32_bf16 a[200:203],v[152:155],v[164:167],a[200:203]\n"
"ds_read_b128 v[192:195],v83 offset:0\n"
"s_waitcnt lgkmcnt(10)\n"
"v_mfma_f32_16x16x32_bf16 a[208:211],v[152:155],v[168:171],a[208:211]\n"
"ds_read_b64_tr_b16 v[224:225],v98 offset:32768\n"
"v_sub_f32 v65,v27,v58\n"
"v_mfma_f32_16x16x32_bf16 a[212:215],v[156:159],v[168:171],a[212:215]\n"
"ds_read_b64_tr_b16 v[226:227],v98 offset:33792\n"
"v_fmac_f32 v60,v65,v65\n"
"v_mfma_f32_16x16x32_bf16 a[220:223],v[156:159],v[172:175],a[220:223]\n"
"ds_read_b64_tr_b16 v[228:229],v99 offset:32768\n"
"v_sub_f32 v62,v28,v58\n"
"v_mfma_f32_16x16x32_bf16 a[216:219],v[152:155],v[172:175],a[216:219]\n"
"ds_read_b64_tr_b16 v[230:231],v99 offset:33792\n"
"s_waitcnt lgkmcnt(10)\n"
"v_mfma_f32_16x16x32_bf16 a[224:227],v[152:155],v[176:179],a[224:227]\n"
"ds_read_b64_tr_b16 v[232:233],v100 offset:32768\n"
"v_fmac_f32 v59,v62,v62\n"
"v_mfma_f32_16x16x32_bf16 a[228:231],v[156:159],v[176:179],a[228:231]\n"
"ds_read_b64_tr_b16 v[234:235],v100 offset:33792\n"
"v_sub_f32 v63,v29,v58\n"
"v_mfma_f32_16x16x32_bf16 a[236:239],v[156:159],v[180:183],a[236:239]\n"
"ds_read_b64_tr_b16 v[236:237],v101 offset:32768\n"
"v_fmac_f32 v60,v63,v63\n"
"v_mfma_f32_16x16x32_bf16 a[232:235],v[152:155],v[180:183],a[232:235]\n"
"ds_read_b64_tr_b16 v[238:239],v101 offset:33792\n"
"s_waitcnt lgkmcnt(10)\n"
"v_mfma_f32_16x16x32_bf16 a[240:243],v[152:155],v[184:187],a[240:243]\n"
"ds_read_b64_tr_b16 v[240:241],v102 offset:32768\n"
"v_sub_f32 v64,v30,v58\n"
"v_mfma_f32_16x16x32_bf16 a[244:247],v[156:159],v[184:187],a[244:247]\n"
"ds_read_b64_tr_b16 v[242:243],v102 offset:33792\n"
"v_fmac_f32 v59,v64,v64\n"
"v_mfma_f32_16x16x32_bf16 a[252:255],v[156:159],v[188:191],a[252:255]\n"
"ds_read_b64_tr_b16 v[244:245],v103 offset:32768\n"
"v_sub_f32 v65,v31,v58\n"
"v_mfma_f32_16x16x32_bf16 a[248:251],v[152:155],v[188:191],a[248:251]\n"
"ds_read_b64_tr_b16 v[246:247],v103 offset:33792\n"
"s_waitcnt lgkmcnt(8)\n"
"v_mfma_f32_16x16x32_bf16 a[0:3],v[128:131],v[160:163],a[0:3]\n"
"ds_read_b64_tr_b16 v[248:249],v104 offset:32768\n"
"v_fmac_f32 v60,v65,v65\n"
"v_mfma_f32_16x16x32_bf16 a[0:3],v[192:195],v[224:227],a[0:3]\n"
"ds_read_b64_tr_b16 v[250:251],v104 offset:33792\n"
"v_sub_f32 v62,v32,v58\n"
"v_mfma_f32_16x16x32_bf16 a[8:11],v[192:195],v[228:231],a[8:11]\n"
"ds_read_b64_tr_b16 v[252:253],v105 offset:32768\n"
"v_fmac_f32 v59,v62,v62\n"
"v_mfma_f32_16x16x32_bf16 a[8:11],v[128:131],v[164:167],a[8:11]\n"
"ds_read_b64_tr_b16 v[254:255],v105 offset:33792\n"
"s_waitcnt lgkmcnt(8)\n"
"v_mfma_f32_16x16x32_bf16 a[16:19],v[128:131],v[168:171],a[16:19]\n"
"ds_read_b128 v[132:135],v82 offset:2048\n"
"v_sub_f32 v63,v33,v58\n"
"v_mfma_f32_16x16x32_bf16 a[16:19],v[192:195],v[232:235],a[16:19]\n"
"ds_read_b128 v[196:199],v83 offset:2048\n"
"v_fmac_f32 v60,v63,v63\n"
"v_mfma_f32_16x16x32_bf16 a[24:27],v[192:195],v[236:239],a[24:27]\n"
"ds_read_b128 v[136:139],v82 offset:4096\n"
"v_sub_f32 v64,v34,v58\n"
"v_mfma_f32_16x16x32_bf16 a[24:27],v[128:131],v[172:175],a[24:27]\n"
"ds_read_b128 v[200:203],v83 offset:4096\n"
"s_waitcnt lgkmcnt(8)\n"
"v_mfma_f32_16x16x32_bf16 a[32:35],v[128:131],v[176:179],a[32:35]\n"
"ds_read_b128 v[140:143],v82 offset:6144\n"
"v_fmac_f32 v59,v64,v64\n"
"v_mfma_f32_16x16x32_bf16 a[32:35],v[192:195],v[240:243],a[32:35]\n"
"ds_read_b128 v[204:207],v83 offset:6144\n"
"v_sub_f32 v65,v35,v58\n"
"v_mfma_f32_16x16x32_bf16 a[40:43],v[192:195],v[244:247],a[40:43]\n"
"ds_read_b128 v[144:147],v82 offset:8192\n"
"v_fmac_f32 v60,v65,v65\n"
"v_mfma_f32_16x16x32_bf16 a[40:43],v[128:131],v[180:183],a[40:43]\n"
"ds_read_b128 v[208:211],v83 offset:8192\n"
"s_waitcnt lgkmcnt(8)\n"
"v_mfma_f32_16x16x32_bf16 a[48:51],v[128:131],v[184:187],a[48:51]\n"
"ds_read_b128 v[148:151],v82 offset:10240\n"
"v_sub_f32 v62,v36,v58\n"
"v_mfma_f32_16x16x32_bf16 a[48:51],v[192:195],v[248:251],a[48:51]\n"
"ds_read_b128 v[212:215],v83 offset:10240\n"
"v_fmac_f32 v59,v62,v62\n"
"v_mfma_f32_16x16x32_bf16 a[56:59],v[192:195],v[252:255],a[56:59]\n"
"ds_read_b128 v[216:219],v83 offset:12288\n"
"v_sub_f32 v63,v37,v58\n"
"v_mfma_f32_16x16x32_bf16 a[56:59],v[128:131],v[188:191],a[56:59]\n"
"ds_read_b128 v[220:223],v83 offset:14336\n"
"s_waitcnt lgkmcnt(10)\n"
"v_mfma_f32_16x16x32_bf16 a[4:7],v[132:135],v[160:163],a[4:7]\n"
"v_fmac_f32 v60,v63,v63\n"
"v_sub_f32 v64,v38,v58\n"
"v_mfma_f32_16x16x32_bf16 a[4:7],v[196:199],v[224:227],a[4:7]\n"
"v_fmac_f32 v59,v64,v64\n"
"v_sub_f32 v65,v39,v58\n"
"v_mfma_f32_16x16x32_bf16 a[12:15],v[196:199],v[228:231],a[12:15]\n"
"v_fmac_f32 v60,v65,v65\n"
"v_sub_f32 v62,v40,v58\n"
"v_mfma_f32_16x16x32_bf16 a[12:15],v[132:135],v[164:167],a[12:15]\n"
"v_fmac_f32 v59,v62,v62\n"
"v_sub_f32 v63,v41,v58\n"
"v_mfma_f32_16x16x32_bf16 a[20:23],v[132:135],v[168:171],a[20:23]\n"
"s_waitcnt lgkmcnt(0)\n"
"s_barrier\n"
"v_mfma_f32_16x16x32_bf16 a[20:23],v[196:199],v[232:235],a[20:23]\n"
"s_mov_b32 s77,2097152\n"
"v_fmac_f32 v60,v63,v63\n"
"s_add_u32 m0,s78,147456\n"
"v_mfma_f32_16x16x32_bf16 a[28:31],v[196:199],v[236:239],a[28:31]\n"
"buffer_load_dwordx4 v110,s[20:23],s77 offen offset:0 lds\n"
"v_mfma_f32_16x16x32_bf16 a[28:31],v[132:135],v[172:175],a[28:31]\n"
"v_sub_f32 v64,v42,v58\n"
"v_fmac_f32 v59,v64,v64\n"
"v_mfma_f32_16x16x32_bf16 a[36:39],v[132:135],v[176:179],a[36:39]\n"
"v_sub_f32 v65,v43,v58\n"
"v_fmac_f32 v60,v65,v65\n"
"v_mfma_f32_16x16x32_bf16 a[36:39],v[196:199],v[240:243],a[36:39]\n"
"v_sub_f32 v62,v44,v58\n"
"v_fmac_f32 v59,v62,v62\n"
"v_mfma_f32_16x16x32_bf16 a[44:47],v[196:199],v[244:247],a[44:47]\n"
"buffer_load_dwordx4 v111,s[20:23],s77 offen offset:1024 lds\n"
"v_mfma_f32_16x16x32_bf16 a[44:47],v[132:135],v[180:183],a[44:47]\n"
"v_sub_f32 v63,v45,v58\n"
"v_fmac_f32 v60,v63,v63\n"
"v_mfma_f32_16x16x32_bf16 a[52:55],v[132:135],v[184:187],a[52:55]\n"
"v_sub_f32 v64,v46,v58\n"
"v_fmac_f32 v59,v64,v64\n"
"v_mfma_f32_16x16x32_bf16 a[52:55],v[196:199],v[248:251],a[52:55]\n"
"v_sub_f32 v65,v47,v58\n"
"v_fmac_f32 v60,v65,v65\n"
"v_mfma_f32_16x16x32_bf16 a[60:63],v[196:199],v[252:255],a[60:63]\n"
"buffer_load_dwordx4 v113,s[20:23],s77 offen offset:2048 lds\n"
"v_mfma_f32_16x16x32_bf16 a[60:63],v[132:135],v[188:191],a[60:63]\n"
"v_sub_f32 v62,v48,v58\n"
"v_fmac_f32 v59,v62,v62\n"
"v_mfma_f32_16x16x32_bf16 a[64:67],v[136:139],v[160:163],a[64:67]\n"
"v_sub_f32 v63,v49,v58\n"
"v_fmac_f32 v60,v63,v63\n"
"v_mfma_f32_16x16x32_bf16 a[64:67],v[200:203],v[224:227],a[64:67]\n"
"v_sub_f32 v64,v50,v58\n"
"v_fmac_f32 v59,v64,v64\n"
"v_mfma_f32_16x16x32_bf16 a[72:75],v[200:203],v[228:231],a[72:75]\n"
"buffer_load_dwordx4 v114,s[20:23],s77 offen offset:3072 lds\n"
"v_mfma_f32_16x16x32_bf16 a[72:75],v[136:139],v[164:167],a[72:75]\n"
"v_sub_f32 v65,v51,v58\n"
"v_fmac_f32 v60,v65,v65\n"
"v_mfma_f32_16x16x32_bf16 a[80:83],v[136:139],v[168:171],a[80:83]\n"
"v_sub_f32 v62,v52,v58\n"
"v_fmac_f32 v59,v62,v62\n"
"v_mfma_f32_16x16x32_bf16 a[80:83],v[200:203],v[232:235],a[80:83]\n"
"s_mov_b32 s77,2359296\n"
"v_sub_f32 v63,v53,v58\n"
"s_add_u32 m0,s78,131072\n"
"v_mfma_f32_16x16x32_bf16 a[88:91],v[200:203],v[236:239],a[88:91]\n"
"buffer_load_dwordx4 v110,s[20:23],s77 offen offset:0 lds\n"
"v_mfma_f32_16x16x32_bf16 a[88:91],v[136:139],v[172:175],a[88:91]\n"
"v_fmac_f32 v60,v63,v63\n"
"v_sub_f32 v64,v54,v58\n"
"v_mfma_f32_16x16x32_bf16 a[96:99],v[136:139],v[176:179],a[96:99]\n"
"v_fmac_f32 v59,v64,v64\n"
"v_sub_f32 v65,v55,v58\n"
"v_mfma_f32_16x16x32_bf16 a[96:99],v[200:203],v[240:243],a[96:99]\n"
"v_fmac_f32 v60,v65,v65\n"
"v_add_f32 v59,v59,v60\n"
"v_mfma_f32_16x16x32_bf16 a[104:107],v[200:203],v[244:247],a[104:107]\n"
"buffer_load_dwordx4 v111,s[20:23],s77 offen offset:1024 lds\n"
"v_mfma_f32_16x16x32_bf16 a[104:107],v[136:139],v[180:183],a[104:107]\n"
"v_mfma_f32_16x16x32_bf16 a[112:115],v[136:139],v[184:187],a[112:115]\n"
"v_add_f32_dpp v59,v59,v59 quad_perm:[1,0,3,2] row_mask:0xf bank_mask:0xf\n"
"v_mfma_f32_16x16x32_bf16 a[112:115],v[200:203],v[248:251],a[112:115]\n"
"s_nop 0\n"
"v_add_f32_dpp v59,v59,v59 quad_perm:[2,3,0,1] row_mask:0xf bank_mask:0xf\n"
"v_mfma_f32_16x16x32_bf16 a[120:123],v[200:203],v[252:255],a[120:123]\n"
"buffer_load_dwordx4 v113,s[20:23],s77 offen offset:2048 lds\n"
"v_mfma_f32_16x16x32_bf16 a[120:123],v[136:139],v[188:191],a[120:123]\n"
"v_add_f32_dpp v59,v59,v59 row_ror:8 row_mask:0xf bank_mask:0xf\n"
"v_mov_b32 v57,v59\n"
"v_mfma_f32_16x16x32_bf16 a[68:71],v[140:143],v[160:163],a[68:71]\n"
"v_mfma_f32_16x16x32_bf16 a[68:71],v[204:207],v[224:227],a[68:71]\n"
"v_permlane16_swap_b32 v57,v59\n"
"v_add_f32 v59,v59,v57\n"
"v_mfma_f32_16x16x32_bf16 a[76:79],v[204:207],v[228:231],a[76:79]\n"
"buffer_load_dwordx4 v114,s[20:23],s77 offen offset:3072 lds\n"
"v_mfma_f32_16x16x32_bf16 a[76:79],v[140:143],v[164:167],a[76:79]\n"
"v_mov_b32 v57,v59\n"
"v_mfma_f32_16x16x32_bf16 a[84:87],v[140:143],v[168:171],a[84:87]\n"
"s_nop 0\n"
"v_permlane32_swap_b32 v57,v59\n"
"v_add_f32 v59,v59,v57\n"
"v_mfma_f32_16x16x32_bf16 a[84:87],v[204:207],v[232:235],a[84:87]\n"
"v_mul_f32 v59,0x3a800000,v59\n"
"v_cvt_pk_bf16_f32 v59,v59,v59\n"
"v_mfma_f32_16x16x32_bf16 a[92:95],v[204:207],v[236:239],a[92:95]\n"
"s_mov_b32 s76,640\n"
"v_lshlrev_b32 v59,16,v59\n"
"s_add_u32 m0,s79,65536\n"
"v_mfma_f32_16x16x32_bf16 a[92:95],v[140:143],v[172:175],a[92:95]\n"
"buffer_load_dwordx4 v106,s[16:19],s76 offen offset:0 lds\n"
"v_mfma_f32_16x16x32_bf16 a[100:103],v[140:143],v[176:179],a[100:103]\n"
"v_add_f32 v59,0x3727c5ac,v59\n"
"v_cvt_pk_bf16_f32 v59,v59,v59\n"
"v_mfma_f32_16x16x32_bf16 a[100:103],v[204:207],v[240:243],a[100:103]\n"
"v_lshlrev_b32 v59,16,v59\n"
"v_sqrt_f32 v59,v59\n"
"v_mfma_f32_16x16x32_bf16 a[108:111],v[204:207],v[244:247],a[108:111]\n"
"v_cvt_pk_bf16_f32 v58,v58,v58\n"
"v_lshlrev_b32 v58,16,v58\n"
"v_mfma_f32_16x16x32_bf16 a[108:111],v[140:143],v[180:183],a[108:111]\n"
"v_cvt_pk_bf16_f32 v59,v59,v59\n"
"v_lshlrev_b32 v59,16,v59\n"
"v_mfma_f32_16x16x32_bf16 a[116:119],v[140:143],v[184:187],a[116:119]\n"
"buffer_load_dwordx4 v107,s[16:19],s76 offen offset:1024 lds\n"
"v_mfma_f32_16x16x32_bf16 a[116:119],v[204:207],v[248:251],a[116:119]\n"
"v_rcp_f32 v66,v59\n"
"v_mfma_f32_16x16x32_bf16 a[124:127],v[204:207],v[252:255],a[124:127]\n"
"v_mul_f32 v66,0x3e0293ee,v66\n"
"v_mul_f32 v58,v58,v66\n"
"v_mfma_f32_16x16x32_bf16 a[124:127],v[140:143],v[188:191],a[124:127]\n"
"v_fma_f32 v24,v24,v66,-v58\n"
"v_fma_f32 v25,v25,v66,-v58\n"
"v_mfma_f32_16x16x32_bf16 a[128:131],v[144:147],v[160:163],a[128:131]\n"
"v_cvt_pk_bf16_f32 v8,v24,v25\n"
"v_fma_f32 v26,v26,v66,-v58\n"
"v_mfma_f32_16x16x32_bf16 a[128:131],v[208:211],v[224:227],a[128:131]\n"
"buffer_load_dwordx4 v108,s[16:19],s76 offen offset:2048 lds\n"
"v_mfma_f32_16x16x32_bf16 a[136:139],v[208:211],v[228:231],a[136:139]\n"
"v_fma_f32 v27,v27,v66,-v58\n"
"v_cvt_pk_bf16_f32 v9,v26,v27\n"
"v_mfma_f32_16x16x32_bf16 a[136:139],v[144:147],v[164:167],a[136:139]\n"
"v_fma_f32 v28,v28,v66,-v58\n"
"v_fma_f32 v29,v29,v66,-v58\n"
"v_mfma_f32_16x16x32_bf16 a[144:147],v[144:147],v[168:171],a[144:147]\n"
"v_cvt_pk_bf16_f32 v10,v28,v29\n"
"v_fma_f32 v30,v30,v66,-v58\n"
"v_mfma_f32_16x16x32_bf16 a[144:147],v[208:211],v[232:235],a[144:147]\n"
"v_fma_f32 v31,v31,v66,-v58\n"
"v_cvt_pk_bf16_f32 v11,v30,v31\n"
"v_mfma_f32_16x16x32_bf16 a[152:155],v[208:211],v[236:239],a[152:155]\n"
"buffer_load_dwordx4 v109,s[16:19],s76 offen offset:3072 lds\n"
"v_mfma_f32_16x16x32_bf16 a[152:155],v[144:147],v[172:175],a[152:155]\n"
"buffer_store_dwordx4 v[8:11],v71,s[24:27],s54 offen sc1\n"
"v_mfma_f32_16x16x32_bf16 a[160:163],v[144:147],v[176:179],a[160:163]\n"
"s_add_u32 s54,s54,131072\n"
"v_fma_f32 v32,v32,v66,-v58\n"
"v_fma_f32 v33,v33,v66,-v58\n"
"v_mfma_f32_16x16x32_bf16 a[160:163],v[208:211],v[240:243],a[160:163]\n"
"v_cvt_pk_bf16_f32 v12,v32,v33\n"
"v_fma_f32 v34,v34,v66,-v58\n"
"v_mfma_f32_16x16x32_bf16 a[168:171],v[208:211],v[244:247],a[168:171]\n"
"s_mov_b32 s76,262784\n"
"v_fma_f32 v35,v35,v66,-v58\n"
"s_add_u32 m0,s79,69632\n"
"v_mfma_f32_16x16x32_bf16 a[168:171],v[144:147],v[180:183],a[168:171]\n"
"buffer_load_dwordx4 v106,s[16:19],s76 offen offset:0 lds\n"
"v_mfma_f32_16x16x32_bf16 a[176:179],v[144:147],v[184:187],a[176:179]\n"
"v_cvt_pk_bf16_f32 v13,v34,v35\n"
"v_fma_f32 v36,v36,v66,-v58\n"
"v_mfma_f32_16x16x32_bf16 a[176:179],v[208:211],v[248:251],a[176:179]\n"
"v_fma_f32 v37,v37,v66,-v58\n"
"v_cvt_pk_bf16_f32 v14,v36,v37\n"
"v_mfma_f32_16x16x32_bf16 a[184:187],v[208:211],v[252:255],a[184:187]\n"
"v_fma_f32 v38,v38,v66,-v58\n"
"v_fma_f32 v39,v39,v66,-v58\n"
"v_cvt_pk_bf16_f32 v15,v38,v39\n"
"v_mfma_f32_16x16x32_bf16 a[184:187],v[144:147],v[188:191],a[184:187]\n"
"buffer_store_dwordx4 v[12:15],v71,s[24:27],s54 offen sc1\n"
"v_mfma_f32_16x16x32_bf16 a[132:135],v[148:151],v[160:163],a[132:135]\n"
"buffer_load_dwordx4 v107,s[16:19],s76 offen offset:1024 lds\n"
"v_mfma_f32_16x16x32_bf16 a[132:135],v[212:215],v[224:227],a[132:135]\n"
"s_add_u32 s54,s54,131072\n"
"v_fma_f32 v40,v40,v66,-v58\n"
"v_mfma_f32_16x16x32_bf16 a[140:143],v[212:215],v[228:231],a[140:143]\n"
"v_fma_f32 v41,v41,v66,-v58\n"
"v_cvt_pk_bf16_f32 v16,v40,v41\n"
"v_mfma_f32_16x16x32_bf16 a[140:143],v[148:151],v[164:167],a[140:143]\n"
"v_fma_f32 v42,v42,v66,-v58\n"
"v_fma_f32 v43,v43,v66,-v58\n"
"v_mfma_f32_16x16x32_bf16 a[148:151],v[148:151],v[168:171],a[148:151]\n"
"v_cvt_pk_bf16_f32 v17,v42,v43\n"
"v_fma_f32 v44,v44,v66,-v58\n"
"v_mfma_f32_16x16x32_bf16 a[148:151],v[212:215],v[232:235],a[148:151]\n"
"buffer_load_dwordx4 v108,s[16:19],s76 offen offset:2048 lds\n"
"v_mfma_f32_16x16x32_bf16 a[156:159],v[212:215],v[236:239],a[156:159]\n"
"v_fma_f32 v45,v45,v66,-v58\n"
"v_cvt_pk_bf16_f32 v18,v44,v45\n"
"v_mfma_f32_16x16x32_bf16 a[156:159],v[148:151],v[172:175],a[156:159]\n"
"v_fma_f32 v46,v46,v66,-v58\n"
"v_fma_f32 v47,v47,v66,-v58\n"
"v_cvt_pk_bf16_f32 v19,v46,v47\n"
"v_mfma_f32_16x16x32_bf16 a[164:167],v[148:151],v[176:179],a[164:167]\n"
"buffer_store_dwordx4 v[16:19],v71,s[24:27],s54 offen sc1\n"
"v_mfma_f32_16x16x32_bf16 a[164:167],v[212:215],v[240:243],a[164:167]\n"
"s_add_u32 s54,s54,131072\n"
"v_fma_f32 v48,v48,v66,-v58\n"
"v_mfma_f32_16x16x32_bf16 a[172:175],v[212:215],v[244:247],a[172:175]\n"
"buffer_load_dwordx4 v109,s[16:19],s76 offen offset:3072 lds\n"
"v_mfma_f32_16x16x32_bf16 a[172:175],v[148:151],v[180:183],a[172:175]\n"
"s_mov_b32 s87,0\n"
"v_fma_f32 v49,v49,v66,-v58\n"
"v_cvt_pk_bf16_f32 v20,v48,v49\n"
"v_mfma_f32_16x16x32_bf16 a[180:183],v[148:151],v[184:187],a[180:183]\n"
"buffer_load_dword v115,v112,s[40:43],s87 offen\n"
"v_mfma_f32_16x16x32_bf16 a[180:183],v[212:215],v[248:251],a[180:183]\n"
"s_mov_b32 s87,1048576\n"
"v_fma_f32 v50,v50,v66,-v58\n"
"v_fma_f32 v51,v51,v66,-v58\n"
"v_mfma_f32_16x16x32_bf16 a[188:191],v[212:215],v[252:255],a[188:191]\n"
"buffer_load_dword v115,v112,s[40:43],s87 offen\n"
"v_mfma_f32_16x16x32_bf16 a[188:191],v[148:151],v[188:191],a[188:191]\n"
"s_mov_b32 s87,2097152\n"
"s_waitcnt vmcnt(31)\n"
"s_barrier\n"
"v_mfma_f32_16x16x32_bf16 a[192:195],v[216:219],v[224:227],a[192:195]\n"
"ds_read_b128 v[152:155],v80 offset:12288\n"
"buffer_load_dword v115,v112,s[40:43],s87 offen\n"
"v_mfma_f32_16x16x32_bf16 a[196:199],v[220:223],v[224:227],a[196:199]\n"
"ds_read_b128 v[156:159],v80 offset:14336\n"
"v_cvt_pk_bf16_f32 v21,v50,v51\n"
"v_mfma_f32_16x16x32_bf16 a[204:207],v[220:223],v[228:231],a[204:207]\n"
"ds_read_b64_tr_b16 v[160:161],v98 offset:0\n"
"v_fma_f32 v52,v52,v66,-v58\n"
"v_mfma_f32_16x16x32_bf16 a[200:203],v[216:219],v[228:231],a[200:203]\n"
"ds_read_b64_tr_b16 v[162:163],v98 offset:1024\n"
"v_fma_f32 v53,v53,v66,-v58\n"
"v_mfma_f32_16x16x32_bf16 a[208:211],v[216:219],v[232:235],a[208:211]\n"
"ds_read_b64_tr_b16 v[164:165],v99 offset:0\n"
"v_cvt_pk_bf16_f32 v22,v52,v53\n"
"v_mfma_f32_16x16x32_bf16 a[212:215],v[220:223],v[232:235],a[212:215]\n"
"ds_read_b64_tr_b16 v[166:167],v99 offset:1024\n"
"v_fma_f32 v54,v54,v66,-v58\n"
"v_mfma_f32_16x16x32_bf16 a[220:223],v[220:223],v[236:239],a[220:223]\n"
"ds_read_b64_tr_b16 v[168:169],v100 offset:0\n"
"v_fma_f32 v55,v55,v66,-v58\n"
"v_mfma_f32_16x16x32_bf16 a[216:219],v[216:219],v[236:239],a[216:219]\n"
"ds_read_b64_tr_b16 v[170:171],v100 offset:1024\n"
"v_cvt_pk_bf16_f32 v23,v54,v55\n"
"v_mfma_f32_16x16x32_bf16 a[224:227],v[216:219],v[240:243],a[224:227]\n"
"ds_read_b64_tr_b16 v[172:173],v101 offset:0\n"
"buffer_store_dwordx4 v[20:23],v71,s[24:27],s54 offen sc1\n"
"v_mfma_f32_16x16x32_bf16 a[228:231],v[220:223],v[240:243],a[228:231]\n"
"ds_read_b64_tr_b16 v[174:175],v101 offset:1024\n"
"v_mfma_f32_16x16x32_bf16 a[236:239],v[220:223],v[244:247],a[236:239]\n"
"ds_read_b64_tr_b16 v[176:177],v102 offset:0\n"
"v_mfma_f32_16x16x32_bf16 a[232:235],v[216:219],v[244:247],a[232:235]\n"
"ds_read_b64_tr_b16 v[178:179],v102 offset:1024\n"
"v_mfma_f32_16x16x32_bf16 a[240:243],v[216:219],v[248:251],a[240:243]\n"
"ds_read_b64_tr_b16 v[180:181],v103 offset:0\n"
"v_mfma_f32_16x16x32_bf16 a[244:247],v[220:223],v[248:251],a[244:247]\n"
"ds_read_b64_tr_b16 v[182:183],v103 offset:1024\n"
"v_mfma_f32_16x16x32_bf16 a[252:255],v[220:223],v[252:255],a[252:255]\n"
"ds_read_b64_tr_b16 v[184:185],v104 offset:0\n"
"v_mfma_f32_16x16x32_bf16 a[248:251],v[216:219],v[252:255],a[248:251]\n"
"ds_read_b64_tr_b16 v[186:187],v104 offset:1024\n"
"s_waitcnt lgkmcnt(10)\n"
"v_mfma_f32_16x16x32_bf16 a[192:195],v[152:155],v[160:163],a[192:195]\n"
"ds_read_b64_tr_b16 v[188:189],v105 offset:0\n"
"v_mfma_f32_16x16x32_bf16 a[196:199],v[156:159],v[160:163],a[196:199]\n"
"ds_read_b64_tr_b16 v[190:191],v105 offset:1024\n"
"v_mfma_f32_16x16x32_bf16 a[204:207],v[156:159],v[164:167],a[204:207]\n"
"ds_read_b128 v[128:131],v80 offset:0\n"
"v_mfma_f32_16x16x32_bf16 a[200:203],v[152:155],v[164:167],a[200:203]\n"
"ds_read_b128 v[192:195],v81 offset:0\n"
"s_waitcnt lgkmcnt(10)\n"
"v_mfma_f32_16x16x32_bf16 a[208:211],v[152:155],v[168:171],a[208:211]\n"
"ds_read_b64_tr_b16 v[224:225],v98 offset:16384\n"
"v_mfma_f32_16x16x32_bf16 a[212:215],v[156:159],v[168:171],a[212:215]\n"
"ds_read_b64_tr_b16 v[226:227],v98 offset:17408\n"
"v_mfma_f32_16x16x32_bf16 a[220:223],v[156:159],v[172:175],a[220:223]\n"
"ds_read_b64_tr_b16 v[228:229],v99 offset:16384\n"
"v_mfma_f32_16x16x32_bf16 a[216:219],v[152:155],v[172:175],a[216:219]\n"
"ds_read_b64_tr_b16 v[230:231],v99 offset:17408\n"
"s_waitcnt lgkmcnt(10)\n"
"v_mfma_f32_16x16x32_bf16 a[224:227],v[152:155],v[176:179],a[224:227]\n"
"ds_read_b64_tr_b16 v[232:233],v100 offset:16384\n"
"v_mfma_f32_16x16x32_bf16 a[228:231],v[156:159],v[176:179],a[228:231]\n"
"ds_read_b64_tr_b16 v[234:235],v100 offset:17408\n"
"v_mfma_f32_16x16x32_bf16 a[236:239],v[156:159],v[180:183],a[236:239]\n"
"ds_read_b64_tr_b16 v[236:237],v101 offset:16384\n"
"v_mfma_f32_16x16x32_bf16 a[232:235],v[152:155],v[180:183],a[232:235]\n"
"ds_read_b64_tr_b16 v[238:239],v101 offset:17408\n"
"s_waitcnt lgkmcnt(10)\n"
"v_mfma_f32_16x16x32_bf16 a[240:243],v[152:155],v[184:187],a[240:243]\n"
"ds_read_b64_tr_b16 v[240:241],v102 offset:16384\n"
"v_mfma_f32_16x16x32_bf16 a[244:247],v[156:159],v[184:187],a[244:247]\n"
"ds_read_b64_tr_b16 v[242:243],v102 offset:17408\n"
"v_mfma_f32_16x16x32_bf16 a[252:255],v[156:159],v[188:191],a[252:255]\n"
"ds_read_b64_tr_b16 v[244:245],v103 offset:16384\n"
"v_mfma_f32_16x16x32_bf16 a[248:251],v[152:155],v[188:191],a[248:251]\n"
"ds_read_b64_tr_b16 v[246:247],v103 offset:17408\n"
"s_waitcnt lgkmcnt(8)\n"
"v_mfma_f32_16x16x32_bf16 a[0:3],v[128:131],v[160:163],a[0:3]\n"
"ds_read_b64_tr_b16 v[248:249],v104 offset:16384\n"
"v_mfma_f32_16x16x32_bf16 a[0:3],v[192:195],v[224:227],a[0:3]\n"
"ds_read_b64_tr_b16 v[250:251],v104 offset:17408\n"
"v_mfma_f32_16x16x32_bf16 a[8:11],v[192:195],v[228:231],a[8:11]\n"
"ds_read_b64_tr_b16 v[252:253],v105 offset:16384\n"
"v_mfma_f32_16x16x32_bf16 a[8:11],v[128:131],v[164:167],a[8:11]\n"
"ds_read_b64_tr_b16 v[254:255],v105 offset:17408\n"
"s_waitcnt lgkmcnt(8)\n"
"v_mfma_f32_16x16x32_bf16 a[16:19],v[128:131],v[168:171],a[16:19]\n"
"ds_read_b128 v[132:135],v80 offset:2048\n"
"v_mfma_f32_16x16x32_bf16 a[16:19],v[192:195],v[232:235],a[16:19]\n"
"ds_read_b128 v[196:199],v81 offset:2048\n"
"v_mfma_f32_16x16x32_bf16 a[24:27],v[192:195],v[236:239],a[24:27]\n"
"ds_read_b128 v[136:139],v80 offset:4096\n"
"v_mfma_f32_16x16x32_bf16 a[24:27],v[128:131],v[172:175],a[24:27]\n"
"ds_read_b128 v[200:203],v81 offset:4096\n"
"s_waitcnt lgkmcnt(8)\n"
"v_mfma_f32_16x16x32_bf16 a[32:35],v[128:131],v[176:179],a[32:35]\n"
"ds_read_b128 v[140:143],v80 offset:6144\n"
"v_mfma_f32_16x16x32_bf16 a[32:35],v[192:195],v[240:243],a[32:35]\n"
"ds_read_b128 v[204:207],v81 offset:6144\n"
"v_mfma_f32_16x16x32_bf16 a[40:43],v[192:195],v[244:247],a[40:43]\n"
"ds_read_b128 v[144:147],v80 offset:8192\n"
"v_mfma_f32_16x16x32_bf16 a[40:43],v[128:131],v[180:183],a[40:43]\n"
"ds_read_b128 v[208:211],v81 offset:8192\n"
"s_waitcnt lgkmcnt(8)\n"
"v_mfma_f32_16x16x32_bf16 a[48:51],v[128:131],v[184:187],a[48:51]\n"
"ds_read_b128 v[148:151],v80 offset:10240\n"
"v_mfma_f32_16x16x32_bf16 a[48:51],v[192:195],v[248:251],a[48:51]\n"
"ds_read_b128 v[212:215],v81 offset:10240\n"
"v_mfma_f32_16x16x32_bf16 a[56:59],v[192:195],v[252:255],a[56:59]\n"
"ds_read_b128 v[216:219],v81 offset:12288\n"
"v_mfma_f32_16x16x32_bf16 a[56:59],v[128:131],v[188:191],a[56:59]\n"
"ds_read_b128 v[220:223],v81 offset:14336\n"
"s_waitcnt lgkmcnt(10)\n"
"v_mfma_f32_16x16x32_bf16 a[4:7],v[132:135],v[160:163],a[4:7]\n"
"v_mfma_f32_16x16x32_bf16 a[4:7],v[196:199],v[224:227],a[4:7]\n"
"v_mfma_f32_16x16x32_bf16 a[12:15],v[196:199],v[228:231],a[12:15]\n"
"v_mfma_f32_16x16x32_bf16 a[12:15],v[132:135],v[164:167],a[12:15]\n"
"v_mfma_f32_16x16x32_bf16 a[20:23],v[132:135],v[168:171],a[20:23]\n"
"s_waitcnt lgkmcnt(0)\n"
"s_barrier\n"
"v_mfma_f32_16x16x32_bf16 a[20:23],v[196:199],v[232:235],a[20:23]\n"
"s_mov_b32 s77,2621440\n"
"s_add_u32 m0,s78,98304\n"
"v_mfma_f32_16x16x32_bf16 a[28:31],v[196:199],v[236:239],a[28:31]\n"
"buffer_load_dwordx4 v110,s[20:23],s77 offen offset:0 lds\n"
"v_mfma_f32_16x16x32_bf16 a[28:31],v[132:135],v[172:175],a[28:31]\n"
"v_mfma_f32_16x16x32_bf16 a[36:39],v[132:135],v[176:179],a[36:39]\n"
"v_mfma_f32_16x16x32_bf16 a[36:39],v[196:199],v[240:243],a[36:39]\n"
"v_mfma_f32_16x16x32_bf16 a[44:47],v[196:199],v[244:247],a[44:47]\n"
"buffer_load_dwordx4 v111,s[20:23],s77 offen offset:1024 lds\n"
"v_mfma_f32_16x16x32_bf16 a[44:47],v[132:135],v[180:183],a[44:47]\n"
"v_mfma_f32_16x16x32_bf16 a[52:55],v[132:135],v[184:187],a[52:55]\n"
"v_mfma_f32_16x16x32_bf16 a[52:55],v[196:199],v[248:251],a[52:55]\n"
"v_mfma_f32_16x16x32_bf16 a[60:63],v[196:199],v[252:255],a[60:63]\n"
"buffer_load_dwordx4 v113,s[20:23],s77 offen offset:2048 lds\n"
"v_mfma_f32_16x16x32_bf16 a[60:63],v[132:135],v[188:191],a[60:63]\n"
"v_mfma_f32_16x16x32_bf16 a[64:67],v[136:139],v[160:163],a[64:67]\n"
"v_mfma_f32_16x16x32_bf16 a[64:67],v[200:203],v[224:227],a[64:67]\n"
"v_mfma_f32_16x16x32_bf16 a[72:75],v[200:203],v[228:231],a[72:75]\n"
"buffer_load_dwordx4 v114,s[20:23],s77 offen offset:3072 lds\n"
"v_mfma_f32_16x16x32_bf16 a[72:75],v[136:139],v[164:167],a[72:75]\n"
"v_mfma_f32_16x16x32_bf16 a[80:83],v[136:139],v[168:171],a[80:83]\n"
"v_mfma_f32_16x16x32_bf16 a[80:83],v[200:203],v[232:235],a[80:83]\n"
"s_mov_b32 s77,2883584\n"
"s_add_u32 m0,s78,114688\n"
"v_mfma_f32_16x16x32_bf16 a[88:91],v[200:203],v[236:239],a[88:91]\n"
"buffer_load_dwordx4 v110,s[20:23],s77 offen offset:0 lds\n"
"v_mfma_f32_16x16x32_bf16 a[88:91],v[136:139],v[172:175],a[88:91]\n"
"v_mfma_f32_16x16x32_bf16 a[96:99],v[136:139],v[176:179],a[96:99]\n"
"v_mfma_f32_16x16x32_bf16 a[96:99],v[200:203],v[240:243],a[96:99]\n"
"v_mfma_f32_16x16x32_bf16 a[104:107],v[200:203],v[244:247],a[104:107]\n"
"buffer_load_dwordx4 v111,s[20:23],s77 offen offset:1024 lds\n"
"v_mfma_f32_16x16x32_bf16 a[104:107],v[136:139],v[180:183],a[104:107]\n"
"v_mfma_f32_16x16x32_bf16 a[112:115],v[136:139],v[184:187],a[112:115]\n"
"v_mfma_f32_16x16x32_bf16 a[112:115],v[200:203],v[248:251],a[112:115]\n"
"v_mfma_f32_16x16x32_bf16 a[120:123],v[200:203],v[252:255],a[120:123]\n"
"buffer_load_dwordx4 v113,s[20:23],s77 offen offset:2048 lds\n"
"v_mfma_f32_16x16x32_bf16 a[120:123],v[136:139],v[188:191],a[120:123]\n"
"v_mfma_f32_16x16x32_bf16 a[68:71],v[140:143],v[160:163],a[68:71]\n"
"v_mfma_f32_16x16x32_bf16 a[68:71],v[204:207],v[224:227],a[68:71]\n"
"v_mfma_f32_16x16x32_bf16 a[76:79],v[204:207],v[228:231],a[76:79]\n"
"buffer_load_dwordx4 v114,s[20:23],s77 offen offset:3072 lds\n"
"v_mfma_f32_16x16x32_bf16 a[76:79],v[140:143],v[164:167],a[76:79]\n"
"v_mfma_f32_16x16x32_bf16 a[84:87],v[140:143],v[168:171],a[84:87]\n"
"v_mfma_f32_16x16x32_bf16 a[84:87],v[204:207],v[232:235],a[84:87]\n"
"v_mfma_f32_16x16x32_bf16 a[92:95],v[204:207],v[236:239],a[92:95]\n"
"s_mov_b32 s76,768\n"
"s_add_u32 m0,s79,0\n"
"v_mfma_f32_16x16x32_bf16 a[92:95],v[140:143],v[172:175],a[92:95]\n"
"buffer_load_dwordx4 v106,s[16:19],s76 offen offset:0 lds\n"
"v_mfma_f32_16x16x32_bf16 a[100:103],v[140:143],v[176:179],a[100:103]\n"
"v_mfma_f32_16x16x32_bf16 a[100:103],v[204:207],v[240:243],a[100:103]\n"
"v_mfma_f32_16x16x32_bf16 a[108:111],v[204:207],v[244:247],a[108:111]\n"
"v_mfma_f32_16x16x32_bf16 a[108:111],v[140:143],v[180:183],a[108:111]\n"
"v_mfma_f32_16x16x32_bf16 a[116:119],v[140:143],v[184:187],a[116:119]\n"
"buffer_load_dwordx4 v107,s[16:19],s76 offen offset:1024 lds\n"
"v_mfma_f32_16x16x32_bf16 a[116:119],v[204:207],v[248:251],a[116:119]\n"
"v_mfma_f32_16x16x32_bf16 a[124:127],v[204:207],v[252:255],a[124:127]\n"
"v_mfma_f32_16x16x32_bf16 a[124:127],v[140:143],v[188:191],a[124:127]\n"
"v_mfma_f32_16x16x32_bf16 a[128:131],v[144:147],v[160:163],a[128:131]\n"
"v_mfma_f32_16x16x32_bf16 a[128:131],v[208:211],v[224:227],a[128:131]\n"
"buffer_load_dwordx4 v108,s[16:19],s76 offen offset:2048 lds\n"
"v_mfma_f32_16x16x32_bf16 a[136:139],v[208:211],v[228:231],a[136:139]\n"
"v_mfma_f32_16x16x32_bf16 a[136:139],v[144:147],v[164:167],a[136:139]\n"
"v_mfma_f32_16x16x32_bf16 a[144:147],v[144:147],v[168:171],a[144:147]\n"
"v_mfma_f32_16x16x32_bf16 a[144:147],v[208:211],v[232:235],a[144:147]\n"
"v_mfma_f32_16x16x32_bf16 a[152:155],v[208:211],v[236:239],a[152:155]\n"
"buffer_load_dwordx4 v109,s[16:19],s76 offen offset:3072 lds\n"
"v_mfma_f32_16x16x32_bf16 a[152:155],v[144:147],v[172:175],a[152:155]\n"
"v_mfma_f32_16x16x32_bf16 a[160:163],v[144:147],v[176:179],a[160:163]\n"
"v_mfma_f32_16x16x32_bf16 a[160:163],v[208:211],v[240:243],a[160:163]\n"
"v_mfma_f32_16x16x32_bf16 a[168:171],v[208:211],v[244:247],a[168:171]\n"
"s_mov_b32 s76,262912\n"
"s_add_u32 m0,s79,4096\n"
"v_mfma_f32_16x16x32_bf16 a[168:171],v[144:147],v[180:183],a[168:171]\n"
"buffer_load_dwordx4 v106,s[16:19],s76 offen offset:0 lds\n"
"v_mfma_f32_16x16x32_bf16 a[176:179],v[144:147],v[184:187],a[176:179]\n"
"v_mfma_f32_16x16x32_bf16 a[176:179],v[208:211],v[248:251],a[176:179]\n"
"v_mfma_f32_16x16x32_bf16 a[184:187],v[208:211],v[252:255],a[184:187]\n"
"v_mfma_f32_16x16x32_bf16 a[184:187],v[144:147],v[188:191],a[184:187]\n"
"v_mfma_f32_16x16x32_bf16 a[132:135],v[148:151],v[160:163],a[132:135]\n"
"buffer_load_dwordx4 v107,s[16:19],s76 offen offset:1024 lds\n"
"v_mfma_f32_16x16x32_bf16 a[132:135],v[212:215],v[224:227],a[132:135]\n"
"v_mfma_f32_16x16x32_bf16 a[140:143],v[212:215],v[228:231],a[140:143]\n"
"v_mfma_f32_16x16x32_bf16 a[140:143],v[148:151],v[164:167],a[140:143]\n"
"v_mfma_f32_16x16x32_bf16 a[148:151],v[148:151],v[168:171],a[148:151]\n"
"v_mfma_f32_16x16x32_bf16 a[148:151],v[212:215],v[232:235],a[148:151]\n"
"buffer_load_dwordx4 v108,s[16:19],s76 offen offset:2048 lds\n"
"v_mfma_f32_16x16x32_bf16 a[156:159],v[212:215],v[236:239],a[156:159]\n"
"v_mfma_f32_16x16x32_bf16 a[156:159],v[148:151],v[172:175],a[156:159]\n"
"v_mfma_f32_16x16x32_bf16 a[164:167],v[148:151],v[176:179],a[164:167]\n"
"v_mfma_f32_16x16x32_bf16 a[164:167],v[212:215],v[240:243],a[164:167]\n"
"v_mfma_f32_16x16x32_bf16 a[172:175],v[212:215],v[244:247],a[172:175]\n"
"buffer_load_dwordx4 v109,s[16:19],s76 offen offset:3072 lds\n"
"v_mfma_f32_16x16x32_bf16 a[172:175],v[148:151],v[180:183],a[172:175]\n"
"s_mov_b32 s87,3145728\n"
"v_mfma_f32_16x16x32_bf16 a[180:183],v[148:151],v[184:187],a[180:183]\n"
"buffer_load_dword v115,v112,s[40:43],s87 offen\n"
"v_mfma_f32_16x16x32_bf16 a[180:183],v[212:215],v[248:251],a[180:183]\n"
"s_mov_b32 s87,4194304\n"
"v_mfma_f32_16x16x32_bf16 a[188:191],v[212:215],v[252:255],a[188:191]\n"
"buffer_load_dword v115,v112,s[40:43],s87 offen\n"
"v_mfma_f32_16x16x32_bf16 a[188:191],v[148:151],v[188:191],a[188:191]\n"
"s_mov_b32 s87,5242880\n"
"s_waitcnt vmcnt(33)\n"
"s_barrier\n"
"v_mfma_f32_16x16x32_bf16 a[192:195],v[216:219],v[224:227],a[192:195]\n"
"ds_read_b128 v[152:155],v80 offset:45056\n"
"buffer_load_dword v115,v112,s[40:43],s87 offen\n"
"v_mfma_f32_16x16x32_bf16 a[196:199],v[220:223],v[224:227],a[196:199]\n"
"ds_read_b128 v[156:159],v80 offset:47104\n"
"v_mfma_f32_16x16x32_bf16 a[204:207],v[220:223],v[228:231],a[204:207]\n"
"ds_read_b64_tr_b16 v[160:161],v98 offset:49152\n"
"v_mfma_f32_16x16x32_bf16 a[200:203],v[216:219],v[228:231],a[200:203]\n"
"ds_read_b64_tr_b16 v[162:163],v98 offset:50176\n"
"v_mfma_f32_16x16x32_bf16 a[208:211],v[216:219],v[232:235],a[208:211]\n"
"ds_read_b64_tr_b16 v[164:165],v99 offset:49152\n"
"v_mfma_f32_16x16x32_bf16 a[212:215],v[220:223],v[232:235],a[212:215]\n"
"ds_read_b64_tr_b16 v[166:167],v99 offset:50176\n"
"v_mfma_f32_16x16x32_bf16 a[220:223],v[220:223],v[236:239],a[220:223]\n"
"ds_read_b64_tr_b16 v[168:169],v100 offset:49152\n"
"v_mfma_f32_16x16x32_bf16 a[216:219],v[216:219],v[236:239],a[216:219]\n"
"ds_read_b64_tr_b16 v[170:171],v100 offset:50176\n"
"v_mfma_f32_16x16x32_bf16 a[224:227],v[216:219],v[240:243],a[224:227]\n"
"ds_read_b64_tr_b16 v[172:173],v101 offset:49152\n"
"v_mfma_f32_16x16x32_bf16 a[228:231],v[220:223],v[240:243],a[228:231]\n"
"ds_read_b64_tr_b16 v[174:175],v101 offset:50176\n"
"v_mfma_f32_16x16x32_bf16 a[236:239],v[220:223],v[244:247],a[236:239]\n"
"ds_read_b64_tr_b16 v[176:177],v102 offset:49152\n"
"v_mfma_f32_16x16x32_bf16 a[232:235],v[216:219],v[244:247],a[232:235]\n"
"ds_read_b64_tr_b16 v[178:179],v102 offset:50176\n"
"v_mfma_f32_16x16x32_bf16 a[240:243],v[216:219],v[248:251],a[240:243]\n"
"ds_read_b64_tr_b16 v[180:181],v103 offset:49152\n"
"v_mfma_f32_16x16x32_bf16 a[244:247],v[220:223],v[248:251],a[244:247]\n"
"ds_read_b64_tr_b16 v[182:183],v103 offset:50176\n"
"v_mfma_f32_16x16x32_bf16 a[252:255],v[220:223],v[252:255],a[252:255]\n"
"ds_read_b64_tr_b16 v[184:185],v104 offset:49152\n"
"ds_read_b64_tr_b16 v[186:187],v104 offset:50176\n"
"v_mfma_f32_16x16x32_bf16 a[248:251],v[216:219],v[252:255],a[248:251]\n"
"s_waitcnt vmcnt(19)\n"
"s_waitcnt lgkmcnt(10)\n"
"v_mfma_f32_16x16x32_bf16 a[192:195],v[152:155],v[160:163],a[192:195]\n"
"ds_read_b64_tr_b16 v[188:189],v105 offset:49152\n"
"ds_read_b64_tr_b16 v[190:191],v105 offset:50176\n"
"v_mfma_f32_16x16x32_bf16 a[196:199],v[156:159],v[160:163],a[196:199]\n"
"buffer_store_dwordx2 v[124:125],off,s[24:27],s57 sc1\n"
"v_mfma_f32_16x16x32_bf16 a[204:207],v[156:159],v[164:167],a[204:207]\n"
"ds_read_b128 v[128:131],v80 offset:32768\n"
"v_mfma_f32_16x16x32_bf16 a[200:203],v[152:155],v[164:167],a[200:203]\n"
"ds_read_b128 v[192:195],v81 offset:32768\n"
"s_waitcnt lgkmcnt(10)\n"
"v_mfma_f32_16x16x32_bf16 a[208:211],v[152:155],v[168:171],a[208:211]\n"
"ds_read_b64_tr_b16 v[224:225],v98 offset:32768\n"
"v_mfma_f32_16x16x32_bf16 a[212:215],v[156:159],v[168:171],a[212:215]\n"
"ds_read_b64_tr_b16 v[226:227],v98 offset:33792\n"
"v_mfma_f32_16x16x32_bf16 a[220:223],v[156:159],v[172:175],a[220:223]\n"
"ds_read_b64_tr_b16 v[228:229],v99 offset:32768\n"
"v_mfma_f32_16x16x32_bf16 a[216:219],v[152:155],v[172:175],a[216:219]\n"
"ds_read_b64_tr_b16 v[230:231],v99 offset:33792\n"
"s_waitcnt lgkmcnt(10)\n"
"v_mfma_f32_16x16x32_bf16 a[224:227],v[152:155],v[176:179],a[224:227]\n"
"ds_read_b64_tr_b16 v[232:233],v100 offset:32768\n"
"v_mfma_f32_16x16x32_bf16 a[228:231],v[156:159],v[176:179],a[228:231]\n"
"ds_read_b64_tr_b16 v[234:235],v100 offset:33792\n"
"v_mfma_f32_16x16x32_bf16 a[236:239],v[156:159],v[180:183],a[236:239]\n"
"ds_read_b64_tr_b16 v[236:237],v101 offset:32768\n"
"v_mfma_f32_16x16x32_bf16 a[232:235],v[152:155],v[180:183],a[232:235]\n"
"ds_read_b64_tr_b16 v[238:239],v101 offset:33792\n"
"s_waitcnt lgkmcnt(10)\n"
"v_mfma_f32_16x16x32_bf16 a[240:243],v[152:155],v[184:187],a[240:243]\n"
"ds_read_b64_tr_b16 v[240:241],v102 offset:32768\n"
"v_mfma_f32_16x16x32_bf16 a[244:247],v[156:159],v[184:187],a[244:247]\n"
"ds_read_b64_tr_b16 v[242:243],v102 offset:33792\n"
"v_mfma_f32_16x16x32_bf16 a[252:255],v[156:159],v[188:191],a[252:255]\n"
"ds_read_b64_tr_b16 v[244:245],v103 offset:32768\n"
"v_mfma_f32_16x16x32_bf16 a[248:251],v[152:155],v[188:191],a[248:251]\n"
"ds_read_b64_tr_b16 v[246:247],v103 offset:33792\n"
"s_waitcnt lgkmcnt(8)\n"
"v_mfma_f32_16x16x32_bf16 a[0:3],v[128:131],v[160:163],a[0:3]\n"
"ds_read_b64_tr_b16 v[248:249],v104 offset:32768\n"
"v_mfma_f32_16x16x32_bf16 a[0:3],v[192:195],v[224:227],a[0:3]\n"
"ds_read_b64_tr_b16 v[250:251],v104 offset:33792\n"
"v_mfma_f32_16x16x32_bf16 a[8:11],v[192:195],v[228:231],a[8:11]\n"
"ds_read_b64_tr_b16 v[252:253],v105 offset:32768\n"
"v_mfma_f32_16x16x32_bf16 a[8:11],v[128:131],v[164:167],a[8:11]\n"
"ds_read_b64_tr_b16 v[254:255],v105 offset:33792\n"
"s_waitcnt lgkmcnt(8)\n"
"v_mfma_f32_16x16x32_bf16 a[16:19],v[128:131],v[168:171],a[16:19]\n"
"ds_read_b128 v[132:135],v80 offset:34816\n"
"v_mfma_f32_16x16x32_bf16 a[16:19],v[192:195],v[232:235],a[16:19]\n"
"ds_read_b128 v[196:199],v81 offset:34816\n"
"v_mfma_f32_16x16x32_bf16 a[24:27],v[192:195],v[236:239],a[24:27]\n"
"ds_read_b128 v[136:139],v80 offset:36864\n"
"v_mfma_f32_16x16x32_bf16 a[24:27],v[128:131],v[172:175],a[24:27]\n"
"ds_read_b128 v[200:203],v81 offset:36864\n"
"s_waitcnt lgkmcnt(8)\n"
"v_mfma_f32_16x16x32_bf16 a[32:35],v[128:131],v[176:179],a[32:35]\n"
"ds_read_b128 v[140:143],v80 offset:38912\n"
"v_mfma_f32_16x16x32_bf16 a[32:35],v[192:195],v[240:243],a[32:35]\n"
"ds_read_b128 v[204:207],v81 offset:38912\n"
"v_mfma_f32_16x16x32_bf16 a[40:43],v[192:195],v[244:247],a[40:43]\n"
"ds_read_b128 v[144:147],v80 offset:40960\n"
"v_mfma_f32_16x16x32_bf16 a[40:43],v[128:131],v[180:183],a[40:43]\n"
"ds_read_b128 v[208:211],v81 offset:40960\n"
"s_waitcnt lgkmcnt(8)\n"
"v_mfma_f32_16x16x32_bf16 a[48:51],v[128:131],v[184:187],a[48:51]\n"
"ds_read_b128 v[148:151],v80 offset:43008\n"
"v_mfma_f32_16x16x32_bf16 a[48:51],v[192:195],v[248:251],a[48:51]\n"
"ds_read_b128 v[212:215],v81 offset:43008\n"
"v_mfma_f32_16x16x32_bf16 a[56:59],v[192:195],v[252:255],a[56:59]\n"
"ds_read_b128 v[216:219],v81 offset:45056\n"
"v_mfma_f32_16x16x32_bf16 a[56:59],v[128:131],v[188:191],a[56:59]\n"
"ds_read_b128 v[220:223],v81 offset:47104\n"
"s_waitcnt lgkmcnt(10)\n"
"v_mfma_f32_16x16x32_bf16 a[4:7],v[132:135],v[160:163],a[4:7]\n"
"v_mfma_f32_16x16x32_bf16 a[4:7],v[196:199],v[224:227],a[4:7]\n"
"v_mfma_f32_16x16x32_bf16 a[12:15],v[196:199],v[228:231],a[12:15]\n"
"v_mfma_f32_16x16x32_bf16 a[12:15],v[132:135],v[164:167],a[12:15]\n"
"v_mfma_f32_16x16x32_bf16 a[20:23],v[132:135],v[168:171],a[20:23]\n"
"s_waitcnt lgkmcnt(0)\n"
"s_barrier\n"
"v_mfma_f32_16x16x32_bf16 a[20:23],v[196:199],v[232:235],a[20:23]\n"
"s_mov_b32 s77,3145728\n"
"s_add_u32 m0,s78,147456\n"
"v_mfma_f32_16x16x32_bf16 a[28:31],v[196:199],v[236:239],a[28:31]\n"
"buffer_load_dwordx4 v110,s[20:23],s77 offen offset:0 lds\n"
"v_mfma_f32_16x16x32_bf16 a[28:31],v[132:135],v[172:175],a[28:31]\n"
"v_mfma_f32_16x16x32_bf16 a[36:39],v[132:135],v[176:179],a[36:39]\n"
"v_mfma_f32_16x16x32_bf16 a[36:39],v[196:199],v[240:243],a[36:39]\n"
"v_mfma_f32_16x16x32_bf16 a[44:47],v[196:199],v[244:247],a[44:47]\n"
"buffer_load_dwordx4 v111,s[20:23],s77 offen offset:1024 lds\n"
"v_mfma_f32_16x16x32_bf16 a[44:47],v[132:135],v[180:183],a[44:47]\n"
"v_mfma_f32_16x16x32_bf16 a[52:55],v[132:135],v[184:187],a[52:55]\n"
"v_mfma_f32_16x16x32_bf16 a[52:55],v[196:199],v[248:251],a[52:55]\n"
"v_mfma_f32_16x16x32_bf16 a[60:63],v[196:199],v[252:255],a[60:63]\n"
"buffer_load_dwordx4 v113,s[20:23],s77 offen offset:2048 lds\n"
"v_mfma_f32_16x16x32_bf16 a[60:63],v[132:135],v[188:191],a[60:63]\n"
"v_mfma_f32_16x16x32_bf16 a[64:67],v[136:139],v[160:163],a[64:67]\n"
"v_mfma_f32_16x16x32_bf16 a[64:67],v[200:203],v[224:227],a[64:67]\n"
"v_mfma_f32_16x16x32_bf16 a[72:75],v[200:203],v[228:231],a[72:75]\n"
"buffer_load_dwordx4 v114,s[20:23],s77 offen offset:3072 lds\n"
"v_mfma_f32_16x16x32_bf16 a[72:75],v[136:139],v[164:167],a[72:75]\n"
"v_mfma_f32_16x16x32_bf16 a[80:83],v[136:139],v[168:171],a[80:83]\n"
"v_mfma_f32_16x16x32_bf16 a[80:83],v[200:203],v[232:235],a[80:83]\n"
"s_mov_b32 s77,3407872\n"
"s_add_u32 m0,s78,131072\n"
"v_mfma_f32_16x16x32_bf16 a[88:91],v[200:203],v[236:239],a[88:91]\n"
"buffer_load_dwordx4 v110,s[20:23],s77 offen offset:0 lds\n"
"v_mfma_f32_16x16x32_bf16 a[88:91],v[136:139],v[172:175],a[88:91]\n"
"v_mfma_f32_16x16x32_bf16 a[96:99],v[136:139],v[176:179],a[96:99]\n"
"v_mfma_f32_16x16x32_bf16 a[96:99],v[200:203],v[240:243],a[96:99]\n"
"v_mfma_f32_16x16x32_bf16 a[104:107],v[200:203],v[244:247],a[104:107]\n"
"buffer_load_dwordx4 v111,s[20:23],s77 offen offset:1024 lds\n"
"v_mfma_f32_16x16x32_bf16 a[104:107],v[136:139],v[180:183],a[104:107]\n"
"v_mfma_f32_16x16x32_bf16 a[112:115],v[136:139],v[184:187],a[112:115]\n"
"v_mfma_f32_16x16x32_bf16 a[112:115],v[200:203],v[248:251],a[112:115]\n"
"v_mfma_f32_16x16x32_bf16 a[120:123],v[200:203],v[252:255],a[120:123]\n"
"buffer_load_dwordx4 v113,s[20:23],s77 offen offset:2048 lds\n"
"v_mfma_f32_16x16x32_bf16 a[120:123],v[136:139],v[188:191],a[120:123]\n"
"v_mfma_f32_16x16x32_bf16 a[68:71],v[140:143],v[160:163],a[68:71]\n"
"v_mfma_f32_16x16x32_bf16 a[68:71],v[204:207],v[224:227],a[68:71]\n"
"v_mfma_f32_16x16x32_bf16 a[76:79],v[204:207],v[228:231],a[76:79]\n"
"buffer_load_dwordx4 v114,s[20:23],s77 offen offset:3072 lds\n"
"v_mfma_f32_16x16x32_bf16 a[76:79],v[140:143],v[164:167],a[76:79]\n"
"v_mfma_f32_16x16x32_bf16 a[84:87],v[140:143],v[168:171],a[84:87]\n"
"v_mfma_f32_16x16x32_bf16 a[84:87],v[204:207],v[232:235],a[84:87]\n"
"v_mfma_f32_16x16x32_bf16 a[92:95],v[204:207],v[236:239],a[92:95]\n"
"s_mov_b32 s76,896\n"
"s_add_u32 m0,s79,32768\n"
"v_mfma_f32_16x16x32_bf16 a[92:95],v[140:143],v[172:175],a[92:95]\n"
"buffer_load_dwordx4 v106,s[16:19],s76 offen offset:0 lds\n"
"v_mfma_f32_16x16x32_bf16 a[100:103],v[140:143],v[176:179],a[100:103]\n"
"v_mfma_f32_16x16x32_bf16 a[100:103],v[204:207],v[240:243],a[100:103]\n"
"v_mfma_f32_16x16x32_bf16 a[108:111],v[204:207],v[244:247],a[108:111]\n"
"v_mfma_f32_16x16x32_bf16 a[108:111],v[140:143],v[180:183],a[108:111]\n"
"v_mfma_f32_16x16x32_bf16 a[116:119],v[140:143],v[184:187],a[116:119]\n"
"buffer_load_dwordx4 v107,s[16:19],s76 offen offset:1024 lds\n"
"v_mfma_f32_16x16x32_bf16 a[116:119],v[204:207],v[248:251],a[116:119]\n"
"v_mfma_f32_16x16x32_bf16 a[124:127],v[204:207],v[252:255],a[124:127]\n"
"v_mfma_f32_16x16x32_bf16 a[124:127],v[140:143],v[188:191],a[124:127]\n"
"v_mfma_f32_16x16x32_bf16 a[128:131],v[144:147],v[160:163],a[128:131]\n"
"v_mfma_f32_16x16x32_bf16 a[128:131],v[208:211],v[224:227],a[128:131]\n"
"buffer_load_dwordx4 v108,s[16:19],s76 offen offset:2048 lds\n"
"v_mfma_f32_16x16x32_bf16 a[136:139],v[208:211],v[228:231],a[136:139]\n"
"v_mfma_f32_16x16x32_bf16 a[136:139],v[144:147],v[164:167],a[136:139]\n"
"v_mfma_f32_16x16x32_bf16 a[144:147],v[144:147],v[168:171],a[144:147]\n"
"v_mfma_f32_16x16x32_bf16 a[144:147],v[208:211],v[232:235],a[144:147]\n"
"v_mfma_f32_16x16x32_bf16 a[152:155],v[208:211],v[236:239],a[152:155]\n"
"buffer_load_dwordx4 v109,s[16:19],s76 offen offset:3072 lds\n"
"v_mfma_f32_16x16x32_bf16 a[152:155],v[144:147],v[172:175],a[152:155]\n"
"v_mfma_f32_16x16x32_bf16 a[160:163],v[144:147],v[176:179],a[160:163]\n"
"v_mfma_f32_16x16x32_bf16 a[160:163],v[208:211],v[240:243],a[160:163]\n"
"v_mfma_f32_16x16x32_bf16 a[168:171],v[208:211],v[244:247],a[168:171]\n"
"s_mov_b32 s76,263040\n"
"s_add_u32 m0,s79,36864\n"
"v_mfma_f32_16x16x32_bf16 a[168:171],v[144:147],v[180:183],a[168:171]\n"
"buffer_load_dwordx4 v106,s[16:19],s76 offen offset:0 lds\n"
"v_mfma_f32_16x16x32_bf16 a[176:179],v[144:147],v[184:187],a[176:179]\n"
"v_mfma_f32_16x16x32_bf16 a[176:179],v[208:211],v[248:251],a[176:179]\n"
"v_mfma_f32_16x16x32_bf16 a[184:187],v[208:211],v[252:255],a[184:187]\n"
"v_mfma_f32_16x16x32_bf16 a[184:187],v[144:147],v[188:191],a[184:187]\n"
"v_mfma_f32_16x16x32_bf16 a[132:135],v[148:151],v[160:163],a[132:135]\n"
"buffer_load_dwordx4 v107,s[16:19],s76 offen offset:1024 lds\n"
"v_mfma_f32_16x16x32_bf16 a[132:135],v[212:215],v[224:227],a[132:135]\n"
"v_mfma_f32_16x16x32_bf16 a[140:143],v[212:215],v[228:231],a[140:143]\n"
"v_mfma_f32_16x16x32_bf16 a[140:143],v[148:151],v[164:167],a[140:143]\n"
"v_mfma_f32_16x16x32_bf16 a[148:151],v[148:151],v[168:171],a[148:151]\n"
"v_mfma_f32_16x16x32_bf16 a[148:151],v[212:215],v[232:235],a[148:151]\n"
"buffer_load_dwordx4 v108,s[16:19],s76 offen offset:2048 lds\n"
"v_mfma_f32_16x16x32_bf16 a[156:159],v[212:215],v[236:239],a[156:159]\n"
"v_mfma_f32_16x16x32_bf16 a[156:159],v[148:151],v[172:175],a[156:159]\n"
"v_mfma_f32_16x16x32_bf16 a[164:167],v[148:151],v[176:179],a[164:167]\n"
"v_mfma_f32_16x16x32_bf16 a[164:167],v[212:215],v[240:243],a[164:167]\n"
"v_mfma_f32_16x16x32_bf16 a[172:175],v[212:215],v[244:247],a[172:175]\n"
"buffer_load_dwordx4 v109,s[16:19],s76 offen offset:3072 lds\n"
"v_mfma_f32_16x16x32_bf16 a[172:175],v[148:151],v[180:183],a[172:175]\n"
"s_mov_b32 s87,6291456\n"
"v_mfma_f32_16x16x32_bf16 a[180:183],v[148:151],v[184:187],a[180:183]\n"
"buffer_load_dword v115,v112,s[40:43],s87 offen\n"
"v_mfma_f32_16x16x32_bf16 a[180:183],v[212:215],v[248:251],a[180:183]\n"
"s_mov_b32 s87,7340032\n"
"v_mfma_f32_16x16x32_bf16 a[188:191],v[212:215],v[252:255],a[188:191]\n"
"buffer_load_dword v115,v112,s[40:43],s87 offen\n"
"v_mfma_f32_16x16x32_bf16 a[188:191],v[148:151],v[188:191],a[188:191]\n"
"s_waitcnt vmcnt(30)\n"
"s_barrier\n"
"v_mfma_f32_16x16x32_bf16 a[192:195],v[216:219],v[224:227],a[192:195]\n"
"ds_read_b128 v[152:155],v82 offset:12288\n"
"v_mfma_f32_16x16x32_bf16 a[196:199],v[220:223],v[224:227],a[196:199]\n"
"ds_read_b128 v[156:159],v82 offset:14336\n"
"v_mfma_f32_16x16x32_bf16 a[204:207],v[220:223],v[228:231],a[204:207]\n"
"ds_read_b64_tr_b16 v[160:161],v98 offset:0\n"
"v_mfma_f32_16x16x32_bf16 a[200:203],v[216:219],v[228:231],a[200:203]\n"
"ds_read_b64_tr_b16 v[162:163],v98 offset:1024\n"
"v_mfma_f32_16x16x32_bf16 a[208:211],v[216:219],v[232:235],a[208:211]\n"
"ds_read_b64_tr_b16 v[164:165],v99 offset:0\n"
"v_mfma_f32_16x16x32_bf16 a[212:215],v[220:223],v[232:235],a[212:215]\n"
"ds_read_b64_tr_b16 v[166:167],v99 offset:1024\n"
"v_mfma_f32_16x16x32_bf16 a[220:223],v[220:223],v[236:239],a[220:223]\n"
"ds_read_b64_tr_b16 v[168:169],v100 offset:0\n"
"v_mfma_f32_16x16x32_bf16 a[216:219],v[216:219],v[236:239],a[216:219]\n"
"ds_read_b64_tr_b16 v[170:171],v100 offset:1024\n"
"v_mfma_f32_16x16x32_bf16 a[224:227],v[216:219],v[240:243],a[224:227]\n"
"ds_read_b64_tr_b16 v[172:173],v101 offset:0\n"
"v_mfma_f32_16x16x32_bf16 a[228:231],v[220:223],v[240:243],a[228:231]\n"
"ds_read_b64_tr_b16 v[174:175],v101 offset:1024\n"
"v_mfma_f32_16x16x32_bf16 a[236:239],v[220:223],v[244:247],a[236:239]\n"
"ds_read_b64_tr_b16 v[176:177],v102 offset:0\n"
"v_mfma_f32_16x16x32_bf16 a[232:235],v[216:219],v[244:247],a[232:235]\n"
"ds_read_b64_tr_b16 v[178:179],v102 offset:1024\n"
"v_mfma_f32_16x16x32_bf16 a[240:243],v[216:219],v[248:251],a[240:243]\n"
"ds_read_b64_tr_b16 v[180:181],v103 offset:0\n"
"v_mfma_f32_16x16x32_bf16 a[244:247],v[220:223],v[248:251],a[244:247]\n"
"ds_read_b64_tr_b16 v[182:183],v103 offset:1024\n"
"v_mfma_f32_16x16x32_bf16 a[252:255],v[220:223],v[252:255],a[252:255]\n"
"ds_read_b64_tr_b16 v[184:185],v104 offset:0\n"
"v_mfma_f32_16x16x32_bf16 a[248:251],v[216:219],v[252:255],a[248:251]\n"
"ds_read_b64_tr_b16 v[186:187],v104 offset:1024\n"
"s_waitcnt lgkmcnt(10)\n"
"v_mfma_f32_16x16x32_bf16 a[192:195],v[152:155],v[160:163],a[192:195]\n"
"ds_read_b64_tr_b16 v[188:189],v105 offset:0\n"
"v_mfma_f32_16x16x32_bf16 a[196:199],v[156:159],v[160:163],a[196:199]\n"
"ds_read_b64_tr_b16 v[190:191],v105 offset:1024\n"
"v_mfma_f32_16x16x32_bf16 a[204:207],v[156:159],v[164:167],a[204:207]\n"
"ds_read_b128 v[128:131],v82 offset:0\n"
"v_mfma_f32_16x16x32_bf16 a[200:203],v[152:155],v[164:167],a[200:203]\n"
"ds_read_b128 v[192:195],v83 offset:0\n"
"s_waitcnt lgkmcnt(10)\n"
"v_mfma_f32_16x16x32_bf16 a[208:211],v[152:155],v[168:171],a[208:211]\n"
"ds_read_b64_tr_b16 v[224:225],v98 offset:16384\n"
"v_mfma_f32_16x16x32_bf16 a[212:215],v[156:159],v[168:171],a[212:215]\n"
"ds_read_b64_tr_b16 v[226:227],v98 offset:17408\n"
"v_mfma_f32_16x16x32_bf16 a[220:223],v[156:159],v[172:175],a[220:223]\n"
"ds_read_b64_tr_b16 v[228:229],v99 offset:16384\n"
"v_mfma_f32_16x16x32_bf16 a[216:219],v[152:155],v[172:175],a[216:219]\n"
"ds_read_b64_tr_b16 v[230:231],v99 offset:17408\n"
"s_waitcnt lgkmcnt(10)\n"
"v_mfma_f32_16x16x32_bf16 a[224:227],v[152:155],v[176:179],a[224:227]\n"
"ds_read_b64_tr_b16 v[232:233],v100 offset:16384\n"
"v_mfma_f32_16x16x32_bf16 a[228:231],v[156:159],v[176:179],a[228:231]\n"
"ds_read_b64_tr_b16 v[234:235],v100 offset:17408\n"
"v_mfma_f32_16x16x32_bf16 a[236:239],v[156:159],v[180:183],a[236:239]\n"
"ds_read_b64_tr_b16 v[236:237],v101 offset:16384\n"
"v_mfma_f32_16x16x32_bf16 a[232:235],v[152:155],v[180:183],a[232:235]\n"
"ds_read_b64_tr_b16 v[238:239],v101 offset:17408\n"
"s_waitcnt lgkmcnt(10)\n"
"v_mfma_f32_16x16x32_bf16 a[240:243],v[152:155],v[184:187],a[240:243]\n"
"ds_read_b64_tr_b16 v[240:241],v102 offset:16384\n"
"v_mfma_f32_16x16x32_bf16 a[244:247],v[156:159],v[184:187],a[244:247]\n"
"ds_read_b64_tr_b16 v[242:243],v102 offset:17408\n"
"v_mfma_f32_16x16x32_bf16 a[252:255],v[156:159],v[188:191],a[252:255]\n"
"ds_read_b64_tr_b16 v[244:245],v103 offset:16384\n"
"v_mfma_f32_16x16x32_bf16 a[248:251],v[152:155],v[188:191],a[248:251]\n"
"ds_read_b64_tr_b16 v[246:247],v103 offset:17408\n"
"s_waitcnt lgkmcnt(8)\n"
"v_mfma_f32_16x16x32_bf16 a[0:3],v[128:131],v[160:163],a[0:3]\n"
"ds_read_b64_tr_b16 v[248:249],v104 offset:16384\n"
"v_mfma_f32_16x16x32_bf16 a[0:3],v[192:195],v[224:227],a[0:3]\n"
"ds_read_b64_tr_b16 v[250:251],v104 offset:17408\n"
"v_mfma_f32_16x16x32_bf16 a[8:11],v[192:195],v[228:231],a[8:11]\n"
"ds_read_b64_tr_b16 v[252:253],v105 offset:16384\n"
"v_mfma_f32_16x16x32_bf16 a[8:11],v[128:131],v[164:167],a[8:11]\n"
"ds_read_b64_tr_b16 v[254:255],v105 offset:17408\n"
"s_waitcnt lgkmcnt(8)\n"
"v_mfma_f32_16x16x32_bf16 a[16:19],v[128:131],v[168:171],a[16:19]\n"
"ds_read_b128 v[132:135],v82 offset:2048\n"
"v_mfma_f32_16x16x32_bf16 a[16:19],v[192:195],v[232:235],a[16:19]\n"
"ds_read_b128 v[196:199],v83 offset:2048\n"
"v_mfma_f32_16x16x32_bf16 a[24:27],v[192:195],v[236:239],a[24:27]\n"
"ds_read_b128 v[136:139],v82 offset:4096\n"
"v_mfma_f32_16x16x32_bf16 a[24:27],v[128:131],v[172:175],a[24:27]\n"
"ds_read_b128 v[200:203],v83 offset:4096\n"
"s_waitcnt lgkmcnt(8)\n"
"v_mfma_f32_16x16x32_bf16 a[32:35],v[128:131],v[176:179],a[32:35]\n"
"ds_read_b128 v[140:143],v82 offset:6144\n"
"v_mfma_f32_16x16x32_bf16 a[32:35],v[192:195],v[240:243],a[32:35]\n"
"ds_read_b128 v[204:207],v83 offset:6144\n"
"v_mfma_f32_16x16x32_bf16 a[40:43],v[192:195],v[244:247],a[40:43]\n"
"ds_read_b128 v[144:147],v82 offset:8192\n"
"v_mfma_f32_16x16x32_bf16 a[40:43],v[128:131],v[180:183],a[40:43]\n"
"ds_read_b128 v[208:211],v83 offset:8192\n"
"s_waitcnt lgkmcnt(8)\n"
"v_mfma_f32_16x16x32_bf16 a[48:51],v[128:131],v[184:187],a[48:51]\n"
"ds_read_b128 v[148:151],v82 offset:10240\n"
"v_mfma_f32_16x16x32_bf16 a[48:51],v[192:195],v[248:251],a[48:51]\n"
"ds_read_b128 v[212:215],v83 offset:10240\n"
"v_mfma_f32_16x16x32_bf16 a[56:59],v[192:195],v[252:255],a[56:59]\n"
"ds_read_b128 v[216:219],v83 offset:12288\n"
"v_mfma_f32_16x16x32_bf16 a[56:59],v[128:131],v[188:191],a[56:59]\n"
"ds_read_b128 v[220:223],v83 offset:14336\n"
"s_waitcnt lgkmcnt(10)\n"
"v_mfma_f32_16x16x32_bf16 a[4:7],v[132:135],v[160:163],a[4:7]\n"
"v_mfma_f32_16x16x32_bf16 a[4:7],v[196:199],v[224:227],a[4:7]\n"
"buffer_load_dwordx4 v[74:77],v112,s[24:27],s58 offen sc1\n"
"v_mfma_f32_16x16x32_bf16 a[12:15],v[196:199],v[228:231],a[12:15]\n"
"v_mfma_f32_16x16x32_bf16 a[12:15],v[132:135],v[164:167],a[12:15]\n"
"v_mfma_f32_16x16x32_bf16 a[20:23],v[132:135],v[168:171],a[20:23]\n"
"s_waitcnt lgkmcnt(0)\n"
"s_barrier\n"
"v_mfma_f32_16x16x32_bf16 a[20:23],v[196:199],v[232:235],a[20:23]\n"
"s_mov_b32 s77,3670016\n"
"s_add_u32 m0,s78,98304\n"
"v_mfma_f32_16x16x32_bf16 a[28:31],v[196:199],v[236:239],a[28:31]\n"
"buffer_load_dwordx4 v110,s[20:23],s77 offen offset:0 lds\n"
"v_mfma_f32_16x16x32_bf16 a[28:31],v[132:135],v[172:175],a[28:31]\n"
"v_mfma_f32_16x16x32_bf16 a[36:39],v[132:135],v[176:179],a[36:39]\n"
"s_load_dwordx2 s[48:49],s[0:1],56\n"
"v_mfma_f32_16x16x32_bf16 a[36:39],v[196:199],v[240:243],a[36:39]\n"
"v_mfma_f32_16x16x32_bf16 a[44:47],v[196:199],v[244:247],a[44:47]\n"
"buffer_load_dwordx4 v111,s[20:23],s77 offen offset:1024 lds\n"
"v_mfma_f32_16x16x32_bf16 a[44:47],v[132:135],v[180:183],a[44:47]\n"
"v_mfma_f32_16x16x32_bf16 a[52:55],v[132:135],v[184:187],a[52:55]\n"
"v_mfma_f32_16x16x32_bf16 a[52:55],v[196:199],v[248:251],a[52:55]\n"
"v_mfma_f32_16x16x32_bf16 a[60:63],v[196:199],v[252:255],a[60:63]\n"
"buffer_load_dwordx4 v113,s[20:23],s77 offen offset:2048 lds\n"
"v_mfma_f32_16x16x32_bf16 a[60:63],v[132:135],v[188:191],a[60:63]\n"
"v_mfma_f32_16x16x32_bf16 a[64:67],v[136:139],v[160:163],a[64:67]\n"
"v_mfma_f32_16x16x32_bf16 a[64:67],v[200:203],v[224:227],a[64:67]\n"
"v_mfma_f32_16x16x32_bf16 a[72:75],v[200:203],v[228:231],a[72:75]\n"
"buffer_load_dwordx4 v114,s[20:23],s77 offen offset:3072 lds\n"
"v_mfma_f32_16x16x32_bf16 a[72:75],v[136:139],v[164:167],a[72:75]\n"
"v_mfma_f32_16x16x32_bf16 a[80:83],v[136:139],v[168:171],a[80:83]\n"
"v_mfma_f32_16x16x32_bf16 a[80:83],v[200:203],v[232:235],a[80:83]\n"
"s_mov_b32 s77,3932160\n"
"s_add_u32 m0,s78,114688\n"
"v_mfma_f32_16x16x32_bf16 a[88:91],v[200:203],v[236:239],a[88:91]\n"
"buffer_load_dwordx4 v110,s[20:23],s77 offen offset:0 lds\n"
"v_mfma_f32_16x16x32_bf16 a[88:91],v[136:139],v[172:175],a[88:91]\n"
"v_mfma_f32_16x16x32_bf16 a[96:99],v[136:139],v[176:179],a[96:99]\n"
"v_mfma_f32_16x16x32_bf16 a[96:99],v[200:203],v[240:243],a[96:99]\n"
"v_mfma_f32_16x16x32_bf16 a[104:107],v[200:203],v[244:247],a[104:107]\n"
"buffer_load_dwordx4 v111,s[20:23],s77 offen offset:1024 lds\n"
"v_mfma_f32_16x16x32_bf16 a[104:107],v[136:139],v[180:183],a[104:107]\n"
"v_mfma_f32_16x16x32_bf16 a[112:115],v[136:139],v[184:187],a[112:115]\n"
"v_mfma_f32_16x16x32_bf16 a[112:115],v[200:203],v[248:251],a[112:115]\n"
"v_mfma_f32_16x16x32_bf16 a[120:123],v[200:203],v[252:255],a[120:123]\n"
"buffer_load_dwordx4 v113,s[20:23],s77 offen offset:2048 lds\n"
"v_mfma_f32_16x16x32_bf16 a[120:123],v[136:139],v[188:191],a[120:123]\n"
"s_mov_b32 s76,s55\n"
"s_waitcnt vmcnt(7)\n"
"v_mfma_f32_16x16x32_bf16 a[68:71],v[140:143],v[160:163],a[68:71]\n"
"v_xor_b32 v116,s80,v74\n"
"v_xor_b32 v117,s81,v75\n"
"v_mfma_f32_16x16x32_bf16 a[68:71],v[204:207],v[224:227],a[68:71]\n"
"v_or_b32 v116,v116,v117\n"
"v_xor_b32 v117,s80,v76\n"
"v_mfma_f32_16x16x32_bf16 a[76:79],v[204:207],v[228:231],a[76:79]\n"
"buffer_load_dwordx4 v114,s[20:23],s77 offen offset:3072 lds\n"
"v_mfma_f32_16x16x32_bf16 a[76:79],v[140:143],v[164:167],a[76:79]\n"
"v_or_b32 v116,v116,v117\n"
"v_xor_b32 v117,s81,v77\n"
"v_mfma_f32_16x16x32_bf16 a[84:87],v[140:143],v[168:171],a[84:87]\n"
"v_or_b32 v116,v116,v117\n"
"v_cmp_ne_u32 vcc,0,v116\n"
"v_mfma_f32_16x16x32_bf16 a[84:87],v[204:207],v[232:235],a[84:87]\n"
"s_cbranch_vccnz .Lfk_xn1_s\n"
".Lfk_xn1_ok:\n"
"v_mfma_f32_16x16x32_bf16 a[92:95],v[204:207],v[236:239],a[92:95]\n"
"s_add_u32 m0,s78,65536\n"
"v_mfma_f32_16x16x32_bf16 a[92:95],v[140:143],v[172:175],a[92:95]\n"
"buffer_load_dwordx4 v112,s[24:27],s76 offen offset:0 sc1 lds\n"
"v_mfma_f32_16x16x32_bf16 a[100:103],v[140:143],v[176:179],a[100:103]\n"
"s_waitcnt lgkmcnt(0)\n"
"s_lshl2_add_u32 s88,s100,s52\n"
"v_mfma_f32_16x16x32_bf16 a[100:103],v[204:207],v[240:243],a[100:103]\n"
"s_mul_i32 s88,s88,7168\n"
"s_add_u32 s88,s88,0\n"
"v_mfma_f32_16x16x32_bf16 a[108:111],v[204:207],v[244:247],a[108:111]\n"
"s_add_u32 s48,s48,s88\n"
"s_addc_u32 s49,s49,0\n"
"s_mov_b32 s50,3584\n"
"v_mfma_f32_16x16x32_bf16 a[108:111],v[140:143],v[180:183],a[108:111]\n"
"s_mov_b32 s51,0x20000\n"
"v_lshlrev_b32 v8,3,v112\n"
"v_mfma_f32_16x16x32_bf16 a[116:119],v[140:143],v[184:187],a[116:119]\n"
"buffer_load_dwordx4 v112,s[24:27],s76 offen offset:1024 sc1 lds\n"
"v_mfma_f32_16x16x32_bf16 a[116:119],v[204:207],v[248:251],a[116:119]\n"
"v_mfma_f32_16x16x32_bf16 a[124:127],v[204:207],v[252:255],a[124:127]\n"
"v_mfma_f32_16x16x32_bf16 a[124:127],v[140:143],v[188:191],a[124:127]\n"
"v_mfma_f32_16x16x32_bf16 a[128:131],v[144:147],v[160:163],a[128:131]\n"
"v_mfma_f32_16x16x32_bf16 a[128:131],v[208:211],v[224:227],a[128:131]\n"
"buffer_load_dwordx4 v112,s[24:27],s76 offen offset:2048 sc1 lds\n"
"v_mfma_f32_16x16x32_bf16 a[136:139],v[208:211],v[228:231],a[136:139]\n"
"v_mfma_f32_16x16x32_bf16 a[136:139],v[144:147],v[164:167],a[136:139]\n"
"v_mfma_f32_16x16x32_bf16 a[144:147],v[144:147],v[168:171],a[144:147]\n"
"v_mfma_f32_16x16x32_bf16 a[144:147],v[208:211],v[232:235],a[144:147]\n"
"buffer_load_dwordx4 v112,s[24:27],s76 offen offset:3072 sc1 lds\n"
"v_mfma_f32_16x16x32_bf16 a[152:155],v[208:211],v[236:239],a[152:155]\n"
"s_xor_b32 s76,s76,16384\n"
"v_mfma_f32_16x16x32_bf16 a[152:155],v[144:147],v[172:175],a[152:155]\n"
"v_mfma_f32_16x16x32_bf16 a[160:163],v[144:147],v[176:179],a[160:163]\n"
"v_mfma_f32_16x16x32_bf16 a[160:163],v[208:211],v[240:243],a[160:163]\n"
"v_mfma_f32_16x16x32_bf16 a[168:171],v[208:211],v[244:247],a[168:171]\n"
"s_add_u32 m0,s78,81920\n"
"v_mfma_f32_16x16x32_bf16 a[168:171],v[144:147],v[180:183],a[168:171]\n"
"buffer_load_dwordx4 v112,s[24:27],s76 offen offset:0 sc1 lds\n"
"v_mfma_f32_16x16x32_bf16 a[176:179],v[144:147],v[184:187],a[176:179]\n"
"v_mfma_f32_16x16x32_bf16 a[176:179],v[208:211],v[248:251],a[176:179]\n"
"v_mfma_f32_16x16x32_bf16 a[184:187],v[208:211],v[252:255],a[184:187]\n"
"v_mfma_f32_16x16x32_bf16 a[184:187],v[144:147],v[188:191],a[184:187]\n"
"v_mfma_f32_16x16x32_bf16 a[132:135],v[148:151],v[160:163],a[132:135]\n"
"buffer_load_dwordx4 v112,s[24:27],s76 offen offset:1024 sc1 lds\n"
"v_mfma_f32_16x16x32_bf16 a[132:135],v[212:215],v[224:227],a[132:135]\n"
"v_mfma_f32_16x16x32_bf16 a[140:143],v[212:215],v[228:231],a[140:143]\n"
"v_mfma_f32_16x16x32_bf16 a[140:143],v[148:151],v[164:167],a[140:143]\n"
"v_mfma_f32_16x16x32_bf16 a[148:151],v[148:151],v[168:171],a[148:151]\n"
"v_mfma_f32_16x16x32_bf16 a[148:151],v[212:215],v[232:235],a[148:151]\n"
"buffer_load_dwordx4 v112,s[24:27],s76 offen offset:2048 sc1 lds\n"
"v_mfma_f32_16x16x32_bf16 a[156:159],v[212:215],v[236:239],a[156:159]\n"
"v_mfma_f32_16x16x32_bf16 a[156:159],v[148:151],v[172:175],a[156:159]\n"
"v_mfma_f32_16x16x32_bf16 a[164:167],v[148:151],v[176:179],a[164:167]\n"
"v_mfma_f32_16x16x32_bf16 a[164:167],v[212:215],v[240:243],a[164:167]\n"
"buffer_load_dwordx4 v112,s[24:27],s76 offen offset:3072 sc1 lds\n"
"v_mfma_f32_16x16x32_bf16 a[172:175],v[212:215],v[244:247],a[172:175]\n"
"s_xor_b32 s76,s76,49152\n"
"v_mfma_f32_16x16x32_bf16 a[172:175],v[148:151],v[180:183],a[172:175]\n"
"v_mfma_f32_16x16x32_bf16 a[180:183],v[148:151],v[184:187],a[180:183]\n"
"buffer_load_dword v115,v8,s[48:51],0 offen\n"
"v_mfma_f32_16x16x32_bf16 a[180:183],v[212:215],v[248:251],a[180:183]\n"
"v_mfma_f32_16x16x32_bf16 a[188:191],v[212:215],v[252:255],a[188:191]\n"
"v_mfma_f32_16x16x32_bf16 a[188:191],v[148:151],v[188:191],a[188:191]\n"
"s_barrier\n"
"v_mfma_f32_16x16x32_bf16 a[192:195],v[216:219],v[224:227],a[192:195]\n"
"ds_read_b128 v[152:155],v80 offset:12288\n"
"v_mfma_f32_16x16x32_bf16 a[196:199],v[220:223],v[224:227],a[196:199]\n"
"ds_read_b128 v[156:159],v80 offset:14336\n"
"v_mfma_f32_16x16x32_bf16 a[204:207],v[220:223],v[228:231],a[204:207]\n"
"ds_read_b64_tr_b16 v[160:161],v98 offset:49152\n"
"v_mfma_f32_16x16x32_bf16 a[200:203],v[216:219],v[228:231],a[200:203]\n"
"ds_read_b64_tr_b16 v[162:163],v98 offset:50176\n"
"v_mfma_f32_16x16x32_bf16 a[208:211],v[216:219],v[232:235],a[208:211]\n"
"ds_read_b64_tr_b16 v[164:165],v99 offset:49152\n"
"v_mfma_f32_16x16x32_bf16 a[212:215],v[220:223],v[232:235],a[212:215]\n"
"ds_read_b64_tr_b16 v[166:167],v99 offset:50176\n"
"v_mfma_f32_16x16x32_bf16 a[220:223],v[220:223],v[236:239],a[220:223]\n"
"ds_read_b64_tr_b16 v[168:169],v100 offset:49152\n"
"v_mfma_f32_16x16x32_bf16 a[216:219],v[216:219],v[236:239],a[216:219]\n"
"ds_read_b64_tr_b16 v[170:171],v100 offset:50176\n"
"v_mfma_f32_16x16x32_bf16 a[224:227],v[216:219],v[240:243],a[224:227]\n"
"ds_read_b64_tr_b16 v[172:173],v101 offset:49152\n"
"v_mfma_f32_16x16x32_bf16 a[228:231],v[220:223],v[240:243],a[228:231]\n"
"ds_read_b64_tr_b16 v[174:175],v101 offset:50176\n"
"v_mfma_f32_16x16x32_bf16 a[236:239],v[220:223],v[244:247],a[236:239]\n"
"ds_read_b64_tr_b16 v[176:177],v102 offset:49152\n"
"v_mfma_f32_16x16x32_bf16 a[232:235],v[216:219],v[244:247],a[232:235]\n"
"ds_read_b64_tr_b16 v[178:179],v102 offset:50176\n"
"v_mfma_f32_16x16x32_bf16 a[240:243],v[216:219],v[248:251],a[240:243]\n"
"ds_read_b64_tr_b16 v[180:181],v103 offset:49152\n"
"v_mfma_f32_16x16x32_bf16 a[244:247],v[220:223],v[248:251],a[244:247]\n"
"ds_read_b64_tr_b16 v[182:183],v103 offset:50176\n"
"ds_read_b64_tr_b16 v[184:185],v104 offset:49152\n"
"v_mfma_f32_16x16x32_bf16 a[252:255],v[220:223],v[252:255],a[252:255]\n"
"v_mov_b32 v110,v72\n"
"v_mov_b32 v111,v73\n"
"v_mfma_f32_16x16x32_bf16 a[248:251],v[216:219],v[252:255],a[248:251]\n"
"ds_read_b64_tr_b16 v[186:187],v104 offset:50176\n"
"s_waitcnt lgkmcnt(10)\n"
"v_mfma_f32_16x16x32_bf16 a[192:195],v[152:155],v[160:163],a[192:195]\n"
"ds_read_b64_tr_b16 v[188:189],v105 offset:49152\n"
"v_mfma_f32_16x16x32_bf16 a[196:199],v[156:159],v[160:163],a[196:199]\n"
"ds_read_b64_tr_b16 v[190:191],v105 offset:50176\n"
"v_mfma_f32_16x16x32_bf16 a[204:207],v[156:159],v[164:167],a[204:207]\n"
"ds_read_b128 v[128:131],v80 offset:0\n"
"v_mfma_f32_16x16x32_bf16 a[200:203],v[152:155],v[164:167],a[200:203]\n"
"ds_read_b128 v[192:195],v81 offset:0\n"
"s_waitcnt lgkmcnt(10)\n"
"v_mfma_f32_16x16x32_bf16 a[208:211],v[152:155],v[168:171],a[208:211]\n"
"ds_read_b64_tr_b16 v[224:225],v98 offset:32768\n"
"v_mfma_f32_16x16x32_bf16 a[212:215],v[156:159],v[168:171],a[212:215]\n"
"ds_read_b64_tr_b16 v[226:227],v98 offset:33792\n"
"v_mfma_f32_16x16x32_bf16 a[220:223],v[156:159],v[172:175],a[220:223]\n"
"ds_read_b64_tr_b16 v[228:229],v99 offset:32768\n"
"v_mfma_f32_16x16x32_bf16 a[216:219],v[152:155],v[172:175],a[216:219]\n"
"ds_read_b64_tr_b16 v[230:231],v99 offset:33792\n"
"s_waitcnt lgkmcnt(10)\n"
"v_mfma_f32_16x16x32_bf16 a[224:227],v[152:155],v[176:179],a[224:227]\n"
"ds_read_b64_tr_b16 v[232:233],v100 offset:32768\n"
"v_mfma_f32_16x16x32_bf16 a[228:231],v[156:159],v[176:179],a[228:231]\n"
"ds_read_b64_tr_b16 v[234:235],v100 offset:33792\n"
"v_mfma_f32_16x16x32_bf16 a[236:239],v[156:159],v[180:183],a[236:239]\n"
"ds_read_b64_tr_b16 v[236:237],v101 offset:32768\n"
"v_mfma_f32_16x16x32_bf16 a[232:235],v[152:155],v[180:183],a[232:235]\n"
"ds_read_b64_tr_b16 v[238:239],v101 offset:33792\n"
"s_waitcnt lgkmcnt(10)\n"
"v_mfma_f32_16x16x32_bf16 a[240:243],v[152:155],v[184:187],a[240:243]\n"
"ds_read_b64_tr_b16 v[240:241],v102 offset:32768\n"
"v_mfma_f32_16x16x32_bf16 a[244:247],v[156:159],v[184:187],a[244:247]\n"
"ds_read_b64_tr_b16 v[242:243],v102 offset:33792\n"
"v_mfma_f32_16x16x32_bf16 a[252:255],v[156:159],v[188:191],a[252:255]\n"
"ds_read_b64_tr_b16 v[244:245],v103 offset:32768\n"
"v_mfma_f32_16x16x32_bf16 a[248:251],v[152:155],v[188:191],a[248:251]\n"
"ds_read_b64_tr_b16 v[246:247],v103 offset:33792\n"
"s_waitcnt lgkmcnt(8)\n"
"v_mfma_f32_16x16x32_bf16 a[0:3],v[128:131],v[160:163],a[0:3]\n"
"ds_read_b64_tr_b16 v[248:249],v104 offset:32768\n"
"v_mfma_f32_16x16x32_bf16 a[0:3],v[192:195],v[224:227],a[0:3]\n"
"ds_read_b64_tr_b16 v[250:251],v104 offset:33792\n"
"v_mfma_f32_16x16x32_bf16 a[8:11],v[192:195],v[228:231],a[8:11]\n"
"ds_read_b64_tr_b16 v[252:253],v105 offset:32768\n"
"v_mfma_f32_16x16x32_bf16 a[8:11],v[128:131],v[164:167],a[8:11]\n"
"ds_read_b64_tr_b16 v[254:255],v105 offset:33792\n"
"s_waitcnt lgkmcnt(8)\n"
"v_mfma_f32_16x16x32_bf16 a[16:19],v[128:131],v[168:171],a[16:19]\n"
"ds_read_b128 v[132:135],v80 offset:2048\n"
"v_mfma_f32_16x16x32_bf16 a[16:19],v[192:195],v[232:235],a[16:19]\n"
"ds_read_b128 v[196:199],v81 offset:2048\n"
"v_mfma_f32_16x16x32_bf16 a[24:27],v[192:195],v[236:239],a[24:27]\n"
"ds_read_b128 v[136:139],v80 offset:4096\n"
"v_mfma_f32_16x16x32_bf16 a[24:27],v[128:131],v[172:175],a[24:27]\n"
"ds_read_b128 v[200:203],v81 offset:4096\n"
"s_waitcnt lgkmcnt(8)\n"
"v_mfma_f32_16x16x32_bf16 a[32:35],v[128:131],v[176:179],a[32:35]\n"
"ds_read_b128 v[140:143],v80 offset:6144\n"
"v_mfma_f32_16x16x32_bf16 a[32:35],v[192:195],v[240:243],a[32:35]\n"
"ds_read_b128 v[204:207],v81 offset:6144\n"
"v_mfma_f32_16x16x32_bf16 a[40:43],v[192:195],v[244:247],a[40:43]\n"
"ds_read_b128 v[144:147],v80 offset:8192\n"
"v_mfma_f32_16x16x32_bf16 a[40:43],v[128:131],v[180:183],a[40:43]\n"
"ds_read_b128 v[208:211],v81 offset:8192\n"
"s_waitcnt lgkmcnt(8)\n"
"v_mfma_f32_16x16x32_bf16 a[48:51],v[128:131],v[184:187],a[48:51]\n"
"ds_read_b128 v[148:151],v80 offset:10240\n"
"v_mfma_f32_16x16x32_bf16 a[48:51],v[192:195],v[248:251],a[48:51]\n"
"ds_read_b128 v[212:215],v81 offset:10240\n"
"v_mfma_f32_16x16x32_bf16 a[56:59],v[192:195],v[252:255],a[56:59]\n"
"ds_read_b128 v[216:219],v81 offset:12288\n"
"v_mfma_f32_16x16x32_bf16 a[56:59],v[128:131],v[188:191],a[56:59]\n"
"ds_read_b128 v[220:223],v81 offset:14336\n"
"s_waitcnt lgkmcnt(10)\n"
"v_mfma_f32_16x16x32_bf16 a[4:7],v[132:135],v[160:163],a[4:7]\n"
"v_mfma_f32_16x16x32_bf16 a[4:7],v[196:199],v[224:227],a[4:7]\n"
"v_mfma_f32_16x16x32_bf16 a[12:15],v[196:199],v[228:231],a[12:15]\n"
"v_mfma_f32_16x16x32_bf16 a[12:15],v[132:135],v[164:167],a[12:15]\n"
"v_mfma_f32_16x16x32_bf16 a[20:23],v[132:135],v[168:171],a[20:23]\n"
"s_waitcnt lgkmcnt(0)\n"
"s_barrier\n"
"v_mfma_f32_16x16x32_bf16 a[20:23],v[196:199],v[232:235],a[20:23]\n"
"s_add_u32 m0,s85,147456\n"
"v_mfma_f32_16x16x32_bf16 a[28:31],v[196:199],v[236:239],a[28:31]\n"
"buffer_load_dwordx4 v110,s[28:31],s86 offen lds\n"
"v_mfma_f32_16x16x32_bf16 a[28:31],v[132:135],v[172:175],a[28:31]\n"
"v_mfma_f32_16x16x32_bf16 a[36:39],v[132:135],v[176:179],a[36:39]\n"
"s_load_dwordx2 s[48:49],s[0:1],56\n"
"v_mfma_f32_16x16x32_bf16 a[36:39],v[196:199],v[240:243],a[36:39]\n"
"s_add_u32 m0,s85,148480\n"
"v_mfma_f32_16x16x32_bf16 a[44:47],v[196:199],v[244:247],a[44:47]\n"
"buffer_load_dwordx4 v111,s[28:31],s86 offen lds\n"
"v_mfma_f32_16x16x32_bf16 a[44:47],v[132:135],v[180:183],a[44:47]\n"
"s_xor_b32 s86,s86,262144\n"
"v_mfma_f32_16x16x32_bf16 a[52:55],v[132:135],v[184:187],a[52:55]\n"
"v_mfma_f32_16x16x32_bf16 a[52:55],v[196:199],v[248:251],a[52:55]\n"
"s_add_u32 m0,s85,155648\n"
"v_mfma_f32_16x16x32_bf16 a[60:63],v[196:199],v[252:255],a[60:63]\n"
"buffer_load_dwordx4 v110,s[28:31],s86 offen lds\n"
"v_mfma_f32_16x16x32_bf16 a[60:63],v[132:135],v[188:191],a[60:63]\n"
"v_mfma_f32_16x16x32_bf16 a[64:67],v[136:139],v[160:163],a[64:67]\n"
"v_mfma_f32_16x16x32_bf16 a[64:67],v[200:203],v[224:227],a[64:67]\n"
"s_add_u32 m0,s85,156672\n"
"v_mfma_f32_16x16x32_bf16 a[72:75],v[200:203],v[228:231],a[72:75]\n"
"buffer_load_dwordx4 v111,s[28:31],s86 offen lds\n"
"v_mfma_f32_16x16x32_bf16 a[72:75],v[136:139],v[164:167],a[72:75]\n"
"s_xor_b32 s86,s86,786432\n"
"v_mfma_f32_16x16x32_bf16 a[80:83],v[136:139],v[168:171],a[80:83]\n"
"v_mfma_f32_16x16x32_bf16 a[80:83],v[200:203],v[232:235],a[80:83]\n"
"s_add_u32 m0,s85,131072\n"
"v_mfma_f32_16x16x32_bf16 a[88:91],v[200:203],v[236:239],a[88:91]\n"
"buffer_load_dwordx4 v110,s[28:31],s86 offen lds\n"
"v_mfma_f32_16x16x32_bf16 a[88:91],v[136:139],v[172:175],a[88:91]\n"
"v_mfma_f32_16x16x32_bf16 a[96:99],v[136:139],v[176:179],a[96:99]\n"
"v_mfma_f32_16x16x32_bf16 a[96:99],v[200:203],v[240:243],a[96:99]\n"
"s_add_u32 m0,s85,132096\n"
"v_mfma_f32_16x16x32_bf16 a[104:107],v[200:203],v[244:247],a[104:107]\n"
"buffer_load_dwordx4 v111,s[28:31],s86 offen lds\n"
"v_mfma_f32_16x16x32_bf16 a[104:107],v[136:139],v[180:183],a[104:107]\n"
"s_xor_b32 s86,s86,262144\n"
"v_mfma_f32_16x16x32_bf16 a[112:115],v[136:139],v[184:187],a[112:115]\n"
"v_mfma_f32_16x16x32_bf16 a[112:115],v[200:203],v[248:251],a[112:115]\n"
"v_mfma_f32_16x16x32_bf16 a[120:123],v[200:203],v[252:255],a[120:123]\n"
"v_mfma_f32_16x16x32_bf16 a[120:123],v[136:139],v[188:191],a[120:123]\n"
"v_mfma_f32_16x16x32_bf16 a[68:71],v[140:143],v[160:163],a[68:71]\n"
"v_mfma_f32_16x16x32_bf16 a[68:71],v[204:207],v[224:227],a[68:71]\n"
"s_add_u32 m0,s85,139264\n"
"v_mfma_f32_16x16x32_bf16 a[76:79],v[204:207],v[228:231],a[76:79]\n"
"buffer_load_dwordx4 v110,s[28:31],s86 offen lds\n"
"v_mfma_f32_16x16x32_bf16 a[76:79],v[140:143],v[164:167],a[76:79]\n"
"v_mfma_f32_16x16x32_bf16 a[84:87],v[140:143],v[168:171],a[84:87]\n"
"v_mfma_f32_16x16x32_bf16 a[84:87],v[204:207],v[232:235],a[84:87]\n"
"s_add_u32 m0,s85,140288\n"
"v_mfma_f32_16x16x32_bf16 a[92:95],v[204:207],v[236:239],a[92:95]\n"
"buffer_load_dwordx4 v111,s[28:31],s86 offen lds\n"
"v_mfma_f32_16x16x32_bf16 a[92:95],v[140:143],v[172:175],a[92:95]\n"
"s_xor_b32 s86,s86,1835008\n"
"v_mfma_f32_16x16x32_bf16 a[100:103],v[140:143],v[176:179],a[100:103]\n"
"s_waitcnt lgkmcnt(0)\n"
"s_lshl2_add_u32 s88,s100,s52\n"
"v_mfma_f32_16x16x32_bf16 a[100:103],v[204:207],v[240:243],a[100:103]\n"
"s_mul_i32 s88,s88,7168\n"
"s_add_u32 m0,s78,0\n"
"v_mfma_f32_16x16x32_bf16 a[108:111],v[204:207],v[244:247],a[108:111]\n"
"buffer_load_dwordx4 v112,s[24:27],s76 offen offset:0 sc1 lds\n"
"v_mfma_f32_16x16x32_bf16 a[108:111],v[140:143],v[180:183],a[108:111]\n"
"s_add_u32 s88,s88,3584\n"
"s_add_u32 s48,s48,s88\n"
"s_addc_u32 s49,s49,0\n"
"v_mfma_f32_16x16x32_bf16 a[116:119],v[140:143],v[184:187],a[116:119]\n"
"s_mov_b32 s50,3584\n"
"s_mov_b32 s51,0x20000\n"
"v_mfma_f32_16x16x32_bf16 a[116:119],v[204:207],v[248:251],a[116:119]\n"
"v_lshlrev_b32 v8,3,v112\n"
"v_mfma_f32_16x16x32_bf16 a[124:127],v[204:207],v[252:255],a[124:127]\n"
"buffer_load_dwordx4 v112,s[24:27],s76 offen offset:1024 sc1 lds\n"
"v_mfma_f32_16x16x32_bf16 a[124:127],v[140:143],v[188:191],a[124:127]\n"
"v_mfma_f32_16x16x32_bf16 a[128:131],v[144:147],v[160:163],a[128:131]\n"
"v_mfma_f32_16x16x32_bf16 a[128:131],v[208:211],v[224:227],a[128:131]\n"
"v_mfma_f32_16x16x32_bf16 a[136:139],v[208:211],v[228:231],a[136:139]\n"
"buffer_load_dwordx4 v112,s[24:27],s76 offen offset:2048 sc1 lds\n"
"v_mfma_f32_16x16x32_bf16 a[136:139],v[144:147],v[164:167],a[136:139]\n"
"v_mfma_f32_16x16x32_bf16 a[144:147],v[144:147],v[168:171],a[144:147]\n"
"v_mfma_f32_16x16x32_bf16 a[144:147],v[208:211],v[232:235],a[144:147]\n"
"buffer_load_dwordx4 v112,s[24:27],s76 offen offset:3072 sc1 lds\n"
"v_mfma_f32_16x16x32_bf16 a[152:155],v[208:211],v[236:239],a[152:155]\n"
"s_xor_b32 s76,s76,16384\n"
"v_mfma_f32_16x16x32_bf16 a[152:155],v[144:147],v[172:175],a[152:155]\n"
"v_mfma_f32_16x16x32_bf16 a[160:163],v[144:147],v[176:179],a[160:163]\n"
"v_mfma_f32_16x16x32_bf16 a[160:163],v[208:211],v[240:243],a[160:163]\n"
"s_add_u32 m0,s78,16384\n"
"v_mfma_f32_16x16x32_bf16 a[168:171],v[208:211],v[244:247],a[168:171]\n"
"buffer_load_dwordx4 v112,s[24:27],s76 offen offset:0 sc1 lds\n"
"v_mfma_f32_16x16x32_bf16 a[168:171],v[144:147],v[180:183],a[168:171]\n"
"v_mfma_f32_16x16x32_bf16 a[176:179],v[144:147],v[184:187],a[176:179]\n"
"v_mfma_f32_16x16x32_bf16 a[176:179],v[208:211],v[248:251],a[176:179]\n"
"v_mfma_f32_16x16x32_bf16 a[184:187],v[208:211],v[252:255],a[184:187]\n"
"buffer_load_dwordx4 v112,s[24:27],s76 offen offset:1024 sc1 lds\n"
"v_mfma_f32_16x16x32_bf16 a[184:187],v[144:147],v[188:191],a[184:187]\n"
"v_mfma_f32_16x16x32_bf16 a[132:135],v[148:151],v[160:163],a[132:135]\n"
"v_mfma_f32_16x16x32_bf16 a[132:135],v[212:215],v[224:227],a[132:135]\n"
"v_mfma_f32_16x16x32_bf16 a[140:143],v[212:215],v[228:231],a[140:143]\n"
"buffer_load_dwordx4 v112,s[24:27],s76 offen offset:2048 sc1 lds\n"
"v_mfma_f32_16x16x32_bf16 a[140:143],v[148:151],v[164:167],a[140:143]\n"
"v_mfma_f32_16x16x32_bf16 a[148:151],v[148:151],v[168:171],a[148:151]\n"
"v_mfma_f32_16x16x32_bf16 a[148:151],v[212:215],v[232:235],a[148:151]\n"
"buffer_load_dwordx4 v112,s[24:27],s76 offen offset:3072 sc1 lds\n"
"v_mfma_f32_16x16x32_bf16 a[156:159],v[212:215],v[236:239],a[156:159]\n"
"s_xor_b32 s76,s76,114688\n"
"v_mfma_f32_16x16x32_bf16 a[156:159],v[148:151],v[172:175],a[156:159]\n"
"v_mfma_f32_16x16x32_bf16 a[164:167],v[148:151],v[176:179],a[164:167]\n"
"buffer_load_dword v115,v8,s[48:51],0 offen\n"
"v_mfma_f32_16x16x32_bf16 a[164:167],v[212:215],v[240:243],a[164:167]\n"
"v_mfma_f32_16x16x32_bf16 a[172:175],v[212:215],v[244:247],a[172:175]\n"
"v_mfma_f32_16x16x32_bf16 a[172:175],v[148:151],v[180:183],a[172:175]\n"
"v_mfma_f32_16x16x32_bf16 a[180:183],v[148:151],v[184:187],a[180:183]\n"
"v_mfma_f32_16x16x32_bf16 a[180:183],v[212:215],v[248:251],a[180:183]\n"
"v_mfma_f32_16x16x32_bf16 a[188:191],v[212:215],v[252:255],a[188:191]\n"
"v_mfma_f32_16x16x32_bf16 a[188:191],v[148:151],v[188:191],a[188:191]\n"
"s_waitcnt vmcnt(26)\n"
"s_barrier\n"
"v_mfma_f32_16x16x32_bf16 a[192:195],v[216:219],v[224:227],a[192:195]\n"
"ds_read_b128 v[152:155],v80 offset:45056\n"
"v_mfma_f32_16x16x32_bf16 a[196:199],v[220:223],v[224:227],a[196:199]\n"
"ds_read_b128 v[156:159],v80 offset:47104\n"
"v_mfma_f32_16x16x32_bf16 a[204:207],v[220:223],v[228:231],a[204:207]\n"
"ds_read_b64_tr_b16 v[160:161],v98 offset:0\n"
"v_mfma_f32_16x16x32_bf16 a[200:203],v[216:219],v[228:231],a[200:203]\n"
"ds_read_b64_tr_b16 v[162:163],v98 offset:1024\n"
"v_mfma_f32_16x16x32_bf16 a[208:211],v[216:219],v[232:235],a[208:211]\n"
"ds_read_b64_tr_b16 v[164:165],v99 offset:0\n"
"v_mfma_f32_16x16x32_bf16 a[212:215],v[220:223],v[232:235],a[212:215]\n"
"ds_read_b64_tr_b16 v[166:167],v99 offset:1024\n"
"v_mfma_f32_16x16x32_bf16 a[220:223],v[220:223],v[236:239],a[220:223]\n"
"ds_read_b64_tr_b16 v[168:169],v100 offset:0\n"
"v_mfma_f32_16x16x32_bf16 a[216:219],v[216:219],v[236:239],a[216:219]\n"
"ds_read_b64_tr_b16 v[170:171],v100 offset:1024\n"
"v_mfma_f32_16x16x32_bf16 a[224:227],v[216:219],v[240:243],a[224:227]\n"
"ds_read_b64_tr_b16 v[172:173],v101 offset:0\n"
"v_mfma_f32_16x16x32_bf16 a[228:231],v[220:223],v[240:243],a[228:231]\n"
"ds_read_b64_tr_b16 v[174:175],v101 offset:1024\n"
"v_mfma_f32_16x16x32_bf16 a[236:239],v[220:223],v[244:247],a[236:239]\n"
"ds_read_b64_tr_b16 v[176:177],v102 offset:0\n"
"v_mfma_f32_16x16x32_bf16 a[232:235],v[216:219],v[244:247],a[232:235]\n"
"ds_read_b64_tr_b16 v[178:179],v102 offset:1024\n"
"v_mfma_f32_16x16x32_bf16 a[240:243],v[216:219],v[248:251],a[240:243]\n"
"ds_read_b64_tr_b16 v[180:181],v103 offset:0\n"
"v_mfma_f32_16x16x32_bf16 a[244:247],v[220:223],v[248:251],a[244:247]\n"
"ds_read_b64_tr_b16 v[182:183],v103 offset:1024\n"
"v_mfma_f32_16x16x32_bf16 a[252:255],v[220:223],v[252:255],a[252:255]\n"
"ds_read_b64_tr_b16 v[184:185],v104 offset:0\n"
"v_mfma_f32_16x16x32_bf16 a[248:251],v[216:219],v[252:255],a[248:251]\n"
"ds_read_b64_tr_b16 v[186:187],v104 offset:1024\n"
"s_waitcnt lgkmcnt(10)\n"
"v_mfma_f32_16x16x32_bf16 a[192:195],v[152:155],v[160:163],a[192:195]\n"
"ds_read_b64_tr_b16 v[188:189],v105 offset:0\n"
"v_mfma_f32_16x16x32_bf16 a[196:199],v[156:159],v[160:163],a[196:199]\n"
"ds_read_b64_tr_b16 v[190:191],v105 offset:1024\n"
"v_mfma_f32_16x16x32_bf16 a[204:207],v[156:159],v[164:167],a[204:207]\n"
"ds_read_b128 v[128:131],v80 offset:32768\n"
"v_mfma_f32_16x16x32_bf16 a[200:203],v[152:155],v[164:167],a[200:203]\n"
"ds_read_b128 v[192:195],v81 offset:32768\n"
"s_waitcnt lgkmcnt(10)\n"
"v_mfma_f32_16x16x32_bf16 a[208:211],v[152:155],v[168:171],a[208:211]\n"
"ds_read_b64_tr_b16 v[224:225],v98 offset:16384\n"
"v_mfma_f32_16x16x32_bf16 a[212:215],v[156:159],v[168:171],a[212:215]\n"
"ds_read_b64_tr_b16 v[226:227],v98 offset:17408\n"
"v_mfma_f32_16x16x32_bf16 a[220:223],v[156:159],v[172:175],a[220:223]\n"
"ds_read_b64_tr_b16 v[228:229],v99 offset:16384\n"
"v_mfma_f32_16x16x32_bf16 a[216:219],v[152:155],v[172:175],a[216:219]\n"
"ds_read_b64_tr_b16 v[230:231],v99 offset:17408\n"
"s_waitcnt lgkmcnt(10)\n"
"v_mfma_f32_16x16x32_bf16 a[224:227],v[152:155],v[176:179],a[224:227]\n"
"ds_read_b64_tr_b16 v[232:233],v100 offset:16384\n"
"v_mfma_f32_16x16x32_bf16 a[228:231],v[156:159],v[176:179],a[228:231]\n"
"ds_read_b64_tr_b16 v[234:235],v100 offset:17408\n"
"v_mfma_f32_16x16x32_bf16 a[236:239],v[156:159],v[180:183],a[236:239]\n"
"ds_read_b64_tr_b16 v[236:237],v101 offset:16384\n"
"v_mfma_f32_16x16x32_bf16 a[232:235],v[152:155],v[180:183],a[232:235]\n"
"ds_read_b64_tr_b16 v[238:239],v101 offset:17408\n"
"s_waitcnt lgkmcnt(10)\n"
"v_mfma_f32_16x16x32_bf16 a[240:243],v[152:155],v[184:187],a[240:243]\n"
"ds_read_b64_tr_b16 v[240:241],v102 offset:16384\n"
"v_mfma_f32_16x16x32_bf16 a[244:247],v[156:159],v[184:187],a[244:247]\n"
"ds_read_b64_tr_b16 v[242:243],v102 offset:17408\n"
"v_mfma_f32_16x16x32_bf16 a[252:255],v[156:159],v[188:191],a[252:255]\n"
"ds_read_b64_tr_b16 v[244:245],v103 offset:16384\n"
"v_mfma_f32_16x16x32_bf16 a[248:251],v[152:155],v[188:191],a[248:251]\n"
"ds_read_b64_tr_b16 v[246:247],v103 offset:17408\n"
"s_waitcnt lgkmcnt(8)\n"
"v_mfma_f32_16x16x32_bf16 a[0:3],v[128:131],v[160:163],a[0:3]\n"
"ds_read_b64_tr_b16 v[248:249],v104 offset:16384\n"
"v_mfma_f32_16x16x32_bf16 a[0:3],v[192:195],v[224:227],a[0:3]\n"
"ds_read_b64_tr_b16 v[250:251],v104 offset:17408\n"
"v_mfma_f32_16x16x32_bf16 a[8:11],v[192:195],v[228:231],a[8:11]\n"
"ds_read_b64_tr_b16 v[252:253],v105 offset:16384\n"
"v_mfma_f32_16x16x32_bf16 a[8:11],v[128:131],v[164:167],a[8:11]\n"
"ds_read_b64_tr_b16 v[254:255],v105 offset:17408\n"
"s_waitcnt lgkmcnt(8)\n"
"v_mfma_f32_16x16x32_bf16 a[16:19],v[128:131],v[168:171],a[16:19]\n"
"ds_read_b128 v[132:135],v80 offset:34816\n"
"v_mfma_f32_16x16x32_bf16 a[16:19],v[192:195],v[232:235],a[16:19]\n"
"ds_read_b128 v[196:199],v81 offset:34816\n"
"v_mfma_f32_16x16x32_bf16 a[24:27],v[192:195],v[236:239],a[24:27]\n"
"ds_read_b128 v[136:139],v80 offset:36864\n"
"v_mfma_f32_16x16x32_bf16 a[24:27],v[128:131],v[172:175],a[24:27]\n"
"ds_read_b128 v[200:203],v81 offset:36864\n"
"s_waitcnt lgkmcnt(8)\n"
"v_mfma_f32_16x16x32_bf16 a[32:35],v[128:131],v[176:179],a[32:35]\n"
"s_and_b32 s82,s52,1\n"
"ds_read_b128 v[140:143],v80 offset:38912\n"
"s_lshl_b32 s82,s82,13\n"
"v_mfma_f32_16x16x32_bf16 a[32:35],v[192:195],v[240:243],a[32:35]\n"
"ds_read_b128 v[204:207],v81 offset:38912\n"
"v_mfma_f32_16x16x32_bf16 a[40:43],v[192:195],v[244:247],a[40:43]\n"
"ds_read_b128 v[144:147],v80 offset:40960\n"
"v_mfma_f32_16x16x32_bf16 a[40:43],v[128:131],v[180:183],a[40:43]\n"
"ds_read_b128 v[208:211],v81 offset:40960\n"
"s_waitcnt lgkmcnt(8)\n"
"v_mfma_f32_16x16x32_bf16 a[48:51],v[128:131],v[184:187],a[48:51]\n"
"ds_read_b128 v[148:151],v80 offset:43008\n"
"v_mfma_f32_16x16x32_bf16 a[48:51],v[192:195],v[248:251],a[48:51]\n"
"ds_read_b128 v[212:215],v81 offset:43008\n"
"v_subrev_u32 v98,s82,v98\n"
"v_mfma_f32_16x16x32_bf16 a[56:59],v[192:195],v[252:255],a[56:59]\n"
"ds_read_b128 v[216:219],v81 offset:45056\n"
"v_add_u32 v96,s78,v97\n"
"v_mfma_f32_16x16x32_bf16 a[56:59],v[128:131],v[188:191],a[56:59]\n"
"ds_read_b128 v[220:223],v81 offset:47104\n"
"s_waitcnt lgkmcnt(10)\n"
"v_mfma_f32_16x16x32_bf16 a[4:7],v[132:135],v[160:163],a[4:7]\n"
"v_add_u32 v97,65536,v96\n"
"v_mfma_f32_16x16x32_bf16 a[4:7],v[196:199],v[224:227],a[4:7]\n"
"v_subrev_u32 v99,s82,v99\n"
"v_mfma_f32_16x16x32_bf16 a[12:15],v[196:199],v[228:231],a[12:15]\n"
"v_mfma_f32_16x16x32_bf16 a[12:15],v[132:135],v[164:167],a[12:15]\n"
"v_mfma_f32_16x16x32_bf16 a[20:23],v[132:135],v[168:171],a[20:23]\n"
"s_waitcnt lgkmcnt(0)\n"
"s_barrier\n"
"v_mfma_f32_16x16x32_bf16 a[20:23],v[196:199],v[232:235],a[20:23]\n"
"v_subrev_u32 v100,s82,v100\n"
"s_add_u32 m0,s85,98304\n"
"v_mfma_f32_16x16x32_bf16 a[28:31],v[196:199],v[236:239],a[28:31]\n"
"buffer_load_dwordx4 v110,s[28:31],s86 offen lds\n"
"v_mfma_f32_16x16x32_bf16 a[28:31],v[132:135],v[172:175],a[28:31]\n"
"v_mfma_f32_16x16x32_bf16 a[36:39],v[132:135],v[176:179],a[36:39]\n"
"v_mfma_f32_16x16x32_bf16 a[36:39],v[196:199],v[240:243],a[36:39]\n"
"v_subrev_u32 v101,s82,v101\n"
"s_add_u32 m0,s85,99328\n"
"v_mfma_f32_16x16x32_bf16 a[44:47],v[196:199],v[244:247],a[44:47]\n"
"buffer_load_dwordx4 v111,s[28:31],s86 offen lds\n"
"v_mfma_f32_16x16x32_bf16 a[44:47],v[132:135],v[180:183],a[44:47]\n"
"s_xor_b32 s86,s86,262144\n"
"v_mfma_f32_16x16x32_bf16 a[52:55],v[132:135],v[184:187],a[52:55]\n"
"v_mfma_f32_16x16x32_bf16 a[52:55],v[196:199],v[248:251],a[52:55]\n"
"v_subrev_u32 v102,s82,v102\n"
"s_add_u32 m0,s78,32768\n"
"v_mfma_f32_16x16x32_bf16 a[60:63],v[196:199],v[252:255],a[60:63]\n"
"buffer_load_dwordx4 v112,s[24:27],s76 offen offset:0 sc1 lds\n"
"v_mfma_f32_16x16x32_bf16 a[60:63],v[132:135],v[188:191],a[60:63]\n"
"v_mfma_f32_16x16x32_bf16 a[64:67],v[136:139],v[160:163],a[64:67]\n"
"v_mfma_f32_16x16x32_bf16 a[64:67],v[200:203],v[224:227],a[64:67]\n"
"v_subrev_u32 v103,s82,v103\n"
"v_mfma_f32_16x16x32_bf16 a[72:75],v[200:203],v[228:231],a[72:75]\n"
"buffer_load_dwordx4 v112,s[24:27],s76 offen offset:1024 sc1 lds\n"
"v_mfma_f32_16x16x32_bf16 a[72:75],v[136:139],v[164:167],a[72:75]\n"
"v_mfma_f32_16x16x32_bf16 a[80:83],v[136:139],v[168:171],a[80:83]\n"
"v_accvgpr_read_b32 v116,a0\n"
"v_accvgpr_read_b32 v117,a1\n"
"v_mfma_f32_16x16x32_bf16 a[80:83],v[200:203],v[232:235],a[80:83]\n"
"v_subrev_u32 v104,s82,v104\n"
"v_accvgpr_read_b32 v118,a2\n"
"v_mfma_f32_16x16x32_bf16 a[88:91],v[200:203],v[236:239],a[88:91]\n"
"buffer_load_dwordx4 v112,s[24:27],s76 offen offset:2048 sc1 lds\n"
"v_mfma_f32_16x16x32_bf16 a[88:91],v[136:139],v[172:175],a[88:91]\n"
"v_accvgpr_read_b32 v119,a3\n"
"v_accvgpr_read_b32 v120,a4\n"
"v_mfma_f32_16x16x32_bf16 a[96:99],v[136:139],v[176:179],a[96:99]\n"
"v_accvgpr_read_b32 v121,a5\n"
"v_accvgpr_read_b32 v122,a6\n"
"v_mfma_f32_16x16x32_bf16 a[96:99],v[200:203],v[240:243],a[96:99]\n"
"v_subrev_u32 v105,s82,v105\n"
"v_accvgpr_read_b32 v123,a7\n"
"v_mfma_f32_16x16x32_bf16 a[104:107],v[200:203],v[244:247],a[104:107]\n"
"buffer_load_dwordx4 v112,s[24:27],s76 offen offset:3072 sc1 lds\n"
"v_mfma_f32_16x16x32_bf16 a[104:107],v[136:139],v[180:183],a[104:107]\n"
"s_xor_b32 s76,s76,16384\n"
"v_cvt_pk_bf16_f32 v116,v116,v117\n"
"v_cvt_pk_bf16_f32 v117,v118,v119\n"
"v_mfma_f32_16x16x32_bf16 a[112:115],v[136:139],v[184:187],a[112:115]\n"
"v_cvt_pk_bf16_f32 v118,v120,v121\n"
"v_cvt_pk_bf16_f32 v119,v122,v123\n"
"v_mfma_f32_16x16x32_bf16 a[112:115],v[200:203],v[248:251],a[112:115]\n"
"buffer_store_dwordx4 v[116:119],v112,s[24:27],s56 offen offset:0 sc1\n"
"v_mfma_f32_16x16x32_bf16 a[120:123],v[200:203],v[252:255],a[120:123]\n"
"s_nop 0\n"
"v_accvgpr_read_b32 v116,a8\n"
"v_accvgpr_read_b32 v117,a9\n"
"v_accvgpr_read_b32 v118,a10\n"
"v_mfma_f32_16x16x32_bf16 a[120:123],v[136:139],v[188:191],a[120:123]\n"
"s_waitcnt vmcnt(20)\n"
"v_accvgpr_read_b32 v119,a11\n"
"v_accvgpr_read_b32 v120,a12\n"
"s_barrier\n"
"v_mfma_f32_16x16x32_bf16 a[68:71],v[140:143],v[160:163],a[68:71]\n"
"ds_read_b128 v[0:3],v97 offset:0\n"
"v_accvgpr_read_b32 v121,a13\n"
"v_mfma_f32_16x16x32_bf16 a[68:71],v[204:207],v[224:227],a[68:71]\n"
"ds_read_b128 v[8:11],v97 offset:1024\n"
"v_accvgpr_read_b32 v122,a14\n"
"v_mfma_f32_16x16x32_bf16 a[76:79],v[204:207],v[228:231],a[76:79]\n"
"ds_read_b128 v[16:19],v97 offset:2048\n"
"v_accvgpr_read_b32 v123,a15\n"
"v_mfma_f32_16x16x32_bf16 a[76:79],v[140:143],v[164:167],a[76:79]\n"
"ds_read_b128 v[24:27],v97 offset:3072\n"
"v_cvt_pk_bf16_f32 v116,v116,v117\n"
"v_mfma_f32_16x16x32_bf16 a[84:87],v[140:143],v[168:171],a[84:87]\n"
"ds_read_b128 v[4:7],v97 offset:16384\n"
"v_cvt_pk_bf16_f32 v117,v118,v119\n"
"v_mfma_f32_16x16x32_bf16 a[84:87],v[204:207],v[232:235],a[84:87]\n"
"ds_read_b128 v[12:15],v97 offset:17408\n"
"v_cvt_pk_bf16_f32 v118,v120,v121\n"
"v_mfma_f32_16x16x32_bf16 a[92:95],v[204:207],v[236:239],a[92:95]\n"
"ds_read_b128 v[20:23],v97 offset:18432\n"
"v_cvt_pk_bf16_f32 v119,v122,v123\n"
"v_mfma_f32_16x16x32_bf16 a[92:95],v[140:143],v[172:175],a[92:95]\n"
"ds_read_b128 v[28:31],v97 offset:19456\n"
"buffer_store_dwordx4 v[116:119],v112,s[24:27],s56 offen offset:1024 sc1\n"
"v_mfma_f32_16x16x32_bf16 a[100:103],v[140:143],v[176:179],a[100:103]\n"
"ds_read_b64_tr_b16 v[32:33],v98 offset:49152\n"
"v_accvgpr_read_b32 v116,a16\n"
"v_mfma_f32_16x16x32_bf16 a[100:103],v[204:207],v[240:243],a[100:103]\n"
"ds_read_b64_tr_b16 v[34:35],v98 offset:50176\n"
"v_accvgpr_read_b32 v117,a17\n"
"v_mfma_f32_16x16x32_bf16 a[108:111],v[204:207],v[244:247],a[108:111]\n"
"ds_read_b64_tr_b16 v[36:37],v98 offset:57344\n"
"v_accvgpr_read_b32 v118,a18\n"
"v_mfma_f32_16x16x32_bf16 a[108:111],v[140:143],v[180:183],a[108:111]\n"
"ds_read_b64_tr_b16 v[38:39],v98 offset:58368\n"
"v_accvgpr_read_b32 v119,a19\n"
"v_mfma_f32_16x16x32_bf16 a[116:119],v[140:143],v[184:187],a[116:119]\n"
"ds_read_b64_tr_b16 v[40:41],v99 offset:49152\n"
"v_accvgpr_read_b32 v120,a20\n"
"v_mfma_f32_16x16x32_bf16 a[116:119],v[204:207],v[248:251],a[116:119]\n"
"ds_read_b64_tr_b16 v[42:43],v99 offset:50176\n"
"v_accvgpr_read_b32 v121,a21\n"
"v_mfma_f32_16x16x32_bf16 a[124:127],v[204:207],v[252:255],a[124:127]\n"
"ds_read_b64_tr_b16 v[44:45],v99 offset:57344\n"
"v_accvgpr_read_b32 v122,a22\n"
"v_mfma_f32_16x16x32_bf16 a[124:127],v[140:143],v[188:191],a[124:127]\n"
"ds_read_b64_tr_b16 v[46:47],v99 offset:58368\n"
"v_accvgpr_read_b32 v123,a23\n"
"v_mfma_f32_16x16x32_bf16 a[128:131],v[144:147],v[160:163],a[128:131]\n"
"ds_read_b64_tr_b16 v[48:49],v100 offset:49152\n"
"v_cvt_pk_bf16_f32 v116,v116,v117\n"
"v_mfma_f32_16x16x32_bf16 a[128:131],v[208:211],v[224:227],a[128:131]\n"
"ds_read_b64_tr_b16 v[50:51],v100 offset:50176\n"
"v_cvt_pk_bf16_f32 v117,v118,v119\n"
"v_mfma_f32_16x16x32_bf16 a[136:139],v[208:211],v[228:231],a[136:139]\n"
"ds_read_b64_tr_b16 v[52:53],v100 offset:57344\n"
"v_cvt_pk_bf16_f32 v118,v120,v121\n"
"v_mfma_f32_16x16x32_bf16 a[136:139],v[144:147],v[164:167],a[136:139]\n"
"ds_read_b64_tr_b16 v[54:55],v100 offset:58368\n"
"v_cvt_pk_bf16_f32 v119,v122,v123\n"
"v_mfma_f32_16x16x32_bf16 a[144:147],v[144:147],v[168:171],a[144:147]\n"
"ds_read_b64_tr_b16 v[56:57],v101 offset:49152\n"
"buffer_store_dwordx4 v[116:119],v112,s[24:27],s56 offen offset:2048 sc1\n"
"v_mfma_f32_16x16x32_bf16 a[144:147],v[208:211],v[232:235],a[144:147]\n"
"ds_read_b64_tr_b16 v[58:59],v101 offset:50176\n"
"v_accvgpr_read_b32 v116,a24\n"
"v_mfma_f32_16x16x32_bf16 a[152:155],v[208:211],v[236:239],a[152:155]\n"
"ds_read_b64_tr_b16 v[60:61],v101 offset:57344\n"
"v_accvgpr_read_b32 v117,a25\n"
"v_mfma_f32_16x16x32_bf16 a[152:155],v[144:147],v[172:175],a[152:155]\n"
"ds_read_b64_tr_b16 v[62:63],v101 offset:58368\n"
"v_accvgpr_read_b32 v118,a26\n"
"v_mfma_f32_16x16x32_bf16 a[160:163],v[144:147],v[176:179],a[160:163]\n"
"ds_read_b64_tr_b16 v[64:65],v102 offset:49152\n"
"v_accvgpr_read_b32 v119,a27\n"
"v_mfma_f32_16x16x32_bf16 a[160:163],v[208:211],v[240:243],a[160:163]\n"
"ds_read_b64_tr_b16 v[66:67],v102 offset:50176\n"
"v_accvgpr_read_b32 v120,a28\n"
"v_mfma_f32_16x16x32_bf16 a[168:171],v[208:211],v[244:247],a[168:171]\n"
"ds_read_b64_tr_b16 v[68:69],v102 offset:57344\n"
"v_accvgpr_read_b32 v121,a29\n"
"v_mfma_f32_16x16x32_bf16 a[168:171],v[144:147],v[180:183],a[168:171]\n"
"ds_read_b64_tr_b16 v[70:71],v102 offset:58368\n"
"v_accvgpr_read_b32 v122,a30\n"
"v_mfma_f32_16x16x32_bf16 a[176:179],v[144:147],v[184:187],a[176:179]\n"
"ds_read_b64_tr_b16 v[72:73],v103 offset:49152\n"
"v_accvgpr_read_b32 v123,a31\n"
"v_mfma_f32_16x16x32_bf16 a[176:179],v[208:211],v[248:251],a[176:179]\n"
"ds_read_b64_tr_b16 v[74:75],v103 offset:50176\n"
"v_cvt_pk_bf16_f32 v116,v116,v117\n"
"v_mfma_f32_16x16x32_bf16 a[184:187],v[208:211],v[252:255],a[184:187]\n"
"ds_read_b64_tr_b16 v[76:77],v103 offset:57344\n"
"v_cvt_pk_bf16_f32 v117,v118,v119\n"
"v_mfma_f32_16x16x32_bf16 a[184:187],v[144:147],v[188:191],a[184:187]\n"
"ds_read_b64_tr_b16 v[78:79],v103 offset:58368\n"
"v_cvt_pk_bf16_f32 v118,v120,v121\n"
"v_mfma_f32_16x16x32_bf16 a[132:135],v[148:151],v[160:163],a[132:135]\n"
"ds_read_b64_tr_b16 v[80:81],v104 offset:49152\n"
"v_cvt_pk_bf16_f32 v119,v122,v123\n"
"v_mfma_f32_16x16x32_bf16 a[132:135],v[212:215],v[224:227],a[132:135]\n"
"ds_read_b64_tr_b16 v[82:83],v104 offset:50176\n"
"buffer_store_dwordx4 v[116:119],v112,s[24:27],s56 offen offset:3072 sc1\n"
"v_mfma_f32_16x16x32_bf16 a[140:143],v[212:215],v[228:231],a[140:143]\n"
"ds_read_b64_tr_b16 v[84:85],v104 offset:57344\n"
"v_mfma_f32_16x16x32_bf16 a[140:143],v[148:151],v[164:167],a[140:143]\n"
"ds_read_b64_tr_b16 v[86:87],v104 offset:58368\n"
"v_mfma_f32_16x16x32_bf16 a[148:151],v[148:151],v[168:171],a[148:151]\n"
"ds_read_b64_tr_b16 v[88:89],v105 offset:49152\n"
"v_mfma_f32_16x16x32_bf16 a[148:151],v[212:215],v[232:235],a[148:151]\n"
"ds_read_b64_tr_b16 v[90:91],v105 offset:50176\n"
"v_mfma_f32_16x16x32_bf16 a[156:159],v[212:215],v[236:239],a[156:159]\n"
"ds_read_b64_tr_b16 v[92:93],v105 offset:57344\n"
"v_mfma_f32_16x16x32_bf16 a[156:159],v[148:151],v[172:175],a[156:159]\n"
"ds_read_b64_tr_b16 v[94:95],v105 offset:58368\n"
"v_mfma_f32_16x16x32_bf16 a[164:167],v[148:151],v[176:179],a[164:167]\n"
"s_add_u32 m0,s85,106496\n"
"v_mfma_f32_16x16x32_bf16 a[164:167],v[212:215],v[240:243],a[164:167]\n"
"buffer_load_dwordx4 v110,s[28:31],s86 offen lds\n"
"v_mfma_f32_16x16x32_bf16 a[172:175],v[212:215],v[244:247],a[172:175]\n"
"v_mfma_f32_16x16x32_bf16 a[172:175],v[148:151],v[180:183],a[172:175]\n"
"s_add_u32 m0,s85,107520\n"
"v_mfma_f32_16x16x32_bf16 a[180:183],v[148:151],v[184:187],a[180:183]\n"
"buffer_load_dwordx4 v111,s[28:31],s86 offen lds\n"
"v_mfma_f32_16x16x32_bf16 a[180:183],v[212:215],v[248:251],a[180:183]\n"
"s_xor_b32 s86,s86,786432\n"
"v_mfma_f32_16x16x32_bf16 a[188:191],v[212:215],v[252:255],a[188:191]\n"
"s_add_u32 m0,s78,49152\n"
"v_mfma_f32_16x16x32_bf16 a[188:191],v[148:151],v[188:191],a[188:191]\n"
"buffer_load_dwordx4 v112,s[24:27],s76 offen offset:0 sc1 lds\n"
"v_mfma_f32_16x16x32_bf16 a[192:195],v[216:219],v[224:227],a[192:195]\n"
"v_mfma_f32_16x16x32_bf16 a[196:199],v[220:223],v[224:227],a[196:199]\n"
"v_mfma_f32_16x16x32_bf16 a[204:207],v[220:223],v[228:231],a[204:207]\n"
"buffer_load_dwordx4 v112,s[24:27],s76 offen offset:1024 sc1 lds\n"
"v_mfma_f32_16x16x32_bf16 a[200:203],v[216:219],v[228:231],a[200:203]\n"
"v_mfma_f32_16x16x32_bf16 a[208:211],v[216:219],v[232:235],a[208:211]\n"
"v_mfma_f32_16x16x32_bf16 a[212:215],v[220:223],v[232:235],a[212:215]\n"
"buffer_load_dwordx4 v112,s[24:27],s76 offen offset:2048 sc1 lds\n"
"v_mfma_f32_16x16x32_bf16 a[220:223],v[220:223],v[236:239],a[220:223]\n"
"v_mfma_f32_16x16x32_bf16 a[216:219],v[216:219],v[236:239],a[216:219]\n"
"buffer_load_dwordx4 v112,s[24:27],s76 offen offset:3072 sc1 lds\n"
"v_mfma_f32_16x16x32_bf16 a[224:227],v[216:219],v[240:243],a[224:227]\n"
"s_xor_b32 s76,s76,49152\n"
"v_mfma_f32_16x16x32_bf16 a[228:231],v[220:223],v[240:243],a[228:231]\n"
"v_mfma_f32_16x16x32_bf16 a[236:239],v[220:223],v[244:247],a[236:239]\n"
"v_mfma_f32_16x16x32_bf16 a[232:235],v[216:219],v[244:247],a[232:235]\n"
"v_mfma_f32_16x16x32_bf16 a[240:243],v[216:219],v[248:251],a[240:243]\n"
"v_mfma_f32_16x16x32_bf16 a[244:247],v[220:223],v[248:251],a[244:247]\n"
"v_mfma_f32_16x16x32_bf16 a[252:255],v[220:223],v[252:255],a[252:255]\n"
"v_mfma_f32_16x16x32_bf16 a[248:251],v[216:219],v[252:255],a[248:251]\n"
".Lfk_ph2:\n"
"s_waitcnt vmcnt(25) lgkmcnt(0)\n"
"s_barrier\n"
"v_mfma_f32_16x16x32_bf16 v[128:131],v[32:35],v[0:3],0\n"
"v_accvgpr_read_b32 v116,a32\n"
"v_accvgpr_read_b32 v117,a33\n"
"v_mfma_f32_16x16x32_bf16 v[128:131],v[36:39],v[4:7],v[128:131]\n"
"v_accvgpr_read_b32 v118,a34\n"
"v_accvgpr_read_b32 v119,a35\n"
"v_mfma_f32_16x16x32_bf16 v[160:163],v[36:39],v[12:15],0\n"
"v_accvgpr_read_b32 v120,a36\n"
"v_accvgpr_read_b32 v121,a37\n"
"v_mfma_f32_16x16x32_bf16 v[160:163],v[32:35],v[8:11],v[160:163]\n"
"s_add_u32 m0,s85,147456\n"
"v_accvgpr_read_b32 v122,a38\n"
"v_mfma_f32_16x16x32_bf16 v[192:195],v[32:35],v[16:19],0\n"
"buffer_load_dwordx4 v110,s[28:31],s86 offen lds\n"
"v_mfma_f32_16x16x32_bf16 v[192:195],v[36:39],v[20:23],v[192:195]\n"
"v_accvgpr_read_b32 v123,a39\n"
"v_cvt_pk_bf16_f32 v116,v116,v117\n"
"v_mfma_f32_16x16x32_bf16 v[224:227],v[36:39],v[28:31],0\n"
"v_cvt_pk_bf16_f32 v117,v118,v119\n"
"v_cvt_pk_bf16_f32 v118,v120,v121\n"
"v_mfma_f32_16x16x32_bf16 v[224:227],v[32:35],v[24:27],v[224:227]\n"
"v_cvt_pk_bf16_f32 v119,v122,v123\n"
"s_add_u32 s56,s56,4096\n"
"v_mfma_f32_16x16x32_bf16 v[228:231],v[40:43],v[24:27],0\n"
"ds_read_b64_tr_b16 v[32:33],v100 offset:32768\n"
"buffer_store_dwordx4 v[116:119],v112,s[24:27],s56 offen offset:0 sc1\n"
"v_mfma_f32_16x16x32_bf16 v[228:231],v[44:47],v[28:31],v[228:231]\n"
"ds_read_b64_tr_b16 v[34:35],v100 offset:33792\n"
"v_accvgpr_read_b32 v116,a40\n"
"v_mfma_f32_16x16x32_bf16 v[196:199],v[44:47],v[20:23],0\n"
"ds_read_b64_tr_b16 v[36:37],v100 offset:40960\n"
"v_accvgpr_read_b32 v117,a41\n"
"v_mfma_f32_16x16x32_bf16 v[196:199],v[40:43],v[16:19],v[196:199]\n"
"s_add_u32 m0,s85,148480\n"
"ds_read_b64_tr_b16 v[38:39],v100 offset:41984\n"
"v_mfma_f32_16x16x32_bf16 v[164:167],v[40:43],v[8:11],0\n"
"buffer_load_dwordx4 v111,s[28:31],s86 offen lds\n"
"v_mfma_f32_16x16x32_bf16 v[164:167],v[44:47],v[12:15],v[164:167]\n"
"s_add_u32 m0,s85,155648\n"
"s_xor_b32 s86,s86,262144\n"
"v_mfma_f32_16x16x32_bf16 v[132:135],v[44:47],v[4:7],0\n"
"buffer_load_dwordx4 v110,s[28:31],s86 offen lds\n"
"v_mfma_f32_16x16x32_bf16 v[132:135],v[40:43],v[0:3],v[132:135]\n"
"v_accvgpr_read_b32 v118,a42\n"
"v_accvgpr_read_b32 v119,a43\n"
"v_mfma_f32_16x16x32_bf16 v[136:139],v[48:51],v[0:3],0\n"
"ds_read_b64_tr_b16 v[40:41],v101 offset:32768\n"
"v_accvgpr_read_b32 v120,a44\n"
"v_mfma_f32_16x16x32_bf16 v[136:139],v[52:55],v[4:7],v[136:139]\n"
"ds_read_b64_tr_b16 v[42:43],v101 offset:33792\n"
"v_accvgpr_read_b32 v121,a45\n"
"v_mfma_f32_16x16x32_bf16 v[168:171],v[52:55],v[12:15],0\n"
"ds_read_b64_tr_b16 v[44:45],v101 offset:40960\n"
"v_accvgpr_read_b32 v122,a46\n"
"v_mfma_f32_16x16x32_bf16 v[168:171],v[48:51],v[8:11],v[168:171]\n"
"s_add_u32 m0,s85,156672\n"
"ds_read_b64_tr_b16 v[46:47],v101 offset:41984\n"
"v_mfma_f32_16x16x32_bf16 v[200:203],v[48:51],v[16:19],0\n"
"buffer_load_dwordx4 v111,s[28:31],s86 offen lds\n"
"v_mfma_f32_16x16x32_bf16 v[200:203],v[52:55],v[20:23],v[200:203]\n"
"s_xor_b32 s86,s86,3932160\n"
"v_accvgpr_read_b32 v123,a47\n"
"v_mfma_f32_16x16x32_bf16 v[232:235],v[52:55],v[28:31],0\n"
"v_cvt_pk_bf16_f32 v116,v116,v117\n"
"v_cvt_pk_bf16_f32 v117,v118,v119\n"
"v_mfma_f32_16x16x32_bf16 v[232:235],v[48:51],v[24:27],v[232:235]\n"
"v_cvt_pk_bf16_f32 v118,v120,v121\n"
"v_cvt_pk_bf16_f32 v119,v122,v123\n"
"v_mfma_f32_16x16x32_bf16 v[236:239],v[56:59],v[24:27],0\n"
"ds_read_b64_tr_b16 v[48:49],v102 offset:32768\n"
"buffer_store_dwordx4 v[116:119],v112,s[24:27],s56 offen offset:1024 sc1\n"
"v_mfma_f32_16x16x32_bf16 v[236:239],v[60:63],v[28:31],v[236:239]\n"
"ds_read_b64_tr_b16 v[50:51],v102 offset:33792\n"
"v_accvgpr_read_b32 v116,a48\n"
"v_mfma_f32_16x16x32_bf16 v[204:207],v[60:63],v[20:23],0\n"
"ds_read_b64_tr_b16 v[52:53],v102 offset:40960\n"
"v_accvgpr_read_b32 v117,a49\n"
"v_mfma_f32_16x16x32_bf16 v[204:207],v[56:59],v[16:19],v[204:207]\n"
"s_add_u32 m0,s78,65536\n"
"ds_read_b64_tr_b16 v[54:55],v102 offset:41984\n"
"v_mfma_f32_16x16x32_bf16 v[172:175],v[56:59],v[8:11],0\n"
"buffer_load_dwordx4 v112,s[24:27],s76 offen offset:0 sc1 lds\n"
"v_mfma_f32_16x16x32_bf16 v[172:175],v[60:63],v[12:15],v[172:175]\n"
"v_accvgpr_read_b32 v118,a50\n"
"v_mfma_f32_16x16x32_bf16 v[140:143],v[60:63],v[4:7],0\n"
"buffer_load_dwordx4 v112,s[24:27],s76 offen offset:1024 sc1 lds\n"
"v_mfma_f32_16x16x32_bf16 v[140:143],v[56:59],v[0:3],v[140:143]\n"
"s_waitcnt vmcnt(25)\n"
"v_mfma_f32_16x16x32_bf16 v[144:147],v[64:67],v[0:3],0\n"
"ds_read_b64_tr_b16 a[32:33],v98 offset:32768\n"
"ds_read_b64_tr_b16 v[56:57],v103 offset:32768\n"
"v_mfma_f32_16x16x32_bf16 v[144:147],v[68:71],v[4:7],v[144:147]\n"
"ds_read_b64_tr_b16 a[34:35],v98 offset:33792\n"
"ds_read_b64_tr_b16 v[58:59],v103 offset:33792\n"
"v_mfma_f32_16x16x32_bf16 v[176:179],v[68:71],v[12:15],0\n"
"ds_read_b64_tr_b16 a[36:37],v98 offset:40960\n"
"ds_read_b64_tr_b16 v[60:61],v103 offset:40960\n"
"v_mfma_f32_16x16x32_bf16 v[176:179],v[64:67],v[8:11],v[176:179]\n"
"ds_read_b64_tr_b16 a[38:39],v98 offset:41984\n"
"ds_read_b64_tr_b16 v[62:63],v103 offset:41984\n"
"v_mfma_f32_16x16x32_bf16 v[208:211],v[64:67],v[16:19],0\n"
"buffer_load_dwordx4 v112,s[24:27],s76 offen offset:2048 sc1 lds\n"
"v_mfma_f32_16x16x32_bf16 v[208:211],v[68:71],v[20:23],v[208:211]\n"
"ds_read_b128 a[0:3],v96 offset:0\n"
"v_accvgpr_read_b32 v119,a51\n"
"v_mfma_f32_16x16x32_bf16 v[240:243],v[68:71],v[28:31],0\n"
"v_accvgpr_read_b32 v120,a52\n"
"v_accvgpr_read_b32 v121,a53\n"
"v_mfma_f32_16x16x32_bf16 v[240:243],v[64:67],v[24:27],v[240:243]\n"
"ds_read_b128 a[8:11],v96 offset:1024\n"
"v_accvgpr_read_b32 v122,a54\n"
"v_mfma_f32_16x16x32_bf16 v[244:247],v[72:75],v[24:27],0\n"
"ds_read_b64_tr_b16 a[40:41],v99 offset:32768\n"
"ds_read_b64_tr_b16 v[64:65],v104 offset:32768\n"
"v_mfma_f32_16x16x32_bf16 v[244:247],v[76:79],v[28:31],v[244:247]\n"
"ds_read_b64_tr_b16 a[42:43],v99 offset:33792\n"
"ds_read_b64_tr_b16 v[66:67],v104 offset:33792\n"
"v_mfma_f32_16x16x32_bf16 v[212:215],v[76:79],v[20:23],0\n"
"ds_read_b64_tr_b16 a[44:45],v99 offset:40960\n"
"ds_read_b64_tr_b16 v[68:69],v104 offset:40960\n"
"v_mfma_f32_16x16x32_bf16 v[212:215],v[72:75],v[16:19],v[212:215]\n"
"ds_read_b64_tr_b16 a[46:47],v99 offset:41984\n"
"ds_read_b64_tr_b16 v[70:71],v104 offset:41984\n"
"v_mfma_f32_16x16x32_bf16 v[180:183],v[72:75],v[8:11],0\n"
"buffer_load_dwordx4 v112,s[24:27],s76 offen offset:3072 sc1 lds\n"
"v_mfma_f32_16x16x32_bf16 v[180:183],v[76:79],v[12:15],v[180:183]\n"
"s_add_u32 m0,s78,81920\n"
"s_xor_b32 s76,s76,16384\n"
"ds_read_b128 a[16:19],v96 offset:2048\n"
"v_mfma_f32_16x16x32_bf16 v[148:151],v[76:79],v[4:7],0\n"
"buffer_load_dwordx4 v112,s[24:27],s76 offen offset:0 sc1 lds\n"
"v_mfma_f32_16x16x32_bf16 v[148:151],v[72:75],v[0:3],v[148:151]\n"
"ds_read_b128 a[24:27],v96 offset:3072\n"
"v_accvgpr_read_b32 v123,a55\n"
"v_mfma_f32_16x16x32_bf16 v[152:155],v[80:83],v[0:3],0\n"
"ds_read_b64_tr_b16 v[72:73],v105 offset:32768\n"
"v_cvt_pk_bf16_f32 v116,v116,v117\n"
"v_mfma_f32_16x16x32_bf16 v[152:155],v[84:87],v[4:7],v[152:155]\n"
"ds_read_b64_tr_b16 v[74:75],v105 offset:33792\n"
"v_cvt_pk_bf16_f32 v117,v118,v119\n"
"v_mfma_f32_16x16x32_bf16 v[184:187],v[84:87],v[12:15],0\n"
"ds_read_b64_tr_b16 v[76:77],v105 offset:40960\n"
"v_cvt_pk_bf16_f32 v118,v120,v121\n"
"v_mfma_f32_16x16x32_bf16 v[184:187],v[80:83],v[8:11],v[184:187]\n"
"ds_read_b64_tr_b16 v[78:79],v105 offset:41984\n"
"v_cvt_pk_bf16_f32 v119,v122,v123\n"
"v_mfma_f32_16x16x32_bf16 v[216:219],v[80:83],v[16:19],0\n"
"ds_read_b128 a[4:7],v96 offset:16384\n"
"buffer_store_dwordx4 v[116:119],v112,s[24:27],s56 offen offset:2048 sc1\n"
"v_mfma_f32_16x16x32_bf16 v[216:219],v[84:87],v[20:23],v[216:219]\n"
"ds_read_b128 a[12:15],v96 offset:17408\n"
"v_accvgpr_read_b32 v116,a56\n"
"v_mfma_f32_16x16x32_bf16 v[248:251],v[84:87],v[28:31],0\n"
"ds_read_b128 a[20:23],v96 offset:18432\n"
"v_accvgpr_read_b32 v117,a57\n"
"v_mfma_f32_16x16x32_bf16 v[248:251],v[80:83],v[24:27],v[248:251]\n"
"ds_read_b128 a[28:31],v96 offset:19456\n"
"v_accvgpr_read_b32 v118,a58\n"
"v_mfma_f32_16x16x32_bf16 v[252:255],v[88:91],v[24:27],0\n"
"buffer_load_dwordx4 v112,s[24:27],s76 offen offset:1024 sc1 lds\n"
"v_mfma_f32_16x16x32_bf16 v[252:255],v[92:95],v[28:31],v[252:255]\n"
"v_accvgpr_read_b32 v119,a59\n"
"v_accvgpr_read_b32 v120,a60\n"
"v_mfma_f32_16x16x32_bf16 v[220:223],v[92:95],v[20:23],0\n"
"v_accvgpr_read_b32 v121,a61\n"
"v_accvgpr_read_b32 v122,a62\n"
"v_mfma_f32_16x16x32_bf16 v[220:223],v[88:91],v[16:19],v[220:223]\n"
"buffer_load_dwordx4 v112,s[24:27],s76 offen offset:2048 sc1 lds\n"
"v_mfma_f32_16x16x32_bf16 v[188:191],v[88:91],v[8:11],0\n"
"v_accvgpr_read_b32 v123,a63\n"
"v_cvt_pk_bf16_f32 v116,v116,v117\n"
"v_mfma_f32_16x16x32_bf16 v[188:191],v[92:95],v[12:15],v[188:191]\n"
"v_cvt_pk_bf16_f32 v117,v118,v119\n"
"v_cvt_pk_bf16_f32 v118,v120,v121\n"
"v_mfma_f32_16x16x32_bf16 v[156:159],v[92:95],v[4:7],0\n"
"buffer_load_dwordx4 v112,s[24:27],s76 offen offset:3072 sc1 lds\n"
"v_mfma_f32_16x16x32_bf16 v[156:159],v[88:91],v[0:3],v[156:159]\n"
"s_xor_b32 s76,s76,245760\n"
"v_cvt_pk_bf16_f32 v119,v122,v123\n"
"s_waitcnt vmcnt(19) lgkmcnt(0)\n"
"s_barrier\n"
"v_mfma_f32_16x16x32_bf16 v[128:131],a[32:35],a[0:3],v[128:131]\n"
"ds_read_b64_tr_b16 v[80:81],v98 offset:0\n"
"ds_read_b64_tr_b16 v[88:89],v99 offset:0\n"
"v_mfma_f32_16x16x32_bf16 v[128:131],a[36:39],a[4:7],v[128:131]\n"
"ds_read_b64_tr_b16 v[82:83],v98 offset:1024\n"
"ds_read_b64_tr_b16 v[90:91],v99 offset:1024\n"
"v_mfma_f32_16x16x32_bf16 v[160:163],a[36:39],a[12:15],v[160:163]\n"
"ds_read_b64_tr_b16 v[84:85],v98 offset:8192\n"
"ds_read_b64_tr_b16 v[92:93],v99 offset:8192\n"
"v_mfma_f32_16x16x32_bf16 v[160:163],a[32:35],a[8:11],v[160:163]\n"
"s_add_u32 m0,s85,131072\n"
"ds_read_b64_tr_b16 v[86:87],v98 offset:9216\n"
"ds_read_b64_tr_b16 v[94:95],v99 offset:9216\n"
"v_mfma_f32_16x16x32_bf16 v[192:195],a[32:35],a[16:19],v[192:195]\n"
"buffer_load_dwordx4 v110,s[28:31],s86 offen lds\n"
"buffer_store_dwordx4 v[116:119],v112,s[24:27],s56 offen offset:3072 sc1\n"
"v_mfma_f32_16x16x32_bf16 v[192:195],a[36:39],a[20:23],v[192:195]\n"
"s_nop 0\n"
"v_accvgpr_read_b32 v116,a64\n"
"v_mfma_f32_16x16x32_bf16 v[224:227],a[36:39],a[28:31],v[224:227]\n"
"v_accvgpr_read_b32 v117,a65\n"
"v_accvgpr_read_b32 v118,a66\n"
"v_mfma_f32_16x16x32_bf16 v[224:227],a[32:35],a[24:27],v[224:227]\n"
"v_accvgpr_read_b32 v119,a67\n"
"v_accvgpr_read_b32 v120,a68\n"
"v_mfma_f32_16x16x32_bf16 v[228:231],a[40:43],a[24:27],v[228:231]\n"
"ds_read_b64_tr_b16 a[32:33],v100 offset:0\n"
"v_accvgpr_read_b32 v121,a69\n"
"v_mfma_f32_16x16x32_bf16 v[228:231],a[44:47],a[28:31],v[228:231]\n"
"ds_read_b64_tr_b16 a[34:35],v100 offset:1024\n"
"v_accvgpr_read_b32 v122,a70\n"
"v_mfma_f32_16x16x32_bf16 v[196:199],a[44:47],a[20:23],v[196:199]\n"
"ds_read_b64_tr_b16 a[36:37],v100 offset:8192\n"
"v_accvgpr_read_b32 v123,a71\n"
"v_mfma_f32_16x16x32_bf16 v[196:199],a[40:43],a[16:19],v[196:199]\n"
"s_add_u32 m0,s85,132096\n"
"ds_read_b64_tr_b16 a[38:39],v100 offset:9216\n"
"v_mfma_f32_16x16x32_bf16 v[164:167],a[40:43],a[8:11],v[164:167]\n"
"buffer_load_dwordx4 v111,s[28:31],s86 offen lds\n"
"v_mfma_f32_16x16x32_bf16 v[164:167],a[44:47],a[12:15],v[164:167]\n"
"s_add_u32 m0,s85,139264\n"
"s_xor_b32 s86,s86,262144\n"
"v_mfma_f32_16x16x32_bf16 v[132:135],a[44:47],a[4:7],v[132:135]\n"
"buffer_load_dwordx4 v110,s[28:31],s86 offen lds\n"
"v_mfma_f32_16x16x32_bf16 v[132:135],a[40:43],a[0:3],v[132:135]\n"
"v_cvt_pk_bf16_f32 v116,v116,v117\n"
"v_cvt_pk_bf16_f32 v117,v118,v119\n"
"v_mfma_f32_16x16x32_bf16 v[136:139],v[32:35],a[0:3],v[136:139]\n"
"ds_read_b64_tr_b16 a[40:41],v101 offset:0\n"
"v_cvt_pk_bf16_f32 v118,v120,v121\n"
"v_mfma_f32_16x16x32_bf16 v[136:139],v[36:39],a[4:7],v[136:139]\n"
"ds_read_b64_tr_b16 a[42:43],v101 offset:1024\n"
"v_cvt_pk_bf16_f32 v119,v122,v123\n"
"v_mfma_f32_16x16x32_bf16 v[168:171],v[36:39],a[12:15],v[168:171]\n"
"ds_read_b64_tr_b16 a[44:45],v101 offset:8192\n"
"s_add_u32 s56,s56,126976\n"
"v_mfma_f32_16x16x32_bf16 v[168:171],v[32:35],a[8:11],v[168:171]\n"
"s_add_u32 m0,s85,140288\n"
"ds_read_b64_tr_b16 a[46:47],v101 offset:9216\n"
"v_mfma_f32_16x16x32_bf16 v[200:203],v[32:35],a[16:19],v[200:203]\n"
"buffer_load_dwordx4 v111,s[28:31],s86 offen lds\n"
"v_mfma_f32_16x16x32_bf16 v[200:203],v[36:39],a[20:23],v[200:203]\n"
"buffer_store_dwordx4 v[116:119],v112,s[24:27],s56 offen offset:0 sc1\n"
"v_mfma_f32_16x16x32_bf16 v[232:235],v[36:39],a[28:31],v[232:235]\n"
"s_xor_b32 s86,s86,786432\n"
"v_accvgpr_read_b32 v116,a72\n"
"v_accvgpr_read_b32 v117,a73\n"
"v_mfma_f32_16x16x32_bf16 v[232:235],v[32:35],a[24:27],v[232:235]\n"
"v_accvgpr_read_b32 v118,a74\n"
"v_accvgpr_read_b32 v119,a75\n"
"v_mfma_f32_16x16x32_bf16 v[236:239],v[40:43],a[24:27],v[236:239]\n"
"ds_read_b64_tr_b16 v[32:33],v102 offset:0\n"
"v_accvgpr_read_b32 v120,a76\n"
"v_mfma_f32_16x16x32_bf16 v[236:239],v[44:47],a[28:31],v[236:239]\n"
"ds_read_b64_tr_b16 v[34:35],v102 offset:1024\n"
"v_accvgpr_read_b32 v121,a77\n"
"v_mfma_f32_16x16x32_bf16 v[204:207],v[44:47],a[20:23],v[204:207]\n"
"ds_read_b64_tr_b16 v[36:37],v102 offset:8192\n"
"v_accvgpr_read_b32 v122,a78\n"
"v_mfma_f32_16x16x32_bf16 v[204:207],v[40:43],a[16:19],v[204:207]\n"
"s_add_u32 m0,s78,0\n"
"ds_read_b64_tr_b16 v[38:39],v102 offset:9216\n"
"v_mfma_f32_16x16x32_bf16 v[172:175],v[40:43],a[8:11],v[172:175]\n"
"buffer_load_dwordx4 v112,s[24:27],s76 offen offset:0 sc1 lds\n"
"v_mfma_f32_16x16x32_bf16 v[172:175],v[44:47],a[12:15],v[172:175]\n"
"v_accvgpr_read_b32 v123,a79\n"
"v_cvt_pk_bf16_f32 v116,v116,v117\n"
"v_mfma_f32_16x16x32_bf16 v[140:143],v[44:47],a[4:7],v[140:143]\n"
"buffer_load_dwordx4 v112,s[24:27],s76 offen offset:1024 sc1 lds\n"
"v_mfma_f32_16x16x32_bf16 v[140:143],v[40:43],a[0:3],v[140:143]\n"
"s_waitcnt vmcnt(23)\n"
"v_cvt_pk_bf16_f32 v117,v118,v119\n"
"v_cvt_pk_bf16_f32 v118,v120,v121\n"
"v_mfma_f32_16x16x32_bf16 v[144:147],v[48:51],a[0:3],v[144:147]\n"
"ds_read_b64_tr_b16 v[40:41],v103 offset:0\n"
"v_cvt_pk_bf16_f32 v119,v122,v123\n"
"v_mfma_f32_16x16x32_bf16 v[144:147],v[52:55],a[4:7],v[144:147]\n"
"ds_read_b64_tr_b16 v[42:43],v103 offset:1024\n"
"buffer_store_dwordx4 v[116:119],v112,s[24:27],s56 offen offset:1024 sc1\n"
"v_mfma_f32_16x16x32_bf16 v[176:179],v[52:55],a[12:15],v[176:179]\n"
"ds_read_b64_tr_b16 v[44:45],v103 offset:8192\n"
"v_accvgpr_read_b32 v116,a80\n"
"v_mfma_f32_16x16x32_bf16 v[176:179],v[48:51],a[8:11],v[176:179]\n"
"ds_read_b64_tr_b16 v[46:47],v103 offset:9216\n"
"v_accvgpr_read_b32 v117,a81\n"
"v_mfma_f32_16x16x32_bf16 v[208:211],v[48:51],a[16:19],v[208:211]\n"
"buffer_load_dwordx4 v112,s[24:27],s76 offen offset:2048 sc1 lds\n"
"v_mfma_f32_16x16x32_bf16 v[208:211],v[52:55],a[20:23],v[208:211]\n"
"ds_read_b128 v[0:3],v96 offset:32768\n"
"v_accvgpr_read_b32 v118,a82\n"
"v_mfma_f32_16x16x32_bf16 v[240:243],v[52:55],a[28:31],v[240:243]\n"
"v_accvgpr_read_b32 v119,a83\n"
"v_accvgpr_read_b32 v120,a84\n"
"v_mfma_f32_16x16x32_bf16 v[240:243],v[48:51],a[24:27],v[240:243]\n"
"ds_read_b128 v[8:11],v96 offset:33792\n"
"v_accvgpr_read_b32 v121,a85\n"
"v_mfma_f32_16x16x32_bf16 v[244:247],v[56:59],a[24:27],v[244:247]\n"
"ds_read_b64_tr_b16 v[48:49],v104 offset:0\n"
"v_accvgpr_read_b32 v122,a86\n"
"v_mfma_f32_16x16x32_bf16 v[244:247],v[60:63],a[28:31],v[244:247]\n"
"ds_read_b64_tr_b16 v[50:51],v104 offset:1024\n"
"v_accvgpr_read_b32 v123,a87\n"
"v_mfma_f32_16x16x32_bf16 v[212:215],v[60:63],a[20:23],v[212:215]\n"
"ds_read_b64_tr_b16 v[52:53],v104 offset:8192\n"
"v_cvt_pk_bf16_f32 v116,v116,v117\n"
"v_mfma_f32_16x16x32_bf16 v[212:215],v[56:59],a[16:19],v[212:215]\n"
"ds_read_b64_tr_b16 v[54:55],v104 offset:9216\n"
"v_cvt_pk_bf16_f32 v117,v118,v119\n"
"v_mfma_f32_16x16x32_bf16 v[180:183],v[56:59],a[8:11],v[180:183]\n"
"buffer_load_dwordx4 v112,s[24:27],s76 offen offset:3072 sc1 lds\n"
"v_mfma_f32_16x16x32_bf16 v[180:183],v[60:63],a[12:15],v[180:183]\n"
"s_add_u32 m0,s78,16384\n"
"s_xor_b32 s76,s76,16384\n"
"ds_read_b128 v[16:19],v96 offset:34816\n"
"v_mfma_f32_16x16x32_bf16 v[148:151],v[60:63],a[4:7],v[148:151]\n"
"buffer_load_dwordx4 v112,s[24:27],s76 offen offset:0 sc1 lds\n"
"v_mfma_f32_16x16x32_bf16 v[148:151],v[56:59],a[0:3],v[148:151]\n"
"ds_read_b128 v[24:27],v96 offset:35840\n"
"v_cvt_pk_bf16_f32 v118,v120,v121\n"
"v_mfma_f32_16x16x32_bf16 v[152:155],v[64:67],a[0:3],v[152:155]\n"
"ds_read_b64_tr_b16 v[56:57],v105 offset:0\n"
"v_cvt_pk_bf16_f32 v119,v122,v123\n"
"v_mfma_f32_16x16x32_bf16 v[152:155],v[68:71],a[4:7],v[152:155]\n"
"ds_read_b64_tr_b16 v[58:59],v105 offset:1024\n"
"buffer_store_dwordx4 v[116:119],v112,s[24:27],s56 offen offset:2048 sc1\n"
"v_mfma_f32_16x16x32_bf16 v[184:187],v[68:71],a[12:15],v[184:187]\n"
"ds_read_b64_tr_b16 v[60:61],v105 offset:8192\n"
"v_accvgpr_read_b32 v116,a88\n"
"v_mfma_f32_16x16x32_bf16 v[184:187],v[64:67],a[8:11],v[184:187]\n"
"ds_read_b64_tr_b16 v[62:63],v105 offset:9216\n"
"v_accvgpr_read_b32 v117,a89\n"
"v_mfma_f32_16x16x32_bf16 v[216:219],v[64:67],a[16:19],v[216:219]\n"
"ds_read_b128 v[4:7],v96 offset:49152\n"
"v_accvgpr_read_b32 v118,a90\n"
"v_mfma_f32_16x16x32_bf16 v[216:219],v[68:71],a[20:23],v[216:219]\n"
"ds_read_b128 v[12:15],v96 offset:50176\n"
"v_accvgpr_read_b32 v119,a91\n"
"v_mfma_f32_16x16x32_bf16 v[248:251],v[68:71],a[28:31],v[248:251]\n"
"ds_read_b128 v[20:23],v96 offset:51200\n"
"v_accvgpr_read_b32 v120,a92\n"
"v_mfma_f32_16x16x32_bf16 v[248:251],v[64:67],a[24:27],v[248:251]\n"
"ds_read_b128 v[28:31],v96 offset:52224\n"
"v_accvgpr_read_b32 v121,a93\n"
"v_mfma_f32_16x16x32_bf16 v[252:255],v[72:75],a[24:27],v[252:255]\n"
"buffer_load_dwordx4 v112,s[24:27],s76 offen offset:1024 sc1 lds\n"
"v_mfma_f32_16x16x32_bf16 v[252:255],v[76:79],a[28:31],v[252:255]\n"
"v_accvgpr_read_b32 v122,a94\n"
"v_accvgpr_read_b32 v123,a95\n"
"v_mfma_f32_16x16x32_bf16 v[220:223],v[76:79],a[20:23],v[220:223]\n"
"v_cvt_pk_bf16_f32 v116,v116,v117\n"
"v_cvt_pk_bf16_f32 v117,v118,v119\n"
"v_mfma_f32_16x16x32_bf16 v[220:223],v[72:75],a[16:19],v[220:223]\n"
"buffer_load_dwordx4 v112,s[24:27],s76 offen offset:2048 sc1 lds\n"
"v_mfma_f32_16x16x32_bf16 v[188:191],v[72:75],a[8:11],v[188:191]\n"
"v_cvt_pk_bf16_f32 v118,v120,v121\n"
"v_cvt_pk_bf16_f32 v119,v122,v123\n"
"v_mfma_f32_16x16x32_bf16 v[188:191],v[76:79],a[12:15],v[188:191]\n"
"buffer_store_dwordx4 v[116:119],v112,s[24:27],s56 offen offset:3072 sc1\n"
"v_mfma_f32_16x16x32_bf16 v[156:159],v[76:79],a[4:7],v[156:159]\n"
"buffer_load_dwordx4 v112,s[24:27],s76 offen offset:3072 sc1 lds\n"
"v_accvgpr_read_b32 v116,a96\n"
"v_mfma_f32_16x16x32_bf16 v[156:159],v[72:75],a[0:3],v[156:159]\n"
"s_xor_b32 s76,s76,49152\n"
"v_accvgpr_read_b32 v117,a97\n"
"s_waitcnt vmcnt(27) lgkmcnt(0)\n"
"s_barrier\n"
"v_mfma_f32_16x16x32_bf16 v[128:131],v[80:83],v[0:3],v[128:131]\n"
"ds_read_b64_tr_b16 v[64:65],v98 offset:49152\n"
"ds_read_b64_tr_b16 v[72:73],v99 offset:49152\n"
"v_mfma_f32_16x16x32_bf16 v[128:131],v[84:87],v[4:7],v[128:131]\n"
"ds_read_b64_tr_b16 v[66:67],v98 offset:50176\n"
"ds_read_b64_tr_b16 v[74:75],v99 offset:50176\n"
"v_mfma_f32_16x16x32_bf16 v[160:163],v[84:87],v[12:15],v[160:163]\n"
"ds_read_b64_tr_b16 v[68:69],v98 offset:57344\n"
"ds_read_b64_tr_b16 v[76:77],v99 offset:57344\n"
"v_mfma_f32_16x16x32_bf16 v[160:163],v[80:83],v[8:11],v[160:163]\n"
"s_add_u32 m0,s85,98304\n"
"ds_read_b64_tr_b16 v[70:71],v98 offset:58368\n"
"ds_read_b64_tr_b16 v[78:79],v99 offset:58368\n"
"v_mfma_f32_16x16x32_bf16 v[192:195],v[80:83],v[16:19],v[192:195]\n"
"buffer_load_dwordx4 v110,s[28:31],s86 offen lds\n"
"v_mfma_f32_16x16x32_bf16 v[192:195],v[84:87],v[20:23],v[192:195]\n"
"v_accvgpr_read_b32 v118,a98\n"
"v_accvgpr_read_b32 v119,a99\n"
"v_mfma_f32_16x16x32_bf16 v[224:227],v[84:87],v[28:31],v[224:227]\n"
"v_accvgpr_read_b32 v120,a100\n"
"v_accvgpr_read_b32 v121,a101\n"
"v_mfma_f32_16x16x32_bf16 v[224:227],v[80:83],v[24:27],v[224:227]\n"
"v_accvgpr_read_b32 v122,a102\n"
"v_accvgpr_read_b32 v123,a103\n"
"v_mfma_f32_16x16x32_bf16 v[228:231],v[88:91],v[24:27],v[228:231]\n"
"ds_read_b64_tr_b16 v[80:81],v100 offset:49152\n"
"v_cvt_pk_bf16_f32 v116,v116,v117\n"
"v_mfma_f32_16x16x32_bf16 v[228:231],v[92:95],v[28:31],v[228:231]\n"
"ds_read_b64_tr_b16 v[82:83],v100 offset:50176\n"
"v_cvt_pk_bf16_f32 v117,v118,v119\n"
"v_mfma_f32_16x16x32_bf16 v[196:199],v[92:95],v[20:23],v[196:199]\n"
"ds_read_b64_tr_b16 v[84:85],v100 offset:57344\n"
"v_cvt_pk_bf16_f32 v118,v120,v121\n"
"v_mfma_f32_16x16x32_bf16 v[196:199],v[88:91],v[16:19],v[196:199]\n"
"s_add_u32 m0,s85,99328\n"
"ds_read_b64_tr_b16 v[86:87],v100 offset:58368\n"
"v_mfma_f32_16x16x32_bf16 v[164:167],v[88:91],v[8:11],v[164:167]\n"
"buffer_load_dwordx4 v111,s[28:31],s86 offen lds\n"
"v_mfma_f32_16x16x32_bf16 v[164:167],v[92:95],v[12:15],v[164:167]\n"
"s_add_u32 m0,s85,106496\n"
"s_xor_b32 s86,s86,262144\n"
"v_mfma_f32_16x16x32_bf16 v[132:135],v[92:95],v[4:7],v[132:135]\n"
"buffer_load_dwordx4 v110,s[28:31],s86 offen lds\n"
"v_mfma_f32_16x16x32_bf16 v[132:135],v[88:91],v[0:3],v[132:135]\n"
"v_cvt_pk_bf16_f32 v119,v122,v123\n"
"s_add_u32 s56,s56,4096\n"
"v_mfma_f32_16x16x32_bf16 v[136:139],a[32:35],v[0:3],v[136:139]\n"
"ds_read_b64_tr_b16 v[88:89],v101 offset:49152\n"
"buffer_store_dwordx4 v[116:119],v112,s[24:27],s56 offen offset:0 sc1\n"
"v_mfma_f32_16x16x32_bf16 v[136:139],a[36:39],v[4:7],v[136:139]\n"
"ds_read_b64_tr_b16 v[90:91],v101 offset:50176\n"
"v_accvgpr_read_b32 v116,a104\n"
"v_mfma_f32_16x16x32_bf16 v[168:171],a[36:39],v[12:15],v[168:171]\n"
"ds_read_b64_tr_b16 v[92:93],v101 offset:57344\n"
"v_accvgpr_read_b32 v117,a105\n"
"v_mfma_f32_16x16x32_bf16 v[168:171],a[32:35],v[8:11],v[168:171]\n"
"s_add_u32 m0,s85,107520\n"
"ds_read_b64_tr_b16 v[94:95],v101 offset:58368\n"
"v_mfma_f32_16x16x32_bf16 v[200:203],a[32:35],v[16:19],v[200:203]\n"
"buffer_load_dwordx4 v111,s[28:31],s86 offen lds\n"
"v_mfma_f32_16x16x32_bf16 v[200:203],a[36:39],v[20:23],v[200:203]\n"
"s_xor_b32 s86,s86,1835008\n"
"v_accvgpr_read_b32 v118,a106\n"
"v_mfma_f32_16x16x32_bf16 v[232:235],a[36:39],v[28:31],v[232:235]\n"
"v_accvgpr_read_b32 v119,a107\n"
"v_accvgpr_read_b32 v120,a108\n"
"v_mfma_f32_16x16x32_bf16 v[232:235],a[32:35],v[24:27],v[232:235]\n"
"v_accvgpr_read_b32 v121,a109\n"
"v_accvgpr_read_b32 v122,a110\n"
"v_mfma_f32_16x16x32_bf16 v[236:239],a[40:43],v[24:27],v[236:239]\n"
"ds_read_b64_tr_b16 a[32:33],v102 offset:49152\n"
"v_accvgpr_read_b32 v123,a111\n"
"v_mfma_f32_16x16x32_bf16 v[236:239],a[44:47],v[28:31],v[236:239]\n"
"ds_read_b64_tr_b16 a[34:35],v102 offset:50176\n"
"v_cvt_pk_bf16_f32 v116,v116,v117\n"
"v_mfma_f32_16x16x32_bf16 v[204:207],a[44:47],v[20:23],v[204:207]\n"
"ds_read_b64_tr_b16 a[36:37],v102 offset:57344\n"
"v_cvt_pk_bf16_f32 v117,v118,v119\n"
"v_mfma_f32_16x16x32_bf16 v[204:207],a[40:43],v[16:19],v[204:207]\n"
"s_add_u32 m0,s78,32768\n"
"ds_read_b64_tr_b16 a[38:39],v102 offset:58368\n"
"v_mfma_f32_16x16x32_bf16 v[172:175],a[40:43],v[8:11],v[172:175]\n"
"buffer_load_dwordx4 v112,s[24:27],s76 offen offset:0 sc1 lds\n"
"v_mfma_f32_16x16x32_bf16 v[172:175],a[44:47],v[12:15],v[172:175]\n"
"v_cvt_pk_bf16_f32 v118,v120,v121\n"
"v_cvt_pk_bf16_f32 v119,v122,v123\n"
"v_mfma_f32_16x16x32_bf16 v[140:143],a[44:47],v[4:7],v[140:143]\n"
"buffer_load_dwordx4 v112,s[24:27],s76 offen offset:1024 sc1 lds\n"
"buffer_store_dwordx4 v[116:119],v112,s[24:27],s56 offen offset:1024 sc1\n"
"v_mfma_f32_16x16x32_bf16 v[140:143],a[40:43],v[0:3],v[140:143]\n"
"s_nop 0\n"
"v_accvgpr_read_b32 v116,a112\n"
"s_waitcnt vmcnt(25)\n"
"v_mfma_f32_16x16x32_bf16 v[144:147],v[32:35],v[0:3],v[144:147]\n"
"ds_read_b64_tr_b16 a[40:41],v103 offset:49152\n"
"v_accvgpr_read_b32 v117,a113\n"
"v_mfma_f32_16x16x32_bf16 v[144:147],v[36:39],v[4:7],v[144:147]\n"
"ds_read_b64_tr_b16 a[42:43],v103 offset:50176\n"
"v_accvgpr_read_b32 v118,a114\n"
"v_mfma_f32_16x16x32_bf16 v[176:179],v[36:39],v[12:15],v[176:179]\n"
"ds_read_b64_tr_b16 a[44:45],v103 offset:57344\n"
"v_accvgpr_read_b32 v119,a115\n"
"v_mfma_f32_16x16x32_bf16 v[176:179],v[32:35],v[8:11],v[176:179]\n"
"ds_read_b64_tr_b16 a[46:47],v103 offset:58368\n"
"v_accvgpr_read_b32 v120,a116\n"
"v_mfma_f32_16x16x32_bf16 v[208:211],v[32:35],v[16:19],v[208:211]\n"
"buffer_load_dwordx4 v112,s[24:27],s76 offen offset:2048 sc1 lds\n"
"v_mfma_f32_16x16x32_bf16 v[208:211],v[36:39],v[20:23],v[208:211]\n"
"ds_read_b128 a[0:3],v97 offset:0\n"
"v_accvgpr_read_b32 v121,a117\n"
"v_mfma_f32_16x16x32_bf16 v[240:243],v[36:39],v[28:31],v[240:243]\n"
"v_accvgpr_read_b32 v122,a118\n"
"v_accvgpr_read_b32 v123,a119\n"
"v_mfma_f32_16x16x32_bf16 v[240:243],v[32:35],v[24:27],v[240:243]\n"
"ds_read_b128 a[8:11],v97 offset:1024\n"
"v_cvt_pk_bf16_f32 v116,v116,v117\n"
"v_mfma_f32_16x16x32_bf16 v[244:247],v[40:43],v[24:27],v[244:247]\n"
"ds_read_b64_tr_b16 v[32:33],v104 offset:49152\n"
"v_cvt_pk_bf16_f32 v117,v118,v119\n"
"v_mfma_f32_16x16x32_bf16 v[244:247],v[44:47],v[28:31],v[244:247]\n"
"ds_read_b64_tr_b16 v[34:35],v104 offset:50176\n"
"v_cvt_pk_bf16_f32 v118,v120,v121\n"
"v_mfma_f32_16x16x32_bf16 v[212:215],v[44:47],v[20:23],v[212:215]\n"
"ds_read_b64_tr_b16 v[36:37],v104 offset:57344\n"
"v_cvt_pk_bf16_f32 v119,v122,v123\n"
"v_mfma_f32_16x16x32_bf16 v[212:215],v[40:43],v[16:19],v[212:215]\n"
"ds_read_b64_tr_b16 v[38:39],v104 offset:58368\n"
"buffer_store_dwordx4 v[116:119],v112,s[24:27],s56 offen offset:2048 sc1\n"
"v_mfma_f32_16x16x32_bf16 v[180:183],v[40:43],v[8:11],v[180:183]\n"
"buffer_load_dwordx4 v112,s[24:27],s76 offen offset:3072 sc1 lds\n"
"v_mfma_f32_16x16x32_bf16 v[180:183],v[44:47],v[12:15],v[180:183]\n"
"s_add_u32 m0,s78,49152\n"
"s_xor_b32 s76,s76,16384\n"
"ds_read_b128 a[16:19],v97 offset:2048\n"
"v_mfma_f32_16x16x32_bf16 v[148:151],v[44:47],v[4:7],v[148:151]\n"
"buffer_load_dwordx4 v112,s[24:27],s76 offen offset:0 sc1 lds\n"
"v_mfma_f32_16x16x32_bf16 v[148:151],v[40:43],v[0:3],v[148:151]\n"
"ds_read_b128 a[24:27],v97 offset:3072\n"
"v_accvgpr_read_b32 v116,a120\n"
"v_mfma_f32_16x16x32_bf16 v[152:155],v[48:51],v[0:3],v[152:155]\n"
"ds_read_b64_tr_b16 v[40:41],v105 offset:49152\n"
"v_accvgpr_read_b32 v117,a121\n"
"v_mfma_f32_16x16x32_bf16 v[152:155],v[52:55],v[4:7],v[152:155]\n"
"ds_read_b64_tr_b16 v[42:43],v105 offset:50176\n"
"v_accvgpr_read_b32 v118,a122\n"
"v_mfma_f32_16x16x32_bf16 v[184:187],v[52:55],v[12:15],v[184:187]\n"
"ds_read_b64_tr_b16 v[44:45],v105 offset:57344\n"
"v_accvgpr_read_b32 v119,a123\n"
"v_mfma_f32_16x16x32_bf16 v[184:187],v[48:51],v[8:11],v[184:187]\n"
"ds_read_b64_tr_b16 v[46:47],v105 offset:58368\n"
"v_accvgpr_read_b32 v120,a124\n"
"v_mfma_f32_16x16x32_bf16 v[216:219],v[48:51],v[16:19],v[216:219]\n"
"ds_read_b128 a[4:7],v97 offset:16384\n"
"v_accvgpr_read_b32 v121,a125\n"
"v_mfma_f32_16x16x32_bf16 v[216:219],v[52:55],v[20:23],v[216:219]\n"
"ds_read_b128 a[12:15],v97 offset:17408\n"
"v_accvgpr_read_b32 v122,a126\n"
"v_mfma_f32_16x16x32_bf16 v[248:251],v[52:55],v[28:31],v[248:251]\n"
"ds_read_b128 a[20:23],v97 offset:18432\n"
"v_accvgpr_read_b32 v123,a127\n"
"v_mfma_f32_16x16x32_bf16 v[248:251],v[48:51],v[24:27],v[248:251]\n"
"ds_read_b128 a[28:31],v97 offset:19456\n"
"v_cvt_pk_bf16_f32 v116,v116,v117\n"
"v_mfma_f32_16x16x32_bf16 v[252:255],v[56:59],v[24:27],v[252:255]\n"
"buffer_load_dwordx4 v112,s[24:27],s76 offen offset:1024 sc1 lds\n"
"v_mfma_f32_16x16x32_bf16 v[252:255],v[60:63],v[28:31],v[252:255]\n"
"v_cvt_pk_bf16_f32 v117,v118,v119\n"
"v_cvt_pk_bf16_f32 v118,v120,v121\n"
"v_cvt_pk_bf16_f32 v119,v122,v123\n"
"v_mfma_f32_16x16x32_bf16 v[220:223],v[60:63],v[20:23],v[220:223]\n"
"buffer_store_dwordx4 v[116:119],v112,s[24:27],s56 offen offset:3072 sc1\n"
"v_mfma_f32_16x16x32_bf16 v[220:223],v[56:59],v[16:19],v[220:223]\n"
"buffer_load_dwordx4 v112,s[24:27],s76 offen offset:2048 sc1 lds\n"
"v_mfma_f32_16x16x32_bf16 v[188:191],v[56:59],v[8:11],v[188:191]\n"
"v_accvgpr_read_b32 v116,a128\n"
"v_accvgpr_read_b32 v117,a129\n"
"v_mfma_f32_16x16x32_bf16 v[188:191],v[60:63],v[12:15],v[188:191]\n"
"v_accvgpr_read_b32 v118,a130\n"
"v_accvgpr_read_b32 v119,a131\n"
"v_mfma_f32_16x16x32_bf16 v[156:159],v[60:63],v[4:7],v[156:159]\n"
"buffer_load_dwordx4 v112,s[24:27],s76 offen offset:3072 sc1 lds\n"
"v_mfma_f32_16x16x32_bf16 v[156:159],v[56:59],v[0:3],v[156:159]\n"
"s_xor_b32 s76,s76,114688\n"
"v_accvgpr_read_b32 v120,a132\n"
"s_waitcnt vmcnt(28) lgkmcnt(0)\n"
"s_barrier\n"
"v_mfma_f32_16x16x32_bf16 v[128:131],v[64:67],a[0:3],v[128:131]\n"
"ds_read_b64_tr_b16 v[48:49],v98 offset:32768\n"
"ds_read_b64_tr_b16 v[56:57],v99 offset:32768\n"
"v_mfma_f32_16x16x32_bf16 v[128:131],v[68:71],a[4:7],v[128:131]\n"
"ds_read_b64_tr_b16 v[50:51],v98 offset:33792\n"
"ds_read_b64_tr_b16 v[58:59],v99 offset:33792\n"
"v_mfma_f32_16x16x32_bf16 v[160:163],v[68:71],a[12:15],v[160:163]\n"
"ds_read_b64_tr_b16 v[52:53],v98 offset:40960\n"
"ds_read_b64_tr_b16 v[60:61],v99 offset:40960\n"
"v_mfma_f32_16x16x32_bf16 v[160:163],v[64:67],a[8:11],v[160:163]\n"
"s_add_u32 m0,s85,147456\n"
"ds_read_b64_tr_b16 v[54:55],v98 offset:41984\n"
"ds_read_b64_tr_b16 v[62:63],v99 offset:41984\n"
"v_mfma_f32_16x16x32_bf16 v[192:195],v[64:67],a[16:19],v[192:195]\n"
"buffer_load_dwordx4 v110,s[28:31],s86 offen lds\n"
"v_mfma_f32_16x16x32_bf16 v[192:195],v[68:71],a[20:23],v[192:195]\n"
"v_accvgpr_read_b32 v121,a133\n"
"v_accvgpr_read_b32 v122,a134\n"
"v_mfma_f32_16x16x32_bf16 v[224:227],v[68:71],a[28:31],v[224:227]\n"
"v_accvgpr_read_b32 v123,a135\n"
"v_cvt_pk_bf16_f32 v116,v116,v117\n"
"v_mfma_f32_16x16x32_bf16 v[224:227],v[64:67],a[24:27],v[224:227]\n"
"v_cvt_pk_bf16_f32 v117,v118,v119\n"
"v_cvt_pk_bf16_f32 v118,v120,v121\n"
"v_mfma_f32_16x16x32_bf16 v[228:231],v[72:75],a[24:27],v[228:231]\n"
"ds_read_b64_tr_b16 v[64:65],v100 offset:32768\n"
"v_cvt_pk_bf16_f32 v119,v122,v123\n"
"v_mfma_f32_16x16x32_bf16 v[228:231],v[76:79],a[28:31],v[228:231]\n"
"ds_read_b64_tr_b16 v[66:67],v100 offset:33792\n"
"s_add_u32 s56,s56,126976\n"
"v_mfma_f32_16x16x32_bf16 v[196:199],v[76:79],a[20:23],v[196:199]\n"
"ds_read_b64_tr_b16 v[68:69],v100 offset:40960\n"
"buffer_store_dwordx4 v[116:119],v112,s[24:27],s56 offen offset:0 sc1\n"
"v_mfma_f32_16x16x32_bf16 v[196:199],v[72:75],a[16:19],v[196:199]\n"
"s_add_u32 m0,s85,148480\n"
"ds_read_b64_tr_b16 v[70:71],v100 offset:41984\n"
"v_mfma_f32_16x16x32_bf16 v[164:167],v[72:75],a[8:11],v[164:167]\n"
"buffer_load_dwordx4 v111,s[28:31],s86 offen lds\n"
"v_mfma_f32_16x16x32_bf16 v[164:167],v[76:79],a[12:15],v[164:167]\n"
"s_add_u32 m0,s85,155648\n"
"s_xor_b32 s86,s86,262144\n"
"v_mfma_f32_16x16x32_bf16 v[132:135],v[76:79],a[4:7],v[132:135]\n"
"buffer_load_dwordx4 v110,s[28:31],s86 offen lds\n"
"v_mfma_f32_16x16x32_bf16 v[132:135],v[72:75],a[0:3],v[132:135]\n"
"v_accvgpr_read_b32 v116,a136\n"
"v_accvgpr_read_b32 v117,a137\n"
"v_mfma_f32_16x16x32_bf16 v[136:139],v[80:83],a[0:3],v[136:139]\n"
"ds_read_b64_tr_b16 v[72:73],v101 offset:32768\n"
"v_accvgpr_read_b32 v118,a138\n"
"v_mfma_f32_16x16x32_bf16 v[136:139],v[84:87],a[4:7],v[136:139]\n"
"ds_read_b64_tr_b16 v[74:75],v101 offset:33792\n"
"v_accvgpr_read_b32 v119,a139\n"
"v_mfma_f32_16x16x32_bf16 v[168:171],v[84:87],a[12:15],v[168:171]\n"
"ds_read_b64_tr_b16 v[76:77],v101 offset:40960\n"
"v_accvgpr_read_b32 v120,a140\n"
"v_mfma_f32_16x16x32_bf16 v[168:171],v[80:83],a[8:11],v[168:171]\n"
"s_add_u32 m0,s85,156672\n"
"ds_read_b64_tr_b16 v[78:79],v101 offset:41984\n"
"v_mfma_f32_16x16x32_bf16 v[200:203],v[80:83],a[16:19],v[200:203]\n"
"buffer_load_dwordx4 v111,s[28:31],s86 offen lds\n"
"v_mfma_f32_16x16x32_bf16 v[200:203],v[84:87],a[20:23],v[200:203]\n"
"s_xor_b32 s86,s86,786432\n"
"v_accvgpr_read_b32 v121,a141\n"
"v_mfma_f32_16x16x32_bf16 v[232:235],v[84:87],a[28:31],v[232:235]\n"
"v_accvgpr_read_b32 v122,a142\n"
"v_accvgpr_read_b32 v123,a143\n"
"v_mfma_f32_16x16x32_bf16 v[232:235],v[80:83],a[24:27],v[232:235]\n"
"v_cvt_pk_bf16_f32 v116,v116,v117\n"
"v_cvt_pk_bf16_f32 v117,v118,v119\n"
"v_mfma_f32_16x16x32_bf16 v[236:239],v[88:91],a[24:27],v[236:239]\n"
"ds_read_b64_tr_b16 v[80:81],v102 offset:32768\n"
"v_cvt_pk_bf16_f32 v118,v120,v121\n"
"v_mfma_f32_16x16x32_bf16 v[236:239],v[92:95],a[28:31],v[236:239]\n"
"ds_read_b64_tr_b16 v[82:83],v102 offset:33792\n"
"v_cvt_pk_bf16_f32 v119,v122,v123\n"
"v_mfma_f32_16x16x32_bf16 v[204:207],v[92:95],a[20:23],v[204:207]\n"
"ds_read_b64_tr_b16 v[84:85],v102 offset:40960\n"
"buffer_store_dwordx4 v[116:119],v112,s[24:27],s56 offen offset:1024 sc1\n"
"v_mfma_f32_16x16x32_bf16 v[204:207],v[88:91],a[16:19],v[204:207]\n"
"s_add_u32 m0,s78,65536\n"
"ds_read_b64_tr_b16 v[86:87],v102 offset:41984\n"
"v_accvgpr_read_b32 v118,a146\n"
"v_mfma_f32_16x16x32_bf16 v[172:175],v[88:91],a[8:11],v[172:175]\n"
"buffer_load_dwordx4 v112,s[24:27],s76 offen offset:0 sc1 lds\n"
"v_mfma_f32_16x16x32_bf16 v[172:175],v[92:95],a[12:15],v[172:175]\n"
"v_accvgpr_read_b32 v116,a144\n"
"v_accvgpr_read_b32 v117,a145\n"
"v_mfma_f32_16x16x32_bf16 v[140:143],v[92:95],a[4:7],v[140:143]\n"
"buffer_load_dwordx4 v112,s[24:27],s76 offen offset:1024 sc1 lds\n"
"v_mfma_f32_16x16x32_bf16 v[140:143],v[88:91],a[0:3],v[140:143]\n"
"v_accvgpr_read_b32 v119,a147\n"
"s_waitcnt vmcnt(24)\n"
"v_mfma_f32_16x16x32_bf16 v[144:147],a[32:35],a[0:3],v[144:147]\n"
"ds_read_b64_tr_b16 v[88:89],v103 offset:32768\n"
"v_accvgpr_read_b32 v120,a148\n"
"v_mfma_f32_16x16x32_bf16 v[144:147],a[36:39],a[4:7],v[144:147]\n"
"ds_read_b64_tr_b16 v[90:91],v103 offset:33792\n"
"v_accvgpr_read_b32 v121,a149\n"
"v_mfma_f32_16x16x32_bf16 v[176:179],a[36:39],a[12:15],v[176:179]\n"
"ds_read_b64_tr_b16 v[92:93],v103 offset:40960\n"
"v_accvgpr_read_b32 v122,a150\n"
"v_mfma_f32_16x16x32_bf16 v[176:179],a[32:35],a[8:11],v[176:179]\n"
"ds_read_b64_tr_b16 v[94:95],v103 offset:41984\n"
"v_accvgpr_read_b32 v123,a151\n"
"v_mfma_f32_16x16x32_bf16 v[208:211],a[32:35],a[16:19],v[208:211]\n"
"buffer_load_dwordx4 v112,s[24:27],s76 offen offset:2048 sc1 lds\n"
"v_mfma_f32_16x16x32_bf16 v[208:211],a[36:39],a[20:23],v[208:211]\n"
"ds_read_b128 v[0:3],v96 offset:0\n"
"v_cvt_pk_bf16_f32 v116,v116,v117\n"
"v_mfma_f32_16x16x32_bf16 v[240:243],a[36:39],a[28:31],v[240:243]\n"
"v_cvt_pk_bf16_f32 v117,v118,v119\n"
"v_cvt_pk_bf16_f32 v118,v120,v121\n"
"v_mfma_f32_16x16x32_bf16 v[240:243],a[32:35],a[24:27],v[240:243]\n"
"ds_read_b128 v[8:11],v96 offset:1024\n"
"v_cvt_pk_bf16_f32 v119,v122,v123\n"
"v_mfma_f32_16x16x32_bf16 v[244:247],a[40:43],a[24:27],v[244:247]\n"
"ds_read_b64_tr_b16 a[32:33],v104 offset:32768\n"
"buffer_store_dwordx4 v[116:119],v112,s[24:27],s56 offen offset:2048 sc1\n"
"v_mfma_f32_16x16x32_bf16 v[244:247],a[44:47],a[28:31],v[244:247]\n"
"ds_read_b64_tr_b16 a[34:35],v104 offset:33792\n"
"v_accvgpr_read_b32 v116,a152\n"
"v_mfma_f32_16x16x32_bf16 v[212:215],a[44:47],a[20:23],v[212:215]\n"
"ds_read_b64_tr_b16 a[36:37],v104 offset:40960\n"
"v_accvgpr_read_b32 v117,a153\n"
"v_mfma_f32_16x16x32_bf16 v[212:215],a[40:43],a[16:19],v[212:215]\n"
"ds_read_b64_tr_b16 a[38:39],v104 offset:41984\n"
"v_accvgpr_read_b32 v118,a154\n"
"v_mfma_f32_16x16x32_bf16 v[180:183],a[40:43],a[8:11],v[180:183]\n"
"buffer_load_dwordx4 v112,s[24:27],s76 offen offset:3072 sc1 lds\n"
"v_mfma_f32_16x16x32_bf16 v[180:183],a[44:47],a[12:15],v[180:183]\n"
"s_add_u32 m0,s78,81920\n"
"s_xor_b32 s76,s76,16384\n"
"ds_read_b128 v[16:19],v96 offset:2048\n"
"v_mfma_f32_16x16x32_bf16 v[148:151],a[44:47],a[4:7],v[148:151]\n"
"buffer_load_dwordx4 v112,s[24:27],s76 offen offset:0 sc1 lds\n"
"v_mfma_f32_16x16x32_bf16 v[148:151],a[40:43],a[0:3],v[148:151]\n"
"ds_read_b128 v[24:27],v96 offset:3072\n"
"v_accvgpr_read_b32 v119,a155\n"
"v_mfma_f32_16x16x32_bf16 v[152:155],v[32:35],a[0:3],v[152:155]\n"
"ds_read_b64_tr_b16 a[40:41],v105 offset:32768\n"
"v_accvgpr_read_b32 v120,a156\n"
"v_mfma_f32_16x16x32_bf16 v[152:155],v[36:39],a[4:7],v[152:155]\n"
"ds_read_b64_tr_b16 a[42:43],v105 offset:33792\n"
"v_accvgpr_read_b32 v121,a157\n"
"v_mfma_f32_16x16x32_bf16 v[184:187],v[36:39],a[12:15],v[184:187]\n"
"ds_read_b64_tr_b16 a[44:45],v105 offset:40960\n"
"v_accvgpr_read_b32 v122,a158\n"
"v_mfma_f32_16x16x32_bf16 v[184:187],v[32:35],a[8:11],v[184:187]\n"
"ds_read_b64_tr_b16 a[46:47],v105 offset:41984\n"
"v_accvgpr_read_b32 v123,a159\n"
"v_mfma_f32_16x16x32_bf16 v[216:219],v[32:35],a[16:19],v[216:219]\n"
"ds_read_b128 v[4:7],v96 offset:16384\n"
"v_cvt_pk_bf16_f32 v116,v116,v117\n"
"v_mfma_f32_16x16x32_bf16 v[216:219],v[36:39],a[20:23],v[216:219]\n"
"ds_read_b128 v[12:15],v96 offset:17408\n"
"v_cvt_pk_bf16_f32 v117,v118,v119\n"
"v_mfma_f32_16x16x32_bf16 v[248:251],v[36:39],a[28:31],v[248:251]\n"
"ds_read_b128 v[20:23],v96 offset:18432\n"
"v_cvt_pk_bf16_f32 v118,v120,v121\n"
"v_mfma_f32_16x16x32_bf16 v[248:251],v[32:35],a[24:27],v[248:251]\n"
"ds_read_b128 v[28:31],v96 offset:19456\n"
"v_cvt_pk_bf16_f32 v119,v122,v123\n"
"v_mfma_f32_16x16x32_bf16 v[252:255],v[40:43],a[24:27],v[252:255]\n"
"buffer_load_dwordx4 v112,s[24:27],s76 offen offset:1024 sc1 lds\n"
"buffer_store_dwordx4 v[116:119],v112,s[24:27],s56 offen offset:3072 sc1\n"
"v_mfma_f32_16x16x32_bf16 v[252:255],v[44:47],a[28:31],v[252:255]\n"
"s_nop 0\n"
"v_accvgpr_read_b32 v116,a160\n"
"v_mfma_f32_16x16x32_bf16 v[220:223],v[44:47],a[20:23],v[220:223]\n"
"v_accvgpr_read_b32 v117,a161\n"
"v_accvgpr_read_b32 v118,a162\n"
"v_mfma_f32_16x16x32_bf16 v[220:223],v[40:43],a[16:19],v[220:223]\n"
"buffer_load_dwordx4 v112,s[24:27],s76 offen offset:2048 sc1 lds\n"
"v_mfma_f32_16x16x32_bf16 v[188:191],v[40:43],a[8:11],v[188:191]\n"
"v_accvgpr_read_b32 v119,a163\n"
"v_accvgpr_read_b32 v120,a164\n"
"v_mfma_f32_16x16x32_bf16 v[188:191],v[44:47],a[12:15],v[188:191]\n"
"v_accvgpr_read_b32 v121,a165\n"
"v_accvgpr_read_b32 v122,a166\n"
"v_mfma_f32_16x16x32_bf16 v[156:159],v[44:47],a[4:7],v[156:159]\n"
"buffer_load_dwordx4 v112,s[24:27],s76 offen offset:3072 sc1 lds\n"
"v_mfma_f32_16x16x32_bf16 v[156:159],v[40:43],a[0:3],v[156:159]\n"
"s_xor_b32 s76,s76,49152\n"
"v_accvgpr_read_b32 v123,a167\n"
"s_waitcnt vmcnt(27) lgkmcnt(0)\n"
"s_barrier\n"
"v_mfma_f32_16x16x32_bf16 v[128:131],v[48:51],v[0:3],v[128:131]\n"
"ds_read_b64_tr_b16 v[32:33],v98 offset:0\n"
"ds_read_b64_tr_b16 v[40:41],v99 offset:0\n"
"v_mfma_f32_16x16x32_bf16 v[128:131],v[52:55],v[4:7],v[128:131]\n"
"ds_read_b64_tr_b16 v[34:35],v98 offset:1024\n"
"ds_read_b64_tr_b16 v[42:43],v99 offset:1024\n"
"v_mfma_f32_16x16x32_bf16 v[160:163],v[52:55],v[12:15],v[160:163]\n"
"ds_read_b64_tr_b16 v[36:37],v98 offset:8192\n"
"ds_read_b64_tr_b16 v[44:45],v99 offset:8192\n"
"v_mfma_f32_16x16x32_bf16 v[160:163],v[48:51],v[8:11],v[160:163]\n"
"s_add_u32 m0,s85,131072\n"
"ds_read_b64_tr_b16 v[38:39],v98 offset:9216\n"
"ds_read_b64_tr_b16 v[46:47],v99 offset:9216\n"
"v_mfma_f32_16x16x32_bf16 v[192:195],v[48:51],v[16:19],v[192:195]\n"
"buffer_load_dwordx4 v110,s[28:31],s86 offen lds\n"
"v_mfma_f32_16x16x32_bf16 v[192:195],v[52:55],v[20:23],v[192:195]\n"
"v_cvt_pk_bf16_f32 v116,v116,v117\n"
"v_cvt_pk_bf16_f32 v117,v118,v119\n"
"v_mfma_f32_16x16x32_bf16 v[224:227],v[52:55],v[28:31],v[224:227]\n"
"s_add_u32 s56,s56,4096\n"
"v_cvt_pk_bf16_f32 v118,v120,v121\n"
"v_cvt_pk_bf16_f32 v119,v122,v123\n"
"v_mfma_f32_16x16x32_bf16 v[224:227],v[48:51],v[24:27],v[224:227]\n"
"buffer_store_dwordx4 v[116:119],v112,s[24:27],s56 offen offset:0 sc1\n"
"v_mfma_f32_16x16x32_bf16 v[228:231],v[56:59],v[24:27],v[228:231]\n"
"ds_read_b64_tr_b16 v[48:49],v100 offset:0\n"
"v_accvgpr_read_b32 v116,a168\n"
"v_mfma_f32_16x16x32_bf16 v[228:231],v[60:63],v[28:31],v[228:231]\n"
"ds_read_b64_tr_b16 v[50:51],v100 offset:1024\n"
"v_accvgpr_read_b32 v117,a169\n"
"v_mfma_f32_16x16x32_bf16 v[196:199],v[60:63],v[20:23],v[196:199]\n"
"ds_read_b64_tr_b16 v[52:53],v100 offset:8192\n"
"v_accvgpr_read_b32 v118,a170\n"
"v_mfma_f32_16x16x32_bf16 v[196:199],v[56:59],v[16:19],v[196:199]\n"
"s_add_u32 m0,s85,132096\n"
"ds_read_b64_tr_b16 v[54:55],v100 offset:9216\n"
"v_mfma_f32_16x16x32_bf16 v[164:167],v[56:59],v[8:11],v[164:167]\n"
"buffer_load_dwordx4 v111,s[28:31],s86 offen lds\n"
"v_mfma_f32_16x16x32_bf16 v[164:167],v[60:63],v[12:15],v[164:167]\n"
"s_add_u32 m0,s85,139264\n"
"s_xor_b32 s86,s86,262144\n"
"v_mfma_f32_16x16x32_bf16 v[132:135],v[60:63],v[4:7],v[132:135]\n"
"buffer_load_dwordx4 v110,s[28:31],s86 offen lds\n"
"v_mfma_f32_16x16x32_bf16 v[132:135],v[56:59],v[0:3],v[132:135]\n"
"v_accvgpr_read_b32 v119,a171\n"
"v_accvgpr_read_b32 v120,a172\n"
"v_mfma_f32_16x16x32_bf16 v[136:139],v[64:67],v[0:3],v[136:139]\n"
"ds_read_b64_tr_b16 v[56:57],v101 offset:0\n"
"v_accvgpr_read_b32 v121,a173\n"
"v_mfma_f32_16x16x32_bf16 v[136:139],v[68:71],v[4:7],v[136:139]\n"
"ds_read_b64_tr_b16 v[58:59],v101 offset:1024\n"
"v_accvgpr_read_b32 v122,a174\n"
"v_mfma_f32_16x16x32_bf16 v[168:171],v[68:71],v[12:15],v[168:171]\n"
"ds_read_b64_tr_b16 v[60:61],v101 offset:8192\n"
"v_accvgpr_read_b32 v123,a175\n"
"v_mfma_f32_16x16x32_bf16 v[168:171],v[64:67],v[8:11],v[168:171]\n"
"s_add_u32 m0,s85,140288\n"
"ds_read_b64_tr_b16 v[62:63],v101 offset:9216\n"
"v_mfma_f32_16x16x32_bf16 v[200:203],v[64:67],v[16:19],v[200:203]\n"
"buffer_load_dwordx4 v111,s[28:31],s86 offen lds\n"
"v_mfma_f32_16x16x32_bf16 v[200:203],v[68:71],v[20:23],v[200:203]\n"
"s_xor_b32 s86,s86,8126464\n"
"v_cvt_pk_bf16_f32 v116,v116,v117\n"
"v_cvt_pk_bf16_f32 v117,v118,v119\n"
"v_mfma_f32_16x16x32_bf16 v[232:235],v[68:71],v[28:31],v[232:235]\n"
"v_cvt_pk_bf16_f32 v118,v120,v121\n"
"v_cvt_pk_bf16_f32 v119,v122,v123\n"
"v_mfma_f32_16x16x32_bf16 v[232:235],v[64:67],v[24:27],v[232:235]\n"
"buffer_store_dwordx4 v[116:119],v112,s[24:27],s56 offen offset:1024 sc1\n"
"v_mfma_f32_16x16x32_bf16 v[236:239],v[72:75],v[24:27],v[236:239]\n"
"ds_read_b64_tr_b16 v[64:65],v102 offset:0\n"
"v_accvgpr_read_b32 v116,a176\n"
"v_mfma_f32_16x16x32_bf16 v[236:239],v[76:79],v[28:31],v[236:239]\n"
"ds_read_b64_tr_b16 v[66:67],v102 offset:1024\n"
"v_accvgpr_read_b32 v117,a177\n"
"v_mfma_f32_16x16x32_bf16 v[204:207],v[76:79],v[20:23],v[204:207]\n"
"ds_read_b64_tr_b16 v[68:69],v102 offset:8192\n"
"v_accvgpr_read_b32 v118,a178\n"
"v_mfma_f32_16x16x32_bf16 v[204:207],v[72:75],v[16:19],v[204:207]\n"
"s_add_u32 m0,s78,0\n"
"ds_read_b64_tr_b16 v[70:71],v102 offset:9216\n"
"v_accvgpr_read_b32 v121,a181\n"
"v_mfma_f32_16x16x32_bf16 v[172:175],v[72:75],v[8:11],v[172:175]\n"
"buffer_load_dwordx4 v112,s[24:27],s76 offen offset:0 sc1 lds\n"
"v_mfma_f32_16x16x32_bf16 v[172:175],v[76:79],v[12:15],v[172:175]\n"
"v_accvgpr_read_b32 v119,a179\n"
"v_accvgpr_read_b32 v120,a180\n"
"v_mfma_f32_16x16x32_bf16 v[140:143],v[76:79],v[4:7],v[140:143]\n"
"buffer_load_dwordx4 v112,s[24:27],s76 offen offset:1024 sc1 lds\n"
"v_mfma_f32_16x16x32_bf16 v[140:143],v[72:75],v[0:3],v[140:143]\n"
"v_accvgpr_read_b32 v122,a182\n"
"s_waitcnt vmcnt(24)\n"
"v_mfma_f32_16x16x32_bf16 v[144:147],v[80:83],v[0:3],v[144:147]\n"
"ds_read_b64_tr_b16 v[72:73],v103 offset:0\n"
"v_accvgpr_read_b32 v123,a183\n"
"v_mfma_f32_16x16x32_bf16 v[144:147],v[84:87],v[4:7],v[144:147]\n"
"ds_read_b64_tr_b16 v[74:75],v103 offset:1024\n"
"v_cvt_pk_bf16_f32 v116,v116,v117\n"
"v_mfma_f32_16x16x32_bf16 v[176:179],v[84:87],v[12:15],v[176:179]\n"
"ds_read_b64_tr_b16 v[76:77],v103 offset:8192\n"
"v_cvt_pk_bf16_f32 v117,v118,v119\n"
"v_mfma_f32_16x16x32_bf16 v[176:179],v[80:83],v[8:11],v[176:179]\n"
"ds_read_b64_tr_b16 v[78:79],v103 offset:9216\n"
"v_cvt_pk_bf16_f32 v118,v120,v121\n"
"v_mfma_f32_16x16x32_bf16 v[208:211],v[80:83],v[16:19],v[208:211]\n"
"buffer_load_dwordx4 v112,s[24:27],s76 offen offset:2048 sc1 lds\n"
"v_mfma_f32_16x16x32_bf16 v[208:211],v[84:87],v[20:23],v[208:211]\n"
"ds_read_b128 a[0:3],v96 offset:32768\n"
"v_cvt_pk_bf16_f32 v119,v122,v123\n"
"v_mfma_f32_16x16x32_bf16 v[240:243],v[84:87],v[28:31],v[240:243]\n"
"buffer_store_dwordx4 v[116:119],v112,s[24:27],s56 offen offset:2048 sc1\n"
"v_mfma_f32_16x16x32_bf16 v[240:243],v[80:83],v[24:27],v[240:243]\n"
"ds_read_b128 a[8:11],v96 offset:33792\n"
"v_accvgpr_read_b32 v116,a184\n"
"v_accvgpr_read_b32 v117,a185\n"
"v_mfma_f32_16x16x32_bf16 v[244:247],v[88:91],v[24:27],v[244:247]\n"
"ds_read_b64_tr_b16 v[80:81],v104 offset:0\n"
"v_accvgpr_read_b32 v118,a186\n"
"v_mfma_f32_16x16x32_bf16 v[244:247],v[92:95],v[28:31],v[244:247]\n"
"ds_read_b64_tr_b16 v[82:83],v104 offset:1024\n"
"v_accvgpr_read_b32 v119,a187\n"
"v_mfma_f32_16x16x32_bf16 v[212:215],v[92:95],v[20:23],v[212:215]\n"
"ds_read_b64_tr_b16 v[84:85],v104 offset:8192\n"
"v_accvgpr_read_b32 v120,a188\n"
"v_mfma_f32_16x16x32_bf16 v[212:215],v[88:91],v[16:19],v[212:215]\n"
"ds_read_b64_tr_b16 v[86:87],v104 offset:9216\n"
"v_accvgpr_read_b32 v121,a189\n"
"v_mfma_f32_16x16x32_bf16 v[180:183],v[88:91],v[8:11],v[180:183]\n"
"buffer_load_dwordx4 v112,s[24:27],s76 offen offset:3072 sc1 lds\n"
"v_mfma_f32_16x16x32_bf16 v[180:183],v[92:95],v[12:15],v[180:183]\n"
"s_add_u32 m0,s78,16384\n"
"s_xor_b32 s76,s76,16384\n"
"ds_read_b128 a[16:19],v96 offset:34816\n"
"v_mfma_f32_16x16x32_bf16 v[148:151],v[92:95],v[4:7],v[148:151]\n"
"buffer_load_dwordx4 v112,s[24:27],s76 offen offset:0 sc1 lds\n"
"v_mfma_f32_16x16x32_bf16 v[148:151],v[88:91],v[0:3],v[148:151]\n"
"ds_read_b128 a[24:27],v96 offset:35840\n"
"v_accvgpr_read_b32 v122,a190\n"
"v_mfma_f32_16x16x32_bf16 v[152:155],a[32:35],v[0:3],v[152:155]\n"
"ds_read_b64_tr_b16 v[88:89],v105 offset:0\n"
"v_accvgpr_read_b32 v123,a191\n"
"v_mfma_f32_16x16x32_bf16 v[152:155],a[36:39],v[4:7],v[152:155]\n"
"ds_read_b64_tr_b16 v[90:91],v105 offset:1024\n"
"v_cvt_pk_bf16_f32 v116,v116,v117\n"
"v_mfma_f32_16x16x32_bf16 v[184:187],a[36:39],v[12:15],v[184:187]\n"
"ds_read_b64_tr_b16 v[92:93],v105 offset:8192\n"
"v_cvt_pk_bf16_f32 v117,v118,v119\n"
"v_mfma_f32_16x16x32_bf16 v[184:187],a[32:35],v[8:11],v[184:187]\n"
"ds_read_b64_tr_b16 v[94:95],v105 offset:9216\n"
"v_cvt_pk_bf16_f32 v118,v120,v121\n"
"v_mfma_f32_16x16x32_bf16 v[216:219],a[32:35],v[16:19],v[216:219]\n"
"ds_read_b128 a[4:7],v96 offset:49152\n"
"v_cvt_pk_bf16_f32 v119,v122,v123\n"
"v_mfma_f32_16x16x32_bf16 v[216:219],a[36:39],v[20:23],v[216:219]\n"
"ds_read_b128 a[12:15],v96 offset:50176\n"
"buffer_store_dwordx4 v[116:119],v112,s[24:27],s56 offen offset:3072 sc1\n"
"v_mfma_f32_16x16x32_bf16 v[248:251],a[36:39],v[28:31],v[248:251]\n"
"ds_read_b128 a[20:23],v96 offset:51200\n"
"v_accvgpr_read_b32 v116,a248\n"
"v_mfma_f32_16x16x32_bf16 v[248:251],a[32:35],v[24:27],v[248:251]\n"
"ds_read_b128 a[28:31],v96 offset:52224\n"
"v_accvgpr_read_b32 v117,a249\n"
"v_mfma_f32_16x16x32_bf16 v[252:255],a[40:43],v[24:27],v[252:255]\n"
"buffer_load_dwordx4 v112,s[24:27],s76 offen offset:1024 sc1 lds\n"
"v_mfma_f32_16x16x32_bf16 v[252:255],a[44:47],v[28:31],v[252:255]\n"
"v_accvgpr_read_b32 v118,a250\n"
"v_accvgpr_read_b32 v119,a251\n"
"v_mfma_f32_16x16x32_bf16 v[220:223],a[44:47],v[20:23],v[220:223]\n"
"v_accvgpr_read_b32 v120,a252\n"
"v_accvgpr_read_b32 v121,a253\n"
"v_mfma_f32_16x16x32_bf16 v[220:223],a[40:43],v[16:19],v[220:223]\n"
"buffer_load_dwordx4 v112,s[24:27],s76 offen offset:2048 sc1 lds\n"
"v_mfma_f32_16x16x32_bf16 v[188:191],a[40:43],v[8:11],v[188:191]\n"
"v_accvgpr_read_b32 v122,a254\n"
"v_accvgpr_read_b32 v123,a255\n"
"v_mfma_f32_16x16x32_bf16 v[188:191],a[44:47],v[12:15],v[188:191]\n"
"v_cvt_pk_bf16_f32 v116,v116,v117\n"
"v_cvt_pk_bf16_f32 v117,v118,v119\n"
"v_mfma_f32_16x16x32_bf16 v[156:159],a[44:47],v[4:7],v[156:159]\n"
"buffer_load_dwordx4 v112,s[24:27],s76 offen offset:3072 sc1 lds\n"
"v_mfma_f32_16x16x32_bf16 v[156:159],a[40:43],v[0:3],v[156:159]\n"
"s_xor_b32 s76,s76,507904\n"
"v_cvt_pk_bf16_f32 v118,v120,v121\n"
"s_waitcnt vmcnt(27) lgkmcnt(0)\n"
"s_barrier\n"
"v_mfma_f32_16x16x32_bf16 v[128:131],v[32:35],a[0:3],v[128:131]\n"
"ds_read_b64_tr_b16 a[32:33],v98 offset:49152\n"
"ds_read_b64_tr_b16 a[40:41],v99 offset:49152\n"
"v_mfma_f32_16x16x32_bf16 v[128:131],v[36:39],a[4:7],v[128:131]\n"
"ds_read_b64_tr_b16 a[34:35],v98 offset:50176\n"
"ds_read_b64_tr_b16 a[42:43],v99 offset:50176\n"
"v_mfma_f32_16x16x32_bf16 v[160:163],v[36:39],a[12:15],v[160:163]\n"
"ds_read_b64_tr_b16 a[36:37],v98 offset:57344\n"
"ds_read_b64_tr_b16 a[44:45],v99 offset:57344\n"
"v_mfma_f32_16x16x32_bf16 v[160:163],v[32:35],a[8:11],v[160:163]\n"
"s_add_u32 m0,s85,98304\n"
"ds_read_b64_tr_b16 a[38:39],v98 offset:58368\n"
"ds_read_b64_tr_b16 a[46:47],v99 offset:58368\n"
"v_mfma_f32_16x16x32_bf16 v[192:195],v[32:35],a[16:19],v[192:195]\n"
"buffer_load_dwordx4 v110,s[28:31],s86 offen lds\n"
"v_mfma_f32_16x16x32_bf16 v[192:195],v[36:39],a[20:23],v[192:195]\n"
"v_cvt_pk_bf16_f32 v119,v122,v123\n"
"v_accvgpr_write_b32 a252,v116\n"
"v_mfma_f32_16x16x32_bf16 v[224:227],v[36:39],a[28:31],v[224:227]\n"
"v_accvgpr_write_b32 a253,v117\n"
"v_accvgpr_write_b32 a254,v118\n"
"v_mfma_f32_16x16x32_bf16 v[224:227],v[32:35],a[24:27],v[224:227]\n"
"v_accvgpr_write_b32 a255,v119\n"
"v_accvgpr_read_b32 v116,a240\n"
"v_mfma_f32_16x16x32_bf16 v[228:231],v[40:43],a[24:27],v[228:231]\n"
"ds_read_b64_tr_b16 v[32:33],v100 offset:49152\n"
"v_accvgpr_read_b32 v117,a241\n"
"v_mfma_f32_16x16x32_bf16 v[228:231],v[44:47],a[28:31],v[228:231]\n"
"ds_read_b64_tr_b16 v[34:35],v100 offset:50176\n"
"v_accvgpr_read_b32 v118,a242\n"
"v_mfma_f32_16x16x32_bf16 v[196:199],v[44:47],a[20:23],v[196:199]\n"
"ds_read_b64_tr_b16 v[36:37],v100 offset:57344\n"
"v_accvgpr_read_b32 v119,a243\n"
"v_mfma_f32_16x16x32_bf16 v[196:199],v[40:43],a[16:19],v[196:199]\n"
"s_add_u32 m0,s85,99328\n"
"ds_read_b64_tr_b16 v[38:39],v100 offset:58368\n"
"v_mfma_f32_16x16x32_bf16 v[164:167],v[40:43],a[8:11],v[164:167]\n"
"buffer_load_dwordx4 v111,s[28:31],s86 offen lds\n"
"v_mfma_f32_16x16x32_bf16 v[164:167],v[44:47],a[12:15],v[164:167]\n"
"s_add_u32 m0,s85,106496\n"
"s_xor_b32 s86,s86,262144\n"
"v_mfma_f32_16x16x32_bf16 v[132:135],v[44:47],a[4:7],v[132:135]\n"
"buffer_load_dwordx4 v110,s[28:31],s86 offen lds\n"
"v_mfma_f32_16x16x32_bf16 v[132:135],v[40:43],a[0:3],v[132:135]\n"
"v_accvgpr_read_b32 v120,a244\n"
"v_accvgpr_read_b32 v121,a245\n"
"v_mfma_f32_16x16x32_bf16 v[136:139],v[48:51],a[0:3],v[136:139]\n"
"ds_read_b64_tr_b16 v[40:41],v101 offset:49152\n"
"v_accvgpr_read_b32 v122,a246\n"
"v_mfma_f32_16x16x32_bf16 v[136:139],v[52:55],a[4:7],v[136:139]\n"
"ds_read_b64_tr_b16 v[42:43],v101 offset:50176\n"
"v_accvgpr_read_b32 v123,a247\n"
"v_mfma_f32_16x16x32_bf16 v[168:171],v[52:55],a[12:15],v[168:171]\n"
"ds_read_b64_tr_b16 v[44:45],v101 offset:57344\n"
"v_cvt_pk_bf16_f32 v116,v116,v117\n"
"v_mfma_f32_16x16x32_bf16 v[168:171],v[48:51],a[8:11],v[168:171]\n"
"s_add_u32 m0,s85,107520\n"
"ds_read_b64_tr_b16 v[46:47],v101 offset:58368\n"
"v_mfma_f32_16x16x32_bf16 v[200:203],v[48:51],a[16:19],v[200:203]\n"
"buffer_load_dwordx4 v111,s[28:31],s86 offen lds\n"
"v_mfma_f32_16x16x32_bf16 v[200:203],v[52:55],a[20:23],v[200:203]\n"
"s_xor_b32 s86,s86,786432\n"
"v_cvt_pk_bf16_f32 v117,v118,v119\n"
"v_mfma_f32_16x16x32_bf16 v[232:235],v[52:55],a[28:31],v[232:235]\n"
"v_cvt_pk_bf16_f32 v118,v120,v121\n"
"v_cvt_pk_bf16_f32 v119,v122,v123\n"
"v_mfma_f32_16x16x32_bf16 v[232:235],v[48:51],a[24:27],v[232:235]\n"
"v_accvgpr_write_b32 a248,v116\n"
"v_accvgpr_write_b32 a249,v117\n"
"v_mfma_f32_16x16x32_bf16 v[236:239],v[56:59],a[24:27],v[236:239]\n"
"ds_read_b64_tr_b16 v[48:49],v102 offset:49152\n"
"v_accvgpr_write_b32 a250,v118\n"
"v_mfma_f32_16x16x32_bf16 v[236:239],v[60:63],a[28:31],v[236:239]\n"
"ds_read_b64_tr_b16 v[50:51],v102 offset:50176\n"
"v_accvgpr_write_b32 a251,v119\n"
"v_mfma_f32_16x16x32_bf16 v[204:207],v[60:63],a[20:23],v[204:207]\n"
"ds_read_b64_tr_b16 v[52:53],v102 offset:57344\n"
"v_accvgpr_read_b32 v116,a232\n"
"v_mfma_f32_16x16x32_bf16 v[204:207],v[56:59],a[16:19],v[204:207]\n"
"s_add_u32 m0,s78,32768\n"
"ds_read_b64_tr_b16 v[54:55],v102 offset:58368\n"
"v_accvgpr_read_b32 v119,a235\n"
"v_mfma_f32_16x16x32_bf16 v[172:175],v[56:59],a[8:11],v[172:175]\n"
"buffer_load_dwordx4 v112,s[24:27],s76 offen offset:0 sc1 lds\n"
"v_mfma_f32_16x16x32_bf16 v[172:175],v[60:63],a[12:15],v[172:175]\n"
"v_accvgpr_read_b32 v117,a233\n"
"v_accvgpr_read_b32 v118,a234\n"
"v_mfma_f32_16x16x32_bf16 v[140:143],v[60:63],a[4:7],v[140:143]\n"
"buffer_load_dwordx4 v112,s[24:27],s76 offen offset:1024 sc1 lds\n"
"v_mfma_f32_16x16x32_bf16 v[140:143],v[56:59],a[0:3],v[140:143]\n"
"v_accvgpr_read_b32 v120,a236\n"
"s_waitcnt vmcnt(22)\n"
"v_mfma_f32_16x16x32_bf16 v[144:147],v[64:67],a[0:3],v[144:147]\n"
"ds_read_b64_tr_b16 v[56:57],v103 offset:49152\n"
"v_accvgpr_read_b32 v121,a237\n"
"v_mfma_f32_16x16x32_bf16 v[144:147],v[68:71],a[4:7],v[144:147]\n"
"ds_read_b64_tr_b16 v[58:59],v103 offset:50176\n"
"v_accvgpr_read_b32 v122,a238\n"
"v_mfma_f32_16x16x32_bf16 v[176:179],v[68:71],a[12:15],v[176:179]\n"
"ds_read_b64_tr_b16 v[60:61],v103 offset:57344\n"
"v_accvgpr_read_b32 v123,a239\n"
"v_mfma_f32_16x16x32_bf16 v[176:179],v[64:67],a[8:11],v[176:179]\n"
"ds_read_b64_tr_b16 v[62:63],v103 offset:58368\n"
"v_cvt_pk_bf16_f32 v116,v116,v117\n"
"v_mfma_f32_16x16x32_bf16 v[208:211],v[64:67],a[16:19],v[208:211]\n"
"buffer_load_dwordx4 v112,s[24:27],s76 offen offset:2048 sc1 lds\n"
"v_mfma_f32_16x16x32_bf16 v[208:211],v[68:71],a[20:23],v[208:211]\n"
"ds_read_b128 v[0:3],v97 offset:0\n"
"v_cvt_pk_bf16_f32 v117,v118,v119\n"
"v_mfma_f32_16x16x32_bf16 v[240:243],v[68:71],a[28:31],v[240:243]\n"
"v_cvt_pk_bf16_f32 v118,v120,v121\n"
"v_cvt_pk_bf16_f32 v119,v122,v123\n"
"v_mfma_f32_16x16x32_bf16 v[240:243],v[64:67],a[24:27],v[240:243]\n"
"ds_read_b128 v[8:11],v97 offset:1024\n"
"v_accvgpr_write_b32 a244,v116\n"
"v_mfma_f32_16x16x32_bf16 v[244:247],v[72:75],a[24:27],v[244:247]\n"
"ds_read_b64_tr_b16 v[64:65],v104 offset:49152\n"
"v_accvgpr_write_b32 a245,v117\n"
"v_mfma_f32_16x16x32_bf16 v[244:247],v[76:79],a[28:31],v[244:247]\n"
"ds_read_b64_tr_b16 v[66:67],v104 offset:50176\n"
"v_accvgpr_write_b32 a246,v118\n"
"v_mfma_f32_16x16x32_bf16 v[212:215],v[76:79],a[20:23],v[212:215]\n"
"ds_read_b64_tr_b16 v[68:69],v104 offset:57344\n"
"v_accvgpr_write_b32 a247,v119\n"
"v_mfma_f32_16x16x32_bf16 v[212:215],v[72:75],a[16:19],v[212:215]\n"
"ds_read_b64_tr_b16 v[70:71],v104 offset:58368\n"
"v_accvgpr_read_b32 v116,a224\n"
"v_mfma_f32_16x16x32_bf16 v[180:183],v[72:75],a[8:11],v[180:183]\n"
"buffer_load_dwordx4 v112,s[24:27],s76 offen offset:3072 sc1 lds\n"
"v_mfma_f32_16x16x32_bf16 v[180:183],v[76:79],a[12:15],v[180:183]\n"
"s_add_u32 m0,s78,49152\n"
"s_xor_b32 s76,s76,16384\n"
"ds_read_b128 v[16:19],v97 offset:2048\n"
"v_mfma_f32_16x16x32_bf16 v[148:151],v[76:79],a[4:7],v[148:151]\n"
"buffer_load_dwordx4 v112,s[24:27],s76 offen offset:0 sc1 lds\n"
"v_mfma_f32_16x16x32_bf16 v[148:151],v[72:75],a[0:3],v[148:151]\n"
"ds_read_b128 v[24:27],v97 offset:3072\n"
"v_accvgpr_read_b32 v117,a225\n"
"v_mfma_f32_16x16x32_bf16 v[152:155],v[80:83],a[0:3],v[152:155]\n"
"ds_read_b64_tr_b16 v[72:73],v105 offset:49152\n"
"v_accvgpr_read_b32 v118,a226\n"
"v_mfma_f32_16x16x32_bf16 v[152:155],v[84:87],a[4:7],v[152:155]\n"
"ds_read_b64_tr_b16 v[74:75],v105 offset:50176\n"
"v_accvgpr_read_b32 v119,a227\n"
"v_mfma_f32_16x16x32_bf16 v[184:187],v[84:87],a[12:15],v[184:187]\n"
"ds_read_b64_tr_b16 v[76:77],v105 offset:57344\n"
"v_accvgpr_read_b32 v120,a228\n"
"v_mfma_f32_16x16x32_bf16 v[184:187],v[80:83],a[8:11],v[184:187]\n"
"ds_read_b64_tr_b16 v[78:79],v105 offset:58368\n"
"v_accvgpr_read_b32 v121,a229\n"
"v_mfma_f32_16x16x32_bf16 v[216:219],v[80:83],a[16:19],v[216:219]\n"
"ds_read_b128 v[4:7],v97 offset:16384\n"
"v_accvgpr_read_b32 v122,a230\n"
"v_mfma_f32_16x16x32_bf16 v[216:219],v[84:87],a[20:23],v[216:219]\n"
"ds_read_b128 v[12:15],v97 offset:17408\n"
"v_accvgpr_read_b32 v123,a231\n"
"v_mfma_f32_16x16x32_bf16 v[248:251],v[84:87],a[28:31],v[248:251]\n"
"ds_read_b128 v[20:23],v97 offset:18432\n"
"v_cvt_pk_bf16_f32 v116,v116,v117\n"
"v_mfma_f32_16x16x32_bf16 v[248:251],v[80:83],a[24:27],v[248:251]\n"
"ds_read_b128 v[28:31],v97 offset:19456\n"
"v_cvt_pk_bf16_f32 v117,v118,v119\n"
"v_mfma_f32_16x16x32_bf16 v[252:255],v[88:91],a[24:27],v[252:255]\n"
"buffer_load_dwordx4 v112,s[24:27],s76 offen offset:1024 sc1 lds\n"
"v_mfma_f32_16x16x32_bf16 v[252:255],v[92:95],a[28:31],v[252:255]\n"
"v_cvt_pk_bf16_f32 v118,v120,v121\n"
"v_cvt_pk_bf16_f32 v119,v122,v123\n"
"v_mfma_f32_16x16x32_bf16 v[220:223],v[92:95],a[20:23],v[220:223]\n"
"v_accvgpr_write_b32 a240,v116\n"
"v_accvgpr_write_b32 a241,v117\n"
"v_mfma_f32_16x16x32_bf16 v[220:223],v[88:91],a[16:19],v[220:223]\n"
"buffer_load_dwordx4 v112,s[24:27],s76 offen offset:2048 sc1 lds\n"
"v_mfma_f32_16x16x32_bf16 v[188:191],v[88:91],a[8:11],v[188:191]\n"
"v_accvgpr_write_b32 a242,v118\n"
"v_accvgpr_write_b32 a243,v119\n"
"v_mfma_f32_16x16x32_bf16 v[188:191],v[92:95],a[12:15],v[188:191]\n"
"v_accvgpr_read_b32 v116,a216\n"
"v_accvgpr_read_b32 v117,a217\n"
"v_mfma_f32_16x16x32_bf16 v[156:159],v[92:95],a[4:7],v[156:159]\n"
"buffer_load_dwordx4 v112,s[24:27],s76 offen offset:3072 sc1 lds\n"
"v_mfma_f32_16x16x32_bf16 v[156:159],v[88:91],a[0:3],v[156:159]\n"
"s_xor_b32 s76,s76,49152\n"
"v_accvgpr_read_b32 v118,a218\n"
"s_waitcnt vmcnt(23) lgkmcnt(0)\n"
"s_barrier\n"
"v_mfma_f32_16x16x32_bf16 v[128:131],a[32:35],v[0:3],v[128:131]\n"
"ds_read_b64_tr_b16 v[80:81],v98 offset:32768\n"
"ds_read_b64_tr_b16 v[88:89],v99 offset:32768\n"
"v_mfma_f32_16x16x32_bf16 v[128:131],a[36:39],v[4:7],v[128:131]\n"
"ds_read_b64_tr_b16 v[82:83],v98 offset:33792\n"
"ds_read_b64_tr_b16 v[90:91],v99 offset:33792\n"
"v_mfma_f32_16x16x32_bf16 v[160:163],a[36:39],v[12:15],v[160:163]\n"
"ds_read_b64_tr_b16 v[84:85],v98 offset:40960\n"
"ds_read_b64_tr_b16 v[92:93],v99 offset:40960\n"
"v_mfma_f32_16x16x32_bf16 v[160:163],a[32:35],v[8:11],v[160:163]\n"
"s_add_u32 m0,s85,147456\n"
"ds_read_b64_tr_b16 v[86:87],v98 offset:41984\n"
"ds_read_b64_tr_b16 v[94:95],v99 offset:41984\n"
"v_mfma_f32_16x16x32_bf16 v[192:195],a[32:35],v[16:19],v[192:195]\n"
"buffer_load_dwordx4 v110,s[28:31],s86 offen lds\n"
"v_mfma_f32_16x16x32_bf16 v[192:195],a[36:39],v[20:23],v[192:195]\n"
"v_accvgpr_read_b32 v119,a219\n"
"v_accvgpr_read_b32 v120,a220\n"
"v_mfma_f32_16x16x32_bf16 v[224:227],a[36:39],v[28:31],v[224:227]\n"
"v_accvgpr_read_b32 v121,a221\n"
"v_accvgpr_read_b32 v122,a222\n"
"v_mfma_f32_16x16x32_bf16 v[224:227],a[32:35],v[24:27],v[224:227]\n"
"v_accvgpr_read_b32 v123,a223\n"
"v_cvt_pk_bf16_f32 v116,v116,v117\n"
"v_mfma_f32_16x16x32_bf16 v[228:231],a[40:43],v[24:27],v[228:231]\n"
"ds_read_b64_tr_b16 a[32:33],v100 offset:32768\n"
"v_cvt_pk_bf16_f32 v117,v118,v119\n"
"v_mfma_f32_16x16x32_bf16 v[228:231],a[44:47],v[28:31],v[228:231]\n"
"ds_read_b64_tr_b16 a[34:35],v100 offset:33792\n"
"v_cvt_pk_bf16_f32 v118,v120,v121\n"
"v_mfma_f32_16x16x32_bf16 v[196:199],a[44:47],v[20:23],v[196:199]\n"
"ds_read_b64_tr_b16 a[36:37],v100 offset:40960\n"
"v_cvt_pk_bf16_f32 v119,v122,v123\n"
"v_mfma_f32_16x16x32_bf16 v[196:199],a[40:43],v[16:19],v[196:199]\n"
"s_add_u32 m0,s85,148480\n"
"ds_read_b64_tr_b16 a[38:39],v100 offset:41984\n"
"v_mfma_f32_16x16x32_bf16 v[164:167],a[40:43],v[8:11],v[164:167]\n"
"buffer_load_dwordx4 v111,s[28:31],s86 offen lds\n"
"v_mfma_f32_16x16x32_bf16 v[164:167],a[44:47],v[12:15],v[164:167]\n"
"s_add_u32 m0,s85,155648\n"
"s_xor_b32 s86,s86,262144\n"
"v_mfma_f32_16x16x32_bf16 v[132:135],a[44:47],v[4:7],v[132:135]\n"
"buffer_load_dwordx4 v110,s[28:31],s86 offen lds\n"
"v_mfma_f32_16x16x32_bf16 v[132:135],a[40:43],v[0:3],v[132:135]\n"
"v_accvgpr_write_b32 a236,v116\n"
"v_accvgpr_write_b32 a237,v117\n"
"v_mfma_f32_16x16x32_bf16 v[136:139],v[32:35],v[0:3],v[136:139]\n"
"ds_read_b64_tr_b16 a[40:41],v101 offset:32768\n"
"v_accvgpr_write_b32 a238,v118\n"
"v_mfma_f32_16x16x32_bf16 v[136:139],v[36:39],v[4:7],v[136:139]\n"
"ds_read_b64_tr_b16 a[42:43],v101 offset:33792\n"
"v_accvgpr_write_b32 a239,v119\n"
"v_mfma_f32_16x16x32_bf16 v[168:171],v[36:39],v[12:15],v[168:171]\n"
"ds_read_b64_tr_b16 a[44:45],v101 offset:40960\n"
"v_accvgpr_read_b32 v116,a208\n"
"v_mfma_f32_16x16x32_bf16 v[168:171],v[32:35],v[8:11],v[168:171]\n"
"s_add_u32 m0,s85,156672\n"
"ds_read_b64_tr_b16 a[46:47],v101 offset:41984\n"
"v_mfma_f32_16x16x32_bf16 v[200:203],v[32:35],v[16:19],v[200:203]\n"
"buffer_load_dwordx4 v111,s[28:31],s86 offen lds\n"
"v_mfma_f32_16x16x32_bf16 v[200:203],v[36:39],v[20:23],v[200:203]\n"
"s_xor_b32 s86,s86,1835008\n"
"v_accvgpr_read_b32 v117,a209\n"
"v_mfma_f32_16x16x32_bf16 v[232:235],v[36:39],v[28:31],v[232:235]\n"
"v_accvgpr_read_b32 v118,a210\n"
"v_accvgpr_read_b32 v119,a211\n"
"v_mfma_f32_16x16x32_bf16 v[232:235],v[32:35],v[24:27],v[232:235]\n"
"v_accvgpr_read_b32 v120,a212\n"
"v_accvgpr_read_b32 v121,a213\n"
"v_mfma_f32_16x16x32_bf16 v[236:239],v[40:43],v[24:27],v[236:239]\n"
"ds_read_b64_tr_b16 v[32:33],v102 offset:32768\n"
"v_accvgpr_read_b32 v122,a214\n"
"v_mfma_f32_16x16x32_bf16 v[236:239],v[44:47],v[28:31],v[236:239]\n"
"ds_read_b64_tr_b16 v[34:35],v102 offset:33792\n"
"v_accvgpr_read_b32 v123,a215\n"
"v_mfma_f32_16x16x32_bf16 v[204:207],v[44:47],v[20:23],v[204:207]\n"
"ds_read_b64_tr_b16 v[36:37],v102 offset:40960\n"
"v_cvt_pk_bf16_f32 v116,v116,v117\n"
"v_mfma_f32_16x16x32_bf16 v[204:207],v[40:43],v[16:19],v[204:207]\n"
"s_add_u32 m0,s78,65536\n"
"ds_read_b64_tr_b16 v[38:39],v102 offset:41984\n"
"v_accvgpr_write_b32 a232,v116\n"
"v_mfma_f32_16x16x32_bf16 v[172:175],v[40:43],v[8:11],v[172:175]\n"
"buffer_load_dwordx4 v112,s[24:27],s76 offen offset:0 sc1 lds\n"
"v_mfma_f32_16x16x32_bf16 v[172:175],v[44:47],v[12:15],v[172:175]\n"
"v_cvt_pk_bf16_f32 v117,v118,v119\n"
"v_cvt_pk_bf16_f32 v118,v120,v121\n"
"v_mfma_f32_16x16x32_bf16 v[140:143],v[44:47],v[4:7],v[140:143]\n"
"buffer_load_dwordx4 v112,s[24:27],s76 offen offset:1024 sc1 lds\n"
"v_mfma_f32_16x16x32_bf16 v[140:143],v[40:43],v[0:3],v[140:143]\n"
"v_cvt_pk_bf16_f32 v119,v122,v123\n"
"s_waitcnt vmcnt(18)\n"
"v_mfma_f32_16x16x32_bf16 v[144:147],v[48:51],v[0:3],v[144:147]\n"
"ds_read_b64_tr_b16 v[40:41],v103 offset:32768\n"
"v_accvgpr_write_b32 a233,v117\n"
"v_lshrrev_b32 v106,8,v112\n"
"v_mfma_f32_16x16x32_bf16 v[144:147],v[52:55],v[4:7],v[144:147]\n"
"ds_read_b64_tr_b16 v[42:43],v103 offset:33792\n"
"v_accvgpr_write_b32 a234,v118\n"
"v_lshlrev_b32 v107,6,v106\n"
"v_mfma_f32_16x16x32_bf16 v[176:179],v[52:55],v[12:15],v[176:179]\n"
"ds_read_b64_tr_b16 v[44:45],v103 offset:40960\n"
"v_accvgpr_write_b32 a235,v119\n"
"v_and_b32 v108,0xf0,v112\n"
"v_mfma_f32_16x16x32_bf16 v[176:179],v[48:51],v[8:11],v[176:179]\n"
"ds_read_b64_tr_b16 v[46:47],v103 offset:41984\n"
"v_accvgpr_read_b32 v116,a200\n"
"v_xor_b32 v107,v107,v108\n"
"v_mfma_f32_16x16x32_bf16 v[208:211],v[48:51],v[16:19],v[208:211]\n"
"buffer_load_dwordx4 v112,s[24:27],s76 offen offset:2048 sc1 lds\n"
"v_lshl_add_u32 v106,v106,11,v107\n"
"v_mfma_f32_16x16x32_bf16 v[208:211],v[52:55],v[20:23],v[208:211]\n"
"ds_read_b128 a[0:3],v96 offset:0\n"
"v_accvgpr_read_b32 v117,a201\n"
"v_lshrrev_b32 v107,6,v112\n"
"v_mfma_f32_16x16x32_bf16 v[240:243],v[52:55],v[28:31],v[240:243]\n"
"v_accvgpr_read_b32 v118,a202\n"
"v_accvgpr_read_b32 v119,a203\n"
"v_and_b32 v108,3,v107\n"
"v_mfma_f32_16x16x32_bf16 v[240:243],v[48:51],v[24:27],v[240:243]\n"
"ds_read_b128 a[8:11],v96 offset:1024\n"
"v_accvgpr_read_b32 v120,a204\n"
"v_lshlrev_b32 v108,6,v108\n"
"v_mfma_f32_16x16x32_bf16 v[244:247],v[56:59],v[24:27],v[244:247]\n"
"ds_read_b64_tr_b16 v[48:49],v104 offset:32768\n"
"v_accvgpr_read_b32 v121,a205\n"
"v_mfma_f32_16x16x32_bf16 v[244:247],v[60:63],v[28:31],v[244:247]\n"
"s_lshl_b32 s82,s52,6\n"
"ds_read_b64_tr_b16 v[50:51],v104 offset:33792\n"
"v_accvgpr_read_b32 v122,a206\n"
"v_xor_b32 v108,s82,v108\n"
"v_mfma_f32_16x16x32_bf16 v[212:215],v[60:63],v[20:23],v[212:215]\n"
"ds_read_b64_tr_b16 v[52:53],v104 offset:40960\n"
"v_accvgpr_read_b32 v123,a207\n"
"v_lshl_add_u32 v107,v107,8,v108\n"
"v_mfma_f32_16x16x32_bf16 v[212:215],v[56:59],v[16:19],v[212:215]\n"
"ds_read_b64_tr_b16 v[54:55],v104 offset:41984\n"
"v_cvt_pk_bf16_f32 v116,v116,v117\n"
"v_and_b32 v108,0x30,v112\n"
"v_mfma_f32_16x16x32_bf16 v[180:183],v[56:59],v[8:11],v[180:183]\n"
"buffer_load_dwordx4 v112,s[24:27],s76 offen offset:3072 sc1 lds\n"
"v_add_u32 v107,v107,v108\n"
"v_mfma_f32_16x16x32_bf16 v[180:183],v[60:63],v[12:15],v[180:183]\n"
"s_add_u32 m0,s78,81920\n"
"s_xor_b32 s76,s76,16384\n"
"ds_read_b128 a[16:19],v96 offset:2048\n"
"v_mfma_f32_16x16x32_bf16 v[148:151],v[60:63],v[4:7],v[148:151]\n"
"buffer_load_dwordx4 v112,s[24:27],s76 offen offset:0 sc1 lds\n"
"v_add_u32 v107,114688,v107\n"
"v_mfma_f32_16x16x32_bf16 v[148:151],v[56:59],v[0:3],v[148:151]\n"
"ds_read_b128 a[24:27],v96 offset:3072\n"
"v_cvt_pk_bf16_f32 v117,v118,v119\n"
"v_mfma_f32_16x16x32_bf16 v[152:155],v[64:67],v[0:3],v[152:155]\n"
"ds_read_b64_tr_b16 v[56:57],v105 offset:32768\n"
"v_cvt_pk_bf16_f32 v118,v120,v121\n"
"v_mfma_f32_16x16x32_bf16 v[152:155],v[68:71],v[4:7],v[152:155]\n"
"ds_read_b64_tr_b16 v[58:59],v105 offset:33792\n"
"v_cvt_pk_bf16_f32 v119,v122,v123\n"
"v_mfma_f32_16x16x32_bf16 v[184:187],v[68:71],v[12:15],v[184:187]\n"
"ds_read_b64_tr_b16 v[60:61],v105 offset:40960\n"
"v_accvgpr_write_b32 a228,v116\n"
"v_mfma_f32_16x16x32_bf16 v[184:187],v[64:67],v[8:11],v[184:187]\n"
"ds_read_b64_tr_b16 v[62:63],v105 offset:41984\n"
"v_accvgpr_write_b32 a229,v117\n"
"v_mfma_f32_16x16x32_bf16 v[216:219],v[64:67],v[16:19],v[216:219]\n"
"ds_read_b128 a[4:7],v96 offset:16384\n"
"v_accvgpr_write_b32 a230,v118\n"
"v_mfma_f32_16x16x32_bf16 v[216:219],v[68:71],v[20:23],v[216:219]\n"
"ds_read_b128 a[12:15],v96 offset:17408\n"
"v_accvgpr_write_b32 a231,v119\n"
"v_mfma_f32_16x16x32_bf16 v[248:251],v[68:71],v[28:31],v[248:251]\n"
"ds_read_b128 a[20:23],v96 offset:18432\n"
"v_accvgpr_read_b32 v116,a192\n"
"v_mfma_f32_16x16x32_bf16 v[248:251],v[64:67],v[24:27],v[248:251]\n"
"ds_read_b128 a[28:31],v96 offset:19456\n"
"v_accvgpr_read_b32 v117,a193\n"
"v_mfma_f32_16x16x32_bf16 v[252:255],v[72:75],v[24:27],v[252:255]\n"
"buffer_load_dwordx4 v112,s[24:27],s76 offen offset:1024 sc1 lds\n"
"v_mfma_f32_16x16x32_bf16 v[252:255],v[76:79],v[28:31],v[252:255]\n"
"v_accvgpr_read_b32 v118,a194\n"
"v_accvgpr_read_b32 v119,a195\n"
"v_mfma_f32_16x16x32_bf16 v[220:223],v[76:79],v[20:23],v[220:223]\n"
"v_accvgpr_read_b32 v120,a196\n"
"v_accvgpr_read_b32 v121,a197\n"
"v_mfma_f32_16x16x32_bf16 v[220:223],v[72:75],v[16:19],v[220:223]\n"
"buffer_load_dwordx4 v112,s[24:27],s76 offen offset:2048 sc1 lds\n"
"v_mfma_f32_16x16x32_bf16 v[188:191],v[72:75],v[8:11],v[188:191]\n"
"v_accvgpr_read_b32 v122,a198\n"
"v_accvgpr_read_b32 v123,a199\n"
"v_mfma_f32_16x16x32_bf16 v[188:191],v[76:79],v[12:15],v[188:191]\n"
"v_cvt_pk_bf16_f32 v116,v116,v117\n"
"v_cvt_pk_bf16_f32 v117,v118,v119\n"
"v_mfma_f32_16x16x32_bf16 v[156:159],v[76:79],v[4:7],v[156:159]\n"
"buffer_load_dwordx4 v112,s[24:27],s76 offen offset:3072 sc1 lds\n"
"v_mfma_f32_16x16x32_bf16 v[156:159],v[72:75],v[0:3],v[156:159]\n"
"s_xor_b32 s76,s76,114688\n"
"v_cvt_pk_bf16_f32 v118,v120,v121\n"
"s_waitcnt vmcnt(20) lgkmcnt(0)\n"
"s_barrier\n"
"v_mfma_f32_16x16x32_bf16 v[128:131],v[80:83],a[0:3],v[128:131]\n"
"ds_read_b64_tr_b16 v[64:65],v98 offset:0\n"
"ds_read_b64_tr_b16 v[72:73],v99 offset:0\n"
"v_mfma_f32_16x16x32_bf16 v[128:131],v[84:87],a[4:7],v[128:131]\n"
"ds_read_b64_tr_b16 v[66:67],v98 offset:1024\n"
"ds_read_b64_tr_b16 v[74:75],v99 offset:1024\n"
"v_mfma_f32_16x16x32_bf16 v[160:163],v[84:87],a[12:15],v[160:163]\n"
"ds_read_b64_tr_b16 v[68:69],v98 offset:8192\n"
"ds_read_b64_tr_b16 v[76:77],v99 offset:8192\n"
"v_mfma_f32_16x16x32_bf16 v[160:163],v[80:83],a[8:11],v[160:163]\n"
"s_add_u32 m0,s85,131072\n"
"ds_read_b64_tr_b16 v[70:71],v98 offset:9216\n"
"ds_read_b64_tr_b16 v[78:79],v99 offset:9216\n"
"v_mfma_f32_16x16x32_bf16 v[192:195],v[80:83],a[16:19],v[192:195]\n"
"buffer_load_dwordx4 v110,s[28:31],s86 offen lds\n"
"v_mfma_f32_16x16x32_bf16 v[192:195],v[84:87],a[20:23],v[192:195]\n"
"s_add_u32 m0,s85,132096\n"
"v_cvt_pk_bf16_f32 v119,v122,v123\n"
"v_mfma_f32_16x16x32_bf16 v[224:227],v[84:87],a[28:31],v[224:227]\n"
"buffer_load_dwordx4 v111,s[28:31],s86 offen lds\n"
"v_mfma_f32_16x16x32_bf16 v[224:227],v[80:83],a[24:27],v[224:227]\n"
"s_xor_b32 s86,s86,262144\n"
"v_accvgpr_write_b32 a224,v116\n"
"v_mfma_f32_16x16x32_bf16 v[228:231],v[88:91],a[24:27],v[228:231]\n"
"ds_read_b64_tr_b16 v[80:81],v100 offset:0\n"
"v_accvgpr_write_b32 a225,v117\n"
"v_mfma_f32_16x16x32_bf16 v[228:231],v[92:95],a[28:31],v[228:231]\n"
"ds_read_b64_tr_b16 v[82:83],v100 offset:1024\n"
"v_accvgpr_write_b32 a226,v118\n"
"v_mfma_f32_16x16x32_bf16 v[196:199],v[92:95],a[20:23],v[196:199]\n"
"ds_read_b64_tr_b16 v[84:85],v100 offset:8192\n"
"v_accvgpr_write_b32 a227,v119\n"
"v_mfma_f32_16x16x32_bf16 v[196:199],v[88:91],a[16:19],v[196:199]\n"
"s_add_u32 m0,s85,139264\n"
"ds_read_b64_tr_b16 v[86:87],v100 offset:9216\n"
"v_mfma_f32_16x16x32_bf16 v[164:167],v[88:91],a[8:11],v[164:167]\n"
"buffer_load_dwordx4 v110,s[28:31],s86 offen lds\n"
"v_mfma_f32_16x16x32_bf16 v[164:167],v[92:95],a[12:15],v[164:167]\n"
"s_add_u32 m0,s85,140288\n"
"s_add_u32 s56,s56,126976\n"
"v_mfma_f32_16x16x32_bf16 v[132:135],v[92:95],a[4:7],v[132:135]\n"
"buffer_load_dwordx4 v111,s[28:31],s86 offen lds\n"
"v_mfma_f32_16x16x32_bf16 v[132:135],v[88:91],a[0:3],v[132:135]\n"
"s_xor_b32 s86,s86,786432\n"
"v_mfma_f32_16x16x32_bf16 v[136:139],a[32:35],a[0:3],v[136:139]\n"
"ds_read_b64_tr_b16 v[88:89],v101 offset:0\n"
"v_mfma_f32_16x16x32_bf16 v[136:139],a[36:39],a[4:7],v[136:139]\n"
"ds_read_b64_tr_b16 v[90:91],v101 offset:1024\n"
"v_mfma_f32_16x16x32_bf16 v[168:171],a[36:39],a[12:15],v[168:171]\n"
"ds_read_b64_tr_b16 v[92:93],v101 offset:8192\n"
"v_mfma_f32_16x16x32_bf16 v[168:171],a[32:35],a[8:11],v[168:171]\n"
"s_add_u32 m0,s78,0\n"
"ds_read_b64_tr_b16 v[94:95],v101 offset:9216\n"
"v_mfma_f32_16x16x32_bf16 v[200:203],a[32:35],a[16:19],v[200:203]\n"
"buffer_load_dwordx4 v112,s[24:27],s76 offen offset:0 sc1 lds\n"
"v_mfma_f32_16x16x32_bf16 v[200:203],a[36:39],a[20:23],v[200:203]\n"
"v_mfma_f32_16x16x32_bf16 v[232:235],a[36:39],a[28:31],v[232:235]\n"
"buffer_load_dwordx4 v112,s[24:27],s76 offen offset:1024 sc1 lds\n"
"v_mfma_f32_16x16x32_bf16 v[232:235],a[32:35],a[24:27],v[232:235]\n"
"v_mfma_f32_16x16x32_bf16 v[236:239],a[40:43],a[24:27],v[236:239]\n"
"ds_read_b64_tr_b16 a[32:33],v102 offset:0\n"
"v_mfma_f32_16x16x32_bf16 v[236:239],a[44:47],a[28:31],v[236:239]\n"
"ds_read_b64_tr_b16 a[34:35],v102 offset:1024\n"
"v_mfma_f32_16x16x32_bf16 v[204:207],a[44:47],a[20:23],v[204:207]\n"
"ds_read_b64_tr_b16 a[36:37],v102 offset:8192\n"
"v_mfma_f32_16x16x32_bf16 v[204:207],a[40:43],a[16:19],v[204:207]\n"
"ds_read_b64_tr_b16 a[38:39],v102 offset:9216\n"
"v_mfma_f32_16x16x32_bf16 v[172:175],a[40:43],a[8:11],v[172:175]\n"
"buffer_load_dwordx4 v112,s[24:27],s76 offen offset:2048 sc1 lds\n"
"v_mfma_f32_16x16x32_bf16 v[172:175],a[44:47],a[12:15],v[172:175]\n"
"v_mfma_f32_16x16x32_bf16 v[140:143],a[44:47],a[4:7],v[140:143]\n"
"buffer_load_dwordx4 v112,s[24:27],s76 offen offset:3072 sc1 lds\n"
"v_mfma_f32_16x16x32_bf16 v[140:143],a[40:43],a[0:3],v[140:143]\n"
"s_xor_b32 s76,s76,16384\n"
"s_waitcnt vmcnt(20)\n"
"v_mfma_f32_16x16x32_bf16 v[144:147],v[32:35],a[0:3],v[144:147]\n"
"ds_read_b64_tr_b16 a[40:41],v103 offset:0\n"
"v_mfma_f32_16x16x32_bf16 v[144:147],v[36:39],a[4:7],v[144:147]\n"
"ds_read_b64_tr_b16 a[42:43],v103 offset:1024\n"
"v_mfma_f32_16x16x32_bf16 v[176:179],v[36:39],a[12:15],v[176:179]\n"
"ds_read_b64_tr_b16 a[44:45],v103 offset:8192\n"
"v_mfma_f32_16x16x32_bf16 v[176:179],v[32:35],a[8:11],v[176:179]\n"
"s_add_u32 m0,s78,16384\n"
"ds_read_b64_tr_b16 a[46:47],v103 offset:9216\n"
"v_mfma_f32_16x16x32_bf16 v[208:211],v[32:35],a[16:19],v[208:211]\n"
"buffer_load_dwordx4 v112,s[24:27],s76 offen offset:0 sc1 lds\n"
"v_mfma_f32_16x16x32_bf16 v[208:211],v[36:39],a[20:23],v[208:211]\n"
"ds_read_b128 v[0:3],v96 offset:32768\n"
"v_mfma_f32_16x16x32_bf16 v[240:243],v[36:39],a[28:31],v[240:243]\n"
"buffer_load_dwordx4 v112,s[24:27],s76 offen offset:1024 sc1 lds\n"
"v_mfma_f32_16x16x32_bf16 v[240:243],v[32:35],a[24:27],v[240:243]\n"
"ds_read_b128 v[8:11],v96 offset:33792\n"
"v_mfma_f32_16x16x32_bf16 v[244:247],v[40:43],a[24:27],v[244:247]\n"
"ds_read_b64_tr_b16 v[32:33],v104 offset:0\n"
"v_mfma_f32_16x16x32_bf16 v[244:247],v[44:47],a[28:31],v[244:247]\n"
"ds_read_b64_tr_b16 v[34:35],v104 offset:1024\n"
"v_mfma_f32_16x16x32_bf16 v[212:215],v[44:47],a[20:23],v[212:215]\n"
"ds_read_b64_tr_b16 v[36:37],v104 offset:8192\n"
"v_mfma_f32_16x16x32_bf16 v[212:215],v[40:43],a[16:19],v[212:215]\n"
"ds_read_b64_tr_b16 v[38:39],v104 offset:9216\n"
"v_mfma_f32_16x16x32_bf16 v[180:183],v[40:43],a[8:11],v[180:183]\n"
"buffer_load_dwordx4 v112,s[24:27],s76 offen offset:2048 sc1 lds\n"
"v_mfma_f32_16x16x32_bf16 v[180:183],v[44:47],a[12:15],v[180:183]\n"
"ds_read_b128 v[16:19],v96 offset:34816\n"
"v_mfma_f32_16x16x32_bf16 v[148:151],v[44:47],a[4:7],v[148:151]\n"
"buffer_load_dwordx4 v112,s[24:27],s76 offen offset:3072 sc1 lds\n"
"v_mfma_f32_16x16x32_bf16 v[148:151],v[40:43],a[0:3],v[148:151]\n"
"s_xor_b32 s76,s76,49152\n"
"ds_read_b128 v[24:27],v96 offset:35840\n"
"v_mfma_f32_16x16x32_bf16 v[152:155],v[48:51],a[0:3],v[152:155]\n"
"ds_read_b64_tr_b16 v[40:41],v105 offset:0\n"
"v_mfma_f32_16x16x32_bf16 v[152:155],v[52:55],a[4:7],v[152:155]\n"
"ds_read_b64_tr_b16 v[42:43],v105 offset:1024\n"
"v_mfma_f32_16x16x32_bf16 v[184:187],v[52:55],a[12:15],v[184:187]\n"
"ds_read_b64_tr_b16 v[44:45],v105 offset:8192\n"
"v_mfma_f32_16x16x32_bf16 v[184:187],v[48:51],a[8:11],v[184:187]\n"
"ds_read_b64_tr_b16 v[46:47],v105 offset:9216\n"
"v_mfma_f32_16x16x32_bf16 v[216:219],v[48:51],a[16:19],v[216:219]\n"
"ds_read_b128 v[4:7],v96 offset:49152\n"
"v_mfma_f32_16x16x32_bf16 v[216:219],v[52:55],a[20:23],v[216:219]\n"
"ds_read_b128 v[12:15],v96 offset:50176\n"
"ds_read_b128 v[20:23],v96 offset:51200\n"
"v_mfma_f32_16x16x32_bf16 v[248:251],v[52:55],a[28:31],v[248:251]\n"
"buffer_store_dwordx2 v[124:125],off,s[24:27],s59 sc1\n"
"v_mfma_f32_16x16x32_bf16 v[248:251],v[48:51],a[24:27],v[248:251]\n"
"s_add_u32 m0,s78,114688\n"
"ds_read_b128 v[28:31],v96 offset:52224\n"
"v_mfma_f32_16x16x32_bf16 v[252:255],v[56:59],a[24:27],v[252:255]\n"
"buffer_load_dwordx4 v106,s[4:7],s13 offen offset:0 nt lds\n"
"v_mfma_f32_16x16x32_bf16 v[252:255],v[60:63],a[28:31],v[252:255]\n"
"s_add_u32 s13,s13,7168\n"
"v_mfma_f32_16x16x32_bf16 v[220:223],v[60:63],a[20:23],v[220:223]\n"
"buffer_load_dwordx4 v106,s[4:7],s13 offen offset:1024 nt lds\n"
"v_mfma_f32_16x16x32_bf16 v[220:223],v[56:59],a[16:19],v[220:223]\n"
"s_add_u32 s13,s13,7168\n"
"v_mfma_f32_16x16x32_bf16 v[188:191],v[56:59],a[8:11],v[188:191]\n"
"buffer_load_dwordx4 v106,s[4:7],s13 offen offset:2048 nt lds\n"
"v_mfma_f32_16x16x32_bf16 v[188:191],v[60:63],a[12:15],v[188:191]\n"
"s_add_u32 s13,s13,7168\n"
"v_mfma_f32_16x16x32_bf16 v[156:159],v[60:63],a[4:7],v[156:159]\n"
"buffer_load_dwordx4 v106,s[4:7],s13 offen offset:3072 nt lds\n"
"v_mfma_f32_16x16x32_bf16 v[156:159],v[56:59],a[0:3],v[156:159]\n"
"s_add_u32 s13,s13,109568\n"
"s_waitcnt vmcnt(25) lgkmcnt(0)\n"
"s_barrier\n"
"v_mfma_f32_16x16x32_bf16 v[128:131],v[64:67],v[0:3],v[128:131]\n"
"ds_read_b64_tr_b16 v[48:49],v98 offset:49152\n"
"ds_read_b64_tr_b16 v[56:57],v99 offset:49152\n"
"v_mfma_f32_16x16x32_bf16 v[128:131],v[68:71],v[4:7],v[128:131]\n"
"ds_read_b64_tr_b16 v[50:51],v98 offset:50176\n"
"ds_read_b64_tr_b16 v[58:59],v99 offset:50176\n"
"v_mfma_f32_16x16x32_bf16 v[160:163],v[68:71],v[12:15],v[160:163]\n"
"ds_read_b64_tr_b16 v[52:53],v98 offset:57344\n"
"ds_read_b64_tr_b16 v[60:61],v99 offset:57344\n"
"v_mfma_f32_16x16x32_bf16 v[160:163],v[64:67],v[8:11],v[160:163]\n"
"s_add_u32 m0,s85,98304\n"
"ds_read_b64_tr_b16 v[54:55],v98 offset:58368\n"
"ds_read_b64_tr_b16 v[62:63],v99 offset:58368\n"
"v_mfma_f32_16x16x32_bf16 v[192:195],v[64:67],v[16:19],v[192:195]\n"
"buffer_load_dwordx4 v110,s[28:31],s86 offen lds\n"
"v_mfma_f32_16x16x32_bf16 v[192:195],v[68:71],v[20:23],v[192:195]\n"
"v_mfma_f32_16x16x32_bf16 v[224:227],v[68:71],v[28:31],v[224:227]\n"
"v_mfma_f32_16x16x32_bf16 v[224:227],v[64:67],v[24:27],v[224:227]\n"
"v_mfma_f32_16x16x32_bf16 v[228:231],v[72:75],v[24:27],v[228:231]\n"
"ds_read_b64_tr_b16 v[64:65],v100 offset:49152\n"
"v_mfma_f32_16x16x32_bf16 v[228:231],v[76:79],v[28:31],v[228:231]\n"
"ds_read_b64_tr_b16 v[66:67],v100 offset:50176\n"
"v_mfma_f32_16x16x32_bf16 v[196:199],v[76:79],v[20:23],v[196:199]\n"
"ds_read_b64_tr_b16 v[68:69],v100 offset:57344\n"
"v_mfma_f32_16x16x32_bf16 v[196:199],v[72:75],v[16:19],v[196:199]\n"
"s_add_u32 m0,s85,99328\n"
"ds_read_b64_tr_b16 v[70:71],v100 offset:58368\n"
"v_mfma_f32_16x16x32_bf16 v[164:167],v[72:75],v[8:11],v[164:167]\n"
"buffer_load_dwordx4 v111,s[28:31],s86 offen lds\n"
"v_mfma_f32_16x16x32_bf16 v[164:167],v[76:79],v[12:15],v[164:167]\n"
"s_add_u32 m0,s85,106496\n"
"s_xor_b32 s86,s86,262144\n"
"v_mfma_f32_16x16x32_bf16 v[132:135],v[76:79],v[4:7],v[132:135]\n"
"buffer_load_dwordx4 v110,s[28:31],s86 offen lds\n"
"v_mfma_f32_16x16x32_bf16 v[132:135],v[72:75],v[0:3],v[132:135]\n"
"v_mfma_f32_16x16x32_bf16 v[136:139],v[80:83],v[0:3],v[136:139]\n"
"ds_read_b64_tr_b16 v[72:73],v101 offset:49152\n"
"v_mfma_f32_16x16x32_bf16 v[136:139],v[84:87],v[4:7],v[136:139]\n"
"ds_read_b64_tr_b16 v[74:75],v101 offset:50176\n"
"v_mfma_f32_16x16x32_bf16 v[168:171],v[84:87],v[12:15],v[168:171]\n"
"ds_read_b64_tr_b16 v[76:77],v101 offset:57344\n"
"v_mfma_f32_16x16x32_bf16 v[168:171],v[80:83],v[8:11],v[168:171]\n"
"s_add_u32 m0,s85,107520\n"
"ds_read_b64_tr_b16 v[78:79],v101 offset:58368\n"
"v_mfma_f32_16x16x32_bf16 v[200:203],v[80:83],v[16:19],v[200:203]\n"
"buffer_load_dwordx4 v111,s[28:31],s86 offen lds\n"
"v_mfma_f32_16x16x32_bf16 v[200:203],v[84:87],v[20:23],v[200:203]\n"
"s_xor_b32 s86,s86,3932160\n"
"v_mfma_f32_16x16x32_bf16 v[232:235],v[84:87],v[28:31],v[232:235]\n"
"v_mfma_f32_16x16x32_bf16 v[232:235],v[80:83],v[24:27],v[232:235]\n"
"v_mfma_f32_16x16x32_bf16 v[236:239],v[88:91],v[24:27],v[236:239]\n"
"ds_read_b64_tr_b16 v[80:81],v102 offset:49152\n"
"v_mfma_f32_16x16x32_bf16 v[236:239],v[92:95],v[28:31],v[236:239]\n"
"ds_read_b64_tr_b16 v[82:83],v102 offset:50176\n"
"v_mfma_f32_16x16x32_bf16 v[204:207],v[92:95],v[20:23],v[204:207]\n"
"ds_read_b64_tr_b16 v[84:85],v102 offset:57344\n"
"v_mfma_f32_16x16x32_bf16 v[204:207],v[88:91],v[16:19],v[204:207]\n"
"s_add_u32 m0,s78,32768\n"
"ds_read_b64_tr_b16 v[86:87],v102 offset:58368\n"
"v_mfma_f32_16x16x32_bf16 v[172:175],v[88:91],v[8:11],v[172:175]\n"
"buffer_load_dwordx4 v112,s[24:27],s76 offen offset:0 sc1 lds\n"
"v_mfma_f32_16x16x32_bf16 v[172:175],v[92:95],v[12:15],v[172:175]\n"
"v_mfma_f32_16x16x32_bf16 v[140:143],v[92:95],v[4:7],v[140:143]\n"
"buffer_load_dwordx4 v112,s[24:27],s76 offen offset:1024 sc1 lds\n"
"v_mfma_f32_16x16x32_bf16 v[140:143],v[88:91],v[0:3],v[140:143]\n"
"s_waitcnt vmcnt(23)\n"
"ds_read_b64_tr_b16 v[88:89],v103 offset:49152\n"
"ds_read_b64_tr_b16 v[90:91],v103 offset:50176\n"
"v_mfma_f32_16x16x32_bf16 v[144:147],a[32:35],v[0:3],v[144:147]\n"
"v_lshrrev_b32 v120,4,v112\n"
"v_and_b32 v121,15,v120\n"
"v_mfma_f32_16x16x32_bf16 v[144:147],a[36:39],v[4:7],v[144:147]\n"
"v_lshrrev_b32 v122,4,v120\n"
"v_lshlrev_b32 v122,2,v122\n"
"v_mfma_f32_16x16x32_bf16 v[176:179],a[36:39],v[12:15],v[176:179]\n"
"ds_read_b64_tr_b16 v[92:93],v103 offset:57344\n"
"v_sub_u32 v121,v121,v122\n"
"v_mfma_f32_16x16x32_bf16 v[176:179],a[32:35],v[8:11],v[176:179]\n"
"s_and_b32 s82,s52,1\n"
"ds_read_b64_tr_b16 v[94:95],v103 offset:58368\n"
"s_lshl_b32 s82,s82,4\n"
"v_add_u32 v121,s82,v121\n"
"v_mfma_f32_16x16x32_bf16 v[208:211],a[32:35],v[16:19],v[208:211]\n"
"buffer_load_dwordx4 v112,s[24:27],s76 offen offset:2048 sc1 lds\n"
"v_cvt_f32_i32 v121,v121\n"
"v_mfma_f32_16x16x32_bf16 v[208:211],a[36:39],v[20:23],v[208:211]\n"
"ds_read_b128 a[96:99],v97 offset:0\n"
"v_add_f32 v121,0.5,v121\n"
"v_mul_f32 v126,0x7149f2ca,v121\n"
"v_mfma_f32_16x16x32_bf16 v[240:243],a[36:39],v[28:31],v[240:243]\n"
"v_and_b32 v120,31,v120\n"
"v_mov_b32 v121,0x3f803f80\n"
"v_mfma_f32_16x16x32_bf16 v[240:243],a[32:35],v[24:27],v[240:243]\n"
"s_mov_b32 s93,0x3f800000\n"
"ds_read_b128 a[104:107],v97 offset:1024\n"
"v_accvgpr_write_b32 a144,v121\n"
"v_mfma_f32_16x16x32_bf16 v[244:247],a[40:43],v[24:27],v[244:247]\n"
"ds_read_b64_tr_b16 a[128:129],v104 offset:49152\n"
"v_accvgpr_write_b32 a146,v121\n"
"v_mfma_f32_16x16x32_bf16 v[244:247],a[44:47],v[28:31],v[244:247]\n"
"ds_read_b64_tr_b16 a[130:131],v104 offset:50176\n"
"v_accvgpr_write_b32 a147,v121\n"
"v_mfma_f32_16x16x32_bf16 v[212:215],a[44:47],v[20:23],v[212:215]\n"
"ds_read_b64_tr_b16 a[132:133],v104 offset:57344\n"
"v_lshlrev_b32 v120,3,v120\n"
"v_mfma_f32_16x16x32_bf16 v[212:215],a[40:43],v[16:19],v[212:215]\n"
"ds_read_b64_tr_b16 a[134:135],v104 offset:58368\n"
"v_accvgpr_write_b32 a145,v121\n"
"v_mfma_f32_16x16x32_bf16 v[180:183],a[40:43],v[8:11],v[180:183]\n"
"buffer_load_dwordx4 v112,s[24:27],s76 offen offset:3072 sc1 lds\n"
"v_mfma_f32_16x16x32_bf16 v[180:183],a[44:47],v[12:15],v[180:183]\n"
"s_add_u32 m0,s78,49152\n"
"s_xor_b32 s76,s76,16384\n"
"ds_read_b128 a[112:115],v97 offset:2048\n"
"v_mfma_f32_16x16x32_bf16 v[148:151],a[44:47],v[4:7],v[148:151]\n"
"buffer_load_dwordx4 v112,s[24:27],s76 offen offset:0 sc1 lds\n"
"v_mfma_f32_16x16x32_bf16 v[148:151],a[40:43],v[0:3],v[148:151]\n"
"ds_read_b128 a[120:123],v97 offset:3072\n"
"v_mfma_f32_16x16x32_bf16 v[152:155],v[32:35],v[0:3],v[152:155]\n"
"ds_read_b64_tr_b16 a[136:137],v105 offset:49152\n"
"v_mfma_f32_16x16x32_bf16 v[152:155],v[36:39],v[4:7],v[152:155]\n"
"ds_read_b64_tr_b16 a[138:139],v105 offset:50176\n"
"v_mfma_f32_16x16x32_bf16 v[184:187],v[36:39],v[12:15],v[184:187]\n"
"ds_read_b64_tr_b16 a[140:141],v105 offset:57344\n"
"v_mfma_f32_16x16x32_bf16 v[184:187],v[32:35],v[8:11],v[184:187]\n"
"ds_read_b64_tr_b16 a[142:143],v105 offset:58368\n"
"v_mfma_f32_16x16x32_bf16 v[216:219],v[32:35],v[16:19],v[216:219]\n"
"ds_read_b128 a[100:103],v97 offset:16384\n"
"v_mfma_f32_16x16x32_bf16 v[216:219],v[36:39],v[20:23],v[216:219]\n"
"ds_read_b128 a[108:111],v97 offset:17408\n"
"v_mfma_f32_16x16x32_bf16 v[248:251],v[36:39],v[28:31],v[248:251]\n"
"ds_read_b128 a[116:119],v97 offset:18432\n"
"v_mfma_f32_16x16x32_bf16 v[248:251],v[32:35],v[24:27],v[248:251]\n"
"ds_read_b128 a[124:127],v97 offset:19456\n"
"v_mfma_f32_16x16x32_bf16 v[252:255],v[40:43],v[24:27],v[252:255]\n"
"buffer_load_dwordx4 v112,s[24:27],s76 offen offset:1024 sc1 lds\n"
"v_mfma_f32_16x16x32_bf16 v[252:255],v[44:47],v[28:31],v[252:255]\n"
"v_mfma_f32_16x16x32_bf16 v[220:223],v[44:47],v[20:23],v[220:223]\n"
"v_mfma_f32_16x16x32_bf16 v[220:223],v[40:43],v[16:19],v[220:223]\n"
"buffer_load_dwordx4 v112,s[24:27],s76 offen offset:2048 sc1 lds\n"
"v_mfma_f32_16x16x32_bf16 v[188:191],v[40:43],v[8:11],v[188:191]\n"
"v_mfma_f32_16x16x32_bf16 v[188:191],v[44:47],v[12:15],v[188:191]\n"
"v_mfma_f32_16x16x32_bf16 v[156:159],v[44:47],v[4:7],v[156:159]\n"
"buffer_load_dwordx4 v112,s[24:27],s76 offen offset:3072 sc1 lds\n"
"v_mfma_f32_16x16x32_bf16 v[156:159],v[40:43],v[0:3],v[156:159]\n"
"s_xor_b32 s76,s76,245760\n"
"s_waitcnt vmcnt(12) lgkmcnt(0)\n"
"s_barrier\n"
"v_mfma_f32_16x16x32_bf16 v[128:131],v[48:51],a[96:99],v[128:131]\n"
"ds_read_b64_tr_b16 v[32:33],v98 offset:32768\n"
"ds_read_b64_tr_b16 v[40:41],v99 offset:32768\n"
"v_mfma_f32_16x16x32_bf16 v[128:131],v[52:55],a[100:103],v[128:131]\n"
"ds_read_b64_tr_b16 v[34:35],v98 offset:33792\n"
"ds_read_b64_tr_b16 v[42:43],v99 offset:33792\n"
"v_mfma_f32_16x16x32_bf16 v[160:163],v[52:55],a[108:111],v[160:163]\n"
"ds_read_b64_tr_b16 v[36:37],v98 offset:40960\n"
"ds_read_b64_tr_b16 v[44:45],v99 offset:40960\n"
"v_mfma_f32_16x16x32_bf16 v[160:163],v[48:51],a[104:107],v[160:163]\n"
"s_add_u32 m0,s85,147456\n"
"ds_read_b64_tr_b16 v[38:39],v98 offset:41984\n"
"ds_read_b64_tr_b16 v[46:47],v99 offset:41984\n"
"v_mfma_f32_16x16x32_bf16 v[192:195],v[48:51],a[112:115],v[192:195]\n"
"buffer_load_dwordx4 v110,s[28:31],s86 offen lds\n"
"ds_read_b64_tr_b16 a[160:161],v107 offset:0\n"
"v_mfma_f32_16x16x32_bf16 v[192:195],v[52:55],a[116:119],v[192:195]\n"
"ds_read_b64_tr_b16 a[162:163],v107 offset:4096\n"
"ds_read_b64_tr_b16 a[164:165],v107 offset:8\n"
"ds_read_b64_tr_b16 a[166:167],v107 offset:4104\n"
"v_mfma_f32_16x16x32_bf16 v[224:227],v[52:55],a[124:127],v[224:227]\n"
"ds_read_b64_tr_b16 a[168:169],v107 offset:8192\n"
"ds_read_b64_tr_b16 a[170:171],v107 offset:12288\n"
"v_mfma_f32_16x16x32_bf16 v[224:227],v[48:51],a[120:123],v[224:227]\n"
"ds_read_b64_tr_b16 a[172:173],v107 offset:8200\n"
"ds_read_b64_tr_b16 a[174:175],v107 offset:12296\n"
"v_mfma_f32_16x16x32_bf16 v[228:231],v[56:59],a[120:123],v[228:231]\n"
"ds_read_b64_tr_b16 v[48:49],v100 offset:32768\n"
"v_mfma_f32_16x16x32_bf16 v[228:231],v[60:63],a[124:127],v[228:231]\n"
"ds_read_b64_tr_b16 v[50:51],v100 offset:33792\n"
"v_mfma_f32_16x16x32_bf16 v[196:199],v[60:63],a[116:119],v[196:199]\n"
"ds_read_b64_tr_b16 v[52:53],v100 offset:40960\n"
"v_mfma_f32_16x16x32_bf16 v[196:199],v[56:59],a[112:115],v[196:199]\n"
"s_add_u32 m0,s85,148480\n"
"ds_read_b64_tr_b16 v[54:55],v100 offset:41984\n"
"v_mfma_f32_16x16x32_bf16 v[164:167],v[56:59],a[104:107],v[164:167]\n"
"buffer_load_dwordx4 v111,s[28:31],s86 offen lds\n"
"v_mfma_f32_16x16x32_bf16 v[164:167],v[60:63],a[108:111],v[164:167]\n"
"s_add_u32 m0,s85,155648\n"
"s_xor_b32 s86,s86,262144\n"
"v_mfma_f32_16x16x32_bf16 v[132:135],v[60:63],a[100:103],v[132:135]\n"
"buffer_load_dwordx4 v110,s[28:31],s86 offen lds\n"
"v_mfma_f32_16x16x32_bf16 v[132:135],v[56:59],a[96:99],v[132:135]\n"
"v_mfma_f32_16x16x32_bf16 v[136:139],v[64:67],a[96:99],v[136:139]\n"
"ds_read_b64_tr_b16 v[56:57],v101 offset:32768\n"
"v_mfma_f32_16x16x32_bf16 v[136:139],v[68:71],a[100:103],v[136:139]\n"
"ds_read_b64_tr_b16 v[58:59],v101 offset:33792\n"
"v_mfma_f32_16x16x32_bf16 v[168:171],v[68:71],a[108:111],v[168:171]\n"
"ds_read_b64_tr_b16 v[60:61],v101 offset:40960\n"
"v_mfma_f32_16x16x32_bf16 v[168:171],v[64:67],a[104:107],v[168:171]\n"
"s_add_u32 m0,s85,156672\n"
"ds_read_b64_tr_b16 v[62:63],v101 offset:41984\n"
"v_mfma_f32_16x16x32_bf16 v[200:203],v[64:67],a[112:115],v[200:203]\n"
"buffer_load_dwordx4 v111,s[28:31],s86 offen lds\n"
"v_mfma_f32_16x16x32_bf16 v[200:203],v[68:71],a[116:119],v[200:203]\n"
"s_xor_b32 s86,s86,786432\n"
"v_mfma_f32_16x16x32_bf16 v[232:235],v[68:71],a[124:127],v[232:235]\n"
"v_mfma_f32_16x16x32_bf16 v[232:235],v[64:67],a[120:123],v[232:235]\n"
"v_mfma_f32_16x16x32_bf16 v[236:239],v[72:75],a[120:123],v[236:239]\n"
"ds_read_b64_tr_b16 v[64:65],v102 offset:32768\n"
"v_mfma_f32_16x16x32_bf16 v[236:239],v[76:79],a[124:127],v[236:239]\n"
"ds_read_b64_tr_b16 v[66:67],v102 offset:33792\n"
"v_mfma_f32_16x16x32_bf16 v[204:207],v[76:79],a[116:119],v[204:207]\n"
"ds_read_b64_tr_b16 v[68:69],v102 offset:40960\n"
"v_mfma_f32_16x16x32_bf16 v[204:207],v[72:75],a[112:115],v[204:207]\n"
"s_add_u32 m0,s78,65536\n"
"ds_read_b64_tr_b16 v[70:71],v102 offset:41984\n"
"v_mfma_f32_16x16x32_bf16 v[172:175],v[72:75],a[104:107],v[172:175]\n"
"buffer_load_dwordx4 v112,s[24:27],s76 offen offset:0 sc1 lds\n"
"v_mfma_f32_16x16x32_bf16 v[172:175],v[76:79],a[108:111],v[172:175]\n"
"v_mfma_f32_16x16x32_bf16 v[140:143],v[76:79],a[100:103],v[140:143]\n"
"buffer_load_dwordx4 v112,s[24:27],s76 offen offset:1024 sc1 lds\n"
"v_mfma_f32_16x16x32_bf16 v[140:143],v[72:75],a[96:99],v[140:143]\n"
"v_mfma_f32_16x16x32_bf16 v[144:147],v[80:83],a[96:99],v[144:147]\n"
"ds_read_b64_tr_b16 v[72:73],v103 offset:32768\n"
"v_accvgpr_write_b32 a156,0\n"
"v_mfma_f32_16x16x32_bf16 v[144:147],v[84:87],a[100:103],v[144:147]\n"
"ds_read_b64_tr_b16 v[74:75],v103 offset:33792\n"
"v_accvgpr_write_b32 a157,0\n"
"v_mfma_f32_16x16x32_bf16 v[176:179],v[84:87],a[108:111],v[176:179]\n"
"ds_read_b64_tr_b16 v[76:77],v103 offset:40960\n"
"v_accvgpr_write_b32 a158,0\n"
"v_mfma_f32_16x16x32_bf16 v[176:179],v[80:83],a[104:107],v[176:179]\n"
"ds_read_b64_tr_b16 v[78:79],v103 offset:41984\n"
"v_accvgpr_write_b32 a159,0\n"
"v_mfma_f32_16x16x32_bf16 v[208:211],v[80:83],a[112:115],v[208:211]\n"
"buffer_load_dwordx4 v112,s[24:27],s76 offen offset:2048 sc1 lds\n"
"v_mfma_f32_16x16x32_bf16 v[208:211],v[84:87],a[116:119],v[208:211]\n"
"ds_read_b128 v[0:3],v96 offset:0\n"
"v_mfma_f32_16x16x32_bf16 v[240:243],v[84:87],a[124:127],v[240:243]\n"
"v_mfma_f32_16x16x32_bf16 v[240:243],v[80:83],a[120:123],v[240:243]\n"
"ds_read_b128 v[8:11],v96 offset:1024\n"
"v_mfma_f32_16x16x32_bf16 v[244:247],v[88:91],a[120:123],v[244:247]\n"
"ds_read_b64_tr_b16 v[80:81],v104 offset:32768\n"
"v_mfma_f32_16x16x32_bf16 v[244:247],v[92:95],a[124:127],v[244:247]\n"
"ds_read_b64_tr_b16 v[82:83],v104 offset:33792\n"
"v_mfma_f32_16x16x32_bf16 v[212:215],v[92:95],a[116:119],v[212:215]\n"
"ds_read_b64_tr_b16 v[84:85],v104 offset:40960\n"
"v_mfma_f32_16x16x32_bf16 v[212:215],v[88:91],a[112:115],v[212:215]\n"
"ds_read_b64_tr_b16 v[86:87],v104 offset:41984\n"
"v_mfma_f32_16x16x32_bf16 v[180:183],v[88:91],a[104:107],v[180:183]\n"
"buffer_load_dwordx4 v112,s[24:27],s76 offen offset:3072 sc1 lds\n"
"v_mfma_f32_16x16x32_bf16 v[180:183],v[92:95],a[108:111],v[180:183]\n"
"s_add_u32 m0,s78,81920\n"
"s_xor_b32 s76,s76,16384\n"
"ds_read_b128 v[16:19],v96 offset:2048\n"
"v_mfma_f32_16x16x32_bf16 v[148:151],v[92:95],a[100:103],v[148:151]\n"
"buffer_load_dwordx4 v112,s[24:27],s76 offen offset:0 sc1 lds\n"
"v_mfma_f32_16x16x32_bf16 v[148:151],v[88:91],a[96:99],v[148:151]\n"
"ds_read_b128 v[24:27],v96 offset:3072\n"
"v_mfma_f32_16x16x32_bf16 v[152:155],a[128:131],a[96:99],v[152:155]\n"
"ds_read_b64_tr_b16 v[88:89],v105 offset:32768\n"
"v_mfma_f32_16x16x32_bf16 v[152:155],a[132:135],a[100:103],v[152:155]\n"
"ds_read_b64_tr_b16 v[90:91],v105 offset:33792\n"
"v_mfma_f32_16x16x32_bf16 v[184:187],a[132:135],a[108:111],v[184:187]\n"
"ds_read_b64_tr_b16 v[92:93],v105 offset:40960\n"
"v_mfma_f32_16x16x32_bf16 v[184:187],a[128:131],a[104:107],v[184:187]\n"
"ds_read_b64_tr_b16 v[94:95],v105 offset:41984\n"
"v_mfma_f32_16x16x32_bf16 v[216:219],a[128:131],a[112:115],v[216:219]\n"
"ds_read_b128 v[4:7],v96 offset:16384\n"
"v_mfma_f32_16x16x32_bf16 v[216:219],a[132:135],a[116:119],v[216:219]\n"
"ds_read_b128 v[12:15],v96 offset:17408\n"
"v_mfma_f32_16x16x32_bf16 v[248:251],a[132:135],a[124:127],v[248:251]\n"
"ds_read_b128 v[20:23],v96 offset:18432\n"
"v_mfma_f32_16x16x32_bf16 v[248:251],a[128:131],a[120:123],v[248:251]\n"
"ds_read_b128 v[28:31],v96 offset:19456\n"
"v_mfma_f32_16x16x32_bf16 v[252:255],a[136:139],a[120:123],v[252:255]\n"
"buffer_load_dwordx4 v112,s[24:27],s76 offen offset:1024 sc1 lds\n"
"v_mfma_f32_16x16x32_bf16 v[252:255],a[140:143],a[124:127],v[252:255]\n"
"v_mfma_f32_16x16x32_bf16 v[220:223],a[140:143],a[116:119],v[220:223]\n"
"v_mfma_f32_16x16x32_bf16 v[220:223],a[136:139],a[112:115],v[220:223]\n"
"buffer_load_dwordx4 v112,s[24:27],s76 offen offset:2048 sc1 lds\n"
"v_mfma_f32_16x16x32_bf16 v[188:191],a[136:139],a[104:107],v[188:191]\n"
"v_mfma_f32_16x16x32_bf16 v[188:191],a[140:143],a[108:111],v[188:191]\n"
"v_mfma_f32_16x16x32_bf16 v[156:159],a[140:143],a[100:103],v[156:159]\n"
"buffer_load_dwordx4 v112,s[24:27],s76 offen offset:3072 sc1 lds\n"
"v_mfma_f32_16x16x32_bf16 v[156:159],a[136:139],a[96:99],v[156:159]\n"
"s_xor_b32 s76,s76,49152\n"
"s_waitcnt vmcnt(20) lgkmcnt(0)\n"
"s_barrier\n"
"v_mfma_f32_16x16x32_bf16 v[128:131],v[32:35],v[0:3],v[128:131]\n"
"ds_read_b64_tr_b16 a[128:129],v98 offset:0\n"
"ds_read_b64_tr_b16 a[136:137],v99 offset:0\n"
"v_mfma_f32_16x16x32_bf16 v[128:131],v[36:39],v[4:7],v[128:131]\n"
"ds_read_b64_tr_b16 a[130:131],v98 offset:1024\n"
"ds_read_b64_tr_b16 a[138:139],v99 offset:1024\n"
"v_mfma_f32_16x16x32_bf16 v[160:163],v[36:39],v[12:15],v[160:163]\n"
"ds_read_b64_tr_b16 a[132:133],v98 offset:8192\n"
"ds_read_b64_tr_b16 a[140:141],v99 offset:8192\n"
"v_mfma_f32_16x16x32_bf16 v[160:163],v[32:35],v[8:11],v[160:163]\n"
"s_add_u32 m0,s85,131072\n"
"ds_read_b64_tr_b16 a[134:135],v98 offset:9216\n"
"ds_read_b64_tr_b16 a[142:143],v99 offset:9216\n"
"v_mfma_f32_16x16x32_bf16 v[192:195],v[32:35],v[16:19],v[192:195]\n"
"buffer_load_dwordx4 v110,s[28:31],s86 offen lds\n"
"v_mfma_f32_16x16x32_bf16 v[192:195],v[36:39],v[20:23],v[192:195]\n"
"s_add_u32 m0,s85,132096\n"
"v_mfma_f32_16x16x32_bf16 v[224:227],v[36:39],v[28:31],v[224:227]\n"
"buffer_load_dwordx4 v111,s[28:31],s86 offen lds\n"
"v_mfma_f32_16x16x32_bf16 v[224:227],v[32:35],v[24:27],v[224:227]\n"
"s_xor_b32 s86,s86,262144\n"
"v_mfma_f32_16x16x32_bf16 v[228:231],v[40:43],v[24:27],v[228:231]\n"
"ds_read_b64_tr_b16 v[32:33],v100 offset:0\n"
"v_mfma_f32_16x16x32_bf16 v[228:231],v[44:47],v[28:31],v[228:231]\n"
"ds_read_b64_tr_b16 v[34:35],v100 offset:1024\n"
"v_mfma_f32_16x16x32_bf16 v[196:199],v[44:47],v[20:23],v[196:199]\n"
"ds_read_b64_tr_b16 v[36:37],v100 offset:8192\n"
"v_mfma_f32_16x16x32_bf16 v[196:199],v[40:43],v[16:19],v[196:199]\n"
"s_add_u32 m0,s85,139264\n"
"ds_read_b64_tr_b16 v[38:39],v100 offset:9216\n"
"v_mfma_f32_16x16x32_bf16 v[164:167],v[40:43],v[8:11],v[164:167]\n"
"buffer_load_dwordx4 v110,s[28:31],s86 offen lds\n"
"v_mfma_f32_16x16x32_bf16 v[164:167],v[44:47],v[12:15],v[164:167]\n"
"s_add_u32 m0,s85,140288\n"
"v_mfma_f32_16x16x32_bf16 v[132:135],v[44:47],v[4:7],v[132:135]\n"
"buffer_load_dwordx4 v111,s[28:31],s86 offen lds\n"
"v_mfma_f32_16x16x32_bf16 v[132:135],v[40:43],v[0:3],v[132:135]\n"
"s_xor_b32 s86,s86,1835008\n"
"v_mfma_f32_16x16x32_bf16 v[136:139],v[48:51],v[0:3],v[136:139]\n"
"ds_read_b64_tr_b16 v[40:41],v101 offset:0\n"
"v_mfma_f32_16x16x32_bf16 v[136:139],v[52:55],v[4:7],v[136:139]\n"
"ds_read_b64_tr_b16 v[42:43],v101 offset:1024\n"
"v_mfma_f32_16x16x32_bf16 v[168:171],v[52:55],v[12:15],v[168:171]\n"
"ds_read_b64_tr_b16 v[44:45],v101 offset:8192\n"
"v_mfma_f32_16x16x32_bf16 v[168:171],v[48:51],v[8:11],v[168:171]\n"
"s_add_u32 m0,s78,0\n"
"ds_read_b64_tr_b16 v[46:47],v101 offset:9216\n"
"v_mfma_f32_16x16x32_bf16 v[200:203],v[48:51],v[16:19],v[200:203]\n"
"buffer_load_dwordx4 v112,s[24:27],s76 offen offset:0 sc1 lds\n"
"v_mfma_f32_16x16x32_bf16 v[200:203],v[52:55],v[20:23],v[200:203]\n"
"v_mfma_f32_16x16x32_bf16 v[232:235],v[52:55],v[28:31],v[232:235]\n"
"buffer_load_dwordx4 v112,s[24:27],s76 offen offset:1024 sc1 lds\n"
"v_mfma_f32_16x16x32_bf16 v[232:235],v[48:51],v[24:27],v[232:235]\n"
"v_mfma_f32_16x16x32_bf16 v[236:239],v[56:59],v[24:27],v[236:239]\n"
"ds_read_b64_tr_b16 v[48:49],v102 offset:0\n"
"v_mfma_f32_16x16x32_bf16 v[236:239],v[60:63],v[28:31],v[236:239]\n"
"ds_read_b64_tr_b16 v[50:51],v102 offset:1024\n"
"v_mfma_f32_16x16x32_bf16 v[204:207],v[60:63],v[20:23],v[204:207]\n"
"ds_read_b64_tr_b16 v[52:53],v102 offset:8192\n"
"v_mfma_f32_16x16x32_bf16 v[204:207],v[56:59],v[16:19],v[204:207]\n"
"ds_read_b64_tr_b16 v[54:55],v102 offset:9216\n"
"v_mfma_f32_16x16x32_bf16 v[172:175],v[56:59],v[8:11],v[172:175]\n"
"buffer_load_dwordx4 v112,s[24:27],s76 offen offset:2048 sc1 lds\n"
"v_mfma_f32_16x16x32_bf16 v[172:175],v[60:63],v[12:15],v[172:175]\n"
"v_mfma_f32_16x16x32_bf16 v[140:143],v[60:63],v[4:7],v[140:143]\n"
"buffer_load_dwordx4 v112,s[24:27],s76 offen offset:3072 sc1 lds\n"
"v_mfma_f32_16x16x32_bf16 v[140:143],v[56:59],v[0:3],v[140:143]\n"
"s_xor_b32 s76,s76,16384\n"
"s_waitcnt vmcnt(20)\n"
"v_mfma_f32_16x16x32_bf16 v[144:147],v[64:67],v[0:3],v[144:147]\n"
"ds_read_b64_tr_b16 v[56:57],v103 offset:0\n"
"v_mfma_f32_16x16x32_bf16 v[144:147],v[68:71],v[4:7],v[144:147]\n"
"ds_read_b64_tr_b16 v[58:59],v103 offset:1024\n"
"v_mfma_f32_16x16x32_bf16 v[176:179],v[68:71],v[12:15],v[176:179]\n"
"ds_read_b64_tr_b16 v[60:61],v103 offset:8192\n"
"v_mfma_f32_16x16x32_bf16 v[176:179],v[64:67],v[8:11],v[176:179]\n"
"s_add_u32 m0,s78,16384\n"
"ds_read_b64_tr_b16 v[62:63],v103 offset:9216\n"
"v_mfma_f32_16x16x32_bf16 v[208:211],v[64:67],v[16:19],v[208:211]\n"
"buffer_load_dwordx4 v112,s[24:27],s76 offen offset:0 sc1 lds\n"
"v_mfma_f32_16x16x32_bf16 v[208:211],v[68:71],v[20:23],v[208:211]\n"
"ds_read_b128 a[96:99],v96 offset:32768\n"
"v_mfma_f32_16x16x32_bf16 v[240:243],v[68:71],v[28:31],v[240:243]\n"
"buffer_load_dwordx4 v112,s[24:27],s76 offen offset:1024 sc1 lds\n"
"v_mfma_f32_16x16x32_bf16 v[240:243],v[64:67],v[24:27],v[240:243]\n"
"ds_read_b128 a[104:107],v96 offset:33792\n"
"v_mfma_f32_16x16x32_bf16 v[244:247],v[72:75],v[24:27],v[244:247]\n"
"ds_read_b64_tr_b16 v[64:65],v104 offset:0\n"
"v_mfma_f32_16x16x32_bf16 v[244:247],v[76:79],v[28:31],v[244:247]\n"
"ds_read_b64_tr_b16 v[66:67],v104 offset:1024\n"
"v_mfma_f32_16x16x32_bf16 v[212:215],v[76:79],v[20:23],v[212:215]\n"
"ds_read_b64_tr_b16 v[68:69],v104 offset:8192\n"
"v_mfma_f32_16x16x32_bf16 v[212:215],v[72:75],v[16:19],v[212:215]\n"
"ds_read_b64_tr_b16 v[70:71],v104 offset:9216\n"
"v_mfma_f32_16x16x32_bf16 v[180:183],v[72:75],v[8:11],v[180:183]\n"
"buffer_load_dwordx4 v112,s[24:27],s76 offen offset:2048 sc1 lds\n"
"v_mfma_f32_16x16x32_bf16 v[180:183],v[76:79],v[12:15],v[180:183]\n"
"ds_read_b128 a[112:115],v96 offset:34816\n"
"v_mfma_f32_16x16x32_bf16 v[148:151],v[76:79],v[4:7],v[148:151]\n"
"buffer_load_dwordx4 v112,s[24:27],s76 offen offset:3072 sc1 lds\n"
"v_mfma_f32_16x16x32_bf16 v[148:151],v[72:75],v[0:3],v[148:151]\n"
"s_xor_b32 s76,s76,114688\n"
"ds_read_b128 a[120:123],v96 offset:35840\n"
"v_mfma_f32_16x16x32_bf16 v[152:155],v[80:83],v[0:3],v[152:155]\n"
"ds_read_b64_tr_b16 v[72:73],v105 offset:0\n"
"v_mfma_f32_16x16x32_bf16 v[152:155],v[84:87],v[4:7],v[152:155]\n"
"ds_read_b64_tr_b16 v[74:75],v105 offset:1024\n"
"v_mfma_f32_16x16x32_bf16 v[184:187],v[84:87],v[12:15],v[184:187]\n"
"ds_read_b64_tr_b16 v[76:77],v105 offset:8192\n"
"v_mfma_f32_16x16x32_bf16 v[184:187],v[80:83],v[8:11],v[184:187]\n"
"ds_read_b64_tr_b16 v[78:79],v105 offset:9216\n"
"v_mfma_f32_16x16x32_bf16 v[216:219],v[80:83],v[16:19],v[216:219]\n"
"ds_read_b128 a[100:103],v96 offset:49152\n"
"v_mfma_f32_16x16x32_bf16 v[216:219],v[84:87],v[20:23],v[216:219]\n"
"ds_read_b128 a[108:111],v96 offset:50176\n"
"ds_read_b128 a[116:119],v96 offset:51200\n"
"v_mfma_f32_16x16x32_bf16 v[248:251],v[84:87],v[28:31],v[248:251]\n"
"s_load_dwordx2 s[32:33],s[0:1],56\n"
"s_load_dwordx2 s[36:37],s[0:1],64\n"
"s_load_dwordx2 s[40:41],s[0:1],72\n"
"v_mfma_f32_16x16x32_bf16 v[248:251],v[80:83],v[24:27],v[248:251]\n"
"s_add_u32 m0,s78,114688\n"
"ds_read_b128 a[124:127],v96 offset:52224\n"
"v_mfma_f32_16x16x32_bf16 v[252:255],v[88:91],v[24:27],v[252:255]\n"
"buffer_load_dwordx4 v106,s[4:7],s13 offen offset:0 nt lds\n"
"v_mfma_f32_16x16x32_bf16 v[252:255],v[92:95],v[28:31],v[252:255]\n"
"s_add_u32 s13,s13,7168\n"
"v_mfma_f32_16x16x32_bf16 v[220:223],v[92:95],v[20:23],v[220:223]\n"
"buffer_load_dwordx4 v106,s[4:7],s13 offen offset:1024 nt lds\n"
"v_mfma_f32_16x16x32_bf16 v[220:223],v[88:91],v[16:19],v[220:223]\n"
"s_add_u32 s13,s13,7168\n"
"v_mfma_f32_16x16x32_bf16 v[188:191],v[88:91],v[8:11],v[188:191]\n"
"buffer_load_dwordx4 v106,s[4:7],s13 offen offset:2048 nt lds\n"
"v_mfma_f32_16x16x32_bf16 v[188:191],v[92:95],v[12:15],v[188:191]\n"
"s_add_u32 s13,s13,7168\n"
"v_mfma_f32_16x16x32_bf16 v[156:159],v[92:95],v[4:7],v[156:159]\n"
"buffer_load_dwordx4 v106,s[4:7],s13 offen offset:3072 nt lds\n"
"v_mfma_f32_16x16x32_bf16 v[156:159],v[88:91],v[0:3],v[156:159]\n"
"s_add_u32 s13,s13,109568\n"
"s_waitcnt vmcnt(24) lgkmcnt(0)\n"
"s_barrier\n"
"v_mfma_f32_16x16x32_bf16 v[128:131],a[128:131],a[96:99],v[128:131]\n"
"ds_read_b64_tr_b16 v[80:81],v98 offset:49152\n"
"ds_read_b64_tr_b16 v[88:89],v99 offset:49152\n"
"v_mfma_f32_16x16x32_bf16 v[128:131],a[132:135],a[100:103],v[128:131]\n"
"ds_read_b64_tr_b16 v[82:83],v98 offset:50176\n"
"ds_read_b64_tr_b16 v[90:91],v99 offset:50176\n"
"v_mfma_f32_16x16x32_bf16 v[160:163],a[132:135],a[108:111],v[160:163]\n"
"ds_read_b64_tr_b16 v[84:85],v98 offset:57344\n"
"ds_read_b64_tr_b16 v[92:93],v99 offset:57344\n"
"v_mfma_f32_16x16x32_bf16 v[160:163],a[128:131],a[104:107],v[160:163]\n"
"s_add_u32 m0,s85,98304\n"
"ds_read_b64_tr_b16 v[86:87],v98 offset:58368\n"
"ds_read_b64_tr_b16 v[94:95],v99 offset:58368\n"
"v_mfma_f32_16x16x32_bf16 v[192:195],a[128:131],a[112:115],v[192:195]\n"
"buffer_load_dwordx4 v110,s[28:31],s86 offen lds\n"
"v_mfma_f32_16x16x32_bf16 v[192:195],a[132:135],a[116:119],v[192:195]\n"
"v_accvgpr_write_b32 a154,v124\n"
"v_accvgpr_write_b32 a155,v125\n"
"v_mfma_f32_16x16x32_bf16 v[224:227],a[132:135],a[124:127],v[224:227]\n"
"v_accvgpr_write_b32 a152,1\n"
"v_lshrrev_b32 v124,2,v112\n"
"v_mfma_f32_16x16x32_bf16 v[224:227],a[128:131],a[120:123],v[224:227]\n"
"v_add_u32 v124,147456,v124\n"
"v_mov_b32 v125,0\n"
"v_mfma_f32_16x16x32_bf16 v[228:231],a[136:139],a[120:123],v[228:231]\n"
"ds_read_b64_tr_b16 a[128:129],v100 offset:49152\n"
"s_mov_b32 s101,0\n"
"v_mfma_f32_16x16x32_bf16 v[228:231],a[140:143],a[124:127],v[228:231]\n"
"ds_read_b64_tr_b16 a[130:131],v100 offset:50176\n"
"v_mfma_f32_16x16x32_bf16 v[196:199],a[140:143],a[116:119],v[196:199]\n"
"ds_read_b64_tr_b16 a[132:133],v100 offset:57344\n"
"v_mfma_f32_16x16x32_bf16 v[196:199],a[136:139],a[112:115],v[196:199]\n"
"s_add_u32 m0,s85,99328\n"
"ds_read_b64_tr_b16 a[134:135],v100 offset:58368\n"
"v_mfma_f32_16x16x32_bf16 v[164:167],a[136:139],a[104:107],v[164:167]\n"
"buffer_load_dwordx4 v111,s[28:31],s86 offen lds\n"
"v_mfma_f32_16x16x32_bf16 v[164:167],a[140:143],a[108:111],v[164:167]\n"
"s_add_u32 m0,s85,106496\n"
"s_xor_b32 s86,s86,262144\n"
"v_mfma_f32_16x16x32_bf16 v[132:135],a[140:143],a[100:103],v[132:135]\n"
"buffer_load_dwordx4 v110,s[28:31],s86 offen lds\n"
"v_mfma_f32_16x16x32_bf16 v[132:135],a[136:139],a[96:99],v[132:135]\n"
"v_mfma_f32_16x16x32_bf16 v[136:139],v[32:35],a[96:99],v[136:139]\n"
"ds_read_b64_tr_b16 a[136:137],v101 offset:49152\n"
"v_mfma_f32_16x16x32_bf16 v[136:139],v[36:39],a[100:103],v[136:139]\n"
"ds_read_b64_tr_b16 a[138:139],v101 offset:50176\n"
"v_mfma_f32_16x16x32_bf16 v[168:171],v[36:39],a[108:111],v[168:171]\n"
"ds_read_b64_tr_b16 a[140:141],v101 offset:57344\n"
"v_mfma_f32_16x16x32_bf16 v[168:171],v[32:35],a[104:107],v[168:171]\n"
"s_add_u32 m0,s85,107520\n"
"ds_read_b64_tr_b16 a[142:143],v101 offset:58368\n"
"v_mfma_f32_16x16x32_bf16 v[200:203],v[32:35],a[112:115],v[200:203]\n"
"buffer_load_dwordx4 v111,s[28:31],s86 offen lds\n"
"v_mfma_f32_16x16x32_bf16 v[200:203],v[36:39],a[116:119],v[200:203]\n"
"s_xor_b32 s86,s86,786432\n"
"v_mfma_f32_16x16x32_bf16 v[232:235],v[36:39],a[124:127],v[232:235]\n"
"v_mfma_f32_16x16x32_bf16 v[232:235],v[32:35],a[120:123],v[232:235]\n"
"v_mfma_f32_16x16x32_bf16 v[236:239],v[40:43],a[120:123],v[236:239]\n"
"ds_read_b64_tr_b16 v[32:33],v102 offset:49152\n"
"v_mfma_f32_16x16x32_bf16 v[236:239],v[44:47],a[124:127],v[236:239]\n"
"ds_read_b64_tr_b16 v[34:35],v102 offset:50176\n"
"v_mfma_f32_16x16x32_bf16 v[204:207],v[44:47],a[116:119],v[204:207]\n"
"ds_read_b64_tr_b16 v[36:37],v102 offset:57344\n"
"v_mfma_f32_16x16x32_bf16 v[204:207],v[40:43],a[112:115],v[204:207]\n"
"s_add_u32 m0,s78,32768\n"
"ds_read_b64_tr_b16 v[38:39],v102 offset:58368\n"
"buffer_load_dwordx4 v112,s[24:27],s76 offen offset:0 sc1 lds\n"
"v_mfma_f32_16x16x32_bf16 v[172:175],v[40:43],a[104:107],v[172:175]\n"
"buffer_load_dwordx2 v[118:119],v120,s[24:27],s63 offen sc1\n"
"v_mfma_f32_16x16x32_bf16 v[172:175],v[44:47],a[108:111],v[172:175]\n"
"v_mfma_f32_16x16x32_bf16 v[140:143],v[44:47],a[100:103],v[140:143]\n"
"buffer_load_dwordx4 v112,s[24:27],s76 offen offset:1024 sc1 lds\n"
"v_mfma_f32_16x16x32_bf16 v[140:143],v[40:43],a[96:99],v[140:143]\n"
"s_waitcnt vmcnt(23)\n"
"v_mfma_f32_16x16x32_bf16 v[144:147],v[48:51],a[96:99],v[144:147]\n"
"ds_read_b64_tr_b16 v[40:41],v103 offset:49152\n"
"s_and_b32 s82,s100,7\n"
"v_mfma_f32_16x16x32_bf16 v[144:147],v[52:55],a[100:103],v[144:147]\n"
"ds_read_b64_tr_b16 v[42:43],v103 offset:50176\n"
"s_mul_i32 s82,s82,28\n"
"v_mfma_f32_16x16x32_bf16 v[176:179],v[52:55],a[108:111],v[176:179]\n"
"ds_read_b64_tr_b16 v[44:45],v103 offset:57344\n"
"s_lshr_b32 s83,s100,3\n"
"v_mfma_f32_16x16x32_bf16 v[176:179],v[48:51],a[104:107],v[176:179]\n"
"ds_read_b64_tr_b16 v[46:47],v103 offset:58368\n"
"s_add_u32 s23,s82,s83\n"
"v_mfma_f32_16x16x32_bf16 v[208:211],v[48:51],a[112:115],v[208:211]\n"
"buffer_load_dwordx4 v112,s[24:27],s76 offen offset:2048 sc1 lds\n"
"v_mfma_f32_16x16x32_bf16 v[208:211],v[52:55],a[116:119],v[208:211]\n"
"s_cmp_lt_u32 s83,28\n"
"s_cselect_b32 s34,1827872,0\n"
"s_cselect_b32 s42,8192,0\n"
"s_cselect_b32 s23,s23,0\n"
"v_mfma_f32_16x16x32_bf16 v[240:243],v[52:55],a[124:127],v[240:243]\n"
"s_mov_b32 s35,0x20000\n"
"ds_read_b128 v[0:3],v97 offset:0\n"
"s_mov_b32 s43,0x20000\n"
"v_mfma_f32_16x16x32_bf16 v[240:243],v[48:51],a[120:123],v[240:243]\n"
"ds_read_b128 v[8:11],v97 offset:1024\n"
"s_lshl2_add_u32 s82,s83,s52\n"
"v_mfma_f32_16x16x32_bf16 v[244:247],v[56:59],a[120:123],v[244:247]\n"
"ds_read_b64_tr_b16 v[48:49],v104 offset:49152\n"
"s_mul_i32 s82,s82,57344\n"
"v_mfma_f32_16x16x32_bf16 v[244:247],v[60:63],a[124:127],v[244:247]\n"
"ds_read_b64_tr_b16 v[50:51],v104 offset:50176\n"
"s_and_b32 s83,s100,7\n"
"v_mfma_f32_16x16x32_bf16 v[212:215],v[60:63],a[116:119],v[212:215]\n"
"ds_read_b64_tr_b16 v[52:53],v104 offset:57344\n"
"s_mul_i32 s83,s83,896\n"
"v_mfma_f32_16x16x32_bf16 v[212:215],v[56:59],a[112:115],v[212:215]\n"
"ds_read_b64_tr_b16 v[54:55],v104 offset:58368\n"
"s_add_u32 s82,s82,s83\n"
"v_mfma_f32_16x16x32_bf16 v[180:183],v[56:59],a[104:107],v[180:183]\n"
"buffer_load_dwordx4 v112,s[24:27],s76 offen offset:3072 sc1 lds\n"
"v_mfma_f32_16x16x32_bf16 v[180:183],v[60:63],a[108:111],v[180:183]\n"
"s_add_u32 m0,s78,49152\n"
"s_xor_b32 s76,s76,16384\n"
"ds_read_b128 v[16:19],v97 offset:2048\n"
"v_mfma_f32_16x16x32_bf16 v[148:151],v[60:63],a[100:103],v[148:151]\n"
"buffer_load_dwordx4 v112,s[24:27],s76 offen offset:0 sc1 lds\n"
"v_mfma_f32_16x16x32_bf16 v[148:151],v[56:59],a[96:99],v[148:151]\n"
"s_add_u32 s44,s32,s82\n"
"ds_read_b128 v[24:27],v97 offset:3072\n"
"s_addc_u32 s45,s33,0\n"
"v_mfma_f32_16x16x32_bf16 v[152:155],v[64:67],a[96:99],v[152:155]\n"
"s_add_u32 s48,s36,s82\n"
"ds_read_b64_tr_b16 v[56:57],v105 offset:49152\n"
"s_addc_u32 s49,s37,0\n"
"v_mfma_f32_16x16x32_bf16 v[152:155],v[68:71],a[100:103],v[152:155]\n"
"ds_read_b64_tr_b16 v[58:59],v105 offset:50176\n"
"s_mov_b32 s46,51072\n"
"v_mfma_f32_16x16x32_bf16 v[184:187],v[68:71],a[108:111],v[184:187]\n"
"ds_read_b64_tr_b16 v[60:61],v105 offset:57344\n"
"s_mov_b32 s50,s46\n"
"v_mfma_f32_16x16x32_bf16 v[184:187],v[64:67],a[104:107],v[184:187]\n"
"ds_read_b64_tr_b16 v[62:63],v105 offset:58368\n"
"s_mov_b32 s47,0x20000\n"
"v_mfma_f32_16x16x32_bf16 v[216:219],v[64:67],a[112:115],v[216:219]\n"
"ds_read_b128 v[4:7],v97 offset:16384\n"
"s_mov_b32 s51,0x20000\n"
"v_mfma_f32_16x16x32_bf16 v[216:219],v[68:71],a[116:119],v[216:219]\n"
"ds_read_b128 v[12:15],v97 offset:17408\n"
"s_mul_i32 s82,s52,1835008\n"
"v_mfma_f32_16x16x32_bf16 v[248:251],v[68:71],a[124:127],v[248:251]\n"
"ds_read_b128 v[20:23],v97 offset:18432\n"
"s_lshl_b32 s83,s23,5\n"
"v_mfma_f32_16x16x32_bf16 v[248:251],v[64:67],a[120:123],v[248:251]\n"
"ds_read_b128 v[28:31],v97 offset:19456\n"
"s_add_u32 s82,s82,s83\n"
"v_mfma_f32_16x16x32_bf16 v[252:255],v[72:75],a[120:123],v[252:255]\n"
"buffer_load_dwordx4 v112,s[24:27],s76 offen offset:1024 sc1 lds\n"
"v_mfma_f32_16x16x32_bf16 v[252:255],v[76:79],a[124:127],v[252:255]\n"
"s_add_u32 s32,s32,s82\n"
"s_addc_u32 s33,s33,0\n"
"s_add_u32 s36,s36,s82\n"
"v_mfma_f32_16x16x32_bf16 v[220:223],v[76:79],a[116:119],v[220:223]\n"
"s_addc_u32 s37,s37,0\n"
"s_lshl2_add_u32 s82,s23,s52\n"
"s_lshl_b32 s82,s82,13\n"
"v_mfma_f32_16x16x32_bf16 v[220:223],v[72:75],a[112:115],v[220:223]\n"
"buffer_load_dwordx4 v112,s[24:27],s76 offen offset:2048 sc1 lds\n"
"v_mfma_f32_16x16x32_bf16 v[188:191],v[72:75],a[104:107],v[188:191]\n"
"s_add_u32 s40,s40,s82\n"
"s_addc_u32 s41,s41,0\n"
"v_bfe_u32 v121,v112,5,2\n"
"v_mfma_f32_16x16x32_bf16 v[188:191],v[76:79],a[108:111],v[188:191]\n"
"v_bfe_u32 v122,v112,7,2\n"
"v_lshl_add_u32 v121,v122,3,v121\n"
"v_mfma_f32_16x16x32_bf16 v[156:159],v[76:79],a[100:103],v[156:159]\n"
"buffer_load_dwordx4 v112,s[24:27],s76 offen offset:3072 sc1 lds\n"
"v_mfma_f32_16x16x32_bf16 v[156:159],v[72:75],a[96:99],v[156:159]\n"
"s_xor_b32 s76,s76,49152\n"
"v_bfe_u32 v122,v112,9,1\n"
"s_waitcnt vmcnt(13) lgkmcnt(0)\n"
"s_barrier\n"
"v_mfma_f32_16x16x32_bf16 v[128:131],v[80:83],v[0:3],v[128:131]\n"
"ds_read_b64_tr_b16 v[64:65],v98 offset:32768\n"
"ds_read_b64_tr_b16 v[72:73],v99 offset:32768\n"
"v_mfma_f32_16x16x32_bf16 v[128:131],v[84:87],v[4:7],v[128:131]\n"
"ds_read_b64_tr_b16 v[66:67],v98 offset:33792\n"
"ds_read_b64_tr_b16 v[74:75],v99 offset:33792\n"
"v_mfma_f32_16x16x32_bf16 v[160:163],v[84:87],v[12:15],v[160:163]\n"
"ds_read_b64_tr_b16 v[68:69],v98 offset:40960\n"
"ds_read_b64_tr_b16 v[76:77],v99 offset:40960\n"
"v_mfma_f32_16x16x32_bf16 v[160:163],v[80:83],v[8:11],v[160:163]\n"
"s_add_u32 m0,s85,147456\n"
"ds_read_b64_tr_b16 v[70:71],v98 offset:41984\n"
"ds_read_b64_tr_b16 v[78:79],v99 offset:41984\n"
"v_mfma_f32_16x16x32_bf16 v[192:195],v[80:83],v[16:19],v[192:195]\n"
"buffer_load_dwordx4 v110,s[28:31],s86 offen lds\n"
"ds_read_b64_tr_b16 a[176:177],v107 offset:0\n"
"v_mfma_f32_16x16x32_bf16 v[192:195],v[84:87],v[20:23],v[192:195]\n"
"s_add_u32 m0,s85,148480\n"
"ds_read_b64_tr_b16 a[178:179],v107 offset:4096\n"
"ds_read_b64_tr_b16 a[180:181],v107 offset:8\n"
"v_mfma_f32_16x16x32_bf16 v[224:227],v[84:87],v[28:31],v[224:227]\n"
"buffer_load_dwordx4 v111,s[28:31],s86 offen lds\n"
"ds_read_b64_tr_b16 a[182:183],v107 offset:4104\n"
"v_mfma_f32_16x16x32_bf16 v[224:227],v[80:83],v[24:27],v[224:227]\n"
"s_xor_b32 s86,s86,262144\n"
"ds_read_b64_tr_b16 a[184:185],v107 offset:8192\n"
"ds_read_b64_tr_b16 a[186:187],v107 offset:12288\n"
"v_mfma_f32_16x16x32_bf16 v[228:231],v[88:91],v[24:27],v[228:231]\n"
"ds_read_b64_tr_b16 v[80:81],v100 offset:32768\n"
"s_waitcnt vmcnt(9)\n"
"v_xor_b32 v116,s80,v118\n"
"v_mfma_f32_16x16x32_bf16 v[228:231],v[92:95],v[28:31],v[228:231]\n"
"ds_read_b64_tr_b16 v[82:83],v100 offset:33792\n"
"ds_read_b64_tr_b16 a[188:189],v107 offset:8200\n"
"v_xor_b32 v117,s81,v119\n"
"v_mfma_f32_16x16x32_bf16 v[196:199],v[92:95],v[20:23],v[196:199]\n"
"ds_read_b64_tr_b16 v[84:85],v100 offset:40960\n"
"ds_read_b64_tr_b16 a[190:191],v107 offset:12296\n"
"v_mfma_f32_16x16x32_bf16 v[196:199],v[88:91],v[16:19],v[196:199]\n"
"s_add_u32 m0,s85,155648\n"
"ds_read_b64_tr_b16 v[86:87],v100 offset:41984\n"
"v_or_b32 v116,v116,v117\n"
"v_mfma_f32_16x16x32_bf16 v[164:167],v[88:91],v[8:11],v[164:167]\n"
"buffer_load_dwordx4 v110,s[28:31],s86 offen lds\n"
"v_mfma_f32_16x16x32_bf16 v[164:167],v[92:95],v[12:15],v[164:167]\n"
"s_add_u32 m0,s85,156672\n"
"v_cmp_ne_u32 vcc,0,v116\n"
"v_mfma_f32_16x16x32_bf16 v[132:135],v[92:95],v[4:7],v[132:135]\n"
"buffer_load_dwordx4 v111,s[28:31],s86 offen lds\n"
"v_mfma_f32_16x16x32_bf16 v[132:135],v[88:91],v[0:3],v[132:135]\n"
"s_xor_b32 s86,s86,16515072\n"
"s_cbranch_vccnz .Lfk_kv_s\n"
".Lfk_kv_ok:\n"
"v_mfma_f32_16x16x32_bf16 v[136:139],a[128:131],v[0:3],v[136:139]\n"
"ds_read_b64_tr_b16 v[88:89],v101 offset:32768\n"
"v_lshl_add_u32 v121,v122,2,v121\n"
"v_mfma_f32_16x16x32_bf16 v[136:139],a[132:135],v[4:7],v[136:139]\n"
"ds_read_b64_tr_b16 v[90:91],v101 offset:33792\n"
"v_mul_u32_u24 v121,7168,v121\n"
"v_mfma_f32_16x16x32_bf16 v[168:171],a[132:135],v[12:15],v[168:171]\n"
"ds_read_b64_tr_b16 v[92:93],v101 offset:40960\n"
"v_and_b32 v122,16,v112\n"
"v_mfma_f32_16x16x32_bf16 v[168:171],a[128:131],v[8:11],v[168:171]\n"
"s_add_u32 m0,s85,81920\n"
"s_add_u32 s77,s73,16384\n"
"ds_read_b64_tr_b16 v[94:95],v101 offset:41984\n"
"v_mfma_f32_16x16x32_bf16 v[200:203],a[128:131],v[16:19],v[200:203]\n"
"buffer_load_dwordx4 v112,s[24:27],s77 offen offset:0 sc1 lds\n"
"v_mfma_f32_16x16x32_bf16 v[200:203],a[132:135],v[20:23],v[200:203]\n"
"v_add_u32 v115,v121,v122\n"
"s_mov_b32 s54,-229376\n"
"v_mfma_f32_16x16x32_bf16 v[232:235],a[132:135],v[28:31],v[232:235]\n"
"buffer_load_dwordx4 v112,s[24:27],s77 offen offset:1024 sc1 lds\n"
"v_mfma_f32_16x16x32_bf16 v[232:235],a[128:131],v[24:27],v[232:235]\n"
"s_lshl_b32 s55,s52,10\n"
"s_add_u32 s55,s55,131072\n"
"v_mfma_f32_16x16x32_bf16 v[236:239],a[136:139],v[24:27],v[236:239]\n"
"ds_read_b64_tr_b16 a[128:129],v102 offset:32768\n"
"s_mov_b32 s22,229376\n"
"v_mfma_f32_16x16x32_bf16 v[236:239],a[140:143],v[28:31],v[236:239]\n"
"ds_read_b64_tr_b16 a[130:131],v102 offset:33792\n"
"v_lshrrev_b32 v121,7,v112\n"
"v_mfma_f32_16x16x32_bf16 v[204:207],a[140:143],v[20:23],v[204:207]\n"
"ds_read_b64_tr_b16 a[132:133],v102 offset:40960\n"
"v_mul_u32_u24 v121,7168,v121\n"
"v_mfma_f32_16x16x32_bf16 v[204:207],a[136:139],v[16:19],v[204:207]\n"
"s_add_u32 m0,s85,65536\n"
"s_add_u32 s77,s73,65536\n"
"ds_read_b64_tr_b16 a[134:135],v102 offset:41984\n"
"v_mfma_f32_16x16x32_bf16 v[172:175],a[136:139],v[8:11],v[172:175]\n"
"buffer_load_dwordx4 v112,s[24:27],s77 offen offset:0 sc1 lds\n"
"v_mfma_f32_16x16x32_bf16 v[172:175],a[140:143],v[12:15],v[172:175]\n"
"v_lshlrev_b32 v123,3,v112\n"
"v_and_b32 v122,0x380,v123\n"
"v_mfma_f32_16x16x32_bf16 v[140:143],a[140:143],v[4:7],v[140:143]\n"
"buffer_load_dwordx4 v112,s[24:27],s77 offen offset:1024 sc1 lds\n"
"v_mfma_f32_16x16x32_bf16 v[140:143],a[136:139],v[0:3],v[140:143]\n"
"v_min_u32 v122,0x300,v122\n"
"v_add_u32 v122,v121,v122\n"
"v_mfma_f32_16x16x32_bf16 v[144:147],v[32:35],v[0:3],v[144:147]\n"
"ds_read_b64_tr_b16 a[136:137],v103 offset:32768\n"
"v_mfma_f32_16x16x32_bf16 v[144:147],v[36:39],v[4:7],v[144:147]\n"
"ds_read_b64_tr_b16 a[138:139],v103 offset:33792\n"
"v_mfma_f32_16x16x32_bf16 v[176:179],v[36:39],v[12:15],v[176:179]\n"
"ds_read_b64_tr_b16 a[140:141],v103 offset:40960\n"
"v_mfma_f32_16x16x32_bf16 v[176:179],v[32:35],v[8:11],v[176:179]\n"
"s_add_u32 m0,s85,73728\n"
"s_add_u32 s77,s73,24576\n"
"ds_read_b64_tr_b16 a[142:143],v103 offset:41984\n"
"v_mfma_f32_16x16x32_bf16 v[208:211],v[32:35],v[16:19],v[208:211]\n"
"buffer_load_dwordx4 v112,s[24:27],s77 offen offset:0 sc1 lds\n"
"v_mfma_f32_16x16x32_bf16 v[208:211],v[36:39],v[20:23],v[208:211]\n"
"ds_read_b128 a[96:99],v96 offset:0\n"
"v_mfma_f32_16x16x32_bf16 v[240:243],v[36:39],v[28:31],v[240:243]\n"
"buffer_load_dwordx4 v112,s[24:27],s77 offen offset:1024 sc1 lds\n"
"v_mfma_f32_16x16x32_bf16 v[240:243],v[32:35],v[24:27],v[240:243]\n"
"ds_read_b128 a[104:107],v96 offset:1024\n"
"v_mfma_f32_16x16x32_bf16 v[244:247],v[40:43],v[24:27],v[244:247]\n"
"ds_read_b64_tr_b16 v[32:33],v104 offset:32768\n"
"v_mfma_f32_16x16x32_bf16 v[244:247],v[44:47],v[28:31],v[244:247]\n"
"ds_read_b64_tr_b16 v[34:35],v104 offset:33792\n"
"v_mfma_f32_16x16x32_bf16 v[212:215],v[44:47],v[20:23],v[212:215]\n"
"ds_read_b64_tr_b16 v[36:37],v104 offset:40960\n"
"v_mfma_f32_16x16x32_bf16 v[212:215],v[40:43],v[16:19],v[212:215]\n"
"ds_read_b64_tr_b16 v[38:39],v104 offset:41984\n"
"s_mov_b32 s50,22400\n"
"v_mfma_f32_16x16x32_bf16 v[180:183],v[40:43],v[8:11],v[180:183]\n"
"buffer_load_dword a148,v122,s[48:51],0 offen\n"
"v_mfma_f32_16x16x32_bf16 v[180:183],v[44:47],v[12:15],v[180:183]\n"
"ds_read_b128 a[112:115],v96 offset:2048\n"
"v_mfma_f32_16x16x32_bf16 v[148:151],v[44:47],v[4:7],v[148:151]\n"
"v_mfma_f32_16x16x32_bf16 v[148:151],v[40:43],v[0:3],v[148:151]\n"
"ds_read_b128 a[120:123],v96 offset:3072\n"
"v_mfma_f32_16x16x32_bf16 v[152:155],v[48:51],v[0:3],v[152:155]\n"
"ds_read_b64_tr_b16 v[40:41],v105 offset:32768\n"
"v_mfma_f32_16x16x32_bf16 v[152:155],v[52:55],v[4:7],v[152:155]\n"
"ds_read_b64_tr_b16 v[42:43],v105 offset:33792\n"
"v_mfma_f32_16x16x32_bf16 v[184:187],v[52:55],v[12:15],v[184:187]\n"
"ds_read_b64_tr_b16 v[44:45],v105 offset:40960\n"
"v_mfma_f32_16x16x32_bf16 v[184:187],v[48:51],v[8:11],v[184:187]\n"
"ds_read_b64_tr_b16 v[46:47],v105 offset:41984\n"
"v_mfma_f32_16x16x32_bf16 v[216:219],v[48:51],v[16:19],v[216:219]\n"
"ds_read_b128 a[100:103],v96 offset:16384\n"
"v_mfma_f32_16x16x32_bf16 v[216:219],v[52:55],v[20:23],v[216:219]\n"
"ds_read_b128 a[108:111],v96 offset:17408\n"
"v_mfma_f32_16x16x32_bf16 v[248:251],v[52:55],v[28:31],v[248:251]\n"
"ds_read_b128 a[116:119],v96 offset:18432\n"
"v_mfma_f32_16x16x32_bf16 v[248:251],v[48:51],v[24:27],v[248:251]\n"
"ds_read_b128 a[124:127],v96 offset:19456\n"
"v_mfma_f32_16x16x32_bf16 v[252:255],v[56:59],v[24:27],v[252:255]\n"
"v_mfma_f32_16x16x32_bf16 v[252:255],v[60:63],v[28:31],v[252:255]\n"
"buffer_store_dwordx4 a[160:163],v112,s[24:27],s3 offen offset:0 sc1\n"
"v_mfma_f32_16x16x32_bf16 v[220:223],v[60:63],v[20:23],v[220:223]\n"
"v_mfma_f32_16x16x32_bf16 v[220:223],v[56:59],v[16:19],v[220:223]\n"
"v_mfma_f32_16x16x32_bf16 v[188:191],v[56:59],v[8:11],v[188:191]\n"
"v_mfma_f32_16x16x32_bf16 v[188:191],v[60:63],v[12:15],v[188:191]\n"
"buffer_store_dwordx4 a[164:167],v112,s[24:27],s3 offen offset:1024 sc1\n"
"v_mfma_f32_16x16x32_bf16 v[156:159],v[60:63],v[4:7],v[156:159]\n"
"v_mfma_f32_16x16x32_bf16 v[156:159],v[56:59],v[0:3],v[156:159]\n"
"s_waitcnt lgkmcnt(0)\n"
"s_barrier\n"
"v_mfma_f32_16x16x32_bf16 v[128:131],v[64:67],a[96:99],v[128:131]\n"
"ds_read_b64_tr_b16 v[48:49],v98 offset:0\n"
"ds_read_b64_tr_b16 v[56:57],v99 offset:0\n"
"v_mfma_f32_16x16x32_bf16 v[128:131],v[68:71],a[100:103],v[128:131]\n"
"ds_read_b64_tr_b16 v[50:51],v98 offset:1024\n"
"ds_read_b64_tr_b16 v[58:59],v99 offset:1024\n"
"v_mfma_f32_16x16x32_bf16 v[160:163],v[68:71],a[108:111],v[160:163]\n"
"ds_read_b64_tr_b16 v[52:53],v98 offset:8192\n"
"ds_read_b64_tr_b16 v[60:61],v99 offset:8192\n"
"v_mfma_f32_16x16x32_bf16 v[160:163],v[64:67],a[104:107],v[160:163]\n"
"s_add_u32 m0,s78,0\n"
"ds_read_b64_tr_b16 v[54:55],v98 offset:9216\n"
"ds_read_b64_tr_b16 v[62:63],v99 offset:9216\n"
"v_mfma_f32_16x16x32_bf16 v[192:195],v[64:67],a[112:115],v[192:195]\n"
"buffer_load_dwordx4 v112,s[24:27],s76 offen offset:0 sc1 lds\n"
"v_mfma_f32_16x16x32_bf16 v[192:195],v[68:71],a[116:119],v[192:195]\n"
"v_mfma_f32_16x16x32_bf16 v[224:227],v[68:71],a[124:127],v[224:227]\n"
"v_mfma_f32_16x16x32_bf16 v[224:227],v[64:67],a[120:123],v[224:227]\n"
"v_mfma_f32_16x16x32_bf16 v[228:231],v[72:75],a[120:123],v[228:231]\n"
"ds_read_b64_tr_b16 v[64:65],v100 offset:0\n"
"v_mfma_f32_16x16x32_bf16 v[228:231],v[76:79],a[124:127],v[228:231]\n"
"ds_read_b64_tr_b16 v[66:67],v100 offset:1024\n"
"v_mfma_f32_16x16x32_bf16 v[196:199],v[76:79],a[116:119],v[196:199]\n"
"ds_read_b64_tr_b16 v[68:69],v100 offset:8192\n"
"v_mfma_f32_16x16x32_bf16 v[196:199],v[72:75],a[112:115],v[196:199]\n"
"ds_read_b64_tr_b16 v[70:71],v100 offset:9216\n"
"v_mfma_f32_16x16x32_bf16 v[164:167],v[72:75],a[104:107],v[164:167]\n"
"buffer_load_dwordx4 v112,s[24:27],s76 offen offset:1024 sc1 lds\n"
"v_mfma_f32_16x16x32_bf16 v[164:167],v[76:79],a[108:111],v[164:167]\n"
"v_mfma_f32_16x16x32_bf16 v[132:135],v[76:79],a[100:103],v[132:135]\n"
"buffer_load_dwordx4 v112,s[24:27],s76 offen offset:2048 sc1 lds\n"
"v_mfma_f32_16x16x32_bf16 v[132:135],v[72:75],a[96:99],v[132:135]\n"
"v_mfma_f32_16x16x32_bf16 v[136:139],v[80:83],a[96:99],v[136:139]\n"
"ds_read_b64_tr_b16 v[72:73],v101 offset:0\n"
"v_mfma_f32_16x16x32_bf16 v[136:139],v[84:87],a[100:103],v[136:139]\n"
"ds_read_b64_tr_b16 v[74:75],v101 offset:1024\n"
"v_mfma_f32_16x16x32_bf16 v[168:171],v[84:87],a[108:111],v[168:171]\n"
"ds_read_b64_tr_b16 v[76:77],v101 offset:8192\n"
"v_mfma_f32_16x16x32_bf16 v[168:171],v[80:83],a[104:107],v[168:171]\n"
"ds_read_b64_tr_b16 v[78:79],v101 offset:9216\n"
"v_mfma_f32_16x16x32_bf16 v[200:203],v[80:83],a[112:115],v[200:203]\n"
"buffer_load_dwordx4 v112,s[24:27],s76 offen offset:3072 sc1 lds\n"
"v_mfma_f32_16x16x32_bf16 v[200:203],v[84:87],a[116:119],v[200:203]\n"
"s_xor_b32 s76,s76,16384\n"
"v_mfma_f32_16x16x32_bf16 v[232:235],v[84:87],a[124:127],v[232:235]\n"
"v_mfma_f32_16x16x32_bf16 v[232:235],v[80:83],a[120:123],v[232:235]\n"
"v_mfma_f32_16x16x32_bf16 v[236:239],v[88:91],a[120:123],v[236:239]\n"
"ds_read_b64_tr_b16 v[80:81],v102 offset:0\n"
"v_mfma_f32_16x16x32_bf16 v[236:239],v[92:95],a[124:127],v[236:239]\n"
"ds_read_b64_tr_b16 v[82:83],v102 offset:1024\n"
"v_mfma_f32_16x16x32_bf16 v[204:207],v[92:95],a[116:119],v[204:207]\n"
"ds_read_b64_tr_b16 v[84:85],v102 offset:8192\n"
"v_mfma_f32_16x16x32_bf16 v[204:207],v[88:91],a[112:115],v[204:207]\n"
"s_add_u32 m0,s78,16384\n"
"ds_read_b64_tr_b16 v[86:87],v102 offset:9216\n"
"v_mfma_f32_16x16x32_bf16 v[172:175],v[88:91],a[104:107],v[172:175]\n"
"buffer_load_dwordx4 v112,s[24:27],s76 offen offset:0 sc1 lds\n"
"v_mfma_f32_16x16x32_bf16 v[172:175],v[92:95],a[108:111],v[172:175]\n"
"v_mfma_f32_16x16x32_bf16 v[140:143],v[92:95],a[100:103],v[140:143]\n"
"buffer_load_dwordx4 v112,s[24:27],s76 offen offset:1024 sc1 lds\n"
"v_mfma_f32_16x16x32_bf16 v[140:143],v[88:91],a[96:99],v[140:143]\n"
".Lfk_q27:\n"
"s_waitcnt vmcnt(19)\n"
"v_mfma_f32_16x16x32_bf16 v[144:147],a[128:131],a[96:99],v[144:147]\n"
"ds_read_b64_tr_b16 v[88:89],v103 offset:0\n"
"v_mfma_f32_16x16x32_bf16 v[144:147],a[132:135],a[100:103],v[144:147]\n"
"ds_read_b64_tr_b16 v[90:91],v103 offset:1024\n"
"v_mfma_f32_16x16x32_bf16 v[176:179],a[132:135],a[108:111],v[176:179]\n"
"ds_read_b64_tr_b16 v[92:93],v103 offset:8192\n"
"v_mfma_f32_16x16x32_bf16 v[176:179],a[128:131],a[104:107],v[176:179]\n"
"ds_read_b64_tr_b16 v[94:95],v103 offset:9216\n"
"v_mfma_f32_16x16x32_bf16 v[208:211],a[128:131],a[112:115],v[208:211]\n"
"buffer_load_dwordx4 v112,s[24:27],s76 offen offset:2048 sc1 lds\n"
"v_mfma_f32_16x16x32_bf16 v[208:211],a[132:135],a[116:119],v[208:211]\n"
"ds_read_b128 v[0:3],v96 offset:32768\n"
"v_mfma_f32_16x16x32_bf16 v[240:243],a[132:135],a[124:127],v[240:243]\n"
"v_mfma_f32_16x16x32_bf16 v[240:243],a[128:131],a[120:123],v[240:243]\n"
"ds_read_b128 v[8:11],v96 offset:33792\n"
"v_mfma_f32_16x16x32_bf16 v[244:247],a[136:139],a[120:123],v[244:247]\n"
"ds_read_b64_tr_b16 a[128:129],v104 offset:0\n"
"v_mfma_f32_16x16x32_bf16 v[244:247],a[140:143],a[124:127],v[244:247]\n"
"ds_read_b64_tr_b16 a[130:131],v104 offset:1024\n"
"v_mfma_f32_16x16x32_bf16 v[212:215],a[140:143],a[116:119],v[212:215]\n"
"ds_read_b64_tr_b16 a[132:133],v104 offset:8192\n"
"v_mfma_f32_16x16x32_bf16 v[212:215],a[136:139],a[112:115],v[212:215]\n"
"ds_read_b64_tr_b16 a[134:135],v104 offset:9216\n"
"v_mfma_f32_16x16x32_bf16 v[180:183],a[136:139],a[104:107],v[180:183]\n"
"buffer_load_dwordx4 v112,s[24:27],s76 offen offset:3072 sc1 lds\n"
"v_mfma_f32_16x16x32_bf16 v[180:183],a[140:143],a[108:111],v[180:183]\n"
"s_add_u32 m0,s85,122880\n"
"s_add_u32 s77,s73,0\n"
"ds_read_b128 v[16:19],v96 offset:34816\n"
"v_mfma_f32_16x16x32_bf16 v[148:151],a[140:143],a[100:103],v[148:151]\n"
"buffer_load_dwordx4 v112,s[24:27],s77 offen offset:0 sc1 lds\n"
"v_mfma_f32_16x16x32_bf16 v[148:151],a[136:139],a[96:99],v[148:151]\n"
"s_xor_b32 s76,s76,1032192\n"
"ds_read_b128 v[24:27],v96 offset:35840\n"
"v_mfma_f32_16x16x32_bf16 v[152:155],v[32:35],a[96:99],v[152:155]\n"
"ds_read_b64_tr_b16 a[136:137],v105 offset:0\n"
"v_mfma_f32_16x16x32_bf16 v[152:155],v[36:39],a[100:103],v[152:155]\n"
"ds_read_b64_tr_b16 a[138:139],v105 offset:1024\n"
"v_mfma_f32_16x16x32_bf16 v[184:187],v[36:39],a[108:111],v[184:187]\n"
"ds_read_b64_tr_b16 a[140:141],v105 offset:8192\n"
"v_mfma_f32_16x16x32_bf16 v[184:187],v[32:35],a[104:107],v[184:187]\n"
"ds_read_b64_tr_b16 a[142:143],v105 offset:9216\n"
"v_mfma_f32_16x16x32_bf16 v[216:219],v[32:35],a[112:115],v[216:219]\n"
"ds_read_b128 v[4:7],v96 offset:49152\n"
"v_mfma_f32_16x16x32_bf16 v[216:219],v[36:39],a[116:119],v[216:219]\n"
"ds_read_b128 v[12:15],v96 offset:50176\n"
"v_mfma_f32_16x16x32_bf16 v[248:251],v[36:39],a[124:127],v[248:251]\n"
"ds_read_b128 v[20:23],v96 offset:51200\n"
"v_mfma_f32_16x16x32_bf16 v[248:251],v[32:35],a[120:123],v[248:251]\n"
"ds_read_b128 v[28:31],v96 offset:52224\n"
"v_mfma_f32_16x16x32_bf16 v[252:255],v[40:43],a[120:123],v[252:255]\n"
"buffer_load_dwordx4 v112,s[24:27],s77 offen offset:1024 sc1 lds\n"
"v_mfma_f32_16x16x32_bf16 v[252:255],v[44:47],a[124:127],v[252:255]\n"
"v_mfma_f32_16x16x32_bf16 v[220:223],v[44:47],a[116:119],v[220:223]\n"
"s_add_u32 m0,s85,114688\n"
"s_add_u32 s77,s73,32768\n"
"v_mfma_f32_16x16x32_bf16 v[220:223],v[40:43],a[112:115],v[220:223]\n"
"buffer_load_dwordx4 v112,s[24:27],s77 offen offset:0 sc1 lds\n"
"v_mfma_f32_16x16x32_bf16 v[188:191],v[40:43],a[104:107],v[188:191]\n"
"v_mfma_f32_16x16x32_bf16 v[188:191],v[44:47],a[108:111],v[188:191]\n"
"v_mfma_f32_16x16x32_bf16 v[156:159],v[44:47],a[100:103],v[156:159]\n"
"buffer_load_dwordx4 v112,s[24:27],s77 offen offset:1024 sc1 lds\n"
"v_mfma_f32_16x16x32_bf16 v[156:159],v[40:43],a[96:99],v[156:159]\n"
"s_waitcnt vmcnt(21) lgkmcnt(0)\n"
"s_barrier\n"
"v_mfma_f32_16x16x32_bf16 v[128:131],v[48:51],v[0:3],v[128:131]\n"
"ds_read_b64_tr_b16 v[32:33],v98 offset:49152\n"
"ds_read_b64_tr_b16 v[40:41],v99 offset:49152\n"
"v_mfma_f32_16x16x32_bf16 v[128:131],v[52:55],v[4:7],v[128:131]\n"
"ds_read_b64_tr_b16 v[34:35],v98 offset:50176\n"
"ds_read_b64_tr_b16 v[42:43],v99 offset:50176\n"
"v_mfma_f32_16x16x32_bf16 v[160:163],v[52:55],v[12:15],v[160:163]\n"
"s_add_u32 m0,s85,98304\n"
"ds_read_b64_tr_b16 v[36:37],v98 offset:57344\n"
"ds_read_b64_tr_b16 v[44:45],v99 offset:57344\n"
"v_mfma_f32_16x16x32_bf16 v[160:163],v[48:51],v[8:11],v[160:163]\n"
"s_add_u32 s77,s73,49152\n"
"ds_read_b64_tr_b16 v[38:39],v98 offset:58368\n"
"ds_read_b64_tr_b16 v[46:47],v99 offset:58368\n"
"v_mfma_f32_16x16x32_bf16 v[192:195],v[48:51],v[16:19],v[192:195]\n"
"buffer_load_dwordx4 v112,s[24:27],s77 offen offset:0 sc1 lds\n"
"v_mfma_f32_16x16x32_bf16 v[192:195],v[52:55],v[20:23],v[192:195]\n"
"v_mfma_f32_16x16x32_bf16 v[224:227],v[52:55],v[28:31],v[224:227]\n"
"buffer_load_dwordx4 v112,s[24:27],s77 offen offset:1024 sc1 lds\n"
"v_mfma_f32_16x16x32_bf16 v[224:227],v[48:51],v[24:27],v[224:227]\n"
"v_mfma_f32_16x16x32_bf16 v[228:231],v[56:59],v[24:27],v[228:231]\n"
"ds_read_b64_tr_b16 v[48:49],v100 offset:49152\n"
"v_mfma_f32_16x16x32_bf16 v[228:231],v[60:63],v[28:31],v[228:231]\n"
"ds_read_b64_tr_b16 v[50:51],v100 offset:50176\n"
"v_mfma_f32_16x16x32_bf16 v[196:199],v[60:63],v[20:23],v[196:199]\n"
"ds_read_b64_tr_b16 v[52:53],v100 offset:57344\n"
"v_mfma_f32_16x16x32_bf16 v[196:199],v[56:59],v[16:19],v[196:199]\n"
"s_add_u32 m0,s85,106496\n"
"s_add_u32 s77,s73,8192\n"
"ds_read_b64_tr_b16 v[54:55],v100 offset:58368\n"
"v_mfma_f32_16x16x32_bf16 v[164:167],v[56:59],v[8:11],v[164:167]\n"
"buffer_load_dwordx4 v112,s[24:27],s77 offen offset:0 sc1 lds\n"
"v_mfma_f32_16x16x32_bf16 v[164:167],v[60:63],v[12:15],v[164:167]\n"
"v_mfma_f32_16x16x32_bf16 v[132:135],v[60:63],v[4:7],v[132:135]\n"
"buffer_load_dwordx4 v112,s[24:27],s77 offen offset:1024 sc1 lds\n"
"v_mfma_f32_16x16x32_bf16 v[132:135],v[56:59],v[0:3],v[132:135]\n"
"v_mfma_f32_16x16x32_bf16 v[136:139],v[64:67],v[0:3],v[136:139]\n"
"ds_read_b64_tr_b16 v[56:57],v101 offset:49152\n"
"v_mfma_f32_16x16x32_bf16 v[136:139],v[68:71],v[4:7],v[136:139]\n"
"ds_read_b64_tr_b16 v[58:59],v101 offset:50176\n"
"v_mfma_f32_16x16x32_bf16 v[168:171],v[68:71],v[12:15],v[168:171]\n"
"ds_read_b64_tr_b16 v[60:61],v101 offset:57344\n"
"ds_read_b64_tr_b16 v[62:63],v101 offset:58368\n"
"v_mfma_f32_16x16x32_bf16 v[168:171],v[64:67],v[8:11],v[168:171]\n"
"s_add_u32 s48,s48,28672\n"
"s_addc_u32 s49,s49,0\n"
"v_mfma_f32_16x16x32_bf16 v[200:203],v[64:67],v[16:19],v[200:203]\n"
"buffer_load_dword a148,v122,s[48:51],0 offen\n"
"v_mfma_f32_16x16x32_bf16 v[200:203],v[68:71],v[20:23],v[200:203]\n"
"v_mfma_f32_16x16x32_bf16 v[232:235],v[68:71],v[28:31],v[232:235]\n"
"v_mfma_f32_16x16x32_bf16 v[232:235],v[64:67],v[24:27],v[232:235]\n"
"v_mfma_f32_16x16x32_bf16 v[236:239],v[72:75],v[24:27],v[236:239]\n"
"ds_read_b64_tr_b16 v[64:65],v102 offset:49152\n"
"v_mfma_f32_16x16x32_bf16 v[236:239],v[76:79],v[28:31],v[236:239]\n"
"ds_read_b64_tr_b16 v[66:67],v102 offset:50176\n"
"v_mfma_f32_16x16x32_bf16 v[204:207],v[76:79],v[20:23],v[204:207]\n"
"ds_read_b64_tr_b16 v[68:69],v102 offset:57344\n"
"v_mfma_f32_16x16x32_bf16 v[204:207],v[72:75],v[16:19],v[204:207]\n"
"ds_read_b64_tr_b16 v[70:71],v102 offset:58368\n"
"v_mfma_f32_16x16x32_bf16 v[172:175],v[72:75],v[8:11],v[172:175]\n"
"v_mfma_f32_16x16x32_bf16 v[172:175],v[76:79],v[12:15],v[172:175]\n"
"v_mfma_f32_16x16x32_bf16 v[140:143],v[76:79],v[4:7],v[140:143]\n"
"v_mfma_f32_16x16x32_bf16 v[140:143],v[72:75],v[0:3],v[140:143]\n"
"s_waitcnt vmcnt(9)\n"
"v_mfma_f32_16x16x32_bf16 v[144:147],v[80:83],v[0:3],v[144:147]\n"
"ds_read_b64_tr_b16 v[72:73],v103 offset:49152\n"
"s_mov_b32 s90,0\n"
"v_mfma_f32_16x16x32_bf16 v[144:147],v[84:87],v[4:7],v[144:147]\n"
"ds_read_b64_tr_b16 v[74:75],v103 offset:50176\n"
"s_mov_b32 s16,0xc21c0000\n"
"v_mfma_f32_16x16x32_bf16 v[176:179],v[84:87],v[12:15],v[176:179]\n"
"ds_read_b64_tr_b16 v[76:77],v103 offset:57344\n"
"s_mov_b32 s17,0x41a00000\n"
"v_mfma_f32_16x16x32_bf16 v[176:179],v[80:83],v[8:11],v[176:179]\n"
"ds_read_b64_tr_b16 v[78:79],v103 offset:58368\n"
"s_mov_b32 s89,0\n"
"v_mfma_f32_16x16x32_bf16 v[208:211],v[80:83],v[16:19],v[208:211]\n"
"s_xor_b32 s82,s78,8192\n"
"v_add_u32 v116,s82,v112\n"
"v_mfma_f32_16x16x32_bf16 v[208:211],v[84:87],v[20:23],v[208:211]\n"
"ds_read_b128 a[96:99],v96 offset:0\n"
"v_add_u32 v117,4096,v112\n"
"v_mfma_f32_16x16x32_bf16 v[240:243],v[84:87],v[28:31],v[240:243]\n"
"s_getpc_b64 s[20:21]\n"
".Lfk_evp:\n"
"s_add_u32 s20,s20,.Lfk_ev-.Lfk_evp\n"
"s_addc_u32 s21,s21,0\n"
"v_mfma_f32_16x16x32_bf16 v[240:243],v[80:83],v[24:27],v[240:243]\n"
"ds_read_b128 a[104:107],v96 offset:1024\n"
"v_mfma_f32_16x16x32_bf16 v[244:247],v[88:91],v[24:27],v[244:247]\n"
"ds_read_b64_tr_b16 v[80:81],v104 offset:49152\n"
"v_mfma_f32_16x16x32_bf16 v[244:247],v[92:95],v[28:31],v[244:247]\n"
"ds_read_b64_tr_b16 v[82:83],v104 offset:50176\n"
"v_mfma_f32_16x16x32_bf16 v[212:215],v[92:95],v[20:23],v[212:215]\n"
"ds_read_b64_tr_b16 v[84:85],v104 offset:57344\n"
"v_mfma_f32_16x16x32_bf16 v[212:215],v[88:91],v[16:19],v[212:215]\n"
"ds_read_b64_tr_b16 v[86:87],v104 offset:58368\n"
"v_mfma_f32_16x16x32_bf16 v[180:183],v[88:91],v[8:11],v[180:183]\n"
"buffer_store_dwordx4 a[156:159],v112,s[24:27],s74 offen offset:0 sc1\n"
"v_mfma_f32_16x16x32_bf16 v[180:183],v[92:95],v[12:15],v[180:183]\n"
"ds_read_b128 a[112:115],v96 offset:2048\n"
"v_mfma_f32_16x16x32_bf16 v[148:151],v[92:95],v[4:7],v[148:151]\n"
"v_mfma_f32_16x16x32_bf16 v[148:151],v[88:91],v[0:3],v[148:151]\n"
"ds_read_b128 a[120:123],v96 offset:3072\n"
"v_mfma_f32_16x16x32_bf16 v[152:155],a[128:131],v[0:3],v[152:155]\n"
"ds_read_b64_tr_b16 v[88:89],v105 offset:49152\n"
"v_mfma_f32_16x16x32_bf16 v[152:155],a[132:135],v[4:7],v[152:155]\n"
"ds_read_b64_tr_b16 v[90:91],v105 offset:50176\n"
"v_mfma_f32_16x16x32_bf16 v[184:187],a[132:135],v[12:15],v[184:187]\n"
"ds_read_b64_tr_b16 v[92:93],v105 offset:57344\n"
"v_mfma_f32_16x16x32_bf16 v[184:187],a[128:131],v[8:11],v[184:187]\n"
"ds_read_b64_tr_b16 v[94:95],v105 offset:58368\n"
"v_mfma_f32_16x16x32_bf16 v[216:219],a[128:131],v[16:19],v[216:219]\n"
"ds_read_b128 a[100:103],v96 offset:16384\n"
"v_mfma_f32_16x16x32_bf16 v[216:219],a[132:135],v[20:23],v[216:219]\n"
"ds_read_b128 a[108:111],v96 offset:17408\n"
"v_add_u32 v113,65536,v112\n"
"v_mfma_f32_16x16x32_bf16 v[248:251],a[132:135],v[28:31],v[248:251]\n"
"ds_read_b128 a[116:119],v96 offset:18432\n"
"v_add_u32 v114,65536,v112\n"
"v_mfma_f32_16x16x32_bf16 v[248:251],a[128:131],v[24:27],v[248:251]\n"
"ds_read_b128 a[124:127],v96 offset:19456\n"
"v_mfma_f32_16x16x32_bf16 v[252:255],a[136:139],v[24:27],v[252:255]\n"
"v_mfma_f32_16x16x32_bf16 v[252:255],a[140:143],v[28:31],v[252:255]\n"
"buffer_store_dwordx4 a[156:159],v112,s[24:27],s74 offen offset:1024 sc1\n"
"v_mfma_f32_16x16x32_bf16 v[220:223],a[140:143],v[20:23],v[220:223]\n"
"s_lshr_b32 s82,s74,1\n"
"s_add_u32 s82,s82,88080384\n"
"v_mfma_f32_16x16x32_bf16 v[220:223],a[136:139],v[16:19],v[220:223]\n"
"buffer_store_dwordx4 a[156:159],v112,s[24:27],s82 offen sc1\n"
"v_mfma_f32_16x16x32_bf16 v[188:191],a[136:139],v[8:11],v[188:191]\n"
"v_mfma_f32_16x16x32_bf16 v[188:191],a[140:143],v[12:15],v[188:191]\n"
"buffer_store_dword a156,off,s[24:27],s72 sc1\n"
"v_mfma_f32_16x16x32_bf16 v[156:159],a[140:143],v[4:7],v[156:159]\n"
"v_mfma_f32_16x16x32_bf16 v[156:159],a[136:139],v[0:3],v[156:159]\n"
"s_waitcnt vmcnt(11) lgkmcnt(0)\n"
"s_barrier\n"
"v_mfma_f32_16x16x32_bf16 v[224:227],v[32:35],a[120:123],v[224:227]\n"
"ds_read_b128 a[64:67],v113 offset:57344\n"
"v_mov_b32 v96,0xc1980000\n"
"v_mfma_f32_16x16x32_bf16 v[224:227],v[36:39],a[124:127],v[224:227]\n"
"ds_read_b128 a[68:71],v113 offset:58368\n"
"v_mov_b32 v97,0xc1980000\n"
"v_mfma_f32_16x16x32_bf16 v[228:231],v[44:47],a[124:127],v[228:231]\n"
"ds_read_b128 a[72:75],v113 offset:59392\n"
"v_mov_b32 v98,0xc1980000\n"
"v_mfma_f32_16x16x32_bf16 v[228:231],v[40:43],a[120:123],v[228:231]\n"
"ds_read_b128 a[76:79],v113 offset:60416\n"
"v_mov_b32 v99,0xc1980000\n"
"v_mfma_f32_16x16x32_bf16 v[232:235],v[48:51],a[120:123],v[232:235]\n"
"ds_read_b128 a[80:83],v113 offset:61440\n"
"v_mov_b32 v100,0xc1980000\n"
"v_mfma_f32_16x16x32_bf16 v[232:235],v[52:55],a[124:127],v[232:235]\n"
"ds_read_b128 a[84:87],v113 offset:62464\n"
"v_mov_b32 v101,0xc1980000\n"
"v_mfma_f32_16x16x32_bf16 v[236:239],v[60:63],a[124:127],v[236:239]\n"
"ds_read_b128 a[88:91],v113 offset:63488\n"
"v_mov_b32 v102,0xc1980000\n"
"v_mfma_f32_16x16x32_bf16 v[236:239],v[56:59],a[120:123],v[236:239]\n"
"ds_read_b128 a[92:95],v113 offset:64512\n"
"v_mov_b32 v103,0xc1980000\n"
"v_mfma_f32_16x16x32_bf16 v[240:243],v[64:67],a[120:123],v[240:243]\n"
"v_mov_b32 v104,0xc1980000\n"
"v_mov_b32 v105,0xc1980000\n"
"v_mfma_f32_16x16x32_bf16 v[240:243],v[68:71],a[124:127],v[240:243]\n"
"ds_write_b32 v124,v125\n"
"v_mfma_f32_16x16x32_bf16 v[244:247],v[76:79],a[124:127],v[244:247]\n"
"v_mov_b32 v107,0xc1980000\n"
"v_mov_b32 v108,0xc1980000\n"
"v_mfma_f32_16x16x32_bf16 v[244:247],v[72:75],a[120:123],v[244:247]\n"
"v_mov_b32 v109,0xc1980000\n"
"v_mov_b32 v110,0xc1980000\n"
"v_mfma_f32_16x16x32_bf16 v[248:251],v[80:83],a[120:123],v[248:251]\n"
"v_mov_b32 v106,0xc1980000\n"
"v_mov_b32 v111,0xc1980000\n"
"v_mfma_f32_16x16x32_bf16 v[248:251],v[84:87],a[124:127],v[248:251]\n"
"v_mfma_f32_16x16x32_bf16 v[252:255],v[92:95],a[124:127],v[252:255]\n"
"v_mfma_f32_16x16x32_bf16 v[252:255],v[88:91],a[120:123],v[252:255]\n"
"v_mfma_f32_16x16x32_bf16 v[128:131],v[32:35],a[96:99],v[128:131]\n"
"v_mfma_f32_16x16x32_bf16 v[128:131],v[36:39],a[100:103],v[128:131]\n"
"v_cvt_pk_bf16_f32 v232,v232,v233\n"
"v_cvt_pk_bf16_f32 v233,v234,v235\n"
"v_mfma_f32_16x16x32_bf16 v[132:135],v[44:47],a[100:103],v[132:135]\n"
"v_cvt_pk_bf16_f32 v224,v224,v225\n"
"v_cvt_pk_bf16_f32 v225,v226,v227\n"
"v_mfma_f32_16x16x32_bf16 v[132:135],v[40:43],a[96:99],v[132:135]\n"
"v_cvt_pk_bf16_f32 v226,v228,v229\n"
"v_cvt_pk_bf16_f32 v227,v230,v231\n"
"v_mfma_f32_16x16x32_bf16 v[136:139],v[48:51],a[96:99],v[136:139]\n"
"ds_write_b128 v116,v[224:227] offset:0\n"
"v_mfma_f32_16x16x32_bf16 v[136:139],v[52:55],a[100:103],v[136:139]\n"
"v_cvt_pk_bf16_f32 v234,v236,v237\n"
"v_cvt_pk_bf16_f32 v235,v238,v239\n"
"v_mfma_f32_16x16x32_bf16 v[140:143],v[60:63],a[100:103],v[140:143]\n"
"ds_write_b128 v116,v[232:235] offset:1024\n"
"v_mfma_f32_16x16x32_bf16 v[140:143],v[56:59],a[96:99],v[140:143]\n"
"v_cvt_pk_bf16_f32 v240,v240,v241\n"
"v_cvt_pk_bf16_f32 v241,v242,v243\n"
"v_mfma_f32_16x16x32_bf16 v[144:147],v[64:67],a[96:99],v[144:147]\n"
"v_cvt_pk_bf16_f32 v242,v244,v245\n"
"v_cvt_pk_bf16_f32 v243,v246,v247\n"
"v_mfma_f32_16x16x32_bf16 v[144:147],v[68:71],a[100:103],v[144:147]\n"
"ds_write_b128 v116,v[240:243] offset:2048\n"
"v_cvt_pk_bf16_f32 v248,v248,v249\n"
"v_mfma_f32_16x16x32_bf16 v[148:151],v[76:79],a[100:103],v[148:151]\n"
"v_cvt_pk_bf16_f32 v249,v250,v251\n"
"v_cvt_pk_bf16_f32 v250,v252,v253\n"
"v_cvt_pk_bf16_f32 v251,v254,v255\n"
"v_mfma_f32_16x16x32_bf16 v[148:151],v[72:75],a[96:99],v[148:151]\n"
"ds_write_b128 v116,v[248:251] offset:3072\n"
"v_mfma_f32_16x16x32_bf16 v[152:155],v[80:83],a[96:99],v[152:155]\n"
"ds_read_b128 a[48:51],v116 offset:0\n"
"ds_read_b128 a[52:55],v116 offset:1024\n"
"v_mfma_f32_16x16x32_bf16 v[152:155],v[84:87],a[100:103],v[152:155]\n"
"ds_read_b128 a[56:59],v116 offset:2048\n"
"ds_read_b128 a[60:63],v116 offset:3072\n"
"v_mfma_f32_16x16x32_bf16 v[156:159],v[92:95],a[100:103],v[156:159]\n"
"v_mfma_f32_16x16x32_bf16 v[156:159],v[88:91],a[96:99],v[156:159]\n"
"v_mfma_f32_16x16x32_bf16 v[160:163],v[32:35],a[104:107],v[160:163]\n"
"v_mfma_f32_16x16x32_bf16 v[160:163],v[36:39],a[108:111],v[160:163]\n"
"v_cvt_pk_bf16_f32 v136,v136,v137\n"
"v_cvt_pk_bf16_f32 v137,v138,v139\n"
"v_mfma_f32_16x16x32_bf16 v[164:167],v[44:47],a[108:111],v[164:167]\n"
"v_cvt_pk_bf16_f32 v128,v128,v129\n"
"v_cvt_pk_bf16_f32 v129,v130,v131\n"
"v_mfma_f32_16x16x32_bf16 v[164:167],v[40:43],a[104:107],v[164:167]\n"
"v_cvt_pk_bf16_f32 v130,v132,v133\n"
"v_cvt_pk_bf16_f32 v131,v134,v135\n"
"v_mfma_f32_16x16x32_bf16 v[168:171],v[48:51],a[104:107],v[168:171]\n"
"ds_write_b128 v116,v[128:131] offset:16384\n"
"v_mfma_f32_16x16x32_bf16 v[168:171],v[52:55],a[108:111],v[168:171]\n"
"v_cvt_pk_bf16_f32 v138,v140,v141\n"
"v_cvt_pk_bf16_f32 v139,v142,v143\n"
"v_mfma_f32_16x16x32_bf16 v[172:175],v[60:63],a[108:111],v[172:175]\n"
"ds_write_b128 v116,v[136:139] offset:17408\n"
"v_mfma_f32_16x16x32_bf16 v[172:175],v[56:59],a[104:107],v[172:175]\n"
"v_cvt_pk_bf16_f32 v144,v144,v145\n"
"v_cvt_pk_bf16_f32 v145,v146,v147\n"
"v_mfma_f32_16x16x32_bf16 v[176:179],v[64:67],a[104:107],v[176:179]\n"
"v_cvt_pk_bf16_f32 v146,v148,v149\n"
"v_cvt_pk_bf16_f32 v147,v150,v151\n"
"v_mfma_f32_16x16x32_bf16 v[176:179],v[68:71],a[108:111],v[176:179]\n"
"ds_write_b128 v116,v[144:147] offset:18432\n"
"v_cvt_pk_bf16_f32 v152,v152,v153\n"
"v_mfma_f32_16x16x32_bf16 v[180:183],v[76:79],a[108:111],v[180:183]\n"
"v_cvt_pk_bf16_f32 v153,v154,v155\n"
"v_cvt_pk_bf16_f32 v154,v156,v157\n"
"v_cvt_pk_bf16_f32 v155,v158,v159\n"
"v_mfma_f32_16x16x32_bf16 v[180:183],v[72:75],a[104:107],v[180:183]\n"
"ds_write_b128 v116,v[152:155] offset:19456\n"
"v_mfma_f32_16x16x32_bf16 v[184:187],v[80:83],a[104:107],v[184:187]\n"
"ds_read_b128 a[0:3],v116 offset:16384\n"
"ds_read_b128 a[4:7],v116 offset:17408\n"
"v_mfma_f32_16x16x32_bf16 v[184:187],v[84:87],a[108:111],v[184:187]\n"
"ds_read_b128 a[8:11],v116 offset:18432\n"
"ds_read_b128 a[12:15],v116 offset:19456\n"
"v_mfma_f32_16x16x32_bf16 v[188:191],v[92:95],a[108:111],v[188:191]\n"
"v_mfma_f32_16x16x32_bf16 v[188:191],v[88:91],a[104:107],v[188:191]\n"
"v_mfma_f32_16x16x32_bf16 v[192:195],v[32:35],a[112:115],v[192:195]\n"
"v_mfma_f32_16x16x32_bf16 v[192:195],v[36:39],a[116:119],v[192:195]\n"
"v_cvt_pk_bf16_f32 v168,v168,v169\n"
"v_cvt_pk_bf16_f32 v169,v170,v171\n"
"v_mfma_f32_16x16x32_bf16 v[196:199],v[44:47],a[116:119],v[196:199]\n"
"v_cvt_pk_bf16_f32 v160,v160,v161\n"
"v_cvt_pk_bf16_f32 v161,v162,v163\n"
"v_mfma_f32_16x16x32_bf16 v[196:199],v[40:43],a[112:115],v[196:199]\n"
"v_cvt_pk_bf16_f32 v162,v164,v165\n"
"v_cvt_pk_bf16_f32 v163,v166,v167\n"
"v_mfma_f32_16x16x32_bf16 v[200:203],v[48:51],a[112:115],v[200:203]\n"
"ds_write_b128 v116,v[160:163] offset:49152\n"
"v_mfma_f32_16x16x32_bf16 v[200:203],v[52:55],a[116:119],v[200:203]\n"
"v_cvt_pk_bf16_f32 v170,v172,v173\n"
"v_cvt_pk_bf16_f32 v171,v174,v175\n"
"v_mfma_f32_16x16x32_bf16 v[204:207],v[60:63],a[116:119],v[204:207]\n"
"ds_write_b128 v116,v[168:171] offset:50176\n"
"v_mfma_f32_16x16x32_bf16 v[204:207],v[56:59],a[112:115],v[204:207]\n"
"v_cvt_pk_bf16_f32 v176,v176,v177\n"
"v_cvt_pk_bf16_f32 v177,v178,v179\n"
"v_mfma_f32_16x16x32_bf16 v[208:211],v[64:67],a[112:115],v[208:211]\n"
"v_cvt_pk_bf16_f32 v178,v180,v181\n"
"v_cvt_pk_bf16_f32 v179,v182,v183\n"
"v_mfma_f32_16x16x32_bf16 v[208:211],v[68:71],a[116:119],v[208:211]\n"
"ds_write_b128 v116,v[176:179] offset:51200\n"
"v_cvt_pk_bf16_f32 v184,v184,v185\n"
"v_mfma_f32_16x16x32_bf16 v[212:215],v[76:79],a[116:119],v[212:215]\n"
"v_cvt_pk_bf16_f32 v185,v186,v187\n"
"v_cvt_pk_bf16_f32 v186,v188,v189\n"
"v_cvt_pk_bf16_f32 v187,v190,v191\n"
"v_mfma_f32_16x16x32_bf16 v[212:215],v[72:75],a[112:115],v[212:215]\n"
"ds_write_b128 v116,v[184:187] offset:52224\n"
"v_mfma_f32_16x16x32_bf16 v[216:219],v[80:83],a[112:115],v[216:219]\n"
"ds_read_b128 a[16:19],v116 offset:49152\n"
"ds_read_b128 a[20:23],v116 offset:50176\n"
"v_mfma_f32_16x16x32_bf16 v[216:219],v[84:87],a[116:119],v[216:219]\n"
"ds_read_b128 a[24:27],v116 offset:51200\n"
"ds_read_b128 a[28:31],v116 offset:52224\n"
"v_mfma_f32_16x16x32_bf16 v[220:223],v[92:95],a[116:119],v[220:223]\n"
"s_lshl2_add_u32 s82,s60,s52\n"
"s_lshr_b32 s82,s82,1\n"
"s_cmp_eq_u32 s82,1\n"
"v_mfma_f32_16x16x32_bf16 v[220:223],v[88:91],a[112:115],v[220:223]\n"
"s_cbranch_scc1 .Lfk_head0\n"
"s_waitcnt lgkmcnt(0)\n"
"s_barrier\n"
"v_mfma_f32_16x16x32_bf16 v[24:27],a[64:67],a[48:51],v[108:111]\n"
"s_andn2_b32 m0,s55,16384\n"
"s_add_u32 s54,s54,s22\n"
"v_cvt_pk_bf16_f32 v200,v200,v201\n"
"v_mfma_f32_16x16x32_bf16 v[24:27],a[68:71],a[52:55],v[24:27]\n"
"buffer_load_dwordx4 v115,s[32:35],s54 offen sc1 lds\n"
"v_mfma_f32_16x16x32_bf16 v[24:27],a[72:75],a[56:59],v[24:27]\n"
"v_cvt_pk_bf16_f32 v192,v192,v193\n"
"v_cvt_pk_bf16_f32 v193,v194,v195\n"
"v_mfma_f32_16x16x32_bf16 v[24:27],a[76:79],a[60:63],v[24:27]\n"
"v_cvt_pk_bf16_f32 v194,v196,v197\n"
"v_cvt_pk_bf16_f32 v195,v198,v199\n"
"v_mfma_f32_16x16x32_bf16 v[28:31],a[92:95],a[60:63],v[108:111]\n"
"ds_write_b128 v116,v[192:195] offset:32768\n"
"v_mfma_f32_16x16x32_bf16 v[28:31],a[88:91],a[56:59],v[28:31]\n"
"v_cvt_pk_bf16_f32 v201,v202,v203\n"
"v_cvt_pk_bf16_f32 v202,v204,v205\n"
"v_cvt_pk_bf16_f32 v203,v206,v207\n"
"v_mfma_f32_16x16x32_bf16 v[28:31],a[84:87],a[52:55],v[28:31]\n"
"ds_write_b128 v116,v[200:203] offset:33792\n"
"v_mfma_f32_16x16x32_bf16 v[28:31],a[80:83],a[48:51],v[28:31]\n"
"v_cvt_pk_bf16_f32 v208,v208,v209\n"
"v_cvt_pk_bf16_f32 v209,v210,v211\n"
"v_mfma_f32_16x16x32_bf16 v[4:7],a[80:83],a[0:3],v[96:99]\n"
"v_cvt_pk_bf16_f32 v210,v212,v213\n"
"v_cvt_pk_bf16_f32 v211,v214,v215\n"
"v_mfma_f32_16x16x32_bf16 v[4:7],a[84:87],a[4:7],v[4:7]\n"
"ds_read_b128 a[96:99],v113 offset:16384\n"
"ds_read_b128 a[100:103],v113 offset:17408\n"
"v_mfma_f32_16x16x32_bf16 v[4:7],a[88:91],a[8:11],v[4:7]\n"
"ds_read_b128 a[104:107],v113 offset:18432\n"
"ds_read_b128 a[108:111],v113 offset:19456\n"
"v_mfma_f32_16x16x32_bf16 v[4:7],a[92:95],a[12:15],v[4:7]\n"
"ds_read_b128 a[112:115],v113 offset:20480\n"
"ds_read_b128 a[116:119],v113 offset:21504\n"
"v_mfma_f32_16x16x32_bf16 v[0:3],a[76:79],a[12:15],v[96:99]\n"
"ds_read_b128 a[120:123],v113 offset:22528\n"
"ds_read_b128 a[124:127],v113 offset:23552\n"
"v_mfma_f32_16x16x32_bf16 v[0:3],a[72:75],a[8:11],v[0:3]\n"
"v_max3_f32 v118,v24,v25,v26\n"
"v_max3_f32 v118,v118,v27,v28\n"
"v_mfma_f32_16x16x32_bf16 v[0:3],a[68:71],a[4:7],v[0:3]\n"
"v_max3_f32 v118,v118,v29,v30\n"
"v_max_f32 v118,v118,v31\n"
"v_mov_b32 v120,v118\n"
"v_mfma_f32_16x16x32_bf16 v[0:3],a[64:67],a[0:3],v[0:3]\n"
"ds_write_b128 v116,v[208:211] offset:34816\n"
"v_mfma_f32_16x16x32_bf16 v[8:11],a[64:67],a[16:19],v[100:103]\n"
"v_cvt_pk_bf16_f32 v216,v216,v217\n"
"v_cvt_pk_bf16_f32 v217,v218,v219\n"
"v_mfma_f32_16x16x32_bf16 v[8:11],a[68:71],a[20:23],v[8:11]\n"
"v_cvt_pk_bf16_f32 v218,v220,v221\n"
"v_cvt_pk_bf16_f32 v219,v222,v223\n"
"v_mfma_f32_16x16x32_bf16 v[8:11],a[72:75],a[24:27],v[8:11]\n"
"ds_write_b128 v116,v[216:219] offset:35840\n"
"ds_read_b128 a[32:35],v116 offset:32768\n"
"v_mfma_f32_16x16x32_bf16 v[8:11],a[76:79],a[28:31],v[8:11]\n"
"v_max3_f32 v119,v0,v1,v2\n"
"v_max3_f32 v119,v119,v3,v4\n"
"v_mfma_f32_16x16x32_bf16 v[12:15],a[92:95],a[28:31],v[100:103]\n"
"v_max3_f32 v119,v119,v5,v6\n"
"v_max_f32 v119,v119,v7\n"
"v_mfma_f32_16x16x32_bf16 v[12:15],a[88:91],a[24:27],v[12:15]\n"
"v_min_f32 v120,v120,v119\n"
"v_max_f32 v118,v118,v119\n"
"v_mfma_f32_16x16x32_bf16 v[12:15],a[84:87],a[20:23],v[12:15]\n"
"ds_read_b128 a[36:39],v116 offset:33792\n"
"ds_read_b128 a[40:43],v116 offset:34816\n"
"v_mfma_f32_16x16x32_bf16 v[12:15],a[80:83],a[16:19],v[12:15]\n"
"ds_read_b128 a[44:47],v116 offset:35840\n"
"s_waitcnt lgkmcnt(0)\n"
"v_mfma_f32_16x16x32_bf16 v[20:23],a[80:83],a[32:35],v[104:107]\n"
"v_mfma_f32_16x16x32_bf16 v[20:23],a[84:87],a[36:39],v[20:23]\n"
"v_mfma_f32_16x16x32_bf16 v[20:23],a[88:91],a[40:43],v[20:23]\n"
"buffer_store_dwordx4 a[224:227],v112,s[24:27],s56 offen offset:0 sc1\n"
"v_mfma_f32_16x16x32_bf16 v[20:23],a[92:95],a[44:47],v[20:23]\n"
"v_max3_f32 v119,v8,v9,v10\n"
"v_mfma_f32_16x16x32_bf16 v[16:19],a[76:79],a[44:47],v[104:107]\n"
"v_max3_f32 v119,v119,v11,v12\n"
"v_max3_f32 v119,v119,v13,v14\n"
"v_mfma_f32_16x16x32_bf16 v[16:19],a[72:75],a[40:43],v[16:19]\n"
"v_max_f32 v119,v119,v15\n"
"v_min_f32 v120,v120,v119\n"
"v_mfma_f32_16x16x32_bf16 v[16:19],a[68:71],a[36:39],v[16:19]\n"
"v_max_f32 v118,v118,v119\n"
"v_mfma_f32_16x16x32_bf16 v[16:19],a[64:67],a[32:35],v[16:19]\n"
"s_nop 7\n"
"v_max3_f32 v119,v16,v17,v18\n"
"v_max3_f32 v119,v119,v19,v20\n"
"v_max3_f32 v119,v119,v21,v22\n"
"v_max_f32 v119,v119,v23\n"
"v_min_f32 v120,v120,v119\n"
"v_max_f32 v118,v118,v119\n"
"v_cmp_gt_f32 s[96:97],s16,v120\n"
"v_cmp_lt_f32 vcc,s93,v118\n"
"s_nop 0\n"
"s_or_b64 s[96:97],s[96:97],vcc\n"
"s_cbranch_scc1 .Lfk_cs_h3\n"
".Lfk_cr_h3:\n"
"s_cmp_ge_u32 s52,2\n"
"s_cbranch_scc1 .Lfk_g0\n"
"s_waitcnt vmcnt(11) lgkmcnt(0)\n"
"s_barrier\n"
"v_mfma_f32_16x16x32_bf16 v[32:35],a[96:99],a[0:3],v[96:99]\n"
"s_add_u32 m0,s55,4096\n"
"v_exp_f32 v0,v0\n"
"v_mfma_f32_16x16x32_bf16 v[32:35],a[100:103],a[4:7],v[32:35]\n"
"s_add_u32 s54,s54,s22\n"
"v_exp_f32 v1,v1\n"
"v_mfma_f32_16x16x32_bf16 v[32:35],a[104:107],a[8:11],v[32:35]\n"
"buffer_load_dwordx4 v115,s[32:35],s54 offen sc1 lds\n"
"v_exp_f32 v2,v2\n"
"v_cvt_pk_bf16_f32 v64,v0,v1\n"
"v_mfma_f32_16x16x32_bf16 v[32:35],a[108:111],a[12:15],v[32:35]\n"
"s_add_u32 m0,s67,16384\n"
"v_exp_f32 v3,v3\n"
"v_mfma_f32_16x16x32_bf16 v[36:39],a[124:127],a[12:15],v[96:99]\n"
"v_exp_f32 v4,v4\n"
"v_cvt_pk_bf16_f32 v65,v2,v3\n"
"v_exp_f32 v5,v5\n"
"v_mfma_f32_16x16x32_bf16 v[36:39],a[120:123],a[8:11],v[36:39]\n"
"buffer_load_dwordx4 v112,s[24:27],s64 offen offset:0 sc1 lds\n"
"v_mfma_f32_16x16x32_bf16 v[36:39],a[116:119],a[4:7],v[36:39]\n"
"v_exp_f32 v6,v6\n"
"v_cvt_pk_bf16_f32 v66,v4,v5\n"
"v_exp_f32 v7,v7\n"
"v_mfma_f32_16x16x32_bf16 v[36:39],a[112:115],a[0:3],v[36:39]\n"
"buffer_load_dwordx4 v112,s[24:27],s64 offen offset:1024 sc1 lds\n"
"v_mfma_f32_16x16x32_bf16 v[44:47],a[112:115],a[16:19],v[100:103]\n"
"v_exp_f32 v8,v8\n"
"v_cvt_pk_bf16_f32 v67,v6,v7\n"
"v_exp_f32 v9,v9\n"
"v_mfma_f32_16x16x32_bf16 v[44:47],a[116:119],a[20:23],v[44:47]\n"
"ds_read_b128 a[64:67],v113 offset:49152\n"
"ds_read_b128 a[68:71],v113 offset:50176\n"
"v_exp_f32 v10,v10\n"
"v_mfma_f32_16x16x32_bf16 v[44:47],a[120:123],a[24:27],v[44:47]\n"
"ds_read_b128 a[72:75],v113 offset:51200\n"
"ds_read_b128 a[76:79],v113 offset:52224\n"
"v_exp_f32 v11,v11\n"
"v_mfma_f32_16x16x32_bf16 v[44:47],a[124:127],a[28:31],v[44:47]\n"
"ds_read_b128 a[80:83],v113 offset:53248\n"
"ds_read_b128 a[84:87],v113 offset:54272\n"
"v_exp_f32 v12,v12\n"
"v_mfma_f32_16x16x32_bf16 v[40:43],a[108:111],a[28:31],v[100:103]\n"
"ds_read_b128 a[88:91],v113 offset:55296\n"
"ds_read_b128 a[92:95],v113 offset:56320\n"
"v_exp_f32 v13,v13\n"
"v_mfma_f32_16x16x32_bf16 v[40:43],a[104:107],a[24:27],v[40:43]\n"
"buffer_load_dwordx4 v112,s[24:27],s64 offen offset:2048 sc1 lds\n"
"v_exp_f32 v14,v14\n"
"v_mfma_f32_16x16x32_bf16 v[40:43],a[100:103],a[20:23],v[40:43]\n"
"buffer_load_dwordx4 v112,s[24:27],s64 offen offset:3072 sc1 lds\n"
"v_mfma_f32_16x16x32_bf16 v[40:43],a[96:99],a[16:19],v[40:43]\n"
"s_add_u32 m0,s67,20480\n"
"v_exp_f32 v15,v15\n"
"v_mfma_f32_16x16x32_bf16 v[48:51],a[96:99],a[32:35],v[104:107]\n"
"buffer_load_dwordx4 v117,s[24:27],s64 offen offset:0 sc1 lds\n"
"buffer_load_dwordx4 v117,s[24:27],s64 offen offset:1024 sc1 lds\n"
"v_exp_f32 v16,v16\n"
"v_exp_f32 v17,v17\n"
"v_mfma_f32_16x16x32_bf16 v[48:51],a[100:103],a[36:39],v[48:51]\n"
"buffer_load_dwordx4 v117,s[24:27],s64 offen offset:2048 sc1 lds\n"
"v_mfma_f32_16x16x32_bf16 v[48:51],a[104:107],a[40:43],v[48:51]\n"
"v_exp_f32 v18,v18\n"
"v_cvt_pk_bf16_f32 v68,v8,v9\n"
"v_cvt_pk_bf16_f32 v69,v10,v11\n"
"v_exp_f32 v19,v19\n"
"v_mfma_f32_16x16x32_bf16 v[48:51],a[108:111],a[44:47],v[48:51]\n"
"buffer_load_dwordx4 v117,s[24:27],s64 offen offset:3072 sc1 lds\n"
"v_mfma_f32_16x16x32_bf16 v[52:55],a[124:127],a[44:47],v[104:107]\n"
"s_add_u32 s64,s64,16384\n"
"v_exp_f32 v20,v20\n"
"v_cvt_pk_bf16_f32 v70,v12,v13\n"
"v_cvt_pk_bf16_f32 v71,v14,v15\n"
"v_mfma_f32_16x16x32_bf16 v[52:55],a[120:123],a[40:43],v[52:55]\n"
"v_exp_f32 v21,v21\n"
"v_cvt_pk_bf16_f32 v72,v16,v17\n"
"v_cvt_pk_bf16_f32 v73,v18,v19\n"
"v_mfma_f32_16x16x32_bf16 v[52:55],a[116:119],a[36:39],v[52:55]\n"
"v_exp_f32 v22,v22\n"
"v_cvt_pk_bf16_f32 v74,v20,v21\n"
"v_mfma_f32_16x16x32_bf16 v[52:55],a[112:115],a[32:35],v[52:55]\n"
"v_exp_f32 v23,v23\n"
"v_mfma_f32_16x16x32_bf16 v[60:63],a[112:115],a[48:51],v[108:111]\n"
"v_exp_f32 v24,v24\n"
"v_cvt_pk_bf16_f32 v75,v22,v23\n"
"v_mfma_f32_16x16x32_bf16 v[60:63],a[116:119],a[52:55],v[60:63]\n"
"v_exp_f32 v25,v25\n"
"v_mfma_f32_16x16x32_bf16 v[60:63],a[120:123],a[56:59],v[60:63]\n"
"v_exp_f32 v26,v26\n"
"v_exp_f32 v27,v27\n"
"v_mfma_f32_16x16x32_bf16 v[60:63],a[124:127],a[60:63],v[60:63]\n"
"buffer_store_dwordx4 a[228:231],v112,s[24:27],s56 offen offset:1024 sc1\n"
"v_mfma_f32_16x16x32_bf16 v[56:59],a[108:111],a[60:63],v[108:111]\n"
"v_exp_f32 v28,v28\n"
"v_cvt_pk_bf16_f32 v77,v26,v27\n"
"v_mfma_f32_16x16x32_bf16 v[56:59],a[104:107],a[56:59],v[56:59]\n"
"v_exp_f32 v29,v29\n"
"v_mfma_f32_16x16x32_bf16 v[56:59],a[100:103],a[52:55],v[56:59]\n"
"v_exp_f32 v30,v30\n"
"v_cvt_pk_bf16_f32 v78,v28,v29\n"
"v_mfma_f32_16x16x32_bf16 v[56:59],a[96:99],a[48:51],v[56:59]\n"
"s_waitcnt vmcnt(17) lgkmcnt(0)\n"
"v_exp_f32 v31,v31\n"
"v_cvt_pk_bf16_f32 v76,v24,v25\n"
"v_cvt_pk_bf16_f32 v79,v30,v31\n"
"s_barrier\n"
"v_mfma_f32_16x16x32_bf16 v[0:3],a[64:67],a[0:3],v[96:99]\n"
"s_add_u32 m0,s55,8192\n"
"v_exp_f32 v122,v32\n"
"v_mfma_f32_16x16x32_bf16 v[0:3],a[68:71],a[4:7],v[0:3]\n"
"s_add_u32 s54,s54,s22\n"
"v_exp_f32 v123,v33\n"
"v_mfma_f32_16x16x32_bf16 v[0:3],a[72:75],a[8:11],v[0:3]\n"
"ds_read_b128 a[96:99],v114 offset:40960\n"
"ds_read_b128 a[100:103],v114 offset:41984\n"
"v_mfma_f32_16x16x32_bf16 v[0:3],a[76:79],a[12:15],v[0:3]\n"
"v_exp_f32 v119,v34\n"
"v_mfma_f32_16x16x32_bf16 v[4:7],a[92:95],a[12:15],v[96:99]\n"
"ds_read_b128 a[104:107],v114 offset:43008\n"
"v_cvt_pk_bf16_f32 v80,v122,v123\n"
"v_mfma_f32_16x16x32_bf16 v[4:7],a[88:91],a[8:11],v[4:7]\n"
"v_exp_f32 v120,v35\n"
"v_mfma_f32_16x16x32_bf16 v[4:7],a[84:87],a[4:7],v[4:7]\n"
"ds_read_b128 a[108:111],v114 offset:44032\n"
"ds_read_b128 a[112:115],v114 offset:45056\n"
"v_mfma_f32_16x16x32_bf16 v[4:7],a[80:83],a[0:3],v[4:7]\n"
"v_exp_f32 v122,v36\n"
"v_mfma_f32_16x16x32_bf16 v[12:15],a[80:83],a[16:19],v[100:103]\n"
"ds_read_b128 a[116:119],v114 offset:46080\n"
"v_cvt_pk_bf16_f32 v81,v119,v120\n"
"v_mfma_f32_16x16x32_bf16 v[12:15],a[84:87],a[20:23],v[12:15]\n"
"v_exp_f32 v123,v37\n"
"v_mfma_f32_16x16x32_bf16 v[12:15],a[88:91],a[24:27],v[12:15]\n"
"ds_read_b128 a[120:123],v114 offset:47104\n"
"ds_read_b128 a[124:127],v114 offset:48128\n"
"v_mfma_f32_16x16x32_bf16 v[12:15],a[92:95],a[28:31],v[12:15]\n"
"v_exp_f32 v119,v38\n"
"v_cvt_pk_bf16_f32 v82,v122,v123\n"
"v_mfma_f32_16x16x32_bf16 v[8:11],a[76:79],a[28:31],v[100:103]\n"
"buffer_store_dwordx4 a[168:171],v112,s[24:27],s3 offen offset:2048 sc1\n"
"v_mfma_f32_16x16x32_bf16 v[8:11],a[72:75],a[24:27],v[8:11]\n"
"v_exp_f32 v120,v39\n"
"v_mfma_f32_16x16x32_bf16 v[8:11],a[68:71],a[20:23],v[8:11]\n"
"buffer_load_dwordx4 v115,s[32:35],s54 offen sc1 lds\n"
"v_mfma_f32_16x16x32_bf16 v[8:11],a[64:67],a[16:19],v[8:11]\n"
"s_add_u32 m0,s67,49152\n"
"v_exp_f32 v122,v40\n"
"v_mfma_f32_16x16x32_bf16 v[16:19],a[64:67],a[32:35],v[104:107]\n"
"v_cvt_pk_bf16_f32 v83,v119,v120\n"
"v_or3_b32 v118,v80,v81,v82\n"
"v_mfma_f32_16x16x32_bf16 v[16:19],a[68:71],a[36:39],v[16:19]\n"
"v_exp_f32 v123,v41\n"
"v_mfma_f32_16x16x32_bf16 v[16:19],a[72:75],a[40:43],v[16:19]\n"
"buffer_load_dwordx4 v112,s[24:27],s64 offen offset:0 sc1 lds\n"
"v_mfma_f32_16x16x32_bf16 v[16:19],a[76:79],a[44:47],v[16:19]\n"
"v_exp_f32 v119,v42\n"
"v_mfma_f32_16x16x32_bf16 v[20:23],a[92:95],a[44:47],v[104:107]\n"
"v_cvt_pk_bf16_f32 v84,v122,v123\n"
"v_or3_b32 v118,v118,v83,v84\n"
"v_mfma_f32_16x16x32_bf16 v[20:23],a[88:91],a[40:43],v[20:23]\n"
"v_exp_f32 v120,v43\n"
"v_mfma_f32_16x16x32_bf16 v[20:23],a[84:87],a[36:39],v[20:23]\n"
"buffer_load_dwordx4 v112,s[24:27],s64 offen offset:1024 sc1 lds\n"
"v_mfma_f32_16x16x32_bf16 v[20:23],a[80:83],a[32:35],v[20:23]\n"
"v_exp_f32 v122,v44\n"
"v_mfma_f32_16x16x32_bf16 v[28:31],a[80:83],a[48:51],v[108:111]\n"
"v_cvt_pk_bf16_f32 v85,v119,v120\n"
"v_mfma_f32_16x16x32_bf16 v[28:31],a[84:87],a[52:55],v[28:31]\n"
"v_exp_f32 v123,v45\n"
"v_mfma_f32_16x16x32_bf16 v[28:31],a[88:91],a[56:59],v[28:31]\n"
"buffer_load_dwordx4 v112,s[24:27],s64 offen offset:2048 sc1 lds\n"
"v_mfma_f32_16x16x32_bf16 v[28:31],a[92:95],a[60:63],v[28:31]\n"
"v_exp_f32 v119,v46\n"
"v_cvt_pk_bf16_f32 v86,v122,v123\n"
"v_mfma_f32_16x16x32_bf16 v[24:27],a[76:79],a[60:63],v[108:111]\n"
"buffer_store_dwordx4 a[172:175],v112,s[24:27],s3 offen offset:3072 sc1\n"
"v_mfma_f32_16x16x32_bf16 v[24:27],a[72:75],a[56:59],v[24:27]\n"
"v_exp_f32 v120,v47\n"
"v_mfma_f32_16x16x32_bf16 v[24:27],a[68:71],a[52:55],v[24:27]\n"
"buffer_load_dwordx4 v112,s[24:27],s64 offen offset:3072 sc1 lds\n"
"v_mfma_f32_16x16x32_bf16 v[24:27],a[64:67],a[48:51],v[24:27]\n"
"s_waitcnt lgkmcnt(0)\n"
"v_exp_f32 v122,v48\n"
"v_mfma_f32_16x16x32_bf16 v[128:131],a[96:99],v[64:67],0\n"
"v_exp_f32 v123,v49\n"
"v_mfma_f32_16x16x32_bf16 v[160:163],a[96:99],v[68:71],0\n"
"ds_read_b128 a[64:67],v113 offset:32768\n"
"v_cvt_pk_bf16_f32 v87,v119,v120\n"
"v_mfma_f32_16x16x32_bf16 v[192:195],a[96:99],v[72:75],0\n"
"s_add_u32 m0,s67,53248\n"
"v_exp_f32 v119,v50\n"
"v_mfma_f32_16x16x32_bf16 v[224:227],a[96:99],v[76:79],0\n"
"ds_read_b128 a[68:71],v113 offset:33792\n"
"v_cvt_pk_bf16_f32 v88,v122,v123\n"
"v_mfma_f32_16x16x32_bf16 a[140:143],a[144:147],v[76:79],0\n"
"v_exp_f32 v120,v51\n"
"v_mfma_f32_16x16x32_bf16 v[228:231],a[100:103],v[76:79],0\n"
"ds_read_b128 a[72:75],v113 offset:34816\n"
"ds_read_b128 a[76:79],v113 offset:35840\n"
"v_mfma_f32_16x16x32_bf16 v[132:135],a[100:103],v[64:67],0\n"
"v_exp_f32 v122,v52\n"
"v_mfma_f32_16x16x32_bf16 v[164:167],a[100:103],v[68:71],0\n"
"ds_read_b128 a[80:83],v113 offset:36864\n"
"v_cvt_pk_bf16_f32 v89,v119,v120\n"
"v_mfma_f32_16x16x32_bf16 v[196:199],a[100:103],v[72:75],0\n"
"v_exp_f32 v123,v53\n"
"v_mfma_f32_16x16x32_bf16 a[136:139],a[144:147],v[72:75],0\n"
"ds_read_b128 a[84:87],v113 offset:37888\n"
"ds_read_b128 a[88:91],v113 offset:38912\n"
"v_mfma_f32_16x16x32_bf16 v[200:203],a[104:107],v[72:75],0\n"
"v_exp_f32 v119,v54\n"
"v_mfma_f32_16x16x32_bf16 v[232:235],a[104:107],v[76:79],0\n"
"ds_read_b128 a[92:95],v113 offset:39936\n"
"v_cvt_pk_bf16_f32 v90,v122,v123\n"
"v_mfma_f32_16x16x32_bf16 v[136:139],a[104:107],v[64:67],0\n"
"v_exp_f32 v120,v55\n"
"v_mfma_f32_16x16x32_bf16 v[168:171],a[104:107],v[68:71],0\n"
"buffer_load_dwordx4 v117,s[24:27],s64 offen offset:0 sc1 lds\n"
"v_mfma_f32_16x16x32_bf16 a[132:135],a[144:147],v[68:71],0\n"
"v_exp_f32 v122,v56\n"
"v_mfma_f32_16x16x32_bf16 v[172:175],a[108:111],v[68:71],0\n"
"v_cvt_pk_bf16_f32 v91,v119,v120\n"
"s_sub_u32 s3,s3,16384\n"
"v_mfma_f32_16x16x32_bf16 v[204:207],a[108:111],v[72:75],0\n"
"v_exp_f32 v123,v57\n"
"v_mfma_f32_16x16x32_bf16 v[236:239],a[108:111],v[76:79],0\n"
"buffer_load_dwordx4 v117,s[24:27],s64 offen offset:1024 sc1 lds\n"
"v_mfma_f32_16x16x32_bf16 v[140:143],a[108:111],v[64:67],0\n"
"v_exp_f32 v119,v58\n"
"v_cvt_pk_bf16_f32 v92,v122,v123\n"
"v_mfma_f32_16x16x32_bf16 a[128:131],a[144:147],v[64:67],0\n"
"buffer_store_dwordx4 a[176:179],v112,s[24:27],s3 offen offset:0 sc1\n"
"v_mfma_f32_16x16x32_bf16 v[144:147],a[112:115],v[64:67],0\n"
"v_exp_f32 v120,v59\n"
"v_mfma_f32_16x16x32_bf16 v[176:179],a[112:115],v[68:71],0\n"
"buffer_load_dwordx4 v117,s[24:27],s64 offen offset:2048 sc1 lds\n"
"v_mfma_f32_16x16x32_bf16 v[208:211],a[112:115],v[72:75],0\n"
"v_exp_f32 v122,v60\n"
"v_mfma_f32_16x16x32_bf16 v[240:243],a[112:115],v[76:79],0\n"
"v_cvt_pk_bf16_f32 v93,v119,v120\n"
"v_or3_b32 v118,v118,v85,v86\n"
"v_mfma_f32_16x16x32_bf16 v[244:247],a[116:119],v[76:79],0\n"
"v_exp_f32 v123,v61\n"
"v_mfma_f32_16x16x32_bf16 v[148:151],a[116:119],v[64:67],0\n"
"buffer_load_dwordx4 v117,s[24:27],s64 offen offset:3072 sc1 lds\n"
"v_mfma_f32_16x16x32_bf16 v[180:183],a[116:119],v[68:71],0\n"
"s_add_u32 s64,s64,16384\n"
"v_exp_f32 v119,v62\n"
"v_cvt_pk_bf16_f32 v94,v122,v123\n"
"v_mfma_f32_16x16x32_bf16 v[212:215],a[116:119],v[72:75],0\n"
"buffer_store_dwordx4 a[232:235],v112,s[24:27],s56 offen offset:2048 sc1\n"
"v_mfma_f32_16x16x32_bf16 v[216:219],a[120:123],v[72:75],0\n"
"v_exp_f32 v120,v63\n"
"v_mfma_f32_16x16x32_bf16 v[248:251],a[120:123],v[76:79],0\n"
"v_cvt_pk_bf16_f32 v95,v119,v120\n"
"v_or3_b32 v118,v118,v87,v88\n"
"v_mfma_f32_16x16x32_bf16 v[152:155],a[120:123],v[64:67],0\n"
"v_or3_b32 v118,v118,v89,v90\n"
"v_or3_b32 v118,v118,v91,v92\n"
"v_mfma_f32_16x16x32_bf16 v[184:187],a[120:123],v[68:71],0\n"
"v_or3_b32 v118,v118,v93,v94\n"
"v_or_b32 v118,v118,v95\n"
"v_mfma_f32_16x16x32_bf16 v[188:191],a[124:127],v[68:71],0\n"
"v_and_b32 v118,0x40004000,v118\n"
"v_cmp_ne_u32 vcc,0,v118\n"
"v_mfma_f32_16x16x32_bf16 v[220:223],a[124:127],v[72:75],0\n"
"v_mfma_f32_16x16x32_bf16 v[252:255],a[124:127],v[76:79],0\n"
"v_mfma_f32_16x16x32_bf16 v[156:159],a[124:127],v[64:67],0\n"
"s_cbranch_vccnz .Lfk_cs_h1\n"
".Lfk_cr_h1:\n"
"s_waitcnt lgkmcnt(0)\n"
"s_barrier\n"
"v_mfma_f32_16x16x32_bf16 v[32:35],a[64:67],a[0:3],v[96:99]\n"
"s_add_u32 m0,s55,12288\n"
"v_exp_f32 v122,v0\n"
"v_mfma_f32_16x16x32_bf16 v[32:35],a[68:71],a[4:7],v[32:35]\n"
"s_add_u32 s54,s54,s22\n"
"v_exp_f32 v123,v1\n"
"v_mfma_f32_16x16x32_bf16 v[32:35],a[72:75],a[8:11],v[32:35]\n"
"ds_read_b128 a[96:99],v114 offset:8192\n"
"ds_read_b128 a[100:103],v114 offset:9216\n"
"v_mfma_f32_16x16x32_bf16 v[32:35],a[76:79],a[12:15],v[32:35]\n"
"v_exp_f32 v119,v2\n"
"v_mfma_f32_16x16x32_bf16 v[36:39],a[92:95],a[12:15],v[96:99]\n"
"ds_read_b128 a[104:107],v114 offset:10240\n"
"v_cvt_pk_bf16_f32 v64,v122,v123\n"
"v_mfma_f32_16x16x32_bf16 v[36:39],a[88:91],a[8:11],v[36:39]\n"
"v_exp_f32 v120,v3\n"
"v_mfma_f32_16x16x32_bf16 v[36:39],a[84:87],a[4:7],v[36:39]\n"
"ds_read_b128 a[108:111],v114 offset:11264\n"
"ds_read_b128 a[112:115],v114 offset:12288\n"
"v_mfma_f32_16x16x32_bf16 v[36:39],a[80:83],a[0:3],v[36:39]\n"
"v_exp_f32 v122,v4\n"
"v_mfma_f32_16x16x32_bf16 v[44:47],a[80:83],a[16:19],v[100:103]\n"
"ds_read_b128 a[116:119],v114 offset:13312\n"
"v_cvt_pk_bf16_f32 v65,v119,v120\n"
"v_mfma_f32_16x16x32_bf16 v[44:47],a[84:87],a[20:23],v[44:47]\n"
"v_exp_f32 v123,v5\n"
"v_mfma_f32_16x16x32_bf16 v[44:47],a[88:91],a[24:27],v[44:47]\n"
"ds_read_b128 a[120:123],v114 offset:14336\n"
"ds_read_b128 a[124:127],v114 offset:15360\n"
"v_mfma_f32_16x16x32_bf16 v[44:47],a[92:95],a[28:31],v[44:47]\n"
"v_exp_f32 v119,v6\n"
"v_cvt_pk_bf16_f32 v66,v122,v123\n"
"v_mfma_f32_16x16x32_bf16 v[40:43],a[76:79],a[28:31],v[100:103]\n"
"buffer_store_dwordx4 a[180:183],v112,s[24:27],s3 offen offset:1024 sc1\n"
"v_mfma_f32_16x16x32_bf16 v[40:43],a[72:75],a[24:27],v[40:43]\n"
"v_exp_f32 v120,v7\n"
"v_mfma_f32_16x16x32_bf16 v[40:43],a[68:71],a[20:23],v[40:43]\n"
"buffer_load_dwordx4 v115,s[32:35],s54 offen sc1 lds\n"
"v_mfma_f32_16x16x32_bf16 v[40:43],a[64:67],a[16:19],v[40:43]\n"
"s_add_u32 m0,s67,32768\n"
"v_exp_f32 v122,v8\n"
"v_mfma_f32_16x16x32_bf16 v[48:51],a[64:67],a[32:35],v[104:107]\n"
"v_cvt_pk_bf16_f32 v67,v119,v120\n"
"v_or3_b32 v118,v64,v65,v66\n"
"v_mfma_f32_16x16x32_bf16 v[48:51],a[68:71],a[36:39],v[48:51]\n"
"v_exp_f32 v123,v9\n"
"v_mfma_f32_16x16x32_bf16 v[48:51],a[72:75],a[40:43],v[48:51]\n"
"buffer_load_dwordx4 v112,s[24:27],s64 offen offset:0 sc1 lds\n"
"v_mfma_f32_16x16x32_bf16 v[48:51],a[76:79],a[44:47],v[48:51]\n"
"v_exp_f32 v119,v10\n"
"v_mfma_f32_16x16x32_bf16 v[52:55],a[92:95],a[44:47],v[104:107]\n"
"v_cvt_pk_bf16_f32 v68,v122,v123\n"
"v_or3_b32 v118,v118,v67,v68\n"
"v_mfma_f32_16x16x32_bf16 v[52:55],a[88:91],a[40:43],v[52:55]\n"
"v_exp_f32 v120,v11\n"
"v_mfma_f32_16x16x32_bf16 v[52:55],a[84:87],a[36:39],v[52:55]\n"
"buffer_load_dwordx4 v112,s[24:27],s64 offen offset:1024 sc1 lds\n"
"v_mfma_f32_16x16x32_bf16 v[52:55],a[80:83],a[32:35],v[52:55]\n"
"v_exp_f32 v122,v12\n"
"v_mfma_f32_16x16x32_bf16 v[60:63],a[80:83],a[48:51],v[108:111]\n"
"v_cvt_pk_bf16_f32 v69,v119,v120\n"
"v_mfma_f32_16x16x32_bf16 v[60:63],a[84:87],a[52:55],v[60:63]\n"
"v_exp_f32 v123,v13\n"
"v_mfma_f32_16x16x32_bf16 v[60:63],a[88:91],a[56:59],v[60:63]\n"
"buffer_load_dwordx4 v112,s[24:27],s64 offen offset:2048 sc1 lds\n"
"v_mfma_f32_16x16x32_bf16 v[60:63],a[92:95],a[60:63],v[60:63]\n"
"v_exp_f32 v119,v14\n"
"v_cvt_pk_bf16_f32 v70,v122,v123\n"
"v_mfma_f32_16x16x32_bf16 v[56:59],a[76:79],a[60:63],v[108:111]\n"
"buffer_store_dwordx4 a[184:187],v112,s[24:27],s3 offen offset:2048 sc1\n"
"v_mfma_f32_16x16x32_bf16 v[56:59],a[72:75],a[56:59],v[56:59]\n"
"v_exp_f32 v120,v15\n"
"v_mfma_f32_16x16x32_bf16 v[56:59],a[68:71],a[52:55],v[56:59]\n"
"buffer_load_dwordx4 v112,s[24:27],s64 offen offset:3072 sc1 lds\n"
"v_mfma_f32_16x16x32_bf16 v[56:59],a[64:67],a[48:51],v[56:59]\n"
"s_waitcnt lgkmcnt(0)\n"
"v_exp_f32 v122,v16\n"
"v_mfma_f32_16x16x32_bf16 v[128:131],a[96:99],v[80:83],v[128:131]\n"
"v_exp_f32 v123,v17\n"
"v_mfma_f32_16x16x32_bf16 v[160:163],a[96:99],v[84:87],v[160:163]\n"
"ds_read_b128 a[64:67],v113 offset:0\n"
"v_cvt_pk_bf16_f32 v71,v119,v120\n"
"v_mfma_f32_16x16x32_bf16 v[192:195],a[96:99],v[88:91],v[192:195]\n"
"s_add_u32 m0,s67,36864\n"
"v_exp_f32 v119,v18\n"
"v_mfma_f32_16x16x32_bf16 v[224:227],a[96:99],v[92:95],v[224:227]\n"
"ds_read_b128 a[68:71],v113 offset:1024\n"
"v_cvt_pk_bf16_f32 v72,v122,v123\n"
"v_mfma_f32_16x16x32_bf16 a[140:143],a[144:147],v[92:95],a[140:143]\n"
"v_exp_f32 v120,v19\n"
"v_mfma_f32_16x16x32_bf16 v[228:231],a[100:103],v[92:95],v[228:231]\n"
"ds_read_b128 a[72:75],v113 offset:2048\n"
"ds_read_b128 a[76:79],v113 offset:3072\n"
"v_mfma_f32_16x16x32_bf16 v[132:135],a[100:103],v[80:83],v[132:135]\n"
"v_exp_f32 v122,v20\n"
"v_mfma_f32_16x16x32_bf16 v[164:167],a[100:103],v[84:87],v[164:167]\n"
"ds_read_b128 a[80:83],v113 offset:4096\n"
"v_cvt_pk_bf16_f32 v73,v119,v120\n"
"v_mfma_f32_16x16x32_bf16 v[196:199],a[100:103],v[88:91],v[196:199]\n"
"v_exp_f32 v123,v21\n"
"v_mfma_f32_16x16x32_bf16 a[136:139],a[144:147],v[88:91],a[136:139]\n"
"ds_read_b128 a[84:87],v113 offset:5120\n"
"ds_read_b128 a[88:91],v113 offset:6144\n"
"v_mfma_f32_16x16x32_bf16 v[200:203],a[104:107],v[88:91],v[200:203]\n"
"v_exp_f32 v119,v22\n"
"v_mfma_f32_16x16x32_bf16 v[232:235],a[104:107],v[92:95],v[232:235]\n"
"ds_read_b128 a[92:95],v113 offset:7168\n"
"v_cvt_pk_bf16_f32 v74,v122,v123\n"
"v_mfma_f32_16x16x32_bf16 v[136:139],a[104:107],v[80:83],v[136:139]\n"
"v_exp_f32 v120,v23\n"
"v_mfma_f32_16x16x32_bf16 v[168:171],a[104:107],v[84:87],v[168:171]\n"
"buffer_load_dwordx4 v117,s[24:27],s64 offen offset:0 sc1 lds\n"
"v_mfma_f32_16x16x32_bf16 a[132:135],a[144:147],v[84:87],a[132:135]\n"
"v_exp_f32 v122,v24\n"
"v_cvt_pk_bf16_f32 v75,v119,v120\n"
"v_mfma_f32_16x16x32_bf16 v[172:175],a[108:111],v[84:87],v[172:175]\n"
"buffer_store_dwordx4 a[188:191],v112,s[24:27],s3 offen offset:3072 sc1\n"
"v_mfma_f32_16x16x32_bf16 v[204:207],a[108:111],v[88:91],v[204:207]\n"
"v_exp_f32 v123,v25\n"
"v_mfma_f32_16x16x32_bf16 v[236:239],a[108:111],v[92:95],v[236:239]\n"
"buffer_load_dwordx4 v117,s[24:27],s64 offen offset:1024 sc1 lds\n"
"v_mfma_f32_16x16x32_bf16 v[140:143],a[108:111],v[80:83],v[140:143]\n"
"v_exp_f32 v119,v26\n"
"v_mfma_f32_16x16x32_bf16 a[128:131],a[144:147],v[80:83],a[128:131]\n"
"v_cvt_pk_bf16_f32 v76,v122,v123\n"
"s_or_b32 s3,s3,0x3c000\n"
"v_mfma_f32_16x16x32_bf16 v[144:147],a[112:115],v[80:83],v[144:147]\n"
"v_exp_f32 v120,v27\n"
"v_mfma_f32_16x16x32_bf16 v[176:179],a[112:115],v[84:87],v[176:179]\n"
"buffer_load_dwordx4 v117,s[24:27],s64 offen offset:2048 sc1 lds\n"
"v_mfma_f32_16x16x32_bf16 v[208:211],a[112:115],v[88:91],v[208:211]\n"
"v_exp_f32 v122,v28\n"
"v_mfma_f32_16x16x32_bf16 v[240:243],a[112:115],v[92:95],v[240:243]\n"
"v_cvt_pk_bf16_f32 v77,v119,v120\n"
"v_or3_b32 v118,v118,v69,v70\n"
"v_mfma_f32_16x16x32_bf16 v[244:247],a[116:119],v[92:95],v[244:247]\n"
"v_exp_f32 v123,v29\n"
"v_mfma_f32_16x16x32_bf16 v[148:151],a[116:119],v[80:83],v[148:151]\n"
"buffer_load_dwordx4 v117,s[24:27],s64 offen offset:3072 sc1 lds\n"
"v_mfma_f32_16x16x32_bf16 v[180:183],a[116:119],v[84:87],v[180:183]\n"
"s_add_u32 s64,s64,16384\n"
"v_exp_f32 v119,v30\n"
"v_cvt_pk_bf16_f32 v78,v122,v123\n"
"v_mfma_f32_16x16x32_bf16 v[212:215],a[116:119],v[88:91],v[212:215]\n"
"buffer_store_dwordx4 a[236:239],v112,s[24:27],s56 offen offset:3072 sc1\n"
"v_mfma_f32_16x16x32_bf16 v[216:219],a[120:123],v[88:91],v[216:219]\n"
"v_exp_f32 v120,v31\n"
"v_mfma_f32_16x16x32_bf16 v[248:251],a[120:123],v[92:95],v[248:251]\n"
"v_cvt_pk_bf16_f32 v79,v119,v120\n"
"v_or3_b32 v118,v118,v71,v72\n"
"v_mfma_f32_16x16x32_bf16 v[152:155],a[120:123],v[80:83],v[152:155]\n"
"v_or3_b32 v118,v118,v73,v74\n"
"v_or3_b32 v118,v118,v75,v76\n"
"v_mfma_f32_16x16x32_bf16 v[184:187],a[120:123],v[84:87],v[184:187]\n"
"v_or3_b32 v118,v118,v77,v78\n"
"v_or_b32 v118,v118,v79\n"
"v_mfma_f32_16x16x32_bf16 v[188:191],a[124:127],v[84:87],v[188:191]\n"
"v_and_b32 v118,0x40004000,v118\n"
"v_cmp_ne_u32 vcc,0,v118\n"
"v_mfma_f32_16x16x32_bf16 v[220:223],a[124:127],v[88:91],v[220:223]\n"
"v_mfma_f32_16x16x32_bf16 v[252:255],a[124:127],v[92:95],v[252:255]\n"
"v_mfma_f32_16x16x32_bf16 v[156:159],a[124:127],v[80:83],v[156:159]\n"
"s_cbranch_vccnz .Lfk_cs_h2\n"
".Lfk_cr_h2:\n"
"s_waitcnt vmcnt(27) lgkmcnt(0)\n"
"s_barrier\n"
".Lfk_g1:\n"
"s_and_b32 s82,s78,4096\n"
"s_lshl1_add_u32 s82,s82,69632\n"
"s_cmp_lt_u32 s52,2\n"
"s_cselect_b32 s64,s64,s62\n"
"s_cselect_b32 s67,s67,s82\n"
"s_add_u32 s89,s89,1\n"
"s_cmp_ge_u32 s89,s60\n"
"s_cbranch_scc1 .Lfk_Fx\n"
".Lfk_F:\n"
"v_mfma_f32_16x16x32_bf16 v[0:3],a[64:67],a[0:3],v[96:99]\n"
"s_swappc_b64 s[94:95],s[20:21]\n"
"v_exp_f32 v122,v32\n"
"v_mfma_f32_16x16x32_bf16 v[0:3],a[68:71],a[4:7],v[0:3]\n"
"v_exp_f32 v123,v33\n"
"v_mfma_f32_16x16x32_bf16 v[0:3],a[72:75],a[8:11],v[0:3]\n"
"ds_read_b128 a[96:99],v114 offset:24576\n"
"ds_read_b128 a[100:103],v114 offset:25600\n"
"v_mfma_f32_16x16x32_bf16 v[0:3],a[76:79],a[12:15],v[0:3]\n"
"v_exp_f32 v119,v34\n"
"v_mfma_f32_16x16x32_bf16 v[4:7],a[92:95],a[12:15],v[96:99]\n"
"s_add_u32 s101,s101,4\n"
"ds_read_b128 a[104:107],v114 offset:26624\n"
"v_cvt_pk_bf16_f32 v80,v122,v123\n"
"v_mfma_f32_16x16x32_bf16 v[4:7],a[88:91],a[8:11],v[4:7]\n"
"v_exp_f32 v120,v35\n"
"v_mfma_f32_16x16x32_bf16 v[4:7],a[84:87],a[4:7],v[4:7]\n"
"ds_read_b128 a[108:111],v114 offset:27648\n"
"ds_read_b128 a[112:115],v114 offset:28672\n"
"v_mfma_f32_16x16x32_bf16 v[4:7],a[80:83],a[0:3],v[4:7]\n"
"v_exp_f32 v122,v36\n"
"v_mfma_f32_16x16x32_bf16 v[12:15],a[80:83],a[16:19],v[100:103]\n"
"ds_read_b128 a[116:119],v114 offset:29696\n"
"v_cvt_pk_bf16_f32 v81,v119,v120\n"
"v_mfma_f32_16x16x32_bf16 v[12:15],a[84:87],a[20:23],v[12:15]\n"
"v_exp_f32 v123,v37\n"
"v_mfma_f32_16x16x32_bf16 v[12:15],a[88:91],a[24:27],v[12:15]\n"
"ds_read_b128 a[120:123],v114 offset:30720\n"
"ds_read_b128 a[124:127],v114 offset:31744\n"
"v_mfma_f32_16x16x32_bf16 v[12:15],a[92:95],a[28:31],v[12:15]\n"
"s_add_u32 m0,s67,0\n"
"v_exp_f32 v119,v38\n"
"v_mfma_f32_16x16x32_bf16 v[8:11],a[76:79],a[28:31],v[100:103]\n"
"v_cvt_pk_bf16_f32 v82,v122,v123\n"
"v_or3_b32 v118,v80,v81,v82\n"
"v_mfma_f32_16x16x32_bf16 v[8:11],a[72:75],a[24:27],v[8:11]\n"
"v_exp_f32 v120,v39\n"
"v_mfma_f32_16x16x32_bf16 v[8:11],a[68:71],a[20:23],v[8:11]\n"
"buffer_load_dwordx4 v112,s[24:27],s64 offen offset:0 sc1 lds\n"
"v_mfma_f32_16x16x32_bf16 v[8:11],a[64:67],a[16:19],v[8:11]\n"
"v_exp_f32 v122,v40\n"
"v_mfma_f32_16x16x32_bf16 v[16:19],a[64:67],a[32:35],v[104:107]\n"
"v_cvt_pk_bf16_f32 v83,v119,v120\n"
"v_mfma_f32_16x16x32_bf16 v[16:19],a[68:71],a[36:39],v[16:19]\n"
"v_exp_f32 v123,v41\n"
"v_mfma_f32_16x16x32_bf16 v[16:19],a[72:75],a[40:43],v[16:19]\n"
"buffer_load_dwordx4 v112,s[24:27],s64 offen offset:1024 sc1 lds\n"
"v_mfma_f32_16x16x32_bf16 v[16:19],a[76:79],a[44:47],v[16:19]\n"
"v_exp_f32 v119,v42\n"
"v_mfma_f32_16x16x32_bf16 v[20:23],a[92:95],a[44:47],v[104:107]\n"
"v_cvt_pk_bf16_f32 v84,v122,v123\n"
"v_or3_b32 v118,v118,v83,v84\n"
"v_mfma_f32_16x16x32_bf16 v[20:23],a[88:91],a[40:43],v[20:23]\n"
"v_exp_f32 v120,v43\n"
"v_mfma_f32_16x16x32_bf16 v[20:23],a[84:87],a[36:39],v[20:23]\n"
"v_cvt_pk_bf16_f32 v85,v119,v120\n"
"v_mfma_f32_16x16x32_bf16 v[20:23],a[80:83],a[32:35],v[20:23]\n"
"v_exp_f32 v122,v44\n"
"v_mfma_f32_16x16x32_bf16 v[28:31],a[80:83],a[48:51],v[108:111]\n"
"buffer_load_dwordx4 v112,s[24:27],s64 offen offset:2048 sc1 lds\n"
"v_mfma_f32_16x16x32_bf16 v[28:31],a[84:87],a[52:55],v[28:31]\n"
"v_exp_f32 v123,v45\n"
"v_mfma_f32_16x16x32_bf16 v[28:31],a[88:91],a[56:59],v[28:31]\n"
"v_cvt_pk_bf16_f32 v86,v122,v123\n"
"v_or3_b32 v118,v118,v85,v86\n"
"v_mfma_f32_16x16x32_bf16 v[28:31],a[92:95],a[60:63],v[28:31]\n"
"v_exp_f32 v119,v46\n"
"v_mfma_f32_16x16x32_bf16 v[24:27],a[76:79],a[60:63],v[108:111]\n"
"v_exp_f32 v122,v48\n"
"v_mfma_f32_16x16x32_bf16 v[24:27],a[72:75],a[56:59],v[24:27]\n"
"v_exp_f32 v120,v47\n"
"v_mfma_f32_16x16x32_bf16 v[24:27],a[68:71],a[52:55],v[24:27]\n"
"buffer_load_dwordx4 v112,s[24:27],s64 offen offset:3072 sc1 lds\n"
"v_mfma_f32_16x16x32_bf16 v[24:27],a[64:67],a[48:51],v[24:27]\n"
"s_add_u32 s64,s64,16384\n"
"s_waitcnt lgkmcnt(0)\n"
"v_mfma_f32_16x16x32_bf16 v[128:131],a[96:99],v[64:67],v[128:131]\n"
"s_andn2_b32 m0,s55,16384\n"
"v_exp_f32 v123,v49\n"
"v_mfma_f32_16x16x32_bf16 v[160:163],a[96:99],v[68:71],v[160:163]\n"
"ds_read_b128 a[64:67],v113 offset:16384\n"
"v_cvt_pk_bf16_f32 v87,v119,v120\n"
"v_mfma_f32_16x16x32_bf16 v[192:195],a[96:99],v[72:75],v[192:195]\n"
"s_add_u32 s54,s54,s22\n"
"v_exp_f32 v119,v50\n"
"v_mfma_f32_16x16x32_bf16 v[224:227],a[96:99],v[76:79],v[224:227]\n"
"ds_read_b128 a[68:71],v113 offset:17408\n"
"v_cvt_pk_bf16_f32 v88,v122,v123\n"
"v_mfma_f32_16x16x32_bf16 a[140:143],a[144:147],v[76:79],a[140:143]\n"
"v_exp_f32 v120,v51\n"
"v_mfma_f32_16x16x32_bf16 v[228:231],a[100:103],v[76:79],v[228:231]\n"
"ds_read_b128 a[72:75],v113 offset:18432\n"
"ds_read_b128 a[76:79],v113 offset:19456\n"
"v_mfma_f32_16x16x32_bf16 v[132:135],a[100:103],v[64:67],v[132:135]\n"
"v_exp_f32 v122,v52\n"
"v_mfma_f32_16x16x32_bf16 v[164:167],a[100:103],v[68:71],v[164:167]\n"
"ds_read_b128 a[80:83],v113 offset:20480\n"
"v_cvt_pk_bf16_f32 v89,v119,v120\n"
"v_mfma_f32_16x16x32_bf16 v[196:199],a[100:103],v[72:75],v[196:199]\n"
"v_exp_f32 v123,v53\n"
"v_mfma_f32_16x16x32_bf16 a[136:139],a[144:147],v[72:75],a[136:139]\n"
"ds_read_b128 a[84:87],v113 offset:21504\n"
"ds_read_b128 a[88:91],v113 offset:22528\n"
"v_mfma_f32_16x16x32_bf16 v[200:203],a[104:107],v[72:75],v[200:203]\n"
"v_exp_f32 v119,v54\n"
"v_mfma_f32_16x16x32_bf16 v[232:235],a[104:107],v[76:79],v[232:235]\n"
"ds_read_b128 a[92:95],v113 offset:23552\n"
"v_cvt_pk_bf16_f32 v90,v122,v123\n"
"v_mfma_f32_16x16x32_bf16 v[136:139],a[104:107],v[64:67],v[136:139]\n"
"v_exp_f32 v120,v55\n"
"v_mfma_f32_16x16x32_bf16 v[168:171],a[104:107],v[68:71],v[168:171]\n"
"s_waitcnt vmcnt(11)\n"
"ds_add_u32 v124,a152\n"
"v_mfma_f32_16x16x32_bf16 a[132:135],a[144:147],v[68:71],a[132:135]\n"
"v_exp_f32 v122,v56\n"
"v_mfma_f32_16x16x32_bf16 v[172:175],a[108:111],v[68:71],v[172:175]\n"
"v_cvt_pk_bf16_f32 v91,v119,v120\n"
"v_or3_b32 v118,v118,v87,v88\n"
"v_mfma_f32_16x16x32_bf16 v[204:207],a[108:111],v[72:75],v[204:207]\n"
"v_exp_f32 v123,v57\n"
"v_mfma_f32_16x16x32_bf16 v[236:239],a[108:111],v[76:79],v[236:239]\n"
"buffer_load_dwordx4 v115,s[32:35],s54 offen sc1 lds\n"
"v_mfma_f32_16x16x32_bf16 v[140:143],a[108:111],v[64:67],v[140:143]\n"
"v_exp_f32 v119,v58\n"
"v_mfma_f32_16x16x32_bf16 a[128:131],a[144:147],v[64:67],a[128:131]\n"
"v_cvt_pk_bf16_f32 v92,v122,v123\n"
"v_or3_b32 v118,v118,v89,v90\n"
"v_mfma_f32_16x16x32_bf16 v[144:147],a[112:115],v[64:67],v[144:147]\n"
"v_exp_f32 v120,v59\n"
"v_mfma_f32_16x16x32_bf16 v[176:179],a[112:115],v[68:71],v[176:179]\n"
"v_cvt_pk_bf16_f32 v93,v119,v120\n"
"v_or3_b32 v118,v118,v91,v92\n"
"v_mfma_f32_16x16x32_bf16 v[208:211],a[112:115],v[72:75],v[208:211]\n"
"v_exp_f32 v122,v60\n"
"v_mfma_f32_16x16x32_bf16 v[240:243],a[112:115],v[76:79],v[240:243]\n"
"v_mfma_f32_16x16x32_bf16 v[244:247],a[116:119],v[76:79],v[244:247]\n"
"v_exp_f32 v123,v61\n"
"v_mfma_f32_16x16x32_bf16 v[148:151],a[116:119],v[64:67],v[148:151]\n"
"v_cvt_pk_bf16_f32 v94,v122,v123\n"
"v_or3_b32 v118,v118,v93,v94\n"
"v_mfma_f32_16x16x32_bf16 v[180:183],a[116:119],v[68:71],v[180:183]\n"
"v_exp_f32 v119,v62\n"
"v_mfma_f32_16x16x32_bf16 v[212:215],a[116:119],v[72:75],v[212:215]\n"
"ds_read_b32 v125,v124\n"
"buffer_store_dwordx4 a[240:243],v112,s[24:27],s56 offen offset:0 sc1\n"
"v_mfma_f32_16x16x32_bf16 v[216:219],a[120:123],v[72:75],v[216:219]\n"
"v_exp_f32 v120,v63\n"
"v_mfma_f32_16x16x32_bf16 v[248:251],a[120:123],v[76:79],v[248:251]\n"
"v_cvt_pk_bf16_f32 v95,v119,v120\n"
"v_mfma_f32_16x16x32_bf16 v[152:155],a[120:123],v[64:67],v[152:155]\n"
"v_mfma_f32_16x16x32_bf16 v[184:187],a[120:123],v[68:71],v[184:187]\n"
"v_mfma_f32_16x16x32_bf16 v[188:191],a[124:127],v[68:71],v[188:191]\n"
"s_waitcnt lgkmcnt(0)\n"
"v_subrev_u32 v125,s101,v125\n"
"v_or3_b32 v125,v118,v95,v125\n"
"v_mfma_f32_16x16x32_bf16 v[220:223],a[124:127],v[72:75],v[220:223]\n"
"v_and_b32 v125,0x40004000,v125\n"
"v_cmp_ne_u32 vcc,0,v125\n"
"v_mfma_f32_16x16x32_bf16 v[252:255],a[124:127],v[76:79],v[252:255]\n"
"v_mfma_f32_16x16x32_bf16 v[156:159],a[124:127],v[64:67],v[156:159]\n"
"s_cbranch_vccnz .Lfk_cs_f3\n"
".Lfk_cr_f3:\n"
"v_mfma_f32_16x16x32_bf16 v[32:35],a[64:67],a[0:3],v[96:99]\n"
"s_add_u32 s101,s101,4\n"
"v_exp_f32 v122,v0\n"
"v_mfma_f32_16x16x32_bf16 v[32:35],a[68:71],a[4:7],v[32:35]\n"
"s_add_u32 m0,s55,4096\n"
"v_exp_f32 v123,v1\n"
"v_mfma_f32_16x16x32_bf16 v[32:35],a[72:75],a[8:11],v[32:35]\n"
"ds_read_b128 a[96:99],v114 offset:57344\n"
"ds_read_b128 a[100:103],v114 offset:58368\n"
"v_mfma_f32_16x16x32_bf16 v[32:35],a[76:79],a[12:15],v[32:35]\n"
"s_add_u32 s54,s54,s22\n"
"v_exp_f32 v119,v2\n"
"v_mfma_f32_16x16x32_bf16 v[36:39],a[92:95],a[12:15],v[96:99]\n"
"ds_read_b128 a[104:107],v114 offset:59392\n"
"v_cvt_pk_bf16_f32 v64,v122,v123\n"
"v_mfma_f32_16x16x32_bf16 v[36:39],a[88:91],a[8:11],v[36:39]\n"
"v_exp_f32 v120,v3\n"
"v_mfma_f32_16x16x32_bf16 v[36:39],a[84:87],a[4:7],v[36:39]\n"
"ds_read_b128 a[108:111],v114 offset:60416\n"
"ds_read_b128 a[112:115],v114 offset:61440\n"
"v_mfma_f32_16x16x32_bf16 v[36:39],a[80:83],a[0:3],v[36:39]\n"
"v_exp_f32 v122,v4\n"
"v_mfma_f32_16x16x32_bf16 v[44:47],a[80:83],a[16:19],v[100:103]\n"
"ds_read_b128 a[116:119],v114 offset:62464\n"
"v_cvt_pk_bf16_f32 v65,v119,v120\n"
"v_mfma_f32_16x16x32_bf16 v[44:47],a[84:87],a[20:23],v[44:47]\n"
"v_exp_f32 v123,v5\n"
"v_mfma_f32_16x16x32_bf16 v[44:47],a[88:91],a[24:27],v[44:47]\n"
"ds_read_b128 a[120:123],v114 offset:63488\n"
"ds_read_b128 a[124:127],v114 offset:64512\n"
"v_mfma_f32_16x16x32_bf16 v[44:47],a[92:95],a[28:31],v[44:47]\n"
"v_exp_f32 v119,v6\n"
"v_mfma_f32_16x16x32_bf16 v[40:43],a[76:79],a[28:31],v[100:103]\n"
"v_cvt_pk_bf16_f32 v66,v122,v123\n"
"v_or3_b32 v118,v64,v65,v66\n"
"v_mfma_f32_16x16x32_bf16 v[40:43],a[72:75],a[24:27],v[40:43]\n"
"v_exp_f32 v120,v7\n"
"v_mfma_f32_16x16x32_bf16 v[40:43],a[68:71],a[20:23],v[40:43]\n"
"buffer_load_dwordx4 v115,s[32:35],s54 offen sc1 lds\n"
"v_mfma_f32_16x16x32_bf16 v[40:43],a[64:67],a[16:19],v[40:43]\n"
"s_add_u32 m0,s67,16384\n"
"v_exp_f32 v122,v8\n"
"v_mfma_f32_16x16x32_bf16 v[48:51],a[64:67],a[32:35],v[104:107]\n"
"v_cvt_pk_bf16_f32 v67,v119,v120\n"
"v_mfma_f32_16x16x32_bf16 v[48:51],a[68:71],a[36:39],v[48:51]\n"
"v_exp_f32 v123,v9\n"
"v_mfma_f32_16x16x32_bf16 v[48:51],a[72:75],a[40:43],v[48:51]\n"
"buffer_load_dwordx4 v112,s[24:27],s64 offen offset:0 sc1 lds\n"
"v_mfma_f32_16x16x32_bf16 v[48:51],a[76:79],a[44:47],v[48:51]\n"
"v_exp_f32 v119,v10\n"
"v_mfma_f32_16x16x32_bf16 v[52:55],a[92:95],a[44:47],v[104:107]\n"
"v_cvt_pk_bf16_f32 v68,v122,v123\n"
"v_or3_b32 v118,v118,v67,v68\n"
"v_mfma_f32_16x16x32_bf16 v[52:55],a[88:91],a[40:43],v[52:55]\n"
"v_exp_f32 v120,v11\n"
"v_mfma_f32_16x16x32_bf16 v[52:55],a[84:87],a[36:39],v[52:55]\n"
"buffer_load_dwordx4 v112,s[24:27],s64 offen offset:1024 sc1 lds\n"
"v_mfma_f32_16x16x32_bf16 v[52:55],a[80:83],a[32:35],v[52:55]\n"
"v_exp_f32 v122,v12\n"
"v_mfma_f32_16x16x32_bf16 v[60:63],a[80:83],a[48:51],v[108:111]\n"
"v_cvt_pk_bf16_f32 v69,v119,v120\n"
"v_mfma_f32_16x16x32_bf16 v[60:63],a[84:87],a[52:55],v[60:63]\n"
"v_exp_f32 v123,v13\n"
"v_mfma_f32_16x16x32_bf16 v[60:63],a[88:91],a[56:59],v[60:63]\n"
"buffer_load_dwordx4 v112,s[24:27],s64 offen offset:2048 sc1 lds\n"
"v_mfma_f32_16x16x32_bf16 v[60:63],a[92:95],a[60:63],v[60:63]\n"
"v_exp_f32 v119,v14\n"
"v_mfma_f32_16x16x32_bf16 v[56:59],a[76:79],a[60:63],v[108:111]\n"
"v_cvt_pk_bf16_f32 v70,v122,v123\n"
"v_or3_b32 v118,v118,v69,v70\n"
"v_mfma_f32_16x16x32_bf16 v[56:59],a[72:75],a[56:59],v[56:59]\n"
"v_exp_f32 v120,v15\n"
"v_mfma_f32_16x16x32_bf16 v[56:59],a[68:71],a[52:55],v[56:59]\n"
"buffer_load_dwordx4 v112,s[24:27],s64 offen offset:3072 sc1 lds\n"
"v_mfma_f32_16x16x32_bf16 v[56:59],a[64:67],a[48:51],v[56:59]\n"
"s_waitcnt lgkmcnt(0)\n"
"v_exp_f32 v122,v16\n"
"v_mfma_f32_16x16x32_bf16 v[128:131],a[96:99],v[80:83],v[128:131]\n"
"v_exp_f32 v123,v17\n"
"v_mfma_f32_16x16x32_bf16 v[160:163],a[96:99],v[84:87],v[160:163]\n"
"s_add_u32 s64,s64,16384\n"
"ds_read_b128 a[64:67],v113 offset:49152\n"
"v_cvt_pk_bf16_f32 v71,v119,v120\n"
"v_mfma_f32_16x16x32_bf16 v[192:195],a[96:99],v[88:91],v[192:195]\n"
"v_exp_f32 v119,v18\n"
"v_mfma_f32_16x16x32_bf16 v[224:227],a[96:99],v[92:95],v[224:227]\n"
"ds_read_b128 a[68:71],v113 offset:50176\n"
"v_cvt_pk_bf16_f32 v72,v122,v123\n"
"v_mfma_f32_16x16x32_bf16 a[140:143],a[144:147],v[92:95],a[140:143]\n"
"v_exp_f32 v120,v19\n"
"v_mfma_f32_16x16x32_bf16 v[228:231],a[100:103],v[92:95],v[228:231]\n"
"ds_read_b128 a[72:75],v113 offset:51200\n"
"ds_read_b128 a[76:79],v113 offset:52224\n"
"v_mfma_f32_16x16x32_bf16 v[132:135],a[100:103],v[80:83],v[132:135]\n"
"v_exp_f32 v122,v20\n"
"v_mfma_f32_16x16x32_bf16 v[164:167],a[100:103],v[84:87],v[164:167]\n"
"ds_read_b128 a[80:83],v113 offset:53248\n"
"v_cvt_pk_bf16_f32 v73,v119,v120\n"
"v_mfma_f32_16x16x32_bf16 v[196:199],a[100:103],v[88:91],v[196:199]\n"
"v_exp_f32 v123,v21\n"
"v_mfma_f32_16x16x32_bf16 a[136:139],a[144:147],v[88:91],a[136:139]\n"
"ds_read_b128 a[84:87],v113 offset:54272\n"
"ds_read_b128 a[88:91],v113 offset:55296\n"
"v_mfma_f32_16x16x32_bf16 v[200:203],a[104:107],v[88:91],v[200:203]\n"
"v_exp_f32 v119,v22\n"
"v_mfma_f32_16x16x32_bf16 v[232:235],a[104:107],v[92:95],v[232:235]\n"
"ds_read_b128 a[92:95],v113 offset:56320\n"
"v_cvt_pk_bf16_f32 v74,v122,v123\n"
"v_mfma_f32_16x16x32_bf16 v[136:139],a[104:107],v[80:83],v[136:139]\n"
"v_exp_f32 v120,v23\n"
"v_mfma_f32_16x16x32_bf16 v[168:171],a[104:107],v[84:87],v[168:171]\n"
"s_waitcnt vmcnt(12)\n"
"ds_add_u32 v124,a152\n"
"v_mfma_f32_16x16x32_bf16 a[132:135],a[144:147],v[84:87],a[132:135]\n"
"v_exp_f32 v122,v24\n"
"v_mfma_f32_16x16x32_bf16 v[172:175],a[108:111],v[84:87],v[172:175]\n"
"v_cvt_pk_bf16_f32 v75,v119,v120\n"
"v_or3_b32 v118,v118,v71,v72\n"
"v_mfma_f32_16x16x32_bf16 v[204:207],a[108:111],v[88:91],v[204:207]\n"
"v_exp_f32 v123,v25\n"
"v_mfma_f32_16x16x32_bf16 v[236:239],a[108:111],v[92:95],v[236:239]\n"
"v_cvt_pk_bf16_f32 v76,v122,v123\n"
"v_or3_b32 v118,v118,v73,v74\n"
"v_mfma_f32_16x16x32_bf16 v[140:143],a[108:111],v[80:83],v[140:143]\n"
"v_exp_f32 v119,v26\n"
"v_mfma_f32_16x16x32_bf16 a[128:131],a[144:147],v[80:83],a[128:131]\n"
"v_or3_b32 v118,v118,v75,v76\n"
"v_mfma_f32_16x16x32_bf16 v[144:147],a[112:115],v[80:83],v[144:147]\n"
"v_exp_f32 v120,v27\n"
"v_mfma_f32_16x16x32_bf16 v[176:179],a[112:115],v[84:87],v[176:179]\n"
"v_cvt_pk_bf16_f32 v77,v119,v120\n"
"v_mfma_f32_16x16x32_bf16 v[208:211],a[112:115],v[88:91],v[208:211]\n"
"v_exp_f32 v122,v28\n"
"v_mfma_f32_16x16x32_bf16 v[240:243],a[112:115],v[92:95],v[240:243]\n"
"v_mfma_f32_16x16x32_bf16 v[244:247],a[116:119],v[92:95],v[244:247]\n"
"v_exp_f32 v123,v29\n"
"v_mfma_f32_16x16x32_bf16 v[148:151],a[116:119],v[80:83],v[148:151]\n"
"v_cvt_pk_bf16_f32 v78,v122,v123\n"
"v_or3_b32 v118,v118,v77,v78\n"
"v_mfma_f32_16x16x32_bf16 v[180:183],a[116:119],v[84:87],v[180:183]\n"
"v_exp_f32 v119,v30\n"
"v_mfma_f32_16x16x32_bf16 v[212:215],a[116:119],v[88:91],v[212:215]\n"
"ds_read_b32 v125,v124\n"
"buffer_store_dwordx4 a[244:247],v112,s[24:27],s56 offen offset:1024 sc1\n"
"v_mfma_f32_16x16x32_bf16 v[216:219],a[120:123],v[88:91],v[216:219]\n"
"v_exp_f32 v120,v31\n"
"v_mfma_f32_16x16x32_bf16 v[248:251],a[120:123],v[92:95],v[248:251]\n"
"v_cvt_pk_bf16_f32 v79,v119,v120\n"
"v_mfma_f32_16x16x32_bf16 v[152:155],a[120:123],v[80:83],v[152:155]\n"
"v_mfma_f32_16x16x32_bf16 v[184:187],a[120:123],v[84:87],v[184:187]\n"
"v_mfma_f32_16x16x32_bf16 v[188:191],a[124:127],v[84:87],v[188:191]\n"
"s_waitcnt lgkmcnt(0)\n"
"v_subrev_u32 v125,s101,v125\n"
"v_or3_b32 v125,v118,v79,v125\n"
"v_mfma_f32_16x16x32_bf16 v[220:223],a[124:127],v[88:91],v[220:223]\n"
"v_and_b32 v125,0x40004000,v125\n"
"v_cmp_ne_u32 vcc,0,v125\n"
"v_mfma_f32_16x16x32_bf16 v[252:255],a[124:127],v[92:95],v[252:255]\n"
"v_mfma_f32_16x16x32_bf16 v[156:159],a[124:127],v[80:83],v[156:159]\n"
"s_cbranch_vccnz .Lfk_cs_f0\n"
".Lfk_cr_f0:\n"
"v_mfma_f32_16x16x32_bf16 v[0:3],a[64:67],a[0:3],v[96:99]\n"
"s_add_u32 s101,s101,4\n"
"v_exp_f32 v122,v32\n"
"v_mfma_f32_16x16x32_bf16 v[0:3],a[68:71],a[4:7],v[0:3]\n"
"s_add_u32 m0,s55,8192\n"
"v_exp_f32 v123,v33\n"
"v_mfma_f32_16x16x32_bf16 v[0:3],a[72:75],a[8:11],v[0:3]\n"
"ds_read_b128 a[96:99],v114 offset:40960\n"
"ds_read_b128 a[100:103],v114 offset:41984\n"
"v_mfma_f32_16x16x32_bf16 v[0:3],a[76:79],a[12:15],v[0:3]\n"
"s_add_u32 s54,s54,s22\n"
"v_exp_f32 v119,v34\n"
"v_mfma_f32_16x16x32_bf16 v[4:7],a[92:95],a[12:15],v[96:99]\n"
"ds_read_b128 a[104:107],v114 offset:43008\n"
"v_cvt_pk_bf16_f32 v80,v122,v123\n"
"v_mfma_f32_16x16x32_bf16 v[4:7],a[88:91],a[8:11],v[4:7]\n"
"v_exp_f32 v120,v35\n"
"v_mfma_f32_16x16x32_bf16 v[4:7],a[84:87],a[4:7],v[4:7]\n"
"ds_read_b128 a[108:111],v114 offset:44032\n"
"ds_read_b128 a[112:115],v114 offset:45056\n"
"v_mfma_f32_16x16x32_bf16 v[4:7],a[80:83],a[0:3],v[4:7]\n"
"v_exp_f32 v122,v36\n"
"v_mfma_f32_16x16x32_bf16 v[12:15],a[80:83],a[16:19],v[100:103]\n"
"ds_read_b128 a[116:119],v114 offset:46080\n"
"v_cvt_pk_bf16_f32 v81,v119,v120\n"
"v_mfma_f32_16x16x32_bf16 v[12:15],a[84:87],a[20:23],v[12:15]\n"
"v_exp_f32 v123,v37\n"
"v_mfma_f32_16x16x32_bf16 v[12:15],a[88:91],a[24:27],v[12:15]\n"
"ds_read_b128 a[120:123],v114 offset:47104\n"
"ds_read_b128 a[124:127],v114 offset:48128\n"
"v_mfma_f32_16x16x32_bf16 v[12:15],a[92:95],a[28:31],v[12:15]\n"
"v_exp_f32 v119,v38\n"
"v_mfma_f32_16x16x32_bf16 v[8:11],a[76:79],a[28:31],v[100:103]\n"
"v_cvt_pk_bf16_f32 v82,v122,v123\n"
"v_or3_b32 v118,v80,v81,v82\n"
"v_mfma_f32_16x16x32_bf16 v[8:11],a[72:75],a[24:27],v[8:11]\n"
"v_exp_f32 v120,v39\n"
"v_mfma_f32_16x16x32_bf16 v[8:11],a[68:71],a[20:23],v[8:11]\n"
"buffer_load_dwordx4 v115,s[32:35],s54 offen sc1 lds\n"
"v_mfma_f32_16x16x32_bf16 v[8:11],a[64:67],a[16:19],v[8:11]\n"
"s_add_u32 m0,s67,49152\n"
"v_exp_f32 v122,v40\n"
"v_mfma_f32_16x16x32_bf16 v[16:19],a[64:67],a[32:35],v[104:107]\n"
"v_cvt_pk_bf16_f32 v83,v119,v120\n"
"v_mfma_f32_16x16x32_bf16 v[16:19],a[68:71],a[36:39],v[16:19]\n"
"v_exp_f32 v123,v41\n"
"v_mfma_f32_16x16x32_bf16 v[16:19],a[72:75],a[40:43],v[16:19]\n"
"buffer_load_dwordx4 v112,s[24:27],s64 offen offset:0 sc1 lds\n"
"v_mfma_f32_16x16x32_bf16 v[16:19],a[76:79],a[44:47],v[16:19]\n"
"v_exp_f32 v119,v42\n"
"v_mfma_f32_16x16x32_bf16 v[20:23],a[92:95],a[44:47],v[104:107]\n"
"v_cvt_pk_bf16_f32 v84,v122,v123\n"
"v_or3_b32 v118,v118,v83,v84\n"
"v_mfma_f32_16x16x32_bf16 v[20:23],a[88:91],a[40:43],v[20:23]\n"
"v_exp_f32 v120,v43\n"
"v_mfma_f32_16x16x32_bf16 v[20:23],a[84:87],a[36:39],v[20:23]\n"
"buffer_load_dwordx4 v112,s[24:27],s64 offen offset:1024 sc1 lds\n"
"v_mfma_f32_16x16x32_bf16 v[20:23],a[80:83],a[32:35],v[20:23]\n"
"v_exp_f32 v122,v44\n"
"v_mfma_f32_16x16x32_bf16 v[28:31],a[80:83],a[48:51],v[108:111]\n"
"v_cvt_pk_bf16_f32 v85,v119,v120\n"
"v_mfma_f32_16x16x32_bf16 v[28:31],a[84:87],a[52:55],v[28:31]\n"
"v_exp_f32 v123,v45\n"
"v_mfma_f32_16x16x32_bf16 v[28:31],a[88:91],a[56:59],v[28:31]\n"
"buffer_load_dwordx4 v112,s[24:27],s64 offen offset:2048 sc1 lds\n"
"v_mfma_f32_16x16x32_bf16 v[28:31],a[92:95],a[60:63],v[28:31]\n"
"v_exp_f32 v119,v46\n"
"v_mfma_f32_16x16x32_bf16 v[24:27],a[76:79],a[60:63],v[108:111]\n"
"v_cvt_pk_bf16_f32 v86,v122,v123\n"
"v_or3_b32 v118,v118,v85,v86\n"
"v_mfma_f32_16x16x32_bf16 v[24:27],a[72:75],a[56:59],v[24:27]\n"
"v_exp_f32 v120,v47\n"
"v_mfma_f32_16x16x32_bf16 v[24:27],a[68:71],a[52:55],v[24:27]\n"
"buffer_load_dwordx4 v112,s[24:27],s64 offen offset:3072 sc1 lds\n"
"v_mfma_f32_16x16x32_bf16 v[24:27],a[64:67],a[48:51],v[24:27]\n"
"s_waitcnt lgkmcnt(0)\n"
"v_exp_f32 v122,v48\n"
"v_mfma_f32_16x16x32_bf16 v[128:131],a[96:99],v[64:67],v[128:131]\n"
"v_exp_f32 v123,v49\n"
"v_mfma_f32_16x16x32_bf16 v[160:163],a[96:99],v[68:71],v[160:163]\n"
"s_add_u32 s64,s64,16384\n"
"ds_read_b128 a[64:67],v113 offset:32768\n"
"v_cvt_pk_bf16_f32 v87,v119,v120\n"
"v_mfma_f32_16x16x32_bf16 v[192:195],a[96:99],v[72:75],v[192:195]\n"
"v_exp_f32 v119,v50\n"
"v_mfma_f32_16x16x32_bf16 v[224:227],a[96:99],v[76:79],v[224:227]\n"
"ds_read_b128 a[68:71],v113 offset:33792\n"
"v_cvt_pk_bf16_f32 v88,v122,v123\n"
"v_mfma_f32_16x16x32_bf16 a[140:143],a[144:147],v[76:79],a[140:143]\n"
"v_exp_f32 v120,v51\n"
"v_mfma_f32_16x16x32_bf16 v[228:231],a[100:103],v[76:79],v[228:231]\n"
"ds_read_b128 a[72:75],v113 offset:34816\n"
"ds_read_b128 a[76:79],v113 offset:35840\n"
"v_mfma_f32_16x16x32_bf16 v[132:135],a[100:103],v[64:67],v[132:135]\n"
"v_exp_f32 v122,v52\n"
"v_mfma_f32_16x16x32_bf16 v[164:167],a[100:103],v[68:71],v[164:167]\n"
"ds_read_b128 a[80:83],v113 offset:36864\n"
"v_cvt_pk_bf16_f32 v89,v119,v120\n"
"v_mfma_f32_16x16x32_bf16 v[196:199],a[100:103],v[72:75],v[196:199]\n"
"v_exp_f32 v123,v53\n"
"v_mfma_f32_16x16x32_bf16 a[136:139],a[144:147],v[72:75],a[136:139]\n"
"ds_read_b128 a[84:87],v113 offset:37888\n"
"ds_read_b128 a[88:91],v113 offset:38912\n"
"v_mfma_f32_16x16x32_bf16 v[200:203],a[104:107],v[72:75],v[200:203]\n"
"v_exp_f32 v119,v54\n"
"v_mfma_f32_16x16x32_bf16 v[232:235],a[104:107],v[76:79],v[232:235]\n"
"ds_read_b128 a[92:95],v113 offset:39936\n"
"v_cvt_pk_bf16_f32 v90,v122,v123\n"
"v_mfma_f32_16x16x32_bf16 v[136:139],a[104:107],v[64:67],v[136:139]\n"
"v_exp_f32 v120,v55\n"
"v_mfma_f32_16x16x32_bf16 v[168:171],a[104:107],v[68:71],v[168:171]\n"
"s_waitcnt vmcnt(12)\n"
"ds_add_u32 v124,a152\n"
"v_mfma_f32_16x16x32_bf16 a[132:135],a[144:147],v[68:71],a[132:135]\n"
"v_exp_f32 v122,v56\n"
"v_mfma_f32_16x16x32_bf16 v[172:175],a[108:111],v[68:71],v[172:175]\n"
"v_cvt_pk_bf16_f32 v91,v119,v120\n"
"v_or3_b32 v118,v118,v87,v88\n"
"v_mfma_f32_16x16x32_bf16 v[204:207],a[108:111],v[72:75],v[204:207]\n"
"v_exp_f32 v123,v57\n"
"v_mfma_f32_16x16x32_bf16 v[236:239],a[108:111],v[76:79],v[236:239]\n"
"v_cvt_pk_bf16_f32 v92,v122,v123\n"
"v_or3_b32 v118,v118,v89,v90\n"
"v_mfma_f32_16x16x32_bf16 v[140:143],a[108:111],v[64:67],v[140:143]\n"
"v_exp_f32 v119,v58\n"
"v_mfma_f32_16x16x32_bf16 a[128:131],a[144:147],v[64:67],a[128:131]\n"
"v_or3_b32 v118,v118,v91,v92\n"
"v_mfma_f32_16x16x32_bf16 v[144:147],a[112:115],v[64:67],v[144:147]\n"
"v_exp_f32 v120,v59\n"
"v_mfma_f32_16x16x32_bf16 v[176:179],a[112:115],v[68:71],v[176:179]\n"
"v_cvt_pk_bf16_f32 v93,v119,v120\n"
"v_mfma_f32_16x16x32_bf16 v[208:211],a[112:115],v[72:75],v[208:211]\n"
"v_exp_f32 v122,v60\n"
"v_mfma_f32_16x16x32_bf16 v[240:243],a[112:115],v[76:79],v[240:243]\n"
"v_mfma_f32_16x16x32_bf16 v[244:247],a[116:119],v[76:79],v[244:247]\n"
"v_exp_f32 v123,v61\n"
"v_mfma_f32_16x16x32_bf16 v[148:151],a[116:119],v[64:67],v[148:151]\n"
"v_cvt_pk_bf16_f32 v94,v122,v123\n"
"v_or3_b32 v118,v118,v93,v94\n"
"v_mfma_f32_16x16x32_bf16 v[180:183],a[116:119],v[68:71],v[180:183]\n"
"v_exp_f32 v119,v62\n"
"v_mfma_f32_16x16x32_bf16 v[212:215],a[116:119],v[72:75],v[212:215]\n"
"ds_read_b32 v125,v124\n"
"buffer_store_dwordx4 a[248:251],v112,s[24:27],s56 offen offset:2048 sc1\n"
"v_mfma_f32_16x16x32_bf16 v[216:219],a[120:123],v[72:75],v[216:219]\n"
"v_exp_f32 v120,v63\n"
"v_mfma_f32_16x16x32_bf16 v[248:251],a[120:123],v[76:79],v[248:251]\n"
"v_cvt_pk_bf16_f32 v95,v119,v120\n"
"v_mfma_f32_16x16x32_bf16 v[152:155],a[120:123],v[64:67],v[152:155]\n"
"v_mfma_f32_16x16x32_bf16 v[184:187],a[120:123],v[68:71],v[184:187]\n"
"v_mfma_f32_16x16x32_bf16 v[188:191],a[124:127],v[68:71],v[188:191]\n"
"s_waitcnt lgkmcnt(0)\n"
"v_subrev_u32 v125,s101,v125\n"
"v_or3_b32 v125,v118,v95,v125\n"
"v_mfma_f32_16x16x32_bf16 v[220:223],a[124:127],v[72:75],v[220:223]\n"
"v_and_b32 v125,0x40004000,v125\n"
"v_cmp_ne_u32 vcc,0,v125\n"
"v_mfma_f32_16x16x32_bf16 v[252:255],a[124:127],v[76:79],v[252:255]\n"
"v_mfma_f32_16x16x32_bf16 v[156:159],a[124:127],v[64:67],v[156:159]\n"
"s_cbranch_vccnz .Lfk_cs_f1\n"
".Lfk_cr_f1:\n"
"v_mfma_f32_16x16x32_bf16 v[32:35],a[64:67],a[0:3],v[96:99]\n"
"s_add_u32 s101,s101,4\n"
"v_exp_f32 v122,v0\n"
"v_mfma_f32_16x16x32_bf16 v[32:35],a[68:71],a[4:7],v[32:35]\n"
"s_add_u32 m0,s55,12288\n"
"v_exp_f32 v123,v1\n"
"v_mfma_f32_16x16x32_bf16 v[32:35],a[72:75],a[8:11],v[32:35]\n"
"ds_read_b128 a[96:99],v114 offset:8192\n"
"ds_read_b128 a[100:103],v114 offset:9216\n"
"v_mfma_f32_16x16x32_bf16 v[32:35],a[76:79],a[12:15],v[32:35]\n"
"s_add_u32 s54,s54,s22\n"
"v_exp_f32 v119,v2\n"
"v_mfma_f32_16x16x32_bf16 v[36:39],a[92:95],a[12:15],v[96:99]\n"
"ds_read_b128 a[104:107],v114 offset:10240\n"
"v_cvt_pk_bf16_f32 v64,v122,v123\n"
"v_mfma_f32_16x16x32_bf16 v[36:39],a[88:91],a[8:11],v[36:39]\n"
"v_exp_f32 v120,v3\n"
"v_mfma_f32_16x16x32_bf16 v[36:39],a[84:87],a[4:7],v[36:39]\n"
"ds_read_b128 a[108:111],v114 offset:11264\n"
"ds_read_b128 a[112:115],v114 offset:12288\n"
"v_mfma_f32_16x16x32_bf16 v[36:39],a[80:83],a[0:3],v[36:39]\n"
"v_exp_f32 v122,v4\n"
"v_mfma_f32_16x16x32_bf16 v[44:47],a[80:83],a[16:19],v[100:103]\n"
"ds_read_b128 a[116:119],v114 offset:13312\n"
"v_cvt_pk_bf16_f32 v65,v119,v120\n"
"v_mfma_f32_16x16x32_bf16 v[44:47],a[84:87],a[20:23],v[44:47]\n"
"v_exp_f32 v123,v5\n"
"v_mfma_f32_16x16x32_bf16 v[44:47],a[88:91],a[24:27],v[44:47]\n"
"ds_read_b128 a[120:123],v114 offset:14336\n"
"ds_read_b128 a[124:127],v114 offset:15360\n"
"v_mfma_f32_16x16x32_bf16 v[44:47],a[92:95],a[28:31],v[44:47]\n"
"v_exp_f32 v119,v6\n"
"v_mfma_f32_16x16x32_bf16 v[40:43],a[76:79],a[28:31],v[100:103]\n"
"v_cvt_pk_bf16_f32 v66,v122,v123\n"
"v_or3_b32 v118,v64,v65,v66\n"
"v_mfma_f32_16x16x32_bf16 v[40:43],a[72:75],a[24:27],v[40:43]\n"
"v_exp_f32 v120,v7\n"
"v_mfma_f32_16x16x32_bf16 v[40:43],a[68:71],a[20:23],v[40:43]\n"
"buffer_load_dwordx4 v115,s[32:35],s54 offen sc1 lds\n"
"v_mfma_f32_16x16x32_bf16 v[40:43],a[64:67],a[16:19],v[40:43]\n"
"s_add_u32 m0,s67,32768\n"
"v_exp_f32 v122,v8\n"
"v_mfma_f32_16x16x32_bf16 v[48:51],a[64:67],a[32:35],v[104:107]\n"
"v_cvt_pk_bf16_f32 v67,v119,v120\n"
"v_mfma_f32_16x16x32_bf16 v[48:51],a[68:71],a[36:39],v[48:51]\n"
"v_exp_f32 v123,v9\n"
"v_mfma_f32_16x16x32_bf16 v[48:51],a[72:75],a[40:43],v[48:51]\n"
"buffer_load_dwordx4 v112,s[24:27],s64 offen offset:0 sc1 lds\n"
"v_mfma_f32_16x16x32_bf16 v[48:51],a[76:79],a[44:47],v[48:51]\n"
"v_exp_f32 v119,v10\n"
"v_mfma_f32_16x16x32_bf16 v[52:55],a[92:95],a[44:47],v[104:107]\n"
"v_cvt_pk_bf16_f32 v68,v122,v123\n"
"v_or3_b32 v118,v118,v67,v68\n"
"v_mfma_f32_16x16x32_bf16 v[52:55],a[88:91],a[40:43],v[52:55]\n"
"v_exp_f32 v120,v11\n"
"v_mfma_f32_16x16x32_bf16 v[52:55],a[84:87],a[36:39],v[52:55]\n"
"buffer_load_dwordx4 v112,s[24:27],s64 offen offset:1024 sc1 lds\n"
"v_mfma_f32_16x16x32_bf16 v[52:55],a[80:83],a[32:35],v[52:55]\n"
"v_exp_f32 v122,v12\n"
"v_mfma_f32_16x16x32_bf16 v[60:63],a[80:83],a[48:51],v[108:111]\n"
"v_cvt_pk_bf16_f32 v69,v119,v120\n"
"v_mfma_f32_16x16x32_bf16 v[60:63],a[84:87],a[52:55],v[60:63]\n"
"v_exp_f32 v123,v13\n"
"v_mfma_f32_16x16x32_bf16 v[60:63],a[88:91],a[56:59],v[60:63]\n"
"buffer_load_dwordx4 v112,s[24:27],s64 offen offset:2048 sc1 lds\n"
"v_mfma_f32_16x16x32_bf16 v[60:63],a[92:95],a[60:63],v[60:63]\n"
"v_exp_f32 v119,v14\n"
"v_mfma_f32_16x16x32_bf16 v[56:59],a[76:79],a[60:63],v[108:111]\n"
"v_cvt_pk_bf16_f32 v70,v122,v123\n"
"v_or3_b32 v118,v118,v69,v70\n"
"v_mfma_f32_16x16x32_bf16 v[56:59],a[72:75],a[56:59],v[56:59]\n"
"v_exp_f32 v120,v15\n"
"v_mfma_f32_16x16x32_bf16 v[56:59],a[68:71],a[52:55],v[56:59]\n"
"buffer_load_dwordx4 v112,s[24:27],s64 offen offset:3072 sc1 lds\n"
"v_mfma_f32_16x16x32_bf16 v[56:59],a[64:67],a[48:51],v[56:59]\n"
"s_waitcnt lgkmcnt(0)\n"
"v_exp_f32 v122,v16\n"
"v_mfma_f32_16x16x32_bf16 v[128:131],a[96:99],v[80:83],v[128:131]\n"
"v_exp_f32 v123,v17\n"
"v_mfma_f32_16x16x32_bf16 v[160:163],a[96:99],v[84:87],v[160:163]\n"
"s_add_u32 s64,s64,16384\n"
"ds_read_b128 a[64:67],v113 offset:0\n"
"v_cvt_pk_bf16_f32 v71,v119,v120\n"
"v_mfma_f32_16x16x32_bf16 v[192:195],a[96:99],v[88:91],v[192:195]\n"
"v_exp_f32 v119,v18\n"
"v_mfma_f32_16x16x32_bf16 v[224:227],a[96:99],v[92:95],v[224:227]\n"
"ds_read_b128 a[68:71],v113 offset:1024\n"
"v_cvt_pk_bf16_f32 v72,v122,v123\n"
"v_mfma_f32_16x16x32_bf16 a[140:143],a[144:147],v[92:95],a[140:143]\n"
"v_exp_f32 v120,v19\n"
"v_mfma_f32_16x16x32_bf16 v[228:231],a[100:103],v[92:95],v[228:231]\n"
"ds_read_b128 a[72:75],v113 offset:2048\n"
"ds_read_b128 a[76:79],v113 offset:3072\n"
"v_mfma_f32_16x16x32_bf16 v[132:135],a[100:103],v[80:83],v[132:135]\n"
"v_exp_f32 v122,v20\n"
"v_mfma_f32_16x16x32_bf16 v[164:167],a[100:103],v[84:87],v[164:167]\n"
"ds_read_b128 a[80:83],v113 offset:4096\n"
"v_cvt_pk_bf16_f32 v73,v119,v120\n"
"v_mfma_f32_16x16x32_bf16 v[196:199],a[100:103],v[88:91],v[196:199]\n"
"v_exp_f32 v123,v21\n"
"v_mfma_f32_16x16x32_bf16 a[136:139],a[144:147],v[88:91],a[136:139]\n"
"ds_read_b128 a[84:87],v113 offset:5120\n"
"ds_read_b128 a[88:91],v113 offset:6144\n"
"v_mfma_f32_16x16x32_bf16 v[200:203],a[104:107],v[88:91],v[200:203]\n"
"v_exp_f32 v119,v22\n"
"v_mfma_f32_16x16x32_bf16 v[232:235],a[104:107],v[92:95],v[232:235]\n"
"ds_read_b128 a[92:95],v113 offset:7168\n"
"v_cvt_pk_bf16_f32 v74,v122,v123\n"
"v_mfma_f32_16x16x32_bf16 v[136:139],a[104:107],v[80:83],v[136:139]\n"
"v_exp_f32 v120,v23\n"
"v_mfma_f32_16x16x32_bf16 v[168:171],a[104:107],v[84:87],v[168:171]\n"
"s_waitcnt vmcnt(12)\n"
"ds_add_u32 v124,a152\n"
"v_mfma_f32_16x16x32_bf16 a[132:135],a[144:147],v[84:87],a[132:135]\n"
"v_exp_f32 v122,v24\n"
"v_mfma_f32_16x16x32_bf16 v[172:175],a[108:111],v[84:87],v[172:175]\n"
"v_cvt_pk_bf16_f32 v75,v119,v120\n"
"v_or3_b32 v118,v118,v71,v72\n"
"v_mfma_f32_16x16x32_bf16 v[204:207],a[108:111],v[88:91],v[204:207]\n"
"v_exp_f32 v123,v25\n"
"v_mfma_f32_16x16x32_bf16 v[236:239],a[108:111],v[92:95],v[236:239]\n"
"v_cvt_pk_bf16_f32 v76,v122,v123\n"
"v_or3_b32 v118,v118,v73,v74\n"
"v_mfma_f32_16x16x32_bf16 v[140:143],a[108:111],v[80:83],v[140:143]\n"
"v_exp_f32 v119,v26\n"
"v_mfma_f32_16x16x32_bf16 a[128:131],a[144:147],v[80:83],a[128:131]\n"
"v_or3_b32 v118,v118,v75,v76\n"
"v_mfma_f32_16x16x32_bf16 v[144:147],a[112:115],v[80:83],v[144:147]\n"
"v_exp_f32 v120,v27\n"
"v_mfma_f32_16x16x32_bf16 v[176:179],a[112:115],v[84:87],v[176:179]\n"
"v_cvt_pk_bf16_f32 v77,v119,v120\n"
"v_mfma_f32_16x16x32_bf16 v[208:211],a[112:115],v[88:91],v[208:211]\n"
"v_exp_f32 v122,v28\n"
"v_mfma_f32_16x16x32_bf16 v[240:243],a[112:115],v[92:95],v[240:243]\n"
"v_mfma_f32_16x16x32_bf16 v[244:247],a[116:119],v[92:95],v[244:247]\n"
"v_exp_f32 v123,v29\n"
"v_mfma_f32_16x16x32_bf16 v[148:151],a[116:119],v[80:83],v[148:151]\n"
"v_cvt_pk_bf16_f32 v78,v122,v123\n"
"v_or3_b32 v118,v118,v77,v78\n"
"v_mfma_f32_16x16x32_bf16 v[180:183],a[116:119],v[84:87],v[180:183]\n"
"v_exp_f32 v119,v30\n"
"v_mfma_f32_16x16x32_bf16 v[212:215],a[116:119],v[88:91],v[212:215]\n"
"ds_read_b32 v125,v124\n"
"buffer_store_dwordx4 a[252:255],v112,s[24:27],s56 offen offset:3072 sc1\n"
"v_mfma_f32_16x16x32_bf16 v[216:219],a[120:123],v[88:91],v[216:219]\n"
"s_add_u32 s89,s89,1\n"
"v_exp_f32 v120,v31\n"
"v_mfma_f32_16x16x32_bf16 v[248:251],a[120:123],v[92:95],v[248:251]\n"
"v_cvt_pk_bf16_f32 v79,v119,v120\n"
"v_mfma_f32_16x16x32_bf16 v[152:155],a[120:123],v[80:83],v[152:155]\n"
"v_mfma_f32_16x16x32_bf16 v[184:187],a[120:123],v[84:87],v[184:187]\n"
"v_mfma_f32_16x16x32_bf16 v[188:191],a[124:127],v[84:87],v[188:191]\n"
"s_waitcnt lgkmcnt(0)\n"
"v_subrev_u32 v125,s101,v125\n"
"v_or3_b32 v125,v118,v79,v125\n"
"v_mfma_f32_16x16x32_bf16 v[220:223],a[124:127],v[88:91],v[220:223]\n"
"v_and_b32 v125,0x40004000,v125\n"
"v_cmp_ne_u32 vcc,0,v125\n"
"v_mfma_f32_16x16x32_bf16 v[252:255],a[124:127],v[92:95],v[252:255]\n"
"v_mfma_f32_16x16x32_bf16 v[156:159],a[124:127],v[80:83],v[156:159]\n"
"s_cbranch_vccnz .Lfk_cs_f2\n"
".Lfk_cr_f2:\n"
"s_cmp_lt_u32 s89,s60\n"
"s_cbranch_scc1 .Lfk_F\n"
"s_branch .Lfk_Fx\n"
".Lfk_cs_f3:\n"
"s_call_b64 s[94:95],.Lfk_meet3\n"
"v_or_b32 v118,v118,v95\n"
"v_and_b32 v118,0x40004000,v118\n"
"v_cmp_ne_u32 vcc,0,v118\n"
"s_nop 0\n"
"s_cbranch_vccz .Lfk_cr_f3\n"
"s_mov_b32 s91,15\n"
"s_call_b64 s[94:95],.Lfk_rare3\n"
"s_branch .Lfk_cr_f3\n"
".Lfk_cs_f0:\n"
"s_call_b64 s[94:95],.Lfk_meet3\n"
"v_or_b32 v118,v118,v79\n"
"v_and_b32 v118,0x40004000,v118\n"
"v_cmp_ne_u32 vcc,0,v118\n"
"s_nop 0\n"
"s_cbranch_vccz .Lfk_cr_f0\n"
"s_mov_b32 s91,15\n"
"s_call_b64 s[94:95],.Lfk_rare2\n"
"s_branch .Lfk_cr_f0\n"
".Lfk_cs_f1:\n"
"s_call_b64 s[94:95],.Lfk_meet3\n"
"v_or_b32 v118,v118,v95\n"
"v_and_b32 v118,0x40004000,v118\n"
"v_cmp_ne_u32 vcc,0,v118\n"
"s_nop 0\n"
"s_cbranch_vccz .Lfk_cr_f1\n"
"s_mov_b32 s91,15\n"
"s_call_b64 s[94:95],.Lfk_rare3\n"
"s_branch .Lfk_cr_f1\n"
".Lfk_cs_f2:\n"
"s_call_b64 s[94:95],.Lfk_meet3\n"
"v_or_b32 v118,v118,v79\n"
"v_and_b32 v118,0x40004000,v118\n"
"v_cmp_ne_u32 vcc,0,v118\n"
"s_nop 0\n"
"s_cbranch_vccz .Lfk_cr_f2\n"
"s_mov_b32 s91,15\n"
"s_call_b64 s[94:95],.Lfk_rare2\n"
"s_branch .Lfk_cr_f2\n"
".Lfk_cs_g3:\n"
"s_call_b64 s[94:95],.Lfk_meet3\n"
"v_or_b32 v118,v118,v95\n"
"v_and_b32 v118,0x40004000,v118\n"
"v_cmp_ne_u32 vcc,0,v118\n"
"s_nop 0\n"
"s_cbranch_vccz .Lfk_cr_g3\n"
"s_mov_b32 s91,15\n"
"s_call_b64 s[94:95],.Lfk_rare3\n"
"s_branch .Lfk_cr_g3\n"
".Lfk_cs_g0:\n"
"s_call_b64 s[94:95],.Lfk_meet3\n"
"v_or_b32 v118,v118,v79\n"
"v_and_b32 v118,0x40004000,v118\n"
"v_cmp_ne_u32 vcc,0,v118\n"
"s_nop 0\n"
"s_cbranch_vccz .Lfk_cr_g0\n"
"s_mov_b32 s91,15\n"
"s_call_b64 s[94:95],.Lfk_rare2\n"
"s_branch .Lfk_cr_g0\n"
".Lfk_cs_g1:\n"
"s_call_b64 s[94:95],.Lfk_meet3\n"
"v_or_b32 v118,v118,v95\n"
"v_and_b32 v118,0x40004000,v118\n"
"v_cmp_ne_u32 vcc,0,v118\n"
"s_nop 0\n"
"s_cbranch_vccz .Lfk_cr_g1\n"
"s_mov_b32 s91,15\n"
"s_call_b64 s[94:95],.Lfk_rare3\n"
"s_branch .Lfk_cr_g1\n"
".Lfk_cs_g2:\n"
"s_call_b64 s[94:95],.Lfk_meet3\n"
"v_or_b32 v118,v118,v79\n"
"v_and_b32 v118,0x40004000,v118\n"
"v_cmp_ne_u32 vcc,0,v118\n"
"s_nop 0\n"
"s_cbranch_vccz .Lfk_cr_g2\n"
"s_mov_b32 s91,15\n"
"s_call_b64 s[94:95],.Lfk_rare2\n"
"s_branch .Lfk_cr_g2\n"
".Lfk_meet3:\n"
"s_sub_u32 s82,s101,0\n"
"s_mov_b32 s84,0x1000000\n"
".Lfk_meetl3:\n"
"ds_read_b32 v125,v124\n"
"s_sub_u32 s84,s84,1\n"
"s_cbranch_scc1 .Lfk_meetx3\n"
"s_waitcnt lgkmcnt(0)\n"
"v_cmp_gt_u32 vcc,s82,v125\n"
"s_nop 0\n"
"s_cbranch_vccnz .Lfk_meetl3\n"
".Lfk_meetx3:\n"
"s_setpc_b64 s[94:95]\n"
".Lfk_Fx:\n"
"s_mov_b32 s82,2146435072\n"
"buffer_load_dword a148,off,s[24:27],s82\n"
"buffer_load_dword a148,off,s[24:27],s82\n"
"buffer_load_dword a148,off,s[24:27],s82\n"
"buffer_load_dword a148,off,s[24:27],s82\n"
"s_cmp_ge_u32 s52,2\n"
"s_cbranch_scc1 .Lfk_winA\n"
".Lfk_H:\n"
"v_mfma_f32_16x16x32_bf16 v[0:3],a[64:67],a[0:3],v[96:99]\n"
"s_swappc_b64 s[94:95],s[20:21]\n"
"s_add_u32 s101,s101,4\n"
"v_exp_f32 v122,v32\n"
"v_mfma_f32_16x16x32_bf16 v[0:3],a[68:71],a[4:7],v[0:3]\n"
"s_cmp_eq_u32 s64,s65\n"
"v_exp_f32 v123,v33\n"
"v_mfma_f32_16x16x32_bf16 v[0:3],a[72:75],a[8:11],v[0:3]\n"
"ds_read_b128 a[96:99],v114 offset:24576\n"
"ds_read_b128 a[100:103],v114 offset:25600\n"
"v_mfma_f32_16x16x32_bf16 v[0:3],a[76:79],a[12:15],v[0:3]\n"
"s_cselect_b32 s64,s66,s64\n"
"v_exp_f32 v119,v34\n"
"v_mfma_f32_16x16x32_bf16 v[4:7],a[92:95],a[12:15],v[96:99]\n"
"ds_read_b128 a[104:107],v114 offset:26624\n"
"v_cvt_pk_bf16_f32 v80,v122,v123\n"
"v_mfma_f32_16x16x32_bf16 v[4:7],a[88:91],a[8:11],v[4:7]\n"
"v_exp_f32 v120,v35\n"
"v_mfma_f32_16x16x32_bf16 v[4:7],a[84:87],a[4:7],v[4:7]\n"
"ds_read_b128 a[108:111],v114 offset:27648\n"
"ds_read_b128 a[112:115],v114 offset:28672\n"
"v_mfma_f32_16x16x32_bf16 v[4:7],a[80:83],a[0:3],v[4:7]\n"
"v_exp_f32 v122,v36\n"
"v_mfma_f32_16x16x32_bf16 v[12:15],a[80:83],a[16:19],v[100:103]\n"
"ds_read_b128 a[116:119],v114 offset:29696\n"
"v_cvt_pk_bf16_f32 v81,v119,v120\n"
"v_mfma_f32_16x16x32_bf16 v[12:15],a[84:87],a[20:23],v[12:15]\n"
"v_exp_f32 v123,v37\n"
"v_mfma_f32_16x16x32_bf16 v[12:15],a[88:91],a[24:27],v[12:15]\n"
"ds_read_b128 a[120:123],v114 offset:30720\n"
"ds_read_b128 a[124:127],v114 offset:31744\n"
"v_mfma_f32_16x16x32_bf16 v[12:15],a[92:95],a[28:31],v[12:15]\n"
"s_add_u32 m0,s67,0\n"
"v_exp_f32 v119,v38\n"
"v_mfma_f32_16x16x32_bf16 v[8:11],a[76:79],a[28:31],v[100:103]\n"
"v_cvt_pk_bf16_f32 v82,v122,v123\n"
"v_or3_b32 v118,v80,v81,v82\n"
"v_mfma_f32_16x16x32_bf16 v[8:11],a[72:75],a[24:27],v[8:11]\n"
"v_exp_f32 v120,v39\n"
"v_mfma_f32_16x16x32_bf16 v[8:11],a[68:71],a[20:23],v[8:11]\n"
"buffer_load_dwordx4 v112,s[24:27],s64 offen offset:0 sc1 lds\n"
"v_mfma_f32_16x16x32_bf16 v[8:11],a[64:67],a[16:19],v[8:11]\n"
"v_exp_f32 v122,v40\n"
"v_mfma_f32_16x16x32_bf16 v[16:19],a[64:67],a[32:35],v[104:107]\n"
"v_cvt_pk_bf16_f32 v83,v119,v120\n"
"v_mfma_f32_16x16x32_bf16 v[16:19],a[68:71],a[36:39],v[16:19]\n"
"v_exp_f32 v123,v41\n"
"v_mfma_f32_16x16x32_bf16 v[16:19],a[72:75],a[40:43],v[16:19]\n"
"buffer_load_dwordx4 v112,s[24:27],s64 offen offset:1024 sc1 lds\n"
"v_mfma_f32_16x16x32_bf16 v[16:19],a[76:79],a[44:47],v[16:19]\n"
"v_exp_f32 v119,v42\n"
"v_mfma_f32_16x16x32_bf16 v[20:23],a[92:95],a[44:47],v[104:107]\n"
"v_cvt_pk_bf16_f32 v84,v122,v123\n"
"v_or3_b32 v118,v118,v83,v84\n"
"v_mfma_f32_16x16x32_bf16 v[20:23],a[88:91],a[40:43],v[20:23]\n"
"v_exp_f32 v120,v43\n"
"v_mfma_f32_16x16x32_bf16 v[20:23],a[84:87],a[36:39],v[20:23]\n"
"v_cvt_pk_bf16_f32 v85,v119,v120\n"
"v_mfma_f32_16x16x32_bf16 v[20:23],a[80:83],a[32:35],v[20:23]\n"
"v_exp_f32 v122,v44\n"
"v_mfma_f32_16x16x32_bf16 v[28:31],a[80:83],a[48:51],v[108:111]\n"
"buffer_load_dwordx4 v112,s[24:27],s64 offen offset:2048 sc1 lds\n"
"v_mfma_f32_16x16x32_bf16 v[28:31],a[84:87],a[52:55],v[28:31]\n"
"v_exp_f32 v123,v45\n"
"v_mfma_f32_16x16x32_bf16 v[28:31],a[88:91],a[56:59],v[28:31]\n"
"v_cvt_pk_bf16_f32 v86,v122,v123\n"
"v_or3_b32 v118,v118,v85,v86\n"
"v_mfma_f32_16x16x32_bf16 v[28:31],a[92:95],a[60:63],v[28:31]\n"
"v_exp_f32 v119,v46\n"
"v_mfma_f32_16x16x32_bf16 v[24:27],a[76:79],a[60:63],v[108:111]\n"
"v_mfma_f32_16x16x32_bf16 v[24:27],a[72:75],a[56:59],v[24:27]\n"
"v_exp_f32 v120,v47\n"
"v_mfma_f32_16x16x32_bf16 v[24:27],a[68:71],a[52:55],v[24:27]\n"
"buffer_load_dwordx4 v112,s[24:27],s64 offen offset:3072 sc1 lds\n"
"v_mfma_f32_16x16x32_bf16 v[24:27],a[64:67],a[48:51],v[24:27]\n"
"s_waitcnt lgkmcnt(0)\n"
"v_exp_f32 v122,v48\n"
"v_mfma_f32_16x16x32_bf16 v[128:131],a[96:99],v[64:67],v[128:131]\n"
"v_exp_f32 v123,v49\n"
"v_mfma_f32_16x16x32_bf16 v[160:163],a[96:99],v[68:71],v[160:163]\n"
"ds_read_b128 a[64:67],v113 offset:16384\n"
"v_cvt_pk_bf16_f32 v87,v119,v120\n"
"v_mfma_f32_16x16x32_bf16 v[192:195],a[96:99],v[72:75],v[192:195]\n"
"s_add_u32 m0,s67,4096\n"
"v_exp_f32 v119,v50\n"
"v_mfma_f32_16x16x32_bf16 v[224:227],a[96:99],v[76:79],v[224:227]\n"
"ds_read_b128 a[68:71],v113 offset:17408\n"
"v_cvt_pk_bf16_f32 v88,v122,v123\n"
"v_mfma_f32_16x16x32_bf16 a[140:143],a[144:147],v[76:79],a[140:143]\n"
"v_exp_f32 v120,v51\n"
"v_mfma_f32_16x16x32_bf16 v[228:231],a[100:103],v[76:79],v[228:231]\n"
"ds_read_b128 a[72:75],v113 offset:18432\n"
"ds_read_b128 a[76:79],v113 offset:19456\n"
"v_mfma_f32_16x16x32_bf16 v[132:135],a[100:103],v[64:67],v[132:135]\n"
"v_exp_f32 v122,v52\n"
"v_mfma_f32_16x16x32_bf16 v[164:167],a[100:103],v[68:71],v[164:167]\n"
"ds_read_b128 a[80:83],v113 offset:20480\n"
"v_cvt_pk_bf16_f32 v89,v119,v120\n"
"v_mfma_f32_16x16x32_bf16 v[196:199],a[100:103],v[72:75],v[196:199]\n"
"v_exp_f32 v123,v53\n"
"v_mfma_f32_16x16x32_bf16 a[136:139],a[144:147],v[72:75],a[136:139]\n"
"ds_read_b128 a[84:87],v113 offset:21504\n"
"ds_read_b128 a[88:91],v113 offset:22528\n"
"v_mfma_f32_16x16x32_bf16 v[200:203],a[104:107],v[72:75],v[200:203]\n"
"v_exp_f32 v119,v54\n"
"v_mfma_f32_16x16x32_bf16 v[232:235],a[104:107],v[76:79],v[232:235]\n"
"ds_read_b128 a[92:95],v113 offset:23552\n"
"v_cvt_pk_bf16_f32 v90,v122,v123\n"
"v_mfma_f32_16x16x32_bf16 v[136:139],a[104:107],v[64:67],v[136:139]\n"
"v_exp_f32 v120,v55\n"
"v_mfma_f32_16x16x32_bf16 v[168:171],a[104:107],v[68:71],v[168:171]\n"
"s_waitcnt vmcnt(15)\n"
"ds_add_u32 v124,a152\n"
"v_mfma_f32_16x16x32_bf16 a[132:135],a[144:147],v[68:71],a[132:135]\n"
"v_exp_f32 v122,v56\n"
"v_mfma_f32_16x16x32_bf16 v[172:175],a[108:111],v[68:71],v[172:175]\n"
"v_cvt_pk_bf16_f32 v91,v119,v120\n"
"v_or3_b32 v118,v118,v87,v88\n"
"v_mfma_f32_16x16x32_bf16 v[204:207],a[108:111],v[72:75],v[204:207]\n"
"v_exp_f32 v123,v57\n"
"v_mfma_f32_16x16x32_bf16 v[236:239],a[108:111],v[76:79],v[236:239]\n"
"buffer_load_dwordx4 v117,s[24:27],s64 offen offset:0 sc1 lds\n"
"v_mfma_f32_16x16x32_bf16 v[140:143],a[108:111],v[64:67],v[140:143]\n"
"v_exp_f32 v119,v58\n"
"v_mfma_f32_16x16x32_bf16 a[128:131],a[144:147],v[64:67],a[128:131]\n"
"v_cvt_pk_bf16_f32 v92,v122,v123\n"
"v_or3_b32 v118,v118,v89,v90\n"
"v_mfma_f32_16x16x32_bf16 v[144:147],a[112:115],v[64:67],v[144:147]\n"
"v_exp_f32 v120,v59\n"
"v_mfma_f32_16x16x32_bf16 v[176:179],a[112:115],v[68:71],v[176:179]\n"
"buffer_load_dwordx4 v117,s[24:27],s64 offen offset:1024 sc1 lds\n"
"v_mfma_f32_16x16x32_bf16 v[208:211],a[112:115],v[72:75],v[208:211]\n"
"v_exp_f32 v122,v60\n"
"v_mfma_f32_16x16x32_bf16 v[240:243],a[112:115],v[76:79],v[240:243]\n"
"v_cvt_pk_bf16_f32 v93,v119,v120\n"
"v_or3_b32 v118,v118,v91,v92\n"
"v_mfma_f32_16x16x32_bf16 v[244:247],a[116:119],v[76:79],v[244:247]\n"
"v_exp_f32 v123,v61\n"
"v_mfma_f32_16x16x32_bf16 v[148:151],a[116:119],v[64:67],v[148:151]\n"
"buffer_load_dwordx4 v117,s[24:27],s64 offen offset:2048 sc1 lds\n"
"v_mfma_f32_16x16x32_bf16 v[180:183],a[116:119],v[68:71],v[180:183]\n"
"v_exp_f32 v119,v62\n"
"v_mfma_f32_16x16x32_bf16 v[212:215],a[116:119],v[72:75],v[212:215]\n"
"ds_read_b32 v125,v124\n"
"v_cvt_pk_bf16_f32 v94,v122,v123\n"
"v_mfma_f32_16x16x32_bf16 v[216:219],a[120:123],v[72:75],v[216:219]\n"
"v_exp_f32 v120,v63\n"
"v_mfma_f32_16x16x32_bf16 v[248:251],a[120:123],v[76:79],v[248:251]\n"
"buffer_load_dwordx4 v117,s[24:27],s64 offen offset:3072 sc1 lds\n"
"v_mfma_f32_16x16x32_bf16 v[152:155],a[120:123],v[64:67],v[152:155]\n"
"s_add_u32 s64,s64,16384\n"
"s_andn2_b32 m0,s55,16384\n"
"v_mfma_f32_16x16x32_bf16 v[184:187],a[120:123],v[68:71],v[184:187]\n"
"s_add_u32 s54,s54,s22\n"
"v_cvt_pk_bf16_f32 v95,v119,v120\n"
"v_mfma_f32_16x16x32_bf16 v[188:191],a[124:127],v[68:71],v[188:191]\n"
"s_waitcnt lgkmcnt(0)\n"
"v_subrev_u32 v125,s101,v125\n"
"v_or3_b32 v118,v118,v93,v94\n"
"v_mfma_f32_16x16x32_bf16 v[220:223],a[124:127],v[72:75],v[220:223]\n"
"v_or3_b32 v125,v118,v95,v125\n"
"v_and_b32 v125,0x40004000,v125\n"
"v_mfma_f32_16x16x32_bf16 v[252:255],a[124:127],v[76:79],v[252:255]\n"
"buffer_load_dwordx4 v115,s[32:35],s54 offen sc1 lds\n"
"buffer_store_dwordx4 a[240:243],v112,s[24:27],s56 offen offset:0 sc1\n"
"v_cmp_ne_u32 vcc,0,v125\n"
"v_mfma_f32_16x16x32_bf16 v[156:159],a[124:127],v[64:67],v[156:159]\n"
"s_cbranch_vccnz .Lfk_cs_g3\n"
".Lfk_cr_g3:\n"
"v_mfma_f32_16x16x32_bf16 v[32:35],a[64:67],a[0:3],v[96:99]\n"
"s_add_u32 s101,s101,4\n"
"v_exp_f32 v122,v0\n"
"v_mfma_f32_16x16x32_bf16 v[32:35],a[68:71],a[4:7],v[32:35]\n"
"s_add_u32 m0,s55,4096\n"
"v_exp_f32 v123,v1\n"
"v_mfma_f32_16x16x32_bf16 v[32:35],a[72:75],a[8:11],v[32:35]\n"
"ds_read_b128 a[96:99],v114 offset:57344\n"
"ds_read_b128 a[100:103],v114 offset:58368\n"
"v_mfma_f32_16x16x32_bf16 v[32:35],a[76:79],a[12:15],v[32:35]\n"
"s_add_u32 s54,s54,s22\n"
"v_exp_f32 v119,v2\n"
"v_mfma_f32_16x16x32_bf16 v[36:39],a[92:95],a[12:15],v[96:99]\n"
"ds_read_b128 a[104:107],v114 offset:59392\n"
"v_cvt_pk_bf16_f32 v64,v122,v123\n"
"v_mfma_f32_16x16x32_bf16 v[36:39],a[88:91],a[8:11],v[36:39]\n"
"v_exp_f32 v120,v3\n"
"v_mfma_f32_16x16x32_bf16 v[36:39],a[84:87],a[4:7],v[36:39]\n"
"ds_read_b128 a[108:111],v114 offset:60416\n"
"ds_read_b128 a[112:115],v114 offset:61440\n"
"v_mfma_f32_16x16x32_bf16 v[36:39],a[80:83],a[0:3],v[36:39]\n"
"v_exp_f32 v122,v4\n"
"v_mfma_f32_16x16x32_bf16 v[44:47],a[80:83],a[16:19],v[100:103]\n"
"ds_read_b128 a[116:119],v114 offset:62464\n"
"v_cvt_pk_bf16_f32 v65,v119,v120\n"
"v_mfma_f32_16x16x32_bf16 v[44:47],a[84:87],a[20:23],v[44:47]\n"
"v_exp_f32 v123,v5\n"
"v_mfma_f32_16x16x32_bf16 v[44:47],a[88:91],a[24:27],v[44:47]\n"
"ds_read_b128 a[120:123],v114 offset:63488\n"
"ds_read_b128 a[124:127],v114 offset:64512\n"
"v_mfma_f32_16x16x32_bf16 v[44:47],a[92:95],a[28:31],v[44:47]\n"
"v_exp_f32 v119,v6\n"
"v_mfma_f32_16x16x32_bf16 v[40:43],a[76:79],a[28:31],v[100:103]\n"
"v_cvt_pk_bf16_f32 v66,v122,v123\n"
"v_or3_b32 v118,v64,v65,v66\n"
"v_mfma_f32_16x16x32_bf16 v[40:43],a[72:75],a[24:27],v[40:43]\n"
"v_exp_f32 v120,v7\n"
"v_mfma_f32_16x16x32_bf16 v[40:43],a[68:71],a[20:23],v[40:43]\n"
"buffer_load_dwordx4 v115,s[32:35],s54 offen sc1 lds\n"
"v_mfma_f32_16x16x32_bf16 v[40:43],a[64:67],a[16:19],v[40:43]\n"
"s_add_u32 m0,s67,16384\n"
"v_exp_f32 v122,v8\n"
"v_mfma_f32_16x16x32_bf16 v[48:51],a[64:67],a[32:35],v[104:107]\n"
"v_cvt_pk_bf16_f32 v67,v119,v120\n"
"v_mfma_f32_16x16x32_bf16 v[48:51],a[68:71],a[36:39],v[48:51]\n"
"v_exp_f32 v123,v9\n"
"v_mfma_f32_16x16x32_bf16 v[48:51],a[72:75],a[40:43],v[48:51]\n"
"buffer_load_dwordx4 v112,s[24:27],s64 offen offset:0 sc1 lds\n"
"v_mfma_f32_16x16x32_bf16 v[48:51],a[76:79],a[44:47],v[48:51]\n"
"v_exp_f32 v119,v10\n"
"v_mfma_f32_16x16x32_bf16 v[52:55],a[92:95],a[44:47],v[104:107]\n"
"v_cvt_pk_bf16_f32 v68,v122,v123\n"
"v_or3_b32 v118,v118,v67,v68\n"
"v_mfma_f32_16x16x32_bf16 v[52:55],a[88:91],a[40:43],v[52:55]\n"
"v_exp_f32 v120,v11\n"
"v_mfma_f32_16x16x32_bf16 v[52:55],a[84:87],a[36:39],v[52:55]\n"
"buffer_load_dwordx4 v112,s[24:27],s64 offen offset:1024 sc1 lds\n"
"v_mfma_f32_16x16x32_bf16 v[52:55],a[80:83],a[32:35],v[52:55]\n"
"v_exp_f32 v122,v12\n"
"v_mfma_f32_16x16x32_bf16 v[60:63],a[80:83],a[48:51],v[108:111]\n"
"v_cvt_pk_bf16_f32 v69,v119,v120\n"
"v_mfma_f32_16x16x32_bf16 v[60:63],a[84:87],a[52:55],v[60:63]\n"
"v_exp_f32 v123,v13\n"
"v_mfma_f32_16x16x32_bf16 v[60:63],a[88:91],a[56:59],v[60:63]\n"
"buffer_load_dwordx4 v112,s[24:27],s64 offen offset:2048 sc1 lds\n"
"v_mfma_f32_16x16x32_bf16 v[60:63],a[92:95],a[60:63],v[60:63]\n"
"v_exp_f32 v119,v14\n"
"v_mfma_f32_16x16x32_bf16 v[56:59],a[76:79],a[60:63],v[108:111]\n"
"v_cvt_pk_bf16_f32 v70,v122,v123\n"
"v_or3_b32 v118,v118,v69,v70\n"
"v_mfma_f32_16x16x32_bf16 v[56:59],a[72:75],a[56:59],v[56:59]\n"
"v_exp_f32 v120,v15\n"
"v_mfma_f32_16x16x32_bf16 v[56:59],a[68:71],a[52:55],v[56:59]\n"
"buffer_load_dwordx4 v112,s[24:27],s64 offen offset:3072 sc1 lds\n"
"v_mfma_f32_16x16x32_bf16 v[56:59],a[64:67],a[48:51],v[56:59]\n"
"s_waitcnt lgkmcnt(0)\n"
"v_exp_f32 v122,v16\n"
"v_mfma_f32_16x16x32_bf16 v[128:131],a[96:99],v[80:83],v[128:131]\n"
"v_exp_f32 v123,v17\n"
"v_mfma_f32_16x16x32_bf16 v[160:163],a[96:99],v[84:87],v[160:163]\n"
"ds_read_b128 a[64:67],v113 offset:49152\n"
"v_cvt_pk_bf16_f32 v71,v119,v120\n"
"v_mfma_f32_16x16x32_bf16 v[192:195],a[96:99],v[88:91],v[192:195]\n"
"s_add_u32 m0,s67,20480\n"
"v_exp_f32 v119,v18\n"
"v_mfma_f32_16x16x32_bf16 v[224:227],a[96:99],v[92:95],v[224:227]\n"
"ds_read_b128 a[68:71],v113 offset:50176\n"
"v_cvt_pk_bf16_f32 v72,v122,v123\n"
"v_mfma_f32_16x16x32_bf16 a[140:143],a[144:147],v[92:95],a[140:143]\n"
"v_exp_f32 v120,v19\n"
"v_mfma_f32_16x16x32_bf16 v[228:231],a[100:103],v[92:95],v[228:231]\n"
"ds_read_b128 a[72:75],v113 offset:51200\n"
"ds_read_b128 a[76:79],v113 offset:52224\n"
"v_mfma_f32_16x16x32_bf16 v[132:135],a[100:103],v[80:83],v[132:135]\n"
"v_exp_f32 v122,v20\n"
"v_mfma_f32_16x16x32_bf16 v[164:167],a[100:103],v[84:87],v[164:167]\n"
"ds_read_b128 a[80:83],v113 offset:53248\n"
"v_cvt_pk_bf16_f32 v73,v119,v120\n"
"v_mfma_f32_16x16x32_bf16 v[196:199],a[100:103],v[88:91],v[196:199]\n"
"v_exp_f32 v123,v21\n"
"v_mfma_f32_16x16x32_bf16 a[136:139],a[144:147],v[88:91],a[136:139]\n"
"ds_read_b128 a[84:87],v113 offset:54272\n"
"ds_read_b128 a[88:91],v113 offset:55296\n"
"v_mfma_f32_16x16x32_bf16 v[200:203],a[104:107],v[88:91],v[200:203]\n"
"v_exp_f32 v119,v22\n"
"v_mfma_f32_16x16x32_bf16 v[232:235],a[104:107],v[92:95],v[232:235]\n"
"ds_read_b128 a[92:95],v113 offset:56320\n"
"v_cvt_pk_bf16_f32 v74,v122,v123\n"
"v_mfma_f32_16x16x32_bf16 v[136:139],a[104:107],v[80:83],v[136:139]\n"
"v_exp_f32 v120,v23\n"
"v_mfma_f32_16x16x32_bf16 v[168:171],a[104:107],v[84:87],v[168:171]\n"
"s_waitcnt vmcnt(16)\n"
"ds_add_u32 v124,a152\n"
"v_mfma_f32_16x16x32_bf16 a[132:135],a[144:147],v[84:87],a[132:135]\n"
"v_exp_f32 v122,v24\n"
"v_mfma_f32_16x16x32_bf16 v[172:175],a[108:111],v[84:87],v[172:175]\n"
"v_cvt_pk_bf16_f32 v75,v119,v120\n"
"v_or3_b32 v118,v118,v71,v72\n"
"v_mfma_f32_16x16x32_bf16 v[204:207],a[108:111],v[88:91],v[204:207]\n"
"v_exp_f32 v123,v25\n"
"v_mfma_f32_16x16x32_bf16 v[236:239],a[108:111],v[92:95],v[236:239]\n"
"buffer_load_dwordx4 v117,s[24:27],s64 offen offset:0 sc1 lds\n"
"v_mfma_f32_16x16x32_bf16 v[140:143],a[108:111],v[80:83],v[140:143]\n"
"v_exp_f32 v119,v26\n"
"v_mfma_f32_16x16x32_bf16 a[128:131],a[144:147],v[80:83],a[128:131]\n"
"v_cvt_pk_bf16_f32 v76,v122,v123\n"
"v_or3_b32 v118,v118,v73,v74\n"
"v_mfma_f32_16x16x32_bf16 v[144:147],a[112:115],v[80:83],v[144:147]\n"
"v_exp_f32 v120,v27\n"
"v_mfma_f32_16x16x32_bf16 v[176:179],a[112:115],v[84:87],v[176:179]\n"
"buffer_load_dwordx4 v117,s[24:27],s64 offen offset:1024 sc1 lds\n"
"v_mfma_f32_16x16x32_bf16 v[208:211],a[112:115],v[88:91],v[208:211]\n"
"v_exp_f32 v122,v28\n"
"v_mfma_f32_16x16x32_bf16 v[240:243],a[112:115],v[92:95],v[240:243]\n"
"v_cvt_pk_bf16_f32 v77,v119,v120\n"
"v_or3_b32 v118,v118,v75,v76\n"
"v_mfma_f32_16x16x32_bf16 v[244:247],a[116:119],v[92:95],v[244:247]\n"
"v_exp_f32 v123,v29\n"
"v_mfma_f32_16x16x32_bf16 v[148:151],a[116:119],v[80:83],v[148:151]\n"
"buffer_load_dwordx4 v117,s[24:27],s64 offen offset:2048 sc1 lds\n"
"v_mfma_f32_16x16x32_bf16 v[180:183],a[116:119],v[84:87],v[180:183]\n"
"v_exp_f32 v119,v30\n"
"v_mfma_f32_16x16x32_bf16 v[212:215],a[116:119],v[88:91],v[212:215]\n"
"ds_read_b32 v125,v124\n"
"v_cvt_pk_bf16_f32 v78,v122,v123\n"
"v_mfma_f32_16x16x32_bf16 v[216:219],a[120:123],v[88:91],v[216:219]\n"
"v_exp_f32 v120,v31\n"
"v_mfma_f32_16x16x32_bf16 v[248:251],a[120:123],v[92:95],v[248:251]\n"
"buffer_load_dwordx4 v117,s[24:27],s64 offen offset:3072 sc1 lds\n"
"v_mfma_f32_16x16x32_bf16 v[152:155],a[120:123],v[80:83],v[152:155]\n"
"buffer_store_dwordx4 a[244:247],v112,s[24:27],s56 offen offset:1024 sc1\n"
"v_mfma_f32_16x16x32_bf16 v[184:187],a[120:123],v[84:87],v[184:187]\n"
"s_add_u32 s64,s64,16384\n"
"v_cvt_pk_bf16_f32 v79,v119,v120\n"
"v_or3_b32 v118,v118,v77,v78\n"
"v_mfma_f32_16x16x32_bf16 v[188:191],a[124:127],v[84:87],v[188:191]\n"
"s_waitcnt lgkmcnt(0)\n"
"v_subrev_u32 v125,s101,v125\n"
"v_or3_b32 v125,v118,v79,v125\n"
"v_mfma_f32_16x16x32_bf16 v[220:223],a[124:127],v[88:91],v[220:223]\n"
"v_and_b32 v125,0x40004000,v125\n"
"v_cmp_ne_u32 vcc,0,v125\n"
"v_mfma_f32_16x16x32_bf16 v[252:255],a[124:127],v[92:95],v[252:255]\n"
"v_mfma_f32_16x16x32_bf16 v[156:159],a[124:127],v[80:83],v[156:159]\n"
"s_cbranch_vccnz .Lfk_cs_g0\n"
".Lfk_cr_g0:\n"
"v_mfma_f32_16x16x32_bf16 v[0:3],a[64:67],a[0:3],v[96:99]\n"
"s_add_u32 s101,s101,4\n"
"v_exp_f32 v122,v32\n"
"v_mfma_f32_16x16x32_bf16 v[0:3],a[68:71],a[4:7],v[0:3]\n"
"s_add_u32 m0,s55,8192\n"
"v_exp_f32 v123,v33\n"
"v_mfma_f32_16x16x32_bf16 v[0:3],a[72:75],a[8:11],v[0:3]\n"
"ds_read_b128 a[96:99],v114 offset:40960\n"
"ds_read_b128 a[100:103],v114 offset:41984\n"
"v_mfma_f32_16x16x32_bf16 v[0:3],a[76:79],a[12:15],v[0:3]\n"
"s_add_u32 s54,s54,s22\n"
"v_exp_f32 v119,v34\n"
"v_mfma_f32_16x16x32_bf16 v[4:7],a[92:95],a[12:15],v[96:99]\n"
"ds_read_b128 a[104:107],v114 offset:43008\n"
"v_cvt_pk_bf16_f32 v80,v122,v123\n"
"v_mfma_f32_16x16x32_bf16 v[4:7],a[88:91],a[8:11],v[4:7]\n"
"v_exp_f32 v120,v35\n"
"v_mfma_f32_16x16x32_bf16 v[4:7],a[84:87],a[4:7],v[4:7]\n"
"ds_read_b128 a[108:111],v114 offset:44032\n"
"ds_read_b128 a[112:115],v114 offset:45056\n"
"v_mfma_f32_16x16x32_bf16 v[4:7],a[80:83],a[0:3],v[4:7]\n"
"v_exp_f32 v122,v36\n"
"v_mfma_f32_16x16x32_bf16 v[12:15],a[80:83],a[16:19],v[100:103]\n"
"ds_read_b128 a[116:119],v114 offset:46080\n"
"v_cvt_pk_bf16_f32 v81,v119,v120\n"
"v_mfma_f32_16x16x32_bf16 v[12:15],a[84:87],a[20:23],v[12:15]\n"
"v_exp_f32 v123,v37\n"
"v_mfma_f32_16x16x32_bf16 v[12:15],a[88:91],a[24:27],v[12:15]\n"
"ds_read_b128 a[120:123],v114 offset:47104\n"
"ds_read_b128 a[124:127],v114 offset:48128\n"
"v_mfma_f32_16x16x32_bf16 v[12:15],a[92:95],a[28:31],v[12:15]\n"
"v_exp_f32 v119,v38\n"
"v_mfma_f32_16x16x32_bf16 v[8:11],a[76:79],a[28:31],v[100:103]\n"
"v_cvt_pk_bf16_f32 v82,v122,v123\n"
"v_or3_b32 v118,v80,v81,v82\n"
"v_mfma_f32_16x16x32_bf16 v[8:11],a[72:75],a[24:27],v[8:11]\n"
"v_exp_f32 v120,v39\n"
"v_mfma_f32_16x16x32_bf16 v[8:11],a[68:71],a[20:23],v[8:11]\n"
"buffer_load_dwordx4 v115,s[32:35],s54 offen sc1 lds\n"
"v_mfma_f32_16x16x32_bf16 v[8:11],a[64:67],a[16:19],v[8:11]\n"
"s_add_u32 m0,s67,49152\n"
"v_exp_f32 v122,v40\n"
"v_mfma_f32_16x16x32_bf16 v[16:19],a[64:67],a[32:35],v[104:107]\n"
"v_cvt_pk_bf16_f32 v83,v119,v120\n"
"v_mfma_f32_16x16x32_bf16 v[16:19],a[68:71],a[36:39],v[16:19]\n"
"v_exp_f32 v123,v41\n"
"v_mfma_f32_16x16x32_bf16 v[16:19],a[72:75],a[40:43],v[16:19]\n"
"buffer_load_dwordx4 v112,s[24:27],s64 offen offset:0 sc1 lds\n"
"v_mfma_f32_16x16x32_bf16 v[16:19],a[76:79],a[44:47],v[16:19]\n"
"v_exp_f32 v119,v42\n"
"v_mfma_f32_16x16x32_bf16 v[20:23],a[92:95],a[44:47],v[104:107]\n"
"v_cvt_pk_bf16_f32 v84,v122,v123\n"
"v_or3_b32 v118,v118,v83,v84\n"
"v_mfma_f32_16x16x32_bf16 v[20:23],a[88:91],a[40:43],v[20:23]\n"
"v_exp_f32 v120,v43\n"
"v_mfma_f32_16x16x32_bf16 v[20:23],a[84:87],a[36:39],v[20:23]\n"
"buffer_load_dwordx4 v112,s[24:27],s64 offen offset:1024 sc1 lds\n"
"v_mfma_f32_16x16x32_bf16 v[20:23],a[80:83],a[32:35],v[20:23]\n"
"v_exp_f32 v122,v44\n"
"v_mfma_f32_16x16x32_bf16 v[28:31],a[80:83],a[48:51],v[108:111]\n"
"v_cvt_pk_bf16_f32 v85,v119,v120\n"
"v_mfma_f32_16x16x32_bf16 v[28:31],a[84:87],a[52:55],v[28:31]\n"
"v_exp_f32 v123,v45\n"
"v_mfma_f32_16x16x32_bf16 v[28:31],a[88:91],a[56:59],v[28:31]\n"
"buffer_load_dwordx4 v112,s[24:27],s64 offen offset:2048 sc1 lds\n"
"v_mfma_f32_16x16x32_bf16 v[28:31],a[92:95],a[60:63],v[28:31]\n"
"v_exp_f32 v119,v46\n"
"v_mfma_f32_16x16x32_bf16 v[24:27],a[76:79],a[60:63],v[108:111]\n"
"v_cvt_pk_bf16_f32 v86,v122,v123\n"
"v_or3_b32 v118,v118,v85,v86\n"
"v_mfma_f32_16x16x32_bf16 v[24:27],a[72:75],a[56:59],v[24:27]\n"
"v_exp_f32 v120,v47\n"
"v_mfma_f32_16x16x32_bf16 v[24:27],a[68:71],a[52:55],v[24:27]\n"
"buffer_load_dwordx4 v112,s[24:27],s64 offen offset:3072 sc1 lds\n"
"v_mfma_f32_16x16x32_bf16 v[24:27],a[64:67],a[48:51],v[24:27]\n"
"s_waitcnt lgkmcnt(0)\n"
"v_exp_f32 v122,v48\n"
"v_mfma_f32_16x16x32_bf16 v[128:131],a[96:99],v[64:67],v[128:131]\n"
"v_exp_f32 v123,v49\n"
"v_mfma_f32_16x16x32_bf16 v[160:163],a[96:99],v[68:71],v[160:163]\n"
"ds_read_b128 a[64:67],v113 offset:32768\n"
"v_cvt_pk_bf16_f32 v87,v119,v120\n"
"v_mfma_f32_16x16x32_bf16 v[192:195],a[96:99],v[72:75],v[192:195]\n"
"s_add_u32 m0,s67,53248\n"
"v_exp_f32 v119,v50\n"
"v_mfma_f32_16x16x32_bf16 v[224:227],a[96:99],v[76:79],v[224:227]\n"
"ds_read_b128 a[68:71],v113 offset:33792\n"
"v_cvt_pk_bf16_f32 v88,v122,v123\n"
"v_mfma_f32_16x16x32_bf16 a[140:143],a[144:147],v[76:79],a[140:143]\n"
"v_exp_f32 v120,v51\n"
"v_mfma_f32_16x16x32_bf16 v[228:231],a[100:103],v[76:79],v[228:231]\n"
"ds_read_b128 a[72:75],v113 offset:34816\n"
"ds_read_b128 a[76:79],v113 offset:35840\n"
"v_mfma_f32_16x16x32_bf16 v[132:135],a[100:103],v[64:67],v[132:135]\n"
"v_exp_f32 v122,v52\n"
"v_mfma_f32_16x16x32_bf16 v[164:167],a[100:103],v[68:71],v[164:167]\n"
"ds_read_b128 a[80:83],v113 offset:36864\n"
"v_cvt_pk_bf16_f32 v89,v119,v120\n"
"v_mfma_f32_16x16x32_bf16 v[196:199],a[100:103],v[72:75],v[196:199]\n"
"v_exp_f32 v123,v53\n"
"v_mfma_f32_16x16x32_bf16 a[136:139],a[144:147],v[72:75],a[136:139]\n"
"ds_read_b128 a[84:87],v113 offset:37888\n"
"ds_read_b128 a[88:91],v113 offset:38912\n"
"v_mfma_f32_16x16x32_bf16 v[200:203],a[104:107],v[72:75],v[200:203]\n"
"v_exp_f32 v119,v54\n"
"v_mfma_f32_16x16x32_bf16 v[232:235],a[104:107],v[76:79],v[232:235]\n"
"ds_read_b128 a[92:95],v113 offset:39936\n"
"v_cvt_pk_bf16_f32 v90,v122,v123\n"
"v_mfma_f32_16x16x32_bf16 v[136:139],a[104:107],v[64:67],v[136:139]\n"
"v_exp_f32 v120,v55\n"
"v_mfma_f32_16x16x32_bf16 v[168:171],a[104:107],v[68:71],v[168:171]\n"
"s_waitcnt vmcnt(16)\n"
"ds_add_u32 v124,a152\n"
"v_mfma_f32_16x16x32_bf16 a[132:135],a[144:147],v[68:71],a[132:135]\n"
"v_exp_f32 v122,v56\n"
"v_mfma_f32_16x16x32_bf16 v[172:175],a[108:111],v[68:71],v[172:175]\n"
"v_cvt_pk_bf16_f32 v91,v119,v120\n"
"v_or3_b32 v118,v118,v87,v88\n"
"v_mfma_f32_16x16x32_bf16 v[204:207],a[108:111],v[72:75],v[204:207]\n"
"v_exp_f32 v123,v57\n"
"v_mfma_f32_16x16x32_bf16 v[236:239],a[108:111],v[76:79],v[236:239]\n"
"buffer_load_dwordx4 v117,s[24:27],s64 offen offset:0 sc1 lds\n"
"v_mfma_f32_16x16x32_bf16 v[140:143],a[108:111],v[64:67],v[140:143]\n"
"v_exp_f32 v119,v58\n"
"v_mfma_f32_16x16x32_bf16 a[128:131],a[144:147],v[64:67],a[128:131]\n"
"v_cvt_pk_bf16_f32 v92,v122,v123\n"
"v_or3_b32 v118,v118,v89,v90\n"
"v_mfma_f32_16x16x32_bf16 v[144:147],a[112:115],v[64:67],v[144:147]\n"
"v_exp_f32 v120,v59\n"
"v_mfma_f32_16x16x32_bf16 v[176:179],a[112:115],v[68:71],v[176:179]\n"
"buffer_load_dwordx4 v117,s[24:27],s64 offen offset:1024 sc1 lds\n"
"v_mfma_f32_16x16x32_bf16 v[208:211],a[112:115],v[72:75],v[208:211]\n"
"v_exp_f32 v122,v60\n"
"v_mfma_f32_16x16x32_bf16 v[240:243],a[112:115],v[76:79],v[240:243]\n"
"v_cvt_pk_bf16_f32 v93,v119,v120\n"
"v_or3_b32 v118,v118,v91,v92\n"
"v_mfma_f32_16x16x32_bf16 v[244:247],a[116:119],v[76:79],v[244:247]\n"
"v_exp_f32 v123,v61\n"
"v_mfma_f32_16x16x32_bf16 v[148:151],a[116:119],v[64:67],v[148:151]\n"
"buffer_load_dwordx4 v117,s[24:27],s64 offen offset:2048 sc1 lds\n"
"v_mfma_f32_16x16x32_bf16 v[180:183],a[116:119],v[68:71],v[180:183]\n"
"v_exp_f32 v119,v62\n"
"v_mfma_f32_16x16x32_bf16 v[212:215],a[116:119],v[72:75],v[212:215]\n"
"ds_read_b32 v125,v124\n"
"v_cvt_pk_bf16_f32 v94,v122,v123\n"
"v_mfma_f32_16x16x32_bf16 v[216:219],a[120:123],v[72:75],v[216:219]\n"
"v_exp_f32 v120,v63\n"
"v_mfma_f32_16x16x32_bf16 v[248:251],a[120:123],v[76:79],v[248:251]\n"
"buffer_load_dwordx4 v117,s[24:27],s64 offen offset:3072 sc1 lds\n"
"v_mfma_f32_16x16x32_bf16 v[152:155],a[120:123],v[64:67],v[152:155]\n"
"buffer_store_dwordx4 a[248:251],v112,s[24:27],s56 offen offset:2048 sc1\n"
"v_mfma_f32_16x16x32_bf16 v[184:187],a[120:123],v[68:71],v[184:187]\n"
"s_add_u32 s64,s64,16384\n"
"v_cvt_pk_bf16_f32 v95,v119,v120\n"
"v_or3_b32 v118,v118,v93,v94\n"
"v_mfma_f32_16x16x32_bf16 v[188:191],a[124:127],v[68:71],v[188:191]\n"
"s_waitcnt lgkmcnt(0)\n"
"v_subrev_u32 v125,s101,v125\n"
"v_or3_b32 v125,v118,v95,v125\n"
"v_mfma_f32_16x16x32_bf16 v[220:223],a[124:127],v[72:75],v[220:223]\n"
"v_and_b32 v125,0x40004000,v125\n"
"v_cmp_ne_u32 vcc,0,v125\n"
"v_mfma_f32_16x16x32_bf16 v[252:255],a[124:127],v[76:79],v[252:255]\n"
"v_mfma_f32_16x16x32_bf16 v[156:159],a[124:127],v[64:67],v[156:159]\n"
"s_cbranch_vccnz .Lfk_cs_g1\n"
".Lfk_cr_g1:\n"
"v_mfma_f32_16x16x32_bf16 v[32:35],a[64:67],a[0:3],v[96:99]\n"
"s_add_u32 s101,s101,4\n"
"v_exp_f32 v122,v0\n"
"v_mfma_f32_16x16x32_bf16 v[32:35],a[68:71],a[4:7],v[32:35]\n"
"s_add_u32 m0,s55,12288\n"
"v_exp_f32 v123,v1\n"
"v_mfma_f32_16x16x32_bf16 v[32:35],a[72:75],a[8:11],v[32:35]\n"
"ds_read_b128 a[96:99],v114 offset:8192\n"
"ds_read_b128 a[100:103],v114 offset:9216\n"
"v_mfma_f32_16x16x32_bf16 v[32:35],a[76:79],a[12:15],v[32:35]\n"
"s_add_u32 s54,s54,s22\n"
"v_exp_f32 v119,v2\n"
"v_mfma_f32_16x16x32_bf16 v[36:39],a[92:95],a[12:15],v[96:99]\n"
"ds_read_b128 a[104:107],v114 offset:10240\n"
"v_cvt_pk_bf16_f32 v64,v122,v123\n"
"v_mfma_f32_16x16x32_bf16 v[36:39],a[88:91],a[8:11],v[36:39]\n"
"v_exp_f32 v120,v3\n"
"v_mfma_f32_16x16x32_bf16 v[36:39],a[84:87],a[4:7],v[36:39]\n"
"ds_read_b128 a[108:111],v114 offset:11264\n"
"ds_read_b128 a[112:115],v114 offset:12288\n"
"v_mfma_f32_16x16x32_bf16 v[36:39],a[80:83],a[0:3],v[36:39]\n"
"v_exp_f32 v122,v4\n"
"v_mfma_f32_16x16x32_bf16 v[44:47],a[80:83],a[16:19],v[100:103]\n"
"ds_read_b128 a[116:119],v114 offset:13312\n"
"v_cvt_pk_bf16_f32 v65,v119,v120\n"
"v_mfma_f32_16x16x32_bf16 v[44:47],a[84:87],a[20:23],v[44:47]\n"
"v_exp_f32 v123,v5\n"
"v_mfma_f32_16x16x32_bf16 v[44:47],a[88:91],a[24:27],v[44:47]\n"
"ds_read_b128 a[120:123],v114 offset:14336\n"
"ds_read_b128 a[124:127],v114 offset:15360\n"
"v_mfma_f32_16x16x32_bf16 v[44:47],a[92:95],a[28:31],v[44:47]\n"
"v_exp_f32 v119,v6\n"
"v_mfma_f32_16x16x32_bf16 v[40:43],a[76:79],a[28:31],v[100:103]\n"
"v_cvt_pk_bf16_f32 v66,v122,v123\n"
"v_or3_b32 v118,v64,v65,v66\n"
"v_mfma_f32_16x16x32_bf16 v[40:43],a[72:75],a[24:27],v[40:43]\n"
"v_exp_f32 v120,v7\n"
"v_mfma_f32_16x16x32_bf16 v[40:43],a[68:71],a[20:23],v[40:43]\n"
"buffer_load_dwordx4 v115,s[32:35],s54 offen sc1 lds\n"
"v_mfma_f32_16x16x32_bf16 v[40:43],a[64:67],a[16:19],v[40:43]\n"
"s_cmp_eq_u32 s64,s65\n"
"v_exp_f32 v122,v8\n"
"v_mfma_f32_16x16x32_bf16 v[48:51],a[64:67],a[32:35],v[104:107]\n"
"v_cvt_pk_bf16_f32 v67,v119,v120\n"
"s_cselect_b32 s64,s66,s64\n"
"v_mfma_f32_16x16x32_bf16 v[48:51],a[68:71],a[36:39],v[48:51]\n"
"s_add_u32 m0,s67,32768\n"
"v_exp_f32 v123,v9\n"
"v_mfma_f32_16x16x32_bf16 v[48:51],a[72:75],a[40:43],v[48:51]\n"
"buffer_load_dwordx4 v112,s[24:27],s64 offen offset:0 sc1 lds\n"
"v_mfma_f32_16x16x32_bf16 v[48:51],a[76:79],a[44:47],v[48:51]\n"
"v_exp_f32 v119,v10\n"
"v_mfma_f32_16x16x32_bf16 v[52:55],a[92:95],a[44:47],v[104:107]\n"
"v_cvt_pk_bf16_f32 v68,v122,v123\n"
"v_or3_b32 v118,v118,v67,v68\n"
"v_mfma_f32_16x16x32_bf16 v[52:55],a[88:91],a[40:43],v[52:55]\n"
"v_exp_f32 v120,v11\n"
"v_mfma_f32_16x16x32_bf16 v[52:55],a[84:87],a[36:39],v[52:55]\n"
"buffer_load_dwordx4 v112,s[24:27],s64 offen offset:1024 sc1 lds\n"
"v_mfma_f32_16x16x32_bf16 v[52:55],a[80:83],a[32:35],v[52:55]\n"
"v_exp_f32 v122,v12\n"
"v_mfma_f32_16x16x32_bf16 v[60:63],a[80:83],a[48:51],v[108:111]\n"
"v_cvt_pk_bf16_f32 v69,v119,v120\n"
"v_mfma_f32_16x16x32_bf16 v[60:63],a[84:87],a[52:55],v[60:63]\n"
"v_exp_f32 v123,v13\n"
"v_mfma_f32_16x16x32_bf16 v[60:63],a[88:91],a[56:59],v[60:63]\n"
"buffer_load_dwordx4 v112,s[24:27],s64 offen offset:2048 sc1 lds\n"
"v_mfma_f32_16x16x32_bf16 v[60:63],a[92:95],a[60:63],v[60:63]\n"
"v_exp_f32 v119,v14\n"
"v_mfma_f32_16x16x32_bf16 v[56:59],a[76:79],a[60:63],v[108:111]\n"
"v_cvt_pk_bf16_f32 v70,v122,v123\n"
"v_or3_b32 v118,v118,v69,v70\n"
"v_mfma_f32_16x16x32_bf16 v[56:59],a[72:75],a[56:59],v[56:59]\n"
"v_exp_f32 v120,v15\n"
"v_mfma_f32_16x16x32_bf16 v[56:59],a[68:71],a[52:55],v[56:59]\n"
"buffer_load_dwordx4 v112,s[24:27],s64 offen offset:3072 sc1 lds\n"
"v_mfma_f32_16x16x32_bf16 v[56:59],a[64:67],a[48:51],v[56:59]\n"
"s_waitcnt lgkmcnt(0)\n"
"v_exp_f32 v122,v16\n"
"v_mfma_f32_16x16x32_bf16 v[128:131],a[96:99],v[80:83],v[128:131]\n"
"v_exp_f32 v123,v17\n"
"v_mfma_f32_16x16x32_bf16 v[160:163],a[96:99],v[84:87],v[160:163]\n"
"ds_read_b128 a[64:67],v113 offset:0\n"
"v_cvt_pk_bf16_f32 v71,v119,v120\n"
"v_mfma_f32_16x16x32_bf16 v[192:195],a[96:99],v[88:91],v[192:195]\n"
"s_add_u32 m0,s67,36864\n"
"v_exp_f32 v119,v18\n"
"v_mfma_f32_16x16x32_bf16 v[224:227],a[96:99],v[92:95],v[224:227]\n"
"ds_read_b128 a[68:71],v113 offset:1024\n"
"v_cvt_pk_bf16_f32 v72,v122,v123\n"
"v_mfma_f32_16x16x32_bf16 a[140:143],a[144:147],v[92:95],a[140:143]\n"
"v_exp_f32 v120,v19\n"
"v_mfma_f32_16x16x32_bf16 v[228:231],a[100:103],v[92:95],v[228:231]\n"
"ds_read_b128 a[72:75],v113 offset:2048\n"
"ds_read_b128 a[76:79],v113 offset:3072\n"
"v_mfma_f32_16x16x32_bf16 v[132:135],a[100:103],v[80:83],v[132:135]\n"
"v_exp_f32 v122,v20\n"
"v_mfma_f32_16x16x32_bf16 v[164:167],a[100:103],v[84:87],v[164:167]\n"
"ds_read_b128 a[80:83],v113 offset:4096\n"
"v_cvt_pk_bf16_f32 v73,v119,v120\n"
"v_mfma_f32_16x16x32_bf16 v[196:199],a[100:103],v[88:91],v[196:199]\n"
"v_exp_f32 v123,v21\n"
"v_mfma_f32_16x16x32_bf16 a[136:139],a[144:147],v[88:91],a[136:139]\n"
"ds_read_b128 a[84:87],v113 offset:5120\n"
"ds_read_b128 a[88:91],v113 offset:6144\n"
"v_mfma_f32_16x16x32_bf16 v[200:203],a[104:107],v[88:91],v[200:203]\n"
"v_exp_f32 v119,v22\n"
"v_mfma_f32_16x16x32_bf16 v[232:235],a[104:107],v[92:95],v[232:235]\n"
"ds_read_b128 a[92:95],v113 offset:7168\n"
"v_cvt_pk_bf16_f32 v74,v122,v123\n"
"v_mfma_f32_16x16x32_bf16 v[136:139],a[104:107],v[80:83],v[136:139]\n"
"v_exp_f32 v120,v23\n"
"v_mfma_f32_16x16x32_bf16 v[168:171],a[104:107],v[84:87],v[168:171]\n"
"s_waitcnt vmcnt(16)\n"
"ds_add_u32 v124,a152\n"
"v_mfma_f32_16x16x32_bf16 a[132:135],a[144:147],v[84:87],a[132:135]\n"
"v_exp_f32 v122,v24\n"
"v_mfma_f32_16x16x32_bf16 v[172:175],a[108:111],v[84:87],v[172:175]\n"
"v_cvt_pk_bf16_f32 v75,v119,v120\n"
"v_or3_b32 v118,v118,v71,v72\n"
"v_mfma_f32_16x16x32_bf16 v[204:207],a[108:111],v[88:91],v[204:207]\n"
"v_exp_f32 v123,v25\n"
"v_mfma_f32_16x16x32_bf16 v[236:239],a[108:111],v[92:95],v[236:239]\n"
"buffer_load_dwordx4 v117,s[24:27],s64 offen offset:0 sc1 lds\n"
"v_mfma_f32_16x16x32_bf16 v[140:143],a[108:111],v[80:83],v[140:143]\n"
"v_exp_f32 v119,v26\n"
"v_mfma_f32_16x16x32_bf16 a[128:131],a[144:147],v[80:83],a[128:131]\n"
"v_cvt_pk_bf16_f32 v76,v122,v123\n"
"v_or3_b32 v118,v118,v73,v74\n"
"v_mfma_f32_16x16x32_bf16 v[144:147],a[112:115],v[80:83],v[144:147]\n"
"v_exp_f32 v120,v27\n"
"v_mfma_f32_16x16x32_bf16 v[176:179],a[112:115],v[84:87],v[176:179]\n"
"buffer_load_dwordx4 v117,s[24:27],s64 offen offset:1024 sc1 lds\n"
"v_mfma_f32_16x16x32_bf16 v[208:211],a[112:115],v[88:91],v[208:211]\n"
"v_exp_f32 v122,v28\n"
"v_mfma_f32_16x16x32_bf16 v[240:243],a[112:115],v[92:95],v[240:243]\n"
"v_cvt_pk_bf16_f32 v77,v119,v120\n"
"v_or3_b32 v118,v118,v75,v76\n"
"v_mfma_f32_16x16x32_bf16 v[244:247],a[116:119],v[92:95],v[244:247]\n"
"v_exp_f32 v123,v29\n"
"v_mfma_f32_16x16x32_bf16 v[148:151],a[116:119],v[80:83],v[148:151]\n"
"buffer_load_dwordx4 v117,s[24:27],s64 offen offset:2048 sc1 lds\n"
"v_mfma_f32_16x16x32_bf16 v[180:183],a[116:119],v[84:87],v[180:183]\n"
"v_exp_f32 v119,v30\n"
"v_mfma_f32_16x16x32_bf16 v[212:215],a[116:119],v[88:91],v[212:215]\n"
"ds_read_b32 v125,v124\n"
"v_cvt_pk_bf16_f32 v78,v122,v123\n"
"v_mfma_f32_16x16x32_bf16 v[216:219],a[120:123],v[88:91],v[216:219]\n"
"v_exp_f32 v120,v31\n"
"v_mfma_f32_16x16x32_bf16 v[248:251],a[120:123],v[92:95],v[248:251]\n"
"buffer_load_dwordx4 v117,s[24:27],s64 offen offset:3072 sc1 lds\n"
"v_mfma_f32_16x16x32_bf16 v[152:155],a[120:123],v[80:83],v[152:155]\n"
"buffer_store_dwordx4 a[252:255],v112,s[24:27],s56 offen offset:3072 sc1\n"
"v_mfma_f32_16x16x32_bf16 v[184:187],a[120:123],v[84:87],v[184:187]\n"
"s_add_u32 s64,s64,16384\n"
"s_add_u32 s89,s89,1\n"
"v_cvt_pk_bf16_f32 v79,v119,v120\n"
"v_mfma_f32_16x16x32_bf16 v[188:191],a[124:127],v[84:87],v[188:191]\n"
"s_waitcnt lgkmcnt(0)\n"
"v_subrev_u32 v125,s101,v125\n"
"v_or3_b32 v118,v118,v77,v78\n"
"v_mfma_f32_16x16x32_bf16 v[220:223],a[124:127],v[88:91],v[220:223]\n"
"v_or3_b32 v125,v118,v79,v125\n"
"v_and_b32 v125,0x40004000,v125\n"
"v_mfma_f32_16x16x32_bf16 v[252:255],a[124:127],v[92:95],v[252:255]\n"
"v_cmp_ne_u32 vcc,0,v125\n"
"v_mfma_f32_16x16x32_bf16 v[156:159],a[124:127],v[80:83],v[156:159]\n"
"s_cbranch_vccnz .Lfk_cs_g2\n"
".Lfk_cr_g2:\n"
".Lfk_Hx:\n"
"s_cmp_lt_u32 s89,8\n"
"s_cbranch_scc1 .Lfk_H\n"
"v_max3_f32 v118,v32,v33,v34\n"
"v_max3_f32 v119,v35,v36,v37\n"
"v_max3_f32 v119,v119,v38,v39\n"
"v_max3_f32 v118,v118,v40,v41\n"
"v_max3_f32 v119,v119,v42,v43\n"
"v_max3_f32 v118,v118,v44,v45\n"
"v_max3_f32 v119,v119,v46,v47\n"
"v_max3_f32 v118,v118,v48,v49\n"
"v_max3_f32 v119,v119,v50,v51\n"
"v_max3_f32 v118,v118,v52,v53\n"
"v_max3_f32 v119,v119,v54,v55\n"
"v_max3_f32 v118,v118,v56,v57\n"
"v_max3_f32 v119,v119,v58,v59\n"
"v_max3_f32 v118,v118,v60,v61\n"
"v_max3_f32 v119,v119,v62,v63\n"
"v_max_f32 v118,v118,v119\n"
"v_cmp_lt_f32 vcc,s93,v118\n"
"s_nop 0\n"
"s_cbranch_vccnz .Lfk_cs_x1\n"
".Lfk_cr_x1:\n"
"s_cmp_ge_u32 s52,2\n"
"s_cbranch_scc1 .Lfk_tailB\n"
".Lfk_tailA:\n"
"s_add_u32 s82,s66,28672\n"
"s_cmp_eq_u32 s52,0\n"
"s_cselect_b32 s67,77824,s67\n"
"s_cselect_b32 s64,s82,s64\n"
"s_waitcnt vmcnt(11) lgkmcnt(0)\n"
"s_barrier\n"
"v_mfma_f32_16x16x32_bf16 v[16:19],a[64:67],a[32:35],v[104:107]\n"
"ds_read_b128 a[96:99],v114 offset:24576\n"
"ds_read_b128 a[100:103],v114 offset:25600\n"
"ds_read_b128 a[104:107],v114 offset:26624\n"
"v_mfma_f32_16x16x32_bf16 v[16:19],a[68:71],a[36:39],v[16:19]\n"
"ds_read_b128 a[108:111],v114 offset:27648\n"
"ds_read_b128 a[112:115],v114 offset:28672\n"
"ds_read_b128 a[116:119],v114 offset:29696\n"
"v_mfma_f32_16x16x32_bf16 v[16:19],a[72:75],a[40:43],v[16:19]\n"
"ds_read_b128 a[120:123],v114 offset:30720\n"
"ds_read_b128 a[124:127],v114 offset:31744\n"
"v_add_f32 v8,0x80000000,v126\n"
"v_mfma_f32_16x16x32_bf16 v[16:19],a[76:79],a[44:47],v[16:19]\n"
"v_min_f32 v8,v8,v100\n"
"v_add_f32 v9,0xf149f2ca,v126\n"
"v_min_f32 v9,v9,v101\n"
"v_mfma_f32_16x16x32_bf16 v[20:23],a[92:95],a[44:47],v[104:107]\n"
"v_add_f32 v10,0xf1c9f2ca,v126\n"
"v_min_f32 v10,v10,v102\n"
"v_add_f32 v11,0xf2177617,v126\n"
"v_mfma_f32_16x16x32_bf16 v[20:23],a[88:91],a[40:43],v[20:23]\n"
"v_min_f32 v11,v11,v103\n"
"v_add_f32 v12,0xf349f2ca,v126\n"
"v_min_f32 v12,v12,v100\n"
"v_mfma_f32_16x16x32_bf16 v[20:23],a[84:87],a[36:39],v[20:23]\n"
"v_add_f32 v13,0xf35691f6,v126\n"
"v_min_f32 v13,v13,v101\n"
"v_add_f32 v14,0xf3633123,v126\n"
"v_mfma_f32_16x16x32_bf16 v[20:23],a[80:83],a[32:35],v[20:23]\n"
"v_min_f32 v14,v14,v102\n"
"v_add_f32 v15,0xf36fd050,v126\n"
"v_min_f32 v15,v15,v103\n"
"v_mfma_f32_16x16x32_bf16 v[28:31],a[80:83],a[48:51],v[108:111]\n"
"s_add_u32 m0,s67,0\n"
"v_exp_f32 v32,v32\n"
"v_mfma_f32_16x16x32_bf16 v[28:31],a[84:87],a[52:55],v[28:31]\n"
"buffer_load_dwordx4 v112,s[24:27],s64 offen offset:0 sc1 lds\n"
"buffer_load_dwordx4 v112,s[24:27],s64 offen offset:1024 sc1 lds\n"
"v_exp_f32 v33,v33\n"
"v_mfma_f32_16x16x32_bf16 v[28:31],a[88:91],a[56:59],v[28:31]\n"
"v_exp_f32 v34,v34\n"
"v_cvt_pk_bf16_f32 v80,v32,v33\n"
"v_mfma_f32_16x16x32_bf16 v[28:31],a[92:95],a[60:63],v[28:31]\n"
"v_exp_f32 v35,v35\n"
"v_max3_f32 v118,v16,v17,v18\n"
"v_exp_f32 v36,v36\n"
"v_mfma_f32_16x16x32_bf16 v[24:27],a[76:79],a[60:63],v[108:111]\n"
"buffer_load_dwordx4 v112,s[24:27],s64 offen offset:2048 sc1 lds\n"
"v_mfma_f32_16x16x32_bf16 v[24:27],a[72:75],a[56:59],v[24:27]\n"
"v_exp_f32 v37,v37\n"
"v_cvt_pk_bf16_f32 v81,v34,v35\n"
"v_mfma_f32_16x16x32_bf16 v[24:27],a[68:71],a[52:55],v[24:27]\n"
"v_exp_f32 v38,v38\n"
"v_cvt_pk_bf16_f32 v82,v36,v37\n"
"v_exp_f32 v39,v39\n"
"v_mfma_f32_16x16x32_bf16 v[24:27],a[64:67],a[48:51],v[24:27]\n"
"buffer_load_dwordx4 v112,s[24:27],s64 offen offset:3072 sc1 lds\n"
"v_mfma_f32_16x16x32_bf16 v[8:11],a[64:67],a[16:19],v[8:11]\n"
"s_add_u32 s64,s64,16384\n"
"v_exp_f32 v40,v40\n"
"v_accvgpr_read_b32 v119,a150\n"
"v_mfma_f32_16x16x32_bf16 v[8:11],a[68:71],a[20:23],v[8:11]\n"
"v_exp_f32 v41,v41\n"
"v_accvgpr_read_b32 v120,a151\n"
"v_mfma_f32_16x16x32_bf16 v[8:11],a[72:75],a[24:27],v[8:11]\n"
"v_exp_f32 v42,v42\n"
"v_xor_b32 v119,s80,v119\n"
"v_mfma_f32_16x16x32_bf16 v[8:11],a[76:79],a[28:31],v[8:11]\n"
"v_exp_f32 v43,v43\n"
"v_xor_b32 v120,s81,v120\n"
"v_mfma_f32_16x16x32_bf16 v[12:15],a[92:95],a[28:31],v[12:15]\n"
"v_exp_f32 v44,v44\n"
"v_or_b32 v119,v119,v120\n"
"v_mfma_f32_16x16x32_bf16 v[12:15],a[88:91],a[24:27],v[12:15]\n"
"s_add_u32 s18,s8,0\n"
"v_exp_f32 v45,v45\n"
"v_cmp_ne_u32 s[14:15],0,v119\n"
"v_mfma_f32_16x16x32_bf16 v[12:15],a[84:87],a[20:23],v[12:15]\n"
"s_cmp_lg_u32 s14,0\n"
"s_cselect_b32 s18,2146435072,s18\n"
"v_exp_f32 v46,v46\n"
"v_mfma_f32_16x16x32_bf16 v[12:15],a[80:83],a[16:19],v[12:15]\n"
"s_add_u32 m0,s30,0\n"
"ds_read_b128 a[64:67],v113 offset:16384\n"
"s_waitcnt lgkmcnt(1)\n"
"v_mfma_f32_16x16x32_bf16 v[128:131],a[96:99],v[64:67],v[128:131]\n"
"ds_read_b128 a[68:71],v113 offset:17408\n"
"ds_read_b128 a[72:75],v113 offset:18432\n"
"ds_read_b128 a[76:79],v113 offset:19456\n"
"v_mfma_f32_16x16x32_bf16 v[160:163],a[96:99],v[68:71],v[160:163]\n"
"ds_read_b128 a[80:83],v113 offset:20480\n"
"v_exp_f32 v47,v47\n"
"v_mfma_f32_16x16x32_bf16 v[192:195],a[96:99],v[72:75],v[192:195]\n"
"ds_read_b128 a[84:87],v113 offset:21504\n"
"ds_read_b128 a[88:91],v113 offset:22528\n"
"ds_read_b128 a[92:95],v113 offset:23552\n"
"v_exp_f32 v48,v48\n"
"v_mfma_f32_16x16x32_bf16 v[224:227],a[96:99],v[76:79],v[224:227]\n"
"buffer_load_dwordx4 v112,s[24:27],s18 offen offset:0 sc1 lds\n"
"v_exp_f32 v49,v49\n"
"v_mfma_f32_16x16x32_bf16 a[140:143],a[144:147],v[76:79],a[140:143]\n"
"buffer_load_dwordx4 v112,s[24:27],s18 offen offset:1024 sc1 lds\n"
"v_mfma_f32_16x16x32_bf16 v[228:231],a[100:103],v[76:79],v[228:231]\n"
"buffer_load_dwordx4 v112,s[24:27],s18 offen offset:2048 sc1 lds\n"
"buffer_load_dwordx4 v112,s[24:27],s18 offen offset:3072 sc1 lds\n"
"v_mfma_f32_16x16x32_bf16 v[132:135],a[100:103],v[64:67],v[132:135]\n"
"s_add_u32 m0,m0,4096\n"
"v_exp_f32 v50,v50\n"
"v_cvt_pk_bf16_f32 v83,v38,v39\n"
"v_mfma_f32_16x16x32_bf16 v[164:167],a[100:103],v[68:71],v[164:167]\n"
"buffer_load_dwordx4 v117,s[24:27],s18 offen offset:0 sc1 lds\n"
"v_cvt_pk_bf16_f32 v84,v40,v41\n"
"v_cvt_pk_bf16_f32 v85,v42,v43\n"
"v_mfma_f32_16x16x32_bf16 v[196:199],a[100:103],v[72:75],v[196:199]\n"
"v_exp_f32 v51,v51\n"
"v_cvt_pk_bf16_f32 v86,v44,v45\n"
"v_mfma_f32_16x16x32_bf16 a[136:139],a[144:147],v[72:75],a[136:139]\n"
"v_exp_f32 v52,v52\n"
"v_cvt_pk_bf16_f32 v87,v46,v47\n"
"v_mfma_f32_16x16x32_bf16 v[200:203],a[104:107],v[72:75],v[200:203]\n"
"buffer_load_dwordx4 v117,s[24:27],s18 offen offset:1024 sc1 lds\n"
"v_cvt_pk_bf16_f32 v88,v48,v49\n"
"v_cvt_pk_bf16_f32 v89,v50,v51\n"
"v_mfma_f32_16x16x32_bf16 v[232:235],a[104:107],v[76:79],v[232:235]\n"
"v_exp_f32 v53,v53\n"
"v_max3_f32 v118,v118,v19,v20\n"
"v_mfma_f32_16x16x32_bf16 v[136:139],a[104:107],v[64:67],v[136:139]\n"
"v_cvt_pk_bf16_f32 v90,v52,v53\n"
"v_max3_f32 v118,v118,v21,v22\n"
"v_max3_f32 v118,v118,v23,v28\n"
"v_exp_f32 v54,v54\n"
"v_mfma_f32_16x16x32_bf16 v[168:171],a[104:107],v[68:71],v[168:171]\n"
"buffer_load_dwordx4 v117,s[24:27],s18 offen offset:2048 sc1 lds\n"
"v_mfma_f32_16x16x32_bf16 a[132:135],a[144:147],v[68:71],a[132:135]\n"
"v_exp_f32 v55,v55\n"
"v_max3_f32 v118,v118,v29,v30\n"
"v_mfma_f32_16x16x32_bf16 v[172:175],a[108:111],v[68:71],v[172:175]\n"
"v_cvt_pk_bf16_f32 v91,v54,v55\n"
"v_max3_f32 v118,v118,v31,v24\n"
"v_max3_f32 v118,v118,v25,v26\n"
"v_exp_f32 v56,v56\n"
"v_mfma_f32_16x16x32_bf16 v[204:207],a[108:111],v[72:75],v[204:207]\n"
"buffer_load_dwordx4 v117,s[24:27],s18 offen offset:3072 sc1 lds\n"
"v_mfma_f32_16x16x32_bf16 v[236:239],a[108:111],v[76:79],v[236:239]\n"
"s_sub_u32 s82,s3,32768\n"
"v_max3_f32 v118,v118,v27,v8\n"
"v_max3_f32 v118,v118,v9,v10\n"
"v_mfma_f32_16x16x32_bf16 v[140:143],a[108:111],v[64:67],v[140:143]\n"
"v_exp_f32 v57,v57\n"
"v_max3_f32 v118,v118,v11,v12\n"
"v_exp_f32 v58,v58\n"
"v_mfma_f32_16x16x32_bf16 a[128:131],a[144:147],v[64:67],a[128:131]\n"
"buffer_load_dwordx4 a[0:3],v112,s[24:27],s82 offen offset:0 sc1\n"
"v_mfma_f32_16x16x32_bf16 v[144:147],a[112:115],v[64:67],v[144:147]\n"
"v_max3_f32 v118,v118,v13,v14\n"
"v_max_f32 v118,v118,v15\n"
"v_mfma_f32_16x16x32_bf16 v[176:179],a[112:115],v[68:71],v[176:179]\n"
"v_exp_f32 v59,v59\n"
"v_mfma_f32_16x16x32_bf16 v[208:211],a[112:115],v[72:75],v[208:211]\n"
"v_cvt_pk_bf16_f32 v92,v56,v57\n"
"v_cvt_pk_bf16_f32 v93,v58,v59\n"
"v_exp_f32 v60,v60\n"
"v_mfma_f32_16x16x32_bf16 v[240:243],a[112:115],v[76:79],v[240:243]\n"
"buffer_load_dwordx4 a[4:7],v112,s[24:27],s82 offen offset:1024 sc1\n"
"v_mfma_f32_16x16x32_bf16 v[244:247],a[116:119],v[76:79],v[244:247]\n"
"v_exp_f32 v61,v61\n"
"v_mfma_f32_16x16x32_bf16 v[148:151],a[116:119],v[64:67],v[148:151]\n"
"v_cmp_lt_f32 vcc,s93,v118\n"
"v_cvt_pk_bf16_f32 v94,v60,v61\n"
"v_mfma_f32_16x16x32_bf16 v[180:183],a[116:119],v[68:71],v[180:183]\n"
"buffer_load_dwordx4 a[8:11],v112,s[24:27],s82 offen offset:2048 sc1\n"
"v_mfma_f32_16x16x32_bf16 v[212:215],a[116:119],v[72:75],v[212:215]\n"
"v_exp_f32 v62,v62\n"
"v_mfma_f32_16x16x32_bf16 v[216:219],a[120:123],v[72:75],v[216:219]\n"
"v_exp_f32 v63,v63\n"
"v_mfma_f32_16x16x32_bf16 v[248:251],a[120:123],v[76:79],v[248:251]\n"
"v_cvt_pk_bf16_f32 v95,v62,v63\n"
"v_mfma_f32_16x16x32_bf16 v[152:155],a[120:123],v[64:67],v[152:155]\n"
"buffer_load_dwordx4 a[12:15],v112,s[24:27],s82 offen offset:3072 sc1\n"
"v_mfma_f32_16x16x32_bf16 v[184:187],a[120:123],v[68:71],v[184:187]\n"
"v_mfma_f32_16x16x32_bf16 v[188:191],a[124:127],v[68:71],v[188:191]\n"
"v_mfma_f32_16x16x32_bf16 v[220:223],a[124:127],v[72:75],v[220:223]\n"
"v_mfma_f32_16x16x32_bf16 v[252:255],a[124:127],v[76:79],v[252:255]\n"
"v_mfma_f32_16x16x32_bf16 v[156:159],a[124:127],v[64:67],v[156:159]\n"
"s_cbranch_vccnz .Lfk_cs_a3\n"
".Lfk_cr_a3:\n"
"s_waitcnt vmcnt(17) lgkmcnt(0)\n"
"s_barrier\n"
"v_mfma_f32_16x16x32_bf16 v[56:59],a[64:67],a[48:51],v[108:111]\n"
"ds_read_b128 a[96:99],v114 offset:57344\n"
"ds_read_b128 a[100:103],v114 offset:58368\n"
"ds_read_b128 a[104:107],v114 offset:59392\n"
"v_mfma_f32_16x16x32_bf16 v[56:59],a[68:71],a[52:55],v[56:59]\n"
"ds_read_b128 a[108:111],v114 offset:60416\n"
"ds_read_b128 a[112:115],v114 offset:61440\n"
"ds_read_b128 a[116:119],v114 offset:62464\n"
"v_mfma_f32_16x16x32_bf16 v[56:59],a[72:75],a[56:59],v[56:59]\n"
"ds_read_b128 a[120:123],v114 offset:63488\n"
"ds_read_b128 a[124:127],v114 offset:64512\n"
"v_add_f32 v48,0x80000000,v126\n"
"v_mfma_f32_16x16x32_bf16 v[56:59],a[76:79],a[60:63],v[56:59]\n"
"v_min_f32 v48,v48,v104\n"
"v_add_f32 v49,0xf149f2ca,v126\n"
"v_min_f32 v49,v49,v105\n"
"v_mfma_f32_16x16x32_bf16 v[60:63],a[92:95],a[60:63],v[108:111]\n"
"v_add_f32 v50,0xf1c9f2ca,v126\n"
"v_add_f32 v51,0xf2177617,v126\n"
"v_mfma_f32_16x16x32_bf16 v[60:63],a[88:91],a[56:59],v[60:63]\n"
"v_add_f32 v52,0xf349f2ca,v126\n"
"v_min_f32 v52,v52,v104\n"
"v_mfma_f32_16x16x32_bf16 v[60:63],a[84:87],a[52:55],v[60:63]\n"
"v_add_f32 v53,0xf35691f6,v126\n"
"v_min_f32 v53,v53,v105\n"
"v_add_f32 v54,0xf3633123,v126\n"
"v_mfma_f32_16x16x32_bf16 v[60:63],a[80:83],a[48:51],v[60:63]\n"
"v_min_f32 v54,v54,v106\n"
"v_add_f32 v55,0xf36fd050,v126\n"
"v_min_f32 v55,v55,v107\n"
"v_min_f32 v51,v51,v107\n"
"v_min_f32 v50,v50,v106\n"
"v_mfma_f32_16x16x32_bf16 v[52:55],a[80:83],a[32:35],v[52:55]\n"
"s_add_u32 m0,s67,16384\n"
"v_exp_f32 v8,v8\n"
"v_mfma_f32_16x16x32_bf16 v[52:55],a[84:87],a[36:39],v[52:55]\n"
"buffer_load_dwordx4 v112,s[24:27],s64 offen offset:0 sc1 lds\n"
"buffer_load_dwordx4 v112,s[24:27],s64 offen offset:1024 sc1 lds\n"
"v_exp_f32 v9,v9\n"
"v_mfma_f32_16x16x32_bf16 v[52:55],a[88:91],a[40:43],v[52:55]\n"
"v_exp_f32 v10,v10\n"
"v_cvt_pk_bf16_f32 v68,v8,v9\n"
"v_mfma_f32_16x16x32_bf16 v[52:55],a[92:95],a[44:47],v[52:55]\n"
"v_exp_f32 v11,v11\n"
"v_max3_f32 v118,v56,v57,v58\n"
"v_exp_f32 v12,v12\n"
"v_mfma_f32_16x16x32_bf16 v[48:51],a[76:79],a[44:47],v[48:51]\n"
"buffer_load_dwordx4 v112,s[24:27],s64 offen offset:2048 sc1 lds\n"
"v_mfma_f32_16x16x32_bf16 v[48:51],a[72:75],a[40:43],v[48:51]\n"
"v_exp_f32 v13,v13\n"
"v_cvt_pk_bf16_f32 v69,v10,v11\n"
"v_mfma_f32_16x16x32_bf16 v[48:51],a[68:71],a[36:39],v[48:51]\n"
"v_exp_f32 v14,v14\n"
"v_cvt_pk_bf16_f32 v70,v12,v13\n"
"v_exp_f32 v15,v15\n"
"v_mfma_f32_16x16x32_bf16 v[48:51],a[64:67],a[32:35],v[48:51]\n"
"ds_read_b128 a[64:67],v113 offset:49152\n"
"s_waitcnt lgkmcnt(1)\n"
"v_mfma_f32_16x16x32_bf16 v[128:131],a[96:99],v[80:83],v[128:131]\n"
"ds_read_b128 a[68:71],v113 offset:50176\n"
"v_exp_f32 v16,v16\n"
"v_mfma_f32_16x16x32_bf16 v[160:163],a[96:99],v[84:87],v[160:163]\n"
"ds_read_b128 a[72:75],v113 offset:51200\n"
"v_exp_f32 v17,v17\n"
"v_mfma_f32_16x16x32_bf16 v[192:195],a[96:99],v[88:91],v[192:195]\n"
"ds_read_b128 a[76:79],v113 offset:52224\n"
"ds_read_b128 a[80:83],v113 offset:53248\n"
"ds_read_b128 a[84:87],v113 offset:54272\n"
"v_mfma_f32_16x16x32_bf16 v[224:227],a[96:99],v[92:95],v[224:227]\n"
"ds_read_b128 a[88:91],v113 offset:55296\n"
"v_exp_f32 v18,v18\n"
"v_mfma_f32_16x16x32_bf16 a[140:143],a[144:147],v[92:95],a[140:143]\n"
"ds_read_b128 a[92:95],v113 offset:56320\n"
"buffer_load_dwordx4 v112,s[24:27],s64 offen offset:3072 sc1 lds\n"
"s_add_u32 s64,s64,16384\n"
"v_mfma_f32_16x16x32_bf16 v[228:231],a[100:103],v[92:95],v[228:231]\n"
"s_sub_u32 s82,s3,49152\n"
"v_exp_f32 v19,v19\n"
"v_mfma_f32_16x16x32_bf16 v[132:135],a[100:103],v[80:83],v[132:135]\n"
"buffer_load_dwordx4 a[16:19],v112,s[24:27],s82 offen offset:0 sc1\n"
"v_cvt_pk_bf16_f32 v71,v14,v15\n"
"v_cvt_pk_bf16_f32 v72,v16,v17\n"
"v_cvt_pk_bf16_f32 v73,v18,v19\n"
"v_exp_f32 v20,v20\n"
"v_mfma_f32_16x16x32_bf16 v[164:167],a[100:103],v[84:87],v[164:167]\n"
"buffer_load_dwordx4 a[20:23],v112,s[24:27],s82 offen offset:1024 sc1\n"
"v_mfma_f32_16x16x32_bf16 v[196:199],a[100:103],v[88:91],v[196:199]\n"
"v_exp_f32 v21,v21\n"
"v_max3_f32 v118,v118,v59,v60\n"
"v_mfma_f32_16x16x32_bf16 a[136:139],a[144:147],v[88:91],a[136:139]\n"
"v_cvt_pk_bf16_f32 v74,v20,v21\n"
"v_max3_f32 v118,v118,v61,v62\n"
"v_max3_f32 v118,v118,v63,v52\n"
"v_mfma_f32_16x16x32_bf16 v[200:203],a[104:107],v[88:91],v[200:203]\n"
"v_exp_f32 v22,v22\n"
"v_max3_f32 v118,v118,v53,v54\n"
"v_mfma_f32_16x16x32_bf16 v[232:235],a[104:107],v[92:95],v[232:235]\n"
"buffer_load_dwordx4 a[24:27],v112,s[24:27],s82 offen offset:2048 sc1\n"
"v_max3_f32 v118,v118,v55,v48\n"
"v_max3_f32 v118,v118,v49,v50\n"
"v_mfma_f32_16x16x32_bf16 v[136:139],a[104:107],v[80:83],v[136:139]\n"
"v_exp_f32 v23,v23\n"
"v_max_f32 v118,v118,v51\n"
"v_mfma_f32_16x16x32_bf16 v[168:171],a[104:107],v[84:87],v[168:171]\n"
"v_cvt_pk_bf16_f32 v75,v22,v23\n"
"v_cmp_lt_f32 vcc,s93,v118\n"
"v_mfma_f32_16x16x32_bf16 a[132:135],a[144:147],v[84:87],a[132:135]\n"
"buffer_load_dwordx4 a[28:31],v112,s[24:27],s82 offen offset:3072 sc1\n"
"v_mfma_f32_16x16x32_bf16 v[172:175],a[108:111],v[84:87],v[172:175]\n"
"v_exp_f32 v24,v24\n"
"v_mfma_f32_16x16x32_bf16 v[204:207],a[108:111],v[88:91],v[204:207]\n"
"s_sub_u32 s82,s3,65536\n"
"v_exp_f32 v25,v25\n"
"v_mfma_f32_16x16x32_bf16 v[236:239],a[108:111],v[92:95],v[236:239]\n"
"v_cvt_pk_bf16_f32 v76,v24,v25\n"
"v_mfma_f32_16x16x32_bf16 v[140:143],a[108:111],v[80:83],v[140:143]\n"
"buffer_load_dwordx4 v[32:35],v112,s[24:27],s82 offen offset:0 sc1\n"
"v_mfma_f32_16x16x32_bf16 a[128:131],a[144:147],v[80:83],a[128:131]\n"
"v_exp_f32 v26,v26\n"
"v_mfma_f32_16x16x32_bf16 v[144:147],a[112:115],v[80:83],v[144:147]\n"
"v_exp_f32 v27,v27\n"
"v_mfma_f32_16x16x32_bf16 v[176:179],a[112:115],v[84:87],v[176:179]\n"
"buffer_load_dwordx4 v[36:39],v112,s[24:27],s82 offen offset:1024 sc1\n"
"v_mfma_f32_16x16x32_bf16 v[208:211],a[112:115],v[88:91],v[208:211]\n"
"v_exp_f32 v28,v28\n"
"v_mfma_f32_16x16x32_bf16 v[240:243],a[112:115],v[92:95],v[240:243]\n"
"v_cvt_pk_bf16_f32 v77,v26,v27\n"
"v_mfma_f32_16x16x32_bf16 v[244:247],a[116:119],v[92:95],v[244:247]\n"
"v_exp_f32 v29,v29\n"
"v_mfma_f32_16x16x32_bf16 v[148:151],a[116:119],v[80:83],v[148:151]\n"
"buffer_load_dwordx4 v[40:43],v112,s[24:27],s82 offen offset:2048 sc1\n"
"v_mfma_f32_16x16x32_bf16 v[180:183],a[116:119],v[84:87],v[180:183]\n"
"v_exp_f32 v30,v30\n"
"v_mfma_f32_16x16x32_bf16 v[212:215],a[116:119],v[88:91],v[212:215]\n"
"v_cvt_pk_bf16_f32 v78,v28,v29\n"
"v_mfma_f32_16x16x32_bf16 v[216:219],a[120:123],v[88:91],v[216:219]\n"
"v_exp_f32 v31,v31\n"
"v_mfma_f32_16x16x32_bf16 v[248:251],a[120:123],v[92:95],v[248:251]\n"
"v_cvt_pk_bf16_f32 v79,v30,v31\n"
"v_mfma_f32_16x16x32_bf16 v[152:155],a[120:123],v[80:83],v[152:155]\n"
"buffer_load_dwordx4 v[44:47],v112,s[24:27],s82 offen offset:3072 sc1\n"
"v_mfma_f32_16x16x32_bf16 v[184:187],a[120:123],v[84:87],v[184:187]\n"
"v_mfma_f32_16x16x32_bf16 v[188:191],a[124:127],v[84:87],v[188:191]\n"
"v_mfma_f32_16x16x32_bf16 v[220:223],a[124:127],v[88:91],v[220:223]\n"
"v_mfma_f32_16x16x32_bf16 v[252:255],a[124:127],v[92:95],v[252:255]\n"
"v_mfma_f32_16x16x32_bf16 v[156:159],a[124:127],v[80:83],v[156:159]\n"
"s_cbranch_vccnz .Lfk_cs_a0\n"
".Lfk_cr_a0:\n"
"s_load_dwordx2 s[36:37],s[0:1],0\n"
"s_load_dwordx2 s[44:45],s[0:1],80\n"
"s_load_dwordx2 s[48:49],s[0:1],88\n"
"s_lshl_b32 s82,s78,2\n"
"s_add_u32 s82,s82,65536\n"
"v_add_u32 v113,s82,v112\n"
"s_lshr_b32 s82,s78,2\n"
"s_add_u32 s82,s82,122880\n"
"v_lshrrev_b32 v115,1,v112\n"
"v_add_u32 v116,s82,v115\n"
"s_waitcnt lgkmcnt(0)\n"
"s_barrier\n"
"v_add_f32 v24,0x80000000,v126\n"
"v_min_f32 v24,v24,v108\n"
"v_add_f32 v25,0xf149f2ca,v126\n"
"v_min_f32 v25,v25,v109\n"
"v_add_f32 v26,0xf1c9f2ca,v126\n"
"v_min_f32 v26,v26,v110\n"
"v_add_f32 v27,0xf2177617,v126\n"
"v_min_f32 v27,v27,v111\n"
"v_add_f32 v28,0xf349f2ca,v126\n"
"v_min_f32 v28,v28,v108\n"
"v_add_f32 v29,0xf35691f6,v126\n"
"v_min_f32 v29,v29,v109\n"
"v_add_f32 v30,0xf3633123,v126\n"
"v_min_f32 v30,v30,v110\n"
"v_add_f32 v31,0xf36fd050,v126\n"
"v_min_f32 v31,v31,v111\n"
"v_exp_f32 v48,v48\n"
"v_mfma_f32_16x16x32_bf16 v[24:27],a[64:67],a[48:51],v[24:27]\n"
"s_add_u32 s83,s18,2097152\n"
"ds_read_b128 a[96:99],v114 offset:40960\n"
"ds_read_b128 a[100:103],v114 offset:41984\n"
"v_exp_f32 v49,v49\n"
"v_mfma_f32_16x16x32_bf16 v[24:27],a[68:71],a[52:55],v[24:27]\n"
"s_add_u32 m0,s30,32768\n"
"ds_read_b128 a[104:107],v114 offset:43008\n"
"ds_read_b128 a[108:111],v114 offset:44032\n"
"v_mfma_f32_16x16x32_bf16 v[24:27],a[72:75],a[56:59],v[24:27]\n"
"ds_read_b128 a[112:115],v114 offset:45056\n"
"ds_read_b128 a[116:119],v114 offset:46080\n"
"ds_read_b128 a[120:123],v114 offset:47104\n"
"ds_read_b128 a[124:127],v114 offset:48128\n"
"v_exp_f32 v50,v50\n"
"v_mfma_f32_16x16x32_bf16 v[24:27],a[76:79],a[60:63],v[24:27]\n"
"buffer_load_dwordx4 v112,s[24:27],s83 offen offset:0 sc1 lds\n"
"v_cvt_pk_bf16_f32 v88,v48,v49\n"
"v_mfma_f32_16x16x32_bf16 v[28:31],a[92:95],a[60:63],v[28:31]\n"
"buffer_load_dwordx4 v112,s[24:27],s83 offen offset:1024 sc1 lds\n"
"v_accvgpr_read_b32 v97,a128\n"
"v_cvt_pk_bf16_f32 v128,v128,v129\n"
"v_cvt_pk_bf16_f32 v129,v130,v131\n"
"v_mfma_f32_16x16x32_bf16 v[28:31],a[88:91],a[56:59],v[28:31]\n"
"v_exp_f32 v51,v51\n"
"v_cvt_pk_bf16_f32 v130,v132,v133\n"
"v_cvt_pk_bf16_f32 v131,v134,v135\n"
"v_cvt_pk_bf16_f32 v89,v50,v51\n"
"v_mfma_f32_16x16x32_bf16 v[28:31],a[84:87],a[52:55],v[28:31]\n"
"ds_write_b128 v113,v[128:131] offset:0\n"
"v_cvt_pk_bf16_f32 v136,v136,v137\n"
"v_cvt_pk_bf16_f32 v137,v138,v139\n"
"v_exp_f32 v52,v52\n"
"v_mfma_f32_16x16x32_bf16 v[28:31],a[80:83],a[48:51],v[28:31]\n"
"buffer_load_dwordx4 v112,s[24:27],s83 offen offset:2048 sc1 lds\n"
"v_max3_f32 v118,v24,v25,v26\n"
"s_waitcnt lgkmcnt(1)\n"
"v_mfma_f32_16x16x32_bf16 v[160:163],a[96:99],v[68:71],v[160:163]\n"
"v_cvt_pk_bf16_f32 v138,v140,v141\n"
"v_cvt_pk_bf16_f32 v139,v142,v143\n"
"v_cvt_pk_bf16_f32 v144,v144,v145\n"
"v_mfma_f32_16x16x32_bf16 a[132:135],a[144:147],v[68:71],a[132:135]\n"
"ds_write_b128 v113,v[136:139] offset:1024\n"
"buffer_load_dwordx4 v112,s[24:27],s83 offen offset:3072 sc1 lds\n"
"v_exp_f32 v53,v53\n"
"v_cvt_pk_bf16_f32 v145,v146,v147\n"
"v_mfma_f32_16x16x32_bf16 v[164:167],a[100:103],v[68:71],v[164:167]\n"
"s_add_u32 m0,m0,4096\n"
"v_exp_f32 v54,v54\n"
"v_cvt_pk_bf16_f32 v90,v52,v53\n"
"v_cvt_pk_bf16_f32 v146,v148,v149\n"
"v_mfma_f32_16x16x32_bf16 v[168:171],a[104:107],v[68:71],v[168:171]\n"
"v_max3_f32 v118,v118,v27,v28\n"
"v_max3_f32 v118,v118,v29,v30\n"
"v_max_f32 v118,v118,v31\n"
"v_cmp_lt_f32 vcc,s93,v118\n"
"v_exp_f32 v55,v55\n"
"v_mfma_f32_16x16x32_bf16 v[172:175],a[108:111],v[68:71],v[172:175]\n"
"buffer_load_dwordx4 v117,s[24:27],s83 offen offset:0 sc1 lds\n"
"v_cvt_pk_bf16_f32 v147,v150,v151\n"
"v_cvt_pk_bf16_f32 v91,v54,v55\n"
"v_mfma_f32_16x16x32_bf16 v[176:179],a[112:115],v[68:71],v[176:179]\n"
"ds_write_b128 v113,v[144:147] offset:2048\n"
"v_cvt_pk_bf16_f32 v152,v152,v153\n"
"v_cvt_pk_bf16_f32 v153,v154,v155\n"
"v_exp_f32 v56,v56\n"
"v_mfma_f32_16x16x32_bf16 v[180:183],a[116:119],v[68:71],v[180:183]\n"
"buffer_load_dwordx4 v117,s[24:27],s83 offen offset:1024 sc1 lds\n"
"v_cvt_pk_bf16_f32 v154,v156,v157\n"
"v_cvt_pk_bf16_f32 v155,v158,v159\n"
"v_mfma_f32_16x16x32_bf16 v[184:187],a[120:123],v[68:71],v[184:187]\n"
"ds_write_b128 v113,v[152:155] offset:3072\n"
"ds_write_b64 v116,v[96:97] offset:0\n"
"v_mfma_f32_16x16x32_bf16 v[188:191],a[124:127],v[68:71],v[188:191]\n"
"v_exp_f32 v57,v57\n"
"v_mfma_f32_16x16x32_bf16 v[192:195],a[96:99],v[72:75],v[192:195]\n"
"buffer_load_dwordx4 v117,s[24:27],s83 offen offset:2048 sc1 lds\n"
"v_cvt_pk_bf16_f32 v92,v56,v57\n"
"v_mfma_f32_16x16x32_bf16 a[136:139],a[144:147],v[72:75],a[136:139]\n"
"v_exp_f32 v58,v58\n"
"v_exp_f32 v59,v59\n"
"v_mfma_f32_16x16x32_bf16 v[196:199],a[100:103],v[72:75],v[196:199]\n"
"buffer_load_dwordx4 v117,s[24:27],s83 offen offset:3072 sc1 lds\n"
"v_accvgpr_read_b32 v101,a132\n"
"v_mfma_f32_16x16x32_bf16 v[200:203],a[104:107],v[72:75],v[200:203]\n"
"s_sub_u32 s82,s3,81920\n"
"v_cvt_pk_bf16_f32 v93,v58,v59\n"
"v_cvt_pk_bf16_f32 v160,v160,v161\n"
"v_cvt_pk_bf16_f32 v161,v162,v163\n"
"v_mfma_f32_16x16x32_bf16 v[204:207],a[108:111],v[72:75],v[204:207]\n"
"v_exp_f32 v60,v60\n"
"v_cvt_pk_bf16_f32 v162,v164,v165\n"
"v_cvt_pk_bf16_f32 v163,v166,v167\n"
"v_mfma_f32_16x16x32_bf16 v[208:211],a[112:115],v[72:75],v[208:211]\n"
"buffer_load_dwordx4 a[32:35],v112,s[24:27],s82 offen offset:0 sc1\n"
"ds_write_b128 v113,v[160:163] offset:4096\n"
"v_cvt_pk_bf16_f32 v168,v168,v169\n"
"v_cvt_pk_bf16_f32 v169,v170,v171\n"
"v_mfma_f32_16x16x32_bf16 v[212:215],a[116:119],v[72:75],v[212:215]\n"
"v_exp_f32 v61,v61\n"
"v_cvt_pk_bf16_f32 v170,v172,v173\n"
"v_cvt_pk_bf16_f32 v171,v174,v175\n"
"v_cvt_pk_bf16_f32 v94,v60,v61\n"
"v_mfma_f32_16x16x32_bf16 v[216:219],a[120:123],v[72:75],v[216:219]\n"
"ds_write_b128 v113,v[168:171] offset:5120\n"
"v_cvt_pk_bf16_f32 v176,v176,v177\n"
"v_cvt_pk_bf16_f32 v177,v178,v179\n"
"v_exp_f32 v62,v62\n"
"v_mfma_f32_16x16x32_bf16 v[220:223],a[124:127],v[72:75],v[220:223]\n"
"buffer_load_dwordx4 a[36:39],v112,s[24:27],s82 offen offset:1024 sc1\n"
"v_cvt_pk_bf16_f32 v178,v180,v181\n"
"v_cvt_pk_bf16_f32 v179,v182,v183\n"
"v_mfma_f32_16x16x32_bf16 v[224:227],a[96:99],v[76:79],v[224:227]\n"
"ds_write_b128 v113,v[176:179] offset:6144\n"
"v_cvt_pk_bf16_f32 v184,v184,v185\n"
"v_cvt_pk_bf16_f32 v185,v186,v187\n"
"v_mfma_f32_16x16x32_bf16 a[140:143],a[144:147],v[76:79],a[140:143]\n"
"v_exp_f32 v63,v63\n"
"v_cvt_pk_bf16_f32 v186,v188,v189\n"
"v_cvt_pk_bf16_f32 v187,v190,v191\n"
"v_mfma_f32_16x16x32_bf16 v[228:231],a[100:103],v[76:79],v[228:231]\n"
"buffer_load_dwordx4 a[40:43],v112,s[24:27],s82 offen offset:2048 sc1\n"
"ds_write_b128 v113,v[184:187] offset:7168\n"
"v_mfma_f32_16x16x32_bf16 v[232:235],a[104:107],v[76:79],v[232:235]\n"
"ds_write_b64 v116,v[100:101] offset:512\n"
"v_cvt_pk_bf16_f32 v95,v62,v63\n"
"v_mfma_f32_16x16x32_bf16 v[236:239],a[108:111],v[76:79],v[236:239]\n"
"buffer_load_dwordx4 a[44:47],v112,s[24:27],s82 offen offset:3072 sc1\n"
"v_mfma_f32_16x16x32_bf16 v[240:243],a[112:115],v[76:79],v[240:243]\n"
"s_sub_u32 s82,s3,131072\n"
"buffer_load_dwordx4 v[128:131],v112,s[24:27],s82 offen offset:0 sc1\n"
"buffer_load_dwordx4 v[132:135],v112,s[24:27],s82 offen offset:1024 sc1\n"
"buffer_load_dwordx4 v[136:139],v112,s[24:27],s82 offen offset:2048 sc1\n"
"buffer_load_dwordx4 v[140:143],v112,s[24:27],s82 offen offset:3072 sc1\n"
"v_mfma_f32_16x16x32_bf16 v[244:247],a[116:119],v[76:79],v[244:247]\n"
"s_sub_u32 s82,s3,147456\n"
"buffer_load_dwordx4 v[144:147],v112,s[24:27],s82 offen offset:0 sc1\n"
"buffer_load_dwordx4 v[148:151],v112,s[24:27],s82 offen offset:1024 sc1\n"
"v_mfma_f32_16x16x32_bf16 v[248:251],a[120:123],v[76:79],v[248:251]\n"
"buffer_load_dwordx4 v[152:155],v112,s[24:27],s82 offen offset:2048 sc1\n"
"buffer_load_dwordx4 v[156:159],v112,s[24:27],s82 offen offset:3072 sc1\n"
"v_mfma_f32_16x16x32_bf16 v[252:255],a[124:127],v[76:79],v[252:255]\n"
"s_cbranch_vccnz .Lfk_cs_a1\n"
".Lfk_cr_a1:\n"
"s_waitcnt vmcnt(28) lgkmcnt(0)\n"
"s_barrier\n"
"ds_read_b128 a[96:99],v114 offset:8192\n"
"ds_read_b128 a[100:103],v114 offset:9216\n"
"ds_read_b128 a[104:107],v114 offset:10240\n"
"ds_read_b128 a[108:111],v114 offset:11264\n"
"ds_read_b128 a[112:115],v114 offset:12288\n"
"ds_read_b128 a[116:119],v114 offset:13312\n"
"ds_read_b128 a[120:123],v114 offset:14336\n"
"ds_read_b128 a[124:127],v114 offset:15360\n"
"v_xor_b32 v113,32768,v113\n"
"v_xor_b32 v116,2048,v116\n"
"ds_read_b64 v[98:99],v116 offset:0\n"
"ds_read_b128 a[64:67],v113 offset:0\n"
"ds_read_b128 a[68:71],v113 offset:1024\n"
"ds_read_b128 a[72:75],v113 offset:2048\n"
"ds_read_b128 a[76:79],v113 offset:3072\n"
"ds_read_b64 v[102:103],v116 offset:512\n"
"ds_read_b128 a[80:83],v113 offset:4096\n"
"ds_read_b128 a[84:87],v113 offset:5120\n"
"ds_read_b128 a[88:91],v113 offset:6144\n"
"ds_read_b128 a[92:95],v113 offset:7168\n"
"v_exp_f32 v24,v24\n"
"v_exp_f32 v25,v25\n"
"v_exp_f32 v26,v26\n"
"v_exp_f32 v27,v27\n"
"v_exp_f32 v28,v28\n"
"v_exp_f32 v29,v29\n"
"v_exp_f32 v30,v30\n"
"v_exp_f32 v31,v31\n"
"v_cvt_pk_bf16_f32 v76,v24,v25\n"
"v_cvt_pk_bf16_f32 v77,v26,v27\n"
"v_cvt_pk_bf16_f32 v78,v28,v29\n"
"v_cvt_pk_bf16_f32 v79,v30,v31\n"
"s_waitcnt lgkmcnt(10)\n"
"v_mfma_f32_16x16x32_bf16 a[136:139],a[144:147],v[88:91],a[136:139]\n"
"s_sub_u32 s82,s3,163840\n"
"v_lshrrev_b32 v70,4,v112\n"
"v_and_b32 v71,15,v70\n"
"v_mfma_f32_16x16x32_bf16 a[140:143],a[144:147],v[92:95],a[140:143]\n"
"buffer_load_dwordx4 v[0:3],v112,s[24:27],s82 offen offset:0 sc1\n"
"v_lshrrev_b32 v70,4,v70\n"
"v_lshlrev_b32 v70,2,v70\n"
"v_mfma_f32_16x16x32_bf16 v[192:195],a[96:99],v[88:91],v[192:195]\n"
"buffer_load_dwordx4 v[4:7],v112,s[24:27],s82 offen offset:1024 sc1\n"
"v_sub_u32 v70,v71,v70\n"
"v_mov_b32 v64,0\n"
"v_mfma_f32_16x16x32_bf16 v[224:227],a[96:99],v[92:95],v[224:227]\n"
"ds_read_b128 a[96:99],v114 offset:24576\n"
"buffer_load_dwordx4 v[8:11],v112,s[24:27],s82 offen offset:2048 sc1\n"
"v_mfma_f32_16x16x32_bf16 v[196:199],a[100:103],v[88:91],v[196:199]\n"
"buffer_load_dwordx4 v[12:15],v112,s[24:27],s82 offen offset:3072 sc1\n"
"v_mov_b32 v69,0\n"
"v_mfma_f32_16x16x32_bf16 v[228:231],a[100:103],v[92:95],v[228:231]\n"
"s_sub_u32 s82,s3,180224\n"
"ds_read_b128 a[100:103],v114 offset:25600\n"
"v_mov_b32 v71,0x3f80\n"
"v_mfma_f32_16x16x32_bf16 v[200:203],a[104:107],v[88:91],v[200:203]\n"
"buffer_load_dwordx4 v[160:163],v112,s[24:27],s82 offen offset:0 sc1\n"
"v_cmp_eq_u32 vcc,0,v70\n"
"v_mov_b32 v68,0\n"
"v_mov_b32 v65,0\n"
"v_cndmask_b32 v66,0,v71,vcc\n"
"v_cmp_eq_u32 vcc,1,v70\n"
"v_mfma_f32_16x16x32_bf16 v[232:235],a[104:107],v[92:95],v[232:235]\n"
"ds_read_b128 a[104:107],v114 offset:26624\n"
"buffer_load_dwordx4 v[164:167],v112,s[24:27],s82 offen offset:1024 sc1\n"
"v_mov_b32 v72,0x3f800000\n"
"v_cndmask_b32 v66,v66,v72,vcc\n"
"v_cmp_eq_u32 vcc,2,v70\n"
"v_mfma_f32_16x16x32_bf16 v[204:207],a[108:111],v[88:91],v[204:207]\n"
"buffer_load_dwordx4 v[168:171],v112,s[24:27],s82 offen offset:2048 sc1\n"
"v_cndmask_b32 v67,0,v71,vcc\n"
"v_cmp_eq_u32 vcc,3,v70\n"
"v_mfma_f32_16x16x32_bf16 v[236:239],a[108:111],v[92:95],v[236:239]\n"
"ds_read_b128 a[108:111],v114 offset:27648\n"
"buffer_load_dwordx4 v[172:175],v112,s[24:27],s82 offen offset:3072 sc1\n"
"v_mfma_f32_16x16x32_bf16 v[208:211],a[112:115],v[88:91],v[208:211]\n"
"v_cndmask_b32 v67,v67,v72,vcc\n"
"s_sub_u32 s82,s3,196608\n"
"v_mfma_f32_16x16x32_bf16 v[240:243],a[112:115],v[92:95],v[240:243]\n"
"ds_read_b128 a[112:115],v114 offset:28672\n"
"buffer_load_dwordx4 v[176:179],v112,s[24:27],s82 offen offset:0 sc1\n"
"v_mfma_f32_16x16x32_bf16 v[212:215],a[116:119],v[88:91],v[212:215]\n"
"buffer_load_dwordx4 v[180:183],v112,s[24:27],s82 offen offset:1024 sc1\n"
"v_mfma_f32_16x16x32_bf16 v[244:247],a[116:119],v[92:95],v[244:247]\n"
"ds_read_b128 a[116:119],v114 offset:29696\n"
"buffer_load_dwordx4 v[184:187],v112,s[24:27],s82 offen offset:2048 sc1\n"
"v_mfma_f32_16x16x32_bf16 v[216:219],a[120:123],v[88:91],v[216:219]\n"
"buffer_load_dwordx4 v[188:191],v112,s[24:27],s82 offen offset:3072 sc1\n"
"v_mfma_f32_16x16x32_bf16 v[248:251],a[120:123],v[92:95],v[248:251]\n"
"ds_read_b128 a[120:123],v114 offset:30720\n"
"v_mfma_f32_16x16x32_bf16 v[220:223],a[124:127],v[88:91],v[220:223]\n"
"v_mfma_f32_16x16x32_bf16 v[252:255],a[124:127],v[92:95],v[252:255]\n"
"ds_read_b128 a[124:127],v114 offset:31744\n"
"s_waitcnt lgkmcnt(13)\n"
"v_cmp_neq_f32 vcc,v98,v104\n"
"s_nop 0\n"
"s_cbranch_vccnz .Lfk_ms_a0\n"
".Lfk_mr_a0:\n"
"v_mfma_f32_16x16x32_bf16 v[192:195],v[66:69],a[64:67],v[192:195]\n"
"v_mfma_f32_16x16x32_bf16 v[196:199],v[64:67],a[64:67],v[196:199]\n"
"v_mfma_f32_16x16x32_bf16 v[200:203],v[66:69],a[68:71],v[200:203]\n"
"v_mfma_f32_16x16x32_bf16 v[204:207],v[64:67],a[68:71],v[204:207]\n"
"v_mfma_f32_16x16x32_bf16 v[208:211],v[66:69],a[72:75],v[208:211]\n"
"v_mfma_f32_16x16x32_bf16 v[212:215],v[64:67],a[72:75],v[212:215]\n"
"v_mfma_f32_16x16x32_bf16 v[216:219],v[66:69],a[76:79],v[216:219]\n"
"v_mfma_f32_16x16x32_bf16 v[220:223],v[64:67],a[76:79],v[220:223]\n"
"s_waitcnt lgkmcnt(0)\n"
"v_mfma_f32_16x16x32_bf16 a[140:143],a[144:147],v[76:79],a[140:143]\n"
"v_mfma_f32_16x16x32_bf16 v[224:227],a[96:99],v[76:79],v[224:227]\n"
"v_mfma_f32_16x16x32_bf16 v[228:231],a[100:103],v[76:79],v[228:231]\n"
"v_accvgpr_read_b32 v122,a136\n"
"v_add_f32 v122,v122,v99\n"
"v_mfma_f32_16x16x32_bf16 v[232:235],a[104:107],v[76:79],v[232:235]\n"
"v_rcp_f32 v122,v122\n"
"v_mfma_f32_16x16x32_bf16 v[236:239],a[108:111],v[76:79],v[236:239]\n"
"v_pk_mul_f32 v[192:193],v[192:193],v[122:123] op_sel_hi:[1,0]\n"
"v_pk_mul_f32 v[194:195],v[194:195],v[122:123] op_sel_hi:[1,0]\n"
"v_pk_mul_f32 v[196:197],v[196:197],v[122:123] op_sel_hi:[1,0]\n"
"v_pk_mul_f32 v[198:199],v[198:199],v[122:123] op_sel_hi:[1,0]\n"
"v_mfma_f32_16x16x32_bf16 v[240:243],a[112:115],v[76:79],v[240:243]\n"
"v_cvt_pk_bf16_f32 v192,v192,v193\n"
"v_cvt_pk_bf16_f32 v193,v194,v195\n"
"v_mfma_f32_16x16x32_bf16 v[244:247],a[116:119],v[76:79],v[244:247]\n"
"v_cvt_pk_bf16_f32 v194,v196,v197\n"
"v_cvt_pk_bf16_f32 v195,v198,v199\n"
"v_mfma_f32_16x16x32_bf16 v[248:251],a[120:123],v[76:79],v[248:251]\n"
"buffer_store_dwordx4 v[192:195],v112,s[24:27],s70 offen offset:0 sc1\n"
"v_pk_mul_f32 v[200:201],v[200:201],v[122:123] op_sel_hi:[1,0]\n"
"v_pk_mul_f32 v[202:203],v[202:203],v[122:123] op_sel_hi:[1,0]\n"
"v_pk_mul_f32 v[204:205],v[204:205],v[122:123] op_sel_hi:[1,0]\n"
"v_pk_mul_f32 v[206:207],v[206:207],v[122:123] op_sel_hi:[1,0]\n"
"v_mfma_f32_16x16x32_bf16 v[252:255],a[124:127],v[76:79],v[252:255]\n"
"v_cvt_pk_bf16_f32 v200,v200,v201\n"
"v_cvt_pk_bf16_f32 v201,v202,v203\n"
"v_cmp_neq_f32 vcc,v102,v108\n"
"s_nop 0\n"
"s_cbranch_vccnz .Lfk_ms_a1\n"
".Lfk_mr_a1:\n"
"v_mfma_f32_16x16x32_bf16 v[224:227],v[66:69],a[80:83],v[224:227]\n"
"v_cvt_pk_bf16_f32 v202,v204,v205\n"
"v_cvt_pk_bf16_f32 v203,v206,v207\n"
"v_mfma_f32_16x16x32_bf16 v[228:231],v[64:67],a[80:83],v[228:231]\n"
"buffer_store_dwordx4 v[200:203],v112,s[24:27],s70 offen offset:1024 sc1\n"
"v_pk_mul_f32 v[208:209],v[208:209],v[122:123] op_sel_hi:[1,0]\n"
"v_pk_mul_f32 v[210:211],v[210:211],v[122:123] op_sel_hi:[1,0]\n"
"v_pk_mul_f32 v[212:213],v[212:213],v[122:123] op_sel_hi:[1,0]\n"
"v_pk_mul_f32 v[214:215],v[214:215],v[122:123] op_sel_hi:[1,0]\n"
"v_mfma_f32_16x16x32_bf16 v[232:235],v[66:69],a[84:87],v[232:235]\n"
"v_cvt_pk_bf16_f32 v208,v208,v209\n"
"v_cvt_pk_bf16_f32 v209,v210,v211\n"
"v_mfma_f32_16x16x32_bf16 v[236:239],v[64:67],a[84:87],v[236:239]\n"
"v_cvt_pk_bf16_f32 v210,v212,v213\n"
"v_cvt_pk_bf16_f32 v211,v214,v215\n"
"v_mfma_f32_16x16x32_bf16 v[240:243],v[66:69],a[88:91],v[240:243]\n"
"buffer_store_dwordx4 v[208:211],v112,s[24:27],s70 offen offset:2048 sc1\n"
"v_pk_mul_f32 v[216:217],v[216:217],v[122:123] op_sel_hi:[1,0]\n"
"v_pk_mul_f32 v[218:219],v[218:219],v[122:123] op_sel_hi:[1,0]\n"
"v_pk_mul_f32 v[220:221],v[220:221],v[122:123] op_sel_hi:[1,0]\n"
"v_pk_mul_f32 v[222:223],v[222:223],v[122:123] op_sel_hi:[1,0]\n"
"v_mfma_f32_16x16x32_bf16 v[244:247],v[64:67],a[88:91],v[244:247]\n"
"v_cvt_pk_bf16_f32 v216,v216,v217\n"
"v_cvt_pk_bf16_f32 v217,v218,v219\n"
"v_mfma_f32_16x16x32_bf16 v[248:251],v[66:69],a[92:95],v[248:251]\n"
"v_cvt_pk_bf16_f32 v218,v220,v221\n"
"v_cvt_pk_bf16_f32 v219,v222,v223\n"
"v_mfma_f32_16x16x32_bf16 v[252:255],v[64:67],a[92:95],v[252:255]\n"
"buffer_store_dwordx4 v[216:219],v112,s[24:27],s70 offen offset:3072 sc1\n"
"s_add_u32 s70,s70,262144\n"
"s_branch .Lfk_p1\n"
".Lfk_look:\n"
"s_lshr_b64 s[28:29],s[14:15],s21\n"
"s_and_b32 s83,s28,s19\n"
"s_cbranch_scc0 .Lfk_lk1\n"
"s_mov_b32 s84,0x40000\n"
"s_waitcnt vmcnt(10)\n"
".Lfk_lk0:\n"
"v_xor_b32 v119,s80,v122\n"
"v_xor_b32 v120,s81,v123\n"
"v_or_b32 v119,v119,v120\n"
"v_cmp_ne_u32 s[14:15],0,v119\n"
"s_nop 0\n"
"s_lshr_b64 s[28:29],s[14:15],s21\n"
"s_and_b32 s83,s28,s19\n"
"s_cbranch_scc0 .Lfk_lk1\n"
"s_sleep 2\n"
"buffer_load_dwordx2 v[122:123],v115,s[24:27],s11 offen sc1\n"
"s_waitcnt vmcnt(0)\n"
"s_sub_u32 s84,s84,1\n"
"s_cbranch_scc0 .Lfk_lk0\n"
".Lfk_lk1:\n"
"s_andn2_b32 s28,s28,s19\n"
"s_lshl_b64 s[14:15],s[28:29],s21\n"
"s_lshl_b32 s83,s19,8\n"
"s_and_b32 s83,s28,s83\n"
"s_cbranch_scc0 .Lfk_lk2\n"
"buffer_load_dwordx2 v[122:123],v115,s[24:27],s11 offen sc1\n"
".Lfk_lk2:\n"
"s_nop 1\n"
"s_setpc_b64 s[94:95]\n"
".Lfk_meet4:\n"
"s_mov_b32 s84,0x1000000\n"
".Lfk_meetl4:\n"
"ds_read_b32 v125,v114\n"
"s_sub_u32 s84,s84,1\n"
"s_cbranch_scc1 .Lfk_meetx4\n"
"s_waitcnt lgkmcnt(0)\n"
"v_cmp_gt_u32 vcc,s16,v125\n"
"s_nop 0\n"
"s_cbranch_vccnz .Lfk_meetl4\n"
".Lfk_meetx4:\n"
"s_setpc_b64 s[94:95]\n"
".Lfk_es_lkone:\n"
"s_call_b64 s[94:95],.Lfk_look\n"
"s_branch .Lfk_er_lkone\n"
".Lfk_z0:\n"
"s_call_b64 s[94:95],.Lfk_meet4\n"
"v_xor_b32 v119,s80,v104\n"
"v_xor_b32 v120,s81,v105\n"
"v_or_b32 v119,v119,v120\n"
"v_xor_b32 v120,s80,v106\n"
"v_or_b32 v119,v119,v120\n"
"v_xor_b32 v120,s81,v107\n"
"v_or_b32 v119,v119,v120\n"
"v_xor_b32 v120,s80,v108\n"
"v_or_b32 v119,v119,v120\n"
"v_xor_b32 v120,s81,v109\n"
"v_or_b32 v119,v119,v120\n"
"v_xor_b32 v120,s80,v110\n"
"v_or_b32 v119,v119,v120\n"
"v_xor_b32 v120,s81,v111\n"
"v_or_b32 v119,v119,v120\n"
"v_cmp_ne_u32 vcc,0,v119\n"
"s_nop 0\n"
"s_cbranch_vccz .Lfk_z1\n"
"s_mov_b32 s84,0x40000\n"
"s_mov_b32 s83,148946944\n"
"buffer_load_dwordx4 v[104:107],v112,s[24:27],s83 offen offset:0 sc1\n"
"buffer_load_dwordx4 v[108:111],v112,s[24:27],s83 offen offset:1024 sc1\n"
"s_waitcnt vmcnt(0)\n"
".Lfk_w0:\n"
"v_xor_b32 v119,s80,v104\n"
"v_xor_b32 v120,s81,v105\n"
"v_or_b32 v119,v119,v120\n"
"v_xor_b32 v120,s80,v106\n"
"v_or_b32 v119,v119,v120\n"
"v_xor_b32 v120,s81,v107\n"
"v_or_b32 v119,v119,v120\n"
"v_xor_b32 v120,s80,v108\n"
"v_or_b32 v119,v119,v120\n"
"v_xor_b32 v120,s81,v109\n"
"v_or_b32 v119,v119,v120\n"
"v_xor_b32 v120,s80,v110\n"
"v_or_b32 v119,v119,v120\n"
"v_xor_b32 v120,s81,v111\n"
"v_or_b32 v119,v119,v120\n"
"v_cmp_ne_u32 vcc,0,v119\n"
"s_nop 0\n"
"s_cbranch_vccz .Lfk_w0_x\n"
"s_sleep 8\n"
"s_mov_b64 exec,vcc\n"
"s_mov_b32 s83,148946944\n"
"buffer_load_dwordx4 v[104:107],v112,s[24:27],s83 offen offset:0 sc1\n"
"buffer_load_dwordx4 v[108:111],v112,s[24:27],s83 offen offset:1024 sc1\n"
"s_mov_b64 exec,-1\n"
"s_sub_u32 s84,s84,1\n"
"s_waitcnt vmcnt(0)\n"
"s_cbranch_scc0 .Lfk_w0\n"
".Lfk_w0_x:\n"
"s_nop 1\n"
"s_branch .Lfk_z1\n"
".Lfk_es_lkonem:\n"
"s_call_b64 s[94:95],.Lfk_look\n"
"s_branch .Lfk_er_lkonem\n"
".Lfk_es_mone:\n"
"s_call_b64 s[94:95],.Lfk_meet4\n"
"s_branch .Lfk_er_mone\n"
".Lfk_es_lktwom:\n"
"s_call_b64 s[94:95],.Lfk_look\n"
"s_branch .Lfk_er_lktwom\n"
".Lfk_es_mtwo:\n"
"s_call_b64 s[94:95],.Lfk_meet4\n"
"s_branch .Lfk_er_mtwo\n"
".Lfk_es_lkloopm:\n"
"s_call_b64 s[94:95],.Lfk_look\n"
"s_branch .Lfk_er_lkloopm\n"
".Lfk_es_me3:\n"
"s_call_b64 s[94:95],.Lfk_meet4\n"
"s_branch .Lfk_er_me3\n"
".Lfk_es_me2:\n"
"s_call_b64 s[94:95],.Lfk_meet4\n"
"s_branch .Lfk_er_me2\n"
".Lfk_p1:\n"
"s_cmp_eq_u32 s18,2146435072\n"
"s_cbranch_scc1 .Lfk_p2\n"
".Lfk_p3:\n"
"s_lshr_b32 s22,s8,2\n"
"s_and_b32 s31,s8,0xfff1ffff\n"
"s_add_u32 s18,s18,16384\n"
"s_andn2_b32 s91,s55,16384\n"
"v_add_u32 v116,s91,v112\n"
"v_accvgpr_read_b32 v122,a140\n"
"v_add_f32 v122,v122,v103\n"
"v_rcp_f32 v122,v122\n"
"s_nop 0\n"
"v_pk_mul_f32 v[224:225],v[224:225],v[122:123] op_sel_hi:[1,0]\n"
"v_pk_mul_f32 v[226:227],v[226:227],v[122:123] op_sel_hi:[1,0]\n"
"v_pk_mul_f32 v[228:229],v[228:229],v[122:123] op_sel_hi:[1,0]\n"
"v_pk_mul_f32 v[230:231],v[230:231],v[122:123] op_sel_hi:[1,0]\n"
"v_pk_mul_f32 v[232:233],v[232:233],v[122:123] op_sel_hi:[1,0]\n"
"v_pk_mul_f32 v[234:235],v[234:235],v[122:123] op_sel_hi:[1,0]\n"
"v_pk_mul_f32 v[236:237],v[236:237],v[122:123] op_sel_hi:[1,0]\n"
"v_pk_mul_f32 v[238:239],v[238:239],v[122:123] op_sel_hi:[1,0]\n"
"v_pk_mul_f32 v[240:241],v[240:241],v[122:123] op_sel_hi:[1,0]\n"
"v_pk_mul_f32 v[242:243],v[242:243],v[122:123] op_sel_hi:[1,0]\n"
"v_pk_mul_f32 v[244:245],v[244:245],v[122:123] op_sel_hi:[1,0]\n"
"v_pk_mul_f32 v[246:247],v[246:247],v[122:123] op_sel_hi:[1,0]\n"
"v_pk_mul_f32 v[248:249],v[248:249],v[122:123] op_sel_hi:[1,0]\n"
"v_pk_mul_f32 v[250:251],v[250:251],v[122:123] op_sel_hi:[1,0]\n"
"v_pk_mul_f32 v[252:253],v[252:253],v[122:123] op_sel_hi:[1,0]\n"
"v_pk_mul_f32 v[254:255],v[254:255],v[122:123] op_sel_hi:[1,0]\n"
"s_waitcnt vmcnt(24) lgkmcnt(0)\n"
"s_barrier\n"
"ds_read_b128 v[192:195],v112 offset:0\n"
"ds_read_b128 v[196:199],v112 offset:1024\n"
"ds_read_b128 v[200:203],v112 offset:2048\n"
"ds_read_b128 v[204:207],v112 offset:3072\n"
"ds_read_b128 v[208:211],v112 offset:4096\n"
"ds_read_b128 v[212:215],v112 offset:5120\n"
"ds_read_b128 v[216:219],v112 offset:6144\n"
"ds_read_b128 v[220:223],v112 offset:7168\n"
"v_cvt_pk_bf16_f32 v224,v224,v225\n"
"v_cvt_pk_bf16_f32 v225,v226,v227\n"
"v_cvt_pk_bf16_f32 v226,v228,v229\n"
"v_cvt_pk_bf16_f32 v227,v230,v231\n"
"v_cvt_pk_bf16_f32 v232,v232,v233\n"
"v_cvt_pk_bf16_f32 v233,v234,v235\n"
"v_cvt_pk_bf16_f32 v234,v236,v237\n"
"v_cvt_pk_bf16_f32 v235,v238,v239\n"
"v_cvt_pk_bf16_f32 v240,v240,v241\n"
"v_cvt_pk_bf16_f32 v241,v242,v243\n"
"v_cvt_pk_bf16_f32 v242,v244,v245\n"
"v_cvt_pk_bf16_f32 v243,v246,v247\n"
"v_cvt_pk_bf16_f32 v248,v248,v249\n"
"v_cvt_pk_bf16_f32 v249,v250,v251\n"
"v_cvt_pk_bf16_f32 v250,v252,v253\n"
"v_cvt_pk_bf16_f32 v251,v254,v255\n"
"buffer_store_dwordx4 v[224:227],v112,s[24:27],s70 offen offset:0 sc1\n"
"buffer_store_dwordx4 v[232:235],v112,s[24:27],s70 offen offset:1024 sc1\n"
"buffer_store_dwordx4 v[240:243],v112,s[24:27],s70 offen offset:2048 sc1\n"
"buffer_store_dwordx4 v[248:251],v112,s[24:27],s70 offen offset:3072 sc1\n"
"s_waitcnt lgkmcnt(4)\n"
"v_mfma_f32_16x16x32_bf16 v[80:83],a[224:227],v[192:195],0\n"
"v_mfma_f32_16x16x32_bf16 v[80:83],a[232:235],v[196:199],v[80:83]\n"
"ds_read_b128 v[224:227],v112 offset:8192\n"
"ds_read_b128 v[228:231],v112 offset:9216\n"
"v_mfma_f32_16x16x32_bf16 v[80:83],a[240:243],v[200:203],v[80:83]\n"
"ds_read_b128 v[232:235],v112 offset:10240\n"
"ds_read_b128 v[236:239],v112 offset:11264\n"
"v_mfma_f32_16x16x32_bf16 v[80:83],a[248:251],v[204:207],v[80:83]\n"
"ds_read_b128 v[240:243],v112 offset:12288\n"
"ds_read_b128 v[244:247],v112 offset:13312\n"
"v_mfma_f32_16x16x32_bf16 v[84:87],a[252:255],v[204:207],0\n"
"ds_read_b128 v[248:251],v112 offset:14336\n"
"ds_read_b128 v[252:255],v112 offset:15360\n"
"v_mfma_f32_16x16x32_bf16 v[84:87],a[244:247],v[200:203],v[84:87]\n"
"ds_read_b128 a[68:71],v116 offset:4096\n"
"ds_read_b128 a[72:75],v116 offset:8192\n"
"v_mfma_f32_16x16x32_bf16 v[84:87],a[236:239],v[196:199],v[84:87]\n"
"ds_read_b128 a[76:79],v116 offset:12288\n"
"ds_read_b128 a[80:83],v116 offset:0\n"
"v_mfma_f32_16x16x32_bf16 v[84:87],a[228:231],v[192:195],v[84:87]\n"
"ds_read_b128 a[84:87],v116 offset:20480\n"
"ds_read_b128 a[88:91],v116 offset:24576\n"
"s_waitcnt lgkmcnt(14)\n"
"v_mfma_f32_16x16x32_bf16 v[84:87],a[4:7],v[208:211],v[84:87]\n"
"ds_read_b128 a[92:95],v116 offset:28672\n"
"v_mfma_f32_16x16x32_bf16 v[84:87],a[12:15],v[212:215],v[84:87]\n"
"ds_read_b128 v[204:207],v112 offset:19456\n"
"s_lshl_b32 s91,s78,3\n"
"v_mfma_f32_16x16x32_bf16 v[84:87],a[20:23],v[216:219],v[84:87]\n"
"ds_read_b128 v[200:203],v112 offset:18432\n"
"s_add_u32 s91,s91,131072\n"
"v_mfma_f32_16x16x32_bf16 v[84:87],a[28:31],v[220:223],v[84:87]\n"
"ds_read_b128 v[196:199],v112 offset:17408\n"
"v_lshrrev_b32 v101,2,v112\n"
"v_mfma_f32_16x16x32_bf16 v[80:83],a[24:27],v[220:223],v[80:83]\n"
"ds_read_b128 v[192:195],v112 offset:16384\n"
"v_add_u32 v101,s91,v101\n"
"v_mfma_f32_16x16x32_bf16 v[80:83],a[16:19],v[216:219],v[80:83]\n"
"ds_read_b128 v[220:223],v112 offset:23552\n"
"v_lshrrev_b32 v114,2,v112\n"
"v_mfma_f32_16x16x32_bf16 v[80:83],a[8:11],v[212:215],v[80:83]\n"
"ds_read_b128 v[216:219],v112 offset:22528\n"
"v_add_u32 v114,131328,v114\n"
"v_mfma_f32_16x16x32_bf16 v[80:83],a[0:3],v[208:211],v[80:83]\n"
"ds_read_b128 v[212:215],v112 offset:21504\n"
"s_waitcnt lgkmcnt(15)\n"
"v_mfma_f32_16x16x32_bf16 v[80:83],v[32:35],v[224:227],v[80:83]\n"
"ds_read_b128 v[208:211],v112 offset:20480\n"
"s_mov_b32 s83,148946944\n"
"v_mfma_f32_16x16x32_bf16 v[80:83],v[40:43],v[228:231],v[80:83]\n"
"buffer_load_dwordx4 v[104:107],v112,s[24:27],s83 offen offset:0 sc1\n"
"v_mov_b32 v116,0\n"
"v_mfma_f32_16x16x32_bf16 v[80:83],a[32:35],v[232:235],v[80:83]\n"
"ds_write_b32 v101,v116 offset:256\n"
"v_mov_b32 v116,1\n"
"v_mfma_f32_16x16x32_bf16 v[80:83],a[40:43],v[236:239],v[80:83]\n"
"buffer_load_dwordx4 v[108:111],v112,s[24:27],s83 offen offset:1024 sc1\n"
"v_mfma_f32_16x16x32_bf16 v[84:87],a[44:47],v[236:239],v[84:87]\n"
"s_mov_b32 s16,4\n"
"ds_read_b128 v[236:239],v112 offset:27648\n"
"v_add_u32 v113,65536,v112\n"
"v_mfma_f32_16x16x32_bf16 v[84:87],a[36:39],v[232:235],v[84:87]\n"
"ds_read_b128 v[232:235],v112 offset:26624\n"
"s_add_u32 s31,s31,8388608\n"
"v_mfma_f32_16x16x32_bf16 v[84:87],v[44:47],v[228:231],v[84:87]\n"
"ds_read_b128 v[228:231],v112 offset:25600\n"
"s_add_u32 s53,s22,786432\n"
"v_mfma_f32_16x16x32_bf16 v[84:87],v[36:39],v[224:227],v[84:87]\n"
"s_add_u32 m0,s30,65536\n"
"ds_read_b128 v[224:227],v112 offset:24576\n"
"s_waitcnt lgkmcnt(13)\n"
"v_mfma_f32_16x16x32_bf16 v[84:87],a[68:71],v[240:243],v[84:87]\n"
"buffer_load_dwordx4 v112,s[24:27],s18 offen offset:0 sc1 lds\n"
"v_mfma_f32_16x16x32_bf16 v[84:87],a[76:79],v[244:247],v[84:87]\n"
"buffer_load_dwordx4 v112,s[24:27],s18 offen offset:1024 sc1 lds\n"
"v_mfma_f32_16x16x32_bf16 v[84:87],a[84:87],v[248:251],v[84:87]\n"
"s_add_u32 s56,s22,1048576\n"
"s_add_u32 s58,s22,1310720\n"
"v_mfma_f32_16x16x32_bf16 v[84:87],a[92:95],v[252:255],v[84:87]\n"
"buffer_load_dwordx4 v112,s[24:27],s18 offen offset:2048 sc1 lds\n"
"v_mfma_f32_16x16x32_bf16 v[80:83],a[88:91],v[252:255],v[80:83]\n"
"ds_read_b128 v[252:255],v112 offset:31744\n"
"s_add_u32 s59,s22,1572864\n"
"v_mfma_f32_16x16x32_bf16 v[80:83],a[80:83],v[248:251],v[80:83]\n"
"ds_read_b128 v[248:251],v112 offset:30720\n"
"s_bfe_u32 s10,s3,0x20015\n"
"v_mfma_f32_16x16x32_bf16 v[80:83],a[72:75],v[244:247],v[80:83]\n"
"ds_read_b128 v[244:247],v112 offset:29696\n"
"s_mul_i32 s10,s10,4186112\n"
"v_mfma_f32_16x16x32_bf16 v[80:83],a[156:159],v[240:243],v[80:83]\n"
"ds_read_b128 v[240:243],v112 offset:28672\n"
"s_waitcnt lgkmcnt(13)\n"
"v_mfma_f32_16x16x32_bf16 v[64:67],a[224:227],v[192:195],0\n"
"buffer_load_dwordx4 v112,s[24:27],s18 offen offset:3072 sc1 lds\n"
"s_add_u32 m0,m0,4096\n"
"v_mfma_f32_16x16x32_bf16 v[64:67],a[232:235],v[196:199],v[64:67]\n"
"buffer_load_dwordx4 v117,s[24:27],s18 offen offset:0 sc1 lds\n"
"v_mfma_f32_16x16x32_bf16 v[64:67],a[240:243],v[200:203],v[64:67]\n"
"s_add_u32 s12,s12,s10\n"
"s_bfe_u32 s10,s3,0x30012\n"
"v_mfma_f32_16x16x32_bf16 v[64:67],a[248:251],v[204:207],v[64:67]\n"
"buffer_load_dwordx4 v117,s[24:27],s18 offen offset:1024 sc1 lds\n"
"v_mfma_f32_16x16x32_bf16 v[68:71],a[252:255],v[204:207],0\n"
"ds_read_b128 v[204:207],v112 offset:35840\n"
"s_lshl_b32 s10,s10,12\n"
"v_mfma_f32_16x16x32_bf16 v[68:71],a[244:247],v[200:203],v[68:71]\n"
"ds_read_b128 v[200:203],v112 offset:34816\n"
"s_add_u32 s12,s12,s10\n"
"v_mfma_f32_16x16x32_bf16 v[68:71],a[236:239],v[196:199],v[68:71]\n"
"ds_read_b128 v[196:199],v112 offset:33792\n"
"s_bfe_u32 s10,s3,0x50012\n"
"v_mfma_f32_16x16x32_bf16 v[68:71],a[228:231],v[192:195],v[68:71]\n"
"ds_read_b128 v[192:195],v112 offset:32768\n"
"s_waitcnt lgkmcnt(13)\n"
"s_waitcnt vmcnt(8) lgkmcnt(12)\n"
"s_barrier\n"
"v_mfma_f32_16x16x32_bf16 v[68:71],a[4:7],v[208:211],v[68:71]\n"
"buffer_store_dwordx2 a[154:155],off,s[24:27],s71 sc1\n"
"buffer_load_dwordx4 v117,s[24:27],s18 offen offset:2048 sc1 lds\n"
"v_mfma_f32_16x16x32_bf16 v[68:71],a[12:15],v[212:215],v[68:71]\n"
"buffer_load_dwordx4 v117,s[24:27],s18 offen offset:3072 sc1 lds\n"
"v_mfma_f32_16x16x32_bf16 v[68:71],a[20:23],v[216:219],v[68:71]\n"
"s_sub_u32 s82,s3,212992\n"
"s_lshl_b32 s10,s10,8\n"
"v_mfma_f32_16x16x32_bf16 v[68:71],a[28:31],v[220:223],v[68:71]\n"
"buffer_load_dwordx4 a[48:51],v112,s[24:27],s82 offen offset:0 sc1\n"
"v_mfma_f32_16x16x32_bf16 v[64:67],a[24:27],v[220:223],v[64:67]\n"
"s_add_u32 s10,s10,149061632\n"
"ds_read_b128 v[220:223],v112 offset:39936\n"
"s_mov_b32 s14,0\n"
"v_mfma_f32_16x16x32_bf16 v[64:67],a[16:19],v[216:219],v[64:67]\n"
"ds_read_b128 v[216:219],v112 offset:38912\n"
"v_not_b32 v122,s80\n"
"v_mfma_f32_16x16x32_bf16 v[64:67],a[8:11],v[212:215],v[64:67]\n"
"s_cmp_eq_u32 s52,0\n"
"ds_read_b128 v[212:215],v112 offset:37888\n"
"s_cselect_b64 s[14:15],s[14:15],0\n"
"v_mfma_f32_16x16x32_bf16 v[64:67],a[0:3],v[208:211],v[64:67]\n"
"s_cselect_b32 s10,s10,2146435072\n"
"ds_read_b128 v[208:211],v112 offset:36864\n"
"s_waitcnt lgkmcnt(12)\n"
"v_mfma_f32_16x16x32_bf16 v[64:67],v[32:35],v[224:227],v[64:67]\n"
"buffer_load_dwordx4 a[52:55],v112,s[24:27],s82 offen offset:1024 sc1\n"
"s_cmp_lg_u32 s15,0\n"
"s_cselect_b32 s91,s11,2146435072\n"
"v_mfma_f32_16x16x32_bf16 v[64:67],v[40:43],v[228:231],v[64:67]\n"
"buffer_load_dwordx4 a[56:59],v112,s[24:27],s82 offen offset:2048 sc1\n"
"buffer_load_dwordx2 v[122:123],v115,s[24:27],s91 offen sc1\n"
"v_mfma_f32_16x16x32_bf16 v[64:67],a[32:35],v[232:235],v[64:67]\n"
"s_mov_b32 s89,0\n"
"s_mov_b32 s20,32768\n"
"v_mfma_f32_16x16x32_bf16 v[64:67],a[40:43],v[236:239],v[64:67]\n"
"buffer_load_dwordx4 a[60:63],v112,s[24:27],s82 offen offset:3072 sc1\n"
"v_mfma_f32_16x16x32_bf16 v[68:71],a[44:47],v[236:239],v[68:71]\n"
"s_mov_b32 s19,0xffff\n"
"ds_read_b128 v[236:239],v112 offset:44032\n"
"s_mov_b32 s21,32\n"
"v_mfma_f32_16x16x32_bf16 v[68:71],a[36:39],v[232:235],v[68:71]\n"
"ds_read_b128 v[232:235],v112 offset:43008\n"
"v_mfma_f32_16x16x32_bf16 v[68:71],v[44:47],v[228:231],v[68:71]\n"
"ds_read_b128 v[228:231],v112 offset:41984\n"
"v_mfma_f32_16x16x32_bf16 v[68:71],v[36:39],v[224:227],v[68:71]\n"
"s_sub_u32 s82,s3,229376\n"
"ds_read_b128 v[224:227],v112 offset:40960\n"
"s_waitcnt lgkmcnt(12)\n"
"v_mfma_f32_16x16x32_bf16 v[68:71],a[68:71],v[240:243],v[68:71]\n"
"buffer_load_dwordx4 v[48:51],v112,s[24:27],s82 offen offset:0 sc1\n"
"v_mfma_f32_16x16x32_bf16 v[68:71],a[76:79],v[244:247],v[68:71]\n"
"buffer_load_dwordx4 v[52:55],v112,s[24:27],s82 offen offset:1024 sc1\n"
"v_mfma_f32_16x16x32_bf16 v[68:71],a[84:87],v[248:251],v[68:71]\n"
"buffer_load_dwordx4 v[56:59],v112,s[24:27],s82 offen offset:2048 sc1\n"
"v_mfma_f32_16x16x32_bf16 v[68:71],a[92:95],v[252:255],v[68:71]\n"
"s_waitcnt vmcnt(17)\n"
"v_mfma_f32_16x16x32_bf16 v[64:67],a[88:91],v[252:255],v[64:67]\n"
"ds_read_b128 v[252:255],v112 offset:48128\n"
"v_xor_b32 v119,s80,v104\n"
"v_mfma_f32_16x16x32_bf16 v[64:67],a[80:83],v[248:251],v[64:67]\n"
"ds_read_b128 v[248:251],v112 offset:47104\n"
"v_xor_b32 v120,s81,v105\n"
"v_mfma_f32_16x16x32_bf16 v[64:67],a[72:75],v[244:247],v[64:67]\n"
"ds_read_b128 v[244:247],v112 offset:46080\n"
"v_or_b32 v119,v119,v120\n"
"v_mfma_f32_16x16x32_bf16 v[64:67],a[156:159],v[240:243],v[64:67]\n"
"ds_read_b128 v[240:243],v112 offset:45056\n"
"s_waitcnt lgkmcnt(12)\n"
"v_mfma_f32_16x16x32_bf16 v[72:75],a[224:227],v[192:195],0\n"
"buffer_load_dwordx4 v[60:63],v112,s[24:27],s82 offen offset:3072 sc1\n"
"s_sub_u32 s82,s3,245760\n"
"v_mfma_f32_16x16x32_bf16 v[72:75],a[232:235],v[196:199],v[72:75]\n"
"buffer_load_dwordx4 v[16:19],v112,s[24:27],s82 offen offset:0 sc1\n"
"v_xor_b32 v120,s80,v106\n"
"v_mfma_f32_16x16x32_bf16 v[72:75],a[240:243],v[200:203],v[72:75]\n"
"v_or_b32 v119,v119,v120\n"
"v_xor_b32 v120,s81,v107\n"
"v_mfma_f32_16x16x32_bf16 v[72:75],a[248:251],v[204:207],v[72:75]\n"
"buffer_load_dwordx4 v[20:23],v112,s[24:27],s82 offen offset:1024 sc1\n"
"v_or_b32 v119,v119,v120\n"
"v_mfma_f32_16x16x32_bf16 v[76:79],a[252:255],v[204:207],0\n"
"ds_read_b128 v[204:207],v112 offset:52224\n"
"v_xor_b32 v120,s80,v108\n"
"v_mfma_f32_16x16x32_bf16 v[76:79],a[244:247],v[200:203],v[76:79]\n"
"ds_read_b128 v[200:203],v112 offset:51200\n"
"v_or_b32 v119,v119,v120\n"
"v_mfma_f32_16x16x32_bf16 v[76:79],a[236:239],v[196:199],v[76:79]\n"
"ds_read_b128 v[196:199],v112 offset:50176\n"
"v_xor_b32 v120,s81,v109\n"
"v_mfma_f32_16x16x32_bf16 v[76:79],a[228:231],v[192:195],v[76:79]\n"
"ds_read_b128 v[192:195],v112 offset:49152\n"
"s_waitcnt lgkmcnt(12)\n"
"v_mfma_f32_16x16x32_bf16 v[76:79],a[4:7],v[208:211],v[76:79]\n"
"buffer_load_dwordx4 v[24:27],v112,s[24:27],s82 offen offset:2048 sc1\n"
"v_or_b32 v119,v119,v120\n"
"v_mfma_f32_16x16x32_bf16 v[76:79],a[12:15],v[212:215],v[76:79]\n"
"buffer_load_dwordx4 v[28:31],v112,s[24:27],s82 offen offset:3072 sc1\n"
"v_mfma_f32_16x16x32_bf16 v[76:79],a[20:23],v[216:219],v[76:79]\n"
"s_add_u32 s18,s18,2097152\n"
"v_xor_b32 v120,s80,v110\n"
"s_add_u32 m0,s30,98304\n"
"v_mfma_f32_16x16x32_bf16 v[76:79],a[28:31],v[220:223],v[76:79]\n"
"buffer_load_dwordx4 v112,s[24:27],s18 offen offset:0 sc1 lds\n"
"v_mfma_f32_16x16x32_bf16 v[72:75],a[24:27],v[220:223],v[72:75]\n"
"ds_read_b128 v[220:223],v112 offset:56320\n"
"v_or_b32 v119,v119,v120\n"
"v_mfma_f32_16x16x32_bf16 v[72:75],a[16:19],v[216:219],v[72:75]\n"
"ds_read_b128 v[216:219],v112 offset:55296\n"
"v_xor_b32 v120,s81,v111\n"
"v_mfma_f32_16x16x32_bf16 v[72:75],a[8:11],v[212:215],v[72:75]\n"
"ds_read_b128 v[212:215],v112 offset:54272\n"
"v_or_b32 v119,v119,v120\n"
"v_mfma_f32_16x16x32_bf16 v[72:75],a[0:3],v[208:211],v[72:75]\n"
"ds_read_b128 v[208:211],v112 offset:53248\n"
"s_waitcnt lgkmcnt(12)\n"
"v_mfma_f32_16x16x32_bf16 v[72:75],v[32:35],v[224:227],v[72:75]\n"
"buffer_load_dwordx4 v112,s[24:27],s18 offen offset:1024 sc1 lds\n"
"v_mfma_f32_16x16x32_bf16 v[72:75],v[40:43],v[228:231],v[72:75]\n"
"buffer_load_dwordx4 v112,s[24:27],s18 offen offset:2048 sc1 lds\n"
"v_mfma_f32_16x16x32_bf16 v[72:75],a[32:35],v[232:235],v[72:75]\n"
"v_cmp_ne_u32 vcc,0,v119\n"
"v_mfma_f32_16x16x32_bf16 v[72:75],a[40:43],v[236:239],v[72:75]\n"
"buffer_load_dwordx4 v112,s[24:27],s18 offen offset:3072 sc1 lds\n"
"v_mfma_f32_16x16x32_bf16 v[76:79],a[44:47],v[236:239],v[76:79]\n"
"ds_read_b128 v[236:239],v112 offset:60416\n"
"v_mfma_f32_16x16x32_bf16 v[76:79],a[36:39],v[232:235],v[76:79]\n"
"ds_read_b128 v[232:235],v112 offset:59392\n"
"v_xor_b32 v119,s80,v122\n"
"v_mfma_f32_16x16x32_bf16 v[76:79],v[44:47],v[228:231],v[76:79]\n"
"ds_read_b128 v[228:231],v112 offset:58368\n"
"v_xor_b32 v120,s81,v123\n"
"v_mfma_f32_16x16x32_bf16 v[76:79],v[36:39],v[224:227],v[76:79]\n"
"s_add_u32 m0,m0,4096\n"
"ds_read_b128 v[224:227],v112 offset:57344\n"
"s_waitcnt lgkmcnt(12)\n"
"v_mfma_f32_16x16x32_bf16 v[76:79],a[68:71],v[240:243],v[76:79]\n"
"buffer_load_dwordx4 v117,s[24:27],s18 offen offset:0 sc1 lds\n"
"v_or_b32 v119,v119,v120\n"
"v_mfma_f32_16x16x32_bf16 v[76:79],a[76:79],v[244:247],v[76:79]\n"
"buffer_load_dwordx4 v117,s[24:27],s18 offen offset:1024 sc1 lds\n"
"v_cmp_ne_u32 s[28:29],0,v119\n"
"v_mfma_f32_16x16x32_bf16 v[76:79],a[84:87],v[248:251],v[76:79]\n"
"s_and_b64 s[14:15],s[14:15],s[28:29]\n"
"s_cselect_b32 s91,s11,2146435072\n"
"buffer_load_dwordx2 v[122:123],v115,s[24:27],s91 offen sc1\n"
"v_mfma_f32_16x16x32_bf16 v[76:79],a[92:95],v[252:255],v[76:79]\n"
"buffer_load_dwordx4 v117,s[24:27],s18 offen offset:2048 sc1 lds\n"
"v_mfma_f32_16x16x32_bf16 v[72:75],a[88:91],v[252:255],v[72:75]\n"
"ds_read_b128 v[252:255],v112 offset:64512\n"
"v_mfma_f32_16x16x32_bf16 v[72:75],a[80:83],v[248:251],v[72:75]\n"
"ds_read_b128 v[248:251],v112 offset:63488\n"
"v_mfma_f32_16x16x32_bf16 v[72:75],a[72:75],v[244:247],v[72:75]\n"
"ds_read_b128 v[244:247],v112 offset:62464\n"
"v_mfma_f32_16x16x32_bf16 v[72:75],a[156:159],v[240:243],v[72:75]\n"
"s_and_b32 s83,s15,0xffff\n"
"ds_read_b128 v[240:243],v112 offset:61440\n"
"s_waitcnt lgkmcnt(12)\n"
"v_mfma_f32_16x16x32_bf16 v[88:91],a[224:227],v[192:195],0\n"
"buffer_load_dwordx4 v117,s[24:27],s18 offen offset:3072 sc1 lds\n"
"v_mfma_f32_16x16x32_bf16 v[88:91],a[232:235],v[196:199],v[88:91]\n"
"s_cbranch_scc1 .Lfk_es_lkone\n"
".Lfk_er_lkone:\n"
"v_mfma_f32_16x16x32_bf16 v[88:91],a[240:243],v[200:203],v[88:91]\n"
"s_waitcnt vmcnt(22)\n"
"ds_add_u32 v114,v116\n"
"v_mfma_f32_16x16x32_bf16 v[88:91],a[248:251],v[204:207],v[88:91]\n"
"v_and_b32 v118,0xf0,v112\n"
"v_lshrrev_b32 v120,8,v112\n"
"v_lshlrev_b32 v120,4,v120\n"
"v_lshl_add_u32 v118,v118,2,v120\n"
"v_mfma_f32_16x16x32_bf16 v[92:95],a[252:255],v[204:207],0\n"
"s_lshr_b32 s82,s78,20\n"
"v_xor_b32 v118,s82,v118\n"
"v_mov_b32 v100,1\n"
"v_mfma_f32_16x16x32_bf16 v[92:95],a[244:247],v[200:203],v[92:95]\n"
"s_mov_b32 s77,0\n"
"v_mov_b32 v102,131076\n"
"s_and_b32 s91,s52,1\n"
"v_mfma_f32_16x16x32_bf16 v[92:95],a[236:239],v[196:199],v[92:95]\n"
"s_lshl_b32 s91,s91,7\n"
"v_xor_b32 v96,s91,v112\n"
"v_xor_b32 v97,32,v96\n"
"v_mfma_f32_16x16x32_bf16 v[92:95],a[228:231],v[192:195],v[92:95]\n"
"v_xor_b32 v99,96,v96\n"
"s_waitcnt lgkmcnt(9)\n"
"v_mfma_f32_16x16x32_bf16 v[92:95],a[4:7],v[208:211],v[92:95]\n"
"v_xor_b32 v98,64,v96\n"
"v_add_u32 v118,s12,v118\n"
"v_mfma_f32_16x16x32_bf16 v[92:95],a[12:15],v[212:215],v[92:95]\n"
"ds_read_b32 v125,v114\n"
"v_mfma_f32_16x16x32_bf16 v[92:95],a[20:23],v[216:219],v[92:95]\n"
"v_mfma_f32_16x16x32_bf16 v[92:95],a[28:31],v[220:223],v[92:95]\n"
"v_mfma_f32_16x16x32_bf16 v[88:91],a[24:27],v[220:223],v[88:91]\n"
"v_mfma_f32_16x16x32_bf16 v[88:91],a[16:19],v[216:219],v[88:91]\n"
"s_waitcnt lgkmcnt(0)\n"
"v_cmp_gt_u32 s[28:29],s16,v125\n"
"v_mfma_f32_16x16x32_bf16 v[88:91],a[8:11],v[212:215],v[88:91]\n"
"s_add_u32 s18,s8,4194304\n"
"s_add_u32 s8,s8,5242880\n"
"s_or_b64 vcc,vcc,s[28:29]\n"
"v_mfma_f32_16x16x32_bf16 v[88:91],a[0:3],v[208:211],v[88:91]\n"
"s_cbranch_vccnz .Lfk_z0\n"
".Lfk_z1:\n"
"ds_read_b128 v[192:195],v113 offset:0\n"
"v_mfma_f32_16x16x32_bf16 v[88:91],v[32:35],v[224:227],v[88:91]\n"
"s_mov_b32 s89,1\n"
"s_mov_b32 s19,0xff\n"
"s_lshl_b64 exec,1,s89\n"
"buffer_atomic_add v100,off,s[24:27],s10 sc0\n"
"s_mov_b64 exec,-1\n"
"v_mfma_f32_16x16x32_bf16 v[88:91],v[40:43],v[228:231],v[88:91]\n"
"ds_read_b128 v[196:199],v113 offset:1024\n"
"s_add_u32 m0,s30,0\n"
"v_mfma_f32_16x16x32_bf16 v[88:91],a[32:35],v[232:235],v[88:91]\n"
"ds_read_b128 v[200:203],v113 offset:2048\n"
"buffer_load_dwordx4 v112,s[24:27],s18 offen offset:0 sc1 lds\n"
"v_mfma_f32_16x16x32_bf16 v[88:91],a[40:43],v[236:239],v[88:91]\n"
"s_add_u32 s89,s89,1\n"
"ds_read_b128 v[204:207],v113 offset:3072\n"
"s_add_u32 s21,s21,16\n"
"v_mfma_f32_16x16x32_bf16 v[92:95],a[44:47],v[236:239],v[92:95]\n"
"s_mov_b32 s38,4194304\n"
"ds_read_b128 v[208:211],v113 offset:16384\n"
"s_mov_b32 s39,0x20000\n"
"v_mfma_f32_16x16x32_bf16 v[92:95],a[36:39],v[232:235],v[92:95]\n"
"s_add_u32 s16,s16,4\n"
"ds_read_b128 v[212:215],v113 offset:17408\n"
"s_mov_b32 s46,2048\n"
"v_mfma_f32_16x16x32_bf16 v[92:95],v[44:47],v[228:231],v[92:95]\n"
"ds_read_b128 v[216:219],v113 offset:18432\n"
"buffer_load_dwordx4 v112,s[24:27],s18 offen offset:1024 sc1 lds\n"
"v_mfma_f32_16x16x32_bf16 v[92:95],v[36:39],v[224:227],v[92:95]\n"
"s_mov_b32 s47,0x20000\n"
"ds_read_b128 v[220:223],v113 offset:19456\n"
"s_mov_b32 s50,4194304\n"
"v_mfma_f32_16x16x32_bf16 v[92:95],a[68:71],v[240:243],v[92:95]\n"
"s_mov_b32 s51,0x20000\n"
"ds_read_b128 v[236:239],v113 offset:7168\n"
"s_and_b32 s60,s100,7\n"
"v_mfma_f32_16x16x32_bf16 v[92:95],a[76:79],v[244:247],v[92:95]\n"
"s_lshl_b32 s60,s60,5\n"
"ds_read_b128 v[232:235],v113 offset:6144\n"
"s_lshr_b32 s73,s100,3\n"
"v_mfma_f32_16x16x32_bf16 v[92:95],a[84:87],v[248:251],v[92:95]\n"
"ds_read_b128 v[228:231],v113 offset:5120\n"
"buffer_load_dwordx4 v112,s[24:27],s18 offen offset:2048 sc1 lds\n"
"v_mfma_f32_16x16x32_bf16 v[92:95],a[92:95],v[252:255],v[92:95]\n"
"s_add_u32 s73,s73,s60\n"
"ds_read_b128 v[224:227],v113 offset:4096\n"
"s_and_b32 s60,s100,7\n"
"v_mfma_f32_16x16x32_bf16 v[88:91],a[88:91],v[252:255],v[88:91]\n"
"s_bfe_u32 s61,s100,0x10003\n"
"ds_read_b128 v[252:255],v113 offset:23552\n"
"s_lshl1_add_u32 s60,s60,s61\n"
"v_mfma_f32_16x16x32_bf16 v[88:91],a[80:83],v[248:251],v[88:91]\n"
"s_and_b32 s61,s100,0xf0\n"
"ds_read_b128 v[248:251],v113 offset:22528\n"
"s_add_u32 s60,s60,s61\n"
"v_mfma_f32_16x16x32_bf16 v[88:91],a[72:75],v[244:247],v[88:91]\n"
"s_lshl_b32 s61,s52,8\n"
"ds_read_b128 v[244:247],v113 offset:21504\n"
"s_add_u32 s60,s60,s61\n"
"v_mfma_f32_16x16x32_bf16 v[88:91],a[156:159],v[240:243],v[88:91]\n"
"ds_read_b128 v[240:243],v113 offset:20480\n"
"s_waitcnt lgkmcnt(12)\n"
"v_mfma_f32_16x16x32_bf16 v[80:83],v[128:131],v[192:195],v[80:83]\n"
"buffer_load_dwordx4 v112,s[24:27],s18 offen offset:3072 sc1 lds\n"
"v_mfma_f32_16x16x32_bf16 v[80:83],v[136:139],v[196:199],v[80:83]\n"
"s_lshl_b32 s61,s60,11\n"
"s_add_u32 s68,s61,2097152\n"
"v_mfma_f32_16x16x32_bf16 v[80:83],v[144:147],v[200:203],v[80:83]\n"
"s_add_u32 m0,m0,4096\n"
"v_mfma_f32_16x16x32_bf16 v[80:83],v[152:155],v[204:207],v[80:83]\n"
"buffer_load_dwordx4 v117,s[24:27],s18 offen offset:0 sc1 lds\n"
"v_mfma_f32_16x16x32_bf16 v[84:87],v[156:159],v[204:207],v[84:87]\n"
"ds_read_b128 v[204:207],v113 offset:11264\n"
"s_add_u32 s20,s20,32768\n"
"v_mfma_f32_16x16x32_bf16 v[84:87],v[148:151],v[200:203],v[84:87]\n"
"ds_read_b128 v[200:203],v113 offset:10240\n"
"s_and_b32 s20,s20,0x1ffff\n"
"v_mfma_f32_16x16x32_bf16 v[84:87],v[140:143],v[196:199],v[84:87]\n"
"ds_read_b128 v[196:199],v113 offset:9216\n"
"v_mfma_f32_16x16x32_bf16 v[84:87],v[132:135],v[192:195],v[84:87]\n"
"ds_read_b128 v[192:195],v113 offset:8192\n"
"s_waitcnt lgkmcnt(12)\n"
"v_mfma_f32_16x16x32_bf16 v[68:71],v[132:135],v[208:211],v[68:71]\n"
"buffer_load_dwordx4 v117,s[24:27],s18 offen offset:1024 sc1 lds\n"
"v_mfma_f32_16x16x32_bf16 v[68:71],v[140:143],v[212:215],v[68:71]\n"
"v_mfma_f32_16x16x32_bf16 v[68:71],v[148:151],v[216:219],v[68:71]\n"
"v_mfma_f32_16x16x32_bf16 v[68:71],v[156:159],v[220:223],v[68:71]\n"
"buffer_load_dwordx4 v117,s[24:27],s18 offen offset:2048 sc1 lds\n"
"v_mfma_f32_16x16x32_bf16 v[64:67],v[152:155],v[220:223],v[64:67]\n"
"ds_read_b128 v[220:223],v113 offset:27648\n"
"v_xor_b32 v119,s80,v122\n"
"v_mfma_f32_16x16x32_bf16 v[64:67],v[144:147],v[216:219],v[64:67]\n"
"ds_read_b128 v[216:219],v113 offset:26624\n"
"v_xor_b32 v120,s81,v123\n"
"v_mfma_f32_16x16x32_bf16 v[64:67],v[136:139],v[212:215],v[64:67]\n"
"ds_read_b128 v[212:215],v113 offset:25600\n"
"v_or_b32 v119,v119,v120\n"
"v_mfma_f32_16x16x32_bf16 v[64:67],v[128:131],v[208:211],v[64:67]\n"
"ds_read_b128 v[208:211],v113 offset:24576\n"
"s_waitcnt lgkmcnt(12)\n"
"v_mfma_f32_16x16x32_bf16 v[80:83],v[0:3],v[224:227],v[80:83]\n"
"buffer_load_dwordx4 v117,s[24:27],s18 offen offset:3072 sc1 lds\n"
"v_cmp_ne_u32 s[28:29],0,v119\n"
"v_mfma_f32_16x16x32_bf16 v[80:83],v[8:11],v[228:231],v[80:83]\n"
"s_and_b64 s[14:15],s[14:15],s[28:29]\n"
"s_cselect_b32 s91,s11,2146435072\n"
"s_add_u32 m0,s30,32768\n"
"v_mfma_f32_16x16x32_bf16 v[80:83],v[160:163],v[232:235],v[80:83]\n"
"buffer_load_dwordx2 v[122:123],v115,s[24:27],s91 offen sc1\n"
"v_mfma_f32_16x16x32_bf16 v[80:83],v[168:171],v[236:239],v[80:83]\n"
"buffer_load_dwordx4 v112,s[24:27],s8 offen offset:0 sc1 lds\n"
"v_mfma_f32_16x16x32_bf16 v[84:87],v[172:175],v[236:239],v[84:87]\n"
"ds_read_b128 v[236:239],v113 offset:15360\n"
"v_mfma_f32_16x16x32_bf16 v[84:87],v[164:167],v[232:235],v[84:87]\n"
"ds_read_b128 v[232:235],v113 offset:14336\n"
"v_mfma_f32_16x16x32_bf16 v[84:87],v[12:15],v[228:231],v[84:87]\n"
"ds_read_b128 v[228:231],v113 offset:13312\n"
"v_mfma_f32_16x16x32_bf16 v[84:87],v[4:7],v[224:227],v[84:87]\n"
"s_and_b32 s83,s15,0xff0000\n"
"ds_read_b128 v[224:227],v113 offset:12288\n"
"s_waitcnt lgkmcnt(12)\n"
"v_mfma_f32_16x16x32_bf16 v[68:71],v[4:7],v[240:243],v[68:71]\n"
"buffer_load_dwordx4 v112,s[24:27],s8 offen offset:1024 sc1 lds\n"
"v_mfma_f32_16x16x32_bf16 v[68:71],v[12:15],v[244:247],v[68:71]\n"
"s_cbranch_scc1 .Lfk_es_lkonem\n"
".Lfk_er_lkonem:\n"
"v_mfma_f32_16x16x32_bf16 v[68:71],v[164:167],v[248:251],v[68:71]\n"
"v_mfma_f32_16x16x32_bf16 v[68:71],v[172:175],v[252:255],v[68:71]\n"
"buffer_load_dwordx4 v112,s[24:27],s8 offen offset:2048 sc1 lds\n"
"v_mfma_f32_16x16x32_bf16 v[64:67],v[168:171],v[252:255],v[64:67]\n"
"ds_read_b128 v[252:255],v113 offset:31744\n"
"v_mfma_f32_16x16x32_bf16 v[64:67],v[160:163],v[248:251],v[64:67]\n"
"ds_read_b128 v[248:251],v113 offset:30720\n"
"v_mfma_f32_16x16x32_bf16 v[64:67],v[8:11],v[244:247],v[64:67]\n"
"ds_read_b128 v[244:247],v113 offset:29696\n"
"v_mfma_f32_16x16x32_bf16 v[64:67],v[0:3],v[240:243],v[64:67]\n"
"ds_read_b128 v[240:243],v113 offset:28672\n"
"s_waitcnt lgkmcnt(12)\n"
"v_mfma_f32_16x16x32_bf16 v[80:83],v[176:179],v[192:195],v[80:83]\n"
"s_waitcnt vmcnt(13)\n"
"ds_add_u32 v114,v116\n"
"v_mfma_f32_16x16x32_bf16 v[80:83],v[184:187],v[196:199],v[80:83]\n"
"buffer_load_dwordx4 v112,s[24:27],s8 offen offset:3072 sc1 lds\n"
"v_mfma_f32_16x16x32_bf16 v[80:83],a[48:51],v[200:203],v[80:83]\n"
"s_add_u32 m0,m0,4096\n"
"v_mfma_f32_16x16x32_bf16 v[80:83],a[56:59],v[204:207],v[80:83]\n"
"s_add_u32 s83,s20,32768\n"
"s_and_b32 s83,s83,0x1ffff\n"
"v_add_u32 v113,s83,v112\n"
"v_mfma_f32_16x16x32_bf16 v[84:87],a[60:63],v[204:207],v[84:87]\n"
"buffer_load_dwordx4 v117,s[24:27],s8 offen offset:0 sc1 lds\n"
"v_mfma_f32_16x16x32_bf16 v[84:87],a[52:55],v[200:203],v[84:87]\n"
"s_cmp_eq_u32 s52,0\n"
"v_not_b32 v104,s80\n"
"s_cselect_b32 s9,s9,2146435072\n"
"v_mfma_f32_16x16x32_bf16 v[84:87],v[188:191],v[196:199],v[84:87]\n"
"buffer_load_dwordx2 v[104:105],v115,s[24:27],s9 offen sc1\n"
"v_mfma_f32_16x16x32_bf16 v[84:87],v[180:183],v[192:195],v[84:87]\n"
"buffer_load_dwordx4 v117,s[24:27],s8 offen offset:1024 sc1 lds\n"
"s_waitcnt lgkmcnt(9)\n"
"v_mfma_f32_16x16x32_bf16 v[68:71],v[180:183],v[208:211],v[68:71]\n"
"v_mfma_f32_16x16x32_bf16 v[68:71],v[188:191],v[212:215],v[68:71]\n"
"ds_read_b32 v125,v114\n"
"v_mfma_f32_16x16x32_bf16 v[68:71],a[52:55],v[216:219],v[68:71]\n"
"buffer_load_dwordx4 v117,s[24:27],s8 offen offset:2048 sc1 lds\n"
"v_mfma_f32_16x16x32_bf16 v[68:71],a[60:63],v[220:223],v[68:71]\n"
"v_mfma_f32_16x16x32_bf16 v[64:67],a[56:59],v[220:223],v[64:67]\n"
"v_mfma_f32_16x16x32_bf16 v[64:67],a[48:51],v[216:219],v[64:67]\n"
"s_waitcnt lgkmcnt(0)\n"
"v_cmp_gt_u32 vcc,s16,v125\n"
"v_mfma_f32_16x16x32_bf16 v[64:67],v[184:187],v[212:215],v[64:67]\n"
"buffer_load_dwordx4 v117,s[24:27],s8 offen offset:3072 sc1 lds\n"
"v_mfma_f32_16x16x32_bf16 v[64:67],v[176:179],v[208:211],v[64:67]\n"
"s_lshl_b64 exec,1,s89\n"
"buffer_atomic_add v100,off,s[24:27],s10 sc0\n"
"s_mov_b64 exec,-1\n"
"s_add_u32 s8,s8,1048576\n"
"s_cbranch_vccnz .Lfk_es_mone\n"
".Lfk_er_mone:\n"
"v_mfma_f32_16x16x32_bf16 v[80:83],v[48:51],v[224:227],v[80:83]\n"
"ds_read_b128 v[192:195],v113 offset:0\n"
"v_mfma_f32_16x16x32_bf16 v[80:83],v[56:59],v[228:231],v[80:83]\n"
"ds_read_b128 v[196:199],v113 offset:1024\n"
"ds_read_b128 v[200:203],v113 offset:2048\n"
"v_mfma_f32_16x16x32_bf16 v[80:83],v[16:19],v[232:235],v[80:83]\n"
"buffer_load_dwordx4 a[96:99],v112,s[36:39],s61 offen offset:0\n"
"v_mfma_f32_16x16x32_bf16 v[80:83],v[24:27],v[236:239],v[80:83]\n"
"s_add_u32 s89,s89,1\n"
"ds_read_b128 v[204:207],v113 offset:3072\n"
"s_add_u32 s21,s21,8\n"
"v_mfma_f32_16x16x32_bf16 v[84:87],v[28:31],v[236:239],v[84:87]\n"
"ds_read_b128 v[208:211],v113 offset:4096\n"
"ds_read_b128 v[212:215],v113 offset:5120\n"
"v_mfma_f32_16x16x32_bf16 v[84:87],v[20:23],v[232:235],v[84:87]\n"
"buffer_load_dwordx4 a[100:103],v112,s[36:39],s61 offen offset:1024\n"
"v_mfma_f32_16x16x32_bf16 v[84:87],v[60:63],v[228:231],v[84:87]\n"
"s_add_u32 s16,s16,4\n"
"ds_read_b128 v[216:219],v113 offset:6144\n"
"v_mfma_f32_16x16x32_bf16 v[84:87],v[52:55],v[224:227],v[84:87]\n"
"ds_read_b128 v[220:223],v113 offset:7168\n"
"ds_read_b128 v[236:239],v113 offset:11264\n"
"v_mfma_f32_16x16x32_bf16 v[68:71],v[52:55],v[240:243],v[68:71]\n"
"buffer_load_dwordx4 a[104:107],v112,s[36:39],s68 offen offset:0\n"
"v_mfma_f32_16x16x32_bf16 v[68:71],v[60:63],v[244:247],v[68:71]\n"
"ds_read_b128 v[232:235],v113 offset:10240\n"
"v_cvt_pk_bf16_f32 v80,v80,v81\n"
"v_mfma_f32_16x16x32_bf16 v[68:71],v[20:23],v[248:251],v[68:71]\n"
"ds_read_b128 v[228:231],v113 offset:9216\n"
"ds_read_b128 v[224:227],v113 offset:8192\n"
"v_cvt_pk_bf16_f32 v81,v82,v83\n"
"v_mfma_f32_16x16x32_bf16 v[68:71],v[28:31],v[252:255],v[68:71]\n"
"buffer_load_dwordx4 a[108:111],v112,s[36:39],s68 offen offset:1024\n"
"v_mfma_f32_16x16x32_bf16 v[64:67],v[24:27],v[252:255],v[64:67]\n"
"ds_read_b128 v[252:255],v113 offset:15360\n"
"v_cvt_pk_bf16_f32 v82,v84,v85\n"
"v_cvt_pk_bf16_f32 v83,v86,v87\n"
"v_mfma_f32_16x16x32_bf16 v[64:67],v[16:19],v[248:251],v[64:67]\n"
"ds_read_b128 v[248:251],v113 offset:14336\n"
"v_max_u32 v80,1,v80\n"
"v_max_u32 v81,1,v81\n"
"v_mfma_f32_16x16x32_bf16 v[64:67],v[56:59],v[244:247],v[64:67]\n"
"ds_read_b128 v[244:247],v113 offset:13312\n"
"v_max_u32 v82,1,v82\n"
"v_max_u32 v83,1,v83\n"
"v_mfma_f32_16x16x32_bf16 v[64:67],v[48:51],v[240:243],v[64:67]\n"
"ds_read_b128 v[240:243],v113 offset:12288\n"
"buffer_store_dwordx4 v[80:83],v118,s[24:27],s22 offen sc1\n"
"s_waitcnt lgkmcnt(12)\n"
"v_mfma_f32_16x16x32_bf16 v[72:75],v[128:131],v[192:195],v[72:75]\n"
"s_add_u32 m0,s30,s20\n"
"s_add_u32 s22,s22,262144\n"
"v_mfma_f32_16x16x32_bf16 v[72:75],v[136:139],v[196:199],v[72:75]\n"
"buffer_load_dwordx4 v112,s[24:27],s8 offen offset:0 sc1 lds\n"
"v_mfma_f32_16x16x32_bf16 v[72:75],v[144:147],v[200:203],v[72:75]\n"
"v_mfma_f32_16x16x32_bf16 v[72:75],v[152:155],v[204:207],v[72:75]\n"
"v_cvt_pk_bf16_f32 v64,v64,v65\n"
"v_mfma_f32_16x16x32_bf16 v[76:79],v[156:159],v[204:207],v[76:79]\n"
"ds_read_b128 v[204:207],v113 offset:19456\n"
"s_add_u32 s20,s20,32768\n"
"v_mfma_f32_16x16x32_bf16 v[76:79],v[148:151],v[200:203],v[76:79]\n"
"ds_read_b128 v[200:203],v113 offset:18432\n"
"s_and_b32 s20,s20,0x1ffff\n"
"v_mfma_f32_16x16x32_bf16 v[76:79],v[140:143],v[196:199],v[76:79]\n"
"ds_read_b128 v[196:199],v113 offset:17408\n"
"v_cvt_pk_bf16_f32 v65,v66,v67\n"
"v_mfma_f32_16x16x32_bf16 v[76:79],v[132:135],v[192:195],v[76:79]\n"
"ds_read_b128 v[192:195],v113 offset:16384\n"
"v_cvt_pk_bf16_f32 v66,v68,v69\n"
"s_waitcnt lgkmcnt(12)\n"
"v_mfma_f32_16x16x32_bf16 v[76:79],v[4:7],v[208:211],v[76:79]\n"
"buffer_load_dwordx4 v112,s[24:27],s8 offen offset:1024 sc1 lds\n"
"v_cvt_pk_bf16_f32 v67,v70,v71\n"
"v_mfma_f32_16x16x32_bf16 v[76:79],v[12:15],v[212:215],v[76:79]\n"
"v_max_u32 v64,1,v64\n"
"v_max_u32 v65,1,v65\n"
"v_max_u32 v66,1,v66\n"
"v_max_u32 v67,1,v67\n"
"v_mfma_f32_16x16x32_bf16 v[76:79],v[164:167],v[216:219],v[76:79]\n"
"buffer_store_dwordx4 v[64:67],v118,s[24:27],s22 offen sc1\n"
"v_mfma_f32_16x16x32_bf16 v[76:79],v[172:175],v[220:223],v[76:79]\n"
"buffer_load_dwordx4 v112,s[24:27],s8 offen offset:2048 sc1 lds\n"
"v_mfma_f32_16x16x32_bf16 v[72:75],v[168:171],v[220:223],v[72:75]\n"
"s_add_u32 s22,s22,262144\n"
"ds_read_b128 v[220:223],v113 offset:23552\n"
"v_mfma_f32_16x16x32_bf16 v[72:75],v[160:163],v[216:219],v[72:75]\n"
"ds_read_b128 v[216:219],v113 offset:22528\n"
"v_mfma_f32_16x16x32_bf16 v[72:75],v[8:11],v[212:215],v[72:75]\n"
"ds_read_b128 v[212:215],v113 offset:21504\n"
"v_mfma_f32_16x16x32_bf16 v[72:75],v[0:3],v[208:211],v[72:75]\n"
"ds_read_b128 v[208:211],v113 offset:20480\n"
"s_waitcnt lgkmcnt(12)\n"
"v_mfma_f32_16x16x32_bf16 v[72:75],v[176:179],v[224:227],v[72:75]\n"
"buffer_load_dwordx4 v112,s[24:27],s8 offen offset:3072 sc1 lds\n"
"v_mfma_f32_16x16x32_bf16 v[72:75],v[184:187],v[228:231],v[72:75]\n"
"v_xor_b32 v119,s80,v122\n"
"v_xor_b32 v120,s81,v123\n"
"v_mfma_f32_16x16x32_bf16 v[72:75],a[48:51],v[232:235],v[72:75]\n"
"v_or_b32 v119,v119,v120\n"
"v_cmp_ne_u32 s[28:29],0,v119\n"
"v_mfma_f32_16x16x32_bf16 v[72:75],a[56:59],v[236:239],v[72:75]\n"
"s_and_b64 s[14:15],s[14:15],s[28:29]\n"
"v_mfma_f32_16x16x32_bf16 v[76:79],a[60:63],v[236:239],v[76:79]\n"
"ds_read_b128 v[236:239],v113 offset:27648\n"
"v_mfma_f32_16x16x32_bf16 v[76:79],a[52:55],v[232:235],v[76:79]\n"
"ds_read_b128 v[232:235],v113 offset:26624\n"
"v_mfma_f32_16x16x32_bf16 v[76:79],v[188:191],v[228:231],v[76:79]\n"
"ds_read_b128 v[228:231],v113 offset:25600\n"
"v_mfma_f32_16x16x32_bf16 v[76:79],v[180:183],v[224:227],v[76:79]\n"
"s_add_u32 m0,m0,4096\n"
"ds_read_b128 v[224:227],v113 offset:24576\n"
"s_waitcnt lgkmcnt(12)\n"
"v_mfma_f32_16x16x32_bf16 v[76:79],v[52:55],v[240:243],v[76:79]\n"
"buffer_load_dwordx4 v117,s[24:27],s8 offen offset:0 sc1 lds\n"
"v_mfma_f32_16x16x32_bf16 v[76:79],v[60:63],v[244:247],v[76:79]\n"
"s_and_b32 s83,s15,0xff000000\n"
"s_cbranch_scc1 .Lfk_es_lktwom\n"
".Lfk_er_lktwom:\n"
"v_mfma_f32_16x16x32_bf16 v[76:79],v[20:23],v[248:251],v[76:79]\n"
"v_xor_b32 v119,s80,v104\n"
"v_xor_b32 v120,s81,v105\n"
"v_or_b32 v119,v119,v120\n"
"v_mfma_f32_16x16x32_bf16 v[76:79],v[28:31],v[252:255],v[76:79]\n"
"buffer_load_dwordx4 v117,s[24:27],s8 offen offset:1024 sc1 lds\n"
"v_cmp_ne_u32 s[28:29],0,v119\n"
"v_mfma_f32_16x16x32_bf16 v[72:75],v[24:27],v[252:255],v[72:75]\n"
"s_cmp_eq_u32 s52,0\n"
"ds_read_b128 v[252:255],v113 offset:31744\n"
"s_cselect_b64 s[14:15],-1,0\n"
"v_mfma_f32_16x16x32_bf16 v[72:75],v[16:19],v[248:251],v[72:75]\n"
"s_mov_b32 s11,s9\n"
"ds_read_b128 v[248:251],v113 offset:30720\n"
"s_waitcnt vmcnt(22)\n"
"v_not_b32 v122,s80\n"
"v_mfma_f32_16x16x32_bf16 v[72:75],v[56:59],v[244:247],v[72:75]\n"
"s_and_b64 s[14:15],s[14:15],s[28:29]\n"
"s_cselect_b32 s91,s11,2146435072\n"
"ds_read_b128 v[244:247],v113 offset:29696\n"
"buffer_load_dwordx2 v[122:123],v115,s[24:27],s91 offen sc1\n"
"v_mfma_f32_16x16x32_bf16 v[72:75],v[48:51],v[240:243],v[72:75]\n"
"ds_read_b128 v[240:243],v113 offset:28672\n"
"s_waitcnt lgkmcnt(12)\n"
"v_mfma_f32_16x16x32_bf16 v[88:91],v[128:131],v[192:195],v[88:91]\n"
"ds_write_b32 v101,v100\n"
"v_mfma_f32_16x16x32_bf16 v[88:91],v[136:139],v[196:199],v[88:91]\n"
"ds_add_u32 v114,v116\n"
"v_mfma_f32_16x16x32_bf16 v[88:91],v[144:147],v[200:203],v[88:91]\n"
"buffer_load_dwordx4 v117,s[24:27],s8 offen offset:2048 sc1 lds\n"
"v_mfma_f32_16x16x32_bf16 v[88:91],v[152:155],v[204:207],v[88:91]\n"
"s_add_u32 s83,s20,32768\n"
"s_and_b32 s83,s83,0x1ffff\n"
"v_add_u32 v113,s83,v112\n"
"v_mfma_f32_16x16x32_bf16 v[92:95],v[156:159],v[204:207],v[92:95]\n"
"v_cvt_pk_bf16_f32 v72,v72,v73\n"
"v_cvt_pk_bf16_f32 v73,v74,v75\n"
"v_cvt_pk_bf16_f32 v74,v76,v77\n"
"v_mfma_f32_16x16x32_bf16 v[92:95],v[148:151],v[200:203],v[92:95]\n"
"v_cvt_pk_bf16_f32 v75,v78,v79\n"
"v_max_u32 v72,1,v72\n"
"v_max_u32 v73,1,v73\n"
"v_mfma_f32_16x16x32_bf16 v[92:95],v[140:143],v[196:199],v[92:95]\n"
"v_max_u32 v74,1,v74\n"
"v_max_u32 v75,1,v75\n"
"v_mfma_f32_16x16x32_bf16 v[92:95],v[132:135],v[192:195],v[92:95]\n"
"buffer_store_dwordx4 v[72:75],v118,s[24:27],s22 offen sc1\n"
"s_waitcnt lgkmcnt(10)\n"
"v_mfma_f32_16x16x32_bf16 v[92:95],v[4:7],v[208:211],v[92:95]\n"
"buffer_load_dwordx4 v117,s[24:27],s8 offen offset:3072 sc1 lds\n"
"v_mfma_f32_16x16x32_bf16 v[92:95],v[12:15],v[212:215],v[92:95]\n"
"s_add_u32 s22,s22,262144\n"
"ds_read_b32 v125,v114\n"
"s_add_u32 s77,s77,4096\n"
"s_add_u32 m0,s30,135168\n"
"v_mfma_f32_16x16x32_bf16 v[92:95],v[164:167],v[216:219],v[92:95]\n"
"buffer_load_dwordx4 v98,s[40:43],s77 offen offset:0 nt lds\n"
"v_mfma_f32_16x16x32_bf16 v[92:95],v[172:175],v[220:223],v[92:95]\n"
"buffer_load_dwordx4 v98,s[40:43],s77 offen offset:1024 nt lds\n"
"v_mfma_f32_16x16x32_bf16 v[88:91],v[168:171],v[220:223],v[88:91]\n"
"s_lshl_b64 exec,1,s89\n"
"buffer_atomic_add v100,off,s[24:27],s10 sc0\n"
"s_mov_b64 exec,-1\n"
"v_mfma_f32_16x16x32_bf16 v[88:91],v[160:163],v[216:219],v[88:91]\n"
"s_waitcnt lgkmcnt(0)\n"
"buffer_load_dwordx4 v99,s[40:43],s77 offen offset:2048 nt lds\n"
"v_cmp_gt_u32 vcc,s16,v125\n"
"v_mfma_f32_16x16x32_bf16 v[88:91],v[8:11],v[212:215],v[88:91]\n"
"buffer_load_dwordx4 v99,s[40:43],s77 offen offset:3072 nt lds\n"
"v_mfma_f32_16x16x32_bf16 v[88:91],v[0:3],v[208:211],v[88:91]\n"
"s_add_u32 s8,s8,1048576\n"
"s_cbranch_vccnz .Lfk_es_mtwo\n"
".Lfk_er_mtwo:\n"
".Lfk_l4:\n"
"v_mfma_f32_16x16x32_bf16 v[88:91],v[176:179],v[224:227],v[88:91]\n"
"ds_read_b128 v[192:195],v113 offset:0\n"
"v_mfma_f32_16x16x32_bf16 v[88:91],v[184:187],v[228:231],v[88:91]\n"
"ds_read_b128 v[196:199],v113 offset:1024\n"
"v_mfma_f32_16x16x32_bf16 v[88:91],a[48:51],v[232:235],v[88:91]\n"
"ds_read_b128 v[200:203],v113 offset:2048\n"
"s_add_u32 s89,s89,1\n"
"v_mfma_f32_16x16x32_bf16 v[88:91],a[56:59],v[236:239],v[88:91]\n"
"ds_read_b128 v[204:207],v113 offset:3072\n"
"v_mfma_f32_16x16x32_bf16 v[92:95],a[60:63],v[236:239],v[92:95]\n"
"ds_read_b128 v[208:211],v113 offset:4096\n"
"v_mfma_f32_16x16x32_bf16 v[92:95],a[52:55],v[232:235],v[92:95]\n"
"ds_read_b128 v[212:215],v113 offset:5120\n"
"s_add_u32 s16,s16,4\n"
"v_mfma_f32_16x16x32_bf16 v[92:95],v[188:191],v[228:231],v[92:95]\n"
"ds_read_b128 v[216:219],v113 offset:6144\n"
"v_mfma_f32_16x16x32_bf16 v[92:95],v[180:183],v[224:227],v[92:95]\n"
"ds_read_b128 v[220:223],v113 offset:7168\n"
"v_mfma_f32_16x16x32_bf16 v[92:95],v[52:55],v[240:243],v[92:95]\n"
"ds_read_b128 v[236:239],v113 offset:11264\n"
"v_mfma_f32_16x16x32_bf16 v[92:95],v[60:63],v[244:247],v[92:95]\n"
"ds_read_b128 v[232:235],v113 offset:10240\n"
"v_mfma_f32_16x16x32_bf16 v[92:95],v[20:23],v[248:251],v[92:95]\n"
"ds_read_b128 v[228:231],v113 offset:9216\n"
"v_mfma_f32_16x16x32_bf16 v[92:95],v[28:31],v[252:255],v[92:95]\n"
"ds_read_b128 v[224:227],v113 offset:8192\n"
"v_mfma_f32_16x16x32_bf16 v[88:91],v[24:27],v[252:255],v[88:91]\n"
"ds_read_b128 v[252:255],v113 offset:15360\n"
"v_mfma_f32_16x16x32_bf16 v[88:91],v[16:19],v[248:251],v[88:91]\n"
"ds_read_b128 v[248:251],v113 offset:14336\n"
"v_mfma_f32_16x16x32_bf16 v[88:91],v[56:59],v[244:247],v[88:91]\n"
"ds_read_b128 v[244:247],v113 offset:13312\n"
"v_mfma_f32_16x16x32_bf16 v[88:91],v[48:51],v[240:243],v[88:91]\n"
"ds_read_b128 v[240:243],v113 offset:12288\n"
"s_waitcnt lgkmcnt(12)\n"
"v_mfma_f32_16x16x32_bf16 v[80:83],a[224:227],v[192:195],0\n"
"s_add_u32 m0,s30,s20\n"
"ds_read_b32 v103,v102\n"
"v_mfma_f32_16x16x32_bf16 v[80:83],a[232:235],v[196:199],v[80:83]\n"
"buffer_load_dwordx4 v112,s[24:27],s8 offen offset:0 sc1 lds\n"
"v_mfma_f32_16x16x32_bf16 v[80:83],a[240:243],v[200:203],v[80:83]\n"
"v_add_u32 v102,4,v102\n"
"v_and_b32 v102,0x200ff,v102\n"
"v_mfma_f32_16x16x32_bf16 v[80:83],a[248:251],v[204:207],v[80:83]\n"
"v_cvt_pk_bf16_f32 v88,v88,v89\n"
"v_cvt_pk_bf16_f32 v89,v90,v91\n"
"v_mfma_f32_16x16x32_bf16 v[84:87],a[252:255],v[204:207],0\n"
"ds_read_b128 v[204:207],v113 offset:19456\n"
"s_add_u32 s20,s20,32768\n"
"v_mfma_f32_16x16x32_bf16 v[84:87],a[244:247],v[200:203],v[84:87]\n"
"ds_read_b128 v[200:203],v113 offset:18432\n"
"s_and_b32 s20,s20,0x1ffff\n"
"v_mfma_f32_16x16x32_bf16 v[84:87],a[236:239],v[196:199],v[84:87]\n"
"ds_read_b128 v[196:199],v113 offset:17408\n"
"v_cvt_pk_bf16_f32 v90,v92,v93\n"
"v_mfma_f32_16x16x32_bf16 v[84:87],a[228:231],v[192:195],v[84:87]\n"
"ds_read_b128 v[192:195],v113 offset:16384\n"
"s_waitcnt lgkmcnt(13)\n"
"v_mfma_f32_16x16x32_bf16 v[84:87],a[4:7],v[208:211],v[84:87]\n"
"buffer_load_dwordx4 v112,s[24:27],s8 offen offset:1024 sc1 lds\n"
"v_mfma_f32_16x16x32_bf16 v[84:87],a[12:15],v[212:215],v[84:87]\n"
"v_cvt_pk_bf16_f32 v91,v94,v95\n"
"v_max_u32 v88,1,v88\n"
"v_mfma_f32_16x16x32_bf16 v[84:87],a[20:23],v[216:219],v[84:87]\n"
"v_max_u32 v89,1,v89\n"
"v_max_u32 v90,1,v90\n"
"v_mfma_f32_16x16x32_bf16 v[84:87],a[28:31],v[220:223],v[84:87]\n"
"buffer_load_dwordx4 v112,s[24:27],s8 offen offset:2048 sc1 lds\n"
"v_mfma_f32_16x16x32_bf16 v[80:83],a[24:27],v[220:223],v[80:83]\n"
"ds_read_b128 v[220:223],v113 offset:23552\n"
"v_max_u32 v91,1,v91\n"
"v_mfma_f32_16x16x32_bf16 v[80:83],a[16:19],v[216:219],v[80:83]\n"
"ds_read_b128 v[216:219],v113 offset:22528\n"
"buffer_store_dwordx4 v[88:91],v118,s[24:27],s53 offen sc1\n"
"v_mfma_f32_16x16x32_bf16 v[80:83],a[8:11],v[212:215],v[80:83]\n"
"ds_read_b128 v[212:215],v113 offset:21504\n"
"v_mfma_f32_16x16x32_bf16 v[80:83],a[0:3],v[208:211],v[80:83]\n"
"ds_read_b128 v[208:211],v113 offset:20480\n"
"s_waitcnt lgkmcnt(13)\n"
"v_mfma_f32_16x16x32_bf16 v[80:83],v[32:35],v[224:227],v[80:83]\n"
"v_mfma_f32_16x16x32_bf16 v[80:83],v[40:43],v[228:231],v[80:83]\n"
"buffer_load_dwordx4 v112,s[24:27],s8 offen offset:3072 sc1 lds\n"
"v_mfma_f32_16x16x32_bf16 v[80:83],a[32:35],v[232:235],v[80:83]\n"
"s_waitcnt lgkmcnt(8)\n"
"v_readfirstlane_b32 s22,v103\n"
"v_mfma_f32_16x16x32_bf16 v[80:83],a[40:43],v[236:239],v[80:83]\n"
"v_mfma_f32_16x16x32_bf16 v[84:87],a[44:47],v[236:239],v[84:87]\n"
"ds_read_b128 v[236:239],v113 offset:27648\n"
"s_and_b32 s21,s22,0x38\n"
"v_mfma_f32_16x16x32_bf16 v[84:87],a[36:39],v[232:235],v[84:87]\n"
"ds_read_b128 v[232:235],v113 offset:26624\n"
"v_mfma_f32_16x16x32_bf16 v[84:87],v[44:47],v[228:231],v[84:87]\n"
"ds_read_b128 v[228:231],v113 offset:25600\n"
"v_mfma_f32_16x16x32_bf16 v[84:87],v[36:39],v[224:227],v[84:87]\n"
"ds_read_b128 v[224:227],v113 offset:24576\n"
"s_add_u32 m0,m0,4096\n"
"v_mfma_f32_16x16x32_bf16 v[84:87],a[68:71],v[240:243],v[84:87]\n"
"buffer_load_dwordx4 v117,s[24:27],s8 offen offset:0 sc1 lds\n"
"v_mfma_f32_16x16x32_bf16 v[84:87],a[76:79],v[244:247],v[84:87]\n"
"s_cmp_lg_u64 s[14:15],0\n"
"s_cbranch_scc1 .Lfk_es_lkloopm\n"
".Lfk_er_lkloopm:\n"
"v_mfma_f32_16x16x32_bf16 v[84:87],a[84:87],v[248:251],v[84:87]\n"
"v_mfma_f32_16x16x32_bf16 v[84:87],a[92:95],v[252:255],v[84:87]\n"
"buffer_load_dwordx4 v117,s[24:27],s8 offen offset:1024 sc1 lds\n"
"v_mfma_f32_16x16x32_bf16 v[80:83],a[88:91],v[252:255],v[80:83]\n"
"ds_read_b128 v[252:255],v113 offset:31744\n"
"s_mov_b32 s53,s56\n"
"v_mfma_f32_16x16x32_bf16 v[80:83],a[80:83],v[248:251],v[80:83]\n"
"ds_read_b128 v[248:251],v113 offset:30720\n"
"s_mov_b32 s56,s58\n"
"v_mfma_f32_16x16x32_bf16 v[80:83],a[72:75],v[244:247],v[80:83]\n"
"ds_read_b128 v[244:247],v113 offset:29696\n"
"s_mov_b32 s58,s59\n"
"v_mfma_f32_16x16x32_bf16 v[80:83],a[156:159],v[240:243],v[80:83]\n"
"ds_read_b128 v[240:243],v113 offset:28672\n"
"s_waitcnt lgkmcnt(12)\n"
"v_mfma_f32_16x16x32_bf16 v[88:91],v[128:131],v[192:195],v[80:83]\n"
"s_waitcnt vmcnt(17)\n"
"ds_write_b32 v101,v100\n"
"v_mfma_f32_16x16x32_bf16 v[88:91],v[136:139],v[196:199],v[88:91]\n"
"ds_add_u32 v114,v116\n"
"v_mfma_f32_16x16x32_bf16 v[88:91],v[144:147],v[200:203],v[88:91]\n"
"buffer_load_dwordx4 v117,s[24:27],s8 offen offset:2048 sc1 lds\n"
"v_mfma_f32_16x16x32_bf16 v[88:91],v[152:155],v[204:207],v[88:91]\n"
"s_add_u32 s83,s20,32768\n"
"s_and_b32 s83,s83,0x1ffff\n"
"v_add_u32 v113,s83,v112\n"
"v_mfma_f32_16x16x32_bf16 v[92:95],v[156:159],v[204:207],v[84:87]\n"
"s_cmp_lt_u32 s22,64\n"
"s_cselect_b32 s17,s16,-1\n"
"v_mfma_f32_16x16x32_bf16 v[92:95],v[148:151],v[200:203],v[92:95]\n"
"v_mfma_f32_16x16x32_bf16 v[92:95],v[140:143],v[196:199],v[92:95]\n"
"v_mfma_f32_16x16x32_bf16 v[92:95],v[132:135],v[192:195],v[92:95]\n"
"s_waitcnt lgkmcnt(10)\n"
"v_mfma_f32_16x16x32_bf16 v[92:95],v[4:7],v[208:211],v[92:95]\n"
"buffer_load_dwordx4 v117,s[24:27],s8 offen offset:3072 sc1 lds\n"
"v_mfma_f32_16x16x32_bf16 v[92:95],v[12:15],v[212:215],v[92:95]\n"
"s_lshr_b32 s59,s8,2\n"
"ds_read_b32 v125,v114\n"
"s_lshl_b32 s8,s22,17\n"
"v_mfma_f32_16x16x32_bf16 v[92:95],v[164:167],v[216:219],v[92:95]\n"
"s_add_u32 s8,s8,s31\n"
"v_mfma_f32_16x16x32_bf16 v[92:95],v[172:175],v[220:223],v[92:95]\n"
"v_mfma_f32_16x16x32_bf16 v[88:91],v[168:171],v[220:223],v[88:91]\n"
"s_lshl_b64 exec,1,s89\n"
"buffer_atomic_add v100,off,s[24:27],s10 sc0\n"
"s_mov_b64 exec,-1\n"
"v_mfma_f32_16x16x32_bf16 v[88:91],v[160:163],v[216:219],v[88:91]\n"
"s_waitcnt lgkmcnt(0)\n"
"v_cmp_gt_u32 vcc,s17,v125\n"
"v_mfma_f32_16x16x32_bf16 v[88:91],v[8:11],v[212:215],v[88:91]\n"
"v_mfma_f32_16x16x32_bf16 v[88:91],v[0:3],v[208:211],v[88:91]\n"
"s_cbranch_vccz .Lfk_l4\n"
"s_call_b64 s[94:95],.Lfk_meet4\n"
"s_cmp_lt_u32 s22,64\n"
"s_cbranch_scc1 .Lfk_l4\n"
"ds_read_b128 v[192:195],v113 offset:0\n"
"v_mfma_f32_16x16x32_bf16 v[88:91],v[176:179],v[224:227],v[88:91]\n"
"s_getpc_b64 s[96:97]\n"
".Lfk_rt2:\n"
"s_add_u32 s96,s96,.Lfk_tail-.Lfk_rt2\n"
"s_addc_u32 s97,s97,0\n"
"s_mov_b32 s98,.Lfk_cold-.Lfk_tail\n"
"s_mov_b32 s99,0x20000\n"
"s_lshr_b32 s82,s100,3\n"
"s_lshl2_add_u32 s82,s82,s52\n"
"v_lshrrev_b32 v121,2,v112\n"
"s_lshl_b32 s82,s82,8\n"
"s_lshr_b32 s62,s60,4\n"
"v_mfma_f32_16x16x32_bf16 v[88:91],v[184:187],v[228:231],v[88:91]\n"
"buffer_load_dword v120,v121,s[96:99],s82 offen\n"
"ds_read_b128 v[196:199],v113 offset:1024\n"
"s_lshl_b32 s62,s62,15\n"
"v_mfma_f32_16x16x32_bf16 v[88:91],a[48:51],v[232:235],v[88:91]\n"
"s_and_b32 s91,s60,15\n"
"ds_read_b128 v[200:203],v113 offset:2048\n"
"v_lshlrev_b32 v102,2,v112\n"
"v_mfma_f32_16x16x32_bf16 v[88:91],a[56:59],v[236:239],v[88:91]\n"
"s_lshl_b32 s91,s91,6\n"
"ds_read_b128 v[204:207],v113 offset:3072\n"
"v_and_b32 v102,0x0,v102\n"
"v_xor_b32 v102,s91,v102\n"
"v_mfma_f32_16x16x32_bf16 v[92:95],a[60:63],v[236:239],v[92:95]\n"
"s_add_u32 s62,s62,71303168\n"
"ds_read_b128 v[208:211],v113 offset:4096\n"
"s_cmp_eq_u32 s42,0\n"
"v_mfma_f32_16x16x32_bf16 v[92:95],a[52:55],v[232:235],v[92:95]\n"
"s_cselect_b32 s72,0x3ff80,s23\n"
"ds_read_b128 v[212:215],v113 offset:5120\n"
"s_add_u32 s16,s16,4\n"
"v_mfma_f32_16x16x32_bf16 v[92:95],v[188:191],v[228:231],v[92:95]\n"
"s_add_u32 s91,s61,2097152\n"
"ds_read_b128 v[216:219],v113 offset:6144\n"
"v_lshrrev_b32 v126,6,v112\n"
"v_and_or_b32 v103,v112,48,v102\n"
"v_mfma_f32_16x16x32_bf16 v[92:95],v[180:183],v[224:227],v[92:95]\n"
"v_lshl_add_u32 v126,v126,10,v103\n"
"v_add_u32 v117,16384,v126\n"
"v_mfma_f32_16x16x32_bf16 v[92:95],v[52:55],v[240:243],v[92:95]\n"
"ds_read_b128 v[220:223],v113 offset:7168\n"
"ds_read_b128 v[236:239],v113 offset:11264\n"
"v_mfma_f32_16x16x32_bf16 v[92:95],v[60:63],v[244:247],v[92:95]\n"
"ds_read_b128 v[232:235],v113 offset:10240\n"
"v_mfma_f32_16x16x32_bf16 v[92:95],v[20:23],v[248:251],v[92:95]\n"
"ds_read_b128 v[228:231],v113 offset:9216\n"
"v_mfma_f32_16x16x32_bf16 v[92:95],v[28:31],v[252:255],v[92:95]\n"
"ds_read_b128 v[224:227],v113 offset:8192\n"
"v_mfma_f32_16x16x32_bf16 v[88:91],v[24:27],v[252:255],v[88:91]\n"
"ds_read_b128 v[252:255],v113 offset:15360\n"
"v_mfma_f32_16x16x32_bf16 v[88:91],v[16:19],v[248:251],v[88:91]\n"
"ds_read_b128 v[248:251],v113 offset:14336\n"
"v_mfma_f32_16x16x32_bf16 v[88:91],v[56:59],v[244:247],v[88:91]\n"
"ds_read_b128 v[244:247],v113 offset:13312\n"
"v_mfma_f32_16x16x32_bf16 v[88:91],v[48:51],v[240:243],v[88:91]\n"
"ds_read_b128 v[240:243],v113 offset:12288\n"
"s_waitcnt lgkmcnt(12)\n"
"v_mfma_f32_16x16x32_bf16 v[80:83],a[224:227],v[192:195],0\n"
"buffer_load_dwordx4 v[72:75],v112,s[44:47],0 offen offset:0\n"
"v_mfma_f32_16x16x32_bf16 v[80:83],a[232:235],v[196:199],v[80:83]\n"
"buffer_load_dwordx4 v[76:79],v112,s[44:47],0 offen offset:1024\n"
"v_mfma_f32_16x16x32_bf16 v[80:83],a[240:243],v[200:203],v[80:83]\n"
"s_nop 0\n"
"v_cvt_pk_bf16_f32 v88,v88,v89\n"
"v_mfma_f32_16x16x32_bf16 v[80:83],a[248:251],v[204:207],v[80:83]\n"
"v_cvt_pk_bf16_f32 v89,v90,v91\n"
"v_mfma_f32_16x16x32_bf16 v[84:87],a[252:255],v[204:207],0\n"
"ds_read_b128 v[204:207],v113 offset:19456\n"
"s_add_u32 s20,s20,32768\n"
"v_mfma_f32_16x16x32_bf16 v[84:87],a[244:247],v[200:203],v[84:87]\n"
"ds_read_b128 v[200:203],v113 offset:18432\n"
"s_and_b32 s20,s20,0x1ffff\n"
"v_mfma_f32_16x16x32_bf16 v[84:87],a[236:239],v[196:199],v[84:87]\n"
"ds_read_b128 v[196:199],v113 offset:17408\n"
"v_cvt_pk_bf16_f32 v90,v92,v93\n"
"v_mfma_f32_16x16x32_bf16 v[84:87],a[228:231],v[192:195],v[84:87]\n"
"s_mov_b32 s91,s62\n"
"ds_read_b128 v[192:195],v113 offset:16384\n"
"s_waitcnt lgkmcnt(12)\n"
"v_mfma_f32_16x16x32_bf16 v[84:87],a[4:7],v[208:211],v[84:87]\n"
"v_cvt_pk_bf16_f32 v91,v94,v95\n"
"v_max_u32 v88,1,v88\n"
"v_mfma_f32_16x16x32_bf16 v[84:87],a[12:15],v[212:215],v[84:87]\n"
"v_max_u32 v89,1,v89\n"
"v_max_u32 v90,1,v90\n"
"v_max_u32 v91,1,v91\n"
"v_mfma_f32_16x16x32_bf16 v[84:87],a[20:23],v[216:219],v[84:87]\n"
"buffer_load_dwordx4 a[112:115],v126,s[24:27],s91 offen sc1\n"
"v_mfma_f32_16x16x32_bf16 v[84:87],a[28:31],v[220:223],v[84:87]\n"
"buffer_store_dwordx4 v[88:91],v118,s[24:27],s53 offen sc1\n"
"v_mfma_f32_16x16x32_bf16 v[80:83],a[24:27],v[220:223],v[80:83]\n"
"ds_read_b128 v[220:223],v113 offset:23552\n"
"buffer_load_dwordx4 a[116:119],v117,s[24:27],s91 offen sc1\n"
"v_mfma_f32_16x16x32_bf16 v[80:83],a[16:19],v[216:219],v[80:83]\n"
"ds_read_b128 v[216:219],v113 offset:22528\n"
"v_mfma_f32_16x16x32_bf16 v[80:83],a[8:11],v[212:215],v[80:83]\n"
"ds_read_b128 v[212:215],v113 offset:21504\n"
"v_mfma_f32_16x16x32_bf16 v[80:83],a[0:3],v[208:211],v[80:83]\n"
"s_add_u32 s91,s91,4194304\n"
"ds_read_b128 v[208:211],v113 offset:20480\n"
"s_waitcnt lgkmcnt(12)\n"
"v_mfma_f32_16x16x32_bf16 v[80:83],v[32:35],v[224:227],v[80:83]\n"
"v_mfma_f32_16x16x32_bf16 v[80:83],v[40:43],v[228:231],v[80:83]\n"
"v_mfma_f32_16x16x32_bf16 v[80:83],a[32:35],v[232:235],v[80:83]\n"
"buffer_load_dwordx4 a[120:123],v126,s[24:27],s91 offen sc1\n"
"v_mfma_f32_16x16x32_bf16 v[80:83],a[40:43],v[236:239],v[80:83]\n"
"v_mfma_f32_16x16x32_bf16 v[84:87],a[44:47],v[236:239],v[84:87]\n"
"ds_read_b128 v[236:239],v113 offset:27648\n"
"v_mfma_f32_16x16x32_bf16 v[84:87],a[36:39],v[232:235],v[84:87]\n"
"ds_read_b128 v[232:235],v113 offset:26624\n"
"buffer_load_dwordx4 a[124:127],v117,s[24:27],s91 offen sc1\n"
"v_mfma_f32_16x16x32_bf16 v[84:87],v[44:47],v[228:231],v[84:87]\n"
"ds_read_b128 v[228:231],v113 offset:25600\n"
"v_mfma_f32_16x16x32_bf16 v[84:87],v[36:39],v[224:227],v[84:87]\n"
"ds_read_b128 v[224:227],v113 offset:24576\n"
"s_waitcnt lgkmcnt(12)\n"
"v_mfma_f32_16x16x32_bf16 v[84:87],a[68:71],v[240:243],v[84:87]\n"
"s_add_u32 s91,s91,4194304\n"
"v_mfma_f32_16x16x32_bf16 v[84:87],a[76:79],v[244:247],v[84:87]\n"
"v_mfma_f32_16x16x32_bf16 v[84:87],a[84:87],v[248:251],v[84:87]\n"
"v_mfma_f32_16x16x32_bf16 v[84:87],a[92:95],v[252:255],v[84:87]\n"
"buffer_load_dwordx4 a[128:131],v126,s[24:27],s91 offen sc1\n"
"v_mfma_f32_16x16x32_bf16 v[80:83],a[88:91],v[252:255],v[80:83]\n"
"ds_read_b128 v[252:255],v113 offset:31744\n"
"s_mov_b32 s53,s56\n"
"v_mfma_f32_16x16x32_bf16 v[80:83],a[80:83],v[248:251],v[80:83]\n"
"ds_read_b128 v[248:251],v113 offset:30720\n"
"s_mov_b32 s56,s58\n"
"v_mfma_f32_16x16x32_bf16 v[80:83],a[72:75],v[244:247],v[80:83]\n"
"s_mov_b32 s58,s59\n"
"ds_read_b128 v[244:247],v113 offset:29696\n"
"buffer_load_dwordx4 a[132:135],v117,s[24:27],s91 offen sc1\n"
"v_mfma_f32_16x16x32_bf16 v[80:83],a[156:159],v[240:243],v[80:83]\n"
"ds_read_b128 v[240:243],v113 offset:28672\n"
"s_waitcnt lgkmcnt(12)\n"
"v_mfma_f32_16x16x32_bf16 v[88:91],v[128:131],v[192:195],v[80:83]\n"
"s_waitcnt vmcnt(21)\n"
"ds_add_u32 v114,v116\n"
"v_mfma_f32_16x16x32_bf16 v[88:91],v[136:139],v[196:199],v[88:91]\n"
"s_add_u32 s91,s91,4194304\n"
"v_mfma_f32_16x16x32_bf16 v[88:91],v[144:147],v[200:203],v[88:91]\n"
"v_mfma_f32_16x16x32_bf16 v[88:91],v[152:155],v[204:207],v[88:91]\n"
"s_add_u32 s83,s20,32768\n"
"s_and_b32 s83,s83,0x1ffff\n"
"v_add_u32 v113,s83,v112\n"
"v_mfma_f32_16x16x32_bf16 v[92:95],v[156:159],v[204:207],v[84:87]\n"
"buffer_load_dwordx4 a[136:139],v126,s[24:27],s91 offen sc1\n"
"v_mfma_f32_16x16x32_bf16 v[92:95],v[148:151],v[200:203],v[92:95]\n"
"v_mfma_f32_16x16x32_bf16 v[92:95],v[140:143],v[196:199],v[92:95]\n"
"buffer_load_dwordx4 a[140:143],v117,s[24:27],s91 offen sc1\n"
"v_mfma_f32_16x16x32_bf16 v[92:95],v[132:135],v[192:195],v[92:95]\n"
"s_waitcnt lgkmcnt(9)\n"
"v_mfma_f32_16x16x32_bf16 v[92:95],v[4:7],v[208:211],v[92:95]\n"
"v_mfma_f32_16x16x32_bf16 v[92:95],v[12:15],v[212:215],v[92:95]\n"
"ds_read_b32 v125,v114\n"
"v_mfma_f32_16x16x32_bf16 v[92:95],v[164:167],v[216:219],v[92:95]\n"
"v_mfma_f32_16x16x32_bf16 v[92:95],v[172:175],v[220:223],v[92:95]\n"
"v_mfma_f32_16x16x32_bf16 v[88:91],v[168:171],v[220:223],v[88:91]\n"
"v_mfma_f32_16x16x32_bf16 v[88:91],v[160:163],v[216:219],v[88:91]\n"
"s_waitcnt lgkmcnt(0)\n"
"v_cmp_gt_u32 vcc,s16,v125\n"
"v_mfma_f32_16x16x32_bf16 v[88:91],v[8:11],v[212:215],v[88:91]\n"
"v_mfma_f32_16x16x32_bf16 v[88:91],v[0:3],v[208:211],v[88:91]\n"
"s_cbranch_vccnz .Lfk_es_me3\n"
".Lfk_er_me3:\n"
"v_mfma_f32_16x16x32_bf16 v[88:91],v[176:179],v[224:227],v[88:91]\n"
"ds_read_b128 v[192:195],v113 offset:0\n"
"v_lshrrev_b32 v101,4,v112\n"
"v_mfma_f32_16x16x32_bf16 v[88:91],v[184:187],v[228:231],v[88:91]\n"
"ds_read_b128 v[196:199],v113 offset:1024\n"
"v_and_b32 v102,15,v101\n"
"v_mfma_f32_16x16x32_bf16 v[88:91],a[48:51],v[232:235],v[88:91]\n"
"ds_read_b128 v[200:203],v113 offset:2048\n"
"v_lshrrev_b32 v101,4,v101\n"
"v_mfma_f32_16x16x32_bf16 v[88:91],a[56:59],v[236:239],v[88:91]\n"
"ds_read_b128 v[204:207],v113 offset:3072\n"
"v_lshlrev_b32 v101,2,v101\n"
"v_mfma_f32_16x16x32_bf16 v[92:95],a[60:63],v[236:239],v[92:95]\n"
"ds_read_b128 v[208:211],v113 offset:4096\n"
"v_sub_u32 v101,v102,v101\n"
"v_mfma_f32_16x16x32_bf16 v[92:95],a[52:55],v[232:235],v[92:95]\n"
"ds_read_b128 v[212:215],v113 offset:5120\n"
"s_add_u32 s16,s16,4\n"
"v_mfma_f32_16x16x32_bf16 v[92:95],v[188:191],v[228:231],v[92:95]\n"
"ds_read_b128 v[216:219],v113 offset:6144\n"
"v_mov_b32 v64,0\n"
"v_mfma_f32_16x16x32_bf16 v[92:95],v[180:183],v[224:227],v[92:95]\n"
"ds_read_b128 v[220:223],v113 offset:7168\n"
"v_mov_b32 v65,0\n"
"v_mfma_f32_16x16x32_bf16 v[92:95],v[52:55],v[240:243],v[92:95]\n"
"ds_read_b128 v[236:239],v113 offset:11264\n"
"v_mov_b32 v68,0\n"
"v_mfma_f32_16x16x32_bf16 v[92:95],v[60:63],v[244:247],v[92:95]\n"
"ds_read_b128 v[232:235],v113 offset:10240\n"
"v_mov_b32 v69,0\n"
"v_mfma_f32_16x16x32_bf16 v[92:95],v[20:23],v[248:251],v[92:95]\n"
"ds_read_b128 v[228:231],v113 offset:9216\n"
"v_mov_b32 v102,0x3f80\n"
"v_mfma_f32_16x16x32_bf16 v[92:95],v[28:31],v[252:255],v[92:95]\n"
"ds_read_b128 v[224:227],v113 offset:8192\n"
"v_mov_b32 v103,0x3f800000\n"
"v_cmp_eq_u32 vcc,0,v101\n"
"v_mfma_f32_16x16x32_bf16 v[88:91],v[24:27],v[252:255],v[88:91]\n"
"ds_read_b128 v[252:255],v113 offset:15360\n"
"v_cndmask_b32 v66,0,v102,vcc\n"
"v_cmp_eq_u32 vcc,1,v101\n"
"v_mfma_f32_16x16x32_bf16 v[88:91],v[16:19],v[248:251],v[88:91]\n"
"ds_read_b128 v[248:251],v113 offset:14336\n"
"v_cndmask_b32 v66,v66,v103,vcc\n"
"v_cmp_eq_u32 vcc,2,v101\n"
"v_mfma_f32_16x16x32_bf16 v[88:91],v[56:59],v[244:247],v[88:91]\n"
"ds_read_b128 v[244:247],v113 offset:13312\n"
"v_cndmask_b32 v67,0,v102,vcc\n"
"v_mfma_f32_16x16x32_bf16 v[88:91],v[48:51],v[240:243],v[88:91]\n"
"ds_read_b128 v[240:243],v113 offset:12288\n"
"s_waitcnt lgkmcnt(12)\n"
"v_cmp_eq_u32 vcc,3,v101\n"
"v_mfma_f32_16x16x32_bf16 v[80:83],a[224:227],v[192:195],0\n"
"s_nop 0\n"
"v_cndmask_b32 v67,v67,v103,vcc\n"
"v_mfma_f32_16x16x32_bf16 v[80:83],a[232:235],v[196:199],v[80:83]\n"
"v_mfma_f32_16x16x32_bf16 v[80:83],a[240:243],v[200:203],v[80:83]\n"
"v_mfma_f32_16x16x32_bf16 v[80:83],a[248:251],v[204:207],v[80:83]\n"
"v_cvt_pk_bf16_f32 v88,v88,v89\n"
"v_cvt_pk_bf16_f32 v89,v90,v91\n"
"v_mfma_f32_16x16x32_bf16 v[84:87],a[252:255],v[204:207],0\n"
"ds_read_b128 v[204:207],v113 offset:19456\n"
"s_add_u32 s20,s20,32768\n"
"v_mfma_f32_16x16x32_bf16 v[84:87],a[244:247],v[200:203],v[84:87]\n"
"ds_read_b128 v[200:203],v113 offset:18432\n"
"s_and_b32 s20,s20,0x1ffff\n"
"v_mfma_f32_16x16x32_bf16 v[84:87],a[236:239],v[196:199],v[84:87]\n"
"ds_read_b128 v[196:199],v113 offset:17408\n"
"v_cvt_pk_bf16_f32 v90,v92,v93\n"
"v_mfma_f32_16x16x32_bf16 v[84:87],a[228:231],v[192:195],v[84:87]\n"
"ds_read_b128 v[192:195],v113 offset:16384\n"
"s_waitcnt lgkmcnt(12)\n"
"v_mfma_f32_16x16x32_bf16 v[84:87],a[4:7],v[208:211],v[84:87]\n"
"v_cvt_pk_bf16_f32 v91,v94,v95\n"
"v_max_u32 v88,1,v88\n"
"v_mfma_f32_16x16x32_bf16 v[84:87],a[12:15],v[212:215],v[84:87]\n"
"v_max_u32 v89,1,v89\n"
"v_max_u32 v90,1,v90\n"
"v_max_u32 v91,1,v91\n"
"v_mfma_f32_16x16x32_bf16 v[84:87],a[20:23],v[216:219],v[84:87]\n"
"buffer_store_dwordx4 v[88:91],v118,s[24:27],s53 offen sc1\n"
"v_mfma_f32_16x16x32_bf16 v[84:87],a[28:31],v[220:223],v[84:87]\n"
"v_mfma_f32_16x16x32_bf16 v[80:83],a[24:27],v[220:223],v[80:83]\n"
"ds_read_b128 v[220:223],v113 offset:23552\n"
"v_mfma_f32_16x16x32_bf16 v[80:83],a[16:19],v[216:219],v[80:83]\n"
"ds_read_b128 v[216:219],v113 offset:22528\n"
"v_mfma_f32_16x16x32_bf16 v[80:83],a[8:11],v[212:215],v[80:83]\n"
"ds_read_b128 v[212:215],v113 offset:21504\n"
"v_mfma_f32_16x16x32_bf16 v[80:83],a[0:3],v[208:211],v[80:83]\n"
"ds_read_b128 v[208:211],v113 offset:20480\n"
"s_waitcnt lgkmcnt(12)\n"
"v_mfma_f32_16x16x32_bf16 v[80:83],v[32:35],v[224:227],v[80:83]\n"
"v_mfma_f32_16x16x32_bf16 v[80:83],v[40:43],v[228:231],v[80:83]\n"
"v_mfma_f32_16x16x32_bf16 v[80:83],a[32:35],v[232:235],v[80:83]\n"
"v_mfma_f32_16x16x32_bf16 v[80:83],a[40:43],v[236:239],v[80:83]\n"
"v_mfma_f32_16x16x32_bf16 v[84:87],a[44:47],v[236:239],v[84:87]\n"
"ds_read_b128 v[236:239],v113 offset:27648\n"
"v_mfma_f32_16x16x32_bf16 v[84:87],a[36:39],v[232:235],v[84:87]\n"
"ds_read_b128 v[232:235],v113 offset:26624\n"
"v_mfma_f32_16x16x32_bf16 v[84:87],v[44:47],v[228:231],v[84:87]\n"
"ds_read_b128 v[228:231],v113 offset:25600\n"
"v_mfma_f32_16x16x32_bf16 v[84:87],v[36:39],v[224:227],v[84:87]\n"
"ds_read_b128 v[224:227],v113 offset:24576\n"
"s_waitcnt lgkmcnt(12)\n"
"v_mfma_f32_16x16x32_bf16 v[84:87],a[68:71],v[240:243],v[84:87]\n"
"v_mfma_f32_16x16x32_bf16 v[84:87],a[76:79],v[244:247],v[84:87]\n"
"v_mfma_f32_16x16x32_bf16 v[84:87],a[84:87],v[248:251],v[84:87]\n"
"v_mfma_f32_16x16x32_bf16 v[84:87],a[92:95],v[252:255],v[84:87]\n"
"v_mfma_f32_16x16x32_bf16 v[80:83],a[88:91],v[252:255],v[80:83]\n"
"ds_read_b128 v[252:255],v113 offset:31744\n"
"s_mov_b32 s53,s56\n"
"v_mfma_f32_16x16x32_bf16 v[80:83],a[80:83],v[248:251],v[80:83]\n"
"ds_read_b128 v[248:251],v113 offset:30720\n"
"s_mov_b32 s56,s58\n"
"v_mfma_f32_16x16x32_bf16 v[80:83],a[72:75],v[244:247],v[80:83]\n"
"ds_read_b128 v[244:247],v113 offset:29696\n"
"s_mov_b32 s58,s59\n"
"v_mfma_f32_16x16x32_bf16 v[80:83],a[156:159],v[240:243],v[80:83]\n"
"ds_read_b128 v[240:243],v113 offset:28672\n"
"s_waitcnt lgkmcnt(12)\n"
"v_mfma_f32_16x16x32_bf16 v[88:91],v[128:131],v[192:195],v[80:83]\n"
"s_waitcnt vmcnt(14)\n"
"ds_add_u32 v114,v116\n"
"v_mfma_f32_16x16x32_bf16 v[88:91],v[136:139],v[196:199],v[88:91]\n"
"v_mfma_f32_16x16x32_bf16 v[88:91],v[144:147],v[200:203],v[88:91]\n"
"v_mfma_f32_16x16x32_bf16 v[88:91],v[152:155],v[204:207],v[88:91]\n"
"s_add_u32 s83,s20,32768\n"
"s_and_b32 s83,s83,0x1ffff\n"
"v_add_u32 v113,s83,v112\n"
"v_mfma_f32_16x16x32_bf16 v[92:95],v[156:159],v[204:207],v[84:87]\n"
"v_mfma_f32_16x16x32_bf16 v[92:95],v[148:151],v[200:203],v[92:95]\n"
"v_mfma_f32_16x16x32_bf16 v[92:95],v[140:143],v[196:199],v[92:95]\n"
"v_mfma_f32_16x16x32_bf16 v[92:95],v[132:135],v[192:195],v[92:95]\n"
"s_waitcnt lgkmcnt(9)\n"
"v_mfma_f32_16x16x32_bf16 v[92:95],v[4:7],v[208:211],v[92:95]\n"
"v_mfma_f32_16x16x32_bf16 v[92:95],v[12:15],v[212:215],v[92:95]\n"
"ds_read_b32 v125,v114\n"
"v_mfma_f32_16x16x32_bf16 v[92:95],v[164:167],v[216:219],v[92:95]\n"
"v_mfma_f32_16x16x32_bf16 v[92:95],v[172:175],v[220:223],v[92:95]\n"
"v_mfma_f32_16x16x32_bf16 v[88:91],v[168:171],v[220:223],v[88:91]\n"
"v_mfma_f32_16x16x32_bf16 v[88:91],v[160:163],v[216:219],v[88:91]\n"
"s_waitcnt lgkmcnt(0)\n"
"v_cmp_gt_u32 vcc,s16,v125\n"
"v_mfma_f32_16x16x32_bf16 v[88:91],v[8:11],v[212:215],v[88:91]\n"
"v_mfma_f32_16x16x32_bf16 v[88:91],v[0:3],v[208:211],v[88:91]\n"
"s_cbranch_vccnz .Lfk_es_me2\n"
".Lfk_er_me2:\n"
"ds_read_b128 v[192:195],v113 offset:0\n"
"v_mfma_f32_16x16x32_bf16 v[88:91],v[176:179],v[224:227],v[88:91]\n"
"v_lshlrev_b32 v104,6,v112\n"
"v_and_b32 v105,0x8000,v104\n"
"v_mfma_f32_16x16x32_bf16 v[88:91],v[184:187],v[228:231],v[88:91]\n"
"ds_read_b128 v[196:199],v113 offset:1024\n"
"v_and_b32 v104,0x7c00,v104\n"
"v_lshl_add_u32 v104,v105,7,v104\n"
"v_mfma_f32_16x16x32_bf16 v[88:91],a[48:51],v[232:235],v[88:91]\n"
"s_and_b32 s82,s60,15\n"
"ds_read_b128 v[200:203],v113 offset:2048\n"
"s_lshl_b32 s82,s82,6\n"
"v_mfma_f32_16x16x32_bf16 v[88:91],a[56:59],v[236:239],v[88:91]\n"
"ds_read_b128 v[204:207],v113 offset:3072\n"
"v_add_u32 v104,s82,v104\n"
"v_mfma_f32_16x16x32_bf16 v[92:95],a[60:63],v[236:239],v[92:95]\n"
"s_and_b32 s82,s60,1\n"
"ds_read_b128 v[208:211],v113 offset:4096\n"
"s_lshl_b32 s82,s82,23\n"
"v_mfma_f32_16x16x32_bf16 v[92:95],a[52:55],v[232:235],v[92:95]\n"
"s_add_u32 s82,s82,s62\n"
"s_add_u32 s82,s82,2097152\n"
"s_cmp_lt_u32 s52,2\n"
"v_mfma_f32_16x16x32_bf16 v[92:95],v[188:191],v[228:231],v[92:95]\n"
"s_cselect_b32 s82,s82,2146435072\n"
"ds_read_b128 v[212:215],v113 offset:5120\n"
"ds_read_b128 v[216:219],v113 offset:6144\n"
"v_mfma_f32_16x16x32_bf16 v[92:95],v[180:183],v[224:227],v[92:95]\n"
"ds_read_b128 v[220:223],v113 offset:7168\n"
"v_mfma_f32_16x16x32_bf16 v[92:95],v[52:55],v[240:243],v[92:95]\n"
"ds_read_b128 v[236:239],v113 offset:11264\n"
"v_mfma_f32_16x16x32_bf16 v[92:95],v[60:63],v[244:247],v[92:95]\n"
"ds_read_b128 v[232:235],v113 offset:10240\n"
"v_mfma_f32_16x16x32_bf16 v[92:95],v[20:23],v[248:251],v[92:95]\n"
"ds_read_b128 v[228:231],v113 offset:9216\n"
"v_mfma_f32_16x16x32_bf16 v[92:95],v[28:31],v[252:255],v[92:95]\n"
"ds_read_b128 v[224:227],v113 offset:8192\n"
"v_mfma_f32_16x16x32_bf16 v[88:91],v[24:27],v[252:255],v[88:91]\n"
"ds_read_b128 v[252:255],v113 offset:15360\n"
"v_mfma_f32_16x16x32_bf16 v[88:91],v[16:19],v[248:251],v[88:91]\n"
"ds_read_b128 v[248:251],v113 offset:14336\n"
"v_mfma_f32_16x16x32_bf16 v[88:91],v[56:59],v[244:247],v[88:91]\n"
"ds_read_b128 v[244:247],v113 offset:13312\n"
"v_mfma_f32_16x16x32_bf16 v[88:91],v[48:51],v[240:243],v[88:91]\n"
"ds_read_b128 v[240:243],v113 offset:12288\n"
"s_waitcnt lgkmcnt(12)\n"
"v_mfma_f32_16x16x32_bf16 v[80:83],a[224:227],v[192:195],0\n"
"buffer_load_dword a148,v104,s[24:27],s82 offen sc1\n"
"v_mfma_f32_16x16x32_bf16 v[80:83],a[232:235],v[196:199],v[80:83]\n"
"v_mfma_f32_16x16x32_bf16 v[80:83],a[240:243],v[200:203],v[80:83]\n"
"v_mfma_f32_16x16x32_bf16 v[80:83],a[248:251],v[204:207],v[80:83]\n"
"s_nop 0\n"
"v_cvt_pk_bf16_f32 v88,v88,v89\n"
"v_cvt_pk_bf16_f32 v89,v90,v91\n"
"v_mfma_f32_16x16x32_bf16 v[84:87],a[252:255],v[204:207],0\n"
"ds_read_b128 v[204:207],v113 offset:19456\n"
"v_cvt_pk_bf16_f32 v90,v92,v93\n"
"v_mfma_f32_16x16x32_bf16 v[84:87],a[244:247],v[200:203],v[84:87]\n"
"ds_read_b128 v[200:203],v113 offset:18432\n"
"v_cvt_pk_bf16_f32 v91,v94,v95\n"
"v_mfma_f32_16x16x32_bf16 v[84:87],a[236:239],v[196:199],v[84:87]\n"
"ds_read_b128 v[196:199],v113 offset:17408\n"
"v_max_u32 v88,1,v88\n"
"v_mfma_f32_16x16x32_bf16 v[84:87],a[228:231],v[192:195],v[84:87]\n"
"ds_read_b128 v[192:195],v113 offset:16384\n"
"s_waitcnt lgkmcnt(12)\n"
"v_mfma_f32_16x16x32_bf16 v[84:87],a[4:7],v[208:211],v[84:87]\n"
"v_max_u32 v89,1,v89\n"
"v_max_u32 v90,1,v90\n"
"v_max_u32 v91,1,v91\n"
"v_mfma_f32_16x16x32_bf16 v[84:87],a[12:15],v[212:215],v[84:87]\n"
"buffer_store_dwordx4 v[88:91],v118,s[24:27],s53 offen sc1\n"
"v_mfma_f32_16x16x32_bf16 v[84:87],a[20:23],v[216:219],v[84:87]\n"
"v_mfma_f32_16x16x32_bf16 v[84:87],a[28:31],v[220:223],v[84:87]\n"
"v_mfma_f32_16x16x32_bf16 v[80:83],a[24:27],v[220:223],v[80:83]\n"
"ds_read_b128 v[220:223],v113 offset:23552\n"
"v_mfma_f32_16x16x32_bf16 v[80:83],a[16:19],v[216:219],v[80:83]\n"
"ds_read_b128 v[216:219],v113 offset:22528\n"
"v_mfma_f32_16x16x32_bf16 v[80:83],a[8:11],v[212:215],v[80:83]\n"
"ds_read_b128 v[212:215],v113 offset:21504\n"
"v_mfma_f32_16x16x32_bf16 v[80:83],a[0:3],v[208:211],v[80:83]\n"
"ds_read_b128 v[208:211],v113 offset:20480\n"
"s_waitcnt lgkmcnt(12)\n"
"v_mfma_f32_16x16x32_bf16 v[80:83],v[32:35],v[224:227],v[80:83]\n"
"v_mfma_f32_16x16x32_bf16 v[80:83],v[40:43],v[228:231],v[80:83]\n"
"v_mfma_f32_16x16x32_bf16 v[80:83],a[32:35],v[232:235],v[80:83]\n"
"v_mfma_f32_16x16x32_bf16 v[80:83],a[40:43],v[236:239],v[80:83]\n"
"v_mfma_f32_16x16x32_bf16 v[84:87],a[44:47],v[236:239],v[84:87]\n"
"ds_read_b128 v[236:239],v113 offset:27648\n"
"v_mfma_f32_16x16x32_bf16 v[84:87],a[36:39],v[232:235],v[84:87]\n"
"ds_read_b128 v[232:235],v113 offset:26624\n"
"v_mfma_f32_16x16x32_bf16 v[84:87],v[44:47],v[228:231],v[84:87]\n"
"ds_read_b128 v[228:231],v113 offset:25600\n"
"v_mfma_f32_16x16x32_bf16 v[84:87],v[36:39],v[224:227],v[84:87]\n"
"ds_read_b128 v[224:227],v113 offset:24576\n"
"s_waitcnt lgkmcnt(12)\n"
"v_mfma_f32_16x16x32_bf16 v[84:87],a[68:71],v[240:243],v[84:87]\n"
"v_mfma_f32_16x16x32_bf16 v[84:87],a[76:79],v[244:247],v[84:87]\n"
"v_mfma_f32_16x16x32_bf16 v[84:87],a[84:87],v[248:251],v[84:87]\n"
"v_mfma_f32_16x16x32_bf16 v[84:87],a[92:95],v[252:255],v[84:87]\n"
"v_mfma_f32_16x16x32_bf16 v[80:83],a[88:91],v[252:255],v[80:83]\n"
"ds_read_b128 v[252:255],v113 offset:31744\n"
"s_mov_b32 s53,s56\n"
"v_mfma_f32_16x16x32_bf16 v[80:83],a[80:83],v[248:251],v[80:83]\n"
"ds_read_b128 v[248:251],v113 offset:30720\n"
"s_mov_b32 s56,s58\n"
"v_mfma_f32_16x16x32_bf16 v[80:83],a[72:75],v[244:247],v[80:83]\n"
"ds_read_b128 v[244:247],v113 offset:29696\n"
"s_mov_b32 s58,s59\n"
"v_mfma_f32_16x16x32_bf16 v[80:83],a[156:159],v[240:243],v[80:83]\n"
"ds_read_b128 v[240:243],v113 offset:28672\n"
"s_waitcnt lgkmcnt(12)\n"
"v_mfma_f32_16x16x32_bf16 v[88:91],v[128:131],v[192:195],v[80:83]\n"
"v_mfma_f32_16x16x32_bf16 v[88:91],v[136:139],v[196:199],v[88:91]\n"
"v_mfma_f32_16x16x32_bf16 v[88:91],v[144:147],v[200:203],v[88:91]\n"
"v_mfma_f32_16x16x32_bf16 v[88:91],v[152:155],v[204:207],v[88:91]\n"
"v_mfma_f32_16x16x32_bf16 v[92:95],v[156:159],v[204:207],v[84:87]\n"
"v_mfma_f32_16x16x32_bf16 v[92:95],v[148:151],v[200:203],v[92:95]\n"
"v_mfma_f32_16x16x32_bf16 v[92:95],v[140:143],v[196:199],v[92:95]\n"
"v_mfma_f32_16x16x32_bf16 v[92:95],v[132:135],v[192:195],v[92:95]\n"
"s_waitcnt lgkmcnt(8)\n"
"v_mfma_f32_16x16x32_bf16 v[92:95],v[4:7],v[208:211],v[92:95]\n"
"v_mfma_f32_16x16x32_bf16 v[92:95],v[12:15],v[212:215],v[92:95]\n"
"v_mfma_f32_16x16x32_bf16 v[92:95],v[164:167],v[216:219],v[92:95]\n"
"v_mfma_f32_16x16x32_bf16 v[92:95],v[172:175],v[220:223],v[92:95]\n"
"v_mfma_f32_16x16x32_bf16 v[88:91],v[168:171],v[220:223],v[88:91]\n"
"s_mov_b32 s91,s62\n"
"v_mfma_f32_16x16x32_bf16 v[88:91],v[160:163],v[216:219],v[88:91]\n"
"v_mfma_f32_16x16x32_bf16 v[88:91],v[8:11],v[212:215],v[88:91]\n"
"v_mfma_f32_16x16x32_bf16 v[88:91],v[0:3],v[208:211],v[88:91]\n"
"s_waitcnt lgkmcnt(0)\n"
"s_waitcnt vmcnt(3)\n"
"v_mfma_f32_16x16x32_bf16 v[88:91],v[176:179],v[224:227],v[88:91]\n"
"v_accvgpr_read_b32 v128,a112\n"
"v_accvgpr_read_b32 v129,a113\n"
"v_mfma_f32_16x16x32_bf16 v[88:91],v[184:187],v[228:231],v[88:91]\n"
"v_accvgpr_read_b32 v130,a114\n"
"v_accvgpr_read_b32 v131,a115\n"
"v_mfma_f32_16x16x32_bf16 v[88:91],a[48:51],v[232:235],v[88:91]\n"
"v_accvgpr_read_b32 v132,a116\n"
"v_accvgpr_read_b32 v133,a117\n"
"v_mfma_f32_16x16x32_bf16 v[88:91],a[56:59],v[236:239],v[88:91]\n"
"v_accvgpr_read_b32 v134,a118\n"
"v_accvgpr_read_b32 v135,a119\n"
"v_mfma_f32_16x16x32_bf16 v[92:95],a[60:63],v[236:239],v[92:95]\n"
"v_accvgpr_read_b32 v136,a120\n"
"v_accvgpr_read_b32 v137,a121\n"
"v_mfma_f32_16x16x32_bf16 v[92:95],a[52:55],v[232:235],v[92:95]\n"
"v_accvgpr_read_b32 v138,a122\n"
"v_accvgpr_read_b32 v139,a123\n"
"v_mfma_f32_16x16x32_bf16 v[92:95],v[188:191],v[228:231],v[92:95]\n"
"v_accvgpr_read_b32 v140,a124\n"
"v_accvgpr_read_b32 v141,a125\n"
"v_mfma_f32_16x16x32_bf16 v[92:95],v[180:183],v[224:227],v[92:95]\n"
"v_accvgpr_read_b32 v142,a126\n"
"v_accvgpr_read_b32 v143,a127\n"
"v_mfma_f32_16x16x32_bf16 v[92:95],v[52:55],v[240:243],v[92:95]\n"
"v_accvgpr_read_b32 v144,a128\n"
"v_accvgpr_read_b32 v145,a129\n"
"v_mfma_f32_16x16x32_bf16 v[92:95],v[60:63],v[244:247],v[92:95]\n"
"v_accvgpr_read_b32 v146,a130\n"
"v_accvgpr_read_b32 v147,a131\n"
"v_mfma_f32_16x16x32_bf16 v[92:95],v[20:23],v[248:251],v[92:95]\n"
"v_accvgpr_read_b32 v148,a132\n"
"v_accvgpr_read_b32 v149,a133\n"
"v_mfma_f32_16x16x32_bf16 v[92:95],v[28:31],v[252:255],v[92:95]\n"
"v_accvgpr_read_b32 v150,a134\n"
"v_accvgpr_read_b32 v151,a135\n"
"v_mfma_f32_16x16x32_bf16 v[88:91],v[24:27],v[252:255],v[88:91]\n"
"v_accvgpr_read_b32 v152,a136\n"
"v_accvgpr_read_b32 v153,a137\n"
"v_mfma_f32_16x16x32_bf16 v[88:91],v[16:19],v[248:251],v[88:91]\n"
"v_accvgpr_read_b32 v154,a138\n"
"v_accvgpr_read_b32 v155,a139\n"
"v_mfma_f32_16x16x32_bf16 v[88:91],v[56:59],v[244:247],v[88:91]\n"
"v_accvgpr_read_b32 v156,a140\n"
"v_accvgpr_read_b32 v157,a141\n"
"v_mfma_f32_16x16x32_bf16 v[88:91],v[48:51],v[240:243],v[88:91]\n"
"v_accvgpr_read_b32 v158,a142\n"
"v_accvgpr_read_b32 v159,a143\n"
".Lfk_tail:\n"
"v_lshlrev_b32 v224,16,v72\n"
"v_and_b32 v225,0xffff0000,v72\n"
"v_lshlrev_b32 v226,16,v73\n"
"v_and_b32 v227,0xffff0000,v73\n"
"v_lshlrev_b32 v228,16,v74\n"
"s_nop 0\n"
"v_cvt_pk_bf16_f32 v88,v88,v89\n"
"v_cvt_pk_bf16_f32 v89,v90,v91\n"
"v_cvt_pk_bf16_f32 v90,v92,v93\n"
"v_cvt_pk_bf16_f32 v91,v94,v95\n"
"v_max_u32 v88,1,v88\n"
"v_max_u32 v89,1,v89\n"
"v_max_u32 v90,1,v90\n"
"v_max_u32 v91,1,v91\n"
"v_mfma_f32_16x16x32_bf16 v[0:3],v[66:69],a[96:99],0\n"
"buffer_store_dwordx4 v[88:91],v118,s[24:27],s53 offen sc1\n"
"v_mfma_f32_16x16x32_bf16 v[4:7],v[64:67],a[96:99],0\n"
"s_mov_b32 s101,s22\n"
"v_and_b32 v231,0xffff0000,v75\n"
"v_lshlrev_b32 v232,16,v76\n"
"v_mfma_f32_16x16x32_bf16 v[8:11],v[66:69],a[100:103],0\n"
"v_and_b32 v229,0xffff0000,v74\n"
"v_lshlrev_b32 v230,16,v75\n"
"v_mfma_f32_16x16x32_bf16 v[12:15],v[64:67],a[100:103],0\n"
"s_mov_b32 s67,0x3a800000\n"
"v_and_b32 v233,0xffff0000,v76\n"
"v_lshlrev_b32 v234,16,v77\n"
"v_and_b32 v235,0xffff0000,v77\n"
"v_lshlrev_b32 v236,16,v78\n"
"v_and_b32 v237,0xffff0000,v78\n"
"v_lshlrev_b32 v238,16,v79\n"
"v_and_b32 v239,0xffff0000,v79\n"
"v_pk_add_f32 v[70:71],v[224:225],v[226:227]\n"
"v_pk_add_f32 v[70:71],v[70:71],v[228:229]\n"
"v_pk_add_f32 v[70:71],v[70:71],v[230:231]\n"
"v_pk_add_f32 v[70:71],v[70:71],v[232:233]\n"
"v_pk_add_f32 v[70:71],v[70:71],v[234:235]\n"
"v_pk_add_f32 v[70:71],v[70:71],v[236:237]\n"
"v_pk_add_f32 v[70:71],v[70:71],v[238:239]\n"
"v_add_f32 v70,v70,v71\n"
"s_nop 1\n"
"v_add_f32_dpp v70,v70,v70 quad_perm:[1,0,3,2] row_mask:0xf bank_mask:0xf\n"
"s_nop 1\n"
"v_add_f32_dpp v70,v70,v70 quad_perm:[2,3,0,1] row_mask:0xf bank_mask:0xf\n"
"s_nop 1\n"
"v_add_f32_dpp v70,v70,v70 row_half_mirror row_mask:0xf bank_mask:0xf\n"
"s_nop 1\n"
"v_add_f32_dpp v70,v70,v70 row_mirror row_mask:0xf bank_mask:0xf\n"
"v_mov_b32 v71,v70\n"
"s_nop 1\n"
"v_permlane16_swap_b32 v71,v70\n"
"v_add_f32 v70,v70,v71\n"
"v_mov_b32 v71,v70\n"
"s_nop 1\n"
"v_permlane32_swap_b32 v71,v70\n"
"v_add_f32 v70,v70,v71\n"
"v_min3_u32 v36,v128,v129,v130\n"
"v_min3_u32 v37,v131,v132,v133\n"
"v_min3_u32 v36,v36,v134,v135\n"
"v_min3_u32 v37,v37,v136,v137\n"
"v_min3_u32 v36,v36,v138,v139\n"
"v_min3_u32 v37,v37,v140,v141\n"
"v_min3_u32 v36,v36,v142,v143\n"
"v_min3_u32 v37,v37,v144,v145\n"
"v_min3_u32 v36,v36,v146,v147\n"
"v_min3_u32 v37,v37,v148,v149\n"
"v_min3_u32 v36,v36,v150,v151\n"
"v_min3_u32 v37,v37,v152,v153\n"
"v_min3_u32 v36,v36,v154,v155\n"
"v_min3_u32 v37,v37,v156,v157\n"
"v_min3_u32 v36,v36,v158,v159\n"
"v_min_u32 v36,v36,v37\n"
"v_cmp_eq_u32 vcc,0,v36\n"
"s_nop 0\n"
"s_cbranch_vccz .Lfk_t0\n"
"s_mov_b32 s84,0x40000\n"
"s_mov_b32 s91,s62\n"
"buffer_load_dwordx4 v[128:131],v126,s[24:27],s91 offen sc1\n"
"buffer_load_dwordx4 v[132:135],v117,s[24:27],s91 offen sc1\n"
"s_add_u32 s91,s91,4194304\n"
"buffer_load_dwordx4 v[136:139],v126,s[24:27],s91 offen sc1\n"
"buffer_load_dwordx4 v[140:143],v117,s[24:27],s91 offen sc1\n"
"s_add_u32 s91,s91,4194304\n"
"buffer_load_dwordx4 v[144:147],v126,s[24:27],s91 offen sc1\n"
"buffer_load_dwordx4 v[148:151],v117,s[24:27],s91 offen sc1\n"
"s_add_u32 s91,s91,4194304\n"
"buffer_load_dwordx4 v[152:155],v126,s[24:27],s91 offen sc1\n"
"buffer_load_dwordx4 v[156:159],v117,s[24:27],s91 offen sc1\n"
"s_waitcnt vmcnt(0)\n"
".Lfk_wa:\n"
"v_min3_u32 v36,v128,v129,v130\n"
"v_min3_u32 v37,v131,v132,v133\n"
"v_min3_u32 v36,v36,v134,v135\n"
"v_min3_u32 v37,v37,v136,v137\n"
"v_min3_u32 v36,v36,v138,v139\n"
"v_min3_u32 v37,v37,v140,v141\n"
"v_min3_u32 v36,v36,v142,v143\n"
"v_min3_u32 v37,v37,v144,v145\n"
"v_min3_u32 v36,v36,v146,v147\n"
"v_min3_u32 v37,v37,v148,v149\n"
"v_min3_u32 v36,v36,v150,v151\n"
"v_min3_u32 v37,v37,v152,v153\n"
"v_min3_u32 v36,v36,v154,v155\n"
"v_min3_u32 v37,v37,v156,v157\n"
"v_min3_u32 v36,v36,v158,v159\n"
"v_min_u32 v36,v36,v37\n"
"v_cmp_eq_u32 vcc,0,v36\n"
"s_nop 0\n"
"s_cbranch_vccz .Lfk_wa_x\n"
"s_sleep 2\n"
"s_mov_b64 exec,vcc\n"
"s_mov_b32 s91,s62\n"
"buffer_load_dwordx4 v[128:131],v126,s[24:27],s91 offen sc1\n"
"buffer_load_dwordx4 v[132:135],v117,s[24:27],s91 offen sc1\n"
"s_add_u32 s91,s91,4194304\n"
"buffer_load_dwordx4 v[136:139],v126,s[24:27],s91 offen sc1\n"
"buffer_load_dwordx4 v[140:143],v117,s[24:27],s91 offen sc1\n"
"s_add_u32 s91,s91,4194304\n"
"buffer_load_dwordx4 v[144:147],v126,s[24:27],s91 offen sc1\n"
"buffer_load_dwordx4 v[148:151],v117,s[24:27],s91 offen sc1\n"
"s_add_u32 s91,s91,4194304\n"
"buffer_load_dwordx4 v[152:155],v126,s[24:27],s91 offen sc1\n"
"buffer_load_dwordx4 v[156:159],v117,s[24:27],s91 offen sc1\n"
"s_mov_b64 exec,-1\n"
"s_sub_u32 s84,s84,1\n"
"s_waitcnt vmcnt(0)\n"
"s_cbranch_scc0 .Lfk_wa\n"
".Lfk_wa_x:\n"
".Lfk_t0:\n"
"s_getpc_b64 s[94:95]\n"
".Lfk_rm0:\n"
"s_add_u32 s94,s94,.Lfk_rm1-.Lfk_rm0\n"
"v_mfma_f32_16x16x32_bf16 v[0:3],v[66:69],v[128:131],v[0:3]\n"
"s_addc_u32 s95,s95,0\n"
"v_mfma_f32_16x16x32_bf16 v[4:7],v[64:67],v[128:131],v[4:7]\n"
"v_mfma_f32_16x16x32_bf16 v[8:11],v[66:69],v[132:135],v[8:11]\n"
"v_mfma_f32_16x16x32_bf16 v[12:15],v[64:67],v[132:135],v[12:15]\n"
"v_mfma_f32_16x16x32_bf16 v[0:3],v[66:69],v[136:139],v[0:3]\n"
"v_mfma_f32_16x16x32_bf16 v[4:7],v[64:67],v[136:139],v[4:7]\n"
"v_mfma_f32_16x16x32_bf16 v[8:11],v[66:69],v[140:143],v[8:11]\n"
"v_mfma_f32_16x16x32_bf16 v[12:15],v[64:67],v[140:143],v[12:15]\n"
"v_mfma_f32_16x16x32_bf16 v[0:3],v[66:69],v[144:147],v[0:3]\n"
"v_mfma_f32_16x16x32_bf16 v[4:7],v[64:67],v[144:147],v[4:7]\n"
"v_mfma_f32_16x16x32_bf16 v[8:11],v[66:69],v[148:151],v[8:11]\n"
"v_mfma_f32_16x16x32_bf16 v[12:15],v[64:67],v[148:151],v[12:15]\n"
"v_mfma_f32_16x16x32_bf16 v[0:3],v[66:69],v[152:155],v[0:3]\n"
"v_mfma_f32_16x16x32_bf16 v[4:7],v[64:67],v[152:155],v[4:7]\n"
"v_mfma_f32_16x16x32_bf16 v[8:11],v[66:69],v[156:159],v[8:11]\n"
"v_mfma_f32_16x16x32_bf16 v[12:15],v[64:67],v[156:159],v[12:15]\n"
"s_nop 7\n"
".Lfk_rm:\n"
"v_readfirstlane_b32 s64,v0\n"
"s_nop 1\n"
"v_mov_b32 v38,s64\n"
"v_pk_add_f32 v[16:17],v[0:1],v[38:39] op_sel_hi:[1,0] neg_lo:[0,1] neg_hi:[0,1]\n"
"v_pk_add_f32 v[18:19],v[2:3],v[38:39] op_sel_hi:[1,0] neg_lo:[0,1] neg_hi:[0,1]\n"
"v_pk_add_f32 v[20:21],v[4:5],v[38:39] op_sel_hi:[1,0] neg_lo:[0,1] neg_hi:[0,1]\n"
"v_pk_add_f32 v[22:23],v[6:7],v[38:39] op_sel_hi:[1,0] neg_lo:[0,1] neg_hi:[0,1]\n"
"v_pk_add_f32 v[24:25],v[8:9],v[38:39] op_sel_hi:[1,0] neg_lo:[0,1] neg_hi:[0,1]\n"
"v_pk_add_f32 v[26:27],v[10:11],v[38:39] op_sel_hi:[1,0] neg_lo:[0,1] neg_hi:[0,1]\n"
"v_pk_add_f32 v[28:29],v[12:13],v[38:39] op_sel_hi:[1,0] neg_lo:[0,1] neg_hi:[0,1]\n"
"v_pk_add_f32 v[30:31],v[14:15],v[38:39] op_sel_hi:[1,0] neg_lo:[0,1] neg_hi:[0,1]\n"
"v_pk_add_f32 v[32:33],v[16:17],v[18:19]\n"
"v_pk_mul_f32 v[34:35],v[16:17],v[16:17]\n"
"v_pk_mul_f32 v[36:37],v[16:17],v[224:225]\n"
"v_pk_fma_f32 v[34:35],v[18:19],v[18:19],v[34:35]\n"
"v_pk_fma_f32 v[36:37],v[18:19],v[226:227],v[36:37]\n"
"v_pk_add_f32 v[32:33],v[32:33],v[20:21]\n"
"v_pk_fma_f32 v[34:35],v[20:21],v[20:21],v[34:35]\n"
"v_pk_fma_f32 v[36:37],v[20:21],v[228:229],v[36:37]\n"
"v_pk_add_f32 v[32:33],v[32:33],v[22:23]\n"
"v_pk_fma_f32 v[34:35],v[22:23],v[22:23],v[34:35]\n"
"v_pk_fma_f32 v[36:37],v[22:23],v[230:231],v[36:37]\n"
"v_pk_add_f32 v[32:33],v[32:33],v[24:25]\n"
"v_pk_fma_f32 v[34:35],v[24:25],v[24:25],v[34:35]\n"
"v_pk_fma_f32 v[36:37],v[24:25],v[232:233],v[36:37]\n"
"v_pk_add_f32 v[32:33],v[32:33],v[26:27]\n"
"v_pk_fma_f32 v[34:35],v[26:27],v[26:27],v[34:35]\n"
"v_pk_fma_f32 v[36:37],v[26:27],v[234:235],v[36:37]\n"
"v_pk_add_f32 v[32:33],v[32:33],v[28:29]\n"
"v_pk_fma_f32 v[34:35],v[28:29],v[28:29],v[34:35]\n"
"v_pk_fma_f32 v[36:37],v[28:29],v[236:237],v[36:37]\n"
"v_pk_add_f32 v[32:33],v[32:33],v[30:31]\n"
"v_pk_fma_f32 v[34:35],v[30:31],v[30:31],v[34:35]\n"
"v_pk_fma_f32 v[36:37],v[30:31],v[238:239],v[36:37]\n"
"v_add_f32 v32,v32,v33\n"
"v_add_f32 v33,v34,v35\n"
"v_add_f32 v34,v36,v37\n"
"v_add_f32_dpp v32,v32,v32 quad_perm:[1,0,3,2] row_mask:0xf bank_mask:0xf\n"
"v_add_f32_dpp v33,v33,v33 quad_perm:[1,0,3,2] row_mask:0xf bank_mask:0xf\n"
"v_add_f32_dpp v34,v34,v34 quad_perm:[1,0,3,2] row_mask:0xf bank_mask:0xf\n"
"v_add_f32_dpp v32,v32,v32 quad_perm:[2,3,0,1] row_mask:0xf bank_mask:0xf\n"
"v_add_f32_dpp v33,v33,v33 quad_perm:[2,3,0,1] row_mask:0xf bank_mask:0xf\n"
"v_add_f32_dpp v34,v34,v34 quad_perm:[2,3,0,1] row_mask:0xf bank_mask:0xf\n"
"v_add_f32_dpp v32,v32,v32 row_half_mirror row_mask:0xf bank_mask:0xf\n"
"v_add_f32_dpp v33,v33,v33 row_half_mirror row_mask:0xf bank_mask:0xf\n"
"v_add_f32_dpp v34,v34,v34 row_half_mirror row_mask:0xf bank_mask:0xf\n"
"v_add_f32_dpp v32,v32,v32 row_mirror row_mask:0xf bank_mask:0xf\n"
"v_add_f32_dpp v33,v33,v33 row_mirror row_mask:0xf bank_mask:0xf\n"
"v_add_f32_dpp v34,v34,v34 row_mirror row_mask:0xf bank_mask:0xf\n"
"v_add_f32_dpp v32,v32,v32 row_bcast:15 row_mask:0xa bank_mask:0xf\n"
"v_add_f32_dpp v33,v33,v33 row_bcast:15 row_mask:0xa bank_mask:0xf\n"
"v_add_f32_dpp v34,v34,v34 row_bcast:15 row_mask:0xa bank_mask:0xf\n"
"v_add_f32_dpp v32,v32,v32 row_bcast:31 row_mask:0xc bank_mask:0xf\n"
"v_add_f32_dpp v33,v33,v33 row_bcast:31 row_mask:0xc bank_mask:0xf\n"
"v_add_f32_dpp v34,v34,v34 row_bcast:31 row_mask:0xc bank_mask:0xf\n"
"v_mul_f32 v32,s67,v32\n"
"v_add_f32 v35,v32,v38\n"
"v_mul_f32 v32,v32,v32\n"
"v_cvt_pk_bf16_f32 v35,v35,v35\n"
"v_lshlrev_b32 v35,16,v35\n"
"v_fma_f32 v33,v33,s67,-v32\n"
"v_sub_f32 v36,v38,v35\n"
"v_max_f32 v33,0,v33\n"
"v_fmac_f32 v34,v36,v70\n"
"v_cvt_pk_bf16_f32 v33,v33,v33\n"
"v_lshlrev_b32 v33,16,v33\n"
"v_add_f32 v33,0x3727c5ac,v33\n"
"v_cvt_pk_bf16_f32 v33,v33,v33\n"
"v_lshlrev_b32 v33,16,v33\n"
"v_sqrt_f32 v33,v33\n"
"s_nop 0\n"
"v_cvt_pk_bf16_f32 v33,v33,v33\n"
"v_lshlrev_b32 v33,16,v33\n"
"v_rcp_f32 v36,v33\n"
"v_lshrrev_b32 v33,16,v33\n"
"v_mul_f32 v37,v34,v36\n"
"v_or_b32 v33,v33,v35\n"
"v_cvt_pk_bf16_f32 v37,v37,v37\n"
"v_ashrrev_i32 v36,31,v37\n"
"v_or_b32 v36,0x80000000,v36\n"
"v_xor_b32 v37,v37,v36\n"
"v_and_b32 v37,0xffff0000,v37\n"
"v_readlane_b32 s64,v33,63\n"
"v_readlane_b32 s63,v37,63\n"
"s_nop 1\n"
"s_setpc_b64 s[94:95]\n"
".Lfk_rm1:\n"
"s_add_u32 s91,s62,2097152\n"
"buffer_load_dwordx4 v[160:163],v126,s[24:27],s91 offen sc1\n"
"buffer_load_dwordx4 v[164:167],v117,s[24:27],s91 offen sc1\n"
"s_add_u32 s91,s91,4194304\n"
"buffer_load_dwordx4 v[168:171],v126,s[24:27],s91 offen sc1\n"
"buffer_load_dwordx4 v[172:175],v117,s[24:27],s91 offen sc1\n"
"s_add_u32 s91,s91,4194304\n"
"buffer_load_dwordx4 v[176:179],v126,s[24:27],s91 offen sc1\n"
"buffer_load_dwordx4 v[180:183],v117,s[24:27],s91 offen sc1\n"
"s_add_u32 s91,s91,4194304\n"
"buffer_load_dwordx4 v[184:187],v126,s[24:27],s91 offen sc1\n"
"buffer_load_dwordx4 v[188:191],v117,s[24:27],s91 offen sc1\n"
"s_sub_u32 s65,0xffff,s60\n"
"s_or_b32 s65,s65,s63\n"
"s_mov_b32 s66,s64\n"
"v_lshlrev_b32 v34,16,s64\n"
"v_mov_b32 v35,s64\n"
"v_rcp_f32 v34,v34\n"
"v_and_b32 v35,0xffff0000,v35\n"
"v_cvt_pk_bf16_f32 v240,v0,v1\n"
"v_cvt_pk_bf16_f32 v241,v2,v3\n"
"v_cvt_pk_bf16_f32 v242,v4,v5\n"
"v_cvt_pk_bf16_f32 v243,v6,v7\n"
"v_cvt_pk_bf16_f32 v244,v8,v9\n"
"v_cvt_pk_bf16_f32 v245,v10,v11\n"
"v_cvt_pk_bf16_f32 v246,v12,v13\n"
"v_cvt_pk_bf16_f32 v247,v14,v15\n"
"v_lshlrev_b32 v0,16,v240\n"
"v_and_b32 v1,0xffff0000,v240\n"
"v_lshlrev_b32 v2,16,v241\n"
"v_and_b32 v3,0xffff0000,v241\n"
"v_lshlrev_b32 v4,16,v242\n"
"v_and_b32 v5,0xffff0000,v242\n"
"v_lshlrev_b32 v6,16,v243\n"
"v_and_b32 v7,0xffff0000,v243\n"
"v_lshlrev_b32 v8,16,v244\n"
"v_and_b32 v9,0xffff0000,v244\n"
"v_lshlrev_b32 v10,16,v245\n"
"v_and_b32 v11,0xffff0000,v245\n"
"v_lshlrev_b32 v12,16,v246\n"
"v_and_b32 v13,0xffff0000,v246\n"
"v_lshlrev_b32 v14,16,v247\n"
"v_and_b32 v15,0xffff0000,v247\n"
"v_pk_add_f32 v[16:17],v[0:1],v[34:35] op_sel:[0,1] neg_lo:[0,1] neg_hi:[0,1]\n"
"v_pk_add_f32 v[18:19],v[2:3],v[34:35] op_sel:[0,1] neg_lo:[0,1] neg_hi:[0,1]\n"
"v_pk_add_f32 v[20:21],v[4:5],v[34:35] op_sel:[0,1] neg_lo:[0,1] neg_hi:[0,1]\n"
"v_pk_add_f32 v[22:23],v[6:7],v[34:35] op_sel:[0,1] neg_lo:[0,1] neg_hi:[0,1]\n"
"v_pk_add_f32 v[24:25],v[8:9],v[34:35] op_sel:[0,1] neg_lo:[0,1] neg_hi:[0,1]\n"
"v_pk_add_f32 v[26:27],v[10:11],v[34:35] op_sel:[0,1] neg_lo:[0,1] neg_hi:[0,1]\n"
"v_pk_add_f32 v[28:29],v[12:13],v[34:35] op_sel:[0,1] neg_lo:[0,1] neg_hi:[0,1]\n"
"v_pk_add_f32 v[30:31],v[14:15],v[34:35] op_sel:[0,1] neg_lo:[0,1] neg_hi:[0,1]\n"
"v_cvt_pk_bf16_f32 v240,v16,v17\n"
"v_cvt_pk_bf16_f32 v241,v18,v19\n"
"v_cvt_pk_bf16_f32 v242,v20,v21\n"
"v_cvt_pk_bf16_f32 v243,v22,v23\n"
"v_cvt_pk_bf16_f32 v244,v24,v25\n"
"v_cvt_pk_bf16_f32 v245,v26,v27\n"
"v_cvt_pk_bf16_f32 v246,v28,v29\n"
"v_cvt_pk_bf16_f32 v247,v30,v31\n"
"v_lshlrev_b32 v16,16,v240\n"
"v_and_b32 v17,0xffff0000,v240\n"
"v_lshlrev_b32 v18,16,v241\n"
"v_and_b32 v19,0xffff0000,v241\n"
"v_lshlrev_b32 v20,16,v242\n"
"v_and_b32 v21,0xffff0000,v242\n"
"v_lshlrev_b32 v22,16,v243\n"
"v_and_b32 v23,0xffff0000,v243\n"
"v_lshlrev_b32 v24,16,v244\n"
"v_and_b32 v25,0xffff0000,v244\n"
"v_lshlrev_b32 v26,16,v245\n"
"v_and_b32 v27,0xffff0000,v245\n"
"v_lshlrev_b32 v28,16,v246\n"
"v_and_b32 v29,0xffff0000,v246\n"
"v_lshlrev_b32 v30,16,v247\n"
"v_and_b32 v31,0xffff0000,v247\n"
"v_pk_mul_f32 v[16:17],v[16:17],v[34:35] op_sel_hi:[1,0]\n"
"v_pk_mul_f32 v[18:19],v[18:19],v[34:35] op_sel_hi:[1,0]\n"
"v_pk_mul_f32 v[20:21],v[20:21],v[34:35] op_sel_hi:[1,0]\n"
"v_pk_mul_f32 v[22:23],v[22:23],v[34:35] op_sel_hi:[1,0]\n"
"v_pk_mul_f32 v[24:25],v[24:25],v[34:35] op_sel_hi:[1,0]\n"
"v_pk_mul_f32 v[26:27],v[26:27],v[34:35] op_sel_hi:[1,0]\n"
"v_pk_mul_f32 v[28:29],v[28:29],v[34:35] op_sel_hi:[1,0]\n"
"v_pk_mul_f32 v[30:31],v[30:31],v[34:35] op_sel_hi:[1,0]\n"
"v_cvt_pk_bf16_f32 v240,v16,v17\n"
"v_cvt_pk_bf16_f32 v241,v18,v19\n"
"v_cvt_pk_bf16_f32 v242,v20,v21\n"
"v_cvt_pk_bf16_f32 v243,v22,v23\n"
"v_cvt_pk_bf16_f32 v244,v24,v25\n"
"v_cvt_pk_bf16_f32 v245,v26,v27\n"
"v_cvt_pk_bf16_f32 v246,v28,v29\n"
"v_cvt_pk_bf16_f32 v247,v30,v31\n"
"v_max_u32 v216,1,v240\n"
"v_max_u32 v217,1,v241\n"
"v_max_u32 v218,1,v242\n"
"v_max_u32 v219,1,v243\n"
"v_max_u32 v220,1,v244\n"
"v_max_u32 v221,1,v245\n"
"v_max_u32 v222,1,v246\n"
"v_max_u32 v223,1,v247\n"
"v_lshlrev_b32 v16,16,v240\n"
"v_and_b32 v17,0xffff0000,v240\n"
"v_lshlrev_b32 v18,16,v241\n"
"v_and_b32 v19,0xffff0000,v241\n"
"v_lshlrev_b32 v20,16,v242\n"
"v_and_b32 v21,0xffff0000,v242\n"
"v_lshlrev_b32 v22,16,v243\n"
"v_and_b32 v23,0xffff0000,v243\n"
"v_lshlrev_b32 v24,16,v244\n"
"v_and_b32 v25,0xffff0000,v244\n"
"v_lshlrev_b32 v26,16,v245\n"
"v_and_b32 v27,0xffff0000,v245\n"
"v_lshlrev_b32 v28,16,v246\n"
"v_and_b32 v29,0xffff0000,v246\n"
"v_lshlrev_b32 v30,16,v247\n"
"v_and_b32 v31,0xffff0000,v247\n"
"v_pk_add_f32 v[16:17],v[16:17],v[0:1]\n"
"v_pk_add_f32 v[18:19],v[18:19],v[2:3]\n"
"v_pk_add_f32 v[20:21],v[20:21],v[4:5]\n"
"v_pk_add_f32 v[22:23],v[22:23],v[6:7]\n"
"v_pk_add_f32 v[24:25],v[24:25],v[8:9]\n"
"v_pk_add_f32 v[26:27],v[26:27],v[10:11]\n"
"v_pk_add_f32 v[28:29],v[28:29],v[12:13]\n"
"v_pk_add_f32 v[30:31],v[30:31],v[14:15]\n"
"v_cvt_pk_bf16_f32 v240,v16,v17\n"
"v_cvt_pk_bf16_f32 v241,v18,v19\n"
"v_cvt_pk_bf16_f32 v242,v20,v21\n"
"v_cvt_pk_bf16_f32 v243,v22,v23\n"
"v_cvt_pk_bf16_f32 v244,v24,v25\n"
"v_cvt_pk_bf16_f32 v245,v26,v27\n"
"v_cvt_pk_bf16_f32 v246,v28,v29\n"
"v_cvt_pk_bf16_f32 v247,v30,v31\n"
"buffer_store_dwordx4 v[240:243],v112,s[48:51],s61 offen offset:0 sc1\n"
"buffer_store_dwordx4 v[244:247],v112,s[48:51],s61 offen offset:1024 sc1\n"
"s_barrier\n"
"s_lshl_b32 s82,s52,13\n"
"s_add_u32 s82,s82,0\n"
"v_bfe_u32 v41,v112,5,2\n"
"v_lshlrev_b32 v41,13,v41\n"
"v_bfe_u32 v42,v112,4,1\n"
"v_lshl_add_u32 v41,v42,3,v41\n"
"s_lshl_b32 s83,s100,4\n"
"v_mfma_f32_16x16x32_bf16 v[200:203],v[66:69],a[104:107],0\n"
"s_add_u32 s83,s83,149946368\n"
"v_mov_b32 v45,s66\n"
"v_add_u32 v41,0,v41\n"
"v_mfma_f32_16x16x32_bf16 v[204:207],v[64:67],a[104:107],0\n"
"v_mov_b32 v40,s82\n"
"v_mov_b32 v51,s80\n"
"v_mfma_f32_16x16x32_bf16 v[208:211],v[66:69],a[108:111],0\n"
"v_mov_b32 v44,s65\n"
"v_lshlrev_b32 v43,8,v112\n"
"v_mfma_f32_16x16x32_bf16 v[212:215],v[64:67],a[108:111],0\n"
"s_mov_b32 s84,0x40000\n"
"v_mov_b32 v49,s80\n"
"s_waitcnt vmcnt(2)\n"
".Lfk_w1:\n"
"v_mfma_f32_16x16x32_bf16 v[0:3],v[66:69],v[160:163],v[200:203]\n"
"v_min3_u32 v36,v160,v161,v162\n"
"v_min3_u32 v37,v163,v164,v165\n"
"v_mfma_f32_16x16x32_bf16 v[4:7],v[64:67],v[160:163],v[204:207]\n"
"v_min3_u32 v36,v36,v166,v167\n"
"v_min3_u32 v37,v37,v168,v169\n"
"v_mfma_f32_16x16x32_bf16 v[8:11],v[66:69],v[164:167],v[208:211]\n"
"v_min3_u32 v36,v36,v170,v171\n"
"v_min3_u32 v37,v37,v172,v173\n"
"v_mfma_f32_16x16x32_bf16 v[12:15],v[64:67],v[164:167],v[212:215]\n"
"v_min3_u32 v36,v36,v174,v175\n"
"v_min3_u32 v37,v37,v176,v177\n"
"v_mfma_f32_16x16x32_bf16 v[0:3],v[66:69],v[168:171],v[0:3]\n"
"v_min3_u32 v36,v36,v178,v179\n"
"v_min3_u32 v37,v37,v180,v181\n"
"v_mfma_f32_16x16x32_bf16 v[4:7],v[64:67],v[168:171],v[4:7]\n"
"v_min3_u32 v36,v36,v182,v183\n"
"v_min3_u32 v37,v37,v184,v185\n"
"v_mfma_f32_16x16x32_bf16 v[8:11],v[66:69],v[172:175],v[8:11]\n"
"v_min3_u32 v36,v36,v186,v187\n"
"v_min3_u32 v37,v37,v188,v189\n"
"v_mfma_f32_16x16x32_bf16 v[12:15],v[64:67],v[172:175],v[12:15]\n"
"v_min3_u32 v36,v36,v190,v191\n"
"v_min_u32 v36,v36,v37\n"
"v_cmp_eq_u32 vcc,0,v36\n"
"v_mfma_f32_16x16x32_bf16 v[0:3],v[66:69],v[176:179],v[0:3]\n"
"s_cbranch_vccz .Lfk_w1_x\n"
"s_mov_b64 exec,vcc\n"
"s_add_u32 s91,s62,2097152\n"
"buffer_load_dwordx4 v[160:163],v126,s[24:27],s91 offen sc1\n"
"buffer_load_dwordx4 v[164:167],v117,s[24:27],s91 offen sc1\n"
"s_add_u32 s91,s91,4194304\n"
"buffer_load_dwordx4 v[168:171],v126,s[24:27],s91 offen sc1\n"
"buffer_load_dwordx4 v[172:175],v117,s[24:27],s91 offen sc1\n"
"s_add_u32 s91,s91,4194304\n"
"buffer_load_dwordx4 v[176:179],v126,s[24:27],s91 offen sc1\n"
"buffer_load_dwordx4 v[180:183],v117,s[24:27],s91 offen sc1\n"
"s_add_u32 s91,s91,4194304\n"
"buffer_load_dwordx4 v[184:187],v126,s[24:27],s91 offen sc1\n"
"buffer_load_dwordx4 v[188:191],v117,s[24:27],s91 offen sc1\n"
"s_mov_b64 exec,-1\n"
"s_sub_u32 s84,s84,1\n"
"s_waitcnt vmcnt(0)\n"
"s_cbranch_scc0 .Lfk_w1\n"
".Lfk_w1_x:\n"
"v_mfma_f32_16x16x32_bf16 v[4:7],v[64:67],v[176:179],v[4:7]\n"
"v_mfma_f32_16x16x32_bf16 v[8:11],v[66:69],v[180:183],v[8:11]\n"
"v_mfma_f32_16x16x32_bf16 v[12:15],v[64:67],v[180:183],v[12:15]\n"
"v_mfma_f32_16x16x32_bf16 v[0:3],v[66:69],v[184:187],v[0:3]\n"
"v_mfma_f32_16x16x32_bf16 v[4:7],v[64:67],v[184:187],v[4:7]\n"
"v_mfma_f32_16x16x32_bf16 v[8:11],v[66:69],v[188:191],v[8:11]\n"
"v_mfma_f32_16x16x32_bf16 v[12:15],v[64:67],v[188:191],v[12:15]\n"
"s_nop 6\n"
"s_call_b64 s[94:95],.Lfk_rm\n"
"s_sub_u32 s82,64511,s60\n"
"s_or_b32 s63,s63,s82\n"
"v_mov_b32 v46,s63\n"
"v_mov_b32 v47,s64\n"
"v_mov_b32 v39,0\n"
"ds_write_b128 v40,v[44:47]\n"
"s_waitcnt vmcnt(0) lgkmcnt(0)\n"
"s_barrier\n"
"ds_read_b64 v[52:53],v41\n"
"s_waitcnt lgkmcnt(0)\n"
"v_mov_b32 v48,v52\n"
"s_nop 1\n"
"v_min_u32_dpp v54,v48,v48 quad_perm:[1,0,3,2] row_mask:0xf bank_mask:0xf\n"
"v_max_u32_dpp v48,v48,v48 quad_perm:[1,0,3,2] row_mask:0xf bank_mask:0xf\n"
"v_max_u32_dpp v39,v39,v39 quad_perm:[1,0,3,2] row_mask:0xf bank_mask:0xf\n"
"v_max_u32 v39,v39,v54\n"
"v_min_u32_dpp v54,v48,v48 quad_perm:[2,3,0,1] row_mask:0xf bank_mask:0xf\n"
"v_max_u32_dpp v48,v48,v48 quad_perm:[2,3,0,1] row_mask:0xf bank_mask:0xf\n"
"v_max_u32_dpp v39,v39,v39 quad_perm:[2,3,0,1] row_mask:0xf bank_mask:0xf\n"
"v_max_u32 v39,v39,v54\n"
"v_min_u32_dpp v54,v48,v48 row_half_mirror row_mask:0xf bank_mask:0xf\n"
"v_max_u32_dpp v48,v48,v48 row_half_mirror row_mask:0xf bank_mask:0xf\n"
"v_max_u32_dpp v39,v39,v39 row_half_mirror row_mask:0xf bank_mask:0xf\n"
"v_max_u32 v39,v39,v54\n"
"v_xor_b32 v50,0x800,v39\n"
"s_cmp_lg_u32 s52,0\n"
"s_cbranch_scc1 .Lfk_t1\n"
"s_mov_b64 exec,0xff\n"
"buffer_store_dwordx4 v[48:51],v43,s[24:27],s83 offen sc1\n"
"s_mov_b64 exec,-1\n"
"v_cmp_eq_u32 vcc,v48,v52\n"
"s_nop 0\n"
"s_ff1_i32_b64 s82,vcc\n"
"v_readlane_b32 s90,v53,s82\n"
"s_nop 1\n"
"v_mov_b32 v44,s90\n"
"v_mov_b32 v45,s80\n"
"v_cmp_eq_u32 vcc,v39,v52\n"
"s_nop 0\n"
"s_ff1_i32_b64 s82,vcc\n"
"v_readlane_b32 s90,v53,s82\n"
"s_nop 1\n"
"v_mov_b32 v46,s90\n"
"v_mov_b32 v47,s80\n"
"s_mov_b64 exec,0x100\n"
"buffer_store_dwordx4 v[44:47],v43,s[24:27],s83 offen sc1\n"
"s_mov_b64 exec,-1\n"
".Lfk_t1:\n"
"v_readfirstlane_b32 s89,v39\n"
"s_add_u32 m0,s30,131072\n"
"s_nop 0\n"
"buffer_load_dwordx4 v96,s[40:43],0 offen offset:0 nt lds\n"
"buffer_load_dwordx4 v96,s[40:43],0 offen offset:1024 nt lds\n"
"buffer_load_dwordx4 v97,s[40:43],0 offen offset:2048 nt lds\n"
"buffer_load_dwordx4 v97,s[40:43],0 offen offset:3072 nt lds\n"
"v_lshlrev_b32 v34,16,s64\n"
"v_mov_b32 v35,s64\n"
"v_rcp_f32 v34,v34\n"
"v_and_b32 v35,0xffff0000,v35\n"
"v_cvt_pk_bf16_f32 v248,v0,v1\n"
"v_cvt_pk_bf16_f32 v249,v2,v3\n"
"v_cvt_pk_bf16_f32 v250,v4,v5\n"
"v_cvt_pk_bf16_f32 v251,v6,v7\n"
"v_cvt_pk_bf16_f32 v252,v8,v9\n"
"v_cvt_pk_bf16_f32 v253,v10,v11\n"
"v_cvt_pk_bf16_f32 v254,v12,v13\n"
"v_cvt_pk_bf16_f32 v255,v14,v15\n"
"v_lshlrev_b32 v0,16,v248\n"
"v_and_b32 v1,0xffff0000,v248\n"
"v_lshlrev_b32 v2,16,v249\n"
"v_and_b32 v3,0xffff0000,v249\n"
"v_lshlrev_b32 v4,16,v250\n"
"v_and_b32 v5,0xffff0000,v250\n"
"v_lshlrev_b32 v6,16,v251\n"
"v_and_b32 v7,0xffff0000,v251\n"
"v_lshlrev_b32 v8,16,v252\n"
"v_and_b32 v9,0xffff0000,v252\n"
"v_lshlrev_b32 v10,16,v253\n"
"v_and_b32 v11,0xffff0000,v253\n"
"v_lshlrev_b32 v12,16,v254\n"
"v_and_b32 v13,0xffff0000,v254\n"
"v_lshlrev_b32 v14,16,v255\n"
"v_and_b32 v15,0xffff0000,v255\n"
"v_pk_add_f32 v[16:17],v[0:1],v[34:35] op_sel:[0,1] neg_lo:[0,1] neg_hi:[0,1]\n"
"v_pk_add_f32 v[18:19],v[2:3],v[34:35] op_sel:[0,1] neg_lo:[0,1] neg_hi:[0,1]\n"
"v_pk_add_f32 v[20:21],v[4:5],v[34:35] op_sel:[0,1] neg_lo:[0,1] neg_hi:[0,1]\n"
"v_pk_add_f32 v[22:23],v[6:7],v[34:35] op_sel:[0,1] neg_lo:[0,1] neg_hi:[0,1]\n"
"v_pk_add_f32 v[24:25],v[8:9],v[34:35] op_sel:[0,1] neg_lo:[0,1] neg_hi:[0,1]\n"
"v_pk_add_f32 v[26:27],v[10:11],v[34:35] op_sel:[0,1] neg_lo:[0,1] neg_hi:[0,1]\n"
"v_pk_add_f32 v[28:29],v[12:13],v[34:35] op_sel:[0,1] neg_lo:[0,1] neg_hi:[0,1]\n"
"v_pk_add_f32 v[30:31],v[14:15],v[34:35] op_sel:[0,1] neg_lo:[0,1] neg_hi:[0,1]\n"
"v_cvt_pk_bf16_f32 v248,v16,v17\n"
"v_cvt_pk_bf16_f32 v249,v18,v19\n"
"v_cvt_pk_bf16_f32 v250,v20,v21\n"
"v_cvt_pk_bf16_f32 v251,v22,v23\n"
"v_cvt_pk_bf16_f32 v252,v24,v25\n"
"v_cvt_pk_bf16_f32 v253,v26,v27\n"
"v_cvt_pk_bf16_f32 v254,v28,v29\n"
"v_cvt_pk_bf16_f32 v255,v30,v31\n"
"v_lshlrev_b32 v16,16,v248\n"
"v_and_b32 v17,0xffff0000,v248\n"
"v_lshlrev_b32 v18,16,v249\n"
"v_and_b32 v19,0xffff0000,v249\n"
"v_lshlrev_b32 v20,16,v250\n"
"v_and_b32 v21,0xffff0000,v250\n"
"v_lshlrev_b32 v22,16,v251\n"
"v_and_b32 v23,0xffff0000,v251\n"
"v_lshlrev_b32 v24,16,v252\n"
"v_and_b32 v25,0xffff0000,v252\n"
"v_lshlrev_b32 v26,16,v253\n"
"v_and_b32 v27,0xffff0000,v253\n"
"v_lshlrev_b32 v28,16,v254\n"
"v_and_b32 v29,0xffff0000,v254\n"
"v_lshlrev_b32 v30,16,v255\n"
"v_and_b32 v31,0xffff0000,v255\n"
"v_pk_mul_f32 v[16:17],v[16:17],v[34:35] op_sel_hi:[1,0]\n"
"v_pk_mul_f32 v[18:19],v[18:19],v[34:35] op_sel_hi:[1,0]\n"
"v_pk_mul_f32 v[20:21],v[20:21],v[34:35] op_sel_hi:[1,0]\n"
"v_pk_mul_f32 v[22:23],v[22:23],v[34:35] op_sel_hi:[1,0]\n"
"v_pk_mul_f32 v[24:25],v[24:25],v[34:35] op_sel_hi:[1,0]\n"
"v_pk_mul_f32 v[26:27],v[26:27],v[34:35] op_sel_hi:[1,0]\n"
"v_pk_mul_f32 v[28:29],v[28:29],v[34:35] op_sel_hi:[1,0]\n"
"v_pk_mul_f32 v[30:31],v[30:31],v[34:35] op_sel_hi:[1,0]\n"
"v_cvt_pk_bf16_f32 v248,v16,v17\n"
"v_cvt_pk_bf16_f32 v249,v18,v19\n"
"v_cvt_pk_bf16_f32 v250,v20,v21\n"
"v_cvt_pk_bf16_f32 v251,v22,v23\n"
"v_cvt_pk_bf16_f32 v252,v24,v25\n"
"v_cvt_pk_bf16_f32 v253,v26,v27\n"
"v_cvt_pk_bf16_f32 v254,v28,v29\n"
"v_cvt_pk_bf16_f32 v255,v30,v31\n"
"v_max_u32 v208,1,v248\n"
"v_max_u32 v209,1,v249\n"
"v_max_u32 v210,1,v250\n"
"v_max_u32 v211,1,v251\n"
"v_max_u32 v212,1,v252\n"
"v_max_u32 v213,1,v253\n"
"v_max_u32 v214,1,v254\n"
"v_max_u32 v215,1,v255\n"
"s_and_b32 s82,s60,255\n"
"s_lshl_b32 s82,s82,12\n"
"s_cmp_eq_u32 s63,s89\n"
"s_cselect_b32 s83,2048,0\n"
"s_add_u32 s82,s82,s83\n"
"s_add_u32 s82,s82,174063616\n"
"s_cmp_ge_u32 s63,s89\n"
"s_cselect_b32 s82,s82,2146435072\n"
"buffer_store_dwordx4 v[208:211],v112,s[24:27],s82 offen offset:0 sc1\n"
"buffer_store_dwordx4 v[212:215],v112,s[24:27],s82 offen offset:1024 sc1\n"
"s_and_b32 s82,s60,255\n"
"s_lshl_b32 s82,s82,12\n"
"s_cmp_eq_u32 s65,s89\n"
"s_cselect_b32 s83,2048,0\n"
"s_add_u32 s82,s82,s83\n"
"s_add_u32 s82,s82,174063616\n"
"s_cmp_ge_u32 s65,s89\n"
"s_cselect_b32 s82,s82,2146435072\n"
"buffer_store_dwordx4 v[216:219],v112,s[24:27],s82 offen offset:0 sc1\n"
"buffer_store_dwordx4 v[220:223],v112,s[24:27],s82 offen offset:1024 sc1\n"
"v_lshlrev_b32 v16,16,v248\n"
"v_and_b32 v17,0xffff0000,v248\n"
"v_lshlrev_b32 v18,16,v249\n"
"v_and_b32 v19,0xffff0000,v249\n"
"v_lshlrev_b32 v20,16,v250\n"
"v_and_b32 v21,0xffff0000,v250\n"
"v_lshlrev_b32 v22,16,v251\n"
"v_and_b32 v23,0xffff0000,v251\n"
"v_lshlrev_b32 v24,16,v252\n"
"v_and_b32 v25,0xffff0000,v252\n"
"v_lshlrev_b32 v26,16,v253\n"
"v_and_b32 v27,0xffff0000,v253\n"
"v_lshlrev_b32 v28,16,v254\n"
"v_and_b32 v29,0xffff0000,v254\n"
"v_lshlrev_b32 v30,16,v255\n"
"v_and_b32 v31,0xffff0000,v255\n"
"v_pk_add_f32 v[16:17],v[16:17],v[0:1]\n"
"v_pk_add_f32 v[18:19],v[18:19],v[2:3]\n"
"v_pk_add_f32 v[20:21],v[20:21],v[4:5]\n"
"v_pk_add_f32 v[22:23],v[22:23],v[6:7]\n"
"v_pk_add_f32 v[24:25],v[24:25],v[8:9]\n"
"v_pk_add_f32 v[26:27],v[26:27],v[10:11]\n"
"v_pk_add_f32 v[28:29],v[28:29],v[12:13]\n"
"v_pk_add_f32 v[30:31],v[30:31],v[14:15]\n"
"v_cvt_pk_bf16_f32 v248,v16,v17\n"
"v_cvt_pk_bf16_f32 v249,v18,v19\n"
"v_cvt_pk_bf16_f32 v250,v20,v21\n"
"v_cvt_pk_bf16_f32 v251,v22,v23\n"
"v_cvt_pk_bf16_f32 v252,v24,v25\n"
"v_cvt_pk_bf16_f32 v253,v26,v27\n"
"v_cvt_pk_bf16_f32 v254,v28,v29\n"
"v_cvt_pk_bf16_f32 v255,v30,v31\n"
"s_add_u32 s68,s61,2097152\n"
"s_cmp_lt_u32 s63,s89\n"
"s_cselect_b32 s83,s68,2146435072\n"
"s_cselect_b32 s68,2146435072,s68\n"
"buffer_store_dwordx4 v[248:251],v112,s[48:51],s83 offen offset:0 sc1\n"
"buffer_store_dwordx4 v[252:255],v112,s[48:51],s83 offen offset:1024 sc1\n"
"v_lshrrev_b32 v40,6,v112\n"
"v_and_b32 v41,7,v40\n"
"v_lshlrev_b32 v41,5,v41\n"
"v_bfe_u32 v42,v112,4,2\n"
"v_lshlrev_b32 v42,3,v42\n"
"v_lshl_add_u32 v40,v40,11,v42\n"
"s_lshl_b32 s82,s52,9\n"
"v_add_u32 v40,s82,v40\n"
"v_add_u32 v40,131072,v40\n"
"v_xor_b32 v42,0,v41\n"
"v_add_u32 v192,v40,v42\n"
"v_xor_b32 v42,32,v41\n"
"v_add_u32 v193,v40,v42\n"
"v_xor_b32 v42,64,v41\n"
"v_add_u32 v194,v40,v42\n"
"v_xor_b32 v42,96,v41\n"
"v_add_u32 v195,v40,v42\n"
"v_xor_b32 v42,128,v41\n"
"v_add_u32 v196,v40,v42\n"
"v_xor_b32 v42,160,v41\n"
"v_add_u32 v197,v40,v42\n"
"v_xor_b32 v42,192,v41\n"
"v_add_u32 v198,v40,v42\n"
"v_xor_b32 v42,224,v41\n"
"v_add_u32 v199,v40,v42\n"
"v_bfe_u32 v56,v112,4,1\n"
"v_lshrrev_b32 v57,1,v112\n"
"v_and_b32 v57,0x1f0,v57\n"
"s_lshl_b32 s83,s52,13\n"
"s_add_u32 s83,s83,1024\n"
"v_lshl_add_u32 v58,v56,9,s83\n"
"v_lshrrev_b32 v59,8,v112\n"
"v_lshl_add_u32 v59,v59,4,s82\n"
"v_add_u32 v59,174063616,v59\n"
"v_add_u32 v58,v58,v57\n"
"v_add_u32 v57,s82,v57\n"
"v_and_b32 v55,0x7c0,v57\n"
"v_and_b32 v54,0x30,v57\n"
"v_lshl_add_u32 v55,v55,4,v54\n"
"v_lshrrev_b32 v54,2,v55\n"
"v_and_b32 v54,0x0,v54\n"
"v_or_b32 v55,v55,v54\n"
"v_add_u32 v60,s83,v112\n"
"v_add_u32 v61,1024,v112\n"
"v_bfe_u32 v62,v112,5,3\n"
"v_cmp_eq_u32 s[8:9],0,v62\n"
"v_cmp_eq_u32 s[10:11],1,v62\n"
"v_cmp_eq_u32 s[12:13],2,v62\n"
"v_cmp_eq_u32 s[14:15],3,v62\n"
"v_cmp_eq_u32 s[16:17],4,v62\n"
"v_cmp_eq_u32 s[18:19],5,v62\n"
"v_cmp_eq_u32 s[20:21],6,v62\n"
"v_cmp_eq_u32 s[22:23],7,v62\n"
"v_lshlrev_b32 v62,6,v62\n"
"v_lshl_add_u32 v62,v56,12,v62\n"
"v_lshrrev_b32 v63,8,v112\n"
"v_lshl_add_u32 v62,v63,4,v62\n"
"s_lshl_b32 s69,s72,13\n"
"s_lshl_b32 s82,s52,10\n"
"s_add_u32 s69,s69,s82\n"
"s_add_u32 s69,s69,171966464\n"
"v_lshrrev_b32 v63,5,v112\n"
"v_min_u32 v63,27,v63\n"
"v_lshlrev_b32 v63,13,v63\n"
"v_lshl_add_u32 v63,v56,4,v63\n"
"s_mul_i32 s70,s52,458752\n"
"s_lshl_b32 s82,s73,5\n"
"s_add_u32 s70,s70,s82\n"
"s_add_u32 s70,s70,171966464\n"
"s_add_u32 s71,s70,229376\n"
"s_and_b32 s83,s100,7\n"
"s_lshl_b32 s83,s83,12\n"
"s_add_u32 s83,s83,149946368\n"
"v_add_u32 v20,0,v112\n"
"v_lshlrev_b32 v20,0,v20\n"
"v_add_u32 v21,1024,v112\n"
"v_lshlrev_b32 v21,0,v21\n"
"v_add_u32 v22,2048,v112\n"
"v_lshlrev_b32 v22,0,v22\n"
"v_add_u32 v23,3072,v112\n"
"v_lshlrev_b32 v23,0,v23\n"
"s_sleep 20\n"
".Lfk_nn:\n"
"s_mov_b32 s84,0x40000\n"
"buffer_load_dwordx4 v[0:3],v20,s[24:27],s83 offen sc1\n"
"buffer_load_dwordx4 v[4:7],v21,s[24:27],s83 offen sc1\n"
"buffer_load_dwordx4 v[8:11],v22,s[24:27],s83 offen sc1\n"
"buffer_load_dwordx4 v[12:15],v23,s[24:27],s83 offen sc1\n"
"s_waitcnt vmcnt(0)\n"
".Lfk_wc:\n"
"v_xor_b32 v24,s80,v1\n"
"v_xor_b32 v25,s80,v3\n"
"v_xor_b32 v26,s80,v5\n"
"v_xor_b32 v27,s80,v7\n"
"v_xor_b32 v28,s80,v9\n"
"v_xor_b32 v29,s80,v11\n"
"v_xor_b32 v30,s80,v13\n"
"v_xor_b32 v31,s80,v15\n"
"v_or3_b32 v16,v24,v25,v26\n"
"v_or3_b32 v17,v27,v28,v29\n"
"v_or3_b32 v16,v16,v30,v31\n"
"v_or_b32 v16,v16,v17\n"
"v_cmp_ne_u32 vcc,0,v16\n"
"s_nop 0\n"
"s_cbranch_vccz .Lfk_wc_x\n"
"s_sleep 2\n"
"s_mov_b64 exec,vcc\n"
"buffer_load_dwordx4 v[0:3],v20,s[24:27],s83 offen sc1\n"
"buffer_load_dwordx4 v[4:7],v21,s[24:27],s83 offen sc1\n"
"buffer_load_dwordx4 v[8:11],v22,s[24:27],s83 offen sc1\n"
"buffer_load_dwordx4 v[12:15],v23,s[24:27],s83 offen sc1\n"
"s_mov_b64 exec,-1\n"
"s_sub_u32 s84,s84,1\n"
"s_waitcnt vmcnt(0)\n"
"s_cbranch_scc0 .Lfk_wc\n"
".Lfk_wc_x:\n"
"v_min_u32 v16,v0,v4\n"
"v_max_u32 v0,v0,v4\n"
"v_max3_u32 v2,v2,v6,v16\n"
"v_min_u32 v16,v8,v12\n"
"v_max_u32 v8,v8,v12\n"
"v_max3_u32 v10,v10,v14,v16\n"
"v_min_u32 v16,v0,v8\n"
"v_max_u32 v0,v0,v8\n"
"v_max3_u32 v2,v2,v10,v16\n"
"v_cmp_eq_u32 vcc,0,v56\n"
"v_min_u32_dpp v16,v0,v0 quad_perm:[1,0,3,2] row_mask:0xf bank_mask:0xf\n"
"v_max_u32_dpp v0,v0,v0 quad_perm:[1,0,3,2] row_mask:0xf bank_mask:0xf\n"
"v_max_u32_dpp v2,v2,v2 quad_perm:[1,0,3,2] row_mask:0xf bank_mask:0xf\n"
"v_max_u32 v2,v2,v16\n"
"v_min_u32_dpp v16,v0,v0 quad_perm:[2,3,0,1] row_mask:0xf bank_mask:0xf\n"
"v_max_u32_dpp v0,v0,v0 quad_perm:[2,3,0,1] row_mask:0xf bank_mask:0xf\n"
"v_max_u32_dpp v2,v2,v2 quad_perm:[2,3,0,1] row_mask:0xf bank_mask:0xf\n"
"v_max_u32 v2,v2,v16\n"
"v_min_u32_dpp v16,v0,v0 row_half_mirror row_mask:0xf bank_mask:0xf\n"
"v_max_u32_dpp v0,v0,v0 row_half_mirror row_mask:0xf bank_mask:0xf\n"
"v_max_u32_dpp v2,v2,v2 row_half_mirror row_mask:0xf bank_mask:0xf\n"
"v_max_u32 v2,v2,v16\n"
"v_min_u32_dpp v16,v0,v0 row_mirror row_mask:0xf bank_mask:0xf\n"
"v_max_u32_dpp v0,v0,v0 row_mirror row_mask:0xf bank_mask:0xf\n"
"v_max_u32_dpp v2,v2,v2 row_mirror row_mask:0xf bank_mask:0xf\n"
"v_max_u32 v2,v2,v16\n"
"v_min_u32_dpp v16,v0,v0 row_bcast:15 row_mask:0xa bank_mask:0xf\n"
"v_max_u32_dpp v0,v0,v0 row_bcast:15 row_mask:0xa bank_mask:0xf\n"
"v_max_u32_dpp v2,v2,v2 row_bcast:15 row_mask:0xa bank_mask:0xf\n"
"v_max_u32 v2,v2,v16\n"
"v_min_u32_dpp v16,v0,v0 row_bcast:31 row_mask:0xc bank_mask:0xf\n"
"v_max_u32_dpp v0,v0,v0 row_bcast:31 row_mask:0xc bank_mask:0xf\n"
"v_max_u32_dpp v2,v2,v2 row_bcast:31 row_mask:0xc bank_mask:0xf\n"
"v_max_u32 v2,v2,v16\n"
"v_readlane_b32 s90,v0,63\n"
"v_readlane_b32 s92,v2,63\n"
"s_nop 0\n"
"v_mov_b32 v4,s90\n"
"v_mov_b32 v5,s92\n"
"v_cndmask_b32 v6,v5,v4,vcc\n"
"v_not_b32 v7,v6\n"
"v_bfe_u32 v9,v7,0,8\n"
"v_and_b32 v11,0x800,v7\n"
"v_lshl_add_u32 v11,v9,12,v11\n"
"v_add_u32 v11,v11,v59\n"
"buffer_load_dwordx4 v[128:131],v11,s[24:27],0 offen offset:0 sc1\n"
"buffer_load_dwordx4 v[132:135],v11,s[24:27],0 offen offset:64 sc1\n"
"buffer_load_dwordx4 v[136:139],v11,s[24:27],0 offen offset:128 sc1\n"
"buffer_load_dwordx4 v[140:143],v11,s[24:27],0 offen offset:192 sc1\n"
"buffer_load_dwordx4 v[144:147],v11,s[24:27],0 offen offset:256 sc1\n"
"buffer_load_dwordx4 v[148:151],v11,s[24:27],0 offen offset:320 sc1\n"
"buffer_load_dwordx4 v[152:155],v11,s[24:27],0 offen offset:384 sc1\n"
"buffer_load_dwordx4 v[156:159],v11,s[24:27],0 offen offset:448 sc1\n"
"v_and_b32 v7,0x7ff,v7\n"
"v_lshl_add_u32 v8,v7,11,v57\n"
"v_and_b32 v9,15,v7\n"
"v_lshlrev_b32 v9,6,v9\n"
"v_xor_b32 v9,v9,v55\n"
"v_and_b32 v10,0x7f0,v7\n"
"v_lshl_add_u32 v10,v10,11,v9\n"
"s_mov_b32 s82,71303168\n"
"buffer_load_dwordx4 a[0:3],v10,s[24:27],s82 offen sc1\n"
"s_add_u32 s82,s82,4194304\n"
"buffer_load_dwordx4 a[4:7],v10,s[24:27],s82 offen sc1\n"
"s_add_u32 s82,s82,4194304\n"
"buffer_load_dwordx4 a[8:11],v10,s[24:27],s82 offen sc1\n"
"s_add_u32 s82,s82,4194304\n"
"buffer_load_dwordx4 a[12:15],v10,s[24:27],s82 offen sc1\n"
"buffer_load_dwordx4 a[16:19],v8,s[36:39],0 offen\n"
"v_bfe_u32 v9,v7,0,8\n"
"v_bfe_u32 v12,v9,1,3\n"
"v_and_b32 v13,1,v9\n"
"v_and_b32 v9,0xf0,v9\n"
"v_lshl_add_u32 v9,v13,3,v9\n"
"v_add_u32 v9,v12,v9\n"
"v_lshlrev_b32 v9,4,v9\n"
"buffer_load_dwordx4 v[12:15],v9,s[24:27],s83 offen sc1\n"
"s_mov_b32 s82,149979136\n"
"buffer_load_dwordx4 v[16:19],v9,s[24:27],s82 offen sc1\n"
"s_barrier\n"
"ds_read_b64_tr_b16 a[224:225],v192 offset:0\n"
"ds_read_b64_tr_b16 a[240:241],v192 offset:256\n"
"ds_read_b64_tr_b16 a[226:227],v193 offset:0\n"
"ds_read_b64_tr_b16 a[242:243],v193 offset:256\n"
"ds_read_b64_tr_b16 a[228:229],v194 offset:0\n"
"ds_read_b64_tr_b16 a[244:245],v194 offset:256\n"
"ds_read_b64_tr_b16 a[230:231],v195 offset:0\n"
"ds_read_b64_tr_b16 a[246:247],v195 offset:256\n"
"ds_read_b64_tr_b16 a[232:233],v196 offset:0\n"
"ds_read_b64_tr_b16 a[248:249],v196 offset:256\n"
"ds_read_b64_tr_b16 a[234:235],v197 offset:0\n"
"ds_read_b64_tr_b16 a[250:251],v197 offset:256\n"
"ds_read_b64_tr_b16 a[236:237],v198 offset:0\n"
"ds_read_b64_tr_b16 a[252:253],v198 offset:256\n"
"ds_read_b64_tr_b16 a[238:239],v199 offset:0\n"
"ds_read_b64_tr_b16 a[254:255],v199 offset:256\n"
"s_mov_b32 s84,0x40000\n"
"s_waitcnt vmcnt(7)\n"
".Lfk_wh:\n"
"v_mfma_f32_16x16x32_bf16 v[40:43],a[160:163],v[128:131],0\n"
"v_min3_u32 v36,v128,v129,v130\n"
"v_min3_u32 v37,v131,v132,v133\n"
"v_mfma_f32_16x16x32_bf16 v[40:43],a[164:167],v[132:135],v[40:43]\n"
"v_min3_u32 v36,v36,v134,v135\n"
"v_min3_u32 v37,v37,v136,v137\n"
"v_mfma_f32_16x16x32_bf16 v[40:43],a[168:171],v[136:139],v[40:43]\n"
"v_min3_u32 v36,v36,v138,v139\n"
"v_min3_u32 v37,v37,v140,v141\n"
"v_mfma_f32_16x16x32_bf16 v[40:43],a[172:175],v[140:143],v[40:43]\n"
"v_min3_u32 v36,v36,v142,v143\n"
"v_min3_u32 v37,v37,v144,v145\n"
"v_mfma_f32_16x16x32_bf16 v[40:43],a[176:179],v[144:147],v[40:43]\n"
"v_min3_u32 v36,v36,v146,v147\n"
"v_min3_u32 v37,v37,v148,v149\n"
"v_mfma_f32_16x16x32_bf16 v[40:43],a[180:183],v[148:151],v[40:43]\n"
"v_min3_u32 v36,v36,v150,v151\n"
"v_min3_u32 v37,v37,v152,v153\n"
"v_mfma_f32_16x16x32_bf16 v[40:43],a[184:187],v[152:155],v[40:43]\n"
"v_min3_u32 v36,v36,v154,v155\n"
"v_min3_u32 v37,v37,v156,v157\n"
"v_mfma_f32_16x16x32_bf16 v[40:43],a[188:191],v[156:159],v[40:43]\n"
"v_min3_u32 v36,v36,v158,v159\n"
"v_min_u32 v36,v36,v37\n"
"v_cmp_eq_u32 vcc,0,v36\n"
"v_mfma_f32_16x16x32_bf16 v[44:47],a[192:195],v[128:131],0\n"
"s_cbranch_vccz .Lfk_wh_x\n"
"s_sleep 1\n"
"s_mov_b64 exec,vcc\n"
"buffer_load_dwordx4 v[128:131],v11,s[24:27],0 offen offset:0 sc1\n"
"buffer_load_dwordx4 v[132:135],v11,s[24:27],0 offen offset:64 sc1\n"
"buffer_load_dwordx4 v[136:139],v11,s[24:27],0 offen offset:128 sc1\n"
"buffer_load_dwordx4 v[140:143],v11,s[24:27],0 offen offset:192 sc1\n"
"buffer_load_dwordx4 v[144:147],v11,s[24:27],0 offen offset:256 sc1\n"
"buffer_load_dwordx4 v[148:151],v11,s[24:27],0 offen offset:320 sc1\n"
"buffer_load_dwordx4 v[152:155],v11,s[24:27],0 offen offset:384 sc1\n"
"buffer_load_dwordx4 v[156:159],v11,s[24:27],0 offen offset:448 sc1\n"
"s_mov_b64 exec,-1\n"
"s_sub_u32 s84,s84,1\n"
"s_waitcnt vmcnt(0)\n"
"s_cbranch_scc0 .Lfk_wh\n"
".Lfk_wh_x:\n"
"v_mfma_f32_16x16x32_bf16 v[44:47],a[196:199],v[132:135],v[44:47]\n"
"v_mfma_f32_16x16x32_bf16 v[44:47],a[200:203],v[136:139],v[44:47]\n"
"v_mfma_f32_16x16x32_bf16 v[44:47],a[204:207],v[140:143],v[44:47]\n"
"ds_write_b128 v60,v[40:43] offset:1024\n"
"v_mfma_f32_16x16x32_bf16 v[44:47],a[208:211],v[144:147],v[44:47]\n"
"v_mfma_f32_16x16x32_bf16 v[44:47],a[212:215],v[148:151],v[44:47]\n"
"v_mfma_f32_16x16x32_bf16 v[44:47],a[216:219],v[152:155],v[44:47]\n"
"v_mfma_f32_16x16x32_bf16 v[44:47],a[220:223],v[156:159],v[44:47]\n"
"s_nop 7\n"
"ds_write_b128 v60,v[44:47] offset:2048\n"
"s_waitcnt lgkmcnt(0)\n"
"s_barrier\n"
"ds_read_b128 v[128:131],v61 offset:1024\n"
"ds_read_b128 v[144:147],v61 offset:2048\n"
"ds_read_b128 v[132:135],v61 offset:9216\n"
"ds_read_b128 v[148:151],v61 offset:10240\n"
"ds_read_b128 v[136:139],v61 offset:17408\n"
"ds_read_b128 v[152:155],v61 offset:18432\n"
"ds_read_b128 v[140:143],v61 offset:25600\n"
"ds_read_b128 v[156:159],v61 offset:26624\n"
"s_waitcnt lgkmcnt(0)\n"
"v_pk_add_f32 v[128:129],v[128:129],v[132:133]\n"
"v_pk_add_f32 v[136:137],v[136:137],v[140:141]\n"
"v_pk_add_f32 v[130:131],v[130:131],v[134:135]\n"
"v_pk_add_f32 v[138:139],v[138:139],v[142:143]\n"
"v_pk_add_f32 v[128:129],v[128:129],v[136:137]\n"
"v_pk_add_f32 v[130:131],v[130:131],v[138:139]\n"
"v_pk_add_f32 v[144:145],v[144:145],v[148:149]\n"
"v_pk_add_f32 v[152:153],v[152:153],v[156:157]\n"
"v_pk_add_f32 v[146:147],v[146:147],v[150:151]\n"
"v_pk_add_f32 v[154:155],v[154:155],v[158:159]\n"
"v_pk_add_f32 v[144:145],v[144:145],v[152:153]\n"
"v_pk_add_f32 v[146:147],v[146:147],v[154:155]\n"
"v_mul_f32 v132,0xbfb8aa3b,v128\n"
"v_mul_f32 v133,0xbfb8aa3b,v129\n"
"v_mul_f32 v134,0xbfb8aa3b,v130\n"
"v_mul_f32 v135,0xbfb8aa3b,v131\n"
"v_exp_f32 v132,v132\n"
"v_exp_f32 v133,v133\n"
"v_exp_f32 v134,v134\n"
"v_exp_f32 v135,v135\n"
"v_add_f32 v132,1.0,v132\n"
"v_add_f32 v133,1.0,v133\n"
"v_add_f32 v134,1.0,v134\n"
"v_add_f32 v135,1.0,v135\n"
"v_rcp_f32 v132,v132\n"
"v_rcp_f32 v133,v133\n"
"v_rcp_f32 v134,v134\n"
"v_rcp_f32 v135,v135\n"
"v_pk_mul_f32 v[128:129],v[128:129],v[132:133]\n"
"v_pk_mul_f32 v[130:131],v[130:131],v[134:135]\n"
"v_pk_mul_f32 v[128:129],v[128:129],v[144:145]\n"
"v_pk_mul_f32 v[130:131],v[130:131],v[146:147]\n"
"v_cvt_pk_bf16_f32 v132,v128,v129\n"
"v_cvt_pk_bf16_f32 v133,v130,v131\n"
"v_cndmask_b32 v136,0,v132,s[8:9]\n"
"v_cndmask_b32 v137,0,v133,s[8:9]\n"
"v_cndmask_b32 v138,0,v132,s[10:11]\n"
"v_cndmask_b32 v139,0,v133,s[10:11]\n"
"v_cndmask_b32 v141,0,v133,s[12:13]\n"
"v_cndmask_b32 v143,0,v133,s[14:15]\n"
"v_mfma_f32_16x16x32_bf16 v[40:43],a[224:227],v[136:139],0\n"
"v_cndmask_b32 v142,0,v132,s[14:15]\n"
"v_cndmask_b32 v140,0,v132,s[12:13]\n"
"v_mfma_f32_16x16x32_bf16 v[44:47],a[240:243],v[136:139],0\n"
"v_cndmask_b32 v145,0,v133,s[16:17]\n"
"v_cndmask_b32 v147,0,v133,s[18:19]\n"
"v_mfma_f32_16x16x32_bf16 v[40:43],a[228:231],v[140:143],v[40:43]\n"
"v_cndmask_b32 v146,0,v132,s[18:19]\n"
"v_cndmask_b32 v144,0,v132,s[16:17]\n"
"v_mfma_f32_16x16x32_bf16 v[44:47],a[244:247],v[140:143],v[44:47]\n"
"v_cndmask_b32 v149,0,v133,s[20:21]\n"
"v_cndmask_b32 v151,0,v133,s[22:23]\n"
"v_mfma_f32_16x16x32_bf16 v[40:43],a[232:235],v[144:147],v[40:43]\n"
"v_cndmask_b32 v150,0,v132,s[22:23]\n"
"v_cndmask_b32 v148,0,v132,s[20:21]\n"
"v_mfma_f32_16x16x32_bf16 v[44:47],a[248:251],v[144:147],v[44:47]\n"
"s_nop 0\n"
"v_mfma_f32_16x16x32_bf16 v[40:43],a[236:239],v[148:151],v[40:43]\n"
"v_mfma_f32_16x16x32_bf16 v[44:47],a[252:255],v[148:151],v[44:47]\n"
"s_or_b32 s82,s90,0x800\n"
"s_cmp_eq_u32 s63,s82\n"
"s_nop 4\n"
"v_or_b32 v40,1,v40\n"
"v_or_b32 v41,1,v41\n"
"v_or_b32 v42,1,v42\n"
"v_or_b32 v43,1,v43\n"
"v_or_b32 v44,1,v44\n"
"v_or_b32 v45,1,v45\n"
"v_or_b32 v46,1,v46\n"
"v_or_b32 v47,1,v47\n"
"buffer_store_dwordx4 v[40:43],v62,s[24:27],s69 offen offset:0 sc1\n"
"buffer_store_dwordx4 v[44:47],v62,s[24:27],s69 offen offset:512 sc1\n"
"s_cselect_b32 s68,2146435072,s68\n"
"s_or_b32 s82,s92,0x800\n"
"s_cmp_eq_u32 s63,s82\n"
"s_cselect_b32 s68,2146435072,s68\n"
"buffer_store_dwordx4 v[248:251],v112,s[48:51],s68 offen offset:0 sc1\n"
"buffer_store_dwordx4 v[252:255],v112,s[48:51],s68 offen offset:1024 sc1\n"
"s_cmp_eq_u32 s60,0\n"
"s_cselect_b32 s82,s75,2146435072\n"
"buffer_store_dwordx2 a[154:155],off,s[24:27],s82 sc1\n"
"s_mov_b32 s84,0x40000\n"
"s_waitcnt vmcnt(5)\n"
".Lfk_ws:\n"
"v_xor_b32 v20,s80,v17\n"
"v_xor_b32 v21,s80,v19\n"
"v_or_b32 v20,v20,v21\n"
"v_cmp_ne_u32 vcc,0,v20\n"
"s_nop 0\n"
"s_cbranch_vccz .Lfk_ws_x\n"
"s_mov_b64 exec,vcc\n"
"s_mov_b32 s82,149979136\n"
"buffer_load_dwordx4 v[12:15],v9,s[24:27],s83 offen sc1\n"
"buffer_load_dwordx4 v[16:19],v9,s[24:27],s82 offen sc1\n"
"s_mov_b64 exec,-1\n"
"s_sub_u32 s84,s84,1\n"
"s_waitcnt vmcnt(0)\n"
"s_cbranch_scc0 .Lfk_ws\n"
".Lfk_ws_x:\n"
"v_mfma_f32_16x16x32_bf16 v[24:27],v[66:69],a[0:3],0\n"
"v_mfma_f32_16x16x32_bf16 v[28:31],v[64:67],a[0:3],0\n"
"v_mfma_f32_16x16x32_bf16 v[24:27],v[66:69],a[4:7],v[24:27]\n"
"v_mfma_f32_16x16x32_bf16 v[28:31],v[64:67],a[4:7],v[28:31]\n"
"v_mfma_f32_16x16x32_bf16 v[24:27],v[66:69],a[8:11],v[24:27]\n"
"v_mfma_f32_16x16x32_bf16 v[28:31],v[64:67],a[8:11],v[28:31]\n"
"v_mfma_f32_16x16x32_bf16 v[24:27],v[66:69],a[12:15],v[24:27]\n"
"v_mfma_f32_16x16x32_bf16 v[28:31],v[64:67],a[12:15],v[28:31]\n"
"v_mfma_f32_16x16x32_bf16 v[24:27],v[66:69],a[16:19],v[24:27]\n"
"v_cmp_eq_u32 vcc,v12,v6\n"
"v_mfma_f32_16x16x32_bf16 v[28:31],v[64:67],a[16:19],v[28:31]\n"
"s_nop 0\n"
"v_cndmask_b32 v10,v18,v16,vcc\n"
"v_lshlrev_b32 v11,16,v10\n"
"v_rcp_f32 v11,v11\n"
"v_and_b32 v10,0xffff0000,v10\n"
"s_nop 0\n"
"v_cvt_pk_bf16_f32 v20,v24,v25\n"
"v_cvt_pk_bf16_f32 v21,v26,v27\n"
"v_lshlrev_b32 v24,16,v20\n"
"v_and_b32 v25,0xffff0000,v20\n"
"v_cvt_pk_bf16_f32 v23,v30,v31\n"
"v_cvt_pk_bf16_f32 v22,v28,v29\n"
"v_lshlrev_b32 v26,16,v21\n"
"v_and_b32 v27,0xffff0000,v21\n"
"v_lshlrev_b32 v28,16,v22\n"
"v_and_b32 v29,0xffff0000,v22\n"
"v_lshlrev_b32 v30,16,v23\n"
"v_and_b32 v31,0xffff0000,v23\n"
"v_pk_add_f32 v[32:33],v[24:25],v[10:11] op_sel_hi:[1,0] neg_lo:[0,1] neg_hi:[0,1]\n"
"v_pk_add_f32 v[34:35],v[26:27],v[10:11] op_sel_hi:[1,0] neg_lo:[0,1] neg_hi:[0,1]\n"
"v_pk_add_f32 v[36:37],v[28:29],v[10:11] op_sel_hi:[1,0] neg_lo:[0,1] neg_hi:[0,1]\n"
"v_pk_add_f32 v[38:39],v[30:31],v[10:11] op_sel_hi:[1,0] neg_lo:[0,1] neg_hi:[0,1]\n"
"v_cvt_pk_bf16_f32 v20,v32,v33\n"
"v_cvt_pk_bf16_f32 v21,v34,v35\n"
"v_cvt_pk_bf16_f32 v22,v36,v37\n"
"v_cvt_pk_bf16_f32 v23,v38,v39\n"
"v_lshlrev_b32 v32,16,v20\n"
"v_and_b32 v33,0xffff0000,v20\n"
"v_lshlrev_b32 v34,16,v21\n"
"v_and_b32 v35,0xffff0000,v21\n"
"v_lshlrev_b32 v36,16,v22\n"
"v_and_b32 v37,0xffff0000,v22\n"
"v_lshlrev_b32 v38,16,v23\n"
"v_and_b32 v39,0xffff0000,v23\n"
"v_pk_mul_f32 v[32:33],v[32:33],v[10:11] op_sel:[0,1]\n"
"v_pk_mul_f32 v[34:35],v[34:35],v[10:11] op_sel:[0,1]\n"
"v_pk_mul_f32 v[36:37],v[36:37],v[10:11] op_sel:[0,1]\n"
"v_pk_mul_f32 v[38:39],v[38:39],v[10:11] op_sel:[0,1]\n"
"v_cvt_pk_bf16_f32 v20,v32,v33\n"
"v_cvt_pk_bf16_f32 v21,v34,v35\n"
"v_cvt_pk_bf16_f32 v22,v36,v37\n"
"v_cvt_pk_bf16_f32 v23,v38,v39\n"
"ds_write_b128 v58,v[20:23]\n"
"ds_write_b128 v60,v[24:27] offset:3072\n"
"ds_write_b128 v60,v[28:31] offset:4096\n"
"s_bfe_u32 s82,s73,0x20005\n"
"s_lshl_b32 s82,s82,13\n"
"s_and_b32 s83,s73,31\n"
"s_lshr_b32 s91,s73,7\n"
"s_lshl_b32 s89,s91,9\n"
"s_lshl4_add_u32 s89,s83,s89\n"
"s_add_u32 s89,s89,s82\n"
"s_add_u32 s89,s89,1024\n"
"s_lshl1_add_u32 s83,s83,s91\n"
"s_lshl4_add_u32 s82,s83,s82\n"
"s_add_u32 s82,s82,4096\n"
"v_bfe_u32 v0,v112,4,3\n"
"v_lshl_add_u32 v1,v0,1,s89\n"
"v_and_b32 v3,3,v0\n"
"v_lshl_add_u32 v3,v3,2,s82\n"
"v_lshrrev_b32 v4,2,v0\n"
"v_lshl_add_u32 v3,v4,10,v3\n"
"s_cmp_eq_u32 s91,0\n"
"s_cselect_b32 s82,s90,s92\n"
"s_not_b32 s83,s82\n"
"s_and_b32 s89,s83,0x7ff\n"
"s_lshl_b32 s89,s89,11\n"
"s_and_b32 s91,s73,127\n"
"s_lshl4_add_u32 s89,s91,s89\n"
"s_ashr_i32 s83,s83,31\n"
"s_or_b32 s83,s83,0x80000000\n"
"s_xor_b32 s82,s82,s83\n"
"s_and_b32 s82,s82,0xffff0000\n"
"v_mov_b32 v5,0xbfb8aa3b\n"
"v_mul_f32 v5,s82,v5\n"
"v_exp_f32 v5,v5\n"
"s_nop 0\n"
"v_add_f32 v5,1.0,v5\n"
"v_rcp_f32 v5,v5\n"
"s_nop 0\n"
"v_cvt_pk_bf16_f32 v5,v5,v5\n"
"v_lshlrev_b32 v5,16,v5\n"
"s_lshl_b32 s82,s52,7\n"
"s_add_u32 s82,s82,64\n"
"v_lshrrev_b32 v6,3,v112\n"
"v_and_b32 v6,0x60,v6\n"
"v_and_b32 v7,16,v112\n"
"v_add3_u32 v6,v6,v7,s82\n"
"v_lshrrev_b32 v7,2,v112\n"
"v_add_u32 v7,64,v7\n"
"v_lshlrev_b32 v8,1,v0\n"
"s_sleep 1\n"
"s_mov_b32 s84,0x40000\n"
"buffer_load_dwordx4 v[16:19],v63,s[24:27],s70 offen sc1\n"
"buffer_load_dwordx4 v[20:23],v63,s[24:27],s71 offen sc1\n"
"s_waitcnt vmcnt(0)\n"
".Lfk_wy:\n"
"v_min3_u32 v24,v16,v17,v18\n"
"v_min3_u32 v25,v19,v20,v21\n"
"v_min3_u32 v24,v24,v22,v23\n"
"v_min_u32 v24,v24,v25\n"
"v_cmp_eq_u32 vcc,0,v24\n"
"s_nop 0\n"
"s_cbranch_vccz .Lfk_wy_x\n"
"s_sleep 1\n"
"s_mov_b64 exec,vcc\n"
"buffer_load_dwordx4 v[16:19],v63,s[24:27],s70 offen sc1\n"
"buffer_load_dwordx4 v[20:23],v63,s[24:27],s71 offen sc1\n"
"s_mov_b64 exec,-1\n"
"s_sub_u32 s84,s84,1\n"
"s_waitcnt vmcnt(0)\n"
"s_cbranch_scc0 .Lfk_wy\n"
".Lfk_wy_x:\n"
"s_mov_b32 vcc_lo,-1\n"
"s_mov_b32 vcc_hi,0x00ffffff\n"
"v_add_f32 v16,v16,v20\n"
"v_add_f32 v17,v17,v21\n"
"v_add_f32 v18,v18,v22\n"
"v_add_f32 v19,v19,v23\n"
"v_cndmask_b32 v16,0,v16,vcc\n"
"v_cndmask_b32 v17,0,v17,vcc\n"
"v_cndmask_b32 v18,0,v18,vcc\n"
"v_cndmask_b32 v19,0,v19,vcc\n"
"v_add_f32_dpp v16,v16,v16 quad_perm:[2,3,0,1] row_mask:0xf bank_mask:0xf\n"
"v_add_f32_dpp v17,v17,v17 quad_perm:[2,3,0,1] row_mask:0xf bank_mask:0xf\n"
"v_add_f32_dpp v18,v18,v18 quad_perm:[2,3,0,1] row_mask:0xf bank_mask:0xf\n"
"v_add_f32_dpp v19,v19,v19 quad_perm:[2,3,0,1] row_mask:0xf bank_mask:0xf\n"
"v_add_f32_dpp v16,v16,v16 row_ror:4 row_mask:0xf bank_mask:0xf\n"
"v_add_f32_dpp v17,v17,v17 row_ror:4 row_mask:0xf bank_mask:0xf\n"
"v_add_f32_dpp v18,v18,v18 row_ror:4 row_mask:0xf bank_mask:0xf\n"
"v_add_f32_dpp v19,v19,v19 row_ror:4 row_mask:0xf bank_mask:0xf\n"
"v_add_f32_dpp v16,v16,v16 row_ror:8 row_mask:0xf bank_mask:0xf\n"
"v_add_f32_dpp v17,v17,v17 row_ror:8 row_mask:0xf bank_mask:0xf\n"
"v_add_f32_dpp v18,v18,v18 row_ror:8 row_mask:0xf bank_mask:0xf\n"
"v_add_f32_dpp v19,v19,v19 row_ror:8 row_mask:0xf bank_mask:0xf\n"
"s_mov_b32 exec_lo,0x30003\n"
"s_mov_b32 exec_hi,0x30003\n"
"ds_write_b128 v6,v[16:19]\n"
"s_mov_b64 exec,-1\n"
"s_waitcnt lgkmcnt(0)\n"
"s_barrier\n"
"s_cmp_lg_u32 s52,0\n"
"s_cbranch_scc1 .Lfk_t2\n"
"ds_read2_b32 v[16:17],v7 offset0:0 offset1:64\n"
"ds_read_u16 v2,v1\n"
"ds_read_b32 v3,v3\n"
"s_waitcnt lgkmcnt(0)\n"
"v_lshlrev_b32 v2,16,v2\n"
"v_add_f32 v16,v16,v17\n"
"s_nop 1\n"
"v_add_f32_dpp v16,v16,v16 row_ror:8 row_mask:0xf bank_mask:0xf\n"
"v_mov_b32 v17,v16\n"
"s_nop 1\n"
"v_permlane16_swap_b32 v17,v16\n"
"v_add_f32 v16,v16,v17\n"
"v_mov_b32 v17,v16\n"
"s_nop 1\n"
"v_permlane32_swap_b32 v17,v16\n"
"v_add_f32 v16,v16,v17\n"
"v_cvt_pk_bf16_f32 v16,v16,v16\n"
"v_lshlrev_b32 v16,16,v16\n"
"v_mul_f32 v16,v16,v5\n"
"v_cvt_pk_bf16_f32 v16,v16,v16\n"
"v_lshlrev_b32 v16,16,v16\n"
"v_add_f32 v16,v16,v2\n"
"v_cvt_pk_bf16_f32 v16,v16,v16\n"
"v_lshlrev_b32 v16,16,v16\n"
"v_add_f32 v16,v16,v3\n"
"s_nop 1\n"
"v_mov_b32_dpp v17,v16 quad_perm:[1,0,3,2] row_mask:0xf bank_mask:0xf\n"
"v_cvt_pk_bf16_f32 v16,v16,v17\n"
"s_mov_b64 exec,0x55\n"
"buffer_store_dword v16,v8,s[48:51],s89 offen sc1\n"
"s_mov_b64 exec,-1\n"
".Lfk_t2:\n"
"s_branch .Lfk_end\n"
".Lfk_g0:\n"
"s_waitcnt vmcnt(11) lgkmcnt(0)\n"
"s_barrier\n"
"v_mfma_f32_16x16x32_bf16 v[32:35],a[96:99],a[0:3],v[96:99]\n"
"s_add_u32 m0,s55,4096\n"
"v_exp_f32 v0,v0\n"
"v_mfma_f32_16x16x32_bf16 v[32:35],a[100:103],a[4:7],v[32:35]\n"
"s_add_u32 s54,s54,s22\n"
"v_exp_f32 v1,v1\n"
"v_mfma_f32_16x16x32_bf16 v[32:35],a[104:107],a[8:11],v[32:35]\n"
"buffer_load_dwordx4 v115,s[32:35],s54 offen sc1 lds\n"
"v_exp_f32 v2,v2\n"
"v_cvt_pk_bf16_f32 v64,v0,v1\n"
"v_mfma_f32_16x16x32_bf16 v[32:35],a[108:111],a[12:15],v[32:35]\n"
"v_exp_f32 v3,v3\n"
"v_mfma_f32_16x16x32_bf16 v[36:39],a[124:127],a[12:15],v[96:99]\n"
"v_exp_f32 v4,v4\n"
"v_cvt_pk_bf16_f32 v65,v2,v3\n"
"v_mfma_f32_16x16x32_bf16 v[36:39],a[120:123],a[8:11],v[36:39]\n"
"v_exp_f32 v5,v5\n"
"v_mfma_f32_16x16x32_bf16 v[36:39],a[116:119],a[4:7],v[36:39]\n"
"v_exp_f32 v6,v6\n"
"v_cvt_pk_bf16_f32 v66,v4,v5\n"
"v_mfma_f32_16x16x32_bf16 v[36:39],a[112:115],a[0:3],v[36:39]\n"
"v_exp_f32 v7,v7\n"
"v_mfma_f32_16x16x32_bf16 v[44:47],a[112:115],a[16:19],v[100:103]\n"
"v_exp_f32 v8,v8\n"
"v_cvt_pk_bf16_f32 v67,v6,v7\n"
"v_exp_f32 v9,v9\n"
"v_mfma_f32_16x16x32_bf16 v[44:47],a[116:119],a[20:23],v[44:47]\n"
"ds_read_b128 a[64:67],v113 offset:49152\n"
"ds_read_b128 a[68:71],v113 offset:50176\n"
"v_exp_f32 v10,v10\n"
"v_mfma_f32_16x16x32_bf16 v[44:47],a[120:123],a[24:27],v[44:47]\n"
"ds_read_b128 a[72:75],v113 offset:51200\n"
"ds_read_b128 a[76:79],v113 offset:52224\n"
"v_exp_f32 v11,v11\n"
"v_mfma_f32_16x16x32_bf16 v[44:47],a[124:127],a[28:31],v[44:47]\n"
"ds_read_b128 a[80:83],v113 offset:53248\n"
"ds_read_b128 a[84:87],v113 offset:54272\n"
"v_exp_f32 v12,v12\n"
"v_mfma_f32_16x16x32_bf16 v[40:43],a[108:111],a[28:31],v[100:103]\n"
"ds_read_b128 a[88:91],v113 offset:55296\n"
"ds_read_b128 a[92:95],v113 offset:56320\n"
"v_mfma_f32_16x16x32_bf16 v[40:43],a[104:107],a[24:27],v[40:43]\n"
"v_exp_f32 v13,v13\n"
"v_cvt_pk_bf16_f32 v68,v8,v9\n"
"v_cvt_pk_bf16_f32 v69,v10,v11\n"
"v_mfma_f32_16x16x32_bf16 v[40:43],a[100:103],a[20:23],v[40:43]\n"
"v_exp_f32 v14,v14\n"
"v_cvt_pk_bf16_f32 v70,v12,v13\n"
"v_mfma_f32_16x16x32_bf16 v[40:43],a[96:99],a[16:19],v[40:43]\n"
"v_exp_f32 v15,v15\n"
"v_mfma_f32_16x16x32_bf16 v[48:51],a[96:99],a[32:35],v[104:107]\n"
"v_exp_f32 v16,v16\n"
"v_cvt_pk_bf16_f32 v71,v14,v15\n"
"v_mfma_f32_16x16x32_bf16 v[48:51],a[100:103],a[36:39],v[48:51]\n"
"v_exp_f32 v17,v17\n"
"v_mfma_f32_16x16x32_bf16 v[48:51],a[104:107],a[40:43],v[48:51]\n"
"v_exp_f32 v18,v18\n"
"v_cvt_pk_bf16_f32 v72,v16,v17\n"
"v_mfma_f32_16x16x32_bf16 v[48:51],a[108:111],a[44:47],v[48:51]\n"
"v_exp_f32 v19,v19\n"
"v_mfma_f32_16x16x32_bf16 v[52:55],a[124:127],a[44:47],v[104:107]\n"
"v_exp_f32 v20,v20\n"
"v_cvt_pk_bf16_f32 v73,v18,v19\n"
"v_mfma_f32_16x16x32_bf16 v[52:55],a[120:123],a[40:43],v[52:55]\n"
"v_exp_f32 v21,v21\n"
"v_mfma_f32_16x16x32_bf16 v[52:55],a[116:119],a[36:39],v[52:55]\n"
"v_exp_f32 v22,v22\n"
"v_cvt_pk_bf16_f32 v74,v20,v21\n"
"v_mfma_f32_16x16x32_bf16 v[52:55],a[112:115],a[32:35],v[52:55]\n"
"v_exp_f32 v23,v23\n"
"v_mfma_f32_16x16x32_bf16 v[60:63],a[112:115],a[48:51],v[108:111]\n"
"v_exp_f32 v24,v24\n"
"v_cvt_pk_bf16_f32 v75,v22,v23\n"
"v_mfma_f32_16x16x32_bf16 v[60:63],a[116:119],a[52:55],v[60:63]\n"
"v_exp_f32 v25,v25\n"
"v_mfma_f32_16x16x32_bf16 v[60:63],a[120:123],a[56:59],v[60:63]\n"
"v_exp_f32 v26,v26\n"
"v_exp_f32 v27,v27\n"
"v_mfma_f32_16x16x32_bf16 v[60:63],a[124:127],a[60:63],v[60:63]\n"
"buffer_store_dwordx4 a[228:231],v112,s[24:27],s56 offen offset:1024 sc1\n"
"v_mfma_f32_16x16x32_bf16 v[56:59],a[108:111],a[60:63],v[108:111]\n"
"v_exp_f32 v28,v28\n"
"v_cvt_pk_bf16_f32 v77,v26,v27\n"
"v_mfma_f32_16x16x32_bf16 v[56:59],a[104:107],a[56:59],v[56:59]\n"
"v_exp_f32 v29,v29\n"
"v_mfma_f32_16x16x32_bf16 v[56:59],a[100:103],a[52:55],v[56:59]\n"
"v_exp_f32 v30,v30\n"
"v_cvt_pk_bf16_f32 v78,v28,v29\n"
"v_mfma_f32_16x16x32_bf16 v[56:59],a[96:99],a[48:51],v[56:59]\n"
"s_waitcnt vmcnt(9) lgkmcnt(0)\n"
"v_exp_f32 v31,v31\n"
"v_cvt_pk_bf16_f32 v76,v24,v25\n"
"v_cvt_pk_bf16_f32 v79,v30,v31\n"
"s_barrier\n"
"v_mfma_f32_16x16x32_bf16 v[0:3],a[64:67],a[0:3],v[96:99]\n"
"s_add_u32 m0,s55,8192\n"
"v_exp_f32 v122,v32\n"
"v_mfma_f32_16x16x32_bf16 v[0:3],a[68:71],a[4:7],v[0:3]\n"
"s_add_u32 s54,s54,s22\n"
"v_exp_f32 v123,v33\n"
"v_mfma_f32_16x16x32_bf16 v[0:3],a[72:75],a[8:11],v[0:3]\n"
"ds_read_b128 a[96:99],v114 offset:40960\n"
"ds_read_b128 a[100:103],v114 offset:41984\n"
"v_mfma_f32_16x16x32_bf16 v[0:3],a[76:79],a[12:15],v[0:3]\n"
"v_exp_f32 v119,v34\n"
"v_mfma_f32_16x16x32_bf16 v[4:7],a[92:95],a[12:15],v[96:99]\n"
"ds_read_b128 a[104:107],v114 offset:43008\n"
"v_cvt_pk_bf16_f32 v80,v122,v123\n"
"v_mfma_f32_16x16x32_bf16 v[4:7],a[88:91],a[8:11],v[4:7]\n"
"v_exp_f32 v120,v35\n"
"v_mfma_f32_16x16x32_bf16 v[4:7],a[84:87],a[4:7],v[4:7]\n"
"ds_read_b128 a[108:111],v114 offset:44032\n"
"ds_read_b128 a[112:115],v114 offset:45056\n"
"v_mfma_f32_16x16x32_bf16 v[4:7],a[80:83],a[0:3],v[4:7]\n"
"v_exp_f32 v122,v36\n"
"v_mfma_f32_16x16x32_bf16 v[12:15],a[80:83],a[16:19],v[100:103]\n"
"ds_read_b128 a[116:119],v114 offset:46080\n"
"v_cvt_pk_bf16_f32 v81,v119,v120\n"
"v_mfma_f32_16x16x32_bf16 v[12:15],a[84:87],a[20:23],v[12:15]\n"
"v_exp_f32 v123,v37\n"
"v_mfma_f32_16x16x32_bf16 v[12:15],a[88:91],a[24:27],v[12:15]\n"
"ds_read_b128 a[120:123],v114 offset:47104\n"
"ds_read_b128 a[124:127],v114 offset:48128\n"
"v_mfma_f32_16x16x32_bf16 v[12:15],a[92:95],a[28:31],v[12:15]\n"
"v_exp_f32 v119,v38\n"
"v_cvt_pk_bf16_f32 v82,v122,v123\n"
"v_mfma_f32_16x16x32_bf16 v[8:11],a[76:79],a[28:31],v[100:103]\n"
"buffer_store_dwordx4 a[168:171],v112,s[24:27],s3 offen offset:2048 sc1\n"
"v_mfma_f32_16x16x32_bf16 v[8:11],a[72:75],a[24:27],v[8:11]\n"
"v_exp_f32 v120,v39\n"
"v_mfma_f32_16x16x32_bf16 v[8:11],a[68:71],a[20:23],v[8:11]\n"
"buffer_load_dwordx4 v115,s[32:35],s54 offen sc1 lds\n"
"v_mfma_f32_16x16x32_bf16 v[8:11],a[64:67],a[16:19],v[8:11]\n"
"v_exp_f32 v122,v40\n"
"v_mfma_f32_16x16x32_bf16 v[16:19],a[64:67],a[32:35],v[104:107]\n"
"v_cvt_pk_bf16_f32 v83,v119,v120\n"
"v_or3_b32 v118,v80,v81,v82\n"
"v_mfma_f32_16x16x32_bf16 v[16:19],a[68:71],a[36:39],v[16:19]\n"
"v_exp_f32 v123,v41\n"
"v_mfma_f32_16x16x32_bf16 v[16:19],a[72:75],a[40:43],v[16:19]\n"
"v_cvt_pk_bf16_f32 v84,v122,v123\n"
"v_or3_b32 v118,v118,v83,v84\n"
"v_mfma_f32_16x16x32_bf16 v[16:19],a[76:79],a[44:47],v[16:19]\n"
"v_exp_f32 v119,v42\n"
"v_mfma_f32_16x16x32_bf16 v[20:23],a[92:95],a[44:47],v[104:107]\n"
"v_mfma_f32_16x16x32_bf16 v[20:23],a[88:91],a[40:43],v[20:23]\n"
"v_exp_f32 v120,v43\n"
"v_mfma_f32_16x16x32_bf16 v[20:23],a[84:87],a[36:39],v[20:23]\n"
"v_cvt_pk_bf16_f32 v85,v119,v120\n"
"v_mfma_f32_16x16x32_bf16 v[20:23],a[80:83],a[32:35],v[20:23]\n"
"v_exp_f32 v122,v44\n"
"v_mfma_f32_16x16x32_bf16 v[28:31],a[80:83],a[48:51],v[108:111]\n"
"buffer_store_dwordx4 a[172:175],v112,s[24:27],s3 offen offset:3072 sc1\n"
"v_mfma_f32_16x16x32_bf16 v[28:31],a[84:87],a[52:55],v[28:31]\n"
"v_exp_f32 v123,v45\n"
"v_mfma_f32_16x16x32_bf16 v[28:31],a[88:91],a[56:59],v[28:31]\n"
"s_sub_u32 s3,s3,16384\n"
"v_mfma_f32_16x16x32_bf16 v[28:31],a[92:95],a[60:63],v[28:31]\n"
"v_exp_f32 v119,v46\n"
"v_mfma_f32_16x16x32_bf16 v[24:27],a[76:79],a[60:63],v[108:111]\n"
"v_cvt_pk_bf16_f32 v86,v122,v123\n"
"v_or3_b32 v118,v118,v85,v86\n"
"v_mfma_f32_16x16x32_bf16 v[24:27],a[72:75],a[56:59],v[24:27]\n"
"v_exp_f32 v120,v47\n"
"v_mfma_f32_16x16x32_bf16 v[24:27],a[68:71],a[52:55],v[24:27]\n"
"v_cvt_pk_bf16_f32 v87,v119,v120\n"
"v_mfma_f32_16x16x32_bf16 v[24:27],a[64:67],a[48:51],v[24:27]\n"
"s_waitcnt lgkmcnt(0)\n"
"v_exp_f32 v122,v48\n"
"v_mfma_f32_16x16x32_bf16 v[128:131],a[96:99],v[64:67],0\n"
"v_exp_f32 v123,v49\n"
"v_mfma_f32_16x16x32_bf16 v[160:163],a[96:99],v[68:71],0\n"
"ds_read_b128 a[64:67],v113 offset:32768\n"
"ds_read_b128 a[68:71],v113 offset:33792\n"
"v_mfma_f32_16x16x32_bf16 v[192:195],a[96:99],v[72:75],0\n"
"v_exp_f32 v119,v50\n"
"v_mfma_f32_16x16x32_bf16 v[224:227],a[96:99],v[76:79],0\n"
"ds_read_b128 a[72:75],v113 offset:34816\n"
"v_cvt_pk_bf16_f32 v88,v122,v123\n"
"v_mfma_f32_16x16x32_bf16 a[140:143],a[144:147],v[76:79],0\n"
"v_exp_f32 v120,v51\n"
"v_mfma_f32_16x16x32_bf16 v[228:231],a[100:103],v[76:79],0\n"
"ds_read_b128 a[76:79],v113 offset:35840\n"
"ds_read_b128 a[80:83],v113 offset:36864\n"
"v_mfma_f32_16x16x32_bf16 v[132:135],a[100:103],v[64:67],0\n"
"v_exp_f32 v122,v52\n"
"v_mfma_f32_16x16x32_bf16 v[164:167],a[100:103],v[68:71],0\n"
"ds_read_b128 a[84:87],v113 offset:37888\n"
"v_cvt_pk_bf16_f32 v89,v119,v120\n"
"v_mfma_f32_16x16x32_bf16 v[196:199],a[100:103],v[72:75],0\n"
"v_exp_f32 v123,v53\n"
"v_mfma_f32_16x16x32_bf16 a[136:139],a[144:147],v[72:75],0\n"
"ds_read_b128 a[88:91],v113 offset:38912\n"
"ds_read_b128 a[92:95],v113 offset:39936\n"
"v_mfma_f32_16x16x32_bf16 v[200:203],a[104:107],v[72:75],0\n"
"v_exp_f32 v119,v54\n"
"v_cvt_pk_bf16_f32 v90,v122,v123\n"
"v_mfma_f32_16x16x32_bf16 v[232:235],a[104:107],v[76:79],0\n"
"buffer_store_dwordx4 a[176:179],v112,s[24:27],s3 offen offset:0 sc1\n"
"v_mfma_f32_16x16x32_bf16 v[136:139],a[104:107],v[64:67],0\n"
"v_exp_f32 v120,v55\n"
"v_mfma_f32_16x16x32_bf16 v[168:171],a[104:107],v[68:71],0\n"
"v_cvt_pk_bf16_f32 v91,v119,v120\n"
"v_or3_b32 v118,v118,v87,v88\n"
"v_mfma_f32_16x16x32_bf16 a[132:135],a[144:147],v[68:71],0\n"
"v_exp_f32 v122,v56\n"
"v_mfma_f32_16x16x32_bf16 v[172:175],a[108:111],v[68:71],0\n"
"v_or3_b32 v118,v118,v89,v90\n"
"v_mfma_f32_16x16x32_bf16 v[204:207],a[108:111],v[72:75],0\n"
"v_exp_f32 v123,v57\n"
"v_mfma_f32_16x16x32_bf16 v[236:239],a[108:111],v[76:79],0\n"
"v_cvt_pk_bf16_f32 v92,v122,v123\n"
"v_or3_b32 v118,v118,v91,v92\n"
"v_mfma_f32_16x16x32_bf16 v[140:143],a[108:111],v[64:67],0\n"
"v_exp_f32 v119,v58\n"
"v_mfma_f32_16x16x32_bf16 a[128:131],a[144:147],v[64:67],0\n"
"v_mfma_f32_16x16x32_bf16 v[144:147],a[112:115],v[64:67],0\n"
"v_exp_f32 v120,v59\n"
"v_mfma_f32_16x16x32_bf16 v[176:179],a[112:115],v[68:71],0\n"
"v_cvt_pk_bf16_f32 v93,v119,v120\n"
"v_mfma_f32_16x16x32_bf16 v[208:211],a[112:115],v[72:75],0\n"
"v_exp_f32 v122,v60\n"
"v_mfma_f32_16x16x32_bf16 v[240:243],a[112:115],v[76:79],0\n"
"v_mfma_f32_16x16x32_bf16 v[244:247],a[116:119],v[76:79],0\n"
"v_exp_f32 v123,v61\n"
"v_mfma_f32_16x16x32_bf16 v[148:151],a[116:119],v[64:67],0\n"
"v_cvt_pk_bf16_f32 v94,v122,v123\n"
"v_or3_b32 v118,v118,v93,v94\n"
"v_mfma_f32_16x16x32_bf16 v[180:183],a[116:119],v[68:71],0\n"
"v_exp_f32 v119,v62\n"
"v_mfma_f32_16x16x32_bf16 v[212:215],a[116:119],v[72:75],0\n"
"buffer_store_dwordx4 a[232:235],v112,s[24:27],s56 offen offset:2048 sc1\n"
"v_mfma_f32_16x16x32_bf16 v[216:219],a[120:123],v[72:75],0\n"
"v_exp_f32 v120,v63\n"
"v_mfma_f32_16x16x32_bf16 v[248:251],a[120:123],v[76:79],0\n"
"v_cvt_pk_bf16_f32 v95,v119,v120\n"
"v_or_b32 v118,v118,v95\n"
"v_mfma_f32_16x16x32_bf16 v[152:155],a[120:123],v[64:67],0\n"
"v_and_b32 v118,0x40004000,v118\n"
"v_cmp_ne_u32 vcc,0,v118\n"
"v_mfma_f32_16x16x32_bf16 v[184:187],a[120:123],v[68:71],0\n"
"v_mfma_f32_16x16x32_bf16 v[188:191],a[124:127],v[68:71],0\n"
"v_mfma_f32_16x16x32_bf16 v[220:223],a[124:127],v[72:75],0\n"
"v_mfma_f32_16x16x32_bf16 v[252:255],a[124:127],v[76:79],0\n"
"v_mfma_f32_16x16x32_bf16 v[156:159],a[124:127],v[64:67],0\n"
"s_cbranch_vccnz .Lfk_cs_k1\n"
".Lfk_cr_k1:\n"
"s_waitcnt lgkmcnt(0)\n"
"s_barrier\n"
"v_mfma_f32_16x16x32_bf16 v[32:35],a[64:67],a[0:3],v[96:99]\n"
"s_add_u32 m0,s55,12288\n"
"v_exp_f32 v122,v0\n"
"v_mfma_f32_16x16x32_bf16 v[32:35],a[68:71],a[4:7],v[32:35]\n"
"s_add_u32 s54,s54,s22\n"
"v_exp_f32 v123,v1\n"
"v_mfma_f32_16x16x32_bf16 v[32:35],a[72:75],a[8:11],v[32:35]\n"
"ds_read_b128 a[96:99],v114 offset:8192\n"
"ds_read_b128 a[100:103],v114 offset:9216\n"
"v_mfma_f32_16x16x32_bf16 v[32:35],a[76:79],a[12:15],v[32:35]\n"
"v_exp_f32 v119,v2\n"
"v_mfma_f32_16x16x32_bf16 v[36:39],a[92:95],a[12:15],v[96:99]\n"
"ds_read_b128 a[104:107],v114 offset:10240\n"
"v_cvt_pk_bf16_f32 v64,v122,v123\n"
"v_mfma_f32_16x16x32_bf16 v[36:39],a[88:91],a[8:11],v[36:39]\n"
"v_exp_f32 v120,v3\n"
"v_mfma_f32_16x16x32_bf16 v[36:39],a[84:87],a[4:7],v[36:39]\n"
"ds_read_b128 a[108:111],v114 offset:11264\n"
"ds_read_b128 a[112:115],v114 offset:12288\n"
"v_mfma_f32_16x16x32_bf16 v[36:39],a[80:83],a[0:3],v[36:39]\n"
"v_exp_f32 v122,v4\n"
"v_mfma_f32_16x16x32_bf16 v[44:47],a[80:83],a[16:19],v[100:103]\n"
"ds_read_b128 a[116:119],v114 offset:13312\n"
"v_cvt_pk_bf16_f32 v65,v119,v120\n"
"v_mfma_f32_16x16x32_bf16 v[44:47],a[84:87],a[20:23],v[44:47]\n"
"v_exp_f32 v123,v5\n"
"v_mfma_f32_16x16x32_bf16 v[44:47],a[88:91],a[24:27],v[44:47]\n"
"ds_read_b128 a[120:123],v114 offset:14336\n"
"ds_read_b128 a[124:127],v114 offset:15360\n"
"v_mfma_f32_16x16x32_bf16 v[44:47],a[92:95],a[28:31],v[44:47]\n"
"v_exp_f32 v119,v6\n"
"v_cvt_pk_bf16_f32 v66,v122,v123\n"
"v_mfma_f32_16x16x32_bf16 v[40:43],a[76:79],a[28:31],v[100:103]\n"
"buffer_store_dwordx4 a[180:183],v112,s[24:27],s3 offen offset:1024 sc1\n"
"v_mfma_f32_16x16x32_bf16 v[40:43],a[72:75],a[24:27],v[40:43]\n"
"v_exp_f32 v120,v7\n"
"v_mfma_f32_16x16x32_bf16 v[40:43],a[68:71],a[20:23],v[40:43]\n"
"buffer_load_dwordx4 v115,s[32:35],s54 offen sc1 lds\n"
"v_mfma_f32_16x16x32_bf16 v[40:43],a[64:67],a[16:19],v[40:43]\n"
"v_exp_f32 v122,v8\n"
"v_mfma_f32_16x16x32_bf16 v[48:51],a[64:67],a[32:35],v[104:107]\n"
"v_cvt_pk_bf16_f32 v67,v119,v120\n"
"v_or3_b32 v118,v64,v65,v66\n"
"v_mfma_f32_16x16x32_bf16 v[48:51],a[68:71],a[36:39],v[48:51]\n"
"v_exp_f32 v123,v9\n"
"v_mfma_f32_16x16x32_bf16 v[48:51],a[72:75],a[40:43],v[48:51]\n"
"v_cvt_pk_bf16_f32 v68,v122,v123\n"
"v_or3_b32 v118,v118,v67,v68\n"
"v_mfma_f32_16x16x32_bf16 v[48:51],a[76:79],a[44:47],v[48:51]\n"
"v_exp_f32 v119,v10\n"
"v_mfma_f32_16x16x32_bf16 v[52:55],a[92:95],a[44:47],v[104:107]\n"
"v_mfma_f32_16x16x32_bf16 v[52:55],a[88:91],a[40:43],v[52:55]\n"
"v_exp_f32 v120,v11\n"
"v_mfma_f32_16x16x32_bf16 v[52:55],a[84:87],a[36:39],v[52:55]\n"
"v_cvt_pk_bf16_f32 v69,v119,v120\n"
"v_mfma_f32_16x16x32_bf16 v[52:55],a[80:83],a[32:35],v[52:55]\n"
"v_exp_f32 v122,v12\n"
"v_mfma_f32_16x16x32_bf16 v[60:63],a[80:83],a[48:51],v[108:111]\n"
"buffer_store_dwordx4 a[184:187],v112,s[24:27],s3 offen offset:2048 sc1\n"
"v_mfma_f32_16x16x32_bf16 v[60:63],a[84:87],a[52:55],v[60:63]\n"
"v_exp_f32 v123,v13\n"
"v_mfma_f32_16x16x32_bf16 v[60:63],a[88:91],a[56:59],v[60:63]\n"
"v_cvt_pk_bf16_f32 v70,v122,v123\n"
"v_mfma_f32_16x16x32_bf16 v[60:63],a[92:95],a[60:63],v[60:63]\n"
"v_exp_f32 v119,v14\n"
"v_mfma_f32_16x16x32_bf16 v[56:59],a[76:79],a[60:63],v[108:111]\n"
"v_or3_b32 v118,v118,v69,v70\n"
"v_mfma_f32_16x16x32_bf16 v[56:59],a[72:75],a[56:59],v[56:59]\n"
"v_exp_f32 v120,v15\n"
"v_mfma_f32_16x16x32_bf16 v[56:59],a[68:71],a[52:55],v[56:59]\n"
"v_cvt_pk_bf16_f32 v71,v119,v120\n"
"v_mfma_f32_16x16x32_bf16 v[56:59],a[64:67],a[48:51],v[56:59]\n"
"s_waitcnt lgkmcnt(0)\n"
"v_exp_f32 v122,v16\n"
"v_mfma_f32_16x16x32_bf16 v[128:131],a[96:99],v[80:83],v[128:131]\n"
"v_exp_f32 v123,v17\n"
"v_mfma_f32_16x16x32_bf16 v[160:163],a[96:99],v[84:87],v[160:163]\n"
"ds_read_b128 a[64:67],v113 offset:0\n"
"ds_read_b128 a[68:71],v113 offset:1024\n"
"v_mfma_f32_16x16x32_bf16 v[192:195],a[96:99],v[88:91],v[192:195]\n"
"v_exp_f32 v119,v18\n"
"v_mfma_f32_16x16x32_bf16 v[224:227],a[96:99],v[92:95],v[224:227]\n"
"ds_read_b128 a[72:75],v113 offset:2048\n"
"v_cvt_pk_bf16_f32 v72,v122,v123\n"
"v_mfma_f32_16x16x32_bf16 a[140:143],a[144:147],v[92:95],a[140:143]\n"
"v_exp_f32 v120,v19\n"
"v_mfma_f32_16x16x32_bf16 v[228:231],a[100:103],v[92:95],v[228:231]\n"
"ds_read_b128 a[76:79],v113 offset:3072\n"
"ds_read_b128 a[80:83],v113 offset:4096\n"
"v_mfma_f32_16x16x32_bf16 v[132:135],a[100:103],v[80:83],v[132:135]\n"
"v_exp_f32 v122,v20\n"
"v_mfma_f32_16x16x32_bf16 v[164:167],a[100:103],v[84:87],v[164:167]\n"
"ds_read_b128 a[84:87],v113 offset:5120\n"
"v_cvt_pk_bf16_f32 v73,v119,v120\n"
"v_mfma_f32_16x16x32_bf16 v[196:199],a[100:103],v[88:91],v[196:199]\n"
"v_exp_f32 v123,v21\n"
"v_mfma_f32_16x16x32_bf16 a[136:139],a[144:147],v[88:91],a[136:139]\n"
"ds_read_b128 a[88:91],v113 offset:6144\n"
"ds_read_b128 a[92:95],v113 offset:7168\n"
"v_mfma_f32_16x16x32_bf16 v[200:203],a[104:107],v[88:91],v[200:203]\n"
"v_exp_f32 v119,v22\n"
"v_mfma_f32_16x16x32_bf16 v[232:235],a[104:107],v[92:95],v[232:235]\n"
"buffer_store_dwordx4 a[188:191],v112,s[24:27],s3 offen offset:3072 sc1\n"
"v_mfma_f32_16x16x32_bf16 v[136:139],a[104:107],v[80:83],v[136:139]\n"
"v_exp_f32 v120,v23\n"
"v_mfma_f32_16x16x32_bf16 v[168:171],a[104:107],v[84:87],v[168:171]\n"
"s_or_b32 s3,s3,0x3c000\n"
"v_cvt_pk_bf16_f32 v74,v122,v123\n"
"v_cvt_pk_bf16_f32 v75,v119,v120\n"
"v_mfma_f32_16x16x32_bf16 a[132:135],a[144:147],v[84:87],a[132:135]\n"
"v_exp_f32 v122,v24\n"
"v_mfma_f32_16x16x32_bf16 v[172:175],a[108:111],v[84:87],v[172:175]\n"
"v_or3_b32 v118,v118,v71,v72\n"
"v_or3_b32 v118,v118,v73,v74\n"
"v_mfma_f32_16x16x32_bf16 v[204:207],a[108:111],v[88:91],v[204:207]\n"
"v_exp_f32 v123,v25\n"
"v_mfma_f32_16x16x32_bf16 v[236:239],a[108:111],v[92:95],v[236:239]\n"
"v_cvt_pk_bf16_f32 v76,v122,v123\n"
"v_or3_b32 v118,v118,v75,v76\n"
"v_mfma_f32_16x16x32_bf16 v[140:143],a[108:111],v[80:83],v[140:143]\n"
"v_exp_f32 v119,v26\n"
"v_mfma_f32_16x16x32_bf16 a[128:131],a[144:147],v[80:83],a[128:131]\n"
"v_mfma_f32_16x16x32_bf16 v[144:147],a[112:115],v[80:83],v[144:147]\n"
"v_exp_f32 v120,v27\n"
"v_mfma_f32_16x16x32_bf16 v[176:179],a[112:115],v[84:87],v[176:179]\n"
"v_cvt_pk_bf16_f32 v77,v119,v120\n"
"v_mfma_f32_16x16x32_bf16 v[208:211],a[112:115],v[88:91],v[208:211]\n"
"v_exp_f32 v122,v28\n"
"v_mfma_f32_16x16x32_bf16 v[240:243],a[112:115],v[92:95],v[240:243]\n"
"v_mfma_f32_16x16x32_bf16 v[244:247],a[116:119],v[92:95],v[244:247]\n"
"v_exp_f32 v123,v29\n"
"v_mfma_f32_16x16x32_bf16 v[148:151],a[116:119],v[80:83],v[148:151]\n"
"v_cvt_pk_bf16_f32 v78,v122,v123\n"
"v_or3_b32 v118,v118,v77,v78\n"
"v_mfma_f32_16x16x32_bf16 v[180:183],a[116:119],v[84:87],v[180:183]\n"
"v_exp_f32 v119,v30\n"
"v_mfma_f32_16x16x32_bf16 v[212:215],a[116:119],v[88:91],v[212:215]\n"
"buffer_store_dwordx4 a[236:239],v112,s[24:27],s56 offen offset:3072 sc1\n"
"v_mfma_f32_16x16x32_bf16 v[216:219],a[120:123],v[88:91],v[216:219]\n"
"v_exp_f32 v120,v31\n"
"v_mfma_f32_16x16x32_bf16 v[248:251],a[120:123],v[92:95],v[248:251]\n"
"v_cvt_pk_bf16_f32 v79,v119,v120\n"
"v_or_b32 v118,v118,v79\n"
"v_mfma_f32_16x16x32_bf16 v[152:155],a[120:123],v[80:83],v[152:155]\n"
"v_and_b32 v118,0x40004000,v118\n"
"v_cmp_ne_u32 vcc,0,v118\n"
"v_mfma_f32_16x16x32_bf16 v[184:187],a[120:123],v[84:87],v[184:187]\n"
"v_mfma_f32_16x16x32_bf16 v[188:191],a[124:127],v[84:87],v[188:191]\n"
"v_mfma_f32_16x16x32_bf16 v[220:223],a[124:127],v[88:91],v[220:223]\n"
"v_mfma_f32_16x16x32_bf16 v[252:255],a[124:127],v[92:95],v[252:255]\n"
"v_mfma_f32_16x16x32_bf16 v[156:159],a[124:127],v[80:83],v[156:159]\n"
"s_cbranch_vccnz .Lfk_cs_k2\n"
".Lfk_cr_k2:\n"
"s_waitcnt vmcnt(13) lgkmcnt(0)\n"
"s_barrier\n"
"s_branch .Lfk_g1\n"
".Lfk_winA:\n"
"v_max3_f32 v118,v32,v33,v34\n"
"v_max3_f32 v119,v35,v36,v37\n"
"v_max3_f32 v119,v119,v38,v39\n"
"v_max3_f32 v118,v118,v40,v41\n"
"v_max3_f32 v119,v119,v42,v43\n"
"v_max3_f32 v118,v118,v44,v45\n"
"v_max3_f32 v119,v119,v46,v47\n"
"v_max3_f32 v118,v118,v48,v49\n"
"v_max3_f32 v119,v119,v50,v51\n"
"v_max3_f32 v118,v118,v52,v53\n"
"v_max3_f32 v119,v119,v54,v55\n"
"v_max3_f32 v118,v118,v56,v57\n"
"v_max3_f32 v119,v119,v58,v59\n"
"v_max3_f32 v118,v118,v60,v61\n"
"v_max3_f32 v119,v119,v62,v63\n"
"v_max_f32 v118,v118,v119\n"
"v_cmp_lt_f32 vcc,s93,v118\n"
"s_nop 0\n"
"s_cbranch_vccnz .Lfk_cs_x2\n"
".Lfk_cr_x2:\n"
"s_mov_b32 s64,s61\n"
"s_cmp_eq_u32 s52,3\n"
"s_cselect_b32 s67,172032,0\n"
"v_mfma_f32_16x16x32_bf16 v[8:11],a[64:67],a[16:19],v[100:103]\n"
"s_swappc_b64 s[94:95],s[20:21]\n"
"ds_read_b128 a[96:99],v114 offset:24576\n"
"ds_read_b128 a[100:103],v114 offset:25600\n"
"v_mfma_f32_16x16x32_bf16 v[8:11],a[68:71],a[20:23],v[8:11]\n"
"ds_read_b128 a[104:107],v114 offset:26624\n"
"ds_read_b128 a[108:111],v114 offset:27648\n"
"v_mfma_f32_16x16x32_bf16 v[8:11],a[72:75],a[24:27],v[8:11]\n"
"ds_read_b128 a[112:115],v114 offset:28672\n"
"ds_read_b128 a[116:119],v114 offset:29696\n"
"v_mfma_f32_16x16x32_bf16 v[8:11],a[76:79],a[28:31],v[8:11]\n"
"ds_read_b128 a[120:123],v114 offset:30720\n"
"ds_read_b128 a[124:127],v114 offset:31744\n"
"v_mfma_f32_16x16x32_bf16 v[12:15],a[92:95],a[28:31],v[100:103]\n"
"v_add_f32 v0,0x80000000,v126\n"
"v_min_f32 v0,v0,v96\n"
"v_mfma_f32_16x16x32_bf16 v[12:15],a[88:91],a[24:27],v[12:15]\n"
"v_add_f32 v1,0xf149f2ca,v126\n"
"v_min_f32 v1,v1,v97\n"
"v_mfma_f32_16x16x32_bf16 v[12:15],a[84:87],a[20:23],v[12:15]\n"
"v_add_f32 v2,0xf1c9f2ca,v126\n"
"v_min_f32 v2,v2,v98\n"
"v_mfma_f32_16x16x32_bf16 v[12:15],a[80:83],a[16:19],v[12:15]\n"
"v_add_f32 v3,0xf2177617,v126\n"
"v_min_f32 v3,v3,v99\n"
"v_mfma_f32_16x16x32_bf16 v[20:23],a[80:83],a[32:35],v[104:107]\n"
"v_add_f32 v4,0xf349f2ca,v126\n"
"v_min_f32 v4,v4,v96\n"
"v_mfma_f32_16x16x32_bf16 v[20:23],a[84:87],a[36:39],v[20:23]\n"
"v_add_f32 v5,0xf35691f6,v126\n"
"v_min_f32 v5,v5,v97\n"
"v_mfma_f32_16x16x32_bf16 v[20:23],a[88:91],a[40:43],v[20:23]\n"
"v_add_f32 v6,0xf3633123,v126\n"
"v_min_f32 v6,v6,v98\n"
"v_mfma_f32_16x16x32_bf16 v[20:23],a[92:95],a[44:47],v[20:23]\n"
"v_add_f32 v7,0xf36fd050,v126\n"
"v_min_f32 v7,v7,v99\n"
"v_mfma_f32_16x16x32_bf16 v[16:19],a[76:79],a[44:47],v[104:107]\n"
"s_add_u32 s101,s101,4\n"
"v_exp_f32 v32,v32\n"
"v_mfma_f32_16x16x32_bf16 v[16:19],a[72:75],a[40:43],v[16:19]\n"
"s_add_u32 m0,s67,0\n"
"v_exp_f32 v33,v33\n"
"v_mfma_f32_16x16x32_bf16 v[16:19],a[68:71],a[36:39],v[16:19]\n"
"v_exp_f32 v34,v34\n"
"v_mfma_f32_16x16x32_bf16 v[16:19],a[64:67],a[32:35],v[16:19]\n"
"v_exp_f32 v35,v35\n"
"v_mfma_f32_16x16x32_bf16 v[24:27],a[64:67],a[48:51],v[108:111]\n"
"v_exp_f32 v36,v36\n"
"v_mfma_f32_16x16x32_bf16 v[24:27],a[68:71],a[52:55],v[24:27]\n"
"v_exp_f32 v37,v37\n"
"v_mfma_f32_16x16x32_bf16 v[24:27],a[72:75],a[56:59],v[24:27]\n"
"v_exp_f32 v38,v38\n"
"v_mfma_f32_16x16x32_bf16 v[24:27],a[76:79],a[60:63],v[24:27]\n"
"v_exp_f32 v39,v39\n"
"v_mfma_f32_16x16x32_bf16 v[28:31],a[92:95],a[60:63],v[108:111]\n"
"v_exp_f32 v40,v40\n"
"v_mfma_f32_16x16x32_bf16 v[28:31],a[88:91],a[56:59],v[28:31]\n"
"v_exp_f32 v41,v41\n"
"v_mfma_f32_16x16x32_bf16 v[28:31],a[84:87],a[52:55],v[28:31]\n"
"v_exp_f32 v42,v42\n"
"v_mfma_f32_16x16x32_bf16 v[28:31],a[80:83],a[48:51],v[28:31]\n"
"v_exp_f32 v43,v43\n"
"v_mfma_f32_16x16x32_bf16 v[4:7],a[80:83],a[0:3],v[4:7]\n"
"v_exp_f32 v44,v44\n"
"v_mfma_f32_16x16x32_bf16 v[4:7],a[84:87],a[4:7],v[4:7]\n"
"v_exp_f32 v45,v45\n"
"v_mfma_f32_16x16x32_bf16 v[4:7],a[88:91],a[8:11],v[4:7]\n"
"buffer_load_dwordx4 v112,s[24:27],s64 offen offset:0 sc1 lds\n"
"v_mfma_f32_16x16x32_bf16 v[4:7],a[92:95],a[12:15],v[4:7]\n"
"v_exp_f32 v46,v46\n"
"v_mfma_f32_16x16x32_bf16 v[0:3],a[76:79],a[12:15],v[0:3]\n"
"buffer_load_dwordx4 v112,s[24:27],s64 offen offset:1024 sc1 lds\n"
"v_mfma_f32_16x16x32_bf16 v[0:3],a[72:75],a[8:11],v[0:3]\n"
"v_exp_f32 v47,v47\n"
"v_mfma_f32_16x16x32_bf16 v[0:3],a[68:71],a[4:7],v[0:3]\n"
"buffer_load_dwordx4 v112,s[24:27],s64 offen offset:2048 sc1 lds\n"
"v_mfma_f32_16x16x32_bf16 v[0:3],a[64:67],a[0:3],v[0:3]\n"
"s_waitcnt lgkmcnt(0)\n"
"v_exp_f32 v48,v48\n"
"v_mfma_f32_16x16x32_bf16 v[128:131],a[96:99],v[64:67],v[128:131]\n"
"v_exp_f32 v49,v49\n"
"v_mfma_f32_16x16x32_bf16 v[160:163],a[96:99],v[68:71],v[160:163]\n"
"ds_read_b128 a[64:67],v113 offset:16384\n"
"ds_read_b128 a[68:71],v113 offset:17408\n"
"v_mfma_f32_16x16x32_bf16 v[192:195],a[96:99],v[72:75],v[192:195]\n"
"v_exp_f32 v50,v50\n"
"v_mfma_f32_16x16x32_bf16 v[224:227],a[96:99],v[76:79],v[224:227]\n"
"ds_read_b128 a[72:75],v113 offset:18432\n"
"ds_read_b128 a[76:79],v113 offset:19456\n"
"v_mfma_f32_16x16x32_bf16 a[140:143],a[144:147],v[76:79],a[140:143]\n"
"v_exp_f32 v51,v51\n"
"v_mfma_f32_16x16x32_bf16 v[228:231],a[100:103],v[76:79],v[228:231]\n"
"ds_read_b128 a[80:83],v113 offset:20480\n"
"ds_read_b128 a[84:87],v113 offset:21504\n"
"v_mfma_f32_16x16x32_bf16 v[132:135],a[100:103],v[64:67],v[132:135]\n"
"v_exp_f32 v52,v52\n"
"v_mfma_f32_16x16x32_bf16 v[164:167],a[100:103],v[68:71],v[164:167]\n"
"ds_read_b128 a[88:91],v113 offset:22528\n"
"ds_read_b128 a[92:95],v113 offset:23552\n"
"v_mfma_f32_16x16x32_bf16 v[196:199],a[100:103],v[72:75],v[196:199]\n"
"v_exp_f32 v53,v53\n"
"v_mfma_f32_16x16x32_bf16 a[136:139],a[144:147],v[72:75],a[136:139]\n"
"s_waitcnt vmcnt(14)\n"
"ds_add_u32 v124,a152\n"
"v_mfma_f32_16x16x32_bf16 v[200:203],a[104:107],v[72:75],v[200:203]\n"
"v_exp_f32 v54,v54\n"
"v_mfma_f32_16x16x32_bf16 v[232:235],a[104:107],v[76:79],v[232:235]\n"
"buffer_load_dwordx4 v112,s[24:27],s64 offen offset:3072 sc1 lds\n"
"v_mfma_f32_16x16x32_bf16 v[136:139],a[104:107],v[64:67],v[136:139]\n"
"s_add_u32 m0,s67,4096\n"
"v_exp_f32 v55,v55\n"
"v_mfma_f32_16x16x32_bf16 v[168:171],a[104:107],v[68:71],v[168:171]\n"
"buffer_load_dwordx4 v117,s[24:27],s64 offen offset:0 sc1 lds\n"
"v_mfma_f32_16x16x32_bf16 a[132:135],a[144:147],v[68:71],a[132:135]\n"
"v_exp_f32 v56,v56\n"
"v_mfma_f32_16x16x32_bf16 v[172:175],a[108:111],v[68:71],v[172:175]\n"
"buffer_load_dwordx4 v117,s[24:27],s64 offen offset:1024 sc1 lds\n"
"v_mfma_f32_16x16x32_bf16 v[204:207],a[108:111],v[72:75],v[204:207]\n"
"v_exp_f32 v57,v57\n"
"v_mfma_f32_16x16x32_bf16 v[236:239],a[108:111],v[76:79],v[236:239]\n"
"buffer_load_dwordx4 v117,s[24:27],s64 offen offset:2048 sc1 lds\n"
"v_mfma_f32_16x16x32_bf16 v[140:143],a[108:111],v[64:67],v[140:143]\n"
"v_exp_f32 v58,v58\n"
"v_mfma_f32_16x16x32_bf16 a[128:131],a[144:147],v[64:67],a[128:131]\n"
"v_cvt_pk_bf16_f32 v80,v32,v33\n"
"v_cvt_pk_bf16_f32 v81,v34,v35\n"
"v_mfma_f32_16x16x32_bf16 v[144:147],a[112:115],v[64:67],v[144:147]\n"
"v_exp_f32 v59,v59\n"
"v_mfma_f32_16x16x32_bf16 v[176:179],a[112:115],v[68:71],v[176:179]\n"
"v_cvt_pk_bf16_f32 v82,v36,v37\n"
"v_cvt_pk_bf16_f32 v83,v38,v39\n"
"v_mfma_f32_16x16x32_bf16 v[208:211],a[112:115],v[72:75],v[208:211]\n"
"v_exp_f32 v60,v60\n"
"v_mfma_f32_16x16x32_bf16 v[240:243],a[112:115],v[76:79],v[240:243]\n"
"buffer_load_dwordx4 v117,s[24:27],s64 offen offset:3072 sc1 lds\n"
"v_mfma_f32_16x16x32_bf16 v[244:247],a[116:119],v[76:79],v[244:247]\n"
"s_add_u32 s64,s64,16384\n"
"v_exp_f32 v61,v61\n"
"v_mfma_f32_16x16x32_bf16 v[148:151],a[116:119],v[64:67],v[148:151]\n"
"s_andn2_b32 m0,s55,16384\n"
"s_add_u32 s54,s54,s22\n"
"v_cvt_pk_bf16_f32 v85,v42,v43\n"
"v_mfma_f32_16x16x32_bf16 v[180:183],a[116:119],v[68:71],v[180:183]\n"
"v_exp_f32 v62,v62\n"
"v_mfma_f32_16x16x32_bf16 v[212:215],a[116:119],v[72:75],v[212:215]\n"
"ds_read_b32 v125,v124\n"
"v_cvt_pk_bf16_f32 v84,v40,v41\n"
"v_mfma_f32_16x16x32_bf16 v[216:219],a[120:123],v[72:75],v[216:219]\n"
"v_exp_f32 v63,v63\n"
"v_mfma_f32_16x16x32_bf16 v[248:251],a[120:123],v[76:79],v[248:251]\n"
"buffer_load_dwordx4 v115,s[32:35],s54 offen sc1 lds\n"
"v_mfma_f32_16x16x32_bf16 v[152:155],a[120:123],v[64:67],v[152:155]\n"
"buffer_store_dwordx4 a[240:243],v112,s[24:27],s56 offen offset:0 sc1\n"
"v_mfma_f32_16x16x32_bf16 v[184:187],a[120:123],v[68:71],v[184:187]\n"
"v_cvt_pk_bf16_f32 v86,v44,v45\n"
"v_cvt_pk_bf16_f32 v87,v46,v47\n"
"v_mfma_f32_16x16x32_bf16 v[188:191],a[124:127],v[68:71],v[188:191]\n"
"s_waitcnt lgkmcnt(0)\n"
"v_cvt_pk_bf16_f32 v88,v48,v49\n"
"v_cvt_pk_bf16_f32 v89,v50,v51\n"
"v_mfma_f32_16x16x32_bf16 v[220:223],a[124:127],v[72:75],v[220:223]\n"
"v_cvt_pk_bf16_f32 v90,v52,v53\n"
"v_cvt_pk_bf16_f32 v91,v54,v55\n"
"v_mfma_f32_16x16x32_bf16 v[252:255],a[124:127],v[76:79],v[252:255]\n"
"v_cvt_pk_bf16_f32 v92,v56,v57\n"
"v_cvt_pk_bf16_f32 v93,v58,v59\n"
"v_mfma_f32_16x16x32_bf16 v[156:159],a[124:127],v[64:67],v[156:159]\n"
"v_cvt_pk_bf16_f32 v94,v60,v61\n"
"v_cvt_pk_bf16_f32 v95,v62,v63\n"
"v_max3_f32 v118,v8,v9,v10\n"
"v_max3_f32 v118,v118,v11,v12\n"
"v_max3_f32 v118,v118,v13,v14\n"
"v_max3_f32 v118,v118,v15,v20\n"
"v_max3_f32 v118,v118,v21,v22\n"
"v_max3_f32 v118,v118,v23,v16\n"
"v_max3_f32 v118,v118,v17,v18\n"
"v_max3_f32 v118,v118,v19,v24\n"
"v_max3_f32 v118,v118,v25,v26\n"
"v_max3_f32 v118,v118,v27,v28\n"
"v_max3_f32 v118,v118,v29,v30\n"
"v_max3_f32 v118,v118,v31,v4\n"
"v_max3_f32 v118,v118,v5,v6\n"
"v_max3_f32 v118,v118,v7,v0\n"
"v_max3_f32 v118,v118,v1,v2\n"
"v_max_f32 v118,v118,v3\n"
"v_cmp_lt_f32 vcc,s93,v118\n"
"s_nop 0\n"
"s_cbranch_vccnz .Lfk_cs_u3\n"
".Lfk_cr_u3:\n"
"v_cmp_gt_u32 vcc,s101,v125\n"
"s_nop 0\n"
"s_cbranch_vccnz .Lfk_cs_mu3\n"
".Lfk_cr_mu3:\n"
"v_mfma_f32_16x16x32_bf16 v[48:51],a[64:67],a[32:35],v[104:107]\n"
"ds_read_b128 a[96:99],v114 offset:57344\n"
"ds_read_b128 a[100:103],v114 offset:58368\n"
"v_mfma_f32_16x16x32_bf16 v[48:51],a[68:71],a[36:39],v[48:51]\n"
"ds_read_b128 a[104:107],v114 offset:59392\n"
"ds_read_b128 a[108:111],v114 offset:60416\n"
"v_mfma_f32_16x16x32_bf16 v[48:51],a[72:75],a[40:43],v[48:51]\n"
"ds_read_b128 a[112:115],v114 offset:61440\n"
"ds_read_b128 a[116:119],v114 offset:62464\n"
"v_mfma_f32_16x16x32_bf16 v[48:51],a[76:79],a[44:47],v[48:51]\n"
"ds_read_b128 a[120:123],v114 offset:63488\n"
"ds_read_b128 a[124:127],v114 offset:64512\n"
"v_mfma_f32_16x16x32_bf16 v[52:55],a[92:95],a[44:47],v[104:107]\n"
"ds_read_b128 a[0:3],v116 offset:40960\n"
"ds_read_b128 a[4:7],v116 offset:41984\n"
"v_mfma_f32_16x16x32_bf16 v[52:55],a[88:91],a[40:43],v[52:55]\n"
"ds_read_b128 a[8:11],v116 offset:43008\n"
"ds_read_b128 a[12:15],v116 offset:44032\n"
"v_mfma_f32_16x16x32_bf16 v[52:55],a[84:87],a[36:39],v[52:55]\n"
"v_add_f32 v40,0x80000000,v126\n"
"v_min_f32 v40,v40,v100\n"
"v_mfma_f32_16x16x32_bf16 v[52:55],a[80:83],a[32:35],v[52:55]\n"
"v_add_f32 v41,0xf149f2ca,v126\n"
"v_min_f32 v41,v41,v101\n"
"v_mfma_f32_16x16x32_bf16 v[60:63],a[80:83],a[48:51],v[108:111]\n"
"v_add_f32 v42,0xf1c9f2ca,v126\n"
"v_min_f32 v42,v42,v102\n"
"v_mfma_f32_16x16x32_bf16 v[60:63],a[84:87],a[52:55],v[60:63]\n"
"v_add_f32 v43,0xf2177617,v126\n"
"v_min_f32 v43,v43,v103\n"
"v_mfma_f32_16x16x32_bf16 v[60:63],a[88:91],a[56:59],v[60:63]\n"
"v_add_f32 v44,0xf349f2ca,v126\n"
"v_min_f32 v44,v44,v100\n"
"v_mfma_f32_16x16x32_bf16 v[60:63],a[92:95],a[60:63],v[60:63]\n"
"v_add_f32 v45,0xf35691f6,v126\n"
"v_min_f32 v45,v45,v101\n"
"v_mfma_f32_16x16x32_bf16 v[56:59],a[76:79],a[60:63],v[108:111]\n"
"v_add_f32 v46,0xf3633123,v126\n"
"v_min_f32 v46,v46,v102\n"
"v_mfma_f32_16x16x32_bf16 v[56:59],a[72:75],a[56:59],v[56:59]\n"
"v_add_f32 v47,0xf36fd050,v126\n"
"v_min_f32 v47,v47,v103\n"
"v_mfma_f32_16x16x32_bf16 v[56:59],a[68:71],a[52:55],v[56:59]\n"
"s_add_u32 s101,s101,4\n"
"v_exp_f32 v0,v0\n"
"v_mfma_f32_16x16x32_bf16 v[56:59],a[64:67],a[48:51],v[56:59]\n"
"s_add_u32 m0,s55,4096\n"
"v_exp_f32 v1,v1\n"
"v_mfma_f32_16x16x32_bf16 v[40:43],a[64:67],a[16:19],v[40:43]\n"
"s_add_u32 s54,s54,s22\n"
"v_exp_f32 v2,v2\n"
"v_mfma_f32_16x16x32_bf16 v[40:43],a[68:71],a[20:23],v[40:43]\n"
"v_exp_f32 v3,v3\n"
"v_mfma_f32_16x16x32_bf16 v[40:43],a[72:75],a[24:27],v[40:43]\n"
"v_exp_f32 v4,v4\n"
"v_mfma_f32_16x16x32_bf16 v[40:43],a[76:79],a[28:31],v[40:43]\n"
"v_exp_f32 v5,v5\n"
"v_mfma_f32_16x16x32_bf16 v[44:47],a[92:95],a[28:31],v[44:47]\n"
"v_exp_f32 v6,v6\n"
"v_mfma_f32_16x16x32_bf16 v[44:47],a[88:91],a[24:27],v[44:47]\n"
"v_exp_f32 v7,v7\n"
"v_mfma_f32_16x16x32_bf16 v[44:47],a[84:87],a[20:23],v[44:47]\n"
"v_exp_f32 v8,v8\n"
"v_mfma_f32_16x16x32_bf16 v[44:47],a[80:83],a[16:19],v[44:47]\n"
"s_waitcnt lgkmcnt(4)\n"
"v_exp_f32 v9,v9\n"
"v_mfma_f32_16x16x32_bf16 v[128:131],a[96:99],v[80:83],v[128:131]\n"
"v_exp_f32 v10,v10\n"
"v_mfma_f32_16x16x32_bf16 v[160:163],a[96:99],v[84:87],v[160:163]\n"
"v_exp_f32 v11,v11\n"
"v_mfma_f32_16x16x32_bf16 v[192:195],a[96:99],v[88:91],v[192:195]\n"
"v_exp_f32 v12,v12\n"
"v_mfma_f32_16x16x32_bf16 v[224:227],a[96:99],v[92:95],v[224:227]\n"
"v_exp_f32 v13,v13\n"
"v_mfma_f32_16x16x32_bf16 a[140:143],a[144:147],v[92:95],a[140:143]\n"
"v_exp_f32 v14,v14\n"
"v_mfma_f32_16x16x32_bf16 v[228:231],a[100:103],v[92:95],v[228:231]\n"
"v_exp_f32 v15,v15\n"
"v_mfma_f32_16x16x32_bf16 v[132:135],a[100:103],v[80:83],v[132:135]\n"
"v_exp_f32 v16,v16\n"
"v_mfma_f32_16x16x32_bf16 v[164:167],a[100:103],v[84:87],v[164:167]\n"
"v_exp_f32 v17,v17\n"
"v_mfma_f32_16x16x32_bf16 v[196:199],a[100:103],v[88:91],v[196:199]\n"
"v_exp_f32 v18,v18\n"
"v_mfma_f32_16x16x32_bf16 a[136:139],a[144:147],v[88:91],a[136:139]\n"
"v_exp_f32 v19,v19\n"
"v_mfma_f32_16x16x32_bf16 v[200:203],a[104:107],v[88:91],v[200:203]\n"
"v_exp_f32 v20,v20\n"
"v_mfma_f32_16x16x32_bf16 v[232:235],a[104:107],v[92:95],v[232:235]\n"
"v_exp_f32 v21,v21\n"
"v_mfma_f32_16x16x32_bf16 v[136:139],a[104:107],v[80:83],v[136:139]\n"
"ds_read_b128 a[64:67],v113 offset:49152\n"
"ds_read_b128 a[68:71],v113 offset:50176\n"
"v_mfma_f32_16x16x32_bf16 v[168:171],a[104:107],v[84:87],v[168:171]\n"
"v_exp_f32 v22,v22\n"
"v_mfma_f32_16x16x32_bf16 a[132:135],a[144:147],v[84:87],a[132:135]\n"
"v_exp_f32 v23,v23\n"
"v_mfma_f32_16x16x32_bf16 v[172:175],a[108:111],v[84:87],v[172:175]\n"
"ds_read_b128 a[72:75],v113 offset:51200\n"
"ds_read_b128 a[76:79],v113 offset:52224\n"
"v_mfma_f32_16x16x32_bf16 v[204:207],a[108:111],v[88:91],v[204:207]\n"
"v_exp_f32 v24,v24\n"
"v_mfma_f32_16x16x32_bf16 v[236:239],a[108:111],v[92:95],v[236:239]\n"
"ds_read_b128 a[80:83],v113 offset:53248\n"
"ds_read_b128 a[84:87],v113 offset:54272\n"
"v_mfma_f32_16x16x32_bf16 v[140:143],a[108:111],v[80:83],v[140:143]\n"
"v_exp_f32 v25,v25\n"
"v_mfma_f32_16x16x32_bf16 a[128:131],a[144:147],v[80:83],a[128:131]\n"
"v_exp_f32 v26,v26\n"
"v_mfma_f32_16x16x32_bf16 v[144:147],a[112:115],v[80:83],v[144:147]\n"
"ds_read_b128 a[88:91],v113 offset:55296\n"
"ds_read_b128 a[92:95],v113 offset:56320\n"
"v_mfma_f32_16x16x32_bf16 v[176:179],a[112:115],v[84:87],v[176:179]\n"
"v_exp_f32 v27,v27\n"
"v_mfma_f32_16x16x32_bf16 v[208:211],a[112:115],v[88:91],v[208:211]\n"
"s_waitcnt vmcnt(11)\n"
"ds_add_u32 v124,a152\n"
"v_mfma_f32_16x16x32_bf16 v[240:243],a[112:115],v[92:95],v[240:243]\n"
"v_exp_f32 v28,v28\n"
"v_mfma_f32_16x16x32_bf16 v[244:247],a[116:119],v[92:95],v[244:247]\n"
"v_exp_f32 v29,v29\n"
"v_mfma_f32_16x16x32_bf16 v[148:151],a[116:119],v[80:83],v[148:151]\n"
"buffer_load_dwordx4 v115,s[32:35],s54 offen sc1 lds\n"
"v_mfma_f32_16x16x32_bf16 v[180:183],a[116:119],v[84:87],v[180:183]\n"
"s_add_u32 m0,s67,16384\n"
"v_exp_f32 v30,v30\n"
"v_mfma_f32_16x16x32_bf16 v[212:215],a[116:119],v[88:91],v[212:215]\n"
"ds_read_b32 v125,v124\n"
"v_cvt_pk_bf16_f32 v64,v0,v1\n"
"v_mfma_f32_16x16x32_bf16 v[216:219],a[120:123],v[88:91],v[216:219]\n"
"v_exp_f32 v31,v31\n"
"v_mfma_f32_16x16x32_bf16 v[248:251],a[120:123],v[92:95],v[248:251]\n"
"buffer_load_dwordx4 v112,s[24:27],s64 offen offset:0 sc1 lds\n"
"v_mfma_f32_16x16x32_bf16 v[152:155],a[120:123],v[80:83],v[152:155]\n"
"buffer_load_dwordx4 v112,s[24:27],s64 offen offset:1024 sc1 lds\n"
"v_mfma_f32_16x16x32_bf16 v[184:187],a[120:123],v[84:87],v[184:187]\n"
"buffer_load_dwordx4 v112,s[24:27],s64 offen offset:2048 sc1 lds\n"
"v_mfma_f32_16x16x32_bf16 v[188:191],a[124:127],v[84:87],v[188:191]\n"
"s_waitcnt lgkmcnt(0)\n"
"buffer_load_dwordx4 v112,s[24:27],s64 offen offset:3072 sc1 lds\n"
"v_mfma_f32_16x16x32_bf16 v[220:223],a[124:127],v[88:91],v[220:223]\n"
"s_add_u32 m0,s67,20480\n"
"v_cvt_pk_bf16_f32 v65,v2,v3\n"
"v_cvt_pk_bf16_f32 v66,v4,v5\n"
"v_mfma_f32_16x16x32_bf16 v[252:255],a[124:127],v[92:95],v[252:255]\n"
"buffer_load_dwordx4 v117,s[24:27],s64 offen offset:0 sc1 lds\n"
"v_mfma_f32_16x16x32_bf16 v[156:159],a[124:127],v[80:83],v[156:159]\n"
"buffer_load_dwordx4 v117,s[24:27],s64 offen offset:1024 sc1 lds\n"
"buffer_load_dwordx4 v117,s[24:27],s64 offen offset:2048 sc1 lds\n"
"buffer_load_dwordx4 v117,s[24:27],s64 offen offset:3072 sc1 lds\n"
"buffer_store_dwordx4 a[244:247],v112,s[24:27],s56 offen offset:1024 sc1\n"
"s_add_u32 s64,s64,16384\n"
"v_cvt_pk_bf16_f32 v67,v6,v7\n"
"v_cvt_pk_bf16_f32 v68,v8,v9\n"
"v_cvt_pk_bf16_f32 v69,v10,v11\n"
"v_cvt_pk_bf16_f32 v70,v12,v13\n"
"v_cvt_pk_bf16_f32 v71,v14,v15\n"
"v_cvt_pk_bf16_f32 v72,v16,v17\n"
"v_cvt_pk_bf16_f32 v73,v18,v19\n"
"v_cvt_pk_bf16_f32 v74,v20,v21\n"
"v_cvt_pk_bf16_f32 v75,v22,v23\n"
"v_cvt_pk_bf16_f32 v76,v24,v25\n"
"v_cvt_pk_bf16_f32 v77,v26,v27\n"
"v_cvt_pk_bf16_f32 v78,v28,v29\n"
"v_cvt_pk_bf16_f32 v79,v30,v31\n"
"v_max3_f32 v118,v48,v49,v50\n"
"v_max3_f32 v118,v118,v51,v52\n"
"v_max3_f32 v118,v118,v53,v54\n"
"v_max3_f32 v118,v118,v55,v60\n"
"v_max3_f32 v118,v118,v61,v62\n"
"v_max3_f32 v118,v118,v63,v56\n"
"v_max3_f32 v118,v118,v57,v58\n"
"v_max3_f32 v118,v118,v59,v40\n"
"v_max3_f32 v118,v118,v41,v42\n"
"v_max3_f32 v118,v118,v43,v44\n"
"v_max3_f32 v118,v118,v45,v46\n"
"v_max_f32 v118,v118,v47\n"
"v_cmp_lt_f32 vcc,s93,v118\n"
"s_nop 0\n"
"s_cbranch_vccnz .Lfk_cs_u0\n"
".Lfk_cr_u0:\n"
"v_cmp_gt_u32 vcc,s101,v125\n"
"s_nop 0\n"
"s_cbranch_vccnz .Lfk_cs_mu0\n"
".Lfk_cr_mu0:\n"
"v_mfma_f32_16x16x32_bf16 v[24:27],a[64:67],a[48:51],v[108:111]\n"
"ds_read_b128 a[96:99],v114 offset:40960\n"
"ds_read_b128 a[100:103],v114 offset:41984\n"
"v_mfma_f32_16x16x32_bf16 v[24:27],a[68:71],a[52:55],v[24:27]\n"
"ds_read_b128 a[104:107],v114 offset:43008\n"
"ds_read_b128 a[108:111],v114 offset:44032\n"
"v_mfma_f32_16x16x32_bf16 v[24:27],a[72:75],a[56:59],v[24:27]\n"
"ds_read_b128 a[112:115],v114 offset:45056\n"
"ds_read_b128 a[116:119],v114 offset:46080\n"
"v_mfma_f32_16x16x32_bf16 v[24:27],a[76:79],a[60:63],v[24:27]\n"
"ds_read_b128 a[120:123],v114 offset:47104\n"
"ds_read_b128 a[124:127],v114 offset:48128\n"
"v_mfma_f32_16x16x32_bf16 v[28:31],a[92:95],a[60:63],v[108:111]\n"
"ds_read_b128 a[16:19],v116 offset:8192\n"
"ds_read_b128 a[20:23],v116 offset:9216\n"
"v_mfma_f32_16x16x32_bf16 v[28:31],a[88:91],a[56:59],v[28:31]\n"
"ds_read_b128 a[24:27],v116 offset:10240\n"
"ds_read_b128 a[28:31],v116 offset:11264\n"
"v_mfma_f32_16x16x32_bf16 v[28:31],a[84:87],a[52:55],v[28:31]\n"
"v_add_f32 v16,0x80000000,v126\n"
"v_min_f32 v16,v16,v104\n"
"v_mfma_f32_16x16x32_bf16 v[28:31],a[80:83],a[48:51],v[28:31]\n"
"v_add_f32 v17,0xf149f2ca,v126\n"
"v_add_f32 v18,0xf1c9f2ca,v126\n"
"v_add_f32 v19,0xf2177617,v126\n"
"v_min_f32 v19,v19,v107\n"
"v_add_f32 v20,0xf349f2ca,v126\n"
"v_min_f32 v20,v20,v104\n"
"v_add_f32 v21,0xf35691f6,v126\n"
"v_min_f32 v21,v21,v105\n"
"v_add_f32 v22,0xf3633123,v126\n"
"v_min_f32 v22,v22,v106\n"
"v_add_f32 v23,0xf36fd050,v126\n"
"v_min_f32 v23,v23,v107\n"
"v_min_f32 v18,v18,v106\n"
"v_min_f32 v17,v17,v105\n"
"v_mfma_f32_16x16x32_bf16 v[20:23],a[80:83],a[32:35],v[20:23]\n"
"s_add_u32 s101,s101,4\n"
"v_exp_f32 v40,v40\n"
"v_mfma_f32_16x16x32_bf16 v[20:23],a[84:87],a[36:39],v[20:23]\n"
"s_add_u32 m0,s55,8192\n"
"v_exp_f32 v41,v41\n"
"v_mfma_f32_16x16x32_bf16 v[20:23],a[88:91],a[40:43],v[20:23]\n"
"s_add_u32 s54,s54,s22\n"
"v_exp_f32 v42,v42\n"
"v_mfma_f32_16x16x32_bf16 v[20:23],a[92:95],a[44:47],v[20:23]\n"
"v_exp_f32 v43,v43\n"
"v_mfma_f32_16x16x32_bf16 v[16:19],a[76:79],a[44:47],v[16:19]\n"
"v_exp_f32 v44,v44\n"
"v_mfma_f32_16x16x32_bf16 v[16:19],a[72:75],a[40:43],v[16:19]\n"
"v_exp_f32 v45,v45\n"
"v_mfma_f32_16x16x32_bf16 v[16:19],a[68:71],a[36:39],v[16:19]\n"
"v_exp_f32 v46,v46\n"
"v_mfma_f32_16x16x32_bf16 v[16:19],a[64:67],a[32:35],v[16:19]\n"
"s_waitcnt lgkmcnt(4)\n"
"v_exp_f32 v47,v47\n"
"v_mfma_f32_16x16x32_bf16 v[128:131],a[96:99],v[64:67],v[128:131]\n"
"v_exp_f32 v48,v48\n"
"v_mfma_f32_16x16x32_bf16 v[160:163],a[96:99],v[68:71],v[160:163]\n"
"v_exp_f32 v49,v49\n"
"v_mfma_f32_16x16x32_bf16 v[192:195],a[96:99],v[72:75],v[192:195]\n"
"ds_read_b128 a[64:67],v113 offset:32768\n"
"ds_read_b128 a[68:71],v113 offset:33792\n"
"v_mfma_f32_16x16x32_bf16 v[224:227],a[96:99],v[76:79],v[224:227]\n"
"v_exp_f32 v50,v50\n"
"v_mfma_f32_16x16x32_bf16 a[140:143],a[144:147],v[76:79],a[140:143]\n"
"ds_read_b128 a[72:75],v113 offset:34816\n"
"ds_read_b128 a[76:79],v113 offset:35840\n"
"v_mfma_f32_16x16x32_bf16 v[228:231],a[100:103],v[76:79],v[228:231]\n"
"v_exp_f32 v51,v51\n"
"v_mfma_f32_16x16x32_bf16 v[132:135],a[100:103],v[64:67],v[132:135]\n"
"ds_read_b128 a[80:83],v113 offset:36864\n"
"ds_read_b128 a[84:87],v113 offset:37888\n"
"v_mfma_f32_16x16x32_bf16 v[164:167],a[100:103],v[68:71],v[164:167]\n"
"v_exp_f32 v52,v52\n"
"v_mfma_f32_16x16x32_bf16 v[196:199],a[100:103],v[72:75],v[196:199]\n"
"v_exp_f32 v53,v53\n"
"v_mfma_f32_16x16x32_bf16 a[136:139],a[144:147],v[72:75],a[136:139]\n"
"ds_read_b128 a[88:91],v113 offset:38912\n"
"ds_read_b128 a[92:95],v113 offset:39936\n"
"v_mfma_f32_16x16x32_bf16 v[200:203],a[104:107],v[72:75],v[200:203]\n"
"v_exp_f32 v54,v54\n"
"v_mfma_f32_16x16x32_bf16 v[232:235],a[104:107],v[76:79],v[232:235]\n"
"s_waitcnt vmcnt(11)\n"
"ds_add_u32 v124,a152\n"
"v_mfma_f32_16x16x32_bf16 v[136:139],a[104:107],v[64:67],v[136:139]\n"
"v_exp_f32 v55,v55\n"
"v_mfma_f32_16x16x32_bf16 v[168:171],a[104:107],v[68:71],v[168:171]\n"
"buffer_load_dwordx4 v115,s[32:35],s54 offen sc1 lds\n"
"v_mfma_f32_16x16x32_bf16 a[132:135],a[144:147],v[68:71],a[132:135]\n"
"s_add_u32 m0,s67,49152\n"
"v_exp_f32 v56,v56\n"
"v_mfma_f32_16x16x32_bf16 v[172:175],a[108:111],v[68:71],v[172:175]\n"
"buffer_load_dwordx4 v112,s[24:27],s64 offen offset:0 sc1 lds\n"
"v_mfma_f32_16x16x32_bf16 v[204:207],a[108:111],v[72:75],v[204:207]\n"
"v_exp_f32 v57,v57\n"
"v_mfma_f32_16x16x32_bf16 v[236:239],a[108:111],v[76:79],v[236:239]\n"
"buffer_load_dwordx4 v112,s[24:27],s64 offen offset:1024 sc1 lds\n"
"v_mfma_f32_16x16x32_bf16 v[140:143],a[108:111],v[64:67],v[140:143]\n"
"v_exp_f32 v58,v58\n"
"v_mfma_f32_16x16x32_bf16 a[128:131],a[144:147],v[64:67],a[128:131]\n"
"buffer_load_dwordx4 v112,s[24:27],s64 offen offset:2048 sc1 lds\n"
"v_mfma_f32_16x16x32_bf16 v[144:147],a[112:115],v[64:67],v[144:147]\n"
"v_exp_f32 v59,v59\n"
"v_mfma_f32_16x16x32_bf16 v[176:179],a[112:115],v[68:71],v[176:179]\n"
"buffer_load_dwordx4 v112,s[24:27],s64 offen offset:3072 sc1 lds\n"
"v_mfma_f32_16x16x32_bf16 v[208:211],a[112:115],v[72:75],v[208:211]\n"
"s_add_u32 m0,s67,53248\n"
"v_exp_f32 v60,v60\n"
"v_mfma_f32_16x16x32_bf16 v[240:243],a[112:115],v[76:79],v[240:243]\n"
"buffer_load_dwordx4 v117,s[24:27],s64 offen offset:0 sc1 lds\n"
"v_mfma_f32_16x16x32_bf16 v[244:247],a[116:119],v[76:79],v[244:247]\n"
"v_exp_f32 v61,v61\n"
"v_mfma_f32_16x16x32_bf16 v[148:151],a[116:119],v[64:67],v[148:151]\n"
"buffer_load_dwordx4 v117,s[24:27],s64 offen offset:1024 sc1 lds\n"
"v_mfma_f32_16x16x32_bf16 v[180:183],a[116:119],v[68:71],v[180:183]\n"
"v_exp_f32 v62,v62\n"
"v_mfma_f32_16x16x32_bf16 v[212:215],a[116:119],v[72:75],v[212:215]\n"
"ds_read_b32 v125,v124\n"
"v_cvt_pk_bf16_f32 v84,v40,v41\n"
"v_mfma_f32_16x16x32_bf16 v[216:219],a[120:123],v[72:75],v[216:219]\n"
"v_exp_f32 v63,v63\n"
"v_mfma_f32_16x16x32_bf16 v[248:251],a[120:123],v[76:79],v[248:251]\n"
"buffer_load_dwordx4 v117,s[24:27],s64 offen offset:2048 sc1 lds\n"
"v_mfma_f32_16x16x32_bf16 v[152:155],a[120:123],v[64:67],v[152:155]\n"
"buffer_load_dwordx4 v117,s[24:27],s64 offen offset:3072 sc1 lds\n"
"v_mfma_f32_16x16x32_bf16 v[184:187],a[120:123],v[68:71],v[184:187]\n"
"s_add_u32 s64,s64,16384\n"
"buffer_store_dwordx4 a[248:251],v112,s[24:27],s56 offen offset:2048 sc1\n"
"v_mfma_f32_16x16x32_bf16 v[188:191],a[124:127],v[68:71],v[188:191]\n"
"s_waitcnt lgkmcnt(0)\n"
"s_and_b32 s67,s67,8192\n"
"v_cvt_pk_bf16_f32 v85,v42,v43\n"
"v_mfma_f32_16x16x32_bf16 v[220:223],a[124:127],v[72:75],v[220:223]\n"
"v_cvt_pk_bf16_f32 v86,v44,v45\n"
"v_cvt_pk_bf16_f32 v87,v46,v47\n"
"v_mfma_f32_16x16x32_bf16 v[252:255],a[124:127],v[76:79],v[252:255]\n"
"v_cvt_pk_bf16_f32 v88,v48,v49\n"
"v_cvt_pk_bf16_f32 v89,v50,v51\n"
"v_mfma_f32_16x16x32_bf16 v[156:159],a[124:127],v[64:67],v[156:159]\n"
"v_cvt_pk_bf16_f32 v90,v52,v53\n"
"v_cvt_pk_bf16_f32 v91,v54,v55\n"
"v_cvt_pk_bf16_f32 v92,v56,v57\n"
"v_cvt_pk_bf16_f32 v93,v58,v59\n"
"v_cvt_pk_bf16_f32 v94,v60,v61\n"
"v_cvt_pk_bf16_f32 v95,v62,v63\n"
"v_max3_f32 v118,v24,v25,v26\n"
"v_max3_f32 v118,v118,v27,v28\n"
"v_max3_f32 v118,v118,v29,v30\n"
"v_max3_f32 v118,v118,v31,v20\n"
"v_max3_f32 v118,v118,v21,v22\n"
"v_max3_f32 v118,v118,v23,v16\n"
"v_max3_f32 v118,v118,v17,v18\n"
"v_max_f32 v118,v118,v19\n"
"v_cmp_lt_f32 vcc,s93,v118\n"
"s_nop 0\n"
"s_cbranch_vccnz .Lfk_cs_u1\n"
".Lfk_cr_u1:\n"
"v_cmp_gt_u32 vcc,s101,v125\n"
"s_nop 0\n"
"s_cbranch_vccnz .Lfk_cs_mu1\n"
".Lfk_cr_mu1:\n"
"v_mov_b32 v113,v112\n"
"v_add_f32 v56,0x80000000,v126\n"
"v_min_f32 v56,v56,v108\n"
"v_add_f32 v57,0xf149f2ca,v126\n"
"v_min_f32 v57,v57,v109\n"
"v_add_f32 v58,0xf1c9f2ca,v126\n"
"v_min_f32 v58,v58,v110\n"
"v_add_f32 v59,0xf2177617,v126\n"
"v_min_f32 v59,v59,v111\n"
"v_add_f32 v60,0xf349f2ca,v126\n"
"v_min_f32 v60,v60,v108\n"
"v_add_f32 v61,0xf35691f6,v126\n"
"v_min_f32 v61,v61,v109\n"
"v_add_f32 v62,0xf3633123,v126\n"
"v_min_f32 v62,v62,v110\n"
"v_add_f32 v63,0xf36fd050,v126\n"
"v_min_f32 v63,v63,v111\n"
"v_exp_f32 v16,v16\n"
"v_mfma_f32_16x16x32_bf16 v[56:59],a[64:67],a[48:51],v[56:59]\n"
"s_add_u32 s101,s101,4\n"
"ds_read_b128 a[96:99],v114 offset:8192\n"
"ds_read_b128 a[100:103],v114 offset:9216\n"
"ds_read_b128 a[104:107],v114 offset:10240\n"
"v_exp_f32 v17,v17\n"
"v_mfma_f32_16x16x32_bf16 v[56:59],a[68:71],a[52:55],v[56:59]\n"
"s_add_u32 m0,s55,12288\n"
"ds_read_b128 a[108:111],v114 offset:11264\n"
"ds_read_b128 a[112:115],v114 offset:12288\n"
"ds_read_b128 a[116:119],v114 offset:13312\n"
"v_mfma_f32_16x16x32_bf16 v[56:59],a[72:75],a[56:59],v[56:59]\n"
"ds_read_b128 a[120:123],v114 offset:14336\n"
"ds_read_b128 a[124:127],v114 offset:15360\n"
"ds_read_b128 a[32:35],v116 offset:24576\n"
"ds_read_b128 a[36:39],v116 offset:25600\n"
"ds_read_b128 a[40:43],v116 offset:26624\n"
"v_mfma_f32_16x16x32_bf16 v[56:59],a[76:79],a[60:63],v[56:59]\n"
"s_add_u32 s54,s54,s22\n"
"ds_read_b128 a[44:47],v116 offset:27648\n"
"v_exp_f32 v18,v18\n"
"v_mfma_f32_16x16x32_bf16 v[60:63],a[92:95],a[60:63],v[60:63]\n"
"buffer_load_dwordx4 v115,s[32:35],s54 offen sc1 lds\n"
"v_cvt_pk_bf16_f32 v72,v16,v17\n"
"v_mfma_f32_16x16x32_bf16 v[60:63],a[88:91],a[56:59],v[60:63]\n"
"s_add_u32 m0,s67,32768\n"
"v_exp_f32 v19,v19\n"
"v_mfma_f32_16x16x32_bf16 v[60:63],a[84:87],a[52:55],v[60:63]\n"
"buffer_load_dwordx4 v112,s[24:27],s64 offen offset:0 sc1 lds\n"
"v_cvt_pk_bf16_f32 v73,v18,v19\n"
"v_exp_f32 v20,v20\n"
"v_mfma_f32_16x16x32_bf16 v[60:63],a[80:83],a[48:51],v[60:63]\n"
"ds_read_b128 a[64:67],v113 offset:0\n"
"ds_read_b128 a[68:71],v113 offset:1024\n"
"ds_read_b128 a[72:75],v113 offset:2048\n"
"s_waitcnt lgkmcnt(7)\n"
"v_mfma_f32_16x16x32_bf16 v[160:163],a[96:99],v[84:87],v[160:163]\n"
"ds_read_b128 a[76:79],v113 offset:3072\n"
"ds_read_b128 a[80:83],v113 offset:4096\n"
"ds_read_b128 a[84:87],v113 offset:5120\n"
"ds_read_b128 a[88:91],v113 offset:6144\n"
"ds_read_b128 a[92:95],v113 offset:7168\n"
"v_exp_f32 v21,v21\n"
"v_mfma_f32_16x16x32_bf16 a[132:135],a[144:147],v[84:87],a[132:135]\n"
"buffer_load_dwordx4 v112,s[24:27],s64 offen offset:1024 sc1 lds\n"
"v_max3_f32 v118,v56,v57,v58\n"
"v_mfma_f32_16x16x32_bf16 v[164:167],a[100:103],v[84:87],v[164:167]\n"
"v_exp_f32 v22,v22\n"
"v_cvt_pk_bf16_f32 v74,v20,v21\n"
"ds_read_b128 a[48:51],v116 offset:57344\n"
"ds_read_b128 a[52:55],v116 offset:58368\n"
"v_mfma_f32_16x16x32_bf16 v[168:171],a[104:107],v[84:87],v[168:171]\n"
"v_max3_f32 v118,v118,v59,v60\n"
"v_max3_f32 v118,v118,v61,v62\n"
"v_max_f32 v118,v118,v63\n"
"v_exp_f32 v23,v23\n"
"v_mfma_f32_16x16x32_bf16 v[172:175],a[108:111],v[84:87],v[172:175]\n"
"ds_read_b128 a[56:59],v116 offset:59392\n"
"buffer_load_dwordx4 v112,s[24:27],s64 offen offset:2048 sc1 lds\n"
"ds_read_b128 a[60:63],v116 offset:60416\n"
"v_mfma_f32_16x16x32_bf16 v[176:179],a[112:115],v[84:87],v[176:179]\n"
"s_waitcnt vmcnt(15)\n"
"v_cvt_pk_bf16_f32 v75,v22,v23\n"
"v_accvgpr_read_b32 v122,a128\n"
"v_rcp_f32 v122,v122\n"
"v_mfma_f32_16x16x32_bf16 v[180:183],a[116:119],v[84:87],v[180:183]\n"
"ds_add_u32 v124,a152\n"
"v_cmp_lt_f32 vcc,s93,v118\n"
"v_exp_f32 v24,v24\n"
"v_pk_mul_f32 v[128:129],v[128:129],v[122:123] op_sel_hi:[1,0]\n"
"v_pk_mul_f32 v[130:131],v[130:131],v[122:123] op_sel_hi:[1,0]\n"
"v_pk_mul_f32 v[132:133],v[132:133],v[122:123] op_sel_hi:[1,0]\n"
"v_pk_mul_f32 v[134:135],v[134:135],v[122:123] op_sel_hi:[1,0]\n"
"v_mfma_f32_16x16x32_bf16 v[184:187],a[120:123],v[84:87],v[184:187]\n"
"buffer_load_dwordx4 v112,s[24:27],s64 offen offset:3072 sc1 lds\n"
"v_cvt_pk_bf16_f32 v128,v128,v129\n"
"v_cvt_pk_bf16_f32 v129,v130,v131\n"
"v_mfma_f32_16x16x32_bf16 v[188:191],a[124:127],v[84:87],v[188:191]\n"
"s_add_u32 m0,s67,36864\n"
"v_cvt_pk_bf16_f32 v130,v132,v133\n"
"v_cvt_pk_bf16_f32 v131,v134,v135\n"
"v_exp_f32 v25,v25\n"
"v_mfma_f32_16x16x32_bf16 v[192:195],a[96:99],v[88:91],v[192:195]\n"
"buffer_store_dwordx4 v[128:131],v112,s[24:27],s70 offen offset:0 sc1\n"
"v_cvt_pk_bf16_f32 v76,v24,v25\n"
"v_pk_mul_f32 v[136:137],v[136:137],v[122:123] op_sel_hi:[1,0]\n"
"v_pk_mul_f32 v[138:139],v[138:139],v[122:123] op_sel_hi:[1,0]\n"
"v_pk_mul_f32 v[140:141],v[140:141],v[122:123] op_sel_hi:[1,0]\n"
"v_pk_mul_f32 v[142:143],v[142:143],v[122:123] op_sel_hi:[1,0]\n"
"v_exp_f32 v26,v26\n"
"v_mfma_f32_16x16x32_bf16 a[136:139],a[144:147],v[88:91],a[136:139]\n"
"buffer_load_dwordx4 v117,s[24:27],s64 offen offset:0 sc1 lds\n"
"v_cvt_pk_bf16_f32 v136,v136,v137\n"
"v_mfma_f32_16x16x32_bf16 v[196:199],a[100:103],v[88:91],v[196:199]\n"
"v_exp_f32 v27,v27\n"
"v_cvt_pk_bf16_f32 v137,v138,v139\n"
"v_cvt_pk_bf16_f32 v138,v140,v141\n"
"v_cvt_pk_bf16_f32 v139,v142,v143\n"
"v_cvt_pk_bf16_f32 v77,v26,v27\n"
"v_mfma_f32_16x16x32_bf16 v[200:203],a[104:107],v[88:91],v[200:203]\n"
"buffer_store_dwordx4 v[136:139],v112,s[24:27],s70 offen offset:1024 sc1\n"
"v_pk_mul_f32 v[144:145],v[144:145],v[122:123] op_sel_hi:[1,0]\n"
"v_pk_mul_f32 v[146:147],v[146:147],v[122:123] op_sel_hi:[1,0]\n"
"v_pk_mul_f32 v[148:149],v[148:149],v[122:123] op_sel_hi:[1,0]\n"
"v_pk_mul_f32 v[150:151],v[150:151],v[122:123] op_sel_hi:[1,0]\n"
"v_mfma_f32_16x16x32_bf16 v[204:207],a[108:111],v[88:91],v[204:207]\n"
"buffer_load_dwordx4 v117,s[24:27],s64 offen offset:1024 sc1 lds\n"
"v_exp_f32 v28,v28\n"
"v_mfma_f32_16x16x32_bf16 v[208:211],a[112:115],v[88:91],v[208:211]\n"
"v_cvt_pk_bf16_f32 v144,v144,v145\n"
"v_cvt_pk_bf16_f32 v145,v146,v147\n"
"v_cvt_pk_bf16_f32 v146,v148,v149\n"
"v_cvt_pk_bf16_f32 v147,v150,v151\n"
"v_mfma_f32_16x16x32_bf16 v[212:215],a[116:119],v[88:91],v[212:215]\n"
"buffer_store_dwordx4 v[144:147],v112,s[24:27],s70 offen offset:2048 sc1\n"
"v_pk_mul_f32 v[152:153],v[152:153],v[122:123] op_sel_hi:[1,0]\n"
"v_pk_mul_f32 v[154:155],v[154:155],v[122:123] op_sel_hi:[1,0]\n"
"v_pk_mul_f32 v[156:157],v[156:157],v[122:123] op_sel_hi:[1,0]\n"
"v_mfma_f32_16x16x32_bf16 v[216:219],a[120:123],v[88:91],v[216:219]\n"
"buffer_load_dwordx4 v117,s[24:27],s64 offen offset:2048 sc1 lds\n"
"v_exp_f32 v29,v29\n"
"v_cvt_pk_bf16_f32 v152,v152,v153\n"
"v_cvt_pk_bf16_f32 v78,v28,v29\n"
"v_pk_mul_f32 v[158:159],v[158:159],v[122:123] op_sel_hi:[1,0]\n"
"v_mfma_f32_16x16x32_bf16 v[220:223],a[124:127],v[88:91],v[220:223]\n"
"v_cvt_pk_bf16_f32 v153,v154,v155\n"
"v_cvt_pk_bf16_f32 v154,v156,v157\n"
"v_cvt_pk_bf16_f32 v155,v158,v159\n"
"v_mfma_f32_16x16x32_bf16 v[224:227],a[96:99],v[92:95],v[224:227]\n"
"ds_read_b32 v125,v124\n"
"buffer_store_dwordx4 v[152:155],v112,s[24:27],s70 offen offset:3072 sc1\n"
"buffer_load_dwordx4 v117,s[24:27],s64 offen offset:3072 sc1 lds\n"
"v_accvgpr_read_b32 v122,a132\n"
"v_mfma_f32_16x16x32_bf16 a[140:143],a[144:147],v[92:95],a[140:143]\n"
"s_add_u32 s70,s70,262144\n"
"v_exp_f32 v31,v31\n"
"v_mfma_f32_16x16x32_bf16 v[228:231],a[100:103],v[92:95],v[228:231]\n"
"s_add_u32 s64,s64,16384\n"
"v_exp_f32 v30,v30\n"
"v_rcp_f32 v122,v122\n"
"v_cvt_pk_bf16_f32 v79,v30,v31\n"
"v_pk_mul_f32 v[160:161],v[160:161],v[122:123] op_sel_hi:[1,0]\n"
"v_pk_mul_f32 v[162:163],v[162:163],v[122:123] op_sel_hi:[1,0]\n"
"v_pk_mul_f32 v[164:165],v[164:165],v[122:123] op_sel_hi:[1,0]\n"
"v_pk_mul_f32 v[166:167],v[166:167],v[122:123] op_sel_hi:[1,0]\n"
"v_mfma_f32_16x16x32_bf16 v[232:235],a[104:107],v[92:95],v[232:235]\n"
"buffer_store_dwordx4 a[252:255],v112,s[24:27],s56 offen offset:3072 sc1\n"
"v_cvt_pk_bf16_f32 v160,v160,v161\n"
"v_cvt_pk_bf16_f32 v161,v162,v163\n"
"v_cvt_pk_bf16_f32 v162,v164,v165\n"
"v_cvt_pk_bf16_f32 v163,v166,v167\n"
"v_mfma_f32_16x16x32_bf16 v[236:239],a[108:111],v[92:95],v[236:239]\n"
"buffer_store_dwordx4 v[160:163],v112,s[24:27],s70 offen offset:0 sc1\n"
"v_pk_mul_f32 v[168:169],v[168:169],v[122:123] op_sel_hi:[1,0]\n"
"v_pk_mul_f32 v[170:171],v[170:171],v[122:123] op_sel_hi:[1,0]\n"
"v_pk_mul_f32 v[172:173],v[172:173],v[122:123] op_sel_hi:[1,0]\n"
"v_pk_mul_f32 v[174:175],v[174:175],v[122:123] op_sel_hi:[1,0]\n"
"v_mfma_f32_16x16x32_bf16 v[240:243],a[112:115],v[92:95],v[240:243]\n"
"s_waitcnt lgkmcnt(0)\n"
"v_cvt_pk_bf16_f32 v168,v168,v169\n"
"v_cvt_pk_bf16_f32 v169,v170,v171\n"
"v_cvt_pk_bf16_f32 v170,v172,v173\n"
"v_cvt_pk_bf16_f32 v171,v174,v175\n"
"v_mfma_f32_16x16x32_bf16 v[244:247],a[116:119],v[92:95],v[244:247]\n"
"buffer_store_dwordx4 v[168:171],v112,s[24:27],s70 offen offset:1024 sc1\n"
"v_pk_mul_f32 v[176:177],v[176:177],v[122:123] op_sel_hi:[1,0]\n"
"v_pk_mul_f32 v[178:179],v[178:179],v[122:123] op_sel_hi:[1,0]\n"
"v_pk_mul_f32 v[180:181],v[180:181],v[122:123] op_sel_hi:[1,0]\n"
"v_pk_mul_f32 v[182:183],v[182:183],v[122:123] op_sel_hi:[1,0]\n"
"v_mfma_f32_16x16x32_bf16 v[248:251],a[120:123],v[92:95],v[248:251]\n"
"v_cvt_pk_bf16_f32 v176,v176,v177\n"
"v_cvt_pk_bf16_f32 v177,v178,v179\n"
"v_cvt_pk_bf16_f32 v178,v180,v181\n"
"v_cvt_pk_bf16_f32 v179,v182,v183\n"
"v_mfma_f32_16x16x32_bf16 v[252:255],a[124:127],v[92:95],v[252:255]\n"
"buffer_store_dwordx4 v[176:179],v112,s[24:27],s70 offen offset:2048 sc1\n"
"v_pk_mul_f32 v[184:185],v[184:185],v[122:123] op_sel_hi:[1,0]\n"
"v_pk_mul_f32 v[186:187],v[186:187],v[122:123] op_sel_hi:[1,0]\n"
"v_pk_mul_f32 v[188:189],v[188:189],v[122:123] op_sel_hi:[1,0]\n"
"v_pk_mul_f32 v[190:191],v[190:191],v[122:123] op_sel_hi:[1,0]\n"
"v_cvt_pk_bf16_f32 v184,v184,v185\n"
"v_cvt_pk_bf16_f32 v185,v186,v187\n"
"v_cvt_pk_bf16_f32 v186,v188,v189\n"
"v_cvt_pk_bf16_f32 v187,v190,v191\n"
"buffer_store_dwordx4 v[184:187],v112,s[24:27],s70 offen offset:3072 sc1\n"
"s_add_u32 s70,s70,262144\n"
"s_cbranch_vccnz .Lfk_cs_u2\n"
".Lfk_cr_u2:\n"
"v_cmp_gt_u32 vcc,s101,v125\n"
"s_nop 0\n"
"s_cbranch_vccnz .Lfk_cs_mu2\n"
".Lfk_cr_mu2:\n"
"s_add_u32 s89,s89,1\n"
"s_cmp_eq_u32 s89,8\n"
"s_cbranch_scc1 .Lfk_tailB7\n"
".Lfk_winB:\n"
"v_mfma_f32_16x16x32_bf16 v[0:3],a[64:67],a[0:3],v[96:99]\n"
"s_swappc_b64 s[94:95],s[20:21]\n"
"ds_read_b128 a[96:99],v114 offset:24576\n"
"v_mfma_f32_16x16x32_bf16 v[0:3],a[68:71],a[4:7],v[0:3]\n"
"s_add_u32 s101,s101,4\n"
"ds_read_b128 a[100:103],v114 offset:25600\n"
"ds_read_b128 a[104:107],v114 offset:26624\n"
"ds_read_b128 a[108:111],v114 offset:27648\n"
"v_mfma_f32_16x16x32_bf16 v[0:3],a[72:75],a[8:11],v[0:3]\n"
"ds_read_b128 a[112:115],v114 offset:28672\n"
"ds_read_b128 a[116:119],v114 offset:29696\n"
"v_mfma_f32_16x16x32_bf16 v[0:3],a[76:79],a[12:15],v[0:3]\n"
"ds_read_b128 a[120:123],v114 offset:30720\n"
"ds_read_b128 a[124:127],v114 offset:31744\n"
"v_mfma_f32_16x16x32_bf16 v[4:7],a[92:95],a[12:15],v[96:99]\n"
"v_exp_f32 v56,v56\n"
"v_mfma_f32_16x16x32_bf16 v[4:7],a[88:91],a[8:11],v[4:7]\n"
"v_exp_f32 v57,v57\n"
"v_mfma_f32_16x16x32_bf16 v[4:7],a[84:87],a[4:7],v[4:7]\n"
"v_cvt_pk_bf16_f32 v92,v56,v57\n"
"v_mfma_f32_16x16x32_bf16 v[4:7],a[80:83],a[0:3],v[4:7]\n"
"s_add_u32 m0,s67,0\n"
"s_waitcnt lgkmcnt(0)\n"
"v_mfma_f32_16x16x32_bf16 v[192:195],a[96:99],v[72:75],v[192:195]\n"
"buffer_load_dwordx4 v112,s[24:27],s64 offen offset:0 sc1 lds\n"
"v_mfma_f32_16x16x32_bf16 a[136:139],a[144:147],v[72:75],a[136:139]\n"
"buffer_load_dwordx4 v112,s[24:27],s64 offen offset:1024 sc1 lds\n"
"v_mfma_f32_16x16x32_bf16 v[196:199],a[100:103],v[72:75],v[196:199]\n"
"v_exp_f32 v58,v58\n"
"v_mfma_f32_16x16x32_bf16 v[200:203],a[104:107],v[72:75],v[200:203]\n"
"v_max3_f32 v118,v0,v1,v2\n"
"v_mfma_f32_16x16x32_bf16 v[204:207],a[108:111],v[72:75],v[204:207]\n"
"v_max3_f32 v118,v118,v3,v4\n"
"v_max3_f32 v118,v118,v5,v6\n"
"v_mfma_f32_16x16x32_bf16 v[208:211],a[112:115],v[72:75],v[208:211]\n"
"v_max_f32 v118,v118,v7\n"
"v_mov_b32 v120,v118\n"
"v_mfma_f32_16x16x32_bf16 v[212:215],a[116:119],v[72:75],v[212:215]\n"
"v_mfma_f32_16x16x32_bf16 v[216:219],a[120:123],v[72:75],v[216:219]\n"
"v_mfma_f32_16x16x32_bf16 v[220:223],a[124:127],v[72:75],v[220:223]\n"
"v_exp_f32 v59,v59\n"
"v_mfma_f32_16x16x32_bf16 v[224:227],a[96:99],v[76:79],v[224:227]\n"
"v_cvt_pk_bf16_f32 v93,v58,v59\n"
"v_accvgpr_read_b32 v122,a136\n"
"v_mfma_f32_16x16x32_bf16 a[140:143],a[144:147],v[76:79],a[140:143]\n"
"buffer_load_dwordx4 v112,s[24:27],s64 offen offset:2048 sc1 lds\n"
"v_mfma_f32_16x16x32_bf16 v[228:231],a[100:103],v[76:79],v[228:231]\n"
"v_rcp_f32 v122,v122\n"
"v_mfma_f32_16x16x32_bf16 v[232:235],a[104:107],v[76:79],v[232:235]\n"
"buffer_load_dwordx4 v112,s[24:27],s64 offen offset:3072 sc1 lds\n"
"v_pk_mul_f32 v[192:193],v[192:193],v[122:123] op_sel_hi:[1,0]\n"
"v_pk_mul_f32 v[194:195],v[194:195],v[122:123] op_sel_hi:[1,0]\n"
"v_mfma_f32_16x16x32_bf16 v[236:239],a[108:111],v[76:79],v[236:239]\n"
"v_cvt_pk_bf16_f32 v192,v192,v193\n"
"v_exp_f32 v60,v60\n"
"v_cvt_pk_bf16_f32 v193,v194,v195\n"
"v_pk_mul_f32 v[196:197],v[196:197],v[122:123] op_sel_hi:[1,0]\n"
"v_pk_mul_f32 v[198:199],v[198:199],v[122:123] op_sel_hi:[1,0]\n"
"v_mfma_f32_16x16x32_bf16 v[240:243],a[112:115],v[76:79],v[240:243]\n"
"s_add_u32 m0,s67,4096\n"
"v_cvt_pk_bf16_f32 v194,v196,v197\n"
"v_cvt_pk_bf16_f32 v195,v198,v199\n"
"v_mfma_f32_16x16x32_bf16 v[244:247],a[116:119],v[76:79],v[244:247]\n"
"v_mfma_f32_16x16x32_bf16 v[248:251],a[120:123],v[76:79],v[248:251]\n"
"buffer_store_dwordx4 v[192:195],v112,s[24:27],s70 offen offset:0 sc1\n"
"v_pk_mul_f32 v[200:201],v[200:201],v[122:123] op_sel_hi:[1,0]\n"
"v_mfma_f32_16x16x32_bf16 v[252:255],a[124:127],v[76:79],v[252:255]\n"
"buffer_load_dwordx4 v117,s[24:27],s64 offen offset:0 sc1 lds\n"
"v_cvt_pk_bf16_f32 v200,v200,v201\n"
"v_exp_f32 v61,v61\n"
"v_pk_mul_f32 v[202:203],v[202:203],v[122:123] op_sel_hi:[1,0]\n"
"v_pk_mul_f32 v[204:205],v[204:205],v[122:123] op_sel_hi:[1,0]\n"
"v_pk_mul_f32 v[206:207],v[206:207],v[122:123] op_sel_hi:[1,0]\n"
"v_mfma_f32_16x16x32_bf16 v[12:15],a[80:83],a[16:19],v[100:103]\n"
"v_cvt_pk_bf16_f32 v201,v202,v203\n"
"v_cvt_pk_bf16_f32 v202,v204,v205\n"
"v_mfma_f32_16x16x32_bf16 v[12:15],a[84:87],a[20:23],v[12:15]\n"
"v_cvt_pk_bf16_f32 v203,v206,v207\n"
"v_cvt_pk_bf16_f32 v94,v60,v61\n"
"v_mfma_f32_16x16x32_bf16 v[12:15],a[88:91],a[24:27],v[12:15]\n"
"buffer_load_dwordx4 v117,s[24:27],s64 offen offset:1024 sc1 lds\n"
"v_mfma_f32_16x16x32_bf16 v[12:15],a[92:95],a[28:31],v[12:15]\n"
"buffer_store_dwordx4 v[200:203],v112,s[24:27],s70 offen offset:1024 sc1\n"
"v_pk_mul_f32 v[208:209],v[208:209],v[122:123] op_sel_hi:[1,0]\n"
"v_pk_mul_f32 v[210:211],v[210:211],v[122:123] op_sel_hi:[1,0]\n"
"v_mfma_f32_16x16x32_bf16 v[8:11],a[76:79],a[28:31],v[100:103]\n"
"buffer_load_dwordx4 v117,s[24:27],s64 offen offset:2048 sc1 lds\n"
"v_cvt_pk_bf16_f32 v208,v208,v209\n"
"v_cvt_pk_bf16_f32 v209,v210,v211\n"
"v_pk_mul_f32 v[212:213],v[212:213],v[122:123] op_sel_hi:[1,0]\n"
"v_pk_mul_f32 v[214:215],v[214:215],v[122:123] op_sel_hi:[1,0]\n"
"v_mfma_f32_16x16x32_bf16 v[8:11],a[72:75],a[24:27],v[8:11]\n"
"v_cvt_pk_bf16_f32 v210,v212,v213\n"
"v_cvt_pk_bf16_f32 v211,v214,v215\n"
"v_mfma_f32_16x16x32_bf16 v[8:11],a[68:71],a[20:23],v[8:11]\n"
"v_exp_f32 v62,v62\n"
"v_mfma_f32_16x16x32_bf16 v[8:11],a[64:67],a[16:19],v[8:11]\n"
"buffer_store_dwordx4 v[208:211],v112,s[24:27],s70 offen offset:2048 sc1\n"
"v_pk_mul_f32 v[216:217],v[216:217],v[122:123] op_sel_hi:[1,0]\n"
"v_mfma_f32_16x16x32_bf16 v[16:19],a[64:67],a[32:35],v[104:107]\n"
"v_cvt_pk_bf16_f32 v216,v216,v217\n"
"buffer_load_dwordx4 v117,s[24:27],s64 offen offset:3072 sc1 lds\n"
"v_pk_mul_f32 v[218:219],v[218:219],v[122:123] op_sel_hi:[1,0]\n"
"v_pk_mul_f32 v[220:221],v[220:221],v[122:123] op_sel_hi:[1,0]\n"
"v_pk_mul_f32 v[222:223],v[222:223],v[122:123] op_sel_hi:[1,0]\n"
"v_mfma_f32_16x16x32_bf16 v[16:19],a[68:71],a[36:39],v[16:19]\n"
"v_cvt_pk_bf16_f32 v217,v218,v219\n"
"v_cvt_pk_bf16_f32 v218,v220,v221\n"
"v_mfma_f32_16x16x32_bf16 v[16:19],a[72:75],a[40:43],v[16:19]\n"
"s_add_u32 s64,s64,16384\n"
"v_exp_f32 v63,v63\n"
"v_mfma_f32_16x16x32_bf16 v[16:19],a[76:79],a[44:47],v[16:19]\n"
"s_andn2_b32 m0,s55,16384\n"
"s_add_u32 s54,s54,s22\n"
"v_max3_f32 v119,v8,v9,v10\n"
"v_mfma_f32_16x16x32_bf16 v[20:23],a[92:95],a[44:47],v[104:107]\n"
"v_max3_f32 v119,v119,v11,v12\n"
"v_cvt_pk_bf16_f32 v95,v62,v63\n"
"v_mfma_f32_16x16x32_bf16 v[20:23],a[88:91],a[40:43],v[20:23]\n"
"buffer_load_dwordx4 v115,s[32:35],s54 offen sc1 lds\n"
"v_mfma_f32_16x16x32_bf16 v[20:23],a[84:87],a[36:39],v[20:23]\n"
"v_max3_f32 v119,v119,v13,v14\n"
"v_max_f32 v119,v119,v15\n"
"v_mfma_f32_16x16x32_bf16 v[20:23],a[80:83],a[32:35],v[20:23]\n"
"v_cvt_pk_bf16_f32 v219,v222,v223\n"
"v_mfma_f32_16x16x32_bf16 v[28:31],a[80:83],a[48:51],v[108:111]\n"
"buffer_store_dwordx4 v[216:219],v112,s[24:27],s70 offen offset:3072 sc1\n"
"buffer_store_dwordx4 a[240:243],v112,s[24:27],s56 offen offset:0 sc1\n"
"v_mfma_f32_16x16x32_bf16 v[28:31],a[84:87],a[52:55],v[28:31]\n"
"s_add_u32 s70,s70,262144\n"
"v_max_f32 v118,v118,v119\n"
"v_min_f32 v120,v120,v119\n"
"v_mfma_f32_16x16x32_bf16 v[28:31],a[88:91],a[56:59],v[28:31]\n"
"v_max3_f32 v119,v16,v17,v18\n"
"v_mfma_f32_16x16x32_bf16 v[28:31],a[92:95],a[60:63],v[28:31]\n"
"v_max3_f32 v119,v119,v19,v20\n"
"v_max3_f32 v119,v119,v21,v22\n"
"v_mfma_f32_16x16x32_bf16 v[24:27],a[76:79],a[60:63],v[108:111]\n"
"v_max_f32 v119,v119,v23\n"
"v_max_f32 v118,v118,v119\n"
"v_mfma_f32_16x16x32_bf16 v[24:27],a[72:75],a[56:59],v[24:27]\n"
"v_min_f32 v120,v120,v119\n"
"v_mfma_f32_16x16x32_bf16 v[24:27],a[68:71],a[52:55],v[24:27]\n"
"v_mfma_f32_16x16x32_bf16 v[24:27],a[64:67],a[48:51],v[24:27]\n"
"ds_read_b128 a[64:67],v113 offset:16384\n"
"ds_read_b128 a[68:71],v113 offset:17408\n"
"ds_read_b128 a[72:75],v113 offset:18432\n"
"ds_read_b128 a[76:79],v113 offset:19456\n"
"ds_read_b128 a[80:83],v113 offset:20480\n"
"ds_read_b128 a[84:87],v113 offset:21504\n"
"ds_read_b128 a[88:91],v113 offset:22528\n"
"ds_read_b128 a[92:95],v113 offset:23552\n"
"v_max3_f32 v119,v24,v25,v26\n"
"v_max3_f32 v119,v119,v27,v28\n"
"v_max3_f32 v119,v119,v29,v30\n"
"v_max_f32 v119,v119,v31\n"
"v_min_f32 v120,v120,v119\n"
"v_max_f32 v118,v118,v119\n"
"v_cmp_gt_f32 s[96:97],s16,v120\n"
"v_cmp_lt_f32 vcc,s93,v118\n"
"s_waitcnt vmcnt(25)\n"
"ds_add_u32 v124,a152\n"
"ds_read_b32 v125,v124\n"
"s_waitcnt lgkmcnt(0)\n"
"s_or_b64 s[96:97],s[96:97],vcc\n"
"s_cbranch_scc1 .Lfk_cs_v3\n"
".Lfk_cr_v3:\n"
"v_cmp_gt_u32 vcc,s101,v125\n"
"s_nop 0\n"
"s_cbranch_vccnz .Lfk_cs_mv3\n"
".Lfk_cr_mv3:\n"
"v_exp_f32 v0,v0\n"
"v_mfma_f32_16x16x32_bf16 v[32:35],a[64:67],a[0:3],v[96:99]\n"
"s_add_u32 s101,s101,4\n"
"ds_read_b128 a[96:99],v114 offset:57344\n"
"ds_read_b128 a[100:103],v114 offset:58368\n"
"v_exp_f32 v1,v1\n"
"v_mfma_f32_16x16x32_bf16 v[32:35],a[68:71],a[4:7],v[32:35]\n"
"s_add_u32 m0,s55,4096\n"
"ds_read_b128 a[104:107],v114 offset:59392\n"
"ds_read_b128 a[108:111],v114 offset:60416\n"
"v_exp_f32 v2,v2\n"
"v_mfma_f32_16x16x32_bf16 v[32:35],a[72:75],a[8:11],v[32:35]\n"
"s_add_u32 s54,s54,s22\n"
"ds_read_b128 a[112:115],v114 offset:61440\n"
"ds_read_b128 a[116:119],v114 offset:62464\n"
"v_exp_f32 v3,v3\n"
"v_mfma_f32_16x16x32_bf16 v[32:35],a[76:79],a[12:15],v[32:35]\n"
"ds_read_b128 a[120:123],v114 offset:63488\n"
"ds_read_b128 a[124:127],v114 offset:64512\n"
"v_exp_f32 v4,v4\n"
"v_mfma_f32_16x16x32_bf16 v[36:39],a[92:95],a[12:15],v[96:99]\n"
"buffer_load_dwordx4 v115,s[32:35],s54 offen sc1 lds\n"
"v_mfma_f32_16x16x32_bf16 v[36:39],a[88:91],a[8:11],v[36:39]\n"
"s_add_u32 m0,s67,16384\n"
"v_exp_f32 v5,v5\n"
"v_cvt_pk_bf16_f32 v64,v0,v1\n"
"v_cvt_pk_bf16_f32 v65,v2,v3\n"
"v_mfma_f32_16x16x32_bf16 v[36:39],a[84:87],a[4:7],v[36:39]\n"
"v_exp_f32 v6,v6\n"
"v_exp_f32 v7,v7\n"
"v_mfma_f32_16x16x32_bf16 v[36:39],a[80:83],a[0:3],v[36:39]\n"
"buffer_load_dwordx4 v112,s[24:27],s64 offen offset:0 sc1 lds\n"
"s_waitcnt lgkmcnt(0)\n"
"v_mfma_f32_16x16x32_bf16 v[224:227],a[96:99],v[92:95],v[224:227]\n"
"v_exp_f32 v8,v8\n"
"v_cvt_pk_bf16_f32 v67,v6,v7\n"
"v_mfma_f32_16x16x32_bf16 a[140:143],a[144:147],v[92:95],a[140:143]\n"
"v_exp_f32 v9,v9\n"
"v_exp_f32 v10,v10\n"
"v_mfma_f32_16x16x32_bf16 v[228:231],a[100:103],v[92:95],v[228:231]\n"
"buffer_load_dwordx4 v112,s[24:27],s64 offen offset:1024 sc1 lds\n"
"v_mfma_f32_16x16x32_bf16 v[232:235],a[104:107],v[92:95],v[232:235]\n"
"v_cvt_pk_bf16_f32 v66,v4,v5\n"
"v_cvt_pk_bf16_f32 v68,v8,v9\n"
"v_mfma_f32_16x16x32_bf16 v[236:239],a[108:111],v[92:95],v[236:239]\n"
"v_exp_f32 v11,v11\n"
"v_exp_f32 v12,v12\n"
"v_mfma_f32_16x16x32_bf16 v[240:243],a[112:115],v[92:95],v[240:243]\n"
"buffer_load_dwordx4 v112,s[24:27],s64 offen offset:2048 sc1 lds\n"
"v_mfma_f32_16x16x32_bf16 v[244:247],a[116:119],v[92:95],v[244:247]\n"
"v_exp_f32 v13,v13\n"
"v_cvt_pk_bf16_f32 v69,v10,v11\n"
"v_mfma_f32_16x16x32_bf16 v[248:251],a[120:123],v[92:95],v[248:251]\n"
"v_exp_f32 v14,v14\n"
"v_cvt_pk_bf16_f32 v70,v12,v13\n"
"v_exp_f32 v15,v15\n"
"v_mfma_f32_16x16x32_bf16 v[252:255],a[124:127],v[92:95],v[252:255]\n"
"buffer_load_dwordx4 v112,s[24:27],s64 offen offset:3072 sc1 lds\n"
"v_mfma_f32_16x16x32_bf16 v[44:47],a[80:83],a[16:19],v[100:103]\n"
"s_add_u32 m0,s67,20480\n"
"v_exp_f32 v16,v16\n"
"v_cvt_pk_bf16_f32 v71,v14,v15\n"
"v_mfma_f32_16x16x32_bf16 v[44:47],a[84:87],a[20:23],v[44:47]\n"
"v_exp_f32 v17,v17\n"
"v_exp_f32 v18,v18\n"
"v_mfma_f32_16x16x32_bf16 v[44:47],a[88:91],a[24:27],v[44:47]\n"
"buffer_load_dwordx4 v117,s[24:27],s64 offen offset:0 sc1 lds\n"
"v_mfma_f32_16x16x32_bf16 v[44:47],a[92:95],a[28:31],v[44:47]\n"
"v_exp_f32 v19,v19\n"
"v_accvgpr_read_b32 v122,a140\n"
"v_mfma_f32_16x16x32_bf16 v[40:43],a[76:79],a[28:31],v[100:103]\n"
"v_cvt_pk_bf16_f32 v73,v18,v19\n"
"v_rcp_f32 v122,v122\n"
"v_mfma_f32_16x16x32_bf16 v[40:43],a[72:75],a[24:27],v[40:43]\n"
"buffer_load_dwordx4 v117,s[24:27],s64 offen offset:1024 sc1 lds\n"
"v_mfma_f32_16x16x32_bf16 v[40:43],a[68:71],a[20:23],v[40:43]\n"
"v_exp_f32 v21,v21\n"
"v_exp_f32 v20,v20\n"
"v_cvt_pk_bf16_f32 v72,v16,v17\n"
"v_pk_mul_f32 v[224:225],v[224:225],v[122:123] op_sel_hi:[1,0]\n"
"v_pk_mul_f32 v[226:227],v[226:227],v[122:123] op_sel_hi:[1,0]\n"
"v_pk_mul_f32 v[228:229],v[228:229],v[122:123] op_sel_hi:[1,0]\n"
"v_pk_mul_f32 v[230:231],v[230:231],v[122:123] op_sel_hi:[1,0]\n"
"v_mfma_f32_16x16x32_bf16 v[40:43],a[64:67],a[16:19],v[40:43]\n"
"v_cvt_pk_bf16_f32 v74,v20,v21\n"
"v_exp_f32 v22,v22\n"
"v_cvt_pk_bf16_f32 v224,v224,v225\n"
"v_exp_f32 v23,v23\n"
"v_mfma_f32_16x16x32_bf16 v[48:51],a[64:67],a[32:35],v[104:107]\n"
"buffer_load_dwordx4 v117,s[24:27],s64 offen offset:2048 sc1 lds\n"
"v_mfma_f32_16x16x32_bf16 v[48:51],a[68:71],a[36:39],v[48:51]\n"
"v_exp_f32 v24,v24\n"
"v_cvt_pk_bf16_f32 v225,v226,v227\n"
"v_mfma_f32_16x16x32_bf16 v[48:51],a[72:75],a[40:43],v[48:51]\n"
"v_exp_f32 v25,v25\n"
"v_cvt_pk_bf16_f32 v226,v228,v229\n"
"v_cvt_pk_bf16_f32 v227,v230,v231\n"
"v_mfma_f32_16x16x32_bf16 v[48:51],a[76:79],a[44:47],v[48:51]\n"
"buffer_load_dwordx4 v117,s[24:27],s64 offen offset:3072 sc1 lds\n"
"v_mfma_f32_16x16x32_bf16 v[52:55],a[92:95],a[44:47],v[104:107]\n"
"buffer_store_dwordx4 v[224:227],v112,s[24:27],s70 offen offset:0 sc1\n"
"v_exp_f32 v27,v27\n"
"v_mfma_f32_16x16x32_bf16 v[52:55],a[88:91],a[40:43],v[52:55]\n"
"s_add_u32 s64,s64,16384\n"
"v_exp_f32 v26,v26\n"
"v_exp_f32 v28,v28\n"
"v_cvt_pk_bf16_f32 v77,v26,v27\n"
"v_pk_mul_f32 v[232:233],v[232:233],v[122:123] op_sel_hi:[1,0]\n"
"v_mfma_f32_16x16x32_bf16 v[52:55],a[84:87],a[36:39],v[52:55]\n"
"v_cvt_pk_bf16_f32 v75,v22,v23\n"
"v_cvt_pk_bf16_f32 v76,v24,v25\n"
"v_exp_f32 v29,v29\n"
"v_exp_f32 v31,v31\n"
"v_pk_mul_f32 v[234:235],v[234:235],v[122:123] op_sel_hi:[1,0]\n"
"v_pk_mul_f32 v[236:237],v[236:237],v[122:123] op_sel_hi:[1,0]\n"
"v_pk_mul_f32 v[238:239],v[238:239],v[122:123] op_sel_hi:[1,0]\n"
"v_mfma_f32_16x16x32_bf16 v[52:55],a[80:83],a[32:35],v[52:55]\n"
"v_exp_f32 v30,v30\n"
"v_cvt_pk_bf16_f32 v78,v28,v29\n"
"v_mfma_f32_16x16x32_bf16 v[60:63],a[80:83],a[48:51],v[108:111]\n"
"v_cvt_pk_bf16_f32 v232,v232,v233\n"
"v_cvt_pk_bf16_f32 v233,v234,v235\n"
"v_mfma_f32_16x16x32_bf16 v[60:63],a[84:87],a[52:55],v[60:63]\n"
"buffer_store_dwordx4 a[244:247],v112,s[24:27],s56 offen offset:1024 sc1\n"
"v_cvt_pk_bf16_f32 v234,v236,v237\n"
"v_cvt_pk_bf16_f32 v235,v238,v239\n"
"v_mfma_f32_16x16x32_bf16 v[60:63],a[88:91],a[56:59],v[60:63]\n"
"buffer_store_dwordx4 v[232:235],v112,s[24:27],s70 offen offset:1024 sc1\n"
"v_pk_mul_f32 v[240:241],v[240:241],v[122:123] op_sel_hi:[1,0]\n"
"v_pk_mul_f32 v[242:243],v[242:243],v[122:123] op_sel_hi:[1,0]\n"
"v_pk_mul_f32 v[244:245],v[244:245],v[122:123] op_sel_hi:[1,0]\n"
"v_pk_mul_f32 v[246:247],v[246:247],v[122:123] op_sel_hi:[1,0]\n"
"v_mfma_f32_16x16x32_bf16 v[60:63],a[92:95],a[60:63],v[60:63]\n"
"v_cvt_pk_bf16_f32 v240,v240,v241\n"
"v_cvt_pk_bf16_f32 v241,v242,v243\n"
"v_cvt_pk_bf16_f32 v242,v244,v245\n"
"v_cvt_pk_bf16_f32 v243,v246,v247\n"
"v_mfma_f32_16x16x32_bf16 v[56:59],a[76:79],a[60:63],v[108:111]\n"
"buffer_store_dwordx4 v[240:243],v112,s[24:27],s70 offen offset:2048 sc1\n"
"v_pk_mul_f32 v[248:249],v[248:249],v[122:123] op_sel_hi:[1,0]\n"
"v_pk_mul_f32 v[250:251],v[250:251],v[122:123] op_sel_hi:[1,0]\n"
"v_mfma_f32_16x16x32_bf16 v[56:59],a[72:75],a[56:59],v[56:59]\n"
"v_cvt_pk_bf16_f32 v79,v30,v31\n"
"v_cvt_pk_bf16_f32 v248,v248,v249\n"
"v_cvt_pk_bf16_f32 v249,v250,v251\n"
"v_pk_mul_f32 v[252:253],v[252:253],v[122:123] op_sel_hi:[1,0]\n"
"v_pk_mul_f32 v[254:255],v[254:255],v[122:123] op_sel_hi:[1,0]\n"
"v_mfma_f32_16x16x32_bf16 v[56:59],a[68:71],a[52:55],v[56:59]\n"
"v_cvt_pk_bf16_f32 v250,v252,v253\n"
"v_cvt_pk_bf16_f32 v251,v254,v255\n"
"v_mfma_f32_16x16x32_bf16 v[56:59],a[64:67],a[48:51],v[56:59]\n"
"buffer_store_dwordx4 v[248:251],v112,s[24:27],s70 offen offset:3072 sc1\n"
"s_add_u32 s70,s70,262144\n"
"ds_read_b128 a[64:67],v113 offset:49152\n"
"ds_read_b128 a[68:71],v113 offset:50176\n"
"ds_read_b128 a[72:75],v113 offset:51200\n"
"ds_read_b128 a[76:79],v113 offset:52224\n"
"ds_read_b128 a[80:83],v113 offset:53248\n"
"ds_read_b128 a[84:87],v113 offset:54272\n"
"ds_read_b128 a[88:91],v113 offset:55296\n"
"ds_read_b128 a[92:95],v113 offset:56320\n"
"v_mov_b32 v114,v112\n"
"s_mov_b32 s70,s68\n"
"s_waitcnt vmcnt(25)\n"
"ds_add_u32 v124,a152\n"
"ds_read_b32 v125,v124\n"
"s_waitcnt lgkmcnt(0)\n"
"v_cmp_gt_u32 vcc,s101,v125\n"
"s_nop 0\n"
"s_cbranch_vccnz .Lfk_cs_mv0\n"
".Lfk_cr_mv0:\n"
"v_mfma_f32_16x16x32_bf16 v[0:3],a[64:67],a[0:3],v[96:99]\n"
"s_add_u32 s101,s101,4\n"
"v_exp_f32 v122,v32\n"
"v_mfma_f32_16x16x32_bf16 v[0:3],a[68:71],a[4:7],v[0:3]\n"
"s_add_u32 m0,s55,8192\n"
"v_exp_f32 v123,v33\n"
"v_mfma_f32_16x16x32_bf16 v[0:3],a[72:75],a[8:11],v[0:3]\n"
"ds_read_b128 a[96:99],v114 offset:40960\n"
"ds_read_b128 a[100:103],v114 offset:41984\n"
"v_mfma_f32_16x16x32_bf16 v[0:3],a[76:79],a[12:15],v[0:3]\n"
"s_add_u32 s54,s54,s22\n"
"v_exp_f32 v119,v34\n"
"v_mfma_f32_16x16x32_bf16 v[4:7],a[92:95],a[12:15],v[96:99]\n"
"ds_read_b128 a[104:107],v114 offset:43008\n"
"v_cvt_pk_bf16_f32 v80,v122,v123\n"
"v_mfma_f32_16x16x32_bf16 v[4:7],a[88:91],a[8:11],v[4:7]\n"
"v_exp_f32 v120,v35\n"
"v_mfma_f32_16x16x32_bf16 v[4:7],a[84:87],a[4:7],v[4:7]\n"
"ds_read_b128 a[108:111],v114 offset:44032\n"
"ds_read_b128 a[112:115],v114 offset:45056\n"
"v_mfma_f32_16x16x32_bf16 v[4:7],a[80:83],a[0:3],v[4:7]\n"
"v_exp_f32 v122,v36\n"
"v_mfma_f32_16x16x32_bf16 v[12:15],a[80:83],a[16:19],v[100:103]\n"
"ds_read_b128 a[116:119],v114 offset:46080\n"
"v_cvt_pk_bf16_f32 v81,v119,v120\n"
"v_mfma_f32_16x16x32_bf16 v[12:15],a[84:87],a[20:23],v[12:15]\n"
"v_exp_f32 v123,v37\n"
"v_mfma_f32_16x16x32_bf16 v[12:15],a[88:91],a[24:27],v[12:15]\n"
"ds_read_b128 a[120:123],v114 offset:47104\n"
"ds_read_b128 a[124:127],v114 offset:48128\n"
"v_mfma_f32_16x16x32_bf16 v[12:15],a[92:95],a[28:31],v[12:15]\n"
"v_exp_f32 v119,v38\n"
"v_mfma_f32_16x16x32_bf16 v[8:11],a[76:79],a[28:31],v[100:103]\n"
"v_cvt_pk_bf16_f32 v82,v122,v123\n"
"v_or3_b32 v118,v80,v81,v82\n"
"v_mfma_f32_16x16x32_bf16 v[8:11],a[72:75],a[24:27],v[8:11]\n"
"v_exp_f32 v120,v39\n"
"v_mfma_f32_16x16x32_bf16 v[8:11],a[68:71],a[20:23],v[8:11]\n"
"buffer_load_dwordx4 v115,s[32:35],s54 offen sc1 lds\n"
"v_mfma_f32_16x16x32_bf16 v[8:11],a[64:67],a[16:19],v[8:11]\n"
"s_add_u32 m0,s67,49152\n"
"v_exp_f32 v122,v40\n"
"v_mfma_f32_16x16x32_bf16 v[16:19],a[64:67],a[32:35],v[104:107]\n"
"v_cvt_pk_bf16_f32 v83,v119,v120\n"
"v_mfma_f32_16x16x32_bf16 v[16:19],a[68:71],a[36:39],v[16:19]\n"
"v_exp_f32 v123,v41\n"
"v_mfma_f32_16x16x32_bf16 v[16:19],a[72:75],a[40:43],v[16:19]\n"
"buffer_load_dwordx4 v112,s[24:27],s64 offen offset:0 sc1 lds\n"
"v_mfma_f32_16x16x32_bf16 v[16:19],a[76:79],a[44:47],v[16:19]\n"
"v_exp_f32 v119,v42\n"
"v_mfma_f32_16x16x32_bf16 v[20:23],a[92:95],a[44:47],v[104:107]\n"
"v_cvt_pk_bf16_f32 v84,v122,v123\n"
"v_or3_b32 v118,v118,v83,v84\n"
"v_mfma_f32_16x16x32_bf16 v[20:23],a[88:91],a[40:43],v[20:23]\n"
"v_exp_f32 v120,v43\n"
"v_mfma_f32_16x16x32_bf16 v[20:23],a[84:87],a[36:39],v[20:23]\n"
"buffer_load_dwordx4 v112,s[24:27],s64 offen offset:1024 sc1 lds\n"
"v_mfma_f32_16x16x32_bf16 v[20:23],a[80:83],a[32:35],v[20:23]\n"
"v_exp_f32 v122,v44\n"
"v_mfma_f32_16x16x32_bf16 v[28:31],a[80:83],a[48:51],v[108:111]\n"
"v_cvt_pk_bf16_f32 v85,v119,v120\n"
"v_mfma_f32_16x16x32_bf16 v[28:31],a[84:87],a[52:55],v[28:31]\n"
"v_exp_f32 v123,v45\n"
"v_mfma_f32_16x16x32_bf16 v[28:31],a[88:91],a[56:59],v[28:31]\n"
"buffer_load_dwordx4 v112,s[24:27],s64 offen offset:2048 sc1 lds\n"
"v_mfma_f32_16x16x32_bf16 v[28:31],a[92:95],a[60:63],v[28:31]\n"
"v_exp_f32 v119,v46\n"
"v_mfma_f32_16x16x32_bf16 v[24:27],a[76:79],a[60:63],v[108:111]\n"
"v_cvt_pk_bf16_f32 v86,v122,v123\n"
"v_or3_b32 v118,v118,v85,v86\n"
"v_mfma_f32_16x16x32_bf16 v[24:27],a[72:75],a[56:59],v[24:27]\n"
"v_exp_f32 v120,v47\n"
"v_mfma_f32_16x16x32_bf16 v[24:27],a[68:71],a[52:55],v[24:27]\n"
"buffer_load_dwordx4 v112,s[24:27],s64 offen offset:3072 sc1 lds\n"
"v_mfma_f32_16x16x32_bf16 v[24:27],a[64:67],a[48:51],v[24:27]\n"
"s_waitcnt lgkmcnt(0)\n"
"v_exp_f32 v122,v48\n"
"v_mfma_f32_16x16x32_bf16 v[128:131],a[96:99],v[64:67],0\n"
"v_exp_f32 v123,v49\n"
"v_mfma_f32_16x16x32_bf16 v[160:163],a[96:99],v[68:71],0\n"
"ds_read_b128 a[64:67],v113 offset:32768\n"
"v_cvt_pk_bf16_f32 v87,v119,v120\n"
"v_mfma_f32_16x16x32_bf16 v[192:195],a[96:99],v[72:75],0\n"
"s_add_u32 m0,s67,53248\n"
"v_exp_f32 v119,v50\n"
"v_mfma_f32_16x16x32_bf16 v[224:227],a[96:99],v[76:79],0\n"
"ds_read_b128 a[68:71],v113 offset:33792\n"
"v_cvt_pk_bf16_f32 v88,v122,v123\n"
"v_mfma_f32_16x16x32_bf16 a[140:143],a[144:147],v[76:79],0\n"
"v_exp_f32 v120,v51\n"
"v_mfma_f32_16x16x32_bf16 v[228:231],a[100:103],v[76:79],0\n"
"ds_read_b128 a[72:75],v113 offset:34816\n"
"ds_read_b128 a[76:79],v113 offset:35840\n"
"v_mfma_f32_16x16x32_bf16 v[132:135],a[100:103],v[64:67],0\n"
"v_exp_f32 v122,v52\n"
"v_mfma_f32_16x16x32_bf16 v[164:167],a[100:103],v[68:71],0\n"
"ds_read_b128 a[80:83],v113 offset:36864\n"
"v_cvt_pk_bf16_f32 v89,v119,v120\n"
"v_mfma_f32_16x16x32_bf16 v[196:199],a[100:103],v[72:75],0\n"
"v_exp_f32 v123,v53\n"
"v_mfma_f32_16x16x32_bf16 a[136:139],a[144:147],v[72:75],0\n"
"ds_read_b128 a[84:87],v113 offset:37888\n"
"ds_read_b128 a[88:91],v113 offset:38912\n"
"v_mfma_f32_16x16x32_bf16 v[200:203],a[104:107],v[72:75],0\n"
"v_exp_f32 v119,v54\n"
"v_mfma_f32_16x16x32_bf16 v[232:235],a[104:107],v[76:79],0\n"
"ds_read_b128 a[92:95],v113 offset:39936\n"
"v_cvt_pk_bf16_f32 v90,v122,v123\n"
"v_mfma_f32_16x16x32_bf16 v[136:139],a[104:107],v[64:67],0\n"
"v_exp_f32 v120,v55\n"
"v_mfma_f32_16x16x32_bf16 v[168:171],a[104:107],v[68:71],0\n"
"s_waitcnt vmcnt(16)\n"
"ds_add_u32 v124,a152\n"
"v_mfma_f32_16x16x32_bf16 a[132:135],a[144:147],v[68:71],0\n"
"v_exp_f32 v122,v56\n"
"v_mfma_f32_16x16x32_bf16 v[172:175],a[108:111],v[68:71],0\n"
"v_cvt_pk_bf16_f32 v91,v119,v120\n"
"v_or3_b32 v118,v118,v87,v88\n"
"v_mfma_f32_16x16x32_bf16 v[204:207],a[108:111],v[72:75],0\n"
"v_exp_f32 v123,v57\n"
"v_mfma_f32_16x16x32_bf16 v[236:239],a[108:111],v[76:79],0\n"
"buffer_load_dwordx4 v117,s[24:27],s64 offen offset:0 sc1 lds\n"
"v_mfma_f32_16x16x32_bf16 v[140:143],a[108:111],v[64:67],0\n"
"v_exp_f32 v119,v58\n"
"v_mfma_f32_16x16x32_bf16 a[128:131],a[144:147],v[64:67],0\n"
"v_cvt_pk_bf16_f32 v92,v122,v123\n"
"v_or3_b32 v118,v118,v89,v90\n"
"v_mfma_f32_16x16x32_bf16 v[144:147],a[112:115],v[64:67],0\n"
"v_exp_f32 v120,v59\n"
"v_mfma_f32_16x16x32_bf16 v[176:179],a[112:115],v[68:71],0\n"
"buffer_load_dwordx4 v117,s[24:27],s64 offen offset:1024 sc1 lds\n"
"v_mfma_f32_16x16x32_bf16 v[208:211],a[112:115],v[72:75],0\n"
"v_exp_f32 v122,v60\n"
"v_mfma_f32_16x16x32_bf16 v[240:243],a[112:115],v[76:79],0\n"
"v_cvt_pk_bf16_f32 v93,v119,v120\n"
"v_or3_b32 v118,v118,v91,v92\n"
"v_mfma_f32_16x16x32_bf16 v[244:247],a[116:119],v[76:79],0\n"
"v_exp_f32 v123,v61\n"
"v_mfma_f32_16x16x32_bf16 v[148:151],a[116:119],v[64:67],0\n"
"buffer_load_dwordx4 v117,s[24:27],s64 offen offset:2048 sc1 lds\n"
"v_mfma_f32_16x16x32_bf16 v[180:183],a[116:119],v[68:71],0\n"
"v_exp_f32 v119,v62\n"
"v_mfma_f32_16x16x32_bf16 v[212:215],a[116:119],v[72:75],0\n"
"ds_read_b32 v125,v124\n"
"v_cvt_pk_bf16_f32 v94,v122,v123\n"
"v_mfma_f32_16x16x32_bf16 v[216:219],a[120:123],v[72:75],0\n"
"v_exp_f32 v120,v63\n"
"v_mfma_f32_16x16x32_bf16 v[248:251],a[120:123],v[76:79],0\n"
"buffer_load_dwordx4 v117,s[24:27],s64 offen offset:3072 sc1 lds\n"
"v_mfma_f32_16x16x32_bf16 v[152:155],a[120:123],v[64:67],0\n"
"buffer_store_dwordx4 a[248:251],v112,s[24:27],s56 offen offset:2048 sc1\n"
"v_mfma_f32_16x16x32_bf16 v[184:187],a[120:123],v[68:71],0\n"
"s_add_u32 s64,s64,16384\n"
"v_cvt_pk_bf16_f32 v95,v119,v120\n"
"v_or3_b32 v118,v118,v93,v94\n"
"v_mfma_f32_16x16x32_bf16 v[188:191],a[124:127],v[68:71],0\n"
"s_waitcnt lgkmcnt(0)\n"
"v_subrev_u32 v125,s101,v125\n"
"v_or3_b32 v125,v118,v95,v125\n"
"v_mfma_f32_16x16x32_bf16 v[220:223],a[124:127],v[72:75],0\n"
"v_and_b32 v125,0x40004000,v125\n"
"v_cmp_ne_u32 vcc,0,v125\n"
"v_mfma_f32_16x16x32_bf16 v[252:255],a[124:127],v[76:79],0\n"
"v_mfma_f32_16x16x32_bf16 v[156:159],a[124:127],v[64:67],0\n"
"s_cbranch_vccnz .Lfk_cs_v1\n"
".Lfk_cr_v1:\n"
"v_mfma_f32_16x16x32_bf16 v[32:35],a[64:67],a[0:3],v[96:99]\n"
"s_add_u32 s101,s101,4\n"
"v_exp_f32 v122,v0\n"
"v_mfma_f32_16x16x32_bf16 v[32:35],a[68:71],a[4:7],v[32:35]\n"
"s_add_u32 m0,s55,12288\n"
"v_exp_f32 v123,v1\n"
"v_mfma_f32_16x16x32_bf16 v[32:35],a[72:75],a[8:11],v[32:35]\n"
"ds_read_b128 a[96:99],v114 offset:8192\n"
"ds_read_b128 a[100:103],v114 offset:9216\n"
"v_mfma_f32_16x16x32_bf16 v[32:35],a[76:79],a[12:15],v[32:35]\n"
"s_add_u32 s54,s54,s22\n"
"v_exp_f32 v119,v2\n"
"v_mfma_f32_16x16x32_bf16 v[36:39],a[92:95],a[12:15],v[96:99]\n"
"ds_read_b128 a[104:107],v114 offset:10240\n"
"v_cvt_pk_bf16_f32 v64,v122,v123\n"
"v_mfma_f32_16x16x32_bf16 v[36:39],a[88:91],a[8:11],v[36:39]\n"
"v_exp_f32 v120,v3\n"
"v_mfma_f32_16x16x32_bf16 v[36:39],a[84:87],a[4:7],v[36:39]\n"
"ds_read_b128 a[108:111],v114 offset:11264\n"
"ds_read_b128 a[112:115],v114 offset:12288\n"
"v_mfma_f32_16x16x32_bf16 v[36:39],a[80:83],a[0:3],v[36:39]\n"
"v_exp_f32 v122,v4\n"
"v_mfma_f32_16x16x32_bf16 v[44:47],a[80:83],a[16:19],v[100:103]\n"
"ds_read_b128 a[116:119],v114 offset:13312\n"
"v_cvt_pk_bf16_f32 v65,v119,v120\n"
"v_mfma_f32_16x16x32_bf16 v[44:47],a[84:87],a[20:23],v[44:47]\n"
"v_exp_f32 v123,v5\n"
"v_mfma_f32_16x16x32_bf16 v[44:47],a[88:91],a[24:27],v[44:47]\n"
"ds_read_b128 a[120:123],v114 offset:14336\n"
"ds_read_b128 a[124:127],v114 offset:15360\n"
"v_mfma_f32_16x16x32_bf16 v[44:47],a[92:95],a[28:31],v[44:47]\n"
"v_exp_f32 v119,v6\n"
"v_mfma_f32_16x16x32_bf16 v[40:43],a[76:79],a[28:31],v[100:103]\n"
"v_cvt_pk_bf16_f32 v66,v122,v123\n"
"v_or3_b32 v118,v64,v65,v66\n"
"v_mfma_f32_16x16x32_bf16 v[40:43],a[72:75],a[24:27],v[40:43]\n"
"v_exp_f32 v120,v7\n"
"v_mfma_f32_16x16x32_bf16 v[40:43],a[68:71],a[20:23],v[40:43]\n"
"buffer_load_dwordx4 v115,s[32:35],s54 offen sc1 lds\n"
"v_mfma_f32_16x16x32_bf16 v[40:43],a[64:67],a[16:19],v[40:43]\n"
"s_add_u32 m0,s67,32768\n"
"v_exp_f32 v122,v8\n"
"v_mfma_f32_16x16x32_bf16 v[48:51],a[64:67],a[32:35],v[104:107]\n"
"v_cvt_pk_bf16_f32 v67,v119,v120\n"
"v_mfma_f32_16x16x32_bf16 v[48:51],a[68:71],a[36:39],v[48:51]\n"
"v_exp_f32 v123,v9\n"
"v_mfma_f32_16x16x32_bf16 v[48:51],a[72:75],a[40:43],v[48:51]\n"
"buffer_load_dwordx4 v112,s[24:27],s64 offen offset:0 sc1 lds\n"
"v_mfma_f32_16x16x32_bf16 v[48:51],a[76:79],a[44:47],v[48:51]\n"
"v_exp_f32 v119,v10\n"
"v_mfma_f32_16x16x32_bf16 v[52:55],a[92:95],a[44:47],v[104:107]\n"
"v_cvt_pk_bf16_f32 v68,v122,v123\n"
"v_or3_b32 v118,v118,v67,v68\n"
"v_mfma_f32_16x16x32_bf16 v[52:55],a[88:91],a[40:43],v[52:55]\n"
"v_exp_f32 v120,v11\n"
"v_mfma_f32_16x16x32_bf16 v[52:55],a[84:87],a[36:39],v[52:55]\n"
"buffer_load_dwordx4 v112,s[24:27],s64 offen offset:1024 sc1 lds\n"
"v_mfma_f32_16x16x32_bf16 v[52:55],a[80:83],a[32:35],v[52:55]\n"
"v_exp_f32 v122,v12\n"
"v_mfma_f32_16x16x32_bf16 v[60:63],a[80:83],a[48:51],v[108:111]\n"
"v_cvt_pk_bf16_f32 v69,v119,v120\n"
"v_mfma_f32_16x16x32_bf16 v[60:63],a[84:87],a[52:55],v[60:63]\n"
"v_exp_f32 v123,v13\n"
"v_mfma_f32_16x16x32_bf16 v[60:63],a[88:91],a[56:59],v[60:63]\n"
"buffer_load_dwordx4 v112,s[24:27],s64 offen offset:2048 sc1 lds\n"
"v_mfma_f32_16x16x32_bf16 v[60:63],a[92:95],a[60:63],v[60:63]\n"
"v_exp_f32 v119,v14\n"
"v_mfma_f32_16x16x32_bf16 v[56:59],a[76:79],a[60:63],v[108:111]\n"
"v_cvt_pk_bf16_f32 v70,v122,v123\n"
"v_or3_b32 v118,v118,v69,v70\n"
"v_mfma_f32_16x16x32_bf16 v[56:59],a[72:75],a[56:59],v[56:59]\n"
"v_exp_f32 v120,v15\n"
"v_mfma_f32_16x16x32_bf16 v[56:59],a[68:71],a[52:55],v[56:59]\n"
"buffer_load_dwordx4 v112,s[24:27],s64 offen offset:3072 sc1 lds\n"
"v_mfma_f32_16x16x32_bf16 v[56:59],a[64:67],a[48:51],v[56:59]\n"
"s_waitcnt lgkmcnt(0)\n"
"v_exp_f32 v122,v16\n"
"v_mfma_f32_16x16x32_bf16 v[128:131],a[96:99],v[80:83],v[128:131]\n"
"v_exp_f32 v123,v17\n"
"v_mfma_f32_16x16x32_bf16 v[160:163],a[96:99],v[84:87],v[160:163]\n"
"ds_read_b128 a[64:67],v113 offset:0\n"
"v_cvt_pk_bf16_f32 v71,v119,v120\n"
"v_mfma_f32_16x16x32_bf16 v[192:195],a[96:99],v[88:91],v[192:195]\n"
"s_add_u32 m0,s67,36864\n"
"v_exp_f32 v119,v18\n"
"v_mfma_f32_16x16x32_bf16 v[224:227],a[96:99],v[92:95],v[224:227]\n"
"ds_read_b128 a[68:71],v113 offset:1024\n"
"v_cvt_pk_bf16_f32 v72,v122,v123\n"
"v_mfma_f32_16x16x32_bf16 a[140:143],a[144:147],v[92:95],a[140:143]\n"
"v_exp_f32 v120,v19\n"
"v_mfma_f32_16x16x32_bf16 v[228:231],a[100:103],v[92:95],v[228:231]\n"
"ds_read_b128 a[72:75],v113 offset:2048\n"
"ds_read_b128 a[76:79],v113 offset:3072\n"
"v_mfma_f32_16x16x32_bf16 v[132:135],a[100:103],v[80:83],v[132:135]\n"
"v_exp_f32 v122,v20\n"
"v_mfma_f32_16x16x32_bf16 v[164:167],a[100:103],v[84:87],v[164:167]\n"
"ds_read_b128 a[80:83],v113 offset:4096\n"
"v_cvt_pk_bf16_f32 v73,v119,v120\n"
"v_mfma_f32_16x16x32_bf16 v[196:199],a[100:103],v[88:91],v[196:199]\n"
"v_exp_f32 v123,v21\n"
"v_mfma_f32_16x16x32_bf16 a[136:139],a[144:147],v[88:91],a[136:139]\n"
"ds_read_b128 a[84:87],v113 offset:5120\n"
"ds_read_b128 a[88:91],v113 offset:6144\n"
"v_mfma_f32_16x16x32_bf16 v[200:203],a[104:107],v[88:91],v[200:203]\n"
"v_exp_f32 v119,v22\n"
"v_mfma_f32_16x16x32_bf16 v[232:235],a[104:107],v[92:95],v[232:235]\n"
"ds_read_b128 a[92:95],v113 offset:7168\n"
"v_cvt_pk_bf16_f32 v74,v122,v123\n"
"v_mfma_f32_16x16x32_bf16 v[136:139],a[104:107],v[80:83],v[136:139]\n"
"v_exp_f32 v120,v23\n"
"v_mfma_f32_16x16x32_bf16 v[168:171],a[104:107],v[84:87],v[168:171]\n"
"s_waitcnt vmcnt(16)\n"
"ds_add_u32 v124,a152\n"
"v_mfma_f32_16x16x32_bf16 a[132:135],a[144:147],v[84:87],a[132:135]\n"
"v_exp_f32 v122,v24\n"
"v_mfma_f32_16x16x32_bf16 v[172:175],a[108:111],v[84:87],v[172:175]\n"
"v_cvt_pk_bf16_f32 v75,v119,v120\n"
"v_or3_b32 v118,v118,v71,v72\n"
"v_mfma_f32_16x16x32_bf16 v[204:207],a[108:111],v[88:91],v[204:207]\n"
"v_exp_f32 v123,v25\n"
"v_mfma_f32_16x16x32_bf16 v[236:239],a[108:111],v[92:95],v[236:239]\n"
"buffer_load_dwordx4 v117,s[24:27],s64 offen offset:0 sc1 lds\n"
"v_mfma_f32_16x16x32_bf16 v[140:143],a[108:111],v[80:83],v[140:143]\n"
"v_exp_f32 v119,v26\n"
"v_mfma_f32_16x16x32_bf16 a[128:131],a[144:147],v[80:83],a[128:131]\n"
"v_cvt_pk_bf16_f32 v76,v122,v123\n"
"v_or3_b32 v118,v118,v73,v74\n"
"v_mfma_f32_16x16x32_bf16 v[144:147],a[112:115],v[80:83],v[144:147]\n"
"v_exp_f32 v120,v27\n"
"v_mfma_f32_16x16x32_bf16 v[176:179],a[112:115],v[84:87],v[176:179]\n"
"buffer_load_dwordx4 v117,s[24:27],s64 offen offset:1024 sc1 lds\n"
"v_mfma_f32_16x16x32_bf16 v[208:211],a[112:115],v[88:91],v[208:211]\n"
"v_exp_f32 v122,v28\n"
"v_mfma_f32_16x16x32_bf16 v[240:243],a[112:115],v[92:95],v[240:243]\n"
"v_cvt_pk_bf16_f32 v77,v119,v120\n"
"v_or3_b32 v118,v118,v75,v76\n"
"v_mfma_f32_16x16x32_bf16 v[244:247],a[116:119],v[92:95],v[244:247]\n"
"v_exp_f32 v123,v29\n"
"v_mfma_f32_16x16x32_bf16 v[148:151],a[116:119],v[80:83],v[148:151]\n"
"buffer_load_dwordx4 v117,s[24:27],s64 offen offset:2048 sc1 lds\n"
"v_mfma_f32_16x16x32_bf16 v[180:183],a[116:119],v[84:87],v[180:183]\n"
"v_exp_f32 v119,v30\n"
"v_mfma_f32_16x16x32_bf16 v[212:215],a[116:119],v[88:91],v[212:215]\n"
"ds_read_b32 v125,v124\n"
"v_cvt_pk_bf16_f32 v78,v122,v123\n"
"v_mfma_f32_16x16x32_bf16 v[216:219],a[120:123],v[88:91],v[216:219]\n"
"v_exp_f32 v120,v31\n"
"v_mfma_f32_16x16x32_bf16 v[248:251],a[120:123],v[92:95],v[248:251]\n"
"buffer_load_dwordx4 v117,s[24:27],s64 offen offset:3072 sc1 lds\n"
"v_mfma_f32_16x16x32_bf16 v[152:155],a[120:123],v[80:83],v[152:155]\n"
"buffer_store_dwordx4 a[252:255],v112,s[24:27],s56 offen offset:3072 sc1\n"
"v_mfma_f32_16x16x32_bf16 v[184:187],a[120:123],v[84:87],v[184:187]\n"
"s_add_u32 s64,s64,16384\n"
"s_add_u32 s89,s89,1\n"
"v_cvt_pk_bf16_f32 v79,v119,v120\n"
"v_mfma_f32_16x16x32_bf16 v[188:191],a[124:127],v[84:87],v[188:191]\n"
"s_waitcnt lgkmcnt(0)\n"
"v_subrev_u32 v125,s101,v125\n"
"v_or3_b32 v118,v118,v77,v78\n"
"v_mfma_f32_16x16x32_bf16 v[220:223],a[124:127],v[88:91],v[220:223]\n"
"v_or3_b32 v125,v118,v79,v125\n"
"v_and_b32 v125,0x40004000,v125\n"
"v_mfma_f32_16x16x32_bf16 v[252:255],a[124:127],v[92:95],v[252:255]\n"
"v_cmp_ne_u32 vcc,0,v125\n"
"v_mfma_f32_16x16x32_bf16 v[156:159],a[124:127],v[80:83],v[156:159]\n"
"s_cbranch_vccnz .Lfk_cs_v2\n"
".Lfk_cr_v2:\n"
"s_waitcnt vmcnt(20)\n"
"buffer_store_dwordx2 a[154:155],off,s[24:27],s69 sc1\n"
"s_branch .Lfk_Hx\n"
".Lfk_tailB:\n"
"s_waitcnt vmcnt(11) lgkmcnt(0)\n"
"s_barrier\n"
"v_mfma_f32_16x16x32_bf16 v[0:3],a[64:67],a[0:3],v[96:99]\n"
"ds_read_b128 a[96:99],v114 offset:24576\n"
"ds_read_b128 a[100:103],v114 offset:25600\n"
"ds_read_b128 a[104:107],v114 offset:26624\n"
"v_mfma_f32_16x16x32_bf16 v[0:3],a[68:71],a[4:7],v[0:3]\n"
"ds_read_b128 a[108:111],v114 offset:27648\n"
"ds_read_b128 a[112:115],v114 offset:28672\n"
"ds_read_b128 a[116:119],v114 offset:29696\n"
"v_mfma_f32_16x16x32_bf16 v[0:3],a[72:75],a[8:11],v[0:3]\n"
"ds_read_b128 a[120:123],v114 offset:30720\n"
"ds_read_b128 a[124:127],v114 offset:31744\n"
"v_add_f32 v16,0x80000000,v126\n"
"v_mfma_f32_16x16x32_bf16 v[0:3],a[76:79],a[12:15],v[0:3]\n"
"v_min_f32 v16,v16,v104\n"
"v_add_f32 v17,0xf149f2ca,v126\n"
"v_min_f32 v17,v17,v105\n"
"v_mfma_f32_16x16x32_bf16 v[4:7],a[92:95],a[12:15],v[96:99]\n"
"v_add_f32 v18,0xf1c9f2ca,v126\n"
"v_min_f32 v18,v18,v106\n"
"v_add_f32 v19,0xf2177617,v126\n"
"v_mfma_f32_16x16x32_bf16 v[4:7],a[88:91],a[8:11],v[4:7]\n"
"v_min_f32 v19,v19,v107\n"
"v_add_f32 v20,0xf349f2ca,v126\n"
"v_min_f32 v20,v20,v104\n"
"v_mfma_f32_16x16x32_bf16 v[4:7],a[84:87],a[4:7],v[4:7]\n"
"v_add_f32 v21,0xf35691f6,v126\n"
"v_min_f32 v21,v21,v105\n"
"v_add_f32 v22,0xf3633123,v126\n"
"v_mfma_f32_16x16x32_bf16 v[4:7],a[80:83],a[0:3],v[4:7]\n"
"v_min_f32 v22,v22,v106\n"
"v_add_f32 v23,0xf36fd050,v126\n"
"v_min_f32 v23,v23,v107\n"
"v_mfma_f32_16x16x32_bf16 v[12:15],a[80:83],a[16:19],v[100:103]\n"
"v_exp_f32 v32,v32\n"
"v_max3_f32 v118,v0,v1,v2\n"
"v_mfma_f32_16x16x32_bf16 v[12:15],a[84:87],a[20:23],v[12:15]\n"
"v_exp_f32 v33,v33\n"
"v_mfma_f32_16x16x32_bf16 v[12:15],a[88:91],a[24:27],v[12:15]\n"
"v_exp_f32 v34,v34\n"
"v_cvt_pk_bf16_f32 v80,v32,v33\n"
"v_mfma_f32_16x16x32_bf16 v[12:15],a[92:95],a[28:31],v[12:15]\n"
"v_exp_f32 v35,v35\n"
"v_max3_f32 v118,v118,v3,v4\n"
"v_mfma_f32_16x16x32_bf16 v[8:11],a[76:79],a[28:31],v[100:103]\n"
"v_exp_f32 v36,v36\n"
"v_cvt_pk_bf16_f32 v81,v34,v35\n"
"v_mfma_f32_16x16x32_bf16 v[8:11],a[72:75],a[24:27],v[8:11]\n"
"v_exp_f32 v37,v37\n"
"v_max3_f32 v118,v118,v5,v6\n"
"v_mfma_f32_16x16x32_bf16 v[8:11],a[68:71],a[20:23],v[8:11]\n"
"v_exp_f32 v38,v38\n"
"v_mfma_f32_16x16x32_bf16 v[8:11],a[64:67],a[16:19],v[8:11]\n"
"v_exp_f32 v39,v39\n"
"v_max3_f32 v118,v118,v7,v12\n"
"v_mfma_f32_16x16x32_bf16 v[24:27],a[64:67],a[48:51],v[108:111]\n"
"v_exp_f32 v40,v40\n"
"v_mfma_f32_16x16x32_bf16 v[24:27],a[68:71],a[52:55],v[24:27]\n"
"v_exp_f32 v41,v41\n"
"v_max3_f32 v118,v118,v13,v14\n"
"v_mfma_f32_16x16x32_bf16 v[24:27],a[72:75],a[56:59],v[24:27]\n"
"v_cvt_pk_bf16_f32 v82,v36,v37\n"
"v_cvt_pk_bf16_f32 v84,v40,v41\n"
"v_mfma_f32_16x16x32_bf16 v[24:27],a[76:79],a[60:63],v[24:27]\n"
"v_exp_f32 v42,v42\n"
"v_max3_f32 v118,v118,v15,v8\n"
"v_mfma_f32_16x16x32_bf16 v[28:31],a[92:95],a[60:63],v[108:111]\n"
"v_cvt_pk_bf16_f32 v83,v38,v39\n"
"v_max3_f32 v118,v118,v9,v10\n"
"v_mfma_f32_16x16x32_bf16 v[28:31],a[88:91],a[56:59],v[28:31]\n"
"v_exp_f32 v43,v43\n"
"v_mfma_f32_16x16x32_bf16 v[28:31],a[84:87],a[52:55],v[28:31]\n"
"v_cvt_pk_bf16_f32 v85,v42,v43\n"
"v_mfma_f32_16x16x32_bf16 v[28:31],a[80:83],a[48:51],v[28:31]\n"
"v_exp_f32 v44,v44\n"
"v_mfma_f32_16x16x32_bf16 v[20:23],a[80:83],a[32:35],v[20:23]\n"
"v_max3_f32 v118,v118,v11,v24\n"
"v_max3_f32 v118,v118,v25,v26\n"
"v_mfma_f32_16x16x32_bf16 v[20:23],a[84:87],a[36:39],v[20:23]\n"
"v_exp_f32 v45,v45\n"
"v_mfma_f32_16x16x32_bf16 v[20:23],a[88:91],a[40:43],v[20:23]\n"
"v_cvt_pk_bf16_f32 v86,v44,v45\n"
"v_mfma_f32_16x16x32_bf16 v[20:23],a[92:95],a[44:47],v[20:23]\n"
"v_exp_f32 v46,v46\n"
"v_mfma_f32_16x16x32_bf16 v[16:19],a[76:79],a[44:47],v[16:19]\n"
"v_max3_f32 v118,v118,v27,v28\n"
"v_max3_f32 v118,v118,v29,v30\n"
"v_mfma_f32_16x16x32_bf16 v[16:19],a[72:75],a[40:43],v[16:19]\n"
"v_exp_f32 v47,v47\n"
"v_mfma_f32_16x16x32_bf16 v[16:19],a[68:71],a[36:39],v[16:19]\n"
"v_cvt_pk_bf16_f32 v87,v46,v47\n"
"v_exp_f32 v48,v48\n"
"v_mfma_f32_16x16x32_bf16 v[16:19],a[64:67],a[32:35],v[16:19]\n"
"v_max3_f32 v118,v118,v31,v20\n"
"s_waitcnt lgkmcnt(0)\n"
"v_mfma_f32_16x16x32_bf16 v[128:131],a[96:99],v[64:67],v[128:131]\n"
"v_exp_f32 v49,v49\n"
"v_max3_f32 v118,v118,v21,v22\n"
"v_cvt_pk_bf16_f32 v88,v48,v49\n"
"v_mfma_f32_16x16x32_bf16 v[160:163],a[96:99],v[68:71],v[160:163]\n"
"ds_read_b128 a[64:67],v114 offset:57344\n"
"ds_read_b128 a[68:71],v114 offset:58368\n"
"v_mfma_f32_16x16x32_bf16 v[192:195],a[96:99],v[72:75],v[192:195]\n"
"ds_read_b128 a[72:75],v114 offset:59392\n"
"v_exp_f32 v50,v50\n"
"v_mfma_f32_16x16x32_bf16 v[224:227],a[96:99],v[76:79],v[224:227]\n"
"v_max3_f32 v118,v118,v23,v16\n"
"v_max3_f32 v118,v118,v17,v18\n"
"v_max_f32 v118,v118,v19\n"
"v_mfma_f32_16x16x32_bf16 a[140:143],a[144:147],v[76:79],a[140:143]\n"
"v_exp_f32 v51,v51\n"
"v_cmp_lt_f32 vcc,s93,v118\n"
"v_cvt_pk_bf16_f32 v89,v50,v51\n"
"v_mfma_f32_16x16x32_bf16 v[228:231],a[100:103],v[76:79],v[228:231]\n"
"ds_read_b128 a[76:79],v114 offset:60416\n"
"ds_read_b128 a[80:83],v114 offset:61440\n"
"v_mfma_f32_16x16x32_bf16 v[132:135],a[100:103],v[64:67],v[132:135]\n"
"ds_read_b128 a[84:87],v114 offset:62464\n"
"v_exp_f32 v52,v52\n"
"v_mfma_f32_16x16x32_bf16 v[164:167],a[100:103],v[68:71],v[164:167]\n"
"s_sub_u32 s82,s3,32768\n"
"ds_read_b128 a[88:91],v114 offset:63488\n"
"ds_read_b128 a[92:95],v114 offset:64512\n"
"v_mfma_f32_16x16x32_bf16 v[196:199],a[100:103],v[72:75],v[196:199]\n"
"buffer_load_dwordx4 a[0:3],v112,s[24:27],s82 offen offset:0 sc1\n"
"v_exp_f32 v53,v53\n"
"v_mfma_f32_16x16x32_bf16 a[136:139],a[144:147],v[72:75],a[136:139]\n"
"buffer_load_dwordx4 a[4:7],v112,s[24:27],s82 offen offset:1024 sc1\n"
"buffer_load_dwordx4 a[8:11],v112,s[24:27],s82 offen offset:2048 sc1\n"
"v_cvt_pk_bf16_f32 v90,v52,v53\n"
"v_exp_f32 v54,v54\n"
"v_mfma_f32_16x16x32_bf16 v[200:203],a[104:107],v[72:75],v[200:203]\n"
"buffer_load_dwordx4 a[12:15],v112,s[24:27],s82 offen offset:3072 sc1\n"
"v_mfma_f32_16x16x32_bf16 v[232:235],a[104:107],v[76:79],v[232:235]\n"
"s_sub_u32 s82,s3,49152\n"
"buffer_load_dwordx4 a[16:19],v112,s[24:27],s82 offen offset:0 sc1\n"
"buffer_load_dwordx4 a[20:23],v112,s[24:27],s82 offen offset:1024 sc1\n"
"v_exp_f32 v55,v55\n"
"v_mfma_f32_16x16x32_bf16 v[136:139],a[104:107],v[64:67],v[136:139]\n"
"buffer_load_dwordx4 a[24:27],v112,s[24:27],s82 offen offset:2048 sc1\n"
"v_mfma_f32_16x16x32_bf16 v[168:171],a[104:107],v[68:71],v[168:171]\n"
"buffer_load_dwordx4 a[28:31],v112,s[24:27],s82 offen offset:3072 sc1\n"
"v_mfma_f32_16x16x32_bf16 a[132:135],a[144:147],v[68:71],a[132:135]\n"
"v_exp_f32 v56,v56\n"
"v_mfma_f32_16x16x32_bf16 v[172:175],a[108:111],v[68:71],v[172:175]\n"
"v_cvt_pk_bf16_f32 v91,v54,v55\n"
"v_mfma_f32_16x16x32_bf16 v[204:207],a[108:111],v[72:75],v[204:207]\n"
"v_exp_f32 v57,v57\n"
"v_mfma_f32_16x16x32_bf16 v[236:239],a[108:111],v[76:79],v[236:239]\n"
"v_cvt_pk_bf16_f32 v92,v56,v57\n"
"v_mfma_f32_16x16x32_bf16 v[140:143],a[108:111],v[64:67],v[140:143]\n"
"v_exp_f32 v58,v58\n"
"v_mfma_f32_16x16x32_bf16 a[128:131],a[144:147],v[64:67],a[128:131]\n"
"v_mfma_f32_16x16x32_bf16 v[144:147],a[112:115],v[64:67],v[144:147]\n"
"v_exp_f32 v59,v59\n"
"v_mfma_f32_16x16x32_bf16 v[176:179],a[112:115],v[68:71],v[176:179]\n"
"v_cvt_pk_bf16_f32 v93,v58,v59\n"
"v_mfma_f32_16x16x32_bf16 v[208:211],a[112:115],v[72:75],v[208:211]\n"
"v_exp_f32 v60,v60\n"
"v_mfma_f32_16x16x32_bf16 v[240:243],a[112:115],v[76:79],v[240:243]\n"
"v_mfma_f32_16x16x32_bf16 v[244:247],a[116:119],v[76:79],v[244:247]\n"
"v_exp_f32 v61,v61\n"
"v_mfma_f32_16x16x32_bf16 v[148:151],a[116:119],v[64:67],v[148:151]\n"
"v_cvt_pk_bf16_f32 v94,v60,v61\n"
"v_mfma_f32_16x16x32_bf16 v[180:183],a[116:119],v[68:71],v[180:183]\n"
"v_exp_f32 v62,v62\n"
"v_mfma_f32_16x16x32_bf16 v[212:215],a[116:119],v[72:75],v[212:215]\n"
"v_mfma_f32_16x16x32_bf16 v[216:219],a[120:123],v[72:75],v[216:219]\n"
"v_exp_f32 v63,v63\n"
"v_mfma_f32_16x16x32_bf16 v[248:251],a[120:123],v[76:79],v[248:251]\n"
"v_cvt_pk_bf16_f32 v95,v62,v63\n"
"v_mfma_f32_16x16x32_bf16 v[152:155],a[120:123],v[64:67],v[152:155]\n"
"v_mfma_f32_16x16x32_bf16 v[184:187],a[120:123],v[68:71],v[184:187]\n"
"v_mfma_f32_16x16x32_bf16 v[188:191],a[124:127],v[68:71],v[188:191]\n"
"v_mfma_f32_16x16x32_bf16 v[220:223],a[124:127],v[72:75],v[220:223]\n"
"v_mfma_f32_16x16x32_bf16 v[252:255],a[124:127],v[76:79],v[252:255]\n"
"v_mfma_f32_16x16x32_bf16 v[156:159],a[124:127],v[64:67],v[156:159]\n"
"s_cbranch_vccnz .Lfk_cs_b3\n"
".Lfk_cr_b3:\n"
"s_waitcnt vmcnt(9) lgkmcnt(0)\n"
"s_barrier\n"
"v_mfma_f32_16x16x32_bf16 v[128:131],a[64:67],v[80:83],v[128:131]\n"
"v_exp_f32 v0,v0\n"
"v_mfma_f32_16x16x32_bf16 v[160:163],a[64:67],v[84:87],v[160:163]\n"
"v_exp_f32 v1,v1\n"
"v_mfma_f32_16x16x32_bf16 v[192:195],a[64:67],v[88:91],v[192:195]\n"
"v_exp_f32 v2,v2\n"
"v_cvt_pk_bf16_f32 v64,v0,v1\n"
"v_mfma_f32_16x16x32_bf16 v[224:227],a[64:67],v[92:95],v[224:227]\n"
"v_exp_f32 v3,v3\n"
"v_mfma_f32_16x16x32_bf16 a[140:143],a[144:147],v[92:95],a[140:143]\n"
"v_exp_f32 v4,v4\n"
"v_cvt_pk_bf16_f32 v65,v2,v3\n"
"v_mfma_f32_16x16x32_bf16 v[228:231],a[68:71],v[92:95],v[228:231]\n"
"v_exp_f32 v5,v5\n"
"v_mfma_f32_16x16x32_bf16 v[132:135],a[68:71],v[80:83],v[132:135]\n"
"v_exp_f32 v6,v6\n"
"v_cvt_pk_bf16_f32 v66,v4,v5\n"
"v_exp_f32 v7,v7\n"
"v_mfma_f32_16x16x32_bf16 v[164:167],a[68:71],v[84:87],v[164:167]\n"
"ds_read_b128 a[96:99],v114 offset:40960\n"
"ds_read_b128 a[100:103],v114 offset:41984\n"
"v_exp_f32 v8,v8\n"
"v_mfma_f32_16x16x32_bf16 v[196:199],a[68:71],v[88:91],v[196:199]\n"
"ds_read_b128 a[104:107],v114 offset:43008\n"
"ds_read_b128 a[108:111],v114 offset:44032\n"
"v_exp_f32 v9,v9\n"
"v_mfma_f32_16x16x32_bf16 a[136:139],a[144:147],v[88:91],a[136:139]\n"
"ds_read_b128 a[112:115],v114 offset:45056\n"
"ds_read_b128 a[116:119],v114 offset:46080\n"
"v_exp_f32 v10,v10\n"
"v_mfma_f32_16x16x32_bf16 v[200:203],a[72:75],v[88:91],v[200:203]\n"
"ds_read_b128 a[120:123],v114 offset:47104\n"
"ds_read_b128 a[124:127],v114 offset:48128\n"
"v_mfma_f32_16x16x32_bf16 v[232:235],a[72:75],v[92:95],v[232:235]\n"
"v_exp_f32 v11,v11\n"
"v_accvgpr_read_b32 v119,a150\n"
"v_accvgpr_read_b32 v120,a151\n"
"v_mfma_f32_16x16x32_bf16 v[136:139],a[72:75],v[80:83],v[136:139]\n"
"v_exp_f32 v12,v12\n"
"v_xor_b32 v119,s80,v119\n"
"v_xor_b32 v120,s81,v120\n"
"v_mfma_f32_16x16x32_bf16 v[168:171],a[72:75],v[84:87],v[168:171]\n"
"v_exp_f32 v13,v13\n"
"v_or_b32 v119,v119,v120\n"
"v_cmp_ne_u32 s[14:15],0,v119\n"
"v_mfma_f32_16x16x32_bf16 a[132:135],a[144:147],v[84:87],a[132:135]\n"
"s_add_u32 s18,s8,1032192\n"
"v_exp_f32 v14,v14\n"
"s_cmp_lg_u32 s14,0\n"
"s_cselect_b32 s18,2146435072,s18\n"
"v_mfma_f32_16x16x32_bf16 v[172:175],a[76:79],v[84:87],v[172:175]\n"
"s_add_u32 m0,s30,0\n"
"v_exp_f32 v15,v15\n"
"buffer_load_dwordx4 v112,s[24:27],s18 offen offset:0 sc1 lds\n"
"buffer_load_dwordx4 v112,s[24:27],s18 offen offset:1024 sc1 lds\n"
"v_exp_f32 v16,v16\n"
"v_mfma_f32_16x16x32_bf16 v[204:207],a[76:79],v[88:91],v[204:207]\n"
"buffer_load_dwordx4 v112,s[24:27],s18 offen offset:2048 sc1 lds\n"
"buffer_load_dwordx4 v112,s[24:27],s18 offen offset:3072 sc1 lds\n"
"v_mfma_f32_16x16x32_bf16 v[236:239],a[76:79],v[92:95],v[236:239]\n"
"s_add_u32 m0,m0,4096\n"
"v_exp_f32 v17,v17\n"
"v_cvt_pk_bf16_f32 v67,v6,v7\n"
"v_mfma_f32_16x16x32_bf16 v[140:143],a[76:79],v[80:83],v[140:143]\n"
"buffer_load_dwordx4 v117,s[24:27],s18 offen offset:0 sc1 lds\n"
"v_exp_f32 v18,v18\n"
"v_cvt_pk_bf16_f32 v68,v8,v9\n"
"v_cvt_pk_bf16_f32 v69,v10,v11\n"
"v_exp_f32 v19,v19\n"
"v_mfma_f32_16x16x32_bf16 a[128:131],a[144:147],v[80:83],a[128:131]\n"
"buffer_load_dwordx4 v117,s[24:27],s18 offen offset:1024 sc1 lds\n"
"v_cvt_pk_bf16_f32 v70,v12,v13\n"
"v_mfma_f32_16x16x32_bf16 v[144:147],a[80:83],v[80:83],v[144:147]\n"
"v_exp_f32 v20,v20\n"
"v_cvt_pk_bf16_f32 v71,v14,v15\n"
"v_cvt_pk_bf16_f32 v72,v16,v17\n"
"v_exp_f32 v21,v21\n"
"v_mfma_f32_16x16x32_bf16 v[176:179],a[80:83],v[84:87],v[176:179]\n"
"buffer_load_dwordx4 v117,s[24:27],s18 offen offset:2048 sc1 lds\n"
"v_cvt_pk_bf16_f32 v73,v18,v19\n"
"v_mfma_f32_16x16x32_bf16 v[208:211],a[80:83],v[88:91],v[208:211]\n"
"v_exp_f32 v22,v22\n"
"v_cvt_pk_bf16_f32 v74,v20,v21\n"
"v_exp_f32 v23,v23\n"
"v_mfma_f32_16x16x32_bf16 v[240:243],a[80:83],v[92:95],v[240:243]\n"
"buffer_load_dwordx4 v117,s[24:27],s18 offen offset:3072 sc1 lds\n"
"v_mfma_f32_16x16x32_bf16 v[244:247],a[84:87],v[92:95],v[244:247]\n"
"s_sub_u32 s82,s3,65536\n"
"v_exp_f32 v24,v24\n"
"v_cvt_pk_bf16_f32 v75,v22,v23\n"
"v_exp_f32 v25,v25\n"
"v_mfma_f32_16x16x32_bf16 v[148:151],a[84:87],v[80:83],v[148:151]\n"
"buffer_load_dwordx4 v[32:35],v112,s[24:27],s82 offen offset:0 sc1\n"
"v_mfma_f32_16x16x32_bf16 v[180:183],a[84:87],v[84:87],v[180:183]\n"
"v_exp_f32 v26,v26\n"
"v_cvt_pk_bf16_f32 v76,v24,v25\n"
"v_exp_f32 v27,v27\n"
"v_mfma_f32_16x16x32_bf16 v[212:215],a[84:87],v[88:91],v[212:215]\n"
"buffer_load_dwordx4 v[36:39],v112,s[24:27],s82 offen offset:1024 sc1\n"
"v_mfma_f32_16x16x32_bf16 v[216:219],a[88:91],v[88:91],v[216:219]\n"
"v_exp_f32 v28,v28\n"
"v_exp_f32 v29,v29\n"
"v_mfma_f32_16x16x32_bf16 v[248:251],a[88:91],v[92:95],v[248:251]\n"
"buffer_load_dwordx4 v[40:43],v112,s[24:27],s82 offen offset:2048 sc1\n"
"v_mfma_f32_16x16x32_bf16 v[152:155],a[88:91],v[80:83],v[152:155]\n"
"v_exp_f32 v30,v30\n"
"v_mfma_f32_16x16x32_bf16 v[184:187],a[88:91],v[84:87],v[184:187]\n"
"v_exp_f32 v31,v31\n"
"v_mfma_f32_16x16x32_bf16 v[188:191],a[92:95],v[84:87],v[188:191]\n"
"buffer_load_dwordx4 v[44:47],v112,s[24:27],s82 offen offset:3072 sc1\n"
"v_mfma_f32_16x16x32_bf16 v[220:223],a[92:95],v[88:91],v[220:223]\n"
"v_cvt_pk_bf16_f32 v78,v28,v29\n"
"v_cvt_pk_bf16_f32 v77,v26,v27\n"
"v_mfma_f32_16x16x32_bf16 v[252:255],a[92:95],v[92:95],v[252:255]\n"
"v_cvt_pk_bf16_f32 v79,v30,v31\n"
"v_mfma_f32_16x16x32_bf16 v[156:159],a[92:95],v[80:83],v[156:159]\n"
"s_load_dwordx2 s[36:37],s[0:1],0\n"
"s_load_dwordx2 s[44:45],s[0:1],80\n"
"s_load_dwordx2 s[48:49],s[0:1],88\n"
"s_lshl_b32 s82,s78,2\n"
"s_add_u32 s82,s82,65536\n"
"v_add_u32 v113,s82,v112\n"
"s_lshr_b32 s82,s78,2\n"
"s_add_u32 s82,s82,122880\n"
"v_lshrrev_b32 v115,1,v112\n"
"v_add_u32 v116,s82,v115\n"
"s_waitcnt lgkmcnt(0)\n"
"s_barrier\n"
"v_mfma_f32_16x16x32_bf16 v[128:131],a[96:99],v[64:67],v[128:131]\n"
"s_add_u32 s83,s18,2097152\n"
"s_add_u32 m0,s30,32768\n"
"v_mfma_f32_16x16x32_bf16 a[128:131],a[144:147],v[64:67],a[128:131]\n"
"buffer_load_dwordx4 v112,s[24:27],s83 offen offset:0 sc1 lds\n"
"v_mfma_f32_16x16x32_bf16 v[132:135],a[100:103],v[64:67],v[132:135]\n"
"buffer_load_dwordx4 v112,s[24:27],s83 offen offset:1024 sc1 lds\n"
"v_mfma_f32_16x16x32_bf16 v[136:139],a[104:107],v[64:67],v[136:139]\n"
"buffer_load_dwordx4 v112,s[24:27],s83 offen offset:2048 sc1 lds\n"
"v_mfma_f32_16x16x32_bf16 v[140:143],a[108:111],v[64:67],v[140:143]\n"
"v_mfma_f32_16x16x32_bf16 v[144:147],a[112:115],v[64:67],v[144:147]\n"
"buffer_load_dwordx4 v112,s[24:27],s83 offen offset:3072 sc1 lds\n"
"v_mfma_f32_16x16x32_bf16 v[148:151],a[116:119],v[64:67],v[148:151]\n"
"s_add_u32 m0,m0,4096\n"
"v_mfma_f32_16x16x32_bf16 v[152:155],a[120:123],v[64:67],v[152:155]\n"
"v_mfma_f32_16x16x32_bf16 v[156:159],a[124:127],v[64:67],v[156:159]\n"
"buffer_load_dwordx4 v117,s[24:27],s83 offen offset:0 sc1 lds\n"
"v_mfma_f32_16x16x32_bf16 v[160:163],a[96:99],v[68:71],v[160:163]\n"
"v_accvgpr_read_b32 v97,a128\n"
"v_cvt_pk_bf16_f32 v136,v136,v137\n"
"v_mfma_f32_16x16x32_bf16 a[132:135],a[144:147],v[68:71],a[132:135]\n"
"buffer_load_dwordx4 v117,s[24:27],s83 offen offset:1024 sc1 lds\n"
"v_mfma_f32_16x16x32_bf16 v[164:167],a[100:103],v[68:71],v[164:167]\n"
"v_cvt_pk_bf16_f32 v128,v128,v129\n"
"v_cvt_pk_bf16_f32 v129,v130,v131\n"
"v_mfma_f32_16x16x32_bf16 v[168:171],a[104:107],v[68:71],v[168:171]\n"
"buffer_load_dwordx4 v117,s[24:27],s83 offen offset:2048 sc1 lds\n"
"v_cvt_pk_bf16_f32 v130,v132,v133\n"
"v_cvt_pk_bf16_f32 v131,v134,v135\n"
"v_mfma_f32_16x16x32_bf16 v[172:175],a[108:111],v[68:71],v[172:175]\n"
"ds_write_b128 v113,v[128:131] offset:0\n"
"v_cvt_pk_bf16_f32 v137,v138,v139\n"
"v_mfma_f32_16x16x32_bf16 v[176:179],a[112:115],v[68:71],v[176:179]\n"
"buffer_load_dwordx4 v117,s[24:27],s83 offen offset:3072 sc1 lds\n"
"v_cvt_pk_bf16_f32 v138,v140,v141\n"
"v_cvt_pk_bf16_f32 v139,v142,v143\n"
"v_mfma_f32_16x16x32_bf16 v[180:183],a[116:119],v[68:71],v[180:183]\n"
"s_sub_u32 s82,s3,81920\n"
"ds_write_b128 v113,v[136:139] offset:1024\n"
"v_cvt_pk_bf16_f32 v144,v144,v145\n"
"v_mfma_f32_16x16x32_bf16 v[184:187],a[120:123],v[68:71],v[184:187]\n"
"buffer_load_dwordx4 a[32:35],v112,s[24:27],s82 offen offset:0 sc1\n"
"v_cvt_pk_bf16_f32 v145,v146,v147\n"
"v_cvt_pk_bf16_f32 v146,v148,v149\n"
"v_cvt_pk_bf16_f32 v147,v150,v151\n"
"v_mfma_f32_16x16x32_bf16 v[188:191],a[124:127],v[68:71],v[188:191]\n"
"ds_write_b128 v113,v[144:147] offset:2048\n"
"v_cvt_pk_bf16_f32 v152,v152,v153\n"
"v_mfma_f32_16x16x32_bf16 v[192:195],a[96:99],v[72:75],v[192:195]\n"
"buffer_load_dwordx4 a[36:39],v112,s[24:27],s82 offen offset:1024 sc1\n"
"v_cvt_pk_bf16_f32 v153,v154,v155\n"
"v_cvt_pk_bf16_f32 v154,v156,v157\n"
"v_cvt_pk_bf16_f32 v155,v158,v159\n"
"v_mfma_f32_16x16x32_bf16 a[136:139],a[144:147],v[72:75],a[136:139]\n"
"ds_write_b128 v113,v[152:155] offset:3072\n"
"ds_write_b64 v116,v[96:97] offset:0\n"
"v_mfma_f32_16x16x32_bf16 v[196:199],a[100:103],v[72:75],v[196:199]\n"
"buffer_load_dwordx4 a[40:43],v112,s[24:27],s82 offen offset:2048 sc1\n"
"v_accvgpr_read_b32 v101,a132\n"
"v_cvt_pk_bf16_f32 v160,v160,v161\n"
"v_mfma_f32_16x16x32_bf16 v[200:203],a[104:107],v[72:75],v[200:203]\n"
"v_cvt_pk_bf16_f32 v161,v162,v163\n"
"v_cvt_pk_bf16_f32 v162,v164,v165\n"
"v_cvt_pk_bf16_f32 v163,v166,v167\n"
"v_mfma_f32_16x16x32_bf16 v[204:207],a[108:111],v[72:75],v[204:207]\n"
"ds_write_b128 v113,v[160:163] offset:4096\n"
"v_cvt_pk_bf16_f32 v168,v168,v169\n"
"v_cvt_pk_bf16_f32 v169,v170,v171\n"
"v_mfma_f32_16x16x32_bf16 v[208:211],a[112:115],v[72:75],v[208:211]\n"
"buffer_load_dwordx4 a[44:47],v112,s[24:27],s82 offen offset:3072 sc1\n"
"v_cvt_pk_bf16_f32 v170,v172,v173\n"
"v_cvt_pk_bf16_f32 v171,v174,v175\n"
"v_mfma_f32_16x16x32_bf16 v[212:215],a[116:119],v[72:75],v[212:215]\n"
"ds_write_b128 v113,v[168:171] offset:5120\n"
"v_cvt_pk_bf16_f32 v176,v176,v177\n"
"v_cvt_pk_bf16_f32 v177,v178,v179\n"
"v_mfma_f32_16x16x32_bf16 v[216:219],a[120:123],v[72:75],v[216:219]\n"
"v_cvt_pk_bf16_f32 v178,v180,v181\n"
"v_cvt_pk_bf16_f32 v179,v182,v183\n"
"v_mfma_f32_16x16x32_bf16 v[220:223],a[124:127],v[72:75],v[220:223]\n"
"ds_write_b128 v113,v[176:179] offset:6144\n"
"v_cvt_pk_bf16_f32 v184,v184,v185\n"
"v_cvt_pk_bf16_f32 v185,v186,v187\n"
"v_cvt_pk_bf16_f32 v186,v188,v189\n"
"v_cvt_pk_bf16_f32 v187,v190,v191\n"
"v_mfma_f32_16x16x32_bf16 v[224:227],a[96:99],v[76:79],v[224:227]\n"
"ds_write_b128 v113,v[184:187] offset:7168\n"
"v_mfma_f32_16x16x32_bf16 a[140:143],a[144:147],v[76:79],a[140:143]\n"
"s_sub_u32 s82,s3,131072\n"
"ds_write_b64 v116,v[100:101] offset:512\n"
"buffer_load_dwordx4 v[128:131],v112,s[24:27],s82 offen offset:0 sc1\n"
"buffer_load_dwordx4 v[132:135],v112,s[24:27],s82 offen offset:1024 sc1\n"
"v_mfma_f32_16x16x32_bf16 v[228:231],a[100:103],v[76:79],v[228:231]\n"
"buffer_load_dwordx4 v[136:139],v112,s[24:27],s82 offen offset:2048 sc1\n"
"buffer_load_dwordx4 v[140:143],v112,s[24:27],s82 offen offset:3072 sc1\n"
"s_sub_u32 s82,s3,147456\n"
"v_mfma_f32_16x16x32_bf16 v[232:235],a[104:107],v[76:79],v[232:235]\n"
"buffer_load_dwordx4 v[144:147],v112,s[24:27],s82 offen offset:0 sc1\n"
"buffer_load_dwordx4 v[148:151],v112,s[24:27],s82 offen offset:1024 sc1\n"
"buffer_load_dwordx4 v[152:155],v112,s[24:27],s82 offen offset:2048 sc1\n"
"v_mfma_f32_16x16x32_bf16 v[236:239],a[108:111],v[76:79],v[236:239]\n"
"buffer_load_dwordx4 v[156:159],v112,s[24:27],s82 offen offset:3072 sc1\n"
"v_mfma_f32_16x16x32_bf16 v[240:243],a[112:115],v[76:79],v[240:243]\n"
"v_mfma_f32_16x16x32_bf16 v[244:247],a[116:119],v[76:79],v[244:247]\n"
"v_mfma_f32_16x16x32_bf16 v[248:251],a[120:123],v[76:79],v[248:251]\n"
"v_mfma_f32_16x16x32_bf16 v[252:255],a[124:127],v[76:79],v[252:255]\n"
".Lfk_endB:\n"
"v_lshrrev_b32 v70,4,v112\n"
"v_and_b32 v71,15,v70\n"
"v_lshrrev_b32 v70,4,v70\n"
"v_lshlrev_b32 v70,2,v70\n"
"v_sub_u32 v70,v71,v70\n"
"v_mov_b32 v71,0x3f80\n"
"v_cmp_eq_u32 vcc,0,v70\n"
"v_mov_b32 v68,0\n"
"v_mov_b32 v64,0\n"
"v_cndmask_b32 v66,0,v71,vcc\n"
"v_cmp_eq_u32 vcc,1,v70\n"
"v_mov_b32 v65,0\n"
"v_mov_b32 v72,0x3f800000\n"
"v_cndmask_b32 v66,v66,v72,vcc\n"
"v_cmp_eq_u32 vcc,2,v70\n"
"v_mov_b32 v69,0\n"
"s_nop 0\n"
"v_cndmask_b32 v67,0,v71,vcc\n"
"v_cmp_eq_u32 vcc,3,v70\n"
"s_nop 1\n"
"v_cndmask_b32 v67,v67,v72,vcc\n"
"s_sub_u32 s82,s3,163840\n"
"buffer_load_dwordx4 v[0:3],v112,s[24:27],s82 offen offset:0 sc1\n"
"buffer_load_dwordx4 v[4:7],v112,s[24:27],s82 offen offset:1024 sc1\n"
"buffer_load_dwordx4 v[8:11],v112,s[24:27],s82 offen offset:2048 sc1\n"
"buffer_load_dwordx4 v[12:15],v112,s[24:27],s82 offen offset:3072 sc1\n"
"s_sub_u32 s82,s3,180224\n"
"buffer_load_dwordx4 v[160:163],v112,s[24:27],s82 offen offset:0 sc1\n"
"buffer_load_dwordx4 v[164:167],v112,s[24:27],s82 offen offset:1024 sc1\n"
"buffer_load_dwordx4 v[168:171],v112,s[24:27],s82 offen offset:2048 sc1\n"
"buffer_load_dwordx4 v[172:175],v112,s[24:27],s82 offen offset:3072 sc1\n"
"s_sub_u32 s82,s3,196608\n"
"buffer_load_dwordx4 v[176:179],v112,s[24:27],s82 offen offset:0 sc1\n"
"buffer_load_dwordx4 v[180:183],v112,s[24:27],s82 offen offset:1024 sc1\n"
"buffer_load_dwordx4 v[184:187],v112,s[24:27],s82 offen offset:2048 sc1\n"
"buffer_load_dwordx4 v[188:191],v112,s[24:27],s82 offen offset:3072 sc1\n"
"s_waitcnt lgkmcnt(0)\n"
"s_barrier\n"
"v_xor_b32 v113,32768,v113\n"
"v_xor_b32 v116,2048,v116\n"
"ds_read_b64 v[98:99],v116 offset:0\n"
"ds_read_b128 a[64:67],v113 offset:0\n"
"ds_read_b128 a[68:71],v113 offset:1024\n"
"ds_read_b128 a[72:75],v113 offset:2048\n"
"ds_read_b128 a[76:79],v113 offset:3072\n"
"ds_read_b64 v[102:103],v116 offset:512\n"
"ds_read_b128 a[80:83],v113 offset:4096\n"
"ds_read_b128 a[84:87],v113 offset:5120\n"
"ds_read_b128 a[88:91],v113 offset:6144\n"
"ds_read_b128 a[92:95],v113 offset:7168\n"
"s_waitcnt lgkmcnt(5)\n"
"v_cmp_neq_f32 vcc,v98,v104\n"
"s_nop 0\n"
"s_cbranch_vccnz .Lfk_ms_b0\n"
".Lfk_mr_b0:\n"
"v_mfma_f32_16x16x32_bf16 v[192:195],v[66:69],a[64:67],v[192:195]\n"
"v_mfma_f32_16x16x32_bf16 v[196:199],v[64:67],a[64:67],v[196:199]\n"
"v_mfma_f32_16x16x32_bf16 v[200:203],v[66:69],a[68:71],v[200:203]\n"
"v_mfma_f32_16x16x32_bf16 v[204:207],v[64:67],a[68:71],v[204:207]\n"
"v_mfma_f32_16x16x32_bf16 v[208:211],v[66:69],a[72:75],v[208:211]\n"
"v_mfma_f32_16x16x32_bf16 v[212:215],v[64:67],a[72:75],v[212:215]\n"
"v_mfma_f32_16x16x32_bf16 v[216:219],v[66:69],a[76:79],v[216:219]\n"
"v_mfma_f32_16x16x32_bf16 v[220:223],v[64:67],a[76:79],v[220:223]\n"
"s_waitcnt lgkmcnt(0)\n"
"v_cmp_neq_f32 vcc,v102,v108\n"
"s_nop 0\n"
"s_cbranch_vccnz .Lfk_ms_b1\n"
".Lfk_mr_b1:\n"
"v_mfma_f32_16x16x32_bf16 v[224:227],v[66:69],a[80:83],v[224:227]\n"
"v_mfma_f32_16x16x32_bf16 v[228:231],v[64:67],a[80:83],v[228:231]\n"
"v_accvgpr_read_b32 v122,a136\n"
"v_add_f32 v122,v122,v99\n"
"v_mfma_f32_16x16x32_bf16 v[232:235],v[66:69],a[84:87],v[232:235]\n"
"v_rcp_f32 v122,v122\n"
"v_mfma_f32_16x16x32_bf16 v[236:239],v[64:67],a[84:87],v[236:239]\n"
"v_pk_mul_f32 v[192:193],v[192:193],v[122:123] op_sel_hi:[1,0]\n"
"v_pk_mul_f32 v[194:195],v[194:195],v[122:123] op_sel_hi:[1,0]\n"
"v_pk_mul_f32 v[196:197],v[196:197],v[122:123] op_sel_hi:[1,0]\n"
"v_pk_mul_f32 v[198:199],v[198:199],v[122:123] op_sel_hi:[1,0]\n"
"v_mfma_f32_16x16x32_bf16 v[240:243],v[66:69],a[88:91],v[240:243]\n"
"v_cvt_pk_bf16_f32 v192,v192,v193\n"
"v_cvt_pk_bf16_f32 v193,v194,v195\n"
"v_mfma_f32_16x16x32_bf16 v[244:247],v[64:67],a[88:91],v[244:247]\n"
"v_cvt_pk_bf16_f32 v194,v196,v197\n"
"v_cvt_pk_bf16_f32 v195,v198,v199\n"
"v_mfma_f32_16x16x32_bf16 v[248:251],v[66:69],a[92:95],v[248:251]\n"
"buffer_store_dwordx4 v[192:195],v112,s[24:27],s70 offen offset:0 sc1\n"
"v_pk_mul_f32 v[200:201],v[200:201],v[122:123] op_sel_hi:[1,0]\n"
"v_pk_mul_f32 v[202:203],v[202:203],v[122:123] op_sel_hi:[1,0]\n"
"v_pk_mul_f32 v[204:205],v[204:205],v[122:123] op_sel_hi:[1,0]\n"
"v_pk_mul_f32 v[206:207],v[206:207],v[122:123] op_sel_hi:[1,0]\n"
"v_mfma_f32_16x16x32_bf16 v[252:255],v[64:67],a[92:95],v[252:255]\n"
"v_cvt_pk_bf16_f32 v200,v200,v201\n"
"v_cvt_pk_bf16_f32 v201,v202,v203\n"
"v_cvt_pk_bf16_f32 v202,v204,v205\n"
"v_cvt_pk_bf16_f32 v203,v206,v207\n"
"v_pk_mul_f32 v[208:209],v[208:209],v[122:123] op_sel_hi:[1,0]\n"
"v_pk_mul_f32 v[210:211],v[210:211],v[122:123] op_sel_hi:[1,0]\n"
"v_pk_mul_f32 v[212:213],v[212:213],v[122:123] op_sel_hi:[1,0]\n"
"v_pk_mul_f32 v[214:215],v[214:215],v[122:123] op_sel_hi:[1,0]\n"
"v_cvt_pk_bf16_f32 v208,v208,v209\n"
"v_cvt_pk_bf16_f32 v209,v210,v211\n"
"v_cvt_pk_bf16_f32 v210,v212,v213\n"
"v_cvt_pk_bf16_f32 v211,v214,v215\n"
"buffer_store_dwordx4 v[200:203],v112,s[24:27],s70 offen offset:1024 sc1\n"
"buffer_store_dwordx4 v[208:211],v112,s[24:27],s70 offen offset:2048 sc1\n"
"v_pk_mul_f32 v[216:217],v[216:217],v[122:123] op_sel_hi:[1,0]\n"
"v_pk_mul_f32 v[218:219],v[218:219],v[122:123] op_sel_hi:[1,0]\n"
"v_pk_mul_f32 v[220:221],v[220:221],v[122:123] op_sel_hi:[1,0]\n"
"v_pk_mul_f32 v[222:223],v[222:223],v[122:123] op_sel_hi:[1,0]\n"
"v_cvt_pk_bf16_f32 v216,v216,v217\n"
"v_cvt_pk_bf16_f32 v217,v218,v219\n"
"v_cvt_pk_bf16_f32 v218,v220,v221\n"
"v_cvt_pk_bf16_f32 v219,v222,v223\n"
"buffer_store_dwordx4 v[216:219],v112,s[24:27],s70 offen offset:3072 sc1\n"
"s_add_u32 s70,s70,262144\n"
"s_branch .Lfk_p1\n"
".Lfk_tailB7:\n"
"s_waitcnt vmcnt(11) lgkmcnt(0)\n"
"s_barrier\n"
"v_mfma_f32_16x16x32_bf16 v[0:3],a[64:67],a[0:3],v[96:99]\n"
"ds_read_b128 a[96:99],v114 offset:24576\n"
"ds_read_b128 a[100:103],v114 offset:25600\n"
"ds_read_b128 a[104:107],v114 offset:26624\n"
"v_mfma_f32_16x16x32_bf16 v[0:3],a[68:71],a[4:7],v[0:3]\n"
"ds_read_b128 a[108:111],v114 offset:27648\n"
"ds_read_b128 a[112:115],v114 offset:28672\n"
"ds_read_b128 a[116:119],v114 offset:29696\n"
"v_mfma_f32_16x16x32_bf16 v[0:3],a[72:75],a[8:11],v[0:3]\n"
"ds_read_b128 a[120:123],v114 offset:30720\n"
"ds_read_b128 a[124:127],v114 offset:31744\n"
"v_add_f32 v16,0x80000000,v126\n"
"v_mfma_f32_16x16x32_bf16 v[0:3],a[76:79],a[12:15],v[0:3]\n"
"v_min_f32 v16,v16,v104\n"
"v_add_f32 v17,0xf149f2ca,v126\n"
"v_min_f32 v17,v17,v105\n"
"v_mfma_f32_16x16x32_bf16 v[4:7],a[92:95],a[12:15],v[96:99]\n"
"v_add_f32 v18,0xf1c9f2ca,v126\n"
"v_min_f32 v18,v18,v106\n"
"v_add_f32 v19,0xf2177617,v126\n"
"v_mfma_f32_16x16x32_bf16 v[4:7],a[88:91],a[8:11],v[4:7]\n"
"v_min_f32 v19,v19,v107\n"
"v_add_f32 v20,0xf349f2ca,v126\n"
"v_mfma_f32_16x16x32_bf16 v[4:7],a[84:87],a[4:7],v[4:7]\n"
"v_add_f32 v21,0xf35691f6,v126\n"
"v_min_f32 v21,v21,v105\n"
"v_add_f32 v22,0xf3633123,v126\n"
"v_mfma_f32_16x16x32_bf16 v[4:7],a[80:83],a[0:3],v[4:7]\n"
"s_waitcnt lgkmcnt(0)\n"
"v_min_f32 v22,v22,v106\n"
"v_add_f32 v23,0xf36fd050,v126\n"
"v_mfma_f32_16x16x32_bf16 v[192:195],a[96:99],v[72:75],v[192:195]\n"
"v_exp_f32 v56,v56\n"
"v_mfma_f32_16x16x32_bf16 a[136:139],a[144:147],v[72:75],a[136:139]\n"
"v_exp_f32 v57,v57\n"
"v_mfma_f32_16x16x32_bf16 v[196:199],a[100:103],v[72:75],v[196:199]\n"
"v_min_f32 v20,v20,v104\n"
"v_cvt_pk_bf16_f32 v92,v56,v57\n"
"v_mfma_f32_16x16x32_bf16 v[200:203],a[104:107],v[72:75],v[200:203]\n"
"v_max3_f32 v118,v0,v1,v2\n"
"v_max3_f32 v118,v118,v3,v4\n"
"v_mfma_f32_16x16x32_bf16 v[204:207],a[108:111],v[72:75],v[204:207]\n"
"v_max3_f32 v118,v118,v5,v6\n"
"v_max_f32 v118,v118,v7\n"
"v_mfma_f32_16x16x32_bf16 v[208:211],a[112:115],v[72:75],v[208:211]\n"
"v_min_f32 v23,v23,v107\n"
"v_mov_b32 v120,v118\n"
"v_mfma_f32_16x16x32_bf16 v[212:215],a[116:119],v[72:75],v[212:215]\n"
"v_exp_f32 v58,v58\n"
"v_mfma_f32_16x16x32_bf16 v[216:219],a[120:123],v[72:75],v[216:219]\n"
"v_mfma_f32_16x16x32_bf16 v[220:223],a[124:127],v[72:75],v[220:223]\n"
"v_exp_f32 v59,v59\n"
"v_mfma_f32_16x16x32_bf16 v[224:227],a[96:99],v[76:79],v[224:227]\n"
"v_cvt_pk_bf16_f32 v93,v58,v59\n"
"v_mfma_f32_16x16x32_bf16 a[140:143],a[144:147],v[76:79],a[140:143]\n"
"v_accvgpr_read_b32 v122,a136\n"
"v_mfma_f32_16x16x32_bf16 v[228:231],a[100:103],v[76:79],v[228:231]\n"
"v_rcp_f32 v122,v122\n"
"v_mfma_f32_16x16x32_bf16 v[232:235],a[104:107],v[76:79],v[232:235]\n"
"v_pk_mul_f32 v[192:193],v[192:193],v[122:123] op_sel_hi:[1,0]\n"
"v_pk_mul_f32 v[194:195],v[194:195],v[122:123] op_sel_hi:[1,0]\n"
"v_pk_mul_f32 v[196:197],v[196:197],v[122:123] op_sel_hi:[1,0]\n"
"v_pk_mul_f32 v[198:199],v[198:199],v[122:123] op_sel_hi:[1,0]\n"
"v_mfma_f32_16x16x32_bf16 v[236:239],a[108:111],v[76:79],v[236:239]\n"
"v_cvt_pk_bf16_f32 v192,v192,v193\n"
"v_cvt_pk_bf16_f32 v193,v194,v195\n"
"v_mfma_f32_16x16x32_bf16 v[240:243],a[112:115],v[76:79],v[240:243]\n"
"v_cvt_pk_bf16_f32 v194,v196,v197\n"
"v_cvt_pk_bf16_f32 v195,v198,v199\n"
"v_mfma_f32_16x16x32_bf16 v[244:247],a[116:119],v[76:79],v[244:247]\n"
"buffer_store_dwordx4 v[192:195],v112,s[24:27],s70 offen offset:0 sc1\n"
"v_pk_mul_f32 v[200:201],v[200:201],v[122:123] op_sel_hi:[1,0]\n"
"v_pk_mul_f32 v[202:203],v[202:203],v[122:123] op_sel_hi:[1,0]\n"
"v_mfma_f32_16x16x32_bf16 v[248:251],a[120:123],v[76:79],v[248:251]\n"
"v_exp_f32 v60,v60\n"
"v_cvt_pk_bf16_f32 v200,v200,v201\n"
"v_cvt_pk_bf16_f32 v201,v202,v203\n"
"v_pk_mul_f32 v[204:205],v[204:205],v[122:123] op_sel_hi:[1,0]\n"
"v_pk_mul_f32 v[206:207],v[206:207],v[122:123] op_sel_hi:[1,0]\n"
"v_mfma_f32_16x16x32_bf16 v[252:255],a[124:127],v[76:79],v[252:255]\n"
"v_cvt_pk_bf16_f32 v202,v204,v205\n"
"v_cvt_pk_bf16_f32 v203,v206,v207\n"
"v_mfma_f32_16x16x32_bf16 v[12:15],a[80:83],a[16:19],v[100:103]\n"
"buffer_store_dwordx4 v[200:203],v112,s[24:27],s70 offen offset:1024 sc1\n"
"v_pk_mul_f32 v[208:209],v[208:209],v[122:123] op_sel_hi:[1,0]\n"
"v_pk_mul_f32 v[210:211],v[210:211],v[122:123] op_sel_hi:[1,0]\n"
"v_pk_mul_f32 v[212:213],v[212:213],v[122:123] op_sel_hi:[1,0]\n"
"v_pk_mul_f32 v[214:215],v[214:215],v[122:123] op_sel_hi:[1,0]\n"
"v_mfma_f32_16x16x32_bf16 v[12:15],a[84:87],a[20:23],v[12:15]\n"
"v_exp_f32 v61,v61\n"
"v_mfma_f32_16x16x32_bf16 v[12:15],a[88:91],a[24:27],v[12:15]\n"
"v_cvt_pk_bf16_f32 v94,v60,v61\n"
"v_cvt_pk_bf16_f32 v208,v208,v209\n"
"v_mfma_f32_16x16x32_bf16 v[12:15],a[92:95],a[28:31],v[12:15]\n"
"v_cvt_pk_bf16_f32 v209,v210,v211\n"
"v_cvt_pk_bf16_f32 v210,v212,v213\n"
"v_cvt_pk_bf16_f32 v211,v214,v215\n"
"v_mfma_f32_16x16x32_bf16 v[8:11],a[76:79],a[28:31],v[100:103]\n"
"buffer_store_dwordx4 v[208:211],v112,s[24:27],s70 offen offset:2048 sc1\n"
"v_pk_mul_f32 v[216:217],v[216:217],v[122:123] op_sel_hi:[1,0]\n"
"v_pk_mul_f32 v[218:219],v[218:219],v[122:123] op_sel_hi:[1,0]\n"
"v_mfma_f32_16x16x32_bf16 v[8:11],a[72:75],a[24:27],v[8:11]\n"
"v_cvt_pk_bf16_f32 v216,v216,v217\n"
"v_cvt_pk_bf16_f32 v217,v218,v219\n"
"v_pk_mul_f32 v[220:221],v[220:221],v[122:123] op_sel_hi:[1,0]\n"
"v_pk_mul_f32 v[222:223],v[222:223],v[122:123] op_sel_hi:[1,0]\n"
"v_mfma_f32_16x16x32_bf16 v[8:11],a[68:71],a[20:23],v[8:11]\n"
"v_cvt_pk_bf16_f32 v218,v220,v221\n"
"v_cvt_pk_bf16_f32 v219,v222,v223\n"
"v_mfma_f32_16x16x32_bf16 v[8:11],a[64:67],a[16:19],v[8:11]\n"
"buffer_store_dwordx4 v[216:219],v112,s[24:27],s70 offen offset:3072 sc1\n"
"v_mfma_f32_16x16x32_bf16 v[24:27],a[64:67],a[48:51],v[108:111]\n"
"s_add_u32 s70,s70,262144\n"
"v_exp_f32 v62,v62\n"
"v_mfma_f32_16x16x32_bf16 v[24:27],a[68:71],a[52:55],v[24:27]\n"
"v_exp_f32 v63,v63\n"
"v_mfma_f32_16x16x32_bf16 v[24:27],a[72:75],a[56:59],v[24:27]\n"
"s_nop 0\n"
"v_max3_f32 v119,v8,v9,v10\n"
"v_mfma_f32_16x16x32_bf16 v[24:27],a[76:79],a[60:63],v[24:27]\n"
"v_max3_f32 v119,v119,v11,v12\n"
"v_max3_f32 v119,v119,v13,v14\n"
"v_mfma_f32_16x16x32_bf16 v[28:31],a[92:95],a[60:63],v[108:111]\n"
"v_max_f32 v119,v119,v15\n"
"v_mfma_f32_16x16x32_bf16 v[28:31],a[88:91],a[56:59],v[28:31]\n"
"v_min_f32 v120,v120,v119\n"
"v_max_f32 v118,v118,v119\n"
"v_mfma_f32_16x16x32_bf16 v[28:31],a[84:87],a[52:55],v[28:31]\n"
"v_mfma_f32_16x16x32_bf16 v[28:31],a[80:83],a[48:51],v[28:31]\n"
"v_cvt_pk_bf16_f32 v95,v62,v63\n"
"v_mfma_f32_16x16x32_bf16 v[20:23],a[80:83],a[32:35],v[20:23]\n"
"v_mfma_f32_16x16x32_bf16 v[20:23],a[84:87],a[36:39],v[20:23]\n"
"v_mfma_f32_16x16x32_bf16 v[20:23],a[88:91],a[40:43],v[20:23]\n"
"v_max3_f32 v119,v24,v25,v26\n"
"v_mfma_f32_16x16x32_bf16 v[20:23],a[92:95],a[44:47],v[20:23]\n"
"s_nop 1\n"
"v_max3_f32 v119,v119,v27,v28\n"
"v_mfma_f32_16x16x32_bf16 v[16:19],a[76:79],a[44:47],v[16:19]\n"
"v_max3_f32 v119,v119,v29,v30\n"
"v_max_f32 v119,v119,v31\n"
"v_mfma_f32_16x16x32_bf16 v[16:19],a[72:75],a[40:43],v[16:19]\n"
"v_mfma_f32_16x16x32_bf16 v[16:19],a[68:71],a[36:39],v[16:19]\n"
"v_mfma_f32_16x16x32_bf16 v[16:19],a[64:67],a[32:35],v[16:19]\n"
"ds_read_b128 a[64:67],v114 offset:57344\n"
"ds_read_b128 a[68:71],v114 offset:58368\n"
"ds_read_b128 a[72:75],v114 offset:59392\n"
"v_max_f32 v118,v118,v119\n"
"v_min_f32 v120,v120,v119\n"
"v_cmp_gt_f32 s[96:97],s16,v120\n"
"s_nop 1\n"
"v_max3_f32 v119,v16,v17,v18\n"
"v_max3_f32 v119,v119,v19,v20\n"
"v_max3_f32 v119,v119,v21,v22\n"
"v_max_f32 v119,v119,v23\n"
"v_cmp_gt_f32 s[28:29],s16,v119\n"
"v_max_f32 v118,v118,v119\n"
"v_cmp_lt_f32 vcc,s93,v118\n"
"ds_read_b128 a[76:79],v114 offset:60416\n"
"ds_read_b128 a[80:83],v114 offset:61440\n"
"ds_read_b128 a[84:87],v114 offset:62464\n"
"ds_read_b128 a[88:91],v114 offset:63488\n"
"ds_read_b128 a[92:95],v114 offset:64512\n"
"v_mov_b32 v114,v112\n"
"s_sub_u32 s82,s3,32768\n"
"buffer_load_dwordx4 a[0:3],v112,s[24:27],s82 offen offset:0 sc1\n"
"buffer_load_dwordx4 a[4:7],v112,s[24:27],s82 offen offset:1024 sc1\n"
"buffer_load_dwordx4 a[8:11],v112,s[24:27],s82 offen offset:2048 sc1\n"
"buffer_load_dwordx4 a[12:15],v112,s[24:27],s82 offen offset:3072 sc1\n"
"s_sub_u32 s82,s3,49152\n"
"buffer_load_dwordx4 a[16:19],v112,s[24:27],s82 offen offset:0 sc1\n"
"buffer_load_dwordx4 a[20:23],v112,s[24:27],s82 offen offset:1024 sc1\n"
"buffer_load_dwordx4 a[24:27],v112,s[24:27],s82 offen offset:2048 sc1\n"
"buffer_load_dwordx4 a[28:31],v112,s[24:27],s82 offen offset:3072 sc1\n"
"s_and_b32 s28,s28,0xffff\n"
"s_or_b32 s96,s96,s28\n"
"s_or_b64 s[96:97],s[96:97],vcc\n"
"s_cbranch_scc1 .Lfk_cs_c3\n"
".Lfk_cr_c3:\n"
"s_waitcnt vmcnt(13) lgkmcnt(0)\n"
"s_barrier\n"
"v_mfma_f32_16x16x32_bf16 v[224:227],a[64:67],v[92:95],v[224:227]\n"
"ds_read_b128 a[96:99],v114 offset:40960\n"
"v_exp_f32 v0,v0\n"
"v_mfma_f32_16x16x32_bf16 a[140:143],a[144:147],v[92:95],a[140:143]\n"
"ds_read_b128 a[100:103],v114 offset:41984\n"
"v_exp_f32 v1,v1\n"
"v_mfma_f32_16x16x32_bf16 v[228:231],a[68:71],v[92:95],v[228:231]\n"
"ds_read_b128 a[104:107],v114 offset:43008\n"
"v_exp_f32 v2,v2\n"
"v_mfma_f32_16x16x32_bf16 v[232:235],a[72:75],v[92:95],v[232:235]\n"
"ds_read_b128 a[108:111],v114 offset:44032\n"
"v_exp_f32 v3,v3\n"
"v_mfma_f32_16x16x32_bf16 v[236:239],a[76:79],v[92:95],v[236:239]\n"
"ds_read_b128 a[112:115],v114 offset:45056\n"
"v_exp_f32 v4,v4\n"
"v_mfma_f32_16x16x32_bf16 v[240:243],a[80:83],v[92:95],v[240:243]\n"
"ds_read_b128 a[116:119],v114 offset:46080\n"
"v_exp_f32 v5,v5\n"
"v_mfma_f32_16x16x32_bf16 v[244:247],a[84:87],v[92:95],v[244:247]\n"
"ds_read_b128 a[120:123],v114 offset:47104\n"
"v_exp_f32 v6,v6\n"
"v_mfma_f32_16x16x32_bf16 v[248:251],a[88:91],v[92:95],v[248:251]\n"
"v_exp_f32 v7,v7\n"
"v_mfma_f32_16x16x32_bf16 v[252:255],a[92:95],v[92:95],v[252:255]\n"
"v_exp_f32 v8,v8\n"
"v_accvgpr_read_b32 v119,a150\n"
"v_accvgpr_read_b32 v120,a151\n"
"v_xor_b32 v119,s80,v119\n"
"v_xor_b32 v120,s81,v120\n"
"v_or_b32 v119,v119,v120\n"
"v_cmp_ne_u32 s[14:15],0,v119\n"
"s_add_u32 s18,s8,1032192\n"
"s_cmp_lg_u32 s14,0\n"
"s_cselect_b32 s18,2146435072,s18\n"
"s_add_u32 m0,s30,0\n"
"ds_read_b128 a[124:127],v114 offset:48128\n"
"buffer_load_dwordx4 v112,s[24:27],s18 offen offset:0 sc1 lds\n"
"buffer_load_dwordx4 v112,s[24:27],s18 offen offset:1024 sc1 lds\n"
"buffer_load_dwordx4 v112,s[24:27],s18 offen offset:2048 sc1 lds\n"
"buffer_load_dwordx4 v112,s[24:27],s18 offen offset:3072 sc1 lds\n"
"s_add_u32 m0,m0,4096\n"
"s_nop 0\n"
"buffer_load_dwordx4 v117,s[24:27],s18 offen offset:0 sc1 lds\n"
"buffer_load_dwordx4 v117,s[24:27],s18 offen offset:1024 sc1 lds\n"
"buffer_load_dwordx4 v117,s[24:27],s18 offen offset:2048 sc1 lds\n"
"buffer_load_dwordx4 v117,s[24:27],s18 offen offset:3072 sc1 lds\n"
"s_sub_u32 s82,s3,65536\n"
"buffer_load_dwordx4 v[32:35],v112,s[24:27],s82 offen offset:0 sc1\n"
"buffer_load_dwordx4 v[36:39],v112,s[24:27],s82 offen offset:1024 sc1\n"
"buffer_load_dwordx4 v[40:43],v112,s[24:27],s82 offen offset:2048 sc1\n"
"buffer_load_dwordx4 v[44:47],v112,s[24:27],s82 offen offset:3072 sc1\n"
"v_exp_f32 v9,v9\n"
"v_cvt_pk_bf16_f32 v64,v0,v1\n"
"v_exp_f32 v10,v10\n"
"v_cvt_pk_bf16_f32 v65,v2,v3\n"
"v_exp_f32 v11,v11\n"
"v_cvt_pk_bf16_f32 v66,v4,v5\n"
"v_exp_f32 v12,v12\n"
"v_cvt_pk_bf16_f32 v67,v6,v7\n"
"v_exp_f32 v13,v13\n"
"v_cvt_pk_bf16_f32 v68,v8,v9\n"
"v_exp_f32 v14,v14\n"
"v_cvt_pk_bf16_f32 v69,v10,v11\n"
"v_exp_f32 v15,v15\n"
"v_cvt_pk_bf16_f32 v70,v12,v13\n"
"v_exp_f32 v16,v16\n"
"v_cvt_pk_bf16_f32 v71,v14,v15\n"
"v_exp_f32 v17,v17\n"
"v_exp_f32 v18,v18\n"
"v_cvt_pk_bf16_f32 v72,v16,v17\n"
"v_exp_f32 v19,v19\n"
"v_exp_f32 v20,v20\n"
"v_cvt_pk_bf16_f32 v73,v18,v19\n"
"v_exp_f32 v21,v21\n"
"v_exp_f32 v22,v22\n"
"v_cvt_pk_bf16_f32 v74,v20,v21\n"
"v_exp_f32 v23,v23\n"
"v_exp_f32 v24,v24\n"
"v_cvt_pk_bf16_f32 v75,v22,v23\n"
"v_exp_f32 v25,v25\n"
"v_exp_f32 v26,v26\n"
"v_cvt_pk_bf16_f32 v76,v24,v25\n"
"v_exp_f32 v27,v27\n"
"v_exp_f32 v28,v28\n"
"v_cvt_pk_bf16_f32 v77,v26,v27\n"
"v_exp_f32 v29,v29\n"
"v_exp_f32 v30,v30\n"
"v_cvt_pk_bf16_f32 v78,v28,v29\n"
"v_exp_f32 v31,v31\n"
"s_nop 0\n"
"v_cvt_pk_bf16_f32 v79,v30,v31\n"
"v_accvgpr_read_b32 v122,a140\n"
"v_rcp_f32 v122,v122\n"
"s_nop 0\n"
"v_pk_mul_f32 v[224:225],v[224:225],v[122:123] op_sel_hi:[1,0]\n"
"v_pk_mul_f32 v[226:227],v[226:227],v[122:123] op_sel_hi:[1,0]\n"
"v_pk_mul_f32 v[228:229],v[228:229],v[122:123] op_sel_hi:[1,0]\n"
"v_pk_mul_f32 v[230:231],v[230:231],v[122:123] op_sel_hi:[1,0]\n"
"v_cvt_pk_bf16_f32 v224,v224,v225\n"
"v_cvt_pk_bf16_f32 v225,v226,v227\n"
"v_cvt_pk_bf16_f32 v226,v228,v229\n"
"v_cvt_pk_bf16_f32 v227,v230,v231\n"
"buffer_store_dwordx4 v[224:227],v112,s[24:27],s70 offen offset:0 sc1\n"
"v_pk_mul_f32 v[232:233],v[232:233],v[122:123] op_sel_hi:[1,0]\n"
"v_pk_mul_f32 v[234:235],v[234:235],v[122:123] op_sel_hi:[1,0]\n"
"v_pk_mul_f32 v[236:237],v[236:237],v[122:123] op_sel_hi:[1,0]\n"
"v_pk_mul_f32 v[238:239],v[238:239],v[122:123] op_sel_hi:[1,0]\n"
"v_cvt_pk_bf16_f32 v232,v232,v233\n"
"v_cvt_pk_bf16_f32 v233,v234,v235\n"
"v_cvt_pk_bf16_f32 v234,v236,v237\n"
"v_cvt_pk_bf16_f32 v235,v238,v239\n"
"buffer_store_dwordx4 v[232:235],v112,s[24:27],s70 offen offset:1024 sc1\n"
"v_pk_mul_f32 v[240:241],v[240:241],v[122:123] op_sel_hi:[1,0]\n"
"v_pk_mul_f32 v[242:243],v[242:243],v[122:123] op_sel_hi:[1,0]\n"
"v_pk_mul_f32 v[244:245],v[244:245],v[122:123] op_sel_hi:[1,0]\n"
"v_pk_mul_f32 v[246:247],v[246:247],v[122:123] op_sel_hi:[1,0]\n"
"v_cvt_pk_bf16_f32 v240,v240,v241\n"
"v_cvt_pk_bf16_f32 v241,v242,v243\n"
"v_cvt_pk_bf16_f32 v242,v244,v245\n"
"v_cvt_pk_bf16_f32 v243,v246,v247\n"
"buffer_store_dwordx4 v[240:243],v112,s[24:27],s70 offen offset:2048 sc1\n"
"v_pk_mul_f32 v[248:249],v[248:249],v[122:123] op_sel_hi:[1,0]\n"
"v_pk_mul_f32 v[250:251],v[250:251],v[122:123] op_sel_hi:[1,0]\n"
"v_pk_mul_f32 v[252:253],v[252:253],v[122:123] op_sel_hi:[1,0]\n"
"v_pk_mul_f32 v[254:255],v[254:255],v[122:123] op_sel_hi:[1,0]\n"
"v_cvt_pk_bf16_f32 v248,v248,v249\n"
"v_cvt_pk_bf16_f32 v249,v250,v251\n"
"v_cvt_pk_bf16_f32 v250,v252,v253\n"
"v_cvt_pk_bf16_f32 v251,v254,v255\n"
"buffer_store_dwordx4 v[248:251],v112,s[24:27],s70 offen offset:3072 sc1\n"
"s_add_u32 s70,s70,262144\n"
"s_mov_b32 s70,s68\n"
"s_load_dwordx2 s[36:37],s[0:1],0\n"
"s_load_dwordx2 s[44:45],s[0:1],80\n"
"s_load_dwordx2 s[48:49],s[0:1],88\n"
"s_lshl_b32 s82,s78,2\n"
"s_add_u32 s82,s82,65536\n"
"v_add_u32 v113,s82,v112\n"
"s_lshr_b32 s82,s78,2\n"
"s_add_u32 s82,s82,122880\n"
"v_lshrrev_b32 v115,1,v112\n"
"v_add_u32 v116,s82,v115\n"
"s_waitcnt lgkmcnt(0)\n"
"s_barrier\n"
"v_mfma_f32_16x16x32_bf16 v[128:131],a[96:99],v[64:67],0\n"
"s_add_u32 s83,s18,2097152\n"
"s_add_u32 m0,s30,32768\n"
"v_mfma_f32_16x16x32_bf16 a[128:131],a[144:147],v[64:67],0\n"
"buffer_load_dwordx4 v112,s[24:27],s83 offen offset:0 sc1 lds\n"
"v_mfma_f32_16x16x32_bf16 v[132:135],a[100:103],v[64:67],0\n"
"buffer_load_dwordx4 v112,s[24:27],s83 offen offset:1024 sc1 lds\n"
"v_mfma_f32_16x16x32_bf16 v[136:139],a[104:107],v[64:67],0\n"
"buffer_load_dwordx4 v112,s[24:27],s83 offen offset:2048 sc1 lds\n"
"v_mfma_f32_16x16x32_bf16 v[140:143],a[108:111],v[64:67],0\n"
"v_mfma_f32_16x16x32_bf16 v[144:147],a[112:115],v[64:67],0\n"
"buffer_load_dwordx4 v112,s[24:27],s83 offen offset:3072 sc1 lds\n"
"v_mfma_f32_16x16x32_bf16 v[148:151],a[116:119],v[64:67],0\n"
"s_add_u32 m0,m0,4096\n"
"v_mfma_f32_16x16x32_bf16 v[152:155],a[120:123],v[64:67],0\n"
"v_mfma_f32_16x16x32_bf16 v[156:159],a[124:127],v[64:67],0\n"
"buffer_load_dwordx4 v117,s[24:27],s83 offen offset:0 sc1 lds\n"
"v_mfma_f32_16x16x32_bf16 v[160:163],a[96:99],v[68:71],0\n"
"v_accvgpr_read_b32 v97,a128\n"
"v_cvt_pk_bf16_f32 v136,v136,v137\n"
"v_mfma_f32_16x16x32_bf16 a[132:135],a[144:147],v[68:71],0\n"
"buffer_load_dwordx4 v117,s[24:27],s83 offen offset:1024 sc1 lds\n"
"v_mfma_f32_16x16x32_bf16 v[164:167],a[100:103],v[68:71],0\n"
"v_cvt_pk_bf16_f32 v128,v128,v129\n"
"v_cvt_pk_bf16_f32 v129,v130,v131\n"
"v_mfma_f32_16x16x32_bf16 v[168:171],a[104:107],v[68:71],0\n"
"buffer_load_dwordx4 v117,s[24:27],s83 offen offset:2048 sc1 lds\n"
"v_cvt_pk_bf16_f32 v130,v132,v133\n"
"v_cvt_pk_bf16_f32 v131,v134,v135\n"
"v_mfma_f32_16x16x32_bf16 v[172:175],a[108:111],v[68:71],0\n"
"ds_write_b128 v113,v[128:131] offset:0\n"
"v_cvt_pk_bf16_f32 v137,v138,v139\n"
"v_mfma_f32_16x16x32_bf16 v[176:179],a[112:115],v[68:71],0\n"
"buffer_load_dwordx4 v117,s[24:27],s83 offen offset:3072 sc1 lds\n"
"v_cvt_pk_bf16_f32 v138,v140,v141\n"
"v_cvt_pk_bf16_f32 v139,v142,v143\n"
"v_mfma_f32_16x16x32_bf16 v[180:183],a[116:119],v[68:71],0\n"
"s_sub_u32 s82,s3,81920\n"
"ds_write_b128 v113,v[136:139] offset:1024\n"
"v_cvt_pk_bf16_f32 v144,v144,v145\n"
"v_mfma_f32_16x16x32_bf16 v[184:187],a[120:123],v[68:71],0\n"
"buffer_load_dwordx4 a[32:35],v112,s[24:27],s82 offen offset:0 sc1\n"
"v_cvt_pk_bf16_f32 v145,v146,v147\n"
"v_cvt_pk_bf16_f32 v146,v148,v149\n"
"v_cvt_pk_bf16_f32 v147,v150,v151\n"
"v_mfma_f32_16x16x32_bf16 v[188:191],a[124:127],v[68:71],0\n"
"ds_write_b128 v113,v[144:147] offset:2048\n"
"v_cvt_pk_bf16_f32 v152,v152,v153\n"
"v_mfma_f32_16x16x32_bf16 v[192:195],a[96:99],v[72:75],0\n"
"buffer_load_dwordx4 a[36:39],v112,s[24:27],s82 offen offset:1024 sc1\n"
"v_cvt_pk_bf16_f32 v153,v154,v155\n"
"v_cvt_pk_bf16_f32 v154,v156,v157\n"
"v_cvt_pk_bf16_f32 v155,v158,v159\n"
"v_mfma_f32_16x16x32_bf16 a[136:139],a[144:147],v[72:75],0\n"
"ds_write_b128 v113,v[152:155] offset:3072\n"
"ds_write_b64 v116,v[96:97] offset:0\n"
"v_mfma_f32_16x16x32_bf16 v[196:199],a[100:103],v[72:75],0\n"
"buffer_load_dwordx4 a[40:43],v112,s[24:27],s82 offen offset:2048 sc1\n"
"v_accvgpr_read_b32 v101,a132\n"
"v_cvt_pk_bf16_f32 v160,v160,v161\n"
"v_mfma_f32_16x16x32_bf16 v[200:203],a[104:107],v[72:75],0\n"
"v_cvt_pk_bf16_f32 v161,v162,v163\n"
"v_cvt_pk_bf16_f32 v162,v164,v165\n"
"v_cvt_pk_bf16_f32 v163,v166,v167\n"
"v_mfma_f32_16x16x32_bf16 v[204:207],a[108:111],v[72:75],0\n"
"ds_write_b128 v113,v[160:163] offset:4096\n"
"v_cvt_pk_bf16_f32 v168,v168,v169\n"
"v_cvt_pk_bf16_f32 v169,v170,v171\n"
"v_mfma_f32_16x16x32_bf16 v[208:211],a[112:115],v[72:75],0\n"
"buffer_load_dwordx4 a[44:47],v112,s[24:27],s82 offen offset:3072 sc1\n"
"v_cvt_pk_bf16_f32 v170,v172,v173\n"
"v_cvt_pk_bf16_f32 v171,v174,v175\n"
"v_mfma_f32_16x16x32_bf16 v[212:215],a[116:119],v[72:75],0\n"
"ds_write_b128 v113,v[168:171] offset:5120\n"
"v_cvt_pk_bf16_f32 v176,v176,v177\n"
"v_cvt_pk_bf16_f32 v177,v178,v179\n"
"v_mfma_f32_16x16x32_bf16 v[216:219],a[120:123],v[72:75],0\n"
"v_cvt_pk_bf16_f32 v178,v180,v181\n"
"v_cvt_pk_bf16_f32 v179,v182,v183\n"
"v_mfma_f32_16x16x32_bf16 v[220:223],a[124:127],v[72:75],0\n"
"ds_write_b128 v113,v[176:179] offset:6144\n"
"v_cvt_pk_bf16_f32 v184,v184,v185\n"
"v_cvt_pk_bf16_f32 v185,v186,v187\n"
"v_cvt_pk_bf16_f32 v186,v188,v189\n"
"v_cvt_pk_bf16_f32 v187,v190,v191\n"
"v_mfma_f32_16x16x32_bf16 v[224:227],a[96:99],v[76:79],0\n"
"ds_write_b128 v113,v[184:187] offset:7168\n"
"v_mfma_f32_16x16x32_bf16 a[140:143],a[144:147],v[76:79],0\n"
"s_sub_u32 s82,s3,131072\n"
"ds_write_b64 v116,v[100:101] offset:512\n"
"buffer_load_dwordx4 v[128:131],v112,s[24:27],s82 offen offset:0 sc1\n"
"buffer_load_dwordx4 v[132:135],v112,s[24:27],s82 offen offset:1024 sc1\n"
"v_mfma_f32_16x16x32_bf16 v[228:231],a[100:103],v[76:79],0\n"
"buffer_load_dwordx4 v[136:139],v112,s[24:27],s82 offen offset:2048 sc1\n"
"buffer_load_dwordx4 v[140:143],v112,s[24:27],s82 offen offset:3072 sc1\n"
"s_sub_u32 s82,s3,147456\n"
"v_mfma_f32_16x16x32_bf16 v[232:235],a[104:107],v[76:79],0\n"
"buffer_load_dwordx4 v[144:147],v112,s[24:27],s82 offen offset:0 sc1\n"
"buffer_load_dwordx4 v[148:151],v112,s[24:27],s82 offen offset:1024 sc1\n"
"buffer_load_dwordx4 v[152:155],v112,s[24:27],s82 offen offset:2048 sc1\n"
"v_mfma_f32_16x16x32_bf16 v[236:239],a[108:111],v[76:79],0\n"
"buffer_load_dwordx4 v[156:159],v112,s[24:27],s82 offen offset:3072 sc1\n"
"v_mfma_f32_16x16x32_bf16 v[240:243],a[112:115],v[76:79],0\n"
"v_mfma_f32_16x16x32_bf16 v[244:247],a[116:119],v[76:79],0\n"
"v_mfma_f32_16x16x32_bf16 v[248:251],a[120:123],v[76:79],0\n"
"v_mfma_f32_16x16x32_bf16 v[252:255],a[124:127],v[76:79],0\n"
"s_waitcnt vmcnt(20)\n"
"buffer_store_dwordx2 a[154:155],off,s[24:27],s69 sc1\n"
"s_branch .Lfk_endB\n"
".Lfk_head0:\n"
"s_mov_b32 s64,s61\n"
"s_cmp_eq_u32 s52,3\n"
"s_cselect_b32 s67,172032,0\n"
"s_waitcnt lgkmcnt(0)\n"
"s_barrier\n"
"v_mfma_f32_16x16x32_bf16 v[24:27],a[64:67],a[48:51],v[108:111]\n"
"v_add_f32 v0,0x80000000,v126\n"
"v_min_f32 v0,v0,v96\n"
"v_mfma_f32_16x16x32_bf16 v[24:27],a[68:71],a[52:55],v[24:27]\n"
"v_add_f32 v1,0xf149f2ca,v126\n"
"v_min_f32 v1,v1,v97\n"
"v_mfma_f32_16x16x32_bf16 v[24:27],a[72:75],a[56:59],v[24:27]\n"
"v_add_f32 v2,0xf1c9f2ca,v126\n"
"v_min_f32 v2,v2,v98\n"
"v_mfma_f32_16x16x32_bf16 v[24:27],a[76:79],a[60:63],v[24:27]\n"
"v_add_f32 v3,0xf2177617,v126\n"
"v_min_f32 v3,v3,v99\n"
"v_mfma_f32_16x16x32_bf16 v[28:31],a[92:95],a[60:63],v[108:111]\n"
"v_add_f32 v4,0xf349f2ca,v126\n"
"v_min_f32 v4,v4,v96\n"
"v_mfma_f32_16x16x32_bf16 v[28:31],a[88:91],a[56:59],v[28:31]\n"
"v_add_f32 v5,0xf35691f6,v126\n"
"v_min_f32 v5,v5,v97\n"
"v_mfma_f32_16x16x32_bf16 v[28:31],a[84:87],a[52:55],v[28:31]\n"
"v_add_f32 v6,0xf3633123,v126\n"
"v_min_f32 v6,v6,v98\n"
"v_mfma_f32_16x16x32_bf16 v[28:31],a[80:83],a[48:51],v[28:31]\n"
"v_add_f32 v7,0xf36fd050,v126\n"
"v_min_f32 v7,v7,v99\n"
"s_nop 1\n"
"v_mfma_f32_16x16x32_bf16 v[4:7],a[80:83],a[0:3],v[4:7]\n"
"s_andn2_b32 m0,s55,16384\n"
"s_add_u32 s54,s54,s22\n"
"v_mfma_f32_16x16x32_bf16 v[4:7],a[84:87],a[4:7],v[4:7]\n"
"ds_read_b128 a[96:99],v113 offset:16384\n"
"ds_read_b128 a[100:103],v113 offset:17408\n"
"v_mfma_f32_16x16x32_bf16 v[4:7],a[88:91],a[8:11],v[4:7]\n"
"ds_read_b128 a[104:107],v113 offset:18432\n"
"ds_read_b128 a[108:111],v113 offset:19456\n"
"v_mfma_f32_16x16x32_bf16 v[4:7],a[92:95],a[12:15],v[4:7]\n"
"ds_read_b128 a[112:115],v113 offset:20480\n"
"ds_read_b128 a[116:119],v113 offset:21504\n"
"v_mfma_f32_16x16x32_bf16 v[0:3],a[76:79],a[12:15],v[0:3]\n"
"ds_read_b128 a[120:123],v113 offset:22528\n"
"ds_read_b128 a[124:127],v113 offset:23552\n"
"v_mfma_f32_16x16x32_bf16 v[0:3],a[72:75],a[8:11],v[0:3]\n"
"buffer_load_dwordx4 v115,s[32:35],s54 offen sc1 lds\n"
"v_mfma_f32_16x16x32_bf16 v[0:3],a[68:71],a[4:7],v[0:3]\n"
"s_add_u32 m0,s67,0\n"
"v_max3_f32 v118,v24,v25,v26\n"
"v_mfma_f32_16x16x32_bf16 v[0:3],a[64:67],a[0:3],v[0:3]\n"
"buffer_load_dwordx4 v112,s[24:27],s64 offen offset:0 sc1 lds\n"
"v_mfma_f32_16x16x32_bf16 v[8:11],a[64:67],a[16:19],v[100:103]\n"
"buffer_load_dwordx4 v112,s[24:27],s64 offen offset:1024 sc1 lds\n"
"v_mfma_f32_16x16x32_bf16 v[8:11],a[68:71],a[20:23],v[8:11]\n"
"buffer_load_dwordx4 v112,s[24:27],s64 offen offset:2048 sc1 lds\n"
"v_mfma_f32_16x16x32_bf16 v[8:11],a[72:75],a[24:27],v[8:11]\n"
"buffer_load_dwordx4 v112,s[24:27],s64 offen offset:3072 sc1 lds\n"
"v_mfma_f32_16x16x32_bf16 v[8:11],a[76:79],a[28:31],v[8:11]\n"
"s_add_u32 m0,s67,4096\n"
"v_max3_f32 v118,v118,v27,v28\n"
"v_cvt_pk_bf16_f32 v192,v192,v193\n"
"v_mfma_f32_16x16x32_bf16 v[12:15],a[92:95],a[28:31],v[100:103]\n"
"buffer_load_dwordx4 v117,s[24:27],s64 offen offset:0 sc1 lds\n"
"v_mfma_f32_16x16x32_bf16 v[12:15],a[88:91],a[24:27],v[12:15]\n"
"buffer_load_dwordx4 v117,s[24:27],s64 offen offset:1024 sc1 lds\n"
"v_mfma_f32_16x16x32_bf16 v[12:15],a[84:87],a[20:23],v[12:15]\n"
"buffer_load_dwordx4 v117,s[24:27],s64 offen offset:2048 sc1 lds\n"
"v_mfma_f32_16x16x32_bf16 v[12:15],a[80:83],a[16:19],v[12:15]\n"
"buffer_load_dwordx4 v117,s[24:27],s64 offen offset:3072 sc1 lds\n"
"v_cvt_pk_bf16_f32 v193,v194,v195\n"
"v_cvt_pk_bf16_f32 v194,v196,v197\n"
"v_cvt_pk_bf16_f32 v195,v198,v199\n"
"v_cvt_pk_bf16_f32 v200,v200,v201\n"
"v_cvt_pk_bf16_f32 v201,v202,v203\n"
"v_cvt_pk_bf16_f32 v202,v204,v205\n"
"v_cvt_pk_bf16_f32 v203,v206,v207\n"
"ds_write_b128 v116,v[192:195] offset:32768\n"
"ds_write_b128 v116,v[200:203] offset:33792\n"
"v_cvt_pk_bf16_f32 v208,v208,v209\n"
"v_cvt_pk_bf16_f32 v209,v210,v211\n"
"v_cvt_pk_bf16_f32 v210,v212,v213\n"
"v_cvt_pk_bf16_f32 v211,v214,v215\n"
"v_cvt_pk_bf16_f32 v216,v216,v217\n"
"v_cvt_pk_bf16_f32 v217,v218,v219\n"
"v_cvt_pk_bf16_f32 v218,v220,v221\n"
"v_cvt_pk_bf16_f32 v219,v222,v223\n"
"ds_write_b128 v116,v[208:211] offset:34816\n"
"ds_write_b128 v116,v[216:219] offset:35840\n"
"ds_read_b128 a[32:35],v116 offset:32768\n"
"ds_read_b128 a[36:39],v116 offset:33792\n"
"ds_read_b128 a[40:43],v116 offset:34816\n"
"ds_read_b128 a[44:47],v116 offset:35840\n"
"s_waitcnt lgkmcnt(0)\n"
"v_mfma_f32_16x16x32_bf16 v[20:23],a[80:83],a[32:35],v[104:107]\n"
"s_add_u32 s64,s64,16384\n"
"v_max3_f32 v118,v118,v29,v30\n"
"v_mfma_f32_16x16x32_bf16 v[20:23],a[84:87],a[36:39],v[20:23]\n"
"v_max_f32 v118,v118,v31\n"
"v_mov_b32 v120,v118\n"
"v_mfma_f32_16x16x32_bf16 v[20:23],a[88:91],a[40:43],v[20:23]\n"
"v_max3_f32 v119,v0,v1,v2\n"
"v_max3_f32 v119,v119,v3,v4\n"
"v_mfma_f32_16x16x32_bf16 v[20:23],a[92:95],a[44:47],v[20:23]\n"
"buffer_store_dwordx4 a[224:227],v112,s[24:27],s56 offen offset:0 sc1\n"
"v_max3_f32 v119,v119,v5,v6\n"
"v_mfma_f32_16x16x32_bf16 v[16:19],a[76:79],a[44:47],v[104:107]\n"
"v_max_f32 v119,v119,v7\n"
"v_cmp_gt_f32 s[28:29],s16,v119\n"
"v_mfma_f32_16x16x32_bf16 v[16:19],a[72:75],a[40:43],v[16:19]\n"
"v_max_f32 v118,v118,v119\n"
"v_max3_f32 v119,v8,v9,v10\n"
"v_mfma_f32_16x16x32_bf16 v[16:19],a[68:71],a[36:39],v[16:19]\n"
"v_max3_f32 v119,v119,v11,v12\n"
"v_max3_f32 v119,v119,v13,v14\n"
"v_mfma_f32_16x16x32_bf16 v[16:19],a[64:67],a[32:35],v[16:19]\n"
"s_and_b32 s28,s28,0xffff\n"
"v_max_f32 v119,v119,v15\n"
"v_min_f32 v120,v120,v119\n"
"v_max_f32 v118,v118,v119\n"
"s_nop 3\n"
"v_max3_f32 v119,v16,v17,v18\n"
"v_max3_f32 v119,v119,v19,v20\n"
"v_max3_f32 v119,v119,v21,v22\n"
"v_max_f32 v119,v119,v23\n"
"v_min_f32 v120,v120,v119\n"
"v_max_f32 v118,v118,v119\n"
"v_cmp_gt_f32 s[96:97],s16,v120\n"
"v_cmp_lt_f32 vcc,s93,v118\n"
"s_or_b32 s96,s96,s28\n"
"s_or_b64 s[96:97],s[96:97],vcc\n"
"s_cbranch_scc1 .Lfk_cs_z3\n"
".Lfk_cr_z3:\n"
"s_waitcnt vmcnt(19) lgkmcnt(0)\n"
"s_barrier\n"
"v_mfma_f32_16x16x32_bf16 v[48:51],a[96:99],a[32:35],v[104:107]\n"
"v_add_f32 v40,0x80000000,v126\n"
"v_min_f32 v40,v40,v100\n"
"v_add_f32 v41,0xf149f2ca,v126\n"
"v_min_f32 v41,v41,v101\n"
"v_mfma_f32_16x16x32_bf16 v[48:51],a[100:103],a[36:39],v[48:51]\n"
"v_add_f32 v42,0xf1c9f2ca,v126\n"
"v_min_f32 v42,v42,v102\n"
"v_add_f32 v43,0xf2177617,v126\n"
"v_min_f32 v43,v43,v103\n"
"v_mfma_f32_16x16x32_bf16 v[48:51],a[104:107],a[40:43],v[48:51]\n"
"v_add_f32 v44,0xf349f2ca,v126\n"
"v_min_f32 v44,v44,v100\n"
"v_add_f32 v45,0xf35691f6,v126\n"
"v_min_f32 v45,v45,v101\n"
"v_mfma_f32_16x16x32_bf16 v[48:51],a[108:111],a[44:47],v[48:51]\n"
"v_add_f32 v46,0xf3633123,v126\n"
"v_min_f32 v46,v46,v102\n"
"v_add_f32 v47,0xf36fd050,v126\n"
"v_min_f32 v47,v47,v103\n"
"v_mfma_f32_16x16x32_bf16 v[52:55],a[124:127],a[44:47],v[104:107]\n"
"s_add_u32 m0,s55,4096\n"
"v_exp_f32 v0,v0\n"
"s_add_u32 s54,s54,s22\n"
"ds_read_b128 a[0:3],v116 offset:40960\n"
"v_exp_f32 v1,v1\n"
"v_mfma_f32_16x16x32_bf16 v[52:55],a[120:123],a[40:43],v[52:55]\n"
"buffer_load_dwordx4 v115,s[32:35],s54 offen sc1 lds\n"
"v_mfma_f32_16x16x32_bf16 v[52:55],a[116:119],a[36:39],v[52:55]\n"
"s_add_u32 m0,s67,16384\n"
"v_exp_f32 v2,v2\n"
"v_mfma_f32_16x16x32_bf16 v[52:55],a[112:115],a[32:35],v[52:55]\n"
"buffer_load_dwordx4 v112,s[24:27],s64 offen offset:0 sc1 lds\n"
"buffer_load_dwordx4 v112,s[24:27],s64 offen offset:1024 sc1 lds\n"
"v_exp_f32 v3,v3\n"
"v_exp_f32 v4,v4\n"
"v_mfma_f32_16x16x32_bf16 v[60:63],a[112:115],a[48:51],v[108:111]\n"
"buffer_load_dwordx4 v112,s[24:27],s64 offen offset:2048 sc1 lds\n"
"v_exp_f32 v5,v5\n"
"v_mfma_f32_16x16x32_bf16 v[60:63],a[116:119],a[52:55],v[60:63]\n"
"ds_read_b128 a[64:67],v113 offset:49152\n"
"ds_read_b128 a[68:71],v113 offset:50176\n"
"v_exp_f32 v6,v6\n"
"v_mfma_f32_16x16x32_bf16 v[60:63],a[120:123],a[56:59],v[60:63]\n"
"ds_read_b128 a[72:75],v113 offset:51200\n"
"ds_read_b128 a[76:79],v113 offset:52224\n"
"v_exp_f32 v7,v7\n"
"v_mfma_f32_16x16x32_bf16 v[60:63],a[124:127],a[60:63],v[60:63]\n"
"ds_read_b128 a[80:83],v113 offset:53248\n"
"ds_read_b128 a[84:87],v113 offset:54272\n"
"v_exp_f32 v8,v8\n"
"v_mfma_f32_16x16x32_bf16 v[56:59],a[108:111],a[60:63],v[108:111]\n"
"ds_read_b128 a[88:91],v113 offset:55296\n"
"ds_read_b128 a[92:95],v113 offset:56320\n"
"v_exp_f32 v9,v9\n"
"v_mfma_f32_16x16x32_bf16 v[56:59],a[104:107],a[56:59],v[56:59]\n"
"buffer_load_dwordx4 v112,s[24:27],s64 offen offset:3072 sc1 lds\n"
"v_mfma_f32_16x16x32_bf16 v[56:59],a[100:103],a[52:55],v[56:59]\n"
"s_add_u32 m0,s67,20480\n"
"v_exp_f32 v10,v10\n"
"v_mfma_f32_16x16x32_bf16 v[56:59],a[96:99],a[48:51],v[56:59]\n"
"buffer_load_dwordx4 v117,s[24:27],s64 offen offset:0 sc1 lds\n"
"buffer_load_dwordx4 v117,s[24:27],s64 offen offset:1024 sc1 lds\n"
"v_exp_f32 v11,v11\n"
"v_exp_f32 v12,v12\n"
"v_mfma_f32_16x16x32_bf16 v[40:43],a[96:99],a[16:19],v[40:43]\n"
"buffer_load_dwordx4 v117,s[24:27],s64 offen offset:2048 sc1 lds\n"
"v_exp_f32 v13,v13\n"
"v_mfma_f32_16x16x32_bf16 v[40:43],a[100:103],a[20:23],v[40:43]\n"
"buffer_store_dwordx4 a[168:171],v112,s[24:27],s3 offen offset:2048 sc1\n"
"v_cvt_pk_bf16_f32 v64,v0,v1\n"
"v_exp_f32 v14,v14\n"
"v_mfma_f32_16x16x32_bf16 v[40:43],a[104:107],a[24:27],v[40:43]\n"
"buffer_load_dwordx4 v117,s[24:27],s64 offen offset:3072 sc1 lds\n"
"v_mfma_f32_16x16x32_bf16 v[40:43],a[108:111],a[28:31],v[40:43]\n"
"s_add_u32 s64,s64,16384\n"
"v_exp_f32 v15,v15\n"
"v_cvt_pk_bf16_f32 v65,v2,v3\n"
"v_cvt_pk_bf16_f32 v66,v4,v5\n"
"v_exp_f32 v16,v16\n"
"v_mfma_f32_16x16x32_bf16 v[44:47],a[124:127],a[28:31],v[44:47]\n"
"buffer_store_dwordx4 a[228:231],v112,s[24:27],s56 offen offset:1024 sc1\n"
"v_cvt_pk_bf16_f32 v67,v6,v7\n"
"v_mfma_f32_16x16x32_bf16 v[44:47],a[120:123],a[24:27],v[44:47]\n"
"v_exp_f32 v17,v17\n"
"v_cvt_pk_bf16_f32 v68,v8,v9\n"
"v_cvt_pk_bf16_f32 v69,v10,v11\n"
"v_mfma_f32_16x16x32_bf16 v[44:47],a[116:119],a[20:23],v[44:47]\n"
"v_exp_f32 v18,v18\n"
"v_cvt_pk_bf16_f32 v70,v12,v13\n"
"v_cvt_pk_bf16_f32 v71,v14,v15\n"
"v_mfma_f32_16x16x32_bf16 v[44:47],a[112:115],a[16:19],v[44:47]\n"
"v_exp_f32 v19,v19\n"
"v_cvt_pk_bf16_f32 v72,v16,v17\n"
"v_max3_f32 v118,v48,v49,v50\n"
"v_exp_f32 v20,v20\n"
"v_cvt_pk_bf16_f32 v73,v18,v19\n"
"v_exp_f32 v21,v21\n"
"ds_read_b128 a[4:7],v116 offset:41984\n"
"ds_read_b128 a[8:11],v116 offset:43008\n"
"ds_read_b128 a[12:15],v116 offset:44032\n"
"v_exp_f32 v22,v22\n"
"v_exp_f32 v23,v23\n"
"v_exp_f32 v24,v24\n"
"v_cvt_pk_bf16_f32 v75,v22,v23\n"
"v_exp_f32 v25,v25\n"
"v_exp_f32 v26,v26\n"
"v_cvt_pk_bf16_f32 v76,v24,v25\n"
"v_exp_f32 v27,v27\n"
"v_exp_f32 v28,v28\n"
"v_cvt_pk_bf16_f32 v77,v26,v27\n"
"v_exp_f32 v29,v29\n"
"v_exp_f32 v30,v30\n"
"v_cvt_pk_bf16_f32 v78,v28,v29\n"
"v_exp_f32 v31,v31\n"
"v_cvt_pk_bf16_f32 v74,v20,v21\n"
"v_cvt_pk_bf16_f32 v79,v30,v31\n"
"v_max3_f32 v118,v118,v51,v52\n"
"v_max3_f32 v118,v118,v53,v54\n"
"v_max3_f32 v118,v118,v55,v60\n"
"v_max3_f32 v118,v118,v61,v62\n"
"v_max3_f32 v118,v118,v63,v56\n"
"v_max3_f32 v118,v118,v57,v58\n"
"v_max3_f32 v118,v118,v59,v40\n"
"v_max3_f32 v118,v118,v41,v42\n"
"v_max3_f32 v118,v118,v43,v44\n"
"v_max3_f32 v118,v118,v45,v46\n"
"v_max_f32 v118,v118,v47\n"
"v_cmp_lt_f32 vcc,s93,v118\n"
"s_nop 0\n"
"s_cbranch_vccnz .Lfk_cs_z0\n"
".Lfk_cr_z0:\n"
"s_waitcnt vmcnt(26) lgkmcnt(0)\n"
"s_barrier\n"
"v_mfma_f32_16x16x32_bf16 v[24:27],a[64:67],a[48:51],v[108:111]\n"
"ds_read_b128 a[96:99],v114 offset:40960\n"
"ds_read_b128 a[100:103],v114 offset:41984\n"
"v_mfma_f32_16x16x32_bf16 v[24:27],a[68:71],a[52:55],v[24:27]\n"
"ds_read_b128 a[104:107],v114 offset:43008\n"
"ds_read_b128 a[108:111],v114 offset:44032\n"
"v_mfma_f32_16x16x32_bf16 v[24:27],a[72:75],a[56:59],v[24:27]\n"
"ds_read_b128 a[112:115],v114 offset:45056\n"
"ds_read_b128 a[116:119],v114 offset:46080\n"
"v_mfma_f32_16x16x32_bf16 v[24:27],a[76:79],a[60:63],v[24:27]\n"
"ds_read_b128 a[120:123],v114 offset:47104\n"
"ds_read_b128 a[124:127],v114 offset:48128\n"
"v_mfma_f32_16x16x32_bf16 v[28:31],a[92:95],a[60:63],v[108:111]\n"
"v_add_f32 v16,0x80000000,v126\n"
"v_min_f32 v16,v16,v104\n"
"v_mfma_f32_16x16x32_bf16 v[28:31],a[88:91],a[56:59],v[28:31]\n"
"v_add_f32 v17,0xf149f2ca,v126\n"
"v_min_f32 v17,v17,v105\n"
"v_mfma_f32_16x16x32_bf16 v[28:31],a[84:87],a[52:55],v[28:31]\n"
"v_add_f32 v18,0xf1c9f2ca,v126\n"
"v_min_f32 v18,v18,v106\n"
"v_mfma_f32_16x16x32_bf16 v[28:31],a[80:83],a[48:51],v[28:31]\n"
"v_add_f32 v20,0xf349f2ca,v126\n"
"v_min_f32 v20,v20,v104\n"
"v_add_f32 v21,0xf35691f6,v126\n"
"v_min_f32 v21,v21,v105\n"
"v_add_f32 v22,0xf3633123,v126\n"
"v_min_f32 v22,v22,v106\n"
"v_add_f32 v23,0xf36fd050,v126\n"
"v_min_f32 v23,v23,v107\n"
"v_add_f32 v19,0xf2177617,v126\n"
"v_min_f32 v19,v19,v107\n"
"v_mfma_f32_16x16x32_bf16 v[20:23],a[80:83],a[32:35],v[20:23]\n"
"s_add_u32 m0,s55,8192\n"
"v_exp_f32 v40,v40\n"
"v_mfma_f32_16x16x32_bf16 v[20:23],a[84:87],a[36:39],v[20:23]\n"
"s_add_u32 s54,s54,s22\n"
"v_exp_f32 v41,v41\n"
"v_mfma_f32_16x16x32_bf16 v[20:23],a[88:91],a[40:43],v[20:23]\n"
"v_exp_f32 v42,v42\n"
"v_mfma_f32_16x16x32_bf16 v[20:23],a[92:95],a[44:47],v[20:23]\n"
"v_exp_f32 v43,v43\n"
"v_mfma_f32_16x16x32_bf16 v[16:19],a[76:79],a[44:47],v[16:19]\n"
"v_exp_f32 v44,v44\n"
"v_mfma_f32_16x16x32_bf16 v[16:19],a[72:75],a[40:43],v[16:19]\n"
"v_exp_f32 v45,v45\n"
"v_mfma_f32_16x16x32_bf16 v[16:19],a[68:71],a[36:39],v[16:19]\n"
"v_exp_f32 v46,v46\n"
"v_mfma_f32_16x16x32_bf16 v[16:19],a[64:67],a[32:35],v[16:19]\n"
"s_waitcnt lgkmcnt(0)\n"
"v_exp_f32 v47,v47\n"
"v_mfma_f32_16x16x32_bf16 v[128:131],a[96:99],v[64:67],0\n"
"v_exp_f32 v48,v48\n"
"v_mfma_f32_16x16x32_bf16 v[160:163],a[96:99],v[68:71],0\n"
"v_exp_f32 v49,v49\n"
"v_mfma_f32_16x16x32_bf16 v[192:195],a[96:99],v[72:75],0\n"
"ds_read_b128 a[64:67],v113 offset:32768\n"
"ds_read_b128 a[68:71],v113 offset:33792\n"
"v_mfma_f32_16x16x32_bf16 v[224:227],a[96:99],v[76:79],0\n"
"v_exp_f32 v50,v50\n"
"v_mfma_f32_16x16x32_bf16 a[140:143],a[144:147],v[76:79],0\n"
"ds_read_b128 a[72:75],v113 offset:34816\n"
"ds_read_b128 a[76:79],v113 offset:35840\n"
"v_mfma_f32_16x16x32_bf16 v[228:231],a[100:103],v[76:79],0\n"
"v_exp_f32 v51,v51\n"
"v_mfma_f32_16x16x32_bf16 v[132:135],a[100:103],v[64:67],0\n"
"ds_read_b128 a[80:83],v113 offset:36864\n"
"ds_read_b128 a[84:87],v113 offset:37888\n"
"v_mfma_f32_16x16x32_bf16 v[164:167],a[100:103],v[68:71],0\n"
"v_exp_f32 v52,v52\n"
"v_mfma_f32_16x16x32_bf16 v[196:199],a[100:103],v[72:75],0\n"
"v_exp_f32 v53,v53\n"
"v_mfma_f32_16x16x32_bf16 a[136:139],a[144:147],v[72:75],0\n"
"ds_read_b128 a[88:91],v113 offset:38912\n"
"ds_read_b128 a[92:95],v113 offset:39936\n"
"v_mfma_f32_16x16x32_bf16 v[200:203],a[104:107],v[72:75],0\n"
"v_exp_f32 v54,v54\n"
"v_mfma_f32_16x16x32_bf16 v[232:235],a[104:107],v[76:79],0\n"
"buffer_load_dwordx4 v115,s[32:35],s54 offen sc1 lds\n"
"v_mfma_f32_16x16x32_bf16 v[136:139],a[104:107],v[64:67],0\n"
"s_add_u32 m0,s67,49152\n"
"v_exp_f32 v55,v55\n"
"v_mfma_f32_16x16x32_bf16 v[168:171],a[104:107],v[68:71],0\n"
"buffer_load_dwordx4 v112,s[24:27],s64 offen offset:0 sc1 lds\n"
"v_mfma_f32_16x16x32_bf16 a[132:135],a[144:147],v[68:71],0\n"
"v_exp_f32 v56,v56\n"
"v_mfma_f32_16x16x32_bf16 v[172:175],a[108:111],v[68:71],0\n"
"buffer_load_dwordx4 v112,s[24:27],s64 offen offset:1024 sc1 lds\n"
"v_mfma_f32_16x16x32_bf16 v[204:207],a[108:111],v[72:75],0\n"
"v_exp_f32 v57,v57\n"
"v_mfma_f32_16x16x32_bf16 v[236:239],a[108:111],v[76:79],0\n"
"buffer_load_dwordx4 v112,s[24:27],s64 offen offset:2048 sc1 lds\n"
"v_mfma_f32_16x16x32_bf16 v[140:143],a[108:111],v[64:67],0\n"
"v_exp_f32 v58,v58\n"
"v_mfma_f32_16x16x32_bf16 a[128:131],a[144:147],v[64:67],0\n"
"buffer_load_dwordx4 v112,s[24:27],s64 offen offset:3072 sc1 lds\n"
"v_mfma_f32_16x16x32_bf16 v[144:147],a[112:115],v[64:67],0\n"
"s_add_u32 m0,s67,53248\n"
"v_exp_f32 v59,v59\n"
"v_mfma_f32_16x16x32_bf16 v[176:179],a[112:115],v[68:71],0\n"
"buffer_load_dwordx4 v117,s[24:27],s64 offen offset:0 sc1 lds\n"
"v_mfma_f32_16x16x32_bf16 v[208:211],a[112:115],v[72:75],0\n"
"v_exp_f32 v60,v60\n"
"v_mfma_f32_16x16x32_bf16 v[240:243],a[112:115],v[76:79],0\n"
"buffer_load_dwordx4 v117,s[24:27],s64 offen offset:1024 sc1 lds\n"
"v_mfma_f32_16x16x32_bf16 v[244:247],a[116:119],v[76:79],0\n"
"v_exp_f32 v61,v61\n"
"v_mfma_f32_16x16x32_bf16 v[148:151],a[116:119],v[64:67],0\n"
"buffer_load_dwordx4 v117,s[24:27],s64 offen offset:2048 sc1 lds\n"
"v_mfma_f32_16x16x32_bf16 v[180:183],a[116:119],v[68:71],0\n"
"v_exp_f32 v62,v62\n"
"v_mfma_f32_16x16x32_bf16 v[212:215],a[116:119],v[72:75],0\n"
"buffer_load_dwordx4 v117,s[24:27],s64 offen offset:3072 sc1 lds\n"
"v_mfma_f32_16x16x32_bf16 v[216:219],a[120:123],v[72:75],0\n"
"s_add_u32 s64,s64,16384\n"
"v_exp_f32 v63,v63\n"
"v_mfma_f32_16x16x32_bf16 v[248:251],a[120:123],v[76:79],0\n"
"buffer_store_dwordx4 a[232:235],v112,s[24:27],s56 offen offset:2048 sc1\n"
"s_and_b32 s67,s67,8192\n"
"v_mfma_f32_16x16x32_bf16 v[152:155],a[120:123],v[64:67],0\n"
"buffer_store_dwordx4 a[172:175],v112,s[24:27],s3 offen offset:3072 sc1\n"
"s_sub_u32 s3,s3,16384\n"
"v_mfma_f32_16x16x32_bf16 v[184:187],a[120:123],v[68:71],0\n"
"buffer_store_dwordx4 a[176:179],v112,s[24:27],s3 offen offset:0 sc1\n"
"v_cvt_pk_bf16_f32 v84,v40,v41\n"
"v_mfma_f32_16x16x32_bf16 v[188:191],a[124:127],v[68:71],0\n"
"v_cvt_pk_bf16_f32 v85,v42,v43\n"
"v_cvt_pk_bf16_f32 v86,v44,v45\n"
"v_mfma_f32_16x16x32_bf16 v[220:223],a[124:127],v[72:75],0\n"
"v_cvt_pk_bf16_f32 v87,v46,v47\n"
"v_cvt_pk_bf16_f32 v88,v48,v49\n"
"v_mfma_f32_16x16x32_bf16 v[252:255],a[124:127],v[76:79],0\n"
"v_cvt_pk_bf16_f32 v89,v50,v51\n"
"v_cvt_pk_bf16_f32 v90,v52,v53\n"
"v_mfma_f32_16x16x32_bf16 v[156:159],a[124:127],v[64:67],0\n"
"v_cvt_pk_bf16_f32 v91,v54,v55\n"
"v_cvt_pk_bf16_f32 v92,v56,v57\n"
"v_cvt_pk_bf16_f32 v93,v58,v59\n"
"ds_read_b128 a[16:19],v116 offset:8192\n"
"ds_read_b128 a[20:23],v116 offset:9216\n"
"ds_read_b128 a[24:27],v116 offset:10240\n"
"ds_read_b128 a[28:31],v116 offset:11264\n"
"v_cvt_pk_bf16_f32 v94,v60,v61\n"
"v_cvt_pk_bf16_f32 v95,v62,v63\n"
"v_max3_f32 v118,v24,v25,v26\n"
"v_max3_f32 v118,v118,v27,v28\n"
"v_max3_f32 v118,v118,v29,v30\n"
"v_max3_f32 v118,v118,v31,v20\n"
"v_max3_f32 v118,v118,v21,v22\n"
"v_max3_f32 v118,v118,v23,v16\n"
"v_max3_f32 v118,v118,v17,v18\n"
"v_max_f32 v118,v118,v19\n"
"v_cmp_lt_f32 vcc,s93,v118\n"
"s_nop 0\n"
"s_cbranch_vccnz .Lfk_cs_z1\n"
".Lfk_cr_z1:\n"
"v_mov_b32 v113,v112\n"
"s_waitcnt vmcnt(21) lgkmcnt(0)\n"
"s_barrier\n"
"v_add_f32 v56,0x80000000,v126\n"
"v_min_f32 v56,v56,v108\n"
"v_add_f32 v57,0xf149f2ca,v126\n"
"v_min_f32 v57,v57,v109\n"
"v_add_f32 v58,0xf1c9f2ca,v126\n"
"v_min_f32 v58,v58,v110\n"
"v_add_f32 v59,0xf2177617,v126\n"
"v_min_f32 v59,v59,v111\n"
"v_add_f32 v60,0xf349f2ca,v126\n"
"v_min_f32 v60,v60,v108\n"
"v_add_f32 v61,0xf35691f6,v126\n"
"v_min_f32 v61,v61,v109\n"
"v_add_f32 v62,0xf3633123,v126\n"
"v_min_f32 v62,v62,v110\n"
"v_add_f32 v63,0xf36fd050,v126\n"
"v_min_f32 v63,v63,v111\n"
"v_exp_f32 v16,v16\n"
"v_mfma_f32_16x16x32_bf16 v[56:59],a[64:67],a[48:51],v[56:59]\n"
"s_add_u32 m0,s55,12288\n"
"ds_read_b128 a[96:99],v114 offset:8192\n"
"ds_read_b128 a[100:103],v114 offset:9216\n"
"ds_read_b128 a[104:107],v114 offset:10240\n"
"v_exp_f32 v17,v17\n"
"v_mfma_f32_16x16x32_bf16 v[56:59],a[68:71],a[52:55],v[56:59]\n"
"s_add_u32 s54,s54,s22\n"
"ds_read_b128 a[108:111],v114 offset:11264\n"
"ds_read_b128 a[112:115],v114 offset:12288\n"
"ds_read_b128 a[116:119],v114 offset:13312\n"
"v_mfma_f32_16x16x32_bf16 v[56:59],a[72:75],a[56:59],v[56:59]\n"
"ds_read_b128 a[120:123],v114 offset:14336\n"
"ds_read_b128 a[124:127],v114 offset:15360\n"
"buffer_load_dwordx4 v115,s[32:35],s54 offen sc1 lds\n"
"v_cvt_pk_bf16_f32 v72,v16,v17\n"
"v_exp_f32 v18,v18\n"
"v_mfma_f32_16x16x32_bf16 v[56:59],a[76:79],a[60:63],v[56:59]\n"
"ds_read_b128 a[32:35],v116 offset:24576\n"
"ds_read_b128 a[36:39],v116 offset:25600\n"
"ds_read_b128 a[40:43],v116 offset:26624\n"
"v_mfma_f32_16x16x32_bf16 v[60:63],a[92:95],a[60:63],v[60:63]\n"
"buffer_store_dwordx4 a[180:183],v112,s[24:27],s3 offen offset:1024 sc1\n"
"ds_read_b128 a[44:47],v116 offset:27648\n"
"v_mfma_f32_16x16x32_bf16 v[60:63],a[88:91],a[56:59],v[60:63]\n"
"s_add_u32 m0,s67,32768\n"
"v_exp_f32 v19,v19\n"
"v_mfma_f32_16x16x32_bf16 v[60:63],a[84:87],a[52:55],v[60:63]\n"
"buffer_load_dwordx4 v112,s[24:27],s64 offen offset:0 sc1 lds\n"
"v_cvt_pk_bf16_f32 v73,v18,v19\n"
"v_exp_f32 v20,v20\n"
"v_mfma_f32_16x16x32_bf16 v[60:63],a[80:83],a[48:51],v[60:63]\n"
"ds_read_b128 a[64:67],v113 offset:0\n"
"ds_read_b128 a[68:71],v113 offset:1024\n"
"ds_read_b128 a[72:75],v113 offset:2048\n"
"s_waitcnt lgkmcnt(7)\n"
"v_mfma_f32_16x16x32_bf16 v[160:163],a[96:99],v[84:87],v[160:163]\n"
"ds_read_b128 a[76:79],v113 offset:3072\n"
"ds_read_b128 a[80:83],v113 offset:4096\n"
"ds_read_b128 a[84:87],v113 offset:5120\n"
"ds_read_b128 a[88:91],v113 offset:6144\n"
"ds_read_b128 a[92:95],v113 offset:7168\n"
"v_exp_f32 v21,v21\n"
"v_mfma_f32_16x16x32_bf16 a[132:135],a[144:147],v[84:87],a[132:135]\n"
"buffer_load_dwordx4 v112,s[24:27],s64 offen offset:1024 sc1 lds\n"
"v_max3_f32 v118,v56,v57,v58\n"
"v_mfma_f32_16x16x32_bf16 v[164:167],a[100:103],v[84:87],v[164:167]\n"
"v_exp_f32 v22,v22\n"
"v_cvt_pk_bf16_f32 v74,v20,v21\n"
"ds_read_b128 a[48:51],v116 offset:57344\n"
"ds_read_b128 a[52:55],v116 offset:58368\n"
"v_mfma_f32_16x16x32_bf16 v[168:171],a[104:107],v[84:87],v[168:171]\n"
"v_max3_f32 v118,v118,v59,v60\n"
"v_max3_f32 v118,v118,v61,v62\n"
"v_max_f32 v118,v118,v63\n"
"v_exp_f32 v23,v23\n"
"v_mfma_f32_16x16x32_bf16 v[172:175],a[108:111],v[84:87],v[172:175]\n"
"ds_read_b128 a[56:59],v116 offset:59392\n"
"buffer_load_dwordx4 v112,s[24:27],s64 offen offset:2048 sc1 lds\n"
"ds_read_b128 a[60:63],v116 offset:60416\n"
"v_mfma_f32_16x16x32_bf16 v[176:179],a[112:115],v[84:87],v[176:179]\n"
"v_cvt_pk_bf16_f32 v75,v22,v23\n"
"v_accvgpr_read_b32 v122,a128\n"
"v_rcp_f32 v122,v122\n"
"v_cmp_lt_f32 vcc,s93,v118\n"
"v_pk_mul_f32 v[128:129],v[128:129],v[122:123] op_sel_hi:[1,0]\n"
"v_mfma_f32_16x16x32_bf16 v[180:183],a[116:119],v[84:87],v[180:183]\n"
"buffer_store_dwordx4 a[184:187],v112,s[24:27],s3 offen offset:2048 sc1\n"
"v_exp_f32 v24,v24\n"
"v_pk_mul_f32 v[130:131],v[130:131],v[122:123] op_sel_hi:[1,0]\n"
"v_pk_mul_f32 v[132:133],v[132:133],v[122:123] op_sel_hi:[1,0]\n"
"v_pk_mul_f32 v[134:135],v[134:135],v[122:123] op_sel_hi:[1,0]\n"
"v_mfma_f32_16x16x32_bf16 v[184:187],a[120:123],v[84:87],v[184:187]\n"
"buffer_load_dwordx4 v112,s[24:27],s64 offen offset:3072 sc1 lds\n"
"v_cvt_pk_bf16_f32 v128,v128,v129\n"
"v_cvt_pk_bf16_f32 v129,v130,v131\n"
"v_cvt_pk_bf16_f32 v130,v132,v133\n"
"v_cvt_pk_bf16_f32 v131,v134,v135\n"
"v_mfma_f32_16x16x32_bf16 v[188:191],a[124:127],v[84:87],v[188:191]\n"
"buffer_store_dwordx4 v[128:131],v112,s[24:27],s70 offen offset:0 sc1\n"
"v_pk_mul_f32 v[136:137],v[136:137],v[122:123] op_sel_hi:[1,0]\n"
"v_mfma_f32_16x16x32_bf16 v[192:195],a[96:99],v[88:91],v[192:195]\n"
"s_add_u32 m0,s67,36864\n"
"v_exp_f32 v25,v25\n"
"v_exp_f32 v27,v27\n"
"v_cvt_pk_bf16_f32 v76,v24,v25\n"
"v_cvt_pk_bf16_f32 v136,v136,v137\n"
"v_pk_mul_f32 v[138:139],v[138:139],v[122:123] op_sel_hi:[1,0]\n"
"v_pk_mul_f32 v[140:141],v[140:141],v[122:123] op_sel_hi:[1,0]\n"
"v_pk_mul_f32 v[142:143],v[142:143],v[122:123] op_sel_hi:[1,0]\n"
"v_mfma_f32_16x16x32_bf16 a[136:139],a[144:147],v[88:91],a[136:139]\n"
"buffer_load_dwordx4 v117,s[24:27],s64 offen offset:0 sc1 lds\n"
"v_cvt_pk_bf16_f32 v137,v138,v139\n"
"v_mfma_f32_16x16x32_bf16 v[196:199],a[100:103],v[88:91],v[196:199]\n"
"v_cvt_pk_bf16_f32 v138,v140,v141\n"
"v_cvt_pk_bf16_f32 v139,v142,v143\n"
"v_exp_f32 v26,v26\n"
"v_mfma_f32_16x16x32_bf16 v[200:203],a[104:107],v[88:91],v[200:203]\n"
"buffer_store_dwordx4 v[136:139],v112,s[24:27],s70 offen offset:1024 sc1\n"
"v_cvt_pk_bf16_f32 v77,v26,v27\n"
"v_pk_mul_f32 v[144:145],v[144:145],v[122:123] op_sel_hi:[1,0]\n"
"v_pk_mul_f32 v[146:147],v[146:147],v[122:123] op_sel_hi:[1,0]\n"
"v_pk_mul_f32 v[148:149],v[148:149],v[122:123] op_sel_hi:[1,0]\n"
"v_pk_mul_f32 v[150:151],v[150:151],v[122:123] op_sel_hi:[1,0]\n"
"v_exp_f32 v28,v28\n"
"v_mfma_f32_16x16x32_bf16 v[204:207],a[108:111],v[88:91],v[204:207]\n"
"buffer_load_dwordx4 v117,s[24:27],s64 offen offset:1024 sc1 lds\n"
"buffer_store_dwordx4 a[188:191],v112,s[24:27],s3 offen offset:3072 sc1\n"
"v_mfma_f32_16x16x32_bf16 v[208:211],a[112:115],v[88:91],v[208:211]\n"
"s_or_b32 s3,s3,0x3c000\n"
"v_cvt_pk_bf16_f32 v144,v144,v145\n"
"v_cvt_pk_bf16_f32 v145,v146,v147\n"
"v_cvt_pk_bf16_f32 v146,v148,v149\n"
"v_cvt_pk_bf16_f32 v147,v150,v151\n"
"v_mfma_f32_16x16x32_bf16 v[212:215],a[116:119],v[88:91],v[212:215]\n"
"buffer_store_dwordx4 v[144:147],v112,s[24:27],s70 offen offset:2048 sc1\n"
"v_pk_mul_f32 v[152:153],v[152:153],v[122:123] op_sel_hi:[1,0]\n"
"v_pk_mul_f32 v[154:155],v[154:155],v[122:123] op_sel_hi:[1,0]\n"
"v_mfma_f32_16x16x32_bf16 v[216:219],a[120:123],v[88:91],v[216:219]\n"
"buffer_load_dwordx4 v117,s[24:27],s64 offen offset:2048 sc1 lds\n"
"v_exp_f32 v29,v29\n"
"v_exp_f32 v31,v31\n"
"v_cvt_pk_bf16_f32 v78,v28,v29\n"
"v_pk_mul_f32 v[156:157],v[156:157],v[122:123] op_sel_hi:[1,0]\n"
"v_pk_mul_f32 v[158:159],v[158:159],v[122:123] op_sel_hi:[1,0]\n"
"v_mfma_f32_16x16x32_bf16 v[220:223],a[124:127],v[88:91],v[220:223]\n"
"v_cvt_pk_bf16_f32 v152,v152,v153\n"
"v_cvt_pk_bf16_f32 v153,v154,v155\n"
"v_cvt_pk_bf16_f32 v154,v156,v157\n"
"v_cvt_pk_bf16_f32 v155,v158,v159\n"
"v_mfma_f32_16x16x32_bf16 v[224:227],a[96:99],v[92:95],v[224:227]\n"
"buffer_store_dwordx4 v[152:155],v112,s[24:27],s70 offen offset:3072 sc1\n"
"v_accvgpr_read_b32 v122,a132\n"
"v_mfma_f32_16x16x32_bf16 a[140:143],a[144:147],v[92:95],a[140:143]\n"
"s_add_u32 s70,s70,262144\n"
"buffer_load_dwordx4 v117,s[24:27],s64 offen offset:3072 sc1 lds\n"
"v_mfma_f32_16x16x32_bf16 v[228:231],a[100:103],v[92:95],v[228:231]\n"
"s_add_u32 s64,s64,16384\n"
"v_exp_f32 v30,v30\n"
"v_rcp_f32 v122,v122\n"
"v_cvt_pk_bf16_f32 v79,v30,v31\n"
"v_pk_mul_f32 v[160:161],v[160:161],v[122:123] op_sel_hi:[1,0]\n"
"v_pk_mul_f32 v[162:163],v[162:163],v[122:123] op_sel_hi:[1,0]\n"
"v_pk_mul_f32 v[164:165],v[164:165],v[122:123] op_sel_hi:[1,0]\n"
"v_pk_mul_f32 v[166:167],v[166:167],v[122:123] op_sel_hi:[1,0]\n"
"v_mfma_f32_16x16x32_bf16 v[232:235],a[104:107],v[92:95],v[232:235]\n"
"buffer_store_dwordx4 a[236:239],v112,s[24:27],s56 offen offset:3072 sc1\n"
"v_cvt_pk_bf16_f32 v160,v160,v161\n"
"v_cvt_pk_bf16_f32 v161,v162,v163\n"
"v_cvt_pk_bf16_f32 v162,v164,v165\n"
"v_cvt_pk_bf16_f32 v163,v166,v167\n"
"v_mfma_f32_16x16x32_bf16 v[236:239],a[108:111],v[92:95],v[236:239]\n"
"buffer_store_dwordx4 v[160:163],v112,s[24:27],s70 offen offset:0 sc1\n"
"v_pk_mul_f32 v[168:169],v[168:169],v[122:123] op_sel_hi:[1,0]\n"
"v_pk_mul_f32 v[170:171],v[170:171],v[122:123] op_sel_hi:[1,0]\n"
"v_pk_mul_f32 v[172:173],v[172:173],v[122:123] op_sel_hi:[1,0]\n"
"v_pk_mul_f32 v[174:175],v[174:175],v[122:123] op_sel_hi:[1,0]\n"
"v_mfma_f32_16x16x32_bf16 v[240:243],a[112:115],v[92:95],v[240:243]\n"
"v_cvt_pk_bf16_f32 v168,v168,v169\n"
"v_cvt_pk_bf16_f32 v169,v170,v171\n"
"v_cvt_pk_bf16_f32 v170,v172,v173\n"
"v_cvt_pk_bf16_f32 v171,v174,v175\n"
"v_mfma_f32_16x16x32_bf16 v[244:247],a[116:119],v[92:95],v[244:247]\n"
"buffer_store_dwordx4 v[168:171],v112,s[24:27],s70 offen offset:1024 sc1\n"
"v_pk_mul_f32 v[176:177],v[176:177],v[122:123] op_sel_hi:[1,0]\n"
"v_pk_mul_f32 v[178:179],v[178:179],v[122:123] op_sel_hi:[1,0]\n"
"v_pk_mul_f32 v[180:181],v[180:181],v[122:123] op_sel_hi:[1,0]\n"
"v_pk_mul_f32 v[182:183],v[182:183],v[122:123] op_sel_hi:[1,0]\n"
"v_mfma_f32_16x16x32_bf16 v[248:251],a[120:123],v[92:95],v[248:251]\n"
"v_cvt_pk_bf16_f32 v176,v176,v177\n"
"v_cvt_pk_bf16_f32 v177,v178,v179\n"
"v_cvt_pk_bf16_f32 v178,v180,v181\n"
"v_cvt_pk_bf16_f32 v179,v182,v183\n"
"v_mfma_f32_16x16x32_bf16 v[252:255],a[124:127],v[92:95],v[252:255]\n"
"buffer_store_dwordx4 v[176:179],v112,s[24:27],s70 offen offset:2048 sc1\n"
"v_pk_mul_f32 v[184:185],v[184:185],v[122:123] op_sel_hi:[1,0]\n"
"v_pk_mul_f32 v[186:187],v[186:187],v[122:123] op_sel_hi:[1,0]\n"
"v_pk_mul_f32 v[188:189],v[188:189],v[122:123] op_sel_hi:[1,0]\n"
"v_pk_mul_f32 v[190:191],v[190:191],v[122:123] op_sel_hi:[1,0]\n"
"v_cvt_pk_bf16_f32 v184,v184,v185\n"
"v_cvt_pk_bf16_f32 v185,v186,v187\n"
"v_cvt_pk_bf16_f32 v186,v188,v189\n"
"v_cvt_pk_bf16_f32 v187,v190,v191\n"
"buffer_store_dwordx4 v[184:187],v112,s[24:27],s70 offen offset:3072 sc1\n"
"s_add_u32 s70,s70,262144\n"
"s_cbranch_vccnz .Lfk_cs_z2\n"
".Lfk_cr_z2:\n"
"s_waitcnt vmcnt(34) lgkmcnt(0)\n"
"s_barrier\n"
"s_add_u32 s89,s89,1\n"
"s_branch .Lfk_winB\n"
".Lfk_ev:\n"
"s_xor_b32 s55,s55,16384\n"
"s_andn2_b32 s91,s55,16384\n"
"v_lshrrev_b32 v121,1,v112\n"
"v_add_u32 v121,s91,v121\n"
"ds_read_b64_tr_b16 a[160:161],v121 offset:0\n"
"ds_read_b64_tr_b16 a[162:163],v121 offset:512\n"
"s_add_u32 s56,s56,4096\n"
"s_swappc_b64 s[20:21],s[94:95]\n"
"s_xor_b32 s55,s55,16384\n"
"s_andn2_b32 s91,s55,16384\n"
"v_lshrrev_b32 v121,1,v112\n"
"v_add_u32 v121,s91,v121\n"
"ds_read_b64_tr_b16 a[164:165],v121 offset:4096\n"
"ds_read_b64_tr_b16 a[166:167],v121 offset:4608\n"
"ds_read_b64_tr_b16 a[168:169],v121 offset:8192\n"
"ds_read_b64_tr_b16 a[170:171],v121 offset:8704\n"
"ds_read_b64_tr_b16 a[172:173],v121 offset:12288\n"
"ds_read_b64_tr_b16 a[174:175],v121 offset:12800\n"
"ds_read_b64_tr_b16 a[176:177],v121 offset:0\n"
"ds_read_b64_tr_b16 a[178:179],v121 offset:512\n"
"buffer_store_dwordx2 a[154:155],off,s[24:27],s2 sc1\n"
"s_and_b32 s2,s2,0xfffffc00\n"
"s_mov_b32 s56,s10\n"
"v_accvgpr_write_b32 a240,0\n"
"v_accvgpr_write_b32 a241,0\n"
"v_accvgpr_write_b32 a242,0\n"
"v_accvgpr_write_b32 a243,0\n"
"v_accvgpr_write_b32 a244,0\n"
"v_accvgpr_write_b32 a245,0\n"
"v_accvgpr_write_b32 a246,0\n"
"v_accvgpr_write_b32 a247,0\n"
"v_accvgpr_write_b32 a248,0\n"
"v_accvgpr_write_b32 a249,0\n"
"v_accvgpr_write_b32 a250,0\n"
"v_accvgpr_write_b32 a251,0\n"
"v_accvgpr_write_b32 a252,0\n"
"v_accvgpr_write_b32 a253,0\n"
"v_accvgpr_write_b32 a254,0\n"
"v_accvgpr_write_b32 a255,0\n"
"s_mov_b64 s[32:33],s[36:37]\n"
"s_mov_b32 s54,-229376\n"
"s_swappc_b64 s[20:21],s[94:95]\n"
"s_xor_b32 s55,s55,16384\n"
"s_add_u32 s56,s56,131072\n"
"s_andn2_b32 s91,s55,16384\n"
"v_lshrrev_b32 v121,1,v112\n"
"v_add_u32 v121,s91,v121\n"
"ds_read_b64_tr_b16 a[180:181],v121 offset:20480\n"
"ds_read_b64_tr_b16 a[182:183],v121 offset:20992\n"
"ds_read_b64_tr_b16 a[184:185],v121 offset:24576\n"
"ds_read_b64_tr_b16 a[186:187],v121 offset:25088\n"
"ds_read_b64_tr_b16 a[188:189],v121 offset:28672\n"
"ds_read_b64_tr_b16 a[190:191],v121 offset:29184\n"
"ds_read_b64_tr_b16 a[192:193],v121 offset:0\n"
"ds_read_b64_tr_b16 a[194:195],v121 offset:512\n"
"buffer_load_dwordx2 a[150:151],v112,s[24:27],s2 offen sc1\n"
"s_add_u32 s82,s59,8192\n"
"buffer_store_dwordx2 a[154:155],off,s[24:27],s82 sc1\n"
"s_swappc_b64 s[20:21],s[94:95]\n"
"s_xor_b32 s55,s55,16384\n"
"s_add_u32 s56,s56,131072\n"
"s_andn2_b32 s91,s55,16384\n"
"v_lshrrev_b32 v121,1,v112\n"
"v_add_u32 v121,s91,v121\n"
"ds_read_b64_tr_b16 a[196:197],v121 offset:4096\n"
"ds_read_b64_tr_b16 a[198:199],v121 offset:4608\n"
"ds_read_b64_tr_b16 a[200:201],v121 offset:8192\n"
"ds_read_b64_tr_b16 a[202:203],v121 offset:8704\n"
"ds_read_b64_tr_b16 a[204:205],v121 offset:12288\n"
"ds_read_b64_tr_b16 a[206:207],v121 offset:12800\n"
"ds_read_b64_tr_b16 a[208:209],v121 offset:0\n"
"ds_read_b64_tr_b16 a[210:211],v121 offset:512\n"
"s_mov_b32 s84,0x40000\n"
".Lfk_w_chk:\n"
"v_accvgpr_read_b32 v119,a150\n"
"v_accvgpr_read_b32 v120,a151\n"
"v_xor_b32 v119,s80,v119\n"
"v_xor_b32 v120,s81,v120\n"
"v_or_b32 v119,v119,v120\n"
"v_cmp_ne_u32 vcc,0,v119\n"
"s_nop 0\n"
"s_cbranch_vccz .Lfk_w_ok\n"
"s_sub_u32 s84,s84,1\n"
"s_cbranch_scc1 .Lfk_w_ok\n"
"s_sleep 4\n"
"buffer_load_dwordx2 a[150:151],v112,s[24:27],s2 offen sc1\n"
"s_waitcnt vmcnt(0)\n"
"s_branch .Lfk_w_chk\n"
".Lfk_w_ok:\n"
"v_lshrrev_b32 v121,1,v112\n"
"v_and_b32 v121,0xf8,v121\n"
"s_add_u32 s82,s63,8192\n"
"buffer_load_dwordx2 a[150:151],v121,s[24:27],s82 offen sc1\n"
"s_mov_b64 s[32:33],s[24:25]\n"
"s_mov_b32 s34,s26\n"
"s_mov_b32 s22,1024\n"
"v_mov_b32 v115,v112\n"
"s_sub_u32 s54,s3,1024\n"
"s_swappc_b64 s[20:21],s[94:95]\n"
"s_xor_b32 s55,s55,16384\n"
"s_add_u32 s56,s56,131072\n"
"s_andn2_b32 s91,s55,16384\n"
"v_lshrrev_b32 v121,1,v112\n"
"v_add_u32 v121,s91,v121\n"
"ds_read_b64_tr_b16 a[212:213],v121 offset:20480\n"
"ds_read_b64_tr_b16 a[214:215],v121 offset:20992\n"
"ds_read_b64_tr_b16 a[216:217],v121 offset:24576\n"
"ds_read_b64_tr_b16 a[218:219],v121 offset:25088\n"
"ds_read_b64_tr_b16 a[220:221],v121 offset:28672\n"
"ds_read_b64_tr_b16 a[222:223],v121 offset:29184\n"
"v_add_u32 v121,s91,v112\n"
"ds_read_b128 a[224:227],v121 offset:0\n"
"s_sub_u32 s54,s3,17408\n"
"s_mov_b32 s84,0x40000\n"
".Lfk_k_chk:\n"
"v_accvgpr_read_b32 v119,a150\n"
"v_accvgpr_read_b32 v120,a151\n"
"v_xor_b32 v119,s80,v119\n"
"v_xor_b32 v120,s81,v120\n"
"v_or_b32 v119,v119,v120\n"
"v_cmp_ne_u32 vcc,0,v119\n"
"s_nop 0\n"
"s_cbranch_vccz .Lfk_k_ok\n"
"s_sub_u32 s84,s84,1\n"
"s_cbranch_scc1 .Lfk_k_ok\n"
"s_sleep 4\n"
"v_lshrrev_b32 v121,1,v112\n"
"v_and_b32 v121,0xf8,v121\n"
"s_add_u32 s82,s63,8192\n"
"buffer_load_dwordx2 a[150:151],v121,s[24:27],s82 offen sc1\n"
"s_waitcnt vmcnt(0)\n"
"s_branch .Lfk_k_chk\n"
".Lfk_k_ok:\n"
"s_swappc_b64 s[20:21],s[94:95]\n"
"s_xor_b32 s55,s55,16384\n"
"s_andn2_b32 s91,s55,16384\n"
"v_add_u32 v121,s91,v112\n"
"ds_read_b128 a[228:231],v121 offset:4096\n"
"ds_read_b128 a[232:235],v121 offset:8192\n"
"ds_read_b128 a[236:239],v121 offset:12288\n"
"ds_read_b128 a[240:243],v121 offset:0\n"
"s_sub_u32 s54,s3,99328\n"
"s_mov_b32 s56,2146435072\n"
"s_getpc_b64 s[96:97]\n"
".Lfk_rt1:\n"
"s_sub_u32 s96,s96,.Lfk_rt1-.Lfk_tailA\n"
"s_subb_u32 s97,s97,0\n"
"s_mov_b32 s98,.Lfk_cold-.Lfk_tailA\n"
"s_mov_b32 s99,0x20000\n"
"s_lshr_b32 s82,s100,3\n"
"s_lshl2_add_u32 s82,s82,s52\n"
"s_lshl_b32 s82,s82,9\n"
"v_lshrrev_b32 v121,1,v112\n"
"buffer_load_dword a148,v121,s[96:99],s82 offen\n"
"s_swappc_b64 s[20:21],s[94:95]\n"
"s_xor_b32 s55,s55,16384\n"
"s_andn2_b32 s91,s55,16384\n"
"v_add_u32 v121,s91,v112\n"
"ds_read_b128 a[244:247],v121 offset:20480\n"
"ds_read_b128 a[248:251],v121 offset:24576\n"
"ds_read_b128 a[252:255],v121 offset:28672\n"
"ds_read_b128 a[156:159],v121 offset:0\n"
"s_sub_u32 s54,s3,115712\n"
"s_lshl_b32 s82,s100,3\n"
"s_add_u32 s82,s82,148946944\n"
"s_cmp_eq_u32 s52,0\n"
"s_cselect_b32 s82,s82,2146435072\n"
"buffer_store_dwordx2 a[154:155],off,s[24:27],s82 sc1\n"
"s_lshr_b32 s82,s3,9\n"
"s_and_b32 s82,s82,0x3000\n"
"s_add_u32 s11,s11,s82\n"
"s_add_u32 s9,s9,s82\n"
"s_lshl3_add_u32 s8,s82,s8\n"
"s_lshr_b32 s82,s3,18\n"
"s_sub_u32 s82,s82,s60\n"
"s_and_b32 s82,s82,7\n"
"s_lshl_b32 s82,s82,17\n"
"s_add_u32 s8,s8,s82\n"
"v_lshrrev_b32 v120,1,v112\n"
"buffer_load_dwordx2 a[150:151],v120,s[24:27],s11 offen sc1\n"
"s_lshl_b32 s30,s78,1\n"
"s_swappc_b64 s[20:21],s[94:95]\n"
"s_nop 1\n"
".Lfk_xn0_s:\n"
"s_mov_b32 s84,0x40000\n"
"buffer_load_dwordx4 v[74:77],v112,s[24:27],s58 offen sc1\n"
"s_waitcnt vmcnt(0)\n"
".Lfk_xn0:\n"
"v_xor_b32 v116,s80,v74\n"
"v_xor_b32 v117,s81,v75\n"
"v_or_b32 v116,v116,v117\n"
"v_xor_b32 v117,s80,v76\n"
"v_or_b32 v116,v116,v117\n"
"v_xor_b32 v117,s81,v77\n"
"v_or_b32 v116,v116,v117\n"
"v_cmp_ne_u32 vcc,0,v116\n"
"s_nop 0\n"
"s_cbranch_vccz .Lfk_xn0_x\n"
"s_sleep 4\n"
"s_mov_b64 exec,vcc\n"
"buffer_load_dwordx4 v[74:77],v112,s[24:27],s58 offen sc1\n"
"s_mov_b64 exec,-1\n"
"s_sub_u32 s84,s84,1\n"
"s_waitcnt vmcnt(0)\n"
"s_cbranch_scc0 .Lfk_xn0\n"
".Lfk_xn0_x:\n"
"s_nop 1\n"
"s_branch .Lfk_xn0_ok\n"
".Lfk_xn1_s:\n"
"s_mov_b32 s84,0x40000\n"
"buffer_load_dwordx4 v[74:77],v112,s[24:27],s58 offen sc1\n"
"s_waitcnt vmcnt(0)\n"
".Lfk_xn1:\n"
"v_xor_b32 v116,s80,v74\n"
"v_xor_b32 v117,s81,v75\n"
"v_or_b32 v116,v116,v117\n"
"v_xor_b32 v117,s80,v76\n"
"v_or_b32 v116,v116,v117\n"
"v_xor_b32 v117,s81,v77\n"
"v_or_b32 v116,v116,v117\n"
"v_cmp_ne_u32 vcc,0,v116\n"
"s_nop 0\n"
"s_cbranch_vccz .Lfk_xn1_x\n"
"s_sleep 4\n"
"s_mov_b64 exec,vcc\n"
"buffer_load_dwordx4 v[74:77],v112,s[24:27],s58 offen sc1\n"
"s_mov_b64 exec,-1\n"
"s_sub_u32 s84,s84,1\n"
"s_waitcnt vmcnt(0)\n"
"s_cbranch_scc0 .Lfk_xn1\n"
".Lfk_xn1_x:\n"
"s_nop 1\n"
"s_branch .Lfk_xn1_ok\n"
".Lfk_kv_s:\n"
"s_mov_b32 s84,0x40000\n"
"buffer_load_dwordx2 v[118:119],v120,s[24:27],s63 offen sc1\n"
"s_waitcnt vmcnt(0)\n"
".Lfk_kv:\n"
"v_xor_b32 v116,s80,v118\n"
"v_xor_b32 v117,s81,v119\n"
"v_or_b32 v116,v116,v117\n"
"v_cmp_ne_u32 vcc,0,v116\n"
"s_nop 0\n"
"s_cbranch_vccz .Lfk_kv_x\n"
"s_sleep 4\n"
"s_mov_b64 exec,vcc\n"
"buffer_load_dwordx2 v[118:119],v120,s[24:27],s63 offen sc1\n"
"s_mov_b64 exec,-1\n"
"s_sub_u32 s84,s84,1\n"
"s_waitcnt vmcnt(0)\n"
"s_cbranch_scc0 .Lfk_kv\n"
".Lfk_kv_x:\n"
"s_nop 1\n"
"s_branch .Lfk_kv_ok\n"
".Lfk_cs_h3:\n"
"s_mov_b32 s91,15\n"
"s_mov_b32 s90,15\n"
"s_call_b64 s[94:95],.Lfk_rare0\n"
"s_branch .Lfk_cr_h3\n"
".Lfk_cs_h1:\n"
"s_mov_b32 s91,15\n"
"s_call_b64 s[94:95],.Lfk_rare3\n"
"s_branch .Lfk_cr_h1\n"
".Lfk_cs_h2:\n"
"s_mov_b32 s91,15\n"
"s_call_b64 s[94:95],.Lfk_rare2\n"
"s_branch .Lfk_cr_h2\n"
".Lfk_cs_x1:\n"
"s_mov_b32 s91,15\n"
"s_call_b64 s[94:95],.Lfk_rare1\n"
"s_branch .Lfk_cr_x1\n"
".Lfk_cs_a3:\n"
"s_mov_b32 s91,14\n"
"s_call_b64 s[94:95],.Lfk_rare0\n"
"s_branch .Lfk_cr_a3\n"
".Lfk_cs_a0:\n"
"s_mov_b32 s91,12\n"
"s_call_b64 s[94:95],.Lfk_rare1\n"
"s_branch .Lfk_cr_a0\n"
".Lfk_cs_a1:\n"
"s_mov_b32 s91,8\n"
"s_call_b64 s[94:95],.Lfk_rare0\n"
"s_branch .Lfk_cr_a1\n"
".Lfk_ms_a0:\n"
"s_call_b64 s[94:95],.Lfk_mslow0\n"
"s_branch .Lfk_mr_a0\n"
".Lfk_ms_a1:\n"
"s_call_b64 s[94:95],.Lfk_mslow1\n"
"s_branch .Lfk_mr_a1\n"
".Lfk_p2:\n"
"s_nop 7\n"
"s_mov_b32 s84,0x40000\n"
"buffer_load_dwordx2 v[122:123],v115,s[24:27],s11 offen sc1\n"
"s_waitcnt vmcnt(0)\n"
".Lfk_pj:\n"
"v_xor_b32 v119,s80,v122\n"
"v_xor_b32 v120,s81,v123\n"
"v_or_b32 v119,v119,v120\n"
"v_cmp_ne_u32 s[14:15],0,v119\n"
"s_nop 0\n"
"s_mov_b32 vcc_lo,s14\n"
"s_mov_b32 vcc_hi,0\n"
"s_cbranch_vccz .Lfk_pj_x\n"
"s_sleep 4\n"
"s_mov_b64 exec,vcc\n"
"buffer_load_dwordx2 v[122:123],v115,s[24:27],s11 offen sc1\n"
"s_mov_b64 exec,-1\n"
"s_sub_u32 s84,s84,1\n"
"s_waitcnt vmcnt(0)\n"
"s_cbranch_scc0 .Lfk_pj\n"
".Lfk_pj_x:\n"
"s_cmp_ge_u32 s52,2\n"
"s_cselect_b32 s18,1032192,0\n"
"s_add_u32 s18,s18,s8\n"
"s_add_u32 m0,s30,0\n"
"s_nop 0\n"
"buffer_load_dwordx4 v112,s[24:27],s18 offen offset:0 sc1 lds\n"
"buffer_load_dwordx4 v112,s[24:27],s18 offen offset:1024 sc1 lds\n"
"buffer_load_dwordx4 v112,s[24:27],s18 offen offset:2048 sc1 lds\n"
"buffer_load_dwordx4 v112,s[24:27],s18 offen offset:3072 sc1 lds\n"
"s_add_u32 m0,m0,4096\n"
"s_nop 0\n"
"buffer_load_dwordx4 v117,s[24:27],s18 offen offset:0 sc1 lds\n"
"buffer_load_dwordx4 v117,s[24:27],s18 offen offset:1024 sc1 lds\n"
"buffer_load_dwordx4 v117,s[24:27],s18 offen offset:2048 sc1 lds\n"
"buffer_load_dwordx4 v117,s[24:27],s18 offen offset:3072 sc1 lds\n"
"s_add_u32 s83,s18,2097152\n"
"s_add_u32 m0,s30,32768\n"
"s_nop 0\n"
"buffer_load_dwordx4 v112,s[24:27],s83 offen offset:0 sc1 lds\n"
"buffer_load_dwordx4 v112,s[24:27],s83 offen offset:1024 sc1 lds\n"
"buffer_load_dwordx4 v112,s[24:27],s83 offen offset:2048 sc1 lds\n"
"buffer_load_dwordx4 v112,s[24:27],s83 offen offset:3072 sc1 lds\n"
"s_add_u32 m0,m0,4096\n"
"s_nop 0\n"
"buffer_load_dwordx4 v117,s[24:27],s83 offen offset:0 sc1 lds\n"
"buffer_load_dwordx4 v117,s[24:27],s83 offen offset:1024 sc1 lds\n"
"buffer_load_dwordx4 v117,s[24:27],s83 offen offset:2048 sc1 lds\n"
"buffer_load_dwordx4 v117,s[24:27],s83 offen offset:3072 sc1 lds\n"
"s_waitcnt vmcnt(0)\n"
"s_nop 1\n"
"s_branch .Lfk_p3\n"
".Lfk_cs_k1:\n"
"s_mov_b32 s91,15\n"
"s_call_b64 s[94:95],.Lfk_rare3\n"
"s_branch .Lfk_cr_k1\n"
".Lfk_cs_k2:\n"
"s_mov_b32 s91,15\n"
"s_call_b64 s[94:95],.Lfk_rare2\n"
"s_branch .Lfk_cr_k2\n"
".Lfk_cs_x2:\n"
"s_mov_b32 s91,15\n"
"s_call_b64 s[94:95],.Lfk_rare1\n"
"s_branch .Lfk_cr_x2\n"
".Lfk_cs_u3:\n"
"s_mov_b32 s91,15\n"
"s_call_b64 s[94:95],.Lfk_rare0\n"
"s_branch .Lfk_cr_u3\n"
".Lfk_cs_mu3:\n"
"s_call_b64 s[94:95],.Lfk_meet3\n"
"s_branch .Lfk_cr_mu3\n"
".Lfk_cs_u0:\n"
"s_mov_b32 s91,14\n"
"s_call_b64 s[94:95],.Lfk_rare1\n"
"s_branch .Lfk_cr_u0\n"
".Lfk_cs_mu0:\n"
"s_call_b64 s[94:95],.Lfk_meet3\n"
"s_branch .Lfk_cr_mu0\n"
".Lfk_cs_u1:\n"
"s_mov_b32 s91,12\n"
"s_call_b64 s[94:95],.Lfk_rare0\n"
"s_branch .Lfk_cr_u1\n"
".Lfk_cs_mu1:\n"
"s_call_b64 s[94:95],.Lfk_meet3\n"
"s_branch .Lfk_cr_mu1\n"
".Lfk_cs_u2:\n"
"s_mov_b32 s91,8\n"
"s_call_b64 s[94:95],.Lfk_rare1\n"
"s_branch .Lfk_cr_u2\n"
".Lfk_cs_mu2:\n"
"s_call_b64 s[94:95],.Lfk_meet3\n"
"s_branch .Lfk_cr_mu2\n"
".Lfk_cs_v3:\n"
"s_mov_b32 s91,15\n"
"s_mov_b32 s90,15\n"
"s_call_b64 s[94:95],.Lfk_rare0\n"
"s_branch .Lfk_cr_v3\n"
".Lfk_cs_mv3:\n"
"s_call_b64 s[94:95],.Lfk_meet3\n"
"s_branch .Lfk_cr_mv3\n"
".Lfk_cs_mv0:\n"
"s_call_b64 s[94:95],.Lfk_meet3\n"
"s_branch .Lfk_cr_mv0\n"
".Lfk_cs_v1:\n"
"s_call_b64 s[94:95],.Lfk_meet3\n"
"v_or_b32 v118,v118,v95\n"
"v_and_b32 v118,0x40004000,v118\n"
"v_cmp_ne_u32 vcc,0,v118\n"
"s_nop 0\n"
"s_cbranch_vccz .Lfk_cr_v1\n"
"s_mov_b32 s91,15\n"
"s_call_b64 s[94:95],.Lfk_rare3\n"
"s_branch .Lfk_cr_v1\n"
".Lfk_cs_v2:\n"
"s_call_b64 s[94:95],.Lfk_meet3\n"
"v_or_b32 v118,v118,v79\n"
"v_and_b32 v118,0x40004000,v118\n"
"v_cmp_ne_u32 vcc,0,v118\n"
"s_nop 0\n"
"s_cbranch_vccz .Lfk_cr_v2\n"
"s_mov_b32 s91,15\n"
"s_call_b64 s[94:95],.Lfk_rare2\n"
"s_branch .Lfk_cr_v2\n"
".Lfk_cs_b3:\n"
"s_mov_b32 s91,15\n"
"s_call_b64 s[94:95],.Lfk_rare0\n"
"s_branch .Lfk_cr_b3\n"
".Lfk_ms_b0:\n"
"s_call_b64 s[94:95],.Lfk_mslow0\n"
"s_branch .Lfk_mr_b0\n"
".Lfk_ms_b1:\n"
"s_call_b64 s[94:95],.Lfk_mslow1\n"
"s_branch .Lfk_mr_b1\n"
".Lfk_cs_c3:\n"
"s_mov_b32 s91,15\n"
"s_mov_b32 s90,15\n"
"s_call_b64 s[94:95],.Lfk_rare0\n"
"s_branch .Lfk_cr_c3\n"
".Lfk_cs_z3:\n"
"s_mov_b32 s91,15\n"
"s_mov_b32 s90,15\n"
"s_call_b64 s[94:95],.Lfk_rare0\n"
"s_branch .Lfk_cr_z3\n"
".Lfk_cs_z0:\n"
"s_mov_b32 s91,14\n"
"s_call_b64 s[94:95],.Lfk_rare1\n"
"s_branch .Lfk_cr_z0\n"
".Lfk_cs_z1:\n"
"s_mov_b32 s91,12\n"
"s_call_b64 s[94:95],.Lfk_rare0\n"
"s_branch .Lfk_cr_z1\n"
".Lfk_cs_z2:\n"
"s_mov_b32 s91,8\n"
"s_call_b64 s[94:95],.Lfk_rare1\n"
"s_branch .Lfk_cr_z2\n"
".Lfk_cold:\n"
".Lfk_rare0:\n"
"s_nop 7\n"
"s_bitcmp1_b32 s91,0\n"
"s_cbranch_scc0 .Lfk_rare0_0\n"
"v_max3_f32 v119,v0,v1,v2\n"
"v_max3_f32 v120,v3,v4,v5\n"
"v_max3_f32 v119,v119,v6,v7\n"
"v_max_f32 v119,v119,v120\n"
"v_mov_b32 v120,v119\n"
"s_nop 1\n"
"v_permlane16_swap_b32 v120,v119\n"
"v_max_f32 v119,v119,v120\n"
"v_mov_b32 v120,v119\n"
"s_nop 1\n"
"v_permlane32_swap_b32 v120,v119\n"
"v_max_f32 v119,v119,v120\n"
"s_bitcmp1_b32 s90,0\n"
"s_cselect_b32 s82,0xff800000,0\n"
"v_add_f32 v119,0x41980000,v119\n"
"v_max_f32 v119,s82,v119\n"
"v_cmp_lt_f32 vcc,s17,|v119|\n"
"s_nop 0\n"
"s_cbranch_vccz .Lfk_rare0_0\n"
"v_ceil_f32 v119,v119\n"
"v_sub_f32 v0,v0,v119\n"
"v_sub_f32 v1,v1,v119\n"
"v_sub_f32 v2,v2,v119\n"
"v_sub_f32 v3,v3,v119\n"
"v_sub_f32 v4,v4,v119\n"
"v_sub_f32 v5,v5,v119\n"
"v_sub_f32 v6,v6,v119\n"
"v_sub_f32 v7,v7,v119\n"
"v_sub_f32 v96,v96,v119\n"
"v_sub_f32 v97,v97,v119\n"
"v_sub_f32 v98,v98,v119\n"
"v_sub_f32 v99,v99,v119\n"
"s_cbranch_scc1 .Lfk_rare0_0\n"
"v_exp_f32 v119,-v119\n"
"s_nop 0\n"
"v_mul_f32 v128,v128,v119\n"
"v_mul_f32 v129,v129,v119\n"
"v_mul_f32 v130,v130,v119\n"
"v_mul_f32 v131,v131,v119\n"
"v_mul_f32 v132,v132,v119\n"
"v_mul_f32 v133,v133,v119\n"
"v_mul_f32 v134,v134,v119\n"
"v_mul_f32 v135,v135,v119\n"
"v_mul_f32 v136,v136,v119\n"
"v_mul_f32 v137,v137,v119\n"
"v_mul_f32 v138,v138,v119\n"
"v_mul_f32 v139,v139,v119\n"
"v_mul_f32 v140,v140,v119\n"
"v_mul_f32 v141,v141,v119\n"
"v_mul_f32 v142,v142,v119\n"
"v_mul_f32 v143,v143,v119\n"
"v_mul_f32 v144,v144,v119\n"
"v_mul_f32 v145,v145,v119\n"
"v_mul_f32 v146,v146,v119\n"
"v_mul_f32 v147,v147,v119\n"
"v_mul_f32 v148,v148,v119\n"
"v_mul_f32 v149,v149,v119\n"
"v_mul_f32 v150,v150,v119\n"
"v_mul_f32 v151,v151,v119\n"
"v_mul_f32 v152,v152,v119\n"
"v_mul_f32 v153,v153,v119\n"
"v_mul_f32 v154,v154,v119\n"
"v_mul_f32 v155,v155,v119\n"
"v_mul_f32 v156,v156,v119\n"
"v_mul_f32 v157,v157,v119\n"
"v_mul_f32 v158,v158,v119\n"
"v_mul_f32 v159,v159,v119\n"
"v_accvgpr_read_b32 v120,a128\n"
"v_mul_f32 v120,v120,v119\n"
"v_accvgpr_write_b32 a128,v120\n"
"v_lshlrev_b32 v120,16,v80\n"
"v_and_b32 v121,0xffff0000,v80\n"
"v_mul_f32 v120,v120,v119\n"
"v_mul_f32 v121,v121,v119\n"
"v_cvt_pk_bf16_f32 v80,v120,v121\n"
"v_accvgpr_read_b32 v120,a129\n"
"v_mul_f32 v120,v120,v119\n"
"v_accvgpr_write_b32 a129,v120\n"
"v_lshlrev_b32 v120,16,v81\n"
"v_and_b32 v121,0xffff0000,v81\n"
"v_mul_f32 v120,v120,v119\n"
"v_mul_f32 v121,v121,v119\n"
"v_cvt_pk_bf16_f32 v81,v120,v121\n"
"v_accvgpr_read_b32 v120,a130\n"
"v_mul_f32 v120,v120,v119\n"
"v_accvgpr_write_b32 a130,v120\n"
"v_lshlrev_b32 v120,16,v82\n"
"v_and_b32 v121,0xffff0000,v82\n"
"v_mul_f32 v120,v120,v119\n"
"v_mul_f32 v121,v121,v119\n"
"v_cvt_pk_bf16_f32 v82,v120,v121\n"
"v_accvgpr_read_b32 v120,a131\n"
"v_mul_f32 v120,v120,v119\n"
"v_accvgpr_write_b32 a131,v120\n"
"v_lshlrev_b32 v120,16,v83\n"
"v_and_b32 v121,0xffff0000,v83\n"
"v_mul_f32 v120,v120,v119\n"
"v_mul_f32 v121,v121,v119\n"
"v_cvt_pk_bf16_f32 v83,v120,v121\n"
".Lfk_rare0_0:\n"
"s_bitcmp1_b32 s91,1\n"
"s_cbranch_scc0 .Lfk_rare0_1\n"
"v_max3_f32 v119,v8,v9,v10\n"
"v_max3_f32 v120,v11,v12,v13\n"
"v_max3_f32 v119,v119,v14,v15\n"
"v_max_f32 v119,v119,v120\n"
"v_mov_b32 v120,v119\n"
"s_nop 1\n"
"v_permlane16_swap_b32 v120,v119\n"
"v_max_f32 v119,v119,v120\n"
"v_mov_b32 v120,v119\n"
"s_nop 1\n"
"v_permlane32_swap_b32 v120,v119\n"
"v_max_f32 v119,v119,v120\n"
"s_bitcmp1_b32 s90,1\n"
"s_cselect_b32 s82,0xff800000,0\n"
"v_add_f32 v119,0x41980000,v119\n"
"v_max_f32 v119,s82,v119\n"
"v_cmp_lt_f32 vcc,s17,|v119|\n"
"s_nop 0\n"
"s_cbranch_vccz .Lfk_rare0_1\n"
"v_ceil_f32 v119,v119\n"
"v_sub_f32 v8,v8,v119\n"
"v_sub_f32 v9,v9,v119\n"
"v_sub_f32 v10,v10,v119\n"
"v_sub_f32 v11,v11,v119\n"
"v_sub_f32 v12,v12,v119\n"
"v_sub_f32 v13,v13,v119\n"
"v_sub_f32 v14,v14,v119\n"
"v_sub_f32 v15,v15,v119\n"
"v_sub_f32 v100,v100,v119\n"
"v_sub_f32 v101,v101,v119\n"
"v_sub_f32 v102,v102,v119\n"
"v_sub_f32 v103,v103,v119\n"
"s_cbranch_scc1 .Lfk_rare0_1\n"
"v_exp_f32 v119,-v119\n"
"s_nop 0\n"
"v_mul_f32 v160,v160,v119\n"
"v_mul_f32 v161,v161,v119\n"
"v_mul_f32 v162,v162,v119\n"
"v_mul_f32 v163,v163,v119\n"
"v_mul_f32 v164,v164,v119\n"
"v_mul_f32 v165,v165,v119\n"
"v_mul_f32 v166,v166,v119\n"
"v_mul_f32 v167,v167,v119\n"
"v_mul_f32 v168,v168,v119\n"
"v_mul_f32 v169,v169,v119\n"
"v_mul_f32 v170,v170,v119\n"
"v_mul_f32 v171,v171,v119\n"
"v_mul_f32 v172,v172,v119\n"
"v_mul_f32 v173,v173,v119\n"
"v_mul_f32 v174,v174,v119\n"
"v_mul_f32 v175,v175,v119\n"
"v_mul_f32 v176,v176,v119\n"
"v_mul_f32 v177,v177,v119\n"
"v_mul_f32 v178,v178,v119\n"
"v_mul_f32 v179,v179,v119\n"
"v_mul_f32 v180,v180,v119\n"
"v_mul_f32 v181,v181,v119\n"
"v_mul_f32 v182,v182,v119\n"
"v_mul_f32 v183,v183,v119\n"
"v_mul_f32 v184,v184,v119\n"
"v_mul_f32 v185,v185,v119\n"
"v_mul_f32 v186,v186,v119\n"
"v_mul_f32 v187,v187,v119\n"
"v_mul_f32 v188,v188,v119\n"
"v_mul_f32 v189,v189,v119\n"
"v_mul_f32 v190,v190,v119\n"
"v_mul_f32 v191,v191,v119\n"
"v_accvgpr_read_b32 v120,a132\n"
"v_mul_f32 v120,v120,v119\n"
"v_accvgpr_write_b32 a132,v120\n"
"v_lshlrev_b32 v120,16,v84\n"
"v_and_b32 v121,0xffff0000,v84\n"
"v_mul_f32 v120,v120,v119\n"
"v_mul_f32 v121,v121,v119\n"
"v_cvt_pk_bf16_f32 v84,v120,v121\n"
"v_accvgpr_read_b32 v120,a133\n"
"v_mul_f32 v120,v120,v119\n"
"v_accvgpr_write_b32 a133,v120\n"
"v_lshlrev_b32 v120,16,v85\n"
"v_and_b32 v121,0xffff0000,v85\n"
"v_mul_f32 v120,v120,v119\n"
"v_mul_f32 v121,v121,v119\n"
"v_cvt_pk_bf16_f32 v85,v120,v121\n"
"v_accvgpr_read_b32 v120,a134\n"
"v_mul_f32 v120,v120,v119\n"
"v_accvgpr_write_b32 a134,v120\n"
"v_lshlrev_b32 v120,16,v86\n"
"v_and_b32 v121,0xffff0000,v86\n"
"v_mul_f32 v120,v120,v119\n"
"v_mul_f32 v121,v121,v119\n"
"v_cvt_pk_bf16_f32 v86,v120,v121\n"
"v_accvgpr_read_b32 v120,a135\n"
"v_mul_f32 v120,v120,v119\n"
"v_accvgpr_write_b32 a135,v120\n"
"v_lshlrev_b32 v120,16,v87\n"
"v_and_b32 v121,0xffff0000,v87\n"
"v_mul_f32 v120,v120,v119\n"
"v_mul_f32 v121,v121,v119\n"
"v_cvt_pk_bf16_f32 v87,v120,v121\n"
".Lfk_rare0_1:\n"
"s_bitcmp1_b32 s91,2\n"
"s_cbranch_scc0 .Lfk_rare0_2\n"
"v_max3_f32 v119,v16,v17,v18\n"
"v_max3_f32 v120,v19,v20,v21\n"
"v_max3_f32 v119,v119,v22,v23\n"
"v_max_f32 v119,v119,v120\n"
"v_mov_b32 v120,v119\n"
"s_nop 1\n"
"v_permlane16_swap_b32 v120,v119\n"
"v_max_f32 v119,v119,v120\n"
"v_mov_b32 v120,v119\n"
"s_nop 1\n"
"v_permlane32_swap_b32 v120,v119\n"
"v_max_f32 v119,v119,v120\n"
"s_bitcmp1_b32 s90,2\n"
"s_cselect_b32 s82,0xff800000,0\n"
"v_add_f32 v119,0x41980000,v119\n"
"v_max_f32 v119,s82,v119\n"
"v_cmp_lt_f32 vcc,s17,|v119|\n"
"s_nop 0\n"
"s_cbranch_vccz .Lfk_rare0_2\n"
"v_ceil_f32 v119,v119\n"
"v_sub_f32 v16,v16,v119\n"
"v_sub_f32 v17,v17,v119\n"
"v_sub_f32 v18,v18,v119\n"
"v_sub_f32 v19,v19,v119\n"
"v_sub_f32 v20,v20,v119\n"
"v_sub_f32 v21,v21,v119\n"
"v_sub_f32 v22,v22,v119\n"
"v_sub_f32 v23,v23,v119\n"
"v_sub_f32 v104,v104,v119\n"
"v_sub_f32 v105,v105,v119\n"
"v_sub_f32 v106,v106,v119\n"
"v_sub_f32 v107,v107,v119\n"
"s_cbranch_scc1 .Lfk_rare0_2\n"
"v_exp_f32 v119,-v119\n"
"s_nop 0\n"
"v_mul_f32 v192,v192,v119\n"
"v_mul_f32 v193,v193,v119\n"
"v_mul_f32 v194,v194,v119\n"
"v_mul_f32 v195,v195,v119\n"
"v_mul_f32 v196,v196,v119\n"
"v_mul_f32 v197,v197,v119\n"
"v_mul_f32 v198,v198,v119\n"
"v_mul_f32 v199,v199,v119\n"
"v_mul_f32 v200,v200,v119\n"
"v_mul_f32 v201,v201,v119\n"
"v_mul_f32 v202,v202,v119\n"
"v_mul_f32 v203,v203,v119\n"
"v_mul_f32 v204,v204,v119\n"
"v_mul_f32 v205,v205,v119\n"
"v_mul_f32 v206,v206,v119\n"
"v_mul_f32 v207,v207,v119\n"
"v_mul_f32 v208,v208,v119\n"
"v_mul_f32 v209,v209,v119\n"
"v_mul_f32 v210,v210,v119\n"
"v_mul_f32 v211,v211,v119\n"
"v_mul_f32 v212,v212,v119\n"
"v_mul_f32 v213,v213,v119\n"
"v_mul_f32 v214,v214,v119\n"
"v_mul_f32 v215,v215,v119\n"
"v_mul_f32 v216,v216,v119\n"
"v_mul_f32 v217,v217,v119\n"
"v_mul_f32 v218,v218,v119\n"
"v_mul_f32 v219,v219,v119\n"
"v_mul_f32 v220,v220,v119\n"
"v_mul_f32 v221,v221,v119\n"
"v_mul_f32 v222,v222,v119\n"
"v_mul_f32 v223,v223,v119\n"
"v_accvgpr_read_b32 v120,a136\n"
"v_mul_f32 v120,v120,v119\n"
"v_accvgpr_write_b32 a136,v120\n"
"v_lshlrev_b32 v120,16,v88\n"
"v_and_b32 v121,0xffff0000,v88\n"
"v_mul_f32 v120,v120,v119\n"
"v_mul_f32 v121,v121,v119\n"
"v_cvt_pk_bf16_f32 v88,v120,v121\n"
"v_accvgpr_read_b32 v120,a137\n"
"v_mul_f32 v120,v120,v119\n"
"v_accvgpr_write_b32 a137,v120\n"
"v_lshlrev_b32 v120,16,v89\n"
"v_and_b32 v121,0xffff0000,v89\n"
"v_mul_f32 v120,v120,v119\n"
"v_mul_f32 v121,v121,v119\n"
"v_cvt_pk_bf16_f32 v89,v120,v121\n"
"v_accvgpr_read_b32 v120,a138\n"
"v_mul_f32 v120,v120,v119\n"
"v_accvgpr_write_b32 a138,v120\n"
"v_lshlrev_b32 v120,16,v90\n"
"v_and_b32 v121,0xffff0000,v90\n"
"v_mul_f32 v120,v120,v119\n"
"v_mul_f32 v121,v121,v119\n"
"v_cvt_pk_bf16_f32 v90,v120,v121\n"
"v_accvgpr_read_b32 v120,a139\n"
"v_mul_f32 v120,v120,v119\n"
"v_accvgpr_write_b32 a139,v120\n"
"v_lshlrev_b32 v120,16,v91\n"
"v_and_b32 v121,0xffff0000,v91\n"
"v_mul_f32 v120,v120,v119\n"
"v_mul_f32 v121,v121,v119\n"
"v_cvt_pk_bf16_f32 v91,v120,v121\n"
".Lfk_rare0_2:\n"
"s_bitcmp1_b32 s91,3\n"
"s_cbranch_scc0 .Lfk_rare0_3\n"
"v_max3_f32 v119,v24,v25,v26\n"
"v_max3_f32 v120,v27,v28,v29\n"
"v_max3_f32 v119,v119,v30,v31\n"
"v_max_f32 v119,v119,v120\n"
"v_mov_b32 v120,v119\n"
"s_nop 1\n"
"v_permlane16_swap_b32 v120,v119\n"
"v_max_f32 v119,v119,v120\n"
"v_mov_b32 v120,v119\n"
"s_nop 1\n"
"v_permlane32_swap_b32 v120,v119\n"
"v_max_f32 v119,v119,v120\n"
"s_bitcmp1_b32 s90,3\n"
"s_cselect_b32 s82,0xff800000,0\n"
"v_add_f32 v119,0x41980000,v119\n"
"v_max_f32 v119,s82,v119\n"
"v_cmp_lt_f32 vcc,s17,|v119|\n"
"s_nop 0\n"
"s_cbranch_vccz .Lfk_rare0_3\n"
"v_ceil_f32 v119,v119\n"
"v_sub_f32 v24,v24,v119\n"
"v_sub_f32 v25,v25,v119\n"
"v_sub_f32 v26,v26,v119\n"
"v_sub_f32 v27,v27,v119\n"
"v_sub_f32 v28,v28,v119\n"
"v_sub_f32 v29,v29,v119\n"
"v_sub_f32 v30,v30,v119\n"
"v_sub_f32 v31,v31,v119\n"
"v_sub_f32 v108,v108,v119\n"
"v_sub_f32 v109,v109,v119\n"
"v_sub_f32 v110,v110,v119\n"
"v_sub_f32 v111,v111,v119\n"
"s_cbranch_scc1 .Lfk_rare0_3\n"
"v_exp_f32 v119,-v119\n"
"s_nop 0\n"
"v_mul_f32 v224,v224,v119\n"
"v_mul_f32 v225,v225,v119\n"
"v_mul_f32 v226,v226,v119\n"
"v_mul_f32 v227,v227,v119\n"
"v_mul_f32 v228,v228,v119\n"
"v_mul_f32 v229,v229,v119\n"
"v_mul_f32 v230,v230,v119\n"
"v_mul_f32 v231,v231,v119\n"
"v_mul_f32 v232,v232,v119\n"
"v_mul_f32 v233,v233,v119\n"
"v_mul_f32 v234,v234,v119\n"
"v_mul_f32 v235,v235,v119\n"
"v_mul_f32 v236,v236,v119\n"
"v_mul_f32 v237,v237,v119\n"
"v_mul_f32 v238,v238,v119\n"
"v_mul_f32 v239,v239,v119\n"
"v_mul_f32 v240,v240,v119\n"
"v_mul_f32 v241,v241,v119\n"
"v_mul_f32 v242,v242,v119\n"
"v_mul_f32 v243,v243,v119\n"
"v_mul_f32 v244,v244,v119\n"
"v_mul_f32 v245,v245,v119\n"
"v_mul_f32 v246,v246,v119\n"
"v_mul_f32 v247,v247,v119\n"
"v_mul_f32 v248,v248,v119\n"
"v_mul_f32 v249,v249,v119\n"
"v_mul_f32 v250,v250,v119\n"
"v_mul_f32 v251,v251,v119\n"
"v_mul_f32 v252,v252,v119\n"
"v_mul_f32 v253,v253,v119\n"
"v_mul_f32 v254,v254,v119\n"
"v_mul_f32 v255,v255,v119\n"
"v_accvgpr_read_b32 v120,a140\n"
"v_mul_f32 v120,v120,v119\n"
"v_accvgpr_write_b32 a140,v120\n"
"v_lshlrev_b32 v120,16,v92\n"
"v_and_b32 v121,0xffff0000,v92\n"
"v_mul_f32 v120,v120,v119\n"
"v_mul_f32 v121,v121,v119\n"
"v_cvt_pk_bf16_f32 v92,v120,v121\n"
"v_accvgpr_read_b32 v120,a141\n"
"v_mul_f32 v120,v120,v119\n"
"v_accvgpr_write_b32 a141,v120\n"
"v_lshlrev_b32 v120,16,v93\n"
"v_and_b32 v121,0xffff0000,v93\n"
"v_mul_f32 v120,v120,v119\n"
"v_mul_f32 v121,v121,v119\n"
"v_cvt_pk_bf16_f32 v93,v120,v121\n"
"v_accvgpr_read_b32 v120,a142\n"
"v_mul_f32 v120,v120,v119\n"
"v_accvgpr_write_b32 a142,v120\n"
"v_lshlrev_b32 v120,16,v94\n"
"v_and_b32 v121,0xffff0000,v94\n"
"v_mul_f32 v120,v120,v119\n"
"v_mul_f32 v121,v121,v119\n"
"v_cvt_pk_bf16_f32 v94,v120,v121\n"
"v_accvgpr_read_b32 v120,a143\n"
"v_mul_f32 v120,v120,v119\n"
"v_accvgpr_write_b32 a143,v120\n"
"v_lshlrev_b32 v120,16,v95\n"
"v_and_b32 v121,0xffff0000,v95\n"
"v_mul_f32 v120,v120,v119\n"
"v_mul_f32 v121,v121,v119\n"
"v_cvt_pk_bf16_f32 v95,v120,v121\n"
".Lfk_rare0_3:\n"
"s_mov_b32 s90,0\n"
"s_nop 1\n"
"s_setpc_b64 s[94:95]\n"
".Lfk_rare2:\n"
"s_nop 7\n"
"v_max3_f32 v119,v0,v1,v2\n"
"v_max3_f32 v120,v3,v4,v5\n"
"v_max3_f32 v119,v119,v6,v7\n"
"v_max_f32 v119,v119,v120\n"
"v_mov_b32 v120,v119\n"
"s_nop 1\n"
"v_permlane16_swap_b32 v120,v119\n"
"v_max_f32 v119,v119,v120\n"
"v_mov_b32 v120,v119\n"
"s_nop 1\n"
"v_permlane32_swap_b32 v120,v119\n"
"v_max_f32 v119,v119,v120\n"
"s_bitcmp1_b32 s90,0\n"
"s_cselect_b32 s82,0xff800000,0\n"
"v_add_f32 v119,0x41980000,v119\n"
"v_max_f32 v119,s82,v119\n"
"v_cmp_lt_f32 vcc,s17,|v119|\n"
"s_nop 0\n"
"s_cbranch_vccz .Lfk_rare2_0\n"
"v_ceil_f32 v119,v119\n"
"v_sub_f32 v0,v0,v119\n"
"v_sub_f32 v1,v1,v119\n"
"v_sub_f32 v2,v2,v119\n"
"v_sub_f32 v3,v3,v119\n"
"v_sub_f32 v4,v4,v119\n"
"v_sub_f32 v5,v5,v119\n"
"v_sub_f32 v6,v6,v119\n"
"v_sub_f32 v7,v7,v119\n"
"v_sub_f32 v96,v96,v119\n"
"v_sub_f32 v97,v97,v119\n"
"v_sub_f32 v98,v98,v119\n"
"v_sub_f32 v99,v99,v119\n"
"v_sub_f32 v32,v32,v119\n"
"v_sub_f32 v33,v33,v119\n"
"v_sub_f32 v34,v34,v119\n"
"v_sub_f32 v35,v35,v119\n"
"v_sub_f32 v36,v36,v119\n"
"v_sub_f32 v37,v37,v119\n"
"v_sub_f32 v38,v38,v119\n"
"v_sub_f32 v39,v39,v119\n"
"v_exp_f32 v0,v0\n"
"v_exp_f32 v1,v1\n"
"v_exp_f32 v2,v2\n"
"v_exp_f32 v3,v3\n"
"v_exp_f32 v4,v4\n"
"v_exp_f32 v5,v5\n"
"v_exp_f32 v6,v6\n"
"v_exp_f32 v7,v7\n"
"v_cvt_pk_bf16_f32 v64,v0,v1\n"
"v_cvt_pk_bf16_f32 v65,v2,v3\n"
"v_cvt_pk_bf16_f32 v66,v4,v5\n"
"v_cvt_pk_bf16_f32 v67,v6,v7\n"
"s_cbranch_scc1 .Lfk_rare2_0\n"
"v_exp_f32 v119,-v119\n"
"s_nop 0\n"
"v_mul_f32 v128,v128,v119\n"
"v_mul_f32 v129,v129,v119\n"
"v_mul_f32 v130,v130,v119\n"
"v_mul_f32 v131,v131,v119\n"
"v_mul_f32 v132,v132,v119\n"
"v_mul_f32 v133,v133,v119\n"
"v_mul_f32 v134,v134,v119\n"
"v_mul_f32 v135,v135,v119\n"
"v_mul_f32 v136,v136,v119\n"
"v_mul_f32 v137,v137,v119\n"
"v_mul_f32 v138,v138,v119\n"
"v_mul_f32 v139,v139,v119\n"
"v_mul_f32 v140,v140,v119\n"
"v_mul_f32 v141,v141,v119\n"
"v_mul_f32 v142,v142,v119\n"
"v_mul_f32 v143,v143,v119\n"
"v_mul_f32 v144,v144,v119\n"
"v_mul_f32 v145,v145,v119\n"
"v_mul_f32 v146,v146,v119\n"
"v_mul_f32 v147,v147,v119\n"
"v_mul_f32 v148,v148,v119\n"
"v_mul_f32 v149,v149,v119\n"
"v_mul_f32 v150,v150,v119\n"
"v_mul_f32 v151,v151,v119\n"
"v_mul_f32 v152,v152,v119\n"
"v_mul_f32 v153,v153,v119\n"
"v_mul_f32 v154,v154,v119\n"
"v_mul_f32 v155,v155,v119\n"
"v_mul_f32 v156,v156,v119\n"
"v_mul_f32 v157,v157,v119\n"
"v_mul_f32 v158,v158,v119\n"
"v_mul_f32 v159,v159,v119\n"
"v_accvgpr_read_b32 v120,a128\n"
"v_mul_f32 v120,v120,v119\n"
"v_accvgpr_write_b32 a128,v120\n"
"v_accvgpr_read_b32 v120,a129\n"
"v_mul_f32 v120,v120,v119\n"
"v_accvgpr_write_b32 a129,v120\n"
"v_accvgpr_read_b32 v120,a130\n"
"v_mul_f32 v120,v120,v119\n"
"v_accvgpr_write_b32 a130,v120\n"
"v_accvgpr_read_b32 v120,a131\n"
"v_mul_f32 v120,v120,v119\n"
"v_accvgpr_write_b32 a131,v120\n"
".Lfk_rare2_0:\n"
"v_max3_f32 v119,v8,v9,v10\n"
"v_max3_f32 v120,v11,v12,v13\n"
"v_max3_f32 v119,v119,v14,v15\n"
"v_max_f32 v119,v119,v120\n"
"v_mov_b32 v120,v119\n"
"s_nop 1\n"
"v_permlane16_swap_b32 v120,v119\n"
"v_max_f32 v119,v119,v120\n"
"v_mov_b32 v120,v119\n"
"s_nop 1\n"
"v_permlane32_swap_b32 v120,v119\n"
"v_max_f32 v119,v119,v120\n"
"s_bitcmp1_b32 s90,1\n"
"s_cselect_b32 s82,0xff800000,0\n"
"v_add_f32 v119,0x41980000,v119\n"
"v_max_f32 v119,s82,v119\n"
"v_cmp_lt_f32 vcc,s17,|v119|\n"
"s_nop 0\n"
"s_cbranch_vccz .Lfk_rare2_1\n"
"v_ceil_f32 v119,v119\n"
"v_sub_f32 v8,v8,v119\n"
"v_sub_f32 v9,v9,v119\n"
"v_sub_f32 v10,v10,v119\n"
"v_sub_f32 v11,v11,v119\n"
"v_sub_f32 v12,v12,v119\n"
"v_sub_f32 v13,v13,v119\n"
"v_sub_f32 v14,v14,v119\n"
"v_sub_f32 v15,v15,v119\n"
"v_sub_f32 v100,v100,v119\n"
"v_sub_f32 v101,v101,v119\n"
"v_sub_f32 v102,v102,v119\n"
"v_sub_f32 v103,v103,v119\n"
"v_sub_f32 v40,v40,v119\n"
"v_sub_f32 v41,v41,v119\n"
"v_sub_f32 v42,v42,v119\n"
"v_sub_f32 v43,v43,v119\n"
"v_sub_f32 v44,v44,v119\n"
"v_sub_f32 v45,v45,v119\n"
"v_sub_f32 v46,v46,v119\n"
"v_sub_f32 v47,v47,v119\n"
"v_exp_f32 v8,v8\n"
"v_exp_f32 v9,v9\n"
"v_exp_f32 v10,v10\n"
"v_exp_f32 v11,v11\n"
"v_exp_f32 v12,v12\n"
"v_exp_f32 v13,v13\n"
"v_exp_f32 v14,v14\n"
"v_exp_f32 v15,v15\n"
"v_cvt_pk_bf16_f32 v68,v8,v9\n"
"v_cvt_pk_bf16_f32 v69,v10,v11\n"
"v_cvt_pk_bf16_f32 v70,v12,v13\n"
"v_cvt_pk_bf16_f32 v71,v14,v15\n"
"s_cbranch_scc1 .Lfk_rare2_1\n"
"v_exp_f32 v119,-v119\n"
"s_nop 0\n"
"v_mul_f32 v160,v160,v119\n"
"v_mul_f32 v161,v161,v119\n"
"v_mul_f32 v162,v162,v119\n"
"v_mul_f32 v163,v163,v119\n"
"v_mul_f32 v164,v164,v119\n"
"v_mul_f32 v165,v165,v119\n"
"v_mul_f32 v166,v166,v119\n"
"v_mul_f32 v167,v167,v119\n"
"v_mul_f32 v168,v168,v119\n"
"v_mul_f32 v169,v169,v119\n"
"v_mul_f32 v170,v170,v119\n"
"v_mul_f32 v171,v171,v119\n"
"v_mul_f32 v172,v172,v119\n"
"v_mul_f32 v173,v173,v119\n"
"v_mul_f32 v174,v174,v119\n"
"v_mul_f32 v175,v175,v119\n"
"v_mul_f32 v176,v176,v119\n"
"v_mul_f32 v177,v177,v119\n"
"v_mul_f32 v178,v178,v119\n"
"v_mul_f32 v179,v179,v119\n"
"v_mul_f32 v180,v180,v119\n"
"v_mul_f32 v181,v181,v119\n"
"v_mul_f32 v182,v182,v119\n"
"v_mul_f32 v183,v183,v119\n"
"v_mul_f32 v184,v184,v119\n"
"v_mul_f32 v185,v185,v119\n"
"v_mul_f32 v186,v186,v119\n"
"v_mul_f32 v187,v187,v119\n"
"v_mul_f32 v188,v188,v119\n"
"v_mul_f32 v189,v189,v119\n"
"v_mul_f32 v190,v190,v119\n"
"v_mul_f32 v191,v191,v119\n"
"v_accvgpr_read_b32 v120,a132\n"
"v_mul_f32 v120,v120,v119\n"
"v_accvgpr_write_b32 a132,v120\n"
"v_accvgpr_read_b32 v120,a133\n"
"v_mul_f32 v120,v120,v119\n"
"v_accvgpr_write_b32 a133,v120\n"
"v_accvgpr_read_b32 v120,a134\n"
"v_mul_f32 v120,v120,v119\n"
"v_accvgpr_write_b32 a134,v120\n"
"v_accvgpr_read_b32 v120,a135\n"
"v_mul_f32 v120,v120,v119\n"
"v_accvgpr_write_b32 a135,v120\n"
".Lfk_rare2_1:\n"
"v_max3_f32 v119,v16,v17,v18\n"
"v_max3_f32 v120,v19,v20,v21\n"
"v_max3_f32 v119,v119,v22,v23\n"
"v_max_f32 v119,v119,v120\n"
"v_mov_b32 v120,v119\n"
"s_nop 1\n"
"v_permlane16_swap_b32 v120,v119\n"
"v_max_f32 v119,v119,v120\n"
"v_mov_b32 v120,v119\n"
"s_nop 1\n"
"v_permlane32_swap_b32 v120,v119\n"
"v_max_f32 v119,v119,v120\n"
"s_bitcmp1_b32 s90,2\n"
"s_cselect_b32 s82,0xff800000,0\n"
"v_add_f32 v119,0x41980000,v119\n"
"v_max_f32 v119,s82,v119\n"
"v_cmp_lt_f32 vcc,s17,|v119|\n"
"s_nop 0\n"
"s_cbranch_vccz .Lfk_rare2_2\n"
"v_ceil_f32 v119,v119\n"
"v_sub_f32 v16,v16,v119\n"
"v_sub_f32 v17,v17,v119\n"
"v_sub_f32 v18,v18,v119\n"
"v_sub_f32 v19,v19,v119\n"
"v_sub_f32 v20,v20,v119\n"
"v_sub_f32 v21,v21,v119\n"
"v_sub_f32 v22,v22,v119\n"
"v_sub_f32 v23,v23,v119\n"
"v_sub_f32 v104,v104,v119\n"
"v_sub_f32 v105,v105,v119\n"
"v_sub_f32 v106,v106,v119\n"
"v_sub_f32 v107,v107,v119\n"
"v_sub_f32 v48,v48,v119\n"
"v_sub_f32 v49,v49,v119\n"
"v_sub_f32 v50,v50,v119\n"
"v_sub_f32 v51,v51,v119\n"
"v_sub_f32 v52,v52,v119\n"
"v_sub_f32 v53,v53,v119\n"
"v_sub_f32 v54,v54,v119\n"
"v_sub_f32 v55,v55,v119\n"
"v_exp_f32 v16,v16\n"
"v_exp_f32 v17,v17\n"
"v_exp_f32 v18,v18\n"
"v_exp_f32 v19,v19\n"
"v_exp_f32 v20,v20\n"
"v_exp_f32 v21,v21\n"
"v_exp_f32 v22,v22\n"
"v_exp_f32 v23,v23\n"
"v_cvt_pk_bf16_f32 v72,v16,v17\n"
"v_cvt_pk_bf16_f32 v73,v18,v19\n"
"v_cvt_pk_bf16_f32 v74,v20,v21\n"
"v_cvt_pk_bf16_f32 v75,v22,v23\n"
"s_cbranch_scc1 .Lfk_rare2_2\n"
"v_exp_f32 v119,-v119\n"
"s_nop 0\n"
"v_mul_f32 v192,v192,v119\n"
"v_mul_f32 v193,v193,v119\n"
"v_mul_f32 v194,v194,v119\n"
"v_mul_f32 v195,v195,v119\n"
"v_mul_f32 v196,v196,v119\n"
"v_mul_f32 v197,v197,v119\n"
"v_mul_f32 v198,v198,v119\n"
"v_mul_f32 v199,v199,v119\n"
"v_mul_f32 v200,v200,v119\n"
"v_mul_f32 v201,v201,v119\n"
"v_mul_f32 v202,v202,v119\n"
"v_mul_f32 v203,v203,v119\n"
"v_mul_f32 v204,v204,v119\n"
"v_mul_f32 v205,v205,v119\n"
"v_mul_f32 v206,v206,v119\n"
"v_mul_f32 v207,v207,v119\n"
"v_mul_f32 v208,v208,v119\n"
"v_mul_f32 v209,v209,v119\n"
"v_mul_f32 v210,v210,v119\n"
"v_mul_f32 v211,v211,v119\n"
"v_mul_f32 v212,v212,v119\n"
"v_mul_f32 v213,v213,v119\n"
"v_mul_f32 v214,v214,v119\n"
"v_mul_f32 v215,v215,v119\n"
"v_mul_f32 v216,v216,v119\n"
"v_mul_f32 v217,v217,v119\n"
"v_mul_f32 v218,v218,v119\n"
"v_mul_f32 v219,v219,v119\n"
"v_mul_f32 v220,v220,v119\n"
"v_mul_f32 v221,v221,v119\n"
"v_mul_f32 v222,v222,v119\n"
"v_mul_f32 v223,v223,v119\n"
"v_accvgpr_read_b32 v120,a136\n"
"v_mul_f32 v120,v120,v119\n"
"v_accvgpr_write_b32 a136,v120\n"
"v_accvgpr_read_b32 v120,a137\n"
"v_mul_f32 v120,v120,v119\n"
"v_accvgpr_write_b32 a137,v120\n"
"v_accvgpr_read_b32 v120,a138\n"
"v_mul_f32 v120,v120,v119\n"
"v_accvgpr_write_b32 a138,v120\n"
"v_accvgpr_read_b32 v120,a139\n"
"v_mul_f32 v120,v120,v119\n"
"v_accvgpr_write_b32 a139,v120\n"
".Lfk_rare2_2:\n"
"v_max3_f32 v119,v24,v25,v26\n"
"v_max3_f32 v120,v27,v28,v29\n"
"v_max3_f32 v119,v119,v30,v31\n"
"v_max_f32 v119,v119,v120\n"
"v_mov_b32 v120,v119\n"
"s_nop 1\n"
"v_permlane16_swap_b32 v120,v119\n"
"v_max_f32 v119,v119,v120\n"
"v_mov_b32 v120,v119\n"
"s_nop 1\n"
"v_permlane32_swap_b32 v120,v119\n"
"v_max_f32 v119,v119,v120\n"
"s_bitcmp1_b32 s90,3\n"
"s_cselect_b32 s82,0xff800000,0\n"
"v_add_f32 v119,0x41980000,v119\n"
"v_max_f32 v119,s82,v119\n"
"v_cmp_lt_f32 vcc,s17,|v119|\n"
"s_nop 0\n"
"s_cbranch_vccz .Lfk_rare2_3\n"
"v_ceil_f32 v119,v119\n"
"v_sub_f32 v24,v24,v119\n"
"v_sub_f32 v25,v25,v119\n"
"v_sub_f32 v26,v26,v119\n"
"v_sub_f32 v27,v27,v119\n"
"v_sub_f32 v28,v28,v119\n"
"v_sub_f32 v29,v29,v119\n"
"v_sub_f32 v30,v30,v119\n"
"v_sub_f32 v31,v31,v119\n"
"v_sub_f32 v108,v108,v119\n"
"v_sub_f32 v109,v109,v119\n"
"v_sub_f32 v110,v110,v119\n"
"v_sub_f32 v111,v111,v119\n"
"v_sub_f32 v56,v56,v119\n"
"v_sub_f32 v57,v57,v119\n"
"v_sub_f32 v58,v58,v119\n"
"v_sub_f32 v59,v59,v119\n"
"v_sub_f32 v60,v60,v119\n"
"v_sub_f32 v61,v61,v119\n"
"v_sub_f32 v62,v62,v119\n"
"v_sub_f32 v63,v63,v119\n"
"v_exp_f32 v24,v24\n"
"v_exp_f32 v25,v25\n"
"v_exp_f32 v26,v26\n"
"v_exp_f32 v27,v27\n"
"v_exp_f32 v28,v28\n"
"v_exp_f32 v29,v29\n"
"v_exp_f32 v30,v30\n"
"v_exp_f32 v31,v31\n"
"v_cvt_pk_bf16_f32 v76,v24,v25\n"
"v_cvt_pk_bf16_f32 v77,v26,v27\n"
"v_cvt_pk_bf16_f32 v78,v28,v29\n"
"v_cvt_pk_bf16_f32 v79,v30,v31\n"
"s_cbranch_scc1 .Lfk_rare2_3\n"
"v_exp_f32 v119,-v119\n"
"s_nop 0\n"
"v_mul_f32 v224,v224,v119\n"
"v_mul_f32 v225,v225,v119\n"
"v_mul_f32 v226,v226,v119\n"
"v_mul_f32 v227,v227,v119\n"
"v_mul_f32 v228,v228,v119\n"
"v_mul_f32 v229,v229,v119\n"
"v_mul_f32 v230,v230,v119\n"
"v_mul_f32 v231,v231,v119\n"
"v_mul_f32 v232,v232,v119\n"
"v_mul_f32 v233,v233,v119\n"
"v_mul_f32 v234,v234,v119\n"
"v_mul_f32 v235,v235,v119\n"
"v_mul_f32 v236,v236,v119\n"
"v_mul_f32 v237,v237,v119\n"
"v_mul_f32 v238,v238,v119\n"
"v_mul_f32 v239,v239,v119\n"
"v_mul_f32 v240,v240,v119\n"
"v_mul_f32 v241,v241,v119\n"
"v_mul_f32 v242,v242,v119\n"
"v_mul_f32 v243,v243,v119\n"
"v_mul_f32 v244,v244,v119\n"
"v_mul_f32 v245,v245,v119\n"
"v_mul_f32 v246,v246,v119\n"
"v_mul_f32 v247,v247,v119\n"
"v_mul_f32 v248,v248,v119\n"
"v_mul_f32 v249,v249,v119\n"
"v_mul_f32 v250,v250,v119\n"
"v_mul_f32 v251,v251,v119\n"
"v_mul_f32 v252,v252,v119\n"
"v_mul_f32 v253,v253,v119\n"
"v_mul_f32 v254,v254,v119\n"
"v_mul_f32 v255,v255,v119\n"
"v_accvgpr_read_b32 v120,a140\n"
"v_mul_f32 v120,v120,v119\n"
"v_accvgpr_write_b32 a140,v120\n"
"v_accvgpr_read_b32 v120,a141\n"
"v_mul_f32 v120,v120,v119\n"
"v_accvgpr_write_b32 a141,v120\n"
"v_accvgpr_read_b32 v120,a142\n"
"v_mul_f32 v120,v120,v119\n"
"v_accvgpr_write_b32 a142,v120\n"
"v_accvgpr_read_b32 v120,a143\n"
"v_mul_f32 v120,v120,v119\n"
"v_accvgpr_write_b32 a143,v120\n"
".Lfk_rare2_3:\n"
"s_mov_b32 s90,0\n"
"s_nop 1\n"
"s_setpc_b64 s[94:95]\n"
".Lfk_mslow0:\n"
"s_nop 7\n"
"v_min_f32 v119,v98,v104\n"
"v_sub_f32 v120,v119,v104\n"
"v_sub_f32 v119,v119,v98\n"
"v_exp_f32 v120,v120\n"
"v_exp_f32 v119,v119\n"
"v_mul_f32 v192,v192,v120\n"
"v_mul_f32 v193,v193,v120\n"
"v_mul_f32 v194,v194,v120\n"
"v_mul_f32 v195,v195,v120\n"
"v_mul_f32 v196,v196,v120\n"
"v_mul_f32 v197,v197,v120\n"
"v_mul_f32 v198,v198,v120\n"
"v_mul_f32 v199,v199,v120\n"
"v_mul_f32 v200,v200,v120\n"
"v_mul_f32 v201,v201,v120\n"
"v_mul_f32 v202,v202,v120\n"
"v_mul_f32 v203,v203,v120\n"
"v_mul_f32 v204,v204,v120\n"
"v_mul_f32 v205,v205,v120\n"
"v_mul_f32 v206,v206,v120\n"
"v_mul_f32 v207,v207,v120\n"
"v_mul_f32 v208,v208,v120\n"
"v_mul_f32 v209,v209,v120\n"
"v_mul_f32 v210,v210,v120\n"
"v_mul_f32 v211,v211,v120\n"
"v_mul_f32 v212,v212,v120\n"
"v_mul_f32 v213,v213,v120\n"
"v_mul_f32 v214,v214,v120\n"
"v_mul_f32 v215,v215,v120\n"
"v_mul_f32 v216,v216,v120\n"
"v_mul_f32 v217,v217,v120\n"
"v_mul_f32 v218,v218,v120\n"
"v_mul_f32 v219,v219,v120\n"
"v_mul_f32 v220,v220,v120\n"
"v_mul_f32 v221,v221,v120\n"
"v_mul_f32 v222,v222,v120\n"
"v_mul_f32 v223,v223,v120\n"
"v_accvgpr_read_b32 v121,a136\n"
"v_mul_f32 v121,v121,v120\n"
"v_accvgpr_write_b32 a136,v121\n"
"v_mul_f32 v99,v99,v119\n"
"v_accvgpr_read_b32 v121,a64\n"
"v_lshlrev_b32 v120,16,v121\n"
"v_and_b32 v121,0xffff0000,v121\n"
"v_mul_f32 v120,v120,v119\n"
"v_mul_f32 v121,v121,v119\n"
"v_cvt_pk_bf16_f32 v121,v120,v121\n"
"v_accvgpr_write_b32 a64,v121\n"
"v_accvgpr_read_b32 v121,a65\n"
"v_lshlrev_b32 v120,16,v121\n"
"v_and_b32 v121,0xffff0000,v121\n"
"v_mul_f32 v120,v120,v119\n"
"v_mul_f32 v121,v121,v119\n"
"v_cvt_pk_bf16_f32 v121,v120,v121\n"
"v_accvgpr_write_b32 a65,v121\n"
"v_accvgpr_read_b32 v121,a66\n"
"v_lshlrev_b32 v120,16,v121\n"
"v_and_b32 v121,0xffff0000,v121\n"
"v_mul_f32 v120,v120,v119\n"
"v_mul_f32 v121,v121,v119\n"
"v_cvt_pk_bf16_f32 v121,v120,v121\n"
"v_accvgpr_write_b32 a66,v121\n"
"v_accvgpr_read_b32 v121,a67\n"
"v_lshlrev_b32 v120,16,v121\n"
"v_and_b32 v121,0xffff0000,v121\n"
"v_mul_f32 v120,v120,v119\n"
"v_mul_f32 v121,v121,v119\n"
"v_cvt_pk_bf16_f32 v121,v120,v121\n"
"v_accvgpr_write_b32 a67,v121\n"
"v_accvgpr_read_b32 v121,a68\n"
"v_lshlrev_b32 v120,16,v121\n"
"v_and_b32 v121,0xffff0000,v121\n"
"v_mul_f32 v120,v120,v119\n"
"v_mul_f32 v121,v121,v119\n"
"v_cvt_pk_bf16_f32 v121,v120,v121\n"
"v_accvgpr_write_b32 a68,v121\n"
"v_accvgpr_read_b32 v121,a69\n"
"v_lshlrev_b32 v120,16,v121\n"
"v_and_b32 v121,0xffff0000,v121\n"
"v_mul_f32 v120,v120,v119\n"
"v_mul_f32 v121,v121,v119\n"
"v_cvt_pk_bf16_f32 v121,v120,v121\n"
"v_accvgpr_write_b32 a69,v121\n"
"v_accvgpr_read_b32 v121,a70\n"
"v_lshlrev_b32 v120,16,v121\n"
"v_and_b32 v121,0xffff0000,v121\n"
"v_mul_f32 v120,v120,v119\n"
"v_mul_f32 v121,v121,v119\n"
"v_cvt_pk_bf16_f32 v121,v120,v121\n"
"v_accvgpr_write_b32 a70,v121\n"
"v_accvgpr_read_b32 v121,a71\n"
"v_lshlrev_b32 v120,16,v121\n"
"v_and_b32 v121,0xffff0000,v121\n"
"v_mul_f32 v120,v120,v119\n"
"v_mul_f32 v121,v121,v119\n"
"v_cvt_pk_bf16_f32 v121,v120,v121\n"
"v_accvgpr_write_b32 a71,v121\n"
"v_accvgpr_read_b32 v121,a72\n"
"v_lshlrev_b32 v120,16,v121\n"
"v_and_b32 v121,0xffff0000,v121\n"
"v_mul_f32 v120,v120,v119\n"
"v_mul_f32 v121,v121,v119\n"
"v_cvt_pk_bf16_f32 v121,v120,v121\n"
"v_accvgpr_write_b32 a72,v121\n"
"v_accvgpr_read_b32 v121,a73\n"
"v_lshlrev_b32 v120,16,v121\n"
"v_and_b32 v121,0xffff0000,v121\n"
"v_mul_f32 v120,v120,v119\n"
"v_mul_f32 v121,v121,v119\n"
"v_cvt_pk_bf16_f32 v121,v120,v121\n"
"v_accvgpr_write_b32 a73,v121\n"
"v_accvgpr_read_b32 v121,a74\n"
"v_lshlrev_b32 v120,16,v121\n"
"v_and_b32 v121,0xffff0000,v121\n"
"v_mul_f32 v120,v120,v119\n"
"v_mul_f32 v121,v121,v119\n"
"v_cvt_pk_bf16_f32 v121,v120,v121\n"
"v_accvgpr_write_b32 a74,v121\n"
"v_accvgpr_read_b32 v121,a75\n"
"v_lshlrev_b32 v120,16,v121\n"
"v_and_b32 v121,0xffff0000,v121\n"
"v_mul_f32 v120,v120,v119\n"
"v_mul_f32 v121,v121,v119\n"
"v_cvt_pk_bf16_f32 v121,v120,v121\n"
"v_accvgpr_write_b32 a75,v121\n"
"v_accvgpr_read_b32 v121,a76\n"
"v_lshlrev_b32 v120,16,v121\n"
"v_and_b32 v121,0xffff0000,v121\n"
"v_mul_f32 v120,v120,v119\n"
"v_mul_f32 v121,v121,v119\n"
"v_cvt_pk_bf16_f32 v121,v120,v121\n"
"v_accvgpr_write_b32 a76,v121\n"
"v_accvgpr_read_b32 v121,a77\n"
"v_lshlrev_b32 v120,16,v121\n"
"v_and_b32 v121,0xffff0000,v121\n"
"v_mul_f32 v120,v120,v119\n"
"v_mul_f32 v121,v121,v119\n"
"v_cvt_pk_bf16_f32 v121,v120,v121\n"
"v_accvgpr_write_b32 a77,v121\n"
"v_accvgpr_read_b32 v121,a78\n"
"v_lshlrev_b32 v120,16,v121\n"
"v_and_b32 v121,0xffff0000,v121\n"
"v_mul_f32 v120,v120,v119\n"
"v_mul_f32 v121,v121,v119\n"
"v_cvt_pk_bf16_f32 v121,v120,v121\n"
"v_accvgpr_write_b32 a78,v121\n"
"v_accvgpr_read_b32 v121,a79\n"
"v_lshlrev_b32 v120,16,v121\n"
"v_and_b32 v121,0xffff0000,v121\n"
"v_mul_f32 v120,v120,v119\n"
"v_mul_f32 v121,v121,v119\n"
"v_cvt_pk_bf16_f32 v121,v120,v121\n"
"v_accvgpr_write_b32 a79,v121\n"
"s_nop 1\n"
"s_setpc_b64 s[94:95]\n"
".Lfk_rare1:\n"
"s_nop 7\n"
"s_bitcmp1_b32 s91,0\n"
"s_cbranch_scc0 .Lfk_rare1_0\n"
"v_max3_f32 v119,v32,v33,v34\n"
"v_max3_f32 v120,v35,v36,v37\n"
"v_max3_f32 v119,v119,v38,v39\n"
"v_max_f32 v119,v119,v120\n"
"v_mov_b32 v120,v119\n"
"s_nop 1\n"
"v_permlane16_swap_b32 v120,v119\n"
"v_max_f32 v119,v119,v120\n"
"v_mov_b32 v120,v119\n"
"s_nop 1\n"
"v_permlane32_swap_b32 v120,v119\n"
"v_max_f32 v119,v119,v120\n"
"s_bitcmp1_b32 s90,0\n"
"s_cselect_b32 s82,0xff800000,0\n"
"v_add_f32 v119,0x41980000,v119\n"
"v_max_f32 v119,s82,v119\n"
"v_cmp_lt_f32 vcc,s17,|v119|\n"
"s_nop 0\n"
"s_cbranch_vccz .Lfk_rare1_0\n"
"v_ceil_f32 v119,v119\n"
"v_sub_f32 v32,v32,v119\n"
"v_sub_f32 v33,v33,v119\n"
"v_sub_f32 v34,v34,v119\n"
"v_sub_f32 v35,v35,v119\n"
"v_sub_f32 v36,v36,v119\n"
"v_sub_f32 v37,v37,v119\n"
"v_sub_f32 v38,v38,v119\n"
"v_sub_f32 v39,v39,v119\n"
"v_sub_f32 v96,v96,v119\n"
"v_sub_f32 v97,v97,v119\n"
"v_sub_f32 v98,v98,v119\n"
"v_sub_f32 v99,v99,v119\n"
"s_cbranch_scc1 .Lfk_rare1_0\n"
"v_exp_f32 v119,-v119\n"
"s_nop 0\n"
"v_mul_f32 v128,v128,v119\n"
"v_mul_f32 v129,v129,v119\n"
"v_mul_f32 v130,v130,v119\n"
"v_mul_f32 v131,v131,v119\n"
"v_mul_f32 v132,v132,v119\n"
"v_mul_f32 v133,v133,v119\n"
"v_mul_f32 v134,v134,v119\n"
"v_mul_f32 v135,v135,v119\n"
"v_mul_f32 v136,v136,v119\n"
"v_mul_f32 v137,v137,v119\n"
"v_mul_f32 v138,v138,v119\n"
"v_mul_f32 v139,v139,v119\n"
"v_mul_f32 v140,v140,v119\n"
"v_mul_f32 v141,v141,v119\n"
"v_mul_f32 v142,v142,v119\n"
"v_mul_f32 v143,v143,v119\n"
"v_mul_f32 v144,v144,v119\n"
"v_mul_f32 v145,v145,v119\n"
"v_mul_f32 v146,v146,v119\n"
"v_mul_f32 v147,v147,v119\n"
"v_mul_f32 v148,v148,v119\n"
"v_mul_f32 v149,v149,v119\n"
"v_mul_f32 v150,v150,v119\n"
"v_mul_f32 v151,v151,v119\n"
"v_mul_f32 v152,v152,v119\n"
"v_mul_f32 v153,v153,v119\n"
"v_mul_f32 v154,v154,v119\n"
"v_mul_f32 v155,v155,v119\n"
"v_mul_f32 v156,v156,v119\n"
"v_mul_f32 v157,v157,v119\n"
"v_mul_f32 v158,v158,v119\n"
"v_mul_f32 v159,v159,v119\n"
"v_accvgpr_read_b32 v120,a128\n"
"v_mul_f32 v120,v120,v119\n"
"v_accvgpr_write_b32 a128,v120\n"
"v_lshlrev_b32 v120,16,v64\n"
"v_and_b32 v121,0xffff0000,v64\n"
"v_mul_f32 v120,v120,v119\n"
"v_mul_f32 v121,v121,v119\n"
"v_cvt_pk_bf16_f32 v64,v120,v121\n"
"v_accvgpr_read_b32 v120,a129\n"
"v_mul_f32 v120,v120,v119\n"
"v_accvgpr_write_b32 a129,v120\n"
"v_lshlrev_b32 v120,16,v65\n"
"v_and_b32 v121,0xffff0000,v65\n"
"v_mul_f32 v120,v120,v119\n"
"v_mul_f32 v121,v121,v119\n"
"v_cvt_pk_bf16_f32 v65,v120,v121\n"
"v_accvgpr_read_b32 v120,a130\n"
"v_mul_f32 v120,v120,v119\n"
"v_accvgpr_write_b32 a130,v120\n"
"v_lshlrev_b32 v120,16,v66\n"
"v_and_b32 v121,0xffff0000,v66\n"
"v_mul_f32 v120,v120,v119\n"
"v_mul_f32 v121,v121,v119\n"
"v_cvt_pk_bf16_f32 v66,v120,v121\n"
"v_accvgpr_read_b32 v120,a131\n"
"v_mul_f32 v120,v120,v119\n"
"v_accvgpr_write_b32 a131,v120\n"
"v_lshlrev_b32 v120,16,v67\n"
"v_and_b32 v121,0xffff0000,v67\n"
"v_mul_f32 v120,v120,v119\n"
"v_mul_f32 v121,v121,v119\n"
"v_cvt_pk_bf16_f32 v67,v120,v121\n"
".Lfk_rare1_0:\n"
"s_bitcmp1_b32 s91,1\n"
"s_cbranch_scc0 .Lfk_rare1_1\n"
"v_max3_f32 v119,v40,v41,v42\n"
"v_max3_f32 v120,v43,v44,v45\n"
"v_max3_f32 v119,v119,v46,v47\n"
"v_max_f32 v119,v119,v120\n"
"v_mov_b32 v120,v119\n"
"s_nop 1\n"
"v_permlane16_swap_b32 v120,v119\n"
"v_max_f32 v119,v119,v120\n"
"v_mov_b32 v120,v119\n"
"s_nop 1\n"
"v_permlane32_swap_b32 v120,v119\n"
"v_max_f32 v119,v119,v120\n"
"s_bitcmp1_b32 s90,1\n"
"s_cselect_b32 s82,0xff800000,0\n"
"v_add_f32 v119,0x41980000,v119\n"
"v_max_f32 v119,s82,v119\n"
"v_cmp_lt_f32 vcc,s17,|v119|\n"
"s_nop 0\n"
"s_cbranch_vccz .Lfk_rare1_1\n"
"v_ceil_f32 v119,v119\n"
"v_sub_f32 v40,v40,v119\n"
"v_sub_f32 v41,v41,v119\n"
"v_sub_f32 v42,v42,v119\n"
"v_sub_f32 v43,v43,v119\n"
"v_sub_f32 v44,v44,v119\n"
"v_sub_f32 v45,v45,v119\n"
"v_sub_f32 v46,v46,v119\n"
"v_sub_f32 v47,v47,v119\n"
"v_sub_f32 v100,v100,v119\n"
"v_sub_f32 v101,v101,v119\n"
"v_sub_f32 v102,v102,v119\n"
"v_sub_f32 v103,v103,v119\n"
"s_cbranch_scc1 .Lfk_rare1_1\n"
"v_exp_f32 v119,-v119\n"
"s_nop 0\n"
"v_mul_f32 v160,v160,v119\n"
"v_mul_f32 v161,v161,v119\n"
"v_mul_f32 v162,v162,v119\n"
"v_mul_f32 v163,v163,v119\n"
"v_mul_f32 v164,v164,v119\n"
"v_mul_f32 v165,v165,v119\n"
"v_mul_f32 v166,v166,v119\n"
"v_mul_f32 v167,v167,v119\n"
"v_mul_f32 v168,v168,v119\n"
"v_mul_f32 v169,v169,v119\n"
"v_mul_f32 v170,v170,v119\n"
"v_mul_f32 v171,v171,v119\n"
"v_mul_f32 v172,v172,v119\n"
"v_mul_f32 v173,v173,v119\n"
"v_mul_f32 v174,v174,v119\n"
"v_mul_f32 v175,v175,v119\n"
"v_mul_f32 v176,v176,v119\n"
"v_mul_f32 v177,v177,v119\n"
"v_mul_f32 v178,v178,v119\n"
"v_mul_f32 v179,v179,v119\n"
"v_mul_f32 v180,v180,v119\n"
"v_mul_f32 v181,v181,v119\n"
"v_mul_f32 v182,v182,v119\n"
"v_mul_f32 v183,v183,v119\n"
"v_mul_f32 v184,v184,v119\n"
"v_mul_f32 v185,v185,v119\n"
"v_mul_f32 v186,v186,v119\n"
"v_mul_f32 v187,v187,v119\n"
"v_mul_f32 v188,v188,v119\n"
"v_mul_f32 v189,v189,v119\n"
"v_mul_f32 v190,v190,v119\n"
"v_mul_f32 v191,v191,v119\n"
"v_accvgpr_read_b32 v120,a132\n"
"v_mul_f32 v120,v120,v119\n"
"v_accvgpr_write_b32 a132,v120\n"
"v_lshlrev_b32 v120,16,v68\n"
"v_and_b32 v121,0xffff0000,v68\n"
"v_mul_f32 v120,v120,v119\n"
"v_mul_f32 v121,v121,v119\n"
"v_cvt_pk_bf16_f32 v68,v120,v121\n"
"v_accvgpr_read_b32 v120,a133\n"
"v_mul_f32 v120,v120,v119\n"
"v_accvgpr_write_b32 a133,v120\n"
"v_lshlrev_b32 v120,16,v69\n"
"v_and_b32 v121,0xffff0000,v69\n"
"v_mul_f32 v120,v120,v119\n"
"v_mul_f32 v121,v121,v119\n"
"v_cvt_pk_bf16_f32 v69,v120,v121\n"
"v_accvgpr_read_b32 v120,a134\n"
"v_mul_f32 v120,v120,v119\n"
"v_accvgpr_write_b32 a134,v120\n"
"v_lshlrev_b32 v120,16,v70\n"
"v_and_b32 v121,0xffff0000,v70\n"
"v_mul_f32 v120,v120,v119\n"
"v_mul_f32 v121,v121,v119\n"
"v_cvt_pk_bf16_f32 v70,v120,v121\n"
"v_accvgpr_read_b32 v120,a135\n"
"v_mul_f32 v120,v120,v119\n"
"v_accvgpr_write_b32 a135,v120\n"
"v_lshlrev_b32 v120,16,v71\n"
"v_and_b32 v121,0xffff0000,v71\n"
"v_mul_f32 v120,v120,v119\n"
"v_mul_f32 v121,v121,v119\n"
"v_cvt_pk_bf16_f32 v71,v120,v121\n"
".Lfk_rare1_1:\n"
"s_bitcmp1_b32 s91,2\n"
"s_cbranch_scc0 .Lfk_rare1_2\n"
"v_max3_f32 v119,v48,v49,v50\n"
"v_max3_f32 v120,v51,v52,v53\n"
"v_max3_f32 v119,v119,v54,v55\n"
"v_max_f32 v119,v119,v120\n"
"v_mov_b32 v120,v119\n"
"s_nop 1\n"
"v_permlane16_swap_b32 v120,v119\n"
"v_max_f32 v119,v119,v120\n"
"v_mov_b32 v120,v119\n"
"s_nop 1\n"
"v_permlane32_swap_b32 v120,v119\n"
"v_max_f32 v119,v119,v120\n"
"s_bitcmp1_b32 s90,2\n"
"s_cselect_b32 s82,0xff800000,0\n"
"v_add_f32 v119,0x41980000,v119\n"
"v_max_f32 v119,s82,v119\n"
"v_cmp_lt_f32 vcc,s17,|v119|\n"
"s_nop 0\n"
"s_cbranch_vccz .Lfk_rare1_2\n"
"v_ceil_f32 v119,v119\n"
"v_sub_f32 v48,v48,v119\n"
"v_sub_f32 v49,v49,v119\n"
"v_sub_f32 v50,v50,v119\n"
"v_sub_f32 v51,v51,v119\n"
"v_sub_f32 v52,v52,v119\n"
"v_sub_f32 v53,v53,v119\n"
"v_sub_f32 v54,v54,v119\n"
"v_sub_f32 v55,v55,v119\n"
"v_sub_f32 v104,v104,v119\n"
"v_sub_f32 v105,v105,v119\n"
"v_sub_f32 v106,v106,v119\n"
"v_sub_f32 v107,v107,v119\n"
"s_cbranch_scc1 .Lfk_rare1_2\n"
"v_exp_f32 v119,-v119\n"
"s_nop 0\n"
"v_mul_f32 v192,v192,v119\n"
"v_mul_f32 v193,v193,v119\n"
"v_mul_f32 v194,v194,v119\n"
"v_mul_f32 v195,v195,v119\n"
"v_mul_f32 v196,v196,v119\n"
"v_mul_f32 v197,v197,v119\n"
"v_mul_f32 v198,v198,v119\n"
"v_mul_f32 v199,v199,v119\n"
"v_mul_f32 v200,v200,v119\n"
"v_mul_f32 v201,v201,v119\n"
"v_mul_f32 v202,v202,v119\n"
"v_mul_f32 v203,v203,v119\n"
"v_mul_f32 v204,v204,v119\n"
"v_mul_f32 v205,v205,v119\n"
"v_mul_f32 v206,v206,v119\n"
"v_mul_f32 v207,v207,v119\n"
"v_mul_f32 v208,v208,v119\n"
"v_mul_f32 v209,v209,v119\n"
"v_mul_f32 v210,v210,v119\n"
"v_mul_f32 v211,v211,v119\n"
"v_mul_f32 v212,v212,v119\n"
"v_mul_f32 v213,v213,v119\n"
"v_mul_f32 v214,v214,v119\n"
"v_mul_f32 v215,v215,v119\n"
"v_mul_f32 v216,v216,v119\n"
"v_mul_f32 v217,v217,v119\n"
"v_mul_f32 v218,v218,v119\n"
"v_mul_f32 v219,v219,v119\n"
"v_mul_f32 v220,v220,v119\n"
"v_mul_f32 v221,v221,v119\n"
"v_mul_f32 v222,v222,v119\n"
"v_mul_f32 v223,v223,v119\n"
"v_accvgpr_read_b32 v120,a136\n"
"v_mul_f32 v120,v120,v119\n"
"v_accvgpr_write_b32 a136,v120\n"
"v_lshlrev_b32 v120,16,v72\n"
"v_and_b32 v121,0xffff0000,v72\n"
"v_mul_f32 v120,v120,v119\n"
"v_mul_f32 v121,v121,v119\n"
"v_cvt_pk_bf16_f32 v72,v120,v121\n"
"v_accvgpr_read_b32 v120,a137\n"
"v_mul_f32 v120,v120,v119\n"
"v_accvgpr_write_b32 a137,v120\n"
"v_lshlrev_b32 v120,16,v73\n"
"v_and_b32 v121,0xffff0000,v73\n"
"v_mul_f32 v120,v120,v119\n"
"v_mul_f32 v121,v121,v119\n"
"v_cvt_pk_bf16_f32 v73,v120,v121\n"
"v_accvgpr_read_b32 v120,a138\n"
"v_mul_f32 v120,v120,v119\n"
"v_accvgpr_write_b32 a138,v120\n"
"v_lshlrev_b32 v120,16,v74\n"
"v_and_b32 v121,0xffff0000,v74\n"
"v_mul_f32 v120,v120,v119\n"
"v_mul_f32 v121,v121,v119\n"
"v_cvt_pk_bf16_f32 v74,v120,v121\n"
"v_accvgpr_read_b32 v120,a139\n"
"v_mul_f32 v120,v120,v119\n"
"v_accvgpr_write_b32 a139,v120\n"
"v_lshlrev_b32 v120,16,v75\n"
"v_and_b32 v121,0xffff0000,v75\n"
"v_mul_f32 v120,v120,v119\n"
"v_mul_f32 v121,v121,v119\n"
"v_cvt_pk_bf16_f32 v75,v120,v121\n"
".Lfk_rare1_2:\n"
"s_bitcmp1_b32 s91,3\n"
"s_cbranch_scc0 .Lfk_rare1_3\n"
"v_max3_f32 v119,v56,v57,v58\n"
"v_max3_f32 v120,v59,v60,v61\n"
"v_max3_f32 v119,v119,v62,v63\n"
"v_max_f32 v119,v119,v120\n"
"v_mov_b32 v120,v119\n"
"s_nop 1\n"
"v_permlane16_swap_b32 v120,v119\n"
"v_max_f32 v119,v119,v120\n"
"v_mov_b32 v120,v119\n"
"s_nop 1\n"
"v_permlane32_swap_b32 v120,v119\n"
"v_max_f32 v119,v119,v120\n"
"s_bitcmp1_b32 s90,3\n"
"s_cselect_b32 s82,0xff800000,0\n"
"v_add_f32 v119,0x41980000,v119\n"
"v_max_f32 v119,s82,v119\n"
"v_cmp_lt_f32 vcc,s17,|v119|\n"
"s_nop 0\n"
"s_cbranch_vccz .Lfk_rare1_3\n"
"v_ceil_f32 v119,v119\n"
"v_sub_f32 v56,v56,v119\n"
"v_sub_f32 v57,v57,v119\n"
"v_sub_f32 v58,v58,v119\n"
"v_sub_f32 v59,v59,v119\n"
"v_sub_f32 v60,v60,v119\n"
"v_sub_f32 v61,v61,v119\n"
"v_sub_f32 v62,v62,v119\n"
"v_sub_f32 v63,v63,v119\n"
"v_sub_f32 v108,v108,v119\n"
"v_sub_f32 v109,v109,v119\n"
"v_sub_f32 v110,v110,v119\n"
"v_sub_f32 v111,v111,v119\n"
"s_cbranch_scc1 .Lfk_rare1_3\n"
"v_exp_f32 v119,-v119\n"
"s_nop 0\n"
"v_mul_f32 v224,v224,v119\n"
"v_mul_f32 v225,v225,v119\n"
"v_mul_f32 v226,v226,v119\n"
"v_mul_f32 v227,v227,v119\n"
"v_mul_f32 v228,v228,v119\n"
"v_mul_f32 v229,v229,v119\n"
"v_mul_f32 v230,v230,v119\n"
"v_mul_f32 v231,v231,v119\n"
"v_mul_f32 v232,v232,v119\n"
"v_mul_f32 v233,v233,v119\n"
"v_mul_f32 v234,v234,v119\n"
"v_mul_f32 v235,v235,v119\n"
"v_mul_f32 v236,v236,v119\n"
"v_mul_f32 v237,v237,v119\n"
"v_mul_f32 v238,v238,v119\n"
"v_mul_f32 v239,v239,v119\n"
"v_mul_f32 v240,v240,v119\n"
"v_mul_f32 v241,v241,v119\n"
"v_mul_f32 v242,v242,v119\n"
"v_mul_f32 v243,v243,v119\n"
"v_mul_f32 v244,v244,v119\n"
"v_mul_f32 v245,v245,v119\n"
"v_mul_f32 v246,v246,v119\n"
"v_mul_f32 v247,v247,v119\n"
"v_mul_f32 v248,v248,v119\n"
"v_mul_f32 v249,v249,v119\n"
"v_mul_f32 v250,v250,v119\n"
"v_mul_f32 v251,v251,v119\n"
"v_mul_f32 v252,v252,v119\n"
"v_mul_f32 v253,v253,v119\n"
"v_mul_f32 v254,v254,v119\n"
"v_mul_f32 v255,v255,v119\n"
"v_accvgpr_read_b32 v120,a140\n"
"v_mul_f32 v120,v120,v119\n"
"v_accvgpr_write_b32 a140,v120\n"
"v_lshlrev_b32 v120,16,v76\n"
"v_and_b32 v121,0xffff0000,v76\n"
"v_mul_f32 v120,v120,v119\n"
"v_mul_f32 v121,v121,v119\n"
"v_cvt_pk_bf16_f32 v76,v120,v121\n"
"v_accvgpr_read_b32 v120,a141\n"
"v_mul_f32 v120,v120,v119\n"
"v_accvgpr_write_b32 a141,v120\n"
"v_lshlrev_b32 v120,16,v77\n"
"v_and_b32 v121,0xffff0000,v77\n"
"v_mul_f32 v120,v120,v119\n"
"v_mul_f32 v121,v121,v119\n"
"v_cvt_pk_bf16_f32 v77,v120,v121\n"
"v_accvgpr_read_b32 v120,a142\n"
"v_mul_f32 v120,v120,v119\n"
"v_accvgpr_write_b32 a142,v120\n"
"v_lshlrev_b32 v120,16,v78\n"
"v_and_b32 v121,0xffff0000,v78\n"
"v_mul_f32 v120,v120,v119\n"
"v_mul_f32 v121,v121,v119\n"
"v_cvt_pk_bf16_f32 v78,v120,v121\n"
"v_accvgpr_read_b32 v120,a143\n"
"v_mul_f32 v120,v120,v119\n"
"v_accvgpr_write_b32 a143,v120\n"
"v_lshlrev_b32 v120,16,v79\n"
"v_and_b32 v121,0xffff0000,v79\n"
"v_mul_f32 v120,v120,v119\n"
"v_mul_f32 v121,v121,v119\n"
"v_cvt_pk_bf16_f32 v79,v120,v121\n"
".Lfk_rare1_3:\n"
"s_mov_b32 s90,0\n"
"s_nop 1\n"
"s_setpc_b64 s[94:95]\n"
".Lfk_rare3:\n"
"s_nop 7\n"
"v_max3_f32 v119,v32,v33,v34\n"
"v_max3_f32 v120,v35,v36,v37\n"
"v_max3_f32 v119,v119,v38,v39\n"
"v_max_f32 v119,v119,v120\n"
"v_mov_b32 v120,v119\n"
"s_nop 1\n"
"v_permlane16_swap_b32 v120,v119\n"
"v_max_f32 v119,v119,v120\n"
"v_mov_b32 v120,v119\n"
"s_nop 1\n"
"v_permlane32_swap_b32 v120,v119\n"
"v_max_f32 v119,v119,v120\n"
"s_bitcmp1_b32 s90,0\n"
"s_cselect_b32 s82,0xff800000,0\n"
"v_add_f32 v119,0x41980000,v119\n"
"v_max_f32 v119,s82,v119\n"
"v_cmp_lt_f32 vcc,s17,|v119|\n"
"s_nop 0\n"
"s_cbranch_vccz .Lfk_rare3_0\n"
"v_ceil_f32 v119,v119\n"
"v_sub_f32 v32,v32,v119\n"
"v_sub_f32 v33,v33,v119\n"
"v_sub_f32 v34,v34,v119\n"
"v_sub_f32 v35,v35,v119\n"
"v_sub_f32 v36,v36,v119\n"
"v_sub_f32 v37,v37,v119\n"
"v_sub_f32 v38,v38,v119\n"
"v_sub_f32 v39,v39,v119\n"
"v_sub_f32 v96,v96,v119\n"
"v_sub_f32 v97,v97,v119\n"
"v_sub_f32 v98,v98,v119\n"
"v_sub_f32 v99,v99,v119\n"
"v_sub_f32 v0,v0,v119\n"
"v_sub_f32 v1,v1,v119\n"
"v_sub_f32 v2,v2,v119\n"
"v_sub_f32 v3,v3,v119\n"
"v_sub_f32 v4,v4,v119\n"
"v_sub_f32 v5,v5,v119\n"
"v_sub_f32 v6,v6,v119\n"
"v_sub_f32 v7,v7,v119\n"
"v_exp_f32 v32,v32\n"
"v_exp_f32 v33,v33\n"
"v_exp_f32 v34,v34\n"
"v_exp_f32 v35,v35\n"
"v_exp_f32 v36,v36\n"
"v_exp_f32 v37,v37\n"
"v_exp_f32 v38,v38\n"
"v_exp_f32 v39,v39\n"
"v_cvt_pk_bf16_f32 v80,v32,v33\n"
"v_cvt_pk_bf16_f32 v81,v34,v35\n"
"v_cvt_pk_bf16_f32 v82,v36,v37\n"
"v_cvt_pk_bf16_f32 v83,v38,v39\n"
"s_cbranch_scc1 .Lfk_rare3_0\n"
"v_exp_f32 v119,-v119\n"
"s_nop 0\n"
"v_mul_f32 v128,v128,v119\n"
"v_mul_f32 v129,v129,v119\n"
"v_mul_f32 v130,v130,v119\n"
"v_mul_f32 v131,v131,v119\n"
"v_mul_f32 v132,v132,v119\n"
"v_mul_f32 v133,v133,v119\n"
"v_mul_f32 v134,v134,v119\n"
"v_mul_f32 v135,v135,v119\n"
"v_mul_f32 v136,v136,v119\n"
"v_mul_f32 v137,v137,v119\n"
"v_mul_f32 v138,v138,v119\n"
"v_mul_f32 v139,v139,v119\n"
"v_mul_f32 v140,v140,v119\n"
"v_mul_f32 v141,v141,v119\n"
"v_mul_f32 v142,v142,v119\n"
"v_mul_f32 v143,v143,v119\n"
"v_mul_f32 v144,v144,v119\n"
"v_mul_f32 v145,v145,v119\n"
"v_mul_f32 v146,v146,v119\n"
"v_mul_f32 v147,v147,v119\n"
"v_mul_f32 v148,v148,v119\n"
"v_mul_f32 v149,v149,v119\n"
"v_mul_f32 v150,v150,v119\n"
"v_mul_f32 v151,v151,v119\n"
"v_mul_f32 v152,v152,v119\n"
"v_mul_f32 v153,v153,v119\n"
"v_mul_f32 v154,v154,v119\n"
"v_mul_f32 v155,v155,v119\n"
"v_mul_f32 v156,v156,v119\n"
"v_mul_f32 v157,v157,v119\n"
"v_mul_f32 v158,v158,v119\n"
"v_mul_f32 v159,v159,v119\n"
"v_accvgpr_read_b32 v120,a128\n"
"v_mul_f32 v120,v120,v119\n"
"v_accvgpr_write_b32 a128,v120\n"
"v_accvgpr_read_b32 v120,a129\n"
"v_mul_f32 v120,v120,v119\n"
"v_accvgpr_write_b32 a129,v120\n"
"v_accvgpr_read_b32 v120,a130\n"
"v_mul_f32 v120,v120,v119\n"
"v_accvgpr_write_b32 a130,v120\n"
"v_accvgpr_read_b32 v120,a131\n"
"v_mul_f32 v120,v120,v119\n"
"v_accvgpr_write_b32 a131,v120\n"
".Lfk_rare3_0:\n"
"v_max3_f32 v119,v40,v41,v42\n"
"v_max3_f32 v120,v43,v44,v45\n"
"v_max3_f32 v119,v119,v46,v47\n"
"v_max_f32 v119,v119,v120\n"
"v_mov_b32 v120,v119\n"
"s_nop 1\n"
"v_permlane16_swap_b32 v120,v119\n"
"v_max_f32 v119,v119,v120\n"
"v_mov_b32 v120,v119\n"
"s_nop 1\n"
"v_permlane32_swap_b32 v120,v119\n"
"v_max_f32 v119,v119,v120\n"
"s_bitcmp1_b32 s90,1\n"
"s_cselect_b32 s82,0xff800000,0\n"
"v_add_f32 v119,0x41980000,v119\n"
"v_max_f32 v119,s82,v119\n"
"v_cmp_lt_f32 vcc,s17,|v119|\n"
"s_nop 0\n"
"s_cbranch_vccz .Lfk_rare3_1\n"
"v_ceil_f32 v119,v119\n"
"v_sub_f32 v40,v40,v119\n"
"v_sub_f32 v41,v41,v119\n"
"v_sub_f32 v42,v42,v119\n"
"v_sub_f32 v43,v43,v119\n"
"v_sub_f32 v44,v44,v119\n"
"v_sub_f32 v45,v45,v119\n"
"v_sub_f32 v46,v46,v119\n"
"v_sub_f32 v47,v47,v119\n"
"v_sub_f32 v100,v100,v119\n"
"v_sub_f32 v101,v101,v119\n"
"v_sub_f32 v102,v102,v119\n"
"v_sub_f32 v103,v103,v119\n"
"v_sub_f32 v8,v8,v119\n"
"v_sub_f32 v9,v9,v119\n"
"v_sub_f32 v10,v10,v119\n"
"v_sub_f32 v11,v11,v119\n"
"v_sub_f32 v12,v12,v119\n"
"v_sub_f32 v13,v13,v119\n"
"v_sub_f32 v14,v14,v119\n"
"v_sub_f32 v15,v15,v119\n"
"v_exp_f32 v40,v40\n"
"v_exp_f32 v41,v41\n"
"v_exp_f32 v42,v42\n"
"v_exp_f32 v43,v43\n"
"v_exp_f32 v44,v44\n"
"v_exp_f32 v45,v45\n"
"v_exp_f32 v46,v46\n"
"v_exp_f32 v47,v47\n"
"v_cvt_pk_bf16_f32 v84,v40,v41\n"
"v_cvt_pk_bf16_f32 v85,v42,v43\n"
"v_cvt_pk_bf16_f32 v86,v44,v45\n"
"v_cvt_pk_bf16_f32 v87,v46,v47\n"
"s_cbranch_scc1 .Lfk_rare3_1\n"
"v_exp_f32 v119,-v119\n"
"s_nop 0\n"
"v_mul_f32 v160,v160,v119\n"
"v_mul_f32 v161,v161,v119\n"
"v_mul_f32 v162,v162,v119\n"
"v_mul_f32 v163,v163,v119\n"
"v_mul_f32 v164,v164,v119\n"
"v_mul_f32 v165,v165,v119\n"
"v_mul_f32 v166,v166,v119\n"
"v_mul_f32 v167,v167,v119\n"
"v_mul_f32 v168,v168,v119\n"
"v_mul_f32 v169,v169,v119\n"
"v_mul_f32 v170,v170,v119\n"
"v_mul_f32 v171,v171,v119\n"
"v_mul_f32 v172,v172,v119\n"
"v_mul_f32 v173,v173,v119\n"
"v_mul_f32 v174,v174,v119\n"
"v_mul_f32 v175,v175,v119\n"
"v_mul_f32 v176,v176,v119\n"
"v_mul_f32 v177,v177,v119\n"
"v_mul_f32 v178,v178,v119\n"
"v_mul_f32 v179,v179,v119\n"
"v_mul_f32 v180,v180,v119\n"
"v_mul_f32 v181,v181,v119\n"
"v_mul_f32 v182,v182,v119\n"
"v_mul_f32 v183,v183,v119\n"
"v_mul_f32 v184,v184,v119\n"
"v_mul_f32 v185,v185,v119\n"
"v_mul_f32 v186,v186,v119\n"
"v_mul_f32 v187,v187,v119\n"
"v_mul_f32 v188,v188,v119\n"
"v_mul_f32 v189,v189,v119\n"
"v_mul_f32 v190,v190,v119\n"
"v_mul_f32 v191,v191,v119\n"
"v_accvgpr_read_b32 v120,a132\n"
"v_mul_f32 v120,v120,v119\n"
"v_accvgpr_write_b32 a132,v120\n"
"v_accvgpr_read_b32 v120,a133\n"
"v_mul_f32 v120,v120,v119\n"
"v_accvgpr_write_b32 a133,v120\n"
"v_accvgpr_read_b32 v120,a134\n"
"v_mul_f32 v120,v120,v119\n"
"v_accvgpr_write_b32 a134,v120\n"
"v_accvgpr_read_b32 v120,a135\n"
"v_mul_f32 v120,v120,v119\n"
"v_accvgpr_write_b32 a135,v120\n"
".Lfk_rare3_1:\n"
"v_max3_f32 v119,v48,v49,v50\n"
"v_max3_f32 v120,v51,v52,v53\n"
"v_max3_f32 v119,v119,v54,v55\n"
"v_max_f32 v119,v119,v120\n"
"v_mov_b32 v120,v119\n"
"s_nop 1\n"
"v_permlane16_swap_b32 v120,v119\n"
"v_max_f32 v119,v119,v120\n"
"v_mov_b32 v120,v119\n"
"s_nop 1\n"
"v_permlane32_swap_b32 v120,v119\n"
"v_max_f32 v119,v119,v120\n"
"s_bitcmp1_b32 s90,2\n"
"s_cselect_b32 s82,0xff800000,0\n"
"v_add_f32 v119,0x41980000,v119\n"
"v_max_f32 v119,s82,v119\n"
"v_cmp_lt_f32 vcc,s17,|v119|\n"
"s_nop 0\n"
"s_cbranch_vccz .Lfk_rare3_2\n"
"v_ceil_f32 v119,v119\n"
"v_sub_f32 v48,v48,v119\n"
"v_sub_f32 v49,v49,v119\n"
"v_sub_f32 v50,v50,v119\n"
"v_sub_f32 v51,v51,v119\n"
"v_sub_f32 v52,v52,v119\n"
"v_sub_f32 v53,v53,v119\n"
"v_sub_f32 v54,v54,v119\n"
"v_sub_f32 v55,v55,v119\n"
"v_sub_f32 v104,v104,v119\n"
"v_sub_f32 v105,v105,v119\n"
"v_sub_f32 v106,v106,v119\n"
"v_sub_f32 v107,v107,v119\n"
"v_sub_f32 v16,v16,v119\n"
"v_sub_f32 v17,v17,v119\n"
"v_sub_f32 v18,v18,v119\n"
"v_sub_f32 v19,v19,v119\n"
"v_sub_f32 v20,v20,v119\n"
"v_sub_f32 v21,v21,v119\n"
"v_sub_f32 v22,v22,v119\n"
"v_sub_f32 v23,v23,v119\n"
"v_exp_f32 v48,v48\n"
"v_exp_f32 v49,v49\n"
"v_exp_f32 v50,v50\n"
"v_exp_f32 v51,v51\n"
"v_exp_f32 v52,v52\n"
"v_exp_f32 v53,v53\n"
"v_exp_f32 v54,v54\n"
"v_exp_f32 v55,v55\n"
"v_cvt_pk_bf16_f32 v88,v48,v49\n"
"v_cvt_pk_bf16_f32 v89,v50,v51\n"
"v_cvt_pk_bf16_f32 v90,v52,v53\n"
"v_cvt_pk_bf16_f32 v91,v54,v55\n"
"s_cbranch_scc1 .Lfk_rare3_2\n"
"v_exp_f32 v119,-v119\n"
"s_nop 0\n"
"v_mul_f32 v192,v192,v119\n"
"v_mul_f32 v193,v193,v119\n"
"v_mul_f32 v194,v194,v119\n"
"v_mul_f32 v195,v195,v119\n"
"v_mul_f32 v196,v196,v119\n"
"v_mul_f32 v197,v197,v119\n"
"v_mul_f32 v198,v198,v119\n"
"v_mul_f32 v199,v199,v119\n"
"v_mul_f32 v200,v200,v119\n"
"v_mul_f32 v201,v201,v119\n"
"v_mul_f32 v202,v202,v119\n"
"v_mul_f32 v203,v203,v119\n"
"v_mul_f32 v204,v204,v119\n"
"v_mul_f32 v205,v205,v119\n"
"v_mul_f32 v206,v206,v119\n"
"v_mul_f32 v207,v207,v119\n"
"v_mul_f32 v208,v208,v119\n"
"v_mul_f32 v209,v209,v119\n"
"v_mul_f32 v210,v210,v119\n"
"v_mul_f32 v211,v211,v119\n"
"v_mul_f32 v212,v212,v119\n"
"v_mul_f32 v213,v213,v119\n"
"v_mul_f32 v214,v214,v119\n"
"v_mul_f32 v215,v215,v119\n"
"v_mul_f32 v216,v216,v119\n"
"v_mul_f32 v217,v217,v119\n"
"v_mul_f32 v218,v218,v119\n"
"v_mul_f32 v219,v219,v119\n"
"v_mul_f32 v220,v220,v119\n"
"v_mul_f32 v221,v221,v119\n"
"v_mul_f32 v222,v222,v119\n"
"v_mul_f32 v223,v223,v119\n"
"v_accvgpr_read_b32 v120,a136\n"
"v_mul_f32 v120,v120,v119\n"
"v_accvgpr_write_b32 a136,v120\n"
"v_accvgpr_read_b32 v120,a137\n"
"v_mul_f32 v120,v120,v119\n"
"v_accvgpr_write_b32 a137,v120\n"
"v_accvgpr_read_b32 v120,a138\n"
"v_mul_f32 v120,v120,v119\n"
"v_accvgpr_write_b32 a138,v120\n"
"v_accvgpr_read_b32 v120,a139\n"
"v_mul_f32 v120,v120,v119\n"
"v_accvgpr_write_b32 a139,v120\n"
".Lfk_rare3_2:\n"
"v_max3_f32 v119,v56,v57,v58\n"
"v_max3_f32 v120,v59,v60,v61\n"
"v_max3_f32 v119,v119,v62,v63\n"
"v_max_f32 v119,v119,v120\n"
"v_mov_b32 v120,v119\n"
"s_nop 1\n"
"v_permlane16_swap_b32 v120,v119\n"
"v_max_f32 v119,v119,v120\n"
"v_mov_b32 v120,v119\n"
"s_nop 1\n"
"v_permlane32_swap_b32 v120,v119\n"
"v_max_f32 v119,v119,v120\n"
"s_bitcmp1_b32 s90,3\n"
"s_cselect_b32 s82,0xff800000,0\n"
"v_add_f32 v119,0x41980000,v119\n"
"v_max_f32 v119,s82,v119\n"
"v_cmp_lt_f32 vcc,s17,|v119|\n"
"s_nop 0\n"
"s_cbranch_vccz .Lfk_rare3_3\n"
"v_ceil_f32 v119,v119\n"
"v_sub_f32 v56,v56,v119\n"
"v_sub_f32 v57,v57,v119\n"
"v_sub_f32 v58,v58,v119\n"
"v_sub_f32 v59,v59,v119\n"
"v_sub_f32 v60,v60,v119\n"
"v_sub_f32 v61,v61,v119\n"
"v_sub_f32 v62,v62,v119\n"
"v_sub_f32 v63,v63,v119\n"
"v_sub_f32 v108,v108,v119\n"
"v_sub_f32 v109,v109,v119\n"
"v_sub_f32 v110,v110,v119\n"
"v_sub_f32 v111,v111,v119\n"
"v_sub_f32 v24,v24,v119\n"
"v_sub_f32 v25,v25,v119\n"
"v_sub_f32 v26,v26,v119\n"
"v_sub_f32 v27,v27,v119\n"
"v_sub_f32 v28,v28,v119\n"
"v_sub_f32 v29,v29,v119\n"
"v_sub_f32 v30,v30,v119\n"
"v_sub_f32 v31,v31,v119\n"
"v_exp_f32 v56,v56\n"
"v_exp_f32 v57,v57\n"
"v_exp_f32 v58,v58\n"
"v_exp_f32 v59,v59\n"
"v_exp_f32 v60,v60\n"
"v_exp_f32 v61,v61\n"
"v_exp_f32 v62,v62\n"
"v_exp_f32 v63,v63\n"
"v_cvt_pk_bf16_f32 v92,v56,v57\n"
"v_cvt_pk_bf16_f32 v93,v58,v59\n"
"v_cvt_pk_bf16_f32 v94,v60,v61\n"
"v_cvt_pk_bf16_f32 v95,v62,v63\n"
"s_cbranch_scc1 .Lfk_rare3_3\n"
"v_exp_f32 v119,-v119\n"
"s_nop 0\n"
"v_mul_f32 v224,v224,v119\n"
"v_mul_f32 v225,v225,v119\n"
"v_mul_f32 v226,v226,v119\n"
"v_mul_f32 v227,v227,v119\n"
"v_mul_f32 v228,v228,v119\n"
"v_mul_f32 v229,v229,v119\n"
"v_mul_f32 v230,v230,v119\n"
"v_mul_f32 v231,v231,v119\n"
"v_mul_f32 v232,v232,v119\n"
"v_mul_f32 v233,v233,v119\n"
"v_mul_f32 v234,v234,v119\n"
"v_mul_f32 v235,v235,v119\n"
"v_mul_f32 v236,v236,v119\n"
"v_mul_f32 v237,v237,v119\n"
"v_mul_f32 v238,v238,v119\n"
"v_mul_f32 v239,v239,v119\n"
"v_mul_f32 v240,v240,v119\n"
"v_mul_f32 v241,v241,v119\n"
"v_mul_f32 v242,v242,v119\n"
"v_mul_f32 v243,v243,v119\n"
"v_mul_f32 v244,v244,v119\n"
"v_mul_f32 v245,v245,v119\n"
"v_mul_f32 v246,v246,v119\n"
"v_mul_f32 v247,v247,v119\n"
"v_mul_f32 v248,v248,v119\n"
"v_mul_f32 v249,v249,v119\n"
"v_mul_f32 v250,v250,v119\n"
"v_mul_f32 v251,v251,v119\n"
"v_mul_f32 v252,v252,v119\n"
"v_mul_f32 v253,v253,v119\n"
"v_mul_f32 v254,v254,v119\n"
"v_mul_f32 v255,v255,v119\n"
"v_accvgpr_read_b32 v120,a140\n"
"v_mul_f32 v120,v120,v119\n"
"v_accvgpr_write_b32 a140,v120\n"
"v_accvgpr_read_b32 v120,a141\n"
"v_mul_f32 v120,v120,v119\n"
"v_accvgpr_write_b32 a141,v120\n"
"v_accvgpr_read_b32 v120,a142\n"
"v_mul_f32 v120,v120,v119\n"
"v_accvgpr_write_b32 a142,v120\n"
"v_accvgpr_read_b32 v120,a143\n"
"v_mul_f32 v120,v120,v119\n"
"v_accvgpr_write_b32 a143,v120\n"
".Lfk_rare3_3:\n"
"s_mov_b32 s90,0\n"
"s_nop 1\n"
"s_setpc_b64 s[94:95]\n"
".Lfk_mslow1:\n"
"s_nop 7\n"
"v_min_f32 v119,v102,v108\n"
"v_sub_f32 v120,v119,v108\n"
"v_sub_f32 v119,v119,v102\n"
"v_exp_f32 v120,v120\n"
"v_exp_f32 v119,v119\n"
"v_mul_f32 v224,v224,v120\n"
"v_mul_f32 v225,v225,v120\n"
"v_mul_f32 v226,v226,v120\n"
"v_mul_f32 v227,v227,v120\n"
"v_mul_f32 v228,v228,v120\n"
"v_mul_f32 v229,v229,v120\n"
"v_mul_f32 v230,v230,v120\n"
"v_mul_f32 v231,v231,v120\n"
"v_mul_f32 v232,v232,v120\n"
"v_mul_f32 v233,v233,v120\n"
"v_mul_f32 v234,v234,v120\n"
"v_mul_f32 v235,v235,v120\n"
"v_mul_f32 v236,v236,v120\n"
"v_mul_f32 v237,v237,v120\n"
"v_mul_f32 v238,v238,v120\n"
"v_mul_f32 v239,v239,v120\n"
"v_mul_f32 v240,v240,v120\n"
"v_mul_f32 v241,v241,v120\n"
"v_mul_f32 v242,v242,v120\n"
"v_mul_f32 v243,v243,v120\n"
"v_mul_f32 v244,v244,v120\n"
"v_mul_f32 v245,v245,v120\n"
"v_mul_f32 v246,v246,v120\n"
"v_mul_f32 v247,v247,v120\n"
"v_mul_f32 v248,v248,v120\n"
"v_mul_f32 v249,v249,v120\n"
"v_mul_f32 v250,v250,v120\n"
"v_mul_f32 v251,v251,v120\n"
"v_mul_f32 v252,v252,v120\n"
"v_mul_f32 v253,v253,v120\n"
"v_mul_f32 v254,v254,v120\n"
"v_mul_f32 v255,v255,v120\n"
"v_accvgpr_read_b32 v121,a140\n"
"v_mul_f32 v121,v121,v120\n"
"v_accvgpr_write_b32 a140,v121\n"
"v_mul_f32 v103,v103,v119\n"
"v_accvgpr_read_b32 v121,a80\n"
"v_lshlrev_b32 v120,16,v121\n"
"v_and_b32 v121,0xffff0000,v121\n"
"v_mul_f32 v120,v120,v119\n"
"v_mul_f32 v121,v121,v119\n"
"v_cvt_pk_bf16_f32 v121,v120,v121\n"
"v_accvgpr_write_b32 a80,v121\n"
"v_accvgpr_read_b32 v121,a81\n"
"v_lshlrev_b32 v120,16,v121\n"
"v_and_b32 v121,0xffff0000,v121\n"
"v_mul_f32 v120,v120,v119\n"
"v_mul_f32 v121,v121,v119\n"
"v_cvt_pk_bf16_f32 v121,v120,v121\n"
"v_accvgpr_write_b32 a81,v121\n"
"v_accvgpr_read_b32 v121,a82\n"
"v_lshlrev_b32 v120,16,v121\n"
"v_and_b32 v121,0xffff0000,v121\n"
"v_mul_f32 v120,v120,v119\n"
"v_mul_f32 v121,v121,v119\n"
"v_cvt_pk_bf16_f32 v121,v120,v121\n"
"v_accvgpr_write_b32 a82,v121\n"
"v_accvgpr_read_b32 v121,a83\n"
"v_lshlrev_b32 v120,16,v121\n"
"v_and_b32 v121,0xffff0000,v121\n"
"v_mul_f32 v120,v120,v119\n"
"v_mul_f32 v121,v121,v119\n"
"v_cvt_pk_bf16_f32 v121,v120,v121\n"
"v_accvgpr_write_b32 a83,v121\n"
"v_accvgpr_read_b32 v121,a84\n"
"v_lshlrev_b32 v120,16,v121\n"
"v_and_b32 v121,0xffff0000,v121\n"
"v_mul_f32 v120,v120,v119\n"
"v_mul_f32 v121,v121,v119\n"
"v_cvt_pk_bf16_f32 v121,v120,v121\n"
"v_accvgpr_write_b32 a84,v121\n"
"v_accvgpr_read_b32 v121,a85\n"
"v_lshlrev_b32 v120,16,v121\n"
"v_and_b32 v121,0xffff0000,v121\n"
"v_mul_f32 v120,v120,v119\n"
"v_mul_f32 v121,v121,v119\n"
"v_cvt_pk_bf16_f32 v121,v120,v121\n"
"v_accvgpr_write_b32 a85,v121\n"
"v_accvgpr_read_b32 v121,a86\n"
"v_lshlrev_b32 v120,16,v121\n"
"v_and_b32 v121,0xffff0000,v121\n"
"v_mul_f32 v120,v120,v119\n"
"v_mul_f32 v121,v121,v119\n"
"v_cvt_pk_bf16_f32 v121,v120,v121\n"
"v_accvgpr_write_b32 a86,v121\n"
"v_accvgpr_read_b32 v121,a87\n"
"v_lshlrev_b32 v120,16,v121\n"
"v_and_b32 v121,0xffff0000,v121\n"
"v_mul_f32 v120,v120,v119\n"
"v_mul_f32 v121,v121,v119\n"
"v_cvt_pk_bf16_f32 v121,v120,v121\n"
"v_accvgpr_write_b32 a87,v121\n"
"v_accvgpr_read_b32 v121,a88\n"
"v_lshlrev_b32 v120,16,v121\n"
"v_and_b32 v121,0xffff0000,v121\n"
"v_mul_f32 v120,v120,v119\n"
"v_mul_f32 v121,v121,v119\n"
"v_cvt_pk_bf16_f32 v121,v120,v121\n"
"v_accvgpr_write_b32 a88,v121\n"
"v_accvgpr_read_b32 v121,a89\n"
"v_lshlrev_b32 v120,16,v121\n"
"v_and_b32 v121,0xffff0000,v121\n"
"v_mul_f32 v120,v120,v119\n"
"v_mul_f32 v121,v121,v119\n"
"v_cvt_pk_bf16_f32 v121,v120,v121\n"
"v_accvgpr_write_b32 a89,v121\n"
"v_accvgpr_read_b32 v121,a90\n"
"v_lshlrev_b32 v120,16,v121\n"
"v_and_b32 v121,0xffff0000,v121\n"
"v_mul_f32 v120,v120,v119\n"
"v_mul_f32 v121,v121,v119\n"
"v_cvt_pk_bf16_f32 v121,v120,v121\n"
"v_accvgpr_write_b32 a90,v121\n"
"v_accvgpr_read_b32 v121,a91\n"
"v_lshlrev_b32 v120,16,v121\n"
"v_and_b32 v121,0xffff0000,v121\n"
"v_mul_f32 v120,v120,v119\n"
"v_mul_f32 v121,v121,v119\n"
"v_cvt_pk_bf16_f32 v121,v120,v121\n"
"v_accvgpr_write_b32 a91,v121\n"
"v_accvgpr_read_b32 v121,a92\n"
"v_lshlrev_b32 v120,16,v121\n"
"v_and_b32 v121,0xffff0000,v121\n"
"v_mul_f32 v120,v120,v119\n"
"v_mul_f32 v121,v121,v119\n"
"v_cvt_pk_bf16_f32 v121,v120,v121\n"
"v_accvgpr_write_b32 a92,v121\n"
"v_accvgpr_read_b32 v121,a93\n"
"v_lshlrev_b32 v120,16,v121\n"
"v_and_b32 v121,0xffff0000,v121\n"
"v_mul_f32 v120,v120,v119\n"
"v_mul_f32 v121,v121,v119\n"
"v_cvt_pk_bf16_f32 v121,v120,v121\n"
"v_accvgpr_write_b32 a93,v121\n"
"v_accvgpr_read_b32 v121,a94\n"
"v_lshlrev_b32 v120,16,v121\n"
"v_and_b32 v121,0xffff0000,v121\n"
"v_mul_f32 v120,v120,v119\n"
"v_mul_f32 v121,v121,v119\n"
"v_cvt_pk_bf16_f32 v121,v120,v121\n"
"v_accvgpr_write_b32 a94,v121\n"
"v_accvgpr_read_b32 v121,a95\n"
"v_lshlrev_b32 v120,16,v121\n"
"v_and_b32 v121,0xffff0000,v121\n"
"v_mul_f32 v120,v120,v119\n"
"v_mul_f32 v121,v121,v119\n"
"v_cvt_pk_bf16_f32 v121,v120,v121\n"
"v_accvgpr_write_b32 a95,v121\n"
"s_nop 1\n"
"s_setpc_b64 s[94:95]\n"
".Lfk_end:\n"
        : "+{v0}"(tid), "+{s2}"(bid)
        : "{s[0:1]}"(__builtin_amdgcn_kernarg_segment_ptr())
        : "v1","v2","v3","v4","v5","v6","v7","v8","v9","v10","v11","v12","v13","v14","v15","v16","v17","v18","v19","v20","v21","v22","v23","v24","v25","v26","v27","v28","v29","v30","v31","v32","v33","v34","v35","v36","v37","v38","v39","v40","v41","v42","v43","v44","v45","v46","v47","v48","v49","v50","v51","v52","v53","v54","v55","v56","v57","v58","v59","v60","v61","v62","v63","v64","v65","v66","v67","v68","v69","v70","v71","v72","v73","v74","v75","v76","v77","v78","v79","v80","v81","v82","v83","v84","v85","v86","v87","v88","v89","v90","v91","v92","v93","v94","v95","v96","v97","v98","v99","v100","v101","v102","v103","v104","v105","v106","v107","v108","v109","v110","v111","v112","v113","v114","v115","v116","v117","v118","v119","v120","v121","v122","v123","v124","v125","v126","v127","v128","v129","v130","v131","v132","v133","v134","v135","v136","v137","v138","v139","v140","v141","v142","v143","v144","v145","v146","v147","v148","v149","v150","v151","v152","v153","v154","v155","v156","v157","v158","v159","v160","v161","v162","v163","v164","v165","v166","v167","v168","v169","v170","v171","v172","v173","v174","v175","v176","v177","v178","v179","v180","v181","v182","v183","v184","v185","v186","v187","v188","v189","v190","v191","v192","v193","v194","v195","v196","v197","v198","v199","v200","v201","v202","v203","v204","v205","v206","v207","v208","v209","v210","v211","v212","v213","v214","v215","v216","v217","v218","v219","v220","v221","v222","v223","v224","v225","v226","v227","v228","v229","v230","v231","v232","v233","v234","v235","v236","v237","v238","v239","v240","v241","v242","v243","v244","v245","v246","v247","v248","v249","v250","v251","v252","v253","v254","v255",
          "a0","a1","a2","a3","a4","a5","a6","a7","a8","a9","a10","a11","a12","a13","a14","a15","a16","a17","a18","a19","a20","a21","a22","a23","a24","a25","a26","a27","a28","a29","a30","a31","a32","a33","a34","a35","a36","a37","a38","a39","a40","a41","a42","a43","a44","a45","a46","a47","a48","a49","a50","a51","a52","a53","a54","a55","a56","a57","a58","a59","a60","a61","a62","a63","a64","a65","a66","a67","a68","a69","a70","a71","a72","a73","a74","a75","a76","a77","a78","a79","a80","a81","a82","a83","a84","a85","a86","a87","a88","a89","a90","a91","a92","a93","a94","a95","a96","a97","a98","a99","a100","a101","a102","a103","a104","a105","a106","a107","a108","a109","a110","a111","a112","a113","a114","a115","a116","a117","a118","a119","a120","a121","a122","a123","a124","a125","a126","a127","a128","a129","a130","a131","a132","a133","a134","a135","a136","a137","a138","a139","a140","a141","a142","a143","a144","a145","a146","a147","a148","a149","a150","a151","a152","a153","a154","a155","a156","a157","a158","a159","a160","a161","a162","a163","a164","a165","a166","a167","a168","a169","a170","a171","a172","a173","a174","a175","a176","a177","a178","a179","a180","a181","a182","a183","a184","a185","a186","a187","a188","a189","a190","a191","a192","a193","a194","a195","a196","a197","a198","a199","a200","a201","a202","a203","a204","a205","a206","a207","a208","a209","a210","a211","a212","a213","a214","a215","a216","a217","a218","a219","a220","a221","a222","a223","a224","a225","a226","a227","a228","a229","a230","a231","a232","a233","a234","a235","a236","a237","a238","a239","a240","a241","a242","a243","a244","a245","a246","a247","a248","a249","a250","a251","a252","a253","a254","a255",
          "s3","s4","s5","s6","s7","s8","s9","s10","s11","s12","s13","s14","s15","s16","s17","s18","s19","s20","s21","s22","s23","s24","s25","s26","s27","s28","s29","s30","s31","s32","s33","s34","s35","s36","s37","s38","s39","s40","s41","s42","s43","s44","s45","s46","s47","s48","s49","s50","s51","s52","s53","s54","s55","s56","s57","s58","s59","s60","s61","s62","s63","s64","s65","s66","s67","s68","s69","s70","s71","s72","s73","s74","s75","s76","s77","s78","s79","s80","s81","s82","s83","s84","s85","s86","s87","s88","s89","s90","s91","s92","s93","s94","s95","s96","s97","s98","s99","s100","s101","vcc","scc","m0","memory");
#endif
}

extern "C" void kernel_launch(void* const* d_in, const int* in_sizes, int n_in, void* d_out, int out_size, void* d_ws, size_t ws_size,
                              hipStream_t stream) {
    (void)hipFuncSetAttribute((const void*)fused_kernel, hipFuncAttributeMaxDynamicSharedMemorySize, 163840);
    fused_kernel<<<256, 256, 163840, stream>>>((const char*)d_in[0], (const char*)d_in[1], (const char*)d_in[2], (const char*)d_in[3], (const char*)d_in[4], (char*)d_ws,
                                               (const char*)d_in[5], (const char*)d_in[8], (const char*)d_in[9], (const char*)d_in[10], (const char*)d_in[7], (char*)d_out);
}
```

```cpp
#include <hip/hip_runtime.h>

extern "C" __global__ __launch_bounds__(256, 1) void fused_kernel(const char* __restrict__ x, const char* __restrict__ ckv, const char* __restrict__ wq,
                                                       const char* __restrict__ wuk, const char* __restrict__ wuv, char* ws,
                                                       const char* __restrict__ pwo, const char* __restrict__ pwg, const char* __restrict__ pwu, const char* __restrict__ pwd,
                                                       const char* __restrict__ pwr, char* pout) {
    unsigned tid = threadIdx.x, bid = blockIdx.x;
#if defined(__HIP_DEVICE_COMPILE__)
    asm volatile(
"s_load_dwordx2 s[16:17],s[0:1],8\n"
"s_load_dword s3,s[0:1],72\n"
"s_getpc_b64 s[8:9]\n"
".Lfk_pc:\n"
"s_lshr_b32 s82,s2,3\n"
"s_lshl_b32 s82,s82,12\n"
"v_lshlrev_b32 v1,4,v0\n"
"s_mov_b32 s10,.Lfk_cold-.Lfk_pc\n"
"s_mov_b32 s11,0x20000\n"
"buffer_load_dword v115,v1,s[8:11],s82 offen\n"
"v_readfirstlane_b32 s52,v0\n"
"s_load_dwordx2 s[32:33],s[0:1],0\n"
"s_load_dwordx2 s[24:25],s[0:1],40\n"
"s_load_dwordx2 s[44:45],s[0:1],8\n"
"s_load_dwordx2 s[28:29],s[0:1],16\n"
"s_load_dwordx2 s[40:41],s[0:1],16\n"
"s_load_dwordx2 s[4:5],s[0:1],48\n"
"s_and_b32 s82,s2,8\n"
"s_add_u32 s82,s82,24\n"
"s_load_dwordx2 s[20:21],s[0:1],s82\n"
"s_lshr_b32 s52,s52,6\n"
"s_lshr_b32 s83,s2,7\n"
"s_lshl_b32 s83,s83,3\n"
"s_add_u32 s83,s83,24\n"
"s_load_dwordx2 s[36:37],s[0:1],s83\n"
"s_mov_b32 s100,s2\n"
"s_lshl_b32 s92,s2,2\n"
"s_add_u32 s92,s92,s52\n"
"v_and_b32 v0,63,v0\n"
"v_lshlrev_b32 v112,4,v0\n"
"s_and_b32 s88,s2,7\n"
"s_lshr_b32 s89,s2,3\n"
"s_lshl_b32 s93,s89,2\n"
"s_add_u32 s93,s93,s52\n"
"v_lshrrev_b32 v2,3,v0\n"
"v_xor_b32 v3,v2,v0\n"
"v_and_b32 v3,7,v3\n"
"v_lshlrev_b32 v3,4,v3\n"
"s_lshl_b32 s82,s52,9\n"
"v_add_u32 v2,s82,v2\n"
"v_lshl_add_u32 v106,v2,10,v3\n"
"v_add_u32 v107,7168,v106\n"
"v_add_u32 v108,14336,v106\n"
"v_add_u32 v109,21504,v106\n"
"v_lshrrev_b32 v4,4,v0\n"
"v_and_b32 v5,15,v0\n"
"v_lshlrev_b32 v6,1,v4\n"
"v_xor_b32 v5,v5,v6\n"
"v_lshlrev_b32 v5,4,v5\n"
"v_xor_b32 v6,0x80,v5\n"
"s_and_b32 s82,s52,1\n"
"s_lshl_b32 s82,s82,17\n"
"s_lshr_b32 s83,s52,1\n"
"s_lshl_b32 s83,s83,8\n"
"s_add_u32 s82,s82,s83\n"
"v_lshl_add_u32 v7,v4,13,s82\n"
"v_add_u32 v110,v7,v5\n"
"v_add_u32 v111,31744,v110\n"
"v_add_u32 v113,v7,v6\n"
"v_add_u32 v113,63488,v113\n"
"v_add_u32 v114,31744,v113\n"
"s_lshl_b32 s78,s52,12\n"
"s_lshl_b32 s79,s52,13\n"
"s_lshl_b32 s85,s52,11\n"
"s_lshr_b32 s82,s2,5\n"
"s_lshl_b32 s82,s82,15\n"
"s_lshl_b32 s83,s88,10\n"
"s_bfe_u32 s97,s2,0x10004\n"
"s_lshl_b32 s97,s97,9\n"
"s_add_u32 s83,s83,s97\n"
"v_lshrrev_b32 v60,3,v0\n"
"v_lshlrev_b32 v60,19,v60\n"
"v_and_b32 v61,7,v0\n"
"v_lshl_add_u32 v60,v61,7,v60\n"
"s_and_b32 s12,s93,63\n"
"s_lshl3_add_u32 s12,s12,s88\n"
"s_lshl_b32 s12,s12,10\n"
"s_add_u32 s38,s12,1049600\n"
"s_mov_b32 s39,0x20000\n"
"s_and_b32 s14,s52,1\n"
"s_mul_i32 s13,s14,507904\n"
"v_subrev_u32 v62,s13,v106\n"
"v_subrev_u32 v63,s13,v107\n"
"s_mul_i32 s14,s14,6144\n"
"s_sub_u32 s13,s79,s14\n"
"s_waitcnt lgkmcnt(0)\n"
"buffer_load_dword v115,v60,s[36:39],s12 offen\n"
"s_lshl_b32 s87,s92,11\n"
"s_add_u32 s44,s44,s87\n"
"s_addc_u32 s45,s45,0\n"
"s_mov_b32 s46,2048\n"
"s_mov_b32 s47,0x20000\n"
"v_lshlrev_b32 v1,5,v0\n"
"buffer_load_dword v115,v1,s[44:47],0 offen\n"
"s_add_u32 s16,s16,s82\n"
"s_addc_u32 s17,s17,0\n"
"s_sub_u32 s18,2097152,s82\n"
"s_mov_b32 s19,0x20000\n"
"s_add_u32 s20,s20,s83\n"
"s_addc_u32 s21,s21,0\n"
"s_sub_u32 s22,4194304,s83\n"
"s_mov_b32 s23,0x20000\n"
"s_mov_b32 s77,0\n"
"s_add_u32 m0,s78,147456\n"
"s_nop 0\n"
"buffer_load_dwordx4 v110,s[20:23],s77 offen offset:0 lds\n"
"buffer_load_dwordx4 v111,s[20:23],s77 offen offset:1024 lds\n"
"buffer_load_dwordx4 v113,s[20:23],s77 offen offset:2048 lds\n"
"buffer_load_dwordx4 v114,s[20:23],s77 offen offset:3072 lds\n"
"s_mov_b32 s76,786432\n"
"s_add_u32 m0,s13,12288\n"
"s_nop 0\n"
"buffer_load_dwordx4 v62,s[16:19],s76 offen offset:0 lds\n"
"buffer_load_dwordx4 v63,s[16:19],s76 offen offset:1024 lds\n"
"s_mov_b32 s77,262144\n"
"s_add_u32 m0,s78,131072\n"
"s_nop 0\n"
"buffer_load_dwordx4 v110,s[20:23],s77 offen offset:0 lds\n"
"buffer_load_dwordx4 v111,s[20:23],s77 offen offset:1024 lds\n"
"buffer_load_dwordx4 v113,s[20:23],s77 offen offset:2048 lds\n"
"buffer_load_dwordx4 v114,s[20:23],s77 offen offset:3072 lds\n"
"s_mov_b32 s76,0\n"
"s_add_u32 m0,s13,0\n"
"s_nop 0\n"
"buffer_load_dwordx4 v62,s[16:19],s76 offen offset:0 lds\n"
"buffer_load_dwordx4 v63,s[16:19],s76 offen offset:1024 lds\n"
"s_mov_b32 s76,262144\n"
"s_add_u32 m0,s13,4096\n"
"s_nop 0\n"
"buffer_load_dwordx4 v62,s[16:19],s76 offen offset:0 lds\n"
"buffer_load_dwordx4 v63,s[16:19],s76 offen offset:1024 lds\n"
"s_mov_b32 s76,524288\n"
"s_add_u32 m0,s13,8192\n"
"s_nop 0\n"
"buffer_load_dwordx4 v62,s[16:19],s76 offen offset:0 lds\n"
"buffer_load_dwordx4 v63,s[16:19],s76 offen offset:1024 lds\n"
"s_lshl_b32 s82,s92,12\n"
"s_add_u32 s32,s32,s82\n"
"s_addc_u32 s33,s33,0\n"
"s_mov_b32 s34,4096\n"
"s_mov_b32 s35,0x20000\n"
"s_mov_b32 s26,176160768\n"
"s_mov_b32 s27,0x20000\n"
"s_mov_b32 s75,148897792\n"
"s_lshr_b32 s54,s2,5\n"
"s_lshl_b32 s54,s54,19\n"
"s_and_b32 s82,s92,0x47\n"
"s_bfe_u32 s83,s92,0x10003\n"
"s_lshl_b32 s83,s83,5\n"
"s_or_b32 s82,s82,s83\n"
"s_bfe_u32 s83,s92,0x20004\n"
"s_lshl3_add_u32 s82,s83,s82\n"
"s_lshl_b32 s82,s82,7\n"
"s_add_u32 s54,s54,s82\n"
"v_lshrrev_b32 v1,3,v0\n"
"v_bfe_u32 v2,v0,2,1\n"
"v_and_b32 v3,3,v0\n"
"v_lshlrev_b32 v3,4,v3\n"
"v_lshl_add_u32 v70,v1,6,v3\n"
"v_lshl_add_u32 v70,v2,11,v70\n"
"s_bfe_u32 s82,s54,0x10009\n"
"s_lshl_b32 s82,s82,5\n"
"v_xor_b32 v3,s82,v3\n"
"v_lshl_add_u32 v71,v2,6,v3\n"
"v_lshl_add_u32 v71,v1,14,v71\n"
"buffer_load_dwordx2 v[78:79],off,s[24:27],s75 sc1\n"
"s_lshl_b32 s82,s52,16\n"
"v_lshl_add_u32 v7,v4,13,s82\n"
"s_bitcmp1_b32 s52,0\n"
"s_cselect_b32 s83,0x80,0\n"
"v_xor_b32 v5,s83,v5\n"
"v_add_u32 v72,v7,v5\n"
"v_add_u32 v73,32768,v72\n"
"s_mov_b32 s86,0\n"
"v_and_b32 v1,15,v0\n"
"v_lshrrev_b32 v2,4,v0\n"
"v_lshlrev_b32 v3,6,v1\n"
"v_lshl_add_u32 v3,v2,4,v3\n"
"v_lshrrev_b32 v4,4,v3\n"
"v_and_b32 v4,32,v4\n"
"v_xor_b32 v97,v3,v4\n"
"s_lshr_b32 s82,s52,1\n"
"s_lshl_b32 s82,s82,14\n"
"v_and_b32 v3,7,v1\n"
"v_xor_b32 v3,v3,v2\n"
"v_lshlrev_b32 v3,4,v3\n"
"v_lshl_add_u32 v3,v1,7,v3\n"
"v_add_u32 v80,s82,v3\n"
"v_xor_b32 v81,64,v80\n"
"v_add_u32 v82,65536,v80\n"
"v_add_u32 v83,65536,v81\n"
"v_bfe_u32 v3,v0,2,2\n"
"v_lshl_add_u32 v4,v2,3,v3\n"
"v_lshlrev_b32 v4,8,v4\n"
"v_and_b32 v5,1,v2\n"
"v_lshl_add_u32 v5,v5,2,v3\n"
"v_lshlrev_b32 v5,5,v5\n"
"v_and_b32 v6,3,v0\n"
"v_lshl_add_u32 v4,v6,3,v4\n"
"s_and_b32 s82,s52,1\n"
"s_lshl_b32 s82,s82,13\n"
"s_add_u32 s82,s82,98304\n"
"v_add_u32 v4,s82,v4\n"
"v_xor_b32 v6,0,v5\n"
"v_add_u32 v98,v4,v6\n"
"v_xor_b32 v6,32,v5\n"
"v_add_u32 v99,v4,v6\n"
"v_xor_b32 v6,64,v5\n"
"v_add_u32 v100,v4,v6\n"
"v_xor_b32 v6,96,v5\n"
"v_add_u32 v101,v4,v6\n"
"v_xor_b32 v6,128,v5\n"
"v_add_u32 v102,v4,v6\n"
"v_xor_b32 v6,160,v5\n"
"v_add_u32 v103,v4,v6\n"
"v_xor_b32 v6,192,v5\n"
"v_add_u32 v104,v4,v6\n"
"v_xor_b32 v6,224,v5\n"
"v_add_u32 v105,v4,v6\n"
"s_and_b32 s53,s89,1\n"
"s_lshl_b32 s83,s88,10\n"
"s_and_b32 s82,s93,63\n"
"s_add_u32 s82,s82,128\n"
"s_lshl_b32 s82,s82,13\n"
"s_add_u32 s82,s82,s83\n"
"s_add_u32 s36,s36,s82\n"
"s_addc_u32 s37,s37,0\n"
"s_mov_b32 s38,2622464\n"
"s_mov_b32 s39,0x20000\n"
"s_lshl_b32 s82,s93,13\n"
"s_add_u32 s82,s82,s83\n"
"s_add_u32 s40,s40,s82\n"
"s_addc_u32 s41,s41,0\n"
"s_mov_b32 s42,7341056\n"
"s_mov_b32 s43,0x20000\n"
"s_cmp_lg_u32 s53,0\n"
"s_cbranch_scc1 .Lfk_v\n"
"s_waitcnt vmcnt(11)\n"
"s_barrier\n"
"ds_read_b128 v[152:155],v80 offset:12288\n"
"ds_read_b128 v[156:159],v80 offset:14336\n"
"ds_read_b64_tr_b16 v[160:161],v98 offset:49152\n"
"ds_read_b64_tr_b16 v[162:163],v98 offset:50176\n"
"ds_read_b64_tr_b16 v[164:165],v99 offset:49152\n"
"ds_read_b64_tr_b16 v[166:167],v99 offset:50176\n"
"ds_read_b64_tr_b16 v[168:169],v100 offset:49152\n"
"ds_read_b64_tr_b16 v[170:171],v100 offset:50176\n"
"ds_read_b64_tr_b16 v[172:173],v101 offset:49152\n"
"ds_read_b64_tr_b16 v[174:175],v101 offset:50176\n"
"ds_read_b64_tr_b16 v[176:177],v102 offset:49152\n"
"ds_read_b64_tr_b16 v[178:179],v102 offset:50176\n"
"ds_read_b64_tr_b16 v[180:181],v103 offset:49152\n"
"ds_read_b64_tr_b16 v[182:183],v103 offset:50176\n"
"ds_read_b64_tr_b16 v[184:185],v104 offset:49152\n"
"ds_read_b64_tr_b16 v[186:187],v104 offset:50176\n"
"ds_read_b64_tr_b16 v[188:189],v105 offset:49152\n"
"ds_read_b64_tr_b16 v[190:191],v105 offset:50176\n"
"s_waitcnt lgkmcnt(12)\n"
"v_mfma_f32_16x16x32_bf16 a[192:195],v[160:163],v[152:155],0\n"
"s_and_b32 s91,s89,7\n"
"s_mov_b32 s76,128\n"
"s_add_u32 m0,s79,32768\n"
"v_mfma_f32_16x16x32_bf16 a[224:227],v[160:163],v[156:159],0\n"
"buffer_load_dwordx4 v106,s[16:19],s76 offen offset:0 lds\n"
"v_mfma_f32_16x16x32_bf16 a[228:231],v[164:167],v[156:159],0\n"
"s_lshr_b32 s97,s89,3\n"
"s_lshl2_add_u32 s90,s88,s97\n"
"v_mfma_f32_16x16x32_bf16 a[196:199],v[164:167],v[152:155],0\n"
"buffer_load_dwordx4 v107,s[16:19],s76 offen offset:1024 lds\n"
"s_waitcnt lgkmcnt(8)\n"
"v_mfma_f32_16x16x32_bf16 a[200:203],v[168:171],v[152:155],0\n"
"s_lshl_b32 vcc_lo,s90,8\n"
"s_add_u32 s28,s28,vcc_lo\n"
"s_addc_u32 s29,s29,0\n"
"v_mfma_f32_16x16x32_bf16 a[232:235],v[168:171],v[156:159],0\n"
"buffer_load_dwordx4 v108,s[16:19],s76 offen offset:2048 lds\n"
"v_mfma_f32_16x16x32_bf16 a[236:239],v[172:175],v[156:159],0\n"
"s_sub_u32 s30,8388608,vcc_lo\n"
"s_mov_b32 s31,0x20000\n"
"buffer_load_dwordx4 v109,s[16:19],s76 offen offset:3072 lds\n"
"v_mfma_f32_16x16x32_bf16 a[204:207],v[172:175],v[152:155],0\n"
"s_waitcnt vmcnt(9)\n"
"s_barrier\n"
"s_waitcnt lgkmcnt(4)\n"
"v_mfma_f32_16x16x32_bf16 a[208:211],v[176:179],v[152:155],0\n"
"ds_read_b128 v[128:131],v80 offset:0\n"
"ds_read_b128 v[192:195],v81 offset:0\n"
"v_mfma_f32_16x16x32_bf16 a[240:243],v[176:179],v[156:159],0\n"
"ds_read_b64_tr_b16 v[224:225],v98 offset:32768\n"
"ds_read_b64_tr_b16 v[226:227],v98 offset:33792\n"
"v_mfma_f32_16x16x32_bf16 a[244:247],v[180:183],v[156:159],0\n"
"ds_read_b64_tr_b16 v[228:229],v99 offset:32768\n"
"ds_read_b64_tr_b16 v[230:231],v99 offset:33792\n"
"v_mfma_f32_16x16x32_bf16 a[212:215],v[180:183],v[152:155],0\n"
"ds_read_b64_tr_b16 v[232:233],v100 offset:32768\n"
"ds_read_b64_tr_b16 v[234:235],v100 offset:33792\n"
"s_waitcnt lgkmcnt(8)\n"
"v_mfma_f32_16x16x32_bf16 a[216:219],v[184:187],v[152:155],0\n"
"ds_read_b64_tr_b16 v[236:237],v101 offset:32768\n"
"ds_read_b64_tr_b16 v[238:239],v101 offset:33792\n"
"v_mfma_f32_16x16x32_bf16 a[248:251],v[184:187],v[156:159],0\n"
"ds_read_b64_tr_b16 v[240:241],v102 offset:32768\n"
"ds_read_b64_tr_b16 v[242:243],v102 offset:33792\n"
"v_mfma_f32_16x16x32_bf16 a[252:255],v[188:191],v[156:159],0\n"
"ds_read_b64_tr_b16 v[244:245],v103 offset:32768\n"
"ds_read_b64_tr_b16 v[246:247],v103 offset:33792\n"
"v_mfma_f32_16x16x32_bf16 a[220:223],v[188:191],v[152:155],0\n"
"ds_read_b64_tr_b16 v[248:249],v104 offset:32768\n"
"ds_read_b64_tr_b16 v[250:251],v104 offset:33792\n"
"s_waitcnt lgkmcnt(10)\n"
"v_mfma_f32_16x16x32_bf16 a[0:3],v[160:163],v[128:131],0\n"
"ds_read_b64_tr_b16 v[252:253],v105 offset:32768\n"
"ds_read_b64_tr_b16 v[254:255],v105 offset:33792\n"
"v_mfma_f32_16x16x32_bf16 a[0:3],v[224:227],v[192:195],a[0:3]\n"
"ds_read_b128 v[132:135],v80 offset:2048\n"
"ds_read_b128 v[196:199],v81 offset:2048\n"
"v_mfma_f32_16x16x32_bf16 a[4:7],v[228:231],v[192:195],0\n"
"ds_read_b128 v[216:219],v81 offset:12288\n"
"ds_read_b128 v[220:223],v81 offset:14336\n"
"v_mfma_f32_16x16x32_bf16 a[4:7],v[164:167],v[128:131],a[4:7]\n"
"s_waitcnt lgkmcnt(12)\n"
"s_mov_b32 s76,262272\n"
"s_add_u32 m0,s79,36864\n"
"v_mfma_f32_16x16x32_bf16 a[8:11],v[168:171],v[128:131],0\n"
"buffer_load_dwordx4 v106,s[16:19],s76 offen offset:0 lds\n"
"v_mfma_f32_16x16x32_bf16 a[8:11],v[232:235],v[192:195],a[8:11]\n"
"buffer_load_dwordx4 v107,s[16:19],s76 offen offset:1024 lds\n"
"v_mfma_f32_16x16x32_bf16 a[12:15],v[236:239],v[192:195],0\n"
"buffer_load_dwordx4 v108,s[16:19],s76 offen offset:2048 lds\n"
"buffer_load_dwordx4 v109,s[16:19],s76 offen offset:3072 lds\n"
"v_mfma_f32_16x16x32_bf16 a[12:15],v[172:175],v[128:131],a[12:15]\n"
"s_waitcnt lgkmcnt(8)\n"
"s_mov_b32 s77,524288\n"
"s_add_u32 m0,s78,98304\n"
"v_mfma_f32_16x16x32_bf16 a[16:19],v[176:179],v[128:131],0\n"
"buffer_load_dwordx4 v110,s[20:23],s77 offen offset:0 lds\n"
"v_mfma_f32_16x16x32_bf16 a[16:19],v[240:243],v[192:195],a[16:19]\n"
"buffer_load_dwordx4 v111,s[20:23],s77 offen offset:1024 lds\n"
"v_mfma_f32_16x16x32_bf16 a[20:23],v[244:247],v[192:195],0\n"
"buffer_load_dwordx4 v113,s[20:23],s77 offen offset:2048 lds\n"
"buffer_load_dwordx4 v114,s[20:23],s77 offen offset:3072 lds\n"
"v_mfma_f32_16x16x32_bf16 a[20:23],v[180:183],v[128:131],a[20:23]\n"
"s_waitcnt lgkmcnt(4)\n"
"s_mov_b32 s77,786432\n"
"s_add_u32 m0,s78,114688\n"
"v_mfma_f32_16x16x32_bf16 a[24:27],v[184:187],v[128:131],0\n"
"buffer_load_dwordx4 v110,s[20:23],s77 offen offset:0 lds\n"
"v_mfma_f32_16x16x32_bf16 a[24:27],v[248:251],v[192:195],a[24:27]\n"
"buffer_load_dwordx4 v111,s[20:23],s77 offen offset:1024 lds\n"
"v_mfma_f32_16x16x32_bf16 a[28:31],v[252:255],v[192:195],0\n"
"buffer_load_dwordx4 v113,s[20:23],s77 offen offset:2048 lds\n"
"buffer_load_dwordx4 v114,s[20:23],s77 offen offset:3072 lds\n"
"v_mfma_f32_16x16x32_bf16 a[28:31],v[188:191],v[128:131],a[28:31]\n"
"s_waitcnt lgkmcnt(2)\n"
"s_mov_b32 s76,256\n"
"s_add_u32 m0,s79,65536\n"
"v_mfma_f32_16x16x32_bf16 a[32:35],v[160:163],v[132:135],0\n"
"buffer_load_dwordx4 v106,s[16:19],s76 offen offset:0 lds\n"
"v_mfma_f32_16x16x32_bf16 a[32:35],v[224:227],v[196:199],a[32:35]\n"
"buffer_load_dwordx4 v107,s[16:19],s76 offen offset:1024 lds\n"
"v_mfma_f32_16x16x32_bf16 a[36:39],v[228:231],v[196:199],0\n"
"buffer_load_dwordx4 v108,s[16:19],s76 offen offset:2048 lds\n"
"buffer_load_dwordx4 v109,s[16:19],s76 offen offset:3072 lds\n"
"v_mfma_f32_16x16x32_bf16 a[36:39],v[164:167],v[132:135],a[36:39]\n"
"s_waitcnt vmcnt(21)\n"
"s_barrier\n"
"v_mfma_f32_16x16x32_bf16 a[40:43],v[168:171],v[132:135],0\n"
"ds_read_b128 v[136:139],v80 offset:4096\n"
"ds_read_b128 v[200:203],v81 offset:4096\n"
"v_mfma_f32_16x16x32_bf16 a[40:43],v[232:235],v[196:199],a[40:43]\n"
"ds_read_b128 v[140:143],v80 offset:6144\n"
"ds_read_b128 v[204:207],v81 offset:6144\n"
"v_mfma_f32_16x16x32_bf16 a[44:47],v[236:239],v[196:199],0\n"
"ds_read_b128 v[144:147],v80 offset:8192\n"
"ds_read_b128 v[208:211],v81 offset:8192\n"
"v_mfma_f32_16x16x32_bf16 a[44:47],v[172:175],v[132:135],a[44:47]\n"
"ds_read_b128 v[148:151],v80 offset:10240\n"
"ds_read_b128 v[212:215],v81 offset:10240\n"
"v_mfma_f32_16x16x32_bf16 a[48:51],v[176:179],v[132:135],0\n"
"s_mov_b32 s60,s91\n"
"s_mov_b32 s76,262400\n"
"s_add_u32 m0,s79,69632\n"
"v_mfma_f32_16x16x32_bf16 a[48:51],v[240:243],v[196:199],a[48:51]\n"
"buffer_load_dwordx4 v106,s[16:19],s76 offen offset:0 lds\n"
"v_mfma_f32_16x16x32_bf16 a[52:55],v[244:247],v[196:199],0\n"
"buffer_load_dwordx4 v107,s[16:19],s76 offen offset:1024 lds\n"
"v_mfma_f32_16x16x32_bf16 a[52:55],v[180:183],v[132:135],a[52:55]\n"
"buffer_load_dwordx4 v108,s[16:19],s76 offen offset:2048 lds\n"
"v_mfma_f32_16x16x32_bf16 a[56:59],v[184:187],v[132:135],0\n"
"buffer_load_dwordx4 v109,s[16:19],s76 offen offset:3072 lds\n"
"v_mfma_f32_16x16x32_bf16 a[56:59],v[248:251],v[196:199],a[56:59]\n"
"s_sub_u32 s94,15,s91\n"
"s_waitcnt lgkmcnt(0)\n"
"s_barrier\n"
"v_mfma_f32_16x16x32_bf16 a[60:63],v[252:255],v[196:199],0\n"
"s_cmp_lt_u32 s52,2\n"
"s_cselect_b32 s95,s94,s91\n"
"s_mov_b32 s77,1048576\n"
"s_add_u32 m0,s78,147456\n"
"v_mfma_f32_16x16x32_bf16 a[60:63],v[188:191],v[132:135],a[60:63]\n"
"buffer_load_dwordx4 v110,s[20:23],s77 offen offset:0 lds\n"
"v_mfma_f32_16x16x32_bf16 a[64:67],v[160:163],v[136:139],0\n"
"s_lshr_b32 vcc_lo,s95,1\n"
"s_lshl_b32 s55,vcc_lo,19\n"
"v_mfma_f32_16x16x32_bf16 a[64:67],v[224:227],v[200:203],a[64:67]\n"
"s_lshl_b32 s58,vcc_lo,10\n"
"s_add_u32 s58,s58,148898816\n"
"v_mfma_f32_16x16x32_bf16 a[68:71],v[228:231],v[200:203],0\n"
"s_and_b32 vcc_lo,s95,1\n"
"s_lshl_b32 vcc_lo,vcc_lo,13\n"
"v_mfma_f32_16x16x32_bf16 a[68:71],v[164:167],v[136:139],a[68:71]\n"
"buffer_load_dwordx4 v111,s[20:23],s77 offen offset:1024 lds\n"
"v_mfma_f32_16x16x32_bf16 a[72:75],v[168:171],v[136:139],0\n"
"s_and_b32 vcc_hi,s78,4096\n"
"s_add_u32 s55,s55,vcc_lo\n"
"v_mfma_f32_16x16x32_bf16 a[72:75],v[232:235],v[200:203],a[72:75]\n"
"s_add_u32 s55,s55,vcc_hi\n"
"s_and_b32 vcc_lo,s89,2\n"
"v_mfma_f32_16x16x32_bf16 a[76:79],v[236:239],v[200:203],0\n"
"s_lshl2_add_u32 vcc_lo,s88,vcc_lo\n"
"s_and_b32 vcc_hi,s52,1\n"
"v_mfma_f32_16x16x32_bf16 a[76:79],v[172:175],v[136:139],a[76:79]\n"
"buffer_load_dwordx4 v113,s[20:23],s77 offen offset:2048 lds\n"
"v_mfma_f32_16x16x32_bf16 a[80:83],v[176:179],v[136:139],0\n"
"s_add_u32 vcc_lo,vcc_lo,vcc_hi\n"
"s_lshr_b32 vcc_hi,s89,2\n"
"v_mfma_f32_16x16x32_bf16 a[80:83],v[240:243],v[200:203],a[80:83]\n"
"s_lshr_b32 s84,s52,1\n"
"s_lshl1_add_u32 vcc_hi,vcc_hi,s84\n"
"v_mfma_f32_16x16x32_bf16 a[84:87],v[244:247],v[200:203],0\n"
"s_mul_i32 s56,vcc_lo,1179648\n"
"s_lshl_b32 s84,s84,19\n"
"v_mfma_f32_16x16x32_bf16 a[84:87],v[180:183],v[136:139],a[84:87]\n"
"buffer_load_dwordx4 v114,s[20:23],s77 offen offset:3072 lds\n"
"v_mfma_f32_16x16x32_bf16 a[88:91],v[184:187],v[136:139],0\n"
"s_add_u32 s56,s56,s84\n"
"s_lshr_b32 s84,s89,2\n"
"v_mfma_f32_16x16x32_bf16 a[88:91],v[248:251],v[200:203],a[88:91]\n"
"s_lshl_b32 s84,s84,14\n"
"s_add_u32 s56,s56,s84\n"
"v_mfma_f32_16x16x32_bf16 a[92:95],v[252:255],v[200:203],0\n"
"s_lshl_b32 s84,s53,13\n"
"s_mov_b32 s77,1310720\n"
"s_add_u32 m0,s78,131072\n"
"v_mfma_f32_16x16x32_bf16 a[92:95],v[188:191],v[136:139],a[92:95]\n"
"buffer_load_dwordx4 v110,s[20:23],s77 offen offset:0 lds\n"
"v_mfma_f32_16x16x32_bf16 a[96:99],v[160:163],v[140:143],0\n"
"s_add_u32 s56,s56,s84\n"
"s_add_u32 s56,s56,4194304\n"
"v_mfma_f32_16x16x32_bf16 a[96:99],v[224:227],v[204:207],a[96:99]\n"
"s_lshl4_add_u32 vcc_hi,s53,vcc_hi\n"
"s_lshl_b32 vcc_lo,vcc_lo,8\n"
"v_mfma_f32_16x16x32_bf16 a[100:103],v[228:231],v[204:207],0\n"
"buffer_load_dwordx4 v111,s[20:23],s77 offen offset:1024 lds\n"
"v_mfma_f32_16x16x32_bf16 a[100:103],v[164:167],v[140:143],a[100:103]\n"
"s_lshl3_add_u32 s59,vcc_hi,vcc_lo\n"
"s_add_u32 s59,s59,148914176\n"
"v_mfma_f32_16x16x32_bf16 a[104:107],v[168:171],v[140:143],0\n"
"s_lshl_b32 s57,s92,3\n"
"s_add_u32 s57,s57,148898816\n"
"v_mfma_f32_16x16x32_bf16 a[104:107],v[232:235],v[204:207],a[104:107]\n"
"s_lshl_b32 s63,s90,8\n"
"s_add_u32 s63,s63,148914176\n"
"v_mfma_f32_16x16x32_bf16 a[108:111],v[236:239],v[204:207],0\n"
"buffer_load_dwordx4 v113,s[20:23],s77 offen offset:2048 lds\n"
"v_mfma_f32_16x16x32_bf16 a[108:111],v[172:175],v[140:143],a[108:111]\n"
"s_lshl_b32 s74,s92,11\n"
"s_add_u32 s74,s74,171966464\n"
"v_mfma_f32_16x16x32_bf16 a[112:115],v[176:179],v[140:143],0\n"
"s_mul_i32 s96,s90,1179648\n"
"s_add_u32 s96,s96,4194304\n"
"v_mfma_f32_16x16x32_bf16 a[112:115],v[240:243],v[204:207],a[112:115]\n"
"s_add_u32 s73,s96,s85\n"
"s_and_b32 vcc_hi,s78,4096\n"
"v_mfma_f32_16x16x32_bf16 a[116:119],v[244:247],v[204:207],0\n"
"buffer_load_dwordx4 v114,s[20:23],s77 offen offset:3072 lds\n"
"v_mfma_f32_16x16x32_bf16 a[116:119],v[180:183],v[140:143],a[116:119]\n"
"s_lshl_b32 vcc_hi,vcc_hi,1\n"
"s_add_u32 vcc_lo,s96,vcc_hi\n"
"v_mfma_f32_16x16x32_bf16 a[120:123],v[184:187],v[140:143],0\n"
"s_add_u32 s65,vcc_lo,524288\n"
"s_lshl_b32 s66,s91,16\n"
"v_mfma_f32_16x16x32_bf16 a[120:123],v[248:251],v[204:207],a[120:123]\n"
"s_sub_u32 s66,vcc_lo,s66\n"
"s_mov_b32 s76,384\n"
"s_add_u32 m0,s79,0\n"
"v_mfma_f32_16x16x32_bf16 a[124:127],v[252:255],v[204:207],0\n"
"buffer_load_dwordx4 v106,s[16:19],s76 offen offset:0 lds\n"
"v_mfma_f32_16x16x32_bf16 a[124:127],v[188:191],v[140:143],a[124:127]\n"
"s_add_u32 s66,s66,999424\n"
"s_add_u32 s67,vcc_hi,65536\n"
"v_mfma_f32_16x16x32_bf16 a[128:131],v[160:163],v[144:147],0\n"
"s_mul_i32 vcc_lo,vcc_hi,5\n"
"s_sub_u32 vcc_lo,s96,vcc_lo\n"
"v_mfma_f32_16x16x32_bf16 a[128:131],v[224:227],v[208:211],a[128:131]\n"
"s_add_u32 s64,vcc_lo,81920\n"
"s_add_u32 s62,vcc_lo,135168\n"
"s_add_u32 s61,vcc_lo,524288\n"
"v_mfma_f32_16x16x32_bf16 a[132:135],v[228:231],v[208:211],0\n"
"buffer_load_dwordx4 v107,s[16:19],s76 offen offset:1024 lds\n"
"v_mfma_f32_16x16x32_bf16 a[132:135],v[164:167],v[144:147],a[132:135]\n"
"s_cmp_lt_u32 s52,2\n"
"s_cselect_b32 s64,s64,2146435072\n"
"s_cselect_b32 s67,s67,163840\n"
"s_cselect_b32 s65,s65,1\n"
"v_mfma_f32_16x16x32_bf16 a[136:139],v[168:171],v[144:147],0\n"
"s_cselect_b32 vcc_lo,4,0\n"
"s_and_b32 vcc_hi,s52,1\n"
"s_add_u32 vcc_lo,vcc_lo,vcc_hi\n"
"v_mfma_f32_16x16x32_bf16 a[136:139],v[232:235],v[208:211],a[136:139]\n"
"s_lshl_b32 vcc_hi,s90,12\n"
"s_add_u32 vcc_hi,vcc_hi,54525952\n"
"v_mfma_f32_16x16x32_bf16 a[140:143],v[236:239],v[208:211],0\n"
"buffer_load_dwordx4 v108,s[16:19],s76 offen offset:2048 lds\n"
"v_mfma_f32_16x16x32_bf16 a[140:143],v[172:175],v[144:147],a[140:143]\n"
"s_lshl3_add_u32 s70,s95,vcc_lo\n"
"s_lshl_b32 s70,s70,17\n"
"v_mfma_f32_16x16x32_bf16 a[144:147],v[176:179],v[144:147],0\n"
"s_add_u32 s70,s70,vcc_hi\n"
"s_lshl3_add_u32 s68,s94,vcc_lo\n"
"v_mfma_f32_16x16x32_bf16 a[144:147],v[240:243],v[208:211],a[144:147]\n"
"s_lshl_b32 s68,s68,17\n"
"s_add_u32 s68,s68,vcc_hi\n"
"v_mfma_f32_16x16x32_bf16 a[148:151],v[244:247],v[208:211],0\n"
"buffer_load_dwordx4 v109,s[16:19],s76 offen offset:3072 lds\n"
"v_mfma_f32_16x16x32_bf16 a[148:151],v[180:183],v[144:147],a[148:151]\n"
"s_lshr_b32 vcc_lo,s90,3\n"
"s_lshl2_add_u32 vcc_lo,vcc_lo,s52\n"
"v_mfma_f32_16x16x32_bf16 a[152:155],v[184:187],v[144:147],0\n"
"s_lshl_b32 vcc_lo,vcc_lo,7\n"
"s_and_b32 vcc_hi,s90,7\n"
"v_mfma_f32_16x16x32_bf16 a[152:155],v[248:251],v[208:211],a[152:155]\n"
"s_add_u32 vcc_lo,vcc_lo,vcc_hi\n"
"s_mov_b32 s76,262528\n"
"s_add_u32 m0,s79,4096\n"
"v_mfma_f32_16x16x32_bf16 a[156:159],v[252:255],v[208:211],0\n"
"buffer_load_dwordx4 v106,s[16:19],s76 offen offset:0 lds\n"
"v_mfma_f32_16x16x32_bf16 a[156:159],v[188:191],v[144:147],a[156:159]\n"
"s_lshl3_add_u32 s69,s91,vcc_lo\n"
"s_lshl_b32 s69,s69,3\n"
"v_mfma_f32_16x16x32_bf16 a[160:163],v[160:163],v[148:151],0\n"
"s_add_u32 s69,s69,148930560\n"
"s_lshl3_add_u32 s71,s94,vcc_lo\n"
"v_mfma_f32_16x16x32_bf16 a[160:163],v[224:227],v[212:215],a[160:163]\n"
"s_lshl_b32 s71,s71,3\n"
"s_add_u32 s71,s71,148930560\n"
"v_mfma_f32_16x16x32_bf16 a[164:167],v[228:231],v[212:215],0\n"
"buffer_load_dwordx4 v107,s[16:19],s76 offen offset:1024 lds\n"
"v_mfma_f32_16x16x32_bf16 a[164:167],v[164:167],v[148:151],a[164:167]\n"
"s_mov_b32 s6,8388608\n"
"s_mov_b32 s7,0x20000\n"
"v_mfma_f32_16x16x32_bf16 a[168:171],v[168:171],v[148:151],0\n"
"s_lshr_b32 s94,s88,1\n"
"s_and_b32 vcc_lo,s91,1\n"
"v_mfma_f32_16x16x32_bf16 a[168:171],v[232:235],v[212:215],a[168:171]\n"
"s_lshl1_add_u32 s95,s97,vcc_lo\n"
"s_and_b32 vcc_lo,s88,1\n"
"v_mfma_f32_16x16x32_bf16 a[172:175],v[236:239],v[212:215],0\n"
"buffer_load_dwordx4 v108,s[16:19],s76 offen offset:2048 lds\n"
"v_mfma_f32_16x16x32_bf16 a[172:175],v[172:175],v[148:151],a[172:175]\n"
"s_lshr_b32 vcc_hi,s91,1\n"
"s_lshl2_add_u32 s96,vcc_lo,vcc_hi\n"
"v_mfma_f32_16x16x32_bf16 a[176:179],v[176:179],v[148:151],0\n"
"s_lshr_b32 vcc_lo,s88,2\n"
"s_lshl2_add_u32 s96,vcc_lo,vcc_hi\n"
"v_mfma_f32_16x16x32_bf16 a[176:179],v[240:243],v[212:215],a[176:179]\n"
"s_sub_u32 vcc_lo,7,s88\n"
"s_min_u32 s94,vcc_lo,s88\n"
"v_mfma_f32_16x16x32_bf16 a[180:183],v[244:247],v[212:215],0\n"
"buffer_load_dwordx4 v109,s[16:19],s76 offen offset:3072 lds\n"
"v_mfma_f32_16x16x32_bf16 a[180:183],v[180:183],v[148:151],a[180:183]\n"
"s_and_b32 s96,s88,4\n"
"s_add_u32 s96,s96,s97\n"
"s_add_u32 s95,s91,s96\n"
"v_mfma_f32_16x16x32_bf16 a[184:187],v[184:187],v[148:151],0\n"
"buffer_load_dwordx4 v[8:11],v70,s[32:35],0 offen offset:0\n"
"v_mfma_f32_16x16x32_bf16 a[184:187],v[248:251],v[212:215],a[184:187]\n"
"buffer_load_dwordx4 v[12:15],v70,s[32:35],0 offen offset:512\n"
"s_and_b32 s95,s95,7\n"
"v_mfma_f32_16x16x32_bf16 a[188:191],v[252:255],v[212:215],0\n"
"buffer_load_dwordx4 v[16:19],v70,s[32:35],0 offen offset:1024\n"
"buffer_load_dwordx4 v[20:23],v70,s[32:35],0 offen offset:1536\n"
"s_lshl3_add_u32 vcc_lo,s94,s96\n"
"v_mfma_f32_16x16x32_bf16 a[188:191],v[188:191],v[148:151],a[188:191]\n"
"s_waitcnt vmcnt(28)\n"
"s_barrier\n"
"v_mfma_f32_16x16x32_bf16 a[192:195],v[224:227],v[216:219],a[192:195]\n"
"ds_read_b128 v[152:155],v80 offset:45056\n"
"s_mov_b32 s87,524288\n"
"v_mfma_f32_16x16x32_bf16 a[224:227],v[224:227],v[220:223],a[224:227]\n"
"ds_read_b128 v[156:159],v80 offset:47104\n"
"buffer_load_dword v115,v112,s[36:39],s87 offen\n"
"v_mfma_f32_16x16x32_bf16 a[228:231],v[228:231],v[220:223],a[228:231]\n"
"ds_read_b64_tr_b16 v[160:161],v98 offset:0\n"
"s_mov_b32 s87,1048576\n"
"v_mfma_f32_16x16x32_bf16 a[196:199],v[228:231],v[216:219],a[196:199]\n"
"ds_read_b64_tr_b16 v[162:163],v98 offset:1024\n"
"buffer_load_dword v115,v112,s[36:39],s87 offen\n"
"v_mfma_f32_16x16x32_bf16 a[200:203],v[232:235],v[216:219],a[200:203]\n"
"ds_read_b64_tr_b16 v[164:165],v99 offset:0\n"
"s_mov_b32 s87,1572864\n"
"v_mfma_f32_16x16x32_bf16 a[232:235],v[232:235],v[220:223],a[232:235]\n"
"ds_read_b64_tr_b16 v[166:167],v99 offset:1024\n"
"buffer_load_dword v115,v112,s[36:39],s87 offen\n"
"v_mfma_f32_16x16x32_bf16 a[236:239],v[236:239],v[220:223],a[236:239]\n"
"ds_read_b64_tr_b16 v[168:169],v100 offset:0\n"
"s_lshl3_add_u32 vcc_lo,vcc_lo,s52\n"
"v_mfma_f32_16x16x32_bf16 a[204:207],v[236:239],v[216:219],a[204:207]\n"
"ds_read_b64_tr_b16 v[170:171],v100 offset:1024\n"
"s_lshl_b32 s13,vcc_lo,15\n"
"v_mfma_f32_16x16x32_bf16 a[208:211],v[240:243],v[216:219],a[208:211]\n"
"ds_read_b64_tr_b16 v[172:173],v101 offset:0\n"
"s_lshl_b32 vcc_lo,s95,8\n"
"v_mfma_f32_16x16x32_bf16 a[240:243],v[240:243],v[220:223],a[240:243]\n"
"ds_read_b64_tr_b16 v[174:175],v101 offset:1024\n"
"s_add_u32 s13,s13,vcc_lo\n"
"v_mfma_f32_16x16x32_bf16 a[244:247],v[244:247],v[220:223],a[244:247]\n"
"ds_read_b64_tr_b16 v[176:177],v102 offset:0\n"
"s_lshl3_add_u32 vcc_hi,s94,s95\n"
"v_mfma_f32_16x16x32_bf16 a[212:215],v[244:247],v[216:219],a[212:215]\n"
"ds_read_b64_tr_b16 v[178:179],v102 offset:1024\n"
"s_lshl_b32 s72,vcc_hi,8\n"
"v_mfma_f32_16x16x32_bf16 a[216:219],v[248:251],v[216:219],a[216:219]\n"
"ds_read_b64_tr_b16 v[180:181],v103 offset:0\n"
"s_add_u32 s72,s72,149061632\n"
"v_mfma_f32_16x16x32_bf16 a[248:251],v[248:251],v[220:223],a[248:251]\n"
"s_or_b32 vcc_lo,s96,s52\n"
"ds_read_b64_tr_b16 v[182:183],v103 offset:1024\n"
"s_cselect_b32 s72,2146435072,s72\n"
"v_mfma_f32_16x16x32_bf16 a[252:255],v[252:255],v[220:223],a[252:255]\n"
"ds_read_b64_tr_b16 v[184:185],v104 offset:0\n"
"s_lshl_b32 s3,vcc_hi,18\n"
"v_mfma_f32_16x16x32_bf16 a[220:223],v[252:255],v[216:219],a[220:223]\n"
"s_lshl_b32 vcc_lo,s96,15\n"
"ds_read_b64_tr_b16 v[186:187],v104 offset:1024\n"
"s_waitcnt lgkmcnt(10)\n"
"v_mfma_f32_16x16x32_bf16 a[192:195],v[160:163],v[152:155],a[192:195]\n"
"ds_read_b64_tr_b16 v[188:189],v105 offset:0\n"
"s_sub_u32 s3,s3,vcc_lo\n"
"v_mfma_f32_16x16x32_bf16 a[224:227],v[160:163],v[156:159],a[224:227]\n"
"ds_read_b64_tr_b16 v[190:191],v105 offset:1024\n"
"s_add_u32 s3,s3,s78\n"
"v_mfma_f32_16x16x32_bf16 a[228:231],v[164:167],v[156:159],a[228:231]\n"
"ds_read_b128 v[128:131],v80 offset:32768\n"
"s_add_u32 s3,s3,100909056\n"
"v_mfma_f32_16x16x32_bf16 a[196:199],v[164:167],v[152:155],a[196:199]\n"
"ds_read_b128 v[192:195],v81 offset:32768\n"
"s_waitcnt lgkmcnt(10)\n"
"v_mfma_f32_16x16x32_bf16 a[200:203],v[168:171],v[152:155],a[200:203]\n"
"ds_read_b64_tr_b16 v[224:225],v98 offset:16384\n"
"s_lshl3_add_u32 vcc_hi,vcc_hi,s96\n"
"v_mfma_f32_16x16x32_bf16 a[232:235],v[168:171],v[156:159],a[232:235]\n"
"ds_read_b64_tr_b16 v[226:227],v98 offset:17408\n"
"s_lshl2_add_u32 vcc_hi,vcc_hi,s52\n"
"v_mfma_f32_16x16x32_bf16 a[236:239],v[172:175],v[156:159],a[236:239]\n"
"ds_read_b64_tr_b16 v[228:229],v99 offset:16384\n"
"s_lshl_b32 s2,vcc_hi,4\n"
"v_mfma_f32_16x16x32_bf16 a[204:207],v[172:175],v[152:155],a[204:207]\n"
"ds_read_b64_tr_b16 v[230:231],v99 offset:17408\n"
"s_waitcnt lgkmcnt(10)\n"
"v_mfma_f32_16x16x32_bf16 a[208:211],v[176:179],v[152:155],a[208:211]\n"
"ds_read_b64_tr_b16 v[232:233],v100 offset:16384\n"
"s_add_u32 s2,s2,149028864\n"
"v_mfma_f32_16x16x32_bf16 a[240:243],v[176:179],v[156:159],a[240:243]\n"
"ds_read_b64_tr_b16 v[234:235],v100 offset:17408\n"
"s_lshl2_add_u32 vcc_lo,s95,s52\n"
"v_mfma_f32_16x16x32_bf16 a[244:247],v[180:183],v[156:159],a[244:247]\n"
"ds_read_b64_tr_b16 v[236:237],v101 offset:16384\n"
"s_lshl_b32 vcc_lo,vcc_lo,10\n"
"v_mfma_f32_16x16x32_bf16 a[212:215],v[180:183],v[152:155],a[212:215]\n"
"ds_read_b64_tr_b16 v[238:239],v101 offset:17408\n"
"s_waitcnt lgkmcnt(10)\n"
"v_mfma_f32_16x16x32_bf16 a[216:219],v[184:187],v[152:155],a[216:219]\n"
"ds_read_b64_tr_b16 v[240:241],v102 offset:16384\n"
"s_lshl_b32 vcc_hi,s94,22\n"
"v_mfma_f32_16x16x32_bf16 a[248:251],v[184:187],v[156:159],a[248:251]\n"
"ds_read_b64_tr_b16 v[242:243],v102 offset:17408\n"
"s_add_u32 s12,vcc_lo,vcc_hi\n"
"v_mfma_f32_16x16x32_bf16 a[252:255],v[188:191],v[156:159],a[252:255]\n"
"ds_read_b64_tr_b16 v[244:245],v103 offset:16384\n"
"s_lshl_b32 vcc_lo,s96,19\n"
"v_mfma_f32_16x16x32_bf16 a[220:223],v[188:191],v[152:155],a[220:223]\n"
"ds_read_b64_tr_b16 v[246:247],v103 offset:17408\n"
"s_waitcnt lgkmcnt(8)\n"
"v_mfma_f32_16x16x32_bf16 a[0:3],v[160:163],v[128:131],a[0:3]\n"
"ds_read_b64_tr_b16 v[248:249],v104 offset:16384\n"
"s_add_u32 s10,s12,vcc_lo\n"
"v_mfma_f32_16x16x32_bf16 a[0:3],v[224:227],v[192:195],a[0:3]\n"
"ds_read_b64_tr_b16 v[250:251],v104 offset:17408\n"
"s_add_u32 s10,s10,71303168\n"
"v_mfma_f32_16x16x32_bf16 a[4:7],v[228:231],v[192:195],a[4:7]\n"
"ds_read_b64_tr_b16 v[252:253],v105 offset:16384\n"
"s_mul_i32 vcc_lo,s52,31744\n"
"v_mfma_f32_16x16x32_bf16 a[4:7],v[164:167],v[128:131],a[4:7]\n"
"ds_read_b64_tr_b16 v[254:255],v105 offset:17408\n"
"s_waitcnt lgkmcnt(8)\n"
"v_mfma_f32_16x16x32_bf16 a[8:11],v[168:171],v[128:131],a[8:11]\n"
"ds_read_b128 v[132:135],v80 offset:34816\n"
"s_add_u32 s10,s10,vcc_lo\n"
"v_mfma_f32_16x16x32_bf16 a[8:11],v[232:235],v[192:195],a[8:11]\n"
"ds_read_b128 v[196:199],v81 offset:34816\n"
"s_lshl_b32 vcc_lo,s52,10\n"
"v_mfma_f32_16x16x32_bf16 a[12:15],v[236:239],v[192:195],a[12:15]\n"
"ds_read_b128 v[136:139],v80 offset:36864\n"
"s_sub_u32 s12,57671680,vcc_lo\n"
"v_mfma_f32_16x16x32_bf16 a[12:15],v[172:175],v[128:131],a[12:15]\n"
"ds_read_b128 v[200:203],v81 offset:36864\n"
"s_waitcnt lgkmcnt(8)\n"
"v_mfma_f32_16x16x32_bf16 a[16:19],v[176:179],v[128:131],a[16:19]\n"
"ds_read_b128 v[140:143],v80 offset:38912\n"
"s_add_u32 s8,s79,54525952\n"
"v_mfma_f32_16x16x32_bf16 a[16:19],v[240:243],v[192:195],a[16:19]\n"
"ds_read_b128 v[204:207],v81 offset:38912\n"
"s_and_b32 vcc_lo,s96,1\n"
"v_mfma_f32_16x16x32_bf16 a[20:23],v[244:247],v[192:195],a[20:23]\n"
"ds_read_b128 v[144:147],v80 offset:40960\n"
"s_lshl_b32 s11,vcc_lo,10\n"
"v_mfma_f32_16x16x32_bf16 a[20:23],v[180:183],v[128:131],a[20:23]\n"
"ds_read_b128 v[208:211],v81 offset:40960\n"
"s_waitcnt lgkmcnt(8)\n"
"v_mfma_f32_16x16x32_bf16 a[24:27],v[184:187],v[128:131],a[24:27]\n"
"ds_read_b128 v[148:151],v80 offset:43008\n"
"s_add_u32 s11,s11,148932608\n"
"v_mfma_f32_16x16x32_bf16 a[24:27],v[248:251],v[192:195],a[24:27]\n"
"ds_read_b128 v[212:215],v81 offset:43008\n"
"s_mov_b32 s9,148931072\n"
"v_mfma_f32_16x16x32_bf16 a[28:31],v[252:255],v[192:195],a[28:31]\n"
"s_cmp_eq_u32 s52,0\n"
"ds_read_b128 v[216:219],v81 offset:45056\n"
"s_cselect_b32 s71,s71,2146435072\n"
"v_mfma_f32_16x16x32_bf16 a[28:31],v[188:191],v[128:131],a[28:31]\n"
"ds_read_b128 v[220:223],v81 offset:47104\n"
"s_waitcnt lgkmcnt(10)\n"
"v_mfma_f32_16x16x32_bf16 a[32:35],v[160:163],v[132:135],a[32:35]\n"
"v_mfma_f32_16x16x32_bf16 a[32:35],v[224:227],v[196:199],a[32:35]\n"
"v_mfma_f32_16x16x32_bf16 a[36:39],v[228:231],v[196:199],a[36:39]\n"
"v_mfma_f32_16x16x32_bf16 a[36:39],v[164:167],v[132:135],a[36:39]\n"
"v_mfma_f32_16x16x32_bf16 a[40:43],v[168:171],v[132:135],a[40:43]\n"
"s_waitcnt lgkmcnt(0)\n"
"s_barrier\n"
"v_mfma_f32_16x16x32_bf16 a[40:43],v[232:235],v[196:199],a[40:43]\n"
"s_mov_b32 s77,1572864\n"
"s_add_u32 m0,s78,98304\n"
"v_mfma_f32_16x16x32_bf16 a[44:47],v[236:239],v[196:199],a[44:47]\n"
"buffer_load_dwordx4 v110,s[20:23],s77 offen offset:0 lds\n"
"v_mfma_f32_16x16x32_bf16 a[44:47],v[172:175],v[132:135],a[44:47]\n"
"v_mfma_f32_16x16x32_bf16 a[48:51],v[176:179],v[132:135],a[48:51]\n"
"v_mfma_f32_16x16x32_bf16 a[48:51],v[240:243],v[196:199],a[48:51]\n"
"v_mfma_f32_16x16x32_bf16 a[52:55],v[244:247],v[196:199],a[52:55]\n"
"buffer_load_dwordx4 v111,s[20:23],s77 offen offset:1024 lds\n"
"v_mfma_f32_16x16x32_bf16 a[52:55],v[180:183],v[132:135],a[52:55]\n"
"v_mfma_f32_16x16x32_bf16 a[56:59],v[184:187],v[132:135],a[56:59]\n"
"v_mfma_f32_16x16x32_bf16 a[56:59],v[248:251],v[196:199],a[56:59]\n"
"v_mfma_f32_16x16x32_bf16 a[60:63],v[252:255],v[196:199],a[60:63]\n"
"buffer_load_dwordx4 v113,s[20:23],s77 offen offset:2048 lds\n"
"v_mfma_f32_16x16x32_bf16 a[60:63],v[188:191],v[132:135],a[60:63]\n"
"v_mfma_f32_16x16x32_bf16 a[64:67],v[160:163],v[136:139],a[64:67]\n"
"v_mfma_f32_16x16x32_bf16 a[64:67],v[224:227],v[200:203],a[64:67]\n"
"v_mfma_f32_16x16x32_bf16 a[68:71],v[228:231],v[200:203],a[68:71]\n"
"buffer_load_dwordx4 v114,s[20:23],s77 offen offset:3072 lds\n"
"v_mfma_f32_16x16x32_bf16 a[68:71],v[164:167],v[136:139],a[68:71]\n"
"v_mfma_f32_16x16x32_bf16 a[72:75],v[168:171],v[136:139],a[72:75]\n"
"v_mfma_f32_16x16x32_bf16 a[72:75],v[232:235],v[200:203],a[72:75]\n"
"s_mov_b32 s77,1835008\n"
"s_add_u32 m0,s78,114688\n"
"v_mfma_f32_16x16x32_bf16 a[76:79],v[236:239],v[200:203],a[76:79]\n"
"buffer_load_dwordx4 v110,s[20:23],s77 offen offset:0 lds\n"
"v_mfma_f32_16x16x32_bf16 a[76:79],v[172:175],v[136:139],a[76:79]\n"
"v_mfma_f32_16x16x32_bf16 a[80:83],v[176:179],v[136:139],a[80:83]\n"
"v_mfma_f32_16x16x32_bf16 a[80:83],v[240:243],v[200:203],a[80:83]\n"
"v_mfma_f32_16x16x32_bf16 a[84:87],v[244:247],v[200:203],a[84:87]\n"
"buffer_load_dwordx4 v111,s[20:23],s77 offen offset:1024 lds\n"
"v_mfma_f32_16x16x32_bf16 a[84:87],v[180:183],v[136:139],a[84:87]\n"
"v_mfma_f32_16x16x32_bf16 a[88:91],v[184:187],v[136:139],a[88:91]\n"
"v_mfma_f32_16x16x32_bf16 a[88:91],v[248:251],v[200:203],a[88:91]\n"
"v_mfma_f32_16x16x32_bf16 a[92:95],v[252:255],v[200:203],a[92:95]\n"
"buffer_load_dwordx4 v113,s[20:23],s77 offen offset:2048 lds\n"
"v_mfma_f32_16x16x32_bf16 a[92:95],v[188:191],v[136:139],a[92:95]\n"
"s_waitcnt vmcnt(10)\n"
"v_mfma_f32_16x16x32_bf16 a[96:99],v[160:163],v[140:143],a[96:99]\n"
"v_lshlrev_b32 v24,16,v8\n"
"v_and_b32 v25,0xffff0000,v8\n"
"v_mfma_f32_16x16x32_bf16 a[96:99],v[224:227],v[204:207],a[96:99]\n"
"v_lshlrev_b32 v26,16,v9\n"
"v_and_b32 v27,0xffff0000,v9\n"
"v_mfma_f32_16x16x32_bf16 a[100:103],v[228:231],v[204:207],a[100:103]\n"
"buffer_load_dwordx4 v114,s[20:23],s77 offen offset:3072 lds\n"
"v_mfma_f32_16x16x32_bf16 a[100:103],v[164:167],v[140:143],a[100:103]\n"
"v_lshlrev_b32 v28,16,v10\n"
"v_and_b32 v29,0xffff0000,v10\n"
"v_mfma_f32_16x16x32_bf16 a[104:107],v[168:171],v[140:143],a[104:107]\n"
"v_lshlrev_b32 v30,16,v11\n"
"v_and_b32 v31,0xffff0000,v11\n"
"v_mfma_f32_16x16x32_bf16 a[104:107],v[232:235],v[204:207],a[104:107]\n"
"v_lshlrev_b32 v32,16,v12\n"
"v_and_b32 v33,0xffff0000,v12\n"
"v_mfma_f32_16x16x32_bf16 a[108:111],v[236:239],v[204:207],a[108:111]\n"
"s_mov_b32 s76,512\n"
"v_lshlrev_b32 v34,16,v13\n"
"s_add_u32 m0,s79,32768\n"
"v_mfma_f32_16x16x32_bf16 a[108:111],v[172:175],v[140:143],a[108:111]\n"
"buffer_load_dwordx4 v106,s[16:19],s76 offen offset:0 lds\n"
"v_mfma_f32_16x16x32_bf16 a[112:115],v[176:179],v[140:143],a[112:115]\n"
"v_and_b32 v35,0xffff0000,v13\n"
"v_lshlrev_b32 v36,16,v14\n"
"v_mfma_f32_16x16x32_bf16 a[112:115],v[240:243],v[204:207],a[112:115]\n"
"v_and_b32 v37,0xffff0000,v14\n"
"v_lshlrev_b32 v38,16,v15\n"
"v_mfma_f32_16x16x32_bf16 a[116:119],v[244:247],v[204:207],a[116:119]\n"
"v_and_b32 v39,0xffff0000,v15\n"
"v_lshlrev_b32 v40,16,v16\n"
"v_mfma_f32_16x16x32_bf16 a[116:119],v[180:183],v[140:143],a[116:119]\n"
"v_and_b32 v41,0xffff0000,v16\n"
"v_lshlrev_b32 v42,16,v17\n"
"v_mfma_f32_16x16x32_bf16 a[120:123],v[184:187],v[140:143],a[120:123]\n"
"buffer_load_dwordx4 v107,s[16:19],s76 offen offset:1024 lds\n"
"v_mfma_f32_16x16x32_bf16 a[120:123],v[248:251],v[204:207],a[120:123]\n"
"v_and_b32 v43,0xffff0000,v17\n"
"v_lshlrev_b32 v44,16,v18\n"
"v_mfma_f32_16x16x32_bf16 a[124:127],v[252:255],v[204:207],a[124:127]\n"
"v_and_b32 v45,0xffff0000,v18\n"
"v_lshlrev_b32 v46,16,v19\n"
"v_mfma_f32_16x16x32_bf16 a[124:127],v[188:191],v[140:143],a[124:127]\n"
"v_and_b32 v47,0xffff0000,v19\n"
"v_lshlrev_b32 v48,16,v20\n"
"v_mfma_f32_16x16x32_bf16 a[128:131],v[160:163],v[144:147],a[128:131]\n"
"v_and_b32 v49,0xffff0000,v20\n"
"v_lshlrev_b32 v50,16,v21\n"
"v_mfma_f32_16x16x32_bf16 a[128:131],v[224:227],v[208:211],a[128:131]\n"
"buffer_load_dwordx4 v108,s[16:19],s76 offen offset:2048 lds\n"
"v_mfma_f32_16x16x32_bf16 a[132:135],v[228:231],v[208:211],a[132:135]\n"
"v_and_b32 v51,0xffff0000,v21\n"
"v_lshlrev_b32 v52,16,v22\n"
"v_mfma_f32_16x16x32_bf16 a[132:135],v[164:167],v[144:147],a[132:135]\n"
"v_and_b32 v53,0xffff0000,v22\n"
"v_lshlrev_b32 v54,16,v23\n"
"v_mfma_f32_16x16x32_bf16 a[136:139],v[168:171],v[144:147],a[136:139]\n"
"v_and_b32 v55,0xffff0000,v23\n"
"v_add_f32 v56,v24,v25\n"
"v_mfma_f32_16x16x32_bf16 a[136:139],v[232:235],v[208:211],a[136:139]\n"
"v_add_f32 v57,v26,v27\n"
"v_add_f32 v56,v56,v28\n"
"v_mfma_f32_16x16x32_bf16 a[140:143],v[236:239],v[208:211],a[140:143]\n"
"buffer_load_dwordx4 v109,s[16:19],s76 offen offset:3072 lds\n"
"v_mfma_f32_16x16x32_bf16 a[140:143],v[172:175],v[144:147],a[140:143]\n"
"v_add_f32 v57,v57,v29\n"
"v_add_f32 v56,v56,v30\n"
"v_mfma_f32_16x16x32_bf16 a[144:147],v[176:179],v[144:147],a[144:147]\n"
"v_add_f32 v57,v57,v31\n"
"v_add_f32 v56,v56,v32\n"
"v_mfma_f32_16x16x32_bf16 a[144:147],v[240:243],v[208:211],a[144:147]\n"
"v_readfirstlane_b32 s82,v78\n"
"v_readfirstlane_b32 s83,v79\n"
"v_mfma_f32_16x16x32_bf16 a[148:151],v[244:247],v[208:211],a[148:151]\n"
"s_mov_b32 s76,262656\n"
"v_add_f32 v57,v57,v33\n"
"s_add_u32 m0,s79,36864\n"
"v_mfma_f32_16x16x32_bf16 a[148:151],v[180:183],v[144:147],a[148:151]\n"
"buffer_load_dwordx4 v106,s[16:19],s76 offen offset:0 lds\n"
"s_mul_i32 s80,s82,0x9e3779b1\n"
"v_mfma_f32_16x16x32_bf16 a[152:155],v[184:187],v[144:147],a[152:155]\n"
"s_add_u32 s80,s80,0x7f4a7c15\n"
"v_add_f32 v56,v56,v34\n"
"v_add_f32 v57,v57,v35\n"
"v_mfma_f32_16x16x32_bf16 a[152:155],v[248:251],v[208:211],a[152:155]\n"
"s_add_u32 s81,s83,s80\n"
"s_add_u32 s81,s81,1\n"
"v_mfma_f32_16x16x32_bf16 a[156:159],v[252:255],v[208:211],a[156:159]\n"
"v_mov_b32 v124,s80\n"
"v_mov_b32 v125,s81\n"
"v_mfma_f32_16x16x32_bf16 a[156:159],v[188:191],v[144:147],a[156:159]\n"
"v_add_f32 v56,v56,v36\n"
"v_add_f32 v57,v57,v37\n"
"v_mfma_f32_16x16x32_bf16 a[160:163],v[160:163],v[148:151],a[160:163]\n"
"buffer_load_dwordx4 v107,s[16:19],s76 offen offset:1024 lds\n"
"v_mfma_f32_16x16x32_bf16 a[160:163],v[224:227],v[212:215],a[160:163]\n"
"v_add_f32 v56,v56,v38\n"
"v_add_f32 v57,v57,v39\n"
"v_mfma_f32_16x16x32_bf16 a[164:167],v[228:231],v[212:215],a[164:167]\n"
"v_add_f32 v56,v56,v40\n"
"v_add_f32 v57,v57,v41\n"
"v_mfma_f32_16x16x32_bf16 a[164:167],v[164:167],v[148:151],a[164:167]\n"
"v_add_f32 v56,v56,v42\n"
"v_add_f32 v57,v57,v43\n"
"v_mfma_f32_16x16x32_bf16 a[168:171],v[168:171],v[148:151],a[168:171]\n"
"v_add_f32 v56,v56,v44\n"
"v_add_f32 v57,v57,v45\n"
"v_mfma_f32_16x16x32_bf16 a[168:171],v[232:235],v[212:215],a[168:171]\n"
"buffer_load_dwordx4 v108,s[16:19],s76 offen offset:2048 lds\n"
"v_mfma_f32_16x16x32_bf16 a[172:175],v[236:239],v[212:215],a[172:175]\n"
"v_add_f32 v56,v56,v46\n"
"v_add_f32 v57,v57,v47\n"
"v_mfma_f32_16x16x32_bf16 a[172:175],v[172:175],v[148:151],a[172:175]\n"
"v_add_f32 v56,v56,v48\n"
"v_add_f32 v57,v57,v49\n"
"v_mfma_f32_16x16x32_bf16 a[176:179],v[176:179],v[148:151],a[176:179]\n"
"v_add_f32 v56,v56,v50\n"
"v_add_f32 v57,v57,v51\n"
"v_mfma_f32_16x16x32_bf16 a[176:179],v[240:243],v[212:215],a[176:179]\n"
"v_add_f32 v56,v56,v52\n"
"v_add_f32 v57,v57,v53\n"
"v_mfma_f32_16x16x32_bf16 a[180:183],v[244:247],v[212:215],a[180:183]\n"
"buffer_load_dwordx4 v109,s[16:19],s76 offen offset:3072 lds\n"
"v_mfma_f32_16x16x32_bf16 a[180:183],v[180:183],v[148:151],a[180:183]\n"
"s_mov_b32 s87,2097152\n"
"v_add_f32 v56,v56,v54\n"
"v_add_f32 v57,v57,v55\n"
"v_mfma_f32_16x16x32_bf16 a[184:187],v[184:187],v[148:151],a[184:187]\n"
"buffer_load_dword v115,v112,s[36:39],s87 offen\n"
"v_mfma_f32_16x16x32_bf16 a[184:187],v[248:251],v[212:215],a[184:187]\n"
"s_mov_b32 s87,2621440\n"
"v_add_f32 v56,v56,v57\n"
"v_mfma_f32_16x16x32_bf16 a[188:191],v[252:255],v[212:215],a[188:191]\n"
"buffer_load_dword v115,v112,s[36:39],s87 offen\n"
"v_add_f32_dpp v56,v56,v56 quad_perm:[1,0,3,2] row_mask:0xf bank_mask:0xf\n"
"v_mfma_f32_16x16x32_bf16 a[188:191],v[188:191],v[148:151],a[188:191]\n"
"s_barrier\n"
"v_mfma_f32_16x16x32_bf16 a[192:195],v[224:227],v[216:219],a[192:195]\n"
"ds_read_b128 v[152:155],v82 offset:12288\n"
"v_mfma_f32_16x16x32_bf16 a[224:227],v[224:227],v[220:223],a[224:227]\n"
"ds_read_b128 v[156:159],v82 offset:14336\n"
"v_add_f32_dpp v56,v56,v56 quad_perm:[2,3,0,1] row_mask:0xf bank_mask:0xf\n"
"v_mfma_f32_16x16x32_bf16 a[228:231],v[228:231],v[220:223],a[228:231]\n"
"ds_read_b64_tr_b16 v[160:161],v98 offset:49152\n"
"v_mfma_f32_16x16x32_bf16 a[196:199],v[228:231],v[216:219],a[196:199]\n"
"ds_read_b64_tr_b16 v[162:163],v98 offset:50176\n"
"v_add_f32_dpp v56,v56,v56 row_ror:8 row_mask:0xf bank_mask:0xf\n"
"v_mfma_f32_16x16x32_bf16 a[200:203],v[232:235],v[216:219],a[200:203]\n"
"ds_read_b64_tr_b16 v[164:165],v99 offset:49152\n"
"v_mov_b32 v57,v56\n"
"v_mfma_f32_16x16x32_bf16 a[232:235],v[232:235],v[220:223],a[232:235]\n"
"ds_read_b64_tr_b16 v[166:167],v99 offset:50176\n"
"v_mfma_f32_16x16x32_bf16 a[236:239],v[236:239],v[220:223],a[236:239]\n"
"ds_read_b64_tr_b16 v[168:169],v100 offset:49152\n"
"v_permlane16_swap_b32 v57,v56\n"
"v_mfma_f32_16x16x32_bf16 a[204:207],v[236:239],v[216:219],a[204:207]\n"
"ds_read_b64_tr_b16 v[170:171],v100 offset:50176\n"
"v_add_f32 v56,v56,v57\n"
"v_mfma_f32_16x16x32_bf16 a[208:211],v[240:243],v[216:219],a[208:211]\n"
"ds_read_b64_tr_b16 v[172:173],v101 offset:49152\n"
"v_mov_b32 v57,v56\n"
"v_mfma_f32_16x16x32_bf16 a[240:243],v[240:243],v[220:223],a[240:243]\n"
"ds_read_b64_tr_b16 v[174:175],v101 offset:50176\n"
"v_mfma_f32_16x16x32_bf16 a[244:247],v[244:247],v[220:223],a[244:247]\n"
"ds_read_b64_tr_b16 v[176:177],v102 offset:49152\n"
"v_permlane32_swap_b32 v57,v56\n"
"v_mfma_f32_16x16x32_bf16 a[212:215],v[244:247],v[216:219],a[212:215]\n"
"ds_read_b64_tr_b16 v[178:179],v102 offset:50176\n"
"v_add_f32 v56,v56,v57\n"
"v_mfma_f32_16x16x32_bf16 a[216:219],v[248:251],v[216:219],a[216:219]\n"
"ds_read_b64_tr_b16 v[180:181],v103 offset:49152\n"
"v_mul_f32 v58,0x3a800000,v56\n"
"v_mfma_f32_16x16x32_bf16 a[248:251],v[248:251],v[220:223],a[248:251]\n"
"ds_read_b64_tr_b16 v[182:183],v103 offset:50176\n"
"v_sub_f32 v62,v24,v58\n"
"v_mfma_f32_16x16x32_bf16 a[252:255],v[252:255],v[220:223],a[252:255]\n"
"ds_read_b64_tr_b16 v[184:185],v104 offset:49152\n"
"v_mul_f32 v59,v62,v62\n"
"v_mfma_f32_16x16x32_bf16 a[220:223],v[252:255],v[216:219],a[220:223]\n"
"ds_read_b64_tr_b16 v[186:187],v104 offset:50176\n"
"v_sub_f32 v63,v25,v58\n"
"s_waitcnt lgkmcnt(10)\n"
"v_mfma_f32_16x16x32_bf16 a[192:195],v[160:163],v[152:155],a[192:195]\n"
"ds_read_b64_tr_b16 v[188:189],v105 offset:49152\n"
"v_mul_f32 v60,v63,v63\n"
"v_mfma_f32_16x16x32_bf16 a[224:227],v[160:163],v[156:159],a[224:227]\n"
"ds_read_b64_tr_b16 v[190:191],v105 offset:50176\n"
"v_sub_f32 v64,v26,v58\n"
"v_mfma_f32_16x16x32_bf16 a[228:231],v[164:167],v[156:159],a[228:231]\n"
"ds_read_b128 v[128:131],v82 offset:0\n"
"v_fmac_f32 v59,v64,v64\n"
"v_mfma_f32_16x16x32_bf16 a[196:199],v[164:167],v[152:155],a[196:199]\n"
"ds_read_b128 v[192:195],v83 offset:0\n"
"s_waitcnt lgkmcnt(10)\n"
"v_mfma_f32_16x16x32_bf16 a[200:203],v[168:171],v[152:155],a[200:203]\n"
"ds_read_b64_tr_b16 v[224:225],v98 offset:32768\n"
"v_sub_f32 v65,v27,v58\n"
"v_mfma_f32_16x16x32_bf16 a[232:235],v[168:171],v[156:159],a[232:235]\n"
"ds_read_b64_tr_b16 v[226:227],v98 offset:33792\n"
"v_fmac_f32 v60,v65,v65\n"
"v_mfma_f32_16x16x32_bf16 a[236:239],v[172:175],v[156:159],a[236:239]\n"
"ds_read_b64_tr_b16 v[228:229],v99 offset:32768\n"
"v_sub_f32 v62,v28,v58\n"
"v_mfma_f32_16x16x32_bf16 a[204:207],v[172:175],v[152:155],a[204:207]\n"
"ds_read_b64_tr_b16 v[230:231],v99 offset:33792\n"
"s_waitcnt lgkmcnt(10)\n"
"v_mfma_f32_16x16x32_bf16 a[208:211],v[176:179],v[152:155],a[208:211]\n"
"ds_read_b64_tr_b16 v[232:233],v100 offset:32768\n"
"v_fmac_f32 v59,v62,v62\n"
"v_mfma_f32_16x16x32_bf16 a[240:243],v[176:179],v[156:159],a[240:243]\n"
"ds_read_b64_tr_b16 v[234:235],v100 offset:33792\n"
"v_sub_f32 v63,v29,v58\n"
"v_mfma_f32_16x16x32_bf16 a[244:247],v[180:183],v[156:159],a[244:247]\n"
"ds_read_b64_tr_b16 v[236:237],v101 offset:32768\n"
"v_fmac_f32 v60,v63,v63\n"
"v_mfma_f32_16x16x32_bf16 a[212:215],v[180:183],v[152:155],a[212:215]\n"
"ds_read_b64_tr_b16 v[238:239],v101 offset:33792\n"
"s_waitcnt lgkmcnt(10)\n"
"v_mfma_f32_16x16x32_bf16 a[216:219],v[184:187],v[152:155],a[216:219]\n"
"ds_read_b64_tr_b16 v[240:241],v102 offset:32768\n"
"v_sub_f32 v64,v30,v58\n"
"v_mfma_f32_16x16x32_bf16 a[248:251],v[184:187],v[156:159],a[248:251]\n"
"ds_read_b64_tr_b16 v[242:243],v102 offset:33792\n"
"v_fmac_f32 v59,v64,v64\n"
"v_mfma_f32_16x16x32_bf16 a[252:255],v[188:191],v[156:159],a[252:255]\n"
"ds_read_b64_tr_b16 v[244:245],v103 offset:32768\n"
"v_sub_f32 v65,v31,v58\n"
"v_mfma_f32_16x16x32_bf16 a[220:223],v[188:191],v[152:155],a[220:223]\n"
"ds_read_b64_tr_b16 v[246:247],v103 offset:33792\n"
"s_waitcnt lgkmcnt(8)\n"
"v_mfma_f32_16x16x32_bf16 a[0:3],v[160:163],v[128:131],a[0:3]\n"
"ds_read_b64_tr_b16 v[248:249],v104 offset:32768\n"
"v_fmac_f32 v60,v65,v65\n"
"v_mfma_f32_16x16x32_bf16 a[0:3],v[224:227],v[192:195],a[0:3]\n"
"ds_read_b64_tr_b16 v[250:251],v104 offset:33792\n"
"v_sub_f32 v62,v32,v58\n"
"v_mfma_f32_16x16x32_bf16 a[4:7],v[228:231],v[192:195],a[4:7]\n"
"ds_read_b64_tr_b16 v[252:253],v105 offset:32768\n"
"v_fmac_f32 v59,v62,v62\n"
"v_mfma_f32_16x16x32_bf16 a[4:7],v[164:167],v[128:131],a[4:7]\n"
"ds_read_b64_tr_b16 v[254:255],v105 offset:33792\n"
"s_waitcnt lgkmcnt(8)\n"
"v_mfma_f32_16x16x32_bf16 a[8:11],v[168:171],v[128:131],a[8:11]\n"
"ds_read_b128 v[132:135],v82 offset:2048\n"
"v_sub_f32 v63,v33,v58\n"
"v_mfma_f32_16x16x32_bf16 a[8:11],v[232:235],v[192:195],a[8:11]\n"
"ds_read_b128 v[196:199],v83 offset:2048\n"
"v_fmac_f32 v60,v63,v63\n"
"v_mfma_f32_16x16x32_bf16 a[12:15],v[236:239],v[192:195],a[12:15]\n"
"ds_read_b128 v[136:139],v82 offset:4096\n"
"v_sub_f32 v64,v34,v58\n"
"v_mfma_f32_16x16x32_bf16 a[12:15],v[172:175],v[128:131],a[12:15]\n"
"ds_read_b128 v[200:203],v83 offset:4096\n"
"s_waitcnt lgkmcnt(8)\n"
"v_mfma_f32_16x16x32_bf16 a[16:19],v[176:179],v[128:131],a[16:19]\n"
"ds_read_b128 v[140:143],v82 offset:6144\n"
"v_fmac_f32 v59,v64,v64\n"
"v_mfma_f32_16x16x32_bf16 a[16:19],v[240:243],v[192:195],a[16:19]\n"
"ds_read_b128 v[204:207],v83 offset:6144\n"
"v_sub_f32 v65,v35,v58\n"
"v_mfma_f32_16x16x32_bf16 a[20:23],v[244:247],v[192:195],a[20:23]\n"
"ds_read_b128 v[144:147],v82 offset:8192\n"
"v_fmac_f32 v60,v65,v65\n"
"v_mfma_f32_16x16x32_bf16 a[20:23],v[180:183],v[128:131],a[20:23]\n"
"ds_read_b128 v[208:211],v83 offset:8192\n"
"s_waitcnt lgkmcnt(8)\n"
"v_mfma_f32_16x16x32_bf16 a[24:27],v[184:187],v[128:131],a[24:27]\n"
"ds_read_b128 v[148:151],v82 offset:10240\n"
"v_sub_f32 v62,v36,v58\n"
"v_mfma_f32_16x16x32_bf16 a[24:27],v[248:251],v[192:195],a[24:27]\n"
"ds_read_b128 v[212:215],v83 offset:10240\n"
"v_fmac_f32 v59,v62,v62\n"
"v_mfma_f32_16x16x32_bf16 a[28:31],v[252:255],v[192:195],a[28:31]\n"
"ds_read_b128 v[216:219],v83 offset:12288\n"
"v_sub_f32 v63,v37,v58\n"
"v_mfma_f32_16x16x32_bf16 a[28:31],v[188:191],v[128:131],a[28:31]\n"
"ds_read_b128 v[220:223],v83 offset:14336\n"
"s_waitcnt lgkmcnt(10)\n"
"v_mfma_f32_16x16x32_bf16 a[32:35],v[160:163],v[132:135],a[32:35]\n"
"v_fmac_f32 v60,v63,v63\n"
"v_sub_f32 v64,v38,v58\n"
"v_mfma_f32_16x16x32_bf16 a[32:35],v[224:227],v[196:199],a[32:35]\n"
"v_fmac_f32 v59,v64,v64\n"
"v_sub_f32 v65,v39,v58\n"
"v_mfma_f32_16x16x32_bf16 a[36:39],v[228:231],v[196:199],a[36:39]\n"
"v_fmac_f32 v60,v65,v65\n"
"v_sub_f32 v62,v40,v58\n"
"v_mfma_f32_16x16x32_bf16 a[36:39],v[164:167],v[132:135],a[36:39]\n"
"v_fmac_f32 v59,v62,v62\n"
"v_sub_f32 v63,v41,v58\n"
"v_mfma_f32_16x16x32_bf16 a[40:43],v[168:171],v[132:135],a[40:43]\n"
"s_waitcnt lgkmcnt(0)\n"
"s_barrier\n"
"v_mfma_f32_16x16x32_bf16 a[40:43],v[232:235],v[196:199],a[40:43]\n"
"s_mov_b32 s77,2097152\n"
"v_fmac_f32 v60,v63,v63\n"
"s_add_u32 m0,s78,147456\n"
"v_mfma_f32_16x16x32_bf16 a[44:47],v[236:239],v[196:199],a[44:47]\n"
"buffer_load_dwordx4 v110,s[20:23],s77 offen offset:0 lds\n"
"v_mfma_f32_16x16x32_bf16 a[44:47],v[172:175],v[132:135],a[44:47]\n"
"v_sub_f32 v64,v42,v58\n"
"v_fmac_f32 v59,v64,v64\n"
"v_mfma_f32_16x16x32_bf16 a[48:51],v[176:179],v[132:135],a[48:51]\n"
"v_sub_f32 v65,v43,v58\n"
"v_fmac_f32 v60,v65,v65\n"
"v_mfma_f32_16x16x32_bf16 a[48:51],v[240:243],v[196:199],a[48:51]\n"
"v_sub_f32 v62,v44,v58\n"
"v_fmac_f32 v59,v62,v62\n"
"v_mfma_f32_16x16x32_bf16 a[52:55],v[244:247],v[196:199],a[52:55]\n"
"buffer_load_dwordx4 v111,s[20:23],s77 offen offset:1024 lds\n"
"v_mfma_f32_16x16x32_bf16 a[52:55],v[180:183],v[132:135],a[52:55]\n"
"v_sub_f32 v63,v45,v58\n"
"v_fmac_f32 v60,v63,v63\n"
"v_mfma_f32_16x16x32_bf16 a[56:59],v[184:187],v[132:135],a[56:59]\n"
"v_sub_f32 v64,v46,v58\n"
"v_fmac_f32 v59,v64,v64\n"
"v_mfma_f32_16x16x32_bf16 a[56:59],v[248:251],v[196:199],a[56:59]\n"
"v_sub_f32 v65,v47,v58\n"
"v_fmac_f32 v60,v65,v65\n"
"v_mfma_f32_16x16x32_bf16 a[60:63],v[252:255],v[196:199],a[60:63]\n"
"buffer_load_dwordx4 v113,s[20:23],s77 offen offset:2048 lds\n"
"v_mfma_f32_16x16x32_bf16 a[60:63],v[188:191],v[132:135],a[60:63]\n"
"v_sub_f32 v62,v48,v58\n"
"v_fmac_f32 v59,v62,v62\n"
"v_mfma_f32_16x16x32_bf16 a[64:67],v[160:163],v[136:139],a[64:67]\n"
"v_sub_f32 v63,v49,v58\n"
"v_fmac_f32 v60,v63,v63\n"
"v_mfma_f32_16x16x32_bf16 a[64:67],v[224:227],v[200:203],a[64:67]\n"
"v_sub_f32 v64,v50,v58\n"
"v_fmac_f32 v59,v64,v64\n"
"v_mfma_f32_16x16x32_bf16 a[68:71],v[228:231],v[200:203],a[68:71]\n"
"buffer_load_dwordx4 v114,s[20:23],s77 offen offset:3072 lds\n"
"v_mfma_f32_16x16x32_bf16 a[68:71],v[164:167],v[136:139],a[68:71]\n"
"v_sub_f32 v65,v51,v58\n"
"v_fmac_f32 v60,v65,v65\n"
"v_mfma_f32_16x16x32_bf16 a[72:75],v[168:171],v[136:139],a[72:75]\n"
"v_sub_f32 v62,v52,v58\n"
"v_fmac_f32 v59,v62,v62\n"
"v_mfma_f32_16x16x32_bf16 a[72:75],v[232:235],v[200:203],a[72:75]\n"
"s_mov_b32 s77,2359296\n"
"v_sub_f32 v63,v53,v58\n"
"s_add_u32 m0,s78,131072\n"
"v_mfma_f32_16x16x32_bf16 a[76:79],v[236:239],v[200:203],a[76:79]\n"
"buffer_load_dwordx4 v110,s[20:23],s77 offen offset:0 lds\n"
"v_mfma_f32_16x16x32_bf16 a[76:79],v[172:175],v[136:139],a[76:79]\n"
"v_fmac_f32 v60,v63,v63\n"
"v_sub_f32 v64,v54,v58\n"
"v_mfma_f32_16x16x32_bf16 a[80:83],v[176:179],v[136:139],a[80:83]\n"
"v_fmac_f32 v59,v64,v64\n"
"v_sub_f32 v65,v55,v58\n"
"v_mfma_f32_16x16x32_bf16 a[80:83],v[240:243],v[200:203],a[80:83]\n"
"v_fmac_f32 v60,v65,v65\n"
"v_add_f32 v59,v59,v60\n"
"v_mfma_f32_16x16x32_bf16 a[84:87],v[244:247],v[200:203],a[84:87]\n"
"buffer_load_dwordx4 v111,s[20:23],s77 offen offset:1024 lds\n"
"v_mfma_f32_16x16x32_bf16 a[84:87],v[180:183],v[136:139],a[84:87]\n"
"v_mfma_f32_16x16x32_bf16 a[88:91],v[184:187],v[136:139],a[88:91]\n"
"v_add_f32_dpp v59,v59,v59 quad_perm:[1,0,3,2] row_mask:0xf bank_mask:0xf\n"
"v_mfma_f32_16x16x32_bf16 a[88:91],v[248:251],v[200:203],a[88:91]\n"
"s_nop 0\n"
"v_add_f32_dpp v59,v59,v59 quad_perm:[2,3,0,1] row_mask:0xf bank_mask:0xf\n"
"v_mfma_f32_16x16x32_bf16 a[92:95],v[252:255],v[200:203],a[92:95]\n"
"buffer_load_dwordx4 v113,s[20:23],s77 offen offset:2048 lds\n"
"v_mfma_f32_16x16x32_bf16 a[92:95],v[188:191],v[136:139],a[92:95]\n"
"v_add_f32_dpp v59,v59,v59 row_ror:8 row_mask:0xf bank_mask:0xf\n"
"v_mov_b32 v57,v59\n"
"v_mfma_f32_16x16x32_bf16 a[96:99],v[160:163],v[140:143],a[96:99]\n"
"v_mfma_f32_16x16x32_bf16 a[96:99],v[224:227],v[204:207],a[96:99]\n"
"v_permlane16_swap_b32 v57,v59\n"
"v_add_f32 v59,v59,v57\n"
"v_mfma_f32_16x16x32_bf16 a[100:103],v[228:231],v[204:207],a[100:103]\n"
"buffer_load_dwordx4 v114,s[20:23],s77 offen offset:3072 lds\n"
"v_mfma_f32_16x16x32_bf16 a[100:103],v[164:167],v[140:143],a[100:103]\n"
"v_mov_b32 v57,v59\n"
"v_mfma_f32_16x16x32_bf16 a[104:107],v[168:171],v[140:143],a[104:107]\n"
"s_nop 0\n"
"v_permlane32_swap_b32 v57,v59\n"
"v_add_f32 v59,v59,v57\n"
"v_mfma_f32_16x16x32_bf16 a[104:107],v[232:235],v[204:207],a[104:107]\n"
"v_mul_f32 v59,0x3a800000,v59\n"
"v_cvt_pk_bf16_f32 v59,v59,v59\n"
"v_mfma_f32_16x16x32_bf16 a[108:111],v[236:239],v[204:207],a[108:111]\n"
"s_mov_b32 s76,640\n"
"v_lshlrev_b32 v59,16,v59\n"
"s_add_u32 m0,s79,65536\n"
"v_mfma_f32_16x16x32_bf16 a[108:111],v[172:175],v[140:143],a[108:111]\n"
"buffer_load_dwordx4 v106,s[16:19],s76 offen offset:0 lds\n"
"v_mfma_f32_16x16x32_bf16 a[112:115],v[176:179],v[140:143],a[112:115]\n"
"v_add_f32 v59,0x3727c5ac,v59\n"
"v_cvt_pk_bf16_f32 v59,v59,v59\n"
"v_mfma_f32_16x16x32_bf16 a[112:115],v[240:243],v[204:207],a[112:115]\n"
"v_lshlrev_b32 v59,16,v59\n"
"v_sqrt_f32 v59,v59\n"
"v_mfma_f32_16x16x32_bf16 a[116:119],v[244:247],v[204:207],a[116:119]\n"
"v_cvt_pk_bf16_f32 v58,v58,v58\n"
"v_lshlrev_b32 v58,16,v58\n"
"v_mfma_f32_16x16x32_bf16 a[116:119],v[180:183],v[140:143],a[116:119]\n"
"v_cvt_pk_bf16_f32 v59,v59,v59\n"
"v_lshlrev_b32 v59,16,v59\n"
"v_mfma_f32_16x16x32_bf16 a[120:123],v[184:187],v[140:143],a[120:123]\n"
"buffer_load_dwordx4 v107,s[16:19],s76 offen offset:1024 lds\n"
"v_mfma_f32_16x16x32_bf16 a[120:123],v[248:251],v[204:207],a[120:123]\n"
"v_rcp_f32 v66,v59\n"
"v_mfma_f32_16x16x32_bf16 a[124:127],v[252:255],v[204:207],a[124:127]\n"
"v_mul_f32 v66,0x3e0293ee,v66\n"
"v_mul_f32 v58,v58,v66\n"
"v_mfma_f32_16x16x32_bf16 a[124:127],v[188:191],v[140:143],a[124:127]\n"
"v_fma_f32 v24,v24,v66,-v58\n"
"v_fma_f32 v25,v25,v66,-v58\n"
"v_mfma_f32_16x16x32_bf16 a[128:131],v[160:163],v[144:147],a[128:131]\n"
"v_cvt_pk_bf16_f32 v8,v24,v25\n"
"v_fma_f32 v26,v26,v66,-v58\n"
"v_mfma_f32_16x16x32_bf16 a[128:131],v[224:227],v[208:211],a[128:131]\n"
"buffer_load_dwordx4 v108,s[16:19],s76 offen offset:2048 lds\n"
"v_mfma_f32_16x16x32_bf16 a[132:135],v[228:231],v[208:211],a[132:135]\n"
"v_fma_f32 v27,v27,v66,-v58\n"
"v_cvt_pk_bf16_f32 v9,v26,v27\n"
"v_mfma_f32_16x16x32_bf16 a[132:135],v[164:167],v[144:147],a[132:135]\n"
"v_fma_f32 v28,v28,v66,-v58\n"
"v_fma_f32 v29,v29,v66,-v58\n"
"v_mfma_f32_16x16x32_bf16 a[136:139],v[168:171],v[144:147],a[136:139]\n"
"v_cvt_pk_bf16_f32 v10,v28,v29\n"
"v_fma_f32 v30,v30,v66,-v58\n"
"v_mfma_f32_16x16x32_bf16 a[136:139],v[232:235],v[208:211],a[136:139]\n"
"v_fma_f32 v31,v31,v66,-v58\n"
"v_cvt_pk_bf16_f32 v11,v30,v31\n"
"v_mfma_f32_16x16x32_bf16 a[140:143],v[236:239],v[208:211],a[140:143]\n"
"buffer_load_dwordx4 v109,s[16:19],s76 offen offset:3072 lds\n"
"v_mfma_f32_16x16x32_bf16 a[140:143],v[172:175],v[144:147],a[140:143]\n"
"buffer_store_dwordx4 v[8:11],v71,s[24:27],s54 offen sc1\n"
"v_mfma_f32_16x16x32_bf16 a[144:147],v[176:179],v[144:147],a[144:147]\n"
"s_add_u32 s54,s54,131072\n"
"v_fma_f32 v32,v32,v66,-v58\n"
"v_fma_f32 v33,v33,v66,-v58\n"
"v_mfma_f32_16x16x32_bf16 a[144:147],v[240:243],v[208:211],a[144:147]\n"
"v_cvt_pk_bf16_f32 v12,v32,v33\n"
"v_fma_f32 v34,v34,v66,-v58\n"
"v_mfma_f32_16x16x32_bf16 a[148:151],v[244:247],v[208:211],a[148:151]\n"
"s_mov_b32 s76,262784\n"
"v_fma_f32 v35,v35,v66,-v58\n"
"s_add_u32 m0,s79,69632\n"
"v_mfma_f32_16x16x32_bf16 a[148:151],v[180:183],v[144:147],a[148:151]\n"
"buffer_load_dwordx4 v106,s[16:19],s76 offen offset:0 lds\n"
"v_mfma_f32_16x16x32_bf16 a[152:155],v[184:187],v[144:147],a[152:155]\n"
"v_cvt_pk_bf16_f32 v13,v34,v35\n"
"v_fma_f32 v36,v36,v66,-v58\n"
"v_mfma_f32_16x16x32_bf16 a[152:155],v[248:251],v[208:211],a[152:155]\n"
"v_fma_f32 v37,v37,v66,-v58\n"
"v_cvt_pk_bf16_f32 v14,v36,v37\n"
"v_mfma_f32_16x16x32_bf16 a[156:159],v[252:255],v[208:211],a[156:159]\n"
"v_fma_f32 v38,v38,v66,-v58\n"
"v_fma_f32 v39,v39,v66,-v58\n"
"v_cvt_pk_bf16_f32 v15,v38,v39\n"
"v_mfma_f32_16x16x32_bf16 a[156:159],v[188:191],v[144:147],a[156:159]\n"
"buffer_store_dwordx4 v[12:15],v71,s[24:27],s54 offen sc1\n"
"v_mfma_f32_16x16x32_bf16 a[160:163],v[160:163],v[148:151],a[160:163]\n"
"buffer_load_dwordx4 v107,s[16:19],s76 offen offset:1024 lds\n"
"v_mfma_f32_16x16x32_bf16 a[160:163],v[224:227],v[212:215],a[160:163]\n"
"s_add_u32 s54,s54,131072\n"
"v_fma_f32 v40,v40,v66,-v58\n"
"v_mfma_f32_16x16x32_bf16 a[164:167],v[228:231],v[212:215],a[164:167]\n"
"v_fma_f32 v41,v41,v66,-v58\n"
"v_cvt_pk_bf16_f32 v16,v40,v41\n"
"v_mfma_f32_16x16x32_bf16 a[164:167],v[164:167],v[148:151],a[164:167]\n"
"v_fma_f32 v42,v42,v66,-v58\n"
"v_fma_f32 v43,v43,v66,-v58\n"
"v_mfma_f32_16x16x32_bf16 a[168:171],v[168:171],v[148:151],a[168:171]\n"
"v_cvt_pk_bf16_f32 v17,v42,v43\n"
"v_fma_f32 v44,v44,v66,-v58\n"
"v_mfma_f32_16x16x32_bf16 a[168:171],v[232:235],v[212:215],a[168:171]\n"
"buffer_load_dwordx4 v108,s[16:19],s76 offen offset:2048 lds\n"
"v_mfma_f32_16x16x32_bf16 a[172:175],v[236:239],v[212:215],a[172:175]\n"
"v_fma_f32 v45,v45,v66,-v58\n"
"v_cvt_pk_bf16_f32 v18,v44,v45\n"
"v_mfma_f32_16x16x32_bf16 a[172:175],v[172:175],v[148:151],a[172:175]\n"
"v_fma_f32 v46,v46,v66,-v58\n"
"v_fma_f32 v47,v47,v66,-v58\n"
"v_cvt_pk_bf16_f32 v19,v46,v47\n"
"v_mfma_f32_16x16x32_bf16 a[176:179],v[176:179],v[148:151],a[176:179]\n"
"buffer_store_dwordx4 v[16:19],v71,s[24:27],s54 offen sc1\n"
"v_mfma_f32_16x16x32_bf16 a[176:179],v[240:243],v[212:215],a[176:179]\n"
"s_add_u32 s54,s54,131072\n"
"v_fma_f32 v48,v48,v66,-v58\n"
"v_mfma_f32_16x16x32_bf16 a[180:183],v[244:247],v[212:215],a[180:183]\n"
"buffer_load_dwordx4 v109,s[16:19],s76 offen offset:3072 lds\n"
"v_mfma_f32_16x16x32_bf16 a[180:183],v[180:183],v[148:151],a[180:183]\n"
"s_mov_b32 s87,0\n"
"v_fma_f32 v49,v49,v66,-v58\n"
"v_cvt_pk_bf16_f32 v20,v48,v49\n"
"v_mfma_f32_16x16x32_bf16 a[184:187],v[184:187],v[148:151],a[184:187]\n"
"buffer_load_dword v115,v112,s[40:43],s87 offen\n"
"v_mfma_f32_16x16x32_bf16 a[184:187],v[248:251],v[212:215],a[184:187]\n"
"s_mov_b32 s87,1048576\n"
"v_fma_f32 v50,v50,v66,-v58\n"
"v_fma_f32 v51,v51,v66,-v58\n"
"v_mfma_f32_16x16x32_bf16 a[188:191],v[252:255],v[212:215],a[188:191]\n"
"buffer_load_dword v115,v112,s[40:43],s87 offen\n"
"v_mfma_f32_16x16x32_bf16 a[188:191],v[188:191],v[148:151],a[188:191]\n"
"s_mov_b32 s87,2097152\n"
"s_waitcnt vmcnt(31)\n"
"s_barrier\n"
"v_mfma_f32_16x16x32_bf16 a[192:195],v[224:227],v[216:219],a[192:195]\n"
"ds_read_b128 v[152:155],v80 offset:12288\n"
"buffer_load_dword v115,v112,s[40:43],s87 offen\n"
"v_mfma_f32_16x16x32_bf16 a[224:227],v[224:227],v[220:223],a[224:227]\n"
"ds_read_b128 v[156:159],v80 offset:14336\n"
"v_cvt_pk_bf16_f32 v21,v50,v51\n"
"v_mfma_f32_16x16x32_bf16 a[228:231],v[228:231],v[220:223],a[228:231]\n"
"ds_read_b64_tr_b16 v[160:161],v98 offset:0\n"
"v_fma_f32 v52,v52,v66,-v58\n"
"v_mfma_f32_16x16x32_bf16 a[196:199],v[228:231],v[216:219],a[196:199]\n"
"ds_read_b64_tr_b16 v[162:163],v98 offset:1024\n"
"v_fma_f32 v53,v53,v66,-v58\n"
"v_mfma_f32_16x16x32_bf16 a[200:203],v[232:235],v[216:219],a[200:203]\n"
"ds_read_b64_tr_b16 v[164:165],v99 offset:0\n"
"v_cvt_pk_bf16_f32 v22,v52,v53\n"
"v_mfma_f32_16x16x32_bf16 a[232:235],v[232:235],v[220:223],a[232:235]\n"
"ds_read_b64_tr_b16 v[166:167],v99 offset:1024\n"
"v_fma_f32 v54,v54,v66,-v58\n"
"v_mfma_f32_16x16x32_bf16 a[236:239],v[236:239],v[220:223],a[236:239]\n"
"ds_read_b64_tr_b16 v[168:169],v100 offset:0\n"
"v_fma_f32 v55,v55,v66,-v58\n"
"v_mfma_f32_16x16x32_bf16 a[204:207],v[236:239],v[216:219],a[204:207]\n"
"ds_read_b64_tr_b16 v[170:171],v100 offset:1024\n"
"v_cvt_pk_bf16_f32 v23,v54,v55\n"
"v_mfma_f32_16x16x32_bf16 a[208:211],v[240:243],v[216:219],a[208:211]\n"
"ds_read_b64_tr_b16 v[172:173],v101 offset:0\n"
"buffer_store_dwordx4 v[20:23],v71,s[24:27],s54 offen sc1\n"
"v_mfma_f32_16x16x32_bf16 a[240:243],v[240:243],v[220:223],a[240:243]\n"
"ds_read_b64_tr_b16 v[174:175],v101 offset:1024\n"
"v_mfma_f32_16x16x32_bf16 a[244:247],v[244:247],v[220:223],a[244:247]\n"
"ds_read_b64_tr_b16 v[176:177],v102 offset:0\n"
"v_mfma_f32_16x16x32_bf16 a[212:215],v[244:247],v[216:219],a[212:215]\n"
"ds_read_b64_tr_b16 v[178:179],v102 offset:1024\n"
"v_mfma_f32_16x16x32_bf16 a[216:219],v[248:251],v[216:219],a[216:219]\n"
"ds_read_b64_tr_b16 v[180:181],v103 offset:0\n"
"v_mfma_f32_16x16x32_bf16 a[248:251],v[248:251],v[220:223],a[248:251]\n"
"ds_read_b64_tr_b16 v[182:183],v103 offset:1024\n"
"v_mfma_f32_16x16x32_bf16 a[252:255],v[252:255],v[220:223],a[252:255]\n"
"ds_read_b64_tr_b16 v[184:185],v104 offset:0\n"
"v_mfma_f32_16x16x32_bf16 a[220:223],v[252:255],v[216:219],a[220:223]\n"
"ds_read_b64_tr_b16 v[186:187],v104 offset:1024\n"
"s_waitcnt lgkmcnt(10)\n"
"v_mfma_f32_16x16x32_bf16 a[192:195],v[160:163],v[152:155],a[192:195]\n"
"ds_read_b64_tr_b16 v[188:189],v105 offset:0\n"
"v_mfma_f32_16x16x32_bf16 a[224:227],v[160:163],v[156:159],a[224:227]\n"
"ds_read_b64_tr_b16 v[190:191],v105 offset:1024\n"
"v_mfma_f32_16x16x32_bf16 a[228:231],v[164:167],v[156:159],a[228:231]\n"
"ds_read_b128 v[128:131],v80 offset:0\n"
"v_mfma_f32_16x16x32_bf16 a[196:199],v[164:167],v[152:155],a[196:199]\n"
"ds_read_b128 v[192:195],v81 offset:0\n"
"s_waitcnt lgkmcnt(10)\n"
"v_mfma_f32_16x16x32_bf16 a[200:203],v[168:171],v[152:155],a[200:203]\n"
"ds_read_b64_tr_b16 v[224:225],v98 offset:16384\n"
"v_mfma_f32_16x16x32_bf16 a[232:235],v[168:171],v[156:159],a[232:235]\n"
"ds_read_b64_tr_b16 v[226:227],v98 offset:17408\n"
"v_mfma_f32_16x16x32_bf16 a[236:239],v[172:175],v[156:159],a[236:239]\n"
"ds_read_b64_tr_b16 v[228:229],v99 offset:16384\n"
"v_mfma_f32_16x16x32_bf16 a[204:207],v[172:175],v[152:155],a[204:207]\n"
"ds_read_b64_tr_b16 v[230:231],v99 offset:17408\n"
"s_waitcnt lgkmcnt(10)\n"
"v_mfma_f32_16x16x32_bf16 a[208:211],v[176:179],v[152:155],a[208:211]\n"
"ds_read_b64_tr_b16 v[232:233],v100 offset:16384\n"
"v_mfma_f32_16x16x32_bf16 a[240:243],v[176:179],v[156:159],a[240:243]\n"
"ds_read_b64_tr_b16 v[234:235],v100 offset:17408\n"
"v_mfma_f32_16x16x32_bf16 a[244:247],v[180:183],v[156:159],a[244:247]\n"
"ds_read_b64_tr_b16 v[236:237],v101 offset:16384\n"
"v_mfma_f32_16x16x32_bf16 a[212:215],v[180:183],v[152:155],a[212:215]\n"
"ds_read_b64_tr_b16 v[238:239],v101 offset:17408\n"
"s_waitcnt lgkmcnt(10)\n"
"v_mfma_f32_16x16x32_bf16 a[216:219],v[184:187],v[152:155],a[216:219]\n"
"ds_read_b64_tr_b16 v[240:241],v102 offset:16384\n"
"v_mfma_f32_16x16x32_bf16 a[248:251],v[184:187],v[156:159],a[248:251]\n"
"ds_read_b64_tr_b16 v[242:243],v102 offset:17408\n"
"v_mfma_f32_16x16x32_bf16 a[252:255],v[188:191],v[156:159],a[252:255]\n"
"ds_read_b64_tr_b16 v[244:245],v103 offset:16384\n"
"v_mfma_f32_16x16x32_bf16 a[220:223],v[188:191],v[152:155],a[220:223]\n"
"ds_read_b64_tr_b16 v[246:247],v103 offset:17408\n"
"s_waitcnt lgkmcnt(8)\n"
"v_mfma_f32_16x16x32_bf16 a[0:3],v[160:163],v[128:131],a[0:3]\n"
"ds_read_b64_tr_b16 v[248:249],v104 offset:16384\n"
"v_mfma_f32_16x16x32_bf16 a[0:3],v[224:227],v[192:195],a[0:3]\n"
"ds_read_b64_tr_b16 v[250:251],v104 offset:17408\n"
"v_mfma_f32_16x16x32_bf16 a[4:7],v[228:231],v[192:195],a[4:7]\n"
"ds_read_b64_tr_b16 v[252:253],v105 offset:16384\n"
"v_mfma_f32_16x16x32_bf16 a[4:7],v[164:167],v[128:131],a[4:7]\n"
"ds_read_b64_tr_b16 v[254:255],v105 offset:17408\n"
"s_waitcnt lgkmcnt(8)\n"
"v_mfma_f32_16x16x32_bf16 a[8:11],v[168:171],v[128:131],a[8:11]\n"
"ds_read_b128 v[132:135],v80 offset:2048\n"
"v_mfma_f32_16x16x32_bf16 a[8:11],v[232:235],v[192:195],a[8:11]\n"
"ds_read_b128 v[196:199],v81 offset:2048\n"
"v_mfma_f32_16x16x32_bf16 a[12:15],v[236:239],v[192:195],a[12:15]\n"
"ds_read_b128 v[136:139],v80 offset:4096\n"
"v_mfma_f32_16x16x32_bf16 a[12:15],v[172:175],v[128:131],a[12:15]\n"
"ds_read_b128 v[200:203],v81 offset:4096\n"
"s_waitcnt lgkmcnt(8)\n"
"v_mfma_f32_16x16x32_bf16 a[16:19],v[176:179],v[128:131],a[16:19]\n"
"ds_read_b128 v[140:143],v80 offset:6144\n"
"v_mfma_f32_16x16x32_bf16 a[16:19],v[240:243],v[192:195],a[16:19]\n"
"ds_read_b128 v[204:207],v81 offset:6144\n"
"v_mfma_f32_16x16x32_bf16 a[20:23],v[244:247],v[192:195],a[20:23]\n"
"ds_read_b128 v[144:147],v80 offset:8192\n"
"v_mfma_f32_16x16x32_bf16 a[20:23],v[180:183],v[128:131],a[20:23]\n"
"ds_read_b128 v[208:211],v81 offset:8192\n"
"s_waitcnt lgkmcnt(8)\n"
"v_mfma_f32_16x16x32_bf16 a[24:27],v[184:187],v[128:131],a[24:27]\n"
"ds_read_b128 v[148:151],v80 offset:10240\n"
"v_mfma_f32_16x16x32_bf16 a[24:27],v[248:251],v[192:195],a[24:27]\n"
"ds_read_b128 v[212:215],v81 offset:10240\n"
"v_mfma_f32_16x16x32_bf16 a[28:31],v[252:255],v[192:195],a[28:31]\n"
"ds_read_b128 v[216:219],v81 offset:12288\n"
"v_mfma_f32_16x16x32_bf16 a[28:31],v[188:191],v[128:131],a[28:31]\n"
"ds_read_b128 v[220:223],v81 offset:14336\n"
"s_waitcnt lgkmcnt(10)\n"
"v_mfma_f32_16x16x32_bf16 a[32:35],v[160:163],v[132:135],a[32:35]\n"
"v_mfma_f32_16x16x32_bf16 a[32:35],v[224:227],v[196:199],a[32:35]\n"
"v_mfma_f32_16x16x32_bf16 a[36:39],v[228:231],v[196:199],a[36:39]\n"
"v_mfma_f32_16x16x32_bf16 a[36:39],v[164:167],v[132:135],a[36:39]\n"
"v_mfma_f32_16x16x32_bf16 a[40:43],v[168:171],v[132:135],a[40:43]\n"
"s_waitcnt lgkmcnt(0)\n"
"s_barrier\n"
"v_mfma_f32_16x16x32_bf16 a[40:43],v[232:235],v[196:199],a[40:43]\n"
"s_mov_b32 s77,2621440\n"
"s_add_u32 m0,s78,98304\n"
"v_mfma_f32_16x16x32_bf16 a[44:47],v[236:239],v[196:199],a[44:47]\n"
"buffer_load_dwordx4 v110,s[20:23],s77 offen offset:0 lds\n"
"v_mfma_f32_16x16x32_bf16 a[44:47],v[172:175],v[132:135],a[44:47]\n"
"v_mfma_f32_16x16x32_bf16 a[48:51],v[176:179],v[132:135],a[48:51]\n"
"v_mfma_f32_16x16x32_bf16 a[48:51],v[240:243],v[196:199],a[48:51]\n"
"v_mfma_f32_16x16x32_bf16 a[52:55],v[244:247],v[196:199],a[52:55]\n"
"buffer_load_dwordx4 v111,s[20:23],s77 offen offset:1024 lds\n"
"v_mfma_f32_16x16x32_bf16 a[52:55],v[180:183],v[132:135],a[52:55]\n"
"v_mfma_f32_16x16x32_bf16 a[56:59],v[184:187],v[132:135],a[56:59]\n"
"v_mfma_f32_16x16x32_bf16 a[56:59],v[248:251],v[196:199],a[56:59]\n"
"v_mfma_f32_16x16x32_bf16 a[60:63],v[252:255],v[196:199],a[60:63]\n"
"buffer_load_dwordx4 v113,s[20:23],s77 offen offset:2048 lds\n"
"v_mfma_f32_16x16x32_bf16 a[60:63],v[188:191],v[132:135],a[60:63]\n"
"v_mfma_f32_16x16x32_bf16 a[64:67],v[160:163],v[136:139],a[64:67]\n"
"v_mfma_f32_16x16x32_bf16 a[64:67],v[224:227],v[200:203],a[64:67]\n"
"v_mfma_f32_16x16x32_bf16 a[68:71],v[228:231],v[200:203],a[68:71]\n"
"buffer_load_dwordx4 v114,s[20:23],s77 offen offset:3072 lds\n"
"v_mfma_f32_16x16x32_bf16 a[68:71],v[164:167],v[136:139],a[68:71]\n"
"v_mfma_f32_16x16x32_bf16 a[72:75],v[168:171],v[136:139],a[72:75]\n"
"v_mfma_f32_16x16x32_bf16 a[72:75],v[232:235],v[200:203],a[72:75]\n"
"s_mov_b32 s77,2883584\n"
"s_add_u32 m0,s78,114688\n"
"v_mfma_f32_16x16x32_bf16 a[76:79],v[236:239],v[200:203],a[76:79]\n"
"buffer_load_dwordx4 v110,s[20:23],s77 offen offset:0 lds\n"
"v_mfma_f32_16x16x32_bf16 a[76:79],v[172:175],v[136:139],a[76:79]\n"
"v_mfma_f32_16x16x32_bf16 a[80:83],v[176:179],v[136:139],a[80:83]\n"
"v_mfma_f32_16x16x32_bf16 a[80:83],v[240:243],v[200:203],a[80:83]\n"
"v_mfma_f32_16x16x32_bf16 a[84:87],v[244:247],v[200:203],a[84:87]\n"
"buffer_load_dwordx4 v111,s[20:23],s77 offen offset:1024 lds\n"
"v_mfma_f32_16x16x32_bf16 a[84:87],v[180:183],v[136:139],a[84:87]\n"
"v_mfma_f32_16x16x32_bf16 a[88:91],v[184:187],v[136:139],a[88:91]\n"
"v_mfma_f32_16x16x32_bf16 a[88:91],v[248:251],v[200:203],a[88:91]\n"
"v_mfma_f32_16x16x32_bf16 a[92:95],v[252:255],v[200:203],a[92:95]\n"
"buffer_load_dwordx4 v113,s[20:23],s77 offen offset:2048 lds\n"
"v_mfma_f32_16x16x32_bf16 a[92:95],v[188:191],v[136:139],a[92:95]\n"
"v_mfma_f32_16x16x32_bf16 a[96:99],v[160:163],v[140:143],a[96:99]\n"
"v_mfma_f32_16x16x32_bf16 a[96:99],v[224:227],v[204:207],a[96:99]\n"
"v_mfma_f32_16x16x32_bf16 a[100:103],v[228:231],v[204:207],a[100:103]\n"
"buffer_load_dwordx4 v114,s[20:23],s77 offen offset:3072 lds\n"
"v_mfma_f32_16x16x32_bf16 a[100:103],v[164:167],v[140:143],a[100:103]\n"
"v_mfma_f32_16x16x32_bf16 a[104:107],v[168:171],v[140:143],a[104:107]\n"
"v_mfma_f32_16x16x32_bf16 a[104:107],v[232:235],v[204:207],a[104:107]\n"
"v_mfma_f32_16x16x32_bf16 a[108:111],v[236:239],v[204:207],a[108:111]\n"
"s_mov_b32 s76,768\n"
"s_add_u32 m0,s79,0\n"
"v_mfma_f32_16x16x32_bf16 a[108:111],v[172:175],v[140:143],a[108:111]\n"
"buffer_load_dwordx4 v106,s[16:19],s76 offen offset:0 lds\n"
"v_mfma_f32_16x16x32_bf16 a[112:115],v[176:179],v[140:143],a[112:115]\n"
"v_mfma_f32_16x16x32_bf16 a[112:115],v[240:243],v[204:207],a[112:115]\n"
"v_mfma_f32_16x16x32_bf16 a[116:119],v[244:247],v[204:207],a[116:119]\n"
"v_mfma_f32_16x16x32_bf16 a[116:119],v[180:183],v[140:143],a[116:119]\n"
"v_mfma_f32_16x16x32_bf16 a[120:123],v[184:187],v[140:143],a[120:123]\n"
"buffer_load_dwordx4 v107,s[16:19],s76 offen offset:1024 lds\n"
"v_mfma_f32_16x16x32_bf16 a[120:123],v[248:251],v[204:207],a[120:123]\n"
"v_mfma_f32_16x16x32_bf16 a[124:127],v[252:255],v[204:207],a[124:127]\n"
"v_mfma_f32_16x16x32_bf16 a[124:127],v[188:191],v[140:143],a[124:127]\n"
"v_mfma_f32_16x16x32_bf16 a[128:131],v[160:163],v[144:147],a[128:131]\n"
"v_mfma_f32_16x16x32_bf16 a[128:131],v[224:227],v[208:211],a[128:131]\n"
"buffer_load_dwordx4 v108,s[16:19],s76 offen offset:2048 lds\n"
"v_mfma_f32_16x16x32_bf16 a[132:135],v[228:231],v[208:211],a[132:135]\n"
"v_mfma_f32_16x16x32_bf16 a[132:135],v[164:167],v[144:147],a[132:135]\n"
"v_mfma_f32_16x16x32_bf16 a[136:139],v[168:171],v[144:147],a[136:139]\n"
"v_mfma_f32_16x16x32_bf16 a[136:139],v[232:235],v[208:211],a[136:139]\n"
"v_mfma_f32_16x16x32_bf16 a[140:143],v[236:239],v[208:211],a[140:143]\n"
"buffer_load_dwordx4 v109,s[16:19],s76 offen offset:3072 lds\n"
"v_mfma_f32_16x16x32_bf16 a[140:143],v[172:175],v[144:147],a[140:143]\n"
"v_mfma_f32_16x16x32_bf16 a[144:147],v[176:179],v[144:147],a[144:147]\n"
"v_mfma_f32_16x16x32_bf16 a[144:147],v[240:243],v[208:211],a[144:147]\n"
"v_mfma_f32_16x16x32_bf16 a[148:151],v[244:247],v[208:211],a[148:151]\n"
"s_mov_b32 s76,262912\n"
"s_add_u32 m0,s79,4096\n"
"v_mfma_f32_16x16x32_bf16 a[148:151],v[180:183],v[144:147],a[148:151]\n"
"buffer_load_dwordx4 v106,s[16:19],s76 offen offset:0 lds\n"
"v_mfma_f32_16x16x32_bf16 a[152:155],v[184:187],v[144:147],a[152:155]\n"
"v_mfma_f32_16x16x32_bf16 a[152:155],v[248:251],v[208:211],a[152:155]\n"
"v_mfma_f32_16x16x32_bf16 a[156:159],v[252:255],v[208:211],a[156:159]\n"
"v_mfma_f32_16x16x32_bf16 a[156:159],v[188:191],v[144:147],a[156:159]\n"
"v_mfma_f32_16x16x32_bf16 a[160:163],v[160:163],v[148:151],a[160:163]\n"
"buffer_load_dwordx4 v107,s[16:19],s76 offen offset:1024 lds\n"
"v_mfma_f32_16x16x32_bf16 a[160:163],v[224:227],v[212:215],a[160:163]\n"
"v_mfma_f32_16x16x32_bf16 a[164:167],v[228:231],v[212:215],a[164:167]\n"
"v_mfma_f32_16x16x32_bf16 a[164:167],v[164:167],v[148:151],a[164:167]\n"
"v_mfma_f32_16x16x32_bf16 a[168:171],v[168:171],v[148:151],a[168:171]\n"
"v_mfma_f32_16x16x32_bf16 a[168:171],v[232:235],v[212:215],a[168:171]\n"
"buffer_load_dwordx4 v108,s[16:19],s76 offen offset:2048 lds\n"
"v_mfma_f32_16x16x32_bf16 a[172:175],v[236:239],v[212:215],a[172:175]\n"
"v_mfma_f32_16x16x32_bf16 a[172:175],v[172:175],v[148:151],a[172:175]\n"
"v_mfma_f32_16x16x32_bf16 a[176:179],v[176:179],v[148:151],a[176:179]\n"
"v_mfma_f32_16x16x32_bf16 a[176:179],v[240:243],v[212:215],a[176:179]\n"
"v_mfma_f32_16x16x32_bf16 a[180:183],v[244:247],v[212:215],a[180:183]\n"
"buffer_load_dwordx4 v109,s[16:19],s76 offen offset:3072 lds\n"
"v_mfma_f32_16x16x32_bf16 a[180:183],v[180:183],v[148:151],a[180:183]\n"
"s_mov_b32 s87,3145728\n"
"v_mfma_f32_16x16x32_bf16 a[184:187],v[184:187],v[148:151],a[184:187]\n"
"buffer_load_dword v115,v112,s[40:43],s87 offen\n"
"v_mfma_f32_16x16x32_bf16 a[184:187],v[248:251],v[212:215],a[184:187]\n"
"s_mov_b32 s87,4194304\n"
"v_mfma_f32_16x16x32_bf16 a[188:191],v[252:255],v[212:215],a[188:191]\n"
"buffer_load_dword v115,v112,s[40:43],s87 offen\n"
"v_mfma_f32_16x16x32_bf16 a[188:191],v[188:191],v[148:151],a[188:191]\n"
"s_mov_b32 s87,5242880\n"
"s_waitcnt vmcnt(33)\n"
"s_barrier\n"
"v_mfma_f32_16x16x32_bf16 a[192:195],v[224:227],v[216:219],a[192:195]\n"
"ds_read_b128 v[152:155],v80 offset:45056\n"
"buffer_load_dword v115,v112,s[40:43],s87 offen\n"
"v_mfma_f32_16x16x32_bf16 a[224:227],v[224:227],v[220:223],a[224:227]\n"
"ds_read_b128 v[156:159],v80 offset:47104\n"
"v_mfma_f32_16x16x32_bf16 a[228:231],v[228:231],v[220:223],a[228:231]\n"
"ds_read_b64_tr_b16 v[160:161],v98 offset:49152\n"
"v_mfma_f32_16x16x32_bf16 a[196:199],v[228:231],v[216:219],a[196:199]\n"
"ds_read_b64_tr_b16 v[162:163],v98 offset:50176\n"
"v_mfma_f32_16x16x32_bf16 a[200:203],v[232:235],v[216:219],a[200:203]\n"
"ds_read_b64_tr_b16 v[164:165],v99 offset:49152\n"
"v_mfma_f32_16x16x32_bf16 a[232:235],v[232:235],v[220:223],a[232:235]\n"
"ds_read_b64_tr_b16 v[166:167],v99 offset:50176\n"
"v_mfma_f32_16x16x32_bf16 a[236:239],v[236:239],v[220:223],a[236:239]\n"
"ds_read_b64_tr_b16 v[168:169],v100 offset:49152\n"
"v_mfma_f32_16x16x32_bf16 a[204:207],v[236:239],v[216:219],a[204:207]\n"
"ds_read_b64_tr_b16 v[170:171],v100 offset:50176\n"
"v_mfma_f32_16x16x32_bf16 a[208:211],v[240:243],v[216:219],a[208:211]\n"
"ds_read_b64_tr_b16 v[172:173],v101 offset:49152\n"
"v_mfma_f32_16x16x32_bf16 a[240:243],v[240:243],v[220:223],a[240:243]\n"
"ds_read_b64_tr_b16 v[174:175],v101 offset:50176\n"
"v_mfma_f32_16x16x32_bf16 a[244:247],v[244:247],v[220:223],a[244:247]\n"
"ds_read_b64_tr_b16 v[176:177],v102 offset:49152\n"
"v_mfma_f32_16x16x32_bf16 a[212:215],v[244:247],v[216:219],a[212:215]\n"
"ds_read_b64_tr_b16 v[178:179],v102 offset:50176\n"
"v_mfma_f32_16x16x32_bf16 a[216:219],v[248:251],v[216:219],a[216:219]\n"
"ds_read_b64_tr_b16 v[180:181],v103 offset:49152\n"
"v_mfma_f32_16x16x32_bf16 a[248:251],v[248:251],v[220:223],a[248:251]\n"
"ds_read_b64_tr_b16 v[182:183],v103 offset:50176\n"
"v_mfma_f32_16x16x32_bf16 a[252:255],v[252:255],v[220:223],a[252:255]\n"
"ds_read_b64_tr_b16 v[184:185],v104 offset:49152\n"
"ds_read_b64_tr_b16 v[186:187],v104 offset:50176\n"
"v_mfma_f32_16x16x32_bf16 a[220:223],v[252:255],v[216:219],a[220:223]\n"
"s_waitcnt vmcnt(19)\n"
"s_waitcnt lgkmcnt(10)\n"
"v_mfma_f32_16x16x32_bf16 a[192:195],v[160:163],v[152:155],a[192:195]\n"
"ds_read_b64_tr_b16 v[188:189],v105 offset:49152\n"
"ds_read_b64_tr_b16 v[190:191],v105 offset:50176\n"
"v_mfma_f32_16x16x32_bf16 a[224:227],v[160:163],v[156:159],a[224:227]\n"
"buffer_store_dwordx2 v[124:125],off,s[24:27],s57 sc1\n"
"v_mfma_f32_16x16x32_bf16 a[228:231],v[164:167],v[156:159],a[228:231]\n"
"ds_read_b128 v[128:131],v80 offset:32768\n"
"v_mfma_f32_16x16x32_bf16 a[196:199],v[164:167],v[152:155],a[196:199]\n"
"ds_read_b128 v[192:195],v81 offset:32768\n"
"s_waitcnt lgkmcnt(10)\n"
"v_mfma_f32_16x16x32_bf16 a[200:203],v[168:171],v[152:155],a[200:203]\n"
"ds_read_b64_tr_b16 v[224:225],v98 offset:32768\n"
"v_mfma_f32_16x16x32_bf16 a[232:235],v[168:171],v[156:159],a[232:235]\n"
"ds_read_b64_tr_b16 v[226:227],v98 offset:33792\n"
"v_mfma_f32_16x16x32_bf16 a[236:239],v[172:175],v[156:159],a[236:239]\n"
"ds_read_b64_tr_b16 v[228:229],v99 offset:32768\n"
"v_mfma_f32_16x16x32_bf16 a[204:207],v[172:175],v[152:155],a[204:207]\n"
"ds_read_b64_tr_b16 v[230:231],v99 offset:33792\n"
"s_waitcnt lgkmcnt(10)\n"
"v_mfma_f32_16x16x32_bf16 a[208:211],v[176:179],v[152:155],a[208:211]\n"
"ds_read_b64_tr_b16 v[232:233],v100 offset:32768\n"
"v_mfma_f32_16x16x32_bf16 a[240:243],v[176:179],v[156:159],a[240:243]\n"
"ds_read_b64_tr_b16 v[234:235],v100 offset:33792\n"
"v_mfma_f32_16x16x32_bf16 a[244:247],v[180:183],v[156:159],a[244:247]\n"
"ds_read_b64_tr_b16 v[236:237],v101 offset:32768\n"
"v_mfma_f32_16x16x32_bf16 a[212:215],v[180:183],v[152:155],a[212:215]\n"
"ds_read_b64_tr_b16 v[238:239],v101 offset:33792\n"
"s_waitcnt lgkmcnt(10)\n"
"v_mfma_f32_16x16x32_bf16 a[216:219],v[184:187],v[152:155],a[216:219]\n"
"ds_read_b64_tr_b16 v[240:241],v102 offset:32768\n"
"v_mfma_f32_16x16x32_bf16 a[248:251],v[184:187],v[156:159],a[248:251]\n"
"ds_read_b64_tr_b16 v[242:243],v102 offset:33792\n"
"v_mfma_f32_16x16x32_bf16 a[252:255],v[188:191],v[156:159],a[252:255]\n"
"ds_read_b64_tr_b16 v[244:245],v103 offset:32768\n"
"v_mfma_f32_16x16x32_bf16 a[220:223],v[188:191],v[152:155],a[220:223]\n"
"ds_read_b64_tr_b16 v[246:247],v103 offset:33792\n"
"s_waitcnt lgkmcnt(8)\n"
"v_mfma_f32_16x16x32_bf16 a[0:3],v[160:163],v[128:131],a[0:3]\n"
"ds_read_b64_tr_b16 v[248:249],v104 offset:32768\n"
"v_mfma_f32_16x16x32_bf16 a[0:3],v[224:227],v[192:195],a[0:3]\n"
"ds_read_b64_tr_b16 v[250:251],v104 offset:33792\n"
"v_mfma_f32_16x16x32_bf16 a[4:7],v[228:231],v[192:195],a[4:7]\n"
"ds_read_b64_tr_b16 v[252:253],v105 offset:32768\n"
"v_mfma_f32_16x16x32_bf16 a[4:7],v[164:167],v[128:131],a[4:7]\n"
"ds_read_b64_tr_b16 v[254:255],v105 offset:33792\n"
"s_waitcnt lgkmcnt(8)\n"
"v_mfma_f32_16x16x32_bf16 a[8:11],v[168:171],v[128:131],a[8:11]\n"
"ds_read_b128 v[132:135],v80 offset:34816\n"
"v_mfma_f32_16x16x32_bf16 a[8:11],v[232:235],v[192:195],a[8:11]\n"
"ds_read_b128 v[196:199],v81 offset:34816\n"
"v_mfma_f32_16x16x32_bf16 a[12:15],v[236:239],v[192:195],a[12:15]\n"
"ds_read_b128 v[136:139],v80 offset:36864\n"
"v_mfma_f32_16x16x32_bf16 a[12:15],v[172:175],v[128:131],a[12:15]\n"
"ds_read_b128 v[200:203],v81 offset:36864\n"
"s_waitcnt lgkmcnt(8)\n"
"v_mfma_f32_16x16x32_bf16 a[16:19],v[176:179],v[128:131],a[16:19]\n"
"ds_read_b128 v[140:143],v80 offset:38912\n"
"v_mfma_f32_16x16x32_bf16 a[16:19],v[240:243],v[192:195],a[16:19]\n"
"ds_read_b128 v[204:207],v81 offset:38912\n"
"v_mfma_f32_16x16x32_bf16 a[20:23],v[244:247],v[192:195],a[20:23]\n"
"ds_read_b128 v[144:147],v80 offset:40960\n"
"v_mfma_f32_16x16x32_bf16 a[20:23],v[180:183],v[128:131],a[20:23]\n"
"ds_read_b128 v[208:211],v81 offset:40960\n"
"s_waitcnt lgkmcnt(8)\n"
"v_mfma_f32_16x16x32_bf16 a[24:27],v[184:187],v[128:131],a[24:27]\n"
"ds_read_b128 v[148:151],v80 offset:43008\n"
"v_mfma_f32_16x16x32_bf16 a[24:27],v[248:251],v[192:195],a[24:27]\n"
"ds_read_b128 v[212:215],v81 offset:43008\n"
"v_mfma_f32_16x16x32_bf16 a[28:31],v[252:255],v[192:195],a[28:31]\n"
"ds_read_b128 v[216:219],v81 offset:45056\n"
"v_mfma_f32_16x16x32_bf16 a[28:31],v[188:191],v[128:131],a[28:31]\n"
"ds_read_b128 v[220:223],v81 offset:47104\n"
"s_waitcnt lgkmcnt(10)\n"
"v_mfma_f32_16x16x32_bf16 a[32:35],v[160:163],v[132:135],a[32:35]\n"
"v_mfma_f32_16x16x32_bf16 a[32:35],v[224:227],v[196:199],a[32:35]\n"
"v_mfma_f32_16x16x32_bf16 a[36:39],v[228:231],v[196:199],a[36:39]\n"
"v_mfma_f32_16x16x32_bf16 a[36:39],v[164:167],v[132:135],a[36:39]\n"
"v_mfma_f32_16x16x32_bf16 a[40:43],v[168:171],v[132:135],a[40:43]\n"
"s_waitcnt lgkmcnt(0)\n"
"s_barrier\n"
"v_mfma_f32_16x16x32_bf16 a[40:43],v[232:235],v[196:199],a[40:43]\n"
"s_mov_b32 s77,3145728\n"
"s_add_u32 m0,s78,147456\n"
"v_mfma_f32_16x16x32_bf16 a[44:47],v[236:239],v[196:199],a[44:47]\n"
"buffer_load_dwordx4 v110,s[20:23],s77 offen offset:0 lds\n"
"v_mfma_f32_16x16x32_bf16 a[44:47],v[172:175],v[132:135],a[44:47]\n"
"v_mfma_f32_16x16x32_bf16 a[48:51],v[176:179],v[132:135],a[48:51]\n"
"v_mfma_f32_16x16x32_bf16 a[48:51],v[240:243],v[196:199],a[48:51]\n"
"v_mfma_f32_16x16x32_bf16 a[52:55],v[244:247],v[196:199],a[52:55]\n"
"buffer_load_dwordx4 v111,s[20:23],s77 offen offset:1024 lds\n"
"v_mfma_f32_16x16x32_bf16 a[52:55],v[180:183],v[132:135],a[52:55]\n"
"v_mfma_f32_16x16x32_bf16 a[56:59],v[184:187],v[132:135],a[56:59]\n"
"v_mfma_f32_16x16x32_bf16 a[56:59],v[248:251],v[196:199],a[56:59]\n"
"v_mfma_f32_16x16x32_bf16 a[60:63],v[252:255],v[196:199],a[60:63]\n"
"buffer_load_dwordx4 v113,s[20:23],s77 offen offset:2048 lds\n"
"v_mfma_f32_16x16x32_bf16 a[60:63],v[188:191],v[132:135],a[60:63]\n"
"v_mfma_f32_16x16x32_bf16 a[64:67],v[160:163],v[136:139],a[64:67]\n"
"v_mfma_f32_16x16x32_bf16 a[64:67],v[224:227],v[200:203],a[64:67]\n"
"v_mfma_f32_16x16x32_bf16 a[68:71],v[228:231],v[200:203],a[68:71]\n"
"buffer_load_dwordx4 v114,s[20:23],s77 offen offset:3072 lds\n"
"v_mfma_f32_16x16x32_bf16 a[68:71],v[164:167],v[136:139],a[68:71]\n"
"v_mfma_f32_16x16x32_bf16 a[72:75],v[168:171],v[136:139],a[72:75]\n"
"v_mfma_f32_16x16x32_bf16 a[72:75],v[232:235],v[200:203],a[72:75]\n"
"s_mov_b32 s77,3407872\n"
"s_add_u32 m0,s78,131072\n"
"v_mfma_f32_16x16x32_bf16 a[76:79],v[236:239],v[200:203],a[76:79]\n"
"buffer_load_dwordx4 v110,s[20:23],s77 offen offset:0 lds\n"
"v_mfma_f32_16x16x32_bf16 a[76:79],v[172:175],v[136:139],a[76:79]\n"
"v_mfma_f32_16x16x32_bf16 a[80:83],v[176:179],v[136:139],a[80:83]\n"
"v_mfma_f32_16x16x32_bf16 a[80:83],v[240:243],v[200:203],a[80:83]\n"
"v_mfma_f32_16x16x32_bf16 a[84:87],v[244:247],v[200:203],a[84:87]\n"
"buffer_load_dwordx4 v111,s[20:23],s77 offen offset:1024 lds\n"
"v_mfma_f32_16x16x32_bf16 a[84:87],v[180:183],v[136:139],a[84:87]\n"
"v_mfma_f32_16x16x32_bf16 a[88:91],v[184:187],v[136:139],a[88:91]\n"
"v_mfma_f32_16x16x32_bf16 a[88:91],v[248:251],v[200:203],a[88:91]\n"
"v_mfma_f32_16x16x32_bf16 a[92:95],v[252:255],v[200:203],a[92:95]\n"
"buffer_load_dwordx4 v113,s[20:23],s77 offen offset:2048 lds\n"
"v_mfma_f32_16x16x32_bf16 a[92:95],v[188:191],v[136:139],a[92:95]\n"
"v_mfma_f32_16x16x32_bf16 a[96:99],v[160:163],v[140:143],a[96:99]\n"
"v_mfma_f32_16x16x32_bf16 a[96:99],v[224:227],v[204:207],a[96:99]\n"
"v_mfma_f32_16x16x32_bf16 a[100:103],v[228:231],v[204:207],a[100:103]\n"
"buffer_load_dwordx4 v114,s[20:23],s77 offen offset:3072 lds\n"
"v_mfma_f32_16x16x32_bf16 a[100:103],v[164:167],v[140:143],a[100:103]\n"
"v_mfma_f32_16x16x32_bf16 a[104:107],v[168:171],v[140:143],a[104:107]\n"
"v_mfma_f32_16x16x32_bf16 a[104:107],v[232:235],v[204:207],a[104:107]\n"
"v_mfma_f32_16x16x32_bf16 a[108:111],v[236:239],v[204:207],a[108:111]\n"
"s_mov_b32 s76,896\n"
"s_add_u32 m0,s79,32768\n"
"v_mfma_f32_16x16x32_bf16 a[108:111],v[172:175],v[140:143],a[108:111]\n"
"buffer_load_dwordx4 v106,s[16:19],s76 offen offset:0 lds\n"
"v_mfma_f32_16x16x32_bf16 a[112:115],v[176:179],v[140:143],a[112:115]\n"
"v_mfma_f32_16x16x32_bf16 a[112:115],v[240:243],v[204:207],a[112:115]\n"
"v_mfma_f32_16x16x32_bf16 a[116:119],v[244:247],v[204:207],a[116:119]\n"
"v_mfma_f32_16x16x32_bf16 a[116:119],v[180:183],v[140:143],a[116:119]\n"
"v_mfma_f32_16x16x32_bf16 a[120:123],v[184:187],v[140:143],a[120:123]\n"
"buffer_load_dwordx4 v107,s[16:19],s76 offen offset:1024 lds\n"
"v_mfma_f32_16x16x32_bf16 a[120:123],v[248:251],v[204:207],a[120:123]\n"
"v_mfma_f32_16x16x32_bf16 a[124:127],v[252:255],v[204:207],a[124:127]\n"
"v_mfma_f32_16x16x32_bf16 a[124:127],v[188:191],v[140:143],a[124:127]\n"
"v_mfma_f32_16x16x32_bf16 a[128:131],v[160:163],v[144:147],a[128:131]\n"
"v_mfma_f32_16x16x32_bf16 a[128:131],v[224:227],v[208:211],a[128:131]\n"
"buffer_load_dwordx4 v108,s[16:19],s76 offen offset:2048 lds\n"
"v_mfma_f32_16x16x32_bf16 a[132:135],v[228:231],v[208:211],a[132:135]\n"
"v_mfma_f32_16x16x32_bf16 a[132:135],v[164:167],v[144:147],a[132:135]\n"
"v_mfma_f32_16x16x32_bf16 a[136:139],v[168:171],v[144:147],a[136:139]\n"
"v_mfma_f32_16x16x32_bf16 a[136:139],v[232:235],v[208:211],a[136:139]\n"
"v_mfma_f32_16x16x32_bf16 a[140:143],v[236:239],v[208:211],a[140:143]\n"
"buffer_load_dwordx4 v109,s[16:19],s76 offen offset:3072 lds\n"
"v_mfma_f32_16x16x32_bf16 a[140:143],v[172:175],v[144:147],a[140:143]\n"
"v_mfma_f32_16x16x32_bf16 a[144:147],v[176:179],v[144:147],a[144:147]\n"
"v_mfma_f32_16x16x32_bf16 a[144:147],v[240:243],v[208:211],a[144:147]\n"
"v_mfma_f32_16x16x32_bf16 a[148:151],v[244:247],v[208:211],a[148:151]\n"
"s_mov_b32 s76,263040\n"
"s_add_u32 m0,s79,36864\n"
"v_mfma_f32_16x16x32_bf16 a[148:151],v[180:183],v[144:147],a[148:151]\n"
"buffer_load_dwordx4 v106,s[16:19],s76 offen offset:0 lds\n"
"v_mfma_f32_16x16x32_bf16 a[152:155],v[184:187],v[144:147],a[152:155]\n"
"v_mfma_f32_16x16x32_bf16 a[152:155],v[248:251],v[208:211],a[152:155]\n"
"v_mfma_f32_16x16x32_bf16 a[156:159],v[252:255],v[208:211],a[156:159]\n"
"v_mfma_f32_16x16x32_bf16 a[156:159],v[188:191],v[144:147],a[156:159]\n"
"v_mfma_f32_16x16x32_bf16 a[160:163],v[160:163],v[148:151],a[160:163]\n"
"buffer_load_dwordx4 v107,s[16:19],s76 offen offset:1024 lds\n"
"v_mfma_f32_16x16x32_bf16 a[160:163],v[224:227],v[212:215],a[160:163]\n"
"v_mfma_f32_16x16x32_bf16 a[164:167],v[228:231],v[212:215],a[164:167]\n"
"v_mfma_f32_16x16x32_bf16 a[164:167],v[164:167],v[148:151],a[164:167]\n"
"v_mfma_f32_16x16x32_bf16 a[168:171],v[168:171],v[148:151],a[168:171]\n"
"v_mfma_f32_16x16x32_bf16 a[168:171],v[232:235],v[212:215],a[168:171]\n"
"buffer_load_dwordx4 v108,s[16:19],s76 offen offset:2048 lds\n"
"v_mfma_f32_16x16x32_bf16 a[172:175],v[236:239],v[212:215],a[172:175]\n"
"v_mfma_f32_16x16x32_bf16 a[172:175],v[172:175],v[148:151],a[172:175]\n"
"v_mfma_f32_16x16x32_bf16 a[176:179],v[176:179],v[148:151],a[176:179]\n"
"v_mfma_f32_16x16x32_bf16 a[176:179],v[240:243],v[212:215],a[176:179]\n"
"v_mfma_f32_16x16x32_bf16 a[180:183],v[244:247],v[212:215],a[180:183]\n"
"buffer_load_dwordx4 v109,s[16:19],s76 offen offset:3072 lds\n"
"v_mfma_f32_16x16x32_bf16 a[180:183],v[180:183],v[148:151],a[180:183]\n"
"s_mov_b32 s87,6291456\n"
"v_mfma_f32_16x16x32_bf16 a[184:187],v[184:187],v[148:151],a[184:187]\n"
"buffer_load_dword v115,v112,s[40:43],s87 offen\n"
"v_mfma_f32_16x16x32_bf16 a[184:187],v[248:251],v[212:215],a[184:187]\n"
"s_mov_b32 s87,7340032\n"
"v_mfma_f32_16x16x32_bf16 a[188:191],v[252:255],v[212:215],a[188:191]\n"
"buffer_load_dword v115,v112,s[40:43],s87 offen\n"
"v_mfma_f32_16x16x32_bf16 a[188:191],v[188:191],v[148:151],a[188:191]\n"
"s_waitcnt vmcnt(30)\n"
"s_barrier\n"
"v_mfma_f32_16x16x32_bf16 a[192:195],v[224:227],v[216:219],a[192:195]\n"
"ds_read_b128 v[152:155],v82 offset:12288\n"
"v_mfma_f32_16x16x32_bf16 a[224:227],v[224:227],v[220:223],a[224:227]\n"
"ds_read_b128 v[156:159],v82 offset:14336\n"
"v_mfma_f32_16x16x32_bf16 a[228:231],v[228:231],v[220:223],a[228:231]\n"
"ds_read_b64_tr_b16 v[160:161],v98 offset:0\n"
"v_mfma_f32_16x16x32_bf16 a[196:199],v[228:231],v[216:219],a[196:199]\n"
"ds_read_b64_tr_b16 v[162:163],v98 offset:1024\n"
"v_mfma_f32_16x16x32_bf16 a[200:203],v[232:235],v[216:219],a[200:203]\n"
"ds_read_b64_tr_b16 v[164:165],v99 offset:0\n"
"v_mfma_f32_16x16x32_bf16 a[232:235],v[232:235],v[220:223],a[232:235]\n"
"ds_read_b64_tr_b16 v[166:167],v99 offset:1024\n"
"v_mfma_f32_16x16x32_bf16 a[236:239],v[236:239],v[220:223],a[236:239]\n"
"ds_read_b64_tr_b16 v[168:169],v100 offset:0\n"
"v_mfma_f32_16x16x32_bf16 a[204:207],v[236:239],v[216:219],a[204:207]\n"
"ds_read_b64_tr_b16 v[170:171],v100 offset:1024\n"
"v_mfma_f32_16x16x32_bf16 a[208:211],v[240:243],v[216:219],a[208:211]\n"
"ds_read_b64_tr_b16 v[172:173],v101 offset:0\n"
"v_mfma_f32_16x16x32_bf16 a[240:243],v[240:243],v[220:223],a[240:243]\n"
"ds_read_b64_tr_b16 v[174:175],v101 offset:1024\n"
"v_mfma_f32_16x16x32_bf16 a[244:247],v[244:247],v[220:223],a[244:247]\n"
"ds_read_b64_tr_b16 v[176:177],v102 offset:0\n"
"v_mfma_f32_16x16x32_bf16 a[212:215],v[244:247],v[216:219],a[212:215]\n"
"ds_read_b64_tr_b16 v[178:179],v102 offset:1024\n"
"v_mfma_f32_16x16x32_bf16 a[216:219],v[248:251],v[216:219],a[216:219]\n"
"ds_read_b64_tr_b16 v[180:181],v103 offset:0\n"
"v_mfma_f32_16x16x32_bf16 a[248:251],v[248:251],v[220:223],a[248:251]\n"
"ds_read_b64_tr_b16 v[182:183],v103 offset:1024\n"
"v_mfma_f32_16x16x32_bf16 a[252:255],v[252:255],v[220:223],a[252:255]\n"
"ds_read_b64_tr_b16 v[184:185],v104 offset:0\n"
"v_mfma_f32_16x16x32_bf16 a[220:223],v[252:255],v[216:219],a[220:223]\n"
"ds_read_b64_tr_b16 v[186:187],v104 offset:1024\n"
"s_waitcnt lgkmcnt(10)\n"
"v_mfma_f32_16x16x32_bf16 a[192:195],v[160:163],v[152:155],a[192:195]\n"
"ds_read_b64_tr_b16 v[188:189],v105 offset:0\n"
"v_mfma_f32_16x16x32_bf16 a[224:227],v[160:163],v[156:159],a[224:227]\n"
"ds_read_b64_tr_b16 v[190:191],v105 offset:1024\n"
"v_mfma_f32_16x16x32_bf16 a[228:231],v[164:167],v[156:159],a[228:231]\n"
"ds_read_b128 v[128:131],v82 offset:0\n"
"v_mfma_f32_16x16x32_bf16 a[196:199],v[164:167],v[152:155],a[196:199]\n"
"ds_read_b128 v[192:195],v83 offset:0\n"
"s_waitcnt lgkmcnt(10)\n"
"v_mfma_f32_16x16x32_bf16 a[200:203],v[168:171],v[152:155],a[200:203]\n"
"ds_read_b64_tr_b16 v[224:225],v98 offset:16384\n"
"v_mfma_f32_16x16x32_bf16 a[232:235],v[168:171],v[156:159],a[232:235]\n"
"ds_read_b64_tr_b16 v[226:227],v98 offset:17408\n"
"v_mfma_f32_16x16x32_bf16 a[236:239],v[172:175],v[156:159],a[236:239]\n"
"ds_read_b64_tr_b16 v[228:229],v99 offset:16384\n"
"v_mfma_f32_16x16x32_bf16 a[204:207],v[172:175],v[152:155],a[204:207]\n"
"ds_read_b64_tr_b16 v[230:231],v99 offset:17408\n"
"s_waitcnt lgkmcnt(10)\n"
"v_mfma_f32_16x16x32_bf16 a[208:211],v[176:179],v[152:155],a[208:211]\n"
"ds_read_b64_tr_b16 v[232:233],v100 offset:16384\n"
"v_mfma_f32_16x16x32_bf16 a[240:243],v[176:179],v[156:159],a[240:243]\n"
"ds_read_b64_tr_b16 v[234:235],v100 offset:17408\n"
"v_mfma_f32_16x16x32_bf16 a[244:247],v[180:183],v[156:159],a[244:247]\n"
"ds_read_b64_tr_b16 v[236:237],v101 offset:16384\n"
"v_mfma_f32_16x16x32_bf16 a[212:215],v[180:183],v[152:155],a[212:215]\n"
"ds_read_b64_tr_b16 v[238:239],v101 offset:17408\n"
"s_waitcnt lgkmcnt(10)\n"
"v_mfma_f32_16x16x32_bf16 a[216:219],v[184:187],v[152:155],a[216:219]\n"
"ds_read_b64_tr_b16 v[240:241],v102 offset:16384\n"
"v_mfma_f32_16x16x32_bf16 a[248:251],v[184:187],v[156:159],a[248:251]\n"
"ds_read_b64_tr_b16 v[242:243],v102 offset:17408\n"
"v_mfma_f32_16x16x32_bf16 a[252:255],v[188:191],v[156:159],a[252:255]\n"
"ds_read_b64_tr_b16 v[244:245],v103 offset:16384\n"
"v_mfma_f32_16x16x32_bf16 a[220:223],v[188:191],v[152:155],a[220:223]\n"
"ds_read_b64_tr_b16 v[246:247],v103 offset:17408\n"
"s_waitcnt lgkmcnt(8)\n"
"v_mfma_f32_16x16x32_bf16 a[0:3],v[160:163],v[128:131],a[0:3]\n"
"ds_read_b64_tr_b16 v[248:249],v104 offset:16384\n"
"v_mfma_f32_16x16x32_bf16 a[0:3],v[224:227],v[192:195],a[0:3]\n"
"ds_read_b64_tr_b16 v[250:251],v104 offset:17408\n"
"v_mfma_f32_16x16x32_bf16 a[4:7],v[228:231],v[192:195],a[4:7]\n"
"ds_read_b64_tr_b16 v[252:253],v105 offset:16384\n"
"v_mfma_f32_16x16x32_bf16 a[4:7],v[164:167],v[128:131],a[4:7]\n"
"ds_read_b64_tr_b16 v[254:255],v105 offset:17408\n"
"s_waitcnt lgkmcnt(8)\n"
"v_mfma_f32_16x16x32_bf16 a[8:11],v[168:171],v[128:131],a[8:11]\n"
"ds_read_b128 v[132:135],v82 offset:2048\n"
"v_mfma_f32_16x16x32_bf16 a[8:11],v[232:235],v[192:195],a[8:11]\n"
"ds_read_b128 v[196:199],v83 offset:2048\n"
"v_mfma_f32_16x16x32_bf16 a[12:15],v[236:239],v[192:195],a[12:15]\n"
"ds_read_b128 v[136:139],v82 offset:4096\n"
"v_mfma_f32_16x16x32_bf16 a[12:15],v[172:175],v[128:131],a[12:15]\n"
"ds_read_b128 v[200:203],v83 offset:4096\n"
"s_waitcnt lgkmcnt(8)\n"
"v_mfma_f32_16x16x32_bf16 a[16:19],v[176:179],v[128:131],a[16:19]\n"
"ds_read_b128 v[140:143],v82 offset:6144\n"
"v_mfma_f32_16x16x32_bf16 a[16:19],v[240:243],v[192:195],a[16:19]\n"
"ds_read_b128 v[204:207],v83 offset:6144\n"
"v_mfma_f32_16x16x32_bf16 a[20:23],v[244:247],v[192:195],a[20:23]\n"
"ds_read_b128 v[144:147],v82 offset:8192\n"
"v_mfma_f32_16x16x32_bf16 a[20:23],v[180:183],v[128:131],a[20:23]\n"
"ds_read_b128 v[208:211],v83 offset:8192\n"
"s_waitcnt lgkmcnt(8)\n"
"v_mfma_f32_16x16x32_bf16 a[24:27],v[184:187],v[128:131],a[24:27]\n"
"ds_read_b128 v[148:151],v82 offset:10240\n"
"v_mfma_f32_16x16x32_bf16 a[24:27],v[248:251],v[192:195],a[24:27]\n"
"ds_read_b128 v[212:215],v83 offset:10240\n"
"v_mfma_f32_16x16x32_bf16 a[28:31],v[252:255],v[192:195],a[28:31]\n"
"ds_read_b128 v[216:219],v83 offset:12288\n"
"v_mfma_f32_16x16x32_bf16 a[28:31],v[188:191],v[128:131],a[28:31]\n"
"ds_read_b128 v[220:223],v83 offset:14336\n"
"s_waitcnt lgkmcnt(10)\n"
"v_mfma_f32_16x16x32_bf16 a[32:35],v[160:163],v[132:135],a[32:35]\n"
"v_mfma_f32_16x16x32_bf16 a[32:35],v[224:227],v[196:199],a[32:35]\n"
"buffer_load_dwordx4 v[74:77],v112,s[24:27],s58 offen sc1\n"
"v_mfma_f32_16x16x32_bf16 a[36:39],v[228:231],v[196:199],a[36:39]\n"
"v_mfma_f32_16x16x32_bf16 a[36:39],v[164:167],v[132:135],a[36:39]\n"
"v_mfma_f32_16x16x32_bf16 a[40:43],v[168:171],v[132:135],a[40:43]\n"
"s_waitcnt lgkmcnt(0)\n"
"s_barrier\n"
"v_mfma_f32_16x16x32_bf16 a[40:43],v[232:235],v[196:199],a[40:43]\n"
"s_mov_b32 s77,3670016\n"
"s_add_u32 m0,s78,98304\n"
"v_mfma_f32_16x16x32_bf16 a[44:47],v[236:239],v[196:199],a[44:47]\n"
"buffer_load_dwordx4 v110,s[20:23],s77 offen offset:0 lds\n"
"v_mfma_f32_16x16x32_bf16 a[44:47],v[172:175],v[132:135],a[44:47]\n"
"v_mfma_f32_16x16x32_bf16 a[48:51],v[176:179],v[132:135],a[48:51]\n"
"s_load_dwordx2 s[48:49],s[0:1],56\n"
"v_mfma_f32_16x16x32_bf16 a[48:51],v[240:243],v[196:199],a[48:51]\n"
"v_mfma_f32_16x16x32_bf16 a[52:55],v[244:247],v[196:199],a[52:55]\n"
"buffer_load_dwordx4 v111,s[20:23],s77 offen offset:1024 lds\n"
"v_mfma_f32_16x16x32_bf16 a[52:55],v[180:183],v[132:135],a[52:55]\n"
"v_mfma_f32_16x16x32_bf16 a[56:59],v[184:187],v[132:135],a[56:59]\n"
"v_mfma_f32_16x16x32_bf16 a[56:59],v[248:251],v[196:199],a[56:59]\n"
"v_mfma_f32_16x16x32_bf16 a[60:63],v[252:255],v[196:199],a[60:63]\n"
"buffer_load_dwordx4 v113,s[20:23],s77 offen offset:2048 lds\n"
"v_mfma_f32_16x16x32_bf16 a[60:63],v[188:191],v[132:135],a[60:63]\n"
"v_mfma_f32_16x16x32_bf16 a[64:67],v[160:163],v[136:139],a[64:67]\n"
"v_mfma_f32_16x16x32_bf16 a[64:67],v[224:227],v[200:203],a[64:67]\n"
"v_mfma_f32_16x16x32_bf16 a[68:71],v[228:231],v[200:203],a[68:71]\n"
"buffer_load_dwordx4 v114,s[20:23],s77 offen offset:3072 lds\n"
"v_mfma_f32_16x16x32_bf16 a[68:71],v[164:167],v[136:139],a[68:71]\n"
"v_mfma_f32_16x16x32_bf16 a[72:75],v[168:171],v[136:139],a[72:75]\n"
"v_mfma_f32_16x16x32_bf16 a[72:75],v[232:235],v[200:203],a[72:75]\n"
"s_mov_b32 s77,3932160\n"
"s_add_u32 m0,s78,114688\n"
"v_mfma_f32_16x16x32_bf16 a[76:79],v[236:239],v[200:203],a[76:79]\n"
"buffer_load_dwordx4 v110,s[20:23],s77 offen offset:0 lds\n"
"v_mfma_f32_16x16x32_bf16 a[76:79],v[172:175],v[136:139],a[76:79]\n"
"v_mfma_f32_16x16x32_bf16 a[80:83],v[176:179],v[136:139],a[80:83]\n"
"v_mfma_f32_16x16x32_bf16 a[80:83],v[240:243],v[200:203],a[80:83]\n"
"v_mfma_f32_16x16x32_bf16 a[84:87],v[244:247],v[200:203],a[84:87]\n"
"buffer_load_dwordx4 v111,s[20:23],s77 offen offset:1024 lds\n"
"v_mfma_f32_16x16x32_bf16 a[84:87],v[180:183],v[136:139],a[84:87]\n"
"v_mfma_f32_16x16x32_bf16 a[88:91],v[184:187],v[136:139],a[88:91]\n"
"v_mfma_f32_16x16x32_bf16 a[88:91],v[248:251],v[200:203],a[88:91]\n"
"v_mfma_f32_16x16x32_bf16 a[92:95],v[252:255],v[200:203],a[92:95]\n"
"buffer_load_dwordx4 v113,s[20:23],s77 offen offset:2048 lds\n"
"v_mfma_f32_16x16x32_bf16 a[92:95],v[188:191],v[136:139],a[92:95]\n"
"s_mov_b32 s76,s55\n"
"s_waitcnt vmcnt(7)\n"
"v_mfma_f32_16x16x32_bf16 a[96:99],v[160:163],v[140:143],a[96:99]\n"
"v_xor_b32 v116,s80,v74\n"
"v_xor_b32 v117,s81,v75\n"
"v_mfma_f32_16x16x32_bf16 a[96:99],v[224:227],v[204:207],a[96:99]\n"
"v_or_b32 v116,v116,v117\n"
"v_xor_b32 v117,s80,v76\n"
"v_mfma_f32_16x16x32_bf16 a[100:103],v[228:231],v[204:207],a[100:103]\n"
"buffer_load_dwordx4 v114,s[20:23],s77 offen offset:3072 lds\n"
"v_mfma_f32_16x16x32_bf16 a[100:103],v[164:167],v[140:143],a[100:103]\n"
"v_or_b32 v116,v116,v117\n"
"v_xor_b32 v117,s81,v77\n"
"v_mfma_f32_16x16x32_bf16 a[104:107],v[168:171],v[140:143],a[104:107]\n"
"v_or_b32 v116,v116,v117\n"
"v_cmp_ne_u32 vcc,0,v116\n"
"v_mfma_f32_16x16x32_bf16 a[104:107],v[232:235],v[204:207],a[104:107]\n"
"s_cbranch_vccnz .Lfk_xn0_s\n"
".Lfk_xn0_ok:\n"
"v_mfma_f32_16x16x32_bf16 a[108:111],v[236:239],v[204:207],a[108:111]\n"
"s_add_u32 m0,s78,65536\n"
"v_mfma_f32_16x16x32_bf16 a[108:111],v[172:175],v[140:143],a[108:111]\n"
"buffer_load_dwordx4 v112,s[24:27],s76 offen offset:0 sc1 lds\n"
"v_mfma_f32_16x16x32_bf16 a[112:115],v[176:179],v[140:143],a[112:115]\n"
"s_waitcnt lgkmcnt(0)\n"
"s_lshl2_add_u32 s88,s100,s52\n"
"v_mfma_f32_16x16x32_bf16 a[112:115],v[240:243],v[204:207],a[112:115]\n"
"s_mul_i32 s88,s88,7168\n"
"s_add_u32 s88,s88,0\n"
"v_mfma_f32_16x16x32_bf16 a[116:119],v[244:247],v[204:207],a[116:119]\n"
"s_add_u32 s48,s48,s88\n"
"s_addc_u32 s49,s49,0\n"
"s_mov_b32 s50,3584\n"
"v_mfma_f32_16x16x32_bf16 a[116:119],v[180:183],v[140:143],a[116:119]\n"
"s_mov_b32 s51,0x20000\n"
"v_lshlrev_b32 v8,3,v112\n"
"v_mfma_f32_16x16x32_bf16 a[120:123],v[184:187],v[140:143],a[120:123]\n"
"buffer_load_dwordx4 v112,s[24:27],s76 offen offset:1024 sc1 lds\n"
"v_mfma_f32_16x16x32_bf16 a[120:123],v[248:251],v[204:207],a[120:123]\n"
"v_mfma_f32_16x16x32_bf16 a[124:127],v[252:255],v[204:207],a[124:127]\n"
"v_mfma_f32_16x16x32_bf16 a[124:127],v[188:191],v[140:143],a[124:127]\n"
"v_mfma_f32_16x16x32_bf16 a[128:131],v[160:163],v[144:147],a[128:131]\n"
"v_mfma_f32_16x16x32_bf16 a[128:131],v[224:227],v[208:211],a[128:131]\n"
"buffer_load_dwordx4 v112,s[24:27],s76 offen offset:2048 sc1 lds\n"
"v_mfma_f32_16x16x32_bf16 a[132:135],v[228:231],v[208:211],a[132:135]\n"
"v_mfma_f32_16x16x32_bf16 a[132:135],v[164:167],v[144:147],a[132:135]\n"
"v_mfma_f32_16x16x32_bf16 a[136:139],v[168:171],v[144:147],a[136:139]\n"
"v_mfma_f32_16x16x32_bf16 a[136:139],v[232:235],v[208:211],a[136:139]\n"
"buffer_load_dwordx4 v112,s[24:27],s76 offen offset:3072 sc1 lds\n"
"v_mfma_f32_16x16x32_bf16 a[140:143],v[236:239],v[208:211],a[140:143]\n"
"s_xor_b32 s76,s76,16384\n"
"v_mfma_f32_16x16x32_bf16 a[140:143],v[172:175],v[144:147],a[140:143]\n"
"v_mfma_f32_16x16x32_bf16 a[144:147],v[176:179],v[144:147],a[144:147]\n"
"v_mfma_f32_16x16x32_bf16 a[144:147],v[240:243],v[208:211],a[144:147]\n"
"v_mfma_f32_16x16x32_bf16 a[148:151],v[244:247],v[208:211],a[148:151]\n"
"s_add_u32 m0,s78,81920\n"
"v_mfma_f32_16x16x32_bf16 a[148:151],v[180:183],v[144:147],a[148:151]\n"
"buffer_load_dwordx4 v112,s[24:27],s76 offen offset:0 sc1 lds\n"
"v_mfma_f32_16x16x32_bf16 a[152:155],v[184:187],v[144:147],a[152:155]\n"
"v_mfma_f32_16x16x32_bf16 a[152:155],v[248:251],v[208:211],a[152:155]\n"
"v_mfma_f32_16x16x32_bf16 a[156:159],v[252:255],v[208:211],a[156:159]\n"
"v_mfma_f32_16x16x32_bf16 a[156:159],v[188:191],v[144:147],a[156:159]\n"
"v_mfma_f32_16x16x32_bf16 a[160:163],v[160:163],v[148:151],a[160:163]\n"
"buffer_load_dwordx4 v112,s[24:27],s76 offen offset:1024 sc1 lds\n"
"v_mfma_f32_16x16x32_bf16 a[160:163],v[224:227],v[212:215],a[160:163]\n"
"v_mfma_f32_16x16x32_bf16 a[164:167],v[228:231],v[212:215],a[164:167]\n"
"v_mfma_f32_16x16x32_bf16 a[164:167],v[164:167],v[148:151],a[164:167]\n"
"v_mfma_f32_16x16x32_bf16 a[168:171],v[168:171],v[148:151],a[168:171]\n"
"v_mfma_f32_16x16x32_bf16 a[168:171],v[232:235],v[212:215],a[168:171]\n"
"buffer_load_dwordx4 v112,s[24:27],s76 offen offset:2048 sc1 lds\n"
"v_mfma_f32_16x16x32_bf16 a[172:175],v[236:239],v[212:215],a[172:175]\n"
"v_mfma_f32_16x16x32_bf16 a[172:175],v[172:175],v[148:151],a[172:175]\n"
"v_mfma_f32_16x16x32_bf16 a[176:179],v[176:179],v[148:151],a[176:179]\n"
"v_mfma_f32_16x16x32_bf16 a[176:179],v[240:243],v[212:215],a[176:179]\n"
"buffer_load_dwordx4 v112,s[24:27],s76 offen offset:3072 sc1 lds\n"
"v_mfma_f32_16x16x32_bf16 a[180:183],v[244:247],v[212:215],a[180:183]\n"
"s_xor_b32 s76,s76,49152\n"
"v_mfma_f32_16x16x32_bf16 a[180:183],v[180:183],v[148:151],a[180:183]\n"
"v_mfma_f32_16x16x32_bf16 a[184:187],v[184:187],v[148:151],a[184:187]\n"
"buffer_load_dword v115,v8,s[48:51],0 offen\n"
"v_mfma_f32_16x16x32_bf16 a[184:187],v[248:251],v[212:215],a[184:187]\n"
"v_mfma_f32_16x16x32_bf16 a[188:191],v[252:255],v[212:215],a[188:191]\n"
"v_mfma_f32_16x16x32_bf16 a[188:191],v[188:191],v[148:151],a[188:191]\n"
"s_barrier\n"
"v_mfma_f32_16x16x32_bf16 a[192:195],v[224:227],v[216:219],a[192:195]\n"
"ds_read_b128 v[152:155],v80 offset:12288\n"
"v_mfma_f32_16x16x32_bf16 a[224:227],v[224:227],v[220:223],a[224:227]\n"
"ds_read_b128 v[156:159],v80 offset:14336\n"
"v_mfma_f32_16x16x32_bf16 a[228:231],v[228:231],v[220:223],a[228:231]\n"
"ds_read_b64_tr_b16 v[160:161],v98 offset:49152\n"
"v_mfma_f32_16x16x32_bf16 a[196:199],v[228:231],v[216:219],a[196:199]\n"
"ds_read_b64_tr_b16 v[162:163],v98 offset:50176\n"
"v_mfma_f32_16x16x32_bf16 a[200:203],v[232:235],v[216:219],a[200:203]\n"
"ds_read_b64_tr_b16 v[164:165],v99 offset:49152\n"
"v_mfma_f32_16x16x32_bf16 a[232:235],v[232:235],v[220:223],a[232:235]\n"
"ds_read_b64_tr_b16 v[166:167],v99 offset:50176\n"
"v_mfma_f32_16x16x32_bf16 a[236:239],v[236:239],v[220:223],a[236:239]\n"
"ds_read_b64_tr_b16 v[168:169],v100 offset:49152\n"
"v_mfma_f32_16x16x32_bf16 a[204:207],v[236:239],v[216:219],a[204:207]\n"
"ds_read_b64_tr_b16 v[170:171],v100 offset:50176\n"
"v_mfma_f32_16x16x32_bf16 a[208:211],v[240:243],v[216:219],a[208:211]\n"
"ds_read_b64_tr_b16 v[172:173],v101 offset:49152\n"
"v_mfma_f32_16x16x32_bf16 a[240:243],v[240:243],v[220:223],a[240:243]\n"
"ds_read_b64_tr_b16 v[174:175],v101 offset:50176\n"
"v_mfma_f32_16x16x32_bf16 a[244:247],v[244:247],v[220:223],a[244:247]\n"
"ds_read_b64_tr_b16 v[176:177],v102 offset:49152\n"
"v_mfma_f32_16x16x32_bf16 a[212:215],v[244:247],v[216:219],a[212:215]\n"
"ds_read_b64_tr_b16 v[178:179],v102 offset:50176\n"
"v_mfma_f32_16x16x32_bf16 a[216:219],v[248:251],v[216:219],a[216:219]\n"
"ds_read_b64_tr_b16 v[180:181],v103 offset:49152\n"
"v_mfma_f32_16x16x32_bf16 a[248:251],v[248:251],v[220:223],a[248:251]\n"
"ds_read_b64_tr_b16 v[182:183],v103 offset:50176\n"
"ds_read_b64_tr_b16 v[184:185],v104 offset:49152\n"
"v_mfma_f32_16x16x32_bf16 a[252:255],v[252:255],v[220:223],a[252:255]\n"
"v_mov_b32 v110,v72\n"
"v_mov_b32 v111,v73\n"
"v_mfma_f32_16x16x32_bf16 a[220:223],v[252:255],v[216:219],a[220:223]\n"
"ds_read_b64_tr_b16 v[186:187],v104 offset:50176\n"
"s_waitcnt lgkmcnt(10)\n"
"v_mfma_f32_16x16x32_bf16 a[192:195],v[160:163],v[152:155],a[192:195]\n"
"ds_read_b64_tr_b16 v[188:189],v105 offset:49152\n"
"v_mfma_f32_16x16x32_bf16 a[224:227],v[160:163],v[156:159],a[224:227]\n"
"ds_read_b64_tr_b16 v[190:191],v105 offset:50176\n"
"v_mfma_f32_16x16x32_bf16 a[228:231],v[164:167],v[156:159],a[228:231]\n"
"ds_read_b128 v[128:131],v80 offset:0\n"
"v_mfma_f32_16x16x32_bf16 a[196:199],v[164:167],v[152:155],a[196:199]\n"
"ds_read_b128 v[192:195],v81 offset:0\n"
"s_waitcnt lgkmcnt(10)\n"
"v_mfma_f32_16x16x32_bf16 a[200:203],v[168:171],v[152:155],a[200:203]\n"
"ds_read_b64_tr_b16 v[224:225],v98 offset:32768\n"
"v_mfma_f32_16x16x32_bf16 a[232:235],v[168:171],v[156:159],a[232:235]\n"
"ds_read_b64_tr_b16 v[226:227],v98 offset:33792\n"
"v_mfma_f32_16x16x32_bf16 a[236:239],v[172:175],v[156:159],a[236:239]\n"
"ds_read_b64_tr_b16 v[228:229],v99 offset:32768\n"
"v_mfma_f32_16x16x32_bf16 a[204:207],v[172:175],v[152:155],a[204:207]\n"
"ds_read_b64_tr_b16 v[230:231],v99 offset:33792\n"
"s_waitcnt lgkmcnt(10)\n"
"v_mfma_f32_16x16x32_bf16 a[208:211],v[176:179],v[152:155],a[208:211]\n"
"ds_read_b64_tr_b16 v[232:233],v100 offset:32768\n"
"v_mfma_f32_16x16x32_bf16 a[240:243],v[176:179],v[156:159],a[240:243]\n"
"ds_read_b64_tr_b16 v[234:235],v100 offset:33792\n"
"v_mfma_f32_16x16x32_bf16 a[244:247],v[180:183],v[156:159],a[244:247]\n"
"ds_read_b64_tr_b16 v[236:237],v101 offset:32768\n"
"v_mfma_f32_16x16x32_bf16 a[212:215],v[180:183],v[152:155],a[212:215]\n"
"ds_read_b64_tr_b16 v[238:239],v101 offset:33792\n"
"s_waitcnt lgkmcnt(10)\n"
"v_mfma_f32_16x16x32_bf16 a[216:219],v[184:187],v[152:155],a[216:219]\n"
"ds_read_b64_tr_b16 v[240:241],v102 offset:32768\n"
"v_mfma_f32_16x16x32_bf16 a[248:251],v[184:187],v[156:159],a[248:251]\n"
"ds_read_b64_tr_b16 v[242:243],v102 offset:33792\n"
"v_mfma_f32_16x16x32_bf16 a[252:255],v[188:191],v[156:159],a[252:255]\n"
"ds_read_b64_tr_b16 v[244:245],v103 offset:32768\n"
"v_mfma_f32_16x16x32_bf16 a[220:223],v[188:191],v[152:155],a[220:223]\n"
"ds_read_b64_tr_b16 v[246:247],v103 offset:33792\n"
"s_waitcnt lgkmcnt(8)\n"
"v_mfma_f32_16x16x32_bf16 a[0:3],v[160:163],v[128:131],a[0:3]\n"
"ds_read_b64_tr_b16 v[248:249],v104 offset:32768\n"
"v_mfma_f32_16x16x32_bf16 a[0:3],v[224:227],v[192:195],a[0:3]\n"
"ds_read_b64_tr_b16 v[250:251],v104 offset:33792\n"
"v_mfma_f32_16x16x32_bf16 a[4:7],v[228:231],v[192:195],a[4:7]\n"
"ds_read_b64_tr_b16 v[252:253],v105 offset:32768\n"
"v_mfma_f32_16x16x32_bf16 a[4:7],v[164:167],v[128:131],a[4:7]\n"
"ds_read_b64_tr_b16 v[254:255],v105 offset:33792\n"
"s_waitcnt lgkmcnt(8)\n"
"v_mfma_f32_16x16x32_bf16 a[8:11],v[168:171],v[128:131],a[8:11]\n"
"ds_read_b128 v[132:135],v80 offset:2048\n"
"v_mfma_f32_16x16x32_bf16 a[8:11],v[232:235],v[192:195],a[8:11]\n"
"ds_read_b128 v[196:199],v81 offset:2048\n"
"v_mfma_f32_16x16x32_bf16 a[12:15],v[236:239],v[192:195],a[12:15]\n"
"ds_read_b128 v[136:139],v80 offset:4096\n"
"v_mfma_f32_16x16x32_bf16 a[12:15],v[172:175],v[128:131],a[12:15]\n"
"ds_read_b128 v[200:203],v81 offset:4096\n"
"s_waitcnt lgkmcnt(8)\n"
"v_mfma_f32_16x16x32_bf16 a[16:19],v[176:179],v[128:131],a[16:19]\n"
"ds_read_b128 v[140:143],v80 offset:6144\n"
"v_mfma_f32_16x16x32_bf16 a[16:19],v[240:243],v[192:195],a[16:19]\n"
"ds_read_b128 v[204:207],v81 offset:6144\n"
"v_mfma_f32_16x16x32_bf16 a[20:23],v[244:247],v[192:195],a[20:23]\n"
"ds_read_b128 v[144:147],v80 offset:8192\n"
"v_mfma_f32_16x16x32_bf16 a[20:23],v[180:183],v[128:131],a[20:23]\n"
"ds_read_b128 v[208:211],v81 offset:8192\n"
"s_waitcnt lgkmcnt(8)\n"
"v_mfma_f32_16x16x32_bf16 a[24:27],v[184:187],v[128:131],a[24:27]\n"
"ds_read_b128 v[148:151],v80 offset:10240\n"
"v_mfma_f32_16x16x32_bf16 a[24:27],v[248:251],v[192:195],a[24:27]\n"
"ds_read_b128 v[212:215],v81 offset:10240\n"
"v_mfma_f32_16x16x32_bf16 a[28:31],v[252:255],v[192:195],a[28:31]\n"
"ds_read_b128 v[216:219],v81 offset:12288\n"
"v_mfma_f32_16x16x32_bf16 a[28:31],v[188:191],v[128:131],a[28:31]\n"
"ds_read_b128 v[220:223],v81 offset:14336\n"
"s_waitcnt lgkmcnt(10)\n"
"v_mfma_f32_16x16x32_bf16 a[32:35],v[160:163],v[132:135],a[32:35]\n"
"v_mfma_f32_16x16x32_bf16 a[32:35],v[224:227],v[196:199],a[32:35]\n"
"v_mfma_f32_16x16x32_bf16 a[36:39],v[228:231],v[196:199],a[36:39]\n"
"v_mfma_f32_16x16x32_bf16 a[36:39],v[164:167],v[132:135],a[36:39]\n"
"v_mfma_f32_16x16x32_bf16 a[40:43],v[168:171],v[132:135],a[40:43]\n"
"s_waitcnt lgkmcnt(0)\n"
"s_barrier\n"
"v_mfma_f32_16x16x32_bf16 a[40:43],v[232:235],v[196:199],a[40:43]\n"
"s_add_u32 m0,s85,147456\n"
"v_mfma_f32_16x16x32_bf16 a[44:47],v[236:239],v[196:199],a[44:47]\n"
"buffer_load_dwordx4 v110,s[28:31],s86 offen lds\n"
"v_mfma_f32_16x16x32_bf16 a[44:47],v[172:175],v[132:135],a[44:47]\n"
"v_mfma_f32_16x16x32_bf16 a[48:51],v[176:179],v[132:135],a[48:51]\n"
"s_load_dwordx2 s[48:49],s[0:1],56\n"
"v_mfma_f32_16x16x32_bf16 a[48:51],v[240:243],v[196:199],a[48:51]\n"
"s_add_u32 m0,s85,148480\n"
"v_mfma_f32_16x16x32_bf16 a[52:55],v[244:247],v[196:199],a[52:55]\n"
"buffer_load_dwordx4 v111,s[28:31],s86 offen lds\n"
"v_mfma_f32_16x16x32_bf16 a[52:55],v[180:183],v[132:135],a[52:55]\n"
"s_xor_b32 s86,s86,262144\n"
"v_mfma_f32_16x16x32_bf16 a[56:59],v[184:187],v[132:135],a[56:59]\n"
"v_mfma_f32_16x16x32_bf16 a[56:59],v[248:251],v[196:199],a[56:59]\n"
"s_add_u32 m0,s85,155648\n"
"v_mfma_f32_16x16x32_bf16 a[60:63],v[252:255],v[196:199],a[60:63]\n"
"buffer_load_dwordx4 v110,s[28:31],s86 offen lds\n"
"v_mfma_f32_16x16x32_bf16 a[60:63],v[188:191],v[132:135],a[60:63]\n"
"v_mfma_f32_16x16x32_bf16 a[64:67],v[160:163],v[136:139],a[64:67]\n"
"v_mfma_f32_16x16x32_bf16 a[64:67],v[224:227],v[200:203],a[64:67]\n"
"s_add_u32 m0,s85,156672\n"
"v_mfma_f32_16x16x32_bf16 a[68:71],v[228:231],v[200:203],a[68:71]\n"
"buffer_load_dwordx4 v111,s[28:31],s86 offen lds\n"
"v_mfma_f32_16x16x32_bf16 a[68:71],v[164:167],v[136:139],a[68:71]\n"
"s_xor_b32 s86,s86,786432\n"
"v_mfma_f32_16x16x32_bf16 a[72:75],v[168:171],v[136:139],a[72:75]\n"
"v_mfma_f32_16x16x32_bf16 a[72:75],v[232:235],v[200:203],a[72:75]\n"
"s_add_u32 m0,s85,131072\n"
"v_mfma_f32_16x16x32_bf16 a[76:79],v[236:239],v[200:203],a[76:79]\n"
"buffer_load_dwordx4 v110,s[28:31],s86 offen lds\n"
"v_mfma_f32_16x16x32_bf16 a[76:79],v[172:175],v[136:139],a[76:79]\n"
"v_mfma_f32_16x16x32_bf16 a[80:83],v[176:179],v[136:139],a[80:83]\n"
"v_mfma_f32_16x16x32_bf16 a[80:83],v[240:243],v[200:203],a[80:83]\n"
"s_add_u32 m0,s85,132096\n"
"v_mfma_f32_16x16x32_bf16 a[84:87],v[244:247],v[200:203],a[84:87]\n"
"buffer_load_dwordx4 v111,s[28:31],s86 offen lds\n"
"v_mfma_f32_16x16x32_bf16 a[84:87],v[180:183],v[136:139],a[84:87]\n"
"s_xor_b32 s86,s86,262144\n"
"v_mfma_f32_16x16x32_bf16 a[88:91],v[184:187],v[136:139],a[88:91]\n"
"v_mfma_f32_16x16x32_bf16 a[88:91],v[248:251],v[200:203],a[88:91]\n"
"v_mfma_f32_16x16x32_bf16 a[92:95],v[252:255],v[200:203],a[92:95]\n"
"v_mfma_f32_16x16x32_bf16 a[92:95],v[188:191],v[136:139],a[92:95]\n"
"v_mfma_f32_16x16x32_bf16 a[96:99],v[160:163],v[140:143],a[96:99]\n"
"v_mfma_f32_16x16x32_bf16 a[96:99],v[224:227],v[204:207],a[96:99]\n"
"s_add_u32 m0,s85,139264\n"
"v_mfma_f32_16x16x32_bf16 a[100:103],v[228:231],v[204:207],a[100:103]\n"
"buffer_load_dwordx4 v110,s[28:31],s86 offen lds\n"
"v_mfma_f32_16x16x32_bf16 a[100:103],v[164:167],v[140:143],a[100:103]\n"
"v_mfma_f32_16x16x32_bf16 a[104:107],v[168:171],v[140:143],a[104:107]\n"
"v_mfma_f32_16x16x32_bf16 a[104:107],v[232:235],v[204:207],a[104:107]\n"
"s_add_u32 m0,s85,140288\n"
"v_mfma_f32_16x16x32_bf16 a[108:111],v[236:239],v[204:207],a[108:111]\n"
"buffer_load_dwordx4 v111,s[28:31],s86 offen lds\n"
"v_mfma_f32_16x16x32_bf16 a[108:111],v[172:175],v[140:143],a[108:111]\n"
"s_xor_b32 s86,s86,1835008\n"
"v_mfma_f32_16x16x32_bf16 a[112:115],v[176:179],v[140:143],a[112:115]\n"
"s_waitcnt lgkmcnt(0)\n"
"s_lshl2_add_u32 s88,s100,s52\n"
"v_mfma_f32_16x16x32_bf16 a[112:115],v[240:243],v[204:207],a[112:115]\n"
"s_mul_i32 s88,s88,7168\n"
"s_add_u32 m0,s78,0\n"
"v_mfma_f32_16x16x32_bf16 a[116:119],v[244:247],v[204:207],a[116:119]\n"
"buffer_load_dwordx4 v112,s[24:27],s76 offen offset:0 sc1 lds\n"
"v_mfma_f32_16x16x32_bf16 a[116:119],v[180:183],v[140:143],a[116:119]\n"
"s_add_u32 s88,s88,3584\n"
"s_add_u32 s48,s48,s88\n"
"s_addc_u32 s49,s49,0\n"
"v_mfma_f32_16x16x32_bf16 a[120:123],v[184:187],v[140:143],a[120:123]\n"
"s_mov_b32 s50,3584\n"
"s_mov_b32 s51,0x20000\n"
"v_mfma_f32_16x16x32_bf16 a[120:123],v[248:251],v[204:207],a[120:123]\n"
"v_lshlrev_b32 v8,3,v112\n"
"v_mfma_f32_16x16x32_bf16 a[124:127],v[252:255],v[204:207],a[124:127]\n"
"buffer_load_dwordx4 v112,s[24:27],s76 offen offset:1024 sc1 lds\n"
"v_mfma_f32_16x16x32_bf16 a[124:127],v[188:191],v[140:143],a[124:127]\n"
"v_mfma_f32_16x16x32_bf16 a[128:131],v[160:163],v[144:147],a[128:131]\n"
"v_mfma_f32_16x16x32_bf16 a[128:131],v[224:227],v[208:211],a[128:131]\n"
"v_mfma_f32_16x16x32_bf16 a[132:135],v[228:231],v[208:211],a[132:135]\n"
"buffer_load_dwordx4 v112,s[24:27],s76 offen offset:2048 sc1 lds\n"
"v_mfma_f32_16x16x32_bf16 a[132:135],v[164:167],v[144:147],a[132:135]\n"
"v_mfma_f32_16x16x32_bf16 a[136:139],v[168:171],v[144:147],a[136:139]\n"
"v_mfma_f32_16x16x32_bf16 a[136:139],v[232:235],v[208:211],a[136:139]\n"
"buffer_load_dwordx4 v112,s[24:27],s76 offen offset:3072 sc1 lds\n"
"v_mfma_f32_16x16x32_bf16 a[140:143],v[236:239],v[208:211],a[140:143]\n"
"s_xor_b32 s76,s76,16384\n"
"v_mfma_f32_16x16x32_bf16 a[140:143],v[172:175],v[144:147],a[140:143]\n"
"v_mfma_f32_16x16x32_bf16 a[144:147],v[176:179],v[144:147],a[144:147]\n"
"v_mfma_f32_16x16x32_bf16 a[144:147],v[240:243],v[208:211],a[144:147]\n"
"s_add_u32 m0,s78,16384\n"
"v_mfma_f32_16x16x32_bf16 a[148:151],v[244:247],v[208:211],a[148:151]\n"
"buffer_load_dwordx4 v112,s[24:27],s76 offen offset:0 sc1 lds\n"
"v_mfma_f32_16x16x32_bf16 a[148:151],v[180:183],v[144:147],a[148:151]\n"
"v_mfma_f32_16x16x32_bf16 a[152:155],v[184:187],v[144:147],a[152:155]\n"
"v_mfma_f32_16x16x32_bf16 a[152:155],v[248:251],v[208:211],a[152:155]\n"
"v_mfma_f32_16x16x32_bf16 a[156:159],v[252:255],v[208:211],a[156:159]\n"
"buffer_load_dwordx4 v112,s[24:27],s76 offen offset:1024 sc1 lds\n"
"v_mfma_f32_16x16x32_bf16 a[156:159],v[188:191],v[144:147],a[156:159]\n"
"v_mfma_f32_16x16x32_bf16 a[160:163],v[160:163],v[148:151],a[160:163]\n"
"v_mfma_f32_16x16x32_bf16 a[160:163],v[224:227],v[212:215],a[160:163]\n"
"v_mfma_f32_16x16x32_bf16 a[164:167],v[228:231],v[212:215],a[164:167]\n"
"buffer_load_dwordx4 v112,s[24:27],s76 offen offset:2048 sc1 lds\n"
"v_mfma_f32_16x16x32_bf16 a[164:167],v[164:167],v[148:151],a[164:167]\n"
"v_mfma_f32_16x16x32_bf16 a[168:171],v[168:171],v[148:151],a[168:171]\n"
"v_mfma_f32_16x16x32_bf16 a[168:171],v[232:235],v[212:215],a[168:171]\n"
"buffer_load_dwordx4 v112,s[24:27],s76 offen offset:3072 sc1 lds\n"
"v_mfma_f32_16x16x32_bf16 a[172:175],v[236:239],v[212:215],a[172:175]\n"
"s_xor_b32 s76,s76,114688\n"
"v_mfma_f32_16x16x32_bf16 a[172:175],v[172:175],v[148:151],a[172:175]\n"
"v_mfma_f32_16x16x32_bf16 a[176:179],v[176:179],v[148:151],a[176:179]\n"
"buffer_load_dword v115,v8,s[48:51],0 offen\n"
"v_mfma_f32_16x16x32_bf16 a[176:179],v[240:243],v[212:215],a[176:179]\n"
"v_mfma_f32_16x16x32_bf16 a[180:183],v[244:247],v[212:215],a[180:183]\n"
"v_mfma_f32_16x16x32_bf16 a[180:183],v[180:183],v[148:151],a[180:183]\n"
"v_mfma_f32_16x16x32_bf16 a[184:187],v[184:187],v[148:151],a[184:187]\n"
"v_mfma_f32_16x16x32_bf16 a[184:187],v[248:251],v[212:215],a[184:187]\n"
"v_mfma_f32_16x16x32_bf16 a[188:191],v[252:255],v[212:215],a[188:191]\n"
"v_mfma_f32_16x16x32_bf16 a[188:191],v[188:191],v[148:151],a[188:191]\n"
"s_waitcnt vmcnt(26)\n"
"s_barrier\n"
"v_mfma_f32_16x16x32_bf16 a[192:195],v[224:227],v[216:219],a[192:195]\n"
"ds_read_b128 v[152:155],v80 offset:45056\n"
"v_mfma_f32_16x16x32_bf16 a[224:227],v[224:227],v[220:223],a[224:227]\n"
"ds_read_b128 v[156:159],v80 offset:47104\n"
"v_mfma_f32_16x16x32_bf16 a[228:231],v[228:231],v[220:223],a[228:231]\n"
"ds_read_b64_tr_b16 v[160:161],v98 offset:0\n"
"v_mfma_f32_16x16x32_bf16 a[196:199],v[228:231],v[216:219],a[196:199]\n"
"ds_read_b64_tr_b16 v[162:163],v98 offset:1024\n"
"v_mfma_f32_16x16x32_bf16 a[200:203],v[232:235],v[216:219],a[200:203]\n"
"ds_read_b64_tr_b16 v[164:165],v99 offset:0\n"
"v_mfma_f32_16x16x32_bf16 a[232:235],v[232:235],v[220:223],a[232:235]\n"
"ds_read_b64_tr_b16 v[166:167],v99 offset:1024\n"
"v_mfma_f32_16x16x32_bf16 a[236:239],v[236:239],v[220:223],a[236:239]\n"
"ds_read_b64_tr_b16 v[168:169],v100 offset:0\n"
"v_mfma_f32_16x16x32_bf16 a[204:207],v[236:239],v[216:219],a[204:207]\n"
"ds_read_b64_tr_b16 v[170:171],v100 offset:1024\n"
"v_mfma_f32_16x16x32_bf16 a[208:211],v[240:243],v[216:219],a[208:211]\n"
"ds_read_b64_tr_b16 v[172:173],v101 offset:0\n"
"v_mfma_f32_16x16x32_bf16 a[240:243],v[240:243],v[220:223],a[240:243]\n"
"ds_read_b64_tr_b16 v[174:175],v101 offset:1024\n"
"v_mfma_f32_16x16x32_bf16 a[244:247],v[244:247],v[220:223],a[244:247]\n"
"ds_read_b64_tr_b16 v[176:177],v102 offset:0\n"
"v_mfma_f32_16x16x32_bf16 a[212:215],v[244:247],v[216:219],a[212:215]\n"
"ds_read_b64_tr_b16 v[178:179],v102 offset:1024\n"
"v_mfma_f32_16x16x32_bf16 a[216:219],v[248:251],v[216:219],a[216:219]\n"
"ds_read_b64_tr_b16 v[180:181],v103 offset:0\n"
"v_mfma_f32_16x16x32_bf16 a[248:251],v[248:251],v[220:223],a[248:251]\n"
"ds_read_b64_tr_b16 v[182:183],v103 offset:1024\n"
"v_mfma_f32_16x16x32_bf16 a[252:255],v[252:255],v[220:223],a[252:255]\n"
"ds_read_b64_tr_b16 v[184:185],v104 offset:0\n"
"v_mfma_f32_16x16x32_bf16 a[220:223],v[252:255],v[216:219],a[220:223]\n"
"ds_read_b64_tr_b16 v[186:187],v104 offset:1024\n"
"s_waitcnt lgkmcnt(10)\n"
"v_mfma_f32_16x16x32_bf16 a[192:195],v[160:163],v[152:155],a[192:195]\n"
"ds_read_b64_tr_b16 v[188:189],v105 offset:0\n"
"v_mfma_f32_16x16x32_bf16 a[224:227],v[160:163],v[156:159],a[224:227]\n"
"ds_read_b64_tr_b16 v[190:191],v105 offset:1024\n"
"v_mfma_f32_16x16x32_bf16 a[228:231],v[164:167],v[156:159],a[228:231]\n"
"ds_read_b128 v[128:131],v80 offset:32768\n"
"v_mfma_f32_16x16x32_bf16 a[196:199],v[164:167],v[152:155],a[196:199]\n"
"ds_read_b128 v[192:195],v81 offset:32768\n"
"s_waitcnt lgkmcnt(10)\n"
"v_mfma_f32_16x16x32_bf16 a[200:203],v[168:171],v[152:155],a[200:203]\n"
"ds_read_b64_tr_b16 v[224:225],v98 offset:16384\n"
"v_mfma_f32_16x16x32_bf16 a[232:235],v[168:171],v[156:159],a[232:235]\n"
"ds_read_b64_tr_b16 v[226:227],v98 offset:17408\n"
"v_mfma_f32_16x16x32_bf16 a[236:239],v[172:175],v[156:159],a[236:239]\n"
"ds_read_b64_tr_b16 v[228:229],v99 offset:16384\n"
"v_mfma_f32_16x16x32_bf16 a[204:207],v[172:175],v[152:155],a[204:207]\n"
"ds_read_b64_tr_b16 v[230:231],v99 offset:17408\n"
"s_waitcnt lgkmcnt(10)\n"
"v_mfma_f32_16x16x32_bf16 a[208:211],v[176:179],v[152:155],a[208:211]\n"
"ds_read_b64_tr_b16 v[232:233],v100 offset:16384\n"
"v_mfma_f32_16x16x32_bf16 a[240:243],v[176:179],v[156:159],a[240:243]\n"
"ds_read_b64_tr_b16 v[234:235],v100 offset:17408\n"
"v_mfma_f32_16x16x32_bf16 a[244:247],v[180:183],v[156:159],a[244:247]\n"
"ds_read_b64_tr_b16 v[236:237],v101 offset:16384\n"
"v_mfma_f32_16x16x32_bf16 a[212:215],v[180:183],v[152:155],a[212:215]\n"
"ds_read_b64_tr_b16 v[238:239],v101 offset:17408\n"
"s_waitcnt lgkmcnt(10)\n"
"v_mfma_f32_16x16x32_bf16 a[216:219],v[184:187],v[152:155],a[216:219]\n"
"ds_read_b64_tr_b16 v[240:241],v102 offset:16384\n"
"v_mfma_f32_16x16x32_bf16 a[248:251],v[184:187],v[156:159],a[248:251]\n"
"ds_read_b64_tr_b16 v[242:243],v102 offset:17408\n"
"v_mfma_f32_16x16x32_bf16 a[252:255],v[188:191],v[156:159],a[252:255]\n"
"ds_read_b64_tr_b16 v[244:245],v103 offset:16384\n"
"v_mfma_f32_16x16x32_bf16 a[220:223],v[188:191],v[152:155],a[220:223]\n"
"ds_read_b64_tr_b16 v[246:247],v103 offset:17408\n"
"s_waitcnt lgkmcnt(8)\n"
"v_mfma_f32_16x16x32_bf16 a[0:3],v[160:163],v[128:131],a[0:3]\n"
"ds_read_b64_tr_b16 v[248:249],v104 offset:16384\n"
"v_mfma_f32_16x16x32_bf16 a[0:3],v[224:227],v[192:195],a[0:3]\n"
"ds_read_b64_tr_b16 v[250:251],v104 offset:17408\n"
"v_mfma_f32_16x16x32_bf16 a[4:7],v[228:231],v[192:195],a[4:7]\n"
"ds_read_b64_tr_b16 v[252:253],v105 offset:16384\n"
"v_mfma_f32_16x16x32_bf16 a[4:7],v[164:167],v[128:131],a[4:7]\n"
"ds_read_b64_tr_b16 v[254:255],v105 offset:17408\n"
"s_waitcnt lgkmcnt(8)\n"
"v_mfma_f32_16x16x32_bf16 a[8:11],v[168:171],v[128:131],a[8:11]\n"
"ds_read_b128 v[132:135],v80 offset:34816\n"
"v_mfma_f32_16x16x32_bf16 a[8:11],v[232:235],v[192:195],a[8:11]\n"
"ds_read_b128 v[196:199],v81 offset:34816\n"
"v_mfma_f32_16x16x32_bf16 a[12:15],v[236:239],v[192:195],a[12:15]\n"
"ds_read_b128 v[136:139],v80 offset:36864\n"
"v_mfma_f32_16x16x32_bf16 a[12:15],v[172:175],v[128:131],a[12:15]\n"
"ds_read_b128 v[200:203],v81 offset:36864\n"
"s_waitcnt lgkmcnt(8)\n"
"v_mfma_f32_16x16x32_bf16 a[16:19],v[176:179],v[128:131],a[16:19]\n"
"s_and_b32 s82,s52,1\n"
"ds_read_b128 v[140:143],v80 offset:38912\n"
"s_lshl_b32 s82,s82,13\n"
"v_mfma_f32_16x16x32_bf16 a[16:19],v[240:243],v[192:195],a[16:19]\n"
"ds_read_b128 v[204:207],v81 offset:38912\n"
"v_mfma_f32_16x16x32_bf16 a[20:23],v[244:247],v[192:195],a[20:23]\n"
"ds_read_b128 v[144:147],v80 offset:40960\n"
"v_mfma_f32_16x16x32_bf16 a[20:23],v[180:183],v[128:131],a[20:23]\n"
"ds_read_b128 v[208:211],v81 offset:40960\n"
"s_waitcnt lgkmcnt(8)\n"
"v_mfma_f32_16x16x32_bf16 a[24:27],v[184:187],v[128:131],a[24:27]\n"
"ds_read_b128 v[148:151],v80 offset:43008\n"
"v_mfma_f32_16x16x32_bf16 a[24:27],v[248:251],v[192:195],a[24:27]\n"
"ds_read_b128 v[212:215],v81 offset:43008\n"
"v_subrev_u32 v98,s82,v98\n"
"v_mfma_f32_16x16x32_bf16 a[28:31],v[252:255],v[192:195],a[28:31]\n"
"ds_read_b128 v[216:219],v81 offset:45056\n"
"v_add_u32 v96,s78,v97\n"
"v_mfma_f32_16x16x32_bf16 a[28:31],v[188:191],v[128:131],a[28:31]\n"
"ds_read_b128 v[220:223],v81 offset:47104\n"
"s_waitcnt lgkmcnt(10)\n"
"v_mfma_f32_16x16x32_bf16 a[32:35],v[160:163],v[132:135],a[32:35]\n"
"v_add_u32 v97,65536,v96\n"
"v_mfma_f32_16x16x32_bf16 a[32:35],v[224:227],v[196:199],a[32:35]\n"
"v_subrev_u32 v99,s82,v99\n"
"v_mfma_f32_16x16x32_bf16 a[36:39],v[228:231],v[196:199],a[36:39]\n"
"v_mfma_f32_16x16x32_bf16 a[36:39],v[164:167],v[132:135],a[36:39]\n"
"v_mfma_f32_16x16x32_bf16 a[40:43],v[168:171],v[132:135],a[40:43]\n"
"s_waitcnt lgkmcnt(0)\n"
"s_barrier\n"
"v_mfma_f32_16x16x32_bf16 a[40:43],v[232:235],v[196:199],a[40:43]\n"
"v_subrev_u32 v100,s82,v100\n"
"s_add_u32 m0,s85,98304\n"
"v_mfma_f32_16x16x32_bf16 a[44:47],v[236:239],v[196:199],a[44:47]\n"
"buffer_load_dwordx4 v110,s[28:31],s86 offen lds\n"
"v_mfma_f32_16x16x32_bf16 a[44:47],v[172:175],v[132:135],a[44:47]\n"
"v_mfma_f32_16x16x32_bf16 a[48:51],v[176:179],v[132:135],a[48:51]\n"
"v_mfma_f32_16x16x32_bf16 a[48:51],v[240:243],v[196:199],a[48:51]\n"
"v_subrev_u32 v101,s82,v101\n"
"s_add_u32 m0,s85,99328\n"
"v_mfma_f32_16x16x32_bf16 a[52:55],v[244:247],v[196:199],a[52:55]\n"
"buffer_load_dwordx4 v111,s[28:31],s86 offen lds\n"
"v_mfma_f32_16x16x32_bf16 a[52:55],v[180:183],v[132:135],a[52:55]\n"
"s_xor_b32 s86,s86,262144\n"
"v_mfma_f32_16x16x32_bf16 a[56:59],v[184:187],v[132:135],a[56:59]\n"
"v_mfma_f32_16x16x32_bf16 a[56:59],v[248:251],v[196:199],a[56:59]\n"
"v_subrev_u32 v102,s82,v102\n"
"s_add_u32 m0,s78,32768\n"
"v_mfma_f32_16x16x32_bf16 a[60:63],v[252:255],v[196:199],a[60:63]\n"
"buffer_load_dwordx4 v112,s[24:27],s76 offen offset:0 sc1 lds\n"
"v_mfma_f32_16x16x32_bf16 a[60:63],v[188:191],v[132:135],a[60:63]\n"
"v_mfma_f32_16x16x32_bf16 a[64:67],v[160:163],v[136:139],a[64:67]\n"
"v_mfma_f32_16x16x32_bf16 a[64:67],v[224:227],v[200:203],a[64:67]\n"
"v_subrev_u32 v103,s82,v103\n"
"v_mfma_f32_16x16x32_bf16 a[68:71],v[228:231],v[200:203],a[68:71]\n"
"buffer_load_dwordx4 v112,s[24:27],s76 offen offset:1024 sc1 lds\n"
"v_mfma_f32_16x16x32_bf16 a[68:71],v[164:167],v[136:139],a[68:71]\n"
"v_mfma_f32_16x16x32_bf16 a[72:75],v[168:171],v[136:139],a[72:75]\n"
"v_accvgpr_read_b32 v116,a0\n"
"v_accvgpr_read_b32 v117,a1\n"
"v_mfma_f32_16x16x32_bf16 a[72:75],v[232:235],v[200:203],a[72:75]\n"
"v_subrev_u32 v104,s82,v104\n"
"v_accvgpr_read_b32 v118,a2\n"
"v_mfma_f32_16x16x32_bf16 a[76:79],v[236:239],v[200:203],a[76:79]\n"
"buffer_load_dwordx4 v112,s[24:27],s76 offen offset:2048 sc1 lds\n"
"v_mfma_f32_16x16x32_bf16 a[76:79],v[172:175],v[136:139],a[76:79]\n"
"v_accvgpr_read_b32 v119,a3\n"
"v_accvgpr_read_b32 v120,a4\n"
"v_mfma_f32_16x16x32_bf16 a[80:83],v[176:179],v[136:139],a[80:83]\n"
"v_accvgpr_read_b32 v121,a5\n"
"v_accvgpr_read_b32 v122,a6\n"
"v_mfma_f32_16x16x32_bf16 a[80:83],v[240:243],v[200:203],a[80:83]\n"
"v_subrev_u32 v105,s82,v105\n"
"v_accvgpr_read_b32 v123,a7\n"
"v_mfma_f32_16x16x32_bf16 a[84:87],v[244:247],v[200:203],a[84:87]\n"
"buffer_load_dwordx4 v112,s[24:27],s76 offen offset:3072 sc1 lds\n"
"v_mfma_f32_16x16x32_bf16 a[84:87],v[180:183],v[136:139],a[84:87]\n"
"s_xor_b32 s76,s76,16384\n"
"v_cvt_pk_bf16_f32 v116,v116,v117\n"
"v_cvt_pk_bf16_f32 v117,v118,v119\n"
"v_mfma_f32_16x16x32_bf16 a[88:91],v[184:187],v[136:139],a[88:91]\n"
"v_cvt_pk_bf16_f32 v118,v120,v121\n"
"v_cvt_pk_bf16_f32 v119,v122,v123\n"
"v_mfma_f32_16x16x32_bf16 a[88:91],v[248:251],v[200:203],a[88:91]\n"
"buffer_store_dwordx4 v[116:119],v112,s[24:27],s56 offen offset:0 sc1\n"
"v_mfma_f32_16x16x32_bf16 a[92:95],v[252:255],v[200:203],a[92:95]\n"
"s_nop 0\n"
"v_accvgpr_read_b32 v116,a8\n"
"v_accvgpr_read_b32 v117,a9\n"
"v_accvgpr_read_b32 v118,a10\n"
"v_mfma_f32_16x16x32_bf16 a[92:95],v[188:191],v[136:139],a[92:95]\n"
"s_waitcnt vmcnt(20)\n"
"v_accvgpr_read_b32 v119,a11\n"
"v_accvgpr_read_b32 v120,a12\n"
"s_barrier\n"
"v_mfma_f32_16x16x32_bf16 a[96:99],v[160:163],v[140:143],a[96:99]\n"
"ds_read_b128 v[0:3],v97 offset:0\n"
"v_accvgpr_read_b32 v121,a13\n"
"v_mfma_f32_16x16x32_bf16 a[96:99],v[224:227],v[204:207],a[96:99]\n"
"ds_read_b128 v[8:11],v97 offset:1024\n"
"v_accvgpr_read_b32 v122,a14\n"
"v_mfma_f32_16x16x32_bf16 a[100:103],v[228:231],v[204:207],a[100:103]\n"
"ds_read_b128 v[16:19],v97 offset:2048\n"
"v_accvgpr_read_b32 v123,a15\n"
"v_mfma_f32_16x16x32_bf16 a[100:103],v[164:167],v[140:143],a[100:103]\n"
"ds_read_b128 v[24:27],v97 offset:3072\n"
"v_cvt_pk_bf16_f32 v116,v116,v117\n"
"v_mfma_f32_16x16x32_bf16 a[104:107],v[168:171],v[140:143],a[104:107]\n"
"ds_read_b128 v[4:7],v97 offset:16384\n"
"v_cvt_pk_bf16_f32 v117,v118,v119\n"
"v_mfma_f32_16x16x32_bf16 a[104:107],v[232:235],v[204:207],a[104:107]\n"
"ds_read_b128 v[12:15],v97 offset:17408\n"
"v_cvt_pk_bf16_f32 v118,v120,v121\n"
"v_mfma_f32_16x16x32_bf16 a[108:111],v[236:239],v[204:207],a[108:111]\n"
"ds_read_b128 v[20:23],v97 offset:18432\n"
"v_cvt_pk_bf16_f32 v119,v122,v123\n"
"v_mfma_f32_16x16x32_bf16 a[108:111],v[172:175],v[140:143],a[108:111]\n"
"ds_read_b128 v[28:31],v97 offset:19456\n"
"buffer_store_dwordx4 v[116:119],v112,s[24:27],s56 offen offset:1024 sc1\n"
"v_mfma_f32_16x16x32_bf16 a[112:115],v[176:179],v[140:143],a[112:115]\n"
"ds_read_b64_tr_b16 v[32:33],v98 offset:49152\n"
"v_accvgpr_read_b32 v116,a16\n"
"v_mfma_f32_16x16x32_bf16 a[112:115],v[240:243],v[204:207],a[112:115]\n"
"ds_read_b64_tr_b16 v[34:35],v98 offset:50176\n"
"v_accvgpr_read_b32 v117,a17\n"
"v_mfma_f32_16x16x32_bf16 a[116:119],v[244:247],v[204:207],a[116:119]\n"
"ds_read_b64_tr_b16 v[36:37],v98 offset:57344\n"
"v_accvgpr_read_b32 v118,a18\n"
"v_mfma_f32_16x16x32_bf16 a[116:119],v[180:183],v[140:143],a[116:119]\n"
"ds_read_b64_tr_b16 v[38:39],v98 offset:58368\n"
"v_accvgpr_read_b32 v119,a19\n"
"v_mfma_f32_16x16x32_bf16 a[120:123],v[184:187],v[140:143],a[120:123]\n"
"ds_read_b64_tr_b16 v[40:41],v99 offset:49152\n"
"v_accvgpr_read_b32 v120,a20\n"
"v_mfma_f32_16x16x32_bf16 a[120:123],v[248:251],v[204:207],a[120:123]\n"
"ds_read_b64_tr_b16 v[42:43],v99 offset:50176\n"
"v_accvgpr_read_b32 v121,a21\n"
"v_mfma_f32_16x16x32_bf16 a[124:127],v[252:255],v[204:207],a[124:127]\n"
"ds_read_b64_tr_b16 v[44:45],v99 offset:57344\n"
"v_accvgpr_read_b32 v122,a22\n"
"v_mfma_f32_16x16x32_bf16 a[124:127],v[188:191],v[140:143],a[124:127]\n"
"ds_read_b64_tr_b16 v[46:47],v99 offset:58368\n"
"v_accvgpr_read_b32 v123,a23\n"
"v_mfma_f32_16x16x32_bf16 a[128:131],v[160:163],v[144:147],a[128:131]\n"
"ds_read_b64_tr_b16 v[48:49],v100 offset:49152\n"
"v_cvt_pk_bf16_f32 v116,v116,v117\n"
"v_mfma_f32_16x16x32_bf16 a[128:131],v[224:227],v[208:211],a[128:131]\n"
"ds_read_b64_tr_b16 v[50:51],v100 offset:50176\n"
"v_cvt_pk_bf16_f32 v117,v118,v119\n"
"v_mfma_f32_16x16x32_bf16 a[132:135],v[228:231],v[208:211],a[132:135]\n"
"ds_read_b64_tr_b16 v[52:53],v100 offset:57344\n"
"v_cvt_pk_bf16_f32 v118,v120,v121\n"
"v_mfma_f32_16x16x32_bf16 a[132:135],v[164:167],v[144:147],a[132:135]\n"
"ds_read_b64_tr_b16 v[54:55],v100 offset:58368\n"
"v_cvt_pk_bf16_f32 v119,v122,v123\n"
"v_mfma_f32_16x16x32_bf16 a[136:139],v[168:171],v[144:147],a[136:139]\n"
"ds_read_b64_tr_b16 v[56:57],v101 offset:49152\n"
"buffer_store_dwordx4 v[116:119],v112,s[24:27],s56 offen offset:2048 sc1\n"
"v_mfma_f32_16x16x32_bf16 a[136:139],v[232:235],v[208:211],a[136:139]\n"
"ds_read_b64_tr_b16 v[58:59],v101 offset:50176\n"
"v_accvgpr_read_b32 v116,a24\n"
"v_mfma_f32_16x16x32_bf16 a[140:143],v[236:239],v[208:211],a[140:143]\n"
"ds_read_b64_tr_b16 v[60:61],v101 offset:57344\n"
"v_accvgpr_read_b32 v117,a25\n"
"v_mfma_f32_16x16x32_bf16 a[140:143],v[172:175],v[144:147],a[140:143]\n"
"ds_read_b64_tr_b16 v[62:63],v101 offset:58368\n"
"v_accvgpr_read_b32 v118,a26\n"
"v_mfma_f32_16x16x32_bf16 a[144:147],v[176:179],v[144:147],a[144:147]\n"
"ds_read_b64_tr_b16 v[64:65],v102 offset:49152\n"
"v_accvgpr_read_b32 v119,a27\n"
"v_mfma_f32_16x16x32_bf16 a[144:147],v[240:243],v[208:211],a[144:147]\n"
"ds_read_b64_tr_b16 v[66:67],v102 offset:50176\n"
"v_accvgpr_read_b32 v120,a28\n"
"v_mfma_f32_16x16x32_bf16 a[148:151],v[244:247],v[208:211],a[148:151]\n"
"ds_read_b64_tr_b16 v[68:69],v102 offset:57344\n"
"v_accvgpr_read_b32 v121,a29\n"
"v_mfma_f32_16x16x32_bf16 a[148:151],v[180:183],v[144:147],a[148:151]\n"
"ds_read_b64_tr_b16 v[70:71],v102 offset:58368\n"
"v_accvgpr_read_b32 v122,a30\n"
"v_mfma_f32_16x16x32_bf16 a[152:155],v[184:187],v[144:147],a[152:155]\n"
"ds_read_b64_tr_b16 v[72:73],v103 offset:49152\n"
"v_accvgpr_read_b32 v123,a31\n"
"v_mfma_f32_16x16x32_bf16 a[152:155],v[248:251],v[208:211],a[152:155]\n"
"ds_read_b64_tr_b16 v[74:75],v103 offset:50176\n"
"v_cvt_pk_bf16_f32 v116,v116,v117\n"
"v_mfma_f32_16x16x32_bf16 a[156:159],v[252:255],v[208:211],a[156:159]\n"
"ds_read_b64_tr_b16 v[76:77],v103 offset:57344\n"
"v_cvt_pk_bf16_f32 v117,v118,v119\n"
"v_mfma_f32_16x16x32_bf16 a[156:159],v[188:191],v[144:147],a[156:159]\n"
"ds_read_b64_tr_b16 v[78:79],v103 offset:58368\n"
"v_cvt_pk_bf16_f32 v118,v120,v121\n"
"v_mfma_f32_16x16x32_bf16 a[160:163],v[160:163],v[148:151],a[160:163]\n"
"ds_read_b64_tr_b16 v[80:81],v104 offset:49152\n"
"v_cvt_pk_bf16_f32 v119,v122,v123\n"
"v_mfma_f32_16x16x32_bf16 a[160:163],v[224:227],v[212:215],a[160:163]\n"
"ds_read_b64_tr_b16 v[82:83],v104 offset:50176\n"
"buffer_store_dwordx4 v[116:119],v112,s[24:27],s56 offen offset:3072 sc1\n"
"v_mfma_f32_16x16x32_bf16 a[164:167],v[228:231],v[212:215],a[164:167]\n"
"ds_read_b64_tr_b16 v[84:85],v104 offset:57344\n"
"v_mfma_f32_16x16x32_bf16 a[164:167],v[164:167],v[148:151],a[164:167]\n"
"ds_read_b64_tr_b16 v[86:87],v104 offset:58368\n"
"v_mfma_f32_16x16x32_bf16 a[168:171],v[168:171],v[148:151],a[168:171]\n"
"ds_read_b64_tr_b16 v[88:89],v105 offset:49152\n"
"v_mfma_f32_16x16x32_bf16 a[168:171],v[232:235],v[212:215],a[168:171]\n"
"ds_read_b64_tr_b16 v[90:91],v105 offset:50176\n"
"v_mfma_f32_16x16x32_bf16 a[172:175],v[236:239],v[212:215],a[172:175]\n"
"ds_read_b64_tr_b16 v[92:93],v105 offset:57344\n"
"v_mfma_f32_16x16x32_bf16 a[172:175],v[172:175],v[148:151],a[172:175]\n"
"ds_read_b64_tr_b16 v[94:95],v105 offset:58368\n"
"v_mfma_f32_16x16x32_bf16 a[176:179],v[176:179],v[148:151],a[176:179]\n"
"s_add_u32 m0,s85,106496\n"
"v_mfma_f32_16x16x32_bf16 a[176:179],v[240:243],v[212:215],a[176:179]\n"
"buffer_load_dwordx4 v110,s[28:31],s86 offen lds\n"
"v_mfma_f32_16x16x32_bf16 a[180:183],v[244:247],v[212:215],a[180:183]\n"
"v_mfma_f32_16x16x32_bf16 a[180:183],v[180:183],v[148:151],a[180:183]\n"
"s_add_u32 m0,s85,107520\n"
"v_mfma_f32_16x16x32_bf16 a[184:187],v[184:187],v[148:151],a[184:187]\n"
"buffer_load_dwordx4 v111,s[28:31],s86 offen lds\n"
"v_mfma_f32_16x16x32_bf16 a[184:187],v[248:251],v[212:215],a[184:187]\n"
"s_xor_b32 s86,s86,786432\n"
"v_mfma_f32_16x16x32_bf16 a[188:191],v[252:255],v[212:215],a[188:191]\n"
"s_add_u32 m0,s78,49152\n"
"v_mfma_f32_16x16x32_bf16 a[188:191],v[188:191],v[148:151],a[188:191]\n"
"buffer_load_dwordx4 v112,s[24:27],s76 offen offset:0 sc1 lds\n"
"v_mfma_f32_16x16x32_bf16 a[192:195],v[224:227],v[216:219],a[192:195]\n"
"v_mfma_f32_16x16x32_bf16 a[224:227],v[224:227],v[220:223],a[224:227]\n"
"v_mfma_f32_16x16x32_bf16 a[228:231],v[228:231],v[220:223],a[228:231]\n"
"buffer_load_dwordx4 v112,s[24:27],s76 offen offset:1024 sc1 lds\n"
"v_mfma_f32_16x16x32_bf16 a[196:199],v[228:231],v[216:219],a[196:199]\n"
"v_mfma_f32_16x16x32_bf16 a[200:203],v[232:235],v[216:219],a[200:203]\n"
"v_mfma_f32_16x16x32_bf16 a[232:235],v[232:235],v[220:223],a[232:235]\n"
"buffer_load_dwordx4 v112,s[24:27],s76 offen offset:2048 sc1 lds\n"
"v_mfma_f32_16x16x32_bf16 a[236:239],v[236:239],v[220:223],a[236:239]\n"
"v_mfma_f32_16x16x32_bf16 a[204:207],v[236:239],v[216:219],a[204:207]\n"
"buffer_load_dwordx4 v112,s[24:27],s76 offen offset:3072 sc1 lds\n"
"v_mfma_f32_16x16x32_bf16 a[208:211],v[240:243],v[216:219],a[208:211]\n"
"s_xor_b32 s76,s76,49152\n"
"v_mfma_f32_16x16x32_bf16 a[240:243],v[240:243],v[220:223],a[240:243]\n"
"v_mfma_f32_16x16x32_bf16 a[244:247],v[244:247],v[220:223],a[244:247]\n"
"v_mfma_f32_16x16x32_bf16 a[212:215],v[244:247],v[216:219],a[212:215]\n"
"v_mfma_f32_16x16x32_bf16 a[216:219],v[248:251],v[216:219],a[216:219]\n"
"v_mfma_f32_16x16x32_bf16 a[248:251],v[248:251],v[220:223],a[248:251]\n"
"v_mfma_f32_16x16x32_bf16 a[252:255],v[252:255],v[220:223],a[252:255]\n"
"v_mfma_f32_16x16x32_bf16 a[220:223],v[252:255],v[216:219],a[220:223]\n"
"s_branch .Lfk_ph2\n"
".Lfk_v:\n"
"s_waitcnt vmcnt(11)\n"
"s_barrier\n"
"ds_read_b128 v[152:155],v80 offset:12288\n"
"ds_read_b128 v[156:159],v80 offset:14336\n"
"ds_read_b64_tr_b16 v[160:161],v98 offset:49152\n"
"ds_read_b64_tr_b16 v[162:163],v98 offset:50176\n"
"ds_read_b64_tr_b16 v[164:165],v99 offset:49152\n"
"ds_read_b64_tr_b16 v[166:167],v99 offset:50176\n"
"ds_read_b64_tr_b16 v[168:169],v100 offset:49152\n"
"ds_read_b64_tr_b16 v[170:171],v100 offset:50176\n"
"ds_read_b64_tr_b16 v[172:173],v101 offset:49152\n"
"ds_read_b64_tr_b16 v[174:175],v101 offset:50176\n"
"ds_read_b64_tr_b16 v[176:177],v102 offset:49152\n"
"ds_read_b64_tr_b16 v[178:179],v102 offset:50176\n"
"ds_read_b64_tr_b16 v[180:181],v103 offset:49152\n"
"ds_read_b64_tr_b16 v[182:183],v103 offset:50176\n"
"ds_read_b64_tr_b16 v[184:185],v104 offset:49152\n"
"ds_read_b64_tr_b16 v[186:187],v104 offset:50176\n"
"ds_read_b64_tr_b16 v[188:189],v105 offset:49152\n"
"ds_read_b64_tr_b16 v[190:191],v105 offset:50176\n"
"s_waitcnt lgkmcnt(12)\n"
"v_mfma_f32_16x16x32_bf16 a[192:195],v[152:155],v[160:163],0\n"
"s_and_b32 s91,s89,7\n"
"s_mov_b32 s76,128\n"
"s_add_u32 m0,s79,32768\n"
"v_mfma_f32_16x16x32_bf16 a[196:199],v[156:159],v[160:163],0\n"
"buffer_load_dwordx4 v106,s[16:19],s76 offen offset:0 lds\n"
"v_mfma_f32_16x16x32_bf16 a[204:207],v[156:159],v[164:167],0\n"
"s_lshr_b32 s97,s89,3\n"
"s_lshl2_add_u32 s90,s88,s97\n"
"v_mfma_f32_16x16x32_bf16 a[200:203],v[152:155],v[164:167],0\n"
"buffer_load_dwordx4 v107,s[16:19],s76 offen offset:1024 lds\n"
"s_waitcnt lgkmcnt(8)\n"
"v_mfma_f32_16x16x32_bf16 a[208:211],v[152:155],v[168:171],0\n"
"s_lshl_b32 vcc_lo,s90,8\n"
"s_add_u32 s28,s28,vcc_lo\n"
"s_addc_u32 s29,s29,0\n"
"v_mfma_f32_16x16x32_bf16 a[212:215],v[156:159],v[168:171],0\n"
"buffer_load_dwordx4 v108,s[16:19],s76 offen offset:2048 lds\n"
"v_mfma_f32_16x16x32_bf16 a[220:223],v[156:159],v[172:175],0\n"
"s_sub_u32 s30,8388608,vcc_lo\n"
"s_mov_b32 s31,0x20000\n"
"buffer_load_dwordx4 v109,s[16:19],s76 offen offset:3072 lds\n"
"v_mfma_f32_16x16x32_bf16 a[216:219],v[152:155],v[172:175],0\n"
"s_waitcnt vmcnt(9)\n"
"s_barrier\n"
"s_waitcnt lgkmcnt(4)\n"
"v_mfma_f32_16x16x32_bf16 a[224:227],v[152:155],v[176:179],0\n"
"ds_read_b128 v[128:131],v80 offset:0\n"
"ds_read_b128 v[192:195],v81 offset:0\n"
"v_mfma_f32_16x16x32_bf16 a[228:231],v[156:159],v[176:179],0\n"
"ds_read_b64_tr_b16 v[224:225],v98 offset:32768\n"
"ds_read_b64_tr_b16 v[226:227],v98 offset:33792\n"
"v_mfma_f32_16x16x32_bf16 a[236:239],v[156:159],v[180:183],0\n"
"ds_read_b64_tr_b16 v[228:229],v99 offset:32768\n"
"ds_read_b64_tr_b16 v[230:231],v99 offset:33792\n"
"v_mfma_f32_16x16x32_bf16 a[232:235],v[152:155],v[180:183],0\n"
"ds_read_b64_tr_b16 v[232:233],v100 offset:32768\n"
"ds_read_b64_tr_b16 v[234:235],v100 offset:33792\n"
"s_waitcnt lgkmcnt(8)\n"
"v_mfma_f32_16x16x32_bf16 a[240:243],v[152:155],v[184:187],0\n"
"ds_read_b64_tr_b16 v[236:237],v101 offset:32768\n"
"ds_read_b64_tr_b16 v[238:239],v101 offset:33792\n"
"v_mfma_f32_16x16x32_bf16 a[244:247],v[156:159],v[184:187],0\n"
"ds_read_b64_tr_b16 v[240:241],v102 offset:32768\n"
"ds_read_b64_tr_b16 v[242:243],v102 offset:33792\n"
"v_mfma_f32_16x16x32_bf16 a[252:255],v[156:159],v[188:191],0\n"
"ds_read_b64_tr_b16 v[244:245],v103 offset:32768\n"
"ds_read_b64_tr_b16 v[246:247],v103 offset:33792\n"
"v_mfma_f32_16x16x32_bf16 a[248:251],v[152:155],v[188:191],0\n"
"ds_read_b64_tr_b16 v[248:249],v104 offset:32768\n"
"ds_read_b64_tr_b16 v[250:251],v104 offset:33792\n"
"s_waitcnt lgkmcnt(10)\n"
"v_mfma_f32_16x16x32_bf16 a[0:3],v[128:131],v[160:163],0\n"
"ds_read_b64_tr_b16 v[252:253],v105 offset:32768\n"
"ds_read_b64_tr_b16 v[254:255],v105 offset:33792\n"
"v_mfma_f32_16x16x32_bf16 a[0:3],v[192:195],v[224:227],a[0:3]\n"
"ds_read_b128 v[132:135],v80 offset:2048\n"
"ds_read_b128 v[196:199],v81 offset:2048\n"
"v_mfma_f32_16x16x32_bf16 a[8:11],v[192:195],v[228:231],0\n"
"ds_read_b128 v[216:219],v81 offset:12288\n"
"ds_read_b128 v[220:223],v81 offset:14336\n"
"v_mfma_f32_16x16x32_bf16 a[8:11],v[128:131],v[164:167],a[8:11]\n"
"s_waitcnt lgkmcnt(12)\n"
"s_mov_b32 s76,262272\n"
"s_add_u32 m0,s79,36864\n"
"v_mfma_f32_16x16x32_bf16 a[16:19],v[128:131],v[168:171],0\n"
"buffer_load_dwordx4 v106,s[16:19],s76 offen offset:0 lds\n"
"v_mfma_f32_16x16x32_bf16 a[16:19],v[192:195],v[232:235],a[16:19]\n"
"buffer_load_dwordx4 v107,s[16:19],s76 offen offset:1024 lds\n"
"v_mfma_f32_16x16x32_bf16 a[24:27],v[192:195],v[236:239],0\n"
"buffer_load_dwordx4 v108,s[16:19],s76 offen offset:2048 lds\n"
"buffer_load_dwordx4 v109,s[16:19],s76 offen offset:3072 lds\n"
"v_mfma_f32_16x16x32_bf16 a[24:27],v[128:131],v[172:175],a[24:27]\n"
"s_waitcnt lgkmcnt(8)\n"
"s_mov_b32 s77,524288\n"
"s_add_u32 m0,s78,98304\n"
"v_mfma_f32_16x16x32_bf16 a[32:35],v[128:131],v[176:179],0\n"
"buffer_load_dwordx4 v110,s[20:23],s77 offen offset:0 lds\n"
"v_mfma_f32_16x16x32_bf16 a[32:35],v[192:195],v[240:243],a[32:35]\n"
"buffer_load_dwordx4 v111,s[20:23],s77 offen offset:1024 lds\n"
"v_mfma_f32_16x16x32_bf16 a[40:43],v[192:195],v[244:247],0\n"
"buffer_load_dwordx4 v113,s[20:23],s77 offen offset:2048 lds\n"
"buffer_load_dwordx4 v114,s[20:23],s77 offen offset:3072 lds\n"
"v_mfma_f32_16x16x32_bf16 a[40:43],v[128:131],v[180:183],a[40:43]\n"
"s_waitcnt lgkmcnt(4)\n"
"s_mov_b32 s77,786432\n"
"s_add_u32 m0,s78,114688\n"
"v_mfma_f32_16x16x32_bf16 a[48:51],v[128:131],v[184:187],0\n"
"buffer_load_dwordx4 v110,s[20:23],s77 offen offset:0 lds\n"
"v_mfma_f32_16x16x32_bf16 a[48:51],v[192:195],v[248:251],a[48:51]\n"
"buffer_load_dwordx4 v111,s[20:23],s77 offen offset:1024 lds\n"
"v_mfma_f32_16x16x32_bf16 a[56:59],v[192:195],v[252:255],0\n"
"buffer_load_dwordx4 v113,s[20:23],s77 offen offset:2048 lds\n"
"buffer_load_dwordx4 v114,s[20:23],s77 offen offset:3072 lds\n"
"v_mfma_f32_16x16x32_bf16 a[56:59],v[128:131],v[188:191],a[56:59]\n"
"s_waitcnt lgkmcnt(2)\n"
"s_mov_b32 s76,256\n"
"s_add_u32 m0,s79,65536\n"
"v_mfma_f32_16x16x32_bf16 a[4:7],v[132:135],v[160:163],0\n"
"buffer_load_dwordx4 v106,s[16:19],s76 offen offset:0 lds\n"
"v_mfma_f32_16x16x32_bf16 a[4:7],v[196:199],v[224:227],a[4:7]\n"
"buffer_load_dwordx4 v107,s[16:19],s76 offen offset:1024 lds\n"
"v_mfma_f32_16x16x32_bf16 a[12:15],v[196:199],v[228:231],0\n"
"buffer_load_dwordx4 v108,s[16:19],s76 offen offset:2048 lds\n"
"buffer_load_dwordx4 v109,s[16:19],s76 offen offset:3072 lds\n"
"v_mfma_f32_16x16x32_bf16 a[12:15],v[132:135],v[164:167],a[12:15]\n"
"s_waitcnt vmcnt(21)\n"
"s_barrier\n"
"v_mfma_f32_16x16x32_bf16 a[20:23],v[132:135],v[168:171],0\n"
"ds_read_b128 v[136:139],v80 offset:4096\n"
"ds_read_b128 v[200:203],v81 offset:4096\n"
"v_mfma_f32_16x16x32_bf16 a[20:23],v[196:199],v[232:235],a[20:23]\n"
"ds_read_b128 v[140:143],v80 offset:6144\n"
"ds_read_b128 v[204:207],v81 offset:6144\n"
"v_mfma_f32_16x16x32_bf16 a[28:31],v[196:199],v[236:239],0\n"
"ds_read_b128 v[144:147],v80 offset:8192\n"
"ds_read_b128 v[208:211],v81 offset:8192\n"
"v_mfma_f32_16x16x32_bf16 a[28:31],v[132:135],v[172:175],a[28:31]\n"
"ds_read_b128 v[148:151],v80 offset:10240\n"
"ds_read_b128 v[212:215],v81 offset:10240\n"
"v_mfma_f32_16x16x32_bf16 a[36:39],v[132:135],v[176:179],0\n"
"s_mov_b32 s60,s91\n"
"s_mov_b32 s76,262400\n"
"s_add_u32 m0,s79,69632\n"
"v_mfma_f32_16x16x32_bf16 a[36:39],v[196:199],v[240:243],a[36:39]\n"
"buffer_load_dwordx4 v106,s[16:19],s76 offen offset:0 lds\n"
"v_mfma_f32_16x16x32_bf16 a[44:47],v[196:199],v[244:247],0\n"
"buffer_load_dwordx4 v107,s[16:19],s76 offen offset:1024 lds\n"
"v_mfma_f32_16x16x32_bf16 a[44:47],v[132:135],v[180:183],a[44:47]\n"
"buffer_load_dwordx4 v108,s[16:19],s76 offen offset:2048 lds\n"
"v_mfma_f32_16x16x32_bf16 a[52:55],v[132:135],v[184:187],0\n"
"buffer_load_dwordx4 v109,s[16:19],s76 offen offset:3072 lds\n"
"v_mfma_f32_16x16x32_bf16 a[52:55],v[196:199],v[248:251],a[52:55]\n"
"s_sub_u32 s94,15,s91\n"
"s_waitcnt lgkmcnt(0)\n"
"s_barrier\n"
"v_mfma_f32_16x16x32_bf16 a[60:63],v[196:199],v[252:255],0\n"
"s_cmp_lt_u32 s52,2\n"
"s_cselect_b32 s95,s94,s91\n"
"s_mov_b32 s77,1048576\n"
"s_add_u32 m0,s78,147456\n"
"v_mfma_f32_16x16x32_bf16 a[60:63],v[132:135],v[188:191],a[60:63]\n"
"buffer_load_dwordx4 v110,s[20:23],s77 offen offset:0 lds\n"
"v_mfma_f32_16x16x32_bf16 a[64:67],v[136:139],v[160:163],0\n"
"s_lshr_b32 vcc_lo,s95,1\n"
"s_lshl_b32 s55,vcc_lo,19\n"
"v_mfma_f32_16x16x32_bf16 a[64:67],v[200:203],v[224:227],a[64:67]\n"
"s_lshl_b32 s58,vcc_lo,10\n"
"s_add_u32 s58,s58,148898816\n"
"v_mfma_f32_16x16x32_bf16 a[72:75],v[200:203],v[228:231],0\n"
"s_and_b32 vcc_lo,s95,1\n"
"s_lshl_b32 vcc_lo,vcc_lo,13\n"
"v_mfma_f32_16x16x32_bf16 a[72:75],v[136:139],v[164:167],a[72:75]\n"
"buffer_load_dwordx4 v111,s[20:23],s77 offen offset:1024 lds\n"
"v_mfma_f32_16x16x32_bf16 a[80:83],v[136:139],v[168:171],0\n"
"s_and_b32 vcc_hi,s78,4096\n"
"s_add_u32 s55,s55,vcc_lo\n"
"v_mfma_f32_16x16x32_bf16 a[80:83],v[200:203],v[232:235],a[80:83]\n"
"s_add_u32 s55,s55,vcc_hi\n"
"s_and_b32 vcc_lo,s89,2\n"
"v_mfma_f32_16x16x32_bf16 a[88:91],v[200:203],v[236:239],0\n"
"s_lshl2_add_u32 vcc_lo,s88,vcc_lo\n"
"s_and_b32 vcc_hi,s52,1\n"
"v_mfma_f32_16x16x32_bf16 a[88:91],v[136:139],v[172:175],a[88:91]\n"
"buffer_load_dwordx4 v113,s[20:23],s77 offen offset:2048 lds\n"
"v_mfma_f32_16x16x32_bf16 a[96:99],v[136:139],v[176:179],0\n"
"s_add_u32 vcc_lo,vcc_lo,vcc_hi\n"
"s_lshr_b32 vcc_hi,s89,2\n"
"v_mfma_f32_16x16x32_bf16 a[96:99],v[200:203],v[240:243],a[96:99]\n"
"s_lshr_b32 s84,s52,1\n"
"s_lshl1_add_u32 vcc_hi,vcc_hi,s84\n"
"v_mfma_f32_16x16x32_bf16 a[104:107],v[200:203],v[244:247],0\n"
"s_mul_i32 s56,vcc_lo,1179648\n"
"s_lshl_b32 s84,s84,19\n"
"v_mfma_f32_16x16x32_bf16 a[104:107],v[136:139],v[180:183],a[104:107]\n"
"buffer_load_dwordx4 v114,s[20:23],s77 offen offset:3072 lds\n"
"v_mfma_f32_16x16x32_bf16 a[112:115],v[136:139],v[184:187],0\n"
"s_add_u32 s56,s56,s84\n"
"s_lshr_b32 s84,s89,2\n"
"v_mfma_f32_16x16x32_bf16 a[112:115],v[200:203],v[248:251],a[112:115]\n"
"s_lshl_b32 s84,s84,14\n"
"s_add_u32 s56,s56,s84\n"
"v_mfma_f32_16x16x32_bf16 a[120:123],v[200:203],v[252:255],0\n"
"s_lshl_b32 s84,s53,13\n"
"s_mov_b32 s77,1310720\n"
"s_add_u32 m0,s78,131072\n"
"v_mfma_f32_16x16x32_bf16 a[120:123],v[136:139],v[188:191],a[120:123]\n"
"buffer_load_dwordx4 v110,s[20:23],s77 offen offset:0 lds\n"
"v_mfma_f32_16x16x32_bf16 a[68:71],v[140:143],v[160:163],0\n"
"s_add_u32 s56,s56,s84\n"
"s_add_u32 s56,s56,4194304\n"
"v_mfma_f32_16x16x32_bf16 a[68:71],v[204:207],v[224:227],a[68:71]\n"
"s_lshl4_add_u32 vcc_hi,s53,vcc_hi\n"
"s_lshl_b32 vcc_lo,vcc_lo,8\n"
"v_mfma_f32_16x16x32_bf16 a[76:79],v[204:207],v[228:231],0\n"
"buffer_load_dwordx4 v111,s[20:23],s77 offen offset:1024 lds\n"
"v_mfma_f32_16x16x32_bf16 a[76:79],v[140:143],v[164:167],a[76:79]\n"
"s_lshl3_add_u32 s59,vcc_hi,vcc_lo\n"
"s_add_u32 s59,s59,148914176\n"
"v_mfma_f32_16x16x32_bf16 a[84:87],v[140:143],v[168:171],0\n"
"s_lshl_b32 s57,s92,3\n"
"s_add_u32 s57,s57,148898816\n"
"v_mfma_f32_16x16x32_bf16 a[84:87],v[204:207],v[232:235],a[84:87]\n"
"s_lshl_b32 s63,s90,8\n"
"s_add_u32 s63,s63,148914176\n"
"v_mfma_f32_16x16x32_bf16 a[92:95],v[204:207],v[236:239],0\n"
"buffer_load_dwordx4 v113,s[20:23],s77 offen offset:2048 lds\n"
"v_mfma_f32_16x16x32_bf16 a[92:95],v[140:143],v[172:175],a[92:95]\n"
"s_lshl_b32 s74,s92,11\n"
"s_add_u32 s74,s74,171966464\n"
"v_mfma_f32_16x16x32_bf16 a[100:103],v[140:143],v[176:179],0\n"
"s_mul_i32 s96,s90,1179648\n"
"s_add_u32 s96,s96,4194304\n"
"v_mfma_f32_16x16x32_bf16 a[100:103],v[204:207],v[240:243],a[100:103]\n"
"s_add_u32 s73,s96,s85\n"
"s_and_b32 vcc_hi,s78,4096\n"
"v_mfma_f32_16x16x32_bf16 a[108:111],v[204:207],v[244:247],0\n"
"buffer_load_dwordx4 v114,s[20:23],s77 offen offset:3072 lds\n"
"v_mfma_f32_16x16x32_bf16 a[108:111],v[140:143],v[180:183],a[108:111]\n"
"s_lshl_b32 vcc_hi,vcc_hi,1\n"
"s_add_u32 vcc_lo,s96,vcc_hi\n"
"v_mfma_f32_16x16x32_bf16 a[116:119],v[140:143],v[184:187],0\n"
"s_add_u32 s65,vcc_lo,524288\n"
"s_lshl_b32 s66,s91,16\n"
"v_mfma_f32_16x16x32_bf16 a[116:119],v[204:207],v[248:251],a[116:119]\n"
"s_sub_u32 s66,vcc_lo,s66\n"
"s_mov_b32 s76,384\n"
"s_add_u32 m0,s79,0\n"
"v_mfma_f32_16x16x32_bf16 a[124:127],v[204:207],v[252:255],0\n"
"buffer_load_dwordx4 v106,s[16:19],s76 offen offset:0 lds\n"
"v_mfma_f32_16x16x32_bf16 a[124:127],v[140:143],v[188:191],a[124:127]\n"
"s_add_u32 s66,s66,999424\n"
"s_add_u32 s67,vcc_hi,65536\n"
"v_mfma_f32_16x16x32_bf16 a[128:131],v[144:147],v[160:163],0\n"
"s_mul_i32 vcc_lo,vcc_hi,5\n"
"s_sub_u32 vcc_lo,s96,vcc_lo\n"
"v_mfma_f32_16x16x32_bf16 a[128:131],v[208:211],v[224:227],a[128:131]\n"
"s_add_u32 s64,vcc_lo,81920\n"
"s_add_u32 s62,vcc_lo,135168\n"
"s_add_u32 s61,vcc_lo,524288\n"
"v_mfma_f32_16x16x32_bf16 a[136:139],v[208:211],v[228:231],0\n"
"buffer_load_dwordx4 v107,s[16:19],s76 offen offset:1024 lds\n"
"v_mfma_f32_16x16x32_bf16 a[136:139],v[144:147],v[164:167],a[136:139]\n"
"s_cmp_lt_u32 s52,2\n"
"s_cselect_b32 s64,s64,2146435072\n"
"s_cselect_b32 s67,s67,163840\n"
"s_cselect_b32 s65,s65,1\n"
"v_mfma_f32_16x16x32_bf16 a[144:147],v[144:147],v[168:171],0\n"
"s_cselect_b32 vcc_lo,4,0\n"
"s_and_b32 vcc_hi,s52,1\n"
"s_add_u32 vcc_lo,vcc_lo,vcc_hi\n"
"v_mfma_f32_16x16x32_bf16 a[144:147],v[208:211],v[232:235],a[144:147]\n"
"s_lshl_b32 vcc_hi,s90,12\n"
"s_add_u32 vcc_hi,vcc_hi,54525952\n"
"v_mfma_f32_16x16x32_bf16 a[152:155],v[208:211],v[236:239],0\n"
"buffer_load_dwordx4 v108,s[16:19],s76 offen offset:2048 lds\n"
"v_mfma_f32_16x16x32_bf16 a[152:155],v[144:147],v[172:175],a[152:155]\n"
"s_lshl3_add_u32 s70,s95,vcc_lo\n"
"s_lshl_b32 s70,s70,17\n"
"v_mfma_f32_16x16x32_bf16 a[160:163],v[144:147],v[176:179],0\n"
"s_add_u32 s70,s70,vcc_hi\n"
"s_lshl3_add_u32 s68,s94,vcc_lo\n"
"v_mfma_f32_16x16x32_bf16 a[160:163],v[208:211],v[240:243],a[160:163]\n"
"s_lshl_b32 s68,s68,17\n"
"s_add_u32 s68,s68,vcc_hi\n"
"v_mfma_f32_16x16x32_bf16 a[168:171],v[208:211],v[244:247],0\n"
"buffer_load_dwordx4 v109,s[16:19],s76 offen offset:3072 lds\n"
"v_mfma_f32_16x16x32_bf16 a[168:171],v[144:147],v[180:183],a[168:171]\n"
"s_lshr_b32 vcc_lo,s90,3\n"
"s_lshl2_add_u32 vcc_lo,vcc_lo,s52\n"
"v_mfma_f32_16x16x32_bf16 a[176:179],v[144:147],v[184:187],0\n"
"s_lshl_b32 vcc_lo,vcc_lo,7\n"
"s_and_b32 vcc_hi,s90,7\n"
"v_mfma_f32_16x16x32_bf16 a[176:179],v[208:211],v[248:251],a[176:179]\n"
"s_add_u32 vcc_lo,vcc_lo,vcc_hi\n"
"s_mov_b32 s76,262528\n"
"s_add_u32 m0,s79,4096\n"
"v_mfma_f32_16x16x32_bf16 a[184:187],v[208:211],v[252:255],0\n"
"buffer_load_dwordx4 v106,s[16:19],s76 offen offset:0 lds\n"
"v_mfma_f32_16x16x32_bf16 a[184:187],v[144:147],v[188:191],a[184:187]\n"
"s_lshl3_add_u32 s69,s91,vcc_lo\n"
"s_lshl_b32 s69,s69,3\n"
"v_mfma_f32_16x16x32_bf16 a[132:135],v[148:151],v[160:163],0\n"
"s_add_u32 s69,s69,148930560\n"
"s_lshl3_add_u32 s71,s94,vcc_lo\n"
"v_mfma_f32_16x16x32_bf16 a[132:135],v[212:215],v[224:227],a[132:135]\n"
"s_lshl_b32 s71,s71,3\n"
"s_add_u32 s71,s71,148930560\n"
"v_mfma_f32_16x16x32_bf16 a[140:143],v[212:215],v[228:231],0\n"
"buffer_load_dwordx4 v107,s[16:19],s76 offen offset:1024 lds\n"
"v_mfma_f32_16x16x32_bf16 a[140:143],v[148:151],v[164:167],a[140:143]\n"
"s_mov_b32 s6,8388608\n"
"s_mov_b32 s7,0x20000\n"
"v_mfma_f32_16x16x32_bf16 a[148:151],v[148:151],v[168:171],0\n"
"s_lshr_b32 s94,s88,1\n"
"s_and_b32 vcc_lo,s91,1\n"
"v_mfma_f32_16x16x32_bf16 a[148:151],v[212:215],v[232:235],a[148:151]\n"
"s_lshl1_add_u32 s95,s97,vcc_lo\n"
"s_and_b32 vcc_lo,s88,1\n"
"v_mfma_f32_16x16x32_bf16 a[156:159],v[212:215],v[236:239],0\n"
"buffer_load_dwordx4 v108,s[16:19],s76 offen offset:2048 lds\n"
"v_mfma_f32_16x16x32_bf16 a[156:159],v[148:151],v[172:175],a[156:159]\n"
"s_lshr_b32 vcc_hi,s91,1\n"
"s_lshl2_add_u32 s96,vcc_lo,vcc_hi\n"
"v_mfma_f32_16x16x32_bf16 a[164:167],v[148:151],v[176:179],0\n"
"s_lshr_b32 vcc_lo,s88,2\n"
"s_lshl2_add_u32 s96,vcc_lo,vcc_hi\n"
"v_mfma_f32_16x16x32_bf16 a[164:167],v[212:215],v[240:243],a[164:167]\n"
"s_sub_u32 vcc_lo,7,s88\n"
"s_min_u32 s94,vcc_lo,s88\n"
"v_mfma_f32_16x16x32_bf16 a[172:175],v[212:215],v[244:247],0\n"
"buffer_load_dwordx4 v109,s[16:19],s76 offen offset:3072 lds\n"
"v_mfma_f32_16x16x32_bf16 a[172:175],v[148:151],v[180:183],a[172:175]\n"
"s_and_b32 s96,s88,4\n"
"s_add_u32 s96,s96,s97\n"
"s_add_u32 s95,s91,s96\n"
"v_mfma_f32_16x16x32_bf16 a[180:183],v[148:151],v[184:187],0\n"
"buffer_load_dwordx4 v[8:11],v70,s[32:35],0 offen offset:0\n"
"v_mfma_f32_16x16x32_bf16 a[180:183],v[212:215],v[248:251],a[180:183]\n"
"buffer_load_dwordx4 v[12:15],v70,s[32:35],0 offen offset:512\n"
"s_and_b32 s95,s95,7\n"
"v_mfma_f32_16x16x32_bf16 a[188:191],v[212:215],v[252:255],0\n"
"buffer_load_dwordx4 v[16:19],v70,s[32:35],0 offen offset:1024\n"
"buffer_load_dwordx4 v[20:23],v70,s[32:35],0 offen offset:1536\n"
"s_lshl3_add_u32 vcc_lo,s94,s96\n"
"v_mfma_f32_16x16x32_bf16 a[188:191],v[148:151],v[188:191],a[188:191]\n"
"s_waitcnt vmcnt(28)\n"
"s_barrier\n"
"v_mfma_f32_16x16x32_bf16 a[192:195],v[216:219],v[224:227],a[192:195]\n"
"ds_read_b128 v[152:155],v80 offset:45056\n"
"s_mov_b32 s87,524288\n"
"v_mfma_f32_16x16x32_bf16 a[196:199],v[220:223],v[224:227],a[196:199]\n"
"ds_read_b128 v[156:159],v80 offset:47104\n"
"buffer_load_dword v115,v112,s[36:39],s87 offen\n"
"v_mfma_f32_16x16x32_bf16 a[204:207],v[220:223],v[228:231],a[204:207]\n"
"ds_read_b64_tr_b16 v[160:161],v98 offset:0\n"
"s_mov_b32 s87,1048576\n"
"v_mfma_f32_16x16x32_bf16 a[200:203],v[216:219],v[228:231],a[200:203]\n"
"ds_read_b64_tr_b16 v[162:163],v98 offset:1024\n"
"buffer_load_dword v115,v112,s[36:39],s87 offen\n"
"v_mfma_f32_16x16x32_bf16 a[208:211],v[216:219],v[232:235],a[208:211]\n"
"ds_read_b64_tr_b16 v[164:165],v99 offset:0\n"
"s_mov_b32 s87,1572864\n"
"v_mfma_f32_16x16x32_bf16 a[212:215],v[220:223],v[232:235],a[212:215]\n"
"ds_read_b64_tr_b16 v[166:167],v99 offset:1024\n"
"buffer_load_dword v115,v112,s[36:39],s87 offen\n"
"v_mfma_f32_16x16x32_bf16 a[220:223],v[220:223],v[236:239],a[220:223]\n"
"ds_read_b64_tr_b16 v[168:169],v100 offset:0\n"
"s_lshl3_add_u32 vcc_lo,vcc_lo,s52\n"
"v_mfma_f32_16x16x32_bf16 a[216:219],v[216:219],v[236:239],a[216:219]\n"
"ds_read_b64_tr_b16 v[170:171],v100 offset:1024\n"
"s_lshl_b32 s13,vcc_lo,15\n"
"v_mfma_f32_16x16x32_bf16 a[224:227],v[216:219],v[240:243],a[224:227]\n"
"ds_read_b64_tr_b16 v[172:173],v101 offset:0\n"
"s_lshl_b32 vcc_lo,s95,8\n"
"v_mfma_f32_16x16x32_bf16 a[228:231],v[220:223],v[240:243],a[228:231]\n"
"ds_read_b64_tr_b16 v[174:175],v101 offset:1024\n"
"s_add_u32 s13,s13,vcc_lo\n"
"v_mfma_f32_16x16x32_bf16 a[236:239],v[220:223],v[244:247],a[236:239]\n"
"ds_read_b64_tr_b16 v[176:177],v102 offset:0\n"
"s_lshl3_add_u32 vcc_hi,s94,s95\n"
"v_mfma_f32_16x16x32_bf16 a[232:235],v[216:219],v[244:247],a[232:235]\n"
"ds_read_b64_tr_b16 v[178:179],v102 offset:1024\n"
"s_lshl_b32 s72,vcc_hi,8\n"
"v_mfma_f32_16x16x32_bf16 a[240:243],v[216:219],v[248:251],a[240:243]\n"
"ds_read_b64_tr_b16 v[180:181],v103 offset:0\n"
"s_add_u32 s72,s72,149061632\n"
"v_mfma_f32_16x16x32_bf16 a[244:247],v[220:223],v[248:251],a[244:247]\n"
"s_or_b32 vcc_lo,s96,s52\n"
"ds_read_b64_tr_b16 v[182:183],v103 offset:1024\n"
"s_cselect_b32 s72,2146435072,s72\n"
"v_mfma_f32_16x16x32_bf16 a[252:255],v[220:223],v[252:255],a[252:255]\n"
"ds_read_b64_tr_b16 v[184:185],v104 offset:0\n"
"s_lshl_b32 s3,vcc_hi,18\n"
"v_mfma_f32_16x16x32_bf16 a[248:251],v[216:219],v[252:255],a[248:251]\n"
"s_lshl_b32 vcc_lo,s96,15\n"
"ds_read_b64_tr_b16 v[186:187],v104 offset:1024\n"
"s_waitcnt lgkmcnt(10)\n"
"v_mfma_f32_16x16x32_bf16 a[192:195],v[152:155],v[160:163],a[192:195]\n"
"ds_read_b64_tr_b16 v[188:189],v105 offset:0\n"
"s_sub_u32 s3,s3,vcc_lo\n"
"v_mfma_f32_16x16x32_bf16 a[196:199],v[156:159],v[160:163],a[196:199]\n"
"ds_read_b64_tr_b16 v[190:191],v105 offset:1024\n"
"s_add_u32 s3,s3,s78\n"
"v_mfma_f32_16x16x32_bf16 a[204:207],v[156:159],v[164:167],a[204:207]\n"
"ds_read_b128 v[128:131],v80 offset:32768\n"
"s_add_u32 s3,s3,100909056\n"
"v_mfma_f32_16x16x32_bf16 a[200:203],v[152:155],v[164:167],a[200:203]\n"
"ds_read_b128 v[192:195],v81 offset:32768\n"
"s_waitcnt lgkmcnt(10)\n"
"v_mfma_f32_16x16x32_bf16 a[208:211],v[152:155],v[168:171],a[208:211]\n"
"ds_read_b64_tr_b16 v[224:225],v98 offset:16384\n"
"s_lshl3_add_u32 vcc_hi,vcc_hi,s96\n"
"v_mfma_f32_16x16x32_bf16 a[212:215],v[156:159],v[168:171],a[212:215]\n"
"ds_read_b64_tr_b16 v[226:227],v98 offset:17408\n"
"s_lshl2_add_u32 vcc_hi,vcc_hi,s52\n"
"v_mfma_f32_16x16x32_bf16 a[220:223],v[156:159],v[172:175],a[220:223]\n"
"ds_read_b64_tr_b16 v[228:229],v99 offset:16384\n"
"s_lshl_b32 s2,vcc_hi,4\n"
"v_mfma_f32_16x16x32_bf16 a[216:219],v[152:155],v[172:175],a[216:219]\n"
"ds_read_b64_tr_b16 v[230:231],v99 offset:17408\n"
"s_waitcnt lgkmcnt(10)\n"
"v_mfma_f32_16x16x32_bf16 a[224:227],v[152:155],v[176:179],a[224:227]\n"
"ds_read_b64_tr_b16 v[232:233],v100 offset:16384\n"
"s_add_u32 s2,s2,149028864\n"
"v_mfma_f32_16x16x32_bf16 a[228:231],v[156:159],v[176:179],a[228:231]\n"
"ds_read_b64_tr_b16 v[234:235],v100 offset:17408\n"
"s_lshl2_add_u32 vcc_lo,s95,s52\n"
"v_mfma_f32_16x16x32_bf16 a[236:239],v[156:159],v[180:183],a[236:239]\n"
"ds_read_b64_tr_b16 v[236:237],v101 offset:16384\n"
"s_lshl_b32 vcc_lo,vcc_lo,10\n"
"v_mfma_f32_16x16x32_bf16 a[232:235],v[152:155],v[180:183],a[232:235]\n"
"ds_read_b64_tr_b16 v[238:239],v101 offset:17408\n"
"s_waitcnt lgkmcnt(10)\n"
"v_mfma_f32_16x16x32_bf16 a[240:243],v[152:155],v[184:187],a[240:243]\n"
"ds_read_b64_tr_b16 v[240:241],v102 offset:16384\n"
"s_lshl_b32 vcc_hi,s94,22\n"
"v_mfma_f32_16x16x32_bf16 a[244:247],v[156:159],v[184:187],a[244:247]\n"
"ds_read_b64_tr_b16 v[242:243],v102 offset:17408\n"
"s_add_u32 s12,vcc_lo,vcc_hi\n"
"v_mfma_f32_16x16x32_bf16 a[252:255],v[156:159],v[188:191],a[252:255]\n"
"ds_read_b64_tr_b16 v[244:245],v103 offset:16384\n"
"s_lshl_b32 vcc_lo,s96,19\n"
"v_mfma_f32_16x16x32_bf16 a[248:251],v[152:155],v[188:191],a[248:251]\n"
"ds_read_b64_tr_b16 v[246:247],v103 offset:17408\n"
"s_waitcnt lgkmcnt(8)\n"
"v_mfma_f32_16x16x32_bf16 a[0:3],v[128:131],v[160:163],a[0:3]\n"
"ds_read_b64_tr_b16 v[248:249],v104 offset:16384\n"
"s_add_u32 s10,s12,vcc_lo\n"
"v_mfma_f32_16x16x32_bf16 a[0:3],v[192:195],v[224:227],a[0:3]\n"
"ds_read_b64_tr_b16 v[250:251],v104 offset:17408\n"
"s_add_u32 s10,s10,71303168\n"
"v_mfma_f32_16x16x32_bf16 a[8:11],v[192:195],v[228:231],a[8:11]\n"
"ds_read_b64_tr_b16 v[252:253],v105 offset:16384\n"
"s_mul_i32 vcc_lo,s52,31744\n"
"v_mfma_f32_16x16x32_bf16 a[8:11],v[128:131],v[164:167],a[8:11]\n"
"ds_read_b64_tr_b16 v[254:255],v105 offset:17408\n"
"s_waitcnt lgkmcnt(8)\n"
"v_mfma_f32_16x16x32_bf16 a[16:19],v[128:131],v[168:171],a[16:19]\n"
"ds_read_b128 v[132:135],v80 offset:34816\n"
"s_add_u32 s10,s10,vcc_lo\n"
"v_mfma_f32_16x16x32_bf16 a[16:19],v[192:195],v[232:235],a[16:19]\n"
"ds_read_b128 v[196:199],v81 offset:34816\n"
"s_lshl_b32 vcc_lo,s52,10\n"
"v_mfma_f32_16x16x32_bf16 a[24:27],v[192:195],v[236:239],a[24:27]\n"
"ds_read_b128 v[136:139],v80 offset:36864\n"
"s_sub_u32 s12,57671680,vcc_lo\n"
"v_mfma_f32_16x16x32_bf16 a[24:27],v[128:131],v[172:175],a[24:27]\n"
"ds_read_b128 v[200:203],v81 offset:36864\n"
"s_waitcnt lgkmcnt(8)\n"
"v_mfma_f32_16x16x32_bf16 a[32:35],v[128:131],v[176:179],a[32:35]\n"
"ds_read_b128 v[140:143],v80 offset:38912\n"
"s_add_u32 s8,s79,54525952\n"
"v_mfma_f32_16x16x32_bf16 a[32:35],v[192:195],v[240:243],a[32:35]\n"
"ds_read_b128 v[204:207],v81 offset:38912\n"
"s_and_b32 vcc_lo,s96,1\n"
"v_mfma_f32_16x16x32_bf16 a[40:43],v[192:195],v[244:247],a[40:43]\n"
"ds_read_b128 v[144:147],v80 offset:40960\n"
"s_lshl_b32 s11,vcc_lo,10\n"
"v_mfma_f32_16x16x32_bf16 a[40:43],v[128:131],v[180:183],a[40:43]\n"
"ds_read_b128 v[208:211],v81 offset:40960\n"
"s_waitcnt lgkmcnt(8)\n"
"v_mfma_f32_16x16x32_bf16 a[48:51],v[128:131],v[184:187],a[48:51]\n"
"ds_read_b128 v[148:151],v80 offset:43008\n"
"s_add_u32 s11,s11,148932608\n"
"v_mfma_f32_16x16x32_bf16 a[48:51],v[192:195],v[248:251],a[48:51]\n"
"ds_read_b128 v[212:215],v81 offset:43008\n"
"s_mov_b32 s9,148931072\n"
"v_mfma_f32_16x16x32_bf16 a[56:59],v[192:195],v[252:255],a[56:59]\n"
"s_cmp_eq_u32 s52,0\n"
"ds_read_b128 v[216:219],v81 offset:45056\n"
"s_cselect_b32 s71,s71,2146435072\n"
"v_mfma_f32_16x16x32_bf16 a[56:59],v[128:131],v[188:191],a[56:59]\n"
"ds_read_b128 v[220:223],v81 offset:47104\n"
"s_waitcnt lgkmcnt(10)\n"
"v_mfma_f32_16x16x32_bf16 a[4:7],v[132:135],v[160:163],a[4:7]\n"
"v_mfma_f32_16x16x32_bf16 a[4:7],v[196:199],v[224:227],a[4:7]\n"
"v_mfma_f32_16x16x32_bf16 a[12:15],v[196:199],v[228:231],a[12:15]\n"
"v_mfma_f32_16x16x32_bf16 a[12:15],v[132:135],v[164:167],a[12:15]\n"
"v_mfma_f32_16x16x32_bf16 a[20:23],v[132:135],v[168:171],a[20:23]\n"
"s_waitcnt lgkmcnt(0)\n"
"s_barrier\n"
"v_mfma_f32_16x16x32_bf16 a[20:23],v[196:199],v[232:235],a[20:23]\n"
"s_mov_b32 s77,1572864\n"
"s_add_u32 m0,s78,98304\n"
"v_mfma_f32_16x16x32_bf16 a[28:31],v[196:199],v[236:239],a[28:31]\n"
"buffer_load_dwordx4 v110,s[20:23],s77 offen offset:0 lds\n"
"v_mfma_f32_16x16x32_bf16 a[28:31],v[132:135],v[172:175],a[28:31]\n"
"v_mfma_f32_16x16x32_bf16 a[36:39],v[132:135],v[176:179],a[36:39]\n"
"v_mfma_f32_16x16x32_bf16 a[36:39],v[196:199],v[240:243],a[36:39]\n"
"v_mfma_f32_16x16x32_bf16 a[44:47],v[196:199],v[244:247],a[44:47]\n"
"buffer_load_dwordx4 v111,s[20:23],s77 offen offset:1024 lds\n"
"v_mfma_f32_16x16x32_bf16 a[44:47],v[132:135],v[180:183],a[44:47]\n"
"v_mfma_f32_16x16x32_bf16 a[52:55],v[132:135],v[184:187],a[52:55]\n"
"v_mfma_f32_16x16x32_bf16 a[52:55],v[196:199],v[248:251],a[52:55]\n"
"v_mfma_f32_16x16x32_bf16 a[60:63],v[196:199],v[252:255],a[60:63]\n"
"buffer_load_dwordx4 v113,s[20:23],s77 offen offset:2048 lds\n"
"v_mfma_f32_16x16x32_bf16 a[60:63],v[132:135],v[188:191],a[60:63]\n"
"v_mfma_f32_16x16x32_bf16 a[64:67],v[136:139],v[160:163],a[64:67]\n"
"v_mfma_f32_16x16x32_bf16 a[64:67],v[200:203],v[224:227],a[64:67]\n"
"v_mfma_f32_16x16x32_bf16 a[72:75],v[200:203],v[228:231],a[72:75]\n"
"buffer_load_dwordx4 v114,s[20:23],s77 offen offset:3072 lds\n"
"v_mfma_f32_16x16x32_bf16 a[72:75],v[136:139],v[164:167],a[72:75]\n"
"v_mfma_f32_16x16x32_bf16 a[80:83],v[136:139],v[168:171],a[80:83]\n"
"v_mfma_f32_16x16x32_bf16 a[80:83],v[200:203],v[232:235],a[80:83]\n"
"s_mov_b32 s77,1835008\n"
"s_add_u32 m0,s78,114688\n"
"v_mfma_f32_16x16x32_bf16 a[88:91],v[200:203],v[236:239],a[88:91]\n"
"buffer_load_dwordx4 v110,s[20:23],s77 offen offset:0 lds\n"
"v_mfma_f32_16x16x32_bf16 a[88:91],v[136:139],v[172:175],a[88:91]\n"
"v_mfma_f32_16x16x32_bf16 a[96:99],v[136:139],v[176:179],a[96:99]\n"
"v_mfma_f32_16x16x32_bf16 a[96:99],v[200:203],v[240:243],a[96:99]\n"
"v_mfma_f32_16x16x32_bf16 a[104:107],v[200:203],v[244:247],a[104:107]\n"
"buffer_load_dwordx4 v111,s[20:23],s77 offen offset:1024 lds\n"
"v_mfma_f32_16x16x32_bf16 a[104:107],v[136:139],v[180:183],a[104:107]\n"
"v_mfma_f32_16x16x32_bf16 a[112:115],v[136:139],v[184:187],a[112:115]\n"
"v_mfma_f32_16x16x32_bf16 a[112:115],v[200:203],v[248:251],a[112:115]\n"
"v_mfma_f32_16x16x32_bf16 a[120:123],v[200:203],v[252:255],a[120:123]\n"
"buffer_load_dwordx4 v113,s[20:23],s77 offen offset:2048 lds\n"
"v_mfma_f32_16x16x32_bf16 a[120:123],v[136:139],v[188:191],a[120:123]\n"
"s_waitcnt vmcnt(10)\n"
"v_mfma_f32_16x16x32_bf16 a[68:71],v[140:143],v[160:163],a[68:71]\n"
"v_lshlrev_b32 v24,16,v8\n"
"v_and_b32 v25,0xffff0000,v8\n"
"v_mfma_f32_16x16x32_bf16 a[68:71],v[204:207],v[224:227],a[68:71]\n"
"v_lshlrev_b32 v26,16,v9\n"
"v_and_b32 v27,0xffff0000,v9\n"
"v_mfma_f32_16x16x32_bf16 a[76:79],v[204:207],v[228:231],a[76:79]\n"
"buffer_load_dwordx4 v114,s[20:23],s77 offen offset:3072 lds\n"
"v_mfma_f32_16x16x32_bf16 a[76:79],v[140:143],v[164:167],a[76:79]\n"
"v_lshlrev_b32 v28,16,v10\n"
"v_and_b32 v29,0xffff0000,v10\n"
"v_mfma_f32_16x16x32_bf16 a[84:87],v[140:143],v[168:171],a[84:87]\n"
"v_lshlrev_b32 v30,16,v11\n"
"v_and_b32 v31,0xffff0000,v11\n"
"v_mfma_f32_16x16x32_bf16 a[84:87],v[204:207],v[232:235],a[84:87]\n"
"v_lshlrev_b32 v32,16,v12\n"
"v_and_b32 v33,0xffff0000,v12\n"
"v_mfma_f32_16x16x32_bf16 a[92:95],v[204:207],v[236:239],a[92:95]\n"
"s_mov_b32 s76,512\n"
"v_lshlrev_b32 v34,16,v13\n"
"s_add_u32 m0,s79,32768\n"
"v_mfma_f32_16x16x32_bf16 a[92:95],v[140:143],v[172:175],a[92:95]\n"
"buffer_load_dwordx4 v106,s[16:19],s76 offen offset:0 lds\n"
"v_mfma_f32_16x16x32_bf16 a[100:103],v[140:143],v[176:179],a[100:103]\n"
"v_and_b32 v35,0xffff0000,v13\n"
"v_lshlrev_b32 v36,16,v14\n"
"v_mfma_f32_16x16x32_bf16 a[100:103],v[204:207],v[240:243],a[100:103]\n"
"v_and_b32 v37,0xffff0000,v14\n"
"v_lshlrev_b32 v38,16,v15\n"
"v_mfma_f32_16x16x32_bf16 a[108:111],v[204:207],v[244:247],a[108:111]\n"
"v_and_b32 v39,0xffff0000,v15\n"
"v_lshlrev_b32 v40,16,v16\n"
"v_mfma_f32_16x16x32_bf16 a[108:111],v[140:143],v[180:183],a[108:111]\n"
"v_and_b32 v41,0xffff0000,v16\n"
"v_lshlrev_b32 v42,16,v17\n"
"v_mfma_f32_16x16x32_bf16 a[116:119],v[140:143],v[184:187],a[116:119]\n"
"buffer_load_dwordx4 v107,s[16:19],s76 offen offset:1024 lds\n"
"v_mfma_f32_16x16x32_bf16 a[116:119],v[204:207],v[248:251],a[116:119]\n"
"v_and_b32 v43,0xffff0000,v17\n"
"v_lshlrev_b32 v44,16,v18\n"
"v_mfma_f32_16x16x32_bf16 a[124:127],v[204:207],v[252:255],a[124:127]\n"
"v_and_b32 v45,0xffff0000,v18\n"
"v_lshlrev_b32 v46,16,v19\n"
"v_mfma_f32_16x16x32_bf16 a[124:127],v[140:143],v[188:191],a[124:127]\n"
"v_and_b32 v47,0xffff0000,v19\n"
"v_lshlrev_b32 v48,16,v20\n"
"v_mfma_f32_16x16x32_bf16 a[128:131],v[144:147],v[160:163],a[128:131]\n"
"v_and_b32 v49,0xffff0000,v20\n"
"v_lshlrev_b32 v50,16,v21\n"
"v_mfma_f32_16x16x32_bf16 a[128:131],v[208:211],v[224:227],a[128:131]\n"
"buffer_load_dwordx4 v108,s[16:19],s76 offen offset:2048 lds\n"
"v_mfma_f32_16x16x32_bf16 a[136:139],v[208:211],v[228:231],a[136:139]\n"
"v_and_b32 v51,0xffff0000,v21\n"
"v_lshlrev_b32 v52,16,v22\n"
"v_mfma_f32_16x16x32_bf16 a[136:139],v[144:147],v[164:167],a[136:139]\n"
"v_and_b32 v53,0xffff0000,v22\n"
"v_lshlrev_b32 v54,16,v23\n"
"v_mfma_f32_16x16x32_bf16 a[144:147],v[144:147],v[168:171],a[144:147]\n"
"v_and_b32 v55,0xffff0000,v23\n"
"v_add_f32 v56,v24,v25\n"
"v_mfma_f32_16x16x32_bf16 a[144:147],v[208:211],v[232:235],a[144:147]\n"
"v_add_f32 v57,v26,v27\n"
"v_add_f32 v56,v56,v28\n"
"v_mfma_f32_16x16x32_bf16 a[152:155],v[208:211],v[236:239],a[152:155]\n"
"buffer_load_dwordx4 v109,s[16:19],s76 offen offset:3072 lds\n"
"v_mfma_f32_16x16x32_bf16 a[152:155],v[144:147],v[172:175],a[152:155]\n"
"v_add_f32 v57,v57,v29\n"
"v_add_f32 v56,v56,v30\n"
"v_mfma_f32_16x16x32_bf16 a[160:163],v[144:147],v[176:179],a[160:163]\n"
"v_add_f32 v57,v57,v31\n"
"v_add_f32 v56,v56,v32\n"
"v_mfma_f32_16x16x32_bf16 a[160:163],v[208:211],v[240:243],a[160:163]\n"
"v_readfirstlane_b32 s82,v78\n"
"v_readfirstlane_b32 s83,v79\n"
"v_mfma_f32_16x16x32_bf16 a[168:171],v[208:211],v[244:247],a[168:171]\n"
"s_mov_b32 s76,262656\n"
"v_add_f32 v57,v57,v33\n"
"s_add_u32 m0,s79,36864\n"
"v_mfma_f32_16x16x32_bf16 a[168:171],v[144:147],v[180:183],a[168:171]\n"
"buffer_load_dwordx4 v106,s[16:19],s76 offen offset:0 lds\n"
"s_mul_i32 s80,s82,0x9e3779b1\n"
"v_mfma_f32_16x16x32_bf16 a[176:179],v[144:147],v[184:187],a[176:179]\n"
"s_add_u32 s80,s80,0x7f4a7c15\n"
"v_add_f32 v56,v56,v34\n"
"v_add_f32 v57,v57,v35\n"
"v_mfma_f32_16x16x32_bf16 a[176:179],v[208:211],v[248:251],a[176:179]\n"
"s_add_u32 s81,s83,s80\n"
"s_add_u32 s81,s81,1\n"
"v_mfma_f32_16x16x32_bf16 a[184:187],v[208:211],v[252:255],a[184:187]\n"
"v_mov_b32 v124,s80\n"
"v_mov_b32 v125,s81\n"
"v_mfma_f32_16x16x32_bf16 a[184:187],v[144:147],v[188:191],a[184:187]\n"
"v_add_f32 v56,v56,v36\n"
"v_add_f32 v57,v57,v37\n"
"v_mfma_f32_16x16x32_bf16 a[132:135],v[148:151],v[160:163],a[132:135]\n"
"buffer_load_dwordx4 v107,s[16:19],s76 offen offset:1024 lds\n"
"v_mfma_f32_16x16x32_bf16 a[132:135],v[212:215],v[224:227],a[132:135]\n"
"v_add_f32 v56,v56,v38\n"
"v_add_f32 v57,v57,v39\n"
"v_mfma_f32_16x16x32_bf16 a[140:143],v[212:215],v[228:231],a[140:143]\n"
"v_add_f32 v56,v56,v40\n"
"v_add_f32 v57,v57,v41\n"
"v_mfma_f32_16x16x32_bf16 a[140:143],v[148:151],v[164:167],a[140:143]\n"
"v_add_f32 v56,v56,v42\n"
"v_add_f32 v57,v57,v43\n"
"v_mfma_f32_16x16x32_bf16 a[148:151],v[148:151],v[168:171],a[148:151]\n"
"v_add_f32 v56,v56,v44\n"
"v_add_f32 v57,v57,v45\n"
"v_mfma_f32_16x16x32_bf16 a[148:151],v[212:215],v[232:235],a[148:151]\n"
"buffer_load_dwordx4 v108,s[16:19],s76 offen offset:2048 lds\n"
"v_mfma_f32_16x16x32_bf16 a[156:159],v[212:215],v[236:239],a[156:159]\n"
"v_add_f32 v56,v56,v46\n"
"v_add_f32 v57,v57,v47\n"
"v_mfma_f32_16x16x32_bf16 a[156:159],v[148:151],v[172:175],a[156:159]\n"
"v_add_f32 v56,v56,v48\n"
"v_add_f32 v57,v57,v49\n"
"v_mfma_f32_16x16x32_bf16 a[164:167],v[148:151],v[176:179],a[164:167]\n"
"v_add_f32 v56,v56,v50\n"
"v_add_f32 v57,v57,v51\n"
"v_mfma_f32_16x16x32_bf16 a[164:167],v[212:215],v[240:243],a[164:167]\n"
"v_add_f32 v56,v56,v52\n"
"v_add_f32 v57,v57,v53\n"
"v_mfma_f32_16x16x32_bf16 a[172:175],v[212:215],v[244:247],a[172:175]\n"
"buffer_load_dwordx4 v109,s[16:19],s76 offen offset:3072 lds\n"
"v_mfma_f32_16x16x32_bf16 a[172:175],v[148:151],v[180:183],a[172:175]\n"
"s_mov_b32 s87,2097152\n"
"v_add_f32 v56,v56,v54\n"
"v_add_f32 v57,v57,v55\n"
"v_mfma_f32_16x16x32_bf16 a[180:183],v[148:151],v[184:187],a[180:183]\n"
"buffer_load_dword v115,v112,s[36:39],s87 offen\n"
"v_mfma_f32_16x16x32_bf16 a[180:183],v[212:215],v[248:251],a[180:183]\n"
"s_mov_b32 s87,2621440\n"
"v_add_f32 v56,v56,v57\n"
"v_mfma_f32_16x16x32_bf16 a[188:191],v[212:215],v[252:255],a[188:191]\n"
"buffer_load_dword v115,v112,s[36:39],s87 offen\n"
"v_add_f32_dpp v56,v56,v56 quad_perm:[1,0,3,2] row_mask:0xf bank_mask:0xf\n"
"v_mfma_f32_16x16x32_bf16 a[188:191],v[148:151],v[188:191],a[188:191]\n"
"s_barrier\n"
"v_mfma_f32_16x16x32_bf16 a[192:195],v[216:219],v[224:227],a[192:195]\n"
"ds_read_b128 v[152:155],v82 offset:12288\n"
"v_mfma_f32_16x16x32_bf16 a[196:199],v[220:223],v[224:227],a[196:199]\n"
"ds_read_b128 v[156:159],v82 offset:14336\n"
"v_add_f32_dpp v56,v56,v56 quad_perm:[2,3,0,1] row_mask:0xf bank_mask:0xf\n"
"v_mfma_f32_16x16x32_bf16 a[204:207],v[220:223],v[228:231],a[204:207]\n"
"ds_read_b64_tr_b16 v[160:161],v98 offset:49152\n"
"v_mfma_f32_16x16x32_bf16 a[200:203],v[216:219],v[228:231],a[200:203]\n"
"ds_read_b64_tr_b16 v[162:163],v98 offset:50176\n"
"v_add_f32_dpp v56,v56,v56 row_ror:8 row_mask:0xf bank_mask:0xf\n"
"v_mfma_f32_16x16x32_bf16 a[208:211],v[216:219],v[232:235],a[208:211]\n"
"ds_read_b64_tr_b16 v[164:165],v99 offset:49152\n"
"v_mov_b32 v57,v56\n"
"v_mfma_f32_16x16x32_bf16 a[212:215],v[220:223],v[232:235],a[212:215]\n"
"ds_read_b64_tr_b16 v[166:167],v99 offset:50176\n"
"v_mfma_f32_16x16x32_bf16 a[220:223],v[220:223],v[236:239],a[220:223]\n"
"ds_read_b64_tr_b16 v[168:169],v100 offset:49152\n"
"v_permlane16_swap_b32 v57,v56\n"
"v_mfma_f32_16x16x32_bf16 a[216:219],v[216:219],v[236:239],a[216:219]\n"
"ds_read_b64_tr_b16 v[170:171],v100 offset:50176\n"
"v_add_f32 v56,v56,v57\n"
"v_mfma_f32_16x16x32_bf16 a[224:227],v[216:219],v[240:243],a[224:227]\n"
"ds_read_b64_tr_b16 v[172:173],v101 offset:49152\n"
"v_mov_b32 v57,v56\n"
"v_mfma_f32_16x16x32_bf16 a[228:231],v[220:223],v[240:243],a[228:231]\n"
"ds_read_b64_tr_b16 v[174:175],v101 offset:50176\n"
"v_mfma_f32_16x16x32_bf16 a[236:239],v[220:223],v[244:247],a[236:239]\n"
"ds_read_b64_tr_b16 v[176:177],v102 offset:49152\n"
"v_permlane32_swap_b32 v57,v56\n"
"v_mfma_f32_16x16x32_bf16 a[232:235],v[216:219],v[244:247],a[232:235]\n"
"ds_read_b64_tr_b16 v[178:179],v102 offset:50176\n"
"v_add_f32 v56,v56,v57\n"
"v_mfma_f32_16x16x32_bf16 a[240:243],v[216:219],v[248:251],a[240:243]\n"
"ds_read_b64_tr_b16 v[180:181],v103 offset:49152\n"
"v_mul_f32 v58,0x3a800000,v56\n"
"v_mfma_f32_16x16x32_bf16 a[244:247],v[220:223],v[248:251],a[244:247]\n"
"ds_read_b64_tr_b16 v[182:183],v103 offset:50176\n"
"v_sub_f32 v62,v24,v58\n"
"v_mfma_f32_16x16x32_bf16 a[252:255],v[220:223],v[252:255],a[252:255]\n"
"ds_read_b64_tr_b16 v[184:185],v104 offset:49152\n"
"v_mul_f32 v59,v62,v62\n"
"v_mfma_f32_16x16x32_bf16 a[248:251],v[216:219],v[252:255],a[248:251]\n"
"ds_read_b64_tr_b16 v[186:187],v104 offset:50176\n"
"v_sub_f32 v63,v25,v58\n"
"s_waitcnt lgkmcnt(10)\n"
"v_mfma_f32_16x16x32_bf16 a[192:195],v[152:155],v[160:163],a[192:195]\n"
"ds_read_b64_tr_b16 v[188:189],v105 offset:49152\n"
"v_mul_f32 v60,v63,v63\n"
"v_mfma_f32_16x16x32_bf16 a[196:199],v[156:159],v[160:163],a[196:199]\n"
"ds_read_b64_tr_b16 v[190:191],v105 offset:50176\n"
"v_sub_f32 v64,v26,v58\n"
"v_mfma_f32_16x16x32_bf16 a[204:207],v[156:159],v[164:167],a[204:207]\n"
"ds_read_b128 v[128:131],v82 offset:0\n"
"v_fmac_f32 v59,v64,v64\n"
"v_mfma_f32_16x16x32_bf16 a[200:203],v[152:155],v[164:167],a[200:203]\n"
"ds_read_b128 v[192:195],v83 offset:0\n"
"s_waitcnt lgkmcnt(10)\n"
"v_mfma_f32_16x16x32_bf16 a[208:211],v[152:155],v[168:171],a[208:211]\n"
"ds_read_b64_tr_b16 v[224:225],v98 offset:32768\n"
"v_sub_f32 v65,v27,v58\n"
"v_mfma_f32_16x16x32_bf16 a[212:215],v[156:159],v[168:171],a[212:215]\n"
"ds_read_b64_tr_b16 v[226:227],v98 offset:33792\n"
"v_fmac_f32 v60,v65,v65\n"
"v_mfma_f32_16x16x32_bf16 a[220:223],v[156:159],v[172:175],a[220:223]\n"
"ds_read_b64_tr_b16 v[228:229],v99 offset:32768\n"
"v_sub_f32 v62,v28,v58\n"
"v_mfma_f32_16x16x32_bf16 a[216:219],v[152:155],v[172:175],a[216:219]\n"
"ds_read_b64_tr_b16 v[230:231],v99 offset:33792\n"
"s_waitcnt lgkmcnt(10)\n"
"v_mfma_f32_16x16x32_bf16 a[224:227],v[152:155],v[176:179],a[224:227]\n"
"ds_read_b64_tr_b16 v[232:233],v100 offset:32768\n"
"v_fmac_f32 v59,v62,v62\n"
"v_mfma_f32_16x16x32_bf16 a[228:231],v[156:159],v[176:179],a[228:231]\n"
"ds_read_b64_tr_b16 v[234:235],v100 offset:33792\n"
"v_sub_f32 v63,v29,v58\n"
"v_mfma_f32_16x16x32_bf16 a[236:239],v[156:159],v[180:183],a[236:239]\n"
"ds_read_b64_tr_b16 v[236:237],v101 offset:32768\n"
"v_fmac_f32 v60,v63,v63\n"
"v_mfma_f32_16x16x32_bf16 a[232:235],v[152:155],v[180:183],a[232:235]\n"
"ds_read_b64_tr_b16 v[238:239],v101 offset:33792\n"
"s_waitcnt lgkmcnt(10)\n"
"v_mfma_f32_16x16x32_bf16 a[240:243],v[152:155],v[184:187],a[240:243]\n"
"ds_read_b64_tr_b16 v[240:241],v102 offset:32768\n"
"v_sub_f32 v64,v30,v58\n"
"v_mfma_f32_16x16x32_bf16 a[244:247],v[156:159],v[184:187],a[244:247]\n"
"ds_read_b64_tr_b16 v[242:243],v102 offset:33792\n"
"v_fmac_f32 v59,v64,v64\n"
"v_mfma_f32_16x16x32_bf16 a[252:255],v[156:159],v[188:191],a[252:255]\n"
"ds_read_b64_tr_b16 v[244:245],v103 offset:32768\n"
"v_sub_f32 v65,v31,v58\n"
"v_mfma_f32_16x16x32_bf16 a[248:251],v[152:155],v[188:191],a[248:251]\n"
"ds_read_b64_tr_b16 v[246:247],v103 offset:33792\n"
"s_waitcnt lgkmcnt(8)\n"
"v_mfma_f32_16x16x32_bf16 a[0:3],v[128:131],v[160:163],a[0:3]\n"
"ds_read_b64_tr_b16 v[248:249],v104 offset:32768\n"
"v_fmac_f32 v60,v65,v65\n"
"v_mfma_f32_16x16x32_bf16 a[0:3],v[192:195],v[224:227],a[0:3]\n"
"ds_read_b64_tr_b16 v[250:251],v104 offset:33792\n"
"v_sub_f32 v62,v32,v58\n"
"v_mfma_f32_16x16x32_bf16 a[8:11],v[192:195],v[228:231],a[8:11]\n"
"ds_read_b64_tr_b16 v[252:253],v105 offset:32768\n"
"v_fmac_f32 v59,v62,v62\n"
"v_mfma_f32_16x16x32_bf16 a[8:11],v[128:131],v[164:167],a[8:11]\n"
"ds_read_b64_tr_b16 v[254:255],v105 offset:33792\n"
"s_waitcnt lgkmcnt(8)\n"
"v_mfma_f32_16x16x32_bf16 a[16:19],v[128:131],v[168:171],a[16:19]\n"
"ds_read_b128 v[132:135],v82 offset:2048\n"
"v_sub_f32 v63,v33,v58\n"
"v_mfma_f32_16x16x32_bf16 a[16:19],v[192:195],v[232:235],a[16:19]\n"
"ds_read_b128 v[196:199],v83 offset:2048\n"
"v_fmac_f32 v60,v63,v63\n"
"v_mfma_f32_16x16x32_bf16 a[24:27],v[192:195],v[236:239],a[24:27]\n"
"ds_read_b128 v[136:139],v82 offset:4096\n"
"v_sub_f32 v64,v34,v58\n"
"v_mfma_f32_16x16x32_bf16 a[24:27],v[128:131],v[172:175],a[24:27]\n"
"ds_read_b128 v[200:203],v83 offset:4096\n"
"s_waitcnt lgkmcnt(8)\n"
"v_mfma_f32_16x16x32_bf16 a[32:35],v[128:131],v[176:179],a[32:35]\n"
"ds_read_b128 v[140:143],v82 offset:6144\n"
"v_fmac_f32 v59,v64,v64\n"
"v_mfma_f32_16x16x32_bf16 a[32:35],v[192:195],v[240:243],a[32:35]\n"
"ds_read_b128 v[204:207],v83 offset:6144\n"
"v_sub_f32 v65,v35,v58\n"
"v_mfma_f32_16x16x32_bf16 a[40:43],v[192:195],v[244:247],a[40:43]\n"
"ds_read_b128 v[144:147],v82 offset:8192\n"
"v_fmac_f32 v60,v65,v65\n"
"v_mfma_f32_16x16x32_bf16 a[40:43],v[128:131],v[180:183],a[40:43]\n"
"ds_read_b128 v[208:211],v83 offset:8192\n"
"s_waitcnt lgkmcnt(8)\n"
"v_mfma_f32_16x16x32_bf16 a[48:51],v[128:131],v[184:187],a[48:51]\n"
"ds_read_b128 v[148:151],v82 offset:10240\n"
"v_sub_f32 v62,v36,v58\n"
"v_mfma_f32_16x16x32_bf16 a[48:51],v[192:195],v[248:251],a[48:51]\n"
"ds_read_b128 v[212:215],v83 offset:10240\n"
"v_fmac_f32 v59,v62,v62\n"
"v_mfma_f32_16x16x32_bf16 a[56:59],v[192:195],v[252:255],a[56:59]\n"
"ds_read_b128 v[216:219],v83 offset:12288\n"
"v_sub_f32 v63,v37,v58\n"
"v_mfma_f32_16x16x32_bf16 a[56:59],v[128:131],v[188:191],a[56:59]\n"
"ds_read_b128 v[220:223],v83 offset:14336\n"
"s_waitcnt lgkmcnt(10)\n"
"v_mfma_f32_16x16x32_bf16 a[4:7],v[132:135],v[160:163],a[4:7]\n"
"v_fmac_f32 v60,v63,v63\n"
"v_sub_f32 v64,v38,v58\n"
"v_mfma_f32_16x16x32_bf16 a[4:7],v[196:199],v[224:227],a[4:7]\n"
"v_fmac_f32 v59,v64,v64\n"
"v_sub_f32 v65,v39,v58\n"
"v_mfma_f32_16x16x32_bf16 a[12:15],v[196:199],v[228:231],a[12:15]\n"
"v_fmac_f32 v60,v65,v65\n"
"v_sub_f32 v62,v40,v58\n"
"v_mfma_f32_16x16x32_bf16 a[12:15],v[132:135],v[164:167],a[12:15]\n"
"v_fmac_f32 v59,v62,v62\n"
"v_sub_f32 v63,v41,v58\n"
"v_mfma_f32_16x16x32_bf16 a[20:23],v[132:135],v[168:171],a[20:23]\n"
"s_waitcnt lgkmcnt(0)\n"
"s_barrier\n"
"v_mfma_f32_16x16x32_bf16 a[20:23],v[196:199],v[232:235],a[20:23]\n"
"s_mov_b32 s77,2097152\n"
"v_fmac_f32 v60,v63,v63\n"
"s_add_u32 m0,s78,147456\n"
"v_mfma_f32_16x16x32_bf16 a[28:31],v[196:199],v[236:239],a[28:31]\n"
"buffer_load_dwordx4 v110,s[20:23],s77 offen offset:0 lds\n"
"v_mfma_f32_16x16x32_bf16 a[28:31],v[132:135],v[172:175],a[28:31]\n"
"v_sub_f32 v64,v42,v58\n"
"v_fmac_f32 v59,v64,v64\n"
"v_mfma_f32_16x16x32_bf16 a[36:39],v[132:135],v[176:179],a[36:39]\n"
"v_sub_f32 v65,v43,v58\n"
"v_fmac_f32 v60,v65,v65\n"
"v_mfma_f32_16x16x32_bf16 a[36:39],v[196:199],v[240:243],a[36:39]\n"
"v_sub_f32 v62,v44,v58\n"
"v_fmac_f32 v59,v62,v62\n"
"v_mfma_f32_16x16x32_bf16 a[44:47],v[196:199],v[244:247],a[44:47]\n"
"buffer_load_dwordx4 v111,s[20:23],s77 offen offset:1024 lds\n"
"v_mfma_f32_16x16x32_bf16 a[44:47],v[132:135],v[180:183],a[44:47]\n"
"v_sub_f32 v63,v45,v58\n"
"v_fmac_f32 v60,v63,v63\n"
"v_mfma_f32_16x16x32_bf16 a[52:55],v[132:135],v[184:187],a[52:55]\n"
"v_sub_f32 v64,v46,v58\n"
"v_fmac_f32 v59,v64,v64\n"
"v_mfma_f32_16x16x32_bf16 a[52:55],v[196:199],v[248:251],a[52:55]\n"
"v_sub_f32 v65,v47,v58\n"
"v_fmac_f32 v60,v65,v65\n"
"v_mfma_f32_16x16x32_bf16 a[60:63],v[196:199],v[252:255],a[60:63]\n"
"buffer_load_dwordx4 v113,s[20:23],s77 offen offset:2048 lds\n"
"v_mfma_f32_16x16x32_bf16 a[60:63],v[132:135],v[188:191],a[60:63]\n"
"v_sub_f32 v62,v48,v58\n"
"v_fmac_f32 v59,v62,v62\n"
"v_mfma_f32_16x16x32_bf16 a[64:67],v[136:139],v[160:163],a[64:67]\n"
"v_sub_f32 v63,v49,v58\n"
"v_fmac_f32 v60,v63,v63\n"
"v_mfma_f32_16x16x32_bf16 a[64:67],v[200:203],v[224:227],a[64:67]\n"
"v_sub_f32 v64,v50,v58\n"
"v_fmac_f32 v59,v64,v64\n"
"v_mfma_f32_16x16x32_bf16 a[72:75],v[200:203],v[228:231],a[72:75]\n"
"buffer_load_dwordx4 v114,s[20:23],s77 offen offset:3072 lds\n"
"v_mfma_f32_16x16x32_bf16 a[72:75],v[136:139],v[164:167],a[72:75]\n"
"v_sub_f32 v65,v51,v58\n"
"v_fmac_f32 v60,v65,v65\n"
"v_mfma_f32_16x16x32_bf16 a[80:83],v[136:139],v[168:171],a[80:83]\n"
"v_sub_f32 v62,v52,v58\n"
"v_fmac_f32 v59,v62,v62\n"
"v_mfma_f32_16x16x32_bf16 a[80:83],v[200:203],v[232:235],a[80:83]\n"
"s_mov_b32 s77,2359296\n"
"v_sub_f32 v63,v53,v58\n"
"s_add_u32 m0,s78,131072\n"
"v_mfma_f32_16x16x32_bf16 a[88:91],v[200:203],v[236:239],a[88:91]\n"
"buffer_load_dwordx4 v110,s[20:23],s77 offen offset:0 lds\n"
"v_mfma_f32_16x16x32_bf16 a[88:91],v[136:139],v[172:175],a[88:91]\n"
"v_fmac_f32 v60,v63,v63\n"
"v_sub_f32 v64,v54,v58\n"
"v_mfma_f32_16x16x32_bf16 a[96:99],v[136:139],v[176:179],a[96:99]\n"
"v_fmac_f32 v59,v64,v64\n"
"v_sub_f32 v65,v55,v58\n"
"v_mfma_f32_16x16x32_bf16 a[96:99],v[200:203],v[240:243],a[96:99]\n"
"v_fmac_f32 v60,v65,v65\n"
"v_add_f32 v59,v59,v60\n"
"v_mfma_f32_16x16x32_bf16 a[104:107],v[200:203],v[244:247],a[104:107]\n"
"buffer_load_dwordx4 v111,s[20:23],s77 offen offset:1024 lds\n"
"v_mfma_f32_16x16x32_bf16 a[104:107],v[136:139],v[180:183],a[104:107]\n"
"v_mfma_f32_16x16x32_bf16 a[112:115],v[136:139],v[184:187],a[112:115]\n"
"v_add_f32_dpp v59,v59,v59 quad_perm:[1,0,3,2] row_mask:0xf bank_mask:0xf\n"
"v_mfma_f32_16x16x32_bf16 a[112:115],v[200:203],v[248:251],a[112:115]\n"
"s_nop 0\n"
"v_add_f32_dpp v59,v59,v59 quad_perm:[2,3,0,1] row_mask:0xf bank_mask:0xf\n"
"v_mfma_f32_16x16x32_bf16 a[120:123],v[200:203],v[252:255],a[120:123]\n"
"buffer_load_dwordx4 v113,s[20:23],s77 offen offset:2048 lds\n"
"v_mfma_f32_16x16x32_bf16 a[120:123],v[136:139],v[188:191],a[120:123]\n"
"v_add_f32_dpp v59,v59,v59 row_ror:8 row_mask:0xf bank_mask:0xf\n"
"v_mov_b32 v57,v59\n"
"v_mfma_f32_16x16x32_bf16 a[68:71],v[140:143],v[160:163],a[68:71]\n"
"v_mfma_f32_16x16x32_bf16 a[68:71],v[204:207],v[224:227],a[68:71]\n"
"v_permlane16_swap_b32 v57,v59\n"
"v_add_f32 v59,v59,v57\n"
"v_mfma_f32_16x16x32_bf16 a[76:79],v[204:207],v[228:231],a[76:79]\n"
"buffer_load_dwordx4 v114,s[20:23],s77 offen offset:3072 lds\n"
"v_mfma_f32_16x16x32_bf16 a[76:79],v[140:143],v[164:167],a[76:79]\n"
"v_mov_b32 v57,v59\n"
"v_mfma_f32_16x16x32_bf16 a[84:87],v[140:143],v[168:171],a[84:87]\n"
"s_nop 0\n"
"v_permlane32_swap_b32 v57,v59\n"
"v_add_f32 v59,v59,v57\n"
"v_mfma_f32_16x16x32_bf16 a[84:87],v[204:207],v[232:235],a[84:87]\n"
"v_mul_f32 v59,0x3a800000,v59\n"
"v_cvt_pk_bf16_f32 v59,v59,v59\n"
"v_mfma_f32_16x16x32_bf16 a[92:95],v[204:207],v[236:239],a[92:95]\n"
"s_mov_b32 s76,640\n"
"v_lshlrev_b32 v59,16,v59\n"
"s_add_u32 m0,s79,65536\n"
"v_mfma_f32_16x16x32_bf16 a[92:95],v[140:143],v[172:175],a[92:95]\n"
"buffer_load_dwordx4 v106,s[16:19],s76 offen offset:0 lds\n"
"v_mfma_f32_16x16x32_bf16 a[100:103],v[140:143],v[176:179],a[100:103]\n"
"v_add_f32 v59,0x3727c5ac,v59\n"
"v_cvt_pk_bf16_f32 v59,v59,v59\n"
"v_mfma_f32_16x16x32_bf16 a[100:103],v[204:207],v[240:243],a[100:103]\n"
"v_lshlrev_b32 v59,16,v59\n"
"v_sqrt_f32 v59,v59\n"
"v_mfma_f32_16x16x32_bf16 a[108:111],v[204:207],v[244:247],a[108:111]\n"
"v_cvt_pk_bf16_f32 v58,v58,v58\n"
"v_lshlrev_b32 v58,16,v58\n"
"v_mfma_f32_16x16x32_bf16 a[108:111],v[140:143],v[180:183],a[108:111]\n"
"v_cvt_pk_bf16_f32 v59,v59,v59\n"
"v_lshlrev_b32 v59,16,v59\n"
"v_mfma_f32_16x16x32_bf16 a[116:119],v[140:143],v[184:187],a[116:119]\n"
"buffer_load_dwordx4 v107,s[16:19],s76 offen offset:1024 lds\n"
"v_mfma_f32_16x16x32_bf16 a[116:119],v[204:207],v[248:251],a[116:119]\n"
"v_rcp_f32 v66,v59\n"
"v_mfma_f32_16x16x32_bf16 a[124:127],v[204:207],v[252:255],a[124:127]\n"
"v_mul_f32 v66,0x3e0293ee,v66\n"
"v_mul_f32 v58,v58,v66\n"
"v_mfma_f32_16x16x32_bf16 a[124:127],v[140:143],v[188:191],a[124:127]\n"
"v_fma_f32 v24,v24,v66,-v58\n"
"v_fma_f32 v25,v25,v66,-v58\n"
"v_mfma_f32_16x16x32_bf16 a[128:131],v[144:147],v[160:163],a[128:131]\n"
"v_cvt_pk_bf16_f32 v8,v24,v25\n"
"v_fma_f32 v26,v26,v66,-v58\n"
"v_mfma_f32_16x16x32_bf16 a[128:131],v[208:211],v[224:227],a[128:131]\n"
"buffer_load_dwordx4 v108,s[16:19],s76 offen offset:2048 lds\n"
"v_mfma_f32_16x16x32_bf16 a[136:139],v[208:211],v[228:231],a[136:139]\n"
"v_fma_f32 v27,v27,v66,-v58\n"
"v_cvt_pk_bf16_f32 v9,v26,v27\n"
"v_mfma_f32_16x16x32_bf16 a[136:139],v[144:147],v[164:167],a[136:139]\n"
"v_fma_f32 v28,v28,v66,-v58\n"
"v_fma_f32 v29,v29,v66,-v58\n"
"v_mfma_f32_16x16x32_bf16 a[144:147],v[144:147],v[168:171],a[144:147]\n"
"v_cvt_pk_bf16_f32 v10,v28,v29\n"
"v_fma_f32 v30,v30,v66,-v58\n"
"v_mfma_f32_16x16x32_bf16 a[144:147],v[208:211],v[232:235],a[144:147]\n"
"v_fma_f32 v31,v31,v66,-v58\n"
"v_cvt_pk_bf16_f32 v11,v30,v31\n"
"v_mfma_f32_16x16x32_bf16 a[152:155],v[208:211],v[236:239],a[152:155]\n"
"buffer_load_dwordx4 v109,s[16:19],s76 offen offset:3072 lds\n"
"v_mfma_f32_16x16x32_bf16 a[152:155],v[144:147],v[172:175],a[152:155]\n"
"buffer_store_dwordx4 v[8:11],v71,s[24:27],s54 offen sc1\n"
"v_mfma_f32_16x16x32_bf16 a[160:163],v[144:147],v[176:179],a[160:163]\n"
"s_add_u32 s54,s54,131072\n"
"v_fma_f32 v32,v32,v66,-v58\n"
"v_fma_f32 v33,v33,v66,-v58\n"
"v_mfma_f32_16x16x32_bf16 a[160:163],v[208:211],v[240:243],a[160:163]\n"
"v_cvt_pk_bf16_f32 v12,v32,v33\n"
"v_fma_f32 v34,v34,v66,-v58\n"
"v_mfma_f32_16x16x32_bf16 a[168:171],v[208:211],v[244:247],a[168:171]\n"
"s_mov_b32 s76,262784\n"
"v_fma_f32 v35,v35,v66,-v58\n"
"s_add_u32 m0,s79,69632\n"
"v_mfma_f32_16x16x32_bf16 a[168:171],v[144:147],v[180:183],a[168:171]\n"
"buffer_load_dwordx4 v106,s[16:19],s76 offen offset:0 lds\n"
"v_mfma_f32_16x16x32_bf16 a[176:179],v[144:147],v[184:187],a[176:179]\n"
"v_cvt_pk_bf16_f32 v13,v34,v35\n"
"v_fma_f32 v36,v36,v66,-v58\n"
"v_mfma_f32_16x16x32_bf16 a[176:179],v[208:211],v[248:251],a[176:179]\n"
"v_fma_f32 v37,v37,v66,-v58\n"
"v_cvt_pk_bf16_f32 v14,v36,v37\n"
"v_mfma_f32_16x16x32_bf16 a[184:187],v[208:211],v[252:255],a[184:187]\n"
"v_fma_f32 v38,v38,v66,-v58\n"
"v_fma_f32 v39,v39,v66,-v58\n"
"v_cvt_pk_bf16_f32 v15,v38,v39\n"
"v_mfma_f32_16x16x32_bf16 a[184:187],v[144:147],v[188:191],a[184:187]\n"
"buffer_store_dwordx4 v[12:15],v71,s[24:27],s54 offen sc1\n"
"v_mfma_f32_16x16x32_bf16 a[132:135],v[148:151],v[160:163],a[132:135]\n"
"buffer_load_dwordx4 v107,s[16:19],s76 offen offset:1024 lds\n"
"v_mfma_f32_16x16x32_bf16 a[132:135],v[212:215],v[224:227],a[132:135]\n"
"s_add_u32 s54,s54,131072\n"
"v_fma_f32 v40,v40,v66,-v58\n"
"v_mfma_f32_16x16x32_bf16 a[140:143],v[212:215],v[228:231],a[140:143]\n"
"v_fma_f32 v41,v41,v66,-v58\n"
"v_cvt_pk_bf16_f32 v16,v40,v41\n"
"v_mfma_f32_16x16x32_bf16 a[140:143],v[148:151],v[164:167],a[140:143]\n"
"v_fma_f32 v42,v42,v66,-v58\n"
"v_fma_f32 v43,v43,v66,-v58\n"
"v_mfma_f32_16x16x32_bf16 a[148:151],v[148:151],v[168:171],a[148:151]\n"
"v_cvt_pk_bf16_f32 v17,v42,v43\n"
"v_fma_f32 v44,v44,v66,-v58\n"
"v_mfma_f32_16x16x32_bf16 a[148:151],v[212:215],v[232:235],a[148:151]\n"
"buffer_load_dwordx4 v108,s[16:19],s76 offen offset:2048 lds\n"
"v_mfma_f32_16x16x32_bf16 a[156:159],v[212:215],v[236:239],a[156:159]\n"
"v_fma_f32 v45,v45,v66,-v58\n"
"v_cvt_pk_bf16_f32 v18,v44,v45\n"
"v_mfma_f32_16x16x32_bf16 a[156:159],v[148:151],v[172:175],a[156:159]\n"
"v_fma_f32 v46,v46,v66,-v58\n"
"v_fma_f32 v47,v47,v66,-v58\n"
"v_cvt_pk_bf16_f32 v19,v46,v47\n"
"v_mfma_f32_16x16x32_bf16 a[164:167],v[148:151],v[176:179],a[164:167]\n"
"buffer_store_dwordx4 v[16:19],v71,s[24:27],s54 offen sc1\n"
"v_mfma_f32_16x16x32_bf16 a[164:167],v[212:215],v[240:243],a[164:167]\n"
"s_add_u32 s54,s54,131072\n"
"v_fma_f32 v48,v48,v66,-v58\n"
"v_mfma_f32_16x16x32_bf16 a[172:175],v[212:215],v[244:247],a[172:175]\n"
"buffer_load_dwordx4 v109,s[16:19],s76 offen offset:3072 lds\n"
"v_mfma_f32_16x16x32_bf16 a[172:175],v[148:151],v[180:183],a[172:175]\n"
"s_mov_b32 s87,0\n"
"v_fma_f32 v49,v49,v66,-v58\n"
"v_cvt_pk_bf16_f32 v20,v48,v49\n"
"v_mfma_f32_16x16x32_bf16 a[180:183],v[148:151],v[184:187],a[180:183]\n"
"buffer_load_dword v115,v112,s[40:43],s87 offen\n"
"v_mfma_f32_16x16x32_bf16 a[180:183],v[212:215],v[248:251],a[180:183]\n"
"s_mov_b32 s87,1048576\n"
"v_fma_f32 v50,v50,v66,-v58\n"
"v_fma_f32 v51,v51,v66,-v58\n"
"v_mfma_f32_16x16x32_bf16 a[188:191],v[212:215],v[252:255],a[188:191]\n"
"buffer_load_dword v115,v112,s[40:43],s87 offen\n"
"v_mfma_f32_16x16x32_bf16 a[188:191],v[148:151],v[188:191],a[188:191]\n"
"s_mov_b32 s87,2097152\n"
"s_waitcnt vmcnt(31)\n"
"s_barrier\n"
"v_mfma_f32_16x16x32_bf16 a[192:195],v[216:219],v[224:227],a[192:195]\n"
"ds_read_b128 v[152:155],v80 offset:12288\n"
"buffer_load_dword v115,v112,s[40:43],s87 offen\n"
"v_mfma_f32_16x16x32_bf16 a[196:199],v[220:223],v[224:227],a[196:199]\n"
"ds_read_b128 v[156:159],v80 offset:14336\n"
"v_cvt_pk_bf16_f32 v21,v50,v51\n"
"v_mfma_f32_16x16x32_bf16 a[204:207],v[220:223],v[228:231],a[204:207]\n"
"ds_read_b64_tr_b16 v[160:161],v98 offset:0\n"
"v_fma_f32 v52,v52,v66,-v58\n"
"v_mfma_f32_16x16x32_bf16 a[200:203],v[216:219],v[228:231],a[200:203]\n"
"ds_read_b64_tr_b16 v[162:163],v98 offset:1024\n"
"v_fma_f32 v53,v53,v66,-v58\n"
"v_mfma_f32_16x16x32_bf16 a[208:211],v[216:219],v[232:235],a[208:211]\n"
"ds_read_b64_tr_b16 v[164:165],v99 offset:0\n"
"v_cvt_pk_bf16_f32 v22,v52,v53\n"
"v_mfma_f32_16x16x32_bf16 a[212:215],v[220:223],v[232:235],a[212:215]\n"
"ds_read_b64_tr_b16 v[166:167],v99 offset:1024\n"
"v_fma_f32 v54,v54,v66,-v58\n"
"v_mfma_f32_16x16x32_bf16 a[220:223],v[220:223],v[236:239],a[220:223]\n"
"ds_read_b64_tr_b16 v[168:169],v100 offset:0\n"
"v_fma_f32 v55,v55,v66,-v58\n"
"v_mfma_f32_16x16x32_bf16 a[216:219],v[216:219],v[236:239],a[216:219]\n"
"ds_read_b64_tr_b16 v[170:171],v100 offset:1024\n"
"v_cvt_pk_bf16_f32 v23,v54,v55\n"
"v_mfma_f32_16x16x32_bf16 a[224:227],v[216:219],v[240:243],a[224:227]\n"
"ds_read_b64_tr_b16 v[172:173],v101 offset:0\n"
"buffer_store_dwordx4 v[20:23],v71,s[24:27],s54 offen sc1\n"
"v_mfma_f32_16x16x32_bf16 a[228:231],v[220:223],v[240:243],a[228:231]\n"
"ds_read_b64_tr_b16 v[174:175],v101 offset:1024\n"
"v_mfma_f32_16x16x32_bf16 a[236:239],v[220:223],v[244:247],a[236:239]\n"
"ds_read_b64_tr_b16 v[176:177],v102 offset:0\n"
"v_mfma_f32_16x16x32_bf16 a[232:235],v[216:219],v[244:247],a[232:235]\n"
"ds_read_b64_tr_b16 v[178:179],v102 offset:1024\n"
"v_mfma_f32_16x16x32_bf16 a[240:243],v[216:219],v[248:251],a[240:243]\n"
"ds_read_b64_tr_b16 v[180:181],v103 offset:0\n"
"v_mfma_f32_16x16x32_bf16 a[244:247],v[220:223],v[248:251],a[244:247]\n"
"ds_read_b64_tr_b16 v[182:183],v103 offset:1024\n"
"v_mfma_f32_16x16x32_bf16 a[252:255],v[220:223],v[252:255],a[252:255]\n"
"ds_read_b64_tr_b16 v[184:185],v104 offset:0\n"
"v_mfma_f32_16x16x32_bf16 a[248:251],v[216:219],v[252:255],a[248:251]\n"
"ds_read_b64_tr_b16 v[186:187],v104 offset:1024\n"
"s_waitcnt lgkmcnt(10)\n"
"v_mfma_f32_16x16x32_bf16 a[192:195],v[152:155],v[160:163],a[192:195]\n"
"ds_read_b64_tr_b16 v[188:189],v105 offset:0\n"
"v_mfma_f32_16x16x32_bf16 a[196:199],v[156:159],v[160:163],a[196:199]\n"
"ds_read_b64_tr_b16 v[190:191],v105 offset:1024\n"
"v_mfma_f32_16x16x32_bf16 a[204:207],v[156:159],v[164:167],a[204:207]\n"
"ds_read_b128 v[128:131],v80 offset:0\n"
"v_mfma_f32_16x16x32_bf16 a[200:203],v[152:155],v[164:167],a[200:203]\n"
"ds_read_b128 v[192:195],v81 offset:0\n"
"s_waitcnt lgkmcnt(10)\n"
"v_mfma_f32_16x16x32_bf16 a[208:211],v[152:155],v[168:171],a[208:211]\n"
"ds_read_b64_tr_b16 v[224:225],v98 offset:16384\n"
"v_mfma_f32_16x16x32_bf16 a[212:215],v[156:159],v[168:171],a[212:215]\n"
"ds_read_b64_tr_b16 v[226:227],v98 offset:17408\n"
"v_mfma_f32_16x16x32_bf16 a[220:223],v[156:159],v[172:175],a[220:223]\n"
"ds_read_b64_tr_b16 v[228:229],v99 offset:16384\n"
"v_mfma_f32_16x16x32_bf16 a[216:219],v[152:155],v[172:175],a[216:219]\n"
"ds_read_b64_tr_b16 v[230:231],v99 offset:17408\n"
"s_waitcnt lgkmcnt(10)\n"
"v_mfma_f32_16x16x32_bf16 a[224:227],v[152:155],v[176:179],a[224:227]\n"
"ds_read_b64_tr_b16 v[232:233],v100 offset:16384\n"
"v_mfma_f32_16x16x32_bf16 a[228:231],v[156:159],v[176:179],a[228:231]\n"
"ds_read_b64_tr_b16 v[234:235],v100 offset:17408\n"
"v_mfma_f32_16x16x32_bf16 a[236:239],v[156:159],v[180:183],a[236:239]\n"
"ds_read_b64_tr_b16 v[236:237],v101 offset:16384\n"
"v_mfma_f32_16x16x32_bf16 a[232:235],v[152:155],v[180:183],a[232:235]\n"
"ds_read_b64_tr_b16 v[238:239],v101 offset:17408\n"
"s_waitcnt lgkmcnt(10)\n"
"v_mfma_f32_16x16x32_bf16 a[240:243],v[152:155],v[184:187],a[240:243]\n"
"ds_read_b64_tr_b16 v[240:241],v102 offset:16384\n"
"v_mfma_f32_16x16x32_bf16 a[244:247],v[156:159],v[184:187],a[244:247]\n"
"ds_read_b64_tr_b16 v[242:243],v102 offset:17408\n"
"v_mfma_f32_16x16x32_bf16 a[252:255],v[156:159],v[188:191],a[252:255]\n"
"ds_read_b64_tr_b16 v[244:245],v103 offset:16384\n"
"v_mfma_f32_16x16x32_bf16 a[248:251],v[152:155],v[188:191],a[248:251]\n"
"ds_read_b64_tr_b16 v[246:247],v103 offset:17408\n"
"s_waitcnt lgkmcnt(8)\n"
"v_mfma_f32_16x16x32_bf16 a[0:3],v[128:131],v[160:163],a[0:3]\n"
"ds_read_b64_tr_b16 v[248:249],v104 offset:16384\n"
"v_mfma_f32_16x16x32_bf16 a[0:3],v[192:195],v[224:227],a[0:3]\n"
"ds_read_b64_tr_b16 v[250:251],v104 offset:17408\n"
"v_mfma_f32_16x16x32_bf16 a[8:11],v[192:195],v[228:231],a[8:11]\n"
"ds_read_b64_tr_b16 v[252:253],v105 offset:16384\n"
"v_mfma_f32_16x16x32_bf16 a[8:11],v[128:131],v[164:167],a[8:11]\n"
"ds_read_b64_tr_b16 v[254:255],v105 offset:17408\n"
"s_waitcnt lgkmcnt(8)\n"
"v_mfma_f32_16x16x32_bf16 a[16:19],v[128:131],v[168:171],a[16:19]\n"
"ds_read_b128 v[132:135],v80 offset:2048\n"
"v_mfma_f32_16x16x32_bf16 a[16:19],v[192:195],v[232:235],a[16:19]\n"
"ds_read_b128 v[196:199],v81 offset:2048\n"
"v_mfma_f32_16x16x32_bf16 a[24:27],v[192:195],v[236:239],a[24:27]\n"
"ds_read_b128 v[136:139],v80 offset:4096\n"
"v_mfma_f32_16x16x32_bf16 a[24:27],v[128:131],v[172:175],a[24:27]\n"
"ds_read_b128 v[200:203],v81 offset:4096\n"
"s_waitcnt lgkmcnt(8)\n"
"v_mfma_f32_16x16x32_bf16 a[32:35],v[128:131],v[176:179],a[32:35]\n"
"ds_read_b128 v[140:143],v80 offset:6144\n"
"v_mfma_f32_16x16x32_bf16 a[32:35],v[192:195],v[240:243],a[32:35]\n"
"ds_read_b128 v[204:207],v81 offset:6144\n"
"v_mfma_f32_16x16x32_bf16 a[40:43],v[192:195],v[244:247],a[40:43]\n"
"ds_read_b128 v[144:147],v80 offset:8192\n"
"v_mfma_f32_16x16x32_bf16 a[40:43],v[128:131],v[180:183],a[40:43]\n"
"ds_read_b128 v[208:211],v81 offset:8192\n"
"s_waitcnt lgkmcnt(8)\n"
"v_mfma_f32_16x16x32_bf16 a[48:51],v[128:131],v[184:187],a[48:51]\n"
"ds_read_b128 v[148:151],v80 offset:10240\n"
"v_mfma_f32_16x16x32_bf16 a[48:51],v[192:195],v[248:251],a[48:51]\n"
"ds_read_b128 v[212:215],v81 offset:10240\n"
"v_mfma_f32_16x16x32_bf16 a[56:59],v[192:195],v[252:255],a[56:59]\n"
"ds_read_b128 v[216:219],v81 offset:12288\n"
"v_mfma_f32_16x16x32_bf16 a[56:59],v[128:131],v[188:191],a[56:59]\n"
"ds_read_b128 v[220:223],v81 offset:14336\n"
"s_waitcnt lgkmcnt(10)\n"
"v_mfma_f32_16x16x32_bf16 a[4:7],v[132:135],v[160:163],a[4:7]\n"
"v_mfma_f32_16x16x32_bf16 a[4:7],v[196:199],v[224:227],a[4:7]\n"
"v_mfma_f32_16x16x32_bf16 a[12:15],v[196:199],v[228:231],a[12:15]\n"
"v_mfma_f32_16x16x32_bf16 a[12:15],v[132:135],v[164:167],a[12:15]\n"
"v_mfma_f32_16x16x32_bf16 a[20:23],v[132:135],v[168:171],a[20:23]\n"
"s_waitcnt lgkmcnt(0)\n"
"s_barrier\n"
"v_mfma_f32_16x16x32_bf16 a[20:23],v[196:199],v[232:235],a[20:23]\n"
"s_mov_b32 s77,2621440\n"
"s_add_u32 m0,s78,98304\n"
"v_mfma_f32_16x16x32_bf16 a[28:31],v[196:199],v[236:239],a[28:31]\n"
"buffer_load_dwordx4 v110,s[20:23],s77 offen offset:0 lds\n"
"v_mfma_f32_16x16x32_bf16 a[28:31],v[132:135],v[172:175],a[28:31]\n"
"v_mfma_f32_16x16x32_bf16 a[36:39],v[132:135],v[176:179],a[36:39]\n"
"v_mfma_f32_16x16x32_bf16 a[36:39],v[196:199],v[240:243],a[36:39]\n"
"v_mfma_f32_16x16x32_bf16 a[44:47],v[196:199],v[244:247],a[44:47]\n"
"buffer_load_dwordx4 v111,s[20:23],s77 offen offset:1024 lds\n"
"v_mfma_f32_16x16x32_bf16 a[44:47],v[132:135],v[180:183],a[44:47]\n"
"v_mfma_f32_16x16x32_bf16 a[52:55],v[132:135],v[184:187],a[52:55]\n"
"v_mfma_f32_16x16x32_bf16 a[52:55],v[196:199],v[248:251],a[52:55]\n"
"v_mfma_f32_16x16x32_bf16 a[60:63],v[196:199],v[252:255],a[60:63]\n"
"buffer_load_dwordx4 v113,s[20:23],s77 offen offset:2048 lds\n"
"v_mfma_f32_16x16x32_bf16 a[60:63],v[132:135],v[188:191],a[60:63]\n"
"v_mfma_f32_16x16x32_bf16 a[64:67],v[136:139],v[160:163],a[64:67]\n"
"v_mfma_f32_16x16x32_bf16 a[64:67],v[200:203],v[224:227],a[64:67]\n"
"v_mfma_f32_16x16x32_bf16 a[72:75],v[200:203],v[228:231],a[72:75]\n"
"buffer_load_dwordx4 v114,s[20:23],s77 offen offset:3072 lds\n"
"v_mfma_f32_16x16x32_bf16 a[72:75],v[136:139],v[164:167],a[72:75]\n"
"v_mfma_f32_16x16x32_bf16 a[80:83],v[136:139],v[168:171],a[80:83]\n"
"v_mfma_f32_16x16x32_bf16 a[80:83],v[200:203],v[232:235],a[80:83]\n"
"s_mov_b32 s77,2883584\n"
"s_add_u32 m0,s78,114688\n"
"v_mfma_f32_16x16x32_bf16 a[88:91],v[200:203],v[236:239],a[88:91]\n"
"buffer_load_dwordx4 v110,s[20:23],s77 offen offset:0 lds\n"
"v_mfma_f32_16x16x32_bf16 a[88:91],v[136:139],v[172:175],a[88:91]\n"
"v_mfma_f32_16x16x32_bf16 a[96:99],v[136:139],v[176:179],a[96:99]\n"
"v_mfma_f32_16x16x32_bf16 a[96:99],v[200:203],v[240:243],a[96:99]\n"
"v_mfma_f32_16x16x32_bf16 a[104:107],v[200:203],v[244:247],a[104:107]\n"
"buffer_load_dwordx4 v111,s[20:23],s77 offen offset:1024 lds\n"
"v_mfma_f32_16x16x32_bf16 a[104:107],v[136:139],v[180:183],a[104:107]\n"
"v_mfma_f32_16x16x32_bf16 a[112:115],v[136:139],v[184:187],a[112:115]\n"
"v_mfma_f32_16x16x32_bf16 a[112:115],v[200:203],v[248:251],a[112:115]\n"
"v_mfma_f32_16x16x32_bf16 a[120:123],v[200:203],v[252:255],a[120:123]\n"
"buffer_load_dwordx4 v113,s[20:23],s77 offen offset:2048 lds\n"
"v_mfma_f32_16x16x32_bf16 a[120:123],v[136:139],v[188:191],a[120:123]\n"
"v_mfma_f32_16x16x32_bf16 a[68:71],v[140:143],v[160:163],a[68:71]\n"
"v_mfma_f32_16x16x32_bf16 a[68:71],v[204:207],v[224:227],a[68:71]\n"
"v_mfma_f32_16x16x32_bf16 a[76:79],v[204:207],v[228:231],a[76:79]\n"
"buffer_load_dwordx4 v114,s[20:23],s77 offen offset:3072 lds\n"
"v_mfma_f32_16x16x32_bf16 a[76:79],v[140:143],v[164:167],a[76:79]\n"
"v_mfma_f32_16x16x32_bf16 a[84:87],v[140:143],v[168:171],a[84:87]\n"
"v_mfma_f32_16x16x32_bf16 a[84:87],v[204:207],v[232:235],a[84:87]\n"
"v_mfma_f32_16x16x32_bf16 a[92:95],v[204:207],v[236:239],a[92:95]\n"
"s_mov_b32 s76,768\n"
"s_add_u32 m0,s79,0\n"
"v_mfma_f32_16x16x32_bf16 a[92:95],v[140:143],v[172:175],a[92:95]\n"
"buffer_load_dwordx4 v106,s[16:19],s76 offen offset:0 lds\n"
"v_mfma_f32_16x16x32_bf16 a[100:103],v[140:143],v[176:179],a[100:103]\n"
"v_mfma_f32_16x16x32_bf16 a[100:103],v[204:207],v[240:243],a[100:103]\n"
"v_mfma_f32_16x16x32_bf16 a[108:111],v[204:207],v[244:247],a[108:111]\n"
"v_mfma_f32_16x16x32_bf16 a[108:111],v[140:143],v[180:183],a[108:111]\n"
"v_mfma_f32_16x16x32_bf16 a[116:119],v[140:143],v[184:187],a[116:119]\n"
"buffer_load_dwordx4 v107,s[16:19],s76 offen offset:1024 lds\n"
"v_mfma_f32_16x16x32_bf16 a[116:119],v[204:207],v[248:251],a[116:119]\n"
"v_mfma_f32_16x16x32_bf16 a[124:127],v[204:207],v[252:255],a[124:127]\n"
"v_mfma_f32_16x16x32_bf16 a[124:127],v[140:143],v[188:191],a[124:127]\n"
"v_mfma_f32_16x16x32_bf16 a[128:131],v[144:147],v[160:163],a[128:131]\n"
"v_mfma_f32_16x16x32_bf16 a[128:131],v[208:211],v[224:227],a[128:131]\n"
"buffer_load_dwordx4 v108,s[16:19],s76 offen offset:2048 lds\n"
"v_mfma_f32_16x16x32_bf16 a[136:139],v[208:211],v[228:231],a[136:139]\n"
"v_mfma_f32_16x16x32_bf16 a[136:139],v[144:147],v[164:167],a[136:139]\n"
"v_mfma_f32_16x16x32_bf16 a[144:147],v[144:147],v[168:171],a[144:147]\n"
"v_mfma_f32_16x16x32_bf16 a[144:147],v[208:211],v[232:235],a[144:147]\n"
"v_mfma_f32_16x16x32_bf16 a[152:155],v[208:211],v[236:239],a[152:155]\n"
"buffer_load_dwordx4 v109,s[16:19],s76 offen offset:3072 lds\n"
"v_mfma_f32_16x16x32_bf16 a[152:155],v[144:147],v[172:175],a[152:155]\n"
"v_mfma_f32_16x16x32_bf16 a[160:163],v[144:147],v[176:179],a[160:163]\n"
"v_mfma_f32_16x16x32_bf16 a[160:163],v[208:211],v[240:243],a[160:163]\n"
"v_mfma_f32_16x16x32_bf16 a[168:171],v[208:211],v[244:247],a[168:171]\n"
"s_mov_b32 s76,262912\n"
"s_add_u32 m0,s79,4096\n"
"v_mfma_f32_16x16x32_bf16 a[168:171],v[144:147],v[180:183],a[168:171]\n"
"buffer_load_dwordx4 v106,s[16:19],s76 offen offset:0 lds\n"
"v_mfma_f32_16x16x32_bf16 a[176:179],v[144:147],v[184:187],a[176:179]\n"
"v_mfma_f32_16x16x32_bf16 a[176:179],v[208:211],v[248:251],a[176:179]\n"
"v_mfma_f32_16x16x32_bf16 a[184:187],v[208:211],v[252:255],a[184:187]\n"
"v_mfma_f32_16x16x32_bf16 a[184:187],v[144:147],v[188:191],a[184:187]\n"
"v_mfma_f32_16x16x32_bf16 a[132:135],v[148:151],v[160:163],a[132:135]\n"
"buffer_load_dwordx4 v107,s[16:19],s76 offen offset:1024 lds\n"
"v_mfma_f32_16x16x32_bf16 a[132:135],v[212:215],v[224:227],a[132:135]\n"
"v_mfma_f32_16x16x32_bf16 a[140:143],v[212:215],v[228:231],a[140:143]\n"
"v_mfma_f32_16x16x32_bf16 a[140:143],v[148:151],v[164:167],a[140:143]\n"
"v_mfma_f32_16x16x32_bf16 a[148:151],v[148:151],v[168:171],a[148:151]\n"
"v_mfma_f32_16x16x32_bf16 a[148:151],v[212:215],v[232:235],a[148:151]\n"
"buffer_load_dwordx4 v108,s[16:19],s76 offen offset:2048 lds\n"
"v_mfma_f32_16x16x32_bf16 a[156:159],v[212:215],v[236:239],a[156:159]\n"
"v_mfma_f32_16x16x32_bf16 a[156:159],v[148:151],v[172:175],a[156:159]\n"
"v_mfma_f32_16x16x32_bf16 a[164:167],v[148:151],v[176:179],a[164:167]\n"
"v_mfma_f32_16x16x32_bf16 a[164:167],v[212:215],v[240:243],a[164:167]\n"
"v_mfma_f32_16x16x32_bf16 a[172:175],v[212:215],v[244:247],a[172:175]\n"
"buffer_load_dwordx4 v109,s[16:19],s76 offen offset:3072 lds\n"
"v_mfma_f32_16x16x32_bf16 a[172:175],v[148:151],v[180:183],a[172:175]\n"
"s_mov_b32 s87,3145728\n"
"v_mfma_f32_16x16x32_bf16 a[180:183],v[148:151],v[184:187],a[180:183]\n"
"buffer_load_dword v115,v112,s[40:43],s87 offen\n"
"v_mfma_f32_16x16x32_bf16 a[180:183],v[212:215],v[248:251],a[180:183]\n"
"s_mov_b32 s87,4194304\n"
"v_mfma_f32_16x16x32_bf16 a[188:191],v[212:215],v[252:255],a[188:191]\n"
"buffer_load_dword v115,v112,s[40:43],s87 offen\n"
"v_mfma_f32_16x16x32_bf16 a[188:191],v[148:151],v[188:191],a[188:191]\n"
"s_mov_b32 s87,5242880\n"
"s_waitcnt vmcnt(33)\n"
"s_barrier\n"
"v_mfma_f32_16x16x32_bf16 a[192:195],v[216:219],v[224:227],a[192:195]\n"
"ds_read_b128 v[152:155],v80 offset:45056\n"
"buffer_load_dword v115,v112,s[40:43],s87 offen\n"
"v_mfma_f32_16x16x32_bf16 a[196:199],v[220:223],v[224:227],a[196:199]\n"
"ds_read_b128 v[156:159],v80 offset:47104\n"
"v_mfma_f32_16x16x32_bf16 a[204:207],v[220:223],v[228:231],a[204:207]\n"
"ds_read_b64_tr_b16 v[160:161],v98 offset:49152\n"
"v_mfma_f32_16x16x32_bf16 a[200:203],v[216:219],v[228:231],a[200:203]\n"
"ds_read_b64_tr_b16 v[162:163],v98 offset:50176\n"
"v_mfma_f32_16x16x32_bf16 a[208:211],v[216:219],v[232:235],a[208:211]\n"
"ds_read_b64_tr_b16 v[164:165],v99 offset:49152\n"
"v_mfma_f32_16x16x32_bf16 a[212:215],v[220:223],v[232:235],a[212:215]\n"
"ds_read_b64_tr_b16 v[166:167],v99 offset:50176\n"
"v_mfma_f32_16x16x32_bf16 a[220:223],v[220:223],v[236:239],a[220:223]\n"
"ds_read_b64_tr_b16 v[168:169],v100 offset:49152\n"
"v_mfma_f32_16x16x32_bf16 a[216:219],v[216:219],v[236:239],a[216:219]\n"
"ds_read_b64_tr_b16 v[170:171],v100 offset:50176\n"
"v_mfma_f32_16x16x32_bf16 a[224:227],v[216:219],v[240:243],a[224:227]\n"
"ds_read_b64_tr_b16 v[172:173],v101 offset:49152\n"
"v_mfma_f32_16x16x32_bf16 a[228:231],v[220:223],v[240:243],a[228:231]\n"
"ds_read_b64_tr_b16 v[174:175],v101 offset:50176\n"
"v_mfma_f32_16x16x32_bf16 a[236:239],v[220:223],v[244:247],a[236:239]\n"
"ds_read_b64_tr_b16 v[176:177],v102 offset:49152\n"
"v_mfma_f32_16x16x32_bf16 a[232:235],v[216:219],v[244:247],a[232:235]\n"
"ds_read_b64_tr_b16 v[178:179],v102 offset:50176\n"
"v_mfma_f32_16x16x32_bf16 a[240:243],v[216:219],v[248:251],a[240:243]\n"
"ds_read_b64_tr_b16 v[180:181],v103 offset:49152\n"
"v_mfma_f32_16x16x32_bf16 a[244:247],v[220:223],v[248:251],a[244:247]\n"
"ds_read_b64_tr_b16 v[182:183],v103 offset:50176\n"
"v_mfma_f32_16x16x32_bf16 a[252:255],v[220:223],v[252:255],a[252:255]\n"
"ds_read_b64_tr_b16 v[184:185],v104 offset:49152\n"
"ds_read_b64_tr_b16 v[186:187],v104 offset:50176\n"
"v_mfma_f32_16x16x32_bf16 a[248:251],v[216:219],v[252:255],a[248:251]\n"
"s_waitcnt vmcnt(19)\n"
"s_waitcnt lgkmcnt(10)\n"
"v_mfma_f32_16x16x32_bf16 a[192:195],v[152:155],v[160:163],a[192:195]\n"
"ds_read_b64_tr_b16 v[188:189],v105 offset:49152\n"
"ds_read_b64_tr_b16 v[190:191],v105 offset:50176\n"
"v_mfma_f32_16x16x32_bf16 a[196:199],v[156:159],v[160:163],a[196:199]\n"
"buffer_store_dwordx2 v[124:125],off,s[24:27],s57 sc1\n"
"v_mfma_f32_16x16x32_bf16 a[204:207],v[156:159],v[164:167],a[204:207]\n"
"ds_read_b128 v[128:131],v80 offset:32768\n"
"v_mfma_f32_16x16x32_bf16 a[200:203],v[152:155],v[164:167],a[200:203]\n"
"ds_read_b128 v[192:195],v81 offset:32768\n"
"s_waitcnt lgkmcnt(10)\n"
"v_mfma_f32_16x16x32_bf16 a[208:211],v[152:155],v[168:171],a[208:211]\n"
"ds_read_b64_tr_b16 v[224:225],v98 offset:32768\n"
"v_mfma_f32_16x16x32_bf16 a[212:215],v[156:159],v[168:171],a[212:215]\n"
"ds_read_b64_tr_b16 v[226:227],v98 offset:33792\n"
"v_mfma_f32_16x16x32_bf16 a[220:223],v[156:159],v[172:175],a[220:223]\n"
"ds_read_b64_tr_b16 v[228:229],v99 offset:32768\n"
"v_mfma_f32_16x16x32_bf16 a[216:219],v[152:155],v[172:175],a[216:219]\n"
"ds_read_b64_tr_b16 v[230:231],v99 offset:33792\n"
"s_waitcnt lgkmcnt(10)\n"
"v_mfma_f32_16x16x32_bf16 a[224:227],v[152:155],v[176:179],a[224:227]\n"
"ds_read_b64_tr_b16 v[232:233],v100 offset:32768\n"
"v_mfma_f32_16x16x32_bf16 a[228:231],v[156:159],v[176:179],a[228:231]\n"
"ds_read_b64_tr_b16 v[234:235],v100 offset:33792\n"
"v_mfma_f32_16x16x32_bf16 a[236:239],v[156:159],v[180:183],a[236:239]\n"
"ds_read_b64_tr_b16 v[236:237],v101 offset:32768\n"
"v_mfma_f32_16x16x32_bf16 a[232:235],v[152:155],v[180:183],a[232:235]\n"
"ds_read_b64_tr_b16 v[238:239],v101 offset:33792\n"
"s_waitcnt lgkmcnt(10)\n"
"v_mfma_f32_16x16x32_bf16 a[240:243],v[152:155],v[184:187],a[240:243]\n"
"ds_read_b64_tr_b16 v[240:241],v102 offset:32768\n"
"v_mfma_f32_16x16x32_bf16 a[244:247],v[156:159],v[184:187],a[244:247]\n"
"ds_read_b64_tr_b16 v[242:243],v102 offset:33792\n"
"v_mfma_f32_16x16x32_bf16 a[252:255],v[156:159],v[188:191],a[252:255]\n"
"ds_read_b64_tr_b16 v[244:245],v103 offset:32768\n"
"v_mfma_f32_16x16x32_bf16 a[248:251],v[152:155],v[188:191],a[248:251]\n"
"ds_read_b64_tr_b16 v[246:247],v103 offset:33792\n"
"s_waitcnt lgkmcnt(8)\n"
"v_mfma_f32_16x16x32_bf16 a[0:3],v[128:131],v[160:163],a[0:3]\n"
"ds_read_b64_tr_b16 v[248:249],v104 offset:32768\n"
"v_mfma_f32_16x16x32_bf16 a[0:3],v[192:195],v[224:227],a[0:3]\n"
"ds_read_b64_tr_b16 v[250:251],v104 offset:33792\n"
"v_mfma_f32_16x16x32_bf16 a[8:11],v[192:195],v[228:231],a[8:11]\n"
"ds_read_b64_tr_b16 v[252:253],v105 offset:32768\n"
"v_mfma_f32_16x16x32_bf16 a[8:11],v[128:131],v[164:167],a[8:11]\n"
"ds_read_b64_tr_b16 v[254:255],v105 offset:33792\n"
"s_waitcnt lgkmcnt(8)\n"
"v_mfma_f32_16x16x32_bf16 a[16:19],v[128:131],v[168:171],a[16:19]\n"
"ds_read_b128 v[132:135],v80 offset:34816\n"
"v_mfma_f32_16x16x32_bf16 a[16:19],v[192:195],v[232:235],a[16:19]\n"
"ds_read_b128 v[196:199],v81 offset:34816\n"
"v_mfma_f32_16x16x32_bf16 a[24:27],v[192:195],v[236:239],a[24:27]\n"
"ds_read_b128 v[136:139],v80 offset:36864\n"
"v_mfma_f32_16x16x32_bf16 a[24:27],v[128:131],v[172:175],a[24:27]\n"
"ds_read_b128 v[200:203],v81 offset:36864\n"
"s_waitcnt lgkmcnt(8)\n"
"v_mfma_f32_16x16x32_bf16 a[32:35],v[128:131],v[176:179],a[32:35]\n"
"ds_read_b128 v[140:143],v80 offset:38912\n"
"v_mfma_f32_16x16x32_bf16 a[32:35],v[192:195],v[240:243],a[32:35]\n"
"ds_read_b128 v[204:207],v81 offset:38912\n"
"v_mfma_f32_16x16x32_bf16 a[40:43],v[192:195],v[244:247],a[40:43]\n"
"ds_read_b128 v[144:147],v80 offset:40960\n"
"v_mfma_f32_16x16x32_bf16 a[40:43],v[128:131],v[180:183],a[40:43]\n"
"ds_read_b128 v[208:211],v81 offset:40960\n"
"s_waitcnt lgkmcnt(8)\n"
"v_mfma_f32_16x16x32_bf16 a[48:51],v[128:131],v[184:187],a[48:51]\n"
"ds_read_b128 v[148:151],v80 offset:43008\n"
"v_mfma_f32_16x16x32_bf16 a[48:51],v[192:195],v[248:251],a[48:51]\n"
"ds_read_b128 v[212:215],v81 offset:43008\n"
"v_mfma_f32_16x16x32_bf16 a[56:59],v[192:195],v[252:255],a[56:59]\n"
"ds_read_b128 v[216:219],v81 offset:45056\n"
"v_mfma_f32_16x16x32_bf16 a[56:59],v[128:131],v[188:191],a[56:59]\n"
"ds_read_b128 v[220:223],v81 offset:47104\n"
"s_waitcnt lgkmcnt(10)\n"
"v_mfma_f32_16x16x32_bf16 a[4:7],v[132:135],v[160:163],a[4:7]\n"
"v_mfma_f32_16x16x32_bf16 a[4:7],v[196:199],v[224:227],a[4:7]\n"
"v_mfma_f32_16x16x32_bf16 a[12:15],v[196:199],v[228:231],a[12:15]\n"
"v_mfma_f32_16x16x32_bf16 a[12:15],v[132:135],v[164:167],a[12:15]\n"
"v_mfma_f32_16x16x32_bf16 a[20:23],v[132:135],v[168:171],a[20:23]\n"
"s_waitcnt lgkmcnt(0)\n"
"s_barrier\n"
"v_mfma_f32_16x16x32_bf16 a[20:23],v[196:199],v[232:235],a[20:23]\n"
"s_mov_b32 s77,3145728\n"
"s_add_u32 m0,s78,147456\n"
"v_mfma_f32_16x16x32_bf16 a[28:31],v[196:199],v[236:239],a[28:31]\n"
"buffer_load_dwordx4 v110,s[20:23],s77 offen offset:0 lds\n"
"v_mfma_f32_16x16x32_bf16 a[28:31],v[132:135],v[172:175],a[28:31]\n"
"v_mfma_f32_16x16x32_bf16 a[36:39],v[132:135],v[176:179],a[36:39]\n"
"v_mfma_f32_16x16x32_bf16 a[36:39],v[196:199],v[240:243],a[36:39]\n"
"v_mfma_f32_16x16x32_bf16 a[44:47],v[196:199],v[244:247],a[44:47]\n"
"buffer_load_dwordx4 v111,s[20:23],s77 offen offset:1024 lds\n"
"v_mfma_f32_16x16x32_bf16 a[44:47],v[132:135],v[180:183],a[44:47]\n"
"v_mfma_f32_16x16x32_bf16 a[52:55],v[132:135],v[184:187],a[52:55]\n"
"v_mfma_f32_16x16x32_bf16 a[52:55],v[196:199],v[248:251],a[52:55]\n"
"v_mfma_f32_16x16x32_bf16 a[60:63],v[196:199],v[252:255],a[60:63]\n"
"buffer_load_dwordx4 v113,s[20:23],s77 offen offset:2048 lds\n"
"v_mfma_f32_16x16x32_bf16 a[60:63],v[132:135],v[188:191],a[60:63]\n"
"v_mfma_f32_16x16x32_bf16 a[64:67],v[136:139],v[160:163],a[64:67]\n"
"v_mfma_f32_16x16x32_bf16 a[64:67],v[200:203],v[224:227],a[64:67]\n"
"v_mfma_f32_16x16x32_bf16 a[72:75],v[200:203],v[228:231],a[72:75]\n"
"buffer_load_dwordx4 v114,s[20:23],s77 offen offset:3072 lds\n"
"v_mfma_f32_16x16x32_bf16 a[72:75],v[136:139],v[164:167],a[72:75]\n"
"v_mfma_f32_16x16x32_bf16 a[80:83],v[136:139],v[168:171],a[80:83]\n"
"v_mfma_f32_16x16x32_bf16 a[80:83],v[200:203],v[232:235],a[80:83]\n"
"s_mov_b32 s77,3407872\n"
"s_add_u32 m0,s78,131072\n"
"v_mfma_f32_16x16x32_bf16 a[88:91],v[200:203],v[236:239],a[88:91]\n"
"buffer_load_dwordx4 v110,s[20:23],s77 offen offset:0 lds\n"
"v_mfma_f32_16x16x32_bf16 a[88:91],v[136:139],v[172:175],a[88:91]\n"
"v_mfma_f32_16x16x32_bf16 a[96:99],v[136:139],v[176:179],a[96:99]\n"
"v_mfma_f32_16x16x32_bf16 a[96:99],v[200:203],v[240:243],a[96:99]\n"
"v_mfma_f32_16x16x32_bf16 a[104:107],v[200:203],v[244:247],a[104:107]\n"
"buffer_load_dwordx4 v111,s[20:23],s77 offen offset:1024 lds\n"
"v_mfma_f32_16x16x32_bf16 a[104:107],v[136:139],v[180:183],a[104:107]\n"
"v_mfma_f32_16x16x32_bf16 a[112:115],v[136:139],v[184:187],a[112:115]\n"
"v_mfma_f32_16x16x32_bf16 a[112:115],v[200:203],v[248:251],a[112:115]\n"
"v_mfma_f32_16x16x32_bf16 a[120:123],v[200:203],v[252:255],a[120:123]\n"
"buffer_load_dwordx4 v113,s[20:23],s77 offen offset:2048 lds\n"
"v_mfma_f32_16x16x32_bf16 a[120:123],v[136:139],v[188:191],a[120:123]\n"
"v_mfma_f32_16x16x32_bf16 a[68:71],v[140:143],v[160:163],a[68:71]\n"
"v_mfma_f32_16x16x32_bf16 a[68:71],v[204:207],v[224:227],a[68:71]\n"
"v_mfma_f32_16x16x32_bf16 a[76:79],v[204:207],v[228:231],a[76:79]\n"
"buffer_load_dwordx4 v114,s[20:23],s77 offen offset:3072 lds\n"
"v_mfma_f32_16x16x32_bf16 a[76:79],v[140:143],v[164:167],a[76:79]\n"
"v_mfma_f32_16x16x32_bf16 a[84:87],v[140:143],v[168:171],a[84:87]\n"
"v_mfma_f32_16x16x32_bf16 a[84:87],v[204:207],v[232:235],a[84:87]\n"
"v_mfma_f32_16x16x32_bf16 a[92:95],v[204:207],v[236:239],a[92:95]\n"
"s_mov_b32 s76,896\n"
"s_add_u32 m0,s79,32768\n"
"v_mfma_f32_16x16x32_bf16 a[92:95],v[140:143],v[172:175],a[92:95]\n"
"buffer_load_dwordx4 v106,s[16:19],s76 offen offset:0 lds\n"
"v_mfma_f32_16x16x32_bf16 a[100:103],v[140:143],v[176:179],a[100:103]\n"
"v_mfma_f32_16x16x32_bf16 a[100:103],v[204:207],v[240:243],a[100:103]\n"
"v_mfma_f32_16x16x32_bf16 a[108:111],v[204:207],v[244:247],a[108:111]\n"
"v_mfma_f32_16x16x32_bf16 a[108:111],v[140:143],v[180:183],a[108:111]\n"
"v_mfma_f32_16x16x32_bf16 a[116:119],v[140:143],v[184:187],a[116:119]\n"
"buffer_load_dwordx4 v107,s[16:19],s76 offen offset:1024 lds\n"
"v_mfma_f32_16x16x32_bf16 a[116:119],v[204:207],v[248:251],a[116:119]\n"
"v_mfma_f32_16x16x32_bf16 a[124:127],v[204:207],v[252:255],a[124:127]\n"
"v_mfma_f32_16x16x32_bf16 a[124:127],v[140:143],v[188:191],a[124:127]\n"
"v_mfma_f32_16x16x32_bf16 a[128:131],v[144:147],v[160:163],a[128:131]\n"
"v_mfma_f32_16x16x32_bf16 a[128:131],v[208:211],v[224:227],a[128:131]\n"
"buffer_load_dwordx4 v108,s[16:19],s76 offen offset:2048 lds\n"
"v_mfma_f32_16x16x32_bf16 a[136:139],v[208:211],v[228:231],a[136:139]\n"
"v_mfma_f32_16x16x32_bf16 a[136:139],v[144:147],v[164:167],a[136:139]\n"
"v_mfma_f32_16x16x32_bf16 a[144:147],v[144:147],v[168:171],a[144:147]\n"
"v_mfma_f32_16x16x32_bf16 a[144:147],v[208:211],v[232:235],a[144:147]\n"
"v_mfma_f32_16x16x32_bf16 a[152:155],v[208:211],v[236:239],a[152:155]\n"
"buffer_load_dwordx4 v109,s[16:19],s76 offen offset:3072 lds\n"
"v_mfma_f32_16x16x32_bf16 a[152:155],v[144:147],v[172:175],a[152:155]\n"
"v_mfma_f32_16x16x32_bf16 a[160:163],v[144:147],v[176:179],a[160:163]\n"
"v_mfma_f32_16x16x32_bf16 a[160:163],v[208:211],v[240:243],a[160:163]\n"
"v_mfma_f32_16x16x32_bf16 a[168:171],v[208:211],v[244:247],a[168:171]\n"
"s_mov_b32 s76,263040\n"
"s_add_u32 m0,s79,36864\n"
"v_mfma_f32_16x16x32_bf16 a[168:171],v[144:147],v[180:183],a[168:171]\n"
"buffer_load_dwordx4 v106,s[16:19],s76 offen offset:0 lds\n"
"v_mfma_f32_16x16x32_bf16 a[176:179],v[144:147],v[184:187],a[176:179]\n"
"v_mfma_f32_16x16x32_bf16 a[176:179],v[208:211],v[248:251],a[176:179]\n"
"v_mfma_f32_16x16x32_bf16 a[184:187],v[208:211],v[252:255],a[184:187]\n"
"v_mfma_f32_16x16x32_bf16 a[184:187],v[144:147],v[188:191],a[184:187]\n"
"v_mfma_f32_16x16x32_bf16 a[132:135],v[148:151],v[160:163],a[132:135]\n"
"buffer_load_dwordx4 v107,s[16:19],s76 offen offset:1024 lds\n"
"v_mfma_f32_16x16x32_bf16 a[132:135],v[212:215],v[224:227],a[132:135]\n"
"v_mfma_f32_16x16x32_bf16 a[140:143],v[212:215],v[228:231],a[140:143]\n"
"v_mfma_f32_16x16x32_bf16 a[140:143],v[148:151],v[164:167],a[140:143]\n"
"v_mfma_f32_16x16x32_bf16 a[148:151],v[148:151],v[168:171],a[148:151]\n"
"v_mfma_f32_16x16x32_bf16 a[148:151],v[212:215],v[232:235],a[148:151]\n"
"buffer_load_dwordx4 v108,s[16:19],s76 offen offset:2048 lds\n"
"v_mfma_f32_16x16x32_bf16 a[156:159],v[212:215],v[236:239],a[156:159]\n"
"v_mfma_f32_16x16x32_bf16 a[156:159],v[148:151],v[172:175],a[156:159]\n"
"v_mfma_f32_16x16x32_bf16 a[164:167],v[148:151],v[176:179],a[164:167]\n"
"v_mfma_f32_16x16x32_bf16 a[164:167],v[212:215],v[240:243],a[164:167]\n"
"v_mfma_f32_16x16x32_bf16 a[172:175],v[212:215],v[244:247],a[172:175]\n"
"buffer_load_dwordx4 v109,s[16:19],s76 offen offset:3072 lds\n"
"v_mfma_f32_16x16x32_bf16 a[172:175],v[148:151],v[180:183],a[172:175]\n"
"s_mov_b32 s87,6291456\n"
"v_mfma_f32_16x16x32_bf16 a[180:183],v[148:151],v[184:187],a[180:183]\n"
"buffer_load_dword v115,v112,s[40:43],s87 offen\n"
"v_mfma_f32_16x16x32_bf16 a[180:183],v[212:215],v[248:251],a[180:183]\n"
"s_mov_b32 s87,7340032\n"
"v_mfma_f32_16x16x32_bf16 a[188:191],v[212:215],v[252:255],a[188:191]\n"
"buffer_load_dword v115,v112,s[40:43],s87 offen\n"
"v_mfma_f32_16x16x32_bf16 a[188:191],v[148:151],v[188:191],a[188:191]\n"
"s_waitcnt vmcnt(30)\n"
"s_barrier\n"
"v_mfma_f32_16x16x32_bf16 a[192:195],v[216:219],v[224:227],a[192:195]\n"
"ds_read_b128 v[152:155],v82 offset:12288\n"
"v_mfma_f32_16x16x32_bf16 a[196:199],v[220:223],v[224:227],a[196:199]\n"
"ds_read_b128 v[156:159],v82 offset:14336\n"
"v_mfma_f32_16x16x32_bf16 a[204:207],v[220:223],v[228:231],a[204:207]\n"
"ds_read_b64_tr_b16 v[160:161],v98 offset:0\n"
"v_mfma_f32_16x16x32_bf16 a[200:203],v[216:219],v[228:231],a[200:203]\n"
"ds_read_b64_tr_b16 v[162:163],v98 offset:1024\n"
"v_mfma_f32_16x16x32_bf16 a[208:211],v[216:219],v[232:235],a[208:211]\n"
"ds_read_b64_tr_b16 v[164:165],v99 offset:0\n"
"v_mfma_f32_16x16x32_bf16 a[212:215],v[220:223],v[232:235],a[212:215]\n"
"ds_read_b64_tr_b16 v[166:167],v99 offset:1024\n"
"v_mfma_f32_16x16x32_bf16 a[220:223],v[220:223],v[236:239],a[220:223]\n"
"ds_read_b64_tr_b16 v[168:169],v100 offset:0\n"
"v_mfma_f32_16x16x32_bf16 a[216:219],v[216:219],v[236:239],a[216:219]\n"
"ds_read_b64_tr_b16 v[170:171],v100 offset:1024\n"
"v_mfma_f32_16x16x32_bf16 a[224:227],v[216:219],v[240:243],a[224:227]\n"
"ds_read_b64_tr_b16 v[172:173],v101 offset:0\n"
"v_mfma_f32_16x16x32_bf16 a[228:231],v[220:223],v[240:243],a[228:231]\n"
"ds_read_b64_tr_b16 v[174:175],v101 offset:1024\n"
"v_mfma_f32_16x16x32_bf16 a[236:239],v[220:223],v[244:247],a[236:239]\n"
"ds_read_b64_tr_b16 v[176:177],v102 offset:0\n"
"v_mfma_f32_16x16x32_bf16 a[232:235],v[216:219],v[244:247],a[232:235]\n"
"ds_read_b64_tr_b16 v[178:179],v102 offset:1024\n"
"v_mfma_f32_16x16x32_bf16 a[240:243],v[216:219],v[248:251],a[240:243]\n"
"ds_read_b64_tr_b16 v[180:181],v103 offset:0\n"
"v_mfma_f32_16x16x32_bf16 a[244:247],v[220:223],v[248:251],a[244:247]\n"
"ds_read_b64_tr_b16 v[182:183],v103 offset:1024\n"
"v_mfma_f32_16x16x32_bf16 a[252:255],v[220:223],v[252:255],a[252:255]\n"
"ds_read_b64_tr_b16 v[184:185],v104 offset:0\n"
"v_mfma_f32_16x16x32_bf16 a[248:251],v[216:219],v[252:255],a[248:251]\n"
"ds_read_b64_tr_b16 v[186:187],v104 offset:1024\n"
"s_waitcnt lgkmcnt(10)\n"
"v_mfma_f32_16x16x32_bf16 a[192:195],v[152:155],v[160:163],a[192:195]\n"
"ds_read_b64_tr_b16 v[188:189],v105 offset:0\n"
"v_mfma_f32_16x16x32_bf16 a[196:199],v[156:159],v[160:163],a[196:199]\n"
"ds_read_b64_tr_b16 v[190:191],v105 offset:1024\n"
"v_mfma_f32_16x16x32_bf16 a[204:207],v[156:159],v[164:167],a[204:207]\n"
"ds_read_b128 v[128:131],v82 offset:0\n"
"v_mfma_f32_16x16x32_bf16 a[200:203],v[152:155],v[164:167],a[200:203]\n"
"ds_read_b128 v[192:195],v83 offset:0\n"
"s_waitcnt lgkmcnt(10)\n"
"v_mfma_f32_16x16x32_bf16 a[208:211],v[152:155],v[168:171],a[208:211]\n"
"ds_read_b64_tr_b16 v[224:225],v98 offset:16384\n"
"v_mfma_f32_16x16x32_bf16 a[212:215],v[156:159],v[168:171],a[212:215]\n"
"ds_read_b64_tr_b16 v[226:227],v98 offset:17408\n"
"v_mfma_f32_16x16x32_bf16 a[220:223],v[156:159],v[172:175],a[220:223]\n"
"ds_read_b64_tr_b16 v[228:229],v99 offset:16384\n"
"v_mfma_f32_16x16x32_bf16 a[216:219],v[152:155],v[172:175],a[216:219]\n"
"ds_read_b64_tr_b16 v[230:231],v99 offset:17408\n"
"s_waitcnt lgkmcnt(10)\n"
"v_mfma_f32_16x16x32_bf16 a[224:227],v[152:155],v[176:179],a[224:227]\n"
"ds_read_b64_tr_b16 v[232:233],v100 offset:16384\n"
"v_mfma_f32_16x16x32_bf16 a[228:231],v[156:159],v[176:179],a[228:231]\n"
"ds_read_b64_tr_b16 v[234:235],v100 offset:17408\n"
"v_mfma_f32_16x16x32_bf16 a[236:239],v[156:159],v[180:183],a[236:239]\n"
"ds_read_b64_tr_b16 v[236:237],v101 offset:16384\n"
"v_mfma_f32_16x16x32_bf16 a[232:235],v[152:155],v[180:183],a[232:235]\n"
"ds_read_b64_tr_b16 v[238:239],v101 offset:17408\n"
"s_waitcnt lgkmcnt(10)\n"
"v_mfma_f32_16x16x32_bf16 a[240:243],v[152:155],v[184:187],a[240:243]\n"
"ds_read_b64_tr_b16 v[240:241],v102 offset:16384\n"
"v_mfma_f32_16x16x32_bf16 a[244:247],v[156:159],v[184:187],a[244:247]\n"
"ds_read_b64_tr_b16 v[242:243],v102 offset:17408\n"
"v_mfma_f32_16x16x32_bf16 a[252:255],v[156:159],v[188:191],a[252:255]\n"
"ds_read_b64_tr_b16 v[244:245],v103 offset:16384\n"
"v_mfma_f32_16x16x32_bf16 a[248:251],v[152:155],v[188:191],a[248:251]\n"
"ds_read_b64_tr_b16 v[246:247],v103 offset:17408\n"
"s_waitcnt lgkmcnt(8)\n"
"v_mfma_f32_16x16x32_bf16 a[0:3],v[128:131],v[160:163],a[0:3]\n"
"ds_read_b64_tr_b16 v[248:249],v104 offset:16384\n"
"v_mfma_f32_16x16x32_bf16 a[0:3],v[192:195],v[224:227],a[0:3]\n"
"ds_read_b64_tr_b16 v[250:251],v104 offset:17408\n"
"v_mfma_f32_16x16x32_bf16 a[8:11],v[192:195],v[228:231],a[8:11]\n"
"ds_read_b64_tr_b16 v[252:253],v105 offset:16384\n"
"v_mfma_f32_16x16x32_bf16 a[8:11],v[128:131],v[164:167],a[8:11]\n"
"ds_read_b64_tr_b16 v[254:255],v105 offset:17408\n"
"s_waitcnt lgkmcnt(8)\n"
"v_mfma_f32_16x16x32_bf16 a[16:19],v[128:131],v[168:171],a[16:19]\n"
"ds_read_b128 v[132:135],v82 offset:2048\n"
"v_mfma_f32_16x16x32_bf16 a[16:19],v[192:195],v[232:235],a[16:19]\n"
"ds_read_b128 v[196:199],v83 offset:2048\n"
"v_mfma_f32_16x16x32_bf16 a[24:27],v[192:195],v[236:239],a[24:27]\n"
"ds_read_b128 v[136:139],v82 offset:4096\n"
"v_mfma_f32_16x16x32_bf16 a[24:27],v[128:131],v[172:175],a[24:27]\n"
"ds_read_b128 v[200:203],v83 offset:4096\n"
"s_waitcnt lgkmcnt(8)\n"
"v_mfma_f32_16x16x32_bf16 a[32:35],v[128:131],v[176:179],a[32:35]\n"
"ds_read_b128 v[140:143],v82 offset:6144\n"
"v_mfma_f32_16x16x32_bf16 a[32:35],v[192:195],v[240:243],a[32:35]\n"
"ds_read_b128 v[204:207],v83 offset:6144\n"
"v_mfma_f32_16x16x32_bf16 a[40:43],v[192:195],v[244:247],a[40:43]\n"
"ds_read_b128 v[144:147],v82 offset:8192\n"
"v_mfma_f32_16x16x32_bf16 a[40:43],v[128:131],v[180:183],a[40:43]\n"
"ds_read_b128 v[208:211],v83 offset:8192\n"
"s_waitcnt lgkmcnt(8)\n"
"v_mfma_f32_16x16x32_bf16 a[48:51],v[128:131],v[184:187],a[48:51]\n"
"ds_read_b128 v[148:151],v82 offset:10240\n"
"v_mfma_f32_16x16x32_bf16 a[48:51],v[192:195],v[248:251],a[48:51]\n"
"ds_read_b128 v[212:215],v83 offset:10240\n"
"v_mfma_f32_16x16x32_bf16 a[56:59],v[192:195],v[252:255],a[56:59]\n"
"ds_read_b128 v[216:219],v83 offset:12288\n"
"v_mfma_f32_16x16x32_bf16 a[56:59],v[128:131],v[188:191],a[56:59]\n"
"ds_read_b128 v[220:223],v83 offset:14336\n"
"s_waitcnt lgkmcnt(10)\n"
"v_mfma_f32_16x16x32_bf16 a[4:7],v[132:135],v[160:163],a[4:7]\n"
"v_mfma_f32_16x16x32_bf16 a[4:7],v[196:199],v[224:227],a[4:7]\n"
"buffer_load_dwordx4 v[74:77],v112,s[24:27],s58 offen sc1\n"
"v_mfma_f32_16x16x32_bf16 a[12:15],v[196:199],v[228:231],a[12:15]\n"
"v_mfma_f32_16x16x32_bf16 a[12:15],v[132:135],v[164:167],a[12:15]\n"
"v_mfma_f32_16x16x32_bf16 a[20:23],v[132:135],v[168:171],a[20:23]\n"
"s_waitcnt lgkmcnt(0)\n"
"s_barrier\n"
"v_mfma_f32_16x16x32_bf16 a[20:23],v[196:199],v[232:235],a[20:23]\n"
"s_mov_b32 s77,3670016\n"
"s_add_u32 m0,s78,98304\n"
"v_mfma_f32_16x16x32_bf16 a[28:31],v[196:199],v[236:239],a[28:31]\n"
"buffer_load_dwordx4 v110,s[20:23],s77 offen offset:0 lds\n"
"v_mfma_f32_16x16x32_bf16 a[28:31],v[132:135],v[172:175],a[28:31]\n"
"v_mfma_f32_16x16x32_bf16 a[36:39],v[132:135],v[176:179],a[36:39]\n"
"s_load_dwordx2 s[48:49],s[0:1],56\n"
"v_mfma_f32_16x16x32_bf16 a[36:39],v[196:199],v[240:243],a[36:39]\n"
"v_mfma_f32_16x16x32_bf16 a[44:47],v[196:199],v[244:247],a[44:47]\n"
"buffer_load_dwordx4 v111,s[20:23],s77 offen offset:1024 lds\n"
"v_mfma_f32_16x16x32_bf16 a[44:47],v[132:135],v[180:183],a[44:47]\n"
"v_mfma_f32_16x16x32_bf16 a[52:55],v[132:135],v[184:187],a[52:55]\n"
"v_mfma_f32_16x16x32_bf16 a[52:55],v[196:199],v[248:251],a[52:55]\n"
"v_mfma_f32_16x16x32_bf16 a[60:63],v[196:199],v[252:255],a[60:63]\n"
"buffer_load_dwordx4 v113,s[20:23],s77 offen offset:2048 lds\n"
"v_mfma_f32_16x16x32_bf16 a[60:63],v[132:135],v[188:191],a[60:63]\n"
"v_mfma_f32_16x16x32_bf16 a[64:67],v[136:139],v[160:163],a[64:67]\n"
"v_mfma_f32_16x16x32_bf16 a[64:67],v[200:203],v[224:227],a[64:67]\n"
"v_mfma_f32_16x16x32_bf16 a[72:75],v[200:203],v[228:231],a[72:75]\n"
"buffer_load_dwordx4 v114,s[20:23],s77 offen offset:3072 lds\n"
"v_mfma_f32_16x16x32_bf16 a[72:75],v[136:139],v[164:167],a[72:75]\n"
"v_mfma_f32_16x16x32_bf16 a[80:83],v[136:139],v[168:171],a[80:83]\n"
"v_mfma_f32_16x16x32_bf16 a[80:83],v[200:203],v[232:235],a[80:83]\n"
"s_mov_b32 s77,3932160\n"
"s_add_u32 m0,s78,114688\n"
"v_mfma_f32_16x16x32_bf16 a[88:91],v[200:203],v[236:239],a[88:91]\n"
"buffer_load_dwordx4 v110,s[20:23],s77 offen offset:0 lds\n"
"v_mfma_f32_16x16x32_bf16 a[88:91],v[136:139],v[172:175],a[88:91]\n"
"v_mfma_f32_16x16x32_bf16 a[96:99],v[136:139],v[176:179],a[96:99]\n"
"v_mfma_f32_16x16x32_bf16 a[96:99],v[200:203],v[240:243],a[96:99]\n"
"v_mfma_f32_16x16x32_bf16 a[104:107],v[200:203],v[244:247],a[104:107]\n"
"buffer_load_dwordx4 v111,s[20:23],s77 offen offset:1024 lds\n"
"v_mfma_f32_16x16x32_bf16 a[104:107],v[136:139],v[180:183],a[104:107]\n"
"v_mfma_f32_16x16x32_bf16 a[112:115],v[136:139],v[184:187],a[112:115]\n"
"v_mfma_f32_16x16x32_bf16 a[112:115],v[200:203],v[248:251],a[112:115]\n"
"v_mfma_f32_16x16x32_bf16 a[120:123],v[200:203],v[252:255],a[120:123]\n"
"buffer_load_dwordx4 v113,s[20:23],s77 offen offset:2048 lds\n"
"v_mfma_f32_16x16x32_bf16 a[120:123],v[136:139],v[188:191],a[120:123]\n"
"s_mov_b32 s76,s55\n"
"s_waitcnt vmcnt(7)\n"
"v_mfma_f32_16x16x32_bf16 a[68:71],v[140:143],v[160:163],a[68:71]\n"
"v_xor_b32 v116,s80,v74\n"
"v_xor_b32 v117,s81,v75\n"
"v_mfma_f32_16x16x32_bf16 a[68:71],v[204:207],v[224:227],a[68:71]\n"
"v_or_b32 v116,v116,v117\n"
"v_xor_b32 v117,s80,v76\n"
"v_mfma_f32_16x16x32_bf16 a[76:79],v[204:207],v[228:231],a[76:79]\n"
"buffer_load_dwordx4 v114,s[20:23],s77 offen offset:3072 lds\n"
"v_mfma_f32_16x16x32_bf16 a[76:79],v[140:143],v[164:167],a[76:79]\n"
"v_or_b32 v116,v116,v117\n"
"v_xor_b32 v117,s81,v77\n"
"v_mfma_f32_16x16x32_bf16 a[84:87],v[140:143],v[168:171],a[84:87]\n"
"v_or_b32 v116,v116,v117\n"
"v_cmp_ne_u32 vcc,0,v116\n"
"v_mfma_f32_16x16x32_bf16 a[84:87],v[204:207],v[232:235],a[84:87]\n"
"s_cbranch_vccnz .Lfk_xn1_s\n"
".Lfk_xn1_ok:\n"
"v_mfma_f32_16x16x32_bf16 a[92:95],v[204:207],v[236:239],a[92:95]\n"
"s_add_u32 m0,s78,65536\n"
"v_mfma_f32_16x16x32_bf16 a[92:95],v[140:143],v[172:175],a[92:95]\n"
"buffer_load_dwordx4 v112,s[24:27],s76 offen offset:0 sc1 lds\n"
"v_mfma_f32_16x16x32_bf16 a[100:103],v[140:143],v[176:179],a[100:103]\n"
"s_waitcnt lgkmcnt(0)\n"
"s_lshl2_add_u32 s88,s100,s52\n"
"v_mfma_f32_16x16x32_bf16 a[100:103],v[204:207],v[240:243],a[100:103]\n"
"s_mul_i32 s88,s88,7168\n"
"s_add_u32 s88,s88,0\n"
"v_mfma_f32_16x16x32_bf16 a[108:111],v[204:207],v[244:247],a[108:111]\n"
"s_add_u32 s48,s48,s88\n"
"s_addc_u32 s49,s49,0\n"
"s_mov_b32 s50,3584\n"
"v_mfma_f32_16x16x32_bf16 a[108:111],v[140:143],v[180:183],a[108:111]\n"
"s_mov_b32 s51,0x20000\n"
"v_lshlrev_b32 v8,3,v112\n"
"v_mfma_f32_16x16x32_bf16 a[116:119],v[140:143],v[184:187],a[116:119]\n"
"buffer_load_dwordx4 v112,s[24:27],s76 offen offset:1024 sc1 lds\n"
"v_mfma_f32_16x16x32_bf16 a[116:119],v[204:207],v[248:251],a[116:119]\n"
"v_mfma_f32_16x16x32_bf16 a[124:127],v[204:207],v[252:255],a[124:127]\n"
"v_mfma_f32_16x16x32_bf16 a[124:127],v[140:143],v[188:191],a[124:127]\n"
"v_mfma_f32_16x16x32_bf16 a[128:131],v[144:147],v[160:163],a[128:131]\n"
"v_mfma_f32_16x16x32_bf16 a[128:131],v[208:211],v[224:227],a[128:131]\n"
"buffer_load_dwordx4 v112,s[24:27],s76 offen offset:2048 sc1 lds\n"
"v_mfma_f32_16x16x32_bf16 a[136:139],v[208:211],v[228:231],a[136:139]\n"
"v_mfma_f32_16x16x32_bf16 a[136:139],v[144:147],v[164:167],a[136:139]\n"
"v_mfma_f32_16x16x32_bf16 a[144:147],v[144:147],v[168:171],a[144:147]\n"
"v_mfma_f32_16x16x32_bf16 a[144:147],v[208:211],v[232:235],a[144:147]\n"
"buffer_load_dwordx4 v112,s[24:27],s76 offen offset:3072 sc1 lds\n"
"v_mfma_f32_16x16x32_bf16 a[152:155],v[208:211],v[236:239],a[152:155]\n"
"s_xor_b32 s76,s76,16384\n"
"v_mfma_f32_16x16x32_bf16 a[152:155],v[144:147],v[172:175],a[152:155]\n"
"v_mfma_f32_16x16x32_bf16 a[160:163],v[144:147],v[176:179],a[160:163]\n"
"v_mfma_f32_16x16x32_bf16 a[160:163],v[208:211],v[240:243],a[160:163]\n"
"v_mfma_f32_16x16x32_bf16 a[168:171],v[208:211],v[244:247],a[168:171]\n"
"s_add_u32 m0,s78,81920\n"
"v_mfma_f32_16x16x32_bf16 a[168:171],v[144:147],v[180:183],a[168:171]\n"
"buffer_load_dwordx4 v112,s[24:27],s76 offen offset:0 sc1 lds\n"
"v_mfma_f32_16x16x32_bf16 a[176:179],v[144:147],v[184:187],a[176:179]\n"
"v_mfma_f32_16x16x32_bf16 a[176:179],v[208:211],v[248:251],a[176:179]\n"
"v_mfma_f32_16x16x32_bf16 a[184:187],v[208:211],v[252:255],a[184:187]\n"
"v_mfma_f32_16x16x32_bf16 a[184:187],v[144:147],v[188:191],a[184:187]\n"
"v_mfma_f32_16x16x32_bf16 a[132:135],v[148:151],v[160:163],a[132:135]\n"
"buffer_load_dwordx4 v112,s[24:27],s76 offen offset:1024 sc1 lds\n"
"v_mfma_f32_16x16x32_bf16 a[132:135],v[212:215],v[224:227],a[132:135]\n"
"v_mfma_f32_16x16x32_bf16 a[140:143],v[212:215],v[228:231],a[140:143]\n"
"v_mfma_f32_16x16x32_bf16 a[140:143],v[148:151],v[164:167],a[140:143]\n"
"v_mfma_f32_16x16x32_bf16 a[148:151],v[148:151],v[168:171],a[148:151]\n"
"v_mfma_f32_16x16x32_bf16 a[148:151],v[212:215],v[232:235],a[148:151]\n"
"buffer_load_dwordx4 v112,s[24:27],s76 offen offset:2048 sc1 lds\n"
"v_mfma_f32_16x16x32_bf16 a[156:159],v[212:215],v[236:239],a[156:159]\n"
"v_mfma_f32_16x16x32_bf16 a[156:159],v[148:151],v[172:175],a[156:159]\n"
"v_mfma_f32_16x16x32_bf16 a[164:167],v[148:151],v[176:179],a[164:167]\n"
"v_mfma_f32_16x16x32_bf16 a[164:167],v[212:215],v[240:243],a[164:167]\n"
"buffer_load_dwordx4 v112,s[24:27],s76 offen offset:3072 sc1 lds\n"
"v_mfma_f32_16x16x32_bf16 a[172:175],v[212:215],v[244:247],a[172:175]\n"
"s_xor_b32 s76,s76,49152\n"
"v_mfma_f32_16x16x32_bf16 a[172:175],v[148:151],v[180:183],a[172:175]\n"
"v_mfma_f32_16x16x32_bf16 a[180:183],v[148:151],v[184:187],a[180:183]\n"
"buffer_load_dword v115,v8,s[48:51],0 offen\n"
"v_mfma_f32_16x16x32_bf16 a[180:183],v[212:215],v[248:251],a[180:183]\n"
"v_mfma_f32_16x16x32_bf16 a[188:191],v[212:215],v[252:255],a[188:191]\n"
"v_mfma_f32_16x16x32_bf16 a[188:191],v[148:151],v[188:191],a[188:191]\n"
"s_barrier\n"
"v_mfma_f32_16x16x32_bf16 a[192:195],v[216:219],v[224:227],a[192:195]\n"
"ds_read_b128 v[152:155],v80 offset:12288\n"
"v_mfma_f32_16x16x32_bf16 a[196:199],v[220:223],v[224:227],a[196:199]\n"
"ds_read_b128 v[156:159],v80 offset:14336\n"
"v_mfma_f32_16x16x32_bf16 a[204:207],v[220:223],v[228:231],a[204:207]\n"
"ds_read_b64_tr_b16 v[160:161],v98 offset:49152\n"
"v_mfma_f32_16x16x32_bf16 a[200:203],v[216:219],v[228:231],a[200:203]\n"
"ds_read_b64_tr_b16 v[162:163],v98 offset:50176\n"
"v_mfma_f32_16x16x32_bf16 a[208:211],v[216:219],v[232:235],a[208:211]\n"
"ds_read_b64_tr_b16 v[164:165],v99 offset:49152\n"
"v_mfma_f32_16x16x32_bf16 a[212:215],v[220:223],v[232:235],a[212:215]\n"
"ds_read_b64_tr_b16 v[166:167],v99 offset:50176\n"
"v_mfma_f32_16x16x32_bf16 a[220:223],v[220:223],v[236:239],a[220:223]\n"
"ds_read_b64_tr_b16 v[168:169],v100 offset:49152\n"
"v_mfma_f32_16x16x32_bf16 a[216:219],v[216:219],v[236:239],a[216:219]\n"
"ds_read_b64_tr_b16 v[170:171],v100 offset:50176\n"
"v_mfma_f32_16x16x32_bf16 a[224:227],v[216:219],v[240:243],a[224:227]\n"
"ds_read_b64_tr_b16 v[172:173],v101 offset:49152\n"
"v_mfma_f32_16x16x32_bf16 a[228:231],v[220:223],v[240:243],a[228:231]\n"
"ds_read_b64_tr_b16 v[174:175],v101 offset:50176\n"
"v_mfma_f32_16x16x32_bf16 a[236:239],v[220:223],v[244:247],a[236:239]\n"
"ds_read_b64_tr_b16 v[176:177],v102 offset:49152\n"
"v_mfma_f32_16x16x32_bf16 a[232:235],v[216:219],v[244:247],a[232:235]\n"
"ds_read_b64_tr_b16 v[178:179],v102 offset:50176\n"
"v_mfma_f32_16x16x32_bf16 a[240:243],v[216:219],v[248:251],a[240:243]\n"
"ds_read_b64_tr_b16 v[180:181],v103 offset:49152\n"
"v_mfma_f32_16x16x32_bf16 a[244:247],v[220:223],v[248:251],a[244:247]\n"
"ds_read_b64_tr_b16 v[182:183],v103 offset:50176\n"
"ds_read_b64_tr_b16 v[184:185],v104 offset:49152\n"
"v_mfma_f32_16x16x32_bf16 a[252:255],v[220:223],v[252:255],a[252:255]\n"
"v_mov_b32 v110,v72\n"
"v_mov_b32 v111,v73\n"
"v_mfma_f32_16x16x32_bf16 a[248:251],v[216:219],v[252:255],a[248:251]\n"
"ds_read_b64_tr_b16 v[186:187],v104 offset:50176\n"
"s_waitcnt lgkmcnt(10)\n"
"v_mfma_f32_16x16x32_bf16 a[192:195],v[152:155],v[160:163],a[192:195]\n"
"ds_read_b64_tr_b16 v[188:189],v105 offset:49152\n"
"v_mfma_f32_16x16x32_bf16 a[196:199],v[156:159],v[160:163],a[196:199]\n"
"ds_read_b64_tr_b16 v[190:191],v105 offset:50176\n"
"v_mfma_f32_16x16x32_bf16 a[204:207],v[156:159],v[164:167],a[204:207]\n"
"ds_read_b128 v[128:131],v80 offset:0\n"
"v_mfma_f32_16x16x32_bf16 a[200:203],v[152:155],v[164:167],a[200:203]\n"
"ds_read_b128 v[192:195],v81 offset:0\n"
"s_waitcnt lgkmcnt(10)\n"
"v_mfma_f32_16x16x32_bf16 a[208:211],v[152:155],v[168:171],a[208:211]\n"
"ds_read_b64_tr_b16 v[224:225],v98 offset:32768\n"
"v_mfma_f32_16x16x32_bf16 a[212:215],v[156:159],v[168:171],a[212:215]\n"
"ds_read_b64_tr_b16 v[226:227],v98 offset:33792\n"
"v_mfma_f32_16x16x32_bf16 a[220:223],v[156:159],v[172:175],a[220:223]\n"
"ds_read_b64_tr_b16 v[228:229],v99 offset:32768\n"
"v_mfma_f32_16x16x32_bf16 a[216:219],v[152:155],v[172:175],a[216:219]\n"
"ds_read_b64_tr_b16 v[230:231],v99 offset:33792\n"
"s_waitcnt lgkmcnt(10)\n"
"v_mfma_f32_16x16x32_bf16 a[224:227],v[152:155],v[176:179],a[224:227]\n"
"ds_read_b64_tr_b16 v[232:233],v100 offset:32768\n"
"v_mfma_f32_16x16x32_bf16 a[228:231],v[156:159],v[176:179],a[228:231]\n"
"ds_read_b64_tr_b16 v[234:235],v100 offset:33792\n"
"v_mfma_f32_16x16x32_bf16 a[236:239],v[156:159],v[180:183],a[236:239]\n"
"ds_read_b64_tr_b16 v[236:237],v101 offset:32768\n"
"v_mfma_f32_16x16x32_bf16 a[232:235],v[152:155],v[180:183],a[232:235]\n"
"ds_read_b64_tr_b16 v[238:239],v101 offset:33792\n"
"s_waitcnt lgkmcnt(10)\n"
"v_mfma_f32_16x16x32_bf16 a[240:243],v[152:155],v[184:187],a[240:243]\n"
"ds_read_b64_tr_b16 v[240:241],v102 offset:32768\n"
"v_mfma_f32_16x16x32_bf16 a[244:247],v[156:159],v[184:187],a[244:247]\n"
"ds_read_b64_tr_b16 v[242:243],v102 offset:33792\n"
"v_mfma_f32_16x16x32_bf16 a[252:255],v[156:159],v[188:191],a[252:255]\n"
"ds_read_b64_tr_b16 v[244:245],v103 offset:32768\n"
"v_mfma_f32_16x16x32_bf16 a[248:251],v[152:155],v[188:191],a[248:251]\n"
"ds_read_b64_tr_b16 v[246:247],v103 offset:33792\n"
"s_waitcnt lgkmcnt(8)\n"
"v_mfma_f32_16x16x32_bf16 a[0:3],v[128:131],v[160:163],a[0:3]\n"
"ds_read_b64_tr_b16 v[248:249],v104 offset:32768\n"
"v_mfma_f32_16x16x32_bf16 a[0:3],v[192:195],v[224:227],a[0:3]\n"
"ds_read_b64_tr_b16 v[250:251],v104 offset:33792\n"
"v_mfma_f32_16x16x32_bf16 a[8:11],v[192:195],v[228:231],a[8:11]\n"
"ds_read_b64_tr_b16 v[252:253],v105 offset:32768\n"
"v_mfma_f32_16x16x32_bf16 a[8:11],v[128:131],v[164:167],a[8:11]\n"
"ds_read_b64_tr_b16 v[254:255],v105 offset:33792\n"
"s_waitcnt lgkmcnt(8)\n"
"v_mfma_f32_16x16x32_bf16 a[16:19],v[128:131],v[168:171],a[16:19]\n"
"ds_read_b128 v[132:135],v80 offset:2048\n"
"v_mfma_f32_16x16x32_bf16 a[16:19],v[192:195],v[232:235],a[16:19]\n"
"ds_read_b128 v[196:199],v81 offset:2048\n"
"v_mfma_f32_16x16x32_bf16 a[24:27],v[192:195],v[236:239],a[24:27]\n"
"ds_read_b128 v[136:139],v80 offset:4096\n"
"v_mfma_f32_16x16x32_bf16 a[24:27],v[128:131],v[172:175],a[24:27]\n"
"ds_read_b128 v[200:203],v81 offset:4096\n"
"s_waitcnt lgkmcnt(8)\n"
"v_mfma_f32_16x16x32_bf16 a[32:35],v[128:131],v[176:179],a[32:35]\n"
"ds_read_b128 v[140:143],v80 offset:6144\n"
"v_mfma_f32_16x16x32_bf16 a[32:35],v[192:195],v[240:243],a[32:35]\n"
"ds_read_b128 v[204:207],v81 offset:6144\n"
"v_mfma_f32_16x16x32_bf16 a[40:43],v[192:195],v[244:247],a[40:43]\n"
"ds_read_b128 v[144:147],v80 offset:8192\n"
"v_mfma_f32_16x16x32_bf16 a[40:43],v[128:131],v[180:183],a[40:43]\n"
"ds_read_b128 v[208:211],v81 offset:8192\n"
"s_waitcnt lgkmcnt(8)\n"
"v_mfma_f32_16x16x32_bf16 a[48:51],v[128:131],v[184:187],a[48:51]\n"
"ds_read_b128 v[148:151],v80 offset:10240\n"
"v_mfma_f32_16x16x32_bf16 a[48:51],v[192:195],v[248:251],a[48:51]\n"
"ds_read_b128 v[212:215],v81 offset:10240\n"
"v_mfma_f32_16x16x32_bf16 a[56:59],v[192:195],v[252:255],a[56:59]\n"
"ds_read_b128 v[216:219],v81 offset:12288\n"
"v_mfma_f32_16x16x32_bf16 a[56:59],v[128:131],v[188:191],a[56:59]\n"
"ds_read_b128 v[220:223],v81 offset:14336\n"
"s_waitcnt lgkmcnt(10)\n"
"v_mfma_f32_16x16x32_bf16 a[4:7],v[132:135],v[160:163],a[4:7]\n"
"v_mfma_f32_16x16x32_bf16 a[4:7],v[196:199],v[224:227],a[4:7]\n"
"v_mfma_f32_16x16x32_bf16 a[12:15],v[196:199],v[228:231],a[12:15]\n"
"v_mfma_f32_16x16x32_bf16 a[12:15],v[132:135],v[164:167],a[12:15]\n"
"v_mfma_f32_16x16x32_bf16 a[20:23],v[132:135],v[168:171],a[20:23]\n"
"s_waitcnt lgkmcnt(0)\n"
"s_barrier\n"
"v_mfma_f32_16x16x32_bf16 a[20:23],v[196:199],v[232:235],a[20:23]\n"
"s_add_u32 m0,s85,147456\n"
"v_mfma_f32_16x16x32_bf16 a[28:31],v[196:199],v[236:239],a[28:31]\n"
"buffer_load_dwordx4 v110,s[28:31],s86 offen lds\n"
"v_mfma_f32_16x16x32_bf16 a[28:31],v[132:135],v[172:175],a[28:31]\n"
"v_mfma_f32_16x16x32_bf16 a[36:39],v[132:135],v[176:179],a[36:39]\n"
"s_load_dwordx2 s[48:49],s[0:1],56\n"
"v_mfma_f32_16x16x32_bf16 a[36:39],v[196:199],v[240:243],a[36:39]\n"
"s_add_u32 m0,s85,148480\n"
"v_mfma_f32_16x16x32_bf16 a[44:47],v[196:199],v[244:247],a[44:47]\n"
"buffer_load_dwordx4 v111,s[28:31],s86 offen lds\n"
"v_mfma_f32_16x16x32_bf16 a[44:47],v[132:135],v[180:183],a[44:47]\n"
"s_xor_b32 s86,s86,262144\n"
"v_mfma_f32_16x16x32_bf16 a[52:55],v[132:135],v[184:187],a[52:55]\n"
"v_mfma_f32_16x16x32_bf16 a[52:55],v[196:199],v[248:251],a[52:55]\n"
"s_add_u32 m0,s85,155648\n"
"v_mfma_f32_16x16x32_bf16 a[60:63],v[196:199],v[252:255],a[60:63]\n"
"buffer_load_dwordx4 v110,s[28:31],s86 offen lds\n"
"v_mfma_f32_16x16x32_bf16 a[60:63],v[132:135],v[188:191],a[60:63]\n"
"v_mfma_f32_16x16x32_bf16 a[64:67],v[136:139],v[160:163],a[64:67]\n"
"v_mfma_f32_16x16x32_bf16 a[64:67],v[200:203],v[224:227],a[64:67]\n"
"s_add_u32 m0,s85,156672\n"
"v_mfma_f32_16x16x32_bf16 a[72:75],v[200:203],v[228:231],a[72:75]\n"
"buffer_load_dwordx4 v111,s[28:31],s86 offen lds\n"
"v_mfma_f32_16x16x32_bf16 a[72:75],v[136:139],v[164:167],a[72:75]\n"
"s_xor_b32 s86,s86,786432\n"
"v_mfma_f32_16x16x32_bf16 a[80:83],v[136:139],v[168:171],a[80:83]\n"
"v_mfma_f32_16x16x32_bf16 a[80:83],v[200:203],v[232:235],a[80:83]\n"
"s_add_u32 m0,s85,131072\n"
"v_mfma_f32_16x16x32_bf16 a[88:91],v[200:203],v[236:239],a[88:91]\n"
"buffer_load_dwordx4 v110,s[28:31],s86 offen lds\n"
"v_mfma_f32_16x16x32_bf16 a[88:91],v[136:139],v[172:175],a[88:91]\n"
"v_mfma_f32_16x16x32_bf16 a[96:99],v[136:139],v[176:179],a[96:99]\n"
"v_mfma_f32_16x16x32_bf16 a[96:99],v[200:203],v[240:243],a[96:99]\n"
"s_add_u32 m0,s85,132096\n"
"v_mfma_f32_16x16x32_bf16 a[104:107],v[200:203],v[244:247],a[104:107]\n"
"buffer_load_dwordx4 v111,s[28:31],s86 offen lds\n"
"v_mfma_f32_16x16x32_bf16 a[104:107],v[136:139],v[180:183],a[104:107]\n"
"s_xor_b32 s86,s86,262144\n"
"v_mfma_f32_16x16x32_bf16 a[112:115],v[136:139],v[184:187],a[112:115]\n"
"v_mfma_f32_16x16x32_bf16 a[112:115],v[200:203],v[248:251],a[112:115]\n"
"v_mfma_f32_16x16x32_bf16 a[120:123],v[200:203],v[252:255],a[120:123]\n"
"v_mfma_f32_16x16x32_bf16 a[120:123],v[136:139],v[188:191],a[120:123]\n"
"v_mfma_f32_16x16x32_bf16 a[68:71],v[140:143],v[160:163],a[68:71]\n"
"v_mfma_f32_16x16x32_bf16 a[68:71],v[204:207],v[224:227],a[68:71]\n"
"s_add_u32 m0,s85,139264\n"
"v_mfma_f32_16x16x32_bf16 a[76:79],v[204:207],v[228:231],a[76:79]\n"
"buffer_load_dwordx4 v110,s[28:31],s86 offen lds\n"
"v_mfma_f32_16x16x32_bf16 a[76:79],v[140:143],v[164:167],a[76:79]\n"
"v_mfma_f32_16x16x32_bf16 a[84:87],v[140:143],v[168:171],a[84:87]\n"
"v_mfma_f32_16x16x32_bf16 a[84:87],v[204:207],v[232:235],a[84:87]\n"
"s_add_u32 m0,s85,140288\n"
"v_mfma_f32_16x16x32_bf16 a[92:95],v[204:207],v[236:239],a[92:95]\n"
"buffer_load_dwordx4 v111,s[28:31],s86 offen lds\n"
"v_mfma_f32_16x16x32_bf16 a[92:95],v[140:143],v[172:175],a[92:95]\n"
"s_xor_b32 s86,s86,1835008\n"
"v_mfma_f32_16x16x32_bf16 a[100:103],v[140:143],v[176:179],a[100:103]\n"
"s_waitcnt lgkmcnt(0)\n"
"s_lshl2_add_u32 s88,s100,s52\n"
"v_mfma_f32_16x16x32_bf16 a[100:103],v[204:207],v[240:243],a[100:103]\n"
"s_mul_i32 s88,s88,7168\n"
"s_add_u32 m0,s78,0\n"
"v_mfma_f32_16x16x32_bf16 a[108:111],v[204:207],v[244:247],a[108:111]\n"
"buffer_load_dwordx4 v112,s[24:27],s76 offen offset:0 sc1 lds\n"
"v_mfma_f32_16x16x32_bf16 a[108:111],v[140:143],v[180:183],a[108:111]\n"
"s_add_u32 s88,s88,3584\n"
"s_add_u32 s48,s48,s88\n"
"s_addc_u32 s49,s49,0\n"
"v_mfma_f32_16x16x32_bf16 a[116:119],v[140:143],v[184:187],a[116:119]\n"
"s_mov_b32 s50,3584\n"
"s_mov_b32 s51,0x20000\n"
"v_mfma_f32_16x16x32_bf16 a[116:119],v[204:207],v[248:251],a[116:119]\n"
"v_lshlrev_b32 v8,3,v112\n"
"v_mfma_f32_16x16x32_bf16 a[124:127],v[204:207],v[252:255],a[124:127]\n"
"buffer_load_dwordx4 v112,s[24:27],s76 offen offset:1024 sc1 lds\n"
"v_mfma_f32_16x16x32_bf16 a[124:127],v[140:143],v[188:191],a[124:127]\n"
"v_mfma_f32_16x16x32_bf16 a[128:131],v[144:147],v[160:163],a[128:131]\n"
"v_mfma_f32_16x16x32_bf16 a[128:131],v[208:211],v[224:227],a[128:131]\n"
"v_mfma_f32_16x16x32_bf16 a[136:139],v[208:211],v[228:231],a[136:139]\n"
"buffer_load_dwordx4 v112,s[24:27],s76 offen offset:2048 sc1 lds\n"
"v_mfma_f32_16x16x32_bf16 a[136:139],v[144:147],v[164:167],a[136:139]\n"
"v_mfma_f32_16x16x32_bf16 a[144:147],v[144:147],v[168:171],a[144:147]\n"
"v_mfma_f32_16x16x32_bf16 a[144:147],v[208:211],v[232:235],a[144:147]\n"
"buffer_load_dwordx4 v112,s[24:27],s76 offen offset:3072 sc1 lds\n"
"v_mfma_f32_16x16x32_bf16 a[152:155],v[208:211],v[236:239],a[152:155]\n"
"s_xor_b32 s76,s76,16384\n"
"v_mfma_f32_16x16x32_bf16 a[152:155],v[144:147],v[172:175],a[152:155]\n"
"v_mfma_f32_16x16x32_bf16 a[160:163],v[144:147],v[176:179],a[160:163]\n"
"v_mfma_f32_16x16x32_bf16 a[160:163],v[208:211],v[240:243],a[160:163]\n"
"s_add_u32 m0,s78,16384\n"
"v_mfma_f32_16x16x32_bf16 a[168:171],v[208:211],v[244:247],a[168:171]\n"
"buffer_load_dwordx4 v112,s[24:27],s76 offen offset:0 sc1 lds\n"
"v_mfma_f32_16x16x32_bf16 a[168:171],v[144:147],v[180:183],a[168:171]\n"
"v_mfma_f32_16x16x32_bf16 a[176:179],v[144:147],v[184:187],a[176:179]\n"
"v_mfma_f32_16x16x32_bf16 a[176:179],v[208:211],v[248:251],a[176:179]\n"
"v_mfma_f32_16x16x32_bf16 a[184:187],v[208:211],v[252:255],a[184:187]\n"
"buffer_load_dwordx4 v112,s[24:27],s76 offen offset:1024 sc1 lds\n"
"v_mfma_f32_16x16x32_bf16 a[184:187],v[144:147],v[188:191],a[184:187]\n"
"v_mfma_f32_16x16x32_bf16 a[132:135],v[148:151],v[160:163],a[132:135]\n"
"v_mfma_f32_16x16x32_bf16 a[132:135],v[212:215],v[224:227],a[132:135]\n"
"v_mfma_f32_16x16x32_bf16 a[140:143],v[212:215],v[228:231],a[140:143]\n"
"buffer_load_dwordx4 v112,s[24:27],s76 offen offset:2048 sc1 lds\n"
"v_mfma_f32_16x16x32_bf16 a[140:143],v[148:151],v[164:167],a[140:143]\n"
"v_mfma_f32_16x16x32_bf16 a[148:151],v[148:151],v[168:171],a[148:151]\n"
"v_mfma_f32_16x16x32_bf16 a[148:151],v[212:215],v[232:235],a[148:151]\n"
"buffer_load_dwordx4 v112,s[24:27],s76 offen offset:3072 sc1 lds\n"
"v_mfma_f32_16x16x32_bf16 a[156:159],v[212:215],v[236:239],a[156:159]\n"
"s_xor_b32 s76,s76,114688\n"
"v_mfma_f32_16x16x32_bf16 a[156:159],v[148:151],v[172:175],a[156:159]\n"
"v_mfma_f32_16x16x32_bf16 a[164:167],v[148:151],v[176:179],a[164:167]\n"
"buffer_load_dword v115,v8,s[48:51],0 offen\n"
"v_mfma_f32_16x16x32_bf16 a[164:167],v[212:215],v[240:243],a[164:167]\n"
"v_mfma_f32_16x16x32_bf16 a[172:175],v[212:215],v[244:247],a[172:175]\n"
"v_mfma_f32_16x16x32_bf16 a[172:175],v[148:151],v[180:183],a[172:175]\n"
"v_mfma_f32_16x16x32_bf16 a[180:183],v[148:151],v[184:187],a[180:183]\n"
"v_mfma_f32_16x16x32_bf16 a[180:183],v[212:215],v[248:251],a[180:183]\n"
"v_mfma_f32_16x16x32_bf16 a[188:191],v[212:215],v[252:255],a[188:191]\n"
"v_mfma_f32_16x16x32_bf16 a[188:191],v[148:151],v[188:191],a[188:191]\n"
"s_waitcnt vmcnt(26)\n"
"s_barrier\n"
"v_mfma_f32_16x16x32_bf16 a[192:195],v[216:219],v[224:227],a[192:195]\n"
"ds_read_b128 v[152:155],v80 offset:45056\n"
"v_mfma_f32_16x16x32_bf16 a[196:199],v[220:223],v[224:227],a[196:199]\n"
"ds_read_b128 v[156:159],v80 offset:47104\n"
"v_mfma_f32_16x16x32_bf16 a[204:207],v[220:223],v[228:231],a[204:207]\n"
"ds_read_b64_tr_b16 v[160:161],v98 offset:0\n"
"v_mfma_f32_16x16x32_bf16 a[200:203],v[216:219],v[228:231],a[200:203]\n"
"ds_read_b64_tr_b16 v[162:163],v98 offset:1024\n"
"v_mfma_f32_16x16x32_bf16 a[208:211],v[216:219],v[232:235],a[208:211]\n"
"ds_read_b64_tr_b16 v[164:165],v99 offset:0\n"
"v_mfma_f32_16x16x32_bf16 a[212:215],v[220:223],v[232:235],a[212:215]\n"
"ds_read_b64_tr_b16 v[166:167],v99 offset:1024\n"
"v_mfma_f32_16x16x32_bf16 a[220:223],v[220:223],v[236:239],a[220:223]\n"
"ds_read_b64_tr_b16 v[168:169],v100 offset:0\n"
"v_mfma_f32_16x16x32_bf16 a[216:219],v[216:219],v[236:239],a[216:219]\n"
"ds_read_b64_tr_b16 v[170:171],v100 offset:1024\n"
"v_mfma_f32_16x16x32_bf16 a[224:227],v[216:219],v[240:243],a[224:227]\n"
"ds_read_b64_tr_b16 v[172:173],v101 offset:0\n"
"v_mfma_f32_16x16x32_bf16 a[228:231],v[220:223],v[240:243],a[228:231]\n"
"ds_read_b64_tr_b16 v[174:175],v101 offset:1024\n"
"v_mfma_f32_16x16x32_bf16 a[236:239],v[220:223],v[244:247],a[236:239]\n"
"ds_read_b64_tr_b16 v[176:177],v102 offset:0\n"
"v_mfma_f32_16x16x32_bf16 a[232:235],v[216:219],v[244:247],a[232:235]\n"
"ds_read_b64_tr_b16 v[178:179],v102 offset:1024\n"
"v_mfma_f32_16x16x32_bf16 a[240:243],v[216:219],v[248:251],a[240:243]\n"
"ds_read_b64_tr_b16 v[180:181],v103 offset:0\n"
"v_mfma_f32_16x16x32_bf16 a[244:247],v[220:223],v[248:251],a[244:247]\n"
"ds_read_b64_tr_b16 v[182:183],v103 offset:1024\n"
"v_mfma_f32_16x16x32_bf16 a[252:255],v[220:223],v[252:255],a[252:255]\n"
"ds_read_b64_tr_b16 v[184:185],v104 offset:0\n"
"v_mfma_f32_16x16x32_bf16 a[248:251],v[216:219],v[252:255],a[248:251]\n"
"ds_read_b64_tr_b16 v[186:187],v104 offset:1024\n"
"s_waitcnt lgkmcnt(10)\n"
"v_mfma_f32_16x16x32_bf16 a[192:195],v[152:155],v[160:163],a[192:195]\n"
"ds_read_b64_tr_b16 v[188:189],v105 offset:0\n"
"v_mfma_f32_16x16x32_bf16 a[196:199],v[156:159],v[160:163],a[196:199]\n"
"ds_read_b64_tr_b16 v[190:191],v105 offset:1024\n"
"v_mfma_f32_16x16x32_bf16 a[204:207],v[156:159],v[164:167],a[204:207]\n"
"ds_read_b128 v[128:131],v80 offset:32768\n"
"v_mfma_f32_16x16x32_bf16 a[200:203],v[152:155],v[164:167],a[200:203]\n"
"ds_read_b128 v[192:195],v81 offset:32768\n"
"s_waitcnt lgkmcnt(10)\n"
"v_mfma_f32_16x16x32_bf16 a[208:211],v[152:155],v[168:171],a[208:211]\n"
"ds_read_b64_tr_b16 v[224:225],v98 offset:16384\n"
"v_mfma_f32_16x16x32_bf16 a[212:215],v[156:159],v[168:171],a[212:215]\n"
"ds_read_b64_tr_b16 v[226:227],v98 offset:17408\n"
"v_mfma_f32_16x16x32_bf16 a[220:223],v[156:159],v[172:175],a[220:223]\n"
"ds_read_b64_tr_b16 v[228:229],v99 offset:16384\n"
"v_mfma_f32_16x16x32_bf16 a[216:219],v[152:155],v[172:175],a[216:219]\n"
"ds_read_b64_tr_b16 v[230:231],v99 offset:17408\n"
"s_waitcnt lgkmcnt(10)\n"
"v_mfma_f32_16x16x32_bf16 a[224:227],v[152:155],v[176:179],a[224:227]\n"
"ds_read_b64_tr_b16 v[232:233],v100 offset:16384\n"
"v_mfma_f32_16x16x32_bf16 a[228:231],v[156:159],v[176:179],a[228:231]\n"
"ds_read_b64_tr_b16 v[234:235],v100 offset:17408\n"
"v_mfma_f32_16x16x32_bf16 a[236:239],v[156:159],v[180:183],a[236:239]\n"
"ds_read_b64_tr_b16 v[236:237],v101 offset:16384\n"
"v_mfma_f32_16x16x32_bf16 a[232:235],v[152:155],v[180:183],a[232:235]\n"
"ds_read_b64_tr_b16 v[238:239],v101 offset:17408\n"
"s_waitcnt lgkmcnt(10)\n"
"v_mfma_f32_16x16x32_bf16 a[240:243],v[152:155],v[184:187],a[240:243]\n"
"ds_read_b64_tr_b16 v[240:241],v102 offset:16384\n"
"v_mfma_f32_16x16x32_bf16 a[244:247],v[156:159],v[184:187],a[244:247]\n"
"ds_read_b64_tr_b16 v[242:243],v102 offset:17408\n"
"v_mfma_f32_16x16x32_bf16 a[252:255],v[156:159],v[188:191],a[252:255]\n"
"ds_read_b64_tr_b16 v[244:245],v103 offset:16384\n"
"v_mfma_f32_16x16x32_bf16 a[248:251],v[152:155],v[188:191],a[248:251]\n"
"ds_read_b64_tr_b16 v[246:247],v103 offset:17408\n"
"s_waitcnt lgkmcnt(8)\n"
"v_mfma_f32_16x16x32_bf16 a[0:3],v[128:131],v[160:163],a[0:3]\n"
"ds_read_b64_tr_b16 v[248:249],v104 offset:16384\n"
"v_mfma_f32_16x16x32_bf16 a[0:3],v[192:195],v[224:227],a[0:3]\n"
"ds_read_b64_tr_b16 v[250:251],v104 offset:17408\n"
"v_mfma_f32_16x16x32_bf16 a[8:11],v[192:195],v[228:231],a[8:11]\n"
"ds_read_b64_tr_b16 v[252:253],v105 offset:16384\n"
"v_mfma_f32_16x16x32_bf16 a[8:11],v[128:131],v[164:167],a[8:11]\n"
"ds_read_b64_tr_b16 v[254:255],v105 offset:17408\n"
"s_waitcnt lgkmcnt(8)\n"
"v_mfma_f32_16x16x32_bf16 a[16:19],v[128:131],v[168:171],a[16:19]\n"
"ds_read_b128 v[132:135],v80 offset:34816\n"
"v_mfma_f32_16x16x32_bf16 a[16:19],v[192:195],v[232:235],a[16:19]\n"
"ds_read_b128 v[196:199],v81 offset:34816\n"
"v_mfma_f32_16x16x32_bf16 a[24:27],v[192:195],v[236:239],a[24:27]\n"
"ds_read_b128 v[136:139],v80 offset:36864\n"
"v_mfma_f32_16x16x32_bf16 a[24:27],v[128:131],v[172:175],a[24:27]\n"
"ds_read_b128 v[200:203],v81 offset:36864\n"
"s_waitcnt lgkmcnt(8)\n"
"v_mfma_f32_16x16x32_bf16 a[32:35],v[128:131],v[176:179],a[32:35]\n"
"s_and_b32 s82,s52,1\n"
"ds_read_b128 v[140:143],v80 offset:38912\n"
"s_lshl_b32 s82,s82,13\n"
"v_mfma_f32_16x16x32_bf16 a[32:35],v[192:195],v[240:243],a[32:35]\n"
"ds_read_b128 v[204:207],v81 offset:38912\n"
"v_mfma_f32_16x16x32_bf16 a[40:43],v[192:195],v[244:247],a[40:43]\n"
"ds_read_b128 v[144:147],v80 offset:40960\n"
"v_mfma_f32_16x16x32_bf16 a[40:43],v[128:131],v[180:183],a[40:43]\n"
"ds_read_b128 v[208:211],v81 offset:40960\n"
"s_waitcnt lgkmcnt(8)\n"
"v_mfma_f32_16x16x32_bf16 a[48:51],v[128:131],v[184:187],a[48:51]\n"
"ds_read_b128 v[148:151],v80 offset:43008\n"
"v_mfma_f32_16x16x32_bf16 a[48:51],v[192:195],v[248:251],a[48:51]\n"
"ds_read_b128 v[212:215],v81 offset:43008\n"
"v_subrev_u32 v98,s82,v98\n"
"v_mfma_f32_16x16x32_bf16 a[56:59],v[192:195],v[252:255],a[56:59]\n"
"ds_read_b128 v[216:219],v81 offset:45056\n"
"v_add_u32 v96,s78,v97\n"
"v_mfma_f32_16x16x32_bf16 a[56:59],v[128:131],v[188:191],a[56:59]\n"
"ds_read_b128 v[220:223],v81 offset:47104\n"
"s_waitcnt lgkmcnt(10)\n"
"v_mfma_f32_16x16x32_bf16 a[4:7],v[132:135],v[160:163],a[4:7]\n"
"v_add_u32 v97,65536,v96\n"
"v_mfma_f32_16x16x32_bf16 a[4:7],v[196:199],v[224:227],a[4:7]\n"
"v_subrev_u32 v99,s82,v99\n"
"v_mfma_f32_16x16x32_bf16 a[12:15],v[196:199],v[228:231],a[12:15]\n"
"v_mfma_f32_16x16x32_bf16 a[12:15],v[132:135],v[164:167],a[12:15]\n"
"v_mfma_f32_16x16x32_bf16 a[20:23],v[132:135],v[168:171],a[20:23]\n"
"s_waitcnt lgkmcnt(0)\n"
"s_barrier\n"
"v_mfma_f32_16x16x32_bf16 a[20:23],v[196:199],v[232:235],a[20:23]\n"
"v_subrev_u32 v100,s82,v100\n"
"s_add_u32 m0,s85,98304\n"
"v_mfma_f32_16x16x32_bf16 a[28:31],v[196:199],v[236:239],a[28:31]\n"
"buffer_load_dwordx4 v110,s[28:31],s86 offen lds\n"
"v_mfma_f32_16x16x32_bf16 a[28:31],v[132:135],v[172:175],a[28:31]\n"
"v_mfma_f32_16x16x32_bf16 a[36:39],v[132:135],v[176:179],a[36:39]\n"
"v_mfma_f32_16x16x32_bf16 a[36:39],v[196:199],v[240:243],a[36:39]\n"
"v_subrev_u32 v101,s82,v101\n"
"s_add_u32 m0,s85,99328\n"
"v_mfma_f32_16x16x32_bf16 a[44:47],v[196:199],v[244:247],a[44:47]\n"
"buffer_load_dwordx4 v111,s[28:31],s86 offen lds\n"
"v_mfma_f32_16x16x32_bf16 a[44:47],v[132:135],v[180:183],a[44:47]\n"
"s_xor_b32 s86,s86,262144\n"
"v_mfma_f32_16x16x32_bf16 a[52:55],v[132:135],v[184:187],a[52:55]\n"
"v_mfma_f32_16x16x32_bf16 a[52:55],v[196:199],v[248:251],a[52:55]\n"
"v_subrev_u32 v102,s82,v102\n"
"s_add_u32 m0,s78,32768\n"
"v_mfma_f32_16x16x32_bf16 a[60:63],v[196:199],v[252:255],a[60:63]\n"
"buffer_load_dwordx4 v112,s[24:27],s76 offen offset:0 sc1 lds\n"
"v_mfma_f32_16x16x32_bf16 a[60:63],v[132:135],v[188:191],a[60:63]\n"
"v_mfma_f32_16x16x32_bf16 a[64:67],v[136:139],v[160:163],a[64:67]\n"
"v_mfma_f32_16x16x32_bf16 a[64:67],v[200:203],v[224:227],a[64:67]\n"
"v_subrev_u32 v103,s82,v103\n"
"v_mfma_f32_16x16x32_bf16 a[72:75],v[200:203],v[228:231],a[72:75]\n"
"buffer_load_dwordx4 v112,s[24:27],s76 offen offset:1024 sc1 lds\n"
"v_mfma_f32_16x16x32_bf16 a[72:75],v[136:139],v[164:167],a[72:75]\n"
"v_mfma_f32_16x16x32_bf16 a[80:83],v[136:139],v[168:171],a[80:83]\n"
"v_accvgpr_read_b32 v116,a0\n"
"v_accvgpr_read_b32 v117,a1\n"
"v_mfma_f32_16x16x32_bf16 a[80:83],v[200:203],v[232:235],a[80:83]\n"
"v_subrev_u32 v104,s82,v104\n"
"v_accvgpr_read_b32 v118,a2\n"
"v_mfma_f32_16x16x32_bf16 a[88:91],v[200:203],v[236:239],a[88:91]\n"
"buffer_load_dwordx4 v112,s[24:27],s76 offen offset:2048 sc1 lds\n"
"v_mfma_f32_16x16x32_bf16 a[88:91],v[136:139],v[172:175],a[88:91]\n"
"v_accvgpr_read_b32 v119,a3\n"
"v_accvgpr_read_b32 v120,a4\n"
"v_mfma_f32_16x16x32_bf16 a[96:99],v[136:139],v[176:179],a[96:99]\n"
"v_accvgpr_read_b32 v121,a5\n"
"v_accvgpr_read_b32 v122,a6\n"
"v_mfma_f32_16x16x32_bf16 a[96:99],v[200:203],v[240:243],a[96:99]\n"
"v_subrev_u32 v105,s82,v105\n"
"v_accvgpr_read_b32 v123,a7\n"
"v_mfma_f32_16x16x32_bf16 a[104:107],v[200:203],v[244:247],a[104:107]\n"
"buffer_load_dwordx4 v112,s[24:27],s76 offen offset:3072 sc1 lds\n"
"v_mfma_f32_16x16x32_bf16 a[104:107],v[136:139],v[180:183],a[104:107]\n"
"s_xor_b32 s76,s76,16384\n"
"v_cvt_pk_bf16_f32 v116,v116,v117\n"
"v_cvt_pk_bf16_f32 v117,v118,v119\n"
"v_mfma_f32_16x16x32_bf16 a[112:115],v[136:139],v[184:187],a[112:115]\n"
"v_cvt_pk_bf16_f32 v118,v120,v121\n"
"v_cvt_pk_bf16_f32 v119,v122,v123\n"
"v_mfma_f32_16x16x32_bf16 a[112:115],v[200:203],v[248:251],a[112:115]\n"
"buffer_store_dwordx4 v[116:119],v112,s[24:27],s56 offen offset:0 sc1\n"
"v_mfma_f32_16x16x32_bf16 a[120:123],v[200:203],v[252:255],a[120:123]\n"
"s_nop 0\n"
"v_accvgpr_read_b32 v116,a8\n"
"v_accvgpr_read_b32 v117,a9\n"
"v_accvgpr_read_b32 v118,a10\n"
"v_mfma_f32_16x16x32_bf16 a[120:123],v[136:139],v[188:191],a[120:123]\n"
"s_waitcnt vmcnt(20)\n"
"v_accvgpr_read_b32 v119,a11\n"
"v_accvgpr_read_b32 v120,a12\n"
"s_barrier\n"
"v_mfma_f32_16x16x32_bf16 a[68:71],v[140:143],v[160:163],a[68:71]\n"
"ds_read_b128 v[0:3],v97 offset:0\n"
"v_accvgpr_read_b32 v121,a13\n"
"v_mfma_f32_16x16x32_bf16 a[68:71],v[204:207],v[224:227],a[68:71]\n"
"ds_read_b128 v[8:11],v97 offset:1024\n"
"v_accvgpr_read_b32 v122,a14\n"
"v_mfma_f32_16x16x32_bf16 a[76:79],v[204:207],v[228:231],a[76:79]\n"
"ds_read_b128 v[16:19],v97 offset:2048\n"
"v_accvgpr_read_b32 v123,a15\n"
"v_mfma_f32_16x16x32_bf16 a[76:79],v[140:143],v[164:167],a[76:79]\n"
"ds_read_b128 v[24:27],v97 offset:3072\n"
"v_cvt_pk_bf16_f32 v116,v116,v117\n"
"v_mfma_f32_16x16x32_bf16 a[84:87],v[140:143],v[168:171],a[84:87]\n"
"ds_read_b128 v[4:7],v97 offset:16384\n"
"v_cvt_pk_bf16_f32 v117,v118,v119\n"
"v_mfma_f32_16x16x32_bf16 a[84:87],v[204:207],v[232:235],a[84:87]\n"
"ds_read_b128 v[12:15],v97 offset:17408\n"
"v_cvt_pk_bf16_f32 v118,v120,v121\n"
"v_mfma_f32_16x16x32_bf16 a[92:95],v[204:207],v[236:239],a[92:95]\n"
"ds_read_b128 v[20:23],v97 offset:18432\n"
"v_cvt_pk_bf16_f32 v119,v122,v123\n"
"v_mfma_f32_16x16x32_bf16 a[92:95],v[140:143],v[172:175],a[92:95]\n"
"ds_read_b128 v[28:31],v97 offset:19456\n"
"buffer_store_dwordx4 v[116:119],v112,s[24:27],s56 offen offset:1024 sc1\n"
"v_mfma_f32_16x16x32_bf16 a[100:103],v[140:143],v[176:179],a[100:103]\n"
"ds_read_b64_tr_b16 v[32:33],v98 offset:49152\n"
"v_accvgpr_read_b32 v116,a16\n"
"v_mfma_f32_16x16x32_bf16 a[100:103],v[204:207],v[240:243],a[100:103]\n"
"ds_read_b64_tr_b16 v[34:35],v98 offset:50176\n"
"v_accvgpr_read_b32 v117,a17\n"
"v_mfma_f32_16x16x32_bf16 a[108:111],v[204:207],v[244:247],a[108:111]\n"
"ds_read_b64_tr_b16 v[36:37],v98 offset:57344\n"
"v_accvgpr_read_b32 v118,a18\n"
"v_mfma_f32_16x16x32_bf16 a[108:111],v[140:143],v[180:183],a[108:111]\n"
"ds_read_b64_tr_b16 v[38:39],v98 offset:58368\n"
"v_accvgpr_read_b32 v119,a19\n"
"v_mfma_f32_16x16x32_bf16 a[116:119],v[140:143],v[184:187],a[116:119]\n"
"ds_read_b64_tr_b16 v[40:41],v99 offset:49152\n"
"v_accvgpr_read_b32 v120,a20\n"
"v_mfma_f32_16x16x32_bf16 a[116:119],v[204:207],v[248:251],a[116:119]\n"
"ds_read_b64_tr_b16 v[42:43],v99 offset:50176\n"
"v_accvgpr_read_b32 v121,a21\n"
"v_mfma_f32_16x16x32_bf16 a[124:127],v[204:207],v[252:255],a[124:127]\n"
"ds_read_b64_tr_b16 v[44:45],v99 offset:57344\n"
"v_accvgpr_read_b32 v122,a22\n"
"v_mfma_f32_16x16x32_bf16 a[124:127],v[140:143],v[188:191],a[124:127]\n"
"ds_read_b64_tr_b16 v[46:47],v99 offset:58368\n"
"v_accvgpr_read_b32 v123,a23\n"
"v_mfma_f32_16x16x32_bf16 a[128:131],v[144:147],v[160:163],a[128:131]\n"
"ds_read_b64_tr_b16 v[48:49],v100 offset:49152\n"
"v_cvt_pk_bf16_f32 v116,v116,v117\n"
"v_mfma_f32_16x16x32_bf16 a[128:131],v[208:211],v[224:227],a[128:131]\n"
"ds_read_b64_tr_b16 v[50:51],v100 offset:50176\n"
"v_cvt_pk_bf16_f32 v117,v118,v119\n"
"v_mfma_f32_16x16x32_bf16 a[136:139],v[208:211],v[228:231],a[136:139]\n"
"ds_read_b64_tr_b16 v[52:53],v100 offset:57344\n"
"v_cvt_pk_bf16_f32 v118,v120,v121\n"
"v_mfma_f32_16x16x32_bf16 a[136:139],v[144:147],v[164:167],a[136:139]\n"
"ds_read_b64_tr_b16 v[54:55],v100 offset:58368\n"
"v_cvt_pk_bf16_f32 v119,v122,v123\n"
"v_mfma_f32_16x16x32_bf16 a[144:147],v[144:147],v[168:171],a[144:147]\n"
"ds_read_b64_tr_b16 v[56:57],v101 offset:49152\n"
"buffer_store_dwordx4 v[116:119],v112,s[24:27],s56 offen offset:2048 sc1\n"
"v_mfma_f32_16x16x32_bf16 a[144:147],v[208:211],v[232:235],a[144:147]\n"
"ds_read_b64_tr_b16 v[58:59],v101 offset:50176\n"
"v_accvgpr_read_b32 v116,a24\n"
"v_mfma_f32_16x16x32_bf16 a[152:155],v[208:211],v[236:239],a[152:155]\n"
"ds_read_b64_tr_b16 v[60:61],v101 offset:57344\n"
"v_accvgpr_read_b32 v117,a25\n"
"v_mfma_f32_16x16x32_bf16 a[152:155],v[144:147],v[172:175],a[152:155]\n"
"ds_read_b64_tr_b16 v[62:63],v101 offset:58368\n"
"v_accvgpr_read_b32 v118,a26\n"
"v_mfma_f32_16x16x32_bf16 a[160:163],v[144:147],v[176:179],a[160:163]\n"
"ds_read_b64_tr_b16 v[64:65],v102 offset:49152\n"
"v_accvgpr_read_b32 v119,a27\n"
"v_mfma_f32_16x16x32_bf16 a[160:163],v[208:211],v[240:243],a[160:163]\n"
"ds_read_b64_tr_b16 v[66:67],v102 offset:50176\n"
"v_accvgpr_read_b32 v120,a28\n"
"v_mfma_f32_16x16x32_bf16 a[168:171],v[208:211],v[244:247],a[168:171]\n"
"ds_read_b64_tr_b16 v[68:69],v102 offset:57344\n"
"v_accvgpr_read_b32 v121,a29\n"
"v_mfma_f32_16x16x32_bf16 a[168:171],v[144:147],v[180:183],a[168:171]\n"
"ds_read_b64_tr_b16 v[70:71],v102 offset:58368\n"
"v_accvgpr_read_b32 v122,a30\n"
"v_mfma_f32_16x16x32_bf16 a[176:179],v[144:147],v[184:187],a[176:179]\n"
"ds_read_b64_tr_b16 v[72:73],v103 offset:49152\n"
"v_accvgpr_read_b32 v123,a31\n"
"v_mfma_f32_16x16x32_bf16 a[176:179],v[208:211],v[248:251],a[176:179]\n"
"ds_read_b64_tr_b16 v[74:75],v103 offset:50176\n"
"v_cvt_pk_bf16_f32 v116,v116,v117\n"
"v_mfma_f32_16x16x32_bf16 a[184:187],v[208:211],v[252:255],a[184:187]\n"
"ds_read_b64_tr_b16 v[76:77],v103 offset:57344\n"
"v_cvt_pk_bf16_f32 v117,v118,v119\n"
"v_mfma_f32_16x16x32_bf16 a[184:187],v[144:147],v[188:191],a[184:187]\n"
"ds_read_b64_tr_b16 v[78:79],v103 offset:58368\n"
"v_cvt_pk_bf16_f32 v118,v120,v121\n"
"v_mfma_f32_16x16x32_bf16 a[132:135],v[148:151],v[160:163],a[132:135]\n"
"ds_read_b64_tr_b16 v[80:81],v104 offset:49152\n"
"v_cvt_pk_bf16_f32 v119,v122,v123\n"
"v_mfma_f32_16x16x32_bf16 a[132:135],v[212:215],v[224:227],a[132:135]\n"
"ds_read_b64_tr_b16 v[82:83],v104 offset:50176\n"
"buffer_store_dwordx4 v[116:119],v112,s[24:27],s56 offen offset:3072 sc1\n"
"v_mfma_f32_16x16x32_bf16 a[140:143],v[212:215],v[228:231],a[140:143]\n"
"ds_read_b64_tr_b16 v[84:85],v104 offset:57344\n"
"v_mfma_f32_16x16x32_bf16 a[140:143],v[148:151],v[164:167],a[140:143]\n"
"ds_read_b64_tr_b16 v[86:87],v104 offset:58368\n"
"v_mfma_f32_16x16x32_bf16 a[148:151],v[148:151],v[168:171],a[148:151]\n"
"ds_read_b64_tr_b16 v[88:89],v105 offset:49152\n"
"v_mfma_f32_16x16x32_bf16 a[148:151],v[212:215],v[232:235],a[148:151]\n"
"ds_read_b64_tr_b16 v[90:91],v105 offset:50176\n"
"v_mfma_f32_16x16x32_bf16 a[156:159],v[212:215],v[236:239],a[156:159]\n"
"ds_read_b64_tr_b16 v[92:93],v105 offset:57344\n"
"v_mfma_f32_16x16x32_bf16 a[156:159],v[148:151],v[172:175],a[156:159]\n"
"ds_read_b64_tr_b16 v[94:95],v105 offset:58368\n"
"v_mfma_f32_16x16x32_bf16 a[164:167],v[148:151],v[176:179],a[164:167]\n"
"s_add_u32 m0,s85,106496\n"
"v_mfma_f32_16x16x32_bf16 a[164:167],v[212:215],v[240:243],a[164:167]\n"
"buffer_load_dwordx4 v110,s[28:31],s86 offen lds\n"
"v_mfma_f32_16x16x32_bf16 a[172:175],v[212:215],v[244:247],a[172:175]\n"
"v_mfma_f32_16x16x32_bf16 a[172:175],v[148:151],v[180:183],a[172:175]\n"
"s_add_u32 m0,s85,107520\n"
"v_mfma_f32_16x16x32_bf16 a[180:183],v[148:151],v[184:187],a[180:183]\n"
"buffer_load_dwordx4 v111,s[28:31],s86 offen lds\n"
"v_mfma_f32_16x16x32_bf16 a[180:183],v[212:215],v[248:251],a[180:183]\n"
"s_xor_b32 s86,s86,786432\n"
"v_mfma_f32_16x16x32_bf16 a[188:191],v[212:215],v[252:255],a[188:191]\n"
"s_add_u32 m0,s78,49152\n"
"v_mfma_f32_16x16x32_bf16 a[188:191],v[148:151],v[188:191],a[188:191]\n"
"buffer_load_dwordx4 v112,s[24:27],s76 offen offset:0 sc1 lds\n"
"v_mfma_f32_16x16x32_bf16 a[192:195],v[216:219],v[224:227],a[192:195]\n"
"v_mfma_f32_16x16x32_bf16 a[196:199],v[220:223],v[224:227],a[196:199]\n"
"v_mfma_f32_16x16x32_bf16 a[204:207],v[220:223],v[228:231],a[204:207]\n"
"buffer_load_dwordx4 v112,s[24:27],s76 offen offset:1024 sc1 lds\n"
"v_mfma_f32_16x16x32_bf16 a[200:203],v[216:219],v[228:231],a[200:203]\n"
"v_mfma_f32_16x16x32_bf16 a[208:211],v[216:219],v[232:235],a[208:211]\n"
"v_mfma_f32_16x16x32_bf16 a[212:215],v[220:223],v[232:235],a[212:215]\n"
"buffer_load_dwordx4 v112,s[24:27],s76 offen offset:2048 sc1 lds\n"
"v_mfma_f32_16x16x32_bf16 a[220:223],v[220:223],v[236:239],a[220:223]\n"
"v_mfma_f32_16x16x32_bf16 a[216:219],v[216:219],v[236:239],a[216:219]\n"
"buffer_load_dwordx4 v112,s[24:27],s76 offen offset:3072 sc1 lds\n"
"v_mfma_f32_16x16x32_bf16 a[224:227],v[216:219],v[240:243],a[224:227]\n"
"s_xor_b32 s76,s76,49152\n"
"v_mfma_f32_16x16x32_bf16 a[228:231],v[220:223],v[240:243],a[228:231]\n"
"v_mfma_f32_16x16x32_bf16 a[236:239],v[220:223],v[244:247],a[236:239]\n"
"v_mfma_f32_16x16x32_bf16 a[232:235],v[216:219],v[244:247],a[232:235]\n"
"v_mfma_f32_16x16x32_bf16 a[240:243],v[216:219],v[248:251],a[240:243]\n"
"v_mfma_f32_16x16x32_bf16 a[244:247],v[220:223],v[248:251],a[244:247]\n"
"v_mfma_f32_16x16x32_bf16 a[252:255],v[220:223],v[252:255],a[252:255]\n"
"v_mfma_f32_16x16x32_bf16 a[248:251],v[216:219],v[252:255],a[248:251]\n"
".Lfk_ph2:\n"
"s_waitcnt vmcnt(25) lgkmcnt(0)\n"
"s_barrier\n"
"v_mfma_f32_16x16x32_bf16 v[128:131],v[32:35],v[0:3],0\n"
"v_accvgpr_read_b32 v116,a32\n"
"v_accvgpr_read_b32 v117,a33\n"
"v_mfma_f32_16x16x32_bf16 v[128:131],v[36:39],v[4:7],v[128:131]\n"
"v_accvgpr_read_b32 v118,a34\n"
"v_accvgpr_read_b32 v119,a35\n"
"v_mfma_f32_16x16x32_bf16 v[160:163],v[36:39],v[12:15],0\n"
"v_accvgpr_read_b32 v120,a36\n"
"v_accvgpr_read_b32 v121,a37\n"
"v_mfma_f32_16x16x32_bf16 v[160:163],v[32:35],v[8:11],v[160:163]\n"
"s_add_u32 m0,s85,147456\n"
"v_accvgpr_read_b32 v122,a38\n"
"v_mfma_f32_16x16x32_bf16 v[192:195],v[32:35],v[16:19],0\n"
"buffer_load_dwordx4 v110,s[28:31],s86 offen lds\n"
"v_mfma_f32_16x16x32_bf16 v[192:195],v[36:39],v[20:23],v[192:195]\n"
"v_accvgpr_read_b32 v123,a39\n"
"v_cvt_pk_bf16_f32 v116,v116,v117\n"
"v_mfma_f32_16x16x32_bf16 v[224:227],v[36:39],v[28:31],0\n"
"v_cvt_pk_bf16_f32 v117,v118,v119\n"
"v_cvt_pk_bf16_f32 v118,v120,v121\n"
"v_mfma_f32_16x16x32_bf16 v[224:227],v[32:35],v[24:27],v[224:227]\n"
"v_cvt_pk_bf16_f32 v119,v122,v123\n"
"s_add_u32 s56,s56,4096\n"
"v_mfma_f32_16x16x32_bf16 v[228:231],v[40:43],v[24:27],0\n"
"ds_read_b64_tr_b16 v[32:33],v100 offset:32768\n"
"buffer_store_dwordx4 v[116:119],v112,s[24:27],s56 offen offset:0 sc1\n"
"v_mfma_f32_16x16x32_bf16 v[228:231],v[44:47],v[28:31],v[228:231]\n"
"ds_read_b64_tr_b16 v[34:35],v100 offset:33792\n"
"v_accvgpr_read_b32 v116,a40\n"
"v_mfma_f32_16x16x32_bf16 v[196:199],v[44:47],v[20:23],0\n"
"ds_read_b64_tr_b16 v[36:37],v100 offset:40960\n"
"v_accvgpr_read_b32 v117,a41\n"
"v_mfma_f32_16x16x32_bf16 v[196:199],v[40:43],v[16:19],v[196:199]\n"
"s_add_u32 m0,s85,148480\n"
"ds_read_b64_tr_b16 v[38:39],v100 offset:41984\n"
"v_mfma_f32_16x16x32_bf16 v[164:167],v[40:43],v[8:11],0\n"
"buffer_load_dwordx4 v111,s[28:31],s86 offen lds\n"
"v_mfma_f32_16x16x32_bf16 v[164:167],v[44:47],v[12:15],v[164:167]\n"
"s_add_u32 m0,s85,155648\n"
"s_xor_b32 s86,s86,262144\n"
"v_mfma_f32_16x16x32_bf16 v[132:135],v[44:47],v[4:7],0\n"
"buffer_load_dwordx4 v110,s[28:31],s86 offen lds\n"
"v_mfma_f32_16x16x32_bf16 v[132:135],v[40:43],v[0:3],v[132:135]\n"
"v_accvgpr_read_b32 v118,a42\n"
"v_accvgpr_read_b32 v119,a43\n"
"v_mfma_f32_16x16x32_bf16 v[136:139],v[48:51],v[0:3],0\n"
"ds_read_b64_tr_b16 v[40:41],v101 offset:32768\n"
"v_accvgpr_read_b32 v120,a44\n"
"v_mfma_f32_16x16x32_bf16 v[136:139],v[52:55],v[4:7],v[136:139]\n"
"ds_read_b64_tr_b16 v[42:43],v101 offset:33792\n"
"v_accvgpr_read_b32 v121,a45\n"
"v_mfma_f32_16x16x32_bf16 v[168:171],v[52:55],v[12:15],0\n"
"ds_read_b64_tr_b16 v[44:45],v101 offset:40960\n"
"v_accvgpr_read_b32 v122,a46\n"
"v_mfma_f32_16x16x32_bf16 v[168:171],v[48:51],v[8:11],v[168:171]\n"
"s_add_u32 m0,s85,156672\n"
"ds_read_b64_tr_b16 v[46:47],v101 offset:41984\n"
"v_mfma_f32_16x16x32_bf16 v[200:203],v[48:51],v[16:19],0\n"
"buffer_load_dwordx4 v111,s[28:31],s86 offen lds\n"
"v_mfma_f32_16x16x32_bf16 v[200:203],v[52:55],v[20:23],v[200:203]\n"
"s_xor_b32 s86,s86,3932160\n"
"v_accvgpr_read_b32 v123,a47\n"
"v_mfma_f32_16x16x32_bf16 v[232:235],v[52:55],v[28:31],0\n"
"v_cvt_pk_bf16_f32 v116,v116,v117\n"
"v_cvt_pk_bf16_f32 v117,v118,v119\n"
"v_mfma_f32_16x16x32_bf16 v[232:235],v[48:51],v[24:27],v[232:235]\n"
"v_cvt_pk_bf16_f32 v118,v120,v121\n"
"v_cvt_pk_bf16_f32 v119,v122,v123\n"
"v_mfma_f32_16x16x32_bf16 v[236:239],v[56:59],v[24:27],0\n"
"ds_read_b64_tr_b16 v[48:49],v102 offset:32768\n"
"buffer_store_dwordx4 v[116:119],v112,s[24:27],s56 offen offset:1024 sc1\n"
"v_mfma_f32_16x16x32_bf16 v[236:239],v[60:63],v[28:31],v[236:239]\n"
"ds_read_b64_tr_b16 v[50:51],v102 offset:33792\n"
"v_accvgpr_read_b32 v116,a48\n"
"v_mfma_f32_16x16x32_bf16 v[204:207],v[60:63],v[20:23],0\n"
"ds_read_b64_tr_b16 v[52:53],v102 offset:40960\n"
"v_accvgpr_read_b32 v117,a49\n"
"v_mfma_f32_16x16x32_bf16 v[204:207],v[56:59],v[16:19],v[204:207]\n"
"s_add_u32 m0,s78,65536\n"
"ds_read_b64_tr_b16 v[54:55],v102 offset:41984\n"
"v_mfma_f32_16x16x32_bf16 v[172:175],v[56:59],v[8:11],0\n"
"buffer_load_dwordx4 v112,s[24:27],s76 offen offset:0 sc1 lds\n"
"v_mfma_f32_16x16x32_bf16 v[172:175],v[60:63],v[12:15],v[172:175]\n"
"v_accvgpr_read_b32 v118,a50\n"
"v_mfma_f32_16x16x32_bf16 v[140:143],v[60:63],v[4:7],0\n"
"buffer_load_dwordx4 v112,s[24:27],s76 offen offset:1024 sc1 lds\n"
"v_mfma_f32_16x16x32_bf16 v[140:143],v[56:59],v[0:3],v[140:143]\n"
"s_waitcnt vmcnt(25)\n"
"v_mfma_f32_16x16x32_bf16 v[144:147],v[64:67],v[0:3],0\n"
"ds_read_b64_tr_b16 a[32:33],v98 offset:32768\n"
"ds_read_b64_tr_b16 v[56:57],v103 offset:32768\n"
"v_mfma_f32_16x16x32_bf16 v[144:147],v[68:71],v[4:7],v[144:147]\n"
"ds_read_b64_tr_b16 a[34:35],v98 offset:33792\n"
"ds_read_b64_tr_b16 v[58:59],v103 offset:33792\n"
"v_mfma_f32_16x16x32_bf16 v[176:179],v[68:71],v[12:15],0\n"
"ds_read_b64_tr_b16 a[36:37],v98 offset:40960\n"
"ds_read_b64_tr_b16 v[60:61],v103 offset:40960\n"
"v_mfma_f32_16x16x32_bf16 v[176:179],v[64:67],v[8:11],v[176:179]\n"
"ds_read_b64_tr_b16 a[38:39],v98 offset:41984\n"
"ds_read_b64_tr_b16 v[62:63],v103 offset:41984\n"
"v_mfma_f32_16x16x32_bf16 v[208:211],v[64:67],v[16:19],0\n"
"buffer_load_dwordx4 v112,s[24:27],s76 offen offset:2048 sc1 lds\n"
"v_mfma_f32_16x16x32_bf16 v[208:211],v[68:71],v[20:23],v[208:211]\n"
"ds_read_b128 a[0:3],v96 offset:0\n"
"v_accvgpr_read_b32 v119,a51\n"
"v_mfma_f32_16x16x32_bf16 v[240:243],v[68:71],v[28:31],0\n"
"v_accvgpr_read_b32 v120,a52\n"
"v_accvgpr_read_b32 v121,a53\n"
"v_mfma_f32_16x16x32_bf16 v[240:243],v[64:67],v[24:27],v[240:243]\n"
"ds_read_b128 a[8:11],v96 offset:1024\n"
"v_accvgpr_read_b32 v122,a54\n"
"v_mfma_f32_16x16x32_bf16 v[244:247],v[72:75],v[24:27],0\n"
"ds_read_b64_tr_b16 a[40:41],v99 offset:32768\n"
"ds_read_b64_tr_b16 v[64:65],v104 offset:32768\n"
"v_mfma_f32_16x16x32_bf16 v[244:247],v[76:79],v[28:31],v[244:247]\n"
"ds_read_b64_tr_b16 a[42:43],v99 offset:33792\n"
"ds_read_b64_tr_b16 v[66:67],v104 offset:33792\n"
"v_mfma_f32_16x16x32_bf16 v[212:215],v[76:79],v[20:23],0\n"
"ds_read_b64_tr_b16 a[44:45],v99 offset:40960\n"
"ds_read_b64_tr_b16 v[68:69],v104 offset:40960\n"
"v_mfma_f32_16x16x32_bf16 v[212:215],v[72:75],v[16:19],v[212:215]\n"
"ds_read_b64_tr_b16 a[46:47],v99 offset:41984\n"
"ds_read_b64_tr_b16 v[70:71],v104 offset:41984\n"
"v_mfma_f32_16x16x32_bf16 v[180:183],v[72:75],v[8:11],0\n"
"buffer_load_dwordx4 v112,s[24:27],s76 offen offset:3072 sc1 lds\n"
"v_mfma_f32_16x16x32_bf16 v[180:183],v[76:79],v[12:15],v[180:183]\n"
"s_add_u32 m0,s78,81920\n"
"s_xor_b32 s76,s76,16384\n"
"ds_read_b128 a[16:19],v96 offset:2048\n"
"v_mfma_f32_16x16x32_bf16 v[148:151],v[76:79],v[4:7],0\n"
"buffer_load_dwordx4 v112,s[24:27],s76 offen offset:0 sc1 lds\n"
"v_mfma_f32_16x16x32_bf16 v[148:151],v[72:75],v[0:3],v[148:151]\n"
"ds_read_b128 a[24:27],v96 offset:3072\n"
"v_accvgpr_read_b32 v123,a55\n"
"v_mfma_f32_16x16x32_bf16 v[152:155],v[80:83],v[0:3],0\n"
"ds_read_b64_tr_b16 v[72:73],v105 offset:32768\n"
"v_cvt_pk_bf16_f32 v116,v116,v117\n"
"v_mfma_f32_16x16x32_bf16 v[152:155],v[84:87],v[4:7],v[152:155]\n"
"ds_read_b64_tr_b16 v[74:75],v105 offset:33792\n"
"v_cvt_pk_bf16_f32 v117,v118,v119\n"
"v_mfma_f32_16x16x32_bf16 v[184:187],v[84:87],v[12:15],0\n"
"ds_read_b64_tr_b16 v[76:77],v105 offset:40960\n"
"v_cvt_pk_bf16_f32 v118,v120,v121\n"
"v_mfma_f32_16x16x32_bf16 v[184:187],v[80:83],v[8:11],v[184:187]\n"
"ds_read_b64_tr_b16 v[78:79],v105 offset:41984\n"
"v_cvt_pk_bf16_f32 v119,v122,v123\n"
"v_mfma_f32_16x16x32_bf16 v[216:219],v[80:83],v[16:19],0\n"
"ds_read_b128 a[4:7],v96 offset:16384\n"
"buffer_store_dwordx4 v[116:119],v112,s[24:27],s56 offen offset:2048 sc1\n"
"v_mfma_f32_16x16x32_bf16 v[216:219],v[84:87],v[20:23],v[216:219]\n"
"ds_read_b128 a[12:15],v96 offset:17408\n"
"v_accvgpr_read_b32 v116,a56\n"
"v_mfma_f32_16x16x32_bf16 v[248:251],v[84:87],v[28:31],0\n"
"ds_read_b128 a[20:23],v96 offset:18432\n"
"v_accvgpr_read_b32 v117,a57\n"
"v_mfma_f32_16x16x32_bf16 v[248:251],v[80:83],v[24:27],v[248:251]\n"
"ds_read_b128 a[28:31],v96 offset:19456\n"
"v_accvgpr_read_b32 v118,a58\n"
"v_mfma_f32_16x16x32_bf16 v[252:255],v[88:91],v[24:27],0\n"
"buffer_load_dwordx4 v112,s[24:27],s76 offen offset:1024 sc1 lds\n"
"v_mfma_f32_16x16x32_bf16 v[252:255],v[92:95],v[28:31],v[252:255]\n"
"v_accvgpr_read_b32 v119,a59\n"
"v_accvgpr_read_b32 v120,a60\n"
"v_mfma_f32_16x16x32_bf16 v[220:223],v[92:95],v[20:23],0\n"
"v_accvgpr_read_b32 v121,a61\n"
"v_accvgpr_read_b32 v122,a62\n"
"v_mfma_f32_16x16x32_bf16 v[220:223],v[88:91],v[16:19],v[220:223]\n"
"buffer_load_dwordx4 v112,s[24:27],s76 offen offset:2048 sc1 lds\n"
"v_mfma_f32_16x16x32_bf16 v[188:191],v[88:91],v[8:11],0\n"
"v_accvgpr_read_b32 v123,a63\n"
"v_cvt_pk_bf16_f32 v116,v116,v117\n"
"v_mfma_f32_16x16x32_bf16 v[188:191],v[92:95],v[12:15],v[188:191]\n"
"v_cvt_pk_bf16_f32 v117,v118,v119\n"
"v_cvt_pk_bf16_f32 v118,v120,v121\n"
"v_mfma_f32_16x16x32_bf16 v[156:159],v[92:95],v[4:7],0\n"
"buffer_load_dwordx4 v112,s[24:27],s76 offen offset:3072 sc1 lds\n"
"v_mfma_f32_16x16x32_bf16 v[156:159],v[88:91],v[0:3],v[156:159]\n"
"s_xor_b32 s76,s76,245760\n"
"v_cvt_pk_bf16_f32 v119,v122,v123\n"
"s_waitcnt vmcnt(19) lgkmcnt(0)\n"
"s_barrier\n"
"v_mfma_f32_16x16x32_bf16 v[128:131],a[32:35],a[0:3],v[128:131]\n"
"ds_read_b64_tr_b16 v[80:81],v98 offset:0\n"
"ds_read_b64_tr_b16 v[88:89],v99 offset:0\n"
"v_mfma_f32_16x16x32_bf16 v[128:131],a[36:39],a[4:7],v[128:131]\n"
"ds_read_b64_tr_b16 v[82:83],v98 offset:1024\n"
"ds_read_b64_tr_b16 v[90:91],v99 offset:1024\n"
"v_mfma_f32_16x16x32_bf16 v[160:163],a[36:39],a[12:15],v[160:163]\n"
"ds_read_b64_tr_b16 v[84:85],v98 offset:8192\n"
"ds_read_b64_tr_b16 v[92:93],v99 offset:8192\n"
"v_mfma_f32_16x16x32_bf16 v[160:163],a[32:35],a[8:11],v[160:163]\n"
"s_add_u32 m0,s85,131072\n"
"ds_read_b64_tr_b16 v[86:87],v98 offset:9216\n"
"ds_read_b64_tr_b16 v[94:95],v99 offset:9216\n"
"v_mfma_f32_16x16x32_bf16 v[192:195],a[32:35],a[16:19],v[192:195]\n"
"buffer_load_dwordx4 v110,s[28:31],s86 offen lds\n"
"buffer_store_dwordx4 v[116:119],v112,s[24:27],s56 offen offset:3072 sc1\n"
"v_mfma_f32_16x16x32_bf16 v[192:195],a[36:39],a[20:23],v[192:195]\n"
"s_nop 0\n"
"v_accvgpr_read_b32 v116,a64\n"
"v_mfma_f32_16x16x32_bf16 v[224:227],a[36:39],a[28:31],v[224:227]\n"
"v_accvgpr_read_b32 v117,a65\n"
"v_accvgpr_read_b32 v118,a66\n"
"v_mfma_f32_16x16x32_bf16 v[224:227],a[32:35],a[24:27],v[224:227]\n"
"v_accvgpr_read_b32 v119,a67\n"
"v_accvgpr_read_b32 v120,a68\n"
"v_mfma_f32_16x16x32_bf16 v[228:231],a[40:43],a[24:27],v[228:231]\n"
"ds_read_b64_tr_b16 a[32:33],v100 offset:0\n"
"v_accvgpr_read_b32 v121,a69\n"
"v_mfma_f32_16x16x32_bf16 v[228:231],a[44:47],a[28:31],v[228:231]\n"
"ds_read_b64_tr_b16 a[34:35],v100 offset:1024\n"
"v_accvgpr_read_b32 v122,a70\n"
"v_mfma_f32_16x16x32_bf16 v[196:199],a[44:47],a[20:23],v[196:199]\n"
"ds_read_b64_tr_b16 a[36:37],v100 offset:8192\n"
"v_accvgpr_read_b32 v123,a71\n"
"v_mfma_f32_16x16x32_bf16 v[196:199],a[40:43],a[16:19],v[196:199]\n"
"s_add_u32 m0,s85,132096\n"
"ds_read_b64_tr_b16 a[38:39],v100 offset:9216\n"
"v_mfma_f32_16x16x32_bf16 v[164:167],a[40:43],a[8:11],v[164:167]\n"
"buffer_load_dwordx4 v111,s[28:31],s86 offen lds\n"
"v_mfma_f32_16x16x32_bf16 v[164:167],a[44:47],a[12:15],v[164:167]\n"
"s_add_u32 m0,s85,139264\n"
"s_xor_b32 s86,s86,262144\n"
"v_mfma_f32_16x16x32_bf16 v[132:135],a[44:47],a[4:7],v[132:135]\n"
"buffer_load_dwordx4 v110,s[28:31],s86 offen lds\n"
"v_mfma_f32_16x16x32_bf16 v[132:135],a[40:43],a[0:3],v[132:135]\n"
"v_cvt_pk_bf16_f32 v116,v116,v117\n"
"v_cvt_pk_bf16_f32 v117,v118,v119\n"
"v_mfma_f32_16x16x32_bf16 v[136:139],v[32:35],a[0:3],v[136:139]\n"
"ds_read_b64_tr_b16 a[40:41],v101 offset:0\n"
"v_cvt_pk_bf16_f32 v118,v120,v121\n"
"v_mfma_f32_16x16x32_bf16 v[136:139],v[36:39],a[4:7],v[136:139]\n"
"ds_read_b64_tr_b16 a[42:43],v101 offset:1024\n"
"v_cvt_pk_bf16_f32 v119,v122,v123\n"
"v_mfma_f32_16x16x32_bf16 v[168:171],v[36:39],a[12:15],v[168:171]\n"
"ds_read_b64_tr_b16 a[44:45],v101 offset:8192\n"
"s_add_u32 s56,s56,126976\n"
"v_mfma_f32_16x16x32_bf16 v[168:171],v[32:35],a[8:11],v[168:171]\n"
"s_add_u32 m0,s85,140288\n"
"ds_read_b64_tr_b16 a[46:47],v101 offset:9216\n"
"v_mfma_f32_16x16x32_bf16 v[200:203],v[32:35],a[16:19],v[200:203]\n"
"buffer_load_dwordx4 v111,s[28:31],s86 offen lds\n"
"v_mfma_f32_16x16x32_bf16 v[200:203],v[36:39],a[20:23],v[200:203]\n"
"buffer_store_dwordx4 v[116:119],v112,s[24:27],s56 offen offset:0 sc1\n"
"v_mfma_f32_16x16x32_bf16 v[232:235],v[36:39],a[28:31],v[232:235]\n"
"s_xor_b32 s86,s86,786432\n"
"v_accvgpr_read_b32 v116,a72\n"
"v_accvgpr_read_b32 v117,a73\n"
"v_mfma_f32_16x16x32_bf16 v[232:235],v[32:35],a[24:27],v[232:235]\n"
"v_accvgpr_read_b32 v118,a74\n"
"v_accvgpr_read_b32 v119,a75\n"
"v_mfma_f32_16x16x32_bf16 v[236:239],v[40:43],a[24:27],v[236:239]\n"
"ds_read_b64_tr_b16 v[32:33],v102 offset:0\n"
"v_accvgpr_read_b32 v120,a76\n"
"v_mfma_f32_16x16x32_bf16 v[236:239],v[44:47],a[28:31],v[236:239]\n"
"ds_read_b64_tr_b16 v[34:35],v102 offset:1024\n"
"v_accvgpr_read_b32 v121,a77\n"
"v_mfma_f32_16x16x32_bf16 v[204:207],v[44:47],a[20:23],v[204:207]\n"
"ds_read_b64_tr_b16 v[36:37],v102 offset:8192\n"
"v_accvgpr_read_b32 v122,a78\n"
"v_mfma_f32_16x16x32_bf16 v[204:207],v[40:43],a[16:19],v[204:207]\n"
"s_add_u32 m0,s78,0\n"
"ds_read_b64_tr_b16 v[38:39],v102 offset:9216\n"
"v_mfma_f32_16x16x32_bf16 v[172:175],v[40:43],a[8:11],v[172:175]\n"
"buffer_load_dwordx4 v112,s[24:27],s76 offen offset:0 sc1 lds\n"
"v_mfma_f32_16x16x32_bf16 v[172:175],v[44:47],a[12:15],v[172:175]\n"
"v_accvgpr_read_b32 v123,a79\n"
"v_cvt_pk_bf16_f32 v116,v116,v117\n"
"v_mfma_f32_16x16x32_bf16 v[140:143],v[44:47],a[4:7],v[140:143]\n"
"buffer_load_dwordx4 v112,s[24:27],s76 offen offset:1024 sc1 lds\n"
"v_mfma_f32_16x16x32_bf16 v[140:143],v[40:43],a[0:3],v[140:143]\n"
"s_waitcnt vmcnt(23)\n"
"v_cvt_pk_bf16_f32 v117,v118,v119\n"
"v_cvt_pk_bf16_f32 v118,v120,v121\n"
"v_mfma_f32_16x16x32_bf16 v[144:147],v[48:51],a[0:3],v[144:147]\n"
"ds_read_b64_tr_b16 v[40:41],v103 offset:0\n"
"v_cvt_pk_bf16_f32 v119,v122,v123\n"
"v_mfma_f32_16x16x32_bf16 v[144:147],v[52:55],a[4:7],v[144:147]\n"
"ds_read_b64_tr_b16 v[42:43],v103 offset:1024\n"
"buffer_store_dwordx4 v[116:119],v112,s[24:27],s56 offen offset:1024 sc1\n"
"v_mfma_f32_16x16x32_bf16 v[176:179],v[52:55],a[12:15],v[176:179]\n"
"ds_read_b64_tr_b16 v[44:45],v103 offset:8192\n"
"v_accvgpr_read_b32 v116,a80\n"
"v_mfma_f32_16x16x32_bf16 v[176:179],v[48:51],a[8:11],v[176:179]\n"
"ds_read_b64_tr_b16 v[46:47],v103 offset:9216\n"
"v_accvgpr_read_b32 v117,a81\n"
"v_mfma_f32_16x16x32_bf16 v[208:211],v[48:51],a[16:19],v[208:211]\n"
"buffer_load_dwordx4 v112,s[24:27],s76 offen offset:2048 sc1 lds\n"
"v_mfma_f32_16x16x32_bf16 v[208:211],v[52:55],a[20:23],v[208:211]\n"
"ds_read_b128 v[0:3],v96 offset:32768\n"
"v_accvgpr_read_b32 v118,a82\n"
"v_mfma_f32_16x16x32_bf16 v[240:243],v[52:55],a[28:31],v[240:243]\n"
"v_accvgpr_read_b32 v119,a83\n"
"v_accvgpr_read_b32 v120,a84\n"
"v_mfma_f32_16x16x32_bf16 v[240:243],v[48:51],a[24:27],v[240:243]\n"
"ds_read_b128 v[8:11],v96 offset:33792\n"
"v_accvgpr_read_b32 v121,a85\n"
"v_mfma_f32_16x16x32_bf16 v[244:247],v[56:59],a[24:27],v[244:247]\n"
"ds_read_b64_tr_b16 v[48:49],v104 offset:0\n"
"v_accvgpr_read_b32 v122,a86\n"
"v_mfma_f32_16x16x32_bf16 v[244:247],v[60:63],a[28:31],v[244:247]\n"
"ds_read_b64_tr_b16 v[50:51],v104 offset:1024\n"
"v_accvgpr_read_b32 v123,a87\n"
"v_mfma_f32_16x16x32_bf16 v[212:215],v[60:63],a[20:23],v[212:215]\n"
"ds_read_b64_tr_b16 v[52:53],v104 offset:8192\n"
"v_cvt_pk_bf16_f32 v116,v116,v117\n"
"v_mfma_f32_16x16x32_bf16 v[212:215],v[56:59],a[16:19],v[212:215]\n"
"ds_read_b64_tr_b16 v[54:55],v104 offset:9216\n"
"v_cvt_pk_bf16_f32 v117,v118,v119\n"
"v_mfma_f32_16x16x32_bf16 v[180:183],v[56:59],a[8:11],v[180:183]\n"
"buffer_load_dwordx4 v112,s[24:27],s76 offen offset:3072 sc1 lds\n"
"v_mfma_f32_16x16x32_bf16 v[180:183],v[60:63],a[12:15],v[180:183]\n"
"s_add_u32 m0,s78,16384\n"
"s_xor_b32 s76,s76,16384\n"
"ds_read_b128 v[16:19],v96 offset:34816\n"
"v_mfma_f32_16x16x32_bf16 v[148:151],v[60:63],a[4:7],v[148:151]\n"
"buffer_load_dwordx4 v112,s[24:27],s76 offen offset:0 sc1 lds\n"
"v_mfma_f32_16x16x32_bf16 v[148:151],v[56:59],a[0:3],v[148:151]\n"
"ds_read_b128 v[24:27],v96 offset:35840\n"
"v_cvt_pk_bf16_f32 v118,v120,v121\n"
"v_mfma_f32_16x16x32_bf16 v[152:155],v[64:67],a[0:3],v[152:155]\n"
"ds_read_b64_tr_b16 v[56:57],v105 offset:0\n"
"v_cvt_pk_bf16_f32 v119,v122,v123\n"
"v_mfma_f32_16x16x32_bf16 v[152:155],v[68:71],a[4:7],v[152:155]\n"
"ds_read_b64_tr_b16 v[58:59],v105 offset:1024\n"
"buffer_store_dwordx4 v[116:119],v112,s[24:27],s56 offen offset:2048 sc1\n"
"v_mfma_f32_16x16x32_bf16 v[184:187],v[68:71],a[12:15],v[184:187]\n"
"ds_read_b64_tr_b16 v[60:61],v105 offset:8192\n"
"v_accvgpr_read_b32 v116,a88\n"
"v_mfma_f32_16x16x32_bf16 v[184:187],v[64:67],a[8:11],v[184:187]\n"
"ds_read_b64_tr_b16 v[62:63],v105 offset:9216\n"
"v_accvgpr_read_b32 v117,a89\n"
"v_mfma_f32_16x16x32_bf16 v[216:219],v[64:67],a[16:19],v[216:219]\n"
"ds_read_b128 v[4:7],v96 offset:49152\n"
"v_accvgpr_read_b32 v118,a90\n"
"v_mfma_f32_16x16x32_bf16 v[216:219],v[68:71],a[20:23],v[216:219]\n"
"ds_read_b128 v[12:15],v96 offset:50176\n"
"v_accvgpr_read_b32 v119,a91\n"
"v_mfma_f32_16x16x32_bf16 v[248:251],v[68:71],a[28:31],v[248:251]\n"
"ds_read_b128 v[20:23],v96 offset:51200\n"
"v_accvgpr_read_b32 v120,a92\n"
"v_mfma_f32_16x16x32_bf16 v[248:251],v[64:67],a[24:27],v[248:251]\n"
"ds_read_b128 v[28:31],v96 offset:52224\n"
"v_accvgpr_read_b32 v121,a93\n"
"v_mfma_f32_16x16x32_bf16 v[252:255],v[72:75],a[24:27],v[252:255]\n"
"buffer_load_dwordx4 v112,s[24:27],s76 offen offset:1024 sc1 lds\n"
"v_mfma_f32_16x16x32_bf16 v[252:255],v[76:79],a[28:31],v[252:255]\n"
"v_accvgpr_read_b32 v122,a94\n"
"v_accvgpr_read_b32 v123,a95\n"
"v_mfma_f32_16x16x32_bf16 v[220:223],v[76:79],a[20:23],v[220:223]\n"
"v_cvt_pk_bf16_f32 v116,v116,v117\n"
"v_cvt_pk_bf16_f32 v117,v118,v119\n"
"v_mfma_f32_16x16x32_bf16 v[220:223],v[72:75],a[16:19],v[220:223]\n"
"buffer_load_dwordx4 v112,s[24:27],s76 offen offset:2048 sc1 lds\n"
"v_mfma_f32_16x16x32_bf16 v[188:191],v[72:75],a[8:11],v[188:191]\n"
"v_cvt_pk_bf16_f32 v118,v120,v121\n"
"v_cvt_pk_bf16_f32 v119,v122,v123\n"
"v_mfma_f32_16x16x32_bf16 v[188:191],v[76:79],a[12:15],v[188:191]\n"
"buffer_store_dwordx4 v[116:119],v112,s[24:27],s56 offen offset:3072 sc1\n"
"v_mfma_f32_16x16x32_bf16 v[156:159],v[76:79],a[4:7],v[156:159]\n"
"buffer_load_dwordx4 v112,s[24:27],s76 offen offset:3072 sc1 lds\n"
"v_accvgpr_read_b32 v116,a96\n"
"v_mfma_f32_16x16x32_bf16 v[156:159],v[72:75],a[0:3],v[156:159]\n"
"s_xor_b32 s76,s76,49152\n"
"v_accvgpr_read_b32 v117,a97\n"
"s_waitcnt vmcnt(27) lgkmcnt(0)\n"
"s_barrier\n"
"v_mfma_f32_16x16x32_bf16 v[128:131],v[80:83],v[0:3],v[128:131]\n"
"ds_read_b64_tr_b16 v[64:65],v98 offset:49152\n"
"ds_read_b64_tr_b16 v[72:73],v99 offset:49152\n"
"v_mfma_f32_16x16x32_bf16 v[128:131],v[84:87],v[4:7],v[128:131]\n"
"ds_read_b64_tr_b16 v[66:67],v98 offset:50176\n"
"ds_read_b64_tr_b16 v[74:75],v99 offset:50176\n"
"v_mfma_f32_16x16x32_bf16 v[160:163],v[84:87],v[12:15],v[160:163]\n"
"ds_read_b64_tr_b16 v[68:69],v98 offset:57344\n"
"ds_read_b64_tr_b16 v[76:77],v99 offset:57344\n"
"v_mfma_f32_16x16x32_bf16 v[160:163],v[80:83],v[8:11],v[160:163]\n"
"s_add_u32 m0,s85,98304\n"
"ds_read_b64_tr_b16 v[70:71],v98 offset:58368\n"
"ds_read_b64_tr_b16 v[78:79],v99 offset:58368\n"
"v_mfma_f32_16x16x32_bf16 v[192:195],v[80:83],v[16:19],v[192:195]\n"
"buffer_load_dwordx4 v110,s[28:31],s86 offen lds\n"
"v_mfma_f32_16x16x32_bf16 v[192:195],v[84:87],v[20:23],v[192:195]\n"
"v_accvgpr_read_b32 v118,a98\n"
"v_accvgpr_read_b32 v119,a99\n"
"v_mfma_f32_16x16x32_bf16 v[224:227],v[84:87],v[28:31],v[224:227]\n"
"v_accvgpr_read_b32 v120,a100\n"
"v_accvgpr_read_b32 v121,a101\n"
"v_mfma_f32_16x16x32_bf16 v[224:227],v[80:83],v[24:27],v[224:227]\n"
"v_accvgpr_read_b32 v122,a102\n"
"v_accvgpr_read_b32 v123,a103\n"
"v_mfma_f32_16x16x32_bf16 v[228:231],v[88:91],v[24:27],v[228:231]\n"
"ds_read_b64_tr_b16 v[80:81],v100 offset:49152\n"
"v_cvt_pk_bf16_f32 v116,v116,v117\n"
"v_mfma_f32_16x16x32_bf16 v[228:231],v[92:95],v[28:31],v[228:231]\n"
"ds_read_b64_tr_b16 v[82:83],v100 offset:50176\n"
"v_cvt_pk_bf16_f32 v117,v118,v119\n"
"v_mfma_f32_16x16x32_bf16 v[196:199],v[92:95],v[20:23],v[196:199]\n"
"ds_read_b64_tr_b16 v[84:85],v100 offset:57344\n"
"v_cvt_pk_bf16_f32 v118,v120,v121\n"
"v_mfma_f32_16x16x32_bf16 v[196:199],v[88:91],v[16:19],v[196:199]\n"
"s_add_u32 m0,s85,99328\n"
"ds_read_b64_tr_b16 v[86:87],v100 offset:58368\n"
"v_mfma_f32_16x16x32_bf16 v[164:167],v[88:91],v[8:11],v[164:167]\n"
"buffer_load_dwordx4 v111,s[28:31],s86 offen lds\n"
"v_mfma_f32_16x16x32_bf16 v[164:167],v[92:95],v[12:15],v[164:167]\n"
"s_add_u32 m0,s85,106496\n"
"s_xor_b32 s86,s86,262144\n"
"v_mfma_f32_16x16x32_bf16 v[132:135],v[92:95],v[4:7],v[132:135]\n"
"buffer_load_dwordx4 v110,s[28:31],s86 offen lds\n"
"v_mfma_f32_16x16x32_bf16 v[132:135],v[88:91],v[0:3],v[132:135]\n"
"v_cvt_pk_bf16_f32 v119,v122,v123\n"
"s_add_u32 s56,s56,4096\n"
"v_mfma_f32_16x16x32_bf16 v[136:139],a[32:35],v[0:3],v[136:139]\n"
"ds_read_b64_tr_b16 v[88:89],v101 offset:49152\n"
"buffer_store_dwordx4 v[116:119],v112,s[24:27],s56 offen offset:0 sc1\n"
"v_mfma_f32_16x16x32_bf16 v[136:139],a[36:39],v[4:7],v[136:139]\n"
"ds_read_b64_tr_b16 v[90:91],v101 offset:50176\n"
"v_accvgpr_read_b32 v116,a104\n"
"v_mfma_f32_16x16x32_bf16 v[168:171],a[36:39],v[12:15],v[168:171]\n"
"ds_read_b64_tr_b16 v[92:93],v101 offset:57344\n"
"v_accvgpr_read_b32 v117,a105\n"
"v_mfma_f32_16x16x32_bf16 v[168:171],a[32:35],v[8:11],v[168:171]\n"
"s_add_u32 m0,s85,107520\n"
"ds_read_b64_tr_b16 v[94:95],v101 offset:58368\n"
"v_mfma_f32_16x16x32_bf16 v[200:203],a[32:35],v[16:19],v[200:203]\n"
"buffer_load_dwordx4 v111,s[28:31],s86 offen lds\n"
"v_mfma_f32_16x16x32_bf16 v[200:203],a[36:39],v[20:23],v[200:203]\n"
"s_xor_b32 s86,s86,1835008\n"
"v_accvgpr_read_b32 v118,a106\n"
"v_mfma_f32_16x16x32_bf16 v[232:235],a[36:39],v[28:31],v[232:235]\n"
"v_accvgpr_read_b32 v119,a107\n"
"v_accvgpr_read_b32 v120,a108\n"
"v_mfma_f32_16x16x32_bf16 v[232:235],a[32:35],v[24:27],v[232:235]\n"
"v_accvgpr_read_b32 v121,a109\n"
"v_accvgpr_read_b32 v122,a110\n"
"v_mfma_f32_16x16x32_bf16 v[236:239],a[40:43],v[24:27],v[236:239]\n"
"ds_read_b64_tr_b16 a[32:33],v102 offset:49152\n"
"v_accvgpr_read_b32 v123,a111\n"
"v_mfma_f32_16x16x32_bf16 v[236:239],a[44:47],v[28:31],v[236:239]\n"
"ds_read_b64_tr_b16 a[34:35],v102 offset:50176\n"
"v_cvt_pk_bf16_f32 v116,v116,v117\n"
"v_mfma_f32_16x16x32_bf16 v[204:207],a[44:47],v[20:23],v[204:207]\n"
"ds_read_b64_tr_b16 a[36:37],v102 offset:57344\n"
"v_cvt_pk_bf16_f32 v117,v118,v119\n"
"v_mfma_f32_16x16x32_bf16 v[204:207],a[40:43],v[16:19],v[204:207]\n"
"s_add_u32 m0,s78,32768\n"
"ds_read_b64_tr_b16 a[38:39],v102 offset:58368\n"
"v_mfma_f32_16x16x32_bf16 v[172:175],a[40:43],v[8:11],v[172:175]\n"
"buffer_load_dwordx4 v112,s[24:27],s76 offen offset:0 sc1 lds\n"
"v_mfma_f32_16x16x32_bf16 v[172:175],a[44:47],v[12:15],v[172:175]\n"
"v_cvt_pk_bf16_f32 v118,v120,v121\n"
"v_cvt_pk_bf16_f32 v119,v122,v123\n"
"v_mfma_f32_16x16x32_bf16 v[140:143],a[44:47],v[4:7],v[140:143]\n"
"buffer_load_dwordx4 v112,s[24:27],s76 offen offset:1024 sc1 lds\n"
"buffer_store_dwordx4 v[116:119],v112,s[24:27],s56 offen offset:1024 sc1\n"
"v_mfma_f32_16x16x32_bf16 v[140:143],a[40:43],v[0:3],v[140:143]\n"
"s_nop 0\n"
"v_accvgpr_read_b32 v116,a112\n"
"s_waitcnt vmcnt(25)\n"
"v_mfma_f32_16x16x32_bf16 v[144:147],v[32:35],v[0:3],v[144:147]\n"
"ds_read_b64_tr_b16 a[40:41],v103 offset:49152\n"
"v_accvgpr_read_b32 v117,a113\n"
"v_mfma_f32_16x16x32_bf16 v[144:147],v[36:39],v[4:7],v[144:147]\n"
"ds_read_b64_tr_b16 a[42:43],v103 offset:50176\n"
"v_accvgpr_read_b32 v118,a114\n"
"v_mfma_f32_16x16x32_bf16 v[176:179],v[36:39],v[12:15],v[176:179]\n"
"ds_read_b64_tr_b16 a[44:45],v103 offset:57344\n"
"v_accvgpr_read_b32 v119,a115\n"
"v_mfma_f32_16x16x32_bf16 v[176:179],v[32:35],v[8:11],v[176:179]\n"
"ds_read_b64_tr_b16 a[46:47],v103 offset:58368\n"
"v_accvgpr_read_b32 v120,a116\n"
"v_mfma_f32_16x16x32_bf16 v[208:211],v[32:35],v[16:19],v[208:211]\n"
"buffer_load_dwordx4 v112,s[24:27],s76 offen offset:2048 sc1 lds\n"
"v_mfma_f32_16x16x32_bf16 v[208:211],v[36:39],v[20:23],v[208:211]\n"
"ds_read_b128 a[0:3],v97 offset:0\n"
"v_accvgpr_read_b32 v121,a117\n"
"v_mfma_f32_16x16x32_bf16 v[240:243],v[36:39],v[28:31],v[240:243]\n"
"v_accvgpr_read_b32 v122,a118\n"
"v_accvgpr_read_b32 v123,a119\n"
"v_mfma_f32_16x16x32_bf16 v[240:243],v[32:35],v[24:27],v[240:243]\n"
"ds_read_b128 a[8:11],v97 offset:1024\n"
"v_cvt_pk_bf16_f32 v116,v116,v117\n"
"v_mfma_f32_16x16x32_bf16 v[244:247],v[40:43],v[24:27],v[244:247]\n"
"ds_read_b64_tr_b16 v[32:33],v104 offset:49152\n"
"v_cvt_pk_bf16_f32 v117,v118,v119\n"
"v_mfma_f32_16x16x32_bf16 v[244:247],v[44:47],v[28:31],v[244:247]\n"
"ds_read_b64_tr_b16 v[34:35],v104 offset:50176\n"
"v_cvt_pk_bf16_f32 v118,v120,v121\n"
"v_mfma_f32_16x16x32_bf16 v[212:215],v[44:47],v[20:23],v[212:215]\n"
"ds_read_b64_tr_b16 v[36:37],v104 offset:57344\n"
"v_cvt_pk_bf16_f32 v119,v122,v123\n"
"v_mfma_f32_16x16x32_bf16 v[212:215],v[40:43],v[16:19],v[212:215]\n"
"ds_read_b64_tr_b16 v[38:39],v104 offset:58368\n"
"buffer_store_dwordx4 v[116:119],v112,s[24:27],s56 offen offset:2048 sc1\n"
"v_mfma_f32_16x16x32_bf16 v[180:183],v[40:43],v[8:11],v[180:183]\n"
"buffer_load_dwordx4 v112,s[24:27],s76 offen offset:3072 sc1 lds\n"
"v_mfma_f32_16x16x32_bf16 v[180:183],v[44:47],v[12:15],v[180:183]\n"
"s_add_u32 m0,s78,49152\n"
"s_xor_b32 s76,s76,16384\n"
"ds_read_b128 a[16:19],v97 offset:2048\n"
"v_mfma_f32_16x16x32_bf16 v[148:151],v[44:47],v[4:7],v[148:151]\n"
"buffer_load_dwordx4 v112,s[24:27],s76 offen offset:0 sc1 lds\n"
"v_mfma_f32_16x16x32_bf16 v[148:151],v[40:43],v[0:3],v[148:151]\n"
"ds_read_b128 a[24:27],v97 offset:3072\n"
"v_accvgpr_read_b32 v116,a120\n"
"v_mfma_f32_16x16x32_bf16 v[152:155],v[48:51],v[0:3],v[152:155]\n"
"ds_read_b64_tr_b16 v[40:41],v105 offset:49152\n"
"v_accvgpr_read_b32 v117,a121\n"
"v_mfma_f32_16x16x32_bf16 v[152:155],v[52:55],v[4:7],v[152:155]\n"
"ds_read_b64_tr_b16 v[42:43],v105 offset:50176\n"
"v_accvgpr_read_b32 v118,a122\n"
"v_mfma_f32_16x16x32_bf16 v[184:187],v[52:55],v[12:15],v[184:187]\n"
"ds_read_b64_tr_b16 v[44:45],v105 offset:57344\n"
"v_accvgpr_read_b32 v119,a123\n"
"v_mfma_f32_16x16x32_bf16 v[184:187],v[48:51],v[8:11],v[184:187]\n"
"ds_read_b64_tr_b16 v[46:47],v105 offset:58368\n"
"v_accvgpr_read_b32 v120,a124\n"
"v_mfma_f32_16x16x32_bf16 v[216:219],v[48:51],v[16:19],v[216:219]\n"
"ds_read_b128 a[4:7],v97 offset:16384\n"
"v_accvgpr_read_b32 v121,a125\n"
"v_mfma_f32_16x16x32_bf16 v[216:219],v[52:55],v[20:23],v[216:219]\n"
"ds_read_b128 a[12:15],v97 offset:17408\n"
"v_accvgpr_read_b32 v122,a126\n"
"v_mfma_f32_16x16x32_bf16 v[248:251],v[52:55],v[28:31],v[248:251]\n"
"ds_read_b128 a[20:23],v97 offset:18432\n"
"v_accvgpr_read_b32 v123,a127\n"
"v_mfma_f32_16x16x32_bf16 v[248:251],v[48:51],v[24:27],v[248:251]\n"
"ds_read_b128 a[28:31],v97 offset:19456\n"
"v_cvt_pk_bf16_f32 v116,v116,v117\n"
"v_mfma_f32_16x16x32_bf16 v[252:255],v[56:59],v[24:27],v[252:255]\n"
"buffer_load_dwordx4 v112,s[24:27],s76 offen offset:1024 sc1 lds\n"
"v_mfma_f32_16x16x32_bf16 v[252:255],v[60:63],v[28:31],v[252:255]\n"
"v_cvt_pk_bf16_f32 v117,v118,v119\n"
"v_cvt_pk_bf16_f32 v118,v120,v121\n"
"v_cvt_pk_bf16_f32 v119,v122,v123\n"
"v_mfma_f32_16x16x32_bf16 v[220:223],v[60:63],v[20:23],v[220:223]\n"
"buffer_store_dwordx4 v[116:119],v112,s[24:27],s56 offen offset:3072 sc1\n"
"v_mfma_f32_16x16x32_bf16 v[220:223],v[56:59],v[16:19],v[220:223]\n"
"buffer_load_dwordx4 v112,s[24:27],s76 offen offset:2048 sc1 lds\n"
"v_mfma_f32_16x16x32_bf16 v[188:191],v[56:59],v[8:11],v[188:191]\n"
"v_accvgpr_read_b32 v116,a128\n"
"v_accvgpr_read_b32 v117,a129\n"
"v_mfma_f32_16x16x32_bf16 v[188:191],v[60:63],v[12:15],v[188:191]\n"
"v_accvgpr_read_b32 v118,a130\n"
"v_accvgpr_read_b32 v119,a131\n"
"v_mfma_f32_16x16x32_bf16 v[156:159],v[60:63],v[4:7],v[156:159]\n"
"buffer_load_dwordx4 v112,s[24:27],s76 offen offset:3072 sc1 lds\n"
"v_mfma_f32_16x16x32_bf16 v[156:159],v[56:59],v[0:3],v[156:159]\n"
"s_xor_b32 s76,s76,114688\n"
"v_accvgpr_read_b32 v120,a132\n"
"s_waitcnt vmcnt(28) lgkmcnt(0)\n"
"s_barrier\n"
"v_mfma_f32_16x16x32_bf16 v[128:131],v[64:67],a[0:3],v[128:131]\n"
"ds_read_b64_tr_b16 v[48:49],v98 offset:32768\n"
"ds_read_b64_tr_b16 v[56:57],v99 offset:32768\n"
"v_mfma_f32_16x16x32_bf16 v[128:131],v[68:71],a[4:7],v[128:131]\n"
"ds_read_b64_tr_b16 v[50:51],v98 offset:33792\n"
"ds_read_b64_tr_b16 v[58:59],v99 offset:33792\n"
"v_mfma_f32_16x16x32_bf16 v[160:163],v[68:71],a[12:15],v[160:163]\n"
"ds_read_b64_tr_b16 v[52:53],v98 offset:40960\n"
"ds_read_b64_tr_b16 v[60:61],v99 offset:40960\n"
"v_mfma_f32_16x16x32_bf16 v[160:163],v[64:67],a[8:11],v[160:163]\n"
"s_add_u32 m0,s85,147456\n"
"ds_read_b64_tr_b16 v[54:55],v98 offset:41984\n"
"ds_read_b64_tr_b16 v[62:63],v99 offset:41984\n"
"v_mfma_f32_16x16x32_bf16 v[192:195],v[64:67],a[16:19],v[192:195]\n"
"buffer_load_dwordx4 v110,s[28:31],s86 offen lds\n"
"v_mfma_f32_16x16x32_bf16 v[192:195],v[68:71],a[20:23],v[192:195]\n"
"v_accvgpr_read_b32 v121,a133\n"
"v_accvgpr_read_b32 v122,a134\n"
"v_mfma_f32_16x16x32_bf16 v[224:227],v[68:71],a[28:31],v[224:227]\n"
"v_accvgpr_read_b32 v123,a135\n"
"v_cvt_pk_bf16_f32 v116,v116,v117\n"
"v_mfma_f32_16x16x32_bf16 v[224:227],v[64:67],a[24:27],v[224:227]\n"
"v_cvt_pk_bf16_f32 v117,v118,v119\n"
"v_cvt_pk_bf16_f32 v118,v120,v121\n"
"v_mfma_f32_16x16x32_bf16 v[228:231],v[72:75],a[24:27],v[228:231]\n"
"ds_read_b64_tr_b16 v[64:65],v100 offset:32768\n"
"v_cvt_pk_bf16_f32 v119,v122,v123\n"
"v_mfma_f32_16x16x32_bf16 v[228:231],v[76:79],a[28:31],v[228:231]\n"
"ds_read_b64_tr_b16 v[66:67],v100 offset:33792\n"
"s_add_u32 s56,s56,126976\n"
"v_mfma_f32_16x16x32_bf16 v[196:199],v[76:79],a[20:23],v[196:199]\n"
"ds_read_b64_tr_b16 v[68:69],v100 offset:40960\n"
"buffer_store_dwordx4 v[116:119],v112,s[24:27],s56 offen offset:0 sc1\n"
"v_mfma_f32_16x16x32_bf16 v[196:199],v[72:75],a[16:19],v[196:199]\n"
"s_add_u32 m0,s85,148480\n"
"ds_read_b64_tr_b16 v[70:71],v100 offset:41984\n"
"v_mfma_f32_16x16x32_bf16 v[164:167],v[72:75],a[8:11],v[164:167]\n"
"buffer_load_dwordx4 v111,s[28:31],s86 offen lds\n"
"v_mfma_f32_16x16x32_bf16 v[164:167],v[76:79],a[12:15],v[164:167]\n"
"s_add_u32 m0,s85,155648\n"
"s_xor_b32 s86,s86,262144\n"
"v_mfma_f32_16x16x32_bf16 v[132:135],v[76:79],a[4:7],v[132:135]\n"
"buffer_load_dwordx4 v110,s[28:31],s86 offen lds\n"
"v_mfma_f32_16x16x32_bf16 v[132:135],v[72:75],a[0:3],v[132:135]\n"
"v_accvgpr_read_b32 v116,a136\n"
"v_accvgpr_read_b32 v117,a137\n"
"v_mfma_f32_16x16x32_bf16 v[136:139],v[80:83],a[0:3],v[136:139]\n"
"ds_read_b64_tr_b16 v[72:73],v101 offset:32768\n"
"v_accvgpr_read_b32 v118,a138\n"
"v_mfma_f32_16x16x32_bf16 v[136:139],v[84:87],a[4:7],v[136:139]\n"
"ds_read_b64_tr_b16 v[74:75],v101 offset:33792\n"
"v_accvgpr_read_b32 v119,a139\n"
"v_mfma_f32_16x16x32_bf16 v[168:171],v[84:87],a[12:15],v[168:171]\n"
"ds_read_b64_tr_b16 v[76:77],v101 offset:40960\n"
"v_accvgpr_read_b32 v120,a140\n"
"v_mfma_f32_16x16x32_bf16 v[168:171],v[80:83],a[8:11],v[168:171]\n"
"s_add_u32 m0,s85,156672\n"
"ds_read_b64_tr_b16 v[78:79],v101 offset:41984\n"
"v_mfma_f32_16x16x32_bf16 v[200:203],v[80:83],a[16:19],v[200:203]\n"
"buffer_load_dwordx4 v111,s[28:31],s86 offen lds\n"
"v_mfma_f32_16x16x32_bf16 v[200:203],v[84:87],a[20:23],v[200:203]\n"
"s_xor_b32 s86,s86,786432\n"
"v_accvgpr_read_b32 v121,a141\n"
"v_mfma_f32_16x16x32_bf16 v[232:235],v[84:87],a[28:31],v[232:235]\n"
"v_accvgpr_read_b32 v122,a142\n"
"v_accvgpr_read_b32 v123,a143\n"
"v_mfma_f32_16x16x32_bf16 v[232:235],v[80:83],a[24:27],v[232:235]\n"
"v_cvt_pk_bf16_f32 v116,v116,v117\n"
"v_cvt_pk_bf16_f32 v117,v118,v119\n"
"v_mfma_f32_16x16x32_bf16 v[236:239],v[88:91],a[24:27],v[236:239]\n"
"ds_read_b64_tr_b16 v[80:81],v102 offset:32768\n"
"v_cvt_pk_bf16_f32 v118,v120,v121\n"
"v_mfma_f32_16x16x32_bf16 v[236:239],v[92:95],a[28:31],v[236:239]\n"
"ds_read_b64_tr_b16 v[82:83],v102 offset:33792\n"
"v_cvt_pk_bf16_f32 v119,v122,v123\n"
"v_mfma_f32_16x16x32_bf16 v[204:207],v[92:95],a[20:23],v[204:207]\n"
"ds_read_b64_tr_b16 v[84:85],v102 offset:40960\n"
"buffer_store_dwordx4 v[116:119],v112,s[24:27],s56 offen offset:1024 sc1\n"
"v_mfma_f32_16x16x32_bf16 v[204:207],v[88:91],a[16:19],v[204:207]\n"
"s_add_u32 m0,s78,65536\n"
"ds_read_b64_tr_b16 v[86:87],v102 offset:41984\n"
"v_accvgpr_read_b32 v118,a146\n"
"v_mfma_f32_16x16x32_bf16 v[172:175],v[88:91],a[8:11],v[172:175]\n"
"buffer_load_dwordx4 v112,s[24:27],s76 offen offset:0 sc1 lds\n"
"v_mfma_f32_16x16x32_bf16 v[172:175],v[92:95],a[12:15],v[172:175]\n"
"v_accvgpr_read_b32 v116,a144\n"
"v_accvgpr_read_b32 v117,a145\n"
"v_mfma_f32_16x16x32_bf16 v[140:143],v[92:95],a[4:7],v[140:143]\n"
"buffer_load_dwordx4 v112,s[24:27],s76 offen offset:1024 sc1 lds\n"
"v_mfma_f32_16x16x32_bf16 v[140:143],v[88:91],a[0:3],v[140:143]\n"
"v_accvgpr_read_b32 v119,a147\n"
"s_waitcnt vmcnt(24)\n"
"v_mfma_f32_16x16x32_bf16 v[144:147],a[32:35],a[0:3],v[144:147]\n"
"ds_read_b64_tr_b16 v[88:89],v103 offset:32768\n"
"v_accvgpr_read_b32 v120,a148\n"
"v_mfma_f32_16x16x32_bf16 v[144:147],a[36:39],a[4:7],v[144:147]\n"
"ds_read_b64_tr_b16 v[90:91],v103 offset:33792\n"
"v_accvgpr_read_b32 v121,a149\n"
"v_mfma_f32_16x16x32_bf16 v[176:179],a[36:39],a[12:15],v[176:179]\n"
"ds_read_b64_tr_b16 v[92:93],v103 offset:40960\n"
"v_accvgpr_read_b32 v122,a150\n"
"v_mfma_f32_16x16x32_bf16 v[176:179],a[32:35],a[8:11],v[176:179]\n"
"ds_read_b64_tr_b16 v[94:95],v103 offset:41984\n"
"v_accvgpr_read_b32 v123,a151\n"
"v_mfma_f32_16x16x32_bf16 v[208:211],a[32:35],a[16:19],v[208:211]\n"
"buffer_load_dwordx4 v112,s[24:27],s76 offen offset:2048 sc1 lds\n"
"v_mfma_f32_16x16x32_bf16 v[208:211],a[36:39],a[20:23],v[208:211]\n"
"ds_read_b128 v[0:3],v96 offset:0\n"
"v_cvt_pk_bf16_f32 v116,v116,v117\n"
"v_mfma_f32_16x16x32_bf16 v[240:243],a[36:39],a[28:31],v[240:243]\n"
"v_cvt_pk_bf16_f32 v117,v118,v119\n"
"v_cvt_pk_bf16_f32 v118,v120,v121\n"
"v_mfma_f32_16x16x32_bf16 v[240:243],a[32:35],a[24:27],v[240:243]\n"
"ds_read_b128 v[8:11],v96 offset:1024\n"
"v_cvt_pk_bf16_f32 v119,v122,v123\n"
"v_mfma_f32_16x16x32_bf16 v[244:247],a[40:43],a[24:27],v[244:247]\n"
"ds_read_b64_tr_b16 a[32:33],v104 offset:32768\n"
"buffer_store_dwordx4 v[116:119],v112,s[24:27],s56 offen offset:2048 sc1\n"
"v_mfma_f32_16x16x32_bf16 v[244:247],a[44:47],a[28:31],v[244:247]\n"
"ds_read_b64_tr_b16 a[34:35],v104 offset:33792\n"
"v_accvgpr_read_b32 v116,a152\n"
"v_mfma_f32_16x16x32_bf16 v[212:215],a[44:47],a[20:23],v[212:215]\n"
"ds_read_b64_tr_b16 a[36:37],v104 offset:40960\n"
"v_accvgpr_read_b32 v117,a153\n"
"v_mfma_f32_16x16x32_bf16 v[212:215],a[40:43],a[16:19],v[212:215]\n"
"ds_read_b64_tr_b16 a[38:39],v104 offset:41984\n"
"v_accvgpr_read_b32 v118,a154\n"
"v_mfma_f32_16x16x32_bf16 v[180:183],a[40:43],a[8:11],v[180:183]\n"
"buffer_load_dwordx4 v112,s[24:27],s76 offen offset:3072 sc1 lds\n"
"v_mfma_f32_16x16x32_bf16 v[180:183],a[44:47],a[12:15],v[180:183]\n"
"s_add_u32 m0,s78,81920\n"
"s_xor_b32 s76,s76,16384\n"
"ds_read_b128 v[16:19],v96 offset:2048\n"
"v_mfma_f32_16x16x32_bf16 v[148:151],a[44:47],a[4:7],v[148:151]\n"
"buffer_load_dwordx4 v112,s[24:27],s76 offen offset:0 sc1 lds\n"
"v_mfma_f32_16x16x32_bf16 v[148:151],a[40:43],a[0:3],v[148:151]\n"
"ds_read_b128 v[24:27],v96 offset:3072\n"
"v_accvgpr_read_b32 v119,a155\n"
"v_mfma_f32_16x16x32_bf16 v[152:155],v[32:35],a[0:3],v[152:155]\n"
"ds_read_b64_tr_b16 a[40:41],v105 offset:32768\n"
"v_accvgpr_read_b32 v120,a156\n"
"v_mfma_f32_16x16x32_bf16 v[152:155],v[36:39],a[4:7],v[152:155]\n"
"ds_read_b64_tr_b16 a[42:43],v105 offset:33792\n"
"v_accvgpr_read_b32 v121,a157\n"
"v_mfma_f32_16x16x32_bf16 v[184:187],v[36:39],a[12:15],v[184:187]\n"
"ds_read_b64_tr_b16 a[44:45],v105 offset:40960\n"
"v_accvgpr_read_b32 v122,a158\n"
"v_mfma_f32_16x16x32_bf16 v[184:187],v[32:35],a[8:11],v[184:187]\n"
"ds_read_b64_tr_b16 a[46:47],v105 offset:41984\n"
"v_accvgpr_read_b32 v123,a159\n"
"v_mfma_f32_16x16x32_bf16 v[216:219],v[32:35],a[16:19],v[216:219]\n"
"ds_read_b128 v[4:7],v96 offset:16384\n"
"v_cvt_pk_bf16_f32 v116,v116,v117\n"
"v_mfma_f32_16x16x32_bf16 v[216:219],v[36:39],a[20:23],v[216:219]\n"
"ds_read_b128 v[12:15],v96 offset:17408\n"
"v_cvt_pk_bf16_f32 v117,v118,v119\n"
"v_mfma_f32_16x16x32_bf16 v[248:251],v[36:39],a[28:31],v[248:251]\n"
"ds_read_b128 v[20:23],v96 offset:18432\n"
"v_cvt_pk_bf16_f32 v118,v120,v121\n"
"v_mfma_f32_16x16x32_bf16 v[248:251],v[32:35],a[24:27],v[248:251]\n"
"ds_read_b128 v[28:31],v96 offset:19456\n"
"v_cvt_pk_bf16_f32 v119,v122,v123\n"
"v_mfma_f32_16x16x32_bf16 v[252:255],v[40:43],a[24:27],v[252:255]\n"
"buffer_load_dwordx4 v112,s[24:27],s76 offen offset:1024 sc1 lds\n"
"buffer_store_dwordx4 v[116:119],v112,s[24:27],s56 offen offset:3072 sc1\n"
"v_mfma_f32_16x16x32_bf16 v[252:255],v[44:47],a[28:31],v[252:255]\n"
"s_nop 0\n"
"v_accvgpr_read_b32 v116,a160\n"
"v_mfma_f32_16x16x32_bf16 v[220:223],v[44:47],a[20:23],v[220:223]\n"
"v_accvgpr_read_b32 v117,a161\n"
"v_accvgpr_read_b32 v118,a162\n"
"v_mfma_f32_16x16x32_bf16 v[220:223],v[40:43],a[16:19],v[220:223]\n"
"buffer_load_dwordx4 v112,s[24:27],s76 offen offset:2048 sc1 lds\n"
"v_mfma_f32_16x16x32_bf16 v[188:191],v[40:43],a[8:11],v[188:191]\n"
"v_accvgpr_read_b32 v119,a163\n"
"v_accvgpr_read_b32 v120,a164\n"
"v_mfma_f32_16x16x32_bf16 v[188:191],v[44:47],a[12:15],v[188:191]\n"
"v_accvgpr_read_b32 v121,a165\n"
"v_accvgpr_read_b32 v122,a166\n"
"v_mfma_f32_16x16x32_bf16 v[156:159],v[44:47],a[4:7],v[156:159]\n"
"buffer_load_dwordx4 v112,s[24:27],s76 offen offset:3072 sc1 lds\n"
"v_mfma_f32_16x16x32_bf16 v[156:159],v[40:43],a[0:3],v[156:159]\n"
"s_xor_b32 s76,s76,49152\n"
"v_accvgpr_read_b32 v123,a167\n"
"s_waitcnt vmcnt(27) lgkmcnt(0)\n"
"s_barrier\n"
"v_mfma_f32_16x16x32_bf16 v[128:131],v[48:51],v[0:3],v[128:131]\n"
"ds_read_b64_tr_b16 v[32:33],v98 offset:0\n"
"ds_read_b64_tr_b16 v[40:41],v99 offset:0\n"
"v_mfma_f32_16x16x32_bf16 v[128:131],v[52:55],v[4:7],v[128:131]\n"
"ds_read_b64_tr_b16 v[34:35],v98 offset:1024\n"
"ds_read_b64_tr_b16 v[42:43],v99 offset:1024\n"
"v_mfma_f32_16x16x32_bf16 v[160:163],v[52:55],v[12:15],v[160:163]\n"
"ds_read_b64_tr_b16 v[36:37],v98 offset:8192\n"
"ds_read_b64_tr_b16 v[44:45],v99 offset:8192\n"
"v_mfma_f32_16x16x32_bf16 v[160:163],v[48:51],v[8:11],v[160:163]\n"
"s_add_u32 m0,s85,131072\n"
"ds_read_b64_tr_b16 v[38:39],v98 offset:9216\n"
"ds_read_b64_tr_b16 v[46:47],v99 offset:9216\n"
"v_mfma_f32_16x16x32_bf16 v[192:195],v[48:51],v[16:19],v[192:195]\n"
"buffer_load_dwordx4 v110,s[28:31],s86 offen lds\n"
"v_mfma_f32_16x16x32_bf16 v[192:195],v[52:55],v[20:23],v[192:195]\n"
"v_cvt_pk_bf16_f32 v116,v116,v117\n"
"v_cvt_pk_bf16_f32 v117,v118,v119\n"
"v_mfma_f32_16x16x32_bf16 v[224:227],v[52:55],v[28:31],v[224:227]\n"
"s_add_u32 s56,s56,4096\n"
"v_cvt_pk_bf16_f32 v118,v120,v121\n"
"v_cvt_pk_bf16_f32 v119,v122,v123\n"
"v_mfma_f32_16x16x32_bf16 v[224:227],v[48:51],v[24:27],v[224:227]\n"
"buffer_store_dwordx4 v[116:119],v112,s[24:27],s56 offen offset:0 sc1\n"
"v_mfma_f32_16x16x32_bf16 v[228:231],v[56:59],v[24:27],v[228:231]\n"
"ds_read_b64_tr_b16 v[48:49],v100 offset:0\n"
"v_accvgpr_read_b32 v116,a168\n"
"v_mfma_f32_16x16x32_bf16 v[228:231],v[60:63],v[28:31],v[228:231]\n"
"ds_read_b64_tr_b16 v[50:51],v100 offset:1024\n"
"v_accvgpr_read_b32 v117,a169\n"
"v_mfma_f32_16x16x32_bf16 v[196:199],v[60:63],v[20:23],v[196:199]\n"
"ds_read_b64_tr_b16 v[52:53],v100 offset:8192\n"
"v_accvgpr_read_b32 v118,a170\n"
"v_mfma_f32_16x16x32_bf16 v[196:199],v[56:59],v[16:19],v[196:199]\n"
"s_add_u32 m0,s85,132096\n"
"ds_read_b64_tr_b16 v[54:55],v100 offset:9216\n"
"v_mfma_f32_16x16x32_bf16 v[164:167],v[56:59],v[8:11],v[164:167]\n"
"buffer_load_dwordx4 v111,s[28:31],s86 offen lds\n"
"v_mfma_f32_16x16x32_bf16 v[164:167],v[60:63],v[12:15],v[164:167]\n"
"s_add_u32 m0,s85,139264\n"
"s_xor_b32 s86,s86,262144\n"
"v_mfma_f32_16x16x32_bf16 v[132:135],v[60:63],v[4:7],v[132:135]\n"
"buffer_load_dwordx4 v110,s[28:31],s86 offen lds\n"
"v_mfma_f32_16x16x32_bf16 v[132:135],v[56:59],v[0:3],v[132:135]\n"
"v_accvgpr_read_b32 v119,a171\n"
"v_accvgpr_read_b32 v120,a172\n"
"v_mfma_f32_16x16x32_bf16 v[136:139],v[64:67],v[0:3],v[136:139]\n"
"ds_read_b64_tr_b16 v[56:57],v101 offset:0\n"
"v_accvgpr_read_b32 v121,a173\n"
"v_mfma_f32_16x16x32_bf16 v[136:139],v[68:71],v[4:7],v[136:139]\n"
"ds_read_b64_tr_b16 v[58:59],v101 offset:1024\n"
"v_accvgpr_read_b32 v122,a174\n"
"v_mfma_f32_16x16x32_bf16 v[168:171],v[68:71],v[12:15],v[168:171]\n"
"ds_read_b64_tr_b16 v[60:61],v101 offset:8192\n"
"v_accvgpr_read_b32 v123,a175\n"
"v_mfma_f32_16x16x32_bf16 v[168:171],v[64:67],v[8:11],v[168:171]\n"
"s_add_u32 m0,s85,140288\n"
"ds_read_b64_tr_b16 v[62:63],v101 offset:9216\n"
"v_mfma_f32_16x16x32_bf16 v[200:203],v[64:67],v[16:19],v[200:203]\n"
"buffer_load_dwordx4 v111,s[28:31],s86 offen lds\n"
"v_mfma_f32_16x16x32_bf16 v[200:203],v[68:71],v[20:23],v[200:203]\n"
"s_xor_b32 s86,s86,8126464\n"
"v_cvt_pk_bf16_f32 v116,v116,v117\n"
"v_cvt_pk_bf16_f32 v117,v118,v119\n"
"v_mfma_f32_16x16x32_bf16 v[232:235],v[68:71],v[28:31],v[232:235]\n"
"v_cvt_pk_bf16_f32 v118,v120,v121\n"
"v_cvt_pk_bf16_f32 v119,v122,v123\n"
"v_mfma_f32_16x16x32_bf16 v[232:235],v[64:67],v[24:27],v[232:235]\n"
"buffer_store_dwordx4 v[116:119],v112,s[24:27],s56 offen offset:1024 sc1\n"
"v_mfma_f32_16x16x32_bf16 v[236:239],v[72:75],v[24:27],v[236:239]\n"
"ds_read_b64_tr_b16 v[64:65],v102 offset:0\n"
"v_accvgpr_read_b32 v116,a176\n"
"v_mfma_f32_16x16x32_bf16 v[236:239],v[76:79],v[28:31],v[236:239]\n"
"ds_read_b64_tr_b16 v[66:67],v102 offset:1024\n"
"v_accvgpr_read_b32 v117,a177\n"
"v_mfma_f32_16x16x32_bf16 v[204:207],v[76:79],v[20:23],v[204:207]\n"
"ds_read_b64_tr_b16 v[68:69],v102 offset:8192\n"
"v_accvgpr_read_b32 v118,a178\n"
"v_mfma_f32_16x16x32_bf16 v[204:207],v[72:75],v[16:19],v[204:207]\n"
"s_add_u32 m0,s78,0\n"
"ds_read_b64_tr_b16 v[70:71],v102 offset:9216\n"
"v_accvgpr_read_b32 v121,a181\n"
"v_mfma_f32_16x16x32_bf16 v[172:175],v[72:75],v[8:11],v[172:175]\n"
"buffer_load_dwordx4 v112,s[24:27],s76 offen offset:0 sc1 lds\n"
"v_mfma_f32_16x16x32_bf16 v[172:175],v[76:79],v[12:15],v[172:175]\n"
"v_accvgpr_read_b32 v119,a179\n"
"v_accvgpr_read_b32 v120,a180\n"
"v_mfma_f32_16x16x32_bf16 v[140:143],v[76:79],v[4:7],v[140:143]\n"
"buffer_load_dwordx4 v112,s[24:27],s76 offen offset:1024 sc1 lds\n"
"v_mfma_f32_16x16x32_bf16 v[140:143],v[72:75],v[0:3],v[140:143]\n"
"v_accvgpr_read_b32 v122,a182\n"
"s_waitcnt vmcnt(24)\n"
"v_mfma_f32_16x16x32_bf16 v[144:147],v[80:83],v[0:3],v[144:147]\n"
"ds_read_b64_tr_b16 v[72:73],v103 offset:0\n"
"v_accvgpr_read_b32 v123,a183\n"
"v_mfma_f32_16x16x32_bf16 v[144:147],v[84:87],v[4:7],v[144:147]\n"
"ds_read_b64_tr_b16 v[74:75],v103 offset:1024\n"
"v_cvt_pk_bf16_f32 v116,v116,v117\n"
"v_mfma_f32_16x16x32_bf16 v[176:179],v[84:87],v[12:15],v[176:179]\n"
"ds_read_b64_tr_b16 v[76:77],v103 offset:8192\n"
"v_cvt_pk_bf16_f32 v117,v118,v119\n"
"v_mfma_f32_16x16x32_bf16 v[176:179],v[80:83],v[8:11],v[176:179]\n"
"ds_read_b64_tr_b16 v[78:79],v103 offset:9216\n"
"v_cvt_pk_bf16_f32 v118,v120,v121\n"
"v_mfma_f32_16x16x32_bf16 v[208:211],v[80:83],v[16:19],v[208:211]\n"
"buffer_load_dwordx4 v112,s[24:27],s76 offen offset:2048 sc1 lds\n"
"v_mfma_f32_16x16x32_bf16 v[208:211],v[84:87],v[20:23],v[208:211]\n"
"ds_read_b128 a[0:3],v96 offset:32768\n"
"v_cvt_pk_bf16_f32 v119,v122,v123\n"
"v_mfma_f32_16x16x32_bf16 v[240:243],v[84:87],v[28:31],v[240:243]\n"
"buffer_store_dwordx4 v[116:119],v112,s[24:27],s56 offen offset:2048 sc1\n"
"v_mfma_f32_16x16x32_bf16 v[240:243],v[80:83],v[24:27],v[240:243]\n"
"ds_read_b128 a[8:11],v96 offset:33792\n"
"v_accvgpr_read_b32 v116,a184\n"
"v_accvgpr_read_b32 v117,a185\n"
"v_mfma_f32_16x16x32_bf16 v[244:247],v[88:91],v[24:27],v[244:247]\n"
"ds_read_b64_tr_b16 v[80:81],v104 offset:0\n"
"v_accvgpr_read_b32 v118,a186\n"
"v_mfma_f32_16x16x32_bf16 v[244:247],v[92:95],v[28:31],v[244:247]\n"
"ds_read_b64_tr_b16 v[82:83],v104 offset:1024\n"
"v_accvgpr_read_b32 v119,a187\n"
"v_mfma_f32_16x16x32_bf16 v[212:215],v[92:95],v[20:23],v[212:215]\n"
"ds_read_b64_tr_b16 v[84:85],v104 offset:8192\n"
"v_accvgpr_read_b32 v120,a188\n"
"v_mfma_f32_16x16x32_bf16 v[212:215],v[88:91],v[16:19],v[212:215]\n"
"ds_read_b64_tr_b16 v[86:87],v104 offset:9216\n"
"v_accvgpr_read_b32 v121,a189\n"
"v_mfma_f32_16x16x32_bf16 v[180:183],v[88:91],v[8:11],v[180:183]\n"
"buffer_load_dwordx4 v112,s[24:27],s76 offen offset:3072 sc1 lds\n"
"v_mfma_f32_16x16x32_bf16 v[180:183],v[92:95],v[12:15],v[180:183]\n"
"s_add_u32 m0,s78,16384\n"
"s_xor_b32 s76,s76,16384\n"
"ds_read_b128 a[16:19],v96 offset:34816\n"
"v_mfma_f32_16x16x32_bf16 v[148:151],v[92:95],v[4:7],v[148:151]\n"
"buffer_load_dwordx4 v112,s[24:27],s76 offen offset:0 sc1 lds\n"
"v_mfma_f32_16x16x32_bf16 v[148:151],v[88:91],v[0:3],v[148:151]\n"
"ds_read_b128 a[24:27],v96 offset:35840\n"
"v_accvgpr_read_b32 v122,a190\n"
"v_mfma_f32_16x16x32_bf16 v[152:155],a[32:35],v[0:3],v[152:155]\n"
"ds_read_b64_tr_b16 v[88:89],v105 offset:0\n"
"v_accvgpr_read_b32 v123,a191\n"
"v_mfma_f32_16x16x32_bf16 v[152:155],a[36:39],v[4:7],v[152:155]\n"
"ds_read_b64_tr_b16 v[90:91],v105 offset:1024\n"
"v_cvt_pk_bf16_f32 v116,v116,v117\n"
"v_mfma_f32_16x16x32_bf16 v[184:187],a[36:39],v[12:15],v[184:187]\n"
"ds_read_b64_tr_b16 v[92:93],v105 offset:8192\n"
"v_cvt_pk_bf16_f32 v117,v118,v119\n"
"v_mfma_f32_16x16x32_bf16 v[184:187],a[32:35],v[8:11],v[184:187]\n"
"ds_read_b64_tr_b16 v[94:95],v105 offset:9216\n"
"v_cvt_pk_bf16_f32 v118,v120,v121\n"
"v_mfma_f32_16x16x32_bf16 v[216:219],a[32:35],v[16:19],v[216:219]\n"
"ds_read_b128 a[4:7],v96 offset:49152\n"
"v_cvt_pk_bf16_f32 v119,v122,v123\n"
"v_mfma_f32_16x16x32_bf16 v[216:219],a[36:39],v[20:23],v[216:219]\n"
"ds_read_b128 a[12:15],v96 offset:50176\n"
"buffer_store_dwordx4 v[116:119],v112,s[24:27],s56 offen offset:3072 sc1\n"
"v_mfma_f32_16x16x32_bf16 v[248:251],a[36:39],v[28:31],v[248:251]\n"
"ds_read_b128 a[20:23],v96 offset:51200\n"
"v_accvgpr_read_b32 v116,a248\n"
"v_mfma_f32_16x16x32_bf16 v[248:251],a[32:35],v[24:27],v[248:251]\n"
"ds_read_b128 a[28:31],v96 offset:52224\n"
"v_accvgpr_read_b32 v117,a249\n"
"v_mfma_f32_16x16x32_bf16 v[252:255],a[40:43],v[24:27],v[252:255]\n"
"buffer_load_dwordx4 v112,s[24:27],s76 offen offset:1024 sc1 lds\n"
"v_mfma_f32_16x16x32_bf16 v[252:255],a[44:47],v[28:31],v[252:255]\n"
"v_accvgpr_read_b32 v118,a250\n"
"v_accvgpr_read_b32 v119,a251\n"
"v_mfma_f32_16x16x32_bf16 v[220:223],a[44:47],v[20:23],v[220:223]\n"
"v_accvgpr_read_b32 v120,a252\n"
"v_accvgpr_read_b32 v121,a253\n"
"v_mfma_f32_16x16x32_bf16 v[220:223],a[40:43],v[16:19],v[220:223]\n"
"buffer_load_dwordx4 v112,s[24:27],s76 offen offset:2048 sc1 lds\n"
"v_mfma_f32_16x16x32_bf16 v[188:191],a[40:43],v[8:11],v[188:191]\n"
"v_accvgpr_read_b32 v122,a254\n"
"v_accvgpr_read_b32 v123,a255\n"
"v_mfma_f32_16x16x32_bf16 v[188:191],a[44:47],v[12:15],v[188:191]\n"
"v_cvt_pk_bf16_f32 v116,v116,v117\n"
"v_cvt_pk_bf16_f32 v117,v118,v119\n"
"v_mfma_f32_16x16x32_bf16 v[156:159],a[44:47],v[4:7],v[156:159]\n"
"buffer_load_dwordx4 v112,s[24:27],s76 offen offset:3072 sc1 lds\n"
"v_mfma_f32_16x16x32_bf16 v[156:159],a[40:43],v[0:3],v[156:159]\n"
"s_xor_b32 s76,s76,507904\n"
"v_cvt_pk_bf16_f32 v118,v120,v121\n"
"s_waitcnt vmcnt(27) lgkmcnt(0)\n"
"s_barrier\n"
"v_mfma_f32_16x16x32_bf16 v[128:131],v[32:35],a[0:3],v[128:131]\n"
"ds_read_b64_tr_b16 a[32:33],v98 offset:49152\n"
"ds_read_b64_tr_b16 a[40:41],v99 offset:49152\n"
"v_mfma_f32_16x16x32_bf16 v[128:131],v[36:39],a[4:7],v[128:131]\n"
"ds_read_b64_tr_b16 a[34:35],v98 offset:50176\n"
"ds_read_b64_tr_b16 a[42:43],v99 offset:50176\n"
"v_mfma_f32_16x16x32_bf16 v[160:163],v[36:39],a[12:15],v[160:163]\n"
"ds_read_b64_tr_b16 a[36:37],v98 offset:57344\n"
"ds_read_b64_tr_b16 a[44:45],v99 offset:57344\n"
"v_mfma_f32_16x16x32_bf16 v[160:163],v[32:35],a[8:11],v[160:163]\n"
"s_add_u32 m0,s85,98304\n"
"ds_read_b64_tr_b16 a[38:39],v98 offset:58368\n"
"ds_read_b64_tr_b16 a[46:47],v99 offset:58368\n"
"v_mfma_f32_16x16x32_bf16 v[192:195],v[32:35],a[16:19],v[192:195]\n"
"buffer_load_dwordx4 v110,s[28:31],s86 offen lds\n"
"v_mfma_f32_16x16x32_bf16 v[192:195],v[36:39],a[20:23],v[192:195]\n"
"v_cvt_pk_bf16_f32 v119,v122,v123\n"
"v_accvgpr_write_b32 a252,v116\n"
"v_mfma_f32_16x16x32_bf16 v[224:227],v[36:39],a[28:31],v[224:227]\n"
"v_accvgpr_write_b32 a253,v117\n"
"v_accvgpr_write_b32 a254,v118\n"
"v_mfma_f32_16x16x32_bf16 v[224:227],v[32:35],a[24:27],v[224:227]\n"
"v_accvgpr_write_b32 a255,v119\n"
"v_accvgpr_read_b32 v116,a240\n"
"v_mfma_f32_16x16x32_bf16 v[228:231],v[40:43],a[24:27],v[228:231]\n"
"ds_read_b64_tr_b16 v[32:33],v100 offset:49152\n"
"v_accvgpr_read_b32 v117,a241\n"
"v_mfma_f32_16x16x32_bf16 v[228:231],v[44:47],a[28:31],v[228:231]\n"
"ds_read_b64_tr_b16 v[34:35],v100 offset:50176\n"
"v_accvgpr_read_b32 v118,a242\n"
"v_mfma_f32_16x16x32_bf16 v[196:199],v[44:47],a[20:23],v[196:199]\n"
"ds_read_b64_tr_b16 v[36:37],v100 offset:57344\n"
"v_accvgpr_read_b32 v119,a243\n"
"v_mfma_f32_16x16x32_bf16 v[196:199],v[40:43],a[16:19],v[196:199]\n"
"s_add_u32 m0,s85,99328\n"
"ds_read_b64_tr_b16 v[38:39],v100 offset:58368\n"
"v_mfma_f32_16x16x32_bf16 v[164:167],v[40:43],a[8:11],v[164:167]\n"
"buffer_load_dwordx4 v111,s[28:31],s86 offen lds\n"
"v_mfma_f32_16x16x32_bf16 v[164:167],v[44:47],a[12:15],v[164:167]\n"
"s_add_u32 m0,s85,106496\n"
"s_xor_b32 s86,s86,262144\n"
"v_mfma_f32_16x16x32_bf16 v[132:135],v[44:47],a[4:7],v[132:135]\n"
"buffer_load_dwordx4 v110,s[28:31],s86 offen lds\n"
"v_mfma_f32_16x16x32_bf16 v[132:135],v[40:43],a[0:3],v[132:135]\n"
"v_accvgpr_read_b32 v120,a244\n"
"v_accvgpr_read_b32 v121,a245\n"
"v_mfma_f32_16x16x32_bf16 v[136:139],v[48:51],a[0:3],v[136:139]\n"
"ds_read_b64_tr_b16 v[40:41],v101 offset:49152\n"
"v_accvgpr_read_b32 v122,a246\n"
"v_mfma_f32_16x16x32_bf16 v[136:139],v[52:55],a[4:7],v[136:139]\n"
"ds_read_b64_tr_b16 v[42:43],v101 offset:50176\n"
"v_accvgpr_read_b32 v123,a247\n"
"v_mfma_f32_16x16x32_bf16 v[168:171],v[52:55],a[12:15],v[168:171]\n"
"ds_read_b64_tr_b16 v[44:45],v101 offset:57344\n"
"v_cvt_pk_bf16_f32 v116,v116,v117\n"
"v_mfma_f32_16x16x32_bf16 v[168:171],v[48:51],a[8:11],v[168:171]\n"
"s_add_u32 m0,s85,107520\n"
"ds_read_b64_tr_b16 v[46:47],v101 offset:58368\n"
"v_mfma_f32_16x16x32_bf16 v[200:203],v[48:51],a[16:19],v[200:203]\n"
"buffer_load_dwordx4 v111,s[28:31],s86 offen lds\n"
"v_mfma_f32_16x16x32_bf16 v[200:203],v[52:55],a[20:23],v[200:203]\n"
"s_xor_b32 s86,s86,786432\n"
"v_cvt_pk_bf16_f32 v117,v118,v119\n"
"v_mfma_f32_16x16x32_bf16 v[232:235],v[52:55],a[28:31],v[232:235]\n"
"v_cvt_pk_bf16_f32 v118,v120,v121\n"
"v_cvt_pk_bf16_f32 v119,v122,v123\n"
"v_mfma_f32_16x16x32_bf16 v[232:235],v[48:51],a[24:27],v[232:235]\n"
"v_accvgpr_write_b32 a248,v116\n"
"v_accvgpr_write_b32 a249,v117\n"
"v_mfma_f32_16x16x32_bf16 v[236:239],v[56:59],a[24:27],v[236:239]\n"
"ds_read_b64_tr_b16 v[48:49],v102 offset:49152\n"
"v_accvgpr_write_b32 a250,v118\n"
"v_mfma_f32_16x16x32_bf16 v[236:239],v[60:63],a[28:31],v[236:239]\n"
"ds_read_b64_tr_b16 v[50:51],v102 offset:50176\n"
"v_accvgpr_write_b32 a251,v119\n"
"v_mfma_f32_16x16x32_bf16 v[204:207],v[60:63],a[20:23],v[204:207]\n"
"ds_read_b64_tr_b16 v[52:53],v102 offset:57344\n"
"v_accvgpr_read_b32 v116,a232\n"
"v_mfma_f32_16x16x32_bf16 v[204:207],v[56:59],a[16:19],v[204:207]\n"
"s_add_u32 m0,s78,32768\n"
"ds_read_b64_tr_b16 v[54:55],v102 offset:58368\n"
"v_accvgpr_read_b32 v119,a235\n"
"v_mfma_f32_16x16x32_bf16 v[172:175],v[56:59],a[8:11],v[172:175]\n"
"buffer_load_dwordx4 v112,s[24:27],s76 offen offset:0 sc1 lds\n"
"v_mfma_f32_16x16x32_bf16 v[172:175],v[60:63],a[12:15],v[172:175]\n"
"v_accvgpr_read_b32 v117,a233\n"
"v_accvgpr_read_b32 v118,a234\n"
"v_mfma_f32_16x16x32_bf16 v[140:143],v[60:63],a[4:7],v[140:143]\n"
"buffer_load_dwordx4 v112,s[24:27],s76 offen offset:1024 sc1 lds\n"
"v_mfma_f32_16x16x32_bf16 v[140:143],v[56:59],a[0:3],v[140:143]\n"
"v_accvgpr_read_b32 v120,a236\n"
"s_waitcnt vmcnt(22)\n"
"v_mfma_f32_16x16x32_bf16 v[144:147],v[64:67],a[0:3],v[144:147]\n"
"ds_read_b64_tr_b16 v[56:57],v103 offset:49152\n"
"v_accvgpr_read_b32 v121,a237\n"
"v_mfma_f32_16x16x32_bf16 v[144:147],v[68:71],a[4:7],v[144:147]\n"
"ds_read_b64_tr_b16 v[58:59],v103 offset:50176\n"
"v_accvgpr_read_b32 v122,a238\n"
"v_mfma_f32_16x16x32_bf16 v[176:179],v[68:71],a[12:15],v[176:179]\n"
"ds_read_b64_tr_b16 v[60:61],v103 offset:57344\n"
"v_accvgpr_read_b32 v123,a239\n"
"v_mfma_f32_16x16x32_bf16 v[176:179],v[64:67],a[8:11],v[176:179]\n"
"ds_read_b64_tr_b16 v[62:63],v103 offset:58368\n"
"v_cvt_pk_bf16_f32 v116,v116,v117\n"
"v_mfma_f32_16x16x32_bf16 v[208:211],v[64:67],a[16:19],v[208:211]\n"
"buffer_load_dwordx4 v112,s[24:27],s76 offen offset:2048 sc1 lds\n"
"v_mfma_f32_16x16x32_bf16 v[208:211],v[68:71],a[20:23],v[208:211]\n"
"ds_read_b128 v[0:3],v97 offset:0\n"
"v_cvt_pk_bf16_f32 v117,v118,v119\n"
"v_mfma_f32_16x16x32_bf16 v[240:243],v[68:71],a[28:31],v[240:243]\n"
"v_cvt_pk_bf16_f32 v118,v120,v121\n"
"v_cvt_pk_bf16_f32 v119,v122,v123\n"
"v_mfma_f32_16x16x32_bf16 v[240:243],v[64:67],a[24:27],v[240:243]\n"
"ds_read_b128 v[8:11],v97 offset:1024\n"
"v_accvgpr_write_b32 a244,v116\n"
"v_mfma_f32_16x16x32_bf16 v[244:247],v[72:75],a[24:27],v[244:247]\n"
"ds_read_b64_tr_b16 v[64:65],v104 offset:49152\n"
"v_accvgpr_write_b32 a245,v117\n"
"v_mfma_f32_16x16x32_bf16 v[244:247],v[76:79],a[28:31],v[244:247]\n"
"ds_read_b64_tr_b16 v[66:67],v104 offset:50176\n"
"v_accvgpr_write_b32 a246,v118\n"
"v_mfma_f32_16x16x32_bf16 v[212:215],v[76:79],a[20:23],v[212:215]\n"
"ds_read_b64_tr_b16 v[68:69],v104 offset:57344\n"
"v_accvgpr_write_b32 a247,v119\n"
"v_mfma_f32_16x16x32_bf16 v[212:215],v[72:75],a[16:19],v[212:215]\n"
"ds_read_b64_tr_b16 v[70:71],v104 offset:58368\n"
"v_accvgpr_read_b32 v116,a224\n"
"v_mfma_f32_16x16x32_bf16 v[180:183],v[72:75],a[8:11],v[180:183]\n"
"buffer_load_dwordx4 v112,s[24:27],s76 offen offset:3072 sc1 lds\n"
"v_mfma_f32_16x16x32_bf16 v[180:183],v[76:79],a[12:15],v[180:183]\n"
"s_add_u32 m0,s78,49152\n"
"s_xor_b32 s76,s76,16384\n"
"ds_read_b128 v[16:19],v97 offset:2048\n"
"v_mfma_f32_16x16x32_bf16 v[148:151],v[76:79],a[4:7],v[148:151]\n"
"buffer_load_dwordx4 v112,s[24:27],s76 offen offset:0 sc1 lds\n"
"v_mfma_f32_16x16x32_bf16 v[148:151],v[72:75],a[0:3],v[148:151]\n"
"ds_read_b128 v[24:27],v97 offset:3072\n"
"v_accvgpr_read_b32 v117,a225\n"
"v_mfma_f32_16x16x32_bf16 v[152:155],v[80:83],a[0:3],v[152:155]\n"
"ds_read_b64_tr_b16 v[72:73],v105 offset:49152\n"
"v_accvgpr_read_b32 v118,a226\n"
"v_mfma_f32_16x16x32_bf16 v[152:155],v[84:87],a[4:7],v[152:155]\n"
"ds_read_b64_tr_b16 v[74:75],v105 offset:50176\n"
"v_accvgpr_read_b32 v119,a227\n"
"v_mfma_f32_16x16x32_bf16 v[184:187],v[84:87],a[12:15],v[184:187]\n"
"ds_read_b64_tr_b16 v[76:77],v105 offset:57344\n"
"v_accvgpr_read_b32 v120,a228\n"
"v_mfma_f32_16x16x32_bf16 v[184:187],v[80:83],a[8:11],v[184:187]\n"
"ds_read_b64_tr_b16 v[78:79],v105 offset:58368\n"
"v_accvgpr_read_b32 v121,a229\n"
"v_mfma_f32_16x16x32_bf16 v[216:219],v[80:83],a[16:19],v[216:219]\n"
"ds_read_b128 v[4:7],v97 offset:16384\n"
"v_accvgpr_read_b32 v122,a230\n"
"v_mfma_f32_16x16x32_bf16 v[216:219],v[84:87],a[20:23],v[216:219]\n"
"ds_read_b128 v[12:15],v97 offset:17408\n"
"v_accvgpr_read_b32 v123,a231\n"
"v_mfma_f32_16x16x32_bf16 v[248:251],v[84:87],a[28:31],v[248:251]\n"
"ds_read_b128 v[20:23],v97 offset:18432\n"
"v_cvt_pk_bf16_f32 v116,v116,v117\n"
"v_mfma_f32_16x16x32_bf16 v[248:251],v[80:83],a[24:27],v[248:251]\n"
"ds_read_b128 v[28:31],v97 offset:19456\n"
"v_cvt_pk_bf16_f32 v117,v118,v119\n"
"v_mfma_f32_16x16x32_bf16 v[252:255],v[88:91],a[24:27],v[252:255]\n"
"buffer_load_dwordx4 v112,s[24:27],s76 offen offset:1024 sc1 lds\n"
"v_mfma_f32_16x16x32_bf16 v[252:255],v[92:95],a[28:31],v[252:255]\n"
"v_cvt_pk_bf16_f32 v118,v120,v121\n"
"v_cvt_pk_bf16_f32 v119,v122,v123\n"
"v_mfma_f32_16x16x32_bf16 v[220:223],v[92:95],a[20:23],v[220:223]\n"
"v_accvgpr_write_b32 a240,v116\n"
"v_accvgpr_write_b32 a241,v117\n"
"v_mfma_f32_16x16x32_bf16 v[220:223],v[88:91],a[16:19],v[220:223]\n"
"buffer_load_dwordx4 v112,s[24:27],s76 offen offset:2048 sc1 lds\n"
"v_mfma_f32_16x16x32_bf16 v[188:191],v[88:91],a[8:11],v[188:191]\n"
"v_accvgpr_write_b32 a242,v118\n"
"v_accvgpr_write_b32 a243,v119\n"
"v_mfma_f32_16x16x32_bf16 v[188:191],v[92:95],a[12:15],v[188:191]\n"
"v_accvgpr_read_b32 v116,a216\n"
"v_accvgpr_read_b32 v117,a217\n"
"v_mfma_f32_16x16x32_bf16 v[156:159],v[92:95],a[4:7],v[156:159]\n"
"buffer_load_dwordx4 v112,s[24:27],s76 offen offset:3072 sc1 lds\n"
"v_mfma_f32_16x16x32_bf16 v[156:159],v[88:91],a[0:3],v[156:159]\n"
"s_xor_b32 s76,s76,49152\n"
"v_accvgpr_read_b32 v118,a218\n"
"s_waitcnt vmcnt(23) lgkmcnt(0)\n"
"s_barrier\n"
"v_mfma_f32_16x16x32_bf16 v[128:131],a[32:35],v[0:3],v[128:131]\n"
"ds_read_b64_tr_b16 v[80:81],v98 offset:32768\n"
"ds_read_b64_tr_b16 v[88:89],v99 offset:32768\n"
"v_mfma_f32_16x16x32_bf16 v[128:131],a[36:39],v[4:7],v[128:131]\n"
"ds_read_b64_tr_b16 v[82:83],v98 offset:33792\n"
"ds_read_b64_tr_b16 v[90:91],v99 offset:33792\n"
"v_mfma_f32_16x16x32_bf16 v[160:163],a[36:39],v[12:15],v[160:163]\n"
"ds_read_b64_tr_b16 v[84:85],v98 offset:40960\n"
"ds_read_b64_tr_b16 v[92:93],v99 offset:40960\n"
"v_mfma_f32_16x16x32_bf16 v[160:163],a[32:35],v[8:11],v[160:163]\n"
"s_add_u32 m0,s85,147456\n"
"ds_read_b64_tr_b16 v[86:87],v98 offset:41984\n"
"ds_read_b64_tr_b16 v[94:95],v99 offset:41984\n"
"v_mfma_f32_16x16x32_bf16 v[192:195],a[32:35],v[16:19],v[192:195]\n"
"buffer_load_dwordx4 v110,s[28:31],s86 offen lds\n"
"v_mfma_f32_16x16x32_bf16 v[192:195],a[36:39],v[20:23],v[192:195]\n"
"v_accvgpr_read_b32 v119,a219\n"
"v_accvgpr_read_b32 v120,a220\n"
"v_mfma_f32_16x16x32_bf16 v[224:227],a[36:39],v[28:31],v[224:227]\n"
"v_accvgpr_read_b32 v121,a221\n"
"v_accvgpr_read_b32 v122,a222\n"
"v_mfma_f32_16x16x32_bf16 v[224:227],a[32:35],v[24:27],v[224:227]\n"
"v_accvgpr_read_b32 v123,a223\n"
"v_cvt_pk_bf16_f32 v116,v116,v117\n"
"v_mfma_f32_16x16x32_bf16 v[228:231],a[40:43],v[24:27],v[228:231]\n"
"ds_read_b64_tr_b16 a[32:33],v100 offset:32768\n"
"v_cvt_pk_bf16_f32 v117,v118,v119\n"
"v_mfma_f32_16x16x32_bf16 v[228:231],a[44:47],v[28:31],v[228:231]\n"
"ds_read_b64_tr_b16 a[34:35],v100 offset:33792\n"
"v_cvt_pk_bf16_f32 v118,v120,v121\n"
"v_mfma_f32_16x16x32_bf16 v[196:199],a[44:47],v[20:23],v[196:199]\n"
"ds_read_b64_tr_b16 a[36:37],v100 offset:40960\n"
"v_cvt_pk_bf16_f32 v119,v122,v123\n"
"v_mfma_f32_16x16x32_bf16 v[196:199],a[40:43],v[16:19],v[196:199]\n"
"s_add_u32 m0,s85,148480\n"
"ds_read_b64_tr_b16 a[38:39],v100 offset:41984\n"
"v_mfma_f32_16x16x32_bf16 v[164:167],a[40:43],v[8:11],v[164:167]\n"
"buffer_load_dwordx4 v111,s[28:31],s86 offen lds\n"
"v_mfma_f32_16x16x32_bf16 v[164:167],a[44:47],v[12:15],v[164:167]\n"
"s_add_u32 m0,s85,155648\n"
"s_xor_b32 s86,s86,262144\n"
"v_mfma_f32_16x16x32_bf16 v[132:135],a[44:47],v[4:7],v[132:135]\n"
"buffer_load_dwordx4 v110,s[28:31],s86 offen lds\n"
"v_mfma_f32_16x16x32_bf16 v[132:135],a[40:43],v[0:3],v[132:135]\n"
"v_accvgpr_write_b32 a236,v116\n"
"v_accvgpr_write_b32 a237,v117\n"
"v_mfma_f32_16x16x32_bf16 v[136:139],v[32:35],v[0:3],v[136:139]\n"
"ds_read_b64_tr_b16 a[40:41],v101 offset:32768\n"
"v_accvgpr_write_b32 a238,v118\n"
"v_mfma_f32_16x16x32_bf16 v[136:139],v[36:39],v[4:7],v[136:139]\n"
"ds_read_b64_tr_b16 a[42:43],v101 offset:33792\n"
"v_accvgpr_write_b32 a239,v119\n"
"v_mfma_f32_16x16x32_bf16 v[168:171],v[36:39],v[12:15],v[168:171]\n"
"ds_read_b64_tr_b16 a[44:45],v101 offset:40960\n"
"v_accvgpr_read_b32 v116,a208\n"
"v_mfma_f32_16x16x32_bf16 v[168:171],v[32:35],v[8:11],v[168:171]\n"
"s_add_u32 m0,s85,156672\n"
"ds_read_b64_tr_b16 a[46:47],v101 offset:41984\n"
"v_mfma_f32_16x16x32_bf16 v[200:203],v[32:35],v[16:19],v[200:203]\n"
"buffer_load_dwordx4 v111,s[28:31],s86 offen lds\n"
"v_mfma_f32_16x16x32_bf16 v[200:203],v[36:39],v[20:23],v[200:203]\n"
"s_xor_b32 s86,s86,1835008\n"
"v_accvgpr_read_b32 v117,a209\n"
"v_mfma_f32_16x16x32_bf16 v[232:235],v[36:39],v[28:31],v[232:235]\n"
"v_accvgpr_read_b32 v118,a210\n"
"v_accvgpr_read_b32 v119,a211\n"
"v_mfma_f32_16x16x32_bf16 v[232:235],v[32:35],v[24:27],v[232:235]\n"
"v_accvgpr_read_b32 v120,a212\n"
"v_accvgpr_read_b32 v121,a213\n"
"v_mfma_f32_16x16x32_bf16 v[236:239],v[40:43],v[24:27],v[236:239]\n"
"ds_read_b64_tr_b16 v[32:33],v102 offset:32768\n"
"v_accvgpr_read_b32 v122,a214\n"
"v_mfma_f32_16x16x32_bf16 v[236:239],v[44:47],v[28:31],v[236:239]\n"
"ds_read_b64_tr_b16 v[34:35],v102 offset:33792\n"
"v_accvgpr_read_b32 v123,a215\n"
"v_mfma_f32_16x16x32_bf16 v[204:207],v[44:47],v[20:23],v[204:207]\n"
"ds_read_b64_tr_b16 v[36:37],v102 offset:40960\n"
"v_cvt_pk_bf16_f32 v116,v116,v117\n"
"v_mfma_f32_16x16x32_bf16 v[204:207],v[40:43],v[16:19],v[204:207]\n"
"s_add_u32 m0,s78,65536\n"
"ds_read_b64_tr_b16 v[38:39],v102 offset:41984\n"
"v_accvgpr_write_b32 a232,v116\n"
"v_mfma_f32_16x16x32_bf16 v[172:175],v[40:43],v[8:11],v[172:175]\n"
"buffer_load_dwordx4 v112,s[24:27],s76 offen offset:0 sc1 lds\n"
"v_mfma_f32_16x16x32_bf16 v[172:175],v[44:47],v[12:15],v[172:175]\n"
"v_cvt_pk_bf16_f32 v117,v118,v119\n"
"v_cvt_pk_bf16_f32 v118,v120,v121\n"
"v_mfma_f32_16x16x32_bf16 v[140:143],v[44:47],v[4:7],v[140:143]\n"
"buffer_load_dwordx4 v112,s[24:27],s76 offen offset:1024 sc1 lds\n"
"v_mfma_f32_16x16x32_bf16 v[140:143],v[40:43],v[0:3],v[140:143]\n"
"v_cvt_pk_bf16_f32 v119,v122,v123\n"
"s_waitcnt vmcnt(18)\n"
"v_mfma_f32_16x16x32_bf16 v[144:147],v[48:51],v[0:3],v[144:147]\n"
"ds_read_b64_tr_b16 v[40:41],v103 offset:32768\n"
"v_accvgpr_write_b32 a233,v117\n"
"v_lshrrev_b32 v106,8,v112\n"
"v_mfma_f32_16x16x32_bf16 v[144:147],v[52:55],v[4:7],v[144:147]\n"
"ds_read_b64_tr_b16 v[42:43],v103 offset:33792\n"
"v_accvgpr_write_b32 a234,v118\n"
"v_lshlrev_b32 v107,6,v106\n"
"v_mfma_f32_16x16x32_bf16 v[176:179],v[52:55],v[12:15],v[176:179]\n"
"ds_read_b64_tr_b16 v[44:45],v103 offset:40960\n"
"v_accvgpr_write_b32 a235,v119\n"
"v_and_b32 v108,0xf0,v112\n"
"v_mfma_f32_16x16x32_bf16 v[176:179],v[48:51],v[8:11],v[176:179]\n"
"ds_read_b64_tr_b16 v[46:47],v103 offset:41984\n"
"v_accvgpr_read_b32 v116,a200\n"
"v_xor_b32 v107,v107,v108\n"
"v_mfma_f32_16x16x32_bf16 v[208:211],v[48:51],v[16:19],v[208:211]\n"
"buffer_load_dwordx4 v112,s[24:27],s76 offen offset:2048 sc1 lds\n"
"v_lshl_add_u32 v106,v106,11,v107\n"
"v_mfma_f32_16x16x32_bf16 v[208:211],v[52:55],v[20:23],v[208:211]\n"
"ds_read_b128 a[0:3],v96 offset:0\n"
"v_accvgpr_read_b32 v117,a201\n"
"v_lshrrev_b32 v107,6,v112\n"
"v_mfma_f32_16x16x32_bf16 v[240:243],v[52:55],v[28:31],v[240:243]\n"
"v_accvgpr_read_b32 v118,a202\n"
"v_accvgpr_read_b32 v119,a203\n"
"v_and_b32 v108,3,v107\n"
"v_mfma_f32_16x16x32_bf16 v[240:243],v[48:51],v[24:27],v[240:243]\n"
"ds_read_b128 a[8:11],v96 offset:1024\n"
"v_accvgpr_read_b32 v120,a204\n"
"v_lshlrev_b32 v108,6,v108\n"
"v_mfma_f32_16x16x32_bf16 v[244:247],v[56:59],v[24:27],v[244:247]\n"
"ds_read_b64_tr_b16 v[48:49],v104 offset:32768\n"
"v_accvgpr_read_b32 v121,a205\n"
"v_mfma_f32_16x16x32_bf16 v[244:247],v[60:63],v[28:31],v[244:247]\n"
"s_lshl_b32 s82,s52,6\n"
"ds_read_b64_tr_b16 v[50:51],v104 offset:33792\n"
"v_accvgpr_read_b32 v122,a206\n"
"v_xor_b32 v108,s82,v108\n"
"v_mfma_f32_16x16x32_bf16 v[212:215],v[60:63],v[20:23],v[212:215]\n"
"ds_read_b64_tr_b16 v[52:53],v104 offset:40960\n"
"v_accvgpr_read_b32 v123,a207\n"
"v_lshl_add_u32 v107,v107,8,v108\n"
"v_mfma_f32_16x16x32_bf16 v[212:215],v[56:59],v[16:19],v[212:215]\n"
"ds_read_b64_tr_b16 v[54:55],v104 offset:41984\n"
"v_cvt_pk_bf16_f32 v116,v116,v117\n"
"v_and_b32 v108,0x30,v112\n"
"v_mfma_f32_16x16x32_bf16 v[180:183],v[56:59],v[8:11],v[180:183]\n"
"buffer_load_dwordx4 v112,s[24:27],s76 offen offset:3072 sc1 lds\n"
"v_add_u32 v107,v107,v108\n"
"v_mfma_f32_16x16x32_bf16 v[180:183],v[60:63],v[12:15],v[180:183]\n"
"s_add_u32 m0,s78,81920\n"
"s_xor_b32 s76,s76,16384\n"
"ds_read_b128 a[16:19],v96 offset:2048\n"
"v_mfma_f32_16x16x32_bf16 v[148:151],v[60:63],v[4:7],v[148:151]\n"
"buffer_load_dwordx4 v112,s[24:27],s76 offen offset:0 sc1 lds\n"
"v_add_u32 v107,114688,v107\n"
"v_mfma_f32_16x16x32_bf16 v[148:151],v[56:59],v[0:3],v[148:151]\n"
"ds_read_b128 a[24:27],v96 offset:3072\n"
"v_cvt_pk_bf16_f32 v117,v118,v119\n"
"v_mfma_f32_16x16x32_bf16 v[152:155],v[64:67],v[0:3],v[152:155]\n"
"ds_read_b64_tr_b16 v[56:57],v105 offset:32768\n"
"v_cvt_pk_bf16_f32 v118,v120,v121\n"
"v_mfma_f32_16x16x32_bf16 v[152:155],v[68:71],v[4:7],v[152:155]\n"
"ds_read_b64_tr_b16 v[58:59],v105 offset:33792\n"
"v_cvt_pk_bf16_f32 v119,v122,v123\n"
"v_mfma_f32_16x16x32_bf16 v[184:187],v[68:71],v[12:15],v[184:187]\n"
"ds_read_b64_tr_b16 v[60:61],v105 offset:40960\n"
"v_accvgpr_write_b32 a228,v116\n"
"v_mfma_f32_16x16x32_bf16 v[184:187],v[64:67],v[8:11],v[184:187]\n"
"ds_read_b64_tr_b16 v[62:63],v105 offset:41984\n"
"v_accvgpr_write_b32 a229,v117\n"
"v_mfma_f32_16x16x32_bf16 v[216:219],v[64:67],v[16:19],v[216:219]\n"
"ds_read_b128 a[4:7],v96 offset:16384\n"
"v_accvgpr_write_b32 a230,v118\n"
"v_mfma_f32_16x16x32_bf16 v[216:219],v[68:71],v[20:23],v[216:219]\n"
"ds_read_b128 a[12:15],v96 offset:17408\n"
"v_accvgpr_write_b32 a231,v119\n"
"v_mfma_f32_16x16x32_bf16 v[248:251],v[68:71],v[28:31],v[248:251]\n"
"ds_read_b128 a[20:23],v96 offset:18432\n"
"v_accvgpr_read_b32 v116,a192\n"
"v_mfma_f32_16x16x32_bf16 v[248:251],v[64:67],v[24:27],v[248:251]\n"
"ds_read_b128 a[28:31],v96 offset:19456\n"
"v_accvgpr_read_b32 v117,a193\n"
"v_mfma_f32_16x16x32_bf16 v[252:255],v[72:75],v[24:27],v[252:255]\n"
"buffer_load_dwordx4 v112,s[24:27],s76 offen offset:1024 sc1 lds\n"
"v_mfma_f32_16x16x32_bf16 v[252:255],v[76:79],v[28:31],v[252:255]\n"
"v_accvgpr_read_b32 v118,a194\n"
"v_accvgpr_read_b32 v119,a195\n"
"v_mfma_f32_16x16x32_bf16 v[220:223],v[76:79],v[20:23],v[220:223]\n"
"v_accvgpr_read_b32 v120,a196\n"
"v_accvgpr_read_b32 v121,a197\n"
"v_mfma_f32_16x16x32_bf16 v[220:223],v[72:75],v[16:19],v[220:223]\n"
"buffer_load_dwordx4 v112,s[24:27],s76 offen offset:2048 sc1 lds\n"
"v_mfma_f32_16x16x32_bf16 v[188:191],v[72:75],v[8:11],v[188:191]\n"
"v_accvgpr_read_b32 v122,a198\n"
"v_accvgpr_read_b32 v123,a199\n"
"v_mfma_f32_16x16x32_bf16 v[188:191],v[76:79],v[12:15],v[188:191]\n"
"v_cvt_pk_bf16_f32 v116,v116,v117\n"
"v_cvt_pk_bf16_f32 v117,v118,v119\n"
"v_mfma_f32_16x16x32_bf16 v[156:159],v[76:79],v[4:7],v[156:159]\n"
"buffer_load_dwordx4 v112,s[24:27],s76 offen offset:3072 sc1 lds\n"
"v_mfma_f32_16x16x32_bf16 v[156:159],v[72:75],v[0:3],v[156:159]\n"
"s_xor_b32 s76,s76,114688\n"
"v_cvt_pk_bf16_f32 v118,v120,v121\n"
"s_waitcnt vmcnt(20) lgkmcnt(0)\n"
"s_barrier\n"
"v_mfma_f32_16x16x32_bf16 v[128:131],v[80:83],a[0:3],v[128:131]\n"
"ds_read_b64_tr_b16 v[64:65],v98 offset:0\n"
"ds_read_b64_tr_b16 v[72:73],v99 offset:0\n"
"v_mfma_f32_16x16x32_bf16 v[128:131],v[84:87],a[4:7],v[128:131]\n"
"ds_read_b64_tr_b16 v[66:67],v98 offset:1024\n"
"ds_read_b64_tr_b16 v[74:75],v99 offset:1024\n"
"v_mfma_f32_16x16x32_bf16 v[160:163],v[84:87],a[12:15],v[160:163]\n"
"ds_read_b64_tr_b16 v[68:69],v98 offset:8192\n"
"ds_read_b64_tr_b16 v[76:77],v99 offset:8192\n"
"v_mfma_f32_16x16x32_bf16 v[160:163],v[80:83],a[8:11],v[160:163]\n"
"s_add_u32 m0,s85,131072\n"
"ds_read_b64_tr_b16 v[70:71],v98 offset:9216\n"
"ds_read_b64_tr_b16 v[78:79],v99 offset:9216\n"
"v_mfma_f32_16x16x32_bf16 v[192:195],v[80:83],a[16:19],v[192:195]\n"
"buffer_load_dwordx4 v110,s[28:31],s86 offen lds\n"
"v_mfma_f32_16x16x32_bf16 v[192:195],v[84:87],a[20:23],v[192:195]\n"
"s_add_u32 m0,s85,132096\n"
"v_cvt_pk_bf16_f32 v119,v122,v123\n"
"v_mfma_f32_16x16x32_bf16 v[224:227],v[84:87],a[28:31],v[224:227]\n"
"buffer_load_dwordx4 v111,s[28:31],s86 offen lds\n"
"v_mfma_f32_16x16x32_bf16 v[224:227],v[80:83],a[24:27],v[224:227]\n"
"s_xor_b32 s86,s86,262144\n"
"v_accvgpr_write_b32 a224,v116\n"
"v_mfma_f32_16x16x32_bf16 v[228:231],v[88:91],a[24:27],v[228:231]\n"
"ds_read_b64_tr_b16 v[80:81],v100 offset:0\n"
"v_accvgpr_write_b32 a225,v117\n"
"v_mfma_f32_16x16x32_bf16 v[228:231],v[92:95],a[28:31],v[228:231]\n"
"ds_read_b64_tr_b16 v[82:83],v100 offset:1024\n"
"v_accvgpr_write_b32 a226,v118\n"
"v_mfma_f32_16x16x32_bf16 v[196:199],v[92:95],a[20:23],v[196:199]\n"
"ds_read_b64_tr_b16 v[84:85],v100 offset:8192\n"
"v_accvgpr_write_b32 a227,v119\n"
"v_mfma_f32_16x16x32_bf16 v[196:199],v[88:91],a[16:19],v[196:199]\n"
"s_add_u32 m0,s85,139264\n"
"ds_read_b64_tr_b16 v[86:87],v100 offset:9216\n"
"v_mfma_f32_16x16x32_bf16 v[164:167],v[88:91],a[8:11],v[164:167]\n"
"buffer_load_dwordx4 v110,s[28:31],s86 offen lds\n"
"v_mfma_f32_16x16x32_bf16 v[164:167],v[92:95],a[12:15],v[164:167]\n"
"s_add_u32 m0,s85,140288\n"
"s_add_u32 s56,s56,126976\n"
"v_mfma_f32_16x16x32_bf16 v[132:135],v[92:95],a[4:7],v[132:135]\n"
"buffer_load_dwordx4 v111,s[28:31],s86 offen lds\n"
"v_mfma_f32_16x16x32_bf16 v[132:135],v[88:91],a[0:3],v[132:135]\n"
"s_xor_b32 s86,s86,786432\n"
"v_mfma_f32_16x16x32_bf16 v[136:139],a[32:35],a[0:3],v[136:139]\n"
"ds_read_b64_tr_b16 v[88:89],v101 offset:0\n"
"v_mfma_f32_16x16x32_bf16 v[136:139],a[36:39],a[4:7],v[136:139]\n"
"ds_read_b64_tr_b16 v[90:91],v101 offset:1024\n"
"v_mfma_f32_16x16x32_bf16 v[168:171],a[36:39],a[12:15],v[168:171]\n"
"ds_read_b64_tr_b16 v[92:93],v101 offset:8192\n"
"v_mfma_f32_16x16x32_bf16 v[168:171],a[32:35],a[8:11],v[168:171]\n"
"s_add_u32 m0,s78,0\n"
"ds_read_b64_tr_b16 v[94:95],v101 offset:9216\n"
"v_mfma_f32_16x16x32_bf16 v[200:203],a[32:35],a[16:19],v[200:203]\n"
"buffer_load_dwordx4 v112,s[24:27],s76 offen offset:0 sc1 lds\n"
"v_mfma_f32_16x16x32_bf16 v[200:203],a[36:39],a[20:23],v[200:203]\n"
"v_mfma_f32_16x16x32_bf16 v[232:235],a[36:39],a[28:31],v[232:235]\n"
"buffer_load_dwordx4 v112,s[24:27],s76 offen offset:1024 sc1 lds\n"
"v_mfma_f32_16x16x32_bf16 v[232:235],a[32:35],a[24:27],v[232:235]\n"
"v_mfma_f32_16x16x32_bf16 v[236:239],a[40:43],a[24:27],v[236:239]\n"
"ds_read_b64_tr_b16 a[32:33],v102 offset:0\n"
"v_mfma_f32_16x16x32_bf16 v[236:239],a[44:47],a[28:31],v[236:239]\n"
"ds_read_b64_tr_b16 a[34:35],v102 offset:1024\n"
"v_mfma_f32_16x16x32_bf16 v[204:207],a[44:47],a[20:23],v[204:207]\n"
"ds_read_b64_tr_b16 a[36:37],v102 offset:8192\n"
"v_mfma_f32_16x16x32_bf16 v[204:207],a[40:43],a[16:19],v[204:207]\n"
"ds_read_b64_tr_b16 a[38:39],v102 offset:9216\n"
"v_mfma_f32_16x16x32_bf16 v[172:175],a[40:43],a[8:11],v[172:175]\n"
"buffer_load_dwordx4 v112,s[24:27],s76 offen offset:2048 sc1 lds\n"
"v_mfma_f32_16x16x32_bf16 v[172:175],a[44:47],a[12:15],v[172:175]\n"
"v_mfma_f32_16x16x32_bf16 v[140:143],a[44:47],a[4:7],v[140:143]\n"
"buffer_load_dwordx4 v112,s[24:27],s76 offen offset:3072 sc1 lds\n"
"v_mfma_f32_16x16x32_bf16 v[140:143],a[40:43],a[0:3],v[140:143]\n"
"s_xor_b32 s76,s76,16384\n"
"s_waitcnt vmcnt(20)\n"
"v_mfma_f32_16x16x32_bf16 v[144:147],v[32:35],a[0:3],v[144:147]\n"
"ds_read_b64_tr_b16 a[40:41],v103 offset:0\n"
"v_mfma_f32_16x16x32_bf16 v[144:147],v[36:39],a[4:7],v[144:147]\n"
"ds_read_b64_tr_b16 a[42:43],v103 offset:1024\n"
"v_mfma_f32_16x16x32_bf16 v[176:179],v[36:39],a[12:15],v[176:179]\n"
"ds_read_b64_tr_b16 a[44:45],v103 offset:8192\n"
"v_mfma_f32_16x16x32_bf16 v[176:179],v[32:35],a[8:11],v[176:179]\n"
"s_add_u32 m0,s78,16384\n"
"ds_read_b64_tr_b16 a[46:47],v103 offset:9216\n"
"v_mfma_f32_16x16x32_bf16 v[208:211],v[32:35],a[16:19],v[208:211]\n"
"buffer_load_dwordx4 v112,s[24:27],s76 offen offset:0 sc1 lds\n"
"v_mfma_f32_16x16x32_bf16 v[208:211],v[36:39],a[20:23],v[208:211]\n"
"ds_read_b128 v[0:3],v96 offset:32768\n"
"v_mfma_f32_16x16x32_bf16 v[240:243],v[36:39],a[28:31],v[240:243]\n"
"buffer_load_dwordx4 v112,s[24:27],s76 offen offset:1024 sc1 lds\n"
"v_mfma_f32_16x16x32_bf16 v[240:243],v[32:35],a[24:27],v[240:243]\n"
"ds_read_b128 v[8:11],v96 offset:33792\n"
"v_mfma_f32_16x16x32_bf16 v[244:247],v[40:43],a[24:27],v[244:247]\n"
"ds_read_b64_tr_b16 v[32:33],v104 offset:0\n"
"v_mfma_f32_16x16x32_bf16 v[244:247],v[44:47],a[28:31],v[244:247]\n"
"ds_read_b64_tr_b16 v[34:35],v104 offset:1024\n"
"v_mfma_f32_16x16x32_bf16 v[212:215],v[44:47],a[20:23],v[212:215]\n"
"ds_read_b64_tr_b16 v[36:37],v104 offset:8192\n"
"v_mfma_f32_16x16x32_bf16 v[212:215],v[40:43],a[16:19],v[212:215]\n"
"ds_read_b64_tr_b16 v[38:39],v104 offset:9216\n"
"v_mfma_f32_16x16x32_bf16 v[180:183],v[40:43],a[8:11],v[180:183]\n"
"buffer_load_dwordx4 v112,s[24:27],s76 offen offset:2048 sc1 lds\n"
"v_mfma_f32_16x16x32_bf16 v[180:183],v[44:47],a[12:15],v[180:183]\n"
"ds_read_b128 v[16:19],v96 offset:34816\n"
"v_mfma_f32_16x16x32_bf16 v[148:151],v[44:47],a[4:7],v[148:151]\n"
"buffer_load_dwordx4 v112,s[24:27],s76 offen offset:3072 sc1 lds\n"
"v_mfma_f32_16x16x32_bf16 v[148:151],v[40:43],a[0:3],v[148:151]\n"
"s_xor_b32 s76,s76,49152\n"
"ds_read_b128 v[24:27],v96 offset:35840\n"
"v_mfma_f32_16x16x32_bf16 v[152:155],v[48:51],a[0:3],v[152:155]\n"
"ds_read_b64_tr_b16 v[40:41],v105 offset:0\n"
"v_mfma_f32_16x16x32_bf16 v[152:155],v[52:55],a[4:7],v[152:155]\n"
"ds_read_b64_tr_b16 v[42:43],v105 offset:1024\n"
"v_mfma_f32_16x16x32_bf16 v[184:187],v[52:55],a[12:15],v[184:187]\n"
"ds_read_b64_tr_b16 v[44:45],v105 offset:8192\n"
"v_mfma_f32_16x16x32_bf16 v[184:187],v[48:51],a[8:11],v[184:187]\n"
"ds_read_b64_tr_b16 v[46:47],v105 offset:9216\n"
"v_mfma_f32_16x16x32_bf16 v[216:219],v[48:51],a[16:19],v[216:219]\n"
"ds_read_b128 v[4:7],v96 offset:49152\n"
"v_mfma_f32_16x16x32_bf16 v[216:219],v[52:55],a[20:23],v[216:219]\n"
"ds_read_b128 v[12:15],v96 offset:50176\n"
"ds_read_b128 v[20:23],v96 offset:51200\n"
"v_mfma_f32_16x16x32_bf16 v[248:251],v[52:55],a[28:31],v[248:251]\n"
"buffer_store_dwordx2 v[124:125],off,s[24:27],s59 sc1\n"
"v_mfma_f32_16x16x32_bf16 v[248:251],v[48:51],a[24:27],v[248:251]\n"
"s_add_u32 m0,s78,114688\n"
"ds_read_b128 v[28:31],v96 offset:52224\n"
"v_mfma_f32_16x16x32_bf16 v[252:255],v[56:59],a[24:27],v[252:255]\n"
"buffer_load_dwordx4 v106,s[4:7],s13 offen offset:0 nt lds\n"
"v_mfma_f32_16x16x32_bf16 v[252:255],v[60:63],a[28:31],v[252:255]\n"
"s_add_u32 s13,s13,7168\n"
"v_mfma_f32_16x16x32_bf16 v[220:223],v[60:63],a[20:23],v[220:223]\n"
"buffer_load_dwordx4 v106,s[4:7],s13 offen offset:1024 nt lds\n"
"v_mfma_f32_16x16x32_bf16 v[220:223],v[56:59],a[16:19],v[220:223]\n"
"s_add_u32 s13,s13,7168\n"
"v_mfma_f32_16x16x32_bf16 v[188:191],v[56:59],a[8:11],v[188:191]\n"
"buffer_load_dwordx4 v106,s[4:7],s13 offen offset:2048 nt lds\n"
"v_mfma_f32_16x16x32_bf16 v[188:191],v[60:63],a[12:15],v[188:191]\n"
"s_add_u32 s13,s13,7168\n"
"v_mfma_f32_16x16x32_bf16 v[156:159],v[60:63],a[4:7],v[156:159]\n"
"buffer_load_dwordx4 v106,s[4:7],s13 offen offset:3072 nt lds\n"
"v_mfma_f32_16x16x32_bf16 v[156:159],v[56:59],a[0:3],v[156:159]\n"
"s_add_u32 s13,s13,109568\n"
"s_waitcnt vmcnt(25) lgkmcnt(0)\n"
"s_barrier\n"
"v_mfma_f32_16x16x32_bf16 v[128:131],v[64:67],v[0:3],v[128:131]\n"
"ds_read_b64_tr_b16 v[48:49],v98 offset:49152\n"
"ds_read_b64_tr_b16 v[56:57],v99 offset:49152\n"
"v_mfma_f32_16x16x32_bf16 v[128:131],v[68:71],v[4:7],v[128:131]\n"
"ds_read_b64_tr_b16 v[50:51],v98 offset:50176\n"
"ds_read_b64_tr_b16 v[58:59],v99 offset:50176\n"
"v_mfma_f32_16x16x32_bf16 v[160:163],v[68:71],v[12:15],v[160:163]\n"
"ds_read_b64_tr_b16 v[52:53],v98 offset:57344\n"
"ds_read_b64_tr_b16 v[60:61],v99 offset:57344\n"
"v_mfma_f32_16x16x32_bf16 v[160:163],v[64:67],v[8:11],v[160:163]\n"
"s_add_u32 m0,s85,98304\n"
"ds_read_b64_tr_b16 v[54:55],v98 offset:58368\n"
"ds_read_b64_tr_b16 v[62:63],v99 offset:58368\n"
"v_mfma_f32_16x16x32_bf16 v[192:195],v[64:67],v[16:19],v[192:195]\n"
"buffer_load_dwordx4 v110,s[28:31],s86 offen lds\n"
"v_mfma_f32_16x16x32_bf16 v[192:195],v[68:71],v[20:23],v[192:195]\n"
"v_mfma_f32_16x16x32_bf16 v[224:227],v[68:71],v[28:31],v[224:227]\n"
"v_mfma_f32_16x16x32_bf16 v[224:227],v[64:67],v[24:27],v[224:227]\n"
"v_mfma_f32_16x16x32_bf16 v[228:231],v[72:75],v[24:27],v[228:231]\n"
"ds_read_b64_tr_b16 v[64:65],v100 offset:49152\n"
"v_mfma_f32_16x16x32_bf16 v[228:231],v[76:79],v[28:31],v[228:231]\n"
"ds_read_b64_tr_b16 v[66:67],v100 offset:50176\n"
"v_mfma_f32_16x16x32_bf16 v[196:199],v[76:79],v[20:23],v[196:199]\n"
"ds_read_b64_tr_b16 v[68:69],v100 offset:57344\n"
"v_mfma_f32_16x16x32_bf16 v[196:199],v[72:75],v[16:19],v[196:199]\n"
"s_add_u32 m0,s85,99328\n"
"ds_read_b64_tr_b16 v[70:71],v100 offset:58368\n"
"v_mfma_f32_16x16x32_bf16 v[164:167],v[72:75],v[8:11],v[164:167]\n"
"buffer_load_dwordx4 v111,s[28:31],s86 offen lds\n"
"v_mfma_f32_16x16x32_bf16 v[164:167],v[76:79],v[12:15],v[164:167]\n"
"s_add_u32 m0,s85,106496\n"
"s_xor_b32 s86,s86,262144\n"
"v_mfma_f32_16x16x32_bf16 v[132:135],v[76:79],v[4:7],v[132:135]\n"
"buffer_load_dwordx4 v110,s[28:31],s86 offen lds\n"
"v_mfma_f32_16x16x32_bf16 v[132:135],v[72:75],v[0:3],v[132:135]\n"
"v_mfma_f32_16x16x32_bf16 v[136:139],v[80:83],v[0:3],v[136:139]\n"
"ds_read_b64_tr_b16 v[72:73],v101 offset:49152\n"
"v_mfma_f32_16x16x32_bf16 v[136:139],v[84:87],v[4:7],v[136:139]\n"
"ds_read_b64_tr_b16 v[74:75],v101 offset:50176\n"
"v_mfma_f32_16x16x32_bf16 v[168:171],v[84:87],v[12:15],v[168:171]\n"
"ds_read_b64_tr_b16 v[76:77],v101 offset:57344\n"
"v_mfma_f32_16x16x32_bf16 v[168:171],v[80:83],v[8:11],v[168:171]\n"
"s_add_u32 m0,s85,107520\n"
"ds_read_b64_tr_b16 v[78:79],v101 offset:58368\n"
"v_mfma_f32_16x16x32_bf16 v[200:203],v[80:83],v[16:19],v[200:203]\n"
"buffer_load_dwordx4 v111,s[28:31],s86 offen lds\n"
"v_mfma_f32_16x16x32_bf16 v[200:203],v[84:87],v[20:23],v[200:203]\n"
"s_xor_b32 s86,s86,3932160\n"
"v_mfma_f32_16x16x32_bf16 v[232:235],v[84:87],v[28:31],v[232:235]\n"
"v_mfma_f32_16x16x32_bf16 v[232:235],v[80:83],v[24:27],v[232:235]\n"
"v_mfma_f32_16x16x32_bf16 v[236:239],v[88:91],v[24:27],v[236:239]\n"
"ds_read_b64_tr_b16 v[80:81],v102 offset:49152\n"
"v_mfma_f32_16x16x32_bf16 v[236:239],v[92:95],v[28:31],v[236:239]\n"
"ds_read_b64_tr_b16 v[82:83],v102 offset:50176\n"
"v_mfma_f32_16x16x32_bf16 v[204:207],v[92:95],v[20:23],v[204:207]\n"
"ds_read_b64_tr_b16 v[84:85],v102 offset:57344\n"
"v_mfma_f32_16x16x32_bf16 v[204:207],v[88:91],v[16:19],v[204:207]\n"
"s_add_u32 m0,s78,32768\n"
"ds_read_b64_tr_b16 v[86:87],v102 offset:58368\n"
"v_mfma_f32_16x16x32_bf16 v[172:175],v[88:91],v[8:11],v[172:175]\n"
"buffer_load_dwordx4 v112,s[24:27],s76 offen offset:0 sc1 lds\n"
"v_mfma_f32_16x16x32_bf16 v[172:175],v[92:95],v[12:15],v[172:175]\n"
"v_mfma_f32_16x16x32_bf16 v[140:143],v[92:95],v[4:7],v[140:143]\n"
"buffer_load_dwordx4 v112,s[24:27],s76 offen offset:1024 sc1 lds\n"
"v_mfma_f32_16x16x32_bf16 v[140:143],v[88:91],v[0:3],v[140:143]\n"
"s_waitcnt vmcnt(23)\n"
"ds_read_b64_tr_b16 v[88:89],v103 offset:49152\n"
"ds_read_b64_tr_b16 v[90:91],v103 offset:50176\n"
"v_mfma_f32_16x16x32_bf16 v[144:147],a[32:35],v[0:3],v[144:147]\n"
"v_lshrrev_b32 v120,4,v112\n"
"v_and_b32 v121,15,v120\n"
"v_mfma_f32_16x16x32_bf16 v[144:147],a[36:39],v[4:7],v[144:147]\n"
"v_lshrrev_b32 v122,4,v120\n"
"v_lshlrev_b32 v122,2,v122\n"
"v_mfma_f32_16x16x32_bf16 v[176:179],a[36:39],v[12:15],v[176:179]\n"
"ds_read_b64_tr_b16 v[92:93],v103 offset:57344\n"
"v_sub_u32 v121,v121,v122\n"
"v_mfma_f32_16x16x32_bf16 v[176:179],a[32:35],v[8:11],v[176:179]\n"
"s_and_b32 s82,s52,1\n"
"ds_read_b64_tr_b16 v[94:95],v103 offset:58368\n"
"s_lshl_b32 s82,s82,4\n"
"v_add_u32 v121,s82,v121\n"
"v_mfma_f32_16x16x32_bf16 v[208:211],a[32:35],v[16:19],v[208:211]\n"
"buffer_load_dwordx4 v112,s[24:27],s76 offen offset:2048 sc1 lds\n"
"v_cvt_f32_i32 v121,v121\n"
"v_mfma_f32_16x16x32_bf16 v[208:211],a[36:39],v[20:23],v[208:211]\n"
"ds_read_b128 a[96:99],v97 offset:0\n"
"v_add_f32 v121,0.5,v121\n"
"v_mul_f32 v126,0x7149f2ca,v121\n"
"v_mfma_f32_16x16x32_bf16 v[240:243],a[36:39],v[28:31],v[240:243]\n"
"v_and_b32 v120,31,v120\n"
"v_mov_b32 v121,0x3f803f80\n"
"v_mfma_f32_16x16x32_bf16 v[240:243],a[32:35],v[24:27],v[240:243]\n"
"s_mov_b32 s93,0x3f800000\n"
"ds_read_b128 a[104:107],v97 offset:1024\n"
"v_accvgpr_write_b32 a144,v121\n"
"v_mfma_f32_16x16x32_bf16 v[244:247],a[40:43],v[24:27],v[244:247]\n"
"ds_read_b64_tr_b16 a[128:129],v104 offset:49152\n"
"v_accvgpr_write_b32 a146,v121\n"
"v_mfma_f32_16x16x32_bf16 v[244:247],a[44:47],v[28:31],v[244:247]\n"
"ds_read_b64_tr_b16 a[130:131],v104 offset:50176\n"
"v_accvgpr_write_b32 a147,v121\n"
"v_mfma_f32_16x16x32_bf16 v[212:215],a[44:47],v[20:23],v[212:215]\n"
"ds_read_b64_tr_b16 a[132:133],v104 offset:57344\n"
"v_lshlrev_b32 v120,3,v120\n"
"v_mfma_f32_16x16x32_bf16 v[212:215],a[40:43],v[16:19],v[212:215]\n"
"ds_read_b64_tr_b16 a[134:135],v104 offset:58368\n"
"v_accvgpr_write_b32 a145,v121\n"
"v_mfma_f32_16x16x32_bf16 v[180:183],a[40:43],v[8:11],v[180:183]\n"
"buffer_load_dwordx4 v112,s[24:27],s76 offen offset:3072 sc1 lds\n"
"v_mfma_f32_16x16x32_bf16 v[180:183],a[44:47],v[12:15],v[180:183]\n"
"s_add_u32 m0,s78,49152\n"
"s_xor_b32 s76,s76,16384\n"
"ds_read_b128 a[112:115],v97 offset:2048\n"
"v_mfma_f32_16x16x32_bf16 v[148:151],a[44:47],v[4:7],v[148:151]\n"
"buffer_load_dwordx4 v112,s[24:27],s76 offen offset:0 sc1 lds\n"
"v_mfma_f32_16x16x32_bf16 v[148:151],a[40:43],v[0:3],v[148:151]\n"
"ds_read_b128 a[120:123],v97 offset:3072\n"
"v_mfma_f32_16x16x32_bf16 v[152:155],v[32:35],v[0:3],v[152:155]\n"
"ds_read_b64_tr_b16 a[136:137],v105 offset:49152\n"
"v_mfma_f32_16x16x32_bf16 v[152:155],v[36:39],v[4:7],v[152:155]\n"
"ds_read_b64_tr_b16 a[138:139],v105 offset:50176\n"
"v_mfma_f32_16x16x32_bf16 v[184:187],v[36:39],v[12:15],v[184:187]\n"
"ds_read_b64_tr_b16 a[140:141],v105 offset:57344\n"
"v_mfma_f32_16x16x32_bf16 v[184:187],v[32:35],v[8:11],v[184:187]\n"
"ds_read_b64_tr_b16 a[142:143],v105 offset:58368\n"
"v_mfma_f32_16x16x32_bf16 v[216:219],v[32:35],v[16:19],v[216:219]\n"
"ds_read_b128 a[100:103],v97 offset:16384\n"
"v_mfma_f32_16x16x32_bf16 v[216:219],v[36:39],v[20:23],v[216:219]\n"
"ds_read_b128 a[108:111],v97 offset:17408\n"
"v_mfma_f32_16x16x32_bf16 v[248:251],v[36:39],v[28:31],v[248:251]\n"
"ds_read_b128 a[116:119],v97 offset:18432\n"
"v_mfma_f32_16x16x32_bf16 v[248:251],v[32:35],v[24:27],v[248:251]\n"
"ds_read_b128 a[124:127],v97 offset:19456\n"
"v_mfma_f32_16x16x32_bf16 v[252:255],v[40:43],v[24:27],v[252:255]\n"
"buffer_load_dwordx4 v112,s[24:27],s76 offen offset:1024 sc1 lds\n"
"v_mfma_f32_16x16x32_bf16 v[252:255],v[44:47],v[28:31],v[252:255]\n"
"v_mfma_f32_16x16x32_bf16 v[220:223],v[44:47],v[20:23],v[220:223]\n"
"v_mfma_f32_16x16x32_bf16 v[220:223],v[40:43],v[16:19],v[220:223]\n"
"buffer_load_dwordx4 v112,s[24:27],s76 offen offset:2048 sc1 lds\n"
"v_mfma_f32_16x16x32_bf16 v[188:191],v[40:43],v[8:11],v[188:191]\n"
"v_mfma_f32_16x16x32_bf16 v[188:191],v[44:47],v[12:15],v[188:191]\n"
"v_mfma_f32_16x16x32_bf16 v[156:159],v[44:47],v[4:7],v[156:159]\n"
"buffer_load_dwordx4 v112,s[24:27],s76 offen offset:3072 sc1 lds\n"
"v_mfma_f32_16x16x32_bf16 v[156:159],v[40:43],v[0:3],v[156:159]\n"
"s_xor_b32 s76,s76,245760\n"
"s_waitcnt vmcnt(12) lgkmcnt(0)\n"
"s_barrier\n"
"v_mfma_f32_16x16x32_bf16 v[128:131],v[48:51],a[96:99],v[128:131]\n"
"ds_read_b64_tr_b16 v[32:33],v98 offset:32768\n"
"ds_read_b64_tr_b16 v[40:41],v99 offset:32768\n"
"v_mfma_f32_16x16x32_bf16 v[128:131],v[52:55],a[100:103],v[128:131]\n"
"ds_read_b64_tr_b16 v[34:35],v98 offset:33792\n"
"ds_read_b64_tr_b16 v[42:43],v99 offset:33792\n"
"v_mfma_f32_16x16x32_bf16 v[160:163],v[52:55],a[108:111],v[160:163]\n"
"ds_read_b64_tr_b16 v[36:37],v98 offset:40960\n"
"ds_read_b64_tr_b16 v[44:45],v99 offset:40960\n"
"v_mfma_f32_16x16x32_bf16 v[160:163],v[48:51],a[104:107],v[160:163]\n"
"s_add_u32 m0,s85,147456\n"
"ds_read_b64_tr_b16 v[38:39],v98 offset:41984\n"
"ds_read_b64_tr_b16 v[46:47],v99 offset:41984\n"
"v_mfma_f32_16x16x32_bf16 v[192:195],v[48:51],a[112:115],v[192:195]\n"
"buffer_load_dwordx4 v110,s[28:31],s86 offen lds\n"
"ds_read_b64_tr_b16 a[160:161],v107 offset:0\n"
"v_mfma_f32_16x16x32_bf16 v[192:195],v[52:55],a[116:119],v[192:195]\n"
"ds_read_b64_tr_b16 a[162:163],v107 offset:4096\n"
"ds_read_b64_tr_b16 a[164:165],v107 offset:8\n"
"ds_read_b64_tr_b16 a[166:167],v107 offset:4104\n"
"v_mfma_f32_16x16x32_bf16 v[224:227],v[52:55],a[124:127],v[224:227]\n"
"ds_read_b64_tr_b16 a[168:169],v107 offset:8192\n"
"ds_read_b64_tr_b16 a[170:171],v107 offset:12288\n"
"v_mfma_f32_16x16x32_bf16 v[224:227],v[48:51],a[120:123],v[224:227]\n"
"ds_read_b64_tr_b16 a[172:173],v107 offset:8200\n"
"ds_read_b64_tr_b16 a[174:175],v107 offset:12296\n"
"v_mfma_f32_16x16x32_bf16 v[228:231],v[56:59],a[120:123],v[228:231]\n"
"ds_read_b64_tr_b16 v[48:49],v100 offset:32768\n"
"v_mfma_f32_16x16x32_bf16 v[228:231],v[60:63],a[124:127],v[228:231]\n"
"ds_read_b64_tr_b16 v[50:51],v100 offset:33792\n"
"v_mfma_f32_16x16x32_bf16 v[196:199],v[60:63],a[116:119],v[196:199]\n"
"ds_read_b64_tr_b16 v[52:53],v100 offset:40960\n"
"v_mfma_f32_16x16x32_bf16 v[196:199],v[56:59],a[112:115],v[196:199]\n"
"s_add_u32 m0,s85,148480\n"
"ds_read_b64_tr_b16 v[54:55],v100 offset:41984\n"
"v_mfma_f32_16x16x32_bf16 v[164:167],v[56:59],a[104:107],v[164:167]\n"
"buffer_load_dwordx4 v111,s[28:31],s86 offen lds\n"
"v_mfma_f32_16x16x32_bf16 v[164:167],v[60:63],a[108:111],v[164:167]\n"
"s_add_u32 m0,s85,155648\n"
"s_xor_b32 s86,s86,262144\n"
"v_mfma_f32_16x16x32_bf16 v[132:135],v[60:63],a[100:103],v[132:135]\n"
"buffer_load_dwordx4 v110,s[28:31],s86 offen lds\n"
"v_mfma_f32_16x16x32_bf16 v[132:135],v[56:59],a[96:99],v[132:135]\n"
"v_mfma_f32_16x16x32_bf16 v[136:139],v[64:67],a[96:99],v[136:139]\n"
"ds_read_b64_tr_b16 v[56:57],v101 offset:32768\n"
"v_mfma_f32_16x16x32_bf16 v[136:139],v[68:71],a[100:103],v[136:139]\n"
"ds_read_b64_tr_b16 v[58:59],v101 offset:33792\n"
"v_mfma_f32_16x16x32_bf16 v[168:171],v[68:71],a[108:111],v[168:171]\n"
"ds_read_b64_tr_b16 v[60:61],v101 offset:40960\n"
"v_mfma_f32_16x16x32_bf16 v[168:171],v[64:67],a[104:107],v[168:171]\n"
"s_add_u32 m0,s85,156672\n"
"ds_read_b64_tr_b16 v[62:63],v101 offset:41984\n"
"v_mfma_f32_16x16x32_bf16 v[200:203],v[64:67],a[112:115],v[200:203]\n"
"buffer_load_dwordx4 v111,s[28:31],s86 offen lds\n"
"v_mfma_f32_16x16x32_bf16 v[200:203],v[68:71],a[116:119],v[200:203]\n"
"s_xor_b32 s86,s86,786432\n"
"v_mfma_f32_16x16x32_bf16 v[232:235],v[68:71],a[124:127],v[232:235]\n"
"v_mfma_f32_16x16x32_bf16 v[232:235],v[64:67],a[120:123],v[232:235]\n"
"v_mfma_f32_16x16x32_bf16 v[236:239],v[72:75],a[120:123],v[236:239]\n"
"ds_read_b64_tr_b16 v[64:65],v102 offset:32768\n"
"v_mfma_f32_16x16x32_bf16 v[236:239],v[76:79],a[124:127],v[236:239]\n"
"ds_read_b64_tr_b16 v[66:67],v102 offset:33792\n"
"v_mfma_f32_16x16x32_bf16 v[204:207],v[76:79],a[116:119],v[204:207]\n"
"ds_read_b64_tr_b16 v[68:69],v102 offset:40960\n"
"v_mfma_f32_16x16x32_bf16 v[204:207],v[72:75],a[112:115],v[204:207]\n"
"s_add_u32 m0,s78,65536\n"
"ds_read_b64_tr_b16 v[70:71],v102 offset:41984\n"
"v_mfma_f32_16x16x32_bf16 v[172:175],v[72:75],a[104:107],v[172:175]\n"
"buffer_load_dwordx4 v112,s[24:27],s76 offen offset:0 sc1 lds\n"
"v_mfma_f32_16x16x32_bf16 v[172:175],v[76:79],a[108:111],v[172:175]\n"
"v_mfma_f32_16x16x32_bf16 v[140:143],v[76:79],a[100:103],v[140:143]\n"
"buffer_load_dwordx4 v112,s[24:27],s76 offen offset:1024 sc1 lds\n"
"v_mfma_f32_16x16x32_bf16 v[140:143],v[72:75],a[96:99],v[140:143]\n"
"v_mfma_f32_16x16x32_bf16 v[144:147],v[80:83],a[96:99],v[144:147]\n"
"ds_read_b64_tr_b16 v[72:73],v103 offset:32768\n"
"v_accvgpr_write_b32 a156,0\n"
"v_mfma_f32_16x16x32_bf16 v[144:147],v[84:87],a[100:103],v[144:147]\n"
"ds_read_b64_tr_b16 v[74:75],v103 offset:33792\n"
"v_accvgpr_write_b32 a157,0\n"
"v_mfma_f32_16x16x32_bf16 v[176:179],v[84:87],a[108:111],v[176:179]\n"
"ds_read_b64_tr_b16 v[76:77],v103 offset:40960\n"
"v_accvgpr_write_b32 a158,0\n"
"v_mfma_f32_16x16x32_bf16 v[176:179],v[80:83],a[104:107],v[176:179]\n"
"ds_read_b64_tr_b16 v[78:79],v103 offset:41984\n"
"v_accvgpr_write_b32 a159,0\n"
"v_mfma_f32_16x16x32_bf16 v[208:211],v[80:83],a[112:115],v[208:211]\n"
"buffer_load_dwordx4 v112,s[24:27],s76 offen offset:2048 sc1 lds\n"
"v_mfma_f32_16x16x32_bf16 v[208:211],v[84:87],a[116:119],v[208:211]\n"
"ds_read_b128 v[0:3],v96 offset:0\n"
"v_mfma_f32_16x16x32_bf16 v[240:243],v[84:87],a[124:127],v[240:243]\n"
"v_mfma_f32_16x16x32_bf16 v[240:243],v[80:83],a[120:123],v[240:243]\n"
"ds_read_b128 v[8:11],v96 offset:1024\n"
"v_mfma_f32_16x16x32_bf16 v[244:247],v[88:91],a[120:123],v[244:247]\n"
"ds_read_b64_tr_b16 v[80:81],v104 offset:32768\n"
"v_mfma_f32_16x16x32_bf16 v[244:247],v[92:95],a[124:127],v[244:247]\n"
"ds_read_b64_tr_b16 v[82:83],v104 offset:33792\n"
"v_mfma_f32_16x16x32_bf16 v[212:215],v[92:95],a[116:119],v[212:215]\n"
"ds_read_b64_tr_b16 v[84:85],v104 offset:40960\n"
"v_mfma_f32_16x16x32_bf16 v[212:215],v[88:91],a[112:115],v[212:215]\n"
"ds_read_b64_tr_b16 v[86:87],v104 offset:41984\n"
"v_mfma_f32_16x16x32_bf16 v[180:183],v[88:91],a[104:107],v[180:183]\n"
"buffer_load_dwordx4 v112,s[24:27],s76 offen offset:3072 sc1 lds\n"
"v_mfma_f32_16x16x32_bf16 v[180:183],v[92:95],a[108:111],v[180:183]\n"
"s_add_u32 m0,s78,81920\n"
"s_xor_b32 s76,s76,16384\n"
"ds_read_b128 v[16:19],v96 offset:2048\n"
"v_mfma_f32_16x16x32_bf16 v[148:151],v[92:95],a[100:103],v[148:151]\n"
"buffer_load_dwordx4 v112,s[24:27],s76 offen offset:0 sc1 lds\n"
"v_mfma_f32_16x16x32_bf16 v[148:151],v[88:91],a[96:99],v[148:151]\n"
"ds_read_b128 v[24:27],v96 offset:3072\n"
"v_mfma_f32_16x16x32_bf16 v[152:155],a[128:131],a[96:99],v[152:155]\n"
"ds_read_b64_tr_b16 v[88:89],v105 offset:32768\n"
"v_mfma_f32_16x16x32_bf16 v[152:155],a[132:135],a[100:103],v[152:155]\n"
"ds_read_b64_tr_b16 v[90:91],v105 offset:33792\n"
"v_mfma_f32_16x16x32_bf16 v[184:187],a[132:135],a[108:111],v[184:187]\n"
"ds_read_b64_tr_b16 v[92:93],v105 offset:40960\n"
"v_mfma_f32_16x16x32_bf16 v[184:187],a[128:131],a[104:107],v[184:187]\n"
"ds_read_b64_tr_b16 v[94:95],v105 offset:41984\n"
"v_mfma_f32_16x16x32_bf16 v[216:219],a[128:131],a[112:115],v[216:219]\n"
"ds_read_b128 v[4:7],v96 offset:16384\n"
"v_mfma_f32_16x16x32_bf16 v[216:219],a[132:135],a[116:119],v[216:219]\n"
"ds_read_b128 v[12:15],v96 offset:17408\n"
"v_mfma_f32_16x16x32_bf16 v[248:251],a[132:135],a[124:127],v[248:251]\n"
"ds_read_b128 v[20:23],v96 offset:18432\n"
"v_mfma_f32_16x16x32_bf16 v[248:251],a[128:131],a[120:123],v[248:251]\n"
"ds_read_b128 v[28:31],v96 offset:19456\n"
"v_mfma_f32_16x16x32_bf16 v[252:255],a[136:139],a[120:123],v[252:255]\n"
"buffer_load_dwordx4 v112,s[24:27],s76 offen offset:1024 sc1 lds\n"
"v_mfma_f32_16x16x32_bf16 v[252:255],a[140:143],a[124:127],v[252:255]\n"
"v_mfma_f32_16x16x32_bf16 v[220:223],a[140:143],a[116:119],v[220:223]\n"
"v_mfma_f32_16x16x32_bf16 v[220:223],a[136:139],a[112:115],v[220:223]\n"
"buffer_load_dwordx4 v112,s[24:27],s76 offen offset:2048 sc1 lds\n"
"v_mfma_f32_16x16x32_bf16 v[188:191],a[136:139],a[104:107],v[188:191]\n"
"v_mfma_f32_16x16x32_bf16 v[188:191],a[140:143],a[108:111],v[188:191]\n"
"v_mfma_f32_16x16x32_bf16 v[156:159],a[140:143],a[100:103],v[156:159]\n"
"buffer_load_dwordx4 v112,s[24:27],s76 offen offset:3072 sc1 lds\n"
"v_mfma_f32_16x16x32_bf16 v[156:159],a[136:139],a[96:99],v[156:159]\n"
"s_xor_b32 s76,s76,49152\n"
"s_waitcnt vmcnt(20) lgkmcnt(0)\n"
"s_barrier\n"
"v_mfma_f32_16x16x32_bf16 v[128:131],v[32:35],v[0:3],v[128:131]\n"
"ds_read_b64_tr_b16 a[128:129],v98 offset:0\n"
"ds_read_b64_tr_b16 a[136:137],v99 offset:0\n"
"v_mfma_f32_16x16x32_bf16 v[128:131],v[36:39],v[4:7],v[128:131]\n"
"ds_read_b64_tr_b16 a[130:131],v98 offset:1024\n"
"ds_read_b64_tr_b16 a[138:139],v99 offset:1024\n"
"v_mfma_f32_16x16x32_bf16 v[160:163],v[36:39],v[12:15],v[160:163]\n"
"ds_read_b64_tr_b16 a[132:133],v98 offset:8192\n"
"ds_read_b64_tr_b16 a[140:141],v99 offset:8192\n"
"v_mfma_f32_16x16x32_bf16 v[160:163],v[32:35],v[8:11],v[160:163]\n"
"s_add_u32 m0,s85,131072\n"
"ds_read_b64_tr_b16 a[134:135],v98 offset:9216\n"
"ds_read_b64_tr_b16 a[142:143],v99 offset:9216\n"
"v_mfma_f32_16x16x32_bf16 v[192:195],v[32:35],v[16:19],v[192:195]\n"
"buffer_load_dwordx4 v110,s[28:31],s86 offen lds\n"
"v_mfma_f32_16x16x32_bf16 v[192:195],v[36:39],v[20:23],v[192:195]\n"
"s_add_u32 m0,s85,132096\n"
"v_mfma_f32_16x16x32_bf16 v[224:227],v[36:39],v[28:31],v[224:227]\n"
"buffer_load_dwordx4 v111,s[28:31],s86 offen lds\n"
"v_mfma_f32_16x16x32_bf16 v[224:227],v[32:35],v[24:27],v[224:227]\n"
"s_xor_b32 s86,s86,262144\n"
"v_mfma_f32_16x16x32_bf16 v[228:231],v[40:43],v[24:27],v[228:231]\n"
"ds_read_b64_tr_b16 v[32:33],v100 offset:0\n"
"v_mfma_f32_16x16x32_bf16 v[228:231],v[44:47],v[28:31],v[228:231]\n"
"ds_read_b64_tr_b16 v[34:35],v100 offset:1024\n"
"v_mfma_f32_16x16x32_bf16 v[196:199],v[44:47],v[20:23],v[196:199]\n"
"ds_read_b64_tr_b16 v[36:37],v100 offset:8192\n"
"v_mfma_f32_16x16x32_bf16 v[196:199],v[40:43],v[16:19],v[196:199]\n"
"s_add_u32 m0,s85,139264\n"
"ds_read_b64_tr_b16 v[38:39],v100 offset:9216\n"
"v_mfma_f32_16x16x32_bf16 v[164:167],v[40:43],v[8:11],v[164:167]\n"
"buffer_load_dwordx4 v110,s[28:31],s86 offen lds\n"
"v_mfma_f32_16x16x32_bf16 v[164:167],v[44:47],v[12:15],v[164:167]\n"
"s_add_u32 m0,s85,140288\n"
"v_mfma_f32_16x16x32_bf16 v[132:135],v[44:47],v[4:7],v[132:135]\n"
"buffer_load_dwordx4 v111,s[28:31],s86 offen lds\n"
"v_mfma_f32_16x16x32_bf16 v[132:135],v[40:43],v[0:3],v[132:135]\n"
"s_xor_b32 s86,s86,1835008\n"
"v_mfma_f32_16x16x32_bf16 v[136:139],v[48:51],v[0:3],v[136:139]\n"
"ds_read_b64_tr_b16 v[40:41],v101 offset:0\n"
"v_mfma_f32_16x16x32_bf16 v[136:139],v[52:55],v[4:7],v[136:139]\n"
"ds_read_b64_tr_b16 v[42:43],v101 offset:1024\n"
"v_mfma_f32_16x16x32_bf16 v[168:171],v[52:55],v[12:15],v[168:171]\n"
"ds_read_b64_tr_b16 v[44:45],v101 offset:8192\n"
"v_mfma_f32_16x16x32_bf16 v[168:171],v[48:51],v[8:11],v[168:171]\n"
"s_add_u32 m0,s78,0\n"
"ds_read_b64_tr_b16 v[46:47],v101 offset:9216\n"
"v_mfma_f32_16x16x32_bf16 v[200:203],v[48:51],v[16:19],v[200:203]\n"
"buffer_load_dwordx4 v112,s[24:27],s76 offen offset:0 sc1 lds\n"
"v_mfma_f32_16x16x32_bf16 v[200:203],v[52:55],v[20:23],v[200:203]\n"
"v_mfma_f32_16x16x32_bf16 v[232:235],v[52:55],v[28:31],v[232:235]\n"
"buffer_load_dwordx4 v112,s[24:27],s76 offen offset:1024 sc1 lds\n"
"v_mfma_f32_16x16x32_bf16 v[232:235],v[48:51],v[24:27],v[232:235]\n"
"v_mfma_f32_16x16x32_bf16 v[236:239],v[56:59],v[24:27],v[236:239]\n"
"ds_read_b64_tr_b16 v[48:49],v102 offset:0\n"
"v_mfma_f32_16x16x32_bf16 v[236:239],v[60:63],v[28:31],v[236:239]\n"
"ds_read_b64_tr_b16 v[50:51],v102 offset:1024\n"
"v_mfma_f32_16x16x32_bf16 v[204:207],v[60:63],v[20:23],v[204:207]\n"
"ds_read_b64_tr_b16 v[52:53],v102 offset:8192\n"
"v_mfma_f32_16x16x32_bf16 v[204:207],v[56:59],v[16:19],v[204:207]\n"
"ds_read_b64_tr_b16 v[54:55],v102 offset:9216\n"
"v_mfma_f32_16x16x32_bf16 v[172:175],v[56:59],v[8:11],v[172:175]\n"
"buffer_load_dwordx4 v112,s[24:27],s76 offen offset:2048 sc1 lds\n"
"v_mfma_f32_16x16x32_bf16 v[172:175],v[60:63],v[12:15],v[172:175]\n"
"v_mfma_f32_16x16x32_bf16 v[140:143],v[60:63],v[4:7],v[140:143]\n"
"buffer_load_dwordx4 v112,s[24:27],s76 offen offset:3072 sc1 lds\n"
"v_mfma_f32_16x16x32_bf16 v[140:143],v[56:59],v[0:3],v[140:143]\n"
"s_xor_b32 s76,s76,16384\n"
"s_waitcnt vmcnt(20)\n"
"v_mfma_f32_16x16x32_bf16 v[144:147],v[64:67],v[0:3],v[144:147]\n"
"ds_read_b64_tr_b16 v[56:57],v103 offset:0\n"
"v_mfma_f32_16x16x32_bf16 v[144:147],v[68:71],v[4:7],v[144:147]\n"
"ds_read_b64_tr_b16 v[58:59],v103 offset:1024\n"
"v_mfma_f32_16x16x32_bf16 v[176:179],v[68:71],v[12:15],v[176:179]\n"
"ds_read_b64_tr_b16 v[60:61],v103 offset:8192\n"
"v_mfma_f32_16x16x32_bf16 v[176:179],v[64:67],v[8:11],v[176:179]\n"
"s_add_u32 m0,s78,16384\n"
"ds_read_b64_tr_b16 v[62:63],v103 offset:9216\n"
"v_mfma_f32_16x16x32_bf16 v[208:211],v[64:67],v[16:19],v[208:211]\n"
"buffer_load_dwordx4 v112,s[24:27],s76 offen offset:0 sc1 lds\n"
"v_mfma_f32_16x16x32_bf16 v[208:211],v[68:71],v[20:23],v[208:211]\n"
"ds_read_b128 a[96:99],v96 offset:32768\n"
"v_mfma_f32_16x16x32_bf16 v[240:243],v[68:71],v[28:31],v[240:243]\n"
"buffer_load_dwordx4 v112,s[24:27],s76 offen offset:1024 sc1 lds\n"
"v_mfma_f32_16x16x32_bf16 v[240:243],v[64:67],v[24:27],v[240:243]\n"
"ds_read_b128 a[104:107],v96 offset:33792\n"
"v_mfma_f32_16x16x32_bf16 v[244:247],v[72:75],v[24:27],v[244:247]\n"
"ds_read_b64_tr_b16 v[64:65],v104 offset:0\n"
"v_mfma_f32_16x16x32_bf16 v[244:247],v[76:79],v[28:31],v[244:247]\n"
"ds_read_b64_tr_b16 v[66:67],v104 offset:1024\n"
"v_mfma_f32_16x16x32_bf16 v[212:215],v[76:79],v[20:23],v[212:215]\n"
"ds_read_b64_tr_b16 v[68:69],v104 offset:8192\n"
"v_mfma_f32_16x16x32_bf16 v[212:215],v[72:75],v[16:19],v[212:215]\n"
"ds_read_b64_tr_b16 v[70:71],v104 offset:9216\n"
"v_mfma_f32_16x16x32_bf16 v[180:183],v[72:75],v[8:11],v[180:183]\n"
"buffer_load_dwordx4 v112,s[24:27],s76 offen offset:2048 sc1 lds\n"
"v_mfma_f32_16x16x32_bf16 v[180:183],v[76:79],v[12:15],v[180:183]\n"
"ds_read_b128 a[112:115],v96 offset:34816\n"
"v_mfma_f32_16x16x32_bf16 v[148:151],v[76:79],v[4:7],v[148:151]\n"
"buffer_load_dwordx4 v112,s[24:27],s76 offen offset:3072 sc1 lds\n"
"v_mfma_f32_16x16x32_bf16 v[148:151],v[72:75],v[0:3],v[148:151]\n"
"s_xor_b32 s76,s76,114688\n"
"ds_read_b128 a[120:123],v96 offset:35840\n"
"v_mfma_f32_16x16x32_bf16 v[152:155],v[80:83],v[0:3],v[152:155]\n"
"ds_read_b64_tr_b16 v[72:73],v105 offset:0\n"
"v_mfma_f32_16x16x32_bf16 v[152:155],v[84:87],v[4:7],v[152:155]\n"
"ds_read_b64_tr_b16 v[74:75],v105 offset:1024\n"
"v_mfma_f32_16x16x32_bf16 v[184:187],v[84:87],v[12:15],v[184:187]\n"
"ds_read_b64_tr_b16 v[76:77],v105 offset:8192\n"
"v_mfma_f32_16x16x32_bf16 v[184:187],v[80:83],v[8:11],v[184:187]\n"
"ds_read_b64_tr_b16 v[78:79],v105 offset:9216\n"
"v_mfma_f32_16x16x32_bf16 v[216:219],v[80:83],v[16:19],v[216:219]\n"
"ds_read_b128 a[100:103],v96 offset:49152\n"
"v_mfma_f32_16x16x32_bf16 v[216:219],v[84:87],v[20:23],v[216:219]\n"
"ds_read_b128 a[108:111],v96 offset:50176\n"
"ds_read_b128 a[116:119],v96 offset:51200\n"
"v_mfma_f32_16x16x32_bf16 v[248:251],v[84:87],v[28:31],v[248:251]\n"
"s_load_dwordx2 s[32:33],s[0:1],56\n"
"s_load_dwordx2 s[36:37],s[0:1],64\n"
"s_load_dwordx2 s[40:41],s[0:1],72\n"
"v_mfma_f32_16x16x32_bf16 v[248:251],v[80:83],v[24:27],v[248:251]\n"
"s_add_u32 m0,s78,114688\n"
"ds_read_b128 a[124:127],v96 offset:52224\n"
"v_mfma_f32_16x16x32_bf16 v[252:255],v[88:91],v[24:27],v[252:255]\n"
"buffer_load_dwordx4 v106,s[4:7],s13 offen offset:0 nt lds\n"
"v_mfma_f32_16x16x32_bf16 v[252:255],v[92:95],v[28:31],v[252:255]\n"
"s_add_u32 s13,s13,7168\n"
"v_mfma_f32_16x16x32_bf16 v[220:223],v[92:95],v[20:23],v[220:223]\n"
"buffer_load_dwordx4 v106,s[4:7],s13 offen offset:1024 nt lds\n"
"v_mfma_f32_16x16x32_bf16 v[220:223],v[88:91],v[16:19],v[220:223]\n"
"s_add_u32 s13,s13,7168\n"
"v_mfma_f32_16x16x32_bf16 v[188:191],v[88:91],v[8:11],v[188:191]\n"
"buffer_load_dwordx4 v106,s[4:7],s13 offen offset:2048 nt lds\n"
"v_mfma_f32_16x16x32_bf16 v[188:191],v[92:95],v[12:15],v[188:191]\n"
"s_add_u32 s13,s13,7168\n"
"v_mfma_f32_16x16x32_bf16 v[156:159],v[92:95],v[4:7],v[156:159]\n"
"buffer_load_dwordx4 v106,s[4:7],s13 offen offset:3072 nt lds\n"
"v_mfma_f32_16x16x32_bf16 v[156:159],v[88:91],v[0:3],v[156:159]\n"
"s_add_u32 s13,s13,109568\n"
"s_waitcnt vmcnt(24) lgkmcnt(0)\n"
"s_barrier\n"
"v_mfma_f32_16x16x32_bf16 v[128:131],a[128:131],a[96:99],v[128:131]\n"
"ds_read_b64_tr_b16 v[80:81],v98 offset:49152\n"
"ds_read_b64_tr_b16 v[88:89],v99 offset:49152\n"
"v_mfma_f32_16x16x32_bf16 v[128:131],a[132:135],a[100:103],v[128:131]\n"
"ds_read_b64_tr_b16 v[82:83],v98 offset:50176\n"
"ds_read_b64_tr_b16 v[90:91],v99 offset:50176\n"
"v_mfma_f32_16x16x32_bf16 v[160:163],a[132:135],a[108:111],v[160:163]\n"
"ds_read_b64_tr_b16 v[84:85],v98 offset:57344\n"
"ds_read_b64_tr_b16 v[92:93],v99 offset:57344\n"
"v_mfma_f32_16x16x32_bf16 v[160:163],a[128:131],a[104:107],v[160:163]\n"
"s_add_u32 m0,s85,98304\n"
"ds_read_b64_tr_b16 v[86:87],v98 offset:58368\n"
"ds_read_b64_tr_b16 v[94:95],v99 offset:58368\n"
"v_mfma_f32_16x16x32_bf16 v[192:195],a[128:131],a[112:115],v[192:195]\n"
"buffer_load_dwordx4 v110,s[28:31],s86 offen lds\n"
"v_mfma_f32_16x16x32_bf16 v[192:195],a[132:135],a[116:119],v[192:195]\n"
"v_accvgpr_write_b32 a154,v124\n"
"v_accvgpr_write_b32 a155,v125\n"
"v_mfma_f32_16x16x32_bf16 v[224:227],a[132:135],a[124:127],v[224:227]\n"
"v_accvgpr_write_b32 a152,1\n"
"v_lshrrev_b32 v124,2,v112\n"
"v_mfma_f32_16x16x32_bf16 v[224:227],a[128:131],a[120:123],v[224:227]\n"
"v_add_u32 v124,147456,v124\n"
"v_mov_b32 v125,0\n"
"v_mfma_f32_16x16x32_bf16 v[228:231],a[136:139],a[120:123],v[228:231]\n"
"ds_read_b64_tr_b16 a[128:129],v100 offset:49152\n"
"s_mov_b32 s101,0\n"
"v_mfma_f32_16x16x32_bf16 v[228:231],a[140:143],a[124:127],v[228:231]\n"
"ds_read_b64_tr_b16 a[130:131],v100 offset:50176\n"
"v_mfma_f32_16x16x32_bf16 v[196:199],a[140:143],a[116:119],v[196:199]\n"
"ds_read_b64_tr_b16 a[132:133],v100 offset:57344\n"
"v_mfma_f32_16x16x32_bf16 v[196:199],a[136:139],a[112:115],v[196:199]\n"
"s_add_u32 m0,s85,99328\n"
"ds_read_b64_tr_b16 a[134:135],v100 offset:58368\n"
"v_mfma_f32_16x16x32_bf16 v[164:167],a[136:139],a[104:107],v[164:167]\n"
"buffer_load_dwordx4 v111,s[28:31],s86 offen lds\n"
"v_mfma_f32_16x16x32_bf16 v[164:167],a[140:143],a[108:111],v[164:167]\n"
"s_add_u32 m0,s85,106496\n"
"s_xor_b32 s86,s86,262144\n"
"v_mfma_f32_16x16x32_bf16 v[132:135],a[140:143],a[100:103],v[132:135]\n"
"buffer_load_dwordx4 v110,s[28:31],s86 offen lds\n"
"v_mfma_f32_16x16x32_bf16 v[132:135],a[136:139],a[96:99],v[132:135]\n"
"v_mfma_f32_16x16x32_bf16 v[136:139],v[32:35],a[96:99],v[136:139]\n"
"ds_read_b64_tr_b16 a[136:137],v101 offset:49152\n"
"v_mfma_f32_16x16x32_bf16 v[136:139],v[36:39],a[100:103],v[136:139]\n"
"ds_read_b64_tr_b16 a[138:139],v101 offset:50176\n"
"v_mfma_f32_16x16x32_bf16 v[168:171],v[36:39],a[108:111],v[168:171]\n"
"ds_read_b64_tr_b16 a[140:141],v101 offset:57344\n"
"v_mfma_f32_16x16x32_bf16 v[168:171],v[32:35],a[104:107],v[168:171]\n"
"s_add_u32 m0,s85,107520\n"
"ds_read_b64_tr_b16 a[142:143],v101 offset:58368\n"
"v_mfma_f32_16x16x32_bf16 v[200:203],v[32:35],a[112:115],v[200:203]\n"
"buffer_load_dwordx4 v111,s[28:31],s86 offen lds\n"
"v_mfma_f32_16x16x32_bf16 v[200:203],v[36:39],a[116:119],v[200:203]\n"
"s_xor_b32 s86,s86,786432\n"
"v_mfma_f32_16x16x32_bf16 v[232:235],v[36:39],a[124:127],v[232:235]\n"
"v_mfma_f32_16x16x32_bf16 v[232:235],v[32:35],a[120:123],v[232:235]\n"
"v_mfma_f32_16x16x32_bf16 v[236:239],v[40:43],a[120:123],v[236:239]\n"
"ds_read_b64_tr_b16 v[32:33],v102 offset:49152\n"
"v_mfma_f32_16x16x32_bf16 v[236:239],v[44:47],a[124:127],v[236:239]\n"
"ds_read_b64_tr_b16 v[34:35],v102 offset:50176\n"
"v_mfma_f32_16x16x32_bf16 v[204:207],v[44:47],a[116:119],v[204:207]\n"
"ds_read_b64_tr_b16 v[36:37],v102 offset:57344\n"
"v_mfma_f32_16x16x32_bf16 v[204:207],v[40:43],a[112:115],v[204:207]\n"
"s_add_u32 m0,s78,32768\n"
"ds_read_b64_tr_b16 v[38:39],v102 offset:58368\n"
"buffer_load_dwordx4 v112,s[24:27],s76 offen offset:0 sc1 lds\n"
"v_mfma_f32_16x16x32_bf16 v[172:175],v[40:43],a[104:107],v[172:175]\n"
"buffer_load_dwordx2 v[118:119],v120,s[24:27],s63 offen sc1\n"
"v_mfma_f32_16x16x32_bf16 v[172:175],v[44:47],a[108:111],v[172:175]\n"
"v_mfma_f32_16x16x32_bf16 v[140:143],v[44:47],a[100:103],v[140:143]\n"
"buffer_load_dwordx4 v112,s[24:27],s76 offen offset:1024 sc1 lds\n"
"v_mfma_f32_16x16x32_bf16 v[140:143],v[40:43],a[96:99],v[140:143]\n"
"s_waitcnt vmcnt(23)\n"
"v_mfma_f32_16x16x32_bf16 v[144:147],v[48:51],a[96:99],v[144:147]\n"
"ds_read_b64_tr_b16 v[40:41],v103 offset:49152\n"
"s_and_b32 s82,s100,7\n"
"v_mfma_f32_16x16x32_bf16 v[144:147],v[52:55],a[100:103],v[144:147]\n"
"ds_read_b64_tr_b16 v[42:43],v103 offset:50176\n"
"s_mul_i32 s82,s82,28\n"
"v_mfma_f32_16x16x32_bf16 v[176:179],v[52:55],a[108:111],v[176:179]\n"
"ds_read_b64_tr_b16 v[44:45],v103 offset:57344\n"
"s_lshr_b32 s83,s100,3\n"
"v_mfma_f32_16x16x32_bf16 v[176:179],v[48:51],a[104:107],v[176:179]\n"
"ds_read_b64_tr_b16 v[46:47],v103 offset:58368\n"
"s_add_u32 s23,s82,s83\n"
"v_mfma_f32_16x16x32_bf16 v[208:211],v[48:51],a[112:115],v[208:211]\n"
"buffer_load_dwordx4 v112,s[24:27],s76 offen offset:2048 sc1 lds\n"
"v_mfma_f32_16x16x32_bf16 v[208:211],v[52:55],a[116:119],v[208:211]\n"
"s_cmp_lt_u32 s83,28\n"
"s_cselect_b32 s34,1827872,0\n"
"s_cselect_b32 s42,8192,0\n"
"s_cselect_b32 s23,s23,0\n"
"v_mfma_f32_16x16x32_bf16 v[240:243],v[52:55],a[124:127],v[240:243]\n"
"s_mov_b32 s35,0x20000\n"
"ds_read_b128 v[0:3],v97 offset:0\n"
"s_mov_b32 s43,0x20000\n"
"v_mfma_f32_16x16x32_bf16 v[240:243],v[48:51],a[120:123],v[240:243]\n"
"ds_read_b128 v[8:11],v97 offset:1024\n"
"s_lshl2_add_u32 s82,s83,s52\n"
"v_mfma_f32_16x16x32_bf16 v[244:247],v[56:59],a[120:123],v[244:247]\n"
"ds_read_b64_tr_b16 v[48:49],v104 offset:49152\n"
"s_mul_i32 s82,s82,57344\n"
"v_mfma_f32_16x16x32_bf16 v[244:247],v[60:63],a[124:127],v[244:247]\n"
"ds_read_b64_tr_b16 v[50:51],v104 offset:50176\n"
"s_and_b32 s83,s100,7\n"
"v_mfma_f32_16x16x32_bf16 v[212:215],v[60:63],a[116:119],v[212:215]\n"
"ds_read_b64_tr_b16 v[52:53],v104 offset:57344\n"
"s_mul_i32 s83,s83,896\n"
"v_mfma_f32_16x16x32_bf16 v[212:215],v[56:59],a[112:115],v[212:215]\n"
"ds_read_b64_tr_b16 v[54:55],v104 offset:58368\n"
"s_add_u32 s82,s82,s83\n"
"v_mfma_f32_16x16x32_bf16 v[180:183],v[56:59],a[104:107],v[180:183]\n"
"buffer_load_dwordx4 v112,s[24:27],s76 offen offset:3072 sc1 lds\n"
"v_mfma_f32_16x16x32_bf16 v[180:183],v[60:63],a[108:111],v[180:183]\n"
"s_add_u32 m0,s78,49152\n"
"s_xor_b32 s76,s76,16384\n"
"ds_read_b128 v[16:19],v97 offset:2048\n"
"v_mfma_f32_16x16x32_bf16 v[148:151],v[60:63],a[100:103],v[148:151]\n"
"buffer_load_dwordx4 v112,s[24:27],s76 offen offset:0 sc1 lds\n"
"v_mfma_f32_16x16x32_bf16 v[148:151],v[56:59],a[96:99],v[148:151]\n"
"s_add_u32 s44,s32,s82\n"
"ds_read_b128 v[24:27],v97 offset:3072\n"
"s_addc_u32 s45,s33,0\n"
"v_mfma_f32_16x16x32_bf16 v[152:155],v[64:67],a[96:99],v[152:155]\n"
"s_add_u32 s48,s36,s82\n"
"ds_read_b64_tr_b16 v[56:57],v105 offset:49152\n"
"s_addc_u32 s49,s37,0\n"
"v_mfma_f32_16x16x32_bf16 v[152:155],v[68:71],a[100:103],v[152:155]\n"
"ds_read_b64_tr_b16 v[58:59],v105 offset:50176\n"
"s_mov_b32 s46,51072\n"
"v_mfma_f32_16x16x32_bf16 v[184:187],v[68:71],a[108:111],v[184:187]\n"
"ds_read_b64_tr_b16 v[60:61],v105 offset:57344\n"
"s_mov_b32 s50,s46\n"
"v_mfma_f32_16x16x32_bf16 v[184:187],v[64:67],a[104:107],v[184:187]\n"
"ds_read_b64_tr_b16 v[62:63],v105 offset:58368\n"
"s_mov_b32 s47,0x20000\n"
"v_mfma_f32_16x16x32_bf16 v[216:219],v[64:67],a[112:115],v[216:219]\n"
"ds_read_b128 v[4:7],v97 offset:16384\n"
"s_mov_b32 s51,0x20000\n"
"v_mfma_f32_16x16x32_bf16 v[216:219],v[68:71],a[116:119],v[216:219]\n"
"ds_read_b128 v[12:15],v97 offset:17408\n"
"s_mul_i32 s82,s52,1835008\n"
"v_mfma_f32_16x16x32_bf16 v[248:251],v[68:71],a[124:127],v[248:251]\n"
"ds_read_b128 v[20:23],v97 offset:18432\n"
"s_lshl_b32 s83,s23,5\n"
"v_mfma_f32_16x16x32_bf16 v[248:251],v[64:67],a[120:123],v[248:251]\n"
"ds_read_b128 v[28:31],v97 offset:19456\n"
"s_add_u32 s82,s82,s83\n"
"v_mfma_f32_16x16x32_bf16 v[252:255],v[72:75],a[120:123],v[252:255]\n"
"buffer_load_dwordx4 v112,s[24:27],s76 offen offset:1024 sc1 lds\n"
"v_mfma_f32_16x16x32_bf16 v[252:255],v[76:79],a[124:127],v[252:255]\n"
"s_add_u32 s32,s32,s82\n"
"s_addc_u32 s33,s33,0\n"
"s_add_u32 s36,s36,s82\n"
"v_mfma_f32_16x16x32_bf16 v[220:223],v[76:79],a[116:119],v[220:223]\n"
"s_addc_u32 s37,s37,0\n"
"s_lshl2_add_u32 s82,s23,s52\n"
"s_lshl_b32 s82,s82,13\n"
"v_mfma_f32_16x16x32_bf16 v[220:223],v[72:75],a[112:115],v[220:223]\n"
"buffer_load_dwordx4 v112,s[24:27],s76 offen offset:2048 sc1 lds\n"
"v_mfma_f32_16x16x32_bf16 v[188:191],v[72:75],a[104:107],v[188:191]\n"
"s_add_u32 s40,s40,s82\n"
"s_addc_u32 s41,s41,0\n"
"v_bfe_u32 v121,v112,5,2\n"
"v_mfma_f32_16x16x32_bf16 v[188:191],v[76:79],a[108:111],v[188:191]\n"
"v_bfe_u32 v122,v112,7,2\n"
"v_lshl_add_u32 v121,v122,3,v121\n"
"v_mfma_f32_16x16x32_bf16 v[156:159],v[76:79],a[100:103],v[156:159]\n"
"buffer_load_dwordx4 v112,s[24:27],s76 offen offset:3072 sc1 lds\n"
"v_mfma_f32_16x16x32_bf16 v[156:159],v[72:75],a[96:99],v[156:159]\n"
"s_xor_b32 s76,s76,49152\n"
"v_bfe_u32 v122,v112,9,1\n"
"s_waitcnt vmcnt(13) lgkmcnt(0)\n"
"s_barrier\n"
"v_mfma_f32_16x16x32_bf16 v[128:131],v[80:83],v[0:3],v[128:131]\n"
"ds_read_b64_tr_b16 v[64:65],v98 offset:32768\n"
"ds_read_b64_tr_b16 v[72:73],v99 offset:32768\n"
"v_mfma_f32_16x16x32_bf16 v[128:131],v[84:87],v[4:7],v[128:131]\n"
"ds_read_b64_tr_b16 v[66:67],v98 offset:33792\n"
"ds_read_b64_tr_b16 v[74:75],v99 offset:33792\n"
"v_mfma_f32_16x16x32_bf16 v[160:163],v[84:87],v[12:15],v[160:163]\n"
"ds_read_b64_tr_b16 v[68:69],v98 offset:40960\n"
"ds_read_b64_tr_b16 v[76:77],v99 offset:40960\n"
"v_mfma_f32_16x16x32_bf16 v[160:163],v[80:83],v[8:11],v[160:163]\n"
"s_add_u32 m0,s85,147456\n"
"ds_read_b64_tr_b16 v[70:71],v98 offset:41984\n"
"ds_read_b64_tr_b16 v[78:79],v99 offset:41984\n"
"v_mfma_f32_16x16x32_bf16 v[192:195],v[80:83],v[16:19],v[192:195]\n"
"buffer_load_dwordx4 v110,s[28:31],s86 offen lds\n"
"ds_read_b64_tr_b16 a[176:177],v107 offset:0\n"
"v_mfma_f32_16x16x32_bf16 v[192:195],v[84:87],v[20:23],v[192:195]\n"
"s_add_u32 m0,s85,148480\n"
"ds_read_b64_tr_b16 a[178:179],v107 offset:4096\n"
"ds_read_b64_tr_b16 a[180:181],v107 offset:8\n"
"v_mfma_f32_16x16x32_bf16 v[224:227],v[84:87],v[28:31],v[224:227]\n"
"buffer_load_dwordx4 v111,s[28:31],s86 offen lds\n"
"ds_read_b64_tr_b16 a[182:183],v107 offset:4104\n"
"v_mfma_f32_16x16x32_bf16 v[224:227],v[80:83],v[24:27],v[224:227]\n"
"s_xor_b32 s86,s86,262144\n"
"ds_read_b64_tr_b16 a[184:185],v107 offset:8192\n"
"ds_read_b64_tr_b16 a[186:187],v107 offset:12288\n"
"v_mfma_f32_16x16x32_bf16 v[228:231],v[88:91],v[24:27],v[228:231]\n"
"ds_read_b64_tr_b16 v[80:81],v100 offset:32768\n"
"s_waitcnt vmcnt(9)\n"
"v_xor_b32 v116,s80,v118\n"
"v_mfma_f32_16x16x32_bf16 v[228:231],v[92:95],v[28:31],v[228:231]\n"
"ds_read_b64_tr_b16 v[82:83],v100 offset:33792\n"
"ds_read_b64_tr_b16 a[188:189],v107 offset:8200\n"
"v_xor_b32 v117,s81,v119\n"
"v_mfma_f32_16x16x32_bf16 v[196:199],v[92:95],v[20:23],v[196:199]\n"
"ds_read_b64_tr_b16 v[84:85],v100 offset:40960\n"
"ds_read_b64_tr_b16 a[190:191],v107 offset:12296\n"
"v_mfma_f32_16x16x32_bf16 v[196:199],v[88:91],v[16:19],v[196:199]\n"
"s_add_u32 m0,s85,155648\n"
"ds_read_b64_tr_b16 v[86:87],v100 offset:41984\n"
"v_or_b32 v116,v116,v117\n"
"v_mfma_f32_16x16x32_bf16 v[164:167],v[88:91],v[8:11],v[164:167]\n"
"buffer_load_dwordx4 v110,s[28:31],s86 offen lds\n"
"v_mfma_f32_16x16x32_bf16 v[164:167],v[92:95],v[12:15],v[164:167]\n"
"s_add_u32 m0,s85,156672\n"
"v_cmp_ne_u32 vcc,0,v116\n"
"v_mfma_f32_16x16x32_bf16 v[132:135],v[92:95],v[4:7],v[132:135]\n"
"buffer_load_dwordx4 v111,s[28:31],s86 offen lds\n"
"v_mfma_f32_16x16x32_bf16 v[132:135],v[88:91],v[0:3],v[132:135]\n"
"s_xor_b32 s86,s86,16515072\n"
"s_cbranch_vccnz .Lfk_kv_s\n"
".Lfk_kv_ok:\n"
"v_mfma_f32_16x16x32_bf16 v[136:139],a[128:131],v[0:3],v[136:139]\n"
"ds_read_b64_tr_b16 v[88:89],v101 offset:32768\n"
"v_lshl_add_u32 v121,v122,2,v121\n"
"v_mfma_f32_16x16x32_bf16 v[136:139],a[132:135],v[4:7],v[136:139]\n"
"ds_read_b64_tr_b16 v[90:91],v101 offset:33792\n"
"v_mul_u32_u24 v121,7168,v121\n"
"v_mfma_f32_16x16x32_bf16 v[168:171],a[132:135],v[12:15],v[168:171]\n"
"ds_read_b64_tr_b16 v[92:93],v101 offset:40960\n"
"v_and_b32 v122,16,v112\n"
"v_mfma_f32_16x16x32_bf16 v[168:171],a[128:131],v[8:11],v[168:171]\n"
"s_add_u32 m0,s85,81920\n"
"s_add_u32 s77,s73,16384\n"
"ds_read_b64_tr_b16 v[94:95],v101 offset:41984\n"
"v_mfma_f32_16x16x32_bf16 v[200:203],a[128:131],v[16:19],v[200:203]\n"
"buffer_load_dwordx4 v112,s[24:27],s77 offen offset:0 sc1 lds\n"
"v_mfma_f32_16x16x32_bf16 v[200:203],a[132:135],v[20:23],v[200:203]\n"
"v_add_u32 v115,v121,v122\n"
"s_mov_b32 s54,-229376\n"
"v_mfma_f32_16x16x32_bf16 v[232:235],a[132:135],v[28:31],v[232:235]\n"
"buffer_load_dwordx4 v112,s[24:27],s77 offen offset:1024 sc1 lds\n"
"v_mfma_f32_16x16x32_bf16 v[232:235],a[128:131],v[24:27],v[232:235]\n"
"s_lshl_b32 s55,s52,10\n"
"s_add_u32 s55,s55,131072\n"
"v_mfma_f32_16x16x32_bf16 v[236:239],a[136:139],v[24:27],v[236:239]\n"
"ds_read_b64_tr_b16 a[128:129],v102 offset:32768\n"
"s_mov_b32 s22,229376\n"
"v_mfma_f32_16x16x32_bf16 v[236:239],a[140:143],v[28:31],v[236:239]\n"
"ds_read_b64_tr_b16 a[130:131],v102 offset:33792\n"
"v_lshrrev_b32 v121,7,v112\n"
"v_mfma_f32_16x16x32_bf16 v[204:207],a[140:143],v[20:23],v[204:207]\n"
"ds_read_b64_tr_b16 a[132:133],v102 offset:40960\n"
"v_mul_u32_u24 v121,7168,v121\n"
"v_mfma_f32_16x16x32_bf16 v[204:207],a[136:139],v[16:19],v[204:207]\n"
"s_add_u32 m0,s85,65536\n"
"s_add_u32 s77,s73,65536\n"
"ds_read_b64_tr_b16 a[134:135],v102 offset:41984\n"
"v_mfma_f32_16x16x32_bf16 v[172:175],a[136:139],v[8:11],v[172:175]\n"
"buffer_load_dwordx4 v112,s[24:27],s77 offen offset:0 sc1 lds\n"
"v_mfma_f32_16x16x32_bf16 v[172:175],a[140:143],v[12:15],v[172:175]\n"
"v_lshlrev_b32 v123,3,v112\n"
"v_and_b32 v122,0x380,v123\n"
"v_mfma_f32_16x16x32_bf16 v[140:143],a[140:143],v[4:7],v[140:143]\n"
"buffer_load_dwordx4 v112,s[24:27],s77 offen offset:1024 sc1 lds\n"
"v_mfma_f32_16x16x32_bf16 v[140:143],a[136:139],v[0:3],v[140:143]\n"
"v_min_u32 v122,0x300,v122\n"
"v_add_u32 v122,v121,v122\n"
"v_mfma_f32_16x16x32_bf16 v[144:147],v[32:35],v[0:3],v[144:147]\n"
"ds_read_b64_tr_b16 a[136:137],v103 offset:32768\n"
"v_mfma_f32_16x16x32_bf16 v[144:147],v[36:39],v[4:7],v[144:147]\n"
"ds_read_b64_tr_b16 a[138:139],v103 offset:33792\n"
"v_mfma_f32_16x16x32_bf16 v[176:179],v[36:39],v[12:15],v[176:179]\n"
"ds_read_b64_tr_b16 a[140:141],v103 offset:40960\n"
"v_mfma_f32_16x16x32_bf16 v[176:179],v[32:35],v[8:11],v[176:179]\n"
"s_add_u32 m0,s85,73728\n"
"s_add_u32 s77,s73,24576\n"
"ds_read_b64_tr_b16 a[142:143],v103 offset:41984\n"
"v_mfma_f32_16x16x32_bf16 v[208:211],v[32:35],v[16:19],v[208:211]\n"
"buffer_load_dwordx4 v112,s[24:27],s77 offen offset:0 sc1 lds\n"
"v_mfma_f32_16x16x32_bf16 v[208:211],v[36:39],v[20:23],v[208:211]\n"
"ds_read_b128 a[96:99],v96 offset:0\n"
"v_mfma_f32_16x16x32_bf16 v[240:243],v[36:39],v[28:31],v[240:243]\n"
"buffer_load_dwordx4 v112,s[24:27],s77 offen offset:1024 sc1 lds\n"
"v_mfma_f32_16x16x32_bf16 v[240:243],v[32:35],v[24:27],v[240:243]\n"
"ds_read_b128 a[104:107],v96 offset:1024\n"
"v_mfma_f32_16x16x32_bf16 v[244:247],v[40:43],v[24:27],v[244:247]\n"
"ds_read_b64_tr_b16 v[32:33],v104 offset:32768\n"
"v_mfma_f32_16x16x32_bf16 v[244:247],v[44:47],v[28:31],v[244:247]\n"
"ds_read_b64_tr_b16 v[34:35],v104 offset:33792\n"
"v_mfma_f32_16x16x32_bf16 v[212:215],v[44:47],v[20:23],v[212:215]\n"
"ds_read_b64_tr_b16 v[36:37],v104 offset:40960\n"
"v_mfma_f32_16x16x32_bf16 v[212:215],v[40:43],v[16:19],v[212:215]\n"
"ds_read_b64_tr_b16 v[38:39],v104 offset:41984\n"
"s_mov_b32 s50,22400\n"
"v_mfma_f32_16x16x32_bf16 v[180:183],v[40:43],v[8:11],v[180:183]\n"
"buffer_load_dword a148,v122,s[48:51],0 offen\n"
"v_mfma_f32_16x16x32_bf16 v[180:183],v[44:47],v[12:15],v[180:183]\n"
"ds_read_b128 a[112:115],v96 offset:2048\n"
"v_mfma_f32_16x16x32_bf16 v[148:151],v[44:47],v[4:7],v[148:151]\n"
"v_mfma_f32_16x16x32_bf16 v[148:151],v[40:43],v[0:3],v[148:151]\n"
"ds_read_b128 a[120:123],v96 offset:3072\n"
"v_mfma_f32_16x16x32_bf16 v[152:155],v[48:51],v[0:3],v[152:155]\n"
"ds_read_b64_tr_b16 v[40:41],v105 offset:32768\n"
"v_mfma_f32_16x16x32_bf16 v[152:155],v[52:55],v[4:7],v[152:155]\n"
"ds_read_b64_tr_b16 v[42:43],v105 offset:33792\n"
"v_mfma_f32_16x16x32_bf16 v[184:187],v[52:55],v[12:15],v[184:187]\n"
"ds_read_b64_tr_b16 v[44:45],v105 offset:40960\n"
"v_mfma_f32_16x16x32_bf16 v[184:187],v[48:51],v[8:11],v[184:187]\n"
"ds_read_b64_tr_b16 v[46:47],v105 offset:41984\n"
"v_mfma_f32_16x16x32_bf16 v[216:219],v[48:51],v[16:19],v[216:219]\n"
"ds_read_b128 a[100:103],v96 offset:16384\n"
"v_mfma_f32_16x16x32_bf16 v[216:219],v[52:55],v[20:23],v[216:219]\n"
"ds_read_b128 a[108:111],v96 offset:17408\n"
"v_mfma_f32_16x16x32_bf16 v[248:251],v[52:55],v[28:31],v[248:251]\n"
"ds_read_b128 a[116:119],v96 offset:18432\n"
"v_mfma_f32_16x16x32_bf16 v[248:251],v[48:51],v[24:27],v[248:251]\n"
"ds_read_b128 a[124:127],v96 offset:19456\n"
"v_mfma_f32_16x16x32_bf16 v[252:255],v[56:59],v[24:27],v[252:255]\n"
"v_mfma_f32_16x16x32_bf16 v[252:255],v[60:63],v[28:31],v[252:255]\n"
"buffer_store_dwordx4 a[160:163],v112,s[24:27],s3 offen offset:0 sc1\n"
"v_mfma_f32_16x16x32_bf16 v[220:223],v[60:63],v[20:23],v[220:223]\n"
"v_mfma_f32_16x16x32_bf16 v[220:223],v[56:59],v[16:19],v[220:223]\n"
"v_mfma_f32_16x16x32_bf16 v[188:191],v[56:59],v[8:11],v[188:191]\n"
"v_mfma_f32_16x16x32_bf16 v[188:191],v[60:63],v[12:15],v[188:191]\n"
"buffer_store_dwordx4 a[164:167],v112,s[24:27],s3 offen offset:1024 sc1\n"
"v_mfma_f32_16x16x32_bf16 v[156:159],v[60:63],v[4:7],v[156:159]\n"
"v_mfma_f32_16x16x32_bf16 v[156:159],v[56:59],v[0:3],v[156:159]\n"
"s_waitcnt lgkmcnt(0)\n"
"s_barrier\n"
"v_mfma_f32_16x16x32_bf16 v[128:131],v[64:67],a[96:99],v[128:131]\n"
"ds_read_b64_tr_b16 v[48:49],v98 offset:0\n"
"ds_read_b64_tr_b16 v[56:57],v99 offset:0\n"
"v_mfma_f32_16x16x32_bf16 v[128:131],v[68:71],a[100:103],v[128:131]\n"
"ds_read_b64_tr_b16 v[50:51],v98 offset:1024\n"
"ds_read_b64_tr_b16 v[58:59],v99 offset:1024\n"
"v_mfma_f32_16x16x32_bf16 v[160:163],v[68:71],a[108:111],v[160:163]\n"
"ds_read_b64_tr_b16 v[52:53],v98 offset:8192\n"
"ds_read_b64_tr_b16 v[60:61],v99 offset:8192\n"
"v_mfma_f32_16x16x32_bf16 v[160:163],v[64:67],a[104:107],v[160:163]\n"
"s_add_u32 m0,s78,0\n"
"ds_read_b64_tr_b16 v[54:55],v98 offset:9216\n"
"ds_read_b64_tr_b16 v[62:63],v99 offset:9216\n"
"v_mfma_f32_16x16x32_bf16 v[192:195],v[64:67],a[112:115],v[192:195]\n"
"buffer_load_dwordx4 v112,s[24:27],s76 offen offset:0 sc1 lds\n"
"v_mfma_f32_16x16x32_bf16 v[192:195],v[68:71],a[116:119],v[192:195]\n"
"v_mfma_f32_16x16x32_bf16 v[224:227],v[68:71],a[124:127],v[224:227]\n"
"v_mfma_f32_16x16x32_bf16 v[224:227],v[64:67],a[120:123],v[224:227]\n"
"v_mfma_f32_16x16x32_bf16 v[228:231],v[72:75],a[120:123],v[228:231]\n"
"ds_read_b64_tr_b16 v[64:65],v100 offset:0\n"
"v_mfma_f32_16x16x32_bf16 v[228:231],v[76:79],a[124:127],v[228:231]\n"
"ds_read_b64_tr_b16 v[66:67],v100 offset:1024\n"
"v_mfma_f32_16x16x32_bf16 v[196:199],v[76:79],a[116:119],v[196:199]\n"
"ds_read_b64_tr_b16 v[68:69],v100 offset:8192\n"
"v_mfma_f32_16x16x32_bf16 v[196:199],v[72:75],a[112:115],v[196:199]\n"
"ds_read_b64_tr_b16 v[70:71],v100 offset:9216\n"
"v_mfma_f32_16x16x32_bf16 v[164:167],v[72:75],a[104:107],v[164:167]\n"
"buffer_load_dwordx4 v112,s[24:27],s76 offen offset:1024 sc1 lds\n"
"v_mfma_f32_16x16x32_bf16 v[164:167],v[76:79],a[108:111],v[164:167]\n"
"v_mfma_f32_16x16x32_bf16 v[132:135],v[76:79],a[100:103],v[132:135]\n"
"buffer_load_dwordx4 v112,s[24:27],s76 offen offset:2048 sc1 lds\n"
"v_mfma_f32_16x16x32_bf16 v[132:135],v[72:75],a[96:99],v[132:135]\n"
"v_mfma_f32_16x16x32_bf16 v[136:139],v[80:83],a[96:99],v[136:139]\n"
"ds_read_b64_tr_b16 v[72:73],v101 offset:0\n"
"v_mfma_f32_16x16x32_bf16 v[136:139],v[84:87],a[100:103],v[136:139]\n"
"ds_read_b64_tr_b16 v[74:75],v101 offset:1024\n"
"v_mfma_f32_16x16x32_bf16 v[168:171],v[84:87],a[108:111],v[168:171]\n"
"ds_read_b64_tr_b16 v[76:77],v101 offset:8192\n"
"v_mfma_f32_16x16x32_bf16 v[168:171],v[80:83],a[104:107],v[168:171]\n"
"ds_read_b64_tr_b16 v[78:79],v101 offset:9216\n"
"v_mfma_f32_16x16x32_bf16 v[200:203],v[80:83],a[112:115],v[200:203]\n"
"buffer_load_dwordx4 v112,s[24:27],s76 offen offset:3072 sc1 lds\n"
"v_mfma_f32_16x16x32_bf16 v[200:203],v[84:87],a[116:119],v[200:203]\n"
"s_xor_b32 s76,s76,16384\n"
"v_mfma_f32_16x16x32_bf16 v[232:235],v[84:87],a[124:127],v[232:235]\n"
"v_mfma_f32_16x16x32_bf16 v[232:235],v[80:83],a[120:123],v[232:235]\n"
"v_mfma_f32_16x16x32_bf16 v[236:239],v[88:91],a[120:123],v[236:239]\n"
"ds_read_b64_tr_b16 v[80:81],v102 offset:0\n"
"v_mfma_f32_16x16x32_bf16 v[236:239],v[92:95],a[124:127],v[236:239]\n"
"ds_read_b64_tr_b16 v[82:83],v102 offset:1024\n"
"v_mfma_f32_16x16x32_bf16 v[204:207],v[92:95],a[116:119],v[204:207]\n"
"ds_read_b64_tr_b16 v[84:85],v102 offset:8192\n"
"v_mfma_f32_16x16x32_bf16 v[204:207],v[88:91],a[112:115],v[204:207]\n"
"s_add_u32 m0,s78,16384\n"
"ds_read_b64_tr_b16 v[86:87],v102 offset:9216\n"
"v_mfma_f32_16x16x32_bf16 v[172:175],v[88:91],a[104:107],v[172:175]\n"
"buffer_load_dwordx4 v112,s[24:27],s76 offen offset:0 sc1 lds\n"
"v_mfma_f32_16x16x32_bf16 v[172:175],v[92:95],a[108:111],v[172:175]\n"
"v_mfma_f32_16x16x32_bf16 v[140:143],v[92:95],a[100:103],v[140:143]\n"
"buffer_load_dwordx4 v112,s[24:27],s76 offen offset:1024 sc1 lds\n"
"v_mfma_f32_16x16x32_bf16 v[140:143],v[88:91],a[96:99],v[140:143]\n"
".Lfk_q27:\n"
"s_waitcnt vmcnt(19)\n"
"v_mfma_f32_16x16x32_bf16 v[144:147],a[128:131],a[96:99],v[144:147]\n"
"ds_read_b64_tr_b16 v[88:89],v103 offset:0\n"
"v_mfma_f32_16x16x32_bf16 v[144:147],a[132:135],a[100:103],v[144:147]\n"
"ds_read_b64_tr_b16 v[90:91],v103 offset:1024\n"
"v_mfma_f32_16x16x32_bf16 v[176:179],a[132:135],a[108:111],v[176:179]\n"
"ds_read_b64_tr_b16 v[92:93],v103 offset:8192\n"
"v_mfma_f32_16x16x32_bf16 v[176:179],a[128:131],a[104:107],v[176:179]\n"
"ds_read_b64_tr_b16 v[94:95],v103 offset:9216\n"
"v_mfma_f32_16x16x32_bf16 v[208:211],a[128:131],a[112:115],v[208:211]\n"
"buffer_load_dwordx4 v112,s[24:27],s76 offen offset:2048 sc1 lds\n"
"v_mfma_f32_16x16x32_bf16 v[208:211],a[132:135],a[116:119],v[208:211]\n"
"ds_read_b128 v[0:3],v96 offset:32768\n"
"v_mfma_f32_16x16x32_bf16 v[240:243],a[132:135],a[124:127],v[240:243]\n"
"v_mfma_f32_16x16x32_bf16 v[240:243],a[128:131],a[120:123],v[240:243]\n"
"ds_read_b128 v[8:11],v96 offset:33792\n"
"v_mfma_f32_16x16x32_bf16 v[244:247],a[136:139],a[120:123],v[244:247]\n"
"ds_read_b64_tr_b16 a[128:129],v104 offset:0\n"
"v_mfma_f32_16x16x32_bf16 v[244:247],a[140:143],a[124:127],v[244:247]\n"
"ds_read_b64_tr_b16 a[130:131],v104 offset:1024\n"
"v_mfma_f32_16x16x32_bf16 v[212:215],a[140:143],a[116:119],v[212:215]\n"
"ds_read_b64_tr_b16 a[132:133],v104 offset:8192\n"
"v_mfma_f32_16x16x32_bf16 v[212:215],a[136:139],a[112:115],v[212:215]\n"
"ds_read_b64_tr_b16 a[134:135],v104 offset:9216\n"
"v_mfma_f32_16x16x32_bf16 v[180:183],a[136:139],a[104:107],v[180:183]\n"
"buffer_load_dwordx4 v112,s[24:27],s76 offen offset:3072 sc1 lds\n"
"v_mfma_f32_16x16x32_bf16 v[180:183],a[140:143],a[108:111],v[180:183]\n"
"s_add_u32 m0,s85,122880\n"
"s_add_u32 s77,s73,0\n"
"ds_read_b128 v[16:19],v96 offset:34816\n"
"v_mfma_f32_16x16x32_bf16 v[148:151],a[140:143],a[100:103],v[148:151]\n"
"buffer_load_dwordx4 v112,s[24:27],s77 offen offset:0 sc1 lds\n"
"v_mfma_f32_16x16x32_bf16 v[148:151],a[136:139],a[96:99],v[148:151]\n"
"s_xor_b32 s76,s76,1032192\n"
"ds_read_b128 v[24:27],v96 offset:35840\n"
"v_mfma_f32_16x16x32_bf16 v[152:155],v[32:35],a[96:99],v[152:155]\n"
"ds_read_b64_tr_b16 a[136:137],v105 offset:0\n"
"v_mfma_f32_16x16x32_bf16 v[152:155],v[36:39],a[100:103],v[152:155]\n"
"ds_read_b64_tr_b16 a[138:139],v105 offset:1024\n"
"v_mfma_f32_16x16x32_bf16 v[184:187],v[36:39],a[108:111],v[184:187]\n"
"ds_read_b64_tr_b16 a[140:141],v105 offset:8192\n"
"v_mfma_f32_16x16x32_bf16 v[184:187],v[32:35],a[104:107],v[184:187]\n"
"ds_read_b64_tr_b16 a[142:143],v105 offset:9216\n"
"v_mfma_f32_16x16x32_bf16 v[216:219],v[32:35],a[112:115],v[216:219]\n"
"ds_read_b128 v[4:7],v96 offset:49152\n"
"v_mfma_f32_16x16x32_bf16 v[216:219],v[36:39],a[116:119],v[216:219]\n"
"ds_read_b128 v[12:15],v96 offset:50176\n"
"v_mfma_f32_16x16x32_bf16 v[248:251],v[36:39],a[124:127],v[248:251]\n"
"ds_read_b128 v[20:23],v96 offset:51200\n"
"v_mfma_f32_16x16x32_bf16 v[248:251],v[32:35],a[120:123],v[248:251]\n"
"ds_read_b128 v[28:31],v96 offset:52224\n"
"v_mfma_f32_16x16x32_bf16 v[252:255],v[40:43],a[120:123],v[252:255]\n"
"buffer_load_dwordx4 v112,s[24:27],s77 offen offset:1024 sc1 lds\n"
"v_mfma_f32_16x16x32_bf16 v[252:255],v[44:47],a[124:127],v[252:255]\n"
"v_mfma_f32_16x16x32_bf16 v[220:223],v[44:47],a[116:119],v[220:223]\n"
"s_add_u32 m0,s85,114688\n"
"s_add_u32 s77,s73,32768\n"
"v_mfma_f32_16x16x32_bf16 v[220:223],v[40:43],a[112:115],v[220:223]\n"
"buffer_load_dwordx4 v112,s[24:27],s77 offen offset:0 sc1 lds\n"
"v_mfma_f32_16x16x32_bf16 v[188:191],v[40:43],a[104:107],v[188:191]\n"
"v_mfma_f32_16x16x32_bf16 v[188:191],v[44:47],a[108:111],v[188:191]\n"
"v_mfma_f32_16x16x32_bf16 v[156:159],v[44:47],a[100:103],v[156:159]\n"
"buffer_load_dwordx4 v112,s[24:27],s77 offen offset:1024 sc1 lds\n"
"v_mfma_f32_16x16x32_bf16 v[156:159],v[40:43],a[96:99],v[156:159]\n"
"s_waitcnt vmcnt(21) lgkmcnt(0)\n"
"s_barrier\n"
"v_mfma_f32_16x16x32_bf16 v[128:131],v[48:51],v[0:3],v[128:131]\n"
"ds_read_b64_tr_b16 v[32:33],v98 offset:49152\n"
"ds_read_b64_tr_b16 v[40:41],v99 offset:49152\n"
"v_mfma_f32_16x16x32_bf16 v[128:131],v[52:55],v[4:7],v[128:131]\n"
"ds_read_b64_tr_b16 v[34:35],v98 offset:50176\n"
"ds_read_b64_tr_b16 v[42:43],v99 offset:50176\n"
"v_mfma_f32_16x16x32_bf16 v[160:163],v[52:55],v[12:15],v[160:163]\n"
"s_add_u32 m0,s85,98304\n"
"ds_read_b64_tr_b16 v[36:37],v98 offset:57344\n"
"ds_read_b64_tr_b16 v[44:45],v99 offset:57344\n"
"v_mfma_f32_16x16x32_bf16 v[160:163],v[48:51],v[8:11],v[160:163]\n"
"s_add_u32 s77,s73,49152\n"
"ds_read_b64_tr_b16 v[38:39],v98 offset:58368\n"
"ds_read_b64_tr_b16 v[46:47],v99 offset:58368\n"
"v_mfma_f32_16x16x32_bf16 v[192:195],v[48:51],v[16:19],v[192:195]\n"
"buffer_load_dwordx4 v112,s[24:27],s77 offen offset:0 sc1 lds\n"
"v_mfma_f32_16x16x32_bf16 v[192:195],v[52:55],v[20:23],v[192:195]\n"
"v_mfma_f32_16x16x32_bf16 v[224:227],v[52:55],v[28:31],v[224:227]\n"
"buffer_load_dwordx4 v112,s[24:27],s77 offen offset:1024 sc1 lds\n"
"v_mfma_f32_16x16x32_bf16 v[224:227],v[48:51],v[24:27],v[224:227]\n"
"v_mfma_f32_16x16x32_bf16 v[228:231],v[56:59],v[24:27],v[228:231]\n"
"ds_read_b64_tr_b16 v[48:49],v100 offset:49152\n"
"v_mfma_f32_16x16x32_bf16 v[228:231],v[60:63],v[28:31],v[228:231]\n"
"ds_read_b64_tr_b16 v[50:51],v100 offset:50176\n"
"v_mfma_f32_16x16x32_bf16 v[196:199],v[60:63],v[20:23],v[196:199]\n"
"ds_read_b64_tr_b16 v[52:53],v100 offset:57344\n"
"v_mfma_f32_16x16x32_bf16 v[196:199],v[56:59],v[16:19],v[196:199]\n"
"s_add_u32 m0,s85,106496\n"
"s_add_u32 s77,s73,8192\n"
"ds_read_b64_tr_b16 v[54:55],v100 offset:58368\n"
"v_mfma_f32_16x16x32_bf16 v[164:167],v[56:59],v[8:11],v[164:167]\n"
"buffer_load_dwordx4 v112,s[24:27],s77 offen offset:0 sc1 lds\n"
"v_mfma_f32_16x16x32_bf16 v[164:167],v[60:63],v[12:15],v[164:167]\n"
"v_mfma_f32_16x16x32_bf16 v[132:135],v[60:63],v[4:7],v[132:135]\n"
"buffer_load_dwordx4 v112,s[24:27],s77 offen offset:1024 sc1 lds\n"
"v_mfma_f32_16x16x32_bf16 v[132:135],v[56:59],v[0:3],v[132:135]\n"
"v_mfma_f32_16x16x32_bf16 v[136:139],v[64:67],v[0:3],v[136:139]\n"
"ds_read_b64_tr_b16 v[56:57],v101 offset:49152\n"
"v_mfma_f32_16x16x32_bf16 v[136:139],v[68:71],v[4:7],v[136:139]\n"
"ds_read_b64_tr_b16 v[58:59],v101 offset:50176\n"
"v_mfma_f32_16x16x32_bf16 v[168:171],v[68:71],v[12:15],v[168:171]\n"
"ds_read_b64_tr_b16 v[60:61],v101 offset:57344\n"
"ds_read_b64_tr_b16 v[62:63],v101 offset:58368\n"
"v_mfma_f32_16x16x32_bf16 v[168:171],v[64:67],v[8:11],v[168:171]\n"
"s_add_u32 s48,s48,28672\n"
"s_addc_u32 s49,s49,0\n"
"v_mfma_f32_16x16x32_bf16 v[200:203],v[64:67],v[16:19],v[200:203]\n"
"buffer_load_dword a148,v122,s[48:51],0 offen\n"
"v_mfma_f32_16x16x32_bf16 v[200:203],v[68:71],v[20:23],v[200:203]\n"
"v_mfma_f32_16x16x32_bf16 v[232:235],v[68:71],v[28:31],v[232:235]\n"
"v_mfma_f32_16x16x32_bf16 v[232:235],v[64:67],v[24:27],v[232:235]\n"
"v_mfma_f32_16x16x32_bf16 v[236:239],v[72:75],v[24:27],v[236:239]\n"
"ds_read_b64_tr_b16 v[64:65],v102 offset:49152\n"
"v_mfma_f32_16x16x32_bf16 v[236:239],v[76:79],v[28:31],v[236:239]\n"
"ds_read_b64_tr_b16 v[66:67],v102 offset:50176\n"
"v_mfma_f32_16x16x32_bf16 v[204:207],v[76:79],v[20:23],v[204:207]\n"
"ds_read_b64_tr_b16 v[68:69],v102 offset:57344\n"
"v_mfma_f32_16x16x32_bf16 v[204:207],v[72:75],v[16:19],v[204:207]\n"
"ds_read_b64_tr_b16 v[70:71],v102 offset:58368\n"
"v_mfma_f32_16x16x32_bf16 v[172:175],v[72:75],v[8:11],v[172:175]\n"
"v_mfma_f32_16x16x32_bf16 v[172:175],v[76:79],v[12:15],v[172:175]\n"
"v_mfma_f32_16x16x32_bf16 v[140:143],v[76:79],v[4:7],v[140:143]\n"
"v_mfma_f32_16x16x32_bf16 v[140:143],v[72:75],v[0:3],v[140:143]\n"
"s_waitcnt vmcnt(9)\n"
"v_mfma_f32_16x16x32_bf16 v[144:147],v[80:83],v[0:3],v[144:147]\n"
"ds_read_b64_tr_b16 v[72:73],v103 offset:49152\n"
"s_mov_b32 s90,0\n"
"v_mfma_f32_16x16x32_bf16 v[144:147],v[84:87],v[4:7],v[144:147]\n"
"ds_read_b64_tr_b16 v[74:75],v103 offset:50176\n"
"s_mov_b32 s16,0xc21c0000\n"
"v_mfma_f32_16x16x32_bf16 v[176:179],v[84:87],v[12:15],v[176:179]\n"
"ds_read_b64_tr_b16 v[76:77],v103 offset:57344\n"
"s_mov_b32 s17,0x41a00000\n"
"v_mfma_f32_16x16x32_bf16 v[176:179],v[80:83],v[8:11],v[176:179]\n"
"ds_read_b64_tr_b16 v[78:79],v103 offset:58368\n"
"s_mov_b32 s89,0\n"
"v_mfma_f32_16x16x32_bf16 v[208:211],v[80:83],v[16:19],v[208:211]\n"
"s_xor_b32 s82,s78,8192\n"
"v_add_u32 v116,s82,v112\n"
"v_mfma_f32_16x16x32_bf16 v[208:211],v[84:87],v[20:23],v[208:211]\n"
"ds_read_b128 a[96:99],v96 offset:0\n"
"v_add_u32 v117,4096,v112\n"
"v_mfma_f32_16x16x32_bf16 v[240:243],v[84:87],v[28:31],v[240:243]\n"
"s_getpc_b64 s[20:21]\n"
".Lfk_evp:\n"
"s_add_u32 s20,s20,.Lfk_ev-.Lfk_evp\n"
"s_addc_u32 s21,s21,0\n"
"v_mfma_f32_16x16x32_bf16 v[240:243],v[80:83],v[24:27],v[240:243]\n"
"ds_read_b128 a[104:107],v96 offset:1024\n"
"v_mfma_f32_16x16x32_bf16 v[244:247],v[88:91],v[24:27],v[244:247]\n"
"ds_read_b64_tr_b16 v[80:81],v104 offset:49152\n"
"v_mfma_f32_16x16x32_bf16 v[244:247],v[92:95],v[28:31],v[244:247]\n"
"ds_read_b64_tr_b16 v[82:83],v104 offset:50176\n"
"v_mfma_f32_16x16x32_bf16 v[212:215],v[92:95],v[20:23],v[212:215]\n"
"ds_read_b64_tr_b16 v[84:85],v104 offset:57344\n"
"v_mfma_f32_16x16x32_bf16 v[212:215],v[88:91],v[16:19],v[212:215]\n"
"ds_read_b64_tr_b16 v[86:87],v104 offset:58368\n"
"v_mfma_f32_16x16x32_bf16 v[180:183],v[88:91],v[8:11],v[180:183]\n"
"buffer_store_dwordx4 a[156:159],v112,s[24:27],s74 offen offset:0 sc1\n"
"v_mfma_f32_16x16x32_bf16 v[180:183],v[92:95],v[12:15],v[180:183]\n"
"ds_read_b128 a[112:115],v96 offset:2048\n"
"v_mfma_f32_16x16x32_bf16 v[148:151],v[92:95],v[4:7],v[148:151]\n"
"v_mfma_f32_16x16x32_bf16 v[148:151],v[88:91],v[0:3],v[148:151]\n"
"ds_read_b128 a[120:123],v96 offset:3072\n"
"v_mfma_f32_16x16x32_bf16 v[152:155],a[128:131],v[0:3],v[152:155]\n"
"ds_read_b64_tr_b16 v[88:89],v105 offset:49152\n"
"v_mfma_f32_16x16x32_bf16 v[152:155],a[132:135],v[4:7],v[152:155]\n"
"ds_read_b64_tr_b16 v[90:91],v105 offset:50176\n"
"v_mfma_f32_16x16x32_bf16 v[184:187],a[132:135],v[12:15],v[184:187]\n"
"ds_read_b64_tr_b16 v[92:93],v105 offset:57344\n"
"v_mfma_f32_16x16x32_bf16 v[184:187],a[128:131],v[8:11],v[184:187]\n"
"ds_read_b64_tr_b16 v[94:95],v105 offset:58368\n"
"v_mfma_f32_16x16x32_bf16 v[216:219],a[128:131],v[16:19],v[216:219]\n"
"ds_read_b128 a[100:103],v96 offset:16384\n"
"v_mfma_f32_16x16x32_bf16 v[216:219],a[132:135],v[20:23],v[216:219]\n"
"ds_read_b128 a[108:111],v96 offset:17408\n"
"v_add_u32 v113,65536,v112\n"
"v_mfma_f32_16x16x32_bf16 v[248:251],a[132:135],v[28:31],v[248:251]\n"
"ds_read_b128 a[116:119],v96 offset:18432\n"
"v_add_u32 v114,65536,v112\n"
"v_mfma_f32_16x16x32_bf16 v[248:251],a[128:131],v[24:27],v[248:251]\n"
"ds_read_b128 a[124:127],v96 offset:19456\n"
"v_mfma_f32_16x16x32_bf16 v[252:255],a[136:139],v[24:27],v[252:255]\n"
"v_mfma_f32_16x16x32_bf16 v[252:255],a[140:143],v[28:31],v[252:255]\n"
"buffer_store_dwordx4 a[156:159],v112,s[24:27],s74 offen offset:1024 sc1\n"
"v_mfma_f32_16x16x32_bf16 v[220:223],a[140:143],v[20:23],v[220:223]\n"
"s_lshr_b32 s82,s74,1\n"
"s_add_u32 s82,s82,88080384\n"
"v_mfma_f32_16x16x32_bf16 v[220:223],a[136:139],v[16:19],v[220:223]\n"
"buffer_store_dwordx4 a[156:159],v112,s[24:27],s82 offen sc1\n"
"v_mfma_f32_16x16x32_bf16 v[188:191],a[136:139],v[8:11],v[188:191]\n"
"v_mfma_f32_16x16x32_bf16 v[188:191],a[140:143],v[12:15],v[188:191]\n"
"buffer_store_dword a156,off,s[24:27],s72 sc1\n"
"v_mfma_f32_16x16x32_bf16 v[156:159],a[140:143],v[4:7],v[156:159]\n"
"v_mfma_f32_16x16x32_bf16 v[156:159],a[136:139],v[0:3],v[156:159]\n"
"s_waitcnt vmcnt(11) lgkmcnt(0)\n"
"s_barrier\n"
"v_mfma_f32_16x16x32_bf16 v[224:227],v[32:35],a[120:123],v[224:227]\n"
"ds_read_b128 a[64:67],v113 offset:57344\n"
"v_mov_b32 v96,0xc1980000\n"
"v_mfma_f32_16x16x32_bf16 v[224:227],v[36:39],a[124:127],v[224:227]\n"
"ds_read_b128 a[68:71],v113 offset:58368\n"
"v_mov_b32 v97,0xc1980000\n"
"v_mfma_f32_16x16x32_bf16 v[228:231],v[44:47],a[124:127],v[228:231]\n"
"ds_read_b128 a[72:75],v113 offset:59392\n"
"v_mov_b32 v98,0xc1980000\n"
"v_mfma_f32_16x16x32_bf16 v[228:231],v[40:43],a[120:123],v[228:231]\n"
"ds_read_b128 a[76:79],v113 offset:60416\n"
"v_mov_b32 v99,0xc1980000\n"
"v_mfma_f32_16x16x32_bf16 v[232:235],v[48:51],a[120:123],v[232:235]\n"
"ds_read_b128 a[80:83],v113 offset:61440\n"
"v_mov_b32 v100,0xc1980000\n"
"v_mfma_f32_16x16x32_bf16 v[232:235],v[52:55],a[124:127],v[232:235]\n"
"ds_read_b128 a[84:87],v113 offset:62464\n"
"v_mov_b32 v101,0xc1980000\n"
"v_mfma_f32_16x16x32_bf16 v[236:239],v[60:63],a[124:127],v[236:239]\n"
"ds_read_b128 a[88:91],v113 offset:63488\n"
"v_mov_b32 v102,0xc1980000\n"
"v_mfma_f32_16x16x32_bf16 v[236:239],v[56:59],a[120:123],v[236:239]\n"
"ds_read_b128 a[92:95],v113 offset:64512\n"
"v_mov_b32 v103,0xc1980000\n"
"v_mfma_f32_16x16x32_bf16 v[240:243],v[64:67],a[120:123],v[240:243]\n"
"v_mov_b32 v104,0xc1980000\n"
"v_mov_b32 v105,0xc1980000\n"
"v_mfma_f32_16x16x32_bf16 v[240:243],v[68:71],a[124:127],v[240:243]\n"
"ds_write_b32 v124,v125\n"
"v_mfma_f32_16x16x32_bf16 v[244:247],v[76:79],a[124:127],v[244:247]\n"
"v_mov_b32 v107,0xc1980000\n"
"v_mov_b32 v108,0xc1980000\n"
"v_mfma_f32_16x16x32_bf16 v[244:247],v[72:75],a[120:123],v[244:247]\n"
"v_mov_b32 v109,0xc1980000\n"
"v_mov_b32 v110,0xc1980000\n"
"v_mfma_f32_16x16x32_bf16 v[248:251],v[80:83],a[120:123],v[248:251]\n"
"v_mov_b32 v106,0xc1980000\n"
"v_mov_b32 v111,0xc1980000\n"
"v_mfma_f32_16x16x32_bf16 v[248:251],v[84:87],a[124:127],v[248:251]\n"
"v_mfma_f32_16x16x32_bf16 v[252:255],v[92:95],a[124:127],v[252:255]\n"
"v_mfma_f32_16x16x32_bf16 v[252:255],v[88:91],a[120:123],v[252:255]\n"
"v_mfma_f32_16x16x32_bf16 v[128:131],v[32:35],a[96:99],v[128:131]\n"
"v_mfma_f32_16x16x32_bf16 v[128:131],v[36:39],a[100:103],v[128:131]\n"
"v_cvt_pk_bf16_f32 v232,v232,v233\n"
"v_cvt_pk_bf16_f32 v233,v234,v235\n"
"v_mfma_f32_16x16x32_bf16 v[132:135],v[44:47],a[100:103],v[132:135]\n"
"v_cvt_pk_bf16_f32 v224,v224,v225\n"
"v_cvt_pk_bf16_f32 v225,v226,v227\n"
"v_mfma_f32_16x16x32_bf16 v[132:135],v[40:43],a[96:99],v[132:135]\n"
"v_cvt_pk_bf16_f32 v226,v228,v229\n"
"v_cvt_pk_bf16_f32 v227,v230,v231\n"
"v_mfma_f32_16x16x32_bf16 v[136:139],v[48:51],a[96:99],v[136:139]\n"
"ds_write_b128 v116,v[224:227] offset:0\n"
"v_mfma_f32_16x16x32_bf16 v[136:139],v[52:55],a[100:103],v[136:139]\n"
"v_cvt_pk_bf16_f32 v234,v236,v237\n"
"v_cvt_pk_bf16_f32 v235,v238,v239\n"
"v_mfma_f32_16x16x32_bf16 v[140:143],v[60:63],a[100:103],v[140:143]\n"
"ds_write_b128 v116,v[232:235] offset:1024\n"
"v_mfma_f32_16x16x32_bf16 v[140:143],v[56:59],a[96:99],v[140:143]\n"
"v_cvt_pk_bf16_f32 v240,v240,v241\n"
"v_cvt_pk_bf16_f32 v241,v242,v243\n"
"v_mfma_f32_16x16x32_bf16 v[144:147],v[64:67],a[96:99],v[144:147]\n"
"v_cvt_pk_bf16_f32 v242,v244,v245\n"
"v_cvt_pk_bf16_f32 v243,v246,v247\n"
"v_mfma_f32_16x16x32_bf16 v[144:147],v[68:71],a[100:103],v[144:147]\n"
"ds_write_b128 v116,v[240:243] offset:2048\n"
"v_cvt_pk_bf16_f32 v248,v248,v249\n"
"v_mfma_f32_16x16x32_bf16 v[148:151],v[76:79],a[100:103],v[148:151]\n"
"v_cvt_pk_bf16_f32 v249,v250,v251\n"
"v_cvt_pk_bf16_f32 v250,v252,v253\n"
"v_cvt_pk_bf16_f32 v251,v254,v255\n"
"v_mfma_f32_16x16x32_bf16 v[148:151],v[72:75],a[96:99],v[148:151]\n"
"ds_write_b128 v116,v[248:251] offset:3072\n"
"v_mfma_f32_16x16x32_bf16 v[152:155],v[80:83],a[96:99],v[152:155]\n"
"ds_read_b128 a[48:51],v116 offset:0\n"
"ds_read_b128 a[52:55],v116 offset:1024\n"
"v_mfma_f32_16x16x32_bf16 v[152:155],v[84:87],a[100:103],v[152:155]\n"
"ds_read_b128 a[56:59],v116 offset:2048\n"
"ds_read_b128 a[60:63],v116 offset:3072\n"
"v_mfma_f32_16x16x32_bf16 v[156:159],v[92:95],a[100:103],v[156:159]\n"
"v_mfma_f32_16x16x32_bf16 v[156:159],v[88:91],a[96:99],v[156:159]\n"
"v_mfma_f32_16x16x32_bf16 v[160:163],v[32:35],a[104:107],v[160:163]\n"
"v_mfma_f32_16x16x32_bf16 v[160:163],v[36:39],a[108:111],v[160:163]\n"
"v_cvt_pk_bf16_f32 v136,v136,v137\n"
"v_cvt_pk_bf16_f32 v137,v138,v139\n"
"v_mfma_f32_16x16x32_bf16 v[164:167],v[44:47],a[108:111],v[164:167]\n"
"v_cvt_pk_bf16_f32 v128,v128,v129\n"
"v_cvt_pk_bf16_f32 v129,v130,v131\n"
"v_mfma_f32_16x16x32_bf16 v[164:167],v[40:43],a[104:107],v[164:167]\n"
"v_cvt_pk_bf16_f32 v130,v132,v133\n"
"v_cvt_pk_bf16_f32 v131,v134,v135\n"
"v_mfma_f32_16x16x32_bf16 v[168:171],v[48:51],a[104:107],v[168:171]\n"
"ds_write_b128 v116,v[128:131] offset:16384\n"
"v_mfma_f32_16x16x32_bf16 v[168:171],v[52:55],a[108:111],v[168:171]\n"
"v_cvt_pk_bf16_f32 v138,v140,v141\n"
"v_cvt_pk_bf16_f32 v139,v142,v143\n"
"v_mfma_f32_16x16x32_bf16 v[172:175],v[60:63],a[108:111],v[172:175]\n"
"ds_write_b128 v116,v[136:139] offset:17408\n"
"v_mfma_f32_16x16x32_bf16 v[172:175],v[56:59],a[104:107],v[172:175]\n"
"v_cvt_pk_bf16_f32 v144,v144,v145\n"
"v_cvt_pk_bf16_f32 v145,v146,v147\n"
"v_mfma_f32_16x16x32_bf16 v[176:179],v[64:67],a[104:107],v[176:179]\n"
"v_cvt_pk_bf16_f32 v146,v148,v149\n"
"v_cvt_pk_bf16_f32 v147,v150,v151\n"
"v_mfma_f32_16x16x32_bf16 v[176:179],v[68:71],a[108:111],v[176:179]\n"
"ds_write_b128 v116,v[144:147] offset:18432\n"
"v_cvt_pk_bf16_f32 v152,v152,v153\n"
"v_mfma_f32_16x16x32_bf16 v[180:183],v[76:79],a[108:111],v[180:183]\n"
"v_cvt_pk_bf16_f32 v153,v154,v155\n"
"v_cvt_pk_bf16_f32 v154,v156,v157\n"
"v_cvt_pk_bf16_f32 v155,v158,v159\n"
"v_mfma_f32_16x16x32_bf16 v[180:183],v[72:75],a[104:107],v[180:183]\n"
"ds_write_b128 v116,v[152:155] offset:19456\n"
"v_mfma_f32_16x16x32_bf16 v[184:187],v[80:83],a[104:107],v[184:187]\n"
"ds_read_b128 a[0:3],v116 offset:16384\n"
"ds_read_b128 a[4:7],v116 offset:17408\n"
"v_mfma_f32_16x16x32_bf16 v[184:187],v[84:87],a[108:111],v[184:187]\n"
"ds_read_b128 a[8:11],v116 offset:18432\n"
"ds_read_b128 a[12:15],v116 offset:19456\n"
"v_mfma_f32_16x16x32_bf16 v[188:191],v[92:95],a[108:111],v[188:191]\n"
"v_mfma_f32_16x16x32_bf16 v[188:191],v[88:91],a[104:107],v[188:191]\n"
"v_mfma_f32_16x16x32_bf16 v[192:195],v[32:35],a[112:115],v[192:195]\n"
"v_mfma_f32_16x16x32_bf16 v[192:195],v[36:39],a[116:119],v[192:195]\n"
"v_cvt_pk_bf16_f32 v168,v168,v169\n"
"v_cvt_pk_bf16_f32 v169,v170,v171\n"
"v_mfma_f32_16x16x32_bf16 v[196:199],v[44:47],a[116:119],v[196:199]\n"
"v_cvt_pk_bf16_f32 v160,v160,v161\n"
"v_cvt_pk_bf16_f32 v161,v162,v163\n"
"v_mfma_f32_16x16x32_bf16 v[196:199],v[40:43],a[112:115],v[196:199]\n"
"v_cvt_pk_bf16_f32 v162,v164,v165\n"
"v_cvt_pk_bf16_f32 v163,v166,v167\n"
"v_mfma_f32_16x16x32_bf16 v[200:203],v[48:51],a[112:115],v[200:203]\n"
"ds_write_b128 v116,v[160:163] offset:49152\n"
"v_mfma_f32_16x16x32_bf16 v[200:203],v[52:55],a[116:119],v[200:203]\n"
"v_cvt_pk_bf16_f32 v170,v172,v173\n"
"v_cvt_pk_bf16_f32 v171,v174,v175\n"
"v_mfma_f32_16x16x32_bf16 v[204:207],v[60:63],a[116:119],v[204:207]\n"
"ds_write_b128 v116,v[168:171] offset:50176\n"
"v_mfma_f32_16x16x32_bf16 v[204:207],v[56:59],a[112:115],v[204:207]\n"
"v_cvt_pk_bf16_f32 v176,v176,v177\n"
"v_cvt_pk_bf16_f32 v177,v178,v179\n"
"v_mfma_f32_16x16x32_bf16 v[208:211],v[64:67],a[112:115],v[208:211]\n"
"v_cvt_pk_bf16_f32 v178,v180,v181\n"
"v_cvt_pk_bf16_f32 v179,v182,v183\n"
"v_mfma_f32_16x16x32_bf16 v[208:211],v[68:71],a[116:119],v[208:211]\n"
"ds_write_b128 v116,v[176:179] offset:51200\n"
"v_cvt_pk_bf16_f32 v184,v184,v185\n"
"v_mfma_f32_16x16x32_bf16 v[212:215],v[76:79],a[116:119],v[212:215]\n"
"v_cvt_pk_bf16_f32 v185,v186,v187\n"
"v_cvt_pk_bf16_f32 v186,v188,v189\n"
"v_cvt_pk_bf16_f32 v187,v190,v191\n"
"v_mfma_f32_16x16x32_bf16 v[212:215],v[72:75],a[112:115],v[212:215]\n"
"ds_write_b128 v116,v[184:187] offset:52224\n"
"v_mfma_f32_16x16x32_bf16 v[216:219],v[80:83],a[112:115],v[216:219]\n"
"ds_read_b128 a[16:19],v116 offset:49152\n"
"ds_read_b128 a[20:23],v116 offset:50176\n"
"v_mfma_f32_16x16x32_bf16 v[216:219],v[84:87],a[116:119],v[216:219]\n"
"ds_read_b128 a[24:27],v116 offset:51200\n"
"ds_read_b128 a[28:31],v116 offset:52224\n"
"v_mfma_f32_16x16x32_bf16 v[220:223],v[92:95],a[116:119],v[220:223]\n"
"s_lshl2_add_u32 s82,s60,s52\n"
"s_lshr_b32 s82,s82,1\n"
"s_cmp_eq_u32 s82,1\n"
"v_mfma_f32_16x16x32_bf16 v[220:223],v[88:91],a[112:115],v[220:223]\n"
"s_cbranch_scc1 .Lfk_head0\n"
"s_waitcnt lgkmcnt(0)\n"
"s_barrier\n"
"v_mfma_f32_16x16x32_bf16 v[24:27],a[64:67],a[48:51],v[108:111]\n"
"s_andn2_b32 m0,s55,16384\n"
"s_add_u32 s54,s54,s22\n"
"v_cvt_pk_bf16_f32 v200,v200,v201\n"
"v_mfma_f32_16x16x32_bf16 v[24:27],a[68:71],a[52:55],v[24:27]\n"
"buffer_load_dwordx4 v115,s[32:35],s54 offen sc1 lds\n"
"v_mfma_f32_16x16x32_bf16 v[24:27],a[72:75],a[56:59],v[24:27]\n"
"v_cvt_pk_bf16_f32 v192,v192,v193\n"
"v_cvt_pk_bf16_f32 v193,v194,v195\n"
"v_mfma_f32_16x16x32_bf16 v[24:27],a[76:79],a[60:63],v[24:27]\n"
"v_cvt_pk_bf16_f32 v194,v196,v197\n"
"v_cvt_pk_bf16_f32 v195,v198,v199\n"
"v_mfma_f32_16x16x32_bf16 v[28:31],a[92:95],a[60:63],v[108:111]\n"
"ds_write_b128 v116,v[192:195] offset:32768\n"
"v_mfma_f32_16x16x32_bf16 v[28:31],a[88:91],a[56:59],v[28:31]\n"
"v_cvt_pk_bf16_f32 v201,v202,v203\n"
"v_cvt_pk_bf16_f32 v202,v204,v205\n"
"v_cvt_pk_bf16_f32 v203,v206,v207\n"
"v_mfma_f32_16x16x32_bf16 v[28:31],a[84:87],a[52:55],v[28:31]\n"
"ds_write_b128 v116,v[200:203] offset:33792\n"
"v_mfma_f32_16x16x32_bf16 v[28:31],a[80:83],a[48:51],v[28:31]\n"
"v_cvt_pk_bf16_f32 v208,v208,v209\n"
"v_cvt_pk_bf16_f32 v209,v210,v211\n"
"v_mfma_f32_16x16x32_bf16 v[4:7],a[80:83],a[0:3],v[96:99]\n"
"v_cvt_pk_bf16_f32 v210,v212,v213\n"
"v_cvt_pk_bf16_f32 v211,v214,v215\n"
"v_mfma_f32_16x16x32_bf16 v[4:7],a[84:87],a[4:7],v[4:7]\n"
"ds_read_b128 a[96:99],v113 offset:16384\n"
"ds_read_b128 a[100:103],v113 offset:17408\n"
"v_mfma_f32_16x16x32_bf16 v[4:7],a[88:91],a[8:11],v[4:7]\n"
"ds_read_b128 a[104:107],v113 offset:18432\n"
"ds_read_b128 a[108:111],v113 offset:19456\n"
"v_mfma_f32_16x16x32_bf16 v[4:7],a[92:95],a[12:15],v[4:7]\n"
"ds_read_b128 a[112:115],v113 offset:20480\n"
"ds_read_b128 a[116:119],v113 offset:21504\n"
"v_mfma_f32_16x16x32_bf16 v[0:3],a[76:79],a[12:15],v[96:99]\n"
"ds_read_b128 a[120:123],v113 offset:22528\n"
"ds_read_b128 a[124:127],v113 offset:23552\n"
"v_mfma_f32_16x16x32_bf16 v[0:3],a[72:75],a[8:11],v[0:3]\n"
"v_max3_f32 v118,v24,v25,v26\n"
"v_max3_f32 v118,v118,v27,v28\n"
"v_mfma_f32_16x16x32_bf16 v[0:3],a[68:71],a[4:7],v[0:3]\n"
"v_max3_f32 v118,v118,v29,v30\n"
"v_max_f32 v118,v118,v31\n"
"v_mov_b32 v120,v118\n"
"v_mfma_f32_16x16x32_bf16 v[0:3],a[64:67],a[0:3],v[0:3]\n"
"ds_write_b128 v116,v[208:211] offset:34816\n"
"v_mfma_f32_16x16x32_bf16 v[8:11],a[64:67],a[16:19],v[100:103]\n"
"v_cvt_pk_bf16_f32 v216,v216,v217\n"
"v_cvt_pk_bf16_f32 v217,v218,v219\n"
"v_mfma_f32_16x16x32_bf16 v[8:11],a[68:71],a[20:23],v[8:11]\n"
"v_cvt_pk_bf16_f32 v218,v220,v221\n"
"v_cvt_pk_bf16_f32 v219,v222,v223\n"
"v_mfma_f32_16x16x32_bf16 v[8:11],a[72:75],a[24:27],v[8:11]\n"
"ds_write_b128 v116,v[216:219] offset:35840\n"
"ds_read_b128 a[32:35],v116 offset:32768\n"
"v_mfma_f32_16x16x32_bf16 v[8:11],a[76:79],a[28:31],v[8:11]\n"
"v_max3_f32 v119,v0,v1,v2\n"
"v_max3_f32 v119,v119,v3,v4\n"
"v_mfma_f32_16x16x32_bf16 v[12:15],a[92:95],a[28:31],v[100:103]\n"
"v_max3_f32 v119,v119,v5,v6\n"
"v_max_f32 v119,v119,v7\n"
"v_mfma_f32_16x16x32_bf16 v[12:15],a[88:91],a[24:27],v[12:15]\n"
"v_min_f32 v120,v120,v119\n"
"v_max_f32 v118,v118,v119\n"
"v_mfma_f32_16x16x32_bf16 v[12:15],a[84:87],a[20:23],v[12:15]\n"
"ds_read_b128 a[36:39],v116 offset:33792\n"
"ds_read_b128 a[40:43],v116 offset:34816\n"
"v_mfma_f32_16x16x32_bf16 v[12:15],a[80:83],a[16:19],v[12:15]\n"
"ds_read_b128 a[44:47],v116 offset:35840\n"
"s_waitcnt lgkmcnt(0)\n"
"v_mfma_f32_16x16x32_bf16 v[20:23],a[80:83],a[32:35],v[104:107]\n"
"v_mfma_f32_16x16x32_bf16 v[20:23],a[84:87],a[36:39],v[20:23]\n"
"v_mfma_f32_16x16x32_bf16 v[20:23],a[88:91],a[40:43],v[20:23]\n"
"buffer_store_dwordx4 a[224:227],v112,s[24:27],s56 offen offset:0 sc1\n"
"v_mfma_f32_16x16x32_bf16 v[20:23],a[92:95],a[44:47],v[20:23]\n"
"v_max3_f32 v119,v8,v9,v10\n"
"v_mfma_f32_16x16x32_bf16 v[16:19],a[76:79],a[44:47],v[104:107]\n"
"v_max3_f32 v119,v119,v11,v12\n"
"v_max3_f32 v119,v119,v13,v14\n"
"v_mfma_f32_16x16x32_bf16 v[16:19],a[72:75],a[40:43],v[16:19]\n"
"v_max_f32 v119,v119,v15\n"
"v_min_f32 v120,v120,v119\n"
"v_mfma_f32_16x16x32_bf16 v[16:19],a[68:71],a[36:39],v[16:19]\n"
"v_max_f32 v118,v118,v119\n"
"v_mfma_f32_16x16x32_bf16 v[16:19],a[64:67],a[32:35],v[16:19]\n"
"s_nop 7\n"
"v_max3_f32 v119,v16,v17,v18\n"
"v_max3_f32 v119,v119,v19,v20\n"
"v_max3_f32 v119,v119,v21,v22\n"
"v_max_f32 v119,v119,v23\n"
"v_min_f32 v120,v120,v119\n"
"v_max_f32 v118,v118,v119\n"
"v_cmp_gt_f32 s[96:97],s16,v120\n"
"v_cmp_lt_f32 vcc,s93,v118\n"
"s_nop 0\n"
"s_or_b64 s[96:97],s[96:97],vcc\n"
"s_cbranch_scc1 .Lfk_cs_h3\n"
".Lfk_cr_h3:\n"
"s_cmp_ge_u32 s52,2\n"
"s_cbranch_scc1 .Lfk_g0\n"
"s_waitcnt vmcnt(11) lgkmcnt(0)\n"
"s_barrier\n"
"v_mfma_f32_16x16x32_bf16 v[32:35],a[96:99],a[0:3],v[96:99]\n"
"s_add_u32 m0,s55,4096\n"
"v_exp_f32 v0,v0\n"
"v_mfma_f32_16x16x32_bf16 v[32:35],a[100:103],a[4:7],v[32:35]\n"
"s_add_u32 s54,s54,s22\n"
"v_exp_f32 v1,v1\n"
"v_mfma_f32_16x16x32_bf16 v[32:35],a[104:107],a[8:11],v[32:35]\n"
"buffer_load_dwordx4 v115,s[32:35],s54 offen sc1 lds\n"
"v_exp_f32 v2,v2\n"
"v_cvt_pk_bf16_f32 v64,v0,v1\n"
"v_mfma_f32_16x16x32_bf16 v[32:35],a[108:111],a[12:15],v[32:35]\n"
"s_add_u32 m0,s67,16384\n"
"v_exp_f32 v3,v3\n"
"v_mfma_f32_16x16x32_bf16 v[36:39],a[124:127],a[12:15],v[96:99]\n"
"v_exp_f32 v4,v4\n"
"v_cvt_pk_bf16_f32 v65,v2,v3\n"
"v_exp_f32 v5,v5\n"
"v_mfma_f32_16x16x32_bf16 v[36:39],a[120:123],a[8:11],v[36:39]\n"
"buffer_load_dwordx4 v112,s[24:27],s64 offen offset:0 sc1 lds\n"
"v_mfma_f32_16x16x32_bf16 v[36:39],a[116:119],a[4:7],v[36:39]\n"
"v_exp_f32 v6,v6\n"
"v_cvt_pk_bf16_f32 v66,v4,v5\n"
"v_exp_f32 v7,v7\n"
"v_mfma_f32_16x16x32_bf16 v[36:39],a[112:115],a[0:3],v[36:39]\n"
"buffer_load_dwordx4 v112,s[24:27],s64 offen offset:1024 sc1 lds\n"
"v_mfma_f32_16x16x32_bf16 v[44:47],a[112:115],a[16:19],v[100:103]\n"
"v_exp_f32 v8,v8\n"
"v_cvt_pk_bf16_f32 v67,v6,v7\n"
"v_exp_f32 v9,v9\n"
"v_mfma_f32_16x16x32_bf16 v[44:47],a[116:119],a[20:23],v[44:47]\n"
"ds_read_b128 a[64:67],v113 offset:49152\n"
"ds_read_b128 a[68:71],v113 offset:50176\n"
"v_exp_f32 v10,v10\n"
"v_mfma_f32_16x16x32_bf16 v[44:47],a[120:123],a[24:27],v[44:47]\n"
"ds_read_b128 a[72:75],v113 offset:51200\n"
"ds_read_b128 a[76:79],v113 offset:52224\n"
"v_exp_f32 v11,v11\n"
"v_mfma_f32_16x16x32_bf16 v[44:47],a[124:127],a[28:31],v[44:47]\n"
"ds_read_b128 a[80:83],v113 offset:53248\n"
"ds_read_b128 a[84:87],v113 offset:54272\n"
"v_exp_f32 v12,v12\n"
"v_mfma_f32_16x16x32_bf16 v[40:43],a[108:111],a[28:31],v[100:103]\n"
"ds_read_b128 a[88:91],v113 offset:55296\n"
"ds_read_b128 a[92:95],v113 offset:56320\n"
"v_exp_f32 v13,v13\n"
"v_mfma_f32_16x16x32_bf16 v[40:43],a[104:107],a[24:27],v[40:43]\n"
"buffer_load_dwordx4 v112,s[24:27],s64 offen offset:2048 sc1 lds\n"
"v_exp_f32 v14,v14\n"
"v_mfma_f32_16x16x32_bf16 v[40:43],a[100:103],a[20:23],v[40:43]\n"
"buffer_load_dwordx4 v112,s[24:27],s64 offen offset:3072 sc1 lds\n"
"v_mfma_f32_16x16x32_bf16 v[40:43],a[96:99],a[16:19],v[40:43]\n"
"s_add_u32 m0,s67,20480\n"
"v_exp_f32 v15,v15\n"
"v_mfma_f32_16x16x32_bf16 v[48:51],a[96:99],a[32:35],v[104:107]\n"
"buffer_load_dwordx4 v117,s[24:27],s64 offen offset:0 sc1 lds\n"
"buffer_load_dwordx4 v117,s[24:27],s64 offen offset:1024 sc1 lds\n"
"v_exp_f32 v16,v16\n"
"v_exp_f32 v17,v17\n"
"v_mfma_f32_16x16x32_bf16 v[48:51],a[100:103],a[36:39],v[48:51]\n"
"buffer_load_dwordx4 v117,s[24:27],s64 offen offset:2048 sc1 lds\n"
"v_mfma_f32_16x16x32_bf16 v[48:51],a[104:107],a[40:43],v[48:51]\n"
"v_exp_f32 v18,v18\n"
"v_cvt_pk_bf16_f32 v68,v8,v9\n"
"v_cvt_pk_bf16_f32 v69,v10,v11\n"
"v_exp_f32 v19,v19\n"
"v_mfma_f32_16x16x32_bf16 v[48:51],a[108:111],a[44:47],v[48:51]\n"
"buffer_load_dwordx4 v117,s[24:27],s64 offen offset:3072 sc1 lds\n"
"v_mfma_f32_16x16x32_bf16 v[52:55],a[124:127],a[44:47],v[104:107]\n"
"s_add_u32 s64,s64,16384\n"
"v_exp_f32 v20,v20\n"
"v_cvt_pk_bf16_f32 v70,v12,v13\n"
"v_cvt_pk_bf16_f32 v71,v14,v15\n"
"v_mfma_f32_16x16x32_bf16 v[52:55],a[120:123],a[40:43],v[52:55]\n"
"v_exp_f32 v21,v21\n"
"v_cvt_pk_bf16_f32 v72,v16,v17\n"
"v_cvt_pk_bf16_f32 v73,v18,v19\n"
"v_mfma_f32_16x16x32_bf16 v[52:55],a[116:119],a[36:39],v[52:55]\n"
"v_exp_f32 v22,v22\n"
"v_cvt_pk_bf16_f32 v74,v20,v21\n"
"v_mfma_f32_16x16x32_bf16 v[52:55],a[112:115],a[32:35],v[52:55]\n"
"v_exp_f32 v23,v23\n"
"v_mfma_f32_16x16x32_bf16 v[60:63],a[112:115],a[48:51],v[108:111]\n"
"v_exp_f32 v24,v24\n"
"v_cvt_pk_bf16_f32 v75,v22,v23\n"
"v_mfma_f32_16x16x32_bf16 v[60:63],a[116:119],a[52:55],v[60:63]\n"
"v_exp_f32 v25,v25\n"
"v_mfma_f32_16x16x32_bf16 v[60:63],a[120:123],a[56:59],v[60:63]\n"
"v_exp_f32 v26,v26\n"
"v_exp_f32 v27,v27\n"
"v_mfma_f32_16x16x32_bf16 v[60:63],a[124:127],a[60:63],v[60:63]\n"
"buffer_store_dwordx4 a[228:231],v112,s[24:27],s56 offen offset:1024 sc1\n"
"v_mfma_f32_16x16x32_bf16 v[56:59],a[108:111],a[60:63],v[108:111]\n"
"v_exp_f32 v28,v28\n"
"v_cvt_pk_bf16_f32 v77,v26,v27\n"
"v_mfma_f32_16x16x32_bf16 v[56:59],a[104:107],a[56:59],v[56:59]\n"
"v_exp_f32 v29,v29\n"
"v_mfma_f32_16x16x32_bf16 v[56:59],a[100:103],a[52:55],v[56:59]\n"
"v_exp_f32 v30,v30\n"
"v_cvt_pk_bf16_f32 v78,v28,v29\n"
"v_mfma_f32_16x16x32_bf16 v[56:59],a[96:99],a[48:51],v[56:59]\n"
"s_waitcnt vmcnt(17) lgkmcnt(0)\n"
"v_exp_f32 v31,v31\n"
"v_cvt_pk_bf16_f32 v76,v24,v25\n"
"v_cvt_pk_bf16_f32 v79,v30,v31\n"
"s_barrier\n"
"v_mfma_f32_16x16x32_bf16 v[0:3],a[64:67],a[0:3],v[96:99]\n"
"s_add_u32 m0,s55,8192\n"
"v_exp_f32 v122,v32\n"
"v_mfma_f32_16x16x32_bf16 v[0:3],a[68:71],a[4:7],v[0:3]\n"
"s_add_u32 s54,s54,s22\n"
"v_exp_f32 v123,v33\n"
"v_mfma_f32_16x16x32_bf16 v[0:3],a[72:75],a[8:11],v[0:3]\n"
"ds_read_b128 a[96:99],v114 offset:40960\n"
"ds_read_b128 a[100:103],v114 offset:41984\n"
"v_mfma_f32_16x16x32_bf16 v[0:3],a[76:79],a[12:15],v[0:3]\n"
"v_exp_f32 v119,v34\n"
"v_mfma_f32_16x16x32_bf16 v[4:7],a[92:95],a[12:15],v[96:99]\n"
"ds_read_b128 a[104:107],v114 offset:43008\n"
"v_cvt_pk_bf16_f32 v80,v122,v123\n"
"v_mfma_f32_16x16x32_bf16 v[4:7],a[88:91],a[8:11],v[4:7]\n"
"v_exp_f32 v120,v35\n"
"v_mfma_f32_16x16x32_bf16 v[4:7],a[84:87],a[4:7],v[4:7]\n"
"ds_read_b128 a[108:111],v114 offset:44032\n"
"ds_read_b128 a[112:115],v114 offset:45056\n"
"v_mfma_f32_16x16x32_bf16 v[4:7],a[80:83],a[0:3],v[4:7]\n"
"v_exp_f32 v122,v36\n"
"v_mfma_f32_16x16x32_bf16 v[12:15],a[80:83],a[16:19],v[100:103]\n"
"ds_read_b128 a[116:119],v114 offset:46080\n"
"v_cvt_pk_bf16_f32 v81,v119,v120\n"
"v_mfma_f32_16x16x32_bf16 v[12:15],a[84:87],a[20:23],v[12:15]\n"
"v_exp_f32 v123,v37\n"
"v_mfma_f32_16x16x32_bf16 v[12:15],a[88:91],a[24:27],v[12:15]\n"
"ds_read_b128 a[120:123],v114 offset:47104\n"
"ds_read_b128 a[124:127],v114 offset:48128\n"
"v_mfma_f32_16x16x32_bf16 v[12:15],a[92:95],a[28:31],v[12:15]\n"
"v_exp_f32 v119,v38\n"
"v_cvt_pk_bf16_f32 v82,v122,v123\n"
"v_mfma_f32_16x16x32_bf16 v[8:11],a[76:79],a[28:31],v[100:103]\n"
"buffer_store_dwordx4 a[168:171],v112,s[24:27],s3 offen offset:2048 sc1\n"
"v_mfma_f32_16x16x32_bf16 v[8:11],a[72:75],a[24:27],v[8:11]\n"
"v_exp_f32 v120,v39\n"
"v_mfma_f32_16x16x32_bf16 v[8:11],a[68:71],a[20:23],v[8:11]\n"
"buffer_load_dwordx4 v115,s[32:35],s54 offen sc1 lds\n"
"v_mfma_f32_16x16x32_bf16 v[8:11],a[64:67],a[16:19],v[8:11]\n"
"s_add_u32 m0,s67,49152\n"
"v_exp_f32 v122,v40\n"
"v_mfma_f32_16x16x32_bf16 v[16:19],a[64:67],a[32:35],v[104:107]\n"
"v_cvt_pk_bf16_f32 v83,v119,v120\n"
"v_or3_b32 v118,v80,v81,v82\n"
"v_mfma_f32_16x16x32_bf16 v[16:19],a[68:71],a[36:39],v[16:19]\n"
"v_exp_f32 v123,v41\n"
"v_mfma_f32_16x16x32_bf16 v[16:19],a[72:75],a[40:43],v[16:19]\n"
"buffer_load_dwordx4 v112,s[24:27],s64 offen offset:0 sc1 lds\n"
"v_mfma_f32_16x16x32_bf16 v[16:19],a[76:79],a[44:47],v[16:19]\n"
"v_exp_f32 v119,v42\n"
"v_mfma_f32_16x16x32_bf16 v[20:23],a[92:95],a[44:47],v[104:107]\n"
"v_cvt_pk_bf16_f32 v84,v122,v123\n"
"v_or3_b32 v118,v118,v83,v84\n"
"v_mfma_f32_16x16x32_bf16 v[20:23],a[88:91],a[40:43],v[20:23]\n"
"v_exp_f32 v120,v43\n"
"v_mfma_f32_16x16x32_bf16 v[20:23],a[84:87],a[36:39],v[20:23]\n"
"buffer_load_dwordx4 v112,s[24:27],s64 offen offset:1024 sc1 lds\n"
"v_mfma_f32_16x16x32_bf16 v[20:23],a[80:83],a[32:35],v[20:23]\n"
"v_exp_f32 v122,v44\n"
"v_mfma_f32_16x16x32_bf16 v[28:31],a[80:83],a[48:51],v[108:111]\n"
"v_cvt_pk_bf16_f32 v85,v119,v120\n"
"v_mfma_f32_16x16x32_bf16 v[28:31],a[84:87],a[52:55],v[28:31]\n"
"v_exp_f32 v123,v45\n"
"v_mfma_f32_16x16x32_bf16 v[28:31],a[88:91],a[56:59],v[28:31]\n"
"buffer_load_dwordx4 v112,s[24:27],s64 offen offset:2048 sc1 lds\n"
"v_mfma_f32_16x16x32_bf16 v[28:31],a[92:95],a[60:63],v[28:31]\n"
"v_exp_f32 v119,v46\n"
"v_cvt_pk_bf16_f32 v86,v122,v123\n"
"v_mfma_f32_16x16x32_bf16 v[24:27],a[76:79],a[60:63],v[108:111]\n"
"buffer_store_dwordx4 a[172:175],v112,s[24:27],s3 offen offset:3072 sc1\n"
"v_mfma_f32_16x16x32_bf16 v[24:27],a[72:75],a[56:59],v[24:27]\n"
"v_exp_f32 v120,v47\n"
"v_mfma_f32_16x16x32_bf16 v[24:27],a[68:71],a[52:55],v[24:27]\n"
"buffer_load_dwordx4 v112,s[24:27],s64 offen offset:3072 sc1 lds\n"
"v_mfma_f32_16x16x32_bf16 v[24:27],a[64:67],a[48:51],v[24:27]\n"
"s_waitcnt lgkmcnt(0)\n"
"v_exp_f32 v122,v48\n"
"v_mfma_f32_16x16x32_bf16 v[128:131],a[96:99],v[64:67],0\n"
"v_exp_f32 v123,v49\n"
"v_mfma_f32_16x16x32_bf16 v[160:163],a[96:99],v[68:71],0\n"
"ds_read_b128 a[64:67],v113 offset:32768\n"
"v_cvt_pk_bf16_f32 v87,v119,v120\n"
"v_mfma_f32_16x16x32_bf16 v[192:195],a[96:99],v[72:75],0\n"
"s_add_u32 m0,s67,53248\n"
"v_exp_f32 v119,v50\n"
"v_mfma_f32_16x16x32_bf16 v[224:227],a[96:99],v[76:79],0\n"
"ds_read_b128 a[68:71],v113 offset:33792\n"
"v_cvt_pk_bf16_f32 v88,v122,v123\n"
"v_mfma_f32_16x16x32_bf16 a[140:143],a[144:147],v[76:79],0\n"
"v_exp_f32 v120,v51\n"
"v_mfma_f32_16x16x32_bf16 v[228:231],a[100:103],v[76:79],0\n"
"ds_read_b128 a[72:75],v113 offset:34816\n"
"ds_read_b128 a[76:79],v113 offset:35840\n"
"v_mfma_f32_16x16x32_bf16 v[132:135],a[100:103],v[64:67],0\n"
"v_exp_f32 v122,v52\n"
"v_mfma_f32_16x16x32_bf16 v[164:167],a[100:103],v[68:71],0\n"
"ds_read_b128 a[80:83],v113 offset:36864\n"
"v_cvt_pk_bf16_f32 v89,v119,v120\n"
"v_mfma_f32_16x16x32_bf16 v[196:199],a[100:103],v[72:75],0\n"
"v_exp_f32 v123,v53\n"
"v_mfma_f32_16x16x32_bf16 a[136:139],a[144:147],v[72:75],0\n"
"ds_read_b128 a[84:87],v113 offset:37888\n"
"ds_read_b128 a[88:91],v113 offset:38912\n"
"v_mfma_f32_16x16x32_bf16 v[200:203],a[104:107],v[72:75],0\n"
"v_exp_f32 v119,v54\n"
"v_mfma_f32_16x16x32_bf16 v[232:235],a[104:107],v[76:79],0\n"
"ds_read_b128 a[92:95],v113 offset:39936\n"
"v_cvt_pk_bf16_f32 v90,v122,v123\n"
"v_mfma_f32_16x16x32_bf16 v[136:139],a[104:107],v[64:67],0\n"
"v_exp_f32 v120,v55\n"
"v_mfma_f32_16x16x32_bf16 v[168:171],a[104:107],v[68:71],0\n"
"buffer_load_dwordx4 v117,s[24:27],s64 offen offset:0 sc1 lds\n"
"v_mfma_f32_16x16x32_bf16 a[132:135],a[144:147],v[68:71],0\n"
"v_exp_f32 v122,v56\n"
"v_mfma_f32_16x16x32_bf16 v[172:175],a[108:111],v[68:71],0\n"
"v_cvt_pk_bf16_f32 v91,v119,v120\n"
"s_sub_u32 s3,s3,16384\n"
"v_mfma_f32_16x16x32_bf16 v[204:207],a[108:111],v[72:75],0\n"
"v_exp_f32 v123,v57\n"
"v_mfma_f32_16x16x32_bf16 v[236:239],a[108:111],v[76:79],0\n"
"buffer_load_dwordx4 v117,s[24:27],s64 offen offset:1024 sc1 lds\n"
"v_mfma_f32_16x16x32_bf16 v[140:143],a[108:111],v[64:67],0\n"
"v_exp_f32 v119,v58\n"
"v_cvt_pk_bf16_f32 v92,v122,v123\n"
"v_mfma_f32_16x16x32_bf16 a[128:131],a[144:147],v[64:67],0\n"
"buffer_store_dwordx4 a[176:179],v112,s[24:27],s3 offen offset:0 sc1\n"
"v_mfma_f32_16x16x32_bf16 v[144:147],a[112:115],v[64:67],0\n"
"v_exp_f32 v120,v59\n"
"v_mfma_f32_16x16x32_bf16 v[176:179],a[112:115],v[68:71],0\n"
"buffer_load_dwordx4 v117,s[24:27],s64 offen offset:2048 sc1 lds\n"
"v_mfma_f32_16x16x32_bf16 v[208:211],a[112:115],v[72:75],0\n"
"v_exp_f32 v122,v60\n"
"v_mfma_f32_16x16x32_bf16 v[240:243],a[112:115],v[76:79],0\n"
"v_cvt_pk_bf16_f32 v93,v119,v120\n"
"v_or3_b32 v118,v118,v85,v86\n"
"v_mfma_f32_16x16x32_bf16 v[244:247],a[116:119],v[76:79],0\n"
"v_exp_f32 v123,v61\n"
"v_mfma_f32_16x16x32_bf16 v[148:151],a[116:119],v[64:67],0\n"
"buffer_load_dwordx4 v117,s[24:27],s64 offen offset:3072 sc1 lds\n"
"v_mfma_f32_16x16x32_bf16 v[180:183],a[116:119],v[68:71],0\n"
"s_add_u32 s64,s64,16384\n"
"v_exp_f32 v119,v62\n"
"v_cvt_pk_bf16_f32 v94,v122,v123\n"
"v_mfma_f32_16x16x32_bf16 v[212:215],a[116:119],v[72:75],0\n"
"buffer_store_dwordx4 a[232:235],v112,s[24:27],s56 offen offset:2048 sc1\n"
"v_mfma_f32_16x16x32_bf16 v[216:219],a[120:123],v[72:75],0\n"
"v_exp_f32 v120,v63\n"
"v_mfma_f32_16x16x32_bf16 v[248:251],a[120:123],v[76:79],0\n"
"v_cvt_pk_bf16_f32 v95,v119,v120\n"
"v_or3_b32 v118,v118,v87,v88\n"
"v_mfma_f32_16x16x32_bf16 v[152:155],a[120:123],v[64:67],0\n"
"v_or3_b32 v118,v118,v89,v90\n"
"v_or3_b32 v118,v118,v91,v92\n"
"v_mfma_f32_16x16x32_bf16 v[184:187],a[120:123],v[68:71],0\n"
"v_or3_b32 v118,v118,v93,v94\n"
"v_or_b32 v118,v118,v95\n"
"v_mfma_f32_16x16x32_bf16 v[188:191],a[124:127],v[68:71],0\n"
"v_and_b32 v118,0x40004000,v118\n"
"v_cmp_ne_u32 vcc,0,v118\n"
"v_mfma_f32_16x16x32_bf16 v[220:223],a[124:127],v[72:75],0\n"
"v_mfma_f32_16x16x32_bf16 v[252:255],a[124:127],v[76:79],0\n"
"v_mfma_f32_16x16x32_bf16 v[156:159],a[124:127],v[64:67],0\n"
"s_cbranch_vccnz .Lfk_cs_h1\n"
".Lfk_cr_h1:\n"
"s_waitcnt lgkmcnt(0)\n"
"s_barrier\n"
"v_mfma_f32_16x16x32_bf16 v[32:35],a[64:67],a[0:3],v[96:99]\n"
"s_add_u32 m0,s55,12288\n"
"v_exp_f32 v122,v0\n"
"v_mfma_f32_16x16x32_bf16 v[32:35],a[68:71],a[4:7],v[32:35]\n"
"s_add_u32 s54,s54,s22\n"
"v_exp_f32 v123,v1\n"
"v_mfma_f32_16x16x32_bf16 v[32:35],a[72:75],a[8:11],v[32:35]\n"
"ds_read_b128 a[96:99],v114 offset:8192\n"
"ds_read_b128 a[100:103],v114 offset:9216\n"
"v_mfma_f32_16x16x32_bf16 v[32:35],a[76:79],a[12:15],v[32:35]\n"
"v_exp_f32 v119,v2\n"
"v_mfma_f32_16x16x32_bf16 v[36:39],a[92:95],a[12:15],v[96:99]\n"
"ds_read_b128 a[104:107],v114 offset:10240\n"
"v_cvt_pk_bf16_f32 v64,v122,v123\n"
"v_mfma_f32_16x16x32_bf16 v[36:39],a[88:91],a[8:11],v[36:39]\n"
"v_exp_f32 v120,v3\n"
"v_mfma_f32_16x16x32_bf16 v[36:39],a[84:87],a[4:7],v[36:39]\n"
"ds_read_b128 a[108:111],v114 offset:11264\n"
"ds_read_b128 a[112:115],v114 offset:12288\n"
"v_mfma_f32_16x16x32_bf16 v[36:39],a[80:83],a[0:3],v[36:39]\n"
"v_exp_f32 v122,v4\n"
"v_mfma_f32_16x16x32_bf16 v[44:47],a[80:83],a[16:19],v[100:103]\n"
"ds_read_b128 a[116:119],v114 offset:13312\n"
"v_cvt_pk_bf16_f32 v65,v119,v120\n"
"v_mfma_f32_16x16x32_bf16 v[44:47],a[84:87],a[20:23],v[44:47]\n"
"v_exp_f32 v123,v5\n"
"v_mfma_f32_16x16x32_bf16 v[44:47],a[88:91],a[24:27],v[44:47]\n"
"ds_read_b128 a[120:123],v114 offset:14336\n"
"ds_read_b128 a[124:127],v114 offset:15360\n"
"v_mfma_f32_16x16x32_bf16 v[44:47],a[92:95],a[28:31],v[44:47]\n"
"v_exp_f32 v119,v6\n"
"v_cvt_pk_bf16_f32 v66,v122,v123\n"
"v_mfma_f32_16x16x32_bf16 v[40:43],a[76:79],a[28:31],v[100:103]\n"
"buffer_store_dwordx4 a[180:183],v112,s[24:27],s3 offen offset:1024 sc1\n"
"v_mfma_f32_16x16x32_bf16 v[40:43],a[72:75],a[24:27],v[40:43]\n"
"v_exp_f32 v120,v7\n"
"v_mfma_f32_16x16x32_bf16 v[40:43],a[68:71],a[20:23],v[40:43]\n"
"buffer_load_dwordx4 v115,s[32:35],s54 offen sc1 lds\n"
"v_mfma_f32_16x16x32_bf16 v[40:43],a[64:67],a[16:19],v[40:43]\n"
"s_add_u32 m0,s67,32768\n"
"v_exp_f32 v122,v8\n"
"v_mfma_f32_16x16x32_bf16 v[48:51],a[64:67],a[32:35],v[104:107]\n"
"v_cvt_pk_bf16_f32 v67,v119,v120\n"
"v_or3_b32 v118,v64,v65,v66\n"
"v_mfma_f32_16x16x32_bf16 v[48:51],a[68:71],a[36:39],v[48:51]\n"
"v_exp_f32 v123,v9\n"
"v_mfma_f32_16x16x32_bf16 v[48:51],a[72:75],a[40:43],v[48:51]\n"
"buffer_load_dwordx4 v112,s[24:27],s64 offen offset:0 sc1 lds\n"
"v_mfma_f32_16x16x32_bf16 v[48:51],a[76:79],a[44:47],v[48:51]\n"
"v_exp_f32 v119,v10\n"
"v_mfma_f32_16x16x32_bf16 v[52:55],a[92:95],a[44:47],v[104:107]\n"
"v_cvt_pk_bf16_f32 v68,v122,v123\n"
"v_or3_b32 v118,v118,v67,v68\n"
"v_mfma_f32_16x16x32_bf16 v[52:55],a[88:91],a[40:43],v[52:55]\n"
"v_exp_f32 v120,v11\n"
"v_mfma_f32_16x16x32_bf16 v[52:55],a[84:87],a[36:39],v[52:55]\n"
"buffer_load_dwordx4 v112,s[24:27],s64 offen offset:1024 sc1 lds\n"
"v_mfma_f32_16x16x32_bf16 v[52:55],a[80:83],a[32:35],v[52:55]\n"
"v_exp_f32 v122,v12\n"
"v_mfma_f32_16x16x32_bf16 v[60:63],a[80:83],a[48:51],v[108:111]\n"
"v_cvt_pk_bf16_f32 v69,v119,v120\n"
"v_mfma_f32_16x16x32_bf16 v[60:63],a[84:87],a[52:55],v[60:63]\n"
"v_exp_f32 v123,v13\n"
"v_mfma_f32_16x16x32_bf16 v[60:63],a[88:91],a[56:59],v[60:63]\n"
"buffer_load_dwordx4 v112,s[24:27],s64 offen offset:2048 sc1 lds\n"
"v_mfma_f32_16x16x32_bf16 v[60:63],a[92:95],a[60:63],v[60:63]\n"
"v_exp_f32 v119,v14\n"
"v_cvt_pk_bf16_f32 v70,v122,v123\n"
"v_mfma_f32_16x16x32_bf16 v[56:59],a[76:79],a[60:63],v[108:111]\n"
"buffer_store_dwordx4 a[184:187],v112,s[24:27],s3 offen offset:2048 sc1\n"
"v_mfma_f32_16x16x32_bf16 v[56:59],a[72:75],a[56:59],v[56:59]\n"
"v_exp_f32 v120,v15\n"
"v_mfma_f32_16x16x32_bf16 v[56:59],a[68:71],a[52:55],v[56:59]\n"
"buffer_load_dwordx4 v112,s[24:27],s64 offen offset:3072 sc1 lds\n"
"v_mfma_f32_16x16x32_bf16 v[56:59],a[64:67],a[48:51],v[56:59]\n"
"s_waitcnt lgkmcnt(0)\n"
"v_exp_f32 v122,v16\n"
"v_mfma_f32_16x16x32_bf16 v[128:131],a[96:99],v[80:83],v[128:131]\n"
"v_exp_f32 v123,v17\n"
"v_mfma_f32_16x16x32_bf16 v[160:163],a[96:99],v[84:87],v[160:163]\n"
"ds_read_b128 a[64:67],v113 offset:0\n"
"v_cvt_pk_bf16_f32 v71,v119,v120\n"
"v_mfma_f32_16x16x32_bf16 v[192:195],a[96:99],v[88:91],v[192:195]\n"
"s_add_u32 m0,s67,36864\n"
"v_exp_f32 v119,v18\n"
"v_mfma_f32_16x16x32_bf16 v[224:227],a[96:99],v[92:95],v[224:227]\n"
"ds_read_b128 a[68:71],v113 offset:1024\n"
"v_cvt_pk_bf16_f32 v72,v122,v123\n"
"v_mfma_f32_16x16x32_bf16 a[140:143],a[144:147],v[92:95],a[140:143]\n"
"v_exp_f32 v120,v19\n"
"v_mfma_f32_16x16x32_bf16 v[228:231],a[100:103],v[92:95],v[228:231]\n"
"ds_read_b128 a[72:75],v113 offset:2048\n"
"ds_read_b128 a[76:79],v113 offset:3072\n"
"v_mfma_f32_16x16x32_bf16 v[132:135],a[100:103],v[80:83],v[132:135]\n"
"v_exp_f32 v122,v20\n"
"v_mfma_f32_16x16x32_bf16 v[164:167],a[100:103],v[84:87],v[164:167]\n"
"ds_read_b128 a[80:83],v113 offset:4096\n"
"v_cvt_pk_bf16_f32 v73,v119,v120\n"
"v_mfma_f32_16x16x32_bf16 v[196:199],a[100:103],v[88:91],v[196:199]\n"
"v_exp_f32 v123,v21\n"
"v_mfma_f32_16x16x32_bf16 a[136:139],a[144:147],v[88:91],a[136:139]\n"
"ds_read_b128 a[84:87],v113 offset:5120\n"
"ds_read_b128 a[88:91],v113 offset:6144\n"
"v_mfma_f32_16x16x32_bf16 v[200:203],a[104:107],v[88:91],v[200:203]\n"
"v_exp_f32 v119,v22\n"
"v_mfma_f32_16x16x32_bf16 v[232:235],a[104:107],v[92:95],v[232:235]\n"
"ds_read_b128 a[92:95],v113 offset:7168\n"
"v_cvt_pk_bf16_f32 v74,v122,v123\n"
"v_mfma_f32_16x16x32_bf16 v[136:139],a[104:107],v[80:83],v[136:139]\n"
"v_exp_f32 v120,v23\n"
"v_mfma_f32_16x16x32_bf16 v[168:171],a[104:107],v[84:87],v[168:171]\n"
"buffer_load_dwordx4 v117,s[24:27],s64 offen offset:0 sc1 lds\n"
"v_mfma_f32_16x16x32_bf16 a[132:135],a[144:147],v[84:87],a[132:135]\n"
"v_exp_f32 v122,v24\n"
"v_cvt_pk_bf16_f32 v75,v119,v120\n"
"v_mfma_f32_16x16x32_bf16 v[172:175],a[108:111],v[84:87],v[172:175]\n"
"buffer_store_dwordx4 a[188:191],v112,s[24:27],s3 offen offset:3072 sc1\n"
"v_mfma_f32_16x16x32_bf16 v[204:207],a[108:111],v[88:91],v[204:207]\n"
"v_exp_f32 v123,v25\n"
"v_mfma_f32_16x16x32_bf16 v[236:239],a[108:111],v[92:95],v[236:239]\n"
"buffer_load_dwordx4 v117,s[24:27],s64 offen offset:1024 sc1 lds\n"
"v_mfma_f32_16x16x32_bf16 v[140:143],a[108:111],v[80:83],v[140:143]\n"
"v_exp_f32 v119,v26\n"
"v_mfma_f32_16x16x32_bf16 a[128:131],a[144:147],v[80:83],a[128:131]\n"
"v_cvt_pk_bf16_f32 v76,v122,v123\n"
"s_or_b32 s3,s3,0x3c000\n"
"v_mfma_f32_16x16x32_bf16 v[144:147],a[112:115],v[80:83],v[144:147]\n"
"v_exp_f32 v120,v27\n"
"v_mfma_f32_16x16x32_bf16 v[176:179],a[112:115],v[84:87],v[176:179]\n"
"buffer_load_dwordx4 v117,s[24:27],s64 offen offset:2048 sc1 lds\n"
"v_mfma_f32_16x16x32_bf16 v[208:211],a[112:115],v[88:91],v[208:211]\n"
"v_exp_f32 v122,v28\n"
"v_mfma_f32_16x16x32_bf16 v[240:243],a[112:115],v[92:95],v[240:243]\n"
"v_cvt_pk_bf16_f32 v77,v119,v120\n"
"v_or3_b32 v118,v118,v69,v70\n"
"v_mfma_f32_16x16x32_bf16 v[244:247],a[116:119],v[92:95],v[244:247]\n"
"v_exp_f32 v123,v29\n"
"v_mfma_f32_16x16x32_bf16 v[148:151],a[116:119],v[80:83],v[148:151]\n"
"buffer_load_dwordx4 v117,s[24:27],s64 offen offset:3072 sc1 lds\n"
"v_mfma_f32_16x16x32_bf16 v[180:183],a[116:119],v[84:87],v[180:183]\n"
"s_add_u32 s64,s64,16384\n"
"v_exp_f32 v119,v30\n"
"v_cvt_pk_bf16_f32 v78,v122,v123\n"
"v_mfma_f32_16x16x32_bf16 v[212:215],a[116:119],v[88:91],v[212:215]\n"
"buffer_store_dwordx4 a[236:239],v112,s[24:27],s56 offen offset:3072 sc1\n"
"v_mfma_f32_16x16x32_bf16 v[216:219],a[120:123],v[88:91],v[216:219]\n"
"v_exp_f32 v120,v31\n"
"v_mfma_f32_16x16x32_bf16 v[248:251],a[120:123],v[92:95],v[248:251]\n"
"v_cvt_pk_bf16_f32 v79,v119,v120\n"
"v_or3_b32 v118,v118,v71,v72\n"
"v_mfma_f32_16x16x32_bf16 v[152:155],a[120:123],v[80:83],v[152:155]\n"
"v_or3_b32 v118,v118,v73,v74\n"
"v_or3_b32 v118,v118,v75,v76\n"
"v_mfma_f32_16x16x32_bf16 v[184:187],a[120:123],v[84:87],v[184:187]\n"
"v_or3_b32 v118,v118,v77,v78\n"
"v_or_b32 v118,v118,v79\n"
"v_mfma_f32_16x16x32_bf16 v[188:191],a[124:127],v[84:87],v[188:191]\n"
"v_and_b32 v118,0x40004000,v118\n"
"v_cmp_ne_u32 vcc,0,v118\n"
"v_mfma_f32_16x16x32_bf16 v[220:223],a[124:127],v[88:91],v[220:223]\n"
"v_mfma_f32_16x16x32_bf16 v[252:255],a[124:127],v[92:95],v[252:255]\n"
"v_mfma_f32_16x16x32_bf16 v[156:159],a[124:127],v[80:83],v[156:159]\n"
"s_cbranch_vccnz .Lfk_cs_h2\n"
".Lfk_cr_h2:\n"
"s_waitcnt vmcnt(27) lgkmcnt(0)\n"
"s_barrier\n"
".Lfk_g1:\n"
"s_and_b32 s82,s78,4096\n"
"s_lshl1_add_u32 s82,s82,69632\n"
"s_cmp_lt_u32 s52,2\n"
"s_cselect_b32 s64,s64,s62\n"
"s_cselect_b32 s67,s67,s82\n"
"s_add_u32 s89,s89,1\n"
"s_cmp_ge_u32 s89,s60\n"
"s_cbranch_scc1 .Lfk_Fx\n"
".Lfk_F:\n"
"v_mfma_f32_16x16x32_bf16 v[0:3],a[64:67],a[0:3],v[96:99]\n"
"s_swappc_b64 s[94:95],s[20:21]\n"
"v_exp_f32 v122,v32\n"
"v_mfma_f32_16x16x32_bf16 v[0:3],a[68:71],a[4:7],v[0:3]\n"
"v_exp_f32 v123,v33\n"
"v_mfma_f32_16x16x32_bf16 v[0:3],a[72:75],a[8:11],v[0:3]\n"
"ds_read_b128 a[96:99],v114 offset:24576\n"
"ds_read_b128 a[100:103],v114 offset:25600\n"
"v_mfma_f32_16x16x32_bf16 v[0:3],a[76:79],a[12:15],v[0:3]\n"
"v_exp_f32 v119,v34\n"
"v_mfma_f32_16x16x32_bf16 v[4:7],a[92:95],a[12:15],v[96:99]\n"
"s_add_u32 s101,s101,4\n"
"ds_read_b128 a[104:107],v114 offset:26624\n"
"v_cvt_pk_bf16_f32 v80,v122,v123\n"
"v_mfma_f32_16x16x32_bf16 v[4:7],a[88:91],a[8:11],v[4:7]\n"
"v_exp_f32 v120,v35\n"
"v_mfma_f32_16x16x32_bf16 v[4:7],a[84:87],a[4:7],v[4:7]\n"
"ds_read_b128 a[108:111],v114 offset:27648\n"
"ds_read_b128 a[112:115],v114 offset:28672\n"
"v_mfma_f32_16x16x32_bf16 v[4:7],a[80:83],a[0:3],v[4:7]\n"
"v_exp_f32 v122,v36\n"
"v_mfma_f32_16x16x32_bf16 v[12:15],a[80:83],a[16:19],v[100:103]\n"
"ds_read_b128 a[116:119],v114 offset:29696\n"
"v_cvt_pk_bf16_f32 v81,v119,v120\n"
"v_mfma_f32_16x16x32_bf16 v[12:15],a[84:87],a[20:23],v[12:15]\n"
"v_exp_f32 v123,v37\n"
"v_mfma_f32_16x16x32_bf16 v[12:15],a[88:91],a[24:27],v[12:15]\n"
"ds_read_b128 a[120:123],v114 offset:30720\n"
"ds_read_b128 a[124:127],v114 offset:31744\n"
"v_mfma_f32_16x16x32_bf16 v[12:15],a[92:95],a[28:31],v[12:15]\n"
"s_add_u32 m0,s67,0\n"
"v_exp_f32 v119,v38\n"
"v_mfma_f32_16x16x32_bf16 v[8:11],a[76:79],a[28:31],v[100:103]\n"
"v_cvt_pk_bf16_f32 v82,v122,v123\n"
"v_or3_b32 v118,v80,v81,v82\n"
"v_mfma_f32_16x16x32_bf16 v[8:11],a[72:75],a[24:27],v[8:11]\n"
"v_exp_f32 v120,v39\n"
"v_mfma_f32_16x16x32_bf16 v[8:11],a[68:71],a[20:23],v[8:11]\n"
"buffer_load_dwordx4 v112,s[24:27],s64 offen offset:0 sc1 lds\n"
"v_mfma_f32_16x16x32_bf16 v[8:11],a[64:67],a[16:19],v[8:11]\n"
"v_exp_f32 v122,v40\n"
"v_mfma_f32_16x16x32_bf16 v[16:19],a[64:67],a[32:35],v[104:107]\n"
"v_cvt_pk_bf16_f32 v83,v119,v120\n"
"v_mfma_f32_16x16x32_bf16 v[16:19],a[68:71],a[36:39],v[16:19]\n"
"v_exp_f32 v123,v41\n"
"v_mfma_f32_16x16x32_bf16 v[16:19],a[72:75],a[40:43],v[16:19]\n"
"v_cvt_pk_bf16_f32 v84,v122,v123\n"
"v_or3_b32 v118,v118,v83,v84\n"
"v_mfma_f32_16x16x32_bf16 v[16:19],a[76:79],a[44:47],v[16:19]\n"
"v_exp_f32 v119,v42\n"
"v_mfma_f32_16x16x32_bf16 v[20:23],a[92:95],a[44:47],v[104:107]\n"
"v_mfma_f32_16x16x32_bf16 v[20:23],a[88:91],a[40:43],v[20:23]\n"
"v_exp_f32 v120,v43\n"
"v_mfma_f32_16x16x32_bf16 v[20:23],a[84:87],a[36:39],v[20:23]\n"
"buffer_load_dwordx4 v112,s[24:27],s64 offen offset:1024 sc1 lds\n"
"v_mfma_f32_16x16x32_bf16 v[20:23],a[80:83],a[32:35],v[20:23]\n"
"v_exp_f32 v122,v44\n"
"v_mfma_f32_16x16x32_bf16 v[28:31],a[80:83],a[48:51],v[108:111]\n"
"v_cvt_pk_bf16_f32 v85,v119,v120\n"
"v_mfma_f32_16x16x32_bf16 v[28:31],a[84:87],a[52:55],v[28:31]\n"
"v_exp_f32 v123,v45\n"
"v_mfma_f32_16x16x32_bf16 v[28:31],a[88:91],a[56:59],v[28:31]\n"
"v_cvt_pk_bf16_f32 v86,v122,v123\n"
"v_or3_b32 v118,v118,v85,v86\n"
"v_mfma_f32_16x16x32_bf16 v[28:31],a[92:95],a[60:63],v[28:31]\n"
"v_exp_f32 v119,v46\n"
"v_mfma_f32_16x16x32_bf16 v[24:27],a[76:79],a[60:63],v[108:111]\n"
"v_mfma_f32_16x16x32_bf16 v[24:27],a[72:75],a[56:59],v[24:27]\n"
"v_exp_f32 v120,v47\n"
"v_mfma_f32_16x16x32_bf16 v[24:27],a[68:71],a[52:55],v[24:27]\n"
"v_cvt_pk_bf16_f32 v87,v119,v120\n"
"v_mfma_f32_16x16x32_bf16 v[24:27],a[64:67],a[48:51],v[24:27]\n"
"s_waitcnt lgkmcnt(0)\n"
"v_exp_f32 v122,v48\n"
"v_mfma_f32_16x16x32_bf16 v[128:131],a[96:99],v[64:67],v[128:131]\n"
"v_exp_f32 v123,v49\n"
"v_mfma_f32_16x16x32_bf16 v[160:163],a[96:99],v[68:71],v[160:163]\n"
"ds_read_b128 a[64:67],v113 offset:16384\n"
"ds_read_b128 a[68:71],v113 offset:17408\n"
"v_mfma_f32_16x16x32_bf16 v[192:195],a[96:99],v[72:75],v[192:195]\n"
"v_exp_f32 v119,v50\n"
"v_mfma_f32_16x16x32_bf16 v[224:227],a[96:99],v[76:79],v[224:227]\n"
"ds_read_b128 a[72:75],v113 offset:18432\n"
"v_cvt_pk_bf16_f32 v88,v122,v123\n"
"v_mfma_f32_16x16x32_bf16 a[140:143],a[144:147],v[76:79],a[140:143]\n"
"v_exp_f32 v120,v51\n"
"v_mfma_f32_16x16x32_bf16 v[228:231],a[100:103],v[76:79],v[228:231]\n"
"ds_read_b128 a[76:79],v113 offset:19456\n"
"ds_read_b128 a[80:83],v113 offset:20480\n"
"v_mfma_f32_16x16x32_bf16 v[132:135],a[100:103],v[64:67],v[132:135]\n"
"v_exp_f32 v122,v52\n"
"v_mfma_f32_16x16x32_bf16 v[164:167],a[100:103],v[68:71],v[164:167]\n"
"ds_read_b128 a[84:87],v113 offset:21504\n"
"v_cvt_pk_bf16_f32 v89,v119,v120\n"
"v_mfma_f32_16x16x32_bf16 v[196:199],a[100:103],v[72:75],v[196:199]\n"
"v_exp_f32 v123,v53\n"
"v_mfma_f32_16x16x32_bf16 a[136:139],a[144:147],v[72:75],a[136:139]\n"
"ds_read_b128 a[88:91],v113 offset:22528\n"
"ds_read_b128 a[92:95],v113 offset:23552\n"
"v_mfma_f32_16x16x32_bf16 v[200:203],a[104:107],v[72:75],v[200:203]\n"
"v_exp_f32 v119,v54\n"
"v_mfma_f32_16x16x32_bf16 v[232:235],a[104:107],v[76:79],v[232:235]\n"
"v_cvt_pk_bf16_f32 v90,v122,v123\n"
"v_or3_b32 v118,v118,v87,v88\n"
"v_mfma_f32_16x16x32_bf16 v[136:139],a[104:107],v[64:67],v[136:139]\n"
"v_exp_f32 v120,v55\n"
"v_mfma_f32_16x16x32_bf16 v[168:171],a[104:107],v[68:71],v[168:171]\n"
"s_waitcnt vmcnt(9)\n"
"ds_add_u32 v124,a152\n"
"v_mfma_f32_16x16x32_bf16 a[132:135],a[144:147],v[68:71],a[132:135]\n"
"v_exp_f32 v122,v56\n"
"v_mfma_f32_16x16x32_bf16 v[172:175],a[108:111],v[68:71],v[172:175]\n"
"v_cvt_pk_bf16_f32 v91,v119,v120\n"
"v_or3_b32 v118,v118,v89,v90\n"
"v_mfma_f32_16x16x32_bf16 v[204:207],a[108:111],v[72:75],v[204:207]\n"
"v_exp_f32 v123,v57\n"
"v_mfma_f32_16x16x32_bf16 v[236:239],a[108:111],v[76:79],v[236:239]\n"
"buffer_load_dwordx4 v112,s[24:27],s64 offen offset:2048 sc1 lds\n"
"v_mfma_f32_16x16x32_bf16 v[140:143],a[108:111],v[64:67],v[140:143]\n"
"v_exp_f32 v119,v58\n"
"v_mfma_f32_16x16x32_bf16 a[128:131],a[144:147],v[64:67],a[128:131]\n"
"v_cvt_pk_bf16_f32 v92,v122,v123\n"
"v_or3_b32 v118,v118,v91,v92\n"
"v_mfma_f32_16x16x32_bf16 v[144:147],a[112:115],v[64:67],v[144:147]\n"
"v_exp_f32 v120,v59\n"
"v_mfma_f32_16x16x32_bf16 v[176:179],a[112:115],v[68:71],v[176:179]\n"
"buffer_load_dwordx4 v112,s[24:27],s64 offen offset:3072 sc1 lds\n"
"v_mfma_f32_16x16x32_bf16 v[208:211],a[112:115],v[72:75],v[208:211]\n"
"s_add_u32 s64,s64,16384\n"
"v_exp_f32 v122,v60\n"
"v_mfma_f32_16x16x32_bf16 v[240:243],a[112:115],v[76:79],v[240:243]\n"
"v_cvt_pk_bf16_f32 v93,v119,v120\n"
"s_andn2_b32 m0,s55,16384\n"
"v_mfma_f32_16x16x32_bf16 v[244:247],a[116:119],v[76:79],v[244:247]\n"
"s_add_u32 s54,s54,s22\n"
"v_exp_f32 v123,v61\n"
"v_mfma_f32_16x16x32_bf16 v[148:151],a[116:119],v[64:67],v[148:151]\n"
"buffer_load_dwordx4 v115,s[32:35],s54 offen sc1 lds\n"
"v_mfma_f32_16x16x32_bf16 v[180:183],a[116:119],v[68:71],v[180:183]\n"
"v_exp_f32 v119,v62\n"
"v_mfma_f32_16x16x32_bf16 v[212:215],a[116:119],v[72:75],v[212:215]\n"
"ds_read_b32 v125,v124\n"
"v_cvt_pk_bf16_f32 v94,v122,v123\n"
"v_mfma_f32_16x16x32_bf16 v[216:219],a[120:123],v[72:75],v[216:219]\n"
"v_exp_f32 v120,v63\n"
"v_mfma_f32_16x16x32_bf16 v[248:251],a[120:123],v[76:79],v[248:251]\n"
"buffer_store_dwordx4 a[240:243],v112,s[24:27],s56 offen offset:0 sc1\n"
"v_mfma_f32_16x16x32_bf16 v[152:155],a[120:123],v[64:67],v[152:155]\n"
"v_cvt_pk_bf16_f32 v95,v119,v120\n"
"v_or3_b32 v118,v118,v93,v94\n"
"v_mfma_f32_16x16x32_bf16 v[184:187],a[120:123],v[68:71],v[184:187]\n"
"v_mfma_f32_16x16x32_bf16 v[188:191],a[124:127],v[68:71],v[188:191]\n"
"s_waitcnt lgkmcnt(0)\n"
"v_subrev_u32 v125,s101,v125\n"
"v_or3_b32 v125,v118,v95,v125\n"
"v_mfma_f32_16x16x32_bf16 v[220:223],a[124:127],v[72:75],v[220:223]\n"
"v_and_b32 v125,0x40004000,v125\n"
"v_cmp_ne_u32 vcc,0,v125\n"
"v_mfma_f32_16x16x32_bf16 v[252:255],a[124:127],v[76:79],v[252:255]\n"
"v_mfma_f32_16x16x32_bf16 v[156:159],a[124:127],v[64:67],v[156:159]\n"
"s_cbranch_vccnz .Lfk_cs_f3\n"
".Lfk_cr_f3:\n"
"v_mfma_f32_16x16x32_bf16 v[32:35],a[64:67],a[0:3],v[96:99]\n"
"s_add_u32 s101,s101,4\n"
"v_exp_f32 v122,v0\n"
"v_mfma_f32_16x16x32_bf16 v[32:35],a[68:71],a[4:7],v[32:35]\n"
"s_add_u32 m0,s55,4096\n"
"v_exp_f32 v123,v1\n"
"v_mfma_f32_16x16x32_bf16 v[32:35],a[72:75],a[8:11],v[32:35]\n"
"ds_read_b128 a[96:99],v114 offset:57344\n"
"ds_read_b128 a[100:103],v114 offset:58368\n"
"v_mfma_f32_16x16x32_bf16 v[32:35],a[76:79],a[12:15],v[32:35]\n"
"s_add_u32 s54,s54,s22\n"
"v_exp_f32 v119,v2\n"
"v_mfma_f32_16x16x32_bf16 v[36:39],a[92:95],a[12:15],v[96:99]\n"
"ds_read_b128 a[104:107],v114 offset:59392\n"
"v_cvt_pk_bf16_f32 v64,v122,v123\n"
"v_mfma_f32_16x16x32_bf16 v[36:39],a[88:91],a[8:11],v[36:39]\n"
"v_exp_f32 v120,v3\n"
"v_mfma_f32_16x16x32_bf16 v[36:39],a[84:87],a[4:7],v[36:39]\n"
"ds_read_b128 a[108:111],v114 offset:60416\n"
"ds_read_b128 a[112:115],v114 offset:61440\n"
"v_mfma_f32_16x16x32_bf16 v[36:39],a[80:83],a[0:3],v[36:39]\n"
"v_exp_f32 v122,v4\n"
"v_mfma_f32_16x16x32_bf16 v[44:47],a[80:83],a[16:19],v[100:103]\n"
"ds_read_b128 a[116:119],v114 offset:62464\n"
"v_cvt_pk_bf16_f32 v65,v119,v120\n"
"v_mfma_f32_16x16x32_bf16 v[44:47],a[84:87],a[20:23],v[44:47]\n"
"v_exp_f32 v123,v5\n"
"v_mfma_f32_16x16x32_bf16 v[44:47],a[88:91],a[24:27],v[44:47]\n"
"ds_read_b128 a[120:123],v114 offset:63488\n"
"ds_read_b128 a[124:127],v114 offset:64512\n"
"v_mfma_f32_16x16x32_bf16 v[44:47],a[92:95],a[28:31],v[44:47]\n"
"v_exp_f32 v119,v6\n"
"v_mfma_f32_16x16x32_bf16 v[40:43],a[76:79],a[28:31],v[100:103]\n"
"v_cvt_pk_bf16_f32 v66,v122,v123\n"
"v_or3_b32 v118,v64,v65,v66\n"
"v_mfma_f32_16x16x32_bf16 v[40:43],a[72:75],a[24:27],v[40:43]\n"
"v_exp_f32 v120,v7\n"
"v_mfma_f32_16x16x32_bf16 v[40:43],a[68:71],a[20:23],v[40:43]\n"
"buffer_load_dwordx4 v115,s[32:35],s54 offen sc1 lds\n"
"v_mfma_f32_16x16x32_bf16 v[40:43],a[64:67],a[16:19],v[40:43]\n"
"s_add_u32 m0,s67,16384\n"
"v_exp_f32 v122,v8\n"
"v_mfma_f32_16x16x32_bf16 v[48:51],a[64:67],a[32:35],v[104:107]\n"
"v_cvt_pk_bf16_f32 v67,v119,v120\n"
"v_mfma_f32_16x16x32_bf16 v[48:51],a[68:71],a[36:39],v[48:51]\n"
"v_exp_f32 v123,v9\n"
"v_mfma_f32_16x16x32_bf16 v[48:51],a[72:75],a[40:43],v[48:51]\n"
"buffer_load_dwordx4 v112,s[24:27],s64 offen offset:0 sc1 lds\n"
"v_mfma_f32_16x16x32_bf16 v[48:51],a[76:79],a[44:47],v[48:51]\n"
"v_exp_f32 v119,v10\n"
"v_mfma_f32_16x16x32_bf16 v[52:55],a[92:95],a[44:47],v[104:107]\n"
"v_cvt_pk_bf16_f32 v68,v122,v123\n"
"v_or3_b32 v118,v118,v67,v68\n"
"v_mfma_f32_16x16x32_bf16 v[52:55],a[88:91],a[40:43],v[52:55]\n"
"v_exp_f32 v120,v11\n"
"v_mfma_f32_16x16x32_bf16 v[52:55],a[84:87],a[36:39],v[52:55]\n"
"buffer_load_dwordx4 v112,s[24:27],s64 offen offset:1024 sc1 lds\n"
"v_mfma_f32_16x16x32_bf16 v[52:55],a[80:83],a[32:35],v[52:55]\n"
"v_exp_f32 v122,v12\n"
"v_mfma_f32_16x16x32_bf16 v[60:63],a[80:83],a[48:51],v[108:111]\n"
"v_cvt_pk_bf16_f32 v69,v119,v120\n"
"v_mfma_f32_16x16x32_bf16 v[60:63],a[84:87],a[52:55],v[60:63]\n"
"v_exp_f32 v123,v13\n"
"v_mfma_f32_16x16x32_bf16 v[60:63],a[88:91],a[56:59],v[60:63]\n"
"v_cvt_pk_bf16_f32 v70,v122,v123\n"
"v_or3_b32 v118,v118,v69,v70\n"
"v_mfma_f32_16x16x32_bf16 v[60:63],a[92:95],a[60:63],v[60:63]\n"
"v_exp_f32 v119,v14\n"
"v_mfma_f32_16x16x32_bf16 v[56:59],a[76:79],a[60:63],v[108:111]\n"
"v_mfma_f32_16x16x32_bf16 v[56:59],a[72:75],a[56:59],v[56:59]\n"
"v_exp_f32 v120,v15\n"
"v_mfma_f32_16x16x32_bf16 v[56:59],a[68:71],a[52:55],v[56:59]\n"
"v_cvt_pk_bf16_f32 v71,v119,v120\n"
"v_mfma_f32_16x16x32_bf16 v[56:59],a[64:67],a[48:51],v[56:59]\n"
"s_waitcnt lgkmcnt(0)\n"
"v_exp_f32 v122,v16\n"
"v_mfma_f32_16x16x32_bf16 v[128:131],a[96:99],v[80:83],v[128:131]\n"
"v_exp_f32 v123,v17\n"
"v_mfma_f32_16x16x32_bf16 v[160:163],a[96:99],v[84:87],v[160:163]\n"
"ds_read_b128 a[64:67],v113 offset:49152\n"
"ds_read_b128 a[68:71],v113 offset:50176\n"
"v_mfma_f32_16x16x32_bf16 v[192:195],a[96:99],v[88:91],v[192:195]\n"
"v_exp_f32 v119,v18\n"
"v_mfma_f32_16x16x32_bf16 v[224:227],a[96:99],v[92:95],v[224:227]\n"
"ds_read_b128 a[72:75],v113 offset:51200\n"
"v_cvt_pk_bf16_f32 v72,v122,v123\n"
"v_mfma_f32_16x16x32_bf16 a[140:143],a[144:147],v[92:95],a[140:143]\n"
"v_exp_f32 v120,v19\n"
"v_mfma_f32_16x16x32_bf16 v[228:231],a[100:103],v[92:95],v[228:231]\n"
"ds_read_b128 a[76:79],v113 offset:52224\n"
"ds_read_b128 a[80:83],v113 offset:53248\n"
"v_mfma_f32_16x16x32_bf16 v[132:135],a[100:103],v[80:83],v[132:135]\n"
"v_exp_f32 v122,v20\n"
"v_mfma_f32_16x16x32_bf16 v[164:167],a[100:103],v[84:87],v[164:167]\n"
"ds_read_b128 a[84:87],v113 offset:54272\n"
"v_cvt_pk_bf16_f32 v73,v119,v120\n"
"v_mfma_f32_16x16x32_bf16 v[196:199],a[100:103],v[88:91],v[196:199]\n"
"v_exp_f32 v123,v21\n"
"v_mfma_f32_16x16x32_bf16 a[136:139],a[144:147],v[88:91],a[136:139]\n"
"ds_read_b128 a[88:91],v113 offset:55296\n"
"ds_read_b128 a[92:95],v113 offset:56320\n"
"v_mfma_f32_16x16x32_bf16 v[200:203],a[104:107],v[88:91],v[200:203]\n"
"v_exp_f32 v119,v22\n"
"v_mfma_f32_16x16x32_bf16 v[232:235],a[104:107],v[92:95],v[232:235]\n"
"v_cvt_pk_bf16_f32 v74,v122,v123\n"
"v_or3_b32 v118,v118,v71,v72\n"
"v_mfma_f32_16x16x32_bf16 v[136:139],a[104:107],v[80:83],v[136:139]\n"
"v_exp_f32 v120,v23\n"
"v_mfma_f32_16x16x32_bf16 v[168:171],a[104:107],v[84:87],v[168:171]\n"
"s_waitcnt vmcnt(10)\n"
"ds_add_u32 v124,a152\n"
"v_mfma_f32_16x16x32_bf16 a[132:135],a[144:147],v[84:87],a[132:135]\n"
"v_exp_f32 v122,v24\n"
"v_mfma_f32_16x16x32_bf16 v[172:175],a[108:111],v[84:87],v[172:175]\n"
"v_cvt_pk_bf16_f32 v75,v119,v120\n"
"v_or3_b32 v118,v118,v73,v74\n"
"v_mfma_f32_16x16x32_bf16 v[204:207],a[108:111],v[88:91],v[204:207]\n"
"v_exp_f32 v123,v25\n"
"v_mfma_f32_16x16x32_bf16 v[236:239],a[108:111],v[92:95],v[236:239]\n"
"buffer_load_dwordx4 v112,s[24:27],s64 offen offset:2048 sc1 lds\n"
"v_mfma_f32_16x16x32_bf16 v[140:143],a[108:111],v[80:83],v[140:143]\n"
"v_exp_f32 v119,v26\n"
"v_mfma_f32_16x16x32_bf16 a[128:131],a[144:147],v[80:83],a[128:131]\n"
"v_cvt_pk_bf16_f32 v76,v122,v123\n"
"v_or3_b32 v118,v118,v75,v76\n"
"v_mfma_f32_16x16x32_bf16 v[144:147],a[112:115],v[80:83],v[144:147]\n"
"v_exp_f32 v120,v27\n"
"v_mfma_f32_16x16x32_bf16 v[176:179],a[112:115],v[84:87],v[176:179]\n"
"buffer_load_dwordx4 v112,s[24:27],s64 offen offset:3072 sc1 lds\n"
"v_mfma_f32_16x16x32_bf16 v[208:211],a[112:115],v[88:91],v[208:211]\n"
"s_add_u32 s64,s64,16384\n"
"v_exp_f32 v122,v28\n"
"v_mfma_f32_16x16x32_bf16 v[240:243],a[112:115],v[92:95],v[240:243]\n"
"v_cvt_pk_bf16_f32 v77,v119,v120\n"
"v_mfma_f32_16x16x32_bf16 v[244:247],a[116:119],v[92:95],v[244:247]\n"
"v_exp_f32 v123,v29\n"
"v_mfma_f32_16x16x32_bf16 v[148:151],a[116:119],v[80:83],v[148:151]\n"
"v_cvt_pk_bf16_f32 v78,v122,v123\n"
"v_or3_b32 v118,v118,v77,v78\n"
"v_mfma_f32_16x16x32_bf16 v[180:183],a[116:119],v[84:87],v[180:183]\n"
"v_exp_f32 v119,v30\n"
"v_mfma_f32_16x16x32_bf16 v[212:215],a[116:119],v[88:91],v[212:215]\n"
"ds_read_b32 v125,v124\n"
"buffer_store_dwordx4 a[244:247],v112,s[24:27],s56 offen offset:1024 sc1\n"
"v_mfma_f32_16x16x32_bf16 v[216:219],a[120:123],v[88:91],v[216:219]\n"
"v_exp_f32 v120,v31\n"
"v_mfma_f32_16x16x32_bf16 v[248:251],a[120:123],v[92:95],v[248:251]\n"
"v_cvt_pk_bf16_f32 v79,v119,v120\n"
"v_mfma_f32_16x16x32_bf16 v[152:155],a[120:123],v[80:83],v[152:155]\n"
"v_mfma_f32_16x16x32_bf16 v[184:187],a[120:123],v[84:87],v[184:187]\n"
"v_mfma_f32_16x16x32_bf16 v[188:191],a[124:127],v[84:87],v[188:191]\n"
"s_waitcnt lgkmcnt(0)\n"
"v_subrev_u32 v125,s101,v125\n"
"v_or3_b32 v125,v118,v79,v125\n"
"v_mfma_f32_16x16x32_bf16 v[220:223],a[124:127],v[88:91],v[220:223]\n"
"v_and_b32 v125,0x40004000,v125\n"
"v_cmp_ne_u32 vcc,0,v125\n"
"v_mfma_f32_16x16x32_bf16 v[252:255],a[124:127],v[92:95],v[252:255]\n"
"v_mfma_f32_16x16x32_bf16 v[156:159],a[124:127],v[80:83],v[156:159]\n"
"s_cbranch_vccnz .Lfk_cs_f0\n"
".Lfk_cr_f0:\n"
"v_mfma_f32_16x16x32_bf16 v[0:3],a[64:67],a[0:3],v[96:99]\n"
"s_add_u32 s101,s101,4\n"
"v_exp_f32 v122,v32\n"
"v_mfma_f32_16x16x32_bf16 v[0:3],a[68:71],a[4:7],v[0:3]\n"
"s_add_u32 m0,s55,8192\n"
"v_exp_f32 v123,v33\n"
"v_mfma_f32_16x16x32_bf16 v[0:3],a[72:75],a[8:11],v[0:3]\n"
"ds_read_b128 a[96:99],v114 offset:40960\n"
"ds_read_b128 a[100:103],v114 offset:41984\n"
"v_mfma_f32_16x16x32_bf16 v[0:3],a[76:79],a[12:15],v[0:3]\n"
"s_add_u32 s54,s54,s22\n"
"v_exp_f32 v119,v34\n"
"v_mfma_f32_16x16x32_bf16 v[4:7],a[92:95],a[12:15],v[96:99]\n"
"ds_read_b128 a[104:107],v114 offset:43008\n"
"v_cvt_pk_bf16_f32 v80,v122,v123\n"
"v_mfma_f32_16x16x32_bf16 v[4:7],a[88:91],a[8:11],v[4:7]\n"
"v_exp_f32 v120,v35\n"
"v_mfma_f32_16x16x32_bf16 v[4:7],a[84:87],a[4:7],v[4:7]\n"
"ds_read_b128 a[108:111],v114 offset:44032\n"
"ds_read_b128 a[112:115],v114 offset:45056\n"
"v_mfma_f32_16x16x32_bf16 v[4:7],a[80:83],a[0:3],v[4:7]\n"
"v_exp_f32 v122,v36\n"
"v_mfma_f32_16x16x32_bf16 v[12:15],a[80:83],a[16:19],v[100:103]\n"
"ds_read_b128 a[116:119],v114 offset:46080\n"
"v_cvt_pk_bf16_f32 v81,v119,v120\n"
"v_mfma_f32_16x16x32_bf16 v[12:15],a[84:87],a[20:23],v[12:15]\n"
"v_exp_f32 v123,v37\n"
"v_mfma_f32_16x16x32_bf16 v[12:15],a[88:91],a[24:27],v[12:15]\n"
"ds_read_b128 a[120:123],v114 offset:47104\n"
"ds_read_b128 a[124:127],v114 offset:48128\n"
"v_mfma_f32_16x16x32_bf16 v[12:15],a[92:95],a[28:31],v[12:15]\n"
"v_exp_f32 v119,v38\n"
"v_mfma_f32_16x16x32_bf16 v[8:11],a[76:79],a[28:31],v[100:103]\n"
"v_cvt_pk_bf16_f32 v82,v122,v123\n"
"v_or3_b32 v118,v80,v81,v82\n"
"v_mfma_f32_16x16x32_bf16 v[8:11],a[72:75],a[24:27],v[8:11]\n"
"v_exp_f32 v120,v39\n"
"v_mfma_f32_16x16x32_bf16 v[8:11],a[68:71],a[20:23],v[8:11]\n"
"buffer_load_dwordx4 v115,s[32:35],s54 offen sc1 lds\n"
"v_mfma_f32_16x16x32_bf16 v[8:11],a[64:67],a[16:19],v[8:11]\n"
"s_add_u32 m0,s67,49152\n"
"v_exp_f32 v122,v40\n"
"v_mfma_f32_16x16x32_bf16 v[16:19],a[64:67],a[32:35],v[104:107]\n"
"v_cvt_pk_bf16_f32 v83,v119,v120\n"
"v_mfma_f32_16x16x32_bf16 v[16:19],a[68:71],a[36:39],v[16:19]\n"
"v_exp_f32 v123,v41\n"
"v_mfma_f32_16x16x32_bf16 v[16:19],a[72:75],a[40:43],v[16:19]\n"
"buffer_load_dwordx4 v112,s[24:27],s64 offen offset:0 sc1 lds\n"
"v_mfma_f32_16x16x32_bf16 v[16:19],a[76:79],a[44:47],v[16:19]\n"
"v_exp_f32 v119,v42\n"
"v_mfma_f32_16x16x32_bf16 v[20:23],a[92:95],a[44:47],v[104:107]\n"
"v_cvt_pk_bf16_f32 v84,v122,v123\n"
"v_or3_b32 v118,v118,v83,v84\n"
"v_mfma_f32_16x16x32_bf16 v[20:23],a[88:91],a[40:43],v[20:23]\n"
"v_exp_f32 v120,v43\n"
"v_mfma_f32_16x16x32_bf16 v[20:23],a[84:87],a[36:39],v[20:23]\n"
"buffer_load_dwordx4 v112,s[24:27],s64 offen offset:1024 sc1 lds\n"
"v_mfma_f32_16x16x32_bf16 v[20:23],a[80:83],a[32:35],v[20:23]\n"
"v_exp_f32 v122,v44\n"
"v_mfma_f32_16x16x32_bf16 v[28:31],a[80:83],a[48:51],v[108:111]\n"
"v_cvt_pk_bf16_f32 v85,v119,v120\n"
"v_mfma_f32_16x16x32_bf16 v[28:31],a[84:87],a[52:55],v[28:31]\n"
"v_exp_f32 v123,v45\n"
"v_mfma_f32_16x16x32_bf16 v[28:31],a[88:91],a[56:59],v[28:31]\n"
"v_cvt_pk_bf16_f32 v86,v122,v123\n"
"v_or3_b32 v118,v118,v85,v86\n"
"v_mfma_f32_16x16x32_bf16 v[28:31],a[92:95],a[60:63],v[28:31]\n"
"v_exp_f32 v119,v46\n"
"v_mfma_f32_16x16x32_bf16 v[24:27],a[76:79],a[60:63],v[108:111]\n"
"v_mfma_f32_16x16x32_bf16 v[24:27],a[72:75],a[56:59],v[24:27]\n"
"v_exp_f32 v120,v47\n"
"v_mfma_f32_16x16x32_bf16 v[24:27],a[68:71],a[52:55],v[24:27]\n"
"v_cvt_pk_bf16_f32 v87,v119,v120\n"
"v_mfma_f32_16x16x32_bf16 v[24:27],a[64:67],a[48:51],v[24:27]\n"
"s_waitcnt lgkmcnt(0)\n"
"v_exp_f32 v122,v48\n"
"v_mfma_f32_16x16x32_bf16 v[128:131],a[96:99],v[64:67],v[128:131]\n"
"v_exp_f32 v123,v49\n"
"v_mfma_f32_16x16x32_bf16 v[160:163],a[96:99],v[68:71],v[160:163]\n"
"ds_read_b128 a[64:67],v113 offset:32768\n"
"ds_read_b128 a[68:71],v113 offset:33792\n"
"v_mfma_f32_16x16x32_bf16 v[192:195],a[96:99],v[72:75],v[192:195]\n"
"v_exp_f32 v119,v50\n"
"v_mfma_f32_16x16x32_bf16 v[224:227],a[96:99],v[76:79],v[224:227]\n"
"ds_read_b128 a[72:75],v113 offset:34816\n"
"v_cvt_pk_bf16_f32 v88,v122,v123\n"
"v_mfma_f32_16x16x32_bf16 a[140:143],a[144:147],v[76:79],a[140:143]\n"
"v_exp_f32 v120,v51\n"
"v_mfma_f32_16x16x32_bf16 v[228:231],a[100:103],v[76:79],v[228:231]\n"
"ds_read_b128 a[76:79],v113 offset:35840\n"
"ds_read_b128 a[80:83],v113 offset:36864\n"
"v_mfma_f32_16x16x32_bf16 v[132:135],a[100:103],v[64:67],v[132:135]\n"
"v_exp_f32 v122,v52\n"
"v_mfma_f32_16x16x32_bf16 v[164:167],a[100:103],v[68:71],v[164:167]\n"
"ds_read_b128 a[84:87],v113 offset:37888\n"
"v_cvt_pk_bf16_f32 v89,v119,v120\n"
"v_mfma_f32_16x16x32_bf16 v[196:199],a[100:103],v[72:75],v[196:199]\n"
"v_exp_f32 v123,v53\n"
"v_mfma_f32_16x16x32_bf16 a[136:139],a[144:147],v[72:75],a[136:139]\n"
"ds_read_b128 a[88:91],v113 offset:38912\n"
"ds_read_b128 a[92:95],v113 offset:39936\n"
"v_mfma_f32_16x16x32_bf16 v[200:203],a[104:107],v[72:75],v[200:203]\n"
"v_exp_f32 v119,v54\n"
"v_mfma_f32_16x16x32_bf16 v[232:235],a[104:107],v[76:79],v[232:235]\n"
"v_cvt_pk_bf16_f32 v90,v122,v123\n"
"v_or3_b32 v118,v118,v87,v88\n"
"v_mfma_f32_16x16x32_bf16 v[136:139],a[104:107],v[64:67],v[136:139]\n"
"v_exp_f32 v120,v55\n"
"v_mfma_f32_16x16x32_bf16 v[168:171],a[104:107],v[68:71],v[168:171]\n"
"s_waitcnt vmcnt(10)\n"
"ds_add_u32 v124,a152\n"
"v_mfma_f32_16x16x32_bf16 a[132:135],a[144:147],v[68:71],a[132:135]\n"
"v_exp_f32 v122,v56\n"
"v_mfma_f32_16x16x32_bf16 v[172:175],a[108:111],v[68:71],v[172:175]\n"
"v_cvt_pk_bf16_f32 v91,v119,v120\n"
"v_or3_b32 v118,v118,v89,v90\n"
"v_mfma_f32_16x16x32_bf16 v[204:207],a[108:111],v[72:75],v[204:207]\n"
"v_exp_f32 v123,v57\n"
"v_mfma_f32_16x16x32_bf16 v[236:239],a[108:111],v[76:79],v[236:239]\n"
"buffer_load_dwordx4 v112,s[24:27],s64 offen offset:2048 sc1 lds\n"
"v_mfma_f32_16x16x32_bf16 v[140:143],a[108:111],v[64:67],v[140:143]\n"
"v_exp_f32 v119,v58\n"
"v_mfma_f32_16x16x32_bf16 a[128:131],a[144:147],v[64:67],a[128:131]\n"
"v_cvt_pk_bf16_f32 v92,v122,v123\n"
"v_or3_b32 v118,v118,v91,v92\n"
"v_mfma_f32_16x16x32_bf16 v[144:147],a[112:115],v[64:67],v[144:147]\n"
"v_exp_f32 v120,v59\n"
"v_mfma_f32_16x16x32_bf16 v[176:179],a[112:115],v[68:71],v[176:179]\n"
"buffer_load_dwordx4 v112,s[24:27],s64 offen offset:3072 sc1 lds\n"
"v_mfma_f32_16x16x32_bf16 v[208:211],a[112:115],v[72:75],v[208:211]\n"
"s_add_u32 s64,s64,16384\n"
"v_exp_f32 v122,v60\n"
"v_mfma_f32_16x16x32_bf16 v[240:243],a[112:115],v[76:79],v[240:243]\n"
"v_cvt_pk_bf16_f32 v93,v119,v120\n"
"v_mfma_f32_16x16x32_bf16 v[244:247],a[116:119],v[76:79],v[244:247]\n"
"v_exp_f32 v123,v61\n"
"v_mfma_f32_16x16x32_bf16 v[148:151],a[116:119],v[64:67],v[148:151]\n"
"v_cvt_pk_bf16_f32 v94,v122,v123\n"
"v_or3_b32 v118,v118,v93,v94\n"
"v_mfma_f32_16x16x32_bf16 v[180:183],a[116:119],v[68:71],v[180:183]\n"
"v_exp_f32 v119,v62\n"
"v_mfma_f32_16x16x32_bf16 v[212:215],a[116:119],v[72:75],v[212:215]\n"
"ds_read_b32 v125,v124\n"
"buffer_store_dwordx4 a[248:251],v112,s[24:27],s56 offen offset:2048 sc1\n"
"v_mfma_f32_16x16x32_bf16 v[216:219],a[120:123],v[72:75],v[216:219]\n"
"v_exp_f32 v120,v63\n"
"v_mfma_f32_16x16x32_bf16 v[248:251],a[120:123],v[76:79],v[248:251]\n"
"v_cvt_pk_bf16_f32 v95,v119,v120\n"
"v_mfma_f32_16x16x32_bf16 v[152:155],a[120:123],v[64:67],v[152:155]\n"
"v_mfma_f32_16x16x32_bf16 v[184:187],a[120:123],v[68:71],v[184:187]\n"
"v_mfma_f32_16x16x32_bf16 v[188:191],a[124:127],v[68:71],v[188:191]\n"
"s_waitcnt lgkmcnt(0)\n"
"v_subrev_u32 v125,s101,v125\n"
"v_or3_b32 v125,v118,v95,v125\n"
"v_mfma_f32_16x16x32_bf16 v[220:223],a[124:127],v[72:75],v[220:223]\n"
"v_and_b32 v125,0x40004000,v125\n"
"v_cmp_ne_u32 vcc,0,v125\n"
"v_mfma_f32_16x16x32_bf16 v[252:255],a[124:127],v[76:79],v[252:255]\n"
"v_mfma_f32_16x16x32_bf16 v[156:159],a[124:127],v[64:67],v[156:159]\n"
"s_cbranch_vccnz .Lfk_cs_f1\n"
".Lfk_cr_f1:\n"
"v_mfma_f32_16x16x32_bf16 v[32:35],a[64:67],a[0:3],v[96:99]\n"
"s_add_u32 s101,s101,4\n"
"v_exp_f32 v122,v0\n"
"v_mfma_f32_16x16x32_bf16 v[32:35],a[68:71],a[4:7],v[32:35]\n"
"s_add_u32 m0,s55,12288\n"
"v_exp_f32 v123,v1\n"
"v_mfma_f32_16x16x32_bf16 v[32:35],a[72:75],a[8:11],v[32:35]\n"
"ds_read_b128 a[96:99],v114 offset:8192\n"
"ds_read_b128 a[100:103],v114 offset:9216\n"
"v_mfma_f32_16x16x32_bf16 v[32:35],a[76:79],a[12:15],v[32:35]\n"
"s_add_u32 s54,s54,s22\n"
"v_exp_f32 v119,v2\n"
"v_mfma_f32_16x16x32_bf16 v[36:39],a[92:95],a[12:15],v[96:99]\n"
"ds_read_b128 a[104:107],v114 offset:10240\n"
"v_cvt_pk_bf16_f32 v64,v122,v123\n"
"v_mfma_f32_16x16x32_bf16 v[36:39],a[88:91],a[8:11],v[36:39]\n"
"v_exp_f32 v120,v3\n"
"v_mfma_f32_16x16x32_bf16 v[36:39],a[84:87],a[4:7],v[36:39]\n"
"ds_read_b128 a[108:111],v114 offset:11264\n"
"ds_read_b128 a[112:115],v114 offset:12288\n"
"v_mfma_f32_16x16x32_bf16 v[36:39],a[80:83],a[0:3],v[36:39]\n"
"v_exp_f32 v122,v4\n"
"v_mfma_f32_16x16x32_bf16 v[44:47],a[80:83],a[16:19],v[100:103]\n"
"ds_read_b128 a[116:119],v114 offset:13312\n"
"v_cvt_pk_bf16_f32 v65,v119,v120\n"
"v_mfma_f32_16x16x32_bf16 v[44:47],a[84:87],a[20:23],v[44:47]\n"
"v_exp_f32 v123,v5\n"
"v_mfma_f32_16x16x32_bf16 v[44:47],a[88:91],a[24:27],v[44:47]\n"
"ds_read_b128 a[120:123],v114 offset:14336\n"
"ds_read_b128 a[124:127],v114 offset:15360\n"
"v_mfma_f32_16x16x32_bf16 v[44:47],a[92:95],a[28:31],v[44:47]\n"
"v_exp_f32 v119,v6\n"
"v_mfma_f32_16x16x32_bf16 v[40:43],a[76:79],a[28:31],v[100:103]\n"
"v_cvt_pk_bf16_f32 v66,v122,v123\n"
"v_or3_b32 v118,v64,v65,v66\n"
"v_mfma_f32_16x16x32_bf16 v[40:43],a[72:75],a[24:27],v[40:43]\n"
"v_exp_f32 v120,v7\n"
"v_mfma_f32_16x16x32_bf16 v[40:43],a[68:71],a[20:23],v[40:43]\n"
"buffer_load_dwordx4 v115,s[32:35],s54 offen sc1 lds\n"
"v_mfma_f32_16x16x32_bf16 v[40:43],a[64:67],a[16:19],v[40:43]\n"
"s_add_u32 m0,s67,32768\n"
"v_exp_f32 v122,v8\n"
"v_mfma_f32_16x16x32_bf16 v[48:51],a[64:67],a[32:35],v[104:107]\n"
"v_cvt_pk_bf16_f32 v67,v119,v120\n"
"v_mfma_f32_16x16x32_bf16 v[48:51],a[68:71],a[36:39],v[48:51]\n"
"v_exp_f32 v123,v9\n"
"v_mfma_f32_16x16x32_bf16 v[48:51],a[72:75],a[40:43],v[48:51]\n"
"buffer_load_dwordx4 v112,s[24:27],s64 offen offset:0 sc1 lds\n"
"v_mfma_f32_16x16x32_bf16 v[48:51],a[76:79],a[44:47],v[48:51]\n"
"v_exp_f32 v119,v10\n"
"v_mfma_f32_16x16x32_bf16 v[52:55],a[92:95],a[44:47],v[104:107]\n"
"v_cvt_pk_bf16_f32 v68,v122,v123\n"
"v_or3_b32 v118,v118,v67,v68\n"
"v_mfma_f32_16x16x32_bf16 v[52:55],a[88:91],a[40:43],v[52:55]\n"
"v_exp_f32 v120,v11\n"
"v_mfma_f32_16x16x32_bf16 v[52:55],a[84:87],a[36:39],v[52:55]\n"
"buffer_load_dwordx4 v112,s[24:27],s64 offen offset:1024 sc1 lds\n"
"v_mfma_f32_16x16x32_bf16 v[52:55],a[80:83],a[32:35],v[52:55]\n"
"v_exp_f32 v122,v12\n"
"v_mfma_f32_16x16x32_bf16 v[60:63],a[80:83],a[48:51],v[108:111]\n"
"v_cvt_pk_bf16_f32 v69,v119,v120\n"
"v_mfma_f32_16x16x32_bf16 v[60:63],a[84:87],a[52:55],v[60:63]\n"
"v_exp_f32 v123,v13\n"
"v_mfma_f32_16x16x32_bf16 v[60:63],a[88:91],a[56:59],v[60:63]\n"
"v_cvt_pk_bf16_f32 v70,v122,v123\n"
"v_or3_b32 v118,v118,v69,v70\n"
"v_mfma_f32_16x16x32_bf16 v[60:63],a[92:95],a[60:63],v[60:63]\n"
"v_exp_f32 v119,v14\n"
"v_mfma_f32_16x16x32_bf16 v[56:59],a[76:79],a[60:63],v[108:111]\n"
"v_mfma_f32_16x16x32_bf16 v[56:59],a[72:75],a[56:59],v[56:59]\n"
"v_exp_f32 v120,v15\n"
"v_mfma_f32_16x16x32_bf16 v[56:59],a[68:71],a[52:55],v[56:59]\n"
"v_cvt_pk_bf16_f32 v71,v119,v120\n"
"v_mfma_f32_16x16x32_bf16 v[56:59],a[64:67],a[48:51],v[56:59]\n"
"s_waitcnt lgkmcnt(0)\n"
"v_exp_f32 v122,v16\n"
"v_mfma_f32_16x16x32_bf16 v[128:131],a[96:99],v[80:83],v[128:131]\n"
"v_exp_f32 v123,v17\n"
"v_mfma_f32_16x16x32_bf16 v[160:163],a[96:99],v[84:87],v[160:163]\n"
"ds_read_b128 a[64:67],v113 offset:0\n"
"ds_read_b128 a[68:71],v113 offset:1024\n"
"v_mfma_f32_16x16x32_bf16 v[192:195],a[96:99],v[88:91],v[192:195]\n"
"v_exp_f32 v119,v18\n"
"v_mfma_f32_16x16x32_bf16 v[224:227],a[96:99],v[92:95],v[224:227]\n"
"ds_read_b128 a[72:75],v113 offset:2048\n"
"v_cvt_pk_bf16_f32 v72,v122,v123\n"
"v_mfma_f32_16x16x32_bf16 a[140:143],a[144:147],v[92:95],a[140:143]\n"
"v_exp_f32 v120,v19\n"
"v_mfma_f32_16x16x32_bf16 v[228:231],a[100:103],v[92:95],v[228:231]\n"
"ds_read_b128 a[76:79],v113 offset:3072\n"
"ds_read_b128 a[80:83],v113 offset:4096\n"
"v_mfma_f32_16x16x32_bf16 v[132:135],a[100:103],v[80:83],v[132:135]\n"
"v_exp_f32 v122,v20\n"
"v_mfma_f32_16x16x32_bf16 v[164:167],a[100:103],v[84:87],v[164:167]\n"
"ds_read_b128 a[84:87],v113 offset:5120\n"
"v_cvt_pk_bf16_f32 v73,v119,v120\n"
"v_mfma_f32_16x16x32_bf16 v[196:199],a[100:103],v[88:91],v[196:199]\n"
"v_exp_f32 v123,v21\n"
"v_mfma_f32_16x16x32_bf16 a[136:139],a[144:147],v[88:91],a[136:139]\n"
"ds_read_b128 a[88:91],v113 offset:6144\n"
"ds_read_b128 a[92:95],v113 offset:7168\n"
"v_mfma_f32_16x16x32_bf16 v[200:203],a[104:107],v[88:91],v[200:203]\n"
"v_exp_f32 v119,v22\n"
"v_mfma_f32_16x16x32_bf16 v[232:235],a[104:107],v[92:95],v[232:235]\n"
"v_cvt_pk_bf16_f32 v74,v122,v123\n"
"v_or3_b32 v118,v118,v71,v72\n"
"v_mfma_f32_16x16x32_bf16 v[136:139],a[104:107],v[80:83],v[136:139]\n"
"v_exp_f32 v120,v23\n"
"v_mfma_f32_16x16x32_bf16 v[168:171],a[104:107],v[84:87],v[168:171]\n"
"s_waitcnt vmcnt(10)\n"
"ds_add_u32 v124,a152\n"
"v_mfma_f32_16x16x32_bf16 a[132:135],a[144:147],v[84:87],a[132:135]\n"
"v_exp_f32 v122,v24\n"
"v_mfma_f32_16x16x32_bf16 v[172:175],a[108:111],v[84:87],v[172:175]\n"
"v_cvt_pk_bf16_f32 v75,v119,v120\n"
"v_or3_b32 v118,v118,v73,v74\n"
"v_mfma_f32_16x16x32_bf16 v[204:207],a[108:111],v[88:91],v[204:207]\n"
"v_exp_f32 v123,v25\n"
"v_mfma_f32_16x16x32_bf16 v[236:239],a[108:111],v[92:95],v[236:239]\n"
"buffer_load_dwordx4 v112,s[24:27],s64 offen offset:2048 sc1 lds\n"
"v_mfma_f32_16x16x32_bf16 v[140:143],a[108:111],v[80:83],v[140:143]\n"
"v_exp_f32 v119,v26\n"
"v_mfma_f32_16x16x32_bf16 a[128:131],a[144:147],v[80:83],a[128:131]\n"
"v_cvt_pk_bf16_f32 v76,v122,v123\n"
"v_or3_b32 v118,v118,v75,v76\n"
"v_mfma_f32_16x16x32_bf16 v[144:147],a[112:115],v[80:83],v[144:147]\n"
"v_exp_f32 v120,v27\n"
"v_mfma_f32_16x16x32_bf16 v[176:179],a[112:115],v[84:87],v[176:179]\n"
"buffer_load_dwordx4 v112,s[24:27],s64 offen offset:3072 sc1 lds\n"
"v_mfma_f32_16x16x32_bf16 v[208:211],a[112:115],v[88:91],v[208:211]\n"
"s_add_u32 s64,s64,16384\n"
"v_exp_f32 v122,v28\n"
"v_mfma_f32_16x16x32_bf16 v[240:243],a[112:115],v[92:95],v[240:243]\n"
"v_cvt_pk_bf16_f32 v77,v119,v120\n"
"v_mfma_f32_16x16x32_bf16 v[244:247],a[116:119],v[92:95],v[244:247]\n"
"v_exp_f32 v123,v29\n"
"v_mfma_f32_16x16x32_bf16 v[148:151],a[116:119],v[80:83],v[148:151]\n"
"v_cvt_pk_bf16_f32 v78,v122,v123\n"
"v_or3_b32 v118,v118,v77,v78\n"
"v_mfma_f32_16x16x32_bf16 v[180:183],a[116:119],v[84:87],v[180:183]\n"
"v_exp_f32 v119,v30\n"
"v_mfma_f32_16x16x32_bf16 v[212:215],a[116:119],v[88:91],v[212:215]\n"
"ds_read_b32 v125,v124\n"
"buffer_store_dwordx4 a[252:255],v112,s[24:27],s56 offen offset:3072 sc1\n"
"v_mfma_f32_16x16x32_bf16 v[216:219],a[120:123],v[88:91],v[216:219]\n"
"s_add_u32 s89,s89,1\n"
"v_exp_f32 v120,v31\n"
"v_mfma_f32_16x16x32_bf16 v[248:251],a[120:123],v[92:95],v[248:251]\n"
"v_cvt_pk_bf16_f32 v79,v119,v120\n"
"v_mfma_f32_16x16x32_bf16 v[152:155],a[120:123],v[80:83],v[152:155]\n"
"v_mfma_f32_16x16x32_bf16 v[184:187],a[120:123],v[84:87],v[184:187]\n"
"v_mfma_f32_16x16x32_bf16 v[188:191],a[124:127],v[84:87],v[188:191]\n"
"s_waitcnt lgkmcnt(0)\n"
"v_subrev_u32 v125,s101,v125\n"
"v_or3_b32 v125,v118,v79,v125\n"
"v_mfma_f32_16x16x32_bf16 v[220:223],a[124:127],v[88:91],v[220:223]\n"
"v_and_b32 v125,0x40004000,v125\n"
"v_cmp_ne_u32 vcc,0,v125\n"
"v_mfma_f32_16x16x32_bf16 v[252:255],a[124:127],v[92:95],v[252:255]\n"
"v_mfma_f32_16x16x32_bf16 v[156:159],a[124:127],v[80:83],v[156:159]\n"
"s_cbranch_vccnz .Lfk_cs_f2\n"
".Lfk_cr_f2:\n"
"s_cmp_lt_u32 s89,s60\n"
"s_cbranch_scc1 .Lfk_F\n"
"s_branch .Lfk_Fx\n"
".Lfk_cs_f3:\n"
"s_call_b64 s[94:95],.Lfk_meet3\n"
"v_or_b32 v118,v118,v95\n"
"v_and_b32 v118,0x40004000,v118\n"
"v_cmp_ne_u32 vcc,0,v118\n"
"s_nop 0\n"
"s_cbranch_vccz .Lfk_cr_f3\n"
"s_mov_b32 s91,15\n"
"s_call_b64 s[94:95],.Lfk_rare3\n"
"s_branch .Lfk_cr_f3\n"
".Lfk_cs_f0:\n"
"s_call_b64 s[94:95],.Lfk_meet3\n"
"v_or_b32 v118,v118,v79\n"
"v_and_b32 v118,0x40004000,v118\n"
"v_cmp_ne_u32 vcc,0,v118\n"
"s_nop 0\n"
"s_cbranch_vccz .Lfk_cr_f0\n"
"s_mov_b32 s91,15\n"
"s_call_b64 s[94:95],.Lfk_rare2\n"
"s_branch .Lfk_cr_f0\n"
".Lfk_cs_f1:\n"
"s_call_b64 s[94:95],.Lfk_meet3\n"
"v_or_b32 v118,v118,v95\n"
"v_and_b32 v118,0x40004000,v118\n"
"v_cmp_ne_u32 vcc,0,v118\n"
"s_nop 0\n"
"s_cbranch_vccz .Lfk_cr_f1\n"
"s_mov_b32 s91,15\n"
"s_call_b64 s[94:95],.Lfk_rare3\n"
"s_branch .Lfk_cr_f1\n"
".Lfk_cs_f2:\n"
"s_call_b64 s[94:95],.Lfk_meet3\n"
"v_or_b32 v118,v118,v79\n"
"v_and_b32 v118,0x40004000,v118\n"
"v_cmp_ne_u32 vcc,0,v118\n"
"s_nop 0\n"
"s_cbranch_vccz .Lfk_cr_f2\n"
"s_mov_b32 s91,15\n"
"s_call_b64 s[94:95],.Lfk_rare2\n"
"s_branch .Lfk_cr_f2\n"
".Lfk_cs_g3:\n"
"s_call_b64 s[94:95],.Lfk_meet3\n"
"v_or_b32 v118,v118,v95\n"
"v_and_b32 v118,0x40004000,v118\n"
"v_cmp_ne_u32 vcc,0,v118\n"
"s_nop 0\n"
"s_cbranch_vccz .Lfk_cr_g3\n"
"s_mov_b32 s91,15\n"
"s_call_b64 s[94:95],.Lfk_rare3\n"
"s_branch .Lfk_cr_g3\n"
".Lfk_cs_g0:\n"
"s_call_b64 s[94:95],.Lfk_meet3\n"
"v_or_b32 v118,v118,v79\n"
"v_and_b32 v118,0x40004000,v118\n"
"v_cmp_ne_u32 vcc,0,v118\n"
"s_nop 0\n"
"s_cbranch_vccz .Lfk_cr_g0\n"
"s_mov_b32 s91,15\n"
"s_call_b64 s[94:95],.Lfk_rare2\n"
"s_branch .Lfk_cr_g0\n"
".Lfk_cs_g1:\n"
"s_call_b64 s[94:95],.Lfk_meet3\n"
"v_or_b32 v118,v118,v95\n"
"v_and_b32 v118,0x40004000,v118\n"
"v_cmp_ne_u32 vcc,0,v118\n"
"s_nop 0\n"
"s_cbranch_vccz .Lfk_cr_g1\n"
"s_mov_b32 s91,15\n"
"s_call_b64 s[94:95],.Lfk_rare3\n"
"s_branch .Lfk_cr_g1\n"
".Lfk_cs_g2:\n"
"s_call_b64 s[94:95],.Lfk_meet3\n"
"v_or_b32 v118,v118,v79\n"
"v_and_b32 v118,0x40004000,v118\n"
"v_cmp_ne_u32 vcc,0,v118\n"
"s_nop 0\n"
"s_cbranch_vccz .Lfk_cr_g2\n"
"s_mov_b32 s91,15\n"
"s_call_b64 s[94:95],.Lfk_rare2\n"
"s_branch .Lfk_cr_g2\n"
".Lfk_meet3:\n"
"s_sub_u32 s82,s101,0\n"
"s_mov_b32 s84,0x1000000\n"
".Lfk_meetl3:\n"
"ds_read_b32 v125,v124\n"
"s_sub_u32 s84,s84,1\n"
"s_cbranch_scc1 .Lfk_meetx3\n"
"s_waitcnt lgkmcnt(0)\n"
"v_cmp_gt_u32 vcc,s82,v125\n"
"s_nop 0\n"
"s_cbranch_vccnz .Lfk_meetl3\n"
".Lfk_meetx3:\n"
"s_setpc_b64 s[94:95]\n"
".Lfk_Fx:\n"
"s_mov_b32 s82,2146435072\n"
"buffer_load_dword a148,off,s[24:27],s82\n"
"buffer_load_dword a148,off,s[24:27],s82\n"
"buffer_load_dword a148,off,s[24:27],s82\n"
"buffer_load_dword a148,off,s[24:27],s82\n"
"s_cmp_ge_u32 s52,2\n"
"s_cbranch_scc1 .Lfk_winA\n"
".Lfk_H:\n"
"v_mfma_f32_16x16x32_bf16 v[0:3],a[64:67],a[0:3],v[96:99]\n"
"s_swappc_b64 s[94:95],s[20:21]\n"
"s_add_u32 s101,s101,4\n"
"v_exp_f32 v122,v32\n"
"v_mfma_f32_16x16x32_bf16 v[0:3],a[68:71],a[4:7],v[0:3]\n"
"s_cmp_eq_u32 s64,s65\n"
"v_exp_f32 v123,v33\n"
"v_mfma_f32_16x16x32_bf16 v[0:3],a[72:75],a[8:11],v[0:3]\n"
"ds_read_b128 a[96:99],v114 offset:24576\n"
"ds_read_b128 a[100:103],v114 offset:25600\n"
"v_mfma_f32_16x16x32_bf16 v[0:3],a[76:79],a[12:15],v[0:3]\n"
"s_cselect_b32 s64,s66,s64\n"
"v_exp_f32 v119,v34\n"
"v_mfma_f32_16x16x32_bf16 v[4:7],a[92:95],a[12:15],v[96:99]\n"
"ds_read_b128 a[104:107],v114 offset:26624\n"
"v_cvt_pk_bf16_f32 v80,v122,v123\n"
"v_mfma_f32_16x16x32_bf16 v[4:7],a[88:91],a[8:11],v[4:7]\n"
"v_exp_f32 v120,v35\n"
"v_mfma_f32_16x16x32_bf16 v[4:7],a[84:87],a[4:7],v[4:7]\n"
"ds_read_b128 a[108:111],v114 offset:27648\n"
"ds_read_b128 a[112:115],v114 offset:28672\n"
"v_mfma_f32_16x16x32_bf16 v[4:7],a[80:83],a[0:3],v[4:7]\n"
"v_exp_f32 v122,v36\n"
"v_mfma_f32_16x16x32_bf16 v[12:15],a[80:83],a[16:19],v[100:103]\n"
"ds_read_b128 a[116:119],v114 offset:29696\n"
"v_cvt_pk_bf16_f32 v81,v119,v120\n"
"v_mfma_f32_16x16x32_bf16 v[12:15],a[84:87],a[20:23],v[12:15]\n"
"v_exp_f32 v123,v37\n"
"v_mfma_f32_16x16x32_bf16 v[12:15],a[88:91],a[24:27],v[12:15]\n"
"ds_read_b128 a[120:123],v114 offset:30720\n"
"ds_read_b128 a[124:127],v114 offset:31744\n"
"v_mfma_f32_16x16x32_bf16 v[12:15],a[92:95],a[28:31],v[12:15]\n"
"s_add_u32 m0,s67,0\n"
"v_exp_f32 v119,v38\n"
"v_mfma_f32_16x16x32_bf16 v[8:11],a[76:79],a[28:31],v[100:103]\n"
"v_cvt_pk_bf16_f32 v82,v122,v123\n"
"v_or3_b32 v118,v80,v81,v82\n"
"v_mfma_f32_16x16x32_bf16 v[8:11],a[72:75],a[24:27],v[8:11]\n"
"v_exp_f32 v120,v39\n"
"v_mfma_f32_16x16x32_bf16 v[8:11],a[68:71],a[20:23],v[8:11]\n"
"buffer_load_dwordx4 v112,s[24:27],s64 offen offset:0 sc1 lds\n"
"v_mfma_f32_16x16x32_bf16 v[8:11],a[64:67],a[16:19],v[8:11]\n"
"v_exp_f32 v122,v40\n"
"v_mfma_f32_16x16x32_bf16 v[16:19],a[64:67],a[32:35],v[104:107]\n"
"v_cvt_pk_bf16_f32 v83,v119,v120\n"
"v_mfma_f32_16x16x32_bf16 v[16:19],a[68:71],a[36:39],v[16:19]\n"
"v_exp_f32 v123,v41\n"
"v_mfma_f32_16x16x32_bf16 v[16:19],a[72:75],a[40:43],v[16:19]\n"
"buffer_load_dwordx4 v112,s[24:27],s64 offen offset:1024 sc1 lds\n"
"v_mfma_f32_16x16x32_bf16 v[16:19],a[76:79],a[44:47],v[16:19]\n"
"v_exp_f32 v119,v42\n"
"v_mfma_f32_16x16x32_bf16 v[20:23],a[92:95],a[44:47],v[104:107]\n"
"v_cvt_pk_bf16_f32 v84,v122,v123\n"
"v_or3_b32 v118,v118,v83,v84\n"
"v_mfma_f32_16x16x32_bf16 v[20:23],a[88:91],a[40:43],v[20:23]\n"
"v_exp_f32 v120,v43\n"
"v_mfma_f32_16x16x32_bf16 v[20:23],a[84:87],a[36:39],v[20:23]\n"
"v_cvt_pk_bf16_f32 v85,v119,v120\n"
"v_mfma_f32_16x16x32_bf16 v[20:23],a[80:83],a[32:35],v[20:23]\n"
"v_exp_f32 v122,v44\n"
"v_mfma_f32_16x16x32_bf16 v[28:31],a[80:83],a[48:51],v[108:111]\n"
"buffer_load_dwordx4 v112,s[24:27],s64 offen offset:2048 sc1 lds\n"
"v_mfma_f32_16x16x32_bf16 v[28:31],a[84:87],a[52:55],v[28:31]\n"
"v_exp_f32 v123,v45\n"
"v_mfma_f32_16x16x32_bf16 v[28:31],a[88:91],a[56:59],v[28:31]\n"
"v_cvt_pk_bf16_f32 v86,v122,v123\n"
"v_or3_b32 v118,v118,v85,v86\n"
"v_mfma_f32_16x16x32_bf16 v[28:31],a[92:95],a[60:63],v[28:31]\n"
"v_exp_f32 v119,v46\n"
"v_mfma_f32_16x16x32_bf16 v[24:27],a[76:79],a[60:63],v[108:111]\n"
"v_mfma_f32_16x16x32_bf16 v[24:27],a[72:75],a[56:59],v[24:27]\n"
"v_exp_f32 v120,v47\n"
"v_mfma_f32_16x16x32_bf16 v[24:27],a[68:71],a[52:55],v[24:27]\n"
"buffer_load_dwordx4 v112,s[24:27],s64 offen offset:3072 sc1 lds\n"
"v_mfma_f32_16x16x32_bf16 v[24:27],a[64:67],a[48:51],v[24:27]\n"
"s_waitcnt lgkmcnt(0)\n"
"v_exp_f32 v122,v48\n"
"v_mfma_f32_16x16x32_bf16 v[128:131],a[96:99],v[64:67],v[128:131]\n"
"v_exp_f32 v123,v49\n"
"v_mfma_f32_16x16x32_bf16 v[160:163],a[96:99],v[68:71],v[160:163]\n"
"ds_read_b128 a[64:67],v113 offset:16384\n"
"v_cvt_pk_bf16_f32 v87,v119,v120\n"
"v_mfma_f32_16x16x32_bf16 v[192:195],a[96:99],v[72:75],v[192:195]\n"
"s_add_u32 m0,s67,4096\n"
"v_exp_f32 v119,v50\n"
"v_mfma_f32_16x16x32_bf16 v[224:227],a[96:99],v[76:79],v[224:227]\n"
"ds_read_b128 a[68:71],v113 offset:17408\n"
"v_cvt_pk_bf16_f32 v88,v122,v123\n"
"v_mfma_f32_16x16x32_bf16 a[140:143],a[144:147],v[76:79],a[140:143]\n"
"v_exp_f32 v120,v51\n"
"v_mfma_f32_16x16x32_bf16 v[228:231],a[100:103],v[76:79],v[228:231]\n"
"ds_read_b128 a[72:75],v113 offset:18432\n"
"ds_read_b128 a[76:79],v113 offset:19456\n"
"v_mfma_f32_16x16x32_bf16 v[132:135],a[100:103],v[64:67],v[132:135]\n"
"v_exp_f32 v122,v52\n"
"v_mfma_f32_16x16x32_bf16 v[164:167],a[100:103],v[68:71],v[164:167]\n"
"ds_read_b128 a[80:83],v113 offset:20480\n"
"v_cvt_pk_bf16_f32 v89,v119,v120\n"
"v_mfma_f32_16x16x32_bf16 v[196:199],a[100:103],v[72:75],v[196:199]\n"
"v_exp_f32 v123,v53\n"
"v_mfma_f32_16x16x32_bf16 a[136:139],a[144:147],v[72:75],a[136:139]\n"
"ds_read_b128 a[84:87],v113 offset:21504\n"
"ds_read_b128 a[88:91],v113 offset:22528\n"
"v_mfma_f32_16x16x32_bf16 v[200:203],a[104:107],v[72:75],v[200:203]\n"
"v_exp_f32 v119,v54\n"
"v_mfma_f32_16x16x32_bf16 v[232:235],a[104:107],v[76:79],v[232:235]\n"
"ds_read_b128 a[92:95],v113 offset:23552\n"
"v_cvt_pk_bf16_f32 v90,v122,v123\n"
"v_mfma_f32_16x16x32_bf16 v[136:139],a[104:107],v[64:67],v[136:139]\n"
"v_exp_f32 v120,v55\n"
"v_mfma_f32_16x16x32_bf16 v[168:171],a[104:107],v[68:71],v[168:171]\n"
"s_waitcnt vmcnt(15)\n"
"ds_add_u32 v124,a152\n"
"v_mfma_f32_16x16x32_bf16 a[132:135],a[144:147],v[68:71],a[132:135]\n"
"v_exp_f32 v122,v56\n"
"v_mfma_f32_16x16x32_bf16 v[172:175],a[108:111],v[68:71],v[172:175]\n"
"v_cvt_pk_bf16_f32 v91,v119,v120\n"
"v_or3_b32 v118,v118,v87,v88\n"
"v_mfma_f32_16x16x32_bf16 v[204:207],a[108:111],v[72:75],v[204:207]\n"
"v_exp_f32 v123,v57\n"
"v_mfma_f32_16x16x32_bf16 v[236:239],a[108:111],v[76:79],v[236:239]\n"
"buffer_load_dwordx4 v117,s[24:27],s64 offen offset:0 sc1 lds\n"
"v_mfma_f32_16x16x32_bf16 v[140:143],a[108:111],v[64:67],v[140:143]\n"
"v_exp_f32 v119,v58\n"
"v_mfma_f32_16x16x32_bf16 a[128:131],a[144:147],v[64:67],a[128:131]\n"
"v_cvt_pk_bf16_f32 v92,v122,v123\n"
"v_or3_b32 v118,v118,v89,v90\n"
"v_mfma_f32_16x16x32_bf16 v[144:147],a[112:115],v[64:67],v[144:147]\n"
"v_exp_f32 v120,v59\n"
"v_mfma_f32_16x16x32_bf16 v[176:179],a[112:115],v[68:71],v[176:179]\n"
"buffer_load_dwordx4 v117,s[24:27],s64 offen offset:1024 sc1 lds\n"
"v_mfma_f32_16x16x32_bf16 v[208:211],a[112:115],v[72:75],v[208:211]\n"
"v_exp_f32 v122,v60\n"
"v_mfma_f32_16x16x32_bf16 v[240:243],a[112:115],v[76:79],v[240:243]\n"
"v_cvt_pk_bf16_f32 v93,v119,v120\n"
"v_or3_b32 v118,v118,v91,v92\n"
"v_mfma_f32_16x16x32_bf16 v[244:247],a[116:119],v[76:79],v[244:247]\n"
"v_exp_f32 v123,v61\n"
"v_mfma_f32_16x16x32_bf16 v[148:151],a[116:119],v[64:67],v[148:151]\n"
"buffer_load_dwordx4 v117,s[24:27],s64 offen offset:2048 sc1 lds\n"
"v_mfma_f32_16x16x32_bf16 v[180:183],a[116:119],v[68:71],v[180:183]\n"
"v_exp_f32 v119,v62\n"
"v_mfma_f32_16x16x32_bf16 v[212:215],a[116:119],v[72:75],v[212:215]\n"
"ds_read_b32 v125,v124\n"
"v_cvt_pk_bf16_f32 v94,v122,v123\n"
"v_mfma_f32_16x16x32_bf16 v[216:219],a[120:123],v[72:75],v[216:219]\n"
"v_exp_f32 v120,v63\n"
"v_mfma_f32_16x16x32_bf16 v[248:251],a[120:123],v[76:79],v[248:251]\n"
"buffer_load_dwordx4 v117,s[24:27],s64 offen offset:3072 sc1 lds\n"
"v_mfma_f32_16x16x32_bf16 v[152:155],a[120:123],v[64:67],v[152:155]\n"
"s_add_u32 s64,s64,16384\n"
"s_andn2_b32 m0,s55,16384\n"
"v_mfma_f32_16x16x32_bf16 v[184:187],a[120:123],v[68:71],v[184:187]\n"
"s_add_u32 s54,s54,s22\n"
"v_cvt_pk_bf16_f32 v95,v119,v120\n"
"v_mfma_f32_16x16x32_bf16 v[188:191],a[124:127],v[68:71],v[188:191]\n"
"s_waitcnt lgkmcnt(0)\n"
"v_subrev_u32 v125,s101,v125\n"
"v_or3_b32 v118,v118,v93,v94\n"
"v_mfma_f32_16x16x32_bf16 v[220:223],a[124:127],v[72:75],v[220:223]\n"
"v_or3_b32 v125,v118,v95,v125\n"
"v_and_b32 v125,0x40004000,v125\n"
"v_mfma_f32_16x16x32_bf16 v[252:255],a[124:127],v[76:79],v[252:255]\n"
"buffer_load_dwordx4 v115,s[32:35],s54 offen sc1 lds\n"
"buffer_store_dwordx4 a[240:243],v112,s[24:27],s56 offen offset:0 sc1\n"
"v_cmp_ne_u32 vcc,0,v125\n"
"v_mfma_f32_16x16x32_bf16 v[156:159],a[124:127],v[64:67],v[156:159]\n"
"s_cbranch_vccnz .Lfk_cs_g3\n"
".Lfk_cr_g3:\n"
"v_mfma_f32_16x16x32_bf16 v[32:35],a[64:67],a[0:3],v[96:99]\n"
"s_add_u32 s101,s101,4\n"
"v_exp_f32 v122,v0\n"
"v_mfma_f32_16x16x32_bf16 v[32:35],a[68:71],a[4:7],v[32:35]\n"
"s_add_u32 m0,s55,4096\n"
"v_exp_f32 v123,v1\n"
"v_mfma_f32_16x16x32_bf16 v[32:35],a[72:75],a[8:11],v[32:35]\n"
"ds_read_b128 a[96:99],v114 offset:57344\n"
"ds_read_b128 a[100:103],v114 offset:58368\n"
"v_mfma_f32_16x16x32_bf16 v[32:35],a[76:79],a[12:15],v[32:35]\n"
"s_add_u32 s54,s54,s22\n"
"v_exp_f32 v119,v2\n"
"v_mfma_f32_16x16x32_bf16 v[36:39],a[92:95],a[12:15],v[96:99]\n"
"ds_read_b128 a[104:107],v114 offset:59392\n"
"v_cvt_pk_bf16_f32 v64,v122,v123\n"
"v_mfma_f32_16x16x32_bf16 v[36:39],a[88:91],a[8:11],v[36:39]\n"
"v_exp_f32 v120,v3\n"
"v_mfma_f32_16x16x32_bf16 v[36:39],a[84:87],a[4:7],v[36:39]\n"
"ds_read_b128 a[108:111],v114 offset:60416\n"
"ds_read_b128 a[112:115],v114 offset:61440\n"
"v_mfma_f32_16x16x32_bf16 v[36:39],a[80:83],a[0:3],v[36:39]\n"
"v_exp_f32 v122,v4\n"
"v_mfma_f32_16x16x32_bf16 v[44:47],a[80:83],a[16:19],v[100:103]\n"
"ds_read_b128 a[116:119],v114 offset:62464\n"
"v_cvt_pk_bf16_f32 v65,v119,v120\n"
"v_mfma_f32_16x16x32_bf16 v[44:47],a[84:87],a[20:23],v[44:47]\n"
"v_exp_f32 v123,v5\n"
"v_mfma_f32_16x16x32_bf16 v[44:47],a[88:91],a[24:27],v[44:47]\n"
"ds_read_b128 a[120:123],v114 offset:63488\n"
"ds_read_b128 a[124:127],v114 offset:64512\n"
"v_mfma_f32_16x16x32_bf16 v[44:47],a[92:95],a[28:31],v[44:47]\n"
"v_exp_f32 v119,v6\n"
"v_mfma_f32_16x16x32_bf16 v[40:43],a[76:79],a[28:31],v[100:103]\n"
"v_cvt_pk_bf16_f32 v66,v122,v123\n"
"v_or3_b32 v118,v64,v65,v66\n"
"v_mfma_f32_16x16x32_bf16 v[40:43],a[72:75],a[24:27],v[40:43]\n"
"v_exp_f32 v120,v7\n"
"v_mfma_f32_16x16x32_bf16 v[40:43],a[68:71],a[20:23],v[40:43]\n"
"buffer_load_dwordx4 v115,s[32:35],s54 offen sc1 lds\n"
"v_mfma_f32_16x16x32_bf16 v[40:43],a[64:67],a[16:19],v[40:43]\n"
"s_add_u32 m0,s67,16384\n"
"v_exp_f32 v122,v8\n"
"v_mfma_f32_16x16x32_bf16 v[48:51],a[64:67],a[32:35],v[104:107]\n"
"v_cvt_pk_bf16_f32 v67,v119,v120\n"
"v_mfma_f32_16x16x32_bf16 v[48:51],a[68:71],a[36:39],v[48:51]\n"
"v_exp_f32 v123,v9\n"
"v_mfma_f32_16x16x32_bf16 v[48:51],a[72:75],a[40:43],v[48:51]\n"
"buffer_load_dwordx4 v112,s[24:27],s64 offen offset:0 sc1 lds\n"
"v_mfma_f32_16x16x32_bf16 v[48:51],a[76:79],a[44:47],v[48:51]\n"
"v_exp_f32 v119,v10\n"
"v_mfma_f32_16x16x32_bf16 v[52:55],a[92:95],a[44:47],v[104:107]\n"
"v_cvt_pk_bf16_f32 v68,v122,v123\n"
"v_or3_b32 v118,v118,v67,v68\n"
"v_mfma_f32_16x16x32_bf16 v[52:55],a[88:91],a[40:43],v[52:55]\n"
"v_exp_f32 v120,v11\n"
"v_mfma_f32_16x16x32_bf16 v[52:55],a[84:87],a[36:39],v[52:55]\n"
"buffer_load_dwordx4 v112,s[24:27],s64 offen offset:1024 sc1 lds\n"
"v_mfma_f32_16x16x32_bf16 v[52:55],a[80:83],a[32:35],v[52:55]\n"
"v_exp_f32 v122,v12\n"
"v_mfma_f32_16x16x32_bf16 v[60:63],a[80:83],a[48:51],v[108:111]\n"
"v_cvt_pk_bf16_f32 v69,v119,v120\n"
"v_mfma_f32_16x16x32_bf16 v[60:63],a[84:87],a[52:55],v[60:63]\n"
"v_exp_f32 v123,v13\n"
"v_mfma_f32_16x16x32_bf16 v[60:63],a[88:91],a[56:59],v[60:63]\n"
"buffer_load_dwordx4 v112,s[24:27],s64 offen offset:2048 sc1 lds\n"
"v_mfma_f32_16x16x32_bf16 v[60:63],a[92:95],a[60:63],v[60:63]\n"
"v_exp_f32 v119,v14\n"
"v_mfma_f32_16x16x32_bf16 v[56:59],a[76:79],a[60:63],v[108:111]\n"
"v_cvt_pk_bf16_f32 v70,v122,v123\n"
"v_or3_b32 v118,v118,v69,v70\n"
"v_mfma_f32_16x16x32_bf16 v[56:59],a[72:75],a[56:59],v[56:59]\n"
"v_exp_f32 v120,v15\n"
"v_mfma_f32_16x16x32_bf16 v[56:59],a[68:71],a[52:55],v[56:59]\n"
"buffer_load_dwordx4 v112,s[24:27],s64 offen offset:3072 sc1 lds\n"
"v_mfma_f32_16x16x32_bf16 v[56:59],a[64:67],a[48:51],v[56:59]\n"
"s_waitcnt lgkmcnt(0)\n"
"v_exp_f32 v122,v16\n"
"v_mfma_f32_16x16x32_bf16 v[128:131],a[96:99],v[80:83],v[128:131]\n"
"v_exp_f32 v123,v17\n"
"v_mfma_f32_16x16x32_bf16 v[160:163],a[96:99],v[84:87],v[160:163]\n"
"ds_read_b128 a[64:67],v113 offset:49152\n"
"v_cvt_pk_bf16_f32 v71,v119,v120\n"
"v_mfma_f32_16x16x32_bf16 v[192:195],a[96:99],v[88:91],v[192:195]\n"
"s_add_u32 m0,s67,20480\n"
"v_exp_f32 v119,v18\n"
"v_mfma_f32_16x16x32_bf16 v[224:227],a[96:99],v[92:95],v[224:227]\n"
"ds_read_b128 a[68:71],v113 offset:50176\n"
"v_cvt_pk_bf16_f32 v72,v122,v123\n"
"v_mfma_f32_16x16x32_bf16 a[140:143],a[144:147],v[92:95],a[140:143]\n"
"v_exp_f32 v120,v19\n"
"v_mfma_f32_16x16x32_bf16 v[228:231],a[100:103],v[92:95],v[228:231]\n"
"ds_read_b128 a[72:75],v113 offset:51200\n"
"ds_read_b128 a[76:79],v113 offset:52224\n"
"v_mfma_f32_16x16x32_bf16 v[132:135],a[100:103],v[80:83],v[132:135]\n"
"v_exp_f32 v122,v20\n"
"v_mfma_f32_16x16x32_bf16 v[164:167],a[100:103],v[84:87],v[164:167]\n"
"ds_read_b128 a[80:83],v113 offset:53248\n"
"v_cvt_pk_bf16_f32 v73,v119,v120\n"
"v_mfma_f32_16x16x32_bf16 v[196:199],a[100:103],v[88:91],v[196:199]\n"
"v_exp_f32 v123,v21\n"
"v_mfma_f32_16x16x32_bf16 a[136:139],a[144:147],v[88:91],a[136:139]\n"
"ds_read_b128 a[84:87],v113 offset:54272\n"
"ds_read_b128 a[88:91],v113 offset:55296\n"
"v_mfma_f32_16x16x32_bf16 v[200:203],a[104:107],v[88:91],v[200:203]\n"
"v_exp_f32 v119,v22\n"
"v_mfma_f32_16x16x32_bf16 v[232:235],a[104:107],v[92:95],v[232:235]\n"
"ds_read_b128 a[92:95],v113 offset:56320\n"
"v_cvt_pk_bf16_f32 v74,v122,v123\n"
"v_mfma_f32_16x16x32_bf16 v[136:139],a[104:107],v[80:83],v[136:139]\n"
"v_exp_f32 v120,v23\n"
"v_mfma_f32_16x16x32_bf16 v[168:171],a[104:107],v[84:87],v[168:171]\n"
"s_waitcnt vmcnt(16)\n"
"ds_add_u32 v124,a152\n"
"v_mfma_f32_16x16x32_bf16 a[132:135],a[144:147],v[84:87],a[132:135]\n"
"v_exp_f32 v122,v24\n"
"v_mfma_f32_16x16x32_bf16 v[172:175],a[108:111],v[84:87],v[172:175]\n"
"v_cvt_pk_bf16_f32 v75,v119,v120\n"
"v_or3_b32 v118,v118,v71,v72\n"
"v_mfma_f32_16x16x32_bf16 v[204:207],a[108:111],v[88:91],v[204:207]\n"
"v_exp_f32 v123,v25\n"
"v_mfma_f32_16x16x32_bf16 v[236:239],a[108:111],v[92:95],v[236:239]\n"
"buffer_load_dwordx4 v117,s[24:27],s64 offen offset:0 sc1 lds\n"
"v_mfma_f32_16x16x32_bf16 v[140:143],a[108:111],v[80:83],v[140:143]\n"
"v_exp_f32 v119,v26\n"
"v_mfma_f32_16x16x32_bf16 a[128:131],a[144:147],v[80:83],a[128:131]\n"
"v_cvt_pk_bf16_f32 v76,v122,v123\n"
"v_or3_b32 v118,v118,v73,v74\n"
"v_mfma_f32_16x16x32_bf16 v[144:147],a[112:115],v[80:83],v[144:147]\n"
"v_exp_f32 v120,v27\n"
"v_mfma_f32_16x16x32_bf16 v[176:179],a[112:115],v[84:87],v[176:179]\n"
"buffer_load_dwordx4 v117,s[24:27],s64 offen offset:1024 sc1 lds\n"
"v_mfma_f32_16x16x32_bf16 v[208:211],a[112:115],v[88:91],v[208:211]\n"
"v_exp_f32 v122,v28\n"
"v_mfma_f32_16x16x32_bf16 v[240:243],a[112:115],v[92:95],v[240:243]\n"
"v_cvt_pk_bf16_f32 v77,v119,v120\n"
"v_or3_b32 v118,v118,v75,v76\n"
"v_mfma_f32_16x16x32_bf16 v[244:247],a[116:119],v[92:95],v[244:247]\n"
"v_exp_f32 v123,v29\n"
"v_mfma_f32_16x16x32_bf16 v[148:151],a[116:119],v[80:83],v[148:151]\n"
"buffer_load_dwordx4 v117,s[24:27],s64 offen offset:2048 sc1 lds\n"
"v_mfma_f32_16x16x32_bf16 v[180:183],a[116:119],v[84:87],v[180:183]\n"
"v_exp_f32 v119,v30\n"
"v_mfma_f32_16x16x32_bf16 v[212:215],a[116:119],v[88:91],v[212:215]\n"
"ds_read_b32 v125,v124\n"
"v_cvt_pk_bf16_f32 v78,v122,v123\n"
"v_mfma_f32_16x16x32_bf16 v[216:219],a[120:123],v[88:91],v[216:219]\n"
"v_exp_f32 v120,v31\n"
"v_mfma_f32_16x16x32_bf16 v[248:251],a[120:123],v[92:95],v[248:251]\n"
"buffer_load_dwordx4 v117,s[24:27],s64 offen offset:3072 sc1 lds\n"
"v_mfma_f32_16x16x32_bf16 v[152:155],a[120:123],v[80:83],v[152:155]\n"
"buffer_store_dwordx4 a[244:247],v112,s[24:27],s56 offen offset:1024 sc1\n"
"v_mfma_f32_16x16x32_bf16 v[184:187],a[120:123],v[84:87],v[184:187]\n"
"s_add_u32 s64,s64,16384\n"
"v_cvt_pk_bf16_f32 v79,v119,v120\n"
"v_or3_b32 v118,v118,v77,v78\n"
"v_mfma_f32_16x16x32_bf16 v[188:191],a[124:127],v[84:87],v[188:191]\n"
"s_waitcnt lgkmcnt(0)\n"
"v_subrev_u32 v125,s101,v125\n"
"v_or3_b32 v125,v118,v79,v125\n"
"v_mfma_f32_16x16x32_bf16 v[220:223],a[124:127],v[88:91],v[220:223]\n"
"v_and_b32 v125,0x40004000,v125\n"
"v_cmp_ne_u32 vcc,0,v125\n"
"v_mfma_f32_16x16x32_bf16 v[252:255],a[124:127],v[92:95],v[252:255]\n"
"v_mfma_f32_16x16x32_bf16 v[156:159],a[124:127],v[80:83],v[156:159]\n"
"s_cbranch_vccnz .Lfk_cs_g0\n"
".Lfk_cr_g0:\n"
"v_mfma_f32_16x16x32_bf16 v[0:3],a[64:67],a[0:3],v[96:99]\n"
"s_add_u32 s101,s101,4\n"
"v_exp_f32 v122,v32\n"
"v_mfma_f32_16x16x32_bf16 v[0:3],a[68:71],a[4:7],v[0:3]\n"
"s_add_u32 m0,s55,8192\n"
"v_exp_f32 v123,v33\n"
"v_mfma_f32_16x16x32_bf16 v[0:3],a[72:75],a[8:11],v[0:3]\n"
"ds_read_b128 a[96:99],v114 offset:40960\n"
"ds_read_b128 a[100:103],v114 offset:41984\n"
"v_mfma_f32_16x16x32_bf16 v[0:3],a[76:79],a[12:15],v[0:3]\n"
"s_add_u32 s54,s54,s22\n"
"v_exp_f32 v119,v34\n"
"v_mfma_f32_16x16x32_bf16 v[4:7],a[92:95],a[12:15],v[96:99]\n"
"ds_read_b128 a[104:107],v114 offset:43008\n"
"v_cvt_pk_bf16_f32 v80,v122,v123\n"
"v_mfma_f32_16x16x32_bf16 v[4:7],a[88:91],a[8:11],v[4:7]\n"
"v_exp_f32 v120,v35\n"
"v_mfma_f32_16x16x32_bf16 v[4:7],a[84:87],a[4:7],v[4:7]\n"
"ds_read_b128 a[108:111],v114 offset:44032\n"
"ds_read_b128 a[112:115],v114 offset:45056\n"
"v_mfma_f32_16x16x32_bf16 v[4:7],a[80:83],a[0:3],v[4:7]\n"
"v_exp_f32 v122,v36\n"
"v_mfma_f32_16x16x32_bf16 v[12:15],a[80:83],a[16:19],v[100:103]\n"
"ds_read_b128 a[116:119],v114 offset:46080\n"
"v_cvt_pk_bf16_f32 v81,v119,v120\n"
"v_mfma_f32_16x16x32_bf16 v[12:15],a[84:87],a[20:23],v[12:15]\n"
"v_exp_f32 v123,v37\n"
"v_mfma_f32_16x16x32_bf16 v[12:15],a[88:91],a[24:27],v[12:15]\n"
"ds_read_b128 a[120:123],v114 offset:47104\n"
"ds_read_b128 a[124:127],v114 offset:48128\n"
"v_mfma_f32_16x16x32_bf16 v[12:15],a[92:95],a[28:31],v[12:15]\n"
"v_exp_f32 v119,v38\n"
"v_mfma_f32_16x16x32_bf16 v[8:11],a[76:79],a[28:31],v[100:103]\n"
"v_cvt_pk_bf16_f32 v82,v122,v123\n"
"v_or3_b32 v118,v80,v81,v82\n"
"v_mfma_f32_16x16x32_bf16 v[8:11],a[72:75],a[24:27],v[8:11]\n"
"v_exp_f32 v120,v39\n"
"v_mfma_f32_16x16x32_bf16 v[8:11],a[68:71],a[20:23],v[8:11]\n"
"buffer_load_dwordx4 v115,s[32:35],s54 offen sc1 lds\n"
"v_mfma_f32_16x16x32_bf16 v[8:11],a[64:67],a[16:19],v[8:11]\n"
"s_add_u32 m0,s67,49152\n"
"v_exp_f32 v122,v40\n"
"v_mfma_f32_16x16x32_bf16 v[16:19],a[64:67],a[32:35],v[104:107]\n"
"v_cvt_pk_bf16_f32 v83,v119,v120\n"
"v_mfma_f32_16x16x32_bf16 v[16:19],a[68:71],a[36:39],v[16:19]\n"
"v_exp_f32 v123,v41\n"
"v_mfma_f32_16x16x32_bf16 v[16:19],a[72:75],a[40:43],v[16:19]\n"
"buffer_load_dwordx4 v112,s[24:27],s64 offen offset:0 sc1 lds\n"
"v_mfma_f32_16x16x32_bf16 v[16:19],a[76:79],a[44:47],v[16:19]\n"
"v_exp_f32 v119,v42\n"
"v_mfma_f32_16x16x32_bf16 v[20:23],a[92:95],a[44:47],v[104:107]\n"
"v_cvt_pk_bf16_f32 v84,v122,v123\n"
"v_or3_b32 v118,v118,v83,v84\n"
"v_mfma_f32_16x16x32_bf16 v[20:23],a[88:91],a[40:43],v[20:23]\n"
"v_exp_f32 v120,v43\n"
"v_mfma_f32_16x16x32_bf16 v[20:23],a[84:87],a[36:39],v[20:23]\n"
"buffer_load_dwordx4 v112,s[24:27],s64 offen offset:1024 sc1 lds\n"
"v_mfma_f32_16x16x32_bf16 v[20:23],a[80:83],a[32:35],v[20:23]\n"
"v_exp_f32 v122,v44\n"
"v_mfma_f32_16x16x32_bf16 v[28:31],a[80:83],a[48:51],v[108:111]\n"
"v_cvt_pk_bf16_f32 v85,v119,v120\n"
"v_mfma_f32_16x16x32_bf16 v[28:31],a[84:87],a[52:55],v[28:31]\n"
"v_exp_f32 v123,v45\n"
"v_mfma_f32_16x16x32_bf16 v[28:31],a[88:91],a[56:59],v[28:31]\n"
"buffer_load_dwordx4 v112,s[24:27],s64 offen offset:2048 sc1 lds\n"
"v_mfma_f32_16x16x32_bf16 v[28:31],a[92:95],a[60:63],v[28:31]\n"
"v_exp_f32 v119,v46\n"
"v_mfma_f32_16x16x32_bf16 v[24:27],a[76:79],a[60:63],v[108:111]\n"
"v_cvt_pk_bf16_f32 v86,v122,v123\n"
"v_or3_b32 v118,v118,v85,v86\n"
"v_mfma_f32_16x16x32_bf16 v[24:27],a[72:75],a[56:59],v[24:27]\n"
"v_exp_f32 v120,v47\n"
"v_mfma_f32_16x16x32_bf16 v[24:27],a[68:71],a[52:55],v[24:27]\n"
"buffer_load_dwordx4 v112,s[24:27],s64 offen offset:3072 sc1 lds\n"
"v_mfma_f32_16x16x32_bf16 v[24:27],a[64:67],a[48:51],v[24:27]\n"
"s_waitcnt lgkmcnt(0)\n"
"v_exp_f32 v122,v48\n"
"v_mfma_f32_16x16x32_bf16 v[128:131],a[96:99],v[64:67],v[128:131]\n"
"v_exp_f32 v123,v49\n"
"v_mfma_f32_16x16x32_bf16 v[160:163],a[96:99],v[68:71],v[160:163]\n"
"ds_read_b128 a[64:67],v113 offset:32768\n"
"v_cvt_pk_bf16_f32 v87,v119,v120\n"
"v_mfma_f32_16x16x32_bf16 v[192:195],a[96:99],v[72:75],v[192:195]\n"
"s_add_u32 m0,s67,53248\n"
"v_exp_f32 v119,v50\n"
"v_mfma_f32_16x16x32_bf16 v[224:227],a[96:99],v[76:79],v[224:227]\n"
"ds_read_b128 a[68:71],v113 offset:33792\n"
"v_cvt_pk_bf16_f32 v88,v122,v123\n"
"v_mfma_f32_16x16x32_bf16 a[140:143],a[144:147],v[76:79],a[140:143]\n"
"v_exp_f32 v120,v51\n"
"v_mfma_f32_16x16x32_bf16 v[228:231],a[100:103],v[76:79],v[228:231]\n"
"ds_read_b128 a[72:75],v113 offset:34816\n"
"ds_read_b128 a[76:79],v113 offset:35840\n"
"v_mfma_f32_16x16x32_bf16 v[132:135],a[100:103],v[64:67],v[132:135]\n"
"v_exp_f32 v122,v52\n"
"v_mfma_f32_16x16x32_bf16 v[164:167],a[100:103],v[68:71],v[164:167]\n"
"ds_read_b128 a[80:83],v113 offset:36864\n"
"v_cvt_pk_bf16_f32 v89,v119,v120\n"
"v_mfma_f32_16x16x32_bf16 v[196:199],a[100:103],v[72:75],v[196:199]\n"
"v_exp_f32 v123,v53\n"
"v_mfma_f32_16x16x32_bf16 a[136:139],a[144:147],v[72:75],a[136:139]\n"
"ds_read_b128 a[84:87],v113 offset:37888\n"
"ds_read_b128 a[88:91],v113 offset:38912\n"
"v_mfma_f32_16x16x32_bf16 v[200:203],a[104:107],v[72:75],v[200:203]\n"
"v_exp_f32 v119,v54\n"
"v_mfma_f32_16x16x32_bf16 v[232:235],a[104:107],v[76:79],v[232:235]\n"
"ds_read_b128 a[92:95],v113 offset:39936\n"
"v_cvt_pk_bf16_f32 v90,v122,v123\n"
"v_mfma_f32_16x16x32_bf16 v[136:139],a[104:107],v[64:67],v[136:139]\n"
"v_exp_f32 v120,v55\n"
"v_mfma_f32_16x16x32_bf16 v[168:171],a[104:107],v[68:71],v[168:171]\n"
"s_waitcnt vmcnt(16)\n"
"ds_add_u32 v124,a152\n"
"v_mfma_f32_16x16x32_bf16 a[132:135],a[144:147],v[68:71],a[132:135]\n"
"v_exp_f32 v122,v56\n"
"v_mfma_f32_16x16x32_bf16 v[172:175],a[108:111],v[68:71],v[172:175]\n"
"v_cvt_pk_bf16_f32 v91,v119,v120\n"
"v_or3_b32 v118,v118,v87,v88\n"
"v_mfma_f32_16x16x32_bf16 v[204:207],a[108:111],v[72:75],v[204:207]\n"
"v_exp_f32 v123,v57\n"
"v_mfma_f32_16x16x32_bf16 v[236:239],a[108:111],v[76:79],v[236:239]\n"
"buffer_load_dwordx4 v117,s[24:27],s64 offen offset:0 sc1 lds\n"
"v_mfma_f32_16x16x32_bf16 v[140:143],a[108:111],v[64:67],v[140:143]\n"
"v_exp_f32 v119,v58\n"
"v_mfma_f32_16x16x32_bf16 a[128:131],a[144:147],v[64:67],a[128:131]\n"
"v_cvt_pk_bf16_f32 v92,v122,v123\n"
"v_or3_b32 v118,v118,v89,v90\n"
"v_mfma_f32_16x16x32_bf16 v[144:147],a[112:115],v[64:67],v[144:147]\n"
"v_exp_f32 v120,v59\n"
"v_mfma_f32_16x16x32_bf16 v[176:179],a[112:115],v[68:71],v[176:179]\n"
"buffer_load_dwordx4 v117,s[24:27],s64 offen offset:1024 sc1 lds\n"
"v_mfma_f32_16x16x32_bf16 v[208:211],a[112:115],v[72:75],v[208:211]\n"
"v_exp_f32 v122,v60\n"
"v_mfma_f32_16x16x32_bf16 v[240:243],a[112:115],v[76:79],v[240:243]\n"
"v_cvt_pk_bf16_f32 v93,v119,v120\n"
"v_or3_b32 v118,v118,v91,v92\n"
"v_mfma_f32_16x16x32_bf16 v[244:247],a[116:119],v[76:79],v[244:247]\n"
"v_exp_f32 v123,v61\n"
"v_mfma_f32_16x16x32_bf16 v[148:151],a[116:119],v[64:67],v[148:151]\n"
"buffer_load_dwordx4 v117,s[24:27],s64 offen offset:2048 sc1 lds\n"
"v_mfma_f32_16x16x32_bf16 v[180:183],a[116:119],v[68:71],v[180:183]\n"
"v_exp_f32 v119,v62\n"
"v_mfma_f32_16x16x32_bf16 v[212:215],a[116:119],v[72:75],v[212:215]\n"
"ds_read_b32 v125,v124\n"
"v_cvt_pk_bf16_f32 v94,v122,v123\n"
"v_mfma_f32_16x16x32_bf16 v[216:219],a[120:123],v[72:75],v[216:219]\n"
"v_exp_f32 v120,v63\n"
"v_mfma_f32_16x16x32_bf16 v[248:251],a[120:123],v[76:79],v[248:251]\n"
"buffer_load_dwordx4 v117,s[24:27],s64 offen offset:3072 sc1 lds\n"
"v_mfma_f32_16x16x32_bf16 v[152:155],a[120:123],v[64:67],v[152:155]\n"
"buffer_store_dwordx4 a[248:251],v112,s[24:27],s56 offen offset:2048 sc1\n"
"v_mfma_f32_16x16x32_bf16 v[184:187],a[120:123],v[68:71],v[184:187]\n"
"s_add_u32 s64,s64,16384\n"
"v_cvt_pk_bf16_f32 v95,v119,v120\n"
"v_or3_b32 v118,v118,v93,v94\n"
"v_mfma_f32_16x16x32_bf16 v[188:191],a[124:127],v[68:71],v[188:191]\n"
"s_waitcnt lgkmcnt(0)\n"
"v_subrev_u32 v125,s101,v125\n"
"v_or3_b32 v125,v118,v95,v125\n"
"v_mfma_f32_16x16x32_bf16 v[220:223],a[124:127],v[72:75],v[220:223]\n"
"v_and_b32 v125,0x40004000,v125\n"
"v_cmp_ne_u32 vcc,0,v125\n"
"v_mfma_f32_16x16x32_bf16 v[252:255],a[124:127],v[76:79],v[252:255]\n"
"v_mfma_f32_16x16x32_bf16 v[156:159],a[124:127],v[64:67],v[156:159]\n"
"s_cbranch_vccnz .Lfk_cs_g1\n"
".Lfk_cr_g1:\n"
"v_mfma_f32_16x16x32_bf16 v[32:35],a[64:67],a[0:3],v[96:99]\n"
"s_add_u32 s101,s101,4\n"
"v_exp_f32 v122,v0\n"
"v_mfma_f32_16x16x32_bf16 v[32:35],a[68:71],a[4:7],v[32:35]\n"
"s_add_u32 m0,s55,12288\n"
"v_exp_f32 v123,v1\n"
"v_mfma_f32_16x16x32_bf16 v[32:35],a[72:75],a[8:11],v[32:35]\n"
"ds_read_b128 a[96:99],v114 offset:8192\n"
"ds_read_b128 a[100:103],v114 offset:9216\n"
"v_mfma_f32_16x16x32_bf16 v[32:35],a[76:79],a[12:15],v[32:35]\n"
"s_add_u32 s54,s54,s22\n"
"v_exp_f32 v119,v2\n"
"v_mfma_f32_16x16x32_bf16 v[36:39],a[92:95],a[12:15],v[96:99]\n"
"ds_read_b128 a[104:107],v114 offset:10240\n"
"v_cvt_pk_bf16_f32 v64,v122,v123\n"
"v_mfma_f32_16x16x32_bf16 v[36:39],a[88:91],a[8:11],v[36:39]\n"
"v_exp_f32 v120,v3\n"
"v_mfma_f32_16x16x32_bf16 v[36:39],a[84:87],a[4:7],v[36:39]\n"
"ds_read_b128 a[108:111],v114 offset:11264\n"
"ds_read_b128 a[112:115],v114 offset:12288\n"
"v_mfma_f32_16x16x32_bf16 v[36:39],a[80:83],a[0:3],v[36:39]\n"
"v_exp_f32 v122,v4\n"
"v_mfma_f32_16x16x32_bf16 v[44:47],a[80:83],a[16:19],v[100:103]\n"
"ds_read_b128 a[116:119],v114 offset:13312\n"
"v_cvt_pk_bf16_f32 v65,v119,v120\n"
"v_mfma_f32_16x16x32_bf16 v[44:47],a[84:87],a[20:23],v[44:47]\n"
"v_exp_f32 v123,v5\n"
"v_mfma_f32_16x16x32_bf16 v[44:47],a[88:91],a[24:27],v[44:47]\n"
"ds_read_b128 a[120:123],v114 offset:14336\n"
"ds_read_b128 a[124:127],v114 offset:15360\n"
"v_mfma_f32_16x16x32_bf16 v[44:47],a[92:95],a[28:31],v[44:47]\n"
"v_exp_f32 v119,v6\n"
"v_mfma_f32_16x16x32_bf16 v[40:43],a[76:79],a[28:31],v[100:103]\n"
"v_cvt_pk_bf16_f32 v66,v122,v123\n"
"v_or3_b32 v118,v64,v65,v66\n"
"v_mfma_f32_16x16x32_bf16 v[40:43],a[72:75],a[24:27],v[40:43]\n"
"v_exp_f32 v120,v7\n"
"v_mfma_f32_16x16x32_bf16 v[40:43],a[68:71],a[20:23],v[40:43]\n"
"buffer_load_dwordx4 v115,s[32:35],s54 offen sc1 lds\n"
"v_mfma_f32_16x16x32_bf16 v[40:43],a[64:67],a[16:19],v[40:43]\n"
"s_cmp_eq_u32 s64,s65\n"
"v_exp_f32 v122,v8\n"
"v_mfma_f32_16x16x32_bf16 v[48:51],a[64:67],a[32:35],v[104:107]\n"
"v_cvt_pk_bf16_f32 v67,v119,v120\n"
"s_cselect_b32 s64,s66,s64\n"
"v_mfma_f32_16x16x32_bf16 v[48:51],a[68:71],a[36:39],v[48:51]\n"
"s_add_u32 m0,s67,32768\n"
"v_exp_f32 v123,v9\n"
"v_mfma_f32_16x16x32_bf16 v[48:51],a[72:75],a[40:43],v[48:51]\n"
"buffer_load_dwordx4 v112,s[24:27],s64 offen offset:0 sc1 lds\n"
"v_mfma_f32_16x16x32_bf16 v[48:51],a[76:79],a[44:47],v[48:51]\n"
"v_exp_f32 v119,v10\n"
"v_mfma_f32_16x16x32_bf16 v[52:55],a[92:95],a[44:47],v[104:107]\n"
"v_cvt_pk_bf16_f32 v68,v122,v123\n"
"v_or3_b32 v118,v118,v67,v68\n"
"v_mfma_f32_16x16x32_bf16 v[52:55],a[88:91],a[40:43],v[52:55]\n"
"v_exp_f32 v120,v11\n"
"v_mfma_f32_16x16x32_bf16 v[52:55],a[84:87],a[36:39],v[52:55]\n"
"buffer_load_dwordx4 v112,s[24:27],s64 offen offset:1024 sc1 lds\n"
"v_mfma_f32_16x16x32_bf16 v[52:55],a[80:83],a[32:35],v[52:55]\n"
"v_exp_f32 v122,v12\n"
"v_mfma_f32_16x16x32_bf16 v[60:63],a[80:83],a[48:51],v[108:111]\n"
"v_cvt_pk_bf16_f32 v69,v119,v120\n"
"v_mfma_f32_16x16x32_bf16 v[60:63],a[84:87],a[52:55],v[60:63]\n"
"v_exp_f32 v123,v13\n"
"v_mfma_f32_16x16x32_bf16 v[60:63],a[88:91],a[56:59],v[60:63]\n"
"buffer_load_dwordx4 v112,s[24:27],s64 offen offset:2048 sc1 lds\n"
"v_mfma_f32_16x16x32_bf16 v[60:63],a[92:95],a[60:63],v[60:63]\n"
"v_exp_f32 v119,v14\n"
"v_mfma_f32_16x16x32_bf16 v[56:59],a[76:79],a[60:63],v[108:111]\n"
"v_cvt_pk_bf16_f32 v70,v122,v123\n"
"v_or3_b32 v118,v118,v69,v70\n"
"v_mfma_f32_16x16x32_bf16 v[56:59],a[72:75],a[56:59],v[56:59]\n"
"v_exp_f32 v120,v15\n"
"v_mfma_f32_16x16x32_bf16 v[56:59],a[68:71],a[52:55],v[56:59]\n"
"buffer_load_dwordx4 v112,s[24:27],s64 offen offset:3072 sc1 lds\n"
"v_mfma_f32_16x16x32_bf16 v[56:59],a[64:67],a[48:51],v[56:59]\n"
"s_waitcnt lgkmcnt(0)\n"
"v_exp_f32 v122,v16\n"
"v_mfma_f32_16x16x32_bf16 v[128:131],a[96:99],v[80:83],v[128:131]\n"
"v_exp_f32 v123,v17\n"
"v_mfma_f32_16x16x32_bf16 v[160:163],a[96:99],v[84:87],v[160:163]\n"
"ds_read_b128 a[64:67],v113 offset:0\n"
"v_cvt_pk_bf16_f32 v71,v119,v120\n"
"v_mfma_f32_16x16x32_bf16 v[192:195],a[96:99],v[88:91],v[192:195]\n"
"s_add_u32 m0,s67,36864\n"
"v_exp_f32 v119,v18\n"
"v_mfma_f32_16x16x32_bf16 v[224:227],a[96:99],v[92:95],v[224:227]\n"
"ds_read_b128 a[68:71],v113 offset:1024\n"
"v_cvt_pk_bf16_f32 v72,v122,v123\n"
"v_mfma_f32_16x16x32_bf16 a[140:143],a[144:147],v[92:95],a[140:143]\n"
"v_exp_f32 v120,v19\n"
"v_mfma_f32_16x16x32_bf16 v[228:231],a[100:103],v[92:95],v[228:231]\n"
"ds_read_b128 a[72:75],v113 offset:2048\n"
"ds_read_b128 a[76:79],v113 offset:3072\n"
"v_mfma_f32_16x16x32_bf16 v[132:135],a[100:103],v[80:83],v[132:135]\n"
"v_exp_f32 v122,v20\n"
"v_mfma_f32_16x16x32_bf16 v[164:167],a[100:103],v[84:87],v[164:167]\n"
"ds_read_b128 a[80:83],v113 offset:4096\n"
"v_cvt_pk_bf16_f32 v73,v119,v120\n"
"v_mfma_f32_16x16x32_bf16 v[196:199],a[100:103],v[88:91],v[196:199]\n"
"v_exp_f32 v123,v21\n"
"v_mfma_f32_16x16x32_bf16 a[136:139],a[144:147],v[88:91],a[136:139]\n"
"ds_read_b128 a[84:87],v113 offset:5120\n"
"ds_read_b128 a[88:91],v113 offset:6144\n"
"v_mfma_f32_16x16x32_bf16 v[200:203],a[104:107],v[88:91],v[200:203]\n"
"v_exp_f32 v119,v22\n"
"v_mfma_f32_16x16x32_bf16 v[232:235],a[104:107],v[92:95],v[232:235]\n"
"ds_read_b128 a[92:95],v113 offset:7168\n"
"v_cvt_pk_bf16_f32 v74,v122,v123\n"
"v_mfma_f32_16x16x32_bf16 v[136:139],a[104:107],v[80:83],v[136:139]\n"
"v_exp_f32 v120,v23\n"
"v_mfma_f32_16x16x32_bf16 v[168:171],a[104:107],v[84:87],v[168:171]\n"
"s_waitcnt vmcnt(16)\n"
"ds_add_u32 v124,a152\n"
"v_mfma_f32_16x16x32_bf16 a[132:135],a[144:147],v[84:87],a[132:135]\n"
"v_exp_f32 v122,v24\n"
"v_mfma_f32_16x16x32_bf16 v[172:175],a[108:111],v[84:87],v[172:175]\n"
"v_cvt_pk_bf16_f32 v75,v119,v120\n"
"v_or3_b32 v118,v118,v71,v72\n"
"v_mfma_f32_16x16x32_bf16 v[204:207],a[108:111],v[88:91],v[204:207]\n"
"v_exp_f32 v123,v25\n"
"v_mfma_f32_16x16x32_bf16 v[236:239],a[108:111],v[92:95],v[236:239]\n"
"buffer_load_dwordx4 v117,s[24:27],s64 offen offset:0 sc1 lds\n"
"v_mfma_f32_16x16x32_bf16 v[140:143],a[108:111],v[80:83],v[140:143]\n"
"v_exp_f32 v119,v26\n"
"v_mfma_f32_16x16x32_bf16 a[128:131],a[144:147],v[80:83],a[128:131]\n"
"v_cvt_pk_bf16_f32 v76,v122,v123\n"
"v_or3_b32 v118,v118,v73,v74\n"
"v_mfma_f32_16x16x32_bf16 v[144:147],a[112:115],v[80:83],v[144:147]\n"
"v_exp_f32 v120,v27\n"
"v_mfma_f32_16x16x32_bf16 v[176:179],a[112:115],v[84:87],v[176:179]\n"
"buffer_load_dwordx4 v117,s[24:27],s64 offen offset:1024 sc1 lds\n"
"v_mfma_f32_16x16x32_bf16 v[208:211],a[112:115],v[88:91],v[208:211]\n"
"v_exp_f32 v122,v28\n"
"v_mfma_f32_16x16x32_bf16 v[240:243],a[112:115],v[92:95],v[240:243]\n"
"v_cvt_pk_bf16_f32 v77,v119,v120\n"
"v_or3_b32 v118,v118,v75,v76\n"
"v_mfma_f32_16x16x32_bf16 v[244:247],a[116:119],v[92:95],v[244:247]\n"
"v_exp_f32 v123,v29\n"
"v_mfma_f32_16x16x32_bf16 v[148:151],a[116:119],v[80:83],v[148:151]\n"
"buffer_load_dwordx4 v117,s[24:27],s64 offen offset:2048 sc1 lds\n"
"v_mfma_f32_16x16x32_bf16 v[180:183],a[116:119],v[84:87],v[180:183]\n"
"v_exp_f32 v119,v30\n"
"v_mfma_f32_16x16x32_bf16 v[212:215],a[116:119],v[88:91],v[212:215]\n"
"ds_read_b32 v125,v124\n"
"v_cvt_pk_bf16_f32 v78,v122,v123\n"
"v_mfma_f32_16x16x32_bf16 v[216:219],a[120:123],v[88:91],v[216:219]\n"
"v_exp_f32 v120,v31\n"
"v_mfma_f32_16x16x32_bf16 v[248:251],a[120:123],v[92:95],v[248:251]\n"
"buffer_load_dwordx4 v117,s[24:27],s64 offen offset:3072 sc1 lds\n"
"v_mfma_f32_16x16x32_bf16 v[152:155],a[120:123],v[80:83],v[152:155]\n"
"buffer_store_dwordx4 a[252:255],v112,s[24:27],s56 offen offset:3072 sc1\n"
"v_mfma_f32_16x16x32_bf16 v[184:187],a[120:123],v[84:87],v[184:187]\n"
"s_add_u32 s64,s64,16384\n"
"s_add_u32 s89,s89,1\n"
"v_cvt_pk_bf16_f32 v79,v119,v120\n"
"v_mfma_f32_16x16x32_bf16 v[188:191],a[124:127],v[84:87],v[188:191]\n"
"s_waitcnt lgkmcnt(0)\n"
"v_subrev_u32 v125,s101,v125\n"
"v_or3_b32 v118,v118,v77,v78\n"
"v_mfma_f32_16x16x32_bf16 v[220:223],a[124:127],v[88:91],v[220:223]\n"
"v_or3_b32 v125,v118,v79,v125\n"
"v_and_b32 v125,0x40004000,v125\n"
"v_mfma_f32_16x16x32_bf16 v[252:255],a[124:127],v[92:95],v[252:255]\n"
"v_cmp_ne_u32 vcc,0,v125\n"
"v_mfma_f32_16x16x32_bf16 v[156:159],a[124:127],v[80:83],v[156:159]\n"
"s_cbranch_vccnz .Lfk_cs_g2\n"
".Lfk_cr_g2:\n"
".Lfk_Hx:\n"
"s_cmp_lt_u32 s89,8\n"
"s_cbranch_scc1 .Lfk_H\n"
"v_max3_f32 v118,v32,v33,v34\n"
"v_max3_f32 v119,v35,v36,v37\n"
"v_max3_f32 v119,v119,v38,v39\n"
"v_max3_f32 v118,v118,v40,v41\n"
"v_max3_f32 v119,v119,v42,v43\n"
"v_max3_f32 v118,v118,v44,v45\n"
"v_max3_f32 v119,v119,v46,v47\n"
"v_max3_f32 v118,v118,v48,v49\n"
"v_max3_f32 v119,v119,v50,v51\n"
"v_max3_f32 v118,v118,v52,v53\n"
"v_max3_f32 v119,v119,v54,v55\n"
"v_max3_f32 v118,v118,v56,v57\n"
"v_max3_f32 v119,v119,v58,v59\n"
"v_max3_f32 v118,v118,v60,v61\n"
"v_max3_f32 v119,v119,v62,v63\n"
"v_max_f32 v118,v118,v119\n"
"v_cmp_lt_f32 vcc,s93,v118\n"
"s_nop 0\n"
"s_cbranch_vccnz .Lfk_cs_x1\n"
".Lfk_cr_x1:\n"
"s_cmp_ge_u32 s52,2\n"
"s_cbranch_scc1 .Lfk_tailB\n"
".Lfk_tailA:\n"
"s_add_u32 s82,s66,28672\n"
"s_cmp_eq_u32 s52,0\n"
"s_cselect_b32 s67,77824,s67\n"
"s_cselect_b32 s64,s82,s64\n"
"s_waitcnt vmcnt(11) lgkmcnt(0)\n"
"s_barrier\n"
"v_mfma_f32_16x16x32_bf16 v[16:19],a[64:67],a[32:35],v[104:107]\n"
"ds_read_b128 a[96:99],v114 offset:24576\n"
"ds_read_b128 a[100:103],v114 offset:25600\n"
"ds_read_b128 a[104:107],v114 offset:26624\n"
"v_mfma_f32_16x16x32_bf16 v[16:19],a[68:71],a[36:39],v[16:19]\n"
"ds_read_b128 a[108:111],v114 offset:27648\n"
"ds_read_b128 a[112:115],v114 offset:28672\n"
"ds_read_b128 a[116:119],v114 offset:29696\n"
"v_mfma_f32_16x16x32_bf16 v[16:19],a[72:75],a[40:43],v[16:19]\n"
"ds_read_b128 a[120:123],v114 offset:30720\n"
"ds_read_b128 a[124:127],v114 offset:31744\n"
"v_add_f32 v8,0x80000000,v126\n"
"v_mfma_f32_16x16x32_bf16 v[16:19],a[76:79],a[44:47],v[16:19]\n"
"v_min_f32 v8,v8,v100\n"
"v_add_f32 v9,0xf149f2ca,v126\n"
"v_min_f32 v9,v9,v101\n"
"v_mfma_f32_16x16x32_bf16 v[20:23],a[92:95],a[44:47],v[104:107]\n"
"v_add_f32 v10,0xf1c9f2ca,v126\n"
"v_min_f32 v10,v10,v102\n"
"v_add_f32 v11,0xf2177617,v126\n"
"v_mfma_f32_16x16x32_bf16 v[20:23],a[88:91],a[40:43],v[20:23]\n"
"v_min_f32 v11,v11,v103\n"
"v_add_f32 v12,0xf349f2ca,v126\n"
"v_min_f32 v12,v12,v100\n"
"v_mfma_f32_16x16x32_bf16 v[20:23],a[84:87],a[36:39],v[20:23]\n"
"v_add_f32 v13,0xf35691f6,v126\n"
"v_min_f32 v13,v13,v101\n"
"v_add_f32 v14,0xf3633123,v126\n"
"v_mfma_f32_16x16x32_bf16 v[20:23],a[80:83],a[32:35],v[20:23]\n"
"v_min_f32 v14,v14,v102\n"
"v_add_f32 v15,0xf36fd050,v126\n"
"v_min_f32 v15,v15,v103\n"
"v_mfma_f32_16x16x32_bf16 v[28:31],a[80:83],a[48:51],v[108:111]\n"
"s_add_u32 m0,s67,0\n"
"v_exp_f32 v32,v32\n"
"v_mfma_f32_16x16x32_bf16 v[28:31],a[84:87],a[52:55],v[28:31]\n"
"buffer_load_dwordx4 v112,s[24:27],s64 offen offset:0 sc1 lds\n"
"buffer_load_dwordx4 v112,s[24:27],s64 offen offset:1024 sc1 lds\n"
"v_exp_f32 v33,v33\n"
"v_mfma_f32_16x16x32_bf16 v[28:31],a[88:91],a[56:59],v[28:31]\n"
"v_exp_f32 v34,v34\n"
"v_cvt_pk_bf16_f32 v80,v32,v33\n"
"v_mfma_f32_16x16x32_bf16 v[28:31],a[92:95],a[60:63],v[28:31]\n"
"v_exp_f32 v35,v35\n"
"v_max3_f32 v118,v16,v17,v18\n"
"v_exp_f32 v36,v36\n"
"v_mfma_f32_16x16x32_bf16 v[24:27],a[76:79],a[60:63],v[108:111]\n"
"buffer_load_dwordx4 v112,s[24:27],s64 offen offset:2048 sc1 lds\n"
"v_mfma_f32_16x16x32_bf16 v[24:27],a[72:75],a[56:59],v[24:27]\n"
"v_exp_f32 v37,v37\n"
"v_cvt_pk_bf16_f32 v81,v34,v35\n"
"v_mfma_f32_16x16x32_bf16 v[24:27],a[68:71],a[52:55],v[24:27]\n"
"v_exp_f32 v38,v38\n"
"v_cvt_pk_bf16_f32 v82,v36,v37\n"
"v_exp_f32 v39,v39\n"
"v_mfma_f32_16x16x32_bf16 v[24:27],a[64:67],a[48:51],v[24:27]\n"
"buffer_load_dwordx4 v112,s[24:27],s64 offen offset:3072 sc1 lds\n"
"v_mfma_f32_16x16x32_bf16 v[8:11],a[64:67],a[16:19],v[8:11]\n"
"s_add_u32 s64,s64,16384\n"
"v_exp_f32 v40,v40\n"
"v_accvgpr_read_b32 v119,a150\n"
"v_mfma_f32_16x16x32_bf16 v[8:11],a[68:71],a[20:23],v[8:11]\n"
"v_exp_f32 v41,v41\n"
"v_accvgpr_read_b32 v120,a151\n"
"v_mfma_f32_16x16x32_bf16 v[8:11],a[72:75],a[24:27],v[8:11]\n"
"v_exp_f32 v42,v42\n"
"v_xor_b32 v119,s80,v119\n"
"v_mfma_f32_16x16x32_bf16 v[8:11],a[76:79],a[28:31],v[8:11]\n"
"v_exp_f32 v43,v43\n"
"v_xor_b32 v120,s81,v120\n"
"v_mfma_f32_16x16x32_bf16 v[12:15],a[92:95],a[28:31],v[12:15]\n"
"v_exp_f32 v44,v44\n"
"v_or_b32 v119,v119,v120\n"
"v_mfma_f32_16x16x32_bf16 v[12:15],a[88:91],a[24:27],v[12:15]\n"
"s_add_u32 s18,s8,0\n"
"v_exp_f32 v45,v45\n"
"v_cmp_ne_u32 s[14:15],0,v119\n"
"v_mfma_f32_16x16x32_bf16 v[12:15],a[84:87],a[20:23],v[12:15]\n"
"s_cmp_lg_u32 s14,0\n"
"s_cselect_b32 s18,2146435072,s18\n"
"v_exp_f32 v46,v46\n"
"v_mfma_f32_16x16x32_bf16 v[12:15],a[80:83],a[16:19],v[12:15]\n"
"s_add_u32 m0,s30,0\n"
"ds_read_b128 a[64:67],v113 offset:16384\n"
"s_waitcnt lgkmcnt(1)\n"
"v_mfma_f32_16x16x32_bf16 v[128:131],a[96:99],v[64:67],v[128:131]\n"
"ds_read_b128 a[68:71],v113 offset:17408\n"
"ds_read_b128 a[72:75],v113 offset:18432\n"
"ds_read_b128 a[76:79],v113 offset:19456\n"
"v_mfma_f32_16x16x32_bf16 v[160:163],a[96:99],v[68:71],v[160:163]\n"
"ds_read_b128 a[80:83],v113 offset:20480\n"
"v_exp_f32 v47,v47\n"
"v_mfma_f32_16x16x32_bf16 v[192:195],a[96:99],v[72:75],v[192:195]\n"
"ds_read_b128 a[84:87],v113 offset:21504\n"
"ds_read_b128 a[88:91],v113 offset:22528\n"
"ds_read_b128 a[92:95],v113 offset:23552\n"
"v_exp_f32 v48,v48\n"
"v_mfma_f32_16x16x32_bf16 v[224:227],a[96:99],v[76:79],v[224:227]\n"
"buffer_load_dwordx4 v112,s[24:27],s18 offen offset:0 sc1 lds\n"
"v_exp_f32 v49,v49\n"
"v_mfma_f32_16x16x32_bf16 a[140:143],a[144:147],v[76:79],a[140:143]\n"
"buffer_load_dwordx4 v112,s[24:27],s18 offen offset:1024 sc1 lds\n"
"v_mfma_f32_16x16x32_bf16 v[228:231],a[100:103],v[76:79],v[228:231]\n"
"buffer_load_dwordx4 v112,s[24:27],s18 offen offset:2048 sc1 lds\n"
"buffer_load_dwordx4 v112,s[24:27],s18 offen offset:3072 sc1 lds\n"
"v_mfma_f32_16x16x32_bf16 v[132:135],a[100:103],v[64:67],v[132:135]\n"
"s_add_u32 m0,m0,4096\n"
"v_exp_f32 v50,v50\n"
"v_cvt_pk_bf16_f32 v83,v38,v39\n"
"v_mfma_f32_16x16x32_bf16 v[164:167],a[100:103],v[68:71],v[164:167]\n"
"buffer_load_dwordx4 v117,s[24:27],s18 offen offset:0 sc1 lds\n"
"v_cvt_pk_bf16_f32 v84,v40,v41\n"
"v_cvt_pk_bf16_f32 v85,v42,v43\n"
"v_mfma_f32_16x16x32_bf16 v[196:199],a[100:103],v[72:75],v[196:199]\n"
"v_exp_f32 v51,v51\n"
"v_cvt_pk_bf16_f32 v86,v44,v45\n"
"v_mfma_f32_16x16x32_bf16 a[136:139],a[144:147],v[72:75],a[136:139]\n"
"v_exp_f32 v52,v52\n"
"v_cvt_pk_bf16_f32 v87,v46,v47\n"
"v_mfma_f32_16x16x32_bf16 v[200:203],a[104:107],v[72:75],v[200:203]\n"
"buffer_load_dwordx4 v117,s[24:27],s18 offen offset:1024 sc1 lds\n"
"v_cvt_pk_bf16_f32 v88,v48,v49\n"
"v_cvt_pk_bf16_f32 v89,v50,v51\n"
"v_mfma_f32_16x16x32_bf16 v[232:235],a[104:107],v[76:79],v[232:235]\n"
"v_exp_f32 v53,v53\n"
"v_max3_f32 v118,v118,v19,v20\n"
"v_mfma_f32_16x16x32_bf16 v[136:139],a[104:107],v[64:67],v[136:139]\n"
"v_cvt_pk_bf16_f32 v90,v52,v53\n"
"v_max3_f32 v118,v118,v21,v22\n"
"v_max3_f32 v118,v118,v23,v28\n"
"v_exp_f32 v54,v54\n"
"v_mfma_f32_16x16x32_bf16 v[168:171],a[104:107],v[68:71],v[168:171]\n"
"buffer_load_dwordx4 v117,s[24:27],s18 offen offset:2048 sc1 lds\n"
"v_mfma_f32_16x16x32_bf16 a[132:135],a[144:147],v[68:71],a[132:135]\n"
"v_exp_f32 v55,v55\n"
"v_max3_f32 v118,v118,v29,v30\n"
"v_mfma_f32_16x16x32_bf16 v[172:175],a[108:111],v[68:71],v[172:175]\n"
"v_cvt_pk_bf16_f32 v91,v54,v55\n"
"v_max3_f32 v118,v118,v31,v24\n"
"v_max3_f32 v118,v118,v25,v26\n"
"v_exp_f32 v56,v56\n"
"v_mfma_f32_16x16x32_bf16 v[204:207],a[108:111],v[72:75],v[204:207]\n"
"buffer_load_dwordx4 v117,s[24:27],s18 offen offset:3072 sc1 lds\n"
"v_mfma_f32_16x16x32_bf16 v[236:239],a[108:111],v[76:79],v[236:239]\n"
"s_sub_u32 s82,s3,32768\n"
"v_max3_f32 v118,v118,v27,v8\n"
"v_max3_f32 v118,v118,v9,v10\n"
"v_mfma_f32_16x16x32_bf16 v[140:143],a[108:111],v[64:67],v[140:143]\n"
"v_exp_f32 v57,v57\n"
"v_max3_f32 v118,v118,v11,v12\n"
"v_exp_f32 v58,v58\n"
"v_mfma_f32_16x16x32_bf16 a[128:131],a[144:147],v[64:67],a[128:131]\n"
"buffer_load_dwordx4 a[0:3],v112,s[24:27],s82 offen offset:0 sc1\n"
"v_mfma_f32_16x16x32_bf16 v[144:147],a[112:115],v[64:67],v[144:147]\n"
"v_max3_f32 v118,v118,v13,v14\n"
"v_max_f32 v118,v118,v15\n"
"v_mfma_f32_16x16x32_bf16 v[176:179],a[112:115],v[68:71],v[176:179]\n"
"v_exp_f32 v59,v59\n"
"v_mfma_f32_16x16x32_bf16 v[208:211],a[112:115],v[72:75],v[208:211]\n"
"v_cvt_pk_bf16_f32 v92,v56,v57\n"
"v_cvt_pk_bf16_f32 v93,v58,v59\n"
"v_exp_f32 v60,v60\n"
"v_mfma_f32_16x16x32_bf16 v[240:243],a[112:115],v[76:79],v[240:243]\n"
"buffer_load_dwordx4 a[4:7],v112,s[24:27],s82 offen offset:1024 sc1\n"
"v_mfma_f32_16x16x32_bf16 v[244:247],a[116:119],v[76:79],v[244:247]\n"
"v_exp_f32 v61,v61\n"
"v_mfma_f32_16x16x32_bf16 v[148:151],a[116:119],v[64:67],v[148:151]\n"
"v_cmp_lt_f32 vcc,s93,v118\n"
"v_cvt_pk_bf16_f32 v94,v60,v61\n"
"v_mfma_f32_16x16x32_bf16 v[180:183],a[116:119],v[68:71],v[180:183]\n"
"buffer_load_dwordx4 a[8:11],v112,s[24:27],s82 offen offset:2048 sc1\n"
"v_mfma_f32_16x16x32_bf16 v[212:215],a[116:119],v[72:75],v[212:215]\n"
"v_exp_f32 v62,v62\n"
"v_mfma_f32_16x16x32_bf16 v[216:219],a[120:123],v[72:75],v[216:219]\n"
"v_exp_f32 v63,v63\n"
"v_mfma_f32_16x16x32_bf16 v[248:251],a[120:123],v[76:79],v[248:251]\n"
"v_cvt_pk_bf16_f32 v95,v62,v63\n"
"v_mfma_f32_16x16x32_bf16 v[152:155],a[120:123],v[64:67],v[152:155]\n"
"buffer_load_dwordx4 a[12:15],v112,s[24:27],s82 offen offset:3072 sc1\n"
"v_mfma_f32_16x16x32_bf16 v[184:187],a[120:123],v[68:71],v[184:187]\n"
"v_mfma_f32_16x16x32_bf16 v[188:191],a[124:127],v[68:71],v[188:191]\n"
"v_mfma_f32_16x16x32_bf16 v[220:223],a[124:127],v[72:75],v[220:223]\n"
"v_mfma_f32_16x16x32_bf16 v[252:255],a[124:127],v[76:79],v[252:255]\n"
"v_mfma_f32_16x16x32_bf16 v[156:159],a[124:127],v[64:67],v[156:159]\n"
"s_cbranch_vccnz .Lfk_cs_a3\n"
".Lfk_cr_a3:\n"
"s_waitcnt vmcnt(17) lgkmcnt(0)\n"
"s_barrier\n"
"v_mfma_f32_16x16x32_bf16 v[56:59],a[64:67],a[48:51],v[108:111]\n"
"ds_read_b128 a[96:99],v114 offset:57344\n"
"ds_read_b128 a[100:103],v114 offset:58368\n"
"ds_read_b128 a[104:107],v114 offset:59392\n"
"v_mfma_f32_16x16x32_bf16 v[56:59],a[68:71],a[52:55],v[56:59]\n"
"ds_read_b128 a[108:111],v114 offset:60416\n"
"ds_read_b128 a[112:115],v114 offset:61440\n"
"ds_read_b128 a[116:119],v114 offset:62464\n"
"v_mfma_f32_16x16x32_bf16 v[56:59],a[72:75],a[56:59],v[56:59]\n"
"ds_read_b128 a[120:123],v114 offset:63488\n"
"ds_read_b128 a[124:127],v114 offset:64512\n"
"v_add_f32 v48,0x80000000,v126\n"
"v_mfma_f32_16x16x32_bf16 v[56:59],a[76:79],a[60:63],v[56:59]\n"
"v_min_f32 v48,v48,v104\n"
"v_add_f32 v49,0xf149f2ca,v126\n"
"v_min_f32 v49,v49,v105\n"
"v_mfma_f32_16x16x32_bf16 v[60:63],a[92:95],a[60:63],v[108:111]\n"
"v_add_f32 v50,0xf1c9f2ca,v126\n"
"v_add_f32 v51,0xf2177617,v126\n"
"v_mfma_f32_16x16x32_bf16 v[60:63],a[88:91],a[56:59],v[60:63]\n"
"v_add_f32 v52,0xf349f2ca,v126\n"
"v_min_f32 v52,v52,v104\n"
"v_mfma_f32_16x16x32_bf16 v[60:63],a[84:87],a[52:55],v[60:63]\n"
"v_add_f32 v53,0xf35691f6,v126\n"
"v_min_f32 v53,v53,v105\n"
"v_add_f32 v54,0xf3633123,v126\n"
"v_mfma_f32_16x16x32_bf16 v[60:63],a[80:83],a[48:51],v[60:63]\n"
"v_min_f32 v54,v54,v106\n"
"v_add_f32 v55,0xf36fd050,v126\n"
"v_min_f32 v55,v55,v107\n"
"v_min_f32 v51,v51,v107\n"
"v_min_f32 v50,v50,v106\n"
"v_mfma_f32_16x16x32_bf16 v[52:55],a[80:83],a[32:35],v[52:55]\n"
"s_add_u32 m0,s67,16384\n"
"v_exp_f32 v8,v8\n"
"v_mfma_f32_16x16x32_bf16 v[52:55],a[84:87],a[36:39],v[52:55]\n"
"buffer_load_dwordx4 v112,s[24:27],s64 offen offset:0 sc1 lds\n"
"buffer_load_dwordx4 v112,s[24:27],s64 offen offset:1024 sc1 lds\n"
"v_exp_f32 v9,v9\n"
"v_mfma_f32_16x16x32_bf16 v[52:55],a[88:91],a[40:43],v[52:55]\n"
"v_exp_f32 v10,v10\n"
"v_cvt_pk_bf16_f32 v68,v8,v9\n"
"v_mfma_f32_16x16x32_bf16 v[52:55],a[92:95],a[44:47],v[52:55]\n"
"v_exp_f32 v11,v11\n"
"v_max3_f32 v118,v56,v57,v58\n"
"v_exp_f32 v12,v12\n"
"v_mfma_f32_16x16x32_bf16 v[48:51],a[76:79],a[44:47],v[48:51]\n"
"buffer_load_dwordx4 v112,s[24:27],s64 offen offset:2048 sc1 lds\n"
"v_mfma_f32_16x16x32_bf16 v[48:51],a[72:75],a[40:43],v[48:51]\n"
"v_exp_f32 v13,v13\n"
"v_cvt_pk_bf16_f32 v69,v10,v11\n"
"v_mfma_f32_16x16x32_bf16 v[48:51],a[68:71],a[36:39],v[48:51]\n"
"v_exp_f32 v14,v14\n"
"v_cvt_pk_bf16_f32 v70,v12,v13\n"
"v_exp_f32 v15,v15\n"
"v_mfma_f32_16x16x32_bf16 v[48:51],a[64:67],a[32:35],v[48:51]\n"
"ds_read_b128 a[64:67],v113 offset:49152\n"
"s_waitcnt lgkmcnt(1)\n"
"v_mfma_f32_16x16x32_bf16 v[128:131],a[96:99],v[80:83],v[128:131]\n"
"ds_read_b128 a[68:71],v113 offset:50176\n"
"v_exp_f32 v16,v16\n"
"v_mfma_f32_16x16x32_bf16 v[160:163],a[96:99],v[84:87],v[160:163]\n"
"ds_read_b128 a[72:75],v113 offset:51200\n"
"v_exp_f32 v17,v17\n"
"v_mfma_f32_16x16x32_bf16 v[192:195],a[96:99],v[88:91],v[192:195]\n"
"ds_read_b128 a[76:79],v113 offset:52224\n"
"ds_read_b128 a[80:83],v113 offset:53248\n"
"ds_read_b128 a[84:87],v113 offset:54272\n"
"v_mfma_f32_16x16x32_bf16 v[224:227],a[96:99],v[92:95],v[224:227]\n"
"ds_read_b128 a[88:91],v113 offset:55296\n"
"v_exp_f32 v18,v18\n"
"v_mfma_f32_16x16x32_bf16 a[140:143],a[144:147],v[92:95],a[140:143]\n"
"ds_read_b128 a[92:95],v113 offset:56320\n"
"buffer_load_dwordx4 v112,s[24:27],s64 offen offset:3072 sc1 lds\n"
"s_add_u32 s64,s64,16384\n"
"v_mfma_f32_16x16x32_bf16 v[228:231],a[100:103],v[92:95],v[228:231]\n"
"s_sub_u32 s82,s3,49152\n"
"v_exp_f32 v19,v19\n"
"v_mfma_f32_16x16x32_bf16 v[132:135],a[100:103],v[80:83],v[132:135]\n"
"buffer_load_dwordx4 a[16:19],v112,s[24:27],s82 offen offset:0 sc1\n"
"v_cvt_pk_bf16_f32 v71,v14,v15\n"
"v_cvt_pk_bf16_f32 v72,v16,v17\n"
"v_cvt_pk_bf16_f32 v73,v18,v19\n"
"v_exp_f32 v20,v20\n"
"v_mfma_f32_16x16x32_bf16 v[164:167],a[100:103],v[84:87],v[164:167]\n"
"buffer_load_dwordx4 a[20:23],v112,s[24:27],s82 offen offset:1024 sc1\n"
"v_mfma_f32_16x16x32_bf16 v[196:199],a[100:103],v[88:91],v[196:199]\n"
"v_exp_f32 v21,v21\n"
"v_max3_f32 v118,v118,v59,v60\n"
"v_mfma_f32_16x16x32_bf16 a[136:139],a[144:147],v[88:91],a[136:139]\n"
"v_cvt_pk_bf16_f32 v74,v20,v21\n"
"v_max3_f32 v118,v118,v61,v62\n"
"v_max3_f32 v118,v118,v63,v52\n"
"v_mfma_f32_16x16x32_bf16 v[200:203],a[104:107],v[88:91],v[200:203]\n"
"v_exp_f32 v22,v22\n"
"v_max3_f32 v118,v118,v53,v54\n"
"v_mfma_f32_16x16x32_bf16 v[232:235],a[104:107],v[92:95],v[232:235]\n"
"buffer_load_dwordx4 a[24:27],v112,s[24:27],s82 offen offset:2048 sc1\n"
"v_max3_f32 v118,v118,v55,v48\n"
"v_max3_f32 v118,v118,v49,v50\n"
"v_mfma_f32_16x16x32_bf16 v[136:139],a[104:107],v[80:83],v[136:139]\n"
"v_exp_f32 v23,v23\n"
"v_max_f32 v118,v118,v51\n"
"v_mfma_f32_16x16x32_bf16 v[168:171],a[104:107],v[84:87],v[168:171]\n"
"v_cvt_pk_bf16_f32 v75,v22,v23\n"
"v_cmp_lt_f32 vcc,s93,v118\n"
"v_mfma_f32_16x16x32_bf16 a[132:135],a[144:147],v[84:87],a[132:135]\n"
"buffer_load_dwordx4 a[28:31],v112,s[24:27],s82 offen offset:3072 sc1\n"
"v_mfma_f32_16x16x32_bf16 v[172:175],a[108:111],v[84:87],v[172:175]\n"
"v_exp_f32 v24,v24\n"
"v_mfma_f32_16x16x32_bf16 v[204:207],a[108:111],v[88:91],v[204:207]\n"
"s_sub_u32 s82,s3,65536\n"
"v_exp_f32 v25,v25\n"
"v_mfma_f32_16x16x32_bf16 v[236:239],a[108:111],v[92:95],v[236:239]\n"
"v_cvt_pk_bf16_f32 v76,v24,v25\n"
"v_mfma_f32_16x16x32_bf16 v[140:143],a[108:111],v[80:83],v[140:143]\n"
"buffer_load_dwordx4 v[32:35],v112,s[24:27],s82 offen offset:0 sc1\n"
"v_mfma_f32_16x16x32_bf16 a[128:131],a[144:147],v[80:83],a[128:131]\n"
"v_exp_f32 v26,v26\n"
"v_mfma_f32_16x16x32_bf16 v[144:147],a[112:115],v[80:83],v[144:147]\n"
"v_exp_f32 v27,v27\n"
"v_mfma_f32_16x16x32_bf16 v[176:179],a[112:115],v[84:87],v[176:179]\n"
"buffer_load_dwordx4 v[36:39],v112,s[24:27],s82 offen offset:1024 sc1\n"
"v_mfma_f32_16x16x32_bf16 v[208:211],a[112:115],v[88:91],v[208:211]\n"
"v_exp_f32 v28,v28\n"
"v_mfma_f32_16x16x32_bf16 v[240:243],a[112:115],v[92:95],v[240:243]\n"
"v_cvt_pk_bf16_f32 v77,v26,v27\n"
"v_mfma_f32_16x16x32_bf16 v[244:247],a[116:119],v[92:95],v[244:247]\n"
"v_exp_f32 v29,v29\n"
"v_mfma_f32_16x16x32_bf16 v[148:151],a[116:119],v[80:83],v[148:151]\n"
"buffer_load_dwordx4 v[40:43],v112,s[24:27],s82 offen offset:2048 sc1\n"
"v_mfma_f32_16x16x32_bf16 v[180:183],a[116:119],v[84:87],v[180:183]\n"
"v_exp_f32 v30,v30\n"
"v_mfma_f32_16x16x32_bf16 v[212:215],a[116:119],v[88:91],v[212:215]\n"
"v_cvt_pk_bf16_f32 v78,v28,v29\n"
"v_mfma_f32_16x16x32_bf16 v[216:219],a[120:123],v[88:91],v[216:219]\n"
"v_exp_f32 v31,v31\n"
"v_mfma_f32_16x16x32_bf16 v[248:251],a[120:123],v[92:95],v[248:251]\n"
"v_cvt_pk_bf16_f32 v79,v30,v31\n"
"v_mfma_f32_16x16x32_bf16 v[152:155],a[120:123],v[80:83],v[152:155]\n"
"buffer_load_dwordx4 v[44:47],v112,s[24:27],s82 offen offset:3072 sc1\n"
"v_mfma_f32_16x16x32_bf16 v[184:187],a[120:123],v[84:87],v[184:187]\n"
"v_mfma_f32_16x16x32_bf16 v[188:191],a[124:127],v[84:87],v[188:191]\n"
"v_mfma_f32_16x16x32_bf16 v[220:223],a[124:127],v[88:91],v[220:223]\n"
"v_mfma_f32_16x16x32_bf16 v[252:255],a[124:127],v[92:95],v[252:255]\n"
"v_mfma_f32_16x16x32_bf16 v[156:159],a[124:127],v[80:83],v[156:159]\n"
"s_cbranch_vccnz .Lfk_cs_a0\n"
".Lfk_cr_a0:\n"
"s_load_dwordx2 s[36:37],s[0:1],0\n"
"s_load_dwordx2 s[44:45],s[0:1],80\n"
"s_load_dwordx2 s[48:49],s[0:1],88\n"
"s_lshl_b32 s82,s78,2\n"
"s_add_u32 s82,s82,65536\n"
"v_add_u32 v113,s82,v112\n"
"s_lshr_b32 s82,s78,2\n"
"s_add_u32 s82,s82,122880\n"
"v_lshrrev_b32 v115,1,v112\n"
"v_add_u32 v116,s82,v115\n"
"s_waitcnt lgkmcnt(0)\n"
"s_barrier\n"
"v_add_f32 v24,0x80000000,v126\n"
"v_min_f32 v24,v24,v108\n"
"v_add_f32 v25,0xf149f2ca,v126\n"
"v_min_f32 v25,v25,v109\n"
"v_add_f32 v26,0xf1c9f2ca,v126\n"
"v_min_f32 v26,v26,v110\n"
"v_add_f32 v27,0xf2177617,v126\n"
"v_min_f32 v27,v27,v111\n"
"v_add_f32 v28,0xf349f2ca,v126\n"
"v_min_f32 v28,v28,v108\n"
"v_add_f32 v29,0xf35691f6,v126\n"
"v_min_f32 v29,v29,v109\n"
"v_add_f32 v30,0xf3633123,v126\n"
"v_min_f32 v30,v30,v110\n"
"v_add_f32 v31,0xf36fd050,v126\n"
"v_min_f32 v31,v31,v111\n"
"v_exp_f32 v48,v48\n"
"v_mfma_f32_16x16x32_bf16 v[24:27],a[64:67],a[48:51],v[24:27]\n"
"s_add_u32 s83,s18,2097152\n"
"ds_read_b128 a[96:99],v114 offset:40960\n"
"ds_read_b128 a[100:103],v114 offset:41984\n"
"v_exp_f32 v49,v49\n"
"v_mfma_f32_16x16x32_bf16 v[24:27],a[68:71],a[52:55],v[24:27]\n"
"s_add_u32 m0,s30,32768\n"
"ds_read_b128 a[104:107],v114 offset:43008\n"
"ds_read_b128 a[108:111],v114 offset:44032\n"
"v_mfma_f32_16x16x32_bf16 v[24:27],a[72:75],a[56:59],v[24:27]\n"
"ds_read_b128 a[112:115],v114 offset:45056\n"
"ds_read_b128 a[116:119],v114 offset:46080\n"
"ds_read_b128 a[120:123],v114 offset:47104\n"
"ds_read_b128 a[124:127],v114 offset:48128\n"
"v_exp_f32 v50,v50\n"
"v_mfma_f32_16x16x32_bf16 v[24:27],a[76:79],a[60:63],v[24:27]\n"
"buffer_load_dwordx4 v112,s[24:27],s83 offen offset:0 sc1 lds\n"
"v_cvt_pk_bf16_f32 v88,v48,v49\n"
"v_mfma_f32_16x16x32_bf16 v[28:31],a[92:95],a[60:63],v[28:31]\n"
"buffer_load_dwordx4 v112,s[24:27],s83 offen offset:1024 sc1 lds\n"
"v_accvgpr_read_b32 v97,a128\n"
"v_cvt_pk_bf16_f32 v128,v128,v129\n"
"v_cvt_pk_bf16_f32 v129,v130,v131\n"
"v_mfma_f32_16x16x32_bf16 v[28:31],a[88:91],a[56:59],v[28:31]\n"
"v_exp_f32 v51,v51\n"
"v_cvt_pk_bf16_f32 v130,v132,v133\n"
"v_cvt_pk_bf16_f32 v131,v134,v135\n"
"v_cvt_pk_bf16_f32 v89,v50,v51\n"
"v_mfma_f32_16x16x32_bf16 v[28:31],a[84:87],a[52:55],v[28:31]\n"
"ds_write_b128 v113,v[128:131] offset:0\n"
"v_cvt_pk_bf16_f32 v136,v136,v137\n"
"v_cvt_pk_bf16_f32 v137,v138,v139\n"
"v_exp_f32 v52,v52\n"
"v_mfma_f32_16x16x32_bf16 v[28:31],a[80:83],a[48:51],v[28:31]\n"
"buffer_load_dwordx4 v112,s[24:27],s83 offen offset:2048 sc1 lds\n"
"v_max3_f32 v118,v24,v25,v26\n"
"s_waitcnt lgkmcnt(1)\n"
"v_mfma_f32_16x16x32_bf16 v[160:163],a[96:99],v[68:71],v[160:163]\n"
"v_cvt_pk_bf16_f32 v138,v140,v141\n"
"v_cvt_pk_bf16_f32 v139,v142,v143\n"
"v_cvt_pk_bf16_f32 v144,v144,v145\n"
"v_mfma_f32_16x16x32_bf16 a[132:135],a[144:147],v[68:71],a[132:135]\n"
"ds_write_b128 v113,v[136:139] offset:1024\n"
"buffer_load_dwordx4 v112,s[24:27],s83 offen offset:3072 sc1 lds\n"
"v_exp_f32 v53,v53\n"
"v_cvt_pk_bf16_f32 v145,v146,v147\n"
"v_mfma_f32_16x16x32_bf16 v[164:167],a[100:103],v[68:71],v[164:167]\n"
"s_add_u32 m0,m0,4096\n"
"v_exp_f32 v54,v54\n"
"v_cvt_pk_bf16_f32 v90,v52,v53\n"
"v_cvt_pk_bf16_f32 v146,v148,v149\n"
"v_mfma_f32_16x16x32_bf16 v[168:171],a[104:107],v[68:71],v[168:171]\n"
"v_max3_f32 v118,v118,v27,v28\n"
"v_max3_f32 v118,v118,v29,v30\n"
"v_max_f32 v118,v118,v31\n"
"v_cmp_lt_f32 vcc,s93,v118\n"
"v_exp_f32 v55,v55\n"
"v_mfma_f32_16x16x32_bf16 v[172:175],a[108:111],v[68:71],v[172:175]\n"
"buffer_load_dwordx4 v117,s[24:27],s83 offen offset:0 sc1 lds\n"
"v_cvt_pk_bf16_f32 v147,v150,v151\n"
"v_cvt_pk_bf16_f32 v91,v54,v55\n"
"v_mfma_f32_16x16x32_bf16 v[176:179],a[112:115],v[68:71],v[176:179]\n"
"ds_write_b128 v113,v[144:147] offset:2048\n"
"v_cvt_pk_bf16_f32 v152,v152,v153\n"
"v_cvt_pk_bf16_f32 v153,v154,v155\n"
"v_exp_f32 v56,v56\n"
"v_mfma_f32_16x16x32_bf16 v[180:183],a[116:119],v[68:71],v[180:183]\n"
"buffer_load_dwordx4 v117,s[24:27],s83 offen offset:1024 sc1 lds\n"
"v_cvt_pk_bf16_f32 v154,v156,v157\n"
"v_cvt_pk_bf16_f32 v155,v158,v159\n"
"v_mfma_f32_16x16x32_bf16 v[184:187],a[120:123],v[68:71],v[184:187]\n"
"ds_write_b128 v113,v[152:155] offset:3072\n"
"ds_write_b64 v116,v[96:97] offset:0\n"
"v_mfma_f32_16x16x32_bf16 v[188:191],a[124:127],v[68:71],v[188:191]\n"
"v_exp_f32 v57,v57\n"
"v_mfma_f32_16x16x32_bf16 v[192:195],a[96:99],v[72:75],v[192:195]\n"
"buffer_load_dwordx4 v117,s[24:27],s83 offen offset:2048 sc1 lds\n"
"v_cvt_pk_bf16_f32 v92,v56,v57\n"
"v_mfma_f32_16x16x32_bf16 a[136:139],a[144:147],v[72:75],a[136:139]\n"
"v_exp_f32 v58,v58\n"
"v_exp_f32 v59,v59\n"
"v_mfma_f32_16x16x32_bf16 v[196:199],a[100:103],v[72:75],v[196:199]\n"
"buffer_load_dwordx4 v117,s[24:27],s83 offen offset:3072 sc1 lds\n"
"v_accvgpr_read_b32 v101,a132\n"
"v_mfma_f32_16x16x32_bf16 v[200:203],a[104:107],v[72:75],v[200:203]\n"
"s_sub_u32 s82,s3,81920\n"
"v_cvt_pk_bf16_f32 v93,v58,v59\n"
"v_cvt_pk_bf16_f32 v160,v160,v161\n"
"v_cvt_pk_bf16_f32 v161,v162,v163\n"
"v_mfma_f32_16x16x32_bf16 v[204:207],a[108:111],v[72:75],v[204:207]\n"
"v_exp_f32 v60,v60\n"
"v_cvt_pk_bf16_f32 v162,v164,v165\n"
"v_cvt_pk_bf16_f32 v163,v166,v167\n"
"v_mfma_f32_16x16x32_bf16 v[208:211],a[112:115],v[72:75],v[208:211]\n"
"buffer_load_dwordx4 a[32:35],v112,s[24:27],s82 offen offset:0 sc1\n"
"ds_write_b128 v113,v[160:163] offset:4096\n"
"v_cvt_pk_bf16_f32 v168,v168,v169\n"
"v_cvt_pk_bf16_f32 v169,v170,v171\n"
"v_mfma_f32_16x16x32_bf16 v[212:215],a[116:119],v[72:75],v[212:215]\n"
"v_exp_f32 v61,v61\n"
"v_cvt_pk_bf16_f32 v170,v172,v173\n"
"v_cvt_pk_bf16_f32 v171,v174,v175\n"
"v_cvt_pk_bf16_f32 v94,v60,v61\n"
"v_mfma_f32_16x16x32_bf16 v[216:219],a[120:123],v[72:75],v[216:219]\n"
"ds_write_b128 v113,v[168:171] offset:5120\n"
"v_cvt_pk_bf16_f32 v176,v176,v177\n"
"v_cvt_pk_bf16_f32 v177,v178,v179\n"
"v_exp_f32 v62,v62\n"
"v_mfma_f32_16x16x32_bf16 v[220:223],a[124:127],v[72:75],v[220:223]\n"
"buffer_load_dwordx4 a[36:39],v112,s[24:27],s82 offen offset:1024 sc1\n"
"v_cvt_pk_bf16_f32 v178,v180,v181\n"
"v_cvt_pk_bf16_f32 v179,v182,v183\n"
"v_mfma_f32_16x16x32_bf16 v[224:227],a[96:99],v[76:79],v[224:227]\n"
"ds_write_b128 v113,v[176:179] offset:6144\n"
"v_cvt_pk_bf16_f32 v184,v184,v185\n"
"v_cvt_pk_bf16_f32 v185,v186,v187\n"
"v_mfma_f32_16x16x32_bf16 a[140:143],a[144:147],v[76:79],a[140:143]\n"
"v_exp_f32 v63,v63\n"
"v_cvt_pk_bf16_f32 v186,v188,v189\n"
"v_cvt_pk_bf16_f32 v187,v190,v191\n"
"v_mfma_f32_16x16x32_bf16 v[228:231],a[100:103],v[76:79],v[228:231]\n"
"buffer_load_dwordx4 a[40:43],v112,s[24:27],s82 offen offset:2048 sc1\n"
"ds_write_b128 v113,v[184:187] offset:7168\n"
"v_mfma_f32_16x16x32_bf16 v[232:235],a[104:107],v[76:79],v[232:235]\n"
"ds_write_b64 v116,v[100:101] offset:512\n"
"v_cvt_pk_bf16_f32 v95,v62,v63\n"
"v_mfma_f32_16x16x32_bf16 v[236:239],a[108:111],v[76:79],v[236:239]\n"
"buffer_load_dwordx4 a[44:47],v112,s[24:27],s82 offen offset:3072 sc1\n"
"v_mfma_f32_16x16x32_bf16 v[240:243],a[112:115],v[76:79],v[240:243]\n"
"s_sub_u32 s82,s3,131072\n"
"buffer_load_dwordx4 v[128:131],v112,s[24:27],s82 offen offset:0 sc1\n"
"buffer_load_dwordx4 v[132:135],v112,s[24:27],s82 offen offset:1024 sc1\n"
"buffer_load_dwordx4 v[136:139],v112,s[24:27],s82 offen offset:2048 sc1\n"
"buffer_load_dwordx4 v[140:143],v112,s[24:27],s82 offen offset:3072 sc1\n"
"v_mfma_f32_16x16x32_bf16 v[244:247],a[116:119],v[76:79],v[244:247]\n"
"s_sub_u32 s82,s3,147456\n"
"buffer_load_dwordx4 v[144:147],v112,s[24:27],s82 offen offset:0 sc1\n"
"buffer_load_dwordx4 v[148:151],v112,s[24:27],s82 offen offset:1024 sc1\n"
"v_mfma_f32_16x16x32_bf16 v[248:251],a[120:123],v[76:79],v[248:251]\n"
"buffer_load_dwordx4 v[152:155],v112,s[24:27],s82 offen offset:2048 sc1\n"
"buffer_load_dwordx4 v[156:159],v112,s[24:27],s82 offen offset:3072 sc1\n"
"v_mfma_f32_16x16x32_bf16 v[252:255],a[124:127],v[76:79],v[252:255]\n"
"s_cbranch_vccnz .Lfk_cs_a1\n"
".Lfk_cr_a1:\n"
"s_waitcnt vmcnt(28) lgkmcnt(0)\n"
"s_barrier\n"
"ds_read_b128 a[96:99],v114 offset:8192\n"
"ds_read_b128 a[100:103],v114 offset:9216\n"
"ds_read_b128 a[104:107],v114 offset:10240\n"
"ds_read_b128 a[108:111],v114 offset:11264\n"
"ds_read_b128 a[112:115],v114 offset:12288\n"
"ds_read_b128 a[116:119],v114 offset:13312\n"
"ds_read_b128 a[120:123],v114 offset:14336\n"
"ds_read_b128 a[124:127],v114 offset:15360\n"
"v_xor_b32 v113,32768,v113\n"
"v_xor_b32 v116,2048,v116\n"
"ds_read_b64 v[98:99],v116 offset:0\n"
"ds_read_b128 a[64:67],v113 offset:0\n"
"ds_read_b128 a[68:71],v113 offset:1024\n"
"ds_read_b128 a[72:75],v113 offset:2048\n"
"ds_read_b128 a[76:79],v113 offset:3072\n"
"ds_read_b64 v[102:103],v116 offset:512\n"
"ds_read_b128 a[80:83],v113 offset:4096\n"
"ds_read_b128 a[84:87],v113 offset:5120\n"
"ds_read_b128 a[88:91],v113 offset:6144\n"
"ds_read_b128 a[92:95],v113 offset:7168\n"
"v_exp_f32 v24,v24\n"
"v_exp_f32 v25,v25\n"
"v_exp_f32 v26,v26\n"
"v_exp_f32 v27,v27\n"
"v_exp_f32 v28,v28\n"
"v_exp_f32 v29,v29\n"
"v_exp_f32 v30,v30\n"
"v_exp_f32 v31,v31\n"
"v_cvt_pk_bf16_f32 v76,v24,v25\n"
"v_cvt_pk_bf16_f32 v77,v26,v27\n"
"v_cvt_pk_bf16_f32 v78,v28,v29\n"
"v_cvt_pk_bf16_f32 v79,v30,v31\n"
"s_waitcnt lgkmcnt(10)\n"
"v_mfma_f32_16x16x32_bf16 a[136:139],a[144:147],v[88:91],a[136:139]\n"
"s_sub_u32 s82,s3,163840\n"
"v_lshrrev_b32 v70,4,v112\n"
"v_and_b32 v71,15,v70\n"
"v_mfma_f32_16x16x32_bf16 a[140:143],a[144:147],v[92:95],a[140:143]\n"
"buffer_load_dwordx4 v[0:3],v112,s[24:27],s82 offen offset:0 sc1\n"
"v_lshrrev_b32 v70,4,v70\n"
"v_lshlrev_b32 v70,2,v70\n"
"v_mfma_f32_16x16x32_bf16 v[192:195],a[96:99],v[88:91],v[192:195]\n"
"buffer_load_dwordx4 v[4:7],v112,s[24:27],s82 offen offset:1024 sc1\n"
"v_sub_u32 v70,v71,v70\n"
"v_mov_b32 v64,0\n"
"v_mfma_f32_16x16x32_bf16 v[224:227],a[96:99],v[92:95],v[224:227]\n"
"ds_read_b128 a[96:99],v114 offset:24576\n"
"buffer_load_dwordx4 v[8:11],v112,s[24:27],s82 offen offset:2048 sc1\n"
"v_mfma_f32_16x16x32_bf16 v[196:199],a[100:103],v[88:91],v[196:199]\n"
"buffer_load_dwordx4 v[12:15],v112,s[24:27],s82 offen offset:3072 sc1\n"
"v_mov_b32 v69,0\n"
"v_mfma_f32_16x16x32_bf16 v[228:231],a[100:103],v[92:95],v[228:231]\n"
"s_sub_u32 s82,s3,180224\n"
"ds_read_b128 a[100:103],v114 offset:25600\n"
"v_mov_b32 v71,0x3f80\n"
"v_mfma_f32_16x16x32_bf16 v[200:203],a[104:107],v[88:91],v[200:203]\n"
"buffer_load_dwordx4 v[160:163],v112,s[24:27],s82 offen offset:0 sc1\n"
"v_cmp_eq_u32 vcc,0,v70\n"
"v_mov_b32 v68,0\n"
"v_mov_b32 v65,0\n"
"v_cndmask_b32 v66,0,v71,vcc\n"
"v_cmp_eq_u32 vcc,1,v70\n"
"v_mfma_f32_16x16x32_bf16 v[232:235],a[104:107],v[92:95],v[232:235]\n"
"ds_read_b128 a[104:107],v114 offset:26624\n"
"buffer_load_dwordx4 v[164:167],v112,s[24:27],s82 offen offset:1024 sc1\n"
"v_mov_b32 v72,0x3f800000\n"
"v_cndmask_b32 v66,v66,v72,vcc\n"
"v_cmp_eq_u32 vcc,2,v70\n"
"v_mfma_f32_16x16x32_bf16 v[204:207],a[108:111],v[88:91],v[204:207]\n"
"buffer_load_dwordx4 v[168:171],v112,s[24:27],s82 offen offset:2048 sc1\n"
"v_cndmask_b32 v67,0,v71,vcc\n"
"v_cmp_eq_u32 vcc,3,v70\n"
"v_mfma_f32_16x16x32_bf16 v[236:239],a[108:111],v[92:95],v[236:239]\n"
"ds_read_b128 a[108:111],v114 offset:27648\n"
"buffer_load_dwordx4 v[172:175],v112,s[24:27],s82 offen offset:3072 sc1\n"
"v_mfma_f32_16x16x32_bf16 v[208:211],a[112:115],v[88:91],v[208:211]\n"
"v_cndmask_b32 v67,v67,v72,vcc\n"
"s_sub_u32 s82,s3,196608\n"
"v_mfma_f32_16x16x32_bf16 v[240:243],a[112:115],v[92:95],v[240:243]\n"
"ds_read_b128 a[112:115],v114 offset:28672\n"
"buffer_load_dwordx4 v[176:179],v112,s[24:27],s82 offen offset:0 sc1\n"
"v_mfma_f32_16x16x32_bf16 v[212:215],a[116:119],v[88:91],v[212:215]\n"
"buffer_load_dwordx4 v[180:183],v112,s[24:27],s82 offen offset:1024 sc1\n"
"v_mfma_f32_16x16x32_bf16 v[244:247],a[116:119],v[92:95],v[244:247]\n"
"ds_read_b128 a[116:119],v114 offset:29696\n"
"buffer_load_dwordx4 v[184:187],v112,s[24:27],s82 offen offset:2048 sc1\n"
"v_mfma_f32_16x16x32_bf16 v[216:219],a[120:123],v[88:91],v[216:219]\n"
"buffer_load_dwordx4 v[188:191],v112,s[24:27],s82 offen offset:3072 sc1\n"
"v_mfma_f32_16x16x32_bf16 v[248:251],a[120:123],v[92:95],v[248:251]\n"
"ds_read_b128 a[120:123],v114 offset:30720\n"
"v_mfma_f32_16x16x32_bf16 v[220:223],a[124:127],v[88:91],v[220:223]\n"
"v_mfma_f32_16x16x32_bf16 v[252:255],a[124:127],v[92:95],v[252:255]\n"
"ds_read_b128 a[124:127],v114 offset:31744\n"
"s_waitcnt lgkmcnt(13)\n"
"v_cmp_neq_f32 vcc,v98,v104\n"
"s_nop 0\n"
"s_cbranch_vccnz .Lfk_ms_a0\n"
".Lfk_mr_a0:\n"
"v_mfma_f32_16x16x32_bf16 v[192:195],v[66:69],a[64:67],v[192:195]\n"
"v_mfma_f32_16x16x32_bf16 v[196:199],v[64:67],a[64:67],v[196:199]\n"
"v_mfma_f32_16x16x32_bf16 v[200:203],v[66:69],a[68:71],v[200:203]\n"
"v_mfma_f32_16x16x32_bf16 v[204:207],v[64:67],a[68:71],v[204:207]\n"
"v_mfma_f32_16x16x32_bf16 v[208:211],v[66:69],a[72:75],v[208:211]\n"
"v_mfma_f32_16x16x32_bf16 v[212:215],v[64:67],a[72:75],v[212:215]\n"
"v_mfma_f32_16x16x32_bf16 v[216:219],v[66:69],a[76:79],v[216:219]\n"
"v_mfma_f32_16x16x32_bf16 v[220:223],v[64:67],a[76:79],v[220:223]\n"
"s_waitcnt lgkmcnt(0)\n"
"v_mfma_f32_16x16x32_bf16 a[140:143],a[144:147],v[76:79],a[140:143]\n"
"v_mfma_f32_16x16x32_bf16 v[224:227],a[96:99],v[76:79],v[224:227]\n"
"v_mfma_f32_16x16x32_bf16 v[228:231],a[100:103],v[76:79],v[228:231]\n"
"v_accvgpr_read_b32 v122,a136\n"
"v_add_f32 v122,v122,v99\n"
"v_mfma_f32_16x16x32_bf16 v[232:235],a[104:107],v[76:79],v[232:235]\n"
"v_rcp_f32 v122,v122\n"
"v_mfma_f32_16x16x32_bf16 v[236:239],a[108:111],v[76:79],v[236:239]\n"
"v_pk_mul_f32 v[192:193],v[192:193],v[122:123] op_sel_hi:[1,0]\n"
"v_pk_mul_f32 v[194:195],v[194:195],v[122:123] op_sel_hi:[1,0]\n"
"v_pk_mul_f32 v[196:197],v[196:197],v[122:123] op_sel_hi:[1,0]\n"
"v_pk_mul_f32 v[198:199],v[198:199],v[122:123] op_sel_hi:[1,0]\n"
"v_mfma_f32_16x16x32_bf16 v[240:243],a[112:115],v[76:79],v[240:243]\n"
"v_cvt_pk_bf16_f32 v192,v192,v193\n"
"v_cvt_pk_bf16_f32 v193,v194,v195\n"
"v_mfma_f32_16x16x32_bf16 v[244:247],a[116:119],v[76:79],v[244:247]\n"
"v_cvt_pk_bf16_f32 v194,v196,v197\n"
"v_cvt_pk_bf16_f32 v195,v198,v199\n"
"v_mfma_f32_16x16x32_bf16 v[248:251],a[120:123],v[76:79],v[248:251]\n"
"buffer_store_dwordx4 v[192:195],v112,s[24:27],s70 offen offset:0 sc1\n"
"v_pk_mul_f32 v[200:201],v[200:201],v[122:123] op_sel_hi:[1,0]\n"
"v_pk_mul_f32 v[202:203],v[202:203],v[122:123] op_sel_hi:[1,0]\n"
"v_pk_mul_f32 v[204:205],v[204:205],v[122:123] op_sel_hi:[1,0]\n"
"v_pk_mul_f32 v[206:207],v[206:207],v[122:123] op_sel_hi:[1,0]\n"
"v_mfma_f32_16x16x32_bf16 v[252:255],a[124:127],v[76:79],v[252:255]\n"
"v_cvt_pk_bf16_f32 v200,v200,v201\n"
"v_cvt_pk_bf16_f32 v201,v202,v203\n"
"v_cmp_neq_f32 vcc,v102,v108\n"
"s_nop 0\n"
"s_cbranch_vccnz .Lfk_ms_a1\n"
".Lfk_mr_a1:\n"
"v_mfma_f32_16x16x32_bf16 v[224:227],v[66:69],a[80:83],v[224:227]\n"
"v_cvt_pk_bf16_f32 v202,v204,v205\n"
"v_cvt_pk_bf16_f32 v203,v206,v207\n"
"v_mfma_f32_16x16x32_bf16 v[228:231],v[64:67],a[80:83],v[228:231]\n"
"buffer_store_dwordx4 v[200:203],v112,s[24:27],s70 offen offset:1024 sc1\n"
"v_pk_mul_f32 v[208:209],v[208:209],v[122:123] op_sel_hi:[1,0]\n"
"v_pk_mul_f32 v[210:211],v[210:211],v[122:123] op_sel_hi:[1,0]\n"
"v_pk_mul_f32 v[212:213],v[212:213],v[122:123] op_sel_hi:[1,0]\n"
"v_pk_mul_f32 v[214:215],v[214:215],v[122:123] op_sel_hi:[1,0]\n"
"v_mfma_f32_16x16x32_bf16 v[232:235],v[66:69],a[84:87],v[232:235]\n"
"v_cvt_pk_bf16_f32 v208,v208,v209\n"
"v_cvt_pk_bf16_f32 v209,v210,v211\n"
"v_mfma_f32_16x16x32_bf16 v[236:239],v[64:67],a[84:87],v[236:239]\n"
"v_cvt_pk_bf16_f32 v210,v212,v213\n"
"v_cvt_pk_bf16_f32 v211,v214,v215\n"
"v_mfma_f32_16x16x32_bf16 v[240:243],v[66:69],a[88:91],v[240:243]\n"
"buffer_store_dwordx4 v[208:211],v112,s[24:27],s70 offen offset:2048 sc1\n"
"v_pk_mul_f32 v[216:217],v[216:217],v[122:123] op_sel_hi:[1,0]\n"
"v_pk_mul_f32 v[218:219],v[218:219],v[122:123] op_sel_hi:[1,0]\n"
"v_pk_mul_f32 v[220:221],v[220:221],v[122:123] op_sel_hi:[1,0]\n"
"v_pk_mul_f32 v[222:223],v[222:223],v[122:123] op_sel_hi:[1,0]\n"
"v_mfma_f32_16x16x32_bf16 v[244:247],v[64:67],a[88:91],v[244:247]\n"
"v_cvt_pk_bf16_f32 v216,v216,v217\n"
"v_cvt_pk_bf16_f32 v217,v218,v219\n"
"v_mfma_f32_16x16x32_bf16 v[248:251],v[66:69],a[92:95],v[248:251]\n"
"v_cvt_pk_bf16_f32 v218,v220,v221\n"
"v_cvt_pk_bf16_f32 v219,v222,v223\n"
"v_mfma_f32_16x16x32_bf16 v[252:255],v[64:67],a[92:95],v[252:255]\n"
"buffer_store_dwordx4 v[216:219],v112,s[24:27],s70 offen offset:3072 sc1\n"
"s_add_u32 s70,s70,262144\n"
"s_branch .Lfk_p1\n"
".Lfk_look:\n"
"s_lshr_b64 s[28:29],s[14:15],s21\n"
"s_and_b32 s83,s28,s19\n"
"s_cbranch_scc0 .Lfk_lk1\n"
"s_mov_b32 s84,0x40000\n"
"s_waitcnt vmcnt(10)\n"
".Lfk_lk0:\n"
"v_xor_b32 v119,s80,v122\n"
"v_xor_b32 v120,s81,v123\n"
"v_or_b32 v119,v119,v120\n"
"v_cmp_ne_u32 s[14:15],0,v119\n"
"s_nop 0\n"
"s_lshr_b64 s[28:29],s[14:15],s21\n"
"s_and_b32 s83,s28,s19\n"
"s_cbranch_scc0 .Lfk_lk1\n"
"s_sleep 2\n"
"buffer_load_dwordx2 v[122:123],v115,s[24:27],s11 offen sc1\n"
"s_waitcnt vmcnt(0)\n"
"s_sub_u32 s84,s84,1\n"
"s_cbranch_scc0 .Lfk_lk0\n"
".Lfk_lk1:\n"
"s_andn2_b32 s28,s28,s19\n"
"s_lshl_b64 s[14:15],s[28:29],s21\n"
"s_lshl_b32 s83,s19,8\n"
"s_and_b32 s83,s28,s83\n"
"s_cbranch_scc0 .Lfk_lk2\n"
"buffer_load_dwordx2 v[122:123],v115,s[24:27],s11 offen sc1\n"
".Lfk_lk2:\n"
"s_nop 1\n"
"s_setpc_b64 s[94:95]\n"
".Lfk_meet4:\n"
"s_mov_b32 s84,0x1000000\n"
".Lfk_meetl4:\n"
"ds_read_b32 v125,v114\n"
"s_sub_u32 s84,s84,1\n"
"s_cbranch_scc1 .Lfk_meetx4\n"
"s_waitcnt lgkmcnt(0)\n"
"v_cmp_gt_u32 vcc,s16,v125\n"
"s_nop 0\n"
"s_cbranch_vccnz .Lfk_meetl4\n"
".Lfk_meetx4:\n"
"s_setpc_b64 s[94:95]\n"
".Lfk_es_lkone:\n"
"s_call_b64 s[94:95],.Lfk_look\n"
"s_branch .Lfk_er_lkone\n"
".Lfk_z0:\n"
"s_call_b64 s[94:95],.Lfk_meet4\n"
"v_xor_b32 v119,s80,v104\n"
"v_xor_b32 v120,s81,v105\n"
"v_or_b32 v119,v119,v120\n"
"v_xor_b32 v120,s80,v106\n"
"v_or_b32 v119,v119,v120\n"
"v_xor_b32 v120,s81,v107\n"
"v_or_b32 v119,v119,v120\n"
"v_xor_b32 v120,s80,v108\n"
"v_or_b32 v119,v119,v120\n"
"v_xor_b32 v120,s81,v109\n"
"v_or_b32 v119,v119,v120\n"
"v_xor_b32 v120,s80,v110\n"
"v_or_b32 v119,v119,v120\n"
"v_xor_b32 v120,s81,v111\n"
"v_or_b32 v119,v119,v120\n"
"v_cmp_ne_u32 vcc,0,v119\n"
"s_nop 0\n"
"s_cbranch_vccz .Lfk_z1\n"
"s_mov_b32 s84,0x40000\n"
"s_mov_b32 s83,148946944\n"
"buffer_load_dwordx4 v[104:107],v112,s[24:27],s83 offen offset:0 sc1\n"
"buffer_load_dwordx4 v[108:111],v112,s[24:27],s83 offen offset:1024 sc1\n"
"s_waitcnt vmcnt(0)\n"
".Lfk_w0:\n"
"v_xor_b32 v119,s80,v104\n"
"v_xor_b32 v120,s81,v105\n"
"v_or_b32 v119,v119,v120\n"
"v_xor_b32 v120,s80,v106\n"
"v_or_b32 v119,v119,v120\n"
"v_xor_b32 v120,s81,v107\n"
"v_or_b32 v119,v119,v120\n"
"v_xor_b32 v120,s80,v108\n"
"v_or_b32 v119,v119,v120\n"
"v_xor_b32 v120,s81,v109\n"
"v_or_b32 v119,v119,v120\n"
"v_xor_b32 v120,s80,v110\n"
"v_or_b32 v119,v119,v120\n"
"v_xor_b32 v120,s81,v111\n"
"v_or_b32 v119,v119,v120\n"
"v_cmp_ne_u32 vcc,0,v119\n"
"s_nop 0\n"
"s_cbranch_vccz .Lfk_w0_x\n"
"s_sleep 8\n"
"s_mov_b64 exec,vcc\n"
"s_mov_b32 s83,148946944\n"
"buffer_load_dwordx4 v[104:107],v112,s[24:27],s83 offen offset:0 sc1\n"
"buffer_load_dwordx4 v[108:111],v112,s[24:27],s83 offen offset:1024 sc1\n"
"s_mov_b64 exec,-1\n"
"s_sub_u32 s84,s84,1\n"
"s_waitcnt vmcnt(0)\n"
"s_cbranch_scc0 .Lfk_w0\n"
".Lfk_w0_x:\n"
"s_nop 1\n"
"s_branch .Lfk_z1\n"
".Lfk_es_lkonem:\n"
"s_call_b64 s[94:95],.Lfk_look\n"
"s_branch .Lfk_er_lkonem\n"
".Lfk_es_mone:\n"
"s_call_b64 s[94:95],.Lfk_meet4\n"
"s_branch .Lfk_er_mone\n"
".Lfk_es_lktwom:\n"
"s_call_b64 s[94:95],.Lfk_look\n"
"s_branch .Lfk_er_lktwom\n"
".Lfk_es_mtwo:\n"
"s_call_b64 s[94:95],.Lfk_meet4\n"
"s_branch .Lfk_er_mtwo\n"
".Lfk_es_lkloopm:\n"
"s_call_b64 s[94:95],.Lfk_look\n"
"s_branch .Lfk_er_lkloopm\n"
".Lfk_es_me3:\n"
"s_call_b64 s[94:95],.Lfk_meet4\n"
"s_branch .Lfk_er_me3\n"
".Lfk_es_me2:\n"
"s_call_b64 s[94:95],.Lfk_meet4\n"
"s_branch .Lfk_er_me2\n"
".Lfk_p1:\n"
"s_cmp_eq_u32 s18,2146435072\n"
"s_cbranch_scc1 .Lfk_p2\n"
".Lfk_p3:\n"
"s_lshr_b32 s22,s8,2\n"
"s_and_b32 s31,s8,0xfff1ffff\n"
"s_add_u32 s18,s18,16384\n"
"s_andn2_b32 s91,s55,16384\n"
"v_add_u32 v116,s91,v112\n"
"v_accvgpr_read_b32 v122,a140\n"
"v_add_f32 v122,v122,v103\n"
"v_rcp_f32 v122,v122\n"
"s_nop 0\n"
"v_pk_mul_f32 v[224:225],v[224:225],v[122:123] op_sel_hi:[1,0]\n"
"v_pk_mul_f32 v[226:227],v[226:227],v[122:123] op_sel_hi:[1,0]\n"
"v_pk_mul_f32 v[228:229],v[228:229],v[122:123] op_sel_hi:[1,0]\n"
"v_pk_mul_f32 v[230:231],v[230:231],v[122:123] op_sel_hi:[1,0]\n"
"v_pk_mul_f32 v[232:233],v[232:233],v[122:123] op_sel_hi:[1,0]\n"
"v_pk_mul_f32 v[234:235],v[234:235],v[122:123] op_sel_hi:[1,0]\n"
"v_pk_mul_f32 v[236:237],v[236:237],v[122:123] op_sel_hi:[1,0]\n"
"v_pk_mul_f32 v[238:239],v[238:239],v[122:123] op_sel_hi:[1,0]\n"
"v_pk_mul_f32 v[240:241],v[240:241],v[122:123] op_sel_hi:[1,0]\n"
"v_pk_mul_f32 v[242:243],v[242:243],v[122:123] op_sel_hi:[1,0]\n"
"v_pk_mul_f32 v[244:245],v[244:245],v[122:123] op_sel_hi:[1,0]\n"
"v_pk_mul_f32 v[246:247],v[246:247],v[122:123] op_sel_hi:[1,0]\n"
"v_pk_mul_f32 v[248:249],v[248:249],v[122:123] op_sel_hi:[1,0]\n"
"v_pk_mul_f32 v[250:251],v[250:251],v[122:123] op_sel_hi:[1,0]\n"
"v_pk_mul_f32 v[252:253],v[252:253],v[122:123] op_sel_hi:[1,0]\n"
"v_pk_mul_f32 v[254:255],v[254:255],v[122:123] op_sel_hi:[1,0]\n"
"s_waitcnt vmcnt(24) lgkmcnt(0)\n"
"s_barrier\n"
"ds_read_b128 v[192:195],v112 offset:0\n"
"ds_read_b128 v[196:199],v112 offset:1024\n"
"ds_read_b128 v[200:203],v112 offset:2048\n"
"ds_read_b128 v[204:207],v112 offset:3072\n"
"ds_read_b128 v[208:211],v112 offset:4096\n"
"ds_read_b128 v[212:215],v112 offset:5120\n"
"ds_read_b128 v[216:219],v112 offset:6144\n"
"ds_read_b128 v[220:223],v112 offset:7168\n"
"v_cvt_pk_bf16_f32 v224,v224,v225\n"
"v_cvt_pk_bf16_f32 v225,v226,v227\n"
"v_cvt_pk_bf16_f32 v226,v228,v229\n"
"v_cvt_pk_bf16_f32 v227,v230,v231\n"
"v_cvt_pk_bf16_f32 v232,v232,v233\n"
"v_cvt_pk_bf16_f32 v233,v234,v235\n"
"v_cvt_pk_bf16_f32 v234,v236,v237\n"
"v_cvt_pk_bf16_f32 v235,v238,v239\n"
"v_cvt_pk_bf16_f32 v240,v240,v241\n"
"v_cvt_pk_bf16_f32 v241,v242,v243\n"
"v_cvt_pk_bf16_f32 v242,v244,v245\n"
"v_cvt_pk_bf16_f32 v243,v246,v247\n"
"v_cvt_pk_bf16_f32 v248,v248,v249\n"
"v_cvt_pk_bf16_f32 v249,v250,v251\n"
"v_cvt_pk_bf16_f32 v250,v252,v253\n"
"v_cvt_pk_bf16_f32 v251,v254,v255\n"
"buffer_store_dwordx4 v[224:227],v112,s[24:27],s70 offen offset:0 sc1\n"
"buffer_store_dwordx4 v[232:235],v112,s[24:27],s70 offen offset:1024 sc1\n"
"buffer_store_dwordx4 v[240:243],v112,s[24:27],s70 offen offset:2048 sc1\n"
"buffer_store_dwordx4 v[248:251],v112,s[24:27],s70 offen offset:3072 sc1\n"
"s_waitcnt lgkmcnt(4)\n"
"v_mfma_f32_16x16x32_bf16 v[80:83],a[224:227],v[192:195],0\n"
"v_mfma_f32_16x16x32_bf16 v[80:83],a[232:235],v[196:199],v[80:83]\n"
"ds_read_b128 v[224:227],v112 offset:8192\n"
"ds_read_b128 v[228:231],v112 offset:9216\n"
"v_mfma_f32_16x16x32_bf16 v[80:83],a[240:243],v[200:203],v[80:83]\n"
"ds_read_b128 v[232:235],v112 offset:10240\n"
"ds_read_b128 v[236:239],v112 offset:11264\n"
"v_mfma_f32_16x16x32_bf16 v[80:83],a[248:251],v[204:207],v[80:83]\n"
"ds_read_b128 v[240:243],v112 offset:12288\n"
"ds_read_b128 v[244:247],v112 offset:13312\n"
"v_mfma_f32_16x16x32_bf16 v[84:87],a[252:255],v[204:207],0\n"
"ds_read_b128 v[248:251],v112 offset:14336\n"
"ds_read_b128 v[252:255],v112 offset:15360\n"
"v_mfma_f32_16x16x32_bf16 v[84:87],a[244:247],v[200:203],v[84:87]\n"
"ds_read_b128 a[68:71],v116 offset:4096\n"
"ds_read_b128 a[72:75],v116 offset:8192\n"
"v_mfma_f32_16x16x32_bf16 v[84:87],a[236:239],v[196:199],v[84:87]\n"
"ds_read_b128 a[76:79],v116 offset:12288\n"
"ds_read_b128 a[80:83],v116 offset:0\n"
"v_mfma_f32_16x16x32_bf16 v[84:87],a[228:231],v[192:195],v[84:87]\n"
"ds_read_b128 a[84:87],v116 offset:20480\n"
"ds_read_b128 a[88:91],v116 offset:24576\n"
"s_waitcnt lgkmcnt(14)\n"
"v_mfma_f32_16x16x32_bf16 v[84:87],a[4:7],v[208:211],v[84:87]\n"
"ds_read_b128 a[92:95],v116 offset:28672\n"
"v_mfma_f32_16x16x32_bf16 v[84:87],a[12:15],v[212:215],v[84:87]\n"
"ds_read_b128 v[204:207],v112 offset:19456\n"
"s_lshl_b32 s91,s78,3\n"
"v_mfma_f32_16x16x32_bf16 v[84:87],a[20:23],v[216:219],v[84:87]\n"
"ds_read_b128 v[200:203],v112 offset:18432\n"
"s_add_u32 s91,s91,131072\n"
"v_mfma_f32_16x16x32_bf16 v[84:87],a[28:31],v[220:223],v[84:87]\n"
"ds_read_b128 v[196:199],v112 offset:17408\n"
"v_lshrrev_b32 v101,2,v112\n"
"v_mfma_f32_16x16x32_bf16 v[80:83],a[24:27],v[220:223],v[80:83]\n"
"ds_read_b128 v[192:195],v112 offset:16384\n"
"v_add_u32 v101,s91,v101\n"
"v_mfma_f32_16x16x32_bf16 v[80:83],a[16:19],v[216:219],v[80:83]\n"
"ds_read_b128 v[220:223],v112 offset:23552\n"
"v_lshrrev_b32 v114,2,v112\n"
"v_mfma_f32_16x16x32_bf16 v[80:83],a[8:11],v[212:215],v[80:83]\n"
"ds_read_b128 v[216:219],v112 offset:22528\n"
"v_add_u32 v114,131328,v114\n"
"v_mfma_f32_16x16x32_bf16 v[80:83],a[0:3],v[208:211],v[80:83]\n"
"ds_read_b128 v[212:215],v112 offset:21504\n"
"s_waitcnt lgkmcnt(15)\n"
"v_mfma_f32_16x16x32_bf16 v[80:83],v[32:35],v[224:227],v[80:83]\n"
"ds_read_b128 v[208:211],v112 offset:20480\n"
"s_mov_b32 s83,148946944\n"
"v_mfma_f32_16x16x32_bf16 v[80:83],v[40:43],v[228:231],v[80:83]\n"
"buffer_load_dwordx4 v[104:107],v112,s[24:27],s83 offen offset:0 sc1\n"
"v_mov_b32 v116,0\n"
"v_mfma_f32_16x16x32_bf16 v[80:83],a[32:35],v[232:235],v[80:83]\n"
"ds_write_b32 v101,v116 offset:256\n"
"v_mov_b32 v116,1\n"
"v_mfma_f32_16x16x32_bf16 v[80:83],a[40:43],v[236:239],v[80:83]\n"
"buffer_load_dwordx4 v[108:111],v112,s[24:27],s83 offen offset:1024 sc1\n"
"v_mfma_f32_16x16x32_bf16 v[84:87],a[44:47],v[236:239],v[84:87]\n"
"s_mov_b32 s16,4\n"
"ds_read_b128 v[236:239],v112 offset:27648\n"
"v_add_u32 v113,65536,v112\n"
"v_mfma_f32_16x16x32_bf16 v[84:87],a[36:39],v[232:235],v[84:87]\n"
"ds_read_b128 v[232:235],v112 offset:26624\n"
"s_add_u32 s31,s31,8388608\n"
"v_mfma_f32_16x16x32_bf16 v[84:87],v[44:47],v[228:231],v[84:87]\n"
"ds_read_b128 v[228:231],v112 offset:25600\n"
"s_add_u32 s53,s22,786432\n"
"v_mfma_f32_16x16x32_bf16 v[84:87],v[36:39],v[224:227],v[84:87]\n"
"s_add_u32 m0,s30,65536\n"
"ds_read_b128 v[224:227],v112 offset:24576\n"
"s_waitcnt lgkmcnt(13)\n"
"v_mfma_f32_16x16x32_bf16 v[84:87],a[68:71],v[240:243],v[84:87]\n"
"buffer_load_dwordx4 v112,s[24:27],s18 offen offset:0 sc1 lds\n"
"v_mfma_f32_16x16x32_bf16 v[84:87],a[76:79],v[244:247],v[84:87]\n"
"buffer_load_dwordx4 v112,s[24:27],s18 offen offset:1024 sc1 lds\n"
"v_mfma_f32_16x16x32_bf16 v[84:87],a[84:87],v[248:251],v[84:87]\n"
"s_add_u32 s56,s22,1048576\n"
"s_add_u32 s58,s22,1310720\n"
"v_mfma_f32_16x16x32_bf16 v[84:87],a[92:95],v[252:255],v[84:87]\n"
"buffer_load_dwordx4 v112,s[24:27],s18 offen offset:2048 sc1 lds\n"
"v_mfma_f32_16x16x32_bf16 v[80:83],a[88:91],v[252:255],v[80:83]\n"
"ds_read_b128 v[252:255],v112 offset:31744\n"
"s_add_u32 s59,s22,1572864\n"
"v_mfma_f32_16x16x32_bf16 v[80:83],a[80:83],v[248:251],v[80:83]\n"
"ds_read_b128 v[248:251],v112 offset:30720\n"
"s_bfe_u32 s10,s3,0x20015\n"
"v_mfma_f32_16x16x32_bf16 v[80:83],a[72:75],v[244:247],v[80:83]\n"
"ds_read_b128 v[244:247],v112 offset:29696\n"
"s_mul_i32 s10,s10,4186112\n"
"v_mfma_f32_16x16x32_bf16 v[80:83],a[156:159],v[240:243],v[80:83]\n"
"ds_read_b128 v[240:243],v112 offset:28672\n"
"s_waitcnt lgkmcnt(13)\n"
"v_mfma_f32_16x16x32_bf16 v[64:67],a[224:227],v[192:195],0\n"
"buffer_load_dwordx4 v112,s[24:27],s18 offen offset:3072 sc1 lds\n"
"s_add_u32 m0,m0,4096\n"
"v_mfma_f32_16x16x32_bf16 v[64:67],a[232:235],v[196:199],v[64:67]\n"
"buffer_load_dwordx4 v117,s[24:27],s18 offen offset:0 sc1 lds\n"
"v_mfma_f32_16x16x32_bf16 v[64:67],a[240:243],v[200:203],v[64:67]\n"
"s_add_u32 s12,s12,s10\n"
"s_bfe_u32 s10,s3,0x30012\n"
"v_mfma_f32_16x16x32_bf16 v[64:67],a[248:251],v[204:207],v[64:67]\n"
"buffer_load_dwordx4 v117,s[24:27],s18 offen offset:1024 sc1 lds\n"
"v_mfma_f32_16x16x32_bf16 v[68:71],a[252:255],v[204:207],0\n"
"ds_read_b128 v[204:207],v112 offset:35840\n"
"s_lshl_b32 s10,s10,12\n"
"v_mfma_f32_16x16x32_bf16 v[68:71],a[244:247],v[200:203],v[68:71]\n"
"ds_read_b128 v[200:203],v112 offset:34816\n"
"s_add_u32 s12,s12,s10\n"
"v_mfma_f32_16x16x32_bf16 v[68:71],a[236:239],v[196:199],v[68:71]\n"
"ds_read_b128 v[196:199],v112 offset:33792\n"
"s_bfe_u32 s10,s3,0x50012\n"
"v_mfma_f32_16x16x32_bf16 v[68:71],a[228:231],v[192:195],v[68:71]\n"
"ds_read_b128 v[192:195],v112 offset:32768\n"
"s_waitcnt lgkmcnt(13)\n"
"s_waitcnt vmcnt(8) lgkmcnt(12)\n"
"s_barrier\n"
"v_mfma_f32_16x16x32_bf16 v[68:71],a[4:7],v[208:211],v[68:71]\n"
"buffer_store_dwordx2 a[154:155],off,s[24:27],s71 sc1\n"
"buffer_load_dwordx4 v117,s[24:27],s18 offen offset:2048 sc1 lds\n"
"v_mfma_f32_16x16x32_bf16 v[68:71],a[12:15],v[212:215],v[68:71]\n"
"buffer_load_dwordx4 v117,s[24:27],s18 offen offset:3072 sc1 lds\n"
"v_mfma_f32_16x16x32_bf16 v[68:71],a[20:23],v[216:219],v[68:71]\n"
"s_sub_u32 s82,s3,212992\n"
"s_lshl_b32 s10,s10,8\n"
"v_mfma_f32_16x16x32_bf16 v[68:71],a[28:31],v[220:223],v[68:71]\n"
"buffer_load_dwordx4 a[48:51],v112,s[24:27],s82 offen offset:0 sc1\n"
"v_mfma_f32_16x16x32_bf16 v[64:67],a[24:27],v[220:223],v[64:67]\n"
"s_add_u32 s10,s10,149061632\n"
"ds_read_b128 v[220:223],v112 offset:39936\n"
"s_mov_b32 s14,0\n"
"v_mfma_f32_16x16x32_bf16 v[64:67],a[16:19],v[216:219],v[64:67]\n"
"ds_read_b128 v[216:219],v112 offset:38912\n"
"v_not_b32 v122,s80\n"
"v_mfma_f32_16x16x32_bf16 v[64:67],a[8:11],v[212:215],v[64:67]\n"
"s_cmp_eq_u32 s52,0\n"
"ds_read_b128 v[212:215],v112 offset:37888\n"
"s_cselect_b64 s[14:15],s[14:15],0\n"
"v_mfma_f32_16x16x32_bf16 v[64:67],a[0:3],v[208:211],v[64:67]\n"
"s_cselect_b32 s10,s10,2146435072\n"
"ds_read_b128 v[208:211],v112 offset:36864\n"
"s_waitcnt lgkmcnt(12)\n"
"v_mfma_f32_16x16x32_bf16 v[64:67],v[32:35],v[224:227],v[64:67]\n"
"buffer_load_dwordx4 a[52:55],v112,s[24:27],s82 offen offset:1024 sc1\n"
"s_cmp_lg_u32 s15,0\n"
"s_cselect_b32 s91,s11,2146435072\n"
"v_mfma_f32_16x16x32_bf16 v[64:67],v[40:43],v[228:231],v[64:67]\n"
"buffer_load_dwordx4 a[56:59],v112,s[24:27],s82 offen offset:2048 sc1\n"
"buffer_load_dwordx2 v[122:123],v115,s[24:27],s91 offen sc1\n"
"v_mfma_f32_16x16x32_bf16 v[64:67],a[32:35],v[232:235],v[64:67]\n"
"s_mov_b32 s89,0\n"
"s_mov_b32 s20,32768\n"
"v_mfma_f32_16x16x32_bf16 v[64:67],a[40:43],v[236:239],v[64:67]\n"
"buffer_load_dwordx4 a[60:63],v112,s[24:27],s82 offen offset:3072 sc1\n"
"v_mfma_f32_16x16x32_bf16 v[68:71],a[44:47],v[236:239],v[68:71]\n"
"s_mov_b32 s19,0xffff\n"
"ds_read_b128 v[236:239],v112 offset:44032\n"
"s_mov_b32 s21,32\n"
"v_mfma_f32_16x16x32_bf16 v[68:71],a[36:39],v[232:235],v[68:71]\n"
"ds_read_b128 v[232:235],v112 offset:43008\n"
"v_mfma_f32_16x16x32_bf16 v[68:71],v[44:47],v[228:231],v[68:71]\n"
"ds_read_b128 v[228:231],v112 offset:41984\n"
"v_mfma_f32_16x16x32_bf16 v[68:71],v[36:39],v[224:227],v[68:71]\n"
"s_sub_u32 s82,s3,229376\n"
"ds_read_b128 v[224:227],v112 offset:40960\n"
"s_waitcnt lgkmcnt(12)\n"
"v_mfma_f32_16x16x32_bf16 v[68:71],a[68:71],v[240:243],v[68:71]\n"
"buffer_load_dwordx4 v[48:51],v112,s[24:27],s82 offen offset:0 sc1\n"
"v_mfma_f32_16x16x32_bf16 v[68:71],a[76:79],v[244:247],v[68:71]\n"
"buffer_load_dwordx4 v[52:55],v112,s[24:27],s82 offen offset:1024 sc1\n"
"v_mfma_f32_16x16x32_bf16 v[68:71],a[84:87],v[248:251],v[68:71]\n"
"buffer_load_dwordx4 v[56:59],v112,s[24:27],s82 offen offset:2048 sc1\n"
"v_mfma_f32_16x16x32_bf16 v[68:71],a[92:95],v[252:255],v[68:71]\n"
"s_waitcnt vmcnt(17)\n"
"v_mfma_f32_16x16x32_bf16 v[64:67],a[88:91],v[252:255],v[64:67]\n"
"ds_read_b128 v[252:255],v112 offset:48128\n"
"v_xor_b32 v119,s80,v104\n"
"v_mfma_f32_16x16x32_bf16 v[64:67],a[80:83],v[248:251],v[64:67]\n"
"ds_read_b128 v[248:251],v112 offset:47104\n"
"v_xor_b32 v120,s81,v105\n"
"v_mfma_f32_16x16x32_bf16 v[64:67],a[72:75],v[244:247],v[64:67]\n"
"ds_read_b128 v[244:247],v112 offset:46080\n"
"v_or_b32 v119,v119,v120\n"
"v_mfma_f32_16x16x32_bf16 v[64:67],a[156:159],v[240:243],v[64:67]\n"
"ds_read_b128 v[240:243],v112 offset:45056\n"
"s_waitcnt lgkmcnt(12)\n"
"v_mfma_f32_16x16x32_bf16 v[72:75],a[224:227],v[192:195],0\n"
"buffer_load_dwordx4 v[60:63],v112,s[24:27],s82 offen offset:3072 sc1\n"
"s_sub_u32 s82,s3,245760\n"
"v_mfma_f32_16x16x32_bf16 v[72:75],a[232:235],v[196:199],v[72:75]\n"
"buffer_load_dwordx4 v[16:19],v112,s[24:27],s82 offen offset:0 sc1\n"
"v_xor_b32 v120,s80,v106\n"
"v_mfma_f32_16x16x32_bf16 v[72:75],a[240:243],v[200:203],v[72:75]\n"
"v_or_b32 v119,v119,v120\n"
"v_xor_b32 v120,s81,v107\n"
"v_mfma_f32_16x16x32_bf16 v[72:75],a[248:251],v[204:207],v[72:75]\n"
"buffer_load_dwordx4 v[20:23],v112,s[24:27],s82 offen offset:1024 sc1\n"
"v_or_b32 v119,v119,v120\n"
"v_mfma_f32_16x16x32_bf16 v[76:79],a[252:255],v[204:207],0\n"
"ds_read_b128 v[204:207],v112 offset:52224\n"
"v_xor_b32 v120,s80,v108\n"
"v_mfma_f32_16x16x32_bf16 v[76:79],a[244:247],v[200:203],v[76:79]\n"
"ds_read_b128 v[200:203],v112 offset:51200\n"
"v_or_b32 v119,v119,v120\n"
"v_mfma_f32_16x16x32_bf16 v[76:79],a[236:239],v[196:199],v[76:79]\n"
"ds_read_b128 v[196:199],v112 offset:50176\n"
"v_xor_b32 v120,s81,v109\n"
"v_mfma_f32_16x16x32_bf16 v[76:79],a[228:231],v[192:195],v[76:79]\n"
"ds_read_b128 v[192:195],v112 offset:49152\n"
"s_waitcnt lgkmcnt(12)\n"
"v_mfma_f32_16x16x32_bf16 v[76:79],a[4:7],v[208:211],v[76:79]\n"
"buffer_load_dwordx4 v[24:27],v112,s[24:27],s82 offen offset:2048 sc1\n"
"v_or_b32 v119,v119,v120\n"
"v_mfma_f32_16x16x32_bf16 v[76:79],a[12:15],v[212:215],v[76:79]\n"
"buffer_load_dwordx4 v[28:31],v112,s[24:27],s82 offen offset:3072 sc1\n"
"v_mfma_f32_16x16x32_bf16 v[76:79],a[20:23],v[216:219],v[76:79]\n"
"s_add_u32 s18,s18,2097152\n"
"v_xor_b32 v120,s80,v110\n"
"s_add_u32 m0,s30,98304\n"
"v_mfma_f32_16x16x32_bf16 v[76:79],a[28:31],v[220:223],v[76:79]\n"
"buffer_load_dwordx4 v112,s[24:27],s18 offen offset:0 sc1 lds\n"
"v_mfma_f32_16x16x32_bf16 v[72:75],a[24:27],v[220:223],v[72:75]\n"
"ds_read_b128 v[220:223],v112 offset:56320\n"
"v_or_b32 v119,v119,v120\n"
"v_mfma_f32_16x16x32_bf16 v[72:75],a[16:19],v[216:219],v[72:75]\n"
"ds_read_b128 v[216:219],v112 offset:55296\n"
"v_xor_b32 v120,s81,v111\n"
"v_mfma_f32_16x16x32_bf16 v[72:75],a[8:11],v[212:215],v[72:75]\n"
"ds_read_b128 v[212:215],v112 offset:54272\n"
"v_or_b32 v119,v119,v120\n"
"v_mfma_f32_16x16x32_bf16 v[72:75],a[0:3],v[208:211],v[72:75]\n"
"ds_read_b128 v[208:211],v112 offset:53248\n"
"s_waitcnt lgkmcnt(12)\n"
"v_mfma_f32_16x16x32_bf16 v[72:75],v[32:35],v[224:227],v[72:75]\n"
"buffer_load_dwordx4 v112,s[24:27],s18 offen offset:1024 sc1 lds\n"
"v_mfma_f32_16x16x32_bf16 v[72:75],v[40:43],v[228:231],v[72:75]\n"
"buffer_load_dwordx4 v112,s[24:27],s18 offen offset:2048 sc1 lds\n"
"v_mfma_f32_16x16x32_bf16 v[72:75],a[32:35],v[232:235],v[72:75]\n"
"v_cmp_ne_u32 vcc,0,v119\n"
"v_mfma_f32_16x16x32_bf16 v[72:75],a[40:43],v[236:239],v[72:75]\n"
"buffer_load_dwordx4 v112,s[24:27],s18 offen offset:3072 sc1 lds\n"
"v_mfma_f32_16x16x32_bf16 v[76:79],a[44:47],v[236:239],v[76:79]\n"
"ds_read_b128 v[236:239],v112 offset:60416\n"
"v_mfma_f32_16x16x32_bf16 v[76:79],a[36:39],v[232:235],v[76:79]\n"
"ds_read_b128 v[232:235],v112 offset:59392\n"
"v_xor_b32 v119,s80,v122\n"
"v_mfma_f32_16x16x32_bf16 v[76:79],v[44:47],v[228:231],v[76:79]\n"
"ds_read_b128 v[228:231],v112 offset:58368\n"
"v_xor_b32 v120,s81,v123\n"
"v_mfma_f32_16x16x32_bf16 v[76:79],v[36:39],v[224:227],v[76:79]\n"
"s_add_u32 m0,m0,4096\n"
"ds_read_b128 v[224:227],v112 offset:57344\n"
"s_waitcnt lgkmcnt(12)\n"
"v_mfma_f32_16x16x32_bf16 v[76:79],a[68:71],v[240:243],v[76:79]\n"
"buffer_load_dwordx4 v117,s[24:27],s18 offen offset:0 sc1 lds\n"
"v_or_b32 v119,v119,v120\n"
"v_mfma_f32_16x16x32_bf16 v[76:79],a[76:79],v[244:247],v[76:79]\n"
"buffer_load_dwordx4 v117,s[24:27],s18 offen offset:1024 sc1 lds\n"
"v_cmp_ne_u32 s[28:29],0,v119\n"
"v_mfma_f32_16x16x32_bf16 v[76:79],a[84:87],v[248:251],v[76:79]\n"
"s_and_b64 s[14:15],s[14:15],s[28:29]\n"
"s_cselect_b32 s91,s11,2146435072\n"
"buffer_load_dwordx2 v[122:123],v115,s[24:27],s91 offen sc1\n"
"v_mfma_f32_16x16x32_bf16 v[76:79],a[92:95],v[252:255],v[76:79]\n"
"buffer_load_dwordx4 v117,s[24:27],s18 offen offset:2048 sc1 lds\n"
"v_mfma_f32_16x16x32_bf16 v[72:75],a[88:91],v[252:255],v[72:75]\n"
"ds_read_b128 v[252:255],v112 offset:64512\n"
"v_mfma_f32_16x16x32_bf16 v[72:75],a[80:83],v[248:251],v[72:75]\n"
"ds_read_b128 v[248:251],v112 offset:63488\n"
"v_mfma_f32_16x16x32_bf16 v[72:75],a[72:75],v[244:247],v[72:75]\n"
"ds_read_b128 v[244:247],v112 offset:62464\n"
"v_mfma_f32_16x16x32_bf16 v[72:75],a[156:159],v[240:243],v[72:75]\n"
"s_and_b32 s83,s15,0xffff\n"
"ds_read_b128 v[240:243],v112 offset:61440\n"
"s_waitcnt lgkmcnt(12)\n"
"v_mfma_f32_16x16x32_bf16 v[88:91],a[224:227],v[192:195],0\n"
"buffer_load_dwordx4 v117,s[24:27],s18 offen offset:3072 sc1 lds\n"
"v_mfma_f32_16x16x32_bf16 v[88:91],a[232:235],v[196:199],v[88:91]\n"
"s_cbranch_scc1 .Lfk_es_lkone\n"
".Lfk_er_lkone:\n"
"v_mfma_f32_16x16x32_bf16 v[88:91],a[240:243],v[200:203],v[88:91]\n"
"s_waitcnt vmcnt(22)\n"
"ds_add_u32 v114,v116\n"
"v_mfma_f32_16x16x32_bf16 v[88:91],a[248:251],v[204:207],v[88:91]\n"
"v_and_b32 v118,0xf0,v112\n"
"v_lshrrev_b32 v120,8,v112\n"
"v_lshlrev_b32 v120,4,v120\n"
"v_lshl_add_u32 v118,v118,2,v120\n"
"v_mfma_f32_16x16x32_bf16 v[92:95],a[252:255],v[204:207],0\n"
"s_lshr_b32 s82,s78,20\n"
"v_xor_b32 v118,s82,v118\n"
"v_mov_b32 v100,1\n"
"v_mfma_f32_16x16x32_bf16 v[92:95],a[244:247],v[200:203],v[92:95]\n"
"s_mov_b32 s77,0\n"
"v_mov_b32 v102,131076\n"
"s_and_b32 s91,s52,1\n"
"v_mfma_f32_16x16x32_bf16 v[92:95],a[236:239],v[196:199],v[92:95]\n"
"s_lshl_b32 s91,s91,7\n"
"v_xor_b32 v96,s91,v112\n"
"v_xor_b32 v97,32,v96\n"
"v_mfma_f32_16x16x32_bf16 v[92:95],a[228:231],v[192:195],v[92:95]\n"
"v_xor_b32 v99,96,v96\n"
"s_waitcnt lgkmcnt(9)\n"
"v_mfma_f32_16x16x32_bf16 v[92:95],a[4:7],v[208:211],v[92:95]\n"
"v_xor_b32 v98,64,v96\n"
"v_add_u32 v118,s12,v118\n"
"v_mfma_f32_16x16x32_bf16 v[92:95],a[12:15],v[212:215],v[92:95]\n"
"ds_read_b32 v125,v114\n"
"v_mfma_f32_16x16x32_bf16 v[92:95],a[20:23],v[216:219],v[92:95]\n"
"v_mfma_f32_16x16x32_bf16 v[92:95],a[28:31],v[220:223],v[92:95]\n"
"v_mfma_f32_16x16x32_bf16 v[88:91],a[24:27],v[220:223],v[88:91]\n"
"v_mfma_f32_16x16x32_bf16 v[88:91],a[16:19],v[216:219],v[88:91]\n"
"s_waitcnt lgkmcnt(0)\n"
"v_cmp_gt_u32 s[28:29],s16,v125\n"
"v_mfma_f32_16x16x32_bf16 v[88:91],a[8:11],v[212:215],v[88:91]\n"
"s_add_u32 s18,s8,4194304\n"
"s_add_u32 s8,s8,5242880\n"
"s_or_b64 vcc,vcc,s[28:29]\n"
"v_mfma_f32_16x16x32_bf16 v[88:91],a[0:3],v[208:211],v[88:91]\n"
"s_cbranch_vccnz .Lfk_z0\n"
".Lfk_z1:\n"
"ds_read_b128 v[192:195],v113 offset:0\n"
"v_mfma_f32_16x16x32_bf16 v[88:91],v[32:35],v[224:227],v[88:91]\n"
"s_mov_b32 s89,1\n"
"s_mov_b32 s19,0xff\n"
"s_lshl_b64 exec,1,s89\n"
"buffer_atomic_add v100,off,s[24:27],s10 sc0\n"
"s_mov_b64 exec,-1\n"
"v_mfma_f32_16x16x32_bf16 v[88:91],v[40:43],v[228:231],v[88:91]\n"
"ds_read_b128 v[196:199],v113 offset:1024\n"
"s_add_u32 m0,s30,0\n"
"v_mfma_f32_16x16x32_bf16 v[88:91],a[32:35],v[232:235],v[88:91]\n"
"ds_read_b128 v[200:203],v113 offset:2048\n"
"buffer_load_dwordx4 v112,s[24:27],s18 offen offset:0 sc1 lds\n"
"v_mfma_f32_16x16x32_bf16 v[88:91],a[40:43],v[236:239],v[88:91]\n"
"s_add_u32 s89,s89,1\n"
"ds_read_b128 v[204:207],v113 offset:3072\n"
"s_add_u32 s21,s21,16\n"
"v_mfma_f32_16x16x32_bf16 v[92:95],a[44:47],v[236:239],v[92:95]\n"
"s_mov_b32 s38,4194304\n"
"ds_read_b128 v[208:211],v113 offset:16384\n"
"s_mov_b32 s39,0x20000\n"
"v_mfma_f32_16x16x32_bf16 v[92:95],a[36:39],v[232:235],v[92:95]\n"
"s_add_u32 s16,s16,4\n"
"ds_read_b128 v[212:215],v113 offset:17408\n"
"s_mov_b32 s46,2048\n"
"v_mfma_f32_16x16x32_bf16 v[92:95],v[44:47],v[228:231],v[92:95]\n"
"ds_read_b128 v[216:219],v113 offset:18432\n"
"buffer_load_dwordx4 v112,s[24:27],s18 offen offset:1024 sc1 lds\n"
"v_mfma_f32_16x16x32_bf16 v[92:95],v[36:39],v[224:227],v[92:95]\n"
"s_mov_b32 s47,0x20000\n"
"ds_read_b128 v[220:223],v113 offset:19456\n"
"s_mov_b32 s50,4194304\n"
"v_mfma_f32_16x16x32_bf16 v[92:95],a[68:71],v[240:243],v[92:95]\n"
"s_mov_b32 s51,0x20000\n"
"ds_read_b128 v[236:239],v113 offset:7168\n"
"s_and_b32 s60,s100,7\n"
"v_mfma_f32_16x16x32_bf16 v[92:95],a[76:79],v[244:247],v[92:95]\n"
"s_lshl_b32 s60,s60,5\n"
"ds_read_b128 v[232:235],v113 offset:6144\n"
"s_lshr_b32 s73,s100,3\n"
"v_mfma_f32_16x16x32_bf16 v[92:95],a[84:87],v[248:251],v[92:95]\n"
"ds_read_b128 v[228:231],v113 offset:5120\n"
"buffer_load_dwordx4 v112,s[24:27],s18 offen offset:2048 sc1 lds\n"
"v_mfma_f32_16x16x32_bf16 v[92:95],a[92:95],v[252:255],v[92:95]\n"
"s_add_u32 s73,s73,s60\n"
"ds_read_b128 v[224:227],v113 offset:4096\n"
"s_and_b32 s60,s100,7\n"
"v_mfma_f32_16x16x32_bf16 v[88:91],a[88:91],v[252:255],v[88:91]\n"
"s_bfe_u32 s61,s100,0x10003\n"
"ds_read_b128 v[252:255],v113 offset:23552\n"
"s_lshl1_add_u32 s60,s60,s61\n"
"v_mfma_f32_16x16x32_bf16 v[88:91],a[80:83],v[248:251],v[88:91]\n"
"s_and_b32 s61,s100,0xf0\n"
"ds_read_b128 v[248:251],v113 offset:22528\n"
"s_add_u32 s60,s60,s61\n"
"v_mfma_f32_16x16x32_bf16 v[88:91],a[72:75],v[244:247],v[88:91]\n"
"s_lshl_b32 s61,s52,8\n"
"ds_read_b128 v[244:247],v113 offset:21504\n"
"s_add_u32 s60,s60,s61\n"
"v_mfma_f32_16x16x32_bf16 v[88:91],a[156:159],v[240:243],v[88:91]\n"
"ds_read_b128 v[240:243],v113 offset:20480\n"
"s_waitcnt lgkmcnt(12)\n"
"v_mfma_f32_16x16x32_bf16 v[80:83],v[128:131],v[192:195],v[80:83]\n"
"buffer_load_dwordx4 v112,s[24:27],s18 offen offset:3072 sc1 lds\n"
"v_mfma_f32_16x16x32_bf16 v[80:83],v[136:139],v[196:199],v[80:83]\n"
"s_lshl_b32 s61,s60,11\n"
"s_add_u32 s68,s61,2097152\n"
"v_mfma_f32_16x16x32_bf16 v[80:83],v[144:147],v[200:203],v[80:83]\n"
"s_add_u32 m0,m0,4096\n"
"v_mfma_f32_16x16x32_bf16 v[80:83],v[152:155],v[204:207],v[80:83]\n"
"buffer_load_dwordx4 v117,s[24:27],s18 offen offset:0 sc1 lds\n"
"v_mfma_f32_16x16x32_bf16 v[84:87],v[156:159],v[204:207],v[84:87]\n"
"ds_read_b128 v[204:207],v113 offset:11264\n"
"s_add_u32 s20,s20,32768\n"
"v_mfma_f32_16x16x32_bf16 v[84:87],v[148:151],v[200:203],v[84:87]\n"
"ds_read_b128 v[200:203],v113 offset:10240\n"
"s_and_b32 s20,s20,0x1ffff\n"
"v_mfma_f32_16x16x32_bf16 v[84:87],v[140:143],v[196:199],v[84:87]\n"
"ds_read_b128 v[196:199],v113 offset:9216\n"
"v_mfma_f32_16x16x32_bf16 v[84:87],v[132:135],v[192:195],v[84:87]\n"
"ds_read_b128 v[192:195],v113 offset:8192\n"
"s_waitcnt lgkmcnt(12)\n"
"v_mfma_f32_16x16x32_bf16 v[68:71],v[132:135],v[208:211],v[68:71]\n"
"buffer_load_dwordx4 v117,s[24:27],s18 offen offset:1024 sc1 lds\n"
"v_mfma_f32_16x16x32_bf16 v[68:71],v[140:143],v[212:215],v[68:71]\n"
"v_mfma_f32_16x16x32_bf16 v[68:71],v[148:151],v[216:219],v[68:71]\n"
"v_mfma_f32_16x16x32_bf16 v[68:71],v[156:159],v[220:223],v[68:71]\n"
"buffer_load_dwordx4 v117,s[24:27],s18 offen offset:2048 sc1 lds\n"
"v_mfma_f32_16x16x32_bf16 v[64:67],v[152:155],v[220:223],v[64:67]\n"
"ds_read_b128 v[220:223],v113 offset:27648\n"
"v_xor_b32 v119,s80,v122\n"
"v_mfma_f32_16x16x32_bf16 v[64:67],v[144:147],v[216:219],v[64:67]\n"
"ds_read_b128 v[216:219],v113 offset:26624\n"
"v_xor_b32 v120,s81,v123\n"
"v_mfma_f32_16x16x32_bf16 v[64:67],v[136:139],v[212:215],v[64:67]\n"
"ds_read_b128 v[212:215],v113 offset:25600\n"
"v_or_b32 v119,v119,v120\n"
"v_mfma_f32_16x16x32_bf16 v[64:67],v[128:131],v[208:211],v[64:67]\n"
"ds_read_b128 v[208:211],v113 offset:24576\n"
"s_waitcnt lgkmcnt(12)\n"
"v_mfma_f32_16x16x32_bf16 v[80:83],v[0:3],v[224:227],v[80:83]\n"
"buffer_load_dwordx4 v117,s[24:27],s18 offen offset:3072 sc1 lds\n"
"v_cmp_ne_u32 s[28:29],0,v119\n"
"v_mfma_f32_16x16x32_bf16 v[80:83],v[8:11],v[228:231],v[80:83]\n"
"s_and_b64 s[14:15],s[14:15],s[28:29]\n"
"s_cselect_b32 s91,s11,2146435072\n"
"s_add_u32 m0,s30,32768\n"
"v_mfma_f32_16x16x32_bf16 v[80:83],v[160:163],v[232:235],v[80:83]\n"
"buffer_load_dwordx2 v[122:123],v115,s[24:27],s91 offen sc1\n"
"v_mfma_f32_16x16x32_bf16 v[80:83],v[168:171],v[236:239],v[80:83]\n"
"buffer_load_dwordx4 v112,s[24:27],s8 offen offset:0 sc1 lds\n"
"v_mfma_f32_16x16x32_bf16 v[84:87],v[172:175],v[236:239],v[84:87]\n"
"ds_read_b128 v[236:239],v113 offset:15360\n"
"v_mfma_f32_16x16x32_bf16 v[84:87],v[164:167],v[232:235],v[84:87]\n"
"ds_read_b128 v[232:235],v113 offset:14336\n"
"v_mfma_f32_16x16x32_bf16 v[84:87],v[12:15],v[228:231],v[84:87]\n"
"ds_read_b128 v[228:231],v113 offset:13312\n"
"v_mfma_f32_16x16x32_bf16 v[84:87],v[4:7],v[224:227],v[84:87]\n"
"s_and_b32 s83,s15,0xff0000\n"
"ds_read_b128 v[224:227],v113 offset:12288\n"
"s_waitcnt lgkmcnt(12)\n"
"v_mfma_f32_16x16x32_bf16 v[68:71],v[4:7],v[240:243],v[68:71]\n"
"buffer_load_dwordx4 v112,s[24:27],s8 offen offset:1024 sc1 lds\n"
"v_mfma_f32_16x16x32_bf16 v[68:71],v[12:15],v[244:247],v[68:71]\n"
"s_cbranch_scc1 .Lfk_es_lkonem\n"
".Lfk_er_lkonem:\n"
"v_mfma_f32_16x16x32_bf16 v[68:71],v[164:167],v[248:251],v[68:71]\n"
"v_mfma_f32_16x16x32_bf16 v[68:71],v[172:175],v[252:255],v[68:71]\n"
"buffer_load_dwordx4 v112,s[24:27],s8 offen offset:2048 sc1 lds\n"
"v_mfma_f32_16x16x32_bf16 v[64:67],v[168:171],v[252:255],v[64:67]\n"
"ds_read_b128 v[252:255],v113 offset:31744\n"
"v_mfma_f32_16x16x32_bf16 v[64:67],v[160:163],v[248:251],v[64:67]\n"
"ds_read_b128 v[248:251],v113 offset:30720\n"
"v_mfma_f32_16x16x32_bf16 v[64:67],v[8:11],v[244:247],v[64:67]\n"
"ds_read_b128 v[244:247],v113 offset:29696\n"
"v_mfma_f32_16x16x32_bf16 v[64:67],v[0:3],v[240:243],v[64:67]\n"
"ds_read_b128 v[240:243],v113 offset:28672\n"
"s_waitcnt lgkmcnt(12)\n"
"v_mfma_f32_16x16x32_bf16 v[80:83],v[176:179],v[192:195],v[80:83]\n"
"s_waitcnt vmcnt(13)\n"
"ds_add_u32 v114,v116\n"
"v_mfma_f32_16x16x32_bf16 v[80:83],v[184:187],v[196:199],v[80:83]\n"
"buffer_load_dwordx4 v112,s[24:27],s8 offen offset:3072 sc1 lds\n"
"v_mfma_f32_16x16x32_bf16 v[80:83],a[48:51],v[200:203],v[80:83]\n"
"s_add_u32 m0,m0,4096\n"
"v_mfma_f32_16x16x32_bf16 v[80:83],a[56:59],v[204:207],v[80:83]\n"
"s_add_u32 s83,s20,32768\n"
"s_and_b32 s83,s83,0x1ffff\n"
"v_add_u32 v113,s83,v112\n"
"v_mfma_f32_16x16x32_bf16 v[84:87],a[60:63],v[204:207],v[84:87]\n"
"buffer_load_dwordx4 v117,s[24:27],s8 offen offset:0 sc1 lds\n"
"v_mfma_f32_16x16x32_bf16 v[84:87],a[52:55],v[200:203],v[84:87]\n"
"s_cmp_eq_u32 s52,0\n"
"v_not_b32 v104,s80\n"
"s_cselect_b32 s9,s9,2146435072\n"
"v_mfma_f32_16x16x32_bf16 v[84:87],v[188:191],v[196:199],v[84:87]\n"
"buffer_load_dwordx2 v[104:105],v115,s[24:27],s9 offen sc1\n"
"v_mfma_f32_16x16x32_bf16 v[84:87],v[180:183],v[192:195],v[84:87]\n"
"buffer_load_dwordx4 v117,s[24:27],s8 offen offset:1024 sc1 lds\n"
"s_waitcnt lgkmcnt(9)\n"
"v_mfma_f32_16x16x32_bf16 v[68:71],v[180:183],v[208:211],v[68:71]\n"
"v_mfma_f32_16x16x32_bf16 v[68:71],v[188:191],v[212:215],v[68:71]\n"
"ds_read_b32 v125,v114\n"
"v_mfma_f32_16x16x32_bf16 v[68:71],a[52:55],v[216:219],v[68:71]\n"
"buffer_load_dwordx4 v117,s[24:27],s8 offen offset:2048 sc1 lds\n"
"v_mfma_f32_16x16x32_bf16 v[68:71],a[60:63],v[220:223],v[68:71]\n"
"v_mfma_f32_16x16x32_bf16 v[64:67],a[56:59],v[220:223],v[64:67]\n"
"v_mfma_f32_16x16x32_bf16 v[64:67],a[48:51],v[216:219],v[64:67]\n"
"s_waitcnt lgkmcnt(0)\n"
"v_cmp_gt_u32 vcc,s16,v125\n"
"v_mfma_f32_16x16x32_bf16 v[64:67],v[184:187],v[212:215],v[64:67]\n"
"buffer_load_dwordx4 v117,s[24:27],s8 offen offset:3072 sc1 lds\n"
"v_mfma_f32_16x16x32_bf16 v[64:67],v[176:179],v[208:211],v[64:67]\n"
"s_lshl_b64 exec,1,s89\n"
"buffer_atomic_add v100,off,s[24:27],s10 sc0\n"
"s_mov_b64 exec,-1\n"
"s_add_u32 s8,s8,1048576\n"
"s_cbranch_vccnz .Lfk_es_mone\n"
".Lfk_er_mone:\n"
"v_mfma_f32_16x16x32_bf16 v[80:83],v[48:51],v[224:227],v[80:83]\n"
"ds_read_b128 v[192:195],v113 offset:0\n"
"v_mfma_f32_16x16x32_bf16 v[80:83],v[56:59],v[228:231],v[80:83]\n"
"ds_read_b128 v[196:199],v113 offset:1024\n"
"ds_read_b128 v[200:203],v113 offset:2048\n"
"v_mfma_f32_16x16x32_bf16 v[80:83],v[16:19],v[232:235],v[80:83]\n"
"buffer_load_dwordx4 a[96:99],v112,s[36:39],s61 offen offset:0\n"
"v_mfma_f32_16x16x32_bf16 v[80:83],v[24:27],v[236:239],v[80:83]\n"
"s_add_u32 s89,s89,1\n"
"ds_read_b128 v[204:207],v113 offset:3072\n"
"s_add_u32 s21,s21,8\n"
"v_mfma_f32_16x16x32_bf16 v[84:87],v[28:31],v[236:239],v[84:87]\n"
"ds_read_b128 v[208:211],v113 offset:4096\n"
"ds_read_b128 v[212:215],v113 offset:5120\n"
"v_mfma_f32_16x16x32_bf16 v[84:87],v[20:23],v[232:235],v[84:87]\n"
"buffer_load_dwordx4 a[100:103],v112,s[36:39],s61 offen offset:1024\n"
"v_mfma_f32_16x16x32_bf16 v[84:87],v[60:63],v[228:231],v[84:87]\n"
"s_add_u32 s16,s16,4\n"
"ds_read_b128 v[216:219],v113 offset:6144\n"
"v_mfma_f32_16x16x32_bf16 v[84:87],v[52:55],v[224:227],v[84:87]\n"
"ds_read_b128 v[220:223],v113 offset:7168\n"
"ds_read_b128 v[236:239],v113 offset:11264\n"
"v_mfma_f32_16x16x32_bf16 v[68:71],v[52:55],v[240:243],v[68:71]\n"
"buffer_load_dwordx4 a[104:107],v112,s[36:39],s68 offen offset:0\n"
"v_mfma_f32_16x16x32_bf16 v[68:71],v[60:63],v[244:247],v[68:71]\n"
"ds_read_b128 v[232:235],v113 offset:10240\n"
"v_cvt_pk_bf16_f32 v80,v80,v81\n"
"v_mfma_f32_16x16x32_bf16 v[68:71],v[20:23],v[248:251],v[68:71]\n"
"ds_read_b128 v[228:231],v113 offset:9216\n"
"ds_read_b128 v[224:227],v113 offset:8192\n"
"v_cvt_pk_bf16_f32 v81,v82,v83\n"
"v_mfma_f32_16x16x32_bf16 v[68:71],v[28:31],v[252:255],v[68:71]\n"
"buffer_load_dwordx4 a[108:111],v112,s[36:39],s68 offen offset:1024\n"
"v_mfma_f32_16x16x32_bf16 v[64:67],v[24:27],v[252:255],v[64:67]\n"
"ds_read_b128 v[252:255],v113 offset:15360\n"
"v_cvt_pk_bf16_f32 v82,v84,v85\n"
"v_cvt_pk_bf16_f32 v83,v86,v87\n"
"v_mfma_f32_16x16x32_bf16 v[64:67],v[16:19],v[248:251],v[64:67]\n"
"ds_read_b128 v[248:251],v113 offset:14336\n"
"v_max_u32 v80,1,v80\n"
"v_max_u32 v81,1,v81\n"
"v_mfma_f32_16x16x32_bf16 v[64:67],v[56:59],v[244:247],v[64:67]\n"
"ds_read_b128 v[244:247],v113 offset:13312\n"
"v_max_u32 v82,1,v82\n"
"v_max_u32 v83,1,v83\n"
"v_mfma_f32_16x16x32_bf16 v[64:67],v[48:51],v[240:243],v[64:67]\n"
"ds_read_b128 v[240:243],v113 offset:12288\n"
"buffer_store_dwordx4 v[80:83],v118,s[24:27],s22 offen sc1\n"
"s_waitcnt lgkmcnt(12)\n"
"v_mfma_f32_16x16x32_bf16 v[72:75],v[128:131],v[192:195],v[72:75]\n"
"s_add_u32 m0,s30,s20\n"
"s_add_u32 s22,s22,262144\n"
"v_mfma_f32_16x16x32_bf16 v[72:75],v[136:139],v[196:199],v[72:75]\n"
"buffer_load_dwordx4 v112,s[24:27],s8 offen offset:0 sc1 lds\n"
"v_mfma_f32_16x16x32_bf16 v[72:75],v[144:147],v[200:203],v[72:75]\n"
"v_mfma_f32_16x16x32_bf16 v[72:75],v[152:155],v[204:207],v[72:75]\n"
"v_cvt_pk_bf16_f32 v64,v64,v65\n"
"v_mfma_f32_16x16x32_bf16 v[76:79],v[156:159],v[204:207],v[76:79]\n"
"ds_read_b128 v[204:207],v113 offset:19456\n"
"s_add_u32 s20,s20,32768\n"
"v_mfma_f32_16x16x32_bf16 v[76:79],v[148:151],v[200:203],v[76:79]\n"
"ds_read_b128 v[200:203],v113 offset:18432\n"
"s_and_b32 s20,s20,0x1ffff\n"
"v_mfma_f32_16x16x32_bf16 v[76:79],v[140:143],v[196:199],v[76:79]\n"
"ds_read_b128 v[196:199],v113 offset:17408\n"
"v_cvt_pk_bf16_f32 v65,v66,v67\n"
"v_mfma_f32_16x16x32_bf16 v[76:79],v[132:135],v[192:195],v[76:79]\n"
"ds_read_b128 v[192:195],v113 offset:16384\n"
"v_cvt_pk_bf16_f32 v66,v68,v69\n"
"s_waitcnt lgkmcnt(12)\n"
"v_mfma_f32_16x16x32_bf16 v[76:79],v[4:7],v[208:211],v[76:79]\n"
"buffer_load_dwordx4 v112,s[24:27],s8 offen offset:1024 sc1 lds\n"
"v_cvt_pk_bf16_f32 v67,v70,v71\n"
"v_mfma_f32_16x16x32_bf16 v[76:79],v[12:15],v[212:215],v[76:79]\n"
"v_max_u32 v64,1,v64\n"
"v_max_u32 v65,1,v65\n"
"v_max_u32 v66,1,v66\n"
"v_max_u32 v67,1,v67\n"
"v_mfma_f32_16x16x32_bf16 v[76:79],v[164:167],v[216:219],v[76:79]\n"
"buffer_store_dwordx4 v[64:67],v118,s[24:27],s22 offen sc1\n"
"v_mfma_f32_16x16x32_bf16 v[76:79],v[172:175],v[220:223],v[76:79]\n"
"buffer_load_dwordx4 v112,s[24:27],s8 offen offset:2048 sc1 lds\n"
"v_mfma_f32_16x16x32_bf16 v[72:75],v[168:171],v[220:223],v[72:75]\n"
"s_add_u32 s22,s22,262144\n"
"ds_read_b128 v[220:223],v113 offset:23552\n"
"v_mfma_f32_16x16x32_bf16 v[72:75],v[160:163],v[216:219],v[72:75]\n"
"ds_read_b128 v[216:219],v113 offset:22528\n"
"v_mfma_f32_16x16x32_bf16 v[72:75],v[8:11],v[212:215],v[72:75]\n"
"ds_read_b128 v[212:215],v113 offset:21504\n"
"v_mfma_f32_16x16x32_bf16 v[72:75],v[0:3],v[208:211],v[72:75]\n"
"ds_read_b128 v[208:211],v113 offset:20480\n"
"s_waitcnt lgkmcnt(12)\n"
"v_mfma_f32_16x16x32_bf16 v[72:75],v[176:179],v[224:227],v[72:75]\n"
"buffer_load_dwordx4 v112,s[24:27],s8 offen offset:3072 sc1 lds\n"
"v_mfma_f32_16x16x32_bf16 v[72:75],v[184:187],v[228:231],v[72:75]\n"
"v_xor_b32 v119,s80,v122\n"
"v_xor_b32 v120,s81,v123\n"
"v_mfma_f32_16x16x32_bf16 v[72:75],a[48:51],v[232:235],v[72:75]\n"
"v_or_b32 v119,v119,v120\n"
"v_cmp_ne_u32 s[28:29],0,v119\n"
"v_mfma_f32_16x16x32_bf16 v[72:75],a[56:59],v[236:239],v[72:75]\n"
"s_and_b64 s[14:15],s[14:15],s[28:29]\n"
"v_mfma_f32_16x16x32_bf16 v[76:79],a[60:63],v[236:239],v[76:79]\n"
"ds_read_b128 v[236:239],v113 offset:27648\n"
"v_mfma_f32_16x16x32_bf16 v[76:79],a[52:55],v[232:235],v[76:79]\n"
"ds_read_b128 v[232:235],v113 offset:26624\n"
"v_mfma_f32_16x16x32_bf16 v[76:79],v[188:191],v[228:231],v[76:79]\n"
"ds_read_b128 v[228:231],v113 offset:25600\n"
"v_mfma_f32_16x16x32_bf16 v[76:79],v[180:183],v[224:227],v[76:79]\n"
"s_add_u32 m0,m0,4096\n"
"ds_read_b128 v[224:227],v113 offset:24576\n"
"s_waitcnt lgkmcnt(12)\n"
"v_mfma_f32_16x16x32_bf16 v[76:79],v[52:55],v[240:243],v[76:79]\n"
"buffer_load_dwordx4 v117,s[24:27],s8 offen offset:0 sc1 lds\n"
"v_mfma_f32_16x16x32_bf16 v[76:79],v[60:63],v[244:247],v[76:79]\n"
"s_and_b32 s83,s15,0xff000000\n"
"s_cbranch_scc1 .Lfk_es_lktwom\n"
".Lfk_er_lktwom:\n"
"v_mfma_f32_16x16x32_bf16 v[76:79],v[20:23],v[248:251],v[76:79]\n"
"v_xor_b32 v119,s80,v104\n"
"v_xor_b32 v120,s81,v105\n"
"v_or_b32 v119,v119,v120\n"
"v_mfma_f32_16x16x32_bf16 v[76:79],v[28:31],v[252:255],v[76:79]\n"
"buffer_load_dwordx4 v117,s[24:27],s8 offen offset:1024 sc1 lds\n"
"v_cmp_ne_u32 s[28:29],0,v119\n"
"v_mfma_f32_16x16x32_bf16 v[72:75],v[24:27],v[252:255],v[72:75]\n"
"s_cmp_eq_u32 s52,0\n"
"ds_read_b128 v[252:255],v113 offset:31744\n"
"s_cselect_b64 s[14:15],-1,0\n"
"v_mfma_f32_16x16x32_bf16 v[72:75],v[16:19],v[248:251],v[72:75]\n"
"s_mov_b32 s11,s9\n"
"ds_read_b128 v[248:251],v113 offset:30720\n"
"s_waitcnt vmcnt(22)\n"
"v_not_b32 v122,s80\n"
"v_mfma_f32_16x16x32_bf16 v[72:75],v[56:59],v[244:247],v[72:75]\n"
"s_and_b64 s[14:15],s[14:15],s[28:29]\n"
"s_cselect_b32 s91,s11,2146435072\n"
"ds_read_b128 v[244:247],v113 offset:29696\n"
"buffer_load_dwordx2 v[122:123],v115,s[24:27],s91 offen sc1\n"
"v_mfma_f32_16x16x32_bf16 v[72:75],v[48:51],v[240:243],v[72:75]\n"
"ds_read_b128 v[240:243],v113 offset:28672\n"
"s_waitcnt lgkmcnt(12)\n"
"v_mfma_f32_16x16x32_bf16 v[88:91],v[128:131],v[192:195],v[88:91]\n"
"ds_write_b32 v101,v100\n"
"v_mfma_f32_16x16x32_bf16 v[88:91],v[136:139],v[196:199],v[88:91]\n"
"ds_add_u32 v114,v116\n"
"v_mfma_f32_16x16x32_bf16 v[88:91],v[144:147],v[200:203],v[88:91]\n"
"buffer_load_dwordx4 v117,s[24:27],s8 offen offset:2048 sc1 lds\n"
"v_mfma_f32_16x16x32_bf16 v[88:91],v[152:155],v[204:207],v[88:91]\n"
"s_add_u32 s83,s20,32768\n"
"s_and_b32 s83,s83,0x1ffff\n"
"v_add_u32 v113,s83,v112\n"
"v_mfma_f32_16x16x32_bf16 v[92:95],v[156:159],v[204:207],v[92:95]\n"
"v_cvt_pk_bf16_f32 v72,v72,v73\n"
"v_cvt_pk_bf16_f32 v73,v74,v75\n"
"v_cvt_pk_bf16_f32 v74,v76,v77\n"
"v_mfma_f32_16x16x32_bf16 v[92:95],v[148:151],v[200:203],v[92:95]\n"
"v_cvt_pk_bf16_f32 v75,v78,v79\n"
"v_max_u32 v72,1,v72\n"
"v_max_u32 v73,1,v73\n"
"v_mfma_f32_16x16x32_bf16 v[92:95],v[140:143],v[196:199],v[92:95]\n"
"v_max_u32 v74,1,v74\n"
"v_max_u32 v75,1,v75\n"
"v_mfma_f32_16x16x32_bf16 v[92:95],v[132:135],v[192:195],v[92:95]\n"
"buffer_store_dwordx4 v[72:75],v118,s[24:27],s22 offen sc1\n"
"s_waitcnt lgkmcnt(10)\n"
"v_mfma_f32_16x16x32_bf16 v[92:95],v[4:7],v[208:211],v[92:95]\n"
"buffer_load_dwordx4 v117,s[24:27],s8 offen offset:3072 sc1 lds\n"
"v_mfma_f32_16x16x32_bf16 v[92:95],v[12:15],v[212:215],v[92:95]\n"
"s_add_u32 s22,s22,262144\n"
"ds_read_b32 v125,v114\n"
"s_add_u32 s77,s77,4096\n"
"s_add_u32 m0,s30,135168\n"
"v_mfma_f32_16x16x32_bf16 v[92:95],v[164:167],v[216:219],v[92:95]\n"
"buffer_load_dwordx4 v98,s[40:43],s77 offen offset:0 nt lds\n"
"v_mfma_f32_16x16x32_bf16 v[92:95],v[172:175],v[220:223],v[92:95]\n"
"buffer_load_dwordx4 v98,s[40:43],s77 offen offset:1024 nt lds\n"
"v_mfma_f32_16x16x32_bf16 v[88:91],v[168:171],v[220:223],v[88:91]\n"
"s_lshl_b64 exec,1,s89\n"
"buffer_atomic_add v100,off,s[24:27],s10 sc0\n"
"s_mov_b64 exec,-1\n"
"v_mfma_f32_16x16x32_bf16 v[88:91],v[160:163],v[216:219],v[88:91]\n"
"s_waitcnt lgkmcnt(0)\n"
"buffer_load_dwordx4 v99,s[40:43],s77 offen offset:2048 nt lds\n"
"v_cmp_gt_u32 vcc,s16,v125\n"
"v_mfma_f32_16x16x32_bf16 v[88:91],v[8:11],v[212:215],v[88:91]\n"
"buffer_load_dwordx4 v99,s[40:43],s77 offen offset:3072 nt lds\n"
"v_mfma_f32_16x16x32_bf16 v[88:91],v[0:3],v[208:211],v[88:91]\n"
"s_add_u32 s8,s8,1048576\n"
"s_cbranch_vccnz .Lfk_es_mtwo\n"
".Lfk_er_mtwo:\n"
".Lfk_l4:\n"
"v_mfma_f32_16x16x32_bf16 v[88:91],v[176:179],v[224:227],v[88:91]\n"
"ds_read_b128 v[192:195],v113 offset:0\n"
"v_mfma_f32_16x16x32_bf16 v[88:91],v[184:187],v[228:231],v[88:91]\n"
"ds_read_b128 v[196:199],v113 offset:1024\n"
"v_mfma_f32_16x16x32_bf16 v[88:91],a[48:51],v[232:235],v[88:91]\n"
"ds_read_b128 v[200:203],v113 offset:2048\n"
"s_add_u32 s89,s89,1\n"
"v_mfma_f32_16x16x32_bf16 v[88:91],a[56:59],v[236:239],v[88:91]\n"
"ds_read_b128 v[204:207],v113 offset:3072\n"
"v_mfma_f32_16x16x32_bf16 v[92:95],a[60:63],v[236:239],v[92:95]\n"
"ds_read_b128 v[208:211],v113 offset:4096\n"
"v_mfma_f32_16x16x32_bf16 v[92:95],a[52:55],v[232:235],v[92:95]\n"
"ds_read_b128 v[212:215],v113 offset:5120\n"
"s_add_u32 s16,s16,4\n"
"v_mfma_f32_16x16x32_bf16 v[92:95],v[188:191],v[228:231],v[92:95]\n"
"ds_read_b128 v[216:219],v113 offset:6144\n"
"v_mfma_f32_16x16x32_bf16 v[92:95],v[180:183],v[224:227],v[92:95]\n"
"ds_read_b128 v[220:223],v113 offset:7168\n"
"v_mfma_f32_16x16x32_bf16 v[92:95],v[52:55],v[240:243],v[92:95]\n"
"ds_read_b128 v[236:239],v113 offset:11264\n"
"v_mfma_f32_16x16x32_bf16 v[92:95],v[60:63],v[244:247],v[92:95]\n"
"ds_read_b128 v[232:235],v113 offset:10240\n"
"v_mfma_f32_16x16x32_bf16 v[92:95],v[20:23],v[248:251],v[92:95]\n"
"ds_read_b128 v[228:231],v113 offset:9216\n"
"v_mfma_f32_16x16x32_bf16 v[92:95],v[28:31],v[252:255],v[92:95]\n"
"ds_read_b128 v[224:227],v113 offset:8192\n"
"v_mfma_f32_16x16x32_bf16 v[88:91],v[24:27],v[252:255],v[88:91]\n"
"ds_read_b128 v[252:255],v113 offset:15360\n"
"v_mfma_f32_16x16x32_bf16 v[88:91],v[16:19],v[248:251],v[88:91]\n"
"ds_read_b128 v[248:251],v113 offset:14336\n"
"v_mfma_f32_16x16x32_bf16 v[88:91],v[56:59],v[244:247],v[88:91]\n"
"ds_read_b128 v[244:247],v113 offset:13312\n"
"v_mfma_f32_16x16x32_bf16 v[88:91],v[48:51],v[240:243],v[88:91]\n"
"ds_read_b128 v[240:243],v113 offset:12288\n"
"s_waitcnt lgkmcnt(12)\n"
"v_mfma_f32_16x16x32_bf16 v[80:83],a[224:227],v[192:195],0\n"
"s_add_u32 m0,s30,s20\n"
"ds_read_b32 v103,v102\n"
"v_mfma_f32_16x16x32_bf16 v[80:83],a[232:235],v[196:199],v[80:83]\n"
"buffer_load_dwordx4 v112,s[24:27],s8 offen offset:0 sc1 lds\n"
"v_mfma_f32_16x16x32_bf16 v[80:83],a[240:243],v[200:203],v[80:83]\n"
"v_add_u32 v102,4,v102\n"
"v_and_b32 v102,0x200ff,v102\n"
"v_mfma_f32_16x16x32_bf16 v[80:83],a[248:251],v[204:207],v[80:83]\n"
"v_cvt_pk_bf16_f32 v88,v88,v89\n"
"v_cvt_pk_bf16_f32 v89,v90,v91\n"
"v_mfma_f32_16x16x32_bf16 v[84:87],a[252:255],v[204:207],0\n"
"ds_read_b128 v[204:207],v113 offset:19456\n"
"s_add_u32 s20,s20,32768\n"
"v_mfma_f32_16x16x32_bf16 v[84:87],a[244:247],v[200:203],v[84:87]\n"
"ds_read_b128 v[200:203],v113 offset:18432\n"
"s_and_b32 s20,s20,0x1ffff\n"
"v_mfma_f32_16x16x32_bf16 v[84:87],a[236:239],v[196:199],v[84:87]\n"
"ds_read_b128 v[196:199],v113 offset:17408\n"
"v_cvt_pk_bf16_f32 v90,v92,v93\n"
"v_mfma_f32_16x16x32_bf16 v[84:87],a[228:231],v[192:195],v[84:87]\n"
"ds_read_b128 v[192:195],v113 offset:16384\n"
"s_waitcnt lgkmcnt(13)\n"
"v_mfma_f32_16x16x32_bf16 v[84:87],a[4:7],v[208:211],v[84:87]\n"
"buffer_load_dwordx4 v112,s[24:27],s8 offen offset:1024 sc1 lds\n"
"v_mfma_f32_16x16x32_bf16 v[84:87],a[12:15],v[212:215],v[84:87]\n"
"v_cvt_pk_bf16_f32 v91,v94,v95\n"
"v_max_u32 v88,1,v88\n"
"v_mfma_f32_16x16x32_bf16 v[84:87],a[20:23],v[216:219],v[84:87]\n"
"v_max_u32 v89,1,v89\n"
"v_max_u32 v90,1,v90\n"
"v_mfma_f32_16x16x32_bf16 v[84:87],a[28:31],v[220:223],v[84:87]\n"
"buffer_load_dwordx4 v112,s[24:27],s8 offen offset:2048 sc1 lds\n"
"v_mfma_f32_16x16x32_bf16 v[80:83],a[24:27],v[220:223],v[80:83]\n"
"ds_read_b128 v[220:223],v113 offset:23552\n"
"v_max_u32 v91,1,v91\n"
"v_mfma_f32_16x16x32_bf16 v[80:83],a[16:19],v[216:219],v[80:83]\n"
"ds_read_b128 v[216:219],v113 offset:22528\n"
"buffer_store_dwordx4 v[88:91],v118,s[24:27],s53 offen sc1\n"
"v_mfma_f32_16x16x32_bf16 v[80:83],a[8:11],v[212:215],v[80:83]\n"
"ds_read_b128 v[212:215],v113 offset:21504\n"
"v_mfma_f32_16x16x32_bf16 v[80:83],a[0:3],v[208:211],v[80:83]\n"
"ds_read_b128 v[208:211],v113 offset:20480\n"
"s_waitcnt lgkmcnt(13)\n"
"v_mfma_f32_16x16x32_bf16 v[80:83],v[32:35],v[224:227],v[80:83]\n"
"v_mfma_f32_16x16x32_bf16 v[80:83],v[40:43],v[228:231],v[80:83]\n"
"buffer_load_dwordx4 v112,s[24:27],s8 offen offset:3072 sc1 lds\n"
"v_mfma_f32_16x16x32_bf16 v[80:83],a[32:35],v[232:235],v[80:83]\n"
"s_waitcnt lgkmcnt(8)\n"
"v_readfirstlane_b32 s22,v103\n"
"v_mfma_f32_16x16x32_bf16 v[80:83],a[40:43],v[236:239],v[80:83]\n"
"v_mfma_f32_16x16x32_bf16 v[84:87],a[44:47],v[236:239],v[84:87]\n"
"ds_read_b128 v[236:239],v113 offset:27648\n"
"s_and_b32 s21,s22,0x38\n"
"v_mfma_f32_16x16x32_bf16 v[84:87],a[36:39],v[232:235],v[84:87]\n"
"ds_read_b128 v[232:235],v113 offset:26624\n"
"v_mfma_f32_16x16x32_bf16 v[84:87],v[44:47],v[228:231],v[84:87]\n"
"ds_read_b128 v[228:231],v113 offset:25600\n"
"v_mfma_f32_16x16x32_bf16 v[84:87],v[36:39],v[224:227],v[84:87]\n"
"ds_read_b128 v[224:227],v113 offset:24576\n"
"s_add_u32 m0,m0,4096\n"
"v_mfma_f32_16x16x32_bf16 v[84:87],a[68:71],v[240:243],v[84:87]\n"
"buffer_load_dwordx4 v117,s[24:27],s8 offen offset:0 sc1 lds\n"
"v_mfma_f32_16x16x32_bf16 v[84:87],a[76:79],v[244:247],v[84:87]\n"
"s_cmp_lg_u64 s[14:15],0\n"
"s_cbranch_scc1 .Lfk_es_lkloopm\n"
".Lfk_er_lkloopm:\n"
"v_mfma_f32_16x16x32_bf16 v[84:87],a[84:87],v[248:251],v[84:87]\n"
"v_mfma_f32_16x16x32_bf16 v[84:87],a[92:95],v[252:255],v[84:87]\n"
"buffer_load_dwordx4 v117,s[24:27],s8 offen offset:1024 sc1 lds\n"
"v_mfma_f32_16x16x32_bf16 v[80:83],a[88:91],v[252:255],v[80:83]\n"
"ds_read_b128 v[252:255],v113 offset:31744\n"
"s_mov_b32 s53,s56\n"
"v_mfma_f32_16x16x32_bf16 v[80:83],a[80:83],v[248:251],v[80:83]\n"
"ds_read_b128 v[248:251],v113 offset:30720\n"
"s_mov_b32 s56,s58\n"
"v_mfma_f32_16x16x32_bf16 v[80:83],a[72:75],v[244:247],v[80:83]\n"
"ds_read_b128 v[244:247],v113 offset:29696\n"
"s_mov_b32 s58,s59\n"
"v_mfma_f32_16x16x32_bf16 v[80:83],a[156:159],v[240:243],v[80:83]\n"
"ds_read_b128 v[240:243],v113 offset:28672\n"
"s_waitcnt lgkmcnt(12)\n"
"v_mfma_f32_16x16x32_bf16 v[88:91],v[128:131],v[192:195],v[80:83]\n"
"s_waitcnt vmcnt(17)\n"
"ds_write_b32 v101,v100\n"
"v_mfma_f32_16x16x32_bf16 v[88:91],v[136:139],v[196:199],v[88:91]\n"
"ds_add_u32 v114,v116\n"
"v_mfma_f32_16x16x32_bf16 v[88:91],v[144:147],v[200:203],v[88:91]\n"
"buffer_load_dwordx4 v117,s[24:27],s8 offen offset:2048 sc1 lds\n"
"v_mfma_f32_16x16x32_bf16 v[88:91],v[152:155],v[204:207],v[88:91]\n"
"s_add_u32 s83,s20,32768\n"
"s_and_b32 s83,s83,0x1ffff\n"
"v_add_u32 v113,s83,v112\n"
"v_mfma_f32_16x16x32_bf16 v[92:95],v[156:159],v[204:207],v[84:87]\n"
"s_cmp_lt_u32 s22,64\n"
"s_cselect_b32 s17,s16,-1\n"
"v_mfma_f32_16x16x32_bf16 v[92:95],v[148:151],v[200:203],v[92:95]\n"
"v_mfma_f32_16x16x32_bf16 v[92:95],v[140:143],v[196:199],v[92:95]\n"
"v_mfma_f32_16x16x32_bf16 v[92:95],v[132:135],v[192:195],v[92:95]\n"
"s_waitcnt lgkmcnt(10)\n"
"v_mfma_f32_16x16x32_bf16 v[92:95],v[4:7],v[208:211],v[92:95]\n"
"buffer_load_dwordx4 v117,s[24:27],s8 offen offset:3072 sc1 lds\n"
"v_mfma_f32_16x16x32_bf16 v[92:95],v[12:15],v[212:215],v[92:95]\n"
"s_lshr_b32 s59,s8,2\n"
"ds_read_b32 v125,v114\n"
"s_lshl_b32 s8,s22,17\n"
"v_mfma_f32_16x16x32_bf16 v[92:95],v[164:167],v[216:219],v[92:95]\n"
"s_add_u32 s8,s8,s31\n"
"v_mfma_f32_16x16x32_bf16 v[92:95],v[172:175],v[220:223],v[92:95]\n"
"v_mfma_f32_16x16x32_bf16 v[88:91],v[168:171],v[220:223],v[88:91]\n"
"s_lshl_b64 exec,1,s89\n"
"buffer_atomic_add v100,off,s[24:27],s10 sc0\n"
"s_mov_b64 exec,-1\n"
"v_mfma_f32_16x16x32_bf16 v[88:91],v[160:163],v[216:219],v[88:91]\n"
"s_waitcnt lgkmcnt(0)\n"
"v_cmp_gt_u32 vcc,s17,v125\n"
"v_mfma_f32_16x16x32_bf16 v[88:91],v[8:11],v[212:215],v[88:91]\n"
"v_mfma_f32_16x16x32_bf16 v[88:91],v[0:3],v[208:211],v[88:91]\n"
"s_cbranch_vccz .Lfk_l4\n"
"s_call_b64 s[94:95],.Lfk_meet4\n"
"s_cmp_lt_u32 s22,64\n"
"s_cbranch_scc1 .Lfk_l4\n"
"ds_read_b128 v[192:195],v113 offset:0\n"
"v_mfma_f32_16x16x32_bf16 v[88:91],v[176:179],v[224:227],v[88:91]\n"
"s_getpc_b64 s[96:97]\n"
".Lfk_rt2:\n"
"s_add_u32 s96,s96,.Lfk_tail-.Lfk_rt2\n"
"s_addc_u32 s97,s97,0\n"
"s_mov_b32 s98,.Lfk_cold-.Lfk_tail\n"
"s_mov_b32 s99,0x20000\n"
"s_lshr_b32 s82,s100,3\n"
"s_lshl2_add_u32 s82,s82,s52\n"
"v_lshrrev_b32 v121,2,v112\n"
"s_lshl_b32 s82,s82,8\n"
"s_lshr_b32 s62,s60,4\n"
"v_mfma_f32_16x16x32_bf16 v[88:91],v[184:187],v[228:231],v[88:91]\n"
"buffer_load_dword v120,v121,s[96:99],s82 offen\n"
"ds_read_b128 v[196:199],v113 offset:1024\n"
"s_lshl_b32 s62,s62,15\n"
"v_mfma_f32_16x16x32_bf16 v[88:91],a[48:51],v[232:235],v[88:91]\n"
"s_and_b32 s91,s60,15\n"
"ds_read_b128 v[200:203],v113 offset:2048\n"
"v_lshlrev_b32 v102,2,v112\n"
"v_mfma_f32_16x16x32_bf16 v[88:91],a[56:59],v[236:239],v[88:91]\n"
"s_lshl_b32 s91,s91,6\n"
"ds_read_b128 v[204:207],v113 offset:3072\n"
"v_and_b32 v102,0x0,v102\n"
"v_xor_b32 v102,s91,v102\n"
"v_mfma_f32_16x16x32_bf16 v[92:95],a[60:63],v[236:239],v[92:95]\n"
"s_add_u32 s62,s62,71303168\n"
"ds_read_b128 v[208:211],v113 offset:4096\n"
"s_cmp_eq_u32 s42,0\n"
"v_mfma_f32_16x16x32_bf16 v[92:95],a[52:55],v[232:235],v[92:95]\n"
"s_cselect_b32 s72,0x3ff80,s23\n"
"ds_read_b128 v[212:215],v113 offset:5120\n"
"s_add_u32 s16,s16,4\n"
"v_mfma_f32_16x16x32_bf16 v[92:95],v[188:191],v[228:231],v[92:95]\n"
"s_add_u32 s91,s61,2097152\n"
"ds_read_b128 v[216:219],v113 offset:6144\n"
"v_lshrrev_b32 v126,6,v112\n"
"v_and_or_b32 v103,v112,48,v102\n"
"v_mfma_f32_16x16x32_bf16 v[92:95],v[180:183],v[224:227],v[92:95]\n"
"v_lshl_add_u32 v126,v126,10,v103\n"
"v_add_u32 v117,16384,v126\n"
"v_mfma_f32_16x16x32_bf16 v[92:95],v[52:55],v[240:243],v[92:95]\n"
"ds_read_b128 v[220:223],v113 offset:7168\n"
"ds_read_b128 v[236:239],v113 offset:11264\n"
"v_mfma_f32_16x16x32_bf16 v[92:95],v[60:63],v[244:247],v[92:95]\n"
"ds_read_b128 v[232:235],v113 offset:10240\n"
"v_mfma_f32_16x16x32_bf16 v[92:95],v[20:23],v[248:251],v[92:95]\n"
"ds_read_b128 v[228:231],v113 offset:9216\n"
"v_mfma_f32_16x16x32_bf16 v[92:95],v[28:31],v[252:255],v[92:95]\n"
"ds_read_b128 v[224:227],v113 offset:8192\n"
"v_mfma_f32_16x16x32_bf16 v[88:91],v[24:27],v[252:255],v[88:91]\n"
"ds_read_b128 v[252:255],v113 offset:15360\n"
"v_mfma_f32_16x16x32_bf16 v[88:91],v[16:19],v[248:251],v[88:91]\n"
"ds_read_b128 v[248:251],v113 offset:14336\n"
"v_mfma_f32_16x16x32_bf16 v[88:91],v[56:59],v[244:247],v[88:91]\n"
"ds_read_b128 v[244:247],v113 offset:13312\n"
"v_mfma_f32_16x16x32_bf16 v[88:91],v[48:51],v[240:243],v[88:91]\n"
"ds_read_b128 v[240:243],v113 offset:12288\n"
"s_waitcnt lgkmcnt(12)\n"
"v_mfma_f32_16x16x32_bf16 v[80:83],a[224:227],v[192:195],0\n"
"buffer_load_dwordx4 v[72:75],v112,s[44:47],0 offen offset:0\n"
"v_mfma_f32_16x16x32_bf16 v[80:83],a[232:235],v[196:199],v[80:83]\n"
"buffer_load_dwordx4 v[76:79],v112,s[44:47],0 offen offset:1024\n"
"v_mfma_f32_16x16x32_bf16 v[80:83],a[240:243],v[200:203],v[80:83]\n"
"s_nop 0\n"
"v_cvt_pk_bf16_f32 v88,v88,v89\n"
"v_mfma_f32_16x16x32_bf16 v[80:83],a[248:251],v[204:207],v[80:83]\n"
"v_cvt_pk_bf16_f32 v89,v90,v91\n"
"v_mfma_f32_16x16x32_bf16 v[84:87],a[252:255],v[204:207],0\n"
"ds_read_b128 v[204:207],v113 offset:19456\n"
"s_add_u32 s20,s20,32768\n"
"v_mfma_f32_16x16x32_bf16 v[84:87],a[244:247],v[200:203],v[84:87]\n"
"ds_read_b128 v[200:203],v113 offset:18432\n"
"s_and_b32 s20,s20,0x1ffff\n"
"v_mfma_f32_16x16x32_bf16 v[84:87],a[236:239],v[196:199],v[84:87]\n"
"ds_read_b128 v[196:199],v113 offset:17408\n"
"v_cvt_pk_bf16_f32 v90,v92,v93\n"
"v_mfma_f32_16x16x32_bf16 v[84:87],a[228:231],v[192:195],v[84:87]\n"
"s_mov_b32 s91,s62\n"
"ds_read_b128 v[192:195],v113 offset:16384\n"
"s_waitcnt lgkmcnt(12)\n"
"v_mfma_f32_16x16x32_bf16 v[84:87],a[4:7],v[208:211],v[84:87]\n"
"v_cvt_pk_bf16_f32 v91,v94,v95\n"
"v_max_u32 v88,1,v88\n"
"v_mfma_f32_16x16x32_bf16 v[84:87],a[12:15],v[212:215],v[84:87]\n"
"v_max_u32 v89,1,v89\n"
"v_max_u32 v90,1,v90\n"
"v_max_u32 v91,1,v91\n"
"v_mfma_f32_16x16x32_bf16 v[84:87],a[20:23],v[216:219],v[84:87]\n"
"buffer_load_dwordx4 a[112:115],v126,s[24:27],s91 offen sc1\n"
"v_mfma_f32_16x16x32_bf16 v[84:87],a[28:31],v[220:223],v[84:87]\n"
"buffer_store_dwordx4 v[88:91],v118,s[24:27],s53 offen sc1\n"
"v_mfma_f32_16x16x32_bf16 v[80:83],a[24:27],v[220:223],v[80:83]\n"
"ds_read_b128 v[220:223],v113 offset:23552\n"
"buffer_load_dwordx4 a[116:119],v117,s[24:27],s91 offen sc1\n"
"v_mfma_f32_16x16x32_bf16 v[80:83],a[16:19],v[216:219],v[80:83]\n"
"ds_read_b128 v[216:219],v113 offset:22528\n"
"v_mfma_f32_16x16x32_bf16 v[80:83],a[8:11],v[212:215],v[80:83]\n"
"ds_read_b128 v[212:215],v113 offset:21504\n"
"v_mfma_f32_16x16x32_bf16 v[80:83],a[0:3],v[208:211],v[80:83]\n"
"s_add_u32 s91,s91,4194304\n"
"ds_read_b128 v[208:211],v113 offset:20480\n"
"s_waitcnt lgkmcnt(12)\n"
"v_mfma_f32_16x16x32_bf16 v[80:83],v[32:35],v[224:227],v[80:83]\n"
"v_mfma_f32_16x16x32_bf16 v[80:83],v[40:43],v[228:231],v[80:83]\n"
"v_mfma_f32_16x16x32_bf16 v[80:83],a[32:35],v[232:235],v[80:83]\n"
"buffer_load_dwordx4 a[120:123],v126,s[24:27],s91 offen sc1\n"
"v_mfma_f32_16x16x32_bf16 v[80:83],a[40:43],v[236:239],v[80:83]\n"
"v_mfma_f32_16x16x32_bf16 v[84:87],a[44:47],v[236:239],v[84:87]\n"
"ds_read_b128 v[236:239],v113 offset:27648\n"
"v_mfma_f32_16x16x32_bf16 v[84:87],a[36:39],v[232:235],v[84:87]\n"
"ds_read_b128 v[232:235],v113 offset:26624\n"
"buffer_load_dwordx4 a[124:127],v117,s[24:27],s91 offen sc1\n"
"v_mfma_f32_16x16x32_bf16 v[84:87],v[44:47],v[228:231],v[84:87]\n"
"ds_read_b128 v[228:231],v113 offset:25600\n"
"v_mfma_f32_16x16x32_bf16 v[84:87],v[36:39],v[224:227],v[84:87]\n"
"ds_read_b128 v[224:227],v113 offset:24576\n"
"s_waitcnt lgkmcnt(12)\n"
"v_mfma_f32_16x16x32_bf16 v[84:87],a[68:71],v[240:243],v[84:87]\n"
"s_add_u32 s91,s91,4194304\n"
"v_mfma_f32_16x16x32_bf16 v[84:87],a[76:79],v[244:247],v[84:87]\n"
"v_mfma_f32_16x16x32_bf16 v[84:87],a[84:87],v[248:251],v[84:87]\n"
"v_mfma_f32_16x16x32_bf16 v[84:87],a[92:95],v[252:255],v[84:87]\n"
"buffer_load_dwordx4 a[128:131],v126,s[24:27],s91 offen sc1\n"
"v_mfma_f32_16x16x32_bf16 v[80:83],a[88:91],v[252:255],v[80:83]\n"
"ds_read_b128 v[252:255],v113 offset:31744\n"
"s_mov_b32 s53,s56\n"
"v_mfma_f32_16x16x32_bf16 v[80:83],a[80:83],v[248:251],v[80:83]\n"
"ds_read_b128 v[248:251],v113 offset:30720\n"
"s_mov_b32 s56,s58\n"
"v_mfma_f32_16x16x32_bf16 v[80:83],a[72:75],v[244:247],v[80:83]\n"
"s_mov_b32 s58,s59\n"
"ds_read_b128 v[244:247],v113 offset:29696\n"
"buffer_load_dwordx4 a[132:135],v117,s[24:27],s91 offen sc1\n"
"v_mfma_f32_16x16x32_bf16 v[80:83],a[156:159],v[240:243],v[80:83]\n"
"ds_read_b128 v[240:243],v113 offset:28672\n"
"s_waitcnt lgkmcnt(12)\n"
"v_mfma_f32_16x16x32_bf16 v[88:91],v[128:131],v[192:195],v[80:83]\n"
"s_waitcnt vmcnt(21)\n"
"ds_add_u32 v114,v116\n"
"v_mfma_f32_16x16x32_bf16 v[88:91],v[136:139],v[196:199],v[88:91]\n"
"s_add_u32 s91,s91,4194304\n"
"v_mfma_f32_16x16x32_bf16 v[88:91],v[144:147],v[200:203],v[88:91]\n"
"v_mfma_f32_16x16x32_bf16 v[88:91],v[152:155],v[204:207],v[88:91]\n"
"s_add_u32 s83,s20,32768\n"
"s_and_b32 s83,s83,0x1ffff\n"
"v_add_u32 v113,s83,v112\n"
"v_mfma_f32_16x16x32_bf16 v[92:95],v[156:159],v[204:207],v[84:87]\n"
"buffer_load_dwordx4 a[136:139],v126,s[24:27],s91 offen sc1\n"
"v_mfma_f32_16x16x32_bf16 v[92:95],v[148:151],v[200:203],v[92:95]\n"
"v_mfma_f32_16x16x32_bf16 v[92:95],v[140:143],v[196:199],v[92:95]\n"
"buffer_load_dwordx4 a[140:143],v117,s[24:27],s91 offen sc1\n"
"v_mfma_f32_16x16x32_bf16 v[92:95],v[132:135],v[192:195],v[92:95]\n"
"s_waitcnt lgkmcnt(9)\n"
"v_mfma_f32_16x16x32_bf16 v[92:95],v[4:7],v[208:211],v[92:95]\n"
"v_mfma_f32_16x16x32_bf16 v[92:95],v[12:15],v[212:215],v[92:95]\n"
"ds_read_b32 v125,v114\n"
"v_mfma_f32_16x16x32_bf16 v[92:95],v[164:167],v[216:219],v[92:95]\n"
"v_mfma_f32_16x16x32_bf16 v[92:95],v[172:175],v[220:223],v[92:95]\n"
"v_mfma_f32_16x16x32_bf16 v[88:91],v[168:171],v[220:223],v[88:91]\n"
"v_mfma_f32_16x16x32_bf16 v[88:91],v[160:163],v[216:219],v[88:91]\n"
"s_waitcnt lgkmcnt(0)\n"
"v_cmp_gt_u32 vcc,s16,v125\n"
"v_mfma_f32_16x16x32_bf16 v[88:91],v[8:11],v[212:215],v[88:91]\n"
"v_mfma_f32_16x16x32_bf16 v[88:91],v[0:3],v[208:211],v[88:91]\n"
"s_cbranch_vccnz .Lfk_es_me3\n"
".Lfk_er_me3:\n"
"v_mfma_f32_16x16x32_bf16 v[88:91],v[176:179],v[224:227],v[88:91]\n"
"ds_read_b128 v[192:195],v113 offset:0\n"
"v_lshrrev_b32 v101,4,v112\n"
"v_mfma_f32_16x16x32_bf16 v[88:91],v[184:187],v[228:231],v[88:91]\n"
"ds_read_b128 v[196:199],v113 offset:1024\n"
"v_and_b32 v102,15,v101\n"
"v_mfma_f32_16x16x32_bf16 v[88:91],a[48:51],v[232:235],v[88:91]\n"
"ds_read_b128 v[200:203],v113 offset:2048\n"
"v_lshrrev_b32 v101,4,v101\n"
"v_mfma_f32_16x16x32_bf16 v[88:91],a[56:59],v[236:239],v[88:91]\n"
"ds_read_b128 v[204:207],v113 offset:3072\n"
"v_lshlrev_b32 v101,2,v101\n"
"v_mfma_f32_16x16x32_bf16 v[92:95],a[60:63],v[236:239],v[92:95]\n"
"ds_read_b128 v[208:211],v113 offset:4096\n"
"v_sub_u32 v101,v102,v101\n"
"v_mfma_f32_16x16x32_bf16 v[92:95],a[52:55],v[232:235],v[92:95]\n"
"ds_read_b128 v[212:215],v113 offset:5120\n"
"s_add_u32 s16,s16,4\n"
"v_mfma_f32_16x16x32_bf16 v[92:95],v[188:191],v[228:231],v[92:95]\n"
"ds_read_b128 v[216:219],v113 offset:6144\n"
"v_mov_b32 v64,0\n"
"v_mfma_f32_16x16x32_bf16 v[92:95],v[180:183],v[224:227],v[92:95]\n"
"ds_read_b128 v[220:223],v113 offset:7168\n"
"v_mov_b32 v65,0\n"
"v_mfma_f32_16x16x32_bf16 v[92:95],v[52:55],v[240:243],v[92:95]\n"
"ds_read_b128 v[236:239],v113 offset:11264\n"
"v_mov_b32 v68,0\n"
"v_mfma_f32_16x16x32_bf16 v[92:95],v[60:63],v[244:247],v[92:95]\n"
"ds_read_b128 v[232:235],v113 offset:10240\n"
"v_mov_b32 v69,0\n"
"v_mfma_f32_16x16x32_bf16 v[92:95],v[20:23],v[248:251],v[92:95]\n"
"ds_read_b128 v[228:231],v113 offset:9216\n"
"v_mov_b32 v102,0x3f80\n"
"v_mfma_f32_16x16x32_bf16 v[92:95],v[28:31],v[252:255],v[92:95]\n"
"ds_read_b128 v[224:227],v113 offset:8192\n"
"v_mov_b32 v103,0x3f800000\n"
"v_cmp_eq_u32 vcc,0,v101\n"
"v_mfma_f32_16x16x32_bf16 v[88:91],v[24:27],v[252:255],v[88:91]\n"
"ds_read_b128 v[252:255],v113 offset:15360\n"
"v_cndmask_b32 v66,0,v102,vcc\n"
"v_cmp_eq_u32 vcc,1,v101\n"
"v_mfma_f32_16x16x32_bf16 v[88:91],v[16:19],v[248:251],v[88:91]\n"
"ds_read_b128 v[248:251],v113 offset:14336\n"
"v_cndmask_b32 v66,v66,v103,vcc\n"
"v_cmp_eq_u32 vcc,2,v101\n"
"v_mfma_f32_16x16x32_bf16 v[88:91],v[56:59],v[244:247],v[88:91]\n"
"ds_read_b128 v[244:247],v113 offset:13312\n"
"v_cndmask_b32 v67,0,v102,vcc\n"
"v_mfma_f32_16x16x32_bf16 v[88:91],v[48:51],v[240:243],v[88:91]\n"
"ds_read_b128 v[240:243],v113 offset:12288\n"
"s_waitcnt lgkmcnt(12)\n"
"v_cmp_eq_u32 vcc,3,v101\n"
"v_mfma_f32_16x16x32_bf16 v[80:83],a[224:227],v[192:195],0\n"
"s_nop 0\n"
"v_cndmask_b32 v67,v67,v103,vcc\n"
"v_mfma_f32_16x16x32_bf16 v[80:83],a[232:235],v[196:199],v[80:83]\n"
"v_mfma_f32_16x16x32_bf16 v[80:83],a[240:243],v[200:203],v[80:83]\n"
"v_mfma_f32_16x16x32_bf16 v[80:83],a[248:251],v[204:207],v[80:83]\n"
"v_cvt_pk_bf16_f32 v88,v88,v89\n"
"v_cvt_pk_bf16_f32 v89,v90,v91\n"
"v_mfma_f32_16x16x32_bf16 v[84:87],a[252:255],v[204:207],0\n"
"ds_read_b128 v[204:207],v113 offset:19456\n"
"s_add_u32 s20,s20,32768\n"
"v_mfma_f32_16x16x32_bf16 v[84:87],a[244:247],v[200:203],v[84:87]\n"
"ds_read_b128 v[200:203],v113 offset:18432\n"
"s_and_b32 s20,s20,0x1ffff\n"
"v_mfma_f32_16x16x32_bf16 v[84:87],a[236:239],v[196:199],v[84:87]\n"
"ds_read_b128 v[196:199],v113 offset:17408\n"
"v_cvt_pk_bf16_f32 v90,v92,v93\n"
"v_mfma_f32_16x16x32_bf16 v[84:87],a[228:231],v[192:195],v[84:87]\n"
"ds_read_b128 v[192:195],v113 offset:16384\n"
"s_waitcnt lgkmcnt(12)\n"
"v_mfma_f32_16x16x32_bf16 v[84:87],a[4:7],v[208:211],v[84:87]\n"
"v_cvt_pk_bf16_f32 v91,v94,v95\n"
"v_max_u32 v88,1,v88\n"
"v_mfma_f32_16x16x32_bf16 v[84:87],a[12:15],v[212:215],v[84:87]\n"
"v_max_u32 v89,1,v89\n"
"v_max_u32 v90,1,v90\n"
"v_max_u32 v91,1,v91\n"
"v_mfma_f32_16x16x32_bf16 v[84:87],a[20:23],v[216:219],v[84:87]\n"
"buffer_store_dwordx4 v[88:91],v118,s[24:27],s53 offen sc1\n"
"v_mfma_f32_16x16x32_bf16 v[84:87],a[28:31],v[220:223],v[84:87]\n"
"v_mfma_f32_16x16x32_bf16 v[80:83],a[24:27],v[220:223],v[80:83]\n"
"ds_read_b128 v[220:223],v113 offset:23552\n"
"v_mfma_f32_16x16x32_bf16 v[80:83],a[16:19],v[216:219],v[80:83]\n"
"ds_read_b128 v[216:219],v113 offset:22528\n"
"v_mfma_f32_16x16x32_bf16 v[80:83],a[8:11],v[212:215],v[80:83]\n"
"ds_read_b128 v[212:215],v113 offset:21504\n"
"v_mfma_f32_16x16x32_bf16 v[80:83],a[0:3],v[208:211],v[80:83]\n"
"ds_read_b128 v[208:211],v113 offset:20480\n"
"s_waitcnt lgkmcnt(12)\n"
"v_mfma_f32_16x16x32_bf16 v[80:83],v[32:35],v[224:227],v[80:83]\n"
"v_mfma_f32_16x16x32_bf16 v[80:83],v[40:43],v[228:231],v[80:83]\n"
"v_mfma_f32_16x16x32_bf16 v[80:83],a[32:35],v[232:235],v[80:83]\n"
"v_mfma_f32_16x16x32_bf16 v[80:83],a[40:43],v[236:239],v[80:83]\n"
"v_mfma_f32_16x16x32_bf16 v[84:87],a[44:47],v[236:239],v[84:87]\n"
"ds_read_b128 v[236:239],v113 offset:27648\n"
"v_mfma_f32_16x16x32_bf16 v[84:87],a[36:39],v[232:235],v[84:87]\n"
"ds_read_b128 v[232:235],v113 offset:26624\n"
"v_mfma_f32_16x16x32_bf16 v[84:87],v[44:47],v[228:231],v[84:87]\n"
"ds_read_b128 v[228:231],v113 offset:25600\n"
"v_mfma_f32_16x16x32_bf16 v[84:87],v[36:39],v[224:227],v[84:87]\n"
"ds_read_b128 v[224:227],v113 offset:24576\n"
"s_waitcnt lgkmcnt(12)\n"
"v_mfma_f32_16x16x32_bf16 v[84:87],a[68:71],v[240:243],v[84:87]\n"
"v_mfma_f32_16x16x32_bf16 v[84:87],a[76:79],v[244:247],v[84:87]\n"
"v_mfma_f32_16x16x32_bf16 v[84:87],a[84:87],v[248:251],v[84:87]\n"
"v_mfma_f32_16x16x32_bf16 v[84:87],a[92:95],v[252:255],v[84:87]\n"
"v_mfma_f32_16x16x32_bf16 v[80:83],a[88:91],v[252:255],v[80:83]\n"
"ds_read_b128 v[252:255],v113 offset:31744\n"
"s_mov_b32 s53,s56\n"
"v_mfma_f32_16x16x32_bf16 v[80:83],a[80:83],v[248:251],v[80:83]\n"
"ds_read_b128 v[248:251],v113 offset:30720\n"
"s_mov_b32 s56,s58\n"
"v_mfma_f32_16x16x32_bf16 v[80:83],a[72:75],v[244:247],v[80:83]\n"
"ds_read_b128 v[244:247],v113 offset:29696\n"
"s_mov_b32 s58,s59\n"
"v_mfma_f32_16x16x32_bf16 v[80:83],a[156:159],v[240:243],v[80:83]\n"
"ds_read_b128 v[240:243],v113 offset:28672\n"
"s_waitcnt lgkmcnt(12)\n"
"v_mfma_f32_16x16x32_bf16 v[88:91],v[128:131],v[192:195],v[80:83]\n"
"s_waitcnt vmcnt(14)\n"
"ds_add_u32 v114,v116\n"
"v_mfma_f32_16x16x32_bf16 v[88:91],v[136:139],v[196:199],v[88:91]\n"
"v_mfma_f32_16x16x32_bf16 v[88:91],v[144:147],v[200:203],v[88:91]\n"
"v_mfma_f32_16x16x32_bf16 v[88:91],v[152:155],v[204:207],v[88:91]\n"
"s_add_u32 s83,s20,32768\n"
"s_and_b32 s83,s83,0x1ffff\n"
"v_add_u32 v113,s83,v112\n"
"v_mfma_f32_16x16x32_bf16 v[92:95],v[156:159],v[204:207],v[84:87]\n"
"v_mfma_f32_16x16x32_bf16 v[92:95],v[148:151],v[200:203],v[92:95]\n"
"v_mfma_f32_16x16x32_bf16 v[92:95],v[140:143],v[196:199],v[92:95]\n"
"v_mfma_f32_16x16x32_bf16 v[92:95],v[132:135],v[192:195],v[92:95]\n"
"s_waitcnt lgkmcnt(9)\n"
"v_mfma_f32_16x16x32_bf16 v[92:95],v[4:7],v[208:211],v[92:95]\n"
"v_mfma_f32_16x16x32_bf16 v[92:95],v[12:15],v[212:215],v[92:95]\n"
"ds_read_b32 v125,v114\n"
"v_mfma_f32_16x16x32_bf16 v[92:95],v[164:167],v[216:219],v[92:95]\n"
"v_mfma_f32_16x16x32_bf16 v[92:95],v[172:175],v[220:223],v[92:95]\n"
"v_mfma_f32_16x16x32_bf16 v[88:91],v[168:171],v[220:223],v[88:91]\n"
"v_mfma_f32_16x16x32_bf16 v[88:91],v[160:163],v[216:219],v[88:91]\n"
"s_waitcnt lgkmcnt(0)\n"
"v_cmp_gt_u32 vcc,s16,v125\n"
"v_mfma_f32_16x16x32_bf16 v[88:91],v[8:11],v[212:215],v[88:91]\n"
"v_mfma_f32_16x16x32_bf16 v[88:91],v[0:3],v[208:211],v[88:91]\n"
"s_cbranch_vccnz .Lfk_es_me2\n"
".Lfk_er_me2:\n"
"ds_read_b128 v[192:195],v113 offset:0\n"
"v_mfma_f32_16x16x32_bf16 v[88:91],v[176:179],v[224:227],v[88:91]\n"
"v_lshlrev_b32 v104,6,v112\n"
"v_and_b32 v105,0x8000,v104\n"
"v_mfma_f32_16x16x32_bf16 v[88:91],v[184:187],v[228:231],v[88:91]\n"
"ds_read_b128 v[196:199],v113 offset:1024\n"
"v_and_b32 v104,0x7c00,v104\n"
"v_lshl_add_u32 v104,v105,7,v104\n"
"v_mfma_f32_16x16x32_bf16 v[88:91],a[48:51],v[232:235],v[88:91]\n"
"s_and_b32 s82,s60,15\n"
"ds_read_b128 v[200:203],v113 offset:2048\n"
"s_lshl_b32 s82,s82,6\n"
"v_mfma_f32_16x16x32_bf16 v[88:91],a[56:59],v[236:239],v[88:91]\n"
"ds_read_b128 v[204:207],v113 offset:3072\n"
"v_add_u32 v104,s82,v104\n"
"v_mfma_f32_16x16x32_bf16 v[92:95],a[60:63],v[236:239],v[92:95]\n"
"s_and_b32 s82,s60,1\n"
"ds_read_b128 v[208:211],v113 offset:4096\n"
"s_lshl_b32 s82,s82,23\n"
"v_mfma_f32_16x16x32_bf16 v[92:95],a[52:55],v[232:235],v[92:95]\n"
"s_add_u32 s82,s82,s62\n"
"s_add_u32 s82,s82,2097152\n"
"s_cmp_lt_u32 s52,2\n"
"v_mfma_f32_16x16x32_bf16 v[92:95],v[188:191],v[228:231],v[92:95]\n"
"s_cselect_b32 s82,s82,2146435072\n"
"ds_read_b128 v[212:215],v113 offset:5120\n"
"ds_read_b128 v[216:219],v113 offset:6144\n"
"v_mfma_f32_16x16x32_bf16 v[92:95],v[180:183],v[224:227],v[92:95]\n"
"ds_read_b128 v[220:223],v113 offset:7168\n"
"v_mfma_f32_16x16x32_bf16 v[92:95],v[52:55],v[240:243],v[92:95]\n"
"ds_read_b128 v[236:239],v113 offset:11264\n"
"v_mfma_f32_16x16x32_bf16 v[92:95],v[60:63],v[244:247],v[92:95]\n"
"ds_read_b128 v[232:235],v113 offset:10240\n"
"v_mfma_f32_16x16x32_bf16 v[92:95],v[20:23],v[248:251],v[92:95]\n"
"ds_read_b128 v[228:231],v113 offset:9216\n"
"v_mfma_f32_16x16x32_bf16 v[92:95],v[28:31],v[252:255],v[92:95]\n"
"ds_read_b128 v[224:227],v113 offset:8192\n"
"v_mfma_f32_16x16x32_bf16 v[88:91],v[24:27],v[252:255],v[88:91]\n"
"ds_read_b128 v[252:255],v113 offset:15360\n"
"v_mfma_f32_16x16x32_bf16 v[88:91],v[16:19],v[248:251],v[88:91]\n"
"ds_read_b128 v[248:251],v113 offset:14336\n"
"v_mfma_f32_16x16x32_bf16 v[88:91],v[56:59],v[244:247],v[88:91]\n"
"ds_read_b128 v[244:247],v113 offset:13312\n"
"v_mfma_f32_16x16x32_bf16 v[88:91],v[48:51],v[240:243],v[88:91]\n"
"ds_read_b128 v[240:243],v113 offset:12288\n"
"s_waitcnt lgkmcnt(12)\n"
"v_mfma_f32_16x16x32_bf16 v[80:83],a[224:227],v[192:195],0\n"
"buffer_load_dword a148,v104,s[24:27],s82 offen sc1\n"
"v_mfma_f32_16x16x32_bf16 v[80:83],a[232:235],v[196:199],v[80:83]\n"
"v_mfma_f32_16x16x32_bf16 v[80:83],a[240:243],v[200:203],v[80:83]\n"
"v_mfma_f32_16x16x32_bf16 v[80:83],a[248:251],v[204:207],v[80:83]\n"
"s_nop 0\n"
"v_cvt_pk_bf16_f32 v88,v88,v89\n"
"v_cvt_pk_bf16_f32 v89,v90,v91\n"
"v_mfma_f32_16x16x32_bf16 v[84:87],a[252:255],v[204:207],0\n"
"ds_read_b128 v[204:207],v113 offset:19456\n"
"v_cvt_pk_bf16_f32 v90,v92,v93\n"
"v_mfma_f32_16x16x32_bf16 v[84:87],a[244:247],v[200:203],v[84:87]\n"
"ds_read_b128 v[200:203],v113 offset:18432\n"
"v_cvt_pk_bf16_f32 v91,v94,v95\n"
"v_mfma_f32_16x16x32_bf16 v[84:87],a[236:239],v[196:199],v[84:87]\n"
"ds_read_b128 v[196:199],v113 offset:17408\n"
"v_max_u32 v88,1,v88\n"
"v_mfma_f32_16x16x32_bf16 v[84:87],a[228:231],v[192:195],v[84:87]\n"
"ds_read_b128 v[192:195],v113 offset:16384\n"
"s_waitcnt lgkmcnt(12)\n"
"v_mfma_f32_16x16x32_bf16 v[84:87],a[4:7],v[208:211],v[84:87]\n"
"v_max_u32 v89,1,v89\n"
"v_max_u32 v90,1,v90\n"
"v_max_u32 v91,1,v91\n"
"v_mfma_f32_16x16x32_bf16 v[84:87],a[12:15],v[212:215],v[84:87]\n"
"buffer_store_dwordx4 v[88:91],v118,s[24:27],s53 offen sc1\n"
"v_mfma_f32_16x16x32_bf16 v[84:87],a[20:23],v[216:219],v[84:87]\n"
"v_mfma_f32_16x16x32_bf16 v[84:87],a[28:31],v[220:223],v[84:87]\n"
"v_mfma_f32_16x16x32_bf16 v[80:83],a[24:27],v[220:223],v[80:83]\n"
"ds_read_b128 v[220:223],v113 offset:23552\n"
"v_mfma_f32_16x16x32_bf16 v[80:83],a[16:19],v[216:219],v[80:83]\n"
"ds_read_b128 v[216:219],v113 offset:22528\n"
"v_mfma_f32_16x16x32_bf16 v[80:83],a[8:11],v[212:215],v[80:83]\n"
"ds_read_b128 v[212:215],v113 offset:21504\n"
"v_mfma_f32_16x16x32_bf16 v[80:83],a[0:3],v[208:211],v[80:83]\n"
"ds_read_b128 v[208:211],v113 offset:20480\n"
"s_waitcnt lgkmcnt(12)\n"
"v_mfma_f32_16x16x32_bf16 v[80:83],v[32:35],v[224:227],v[80:83]\n"
"v_mfma_f32_16x16x32_bf16 v[80:83],v[40:43],v[228:231],v[80:83]\n"
"v_mfma_f32_16x16x32_bf16 v[80:83],a[32:35],v[232:235],v[80:83]\n"
"v_mfma_f32_16x16x32_bf16 v[80:83],a[40:43],v[236:239],v[80:83]\n"
"v_mfma_f32_16x16x32_bf16 v[84:87],a[44:47],v[236:239],v[84:87]\n"
"ds_read_b128 v[236:239],v113 offset:27648\n"
"v_mfma_f32_16x16x32_bf16 v[84:87],a[36:39],v[232:235],v[84:87]\n"
"ds_read_b128 v[232:235],v113 offset:26624\n"
"v_mfma_f32_16x16x32_bf16 v[84:87],v[44:47],v[228:231],v[84:87]\n"
"ds_read_b128 v[228:231],v113 offset:25600\n"
"v_mfma_f32_16x16x32_bf16 v[84:87],v[36:39],v[224:227],v[84:87]\n"
"ds_read_b128 v[224:227],v113 offset:24576\n"
"s_waitcnt lgkmcnt(12)\n"
"v_mfma_f32_16x16x32_bf16 v[84:87],a[68:71],v[240:243],v[84:87]\n"
"v_mfma_f32_16x16x32_bf16 v[84:87],a[76:79],v[244:247],v[84:87]\n"
"v_mfma_f32_16x16x32_bf16 v[84:87],a[84:87],v[248:251],v[84:87]\n"
"v_mfma_f32_16x16x32_bf16 v[84:87],a[92:95],v[252:255],v[84:87]\n"
"v_mfma_f32_16x16x32_bf16 v[80:83],a[88:91],v[252:255],v[80:83]\n"
"ds_read_b128 v[252:255],v113 offset:31744\n"
"s_mov_b32 s53,s56\n"
"v_mfma_f32_16x16x32_bf16 v[80:83],a[80:83],v[248:251],v[80:83]\n"
"ds_read_b128 v[248:251],v113 offset:30720\n"
"s_mov_b32 s56,s58\n"
"v_mfma_f32_16x16x32_bf16 v[80:83],a[72:75],v[244:247],v[80:83]\n"
"ds_read_b128 v[244:247],v113 offset:29696\n"
"s_mov_b32 s58,s59\n"
"v_mfma_f32_16x16x32_bf16 v[80:83],a[156:159],v[240:243],v[80:83]\n"
"ds_read_b128 v[240:243],v113 offset:28672\n"
"s_waitcnt lgkmcnt(12)\n"
"v_mfma_f32_16x16x32_bf16 v[88:91],v[128:131],v[192:195],v[80:83]\n"
"v_mfma_f32_16x16x32_bf16 v[88:91],v[136:139],v[196:199],v[88:91]\n"
"v_mfma_f32_16x16x32_bf16 v[88:91],v[144:147],v[200:203],v[88:91]\n"
"v_mfma_f32_16x16x32_bf16 v[88:91],v[152:155],v[204:207],v[88:91]\n"
"v_mfma_f32_16x16x32_bf16 v[92:95],v[156:159],v[204:207],v[84:87]\n"
"v_mfma_f32_16x16x32_bf16 v[92:95],v[148:151],v[200:203],v[92:95]\n"
"v_mfma_f32_16x16x32_bf16 v[92:95],v[140:143],v[196:199],v[92:95]\n"
"v_mfma_f32_16x16x32_bf16 v[92:95],v[132:135],v[192:195],v[92:95]\n"
"s_waitcnt lgkmcnt(8)\n"
"v_mfma_f32_16x16x32_bf16 v[92:95],v[4:7],v[208:211],v[92:95]\n"
"v_mfma_f32_16x16x32_bf16 v[92:95],v[12:15],v[212:215],v[92:95]\n"
"v_mfma_f32_16x16x32_bf16 v[92:95],v[164:167],v[216:219],v[92:95]\n"
"v_mfma_f32_16x16x32_bf16 v[92:95],v[172:175],v[220:223],v[92:95]\n"
"v_mfma_f32_16x16x32_bf16 v[88:91],v[168:171],v[220:223],v[88:91]\n"
"s_mov_b32 s91,s62\n"
"v_mfma_f32_16x16x32_bf16 v[88:91],v[160:163],v[216:219],v[88:91]\n"
"v_mfma_f32_16x16x32_bf16 v[88:91],v[8:11],v[212:215],v[88:91]\n"
"v_mfma_f32_16x16x32_bf16 v[88:91],v[0:3],v[208:211],v[88:91]\n"
"s_waitcnt lgkmcnt(0)\n"
"s_waitcnt vmcnt(3)\n"
"v_mfma_f32_16x16x32_bf16 v[88:91],v[176:179],v[224:227],v[88:91]\n"
"v_accvgpr_read_b32 v128,a112\n"
"v_accvgpr_read_b32 v129,a113\n"
"v_mfma_f32_16x16x32_bf16 v[88:91],v[184:187],v[228:231],v[88:91]\n"
"v_accvgpr_read_b32 v130,a114\n"
"v_accvgpr_read_b32 v131,a115\n"
"v_mfma_f32_16x16x32_bf16 v[88:91],a[48:51],v[232:235],v[88:91]\n"
"v_accvgpr_read_b32 v132,a116\n"
"v_accvgpr_read_b32 v133,a117\n"
"v_mfma_f32_16x16x32_bf16 v[88:91],a[56:59],v[236:239],v[88:91]\n"
"v_accvgpr_read_b32 v134,a118\n"
"v_accvgpr_read_b32 v135,a119\n"
"v_mfma_f32_16x16x32_bf16 v[92:95],a[60:63],v[236:239],v[92:95]\n"
"v_accvgpr_read_b32 v136,a120\n"
"v_accvgpr_read_b32 v137,a121\n"
"v_mfma_f32_16x16x32_bf16 v[92:95],a[52:55],v[232:235],v[92:95]\n"
"v_accvgpr_read_b32 v138,a122\n"
"v_accvgpr_read_b32 v139,a123\n"
"v_mfma_f32_16x16x32_bf16 v[92:95],v[188:191],v[228:231],v[92:95]\n"
"v_accvgpr_read_b32 v140,a124\n"
"v_accvgpr_read_b32 v141,a125\n"
"v_mfma_f32_16x16x32_bf16 v[92:95],v[180:183],v[224:227],v[92:95]\n"
"v_accvgpr_read_b32 v142,a126\n"
"v_accvgpr_read_b32 v143,a127\n"
"v_mfma_f32_16x16x32_bf16 v[92:95],v[52:55],v[240:243],v[92:95]\n"
"v_accvgpr_read_b32 v144,a128\n"
"v_accvgpr_read_b32 v145,a129\n"
"v_mfma_f32_16x16x32_bf16 v[92:95],v[60:63],v[244:247],v[92:95]\n"
"v_accvgpr_read_b32 v146,a130\n"
"v_accvgpr_read_b32 v147,a131\n"
"v_mfma_f32_16x16x32_bf16 v[92:95],v[20:23],v[248:251],v[92:95]\n"
"v_accvgpr_read_b32 v148,a132\n"
"v_accvgpr_read_b32 v149,a133\n"
"v_mfma_f32_16x16x32_bf16 v[92:95],v[28:31],v[252:255],v[92:95]\n"
"v_accvgpr_read_b32 v150,a134\n"
"v_accvgpr_read_b32 v151,a135\n"
"v_mfma_f32_16x16x32_bf16 v[88:91],v[24:27],v[252:255],v[88:91]\n"
"v_accvgpr_read_b32 v152,a136\n"
"v_accvgpr_read_b32 v153,a137\n"
"v_mfma_f32_16x16x32_bf16 v[88:91],v[16:19],v[248:251],v[88:91]\n"
"v_accvgpr_read_b32 v154,a138\n"
"v_accvgpr_read_b32 v155,a139\n"
"v_mfma_f32_16x16x32_bf16 v[88:91],v[56:59],v[244:247],v[88:91]\n"
"v_accvgpr_read_b32 v156,a140\n"
"v_accvgpr_read_b32 v157,a141\n"
"v_mfma_f32_16x16x32_bf16 v[88:91],v[48:51],v[240:243],v[88:91]\n"
"v_accvgpr_read_b32 v158,a142\n"
"v_accvgpr_read_b32 v159,a143\n"
".Lfk_tail:\n"
"v_lshlrev_b32 v224,16,v72\n"
"v_and_b32 v225,0xffff0000,v72\n"
"v_lshlrev_b32 v226,16,v73\n"
"v_and_b32 v227,0xffff0000,v73\n"
"v_lshlrev_b32 v228,16,v74\n"
"s_nop 0\n"
"v_cvt_pk_bf16_f32 v88,v88,v89\n"
"v_cvt_pk_bf16_f32 v89,v90,v91\n"
"v_cvt_pk_bf16_f32 v90,v92,v93\n"
"v_cvt_pk_bf16_f32 v91,v94,v95\n"
"v_max_u32 v88,1,v88\n"
"v_max_u32 v89,1,v89\n"
"v_max_u32 v90,1,v90\n"
"v_max_u32 v91,1,v91\n"
"v_mfma_f32_16x16x32_bf16 v[0:3],v[66:69],a[96:99],0\n"
"buffer_store_dwordx4 v[88:91],v118,s[24:27],s53 offen sc1\n"
"v_mfma_f32_16x16x32_bf16 v[4:7],v[64:67],a[96:99],0\n"
"s_mov_b32 s101,s22\n"
"v_and_b32 v231,0xffff0000,v75\n"
"v_lshlrev_b32 v232,16,v76\n"
"v_mfma_f32_16x16x32_bf16 v[8:11],v[66:69],a[100:103],0\n"
"v_and_b32 v229,0xffff0000,v74\n"
"v_lshlrev_b32 v230,16,v75\n"
"v_mfma_f32_16x16x32_bf16 v[12:15],v[64:67],a[100:103],0\n"
"s_mov_b32 s67,0x3a800000\n"
"v_and_b32 v233,0xffff0000,v76\n"
"v_lshlrev_b32 v234,16,v77\n"
"v_and_b32 v235,0xffff0000,v77\n"
"v_lshlrev_b32 v236,16,v78\n"
"v_and_b32 v237,0xffff0000,v78\n"
"v_lshlrev_b32 v238,16,v79\n"
"v_and_b32 v239,0xffff0000,v79\n"
"v_pk_add_f32 v[70:71],v[224:225],v[226:227]\n"
"v_pk_add_f32 v[70:71],v[70:71],v[228:229]\n"
"v_pk_add_f32 v[70:71],v[70:71],v[230:231]\n"
"v_pk_add_f32 v[70:71],v[70:71],v[232:233]\n"
"v_pk_add_f32 v[70:71],v[70:71],v[234:235]\n"
"v_pk_add_f32 v[70:71],v[70:71],v[236:237]\n"
"v_pk_add_f32 v[70:71],v[70:71],v[238:239]\n"
"v_add_f32 v70,v70,v71\n"
"s_nop 1\n"
"v_add_f32_dpp v70,v70,v70 quad_perm:[1,0,3,2] row_mask:0xf bank_mask:0xf\n"
"s_nop 1\n"
"v_add_f32_dpp v70,v70,v70 quad_perm:[2,3,0,1] row_mask:0xf bank_mask:0xf\n"
"s_nop 1\n"
"v_add_f32_dpp v70,v70,v70 row_half_mirror row_mask:0xf bank_mask:0xf\n"
"s_nop 1\n"
"v_add_f32_dpp v70,v70,v70 row_mirror row_mask:0xf bank_mask:0xf\n"
"v_mov_b32 v71,v70\n"
"s_nop 1\n"
"v_permlane16_swap_b32 v71,v70\n"
"v_add_f32 v70,v70,v71\n"
"v_mov_b32 v71,v70\n"
"s_nop 1\n"
"v_permlane32_swap_b32 v71,v70\n"
"v_add_f32 v70,v70,v71\n"
"v_min3_u32 v36,v128,v129,v130\n"
"v_min3_u32 v37,v131,v132,v133\n"
"v_min3_u32 v36,v36,v134,v135\n"
"v_min3_u32 v37,v37,v136,v137\n"
"v_min3_u32 v36,v36,v138,v139\n"
"v_min3_u32 v37,v37,v140,v141\n"
"v_min3_u32 v36,v36,v142,v143\n"
"v_min3_u32 v37,v37,v144,v145\n"
"v_min3_u32 v36,v36,v146,v147\n"
"v_min3_u32 v37,v37,v148,v149\n"
"v_min3_u32 v36,v36,v150,v151\n"
"v_min3_u32 v37,v37,v152,v153\n"
"v_min3_u32 v36,v36,v154,v155\n"
"v_min3_u32 v37,v37,v156,v157\n"
"v_min3_u32 v36,v36,v158,v159\n"
"v_min_u32 v36,v36,v37\n"
"v_cmp_eq_u32 vcc,0,v36\n"
"s_nop 0\n"
"s_cbranch_vccz .Lfk_t0\n"
"s_mov_b32 s84,0x40000\n"
"s_mov_b32 s91,s62\n"
"buffer_load_dwordx4 v[128:131],v126,s[24:27],s91 offen sc1\n"
"buffer_load_dwordx4 v[132:135],v117,s[24:27],s91 offen sc1\n"
"s_add_u32 s91,s91,4194304\n"
"buffer_load_dwordx4 v[136:139],v126,s[24:27],s91 offen sc1\n"
"buffer_load_dwordx4 v[140:143],v117,s[24:27],s91 offen sc1\n"
"s_add_u32 s91,s91,4194304\n"
"buffer_load_dwordx4 v[144:147],v126,s[24:27],s91 offen sc1\n"
"buffer_load_dwordx4 v[148:151],v117,s[24:27],s91 offen sc1\n"
"s_add_u32 s91,s91,4194304\n"
"buffer_load_dwordx4 v[152:155],v126,s[24:27],s91 offen sc1\n"
"buffer_load_dwordx4 v[156:159],v117,s[24:27],s91 offen sc1\n"
"s_waitcnt vmcnt(0)\n"
".Lfk_wa:\n"
"v_min3_u32 v36,v128,v129,v130\n"
"v_min3_u32 v37,v131,v132,v133\n"
"v_min3_u32 v36,v36,v134,v135\n"
"v_min3_u32 v37,v37,v136,v137\n"
"v_min3_u32 v36,v36,v138,v139\n"
"v_min3_u32 v37,v37,v140,v141\n"
"v_min3_u32 v36,v36,v142,v143\n"
"v_min3_u32 v37,v37,v144,v145\n"
"v_min3_u32 v36,v36,v146,v147\n"
"v_min3_u32 v37,v37,v148,v149\n"
"v_min3_u32 v36,v36,v150,v151\n"
"v_min3_u32 v37,v37,v152,v153\n"
"v_min3_u32 v36,v36,v154,v155\n"
"v_min3_u32 v37,v37,v156,v157\n"
"v_min3_u32 v36,v36,v158,v159\n"
"v_min_u32 v36,v36,v37\n"
"v_cmp_eq_u32 vcc,0,v36\n"
"s_nop 0\n"
"s_cbranch_vccz .Lfk_wa_x\n"
"s_sleep 2\n"
"s_mov_b64 exec,vcc\n"
"s_mov_b32 s91,s62\n"
"buffer_load_dwordx4 v[128:131],v126,s[24:27],s91 offen sc1\n"
"buffer_load_dwordx4 v[132:135],v117,s[24:27],s91 offen sc1\n"
"s_add_u32 s91,s91,4194304\n"
"buffer_load_dwordx4 v[136:139],v126,s[24:27],s91 offen sc1\n"
"buffer_load_dwordx4 v[140:143],v117,s[24:27],s91 offen sc1\n"
"s_add_u32 s91,s91,4194304\n"
"buffer_load_dwordx4 v[144:147],v126,s[24:27],s91 offen sc1\n"
"buffer_load_dwordx4 v[148:151],v117,s[24:27],s91 offen sc1\n"
"s_add_u32 s91,s91,4194304\n"
"buffer_load_dwordx4 v[152:155],v126,s[24:27],s91 offen sc1\n"
"buffer_load_dwordx4 v[156:159],v117,s[24:27],s91 offen sc1\n"
"s_mov_b64 exec,-1\n"
"s_sub_u32 s84,s84,1\n"
"s_waitcnt vmcnt(0)\n"
"s_cbranch_scc0 .Lfk_wa\n"
".Lfk_wa_x:\n"
".Lfk_t0:\n"
"s_getpc_b64 s[94:95]\n"
".Lfk_rm0:\n"
"s_add_u32 s94,s94,.Lfk_rm1-.Lfk_rm0\n"
"v_mfma_f32_16x16x32_bf16 v[0:3],v[66:69],v[128:131],v[0:3]\n"
"s_addc_u32 s95,s95,0\n"
"v_mfma_f32_16x16x32_bf16 v[4:7],v[64:67],v[128:131],v[4:7]\n"
"v_mfma_f32_16x16x32_bf16 v[8:11],v[66:69],v[132:135],v[8:11]\n"
"v_mfma_f32_16x16x32_bf16 v[12:15],v[64:67],v[132:135],v[12:15]\n"
"v_mfma_f32_16x16x32_bf16 v[0:3],v[66:69],v[136:139],v[0:3]\n"
"v_mfma_f32_16x16x32_bf16 v[4:7],v[64:67],v[136:139],v[4:7]\n"
"v_mfma_f32_16x16x32_bf16 v[8:11],v[66:69],v[140:143],v[8:11]\n"
"v_mfma_f32_16x16x32_bf16 v[12:15],v[64:67],v[140:143],v[12:15]\n"
"v_mfma_f32_16x16x32_bf16 v[0:3],v[66:69],v[144:147],v[0:3]\n"
"v_mfma_f32_16x16x32_bf16 v[4:7],v[64:67],v[144:147],v[4:7]\n"
"v_mfma_f32_16x16x32_bf16 v[8:11],v[66:69],v[148:151],v[8:11]\n"
"v_mfma_f32_16x16x32_bf16 v[12:15],v[64:67],v[148:151],v[12:15]\n"
"v_mfma_f32_16x16x32_bf16 v[0:3],v[66:69],v[152:155],v[0:3]\n"
"v_mfma_f32_16x16x32_bf16 v[4:7],v[64:67],v[152:155],v[4:7]\n"
"v_mfma_f32_16x16x32_bf16 v[8:11],v[66:69],v[156:159],v[8:11]\n"
"v_mfma_f32_16x16x32_bf16 v[12:15],v[64:67],v[156:159],v[12:15]\n"
"s_nop 7\n"
".Lfk_rm:\n"
"v_readfirstlane_b32 s64,v0\n"
"s_nop 1\n"
"v_mov_b32 v38,s64\n"
"v_pk_add_f32 v[16:17],v[0:1],v[38:39] op_sel_hi:[1,0] neg_lo:[0,1] neg_hi:[0,1]\n"
"v_pk_add_f32 v[18:19],v[2:3],v[38:39] op_sel_hi:[1,0] neg_lo:[0,1] neg_hi:[0,1]\n"
"v_pk_add_f32 v[20:21],v[4:5],v[38:39] op_sel_hi:[1,0] neg_lo:[0,1] neg_hi:[0,1]\n"
"v_pk_add_f32 v[22:23],v[6:7],v[38:39] op_sel_hi:[1,0] neg_lo:[0,1] neg_hi:[0,1]\n"
"v_pk_add_f32 v[24:25],v[8:9],v[38:39] op_sel_hi:[1,0] neg_lo:[0,1] neg_hi:[0,1]\n"
"v_pk_add_f32 v[26:27],v[10:11],v[38:39] op_sel_hi:[1,0] neg_lo:[0,1] neg_hi:[0,1]\n"
"v_pk_add_f32 v[28:29],v[12:13],v[38:39] op_sel_hi:[1,0] neg_lo:[0,1] neg_hi:[0,1]\n"
"v_pk_add_f32 v[30:31],v[14:15],v[38:39] op_sel_hi:[1,0] neg_lo:[0,1] neg_hi:[0,1]\n"
"v_pk_add_f32 v[32:33],v[16:17],v[18:19]\n"
"v_pk_mul_f32 v[34:35],v[16:17],v[16:17]\n"
"v_pk_mul_f32 v[36:37],v[16:17],v[224:225]\n"
"v_pk_fma_f32 v[34:35],v[18:19],v[18:19],v[34:35]\n"
"v_pk_fma_f32 v[36:37],v[18:19],v[226:227],v[36:37]\n"
"v_pk_add_f32 v[32:33],v[32:33],v[20:21]\n"
"v_pk_fma_f32 v[34:35],v[20:21],v[20:21],v[34:35]\n"
"v_pk_fma_f32 v[36:37],v[20:21],v[228:229],v[36:37]\n"
"v_pk_add_f32 v[32:33],v[32:33],v[22:23]\n"
"v_pk_fma_f32 v[34:35],v[22:23],v[22:23],v[34:35]\n"
"v_pk_fma_f32 v[36:37],v[22:23],v[230:231],v[36:37]\n"
"v_pk_add_f32 v[32:33],v[32:33],v[24:25]\n"
"v_pk_fma_f32 v[34:35],v[24:25],v[24:25],v[34:35]\n"
"v_pk_fma_f32 v[36:37],v[24:25],v[232:233],v[36:37]\n"
"v_pk_add_f32 v[32:33],v[32:33],v[26:27]\n"
"v_pk_fma_f32 v[34:35],v[26:27],v[26:27],v[34:35]\n"
"v_pk_fma_f32 v[36:37],v[26:27],v[234:235],v[36:37]\n"
"v_pk_add_f32 v[32:33],v[32:33],v[28:29]\n"
"v_pk_fma_f32 v[34:35],v[28:29],v[28:29],v[34:35]\n"
"v_pk_fma_f32 v[36:37],v[28:29],v[236:237],v[36:37]\n"
"v_pk_add_f32 v[32:33],v[32:33],v[30:31]\n"
"v_pk_fma_f32 v[34:35],v[30:31],v[30:31],v[34:35]\n"
"v_pk_fma_f32 v[36:37],v[30:31],v[238:239],v[36:37]\n"
"v_add_f32 v32,v32,v33\n"
"v_add_f32 v33,v34,v35\n"
"v_add_f32 v34,v36,v37\n"
"v_add_f32_dpp v32,v32,v32 quad_perm:[1,0,3,2] row_mask:0xf bank_mask:0xf\n"
"v_add_f32_dpp v33,v33,v33 quad_perm:[1,0,3,2] row_mask:0xf bank_mask:0xf\n"
"v_add_f32_dpp v34,v34,v34 quad_perm:[1,0,3,2] row_mask:0xf bank_mask:0xf\n"
"v_add_f32_dpp v32,v32,v32 quad_perm:[2,3,0,1] row_mask:0xf bank_mask:0xf\n"
"v_add_f32_dpp v33,v33,v33 quad_perm:[2,3,0,1] row_mask:0xf bank_mask:0xf\n"
"v_add_f32_dpp v34,v34,v34 quad_perm:[2,3,0,1] row_mask:0xf bank_mask:0xf\n"
"v_add_f32_dpp v32,v32,v32 row_half_mirror row_mask:0xf bank_mask:0xf\n"
"v_add_f32_dpp v33,v33,v33 row_half_mirror row_mask:0xf bank_mask:0xf\n"
"v_add_f32_dpp v34,v34,v34 row_half_mirror row_mask:0xf bank_mask:0xf\n"
"v_add_f32_dpp v32,v32,v32 row_mirror row_mask:0xf bank_mask:0xf\n"
"v_add_f32_dpp v33,v33,v33 row_mirror row_mask:0xf bank_mask:0xf\n"
"v_add_f32_dpp v34,v34,v34 row_mirror row_mask:0xf bank_mask:0xf\n"
"v_add_f32_dpp v32,v32,v32 row_bcast:15 row_mask:0xa bank_mask:0xf\n"
"v_add_f32_dpp v33,v33,v33 row_bcast:15 row_mask:0xa bank_mask:0xf\n"
"v_add_f32_dpp v34,v34,v34 row_bcast:15 row_mask:0xa bank_mask:0xf\n"
"v_add_f32_dpp v32,v32,v32 row_bcast:31 row_mask:0xc bank_mask:0xf\n"
"v_add_f32_dpp v33,v33,v33 row_bcast:31 row_mask:0xc bank_mask:0xf\n"
"v_add_f32_dpp v34,v34,v34 row_bcast:31 row_mask:0xc bank_mask:0xf\n"
"v_mul_f32 v32,s67,v32\n"
"v_add_f32 v35,v32,v38\n"
"v_mul_f32 v32,v32,v32\n"
"v_cvt_pk_bf16_f32 v35,v35,v35\n"
"v_lshlrev_b32 v35,16,v35\n"
"v_fma_f32 v33,v33,s67,-v32\n"
"v_sub_f32 v36,v38,v35\n"
"v_max_f32 v33,0,v33\n"
"v_fmac_f32 v34,v36,v70\n"
"v_cvt_pk_bf16_f32 v33,v33,v33\n"
"v_lshlrev_b32 v33,16,v33\n"
"v_add_f32 v33,0x3727c5ac,v33\n"
"v_cvt_pk_bf16_f32 v33,v33,v33\n"
"v_lshlrev_b32 v33,16,v33\n"
"v_sqrt_f32 v33,v33\n"
"s_nop 0\n"
"v_cvt_pk_bf16_f32 v33,v33,v33\n"
"v_lshlrev_b32 v33,16,v33\n"
"v_rcp_f32 v36,v33\n"
"v_lshrrev_b32 v33,16,v33\n"
"v_mul_f32 v37,v34,v36\n"
"v_or_b32 v33,v33,v35\n"
"v_cvt_pk_bf16_f32 v37,v37,v37\n"
"v_ashrrev_i32 v36,31,v37\n"
"v_or_b32 v36,0x80000000,v36\n"
"v_xor_b32 v37,v37,v36\n"
"v_and_b32 v37,0xffff0000,v37\n"
"v_readlane_b32 s64,v33,63\n"
"v_readlane_b32 s63,v37,63\n"
"s_nop 1\n"
"s_setpc_b64 s[94:95]\n"
".Lfk_rm1:\n"
"s_add_u32 s91,s62,2097152\n"
"buffer_load_dwordx4 v[160:163],v126,s[24:27],s91 offen sc1\n"
"buffer_load_dwordx4 v[164:167],v117,s[24:27],s91 offen sc1\n"
"s_add_u32 s91,s91,4194304\n"
"buffer_load_dwordx4 v[168:171],v126,s[24:27],s91 offen sc1\n"
"buffer_load_dwordx4 v[172:175],v117,s[24:27],s91 offen sc1\n"
"s_add_u32 s91,s91,4194304\n"
"buffer_load_dwordx4 v[176:179],v126,s[24:27],s91 offen sc1\n"
"buffer_load_dwordx4 v[180:183],v117,s[24:27],s91 offen sc1\n"
"s_add_u32 s91,s91,4194304\n"
"buffer_load_dwordx4 v[184:187],v126,s[24:27],s91 offen sc1\n"
"buffer_load_dwordx4 v[188:191],v117,s[24:27],s91 offen sc1\n"
"s_sub_u32 s65,0xffff,s60\n"
"s_or_b32 s65,s65,s63\n"
"s_mov_b32 s66,s64\n"
"v_lshlrev_b32 v34,16,s64\n"
"v_mov_b32 v35,s64\n"
"v_rcp_f32 v34,v34\n"
"v_and_b32 v35,0xffff0000,v35\n"
"v_cvt_pk_bf16_f32 v240,v0,v1\n"
"v_cvt_pk_bf16_f32 v241,v2,v3\n"
"v_cvt_pk_bf16_f32 v242,v4,v5\n"
"v_cvt_pk_bf16_f32 v243,v6,v7\n"
"v_cvt_pk_bf16_f32 v244,v8,v9\n"
"v_cvt_pk_bf16_f32 v245,v10,v11\n"
"v_cvt_pk_bf16_f32 v246,v12,v13\n"
"v_cvt_pk_bf16_f32 v247,v14,v15\n"
"v_lshlrev_b32 v0,16,v240\n"
"v_and_b32 v1,0xffff0000,v240\n"
"v_lshlrev_b32 v2,16,v241\n"
"v_and_b32 v3,0xffff0000,v241\n"
"v_lshlrev_b32 v4,16,v242\n"
"v_and_b32 v5,0xffff0000,v242\n"
"v_lshlrev_b32 v6,16,v243\n"
"v_and_b32 v7,0xffff0000,v243\n"
"v_lshlrev_b32 v8,16,v244\n"
"v_and_b32 v9,0xffff0000,v244\n"
"v_lshlrev_b32 v10,16,v245\n"
"v_and_b32 v11,0xffff0000,v245\n"
"v_lshlrev_b32 v12,16,v246\n"
"v_and_b32 v13,0xffff0000,v246\n"
"v_lshlrev_b32 v14,16,v247\n"
"v_and_b32 v15,0xffff0000,v247\n"
"v_pk_add_f32 v[16:17],v[0:1],v[34:35] op_sel:[0,1] neg_lo:[0,1] neg_hi:[0,1]\n"
"v_pk_add_f32 v[18:19],v[2:3],v[34:35] op_sel:[0,1] neg_lo:[0,1] neg_hi:[0,1]\n"
"v_pk_add_f32 v[20:21],v[4:5],v[34:35] op_sel:[0,1] neg_lo:[0,1] neg_hi:[0,1]\n"
"v_pk_add_f32 v[22:23],v[6:7],v[34:35] op_sel:[0,1] neg_lo:[0,1] neg_hi:[0,1]\n"
"v_pk_add_f32 v[24:25],v[8:9],v[34:35] op_sel:[0,1] neg_lo:[0,1] neg_hi:[0,1]\n"
"v_pk_add_f32 v[26:27],v[10:11],v[34:35] op_sel:[0,1] neg_lo:[0,1] neg_hi:[0,1]\n"
"v_pk_add_f32 v[28:29],v[12:13],v[34:35] op_sel:[0,1] neg_lo:[0,1] neg_hi:[0,1]\n"
"v_pk_add_f32 v[30:31],v[14:15],v[34:35] op_sel:[0,1] neg_lo:[0,1] neg_hi:[0,1]\n"
"v_cvt_pk_bf16_f32 v240,v16,v17\n"
"v_cvt_pk_bf16_f32 v241,v18,v19\n"
"v_cvt_pk_bf16_f32 v242,v20,v21\n"
"v_cvt_pk_bf16_f32 v243,v22,v23\n"
"v_cvt_pk_bf16_f32 v244,v24,v25\n"
"v_cvt_pk_bf16_f32 v245,v26,v27\n"
"v_cvt_pk_bf16_f32 v246,v28,v29\n"
"v_cvt_pk_bf16_f32 v247,v30,v31\n"
"v_lshlrev_b32 v16,16,v240\n"
"v_and_b32 v17,0xffff0000,v240\n"
"v_lshlrev_b32 v18,16,v241\n"
"v_and_b32 v19,0xffff0000,v241\n"
"v_lshlrev_b32 v20,16,v242\n"
"v_and_b32 v21,0xffff0000,v242\n"
"v_lshlrev_b32 v22,16,v243\n"
"v_and_b32 v23,0xffff0000,v243\n"
"v_lshlrev_b32 v24,16,v244\n"
"v_and_b32 v25,0xffff0000,v244\n"
"v_lshlrev_b32 v26,16,v245\n"
"v_and_b32 v27,0xffff0000,v245\n"
"v_lshlrev_b32 v28,16,v246\n"
"v_and_b32 v29,0xffff0000,v246\n"
"v_lshlrev_b32 v30,16,v247\n"
"v_and_b32 v31,0xffff0000,v247\n"
"v_pk_mul_f32 v[16:17],v[16:17],v[34:35] op_sel_hi:[1,0]\n"
"v_pk_mul_f32 v[18:19],v[18:19],v[34:35] op_sel_hi:[1,0]\n"
"v_pk_mul_f32 v[20:21],v[20:21],v[34:35] op_sel_hi:[1,0]\n"
"v_pk_mul_f32 v[22:23],v[22:23],v[34:35] op_sel_hi:[1,0]\n"
"v_pk_mul_f32 v[24:25],v[24:25],v[34:35] op_sel_hi:[1,0]\n"
"v_pk_mul_f32 v[26:27],v[26:27],v[34:35] op_sel_hi:[1,0]\n"
"v_pk_mul_f32 v[28:29],v[28:29],v[34:35] op_sel_hi:[1,0]\n"
"v_pk_mul_f32 v[30:31],v[30:31],v[34:35] op_sel_hi:[1,0]\n"
"v_cvt_pk_bf16_f32 v240,v16,v17\n"
"v_cvt_pk_bf16_f32 v241,v18,v19\n"
"v_cvt_pk_bf16_f32 v242,v20,v21\n"
"v_cvt_pk_bf16_f32 v243,v22,v23\n"
"v_cvt_pk_bf16_f32 v244,v24,v25\n"
"v_cvt_pk_bf16_f32 v245,v26,v27\n"
"v_cvt_pk_bf16_f32 v246,v28,v29\n"
"v_cvt_pk_bf16_f32 v247,v30,v31\n"
"v_max_u32 v216,1,v240\n"
"v_max_u32 v217,1,v241\n"
"v_max_u32 v218,1,v242\n"
"v_max_u32 v219,1,v243\n"
"v_max_u32 v220,1,v244\n"
"v_max_u32 v221,1,v245\n"
"v_max_u32 v222,1,v246\n"
"v_max_u32 v223,1,v247\n"
"v_lshlrev_b32 v16,16,v240\n"
"v_and_b32 v17,0xffff0000,v240\n"
"v_lshlrev_b32 v18,16,v241\n"
"v_and_b32 v19,0xffff0000,v241\n"
"v_lshlrev_b32 v20,16,v242\n"
"v_and_b32 v21,0xffff0000,v242\n"
"v_lshlrev_b32 v22,16,v243\n"
"v_and_b32 v23,0xffff0000,v243\n"
"v_lshlrev_b32 v24,16,v244\n"
"v_and_b32 v25,0xffff0000,v244\n"
"v_lshlrev_b32 v26,16,v245\n"
"v_and_b32 v27,0xffff0000,v245\n"
"v_lshlrev_b32 v28,16,v246\n"
"v_and_b32 v29,0xffff0000,v246\n"
"v_lshlrev_b32 v30,16,v247\n"
"v_and_b32 v31,0xffff0000,v247\n"
"v_pk_add_f32 v[16:17],v[16:17],v[0:1]\n"
"v_pk_add_f32 v[18:19],v[18:19],v[2:3]\n"
"v_pk_add_f32 v[20:21],v[20:21],v[4:5]\n"
"v_pk_add_f32 v[22:23],v[22:23],v[6:7]\n"
"v_pk_add_f32 v[24:25],v[24:25],v[8:9]\n"
"v_pk_add_f32 v[26:27],v[26:27],v[10:11]\n"
"v_pk_add_f32 v[28:29],v[28:29],v[12:13]\n"
"v_pk_add_f32 v[30:31],v[30:31],v[14:15]\n"
"v_cvt_pk_bf16_f32 v240,v16,v17\n"
"v_cvt_pk_bf16_f32 v241,v18,v19\n"
"v_cvt_pk_bf16_f32 v242,v20,v21\n"
"v_cvt_pk_bf16_f32 v243,v22,v23\n"
"v_cvt_pk_bf16_f32 v244,v24,v25\n"
"v_cvt_pk_bf16_f32 v245,v26,v27\n"
"v_cvt_pk_bf16_f32 v246,v28,v29\n"
"v_cvt_pk_bf16_f32 v247,v30,v31\n"
"buffer_store_dwordx4 v[240:243],v112,s[48:51],s61 offen offset:0 sc1\n"
"buffer_store_dwordx4 v[244:247],v112,s[48:51],s61 offen offset:1024 sc1\n"
"s_barrier\n"
"s_lshl_b32 s82,s52,13\n"
"s_add_u32 s82,s82,0\n"
"v_bfe_u32 v41,v112,5,2\n"
"v_lshlrev_b32 v41,13,v41\n"
"v_bfe_u32 v42,v112,4,1\n"
"v_lshl_add_u32 v41,v42,3,v41\n"
"s_lshl_b32 s83,s100,4\n"
"v_mfma_f32_16x16x32_bf16 v[200:203],v[66:69],a[104:107],0\n"
"s_add_u32 s83,s83,149946368\n"
"v_mov_b32 v45,s66\n"
"v_add_u32 v41,0,v41\n"
"v_mfma_f32_16x16x32_bf16 v[204:207],v[64:67],a[104:107],0\n"
"v_mov_b32 v40,s82\n"
"v_mov_b32 v51,s80\n"
"v_mfma_f32_16x16x32_bf16 v[208:211],v[66:69],a[108:111],0\n"
"v_mov_b32 v44,s65\n"
"v_lshlrev_b32 v43,8,v112\n"
"v_mfma_f32_16x16x32_bf16 v[212:215],v[64:67],a[108:111],0\n"
"s_mov_b32 s84,0x40000\n"
"v_mov_b32 v49,s80\n"
"s_waitcnt vmcnt(2)\n"
".Lfk_w1:\n"
"v_mfma_f32_16x16x32_bf16 v[0:3],v[66:69],v[160:163],v[200:203]\n"
"v_min3_u32 v36,v160,v161,v162\n"
"v_min3_u32 v37,v163,v164,v165\n"
"v_mfma_f32_16x16x32_bf16 v[4:7],v[64:67],v[160:163],v[204:207]\n"
"v_min3_u32 v36,v36,v166,v167\n"
"v_min3_u32 v37,v37,v168,v169\n"
"v_mfma_f32_16x16x32_bf16 v[8:11],v[66:69],v[164:167],v[208:211]\n"
"v_min3_u32 v36,v36,v170,v171\n"
"v_min3_u32 v37,v37,v172,v173\n"
"v_mfma_f32_16x16x32_bf16 v[12:15],v[64:67],v[164:167],v[212:215]\n"
"v_min3_u32 v36,v36,v174,v175\n"
"v_min3_u32 v37,v37,v176,v177\n"
"v_mfma_f32_16x16x32_bf16 v[0:3],v[66:69],v[168:171],v[0:3]\n"
"v_min3_u32 v36,v36,v178,v179\n"
"v_min3_u32 v37,v37,v180,v181\n"
"v_mfma_f32_16x16x32_bf16 v[4:7],v[64:67],v[168:171],v[4:7]\n"
"v_min3_u32 v36,v36,v182,v183\n"
"v_min3_u32 v37,v37,v184,v185\n"
"v_mfma_f32_16x16x32_bf16 v[8:11],v[66:69],v[172:175],v[8:11]\n"
"v_min3_u32 v36,v36,v186,v187\n"
"v_min3_u32 v37,v37,v188,v189\n"
"v_mfma_f32_16x16x32_bf16 v[12:15],v[64:67],v[172:175],v[12:15]\n"
"v_min3_u32 v36,v36,v190,v191\n"
"v_min_u32 v36,v36,v37\n"
"v_cmp_eq_u32 vcc,0,v36\n"
"v_mfma_f32_16x16x32_bf16 v[0:3],v[66:69],v[176:179],v[0:3]\n"
"s_cbranch_vccz .Lfk_w1_x\n"
"s_mov_b64 exec,vcc\n"
"s_add_u32 s91,s62,2097152\n"
"buffer_load_dwordx4 v[160:163],v126,s[24:27],s91 offen sc1\n"
"buffer_load_dwordx4 v[164:167],v117,s[24:27],s91 offen sc1\n"
"s_add_u32 s91,s91,4194304\n"
"buffer_load_dwordx4 v[168:171],v126,s[24:27],s91 offen sc1\n"
"buffer_load_dwordx4 v[172:175],v117,s[24:27],s91 offen sc1\n"
"s_add_u32 s91,s91,4194304\n"
"buffer_load_dwordx4 v[176:179],v126,s[24:27],s91 offen sc1\n"
"buffer_load_dwordx4 v[180:183],v117,s[24:27],s91 offen sc1\n"
"s_add_u32 s91,s91,4194304\n"
"buffer_load_dwordx4 v[184:187],v126,s[24:27],s91 offen sc1\n"
"buffer_load_dwordx4 v[188:191],v117,s[24:27],s91 offen sc1\n"
"s_mov_b64 exec,-1\n"
"s_sub_u32 s84,s84,1\n"
"s_waitcnt vmcnt(0)\n"
"s_cbranch_scc0 .Lfk_w1\n"
".Lfk_w1_x:\n"
"v_mfma_f32_16x16x32_bf16 v[4:7],v[64:67],v[176:179],v[4:7]\n"
"v_mfma_f32_16x16x32_bf16 v[8:11],v[66:69],v[180:183],v[8:11]\n"
"v_mfma_f32_16x16x32_bf16 v[12:15],v[64:67],v[180:183],v[12:15]\n"
"v_mfma_f32_16x16x32_bf16 v[0:3],v[66:69],v[184:187],v[0:3]\n"
"v_mfma_f32_16x16x32_bf16 v[4:7],v[64:67],v[184:187],v[4:7]\n"
"v_mfma_f32_16x16x32_bf16 v[8:11],v[66:69],v[188:191],v[8:11]\n"
"v_mfma_f32_16x16x32_bf16 v[12:15],v[64:67],v[188:191],v[12:15]\n"
"s_nop 6\n"
"s_call_b64 s[94:95],.Lfk_rm\n"
"s_sub_u32 s82,64511,s60\n"
"s_or_b32 s63,s63,s82\n"
"v_mov_b32 v46,s63\n"
"v_mov_b32 v47,s64\n"
"v_mov_b32 v39,0\n"
"ds_write_b128 v40,v[44:47]\n"
"s_waitcnt vmcnt(0) lgkmcnt(0)\n"
"s_barrier\n"
"ds_read_b64 v[52:53],v41\n"
"s_waitcnt lgkmcnt(0)\n"
"v_mov_b32 v48,v52\n"
"s_nop 1\n"
"v_min_u32_dpp v54,v48,v48 quad_perm:[1,0,3,2] row_mask:0xf bank_mask:0xf\n"
"v_max_u32_dpp v48,v48,v48 quad_perm:[1,0,3,2] row_mask:0xf bank_mask:0xf\n"
"v_max_u32_dpp v39,v39,v39 quad_perm:[1,0,3,2] row_mask:0xf bank_mask:0xf\n"
"v_max_u32 v39,v39,v54\n"
"v_min_u32_dpp v54,v48,v48 quad_perm:[2,3,0,1] row_mask:0xf bank_mask:0xf\n"
"v_max_u32_dpp v48,v48,v48 quad_perm:[2,3,0,1] row_mask:0xf bank_mask:0xf\n"
"v_max_u32_dpp v39,v39,v39 quad_perm:[2,3,0,1] row_mask:0xf bank_mask:0xf\n"
"v_max_u32 v39,v39,v54\n"
"v_min_u32_dpp v54,v48,v48 row_half_mirror row_mask:0xf bank_mask:0xf\n"
"v_max_u32_dpp v48,v48,v48 row_half_mirror row_mask:0xf bank_mask:0xf\n"
"v_max_u32_dpp v39,v39,v39 row_half_mirror row_mask:0xf bank_mask:0xf\n"
"v_max_u32 v39,v39,v54\n"
"v_xor_b32 v50,0x800,v39\n"
"s_cmp_lg_u32 s52,0\n"
"s_cbranch_scc1 .Lfk_t1\n"
"s_mov_b64 exec,0xff\n"
"buffer_store_dwordx4 v[48:51],v43,s[24:27],s83 offen sc1\n"
"s_mov_b64 exec,-1\n"
"s_add_u32 s83,s83,0x40000\n"
"v_cmp_eq_u32 vcc,v48,v52\n"
"s_nop 0\n"
"s_ff1_i32_b64 s82,vcc\n"
"v_readlane_b32 s90,v53,s82\n"
"s_nop 1\n"
"v_mov_b32 v44,s90\n"
"v_mov_b32 v45,s80\n"
"v_cmp_eq_u32 vcc,v39,v52\n"
"s_nop 0\n"
"s_ff1_i32_b64 s82,vcc\n"
"v_readlane_b32 s90,v53,s82\n"
"s_nop 1\n"
"v_mov_b32 v46,s90\n"
"v_mov_b32 v47,s80\n"
"s_mov_b64 exec,0x100\n"
"buffer_store_dwordx4 v[44:47],v43,s[24:27],s83 offen sc1\n"
"s_mov_b64 exec,-1\n"
".Lfk_t1:\n"
"v_readfirstlane_b32 s89,v39\n"
"s_add_u32 m0,s30,131072\n"
"s_nop 0\n"
"buffer_load_dwordx4 v96,s[40:43],0 offen offset:0 nt lds\n"
"buffer_load_dwordx4 v96,s[40:43],0 offen offset:1024 nt lds\n"
"buffer_load_dwordx4 v97,s[40:43],0 offen offset:2048 nt lds\n"
"buffer_load_dwordx4 v97,s[40:43],0 offen offset:3072 nt lds\n"
"v_lshlrev_b32 v34,16,s64\n"
"v_mov_b32 v35,s64\n"
"v_rcp_f32 v34,v34\n"
"v_and_b32 v35,0xffff0000,v35\n"
"v_cvt_pk_bf16_f32 v248,v0,v1\n"
"v_cvt_pk_bf16_f32 v249,v2,v3\n"
"v_cvt_pk_bf16_f32 v250,v4,v5\n"
"v_cvt_pk_bf16_f32 v251,v6,v7\n"
"v_cvt_pk_bf16_f32 v252,v8,v9\n"
"v_cvt_pk_bf16_f32 v253,v10,v11\n"
"v_cvt_pk_bf16_f32 v254,v12,v13\n"
"v_cvt_pk_bf16_f32 v255,v14,v15\n"
"v_lshlrev_b32 v0,16,v248\n"
"v_and_b32 v1,0xffff0000,v248\n"
"v_lshlrev_b32 v2,16,v249\n"
"v_and_b32 v3,0xffff0000,v249\n"
"v_lshlrev_b32 v4,16,v250\n"
"v_and_b32 v5,0xffff0000,v250\n"
"v_lshlrev_b32 v6,16,v251\n"
"v_and_b32 v7,0xffff0000,v251\n"
"v_lshlrev_b32 v8,16,v252\n"
"v_and_b32 v9,0xffff0000,v252\n"
"v_lshlrev_b32 v10,16,v253\n"
"v_and_b32 v11,0xffff0000,v253\n"
"v_lshlrev_b32 v12,16,v254\n"
"v_and_b32 v13,0xffff0000,v254\n"
"v_lshlrev_b32 v14,16,v255\n"
"v_and_b32 v15,0xffff0000,v255\n"
"v_pk_add_f32 v[16:17],v[0:1],v[34:35] op_sel:[0,1] neg_lo:[0,1] neg_hi:[0,1]\n"
"v_pk_add_f32 v[18:19],v[2:3],v[34:35] op_sel:[0,1] neg_lo:[0,1] neg_hi:[0,1]\n"
"v_pk_add_f32 v[20:21],v[4:5],v[34:35] op_sel:[0,1] neg_lo:[0,1] neg_hi:[0,1]\n"
"v_pk_add_f32 v[22:23],v[6:7],v[34:35] op_sel:[0,1] neg_lo:[0,1] neg_hi:[0,1]\n"
"v_pk_add_f32 v[24:25],v[8:9],v[34:35] op_sel:[0,1] neg_lo:[0,1] neg_hi:[0,1]\n"
"v_pk_add_f32 v[26:27],v[10:11],v[34:35] op_sel:[0,1] neg_lo:[0,1] neg_hi:[0,1]\n"
"v_pk_add_f32 v[28:29],v[12:13],v[34:35] op_sel:[0,1] neg_lo:[0,1] neg_hi:[0,1]\n"
"v_pk_add_f32 v[30:31],v[14:15],v[34:35] op_sel:[0,1] neg_lo:[0,1] neg_hi:[0,1]\n"
"v_cvt_pk_bf16_f32 v248,v16,v17\n"
"v_cvt_pk_bf16_f32 v249,v18,v19\n"
"v_cvt_pk_bf16_f32 v250,v20,v21\n"
"v_cvt_pk_bf16_f32 v251,v22,v23\n"
"v_cvt_pk_bf16_f32 v252,v24,v25\n"
"v_cvt_pk_bf16_f32 v253,v26,v27\n"
"v_cvt_pk_bf16_f32 v254,v28,v29\n"
"v_cvt_pk_bf16_f32 v255,v30,v31\n"
"v_lshlrev_b32 v16,16,v248\n"
"v_and_b32 v17,0xffff0000,v248\n"
"v_lshlrev_b32 v18,16,v249\n"
"v_and_b32 v19,0xffff0000,v249\n"
"v_lshlrev_b32 v20,16,v250\n"
"v_and_b32 v21,0xffff0000,v250\n"
"v_lshlrev_b32 v22,16,v251\n"
"v_and_b32 v23,0xffff0000,v251\n"
"v_lshlrev_b32 v24,16,v252\n"
"v_and_b32 v25,0xffff0000,v252\n"
"v_lshlrev_b32 v26,16,v253\n"
"v_and_b32 v27,0xffff0000,v253\n"
"v_lshlrev_b32 v28,16,v254\n"
"v_and_b32 v29,0xffff0000,v254\n"
"v_lshlrev_b32 v30,16,v255\n"
"v_and_b32 v31,0xffff0000,v255\n"
"v_pk_mul_f32 v[16:17],v[16:17],v[34:35] op_sel_hi:[1,0]\n"
"v_pk_mul_f32 v[18:19],v[18:19],v[34:35] op_sel_hi:[1,0]\n"
"v_pk_mul_f32 v[20:21],v[20:21],v[34:35] op_sel_hi:[1,0]\n"
"v_pk_mul_f32 v[22:23],v[22:23],v[34:35] op_sel_hi:[1,0]\n"
"v_pk_mul_f32 v[24:25],v[24:25],v[34:35] op_sel_hi:[1,0]\n"
"v_pk_mul_f32 v[26:27],v[26:27],v[34:35] op_sel_hi:[1,0]\n"
"v_pk_mul_f32 v[28:29],v[28:29],v[34:35] op_sel_hi:[1,0]\n"
"v_pk_mul_f32 v[30:31],v[30:31],v[34:35] op_sel_hi:[1,0]\n"
"v_cvt_pk_bf16_f32 v248,v16,v17\n"
"v_cvt_pk_bf16_f32 v249,v18,v19\n"
"v_cvt_pk_bf16_f32 v250,v20,v21\n"
"v_cvt_pk_bf16_f32 v251,v22,v23\n"
"v_cvt_pk_bf16_f32 v252,v24,v25\n"
"v_cvt_pk_bf16_f32 v253,v26,v27\n"
"v_cvt_pk_bf16_f32 v254,v28,v29\n"
"v_cvt_pk_bf16_f32 v255,v30,v31\n"
"v_max_u32 v208,1,v248\n"
"v_max_u32 v209,1,v249\n"
"v_max_u32 v210,1,v250\n"
"v_max_u32 v211,1,v251\n"
"v_max_u32 v212,1,v252\n"
"v_max_u32 v213,1,v253\n"
"v_max_u32 v214,1,v254\n"
"v_max_u32 v215,1,v255\n"
"s_and_b32 s82,s60,255\n"
"s_lshl_b32 s82,s82,12\n"
"s_cmp_eq_u32 s63,s89\n"
"s_cselect_b32 s83,2048,0\n"
"s_add_u32 s82,s82,s83\n"
"s_add_u32 s82,s82,174063616\n"
"s_cmp_ge_u32 s63,s89\n"
"s_cselect_b32 s82,s82,2146435072\n"
"buffer_store_dwordx4 v[208:211],v112,s[24:27],s82 offen offset:0 sc1\n"
"buffer_store_dwordx4 v[212:215],v112,s[24:27],s82 offen offset:1024 sc1\n"
"s_and_b32 s82,s60,255\n"
"s_lshl_b32 s82,s82,12\n"
"s_cmp_eq_u32 s65,s89\n"
"s_cselect_b32 s83,2048,0\n"
"s_add_u32 s82,s82,s83\n"
"s_add_u32 s82,s82,174063616\n"
"s_cmp_ge_u32 s65,s89\n"
"s_cselect_b32 s82,s82,2146435072\n"
"buffer_store_dwordx4 v[216:219],v112,s[24:27],s82 offen offset:0 sc1\n"
"buffer_store_dwordx4 v[220:223],v112,s[24:27],s82 offen offset:1024 sc1\n"
"v_lshlrev_b32 v16,16,v248\n"
"v_and_b32 v17,0xffff0000,v248\n"
"v_lshlrev_b32 v18,16,v249\n"
"v_and_b32 v19,0xffff0000,v249\n"
"v_lshlrev_b32 v20,16,v250\n"
"v_and_b32 v21,0xffff0000,v250\n"
"v_lshlrev_b32 v22,16,v251\n"
"v_and_b32 v23,0xffff0000,v251\n"
"v_lshlrev_b32 v24,16,v252\n"
"v_and_b32 v25,0xffff0000,v252\n"
"v_lshlrev_b32 v26,16,v253\n"
"v_and_b32 v27,0xffff0000,v253\n"
"v_lshlrev_b32 v28,16,v254\n"
"v_and_b32 v29,0xffff0000,v254\n"
"v_lshlrev_b32 v30,16,v255\n"
"v_and_b32 v31,0xffff0000,v255\n"
"v_pk_add_f32 v[16:17],v[16:17],v[0:1]\n"
"v_pk_add_f32 v[18:19],v[18:19],v[2:3]\n"
"v_pk_add_f32 v[20:21],v[20:21],v[4:5]\n"
"v_pk_add_f32 v[22:23],v[22:23],v[6:7]\n"
"v_pk_add_f32 v[24:25],v[24:25],v[8:9]\n"
"v_pk_add_f32 v[26:27],v[26:27],v[10:11]\n"
"v_pk_add_f32 v[28:29],v[28:29],v[12:13]\n"
"v_pk_add_f32 v[30:31],v[30:31],v[14:15]\n"
"v_cvt_pk_bf16_f32 v248,v16,v17\n"
"v_cvt_pk_bf16_f32 v249,v18,v19\n"
"v_cvt_pk_bf16_f32 v250,v20,v21\n"
"v_cvt_pk_bf16_f32 v251,v22,v23\n"
"v_cvt_pk_bf16_f32 v252,v24,v25\n"
"v_cvt_pk_bf16_f32 v253,v26,v27\n"
"v_cvt_pk_bf16_f32 v254,v28,v29\n"
"v_cvt_pk_bf16_f32 v255,v30,v31\n"
"s_add_u32 s68,s61,2097152\n"
"s_cmp_lt_u32 s63,s89\n"
"s_cselect_b32 s83,s68,2146435072\n"
"s_cselect_b32 s68,2146435072,s68\n"
"buffer_store_dwordx4 v[248:251],v112,s[48:51],s83 offen offset:0 sc1\n"
"buffer_store_dwordx4 v[252:255],v112,s[48:51],s83 offen offset:1024 sc1\n"
"v_lshrrev_b32 v40,6,v112\n"
"v_and_b32 v41,7,v40\n"
"v_lshlrev_b32 v41,5,v41\n"
"v_bfe_u32 v42,v112,4,2\n"
"v_lshlrev_b32 v42,3,v42\n"
"v_lshl_add_u32 v40,v40,11,v42\n"
"s_lshl_b32 s82,s52,9\n"
"v_add_u32 v40,s82,v40\n"
"v_add_u32 v40,131072,v40\n"
"v_xor_b32 v42,0,v41\n"
"v_add_u32 v192,v40,v42\n"
"v_xor_b32 v42,32,v41\n"
"v_add_u32 v193,v40,v42\n"
"v_xor_b32 v42,64,v41\n"
"v_add_u32 v194,v40,v42\n"
"v_xor_b32 v42,96,v41\n"
"v_add_u32 v195,v40,v42\n"
"v_xor_b32 v42,128,v41\n"
"v_add_u32 v196,v40,v42\n"
"v_xor_b32 v42,160,v41\n"
"v_add_u32 v197,v40,v42\n"
"v_xor_b32 v42,192,v41\n"
"v_add_u32 v198,v40,v42\n"
"v_xor_b32 v42,224,v41\n"
"v_add_u32 v199,v40,v42\n"
"v_bfe_u32 v56,v112,4,1\n"
"v_lshrrev_b32 v57,1,v112\n"
"v_and_b32 v57,0x1f0,v57\n"
"s_lshl_b32 s83,s52,13\n"
"s_add_u32 s83,s83,1024\n"
"v_lshl_add_u32 v58,v56,9,s83\n"
"v_lshrrev_b32 v59,8,v112\n"
"v_lshl_add_u32 v59,v59,4,s82\n"
"v_add_u32 v59,174063616,v59\n"
"v_add_u32 v58,v58,v57\n"
"v_add_u32 v57,s82,v57\n"
"v_and_b32 v55,0x7c0,v57\n"
"v_and_b32 v54,0x30,v57\n"
"v_lshl_add_u32 v55,v55,4,v54\n"
"v_lshrrev_b32 v54,2,v55\n"
"v_and_b32 v54,0x0,v54\n"
"v_or_b32 v55,v55,v54\n"
"v_add_u32 v60,s83,v112\n"
"v_add_u32 v61,1024,v112\n"
"v_bfe_u32 v62,v112,5,3\n"
"v_cmp_eq_u32 s[8:9],0,v62\n"
"v_cmp_eq_u32 s[10:11],1,v62\n"
"v_cmp_eq_u32 s[12:13],2,v62\n"
"v_cmp_eq_u32 s[14:15],3,v62\n"
"v_cmp_eq_u32 s[16:17],4,v62\n"
"v_cmp_eq_u32 s[18:19],5,v62\n"
"v_cmp_eq_u32 s[20:21],6,v62\n"
"v_cmp_eq_u32 s[22:23],7,v62\n"
"v_lshlrev_b32 v62,6,v62\n"
"v_lshl_add_u32 v62,v56,12,v62\n"
"v_lshrrev_b32 v63,8,v112\n"
"v_lshl_add_u32 v62,v63,4,v62\n"
"s_lshl_b32 s69,s72,13\n"
"s_lshl_b32 s82,s52,10\n"
"s_add_u32 s69,s69,s82\n"
"s_add_u32 s69,s69,171966464\n"
"v_lshrrev_b32 v63,5,v112\n"
"v_min_u32 v63,27,v63\n"
"v_lshlrev_b32 v63,13,v63\n"
"v_lshl_add_u32 v63,v56,4,v63\n"
"s_mul_i32 s70,s52,458752\n"
"s_lshl_b32 s82,s73,5\n"
"s_add_u32 s70,s70,s82\n"
"s_add_u32 s70,s70,171966464\n"
"s_add_u32 s71,s70,229376\n"
"s_and_b32 s83,s100,7\n"
"s_lshl_b32 s83,s83,12\n"
"s_add_u32 s83,s83,149946368\n"
"v_add_u32 v20,0,v112\n"
"v_lshlrev_b32 v20,0,v20\n"
"v_add_u32 v21,1024,v112\n"
"v_lshlrev_b32 v21,0,v21\n"
"v_add_u32 v22,2048,v112\n"
"v_lshlrev_b32 v22,0,v22\n"
"v_add_u32 v23,3072,v112\n"
"v_lshlrev_b32 v23,0,v23\n"
"s_sleep 20\n"
".Lfk_nn:\n"
"s_mov_b32 s84,0x40000\n"
"buffer_load_dwordx4 v[0:3],v20,s[24:27],s83 offen sc1\n"
"buffer_load_dwordx4 v[4:7],v21,s[24:27],s83 offen sc1\n"
"buffer_load_dwordx4 v[8:11],v22,s[24:27],s83 offen sc1\n"
"buffer_load_dwordx4 v[12:15],v23,s[24:27],s83 offen sc1\n"
"s_waitcnt vmcnt(0)\n"
".Lfk_wc:\n"
"v_xor_b32 v24,s80,v1\n"
"v_xor_b32 v25,s80,v3\n"
"v_xor_b32 v26,s80,v5\n"
"v_xor_b32 v27,s80,v7\n"
"v_xor_b32 v28,s80,v9\n"
"v_xor_b32 v29,s80,v11\n"
"v_xor_b32 v30,s80,v13\n"
"v_xor_b32 v31,s80,v15\n"
"v_or3_b32 v16,v24,v25,v26\n"
"v_or3_b32 v17,v27,v28,v29\n"
"v_or3_b32 v16,v16,v30,v31\n"
"v_or_b32 v16,v16,v17\n"
"v_cmp_ne_u32 vcc,0,v16\n"
"s_nop 0\n"
"s_cbranch_vccz .Lfk_wc_x\n"
"s_sleep 2\n"
"s_mov_b64 exec,vcc\n"
"buffer_load_dwordx4 v[0:3],v20,s[24:27],s83 offen sc1\n"
"buffer_load_dwordx4 v[4:7],v21,s[24:27],s83 offen sc1\n"
"buffer_load_dwordx4 v[8:11],v22,s[24:27],s83 offen sc1\n"
"buffer_load_dwordx4 v[12:15],v23,s[24:27],s83 offen sc1\n"
"s_mov_b64 exec,-1\n"
"s_sub_u32 s84,s84,1\n"
"s_waitcnt vmcnt(0)\n"
"s_cbranch_scc0 .Lfk_wc\n"
".Lfk_wc_x:\n"
"v_min_u32 v16,v0,v4\n"
"v_max_u32 v0,v0,v4\n"
"v_max3_u32 v2,v2,v6,v16\n"
"v_min_u32 v16,v8,v12\n"
"v_max_u32 v8,v8,v12\n"
"v_max3_u32 v10,v10,v14,v16\n"
"v_min_u32 v16,v0,v8\n"
"v_max_u32 v0,v0,v8\n"
"v_max3_u32 v2,v2,v10,v16\n"
"v_cmp_eq_u32 vcc,0,v56\n"
"v_min_u32_dpp v16,v0,v0 quad_perm:[1,0,3,2] row_mask:0xf bank_mask:0xf\n"
"v_max_u32_dpp v0,v0,v0 quad_perm:[1,0,3,2] row_mask:0xf bank_mask:0xf\n"
"v_max_u32_dpp v2,v2,v2 quad_perm:[1,0,3,2] row_mask:0xf bank_mask:0xf\n"
"v_max_u32 v2,v2,v16\n"
"v_min_u32_dpp v16,v0,v0 quad_perm:[2,3,0,1] row_mask:0xf bank_mask:0xf\n"
"v_max_u32_dpp v0,v0,v0 quad_perm:[2,3,0,1] row_mask:0xf bank_mask:0xf\n"
"v_max_u32_dpp v2,v2,v2 quad_perm:[2,3,0,1] row_mask:0xf bank_mask:0xf\n"
"v_max_u32 v2,v2,v16\n"
"v_min_u32_dpp v16,v0,v0 row_half_mirror row_mask:0xf bank_mask:0xf\n"
"v_max_u32_dpp v0,v0,v0 row_half_mirror row_mask:0xf bank_mask:0xf\n"
"v_max_u32_dpp v2,v2,v2 row_half_mirror row_mask:0xf bank_mask:0xf\n"
"v_max_u32 v2,v2,v16\n"
"v_min_u32_dpp v16,v0,v0 row_mirror row_mask:0xf bank_mask:0xf\n"
"v_max_u32_dpp v0,v0,v0 row_mirror row_mask:0xf bank_mask:0xf\n"
"v_max_u32_dpp v2,v2,v2 row_mirror row_mask:0xf bank_mask:0xf\n"
"v_max_u32 v2,v2,v16\n"
"v_min_u32_dpp v16,v0,v0 row_bcast:15 row_mask:0xa bank_mask:0xf\n"
"v_max_u32_dpp v0,v0,v0 row_bcast:15 row_mask:0xa bank_mask:0xf\n"
"v_max_u32_dpp v2,v2,v2 row_bcast:15 row_mask:0xa bank_mask:0xf\n"
"v_max_u32 v2,v2,v16\n"
"v_min_u32_dpp v16,v0,v0 row_bcast:31 row_mask:0xc bank_mask:0xf\n"
"v_max_u32_dpp v0,v0,v0 row_bcast:31 row_mask:0xc bank_mask:0xf\n"
"v_max_u32_dpp v2,v2,v2 row_bcast:31 row_mask:0xc bank_mask:0xf\n"
"v_max_u32 v2,v2,v16\n"
"v_readlane_b32 s90,v0,63\n"
"v_readlane_b32 s92,v2,63\n"
"s_nop 0\n"
"v_mov_b32 v4,s90\n"
"v_mov_b32 v5,s92\n"
"v_cndmask_b32 v6,v5,v4,vcc\n"
"v_not_b32 v7,v6\n"
"v_bfe_u32 v9,v7,0,8\n"
"v_and_b32 v11,0x800,v7\n"
"v_lshl_add_u32 v11,v9,12,v11\n"
"v_add_u32 v11,v11,v59\n"
"buffer_load_dwordx4 v[128:131],v11,s[24:27],0 offen offset:0 sc1\n"
"buffer_load_dwordx4 v[132:135],v11,s[24:27],0 offen offset:64 sc1\n"
"buffer_load_dwordx4 v[136:139],v11,s[24:27],0 offen offset:128 sc1\n"
"buffer_load_dwordx4 v[140:143],v11,s[24:27],0 offen offset:192 sc1\n"
"buffer_load_dwordx4 v[144:147],v11,s[24:27],0 offen offset:256 sc1\n"
"buffer_load_dwordx4 v[148:151],v11,s[24:27],0 offen offset:320 sc1\n"
"buffer_load_dwordx4 v[152:155],v11,s[24:27],0 offen offset:384 sc1\n"
"buffer_load_dwordx4 v[156:159],v11,s[24:27],0 offen offset:448 sc1\n"
"v_and_b32 v7,0x7ff,v7\n"
"v_lshl_add_u32 v8,v7,11,v57\n"
"v_and_b32 v9,15,v7\n"
"v_lshlrev_b32 v9,6,v9\n"
"v_xor_b32 v9,v9,v55\n"
"v_and_b32 v10,0x7f0,v7\n"
"v_lshl_add_u32 v10,v10,11,v9\n"
"s_mov_b32 s82,71303168\n"
"buffer_load_dwordx4 a[0:3],v10,s[24:27],s82 offen sc1\n"
"s_add_u32 s82,s82,4194304\n"
"buffer_load_dwordx4 a[4:7],v10,s[24:27],s82 offen sc1\n"
"s_add_u32 s82,s82,4194304\n"
"buffer_load_dwordx4 a[8:11],v10,s[24:27],s82 offen sc1\n"
"s_add_u32 s82,s82,4194304\n"
"buffer_load_dwordx4 a[12:15],v10,s[24:27],s82 offen sc1\n"
"buffer_load_dwordx4 a[16:19],v8,s[36:39],0 offen\n"
"v_bfe_u32 v9,v7,0,8\n"
"v_bfe_u32 v12,v9,1,3\n"
"v_and_b32 v13,1,v9\n"
"v_and_b32 v9,0xf0,v9\n"
"v_lshl_add_u32 v9,v13,3,v9\n"
"v_add_u32 v9,v12,v9\n"
"v_lshlrev_b32 v9,4,v9\n"
"s_mov_b32 s82,150241280\n"
"buffer_load_dwordx4 v[16:19],v9,s[24:27],s82 offen sc1\n"
"s_barrier\n"
"ds_read_b64_tr_b16 a[224:225],v192 offset:0\n"
"ds_read_b64_tr_b16 a[240:241],v192 offset:256\n"
"ds_read_b64_tr_b16 a[226:227],v193 offset:0\n"
"ds_read_b64_tr_b16 a[242:243],v193 offset:256\n"
"ds_read_b64_tr_b16 a[228:229],v194 offset:0\n"
"ds_read_b64_tr_b16 a[244:245],v194 offset:256\n"
"ds_read_b64_tr_b16 a[230:231],v195 offset:0\n"
"ds_read_b64_tr_b16 a[246:247],v195 offset:256\n"
"ds_read_b64_tr_b16 a[232:233],v196 offset:0\n"
"ds_read_b64_tr_b16 a[248:249],v196 offset:256\n"
"ds_read_b64_tr_b16 a[234:235],v197 offset:0\n"
"ds_read_b64_tr_b16 a[250:251],v197 offset:256\n"
"ds_read_b64_tr_b16 a[236:237],v198 offset:0\n"
"ds_read_b64_tr_b16 a[252:253],v198 offset:256\n"
"ds_read_b64_tr_b16 a[238:239],v199 offset:0\n"
"ds_read_b64_tr_b16 a[254:255],v199 offset:256\n"
"s_mov_b32 s84,0x40000\n"
"s_waitcnt vmcnt(6)\n"
".Lfk_wh:\n"
"v_mfma_f32_16x16x32_bf16 v[40:43],a[160:163],v[128:131],0\n"
"v_min3_u32 v36,v128,v129,v130\n"
"v_min3_u32 v37,v131,v132,v133\n"
"v_mfma_f32_16x16x32_bf16 v[40:43],a[164:167],v[132:135],v[40:43]\n"
"v_min3_u32 v36,v36,v134,v135\n"
"v_min3_u32 v37,v37,v136,v137\n"
"v_mfma_f32_16x16x32_bf16 v[40:43],a[168:171],v[136:139],v[40:43]\n"
"v_min3_u32 v36,v36,v138,v139\n"
"v_min3_u32 v37,v37,v140,v141\n"
"v_mfma_f32_16x16x32_bf16 v[40:43],a[172:175],v[140:143],v[40:43]\n"
"v_min3_u32 v36,v36,v142,v143\n"
"v_min3_u32 v37,v37,v144,v145\n"
"v_mfma_f32_16x16x32_bf16 v[40:43],a[176:179],v[144:147],v[40:43]\n"
"v_min3_u32 v36,v36,v146,v147\n"
"v_min3_u32 v37,v37,v148,v149\n"
"v_mfma_f32_16x16x32_bf16 v[40:43],a[180:183],v[148:151],v[40:43]\n"
"v_min3_u32 v36,v36,v150,v151\n"
"v_min3_u32 v37,v37,v152,v153\n"
"v_mfma_f32_16x16x32_bf16 v[40:43],a[184:187],v[152:155],v[40:43]\n"
"v_min3_u32 v36,v36,v154,v155\n"
"v_min3_u32 v37,v37,v156,v157\n"
"v_mfma_f32_16x16x32_bf16 v[40:43],a[188:191],v[156:159],v[40:43]\n"
"v_min3_u32 v36,v36,v158,v159\n"
"v_min_u32 v36,v36,v37\n"
"v_cmp_eq_u32 vcc,0,v36\n"
"v_mfma_f32_16x16x32_bf16 v[44:47],a[192:195],v[128:131],0\n"
"s_cbranch_vccz .Lfk_wh_x\n"
"s_sleep 1\n"
"s_mov_b64 exec,vcc\n"
"buffer_load_dwordx4 v[128:131],v11,s[24:27],0 offen offset:0 sc1\n"
"buffer_load_dwordx4 v[132:135],v11,s[24:27],0 offen offset:64 sc1\n"
"buffer_load_dwordx4 v[136:139],v11,s[24:27],0 offen offset:128 sc1\n"
"buffer_load_dwordx4 v[140:143],v11,s[24:27],0 offen offset:192 sc1\n"
"buffer_load_dwordx4 v[144:147],v11,s[24:27],0 offen offset:256 sc1\n"
"buffer_load_dwordx4 v[148:151],v11,s[24:27],0 offen offset:320 sc1\n"
"buffer_load_dwordx4 v[152:155],v11,s[24:27],0 offen offset:384 sc1\n"
"buffer_load_dwordx4 v[156:159],v11,s[24:27],0 offen offset:448 sc1\n"
"s_mov_b64 exec,-1\n"
"s_sub_u32 s84,s84,1\n"
"s_waitcnt vmcnt(0)\n"
"s_cbranch_scc0 .Lfk_wh\n"
".Lfk_wh_x:\n"
"v_mfma_f32_16x16x32_bf16 v[44:47],a[196:199],v[132:135],v[44:47]\n"
"v_mfma_f32_16x16x32_bf16 v[44:47],a[200:203],v[136:139],v[44:47]\n"
"v_mfma_f32_16x16x32_bf16 v[44:47],a[204:207],v[140:143],v[44:47]\n"
"ds_write_b128 v60,v[40:43] offset:1024\n"
"v_mfma_f32_16x16x32_bf16 v[44:47],a[208:211],v[144:147],v[44:47]\n"
"v_mfma_f32_16x16x32_bf16 v[44:47],a[212:215],v[148:151],v[44:47]\n"
"v_mfma_f32_16x16x32_bf16 v[44:47],a[216:219],v[152:155],v[44:47]\n"
"v_mfma_f32_16x16x32_bf16 v[44:47],a[220:223],v[156:159],v[44:47]\n"
"s_nop 7\n"
"ds_write_b128 v60,v[44:47] offset:2048\n"
"s_waitcnt lgkmcnt(0)\n"
"s_barrier\n"
"ds_read_b128 v[128:131],v61 offset:1024\n"
"ds_read_b128 v[144:147],v61 offset:2048\n"
"ds_read_b128 v[132:135],v61 offset:9216\n"
"ds_read_b128 v[148:151],v61 offset:10240\n"
"ds_read_b128 v[136:139],v61 offset:17408\n"
"ds_read_b128 v[152:155],v61 offset:18432\n"
"ds_read_b128 v[140:143],v61 offset:25600\n"
"ds_read_b128 v[156:159],v61 offset:26624\n"
"s_waitcnt lgkmcnt(0)\n"
"v_pk_add_f32 v[128:129],v[128:129],v[132:133]\n"
"v_pk_add_f32 v[136:137],v[136:137],v[140:141]\n"
"v_pk_add_f32 v[130:131],v[130:131],v[134:135]\n"
"v_pk_add_f32 v[138:139],v[138:139],v[142:143]\n"
"v_pk_add_f32 v[128:129],v[128:129],v[136:137]\n"
"v_pk_add_f32 v[130:131],v[130:131],v[138:139]\n"
"v_pk_add_f32 v[144:145],v[144:145],v[148:149]\n"
"v_pk_add_f32 v[152:153],v[152:153],v[156:157]\n"
"v_pk_add_f32 v[146:147],v[146:147],v[150:151]\n"
"v_pk_add_f32 v[154:155],v[154:155],v[158:159]\n"
"v_pk_add_f32 v[144:145],v[144:145],v[152:153]\n"
"v_pk_add_f32 v[146:147],v[146:147],v[154:155]\n"
"v_mul_f32 v132,0xbfb8aa3b,v128\n"
"v_mul_f32 v133,0xbfb8aa3b,v129\n"
"v_mul_f32 v134,0xbfb8aa3b,v130\n"
"v_mul_f32 v135,0xbfb8aa3b,v131\n"
"v_exp_f32 v132,v132\n"
"v_exp_f32 v133,v133\n"
"v_exp_f32 v134,v134\n"
"v_exp_f32 v135,v135\n"
"v_add_f32 v132,1.0,v132\n"
"v_add_f32 v133,1.0,v133\n"
"v_add_f32 v134,1.0,v134\n"
"v_add_f32 v135,1.0,v135\n"
"v_rcp_f32 v132,v132\n"
"v_rcp_f32 v133,v133\n"
"v_rcp_f32 v134,v134\n"
"v_rcp_f32 v135,v135\n"
"v_pk_mul_f32 v[128:129],v[128:129],v[132:133]\n"
"v_pk_mul_f32 v[130:131],v[130:131],v[134:135]\n"
"v_pk_mul_f32 v[128:129],v[128:129],v[144:145]\n"
"v_pk_mul_f32 v[130:131],v[130:131],v[146:147]\n"
"v_cvt_pk_bf16_f32 v132,v128,v129\n"
"v_cvt_pk_bf16_f32 v133,v130,v131\n"
"v_cndmask_b32 v136,0,v132,s[8:9]\n"
"v_cndmask_b32 v137,0,v133,s[8:9]\n"
"v_cndmask_b32 v138,0,v132,s[10:11]\n"
"v_cndmask_b32 v139,0,v133,s[10:11]\n"
"v_cndmask_b32 v141,0,v133,s[12:13]\n"
"v_cndmask_b32 v143,0,v133,s[14:15]\n"
"v_mfma_f32_16x16x32_bf16 v[40:43],a[224:227],v[136:139],0\n"
"v_cndmask_b32 v142,0,v132,s[14:15]\n"
"v_cndmask_b32 v140,0,v132,s[12:13]\n"
"v_mfma_f32_16x16x32_bf16 v[44:47],a[240:243],v[136:139],0\n"
"v_cndmask_b32 v145,0,v133,s[16:17]\n"
"v_cndmask_b32 v147,0,v133,s[18:19]\n"
"v_mfma_f32_16x16x32_bf16 v[40:43],a[228:231],v[140:143],v[40:43]\n"
"v_cndmask_b32 v146,0,v132,s[18:19]\n"
"v_cndmask_b32 v144,0,v132,s[16:17]\n"
"v_mfma_f32_16x16x32_bf16 v[44:47],a[244:247],v[140:143],v[44:47]\n"
"v_cndmask_b32 v149,0,v133,s[20:21]\n"
"v_cndmask_b32 v151,0,v133,s[22:23]\n"
"v_mfma_f32_16x16x32_bf16 v[40:43],a[232:235],v[144:147],v[40:43]\n"
"v_cndmask_b32 v150,0,v132,s[22:23]\n"
"v_cndmask_b32 v148,0,v132,s[20:21]\n"
"v_mfma_f32_16x16x32_bf16 v[44:47],a[248:251],v[144:147],v[44:47]\n"
"s_nop 0\n"
"v_mfma_f32_16x16x32_bf16 v[40:43],a[236:239],v[148:151],v[40:43]\n"
"v_mfma_f32_16x16x32_bf16 v[44:47],a[252:255],v[148:151],v[44:47]\n"
"s_or_b32 s82,s90,0x800\n"
"s_cmp_eq_u32 s63,s82\n"
"s_nop 4\n"
"v_or_b32 v40,1,v40\n"
"v_or_b32 v41,1,v41\n"
"v_or_b32 v42,1,v42\n"
"v_or_b32 v43,1,v43\n"
"v_or_b32 v44,1,v44\n"
"v_or_b32 v45,1,v45\n"
"v_or_b32 v46,1,v46\n"
"v_or_b32 v47,1,v47\n"
"buffer_store_dwordx4 v[40:43],v62,s[24:27],s69 offen offset:0 sc1\n"
"buffer_store_dwordx4 v[44:47],v62,s[24:27],s69 offen offset:512 sc1\n"
"s_cselect_b32 s68,2146435072,s68\n"
"s_or_b32 s82,s92,0x800\n"
"s_cmp_eq_u32 s63,s82\n"
"s_cselect_b32 s68,2146435072,s68\n"
"buffer_store_dwordx4 v[248:251],v112,s[48:51],s68 offen offset:0 sc1\n"
"buffer_store_dwordx4 v[252:255],v112,s[48:51],s68 offen offset:1024 sc1\n"
"s_cmp_eq_u32 s60,0\n"
"s_cselect_b32 s82,s75,2146435072\n"
"buffer_store_dwordx2 a[154:155],off,s[24:27],s82 sc1\n"
"s_mov_b32 s84,0x40000\n"
"s_waitcnt vmcnt(5)\n"
".Lfk_ws:\n"
"v_xor_b32 v20,s80,v17\n"
"v_xor_b32 v21,s80,v19\n"
"v_or_b32 v20,v20,v21\n"
"v_cmp_ne_u32 vcc,0,v20\n"
"s_nop 0\n"
"s_cbranch_vccz .Lfk_ws_x\n"
"s_mov_b64 exec,vcc\n"
"s_mov_b32 s82,150241280\n"
"buffer_load_dwordx4 v[16:19],v9,s[24:27],s82 offen sc1\n"
"s_mov_b64 exec,-1\n"
"s_sub_u32 s84,s84,1\n"
"s_waitcnt vmcnt(0)\n"
"s_cbranch_scc0 .Lfk_ws\n"
".Lfk_ws_x:\n"
"v_mfma_f32_16x16x32_bf16 v[24:27],v[66:69],a[0:3],0\n"
"v_mfma_f32_16x16x32_bf16 v[28:31],v[64:67],a[0:3],0\n"
"v_mfma_f32_16x16x32_bf16 v[24:27],v[66:69],a[4:7],v[24:27]\n"
"v_mfma_f32_16x16x32_bf16 v[28:31],v[64:67],a[4:7],v[28:31]\n"
"v_mfma_f32_16x16x32_bf16 v[24:27],v[66:69],a[8:11],v[24:27]\n"
"v_mfma_f32_16x16x32_bf16 v[28:31],v[64:67],a[8:11],v[28:31]\n"
"v_mfma_f32_16x16x32_bf16 v[24:27],v[66:69],a[12:15],v[24:27]\n"
"v_mfma_f32_16x16x32_bf16 v[28:31],v[64:67],a[12:15],v[28:31]\n"
"v_mfma_f32_16x16x32_bf16 v[24:27],v[66:69],a[16:19],v[24:27]\n"
"v_mfma_f32_16x16x32_bf16 v[28:31],v[64:67],a[16:19],v[28:31]\n"
"v_and_b32 v12,0x800,v6\n"
"v_cmp_ne_u32 vcc,0,v12\n"
"s_nop 1\n"
"v_cndmask_b32 v10,v18,v16,vcc\n"
"v_lshlrev_b32 v11,16,v10\n"
"v_rcp_f32 v11,v11\n"
"v_and_b32 v10,0xffff0000,v10\n"
"v_cvt_pk_bf16_f32 v20,v24,v25\n"
"v_cvt_pk_bf16_f32 v21,v26,v27\n"
"v_cvt_pk_bf16_f32 v22,v28,v29\n"
"v_cvt_pk_bf16_f32 v23,v30,v31\n"
"v_lshlrev_b32 v24,16,v20\n"
"v_and_b32 v25,0xffff0000,v20\n"
"v_lshlrev_b32 v26,16,v21\n"
"v_and_b32 v27,0xffff0000,v21\n"
"v_lshlrev_b32 v28,16,v22\n"
"v_and_b32 v29,0xffff0000,v22\n"
"v_lshlrev_b32 v30,16,v23\n"
"v_and_b32 v31,0xffff0000,v23\n"
"v_pk_add_f32 v[32:33],v[24:25],v[10:11] op_sel_hi:[1,0] neg_lo:[0,1] neg_hi:[0,1]\n"
"v_pk_add_f32 v[34:35],v[26:27],v[10:11] op_sel_hi:[1,0] neg_lo:[0,1] neg_hi:[0,1]\n"
"v_pk_add_f32 v[36:37],v[28:29],v[10:11] op_sel_hi:[1,0] neg_lo:[0,1] neg_hi:[0,1]\n"
"v_pk_add_f32 v[38:39],v[30:31],v[10:11] op_sel_hi:[1,0] neg_lo:[0,1] neg_hi:[0,1]\n"
"v_cvt_pk_bf16_f32 v20,v32,v33\n"
"v_cvt_pk_bf16_f32 v21,v34,v35\n"
"v_cvt_pk_bf16_f32 v22,v36,v37\n"
"v_cvt_pk_bf16_f32 v23,v38,v39\n"
"v_lshlrev_b32 v32,16,v20\n"
"v_and_b32 v33,0xffff0000,v20\n"
"v_lshlrev_b32 v34,16,v21\n"
"v_and_b32 v35,0xffff0000,v21\n"
"v_lshlrev_b32 v36,16,v22\n"
"v_and_b32 v37,0xffff0000,v22\n"
"v_lshlrev_b32 v38,16,v23\n"
"v_and_b32 v39,0xffff0000,v23\n"
"v_pk_mul_f32 v[32:33],v[32:33],v[10:11] op_sel:[0,1]\n"
"v_pk_mul_f32 v[34:35],v[34:35],v[10:11] op_sel:[0,1]\n"
"v_pk_mul_f32 v[36:37],v[36:37],v[10:11] op_sel:[0,1]\n"
"v_pk_mul_f32 v[38:39],v[38:39],v[10:11] op_sel:[0,1]\n"
"v_cvt_pk_bf16_f32 v20,v32,v33\n"
"v_cvt_pk_bf16_f32 v21,v34,v35\n"
"v_cvt_pk_bf16_f32 v22,v36,v37\n"
"v_cvt_pk_bf16_f32 v23,v38,v39\n"
"ds_write_b128 v58,v[20:23]\n"
"ds_write_b128 v60,v[24:27] offset:3072\n"
"ds_write_b128 v60,v[28:31] offset:4096\n"
"s_bfe_u32 s82,s73,0x20005\n"
"s_lshl_b32 s82,s82,13\n"
"s_and_b32 s83,s73,31\n"
"s_lshr_b32 s91,s73,7\n"
"s_lshl_b32 s89,s91,9\n"
"s_lshl4_add_u32 s89,s83,s89\n"
"s_add_u32 s89,s89,s82\n"
"s_add_u32 s89,s89,1024\n"
"s_lshl1_add_u32 s83,s83,s91\n"
"s_lshl4_add_u32 s82,s83,s82\n"
"s_add_u32 s82,s82,4096\n"
"v_bfe_u32 v0,v112,4,3\n"
"v_lshl_add_u32 v1,v0,1,s89\n"
"v_and_b32 v3,3,v0\n"
"v_lshl_add_u32 v3,v3,2,s82\n"
"v_lshrrev_b32 v4,2,v0\n"
"v_lshl_add_u32 v3,v4,10,v3\n"
"s_cmp_eq_u32 s91,0\n"
"s_cselect_b32 s82,s90,s92\n"
"s_not_b32 s83,s82\n"
"s_and_b32 s89,s83,0x7ff\n"
"s_lshl_b32 s89,s89,11\n"
"s_and_b32 s91,s73,127\n"
"s_lshl4_add_u32 s89,s91,s89\n"
"s_ashr_i32 s83,s83,31\n"
"s_or_b32 s83,s83,0x80000000\n"
"s_xor_b32 s82,s82,s83\n"
"s_and_b32 s82,s82,0xffff0000\n"
"v_mov_b32 v5,0xbfb8aa3b\n"
"v_mul_f32 v5,s82,v5\n"
"v_exp_f32 v5,v5\n"
"s_nop 0\n"
"v_add_f32 v5,1.0,v5\n"
"v_rcp_f32 v5,v5\n"
"s_nop 0\n"
"v_cvt_pk_bf16_f32 v5,v5,v5\n"
"v_lshlrev_b32 v5,16,v5\n"
"s_lshl_b32 s82,s52,7\n"
"s_add_u32 s82,s82,64\n"
"v_lshrrev_b32 v6,3,v112\n"
"v_and_b32 v6,0x60,v6\n"
"v_and_b32 v7,16,v112\n"
"v_add3_u32 v6,v6,v7,s82\n"
"v_lshrrev_b32 v7,2,v112\n"
"v_add_u32 v7,64,v7\n"
"v_lshlrev_b32 v8,1,v0\n"
"s_sleep 1\n"
"s_mov_b32 s84,0x40000\n"
"buffer_load_dwordx4 v[16:19],v63,s[24:27],s70 offen sc1\n"
"buffer_load_dwordx4 v[20:23],v63,s[24:27],s71 offen sc1\n"
"s_waitcnt vmcnt(0)\n"
".Lfk_wy:\n"
"v_min3_u32 v24,v16,v17,v18\n"
"v_min3_u32 v25,v19,v20,v21\n"
"v_min3_u32 v24,v24,v22,v23\n"
"v_min_u32 v24,v24,v25\n"
"v_cmp_eq_u32 vcc,0,v24\n"
"s_nop 0\n"
"s_cbranch_vccz .Lfk_wy_x\n"
"s_sleep 1\n"
"s_mov_b64 exec,vcc\n"
"buffer_load_dwordx4 v[16:19],v63,s[24:27],s70 offen sc1\n"
"buffer_load_dwordx4 v[20:23],v63,s[24:27],s71 offen sc1\n"
"s_mov_b64 exec,-1\n"
"s_sub_u32 s84,s84,1\n"
"s_waitcnt vmcnt(0)\n"
"s_cbranch_scc0 .Lfk_wy\n"
".Lfk_wy_x:\n"
"s_mov_b32 vcc_lo,-1\n"
"s_mov_b32 vcc_hi,0x00ffffff\n"
"v_add_f32 v16,v16,v20\n"
"v_add_f32 v17,v17,v21\n"
"v_add_f32 v18,v18,v22\n"
"v_add_f32 v19,v19,v23\n"
"v_cndmask_b32 v16,0,v16,vcc\n"
"v_cndmask_b32 v17,0,v17,vcc\n"
"v_cndmask_b32 v18,0,v18,vcc\n"
"v_cndmask_b32 v19,0,v19,vcc\n"
"v_add_f32_dpp v16,v16,v16 quad_perm:[2,3,0,1] row_mask:0xf bank_mask:0xf\n"
"v_add_f32_dpp v17,v17,v17 quad_perm:[2,3,0,1] row_mask:0xf bank_mask:0xf\n"
"v_add_f32_dpp v18,v18,v18 quad_perm:[2,3,0,1] row_mask:0xf bank_mask:0xf\n"
"v_add_f32_dpp v19,v19,v19 quad_perm:[2,3,0,1] row_mask:0xf bank_mask:0xf\n"
"v_add_f32_dpp v16,v16,v16 row_ror:4 row_mask:0xf bank_mask:0xf\n"
"v_add_f32_dpp v17,v17,v17 row_ror:4 row_mask:0xf bank_mask:0xf\n"
"v_add_f32_dpp v18,v18,v18 row_ror:4 row_mask:0xf bank_mask:0xf\n"
"v_add_f32_dpp v19,v19,v19 row_ror:4 row_mask:0xf bank_mask:0xf\n"
"v_add_f32_dpp v16,v16,v16 row_ror:8 row_mask:0xf bank_mask:0xf\n"
"v_add_f32_dpp v17,v17,v17 row_ror:8 row_mask:0xf bank_mask:0xf\n"
"v_add_f32_dpp v18,v18,v18 row_ror:8 row_mask:0xf bank_mask:0xf\n"
"v_add_f32_dpp v19,v19,v19 row_ror:8 row_mask:0xf bank_mask:0xf\n"
"s_mov_b32 exec_lo,0x30003\n"
"s_mov_b32 exec_hi,0x30003\n"
"ds_write_b128 v6,v[16:19]\n"
"s_mov_b64 exec,-1\n"
"s_waitcnt lgkmcnt(0)\n"
"s_barrier\n"
"s_cmp_lg_u32 s52,0\n"
"s_cbranch_scc1 .Lfk_t2\n"
"ds_read2_b32 v[16:17],v7 offset0:0 offset1:64\n"
"ds_read_u16 v2,v1\n"
"ds_read_b32 v3,v3\n"
"s_waitcnt lgkmcnt(0)\n"
"v_lshlrev_b32 v2,16,v2\n"
"v_add_f32 v16,v16,v17\n"
"s_nop 1\n"
"v_add_f32_dpp v16,v16,v16 row_ror:8 row_mask:0xf bank_mask:0xf\n"
"v_mov_b32 v17,v16\n"
"s_nop 1\n"
"v_permlane16_swap_b32 v17,v16\n"
"v_add_f32 v16,v16,v17\n"
"v_mov_b32 v17,v16\n"
"s_nop 1\n"
"v_permlane32_swap_b32 v17,v16\n"
"v_add_f32 v16,v16,v17\n"
"v_cvt_pk_bf16_f32 v16,v16,v16\n"
"v_lshlrev_b32 v16,16,v16\n"
"v_mul_f32 v16,v16,v5\n"
"v_cvt_pk_bf16_f32 v16,v16,v16\n"
"v_lshlrev_b32 v16,16,v16\n"
"v_add_f32 v16,v16,v2\n"
"v_cvt_pk_bf16_f32 v16,v16,v16\n"
"v_lshlrev_b32 v16,16,v16\n"
"v_add_f32 v16,v16,v3\n"
"s_nop 1\n"
"v_mov_b32_dpp v17,v16 quad_perm:[1,0,3,2] row_mask:0xf bank_mask:0xf\n"
"v_cvt_pk_bf16_f32 v16,v16,v17\n"
"s_mov_b64 exec,0x55\n"
"buffer_store_dword v16,v8,s[48:51],s89 offen sc1\n"
"s_mov_b64 exec,-1\n"
".Lfk_t2:\n"
"s_branch .Lfk_end\n"
".Lfk_g0:\n"
"s_waitcnt vmcnt(11) lgkmcnt(0)\n"
"s_barrier\n"
"v_mfma_f32_16x16x32_bf16 v[32:35],a[96:99],a[0:3],v[96:99]\n"
"s_add_u32 m0,s55,4096\n"
"v_exp_f32 v0,v0\n"
"v_mfma_f32_16x16x32_bf16 v[32:35],a[100:103],a[4:7],v[32:35]\n"
"s_add_u32 s54,s54,s22\n"
"v_exp_f32 v1,v1\n"
"v_mfma_f32_16x16x32_bf16 v[32:35],a[104:107],a[8:11],v[32:35]\n"
"buffer_load_dwordx4 v115,s[32:35],s54 offen sc1 lds\n"
"v_exp_f32 v2,v2\n"
"v_cvt_pk_bf16_f32 v64,v0,v1\n"
"v_mfma_f32_16x16x32_bf16 v[32:35],a[108:111],a[12:15],v[32:35]\n"
"v_exp_f32 v3,v3\n"
"v_mfma_f32_16x16x32_bf16 v[36:39],a[124:127],a[12:15],v[96:99]\n"
"v_exp_f32 v4,v4\n"
"v_cvt_pk_bf16_f32 v65,v2,v3\n"
"v_mfma_f32_16x16x32_bf16 v[36:39],a[120:123],a[8:11],v[36:39]\n"
"v_exp_f32 v5,v5\n"
"v_mfma_f32_16x16x32_bf16 v[36:39],a[116:119],a[4:7],v[36:39]\n"
"v_exp_f32 v6,v6\n"
"v_cvt_pk_bf16_f32 v66,v4,v5\n"
"v_mfma_f32_16x16x32_bf16 v[36:39],a[112:115],a[0:3],v[36:39]\n"
"v_exp_f32 v7,v7\n"
"v_mfma_f32_16x16x32_bf16 v[44:47],a[112:115],a[16:19],v[100:103]\n"
"v_exp_f32 v8,v8\n"
"v_cvt_pk_bf16_f32 v67,v6,v7\n"
"v_exp_f32 v9,v9\n"
"v_mfma_f32_16x16x32_bf16 v[44:47],a[116:119],a[20:23],v[44:47]\n"
"ds_read_b128 a[64:67],v113 offset:49152\n"
"ds_read_b128 a[68:71],v113 offset:50176\n"
"v_exp_f32 v10,v10\n"
"v_mfma_f32_16x16x32_bf16 v[44:47],a[120:123],a[24:27],v[44:47]\n"
"ds_read_b128 a[72:75],v113 offset:51200\n"
"ds_read_b128 a[76:79],v113 offset:52224\n"
"v_exp_f32 v11,v11\n"
"v_mfma_f32_16x16x32_bf16 v[44:47],a[124:127],a[28:31],v[44:47]\n"
"ds_read_b128 a[80:83],v113 offset:53248\n"
"ds_read_b128 a[84:87],v113 offset:54272\n"
"v_exp_f32 v12,v12\n"
"v_mfma_f32_16x16x32_bf16 v[40:43],a[108:111],a[28:31],v[100:103]\n"
"ds_read_b128 a[88:91],v113 offset:55296\n"
"ds_read_b128 a[92:95],v113 offset:56320\n"
"v_mfma_f32_16x16x32_bf16 v[40:43],a[104:107],a[24:27],v[40:43]\n"
"v_exp_f32 v13,v13\n"
"v_cvt_pk_bf16_f32 v68,v8,v9\n"
"v_cvt_pk_bf16_f32 v69,v10,v11\n"
"v_mfma_f32_16x16x32_bf16 v[40:43],a[100:103],a[20:23],v[40:43]\n"
"v_exp_f32 v14,v14\n"
"v_cvt_pk_bf16_f32 v70,v12,v13\n"
"v_mfma_f32_16x16x32_bf16 v[40:43],a[96:99],a[16:19],v[40:43]\n"
"v_exp_f32 v15,v15\n"
"v_mfma_f32_16x16x32_bf16 v[48:51],a[96:99],a[32:35],v[104:107]\n"
"v_exp_f32 v16,v16\n"
"v_cvt_pk_bf16_f32 v71,v14,v15\n"
"v_mfma_f32_16x16x32_bf16 v[48:51],a[100:103],a[36:39],v[48:51]\n"
"v_exp_f32 v17,v17\n"
"v_mfma_f32_16x16x32_bf16 v[48:51],a[104:107],a[40:43],v[48:51]\n"
"v_exp_f32 v18,v18\n"
"v_cvt_pk_bf16_f32 v72,v16,v17\n"
"v_mfma_f32_16x16x32_bf16 v[48:51],a[108:111],a[44:47],v[48:51]\n"
"v_exp_f32 v19,v19\n"
"v_mfma_f32_16x16x32_bf16 v[52:55],a[124:127],a[44:47],v[104:107]\n"
"v_exp_f32 v20,v20\n"
"v_cvt_pk_bf16_f32 v73,v18,v19\n"
"v_mfma_f32_16x16x32_bf16 v[52:55],a[120:123],a[40:43],v[52:55]\n"
"v_exp_f32 v21,v21\n"
"v_mfma_f32_16x16x32_bf16 v[52:55],a[116:119],a[36:39],v[52:55]\n"
"v_exp_f32 v22,v22\n"
"v_cvt_pk_bf16_f32 v74,v20,v21\n"
"v_mfma_f32_16x16x32_bf16 v[52:55],a[112:115],a[32:35],v[52:55]\n"
"v_exp_f32 v23,v23\n"
"v_mfma_f32_16x16x32_bf16 v[60:63],a[112:115],a[48:51],v[108:111]\n"
"v_exp_f32 v24,v24\n"
"v_cvt_pk_bf16_f32 v75,v22,v23\n"
"v_mfma_f32_16x16x32_bf16 v[60:63],a[116:119],a[52:55],v[60:63]\n"
"v_exp_f32 v25,v25\n"
"v_mfma_f32_16x16x32_bf16 v[60:63],a[120:123],a[56:59],v[60:63]\n"
"v_exp_f32 v26,v26\n"
"v_exp_f32 v27,v27\n"
"v_mfma_f32_16x16x32_bf16 v[60:63],a[124:127],a[60:63],v[60:63]\n"
"buffer_store_dwordx4 a[228:231],v112,s[24:27],s56 offen offset:1024 sc1\n"
"v_mfma_f32_16x16x32_bf16 v[56:59],a[108:111],a[60:63],v[108:111]\n"
"v_exp_f32 v28,v28\n"
"v_cvt_pk_bf16_f32 v77,v26,v27\n"
"v_mfma_f32_16x16x32_bf16 v[56:59],a[104:107],a[56:59],v[56:59]\n"
"v_exp_f32 v29,v29\n"
"v_mfma_f32_16x16x32_bf16 v[56:59],a[100:103],a[52:55],v[56:59]\n"
"v_exp_f32 v30,v30\n"
"v_cvt_pk_bf16_f32 v78,v28,v29\n"
"v_mfma_f32_16x16x32_bf16 v[56:59],a[96:99],a[48:51],v[56:59]\n"
"s_waitcnt vmcnt(9) lgkmcnt(0)\n"
"v_exp_f32 v31,v31\n"
"v_cvt_pk_bf16_f32 v76,v24,v25\n"
"v_cvt_pk_bf16_f32 v79,v30,v31\n"
"s_barrier\n"
"v_mfma_f32_16x16x32_bf16 v[0:3],a[64:67],a[0:3],v[96:99]\n"
"s_add_u32 m0,s55,8192\n"
"v_exp_f32 v122,v32\n"
"v_mfma_f32_16x16x32_bf16 v[0:3],a[68:71],a[4:7],v[0:3]\n"
"s_add_u32 s54,s54,s22\n"
"v_exp_f32 v123,v33\n"
"v_mfma_f32_16x16x32_bf16 v[0:3],a[72:75],a[8:11],v[0:3]\n"
"ds_read_b128 a[96:99],v114 offset:40960\n"
"ds_read_b128 a[100:103],v114 offset:41984\n"
"v_mfma_f32_16x16x32_bf16 v[0:3],a[76:79],a[12:15],v[0:3]\n"
"v_exp_f32 v119,v34\n"
"v_mfma_f32_16x16x32_bf16 v[4:7],a[92:95],a[12:15],v[96:99]\n"
"ds_read_b128 a[104:107],v114 offset:43008\n"
"v_cvt_pk_bf16_f32 v80,v122,v123\n"
"v_mfma_f32_16x16x32_bf16 v[4:7],a[88:91],a[8:11],v[4:7]\n"
"v_exp_f32 v120,v35\n"
"v_mfma_f32_16x16x32_bf16 v[4:7],a[84:87],a[4:7],v[4:7]\n"
"ds_read_b128 a[108:111],v114 offset:44032\n"
"ds_read_b128 a[112:115],v114 offset:45056\n"
"v_mfma_f32_16x16x32_bf16 v[4:7],a[80:83],a[0:3],v[4:7]\n"
"v_exp_f32 v122,v36\n"
"v_mfma_f32_16x16x32_bf16 v[12:15],a[80:83],a[16:19],v[100:103]\n"
"ds_read_b128 a[116:119],v114 offset:46080\n"
"v_cvt_pk_bf16_f32 v81,v119,v120\n"
"v_mfma_f32_16x16x32_bf16 v[12:15],a[84:87],a[20:23],v[12:15]\n"
"v_exp_f32 v123,v37\n"
"v_mfma_f32_16x16x32_bf16 v[12:15],a[88:91],a[24:27],v[12:15]\n"
"ds_read_b128 a[120:123],v114 offset:47104\n"
"ds_read_b128 a[124:127],v114 offset:48128\n"
"v_mfma_f32_16x16x32_bf16 v[12:15],a[92:95],a[28:31],v[12:15]\n"
"v_exp_f32 v119,v38\n"
"v_cvt_pk_bf16_f32 v82,v122,v123\n"
"v_mfma_f32_16x16x32_bf16 v[8:11],a[76:79],a[28:31],v[100:103]\n"
"buffer_store_dwordx4 a[168:171],v112,s[24:27],s3 offen offset:2048 sc1\n"
"v_mfma_f32_16x16x32_bf16 v[8:11],a[72:75],a[24:27],v[8:11]\n"
"v_exp_f32 v120,v39\n"
"v_mfma_f32_16x16x32_bf16 v[8:11],a[68:71],a[20:23],v[8:11]\n"
"buffer_load_dwordx4 v115,s[32:35],s54 offen sc1 lds\n"
"v_mfma_f32_16x16x32_bf16 v[8:11],a[64:67],a[16:19],v[8:11]\n"
"v_exp_f32 v122,v40\n"
"v_mfma_f32_16x16x32_bf16 v[16:19],a[64:67],a[32:35],v[104:107]\n"
"v_cvt_pk_bf16_f32 v83,v119,v120\n"
"v_or3_b32 v118,v80,v81,v82\n"
"v_mfma_f32_16x16x32_bf16 v[16:19],a[68:71],a[36:39],v[16:19]\n"
"v_exp_f32 v123,v41\n"
"v_mfma_f32_16x16x32_bf16 v[16:19],a[72:75],a[40:43],v[16:19]\n"
"v_cvt_pk_bf16_f32 v84,v122,v123\n"
"v_or3_b32 v118,v118,v83,v84\n"
"v_mfma_f32_16x16x32_bf16 v[16:19],a[76:79],a[44:47],v[16:19]\n"
"v_exp_f32 v119,v42\n"
"v_mfma_f32_16x16x32_bf16 v[20:23],a[92:95],a[44:47],v[104:107]\n"
"v_mfma_f32_16x16x32_bf16 v[20:23],a[88:91],a[40:43],v[20:23]\n"
"v_exp_f32 v120,v43\n"
"v_mfma_f32_16x16x32_bf16 v[20:23],a[84:87],a[36:39],v[20:23]\n"
"v_cvt_pk_bf16_f32 v85,v119,v120\n"
"v_mfma_f32_16x16x32_bf16 v[20:23],a[80:83],a[32:35],v[20:23]\n"
"v_exp_f32 v122,v44\n"
"v_mfma_f32_16x16x32_bf16 v[28:31],a[80:83],a[48:51],v[108:111]\n"
"buffer_store_dwordx4 a[172:175],v112,s[24:27],s3 offen offset:3072 sc1\n"
"v_mfma_f32_16x16x32_bf16 v[28:31],a[84:87],a[52:55],v[28:31]\n"
"v_exp_f32 v123,v45\n"
"v_mfma_f32_16x16x32_bf16 v[28:31],a[88:91],a[56:59],v[28:31]\n"
"s_sub_u32 s3,s3,16384\n"
"v_mfma_f32_16x16x32_bf16 v[28:31],a[92:95],a[60:63],v[28:31]\n"
"v_exp_f32 v119,v46\n"
"v_mfma_f32_16x16x32_bf16 v[24:27],a[76:79],a[60:63],v[108:111]\n"
"v_cvt_pk_bf16_f32 v86,v122,v123\n"
"v_or3_b32 v118,v118,v85,v86\n"
"v_mfma_f32_16x16x32_bf16 v[24:27],a[72:75],a[56:59],v[24:27]\n"
"v_exp_f32 v120,v47\n"
"v_mfma_f32_16x16x32_bf16 v[24:27],a[68:71],a[52:55],v[24:27]\n"
"v_cvt_pk_bf16_f32 v87,v119,v120\n"
"v_mfma_f32_16x16x32_bf16 v[24:27],a[64:67],a[48:51],v[24:27]\n"
"s_waitcnt lgkmcnt(0)\n"
"v_exp_f32 v122,v48\n"
"v_mfma_f32_16x16x32_bf16 v[128:131],a[96:99],v[64:67],0\n"
"v_exp_f32 v123,v49\n"
"v_mfma_f32_16x16x32_bf16 v[160:163],a[96:99],v[68:71],0\n"
"ds_read_b128 a[64:67],v113 offset:32768\n"
"ds_read_b128 a[68:71],v113 offset:33792\n"
"v_mfma_f32_16x16x32_bf16 v[192:195],a[96:99],v[72:75],0\n"
"v_exp_f32 v119,v50\n"
"v_mfma_f32_16x16x32_bf16 v[224:227],a[96:99],v[76:79],0\n"
"ds_read_b128 a[72:75],v113 offset:34816\n"
"v_cvt_pk_bf16_f32 v88,v122,v123\n"
"v_mfma_f32_16x16x32_bf16 a[140:143],a[144:147],v[76:79],0\n"
"v_exp_f32 v120,v51\n"
"v_mfma_f32_16x16x32_bf16 v[228:231],a[100:103],v[76:79],0\n"
"ds_read_b128 a[76:79],v113 offset:35840\n"
"ds_read_b128 a[80:83],v113 offset:36864\n"
"v_mfma_f32_16x16x32_bf16 v[132:135],a[100:103],v[64:67],0\n"
"v_exp_f32 v122,v52\n"
"v_mfma_f32_16x16x32_bf16 v[164:167],a[100:103],v[68:71],0\n"
"ds_read_b128 a[84:87],v113 offset:37888\n"
"v_cvt_pk_bf16_f32 v89,v119,v120\n"
"v_mfma_f32_16x16x32_bf16 v[196:199],a[100:103],v[72:75],0\n"
"v_exp_f32 v123,v53\n"
"v_mfma_f32_16x16x32_bf16 a[136:139],a[144:147],v[72:75],0\n"
"ds_read_b128 a[88:91],v113 offset:38912\n"
"ds_read_b128 a[92:95],v113 offset:39936\n"
"v_mfma_f32_16x16x32_bf16 v[200:203],a[104:107],v[72:75],0\n"
"v_exp_f32 v119,v54\n"
"v_cvt_pk_bf16_f32 v90,v122,v123\n"
"v_mfma_f32_16x16x32_bf16 v[232:235],a[104:107],v[76:79],0\n"
"buffer_store_dwordx4 a[176:179],v112,s[24:27],s3 offen offset:0 sc1\n"
"v_mfma_f32_16x16x32_bf16 v[136:139],a[104:107],v[64:67],0\n"
"v_exp_f32 v120,v55\n"
"v_mfma_f32_16x16x32_bf16 v[168:171],a[104:107],v[68:71],0\n"
"v_cvt_pk_bf16_f32 v91,v119,v120\n"
"v_or3_b32 v118,v118,v87,v88\n"
"v_mfma_f32_16x16x32_bf16 a[132:135],a[144:147],v[68:71],0\n"
"v_exp_f32 v122,v56\n"
"v_mfma_f32_16x16x32_bf16 v[172:175],a[108:111],v[68:71],0\n"
"v_or3_b32 v118,v118,v89,v90\n"
"v_mfma_f32_16x16x32_bf16 v[204:207],a[108:111],v[72:75],0\n"
"v_exp_f32 v123,v57\n"
"v_mfma_f32_16x16x32_bf16 v[236:239],a[108:111],v[76:79],0\n"
"v_cvt_pk_bf16_f32 v92,v122,v123\n"
"v_or3_b32 v118,v118,v91,v92\n"
"v_mfma_f32_16x16x32_bf16 v[140:143],a[108:111],v[64:67],0\n"
"v_exp_f32 v119,v58\n"
"v_mfma_f32_16x16x32_bf16 a[128:131],a[144:147],v[64:67],0\n"
"v_mfma_f32_16x16x32_bf16 v[144:147],a[112:115],v[64:67],0\n"
"v_exp_f32 v120,v59\n"
"v_mfma_f32_16x16x32_bf16 v[176:179],a[112:115],v[68:71],0\n"
"v_cvt_pk_bf16_f32 v93,v119,v120\n"
"v_mfma_f32_16x16x32_bf16 v[208:211],a[112:115],v[72:75],0\n"
"v_exp_f32 v122,v60\n"
"v_mfma_f32_16x16x32_bf16 v[240:243],a[112:115],v[76:79],0\n"
"v_mfma_f32_16x16x32_bf16 v[244:247],a[116:119],v[76:79],0\n"
"v_exp_f32 v123,v61\n"
"v_mfma_f32_16x16x32_bf16 v[148:151],a[116:119],v[64:67],0\n"
"v_cvt_pk_bf16_f32 v94,v122,v123\n"
"v_or3_b32 v118,v118,v93,v94\n"
"v_mfma_f32_16x16x32_bf16 v[180:183],a[116:119],v[68:71],0\n"
"v_exp_f32 v119,v62\n"
"v_mfma_f32_16x16x32_bf16 v[212:215],a[116:119],v[72:75],0\n"
"buffer_store_dwordx4 a[232:235],v112,s[24:27],s56 offen offset:2048 sc1\n"
"v_mfma_f32_16x16x32_bf16 v[216:219],a[120:123],v[72:75],0\n"
"v_exp_f32 v120,v63\n"
"v_mfma_f32_16x16x32_bf16 v[248:251],a[120:123],v[76:79],0\n"
"v_cvt_pk_bf16_f32 v95,v119,v120\n"
"v_or_b32 v118,v118,v95\n"
"v_mfma_f32_16x16x32_bf16 v[152:155],a[120:123],v[64:67],0\n"
"v_and_b32 v118,0x40004000,v118\n"
"v_cmp_ne_u32 vcc,0,v118\n"
"v_mfma_f32_16x16x32_bf16 v[184:187],a[120:123],v[68:71],0\n"
"v_mfma_f32_16x16x32_bf16 v[188:191],a[124:127],v[68:71],0\n"
"v_mfma_f32_16x16x32_bf16 v[220:223],a[124:127],v[72:75],0\n"
"v_mfma_f32_16x16x32_bf16 v[252:255],a[124:127],v[76:79],0\n"
"v_mfma_f32_16x16x32_bf16 v[156:159],a[124:127],v[64:67],0\n"
"s_cbranch_vccnz .Lfk_cs_k1\n"
".Lfk_cr_k1:\n"
"s_waitcnt lgkmcnt(0)\n"
"s_barrier\n"
"v_mfma_f32_16x16x32_bf16 v[32:35],a[64:67],a[0:3],v[96:99]\n"
"s_add_u32 m0,s55,12288\n"
"v_exp_f32 v122,v0\n"
"v_mfma_f32_16x16x32_bf16 v[32:35],a[68:71],a[4:7],v[32:35]\n"
"s_add_u32 s54,s54,s22\n"
"v_exp_f32 v123,v1\n"
"v_mfma_f32_16x16x32_bf16 v[32:35],a[72:75],a[8:11],v[32:35]\n"
"ds_read_b128 a[96:99],v114 offset:8192\n"
"ds_read_b128 a[100:103],v114 offset:9216\n"
"v_mfma_f32_16x16x32_bf16 v[32:35],a[76:79],a[12:15],v[32:35]\n"
"v_exp_f32 v119,v2\n"
"v_mfma_f32_16x16x32_bf16 v[36:39],a[92:95],a[12:15],v[96:99]\n"
"ds_read_b128 a[104:107],v114 offset:10240\n"
"v_cvt_pk_bf16_f32 v64,v122,v123\n"
"v_mfma_f32_16x16x32_bf16 v[36:39],a[88:91],a[8:11],v[36:39]\n"
"v_exp_f32 v120,v3\n"
"v_mfma_f32_16x16x32_bf16 v[36:39],a[84:87],a[4:7],v[36:39]\n"
"ds_read_b128 a[108:111],v114 offset:11264\n"
"ds_read_b128 a[112:115],v114 offset:12288\n"
"v_mfma_f32_16x16x32_bf16 v[36:39],a[80:83],a[0:3],v[36:39]\n"
"v_exp_f32 v122,v4\n"
"v_mfma_f32_16x16x32_bf16 v[44:47],a[80:83],a[16:19],v[100:103]\n"
"ds_read_b128 a[116:119],v114 offset:13312\n"
"v_cvt_pk_bf16_f32 v65,v119,v120\n"
"v_mfma_f32_16x16x32_bf16 v[44:47],a[84:87],a[20:23],v[44:47]\n"
"v_exp_f32 v123,v5\n"
"v_mfma_f32_16x16x32_bf16 v[44:47],a[88:91],a[24:27],v[44:47]\n"
"ds_read_b128 a[120:123],v114 offset:14336\n"
"ds_read_b128 a[124:127],v114 offset:15360\n"
"v_mfma_f32_16x16x32_bf16 v[44:47],a[92:95],a[28:31],v[44:47]\n"
"v_exp_f32 v119,v6\n"
"v_cvt_pk_bf16_f32 v66,v122,v123\n"
"v_mfma_f32_16x16x32_bf16 v[40:43],a[76:79],a[28:31],v[100:103]\n"
"buffer_store_dwordx4 a[180:183],v112,s[24:27],s3 offen offset:1024 sc1\n"
"v_mfma_f32_16x16x32_bf16 v[40:43],a[72:75],a[24:27],v[40:43]\n"
"v_exp_f32 v120,v7\n"
"v_mfma_f32_16x16x32_bf16 v[40:43],a[68:71],a[20:23],v[40:43]\n"
"buffer_load_dwordx4 v115,s[32:35],s54 offen sc1 lds\n"
"v_mfma_f32_16x16x32_bf16 v[40:43],a[64:67],a[16:19],v[40:43]\n"
"v_exp_f32 v122,v8\n"
"v_mfma_f32_16x16x32_bf16 v[48:51],a[64:67],a[32:35],v[104:107]\n"
"v_cvt_pk_bf16_f32 v67,v119,v120\n"
"v_or3_b32 v118,v64,v65,v66\n"
"v_mfma_f32_16x16x32_bf16 v[48:51],a[68:71],a[36:39],v[48:51]\n"
"v_exp_f32 v123,v9\n"
"v_mfma_f32_16x16x32_bf16 v[48:51],a[72:75],a[40:43],v[48:51]\n"
"v_cvt_pk_bf16_f32 v68,v122,v123\n"
"v_or3_b32 v118,v118,v67,v68\n"
"v_mfma_f32_16x16x32_bf16 v[48:51],a[76:79],a[44:47],v[48:51]\n"
"v_exp_f32 v119,v10\n"
"v_mfma_f32_16x16x32_bf16 v[52:55],a[92:95],a[44:47],v[104:107]\n"
"v_mfma_f32_16x16x32_bf16 v[52:55],a[88:91],a[40:43],v[52:55]\n"
"v_exp_f32 v120,v11\n"
"v_mfma_f32_16x16x32_bf16 v[52:55],a[84:87],a[36:39],v[52:55]\n"
"v_cvt_pk_bf16_f32 v69,v119,v120\n"
"v_mfma_f32_16x16x32_bf16 v[52:55],a[80:83],a[32:35],v[52:55]\n"
"v_exp_f32 v122,v12\n"
"v_mfma_f32_16x16x32_bf16 v[60:63],a[80:83],a[48:51],v[108:111]\n"
"buffer_store_dwordx4 a[184:187],v112,s[24:27],s3 offen offset:2048 sc1\n"
"v_mfma_f32_16x16x32_bf16 v[60:63],a[84:87],a[52:55],v[60:63]\n"
"v_exp_f32 v123,v13\n"
"v_mfma_f32_16x16x32_bf16 v[60:63],a[88:91],a[56:59],v[60:63]\n"
"v_cvt_pk_bf16_f32 v70,v122,v123\n"
"v_mfma_f32_16x16x32_bf16 v[60:63],a[92:95],a[60:63],v[60:63]\n"
"v_exp_f32 v119,v14\n"
"v_mfma_f32_16x16x32_bf16 v[56:59],a[76:79],a[60:63],v[108:111]\n"
"v_or3_b32 v118,v118,v69,v70\n"
"v_mfma_f32_16x16x32_bf16 v[56:59],a[72:75],a[56:59],v[56:59]\n"
"v_exp_f32 v120,v15\n"
"v_mfma_f32_16x16x32_bf16 v[56:59],a[68:71],a[52:55],v[56:59]\n"
"v_cvt_pk_bf16_f32 v71,v119,v120\n"
"v_mfma_f32_16x16x32_bf16 v[56:59],a[64:67],a[48:51],v[56:59]\n"
"s_waitcnt lgkmcnt(0)\n"
"v_exp_f32 v122,v16\n"
"v_mfma_f32_16x16x32_bf16 v[128:131],a[96:99],v[80:83],v[128:131]\n"
"v_exp_f32 v123,v17\n"
"v_mfma_f32_16x16x32_bf16 v[160:163],a[96:99],v[84:87],v[160:163]\n"
"ds_read_b128 a[64:67],v113 offset:0\n"
"ds_read_b128 a[68:71],v113 offset:1024\n"
"v_mfma_f32_16x16x32_bf16 v[192:195],a[96:99],v[88:91],v[192:195]\n"
"v_exp_f32 v119,v18\n"
"v_mfma_f32_16x16x32_bf16 v[224:227],a[96:99],v[92:95],v[224:227]\n"
"ds_read_b128 a[72:75],v113 offset:2048\n"
"v_cvt_pk_bf16_f32 v72,v122,v123\n"
"v_mfma_f32_16x16x32_bf16 a[140:143],a[144:147],v[92:95],a[140:143]\n"
"v_exp_f32 v120,v19\n"
"v_mfma_f32_16x16x32_bf16 v[228:231],a[100:103],v[92:95],v[228:231]\n"
"ds_read_b128 a[76:79],v113 offset:3072\n"
"ds_read_b128 a[80:83],v113 offset:4096\n"
"v_mfma_f32_16x16x32_bf16 v[132:135],a[100:103],v[80:83],v[132:135]\n"
"v_exp_f32 v122,v20\n"
"v_mfma_f32_16x16x32_bf16 v[164:167],a[100:103],v[84:87],v[164:167]\n"
"ds_read_b128 a[84:87],v113 offset:5120\n"
"v_cvt_pk_bf16_f32 v73,v119,v120\n"
"v_mfma_f32_16x16x32_bf16 v[196:199],a[100:103],v[88:91],v[196:199]\n"
"v_exp_f32 v123,v21\n"
"v_mfma_f32_16x16x32_bf16 a[136:139],a[144:147],v[88:91],a[136:139]\n"
"ds_read_b128 a[88:91],v113 offset:6144\n"
"ds_read_b128 a[92:95],v113 offset:7168\n"
"v_mfma_f32_16x16x32_bf16 v[200:203],a[104:107],v[88:91],v[200:203]\n"
"v_exp_f32 v119,v22\n"
"v_mfma_f32_16x16x32_bf16 v[232:235],a[104:107],v[92:95],v[232:235]\n"
"buffer_store_dwordx4 a[188:191],v112,s[24:27],s3 offen offset:3072 sc1\n"
"v_mfma_f32_16x16x32_bf16 v[136:139],a[104:107],v[80:83],v[136:139]\n"
"v_exp_f32 v120,v23\n"
"v_mfma_f32_16x16x32_bf16 v[168:171],a[104:107],v[84:87],v[168:171]\n"
"s_or_b32 s3,s3,0x3c000\n"
"v_cvt_pk_bf16_f32 v74,v122,v123\n"
"v_cvt_pk_bf16_f32 v75,v119,v120\n"
"v_mfma_f32_16x16x32_bf16 a[132:135],a[144:147],v[84:87],a[132:135]\n"
"v_exp_f32 v122,v24\n"
"v_mfma_f32_16x16x32_bf16 v[172:175],a[108:111],v[84:87],v[172:175]\n"
"v_or3_b32 v118,v118,v71,v72\n"
"v_or3_b32 v118,v118,v73,v74\n"
"v_mfma_f32_16x16x32_bf16 v[204:207],a[108:111],v[88:91],v[204:207]\n"
"v_exp_f32 v123,v25\n"
"v_mfma_f32_16x16x32_bf16 v[236:239],a[108:111],v[92:95],v[236:239]\n"
"v_cvt_pk_bf16_f32 v76,v122,v123\n"
"v_or3_b32 v118,v118,v75,v76\n"
"v_mfma_f32_16x16x32_bf16 v[140:143],a[108:111],v[80:83],v[140:143]\n"
"v_exp_f32 v119,v26\n"
"v_mfma_f32_16x16x32_bf16 a[128:131],a[144:147],v[80:83],a[128:131]\n"
"v_mfma_f32_16x16x32_bf16 v[144:147],a[112:115],v[80:83],v[144:147]\n"
"v_exp_f32 v120,v27\n"
"v_mfma_f32_16x16x32_bf16 v[176:179],a[112:115],v[84:87],v[176:179]\n"
"v_cvt_pk_bf16_f32 v77,v119,v120\n"
"v_mfma_f32_16x16x32_bf16 v[208:211],a[112:115],v[88:91],v[208:211]\n"
"v_exp_f32 v122,v28\n"
"v_mfma_f32_16x16x32_bf16 v[240:243],a[112:115],v[92:95],v[240:243]\n"
"v_mfma_f32_16x16x32_bf16 v[244:247],a[116:119],v[92:95],v[244:247]\n"
"v_exp_f32 v123,v29\n"
"v_mfma_f32_16x16x32_bf16 v[148:151],a[116:119],v[80:83],v[148:151]\n"
"v_cvt_pk_bf16_f32 v78,v122,v123\n"
"v_or3_b32 v118,v118,v77,v78\n"
"v_mfma_f32_16x16x32_bf16 v[180:183],a[116:119],v[84:87],v[180:183]\n"
"v_exp_f32 v119,v30\n"
"v_mfma_f32_16x16x32_bf16 v[212:215],a[116:119],v[88:91],v[212:215]\n"
"buffer_store_dwordx4 a[236:239],v112,s[24:27],s56 offen offset:3072 sc1\n"
"v_mfma_f32_16x16x32_bf16 v[216:219],a[120:123],v[88:91],v[216:219]\n"
"v_exp_f32 v120,v31\n"
"v_mfma_f32_16x16x32_bf16 v[248:251],a[120:123],v[92:95],v[248:251]\n"
"v_cvt_pk_bf16_f32 v79,v119,v120\n"
"v_or_b32 v118,v118,v79\n"
"v_mfma_f32_16x16x32_bf16 v[152:155],a[120:123],v[80:83],v[152:155]\n"
"v_and_b32 v118,0x40004000,v118\n"
"v_cmp_ne_u32 vcc,0,v118\n"
"v_mfma_f32_16x16x32_bf16 v[184:187],a[120:123],v[84:87],v[184:187]\n"
"v_mfma_f32_16x16x32_bf16 v[188:191],a[124:127],v[84:87],v[188:191]\n"
"v_mfma_f32_16x16x32_bf16 v[220:223],a[124:127],v[88:91],v[220:223]\n"
"v_mfma_f32_16x16x32_bf16 v[252:255],a[124:127],v[92:95],v[252:255]\n"
"v_mfma_f32_16x16x32_bf16 v[156:159],a[124:127],v[80:83],v[156:159]\n"
"s_cbranch_vccnz .Lfk_cs_k2\n"
".Lfk_cr_k2:\n"
"s_waitcnt vmcnt(13) lgkmcnt(0)\n"
"s_barrier\n"
"s_branch .Lfk_g1\n"
".Lfk_winA:\n"
"v_max3_f32 v118,v32,v33,v34\n"
"v_max3_f32 v119,v35,v36,v37\n"
"v_max3_f32 v119,v119,v38,v39\n"
"v_max3_f32 v118,v118,v40,v41\n"
"v_max3_f32 v119,v119,v42,v43\n"
"v_max3_f32 v118,v118,v44,v45\n"
"v_max3_f32 v119,v119,v46,v47\n"
"v_max3_f32 v118,v118,v48,v49\n"
"v_max3_f32 v119,v119,v50,v51\n"
"v_max3_f32 v118,v118,v52,v53\n"
"v_max3_f32 v119,v119,v54,v55\n"
"v_max3_f32 v118,v118,v56,v57\n"
"v_max3_f32 v119,v119,v58,v59\n"
"v_max3_f32 v118,v118,v60,v61\n"
"v_max3_f32 v119,v119,v62,v63\n"
"v_max_f32 v118,v118,v119\n"
"v_cmp_lt_f32 vcc,s93,v118\n"
"s_nop 0\n"
"s_cbranch_vccnz .Lfk_cs_x2\n"
".Lfk_cr_x2:\n"
"s_mov_b32 s64,s61\n"
"s_cmp_eq_u32 s52,3\n"
"s_cselect_b32 s67,172032,0\n"
"v_mfma_f32_16x16x32_bf16 v[8:11],a[64:67],a[16:19],v[100:103]\n"
"s_swappc_b64 s[94:95],s[20:21]\n"
"ds_read_b128 a[96:99],v114 offset:24576\n"
"ds_read_b128 a[100:103],v114 offset:25600\n"
"v_mfma_f32_16x16x32_bf16 v[8:11],a[68:71],a[20:23],v[8:11]\n"
"ds_read_b128 a[104:107],v114 offset:26624\n"
"ds_read_b128 a[108:111],v114 offset:27648\n"
"v_mfma_f32_16x16x32_bf16 v[8:11],a[72:75],a[24:27],v[8:11]\n"
"ds_read_b128 a[112:115],v114 offset:28672\n"
"ds_read_b128 a[116:119],v114 offset:29696\n"
"v_mfma_f32_16x16x32_bf16 v[8:11],a[76:79],a[28:31],v[8:11]\n"
"ds_read_b128 a[120:123],v114 offset:30720\n"
"ds_read_b128 a[124:127],v114 offset:31744\n"
"v_mfma_f32_16x16x32_bf16 v[12:15],a[92:95],a[28:31],v[100:103]\n"
"v_add_f32 v0,0x80000000,v126\n"
"v_min_f32 v0,v0,v96\n"
"v_mfma_f32_16x16x32_bf16 v[12:15],a[88:91],a[24:27],v[12:15]\n"
"v_add_f32 v1,0xf149f2ca,v126\n"
"v_min_f32 v1,v1,v97\n"
"v_mfma_f32_16x16x32_bf16 v[12:15],a[84:87],a[20:23],v[12:15]\n"
"v_add_f32 v2,0xf1c9f2ca,v126\n"
"v_min_f32 v2,v2,v98\n"
"v_mfma_f32_16x16x32_bf16 v[12:15],a[80:83],a[16:19],v[12:15]\n"
"v_add_f32 v3,0xf2177617,v126\n"
"v_min_f32 v3,v3,v99\n"
"v_mfma_f32_16x16x32_bf16 v[20:23],a[80:83],a[32:35],v[104:107]\n"
"v_add_f32 v4,0xf349f2ca,v126\n"
"v_min_f32 v4,v4,v96\n"
"v_mfma_f32_16x16x32_bf16 v[20:23],a[84:87],a[36:39],v[20:23]\n"
"v_add_f32 v5,0xf35691f6,v126\n"
"v_min_f32 v5,v5,v97\n"
"v_mfma_f32_16x16x32_bf16 v[20:23],a[88:91],a[40:43],v[20:23]\n"
"v_add_f32 v6,0xf3633123,v126\n"
"v_min_f32 v6,v6,v98\n"
"v_mfma_f32_16x16x32_bf16 v[20:23],a[92:95],a[44:47],v[20:23]\n"
"v_add_f32 v7,0xf36fd050,v126\n"
"v_min_f32 v7,v7,v99\n"
"v_mfma_f32_16x16x32_bf16 v[16:19],a[76:79],a[44:47],v[104:107]\n"
"s_add_u32 s101,s101,4\n"
"v_exp_f32 v32,v32\n"
"v_mfma_f32_16x16x32_bf16 v[16:19],a[72:75],a[40:43],v[16:19]\n"
"s_add_u32 m0,s67,0\n"
"v_exp_f32 v33,v33\n"
"v_mfma_f32_16x16x32_bf16 v[16:19],a[68:71],a[36:39],v[16:19]\n"
"v_exp_f32 v34,v34\n"
"v_mfma_f32_16x16x32_bf16 v[16:19],a[64:67],a[32:35],v[16:19]\n"
"v_exp_f32 v35,v35\n"
"v_mfma_f32_16x16x32_bf16 v[24:27],a[64:67],a[48:51],v[108:111]\n"
"v_exp_f32 v36,v36\n"
"v_mfma_f32_16x16x32_bf16 v[24:27],a[68:71],a[52:55],v[24:27]\n"
"v_exp_f32 v37,v37\n"
"v_mfma_f32_16x16x32_bf16 v[24:27],a[72:75],a[56:59],v[24:27]\n"
"v_exp_f32 v38,v38\n"
"v_mfma_f32_16x16x32_bf16 v[24:27],a[76:79],a[60:63],v[24:27]\n"
"v_exp_f32 v39,v39\n"
"v_mfma_f32_16x16x32_bf16 v[28:31],a[92:95],a[60:63],v[108:111]\n"
"v_exp_f32 v40,v40\n"
"v_mfma_f32_16x16x32_bf16 v[28:31],a[88:91],a[56:59],v[28:31]\n"
"v_exp_f32 v41,v41\n"
"v_mfma_f32_16x16x32_bf16 v[28:31],a[84:87],a[52:55],v[28:31]\n"
"v_exp_f32 v42,v42\n"
"v_mfma_f32_16x16x32_bf16 v[28:31],a[80:83],a[48:51],v[28:31]\n"
"v_exp_f32 v43,v43\n"
"v_mfma_f32_16x16x32_bf16 v[4:7],a[80:83],a[0:3],v[4:7]\n"
"v_exp_f32 v44,v44\n"
"v_mfma_f32_16x16x32_bf16 v[4:7],a[84:87],a[4:7],v[4:7]\n"
"v_exp_f32 v45,v45\n"
"v_mfma_f32_16x16x32_bf16 v[4:7],a[88:91],a[8:11],v[4:7]\n"
"buffer_load_dwordx4 v112,s[24:27],s64 offen offset:0 sc1 lds\n"
"v_mfma_f32_16x16x32_bf16 v[4:7],a[92:95],a[12:15],v[4:7]\n"
"v_exp_f32 v46,v46\n"
"v_mfma_f32_16x16x32_bf16 v[0:3],a[76:79],a[12:15],v[0:3]\n"
"buffer_load_dwordx4 v112,s[24:27],s64 offen offset:1024 sc1 lds\n"
"v_mfma_f32_16x16x32_bf16 v[0:3],a[72:75],a[8:11],v[0:3]\n"
"v_exp_f32 v47,v47\n"
"v_mfma_f32_16x16x32_bf16 v[0:3],a[68:71],a[4:7],v[0:3]\n"
"buffer_load_dwordx4 v112,s[24:27],s64 offen offset:2048 sc1 lds\n"
"v_mfma_f32_16x16x32_bf16 v[0:3],a[64:67],a[0:3],v[0:3]\n"
"s_waitcnt lgkmcnt(0)\n"
"v_exp_f32 v48,v48\n"
"v_mfma_f32_16x16x32_bf16 v[128:131],a[96:99],v[64:67],v[128:131]\n"
"v_exp_f32 v49,v49\n"
"v_mfma_f32_16x16x32_bf16 v[160:163],a[96:99],v[68:71],v[160:163]\n"
"ds_read_b128 a[64:67],v113 offset:16384\n"
"ds_read_b128 a[68:71],v113 offset:17408\n"
"v_mfma_f32_16x16x32_bf16 v[192:195],a[96:99],v[72:75],v[192:195]\n"
"v_exp_f32 v50,v50\n"
"v_mfma_f32_16x16x32_bf16 v[224:227],a[96:99],v[76:79],v[224:227]\n"
"ds_read_b128 a[72:75],v113 offset:18432\n"
"ds_read_b128 a[76:79],v113 offset:19456\n"
"v_mfma_f32_16x16x32_bf16 a[140:143],a[144:147],v[76:79],a[140:143]\n"
"v_exp_f32 v51,v51\n"
"v_mfma_f32_16x16x32_bf16 v[228:231],a[100:103],v[76:79],v[228:231]\n"
"ds_read_b128 a[80:83],v113 offset:20480\n"
"ds_read_b128 a[84:87],v113 offset:21504\n"
"v_mfma_f32_16x16x32_bf16 v[132:135],a[100:103],v[64:67],v[132:135]\n"
"v_exp_f32 v52,v52\n"
"v_mfma_f32_16x16x32_bf16 v[164:167],a[100:103],v[68:71],v[164:167]\n"
"ds_read_b128 a[88:91],v113 offset:22528\n"
"ds_read_b128 a[92:95],v113 offset:23552\n"
"v_mfma_f32_16x16x32_bf16 v[196:199],a[100:103],v[72:75],v[196:199]\n"
"v_exp_f32 v53,v53\n"
"v_mfma_f32_16x16x32_bf16 a[136:139],a[144:147],v[72:75],a[136:139]\n"
"s_waitcnt vmcnt(14)\n"
"ds_add_u32 v124,a152\n"
"v_mfma_f32_16x16x32_bf16 v[200:203],a[104:107],v[72:75],v[200:203]\n"
"v_exp_f32 v54,v54\n"
"v_mfma_f32_16x16x32_bf16 v[232:235],a[104:107],v[76:79],v[232:235]\n"
"buffer_load_dwordx4 v112,s[24:27],s64 offen offset:3072 sc1 lds\n"
"v_mfma_f32_16x16x32_bf16 v[136:139],a[104:107],v[64:67],v[136:139]\n"
"s_add_u32 m0,s67,4096\n"
"v_exp_f32 v55,v55\n"
"v_mfma_f32_16x16x32_bf16 v[168:171],a[104:107],v[68:71],v[168:171]\n"
"buffer_load_dwordx4 v117,s[24:27],s64 offen offset:0 sc1 lds\n"
"v_mfma_f32_16x16x32_bf16 a[132:135],a[144:147],v[68:71],a[132:135]\n"
"v_exp_f32 v56,v56\n"
"v_mfma_f32_16x16x32_bf16 v[172:175],a[108:111],v[68:71],v[172:175]\n"
"buffer_load_dwordx4 v117,s[24:27],s64 offen offset:1024 sc1 lds\n"
"v_mfma_f32_16x16x32_bf16 v[204:207],a[108:111],v[72:75],v[204:207]\n"
"v_exp_f32 v57,v57\n"
"v_mfma_f32_16x16x32_bf16 v[236:239],a[108:111],v[76:79],v[236:239]\n"
"buffer_load_dwordx4 v117,s[24:27],s64 offen offset:2048 sc1 lds\n"
"v_mfma_f32_16x16x32_bf16 v[140:143],a[108:111],v[64:67],v[140:143]\n"
"v_exp_f32 v58,v58\n"
"v_mfma_f32_16x16x32_bf16 a[128:131],a[144:147],v[64:67],a[128:131]\n"
"v_cvt_pk_bf16_f32 v80,v32,v33\n"
"v_cvt_pk_bf16_f32 v81,v34,v35\n"
"v_mfma_f32_16x16x32_bf16 v[144:147],a[112:115],v[64:67],v[144:147]\n"
"v_exp_f32 v59,v59\n"
"v_mfma_f32_16x16x32_bf16 v[176:179],a[112:115],v[68:71],v[176:179]\n"
"v_cvt_pk_bf16_f32 v82,v36,v37\n"
"v_cvt_pk_bf16_f32 v83,v38,v39\n"
"v_mfma_f32_16x16x32_bf16 v[208:211],a[112:115],v[72:75],v[208:211]\n"
"v_exp_f32 v60,v60\n"
"v_mfma_f32_16x16x32_bf16 v[240:243],a[112:115],v[76:79],v[240:243]\n"
"buffer_load_dwordx4 v117,s[24:27],s64 offen offset:3072 sc1 lds\n"
"v_mfma_f32_16x16x32_bf16 v[244:247],a[116:119],v[76:79],v[244:247]\n"
"s_add_u32 s64,s64,16384\n"
"v_exp_f32 v61,v61\n"
"v_mfma_f32_16x16x32_bf16 v[148:151],a[116:119],v[64:67],v[148:151]\n"
"s_andn2_b32 m0,s55,16384\n"
"s_add_u32 s54,s54,s22\n"
"v_cvt_pk_bf16_f32 v85,v42,v43\n"
"v_mfma_f32_16x16x32_bf16 v[180:183],a[116:119],v[68:71],v[180:183]\n"
"v_exp_f32 v62,v62\n"
"v_mfma_f32_16x16x32_bf16 v[212:215],a[116:119],v[72:75],v[212:215]\n"
"ds_read_b32 v125,v124\n"
"v_cvt_pk_bf16_f32 v84,v40,v41\n"
"v_mfma_f32_16x16x32_bf16 v[216:219],a[120:123],v[72:75],v[216:219]\n"
"v_exp_f32 v63,v63\n"
"v_mfma_f32_16x16x32_bf16 v[248:251],a[120:123],v[76:79],v[248:251]\n"
"buffer_load_dwordx4 v115,s[32:35],s54 offen sc1 lds\n"
"v_mfma_f32_16x16x32_bf16 v[152:155],a[120:123],v[64:67],v[152:155]\n"
"buffer_store_dwordx4 a[240:243],v112,s[24:27],s56 offen offset:0 sc1\n"
"v_mfma_f32_16x16x32_bf16 v[184:187],a[120:123],v[68:71],v[184:187]\n"
"v_cvt_pk_bf16_f32 v86,v44,v45\n"
"v_cvt_pk_bf16_f32 v87,v46,v47\n"
"v_mfma_f32_16x16x32_bf16 v[188:191],a[124:127],v[68:71],v[188:191]\n"
"s_waitcnt lgkmcnt(0)\n"
"v_cvt_pk_bf16_f32 v88,v48,v49\n"
"v_cvt_pk_bf16_f32 v89,v50,v51\n"
"v_mfma_f32_16x16x32_bf16 v[220:223],a[124:127],v[72:75],v[220:223]\n"
"v_cvt_pk_bf16_f32 v90,v52,v53\n"
"v_cvt_pk_bf16_f32 v91,v54,v55\n"
"v_mfma_f32_16x16x32_bf16 v[252:255],a[124:127],v[76:79],v[252:255]\n"
"v_cvt_pk_bf16_f32 v92,v56,v57\n"
"v_cvt_pk_bf16_f32 v93,v58,v59\n"
"v_mfma_f32_16x16x32_bf16 v[156:159],a[124:127],v[64:67],v[156:159]\n"
"v_cvt_pk_bf16_f32 v94,v60,v61\n"
"v_cvt_pk_bf16_f32 v95,v62,v63\n"
"v_max3_f32 v118,v8,v9,v10\n"
"v_max3_f32 v118,v118,v11,v12\n"
"v_max3_f32 v118,v118,v13,v14\n"
"v_max3_f32 v118,v118,v15,v20\n"
"v_max3_f32 v118,v118,v21,v22\n"
"v_max3_f32 v118,v118,v23,v16\n"
"v_max3_f32 v118,v118,v17,v18\n"
"v_max3_f32 v118,v118,v19,v24\n"
"v_max3_f32 v118,v118,v25,v26\n"
"v_max3_f32 v118,v118,v27,v28\n"
"v_max3_f32 v118,v118,v29,v30\n"
"v_max3_f32 v118,v118,v31,v4\n"
"v_max3_f32 v118,v118,v5,v6\n"
"v_max3_f32 v118,v118,v7,v0\n"
"v_max3_f32 v118,v118,v1,v2\n"
"v_max_f32 v118,v118,v3\n"
"v_cmp_lt_f32 vcc,s93,v118\n"
"s_nop 0\n"
"s_cbranch_vccnz .Lfk_cs_u3\n"
".Lfk_cr_u3:\n"
"v_cmp_gt_u32 vcc,s101,v125\n"
"s_nop 0\n"
"s_cbranch_vccnz .Lfk_cs_mu3\n"
".Lfk_cr_mu3:\n"
"v_mfma_f32_16x16x32_bf16 v[48:51],a[64:67],a[32:35],v[104:107]\n"
"ds_read_b128 a[96:99],v114 offset:57344\n"
"ds_read_b128 a[100:103],v114 offset:58368\n"
"v_mfma_f32_16x16x32_bf16 v[48:51],a[68:71],a[36:39],v[48:51]\n"
"ds_read_b128 a[104:107],v114 offset:59392\n"
"ds_read_b128 a[108:111],v114 offset:60416\n"
"v_mfma_f32_16x16x32_bf16 v[48:51],a[72:75],a[40:43],v[48:51]\n"
"ds_read_b128 a[112:115],v114 offset:61440\n"
"ds_read_b128 a[116:119],v114 offset:62464\n"
"v_mfma_f32_16x16x32_bf16 v[48:51],a[76:79],a[44:47],v[48:51]\n"
"ds_read_b128 a[120:123],v114 offset:63488\n"
"ds_read_b128 a[124:127],v114 offset:64512\n"
"v_mfma_f32_16x16x32_bf16 v[52:55],a[92:95],a[44:47],v[104:107]\n"
"ds_read_b128 a[0:3],v116 offset:40960\n"
"ds_read_b128 a[4:7],v116 offset:41984\n"
"v_mfma_f32_16x16x32_bf16 v[52:55],a[88:91],a[40:43],v[52:55]\n"
"ds_read_b128 a[8:11],v116 offset:43008\n"
"ds_read_b128 a[12:15],v116 offset:44032\n"
"v_mfma_f32_16x16x32_bf16 v[52:55],a[84:87],a[36:39],v[52:55]\n"
"v_add_f32 v40,0x80000000,v126\n"
"v_min_f32 v40,v40,v100\n"
"v_mfma_f32_16x16x32_bf16 v[52:55],a[80:83],a[32:35],v[52:55]\n"
"v_add_f32 v41,0xf149f2ca,v126\n"
"v_min_f32 v41,v41,v101\n"
"v_mfma_f32_16x16x32_bf16 v[60:63],a[80:83],a[48:51],v[108:111]\n"
"v_add_f32 v42,0xf1c9f2ca,v126\n"
"v_min_f32 v42,v42,v102\n"
"v_mfma_f32_16x16x32_bf16 v[60:63],a[84:87],a[52:55],v[60:63]\n"
"v_add_f32 v43,0xf2177617,v126\n"
"v_min_f32 v43,v43,v103\n"
"v_mfma_f32_16x16x32_bf16 v[60:63],a[88:91],a[56:59],v[60:63]\n"
"v_add_f32 v44,0xf349f2ca,v126\n"
"v_min_f32 v44,v44,v100\n"
"v_mfma_f32_16x16x32_bf16 v[60:63],a[92:95],a[60:63],v[60:63]\n"
"v_add_f32 v45,0xf35691f6,v126\n"
"v_min_f32 v45,v45,v101\n"
"v_mfma_f32_16x16x32_bf16 v[56:59],a[76:79],a[60:63],v[108:111]\n"
"v_add_f32 v46,0xf3633123,v126\n"
"v_min_f32 v46,v46,v102\n"
"v_mfma_f32_16x16x32_bf16 v[56:59],a[72:75],a[56:59],v[56:59]\n"
"v_add_f32 v47,0xf36fd050,v126\n"
"v_min_f32 v47,v47,v103\n"
"v_mfma_f32_16x16x32_bf16 v[56:59],a[68:71],a[52:55],v[56:59]\n"
"s_add_u32 s101,s101,4\n"
"v_exp_f32 v0,v0\n"
"v_mfma_f32_16x16x32_bf16 v[56:59],a[64:67],a[48:51],v[56:59]\n"
"s_add_u32 m0,s55,4096\n"
"v_exp_f32 v1,v1\n"
"v_mfma_f32_16x16x32_bf16 v[40:43],a[64:67],a[16:19],v[40:43]\n"
"s_add_u32 s54,s54,s22\n"
"v_exp_f32 v2,v2\n"
"v_mfma_f32_16x16x32_bf16 v[40:43],a[68:71],a[20:23],v[40:43]\n"
"v_exp_f32 v3,v3\n"
"v_mfma_f32_16x16x32_bf16 v[40:43],a[72:75],a[24:27],v[40:43]\n"
"v_exp_f32 v4,v4\n"
"v_mfma_f32_16x16x32_bf16 v[40:43],a[76:79],a[28:31],v[40:43]\n"
"v_exp_f32 v5,v5\n"
"v_mfma_f32_16x16x32_bf16 v[44:47],a[92:95],a[28:31],v[44:47]\n"
"v_exp_f32 v6,v6\n"
"v_mfma_f32_16x16x32_bf16 v[44:47],a[88:91],a[24:27],v[44:47]\n"
"v_exp_f32 v7,v7\n"
"v_mfma_f32_16x16x32_bf16 v[44:47],a[84:87],a[20:23],v[44:47]\n"
"v_exp_f32 v8,v8\n"
"v_mfma_f32_16x16x32_bf16 v[44:47],a[80:83],a[16:19],v[44:47]\n"
"s_waitcnt lgkmcnt(4)\n"
"v_exp_f32 v9,v9\n"
"v_mfma_f32_16x16x32_bf16 v[128:131],a[96:99],v[80:83],v[128:131]\n"
"v_exp_f32 v10,v10\n"
"v_mfma_f32_16x16x32_bf16 v[160:163],a[96:99],v[84:87],v[160:163]\n"
"v_exp_f32 v11,v11\n"
"v_mfma_f32_16x16x32_bf16 v[192:195],a[96:99],v[88:91],v[192:195]\n"
"v_exp_f32 v12,v12\n"
"v_mfma_f32_16x16x32_bf16 v[224:227],a[96:99],v[92:95],v[224:227]\n"
"v_exp_f32 v13,v13\n"
"v_mfma_f32_16x16x32_bf16 a[140:143],a[144:147],v[92:95],a[140:143]\n"
"v_exp_f32 v14,v14\n"
"v_mfma_f32_16x16x32_bf16 v[228:231],a[100:103],v[92:95],v[228:231]\n"
"v_exp_f32 v15,v15\n"
"v_mfma_f32_16x16x32_bf16 v[132:135],a[100:103],v[80:83],v[132:135]\n"
"v_exp_f32 v16,v16\n"
"v_mfma_f32_16x16x32_bf16 v[164:167],a[100:103],v[84:87],v[164:167]\n"
"v_exp_f32 v17,v17\n"
"v_mfma_f32_16x16x32_bf16 v[196:199],a[100:103],v[88:91],v[196:199]\n"
"v_exp_f32 v18,v18\n"
"v_mfma_f32_16x16x32_bf16 a[136:139],a[144:147],v[88:91],a[136:139]\n"
"v_exp_f32 v19,v19\n"
"v_mfma_f32_16x16x32_bf16 v[200:203],a[104:107],v[88:91],v[200:203]\n"
"v_exp_f32 v20,v20\n"
"v_mfma_f32_16x16x32_bf16 v[232:235],a[104:107],v[92:95],v[232:235]\n"
"v_exp_f32 v21,v21\n"
"v_mfma_f32_16x16x32_bf16 v[136:139],a[104:107],v[80:83],v[136:139]\n"
"ds_read_b128 a[64:67],v113 offset:49152\n"
"ds_read_b128 a[68:71],v113 offset:50176\n"
"v_mfma_f32_16x16x32_bf16 v[168:171],a[104:107],v[84:87],v[168:171]\n"
"v_exp_f32 v22,v22\n"
"v_mfma_f32_16x16x32_bf16 a[132:135],a[144:147],v[84:87],a[132:135]\n"
"v_exp_f32 v23,v23\n"
"v_mfma_f32_16x16x32_bf16 v[172:175],a[108:111],v[84:87],v[172:175]\n"
"ds_read_b128 a[72:75],v113 offset:51200\n"
"ds_read_b128 a[76:79],v113 offset:52224\n"
"v_mfma_f32_16x16x32_bf16 v[204:207],a[108:111],v[88:91],v[204:207]\n"
"v_exp_f32 v24,v24\n"
"v_mfma_f32_16x16x32_bf16 v[236:239],a[108:111],v[92:95],v[236:239]\n"
"ds_read_b128 a[80:83],v113 offset:53248\n"
"ds_read_b128 a[84:87],v113 offset:54272\n"
"v_mfma_f32_16x16x32_bf16 v[140:143],a[108:111],v[80:83],v[140:143]\n"
"v_exp_f32 v25,v25\n"
"v_mfma_f32_16x16x32_bf16 a[128:131],a[144:147],v[80:83],a[128:131]\n"
"v_exp_f32 v26,v26\n"
"v_mfma_f32_16x16x32_bf16 v[144:147],a[112:115],v[80:83],v[144:147]\n"
"ds_read_b128 a[88:91],v113 offset:55296\n"
"ds_read_b128 a[92:95],v113 offset:56320\n"
"v_mfma_f32_16x16x32_bf16 v[176:179],a[112:115],v[84:87],v[176:179]\n"
"v_exp_f32 v27,v27\n"
"v_mfma_f32_16x16x32_bf16 v[208:211],a[112:115],v[88:91],v[208:211]\n"
"s_waitcnt vmcnt(11)\n"
"ds_add_u32 v124,a152\n"
"v_mfma_f32_16x16x32_bf16 v[240:243],a[112:115],v[92:95],v[240:243]\n"
"v_exp_f32 v28,v28\n"
"v_mfma_f32_16x16x32_bf16 v[244:247],a[116:119],v[92:95],v[244:247]\n"
"v_exp_f32 v29,v29\n"
"v_mfma_f32_16x16x32_bf16 v[148:151],a[116:119],v[80:83],v[148:151]\n"
"buffer_load_dwordx4 v115,s[32:35],s54 offen sc1 lds\n"
"v_mfma_f32_16x16x32_bf16 v[180:183],a[116:119],v[84:87],v[180:183]\n"
"s_add_u32 m0,s67,16384\n"
"v_exp_f32 v30,v30\n"
"v_mfma_f32_16x16x32_bf16 v[212:215],a[116:119],v[88:91],v[212:215]\n"
"ds_read_b32 v125,v124\n"
"v_cvt_pk_bf16_f32 v64,v0,v1\n"
"v_mfma_f32_16x16x32_bf16 v[216:219],a[120:123],v[88:91],v[216:219]\n"
"v_exp_f32 v31,v31\n"
"v_mfma_f32_16x16x32_bf16 v[248:251],a[120:123],v[92:95],v[248:251]\n"
"buffer_load_dwordx4 v112,s[24:27],s64 offen offset:0 sc1 lds\n"
"v_mfma_f32_16x16x32_bf16 v[152:155],a[120:123],v[80:83],v[152:155]\n"
"buffer_load_dwordx4 v112,s[24:27],s64 offen offset:1024 sc1 lds\n"
"v_mfma_f32_16x16x32_bf16 v[184:187],a[120:123],v[84:87],v[184:187]\n"
"buffer_load_dwordx4 v112,s[24:27],s64 offen offset:2048 sc1 lds\n"
"v_mfma_f32_16x16x32_bf16 v[188:191],a[124:127],v[84:87],v[188:191]\n"
"s_waitcnt lgkmcnt(0)\n"
"buffer_load_dwordx4 v112,s[24:27],s64 offen offset:3072 sc1 lds\n"
"v_mfma_f32_16x16x32_bf16 v[220:223],a[124:127],v[88:91],v[220:223]\n"
"s_add_u32 m0,s67,20480\n"
"v_cvt_pk_bf16_f32 v65,v2,v3\n"
"v_cvt_pk_bf16_f32 v66,v4,v5\n"
"v_mfma_f32_16x16x32_bf16 v[252:255],a[124:127],v[92:95],v[252:255]\n"
"buffer_load_dwordx4 v117,s[24:27],s64 offen offset:0 sc1 lds\n"
"v_mfma_f32_16x16x32_bf16 v[156:159],a[124:127],v[80:83],v[156:159]\n"
"buffer_load_dwordx4 v117,s[24:27],s64 offen offset:1024 sc1 lds\n"
"buffer_load_dwordx4 v117,s[24:27],s64 offen offset:2048 sc1 lds\n"
"buffer_load_dwordx4 v117,s[24:27],s64 offen offset:3072 sc1 lds\n"
"buffer_store_dwordx4 a[244:247],v112,s[24:27],s56 offen offset:1024 sc1\n"
"s_add_u32 s64,s64,16384\n"
"v_cvt_pk_bf16_f32 v67,v6,v7\n"
"v_cvt_pk_bf16_f32 v68,v8,v9\n"
"v_cvt_pk_bf16_f32 v69,v10,v11\n"
"v_cvt_pk_bf16_f32 v70,v12,v13\n"
"v_cvt_pk_bf16_f32 v71,v14,v15\n"
"v_cvt_pk_bf16_f32 v72,v16,v17\n"
"v_cvt_pk_bf16_f32 v73,v18,v19\n"
"v_cvt_pk_bf16_f32 v74,v20,v21\n"
"v_cvt_pk_bf16_f32 v75,v22,v23\n"
"v_cvt_pk_bf16_f32 v76,v24,v25\n"
"v_cvt_pk_bf16_f32 v77,v26,v27\n"
"v_cvt_pk_bf16_f32 v78,v28,v29\n"
"v_cvt_pk_bf16_f32 v79,v30,v31\n"
"v_max3_f32 v118,v48,v49,v50\n"
"v_max3_f32 v118,v118,v51,v52\n"
"v_max3_f32 v118,v118,v53,v54\n"
"v_max3_f32 v118,v118,v55,v60\n"
"v_max3_f32 v118,v118,v61,v62\n"
"v_max3_f32 v118,v118,v63,v56\n"
"v_max3_f32 v118,v118,v57,v58\n"
"v_max3_f32 v118,v118,v59,v40\n"
"v_max3_f32 v118,v118,v41,v42\n"
"v_max3_f32 v118,v118,v43,v44\n"
"v_max3_f32 v118,v118,v45,v46\n"
"v_max_f32 v118,v118,v47\n"
"v_cmp_lt_f32 vcc,s93,v118\n"
"s_nop 0\n"
"s_cbranch_vccnz .Lfk_cs_u0\n"
".Lfk_cr_u0:\n"
"v_cmp_gt_u32 vcc,s101,v125\n"
"s_nop 0\n"
"s_cbranch_vccnz .Lfk_cs_mu0\n"
".Lfk_cr_mu0:\n"
"v_mfma_f32_16x16x32_bf16 v[24:27],a[64:67],a[48:51],v[108:111]\n"
"ds_read_b128 a[96:99],v114 offset:40960\n"
"ds_read_b128 a[100:103],v114 offset:41984\n"
"v_mfma_f32_16x16x32_bf16 v[24:27],a[68:71],a[52:55],v[24:27]\n"
"ds_read_b128 a[104:107],v114 offset:43008\n"
"ds_read_b128 a[108:111],v114 offset:44032\n"
"v_mfma_f32_16x16x32_bf16 v[24:27],a[72:75],a[56:59],v[24:27]\n"
"ds_read_b128 a[112:115],v114 offset:45056\n"
"ds_read_b128 a[116:119],v114 offset:46080\n"
"v_mfma_f32_16x16x32_bf16 v[24:27],a[76:79],a[60:63],v[24:27]\n"
"ds_read_b128 a[120:123],v114 offset:47104\n"
"ds_read_b128 a[124:127],v114 offset:48128\n"
"v_mfma_f32_16x16x32_bf16 v[28:31],a[92:95],a[60:63],v[108:111]\n"
"ds_read_b128 a[16:19],v116 offset:8192\n"
"ds_read_b128 a[20:23],v116 offset:9216\n"
"v_mfma_f32_16x16x32_bf16 v[28:31],a[88:91],a[56:59],v[28:31]\n"
"ds_read_b128 a[24:27],v116 offset:10240\n"
"ds_read_b128 a[28:31],v116 offset:11264\n"
"v_mfma_f32_16x16x32_bf16 v[28:31],a[84:87],a[52:55],v[28:31]\n"
"v_add_f32 v16,0x80000000,v126\n"
"v_min_f32 v16,v16,v104\n"
"v_mfma_f32_16x16x32_bf16 v[28:31],a[80:83],a[48:51],v[28:31]\n"
"v_add_f32 v17,0xf149f2ca,v126\n"
"v_add_f32 v18,0xf1c9f2ca,v126\n"
"v_add_f32 v19,0xf2177617,v126\n"
"v_min_f32 v19,v19,v107\n"
"v_add_f32 v20,0xf349f2ca,v126\n"
"v_min_f32 v20,v20,v104\n"
"v_add_f32 v21,0xf35691f6,v126\n"
"v_min_f32 v21,v21,v105\n"
"v_add_f32 v22,0xf3633123,v126\n"
"v_min_f32 v22,v22,v106\n"
"v_add_f32 v23,0xf36fd050,v126\n"
"v_min_f32 v23,v23,v107\n"
"v_min_f32 v18,v18,v106\n"
"v_min_f32 v17,v17,v105\n"
"v_mfma_f32_16x16x32_bf16 v[20:23],a[80:83],a[32:35],v[20:23]\n"
"s_add_u32 s101,s101,4\n"
"v_exp_f32 v40,v40\n"
"v_mfma_f32_16x16x32_bf16 v[20:23],a[84:87],a[36:39],v[20:23]\n"
"s_add_u32 m0,s55,8192\n"
"v_exp_f32 v41,v41\n"
"v_mfma_f32_16x16x32_bf16 v[20:23],a[88:91],a[40:43],v[20:23]\n"
"s_add_u32 s54,s54,s22\n"
"v_exp_f32 v42,v42\n"
"v_mfma_f32_16x16x32_bf16 v[20:23],a[92:95],a[44:47],v[20:23]\n"
"v_exp_f32 v43,v43\n"
"v_mfma_f32_16x16x32_bf16 v[16:19],a[76:79],a[44:47],v[16:19]\n"
"v_exp_f32 v44,v44\n"
"v_mfma_f32_16x16x32_bf16 v[16:19],a[72:75],a[40:43],v[16:19]\n"
"v_exp_f32 v45,v45\n"
"v_mfma_f32_16x16x32_bf16 v[16:19],a[68:71],a[36:39],v[16:19]\n"
"v_exp_f32 v46,v46\n"
"v_mfma_f32_16x16x32_bf16 v[16:19],a[64:67],a[32:35],v[16:19]\n"
"s_waitcnt lgkmcnt(4)\n"
"v_exp_f32 v47,v47\n"
"v_mfma_f32_16x16x32_bf16 v[128:131],a[96:99],v[64:67],v[128:131]\n"
"v_exp_f32 v48,v48\n"
"v_mfma_f32_16x16x32_bf16 v[160:163],a[96:99],v[68:71],v[160:163]\n"
"v_exp_f32 v49,v49\n"
"v_mfma_f32_16x16x32_bf16 v[192:195],a[96:99],v[72:75],v[192:195]\n"
"ds_read_b128 a[64:67],v113 offset:32768\n"
"ds_read_b128 a[68:71],v113 offset:33792\n"
"v_mfma_f32_16x16x32_bf16 v[224:227],a[96:99],v[76:79],v[224:227]\n"
"v_exp_f32 v50,v50\n"
"v_mfma_f32_16x16x32_bf16 a[140:143],a[144:147],v[76:79],a[140:143]\n"
"ds_read_b128 a[72:75],v113 offset:34816\n"
"ds_read_b128 a[76:79],v113 offset:35840\n"
"v_mfma_f32_16x16x32_bf16 v[228:231],a[100:103],v[76:79],v[228:231]\n"
"v_exp_f32 v51,v51\n"
"v_mfma_f32_16x16x32_bf16 v[132:135],a[100:103],v[64:67],v[132:135]\n"
"ds_read_b128 a[80:83],v113 offset:36864\n"
"ds_read_b128 a[84:87],v113 offset:37888\n"
"v_mfma_f32_16x16x32_bf16 v[164:167],a[100:103],v[68:71],v[164:167]\n"
"v_exp_f32 v52,v52\n"
"v_mfma_f32_16x16x32_bf16 v[196:199],a[100:103],v[72:75],v[196:199]\n"
"v_exp_f32 v53,v53\n"
"v_mfma_f32_16x16x32_bf16 a[136:139],a[144:147],v[72:75],a[136:139]\n"
"ds_read_b128 a[88:91],v113 offset:38912\n"
"ds_read_b128 a[92:95],v113 offset:39936\n"
"v_mfma_f32_16x16x32_bf16 v[200:203],a[104:107],v[72:75],v[200:203]\n"
"v_exp_f32 v54,v54\n"
"v_mfma_f32_16x16x32_bf16 v[232:235],a[104:107],v[76:79],v[232:235]\n"
"s_waitcnt vmcnt(11)\n"
"ds_add_u32 v124,a152\n"
"v_mfma_f32_16x16x32_bf16 v[136:139],a[104:107],v[64:67],v[136:139]\n"
"v_exp_f32 v55,v55\n"
"v_mfma_f32_16x16x32_bf16 v[168:171],a[104:107],v[68:71],v[168:171]\n"
"buffer_load_dwordx4 v115,s[32:35],s54 offen sc1 lds\n"
"v_mfma_f32_16x16x32_bf16 a[132:135],a[144:147],v[68:71],a[132:135]\n"
"s_add_u32 m0,s67,49152\n"
"v_exp_f32 v56,v56\n"
"v_mfma_f32_16x16x32_bf16 v[172:175],a[108:111],v[68:71],v[172:175]\n"
"buffer_load_dwordx4 v112,s[24:27],s64 offen offset:0 sc1 lds\n"
"v_mfma_f32_16x16x32_bf16 v[204:207],a[108:111],v[72:75],v[204:207]\n"
"v_exp_f32 v57,v57\n"
"v_mfma_f32_16x16x32_bf16 v[236:239],a[108:111],v[76:79],v[236:239]\n"
"buffer_load_dwordx4 v112,s[24:27],s64 offen offset:1024 sc1 lds\n"
"v_mfma_f32_16x16x32_bf16 v[140:143],a[108:111],v[64:67],v[140:143]\n"
"v_exp_f32 v58,v58\n"
"v_mfma_f32_16x16x32_bf16 a[128:131],a[144:147],v[64:67],a[128:131]\n"
"buffer_load_dwordx4 v112,s[24:27],s64 offen offset:2048 sc1 lds\n"
"v_mfma_f32_16x16x32_bf16 v[144:147],a[112:115],v[64:67],v[144:147]\n"
"v_exp_f32 v59,v59\n"
"v_mfma_f32_16x16x32_bf16 v[176:179],a[112:115],v[68:71],v[176:179]\n"
"buffer_load_dwordx4 v112,s[24:27],s64 offen offset:3072 sc1 lds\n"
"v_mfma_f32_16x16x32_bf16 v[208:211],a[112:115],v[72:75],v[208:211]\n"
"s_add_u32 m0,s67,53248\n"
"v_exp_f32 v60,v60\n"
"v_mfma_f32_16x16x32_bf16 v[240:243],a[112:115],v[76:79],v[240:243]\n"
"buffer_load_dwordx4 v117,s[24:27],s64 offen offset:0 sc1 lds\n"
"v_mfma_f32_16x16x32_bf16 v[244:247],a[116:119],v[76:79],v[244:247]\n"
"v_exp_f32 v61,v61\n"
"v_mfma_f32_16x16x32_bf16 v[148:151],a[116:119],v[64:67],v[148:151]\n"
"buffer_load_dwordx4 v117,s[24:27],s64 offen offset:1024 sc1 lds\n"
"v_mfma_f32_16x16x32_bf16 v[180:183],a[116:119],v[68:71],v[180:183]\n"
"v_exp_f32 v62,v62\n"
"v_mfma_f32_16x16x32_bf16 v[212:215],a[116:119],v[72:75],v[212:215]\n"
"ds_read_b32 v125,v124\n"
"v_cvt_pk_bf16_f32 v84,v40,v41\n"
"v_mfma_f32_16x16x32_bf16 v[216:219],a[120:123],v[72:75],v[216:219]\n"
"v_exp_f32 v63,v63\n"
"v_mfma_f32_16x16x32_bf16 v[248:251],a[120:123],v[76:79],v[248:251]\n"
"buffer_load_dwordx4 v117,s[24:27],s64 offen offset:2048 sc1 lds\n"
"v_mfma_f32_16x16x32_bf16 v[152:155],a[120:123],v[64:67],v[152:155]\n"
"buffer_load_dwordx4 v117,s[24:27],s64 offen offset:3072 sc1 lds\n"
"v_mfma_f32_16x16x32_bf16 v[184:187],a[120:123],v[68:71],v[184:187]\n"
"s_add_u32 s64,s64,16384\n"
"buffer_store_dwordx4 a[248:251],v112,s[24:27],s56 offen offset:2048 sc1\n"
"v_mfma_f32_16x16x32_bf16 v[188:191],a[124:127],v[68:71],v[188:191]\n"
"s_waitcnt lgkmcnt(0)\n"
"s_and_b32 s67,s67,8192\n"
"v_cvt_pk_bf16_f32 v85,v42,v43\n"
"v_mfma_f32_16x16x32_bf16 v[220:223],a[124:127],v[72:75],v[220:223]\n"
"v_cvt_pk_bf16_f32 v86,v44,v45\n"
"v_cvt_pk_bf16_f32 v87,v46,v47\n"
"v_mfma_f32_16x16x32_bf16 v[252:255],a[124:127],v[76:79],v[252:255]\n"
"v_cvt_pk_bf16_f32 v88,v48,v49\n"
"v_cvt_pk_bf16_f32 v89,v50,v51\n"
"v_mfma_f32_16x16x32_bf16 v[156:159],a[124:127],v[64:67],v[156:159]\n"
"v_cvt_pk_bf16_f32 v90,v52,v53\n"
"v_cvt_pk_bf16_f32 v91,v54,v55\n"
"v_cvt_pk_bf16_f32 v92,v56,v57\n"
"v_cvt_pk_bf16_f32 v93,v58,v59\n"
"v_cvt_pk_bf16_f32 v94,v60,v61\n"
"v_cvt_pk_bf16_f32 v95,v62,v63\n"
"v_max3_f32 v118,v24,v25,v26\n"
"v_max3_f32 v118,v118,v27,v28\n"
"v_max3_f32 v118,v118,v29,v30\n"
"v_max3_f32 v118,v118,v31,v20\n"
"v_max3_f32 v118,v118,v21,v22\n"
"v_max3_f32 v118,v118,v23,v16\n"
"v_max3_f32 v118,v118,v17,v18\n"
"v_max_f32 v118,v118,v19\n"
"v_cmp_lt_f32 vcc,s93,v118\n"
"s_nop 0\n"
"s_cbranch_vccnz .Lfk_cs_u1\n"
".Lfk_cr_u1:\n"
"v_cmp_gt_u32 vcc,s101,v125\n"
"s_nop 0\n"
"s_cbranch_vccnz .Lfk_cs_mu1\n"
".Lfk_cr_mu1:\n"
"v_mov_b32 v113,v112\n"
"v_add_f32 v56,0x80000000,v126\n"
"v_min_f32 v56,v56,v108\n"
"v_add_f32 v57,0xf149f2ca,v126\n"
"v_min_f32 v57,v57,v109\n"
"v_add_f32 v58,0xf1c9f2ca,v126\n"
"v_min_f32 v58,v58,v110\n"
"v_add_f32 v59,0xf2177617,v126\n"
"v_min_f32 v59,v59,v111\n"
"v_add_f32 v60,0xf349f2ca,v126\n"
"v_min_f32 v60,v60,v108\n"
"v_add_f32 v61,0xf35691f6,v126\n"
"v_min_f32 v61,v61,v109\n"
"v_add_f32 v62,0xf3633123,v126\n"
"v_min_f32 v62,v62,v110\n"
"v_add_f32 v63,0xf36fd050,v126\n"
"v_min_f32 v63,v63,v111\n"
"v_exp_f32 v16,v16\n"
"v_mfma_f32_16x16x32_bf16 v[56:59],a[64:67],a[48:51],v[56:59]\n"
"s_add_u32 s101,s101,4\n"
"ds_read_b128 a[96:99],v114 offset:8192\n"
"ds_read_b128 a[100:103],v114 offset:9216\n"
"ds_read_b128 a[104:107],v114 offset:10240\n"
"v_exp_f32 v17,v17\n"
"v_mfma_f32_16x16x32_bf16 v[56:59],a[68:71],a[52:55],v[56:59]\n"
"s_add_u32 m0,s55,12288\n"
"ds_read_b128 a[108:111],v114 offset:11264\n"
"ds_read_b128 a[112:115],v114 offset:12288\n"
"ds_read_b128 a[116:119],v114 offset:13312\n"
"v_mfma_f32_16x16x32_bf16 v[56:59],a[72:75],a[56:59],v[56:59]\n"
"ds_read_b128 a[120:123],v114 offset:14336\n"
"ds_read_b128 a[124:127],v114 offset:15360\n"
"ds_read_b128 a[32:35],v116 offset:24576\n"
"ds_read_b128 a[36:39],v116 offset:25600\n"
"ds_read_b128 a[40:43],v116 offset:26624\n"
"v_mfma_f32_16x16x32_bf16 v[56:59],a[76:79],a[60:63],v[56:59]\n"
"s_add_u32 s54,s54,s22\n"
"ds_read_b128 a[44:47],v116 offset:27648\n"
"v_exp_f32 v18,v18\n"
"v_mfma_f32_16x16x32_bf16 v[60:63],a[92:95],a[60:63],v[60:63]\n"
"buffer_load_dwordx4 v115,s[32:35],s54 offen sc1 lds\n"
"v_cvt_pk_bf16_f32 v72,v16,v17\n"
"v_mfma_f32_16x16x32_bf16 v[60:63],a[88:91],a[56:59],v[60:63]\n"
"s_add_u32 m0,s67,32768\n"
"v_exp_f32 v19,v19\n"
"v_mfma_f32_16x16x32_bf16 v[60:63],a[84:87],a[52:55],v[60:63]\n"
"buffer_load_dwordx4 v112,s[24:27],s64 offen offset:0 sc1 lds\n"
"v_cvt_pk_bf16_f32 v73,v18,v19\n"
"v_exp_f32 v20,v20\n"
"v_mfma_f32_16x16x32_bf16 v[60:63],a[80:83],a[48:51],v[60:63]\n"
"ds_read_b128 a[64:67],v113 offset:0\n"
"ds_read_b128 a[68:71],v113 offset:1024\n"
"ds_read_b128 a[72:75],v113 offset:2048\n"
"s_waitcnt lgkmcnt(7)\n"
"v_mfma_f32_16x16x32_bf16 v[160:163],a[96:99],v[84:87],v[160:163]\n"
"ds_read_b128 a[76:79],v113 offset:3072\n"
"ds_read_b128 a[80:83],v113 offset:4096\n"
"ds_read_b128 a[84:87],v113 offset:5120\n"
"ds_read_b128 a[88:91],v113 offset:6144\n"
"ds_read_b128 a[92:95],v113 offset:7168\n"
"v_exp_f32 v21,v21\n"
"v_mfma_f32_16x16x32_bf16 a[132:135],a[144:147],v[84:87],a[132:135]\n"
"buffer_load_dwordx4 v112,s[24:27],s64 offen offset:1024 sc1 lds\n"
"v_max3_f32 v118,v56,v57,v58\n"
"v_mfma_f32_16x16x32_bf16 v[164:167],a[100:103],v[84:87],v[164:167]\n"
"v_exp_f32 v22,v22\n"
"v_cvt_pk_bf16_f32 v74,v20,v21\n"
"ds_read_b128 a[48:51],v116 offset:57344\n"
"ds_read_b128 a[52:55],v116 offset:58368\n"
"v_mfma_f32_16x16x32_bf16 v[168:171],a[104:107],v[84:87],v[168:171]\n"
"v_max3_f32 v118,v118,v59,v60\n"
"v_max3_f32 v118,v118,v61,v62\n"
"v_max_f32 v118,v118,v63\n"
"v_exp_f32 v23,v23\n"
"v_mfma_f32_16x16x32_bf16 v[172:175],a[108:111],v[84:87],v[172:175]\n"
"ds_read_b128 a[56:59],v116 offset:59392\n"
"buffer_load_dwordx4 v112,s[24:27],s64 offen offset:2048 sc1 lds\n"
"ds_read_b128 a[60:63],v116 offset:60416\n"
"v_mfma_f32_16x16x32_bf16 v[176:179],a[112:115],v[84:87],v[176:179]\n"
"s_waitcnt vmcnt(15)\n"
"v_cvt_pk_bf16_f32 v75,v22,v23\n"
"v_accvgpr_read_b32 v122,a128\n"
"v_rcp_f32 v122,v122\n"
"v_mfma_f32_16x16x32_bf16 v[180:183],a[116:119],v[84:87],v[180:183]\n"
"ds_add_u32 v124,a152\n"
"v_cmp_lt_f32 vcc,s93,v118\n"
"v_exp_f32 v24,v24\n"
"v_pk_mul_f32 v[128:129],v[128:129],v[122:123] op_sel_hi:[1,0]\n"
"v_pk_mul_f32 v[130:131],v[130:131],v[122:123] op_sel_hi:[1,0]\n"
"v_pk_mul_f32 v[132:133],v[132:133],v[122:123] op_sel_hi:[1,0]\n"
"v_pk_mul_f32 v[134:135],v[134:135],v[122:123] op_sel_hi:[1,0]\n"
"v_mfma_f32_16x16x32_bf16 v[184:187],a[120:123],v[84:87],v[184:187]\n"
"buffer_load_dwordx4 v112,s[24:27],s64 offen offset:3072 sc1 lds\n"
"v_cvt_pk_bf16_f32 v128,v128,v129\n"
"v_cvt_pk_bf16_f32 v129,v130,v131\n"
"v_mfma_f32_16x16x32_bf16 v[188:191],a[124:127],v[84:87],v[188:191]\n"
"s_add_u32 m0,s67,36864\n"
"v_cvt_pk_bf16_f32 v130,v132,v133\n"
"v_cvt_pk_bf16_f32 v131,v134,v135\n"
"v_exp_f32 v25,v25\n"
"v_mfma_f32_16x16x32_bf16 v[192:195],a[96:99],v[88:91],v[192:195]\n"
"buffer_store_dwordx4 v[128:131],v112,s[24:27],s70 offen offset:0 sc1\n"
"v_cvt_pk_bf16_f32 v76,v24,v25\n"
"v_pk_mul_f32 v[136:137],v[136:137],v[122:123] op_sel_hi:[1,0]\n"
"v_pk_mul_f32 v[138:139],v[138:139],v[122:123] op_sel_hi:[1,0]\n"
"v_pk_mul_f32 v[140:141],v[140:141],v[122:123] op_sel_hi:[1,0]\n"
"v_pk_mul_f32 v[142:143],v[142:143],v[122:123] op_sel_hi:[1,0]\n"
"v_exp_f32 v26,v26\n"
"v_mfma_f32_16x16x32_bf16 a[136:139],a[144:147],v[88:91],a[136:139]\n"
"buffer_load_dwordx4 v117,s[24:27],s64 offen offset:0 sc1 lds\n"
"v_cvt_pk_bf16_f32 v136,v136,v137\n"
"v_mfma_f32_16x16x32_bf16 v[196:199],a[100:103],v[88:91],v[196:199]\n"
"v_exp_f32 v27,v27\n"
"v_cvt_pk_bf16_f32 v137,v138,v139\n"
"v_cvt_pk_bf16_f32 v138,v140,v141\n"
"v_cvt_pk_bf16_f32 v139,v142,v143\n"
"v_cvt_pk_bf16_f32 v77,v26,v27\n"
"v_mfma_f32_16x16x32_bf16 v[200:203],a[104:107],v[88:91],v[200:203]\n"
"buffer_store_dwordx4 v[136:139],v112,s[24:27],s70 offen offset:1024 sc1\n"
"v_pk_mul_f32 v[144:145],v[144:145],v[122:123] op_sel_hi:[1,0]\n"
"v_pk_mul_f32 v[146:147],v[146:147],v[122:123] op_sel_hi:[1,0]\n"
"v_pk_mul_f32 v[148:149],v[148:149],v[122:123] op_sel_hi:[1,0]\n"
"v_pk_mul_f32 v[150:151],v[150:151],v[122:123] op_sel_hi:[1,0]\n"
"v_mfma_f32_16x16x32_bf16 v[204:207],a[108:111],v[88:91],v[204:207]\n"
"buffer_load_dwordx4 v117,s[24:27],s64 offen offset:1024 sc1 lds\n"
"v_exp_f32 v28,v28\n"
"v_mfma_f32_16x16x32_bf16 v[208:211],a[112:115],v[88:91],v[208:211]\n"
"v_cvt_pk_bf16_f32 v144,v144,v145\n"
"v_cvt_pk_bf16_f32 v145,v146,v147\n"
"v_cvt_pk_bf16_f32 v146,v148,v149\n"
"v_cvt_pk_bf16_f32 v147,v150,v151\n"
"v_mfma_f32_16x16x32_bf16 v[212:215],a[116:119],v[88:91],v[212:215]\n"
"buffer_store_dwordx4 v[144:147],v112,s[24:27],s70 offen offset:2048 sc1\n"
"v_pk_mul_f32 v[152:153],v[152:153],v[122:123] op_sel_hi:[1,0]\n"
"v_pk_mul_f32 v[154:155],v[154:155],v[122:123] op_sel_hi:[1,0]\n"
"v_pk_mul_f32 v[156:157],v[156:157],v[122:123] op_sel_hi:[1,0]\n"
"v_mfma_f32_16x16x32_bf16 v[216:219],a[120:123],v[88:91],v[216:219]\n"
"buffer_load_dwordx4 v117,s[24:27],s64 offen offset:2048 sc1 lds\n"
"v_exp_f32 v29,v29\n"
"v_cvt_pk_bf16_f32 v152,v152,v153\n"
"v_cvt_pk_bf16_f32 v78,v28,v29\n"
"v_pk_mul_f32 v[158:159],v[158:159],v[122:123] op_sel_hi:[1,0]\n"
"v_mfma_f32_16x16x32_bf16 v[220:223],a[124:127],v[88:91],v[220:223]\n"
"v_cvt_pk_bf16_f32 v153,v154,v155\n"
"v_cvt_pk_bf16_f32 v154,v156,v157\n"
"v_cvt_pk_bf16_f32 v155,v158,v159\n"
"v_mfma_f32_16x16x32_bf16 v[224:227],a[96:99],v[92:95],v[224:227]\n"
"ds_read_b32 v125,v124\n"
"buffer_store_dwordx4 v[152:155],v112,s[24:27],s70 offen offset:3072 sc1\n"
"buffer_load_dwordx4 v117,s[24:27],s64 offen offset:3072 sc1 lds\n"
"v_accvgpr_read_b32 v122,a132\n"
"v_mfma_f32_16x16x32_bf16 a[140:143],a[144:147],v[92:95],a[140:143]\n"
"s_add_u32 s70,s70,262144\n"
"v_exp_f32 v31,v31\n"
"v_mfma_f32_16x16x32_bf16 v[228:231],a[100:103],v[92:95],v[228:231]\n"
"s_add_u32 s64,s64,16384\n"
"v_exp_f32 v30,v30\n"
"v_rcp_f32 v122,v122\n"
"v_cvt_pk_bf16_f32 v79,v30,v31\n"
"v_pk_mul_f32 v[160:161],v[160:161],v[122:123] op_sel_hi:[1,0]\n"
"v_pk_mul_f32 v[162:163],v[162:163],v[122:123] op_sel_hi:[1,0]\n"
"v_pk_mul_f32 v[164:165],v[164:165],v[122:123] op_sel_hi:[1,0]\n"
"v_pk_mul_f32 v[166:167],v[166:167],v[122:123] op_sel_hi:[1,0]\n"
"v_mfma_f32_16x16x32_bf16 v[232:235],a[104:107],v[92:95],v[232:235]\n"
"buffer_store_dwordx4 a[252:255],v112,s[24:27],s56 offen offset:3072 sc1\n"
"v_cvt_pk_bf16_f32 v160,v160,v161\n"
"v_cvt_pk_bf16_f32 v161,v162,v163\n"
"v_cvt_pk_bf16_f32 v162,v164,v165\n"
"v_cvt_pk_bf16_f32 v163,v166,v167\n"
"v_mfma_f32_16x16x32_bf16 v[236:239],a[108:111],v[92:95],v[236:239]\n"
"buffer_store_dwordx4 v[160:163],v112,s[24:27],s70 offen offset:0 sc1\n"
"v_pk_mul_f32 v[168:169],v[168:169],v[122:123] op_sel_hi:[1,0]\n"
"v_pk_mul_f32 v[170:171],v[170:171],v[122:123] op_sel_hi:[1,0]\n"
"v_pk_mul_f32 v[172:173],v[172:173],v[122:123] op_sel_hi:[1,0]\n"
"v_pk_mul_f32 v[174:175],v[174:175],v[122:123] op_sel_hi:[1,0]\n"
"v_mfma_f32_16x16x32_bf16 v[240:243],a[112:115],v[92:95],v[240:243]\n"
"s_waitcnt lgkmcnt(0)\n"
"v_cvt_pk_bf16_f32 v168,v168,v169\n"
"v_cvt_pk_bf16_f32 v169,v170,v171\n"
"v_cvt_pk_bf16_f32 v170,v172,v173\n"
"v_cvt_pk_bf16_f32 v171,v174,v175\n"
"v_mfma_f32_16x16x32_bf16 v[244:247],a[116:119],v[92:95],v[244:247]\n"
"buffer_store_dwordx4 v[168:171],v112,s[24:27],s70 offen offset:1024 sc1\n"
"v_pk_mul_f32 v[176:177],v[176:177],v[122:123] op_sel_hi:[1,0]\n"
"v_pk_mul_f32 v[178:179],v[178:179],v[122:123] op_sel_hi:[1,0]\n"
"v_pk_mul_f32 v[180:181],v[180:181],v[122:123] op_sel_hi:[1,0]\n"
"v_pk_mul_f32 v[182:183],v[182:183],v[122:123] op_sel_hi:[1,0]\n"
"v_mfma_f32_16x16x32_bf16 v[248:251],a[120:123],v[92:95],v[248:251]\n"
"v_cvt_pk_bf16_f32 v176,v176,v177\n"
"v_cvt_pk_bf16_f32 v177,v178,v179\n"
"v_cvt_pk_bf16_f32 v178,v180,v181\n"
"v_cvt_pk_bf16_f32 v179,v182,v183\n"
"v_mfma_f32_16x16x32_bf16 v[252:255],a[124:127],v[92:95],v[252:255]\n"
"buffer_store_dwordx4 v[176:179],v112,s[24:27],s70 offen offset:2048 sc1\n"
"v_pk_mul_f32 v[184:185],v[184:185],v[122:123] op_sel_hi:[1,0]\n"
"v_pk_mul_f32 v[186:187],v[186:187],v[122:123] op_sel_hi:[1,0]\n"
"v_pk_mul_f32 v[188:189],v[188:189],v[122:123] op_sel_hi:[1,0]\n"
"v_pk_mul_f32 v[190:191],v[190:191],v[122:123] op_sel_hi:[1,0]\n"
"v_cvt_pk_bf16_f32 v184,v184,v185\n"
"v_cvt_pk_bf16_f32 v185,v186,v187\n"
"v_cvt_pk_bf16_f32 v186,v188,v189\n"
"v_cvt_pk_bf16_f32 v187,v190,v191\n"
"buffer_store_dwordx4 v[184:187],v112,s[24:27],s70 offen offset:3072 sc1\n"
"s_add_u32 s70,s70,262144\n"
"s_cbranch_vccnz .Lfk_cs_u2\n"
".Lfk_cr_u2:\n"
"v_cmp_gt_u32 vcc,s101,v125\n"
"s_nop 0\n"
"s_cbranch_vccnz .Lfk_cs_mu2\n"
".Lfk_cr_mu2:\n"
"s_add_u32 s89,s89,1\n"
"s_cmp_eq_u32 s89,8\n"
"s_cbranch_scc1 .Lfk_tailB7\n"
".Lfk_winB:\n"
"v_mfma_f32_16x16x32_bf16 v[0:3],a[64:67],a[0:3],v[96:99]\n"
"s_swappc_b64 s[94:95],s[20:21]\n"
"ds_read_b128 a[96:99],v114 offset:24576\n"
"v_mfma_f32_16x16x32_bf16 v[0:3],a[68:71],a[4:7],v[0:3]\n"
"s_add_u32 s101,s101,4\n"
"ds_read_b128 a[100:103],v114 offset:25600\n"
"ds_read_b128 a[104:107],v114 offset:26624\n"
"ds_read_b128 a[108:111],v114 offset:27648\n"
"v_mfma_f32_16x16x32_bf16 v[0:3],a[72:75],a[8:11],v[0:3]\n"
"ds_read_b128 a[112:115],v114 offset:28672\n"
"ds_read_b128 a[116:119],v114 offset:29696\n"
"v_mfma_f32_16x16x32_bf16 v[0:3],a[76:79],a[12:15],v[0:3]\n"
"ds_read_b128 a[120:123],v114 offset:30720\n"
"ds_read_b128 a[124:127],v114 offset:31744\n"
"v_mfma_f32_16x16x32_bf16 v[4:7],a[92:95],a[12:15],v[96:99]\n"
"v_exp_f32 v56,v56\n"
"v_mfma_f32_16x16x32_bf16 v[4:7],a[88:91],a[8:11],v[4:7]\n"
"v_exp_f32 v57,v57\n"
"v_mfma_f32_16x16x32_bf16 v[4:7],a[84:87],a[4:7],v[4:7]\n"
"v_cvt_pk_bf16_f32 v92,v56,v57\n"
"v_mfma_f32_16x16x32_bf16 v[4:7],a[80:83],a[0:3],v[4:7]\n"
"s_add_u32 m0,s67,0\n"
"s_waitcnt lgkmcnt(0)\n"
"v_mfma_f32_16x16x32_bf16 v[192:195],a[96:99],v[72:75],v[192:195]\n"
"buffer_load_dwordx4 v112,s[24:27],s64 offen offset:0 sc1 lds\n"
"v_mfma_f32_16x16x32_bf16 a[136:139],a[144:147],v[72:75],a[136:139]\n"
"buffer_load_dwordx4 v112,s[24:27],s64 offen offset:1024 sc1 lds\n"
"v_mfma_f32_16x16x32_bf16 v[196:199],a[100:103],v[72:75],v[196:199]\n"
"v_exp_f32 v58,v58\n"
"v_mfma_f32_16x16x32_bf16 v[200:203],a[104:107],v[72:75],v[200:203]\n"
"v_max3_f32 v118,v0,v1,v2\n"
"v_mfma_f32_16x16x32_bf16 v[204:207],a[108:111],v[72:75],v[204:207]\n"
"v_max3_f32 v118,v118,v3,v4\n"
"v_max3_f32 v118,v118,v5,v6\n"
"v_mfma_f32_16x16x32_bf16 v[208:211],a[112:115],v[72:75],v[208:211]\n"
"v_max_f32 v118,v118,v7\n"
"v_mov_b32 v120,v118\n"
"v_mfma_f32_16x16x32_bf16 v[212:215],a[116:119],v[72:75],v[212:215]\n"
"v_mfma_f32_16x16x32_bf16 v[216:219],a[120:123],v[72:75],v[216:219]\n"
"v_mfma_f32_16x16x32_bf16 v[220:223],a[124:127],v[72:75],v[220:223]\n"
"v_exp_f32 v59,v59\n"
"v_mfma_f32_16x16x32_bf16 v[224:227],a[96:99],v[76:79],v[224:227]\n"
"v_cvt_pk_bf16_f32 v93,v58,v59\n"
"v_accvgpr_read_b32 v122,a136\n"
"v_mfma_f32_16x16x32_bf16 a[140:143],a[144:147],v[76:79],a[140:143]\n"
"buffer_load_dwordx4 v112,s[24:27],s64 offen offset:2048 sc1 lds\n"
"v_mfma_f32_16x16x32_bf16 v[228:231],a[100:103],v[76:79],v[228:231]\n"
"v_rcp_f32 v122,v122\n"
"v_mfma_f32_16x16x32_bf16 v[232:235],a[104:107],v[76:79],v[232:235]\n"
"buffer_load_dwordx4 v112,s[24:27],s64 offen offset:3072 sc1 lds\n"
"v_pk_mul_f32 v[192:193],v[192:193],v[122:123] op_sel_hi:[1,0]\n"
"v_pk_mul_f32 v[194:195],v[194:195],v[122:123] op_sel_hi:[1,0]\n"
"v_mfma_f32_16x16x32_bf16 v[236:239],a[108:111],v[76:79],v[236:239]\n"
"v_cvt_pk_bf16_f32 v192,v192,v193\n"
"v_exp_f32 v60,v60\n"
"v_cvt_pk_bf16_f32 v193,v194,v195\n"
"v_pk_mul_f32 v[196:197],v[196:197],v[122:123] op_sel_hi:[1,0]\n"
"v_pk_mul_f32 v[198:199],v[198:199],v[122:123] op_sel_hi:[1,0]\n"
"v_mfma_f32_16x16x32_bf16 v[240:243],a[112:115],v[76:79],v[240:243]\n"
"s_add_u32 m0,s67,4096\n"
"v_cvt_pk_bf16_f32 v194,v196,v197\n"
"v_cvt_pk_bf16_f32 v195,v198,v199\n"
"v_mfma_f32_16x16x32_bf16 v[244:247],a[116:119],v[76:79],v[244:247]\n"
"v_mfma_f32_16x16x32_bf16 v[248:251],a[120:123],v[76:79],v[248:251]\n"
"buffer_store_dwordx4 v[192:195],v112,s[24:27],s70 offen offset:0 sc1\n"
"v_pk_mul_f32 v[200:201],v[200:201],v[122:123] op_sel_hi:[1,0]\n"
"v_mfma_f32_16x16x32_bf16 v[252:255],a[124:127],v[76:79],v[252:255]\n"
"buffer_load_dwordx4 v117,s[24:27],s64 offen offset:0 sc1 lds\n"
"v_cvt_pk_bf16_f32 v200,v200,v201\n"
"v_exp_f32 v61,v61\n"
"v_pk_mul_f32 v[202:203],v[202:203],v[122:123] op_sel_hi:[1,0]\n"
"v_pk_mul_f32 v[204:205],v[204:205],v[122:123] op_sel_hi:[1,0]\n"
"v_pk_mul_f32 v[206:207],v[206:207],v[122:123] op_sel_hi:[1,0]\n"
"v_mfma_f32_16x16x32_bf16 v[12:15],a[80:83],a[16:19],v[100:103]\n"
"v_cvt_pk_bf16_f32 v201,v202,v203\n"
"v_cvt_pk_bf16_f32 v202,v204,v205\n"
"v_mfma_f32_16x16x32_bf16 v[12:15],a[84:87],a[20:23],v[12:15]\n"
"v_cvt_pk_bf16_f32 v203,v206,v207\n"
"v_cvt_pk_bf16_f32 v94,v60,v61\n"
"v_mfma_f32_16x16x32_bf16 v[12:15],a[88:91],a[24:27],v[12:15]\n"
"buffer_load_dwordx4 v117,s[24:27],s64 offen offset:1024 sc1 lds\n"
"v_mfma_f32_16x16x32_bf16 v[12:15],a[92:95],a[28:31],v[12:15]\n"
"buffer_store_dwordx4 v[200:203],v112,s[24:27],s70 offen offset:1024 sc1\n"
"v_pk_mul_f32 v[208:209],v[208:209],v[122:123] op_sel_hi:[1,0]\n"
"v_pk_mul_f32 v[210:211],v[210:211],v[122:123] op_sel_hi:[1,0]\n"
"v_mfma_f32_16x16x32_bf16 v[8:11],a[76:79],a[28:31],v[100:103]\n"
"buffer_load_dwordx4 v117,s[24:27],s64 offen offset:2048 sc1 lds\n"
"v_cvt_pk_bf16_f32 v208,v208,v209\n"
"v_cvt_pk_bf16_f32 v209,v210,v211\n"
"v_pk_mul_f32 v[212:213],v[212:213],v[122:123] op_sel_hi:[1,0]\n"
"v_pk_mul_f32 v[214:215],v[214:215],v[122:123] op_sel_hi:[1,0]\n"
"v_mfma_f32_16x16x32_bf16 v[8:11],a[72:75],a[24:27],v[8:11]\n"
"v_cvt_pk_bf16_f32 v210,v212,v213\n"
"v_cvt_pk_bf16_f32 v211,v214,v215\n"
"v_mfma_f32_16x16x32_bf16 v[8:11],a[68:71],a[20:23],v[8:11]\n"
"v_exp_f32 v62,v62\n"
"v_mfma_f32_16x16x32_bf16 v[8:11],a[64:67],a[16:19],v[8:11]\n"
"buffer_store_dwordx4 v[208:211],v112,s[24:27],s70 offen offset:2048 sc1\n"
"v_pk_mul_f32 v[216:217],v[216:217],v[122:123] op_sel_hi:[1,0]\n"
"v_mfma_f32_16x16x32_bf16 v[16:19],a[64:67],a[32:35],v[104:107]\n"
"v_cvt_pk_bf16_f32 v216,v216,v217\n"
"buffer_load_dwordx4 v117,s[24:27],s64 offen offset:3072 sc1 lds\n"
"v_pk_mul_f32 v[218:219],v[218:219],v[122:123] op_sel_hi:[1,0]\n"
"v_pk_mul_f32 v[220:221],v[220:221],v[122:123] op_sel_hi:[1,0]\n"
"v_pk_mul_f32 v[222:223],v[222:223],v[122:123] op_sel_hi:[1,0]\n"
"v_mfma_f32_16x16x32_bf16 v[16:19],a[68:71],a[36:39],v[16:19]\n"
"v_cvt_pk_bf16_f32 v217,v218,v219\n"
"v_cvt_pk_bf16_f32 v218,v220,v221\n"
"v_mfma_f32_16x16x32_bf16 v[16:19],a[72:75],a[40:43],v[16:19]\n"
"s_add_u32 s64,s64,16384\n"
"v_exp_f32 v63,v63\n"
"v_mfma_f32_16x16x32_bf16 v[16:19],a[76:79],a[44:47],v[16:19]\n"
"s_andn2_b32 m0,s55,16384\n"
"s_add_u32 s54,s54,s22\n"
"v_max3_f32 v119,v8,v9,v10\n"
"v_mfma_f32_16x16x32_bf16 v[20:23],a[92:95],a[44:47],v[104:107]\n"
"v_max3_f32 v119,v119,v11,v12\n"
"v_cvt_pk_bf16_f32 v95,v62,v63\n"
"v_mfma_f32_16x16x32_bf16 v[20:23],a[88:91],a[40:43],v[20:23]\n"
"buffer_load_dwordx4 v115,s[32:35],s54 offen sc1 lds\n"
"v_mfma_f32_16x16x32_bf16 v[20:23],a[84:87],a[36:39],v[20:23]\n"
"v_max3_f32 v119,v119,v13,v14\n"
"v_max_f32 v119,v119,v15\n"
"v_mfma_f32_16x16x32_bf16 v[20:23],a[80:83],a[32:35],v[20:23]\n"
"v_cvt_pk_bf16_f32 v219,v222,v223\n"
"v_mfma_f32_16x16x32_bf16 v[28:31],a[80:83],a[48:51],v[108:111]\n"
"buffer_store_dwordx4 v[216:219],v112,s[24:27],s70 offen offset:3072 sc1\n"
"buffer_store_dwordx4 a[240:243],v112,s[24:27],s56 offen offset:0 sc1\n"
"v_mfma_f32_16x16x32_bf16 v[28:31],a[84:87],a[52:55],v[28:31]\n"
"s_add_u32 s70,s70,262144\n"
"v_max_f32 v118,v118,v119\n"
"v_min_f32 v120,v120,v119\n"
"v_mfma_f32_16x16x32_bf16 v[28:31],a[88:91],a[56:59],v[28:31]\n"
"v_max3_f32 v119,v16,v17,v18\n"
"v_mfma_f32_16x16x32_bf16 v[28:31],a[92:95],a[60:63],v[28:31]\n"
"v_max3_f32 v119,v119,v19,v20\n"
"v_max3_f32 v119,v119,v21,v22\n"
"v_mfma_f32_16x16x32_bf16 v[24:27],a[76:79],a[60:63],v[108:111]\n"
"v_max_f32 v119,v119,v23\n"
"v_max_f32 v118,v118,v119\n"
"v_mfma_f32_16x16x32_bf16 v[24:27],a[72:75],a[56:59],v[24:27]\n"
"v_min_f32 v120,v120,v119\n"
"v_mfma_f32_16x16x32_bf16 v[24:27],a[68:71],a[52:55],v[24:27]\n"
"v_mfma_f32_16x16x32_bf16 v[24:27],a[64:67],a[48:51],v[24:27]\n"
"ds_read_b128 a[64:67],v113 offset:16384\n"
"ds_read_b128 a[68:71],v113 offset:17408\n"
"ds_read_b128 a[72:75],v113 offset:18432\n"
"ds_read_b128 a[76:79],v113 offset:19456\n"
"ds_read_b128 a[80:83],v113 offset:20480\n"
"ds_read_b128 a[84:87],v113 offset:21504\n"
"ds_read_b128 a[88:91],v113 offset:22528\n"
"ds_read_b128 a[92:95],v113 offset:23552\n"
"v_max3_f32 v119,v24,v25,v26\n"
"v_max3_f32 v119,v119,v27,v28\n"
"v_max3_f32 v119,v119,v29,v30\n"
"v_max_f32 v119,v119,v31\n"
"v_min_f32 v120,v120,v119\n"
"v_max_f32 v118,v118,v119\n"
"v_cmp_gt_f32 s[96:97],s16,v120\n"
"v_cmp_lt_f32 vcc,s93,v118\n"
"s_waitcnt vmcnt(25)\n"
"ds_add_u32 v124,a152\n"
"ds_read_b32 v125,v124\n"
"s_waitcnt lgkmcnt(0)\n"
"s_or_b64 s[96:97],s[96:97],vcc\n"
"s_cbranch_scc1 .Lfk_cs_v3\n"
".Lfk_cr_v3:\n"
"v_cmp_gt_u32 vcc,s101,v125\n"
"s_nop 0\n"
"s_cbranch_vccnz .Lfk_cs_mv3\n"
".Lfk_cr_mv3:\n"
"v_exp_f32 v0,v0\n"
"v_mfma_f32_16x16x32_bf16 v[32:35],a[64:67],a[0:3],v[96:99]\n"
"s_add_u32 s101,s101,4\n"
"ds_read_b128 a[96:99],v114 offset:57344\n"
"ds_read_b128 a[100:103],v114 offset:58368\n"
"v_exp_f32 v1,v1\n"
"v_mfma_f32_16x16x32_bf16 v[32:35],a[68:71],a[4:7],v[32:35]\n"
"s_add_u32 m0,s55,4096\n"
"ds_read_b128 a[104:107],v114 offset:59392\n"
"ds_read_b128 a[108:111],v114 offset:60416\n"
"v_exp_f32 v2,v2\n"
"v_mfma_f32_16x16x32_bf16 v[32:35],a[72:75],a[8:11],v[32:35]\n"
"s_add_u32 s54,s54,s22\n"
"ds_read_b128 a[112:115],v114 offset:61440\n"
"ds_read_b128 a[116:119],v114 offset:62464\n"
"v_exp_f32 v3,v3\n"
"v_mfma_f32_16x16x32_bf16 v[32:35],a[76:79],a[12:15],v[32:35]\n"
"ds_read_b128 a[120:123],v114 offset:63488\n"
"ds_read_b128 a[124:127],v114 offset:64512\n"
"v_exp_f32 v4,v4\n"
"v_mfma_f32_16x16x32_bf16 v[36:39],a[92:95],a[12:15],v[96:99]\n"
"buffer_load_dwordx4 v115,s[32:35],s54 offen sc1 lds\n"
"v_mfma_f32_16x16x32_bf16 v[36:39],a[88:91],a[8:11],v[36:39]\n"
"s_add_u32 m0,s67,16384\n"
"v_exp_f32 v5,v5\n"
"v_cvt_pk_bf16_f32 v64,v0,v1\n"
"v_cvt_pk_bf16_f32 v65,v2,v3\n"
"v_mfma_f32_16x16x32_bf16 v[36:39],a[84:87],a[4:7],v[36:39]\n"
"v_exp_f32 v6,v6\n"
"v_exp_f32 v7,v7\n"
"v_mfma_f32_16x16x32_bf16 v[36:39],a[80:83],a[0:3],v[36:39]\n"
"buffer_load_dwordx4 v112,s[24:27],s64 offen offset:0 sc1 lds\n"
"s_waitcnt lgkmcnt(0)\n"
"v_mfma_f32_16x16x32_bf16 v[224:227],a[96:99],v[92:95],v[224:227]\n"
"v_exp_f32 v8,v8\n"
"v_cvt_pk_bf16_f32 v67,v6,v7\n"
"v_mfma_f32_16x16x32_bf16 a[140:143],a[144:147],v[92:95],a[140:143]\n"
"v_exp_f32 v9,v9\n"
"v_exp_f32 v10,v10\n"
"v_mfma_f32_16x16x32_bf16 v[228:231],a[100:103],v[92:95],v[228:231]\n"
"buffer_load_dwordx4 v112,s[24:27],s64 offen offset:1024 sc1 lds\n"
"v_mfma_f32_16x16x32_bf16 v[232:235],a[104:107],v[92:95],v[232:235]\n"
"v_cvt_pk_bf16_f32 v66,v4,v5\n"
"v_cvt_pk_bf16_f32 v68,v8,v9\n"
"v_mfma_f32_16x16x32_bf16 v[236:239],a[108:111],v[92:95],v[236:239]\n"
"v_exp_f32 v11,v11\n"
"v_exp_f32 v12,v12\n"
"v_mfma_f32_16x16x32_bf16 v[240:243],a[112:115],v[92:95],v[240:243]\n"
"buffer_load_dwordx4 v112,s[24:27],s64 offen offset:2048 sc1 lds\n"
"v_mfma_f32_16x16x32_bf16 v[244:247],a[116:119],v[92:95],v[244:247]\n"
"v_exp_f32 v13,v13\n"
"v_cvt_pk_bf16_f32 v69,v10,v11\n"
"v_mfma_f32_16x16x32_bf16 v[248:251],a[120:123],v[92:95],v[248:251]\n"
"v_exp_f32 v14,v14\n"
"v_cvt_pk_bf16_f32 v70,v12,v13\n"
"v_exp_f32 v15,v15\n"
"v_mfma_f32_16x16x32_bf16 v[252:255],a[124:127],v[92:95],v[252:255]\n"
"buffer_load_dwordx4 v112,s[24:27],s64 offen offset:3072 sc1 lds\n"
"v_mfma_f32_16x16x32_bf16 v[44:47],a[80:83],a[16:19],v[100:103]\n"
"s_add_u32 m0,s67,20480\n"
"v_exp_f32 v16,v16\n"
"v_cvt_pk_bf16_f32 v71,v14,v15\n"
"v_mfma_f32_16x16x32_bf16 v[44:47],a[84:87],a[20:23],v[44:47]\n"
"v_exp_f32 v17,v17\n"
"v_exp_f32 v18,v18\n"
"v_mfma_f32_16x16x32_bf16 v[44:47],a[88:91],a[24:27],v[44:47]\n"
"buffer_load_dwordx4 v117,s[24:27],s64 offen offset:0 sc1 lds\n"
"v_mfma_f32_16x16x32_bf16 v[44:47],a[92:95],a[28:31],v[44:47]\n"
"v_exp_f32 v19,v19\n"
"v_accvgpr_read_b32 v122,a140\n"
"v_mfma_f32_16x16x32_bf16 v[40:43],a[76:79],a[28:31],v[100:103]\n"
"v_cvt_pk_bf16_f32 v73,v18,v19\n"
"v_rcp_f32 v122,v122\n"
"v_mfma_f32_16x16x32_bf16 v[40:43],a[72:75],a[24:27],v[40:43]\n"
"buffer_load_dwordx4 v117,s[24:27],s64 offen offset:1024 sc1 lds\n"
"v_mfma_f32_16x16x32_bf16 v[40:43],a[68:71],a[20:23],v[40:43]\n"
"v_exp_f32 v21,v21\n"
"v_exp_f32 v20,v20\n"
"v_cvt_pk_bf16_f32 v72,v16,v17\n"
"v_pk_mul_f32 v[224:225],v[224:225],v[122:123] op_sel_hi:[1,0]\n"
"v_pk_mul_f32 v[226:227],v[226:227],v[122:123] op_sel_hi:[1,0]\n"
"v_pk_mul_f32 v[228:229],v[228:229],v[122:123] op_sel_hi:[1,0]\n"
"v_pk_mul_f32 v[230:231],v[230:231],v[122:123] op_sel_hi:[1,0]\n"
"v_mfma_f32_16x16x32_bf16 v[40:43],a[64:67],a[16:19],v[40:43]\n"
"v_cvt_pk_bf16_f32 v74,v20,v21\n"
"v_exp_f32 v22,v22\n"
"v_cvt_pk_bf16_f32 v224,v224,v225\n"
"v_exp_f32 v23,v23\n"
"v_mfma_f32_16x16x32_bf16 v[48:51],a[64:67],a[32:35],v[104:107]\n"
"buffer_load_dwordx4 v117,s[24:27],s64 offen offset:2048 sc1 lds\n"
"v_mfma_f32_16x16x32_bf16 v[48:51],a[68:71],a[36:39],v[48:51]\n"
"v_exp_f32 v24,v24\n"
"v_cvt_pk_bf16_f32 v225,v226,v227\n"
"v_mfma_f32_16x16x32_bf16 v[48:51],a[72:75],a[40:43],v[48:51]\n"
"v_exp_f32 v25,v25\n"
"v_cvt_pk_bf16_f32 v226,v228,v229\n"
"v_cvt_pk_bf16_f32 v227,v230,v231\n"
"v_mfma_f32_16x16x32_bf16 v[48:51],a[76:79],a[44:47],v[48:51]\n"
"buffer_load_dwordx4 v117,s[24:27],s64 offen offset:3072 sc1 lds\n"
"v_mfma_f32_16x16x32_bf16 v[52:55],a[92:95],a[44:47],v[104:107]\n"
"buffer_store_dwordx4 v[224:227],v112,s[24:27],s70 offen offset:0 sc1\n"
"v_exp_f32 v27,v27\n"
"v_mfma_f32_16x16x32_bf16 v[52:55],a[88:91],a[40:43],v[52:55]\n"
"s_add_u32 s64,s64,16384\n"
"v_exp_f32 v26,v26\n"
"v_exp_f32 v28,v28\n"
"v_cvt_pk_bf16_f32 v77,v26,v27\n"
"v_pk_mul_f32 v[232:233],v[232:233],v[122:123] op_sel_hi:[1,0]\n"
"v_mfma_f32_16x16x32_bf16 v[52:55],a[84:87],a[36:39],v[52:55]\n"
"v_cvt_pk_bf16_f32 v75,v22,v23\n"
"v_cvt_pk_bf16_f32 v76,v24,v25\n"
"v_exp_f32 v29,v29\n"
"v_exp_f32 v31,v31\n"
"v_pk_mul_f32 v[234:235],v[234:235],v[122:123] op_sel_hi:[1,0]\n"
"v_pk_mul_f32 v[236:237],v[236:237],v[122:123] op_sel_hi:[1,0]\n"
"v_pk_mul_f32 v[238:239],v[238:239],v[122:123] op_sel_hi:[1,0]\n"
"v_mfma_f32_16x16x32_bf16 v[52:55],a[80:83],a[32:35],v[52:55]\n"
"v_exp_f32 v30,v30\n"
"v_cvt_pk_bf16_f32 v78,v28,v29\n"
"v_mfma_f32_16x16x32_bf16 v[60:63],a[80:83],a[48:51],v[108:111]\n"
"v_cvt_pk_bf16_f32 v232,v232,v233\n"
"v_cvt_pk_bf16_f32 v233,v234,v235\n"
"v_mfma_f32_16x16x32_bf16 v[60:63],a[84:87],a[52:55],v[60:63]\n"
"buffer_store_dwordx4 a[244:247],v112,s[24:27],s56 offen offset:1024 sc1\n"
"v_cvt_pk_bf16_f32 v234,v236,v237\n"
"v_cvt_pk_bf16_f32 v235,v238,v239\n"
"v_mfma_f32_16x16x32_bf16 v[60:63],a[88:91],a[56:59],v[60:63]\n"
"buffer_store_dwordx4 v[232:235],v112,s[24:27],s70 offen offset:1024 sc1\n"
"v_pk_mul_f32 v[240:241],v[240:241],v[122:123] op_sel_hi:[1,0]\n"
"v_pk_mul_f32 v[242:243],v[242:243],v[122:123] op_sel_hi:[1,0]\n"
"v_pk_mul_f32 v[244:245],v[244:245],v[122:123] op_sel_hi:[1,0]\n"
"v_pk_mul_f32 v[246:247],v[246:247],v[122:123] op_sel_hi:[1,0]\n"
"v_mfma_f32_16x16x32_bf16 v[60:63],a[92:95],a[60:63],v[60:63]\n"
"v_cvt_pk_bf16_f32 v240,v240,v241\n"
"v_cvt_pk_bf16_f32 v241,v242,v243\n"
"v_cvt_pk_bf16_f32 v242,v244,v245\n"
"v_cvt_pk_bf16_f32 v243,v246,v247\n"
"v_mfma_f32_16x16x32_bf16 v[56:59],a[76:79],a[60:63],v[108:111]\n"
"buffer_store_dwordx4 v[240:243],v112,s[24:27],s70 offen offset:2048 sc1\n"
"v_pk_mul_f32 v[248:249],v[248:249],v[122:123] op_sel_hi:[1,0]\n"
"v_pk_mul_f32 v[250:251],v[250:251],v[122:123] op_sel_hi:[1,0]\n"
"v_mfma_f32_16x16x32_bf16 v[56:59],a[72:75],a[56:59],v[56:59]\n"
"v_cvt_pk_bf16_f32 v79,v30,v31\n"
"v_cvt_pk_bf16_f32 v248,v248,v249\n"
"v_cvt_pk_bf16_f32 v249,v250,v251\n"
"v_pk_mul_f32 v[252:253],v[252:253],v[122:123] op_sel_hi:[1,0]\n"
"v_pk_mul_f32 v[254:255],v[254:255],v[122:123] op_sel_hi:[1,0]\n"
"v_mfma_f32_16x16x32_bf16 v[56:59],a[68:71],a[52:55],v[56:59]\n"
"v_cvt_pk_bf16_f32 v250,v252,v253\n"
"v_cvt_pk_bf16_f32 v251,v254,v255\n"
"v_mfma_f32_16x16x32_bf16 v[56:59],a[64:67],a[48:51],v[56:59]\n"
"buffer_store_dwordx4 v[248:251],v112,s[24:27],s70 offen offset:3072 sc1\n"
"s_add_u32 s70,s70,262144\n"
"ds_read_b128 a[64:67],v113 offset:49152\n"
"ds_read_b128 a[68:71],v113 offset:50176\n"
"ds_read_b128 a[72:75],v113 offset:51200\n"
"ds_read_b128 a[76:79],v113 offset:52224\n"
"ds_read_b128 a[80:83],v113 offset:53248\n"
"ds_read_b128 a[84:87],v113 offset:54272\n"
"ds_read_b128 a[88:91],v113 offset:55296\n"
"ds_read_b128 a[92:95],v113 offset:56320\n"
"v_mov_b32 v114,v112\n"
"s_mov_b32 s70,s68\n"
"s_waitcnt vmcnt(25)\n"
"ds_add_u32 v124,a152\n"
"ds_read_b32 v125,v124\n"
"s_waitcnt lgkmcnt(0)\n"
"v_cmp_gt_u32 vcc,s101,v125\n"
"s_nop 0\n"
"s_cbranch_vccnz .Lfk_cs_mv0\n"
".Lfk_cr_mv0:\n"
"v_mfma_f32_16x16x32_bf16 v[0:3],a[64:67],a[0:3],v[96:99]\n"
"s_add_u32 s101,s101,4\n"
"v_exp_f32 v122,v32\n"
"v_mfma_f32_16x16x32_bf16 v[0:3],a[68:71],a[4:7],v[0:3]\n"
"s_add_u32 m0,s55,8192\n"
"v_exp_f32 v123,v33\n"
"v_mfma_f32_16x16x32_bf16 v[0:3],a[72:75],a[8:11],v[0:3]\n"
"ds_read_b128 a[96:99],v114 offset:40960\n"
"ds_read_b128 a[100:103],v114 offset:41984\n"
"v_mfma_f32_16x16x32_bf16 v[0:3],a[76:79],a[12:15],v[0:3]\n"
"s_add_u32 s54,s54,s22\n"
"v_exp_f32 v119,v34\n"
"v_mfma_f32_16x16x32_bf16 v[4:7],a[92:95],a[12:15],v[96:99]\n"
"ds_read_b128 a[104:107],v114 offset:43008\n"
"v_cvt_pk_bf16_f32 v80,v122,v123\n"
"v_mfma_f32_16x16x32_bf16 v[4:7],a[88:91],a[8:11],v[4:7]\n"
"v_exp_f32 v120,v35\n"
"v_mfma_f32_16x16x32_bf16 v[4:7],a[84:87],a[4:7],v[4:7]\n"
"ds_read_b128 a[108:111],v114 offset:44032\n"
"ds_read_b128 a[112:115],v114 offset:45056\n"
"v_mfma_f32_16x16x32_bf16 v[4:7],a[80:83],a[0:3],v[4:7]\n"
"v_exp_f32 v122,v36\n"
"v_mfma_f32_16x16x32_bf16 v[12:15],a[80:83],a[16:19],v[100:103]\n"
"ds_read_b128 a[116:119],v114 offset:46080\n"
"v_cvt_pk_bf16_f32 v81,v119,v120\n"
"v_mfma_f32_16x16x32_bf16 v[12:15],a[84:87],a[20:23],v[12:15]\n"
"v_exp_f32 v123,v37\n"
"v_mfma_f32_16x16x32_bf16 v[12:15],a[88:91],a[24:27],v[12:15]\n"
"ds_read_b128 a[120:123],v114 offset:47104\n"
"ds_read_b128 a[124:127],v114 offset:48128\n"
"v_mfma_f32_16x16x32_bf16 v[12:15],a[92:95],a[28:31],v[12:15]\n"
"v_exp_f32 v119,v38\n"
"v_mfma_f32_16x16x32_bf16 v[8:11],a[76:79],a[28:31],v[100:103]\n"
"v_cvt_pk_bf16_f32 v82,v122,v123\n"
"v_or3_b32 v118,v80,v81,v82\n"
"v_mfma_f32_16x16x32_bf16 v[8:11],a[72:75],a[24:27],v[8:11]\n"
"v_exp_f32 v120,v39\n"
"v_mfma_f32_16x16x32_bf16 v[8:11],a[68:71],a[20:23],v[8:11]\n"
"buffer_load_dwordx4 v115,s[32:35],s54 offen sc1 lds\n"
"v_mfma_f32_16x16x32_bf16 v[8:11],a[64:67],a[16:19],v[8:11]\n"
"s_add_u32 m0,s67,49152\n"
"v_exp_f32 v122,v40\n"
"v_mfma_f32_16x16x32_bf16 v[16:19],a[64:67],a[32:35],v[104:107]\n"
"v_cvt_pk_bf16_f32 v83,v119,v120\n"
"v_mfma_f32_16x16x32_bf16 v[16:19],a[68:71],a[36:39],v[16:19]\n"
"v_exp_f32 v123,v41\n"
"v_mfma_f32_16x16x32_bf16 v[16:19],a[72:75],a[40:43],v[16:19]\n"
"buffer_load_dwordx4 v112,s[24:27],s64 offen offset:0 sc1 lds\n"
"v_mfma_f32_16x16x32_bf16 v[16:19],a[76:79],a[44:47],v[16:19]\n"
"v_exp_f32 v119,v42\n"
"v_mfma_f32_16x16x32_bf16 v[20:23],a[92:95],a[44:47],v[104:107]\n"
"v_cvt_pk_bf16_f32 v84,v122,v123\n"
"v_or3_b32 v118,v118,v83,v84\n"
"v_mfma_f32_16x16x32_bf16 v[20:23],a[88:91],a[40:43],v[20:23]\n"
"v_exp_f32 v120,v43\n"
"v_mfma_f32_16x16x32_bf16 v[20:23],a[84:87],a[36:39],v[20:23]\n"
"buffer_load_dwordx4 v112,s[24:27],s64 offen offset:1024 sc1 lds\n"
"v_mfma_f32_16x16x32_bf16 v[20:23],a[80:83],a[32:35],v[20:23]\n"
"v_exp_f32 v122,v44\n"
"v_mfma_f32_16x16x32_bf16 v[28:31],a[80:83],a[48:51],v[108:111]\n"
"v_cvt_pk_bf16_f32 v85,v119,v120\n"
"v_mfma_f32_16x16x32_bf16 v[28:31],a[84:87],a[52:55],v[28:31]\n"
"v_exp_f32 v123,v45\n"
"v_mfma_f32_16x16x32_bf16 v[28:31],a[88:91],a[56:59],v[28:31]\n"
"buffer_load_dwordx4 v112,s[24:27],s64 offen offset:2048 sc1 lds\n"
"v_mfma_f32_16x16x32_bf16 v[28:31],a[92:95],a[60:63],v[28:31]\n"
"v_exp_f32 v119,v46\n"
"v_mfma_f32_16x16x32_bf16 v[24:27],a[76:79],a[60:63],v[108:111]\n"
"v_cvt_pk_bf16_f32 v86,v122,v123\n"
"v_or3_b32 v118,v118,v85,v86\n"
"v_mfma_f32_16x16x32_bf16 v[24:27],a[72:75],a[56:59],v[24:27]\n"
"v_exp_f32 v120,v47\n"
"v_mfma_f32_16x16x32_bf16 v[24:27],a[68:71],a[52:55],v[24:27]\n"
"buffer_load_dwordx4 v112,s[24:27],s64 offen offset:3072 sc1 lds\n"
"v_mfma_f32_16x16x32_bf16 v[24:27],a[64:67],a[48:51],v[24:27]\n"
"s_waitcnt lgkmcnt(0)\n"
"v_exp_f32 v122,v48\n"
"v_mfma_f32_16x16x32_bf16 v[128:131],a[96:99],v[64:67],0\n"
"v_exp_f32 v123,v49\n"
"v_mfma_f32_16x16x32_bf16 v[160:163],a[96:99],v[68:71],0\n"
"ds_read_b128 a[64:67],v113 offset:32768\n"
"v_cvt_pk_bf16_f32 v87,v119,v120\n"
"v_mfma_f32_16x16x32_bf16 v[192:195],a[96:99],v[72:75],0\n"
"s_add_u32 m0,s67,53248\n"
"v_exp_f32 v119,v50\n"
"v_mfma_f32_16x16x32_bf16 v[224:227],a[96:99],v[76:79],0\n"
"ds_read_b128 a[68:71],v113 offset:33792\n"
"v_cvt_pk_bf16_f32 v88,v122,v123\n"
"v_mfma_f32_16x16x32_bf16 a[140:143],a[144:147],v[76:79],0\n"
"v_exp_f32 v120,v51\n"
"v_mfma_f32_16x16x32_bf16 v[228:231],a[100:103],v[76:79],0\n"
"ds_read_b128 a[72:75],v113 offset:34816\n"
"ds_read_b128 a[76:79],v113 offset:35840\n"
"v_mfma_f32_16x16x32_bf16 v[132:135],a[100:103],v[64:67],0\n"
"v_exp_f32 v122,v52\n"
"v_mfma_f32_16x16x32_bf16 v[164:167],a[100:103],v[68:71],0\n"
"ds_read_b128 a[80:83],v113 offset:36864\n"
"v_cvt_pk_bf16_f32 v89,v119,v120\n"
"v_mfma_f32_16x16x32_bf16 v[196:199],a[100:103],v[72:75],0\n"
"v_exp_f32 v123,v53\n"
"v_mfma_f32_16x16x32_bf16 a[136:139],a[144:147],v[72:75],0\n"
"ds_read_b128 a[84:87],v113 offset:37888\n"
"ds_read_b128 a[88:91],v113 offset:38912\n"
"v_mfma_f32_16x16x32_bf16 v[200:203],a[104:107],v[72:75],0\n"
"v_exp_f32 v119,v54\n"
"v_mfma_f32_16x16x32_bf16 v[232:235],a[104:107],v[76:79],0\n"
"ds_read_b128 a[92:95],v113 offset:39936\n"
"v_cvt_pk_bf16_f32 v90,v122,v123\n"
"v_mfma_f32_16x16x32_bf16 v[136:139],a[104:107],v[64:67],0\n"
"v_exp_f32 v120,v55\n"
"v_mfma_f32_16x16x32_bf16 v[168:171],a[104:107],v[68:71],0\n"
"s_waitcnt vmcnt(16)\n"
"ds_add_u32 v124,a152\n"
"v_mfma_f32_16x16x32_bf16 a[132:135],a[144:147],v[68:71],0\n"
"v_exp_f32 v122,v56\n"
"v_mfma_f32_16x16x32_bf16 v[172:175],a[108:111],v[68:71],0\n"
"v_cvt_pk_bf16_f32 v91,v119,v120\n"
"v_or3_b32 v118,v118,v87,v88\n"
"v_mfma_f32_16x16x32_bf16 v[204:207],a[108:111],v[72:75],0\n"
"v_exp_f32 v123,v57\n"
"v_mfma_f32_16x16x32_bf16 v[236:239],a[108:111],v[76:79],0\n"
"buffer_load_dwordx4 v117,s[24:27],s64 offen offset:0 sc1 lds\n"
"v_mfma_f32_16x16x32_bf16 v[140:143],a[108:111],v[64:67],0\n"
"v_exp_f32 v119,v58\n"
"v_mfma_f32_16x16x32_bf16 a[128:131],a[144:147],v[64:67],0\n"
"v_cvt_pk_bf16_f32 v92,v122,v123\n"
"v_or3_b32 v118,v118,v89,v90\n"
"v_mfma_f32_16x16x32_bf16 v[144:147],a[112:115],v[64:67],0\n"
"v_exp_f32 v120,v59\n"
"v_mfma_f32_16x16x32_bf16 v[176:179],a[112:115],v[68:71],0\n"
"buffer_load_dwordx4 v117,s[24:27],s64 offen offset:1024 sc1 lds\n"
"v_mfma_f32_16x16x32_bf16 v[208:211],a[112:115],v[72:75],0\n"
"v_exp_f32 v122,v60\n"
"v_mfma_f32_16x16x32_bf16 v[240:243],a[112:115],v[76:79],0\n"
"v_cvt_pk_bf16_f32 v93,v119,v120\n"
"v_or3_b32 v118,v118,v91,v92\n"
"v_mfma_f32_16x16x32_bf16 v[244:247],a[116:119],v[76:79],0\n"
"v_exp_f32 v123,v61\n"
"v_mfma_f32_16x16x32_bf16 v[148:151],a[116:119],v[64:67],0\n"
"buffer_load_dwordx4 v117,s[24:27],s64 offen offset:2048 sc1 lds\n"
"v_mfma_f32_16x16x32_bf16 v[180:183],a[116:119],v[68:71],0\n"
"v_exp_f32 v119,v62\n"
"v_mfma_f32_16x16x32_bf16 v[212:215],a[116:119],v[72:75],0\n"
"ds_read_b32 v125,v124\n"
"v_cvt_pk_bf16_f32 v94,v122,v123\n"
"v_mfma_f32_16x16x32_bf16 v[216:219],a[120:123],v[72:75],0\n"
"v_exp_f32 v120,v63\n"
"v_mfma_f32_16x16x32_bf16 v[248:251],a[120:123],v[76:79],0\n"
"buffer_load_dwordx4 v117,s[24:27],s64 offen offset:3072 sc1 lds\n"
"v_mfma_f32_16x16x32_bf16 v[152:155],a[120:123],v[64:67],0\n"
"buffer_store_dwordx4 a[248:251],v112,s[24:27],s56 offen offset:2048 sc1\n"
"v_mfma_f32_16x16x32_bf16 v[184:187],a[120:123],v[68:71],0\n"
"s_add_u32 s64,s64,16384\n"
"v_cvt_pk_bf16_f32 v95,v119,v120\n"
"v_or3_b32 v118,v118,v93,v94\n"
"v_mfma_f32_16x16x32_bf16 v[188:191],a[124:127],v[68:71],0\n"
"s_waitcnt lgkmcnt(0)\n"
"v_subrev_u32 v125,s101,v125\n"
"v_or3_b32 v125,v118,v95,v125\n"
"v_mfma_f32_16x16x32_bf16 v[220:223],a[124:127],v[72:75],0\n"
"v_and_b32 v125,0x40004000,v125\n"
"v_cmp_ne_u32 vcc,0,v125\n"
"v_mfma_f32_16x16x32_bf16 v[252:255],a[124:127],v[76:79],0\n"
"v_mfma_f32_16x16x32_bf16 v[156:159],a[124:127],v[64:67],0\n"
"s_cbranch_vccnz .Lfk_cs_v1\n"
".Lfk_cr_v1:\n"
"v_mfma_f32_16x16x32_bf16 v[32:35],a[64:67],a[0:3],v[96:99]\n"
"s_add_u32 s101,s101,4\n"
"v_exp_f32 v122,v0\n"
"v_mfma_f32_16x16x32_bf16 v[32:35],a[68:71],a[4:7],v[32:35]\n"
"s_add_u32 m0,s55,12288\n"
"v_exp_f32 v123,v1\n"
"v_mfma_f32_16x16x32_bf16 v[32:35],a[72:75],a[8:11],v[32:35]\n"
"ds_read_b128 a[96:99],v114 offset:8192\n"
"ds_read_b128 a[100:103],v114 offset:9216\n"
"v_mfma_f32_16x16x32_bf16 v[32:35],a[76:79],a[12:15],v[32:35]\n"
"s_add_u32 s54,s54,s22\n"
"v_exp_f32 v119,v2\n"
"v_mfma_f32_16x16x32_bf16 v[36:39],a[92:95],a[12:15],v[96:99]\n"
"ds_read_b128 a[104:107],v114 offset:10240\n"
"v_cvt_pk_bf16_f32 v64,v122,v123\n"
"v_mfma_f32_16x16x32_bf16 v[36:39],a[88:91],a[8:11],v[36:39]\n"
"v_exp_f32 v120,v3\n"
"v_mfma_f32_16x16x32_bf16 v[36:39],a[84:87],a[4:7],v[36:39]\n"
"ds_read_b128 a[108:111],v114 offset:11264\n"
"ds_read_b128 a[112:115],v114 offset:12288\n"
"v_mfma_f32_16x16x32_bf16 v[36:39],a[80:83],a[0:3],v[36:39]\n"
"v_exp_f32 v122,v4\n"
"v_mfma_f32_16x16x32_bf16 v[44:47],a[80:83],a[16:19],v[100:103]\n"
"ds_read_b128 a[116:119],v114 offset:13312\n"
"v_cvt_pk_bf16_f32 v65,v119,v120\n"
"v_mfma_f32_16x16x32_bf16 v[44:47],a[84:87],a[20:23],v[44:47]\n"
"v_exp_f32 v123,v5\n"
"v_mfma_f32_16x16x32_bf16 v[44:47],a[88:91],a[24:27],v[44:47]\n"
"ds_read_b128 a[120:123],v114 offset:14336\n"
"ds_read_b128 a[124:127],v114 offset:15360\n"
"v_mfma_f32_16x16x32_bf16 v[44:47],a[92:95],a[28:31],v[44:47]\n"
"v_exp_f32 v119,v6\n"
"v_mfma_f32_16x16x32_bf16 v[40:43],a[76:79],a[28:31],v[100:103]\n"
"v_cvt_pk_bf16_f32 v66,v122,v123\n"
"v_or3_b32 v118,v64,v65,v66\n"
"v_mfma_f32_16x16x32_bf16 v[40:43],a[72:75],a[24:27],v[40:43]\n"
"v_exp_f32 v120,v7\n"
"v_mfma_f32_16x16x32_bf16 v[40:43],a[68:71],a[20:23],v[40:43]\n"
"buffer_load_dwordx4 v115,s[32:35],s54 offen sc1 lds\n"
"v_mfma_f32_16x16x32_bf16 v[40:43],a[64:67],a[16:19],v[40:43]\n"
"s_add_u32 m0,s67,32768\n"
"v_exp_f32 v122,v8\n"
"v_mfma_f32_16x16x32_bf16 v[48:51],a[64:67],a[32:35],v[104:107]\n"
"v_cvt_pk_bf16_f32 v67,v119,v120\n"
"v_mfma_f32_16x16x32_bf16 v[48:51],a[68:71],a[36:39],v[48:51]\n"
"v_exp_f32 v123,v9\n"
"v_mfma_f32_16x16x32_bf16 v[48:51],a[72:75],a[40:43],v[48:51]\n"
"buffer_load_dwordx4 v112,s[24:27],s64 offen offset:0 sc1 lds\n"
"v_mfma_f32_16x16x32_bf16 v[48:51],a[76:79],a[44:47],v[48:51]\n"
"v_exp_f32 v119,v10\n"
"v_mfma_f32_16x16x32_bf16 v[52:55],a[92:95],a[44:47],v[104:107]\n"
"v_cvt_pk_bf16_f32 v68,v122,v123\n"
"v_or3_b32 v118,v118,v67,v68\n"
"v_mfma_f32_16x16x32_bf16 v[52:55],a[88:91],a[40:43],v[52:55]\n"
"v_exp_f32 v120,v11\n"
"v_mfma_f32_16x16x32_bf16 v[52:55],a[84:87],a[36:39],v[52:55]\n"
"buffer_load_dwordx4 v112,s[24:27],s64 offen offset:1024 sc1 lds\n"
"v_mfma_f32_16x16x32_bf16 v[52:55],a[80:83],a[32:35],v[52:55]\n"
"v_exp_f32 v122,v12\n"
"v_mfma_f32_16x16x32_bf16 v[60:63],a[80:83],a[48:51],v[108:111]\n"
"v_cvt_pk_bf16_f32 v69,v119,v120\n"
"v_mfma_f32_16x16x32_bf16 v[60:63],a[84:87],a[52:55],v[60:63]\n"
"v_exp_f32 v123,v13\n"
"v_mfma_f32_16x16x32_bf16 v[60:63],a[88:91],a[56:59],v[60:63]\n"
"buffer_load_dwordx4 v112,s[24:27],s64 offen offset:2048 sc1 lds\n"
"v_mfma_f32_16x16x32_bf16 v[60:63],a[92:95],a[60:63],v[60:63]\n"
"v_exp_f32 v119,v14\n"
"v_mfma_f32_16x16x32_bf16 v[56:59],a[76:79],a[60:63],v[108:111]\n"
"v_cvt_pk_bf16_f32 v70,v122,v123\n"
"v_or3_b32 v118,v118,v69,v70\n"
"v_mfma_f32_16x16x32_bf16 v[56:59],a[72:75],a[56:59],v[56:59]\n"
"v_exp_f32 v120,v15\n"
"v_mfma_f32_16x16x32_bf16 v[56:59],a[68:71],a[52:55],v[56:59]\n"
"buffer_load_dwordx4 v112,s[24:27],s64 offen offset:3072 sc1 lds\n"
"v_mfma_f32_16x16x32_bf16 v[56:59],a[64:67],a[48:51],v[56:59]\n"
"s_waitcnt lgkmcnt(0)\n"
"v_exp_f32 v122,v16\n"
"v_mfma_f32_16x16x32_bf16 v[128:131],a[96:99],v[80:83],v[128:131]\n"
"v_exp_f32 v123,v17\n"
"v_mfma_f32_16x16x32_bf16 v[160:163],a[96:99],v[84:87],v[160:163]\n"
"ds_read_b128 a[64:67],v113 offset:0\n"
"v_cvt_pk_bf16_f32 v71,v119,v120\n"
"v_mfma_f32_16x16x32_bf16 v[192:195],a[96:99],v[88:91],v[192:195]\n"
"s_add_u32 m0,s67,36864\n"
"v_exp_f32 v119,v18\n"
"v_mfma_f32_16x16x32_bf16 v[224:227],a[96:99],v[92:95],v[224:227]\n"
"ds_read_b128 a[68:71],v113 offset:1024\n"
"v_cvt_pk_bf16_f32 v72,v122,v123\n"
"v_mfma_f32_16x16x32_bf16 a[140:143],a[144:147],v[92:95],a[140:143]\n"
"v_exp_f32 v120,v19\n"
"v_mfma_f32_16x16x32_bf16 v[228:231],a[100:103],v[92:95],v[228:231]\n"
"ds_read_b128 a[72:75],v113 offset:2048\n"
"ds_read_b128 a[76:79],v113 offset:3072\n"
"v_mfma_f32_16x16x32_bf16 v[132:135],a[100:103],v[80:83],v[132:135]\n"
"v_exp_f32 v122,v20\n"
"v_mfma_f32_16x16x32_bf16 v[164:167],a[100:103],v[84:87],v[164:167]\n"
"ds_read_b128 a[80:83],v113 offset:4096\n"
"v_cvt_pk_bf16_f32 v73,v119,v120\n"
"v_mfma_f32_16x16x32_bf16 v[196:199],a[100:103],v[88:91],v[196:199]\n"
"v_exp_f32 v123,v21\n"
"v_mfma_f32_16x16x32_bf16 a[136:139],a[144:147],v[88:91],a[136:139]\n"
"ds_read_b128 a[84:87],v113 offset:5120\n"
"ds_read_b128 a[88:91],v113 offset:6144\n"
"v_mfma_f32_16x16x32_bf16 v[200:203],a[104:107],v[88:91],v[200:203]\n"
"v_exp_f32 v119,v22\n"
"v_mfma_f32_16x16x32_bf16 v[232:235],a[104:107],v[92:95],v[232:235]\n"
"ds_read_b128 a[92:95],v113 offset:7168\n"
"v_cvt_pk_bf16_f32 v74,v122,v123\n"
"v_mfma_f32_16x16x32_bf16 v[136:139],a[104:107],v[80:83],v[136:139]\n"
"v_exp_f32 v120,v23\n"
"v_mfma_f32_16x16x32_bf16 v[168:171],a[104:107],v[84:87],v[168:171]\n"
"s_waitcnt vmcnt(16)\n"
"ds_add_u32 v124,a152\n"
"v_mfma_f32_16x16x32_bf16 a[132:135],a[144:147],v[84:87],a[132:135]\n"
"v_exp_f32 v122,v24\n"
"v_mfma_f32_16x16x32_bf16 v[172:175],a[108:111],v[84:87],v[172:175]\n"
"v_cvt_pk_bf16_f32 v75,v119,v120\n"
"v_or3_b32 v118,v118,v71,v72\n"
"v_mfma_f32_16x16x32_bf16 v[204:207],a[108:111],v[88:91],v[204:207]\n"
"v_exp_f32 v123,v25\n"
"v_mfma_f32_16x16x32_bf16 v[236:239],a[108:111],v[92:95],v[236:239]\n"
"buffer_load_dwordx4 v117,s[24:27],s64 offen offset:0 sc1 lds\n"
"v_mfma_f32_16x16x32_bf16 v[140:143],a[108:111],v[80:83],v[140:143]\n"
"v_exp_f32 v119,v26\n"
"v_mfma_f32_16x16x32_bf16 a[128:131],a[144:147],v[80:83],a[128:131]\n"
"v_cvt_pk_bf16_f32 v76,v122,v123\n"
"v_or3_b32 v118,v118,v73,v74\n"
"v_mfma_f32_16x16x32_bf16 v[144:147],a[112:115],v[80:83],v[144:147]\n"
"v_exp_f32 v120,v27\n"
"v_mfma_f32_16x16x32_bf16 v[176:179],a[112:115],v[84:87],v[176:179]\n"
"buffer_load_dwordx4 v117,s[24:27],s64 offen offset:1024 sc1 lds\n"
"v_mfma_f32_16x16x32_bf16 v[208:211],a[112:115],v[88:91],v[208:211]\n"
"v_exp_f32 v122,v28\n"
"v_mfma_f32_16x16x32_bf16 v[240:243],a[112:115],v[92:95],v[240:243]\n"
"v_cvt_pk_bf16_f32 v77,v119,v120\n"
"v_or3_b32 v118,v118,v75,v76\n"
"v_mfma_f32_16x16x32_bf16 v[244:247],a[116:119],v[92:95],v[244:247]\n"
"v_exp_f32 v123,v29\n"
"v_mfma_f32_16x16x32_bf16 v[148:151],a[116:119],v[80:83],v[148:151]\n"
"buffer_load_dwordx4 v117,s[24:27],s64 offen offset:2048 sc1 lds\n"
"v_mfma_f32_16x16x32_bf16 v[180:183],a[116:119],v[84:87],v[180:183]\n"
"v_exp_f32 v119,v30\n"
"v_mfma_f32_16x16x32_bf16 v[212:215],a[116:119],v[88:91],v[212:215]\n"
"ds_read_b32 v125,v124\n"
"v_cvt_pk_bf16_f32 v78,v122,v123\n"
"v_mfma_f32_16x16x32_bf16 v[216:219],a[120:123],v[88:91],v[216:219]\n"
"v_exp_f32 v120,v31\n"
"v_mfma_f32_16x16x32_bf16 v[248:251],a[120:123],v[92:95],v[248:251]\n"
"buffer_load_dwordx4 v117,s[24:27],s64 offen offset:3072 sc1 lds\n"
"v_mfma_f32_16x16x32_bf16 v[152:155],a[120:123],v[80:83],v[152:155]\n"
"buffer_store_dwordx4 a[252:255],v112,s[24:27],s56 offen offset:3072 sc1\n"
"v_mfma_f32_16x16x32_bf16 v[184:187],a[120:123],v[84:87],v[184:187]\n"
"s_add_u32 s64,s64,16384\n"
"s_add_u32 s89,s89,1\n"
"v_cvt_pk_bf16_f32 v79,v119,v120\n"
"v_mfma_f32_16x16x32_bf16 v[188:191],a[124:127],v[84:87],v[188:191]\n"
"s_waitcnt lgkmcnt(0)\n"
"v_subrev_u32 v125,s101,v125\n"
"v_or3_b32 v118,v118,v77,v78\n"
"v_mfma_f32_16x16x32_bf16 v[220:223],a[124:127],v[88:91],v[220:223]\n"
"v_or3_b32 v125,v118,v79,v125\n"
"v_and_b32 v125,0x40004000,v125\n"
"v_mfma_f32_16x16x32_bf16 v[252:255],a[124:127],v[92:95],v[252:255]\n"
"v_cmp_ne_u32 vcc,0,v125\n"
"v_mfma_f32_16x16x32_bf16 v[156:159],a[124:127],v[80:83],v[156:159]\n"
"s_cbranch_vccnz .Lfk_cs_v2\n"
".Lfk_cr_v2:\n"
"s_waitcnt vmcnt(20)\n"
"buffer_store_dwordx2 a[154:155],off,s[24:27],s69 sc1\n"
"s_branch .Lfk_Hx\n"
".Lfk_tailB:\n"
"s_waitcnt vmcnt(11) lgkmcnt(0)\n"
"s_barrier\n"
"v_mfma_f32_16x16x32_bf16 v[0:3],a[64:67],a[0:3],v[96:99]\n"
"ds_read_b128 a[96:99],v114 offset:24576\n"
"ds_read_b128 a[100:103],v114 offset:25600\n"
"ds_read_b128 a[104:107],v114 offset:26624\n"
"v_mfma_f32_16x16x32_bf16 v[0:3],a[68:71],a[4:7],v[0:3]\n"
"ds_read_b128 a[108:111],v114 offset:27648\n"
"ds_read_b128 a[112:115],v114 offset:28672\n"
"ds_read_b128 a[116:119],v114 offset:29696\n"
"v_mfma_f32_16x16x32_bf16 v[0:3],a[72:75],a[8:11],v[0:3]\n"
"ds_read_b128 a[120:123],v114 offset:30720\n"
"ds_read_b128 a[124:127],v114 offset:31744\n"
"v_add_f32 v16,0x80000000,v126\n"
"v_mfma_f32_16x16x32_bf16 v[0:3],a[76:79],a[12:15],v[0:3]\n"
"v_min_f32 v16,v16,v104\n"
"v_add_f32 v17,0xf149f2ca,v126\n"
"v_min_f32 v17,v17,v105\n"
"v_mfma_f32_16x16x32_bf16 v[4:7],a[92:95],a[12:15],v[96:99]\n"
"v_add_f32 v18,0xf1c9f2ca,v126\n"
"v_min_f32 v18,v18,v106\n"
"v_add_f32 v19,0xf2177617,v126\n"
"v_mfma_f32_16x16x32_bf16 v[4:7],a[88:91],a[8:11],v[4:7]\n"
"v_min_f32 v19,v19,v107\n"
"v_add_f32 v20,0xf349f2ca,v126\n"
"v_min_f32 v20,v20,v104\n"
"v_mfma_f32_16x16x32_bf16 v[4:7],a[84:87],a[4:7],v[4:7]\n"
"v_add_f32 v21,0xf35691f6,v126\n"
"v_min_f32 v21,v21,v105\n"
"v_add_f32 v22,0xf3633123,v126\n"
"v_mfma_f32_16x16x32_bf16 v[4:7],a[80:83],a[0:3],v[4:7]\n"
"v_min_f32 v22,v22,v106\n"
"v_add_f32 v23,0xf36fd050,v126\n"
"v_min_f32 v23,v23,v107\n"
"v_mfma_f32_16x16x32_bf16 v[12:15],a[80:83],a[16:19],v[100:103]\n"
"v_exp_f32 v32,v32\n"
"v_max3_f32 v118,v0,v1,v2\n"
"v_mfma_f32_16x16x32_bf16 v[12:15],a[84:87],a[20:23],v[12:15]\n"
"v_exp_f32 v33,v33\n"
"v_mfma_f32_16x16x32_bf16 v[12:15],a[88:91],a[24:27],v[12:15]\n"
"v_exp_f32 v34,v34\n"
"v_cvt_pk_bf16_f32 v80,v32,v33\n"
"v_mfma_f32_16x16x32_bf16 v[12:15],a[92:95],a[28:31],v[12:15]\n"
"v_exp_f32 v35,v35\n"
"v_max3_f32 v118,v118,v3,v4\n"
"v_mfma_f32_16x16x32_bf16 v[8:11],a[76:79],a[28:31],v[100:103]\n"
"v_exp_f32 v36,v36\n"
"v_cvt_pk_bf16_f32 v81,v34,v35\n"
"v_mfma_f32_16x16x32_bf16 v[8:11],a[72:75],a[24:27],v[8:11]\n"
"v_exp_f32 v37,v37\n"
"v_max3_f32 v118,v118,v5,v6\n"
"v_mfma_f32_16x16x32_bf16 v[8:11],a[68:71],a[20:23],v[8:11]\n"
"v_exp_f32 v38,v38\n"
"v_mfma_f32_16x16x32_bf16 v[8:11],a[64:67],a[16:19],v[8:11]\n"
"v_exp_f32 v39,v39\n"
"v_max3_f32 v118,v118,v7,v12\n"
"v_mfma_f32_16x16x32_bf16 v[24:27],a[64:67],a[48:51],v[108:111]\n"
"v_exp_f32 v40,v40\n"
"v_mfma_f32_16x16x32_bf16 v[24:27],a[68:71],a[52:55],v[24:27]\n"
"v_exp_f32 v41,v41\n"
"v_max3_f32 v118,v118,v13,v14\n"
"v_mfma_f32_16x16x32_bf16 v[24:27],a[72:75],a[56:59],v[24:27]\n"
"v_cvt_pk_bf16_f32 v82,v36,v37\n"
"v_cvt_pk_bf16_f32 v84,v40,v41\n"
"v_mfma_f32_16x16x32_bf16 v[24:27],a[76:79],a[60:63],v[24:27]\n"
"v_exp_f32 v42,v42\n"
"v_max3_f32 v118,v118,v15,v8\n"
"v_mfma_f32_16x16x32_bf16 v[28:31],a[92:95],a[60:63],v[108:111]\n"
"v_cvt_pk_bf16_f32 v83,v38,v39\n"
"v_max3_f32 v118,v118,v9,v10\n"
"v_mfma_f32_16x16x32_bf16 v[28:31],a[88:91],a[56:59],v[28:31]\n"
"v_exp_f32 v43,v43\n"
"v_mfma_f32_16x16x32_bf16 v[28:31],a[84:87],a[52:55],v[28:31]\n"
"v_cvt_pk_bf16_f32 v85,v42,v43\n"
"v_mfma_f32_16x16x32_bf16 v[28:31],a[80:83],a[48:51],v[28:31]\n"
"v_exp_f32 v44,v44\n"
"v_mfma_f32_16x16x32_bf16 v[20:23],a[80:83],a[32:35],v[20:23]\n"
"v_max3_f32 v118,v118,v11,v24\n"
"v_max3_f32 v118,v118,v25,v26\n"
"v_mfma_f32_16x16x32_bf16 v[20:23],a[84:87],a[36:39],v[20:23]\n"
"v_exp_f32 v45,v45\n"
"v_mfma_f32_16x16x32_bf16 v[20:23],a[88:91],a[40:43],v[20:23]\n"
"v_cvt_pk_bf16_f32 v86,v44,v45\n"
"v_mfma_f32_16x16x32_bf16 v[20:23],a[92:95],a[44:47],v[20:23]\n"
"v_exp_f32 v46,v46\n"
"v_mfma_f32_16x16x32_bf16 v[16:19],a[76:79],a[44:47],v[16:19]\n"
"v_max3_f32 v118,v118,v27,v28\n"
"v_max3_f32 v118,v118,v29,v30\n"
"v_mfma_f32_16x16x32_bf16 v[16:19],a[72:75],a[40:43],v[16:19]\n"
"v_exp_f32 v47,v47\n"
"v_mfma_f32_16x16x32_bf16 v[16:19],a[68:71],a[36:39],v[16:19]\n"
"v_cvt_pk_bf16_f32 v87,v46,v47\n"
"v_exp_f32 v48,v48\n"
"v_mfma_f32_16x16x32_bf16 v[16:19],a[64:67],a[32:35],v[16:19]\n"
"v_max3_f32 v118,v118,v31,v20\n"
"s_waitcnt lgkmcnt(0)\n"
"v_mfma_f32_16x16x32_bf16 v[128:131],a[96:99],v[64:67],v[128:131]\n"
"v_exp_f32 v49,v49\n"
"v_max3_f32 v118,v118,v21,v22\n"
"v_cvt_pk_bf16_f32 v88,v48,v49\n"
"v_mfma_f32_16x16x32_bf16 v[160:163],a[96:99],v[68:71],v[160:163]\n"
"ds_read_b128 a[64:67],v114 offset:57344\n"
"ds_read_b128 a[68:71],v114 offset:58368\n"
"v_mfma_f32_16x16x32_bf16 v[192:195],a[96:99],v[72:75],v[192:195]\n"
"ds_read_b128 a[72:75],v114 offset:59392\n"
"v_exp_f32 v50,v50\n"
"v_mfma_f32_16x16x32_bf16 v[224:227],a[96:99],v[76:79],v[224:227]\n"
"v_max3_f32 v118,v118,v23,v16\n"
"v_max3_f32 v118,v118,v17,v18\n"
"v_max_f32 v118,v118,v19\n"
"v_mfma_f32_16x16x32_bf16 a[140:143],a[144:147],v[76:79],a[140:143]\n"
"v_exp_f32 v51,v51\n"
"v_cmp_lt_f32 vcc,s93,v118\n"
"v_cvt_pk_bf16_f32 v89,v50,v51\n"
"v_mfma_f32_16x16x32_bf16 v[228:231],a[100:103],v[76:79],v[228:231]\n"
"ds_read_b128 a[76:79],v114 offset:60416\n"
"ds_read_b128 a[80:83],v114 offset:61440\n"
"v_mfma_f32_16x16x32_bf16 v[132:135],a[100:103],v[64:67],v[132:135]\n"
"ds_read_b128 a[84:87],v114 offset:62464\n"
"v_exp_f32 v52,v52\n"
"v_mfma_f32_16x16x32_bf16 v[164:167],a[100:103],v[68:71],v[164:167]\n"
"s_sub_u32 s82,s3,32768\n"
"ds_read_b128 a[88:91],v114 offset:63488\n"
"ds_read_b128 a[92:95],v114 offset:64512\n"
"v_mfma_f32_16x16x32_bf16 v[196:199],a[100:103],v[72:75],v[196:199]\n"
"buffer_load_dwordx4 a[0:3],v112,s[24:27],s82 offen offset:0 sc1\n"
"v_exp_f32 v53,v53\n"
"v_mfma_f32_16x16x32_bf16 a[136:139],a[144:147],v[72:75],a[136:139]\n"
"buffer_load_dwordx4 a[4:7],v112,s[24:27],s82 offen offset:1024 sc1\n"
"buffer_load_dwordx4 a[8:11],v112,s[24:27],s82 offen offset:2048 sc1\n"
"v_cvt_pk_bf16_f32 v90,v52,v53\n"
"v_exp_f32 v54,v54\n"
"v_mfma_f32_16x16x32_bf16 v[200:203],a[104:107],v[72:75],v[200:203]\n"
"buffer_load_dwordx4 a[12:15],v112,s[24:27],s82 offen offset:3072 sc1\n"
"v_mfma_f32_16x16x32_bf16 v[232:235],a[104:107],v[76:79],v[232:235]\n"
"s_sub_u32 s82,s3,49152\n"
"buffer_load_dwordx4 a[16:19],v112,s[24:27],s82 offen offset:0 sc1\n"
"buffer_load_dwordx4 a[20:23],v112,s[24:27],s82 offen offset:1024 sc1\n"
"v_exp_f32 v55,v55\n"
"v_mfma_f32_16x16x32_bf16 v[136:139],a[104:107],v[64:67],v[136:139]\n"
"buffer_load_dwordx4 a[24:27],v112,s[24:27],s82 offen offset:2048 sc1\n"
"v_mfma_f32_16x16x32_bf16 v[168:171],a[104:107],v[68:71],v[168:171]\n"
"buffer_load_dwordx4 a[28:31],v112,s[24:27],s82 offen offset:3072 sc1\n"
"v_mfma_f32_16x16x32_bf16 a[132:135],a[144:147],v[68:71],a[132:135]\n"
"v_exp_f32 v56,v56\n"
"v_mfma_f32_16x16x32_bf16 v[172:175],a[108:111],v[68:71],v[172:175]\n"
"v_cvt_pk_bf16_f32 v91,v54,v55\n"
"v_mfma_f32_16x16x32_bf16 v[204:207],a[108:111],v[72:75],v[204:207]\n"
"v_exp_f32 v57,v57\n"
"v_mfma_f32_16x16x32_bf16 v[236:239],a[108:111],v[76:79],v[236:239]\n"
"v_cvt_pk_bf16_f32 v92,v56,v57\n"
"v_mfma_f32_16x16x32_bf16 v[140:143],a[108:111],v[64:67],v[140:143]\n"
"v_exp_f32 v58,v58\n"
"v_mfma_f32_16x16x32_bf16 a[128:131],a[144:147],v[64:67],a[128:131]\n"
"v_mfma_f32_16x16x32_bf16 v[144:147],a[112:115],v[64:67],v[144:147]\n"
"v_exp_f32 v59,v59\n"
"v_mfma_f32_16x16x32_bf16 v[176:179],a[112:115],v[68:71],v[176:179]\n"
"v_cvt_pk_bf16_f32 v93,v58,v59\n"
"v_mfma_f32_16x16x32_bf16 v[208:211],a[112:115],v[72:75],v[208:211]\n"
"v_exp_f32 v60,v60\n"
"v_mfma_f32_16x16x32_bf16 v[240:243],a[112:115],v[76:79],v[240:243]\n"
"v_mfma_f32_16x16x32_bf16 v[244:247],a[116:119],v[76:79],v[244:247]\n"
"v_exp_f32 v61,v61\n"
"v_mfma_f32_16x16x32_bf16 v[148:151],a[116:119],v[64:67],v[148:151]\n"
"v_cvt_pk_bf16_f32 v94,v60,v61\n"
"v_mfma_f32_16x16x32_bf16 v[180:183],a[116:119],v[68:71],v[180:183]\n"
"v_exp_f32 v62,v62\n"
"v_mfma_f32_16x16x32_bf16 v[212:215],a[116:119],v[72:75],v[212:215]\n"
"v_mfma_f32_16x16x32_bf16 v[216:219],a[120:123],v[72:75],v[216:219]\n"
"v_exp_f32 v63,v63\n"
"v_mfma_f32_16x16x32_bf16 v[248:251],a[120:123],v[76:79],v[248:251]\n"
"v_cvt_pk_bf16_f32 v95,v62,v63\n"
"v_mfma_f32_16x16x32_bf16 v[152:155],a[120:123],v[64:67],v[152:155]\n"
"v_mfma_f32_16x16x32_bf16 v[184:187],a[120:123],v[68:71],v[184:187]\n"
"v_mfma_f32_16x16x32_bf16 v[188:191],a[124:127],v[68:71],v[188:191]\n"
"v_mfma_f32_16x16x32_bf16 v[220:223],a[124:127],v[72:75],v[220:223]\n"
"v_mfma_f32_16x16x32_bf16 v[252:255],a[124:127],v[76:79],v[252:255]\n"
"v_mfma_f32_16x16x32_bf16 v[156:159],a[124:127],v[64:67],v[156:159]\n"
"s_cbranch_vccnz .Lfk_cs_b3\n"
".Lfk_cr_b3:\n"
"s_waitcnt vmcnt(9) lgkmcnt(0)\n"
"s_barrier\n"
"v_mfma_f32_16x16x32_bf16 v[128:131],a[64:67],v[80:83],v[128:131]\n"
"v_exp_f32 v0,v0\n"
"v_mfma_f32_16x16x32_bf16 v[160:163],a[64:67],v[84:87],v[160:163]\n"
"v_exp_f32 v1,v1\n"
"v_mfma_f32_16x16x32_bf16 v[192:195],a[64:67],v[88:91],v[192:195]\n"
"v_exp_f32 v2,v2\n"
"v_cvt_pk_bf16_f32 v64,v0,v1\n"
"v_mfma_f32_16x16x32_bf16 v[224:227],a[64:67],v[92:95],v[224:227]\n"
"v_exp_f32 v3,v3\n"
"v_mfma_f32_16x16x32_bf16 a[140:143],a[144:147],v[92:95],a[140:143]\n"
"v_exp_f32 v4,v4\n"
"v_cvt_pk_bf16_f32 v65,v2,v3\n"
"v_mfma_f32_16x16x32_bf16 v[228:231],a[68:71],v[92:95],v[228:231]\n"
"v_exp_f32 v5,v5\n"
"v_mfma_f32_16x16x32_bf16 v[132:135],a[68:71],v[80:83],v[132:135]\n"
"v_exp_f32 v6,v6\n"
"v_cvt_pk_bf16_f32 v66,v4,v5\n"
"v_exp_f32 v7,v7\n"
"v_mfma_f32_16x16x32_bf16 v[164:167],a[68:71],v[84:87],v[164:167]\n"
"ds_read_b128 a[96:99],v114 offset:40960\n"
"ds_read_b128 a[100:103],v114 offset:41984\n"
"v_exp_f32 v8,v8\n"
"v_mfma_f32_16x16x32_bf16 v[196:199],a[68:71],v[88:91],v[196:199]\n"
"ds_read_b128 a[104:107],v114 offset:43008\n"
"ds_read_b128 a[108:111],v114 offset:44032\n"
"v_exp_f32 v9,v9\n"
"v_mfma_f32_16x16x32_bf16 a[136:139],a[144:147],v[88:91],a[136:139]\n"
"ds_read_b128 a[112:115],v114 offset:45056\n"
"ds_read_b128 a[116:119],v114 offset:46080\n"
"v_exp_f32 v10,v10\n"
"v_mfma_f32_16x16x32_bf16 v[200:203],a[72:75],v[88:91],v[200:203]\n"
"ds_read_b128 a[120:123],v114 offset:47104\n"
"ds_read_b128 a[124:127],v114 offset:48128\n"
"v_mfma_f32_16x16x32_bf16 v[232:235],a[72:75],v[92:95],v[232:235]\n"
"v_exp_f32 v11,v11\n"
"v_accvgpr_read_b32 v119,a150\n"
"v_accvgpr_read_b32 v120,a151\n"
"v_mfma_f32_16x16x32_bf16 v[136:139],a[72:75],v[80:83],v[136:139]\n"
"v_exp_f32 v12,v12\n"
"v_xor_b32 v119,s80,v119\n"
"v_xor_b32 v120,s81,v120\n"
"v_mfma_f32_16x16x32_bf16 v[168:171],a[72:75],v[84:87],v[168:171]\n"
"v_exp_f32 v13,v13\n"
"v_or_b32 v119,v119,v120\n"
"v_cmp_ne_u32 s[14:15],0,v119\n"
"v_mfma_f32_16x16x32_bf16 a[132:135],a[144:147],v[84:87],a[132:135]\n"
"s_add_u32 s18,s8,1032192\n"
"v_exp_f32 v14,v14\n"
"s_cmp_lg_u32 s14,0\n"
"s_cselect_b32 s18,2146435072,s18\n"
"v_mfma_f32_16x16x32_bf16 v[172:175],a[76:79],v[84:87],v[172:175]\n"
"s_add_u32 m0,s30,0\n"
"v_exp_f32 v15,v15\n"
"buffer_load_dwordx4 v112,s[24:27],s18 offen offset:0 sc1 lds\n"
"buffer_load_dwordx4 v112,s[24:27],s18 offen offset:1024 sc1 lds\n"
"v_exp_f32 v16,v16\n"
"v_mfma_f32_16x16x32_bf16 v[204:207],a[76:79],v[88:91],v[204:207]\n"
"buffer_load_dwordx4 v112,s[24:27],s18 offen offset:2048 sc1 lds\n"
"buffer_load_dwordx4 v112,s[24:27],s18 offen offset:3072 sc1 lds\n"
"v_mfma_f32_16x16x32_bf16 v[236:239],a[76:79],v[92:95],v[236:239]\n"
"s_add_u32 m0,m0,4096\n"
"v_exp_f32 v17,v17\n"
"v_cvt_pk_bf16_f32 v67,v6,v7\n"
"v_mfma_f32_16x16x32_bf16 v[140:143],a[76:79],v[80:83],v[140:143]\n"
"buffer_load_dwordx4 v117,s[24:27],s18 offen offset:0 sc1 lds\n"
"v_exp_f32 v18,v18\n"
"v_cvt_pk_bf16_f32 v68,v8,v9\n"
"v_cvt_pk_bf16_f32 v69,v10,v11\n"
"v_exp_f32 v19,v19\n"
"v_mfma_f32_16x16x32_bf16 a[128:131],a[144:147],v[80:83],a[128:131]\n"
"buffer_load_dwordx4 v117,s[24:27],s18 offen offset:1024 sc1 lds\n"
"v_cvt_pk_bf16_f32 v70,v12,v13\n"
"v_mfma_f32_16x16x32_bf16 v[144:147],a[80:83],v[80:83],v[144:147]\n"
"v_exp_f32 v20,v20\n"
"v_cvt_pk_bf16_f32 v71,v14,v15\n"
"v_cvt_pk_bf16_f32 v72,v16,v17\n"
"v_exp_f32 v21,v21\n"
"v_mfma_f32_16x16x32_bf16 v[176:179],a[80:83],v[84:87],v[176:179]\n"
"buffer_load_dwordx4 v117,s[24:27],s18 offen offset:2048 sc1 lds\n"
"v_cvt_pk_bf16_f32 v73,v18,v19\n"
"v_mfma_f32_16x16x32_bf16 v[208:211],a[80:83],v[88:91],v[208:211]\n"
"v_exp_f32 v22,v22\n"
"v_cvt_pk_bf16_f32 v74,v20,v21\n"
"v_exp_f32 v23,v23\n"
"v_mfma_f32_16x16x32_bf16 v[240:243],a[80:83],v[92:95],v[240:243]\n"
"buffer_load_dwordx4 v117,s[24:27],s18 offen offset:3072 sc1 lds\n"
"v_mfma_f32_16x16x32_bf16 v[244:247],a[84:87],v[92:95],v[244:247]\n"
"s_sub_u32 s82,s3,65536\n"
"v_exp_f32 v24,v24\n"
"v_cvt_pk_bf16_f32 v75,v22,v23\n"
"v_exp_f32 v25,v25\n"
"v_mfma_f32_16x16x32_bf16 v[148:151],a[84:87],v[80:83],v[148:151]\n"
"buffer_load_dwordx4 v[32:35],v112,s[24:27],s82 offen offset:0 sc1\n"
"v_mfma_f32_16x16x32_bf16 v[180:183],a[84:87],v[84:87],v[180:183]\n"
"v_exp_f32 v26,v26\n"
"v_cvt_pk_bf16_f32 v76,v24,v25\n"
"v_exp_f32 v27,v27\n"
"v_mfma_f32_16x16x32_bf16 v[212:215],a[84:87],v[88:91],v[212:215]\n"
"buffer_load_dwordx4 v[36:39],v112,s[24:27],s82 offen offset:1024 sc1\n"
"v_mfma_f32_16x16x32_bf16 v[216:219],a[88:91],v[88:91],v[216:219]\n"
"v_exp_f32 v28,v28\n"
"v_exp_f32 v29,v29\n"
"v_mfma_f32_16x16x32_bf16 v[248:251],a[88:91],v[92:95],v[248:251]\n"
"buffer_load_dwordx4 v[40:43],v112,s[24:27],s82 offen offset:2048 sc1\n"
"v_mfma_f32_16x16x32_bf16 v[152:155],a[88:91],v[80:83],v[152:155]\n"
"v_exp_f32 v30,v30\n"
"v_mfma_f32_16x16x32_bf16 v[184:187],a[88:91],v[84:87],v[184:187]\n"
"v_exp_f32 v31,v31\n"
"v_mfma_f32_16x16x32_bf16 v[188:191],a[92:95],v[84:87],v[188:191]\n"
"buffer_load_dwordx4 v[44:47],v112,s[24:27],s82 offen offset:3072 sc1\n"
"v_mfma_f32_16x16x32_bf16 v[220:223],a[92:95],v[88:91],v[220:223]\n"
"v_cvt_pk_bf16_f32 v78,v28,v29\n"
"v_cvt_pk_bf16_f32 v77,v26,v27\n"
"v_mfma_f32_16x16x32_bf16 v[252:255],a[92:95],v[92:95],v[252:255]\n"
"v_cvt_pk_bf16_f32 v79,v30,v31\n"
"v_mfma_f32_16x16x32_bf16 v[156:159],a[92:95],v[80:83],v[156:159]\n"
"s_load_dwordx2 s[36:37],s[0:1],0\n"
"s_load_dwordx2 s[44:45],s[0:1],80\n"
"s_load_dwordx2 s[48:49],s[0:1],88\n"
"s_lshl_b32 s82,s78,2\n"
"s_add_u32 s82,s82,65536\n"
"v_add_u32 v113,s82,v112\n"
"s_lshr_b32 s82,s78,2\n"
"s_add_u32 s82,s82,122880\n"
"v_lshrrev_b32 v115,1,v112\n"
"v_add_u32 v116,s82,v115\n"
"s_waitcnt lgkmcnt(0)\n"
"s_barrier\n"
"v_mfma_f32_16x16x32_bf16 v[128:131],a[96:99],v[64:67],v[128:131]\n"
"s_add_u32 s83,s18,2097152\n"
"s_add_u32 m0,s30,32768\n"
"v_mfma_f32_16x16x32_bf16 a[128:131],a[144:147],v[64:67],a[128:131]\n"
"buffer_load_dwordx4 v112,s[24:27],s83 offen offset:0 sc1 lds\n"
"v_mfma_f32_16x16x32_bf16 v[132:135],a[100:103],v[64:67],v[132:135]\n"
"buffer_load_dwordx4 v112,s[24:27],s83 offen offset:1024 sc1 lds\n"
"v_mfma_f32_16x16x32_bf16 v[136:139],a[104:107],v[64:67],v[136:139]\n"
"buffer_load_dwordx4 v112,s[24:27],s83 offen offset:2048 sc1 lds\n"
"v_mfma_f32_16x16x32_bf16 v[140:143],a[108:111],v[64:67],v[140:143]\n"
"v_mfma_f32_16x16x32_bf16 v[144:147],a[112:115],v[64:67],v[144:147]\n"
"buffer_load_dwordx4 v112,s[24:27],s83 offen offset:3072 sc1 lds\n"
"v_mfma_f32_16x16x32_bf16 v[148:151],a[116:119],v[64:67],v[148:151]\n"
"s_add_u32 m0,m0,4096\n"
"v_mfma_f32_16x16x32_bf16 v[152:155],a[120:123],v[64:67],v[152:155]\n"
"v_mfma_f32_16x16x32_bf16 v[156:159],a[124:127],v[64:67],v[156:159]\n"
"buffer_load_dwordx4 v117,s[24:27],s83 offen offset:0 sc1 lds\n"
"v_mfma_f32_16x16x32_bf16 v[160:163],a[96:99],v[68:71],v[160:163]\n"
"v_accvgpr_read_b32 v97,a128\n"
"v_cvt_pk_bf16_f32 v136,v136,v137\n"
"v_mfma_f32_16x16x32_bf16 a[132:135],a[144:147],v[68:71],a[132:135]\n"
"buffer_load_dwordx4 v117,s[24:27],s83 offen offset:1024 sc1 lds\n"
"v_mfma_f32_16x16x32_bf16 v[164:167],a[100:103],v[68:71],v[164:167]\n"
"v_cvt_pk_bf16_f32 v128,v128,v129\n"
"v_cvt_pk_bf16_f32 v129,v130,v131\n"
"v_mfma_f32_16x16x32_bf16 v[168:171],a[104:107],v[68:71],v[168:171]\n"
"buffer_load_dwordx4 v117,s[24:27],s83 offen offset:2048 sc1 lds\n"
"v_cvt_pk_bf16_f32 v130,v132,v133\n"
"v_cvt_pk_bf16_f32 v131,v134,v135\n"
"v_mfma_f32_16x16x32_bf16 v[172:175],a[108:111],v[68:71],v[172:175]\n"
"ds_write_b128 v113,v[128:131] offset:0\n"
"v_cvt_pk_bf16_f32 v137,v138,v139\n"
"v_mfma_f32_16x16x32_bf16 v[176:179],a[112:115],v[68:71],v[176:179]\n"
"buffer_load_dwordx4 v117,s[24:27],s83 offen offset:3072 sc1 lds\n"
"v_cvt_pk_bf16_f32 v138,v140,v141\n"
"v_cvt_pk_bf16_f32 v139,v142,v143\n"
"v_mfma_f32_16x16x32_bf16 v[180:183],a[116:119],v[68:71],v[180:183]\n"
"s_sub_u32 s82,s3,81920\n"
"ds_write_b128 v113,v[136:139] offset:1024\n"
"v_cvt_pk_bf16_f32 v144,v144,v145\n"
"v_mfma_f32_16x16x32_bf16 v[184:187],a[120:123],v[68:71],v[184:187]\n"
"buffer_load_dwordx4 a[32:35],v112,s[24:27],s82 offen offset:0 sc1\n"
"v_cvt_pk_bf16_f32 v145,v146,v147\n"
"v_cvt_pk_bf16_f32 v146,v148,v149\n"
"v_cvt_pk_bf16_f32 v147,v150,v151\n"
"v_mfma_f32_16x16x32_bf16 v[188:191],a[124:127],v[68:71],v[188:191]\n"
"ds_write_b128 v113,v[144:147] offset:2048\n"
"v_cvt_pk_bf16_f32 v152,v152,v153\n"
"v_mfma_f32_16x16x32_bf16 v[192:195],a[96:99],v[72:75],v[192:195]\n"
"buffer_load_dwordx4 a[36:39],v112,s[24:27],s82 offen offset:1024 sc1\n"
"v_cvt_pk_bf16_f32 v153,v154,v155\n"
"v_cvt_pk_bf16_f32 v154,v156,v157\n"
"v_cvt_pk_bf16_f32 v155,v158,v159\n"
"v_mfma_f32_16x16x32_bf16 a[136:139],a[144:147],v[72:75],a[136:139]\n"
"ds_write_b128 v113,v[152:155] offset:3072\n"
"ds_write_b64 v116,v[96:97] offset:0\n"
"v_mfma_f32_16x16x32_bf16 v[196:199],a[100:103],v[72:75],v[196:199]\n"
"buffer_load_dwordx4 a[40:43],v112,s[24:27],s82 offen offset:2048 sc1\n"
"v_accvgpr_read_b32 v101,a132\n"
"v_cvt_pk_bf16_f32 v160,v160,v161\n"
"v_mfma_f32_16x16x32_bf16 v[200:203],a[104:107],v[72:75],v[200:203]\n"
"v_cvt_pk_bf16_f32 v161,v162,v163\n"
"v_cvt_pk_bf16_f32 v162,v164,v165\n"
"v_cvt_pk_bf16_f32 v163,v166,v167\n"
"v_mfma_f32_16x16x32_bf16 v[204:207],a[108:111],v[72:75],v[204:207]\n"
"ds_write_b128 v113,v[160:163] offset:4096\n"
"v_cvt_pk_bf16_f32 v168,v168,v169\n"
"v_cvt_pk_bf16_f32 v169,v170,v171\n"
"v_mfma_f32_16x16x32_bf16 v[208:211],a[112:115],v[72:75],v[208:211]\n"
"buffer_load_dwordx4 a[44:47],v112,s[24:27],s82 offen offset:3072 sc1\n"
"v_cvt_pk_bf16_f32 v170,v172,v173\n"
"v_cvt_pk_bf16_f32 v171,v174,v175\n"
"v_mfma_f32_16x16x32_bf16 v[212:215],a[116:119],v[72:75],v[212:215]\n"
"ds_write_b128 v113,v[168:171] offset:5120\n"
"v_cvt_pk_bf16_f32 v176,v176,v177\n"
"v_cvt_pk_bf16_f32 v177,v178,v179\n"
"v_mfma_f32_16x16x32_bf16 v[216:219],a[120:123],v[72:75],v[216:219]\n"
"v_cvt_pk_bf16_f32 v178,v180,v181\n"
"v_cvt_pk_bf16_f32 v179,v182,v183\n"
"v_mfma_f32_16x16x32_bf16 v[220:223],a[124:127],v[72:75],v[220:223]\n"
"ds_write_b128 v113,v[176:179] offset:6144\n"
"v_cvt_pk_bf16_f32 v184,v184,v185\n"
"v_cvt_pk_bf16_f32 v185,v186,v187\n"
"v_cvt_pk_bf16_f32 v186,v188,v189\n"
"v_cvt_pk_bf16_f32 v187,v190,v191\n"
"v_mfma_f32_16x16x32_bf16 v[224:227],a[96:99],v[76:79],v[224:227]\n"
"ds_write_b128 v113,v[184:187] offset:7168\n"
"v_mfma_f32_16x16x32_bf16 a[140:143],a[144:147],v[76:79],a[140:143]\n"
"s_sub_u32 s82,s3,131072\n"
"ds_write_b64 v116,v[100:101] offset:512\n"
"buffer_load_dwordx4 v[128:131],v112,s[24:27],s82 offen offset:0 sc1\n"
"buffer_load_dwordx4 v[132:135],v112,s[24:27],s82 offen offset:1024 sc1\n"
"v_mfma_f32_16x16x32_bf16 v[228:231],a[100:103],v[76:79],v[228:231]\n"
"buffer_load_dwordx4 v[136:139],v112,s[24:27],s82 offen offset:2048 sc1\n"
"buffer_load_dwordx4 v[140:143],v112,s[24:27],s82 offen offset:3072 sc1\n"
"s_sub_u32 s82,s3,147456\n"
"v_mfma_f32_16x16x32_bf16 v[232:235],a[104:107],v[76:79],v[232:235]\n"
"buffer_load_dwordx4 v[144:147],v112,s[24:27],s82 offen offset:0 sc1\n"
"buffer_load_dwordx4 v[148:151],v112,s[24:27],s82 offen offset:1024 sc1\n"
"buffer_load_dwordx4 v[152:155],v112,s[24:27],s82 offen offset:2048 sc1\n"
"v_mfma_f32_16x16x32_bf16 v[236:239],a[108:111],v[76:79],v[236:239]\n"
"buffer_load_dwordx4 v[156:159],v112,s[24:27],s82 offen offset:3072 sc1\n"
"v_mfma_f32_16x16x32_bf16 v[240:243],a[112:115],v[76:79],v[240:243]\n"
"v_mfma_f32_16x16x32_bf16 v[244:247],a[116:119],v[76:79],v[244:247]\n"
"v_mfma_f32_16x16x32_bf16 v[248:251],a[120:123],v[76:79],v[248:251]\n"
"v_mfma_f32_16x16x32_bf16 v[252:255],a[124:127],v[76:79],v[252:255]\n"
".Lfk_endB:\n"
"v_lshrrev_b32 v70,4,v112\n"
"v_and_b32 v71,15,v70\n"
"v_lshrrev_b32 v70,4,v70\n"
"v_lshlrev_b32 v70,2,v70\n"
"v_sub_u32 v70,v71,v70\n"
"v_mov_b32 v71,0x3f80\n"
"v_cmp_eq_u32 vcc,0,v70\n"
"v_mov_b32 v68,0\n"
"v_mov_b32 v64,0\n"
"v_cndmask_b32 v66,0,v71,vcc\n"
"v_cmp_eq_u32 vcc,1,v70\n"
"v_mov_b32 v65,0\n"
"v_mov_b32 v72,0x3f800000\n"
"v_cndmask_b32 v66,v66,v72,vcc\n"
"v_cmp_eq_u32 vcc,2,v70\n"
"v_mov_b32 v69,0\n"
"s_nop 0\n"
"v_cndmask_b32 v67,0,v71,vcc\n"
"v_cmp_eq_u32 vcc,3,v70\n"
"s_nop 1\n"
"v_cndmask_b32 v67,v67,v72,vcc\n"
"s_sub_u32 s82,s3,163840\n"
"buffer_load_dwordx4 v[0:3],v112,s[24:27],s82 offen offset:0 sc1\n"
"buffer_load_dwordx4 v[4:7],v112,s[24:27],s82 offen offset:1024 sc1\n"
"buffer_load_dwordx4 v[8:11],v112,s[24:27],s82 offen offset:2048 sc1\n"
"buffer_load_dwordx4 v[12:15],v112,s[24:27],s82 offen offset:3072 sc1\n"
"s_sub_u32 s82,s3,180224\n"
"buffer_load_dwordx4 v[160:163],v112,s[24:27],s82 offen offset:0 sc1\n"
"buffer_load_dwordx4 v[164:167],v112,s[24:27],s82 offen offset:1024 sc1\n"
"buffer_load_dwordx4 v[168:171],v112,s[24:27],s82 offen offset:2048 sc1\n"
"buffer_load_dwordx4 v[172:175],v112,s[24:27],s82 offen offset:3072 sc1\n"
"s_sub_u32 s82,s3,196608\n"
"buffer_load_dwordx4 v[176:179],v112,s[24:27],s82 offen offset:0 sc1\n"
"buffer_load_dwordx4 v[180:183],v112,s[24:27],s82 offen offset:1024 sc1\n"
"buffer_load_dwordx4 v[184:187],v112,s[24:27],s82 offen offset:2048 sc1\n"
"buffer_load_dwordx4 v[188:191],v112,s[24:27],s82 offen offset:3072 sc1\n"
"s_waitcnt lgkmcnt(0)\n"
"s_barrier\n"
"v_xor_b32 v113,32768,v113\n"
"v_xor_b32 v116,2048,v116\n"
"ds_read_b64 v[98:99],v116 offset:0\n"
"ds_read_b128 a[64:67],v113 offset:0\n"
"ds_read_b128 a[68:71],v113 offset:1024\n"
"ds_read_b128 a[72:75],v113 offset:2048\n"
"ds_read_b128 a[76:79],v113 offset:3072\n"
"ds_read_b64 v[102:103],v116 offset:512\n"
"ds_read_b128 a[80:83],v113 offset:4096\n"
"ds_read_b128 a[84:87],v113 offset:5120\n"
"ds_read_b128 a[88:91],v113 offset:6144\n"
"ds_read_b128 a[92:95],v113 offset:7168\n"
"s_waitcnt lgkmcnt(5)\n"
"v_cmp_neq_f32 vcc,v98,v104\n"
"s_nop 0\n"
"s_cbranch_vccnz .Lfk_ms_b0\n"
".Lfk_mr_b0:\n"
"v_mfma_f32_16x16x32_bf16 v[192:195],v[66:69],a[64:67],v[192:195]\n"
"v_mfma_f32_16x16x32_bf16 v[196:199],v[64:67],a[64:67],v[196:199]\n"
"v_mfma_f32_16x16x32_bf16 v[200:203],v[66:69],a[68:71],v[200:203]\n"
"v_mfma_f32_16x16x32_bf16 v[204:207],v[64:67],a[68:71],v[204:207]\n"
"v_mfma_f32_16x16x32_bf16 v[208:211],v[66:69],a[72:75],v[208:211]\n"
"v_mfma_f32_16x16x32_bf16 v[212:215],v[64:67],a[72:75],v[212:215]\n"
"v_mfma_f32_16x16x32_bf16 v[216:219],v[66:69],a[76:79],v[216:219]\n"
"v_mfma_f32_16x16x32_bf16 v[220:223],v[64:67],a[76:79],v[220:223]\n"
"s_waitcnt lgkmcnt(0)\n"
"v_cmp_neq_f32 vcc,v102,v108\n"
"s_nop 0\n"
"s_cbranch_vccnz .Lfk_ms_b1\n"
".Lfk_mr_b1:\n"
"v_mfma_f32_16x16x32_bf16 v[224:227],v[66:69],a[80:83],v[224:227]\n"
"v_mfma_f32_16x16x32_bf16 v[228:231],v[64:67],a[80:83],v[228:231]\n"
"v_accvgpr_read_b32 v122,a136\n"
"v_add_f32 v122,v122,v99\n"
"v_mfma_f32_16x16x32_bf16 v[232:235],v[66:69],a[84:87],v[232:235]\n"
"v_rcp_f32 v122,v122\n"
"v_mfma_f32_16x16x32_bf16 v[236:239],v[64:67],a[84:87],v[236:239]\n"
"v_pk_mul_f32 v[192:193],v[192:193],v[122:123] op_sel_hi:[1,0]\n"
"v_pk_mul_f32 v[194:195],v[194:195],v[122:123] op_sel_hi:[1,0]\n"
"v_pk_mul_f32 v[196:197],v[196:197],v[122:123] op_sel_hi:[1,0]\n"
"v_pk_mul_f32 v[198:199],v[198:199],v[122:123] op_sel_hi:[1,0]\n"
"v_mfma_f32_16x16x32_bf16 v[240:243],v[66:69],a[88:91],v[240:243]\n"
"v_cvt_pk_bf16_f32 v192,v192,v193\n"
"v_cvt_pk_bf16_f32 v193,v194,v195\n"
"v_mfma_f32_16x16x32_bf16 v[244:247],v[64:67],a[88:91],v[244:247]\n"
"v_cvt_pk_bf16_f32 v194,v196,v197\n"
"v_cvt_pk_bf16_f32 v195,v198,v199\n"
"v_mfma_f32_16x16x32_bf16 v[248:251],v[66:69],a[92:95],v[248:251]\n"
"buffer_store_dwordx4 v[192:195],v112,s[24:27],s70 offen offset:0 sc1\n"
"v_pk_mul_f32 v[200:201],v[200:201],v[122:123] op_sel_hi:[1,0]\n"
"v_pk_mul_f32 v[202:203],v[202:203],v[122:123] op_sel_hi:[1,0]\n"
"v_pk_mul_f32 v[204:205],v[204:205],v[122:123] op_sel_hi:[1,0]\n"
"v_pk_mul_f32 v[206:207],v[206:207],v[122:123] op_sel_hi:[1,0]\n"
"v_mfma_f32_16x16x32_bf16 v[252:255],v[64:67],a[92:95],v[252:255]\n"
"v_cvt_pk_bf16_f32 v200,v200,v201\n"
"v_cvt_pk_bf16_f32 v201,v202,v203\n"
"v_cvt_pk_bf16_f32 v202,v204,v205\n"
"v_cvt_pk_bf16_f32 v203,v206,v207\n"
"v_pk_mul_f32 v[208:209],v[208:209],v[122:123] op_sel_hi:[1,0]\n"
"v_pk_mul_f32 v[210:211],v[210:211],v[122:123] op_sel_hi:[1,0]\n"
"v_pk_mul_f32 v[212:213],v[212:213],v[122:123] op_sel_hi:[1,0]\n"
"v_pk_mul_f32 v[214:215],v[214:215],v[122:123] op_sel_hi:[1,0]\n"
"v_cvt_pk_bf16_f32 v208,v208,v209\n"
"v_cvt_pk_bf16_f32 v209,v210,v211\n"
"v_cvt_pk_bf16_f32 v210,v212,v213\n"
"v_cvt_pk_bf16_f32 v211,v214,v215\n"
"buffer_store_dwordx4 v[200:203],v112,s[24:27],s70 offen offset:1024 sc1\n"
"buffer_store_dwordx4 v[208:211],v112,s[24:27],s70 offen offset:2048 sc1\n"
"v_pk_mul_f32 v[216:217],v[216:217],v[122:123] op_sel_hi:[1,0]\n"
"v_pk_mul_f32 v[218:219],v[218:219],v[122:123] op_sel_hi:[1,0]\n"
"v_pk_mul_f32 v[220:221],v[220:221],v[122:123] op_sel_hi:[1,0]\n"
"v_pk_mul_f32 v[222:223],v[222:223],v[122:123] op_sel_hi:[1,0]\n"
"v_cvt_pk_bf16_f32 v216,v216,v217\n"
"v_cvt_pk_bf16_f32 v217,v218,v219\n"
"v_cvt_pk_bf16_f32 v218,v220,v221\n"
"v_cvt_pk_bf16_f32 v219,v222,v223\n"
"buffer_store_dwordx4 v[216:219],v112,s[24:27],s70 offen offset:3072 sc1\n"
"s_add_u32 s70,s70,262144\n"
"s_branch .Lfk_p1\n"
".Lfk_tailB7:\n"
"s_waitcnt vmcnt(11) lgkmcnt(0)\n"
"s_barrier\n"
"v_mfma_f32_16x16x32_bf16 v[0:3],a[64:67],a[0:3],v[96:99]\n"
"ds_read_b128 a[96:99],v114 offset:24576\n"
"ds_read_b128 a[100:103],v114 offset:25600\n"
"ds_read_b128 a[104:107],v114 offset:26624\n"
"v_mfma_f32_16x16x32_bf16 v[0:3],a[68:71],a[4:7],v[0:3]\n"
"ds_read_b128 a[108:111],v114 offset:27648\n"
"ds_read_b128 a[112:115],v114 offset:28672\n"
"ds_read_b128 a[116:119],v114 offset:29696\n"
"v_mfma_f32_16x16x32_bf16 v[0:3],a[72:75],a[8:11],v[0:3]\n"
"ds_read_b128 a[120:123],v114 offset:30720\n"
"ds_read_b128 a[124:127],v114 offset:31744\n"
"v_add_f32 v16,0x80000000,v126\n"
"v_mfma_f32_16x16x32_bf16 v[0:3],a[76:79],a[12:15],v[0:3]\n"
"v_min_f32 v16,v16,v104\n"
"v_add_f32 v17,0xf149f2ca,v126\n"
"v_min_f32 v17,v17,v105\n"
"v_mfma_f32_16x16x32_bf16 v[4:7],a[92:95],a[12:15],v[96:99]\n"
"v_add_f32 v18,0xf1c9f2ca,v126\n"
"v_min_f32 v18,v18,v106\n"
"v_add_f32 v19,0xf2177617,v126\n"
"v_mfma_f32_16x16x32_bf16 v[4:7],a[88:91],a[8:11],v[4:7]\n"
"v_min_f32 v19,v19,v107\n"
"v_add_f32 v20,0xf349f2ca,v126\n"
"v_mfma_f32_16x16x32_bf16 v[4:7],a[84:87],a[4:7],v[4:7]\n"
"v_add_f32 v21,0xf35691f6,v126\n"
"v_min_f32 v21,v21,v105\n"
"v_add_f32 v22,0xf3633123,v126\n"
"v_mfma_f32_16x16x32_bf16 v[4:7],a[80:83],a[0:3],v[4:7]\n"
"s_waitcnt lgkmcnt(0)\n"
"v_min_f32 v22,v22,v106\n"
"v_add_f32 v23,0xf36fd050,v126\n"
"v_mfma_f32_16x16x32_bf16 v[192:195],a[96:99],v[72:75],v[192:195]\n"
"v_exp_f32 v56,v56\n"
"v_mfma_f32_16x16x32_bf16 a[136:139],a[144:147],v[72:75],a[136:139]\n"
"v_exp_f32 v57,v57\n"
"v_mfma_f32_16x16x32_bf16 v[196:199],a[100:103],v[72:75],v[196:199]\n"
"v_min_f32 v20,v20,v104\n"
"v_cvt_pk_bf16_f32 v92,v56,v57\n"
"v_mfma_f32_16x16x32_bf16 v[200:203],a[104:107],v[72:75],v[200:203]\n"
"v_max3_f32 v118,v0,v1,v2\n"
"v_max3_f32 v118,v118,v3,v4\n"
"v_mfma_f32_16x16x32_bf16 v[204:207],a[108:111],v[72:75],v[204:207]\n"
"v_max3_f32 v118,v118,v5,v6\n"
"v_max_f32 v118,v118,v7\n"
"v_mfma_f32_16x16x32_bf16 v[208:211],a[112:115],v[72:75],v[208:211]\n"
"v_min_f32 v23,v23,v107\n"
"v_mov_b32 v120,v118\n"
"v_mfma_f32_16x16x32_bf16 v[212:215],a[116:119],v[72:75],v[212:215]\n"
"v_exp_f32 v58,v58\n"
"v_mfma_f32_16x16x32_bf16 v[216:219],a[120:123],v[72:75],v[216:219]\n"
"v_mfma_f32_16x16x32_bf16 v[220:223],a[124:127],v[72:75],v[220:223]\n"
"v_exp_f32 v59,v59\n"
"v_mfma_f32_16x16x32_bf16 v[224:227],a[96:99],v[76:79],v[224:227]\n"
"v_cvt_pk_bf16_f32 v93,v58,v59\n"
"v_mfma_f32_16x16x32_bf16 a[140:143],a[144:147],v[76:79],a[140:143]\n"
"v_accvgpr_read_b32 v122,a136\n"
"v_mfma_f32_16x16x32_bf16 v[228:231],a[100:103],v[76:79],v[228:231]\n"
"v_rcp_f32 v122,v122\n"
"v_mfma_f32_16x16x32_bf16 v[232:235],a[104:107],v[76:79],v[232:235]\n"
"v_pk_mul_f32 v[192:193],v[192:193],v[122:123] op_sel_hi:[1,0]\n"
"v_pk_mul_f32 v[194:195],v[194:195],v[122:123] op_sel_hi:[1,0]\n"
"v_pk_mul_f32 v[196:197],v[196:197],v[122:123] op_sel_hi:[1,0]\n"
"v_pk_mul_f32 v[198:199],v[198:199],v[122:123] op_sel_hi:[1,0]\n"
"v_mfma_f32_16x16x32_bf16 v[236:239],a[108:111],v[76:79],v[236:239]\n"
"v_cvt_pk_bf16_f32 v192,v192,v193\n"
"v_cvt_pk_bf16_f32 v193,v194,v195\n"
"v_mfma_f32_16x16x32_bf16 v[240:243],a[112:115],v[76:79],v[240:243]\n"
"v_cvt_pk_bf16_f32 v194,v196,v197\n"
"v_cvt_pk_bf16_f32 v195,v198,v199\n"
"v_mfma_f32_16x16x32_bf16 v[244:247],a[116:119],v[76:79],v[244:247]\n"
"buffer_store_dwordx4 v[192:195],v112,s[24:27],s70 offen offset:0 sc1\n"
"v_pk_mul_f32 v[200:201],v[200:201],v[122:123] op_sel_hi:[1,0]\n"
"v_pk_mul_f32 v[202:203],v[202:203],v[122:123] op_sel_hi:[1,0]\n"
"v_mfma_f32_16x16x32_bf16 v[248:251],a[120:123],v[76:79],v[248:251]\n"
"v_exp_f32 v60,v60\n"
"v_cvt_pk_bf16_f32 v200,v200,v201\n"
"v_cvt_pk_bf16_f32 v201,v202,v203\n"
"v_pk_mul_f32 v[204:205],v[204:205],v[122:123] op_sel_hi:[1,0]\n"
"v_pk_mul_f32 v[206:207],v[206:207],v[122:123] op_sel_hi:[1,0]\n"
"v_mfma_f32_16x16x32_bf16 v[252:255],a[124:127],v[76:79],v[252:255]\n"
"v_cvt_pk_bf16_f32 v202,v204,v205\n"
"v_cvt_pk_bf16_f32 v203,v206,v207\n"
"v_mfma_f32_16x16x32_bf16 v[12:15],a[80:83],a[16:19],v[100:103]\n"
"buffer_store_dwordx4 v[200:203],v112,s[24:27],s70 offen offset:1024 sc1\n"
"v_pk_mul_f32 v[208:209],v[208:209],v[122:123] op_sel_hi:[1,0]\n"
"v_pk_mul_f32 v[210:211],v[210:211],v[122:123] op_sel_hi:[1,0]\n"
"v_pk_mul_f32 v[212:213],v[212:213],v[122:123] op_sel_hi:[1,0]\n"
"v_pk_mul_f32 v[214:215],v[214:215],v[122:123] op_sel_hi:[1,0]\n"
"v_mfma_f32_16x16x32_bf16 v[12:15],a[84:87],a[20:23],v[12:15]\n"
"v_exp_f32 v61,v61\n"
"v_mfma_f32_16x16x32_bf16 v[12:15],a[88:91],a[24:27],v[12:15]\n"
"v_cvt_pk_bf16_f32 v94,v60,v61\n"
"v_cvt_pk_bf16_f32 v208,v208,v209\n"
"v_mfma_f32_16x16x32_bf16 v[12:15],a[92:95],a[28:31],v[12:15]\n"
"v_cvt_pk_bf16_f32 v209,v210,v211\n"
"v_cvt_pk_bf16_f32 v210,v212,v213\n"
"v_cvt_pk_bf16_f32 v211,v214,v215\n"
"v_mfma_f32_16x16x32_bf16 v[8:11],a[76:79],a[28:31],v[100:103]\n"
"buffer_store_dwordx4 v[208:211],v112,s[24:27],s70 offen offset:2048 sc1\n"
"v_pk_mul_f32 v[216:217],v[216:217],v[122:123] op_sel_hi:[1,0]\n"
"v_pk_mul_f32 v[218:219],v[218:219],v[122:123] op_sel_hi:[1,0]\n"
"v_mfma_f32_16x16x32_bf16 v[8:11],a[72:75],a[24:27],v[8:11]\n"
"v_cvt_pk_bf16_f32 v216,v216,v217\n"
"v_cvt_pk_bf16_f32 v217,v218,v219\n"
"v_pk_mul_f32 v[220:221],v[220:221],v[122:123] op_sel_hi:[1,0]\n"
"v_pk_mul_f32 v[222:223],v[222:223],v[122:123] op_sel_hi:[1,0]\n"
"v_mfma_f32_16x16x32_bf16 v[8:11],a[68:71],a[20:23],v[8:11]\n"
"v_cvt_pk_bf16_f32 v218,v220,v221\n"
"v_cvt_pk_bf16_f32 v219,v222,v223\n"
"v_mfma_f32_16x16x32_bf16 v[8:11],a[64:67],a[16:19],v[8:11]\n"
"buffer_store_dwordx4 v[216:219],v112,s[24:27],s70 offen offset:3072 sc1\n"
"v_mfma_f32_16x16x32_bf16 v[24:27],a[64:67],a[48:51],v[108:111]\n"
"s_add_u32 s70,s70,262144\n"
"v_exp_f32 v62,v62\n"
"v_mfma_f32_16x16x32_bf16 v[24:27],a[68:71],a[52:55],v[24:27]\n"
"v_exp_f32 v63,v63\n"
"v_mfma_f32_16x16x32_bf16 v[24:27],a[72:75],a[56:59],v[24:27]\n"
"s_nop 0\n"
"v_max3_f32 v119,v8,v9,v10\n"
"v_mfma_f32_16x16x32_bf16 v[24:27],a[76:79],a[60:63],v[24:27]\n"
"v_max3_f32 v119,v119,v11,v12\n"
"v_max3_f32 v119,v119,v13,v14\n"
"v_mfma_f32_16x16x32_bf16 v[28:31],a[92:95],a[60:63],v[108:111]\n"
"v_max_f32 v119,v119,v15\n"
"v_mfma_f32_16x16x32_bf16 v[28:31],a[88:91],a[56:59],v[28:31]\n"
"v_min_f32 v120,v120,v119\n"
"v_max_f32 v118,v118,v119\n"
"v_mfma_f32_16x16x32_bf16 v[28:31],a[84:87],a[52:55],v[28:31]\n"
"v_mfma_f32_16x16x32_bf16 v[28:31],a[80:83],a[48:51],v[28:31]\n"
"v_cvt_pk_bf16_f32 v95,v62,v63\n"
"v_mfma_f32_16x16x32_bf16 v[20:23],a[80:83],a[32:35],v[20:23]\n"
"v_mfma_f32_16x16x32_bf16 v[20:23],a[84:87],a[36:39],v[20:23]\n"
"v_mfma_f32_16x16x32_bf16 v[20:23],a[88:91],a[40:43],v[20:23]\n"
"v_max3_f32 v119,v24,v25,v26\n"
"v_mfma_f32_16x16x32_bf16 v[20:23],a[92:95],a[44:47],v[20:23]\n"
"s_nop 1\n"
"v_max3_f32 v119,v119,v27,v28\n"
"v_mfma_f32_16x16x32_bf16 v[16:19],a[76:79],a[44:47],v[16:19]\n"
"v_max3_f32 v119,v119,v29,v30\n"
"v_max_f32 v119,v119,v31\n"
"v_mfma_f32_16x16x32_bf16 v[16:19],a[72:75],a[40:43],v[16:19]\n"
"v_mfma_f32_16x16x32_bf16 v[16:19],a[68:71],a[36:39],v[16:19]\n"
"v_mfma_f32_16x16x32_bf16 v[16:19],a[64:67],a[32:35],v[16:19]\n"
"ds_read_b128 a[64:67],v114 offset:57344\n"
"ds_read_b128 a[68:71],v114 offset:58368\n"
"ds_read_b128 a[72:75],v114 offset:59392\n"
"v_max_f32 v118,v118,v119\n"
"v_min_f32 v120,v120,v119\n"
"v_cmp_gt_f32 s[96:97],s16,v120\n"
"s_nop 1\n"
"v_max3_f32 v119,v16,v17,v18\n"
"v_max3_f32 v119,v119,v19,v20\n"
"v_max3_f32 v119,v119,v21,v22\n"
"v_max_f32 v119,v119,v23\n"
"v_cmp_gt_f32 s[28:29],s16,v119\n"
"v_max_f32 v118,v118,v119\n"
"v_cmp_lt_f32 vcc,s93,v118\n"
"ds_read_b128 a[76:79],v114 offset:60416\n"
"ds_read_b128 a[80:83],v114 offset:61440\n"
"ds_read_b128 a[84:87],v114 offset:62464\n"
"ds_read_b128 a[88:91],v114 offset:63488\n"
"ds_read_b128 a[92:95],v114 offset:64512\n"
"v_mov_b32 v114,v112\n"
"s_sub_u32 s82,s3,32768\n"
"buffer_load_dwordx4 a[0:3],v112,s[24:27],s82 offen offset:0 sc1\n"
"buffer_load_dwordx4 a[4:7],v112,s[24:27],s82 offen offset:1024 sc1\n"
"buffer_load_dwordx4 a[8:11],v112,s[24:27],s82 offen offset:2048 sc1\n"
"buffer_load_dwordx4 a[12:15],v112,s[24:27],s82 offen offset:3072 sc1\n"
"s_sub_u32 s82,s3,49152\n"
"buffer_load_dwordx4 a[16:19],v112,s[24:27],s82 offen offset:0 sc1\n"
"buffer_load_dwordx4 a[20:23],v112,s[24:27],s82 offen offset:1024 sc1\n"
"buffer_load_dwordx4 a[24:27],v112,s[24:27],s82 offen offset:2048 sc1\n"
"buffer_load_dwordx4 a[28:31],v112,s[24:27],s82 offen offset:3072 sc1\n"
"s_and_b32 s28,s28,0xffff\n"
"s_or_b32 s96,s96,s28\n"
"s_or_b64 s[96:97],s[96:97],vcc\n"
"s_cbranch_scc1 .Lfk_cs_c3\n"
".Lfk_cr_c3:\n"
"s_waitcnt vmcnt(13) lgkmcnt(0)\n"
"s_barrier\n"
"v_mfma_f32_16x16x32_bf16 v[224:227],a[64:67],v[92:95],v[224:227]\n"
"ds_read_b128 a[96:99],v114 offset:40960\n"
"v_exp_f32 v0,v0\n"
"v_mfma_f32_16x16x32_bf16 a[140:143],a[144:147],v[92:95],a[140:143]\n"
"ds_read_b128 a[100:103],v114 offset:41984\n"
"v_exp_f32 v1,v1\n"
"v_mfma_f32_16x16x32_bf16 v[228:231],a[68:71],v[92:95],v[228:231]\n"
"ds_read_b128 a[104:107],v114 offset:43008\n"
"v_exp_f32 v2,v2\n"
"v_mfma_f32_16x16x32_bf16 v[232:235],a[72:75],v[92:95],v[232:235]\n"
"ds_read_b128 a[108:111],v114 offset:44032\n"
"v_exp_f32 v3,v3\n"
"v_mfma_f32_16x16x32_bf16 v[236:239],a[76:79],v[92:95],v[236:239]\n"
"ds_read_b128 a[112:115],v114 offset:45056\n"
"v_exp_f32 v4,v4\n"
"v_mfma_f32_16x16x32_bf16 v[240:243],a[80:83],v[92:95],v[240:243]\n"
"ds_read_b128 a[116:119],v114 offset:46080\n"
"v_exp_f32 v5,v5\n"
"v_mfma_f32_16x16x32_bf16 v[244:247],a[84:87],v[92:95],v[244:247]\n"
"ds_read_b128 a[120:123],v114 offset:47104\n"
"v_exp_f32 v6,v6\n"
"v_mfma_f32_16x16x32_bf16 v[248:251],a[88:91],v[92:95],v[248:251]\n"
"v_exp_f32 v7,v7\n"
"v_mfma_f32_16x16x32_bf16 v[252:255],a[92:95],v[92:95],v[252:255]\n"
"v_exp_f32 v8,v8\n"
"v_accvgpr_read_b32 v119,a150\n"
"v_accvgpr_read_b32 v120,a151\n"
"v_xor_b32 v119,s80,v119\n"
"v_xor_b32 v120,s81,v120\n"
"v_or_b32 v119,v119,v120\n"
"v_cmp_ne_u32 s[14:15],0,v119\n"
"s_add_u32 s18,s8,1032192\n"
"s_cmp_lg_u32 s14,0\n"
"s_cselect_b32 s18,2146435072,s18\n"
"s_add_u32 m0,s30,0\n"
"ds_read_b128 a[124:127],v114 offset:48128\n"
"buffer_load_dwordx4 v112,s[24:27],s18 offen offset:0 sc1 lds\n"
"buffer_load_dwordx4 v112,s[24:27],s18 offen offset:1024 sc1 lds\n"
"buffer_load_dwordx4 v112,s[24:27],s18 offen offset:2048 sc1 lds\n"
"buffer_load_dwordx4 v112,s[24:27],s18 offen offset:3072 sc1 lds\n"
"s_add_u32 m0,m0,4096\n"
"s_nop 0\n"
"buffer_load_dwordx4 v117,s[24:27],s18 offen offset:0 sc1 lds\n"
"buffer_load_dwordx4 v117,s[24:27],s18 offen offset:1024 sc1 lds\n"
"buffer_load_dwordx4 v117,s[24:27],s18 offen offset:2048 sc1 lds\n"
"buffer_load_dwordx4 v117,s[24:27],s18 offen offset:3072 sc1 lds\n"
"s_sub_u32 s82,s3,65536\n"
"buffer_load_dwordx4 v[32:35],v112,s[24:27],s82 offen offset:0 sc1\n"
"buffer_load_dwordx4 v[36:39],v112,s[24:27],s82 offen offset:1024 sc1\n"
"buffer_load_dwordx4 v[40:43],v112,s[24:27],s82 offen offset:2048 sc1\n"
"buffer_load_dwordx4 v[44:47],v112,s[24:27],s82 offen offset:3072 sc1\n"
"v_exp_f32 v9,v9\n"
"v_cvt_pk_bf16_f32 v64,v0,v1\n"
"v_exp_f32 v10,v10\n"
"v_cvt_pk_bf16_f32 v65,v2,v3\n"
"v_exp_f32 v11,v11\n"
"v_cvt_pk_bf16_f32 v66,v4,v5\n"
"v_exp_f32 v12,v12\n"
"v_cvt_pk_bf16_f32 v67,v6,v7\n"
"v_exp_f32 v13,v13\n"
"v_cvt_pk_bf16_f32 v68,v8,v9\n"
"v_exp_f32 v14,v14\n"
"v_cvt_pk_bf16_f32 v69,v10,v11\n"
"v_exp_f32 v15,v15\n"
"v_cvt_pk_bf16_f32 v70,v12,v13\n"
"v_exp_f32 v16,v16\n"
"v_cvt_pk_bf16_f32 v71,v14,v15\n"
"v_exp_f32 v17,v17\n"
"v_exp_f32 v18,v18\n"
"v_cvt_pk_bf16_f32 v72,v16,v17\n"
"v_exp_f32 v19,v19\n"
"v_exp_f32 v20,v20\n"
"v_cvt_pk_bf16_f32 v73,v18,v19\n"
"v_exp_f32 v21,v21\n"
"v_exp_f32 v22,v22\n"
"v_cvt_pk_bf16_f32 v74,v20,v21\n"
"v_exp_f32 v23,v23\n"
"v_exp_f32 v24,v24\n"
"v_cvt_pk_bf16_f32 v75,v22,v23\n"
"v_exp_f32 v25,v25\n"
"v_exp_f32 v26,v26\n"
"v_cvt_pk_bf16_f32 v76,v24,v25\n"
"v_exp_f32 v27,v27\n"
"v_exp_f32 v28,v28\n"
"v_cvt_pk_bf16_f32 v77,v26,v27\n"
"v_exp_f32 v29,v29\n"
"v_exp_f32 v30,v30\n"
"v_cvt_pk_bf16_f32 v78,v28,v29\n"
"v_exp_f32 v31,v31\n"
"s_nop 0\n"
"v_cvt_pk_bf16_f32 v79,v30,v31\n"
"v_accvgpr_read_b32 v122,a140\n"
"v_rcp_f32 v122,v122\n"
"s_nop 0\n"
"v_pk_mul_f32 v[224:225],v[224:225],v[122:123] op_sel_hi:[1,0]\n"
"v_pk_mul_f32 v[226:227],v[226:227],v[122:123] op_sel_hi:[1,0]\n"
"v_pk_mul_f32 v[228:229],v[228:229],v[122:123] op_sel_hi:[1,0]\n"
"v_pk_mul_f32 v[230:231],v[230:231],v[122:123] op_sel_hi:[1,0]\n"
"v_cvt_pk_bf16_f32 v224,v224,v225\n"
"v_cvt_pk_bf16_f32 v225,v226,v227\n"
"v_cvt_pk_bf16_f32 v226,v228,v229\n"
"v_cvt_pk_bf16_f32 v227,v230,v231\n"
"buffer_store_dwordx4 v[224:227],v112,s[24:27],s70 offen offset:0 sc1\n"
"v_pk_mul_f32 v[232:233],v[232:233],v[122:123] op_sel_hi:[1,0]\n"
"v_pk_mul_f32 v[234:235],v[234:235],v[122:123] op_sel_hi:[1,0]\n"
"v_pk_mul_f32 v[236:237],v[236:237],v[122:123] op_sel_hi:[1,0]\n"
"v_pk_mul_f32 v[238:239],v[238:239],v[122:123] op_sel_hi:[1,0]\n"
"v_cvt_pk_bf16_f32 v232,v232,v233\n"
"v_cvt_pk_bf16_f32 v233,v234,v235\n"
"v_cvt_pk_bf16_f32 v234,v236,v237\n"
"v_cvt_pk_bf16_f32 v235,v238,v239\n"
"buffer_store_dwordx4 v[232:235],v112,s[24:27],s70 offen offset:1024 sc1\n"
"v_pk_mul_f32 v[240:241],v[240:241],v[122:123] op_sel_hi:[1,0]\n"
"v_pk_mul_f32 v[242:243],v[242:243],v[122:123] op_sel_hi:[1,0]\n"
"v_pk_mul_f32 v[244:245],v[244:245],v[122:123] op_sel_hi:[1,0]\n"
"v_pk_mul_f32 v[246:247],v[246:247],v[122:123] op_sel_hi:[1,0]\n"
"v_cvt_pk_bf16_f32 v240,v240,v241\n"
"v_cvt_pk_bf16_f32 v241,v242,v243\n"
"v_cvt_pk_bf16_f32 v242,v244,v245\n"
"v_cvt_pk_bf16_f32 v243,v246,v247\n"
"buffer_store_dwordx4 v[240:243],v112,s[24:27],s70 offen offset:2048 sc1\n"
"v_pk_mul_f32 v[248:249],v[248:249],v[122:123] op_sel_hi:[1,0]\n"
"v_pk_mul_f32 v[250:251],v[250:251],v[122:123] op_sel_hi:[1,0]\n"
"v_pk_mul_f32 v[252:253],v[252:253],v[122:123] op_sel_hi:[1,0]\n"
"v_pk_mul_f32 v[254:255],v[254:255],v[122:123] op_sel_hi:[1,0]\n"
"v_cvt_pk_bf16_f32 v248,v248,v249\n"
"v_cvt_pk_bf16_f32 v249,v250,v251\n"
"v_cvt_pk_bf16_f32 v250,v252,v253\n"
"v_cvt_pk_bf16_f32 v251,v254,v255\n"
"buffer_store_dwordx4 v[248:251],v112,s[24:27],s70 offen offset:3072 sc1\n"
"s_add_u32 s70,s70,262144\n"
"s_mov_b32 s70,s68\n"
"s_load_dwordx2 s[36:37],s[0:1],0\n"
"s_load_dwordx2 s[44:45],s[0:1],80\n"
"s_load_dwordx2 s[48:49],s[0:1],88\n"
"s_lshl_b32 s82,s78,2\n"
"s_add_u32 s82,s82,65536\n"
"v_add_u32 v113,s82,v112\n"
"s_lshr_b32 s82,s78,2\n"
"s_add_u32 s82,s82,122880\n"
"v_lshrrev_b32 v115,1,v112\n"
"v_add_u32 v116,s82,v115\n"
"s_waitcnt lgkmcnt(0)\n"
"s_barrier\n"
"v_mfma_f32_16x16x32_bf16 v[128:131],a[96:99],v[64:67],0\n"
"s_add_u32 s83,s18,2097152\n"
"s_add_u32 m0,s30,32768\n"
"v_mfma_f32_16x16x32_bf16 a[128:131],a[144:147],v[64:67],0\n"
"buffer_load_dwordx4 v112,s[24:27],s83 offen offset:0 sc1 lds\n"
"v_mfma_f32_16x16x32_bf16 v[132:135],a[100:103],v[64:67],0\n"
"buffer_load_dwordx4 v112,s[24:27],s83 offen offset:1024 sc1 lds\n"
"v_mfma_f32_16x16x32_bf16 v[136:139],a[104:107],v[64:67],0\n"
"buffer_load_dwordx4 v112,s[24:27],s83 offen offset:2048 sc1 lds\n"
"v_mfma_f32_16x16x32_bf16 v[140:143],a[108:111],v[64:67],0\n"
"v_mfma_f32_16x16x32_bf16 v[144:147],a[112:115],v[64:67],0\n"
"buffer_load_dwordx4 v112,s[24:27],s83 offen offset:3072 sc1 lds\n"
"v_mfma_f32_16x16x32_bf16 v[148:151],a[116:119],v[64:67],0\n"
"s_add_u32 m0,m0,4096\n"
"v_mfma_f32_16x16x32_bf16 v[152:155],a[120:123],v[64:67],0\n"
"v_mfma_f32_16x16x32_bf16 v[156:159],a[124:127],v[64:67],0\n"
"buffer_load_dwordx4 v117,s[24:27],s83 offen offset:0 sc1 lds\n"
"v_mfma_f32_16x16x32_bf16 v[160:163],a[96:99],v[68:71],0\n"
"v_accvgpr_read_b32 v97,a128\n"
"v_cvt_pk_bf16_f32 v136,v136,v137\n"
"v_mfma_f32_16x16x32_bf16 a[132:135],a[144:147],v[68:71],0\n"
"buffer_load_dwordx4 v117,s[24:27],s83 offen offset:1024 sc1 lds\n"
"v_mfma_f32_16x16x32_bf16 v[164:167],a[100:103],v[68:71],0\n"
"v_cvt_pk_bf16_f32 v128,v128,v129\n"
"v_cvt_pk_bf16_f32 v129,v130,v131\n"
"v_mfma_f32_16x16x32_bf16 v[168:171],a[104:107],v[68:71],0\n"
"buffer_load_dwordx4 v117,s[24:27],s83 offen offset:2048 sc1 lds\n"
"v_cvt_pk_bf16_f32 v130,v132,v133\n"
"v_cvt_pk_bf16_f32 v131,v134,v135\n"
"v_mfma_f32_16x16x32_bf16 v[172:175],a[108:111],v[68:71],0\n"
"ds_write_b128 v113,v[128:131] offset:0\n"
"v_cvt_pk_bf16_f32 v137,v138,v139\n"
"v_mfma_f32_16x16x32_bf16 v[176:179],a[112:115],v[68:71],0\n"
"buffer_load_dwordx4 v117,s[24:27],s83 offen offset:3072 sc1 lds\n"
"v_cvt_pk_bf16_f32 v138,v140,v141\n"
"v_cvt_pk_bf16_f32 v139,v142,v143\n"
"v_mfma_f32_16x16x32_bf16 v[180:183],a[116:119],v[68:71],0\n"
"s_sub_u32 s82,s3,81920\n"
"ds_write_b128 v113,v[136:139] offset:1024\n"
"v_cvt_pk_bf16_f32 v144,v144,v145\n"
"v_mfma_f32_16x16x32_bf16 v[184:187],a[120:123],v[68:71],0\n"
"buffer_load_dwordx4 a[32:35],v112,s[24:27],s82 offen offset:0 sc1\n"
"v_cvt_pk_bf16_f32 v145,v146,v147\n"
"v_cvt_pk_bf16_f32 v146,v148,v149\n"
"v_cvt_pk_bf16_f32 v147,v150,v151\n"
"v_mfma_f32_16x16x32_bf16 v[188:191],a[124:127],v[68:71],0\n"
"ds_write_b128 v113,v[144:147] offset:2048\n"
"v_cvt_pk_bf16_f32 v152,v152,v153\n"
"v_mfma_f32_16x16x32_bf16 v[192:195],a[96:99],v[72:75],0\n"
"buffer_load_dwordx4 a[36:39],v112,s[24:27],s82 offen offset:1024 sc1\n"
"v_cvt_pk_bf16_f32 v153,v154,v155\n"
"v_cvt_pk_bf16_f32 v154,v156,v157\n"
"v_cvt_pk_bf16_f32 v155,v158,v159\n"
"v_mfma_f32_16x16x32_bf16 a[136:139],a[144:147],v[72:75],0\n"
"ds_write_b128 v113,v[152:155] offset:3072\n"
"ds_write_b64 v116,v[96:97] offset:0\n"
"v_mfma_f32_16x16x32_bf16 v[196:199],a[100:103],v[72:75],0\n"
"buffer_load_dwordx4 a[40:43],v112,s[24:27],s82 offen offset:2048 sc1\n"
"v_accvgpr_read_b32 v101,a132\n"
"v_cvt_pk_bf16_f32 v160,v160,v161\n"
"v_mfma_f32_16x16x32_bf16 v[200:203],a[104:107],v[72:75],0\n"
"v_cvt_pk_bf16_f32 v161,v162,v163\n"
"v_cvt_pk_bf16_f32 v162,v164,v165\n"
"v_cvt_pk_bf16_f32 v163,v166,v167\n"
"v_mfma_f32_16x16x32_bf16 v[204:207],a[108:111],v[72:75],0\n"
"ds_write_b128 v113,v[160:163] offset:4096\n"
"v_cvt_pk_bf16_f32 v168,v168,v169\n"
"v_cvt_pk_bf16_f32 v169,v170,v171\n"
"v_mfma_f32_16x16x32_bf16 v[208:211],a[112:115],v[72:75],0\n"
"buffer_load_dwordx4 a[44:47],v112,s[24:27],s82 offen offset:3072 sc1\n"
"v_cvt_pk_bf16_f32 v170,v172,v173\n"
"v_cvt_pk_bf16_f32 v171,v174,v175\n"
"v_mfma_f32_16x16x32_bf16 v[212:215],a[116:119],v[72:75],0\n"
"ds_write_b128 v113,v[168:171] offset:5120\n"
"v_cvt_pk_bf16_f32 v176,v176,v177\n"
"v_cvt_pk_bf16_f32 v177,v178,v179\n"
"v_mfma_f32_16x16x32_bf16 v[216:219],a[120:123],v[72:75],0\n"
"v_cvt_pk_bf16_f32 v178,v180,v181\n"
"v_cvt_pk_bf16_f32 v179,v182,v183\n"
"v_mfma_f32_16x16x32_bf16 v[220:223],a[124:127],v[72:75],0\n"
"ds_write_b128 v113,v[176:179] offset:6144\n"
"v_cvt_pk_bf16_f32 v184,v184,v185\n"
"v_cvt_pk_bf16_f32 v185,v186,v187\n"
"v_cvt_pk_bf16_f32 v186,v188,v189\n"
"v_cvt_pk_bf16_f32 v187,v190,v191\n"
"v_mfma_f32_16x16x32_bf16 v[224:227],a[96:99],v[76:79],0\n"
"ds_write_b128 v113,v[184:187] offset:7168\n"
"v_mfma_f32_16x16x32_bf16 a[140:143],a[144:147],v[76:79],0\n"
"s_sub_u32 s82,s3,131072\n"
"ds_write_b64 v116,v[100:101] offset:512\n"
"buffer_load_dwordx4 v[128:131],v112,s[24:27],s82 offen offset:0 sc1\n"
"buffer_load_dwordx4 v[132:135],v112,s[24:27],s82 offen offset:1024 sc1\n"
"v_mfma_f32_16x16x32_bf16 v[228:231],a[100:103],v[76:79],0\n"
"buffer_load_dwordx4 v[136:139],v112,s[24:27],s82 offen offset:2048 sc1\n"
"buffer_load_dwordx4 v[140:143],v112,s[24:27],s82 offen offset:3072 sc1\n"
"s_sub_u32 s82,s3,147456\n"
"v_mfma_f32_16x16x32_bf16 v[232:235],a[104:107],v[76:79],0\n"
"buffer_load_dwordx4 v[144:147],v112,s[24:27],s82 offen offset:0 sc1\n"
"buffer_load_dwordx4 v[148:151],v112,s[24:27],s82 offen offset:1024 sc1\n"
"buffer_load_dwordx4 v[152:155],v112,s[24:27],s82 offen offset:2048 sc1\n"
"v_mfma_f32_16x16x32_bf16 v[236:239],a[108:111],v[76:79],0\n"
"buffer_load_dwordx4 v[156:159],v112,s[24:27],s82 offen offset:3072 sc1\n"
"v_mfma_f32_16x16x32_bf16 v[240:243],a[112:115],v[76:79],0\n"
"v_mfma_f32_16x16x32_bf16 v[244:247],a[116:119],v[76:79],0\n"
"v_mfma_f32_16x16x32_bf16 v[248:251],a[120:123],v[76:79],0\n"
"v_mfma_f32_16x16x32_bf16 v[252:255],a[124:127],v[76:79],0\n"
"s_waitcnt vmcnt(20)\n"
"buffer_store_dwordx2 a[154:155],off,s[24:27],s69 sc1\n"
"s_branch .Lfk_endB\n"
".Lfk_head0:\n"
"s_mov_b32 s64,s61\n"
"s_cmp_eq_u32 s52,3\n"
"s_cselect_b32 s67,172032,0\n"
"s_waitcnt lgkmcnt(0)\n"
"s_barrier\n"
"v_mfma_f32_16x16x32_bf16 v[24:27],a[64:67],a[48:51],v[108:111]\n"
"v_add_f32 v0,0x80000000,v126\n"
"v_min_f32 v0,v0,v96\n"
"v_mfma_f32_16x16x32_bf16 v[24:27],a[68:71],a[52:55],v[24:27]\n"
"v_add_f32 v1,0xf149f2ca,v126\n"
"v_min_f32 v1,v1,v97\n"
"v_mfma_f32_16x16x32_bf16 v[24:27],a[72:75],a[56:59],v[24:27]\n"
"v_add_f32 v2,0xf1c9f2ca,v126\n"
"v_min_f32 v2,v2,v98\n"
"v_mfma_f32_16x16x32_bf16 v[24:27],a[76:79],a[60:63],v[24:27]\n"
"v_add_f32 v3,0xf2177617,v126\n"
"v_min_f32 v3,v3,v99\n"
"v_mfma_f32_16x16x32_bf16 v[28:31],a[92:95],a[60:63],v[108:111]\n"
"v_add_f32 v4,0xf349f2ca,v126\n"
"v_min_f32 v4,v4,v96\n"
"v_mfma_f32_16x16x32_bf16 v[28:31],a[88:91],a[56:59],v[28:31]\n"
"v_add_f32 v5,0xf35691f6,v126\n"
"v_min_f32 v5,v5,v97\n"
"v_mfma_f32_16x16x32_bf16 v[28:31],a[84:87],a[52:55],v[28:31]\n"
"v_add_f32 v6,0xf3633123,v126\n"
"v_min_f32 v6,v6,v98\n"
"v_mfma_f32_16x16x32_bf16 v[28:31],a[80:83],a[48:51],v[28:31]\n"
"v_add_f32 v7,0xf36fd050,v126\n"
"v_min_f32 v7,v7,v99\n"
"s_nop 1\n"
"v_mfma_f32_16x16x32_bf16 v[4:7],a[80:83],a[0:3],v[4:7]\n"
"s_andn2_b32 m0,s55,16384\n"
"s_add_u32 s54,s54,s22\n"
"v_mfma_f32_16x16x32_bf16 v[4:7],a[84:87],a[4:7],v[4:7]\n"
"ds_read_b128 a[96:99],v113 offset:16384\n"
"ds_read_b128 a[100:103],v113 offset:17408\n"
"v_mfma_f32_16x16x32_bf16 v[4:7],a[88:91],a[8:11],v[4:7]\n"
"ds_read_b128 a[104:107],v113 offset:18432\n"
"ds_read_b128 a[108:111],v113 offset:19456\n"
"v_mfma_f32_16x16x32_bf16 v[4:7],a[92:95],a[12:15],v[4:7]\n"
"ds_read_b128 a[112:115],v113 offset:20480\n"
"ds_read_b128 a[116:119],v113 offset:21504\n"
"v_mfma_f32_16x16x32_bf16 v[0:3],a[76:79],a[12:15],v[0:3]\n"
"ds_read_b128 a[120:123],v113 offset:22528\n"
"ds_read_b128 a[124:127],v113 offset:23552\n"
"v_mfma_f32_16x16x32_bf16 v[0:3],a[72:75],a[8:11],v[0:3]\n"
"buffer_load_dwordx4 v115,s[32:35],s54 offen sc1 lds\n"
"v_mfma_f32_16x16x32_bf16 v[0:3],a[68:71],a[4:7],v[0:3]\n"
"s_add_u32 m0,s67,0\n"
"v_max3_f32 v118,v24,v25,v26\n"
"v_mfma_f32_16x16x32_bf16 v[0:3],a[64:67],a[0:3],v[0:3]\n"
"buffer_load_dwordx4 v112,s[24:27],s64 offen offset:0 sc1 lds\n"
"v_mfma_f32_16x16x32_bf16 v[8:11],a[64:67],a[16:19],v[100:103]\n"
"buffer_load_dwordx4 v112,s[24:27],s64 offen offset:1024 sc1 lds\n"
"v_mfma_f32_16x16x32_bf16 v[8:11],a[68:71],a[20:23],v[8:11]\n"
"buffer_load_dwordx4 v112,s[24:27],s64 offen offset:2048 sc1 lds\n"
"v_mfma_f32_16x16x32_bf16 v[8:11],a[72:75],a[24:27],v[8:11]\n"
"buffer_load_dwordx4 v112,s[24:27],s64 offen offset:3072 sc1 lds\n"
"v_mfma_f32_16x16x32_bf16 v[8:11],a[76:79],a[28:31],v[8:11]\n"
"s_add_u32 m0,s67,4096\n"
"v_max3_f32 v118,v118,v27,v28\n"
"v_cvt_pk_bf16_f32 v192,v192,v193\n"
"v_mfma_f32_16x16x32_bf16 v[12:15],a[92:95],a[28:31],v[100:103]\n"
"buffer_load_dwordx4 v117,s[24:27],s64 offen offset:0 sc1 lds\n"
"v_mfma_f32_16x16x32_bf16 v[12:15],a[88:91],a[24:27],v[12:15]\n"
"buffer_load_dwordx4 v117,s[24:27],s64 offen offset:1024 sc1 lds\n"
"v_mfma_f32_16x16x32_bf16 v[12:15],a[84:87],a[20:23],v[12:15]\n"
"buffer_load_dwordx4 v117,s[24:27],s64 offen offset:2048 sc1 lds\n"
"v_mfma_f32_16x16x32_bf16 v[12:15],a[80:83],a[16:19],v[12:15]\n"
"buffer_load_dwordx4 v117,s[24:27],s64 offen offset:3072 sc1 lds\n"
"v_cvt_pk_bf16_f32 v193,v194,v195\n"
"v_cvt_pk_bf16_f32 v194,v196,v197\n"
"v_cvt_pk_bf16_f32 v195,v198,v199\n"
"v_cvt_pk_bf16_f32 v200,v200,v201\n"
"v_cvt_pk_bf16_f32 v201,v202,v203\n"
"v_cvt_pk_bf16_f32 v202,v204,v205\n"
"v_cvt_pk_bf16_f32 v203,v206,v207\n"
"ds_write_b128 v116,v[192:195] offset:32768\n"
"ds_write_b128 v116,v[200:203] offset:33792\n"
"v_cvt_pk_bf16_f32 v208,v208,v209\n"
"v_cvt_pk_bf16_f32 v209,v210,v211\n"
"v_cvt_pk_bf16_f32 v210,v212,v213\n"
"v_cvt_pk_bf16_f32 v211,v214,v215\n"
"v_cvt_pk_bf16_f32 v216,v216,v217\n"
"v_cvt_pk_bf16_f32 v217,v218,v219\n"
"v_cvt_pk_bf16_f32 v218,v220,v221\n"
"v_cvt_pk_bf16_f32 v219,v222,v223\n"
"ds_write_b128 v116,v[208:211] offset:34816\n"
"ds_write_b128 v116,v[216:219] offset:35840\n"
"ds_read_b128 a[32:35],v116 offset:32768\n"
"ds_read_b128 a[36:39],v116 offset:33792\n"
"ds_read_b128 a[40:43],v116 offset:34816\n"
"ds_read_b128 a[44:47],v116 offset:35840\n"
"s_waitcnt lgkmcnt(0)\n"
"v_mfma_f32_16x16x32_bf16 v[20:23],a[80:83],a[32:35],v[104:107]\n"
"s_add_u32 s64,s64,16384\n"
"v_max3_f32 v118,v118,v29,v30\n"
"v_mfma_f32_16x16x32_bf16 v[20:23],a[84:87],a[36:39],v[20:23]\n"
"v_max_f32 v118,v118,v31\n"
"v_mov_b32 v120,v118\n"
"v_mfma_f32_16x16x32_bf16 v[20:23],a[88:91],a[40:43],v[20:23]\n"
"v_max3_f32 v119,v0,v1,v2\n"
"v_max3_f32 v119,v119,v3,v4\n"
"v_mfma_f32_16x16x32_bf16 v[20:23],a[92:95],a[44:47],v[20:23]\n"
"buffer_store_dwordx4 a[224:227],v112,s[24:27],s56 offen offset:0 sc1\n"
"v_max3_f32 v119,v119,v5,v6\n"
"v_mfma_f32_16x16x32_bf16 v[16:19],a[76:79],a[44:47],v[104:107]\n"
"v_max_f32 v119,v119,v7\n"
"v_cmp_gt_f32 s[28:29],s16,v119\n"
"v_mfma_f32_16x16x32_bf16 v[16:19],a[72:75],a[40:43],v[16:19]\n"
"v_max_f32 v118,v118,v119\n"
"v_max3_f32 v119,v8,v9,v10\n"
"v_mfma_f32_16x16x32_bf16 v[16:19],a[68:71],a[36:39],v[16:19]\n"
"v_max3_f32 v119,v119,v11,v12\n"
"v_max3_f32 v119,v119,v13,v14\n"
"v_mfma_f32_16x16x32_bf16 v[16:19],a[64:67],a[32:35],v[16:19]\n"
"s_and_b32 s28,s28,0xffff\n"
"v_max_f32 v119,v119,v15\n"
"v_min_f32 v120,v120,v119\n"
"v_max_f32 v118,v118,v119\n"
"s_nop 3\n"
"v_max3_f32 v119,v16,v17,v18\n"
"v_max3_f32 v119,v119,v19,v20\n"
"v_max3_f32 v119,v119,v21,v22\n"
"v_max_f32 v119,v119,v23\n"
"v_min_f32 v120,v120,v119\n"
"v_max_f32 v118,v118,v119\n"
"v_cmp_gt_f32 s[96:97],s16,v120\n"
"v_cmp_lt_f32 vcc,s93,v118\n"
"s_or_b32 s96,s96,s28\n"
"s_or_b64 s[96:97],s[96:97],vcc\n"
"s_cbranch_scc1 .Lfk_cs_z3\n"
".Lfk_cr_z3:\n"
"s_waitcnt vmcnt(19) lgkmcnt(0)\n"
"s_barrier\n"
"v_mfma_f32_16x16x32_bf16 v[48:51],a[96:99],a[32:35],v[104:107]\n"
"v_add_f32 v40,0x80000000,v126\n"
"v_min_f32 v40,v40,v100\n"
"v_add_f32 v41,0xf149f2ca,v126\n"
"v_min_f32 v41,v41,v101\n"
"v_mfma_f32_16x16x32_bf16 v[48:51],a[100:103],a[36:39],v[48:51]\n"
"v_add_f32 v42,0xf1c9f2ca,v126\n"
"v_min_f32 v42,v42,v102\n"
"v_add_f32 v43,0xf2177617,v126\n"
"v_min_f32 v43,v43,v103\n"
"v_mfma_f32_16x16x32_bf16 v[48:51],a[104:107],a[40:43],v[48:51]\n"
"v_add_f32 v44,0xf349f2ca,v126\n"
"v_min_f32 v44,v44,v100\n"
"v_add_f32 v45,0xf35691f6,v126\n"
"v_min_f32 v45,v45,v101\n"
"v_mfma_f32_16x16x32_bf16 v[48:51],a[108:111],a[44:47],v[48:51]\n"
"v_add_f32 v46,0xf3633123,v126\n"
"v_min_f32 v46,v46,v102\n"
"v_add_f32 v47,0xf36fd050,v126\n"
"v_min_f32 v47,v47,v103\n"
"v_mfma_f32_16x16x32_bf16 v[52:55],a[124:127],a[44:47],v[104:107]\n"
"s_add_u32 m0,s55,4096\n"
"v_exp_f32 v0,v0\n"
"s_add_u32 s54,s54,s22\n"
"ds_read_b128 a[0:3],v116 offset:40960\n"
"v_exp_f32 v1,v1\n"
"v_mfma_f32_16x16x32_bf16 v[52:55],a[120:123],a[40:43],v[52:55]\n"
"buffer_load_dwordx4 v115,s[32:35],s54 offen sc1 lds\n"
"v_mfma_f32_16x16x32_bf16 v[52:55],a[116:119],a[36:39],v[52:55]\n"
"s_add_u32 m0,s67,16384\n"
"v_exp_f32 v2,v2\n"
"v_mfma_f32_16x16x32_bf16 v[52:55],a[112:115],a[32:35],v[52:55]\n"
"buffer_load_dwordx4 v112,s[24:27],s64 offen offset:0 sc1 lds\n"
"buffer_load_dwordx4 v112,s[24:27],s64 offen offset:1024 sc1 lds\n"
"v_exp_f32 v3,v3\n"
"v_exp_f32 v4,v4\n"
"v_mfma_f32_16x16x32_bf16 v[60:63],a[112:115],a[48:51],v[108:111]\n"
"buffer_load_dwordx4 v112,s[24:27],s64 offen offset:2048 sc1 lds\n"
"v_exp_f32 v5,v5\n"
"v_mfma_f32_16x16x32_bf16 v[60:63],a[116:119],a[52:55],v[60:63]\n"
"ds_read_b128 a[64:67],v113 offset:49152\n"
"ds_read_b128 a[68:71],v113 offset:50176\n"
"v_exp_f32 v6,v6\n"
"v_mfma_f32_16x16x32_bf16 v[60:63],a[120:123],a[56:59],v[60:63]\n"
"ds_read_b128 a[72:75],v113 offset:51200\n"
"ds_read_b128 a[76:79],v113 offset:52224\n"
"v_exp_f32 v7,v7\n"
"v_mfma_f32_16x16x32_bf16 v[60:63],a[124:127],a[60:63],v[60:63]\n"
"ds_read_b128 a[80:83],v113 offset:53248\n"
"ds_read_b128 a[84:87],v113 offset:54272\n"
"v_exp_f32 v8,v8\n"
"v_mfma_f32_16x16x32_bf16 v[56:59],a[108:111],a[60:63],v[108:111]\n"
"ds_read_b128 a[88:91],v113 offset:55296\n"
"ds_read_b128 a[92:95],v113 offset:56320\n"
"v_exp_f32 v9,v9\n"
"v_mfma_f32_16x16x32_bf16 v[56:59],a[104:107],a[56:59],v[56:59]\n"
"buffer_load_dwordx4 v112,s[24:27],s64 offen offset:3072 sc1 lds\n"
"v_mfma_f32_16x16x32_bf16 v[56:59],a[100:103],a[52:55],v[56:59]\n"
"s_add_u32 m0,s67,20480\n"
"v_exp_f32 v10,v10\n"
"v_mfma_f32_16x16x32_bf16 v[56:59],a[96:99],a[48:51],v[56:59]\n"
"buffer_load_dwordx4 v117,s[24:27],s64 offen offset:0 sc1 lds\n"
"buffer_load_dwordx4 v117,s[24:27],s64 offen offset:1024 sc1 lds\n"
"v_exp_f32 v11,v11\n"
"v_exp_f32 v12,v12\n"
"v_mfma_f32_16x16x32_bf16 v[40:43],a[96:99],a[16:19],v[40:43]\n"
"buffer_load_dwordx4 v117,s[24:27],s64 offen offset:2048 sc1 lds\n"
"v_exp_f32 v13,v13\n"
"v_mfma_f32_16x16x32_bf16 v[40:43],a[100:103],a[20:23],v[40:43]\n"
"buffer_store_dwordx4 a[168:171],v112,s[24:27],s3 offen offset:2048 sc1\n"
"v_cvt_pk_bf16_f32 v64,v0,v1\n"
"v_exp_f32 v14,v14\n"
"v_mfma_f32_16x16x32_bf16 v[40:43],a[104:107],a[24:27],v[40:43]\n"
"buffer_load_dwordx4 v117,s[24:27],s64 offen offset:3072 sc1 lds\n"
"v_mfma_f32_16x16x32_bf16 v[40:43],a[108:111],a[28:31],v[40:43]\n"
"s_add_u32 s64,s64,16384\n"
"v_exp_f32 v15,v15\n"
"v_cvt_pk_bf16_f32 v65,v2,v3\n"
"v_cvt_pk_bf16_f32 v66,v4,v5\n"
"v_exp_f32 v16,v16\n"
"v_mfma_f32_16x16x32_bf16 v[44:47],a[124:127],a[28:31],v[44:47]\n"
"buffer_store_dwordx4 a[228:231],v112,s[24:27],s56 offen offset:1024 sc1\n"
"v_cvt_pk_bf16_f32 v67,v6,v7\n"
"v_mfma_f32_16x16x32_bf16 v[44:47],a[120:123],a[24:27],v[44:47]\n"
"v_exp_f32 v17,v17\n"
"v_cvt_pk_bf16_f32 v68,v8,v9\n"
"v_cvt_pk_bf16_f32 v69,v10,v11\n"
"v_mfma_f32_16x16x32_bf16 v[44:47],a[116:119],a[20:23],v[44:47]\n"
"v_exp_f32 v18,v18\n"
"v_cvt_pk_bf16_f32 v70,v12,v13\n"
"v_cvt_pk_bf16_f32 v71,v14,v15\n"
"v_mfma_f32_16x16x32_bf16 v[44:47],a[112:115],a[16:19],v[44:47]\n"
"v_exp_f32 v19,v19\n"
"v_cvt_pk_bf16_f32 v72,v16,v17\n"
"v_max3_f32 v118,v48,v49,v50\n"
"v_exp_f32 v20,v20\n"
"v_cvt_pk_bf16_f32 v73,v18,v19\n"
"v_exp_f32 v21,v21\n"
"ds_read_b128 a[4:7],v116 offset:41984\n"
"ds_read_b128 a[8:11],v116 offset:43008\n"
"ds_read_b128 a[12:15],v116 offset:44032\n"
"v_exp_f32 v22,v22\n"
"v_exp_f32 v23,v23\n"
"v_exp_f32 v24,v24\n"
"v_cvt_pk_bf16_f32 v75,v22,v23\n"
"v_exp_f32 v25,v25\n"
"v_exp_f32 v26,v26\n"
"v_cvt_pk_bf16_f32 v76,v24,v25\n"
"v_exp_f32 v27,v27\n"
"v_exp_f32 v28,v28\n"
"v_cvt_pk_bf16_f32 v77,v26,v27\n"
"v_exp_f32 v29,v29\n"
"v_exp_f32 v30,v30\n"
"v_cvt_pk_bf16_f32 v78,v28,v29\n"
"v_exp_f32 v31,v31\n"
"v_cvt_pk_bf16_f32 v74,v20,v21\n"
"v_cvt_pk_bf16_f32 v79,v30,v31\n"
"v_max3_f32 v118,v118,v51,v52\n"
"v_max3_f32 v118,v118,v53,v54\n"
"v_max3_f32 v118,v118,v55,v60\n"
"v_max3_f32 v118,v118,v61,v62\n"
"v_max3_f32 v118,v118,v63,v56\n"
"v_max3_f32 v118,v118,v57,v58\n"
"v_max3_f32 v118,v118,v59,v40\n"
"v_max3_f32 v118,v118,v41,v42\n"
"v_max3_f32 v118,v118,v43,v44\n"
"v_max3_f32 v118,v118,v45,v46\n"
"v_max_f32 v118,v118,v47\n"
"v_cmp_lt_f32 vcc,s93,v118\n"
"s_nop 0\n"
"s_cbranch_vccnz .Lfk_cs_z0\n"
".Lfk_cr_z0:\n"
"s_waitcnt vmcnt(26) lgkmcnt(0)\n"
"s_barrier\n"
"v_mfma_f32_16x16x32_bf16 v[24:27],a[64:67],a[48:51],v[108:111]\n"
"ds_read_b128 a[96:99],v114 offset:40960\n"
"ds_read_b128 a[100:103],v114 offset:41984\n"
"v_mfma_f32_16x16x32_bf16 v[24:27],a[68:71],a[52:55],v[24:27]\n"
"ds_read_b128 a[104:107],v114 offset:43008\n"
"ds_read_b128 a[108:111],v114 offset:44032\n"
"v_mfma_f32_16x16x32_bf16 v[24:27],a[72:75],a[56:59],v[24:27]\n"
"ds_read_b128 a[112:115],v114 offset:45056\n"
"ds_read_b128 a[116:119],v114 offset:46080\n"
"v_mfma_f32_16x16x32_bf16 v[24:27],a[76:79],a[60:63],v[24:27]\n"
"ds_read_b128 a[120:123],v114 offset:47104\n"
"ds_read_b128 a[124:127],v114 offset:48128\n"
"v_mfma_f32_16x16x32_bf16 v[28:31],a[92:95],a[60:63],v[108:111]\n"
"v_add_f32 v16,0x80000000,v126\n"
"v_min_f32 v16,v16,v104\n"
"v_mfma_f32_16x16x32_bf16 v[28:31],a[88:91],a[56:59],v[28:31]\n"
"v_add_f32 v17,0xf149f2ca,v126\n"
"v_min_f32 v17,v17,v105\n"
"v_mfma_f32_16x16x32_bf16 v[28:31],a[84:87],a[52:55],v[28:31]\n"
"v_add_f32 v18,0xf1c9f2ca,v126\n"
"v_min_f32 v18,v18,v106\n"
"v_mfma_f32_16x16x32_bf16 v[28:31],a[80:83],a[48:51],v[28:31]\n"
"v_add_f32 v20,0xf349f2ca,v126\n"
"v_min_f32 v20,v20,v104\n"
"v_add_f32 v21,0xf35691f6,v126\n"
"v_min_f32 v21,v21,v105\n"
"v_add_f32 v22,0xf3633123,v126\n"
"v_min_f32 v22,v22,v106\n"
"v_add_f32 v23,0xf36fd050,v126\n"
"v_min_f32 v23,v23,v107\n"
"v_add_f32 v19,0xf2177617,v126\n"
"v_min_f32 v19,v19,v107\n"
"v_mfma_f32_16x16x32_bf16 v[20:23],a[80:83],a[32:35],v[20:23]\n"
"s_add_u32 m0,s55,8192\n"
"v_exp_f32 v40,v40\n"
"v_mfma_f32_16x16x32_bf16 v[20:23],a[84:87],a[36:39],v[20:23]\n"
"s_add_u32 s54,s54,s22\n"
"v_exp_f32 v41,v41\n"
"v_mfma_f32_16x16x32_bf16 v[20:23],a[88:91],a[40:43],v[20:23]\n"
"v_exp_f32 v42,v42\n"
"v_mfma_f32_16x16x32_bf16 v[20:23],a[92:95],a[44:47],v[20:23]\n"
"v_exp_f32 v43,v43\n"
"v_mfma_f32_16x16x32_bf16 v[16:19],a[76:79],a[44:47],v[16:19]\n"
"v_exp_f32 v44,v44\n"
"v_mfma_f32_16x16x32_bf16 v[16:19],a[72:75],a[40:43],v[16:19]\n"
"v_exp_f32 v45,v45\n"
"v_mfma_f32_16x16x32_bf16 v[16:19],a[68:71],a[36:39],v[16:19]\n"
"v_exp_f32 v46,v46\n"
"v_mfma_f32_16x16x32_bf16 v[16:19],a[64:67],a[32:35],v[16:19]\n"
"s_waitcnt lgkmcnt(0)\n"
"v_exp_f32 v47,v47\n"
"v_mfma_f32_16x16x32_bf16 v[128:131],a[96:99],v[64:67],0\n"
"v_exp_f32 v48,v48\n"
"v_mfma_f32_16x16x32_bf16 v[160:163],a[96:99],v[68:71],0\n"
"v_exp_f32 v49,v49\n"
"v_mfma_f32_16x16x32_bf16 v[192:195],a[96:99],v[72:75],0\n"
"ds_read_b128 a[64:67],v113 offset:32768\n"
"ds_read_b128 a[68:71],v113 offset:33792\n"
"v_mfma_f32_16x16x32_bf16 v[224:227],a[96:99],v[76:79],0\n"
"v_exp_f32 v50,v50\n"
"v_mfma_f32_16x16x32_bf16 a[140:143],a[144:147],v[76:79],0\n"
"ds_read_b128 a[72:75],v113 offset:34816\n"
"ds_read_b128 a[76:79],v113 offset:35840\n"
"v_mfma_f32_16x16x32_bf16 v[228:231],a[100:103],v[76:79],0\n"
"v_exp_f32 v51,v51\n"
"v_mfma_f32_16x16x32_bf16 v[132:135],a[100:103],v[64:67],0\n"
"ds_read_b128 a[80:83],v113 offset:36864\n"
"ds_read_b128 a[84:87],v113 offset:37888\n"
"v_mfma_f32_16x16x32_bf16 v[164:167],a[100:103],v[68:71],0\n"
"v_exp_f32 v52,v52\n"
"v_mfma_f32_16x16x32_bf16 v[196:199],a[100:103],v[72:75],0\n"
"v_exp_f32 v53,v53\n"
"v_mfma_f32_16x16x32_bf16 a[136:139],a[144:147],v[72:75],0\n"
"ds_read_b128 a[88:91],v113 offset:38912\n"
"ds_read_b128 a[92:95],v113 offset:39936\n"
"v_mfma_f32_16x16x32_bf16 v[200:203],a[104:107],v[72:75],0\n"
"v_exp_f32 v54,v54\n"
"v_mfma_f32_16x16x32_bf16 v[232:235],a[104:107],v[76:79],0\n"
"buffer_load_dwordx4 v115,s[32:35],s54 offen sc1 lds\n"
"v_mfma_f32_16x16x32_bf16 v[136:139],a[104:107],v[64:67],0\n"
"s_add_u32 m0,s67,49152\n"
"v_exp_f32 v55,v55\n"
"v_mfma_f32_16x16x32_bf16 v[168:171],a[104:107],v[68:71],0\n"
"buffer_load_dwordx4 v112,s[24:27],s64 offen offset:0 sc1 lds\n"
"v_mfma_f32_16x16x32_bf16 a[132:135],a[144:147],v[68:71],0\n"
"v_exp_f32 v56,v56\n"
"v_mfma_f32_16x16x32_bf16 v[172:175],a[108:111],v[68:71],0\n"
"buffer_load_dwordx4 v112,s[24:27],s64 offen offset:1024 sc1 lds\n"
"v_mfma_f32_16x16x32_bf16 v[204:207],a[108:111],v[72:75],0\n"
"v_exp_f32 v57,v57\n"
"v_mfma_f32_16x16x32_bf16 v[236:239],a[108:111],v[76:79],0\n"
"buffer_load_dwordx4 v112,s[24:27],s64 offen offset:2048 sc1 lds\n"
"v_mfma_f32_16x16x32_bf16 v[140:143],a[108:111],v[64:67],0\n"
"v_exp_f32 v58,v58\n"
"v_mfma_f32_16x16x32_bf16 a[128:131],a[144:147],v[64:67],0\n"
"buffer_load_dwordx4 v112,s[24:27],s64 offen offset:3072 sc1 lds\n"
"v_mfma_f32_16x16x32_bf16 v[144:147],a[112:115],v[64:67],0\n"
"s_add_u32 m0,s67,53248\n"
"v_exp_f32 v59,v59\n"
"v_mfma_f32_16x16x32_bf16 v[176:179],a[112:115],v[68:71],0\n"
"buffer_load_dwordx4 v117,s[24:27],s64 offen offset:0 sc1 lds\n"
"v_mfma_f32_16x16x32_bf16 v[208:211],a[112:115],v[72:75],0\n"
"v_exp_f32 v60,v60\n"
"v_mfma_f32_16x16x32_bf16 v[240:243],a[112:115],v[76:79],0\n"
"buffer_load_dwordx4 v117,s[24:27],s64 offen offset:1024 sc1 lds\n"
"v_mfma_f32_16x16x32_bf16 v[244:247],a[116:119],v[76:79],0\n"
"v_exp_f32 v61,v61\n"
"v_mfma_f32_16x16x32_bf16 v[148:151],a[116:119],v[64:67],0\n"
"buffer_load_dwordx4 v117,s[24:27],s64 offen offset:2048 sc1 lds\n"
"v_mfma_f32_16x16x32_bf16 v[180:183],a[116:119],v[68:71],0\n"
"v_exp_f32 v62,v62\n"
"v_mfma_f32_16x16x32_bf16 v[212:215],a[116:119],v[72:75],0\n"
"buffer_load_dwordx4 v117,s[24:27],s64 offen offset:3072 sc1 lds\n"
"v_mfma_f32_16x16x32_bf16 v[216:219],a[120:123],v[72:75],0\n"
"s_add_u32 s64,s64,16384\n"
"v_exp_f32 v63,v63\n"
"v_mfma_f32_16x16x32_bf16 v[248:251],a[120:123],v[76:79],0\n"
"buffer_store_dwordx4 a[232:235],v112,s[24:27],s56 offen offset:2048 sc1\n"
"s_and_b32 s67,s67,8192\n"
"v_mfma_f32_16x16x32_bf16 v[152:155],a[120:123],v[64:67],0\n"
"buffer_store_dwordx4 a[172:175],v112,s[24:27],s3 offen offset:3072 sc1\n"
"s_sub_u32 s3,s3,16384\n"
"v_mfma_f32_16x16x32_bf16 v[184:187],a[120:123],v[68:71],0\n"
"buffer_store_dwordx4 a[176:179],v112,s[24:27],s3 offen offset:0 sc1\n"
"v_cvt_pk_bf16_f32 v84,v40,v41\n"
"v_mfma_f32_16x16x32_bf16 v[188:191],a[124:127],v[68:71],0\n"
"v_cvt_pk_bf16_f32 v85,v42,v43\n"
"v_cvt_pk_bf16_f32 v86,v44,v45\n"
"v_mfma_f32_16x16x32_bf16 v[220:223],a[124:127],v[72:75],0\n"
"v_cvt_pk_bf16_f32 v87,v46,v47\n"
"v_cvt_pk_bf16_f32 v88,v48,v49\n"
"v_mfma_f32_16x16x32_bf16 v[252:255],a[124:127],v[76:79],0\n"
"v_cvt_pk_bf16_f32 v89,v50,v51\n"
"v_cvt_pk_bf16_f32 v90,v52,v53\n"
"v_mfma_f32_16x16x32_bf16 v[156:159],a[124:127],v[64:67],0\n"
"v_cvt_pk_bf16_f32 v91,v54,v55\n"
"v_cvt_pk_bf16_f32 v92,v56,v57\n"
"v_cvt_pk_bf16_f32 v93,v58,v59\n"
"ds_read_b128 a[16:19],v116 offset:8192\n"
"ds_read_b128 a[20:23],v116 offset:9216\n"
"ds_read_b128 a[24:27],v116 offset:10240\n"
"ds_read_b128 a[28:31],v116 offset:11264\n"
"v_cvt_pk_bf16_f32 v94,v60,v61\n"
"v_cvt_pk_bf16_f32 v95,v62,v63\n"
"v_max3_f32 v118,v24,v25,v26\n"
"v_max3_f32 v118,v118,v27,v28\n"
"v_max3_f32 v118,v118,v29,v30\n"
"v_max3_f32 v118,v118,v31,v20\n"
"v_max3_f32 v118,v118,v21,v22\n"
"v_max3_f32 v118,v118,v23,v16\n"
"v_max3_f32 v118,v118,v17,v18\n"
"v_max_f32 v118,v118,v19\n"
"v_cmp_lt_f32 vcc,s93,v118\n"
"s_nop 0\n"
"s_cbranch_vccnz .Lfk_cs_z1\n"
".Lfk_cr_z1:\n"
"v_mov_b32 v113,v112\n"
"s_waitcnt vmcnt(21) lgkmcnt(0)\n"
"s_barrier\n"
"v_add_f32 v56,0x80000000,v126\n"
"v_min_f32 v56,v56,v108\n"
"v_add_f32 v57,0xf149f2ca,v126\n"
"v_min_f32 v57,v57,v109\n"
"v_add_f32 v58,0xf1c9f2ca,v126\n"
"v_min_f32 v58,v58,v110\n"
"v_add_f32 v59,0xf2177617,v126\n"
"v_min_f32 v59,v59,v111\n"
"v_add_f32 v60,0xf349f2ca,v126\n"
"v_min_f32 v60,v60,v108\n"
"v_add_f32 v61,0xf35691f6,v126\n"
"v_min_f32 v61,v61,v109\n"
"v_add_f32 v62,0xf3633123,v126\n"
"v_min_f32 v62,v62,v110\n"
"v_add_f32 v63,0xf36fd050,v126\n"
"v_min_f32 v63,v63,v111\n"
"v_exp_f32 v16,v16\n"
"v_mfma_f32_16x16x32_bf16 v[56:59],a[64:67],a[48:51],v[56:59]\n"
"s_add_u32 m0,s55,12288\n"
"ds_read_b128 a[96:99],v114 offset:8192\n"
"ds_read_b128 a[100:103],v114 offset:9216\n"
"ds_read_b128 a[104:107],v114 offset:10240\n"
"v_exp_f32 v17,v17\n"
"v_mfma_f32_16x16x32_bf16 v[56:59],a[68:71],a[52:55],v[56:59]\n"
"s_add_u32 s54,s54,s22\n"
"ds_read_b128 a[108:111],v114 offset:11264\n"
"ds_read_b128 a[112:115],v114 offset:12288\n"
"ds_read_b128 a[116:119],v114 offset:13312\n"
"v_mfma_f32_16x16x32_bf16 v[56:59],a[72:75],a[56:59],v[56:59]\n"
"ds_read_b128 a[120:123],v114 offset:14336\n"
"ds_read_b128 a[124:127],v114 offset:15360\n"
"buffer_load_dwordx4 v115,s[32:35],s54 offen sc1 lds\n"
"v_cvt_pk_bf16_f32 v72,v16,v17\n"
"v_exp_f32 v18,v18\n"
"v_mfma_f32_16x16x32_bf16 v[56:59],a[76:79],a[60:63],v[56:59]\n"
"ds_read_b128 a[32:35],v116 offset:24576\n"
"ds_read_b128 a[36:39],v116 offset:25600\n"
"ds_read_b128 a[40:43],v116 offset:26624\n"
"v_mfma_f32_16x16x32_bf16 v[60:63],a[92:95],a[60:63],v[60:63]\n"
"buffer_store_dwordx4 a[180:183],v112,s[24:27],s3 offen offset:1024 sc1\n"
"ds_read_b128 a[44:47],v116 offset:27648\n"
"v_mfma_f32_16x16x32_bf16 v[60:63],a[88:91],a[56:59],v[60:63]\n"
"s_add_u32 m0,s67,32768\n"
"v_exp_f32 v19,v19\n"
"v_mfma_f32_16x16x32_bf16 v[60:63],a[84:87],a[52:55],v[60:63]\n"
"buffer_load_dwordx4 v112,s[24:27],s64 offen offset:0 sc1 lds\n"
"v_cvt_pk_bf16_f32 v73,v18,v19\n"
"v_exp_f32 v20,v20\n"
"v_mfma_f32_16x16x32_bf16 v[60:63],a[80:83],a[48:51],v[60:63]\n"
"ds_read_b128 a[64:67],v113 offset:0\n"
"ds_read_b128 a[68:71],v113 offset:1024\n"
"ds_read_b128 a[72:75],v113 offset:2048\n"
"s_waitcnt lgkmcnt(7)\n"
"v_mfma_f32_16x16x32_bf16 v[160:163],a[96:99],v[84:87],v[160:163]\n"
"ds_read_b128 a[76:79],v113 offset:3072\n"
"ds_read_b128 a[80:83],v113 offset:4096\n"
"ds_read_b128 a[84:87],v113 offset:5120\n"
"ds_read_b128 a[88:91],v113 offset:6144\n"
"ds_read_b128 a[92:95],v113 offset:7168\n"
"v_exp_f32 v21,v21\n"
"v_mfma_f32_16x16x32_bf16 a[132:135],a[144:147],v[84:87],a[132:135]\n"
"buffer_load_dwordx4 v112,s[24:27],s64 offen offset:1024 sc1 lds\n"
"v_max3_f32 v118,v56,v57,v58\n"
"v_mfma_f32_16x16x32_bf16 v[164:167],a[100:103],v[84:87],v[164:167]\n"
"v_exp_f32 v22,v22\n"
"v_cvt_pk_bf16_f32 v74,v20,v21\n"
"ds_read_b128 a[48:51],v116 offset:57344\n"
"ds_read_b128 a[52:55],v116 offset:58368\n"
"v_mfma_f32_16x16x32_bf16 v[168:171],a[104:107],v[84:87],v[168:171]\n"
"v_max3_f32 v118,v118,v59,v60\n"
"v_max3_f32 v118,v118,v61,v62\n"
"v_max_f32 v118,v118,v63\n"
"v_exp_f32 v23,v23\n"
"v_mfma_f32_16x16x32_bf16 v[172:175],a[108:111],v[84:87],v[172:175]\n"
"ds_read_b128 a[56:59],v116 offset:59392\n"
"buffer_load_dwordx4 v112,s[24:27],s64 offen offset:2048 sc1 lds\n"
"ds_read_b128 a[60:63],v116 offset:60416\n"
"v_mfma_f32_16x16x32_bf16 v[176:179],a[112:115],v[84:87],v[176:179]\n"
"v_cvt_pk_bf16_f32 v75,v22,v23\n"
"v_accvgpr_read_b32 v122,a128\n"
"v_rcp_f32 v122,v122\n"
"v_cmp_lt_f32 vcc,s93,v118\n"
"v_pk_mul_f32 v[128:129],v[128:129],v[122:123] op_sel_hi:[1,0]\n"
"v_mfma_f32_16x16x32_bf16 v[180:183],a[116:119],v[84:87],v[180:183]\n"
"buffer_store_dwordx4 a[184:187],v112,s[24:27],s3 offen offset:2048 sc1\n"
"v_exp_f32 v24,v24\n"
"v_pk_mul_f32 v[130:131],v[130:131],v[122:123] op_sel_hi:[1,0]\n"
"v_pk_mul_f32 v[132:133],v[132:133],v[122:123] op_sel_hi:[1,0]\n"
"v_pk_mul_f32 v[134:135],v[134:135],v[122:123] op_sel_hi:[1,0]\n"
"v_mfma_f32_16x16x32_bf16 v[184:187],a[120:123],v[84:87],v[184:187]\n"
"buffer_load_dwordx4 v112,s[24:27],s64 offen offset:3072 sc1 lds\n"
"v_cvt_pk_bf16_f32 v128,v128,v129\n"
"v_cvt_pk_bf16_f32 v129,v130,v131\n"
"v_cvt_pk_bf16_f32 v130,v132,v133\n"
"v_cvt_pk_bf16_f32 v131,v134,v135\n"
"v_mfma_f32_16x16x32_bf16 v[188:191],a[124:127],v[84:87],v[188:191]\n"
"buffer_store_dwordx4 v[128:131],v112,s[24:27],s70 offen offset:0 sc1\n"
"v_pk_mul_f32 v[136:137],v[136:137],v[122:123] op_sel_hi:[1,0]\n"
"v_mfma_f32_16x16x32_bf16 v[192:195],a[96:99],v[88:91],v[192:195]\n"
"s_add_u32 m0,s67,36864\n"
"v_exp_f32 v25,v25\n"
"v_exp_f32 v27,v27\n"
"v_cvt_pk_bf16_f32 v76,v24,v25\n"
"v_cvt_pk_bf16_f32 v136,v136,v137\n"
"v_pk_mul_f32 v[138:139],v[138:139],v[122:123] op_sel_hi:[1,0]\n"
"v_pk_mul_f32 v[140:141],v[140:141],v[122:123] op_sel_hi:[1,0]\n"
"v_pk_mul_f32 v[142:143],v[142:143],v[122:123] op_sel_hi:[1,0]\n"
"v_mfma_f32_16x16x32_bf16 a[136:139],a[144:147],v[88:91],a[136:139]\n"
"buffer_load_dwordx4 v117,s[24:27],s64 offen offset:0 sc1 lds\n"
"v_cvt_pk_bf16_f32 v137,v138,v139\n"
"v_mfma_f32_16x16x32_bf16 v[196:199],a[100:103],v[88:91],v[196:199]\n"
"v_cvt_pk_bf16_f32 v138,v140,v141\n"
"v_cvt_pk_bf16_f32 v139,v142,v143\n"
"v_exp_f32 v26,v26\n"
"v_mfma_f32_16x16x32_bf16 v[200:203],a[104:107],v[88:91],v[200:203]\n"
"buffer_store_dwordx4 v[136:139],v112,s[24:27],s70 offen offset:1024 sc1\n"
"v_cvt_pk_bf16_f32 v77,v26,v27\n"
"v_pk_mul_f32 v[144:145],v[144:145],v[122:123] op_sel_hi:[1,0]\n"
"v_pk_mul_f32 v[146:147],v[146:147],v[122:123] op_sel_hi:[1,0]\n"
"v_pk_mul_f32 v[148:149],v[148:149],v[122:123] op_sel_hi:[1,0]\n"
"v_pk_mul_f32 v[150:151],v[150:151],v[122:123] op_sel_hi:[1,0]\n"
"v_exp_f32 v28,v28\n"
"v_mfma_f32_16x16x32_bf16 v[204:207],a[108:111],v[88:91],v[204:207]\n"
"buffer_load_dwordx4 v117,s[24:27],s64 offen offset:1024 sc1 lds\n"
"buffer_store_dwordx4 a[188:191],v112,s[24:27],s3 offen offset:3072 sc1\n"
"v_mfma_f32_16x16x32_bf16 v[208:211],a[112:115],v[88:91],v[208:211]\n"
"s_or_b32 s3,s3,0x3c000\n"
"v_cvt_pk_bf16_f32 v144,v144,v145\n"
"v_cvt_pk_bf16_f32 v145,v146,v147\n"
"v_cvt_pk_bf16_f32 v146,v148,v149\n"
"v_cvt_pk_bf16_f32 v147,v150,v151\n"
"v_mfma_f32_16x16x32_bf16 v[212:215],a[116:119],v[88:91],v[212:215]\n"
"buffer_store_dwordx4 v[144:147],v112,s[24:27],s70 offen offset:2048 sc1\n"
"v_pk_mul_f32 v[152:153],v[152:153],v[122:123] op_sel_hi:[1,0]\n"
"v_pk_mul_f32 v[154:155],v[154:155],v[122:123] op_sel_hi:[1,0]\n"
"v_mfma_f32_16x16x32_bf16 v[216:219],a[120:123],v[88:91],v[216:219]\n"
"buffer_load_dwordx4 v117,s[24:27],s64 offen offset:2048 sc1 lds\n"
"v_exp_f32 v29,v29\n"
"v_exp_f32 v31,v31\n"
"v_cvt_pk_bf16_f32 v78,v28,v29\n"
"v_pk_mul_f32 v[156:157],v[156:157],v[122:123] op_sel_hi:[1,0]\n"
"v_pk_mul_f32 v[158:159],v[158:159],v[122:123] op_sel_hi:[1,0]\n"
"v_mfma_f32_16x16x32_bf16 v[220:223],a[124:127],v[88:91],v[220:223]\n"
"v_cvt_pk_bf16_f32 v152,v152,v153\n"
"v_cvt_pk_bf16_f32 v153,v154,v155\n"
"v_cvt_pk_bf16_f32 v154,v156,v157\n"
"v_cvt_pk_bf16_f32 v155,v158,v159\n"
"v_mfma_f32_16x16x32_bf16 v[224:227],a[96:99],v[92:95],v[224:227]\n"
"buffer_store_dwordx4 v[152:155],v112,s[24:27],s70 offen offset:3072 sc1\n"
"v_accvgpr_read_b32 v122,a132\n"
"v_mfma_f32_16x16x32_bf16 a[140:143],a[144:147],v[92:95],a[140:143]\n"
"s_add_u32 s70,s70,262144\n"
"buffer_load_dwordx4 v117,s[24:27],s64 offen offset:3072 sc1 lds\n"
"v_mfma_f32_16x16x32_bf16 v[228:231],a[100:103],v[92:95],v[228:231]\n"
"s_add_u32 s64,s64,16384\n"
"v_exp_f32 v30,v30\n"
"v_rcp_f32 v122,v122\n"
"v_cvt_pk_bf16_f32 v79,v30,v31\n"
"v_pk_mul_f32 v[160:161],v[160:161],v[122:123] op_sel_hi:[1,0]\n"
"v_pk_mul_f32 v[162:163],v[162:163],v[122:123] op_sel_hi:[1,0]\n"
"v_pk_mul_f32 v[164:165],v[164:165],v[122:123] op_sel_hi:[1,0]\n"
"v_pk_mul_f32 v[166:167],v[166:167],v[122:123] op_sel_hi:[1,0]\n"
"v_mfma_f32_16x16x32_bf16 v[232:235],a[104:107],v[92:95],v[232:235]\n"
"buffer_store_dwordx4 a[236:239],v112,s[24:27],s56 offen offset:3072 sc1\n"
"v_cvt_pk_bf16_f32 v160,v160,v161\n"
"v_cvt_pk_bf16_f32 v161,v162,v163\n"
"v_cvt_pk_bf16_f32 v162,v164,v165\n"
"v_cvt_pk_bf16_f32 v163,v166,v167\n"
"v_mfma_f32_16x16x32_bf16 v[236:239],a[108:111],v[92:95],v[236:239]\n"
"buffer_store_dwordx4 v[160:163],v112,s[24:27],s70 offen offset:0 sc1\n"
"v_pk_mul_f32 v[168:169],v[168:169],v[122:123] op_sel_hi:[1,0]\n"
"v_pk_mul_f32 v[170:171],v[170:171],v[122:123] op_sel_hi:[1,0]\n"
"v_pk_mul_f32 v[172:173],v[172:173],v[122:123] op_sel_hi:[1,0]\n"
"v_pk_mul_f32 v[174:175],v[174:175],v[122:123] op_sel_hi:[1,0]\n"
"v_mfma_f32_16x16x32_bf16 v[240:243],a[112:115],v[92:95],v[240:243]\n"
"v_cvt_pk_bf16_f32 v168,v168,v169\n"
"v_cvt_pk_bf16_f32 v169,v170,v171\n"
"v_cvt_pk_bf16_f32 v170,v172,v173\n"
"v_cvt_pk_bf16_f32 v171,v174,v175\n"
"v_mfma_f32_16x16x32_bf16 v[244:247],a[116:119],v[92:95],v[244:247]\n"
"buffer_store_dwordx4 v[168:171],v112,s[24:27],s70 offen offset:1024 sc1\n"
"v_pk_mul_f32 v[176:177],v[176:177],v[122:123] op_sel_hi:[1,0]\n"
"v_pk_mul_f32 v[178:179],v[178:179],v[122:123] op_sel_hi:[1,0]\n"
"v_pk_mul_f32 v[180:181],v[180:181],v[122:123] op_sel_hi:[1,0]\n"
"v_pk_mul_f32 v[182:183],v[182:183],v[122:123] op_sel_hi:[1,0]\n"
"v_mfma_f32_16x16x32_bf16 v[248:251],a[120:123],v[92:95],v[248:251]\n"
"v_cvt_pk_bf16_f32 v176,v176,v177\n"
"v_cvt_pk_bf16_f32 v177,v178,v179\n"
"v_cvt_pk_bf16_f32 v178,v180,v181\n"
"v_cvt_pk_bf16_f32 v179,v182,v183\n"
"v_mfma_f32_16x16x32_bf16 v[252:255],a[124:127],v[92:95],v[252:255]\n"
"buffer_store_dwordx4 v[176:179],v112,s[24:27],s70 offen offset:2048 sc1\n"
"v_pk_mul_f32 v[184:185],v[184:185],v[122:123] op_sel_hi:[1,0]\n"
"v_pk_mul_f32 v[186:187],v[186:187],v[122:123] op_sel_hi:[1,0]\n"
"v_pk_mul_f32 v[188:189],v[188:189],v[122:123] op_sel_hi:[1,0]\n"
"v_pk_mul_f32 v[190:191],v[190:191],v[122:123] op_sel_hi:[1,0]\n"
"v_cvt_pk_bf16_f32 v184,v184,v185\n"
"v_cvt_pk_bf16_f32 v185,v186,v187\n"
"v_cvt_pk_bf16_f32 v186,v188,v189\n"
"v_cvt_pk_bf16_f32 v187,v190,v191\n"
"buffer_store_dwordx4 v[184:187],v112,s[24:27],s70 offen offset:3072 sc1\n"
"s_add_u32 s70,s70,262144\n"
"s_cbranch_vccnz .Lfk_cs_z2\n"
".Lfk_cr_z2:\n"
"s_waitcnt vmcnt(34) lgkmcnt(0)\n"
"s_barrier\n"
"s_add_u32 s89,s89,1\n"
"s_branch .Lfk_winB\n"
".Lfk_ev:\n"
"s_xor_b32 s55,s55,16384\n"
"s_andn2_b32 s91,s55,16384\n"
"v_lshrrev_b32 v121,1,v112\n"
"v_add_u32 v121,s91,v121\n"
"ds_read_b64_tr_b16 a[160:161],v121 offset:0\n"
"ds_read_b64_tr_b16 a[162:163],v121 offset:512\n"
"s_add_u32 s56,s56,4096\n"
"s_swappc_b64 s[20:21],s[94:95]\n"
"s_xor_b32 s55,s55,16384\n"
"s_andn2_b32 s91,s55,16384\n"
"v_lshrrev_b32 v121,1,v112\n"
"v_add_u32 v121,s91,v121\n"
"ds_read_b64_tr_b16 a[164:165],v121 offset:4096\n"
"ds_read_b64_tr_b16 a[166:167],v121 offset:4608\n"
"ds_read_b64_tr_b16 a[168:169],v121 offset:8192\n"
"ds_read_b64_tr_b16 a[170:171],v121 offset:8704\n"
"ds_read_b64_tr_b16 a[172:173],v121 offset:12288\n"
"ds_read_b64_tr_b16 a[174:175],v121 offset:12800\n"
"ds_read_b64_tr_b16 a[176:177],v121 offset:0\n"
"ds_read_b64_tr_b16 a[178:179],v121 offset:512\n"
"buffer_store_dwordx2 a[154:155],off,s[24:27],s2 sc1\n"
"s_and_b32 s2,s2,0xfffffc00\n"
"s_mov_b32 s56,s10\n"
"v_accvgpr_write_b32 a240,0\n"
"v_accvgpr_write_b32 a241,0\n"
"v_accvgpr_write_b32 a242,0\n"
"v_accvgpr_write_b32 a243,0\n"
"v_accvgpr_write_b32 a244,0\n"
"v_accvgpr_write_b32 a245,0\n"
"v_accvgpr_write_b32 a246,0\n"
"v_accvgpr_write_b32 a247,0\n"
"v_accvgpr_write_b32 a248,0\n"
"v_accvgpr_write_b32 a249,0\n"
"v_accvgpr_write_b32 a250,0\n"
"v_accvgpr_write_b32 a251,0\n"
"v_accvgpr_write_b32 a252,0\n"
"v_accvgpr_write_b32 a253,0\n"
"v_accvgpr_write_b32 a254,0\n"
"v_accvgpr_write_b32 a255,0\n"
"s_mov_b64 s[32:33],s[36:37]\n"
"s_mov_b32 s54,-229376\n"
"s_swappc_b64 s[20:21],s[94:95]\n"
"s_xor_b32 s55,s55,16384\n"
"s_add_u32 s56,s56,131072\n"
"s_andn2_b32 s91,s55,16384\n"
"v_lshrrev_b32 v121,1,v112\n"
"v_add_u32 v121,s91,v121\n"
"ds_read_b64_tr_b16 a[180:181],v121 offset:20480\n"
"ds_read_b64_tr_b16 a[182:183],v121 offset:20992\n"
"ds_read_b64_tr_b16 a[184:185],v121 offset:24576\n"
"ds_read_b64_tr_b16 a[186:187],v121 offset:25088\n"
"ds_read_b64_tr_b16 a[188:189],v121 offset:28672\n"
"ds_read_b64_tr_b16 a[190:191],v121 offset:29184\n"
"ds_read_b64_tr_b16 a[192:193],v121 offset:0\n"
"ds_read_b64_tr_b16 a[194:195],v121 offset:512\n"
"buffer_load_dwordx2 a[150:151],v112,s[24:27],s2 offen sc1\n"
"s_add_u32 s82,s59,8192\n"
"buffer_store_dwordx2 a[154:155],off,s[24:27],s82 sc1\n"
"s_swappc_b64 s[20:21],s[94:95]\n"
"s_xor_b32 s55,s55,16384\n"
"s_add_u32 s56,s56,131072\n"
"s_andn2_b32 s91,s55,16384\n"
"v_lshrrev_b32 v121,1,v112\n"
"v_add_u32 v121,s91,v121\n"
"ds_read_b64_tr_b16 a[196:197],v121 offset:4096\n"
"ds_read_b64_tr_b16 a[198:199],v121 offset:4608\n"
"ds_read_b64_tr_b16 a[200:201],v121 offset:8192\n"
"ds_read_b64_tr_b16 a[202:203],v121 offset:8704\n"
"ds_read_b64_tr_b16 a[204:205],v121 offset:12288\n"
"ds_read_b64_tr_b16 a[206:207],v121 offset:12800\n"
"ds_read_b64_tr_b16 a[208:209],v121 offset:0\n"
"ds_read_b64_tr_b16 a[210:211],v121 offset:512\n"
"s_mov_b32 s84,0x40000\n"
".Lfk_w_chk:\n"
"v_accvgpr_read_b32 v119,a150\n"
"v_accvgpr_read_b32 v120,a151\n"
"v_xor_b32 v119,s80,v119\n"
"v_xor_b32 v120,s81,v120\n"
"v_or_b32 v119,v119,v120\n"
"v_cmp_ne_u32 vcc,0,v119\n"
"s_nop 0\n"
"s_cbranch_vccz .Lfk_w_ok\n"
"s_sub_u32 s84,s84,1\n"
"s_cbranch_scc1 .Lfk_w_ok\n"
"s_sleep 4\n"
"buffer_load_dwordx2 a[150:151],v112,s[24:27],s2 offen sc1\n"
"s_waitcnt vmcnt(0)\n"
"s_branch .Lfk_w_chk\n"
".Lfk_w_ok:\n"
"v_lshrrev_b32 v121,1,v112\n"
"v_and_b32 v121,0xf8,v121\n"
"s_add_u32 s82,s63,8192\n"
"buffer_load_dwordx2 a[150:151],v121,s[24:27],s82 offen sc1\n"
"s_mov_b64 s[32:33],s[24:25]\n"
"s_mov_b32 s34,s26\n"
"s_mov_b32 s22,1024\n"
"v_mov_b32 v115,v112\n"
"s_sub_u32 s54,s3,1024\n"
"s_swappc_b64 s[20:21],s[94:95]\n"
"s_xor_b32 s55,s55,16384\n"
"s_add_u32 s56,s56,131072\n"
"s_andn2_b32 s91,s55,16384\n"
"v_lshrrev_b32 v121,1,v112\n"
"v_add_u32 v121,s91,v121\n"
"ds_read_b64_tr_b16 a[212:213],v121 offset:20480\n"
"ds_read_b64_tr_b16 a[214:215],v121 offset:20992\n"
"ds_read_b64_tr_b16 a[216:217],v121 offset:24576\n"
"ds_read_b64_tr_b16 a[218:219],v121 offset:25088\n"
"ds_read_b64_tr_b16 a[220:221],v121 offset:28672\n"
"ds_read_b64_tr_b16 a[222:223],v121 offset:29184\n"
"v_add_u32 v121,s91,v112\n"
"ds_read_b128 a[224:227],v121 offset:0\n"
"s_sub_u32 s54,s3,17408\n"
"s_mov_b32 s84,0x40000\n"
".Lfk_k_chk:\n"
"v_accvgpr_read_b32 v119,a150\n"
"v_accvgpr_read_b32 v120,a151\n"
"v_xor_b32 v119,s80,v119\n"
"v_xor_b32 v120,s81,v120\n"
"v_or_b32 v119,v119,v120\n"
"v_cmp_ne_u32 vcc,0,v119\n"
"s_nop 0\n"
"s_cbranch_vccz .Lfk_k_ok\n"
"s_sub_u32 s84,s84,1\n"
"s_cbranch_scc1 .Lfk_k_ok\n"
"s_sleep 4\n"
"v_lshrrev_b32 v121,1,v112\n"
"v_and_b32 v121,0xf8,v121\n"
"s_add_u32 s82,s63,8192\n"
"buffer_load_dwordx2 a[150:151],v121,s[24:27],s82 offen sc1\n"
"s_waitcnt vmcnt(0)\n"
"s_branch .Lfk_k_chk\n"
".Lfk_k_ok:\n"
"s_swappc_b64 s[20:21],s[94:95]\n"
"s_xor_b32 s55,s55,16384\n"
"s_andn2_b32 s91,s55,16384\n"
"v_add_u32 v121,s91,v112\n"
"ds_read_b128 a[228:231],v121 offset:4096\n"
"ds_read_b128 a[232:235],v121 offset:8192\n"
"ds_read_b128 a[236:239],v121 offset:12288\n"
"ds_read_b128 a[240:243],v121 offset:0\n"
"s_sub_u32 s54,s3,99328\n"
"s_mov_b32 s56,2146435072\n"
"s_getpc_b64 s[96:97]\n"
".Lfk_rt1:\n"
"s_sub_u32 s96,s96,.Lfk_rt1-.Lfk_tailA\n"
"s_subb_u32 s97,s97,0\n"
"s_mov_b32 s98,.Lfk_cold-.Lfk_tailA\n"
"s_mov_b32 s99,0x20000\n"
"s_lshr_b32 s82,s100,3\n"
"s_lshl2_add_u32 s82,s82,s52\n"
"s_lshl_b32 s82,s82,9\n"
"v_lshrrev_b32 v121,1,v112\n"
"buffer_load_dword a148,v121,s[96:99],s82 offen\n"
"s_swappc_b64 s[20:21],s[94:95]\n"
"s_xor_b32 s55,s55,16384\n"
"s_andn2_b32 s91,s55,16384\n"
"v_add_u32 v121,s91,v112\n"
"ds_read_b128 a[244:247],v121 offset:20480\n"
"ds_read_b128 a[248:251],v121 offset:24576\n"
"ds_read_b128 a[252:255],v121 offset:28672\n"
"ds_read_b128 a[156:159],v121 offset:0\n"
"s_sub_u32 s54,s3,115712\n"
"s_lshl_b32 s82,s100,3\n"
"s_add_u32 s82,s82,148946944\n"
"s_cmp_eq_u32 s52,0\n"
"s_cselect_b32 s82,s82,2146435072\n"
"buffer_store_dwordx2 a[154:155],off,s[24:27],s82 sc1\n"
"s_lshr_b32 s82,s3,9\n"
"s_and_b32 s82,s82,0x3000\n"
"s_add_u32 s11,s11,s82\n"
"s_add_u32 s9,s9,s82\n"
"s_lshl3_add_u32 s8,s82,s8\n"
"s_lshr_b32 s82,s3,18\n"
"s_sub_u32 s82,s82,s60\n"
"s_and_b32 s82,s82,7\n"
"s_lshl_b32 s82,s82,17\n"
"s_add_u32 s8,s8,s82\n"
"v_lshrrev_b32 v120,1,v112\n"
"buffer_load_dwordx2 a[150:151],v120,s[24:27],s11 offen sc1\n"
"s_lshl_b32 s30,s78,1\n"
"s_swappc_b64 s[20:21],s[94:95]\n"
"s_nop 1\n"
".Lfk_xn0_s:\n"
"s_mov_b32 s84,0x40000\n"
"buffer_load_dwordx4 v[74:77],v112,s[24:27],s58 offen sc1\n"
"s_waitcnt vmcnt(0)\n"
".Lfk_xn0:\n"
"v_xor_b32 v116,s80,v74\n"
"v_xor_b32 v117,s81,v75\n"
"v_or_b32 v116,v116,v117\n"
"v_xor_b32 v117,s80,v76\n"
"v_or_b32 v116,v116,v117\n"
"v_xor_b32 v117,s81,v77\n"
"v_or_b32 v116,v116,v117\n"
"v_cmp_ne_u32 vcc,0,v116\n"
"s_nop 0\n"
"s_cbranch_vccz .Lfk_xn0_x\n"
"s_sleep 4\n"
"s_mov_b64 exec,vcc\n"
"buffer_load_dwordx4 v[74:77],v112,s[24:27],s58 offen sc1\n"
"s_mov_b64 exec,-1\n"
"s_sub_u32 s84,s84,1\n"
"s_waitcnt vmcnt(0)\n"
"s_cbranch_scc0 .Lfk_xn0\n"
".Lfk_xn0_x:\n"
"s_nop 1\n"
"s_branch .Lfk_xn0_ok\n"
".Lfk_xn1_s:\n"
"s_mov_b32 s84,0x40000\n"
"buffer_load_dwordx4 v[74:77],v112,s[24:27],s58 offen sc1\n"
"s_waitcnt vmcnt(0)\n"
".Lfk_xn1:\n"
"v_xor_b32 v116,s80,v74\n"
"v_xor_b32 v117,s81,v75\n"
"v_or_b32 v116,v116,v117\n"
"v_xor_b32 v117,s80,v76\n"
"v_or_b32 v116,v116,v117\n"
"v_xor_b32 v117,s81,v77\n"
"v_or_b32 v116,v116,v117\n"
"v_cmp_ne_u32 vcc,0,v116\n"
"s_nop 0\n"
"s_cbranch_vccz .Lfk_xn1_x\n"
"s_sleep 4\n"
"s_mov_b64 exec,vcc\n"
"buffer_load_dwordx4 v[74:77],v112,s[24:27],s58 offen sc1\n"
"s_mov_b64 exec,-1\n"
"s_sub_u32 s84,s84,1\n"
"s_waitcnt vmcnt(0)\n"
"s_cbranch_scc0 .Lfk_xn1\n"
".Lfk_xn1_x:\n"
"s_nop 1\n"
"s_branch .Lfk_xn1_ok\n"
".Lfk_kv_s:\n"
"s_mov_b32 s84,0x40000\n"
"buffer_load_dwordx2 v[118:119],v120,s[24:27],s63 offen sc1\n"
"s_waitcnt vmcnt(0)\n"
".Lfk_kv:\n"
"v_xor_b32 v116,s80,v118\n"
"v_xor_b32 v117,s81,v119\n"
"v_or_b32 v116,v116,v117\n"
"v_cmp_ne_u32 vcc,0,v116\n"
"s_nop 0\n"
"s_cbranch_vccz .Lfk_kv_x\n"
"s_sleep 4\n"
"s_mov_b64 exec,vcc\n"
"buffer_load_dwordx2 v[118:119],v120,s[24:27],s63 offen sc1\n"
"s_mov_b64 exec,-1\n"
"s_sub_u32 s84,s84,1\n"
"s_waitcnt vmcnt(0)\n"
"s_cbranch_scc0 .Lfk_kv\n"
".Lfk_kv_x:\n"
"s_nop 1\n"
"s_branch .Lfk_kv_ok\n"
".Lfk_cs_h3:\n"
"s_mov_b32 s91,15\n"
"s_mov_b32 s90,15\n"
"s_call_b64 s[94:95],.Lfk_rare0\n"
"s_branch .Lfk_cr_h3\n"
".Lfk_cs_h1:\n"
"s_mov_b32 s91,15\n"
"s_call_b64 s[94:95],.Lfk_rare3\n"
"s_branch .Lfk_cr_h1\n"
".Lfk_cs_h2:\n"
"s_mov_b32 s91,15\n"
"s_call_b64 s[94:95],.Lfk_rare2\n"
"s_branch .Lfk_cr_h2\n"
".Lfk_cs_x1:\n"
"s_mov_b32 s91,15\n"
"s_call_b64 s[94:95],.Lfk_rare1\n"
"s_branch .Lfk_cr_x1\n"
".Lfk_cs_a3:\n"
"s_mov_b32 s91,14\n"
"s_call_b64 s[94:95],.Lfk_rare0\n"
"s_branch .Lfk_cr_a3\n"
".Lfk_cs_a0:\n"
"s_mov_b32 s91,12\n"
"s_call_b64 s[94:95],.Lfk_rare1\n"
"s_branch .Lfk_cr_a0\n"
".Lfk_cs_a1:\n"
"s_mov_b32 s91,8\n"
"s_call_b64 s[94:95],.Lfk_rare0\n"
"s_branch .Lfk_cr_a1\n"
".Lfk_ms_a0:\n"
"s_call_b64 s[94:95],.Lfk_mslow0\n"
"s_branch .Lfk_mr_a0\n"
".Lfk_ms_a1:\n"
"s_call_b64 s[94:95],.Lfk_mslow1\n"
"s_branch .Lfk_mr_a1\n"
".Lfk_p2:\n"
"s_nop 7\n"
"s_mov_b32 s84,0x40000\n"
"buffer_load_dwordx2 v[122:123],v115,s[24:27],s11 offen sc1\n"
"s_waitcnt vmcnt(0)\n"
".Lfk_pj:\n"
"v_xor_b32 v119,s80,v122\n"
"v_xor_b32 v120,s81,v123\n"
"v_or_b32 v119,v119,v120\n"
"v_cmp_ne_u32 s[14:15],0,v119\n"
"s_nop 0\n"
"s_mov_b32 vcc_lo,s14\n"
"s_mov_b32 vcc_hi,0\n"
"s_cbranch_vccz .Lfk_pj_x\n"
"s_sleep 4\n"
"s_mov_b64 exec,vcc\n"
"buffer_load_dwordx2 v[122:123],v115,s[24:27],s11 offen sc1\n"
"s_mov_b64 exec,-1\n"
"s_sub_u32 s84,s84,1\n"
"s_waitcnt vmcnt(0)\n"
"s_cbranch_scc0 .Lfk_pj\n"
".Lfk_pj_x:\n"
"s_cmp_ge_u32 s52,2\n"
"s_cselect_b32 s18,1032192,0\n"
"s_add_u32 s18,s18,s8\n"
"s_add_u32 m0,s30,0\n"
"s_nop 0\n"
"buffer_load_dwordx4 v112,s[24:27],s18 offen offset:0 sc1 lds\n"
"buffer_load_dwordx4 v112,s[24:27],s18 offen offset:1024 sc1 lds\n"
"buffer_load_dwordx4 v112,s[24:27],s18 offen offset:2048 sc1 lds\n"
"buffer_load_dwordx4 v112,s[24:27],s18 offen offset:3072 sc1 lds\n"
"s_add_u32 m0,m0,4096\n"
"s_nop 0\n"
"buffer_load_dwordx4 v117,s[24:27],s18 offen offset:0 sc1 lds\n"
"buffer_load_dwordx4 v117,s[24:27],s18 offen offset:1024 sc1 lds\n"
"buffer_load_dwordx4 v117,s[24:27],s18 offen offset:2048 sc1 lds\n"
"buffer_load_dwordx4 v117,s[24:27],s18 offen offset:3072 sc1 lds\n"
"s_add_u32 s83,s18,2097152\n"
"s_add_u32 m0,s30,32768\n"
"s_nop 0\n"
"buffer_load_dwordx4 v112,s[24:27],s83 offen offset:0 sc1 lds\n"
"buffer_load_dwordx4 v112,s[24:27],s83 offen offset:1024 sc1 lds\n"
"buffer_load_dwordx4 v112,s[24:27],s83 offen offset:2048 sc1 lds\n"
"buffer_load_dwordx4 v112,s[24:27],s83 offen offset:3072 sc1 lds\n"
"s_add_u32 m0,m0,4096\n"
"s_nop 0\n"
"buffer_load_dwordx4 v117,s[24:27],s83 offen offset:0 sc1 lds\n"
"buffer_load_dwordx4 v117,s[24:27],s83 offen offset:1024 sc1 lds\n"
"buffer_load_dwordx4 v117,s[24:27],s83 offen offset:2048 sc1 lds\n"
"buffer_load_dwordx4 v117,s[24:27],s83 offen offset:3072 sc1 lds\n"
"s_waitcnt vmcnt(0)\n"
"s_nop 1\n"
"s_branch .Lfk_p3\n"
".Lfk_cs_k1:\n"
"s_mov_b32 s91,15\n"
"s_call_b64 s[94:95],.Lfk_rare3\n"
"s_branch .Lfk_cr_k1\n"
".Lfk_cs_k2:\n"
"s_mov_b32 s91,15\n"
"s_call_b64 s[94:95],.Lfk_rare2\n"
"s_branch .Lfk_cr_k2\n"
".Lfk_cs_x2:\n"
"s_mov_b32 s91,15\n"
"s_call_b64 s[94:95],.Lfk_rare1\n"
"s_branch .Lfk_cr_x2\n"
".Lfk_cs_u3:\n"
"s_mov_b32 s91,15\n"
"s_call_b64 s[94:95],.Lfk_rare0\n"
"s_branch .Lfk_cr_u3\n"
".Lfk_cs_mu3:\n"
"s_call_b64 s[94:95],.Lfk_meet3\n"
"s_branch .Lfk_cr_mu3\n"
".Lfk_cs_u0:\n"
"s_mov_b32 s91,14\n"
"s_call_b64 s[94:95],.Lfk_rare1\n"
"s_branch .Lfk_cr_u0\n"
".Lfk_cs_mu0:\n"
"s_call_b64 s[94:95],.Lfk_meet3\n"
"s_branch .Lfk_cr_mu0\n"
".Lfk_cs_u1:\n"
"s_mov_b32 s91,12\n"
"s_call_b64 s[94:95],.Lfk_rare0\n"
"s_branch .Lfk_cr_u1\n"
".Lfk_cs_mu1:\n"
"s_call_b64 s[94:95],.Lfk_meet3\n"
"s_branch .Lfk_cr_mu1\n"
".Lfk_cs_u2:\n"
"s_mov_b32 s91,8\n"
"s_call_b64 s[94:95],.Lfk_rare1\n"
"s_branch .Lfk_cr_u2\n"
".Lfk_cs_mu2:\n"
"s_call_b64 s[94:95],.Lfk_meet3\n"
"s_branch .Lfk_cr_mu2\n"
".Lfk_cs_v3:\n"
"s_mov_b32 s91,15\n"
"s_mov_b32 s90,15\n"
"s_call_b64 s[94:95],.Lfk_rare0\n"
"s_branch .Lfk_cr_v3\n"
".Lfk_cs_mv3:\n"
"s_call_b64 s[94:95],.Lfk_meet3\n"
"s_branch .Lfk_cr_mv3\n"
".Lfk_cs_mv0:\n"
"s_call_b64 s[94:95],.Lfk_meet3\n"
"s_branch .Lfk_cr_mv0\n"
".Lfk_cs_v1:\n"
"s_call_b64 s[94:95],.Lfk_meet3\n"
"v_or_b32 v118,v118,v95\n"
"v_and_b32 v118,0x40004000,v118\n"
"v_cmp_ne_u32 vcc,0,v118\n"
"s_nop 0\n"
"s_cbranch_vccz .Lfk_cr_v1\n"
"s_mov_b32 s91,15\n"
"s_call_b64 s[94:95],.Lfk_rare3\n"
"s_branch .Lfk_cr_v1\n"
".Lfk_cs_v2:\n"
"s_call_b64 s[94:95],.Lfk_meet3\n"
"v_or_b32 v118,v118,v79\n"
"v_and_b32 v118,0x40004000,v118\n"
"v_cmp_ne_u32 vcc,0,v118\n"
"s_nop 0\n"
"s_cbranch_vccz .Lfk_cr_v2\n"
"s_mov_b32 s91,15\n"
"s_call_b64 s[94:95],.Lfk_rare2\n"
"s_branch .Lfk_cr_v2\n"
".Lfk_cs_b3:\n"
"s_mov_b32 s91,15\n"
"s_call_b64 s[94:95],.Lfk_rare0\n"
"s_branch .Lfk_cr_b3\n"
".Lfk_ms_b0:\n"
"s_call_b64 s[94:95],.Lfk_mslow0\n"
"s_branch .Lfk_mr_b0\n"
".Lfk_ms_b1:\n"
"s_call_b64 s[94:95],.Lfk_mslow1\n"
"s_branch .Lfk_mr_b1\n"
".Lfk_cs_c3:\n"
"s_mov_b32 s91,15\n"
"s_mov_b32 s90,15\n"
"s_call_b64 s[94:95],.Lfk_rare0\n"
"s_branch .Lfk_cr_c3\n"
".Lfk_cs_z3:\n"
"s_mov_b32 s91,15\n"
"s_mov_b32 s90,15\n"
"s_call_b64 s[94:95],.Lfk_rare0\n"
"s_branch .Lfk_cr_z3\n"
".Lfk_cs_z0:\n"
"s_mov_b32 s91,14\n"
"s_call_b64 s[94:95],.Lfk_rare1\n"
"s_branch .Lfk_cr_z0\n"
".Lfk_cs_z1:\n"
"s_mov_b32 s91,12\n"
"s_call_b64 s[94:95],.Lfk_rare0\n"
"s_branch .Lfk_cr_z1\n"
".Lfk_cs_z2:\n"
"s_mov_b32 s91,8\n"
"s_call_b64 s[94:95],.Lfk_rare1\n"
"s_branch .Lfk_cr_z2\n"
".Lfk_cold:\n"
".Lfk_rare0:\n"
"s_nop 7\n"
"s_bitcmp1_b32 s91,0\n"
"s_cbranch_scc0 .Lfk_rare0_0\n"
"v_max3_f32 v119,v0,v1,v2\n"
"v_max3_f32 v120,v3,v4,v5\n"
"v_max3_f32 v119,v119,v6,v7\n"
"v_max_f32 v119,v119,v120\n"
"v_mov_b32 v120,v119\n"
"s_nop 1\n"
"v_permlane16_swap_b32 v120,v119\n"
"v_max_f32 v119,v119,v120\n"
"v_mov_b32 v120,v119\n"
"s_nop 1\n"
"v_permlane32_swap_b32 v120,v119\n"
"v_max_f32 v119,v119,v120\n"
"s_bitcmp1_b32 s90,0\n"
"s_cselect_b32 s82,0xff800000,0\n"
"v_add_f32 v119,0x41980000,v119\n"
"v_max_f32 v119,s82,v119\n"
"v_cmp_lt_f32 vcc,s17,|v119|\n"
"s_nop 0\n"
"s_cbranch_vccz .Lfk_rare0_0\n"
"v_ceil_f32 v119,v119\n"
"v_sub_f32 v0,v0,v119\n"
"v_sub_f32 v1,v1,v119\n"
"v_sub_f32 v2,v2,v119\n"
"v_sub_f32 v3,v3,v119\n"
"v_sub_f32 v4,v4,v119\n"
"v_sub_f32 v5,v5,v119\n"
"v_sub_f32 v6,v6,v119\n"
"v_sub_f32 v7,v7,v119\n"
"v_sub_f32 v96,v96,v119\n"
"v_sub_f32 v97,v97,v119\n"
"v_sub_f32 v98,v98,v119\n"
"v_sub_f32 v99,v99,v119\n"
"s_cbranch_scc1 .Lfk_rare0_0\n"
"v_exp_f32 v119,-v119\n"
"s_nop 0\n"
"v_mul_f32 v128,v128,v119\n"
"v_mul_f32 v129,v129,v119\n"
"v_mul_f32 v130,v130,v119\n"
"v_mul_f32 v131,v131,v119\n"
"v_mul_f32 v132,v132,v119\n"
"v_mul_f32 v133,v133,v119\n"
"v_mul_f32 v134,v134,v119\n"
"v_mul_f32 v135,v135,v119\n"
"v_mul_f32 v136,v136,v119\n"
"v_mul_f32 v137,v137,v119\n"
"v_mul_f32 v138,v138,v119\n"
"v_mul_f32 v139,v139,v119\n"
"v_mul_f32 v140,v140,v119\n"
"v_mul_f32 v141,v141,v119\n"
"v_mul_f32 v142,v142,v119\n"
"v_mul_f32 v143,v143,v119\n"
"v_mul_f32 v144,v144,v119\n"
"v_mul_f32 v145,v145,v119\n"
"v_mul_f32 v146,v146,v119\n"
"v_mul_f32 v147,v147,v119\n"
"v_mul_f32 v148,v148,v119\n"
"v_mul_f32 v149,v149,v119\n"
"v_mul_f32 v150,v150,v119\n"
"v_mul_f32 v151,v151,v119\n"
"v_mul_f32 v152,v152,v119\n"
"v_mul_f32 v153,v153,v119\n"
"v_mul_f32 v154,v154,v119\n"
"v_mul_f32 v155,v155,v119\n"
"v_mul_f32 v156,v156,v119\n"
"v_mul_f32 v157,v157,v119\n"
"v_mul_f32 v158,v158,v119\n"
"v_mul_f32 v159,v159,v119\n"
"v_accvgpr_read_b32 v120,a128\n"
"v_mul_f32 v120,v120,v119\n"
"v_accvgpr_write_b32 a128,v120\n"
"v_lshlrev_b32 v120,16,v80\n"
"v_and_b32 v121,0xffff0000,v80\n"
"v_mul_f32 v120,v120,v119\n"
"v_mul_f32 v121,v121,v119\n"
"v_cvt_pk_bf16_f32 v80,v120,v121\n"
"v_accvgpr_read_b32 v120,a129\n"
"v_mul_f32 v120,v120,v119\n"
"v_accvgpr_write_b32 a129,v120\n"
"v_lshlrev_b32 v120,16,v81\n"
"v_and_b32 v121,0xffff0000,v81\n"
"v_mul_f32 v120,v120,v119\n"
"v_mul_f32 v121,v121,v119\n"
"v_cvt_pk_bf16_f32 v81,v120,v121\n"
"v_accvgpr_read_b32 v120,a130\n"
"v_mul_f32 v120,v120,v119\n"
"v_accvgpr_write_b32 a130,v120\n"
"v_lshlrev_b32 v120,16,v82\n"
"v_and_b32 v121,0xffff0000,v82\n"
"v_mul_f32 v120,v120,v119\n"
"v_mul_f32 v121,v121,v119\n"
"v_cvt_pk_bf16_f32 v82,v120,v121\n"
"v_accvgpr_read_b32 v120,a131\n"
"v_mul_f32 v120,v120,v119\n"
"v_accvgpr_write_b32 a131,v120\n"
"v_lshlrev_b32 v120,16,v83\n"
"v_and_b32 v121,0xffff0000,v83\n"
"v_mul_f32 v120,v120,v119\n"
"v_mul_f32 v121,v121,v119\n"
"v_cvt_pk_bf16_f32 v83,v120,v121\n"
".Lfk_rare0_0:\n"
"s_bitcmp1_b32 s91,1\n"
"s_cbranch_scc0 .Lfk_rare0_1\n"
"v_max3_f32 v119,v8,v9,v10\n"
"v_max3_f32 v120,v11,v12,v13\n"
"v_max3_f32 v119,v119,v14,v15\n"
"v_max_f32 v119,v119,v120\n"
"v_mov_b32 v120,v119\n"
"s_nop 1\n"
"v_permlane16_swap_b32 v120,v119\n"
"v_max_f32 v119,v119,v120\n"
"v_mov_b32 v120,v119\n"
"s_nop 1\n"
"v_permlane32_swap_b32 v120,v119\n"
"v_max_f32 v119,v119,v120\n"
"s_bitcmp1_b32 s90,1\n"
"s_cselect_b32 s82,0xff800000,0\n"
"v_add_f32 v119,0x41980000,v119\n"
"v_max_f32 v119,s82,v119\n"
"v_cmp_lt_f32 vcc,s17,|v119|\n"
"s_nop 0\n"
"s_cbranch_vccz .Lfk_rare0_1\n"
"v_ceil_f32 v119,v119\n"
"v_sub_f32 v8,v8,v119\n"
"v_sub_f32 v9,v9,v119\n"
"v_sub_f32 v10,v10,v119\n"
"v_sub_f32 v11,v11,v119\n"
"v_sub_f32 v12,v12,v119\n"
"v_sub_f32 v13,v13,v119\n"
"v_sub_f32 v14,v14,v119\n"
"v_sub_f32 v15,v15,v119\n"
"v_sub_f32 v100,v100,v119\n"
"v_sub_f32 v101,v101,v119\n"
"v_sub_f32 v102,v102,v119\n"
"v_sub_f32 v103,v103,v119\n"
"s_cbranch_scc1 .Lfk_rare0_1\n"
"v_exp_f32 v119,-v119\n"
"s_nop 0\n"
"v_mul_f32 v160,v160,v119\n"
"v_mul_f32 v161,v161,v119\n"
"v_mul_f32 v162,v162,v119\n"
"v_mul_f32 v163,v163,v119\n"
"v_mul_f32 v164,v164,v119\n"
"v_mul_f32 v165,v165,v119\n"
"v_mul_f32 v166,v166,v119\n"
"v_mul_f32 v167,v167,v119\n"
"v_mul_f32 v168,v168,v119\n"
"v_mul_f32 v169,v169,v119\n"
"v_mul_f32 v170,v170,v119\n"
"v_mul_f32 v171,v171,v119\n"
"v_mul_f32 v172,v172,v119\n"
"v_mul_f32 v173,v173,v119\n"
"v_mul_f32 v174,v174,v119\n"
"v_mul_f32 v175,v175,v119\n"
"v_mul_f32 v176,v176,v119\n"
"v_mul_f32 v177,v177,v119\n"
"v_mul_f32 v178,v178,v119\n"
"v_mul_f32 v179,v179,v119\n"
"v_mul_f32 v180,v180,v119\n"
"v_mul_f32 v181,v181,v119\n"
"v_mul_f32 v182,v182,v119\n"
"v_mul_f32 v183,v183,v119\n"
"v_mul_f32 v184,v184,v119\n"
"v_mul_f32 v185,v185,v119\n"
"v_mul_f32 v186,v186,v119\n"
"v_mul_f32 v187,v187,v119\n"
"v_mul_f32 v188,v188,v119\n"
"v_mul_f32 v189,v189,v119\n"
"v_mul_f32 v190,v190,v119\n"
"v_mul_f32 v191,v191,v119\n"
"v_accvgpr_read_b32 v120,a132\n"
"v_mul_f32 v120,v120,v119\n"
"v_accvgpr_write_b32 a132,v120\n"
"v_lshlrev_b32 v120,16,v84\n"
"v_and_b32 v121,0xffff0000,v84\n"
"v_mul_f32 v120,v120,v119\n"
"v_mul_f32 v121,v121,v119\n"
"v_cvt_pk_bf16_f32 v84,v120,v121\n"
"v_accvgpr_read_b32 v120,a133\n"
"v_mul_f32 v120,v120,v119\n"
"v_accvgpr_write_b32 a133,v120\n"
"v_lshlrev_b32 v120,16,v85\n"
"v_and_b32 v121,0xffff0000,v85\n"
"v_mul_f32 v120,v120,v119\n"
"v_mul_f32 v121,v121,v119\n"
"v_cvt_pk_bf16_f32 v85,v120,v121\n"
"v_accvgpr_read_b32 v120,a134\n"
"v_mul_f32 v120,v120,v119\n"
"v_accvgpr_write_b32 a134,v120\n"
"v_lshlrev_b32 v120,16,v86\n"
"v_and_b32 v121,0xffff0000,v86\n"
"v_mul_f32 v120,v120,v119\n"
"v_mul_f32 v121,v121,v119\n"
"v_cvt_pk_bf16_f32 v86,v120,v121\n"
"v_accvgpr_read_b32 v120,a135\n"
"v_mul_f32 v120,v120,v119\n"
"v_accvgpr_write_b32 a135,v120\n"
"v_lshlrev_b32 v120,16,v87\n"
"v_and_b32 v121,0xffff0000,v87\n"
"v_mul_f32 v120,v120,v119\n"
"v_mul_f32 v121,v121,v119\n"
"v_cvt_pk_bf16_f32 v87,v120,v121\n"
".Lfk_rare0_1:\n"
"s_bitcmp1_b32 s91,2\n"
"s_cbranch_scc0 .Lfk_rare0_2\n"
"v_max3_f32 v119,v16,v17,v18\n"
"v_max3_f32 v120,v19,v20,v21\n"
"v_max3_f32 v119,v119,v22,v23\n"
"v_max_f32 v119,v119,v120\n"
"v_mov_b32 v120,v119\n"
"s_nop 1\n"
"v_permlane16_swap_b32 v120,v119\n"
"v_max_f32 v119,v119,v120\n"
"v_mov_b32 v120,v119\n"
"s_nop 1\n"
"v_permlane32_swap_b32 v120,v119\n"
"v_max_f32 v119,v119,v120\n"
"s_bitcmp1_b32 s90,2\n"
"s_cselect_b32 s82,0xff800000,0\n"
"v_add_f32 v119,0x41980000,v119\n"
"v_max_f32 v119,s82,v119\n"
"v_cmp_lt_f32 vcc,s17,|v119|\n"
"s_nop 0\n"
"s_cbranch_vccz .Lfk_rare0_2\n"
"v_ceil_f32 v119,v119\n"
"v_sub_f32 v16,v16,v119\n"
"v_sub_f32 v17,v17,v119\n"
"v_sub_f32 v18,v18,v119\n"
"v_sub_f32 v19,v19,v119\n"
"v_sub_f32 v20,v20,v119\n"
"v_sub_f32 v21,v21,v119\n"
"v_sub_f32 v22,v22,v119\n"
"v_sub_f32 v23,v23,v119\n"
"v_sub_f32 v104,v104,v119\n"
"v_sub_f32 v105,v105,v119\n"
"v_sub_f32 v106,v106,v119\n"
"v_sub_f32 v107,v107,v119\n"
"s_cbranch_scc1 .Lfk_rare0_2\n"
"v_exp_f32 v119,-v119\n"
"s_nop 0\n"
"v_mul_f32 v192,v192,v119\n"
"v_mul_f32 v193,v193,v119\n"
"v_mul_f32 v194,v194,v119\n"
"v_mul_f32 v195,v195,v119\n"
"v_mul_f32 v196,v196,v119\n"
"v_mul_f32 v197,v197,v119\n"
"v_mul_f32 v198,v198,v119\n"
"v_mul_f32 v199,v199,v119\n"
"v_mul_f32 v200,v200,v119\n"
"v_mul_f32 v201,v201,v119\n"
"v_mul_f32 v202,v202,v119\n"
"v_mul_f32 v203,v203,v119\n"
"v_mul_f32 v204,v204,v119\n"
"v_mul_f32 v205,v205,v119\n"
"v_mul_f32 v206,v206,v119\n"
"v_mul_f32 v207,v207,v119\n"
"v_mul_f32 v208,v208,v119\n"
"v_mul_f32 v209,v209,v119\n"
"v_mul_f32 v210,v210,v119\n"
"v_mul_f32 v211,v211,v119\n"
"v_mul_f32 v212,v212,v119\n"
"v_mul_f32 v213,v213,v119\n"
"v_mul_f32 v214,v214,v119\n"
"v_mul_f32 v215,v215,v119\n"
"v_mul_f32 v216,v216,v119\n"
"v_mul_f32 v217,v217,v119\n"
"v_mul_f32 v218,v218,v119\n"
"v_mul_f32 v219,v219,v119\n"
"v_mul_f32 v220,v220,v119\n"
"v_mul_f32 v221,v221,v119\n"
"v_mul_f32 v222,v222,v119\n"
"v_mul_f32 v223,v223,v119\n"
"v_accvgpr_read_b32 v120,a136\n"
"v_mul_f32 v120,v120,v119\n"
"v_accvgpr_write_b32 a136,v120\n"
"v_lshlrev_b32 v120,16,v88\n"
"v_and_b32 v121,0xffff0000,v88\n"
"v_mul_f32 v120,v120,v119\n"
"v_mul_f32 v121,v121,v119\n"
"v_cvt_pk_bf16_f32 v88,v120,v121\n"
"v_accvgpr_read_b32 v120,a137\n"
"v_mul_f32 v120,v120,v119\n"
"v_accvgpr_write_b32 a137,v120\n"
"v_lshlrev_b32 v120,16,v89\n"
"v_and_b32 v121,0xffff0000,v89\n"
"v_mul_f32 v120,v120,v119\n"
"v_mul_f32 v121,v121,v119\n"
"v_cvt_pk_bf16_f32 v89,v120,v121\n"
"v_accvgpr_read_b32 v120,a138\n"
"v_mul_f32 v120,v120,v119\n"
"v_accvgpr_write_b32 a138,v120\n"
"v_lshlrev_b32 v120,16,v90\n"
"v_and_b32 v121,0xffff0000,v90\n"
"v_mul_f32 v120,v120,v119\n"
"v_mul_f32 v121,v121,v119\n"
"v_cvt_pk_bf16_f32 v90,v120,v121\n"
"v_accvgpr_read_b32 v120,a139\n"
"v_mul_f32 v120,v120,v119\n"
"v_accvgpr_write_b32 a139,v120\n"
"v_lshlrev_b32 v120,16,v91\n"
"v_and_b32 v121,0xffff0000,v91\n"
"v_mul_f32 v120,v120,v119\n"
"v_mul_f32 v121,v121,v119\n"
"v_cvt_pk_bf16_f32 v91,v120,v121\n"
".Lfk_rare0_2:\n"
"s_bitcmp1_b32 s91,3\n"
"s_cbranch_scc0 .Lfk_rare0_3\n"
"v_max3_f32 v119,v24,v25,v26\n"
"v_max3_f32 v120,v27,v28,v29\n"
"v_max3_f32 v119,v119,v30,v31\n"
"v_max_f32 v119,v119,v120\n"
"v_mov_b32 v120,v119\n"
"s_nop 1\n"
"v_permlane16_swap_b32 v120,v119\n"
"v_max_f32 v119,v119,v120\n"
"v_mov_b32 v120,v119\n"
"s_nop 1\n"
"v_permlane32_swap_b32 v120,v119\n"
"v_max_f32 v119,v119,v120\n"
"s_bitcmp1_b32 s90,3\n"
"s_cselect_b32 s82,0xff800000,0\n"
"v_add_f32 v119,0x41980000,v119\n"
"v_max_f32 v119,s82,v119\n"
"v_cmp_lt_f32 vcc,s17,|v119|\n"
"s_nop 0\n"
"s_cbranch_vccz .Lfk_rare0_3\n"
"v_ceil_f32 v119,v119\n"
"v_sub_f32 v24,v24,v119\n"
"v_sub_f32 v25,v25,v119\n"
"v_sub_f32 v26,v26,v119\n"
"v_sub_f32 v27,v27,v119\n"
"v_sub_f32 v28,v28,v119\n"
"v_sub_f32 v29,v29,v119\n"
"v_sub_f32 v30,v30,v119\n"
"v_sub_f32 v31,v31,v119\n"
"v_sub_f32 v108,v108,v119\n"
"v_sub_f32 v109,v109,v119\n"
"v_sub_f32 v110,v110,v119\n"
"v_sub_f32 v111,v111,v119\n"
"s_cbranch_scc1 .Lfk_rare0_3\n"
"v_exp_f32 v119,-v119\n"
"s_nop 0\n"
"v_mul_f32 v224,v224,v119\n"
"v_mul_f32 v225,v225,v119\n"
"v_mul_f32 v226,v226,v119\n"
"v_mul_f32 v227,v227,v119\n"
"v_mul_f32 v228,v228,v119\n"
"v_mul_f32 v229,v229,v119\n"
"v_mul_f32 v230,v230,v119\n"
"v_mul_f32 v231,v231,v119\n"
"v_mul_f32 v232,v232,v119\n"
"v_mul_f32 v233,v233,v119\n"
"v_mul_f32 v234,v234,v119\n"
"v_mul_f32 v235,v235,v119\n"
"v_mul_f32 v236,v236,v119\n"
"v_mul_f32 v237,v237,v119\n"
"v_mul_f32 v238,v238,v119\n"
"v_mul_f32 v239,v239,v119\n"
"v_mul_f32 v240,v240,v119\n"
"v_mul_f32 v241,v241,v119\n"
"v_mul_f32 v242,v242,v119\n"
"v_mul_f32 v243,v243,v119\n"
"v_mul_f32 v244,v244,v119\n"
"v_mul_f32 v245,v245,v119\n"
"v_mul_f32 v246,v246,v119\n"
"v_mul_f32 v247,v247,v119\n"
"v_mul_f32 v248,v248,v119\n"
"v_mul_f32 v249,v249,v119\n"
"v_mul_f32 v250,v250,v119\n"
"v_mul_f32 v251,v251,v119\n"
"v_mul_f32 v252,v252,v119\n"
"v_mul_f32 v253,v253,v119\n"
"v_mul_f32 v254,v254,v119\n"
"v_mul_f32 v255,v255,v119\n"
"v_accvgpr_read_b32 v120,a140\n"
"v_mul_f32 v120,v120,v119\n"
"v_accvgpr_write_b32 a140,v120\n"
"v_lshlrev_b32 v120,16,v92\n"
"v_and_b32 v121,0xffff0000,v92\n"
"v_mul_f32 v120,v120,v119\n"
"v_mul_f32 v121,v121,v119\n"
"v_cvt_pk_bf16_f32 v92,v120,v121\n"
"v_accvgpr_read_b32 v120,a141\n"
"v_mul_f32 v120,v120,v119\n"
"v_accvgpr_write_b32 a141,v120\n"
"v_lshlrev_b32 v120,16,v93\n"
"v_and_b32 v121,0xffff0000,v93\n"
"v_mul_f32 v120,v120,v119\n"
"v_mul_f32 v121,v121,v119\n"
"v_cvt_pk_bf16_f32 v93,v120,v121\n"
"v_accvgpr_read_b32 v120,a142\n"
"v_mul_f32 v120,v120,v119\n"
"v_accvgpr_write_b32 a142,v120\n"
"v_lshlrev_b32 v120,16,v94\n"
"v_and_b32 v121,0xffff0000,v94\n"
"v_mul_f32 v120,v120,v119\n"
"v_mul_f32 v121,v121,v119\n"
"v_cvt_pk_bf16_f32 v94,v120,v121\n"
"v_accvgpr_read_b32 v120,a143\n"
"v_mul_f32 v120,v120,v119\n"
"v_accvgpr_write_b32 a143,v120\n"
"v_lshlrev_b32 v120,16,v95\n"
"v_and_b32 v121,0xffff0000,v95\n"
"v_mul_f32 v120,v120,v119\n"
"v_mul_f32 v121,v121,v119\n"
"v_cvt_pk_bf16_f32 v95,v120,v121\n"
".Lfk_rare0_3:\n"
"s_mov_b32 s90,0\n"
"s_nop 1\n"
"s_setpc_b64 s[94:95]\n"
".Lfk_rare2:\n"
"s_nop 7\n"
"v_max3_f32 v119,v0,v1,v2\n"
"v_max3_f32 v120,v3,v4,v5\n"
"v_max3_f32 v119,v119,v6,v7\n"
"v_max_f32 v119,v119,v120\n"
"v_mov_b32 v120,v119\n"
"s_nop 1\n"
"v_permlane16_swap_b32 v120,v119\n"
"v_max_f32 v119,v119,v120\n"
"v_mov_b32 v120,v119\n"
"s_nop 1\n"
"v_permlane32_swap_b32 v120,v119\n"
"v_max_f32 v119,v119,v120\n"
"s_bitcmp1_b32 s90,0\n"
"s_cselect_b32 s82,0xff800000,0\n"
"v_add_f32 v119,0x41980000,v119\n"
"v_max_f32 v119,s82,v119\n"
"v_cmp_lt_f32 vcc,s17,|v119|\n"
"s_nop 0\n"
"s_cbranch_vccz .Lfk_rare2_0\n"
"v_ceil_f32 v119,v119\n"
"v_sub_f32 v0,v0,v119\n"
"v_sub_f32 v1,v1,v119\n"
"v_sub_f32 v2,v2,v119\n"
"v_sub_f32 v3,v3,v119\n"
"v_sub_f32 v4,v4,v119\n"
"v_sub_f32 v5,v5,v119\n"
"v_sub_f32 v6,v6,v119\n"
"v_sub_f32 v7,v7,v119\n"
"v_sub_f32 v96,v96,v119\n"
"v_sub_f32 v97,v97,v119\n"
"v_sub_f32 v98,v98,v119\n"
"v_sub_f32 v99,v99,v119\n"
"v_sub_f32 v32,v32,v119\n"
"v_sub_f32 v33,v33,v119\n"
"v_sub_f32 v34,v34,v119\n"
"v_sub_f32 v35,v35,v119\n"
"v_sub_f32 v36,v36,v119\n"
"v_sub_f32 v37,v37,v119\n"
"v_sub_f32 v38,v38,v119\n"
"v_sub_f32 v39,v39,v119\n"
"v_exp_f32 v0,v0\n"
"v_exp_f32 v1,v1\n"
"v_exp_f32 v2,v2\n"
"v_exp_f32 v3,v3\n"
"v_exp_f32 v4,v4\n"
"v_exp_f32 v5,v5\n"
"v_exp_f32 v6,v6\n"
"v_exp_f32 v7,v7\n"
"v_cvt_pk_bf16_f32 v64,v0,v1\n"
"v_cvt_pk_bf16_f32 v65,v2,v3\n"
"v_cvt_pk_bf16_f32 v66,v4,v5\n"
"v_cvt_pk_bf16_f32 v67,v6,v7\n"
"s_cbranch_scc1 .Lfk_rare2_0\n"
"v_exp_f32 v119,-v119\n"
"s_nop 0\n"
"v_mul_f32 v128,v128,v119\n"
"v_mul_f32 v129,v129,v119\n"
"v_mul_f32 v130,v130,v119\n"
"v_mul_f32 v131,v131,v119\n"
"v_mul_f32 v132,v132,v119\n"
"v_mul_f32 v133,v133,v119\n"
"v_mul_f32 v134,v134,v119\n"
"v_mul_f32 v135,v135,v119\n"
"v_mul_f32 v136,v136,v119\n"
"v_mul_f32 v137,v137,v119\n"
"v_mul_f32 v138,v138,v119\n"
"v_mul_f32 v139,v139,v119\n"
"v_mul_f32 v140,v140,v119\n"
"v_mul_f32 v141,v141,v119\n"
"v_mul_f32 v142,v142,v119\n"
"v_mul_f32 v143,v143,v119\n"
"v_mul_f32 v144,v144,v119\n"
"v_mul_f32 v145,v145,v119\n"
"v_mul_f32 v146,v146,v119\n"
"v_mul_f32 v147,v147,v119\n"
"v_mul_f32 v148,v148,v119\n"
"v_mul_f32 v149,v149,v119\n"
"v_mul_f32 v150,v150,v119\n"
"v_mul_f32 v151,v151,v119\n"
"v_mul_f32 v152,v152,v119\n"
"v_mul_f32 v153,v153,v119\n"
"v_mul_f32 v154,v154,v119\n"
"v_mul_f32 v155,v155,v119\n"
"v_mul_f32 v156,v156,v119\n"
"v_mul_f32 v157,v157,v119\n"
"v_mul_f32 v158,v158,v119\n"
"v_mul_f32 v159,v159,v119\n"
"v_accvgpr_read_b32 v120,a128\n"
"v_mul_f32 v120,v120,v119\n"
"v_accvgpr_write_b32 a128,v120\n"
"v_accvgpr_read_b32 v120,a129\n"
"v_mul_f32 v120,v120,v119\n"
"v_accvgpr_write_b32 a129,v120\n"
"v_accvgpr_read_b32 v120,a130\n"
"v_mul_f32 v120,v120,v119\n"
"v_accvgpr_write_b32 a130,v120\n"
"v_accvgpr_read_b32 v120,a131\n"
"v_mul_f32 v120,v120,v119\n"
"v_accvgpr_write_b32 a131,v120\n"
".Lfk_rare2_0:\n"
"v_max3_f32 v119,v8,v9,v10\n"
"v_max3_f32 v120,v11,v12,v13\n"
"v_max3_f32 v119,v119,v14,v15\n"
"v_max_f32 v119,v119,v120\n"
"v_mov_b32 v120,v119\n"
"s_nop 1\n"
"v_permlane16_swap_b32 v120,v119\n"
"v_max_f32 v119,v119,v120\n"
"v_mov_b32 v120,v119\n"
"s_nop 1\n"
"v_permlane32_swap_b32 v120,v119\n"
"v_max_f32 v119,v119,v120\n"
"s_bitcmp1_b32 s90,1\n"
"s_cselect_b32 s82,0xff800000,0\n"
"v_add_f32 v119,0x41980000,v119\n"
"v_max_f32 v119,s82,v119\n"
"v_cmp_lt_f32 vcc,s17,|v119|\n"
"s_nop 0\n"
"s_cbranch_vccz .Lfk_rare2_1\n"
"v_ceil_f32 v119,v119\n"
"v_sub_f32 v8,v8,v119\n"
"v_sub_f32 v9,v9,v119\n"
"v_sub_f32 v10,v10,v119\n"
"v_sub_f32 v11,v11,v119\n"
"v_sub_f32 v12,v12,v119\n"
"v_sub_f32 v13,v13,v119\n"
"v_sub_f32 v14,v14,v119\n"
"v_sub_f32 v15,v15,v119\n"
"v_sub_f32 v100,v100,v119\n"
"v_sub_f32 v101,v101,v119\n"
"v_sub_f32 v102,v102,v119\n"
"v_sub_f32 v103,v103,v119\n"
"v_sub_f32 v40,v40,v119\n"
"v_sub_f32 v41,v41,v119\n"
"v_sub_f32 v42,v42,v119\n"
"v_sub_f32 v43,v43,v119\n"
"v_sub_f32 v44,v44,v119\n"
"v_sub_f32 v45,v45,v119\n"
"v_sub_f32 v46,v46,v119\n"
"v_sub_f32 v47,v47,v119\n"
"v_exp_f32 v8,v8\n"
"v_exp_f32 v9,v9\n"
"v_exp_f32 v10,v10\n"
"v_exp_f32 v11,v11\n"
"v_exp_f32 v12,v12\n"
"v_exp_f32 v13,v13\n"
"v_exp_f32 v14,v14\n"
"v_exp_f32 v15,v15\n"
"v_cvt_pk_bf16_f32 v68,v8,v9\n"
"v_cvt_pk_bf16_f32 v69,v10,v11\n"
"v_cvt_pk_bf16_f32 v70,v12,v13\n"
"v_cvt_pk_bf16_f32 v71,v14,v15\n"
"s_cbranch_scc1 .Lfk_rare2_1\n"
"v_exp_f32 v119,-v119\n"
"s_nop 0\n"
"v_mul_f32 v160,v160,v119\n"
"v_mul_f32 v161,v161,v119\n"
"v_mul_f32 v162,v162,v119\n"
"v_mul_f32 v163,v163,v119\n"
"v_mul_f32 v164,v164,v119\n"
"v_mul_f32 v165,v165,v119\n"
"v_mul_f32 v166,v166,v119\n"
"v_mul_f32 v167,v167,v119\n"
"v_mul_f32 v168,v168,v119\n"
"v_mul_f32 v169,v169,v119\n"
"v_mul_f32 v170,v170,v119\n"
"v_mul_f32 v171,v171,v119\n"
"v_mul_f32 v172,v172,v119\n"
"v_mul_f32 v173,v173,v119\n"
"v_mul_f32 v174,v174,v119\n"
"v_mul_f32 v175,v175,v119\n"
"v_mul_f32 v176,v176,v119\n"
"v_mul_f32 v177,v177,v119\n"
"v_mul_f32 v178,v178,v119\n"
"v_mul_f32 v179,v179,v119\n"
"v_mul_f32 v180,v180,v119\n"
"v_mul_f32 v181,v181,v119\n"
"v_mul_f32 v182,v182,v119\n"
"v_mul_f32 v183,v183,v119\n"
"v_mul_f32 v184,v184,v119\n"
"v_mul_f32 v185,v185,v119\n"
"v_mul_f32 v186,v186,v119\n"
"v_mul_f32 v187,v187,v119\n"
"v_mul_f32 v188,v188,v119\n"
"v_mul_f32 v189,v189,v119\n"
"v_mul_f32 v190,v190,v119\n"
"v_mul_f32 v191,v191,v119\n"
"v_accvgpr_read_b32 v120,a132\n"
"v_mul_f32 v120,v120,v119\n"
"v_accvgpr_write_b32 a132,v120\n"
"v_accvgpr_read_b32 v120,a133\n"
"v_mul_f32 v120,v120,v119\n"
"v_accvgpr_write_b32 a133,v120\n"
"v_accvgpr_read_b32 v120,a134\n"
"v_mul_f32 v120,v120,v119\n"
"v_accvgpr_write_b32 a134,v120\n"
"v_accvgpr_read_b32 v120,a135\n"
"v_mul_f32 v120,v120,v119\n"
"v_accvgpr_write_b32 a135,v120\n"
".Lfk_rare2_1:\n"
"v_max3_f32 v119,v16,v17,v18\n"
"v_max3_f32 v120,v19,v20,v21\n"
"v_max3_f32 v119,v119,v22,v23\n"
"v_max_f32 v119,v119,v120\n"
"v_mov_b32 v120,v119\n"
"s_nop 1\n"
"v_permlane16_swap_b32 v120,v119\n"
"v_max_f32 v119,v119,v120\n"
"v_mov_b32 v120,v119\n"
"s_nop 1\n"
"v_permlane32_swap_b32 v120,v119\n"
"v_max_f32 v119,v119,v120\n"
"s_bitcmp1_b32 s90,2\n"
"s_cselect_b32 s82,0xff800000,0\n"
"v_add_f32 v119,0x41980000,v119\n"
"v_max_f32 v119,s82,v119\n"
"v_cmp_lt_f32 vcc,s17,|v119|\n"
"s_nop 0\n"
"s_cbranch_vccz .Lfk_rare2_2\n"
"v_ceil_f32 v119,v119\n"
"v_sub_f32 v16,v16,v119\n"
"v_sub_f32 v17,v17,v119\n"
"v_sub_f32 v18,v18,v119\n"
"v_sub_f32 v19,v19,v119\n"
"v_sub_f32 v20,v20,v119\n"
"v_sub_f32 v21,v21,v119\n"
"v_sub_f32 v22,v22,v119\n"
"v_sub_f32 v23,v23,v119\n"
"v_sub_f32 v104,v104,v119\n"
"v_sub_f32 v105,v105,v119\n"
"v_sub_f32 v106,v106,v119\n"
"v_sub_f32 v107,v107,v119\n"
"v_sub_f32 v48,v48,v119\n"
"v_sub_f32 v49,v49,v119\n"
"v_sub_f32 v50,v50,v119\n"
"v_sub_f32 v51,v51,v119\n"
"v_sub_f32 v52,v52,v119\n"
"v_sub_f32 v53,v53,v119\n"
"v_sub_f32 v54,v54,v119\n"
"v_sub_f32 v55,v55,v119\n"
"v_exp_f32 v16,v16\n"
"v_exp_f32 v17,v17\n"
"v_exp_f32 v18,v18\n"
"v_exp_f32 v19,v19\n"
"v_exp_f32 v20,v20\n"
"v_exp_f32 v21,v21\n"
"v_exp_f32 v22,v22\n"
"v_exp_f32 v23,v23\n"
"v_cvt_pk_bf16_f32 v72,v16,v17\n"
"v_cvt_pk_bf16_f32 v73,v18,v19\n"
"v_cvt_pk_bf16_f32 v74,v20,v21\n"
"v_cvt_pk_bf16_f32 v75,v22,v23\n"
"s_cbranch_scc1 .Lfk_rare2_2\n"
"v_exp_f32 v119,-v119\n"
"s_nop 0\n"
"v_mul_f32 v192,v192,v119\n"
"v_mul_f32 v193,v193,v119\n"
"v_mul_f32 v194,v194,v119\n"
"v_mul_f32 v195,v195,v119\n"
"v_mul_f32 v196,v196,v119\n"
"v_mul_f32 v197,v197,v119\n"
"v_mul_f32 v198,v198,v119\n"
"v_mul_f32 v199,v199,v119\n"
"v_mul_f32 v200,v200,v119\n"
"v_mul_f32 v201,v201,v119\n"
"v_mul_f32 v202,v202,v119\n"
"v_mul_f32 v203,v203,v119\n"
"v_mul_f32 v204,v204,v119\n"
"v_mul_f32 v205,v205,v119\n"
"v_mul_f32 v206,v206,v119\n"
"v_mul_f32 v207,v207,v119\n"
"v_mul_f32 v208,v208,v119\n"
"v_mul_f32 v209,v209,v119\n"
"v_mul_f32 v210,v210,v119\n"
"v_mul_f32 v211,v211,v119\n"
"v_mul_f32 v212,v212,v119\n"
"v_mul_f32 v213,v213,v119\n"
"v_mul_f32 v214,v214,v119\n"
"v_mul_f32 v215,v215,v119\n"
"v_mul_f32 v216,v216,v119\n"
"v_mul_f32 v217,v217,v119\n"
"v_mul_f32 v218,v218,v119\n"
"v_mul_f32 v219,v219,v119\n"
"v_mul_f32 v220,v220,v119\n"
"v_mul_f32 v221,v221,v119\n"
"v_mul_f32 v222,v222,v119\n"
"v_mul_f32 v223,v223,v119\n"
"v_accvgpr_read_b32 v120,a136\n"
"v_mul_f32 v120,v120,v119\n"
"v_accvgpr_write_b32 a136,v120\n"
"v_accvgpr_read_b32 v120,a137\n"
"v_mul_f32 v120,v120,v119\n"
"v_accvgpr_write_b32 a137,v120\n"
"v_accvgpr_read_b32 v120,a138\n"
"v_mul_f32 v120,v120,v119\n"
"v_accvgpr_write_b32 a138,v120\n"
"v_accvgpr_read_b32 v120,a139\n"
"v_mul_f32 v120,v120,v119\n"
"v_accvgpr_write_b32 a139,v120\n"
".Lfk_rare2_2:\n"
"v_max3_f32 v119,v24,v25,v26\n"
"v_max3_f32 v120,v27,v28,v29\n"
"v_max3_f32 v119,v119,v30,v31\n"
"v_max_f32 v119,v119,v120\n"
"v_mov_b32 v120,v119\n"
"s_nop 1\n"
"v_permlane16_swap_b32 v120,v119\n"
"v_max_f32 v119,v119,v120\n"
"v_mov_b32 v120,v119\n"
"s_nop 1\n"
"v_permlane32_swap_b32 v120,v119\n"
"v_max_f32 v119,v119,v120\n"
"s_bitcmp1_b32 s90,3\n"
"s_cselect_b32 s82,0xff800000,0\n"
"v_add_f32 v119,0x41980000,v119\n"
"v_max_f32 v119,s82,v119\n"
"v_cmp_lt_f32 vcc,s17,|v119|\n"
"s_nop 0\n"
"s_cbranch_vccz .Lfk_rare2_3\n"
"v_ceil_f32 v119,v119\n"
"v_sub_f32 v24,v24,v119\n"
"v_sub_f32 v25,v25,v119\n"
"v_sub_f32 v26,v26,v119\n"
"v_sub_f32 v27,v27,v119\n"
"v_sub_f32 v28,v28,v119\n"
"v_sub_f32 v29,v29,v119\n"
"v_sub_f32 v30,v30,v119\n"
"v_sub_f32 v31,v31,v119\n"
"v_sub_f32 v108,v108,v119\n"
"v_sub_f32 v109,v109,v119\n"
"v_sub_f32 v110,v110,v119\n"
"v_sub_f32 v111,v111,v119\n"
"v_sub_f32 v56,v56,v119\n"
"v_sub_f32 v57,v57,v119\n"
"v_sub_f32 v58,v58,v119\n"
"v_sub_f32 v59,v59,v119\n"
"v_sub_f32 v60,v60,v119\n"
"v_sub_f32 v61,v61,v119\n"
"v_sub_f32 v62,v62,v119\n"
"v_sub_f32 v63,v63,v119\n"
"v_exp_f32 v24,v24\n"
"v_exp_f32 v25,v25\n"
"v_exp_f32 v26,v26\n"
"v_exp_f32 v27,v27\n"
"v_exp_f32 v28,v28\n"
"v_exp_f32 v29,v29\n"
"v_exp_f32 v30,v30\n"
"v_exp_f32 v31,v31\n"
"v_cvt_pk_bf16_f32 v76,v24,v25\n"
"v_cvt_pk_bf16_f32 v77,v26,v27\n"
"v_cvt_pk_bf16_f32 v78,v28,v29\n"
"v_cvt_pk_bf16_f32 v79,v30,v31\n"
"s_cbranch_scc1 .Lfk_rare2_3\n"
"v_exp_f32 v119,-v119\n"
"s_nop 0\n"
"v_mul_f32 v224,v224,v119\n"
"v_mul_f32 v225,v225,v119\n"
"v_mul_f32 v226,v226,v119\n"
"v_mul_f32 v227,v227,v119\n"
"v_mul_f32 v228,v228,v119\n"
"v_mul_f32 v229,v229,v119\n"
"v_mul_f32 v230,v230,v119\n"
"v_mul_f32 v231,v231,v119\n"
"v_mul_f32 v232,v232,v119\n"
"v_mul_f32 v233,v233,v119\n"
"v_mul_f32 v234,v234,v119\n"
"v_mul_f32 v235,v235,v119\n"
"v_mul_f32 v236,v236,v119\n"
"v_mul_f32 v237,v237,v119\n"
"v_mul_f32 v238,v238,v119\n"
"v_mul_f32 v239,v239,v119\n"
"v_mul_f32 v240,v240,v119\n"
"v_mul_f32 v241,v241,v119\n"
"v_mul_f32 v242,v242,v119\n"
"v_mul_f32 v243,v243,v119\n"
"v_mul_f32 v244,v244,v119\n"
"v_mul_f32 v245,v245,v119\n"
"v_mul_f32 v246,v246,v119\n"
"v_mul_f32 v247,v247,v119\n"
"v_mul_f32 v248,v248,v119\n"
"v_mul_f32 v249,v249,v119\n"
"v_mul_f32 v250,v250,v119\n"
"v_mul_f32 v251,v251,v119\n"
"v_mul_f32 v252,v252,v119\n"
"v_mul_f32 v253,v253,v119\n"
"v_mul_f32 v254,v254,v119\n"
"v_mul_f32 v255,v255,v119\n"
"v_accvgpr_read_b32 v120,a140\n"
"v_mul_f32 v120,v120,v119\n"
"v_accvgpr_write_b32 a140,v120\n"
"v_accvgpr_read_b32 v120,a141\n"
"v_mul_f32 v120,v120,v119\n"
"v_accvgpr_write_b32 a141,v120\n"
"v_accvgpr_read_b32 v120,a142\n"
"v_mul_f32 v120,v120,v119\n"
"v_accvgpr_write_b32 a142,v120\n"
"v_accvgpr_read_b32 v120,a143\n"
"v_mul_f32 v120,v120,v119\n"
"v_accvgpr_write_b32 a143,v120\n"
".Lfk_rare2_3:\n"
"s_mov_b32 s90,0\n"
"s_nop 1\n"
"s_setpc_b64 s[94:95]\n"
".Lfk_mslow0:\n"
"s_nop 7\n"
"v_min_f32 v119,v98,v104\n"
"v_sub_f32 v120,v119,v104\n"
"v_sub_f32 v119,v119,v98\n"
"v_exp_f32 v120,v120\n"
"v_exp_f32 v119,v119\n"
"v_mul_f32 v192,v192,v120\n"
"v_mul_f32 v193,v193,v120\n"
"v_mul_f32 v194,v194,v120\n"
"v_mul_f32 v195,v195,v120\n"
"v_mul_f32 v196,v196,v120\n"
"v_mul_f32 v197,v197,v120\n"
"v_mul_f32 v198,v198,v120\n"
"v_mul_f32 v199,v199,v120\n"
"v_mul_f32 v200,v200,v120\n"
"v_mul_f32 v201,v201,v120\n"
"v_mul_f32 v202,v202,v120\n"
"v_mul_f32 v203,v203,v120\n"
"v_mul_f32 v204,v204,v120\n"
"v_mul_f32 v205,v205,v120\n"
"v_mul_f32 v206,v206,v120\n"
"v_mul_f32 v207,v207,v120\n"
"v_mul_f32 v208,v208,v120\n"
"v_mul_f32 v209,v209,v120\n"
"v_mul_f32 v210,v210,v120\n"
"v_mul_f32 v211,v211,v120\n"
"v_mul_f32 v212,v212,v120\n"
"v_mul_f32 v213,v213,v120\n"
"v_mul_f32 v214,v214,v120\n"
"v_mul_f32 v215,v215,v120\n"
"v_mul_f32 v216,v216,v120\n"
"v_mul_f32 v217,v217,v120\n"
"v_mul_f32 v218,v218,v120\n"
"v_mul_f32 v219,v219,v120\n"
"v_mul_f32 v220,v220,v120\n"
"v_mul_f32 v221,v221,v120\n"
"v_mul_f32 v222,v222,v120\n"
"v_mul_f32 v223,v223,v120\n"
"v_accvgpr_read_b32 v121,a136\n"
"v_mul_f32 v121,v121,v120\n"
"v_accvgpr_write_b32 a136,v121\n"
"v_mul_f32 v99,v99,v119\n"
"v_accvgpr_read_b32 v121,a64\n"
"v_lshlrev_b32 v120,16,v121\n"
"v_and_b32 v121,0xffff0000,v121\n"
"v_mul_f32 v120,v120,v119\n"
"v_mul_f32 v121,v121,v119\n"
"v_cvt_pk_bf16_f32 v121,v120,v121\n"
"v_accvgpr_write_b32 a64,v121\n"
"v_accvgpr_read_b32 v121,a65\n"
"v_lshlrev_b32 v120,16,v121\n"
"v_and_b32 v121,0xffff0000,v121\n"
"v_mul_f32 v120,v120,v119\n"
"v_mul_f32 v121,v121,v119\n"
"v_cvt_pk_bf16_f32 v121,v120,v121\n"
"v_accvgpr_write_b32 a65,v121\n"
"v_accvgpr_read_b32 v121,a66\n"
"v_lshlrev_b32 v120,16,v121\n"
"v_and_b32 v121,0xffff0000,v121\n"
"v_mul_f32 v120,v120,v119\n"
"v_mul_f32 v121,v121,v119\n"
"v_cvt_pk_bf16_f32 v121,v120,v121\n"
"v_accvgpr_write_b32 a66,v121\n"
"v_accvgpr_read_b32 v121,a67\n"
"v_lshlrev_b32 v120,16,v121\n"
"v_and_b32 v121,0xffff0000,v121\n"
"v_mul_f32 v120,v120,v119\n"
"v_mul_f32 v121,v121,v119\n"
"v_cvt_pk_bf16_f32 v121,v120,v121\n"
"v_accvgpr_write_b32 a67,v121\n"
"v_accvgpr_read_b32 v121,a68\n"
"v_lshlrev_b32 v120,16,v121\n"
"v_and_b32 v121,0xffff0000,v121\n"
"v_mul_f32 v120,v120,v119\n"
"v_mul_f32 v121,v121,v119\n"
"v_cvt_pk_bf16_f32 v121,v120,v121\n"
"v_accvgpr_write_b32 a68,v121\n"
"v_accvgpr_read_b32 v121,a69\n"
"v_lshlrev_b32 v120,16,v121\n"
"v_and_b32 v121,0xffff0000,v121\n"
"v_mul_f32 v120,v120,v119\n"
"v_mul_f32 v121,v121,v119\n"
"v_cvt_pk_bf16_f32 v121,v120,v121\n"
"v_accvgpr_write_b32 a69,v121\n"
"v_accvgpr_read_b32 v121,a70\n"
"v_lshlrev_b32 v120,16,v121\n"
"v_and_b32 v121,0xffff0000,v121\n"
"v_mul_f32 v120,v120,v119\n"
"v_mul_f32 v121,v121,v119\n"
"v_cvt_pk_bf16_f32 v121,v120,v121\n"
"v_accvgpr_write_b32 a70,v121\n"
"v_accvgpr_read_b32 v121,a71\n"
"v_lshlrev_b32 v120,16,v121\n"
"v_and_b32 v121,0xffff0000,v121\n"
"v_mul_f32 v120,v120,v119\n"
"v_mul_f32 v121,v121,v119\n"
"v_cvt_pk_bf16_f32 v121,v120,v121\n"
"v_accvgpr_write_b32 a71,v121\n"
"v_accvgpr_read_b32 v121,a72\n"
"v_lshlrev_b32 v120,16,v121\n"
"v_and_b32 v121,0xffff0000,v121\n"
"v_mul_f32 v120,v120,v119\n"
"v_mul_f32 v121,v121,v119\n"
"v_cvt_pk_bf16_f32 v121,v120,v121\n"
"v_accvgpr_write_b32 a72,v121\n"
"v_accvgpr_read_b32 v121,a73\n"
"v_lshlrev_b32 v120,16,v121\n"
"v_and_b32 v121,0xffff0000,v121\n"
"v_mul_f32 v120,v120,v119\n"
"v_mul_f32 v121,v121,v119\n"
"v_cvt_pk_bf16_f32 v121,v120,v121\n"
"v_accvgpr_write_b32 a73,v121\n"
"v_accvgpr_read_b32 v121,a74\n"
"v_lshlrev_b32 v120,16,v121\n"
"v_and_b32 v121,0xffff0000,v121\n"
"v_mul_f32 v120,v120,v119\n"
"v_mul_f32 v121,v121,v119\n"
"v_cvt_pk_bf16_f32 v121,v120,v121\n"
"v_accvgpr_write_b32 a74,v121\n"
"v_accvgpr_read_b32 v121,a75\n"
"v_lshlrev_b32 v120,16,v121\n"
"v_and_b32 v121,0xffff0000,v121\n"
"v_mul_f32 v120,v120,v119\n"
"v_mul_f32 v121,v121,v119\n"
"v_cvt_pk_bf16_f32 v121,v120,v121\n"
"v_accvgpr_write_b32 a75,v121\n"
"v_accvgpr_read_b32 v121,a76\n"
"v_lshlrev_b32 v120,16,v121\n"
"v_and_b32 v121,0xffff0000,v121\n"
"v_mul_f32 v120,v120,v119\n"
"v_mul_f32 v121,v121,v119\n"
"v_cvt_pk_bf16_f32 v121,v120,v121\n"
"v_accvgpr_write_b32 a76,v121\n"
"v_accvgpr_read_b32 v121,a77\n"
"v_lshlrev_b32 v120,16,v121\n"
"v_and_b32 v121,0xffff0000,v121\n"
"v_mul_f32 v120,v120,v119\n"
"v_mul_f32 v121,v121,v119\n"
"v_cvt_pk_bf16_f32 v121,v120,v121\n"
"v_accvgpr_write_b32 a77,v121\n"
"v_accvgpr_read_b32 v121,a78\n"
"v_lshlrev_b32 v120,16,v121\n"
"v_and_b32 v121,0xffff0000,v121\n"
"v_mul_f32 v120,v120,v119\n"
"v_mul_f32 v121,v121,v119\n"
"v_cvt_pk_bf16_f32 v121,v120,v121\n"
"v_accvgpr_write_b32 a78,v121\n"
"v_accvgpr_read_b32 v121,a79\n"
"v_lshlrev_b32 v120,16,v121\n"
"v_and_b32 v121,0xffff0000,v121\n"
"v_mul_f32 v120,v120,v119\n"
"v_mul_f32 v121,v121,v119\n"
"v_cvt_pk_bf16_f32 v121,v120,v121\n"
"v_accvgpr_write_b32 a79,v121\n"
"s_nop 1\n"
"s_setpc_b64 s[94:95]\n"
".Lfk_rare1:\n"
"s_nop 7\n"
"s_bitcmp1_b32 s91,0\n"
"s_cbranch_scc0 .Lfk_rare1_0\n"
"v_max3_f32 v119,v32,v33,v34\n"
"v_max3_f32 v120,v35,v36,v37\n"
"v_max3_f32 v119,v119,v38,v39\n"
"v_max_f32 v119,v119,v120\n"
"v_mov_b32 v120,v119\n"
"s_nop 1\n"
"v_permlane16_swap_b32 v120,v119\n"
"v_max_f32 v119,v119,v120\n"
"v_mov_b32 v120,v119\n"
"s_nop 1\n"
"v_permlane32_swap_b32 v120,v119\n"
"v_max_f32 v119,v119,v120\n"
"s_bitcmp1_b32 s90,0\n"
"s_cselect_b32 s82,0xff800000,0\n"
"v_add_f32 v119,0x41980000,v119\n"
"v_max_f32 v119,s82,v119\n"
"v_cmp_lt_f32 vcc,s17,|v119|\n"
"s_nop 0\n"
"s_cbranch_vccz .Lfk_rare1_0\n"
"v_ceil_f32 v119,v119\n"
"v_sub_f32 v32,v32,v119\n"
"v_sub_f32 v33,v33,v119\n"
"v_sub_f32 v34,v34,v119\n"
"v_sub_f32 v35,v35,v119\n"
"v_sub_f32 v36,v36,v119\n"
"v_sub_f32 v37,v37,v119\n"
"v_sub_f32 v38,v38,v119\n"
"v_sub_f32 v39,v39,v119\n"
"v_sub_f32 v96,v96,v119\n"
"v_sub_f32 v97,v97,v119\n"
"v_sub_f32 v98,v98,v119\n"
"v_sub_f32 v99,v99,v119\n"
"s_cbranch_scc1 .Lfk_rare1_0\n"
"v_exp_f32 v119,-v119\n"
"s_nop 0\n"
"v_mul_f32 v128,v128,v119\n"
"v_mul_f32 v129,v129,v119\n"
"v_mul_f32 v130,v130,v119\n"
"v_mul_f32 v131,v131,v119\n"
"v_mul_f32 v132,v132,v119\n"
"v_mul_f32 v133,v133,v119\n"
"v_mul_f32 v134,v134,v119\n"
"v_mul_f32 v135,v135,v119\n"
"v_mul_f32 v136,v136,v119\n"
"v_mul_f32 v137,v137,v119\n"
"v_mul_f32 v138,v138,v119\n"
"v_mul_f32 v139,v139,v119\n"
"v_mul_f32 v140,v140,v119\n"
"v_mul_f32 v141,v141,v119\n"
"v_mul_f32 v142,v142,v119\n"
"v_mul_f32 v143,v143,v119\n"
"v_mul_f32 v144,v144,v119\n"
"v_mul_f32 v145,v145,v119\n"
"v_mul_f32 v146,v146,v119\n"
"v_mul_f32 v147,v147,v119\n"
"v_mul_f32 v148,v148,v119\n"
"v_mul_f32 v149,v149,v119\n"
"v_mul_f32 v150,v150,v119\n"
"v_mul_f32 v151,v151,v119\n"
"v_mul_f32 v152,v152,v119\n"
"v_mul_f32 v153,v153,v119\n"
"v_mul_f32 v154,v154,v119\n"
"v_mul_f32 v155,v155,v119\n"
"v_mul_f32 v156,v156,v119\n"
"v_mul_f32 v157,v157,v119\n"
"v_mul_f32 v158,v158,v119\n"
"v_mul_f32 v159,v159,v119\n"
"v_accvgpr_read_b32 v120,a128\n"
"v_mul_f32 v120,v120,v119\n"
"v_accvgpr_write_b32 a128,v120\n"
"v_lshlrev_b32 v120,16,v64\n"
"v_and_b32 v121,0xffff0000,v64\n"
"v_mul_f32 v120,v120,v119\n"
"v_mul_f32 v121,v121,v119\n"
"v_cvt_pk_bf16_f32 v64,v120,v121\n"
"v_accvgpr_read_b32 v120,a129\n"
"v_mul_f32 v120,v120,v119\n"
"v_accvgpr_write_b32 a129,v120\n"
"v_lshlrev_b32 v120,16,v65\n"
"v_and_b32 v121,0xffff0000,v65\n"
"v_mul_f32 v120,v120,v119\n"
"v_mul_f32 v121,v121,v119\n"
"v_cvt_pk_bf16_f32 v65,v120,v121\n"
"v_accvgpr_read_b32 v120,a130\n"
"v_mul_f32 v120,v120,v119\n"
"v_accvgpr_write_b32 a130,v120\n"
"v_lshlrev_b32 v120,16,v66\n"
"v_and_b32 v121,0xffff0000,v66\n"
"v_mul_f32 v120,v120,v119\n"
"v_mul_f32 v121,v121,v119\n"
"v_cvt_pk_bf16_f32 v66,v120,v121\n"
"v_accvgpr_read_b32 v120,a131\n"
"v_mul_f32 v120,v120,v119\n"
"v_accvgpr_write_b32 a131,v120\n"
"v_lshlrev_b32 v120,16,v67\n"
"v_and_b32 v121,0xffff0000,v67\n"
"v_mul_f32 v120,v120,v119\n"
"v_mul_f32 v121,v121,v119\n"
"v_cvt_pk_bf16_f32 v67,v120,v121\n"
".Lfk_rare1_0:\n"
"s_bitcmp1_b32 s91,1\n"
"s_cbranch_scc0 .Lfk_rare1_1\n"
"v_max3_f32 v119,v40,v41,v42\n"
"v_max3_f32 v120,v43,v44,v45\n"
"v_max3_f32 v119,v119,v46,v47\n"
"v_max_f32 v119,v119,v120\n"
"v_mov_b32 v120,v119\n"
"s_nop 1\n"
"v_permlane16_swap_b32 v120,v119\n"
"v_max_f32 v119,v119,v120\n"
"v_mov_b32 v120,v119\n"
"s_nop 1\n"
"v_permlane32_swap_b32 v120,v119\n"
"v_max_f32 v119,v119,v120\n"
"s_bitcmp1_b32 s90,1\n"
"s_cselect_b32 s82,0xff800000,0\n"
"v_add_f32 v119,0x41980000,v119\n"
"v_max_f32 v119,s82,v119\n"
"v_cmp_lt_f32 vcc,s17,|v119|\n"
"s_nop 0\n"
"s_cbranch_vccz .Lfk_rare1_1\n"
"v_ceil_f32 v119,v119\n"
"v_sub_f32 v40,v40,v119\n"
"v_sub_f32 v41,v41,v119\n"
"v_sub_f32 v42,v42,v119\n"
"v_sub_f32 v43,v43,v119\n"
"v_sub_f32 v44,v44,v119\n"
"v_sub_f32 v45,v45,v119\n"
"v_sub_f32 v46,v46,v119\n"
"v_sub_f32 v47,v47,v119\n"
"v_sub_f32 v100,v100,v119\n"
"v_sub_f32 v101,v101,v119\n"
"v_sub_f32 v102,v102,v119\n"
"v_sub_f32 v103,v103,v119\n"
"s_cbranch_scc1 .Lfk_rare1_1\n"
"v_exp_f32 v119,-v119\n"
"s_nop 0\n"
"v_mul_f32 v160,v160,v119\n"
"v_mul_f32 v161,v161,v119\n"
"v_mul_f32 v162,v162,v119\n"
"v_mul_f32 v163,v163,v119\n"
"v_mul_f32 v164,v164,v119\n"
"v_mul_f32 v165,v165,v119\n"
"v_mul_f32 v166,v166,v119\n"
"v_mul_f32 v167,v167,v119\n"
"v_mul_f32 v168,v168,v119\n"
"v_mul_f32 v169,v169,v119\n"
"v_mul_f32 v170,v170,v119\n"
"v_mul_f32 v171,v171,v119\n"
"v_mul_f32 v172,v172,v119\n"
"v_mul_f32 v173,v173,v119\n"
"v_mul_f32 v174,v174,v119\n"
"v_mul_f32 v175,v175,v119\n"
"v_mul_f32 v176,v176,v119\n"
"v_mul_f32 v177,v177,v119\n"
"v_mul_f32 v178,v178,v119\n"
"v_mul_f32 v179,v179,v119\n"
"v_mul_f32 v180,v180,v119\n"
"v_mul_f32 v181,v181,v119\n"
"v_mul_f32 v182,v182,v119\n"
"v_mul_f32 v183,v183,v119\n"
"v_mul_f32 v184,v184,v119\n"
"v_mul_f32 v185,v185,v119\n"
"v_mul_f32 v186,v186,v119\n"
"v_mul_f32 v187,v187,v119\n"
"v_mul_f32 v188,v188,v119\n"
"v_mul_f32 v189,v189,v119\n"
"v_mul_f32 v190,v190,v119\n"
"v_mul_f32 v191,v191,v119\n"
"v_accvgpr_read_b32 v120,a132\n"
"v_mul_f32 v120,v120,v119\n"
"v_accvgpr_write_b32 a132,v120\n"
"v_lshlrev_b32 v120,16,v68\n"
"v_and_b32 v121,0xffff0000,v68\n"
"v_mul_f32 v120,v120,v119\n"
"v_mul_f32 v121,v121,v119\n"
"v_cvt_pk_bf16_f32 v68,v120,v121\n"
"v_accvgpr_read_b32 v120,a133\n"
"v_mul_f32 v120,v120,v119\n"
"v_accvgpr_write_b32 a133,v120\n"
"v_lshlrev_b32 v120,16,v69\n"
"v_and_b32 v121,0xffff0000,v69\n"
"v_mul_f32 v120,v120,v119\n"
"v_mul_f32 v121,v121,v119\n"
"v_cvt_pk_bf16_f32 v69,v120,v121\n"
"v_accvgpr_read_b32 v120,a134\n"
"v_mul_f32 v120,v120,v119\n"
"v_accvgpr_write_b32 a134,v120\n"
"v_lshlrev_b32 v120,16,v70\n"
"v_and_b32 v121,0xffff0000,v70\n"
"v_mul_f32 v120,v120,v119\n"
"v_mul_f32 v121,v121,v119\n"
"v_cvt_pk_bf16_f32 v70,v120,v121\n"
"v_accvgpr_read_b32 v120,a135\n"
"v_mul_f32 v120,v120,v119\n"
"v_accvgpr_write_b32 a135,v120\n"
"v_lshlrev_b32 v120,16,v71\n"
"v_and_b32 v121,0xffff0000,v71\n"
"v_mul_f32 v120,v120,v119\n"
"v_mul_f32 v121,v121,v119\n"
"v_cvt_pk_bf16_f32 v71,v120,v121\n"
".Lfk_rare1_1:\n"
"s_bitcmp1_b32 s91,2\n"
"s_cbranch_scc0 .Lfk_rare1_2\n"
"v_max3_f32 v119,v48,v49,v50\n"
"v_max3_f32 v120,v51,v52,v53\n"
"v_max3_f32 v119,v119,v54,v55\n"
"v_max_f32 v119,v119,v120\n"
"v_mov_b32 v120,v119\n"
"s_nop 1\n"
"v_permlane16_swap_b32 v120,v119\n"
"v_max_f32 v119,v119,v120\n"
"v_mov_b32 v120,v119\n"
"s_nop 1\n"
"v_permlane32_swap_b32 v120,v119\n"
"v_max_f32 v119,v119,v120\n"
"s_bitcmp1_b32 s90,2\n"
"s_cselect_b32 s82,0xff800000,0\n"
"v_add_f32 v119,0x41980000,v119\n"
"v_max_f32 v119,s82,v119\n"
"v_cmp_lt_f32 vcc,s17,|v119|\n"
"s_nop 0\n"
"s_cbranch_vccz .Lfk_rare1_2\n"
"v_ceil_f32 v119,v119\n"
"v_sub_f32 v48,v48,v119\n"
"v_sub_f32 v49,v49,v119\n"
"v_sub_f32 v50,v50,v119\n"
"v_sub_f32 v51,v51,v119\n"
"v_sub_f32 v52,v52,v119\n"
"v_sub_f32 v53,v53,v119\n"
"v_sub_f32 v54,v54,v119\n"
"v_sub_f32 v55,v55,v119\n"
"v_sub_f32 v104,v104,v119\n"
"v_sub_f32 v105,v105,v119\n"
"v_sub_f32 v106,v106,v119\n"
"v_sub_f32 v107,v107,v119\n"
"s_cbranch_scc1 .Lfk_rare1_2\n"
"v_exp_f32 v119,-v119\n"
"s_nop 0\n"
"v_mul_f32 v192,v192,v119\n"
"v_mul_f32 v193,v193,v119\n"
"v_mul_f32 v194,v194,v119\n"
"v_mul_f32 v195,v195,v119\n"
"v_mul_f32 v196,v196,v119\n"
"v_mul_f32 v197,v197,v119\n"
"v_mul_f32 v198,v198,v119\n"
"v_mul_f32 v199,v199,v119\n"
"v_mul_f32 v200,v200,v119\n"
"v_mul_f32 v201,v201,v119\n"
"v_mul_f32 v202,v202,v119\n"
"v_mul_f32 v203,v203,v119\n"
"v_mul_f32 v204,v204,v119\n"
"v_mul_f32 v205,v205,v119\n"
"v_mul_f32 v206,v206,v119\n"
"v_mul_f32 v207,v207,v119\n"
"v_mul_f32 v208,v208,v119\n"
"v_mul_f32 v209,v209,v119\n"
"v_mul_f32 v210,v210,v119\n"
"v_mul_f32 v211,v211,v119\n"
"v_mul_f32 v212,v212,v119\n"
"v_mul_f32 v213,v213,v119\n"
"v_mul_f32 v214,v214,v119\n"
"v_mul_f32 v215,v215,v119\n"
"v_mul_f32 v216,v216,v119\n"
"v_mul_f32 v217,v217,v119\n"
"v_mul_f32 v218,v218,v119\n"
"v_mul_f32 v219,v219,v119\n"
"v_mul_f32 v220,v220,v119\n"
"v_mul_f32 v221,v221,v119\n"
"v_mul_f32 v222,v222,v119\n"
"v_mul_f32 v223,v223,v119\n"
"v_accvgpr_read_b32 v120,a136\n"
"v_mul_f32 v120,v120,v119\n"
"v_accvgpr_write_b32 a136,v120\n"
"v_lshlrev_b32 v120,16,v72\n"
"v_and_b32 v121,0xffff0000,v72\n"
"v_mul_f32 v120,v120,v119\n"
"v_mul_f32 v121,v121,v119\n"
"v_cvt_pk_bf16_f32 v72,v120,v121\n"
"v_accvgpr_read_b32 v120,a137\n"
"v_mul_f32 v120,v120,v119\n"
"v_accvgpr_write_b32 a137,v120\n"
"v_lshlrev_b32 v120,16,v73\n"
"v_and_b32 v121,0xffff0000,v73\n"
"v_mul_f32 v120,v120,v119\n"
"v_mul_f32 v121,v121,v119\n"
"v_cvt_pk_bf16_f32 v73,v120,v121\n"
"v_accvgpr_read_b32 v120,a138\n"
"v_mul_f32 v120,v120,v119\n"
"v_accvgpr_write_b32 a138,v120\n"
"v_lshlrev_b32 v120,16,v74\n"
"v_and_b32 v121,0xffff0000,v74\n"
"v_mul_f32 v120,v120,v119\n"
"v_mul_f32 v121,v121,v119\n"
"v_cvt_pk_bf16_f32 v74,v120,v121\n"
"v_accvgpr_read_b32 v120,a139\n"
"v_mul_f32 v120,v120,v119\n"
"v_accvgpr_write_b32 a139,v120\n"
"v_lshlrev_b32 v120,16,v75\n"
"v_and_b32 v121,0xffff0000,v75\n"
"v_mul_f32 v120,v120,v119\n"
"v_mul_f32 v121,v121,v119\n"
"v_cvt_pk_bf16_f32 v75,v120,v121\n"
".Lfk_rare1_2:\n"
"s_bitcmp1_b32 s91,3\n"
"s_cbranch_scc0 .Lfk_rare1_3\n"
"v_max3_f32 v119,v56,v57,v58\n"
"v_max3_f32 v120,v59,v60,v61\n"
"v_max3_f32 v119,v119,v62,v63\n"
"v_max_f32 v119,v119,v120\n"
"v_mov_b32 v120,v119\n"
"s_nop 1\n"
"v_permlane16_swap_b32 v120,v119\n"
"v_max_f32 v119,v119,v120\n"
"v_mov_b32 v120,v119\n"
"s_nop 1\n"
"v_permlane32_swap_b32 v120,v119\n"
"v_max_f32 v119,v119,v120\n"
"s_bitcmp1_b32 s90,3\n"
"s_cselect_b32 s82,0xff800000,0\n"
"v_add_f32 v119,0x41980000,v119\n"
"v_max_f32 v119,s82,v119\n"
"v_cmp_lt_f32 vcc,s17,|v119|\n"
"s_nop 0\n"
"s_cbranch_vccz .Lfk_rare1_3\n"
"v_ceil_f32 v119,v119\n"
"v_sub_f32 v56,v56,v119\n"
"v_sub_f32 v57,v57,v119\n"
"v_sub_f32 v58,v58,v119\n"
"v_sub_f32 v59,v59,v119\n"
"v_sub_f32 v60,v60,v119\n"
"v_sub_f32 v61,v61,v119\n"
"v_sub_f32 v62,v62,v119\n"
"v_sub_f32 v63,v63,v119\n"
"v_sub_f32 v108,v108,v119\n"
"v_sub_f32 v109,v109,v119\n"
"v_sub_f32 v110,v110,v119\n"
"v_sub_f32 v111,v111,v119\n"
"s_cbranch_scc1 .Lfk_rare1_3\n"
"v_exp_f32 v119,-v119\n"
"s_nop 0\n"
"v_mul_f32 v224,v224,v119\n"
"v_mul_f32 v225,v225,v119\n"
"v_mul_f32 v226,v226,v119\n"
"v_mul_f32 v227,v227,v119\n"
"v_mul_f32 v228,v228,v119\n"
"v_mul_f32 v229,v229,v119\n"
"v_mul_f32 v230,v230,v119\n"
"v_mul_f32 v231,v231,v119\n"
"v_mul_f32 v232,v232,v119\n"
"v_mul_f32 v233,v233,v119\n"
"v_mul_f32 v234,v234,v119\n"
"v_mul_f32 v235,v235,v119\n"
"v_mul_f32 v236,v236,v119\n"
"v_mul_f32 v237,v237,v119\n"
"v_mul_f32 v238,v238,v119\n"
"v_mul_f32 v239,v239,v119\n"
"v_mul_f32 v240,v240,v119\n"
"v_mul_f32 v241,v241,v119\n"
"v_mul_f32 v242,v242,v119\n"
"v_mul_f32 v243,v243,v119\n"
"v_mul_f32 v244,v244,v119\n"
"v_mul_f32 v245,v245,v119\n"
"v_mul_f32 v246,v246,v119\n"
"v_mul_f32 v247,v247,v119\n"
"v_mul_f32 v248,v248,v119\n"
"v_mul_f32 v249,v249,v119\n"
"v_mul_f32 v250,v250,v119\n"
"v_mul_f32 v251,v251,v119\n"
"v_mul_f32 v252,v252,v119\n"
"v_mul_f32 v253,v253,v119\n"
"v_mul_f32 v254,v254,v119\n"
"v_mul_f32 v255,v255,v119\n"
"v_accvgpr_read_b32 v120,a140\n"
"v_mul_f32 v120,v120,v119\n"
"v_accvgpr_write_b32 a140,v120\n"
"v_lshlrev_b32 v120,16,v76\n"
"v_and_b32 v121,0xffff0000,v76\n"
"v_mul_f32 v120,v120,v119\n"
"v_mul_f32 v121,v121,v119\n"
"v_cvt_pk_bf16_f32 v76,v120,v121\n"
"v_accvgpr_read_b32 v120,a141\n"
"v_mul_f32 v120,v120,v119\n"
"v_accvgpr_write_b32 a141,v120\n"
"v_lshlrev_b32 v120,16,v77\n"
"v_and_b32 v121,0xffff0000,v77\n"
"v_mul_f32 v120,v120,v119\n"
"v_mul_f32 v121,v121,v119\n"
"v_cvt_pk_bf16_f32 v77,v120,v121\n"
"v_accvgpr_read_b32 v120,a142\n"
"v_mul_f32 v120,v120,v119\n"
"v_accvgpr_write_b32 a142,v120\n"
"v_lshlrev_b32 v120,16,v78\n"
"v_and_b32 v121,0xffff0000,v78\n"
"v_mul_f32 v120,v120,v119\n"
"v_mul_f32 v121,v121,v119\n"
"v_cvt_pk_bf16_f32 v78,v120,v121\n"
"v_accvgpr_read_b32 v120,a143\n"
"v_mul_f32 v120,v120,v119\n"
"v_accvgpr_write_b32 a143,v120\n"
"v_lshlrev_b32 v120,16,v79\n"
"v_and_b32 v121,0xffff0000,v79\n"
"v_mul_f32 v120,v120,v119\n"
"v_mul_f32 v121,v121,v119\n"
"v_cvt_pk_bf16_f32 v79,v120,v121\n"
".Lfk_rare1_3:\n"
"s_mov_b32 s90,0\n"
"s_nop 1\n"
"s_setpc_b64 s[94:95]\n"
".Lfk_rare3:\n"
"s_nop 7\n"
"v_max3_f32 v119,v32,v33,v34\n"
"v_max3_f32 v120,v35,v36,v37\n"
"v_max3_f32 v119,v119,v38,v39\n"
"v_max_f32 v119,v119,v120\n"
"v_mov_b32 v120,v119\n"
"s_nop 1\n"
"v_permlane16_swap_b32 v120,v119\n"
"v_max_f32 v119,v119,v120\n"
"v_mov_b32 v120,v119\n"
"s_nop 1\n"
"v_permlane32_swap_b32 v120,v119\n"
"v_max_f32 v119,v119,v120\n"
"s_bitcmp1_b32 s90,0\n"
"s_cselect_b32 s82,0xff800000,0\n"
"v_add_f32 v119,0x41980000,v119\n"
"v_max_f32 v119,s82,v119\n"
"v_cmp_lt_f32 vcc,s17,|v119|\n"
"s_nop 0\n"
"s_cbranch_vccz .Lfk_rare3_0\n"
"v_ceil_f32 v119,v119\n"
"v_sub_f32 v32,v32,v119\n"
"v_sub_f32 v33,v33,v119\n"
"v_sub_f32 v34,v34,v119\n"
"v_sub_f32 v35,v35,v119\n"
"v_sub_f32 v36,v36,v119\n"
"v_sub_f32 v37,v37,v119\n"
"v_sub_f32 v38,v38,v119\n"
"v_sub_f32 v39,v39,v119\n"
"v_sub_f32 v96,v96,v119\n"
"v_sub_f32 v97,v97,v119\n"
"v_sub_f32 v98,v98,v119\n"
"v_sub_f32 v99,v99,v119\n"
"v_sub_f32 v0,v0,v119\n"
"v_sub_f32 v1,v1,v119\n"
"v_sub_f32 v2,v2,v119\n"
"v_sub_f32 v3,v3,v119\n"
"v_sub_f32 v4,v4,v119\n"
"v_sub_f32 v5,v5,v119\n"
"v_sub_f32 v6,v6,v119\n"
"v_sub_f32 v7,v7,v119\n"
"v_exp_f32 v32,v32\n"
"v_exp_f32 v33,v33\n"
"v_exp_f32 v34,v34\n"
"v_exp_f32 v35,v35\n"
"v_exp_f32 v36,v36\n"
"v_exp_f32 v37,v37\n"
"v_exp_f32 v38,v38\n"
"v_exp_f32 v39,v39\n"
"v_cvt_pk_bf16_f32 v80,v32,v33\n"
"v_cvt_pk_bf16_f32 v81,v34,v35\n"
"v_cvt_pk_bf16_f32 v82,v36,v37\n"
"v_cvt_pk_bf16_f32 v83,v38,v39\n"
"s_cbranch_scc1 .Lfk_rare3_0\n"
"v_exp_f32 v119,-v119\n"
"s_nop 0\n"
"v_mul_f32 v128,v128,v119\n"
"v_mul_f32 v129,v129,v119\n"
"v_mul_f32 v130,v130,v119\n"
"v_mul_f32 v131,v131,v119\n"
"v_mul_f32 v132,v132,v119\n"
"v_mul_f32 v133,v133,v119\n"
"v_mul_f32 v134,v134,v119\n"
"v_mul_f32 v135,v135,v119\n"
"v_mul_f32 v136,v136,v119\n"
"v_mul_f32 v137,v137,v119\n"
"v_mul_f32 v138,v138,v119\n"
"v_mul_f32 v139,v139,v119\n"
"v_mul_f32 v140,v140,v119\n"
"v_mul_f32 v141,v141,v119\n"
"v_mul_f32 v142,v142,v119\n"
"v_mul_f32 v143,v143,v119\n"
"v_mul_f32 v144,v144,v119\n"
"v_mul_f32 v145,v145,v119\n"
"v_mul_f32 v146,v146,v119\n"
"v_mul_f32 v147,v147,v119\n"
"v_mul_f32 v148,v148,v119\n"
"v_mul_f32 v149,v149,v119\n"
"v_mul_f32 v150,v150,v119\n"
"v_mul_f32 v151,v151,v119\n"
"v_mul_f32 v152,v152,v119\n"
"v_mul_f32 v153,v153,v119\n"
"v_mul_f32 v154,v154,v119\n"
"v_mul_f32 v155,v155,v119\n"
"v_mul_f32 v156,v156,v119\n"
"v_mul_f32 v157,v157,v119\n"
"v_mul_f32 v158,v158,v119\n"
"v_mul_f32 v159,v159,v119\n"
"v_accvgpr_read_b32 v120,a128\n"
"v_mul_f32 v120,v120,v119\n"
"v_accvgpr_write_b32 a128,v120\n"
"v_accvgpr_read_b32 v120,a129\n"
"v_mul_f32 v120,v120,v119\n"
"v_accvgpr_write_b32 a129,v120\n"
"v_accvgpr_read_b32 v120,a130\n"
"v_mul_f32 v120,v120,v119\n"
"v_accvgpr_write_b32 a130,v120\n"
"v_accvgpr_read_b32 v120,a131\n"
"v_mul_f32 v120,v120,v119\n"
"v_accvgpr_write_b32 a131,v120\n"
".Lfk_rare3_0:\n"
"v_max3_f32 v119,v40,v41,v42\n"
"v_max3_f32 v120,v43,v44,v45\n"
"v_max3_f32 v119,v119,v46,v47\n"
"v_max_f32 v119,v119,v120\n"
"v_mov_b32 v120,v119\n"
"s_nop 1\n"
"v_permlane16_swap_b32 v120,v119\n"
"v_max_f32 v119,v119,v120\n"
"v_mov_b32 v120,v119\n"
"s_nop 1\n"
"v_permlane32_swap_b32 v120,v119\n"
"v_max_f32 v119,v119,v120\n"
"s_bitcmp1_b32 s90,1\n"
"s_cselect_b32 s82,0xff800000,0\n"
"v_add_f32 v119,0x41980000,v119\n"
"v_max_f32 v119,s82,v119\n"
"v_cmp_lt_f32 vcc,s17,|v119|\n"
"s_nop 0\n"
"s_cbranch_vccz .Lfk_rare3_1\n"
"v_ceil_f32 v119,v119\n"
"v_sub_f32 v40,v40,v119\n"
"v_sub_f32 v41,v41,v119\n"
"v_sub_f32 v42,v42,v119\n"
"v_sub_f32 v43,v43,v119\n"
"v_sub_f32 v44,v44,v119\n"
"v_sub_f32 v45,v45,v119\n"
"v_sub_f32 v46,v46,v119\n"
"v_sub_f32 v47,v47,v119\n"
"v_sub_f32 v100,v100,v119\n"
"v_sub_f32 v101,v101,v119\n"
"v_sub_f32 v102,v102,v119\n"
"v_sub_f32 v103,v103,v119\n"
"v_sub_f32 v8,v8,v119\n"
"v_sub_f32 v9,v9,v119\n"
"v_sub_f32 v10,v10,v119\n"
"v_sub_f32 v11,v11,v119\n"
"v_sub_f32 v12,v12,v119\n"
"v_sub_f32 v13,v13,v119\n"
"v_sub_f32 v14,v14,v119\n"
"v_sub_f32 v15,v15,v119\n"
"v_exp_f32 v40,v40\n"
"v_exp_f32 v41,v41\n"
"v_exp_f32 v42,v42\n"
"v_exp_f32 v43,v43\n"
"v_exp_f32 v44,v44\n"
"v_exp_f32 v45,v45\n"
"v_exp_f32 v46,v46\n"
"v_exp_f32 v47,v47\n"
"v_cvt_pk_bf16_f32 v84,v40,v41\n"
"v_cvt_pk_bf16_f32 v85,v42,v43\n"
"v_cvt_pk_bf16_f32 v86,v44,v45\n"
"v_cvt_pk_bf16_f32 v87,v46,v47\n"
"s_cbranch_scc1 .Lfk_rare3_1\n"
"v_exp_f32 v119,-v119\n"
"s_nop 0\n"
"v_mul_f32 v160,v160,v119\n"
"v_mul_f32 v161,v161,v119\n"
"v_mul_f32 v162,v162,v119\n"
"v_mul_f32 v163,v163,v119\n"
"v_mul_f32 v164,v164,v119\n"
"v_mul_f32 v165,v165,v119\n"
"v_mul_f32 v166,v166,v119\n"
"v_mul_f32 v167,v167,v119\n"
"v_mul_f32 v168,v168,v119\n"
"v_mul_f32 v169,v169,v119\n"
"v_mul_f32 v170,v170,v119\n"
"v_mul_f32 v171,v171,v119\n"
"v_mul_f32 v172,v172,v119\n"
"v_mul_f32 v173,v173,v119\n"
"v_mul_f32 v174,v174,v119\n"
"v_mul_f32 v175,v175,v119\n"
"v_mul_f32 v176,v176,v119\n"
"v_mul_f32 v177,v177,v119\n"
"v_mul_f32 v178,v178,v119\n"
"v_mul_f32 v179,v179,v119\n"
"v_mul_f32 v180,v180,v119\n"
"v_mul_f32 v181,v181,v119\n"
"v_mul_f32 v182,v182,v119\n"
"v_mul_f32 v183,v183,v119\n"
"v_mul_f32 v184,v184,v119\n"
"v_mul_f32 v185,v185,v119\n"
"v_mul_f32 v186,v186,v119\n"
"v_mul_f32 v187,v187,v119\n"
"v_mul_f32 v188,v188,v119\n"
"v_mul_f32 v189,v189,v119\n"
"v_mul_f32 v190,v190,v119\n"
"v_mul_f32 v191,v191,v119\n"
"v_accvgpr_read_b32 v120,a132\n"
"v_mul_f32 v120,v120,v119\n"
"v_accvgpr_write_b32 a132,v120\n"
"v_accvgpr_read_b32 v120,a133\n"
"v_mul_f32 v120,v120,v119\n"
"v_accvgpr_write_b32 a133,v120\n"
"v_accvgpr_read_b32 v120,a134\n"
"v_mul_f32 v120,v120,v119\n"
"v_accvgpr_write_b32 a134,v120\n"
"v_accvgpr_read_b32 v120,a135\n"
"v_mul_f32 v120,v120,v119\n"
"v_accvgpr_write_b32 a135,v120\n"
".Lfk_rare3_1:\n"
"v_max3_f32 v119,v48,v49,v50\n"
"v_max3_f32 v120,v51,v52,v53\n"
"v_max3_f32 v119,v119,v54,v55\n"
"v_max_f32 v119,v119,v120\n"
"v_mov_b32 v120,v119\n"
"s_nop 1\n"
"v_permlane16_swap_b32 v120,v119\n"
"v_max_f32 v119,v119,v120\n"
"v_mov_b32 v120,v119\n"
"s_nop 1\n"
"v_permlane32_swap_b32 v120,v119\n"
"v_max_f32 v119,v119,v120\n"
"s_bitcmp1_b32 s90,2\n"
"s_cselect_b32 s82,0xff800000,0\n"
"v_add_f32 v119,0x41980000,v119\n"
"v_max_f32 v119,s82,v119\n"
"v_cmp_lt_f32 vcc,s17,|v119|\n"
"s_nop 0\n"
"s_cbranch_vccz .Lfk_rare3_2\n"
"v_ceil_f32 v119,v119\n"
"v_sub_f32 v48,v48,v119\n"
"v_sub_f32 v49,v49,v119\n"
"v_sub_f32 v50,v50,v119\n"
"v_sub_f32 v51,v51,v119\n"
"v_sub_f32 v52,v52,v119\n"
"v_sub_f32 v53,v53,v119\n"
"v_sub_f32 v54,v54,v119\n"
"v_sub_f32 v55,v55,v119\n"
"v_sub_f32 v104,v104,v119\n"
"v_sub_f32 v105,v105,v119\n"
"v_sub_f32 v106,v106,v119\n"
"v_sub_f32 v107,v107,v119\n"
"v_sub_f32 v16,v16,v119\n"
"v_sub_f32 v17,v17,v119\n"
"v_sub_f32 v18,v18,v119\n"
"v_sub_f32 v19,v19,v119\n"
"v_sub_f32 v20,v20,v119\n"
"v_sub_f32 v21,v21,v119\n"
"v_sub_f32 v22,v22,v119\n"
"v_sub_f32 v23,v23,v119\n"
"v_exp_f32 v48,v48\n"
"v_exp_f32 v49,v49\n"
"v_exp_f32 v50,v50\n"
"v_exp_f32 v51,v51\n"
"v_exp_f32 v52,v52\n"
"v_exp_f32 v53,v53\n"
"v_exp_f32 v54,v54\n"
"v_exp_f32 v55,v55\n"
"v_cvt_pk_bf16_f32 v88,v48,v49\n"
"v_cvt_pk_bf16_f32 v89,v50,v51\n"
"v_cvt_pk_bf16_f32 v90,v52,v53\n"
"v_cvt_pk_bf16_f32 v91,v54,v55\n"
"s_cbranch_scc1 .Lfk_rare3_2\n"
"v_exp_f32 v119,-v119\n"
"s_nop 0\n"
"v_mul_f32 v192,v192,v119\n"
"v_mul_f32 v193,v193,v119\n"
"v_mul_f32 v194,v194,v119\n"
"v_mul_f32 v195,v195,v119\n"
"v_mul_f32 v196,v196,v119\n"
"v_mul_f32 v197,v197,v119\n"
"v_mul_f32 v198,v198,v119\n"
"v_mul_f32 v199,v199,v119\n"
"v_mul_f32 v200,v200,v119\n"
"v_mul_f32 v201,v201,v119\n"
"v_mul_f32 v202,v202,v119\n"
"v_mul_f32 v203,v203,v119\n"
"v_mul_f32 v204,v204,v119\n"
"v_mul_f32 v205,v205,v119\n"
"v_mul_f32 v206,v206,v119\n"
"v_mul_f32 v207,v207,v119\n"
"v_mul_f32 v208,v208,v119\n"
"v_mul_f32 v209,v209,v119\n"
"v_mul_f32 v210,v210,v119\n"
"v_mul_f32 v211,v211,v119\n"
"v_mul_f32 v212,v212,v119\n"
"v_mul_f32 v213,v213,v119\n"
"v_mul_f32 v214,v214,v119\n"
"v_mul_f32 v215,v215,v119\n"
"v_mul_f32 v216,v216,v119\n"
"v_mul_f32 v217,v217,v119\n"
"v_mul_f32 v218,v218,v119\n"
"v_mul_f32 v219,v219,v119\n"
"v_mul_f32 v220,v220,v119\n"
"v_mul_f32 v221,v221,v119\n"
"v_mul_f32 v222,v222,v119\n"
"v_mul_f32 v223,v223,v119\n"
"v_accvgpr_read_b32 v120,a136\n"
"v_mul_f32 v120,v120,v119\n"
"v_accvgpr_write_b32 a136,v120\n"
"v_accvgpr_read_b32 v120,a137\n"
"v_mul_f32 v120,v120,v119\n"
"v_accvgpr_write_b32 a137,v120\n"
"v_accvgpr_read_b32 v120,a138\n"
"v_mul_f32 v120,v120,v119\n"
"v_accvgpr_write_b32 a138,v120\n"
"v_accvgpr_read_b32 v120,a139\n"
"v_mul_f32 v120,v120,v119\n"
"v_accvgpr_write_b32 a139,v120\n"
".Lfk_rare3_2:\n"
"v_max3_f32 v119,v56,v57,v58\n"
"v_max3_f32 v120,v59,v60,v61\n"
"v_max3_f32 v119,v119,v62,v63\n"
"v_max_f32 v119,v119,v120\n"
"v_mov_b32 v120,v119\n"
"s_nop 1\n"
"v_permlane16_swap_b32 v120,v119\n"
"v_max_f32 v119,v119,v120\n"
"v_mov_b32 v120,v119\n"
"s_nop 1\n"
"v_permlane32_swap_b32 v120,v119\n"
"v_max_f32 v119,v119,v120\n"
"s_bitcmp1_b32 s90,3\n"
"s_cselect_b32 s82,0xff800000,0\n"
"v_add_f32 v119,0x41980000,v119\n"
"v_max_f32 v119,s82,v119\n"
"v_cmp_lt_f32 vcc,s17,|v119|\n"
"s_nop 0\n"
"s_cbranch_vccz .Lfk_rare3_3\n"
"v_ceil_f32 v119,v119\n"
"v_sub_f32 v56,v56,v119\n"
"v_sub_f32 v57,v57,v119\n"
"v_sub_f32 v58,v58,v119\n"
"v_sub_f32 v59,v59,v119\n"
"v_sub_f32 v60,v60,v119\n"
"v_sub_f32 v61,v61,v119\n"
"v_sub_f32 v62,v62,v119\n"
"v_sub_f32 v63,v63,v119\n"
"v_sub_f32 v108,v108,v119\n"
"v_sub_f32 v109,v109,v119\n"
"v_sub_f32 v110,v110,v119\n"
"v_sub_f32 v111,v111,v119\n"
"v_sub_f32 v24,v24,v119\n"
"v_sub_f32 v25,v25,v119\n"
"v_sub_f32 v26,v26,v119\n"
"v_sub_f32 v27,v27,v119\n"
"v_sub_f32 v28,v28,v119\n"
"v_sub_f32 v29,v29,v119\n"
"v_sub_f32 v30,v30,v119\n"
"v_sub_f32 v31,v31,v119\n"
"v_exp_f32 v56,v56\n"
"v_exp_f32 v57,v57\n"
"v_exp_f32 v58,v58\n"
"v_exp_f32 v59,v59\n"
"v_exp_f32 v60,v60\n"
"v_exp_f32 v61,v61\n"
"v_exp_f32 v62,v62\n"
"v_exp_f32 v63,v63\n"
"v_cvt_pk_bf16_f32 v92,v56,v57\n"
"v_cvt_pk_bf16_f32 v93,v58,v59\n"
"v_cvt_pk_bf16_f32 v94,v60,v61\n"
"v_cvt_pk_bf16_f32 v95,v62,v63\n"
"s_cbranch_scc1 .Lfk_rare3_3\n"
"v_exp_f32 v119,-v119\n"
"s_nop 0\n"
"v_mul_f32 v224,v224,v119\n"
"v_mul_f32 v225,v225,v119\n"
"v_mul_f32 v226,v226,v119\n"
"v_mul_f32 v227,v227,v119\n"
"v_mul_f32 v228,v228,v119\n"
"v_mul_f32 v229,v229,v119\n"
"v_mul_f32 v230,v230,v119\n"
"v_mul_f32 v231,v231,v119\n"
"v_mul_f32 v232,v232,v119\n"
"v_mul_f32 v233,v233,v119\n"
"v_mul_f32 v234,v234,v119\n"
"v_mul_f32 v235,v235,v119\n"
"v_mul_f32 v236,v236,v119\n"
"v_mul_f32 v237,v237,v119\n"
"v_mul_f32 v238,v238,v119\n"
"v_mul_f32 v239,v239,v119\n"
"v_mul_f32 v240,v240,v119\n"
"v_mul_f32 v241,v241,v119\n"
"v_mul_f32 v242,v242,v119\n"
"v_mul_f32 v243,v243,v119\n"
"v_mul_f32 v244,v244,v119\n"
"v_mul_f32 v245,v245,v119\n"
"v_mul_f32 v246,v246,v119\n"
"v_mul_f32 v247,v247,v119\n"
"v_mul_f32 v248,v248,v119\n"
"v_mul_f32 v249,v249,v119\n"
"v_mul_f32 v250,v250,v119\n"
"v_mul_f32 v251,v251,v119\n"
"v_mul_f32 v252,v252,v119\n"
"v_mul_f32 v253,v253,v119\n"
"v_mul_f32 v254,v254,v119\n"
"v_mul_f32 v255,v255,v119\n"
"v_accvgpr_read_b32 v120,a140\n"
"v_mul_f32 v120,v120,v119\n"
"v_accvgpr_write_b32 a140,v120\n"
"v_accvgpr_read_b32 v120,a141\n"
"v_mul_f32 v120,v120,v119\n"
"v_accvgpr_write_b32 a141,v120\n"
"v_accvgpr_read_b32 v120,a142\n"
"v_mul_f32 v120,v120,v119\n"
"v_accvgpr_write_b32 a142,v120\n"
"v_accvgpr_read_b32 v120,a143\n"
"v_mul_f32 v120,v120,v119\n"
"v_accvgpr_write_b32 a143,v120\n"
".Lfk_rare3_3:\n"
"s_mov_b32 s90,0\n"
"s_nop 1\n"
"s_setpc_b64 s[94:95]\n"
".Lfk_mslow1:\n"
"s_nop 7\n"
"v_min_f32 v119,v102,v108\n"
"v_sub_f32 v120,v119,v108\n"
"v_sub_f32 v119,v119,v102\n"
"v_exp_f32 v120,v120\n"
"v_exp_f32 v119,v119\n"
"v_mul_f32 v224,v224,v120\n"
"v_mul_f32 v225,v225,v120\n"
"v_mul_f32 v226,v226,v120\n"
"v_mul_f32 v227,v227,v120\n"
"v_mul_f32 v228,v228,v120\n"
"v_mul_f32 v229,v229,v120\n"
"v_mul_f32 v230,v230,v120\n"
"v_mul_f32 v231,v231,v120\n"
"v_mul_f32 v232,v232,v120\n"
"v_mul_f32 v233,v233,v120\n"
"v_mul_f32 v234,v234,v120\n"
"v_mul_f32 v235,v235,v120\n"
"v_mul_f32 v236,v236,v120\n"
"v_mul_f32 v237,v237,v120\n"
"v_mul_f32 v238,v238,v120\n"
"v_mul_f32 v239,v239,v120\n"
"v_mul_f32 v240,v240,v120\n"
"v_mul_f32 v241,v241,v120\n"
"v_mul_f32 v242,v242,v120\n"
"v_mul_f32 v243,v243,v120\n"
"v_mul_f32 v244,v244,v120\n"
"v_mul_f32 v245,v245,v120\n"
"v_mul_f32 v246,v246,v120\n"
"v_mul_f32 v247,v247,v120\n"
"v_mul_f32 v248,v248,v120\n"
"v_mul_f32 v249,v249,v120\n"
"v_mul_f32 v250,v250,v120\n"
"v_mul_f32 v251,v251,v120\n"
"v_mul_f32 v252,v252,v120\n"
"v_mul_f32 v253,v253,v120\n"
"v_mul_f32 v254,v254,v120\n"
"v_mul_f32 v255,v255,v120\n"
"v_accvgpr_read_b32 v121,a140\n"
"v_mul_f32 v121,v121,v120\n"
"v_accvgpr_write_b32 a140,v121\n"
"v_mul_f32 v103,v103,v119\n"
"v_accvgpr_read_b32 v121,a80\n"
"v_lshlrev_b32 v120,16,v121\n"
"v_and_b32 v121,0xffff0000,v121\n"
"v_mul_f32 v120,v120,v119\n"
"v_mul_f32 v121,v121,v119\n"
"v_cvt_pk_bf16_f32 v121,v120,v121\n"
"v_accvgpr_write_b32 a80,v121\n"
"v_accvgpr_read_b32 v121,a81\n"
"v_lshlrev_b32 v120,16,v121\n"
"v_and_b32 v121,0xffff0000,v121\n"
"v_mul_f32 v120,v120,v119\n"
"v_mul_f32 v121,v121,v119\n"
"v_cvt_pk_bf16_f32 v121,v120,v121\n"
"v_accvgpr_write_b32 a81,v121\n"
"v_accvgpr_read_b32 v121,a82\n"
"v_lshlrev_b32 v120,16,v121\n"
"v_and_b32 v121,0xffff0000,v121\n"
"v_mul_f32 v120,v120,v119\n"
"v_mul_f32 v121,v121,v119\n"
"v_cvt_pk_bf16_f32 v121,v120,v121\n"
"v_accvgpr_write_b32 a82,v121\n"
"v_accvgpr_read_b32 v121,a83\n"
"v_lshlrev_b32 v120,16,v121\n"
"v_and_b32 v121,0xffff0000,v121\n"
"v_mul_f32 v120,v120,v119\n"
"v_mul_f32 v121,v121,v119\n"
"v_cvt_pk_bf16_f32 v121,v120,v121\n"
"v_accvgpr_write_b32 a83,v121\n"
"v_accvgpr_read_b32 v121,a84\n"
"v_lshlrev_b32 v120,16,v121\n"
"v_and_b32 v121,0xffff0000,v121\n"
"v_mul_f32 v120,v120,v119\n"
"v_mul_f32 v121,v121,v119\n"
"v_cvt_pk_bf16_f32 v121,v120,v121\n"
"v_accvgpr_write_b32 a84,v121\n"
"v_accvgpr_read_b32 v121,a85\n"
"v_lshlrev_b32 v120,16,v121\n"
"v_and_b32 v121,0xffff0000,v121\n"
"v_mul_f32 v120,v120,v119\n"
"v_mul_f32 v121,v121,v119\n"
"v_cvt_pk_bf16_f32 v121,v120,v121\n"
"v_accvgpr_write_b32 a85,v121\n"
"v_accvgpr_read_b32 v121,a86\n"
"v_lshlrev_b32 v120,16,v121\n"
"v_and_b32 v121,0xffff0000,v121\n"
"v_mul_f32 v120,v120,v119\n"
"v_mul_f32 v121,v121,v119\n"
"v_cvt_pk_bf16_f32 v121,v120,v121\n"
"v_accvgpr_write_b32 a86,v121\n"
"v_accvgpr_read_b32 v121,a87\n"
"v_lshlrev_b32 v120,16,v121\n"
"v_and_b32 v121,0xffff0000,v121\n"
"v_mul_f32 v120,v120,v119\n"
"v_mul_f32 v121,v121,v119\n"
"v_cvt_pk_bf16_f32 v121,v120,v121\n"
"v_accvgpr_write_b32 a87,v121\n"
"v_accvgpr_read_b32 v121,a88\n"
"v_lshlrev_b32 v120,16,v121\n"
"v_and_b32 v121,0xffff0000,v121\n"
"v_mul_f32 v120,v120,v119\n"
"v_mul_f32 v121,v121,v119\n"
"v_cvt_pk_bf16_f32 v121,v120,v121\n"
"v_accvgpr_write_b32 a88,v121\n"
"v_accvgpr_read_b32 v121,a89\n"
"v_lshlrev_b32 v120,16,v121\n"
"v_and_b32 v121,0xffff0000,v121\n"
"v_mul_f32 v120,v120,v119\n"
"v_mul_f32 v121,v121,v119\n"
"v_cvt_pk_bf16_f32 v121,v120,v121\n"
"v_accvgpr_write_b32 a89,v121\n"
"v_accvgpr_read_b32 v121,a90\n"
"v_lshlrev_b32 v120,16,v121\n"
"v_and_b32 v121,0xffff0000,v121\n"
"v_mul_f32 v120,v120,v119\n"
"v_mul_f32 v121,v121,v119\n"
"v_cvt_pk_bf16_f32 v121,v120,v121\n"
"v_accvgpr_write_b32 a90,v121\n"
"v_accvgpr_read_b32 v121,a91\n"
"v_lshlrev_b32 v120,16,v121\n"
"v_and_b32 v121,0xffff0000,v121\n"
"v_mul_f32 v120,v120,v119\n"
"v_mul_f32 v121,v121,v119\n"
"v_cvt_pk_bf16_f32 v121,v120,v121\n"
"v_accvgpr_write_b32 a91,v121\n"
"v_accvgpr_read_b32 v121,a92\n"
"v_lshlrev_b32 v120,16,v121\n"
"v_and_b32 v121,0xffff0000,v121\n"
"v_mul_f32 v120,v120,v119\n"
"v_mul_f32 v121,v121,v119\n"
"v_cvt_pk_bf16_f32 v121,v120,v121\n"
"v_accvgpr_write_b32 a92,v121\n"
"v_accvgpr_read_b32 v121,a93\n"
"v_lshlrev_b32 v120,16,v121\n"
"v_and_b32 v121,0xffff0000,v121\n"
"v_mul_f32 v120,v120,v119\n"
"v_mul_f32 v121,v121,v119\n"
"v_cvt_pk_bf16_f32 v121,v120,v121\n"
"v_accvgpr_write_b32 a93,v121\n"
"v_accvgpr_read_b32 v121,a94\n"
"v_lshlrev_b32 v120,16,v121\n"
"v_and_b32 v121,0xffff0000,v121\n"
"v_mul_f32 v120,v120,v119\n"
"v_mul_f32 v121,v121,v119\n"
"v_cvt_pk_bf16_f32 v121,v120,v121\n"
"v_accvgpr_write_b32 a94,v121\n"
"v_accvgpr_read_b32 v121,a95\n"
"v_lshlrev_b32 v120,16,v121\n"
"v_and_b32 v121,0xffff0000,v121\n"
"v_mul_f32 v120,v120,v119\n"
"v_mul_f32 v121,v121,v119\n"
"v_cvt_pk_bf16_f32 v121,v120,v121\n"
"v_accvgpr_write_b32 a95,v121\n"
"s_nop 1\n"
"s_setpc_b64 s[94:95]\n"
".Lfk_end:\n"
        : "+{v0}"(tid), "+{s2}"(bid)
        : "{s[0:1]}"(__builtin_amdgcn_kernarg_segment_ptr())
        : "v1","v2","v3","v4","v5","v6","v7","v8","v9","v10","v11","v12","v13","v14","v15","v16","v17","v18","v19","v20","v21","v22","v23","v24","v25","v26","v27","v28","v29","v30","v31","v32","v33","v34","v35","v36","v37","v38","v39","v40","v41","v42","v43","v44","v45","v46","v47","v48","v49","v50","v51","v52","v53","v54","v55","v56","v57","v58","v59","v60","v61","v62","v63","v64","v65","v66","v67","v68","v69","v70","v71","v72","v73","v74","v75","v76","v77","v78","v79","v80","v81","v82","v83","v84","v85","v86","v87","v88","v89","v90","v91","v92","v93","v94","v95","v96","v97","v98","v99","v100","v101","v102","v103","v104","v105","v106","v107","v108","v109","v110","v111","v112","v113","v114","v115","v116","v117","v118","v119","v120","v121","v122","v123","v124","v125","v126","v127","v128","v129","v130","v131","v132","v133","v134","v135","v136","v137","v138","v139","v140","v141","v142","v143","v144","v145","v146","v147","v148","v149","v150","v151","v152","v153","v154","v155","v156","v157","v158","v159","v160","v161","v162","v163","v164","v165","v166","v167","v168","v169","v170","v171","v172","v173","v174","v175","v176","v177","v178","v179","v180","v181","v182","v183","v184","v185","v186","v187","v188","v189","v190","v191","v192","v193","v194","v195","v196","v197","v198","v199","v200","v201","v202","v203","v204","v205","v206","v207","v208","v209","v210","v211","v212","v213","v214","v215","v216","v217","v218","v219","v220","v221","v222","v223","v224","v225","v226","v227","v228","v229","v230","v231","v232","v233","v234","v235","v236","v237","v238","v239","v240","v241","v242","v243","v244","v245","v246","v247","v248","v249","v250","v251","v252","v253","v254","v255",
          "a0","a1","a2","a3","a4","a5","a6","a7","a8","a9","a10","a11","a12","a13","a14","a15","a16","a17","a18","a19","a20","a21","a22","a23","a24","a25","a26","a27","a28","a29","a30","a31","a32","a33","a34","a35","a36","a37","a38","a39","a40","a41","a42","a43","a44","a45","a46","a47","a48","a49","a50","a51","a52","a53","a54","a55","a56","a57","a58","a59","a60","a61","a62","a63","a64","a65","a66","a67","a68","a69","a70","a71","a72","a73","a74","a75","a76","a77","a78","a79","a80","a81","a82","a83","a84","a85","a86","a87","a88","a89","a90","a91","a92","a93","a94","a95","a96","a97","a98","a99","a100","a101","a102","a103","a104","a105","a106","a107","a108","a109","a110","a111","a112","a113","a114","a115","a116","a117","a118","a119","a120","a121","a122","a123","a124","a125","a126","a127","a128","a129","a130","a131","a132","a133","a134","a135","a136","a137","a138","a139","a140","a141","a142","a143","a144","a145","a146","a147","a148","a149","a150","a151","a152","a153","a154","a155","a156","a157","a158","a159","a160","a161","a162","a163","a164","a165","a166","a167","a168","a169","a170","a171","a172","a173","a174","a175","a176","a177","a178","a179","a180","a181","a182","a183","a184","a185","a186","a187","a188","a189","a190","a191","a192","a193","a194","a195","a196","a197","a198","a199","a200","a201","a202","a203","a204","a205","a206","a207","a208","a209","a210","a211","a212","a213","a214","a215","a216","a217","a218","a219","a220","a221","a222","a223","a224","a225","a226","a227","a228","a229","a230","a231","a232","a233","a234","a235","a236","a237","a238","a239","a240","a241","a242","a243","a244","a245","a246","a247","a248","a249","a250","a251","a252","a253","a254","a255",
          "s3","s4","s5","s6","s7","s8","s9","s10","s11","s12","s13","s14","s15","s16","s17","s18","s19","s20","s21","s22","s23","s24","s25","s26","s27","s28","s29","s30","s31","s32","s33","s34","s35","s36","s37","s38","s39","s40","s41","s42","s43","s44","s45","s46","s47","s48","s49","s50","s51","s52","s53","s54","s55","s56","s57","s58","s59","s60","s61","s62","s63","s64","s65","s66","s67","s68","s69","s70","s71","s72","s73","s74","s75","s76","s77","s78","s79","s80","s81","s82","s83","s84","s85","s86","s87","s88","s89","s90","s91","s92","s93","s94","s95","s96","s97","s98","s99","s100","s101","vcc","scc","m0","memory");
#endif
}

extern "C" void kernel_launch(void* const* d_in, const int* in_sizes, int n_in, void* d_out, int out_size, void* d_ws, size_t ws_size,
                              hipStream_t stream) {
    (void)hipFuncSetAttribute((const void*)fused_kernel, hipFuncAttributeMaxDynamicSharedMemorySize, 163840);
    fused_kernel<<<256, 256, 163840, stream>>>((const char*)d_in[0], (const char*)d_in[1], (const char*)d_in[2], (const char*)d_in[3], (const char*)d_in[4], (char*)d_ws,
                                               (const char*)d_in[5], (const char*)d_in[8], (const char*)d_in[9], (const char*)d_in[10], (const char*)d_in[7], (char*)d_out);
}
```

```cpp
#include <hip/hip_runtime.h>

extern "C" __global__ __launch_bounds__(256, 1) void fused_kernel(const char* __restrict__ x, const char* __restrict__ ckv, const char* __restrict__ wq,
                                                       const char* __restrict__ wuk, const char* __restrict__ wuv, char* ws,
                                                       const char* __restrict__ pwo, const char* __restrict__ pwg, const char* __restrict__ pwu, const char* __restrict__ pwd,
                                                       const char* __restrict__ pwr, char* pout) {
    unsigned tid = threadIdx.x, bid = blockIdx.x;
#if defined(__HIP_DEVICE_COMPILE__)
    asm volatile(
"s_load_dwordx2 s[16:17],s[0:1],8\n"
"s_load_dword s3,s[0:1],72\n"
"s_and_b32 s82,s2,7\n"
"s_lshl_b32 s82,s82,2\n"
"s_lshr_b32 s82,0x13576420,s82\n"
"s_and_b32 s82,s82,7\n"
"s_andn2_b32 s2,s2,7\n"
"s_or_b32 s2,s2,s82\n"
"s_getpc_b64 s[8:9]\n"
".Lfk_pc:\n"
"s_lshr_b32 s82,s2,3\n"
"s_lshl_b32 s82,s82,12\n"
"v_lshlrev_b32 v1,4,v0\n"
"s_mov_b32 s10,.Lfk_cold-.Lfk_pc\n"
"s_mov_b32 s11,0x20000\n"
"buffer_load_dword v115,v1,s[8:11],s82 offen\n"
"v_readfirstlane_b32 s52,v0\n"
"s_load_dwordx2 s[32:33],s[0:1],0\n"
"s_load_dwordx2 s[24:25],s[0:1],40\n"
"s_load_dwordx2 s[44:45],s[0:1],8\n"
"s_load_dwordx2 s[28:29],s[0:1],16\n"
"s_load_dwordx2 s[40:41],s[0:1],16\n"
"s_load_dwordx2 s[4:5],s[0:1],48\n"
"s_and_b32 s82,s2,8\n"
"s_add_u32 s82,s82,24\n"
"s_load_dwordx2 s[20:21],s[0:1],s82\n"
"s_lshr_b32 s52,s52,6\n"
"s_lshr_b32 s83,s2,7\n"
"s_lshl_b32 s83,s83,3\n"
"s_add_u32 s83,s83,24\n"
"s_load_dwordx2 s[36:37],s[0:1],s83\n"
"s_mov_b32 s100,s2\n"
"s_lshl_b32 s92,s2,2\n"
"s_add_u32 s92,s92,s52\n"
"v_and_b32 v0,63,v0\n"
"v_lshlrev_b32 v112,4,v0\n"
"s_and_b32 s88,s2,7\n"
"s_lshr_b32 s89,s2,3\n"
"s_lshl_b32 s93,s89,2\n"
"s_add_u32 s93,s93,s52\n"
"v_lshrrev_b32 v2,3,v0\n"
"v_xor_b32 v3,v2,v0\n"
"v_and_b32 v3,7,v3\n"
"v_lshlrev_b32 v3,4,v3\n"
"s_lshl_b32 s82,s52,9\n"
"v_add_u32 v2,s82,v2\n"
"v_lshl_add_u32 v106,v2,10,v3\n"
"v_add_u32 v107,7168,v106\n"
"v_add_u32 v108,14336,v106\n"
"v_add_u32 v109,21504,v106\n"
"v_lshrrev_b32 v4,4,v0\n"
"v_and_b32 v5,15,v0\n"
"v_lshlrev_b32 v6,1,v4\n"
"v_xor_b32 v5,v5,v6\n"
"v_lshlrev_b32 v5,4,v5\n"
"v_xor_b32 v6,0x80,v5\n"
"s_and_b32 s82,s52,1\n"
"s_lshl_b32 s82,s82,17\n"
"s_lshr_b32 s83,s52,1\n"
"s_lshl_b32 s83,s83,8\n"
"s_add_u32 s82,s82,s83\n"
"v_lshl_add_u32 v7,v4,13,s82\n"
"v_add_u32 v110,v7,v5\n"
"v_add_u32 v111,31744,v110\n"
"v_add_u32 v113,v7,v6\n"
"v_add_u32 v113,63488,v113\n"
"v_add_u32 v114,31744,v113\n"
"s_lshl_b32 s78,s52,12\n"
"s_lshl_b32 s79,s52,13\n"
"s_lshl_b32 s85,s52,11\n"
"s_lshr_b32 s82,s2,5\n"
"s_lshl_b32 s82,s82,15\n"
"s_lshl_b32 s83,s88,10\n"
"s_bfe_u32 s97,s2,0x10004\n"
"s_lshl_b32 s97,s97,9\n"
"s_add_u32 s83,s83,s97\n"
"v_lshrrev_b32 v60,3,v0\n"
"v_lshlrev_b32 v60,19,v60\n"
"v_and_b32 v61,7,v0\n"
"v_lshl_add_u32 v60,v61,7,v60\n"
"s_and_b32 s12,s93,63\n"
"s_lshl3_add_u32 s12,s12,s88\n"
"s_lshl_b32 s12,s12,10\n"
"s_add_u32 s38,s12,1049600\n"
"s_mov_b32 s39,0x20000\n"
"s_and_b32 s14,s52,1\n"
"s_mul_i32 s13,s14,507904\n"
"v_subrev_u32 v62,s13,v106\n"
"v_subrev_u32 v63,s13,v107\n"
"s_mul_i32 s14,s14,6144\n"
"s_sub_u32 s13,s79,s14\n"
"s_waitcnt lgkmcnt(0)\n"
"buffer_load_dword v115,v60,s[36:39],s12 offen\n"
"s_lshl_b32 s87,s92,11\n"
"s_add_u32 s44,s44,s87\n"
"s_addc_u32 s45,s45,0\n"
"s_mov_b32 s46,2048\n"
"s_mov_b32 s47,0x20000\n"
"v_lshlrev_b32 v1,5,v0\n"
"buffer_load_dword v115,v1,s[44:47],0 offen\n"
"s_add_u32 s16,s16,s82\n"
"s_addc_u32 s17,s17,0\n"
"s_sub_u32 s18,2097152,s82\n"
"s_mov_b32 s19,0x20000\n"
"s_add_u32 s20,s20,s83\n"
"s_addc_u32 s21,s21,0\n"
"s_sub_u32 s22,4194304,s83\n"
"s_mov_b32 s23,0x20000\n"
"s_mov_b32 s77,0\n"
"s_add_u32 m0,s78,147456\n"
"s_nop 0\n"
"buffer_load_dwordx4 v110,s[20:23],s77 offen offset:0 lds\n"
"buffer_load_dwordx4 v111,s[20:23],s77 offen offset:1024 lds\n"
"buffer_load_dwordx4 v113,s[20:23],s77 offen offset:2048 lds\n"
"buffer_load_dwordx4 v114,s[20:23],s77 offen offset:3072 lds\n"
"s_mov_b32 s76,786432\n"
"s_add_u32 m0,s13,12288\n"
"s_nop 0\n"
"buffer_load_dwordx4 v62,s[16:19],s76 offen offset:0 lds\n"
"buffer_load_dwordx4 v63,s[16:19],s76 offen offset:1024 lds\n"
"s_mov_b32 s77,262144\n"
"s_add_u32 m0,s78,131072\n"
"s_nop 0\n"
"buffer_load_dwordx4 v110,s[20:23],s77 offen offset:0 lds\n"
"buffer_load_dwordx4 v111,s[20:23],s77 offen offset:1024 lds\n"
"buffer_load_dwordx4 v113,s[20:23],s77 offen offset:2048 lds\n"
"buffer_load_dwordx4 v114,s[20:23],s77 offen offset:3072 lds\n"
"s_mov_b32 s76,0\n"
"s_add_u32 m0,s13,0\n"
"s_nop 0\n"
"buffer_load_dwordx4 v62,s[16:19],s76 offen offset:0 lds\n"
"buffer_load_dwordx4 v63,s[16:19],s76 offen offset:1024 lds\n"
"s_mov_b32 s76,262144\n"
"s_add_u32 m0,s13,4096\n"
"s_nop 0\n"
"buffer_load_dwordx4 v62,s[16:19],s76 offen offset:0 lds\n"
"buffer_load_dwordx4 v63,s[16:19],s76 offen offset:1024 lds\n"
"s_mov_b32 s76,524288\n"
"s_add_u32 m0,s13,8192\n"
"s_nop 0\n"
"buffer_load_dwordx4 v62,s[16:19],s76 offen offset:0 lds\n"
"buffer_load_dwordx4 v63,s[16:19],s76 offen offset:1024 lds\n"
"s_lshl_b32 s82,s92,12\n"
"s_add_u32 s32,s32,s82\n"
"s_addc_u32 s33,s33,0\n"
"s_mov_b32 s34,4096\n"
"s_mov_b32 s35,0x20000\n"
"s_mov_b32 s26,176160768\n"
"s_mov_b32 s27,0x20000\n"
"s_mov_b32 s75,148897792\n"
"s_lshr_b32 s54,s2,5\n"
"s_lshl_b32 s54,s54,19\n"
"s_and_b32 s82,s92,0x47\n"
"s_bfe_u32 s83,s92,0x10003\n"
"s_lshl_b32 s83,s83,5\n"
"s_or_b32 s82,s82,s83\n"
"s_bfe_u32 s83,s92,0x20004\n"
"s_lshl3_add_u32 s82,s83,s82\n"
"s_lshl_b32 s82,s82,7\n"
"s_add_u32 s54,s54,s82\n"
"v_lshrrev_b32 v1,3,v0\n"
"v_bfe_u32 v2,v0,2,1\n"
"v_and_b32 v3,3,v0\n"
"v_lshlrev_b32 v3,4,v3\n"
"v_lshl_add_u32 v70,v1,6,v3\n"
"v_lshl_add_u32 v70,v2,11,v70\n"
"s_bfe_u32 s82,s54,0x10009\n"
"s_lshl_b32 s82,s82,5\n"
"v_xor_b32 v3,s82,v3\n"
"v_lshl_add_u32 v71,v2,6,v3\n"
"v_lshl_add_u32 v71,v1,14,v71\n"
"buffer_load_dwordx2 v[78:79],off,s[24:27],s75 sc1\n"
"s_lshl_b32 s82,s52,16\n"
"v_lshl_add_u32 v7,v4,13,s82\n"
"s_bitcmp1_b32 s52,0\n"
"s_cselect_b32 s83,0x80,0\n"
"v_xor_b32 v5,s83,v5\n"
"v_add_u32 v72,v7,v5\n"
"v_add_u32 v73,32768,v72\n"
"s_mov_b32 s86,0\n"
"v_and_b32 v1,15,v0\n"
"v_lshrrev_b32 v2,4,v0\n"
"v_lshlrev_b32 v3,6,v1\n"
"v_lshl_add_u32 v3,v2,4,v3\n"
"v_lshrrev_b32 v4,4,v3\n"
"v_and_b32 v4,32,v4\n"
"v_xor_b32 v97,v3,v4\n"
"s_lshr_b32 s82,s52,1\n"
"s_lshl_b32 s82,s82,14\n"
"v_and_b32 v3,7,v1\n"
"v_xor_b32 v3,v3,v2\n"
"v_lshlrev_b32 v3,4,v3\n"
"v_lshl_add_u32 v3,v1,7,v3\n"
"v_add_u32 v80,s82,v3\n"
"v_xor_b32 v81,64,v80\n"
"v_add_u32 v82,65536,v80\n"
"v_add_u32 v83,65536,v81\n"
"v_bfe_u32 v3,v0,2,2\n"
"v_lshl_add_u32 v4,v2,3,v3\n"
"v_lshlrev_b32 v4,8,v4\n"
"v_and_b32 v5,1,v2\n"
"v_lshl_add_u32 v5,v5,2,v3\n"
"v_lshlrev_b32 v5,5,v5\n"
"v_and_b32 v6,3,v0\n"
"v_lshl_add_u32 v4,v6,3,v4\n"
"s_and_b32 s82,s52,1\n"
"s_lshl_b32 s82,s82,13\n"
"s_add_u32 s82,s82,98304\n"
"v_add_u32 v4,s82,v4\n"
"v_xor_b32 v6,0,v5\n"
"v_add_u32 v98,v4,v6\n"
"v_xor_b32 v6,32,v5\n"
"v_add_u32 v99,v4,v6\n"
"v_xor_b32 v6,64,v5\n"
"v_add_u32 v100,v4,v6\n"
"v_xor_b32 v6,96,v5\n"
"v_add_u32 v101,v4,v6\n"
"v_xor_b32 v6,128,v5\n"
"v_add_u32 v102,v4,v6\n"
"v_xor_b32 v6,160,v5\n"
"v_add_u32 v103,v4,v6\n"
"v_xor_b32 v6,192,v5\n"
"v_add_u32 v104,v4,v6\n"
"v_xor_b32 v6,224,v5\n"
"v_add_u32 v105,v4,v6\n"
"s_and_b32 s53,s89,1\n"
"s_lshl_b32 s83,s88,10\n"
"s_and_b32 s82,s93,63\n"
"s_add_u32 s82,s82,128\n"
"s_lshl_b32 s82,s82,13\n"
"s_add_u32 s82,s82,s83\n"
"s_add_u32 s36,s36,s82\n"
"s_addc_u32 s37,s37,0\n"
"s_mov_b32 s38,2622464\n"
"s_mov_b32 s39,0x20000\n"
"s_lshl_b32 s82,s93,13\n"
"s_add_u32 s82,s82,s83\n"
"s_add_u32 s40,s40,s82\n"
"s_addc_u32 s41,s41,0\n"
"s_mov_b32 s42,7341056\n"
"s_mov_b32 s43,0x20000\n"
"s_cmp_lg_u32 s53,0\n"
"s_cbranch_scc1 .Lfk_v\n"
"s_waitcnt vmcnt(11)\n"
"s_barrier\n"
"ds_read_b128 v[152:155],v80 offset:12288\n"
"ds_read_b128 v[156:159],v80 offset:14336\n"
"ds_read_b64_tr_b16 v[160:161],v98 offset:49152\n"
"ds_read_b64_tr_b16 v[162:163],v98 offset:50176\n"
"ds_read_b64_tr_b16 v[164:165],v99 offset:49152\n"
"ds_read_b64_tr_b16 v[166:167],v99 offset:50176\n"
"ds_read_b64_tr_b16 v[168:169],v100 offset:49152\n"
"ds_read_b64_tr_b16 v[170:171],v100 offset:50176\n"
"ds_read_b64_tr_b16 v[172:173],v101 offset:49152\n"
"ds_read_b64_tr_b16 v[174:175],v101 offset:50176\n"
"ds_read_b64_tr_b16 v[176:177],v102 offset:49152\n"
"ds_read_b64_tr_b16 v[178:179],v102 offset:50176\n"
"ds_read_b64_tr_b16 v[180:181],v103 offset:49152\n"
"ds_read_b64_tr_b16 v[182:183],v103 offset:50176\n"
"ds_read_b64_tr_b16 v[184:185],v104 offset:49152\n"
"ds_read_b64_tr_b16 v[186:187],v104 offset:50176\n"
"ds_read_b64_tr_b16 v[188:189],v105 offset:49152\n"
"ds_read_b64_tr_b16 v[190:191],v105 offset:50176\n"
"s_waitcnt lgkmcnt(12)\n"
"v_mfma_f32_16x16x32_bf16 a[192:195],v[160:163],v[152:155],0\n"
"s_and_b32 s91,s89,7\n"
"s_mov_b32 s76,128\n"
"s_add_u32 m0,s79,32768\n"
"v_mfma_f32_16x16x32_bf16 a[224:227],v[160:163],v[156:159],0\n"
"buffer_load_dwordx4 v106,s[16:19],s76 offen offset:0 lds\n"
"v_mfma_f32_16x16x32_bf16 a[228:231],v[164:167],v[156:159],0\n"
"s_lshr_b32 s97,s89,3\n"
"s_lshl2_add_u32 s90,s88,s97\n"
"v_mfma_f32_16x16x32_bf16 a[196:199],v[164:167],v[152:155],0\n"
"buffer_load_dwordx4 v107,s[16:19],s76 offen offset:1024 lds\n"
"s_waitcnt lgkmcnt(8)\n"
"v_mfma_f32_16x16x32_bf16 a[200:203],v[168:171],v[152:155],0\n"
"s_lshl_b32 vcc_lo,s90,8\n"
"s_add_u32 s28,s28,vcc_lo\n"
"s_addc_u32 s29,s29,0\n"
"v_mfma_f32_16x16x32_bf16 a[232:235],v[168:171],v[156:159],0\n"
"buffer_load_dwordx4 v108,s[16:19],s76 offen offset:2048 lds\n"
"v_mfma_f32_16x16x32_bf16 a[236:239],v[172:175],v[156:159],0\n"
"s_sub_u32 s30,8388608,vcc_lo\n"
"s_mov_b32 s31,0x20000\n"
"buffer_load_dwordx4 v109,s[16:19],s76 offen offset:3072 lds\n"
"v_mfma_f32_16x16x32_bf16 a[204:207],v[172:175],v[152:155],0\n"
"s_waitcnt vmcnt(9)\n"
"s_barrier\n"
"s_waitcnt lgkmcnt(4)\n"
"v_mfma_f32_16x16x32_bf16 a[208:211],v[176:179],v[152:155],0\n"
"ds_read_b128 v[128:131],v80 offset:0\n"
"ds_read_b128 v[192:195],v81 offset:0\n"
"v_mfma_f32_16x16x32_bf16 a[240:243],v[176:179],v[156:159],0\n"
"ds_read_b64_tr_b16 v[224:225],v98 offset:32768\n"
"ds_read_b64_tr_b16 v[226:227],v98 offset:33792\n"
"v_mfma_f32_16x16x32_bf16 a[244:247],v[180:183],v[156:159],0\n"
"ds_read_b64_tr_b16 v[228:229],v99 offset:32768\n"
"ds_read_b64_tr_b16 v[230:231],v99 offset:33792\n"
"v_mfma_f32_16x16x32_bf16 a[212:215],v[180:183],v[152:155],0\n"
"ds_read_b64_tr_b16 v[232:233],v100 offset:32768\n"
"ds_read_b64_tr_b16 v[234:235],v100 offset:33792\n"
"s_waitcnt lgkmcnt(8)\n"
"v_mfma_f32_16x16x32_bf16 a[216:219],v[184:187],v[152:155],0\n"
"ds_read_b64_tr_b16 v[236:237],v101 offset:32768\n"
"ds_read_b64_tr_b16 v[238:239],v101 offset:33792\n"
"v_mfma_f32_16x16x32_bf16 a[248:251],v[184:187],v[156:159],0\n"
"ds_read_b64_tr_b16 v[240:241],v102 offset:32768\n"
"ds_read_b64_tr_b16 v[242:243],v102 offset:33792\n"
"v_mfma_f32_16x16x32_bf16 a[252:255],v[188:191],v[156:159],0\n"
"ds_read_b64_tr_b16 v[244:245],v103 offset:32768\n"
"ds_read_b64_tr_b16 v[246:247],v103 offset:33792\n"
"v_mfma_f32_16x16x32_bf16 a[220:223],v[188:191],v[152:155],0\n"
"ds_read_b64_tr_b16 v[248:249],v104 offset:32768\n"
"ds_read_b64_tr_b16 v[250:251],v104 offset:33792\n"
"s_waitcnt lgkmcnt(10)\n"
"v_mfma_f32_16x16x32_bf16 a[0:3],v[160:163],v[128:131],0\n"
"ds_read_b64_tr_b16 v[252:253],v105 offset:32768\n"
"ds_read_b64_tr_b16 v[254:255],v105 offset:33792\n"
"v_mfma_f32_16x16x32_bf16 a[0:3],v[224:227],v[192:195],a[0:3]\n"
"ds_read_b128 v[132:135],v80 offset:2048\n"
"ds_read_b128 v[196:199],v81 offset:2048\n"
"v_mfma_f32_16x16x32_bf16 a[4:7],v[228:231],v[192:195],0\n"
"ds_read_b128 v[216:219],v81 offset:12288\n"
"ds_read_b128 v[220:223],v81 offset:14336\n"
"v_mfma_f32_16x16x32_bf16 a[4:7],v[164:167],v[128:131],a[4:7]\n"
"s_waitcnt lgkmcnt(12)\n"
"s_mov_b32 s76,262272\n"
"s_add_u32 m0,s79,36864\n"
"v_mfma_f32_16x16x32_bf16 a[8:11],v[168:171],v[128:131],0\n"
"buffer_load_dwordx4 v106,s[16:19],s76 offen offset:0 lds\n"
"v_mfma_f32_16x16x32_bf16 a[8:11],v[232:235],v[192:195],a[8:11]\n"
"buffer_load_dwordx4 v107,s[16:19],s76 offen offset:1024 lds\n"
"v_mfma_f32_16x16x32_bf16 a[12:15],v[236:239],v[192:195],0\n"
"buffer_load_dwordx4 v108,s[16:19],s76 offen offset:2048 lds\n"
"buffer_load_dwordx4 v109,s[16:19],s76 offen offset:3072 lds\n"
"v_mfma_f32_16x16x32_bf16 a[12:15],v[172:175],v[128:131],a[12:15]\n"
"s_waitcnt lgkmcnt(8)\n"
"s_mov_b32 s77,524288\n"
"s_add_u32 m0,s78,98304\n"
"v_mfma_f32_16x16x32_bf16 a[16:19],v[176:179],v[128:131],0\n"
"buffer_load_dwordx4 v110,s[20:23],s77 offen offset:0 lds\n"
"v_mfma_f32_16x16x32_bf16 a[16:19],v[240:243],v[192:195],a[16:19]\n"
"buffer_load_dwordx4 v111,s[20:23],s77 offen offset:1024 lds\n"
"v_mfma_f32_16x16x32_bf16 a[20:23],v[244:247],v[192:195],0\n"
"buffer_load_dwordx4 v113,s[20:23],s77 offen offset:2048 lds\n"
"buffer_load_dwordx4 v114,s[20:23],s77 offen offset:3072 lds\n"
"v_mfma_f32_16x16x32_bf16 a[20:23],v[180:183],v[128:131],a[20:23]\n"
"s_waitcnt lgkmcnt(4)\n"
"s_mov_b32 s77,786432\n"
"s_add_u32 m0,s78,114688\n"
"v_mfma_f32_16x16x32_bf16 a[24:27],v[184:187],v[128:131],0\n"
"buffer_load_dwordx4 v110,s[20:23],s77 offen offset:0 lds\n"
"v_mfma_f32_16x16x32_bf16 a[24:27],v[248:251],v[192:195],a[24:27]\n"
"buffer_load_dwordx4 v111,s[20:23],s77 offen offset:1024 lds\n"
"v_mfma_f32_16x16x32_bf16 a[28:31],v[252:255],v[192:195],0\n"
"buffer_load_dwordx4 v113,s[20:23],s77 offen offset:2048 lds\n"
"buffer_load_dwordx4 v114,s[20:23],s77 offen offset:3072 lds\n"
"v_mfma_f32_16x16x32_bf16 a[28:31],v[188:191],v[128:131],a[28:31]\n"
"s_waitcnt lgkmcnt(2)\n"
"s_mov_b32 s76,256\n"
"s_add_u32 m0,s79,65536\n"
"v_mfma_f32_16x16x32_bf16 a[32:35],v[160:163],v[132:135],0\n"
"buffer_load_dwordx4 v106,s[16:19],s76 offen offset:0 lds\n"
"v_mfma_f32_16x16x32_bf16 a[32:35],v[224:227],v[196:199],a[32:35]\n"
"buffer_load_dwordx4 v107,s[16:19],s76 offen offset:1024 lds\n"
"v_mfma_f32_16x16x32_bf16 a[36:39],v[228:231],v[196:199],0\n"
"buffer_load_dwordx4 v108,s[16:19],s76 offen offset:2048 lds\n"
"buffer_load_dwordx4 v109,s[16:19],s76 offen offset:3072 lds\n"
"v_mfma_f32_16x16x32_bf16 a[36:39],v[164:167],v[132:135],a[36:39]\n"
"s_waitcnt vmcnt(21)\n"
"s_barrier\n"
"v_mfma_f32_16x16x32_bf16 a[40:43],v[168:171],v[132:135],0\n"
"ds_read_b128 v[136:139],v80 offset:4096\n"
"ds_read_b128 v[200:203],v81 offset:4096\n"
"v_mfma_f32_16x16x32_bf16 a[40:43],v[232:235],v[196:199],a[40:43]\n"
"ds_read_b128 v[140:143],v80 offset:6144\n"
"ds_read_b128 v[204:207],v81 offset:6144\n"
"v_mfma_f32_16x16x32_bf16 a[44:47],v[236:239],v[196:199],0\n"
"ds_read_b128 v[144:147],v80 offset:8192\n"
"ds_read_b128 v[208:211],v81 offset:8192\n"
"v_mfma_f32_16x16x32_bf16 a[44:47],v[172:175],v[132:135],a[44:47]\n"
"ds_read_b128 v[148:151],v80 offset:10240\n"
"ds_read_b128 v[212:215],v81 offset:10240\n"
"v_mfma_f32_16x16x32_bf16 a[48:51],v[176:179],v[132:135],0\n"
"s_mov_b32 s60,s91\n"
"s_mov_b32 s76,262400\n"
"s_add_u32 m0,s79,69632\n"
"v_mfma_f32_16x16x32_bf16 a[48:51],v[240:243],v[196:199],a[48:51]\n"
"buffer_load_dwordx4 v106,s[16:19],s76 offen offset:0 lds\n"
"v_mfma_f32_16x16x32_bf16 a[52:55],v[244:247],v[196:199],0\n"
"buffer_load_dwordx4 v107,s[16:19],s76 offen offset:1024 lds\n"
"v_mfma_f32_16x16x32_bf16 a[52:55],v[180:183],v[132:135],a[52:55]\n"
"buffer_load_dwordx4 v108,s[16:19],s76 offen offset:2048 lds\n"
"v_mfma_f32_16x16x32_bf16 a[56:59],v[184:187],v[132:135],0\n"
"buffer_load_dwordx4 v109,s[16:19],s76 offen offset:3072 lds\n"
"v_mfma_f32_16x16x32_bf16 a[56:59],v[248:251],v[196:199],a[56:59]\n"
"s_sub_u32 s94,15,s91\n"
"s_waitcnt lgkmcnt(0)\n"
"s_barrier\n"
"v_mfma_f32_16x16x32_bf16 a[60:63],v[252:255],v[196:199],0\n"
"s_cmp_lt_u32 s52,2\n"
"s_cselect_b32 s95,s94,s91\n"
"s_mov_b32 s77,1048576\n"
"s_add_u32 m0,s78,147456\n"
"v_mfma_f32_16x16x32_bf16 a[60:63],v[188:191],v[132:135],a[60:63]\n"
"buffer_load_dwordx4 v110,s[20:23],s77 offen offset:0 lds\n"
"v_mfma_f32_16x16x32_bf16 a[64:67],v[160:163],v[136:139],0\n"
"s_lshr_b32 vcc_lo,s95,1\n"
"s_lshl_b32 s55,vcc_lo,19\n"
"v_mfma_f32_16x16x32_bf16 a[64:67],v[224:227],v[200:203],a[64:67]\n"
"s_lshl_b32 s58,vcc_lo,10\n"
"s_add_u32 s58,s58,148898816\n"
"v_mfma_f32_16x16x32_bf16 a[68:71],v[228:231],v[200:203],0\n"
"s_and_b32 vcc_lo,s95,1\n"
"s_lshl_b32 vcc_lo,vcc_lo,13\n"
"v_mfma_f32_16x16x32_bf16 a[68:71],v[164:167],v[136:139],a[68:71]\n"
"buffer_load_dwordx4 v111,s[20:23],s77 offen offset:1024 lds\n"
"v_mfma_f32_16x16x32_bf16 a[72:75],v[168:171],v[136:139],0\n"
"s_and_b32 vcc_hi,s78,4096\n"
"s_add_u32 s55,s55,vcc_lo\n"
"v_mfma_f32_16x16x32_bf16 a[72:75],v[232:235],v[200:203],a[72:75]\n"
"s_add_u32 s55,s55,vcc_hi\n"
"s_and_b32 vcc_lo,s89,2\n"
"v_mfma_f32_16x16x32_bf16 a[76:79],v[236:239],v[200:203],0\n"
"s_lshl2_add_u32 vcc_lo,s88,vcc_lo\n"
"s_and_b32 vcc_hi,s52,1\n"
"v_mfma_f32_16x16x32_bf16 a[76:79],v[172:175],v[136:139],a[76:79]\n"
"buffer_load_dwordx4 v113,s[20:23],s77 offen offset:2048 lds\n"
"v_mfma_f32_16x16x32_bf16 a[80:83],v[176:179],v[136:139],0\n"
"s_add_u32 vcc_lo,vcc_lo,vcc_hi\n"
"s_lshr_b32 vcc_hi,s89,2\n"
"v_mfma_f32_16x16x32_bf16 a[80:83],v[240:243],v[200:203],a[80:83]\n"
"s_lshr_b32 s84,s52,1\n"
"s_lshl1_add_u32 vcc_hi,vcc_hi,s84\n"
"v_mfma_f32_16x16x32_bf16 a[84:87],v[244:247],v[200:203],0\n"
"s_mul_i32 s56,vcc_lo,1179648\n"
"s_lshl_b32 s84,s84,19\n"
"v_mfma_f32_16x16x32_bf16 a[84:87],v[180:183],v[136:139],a[84:87]\n"
"buffer_load_dwordx4 v114,s[20:23],s77 offen offset:3072 lds\n"
"v_mfma_f32_16x16x32_bf16 a[88:91],v[184:187],v[136:139],0\n"
"s_add_u32 s56,s56,s84\n"
"s_lshr_b32 s84,s89,2\n"
"v_mfma_f32_16x16x32_bf16 a[88:91],v[248:251],v[200:203],a[88:91]\n"
"s_lshl_b32 s84,s84,14\n"
"s_add_u32 s56,s56,s84\n"
"v_mfma_f32_16x16x32_bf16 a[92:95],v[252:255],v[200:203],0\n"
"s_lshl_b32 s84,s53,13\n"
"s_mov_b32 s77,1310720\n"
"s_add_u32 m0,s78,131072\n"
"v_mfma_f32_16x16x32_bf16 a[92:95],v[188:191],v[136:139],a[92:95]\n"
"buffer_load_dwordx4 v110,s[20:23],s77 offen offset:0 lds\n"
"v_mfma_f32_16x16x32_bf16 a[96:99],v[160:163],v[140:143],0\n"
"s_add_u32 s56,s56,s84\n"
"s_add_u32 s56,s56,4194304\n"
"v_mfma_f32_16x16x32_bf16 a[96:99],v[224:227],v[204:207],a[96:99]\n"
"s_lshl4_add_u32 vcc_hi,s53,vcc_hi\n"
"s_lshl_b32 vcc_lo,vcc_lo,8\n"
"v_mfma_f32_16x16x32_bf16 a[100:103],v[228:231],v[204:207],0\n"
"buffer_load_dwordx4 v111,s[20:23],s77 offen offset:1024 lds\n"
"v_mfma_f32_16x16x32_bf16 a[100:103],v[164:167],v[140:143],a[100:103]\n"
"s_lshl3_add_u32 s59,vcc_hi,vcc_lo\n"
"s_add_u32 s59,s59,148914176\n"
"v_mfma_f32_16x16x32_bf16 a[104:107],v[168:171],v[140:143],0\n"
"s_lshl_b32 s57,s92,3\n"
"s_add_u32 s57,s57,148898816\n"
"v_mfma_f32_16x16x32_bf16 a[104:107],v[232:235],v[204:207],a[104:107]\n"
"s_lshl_b32 s63,s90,8\n"
"s_add_u32 s63,s63,148914176\n"
"v_mfma_f32_16x16x32_bf16 a[108:111],v[236:239],v[204:207],0\n"
"buffer_load_dwordx4 v113,s[20:23],s77 offen offset:2048 lds\n"
"v_mfma_f32_16x16x32_bf16 a[108:111],v[172:175],v[140:143],a[108:111]\n"
"s_lshl_b32 s74,s92,11\n"
"s_add_u32 s74,s74,171966464\n"
"v_mfma_f32_16x16x32_bf16 a[112:115],v[176:179],v[140:143],0\n"
"s_mul_i32 s96,s90,1179648\n"
"s_add_u32 s96,s96,4194304\n"
"v_mfma_f32_16x16x32_bf16 a[112:115],v[240:243],v[204:207],a[112:115]\n"
"s_add_u32 s73,s96,s85\n"
"s_and_b32 vcc_hi,s78,4096\n"
"v_mfma_f32_16x16x32_bf16 a[116:119],v[244:247],v[204:207],0\n"
"buffer_load_dwordx4 v114,s[20:23],s77 offen offset:3072 lds\n"
"v_mfma_f32_16x16x32_bf16 a[116:119],v[180:183],v[140:143],a[116:119]\n"
"s_lshl_b32 vcc_hi,vcc_hi,1\n"
"s_add_u32 vcc_lo,s96,vcc_hi\n"
"v_mfma_f32_16x16x32_bf16 a[120:123],v[184:187],v[140:143],0\n"
"s_add_u32 s65,vcc_lo,524288\n"
"s_lshl_b32 s66,s91,16\n"
"v_mfma_f32_16x16x32_bf16 a[120:123],v[248:251],v[204:207],a[120:123]\n"
"s_sub_u32 s66,vcc_lo,s66\n"
"s_mov_b32 s76,384\n"
"s_add_u32 m0,s79,0\n"
"v_mfma_f32_16x16x32_bf16 a[124:127],v[252:255],v[204:207],0\n"
"buffer_load_dwordx4 v106,s[16:19],s76 offen offset:0 lds\n"
"v_mfma_f32_16x16x32_bf16 a[124:127],v[188:191],v[140:143],a[124:127]\n"
"s_add_u32 s66,s66,999424\n"
"s_add_u32 s67,vcc_hi,65536\n"
"v_mfma_f32_16x16x32_bf16 a[128:131],v[160:163],v[144:147],0\n"
"s_mul_i32 vcc_lo,vcc_hi,5\n"
"s_sub_u32 vcc_lo,s96,vcc_lo\n"
"v_mfma_f32_16x16x32_bf16 a[128:131],v[224:227],v[208:211],a[128:131]\n"
"s_add_u32 s64,vcc_lo,81920\n"
"s_add_u32 s62,vcc_lo,135168\n"
"s_add_u32 s61,vcc_lo,524288\n"
"v_mfma_f32_16x16x32_bf16 a[132:135],v[228:231],v[208:211],0\n"
"buffer_load_dwordx4 v107,s[16:19],s76 offen offset:1024 lds\n"
"v_mfma_f32_16x16x32_bf16 a[132:135],v[164:167],v[144:147],a[132:135]\n"
"s_cmp_lt_u32 s52,2\n"
"s_cselect_b32 s64,s64,2146435072\n"
"s_cselect_b32 s67,s67,163840\n"
"s_cselect_b32 s65,s65,1\n"
"v_mfma_f32_16x16x32_bf16 a[136:139],v[168:171],v[144:147],0\n"
"s_cselect_b32 vcc_lo,4,0\n"
"s_and_b32 vcc_hi,s52,1\n"
"s_add_u32 vcc_lo,vcc_lo,vcc_hi\n"
"v_mfma_f32_16x16x32_bf16 a[136:139],v[232:235],v[208:211],a[136:139]\n"
"s_lshl_b32 vcc_hi,s90,12\n"
"s_add_u32 vcc_hi,vcc_hi,54525952\n"
"v_mfma_f32_16x16x32_bf16 a[140:143],v[236:239],v[208:211],0\n"
"buffer_load_dwordx4 v108,s[16:19],s76 offen offset:2048 lds\n"
"v_mfma_f32_16x16x32_bf16 a[140:143],v[172:175],v[144:147],a[140:143]\n"
"s_lshl3_add_u32 s70,s95,vcc_lo\n"
"s_lshl_b32 s70,s70,17\n"
"v_mfma_f32_16x16x32_bf16 a[144:147],v[176:179],v[144:147],0\n"
"s_add_u32 s70,s70,vcc_hi\n"
"s_lshl3_add_u32 s68,s94,vcc_lo\n"
"v_mfma_f32_16x16x32_bf16 a[144:147],v[240:243],v[208:211],a[144:147]\n"
"s_lshl_b32 s68,s68,17\n"
"s_add_u32 s68,s68,vcc_hi\n"
"v_mfma_f32_16x16x32_bf16 a[148:151],v[244:247],v[208:211],0\n"
"buffer_load_dwordx4 v109,s[16:19],s76 offen offset:3072 lds\n"
"v_mfma_f32_16x16x32_bf16 a[148:151],v[180:183],v[144:147],a[148:151]\n"
"s_lshr_b32 vcc_lo,s90,3\n"
"s_lshl2_add_u32 vcc_lo,vcc_lo,s52\n"
"v_mfma_f32_16x16x32_bf16 a[152:155],v[184:187],v[144:147],0\n"
"s_lshl_b32 vcc_lo,vcc_lo,7\n"
"s_and_b32 vcc_hi,s90,7\n"
"v_mfma_f32_16x16x32_bf16 a[152:155],v[248:251],v[208:211],a[152:155]\n"
"s_add_u32 vcc_lo,vcc_lo,vcc_hi\n"
"s_mov_b32 s76,262528\n"
"s_add_u32 m0,s79,4096\n"
"v_mfma_f32_16x16x32_bf16 a[156:159],v[252:255],v[208:211],0\n"
"buffer_load_dwordx4 v106,s[16:19],s76 offen offset:0 lds\n"
"v_mfma_f32_16x16x32_bf16 a[156:159],v[188:191],v[144:147],a[156:159]\n"
"s_lshl3_add_u32 s69,s91,vcc_lo\n"
"s_lshl_b32 s69,s69,3\n"
"v_mfma_f32_16x16x32_bf16 a[160:163],v[160:163],v[148:151],0\n"
"s_add_u32 s69,s69,148930560\n"
"s_lshl3_add_u32 s71,s94,vcc_lo\n"
"v_mfma_f32_16x16x32_bf16 a[160:163],v[224:227],v[212:215],a[160:163]\n"
"s_lshl_b32 s71,s71,3\n"
"s_add_u32 s71,s71,148930560\n"
"v_mfma_f32_16x16x32_bf16 a[164:167],v[228:231],v[212:215],0\n"
"buffer_load_dwordx4 v107,s[16:19],s76 offen offset:1024 lds\n"
"v_mfma_f32_16x16x32_bf16 a[164:167],v[164:167],v[148:151],a[164:167]\n"
"s_mov_b32 s6,8388608\n"
"s_mov_b32 s7,0x20000\n"
"v_mfma_f32_16x16x32_bf16 a[168:171],v[168:171],v[148:151],0\n"
"s_lshr_b32 s94,s88,1\n"
"s_and_b32 vcc_lo,s91,1\n"
"v_mfma_f32_16x16x32_bf16 a[168:171],v[232:235],v[212:215],a[168:171]\n"
"s_lshl1_add_u32 s95,s97,vcc_lo\n"
"s_and_b32 vcc_lo,s88,1\n"
"v_mfma_f32_16x16x32_bf16 a[172:175],v[236:239],v[212:215],0\n"
"buffer_load_dwordx4 v108,s[16:19],s76 offen offset:2048 lds\n"
"v_mfma_f32_16x16x32_bf16 a[172:175],v[172:175],v[148:151],a[172:175]\n"
"s_lshr_b32 vcc_hi,s91,1\n"
"s_lshl2_add_u32 s96,vcc_lo,vcc_hi\n"
"v_mfma_f32_16x16x32_bf16 a[176:179],v[176:179],v[148:151],0\n"
"s_lshr_b32 vcc_lo,s88,2\n"
"s_lshl2_add_u32 s96,vcc_lo,vcc_hi\n"
"v_mfma_f32_16x16x32_bf16 a[176:179],v[240:243],v[212:215],a[176:179]\n"
"s_sub_u32 vcc_lo,7,s88\n"
"s_min_u32 s94,vcc_lo,s88\n"
"v_mfma_f32_16x16x32_bf16 a[180:183],v[244:247],v[212:215],0\n"
"buffer_load_dwordx4 v109,s[16:19],s76 offen offset:3072 lds\n"
"v_mfma_f32_16x16x32_bf16 a[180:183],v[180:183],v[148:151],a[180:183]\n"
"s_lshr_b32 s94,s88,1\n"
"s_xor_b32 s94,s94,2\n"
"s_lshl_b32 s96,s88,2\n"
"v_mfma_f32_16x16x32_bf16 a[184:187],v[184:187],v[148:151],0\n"
"buffer_load_dwordx4 v[8:11],v70,s[32:35],0 offen offset:0\n"
"v_mfma_f32_16x16x32_bf16 a[184:187],v[248:251],v[212:215],a[184:187]\n"
"buffer_load_dwordx4 v[12:15],v70,s[32:35],0 offen offset:512\n"
"s_and_b32 s96,s96,4\n"
"v_mfma_f32_16x16x32_bf16 a[188:191],v[252:255],v[212:215],0\n"
"buffer_load_dwordx4 v[16:19],v70,s[32:35],0 offen offset:1024\n"
"buffer_load_dwordx4 v[20:23],v70,s[32:35],0 offen offset:1536\n"
"s_add_u32 s96,s96,s97\n"
"v_mfma_f32_16x16x32_bf16 a[188:191],v[188:191],v[148:151],a[188:191]\n"
"s_waitcnt vmcnt(28)\n"
"s_barrier\n"
"v_mfma_f32_16x16x32_bf16 a[192:195],v[224:227],v[216:219],a[192:195]\n"
"ds_read_b128 v[152:155],v80 offset:45056\n"
"s_mov_b32 s87,524288\n"
"v_mfma_f32_16x16x32_bf16 a[224:227],v[224:227],v[220:223],a[224:227]\n"
"ds_read_b128 v[156:159],v80 offset:47104\n"
"buffer_load_dword v115,v112,s[36:39],s87 offen\n"
"v_mfma_f32_16x16x32_bf16 a[228:231],v[228:231],v[220:223],a[228:231]\n"
"ds_read_b64_tr_b16 v[160:161],v98 offset:0\n"
"s_mov_b32 s87,1048576\n"
"v_mfma_f32_16x16x32_bf16 a[196:199],v[228:231],v[216:219],a[196:199]\n"
"ds_read_b64_tr_b16 v[162:163],v98 offset:1024\n"
"buffer_load_dword v115,v112,s[36:39],s87 offen\n"
"v_mfma_f32_16x16x32_bf16 a[200:203],v[232:235],v[216:219],a[200:203]\n"
"ds_read_b64_tr_b16 v[164:165],v99 offset:0\n"
"s_mov_b32 s87,1572864\n"
"v_mfma_f32_16x16x32_bf16 a[232:235],v[232:235],v[220:223],a[232:235]\n"
"ds_read_b64_tr_b16 v[166:167],v99 offset:1024\n"
"buffer_load_dword v115,v112,s[36:39],s87 offen\n"
"v_mfma_f32_16x16x32_bf16 a[236:239],v[236:239],v[220:223],a[236:239]\n"
"ds_read_b64_tr_b16 v[168:169],v100 offset:0\n"
"s_add_u32 s95,s91,s96\n"
"v_mfma_f32_16x16x32_bf16 a[204:207],v[236:239],v[216:219],a[204:207]\n"
"ds_read_b64_tr_b16 v[170:171],v100 offset:1024\n"
"s_and_b32 s95,s95,7\n"
"v_mfma_f32_16x16x32_bf16 a[208:211],v[240:243],v[216:219],a[208:211]\n"
"ds_read_b64_tr_b16 v[172:173],v101 offset:0\n"
"s_lshl3_add_u32 vcc_lo,s94,s96\n"
"v_mfma_f32_16x16x32_bf16 a[240:243],v[240:243],v[220:223],a[240:243]\n"
"ds_read_b64_tr_b16 v[174:175],v101 offset:1024\n"
"s_lshl3_add_u32 vcc_lo,vcc_lo,s52\n"
"v_mfma_f32_16x16x32_bf16 a[244:247],v[244:247],v[220:223],a[244:247]\n"
"ds_read_b64_tr_b16 v[176:177],v102 offset:0\n"
"s_lshl_b32 s13,vcc_lo,15\n"
"v_mfma_f32_16x16x32_bf16 a[212:215],v[244:247],v[216:219],a[212:215]\n"
"ds_read_b64_tr_b16 v[178:179],v102 offset:1024\n"
"s_lshl_b32 vcc_lo,s95,8\n"
"v_mfma_f32_16x16x32_bf16 a[216:219],v[248:251],v[216:219],a[216:219]\n"
"ds_read_b64_tr_b16 v[180:181],v103 offset:0\n"
"s_add_u32 s13,s13,vcc_lo\n"
"v_mfma_f32_16x16x32_bf16 a[248:251],v[248:251],v[220:223],a[248:251]\n"
"ds_read_b64_tr_b16 v[182:183],v103 offset:1024\n"
"s_lshl3_add_u32 vcc_hi,s94,s95\n"
"v_mfma_f32_16x16x32_bf16 a[252:255],v[252:255],v[220:223],a[252:255]\n"
"ds_read_b64_tr_b16 v[184:185],v104 offset:0\n"
"s_lshl_b32 s72,vcc_hi,8\n"
"v_mfma_f32_16x16x32_bf16 a[220:223],v[252:255],v[216:219],a[220:223]\n"
"s_add_u32 s72,s72,149061632\n"
"ds_read_b64_tr_b16 v[186:187],v104 offset:1024\n"
"s_waitcnt lgkmcnt(10)\n"
"v_mfma_f32_16x16x32_bf16 a[192:195],v[160:163],v[152:155],a[192:195]\n"
"s_or_b32 vcc_lo,s96,s52\n"
"ds_read_b64_tr_b16 v[188:189],v105 offset:0\n"
"s_cselect_b32 s72,2146435072,s72\n"
"v_mfma_f32_16x16x32_bf16 a[224:227],v[160:163],v[156:159],a[224:227]\n"
"ds_read_b64_tr_b16 v[190:191],v105 offset:1024\n"
"s_lshl_b32 s3,vcc_hi,18\n"
"v_mfma_f32_16x16x32_bf16 a[228:231],v[164:167],v[156:159],a[228:231]\n"
"ds_read_b128 v[128:131],v80 offset:32768\n"
"s_lshl_b32 vcc_lo,s96,15\n"
"v_mfma_f32_16x16x32_bf16 a[196:199],v[164:167],v[152:155],a[196:199]\n"
"ds_read_b128 v[192:195],v81 offset:32768\n"
"s_waitcnt lgkmcnt(10)\n"
"v_mfma_f32_16x16x32_bf16 a[200:203],v[168:171],v[152:155],a[200:203]\n"
"ds_read_b64_tr_b16 v[224:225],v98 offset:16384\n"
"s_sub_u32 s3,s3,vcc_lo\n"
"v_mfma_f32_16x16x32_bf16 a[232:235],v[168:171],v[156:159],a[232:235]\n"
"ds_read_b64_tr_b16 v[226:227],v98 offset:17408\n"
"s_add_u32 s3,s3,s78\n"
"v_mfma_f32_16x16x32_bf16 a[236:239],v[172:175],v[156:159],a[236:239]\n"
"ds_read_b64_tr_b16 v[228:229],v99 offset:16384\n"
"s_add_u32 s3,s3,100909056\n"
"v_mfma_f32_16x16x32_bf16 a[204:207],v[172:175],v[152:155],a[204:207]\n"
"ds_read_b64_tr_b16 v[230:231],v99 offset:17408\n"
"s_waitcnt lgkmcnt(10)\n"
"v_mfma_f32_16x16x32_bf16 a[208:211],v[176:179],v[152:155],a[208:211]\n"
"ds_read_b64_tr_b16 v[232:233],v100 offset:16384\n"
"s_lshl3_add_u32 vcc_hi,vcc_hi,s96\n"
"v_mfma_f32_16x16x32_bf16 a[240:243],v[176:179],v[156:159],a[240:243]\n"
"ds_read_b64_tr_b16 v[234:235],v100 offset:17408\n"
"s_lshl2_add_u32 vcc_hi,vcc_hi,s52\n"
"v_mfma_f32_16x16x32_bf16 a[244:247],v[180:183],v[156:159],a[244:247]\n"
"ds_read_b64_tr_b16 v[236:237],v101 offset:16384\n"
"s_lshl_b32 s2,vcc_hi,4\n"
"v_mfma_f32_16x16x32_bf16 a[212:215],v[180:183],v[152:155],a[212:215]\n"
"ds_read_b64_tr_b16 v[238:239],v101 offset:17408\n"
"s_waitcnt lgkmcnt(10)\n"
"v_mfma_f32_16x16x32_bf16 a[216:219],v[184:187],v[152:155],a[216:219]\n"
"ds_read_b64_tr_b16 v[240:241],v102 offset:16384\n"
"s_add_u32 s2,s2,149028864\n"
"v_mfma_f32_16x16x32_bf16 a[248:251],v[184:187],v[156:159],a[248:251]\n"
"ds_read_b64_tr_b16 v[242:243],v102 offset:17408\n"
"s_lshl2_add_u32 vcc_lo,s95,s52\n"
"v_mfma_f32_16x16x32_bf16 a[252:255],v[188:191],v[156:159],a[252:255]\n"
"ds_read_b64_tr_b16 v[244:245],v103 offset:16384\n"
"s_lshl_b32 vcc_lo,vcc_lo,10\n"
"v_mfma_f32_16x16x32_bf16 a[220:223],v[188:191],v[152:155],a[220:223]\n"
"ds_read_b64_tr_b16 v[246:247],v103 offset:17408\n"
"s_waitcnt lgkmcnt(8)\n"
"v_mfma_f32_16x16x32_bf16 a[0:3],v[160:163],v[128:131],a[0:3]\n"
"ds_read_b64_tr_b16 v[248:249],v104 offset:16384\n"
"s_lshl_b32 vcc_hi,s94,22\n"
"v_mfma_f32_16x16x32_bf16 a[0:3],v[224:227],v[192:195],a[0:3]\n"
"ds_read_b64_tr_b16 v[250:251],v104 offset:17408\n"
"s_add_u32 s12,vcc_lo,vcc_hi\n"
"v_mfma_f32_16x16x32_bf16 a[4:7],v[228:231],v[192:195],a[4:7]\n"
"ds_read_b64_tr_b16 v[252:253],v105 offset:16384\n"
"s_lshl_b32 vcc_lo,s96,19\n"
"v_mfma_f32_16x16x32_bf16 a[4:7],v[164:167],v[128:131],a[4:7]\n"
"ds_read_b64_tr_b16 v[254:255],v105 offset:17408\n"
"s_waitcnt lgkmcnt(8)\n"
"v_mfma_f32_16x16x32_bf16 a[8:11],v[168:171],v[128:131],a[8:11]\n"
"ds_read_b128 v[132:135],v80 offset:34816\n"
"s_add_u32 s10,s12,vcc_lo\n"
"v_mfma_f32_16x16x32_bf16 a[8:11],v[232:235],v[192:195],a[8:11]\n"
"ds_read_b128 v[196:199],v81 offset:34816\n"
"s_add_u32 s10,s10,71303168\n"
"v_mfma_f32_16x16x32_bf16 a[12:15],v[236:239],v[192:195],a[12:15]\n"
"ds_read_b128 v[136:139],v80 offset:36864\n"
"s_mul_i32 vcc_lo,s52,31744\n"
"v_mfma_f32_16x16x32_bf16 a[12:15],v[172:175],v[128:131],a[12:15]\n"
"ds_read_b128 v[200:203],v81 offset:36864\n"
"s_waitcnt lgkmcnt(8)\n"
"v_mfma_f32_16x16x32_bf16 a[16:19],v[176:179],v[128:131],a[16:19]\n"
"ds_read_b128 v[140:143],v80 offset:38912\n"
"s_add_u32 s10,s10,vcc_lo\n"
"v_mfma_f32_16x16x32_bf16 a[16:19],v[240:243],v[192:195],a[16:19]\n"
"ds_read_b128 v[204:207],v81 offset:38912\n"
"s_lshl_b32 vcc_lo,s52,10\n"
"v_mfma_f32_16x16x32_bf16 a[20:23],v[244:247],v[192:195],a[20:23]\n"
"ds_read_b128 v[144:147],v80 offset:40960\n"
"s_sub_u32 s12,57671680,vcc_lo\n"
"v_mfma_f32_16x16x32_bf16 a[20:23],v[180:183],v[128:131],a[20:23]\n"
"ds_read_b128 v[208:211],v81 offset:40960\n"
"s_waitcnt lgkmcnt(8)\n"
"v_mfma_f32_16x16x32_bf16 a[24:27],v[184:187],v[128:131],a[24:27]\n"
"ds_read_b128 v[148:151],v80 offset:43008\n"
"s_add_u32 s8,s79,54525952\n"
"v_mfma_f32_16x16x32_bf16 a[24:27],v[248:251],v[192:195],a[24:27]\n"
"ds_read_b128 v[212:215],v81 offset:43008\n"
"s_and_b32 vcc_lo,s96,1\n"
"v_mfma_f32_16x16x32_bf16 a[28:31],v[252:255],v[192:195],a[28:31]\n"
"ds_read_b128 v[216:219],v81 offset:45056\n"
"s_lshl_b32 s11,vcc_lo,10\n"
"v_mfma_f32_16x16x32_bf16 a[28:31],v[188:191],v[128:131],a[28:31]\n"
"ds_read_b128 v[220:223],v81 offset:47104\n"
"s_waitcnt lgkmcnt(10)\n"
"v_mfma_f32_16x16x32_bf16 a[32:35],v[160:163],v[132:135],a[32:35]\n"
"s_add_u32 s11,s11,148932608\n"
"s_mov_b32 s9,148931072\n"
"v_mfma_f32_16x16x32_bf16 a[32:35],v[224:227],v[196:199],a[32:35]\n"
"s_cmp_eq_u32 s52,0\n"
"s_cselect_b32 s71,s71,2146435072\n"
"v_mfma_f32_16x16x32_bf16 a[36:39],v[228:231],v[196:199],a[36:39]\n"
"v_mfma_f32_16x16x32_bf16 a[36:39],v[164:167],v[132:135],a[36:39]\n"
"v_mfma_f32_16x16x32_bf16 a[40:43],v[168:171],v[132:135],a[40:43]\n"
"s_waitcnt lgkmcnt(0)\n"
"s_barrier\n"
"v_mfma_f32_16x16x32_bf16 a[40:43],v[232:235],v[196:199],a[40:43]\n"
"s_mov_b32 s77,1572864\n"
"s_add_u32 m0,s78,98304\n"
"v_mfma_f32_16x16x32_bf16 a[44:47],v[236:239],v[196:199],a[44:47]\n"
"buffer_load_dwordx4 v110,s[20:23],s77 offen offset:0 lds\n"
"v_mfma_f32_16x16x32_bf16 a[44:47],v[172:175],v[132:135],a[44:47]\n"
"v_mfma_f32_16x16x32_bf16 a[48:51],v[176:179],v[132:135],a[48:51]\n"
"v_mfma_f32_16x16x32_bf16 a[48:51],v[240:243],v[196:199],a[48:51]\n"
"v_mfma_f32_16x16x32_bf16 a[52:55],v[244:247],v[196:199],a[52:55]\n"
"buffer_load_dwordx4 v111,s[20:23],s77 offen offset:1024 lds\n"
"v_mfma_f32_16x16x32_bf16 a[52:55],v[180:183],v[132:135],a[52:55]\n"
"v_mfma_f32_16x16x32_bf16 a[56:59],v[184:187],v[132:135],a[56:59]\n"
"v_mfma_f32_16x16x32_bf16 a[56:59],v[248:251],v[196:199],a[56:59]\n"
"v_mfma_f32_16x16x32_bf16 a[60:63],v[252:255],v[196:199],a[60:63]\n"
"buffer_load_dwordx4 v113,s[20:23],s77 offen offset:2048 lds\n"
"v_mfma_f32_16x16x32_bf16 a[60:63],v[188:191],v[132:135],a[60:63]\n"
"v_mfma_f32_16x16x32_bf16 a[64:67],v[160:163],v[136:139],a[64:67]\n"
"v_mfma_f32_16x16x32_bf16 a[64:67],v[224:227],v[200:203],a[64:67]\n"
"v_mfma_f32_16x16x32_bf16 a[68:71],v[228:231],v[200:203],a[68:71]\n"
"buffer_load_dwordx4 v114,s[20:23],s77 offen offset:3072 lds\n"
"v_mfma_f32_16x16x32_bf16 a[68:71],v[164:167],v[136:139],a[68:71]\n"
"v_mfma_f32_16x16x32_bf16 a[72:75],v[168:171],v[136:139],a[72:75]\n"
"v_mfma_f32_16x16x32_bf16 a[72:75],v[232:235],v[200:203],a[72:75]\n"
"s_mov_b32 s77,1835008\n"
"s_add_u32 m0,s78,114688\n"
"v_mfma_f32_16x16x32_bf16 a[76:79],v[236:239],v[200:203],a[76:79]\n"
"buffer_load_dwordx4 v110,s[20:23],s77 offen offset:0 lds\n"
"v_mfma_f32_16x16x32_bf16 a[76:79],v[172:175],v[136:139],a[76:79]\n"
"v_mfma_f32_16x16x32_bf16 a[80:83],v[176:179],v[136:139],a[80:83]\n"
"v_mfma_f32_16x16x32_bf16 a[80:83],v[240:243],v[200:203],a[80:83]\n"
"v_mfma_f32_16x16x32_bf16 a[84:87],v[244:247],v[200:203],a[84:87]\n"
"buffer_load_dwordx4 v111,s[20:23],s77 offen offset:1024 lds\n"
"v_mfma_f32_16x16x32_bf16 a[84:87],v[180:183],v[136:139],a[84:87]\n"
"v_mfma_f32_16x16x32_bf16 a[88:91],v[184:187],v[136:139],a[88:91]\n"
"v_mfma_f32_16x16x32_bf16 a[88:91],v[248:251],v[200:203],a[88:91]\n"
"v_mfma_f32_16x16x32_bf16 a[92:95],v[252:255],v[200:203],a[92:95]\n"
"buffer_load_dwordx4 v113,s[20:23],s77 offen offset:2048 lds\n"
"v_mfma_f32_16x16x32_bf16 a[92:95],v[188:191],v[136:139],a[92:95]\n"
"s_waitcnt vmcnt(10)\n"
"v_mfma_f32_16x16x32_bf16 a[96:99],v[160:163],v[140:143],a[96:99]\n"
"v_lshlrev_b32 v24,16,v8\n"
"v_and_b32 v25,0xffff0000,v8\n"
"v_mfma_f32_16x16x32_bf16 a[96:99],v[224:227],v[204:207],a[96:99]\n"
"v_lshlrev_b32 v26,16,v9\n"
"v_and_b32 v27,0xffff0000,v9\n"
"v_mfma_f32_16x16x32_bf16 a[100:103],v[228:231],v[204:207],a[100:103]\n"
"buffer_load_dwordx4 v114,s[20:23],s77 offen offset:3072 lds\n"
"v_mfma_f32_16x16x32_bf16 a[100:103],v[164:167],v[140:143],a[100:103]\n"
"v_lshlrev_b32 v28,16,v10\n"
"v_and_b32 v29,0xffff0000,v10\n"
"v_mfma_f32_16x16x32_bf16 a[104:107],v[168:171],v[140:143],a[104:107]\n"
"v_lshlrev_b32 v30,16,v11\n"
"v_and_b32 v31,0xffff0000,v11\n"
"v_mfma_f32_16x16x32_bf16 a[104:107],v[232:235],v[204:207],a[104:107]\n"
"v_lshlrev_b32 v32,16,v12\n"
"v_and_b32 v33,0xffff0000,v12\n"
"v_mfma_f32_16x16x32_bf16 a[108:111],v[236:239],v[204:207],a[108:111]\n"
"s_mov_b32 s76,512\n"
"v_lshlrev_b32 v34,16,v13\n"
"s_add_u32 m0,s79,32768\n"
"v_mfma_f32_16x16x32_bf16 a[108:111],v[172:175],v[140:143],a[108:111]\n"
"buffer_load_dwordx4 v106,s[16:19],s76 offen offset:0 lds\n"
"v_mfma_f32_16x16x32_bf16 a[112:115],v[176:179],v[140:143],a[112:115]\n"
"v_and_b32 v35,0xffff0000,v13\n"
"v_lshlrev_b32 v36,16,v14\n"
"v_mfma_f32_16x16x32_bf16 a[112:115],v[240:243],v[204:207],a[112:115]\n"
"v_and_b32 v37,0xffff0000,v14\n"
"v_lshlrev_b32 v38,16,v15\n"
"v_mfma_f32_16x16x32_bf16 a[116:119],v[244:247],v[204:207],a[116:119]\n"
"v_and_b32 v39,0xffff0000,v15\n"
"v_lshlrev_b32 v40,16,v16\n"
"v_mfma_f32_16x16x32_bf16 a[116:119],v[180:183],v[140:143],a[116:119]\n"
"v_and_b32 v41,0xffff0000,v16\n"
"v_lshlrev_b32 v42,16,v17\n"
"v_mfma_f32_16x16x32_bf16 a[120:123],v[184:187],v[140:143],a[120:123]\n"
"buffer_load_dwordx4 v107,s[16:19],s76 offen offset:1024 lds\n"
"v_mfma_f32_16x16x32_bf16 a[120:123],v[248:251],v[204:207],a[120:123]\n"
"v_and_b32 v43,0xffff0000,v17\n"
"v_lshlrev_b32 v44,16,v18\n"
"v_mfma_f32_16x16x32_bf16 a[124:127],v[252:255],v[204:207],a[124:127]\n"
"v_and_b32 v45,0xffff0000,v18\n"
"v_lshlrev_b32 v46,16,v19\n"
"v_mfma_f32_16x16x32_bf16 a[124:127],v[188:191],v[140:143],a[124:127]\n"
"v_and_b32 v47,0xffff0000,v19\n"
"v_lshlrev_b32 v48,16,v20\n"
"v_mfma_f32_16x16x32_bf16 a[128:131],v[160:163],v[144:147],a[128:131]\n"
"v_and_b32 v49,0xffff0000,v20\n"
"v_lshlrev_b32 v50,16,v21\n"
"v_mfma_f32_16x16x32_bf16 a[128:131],v[224:227],v[208:211],a[128:131]\n"
"buffer_load_dwordx4 v108,s[16:19],s76 offen offset:2048 lds\n"
"v_mfma_f32_16x16x32_bf16 a[132:135],v[228:231],v[208:211],a[132:135]\n"
"v_and_b32 v51,0xffff0000,v21\n"
"v_lshlrev_b32 v52,16,v22\n"
"v_mfma_f32_16x16x32_bf16 a[132:135],v[164:167],v[144:147],a[132:135]\n"
"v_and_b32 v53,0xffff0000,v22\n"
"v_lshlrev_b32 v54,16,v23\n"
"v_mfma_f32_16x16x32_bf16 a[136:139],v[168:171],v[144:147],a[136:139]\n"
"v_and_b32 v55,0xffff0000,v23\n"
"v_add_f32 v56,v24,v25\n"
"v_mfma_f32_16x16x32_bf16 a[136:139],v[232:235],v[208:211],a[136:139]\n"
"v_add_f32 v57,v26,v27\n"
"v_add_f32 v56,v56,v28\n"
"v_mfma_f32_16x16x32_bf16 a[140:143],v[236:239],v[208:211],a[140:143]\n"
"buffer_load_dwordx4 v109,s[16:19],s76 offen offset:3072 lds\n"
"v_mfma_f32_16x16x32_bf16 a[140:143],v[172:175],v[144:147],a[140:143]\n"
"v_add_f32 v57,v57,v29\n"
"v_add_f32 v56,v56,v30\n"
"v_mfma_f32_16x16x32_bf16 a[144:147],v[176:179],v[144:147],a[144:147]\n"
"v_add_f32 v57,v57,v31\n"
"v_add_f32 v56,v56,v32\n"
"v_mfma_f32_16x16x32_bf16 a[144:147],v[240:243],v[208:211],a[144:147]\n"
"v_readfirstlane_b32 s82,v78\n"
"v_readfirstlane_b32 s83,v79\n"
"v_mfma_f32_16x16x32_bf16 a[148:151],v[244:247],v[208:211],a[148:151]\n"
"s_mov_b32 s76,262656\n"
"v_add_f32 v57,v57,v33\n"
"s_add_u32 m0,s79,36864\n"
"v_mfma_f32_16x16x32_bf16 a[148:151],v[180:183],v[144:147],a[148:151]\n"
"buffer_load_dwordx4 v106,s[16:19],s76 offen offset:0 lds\n"
"s_mul_i32 s80,s82,0x9e3779b1\n"
"v_mfma_f32_16x16x32_bf16 a[152:155],v[184:187],v[144:147],a[152:155]\n"
"s_add_u32 s80,s80,0x7f4a7c15\n"
"v_add_f32 v56,v56,v34\n"
"v_add_f32 v57,v57,v35\n"
"v_mfma_f32_16x16x32_bf16 a[152:155],v[248:251],v[208:211],a[152:155]\n"
"s_add_u32 s81,s83,s80\n"
"s_add_u32 s81,s81,1\n"
"v_mfma_f32_16x16x32_bf16 a[156:159],v[252:255],v[208:211],a[156:159]\n"
"v_mov_b32 v124,s80\n"
"v_mov_b32 v125,s81\n"
"v_mfma_f32_16x16x32_bf16 a[156:159],v[188:191],v[144:147],a[156:159]\n"
"v_add_f32 v56,v56,v36\n"
"v_add_f32 v57,v57,v37\n"
"v_mfma_f32_16x16x32_bf16 a[160:163],v[160:163],v[148:151],a[160:163]\n"
"buffer_load_dwordx4 v107,s[16:19],s76 offen offset:1024 lds\n"
"v_mfma_f32_16x16x32_bf16 a[160:163],v[224:227],v[212:215],a[160:163]\n"
"v_add_f32 v56,v56,v38\n"
"v_add_f32 v57,v57,v39\n"
"v_mfma_f32_16x16x32_bf16 a[164:167],v[228:231],v[212:215],a[164:167]\n"
"v_add_f32 v56,v56,v40\n"
"v_add_f32 v57,v57,v41\n"
"v_mfma_f32_16x16x32_bf16 a[164:167],v[164:167],v[148:151],a[164:167]\n"
"v_add_f32 v56,v56,v42\n"
"v_add_f32 v57,v57,v43\n"
"v_mfma_f32_16x16x32_bf16 a[168:171],v[168:171],v[148:151],a[168:171]\n"
"v_add_f32 v56,v56,v44\n"
"v_add_f32 v57,v57,v45\n"
"v_mfma_f32_16x16x32_bf16 a[168:171],v[232:235],v[212:215],a[168:171]\n"
"buffer_load_dwordx4 v108,s[16:19],s76 offen offset:2048 lds\n"
"v_mfma_f32_16x16x32_bf16 a[172:175],v[236:239],v[212:215],a[172:175]\n"
"v_add_f32 v56,v56,v46\n"
"v_add_f32 v57,v57,v47\n"
"v_mfma_f32_16x16x32_bf16 a[172:175],v[172:175],v[148:151],a[172:175]\n"
"v_add_f32 v56,v56,v48\n"
"v_add_f32 v57,v57,v49\n"
"v_mfma_f32_16x16x32_bf16 a[176:179],v[176:179],v[148:151],a[176:179]\n"
"v_add_f32 v56,v56,v50\n"
"v_add_f32 v57,v57,v51\n"
"v_mfma_f32_16x16x32_bf16 a[176:179],v[240:243],v[212:215],a[176:179]\n"
"v_add_f32 v56,v56,v52\n"
"v_add_f32 v57,v57,v53\n"
"v_mfma_f32_16x16x32_bf16 a[180:183],v[244:247],v[212:215],a[180:183]\n"
"buffer_load_dwordx4 v109,s[16:19],s76 offen offset:3072 lds\n"
"v_mfma_f32_16x16x32_bf16 a[180:183],v[180:183],v[148:151],a[180:183]\n"
"s_mov_b32 s87,2097152\n"
"v_add_f32 v56,v56,v54\n"
"v_add_f32 v57,v57,v55\n"
"v_mfma_f32_16x16x32_bf16 a[184:187],v[184:187],v[148:151],a[184:187]\n"
"buffer_load_dword v115,v112,s[36:39],s87 offen\n"
"v_mfma_f32_16x16x32_bf16 a[184:187],v[248:251],v[212:215],a[184:187]\n"
"s_mov_b32 s87,2621440\n"
"v_add_f32 v56,v56,v57\n"
"v_mfma_f32_16x16x32_bf16 a[188:191],v[252:255],v[212:215],a[188:191]\n"
"buffer_load_dword v115,v112,s[36:39],s87 offen\n"
"v_add_f32_dpp v56,v56,v56 quad_perm:[1,0,3,2] row_mask:0xf bank_mask:0xf\n"
"v_mfma_f32_16x16x32_bf16 a[188:191],v[188:191],v[148:151],a[188:191]\n"
"s_barrier\n"
"v_mfma_f32_16x16x32_bf16 a[192:195],v[224:227],v[216:219],a[192:195]\n"
"ds_read_b128 v[152:155],v82 offset:12288\n"
"v_mfma_f32_16x16x32_bf16 a[224:227],v[224:227],v[220:223],a[224:227]\n"
"ds_read_b128 v[156:159],v82 offset:14336\n"
"v_add_f32_dpp v56,v56,v56 quad_perm:[2,3,0,1] row_mask:0xf bank_mask:0xf\n"
"v_mfma_f32_16x16x32_bf16 a[228:231],v[228:231],v[220:223],a[228:231]\n"
"ds_read_b64_tr_b16 v[160:161],v98 offset:49152\n"
"v_mfma_f32_16x16x32_bf16 a[196:199],v[228:231],v[216:219],a[196:199]\n"
"ds_read_b64_tr_b16 v[162:163],v98 offset:50176\n"
"v_add_f32_dpp v56,v56,v56 row_ror:8 row_mask:0xf bank_mask:0xf\n"
"v_mfma_f32_16x16x32_bf16 a[200:203],v[232:235],v[216:219],a[200:203]\n"
"ds_read_b64_tr_b16 v[164:165],v99 offset:49152\n"
"v_mov_b32 v57,v56\n"
"v_mfma_f32_16x16x32_bf16 a[232:235],v[232:235],v[220:223],a[232:235]\n"
"ds_read_b64_tr_b16 v[166:167],v99 offset:50176\n"
"v_mfma_f32_16x16x32_bf16 a[236:239],v[236:239],v[220:223],a[236:239]\n"
"ds_read_b64_tr_b16 v[168:169],v100 offset:49152\n"
"v_permlane16_swap_b32 v57,v56\n"
"v_mfma_f32_16x16x32_bf16 a[204:207],v[236:239],v[216:219],a[204:207]\n"
"ds_read_b64_tr_b16 v[170:171],v100 offset:50176\n"
"v_add_f32 v56,v56,v57\n"
"v_mfma_f32_16x16x32_bf16 a[208:211],v[240:243],v[216:219],a[208:211]\n"
"ds_read_b64_tr_b16 v[172:173],v101 offset:49152\n"
"v_mov_b32 v57,v56\n"
"v_mfma_f32_16x16x32_bf16 a[240:243],v[240:243],v[220:223],a[240:243]\n"
"ds_read_b64_tr_b16 v[174:175],v101 offset:50176\n"
"v_mfma_f32_16x16x32_bf16 a[244:247],v[244:247],v[220:223],a[244:247]\n"
"ds_read_b64_tr_b16 v[176:177],v102 offset:49152\n"
"v_permlane32_swap_b32 v57,v56\n"
"v_mfma_f32_16x16x32_bf16 a[212:215],v[244:247],v[216:219],a[212:215]\n"
"ds_read_b64_tr_b16 v[178:179],v102 offset:50176\n"
"v_add_f32 v56,v56,v57\n"
"v_mfma_f32_16x16x32_bf16 a[216:219],v[248:251],v[216:219],a[216:219]\n"
"ds_read_b64_tr_b16 v[180:181],v103 offset:49152\n"
"v_mul_f32 v58,0x3a800000,v56\n"
"v_mfma_f32_16x16x32_bf16 a[248:251],v[248:251],v[220:223],a[248:251]\n"
"ds_read_b64_tr_b16 v[182:183],v103 offset:50176\n"
"v_sub_f32 v62,v24,v58\n"
"v_mfma_f32_16x16x32_bf16 a[252:255],v[252:255],v[220:223],a[252:255]\n"
"ds_read_b64_tr_b16 v[184:185],v104 offset:49152\n"
"v_mul_f32 v59,v62,v62\n"
"v_mfma_f32_16x16x32_bf16 a[220:223],v[252:255],v[216:219],a[220:223]\n"
"ds_read_b64_tr_b16 v[186:187],v104 offset:50176\n"
"v_sub_f32 v63,v25,v58\n"
"s_waitcnt lgkmcnt(10)\n"
"v_mfma_f32_16x16x32_bf16 a[192:195],v[160:163],v[152:155],a[192:195]\n"
"ds_read_b64_tr_b16 v[188:189],v105 offset:49152\n"
"v_mul_f32 v60,v63,v63\n"
"v_mfma_f32_16x16x32_bf16 a[224:227],v[160:163],v[156:159],a[224:227]\n"
"ds_read_b64_tr_b16 v[190:191],v105 offset:50176\n"
"v_sub_f32 v64,v26,v58\n"
"v_mfma_f32_16x16x32_bf16 a[228:231],v[164:167],v[156:159],a[228:231]\n"
"ds_read_b128 v[128:131],v82 offset:0\n"
"v_fmac_f32 v59,v64,v64\n"
"v_mfma_f32_16x16x32_bf16 a[196:199],v[164:167],v[152:155],a[196:199]\n"
"ds_read_b128 v[192:195],v83 offset:0\n"
"s_waitcnt lgkmcnt(10)\n"
"v_mfma_f32_16x16x32_bf16 a[200:203],v[168:171],v[152:155],a[200:203]\n"
"ds_read_b64_tr_b16 v[224:225],v98 offset:32768\n"
"v_sub_f32 v65,v27,v58\n"
"v_mfma_f32_16x16x32_bf16 a[232:235],v[168:171],v[156:159],a[232:235]\n"
"ds_read_b64_tr_b16 v[226:227],v98 offset:33792\n"
"v_fmac_f32 v60,v65,v65\n"
"v_mfma_f32_16x16x32_bf16 a[236:239],v[172:175],v[156:159],a[236:239]\n"
"ds_read_b64_tr_b16 v[228:229],v99 offset:32768\n"
"v_sub_f32 v62,v28,v58\n"
"v_mfma_f32_16x16x32_bf16 a[204:207],v[172:175],v[152:155],a[204:207]\n"
"ds_read_b64_tr_b16 v[230:231],v99 offset:33792\n"
"s_waitcnt lgkmcnt(10)\n"
"v_mfma_f32_16x16x32_bf16 a[208:211],v[176:179],v[152:155],a[208:211]\n"
"ds_read_b64_tr_b16 v[232:233],v100 offset:32768\n"
"v_fmac_f32 v59,v62,v62\n"
"v_mfma_f32_16x16x32_bf16 a[240:243],v[176:179],v[156:159],a[240:243]\n"
"ds_read_b64_tr_b16 v[234:235],v100 offset:33792\n"
"v_sub_f32 v63,v29,v58\n"
"v_mfma_f32_16x16x32_bf16 a[244:247],v[180:183],v[156:159],a[244:247]\n"
"ds_read_b64_tr_b16 v[236:237],v101 offset:32768\n"
"v_fmac_f32 v60,v63,v63\n"
"v_mfma_f32_16x16x32_bf16 a[212:215],v[180:183],v[152:155],a[212:215]\n"
"ds_read_b64_tr_b16 v[238:239],v101 offset:33792\n"
"s_waitcnt lgkmcnt(10)\n"
"v_mfma_f32_16x16x32_bf16 a[216:219],v[184:187],v[152:155],a[216:219]\n"
"ds_read_b64_tr_b16 v[240:241],v102 offset:32768\n"
"v_sub_f32 v64,v30,v58\n"
"v_mfma_f32_16x16x32_bf16 a[248:251],v[184:187],v[156:159],a[248:251]\n"
"ds_read_b64_tr_b16 v[242:243],v102 offset:33792\n"
"v_fmac_f32 v59,v64,v64\n"
"v_mfma_f32_16x16x32_bf16 a[252:255],v[188:191],v[156:159],a[252:255]\n"
"ds_read_b64_tr_b16 v[244:245],v103 offset:32768\n"
"v_sub_f32 v65,v31,v58\n"
"v_mfma_f32_16x16x32_bf16 a[220:223],v[188:191],v[152:155],a[220:223]\n"
"ds_read_b64_tr_b16 v[246:247],v103 offset:33792\n"
"s_waitcnt lgkmcnt(8)\n"
"v_mfma_f32_16x16x32_bf16 a[0:3],v[160:163],v[128:131],a[0:3]\n"
"ds_read_b64_tr_b16 v[248:249],v104 offset:32768\n"
"v_fmac_f32 v60,v65,v65\n"
"v_mfma_f32_16x16x32_bf16 a[0:3],v[224:227],v[192:195],a[0:3]\n"
"ds_read_b64_tr_b16 v[250:251],v104 offset:33792\n"
"v_sub_f32 v62,v32,v58\n"
"v_mfma_f32_16x16x32_bf16 a[4:7],v[228:231],v[192:195],a[4:7]\n"
"ds_read_b64_tr_b16 v[252:253],v105 offset:32768\n"
"v_fmac_f32 v59,v62,v62\n"
"v_mfma_f32_16x16x32_bf16 a[4:7],v[164:167],v[128:131],a[4:7]\n"
"ds_read_b64_tr_b16 v[254:255],v105 offset:33792\n"
"s_waitcnt lgkmcnt(8)\n"
"v_mfma_f32_16x16x32_bf16 a[8:11],v[168:171],v[128:131],a[8:11]\n"
"ds_read_b128 v[132:135],v82 offset:2048\n"
"v_sub_f32 v63,v33,v58\n"
"v_mfma_f32_16x16x32_bf16 a[8:11],v[232:235],v[192:195],a[8:11]\n"
"ds_read_b128 v[196:199],v83 offset:2048\n"
"v_fmac_f32 v60,v63,v63\n"
"v_mfma_f32_16x16x32_bf16 a[12:15],v[236:239],v[192:195],a[12:15]\n"
"ds_read_b128 v[136:139],v82 offset:4096\n"
"v_sub_f32 v64,v34,v58\n"
"v_mfma_f32_16x16x32_bf16 a[12:15],v[172:175],v[128:131],a[12:15]\n"
"ds_read_b128 v[200:203],v83 offset:4096\n"
"s_waitcnt lgkmcnt(8)\n"
"v_mfma_f32_16x16x32_bf16 a[16:19],v[176:179],v[128:131],a[16:19]\n"
"ds_read_b128 v[140:143],v82 offset:6144\n"
"v_fmac_f32 v59,v64,v64\n"
"v_mfma_f32_16x16x32_bf16 a[16:19],v[240:243],v[192:195],a[16:19]\n"
"ds_read_b128 v[204:207],v83 offset:6144\n"
"v_sub_f32 v65,v35,v58\n"
"v_mfma_f32_16x16x32_bf16 a[20:23],v[244:247],v[192:195],a[20:23]\n"
"ds_read_b128 v[144:147],v82 offset:8192\n"
"v_fmac_f32 v60,v65,v65\n"
"v_mfma_f32_16x16x32_bf16 a[20:23],v[180:183],v[128:131],a[20:23]\n"
"ds_read_b128 v[208:211],v83 offset:8192\n"
"s_waitcnt lgkmcnt(8)\n"
"v_mfma_f32_16x16x32_bf16 a[24:27],v[184:187],v[128:131],a[24:27]\n"
"ds_read_b128 v[148:151],v82 offset:10240\n"
"v_sub_f32 v62,v36,v58\n"
"v_mfma_f32_16x16x32_bf16 a[24:27],v[248:251],v[192:195],a[24:27]\n"
"ds_read_b128 v[212:215],v83 offset:10240\n"
"v_fmac_f32 v59,v62,v62\n"
"v_mfma_f32_16x16x32_bf16 a[28:31],v[252:255],v[192:195],a[28:31]\n"
"ds_read_b128 v[216:219],v83 offset:12288\n"
"v_sub_f32 v63,v37,v58\n"
"v_mfma_f32_16x16x32_bf16 a[28:31],v[188:191],v[128:131],a[28:31]\n"
"ds_read_b128 v[220:223],v83 offset:14336\n"
"s_waitcnt lgkmcnt(10)\n"
"v_mfma_f32_16x16x32_bf16 a[32:35],v[160:163],v[132:135],a[32:35]\n"
"v_fmac_f32 v60,v63,v63\n"
"v_sub_f32 v64,v38,v58\n"
"v_mfma_f32_16x16x32_bf16 a[32:35],v[224:227],v[196:199],a[32:35]\n"
"v_fmac_f32 v59,v64,v64\n"
"v_sub_f32 v65,v39,v58\n"
"v_mfma_f32_16x16x32_bf16 a[36:39],v[228:231],v[196:199],a[36:39]\n"
"v_fmac_f32 v60,v65,v65\n"
"v_sub_f32 v62,v40,v58\n"
"v_mfma_f32_16x16x32_bf16 a[36:39],v[164:167],v[132:135],a[36:39]\n"
"v_fmac_f32 v59,v62,v62\n"
"v_sub_f32 v63,v41,v58\n"
"v_mfma_f32_16x16x32_bf16 a[40:43],v[168:171],v[132:135],a[40:43]\n"
"s_waitcnt lgkmcnt(0)\n"
"s_barrier\n"
"v_mfma_f32_16x16x32_bf16 a[40:43],v[232:235],v[196:199],a[40:43]\n"
"s_mov_b32 s77,2097152\n"
"v_fmac_f32 v60,v63,v63\n"
"s_add_u32 m0,s78,147456\n"
"v_mfma_f32_16x16x32_bf16 a[44:47],v[236:239],v[196:199],a[44:47]\n"
"buffer_load_dwordx4 v110,s[20:23],s77 offen offset:0 lds\n"
"v_mfma_f32_16x16x32_bf16 a[44:47],v[172:175],v[132:135],a[44:47]\n"
"v_sub_f32 v64,v42,v58\n"
"v_fmac_f32 v59,v64,v64\n"
"v_mfma_f32_16x16x32_bf16 a[48:51],v[176:179],v[132:135],a[48:51]\n"
"v_sub_f32 v65,v43,v58\n"
"v_fmac_f32 v60,v65,v65\n"
"v_mfma_f32_16x16x32_bf16 a[48:51],v[240:243],v[196:199],a[48:51]\n"
"v_sub_f32 v62,v44,v58\n"
"v_fmac_f32 v59,v62,v62\n"
"v_mfma_f32_16x16x32_bf16 a[52:55],v[244:247],v[196:199],a[52:55]\n"
"buffer_load_dwordx4 v111,s[20:23],s77 offen offset:1024 lds\n"
"v_mfma_f32_16x16x32_bf16 a[52:55],v[180:183],v[132:135],a[52:55]\n"
"v_sub_f32 v63,v45,v58\n"
"v_fmac_f32 v60,v63,v63\n"
"v_mfma_f32_16x16x32_bf16 a[56:59],v[184:187],v[132:135],a[56:59]\n"
"v_sub_f32 v64,v46,v58\n"
"v_fmac_f32 v59,v64,v64\n"
"v_mfma_f32_16x16x32_bf16 a[56:59],v[248:251],v[196:199],a[56:59]\n"
"v_sub_f32 v65,v47,v58\n"
"v_fmac_f32 v60,v65,v65\n"
"v_mfma_f32_16x16x32_bf16 a[60:63],v[252:255],v[196:199],a[60:63]\n"
"buffer_load_dwordx4 v113,s[20:23],s77 offen offset:2048 lds\n"
"v_mfma_f32_16x16x32_bf16 a[60:63],v[188:191],v[132:135],a[60:63]\n"
"v_sub_f32 v62,v48,v58\n"
"v_fmac_f32 v59,v62,v62\n"
"v_mfma_f32_16x16x32_bf16 a[64:67],v[160:163],v[136:139],a[64:67]\n"
"v_sub_f32 v63,v49,v58\n"
"v_fmac_f32 v60,v63,v63\n"
"v_mfma_f32_16x16x32_bf16 a[64:67],v[224:227],v[200:203],a[64:67]\n"
"v_sub_f32 v64,v50,v58\n"
"v_fmac_f32 v59,v64,v64\n"
"v_mfma_f32_16x16x32_bf16 a[68:71],v[228:231],v[200:203],a[68:71]\n"
"buffer_load_dwordx4 v114,s[20:23],s77 offen offset:3072 lds\n"
"v_mfma_f32_16x16x32_bf16 a[68:71],v[164:167],v[136:139],a[68:71]\n"
"v_sub_f32 v65,v51,v58\n"
"v_fmac_f32 v60,v65,v65\n"
"v_mfma_f32_16x16x32_bf16 a[72:75],v[168:171],v[136:139],a[72:75]\n"
"v_sub_f32 v62,v52,v58\n"
"v_fmac_f32 v59,v62,v62\n"
"v_mfma_f32_16x16x32_bf16 a[72:75],v[232:235],v[200:203],a[72:75]\n"
"s_mov_b32 s77,2359296\n"
"v_sub_f32 v63,v53,v58\n"
"s_add_u32 m0,s78,131072\n"
"v_mfma_f32_16x16x32_bf16 a[76:79],v[236:239],v[200:203],a[76:79]\n"
"buffer_load_dwordx4 v110,s[20:23],s77 offen offset:0 lds\n"
"v_mfma_f32_16x16x32_bf16 a[76:79],v[172:175],v[136:139],a[76:79]\n"
"v_fmac_f32 v60,v63,v63\n"
"v_sub_f32 v64,v54,v58\n"
"v_mfma_f32_16x16x32_bf16 a[80:83],v[176:179],v[136:139],a[80:83]\n"
"v_fmac_f32 v59,v64,v64\n"
"v_sub_f32 v65,v55,v58\n"
"v_mfma_f32_16x16x32_bf16 a[80:83],v[240:243],v[200:203],a[80:83]\n"
"v_fmac_f32 v60,v65,v65\n"
"v_add_f32 v59,v59,v60\n"
"v_mfma_f32_16x16x32_bf16 a[84:87],v[244:247],v[200:203],a[84:87]\n"
"buffer_load_dwordx4 v111,s[20:23],s77 offen offset:1024 lds\n"
"v_mfma_f32_16x16x32_bf16 a[84:87],v[180:183],v[136:139],a[84:87]\n"
"v_mfma_f32_16x16x32_bf16 a[88:91],v[184:187],v[136:139],a[88:91]\n"
"v_add_f32_dpp v59,v59,v59 quad_perm:[1,0,3,2] row_mask:0xf bank_mask:0xf\n"
"v_mfma_f32_16x16x32_bf16 a[88:91],v[248:251],v[200:203],a[88:91]\n"
"s_nop 0\n"
"v_add_f32_dpp v59,v59,v59 quad_perm:[2,3,0,1] row_mask:0xf bank_mask:0xf\n"
"v_mfma_f32_16x16x32_bf16 a[92:95],v[252:255],v[200:203],a[92:95]\n"
"buffer_load_dwordx4 v113,s[20:23],s77 offen offset:2048 lds\n"
"v_mfma_f32_16x16x32_bf16 a[92:95],v[188:191],v[136:139],a[92:95]\n"
"v_add_f32_dpp v59,v59,v59 row_ror:8 row_mask:0xf bank_mask:0xf\n"
"v_mov_b32 v57,v59\n"
"v_mfma_f32_16x16x32_bf16 a[96:99],v[160:163],v[140:143],a[96:99]\n"
"v_mfma_f32_16x16x32_bf16 a[96:99],v[224:227],v[204:207],a[96:99]\n"
"v_permlane16_swap_b32 v57,v59\n"
"v_add_f32 v59,v59,v57\n"
"v_mfma_f32_16x16x32_bf16 a[100:103],v[228:231],v[204:207],a[100:103]\n"
"buffer_load_dwordx4 v114,s[20:23],s77 offen offset:3072 lds\n"
"v_mfma_f32_16x16x32_bf16 a[100:103],v[164:167],v[140:143],a[100:103]\n"
"v_mov_b32 v57,v59\n"
"v_mfma_f32_16x16x32_bf16 a[104:107],v[168:171],v[140:143],a[104:107]\n"
"s_nop 0\n"
"v_permlane32_swap_b32 v57,v59\n"
"v_add_f32 v59,v59,v57\n"
"v_mfma_f32_16x16x32_bf16 a[104:107],v[232:235],v[204:207],a[104:107]\n"
"v_mul_f32 v59,0x3a800000,v59\n"
"v_cvt_pk_bf16_f32 v59,v59,v59\n"
"v_mfma_f32_16x16x32_bf16 a[108:111],v[236:239],v[204:207],a[108:111]\n"
"s_mov_b32 s76,640\n"
"v_lshlrev_b32 v59,16,v59\n"
"s_add_u32 m0,s79,65536\n"
"v_mfma_f32_16x16x32_bf16 a[108:111],v[172:175],v[140:143],a[108:111]\n"
"buffer_load_dwordx4 v106,s[16:19],s76 offen offset:0 lds\n"
"v_mfma_f32_16x16x32_bf16 a[112:115],v[176:179],v[140:143],a[112:115]\n"
"v_add_f32 v59,0x3727c5ac,v59\n"
"v_cvt_pk_bf16_f32 v59,v59,v59\n"
"v_mfma_f32_16x16x32_bf16 a[112:115],v[240:243],v[204:207],a[112:115]\n"
"v_lshlrev_b32 v59,16,v59\n"
"v_sqrt_f32 v59,v59\n"
"v_mfma_f32_16x16x32_bf16 a[116:119],v[244:247],v[204:207],a[116:119]\n"
"v_cvt_pk_bf16_f32 v58,v58,v58\n"
"v_lshlrev_b32 v58,16,v58\n"
"v_mfma_f32_16x16x32_bf16 a[116:119],v[180:183],v[140:143],a[116:119]\n"
"v_cvt_pk_bf16_f32 v59,v59,v59\n"
"v_lshlrev_b32 v59,16,v59\n"
"v_mfma_f32_16x16x32_bf16 a[120:123],v[184:187],v[140:143],a[120:123]\n"
"buffer_load_dwordx4 v107,s[16:19],s76 offen offset:1024 lds\n"
"v_mfma_f32_16x16x32_bf16 a[120:123],v[248:251],v[204:207],a[120:123]\n"
"v_rcp_f32 v66,v59\n"
"v_mfma_f32_16x16x32_bf16 a[124:127],v[252:255],v[204:207],a[124:127]\n"
"v_mul_f32 v66,0x3e0293ee,v66\n"
"v_mul_f32 v58,v58,v66\n"
"v_mfma_f32_16x16x32_bf16 a[124:127],v[188:191],v[140:143],a[124:127]\n"
"v_fma_f32 v24,v24,v66,-v58\n"
"v_fma_f32 v25,v25,v66,-v58\n"
"v_mfma_f32_16x16x32_bf16 a[128:131],v[160:163],v[144:147],a[128:131]\n"
"v_cvt_pk_bf16_f32 v8,v24,v25\n"
"v_fma_f32 v26,v26,v66,-v58\n"
"v_mfma_f32_16x16x32_bf16 a[128:131],v[224:227],v[208:211],a[128:131]\n"
"buffer_load_dwordx4 v108,s[16:19],s76 offen offset:2048 lds\n"
"v_mfma_f32_16x16x32_bf16 a[132:135],v[228:231],v[208:211],a[132:135]\n"
"v_fma_f32 v27,v27,v66,-v58\n"
"v_cvt_pk_bf16_f32 v9,v26,v27\n"
"v_mfma_f32_16x16x32_bf16 a[132:135],v[164:167],v[144:147],a[132:135]\n"
"v_fma_f32 v28,v28,v66,-v58\n"
"v_fma_f32 v29,v29,v66,-v58\n"
"v_mfma_f32_16x16x32_bf16 a[136:139],v[168:171],v[144:147],a[136:139]\n"
"v_cvt_pk_bf16_f32 v10,v28,v29\n"
"v_fma_f32 v30,v30,v66,-v58\n"
"v_mfma_f32_16x16x32_bf16 a[136:139],v[232:235],v[208:211],a[136:139]\n"
"v_fma_f32 v31,v31,v66,-v58\n"
"v_cvt_pk_bf16_f32 v11,v30,v31\n"
"v_mfma_f32_16x16x32_bf16 a[140:143],v[236:239],v[208:211],a[140:143]\n"
"buffer_load_dwordx4 v109,s[16:19],s76 offen offset:3072 lds\n"
"v_mfma_f32_16x16x32_bf16 a[140:143],v[172:175],v[144:147],a[140:143]\n"
"buffer_store_dwordx4 v[8:11],v71,s[24:27],s54 offen sc1\n"
"v_mfma_f32_16x16x32_bf16 a[144:147],v[176:179],v[144:147],a[144:147]\n"
"s_add_u32 s54,s54,131072\n"
"v_fma_f32 v32,v32,v66,-v58\n"
"v_fma_f32 v33,v33,v66,-v58\n"
"v_mfma_f32_16x16x32_bf16 a[144:147],v[240:243],v[208:211],a[144:147]\n"
"v_cvt_pk_bf16_f32 v12,v32,v33\n"
"v_fma_f32 v34,v34,v66,-v58\n"
"v_mfma_f32_16x16x32_bf16 a[148:151],v[244:247],v[208:211],a[148:151]\n"
"s_mov_b32 s76,262784\n"
"v_fma_f32 v35,v35,v66,-v58\n"
"s_add_u32 m0,s79,69632\n"
"v_mfma_f32_16x16x32_bf16 a[148:151],v[180:183],v[144:147],a[148:151]\n"
"buffer_load_dwordx4 v106,s[16:19],s76 offen offset:0 lds\n"
"v_mfma_f32_16x16x32_bf16 a[152:155],v[184:187],v[144:147],a[152:155]\n"
"v_cvt_pk_bf16_f32 v13,v34,v35\n"
"v_fma_f32 v36,v36,v66,-v58\n"
"v_mfma_f32_16x16x32_bf16 a[152:155],v[248:251],v[208:211],a[152:155]\n"
"v_fma_f32 v37,v37,v66,-v58\n"
"v_cvt_pk_bf16_f32 v14,v36,v37\n"
"v_mfma_f32_16x16x32_bf16 a[156:159],v[252:255],v[208:211],a[156:159]\n"
"v_fma_f32 v38,v38,v66,-v58\n"
"v_fma_f32 v39,v39,v66,-v58\n"
"v_cvt_pk_bf16_f32 v15,v38,v39\n"
"v_mfma_f32_16x16x32_bf16 a[156:159],v[188:191],v[144:147],a[156:159]\n"
"buffer_store_dwordx4 v[12:15],v71,s[24:27],s54 offen sc1\n"
"v_mfma_f32_16x16x32_bf16 a[160:163],v[160:163],v[148:151],a[160:163]\n"
"buffer_load_dwordx4 v107,s[16:19],s76 offen offset:1024 lds\n"
"v_mfma_f32_16x16x32_bf16 a[160:163],v[224:227],v[212:215],a[160:163]\n"
"s_add_u32 s54,s54,131072\n"
"v_fma_f32 v40,v40,v66,-v58\n"
"v_mfma_f32_16x16x32_bf16 a[164:167],v[228:231],v[212:215],a[164:167]\n"
"v_fma_f32 v41,v41,v66,-v58\n"
"v_cvt_pk_bf16_f32 v16,v40,v41\n"
"v_mfma_f32_16x16x32_bf16 a[164:167],v[164:167],v[148:151],a[164:167]\n"
"v_fma_f32 v42,v42,v66,-v58\n"
"v_fma_f32 v43,v43,v66,-v58\n"
"v_mfma_f32_16x16x32_bf16 a[168:171],v[168:171],v[148:151],a[168:171]\n"
"v_cvt_pk_bf16_f32 v17,v42,v43\n"
"v_fma_f32 v44,v44,v66,-v58\n"
"v_mfma_f32_16x16x32_bf16 a[168:171],v[232:235],v[212:215],a[168:171]\n"
"buffer_load_dwordx4 v108,s[16:19],s76 offen offset:2048 lds\n"
"v_mfma_f32_16x16x32_bf16 a[172:175],v[236:239],v[212:215],a[172:175]\n"
"v_fma_f32 v45,v45,v66,-v58\n"
"v_cvt_pk_bf16_f32 v18,v44,v45\n"
"v_mfma_f32_16x16x32_bf16 a[172:175],v[172:175],v[148:151],a[172:175]\n"
"v_fma_f32 v46,v46,v66,-v58\n"
"v_fma_f32 v47,v47,v66,-v58\n"
"v_cvt_pk_bf16_f32 v19,v46,v47\n"
"v_mfma_f32_16x16x32_bf16 a[176:179],v[176:179],v[148:151],a[176:179]\n"
"buffer_store_dwordx4 v[16:19],v71,s[24:27],s54 offen sc1\n"
"v_mfma_f32_16x16x32_bf16 a[176:179],v[240:243],v[212:215],a[176:179]\n"
"s_add_u32 s54,s54,131072\n"
"v_fma_f32 v48,v48,v66,-v58\n"
"v_mfma_f32_16x16x32_bf16 a[180:183],v[244:247],v[212:215],a[180:183]\n"
"buffer_load_dwordx4 v109,s[16:19],s76 offen offset:3072 lds\n"
"v_mfma_f32_16x16x32_bf16 a[180:183],v[180:183],v[148:151],a[180:183]\n"
"s_mov_b32 s87,0\n"
"v_fma_f32 v49,v49,v66,-v58\n"
"v_cvt_pk_bf16_f32 v20,v48,v49\n"
"v_mfma_f32_16x16x32_bf16 a[184:187],v[184:187],v[148:151],a[184:187]\n"
"buffer_load_dword v115,v112,s[40:43],s87 offen\n"
"v_mfma_f32_16x16x32_bf16 a[184:187],v[248:251],v[212:215],a[184:187]\n"
"s_mov_b32 s87,1048576\n"
"v_fma_f32 v50,v50,v66,-v58\n"
"v_fma_f32 v51,v51,v66,-v58\n"
"v_mfma_f32_16x16x32_bf16 a[188:191],v[252:255],v[212:215],a[188:191]\n"
"buffer_load_dword v115,v112,s[40:43],s87 offen\n"
"v_mfma_f32_16x16x32_bf16 a[188:191],v[188:191],v[148:151],a[188:191]\n"
"s_mov_b32 s87,2097152\n"
"s_waitcnt vmcnt(31)\n"
"s_barrier\n"
"v_mfma_f32_16x16x32_bf16 a[192:195],v[224:227],v[216:219],a[192:195]\n"
"ds_read_b128 v[152:155],v80 offset:12288\n"
"buffer_load_dword v115,v112,s[40:43],s87 offen\n"
"v_mfma_f32_16x16x32_bf16 a[224:227],v[224:227],v[220:223],a[224:227]\n"
"ds_read_b128 v[156:159],v80 offset:14336\n"
"v_cvt_pk_bf16_f32 v21,v50,v51\n"
"v_mfma_f32_16x16x32_bf16 a[228:231],v[228:231],v[220:223],a[228:231]\n"
"ds_read_b64_tr_b16 v[160:161],v98 offset:0\n"
"v_fma_f32 v52,v52,v66,-v58\n"
"v_mfma_f32_16x16x32_bf16 a[196:199],v[228:231],v[216:219],a[196:199]\n"
"ds_read_b64_tr_b16 v[162:163],v98 offset:1024\n"
"v_fma_f32 v53,v53,v66,-v58\n"
"v_mfma_f32_16x16x32_bf16 a[200:203],v[232:235],v[216:219],a[200:203]\n"
"ds_read_b64_tr_b16 v[164:165],v99 offset:0\n"
"v_cvt_pk_bf16_f32 v22,v52,v53\n"
"v_mfma_f32_16x16x32_bf16 a[232:235],v[232:235],v[220:223],a[232:235]\n"
"ds_read_b64_tr_b16 v[166:167],v99 offset:1024\n"
"v_fma_f32 v54,v54,v66,-v58\n"
"v_mfma_f32_16x16x32_bf16 a[236:239],v[236:239],v[220:223],a[236:239]\n"
"ds_read_b64_tr_b16 v[168:169],v100 offset:0\n"
"v_fma_f32 v55,v55,v66,-v58\n"
"v_mfma_f32_16x16x32_bf16 a[204:207],v[236:239],v[216:219],a[204:207]\n"
"ds_read_b64_tr_b16 v[170:171],v100 offset:1024\n"
"v_cvt_pk_bf16_f32 v23,v54,v55\n"
"v_mfma_f32_16x16x32_bf16 a[208:211],v[240:243],v[216:219],a[208:211]\n"
"ds_read_b64_tr_b16 v[172:173],v101 offset:0\n"
"buffer_store_dwordx4 v[20:23],v71,s[24:27],s54 offen sc1\n"
"v_mfma_f32_16x16x32_bf16 a[240:243],v[240:243],v[220:223],a[240:243]\n"
"ds_read_b64_tr_b16 v[174:175],v101 offset:1024\n"
"v_mfma_f32_16x16x32_bf16 a[244:247],v[244:247],v[220:223],a[244:247]\n"
"ds_read_b64_tr_b16 v[176:177],v102 offset:0\n"
"v_mfma_f32_16x16x32_bf16 a[212:215],v[244:247],v[216:219],a[212:215]\n"
"ds_read_b64_tr_b16 v[178:179],v102 offset:1024\n"
"v_mfma_f32_16x16x32_bf16 a[216:219],v[248:251],v[216:219],a[216:219]\n"
"ds_read_b64_tr_b16 v[180:181],v103 offset:0\n"
"v_mfma_f32_16x16x32_bf16 a[248:251],v[248:251],v[220:223],a[248:251]\n"
"ds_read_b64_tr_b16 v[182:183],v103 offset:1024\n"
"v_mfma_f32_16x16x32_bf16 a[252:255],v[252:255],v[220:223],a[252:255]\n"
"ds_read_b64_tr_b16 v[184:185],v104 offset:0\n"
"v_mfma_f32_16x16x32_bf16 a[220:223],v[252:255],v[216:219],a[220:223]\n"
"ds_read_b64_tr_b16 v[186:187],v104 offset:1024\n"
"s_waitcnt lgkmcnt(10)\n"
"v_mfma_f32_16x16x32_bf16 a[192:195],v[160:163],v[152:155],a[192:195]\n"
"ds_read_b64_tr_b16 v[188:189],v105 offset:0\n"
"v_mfma_f32_16x16x32_bf16 a[224:227],v[160:163],v[156:159],a[224:227]\n"
"ds_read_b64_tr_b16 v[190:191],v105 offset:1024\n"
"v_mfma_f32_16x16x32_bf16 a[228:231],v[164:167],v[156:159],a[228:231]\n"
"ds_read_b128 v[128:131],v80 offset:0\n"
"v_mfma_f32_16x16x32_bf16 a[196:199],v[164:167],v[152:155],a[196:199]\n"
"ds_read_b128 v[192:195],v81 offset:0\n"
"s_waitcnt lgkmcnt(10)\n"
"v_mfma_f32_16x16x32_bf16 a[200:203],v[168:171],v[152:155],a[200:203]\n"
"ds_read_b64_tr_b16 v[224:225],v98 offset:16384\n"
"v_mfma_f32_16x16x32_bf16 a[232:235],v[168:171],v[156:159],a[232:235]\n"
"ds_read_b64_tr_b16 v[226:227],v98 offset:17408\n"
"v_mfma_f32_16x16x32_bf16 a[236:239],v[172:175],v[156:159],a[236:239]\n"
"ds_read_b64_tr_b16 v[228:229],v99 offset:16384\n"
"v_mfma_f32_16x16x32_bf16 a[204:207],v[172:175],v[152:155],a[204:207]\n"
"ds_read_b64_tr_b16 v[230:231],v99 offset:17408\n"
"s_waitcnt lgkmcnt(10)\n"
"v_mfma_f32_16x16x32_bf16 a[208:211],v[176:179],v[152:155],a[208:211]\n"
"ds_read_b64_tr_b16 v[232:233],v100 offset:16384\n"
"v_mfma_f32_16x16x32_bf16 a[240:243],v[176:179],v[156:159],a[240:243]\n"
"ds_read_b64_tr_b16 v[234:235],v100 offset:17408\n"
"v_mfma_f32_16x16x32_bf16 a[244:247],v[180:183],v[156:159],a[244:247]\n"
"ds_read_b64_tr_b16 v[236:237],v101 offset:16384\n"
"v_mfma_f32_16x16x32_bf16 a[212:215],v[180:183],v[152:155],a[212:215]\n"
"ds_read_b64_tr_b16 v[238:239],v101 offset:17408\n"
"s_waitcnt lgkmcnt(10)\n"
"v_mfma_f32_16x16x32_bf16 a[216:219],v[184:187],v[152:155],a[216:219]\n"
"ds_read_b64_tr_b16 v[240:241],v102 offset:16384\n"
"v_mfma_f32_16x16x32_bf16 a[248:251],v[184:187],v[156:159],a[248:251]\n"
"ds_read_b64_tr_b16 v[242:243],v102 offset:17408\n"
"v_mfma_f32_16x16x32_bf16 a[252:255],v[188:191],v[156:159],a[252:255]\n"
"ds_read_b64_tr_b16 v[244:245],v103 offset:16384\n"
"v_mfma_f32_16x16x32_bf16 a[220:223],v[188:191],v[152:155],a[220:223]\n"
"ds_read_b64_tr_b16 v[246:247],v103 offset:17408\n"
"s_waitcnt lgkmcnt(8)\n"
"v_mfma_f32_16x16x32_bf16 a[0:3],v[160:163],v[128:131],a[0:3]\n"
"ds_read_b64_tr_b16 v[248:249],v104 offset:16384\n"
"v_mfma_f32_16x16x32_bf16 a[0:3],v[224:227],v[192:195],a[0:3]\n"
"ds_read_b64_tr_b16 v[250:251],v104 offset:17408\n"
"v_mfma_f32_16x16x32_bf16 a[4:7],v[228:231],v[192:195],a[4:7]\n"
"ds_read_b64_tr_b16 v[252:253],v105 offset:16384\n"
"v_mfma_f32_16x16x32_bf16 a[4:7],v[164:167],v[128:131],a[4:7]\n"
"ds_read_b64_tr_b16 v[254:255],v105 offset:17408\n"
"s_waitcnt lgkmcnt(8)\n"
"v_mfma_f32_16x16x32_bf16 a[8:11],v[168:171],v[128:131],a[8:11]\n"
"ds_read_b128 v[132:135],v80 offset:2048\n"
"v_mfma_f32_16x16x32_bf16 a[8:11],v[232:235],v[192:195],a[8:11]\n"
"ds_read_b128 v[196:199],v81 offset:2048\n"
"v_mfma_f32_16x16x32_bf16 a[12:15],v[236:239],v[192:195],a[12:15]\n"
"ds_read_b128 v[136:139],v80 offset:4096\n"
"v_mfma_f32_16x16x32_bf16 a[12:15],v[172:175],v[128:131],a[12:15]\n"
"ds_read_b128 v[200:203],v81 offset:4096\n"
"s_waitcnt lgkmcnt(8)\n"
"v_mfma_f32_16x16x32_bf16 a[16:19],v[176:179],v[128:131],a[16:19]\n"
"ds_read_b128 v[140:143],v80 offset:6144\n"
"v_mfma_f32_16x16x32_bf16 a[16:19],v[240:243],v[192:195],a[16:19]\n"
"ds_read_b128 v[204:207],v81 offset:6144\n"
"v_mfma_f32_16x16x32_bf16 a[20:23],v[244:247],v[192:195],a[20:23]\n"
"ds_read_b128 v[144:147],v80 offset:8192\n"
"v_mfma_f32_16x16x32_bf16 a[20:23],v[180:183],v[128:131],a[20:23]\n"
"ds_read_b128 v[208:211],v81 offset:8192\n"
"s_waitcnt lgkmcnt(8)\n"
"v_mfma_f32_16x16x32_bf16 a[24:27],v[184:187],v[128:131],a[24:27]\n"
"ds_read_b128 v[148:151],v80 offset:10240\n"
"v_mfma_f32_16x16x32_bf16 a[24:27],v[248:251],v[192:195],a[24:27]\n"
"ds_read_b128 v[212:215],v81 offset:10240\n"
"v_mfma_f32_16x16x32_bf16 a[28:31],v[252:255],v[192:195],a[28:31]\n"
"ds_read_b128 v[216:219],v81 offset:12288\n"
"v_mfma_f32_16x16x32_bf16 a[28:31],v[188:191],v[128:131],a[28:31]\n"
"ds_read_b128 v[220:223],v81 offset:14336\n"
"s_waitcnt lgkmcnt(10)\n"
"v_mfma_f32_16x16x32_bf16 a[32:35],v[160:163],v[132:135],a[32:35]\n"
"v_mfma_f32_16x16x32_bf16 a[32:35],v[224:227],v[196:199],a[32:35]\n"
"v_mfma_f32_16x16x32_bf16 a[36:39],v[228:231],v[196:199],a[36:39]\n"
"v_mfma_f32_16x16x32_bf16 a[36:39],v[164:167],v[132:135],a[36:39]\n"
"v_mfma_f32_16x16x32_bf16 a[40:43],v[168:171],v[132:135],a[40:43]\n"
"s_waitcnt lgkmcnt(0)\n"
"s_barrier\n"
"v_mfma_f32_16x16x32_bf16 a[40:43],v[232:235],v[196:199],a[40:43]\n"
"s_mov_b32 s77,2621440\n"
"s_add_u32 m0,s78,98304\n"
"v_mfma_f32_16x16x32_bf16 a[44:47],v[236:239],v[196:199],a[44:47]\n"
"buffer_load_dwordx4 v110,s[20:23],s77 offen offset:0 lds\n"
"v_mfma_f32_16x16x32_bf16 a[44:47],v[172:175],v[132:135],a[44:47]\n"
"v_mfma_f32_16x16x32_bf16 a[48:51],v[176:179],v[132:135],a[48:51]\n"
"v_mfma_f32_16x16x32_bf16 a[48:51],v[240:243],v[196:199],a[48:51]\n"
"v_mfma_f32_16x16x32_bf16 a[52:55],v[244:247],v[196:199],a[52:55]\n"
"buffer_load_dwordx4 v111,s[20:23],s77 offen offset:1024 lds\n"
"v_mfma_f32_16x16x32_bf16 a[52:55],v[180:183],v[132:135],a[52:55]\n"
"v_mfma_f32_16x16x32_bf16 a[56:59],v[184:187],v[132:135],a[56:59]\n"
"v_mfma_f32_16x16x32_bf16 a[56:59],v[248:251],v[196:199],a[56:59]\n"
"v_mfma_f32_16x16x32_bf16 a[60:63],v[252:255],v[196:199],a[60:63]\n"
"buffer_load_dwordx4 v113,s[20:23],s77 offen offset:2048 lds\n"
"v_mfma_f32_16x16x32_bf16 a[60:63],v[188:191],v[132:135],a[60:63]\n"
"v_mfma_f32_16x16x32_bf16 a[64:67],v[160:163],v[136:139],a[64:67]\n"
"v_mfma_f32_16x16x32_bf16 a[64:67],v[224:227],v[200:203],a[64:67]\n"
"v_mfma_f32_16x16x32_bf16 a[68:71],v[228:231],v[200:203],a[68:71]\n"
"buffer_load_dwordx4 v114,s[20:23],s77 offen offset:3072 lds\n"
"v_mfma_f32_16x16x32_bf16 a[68:71],v[164:167],v[136:139],a[68:71]\n"
"v_mfma_f32_16x16x32_bf16 a[72:75],v[168:171],v[136:139],a[72:75]\n"
"v_mfma_f32_16x16x32_bf16 a[72:75],v[232:235],v[200:203],a[72:75]\n"
"s_mov_b32 s77,2883584\n"
"s_add_u32 m0,s78,114688\n"
"v_mfma_f32_16x16x32_bf16 a[76:79],v[236:239],v[200:203],a[76:79]\n"
"buffer_load_dwordx4 v110,s[20:23],s77 offen offset:0 lds\n"
"v_mfma_f32_16x16x32_bf16 a[76:79],v[172:175],v[136:139],a[76:79]\n"
"v_mfma_f32_16x16x32_bf16 a[80:83],v[176:179],v[136:139],a[80:83]\n"
"v_mfma_f32_16x16x32_bf16 a[80:83],v[240:243],v[200:203],a[80:83]\n"
"v_mfma_f32_16x16x32_bf16 a[84:87],v[244:247],v[200:203],a[84:87]\n"
"buffer_load_dwordx4 v111,s[20:23],s77 offen offset:1024 lds\n"
"v_mfma_f32_16x16x32_bf16 a[84:87],v[180:183],v[136:139],a[84:87]\n"
"v_mfma_f32_16x16x32_bf16 a[88:91],v[184:187],v[136:139],a[88:91]\n"
"v_mfma_f32_16x16x32_bf16 a[88:91],v[248:251],v[200:203],a[88:91]\n"
"v_mfma_f32_16x16x32_bf16 a[92:95],v[252:255],v[200:203],a[92:95]\n"
"buffer_load_dwordx4 v113,s[20:23],s77 offen offset:2048 lds\n"
"v_mfma_f32_16x16x32_bf16 a[92:95],v[188:191],v[136:139],a[92:95]\n"
"v_mfma_f32_16x16x32_bf16 a[96:99],v[160:163],v[140:143],a[96:99]\n"
"v_mfma_f32_16x16x32_bf16 a[96:99],v[224:227],v[204:207],a[96:99]\n"
"v_mfma_f32_16x16x32_bf16 a[100:103],v[228:231],v[204:207],a[100:103]\n"
"buffer_load_dwordx4 v114,s[20:23],s77 offen offset:3072 lds\n"
"v_mfma_f32_16x16x32_bf16 a[100:103],v[164:167],v[140:143],a[100:103]\n"
"v_mfma_f32_16x16x32_bf16 a[104:107],v[168:171],v[140:143],a[104:107]\n"
"v_mfma_f32_16x16x32_bf16 a[104:107],v[232:235],v[204:207],a[104:107]\n"
"v_mfma_f32_16x16x32_bf16 a[108:111],v[236:239],v[204:207],a[108:111]\n"
"s_mov_b32 s76,768\n"
"s_add_u32 m0,s79,0\n"
"v_mfma_f32_16x16x32_bf16 a[108:111],v[172:175],v[140:143],a[108:111]\n"
"buffer_load_dwordx4 v106,s[16:19],s76 offen offset:0 lds\n"
"v_mfma_f32_16x16x32_bf16 a[112:115],v[176:179],v[140:143],a[112:115]\n"
"v_mfma_f32_16x16x32_bf16 a[112:115],v[240:243],v[204:207],a[112:115]\n"
"v_mfma_f32_16x16x32_bf16 a[116:119],v[244:247],v[204:207],a[116:119]\n"
"v_mfma_f32_16x16x32_bf16 a[116:119],v[180:183],v[140:143],a[116:119]\n"
"v_mfma_f32_16x16x32_bf16 a[120:123],v[184:187],v[140:143],a[120:123]\n"
"buffer_load_dwordx4 v107,s[16:19],s76 offen offset:1024 lds\n"
"v_mfma_f32_16x16x32_bf16 a[120:123],v[248:251],v[204:207],a[120:123]\n"
"v_mfma_f32_16x16x32_bf16 a[124:127],v[252:255],v[204:207],a[124:127]\n"
"v_mfma_f32_16x16x32_bf16 a[124:127],v[188:191],v[140:143],a[124:127]\n"
"v_mfma_f32_16x16x32_bf16 a[128:131],v[160:163],v[144:147],a[128:131]\n"
"v_mfma_f32_16x16x32_bf16 a[128:131],v[224:227],v[208:211],a[128:131]\n"
"buffer_load_dwordx4 v108,s[16:19],s76 offen offset:2048 lds\n"
"v_mfma_f32_16x16x32_bf16 a[132:135],v[228:231],v[208:211],a[132:135]\n"
"v_mfma_f32_16x16x32_bf16 a[132:135],v[164:167],v[144:147],a[132:135]\n"
"v_mfma_f32_16x16x32_bf16 a[136:139],v[168:171],v[144:147],a[136:139]\n"
"v_mfma_f32_16x16x32_bf16 a[136:139],v[232:235],v[208:211],a[136:139]\n"
"v_mfma_f32_16x16x32_bf16 a[140:143],v[236:239],v[208:211],a[140:143]\n"
"buffer_load_dwordx4 v109,s[16:19],s76 offen offset:3072 lds\n"
"v_mfma_f32_16x16x32_bf16 a[140:143],v[172:175],v[144:147],a[140:143]\n"
"v_mfma_f32_16x16x32_bf16 a[144:147],v[176:179],v[144:147],a[144:147]\n"
"v_mfma_f32_16x16x32_bf16 a[144:147],v[240:243],v[208:211],a[144:147]\n"
"v_mfma_f32_16x16x32_bf16 a[148:151],v[244:247],v[208:211],a[148:151]\n"
"s_mov_b32 s76,262912\n"
"s_add_u32 m0,s79,4096\n"
"v_mfma_f32_16x16x32_bf16 a[148:151],v[180:183],v[144:147],a[148:151]\n"
"buffer_load_dwordx4 v106,s[16:19],s76 offen offset:0 lds\n"
"v_mfma_f32_16x16x32_bf16 a[152:155],v[184:187],v[144:147],a[152:155]\n"
"v_mfma_f32_16x16x32_bf16 a[152:155],v[248:251],v[208:211],a[152:155]\n"
"v_mfma_f32_16x16x32_bf16 a[156:159],v[252:255],v[208:211],a[156:159]\n"
"v_mfma_f32_16x16x32_bf16 a[156:159],v[188:191],v[144:147],a[156:159]\n"
"v_mfma_f32_16x16x32_bf16 a[160:163],v[160:163],v[148:151],a[160:163]\n"
"buffer_load_dwordx4 v107,s[16:19],s76 offen offset:1024 lds\n"
"v_mfma_f32_16x16x32_bf16 a[160:163],v[224:227],v[212:215],a[160:163]\n"
"v_mfma_f32_16x16x32_bf16 a[164:167],v[228:231],v[212:215],a[164:167]\n"
"v_mfma_f32_16x16x32_bf16 a[164:167],v[164:167],v[148:151],a[164:167]\n"
"v_mfma_f32_16x16x32_bf16 a[168:171],v[168:171],v[148:151],a[168:171]\n"
"v_mfma_f32_16x16x32_bf16 a[168:171],v[232:235],v[212:215],a[168:171]\n"
"buffer_load_dwordx4 v108,s[16:19],s76 offen offset:2048 lds\n"
"v_mfma_f32_16x16x32_bf16 a[172:175],v[236:239],v[212:215],a[172:175]\n"
"v_mfma_f32_16x16x32_bf16 a[172:175],v[172:175],v[148:151],a[172:175]\n"
"v_mfma_f32_16x16x32_bf16 a[176:179],v[176:179],v[148:151],a[176:179]\n"
"v_mfma_f32_16x16x32_bf16 a[176:179],v[240:243],v[212:215],a[176:179]\n"
"v_mfma_f32_16x16x32_bf16 a[180:183],v[244:247],v[212:215],a[180:183]\n"
"buffer_load_dwordx4 v109,s[16:19],s76 offen offset:3072 lds\n"
"v_mfma_f32_16x16x32_bf16 a[180:183],v[180:183],v[148:151],a[180:183]\n"
"s_mov_b32 s87,3145728\n"
"v_mfma_f32_16x16x32_bf16 a[184:187],v[184:187],v[148:151],a[184:187]\n"
"buffer_load_dword v115,v112,s[40:43],s87 offen\n"
"v_mfma_f32_16x16x32_bf16 a[184:187],v[248:251],v[212:215],a[184:187]\n"
"s_mov_b32 s87,4194304\n"
"v_mfma_f32_16x16x32_bf16 a[188:191],v[252:255],v[212:215],a[188:191]\n"
"buffer_load_dword v115,v112,s[40:43],s87 offen\n"
"v_mfma_f32_16x16x32_bf16 a[188:191],v[188:191],v[148:151],a[188:191]\n"
"s_mov_b32 s87,5242880\n"
"s_waitcnt vmcnt(33)\n"
"s_barrier\n"
"v_mfma_f32_16x16x32_bf16 a[192:195],v[224:227],v[216:219],a[192:195]\n"
"ds_read_b128 v[152:155],v80 offset:45056\n"
"buffer_load_dword v115,v112,s[40:43],s87 offen\n"
"v_mfma_f32_16x16x32_bf16 a[224:227],v[224:227],v[220:223],a[224:227]\n"
"ds_read_b128 v[156:159],v80 offset:47104\n"
"v_mfma_f32_16x16x32_bf16 a[228:231],v[228:231],v[220:223],a[228:231]\n"
"ds_read_b64_tr_b16 v[160:161],v98 offset:49152\n"
"v_mfma_f32_16x16x32_bf16 a[196:199],v[228:231],v[216:219],a[196:199]\n"
"ds_read_b64_tr_b16 v[162:163],v98 offset:50176\n"
"v_mfma_f32_16x16x32_bf16 a[200:203],v[232:235],v[216:219],a[200:203]\n"
"ds_read_b64_tr_b16 v[164:165],v99 offset:49152\n"
"v_mfma_f32_16x16x32_bf16 a[232:235],v[232:235],v[220:223],a[232:235]\n"
"ds_read_b64_tr_b16 v[166:167],v99 offset:50176\n"
"v_mfma_f32_16x16x32_bf16 a[236:239],v[236:239],v[220:223],a[236:239]\n"
"ds_read_b64_tr_b16 v[168:169],v100 offset:49152\n"
"v_mfma_f32_16x16x32_bf16 a[204:207],v[236:239],v[216:219],a[204:207]\n"
"ds_read_b64_tr_b16 v[170:171],v100 offset:50176\n"
"v_mfma_f32_16x16x32_bf16 a[208:211],v[240:243],v[216:219],a[208:211]\n"
"ds_read_b64_tr_b16 v[172:173],v101 offset:49152\n"
"v_mfma_f32_16x16x32_bf16 a[240:243],v[240:243],v[220:223],a[240:243]\n"
"ds_read_b64_tr_b16 v[174:175],v101 offset:50176\n"
"v_mfma_f32_16x16x32_bf16 a[244:247],v[244:247],v[220:223],a[244:247]\n"
"ds_read_b64_tr_b16 v[176:177],v102 offset:49152\n"
"v_mfma_f32_16x16x32_bf16 a[212:215],v[244:247],v[216:219],a[212:215]\n"
"ds_read_b64_tr_b16 v[178:179],v102 offset:50176\n"
"v_mfma_f32_16x16x32_bf16 a[216:219],v[248:251],v[216:219],a[216:219]\n"
"ds_read_b64_tr_b16 v[180:181],v103 offset:49152\n"
"v_mfma_f32_16x16x32_bf16 a[248:251],v[248:251],v[220:223],a[248:251]\n"
"ds_read_b64_tr_b16 v[182:183],v103 offset:50176\n"
"v_mfma_f32_16x16x32_bf16 a[252:255],v[252:255],v[220:223],a[252:255]\n"
"ds_read_b64_tr_b16 v[184:185],v104 offset:49152\n"
"ds_read_b64_tr_b16 v[186:187],v104 offset:50176\n"
"v_mfma_f32_16x16x32_bf16 a[220:223],v[252:255],v[216:219],a[220:223]\n"
"s_waitcnt vmcnt(19)\n"
"s_waitcnt lgkmcnt(10)\n"
"v_mfma_f32_16x16x32_bf16 a[192:195],v[160:163],v[152:155],a[192:195]\n"
"ds_read_b64_tr_b16 v[188:189],v105 offset:49152\n"
"ds_read_b64_tr_b16 v[190:191],v105 offset:50176\n"
"v_mfma_f32_16x16x32_bf16 a[224:227],v[160:163],v[156:159],a[224:227]\n"
"buffer_store_dwordx2 v[124:125],off,s[24:27],s57 sc1\n"
"v_mfma_f32_16x16x32_bf16 a[228:231],v[164:167],v[156:159],a[228:231]\n"
"ds_read_b128 v[128:131],v80 offset:32768\n"
"v_mfma_f32_16x16x32_bf16 a[196:199],v[164:167],v[152:155],a[196:199]\n"
"ds_read_b128 v[192:195],v81 offset:32768\n"
"s_waitcnt lgkmcnt(10)\n"
"v_mfma_f32_16x16x32_bf16 a[200:203],v[168:171],v[152:155],a[200:203]\n"
"ds_read_b64_tr_b16 v[224:225],v98 offset:32768\n"
"v_mfma_f32_16x16x32_bf16 a[232:235],v[168:171],v[156:159],a[232:235]\n"
"ds_read_b64_tr_b16 v[226:227],v98 offset:33792\n"
"v_mfma_f32_16x16x32_bf16 a[236:239],v[172:175],v[156:159],a[236:239]\n"
"ds_read_b64_tr_b16 v[228:229],v99 offset:32768\n"
"v_mfma_f32_16x16x32_bf16 a[204:207],v[172:175],v[152:155],a[204:207]\n"
"ds_read_b64_tr_b16 v[230:231],v99 offset:33792\n"
"s_waitcnt lgkmcnt(10)\n"
"v_mfma_f32_16x16x32_bf16 a[208:211],v[176:179],v[152:155],a[208:211]\n"
"ds_read_b64_tr_b16 v[232:233],v100 offset:32768\n"
"v_mfma_f32_16x16x32_bf16 a[240:243],v[176:179],v[156:159],a[240:243]\n"
"ds_read_b64_tr_b16 v[234:235],v100 offset:33792\n"
"v_mfma_f32_16x16x32_bf16 a[244:247],v[180:183],v[156:159],a[244:247]\n"
"ds_read_b64_tr_b16 v[236:237],v101 offset:32768\n"
"v_mfma_f32_16x16x32_bf16 a[212:215],v[180:183],v[152:155],a[212:215]\n"
"ds_read_b64_tr_b16 v[238:239],v101 offset:33792\n"
"s_waitcnt lgkmcnt(10)\n"
"v_mfma_f32_16x16x32_bf16 a[216:219],v[184:187],v[152:155],a[216:219]\n"
"ds_read_b64_tr_b16 v[240:241],v102 offset:32768\n"
"v_mfma_f32_16x16x32_bf16 a[248:251],v[184:187],v[156:159],a[248:251]\n"
"ds_read_b64_tr_b16 v[242:243],v102 offset:33792\n"
"v_mfma_f32_16x16x32_bf16 a[252:255],v[188:191],v[156:159],a[252:255]\n"
"ds_read_b64_tr_b16 v[244:245],v103 offset:32768\n"
"v_mfma_f32_16x16x32_bf16 a[220:223],v[188:191],v[152:155],a[220:223]\n"
"ds_read_b64_tr_b16 v[246:247],v103 offset:33792\n"
"s_waitcnt lgkmcnt(8)\n"
"v_mfma_f32_16x16x32_bf16 a[0:3],v[160:163],v[128:131],a[0:3]\n"
"ds_read_b64_tr_b16 v[248:249],v104 offset:32768\n"
"v_mfma_f32_16x16x32_bf16 a[0:3],v[224:227],v[192:195],a[0:3]\n"
"ds_read_b64_tr_b16 v[250:251],v104 offset:33792\n"
"v_mfma_f32_16x16x32_bf16 a[4:7],v[228:231],v[192:195],a[4:7]\n"
"ds_read_b64_tr_b16 v[252:253],v105 offset:32768\n"
"v_mfma_f32_16x16x32_bf16 a[4:7],v[164:167],v[128:131],a[4:7]\n"
"ds_read_b64_tr_b16 v[254:255],v105 offset:33792\n"
"s_waitcnt lgkmcnt(8)\n"
"v_mfma_f32_16x16x32_bf16 a[8:11],v[168:171],v[128:131],a[8:11]\n"
"ds_read_b128 v[132:135],v80 offset:34816\n"
"v_mfma_f32_16x16x32_bf16 a[8:11],v[232:235],v[192:195],a[8:11]\n"
"ds_read_b128 v[196:199],v81 offset:34816\n"
"v_mfma_f32_16x16x32_bf16 a[12:15],v[236:239],v[192:195],a[12:15]\n"
"ds_read_b128 v[136:139],v80 offset:36864\n"
"v_mfma_f32_16x16x32_bf16 a[12:15],v[172:175],v[128:131],a[12:15]\n"
"ds_read_b128 v[200:203],v81 offset:36864\n"
"s_waitcnt lgkmcnt(8)\n"
"v_mfma_f32_16x16x32_bf16 a[16:19],v[176:179],v[128:131],a[16:19]\n"
"ds_read_b128 v[140:143],v80 offset:38912\n"
"v_mfma_f32_16x16x32_bf16 a[16:19],v[240:243],v[192:195],a[16:19]\n"
"ds_read_b128 v[204:207],v81 offset:38912\n"
"v_mfma_f32_16x16x32_bf16 a[20:23],v[244:247],v[192:195],a[20:23]\n"
"ds_read_b128 v[144:147],v80 offset:40960\n"
"v_mfma_f32_16x16x32_bf16 a[20:23],v[180:183],v[128:131],a[20:23]\n"
"ds_read_b128 v[208:211],v81 offset:40960\n"
"s_waitcnt lgkmcnt(8)\n"
"v_mfma_f32_16x16x32_bf16 a[24:27],v[184:187],v[128:131],a[24:27]\n"
"ds_read_b128 v[148:151],v80 offset:43008\n"
"v_mfma_f32_16x16x32_bf16 a[24:27],v[248:251],v[192:195],a[24:27]\n"
"ds_read_b128 v[212:215],v81 offset:43008\n"
"v_mfma_f32_16x16x32_bf16 a[28:31],v[252:255],v[192:195],a[28:31]\n"
"ds_read_b128 v[216:219],v81 offset:45056\n"
"v_mfma_f32_16x16x32_bf16 a[28:31],v[188:191],v[128:131],a[28:31]\n"
"ds_read_b128 v[220:223],v81 offset:47104\n"
"s_waitcnt lgkmcnt(10)\n"
"v_mfma_f32_16x16x32_bf16 a[32:35],v[160:163],v[132:135],a[32:35]\n"
"v_mfma_f32_16x16x32_bf16 a[32:35],v[224:227],v[196:199],a[32:35]\n"
"v_mfma_f32_16x16x32_bf16 a[36:39],v[228:231],v[196:199],a[36:39]\n"
"v_mfma_f32_16x16x32_bf16 a[36:39],v[164:167],v[132:135],a[36:39]\n"
"v_mfma_f32_16x16x32_bf16 a[40:43],v[168:171],v[132:135],a[40:43]\n"
"s_waitcnt lgkmcnt(0)\n"
"s_barrier\n"
"v_mfma_f32_16x16x32_bf16 a[40:43],v[232:235],v[196:199],a[40:43]\n"
"s_mov_b32 s77,3145728\n"
"s_add_u32 m0,s78,147456\n"
"v_mfma_f32_16x16x32_bf16 a[44:47],v[236:239],v[196:199],a[44:47]\n"
"buffer_load_dwordx4 v110,s[20:23],s77 offen offset:0 lds\n"
"v_mfma_f32_16x16x32_bf16 a[44:47],v[172:175],v[132:135],a[44:47]\n"
"v_mfma_f32_16x16x32_bf16 a[48:51],v[176:179],v[132:135],a[48:51]\n"
"v_mfma_f32_16x16x32_bf16 a[48:51],v[240:243],v[196:199],a[48:51]\n"
"v_mfma_f32_16x16x32_bf16 a[52:55],v[244:247],v[196:199],a[52:55]\n"
"buffer_load_dwordx4 v111,s[20:23],s77 offen offset:1024 lds\n"
"v_mfma_f32_16x16x32_bf16 a[52:55],v[180:183],v[132:135],a[52:55]\n"
"v_mfma_f32_16x16x32_bf16 a[56:59],v[184:187],v[132:135],a[56:59]\n"
"v_mfma_f32_16x16x32_bf16 a[56:59],v[248:251],v[196:199],a[56:59]\n"
"v_mfma_f32_16x16x32_bf16 a[60:63],v[252:255],v[196:199],a[60:63]\n"
"buffer_load_dwordx4 v113,s[20:23],s77 offen offset:2048 lds\n"
"v_mfma_f32_16x16x32_bf16 a[60:63],v[188:191],v[132:135],a[60:63]\n"
"v_mfma_f32_16x16x32_bf16 a[64:67],v[160:163],v[136:139],a[64:67]\n"
"v_mfma_f32_16x16x32_bf16 a[64:67],v[224:227],v[200:203],a[64:67]\n"
"v_mfma_f32_16x16x32_bf16 a[68:71],v[228:231],v[200:203],a[68:71]\n"
"buffer_load_dwordx4 v114,s[20:23],s77 offen offset:3072 lds\n"
"v_mfma_f32_16x16x32_bf16 a[68:71],v[164:167],v[136:139],a[68:71]\n"
"v_mfma_f32_16x16x32_bf16 a[72:75],v[168:171],v[136:139],a[72:75]\n"
"v_mfma_f32_16x16x32_bf16 a[72:75],v[232:235],v[200:203],a[72:75]\n"
"s_mov_b32 s77,3407872\n"
"s_add_u32 m0,s78,131072\n"
"v_mfma_f32_16x16x32_bf16 a[76:79],v[236:239],v[200:203],a[76:79]\n"
"buffer_load_dwordx4 v110,s[20:23],s77 offen offset:0 lds\n"
"v_mfma_f32_16x16x32_bf16 a[76:79],v[172:175],v[136:139],a[76:79]\n"
"v_mfma_f32_16x16x32_bf16 a[80:83],v[176:179],v[136:139],a[80:83]\n"
"v_mfma_f32_16x16x32_bf16 a[80:83],v[240:243],v[200:203],a[80:83]\n"
"v_mfma_f32_16x16x32_bf16 a[84:87],v[244:247],v[200:203],a[84:87]\n"
"buffer_load_dwordx4 v111,s[20:23],s77 offen offset:1024 lds\n"
"v_mfma_f32_16x16x32_bf16 a[84:87],v[180:183],v[136:139],a[84:87]\n"
"v_mfma_f32_16x16x32_bf16 a[88:91],v[184:187],v[136:139],a[88:91]\n"
"v_mfma_f32_16x16x32_bf16 a[88:91],v[248:251],v[200:203],a[88:91]\n"
"v_mfma_f32_16x16x32_bf16 a[92:95],v[252:255],v[200:203],a[92:95]\n"
"buffer_load_dwordx4 v113,s[20:23],s77 offen offset:2048 lds\n"
"v_mfma_f32_16x16x32_bf16 a[92:95],v[188:191],v[136:139],a[92:95]\n"
"v_mfma_f32_16x16x32_bf16 a[96:99],v[160:163],v[140:143],a[96:99]\n"
"v_mfma_f32_16x16x32_bf16 a[96:99],v[224:227],v[204:207],a[96:99]\n"
"v_mfma_f32_16x16x32_bf16 a[100:103],v[228:231],v[204:207],a[100:103]\n"
"buffer_load_dwordx4 v114,s[20:23],s77 offen offset:3072 lds\n"
"v_mfma_f32_16x16x32_bf16 a[100:103],v[164:167],v[140:143],a[100:103]\n"
"v_mfma_f32_16x16x32_bf16 a[104:107],v[168:171],v[140:143],a[104:107]\n"
"v_mfma_f32_16x16x32_bf16 a[104:107],v[232:235],v[204:207],a[104:107]\n"
"v_mfma_f32_16x16x32_bf16 a[108:111],v[236:239],v[204:207],a[108:111]\n"
"s_mov_b32 s76,896\n"
"s_add_u32 m0,s79,32768\n"
"v_mfma_f32_16x16x32_bf16 a[108:111],v[172:175],v[140:143],a[108:111]\n"
"buffer_load_dwordx4 v106,s[16:19],s76 offen offset:0 lds\n"
"v_mfma_f32_16x16x32_bf16 a[112:115],v[176:179],v[140:143],a[112:115]\n"
"v_mfma_f32_16x16x32_bf16 a[112:115],v[240:243],v[204:207],a[112:115]\n"
"v_mfma_f32_16x16x32_bf16 a[116:119],v[244:247],v[204:207],a[116:119]\n"
"v_mfma_f32_16x16x32_bf16 a[116:119],v[180:183],v[140:143],a[116:119]\n"
"v_mfma_f32_16x16x32_bf16 a[120:123],v[184:187],v[140:143],a[120:123]\n"
"buffer_load_dwordx4 v107,s[16:19],s76 offen offset:1024 lds\n"
"v_mfma_f32_16x16x32_bf16 a[120:123],v[248:251],v[204:207],a[120:123]\n"
"v_mfma_f32_16x16x32_bf16 a[124:127],v[252:255],v[204:207],a[124:127]\n"
"v_mfma_f32_16x16x32_bf16 a[124:127],v[188:191],v[140:143],a[124:127]\n"
"v_mfma_f32_16x16x32_bf16 a[128:131],v[160:163],v[144:147],a[128:131]\n"
"v_mfma_f32_16x16x32_bf16 a[128:131],v[224:227],v[208:211],a[128:131]\n"
"buffer_load_dwordx4 v108,s[16:19],s76 offen offset:2048 lds\n"
"v_mfma_f32_16x16x32_bf16 a[132:135],v[228:231],v[208:211],a[132:135]\n"
"v_mfma_f32_16x16x32_bf16 a[132:135],v[164:167],v[144:147],a[132:135]\n"
"v_mfma_f32_16x16x32_bf16 a[136:139],v[168:171],v[144:147],a[136:139]\n"
"v_mfma_f32_16x16x32_bf16 a[136:139],v[232:235],v[208:211],a[136:139]\n"
"v_mfma_f32_16x16x32_bf16 a[140:143],v[236:239],v[208:211],a[140:143]\n"
"buffer_load_dwordx4 v109,s[16:19],s76 offen offset:3072 lds\n"
"v_mfma_f32_16x16x32_bf16 a[140:143],v[172:175],v[144:147],a[140:143]\n"
"v_mfma_f32_16x16x32_bf16 a[144:147],v[176:179],v[144:147],a[144:147]\n"
"v_mfma_f32_16x16x32_bf16 a[144:147],v[240:243],v[208:211],a[144:147]\n"
"v_mfma_f32_16x16x32_bf16 a[148:151],v[244:247],v[208:211],a[148:151]\n"
"s_mov_b32 s76,263040\n"
"s_add_u32 m0,s79,36864\n"
"v_mfma_f32_16x16x32_bf16 a[148:151],v[180:183],v[144:147],a[148:151]\n"
"buffer_load_dwordx4 v106,s[16:19],s76 offen offset:0 lds\n"
"v_mfma_f32_16x16x32_bf16 a[152:155],v[184:187],v[144:147],a[152:155]\n"
"v_mfma_f32_16x16x32_bf16 a[152:155],v[248:251],v[208:211],a[152:155]\n"
"v_mfma_f32_16x16x32_bf16 a[156:159],v[252:255],v[208:211],a[156:159]\n"
"v_mfma_f32_16x16x32_bf16 a[156:159],v[188:191],v[144:147],a[156:159]\n"
"v_mfma_f32_16x16x32_bf16 a[160:163],v[160:163],v[148:151],a[160:163]\n"
"buffer_load_dwordx4 v107,s[16:19],s76 offen offset:1024 lds\n"
"v_mfma_f32_16x16x32_bf16 a[160:163],v[224:227],v[212:215],a[160:163]\n"
"v_mfma_f32_16x16x32_bf16 a[164:167],v[228:231],v[212:215],a[164:167]\n"
"v_mfma_f32_16x16x32_bf16 a[164:167],v[164:167],v[148:151],a[164:167]\n"
"v_mfma_f32_16x16x32_bf16 a[168:171],v[168:171],v[148:151],a[168:171]\n"
"v_mfma_f32_16x16x32_bf16 a[168:171],v[232:235],v[212:215],a[168:171]\n"
"buffer_load_dwordx4 v108,s[16:19],s76 offen offset:2048 lds\n"
"v_mfma_f32_16x16x32_bf16 a[172:175],v[236:239],v[212:215],a[172:175]\n"
"v_mfma_f32_16x16x32_bf16 a[172:175],v[172:175],v[148:151],a[172:175]\n"
"v_mfma_f32_16x16x32_bf16 a[176:179],v[176:179],v[148:151],a[176:179]\n"
"v_mfma_f32_16x16x32_bf16 a[176:179],v[240:243],v[212:215],a[176:179]\n"
"v_mfma_f32_16x16x32_bf16 a[180:183],v[244:247],v[212:215],a[180:183]\n"
"buffer_load_dwordx4 v109,s[16:19],s76 offen offset:3072 lds\n"
"v_mfma_f32_16x16x32_bf16 a[180:183],v[180:183],v[148:151],a[180:183]\n"
"s_mov_b32 s87,6291456\n"
"v_mfma_f32_16x16x32_bf16 a[184:187],v[184:187],v[148:151],a[184:187]\n"
"buffer_load_dword v115,v112,s[40:43],s87 offen\n"
"v_mfma_f32_16x16x32_bf16 a[184:187],v[248:251],v[212:215],a[184:187]\n"
"s_mov_b32 s87,7340032\n"
"v_mfma_f32_16x16x32_bf16 a[188:191],v[252:255],v[212:215],a[188:191]\n"
"buffer_load_dword v115,v112,s[40:43],s87 offen\n"
"v_mfma_f32_16x16x32_bf16 a[188:191],v[188:191],v[148:151],a[188:191]\n"
"s_waitcnt vmcnt(30)\n"
"s_barrier\n"
"v_mfma_f32_16x16x32_bf16 a[192:195],v[224:227],v[216:219],a[192:195]\n"
"ds_read_b128 v[152:155],v82 offset:12288\n"
"v_mfma_f32_16x16x32_bf16 a[224:227],v[224:227],v[220:223],a[224:227]\n"
"ds_read_b128 v[156:159],v82 offset:14336\n"
"v_mfma_f32_16x16x32_bf16 a[228:231],v[228:231],v[220:223],a[228:231]\n"
"ds_read_b64_tr_b16 v[160:161],v98 offset:0\n"
"v_mfma_f32_16x16x32_bf16 a[196:199],v[228:231],v[216:219],a[196:199]\n"
"ds_read_b64_tr_b16 v[162:163],v98 offset:1024\n"
"v_mfma_f32_16x16x32_bf16 a[200:203],v[232:235],v[216:219],a[200:203]\n"
"ds_read_b64_tr_b16 v[164:165],v99 offset:0\n"
"v_mfma_f32_16x16x32_bf16 a[232:235],v[232:235],v[220:223],a[232:235]\n"
"ds_read_b64_tr_b16 v[166:167],v99 offset:1024\n"
"v_mfma_f32_16x16x32_bf16 a[236:239],v[236:239],v[220:223],a[236:239]\n"
"ds_read_b64_tr_b16 v[168:169],v100 offset:0\n"
"v_mfma_f32_16x16x32_bf16 a[204:207],v[236:239],v[216:219],a[204:207]\n"
"ds_read_b64_tr_b16 v[170:171],v100 offset:1024\n"
"v_mfma_f32_16x16x32_bf16 a[208:211],v[240:243],v[216:219],a[208:211]\n"
"ds_read_b64_tr_b16 v[172:173],v101 offset:0\n"
"v_mfma_f32_16x16x32_bf16 a[240:243],v[240:243],v[220:223],a[240:243]\n"
"ds_read_b64_tr_b16 v[174:175],v101 offset:1024\n"
"v_mfma_f32_16x16x32_bf16 a[244:247],v[244:247],v[220:223],a[244:247]\n"
"ds_read_b64_tr_b16 v[176:177],v102 offset:0\n"
"v_mfma_f32_16x16x32_bf16 a[212:215],v[244:247],v[216:219],a[212:215]\n"
"ds_read_b64_tr_b16 v[178:179],v102 offset:1024\n"
"v_mfma_f32_16x16x32_bf16 a[216:219],v[248:251],v[216:219],a[216:219]\n"
"ds_read_b64_tr_b16 v[180:181],v103 offset:0\n"
"v_mfma_f32_16x16x32_bf16 a[248:251],v[248:251],v[220:223],a[248:251]\n"
"ds_read_b64_tr_b16 v[182:183],v103 offset:1024\n"
"v_mfma_f32_16x16x32_bf16 a[252:255],v[252:255],v[220:223],a[252:255]\n"
"ds_read_b64_tr_b16 v[184:185],v104 offset:0\n"
"v_mfma_f32_16x16x32_bf16 a[220:223],v[252:255],v[216:219],a[220:223]\n"
"ds_read_b64_tr_b16 v[186:187],v104 offset:1024\n"
"s_waitcnt lgkmcnt(10)\n"
"v_mfma_f32_16x16x32_bf16 a[192:195],v[160:163],v[152:155],a[192:195]\n"
"ds_read_b64_tr_b16 v[188:189],v105 offset:0\n"
"v_mfma_f32_16x16x32_bf16 a[224:227],v[160:163],v[156:159],a[224:227]\n"
"ds_read_b64_tr_b16 v[190:191],v105 offset:1024\n"
"v_mfma_f32_16x16x32_bf16 a[228:231],v[164:167],v[156:159],a[228:231]\n"
"ds_read_b128 v[128:131],v82 offset:0\n"
"v_mfma_f32_16x16x32_bf16 a[196:199],v[164:167],v[152:155],a[196:199]\n"
"ds_read_b128 v[192:195],v83 offset:0\n"
"s_waitcnt lgkmcnt(10)\n"
"v_mfma_f32_16x16x32_bf16 a[200:203],v[168:171],v[152:155],a[200:203]\n"
"ds_read_b64_tr_b16 v[224:225],v98 offset:16384\n"
"v_mfma_f32_16x16x32_bf16 a[232:235],v[168:171],v[156:159],a[232:235]\n"
"ds_read_b64_tr_b16 v[226:227],v98 offset:17408\n"
"v_mfma_f32_16x16x32_bf16 a[236:239],v[172:175],v[156:159],a[236:239]\n"
"ds_read_b64_tr_b16 v[228:229],v99 offset:16384\n"
"v_mfma_f32_16x16x32_bf16 a[204:207],v[172:175],v[152:155],a[204:207]\n"
"ds_read_b64_tr_b16 v[230:231],v99 offset:17408\n"
"s_waitcnt lgkmcnt(10)\n"
"v_mfma_f32_16x16x32_bf16 a[208:211],v[176:179],v[152:155],a[208:211]\n"
"ds_read_b64_tr_b16 v[232:233],v100 offset:16384\n"
"v_mfma_f32_16x16x32_bf16 a[240:243],v[176:179],v[156:159],a[240:243]\n"
"ds_read_b64_tr_b16 v[234:235],v100 offset:17408\n"
"v_mfma_f32_16x16x32_bf16 a[244:247],v[180:183],v[156:159],a[244:247]\n"
"ds_read_b64_tr_b16 v[236:237],v101 offset:16384\n"
"v_mfma_f32_16x16x32_bf16 a[212:215],v[180:183],v[152:155],a[212:215]\n"
"ds_read_b64_tr_b16 v[238:239],v101 offset:17408\n"
"s_waitcnt lgkmcnt(10)\n"
"v_mfma_f32_16x16x32_bf16 a[216:219],v[184:187],v[152:155],a[216:219]\n"
"ds_read_b64_tr_b16 v[240:241],v102 offset:16384\n"
"v_mfma_f32_16x16x32_bf16 a[248:251],v[184:187],v[156:159],a[248:251]\n"
"ds_read_b64_tr_b16 v[242:243],v102 offset:17408\n"
"v_mfma_f32_16x16x32_bf16 a[252:255],v[188:191],v[156:159],a[252:255]\n"
"ds_read_b64_tr_b16 v[244:245],v103 offset:16384\n"
"v_mfma_f32_16x16x32_bf16 a[220:223],v[188:191],v[152:155],a[220:223]\n"
"ds_read_b64_tr_b16 v[246:247],v103 offset:17408\n"
"s_waitcnt lgkmcnt(8)\n"
"v_mfma_f32_16x16x32_bf16 a[0:3],v[160:163],v[128:131],a[0:3]\n"
"ds_read_b64_tr_b16 v[248:249],v104 offset:16384\n"
"v_mfma_f32_16x16x32_bf16 a[0:3],v[224:227],v[192:195],a[0:3]\n"
"ds_read_b64_tr_b16 v[250:251],v104 offset:17408\n"
"v_mfma_f32_16x16x32_bf16 a[4:7],v[228:231],v[192:195],a[4:7]\n"
"ds_read_b64_tr_b16 v[252:253],v105 offset:16384\n"
"v_mfma_f32_16x16x32_bf16 a[4:7],v[164:167],v[128:131],a[4:7]\n"
"ds_read_b64_tr_b16 v[254:255],v105 offset:17408\n"
"s_waitcnt lgkmcnt(8)\n"
"v_mfma_f32_16x16x32_bf16 a[8:11],v[168:171],v[128:131],a[8:11]\n"
"ds_read_b128 v[132:135],v82 offset:2048\n"
"v_mfma_f32_16x16x32_bf16 a[8:11],v[232:235],v[192:195],a[8:11]\n"
"ds_read_b128 v[196:199],v83 offset:2048\n"
"v_mfma_f32_16x16x32_bf16 a[12:15],v[236:239],v[192:195],a[12:15]\n"
"ds_read_b128 v[136:139],v82 offset:4096\n"
"v_mfma_f32_16x16x32_bf16 a[12:15],v[172:175],v[128:131],a[12:15]\n"
"ds_read_b128 v[200:203],v83 offset:4096\n"
"s_waitcnt lgkmcnt(8)\n"
"v_mfma_f32_16x16x32_bf16 a[16:19],v[176:179],v[128:131],a[16:19]\n"
"ds_read_b128 v[140:143],v82 offset:6144\n"
"v_mfma_f32_16x16x32_bf16 a[16:19],v[240:243],v[192:195],a[16:19]\n"
"ds_read_b128 v[204:207],v83 offset:6144\n"
"v_mfma_f32_16x16x32_bf16 a[20:23],v[244:247],v[192:195],a[20:23]\n"
"ds_read_b128 v[144:147],v82 offset:8192\n"
"v_mfma_f32_16x16x32_bf16 a[20:23],v[180:183],v[128:131],a[20:23]\n"
"ds_read_b128 v[208:211],v83 offset:8192\n"
"s_waitcnt lgkmcnt(8)\n"
"v_mfma_f32_16x16x32_bf16 a[24:27],v[184:187],v[128:131],a[24:27]\n"
"ds_read_b128 v[148:151],v82 offset:10240\n"
"v_mfma_f32_16x16x32_bf16 a[24:27],v[248:251],v[192:195],a[24:27]\n"
"ds_read_b128 v[212:215],v83 offset:10240\n"
"v_mfma_f32_16x16x32_bf16 a[28:31],v[252:255],v[192:195],a[28:31]\n"
"ds_read_b128 v[216:219],v83 offset:12288\n"
"v_mfma_f32_16x16x32_bf16 a[28:31],v[188:191],v[128:131],a[28:31]\n"
"ds_read_b128 v[220:223],v83 offset:14336\n"
"s_waitcnt lgkmcnt(10)\n"
"v_mfma_f32_16x16x32_bf16 a[32:35],v[160:163],v[132:135],a[32:35]\n"
"v_mfma_f32_16x16x32_bf16 a[32:35],v[224:227],v[196:199],a[32:35]\n"
"buffer_load_dwordx4 v[74:77],v112,s[24:27],s58 offen sc1\n"
"v_mfma_f32_16x16x32_bf16 a[36:39],v[228:231],v[196:199],a[36:39]\n"
"v_mfma_f32_16x16x32_bf16 a[36:39],v[164:167],v[132:135],a[36:39]\n"
"v_mfma_f32_16x16x32_bf16 a[40:43],v[168:171],v[132:135],a[40:43]\n"
"s_waitcnt lgkmcnt(0)\n"
"s_barrier\n"
"v_mfma_f32_16x16x32_bf16 a[40:43],v[232:235],v[196:199],a[40:43]\n"
"s_mov_b32 s77,3670016\n"
"s_add_u32 m0,s78,98304\n"
"v_mfma_f32_16x16x32_bf16 a[44:47],v[236:239],v[196:199],a[44:47]\n"
"buffer_load_dwordx4 v110,s[20:23],s77 offen offset:0 lds\n"
"v_mfma_f32_16x16x32_bf16 a[44:47],v[172:175],v[132:135],a[44:47]\n"
"v_mfma_f32_16x16x32_bf16 a[48:51],v[176:179],v[132:135],a[48:51]\n"
"s_load_dwordx2 s[48:49],s[0:1],56\n"
"v_mfma_f32_16x16x32_bf16 a[48:51],v[240:243],v[196:199],a[48:51]\n"
"v_mfma_f32_16x16x32_bf16 a[52:55],v[244:247],v[196:199],a[52:55]\n"
"buffer_load_dwordx4 v111,s[20:23],s77 offen offset:1024 lds\n"
"v_mfma_f32_16x16x32_bf16 a[52:55],v[180:183],v[132:135],a[52:55]\n"
"v_mfma_f32_16x16x32_bf16 a[56:59],v[184:187],v[132:135],a[56:59]\n"
"v_mfma_f32_16x16x32_bf16 a[56:59],v[248:251],v[196:199],a[56:59]\n"
"v_mfma_f32_16x16x32_bf16 a[60:63],v[252:255],v[196:199],a[60:63]\n"
"buffer_load_dwordx4 v113,s[20:23],s77 offen offset:2048 lds\n"
"v_mfma_f32_16x16x32_bf16 a[60:63],v[188:191],v[132:135],a[60:63]\n"
"v_mfma_f32_16x16x32_bf16 a[64:67],v[160:163],v[136:139],a[64:67]\n"
"v_mfma_f32_16x16x32_bf16 a[64:67],v[224:227],v[200:203],a[64:67]\n"
"v_mfma_f32_16x16x32_bf16 a[68:71],v[228:231],v[200:203],a[68:71]\n"
"buffer_load_dwordx4 v114,s[20:23],s77 offen offset:3072 lds\n"
"v_mfma_f32_16x16x32_bf16 a[68:71],v[164:167],v[136:139],a[68:71]\n"
"v_mfma_f32_16x16x32_bf16 a[72:75],v[168:171],v[136:139],a[72:75]\n"
"v_mfma_f32_16x16x32_bf16 a[72:75],v[232:235],v[200:203],a[72:75]\n"
"s_mov_b32 s77,3932160\n"
"s_add_u32 m0,s78,114688\n"
"v_mfma_f32_16x16x32_bf16 a[76:79],v[236:239],v[200:203],a[76:79]\n"
"buffer_load_dwordx4 v110,s[20:23],s77 offen offset:0 lds\n"
"v_mfma_f32_16x16x32_bf16 a[76:79],v[172:175],v[136:139],a[76:79]\n"
"v_mfma_f32_16x16x32_bf16 a[80:83],v[176:179],v[136:139],a[80:83]\n"
"v_mfma_f32_16x16x32_bf16 a[80:83],v[240:243],v[200:203],a[80:83]\n"
"v_mfma_f32_16x16x32_bf16 a[84:87],v[244:247],v[200:203],a[84:87]\n"
"buffer_load_dwordx4 v111,s[20:23],s77 offen offset:1024 lds\n"
"v_mfma_f32_16x16x32_bf16 a[84:87],v[180:183],v[136:139],a[84:87]\n"
"v_mfma_f32_16x16x32_bf16 a[88:91],v[184:187],v[136:139],a[88:91]\n"
"v_mfma_f32_16x16x32_bf16 a[88:91],v[248:251],v[200:203],a[88:91]\n"
"v_mfma_f32_16x16x32_bf16 a[92:95],v[252:255],v[200:203],a[92:95]\n"
"buffer_load_dwordx4 v113,s[20:23],s77 offen offset:2048 lds\n"
"v_mfma_f32_16x16x32_bf16 a[92:95],v[188:191],v[136:139],a[92:95]\n"
"s_mov_b32 s76,s55\n"
"s_waitcnt vmcnt(7)\n"
"v_mfma_f32_16x16x32_bf16 a[96:99],v[160:163],v[140:143],a[96:99]\n"
"v_xor_b32 v116,s80,v74\n"
"v_xor_b32 v117,s81,v75\n"
"v_mfma_f32_16x16x32_bf16 a[96:99],v[224:227],v[204:207],a[96:99]\n"
"v_or_b32 v116,v116,v117\n"
"v_xor_b32 v117,s80,v76\n"
"v_mfma_f32_16x16x32_bf16 a[100:103],v[228:231],v[204:207],a[100:103]\n"
"buffer_load_dwordx4 v114,s[20:23],s77 offen offset:3072 lds\n"
"v_mfma_f32_16x16x32_bf16 a[100:103],v[164:167],v[140:143],a[100:103]\n"
"v_or_b32 v116,v116,v117\n"
"v_xor_b32 v117,s81,v77\n"
"v_mfma_f32_16x16x32_bf16 a[104:107],v[168:171],v[140:143],a[104:107]\n"
"v_or_b32 v116,v116,v117\n"
"v_cmp_ne_u32 vcc,0,v116\n"
"v_mfma_f32_16x16x32_bf16 a[104:107],v[232:235],v[204:207],a[104:107]\n"
"s_cbranch_vccnz .Lfk_xn0_s\n"
".Lfk_xn0_ok:\n"
"v_mfma_f32_16x16x32_bf16 a[108:111],v[236:239],v[204:207],a[108:111]\n"
"s_add_u32 m0,s78,65536\n"
"v_mfma_f32_16x16x32_bf16 a[108:111],v[172:175],v[140:143],a[108:111]\n"
"buffer_load_dwordx4 v112,s[24:27],s76 offen offset:0 sc1 lds\n"
"v_mfma_f32_16x16x32_bf16 a[112:115],v[176:179],v[140:143],a[112:115]\n"
"s_waitcnt lgkmcnt(0)\n"
"s_lshl2_add_u32 s88,s100,s52\n"
"v_mfma_f32_16x16x32_bf16 a[112:115],v[240:243],v[204:207],a[112:115]\n"
"s_mul_i32 s88,s88,7168\n"
"s_add_u32 s88,s88,0\n"
"v_mfma_f32_16x16x32_bf16 a[116:119],v[244:247],v[204:207],a[116:119]\n"
"s_add_u32 s48,s48,s88\n"
"s_addc_u32 s49,s49,0\n"
"s_mov_b32 s50,3584\n"
"v_mfma_f32_16x16x32_bf16 a[116:119],v[180:183],v[140:143],a[116:119]\n"
"s_mov_b32 s51,0x20000\n"
"v_lshlrev_b32 v8,3,v112\n"
"v_mfma_f32_16x16x32_bf16 a[120:123],v[184:187],v[140:143],a[120:123]\n"
"buffer_load_dwordx4 v112,s[24:27],s76 offen offset:1024 sc1 lds\n"
"v_mfma_f32_16x16x32_bf16 a[120:123],v[248:251],v[204:207],a[120:123]\n"
"v_mfma_f32_16x16x32_bf16 a[124:127],v[252:255],v[204:207],a[124:127]\n"
"v_mfma_f32_16x16x32_bf16 a[124:127],v[188:191],v[140:143],a[124:127]\n"
"v_mfma_f32_16x16x32_bf16 a[128:131],v[160:163],v[144:147],a[128:131]\n"
"v_mfma_f32_16x16x32_bf16 a[128:131],v[224:227],v[208:211],a[128:131]\n"
"buffer_load_dwordx4 v112,s[24:27],s76 offen offset:2048 sc1 lds\n"
"v_mfma_f32_16x16x32_bf16 a[132:135],v[228:231],v[208:211],a[132:135]\n"
"v_mfma_f32_16x16x32_bf16 a[132:135],v[164:167],v[144:147],a[132:135]\n"
"v_mfma_f32_16x16x32_bf16 a[136:139],v[168:171],v[144:147],a[136:139]\n"
"v_mfma_f32_16x16x32_bf16 a[136:139],v[232:235],v[208:211],a[136:139]\n"
"buffer_load_dwordx4 v112,s[24:27],s76 offen offset:3072 sc1 lds\n"
"v_mfma_f32_16x16x32_bf16 a[140:143],v[236:239],v[208:211],a[140:143]\n"
"s_xor_b32 s76,s76,16384\n"
"v_mfma_f32_16x16x32_bf16 a[140:143],v[172:175],v[144:147],a[140:143]\n"
"v_mfma_f32_16x16x32_bf16 a[144:147],v[176:179],v[144:147],a[144:147]\n"
"v_mfma_f32_16x16x32_bf16 a[144:147],v[240:243],v[208:211],a[144:147]\n"
"v_mfma_f32_16x16x32_bf16 a[148:151],v[244:247],v[208:211],a[148:151]\n"
"s_add_u32 m0,s78,81920\n"
"v_mfma_f32_16x16x32_bf16 a[148:151],v[180:183],v[144:147],a[148:151]\n"
"buffer_load_dwordx4 v112,s[24:27],s76 offen offset:0 sc1 lds\n"
"v_mfma_f32_16x16x32_bf16 a[152:155],v[184:187],v[144:147],a[152:155]\n"
"v_mfma_f32_16x16x32_bf16 a[152:155],v[248:251],v[208:211],a[152:155]\n"
"v_mfma_f32_16x16x32_bf16 a[156:159],v[252:255],v[208:211],a[156:159]\n"
"v_mfma_f32_16x16x32_bf16 a[156:159],v[188:191],v[144:147],a[156:159]\n"
"v_mfma_f32_16x16x32_bf16 a[160:163],v[160:163],v[148:151],a[160:163]\n"
"buffer_load_dwordx4 v112,s[24:27],s76 offen offset:1024 sc1 lds\n"
"v_mfma_f32_16x16x32_bf16 a[160:163],v[224:227],v[212:215],a[160:163]\n"
"v_mfma_f32_16x16x32_bf16 a[164:167],v[228:231],v[212:215],a[164:167]\n"
"v_mfma_f32_16x16x32_bf16 a[164:167],v[164:167],v[148:151],a[164:167]\n"
"v_mfma_f32_16x16x32_bf16 a[168:171],v[168:171],v[148:151],a[168:171]\n"
"v_mfma_f32_16x16x32_bf16 a[168:171],v[232:235],v[212:215],a[168:171]\n"
"buffer_load_dwordx4 v112,s[24:27],s76 offen offset:2048 sc1 lds\n"
"v_mfma_f32_16x16x32_bf16 a[172:175],v[236:239],v[212:215],a[172:175]\n"
"v_mfma_f32_16x16x32_bf16 a[172:175],v[172:175],v[148:151],a[172:175]\n"
"v_mfma_f32_16x16x32_bf16 a[176:179],v[176:179],v[148:151],a[176:179]\n"
"v_mfma_f32_16x16x32_bf16 a[176:179],v[240:243],v[212:215],a[176:179]\n"
"buffer_load_dwordx4 v112,s[24:27],s76 offen offset:3072 sc1 lds\n"
"v_mfma_f32_16x16x32_bf16 a[180:183],v[244:247],v[212:215],a[180:183]\n"
"s_xor_b32 s76,s76,49152\n"
"v_mfma_f32_16x16x32_bf16 a[180:183],v[180:183],v[148:151],a[180:183]\n"
"v_mfma_f32_16x16x32_bf16 a[184:187],v[184:187],v[148:151],a[184:187]\n"
"buffer_load_dword v115,v8,s[48:51],0 offen\n"
"v_mfma_f32_16x16x32_bf16 a[184:187],v[248:251],v[212:215],a[184:187]\n"
"v_mfma_f32_16x16x32_bf16 a[188:191],v[252:255],v[212:215],a[188:191]\n"
"v_mfma_f32_16x16x32_bf16 a[188:191],v[188:191],v[148:151],a[188:191]\n"
"s_barrier\n"
"v_mfma_f32_16x16x32_bf16 a[192:195],v[224:227],v[216:219],a[192:195]\n"
"ds_read_b128 v[152:155],v80 offset:12288\n"
"v_mfma_f32_16x16x32_bf16 a[224:227],v[224:227],v[220:223],a[224:227]\n"
"ds_read_b128 v[156:159],v80 offset:14336\n"
"v_mfma_f32_16x16x32_bf16 a[228:231],v[228:231],v[220:223],a[228:231]\n"
"ds_read_b64_tr_b16 v[160:161],v98 offset:49152\n"
"v_mfma_f32_16x16x32_bf16 a[196:199],v[228:231],v[216:219],a[196:199]\n"
"ds_read_b64_tr_b16 v[162:163],v98 offset:50176\n"
"v_mfma_f32_16x16x32_bf16 a[200:203],v[232:235],v[216:219],a[200:203]\n"
"ds_read_b64_tr_b16 v[164:165],v99 offset:49152\n"
"v_mfma_f32_16x16x32_bf16 a[232:235],v[232:235],v[220:223],a[232:235]\n"
"ds_read_b64_tr_b16 v[166:167],v99 offset:50176\n"
"v_mfma_f32_16x16x32_bf16 a[236:239],v[236:239],v[220:223],a[236:239]\n"
"ds_read_b64_tr_b16 v[168:169],v100 offset:49152\n"
"v_mfma_f32_16x16x32_bf16 a[204:207],v[236:239],v[216:219],a[204:207]\n"
"ds_read_b64_tr_b16 v[170:171],v100 offset:50176\n"
"v_mfma_f32_16x16x32_bf16 a[208:211],v[240:243],v[216:219],a[208:211]\n"
"ds_read_b64_tr_b16 v[172:173],v101 offset:49152\n"
"v_mfma_f32_16x16x32_bf16 a[240:243],v[240:243],v[220:223],a[240:243]\n"
"ds_read_b64_tr_b16 v[174:175],v101 offset:50176\n"
"v_mfma_f32_16x16x32_bf16 a[244:247],v[244:247],v[220:223],a[244:247]\n"
"ds_read_b64_tr_b16 v[176:177],v102 offset:49152\n"
"v_mfma_f32_16x16x32_bf16 a[212:215],v[244:247],v[216:219],a[212:215]\n"
"ds_read_b64_tr_b16 v[178:179],v102 offset:50176\n"
"v_mfma_f32_16x16x32_bf16 a[216:219],v[248:251],v[216:219],a[216:219]\n"
"ds_read_b64_tr_b16 v[180:181],v103 offset:49152\n"
"v_mfma_f32_16x16x32_bf16 a[248:251],v[248:251],v[220:223],a[248:251]\n"
"ds_read_b64_tr_b16 v[182:183],v103 offset:50176\n"
"ds_read_b64_tr_b16 v[184:185],v104 offset:49152\n"
"v_mfma_f32_16x16x32_bf16 a[252:255],v[252:255],v[220:223],a[252:255]\n"
"v_mov_b32 v110,v72\n"
"v_mov_b32 v111,v73\n"
"v_mfma_f32_16x16x32_bf16 a[220:223],v[252:255],v[216:219],a[220:223]\n"
"ds_read_b64_tr_b16 v[186:187],v104 offset:50176\n"
"s_waitcnt lgkmcnt(10)\n"
"v_mfma_f32_16x16x32_bf16 a[192:195],v[160:163],v[152:155],a[192:195]\n"
"ds_read_b64_tr_b16 v[188:189],v105 offset:49152\n"
"v_mfma_f32_16x16x32_bf16 a[224:227],v[160:163],v[156:159],a[224:227]\n"
"ds_read_b64_tr_b16 v[190:191],v105 offset:50176\n"
"v_mfma_f32_16x16x32_bf16 a[228:231],v[164:167],v[156:159],a[228:231]\n"
"ds_read_b128 v[128:131],v80 offset:0\n"
"v_mfma_f32_16x16x32_bf16 a[196:199],v[164:167],v[152:155],a[196:199]\n"
"ds_read_b128 v[192:195],v81 offset:0\n"
"s_waitcnt lgkmcnt(10)\n"
"v_mfma_f32_16x16x32_bf16 a[200:203],v[168:171],v[152:155],a[200:203]\n"
"ds_read_b64_tr_b16 v[224:225],v98 offset:32768\n"
"v_mfma_f32_16x16x32_bf16 a[232:235],v[168:171],v[156:159],a[232:235]\n"
"ds_read_b64_tr_b16 v[226:227],v98 offset:33792\n"
"v_mfma_f32_16x16x32_bf16 a[236:239],v[172:175],v[156:159],a[236:239]\n"
"ds_read_b64_tr_b16 v[228:229],v99 offset:32768\n"
"v_mfma_f32_16x16x32_bf16 a[204:207],v[172:175],v[152:155],a[204:207]\n"
"ds_read_b64_tr_b16 v[230:231],v99 offset:33792\n"
"s_waitcnt lgkmcnt(10)\n"
"v_mfma_f32_16x16x32_bf16 a[208:211],v[176:179],v[152:155],a[208:211]\n"
"ds_read_b64_tr_b16 v[232:233],v100 offset:32768\n"
"v_mfma_f32_16x16x32_bf16 a[240:243],v[176:179],v[156:159],a[240:243]\n"
"ds_read_b64_tr_b16 v[234:235],v100 offset:33792\n"
"v_mfma_f32_16x16x32_bf16 a[244:247],v[180:183],v[156:159],a[244:247]\n"
"ds_read_b64_tr_b16 v[236:237],v101 offset:32768\n"
"v_mfma_f32_16x16x32_bf16 a[212:215],v[180:183],v[152:155],a[212:215]\n"
"ds_read_b64_tr_b16 v[238:239],v101 offset:33792\n"
"s_waitcnt lgkmcnt(10)\n"
"v_mfma_f32_16x16x32_bf16 a[216:219],v[184:187],v[152:155],a[216:219]\n"
"ds_read_b64_tr_b16 v[240:241],v102 offset:32768\n"
"v_mfma_f32_16x16x32_bf16 a[248:251],v[184:187],v[156:159],a[248:251]\n"
"ds_read_b64_tr_b16 v[242:243],v102 offset:33792\n"
"v_mfma_f32_16x16x32_bf16 a[252:255],v[188:191],v[156:159],a[252:255]\n"
"ds_read_b64_tr_b16 v[244:245],v103 offset:32768\n"
"v_mfma_f32_16x16x32_bf16 a[220:223],v[188:191],v[152:155],a[220:223]\n"
"ds_read_b64_tr_b16 v[246:247],v103 offset:33792\n"
"s_waitcnt lgkmcnt(8)\n"
"v_mfma_f32_16x16x32_bf16 a[0:3],v[160:163],v[128:131],a[0:3]\n"
"ds_read_b64_tr_b16 v[248:249],v104 offset:32768\n"
"v_mfma_f32_16x16x32_bf16 a[0:3],v[224:227],v[192:195],a[0:3]\n"
"ds_read_b64_tr_b16 v[250:251],v104 offset:33792\n"
"v_mfma_f32_16x16x32_bf16 a[4:7],v[228:231],v[192:195],a[4:7]\n"
"ds_read_b64_tr_b16 v[252:253],v105 offset:32768\n"
"v_mfma_f32_16x16x32_bf16 a[4:7],v[164:167],v[128:131],a[4:7]\n"
"ds_read_b64_tr_b16 v[254:255],v105 offset:33792\n"
"s_waitcnt lgkmcnt(8)\n"
"v_mfma_f32_16x16x32_bf16 a[8:11],v[168:171],v[128:131],a[8:11]\n"
"ds_read_b128 v[132:135],v80 offset:2048\n"
"v_mfma_f32_16x16x32_bf16 a[8:11],v[232:235],v[192:195],a[8:11]\n"
"ds_read_b128 v[196:199],v81 offset:2048\n"
"v_mfma_f32_16x16x32_bf16 a[12:15],v[236:239],v[192:195],a[12:15]\n"
"ds_read_b128 v[136:139],v80 offset:4096\n"
"v_mfma_f32_16x16x32_bf16 a[12:15],v[172:175],v[128:131],a[12:15]\n"
"ds_read_b128 v[200:203],v81 offset:4096\n"
"s_waitcnt lgkmcnt(8)\n"
"v_mfma_f32_16x16x32_bf16 a[16:19],v[176:179],v[128:131],a[16:19]\n"
"ds_read_b128 v[140:143],v80 offset:6144\n"
"v_mfma_f32_16x16x32_bf16 a[16:19],v[240:243],v[192:195],a[16:19]\n"
"ds_read_b128 v[204:207],v81 offset:6144\n"
"v_mfma_f32_16x16x32_bf16 a[20:23],v[244:247],v[192:195],a[20:23]\n"
"ds_read_b128 v[144:147],v80 offset:8192\n"
"v_mfma_f32_16x16x32_bf16 a[20:23],v[180:183],v[128:131],a[20:23]\n"
"ds_read_b128 v[208:211],v81 offset:8192\n"
"s_waitcnt lgkmcnt(8)\n"
"v_mfma_f32_16x16x32_bf16 a[24:27],v[184:187],v[128:131],a[24:27]\n"
"ds_read_b128 v[148:151],v80 offset:10240\n"
"v_mfma_f32_16x16x32_bf16 a[24:27],v[248:251],v[192:195],a[24:27]\n"
"ds_read_b128 v[212:215],v81 offset:10240\n"
"v_mfma_f32_16x16x32_bf16 a[28:31],v[252:255],v[192:195],a[28:31]\n"
"ds_read_b128 v[216:219],v81 offset:12288\n"
"v_mfma_f32_16x16x32_bf16 a[28:31],v[188:191],v[128:131],a[28:31]\n"
"ds_read_b128 v[220:223],v81 offset:14336\n"
"s_waitcnt lgkmcnt(10)\n"
"v_mfma_f32_16x16x32_bf16 a[32:35],v[160:163],v[132:135],a[32:35]\n"
"v_mfma_f32_16x16x32_bf16 a[32:35],v[224:227],v[196:199],a[32:35]\n"
"v_mfma_f32_16x16x32_bf16 a[36:39],v[228:231],v[196:199],a[36:39]\n"
"v_mfma_f32_16x16x32_bf16 a[36:39],v[164:167],v[132:135],a[36:39]\n"
"v_mfma_f32_16x16x32_bf16 a[40:43],v[168:171],v[132:135],a[40:43]\n"
"s_waitcnt lgkmcnt(0)\n"
"s_barrier\n"
"v_mfma_f32_16x16x32_bf16 a[40:43],v[232:235],v[196:199],a[40:43]\n"
"s_add_u32 m0,s85,147456\n"
"v_mfma_f32_16x16x32_bf16 a[44:47],v[236:239],v[196:199],a[44:47]\n"
"buffer_load_dwordx4 v110,s[28:31],s86 offen lds\n"
"v_mfma_f32_16x16x32_bf16 a[44:47],v[172:175],v[132:135],a[44:47]\n"
"v_mfma_f32_16x16x32_bf16 a[48:51],v[176:179],v[132:135],a[48:51]\n"
"s_load_dwordx2 s[48:49],s[0:1],56\n"
"v_mfma_f32_16x16x32_bf16 a[48:51],v[240:243],v[196:199],a[48:51]\n"
"s_add_u32 m0,s85,148480\n"
"v_mfma_f32_16x16x32_bf16 a[52:55],v[244:247],v[196:199],a[52:55]\n"
"buffer_load_dwordx4 v111,s[28:31],s86 offen lds\n"
"v_mfma_f32_16x16x32_bf16 a[52:55],v[180:183],v[132:135],a[52:55]\n"
"s_xor_b32 s86,s86,262144\n"
"v_mfma_f32_16x16x32_bf16 a[56:59],v[184:187],v[132:135],a[56:59]\n"
"v_mfma_f32_16x16x32_bf16 a[56:59],v[248:251],v[196:199],a[56:59]\n"
"s_add_u32 m0,s85,155648\n"
"v_mfma_f32_16x16x32_bf16 a[60:63],v[252:255],v[196:199],a[60:63]\n"
"buffer_load_dwordx4 v110,s[28:31],s86 offen lds\n"
"v_mfma_f32_16x16x32_bf16 a[60:63],v[188:191],v[132:135],a[60:63]\n"
"v_mfma_f32_16x16x32_bf16 a[64:67],v[160:163],v[136:139],a[64:67]\n"
"v_mfma_f32_16x16x32_bf16 a[64:67],v[224:227],v[200:203],a[64:67]\n"
"s_add_u32 m0,s85,156672\n"
"v_mfma_f32_16x16x32_bf16 a[68:71],v[228:231],v[200:203],a[68:71]\n"
"buffer_load_dwordx4 v111,s[28:31],s86 offen lds\n"
"v_mfma_f32_16x16x32_bf16 a[68:71],v[164:167],v[136:139],a[68:71]\n"
"s_xor_b32 s86,s86,786432\n"
"v_mfma_f32_16x16x32_bf16 a[72:75],v[168:171],v[136:139],a[72:75]\n"
"v_mfma_f32_16x16x32_bf16 a[72:75],v[232:235],v[200:203],a[72:75]\n"
"s_add_u32 m0,s85,131072\n"
"v_mfma_f32_16x16x32_bf16 a[76:79],v[236:239],v[200:203],a[76:79]\n"
"buffer_load_dwordx4 v110,s[28:31],s86 offen lds\n"
"v_mfma_f32_16x16x32_bf16 a[76:79],v[172:175],v[136:139],a[76:79]\n"
"v_mfma_f32_16x16x32_bf16 a[80:83],v[176:179],v[136:139],a[80:83]\n"
"v_mfma_f32_16x16x32_bf16 a[80:83],v[240:243],v[200:203],a[80:83]\n"
"s_add_u32 m0,s85,132096\n"
"v_mfma_f32_16x16x32_bf16 a[84:87],v[244:247],v[200:203],a[84:87]\n"
"buffer_load_dwordx4 v111,s[28:31],s86 offen lds\n"
"v_mfma_f32_16x16x32_bf16 a[84:87],v[180:183],v[136:139],a[84:87]\n"
"s_xor_b32 s86,s86,262144\n"
"v_mfma_f32_16x16x32_bf16 a[88:91],v[184:187],v[136:139],a[88:91]\n"
"v_mfma_f32_16x16x32_bf16 a[88:91],v[248:251],v[200:203],a[88:91]\n"
"v_mfma_f32_16x16x32_bf16 a[92:95],v[252:255],v[200:203],a[92:95]\n"
"v_mfma_f32_16x16x32_bf16 a[92:95],v[188:191],v[136:139],a[92:95]\n"
"v_mfma_f32_16x16x32_bf16 a[96:99],v[160:163],v[140:143],a[96:99]\n"
"v_mfma_f32_16x16x32_bf16 a[96:99],v[224:227],v[204:207],a[96:99]\n"
"s_add_u32 m0,s85,139264\n"
"v_mfma_f32_16x16x32_bf16 a[100:103],v[228:231],v[204:207],a[100:103]\n"
"buffer_load_dwordx4 v110,s[28:31],s86 offen lds\n"
"v_mfma_f32_16x16x32_bf16 a[100:103],v[164:167],v[140:143],a[100:103]\n"
"v_mfma_f32_16x16x32_bf16 a[104:107],v[168:171],v[140:143],a[104:107]\n"
"v_mfma_f32_16x16x32_bf16 a[104:107],v[232:235],v[204:207],a[104:107]\n"
"s_add_u32 m0,s85,140288\n"
"v_mfma_f32_16x16x32_bf16 a[108:111],v[236:239],v[204:207],a[108:111]\n"
"buffer_load_dwordx4 v111,s[28:31],s86 offen lds\n"
"v_mfma_f32_16x16x32_bf16 a[108:111],v[172:175],v[140:143],a[108:111]\n"
"s_xor_b32 s86,s86,1835008\n"
"v_mfma_f32_16x16x32_bf16 a[112:115],v[176:179],v[140:143],a[112:115]\n"
"s_waitcnt lgkmcnt(0)\n"
"s_lshl2_add_u32 s88,s100,s52\n"
"v_mfma_f32_16x16x32_bf16 a[112:115],v[240:243],v[204:207],a[112:115]\n"
"s_mul_i32 s88,s88,7168\n"
"s_add_u32 m0,s78,0\n"
"v_mfma_f32_16x16x32_bf16 a[116:119],v[244:247],v[204:207],a[116:119]\n"
"buffer_load_dwordx4 v112,s[24:27],s76 offen offset:0 sc1 lds\n"
"v_mfma_f32_16x16x32_bf16 a[116:119],v[180:183],v[140:143],a[116:119]\n"
"s_add_u32 s88,s88,3584\n"
"s_add_u32 s48,s48,s88\n"
"s_addc_u32 s49,s49,0\n"
"v_mfma_f32_16x16x32_bf16 a[120:123],v[184:187],v[140:143],a[120:123]\n"
"s_mov_b32 s50,3584\n"
"s_mov_b32 s51,0x20000\n"
"v_mfma_f32_16x16x32_bf16 a[120:123],v[248:251],v[204:207],a[120:123]\n"
"v_lshlrev_b32 v8,3,v112\n"
"v_mfma_f32_16x16x32_bf16 a[124:127],v[252:255],v[204:207],a[124:127]\n"
"buffer_load_dwordx4 v112,s[24:27],s76 offen offset:1024 sc1 lds\n"
"v_mfma_f32_16x16x32_bf16 a[124:127],v[188:191],v[140:143],a[124:127]\n"
"v_mfma_f32_16x16x32_bf16 a[128:131],v[160:163],v[144:147],a[128:131]\n"
"v_mfma_f32_16x16x32_bf16 a[128:131],v[224:227],v[208:211],a[128:131]\n"
"v_mfma_f32_16x16x32_bf16 a[132:135],v[228:231],v[208:211],a[132:135]\n"
"buffer_load_dwordx4 v112,s[24:27],s76 offen offset:2048 sc1 lds\n"
"v_mfma_f32_16x16x32_bf16 a[132:135],v[164:167],v[144:147],a[132:135]\n"
"v_mfma_f32_16x16x32_bf16 a[136:139],v[168:171],v[144:147],a[136:139]\n"
"v_mfma_f32_16x16x32_bf16 a[136:139],v[232:235],v[208:211],a[136:139]\n"
"buffer_load_dwordx4 v112,s[24:27],s76 offen offset:3072 sc1 lds\n"
"v_mfma_f32_16x16x32_bf16 a[140:143],v[236:239],v[208:211],a[140:143]\n"
"s_xor_b32 s76,s76,16384\n"
"v_mfma_f32_16x16x32_bf16 a[140:143],v[172:175],v[144:147],a[140:143]\n"
"v_mfma_f32_16x16x32_bf16 a[144:147],v[176:179],v[144:147],a[144:147]\n"
"v_mfma_f32_16x16x32_bf16 a[144:147],v[240:243],v[208:211],a[144:147]\n"
"s_add_u32 m0,s78,16384\n"
"v_mfma_f32_16x16x32_bf16 a[148:151],v[244:247],v[208:211],a[148:151]\n"
"buffer_load_dwordx4 v112,s[24:27],s76 offen offset:0 sc1 lds\n"
"v_mfma_f32_16x16x32_bf16 a[148:151],v[180:183],v[144:147],a[148:151]\n"
"v_mfma_f32_16x16x32_bf16 a[152:155],v[184:187],v[144:147],a[152:155]\n"
"v_mfma_f32_16x16x32_bf16 a[152:155],v[248:251],v[208:211],a[152:155]\n"
"v_mfma_f32_16x16x32_bf16 a[156:159],v[252:255],v[208:211],a[156:159]\n"
"buffer_load_dwordx4 v112,s[24:27],s76 offen offset:1024 sc1 lds\n"
"v_mfma_f32_16x16x32_bf16 a[156:159],v[188:191],v[144:147],a[156:159]\n"
"v_mfma_f32_16x16x32_bf16 a[160:163],v[160:163],v[148:151],a[160:163]\n"
"v_mfma_f32_16x16x32_bf16 a[160:163],v[224:227],v[212:215],a[160:163]\n"
"v_mfma_f32_16x16x32_bf16 a[164:167],v[228:231],v[212:215],a[164:167]\n"
"buffer_load_dwordx4 v112,s[24:27],s76 offen offset:2048 sc1 lds\n"
"v_mfma_f32_16x16x32_bf16 a[164:167],v[164:167],v[148:151],a[164:167]\n"
"v_mfma_f32_16x16x32_bf16 a[168:171],v[168:171],v[148:151],a[168:171]\n"
"v_mfma_f32_16x16x32_bf16 a[168:171],v[232:235],v[212:215],a[168:171]\n"
"buffer_load_dwordx4 v112,s[24:27],s76 offen offset:3072 sc1 lds\n"
"v_mfma_f32_16x16x32_bf16 a[172:175],v[236:239],v[212:215],a[172:175]\n"
"s_xor_b32 s76,s76,114688\n"
"v_mfma_f32_16x16x32_bf16 a[172:175],v[172:175],v[148:151],a[172:175]\n"
"v_mfma_f32_16x16x32_bf16 a[176:179],v[176:179],v[148:151],a[176:179]\n"
"buffer_load_dword v115,v8,s[48:51],0 offen\n"
"v_mfma_f32_16x16x32_bf16 a[176:179],v[240:243],v[212:215],a[176:179]\n"
"v_mfma_f32_16x16x32_bf16 a[180:183],v[244:247],v[212:215],a[180:183]\n"
"v_mfma_f32_16x16x32_bf16 a[180:183],v[180:183],v[148:151],a[180:183]\n"
"v_mfma_f32_16x16x32_bf16 a[184:187],v[184:187],v[148:151],a[184:187]\n"
"v_mfma_f32_16x16x32_bf16 a[184:187],v[248:251],v[212:215],a[184:187]\n"
"v_mfma_f32_16x16x32_bf16 a[188:191],v[252:255],v[212:215],a[188:191]\n"
"v_mfma_f32_16x16x32_bf16 a[188:191],v[188:191],v[148:151],a[188:191]\n"
"s_waitcnt vmcnt(26)\n"
"s_barrier\n"
"v_mfma_f32_16x16x32_bf16 a[192:195],v[224:227],v[216:219],a[192:195]\n"
"ds_read_b128 v[152:155],v80 offset:45056\n"
"v_mfma_f32_16x16x32_bf16 a[224:227],v[224:227],v[220:223],a[224:227]\n"
"ds_read_b128 v[156:159],v80 offset:47104\n"
"v_mfma_f32_16x16x32_bf16 a[228:231],v[228:231],v[220:223],a[228:231]\n"
"ds_read_b64_tr_b16 v[160:161],v98 offset:0\n"
"v_mfma_f32_16x16x32_bf16 a[196:199],v[228:231],v[216:219],a[196:199]\n"
"ds_read_b64_tr_b16 v[162:163],v98 offset:1024\n"
"v_mfma_f32_16x16x32_bf16 a[200:203],v[232:235],v[216:219],a[200:203]\n"
"ds_read_b64_tr_b16 v[164:165],v99 offset:0\n"
"v_mfma_f32_16x16x32_bf16 a[232:235],v[232:235],v[220:223],a[232:235]\n"
"ds_read_b64_tr_b16 v[166:167],v99 offset:1024\n"
"v_mfma_f32_16x16x32_bf16 a[236:239],v[236:239],v[220:223],a[236:239]\n"
"ds_read_b64_tr_b16 v[168:169],v100 offset:0\n"
"v_mfma_f32_16x16x32_bf16 a[204:207],v[236:239],v[216:219],a[204:207]\n"
"ds_read_b64_tr_b16 v[170:171],v100 offset:1024\n"
"v_mfma_f32_16x16x32_bf16 a[208:211],v[240:243],v[216:219],a[208:211]\n"
"ds_read_b64_tr_b16 v[172:173],v101 offset:0\n"
"v_mfma_f32_16x16x32_bf16 a[240:243],v[240:243],v[220:223],a[240:243]\n"
"ds_read_b64_tr_b16 v[174:175],v101 offset:1024\n"
"v_mfma_f32_16x16x32_bf16 a[244:247],v[244:247],v[220:223],a[244:247]\n"
"ds_read_b64_tr_b16 v[176:177],v102 offset:0\n"
"v_mfma_f32_16x16x32_bf16 a[212:215],v[244:247],v[216:219],a[212:215]\n"
"ds_read_b64_tr_b16 v[178:179],v102 offset:1024\n"
"v_mfma_f32_16x16x32_bf16 a[216:219],v[248:251],v[216:219],a[216:219]\n"
"ds_read_b64_tr_b16 v[180:181],v103 offset:0\n"
"v_mfma_f32_16x16x32_bf16 a[248:251],v[248:251],v[220:223],a[248:251]\n"
"ds_read_b64_tr_b16 v[182:183],v103 offset:1024\n"
"v_mfma_f32_16x16x32_bf16 a[252:255],v[252:255],v[220:223],a[252:255]\n"
"ds_read_b64_tr_b16 v[184:185],v104 offset:0\n"
"v_mfma_f32_16x16x32_bf16 a[220:223],v[252:255],v[216:219],a[220:223]\n"
"ds_read_b64_tr_b16 v[186:187],v104 offset:1024\n"
"s_waitcnt lgkmcnt(10)\n"
"v_mfma_f32_16x16x32_bf16 a[192:195],v[160:163],v[152:155],a[192:195]\n"
"ds_read_b64_tr_b16 v[188:189],v105 offset:0\n"
"v_mfma_f32_16x16x32_bf16 a[224:227],v[160:163],v[156:159],a[224:227]\n"
"ds_read_b64_tr_b16 v[190:191],v105 offset:1024\n"
"v_mfma_f32_16x16x32_bf16 a[228:231],v[164:167],v[156:159],a[228:231]\n"
"ds_read_b128 v[128:131],v80 offset:32768\n"
"v_mfma_f32_16x16x32_bf16 a[196:199],v[164:167],v[152:155],a[196:199]\n"
"ds_read_b128 v[192:195],v81 offset:32768\n"
"s_waitcnt lgkmcnt(10)\n"
"v_mfma_f32_16x16x32_bf16 a[200:203],v[168:171],v[152:155],a[200:203]\n"
"ds_read_b64_tr_b16 v[224:225],v98 offset:16384\n"
"v_mfma_f32_16x16x32_bf16 a[232:235],v[168:171],v[156:159],a[232:235]\n"
"ds_read_b64_tr_b16 v[226:227],v98 offset:17408\n"
"v_mfma_f32_16x16x32_bf16 a[236:239],v[172:175],v[156:159],a[236:239]\n"
"ds_read_b64_tr_b16 v[228:229],v99 offset:16384\n"
"v_mfma_f32_16x16x32_bf16 a[204:207],v[172:175],v[152:155],a[204:207]\n"
"ds_read_b64_tr_b16 v[230:231],v99 offset:17408\n"
"s_waitcnt lgkmcnt(10)\n"
"v_mfma_f32_16x16x32_bf16 a[208:211],v[176:179],v[152:155],a[208:211]\n"
"ds_read_b64_tr_b16 v[232:233],v100 offset:16384\n"
"v_mfma_f32_16x16x32_bf16 a[240:243],v[176:179],v[156:159],a[240:243]\n"
"ds_read_b64_tr_b16 v[234:235],v100 offset:17408\n"
"v_mfma_f32_16x16x32_bf16 a[244:247],v[180:183],v[156:159],a[244:247]\n"
"ds_read_b64_tr_b16 v[236:237],v101 offset:16384\n"
"v_mfma_f32_16x16x32_bf16 a[212:215],v[180:183],v[152:155],a[212:215]\n"
"ds_read_b64_tr_b16 v[238:239],v101 offset:17408\n"
"s_waitcnt lgkmcnt(10)\n"
"v_mfma_f32_16x16x32_bf16 a[216:219],v[184:187],v[152:155],a[216:219]\n"
"ds_read_b64_tr_b16 v[240:241],v102 offset:16384\n"
"v_mfma_f32_16x16x32_bf16 a[248:251],v[184:187],v[156:159],a[248:251]\n"
"ds_read_b64_tr_b16 v[242:243],v102 offset:17408\n"
"v_mfma_f32_16x16x32_bf16 a[252:255],v[188:191],v[156:159],a[252:255]\n"
"ds_read_b64_tr_b16 v[244:245],v103 offset:16384\n"
"v_mfma_f32_16x16x32_bf16 a[220:223],v[188:191],v[152:155],a[220:223]\n"
"ds_read_b64_tr_b16 v[246:247],v103 offset:17408\n"
"s_waitcnt lgkmcnt(8)\n"
"v_mfma_f32_16x16x32_bf16 a[0:3],v[160:163],v[128:131],a[0:3]\n"
"ds_read_b64_tr_b16 v[248:249],v104 offset:16384\n"
"v_mfma_f32_16x16x32_bf16 a[0:3],v[224:227],v[192:195],a[0:3]\n"
"ds_read_b64_tr_b16 v[250:251],v104 offset:17408\n"
"v_mfma_f32_16x16x32_bf16 a[4:7],v[228:231],v[192:195],a[4:7]\n"
"ds_read_b64_tr_b16 v[252:253],v105 offset:16384\n"
"v_mfma_f32_16x16x32_bf16 a[4:7],v[164:167],v[128:131],a[4:7]\n"
"ds_read_b64_tr_b16 v[254:255],v105 offset:17408\n"
"s_waitcnt lgkmcnt(8)\n"
"v_mfma_f32_16x16x32_bf16 a[8:11],v[168:171],v[128:131],a[8:11]\n"
"ds_read_b128 v[132:135],v80 offset:34816\n"
"v_mfma_f32_16x16x32_bf16 a[8:11],v[232:235],v[192:195],a[8:11]\n"
"ds_read_b128 v[196:199],v81 offset:34816\n"
"v_mfma_f32_16x16x32_bf16 a[12:15],v[236:239],v[192:195],a[12:15]\n"
"ds_read_b128 v[136:139],v80 offset:36864\n"
"v_mfma_f32_16x16x32_bf16 a[12:15],v[172:175],v[128:131],a[12:15]\n"
"ds_read_b128 v[200:203],v81 offset:36864\n"
"s_waitcnt lgkmcnt(8)\n"
"v_mfma_f32_16x16x32_bf16 a[16:19],v[176:179],v[128:131],a[16:19]\n"
"s_and_b32 s82,s52,1\n"
"ds_read_b128 v[140:143],v80 offset:38912\n"
"s_lshl_b32 s82,s82,13\n"
"v_mfma_f32_16x16x32_bf16 a[16:19],v[240:243],v[192:195],a[16:19]\n"
"ds_read_b128 v[204:207],v81 offset:38912\n"
"v_mfma_f32_16x16x32_bf16 a[20:23],v[244:247],v[192:195],a[20:23]\n"
"ds_read_b128 v[144:147],v80 offset:40960\n"
"v_mfma_f32_16x16x32_bf16 a[20:23],v[180:183],v[128:131],a[20:23]\n"
"ds_read_b128 v[208:211],v81 offset:40960\n"
"s_waitcnt lgkmcnt(8)\n"
"v_mfma_f32_16x16x32_bf16 a[24:27],v[184:187],v[128:131],a[24:27]\n"
"ds_read_b128 v[148:151],v80 offset:43008\n"
"v_mfma_f32_16x16x32_bf16 a[24:27],v[248:251],v[192:195],a[24:27]\n"
"ds_read_b128 v[212:215],v81 offset:43008\n"
"v_subrev_u32 v98,s82,v98\n"
"v_mfma_f32_16x16x32_bf16 a[28:31],v[252:255],v[192:195],a[28:31]\n"
"ds_read_b128 v[216:219],v81 offset:45056\n"
"v_add_u32 v96,s78,v97\n"
"v_mfma_f32_16x16x32_bf16 a[28:31],v[188:191],v[128:131],a[28:31]\n"
"ds_read_b128 v[220:223],v81 offset:47104\n"
"s_waitcnt lgkmcnt(10)\n"
"v_mfma_f32_16x16x32_bf16 a[32:35],v[160:163],v[132:135],a[32:35]\n"
"v_add_u32 v97,65536,v96\n"
"v_mfma_f32_16x16x32_bf16 a[32:35],v[224:227],v[196:199],a[32:35]\n"
"v_subrev_u32 v99,s82,v99\n"
"v_mfma_f32_16x16x32_bf16 a[36:39],v[228:231],v[196:199],a[36:39]\n"
"v_mfma_f32_16x16x32_bf16 a[36:39],v[164:167],v[132:135],a[36:39]\n"
"v_mfma_f32_16x16x32_bf16 a[40:43],v[168:171],v[132:135],a[40:43]\n"
"s_waitcnt lgkmcnt(0)\n"
"s_barrier\n"
"v_mfma_f32_16x16x32_bf16 a[40:43],v[232:235],v[196:199],a[40:43]\n"
"v_subrev_u32 v100,s82,v100\n"
"s_add_u32 m0,s85,98304\n"
"v_mfma_f32_16x16x32_bf16 a[44:47],v[236:239],v[196:199],a[44:47]\n"
"buffer_load_dwordx4 v110,s[28:31],s86 offen lds\n"
"v_mfma_f32_16x16x32_bf16 a[44:47],v[172:175],v[132:135],a[44:47]\n"
"v_mfma_f32_16x16x32_bf16 a[48:51],v[176:179],v[132:135],a[48:51]\n"
"v_mfma_f32_16x16x32_bf16 a[48:51],v[240:243],v[196:199],a[48:51]\n"
"v_subrev_u32 v101,s82,v101\n"
"s_add_u32 m0,s85,99328\n"
"v_mfma_f32_16x16x32_bf16 a[52:55],v[244:247],v[196:199],a[52:55]\n"
"buffer_load_dwordx4 v111,s[28:31],s86 offen lds\n"
"v_mfma_f32_16x16x32_bf16 a[52:55],v[180:183],v[132:135],a[52:55]\n"
"s_xor_b32 s86,s86,262144\n"
"v_mfma_f32_16x16x32_bf16 a[56:59],v[184:187],v[132:135],a[56:59]\n"
"v_mfma_f32_16x16x32_bf16 a[56:59],v[248:251],v[196:199],a[56:59]\n"
"v_subrev_u32 v102,s82,v102\n"
"s_add_u32 m0,s78,32768\n"
"v_mfma_f32_16x16x32_bf16 a[60:63],v[252:255],v[196:199],a[60:63]\n"
"buffer_load_dwordx4 v112,s[24:27],s76 offen offset:0 sc1 lds\n"
"v_mfma_f32_16x16x32_bf16 a[60:63],v[188:191],v[132:135],a[60:63]\n"
"v_mfma_f32_16x16x32_bf16 a[64:67],v[160:163],v[136:139],a[64:67]\n"
"v_mfma_f32_16x16x32_bf16 a[64:67],v[224:227],v[200:203],a[64:67]\n"
"v_subrev_u32 v103,s82,v103\n"
"v_mfma_f32_16x16x32_bf16 a[68:71],v[228:231],v[200:203],a[68:71]\n"
"buffer_load_dwordx4 v112,s[24:27],s76 offen offset:1024 sc1 lds\n"
"v_mfma_f32_16x16x32_bf16 a[68:71],v[164:167],v[136:139],a[68:71]\n"
"v_mfma_f32_16x16x32_bf16 a[72:75],v[168:171],v[136:139],a[72:75]\n"
"v_accvgpr_read_b32 v116,a0\n"
"v_accvgpr_read_b32 v117,a1\n"
"v_mfma_f32_16x16x32_bf16 a[72:75],v[232:235],v[200:203],a[72:75]\n"
"v_subrev_u32 v104,s82,v104\n"
"v_accvgpr_read_b32 v118,a2\n"
"v_mfma_f32_16x16x32_bf16 a[76:79],v[236:239],v[200:203],a[76:79]\n"
"buffer_load_dwordx4 v112,s[24:27],s76 offen offset:2048 sc1 lds\n"
"v_mfma_f32_16x16x32_bf16 a[76:79],v[172:175],v[136:139],a[76:79]\n"
"v_accvgpr_read_b32 v119,a3\n"
"v_accvgpr_read_b32 v120,a4\n"
"v_mfma_f32_16x16x32_bf16 a[80:83],v[176:179],v[136:139],a[80:83]\n"
"v_accvgpr_read_b32 v121,a5\n"
"v_accvgpr_read_b32 v122,a6\n"
"v_mfma_f32_16x16x32_bf16 a[80:83],v[240:243],v[200:203],a[80:83]\n"
"v_subrev_u32 v105,s82,v105\n"
"v_accvgpr_read_b32 v123,a7\n"
"v_mfma_f32_16x16x32_bf16 a[84:87],v[244:247],v[200:203],a[84:87]\n"
"buffer_load_dwordx4 v112,s[24:27],s76 offen offset:3072 sc1 lds\n"
"v_mfma_f32_16x16x32_bf16 a[84:87],v[180:183],v[136:139],a[84:87]\n"
"s_xor_b32 s76,s76,16384\n"
"v_cvt_pk_bf16_f32 v116,v116,v117\n"
"v_cvt_pk_bf16_f32 v117,v118,v119\n"
"v_mfma_f32_16x16x32_bf16 a[88:91],v[184:187],v[136:139],a[88:91]\n"
"v_cvt_pk_bf16_f32 v118,v120,v121\n"
"v_cvt_pk_bf16_f32 v119,v122,v123\n"
"v_mfma_f32_16x16x32_bf16 a[88:91],v[248:251],v[200:203],a[88:91]\n"
"buffer_store_dwordx4 v[116:119],v112,s[24:27],s56 offen offset:0 sc1\n"
"v_mfma_f32_16x16x32_bf16 a[92:95],v[252:255],v[200:203],a[92:95]\n"
"s_nop 0\n"
"v_accvgpr_read_b32 v116,a8\n"
"v_accvgpr_read_b32 v117,a9\n"
"v_accvgpr_read_b32 v118,a10\n"
"v_mfma_f32_16x16x32_bf16 a[92:95],v[188:191],v[136:139],a[92:95]\n"
"s_waitcnt vmcnt(20)\n"
"v_accvgpr_read_b32 v119,a11\n"
"v_accvgpr_read_b32 v120,a12\n"
"s_barrier\n"
"v_mfma_f32_16x16x32_bf16 a[96:99],v[160:163],v[140:143],a[96:99]\n"
"ds_read_b128 v[0:3],v97 offset:0\n"
"v_accvgpr_read_b32 v121,a13\n"
"v_mfma_f32_16x16x32_bf16 a[96:99],v[224:227],v[204:207],a[96:99]\n"
"ds_read_b128 v[8:11],v97 offset:1024\n"
"v_accvgpr_read_b32 v122,a14\n"
"v_mfma_f32_16x16x32_bf16 a[100:103],v[228:231],v[204:207],a[100:103]\n"
"ds_read_b128 v[16:19],v97 offset:2048\n"
"v_accvgpr_read_b32 v123,a15\n"
"v_mfma_f32_16x16x32_bf16 a[100:103],v[164:167],v[140:143],a[100:103]\n"
"ds_read_b128 v[24:27],v97 offset:3072\n"
"v_cvt_pk_bf16_f32 v116,v116,v117\n"
"v_mfma_f32_16x16x32_bf16 a[104:107],v[168:171],v[140:143],a[104:107]\n"
"ds_read_b128 v[4:7],v97 offset:16384\n"
"v_cvt_pk_bf16_f32 v117,v118,v119\n"
"v_mfma_f32_16x16x32_bf16 a[104:107],v[232:235],v[204:207],a[104:107]\n"
"ds_read_b128 v[12:15],v97 offset:17408\n"
"v_cvt_pk_bf16_f32 v118,v120,v121\n"
"v_mfma_f32_16x16x32_bf16 a[108:111],v[236:239],v[204:207],a[108:111]\n"
"ds_read_b128 v[20:23],v97 offset:18432\n"
"v_cvt_pk_bf16_f32 v119,v122,v123\n"
"v_mfma_f32_16x16x32_bf16 a[108:111],v[172:175],v[140:143],a[108:111]\n"
"ds_read_b128 v[28:31],v97 offset:19456\n"
"buffer_store_dwordx4 v[116:119],v112,s[24:27],s56 offen offset:1024 sc1\n"
"v_mfma_f32_16x16x32_bf16 a[112:115],v[176:179],v[140:143],a[112:115]\n"
"ds_read_b64_tr_b16 v[32:33],v98 offset:49152\n"
"v_accvgpr_read_b32 v116,a16\n"
"v_mfma_f32_16x16x32_bf16 a[112:115],v[240:243],v[204:207],a[112:115]\n"
"ds_read_b64_tr_b16 v[34:35],v98 offset:50176\n"
"v_accvgpr_read_b32 v117,a17\n"
"v_mfma_f32_16x16x32_bf16 a[116:119],v[244:247],v[204:207],a[116:119]\n"
"ds_read_b64_tr_b16 v[36:37],v98 offset:57344\n"
"v_accvgpr_read_b32 v118,a18\n"
"v_mfma_f32_16x16x32_bf16 a[116:119],v[180:183],v[140:143],a[116:119]\n"
"ds_read_b64_tr_b16 v[38:39],v98 offset:58368\n"
"v_accvgpr_read_b32 v119,a19\n"
"v_mfma_f32_16x16x32_bf16 a[120:123],v[184:187],v[140:143],a[120:123]\n"
"ds_read_b64_tr_b16 v[40:41],v99 offset:49152\n"
"v_accvgpr_read_b32 v120,a20\n"
"v_mfma_f32_16x16x32_bf16 a[120:123],v[248:251],v[204:207],a[120:123]\n"
"ds_read_b64_tr_b16 v[42:43],v99 offset:50176\n"
"v_accvgpr_read_b32 v121,a21\n"
"v_mfma_f32_16x16x32_bf16 a[124:127],v[252:255],v[204:207],a[124:127]\n"
"ds_read_b64_tr_b16 v[44:45],v99 offset:57344\n"
"v_accvgpr_read_b32 v122,a22\n"
"v_mfma_f32_16x16x32_bf16 a[124:127],v[188:191],v[140:143],a[124:127]\n"
"ds_read_b64_tr_b16 v[46:47],v99 offset:58368\n"
"v_accvgpr_read_b32 v123,a23\n"
"v_mfma_f32_16x16x32_bf16 a[128:131],v[160:163],v[144:147],a[128:131]\n"
"ds_read_b64_tr_b16 v[48:49],v100 offset:49152\n"
"v_cvt_pk_bf16_f32 v116,v116,v117\n"
"v_mfma_f32_16x16x32_bf16 a[128:131],v[224:227],v[208:211],a[128:131]\n"
"ds_read_b64_tr_b16 v[50:51],v100 offset:50176\n"
"v_cvt_pk_bf16_f32 v117,v118,v119\n"
"v_mfma_f32_16x16x32_bf16 a[132:135],v[228:231],v[208:211],a[132:135]\n"
"ds_read_b64_tr_b16 v[52:53],v100 offset:57344\n"
"v_cvt_pk_bf16_f32 v118,v120,v121\n"
"v_mfma_f32_16x16x32_bf16 a[132:135],v[164:167],v[144:147],a[132:135]\n"
"ds_read_b64_tr_b16 v[54:55],v100 offset:58368\n"
"v_cvt_pk_bf16_f32 v119,v122,v123\n"
"v_mfma_f32_16x16x32_bf16 a[136:139],v[168:171],v[144:147],a[136:139]\n"
"ds_read_b64_tr_b16 v[56:57],v101 offset:49152\n"
"buffer_store_dwordx4 v[116:119],v112,s[24:27],s56 offen offset:2048 sc1\n"
"v_mfma_f32_16x16x32_bf16 a[136:139],v[232:235],v[208:211],a[136:139]\n"
"ds_read_b64_tr_b16 v[58:59],v101 offset:50176\n"
"v_accvgpr_read_b32 v116,a24\n"
"v_mfma_f32_16x16x32_bf16 a[140:143],v[236:239],v[208:211],a[140:143]\n"
"ds_read_b64_tr_b16 v[60:61],v101 offset:57344\n"
"v_accvgpr_read_b32 v117,a25\n"
"v_mfma_f32_16x16x32_bf16 a[140:143],v[172:175],v[144:147],a[140:143]\n"
"ds_read_b64_tr_b16 v[62:63],v101 offset:58368\n"
"v_accvgpr_read_b32 v118,a26\n"
"v_mfma_f32_16x16x32_bf16 a[144:147],v[176:179],v[144:147],a[144:147]\n"
"ds_read_b64_tr_b16 v[64:65],v102 offset:49152\n"
"v_accvgpr_read_b32 v119,a27\n"
"v_mfma_f32_16x16x32_bf16 a[144:147],v[240:243],v[208:211],a[144:147]\n"
"ds_read_b64_tr_b16 v[66:67],v102 offset:50176\n"
"v_accvgpr_read_b32 v120,a28\n"
"v_mfma_f32_16x16x32_bf16 a[148:151],v[244:247],v[208:211],a[148:151]\n"
"ds_read_b64_tr_b16 v[68:69],v102 offset:57344\n"
"v_accvgpr_read_b32 v121,a29\n"
"v_mfma_f32_16x16x32_bf16 a[148:151],v[180:183],v[144:147],a[148:151]\n"
"ds_read_b64_tr_b16 v[70:71],v102 offset:58368\n"
"v_accvgpr_read_b32 v122,a30\n"
"v_mfma_f32_16x16x32_bf16 a[152:155],v[184:187],v[144:147],a[152:155]\n"
"ds_read_b64_tr_b16 v[72:73],v103 offset:49152\n"
"v_accvgpr_read_b32 v123,a31\n"
"v_mfma_f32_16x16x32_bf16 a[152:155],v[248:251],v[208:211],a[152:155]\n"
"ds_read_b64_tr_b16 v[74:75],v103 offset:50176\n"
"v_cvt_pk_bf16_f32 v116,v116,v117\n"
"v_mfma_f32_16x16x32_bf16 a[156:159],v[252:255],v[208:211],a[156:159]\n"
"ds_read_b64_tr_b16 v[76:77],v103 offset:57344\n"
"v_cvt_pk_bf16_f32 v117,v118,v119\n"
"v_mfma_f32_16x16x32_bf16 a[156:159],v[188:191],v[144:147],a[156:159]\n"
"ds_read_b64_tr_b16 v[78:79],v103 offset:58368\n"
"v_cvt_pk_bf16_f32 v118,v120,v121\n"
"v_mfma_f32_16x16x32_bf16 a[160:163],v[160:163],v[148:151],a[160:163]\n"
"ds_read_b64_tr_b16 v[80:81],v104 offset:49152\n"
"v_cvt_pk_bf16_f32 v119,v122,v123\n"
"v_mfma_f32_16x16x32_bf16 a[160:163],v[224:227],v[212:215],a[160:163]\n"
"ds_read_b64_tr_b16 v[82:83],v104 offset:50176\n"
"buffer_store_dwordx4 v[116:119],v112,s[24:27],s56 offen offset:3072 sc1\n"
"v_mfma_f32_16x16x32_bf16 a[164:167],v[228:231],v[212:215],a[164:167]\n"
"ds_read_b64_tr_b16 v[84:85],v104 offset:57344\n"
"v_mfma_f32_16x16x32_bf16 a[164:167],v[164:167],v[148:151],a[164:167]\n"
"ds_read_b64_tr_b16 v[86:87],v104 offset:58368\n"
"v_mfma_f32_16x16x32_bf16 a[168:171],v[168:171],v[148:151],a[168:171]\n"
"ds_read_b64_tr_b16 v[88:89],v105 offset:49152\n"
"v_mfma_f32_16x16x32_bf16 a[168:171],v[232:235],v[212:215],a[168:171]\n"
"ds_read_b64_tr_b16 v[90:91],v105 offset:50176\n"
"v_mfma_f32_16x16x32_bf16 a[172:175],v[236:239],v[212:215],a[172:175]\n"
"ds_read_b64_tr_b16 v[92:93],v105 offset:57344\n"
"v_mfma_f32_16x16x32_bf16 a[172:175],v[172:175],v[148:151],a[172:175]\n"
"ds_read_b64_tr_b16 v[94:95],v105 offset:58368\n"
"v_mfma_f32_16x16x32_bf16 a[176:179],v[176:179],v[148:151],a[176:179]\n"
"s_add_u32 m0,s85,106496\n"
"v_mfma_f32_16x16x32_bf16 a[176:179],v[240:243],v[212:215],a[176:179]\n"
"buffer_load_dwordx4 v110,s[28:31],s86 offen lds\n"
"v_mfma_f32_16x16x32_bf16 a[180:183],v[244:247],v[212:215],a[180:183]\n"
"v_mfma_f32_16x16x32_bf16 a[180:183],v[180:183],v[148:151],a[180:183]\n"
"s_add_u32 m0,s85,107520\n"
"v_mfma_f32_16x16x32_bf16 a[184:187],v[184:187],v[148:151],a[184:187]\n"
"buffer_load_dwordx4 v111,s[28:31],s86 offen lds\n"
"v_mfma_f32_16x16x32_bf16 a[184:187],v[248:251],v[212:215],a[184:187]\n"
"s_xor_b32 s86,s86,786432\n"
"v_mfma_f32_16x16x32_bf16 a[188:191],v[252:255],v[212:215],a[188:191]\n"
"s_add_u32 m0,s78,49152\n"
"v_mfma_f32_16x16x32_bf16 a[188:191],v[188:191],v[148:151],a[188:191]\n"
"buffer_load_dwordx4 v112,s[24:27],s76 offen offset:0 sc1 lds\n"
"v_mfma_f32_16x16x32_bf16 a[192:195],v[224:227],v[216:219],a[192:195]\n"
"v_mfma_f32_16x16x32_bf16 a[224:227],v[224:227],v[220:223],a[224:227]\n"
"v_mfma_f32_16x16x32_bf16 a[228:231],v[228:231],v[220:223],a[228:231]\n"
"buffer_load_dwordx4 v112,s[24:27],s76 offen offset:1024 sc1 lds\n"
"v_mfma_f32_16x16x32_bf16 a[196:199],v[228:231],v[216:219],a[196:199]\n"
"v_mfma_f32_16x16x32_bf16 a[200:203],v[232:235],v[216:219],a[200:203]\n"
"v_mfma_f32_16x16x32_bf16 a[232:235],v[232:235],v[220:223],a[232:235]\n"
"buffer_load_dwordx4 v112,s[24:27],s76 offen offset:2048 sc1 lds\n"
"v_mfma_f32_16x16x32_bf16 a[236:239],v[236:239],v[220:223],a[236:239]\n"
"v_mfma_f32_16x16x32_bf16 a[204:207],v[236:239],v[216:219],a[204:207]\n"
"buffer_load_dwordx4 v112,s[24:27],s76 offen offset:3072 sc1 lds\n"
"v_mfma_f32_16x16x32_bf16 a[208:211],v[240:243],v[216:219],a[208:211]\n"
"s_xor_b32 s76,s76,49152\n"
"v_mfma_f32_16x16x32_bf16 a[240:243],v[240:243],v[220:223],a[240:243]\n"
"v_mfma_f32_16x16x32_bf16 a[244:247],v[244:247],v[220:223],a[244:247]\n"
"v_mfma_f32_16x16x32_bf16 a[212:215],v[244:247],v[216:219],a[212:215]\n"
"v_mfma_f32_16x16x32_bf16 a[216:219],v[248:251],v[216:219],a[216:219]\n"
"v_mfma_f32_16x16x32_bf16 a[248:251],v[248:251],v[220:223],a[248:251]\n"
"v_mfma_f32_16x16x32_bf16 a[252:255],v[252:255],v[220:223],a[252:255]\n"
"v_mfma_f32_16x16x32_bf16 a[220:223],v[252:255],v[216:219],a[220:223]\n"
"s_branch .Lfk_ph2\n"
".Lfk_v:\n"
"s_waitcnt vmcnt(11)\n"
"s_barrier\n"
"ds_read_b128 v[152:155],v80 offset:12288\n"
"ds_read_b128 v[156:159],v80 offset:14336\n"
"ds_read_b64_tr_b16 v[160:161],v98 offset:49152\n"
"ds_read_b64_tr_b16 v[162:163],v98 offset:50176\n"
"ds_read_b64_tr_b16 v[164:165],v99 offset:49152\n"
"ds_read_b64_tr_b16 v[166:167],v99 offset:50176\n"
"ds_read_b64_tr_b16 v[168:169],v100 offset:49152\n"
"ds_read_b64_tr_b16 v[170:171],v100 offset:50176\n"
"ds_read_b64_tr_b16 v[172:173],v101 offset:49152\n"
"ds_read_b64_tr_b16 v[174:175],v101 offset:50176\n"
"ds_read_b64_tr_b16 v[176:177],v102 offset:49152\n"
"ds_read_b64_tr_b16 v[178:179],v102 offset:50176\n"
"ds_read_b64_tr_b16 v[180:181],v103 offset:49152\n"
"ds_read_b64_tr_b16 v[182:183],v103 offset:50176\n"
"ds_read_b64_tr_b16 v[184:185],v104 offset:49152\n"
"ds_read_b64_tr_b16 v[186:187],v104 offset:50176\n"
"ds_read_b64_tr_b16 v[188:189],v105 offset:49152\n"
"ds_read_b64_tr_b16 v[190:191],v105 offset:50176\n"
"s_waitcnt lgkmcnt(12)\n"
"v_mfma_f32_16x16x32_bf16 a[192:195],v[152:155],v[160:163],0\n"
"s_and_b32 s91,s89,7\n"
"s_mov_b32 s76,128\n"
"s_add_u32 m0,s79,32768\n"
"v_mfma_f32_16x16x32_bf16 a[196:199],v[156:159],v[160:163],0\n"
"buffer_load_dwordx4 v106,s[16:19],s76 offen offset:0 lds\n"
"v_mfma_f32_16x16x32_bf16 a[204:207],v[156:159],v[164:167],0\n"
"s_lshr_b32 s97,s89,3\n"
"s_lshl2_add_u32 s90,s88,s97\n"
"v_mfma_f32_16x16x32_bf16 a[200:203],v[152:155],v[164:167],0\n"
"buffer_load_dwordx4 v107,s[16:19],s76 offen offset:1024 lds\n"
"s_waitcnt lgkmcnt(8)\n"
"v_mfma_f32_16x16x32_bf16 a[208:211],v[152:155],v[168:171],0\n"
"s_lshl_b32 vcc_lo,s90,8\n"
"s_add_u32 s28,s28,vcc_lo\n"
"s_addc_u32 s29,s29,0\n"
"v_mfma_f32_16x16x32_bf16 a[212:215],v[156:159],v[168:171],0\n"
"buffer_load_dwordx4 v108,s[16:19],s76 offen offset:2048 lds\n"
"v_mfma_f32_16x16x32_bf16 a[220:223],v[156:159],v[172:175],0\n"
"s_sub_u32 s30,8388608,vcc_lo\n"
"s_mov_b32 s31,0x20000\n"
"buffer_load_dwordx4 v109,s[16:19],s76 offen offset:3072 lds\n"
"v_mfma_f32_16x16x32_bf16 a[216:219],v[152:155],v[172:175],0\n"
"s_waitcnt vmcnt(9)\n"
"s_barrier\n"
"s_waitcnt lgkmcnt(4)\n"
"v_mfma_f32_16x16x32_bf16 a[224:227],v[152:155],v[176:179],0\n"
"ds_read_b128 v[128:131],v80 offset:0\n"
"ds_read_b128 v[192:195],v81 offset:0\n"
"v_mfma_f32_16x16x32_bf16 a[228:231],v[156:159],v[176:179],0\n"
"ds_read_b64_tr_b16 v[224:225],v98 offset:32768\n"
"ds_read_b64_tr_b16 v[226:227],v98 offset:33792\n"
"v_mfma_f32_16x16x32_bf16 a[236:239],v[156:159],v[180:183],0\n"
"ds_read_b64_tr_b16 v[228:229],v99 offset:32768\n"
"ds_read_b64_tr_b16 v[230:231],v99 offset:33792\n"
"v_mfma_f32_16x16x32_bf16 a[232:235],v[152:155],v[180:183],0\n"
"ds_read_b64_tr_b16 v[232:233],v100 offset:32768\n"
"ds_read_b64_tr_b16 v[234:235],v100 offset:33792\n"
"s_waitcnt lgkmcnt(8)\n"
"v_mfma_f32_16x16x32_bf16 a[240:243],v[152:155],v[184:187],0\n"
"ds_read_b64_tr_b16 v[236:237],v101 offset:32768\n"
"ds_read_b64_tr_b16 v[238:239],v101 offset:33792\n"
"v_mfma_f32_16x16x32_bf16 a[244:247],v[156:159],v[184:187],0\n"
"ds_read_b64_tr_b16 v[240:241],v102 offset:32768\n"
"ds_read_b64_tr_b16 v[242:243],v102 offset:33792\n"
"v_mfma_f32_16x16x32_bf16 a[252:255],v[156:159],v[188:191],0\n"
"ds_read_b64_tr_b16 v[244:245],v103 offset:32768\n"
"ds_read_b64_tr_b16 v[246:247],v103 offset:33792\n"
"v_mfma_f32_16x16x32_bf16 a[248:251],v[152:155],v[188:191],0\n"
"ds_read_b64_tr_b16 v[248:249],v104 offset:32768\n"
"ds_read_b64_tr_b16 v[250:251],v104 offset:33792\n"
"s_waitcnt lgkmcnt(10)\n"
"v_mfma_f32_16x16x32_bf16 a[0:3],v[128:131],v[160:163],0\n"
"ds_read_b64_tr_b16 v[252:253],v105 offset:32768\n"
"ds_read_b64_tr_b16 v[254:255],v105 offset:33792\n"
"v_mfma_f32_16x16x32_bf16 a[0:3],v[192:195],v[224:227],a[0:3]\n"
"ds_read_b128 v[132:135],v80 offset:2048\n"
"ds_read_b128 v[196:199],v81 offset:2048\n"
"v_mfma_f32_16x16x32_bf16 a[8:11],v[192:195],v[228:231],0\n"
"ds_read_b128 v[216:219],v81 offset:12288\n"
"ds_read_b128 v[220:223],v81 offset:14336\n"
"v_mfma_f32_16x16x32_bf16 a[8:11],v[128:131],v[164:167],a[8:11]\n"
"s_waitcnt lgkmcnt(12)\n"
"s_mov_b32 s76,262272\n"
"s_add_u32 m0,s79,36864\n"
"v_mfma_f32_16x16x32_bf16 a[16:19],v[128:131],v[168:171],0\n"
"buffer_load_dwordx4 v106,s[16:19],s76 offen offset:0 lds\n"
"v_mfma_f32_16x16x32_bf16 a[16:19],v[192:195],v[232:235],a[16:19]\n"
"buffer_load_dwordx4 v107,s[16:19],s76 offen offset:1024 lds\n"
"v_mfma_f32_16x16x32_bf16 a[24:27],v[192:195],v[236:239],0\n"
"buffer_load_dwordx4 v108,s[16:19],s76 offen offset:2048 lds\n"
"buffer_load_dwordx4 v109,s[16:19],s76 offen offset:3072 lds\n"
"v_mfma_f32_16x16x32_bf16 a[24:27],v[128:131],v[172:175],a[24:27]\n"
"s_waitcnt lgkmcnt(8)\n"
"s_mov_b32 s77,524288\n"
"s_add_u32 m0,s78,98304\n"
"v_mfma_f32_16x16x32_bf16 a[32:35],v[128:131],v[176:179],0\n"
"buffer_load_dwordx4 v110,s[20:23],s77 offen offset:0 lds\n"
"v_mfma_f32_16x16x32_bf16 a[32:35],v[192:195],v[240:243],a[32:35]\n"
"buffer_load_dwordx4 v111,s[20:23],s77 offen offset:1024 lds\n"
"v_mfma_f32_16x16x32_bf16 a[40:43],v[192:195],v[244:247],0\n"
"buffer_load_dwordx4 v113,s[20:23],s77 offen offset:2048 lds\n"
"buffer_load_dwordx4 v114,s[20:23],s77 offen offset:3072 lds\n"
"v_mfma_f32_16x16x32_bf16 a[40:43],v[128:131],v[180:183],a[40:43]\n"
"s_waitcnt lgkmcnt(4)\n"
"s_mov_b32 s77,786432\n"
"s_add_u32 m0,s78,114688\n"
"v_mfma_f32_16x16x32_bf16 a[48:51],v[128:131],v[184:187],0\n"
"buffer_load_dwordx4 v110,s[20:23],s77 offen offset:0 lds\n"
"v_mfma_f32_16x16x32_bf16 a[48:51],v[192:195],v[248:251],a[48:51]\n"
"buffer_load_dwordx4 v111,s[20:23],s77 offen offset:1024 lds\n"
"v_mfma_f32_16x16x32_bf16 a[56:59],v[192:195],v[252:255],0\n"
"buffer_load_dwordx4 v113,s[20:23],s77 offen offset:2048 lds\n"
"buffer_load_dwordx4 v114,s[20:23],s77 offen offset:3072 lds\n"
"v_mfma_f32_16x16x32_bf16 a[56:59],v[128:131],v[188:191],a[56:59]\n"
"s_waitcnt lgkmcnt(2)\n"
"s_mov_b32 s76,256\n"
"s_add_u32 m0,s79,65536\n"
"v_mfma_f32_16x16x32_bf16 a[4:7],v[132:135],v[160:163],0\n"
"buffer_load_dwordx4 v106,s[16:19],s76 offen offset:0 lds\n"
"v_mfma_f32_16x16x32_bf16 a[4:7],v[196:199],v[224:227],a[4:7]\n"
"buffer_load_dwordx4 v107,s[16:19],s76 offen offset:1024 lds\n"
"v_mfma_f32_16x16x32_bf16 a[12:15],v[196:199],v[228:231],0\n"
"buffer_load_dwordx4 v108,s[16:19],s76 offen offset:2048 lds\n"
"buffer_load_dwordx4 v109,s[16:19],s76 offen offset:3072 lds\n"
"v_mfma_f32_16x16x32_bf16 a[12:15],v[132:135],v[164:167],a[12:15]\n"
"s_waitcnt vmcnt(21)\n"
"s_barrier\n"
"v_mfma_f32_16x16x32_bf16 a[20:23],v[132:135],v[168:171],0\n"
"ds_read_b128 v[136:139],v80 offset:4096\n"
"ds_read_b128 v[200:203],v81 offset:4096\n"
"v_mfma_f32_16x16x32_bf16 a[20:23],v[196:199],v[232:235],a[20:23]\n"
"ds_read_b128 v[140:143],v80 offset:6144\n"
"ds_read_b128 v[204:207],v81 offset:6144\n"
"v_mfma_f32_16x16x32_bf16 a[28:31],v[196:199],v[236:239],0\n"
"ds_read_b128 v[144:147],v80 offset:8192\n"
"ds_read_b128 v[208:211],v81 offset:8192\n"
"v_mfma_f32_16x16x32_bf16 a[28:31],v[132:135],v[172:175],a[28:31]\n"
"ds_read_b128 v[148:151],v80 offset:10240\n"
"ds_read_b128 v[212:215],v81 offset:10240\n"
"v_mfma_f32_16x16x32_bf16 a[36:39],v[132:135],v[176:179],0\n"
"s_mov_b32 s60,s91\n"
"s_mov_b32 s76,262400\n"
"s_add_u32 m0,s79,69632\n"
"v_mfma_f32_16x16x32_bf16 a[36:39],v[196:199],v[240:243],a[36:39]\n"
"buffer_load_dwordx4 v106,s[16:19],s76 offen offset:0 lds\n"
"v_mfma_f32_16x16x32_bf16 a[44:47],v[196:199],v[244:247],0\n"
"buffer_load_dwordx4 v107,s[16:19],s76 offen offset:1024 lds\n"
"v_mfma_f32_16x16x32_bf16 a[44:47],v[132:135],v[180:183],a[44:47]\n"
"buffer_load_dwordx4 v108,s[16:19],s76 offen offset:2048 lds\n"
"v_mfma_f32_16x16x32_bf16 a[52:55],v[132:135],v[184:187],0\n"
"buffer_load_dwordx4 v109,s[16:19],s76 offen offset:3072 lds\n"
"v_mfma_f32_16x16x32_bf16 a[52:55],v[196:199],v[248:251],a[52:55]\n"
"s_sub_u32 s94,15,s91\n"
"s_waitcnt lgkmcnt(0)\n"
"s_barrier\n"
"v_mfma_f32_16x16x32_bf16 a[60:63],v[196:199],v[252:255],0\n"
"s_cmp_lt_u32 s52,2\n"
"s_cselect_b32 s95,s94,s91\n"
"s_mov_b32 s77,1048576\n"
"s_add_u32 m0,s78,147456\n"
"v_mfma_f32_16x16x32_bf16 a[60:63],v[132:135],v[188:191],a[60:63]\n"
"buffer_load_dwordx4 v110,s[20:23],s77 offen offset:0 lds\n"
"v_mfma_f32_16x16x32_bf16 a[64:67],v[136:139],v[160:163],0\n"
"s_lshr_b32 vcc_lo,s95,1\n"
"s_lshl_b32 s55,vcc_lo,19\n"
"v_mfma_f32_16x16x32_bf16 a[64:67],v[200:203],v[224:227],a[64:67]\n"
"s_lshl_b32 s58,vcc_lo,10\n"
"s_add_u32 s58,s58,148898816\n"
"v_mfma_f32_16x16x32_bf16 a[72:75],v[200:203],v[228:231],0\n"
"s_and_b32 vcc_lo,s95,1\n"
"s_lshl_b32 vcc_lo,vcc_lo,13\n"
"v_mfma_f32_16x16x32_bf16 a[72:75],v[136:139],v[164:167],a[72:75]\n"
"buffer_load_dwordx4 v111,s[20:23],s77 offen offset:1024 lds\n"
"v_mfma_f32_16x16x32_bf16 a[80:83],v[136:139],v[168:171],0\n"
"s_and_b32 vcc_hi,s78,4096\n"
"s_add_u32 s55,s55,vcc_lo\n"
"v_mfma_f32_16x16x32_bf16 a[80:83],v[200:203],v[232:235],a[80:83]\n"
"s_add_u32 s55,s55,vcc_hi\n"
"s_and_b32 vcc_lo,s89,2\n"
"v_mfma_f32_16x16x32_bf16 a[88:91],v[200:203],v[236:239],0\n"
"s_lshl2_add_u32 vcc_lo,s88,vcc_lo\n"
"s_and_b32 vcc_hi,s52,1\n"
"v_mfma_f32_16x16x32_bf16 a[88:91],v[136:139],v[172:175],a[88:91]\n"
"buffer_load_dwordx4 v113,s[20:23],s77 offen offset:2048 lds\n"
"v_mfma_f32_16x16x32_bf16 a[96:99],v[136:139],v[176:179],0\n"
"s_add_u32 vcc_lo,vcc_lo,vcc_hi\n"
"s_lshr_b32 vcc_hi,s89,2\n"
"v_mfma_f32_16x16x32_bf16 a[96:99],v[200:203],v[240:243],a[96:99]\n"
"s_lshr_b32 s84,s52,1\n"
"s_lshl1_add_u32 vcc_hi,vcc_hi,s84\n"
"v_mfma_f32_16x16x32_bf16 a[104:107],v[200:203],v[244:247],0\n"
"s_mul_i32 s56,vcc_lo,1179648\n"
"s_lshl_b32 s84,s84,19\n"
"v_mfma_f32_16x16x32_bf16 a[104:107],v[136:139],v[180:183],a[104:107]\n"
"buffer_load_dwordx4 v114,s[20:23],s77 offen offset:3072 lds\n"
"v_mfma_f32_16x16x32_bf16 a[112:115],v[136:139],v[184:187],0\n"
"s_add_u32 s56,s56,s84\n"
"s_lshr_b32 s84,s89,2\n"
"v_mfma_f32_16x16x32_bf16 a[112:115],v[200:203],v[248:251],a[112:115]\n"
"s_lshl_b32 s84,s84,14\n"
"s_add_u32 s56,s56,s84\n"
"v_mfma_f32_16x16x32_bf16 a[120:123],v[200:203],v[252:255],0\n"
"s_lshl_b32 s84,s53,13\n"
"s_mov_b32 s77,1310720\n"
"s_add_u32 m0,s78,131072\n"
"v_mfma_f32_16x16x32_bf16 a[120:123],v[136:139],v[188:191],a[120:123]\n"
"buffer_load_dwordx4 v110,s[20:23],s77 offen offset:0 lds\n"
"v_mfma_f32_16x16x32_bf16 a[68:71],v[140:143],v[160:163],0\n"
"s_add_u32 s56,s56,s84\n"
"s_add_u32 s56,s56,4194304\n"
"v_mfma_f32_16x16x32_bf16 a[68:71],v[204:207],v[224:227],a[68:71]\n"
"s_lshl4_add_u32 vcc_hi,s53,vcc_hi\n"
"s_lshl_b32 vcc_lo,vcc_lo,8\n"
"v_mfma_f32_16x16x32_bf16 a[76:79],v[204:207],v[228:231],0\n"
"buffer_load_dwordx4 v111,s[20:23],s77 offen offset:1024 lds\n"
"v_mfma_f32_16x16x32_bf16 a[76:79],v[140:143],v[164:167],a[76:79]\n"
"s_lshl3_add_u32 s59,vcc_hi,vcc_lo\n"
"s_add_u32 s59,s59,148914176\n"
"v_mfma_f32_16x16x32_bf16 a[84:87],v[140:143],v[168:171],0\n"
"s_lshl_b32 s57,s92,3\n"
"s_add_u32 s57,s57,148898816\n"
"v_mfma_f32_16x16x32_bf16 a[84:87],v[204:207],v[232:235],a[84:87]\n"
"s_lshl_b32 s63,s90,8\n"
"s_add_u32 s63,s63,148914176\n"
"v_mfma_f32_16x16x32_bf16 a[92:95],v[204:207],v[236:239],0\n"
"buffer_load_dwordx4 v113,s[20:23],s77 offen offset:2048 lds\n"
"v_mfma_f32_16x16x32_bf16 a[92:95],v[140:143],v[172:175],a[92:95]\n"
"s_lshl_b32 s74,s92,11\n"
"s_add_u32 s74,s74,171966464\n"
"v_mfma_f32_16x16x32_bf16 a[100:103],v[140:143],v[176:179],0\n"
"s_mul_i32 s96,s90,1179648\n"
"s_add_u32 s96,s96,4194304\n"
"v_mfma_f32_16x16x32_bf16 a[100:103],v[204:207],v[240:243],a[100:103]\n"
"s_add_u32 s73,s96,s85\n"
"s_and_b32 vcc_hi,s78,4096\n"
"v_mfma_f32_16x16x32_bf16 a[108:111],v[204:207],v[244:247],0\n"
"buffer_load_dwordx4 v114,s[20:23],s77 offen offset:3072 lds\n"
"v_mfma_f32_16x16x32_bf16 a[108:111],v[140:143],v[180:183],a[108:111]\n"
"s_lshl_b32 vcc_hi,vcc_hi,1\n"
"s_add_u32 vcc_lo,s96,vcc_hi\n"
"v_mfma_f32_16x16x32_bf16 a[116:119],v[140:143],v[184:187],0\n"
"s_add_u32 s65,vcc_lo,524288\n"
"s_lshl_b32 s66,s91,16\n"
"v_mfma_f32_16x16x32_bf16 a[116:119],v[204:207],v[248:251],a[116:119]\n"
"s_sub_u32 s66,vcc_lo,s66\n"
"s_mov_b32 s76,384\n"
"s_add_u32 m0,s79,0\n"
"v_mfma_f32_16x16x32_bf16 a[124:127],v[204:207],v[252:255],0\n"
"buffer_load_dwordx4 v106,s[16:19],s76 offen offset:0 lds\n"
"v_mfma_f32_16x16x32_bf16 a[124:127],v[140:143],v[188:191],a[124:127]\n"
"s_add_u32 s66,s66,999424\n"
"s_add_u32 s67,vcc_hi,65536\n"
"v_mfma_f32_16x16x32_bf16 a[128:131],v[144:147],v[160:163],0\n"
"s_mul_i32 vcc_lo,vcc_hi,5\n"
"s_sub_u32 vcc_lo,s96,vcc_lo\n"
"v_mfma_f32_16x16x32_bf16 a[128:131],v[208:211],v[224:227],a[128:131]\n"
"s_add_u32 s64,vcc_lo,81920\n"
"s_add_u32 s62,vcc_lo,135168\n"
"s_add_u32 s61,vcc_lo,524288\n"
"v_mfma_f32_16x16x32_bf16 a[136:139],v[208:211],v[228:231],0\n"
"buffer_load_dwordx4 v107,s[16:19],s76 offen offset:1024 lds\n"
"v_mfma_f32_16x16x32_bf16 a[136:139],v[144:147],v[164:167],a[136:139]\n"
"s_cmp_lt_u32 s52,2\n"
"s_cselect_b32 s64,s64,2146435072\n"
"s_cselect_b32 s67,s67,163840\n"
"s_cselect_b32 s65,s65,1\n"
"v_mfma_f32_16x16x32_bf16 a[144:147],v[144:147],v[168:171],0\n"
"s_cselect_b32 vcc_lo,4,0\n"
"s_and_b32 vcc_hi,s52,1\n"
"s_add_u32 vcc_lo,vcc_lo,vcc_hi\n"
"v_mfma_f32_16x16x32_bf16 a[144:147],v[208:211],v[232:235],a[144:147]\n"
"s_lshl_b32 vcc_hi,s90,12\n"
"s_add_u32 vcc_hi,vcc_hi,54525952\n"
"v_mfma_f32_16x16x32_bf16 a[152:155],v[208:211],v[236:239],0\n"
"buffer_load_dwordx4 v108,s[16:19],s76 offen offset:2048 lds\n"
"v_mfma_f32_16x16x32_bf16 a[152:155],v[144:147],v[172:175],a[152:155]\n"
"s_lshl3_add_u32 s70,s95,vcc_lo\n"
"s_lshl_b32 s70,s70,17\n"
"v_mfma_f32_16x16x32_bf16 a[160:163],v[144:147],v[176:179],0\n"
"s_add_u32 s70,s70,vcc_hi\n"
"s_lshl3_add_u32 s68,s94,vcc_lo\n"
"v_mfma_f32_16x16x32_bf16 a[160:163],v[208:211],v[240:243],a[160:163]\n"
"s_lshl_b32 s68,s68,17\n"
"s_add_u32 s68,s68,vcc_hi\n"
"v_mfma_f32_16x16x32_bf16 a[168:171],v[208:211],v[244:247],0\n"
"buffer_load_dwordx4 v109,s[16:19],s76 offen offset:3072 lds\n"
"v_mfma_f32_16x16x32_bf16 a[168:171],v[144:147],v[180:183],a[168:171]\n"
"s_lshr_b32 vcc_lo,s90,3\n"
"s_lshl2_add_u32 vcc_lo,vcc_lo,s52\n"
"v_mfma_f32_16x16x32_bf16 a[176:179],v[144:147],v[184:187],0\n"
"s_lshl_b32 vcc_lo,vcc_lo,7\n"
"s_and_b32 vcc_hi,s90,7\n"
"v_mfma_f32_16x16x32_bf16 a[176:179],v[208:211],v[248:251],a[176:179]\n"
"s_add_u32 vcc_lo,vcc_lo,vcc_hi\n"
"s_mov_b32 s76,262528\n"
"s_add_u32 m0,s79,4096\n"
"v_mfma_f32_16x16x32_bf16 a[184:187],v[208:211],v[252:255],0\n"
"buffer_load_dwordx4 v106,s[16:19],s76 offen offset:0 lds\n"
"v_mfma_f32_16x16x32_bf16 a[184:187],v[144:147],v[188:191],a[184:187]\n"
"s_lshl3_add_u32 s69,s91,vcc_lo\n"
"s_lshl_b32 s69,s69,3\n"
"v_mfma_f32_16x16x32_bf16 a[132:135],v[148:151],v[160:163],0\n"
"s_add_u32 s69,s69,148930560\n"
"s_lshl3_add_u32 s71,s94,vcc_lo\n"
"v_mfma_f32_16x16x32_bf16 a[132:135],v[212:215],v[224:227],a[132:135]\n"
"s_lshl_b32 s71,s71,3\n"
"s_add_u32 s71,s71,148930560\n"
"v_mfma_f32_16x16x32_bf16 a[140:143],v[212:215],v[228:231],0\n"
"buffer_load_dwordx4 v107,s[16:19],s76 offen offset:1024 lds\n"
"v_mfma_f32_16x16x32_bf16 a[140:143],v[148:151],v[164:167],a[140:143]\n"
"s_mov_b32 s6,8388608\n"
"s_mov_b32 s7,0x20000\n"
"v_mfma_f32_16x16x32_bf16 a[148:151],v[148:151],v[168:171],0\n"
"s_lshr_b32 s94,s88,1\n"
"s_and_b32 vcc_lo,s91,1\n"
"v_mfma_f32_16x16x32_bf16 a[148:151],v[212:215],v[232:235],a[148:151]\n"
"s_lshl1_add_u32 s95,s97,vcc_lo\n"
"s_and_b32 vcc_lo,s88,1\n"
"v_mfma_f32_16x16x32_bf16 a[156:159],v[212:215],v[236:239],0\n"
"buffer_load_dwordx4 v108,s[16:19],s76 offen offset:2048 lds\n"
"v_mfma_f32_16x16x32_bf16 a[156:159],v[148:151],v[172:175],a[156:159]\n"
"s_lshr_b32 vcc_hi,s91,1\n"
"s_lshl2_add_u32 s96,vcc_lo,vcc_hi\n"
"v_mfma_f32_16x16x32_bf16 a[164:167],v[148:151],v[176:179],0\n"
"s_lshr_b32 vcc_lo,s88,2\n"
"s_lshl2_add_u32 s96,vcc_lo,vcc_hi\n"
"v_mfma_f32_16x16x32_bf16 a[164:167],v[212:215],v[240:243],a[164:167]\n"
"s_sub_u32 vcc_lo,7,s88\n"
"s_min_u32 s94,vcc_lo,s88\n"
"v_mfma_f32_16x16x32_bf16 a[172:175],v[212:215],v[244:247],0\n"
"buffer_load_dwordx4 v109,s[16:19],s76 offen offset:3072 lds\n"
"v_mfma_f32_16x16x32_bf16 a[172:175],v[148:151],v[180:183],a[172:175]\n"
"s_lshr_b32 s94,s88,1\n"
"s_xor_b32 s94,s94,2\n"
"s_lshl_b32 s96,s88,2\n"
"v_mfma_f32_16x16x32_bf16 a[180:183],v[148:151],v[184:187],0\n"
"buffer_load_dwordx4 v[8:11],v70,s[32:35],0 offen offset:0\n"
"v_mfma_f32_16x16x32_bf16 a[180:183],v[212:215],v[248:251],a[180:183]\n"
"buffer_load_dwordx4 v[12:15],v70,s[32:35],0 offen offset:512\n"
"s_and_b32 s96,s96,4\n"
"v_mfma_f32_16x16x32_bf16 a[188:191],v[212:215],v[252:255],0\n"
"buffer_load_dwordx4 v[16:19],v70,s[32:35],0 offen offset:1024\n"
"buffer_load_dwordx4 v[20:23],v70,s[32:35],0 offen offset:1536\n"
"s_add_u32 s96,s96,s97\n"
"v_mfma_f32_16x16x32_bf16 a[188:191],v[148:151],v[188:191],a[188:191]\n"
"s_waitcnt vmcnt(28)\n"
"s_barrier\n"
"v_mfma_f32_16x16x32_bf16 a[192:195],v[216:219],v[224:227],a[192:195]\n"
"ds_read_b128 v[152:155],v80 offset:45056\n"
"s_mov_b32 s87,524288\n"
"v_mfma_f32_16x16x32_bf16 a[196:199],v[220:223],v[224:227],a[196:199]\n"
"ds_read_b128 v[156:159],v80 offset:47104\n"
"buffer_load_dword v115,v112,s[36:39],s87 offen\n"
"v_mfma_f32_16x16x32_bf16 a[204:207],v[220:223],v[228:231],a[204:207]\n"
"ds_read_b64_tr_b16 v[160:161],v98 offset:0\n"
"s_mov_b32 s87,1048576\n"
"v_mfma_f32_16x16x32_bf16 a[200:203],v[216:219],v[228:231],a[200:203]\n"
"ds_read_b64_tr_b16 v[162:163],v98 offset:1024\n"
"buffer_load_dword v115,v112,s[36:39],s87 offen\n"
"v_mfma_f32_16x16x32_bf16 a[208:211],v[216:219],v[232:235],a[208:211]\n"
"ds_read_b64_tr_b16 v[164:165],v99 offset:0\n"
"s_mov_b32 s87,1572864\n"
"v_mfma_f32_16x16x32_bf16 a[212:215],v[220:223],v[232:235],a[212:215]\n"
"ds_read_b64_tr_b16 v[166:167],v99 offset:1024\n"
"buffer_load_dword v115,v112,s[36:39],s87 offen\n"
"v_mfma_f32_16x16x32_bf16 a[220:223],v[220:223],v[236:239],a[220:223]\n"
"ds_read_b64_tr_b16 v[168:169],v100 offset:0\n"
"s_add_u32 s95,s91,s96\n"
"v_mfma_f32_16x16x32_bf16 a[216:219],v[216:219],v[236:239],a[216:219]\n"
"ds_read_b64_tr_b16 v[170:171],v100 offset:1024\n"
"s_and_b32 s95,s95,7\n"
"v_mfma_f32_16x16x32_bf16 a[224:227],v[216:219],v[240:243],a[224:227]\n"
"ds_read_b64_tr_b16 v[172:173],v101 offset:0\n"
"s_lshl3_add_u32 vcc_lo,s94,s96\n"
"v_mfma_f32_16x16x32_bf16 a[228:231],v[220:223],v[240:243],a[228:231]\n"
"ds_read_b64_tr_b16 v[174:175],v101 offset:1024\n"
"s_lshl3_add_u32 vcc_lo,vcc_lo,s52\n"
"v_mfma_f32_16x16x32_bf16 a[236:239],v[220:223],v[244:247],a[236:239]\n"
"ds_read_b64_tr_b16 v[176:177],v102 offset:0\n"
"s_lshl_b32 s13,vcc_lo,15\n"
"v_mfma_f32_16x16x32_bf16 a[232:235],v[216:219],v[244:247],a[232:235]\n"
"ds_read_b64_tr_b16 v[178:179],v102 offset:1024\n"
"s_lshl_b32 vcc_lo,s95,8\n"
"v_mfma_f32_16x16x32_bf16 a[240:243],v[216:219],v[248:251],a[240:243]\n"
"ds_read_b64_tr_b16 v[180:181],v103 offset:0\n"
"s_add_u32 s13,s13,vcc_lo\n"
"v_mfma_f32_16x16x32_bf16 a[244:247],v[220:223],v[248:251],a[244:247]\n"
"ds_read_b64_tr_b16 v[182:183],v103 offset:1024\n"
"s_lshl3_add_u32 vcc_hi,s94,s95\n"
"v_mfma_f32_16x16x32_bf16 a[252:255],v[220:223],v[252:255],a[252:255]\n"
"ds_read_b64_tr_b16 v[184:185],v104 offset:0\n"
"s_lshl_b32 s72,vcc_hi,8\n"
"v_mfma_f32_16x16x32_bf16 a[248:251],v[216:219],v[252:255],a[248:251]\n"
"s_add_u32 s72,s72,149061632\n"
"ds_read_b64_tr_b16 v[186:187],v104 offset:1024\n"
"s_waitcnt lgkmcnt(10)\n"
"v_mfma_f32_16x16x32_bf16 a[192:195],v[152:155],v[160:163],a[192:195]\n"
"s_or_b32 vcc_lo,s96,s52\n"
"ds_read_b64_tr_b16 v[188:189],v105 offset:0\n"
"s_cselect_b32 s72,2146435072,s72\n"
"v_mfma_f32_16x16x32_bf16 a[196:199],v[156:159],v[160:163],a[196:199]\n"
"ds_read_b64_tr_b16 v[190:191],v105 offset:1024\n"
"s_lshl_b32 s3,vcc_hi,18\n"
"v_mfma_f32_16x16x32_bf16 a[204:207],v[156:159],v[164:167],a[204:207]\n"
"ds_read_b128 v[128:131],v80 offset:32768\n"
"s_lshl_b32 vcc_lo,s96,15\n"
"v_mfma_f32_16x16x32_bf16 a[200:203],v[152:155],v[164:167],a[200:203]\n"
"ds_read_b128 v[192:195],v81 offset:32768\n"
"s_waitcnt lgkmcnt(10)\n"
"v_mfma_f32_16x16x32_bf16 a[208:211],v[152:155],v[168:171],a[208:211]\n"
"ds_read_b64_tr_b16 v[224:225],v98 offset:16384\n"
"s_sub_u32 s3,s3,vcc_lo\n"
"v_mfma_f32_16x16x32_bf16 a[212:215],v[156:159],v[168:171],a[212:215]\n"
"ds_read_b64_tr_b16 v[226:227],v98 offset:17408\n"
"s_add_u32 s3,s3,s78\n"
"v_mfma_f32_16x16x32_bf16 a[220:223],v[156:159],v[172:175],a[220:223]\n"
"ds_read_b64_tr_b16 v[228:229],v99 offset:16384\n"
"s_add_u32 s3,s3,100909056\n"
"v_mfma_f32_16x16x32_bf16 a[216:219],v[152:155],v[172:175],a[216:219]\n"
"ds_read_b64_tr_b16 v[230:231],v99 offset:17408\n"
"s_waitcnt lgkmcnt(10)\n"
"v_mfma_f32_16x16x32_bf16 a[224:227],v[152:155],v[176:179],a[224:227]\n"
"ds_read_b64_tr_b16 v[232:233],v100 offset:16384\n"
"s_lshl3_add_u32 vcc_hi,vcc_hi,s96\n"
"v_mfma_f32_16x16x32_bf16 a[228:231],v[156:159],v[176:179],a[228:231]\n"
"ds_read_b64_tr_b16 v[234:235],v100 offset:17408\n"
"s_lshl2_add_u32 vcc_hi,vcc_hi,s52\n"
"v_mfma_f32_16x16x32_bf16 a[236:239],v[156:159],v[180:183],a[236:239]\n"
"ds_read_b64_tr_b16 v[236:237],v101 offset:16384\n"
"s_lshl_b32 s2,vcc_hi,4\n"
"v_mfma_f32_16x16x32_bf16 a[232:235],v[152:155],v[180:183],a[232:235]\n"
"ds_read_b64_tr_b16 v[238:239],v101 offset:17408\n"
"s_waitcnt lgkmcnt(10)\n"
"v_mfma_f32_16x16x32_bf16 a[240:243],v[152:155],v[184:187],a[240:243]\n"
"ds_read_b64_tr_b16 v[240:241],v102 offset:16384\n"
"s_add_u32 s2,s2,149028864\n"
"v_mfma_f32_16x16x32_bf16 a[244:247],v[156:159],v[184:187],a[244:247]\n"
"ds_read_b64_tr_b16 v[242:243],v102 offset:17408\n"
"s_lshl2_add_u32 vcc_lo,s95,s52\n"
"v_mfma_f32_16x16x32_bf16 a[252:255],v[156:159],v[188:191],a[252:255]\n"
"ds_read_b64_tr_b16 v[244:245],v103 offset:16384\n"
"s_lshl_b32 vcc_lo,vcc_lo,10\n"
"v_mfma_f32_16x16x32_bf16 a[248:251],v[152:155],v[188:191],a[248:251]\n"
"ds_read_b64_tr_b16 v[246:247],v103 offset:17408\n"
"s_waitcnt lgkmcnt(8)\n"
"v_mfma_f32_16x16x32_bf16 a[0:3],v[128:131],v[160:163],a[0:3]\n"
"ds_read_b64_tr_b16 v[248:249],v104 offset:16384\n"
"s_lshl_b32 vcc_hi,s94,22\n"
"v_mfma_f32_16x16x32_bf16 a[0:3],v[192:195],v[224:227],a[0:3]\n"
"ds_read_b64_tr_b16 v[250:251],v104 offset:17408\n"
"s_add_u32 s12,vcc_lo,vcc_hi\n"
"v_mfma_f32_16x16x32_bf16 a[8:11],v[192:195],v[228:231],a[8:11]\n"
"ds_read_b64_tr_b16 v[252:253],v105 offset:16384\n"
"s_lshl_b32 vcc_lo,s96,19\n"
"v_mfma_f32_16x16x32_bf16 a[8:11],v[128:131],v[164:167],a[8:11]\n"
"ds_read_b64_tr_b16 v[254:255],v105 offset:17408\n"
"s_waitcnt lgkmcnt(8)\n"
"v_mfma_f32_16x16x32_bf16 a[16:19],v[128:131],v[168:171],a[16:19]\n"
"ds_read_b128 v[132:135],v80 offset:34816\n"
"s_add_u32 s10,s12,vcc_lo\n"
"v_mfma_f32_16x16x32_bf16 a[16:19],v[192:195],v[232:235],a[16:19]\n"
"ds_read_b128 v[196:199],v81 offset:34816\n"
"s_add_u32 s10,s10,71303168\n"
"v_mfma_f32_16x16x32_bf16 a[24:27],v[192:195],v[236:239],a[24:27]\n"
"ds_read_b128 v[136:139],v80 offset:36864\n"
"s_mul_i32 vcc_lo,s52,31744\n"
"v_mfma_f32_16x16x32_bf16 a[24:27],v[128:131],v[172:175],a[24:27]\n"
"ds_read_b128 v[200:203],v81 offset:36864\n"
"s_waitcnt lgkmcnt(8)\n"
"v_mfma_f32_16x16x32_bf16 a[32:35],v[128:131],v[176:179],a[32:35]\n"
"ds_read_b128 v[140:143],v80 offset:38912\n"
"s_add_u32 s10,s10,vcc_lo\n"
"v_mfma_f32_16x16x32_bf16 a[32:35],v[192:195],v[240:243],a[32:35]\n"
"ds_read_b128 v[204:207],v81 offset:38912\n"
"s_lshl_b32 vcc_lo,s52,10\n"
"v_mfma_f32_16x16x32_bf16 a[40:43],v[192:195],v[244:247],a[40:43]\n"
"ds_read_b128 v[144:147],v80 offset:40960\n"
"s_sub_u32 s12,57671680,vcc_lo\n"
"v_mfma_f32_16x16x32_bf16 a[40:43],v[128:131],v[180:183],a[40:43]\n"
"ds_read_b128 v[208:211],v81 offset:40960\n"
"s_waitcnt lgkmcnt(8)\n"
"v_mfma_f32_16x16x32_bf16 a[48:51],v[128:131],v[184:187],a[48:51]\n"
"ds_read_b128 v[148:151],v80 offset:43008\n"
"s_add_u32 s8,s79,54525952\n"
"v_mfma_f32_16x16x32_bf16 a[48:51],v[192:195],v[248:251],a[48:51]\n"
"ds_read_b128 v[212:215],v81 offset:43008\n"
"s_and_b32 vcc_lo,s96,1\n"
"v_mfma_f32_16x16x32_bf16 a[56:59],v[192:195],v[252:255],a[56:59]\n"
"ds_read_b128 v[216:219],v81 offset:45056\n"
"s_lshl_b32 s11,vcc_lo,10\n"
"v_mfma_f32_16x16x32_bf16 a[56:59],v[128:131],v[188:191],a[56:59]\n"
"ds_read_b128 v[220:223],v81 offset:47104\n"
"s_waitcnt lgkmcnt(10)\n"
"v_mfma_f32_16x16x32_bf16 a[4:7],v[132:135],v[160:163],a[4:7]\n"
"s_add_u32 s11,s11,148932608\n"
"s_mov_b32 s9,148931072\n"
"v_mfma_f32_16x16x32_bf16 a[4:7],v[196:199],v[224:227],a[4:7]\n"
"s_cmp_eq_u32 s52,0\n"
"s_cselect_b32 s71,s71,2146435072\n"
"v_mfma_f32_16x16x32_bf16 a[12:15],v[196:199],v[228:231],a[12:15]\n"
"v_mfma_f32_16x16x32_bf16 a[12:15],v[132:135],v[164:167],a[12:15]\n"
"v_mfma_f32_16x16x32_bf16 a[20:23],v[132:135],v[168:171],a[20:23]\n"
"s_waitcnt lgkmcnt(0)\n"
"s_barrier\n"
"v_mfma_f32_16x16x32_bf16 a[20:23],v[196:199],v[232:235],a[20:23]\n"
"s_mov_b32 s77,1572864\n"
"s_add_u32 m0,s78,98304\n"
"v_mfma_f32_16x16x32_bf16 a[28:31],v[196:199],v[236:239],a[28:31]\n"
"buffer_load_dwordx4 v110,s[20:23],s77 offen offset:0 lds\n"
"v_mfma_f32_16x16x32_bf16 a[28:31],v[132:135],v[172:175],a[28:31]\n"
"v_mfma_f32_16x16x32_bf16 a[36:39],v[132:135],v[176:179],a[36:39]\n"
"v_mfma_f32_16x16x32_bf16 a[36:39],v[196:199],v[240:243],a[36:39]\n"
"v_mfma_f32_16x16x32_bf16 a[44:47],v[196:199],v[244:247],a[44:47]\n"
"buffer_load_dwordx4 v111,s[20:23],s77 offen offset:1024 lds\n"
"v_mfma_f32_16x16x32_bf16 a[44:47],v[132:135],v[180:183],a[44:47]\n"
"v_mfma_f32_16x16x32_bf16 a[52:55],v[132:135],v[184:187],a[52:55]\n"
"v_mfma_f32_16x16x32_bf16 a[52:55],v[196:199],v[248:251],a[52:55]\n"
"v_mfma_f32_16x16x32_bf16 a[60:63],v[196:199],v[252:255],a[60:63]\n"
"buffer_load_dwordx4 v113,s[20:23],s77 offen offset:2048 lds\n"
"v_mfma_f32_16x16x32_bf16 a[60:63],v[132:135],v[188:191],a[60:63]\n"
"v_mfma_f32_16x16x32_bf16 a[64:67],v[136:139],v[160:163],a[64:67]\n"
"v_mfma_f32_16x16x32_bf16 a[64:67],v[200:203],v[224:227],a[64:67]\n"
"v_mfma_f32_16x16x32_bf16 a[72:75],v[200:203],v[228:231],a[72:75]\n"
"buffer_load_dwordx4 v114,s[20:23],s77 offen offset:3072 lds\n"
"v_mfma_f32_16x16x32_bf16 a[72:75],v[136:139],v[164:167],a[72:75]\n"
"v_mfma_f32_16x16x32_bf16 a[80:83],v[136:139],v[168:171],a[80:83]\n"
"v_mfma_f32_16x16x32_bf16 a[80:83],v[200:203],v[232:235],a[80:83]\n"
"s_mov_b32 s77,1835008\n"
"s_add_u32 m0,s78,114688\n"
"v_mfma_f32_16x16x32_bf16 a[88:91],v[200:203],v[236:239],a[88:91]\n"
"buffer_load_dwordx4 v110,s[20:23],s77 offen offset:0 lds\n"
"v_mfma_f32_16x16x32_bf16 a[88:91],v[136:139],v[172:175],a[88:91]\n"
"v_mfma_f32_16x16x32_bf16 a[96:99],v[136:139],v[176:179],a[96:99]\n"
"v_mfma_f32_16x16x32_bf16 a[96:99],v[200:203],v[240:243],a[96:99]\n"
"v_mfma_f32_16x16x32_bf16 a[104:107],v[200:203],v[244:247],a[104:107]\n"
"buffer_load_dwordx4 v111,s[20:23],s77 offen offset:1024 lds\n"
"v_mfma_f32_16x16x32_bf16 a[104:107],v[136:139],v[180:183],a[104:107]\n"
"v_mfma_f32_16x16x32_bf16 a[112:115],v[136:139],v[184:187],a[112:115]\n"
"v_mfma_f32_16x16x32_bf16 a[112:115],v[200:203],v[248:251],a[112:115]\n"
"v_mfma_f32_16x16x32_bf16 a[120:123],v[200:203],v[252:255],a[120:123]\n"
"buffer_load_dwordx4 v113,s[20:23],s77 offen offset:2048 lds\n"
"v_mfma_f32_16x16x32_bf16 a[120:123],v[136:139],v[188:191],a[120:123]\n"
"s_waitcnt vmcnt(10)\n"
"v_mfma_f32_16x16x32_bf16 a[68:71],v[140:143],v[160:163],a[68:71]\n"
"v_lshlrev_b32 v24,16,v8\n"
"v_and_b32 v25,0xffff0000,v8\n"
"v_mfma_f32_16x16x32_bf16 a[68:71],v[204:207],v[224:227],a[68:71]\n"
"v_lshlrev_b32 v26,16,v9\n"
"v_and_b32 v27,0xffff0000,v9\n"
"v_mfma_f32_16x16x32_bf16 a[76:79],v[204:207],v[228:231],a[76:79]\n"
"buffer_load_dwordx4 v114,s[20:23],s77 offen offset:3072 lds\n"
"v_mfma_f32_16x16x32_bf16 a[76:79],v[140:143],v[164:167],a[76:79]\n"
"v_lshlrev_b32 v28,16,v10\n"
"v_and_b32 v29,0xffff0000,v10\n"
"v_mfma_f32_16x16x32_bf16 a[84:87],v[140:143],v[168:171],a[84:87]\n"
"v_lshlrev_b32 v30,16,v11\n"
"v_and_b32 v31,0xffff0000,v11\n"
"v_mfma_f32_16x16x32_bf16 a[84:87],v[204:207],v[232:235],a[84:87]\n"
"v_lshlrev_b32 v32,16,v12\n"
"v_and_b32 v33,0xffff0000,v12\n"
"v_mfma_f32_16x16x32_bf16 a[92:95],v[204:207],v[236:239],a[92:95]\n"
"s_mov_b32 s76,512\n"
"v_lshlrev_b32 v34,16,v13\n"
"s_add_u32 m0,s79,32768\n"
"v_mfma_f32_16x16x32_bf16 a[92:95],v[140:143],v[172:175],a[92:95]\n"
"buffer_load_dwordx4 v106,s[16:19],s76 offen offset:0 lds\n"
"v_mfma_f32_16x16x32_bf16 a[100:103],v[140:143],v[176:179],a[100:103]\n"
"v_and_b32 v35,0xffff0000,v13\n"
"v_lshlrev_b32 v36,16,v14\n"
"v_mfma_f32_16x16x32_bf16 a[100:103],v[204:207],v[240:243],a[100:103]\n"
"v_and_b32 v37,0xffff0000,v14\n"
"v_lshlrev_b32 v38,16,v15\n"
"v_mfma_f32_16x16x32_bf16 a[108:111],v[204:207],v[244:247],a[108:111]\n"
"v_and_b32 v39,0xffff0000,v15\n"
"v_lshlrev_b32 v40,16,v16\n"
"v_mfma_f32_16x16x32_bf16 a[108:111],v[140:143],v[180:183],a[108:111]\n"
"v_and_b32 v41,0xffff0000,v16\n"
"v_lshlrev_b32 v42,16,v17\n"
"v_mfma_f32_16x16x32_bf16 a[116:119],v[140:143],v[184:187],a[116:119]\n"
"buffer_load_dwordx4 v107,s[16:19],s76 offen offset:1024 lds\n"
"v_mfma_f32_16x16x32_bf16 a[116:119],v[204:207],v[248:251],a[116:119]\n"
"v_and_b32 v43,0xffff0000,v17\n"
"v_lshlrev_b32 v44,16,v18\n"
"v_mfma_f32_16x16x32_bf16 a[124:127],v[204:207],v[252:255],a[124:127]\n"
"v_and_b32 v45,0xffff0000,v18\n"
"v_lshlrev_b32 v46,16,v19\n"
"v_mfma_f32_16x16x32_bf16 a[124:127],v[140:143],v[188:191],a[124:127]\n"
"v_and_b32 v47,0xffff0000,v19\n"
"v_lshlrev_b32 v48,16,v20\n"
"v_mfma_f32_16x16x32_bf16 a[128:131],v[144:147],v[160:163],a[128:131]\n"
"v_and_b32 v49,0xffff0000,v20\n"
"v_lshlrev_b32 v50,16,v21\n"
"v_mfma_f32_16x16x32_bf16 a[128:131],v[208:211],v[224:227],a[128:131]\n"
"buffer_load_dwordx4 v108,s[16:19],s76 offen offset:2048 lds\n"
"v_mfma_f32_16x16x32_bf16 a[136:139],v[208:211],v[228:231],a[136:139]\n"
"v_and_b32 v51,0xffff0000,v21\n"
"v_lshlrev_b32 v52,16,v22\n"
"v_mfma_f32_16x16x32_bf16 a[136:139],v[144:147],v[164:167],a[136:139]\n"
"v_and_b32 v53,0xffff0000,v22\n"
"v_lshlrev_b32 v54,16,v23\n"
"v_mfma_f32_16x16x32_bf16 a[144:147],v[144:147],v[168:171],a[144:147]\n"
"v_and_b32 v55,0xffff0000,v23\n"
"v_add_f32 v56,v24,v25\n"
"v_mfma_f32_16x16x32_bf16 a[144:147],v[208:211],v[232:235],a[144:147]\n"
"v_add_f32 v57,v26,v27\n"
"v_add_f32 v56,v56,v28\n"
"v_mfma_f32_16x16x32_bf16 a[152:155],v[208:211],v[236:239],a[152:155]\n"
"buffer_load_dwordx4 v109,s[16:19],s76 offen offset:3072 lds\n"
"v_mfma_f32_16x16x32_bf16 a[152:155],v[144:147],v[172:175],a[152:155]\n"
"v_add_f32 v57,v57,v29\n"
"v_add_f32 v56,v56,v30\n"
"v_mfma_f32_16x16x32_bf16 a[160:163],v[144:147],v[176:179],a[160:163]\n"
"v_add_f32 v57,v57,v31\n"
"v_add_f32 v56,v56,v32\n"
"v_mfma_f32_16x16x32_bf16 a[160:163],v[208:211],v[240:243],a[160:163]\n"
"v_readfirstlane_b32 s82,v78\n"
"v_readfirstlane_b32 s83,v79\n"
"v_mfma_f32_16x16x32_bf16 a[168:171],v[208:211],v[244:247],a[168:171]\n"
"s_mov_b32 s76,262656\n"
"v_add_f32 v57,v57,v33\n"
"s_add_u32 m0,s79,36864\n"
"v_mfma_f32_16x16x32_bf16 a[168:171],v[144:147],v[180:183],a[168:171]\n"
"buffer_load_dwordx4 v106,s[16:19],s76 offen offset:0 lds\n"
"s_mul_i32 s80,s82,0x9e3779b1\n"
"v_mfma_f32_16x16x32_bf16 a[176:179],v[144:147],v[184:187],a[176:179]\n"
"s_add_u32 s80,s80,0x7f4a7c15\n"
"v_add_f32 v56,v56,v34\n"
"v_add_f32 v57,v57,v35\n"
"v_mfma_f32_16x16x32_bf16 a[176:179],v[208:211],v[248:251],a[176:179]\n"
"s_add_u32 s81,s83,s80\n"
"s_add_u32 s81,s81,1\n"
"v_mfma_f32_16x16x32_bf16 a[184:187],v[208:211],v[252:255],a[184:187]\n"
"v_mov_b32 v124,s80\n"
"v_mov_b32 v125,s81\n"
"v_mfma_f32_16x16x32_bf16 a[184:187],v[144:147],v[188:191],a[184:187]\n"
"v_add_f32 v56,v56,v36\n"
"v_add_f32 v57,v57,v37\n"
"v_mfma_f32_16x16x32_bf16 a[132:135],v[148:151],v[160:163],a[132:135]\n"
"buffer_load_dwordx4 v107,s[16:19],s76 offen offset:1024 lds\n"
"v_mfma_f32_16x16x32_bf16 a[132:135],v[212:215],v[224:227],a[132:135]\n"
"v_add_f32 v56,v56,v38\n"
"v_add_f32 v57,v57,v39\n"
"v_mfma_f32_16x16x32_bf16 a[140:143],v[212:215],v[228:231],a[140:143]\n"
"v_add_f32 v56,v56,v40\n"
"v_add_f32 v57,v57,v41\n"
"v_mfma_f32_16x16x32_bf16 a[140:143],v[148:151],v[164:167],a[140:143]\n"
"v_add_f32 v56,v56,v42\n"
"v_add_f32 v57,v57,v43\n"
"v_mfma_f32_16x16x32_bf16 a[148:151],v[148:151],v[168:171],a[148:151]\n"
"v_add_f32 v56,v56,v44\n"
"v_add_f32 v57,v57,v45\n"
"v_mfma_f32_16x16x32_bf16 a[148:151],v[212:215],v[232:235],a[148:151]\n"
"buffer_load_dwordx4 v108,s[16:19],s76 offen offset:2048 lds\n"
"v_mfma_f32_16x16x32_bf16 a[156:159],v[212:215],v[236:239],a[156:159]\n"
"v_add_f32 v56,v56,v46\n"
"v_add_f32 v57,v57,v47\n"
"v_mfma_f32_16x16x32_bf16 a[156:159],v[148:151],v[172:175],a[156:159]\n"
"v_add_f32 v56,v56,v48\n"
"v_add_f32 v57,v57,v49\n"
"v_mfma_f32_16x16x32_bf16 a[164:167],v[148:151],v[176:179],a[164:167]\n"
"v_add_f32 v56,v56,v50\n"
"v_add_f32 v57,v57,v51\n"
"v_mfma_f32_16x16x32_bf16 a[164:167],v[212:215],v[240:243],a[164:167]\n"
"v_add_f32 v56,v56,v52\n"
"v_add_f32 v57,v57,v53\n"
"v_mfma_f32_16x16x32_bf16 a[172:175],v[212:215],v[244:247],a[172:175]\n"
"buffer_load_dwordx4 v109,s[16:19],s76 offen offset:3072 lds\n"
"v_mfma_f32_16x16x32_bf16 a[172:175],v[148:151],v[180:183],a[172:175]\n"
"s_mov_b32 s87,2097152\n"
"v_add_f32 v56,v56,v54\n"
"v_add_f32 v57,v57,v55\n"
"v_mfma_f32_16x16x32_bf16 a[180:183],v[148:151],v[184:187],a[180:183]\n"
"buffer_load_dword v115,v112,s[36:39],s87 offen\n"
"v_mfma_f32_16x16x32_bf16 a[180:183],v[212:215],v[248:251],a[180:183]\n"
"s_mov_b32 s87,2621440\n"
"v_add_f32 v56,v56,v57\n"
"v_mfma_f32_16x16x32_bf16 a[188:191],v[212:215],v[252:255],a[188:191]\n"
"buffer_load_dword v115,v112,s[36:39],s87 offen\n"
"v_add_f32_dpp v56,v56,v56 quad_perm:[1,0,3,2] row_mask:0xf bank_mask:0xf\n"
"v_mfma_f32_16x16x32_bf16 a[188:191],v[148:151],v[188:191],a[188:191]\n"
"s_barrier\n"
"v_mfma_f32_16x16x32_bf16 a[192:195],v[216:219],v[224:227],a[192:195]\n"
"ds_read_b128 v[152:155],v82 offset:12288\n"
"v_mfma_f32_16x16x32_bf16 a[196:199],v[220:223],v[224:227],a[196:199]\n"
"ds_read_b128 v[156:159],v82 offset:14336\n"
"v_add_f32_dpp v56,v56,v56 quad_perm:[2,3,0,1] row_mask:0xf bank_mask:0xf\n"
"v_mfma_f32_16x16x32_bf16 a[204:207],v[220:223],v[228:231],a[204:207]\n"
"ds_read_b64_tr_b16 v[160:161],v98 offset:49152\n"
"v_mfma_f32_16x16x32_bf16 a[200:203],v[216:219],v[228:231],a[200:203]\n"
"ds_read_b64_tr_b16 v[162:163],v98 offset:50176\n"
"v_add_f32_dpp v56,v56,v56 row_ror:8 row_mask:0xf bank_mask:0xf\n"
"v_mfma_f32_16x16x32_bf16 a[208:211],v[216:219],v[232:235],a[208:211]\n"
"ds_read_b64_tr_b16 v[164:165],v99 offset:49152\n"
"v_mov_b32 v57,v56\n"
"v_mfma_f32_16x16x32_bf16 a[212:215],v[220:223],v[232:235],a[212:215]\n"
"ds_read_b64_tr_b16 v[166:167],v99 offset:50176\n"
"v_mfma_f32_16x16x32_bf16 a[220:223],v[220:223],v[236:239],a[220:223]\n"
"ds_read_b64_tr_b16 v[168:169],v100 offset:49152\n"
"v_permlane16_swap_b32 v57,v56\n"
"v_mfma_f32_16x16x32_bf16 a[216:219],v[216:219],v[236:239],a[216:219]\n"
"ds_read_b64_tr_b16 v[170:171],v100 offset:50176\n"
"v_add_f32 v56,v56,v57\n"
"v_mfma_f32_16x16x32_bf16 a[224:227],v[216:219],v[240:243],a[224:227]\n"
"ds_read_b64_tr_b16 v[172:173],v101 offset:49152\n"
"v_mov_b32 v57,v56\n"
"v_mfma_f32_16x16x32_bf16 a[228:231],v[220:223],v[240:243],a[228:231]\n"
"ds_read_b64_tr_b16 v[174:175],v101 offset:50176\n"
"v_mfma_f32_16x16x32_bf16 a[236:239],v[220:223],v[244:247],a[236:239]\n"
"ds_read_b64_tr_b16 v[176:177],v102 offset:49152\n"
"v_permlane32_swap_b32 v57,v56\n"
"v_mfma_f32_16x16x32_bf16 a[232:235],v[216:219],v[244:247],a[232:235]\n"
"ds_read_b64_tr_b16 v[178:179],v102 offset:50176\n"
"v_add_f32 v56,v56,v57\n"
"v_mfma_f32_16x16x32_bf16 a[240:243],v[216:219],v[248:251],a[240:243]\n"
"ds_read_b64_tr_b16 v[180:181],v103 offset:49152\n"
"v_mul_f32 v58,0x3a800000,v56\n"
"v_mfma_f32_16x16x32_bf16 a[244:247],v[220:223],v[248:251],a[244:247]\n"
"ds_read_b64_tr_b16 v[182:183],v103 offset:50176\n"
"v_sub_f32 v62,v24,v58\n"
"v_mfma_f32_16x16x32_bf16 a[252:255],v[220:223],v[252:255],a[252:255]\n"
"ds_read_b64_tr_b16 v[184:185],v104 offset:49152\n"
"v_mul_f32 v59,v62,v62\n"
"v_mfma_f32_16x16x32_bf16 a[248:251],v[216:219],v[252:255],a[248:251]\n"
"ds_read_b64_tr_b16 v[186:187],v104 offset:50176\n"
"v_sub_f32 v63,v25,v58\n"
"s_waitcnt lgkmcnt(10)\n"
"v_mfma_f32_16x16x32_bf16 a[192:195],v[152:155],v[160:163],a[192:195]\n"
"ds_read_b64_tr_b16 v[188:189],v105 offset:49152\n"
"v_mul_f32 v60,v63,v63\n"
"v_mfma_f32_16x16x32_bf16 a[196:199],v[156:159],v[160:163],a[196:199]\n"
"ds_read_b64_tr_b16 v[190:191],v105 offset:50176\n"
"v_sub_f32 v64,v26,v58\n"
"v_mfma_f32_16x16x32_bf16 a[204:207],v[156:159],v[164:167],a[204:207]\n"
"ds_read_b128 v[128:131],v82 offset:0\n"
"v_fmac_f32 v59,v64,v64\n"
"v_mfma_f32_16x16x32_bf16 a[200:203],v[152:155],v[164:167],a[200:203]\n"
"ds_read_b128 v[192:195],v83 offset:0\n"
"s_waitcnt lgkmcnt(10)\n"
"v_mfma_f32_16x16x32_bf16 a[208:211],v[152:155],v[168:171],a[208:211]\n"
"ds_read_b64_tr_b16 v[224:225],v98 offset:32768\n"
"v_sub_f32 v65,v27,v58\n"
"v_mfma_f32_16x16x32_bf16 a[212:215],v[156:159],v[168:171],a[212:215]\n"
"ds_read_b64_tr_b16 v[226:227],v98 offset:33792\n"
"v_fmac_f32 v60,v65,v65\n"
"v_mfma_f32_16x16x32_bf16 a[220:223],v[156:159],v[172:175],a[220:223]\n"
"ds_read_b64_tr_b16 v[228:229],v99 offset:32768\n"
"v_sub_f32 v62,v28,v58\n"
"v_mfma_f32_16x16x32_bf16 a[216:219],v[152:155],v[172:175],a[216:219]\n"
"ds_read_b64_tr_b16 v[230:231],v99 offset:33792\n"
"s_waitcnt lgkmcnt(10)\n"
"v_mfma_f32_16x16x32_bf16 a[224:227],v[152:155],v[176:179],a[224:227]\n"
"ds_read_b64_tr_b16 v[232:233],v100 offset:32768\n"
"v_fmac_f32 v59,v62,v62\n"
"v_mfma_f32_16x16x32_bf16 a[228:231],v[156:159],v[176:179],a[228:231]\n"
"ds_read_b64_tr_b16 v[234:235],v100 offset:33792\n"
"v_sub_f32 v63,v29,v58\n"
"v_mfma_f32_16x16x32_bf16 a[236:239],v[156:159],v[180:183],a[236:239]\n"
"ds_read_b64_tr_b16 v[236:237],v101 offset:32768\n"
"v_fmac_f32 v60,v63,v63\n"
"v_mfma_f32_16x16x32_bf16 a[232:235],v[152:155],v[180:183],a[232:235]\n"
"ds_read_b64_tr_b16 v[238:239],v101 offset:33792\n"
"s_waitcnt lgkmcnt(10)\n"
"v_mfma_f32_16x16x32_bf16 a[240:243],v[152:155],v[184:187],a[240:243]\n"
"ds_read_b64_tr_b16 v[240:241],v102 offset:32768\n"
"v_sub_f32 v64,v30,v58\n"
"v_mfma_f32_16x16x32_bf16 a[244:247],v[156:159],v[184:187],a[244:247]\n"
"ds_read_b64_tr_b16 v[242:243],v102 offset:33792\n"
"v_fmac_f32 v59,v64,v64\n"
"v_mfma_f32_16x16x32_bf16 a[252:255],v[156:159],v[188:191],a[252:255]\n"
"ds_read_b64_tr_b16 v[244:245],v103 offset:32768\n"
"v_sub_f32 v65,v31,v58\n"
"v_mfma_f32_16x16x32_bf16 a[248:251],v[152:155],v[188:191],a[248:251]\n"
"ds_read_b64_tr_b16 v[246:247],v103 offset:33792\n"
"s_waitcnt lgkmcnt(8)\n"
"v_mfma_f32_16x16x32_bf16 a[0:3],v[128:131],v[160:163],a[0:3]\n"
"ds_read_b64_tr_b16 v[248:249],v104 offset:32768\n"
"v_fmac_f32 v60,v65,v65\n"
"v_mfma_f32_16x16x32_bf16 a[0:3],v[192:195],v[224:227],a[0:3]\n"
"ds_read_b64_tr_b16 v[250:251],v104 offset:33792\n"
"v_sub_f32 v62,v32,v58\n"
"v_mfma_f32_16x16x32_bf16 a[8:11],v[192:195],v[228:231],a[8:11]\n"
"ds_read_b64_tr_b16 v[252:253],v105 offset:32768\n"
"v_fmac_f32 v59,v62,v62\n"
"v_mfma_f32_16x16x32_bf16 a[8:11],v[128:131],v[164:167],a[8:11]\n"
"ds_read_b64_tr_b16 v[254:255],v105 offset:33792\n"
"s_waitcnt lgkmcnt(8)\n"
"v_mfma_f32_16x16x32_bf16 a[16:19],v[128:131],v[168:171],a[16:19]\n"
"ds_read_b128 v[132:135],v82 offset:2048\n"
"v_sub_f32 v63,v33,v58\n"
"v_mfma_f32_16x16x32_bf16 a[16:19],v[192:195],v[232:235],a[16:19]\n"
"ds_read_b128 v[196:199],v83 offset:2048\n"
"v_fmac_f32 v60,v63,v63\n"
"v_mfma_f32_16x16x32_bf16 a[24:27],v[192:195],v[236:239],a[24:27]\n"
"ds_read_b128 v[136:139],v82 offset:4096\n"
"v_sub_f32 v64,v34,v58\n"
"v_mfma_f32_16x16x32_bf16 a[24:27],v[128:131],v[172:175],a[24:27]\n"
"ds_read_b128 v[200:203],v83 offset:4096\n"
"s_waitcnt lgkmcnt(8)\n"
"v_mfma_f32_16x16x32_bf16 a[32:35],v[128:131],v[176:179],a[32:35]\n"
"ds_read_b128 v[140:143],v82 offset:6144\n"
"v_fmac_f32 v59,v64,v64\n"
"v_mfma_f32_16x16x32_bf16 a[32:35],v[192:195],v[240:243],a[32:35]\n"
"ds_read_b128 v[204:207],v83 offset:6144\n"
"v_sub_f32 v65,v35,v58\n"
"v_mfma_f32_16x16x32_bf16 a[40:43],v[192:195],v[244:247],a[40:43]\n"
"ds_read_b128 v[144:147],v82 offset:8192\n"
"v_fmac_f32 v60,v65,v65\n"
"v_mfma_f32_16x16x32_bf16 a[40:43],v[128:131],v[180:183],a[40:43]\n"
"ds_read_b128 v[208:211],v83 offset:8192\n"
"s_waitcnt lgkmcnt(8)\n"
"v_mfma_f32_16x16x32_bf16 a[48:51],v[128:131],v[184:187],a[48:51]\n"
"ds_read_b128 v[148:151],v82 offset:10240\n"
"v_sub_f32 v62,v36,v58\n"
"v_mfma_f32_16x16x32_bf16 a[48:51],v[192:195],v[248:251],a[48:51]\n"
"ds_read_b128 v[212:215],v83 offset:10240\n"
"v_fmac_f32 v59,v62,v62\n"
"v_mfma_f32_16x16x32_bf16 a[56:59],v[192:195],v[252:255],a[56:59]\n"
"ds_read_b128 v[216:219],v83 offset:12288\n"
"v_sub_f32 v63,v37,v58\n"
"v_mfma_f32_16x16x32_bf16 a[56:59],v[128:131],v[188:191],a[56:59]\n"
"ds_read_b128 v[220:223],v83 offset:14336\n"
"s_waitcnt lgkmcnt(10)\n"
"v_mfma_f32_16x16x32_bf16 a[4:7],v[132:135],v[160:163],a[4:7]\n"
"v_fmac_f32 v60,v63,v63\n"
"v_sub_f32 v64,v38,v58\n"
"v_mfma_f32_16x16x32_bf16 a[4:7],v[196:199],v[224:227],a[4:7]\n"
"v_fmac_f32 v59,v64,v64\n"
"v_sub_f32 v65,v39,v58\n"
"v_mfma_f32_16x16x32_bf16 a[12:15],v[196:199],v[228:231],a[12:15]\n"
"v_fmac_f32 v60,v65,v65\n"
"v_sub_f32 v62,v40,v58\n"
"v_mfma_f32_16x16x32_bf16 a[12:15],v[132:135],v[164:167],a[12:15]\n"
"v_fmac_f32 v59,v62,v62\n"
"v_sub_f32 v63,v41,v58\n"
"v_mfma_f32_16x16x32_bf16 a[20:23],v[132:135],v[168:171],a[20:23]\n"
"s_waitcnt lgkmcnt(0)\n"
"s_barrier\n"
"v_mfma_f32_16x16x32_bf16 a[20:23],v[196:199],v[232:235],a[20:23]\n"
"s_mov_b32 s77,2097152\n"
"v_fmac_f32 v60,v63,v63\n"
"s_add_u32 m0,s78,147456\n"
"v_mfma_f32_16x16x32_bf16 a[28:31],v[196:199],v[236:239],a[28:31]\n"
"buffer_load_dwordx4 v110,s[20:23],s77 offen offset:0 lds\n"
"v_mfma_f32_16x16x32_bf16 a[28:31],v[132:135],v[172:175],a[28:31]\n"
"v_sub_f32 v64,v42,v58\n"
"v_fmac_f32 v59,v64,v64\n"
"v_mfma_f32_16x16x32_bf16 a[36:39],v[132:135],v[176:179],a[36:39]\n"
"v_sub_f32 v65,v43,v58\n"
"v_fmac_f32 v60,v65,v65\n"
"v_mfma_f32_16x16x32_bf16 a[36:39],v[196:199],v[240:243],a[36:39]\n"
"v_sub_f32 v62,v44,v58\n"
"v_fmac_f32 v59,v62,v62\n"
"v_mfma_f32_16x16x32_bf16 a[44:47],v[196:199],v[244:247],a[44:47]\n"
"buffer_load_dwordx4 v111,s[20:23],s77 offen offset:1024 lds\n"
"v_mfma_f32_16x16x32_bf16 a[44:47],v[132:135],v[180:183],a[44:47]\n"
"v_sub_f32 v63,v45,v58\n"
"v_fmac_f32 v60,v63,v63\n"
"v_mfma_f32_16x16x32_bf16 a[52:55],v[132:135],v[184:187],a[52:55]\n"
"v_sub_f32 v64,v46,v58\n"
"v_fmac_f32 v59,v64,v64\n"
"v_mfma_f32_16x16x32_bf16 a[52:55],v[196:199],v[248:251],a[52:55]\n"
"v_sub_f32 v65,v47,v58\n"
"v_fmac_f32 v60,v65,v65\n"
"v_mfma_f32_16x16x32_bf16 a[60:63],v[196:199],v[252:255],a[60:63]\n"
"buffer_load_dwordx4 v113,s[20:23],s77 offen offset:2048 lds\n"
"v_mfma_f32_16x16x32_bf16 a[60:63],v[132:135],v[188:191],a[60:63]\n"
"v_sub_f32 v62,v48,v58\n"
"v_fmac_f32 v59,v62,v62\n"
"v_mfma_f32_16x16x32_bf16 a[64:67],v[136:139],v[160:163],a[64:67]\n"
"v_sub_f32 v63,v49,v58\n"
"v_fmac_f32 v60,v63,v63\n"
"v_mfma_f32_16x16x32_bf16 a[64:67],v[200:203],v[224:227],a[64:67]\n"
"v_sub_f32 v64,v50,v58\n"
"v_fmac_f32 v59,v64,v64\n"
"v_mfma_f32_16x16x32_bf16 a[72:75],v[200:203],v[228:231],a[72:75]\n"
"buffer_load_dwordx4 v114,s[20:23],s77 offen offset:3072 lds\n"
"v_mfma_f32_16x16x32_bf16 a[72:75],v[136:139],v[164:167],a[72:75]\n"
"v_sub_f32 v65,v51,v58\n"
"v_fmac_f32 v60,v65,v65\n"
"v_mfma_f32_16x16x32_bf16 a[80:83],v[136:139],v[168:171],a[80:83]\n"
"v_sub_f32 v62,v52,v58\n"
"v_fmac_f32 v59,v62,v62\n"
"v_mfma_f32_16x16x32_bf16 a[80:83],v[200:203],v[232:235],a[80:83]\n"
"s_mov_b32 s77,2359296\n"
"v_sub_f32 v63,v53,v58\n"
"s_add_u32 m0,s78,131072\n"
"v_mfma_f32_16x16x32_bf16 a[88:91],v[200:203],v[236:239],a[88:91]\n"
"buffer_load_dwordx4 v110,s[20:23],s77 offen offset:0 lds\n"
"v_mfma_f32_16x16x32_bf16 a[88:91],v[136:139],v[172:175],a[88:91]\n"
"v_fmac_f32 v60,v63,v63\n"
"v_sub_f32 v64,v54,v58\n"
"v_mfma_f32_16x16x32_bf16 a[96:99],v[136:139],v[176:179],a[96:99]\n"
"v_fmac_f32 v59,v64,v64\n"
"v_sub_f32 v65,v55,v58\n"
"v_mfma_f32_16x16x32_bf16 a[96:99],v[200:203],v[240:243],a[96:99]\n"
"v_fmac_f32 v60,v65,v65\n"
"v_add_f32 v59,v59,v60\n"
"v_mfma_f32_16x16x32_bf16 a[104:107],v[200:203],v[244:247],a[104:107]\n"
"buffer_load_dwordx4 v111,s[20:23],s77 offen offset:1024 lds\n"
"v_mfma_f32_16x16x32_bf16 a[104:107],v[136:139],v[180:183],a[104:107]\n"
"v_mfma_f32_16x16x32_bf16 a[112:115],v[136:139],v[184:187],a[112:115]\n"
"v_add_f32_dpp v59,v59,v59 quad_perm:[1,0,3,2] row_mask:0xf bank_mask:0xf\n"
"v_mfma_f32_16x16x32_bf16 a[112:115],v[200:203],v[248:251],a[112:115]\n"
"s_nop 0\n"
"v_add_f32_dpp v59,v59,v59 quad_perm:[2,3,0,1] row_mask:0xf bank_mask:0xf\n"
"v_mfma_f32_16x16x32_bf16 a[120:123],v[200:203],v[252:255],a[120:123]\n"
"buffer_load_dwordx4 v113,s[20:23],s77 offen offset:2048 lds\n"
"v_mfma_f32_16x16x32_bf16 a[120:123],v[136:139],v[188:191],a[120:123]\n"
"v_add_f32_dpp v59,v59,v59 row_ror:8 row_mask:0xf bank_mask:0xf\n"
"v_mov_b32 v57,v59\n"
"v_mfma_f32_16x16x32_bf16 a[68:71],v[140:143],v[160:163],a[68:71]\n"
"v_mfma_f32_16x16x32_bf16 a[68:71],v[204:207],v[224:227],a[68:71]\n"
"v_permlane16_swap_b32 v57,v59\n"
"v_add_f32 v59,v59,v57\n"
"v_mfma_f32_16x16x32_bf16 a[76:79],v[204:207],v[228:231],a[76:79]\n"
"buffer_load_dwordx4 v114,s[20:23],s77 offen offset:3072 lds\n"
"v_mfma_f32_16x16x32_bf16 a[76:79],v[140:143],v[164:167],a[76:79]\n"
"v_mov_b32 v57,v59\n"
"v_mfma_f32_16x16x32_bf16 a[84:87],v[140:143],v[168:171],a[84:87]\n"
"s_nop 0\n"
"v_permlane32_swap_b32 v57,v59\n"
"v_add_f32 v59,v59,v57\n"
"v_mfma_f32_16x16x32_bf16 a[84:87],v[204:207],v[232:235],a[84:87]\n"
"v_mul_f32 v59,0x3a800000,v59\n"
"v_cvt_pk_bf16_f32 v59,v59,v59\n"
"v_mfma_f32_16x16x32_bf16 a[92:95],v[204:207],v[236:239],a[92:95]\n"
"s_mov_b32 s76,640\n"
"v_lshlrev_b32 v59,16,v59\n"
"s_add_u32 m0,s79,65536\n"
"v_mfma_f32_16x16x32_bf16 a[92:95],v[140:143],v[172:175],a[92:95]\n"
"buffer_load_dwordx4 v106,s[16:19],s76 offen offset:0 lds\n"
"v_mfma_f32_16x16x32_bf16 a[100:103],v[140:143],v[176:179],a[100:103]\n"
"v_add_f32 v59,0x3727c5ac,v59\n"
"v_cvt_pk_bf16_f32 v59,v59,v59\n"
"v_mfma_f32_16x16x32_bf16 a[100:103],v[204:207],v[240:243],a[100:103]\n"
"v_lshlrev_b32 v59,16,v59\n"
"v_sqrt_f32 v59,v59\n"
"v_mfma_f32_16x16x32_bf16 a[108:111],v[204:207],v[244:247],a[108:111]\n"
"v_cvt_pk_bf16_f32 v58,v58,v58\n"
"v_lshlrev_b32 v58,16,v58\n"
"v_mfma_f32_16x16x32_bf16 a[108:111],v[140:143],v[180:183],a[108:111]\n"
"v_cvt_pk_bf16_f32 v59,v59,v59\n"
"v_lshlrev_b32 v59,16,v59\n"
"v_mfma_f32_16x16x32_bf16 a[116:119],v[140:143],v[184:187],a[116:119]\n"
"buffer_load_dwordx4 v107,s[16:19],s76 offen offset:1024 lds\n"
"v_mfma_f32_16x16x32_bf16 a[116:119],v[204:207],v[248:251],a[116:119]\n"
"v_rcp_f32 v66,v59\n"
"v_mfma_f32_16x16x32_bf16 a[124:127],v[204:207],v[252:255],a[124:127]\n"
"v_mul_f32 v66,0x3e0293ee,v66\n"
"v_mul_f32 v58,v58,v66\n"
"v_mfma_f32_16x16x32_bf16 a[124:127],v[140:143],v[188:191],a[124:127]\n"
"v_fma_f32 v24,v24,v66,-v58\n"
"v_fma_f32 v25,v25,v66,-v58\n"
"v_mfma_f32_16x16x32_bf16 a[128:131],v[144:147],v[160:163],a[128:131]\n"
"v_cvt_pk_bf16_f32 v8,v24,v25\n"
"v_fma_f32 v26,v26,v66,-v58\n"
"v_mfma_f32_16x16x32_bf16 a[128:131],v[208:211],v[224:227],a[128:131]\n"
"buffer_load_dwordx4 v108,s[16:19],s76 offen offset:2048 lds\n"
"v_mfma_f32_16x16x32_bf16 a[136:139],v[208:211],v[228:231],a[136:139]\n"
"v_fma_f32 v27,v27,v66,-v58\n"
"v_cvt_pk_bf16_f32 v9,v26,v27\n"
"v_mfma_f32_16x16x32_bf16 a[136:139],v[144:147],v[164:167],a[136:139]\n"
"v_fma_f32 v28,v28,v66,-v58\n"
"v_fma_f32 v29,v29,v66,-v58\n"
"v_mfma_f32_16x16x32_bf16 a[144:147],v[144:147],v[168:171],a[144:147]\n"
"v_cvt_pk_bf16_f32 v10,v28,v29\n"
"v_fma_f32 v30,v30,v66,-v58\n"
"v_mfma_f32_16x16x32_bf16 a[144:147],v[208:211],v[232:235],a[144:147]\n"
"v_fma_f32 v31,v31,v66,-v58\n"
"v_cvt_pk_bf16_f32 v11,v30,v31\n"
"v_mfma_f32_16x16x32_bf16 a[152:155],v[208:211],v[236:239],a[152:155]\n"
"buffer_load_dwordx4 v109,s[16:19],s76 offen offset:3072 lds\n"
"v_mfma_f32_16x16x32_bf16 a[152:155],v[144:147],v[172:175],a[152:155]\n"
"buffer_store_dwordx4 v[8:11],v71,s[24:27],s54 offen sc1\n"
"v_mfma_f32_16x16x32_bf16 a[160:163],v[144:147],v[176:179],a[160:163]\n"
"s_add_u32 s54,s54,131072\n"
"v_fma_f32 v32,v32,v66,-v58\n"
"v_fma_f32 v33,v33,v66,-v58\n"
"v_mfma_f32_16x16x32_bf16 a[160:163],v[208:211],v[240:243],a[160:163]\n"
"v_cvt_pk_bf16_f32 v12,v32,v33\n"
"v_fma_f32 v34,v34,v66,-v58\n"
"v_mfma_f32_16x16x32_bf16 a[168:171],v[208:211],v[244:247],a[168:171]\n"
"s_mov_b32 s76,262784\n"
"v_fma_f32 v35,v35,v66,-v58\n"
"s_add_u32 m0,s79,69632\n"
"v_mfma_f32_16x16x32_bf16 a[168:171],v[144:147],v[180:183],a[168:171]\n"
"buffer_load_dwordx4 v106,s[16:19],s76 offen offset:0 lds\n"
"v_mfma_f32_16x16x32_bf16 a[176:179],v[144:147],v[184:187],a[176:179]\n"
"v_cvt_pk_bf16_f32 v13,v34,v35\n"
"v_fma_f32 v36,v36,v66,-v58\n"
"v_mfma_f32_16x16x32_bf16 a[176:179],v[208:211],v[248:251],a[176:179]\n"
"v_fma_f32 v37,v37,v66,-v58\n"
"v_cvt_pk_bf16_f32 v14,v36,v37\n"
"v_mfma_f32_16x16x32_bf16 a[184:187],v[208:211],v[252:255],a[184:187]\n"
"v_fma_f32 v38,v38,v66,-v58\n"
"v_fma_f32 v39,v39,v66,-v58\n"
"v_cvt_pk_bf16_f32 v15,v38,v39\n"
"v_mfma_f32_16x16x32_bf16 a[184:187],v[144:147],v[188:191],a[184:187]\n"
"buffer_store_dwordx4 v[12:15],v71,s[24:27],s54 offen sc1\n"
"v_mfma_f32_16x16x32_bf16 a[132:135],v[148:151],v[160:163],a[132:135]\n"
"buffer_load_dwordx4 v107,s[16:19],s76 offen offset:1024 lds\n"
"v_mfma_f32_16x16x32_bf16 a[132:135],v[212:215],v[224:227],a[132:135]\n"
"s_add_u32 s54,s54,131072\n"
"v_fma_f32 v40,v40,v66,-v58\n"
"v_mfma_f32_16x16x32_bf16 a[140:143],v[212:215],v[228:231],a[140:143]\n"
"v_fma_f32 v41,v41,v66,-v58\n"
"v_cvt_pk_bf16_f32 v16,v40,v41\n"
"v_mfma_f32_16x16x32_bf16 a[140:143],v[148:151],v[164:167],a[140:143]\n"
"v_fma_f32 v42,v42,v66,-v58\n"
"v_fma_f32 v43,v43,v66,-v58\n"
"v_mfma_f32_16x16x32_bf16 a[148:151],v[148:151],v[168:171],a[148:151]\n"
"v_cvt_pk_bf16_f32 v17,v42,v43\n"
"v_fma_f32 v44,v44,v66,-v58\n"
"v_mfma_f32_16x16x32_bf16 a[148:151],v[212:215],v[232:235],a[148:151]\n"
"buffer_load_dwordx4 v108,s[16:19],s76 offen offset:2048 lds\n"
"v_mfma_f32_16x16x32_bf16 a[156:159],v[212:215],v[236:239],a[156:159]\n"
"v_fma_f32 v45,v45,v66,-v58\n"
"v_cvt_pk_bf16_f32 v18,v44,v45\n"
"v_mfma_f32_16x16x32_bf16 a[156:159],v[148:151],v[172:175],a[156:159]\n"
"v_fma_f32 v46,v46,v66,-v58\n"
"v_fma_f32 v47,v47,v66,-v58\n"
"v_cvt_pk_bf16_f32 v19,v46,v47\n"
"v_mfma_f32_16x16x32_bf16 a[164:167],v[148:151],v[176:179],a[164:167]\n"
"buffer_store_dwordx4 v[16:19],v71,s[24:27],s54 offen sc1\n"
"v_mfma_f32_16x16x32_bf16 a[164:167],v[212:215],v[240:243],a[164:167]\n"
"s_add_u32 s54,s54,131072\n"
"v_fma_f32 v48,v48,v66,-v58\n"
"v_mfma_f32_16x16x32_bf16 a[172:175],v[212:215],v[244:247],a[172:175]\n"
"buffer_load_dwordx4 v109,s[16:19],s76 offen offset:3072 lds\n"
"v_mfma_f32_16x16x32_bf16 a[172:175],v[148:151],v[180:183],a[172:175]\n"
"s_mov_b32 s87,0\n"
"v_fma_f32 v49,v49,v66,-v58\n"
"v_cvt_pk_bf16_f32 v20,v48,v49\n"
"v_mfma_f32_16x16x32_bf16 a[180:183],v[148:151],v[184:187],a[180:183]\n"
"buffer_load_dword v115,v112,s[40:43],s87 offen\n"
"v_mfma_f32_16x16x32_bf16 a[180:183],v[212:215],v[248:251],a[180:183]\n"
"s_mov_b32 s87,1048576\n"
"v_fma_f32 v50,v50,v66,-v58\n"
"v_fma_f32 v51,v51,v66,-v58\n"
"v_mfma_f32_16x16x32_bf16 a[188:191],v[212:215],v[252:255],a[188:191]\n"
"buffer_load_dword v115,v112,s[40:43],s87 offen\n"
"v_mfma_f32_16x16x32_bf16 a[188:191],v[148:151],v[188:191],a[188:191]\n"
"s_mov_b32 s87,2097152\n"
"s_waitcnt vmcnt(31)\n"
"s_barrier\n"
"v_mfma_f32_16x16x32_bf16 a[192:195],v[216:219],v[224:227],a[192:195]\n"
"ds_read_b128 v[152:155],v80 offset:12288\n"
"buffer_load_dword v115,v112,s[40:43],s87 offen\n"
"v_mfma_f32_16x16x32_bf16 a[196:199],v[220:223],v[224:227],a[196:199]\n"
"ds_read_b128 v[156:159],v80 offset:14336\n"
"v_cvt_pk_bf16_f32 v21,v50,v51\n"
"v_mfma_f32_16x16x32_bf16 a[204:207],v[220:223],v[228:231],a[204:207]\n"
"ds_read_b64_tr_b16 v[160:161],v98 offset:0\n"
"v_fma_f32 v52,v52,v66,-v58\n"
"v_mfma_f32_16x16x32_bf16 a[200:203],v[216:219],v[228:231],a[200:203]\n"
"ds_read_b64_tr_b16 v[162:163],v98 offset:1024\n"
"v_fma_f32 v53,v53,v66,-v58\n"
"v_mfma_f32_16x16x32_bf16 a[208:211],v[216:219],v[232:235],a[208:211]\n"
"ds_read_b64_tr_b16 v[164:165],v99 offset:0\n"
"v_cvt_pk_bf16_f32 v22,v52,v53\n"
"v_mfma_f32_16x16x32_bf16 a[212:215],v[220:223],v[232:235],a[212:215]\n"
"ds_read_b64_tr_b16 v[166:167],v99 offset:1024\n"
"v_fma_f32 v54,v54,v66,-v58\n"
"v_mfma_f32_16x16x32_bf16 a[220:223],v[220:223],v[236:239],a[220:223]\n"
"ds_read_b64_tr_b16 v[168:169],v100 offset:0\n"
"v_fma_f32 v55,v55,v66,-v58\n"
"v_mfma_f32_16x16x32_bf16 a[216:219],v[216:219],v[236:239],a[216:219]\n"
"ds_read_b64_tr_b16 v[170:171],v100 offset:1024\n"
"v_cvt_pk_bf16_f32 v23,v54,v55\n"
"v_mfma_f32_16x16x32_bf16 a[224:227],v[216:219],v[240:243],a[224:227]\n"
"ds_read_b64_tr_b16 v[172:173],v101 offset:0\n"
"buffer_store_dwordx4 v[20:23],v71,s[24:27],s54 offen sc1\n"
"v_mfma_f32_16x16x32_bf16 a[228:231],v[220:223],v[240:243],a[228:231]\n"
"ds_read_b64_tr_b16 v[174:175],v101 offset:1024\n"
"v_mfma_f32_16x16x32_bf16 a[236:239],v[220:223],v[244:247],a[236:239]\n"
"ds_read_b64_tr_b16 v[176:177],v102 offset:0\n"
"v_mfma_f32_16x16x32_bf16 a[232:235],v[216:219],v[244:247],a[232:235]\n"
"ds_read_b64_tr_b16 v[178:179],v102 offset:1024\n"
"v_mfma_f32_16x16x32_bf16 a[240:243],v[216:219],v[248:251],a[240:243]\n"
"ds_read_b64_tr_b16 v[180:181],v103 offset:0\n"
"v_mfma_f32_16x16x32_bf16 a[244:247],v[220:223],v[248:251],a[244:247]\n"
"ds_read_b64_tr_b16 v[182:183],v103 offset:1024\n"
"v_mfma_f32_16x16x32_bf16 a[252:255],v[220:223],v[252:255],a[252:255]\n"
"ds_read_b64_tr_b16 v[184:185],v104 offset:0\n"
"v_mfma_f32_16x16x32_bf16 a[248:251],v[216:219],v[252:255],a[248:251]\n"
"ds_read_b64_tr_b16 v[186:187],v104 offset:1024\n"
"s_waitcnt lgkmcnt(10)\n"
"v_mfma_f32_16x16x32_bf16 a[192:195],v[152:155],v[160:163],a[192:195]\n"
"ds_read_b64_tr_b16 v[188:189],v105 offset:0\n"
"v_mfma_f32_16x16x32_bf16 a[196:199],v[156:159],v[160:163],a[196:199]\n"
"ds_read_b64_tr_b16 v[190:191],v105 offset:1024\n"
"v_mfma_f32_16x16x32_bf16 a[204:207],v[156:159],v[164:167],a[204:207]\n"
"ds_read_b128 v[128:131],v80 offset:0\n"
"v_mfma_f32_16x16x32_bf16 a[200:203],v[152:155],v[164:167],a[200:203]\n"
"ds_read_b128 v[192:195],v81 offset:0\n"
"s_waitcnt lgkmcnt(10)\n"
"v_mfma_f32_16x16x32_bf16 a[208:211],v[152:155],v[168:171],a[208:211]\n"
"ds_read_b64_tr_b16 v[224:225],v98 offset:16384\n"
"v_mfma_f32_16x16x32_bf16 a[212:215],v[156:159],v[168:171],a[212:215]\n"
"ds_read_b64_tr_b16 v[226:227],v98 offset:17408\n"
"v_mfma_f32_16x16x32_bf16 a[220:223],v[156:159],v[172:175],a[220:223]\n"
"ds_read_b64_tr_b16 v[228:229],v99 offset:16384\n"
"v_mfma_f32_16x16x32_bf16 a[216:219],v[152:155],v[172:175],a[216:219]\n"
"ds_read_b64_tr_b16 v[230:231],v99 offset:17408\n"
"s_waitcnt lgkmcnt(10)\n"
"v_mfma_f32_16x16x32_bf16 a[224:227],v[152:155],v[176:179],a[224:227]\n"
"ds_read_b64_tr_b16 v[232:233],v100 offset:16384\n"
"v_mfma_f32_16x16x32_bf16 a[228:231],v[156:159],v[176:179],a[228:231]\n"
"ds_read_b64_tr_b16 v[234:235],v100 offset:17408\n"
"v_mfma_f32_16x16x32_bf16 a[236:239],v[156:159],v[180:183],a[236:239]\n"
"ds_read_b64_tr_b16 v[236:237],v101 offset:16384\n"
"v_mfma_f32_16x16x32_bf16 a[232:235],v[152:155],v[180:183],a[232:235]\n"
"ds_read_b64_tr_b16 v[238:239],v101 offset:17408\n"
"s_waitcnt lgkmcnt(10)\n"
"v_mfma_f32_16x16x32_bf16 a[240:243],v[152:155],v[184:187],a[240:243]\n"
"ds_read_b64_tr_b16 v[240:241],v102 offset:16384\n"
"v_mfma_f32_16x16x32_bf16 a[244:247],v[156:159],v[184:187],a[244:247]\n"
"ds_read_b64_tr_b16 v[242:243],v102 offset:17408\n"
"v_mfma_f32_16x16x32_bf16 a[252:255],v[156:159],v[188:191],a[252:255]\n"
"ds_read_b64_tr_b16 v[244:245],v103 offset:16384\n"
"v_mfma_f32_16x16x32_bf16 a[248:251],v[152:155],v[188:191],a[248:251]\n"
"ds_read_b64_tr_b16 v[246:247],v103 offset:17408\n"
"s_waitcnt lgkmcnt(8)\n"
"v_mfma_f32_16x16x32_bf16 a[0:3],v[128:131],v[160:163],a[0:3]\n"
"ds_read_b64_tr_b16 v[248:249],v104 offset:16384\n"
"v_mfma_f32_16x16x32_bf16 a[0:3],v[192:195],v[224:227],a[0:3]\n"
"ds_read_b64_tr_b16 v[250:251],v104 offset:17408\n"
"v_mfma_f32_16x16x32_bf16 a[8:11],v[192:195],v[228:231],a[8:11]\n"
"ds_read_b64_tr_b16 v[252:253],v105 offset:16384\n"
"v_mfma_f32_16x16x32_bf16 a[8:11],v[128:131],v[164:167],a[8:11]\n"
"ds_read_b64_tr_b16 v[254:255],v105 offset:17408\n"
"s_waitcnt lgkmcnt(8)\n"
"v_mfma_f32_16x16x32_bf16 a[16:19],v[128:131],v[168:171],a[16:19]\n"
"ds_read_b128 v[132:135],v80 offset:2048\n"
"v_mfma_f32_16x16x32_bf16 a[16:19],v[192:195],v[232:235],a[16:19]\n"
"ds_read_b128 v[196:199],v81 offset:2048\n"
"v_mfma_f32_16x16x32_bf16 a[24:27],v[192:195],v[236:239],a[24:27]\n"
"ds_read_b128 v[136:139],v80 offset:4096\n"
"v_mfma_f32_16x16x32_bf16 a[24:27],v[128:131],v[172:175],a[24:27]\n"
"ds_read_b128 v[200:203],v81 offset:4096\n"
"s_waitcnt lgkmcnt(8)\n"
"v_mfma_f32_16x16x32_bf16 a[32:35],v[128:131],v[176:179],a[32:35]\n"
"ds_read_b128 v[140:143],v80 offset:6144\n"
"v_mfma_f32_16x16x32_bf16 a[32:35],v[192:195],v[240:243],a[32:35]\n"
"ds_read_b128 v[204:207],v81 offset:6144\n"
"v_mfma_f32_16x16x32_bf16 a[40:43],v[192:195],v[244:247],a[40:43]\n"
"ds_read_b128 v[144:147],v80 offset:8192\n"
"v_mfma_f32_16x16x32_bf16 a[40:43],v[128:131],v[180:183],a[40:43]\n"
"ds_read_b128 v[208:211],v81 offset:8192\n"
"s_waitcnt lgkmcnt(8)\n"
"v_mfma_f32_16x16x32_bf16 a[48:51],v[128:131],v[184:187],a[48:51]\n"
"ds_read_b128 v[148:151],v80 offset:10240\n"
"v_mfma_f32_16x16x32_bf16 a[48:51],v[192:195],v[248:251],a[48:51]\n"
"ds_read_b128 v[212:215],v81 offset:10240\n"
"v_mfma_f32_16x16x32_bf16 a[56:59],v[192:195],v[252:255],a[56:59]\n"
"ds_read_b128 v[216:219],v81 offset:12288\n"
"v_mfma_f32_16x16x32_bf16 a[56:59],v[128:131],v[188:191],a[56:59]\n"
"ds_read_b128 v[220:223],v81 offset:14336\n"
"s_waitcnt lgkmcnt(10)\n"
"v_mfma_f32_16x16x32_bf16 a[4:7],v[132:135],v[160:163],a[4:7]\n"
"v_mfma_f32_16x16x32_bf16 a[4:7],v[196:199],v[224:227],a[4:7]\n"
"v_mfma_f32_16x16x32_bf16 a[12:15],v[196:199],v[228:231],a[12:15]\n"
"v_mfma_f32_16x16x32_bf16 a[12:15],v[132:135],v[164:167],a[12:15]\n"
"v_mfma_f32_16x16x32_bf16 a[20:23],v[132:135],v[168:171],a[20:23]\n"
"s_waitcnt lgkmcnt(0)\n"
"s_barrier\n"
"v_mfma_f32_16x16x32_bf16 a[20:23],v[196:199],v[232:235],a[20:23]\n"
"s_mov_b32 s77,2621440\n"
"s_add_u32 m0,s78,98304\n"
"v_mfma_f32_16x16x32_bf16 a[28:31],v[196:199],v[236:239],a[28:31]\n"
"buffer_load_dwordx4 v110,s[20:23],s77 offen offset:0 lds\n"
"v_mfma_f32_16x16x32_bf16 a[28:31],v[132:135],v[172:175],a[28:31]\n"
"v_mfma_f32_16x16x32_bf16 a[36:39],v[132:135],v[176:179],a[36:39]\n"
"v_mfma_f32_16x16x32_bf16 a[36:39],v[196:199],v[240:243],a[36:39]\n"
"v_mfma_f32_16x16x32_bf16 a[44:47],v[196:199],v[244:247],a[44:47]\n"
"buffer_load_dwordx4 v111,s[20:23],s77 offen offset:1024 lds\n"
"v_mfma_f32_16x16x32_bf16 a[44:47],v[132:135],v[180:183],a[44:47]\n"
"v_mfma_f32_16x16x32_bf16 a[52:55],v[132:135],v[184:187],a[52:55]\n"
"v_mfma_f32_16x16x32_bf16 a[52:55],v[196:199],v[248:251],a[52:55]\n"
"v_mfma_f32_16x16x32_bf16 a[60:63],v[196:199],v[252:255],a[60:63]\n"
"buffer_load_dwordx4 v113,s[20:23],s77 offen offset:2048 lds\n"
"v_mfma_f32_16x16x32_bf16 a[60:63],v[132:135],v[188:191],a[60:63]\n"
"v_mfma_f32_16x16x32_bf16 a[64:67],v[136:139],v[160:163],a[64:67]\n"
"v_mfma_f32_16x16x32_bf16 a[64:67],v[200:203],v[224:227],a[64:67]\n"
"v_mfma_f32_16x16x32_bf16 a[72:75],v[200:203],v[228:231],a[72:75]\n"
"buffer_load_dwordx4 v114,s[20:23],s77 offen offset:3072 lds\n"
"v_mfma_f32_16x16x32_bf16 a[72:75],v[136:139],v[164:167],a[72:75]\n"
"v_mfma_f32_16x16x32_bf16 a[80:83],v[136:139],v[168:171],a[80:83]\n"
"v_mfma_f32_16x16x32_bf16 a[80:83],v[200:203],v[232:235],a[80:83]\n"
"s_mov_b32 s77,2883584\n"
"s_add_u32 m0,s78,114688\n"
"v_mfma_f32_16x16x32_bf16 a[88:91],v[200:203],v[236:239],a[88:91]\n"
"buffer_load_dwordx4 v110,s[20:23],s77 offen offset:0 lds\n"
"v_mfma_f32_16x16x32_bf16 a[88:91],v[136:139],v[172:175],a[88:91]\n"
"v_mfma_f32_16x16x32_bf16 a[96:99],v[136:139],v[176:179],a[96:99]\n"
"v_mfma_f32_16x16x32_bf16 a[96:99],v[200:203],v[240:243],a[96:99]\n"
"v_mfma_f32_16x16x32_bf16 a[104:107],v[200:203],v[244:247],a[104:107]\n"
"buffer_load_dwordx4 v111,s[20:23],s77 offen offset:1024 lds\n"
"v_mfma_f32_16x16x32_bf16 a[104:107],v[136:139],v[180:183],a[104:107]\n"
"v_mfma_f32_16x16x32_bf16 a[112:115],v[136:139],v[184:187],a[112:115]\n"
"v_mfma_f32_16x16x32_bf16 a[112:115],v[200:203],v[248:251],a[112:115]\n"
"v_mfma_f32_16x16x32_bf16 a[120:123],v[200:203],v[252:255],a[120:123]\n"
"buffer_load_dwordx4 v113,s[20:23],s77 offen offset:2048 lds\n"
"v_mfma_f32_16x16x32_bf16 a[120:123],v[136:139],v[188:191],a[120:123]\n"
"v_mfma_f32_16x16x32_bf16 a[68:71],v[140:143],v[160:163],a[68:71]\n"
"v_mfma_f32_16x16x32_bf16 a[68:71],v[204:207],v[224:227],a[68:71]\n"
"v_mfma_f32_16x16x32_bf16 a[76:79],v[204:207],v[228:231],a[76:79]\n"
"buffer_load_dwordx4 v114,s[20:23],s77 offen offset:3072 lds\n"
"v_mfma_f32_16x16x32_bf16 a[76:79],v[140:143],v[164:167],a[76:79]\n"
"v_mfma_f32_16x16x32_bf16 a[84:87],v[140:143],v[168:171],a[84:87]\n"
"v_mfma_f32_16x16x32_bf16 a[84:87],v[204:207],v[232:235],a[84:87]\n"
"v_mfma_f32_16x16x32_bf16 a[92:95],v[204:207],v[236:239],a[92:95]\n"
"s_mov_b32 s76,768\n"
"s_add_u32 m0,s79,0\n"
"v_mfma_f32_16x16x32_bf16 a[92:95],v[140:143],v[172:175],a[92:95]\n"
"buffer_load_dwordx4 v106,s[16:19],s76 offen offset:0 lds\n"
"v_mfma_f32_16x16x32_bf16 a[100:103],v[140:143],v[176:179],a[100:103]\n"
"v_mfma_f32_16x16x32_bf16 a[100:103],v[204:207],v[240:243],a[100:103]\n"
"v_mfma_f32_16x16x32_bf16 a[108:111],v[204:207],v[244:247],a[108:111]\n"
"v_mfma_f32_16x16x32_bf16 a[108:111],v[140:143],v[180:183],a[108:111]\n"
"v_mfma_f32_16x16x32_bf16 a[116:119],v[140:143],v[184:187],a[116:119]\n"
"buffer_load_dwordx4 v107,s[16:19],s76 offen offset:1024 lds\n"
"v_mfma_f32_16x16x32_bf16 a[116:119],v[204:207],v[248:251],a[116:119]\n"
"v_mfma_f32_16x16x32_bf16 a[124:127],v[204:207],v[252:255],a[124:127]\n"
"v_mfma_f32_16x16x32_bf16 a[124:127],v[140:143],v[188:191],a[124:127]\n"
"v_mfma_f32_16x16x32_bf16 a[128:131],v[144:147],v[160:163],a[128:131]\n"
"v_mfma_f32_16x16x32_bf16 a[128:131],v[208:211],v[224:227],a[128:131]\n"
"buffer_load_dwordx4 v108,s[16:19],s76 offen offset:2048 lds\n"
"v_mfma_f32_16x16x32_bf16 a[136:139],v[208:211],v[228:231],a[136:139]\n"
"v_mfma_f32_16x16x32_bf16 a[136:139],v[144:147],v[164:167],a[136:139]\n"
"v_mfma_f32_16x16x32_bf16 a[144:147],v[144:147],v[168:171],a[144:147]\n"
"v_mfma_f32_16x16x32_bf16 a[144:147],v[208:211],v[232:235],a[144:147]\n"
"v_mfma_f32_16x16x32_bf16 a[152:155],v[208:211],v[236:239],a[152:155]\n"
"buffer_load_dwordx4 v109,s[16:19],s76 offen offset:3072 lds\n"
"v_mfma_f32_16x16x32_bf16 a[152:155],v[144:147],v[172:175],a[152:155]\n"
"v_mfma_f32_16x16x32_bf16 a[160:163],v[144:147],v[176:179],a[160:163]\n"
"v_mfma_f32_16x16x32_bf16 a[160:163],v[208:211],v[240:243],a[160:163]\n"
"v_mfma_f32_16x16x32_bf16 a[168:171],v[208:211],v[244:247],a[168:171]\n"
"s_mov_b32 s76,262912\n"
"s_add_u32 m0,s79,4096\n"
"v_mfma_f32_16x16x32_bf16 a[168:171],v[144:147],v[180:183],a[168:171]\n"
"buffer_load_dwordx4 v106,s[16:19],s76 offen offset:0 lds\n"
"v_mfma_f32_16x16x32_bf16 a[176:179],v[144:147],v[184:187],a[176:179]\n"
"v_mfma_f32_16x16x32_bf16 a[176:179],v[208:211],v[248:251],a[176:179]\n"
"v_mfma_f32_16x16x32_bf16 a[184:187],v[208:211],v[252:255],a[184:187]\n"
"v_mfma_f32_16x16x32_bf16 a[184:187],v[144:147],v[188:191],a[184:187]\n"
"v_mfma_f32_16x16x32_bf16 a[132:135],v[148:151],v[160:163],a[132:135]\n"
"buffer_load_dwordx4 v107,s[16:19],s76 offen offset:1024 lds\n"
"v_mfma_f32_16x16x32_bf16 a[132:135],v[212:215],v[224:227],a[132:135]\n"
"v_mfma_f32_16x16x32_bf16 a[140:143],v[212:215],v[228:231],a[140:143]\n"
"v_mfma_f32_16x16x32_bf16 a[140:143],v[148:151],v[164:167],a[140:143]\n"
"v_mfma_f32_16x16x32_bf16 a[148:151],v[148:151],v[168:171],a[148:151]\n"
"v_mfma_f32_16x16x32_bf16 a[148:151],v[212:215],v[232:235],a[148:151]\n"
"buffer_load_dwordx4 v108,s[16:19],s76 offen offset:2048 lds\n"
"v_mfma_f32_16x16x32_bf16 a[156:159],v[212:215],v[236:239],a[156:159]\n"
"v_mfma_f32_16x16x32_bf16 a[156:159],v[148:151],v[172:175],a[156:159]\n"
"v_mfma_f32_16x16x32_bf16 a[164:167],v[148:151],v[176:179],a[164:167]\n"
"v_mfma_f32_16x16x32_bf16 a[164:167],v[212:215],v[240:243],a[164:167]\n"
"v_mfma_f32_16x16x32_bf16 a[172:175],v[212:215],v[244:247],a[172:175]\n"
"buffer_load_dwordx4 v109,s[16:19],s76 offen offset:3072 lds\n"
"v_mfma_f32_16x16x32_bf16 a[172:175],v[148:151],v[180:183],a[172:175]\n"
"s_mov_b32 s87,3145728\n"
"v_mfma_f32_16x16x32_bf16 a[180:183],v[148:151],v[184:187],a[180:183]\n"
"buffer_load_dword v115,v112,s[40:43],s87 offen\n"
"v_mfma_f32_16x16x32_bf16 a[180:183],v[212:215],v[248:251],a[180:183]\n"
"s_mov_b32 s87,4194304\n"
"v_mfma_f32_16x16x32_bf16 a[188:191],v[212:215],v[252:255],a[188:191]\n"
"buffer_load_dword v115,v112,s[40:43],s87 offen\n"
"v_mfma_f32_16x16x32_bf16 a[188:191],v[148:151],v[188:191],a[188:191]\n"
"s_mov_b32 s87,5242880\n"
"s_waitcnt vmcnt(33)\n"
"s_barrier\n"
"v_mfma_f32_16x16x32_bf16 a[192:195],v[216:219],v[224:227],a[192:195]\n"
"ds_read_b128 v[152:155],v80 offset:45056\n"
"buffer_load_dword v115,v112,s[40:43],s87 offen\n"
"v_mfma_f32_16x16x32_bf16 a[196:199],v[220:223],v[224:227],a[196:199]\n"
"ds_read_b128 v[156:159],v80 offset:47104\n"
"v_mfma_f32_16x16x32_bf16 a[204:207],v[220:223],v[228:231],a[204:207]\n"
"ds_read_b64_tr_b16 v[160:161],v98 offset:49152\n"
"v_mfma_f32_16x16x32_bf16 a[200:203],v[216:219],v[228:231],a[200:203]\n"
"ds_read_b64_tr_b16 v[162:163],v98 offset:50176\n"
"v_mfma_f32_16x16x32_bf16 a[208:211],v[216:219],v[232:235],a[208:211]\n"
"ds_read_b64_tr_b16 v[164:165],v99 offset:49152\n"
"v_mfma_f32_16x16x32_bf16 a[212:215],v[220:223],v[232:235],a[212:215]\n"
"ds_read_b64_tr_b16 v[166:167],v99 offset:50176\n"
"v_mfma_f32_16x16x32_bf16 a[220:223],v[220:223],v[236:239],a[220:223]\n"
"ds_read_b64_tr_b16 v[168:169],v100 offset:49152\n"
"v_mfma_f32_16x16x32_bf16 a[216:219],v[216:219],v[236:239],a[216:219]\n"
"ds_read_b64_tr_b16 v[170:171],v100 offset:50176\n"
"v_mfma_f32_16x16x32_bf16 a[224:227],v[216:219],v[240:243],a[224:227]\n"
"ds_read_b64_tr_b16 v[172:173],v101 offset:49152\n"
"v_mfma_f32_16x16x32_bf16 a[228:231],v[220:223],v[240:243],a[228:231]\n"
"ds_read_b64_tr_b16 v[174:175],v101 offset:50176\n"
"v_mfma_f32_16x16x32_bf16 a[236:239],v[220:223],v[244:247],a[236:239]\n"
"ds_read_b64_tr_b16 v[176:177],v102 offset:49152\n"
"v_mfma_f32_16x16x32_bf16 a[232:235],v[216:219],v[244:247],a[232:235]\n"
"ds_read_b64_tr_b16 v[178:179],v102 offset:50176\n"
"v_mfma_f32_16x16x32_bf16 a[240:243],v[216:219],v[248:251],a[240:243]\n"
"ds_read_b64_tr_b16 v[180:181],v103 offset:49152\n"
"v_mfma_f32_16x16x32_bf16 a[244:247],v[220:223],v[248:251],a[244:247]\n"
"ds_read_b64_tr_b16 v[182:183],v103 offset:50176\n"
"v_mfma_f32_16x16x32_bf16 a[252:255],v[220:223],v[252:255],a[252:255]\n"
"ds_read_b64_tr_b16 v[184:185],v104 offset:49152\n"
"ds_read_b64_tr_b16 v[186:187],v104 offset:50176\n"
"v_mfma_f32_16x16x32_bf16 a[248:251],v[216:219],v[252:255],a[248:251]\n"
"s_waitcnt vmcnt(19)\n"
"s_waitcnt lgkmcnt(10)\n"
"v_mfma_f32_16x16x32_bf16 a[192:195],v[152:155],v[160:163],a[192:195]\n"
"ds_read_b64_tr_b16 v[188:189],v105 offset:49152\n"
"ds_read_b64_tr_b16 v[190:191],v105 offset:50176\n"
"v_mfma_f32_16x16x32_bf16 a[196:199],v[156:159],v[160:163],a[196:199]\n"
"buffer_store_dwordx2 v[124:125],off,s[24:27],s57 sc1\n"
"v_mfma_f32_16x16x32_bf16 a[204:207],v[156:159],v[164:167],a[204:207]\n"
"ds_read_b128 v[128:131],v80 offset:32768\n"
"v_mfma_f32_16x16x32_bf16 a[200:203],v[152:155],v[164:167],a[200:203]\n"
"ds_read_b128 v[192:195],v81 offset:32768\n"
"s_waitcnt lgkmcnt(10)\n"
"v_mfma_f32_16x16x32_bf16 a[208:211],v[152:155],v[168:171],a[208:211]\n"
"ds_read_b64_tr_b16 v[224:225],v98 offset:32768\n"
"v_mfma_f32_16x16x32_bf16 a[212:215],v[156:159],v[168:171],a[212:215]\n"
"ds_read_b64_tr_b16 v[226:227],v98 offset:33792\n"
"v_mfma_f32_16x16x32_bf16 a[220:223],v[156:159],v[172:175],a[220:223]\n"
"ds_read_b64_tr_b16 v[228:229],v99 offset:32768\n"
"v_mfma_f32_16x16x32_bf16 a[216:219],v[152:155],v[172:175],a[216:219]\n"
"ds_read_b64_tr_b16 v[230:231],v99 offset:33792\n"
"s_waitcnt lgkmcnt(10)\n"
"v_mfma_f32_16x16x32_bf16 a[224:227],v[152:155],v[176:179],a[224:227]\n"
"ds_read_b64_tr_b16 v[232:233],v100 offset:32768\n"
"v_mfma_f32_16x16x32_bf16 a[228:231],v[156:159],v[176:179],a[228:231]\n"
"ds_read_b64_tr_b16 v[234:235],v100 offset:33792\n"
"v_mfma_f32_16x16x32_bf16 a[236:239],v[156:159],v[180:183],a[236:239]\n"
"ds_read_b64_tr_b16 v[236:237],v101 offset:32768\n"
"v_mfma_f32_16x16x32_bf16 a[232:235],v[152:155],v[180:183],a[232:235]\n"
"ds_read_b64_tr_b16 v[238:239],v101 offset:33792\n"
"s_waitcnt lgkmcnt(10)\n"
"v_mfma_f32_16x16x32_bf16 a[240:243],v[152:155],v[184:187],a[240:243]\n"
"ds_read_b64_tr_b16 v[240:241],v102 offset:32768\n"
"v_mfma_f32_16x16x32_bf16 a[244:247],v[156:159],v[184:187],a[244:247]\n"
"ds_read_b64_tr_b16 v[242:243],v102 offset:33792\n"
"v_mfma_f32_16x16x32_bf16 a[252:255],v[156:159],v[188:191],a[252:255]\n"
"ds_read_b64_tr_b16 v[244:245],v103 offset:32768\n"
"v_mfma_f32_16x16x32_bf16 a[248:251],v[152:155],v[188:191],a[248:251]\n"
"ds_read_b64_tr_b16 v[246:247],v103 offset:33792\n"
"s_waitcnt lgkmcnt(8)\n"
"v_mfma_f32_16x16x32_bf16 a[0:3],v[128:131],v[160:163],a[0:3]\n"
"ds_read_b64_tr_b16 v[248:249],v104 offset:32768\n"
"v_mfma_f32_16x16x32_bf16 a[0:3],v[192:195],v[224:227],a[0:3]\n"
"ds_read_b64_tr_b16 v[250:251],v104 offset:33792\n"
"v_mfma_f32_16x16x32_bf16 a[8:11],v[192:195],v[228:231],a[8:11]\n"
"ds_read_b64_tr_b16 v[252:253],v105 offset:32768\n"
"v_mfma_f32_16x16x32_bf16 a[8:11],v[128:131],v[164:167],a[8:11]\n"
"ds_read_b64_tr_b16 v[254:255],v105 offset:33792\n"
"s_waitcnt lgkmcnt(8)\n"
"v_mfma_f32_16x16x32_bf16 a[16:19],v[128:131],v[168:171],a[16:19]\n"
"ds_read_b128 v[132:135],v80 offset:34816\n"
"v_mfma_f32_16x16x32_bf16 a[16:19],v[192:195],v[232:235],a[16:19]\n"
"ds_read_b128 v[196:199],v81 offset:34816\n"
"v_mfma_f32_16x16x32_bf16 a[24:27],v[192:195],v[236:239],a[24:27]\n"
"ds_read_b128 v[136:139],v80 offset:36864\n"
"v_mfma_f32_16x16x32_bf16 a[24:27],v[128:131],v[172:175],a[24:27]\n"
"ds_read_b128 v[200:203],v81 offset:36864\n"
"s_waitcnt lgkmcnt(8)\n"
"v_mfma_f32_16x16x32_bf16 a[32:35],v[128:131],v[176:179],a[32:35]\n"
"ds_read_b128 v[140:143],v80 offset:38912\n"
"v_mfma_f32_16x16x32_bf16 a[32:35],v[192:195],v[240:243],a[32:35]\n"
"ds_read_b128 v[204:207],v81 offset:38912\n"
"v_mfma_f32_16x16x32_bf16 a[40:43],v[192:195],v[244:247],a[40:43]\n"
"ds_read_b128 v[144:147],v80 offset:40960\n"
"v_mfma_f32_16x16x32_bf16 a[40:43],v[128:131],v[180:183],a[40:43]\n"
"ds_read_b128 v[208:211],v81 offset:40960\n"
"s_waitcnt lgkmcnt(8)\n"
"v_mfma_f32_16x16x32_bf16 a[48:51],v[128:131],v[184:187],a[48:51]\n"
"ds_read_b128 v[148:151],v80 offset:43008\n"
"v_mfma_f32_16x16x32_bf16 a[48:51],v[192:195],v[248:251],a[48:51]\n"
"ds_read_b128 v[212:215],v81 offset:43008\n"
"v_mfma_f32_16x16x32_bf16 a[56:59],v[192:195],v[252:255],a[56:59]\n"
"ds_read_b128 v[216:219],v81 offset:45056\n"
"v_mfma_f32_16x16x32_bf16 a[56:59],v[128:131],v[188:191],a[56:59]\n"
"ds_read_b128 v[220:223],v81 offset:47104\n"
"s_waitcnt lgkmcnt(10)\n"
"v_mfma_f32_16x16x32_bf16 a[4:7],v[132:135],v[160:163],a[4:7]\n"
"v_mfma_f32_16x16x32_bf16 a[4:7],v[196:199],v[224:227],a[4:7]\n"
"v_mfma_f32_16x16x32_bf16 a[12:15],v[196:199],v[228:231],a[12:15]\n"
"v_mfma_f32_16x16x32_bf16 a[12:15],v[132:135],v[164:167],a[12:15]\n"
"v_mfma_f32_16x16x32_bf16 a[20:23],v[132:135],v[168:171],a[20:23]\n"
"s_waitcnt lgkmcnt(0)\n"
"s_barrier\n"
"v_mfma_f32_16x16x32_bf16 a[20:23],v[196:199],v[232:235],a[20:23]\n"
"s_mov_b32 s77,3145728\n"
"s_add_u32 m0,s78,147456\n"
"v_mfma_f32_16x16x32_bf16 a[28:31],v[196:199],v[236:239],a[28:31]\n"
"buffer_load_dwordx4 v110,s[20:23],s77 offen offset:0 lds\n"
"v_mfma_f32_16x16x32_bf16 a[28:31],v[132:135],v[172:175],a[28:31]\n"
"v_mfma_f32_16x16x32_bf16 a[36:39],v[132:135],v[176:179],a[36:39]\n"
"v_mfma_f32_16x16x32_bf16 a[36:39],v[196:199],v[240:243],a[36:39]\n"
"v_mfma_f32_16x16x32_bf16 a[44:47],v[196:199],v[244:247],a[44:47]\n"
"buffer_load_dwordx4 v111,s[20:23],s77 offen offset:1024 lds\n"
"v_mfma_f32_16x16x32_bf16 a[44:47],v[132:135],v[180:183],a[44:47]\n"
"v_mfma_f32_16x16x32_bf16 a[52:55],v[132:135],v[184:187],a[52:55]\n"
"v_mfma_f32_16x16x32_bf16 a[52:55],v[196:199],v[248:251],a[52:55]\n"
"v_mfma_f32_16x16x32_bf16 a[60:63],v[196:199],v[252:255],a[60:63]\n"
"buffer_load_dwordx4 v113,s[20:23],s77 offen offset:2048 lds\n"
"v_mfma_f32_16x16x32_bf16 a[60:63],v[132:135],v[188:191],a[60:63]\n"
"v_mfma_f32_16x16x32_bf16 a[64:67],v[136:139],v[160:163],a[64:67]\n"
"v_mfma_f32_16x16x32_bf16 a[64:67],v[200:203],v[224:227],a[64:67]\n"
"v_mfma_f32_16x16x32_bf16 a[72:75],v[200:203],v[228:231],a[72:75]\n"
"buffer_load_dwordx4 v114,s[20:23],s77 offen offset:3072 lds\n"
"v_mfma_f32_16x16x32_bf16 a[72:75],v[136:139],v[164:167],a[72:75]\n"
"v_mfma_f32_16x16x32_bf16 a[80:83],v[136:139],v[168:171],a[80:83]\n"
"v_mfma_f32_16x16x32_bf16 a[80:83],v[200:203],v[232:235],a[80:83]\n"
"s_mov_b32 s77,3407872\n"
"s_add_u32 m0,s78,131072\n"
"v_mfma_f32_16x16x32_bf16 a[88:91],v[200:203],v[236:239],a[88:91]\n"
"buffer_load_dwordx4 v110,s[20:23],s77 offen offset:0 lds\n"
"v_mfma_f32_16x16x32_bf16 a[88:91],v[136:139],v[172:175],a[88:91]\n"
"v_mfma_f32_16x16x32_bf16 a[96:99],v[136:139],v[176:179],a[96:99]\n"
"v_mfma_f32_16x16x32_bf16 a[96:99],v[200:203],v[240:243],a[96:99]\n"
"v_mfma_f32_16x16x32_bf16 a[104:107],v[200:203],v[244:247],a[104:107]\n"
"buffer_load_dwordx4 v111,s[20:23],s77 offen offset:1024 lds\n"
"v_mfma_f32_16x16x32_bf16 a[104:107],v[136:139],v[180:183],a[104:107]\n"
"v_mfma_f32_16x16x32_bf16 a[112:115],v[136:139],v[184:187],a[112:115]\n"
"v_mfma_f32_16x16x32_bf16 a[112:115],v[200:203],v[248:251],a[112:115]\n"
"v_mfma_f32_16x16x32_bf16 a[120:123],v[200:203],v[252:255],a[120:123]\n"
"buffer_load_dwordx4 v113,s[20:23],s77 offen offset:2048 lds\n"
"v_mfma_f32_16x16x32_bf16 a[120:123],v[136:139],v[188:191],a[120:123]\n"
"v_mfma_f32_16x16x32_bf16 a[68:71],v[140:143],v[160:163],a[68:71]\n"
"v_mfma_f32_16x16x32_bf16 a[68:71],v[204:207],v[224:227],a[68:71]\n"
"v_mfma_f32_16x16x32_bf16 a[76:79],v[204:207],v[228:231],a[76:79]\n"
"buffer_load_dwordx4 v114,s[20:23],s77 offen offset:3072 lds\n"
"v_mfma_f32_16x16x32_bf16 a[76:79],v[140:143],v[164:167],a[76:79]\n"
"v_mfma_f32_16x16x32_bf16 a[84:87],v[140:143],v[168:171],a[84:87]\n"
"v_mfma_f32_16x16x32_bf16 a[84:87],v[204:207],v[232:235],a[84:87]\n"
"v_mfma_f32_16x16x32_bf16 a[92:95],v[204:207],v[236:239],a[92:95]\n"
"s_mov_b32 s76,896\n"
"s_add_u32 m0,s79,32768\n"
"v_mfma_f32_16x16x32_bf16 a[92:95],v[140:143],v[172:175],a[92:95]\n"
"buffer_load_dwordx4 v106,s[16:19],s76 offen offset:0 lds\n"
"v_mfma_f32_16x16x32_bf16 a[100:103],v[140:143],v[176:179],a[100:103]\n"
"v_mfma_f32_16x16x32_bf16 a[100:103],v[204:207],v[240:243],a[100:103]\n"
"v_mfma_f32_16x16x32_bf16 a[108:111],v[204:207],v[244:247],a[108:111]\n"
"v_mfma_f32_16x16x32_bf16 a[108:111],v[140:143],v[180:183],a[108:111]\n"
"v_mfma_f32_16x16x32_bf16 a[116:119],v[140:143],v[184:187],a[116:119]\n"
"buffer_load_dwordx4 v107,s[16:19],s76 offen offset:1024 lds\n"
"v_mfma_f32_16x16x32_bf16 a[116:119],v[204:207],v[248:251],a[116:119]\n"
"v_mfma_f32_16x16x32_bf16 a[124:127],v[204:207],v[252:255],a[124:127]\n"
"v_mfma_f32_16x16x32_bf16 a[124:127],v[140:143],v[188:191],a[124:127]\n"
"v_mfma_f32_16x16x32_bf16 a[128:131],v[144:147],v[160:163],a[128:131]\n"
"v_mfma_f32_16x16x32_bf16 a[128:131],v[208:211],v[224:227],a[128:131]\n"
"buffer_load_dwordx4 v108,s[16:19],s76 offen offset:2048 lds\n"
"v_mfma_f32_16x16x32_bf16 a[136:139],v[208:211],v[228:231],a[136:139]\n"
"v_mfma_f32_16x16x32_bf16 a[136:139],v[144:147],v[164:167],a[136:139]\n"
"v_mfma_f32_16x16x32_bf16 a[144:147],v[144:147],v[168:171],a[144:147]\n"
"v_mfma_f32_16x16x32_bf16 a[144:147],v[208:211],v[232:235],a[144:147]\n"
"v_mfma_f32_16x16x32_bf16 a[152:155],v[208:211],v[236:239],a[152:155]\n"
"buffer_load_dwordx4 v109,s[16:19],s76 offen offset:3072 lds\n"
"v_mfma_f32_16x16x32_bf16 a[152:155],v[144:147],v[172:175],a[152:155]\n"
"v_mfma_f32_16x16x32_bf16 a[160:163],v[144:147],v[176:179],a[160:163]\n"
"v_mfma_f32_16x16x32_bf16 a[160:163],v[208:211],v[240:243],a[160:163]\n"
"v_mfma_f32_16x16x32_bf16 a[168:171],v[208:211],v[244:247],a[168:171]\n"
"s_mov_b32 s76,263040\n"
"s_add_u32 m0,s79,36864\n"
"v_mfma_f32_16x16x32_bf16 a[168:171],v[144:147],v[180:183],a[168:171]\n"
"buffer_load_dwordx4 v106,s[16:19],s76 offen offset:0 lds\n"
"v_mfma_f32_16x16x32_bf16 a[176:179],v[144:147],v[184:187],a[176:179]\n"
"v_mfma_f32_16x16x32_bf16 a[176:179],v[208:211],v[248:251],a[176:179]\n"
"v_mfma_f32_16x16x32_bf16 a[184:187],v[208:211],v[252:255],a[184:187]\n"
"v_mfma_f32_16x16x32_bf16 a[184:187],v[144:147],v[188:191],a[184:187]\n"
"v_mfma_f32_16x16x32_bf16 a[132:135],v[148:151],v[160:163],a[132:135]\n"
"buffer_load_dwordx4 v107,s[16:19],s76 offen offset:1024 lds\n"
"v_mfma_f32_16x16x32_bf16 a[132:135],v[212:215],v[224:227],a[132:135]\n"
"v_mfma_f32_16x16x32_bf16 a[140:143],v[212:215],v[228:231],a[140:143]\n"
"v_mfma_f32_16x16x32_bf16 a[140:143],v[148:151],v[164:167],a[140:143]\n"
"v_mfma_f32_16x16x32_bf16 a[148:151],v[148:151],v[168:171],a[148:151]\n"
"v_mfma_f32_16x16x32_bf16 a[148:151],v[212:215],v[232:235],a[148:151]\n"
"buffer_load_dwordx4 v108,s[16:19],s76 offen offset:2048 lds\n"
"v_mfma_f32_16x16x32_bf16 a[156:159],v[212:215],v[236:239],a[156:159]\n"
"v_mfma_f32_16x16x32_bf16 a[156:159],v[148:151],v[172:175],a[156:159]\n"
"v_mfma_f32_16x16x32_bf16 a[164:167],v[148:151],v[176:179],a[164:167]\n"
"v_mfma_f32_16x16x32_bf16 a[164:167],v[212:215],v[240:243],a[164:167]\n"
"v_mfma_f32_16x16x32_bf16 a[172:175],v[212:215],v[244:247],a[172:175]\n"
"buffer_load_dwordx4 v109,s[16:19],s76 offen offset:3072 lds\n"
"v_mfma_f32_16x16x32_bf16 a[172:175],v[148:151],v[180:183],a[172:175]\n"
"s_mov_b32 s87,6291456\n"
"v_mfma_f32_16x16x32_bf16 a[180:183],v[148:151],v[184:187],a[180:183]\n"
"buffer_load_dword v115,v112,s[40:43],s87 offen\n"
"v_mfma_f32_16x16x32_bf16 a[180:183],v[212:215],v[248:251],a[180:183]\n"
"s_mov_b32 s87,7340032\n"
"v_mfma_f32_16x16x32_bf16 a[188:191],v[212:215],v[252:255],a[188:191]\n"
"buffer_load_dword v115,v112,s[40:43],s87 offen\n"
"v_mfma_f32_16x16x32_bf16 a[188:191],v[148:151],v[188:191],a[188:191]\n"
"s_waitcnt vmcnt(30)\n"
"s_barrier\n"
"v_mfma_f32_16x16x32_bf16 a[192:195],v[216:219],v[224:227],a[192:195]\n"
"ds_read_b128 v[152:155],v82 offset:12288\n"
"v_mfma_f32_16x16x32_bf16 a[196:199],v[220:223],v[224:227],a[196:199]\n"
"ds_read_b128 v[156:159],v82 offset:14336\n"
"v_mfma_f32_16x16x32_bf16 a[204:207],v[220:223],v[228:231],a[204:207]\n"
"ds_read_b64_tr_b16 v[160:161],v98 offset:0\n"
"v_mfma_f32_16x16x32_bf16 a[200:203],v[216:219],v[228:231],a[200:203]\n"
"ds_read_b64_tr_b16 v[162:163],v98 offset:1024\n"
"v_mfma_f32_16x16x32_bf16 a[208:211],v[216:219],v[232:235],a[208:211]\n"
"ds_read_b64_tr_b16 v[164:165],v99 offset:0\n"
"v_mfma_f32_16x16x32_bf16 a[212:215],v[220:223],v[232:235],a[212:215]\n"
"ds_read_b64_tr_b16 v[166:167],v99 offset:1024\n"
"v_mfma_f32_16x16x32_bf16 a[220:223],v[220:223],v[236:239],a[220:223]\n"
"ds_read_b64_tr_b16 v[168:169],v100 offset:0\n"
"v_mfma_f32_16x16x32_bf16 a[216:219],v[216:219],v[236:239],a[216:219]\n"
"ds_read_b64_tr_b16 v[170:171],v100 offset:1024\n"
"v_mfma_f32_16x16x32_bf16 a[224:227],v[216:219],v[240:243],a[224:227]\n"
"ds_read_b64_tr_b16 v[172:173],v101 offset:0\n"
"v_mfma_f32_16x16x32_bf16 a[228:231],v[220:223],v[240:243],a[228:231]\n"
"ds_read_b64_tr_b16 v[174:175],v101 offset:1024\n"
"v_mfma_f32_16x16x32_bf16 a[236:239],v[220:223],v[244:247],a[236:239]\n"
"ds_read_b64_tr_b16 v[176:177],v102 offset:0\n"
"v_mfma_f32_16x16x32_bf16 a[232:235],v[216:219],v[244:247],a[232:235]\n"
"ds_read_b64_tr_b16 v[178:179],v102 offset:1024\n"
"v_mfma_f32_16x16x32_bf16 a[240:243],v[216:219],v[248:251],a[240:243]\n"
"ds_read_b64_tr_b16 v[180:181],v103 offset:0\n"
"v_mfma_f32_16x16x32_bf16 a[244:247],v[220:223],v[248:251],a[244:247]\n"
"ds_read_b64_tr_b16 v[182:183],v103 offset:1024\n"
"v_mfma_f32_16x16x32_bf16 a[252:255],v[220:223],v[252:255],a[252:255]\n"
"ds_read_b64_tr_b16 v[184:185],v104 offset:0\n"
"v_mfma_f32_16x16x32_bf16 a[248:251],v[216:219],v[252:255],a[248:251]\n"
"ds_read_b64_tr_b16 v[186:187],v104 offset:1024\n"
"s_waitcnt lgkmcnt(10)\n"
"v_mfma_f32_16x16x32_bf16 a[192:195],v[152:155],v[160:163],a[192:195]\n"
"ds_read_b64_tr_b16 v[188:189],v105 offset:0\n"
"v_mfma_f32_16x16x32_bf16 a[196:199],v[156:159],v[160:163],a[196:199]\n"
"ds_read_b64_tr_b16 v[190:191],v105 offset:1024\n"
"v_mfma_f32_16x16x32_bf16 a[204:207],v[156:159],v[164:167],a[204:207]\n"
"ds_read_b128 v[128:131],v82 offset:0\n"
"v_mfma_f32_16x16x32_bf16 a[200:203],v[152:155],v[164:167],a[200:203]\n"
"ds_read_b128 v[192:195],v83 offset:0\n"
"s_waitcnt lgkmcnt(10)\n"
"v_mfma_f32_16x16x32_bf16 a[208:211],v[152:155],v[168:171],a[208:211]\n"
"ds_read_b64_tr_b16 v[224:225],v98 offset:16384\n"
"v_mfma_f32_16x16x32_bf16 a[212:215],v[156:159],v[168:171],a[212:215]\n"
"ds_read_b64_tr_b16 v[226:227],v98 offset:17408\n"
"v_mfma_f32_16x16x32_bf16 a[220:223],v[156:159],v[172:175],a[220:223]\n"
"ds_read_b64_tr_b16 v[228:229],v99 offset:16384\n"
"v_mfma_f32_16x16x32_bf16 a[216:219],v[152:155],v[172:175],a[216:219]\n"
"ds_read_b64_tr_b16 v[230:231],v99 offset:17408\n"
"s_waitcnt lgkmcnt(10)\n"
"v_mfma_f32_16x16x32_bf16 a[224:227],v[152:155],v[176:179],a[224:227]\n"
"ds_read_b64_tr_b16 v[232:233],v100 offset:16384\n"
"v_mfma_f32_16x16x32_bf16 a[228:231],v[156:159],v[176:179],a[228:231]\n"
"ds_read_b64_tr_b16 v[234:235],v100 offset:17408\n"
"v_mfma_f32_16x16x32_bf16 a[236:239],v[156:159],v[180:183],a[236:239]\n"
"ds_read_b64_tr_b16 v[236:237],v101 offset:16384\n"
"v_mfma_f32_16x16x32_bf16 a[232:235],v[152:155],v[180:183],a[232:235]\n"
"ds_read_b64_tr_b16 v[238:239],v101 offset:17408\n"
"s_waitcnt lgkmcnt(10)\n"
"v_mfma_f32_16x16x32_bf16 a[240:243],v[152:155],v[184:187],a[240:243]\n"
"ds_read_b64_tr_b16 v[240:241],v102 offset:16384\n"
"v_mfma_f32_16x16x32_bf16 a[244:247],v[156:159],v[184:187],a[244:247]\n"
"ds_read_b64_tr_b16 v[242:243],v102 offset:17408\n"
"v_mfma_f32_16x16x32_bf16 a[252:255],v[156:159],v[188:191],a[252:255]\n"
"ds_read_b64_tr_b16 v[244:245],v103 offset:16384\n"
"v_mfma_f32_16x16x32_bf16 a[248:251],v[152:155],v[188:191],a[248:251]\n"
"ds_read_b64_tr_b16 v[246:247],v103 offset:17408\n"
"s_waitcnt lgkmcnt(8)\n"
"v_mfma_f32_16x16x32_bf16 a[0:3],v[128:131],v[160:163],a[0:3]\n"
"ds_read_b64_tr_b16 v[248:249],v104 offset:16384\n"
"v_mfma_f32_16x16x32_bf16 a[0:3],v[192:195],v[224:227],a[0:3]\n"
"ds_read_b64_tr_b16 v[250:251],v104 offset:17408\n"
"v_mfma_f32_16x16x32_bf16 a[8:11],v[192:195],v[228:231],a[8:11]\n"
"ds_read_b64_tr_b16 v[252:253],v105 offset:16384\n"
"v_mfma_f32_16x16x32_bf16 a[8:11],v[128:131],v[164:167],a[8:11]\n"
"ds_read_b64_tr_b16 v[254:255],v105 offset:17408\n"
"s_waitcnt lgkmcnt(8)\n"
"v_mfma_f32_16x16x32_bf16 a[16:19],v[128:131],v[168:171],a[16:19]\n"
"ds_read_b128 v[132:135],v82 offset:2048\n"
"v_mfma_f32_16x16x32_bf16 a[16:19],v[192:195],v[232:235],a[16:19]\n"
"ds_read_b128 v[196:199],v83 offset:2048\n"
"v_mfma_f32_16x16x32_bf16 a[24:27],v[192:195],v[236:239],a[24:27]\n"
"ds_read_b128 v[136:139],v82 offset:4096\n"
"v_mfma_f32_16x16x32_bf16 a[24:27],v[128:131],v[172:175],a[24:27]\n"
"ds_read_b128 v[200:203],v83 offset:4096\n"
"s_waitcnt lgkmcnt(8)\n"
"v_mfma_f32_16x16x32_bf16 a[32:35],v[128:131],v[176:179],a[32:35]\n"
"ds_read_b128 v[140:143],v82 offset:6144\n"
"v_mfma_f32_16x16x32_bf16 a[32:35],v[192:195],v[240:243],a[32:35]\n"
"ds_read_b128 v[204:207],v83 offset:6144\n"
"v_mfma_f32_16x16x32_bf16 a[40:43],v[192:195],v[244:247],a[40:43]\n"
"ds_read_b128 v[144:147],v82 offset:8192\n"
"v_mfma_f32_16x16x32_bf16 a[40:43],v[128:131],v[180:183],a[40:43]\n"
"ds_read_b128 v[208:211],v83 offset:8192\n"
"s_waitcnt lgkmcnt(8)\n"
"v_mfma_f32_16x16x32_bf16 a[48:51],v[128:131],v[184:187],a[48:51]\n"
"ds_read_b128 v[148:151],v82 offset:10240\n"
"v_mfma_f32_16x16x32_bf16 a[48:51],v[192:195],v[248:251],a[48:51]\n"
"ds_read_b128 v[212:215],v83 offset:10240\n"
"v_mfma_f32_16x16x32_bf16 a[56:59],v[192:195],v[252:255],a[56:59]\n"
"ds_read_b128 v[216:219],v83 offset:12288\n"
"v_mfma_f32_16x16x32_bf16 a[56:59],v[128:131],v[188:191],a[56:59]\n"
"ds_read_b128 v[220:223],v83 offset:14336\n"
"s_waitcnt lgkmcnt(10)\n"
"v_mfma_f32_16x16x32_bf16 a[4:7],v[132:135],v[160:163],a[4:7]\n"
"v_mfma_f32_16x16x32_bf16 a[4:7],v[196:199],v[224:227],a[4:7]\n"
"buffer_load_dwordx4 v[74:77],v112,s[24:27],s58 offen sc1\n"
"v_mfma_f32_16x16x32_bf16 a[12:15],v[196:199],v[228:231],a[12:15]\n"
"v_mfma_f32_16x16x32_bf16 a[12:15],v[132:135],v[164:167],a[12:15]\n"
"v_mfma_f32_16x16x32_bf16 a[20:23],v[132:135],v[168:171],a[20:23]\n"
"s_waitcnt lgkmcnt(0)\n"
"s_barrier\n"
"v_mfma_f32_16x16x32_bf16 a[20:23],v[196:199],v[232:235],a[20:23]\n"
"s_mov_b32 s77,3670016\n"
"s_add_u32 m0,s78,98304\n"
"v_mfma_f32_16x16x32_bf16 a[28:31],v[196:199],v[236:239],a[28:31]\n"
"buffer_load_dwordx4 v110,s[20:23],s77 offen offset:0 lds\n"
"v_mfma_f32_16x16x32_bf16 a[28:31],v[132:135],v[172:175],a[28:31]\n"
"v_mfma_f32_16x16x32_bf16 a[36:39],v[132:135],v[176:179],a[36:39]\n"
"s_load_dwordx2 s[48:49],s[0:1],56\n"
"v_mfma_f32_16x16x32_bf16 a[36:39],v[196:199],v[240:243],a[36:39]\n"
"v_mfma_f32_16x16x32_bf16 a[44:47],v[196:199],v[244:247],a[44:47]\n"
"buffer_load_dwordx4 v111,s[20:23],s77 offen offset:1024 lds\n"
"v_mfma_f32_16x16x32_bf16 a[44:47],v[132:135],v[180:183],a[44:47]\n"
"v_mfma_f32_16x16x32_bf16 a[52:55],v[132:135],v[184:187],a[52:55]\n"
"v_mfma_f32_16x16x32_bf16 a[52:55],v[196:199],v[248:251],a[52:55]\n"
"v_mfma_f32_16x16x32_bf16 a[60:63],v[196:199],v[252:255],a[60:63]\n"
"buffer_load_dwordx4 v113,s[20:23],s77 offen offset:2048 lds\n"
"v_mfma_f32_16x16x32_bf16 a[60:63],v[132:135],v[188:191],a[60:63]\n"
"v_mfma_f32_16x16x32_bf16 a[64:67],v[136:139],v[160:163],a[64:67]\n"
"v_mfma_f32_16x16x32_bf16 a[64:67],v[200:203],v[224:227],a[64:67]\n"
"v_mfma_f32_16x16x32_bf16 a[72:75],v[200:203],v[228:231],a[72:75]\n"
"buffer_load_dwordx4 v114,s[20:23],s77 offen offset:3072 lds\n"
"v_mfma_f32_16x16x32_bf16 a[72:75],v[136:139],v[164:167],a[72:75]\n"
"v_mfma_f32_16x16x32_bf16 a[80:83],v[136:139],v[168:171],a[80:83]\n"
"v_mfma_f32_16x16x32_bf16 a[80:83],v[200:203],v[232:235],a[80:83]\n"
"s_mov_b32 s77,3932160\n"
"s_add_u32 m0,s78,114688\n"
"v_mfma_f32_16x16x32_bf16 a[88:91],v[200:203],v[236:239],a[88:91]\n"
"buffer_load_dwordx4 v110,s[20:23],s77 offen offset:0 lds\n"
"v_mfma_f32_16x16x32_bf16 a[88:91],v[136:139],v[172:175],a[88:91]\n"
"v_mfma_f32_16x16x32_bf16 a[96:99],v[136:139],v[176:179],a[96:99]\n"
"v_mfma_f32_16x16x32_bf16 a[96:99],v[200:203],v[240:243],a[96:99]\n"
"v_mfma_f32_16x16x32_bf16 a[104:107],v[200:203],v[244:247],a[104:107]\n"
"buffer_load_dwordx4 v111,s[20:23],s77 offen offset:1024 lds\n"
"v_mfma_f32_16x16x32_bf16 a[104:107],v[136:139],v[180:183],a[104:107]\n"
"v_mfma_f32_16x16x32_bf16 a[112:115],v[136:139],v[184:187],a[112:115]\n"
"v_mfma_f32_16x16x32_bf16 a[112:115],v[200:203],v[248:251],a[112:115]\n"
"v_mfma_f32_16x16x32_bf16 a[120:123],v[200:203],v[252:255],a[120:123]\n"
"buffer_load_dwordx4 v113,s[20:23],s77 offen offset:2048 lds\n"
"v_mfma_f32_16x16x32_bf16 a[120:123],v[136:139],v[188:191],a[120:123]\n"
"s_mov_b32 s76,s55\n"
"s_waitcnt vmcnt(7)\n"
"v_mfma_f32_16x16x32_bf16 a[68:71],v[140:143],v[160:163],a[68:71]\n"
"v_xor_b32 v116,s80,v74\n"
"v_xor_b32 v117,s81,v75\n"
"v_mfma_f32_16x16x32_bf16 a[68:71],v[204:207],v[224:227],a[68:71]\n"
"v_or_b32 v116,v116,v117\n"
"v_xor_b32 v117,s80,v76\n"
"v_mfma_f32_16x16x32_bf16 a[76:79],v[204:207],v[228:231],a[76:79]\n"
"buffer_load_dwordx4 v114,s[20:23],s77 offen offset:3072 lds\n"
"v_mfma_f32_16x16x32_bf16 a[76:79],v[140:143],v[164:167],a[76:79]\n"
"v_or_b32 v116,v116,v117\n"
"v_xor_b32 v117,s81,v77\n"
"v_mfma_f32_16x16x32_bf16 a[84:87],v[140:143],v[168:171],a[84:87]\n"
"v_or_b32 v116,v116,v117\n"
"v_cmp_ne_u32 vcc,0,v116\n"
"v_mfma_f32_16x16x32_bf16 a[84:87],v[204:207],v[232:235],a[84:87]\n"
"s_cbranch_vccnz .Lfk_xn1_s\n"
".Lfk_xn1_ok:\n"
"v_mfma_f32_16x16x32_bf16 a[92:95],v[204:207],v[236:239],a[92:95]\n"
"s_add_u32 m0,s78,65536\n"
"v_mfma_f32_16x16x32_bf16 a[92:95],v[140:143],v[172:175],a[92:95]\n"
"buffer_load_dwordx4 v112,s[24:27],s76 offen offset:0 sc1 lds\n"
"v_mfma_f32_16x16x32_bf16 a[100:103],v[140:143],v[176:179],a[100:103]\n"
"s_waitcnt lgkmcnt(0)\n"
"s_lshl2_add_u32 s88,s100,s52\n"
"v_mfma_f32_16x16x32_bf16 a[100:103],v[204:207],v[240:243],a[100:103]\n"
"s_mul_i32 s88,s88,7168\n"
"s_add_u32 s88,s88,0\n"
"v_mfma_f32_16x16x32_bf16 a[108:111],v[204:207],v[244:247],a[108:111]\n"
"s_add_u32 s48,s48,s88\n"
"s_addc_u32 s49,s49,0\n"
"s_mov_b32 s50,3584\n"
"v_mfma_f32_16x16x32_bf16 a[108:111],v[140:143],v[180:183],a[108:111]\n"
"s_mov_b32 s51,0x20000\n"
"v_lshlrev_b32 v8,3,v112\n"
"v_mfma_f32_16x16x32_bf16 a[116:119],v[140:143],v[184:187],a[116:119]\n"
"buffer_load_dwordx4 v112,s[24:27],s76 offen offset:1024 sc1 lds\n"
"v_mfma_f32_16x16x32_bf16 a[116:119],v[204:207],v[248:251],a[116:119]\n"
"v_mfma_f32_16x16x32_bf16 a[124:127],v[204:207],v[252:255],a[124:127]\n"
"v_mfma_f32_16x16x32_bf16 a[124:127],v[140:143],v[188:191],a[124:127]\n"
"v_mfma_f32_16x16x32_bf16 a[128:131],v[144:147],v[160:163],a[128:131]\n"
"v_mfma_f32_16x16x32_bf16 a[128:131],v[208:211],v[224:227],a[128:131]\n"
"buffer_load_dwordx4 v112,s[24:27],s76 offen offset:2048 sc1 lds\n"
"v_mfma_f32_16x16x32_bf16 a[136:139],v[208:211],v[228:231],a[136:139]\n"
"v_mfma_f32_16x16x32_bf16 a[136:139],v[144:147],v[164:167],a[136:139]\n"
"v_mfma_f32_16x16x32_bf16 a[144:147],v[144:147],v[168:171],a[144:147]\n"
"v_mfma_f32_16x16x32_bf16 a[144:147],v[208:211],v[232:235],a[144:147]\n"
"buffer_load_dwordx4 v112,s[24:27],s76 offen offset:3072 sc1 lds\n"
"v_mfma_f32_16x16x32_bf16 a[152:155],v[208:211],v[236:239],a[152:155]\n"
"s_xor_b32 s76,s76,16384\n"
"v_mfma_f32_16x16x32_bf16 a[152:155],v[144:147],v[172:175],a[152:155]\n"
"v_mfma_f32_16x16x32_bf16 a[160:163],v[144:147],v[176:179],a[160:163]\n"
"v_mfma_f32_16x16x32_bf16 a[160:163],v[208:211],v[240:243],a[160:163]\n"
"v_mfma_f32_16x16x32_bf16 a[168:171],v[208:211],v[244:247],a[168:171]\n"
"s_add_u32 m0,s78,81920\n"
"v_mfma_f32_16x16x32_bf16 a[168:171],v[144:147],v[180:183],a[168:171]\n"
"buffer_load_dwordx4 v112,s[24:27],s76 offen offset:0 sc1 lds\n"
"v_mfma_f32_16x16x32_bf16 a[176:179],v[144:147],v[184:187],a[176:179]\n"
"v_mfma_f32_16x16x32_bf16 a[176:179],v[208:211],v[248:251],a[176:179]\n"
"v_mfma_f32_16x16x32_bf16 a[184:187],v[208:211],v[252:255],a[184:187]\n"
"v_mfma_f32_16x16x32_bf16 a[184:187],v[144:147],v[188:191],a[184:187]\n"
"v_mfma_f32_16x16x32_bf16 a[132:135],v[148:151],v[160:163],a[132:135]\n"
"buffer_load_dwordx4 v112,s[24:27],s76 offen offset:1024 sc1 lds\n"
"v_mfma_f32_16x16x32_bf16 a[132:135],v[212:215],v[224:227],a[132:135]\n"
"v_mfma_f32_16x16x32_bf16 a[140:143],v[212:215],v[228:231],a[140:143]\n"
"v_mfma_f32_16x16x32_bf16 a[140:143],v[148:151],v[164:167],a[140:143]\n"
"v_mfma_f32_16x16x32_bf16 a[148:151],v[148:151],v[168:171],a[148:151]\n"
"v_mfma_f32_16x16x32_bf16 a[148:151],v[212:215],v[232:235],a[148:151]\n"
"buffer_load_dwordx4 v112,s[24:27],s76 offen offset:2048 sc1 lds\n"
"v_mfma_f32_16x16x32_bf16 a[156:159],v[212:215],v[236:239],a[156:159]\n"
"v_mfma_f32_16x16x32_bf16 a[156:159],v[148:151],v[172:175],a[156:159]\n"
"v_mfma_f32_16x16x32_bf16 a[164:167],v[148:151],v[176:179],a[164:167]\n"
"v_mfma_f32_16x16x32_bf16 a[164:167],v[212:215],v[240:243],a[164:167]\n"
"buffer_load_dwordx4 v112,s[24:27],s76 offen offset:3072 sc1 lds\n"
"v_mfma_f32_16x16x32_bf16 a[172:175],v[212:215],v[244:247],a[172:175]\n"
"s_xor_b32 s76,s76,49152\n"
"v_mfma_f32_16x16x32_bf16 a[172:175],v[148:151],v[180:183],a[172:175]\n"
"v_mfma_f32_16x16x32_bf16 a[180:183],v[148:151],v[184:187],a[180:183]\n"
"buffer_load_dword v115,v8,s[48:51],0 offen\n"
"v_mfma_f32_16x16x32_bf16 a[180:183],v[212:215],v[248:251],a[180:183]\n"
"v_mfma_f32_16x16x32_bf16 a[188:191],v[212:215],v[252:255],a[188:191]\n"
"v_mfma_f32_16x16x32_bf16 a[188:191],v[148:151],v[188:191],a[188:191]\n"
"s_barrier\n"
"v_mfma_f32_16x16x32_bf16 a[192:195],v[216:219],v[224:227],a[192:195]\n"
"ds_read_b128 v[152:155],v80 offset:12288\n"
"v_mfma_f32_16x16x32_bf16 a[196:199],v[220:223],v[224:227],a[196:199]\n"
"ds_read_b128 v[156:159],v80 offset:14336\n"
"v_mfma_f32_16x16x32_bf16 a[204:207],v[220:223],v[228:231],a[204:207]\n"
"ds_read_b64_tr_b16 v[160:161],v98 offset:49152\n"
"v_mfma_f32_16x16x32_bf16 a[200:203],v[216:219],v[228:231],a[200:203]\n"
"ds_read_b64_tr_b16 v[162:163],v98 offset:50176\n"
"v_mfma_f32_16x16x32_bf16 a[208:211],v[216:219],v[232:235],a[208:211]\n"
"ds_read_b64_tr_b16 v[164:165],v99 offset:49152\n"
"v_mfma_f32_16x16x32_bf16 a[212:215],v[220:223],v[232:235],a[212:215]\n"
"ds_read_b64_tr_b16 v[166:167],v99 offset:50176\n"
"v_mfma_f32_16x16x32_bf16 a[220:223],v[220:223],v[236:239],a[220:223]\n"
"ds_read_b64_tr_b16 v[168:169],v100 offset:49152\n"
"v_mfma_f32_16x16x32_bf16 a[216:219],v[216:219],v[236:239],a[216:219]\n"
"ds_read_b64_tr_b16 v[170:171],v100 offset:50176\n"
"v_mfma_f32_16x16x32_bf16 a[224:227],v[216:219],v[240:243],a[224:227]\n"
"ds_read_b64_tr_b16 v[172:173],v101 offset:49152\n"
"v_mfma_f32_16x16x32_bf16 a[228:231],v[220:223],v[240:243],a[228:231]\n"
"ds_read_b64_tr_b16 v[174:175],v101 offset:50176\n"
"v_mfma_f32_16x16x32_bf16 a[236:239],v[220:223],v[244:247],a[236:239]\n"
"ds_read_b64_tr_b16 v[176:177],v102 offset:49152\n"
"v_mfma_f32_16x16x32_bf16 a[232:235],v[216:219],v[244:247],a[232:235]\n"
"ds_read_b64_tr_b16 v[178:179],v102 offset:50176\n"
"v_mfma_f32_16x16x32_bf16 a[240:243],v[216:219],v[248:251],a[240:243]\n"
"ds_read_b64_tr_b16 v[180:181],v103 offset:49152\n"
"v_mfma_f32_16x16x32_bf16 a[244:247],v[220:223],v[248:251],a[244:247]\n"
"ds_read_b64_tr_b16 v[182:183],v103 offset:50176\n"
"ds_read_b64_tr_b16 v[184:185],v104 offset:49152\n"
"v_mfma_f32_16x16x32_bf16 a[252:255],v[220:223],v[252:255],a[252:255]\n"
"v_mov_b32 v110,v72\n"
"v_mov_b32 v111,v73\n"
"v_mfma_f32_16x16x32_bf16 a[248:251],v[216:219],v[252:255],a[248:251]\n"
"ds_read_b64_tr_b16 v[186:187],v104 offset:50176\n"
"s_waitcnt lgkmcnt(10)\n"
"v_mfma_f32_16x16x32_bf16 a[192:195],v[152:155],v[160:163],a[192:195]\n"
"ds_read_b64_tr_b16 v[188:189],v105 offset:49152\n"
"v_mfma_f32_16x16x32_bf16 a[196:199],v[156:159],v[160:163],a[196:199]\n"
"ds_read_b64_tr_b16 v[190:191],v105 offset:50176\n"
"v_mfma_f32_16x16x32_bf16 a[204:207],v[156:159],v[164:167],a[204:207]\n"
"ds_read_b128 v[128:131],v80 offset:0\n"
"v_mfma_f32_16x16x32_bf16 a[200:203],v[152:155],v[164:167],a[200:203]\n"
"ds_read_b128 v[192:195],v81 offset:0\n"
"s_waitcnt lgkmcnt(10)\n"
"v_mfma_f32_16x16x32_bf16 a[208:211],v[152:155],v[168:171],a[208:211]\n"
"ds_read_b64_tr_b16 v[224:225],v98 offset:32768\n"
"v_mfma_f32_16x16x32_bf16 a[212:215],v[156:159],v[168:171],a[212:215]\n"
"ds_read_b64_tr_b16 v[226:227],v98 offset:33792\n"
"v_mfma_f32_16x16x32_bf16 a[220:223],v[156:159],v[172:175],a[220:223]\n"
"ds_read_b64_tr_b16 v[228:229],v99 offset:32768\n"
"v_mfma_f32_16x16x32_bf16 a[216:219],v[152:155],v[172:175],a[216:219]\n"
"ds_read_b64_tr_b16 v[230:231],v99 offset:33792\n"
"s_waitcnt lgkmcnt(10)\n"
"v_mfma_f32_16x16x32_bf16 a[224:227],v[152:155],v[176:179],a[224:227]\n"
"ds_read_b64_tr_b16 v[232:233],v100 offset:32768\n"
"v_mfma_f32_16x16x32_bf16 a[228:231],v[156:159],v[176:179],a[228:231]\n"
"ds_read_b64_tr_b16 v[234:235],v100 offset:33792\n"
"v_mfma_f32_16x16x32_bf16 a[236:239],v[156:159],v[180:183],a[236:239]\n"
"ds_read_b64_tr_b16 v[236:237],v101 offset:32768\n"
"v_mfma_f32_16x16x32_bf16 a[232:235],v[152:155],v[180:183],a[232:235]\n"
"ds_read_b64_tr_b16 v[238:239],v101 offset:33792\n"
"s_waitcnt lgkmcnt(10)\n"
"v_mfma_f32_16x16x32_bf16 a[240:243],v[152:155],v[184:187],a[240:243]\n"
"ds_read_b64_tr_b16 v[240:241],v102 offset:32768\n"
"v_mfma_f32_16x16x32_bf16 a[244:247],v[156:159],v[184:187],a[244:247]\n"
"ds_read_b64_tr_b16 v[242:243],v102 offset:33792\n"
"v_mfma_f32_16x16x32_bf16 a[252:255],v[156:159],v[188:191],a[252:255]\n"
"ds_read_b64_tr_b16 v[244:245],v103 offset:32768\n"
"v_mfma_f32_16x16x32_bf16 a[248:251],v[152:155],v[188:191],a[248:251]\n"
"ds_read_b64_tr_b16 v[246:247],v103 offset:33792\n"
"s_waitcnt lgkmcnt(8)\n"
"v_mfma_f32_16x16x32_bf16 a[0:3],v[128:131],v[160:163],a[0:3]\n"
"ds_read_b64_tr_b16 v[248:249],v104 offset:32768\n"
"v_mfma_f32_16x16x32_bf16 a[0:3],v[192:195],v[224:227],a[0:3]\n"
"ds_read_b64_tr_b16 v[250:251],v104 offset:33792\n"
"v_mfma_f32_16x16x32_bf16 a[8:11],v[192:195],v[228:231],a[8:11]\n"
"ds_read_b64_tr_b16 v[252:253],v105 offset:32768\n"
"v_mfma_f32_16x16x32_bf16 a[8:11],v[128:131],v[164:167],a[8:11]\n"
"ds_read_b64_tr_b16 v[254:255],v105 offset:33792\n"
"s_waitcnt lgkmcnt(8)\n"
"v_mfma_f32_16x16x32_bf16 a[16:19],v[128:131],v[168:171],a[16:19]\n"
"ds_read_b128 v[132:135],v80 offset:2048\n"
"v_mfma_f32_16x16x32_bf16 a[16:19],v[192:195],v[232:235],a[16:19]\n"
"ds_read_b128 v[196:199],v81 offset:2048\n"
"v_mfma_f32_16x16x32_bf16 a[24:27],v[192:195],v[236:239],a[24:27]\n"
"ds_read_b128 v[136:139],v80 offset:4096\n"
"v_mfma_f32_16x16x32_bf16 a[24:27],v[128:131],v[172:175],a[24:27]\n"
"ds_read_b128 v[200:203],v81 offset:4096\n"
"s_waitcnt lgkmcnt(8)\n"
"v_mfma_f32_16x16x32_bf16 a[32:35],v[128:131],v[176:179],a[32:35]\n"
"ds_read_b128 v[140:143],v80 offset:6144\n"
"v_mfma_f32_16x16x32_bf16 a[32:35],v[192:195],v[240:243],a[32:35]\n"
"ds_read_b128 v[204:207],v81 offset:6144\n"
"v_mfma_f32_16x16x32_bf16 a[40:43],v[192:195],v[244:247],a[40:43]\n"
"ds_read_b128 v[144:147],v80 offset:8192\n"
"v_mfma_f32_16x16x32_bf16 a[40:43],v[128:131],v[180:183],a[40:43]\n"
"ds_read_b128 v[208:211],v81 offset:8192\n"
"s_waitcnt lgkmcnt(8)\n"
"v_mfma_f32_16x16x32_bf16 a[48:51],v[128:131],v[184:187],a[48:51]\n"
"ds_read_b128 v[148:151],v80 offset:10240\n"
"v_mfma_f32_16x16x32_bf16 a[48:51],v[192:195],v[248:251],a[48:51]\n"
"ds_read_b128 v[212:215],v81 offset:10240\n"
"v_mfma_f32_16x16x32_bf16 a[56:59],v[192:195],v[252:255],a[56:59]\n"
"ds_read_b128 v[216:219],v81 offset:12288\n"
"v_mfma_f32_16x16x32_bf16 a[56:59],v[128:131],v[188:191],a[56:59]\n"
"ds_read_b128 v[220:223],v81 offset:14336\n"
"s_waitcnt lgkmcnt(10)\n"
"v_mfma_f32_16x16x32_bf16 a[4:7],v[132:135],v[160:163],a[4:7]\n"
"v_mfma_f32_16x16x32_bf16 a[4:7],v[196:199],v[224:227],a[4:7]\n"
"v_mfma_f32_16x16x32_bf16 a[12:15],v[196:199],v[228:231],a[12:15]\n"
"v_mfma_f32_16x16x32_bf16 a[12:15],v[132:135],v[164:167],a[12:15]\n"
"v_mfma_f32_16x16x32_bf16 a[20:23],v[132:135],v[168:171],a[20:23]\n"
"s_waitcnt lgkmcnt(0)\n"
"s_barrier\n"
"v_mfma_f32_16x16x32_bf16 a[20:23],v[196:199],v[232:235],a[20:23]\n"
"s_add_u32 m0,s85,147456\n"
"v_mfma_f32_16x16x32_bf16 a[28:31],v[196:199],v[236:239],a[28:31]\n"
"buffer_load_dwordx4 v110,s[28:31],s86 offen lds\n"
"v_mfma_f32_16x16x32_bf16 a[28:31],v[132:135],v[172:175],a[28:31]\n"
"v_mfma_f32_16x16x32_bf16 a[36:39],v[132:135],v[176:179],a[36:39]\n"
"s_load_dwordx2 s[48:49],s[0:1],56\n"
"v_mfma_f32_16x16x32_bf16 a[36:39],v[196:199],v[240:243],a[36:39]\n"
"s_add_u32 m0,s85,148480\n"
"v_mfma_f32_16x16x32_bf16 a[44:47],v[196:199],v[244:247],a[44:47]\n"
"buffer_load_dwordx4 v111,s[28:31],s86 offen lds\n"
"v_mfma_f32_16x16x32_bf16 a[44:47],v[132:135],v[180:183],a[44:47]\n"
"s_xor_b32 s86,s86,262144\n"
"v_mfma_f32_16x16x32_bf16 a[52:55],v[132:135],v[184:187],a[52:55]\n"
"v_mfma_f32_16x16x32_bf16 a[52:55],v[196:199],v[248:251],a[52:55]\n"
"s_add_u32 m0,s85,155648\n"
"v_mfma_f32_16x16x32_bf16 a[60:63],v[196:199],v[252:255],a[60:63]\n"
"buffer_load_dwordx4 v110,s[28:31],s86 offen lds\n"
"v_mfma_f32_16x16x32_bf16 a[60:63],v[132:135],v[188:191],a[60:63]\n"
"v_mfma_f32_16x16x32_bf16 a[64:67],v[136:139],v[160:163],a[64:67]\n"
"v_mfma_f32_16x16x32_bf16 a[64:67],v[200:203],v[224:227],a[64:67]\n"
"s_add_u32 m0,s85,156672\n"
"v_mfma_f32_16x16x32_bf16 a[72:75],v[200:203],v[228:231],a[72:75]\n"
"buffer_load_dwordx4 v111,s[28:31],s86 offen lds\n"
"v_mfma_f32_16x16x32_bf16 a[72:75],v[136:139],v[164:167],a[72:75]\n"
"s_xor_b32 s86,s86,786432\n"
"v_mfma_f32_16x16x32_bf16 a[80:83],v[136:139],v[168:171],a[80:83]\n"
"v_mfma_f32_16x16x32_bf16 a[80:83],v[200:203],v[232:235],a[80:83]\n"
"s_add_u32 m0,s85,131072\n"
"v_mfma_f32_16x16x32_bf16 a[88:91],v[200:203],v[236:239],a[88:91]\n"
"buffer_load_dwordx4 v110,s[28:31],s86 offen lds\n"
"v_mfma_f32_16x16x32_bf16 a[88:91],v[136:139],v[172:175],a[88:91]\n"
"v_mfma_f32_16x16x32_bf16 a[96:99],v[136:139],v[176:179],a[96:99]\n"
"v_mfma_f32_16x16x32_bf16 a[96:99],v[200:203],v[240:243],a[96:99]\n"
"s_add_u32 m0,s85,132096\n"
"v_mfma_f32_16x16x32_bf16 a[104:107],v[200:203],v[244:247],a[104:107]\n"
"buffer_load_dwordx4 v111,s[28:31],s86 offen lds\n"
"v_mfma_f32_16x16x32_bf16 a[104:107],v[136:139],v[180:183],a[104:107]\n"
"s_xor_b32 s86,s86,262144\n"
"v_mfma_f32_16x16x32_bf16 a[112:115],v[136:139],v[184:187],a[112:115]\n"
"v_mfma_f32_16x16x32_bf16 a[112:115],v[200:203],v[248:251],a[112:115]\n"
"v_mfma_f32_16x16x32_bf16 a[120:123],v[200:203],v[252:255],a[120:123]\n"
"v_mfma_f32_16x16x32_bf16 a[120:123],v[136:139],v[188:191],a[120:123]\n"
"v_mfma_f32_16x16x32_bf16 a[68:71],v[140:143],v[160:163],a[68:71]\n"
"v_mfma_f32_16x16x32_bf16 a[68:71],v[204:207],v[224:227],a[68:71]\n"
"s_add_u32 m0,s85,139264\n"
"v_mfma_f32_16x16x32_bf16 a[76:79],v[204:207],v[228:231],a[76:79]\n"
"buffer_load_dwordx4 v110,s[28:31],s86 offen lds\n"
"v_mfma_f32_16x16x32_bf16 a[76:79],v[140:143],v[164:167],a[76:79]\n"
"v_mfma_f32_16x16x32_bf16 a[84:87],v[140:143],v[168:171],a[84:87]\n"
"v_mfma_f32_16x16x32_bf16 a[84:87],v[204:207],v[232:235],a[84:87]\n"
"s_add_u32 m0,s85,140288\n"
"v_mfma_f32_16x16x32_bf16 a[92:95],v[204:207],v[236:239],a[92:95]\n"
"buffer_load_dwordx4 v111,s[28:31],s86 offen lds\n"
"v_mfma_f32_16x16x32_bf16 a[92:95],v[140:143],v[172:175],a[92:95]\n"
"s_xor_b32 s86,s86,1835008\n"
"v_mfma_f32_16x16x32_bf16 a[100:103],v[140:143],v[176:179],a[100:103]\n"
"s_waitcnt lgkmcnt(0)\n"
"s_lshl2_add_u32 s88,s100,s52\n"
"v_mfma_f32_16x16x32_bf16 a[100:103],v[204:207],v[240:243],a[100:103]\n"
"s_mul_i32 s88,s88,7168\n"
"s_add_u32 m0,s78,0\n"
"v_mfma_f32_16x16x32_bf16 a[108:111],v[204:207],v[244:247],a[108:111]\n"
"buffer_load_dwordx4 v112,s[24:27],s76 offen offset:0 sc1 lds\n"
"v_mfma_f32_16x16x32_bf16 a[108:111],v[140:143],v[180:183],a[108:111]\n"
"s_add_u32 s88,s88,3584\n"
"s_add_u32 s48,s48,s88\n"
"s_addc_u32 s49,s49,0\n"
"v_mfma_f32_16x16x32_bf16 a[116:119],v[140:143],v[184:187],a[116:119]\n"
"s_mov_b32 s50,3584\n"
"s_mov_b32 s51,0x20000\n"
"v_mfma_f32_16x16x32_bf16 a[116:119],v[204:207],v[248:251],a[116:119]\n"
"v_lshlrev_b32 v8,3,v112\n"
"v_mfma_f32_16x16x32_bf16 a[124:127],v[204:207],v[252:255],a[124:127]\n"
"buffer_load_dwordx4 v112,s[24:27],s76 offen offset:1024 sc1 lds\n"
"v_mfma_f32_16x16x32_bf16 a[124:127],v[140:143],v[188:191],a[124:127]\n"
"v_mfma_f32_16x16x32_bf16 a[128:131],v[144:147],v[160:163],a[128:131]\n"
"v_mfma_f32_16x16x32_bf16 a[128:131],v[208:211],v[224:227],a[128:131]\n"
"v_mfma_f32_16x16x32_bf16 a[136:139],v[208:211],v[228:231],a[136:139]\n"
"buffer_load_dwordx4 v112,s[24:27],s76 offen offset:2048 sc1 lds\n"
"v_mfma_f32_16x16x32_bf16 a[136:139],v[144:147],v[164:167],a[136:139]\n"
"v_mfma_f32_16x16x32_bf16 a[144:147],v[144:147],v[168:171],a[144:147]\n"
"v_mfma_f32_16x16x32_bf16 a[144:147],v[208:211],v[232:235],a[144:147]\n"
"buffer_load_dwordx4 v112,s[24:27],s76 offen offset:3072 sc1 lds\n"
"v_mfma_f32_16x16x32_bf16 a[152:155],v[208:211],v[236:239],a[152:155]\n"
"s_xor_b32 s76,s76,16384\n"
"v_mfma_f32_16x16x32_bf16 a[152:155],v[144:147],v[172:175],a[152:155]\n"
"v_mfma_f32_16x16x32_bf16 a[160:163],v[144:147],v[176:179],a[160:163]\n"
"v_mfma_f32_16x16x32_bf16 a[160:163],v[208:211],v[240:243],a[160:163]\n"
"s_add_u32 m0,s78,16384\n"
"v_mfma_f32_16x16x32_bf16 a[168:171],v[208:211],v[244:247],a[168:171]\n"
"buffer_load_dwordx4 v112,s[24:27],s76 offen offset:0 sc1 lds\n"
"v_mfma_f32_16x16x32_bf16 a[168:171],v[144:147],v[180:183],a[168:171]\n"
"v_mfma_f32_16x16x32_bf16 a[176:179],v[144:147],v[184:187],a[176:179]\n"
"v_mfma_f32_16x16x32_bf16 a[176:179],v[208:211],v[248:251],a[176:179]\n"
"v_mfma_f32_16x16x32_bf16 a[184:187],v[208:211],v[252:255],a[184:187]\n"
"buffer_load_dwordx4 v112,s[24:27],s76 offen offset:1024 sc1 lds\n"
"v_mfma_f32_16x16x32_bf16 a[184:187],v[144:147],v[188:191],a[184:187]\n"
"v_mfma_f32_16x16x32_bf16 a[132:135],v[148:151],v[160:163],a[132:135]\n"
"v_mfma_f32_16x16x32_bf16 a[132:135],v[212:215],v[224:227],a[132:135]\n"
"v_mfma_f32_16x16x32_bf16 a[140:143],v[212:215],v[228:231],a[140:143]\n"
"buffer_load_dwordx4 v112,s[24:27],s76 offen offset:2048 sc1 lds\n"
"v_mfma_f32_16x16x32_bf16 a[140:143],v[148:151],v[164:167],a[140:143]\n"
"v_mfma_f32_16x16x32_bf16 a[148:151],v[148:151],v[168:171],a[148:151]\n"
"v_mfma_f32_16x16x32_bf16 a[148:151],v[212:215],v[232:235],a[148:151]\n"
"buffer_load_dwordx4 v112,s[24:27],s76 offen offset:3072 sc1 lds\n"
"v_mfma_f32_16x16x32_bf16 a[156:159],v[212:215],v[236:239],a[156:159]\n"
"s_xor_b32 s76,s76,114688\n"
"v_mfma_f32_16x16x32_bf16 a[156:159],v[148:151],v[172:175],a[156:159]\n"
"v_mfma_f32_16x16x32_bf16 a[164:167],v[148:151],v[176:179],a[164:167]\n"
"buffer_load_dword v115,v8,s[48:51],0 offen\n"
"v_mfma_f32_16x16x32_bf16 a[164:167],v[212:215],v[240:243],a[164:167]\n"
"v_mfma_f32_16x16x32_bf16 a[172:175],v[212:215],v[244:247],a[172:175]\n"
"v_mfma_f32_16x16x32_bf16 a[172:175],v[148:151],v[180:183],a[172:175]\n"
"v_mfma_f32_16x16x32_bf16 a[180:183],v[148:151],v[184:187],a[180:183]\n"
"v_mfma_f32_16x16x32_bf16 a[180:183],v[212:215],v[248:251],a[180:183]\n"
"v_mfma_f32_16x16x32_bf16 a[188:191],v[212:215],v[252:255],a[188:191]\n"
"v_mfma_f32_16x16x32_bf16 a[188:191],v[148:151],v[188:191],a[188:191]\n"
"s_waitcnt vmcnt(26)\n"
"s_barrier\n"
"v_mfma_f32_16x16x32_bf16 a[192:195],v[216:219],v[224:227],a[192:195]\n"
"ds_read_b128 v[152:155],v80 offset:45056\n"
"v_mfma_f32_16x16x32_bf16 a[196:199],v[220:223],v[224:227],a[196:199]\n"
"ds_read_b128 v[156:159],v80 offset:47104\n"
"v_mfma_f32_16x16x32_bf16 a[204:207],v[220:223],v[228:231],a[204:207]\n"
"ds_read_b64_tr_b16 v[160:161],v98 offset:0\n"
"v_mfma_f32_16x16x32_bf16 a[200:203],v[216:219],v[228:231],a[200:203]\n"
"ds_read_b64_tr_b16 v[162:163],v98 offset:1024\n"
"v_mfma_f32_16x16x32_bf16 a[208:211],v[216:219],v[232:235],a[208:211]\n"
"ds_read_b64_tr_b16 v[164:165],v99 offset:0\n"
"v_mfma_f32_16x16x32_bf16 a[212:215],v[220:223],v[232:235],a[212:215]\n"
"ds_read_b64_tr_b16 v[166:167],v99 offset:1024\n"
"v_mfma_f32_16x16x32_bf16 a[220:223],v[220:223],v[236:239],a[220:223]\n"
"ds_read_b64_tr_b16 v[168:169],v100 offset:0\n"
"v_mfma_f32_16x16x32_bf16 a[216:219],v[216:219],v[236:239],a[216:219]\n"
"ds_read_b64_tr_b16 v[170:171],v100 offset:1024\n"
"v_mfma_f32_16x16x32_bf16 a[224:227],v[216:219],v[240:243],a[224:227]\n"
"ds_read_b64_tr_b16 v[172:173],v101 offset:0\n"
"v_mfma_f32_16x16x32_bf16 a[228:231],v[220:223],v[240:243],a[228:231]\n"
"ds_read_b64_tr_b16 v[174:175],v101 offset:1024\n"
"v_mfma_f32_16x16x32_bf16 a[236:239],v[220:223],v[244:247],a[236:239]\n"
"ds_read_b64_tr_b16 v[176:177],v102 offset:0\n"
"v_mfma_f32_16x16x32_bf16 a[232:235],v[216:219],v[244:247],a[232:235]\n"
"ds_read_b64_tr_b16 v[178:179],v102 offset:1024\n"
"v_mfma_f32_16x16x32_bf16 a[240:243],v[216:219],v[248:251],a[240:243]\n"
"ds_read_b64_tr_b16 v[180:181],v103 offset:0\n"
"v_mfma_f32_16x16x32_bf16 a[244:247],v[220:223],v[248:251],a[244:247]\n"
"ds_read_b64_tr_b16 v[182:183],v103 offset:1024\n"
"v_mfma_f32_16x16x32_bf16 a[252:255],v[220:223],v[252:255],a[252:255]\n"
"ds_read_b64_tr_b16 v[184:185],v104 offset:0\n"
"v_mfma_f32_16x16x32_bf16 a[248:251],v[216:219],v[252:255],a[248:251]\n"
"ds_read_b64_tr_b16 v[186:187],v104 offset:1024\n"
"s_waitcnt lgkmcnt(10)\n"
"v_mfma_f32_16x16x32_bf16 a[192:195],v[152:155],v[160:163],a[192:195]\n"
"ds_read_b64_tr_b16 v[188:189],v105 offset:0\n"
"v_mfma_f32_16x16x32_bf16 a[196:199],v[156:159],v[160:163],a[196:199]\n"
"ds_read_b64_tr_b16 v[190:191],v105 offset:1024\n"
"v_mfma_f32_16x16x32_bf16 a[204:207],v[156:159],v[164:167],a[204:207]\n"
"ds_read_b128 v[128:131],v80 offset:32768\n"
"v_mfma_f32_16x16x32_bf16 a[200:203],v[152:155],v[164:167],a[200:203]\n"
"ds_read_b128 v[192:195],v81 offset:32768\n"
"s_waitcnt lgkmcnt(10)\n"
"v_mfma_f32_16x16x32_bf16 a[208:211],v[152:155],v[168:171],a[208:211]\n"
"ds_read_b64_tr_b16 v[224:225],v98 offset:16384\n"
"v_mfma_f32_16x16x32_bf16 a[212:215],v[156:159],v[168:171],a[212:215]\n"
"ds_read_b64_tr_b16 v[226:227],v98 offset:17408\n"
"v_mfma_f32_16x16x32_bf16 a[220:223],v[156:159],v[172:175],a[220:223]\n"
"ds_read_b64_tr_b16 v[228:229],v99 offset:16384\n"
"v_mfma_f32_16x16x32_bf16 a[216:219],v[152:155],v[172:175],a[216:219]\n"
"ds_read_b64_tr_b16 v[230:231],v99 offset:17408\n"
"s_waitcnt lgkmcnt(10)\n"
"v_mfma_f32_16x16x32_bf16 a[224:227],v[152:155],v[176:179],a[224:227]\n"
"ds_read_b64_tr_b16 v[232:233],v100 offset:16384\n"
"v_mfma_f32_16x16x32_bf16 a[228:231],v[156:159],v[176:179],a[228:231]\n"
"ds_read_b64_tr_b16 v[234:235],v100 offset:17408\n"
"v_mfma_f32_16x16x32_bf16 a[236:239],v[156:159],v[180:183],a[236:239]\n"
"ds_read_b64_tr_b16 v[236:237],v101 offset:16384\n"
"v_mfma_f32_16x16x32_bf16 a[232:235],v[152:155],v[180:183],a[232:235]\n"
"ds_read_b64_tr_b16 v[238:239],v101 offset:17408\n"
"s_waitcnt lgkmcnt(10)\n"
"v_mfma_f32_16x16x32_bf16 a[240:243],v[152:155],v[184:187],a[240:243]\n"
"ds_read_b64_tr_b16 v[240:241],v102 offset:16384\n"
"v_mfma_f32_16x16x32_bf16 a[244:247],v[156:159],v[184:187],a[244:247]\n"
"ds_read_b64_tr_b16 v[242:243],v102 offset:17408\n"
"v_mfma_f32_16x16x32_bf16 a[252:255],v[156:159],v[188:191],a[252:255]\n"
"ds_read_b64_tr_b16 v[244:245],v103 offset:16384\n"
"v_mfma_f32_16x16x32_bf16 a[248:251],v[152:155],v[188:191],a[248:251]\n"
"ds_read_b64_tr_b16 v[246:247],v103 offset:17408\n"
"s_waitcnt lgkmcnt(8)\n"
"v_mfma_f32_16x16x32_bf16 a[0:3],v[128:131],v[160:163],a[0:3]\n"
"ds_read_b64_tr_b16 v[248:249],v104 offset:16384\n"
"v_mfma_f32_16x16x32_bf16 a[0:3],v[192:195],v[224:227],a[0:3]\n"
"ds_read_b64_tr_b16 v[250:251],v104 offset:17408\n"
"v_mfma_f32_16x16x32_bf16 a[8:11],v[192:195],v[228:231],a[8:11]\n"
"ds_read_b64_tr_b16 v[252:253],v105 offset:16384\n"
"v_mfma_f32_16x16x32_bf16 a[8:11],v[128:131],v[164:167],a[8:11]\n"
"ds_read_b64_tr_b16 v[254:255],v105 offset:17408\n"
"s_waitcnt lgkmcnt(8)\n"
"v_mfma_f32_16x16x32_bf16 a[16:19],v[128:131],v[168:171],a[16:19]\n"
"ds_read_b128 v[132:135],v80 offset:34816\n"
"v_mfma_f32_16x16x32_bf16 a[16:19],v[192:195],v[232:235],a[16:19]\n"
"ds_read_b128 v[196:199],v81 offset:34816\n"
"v_mfma_f32_16x16x32_bf16 a[24:27],v[192:195],v[236:239],a[24:27]\n"
"ds_read_b128 v[136:139],v80 offset:36864\n"
"v_mfma_f32_16x16x32_bf16 a[24:27],v[128:131],v[172:175],a[24:27]\n"
"ds_read_b128 v[200:203],v81 offset:36864\n"
"s_waitcnt lgkmcnt(8)\n"
"v_mfma_f32_16x16x32_bf16 a[32:35],v[128:131],v[176:179],a[32:35]\n"
"s_and_b32 s82,s52,1\n"
"ds_read_b128 v[140:143],v80 offset:38912\n"
"s_lshl_b32 s82,s82,13\n"
"v_mfma_f32_16x16x32_bf16 a[32:35],v[192:195],v[240:243],a[32:35]\n"
"ds_read_b128 v[204:207],v81 offset:38912\n"
"v_mfma_f32_16x16x32_bf16 a[40:43],v[192:195],v[244:247],a[40:43]\n"
"ds_read_b128 v[144:147],v80 offset:40960\n"
"v_mfma_f32_16x16x32_bf16 a[40:43],v[128:131],v[180:183],a[40:43]\n"
"ds_read_b128 v[208:211],v81 offset:40960\n"
"s_waitcnt lgkmcnt(8)\n"
"v_mfma_f32_16x16x32_bf16 a[48:51],v[128:131],v[184:187],a[48:51]\n"
"ds_read_b128 v[148:151],v80 offset:43008\n"
"v_mfma_f32_16x16x32_bf16 a[48:51],v[192:195],v[248:251],a[48:51]\n"
"ds_read_b128 v[212:215],v81 offset:43008\n"
"v_subrev_u32 v98,s82,v98\n"
"v_mfma_f32_16x16x32_bf16 a[56:59],v[192:195],v[252:255],a[56:59]\n"
"ds_read_b128 v[216:219],v81 offset:45056\n"
"v_add_u32 v96,s78,v97\n"
"v_mfma_f32_16x16x32_bf16 a[56:59],v[128:131],v[188:191],a[56:59]\n"
"ds_read_b128 v[220:223],v81 offset:47104\n"
"s_waitcnt lgkmcnt(10)\n"
"v_mfma_f32_16x16x32_bf16 a[4:7],v[132:135],v[160:163],a[4:7]\n"
"v_add_u32 v97,65536,v96\n"
"v_mfma_f32_16x16x32_bf16 a[4:7],v[196:199],v[224:227],a[4:7]\n"
"v_subrev_u32 v99,s82,v99\n"
"v_mfma_f32_16x16x32_bf16 a[12:15],v[196:199],v[228:231],a[12:15]\n"
"v_mfma_f32_16x16x32_bf16 a[12:15],v[132:135],v[164:167],a[12:15]\n"
"v_mfma_f32_16x16x32_bf16 a[20:23],v[132:135],v[168:171],a[20:23]\n"
"s_waitcnt lgkmcnt(0)\n"
"s_barrier\n"
"v_mfma_f32_16x16x32_bf16 a[20:23],v[196:199],v[232:235],a[20:23]\n"
"v_subrev_u32 v100,s82,v100\n"
"s_add_u32 m0,s85,98304\n"
"v_mfma_f32_16x16x32_bf16 a[28:31],v[196:199],v[236:239],a[28:31]\n"
"buffer_load_dwordx4 v110,s[28:31],s86 offen lds\n"
"v_mfma_f32_16x16x32_bf16 a[28:31],v[132:135],v[172:175],a[28:31]\n"
"v_mfma_f32_16x16x32_bf16 a[36:39],v[132:135],v[176:179],a[36:39]\n"
"v_mfma_f32_16x16x32_bf16 a[36:39],v[196:199],v[240:243],a[36:39]\n"
"v_subrev_u32 v101,s82,v101\n"
"s_add_u32 m0,s85,99328\n"
"v_mfma_f32_16x16x32_bf16 a[44:47],v[196:199],v[244:247],a[44:47]\n"
"buffer_load_dwordx4 v111,s[28:31],s86 offen lds\n"
"v_mfma_f32_16x16x32_bf16 a[44:47],v[132:135],v[180:183],a[44:47]\n"
"s_xor_b32 s86,s86,262144\n"
"v_mfma_f32_16x16x32_bf16 a[52:55],v[132:135],v[184:187],a[52:55]\n"
"v_mfma_f32_16x16x32_bf16 a[52:55],v[196:199],v[248:251],a[52:55]\n"
"v_subrev_u32 v102,s82,v102\n"
"s_add_u32 m0,s78,32768\n"
"v_mfma_f32_16x16x32_bf16 a[60:63],v[196:199],v[252:255],a[60:63]\n"
"buffer_load_dwordx4 v112,s[24:27],s76 offen offset:0 sc1 lds\n"
"v_mfma_f32_16x16x32_bf16 a[60:63],v[132:135],v[188:191],a[60:63]\n"
"v_mfma_f32_16x16x32_bf16 a[64:67],v[136:139],v[160:163],a[64:67]\n"
"v_mfma_f32_16x16x32_bf16 a[64:67],v[200:203],v[224:227],a[64:67]\n"
"v_subrev_u32 v103,s82,v103\n"
"v_mfma_f32_16x16x32_bf16 a[72:75],v[200:203],v[228:231],a[72:75]\n"
"buffer_load_dwordx4 v112,s[24:27],s76 offen offset:1024 sc1 lds\n"
"v_mfma_f32_16x16x32_bf16 a[72:75],v[136:139],v[164:167],a[72:75]\n"
"v_mfma_f32_16x16x32_bf16 a[80:83],v[136:139],v[168:171],a[80:83]\n"
"v_accvgpr_read_b32 v116,a0\n"
"v_accvgpr_read_b32 v117,a1\n"
"v_mfma_f32_16x16x32_bf16 a[80:83],v[200:203],v[232:235],a[80:83]\n"
"v_subrev_u32 v104,s82,v104\n"
"v_accvgpr_read_b32 v118,a2\n"
"v_mfma_f32_16x16x32_bf16 a[88:91],v[200:203],v[236:239],a[88:91]\n"
"buffer_load_dwordx4 v112,s[24:27],s76 offen offset:2048 sc1 lds\n"
"v_mfma_f32_16x16x32_bf16 a[88:91],v[136:139],v[172:175],a[88:91]\n"
"v_accvgpr_read_b32 v119,a3\n"
"v_accvgpr_read_b32 v120,a4\n"
"v_mfma_f32_16x16x32_bf16 a[96:99],v[136:139],v[176:179],a[96:99]\n"
"v_accvgpr_read_b32 v121,a5\n"
"v_accvgpr_read_b32 v122,a6\n"
"v_mfma_f32_16x16x32_bf16 a[96:99],v[200:203],v[240:243],a[96:99]\n"
"v_subrev_u32 v105,s82,v105\n"
"v_accvgpr_read_b32 v123,a7\n"
"v_mfma_f32_16x16x32_bf16 a[104:107],v[200:203],v[244:247],a[104:107]\n"
"buffer_load_dwordx4 v112,s[24:27],s76 offen offset:3072 sc1 lds\n"
"v_mfma_f32_16x16x32_bf16 a[104:107],v[136:139],v[180:183],a[104:107]\n"
"s_xor_b32 s76,s76,16384\n"
"v_cvt_pk_bf16_f32 v116,v116,v117\n"
"v_cvt_pk_bf16_f32 v117,v118,v119\n"
"v_mfma_f32_16x16x32_bf16 a[112:115],v[136:139],v[184:187],a[112:115]\n"
"v_cvt_pk_bf16_f32 v118,v120,v121\n"
"v_cvt_pk_bf16_f32 v119,v122,v123\n"
"v_mfma_f32_16x16x32_bf16 a[112:115],v[200:203],v[248:251],a[112:115]\n"
"buffer_store_dwordx4 v[116:119],v112,s[24:27],s56 offen offset:0 sc1\n"
"v_mfma_f32_16x16x32_bf16 a[120:123],v[200:203],v[252:255],a[120:123]\n"
"s_nop 0\n"
"v_accvgpr_read_b32 v116,a8\n"
"v_accvgpr_read_b32 v117,a9\n"
"v_accvgpr_read_b32 v118,a10\n"
"v_mfma_f32_16x16x32_bf16 a[120:123],v[136:139],v[188:191],a[120:123]\n"
"s_waitcnt vmcnt(20)\n"
"v_accvgpr_read_b32 v119,a11\n"
"v_accvgpr_read_b32 v120,a12\n"
"s_barrier\n"
"v_mfma_f32_16x16x32_bf16 a[68:71],v[140:143],v[160:163],a[68:71]\n"
"ds_read_b128 v[0:3],v97 offset:0\n"
"v_accvgpr_read_b32 v121,a13\n"
"v_mfma_f32_16x16x32_bf16 a[68:71],v[204:207],v[224:227],a[68:71]\n"
"ds_read_b128 v[8:11],v97 offset:1024\n"
"v_accvgpr_read_b32 v122,a14\n"
"v_mfma_f32_16x16x32_bf16 a[76:79],v[204:207],v[228:231],a[76:79]\n"
"ds_read_b128 v[16:19],v97 offset:2048\n"
"v_accvgpr_read_b32 v123,a15\n"
"v_mfma_f32_16x16x32_bf16 a[76:79],v[140:143],v[164:167],a[76:79]\n"
"ds_read_b128 v[24:27],v97 offset:3072\n"
"v_cvt_pk_bf16_f32 v116,v116,v117\n"
"v_mfma_f32_16x16x32_bf16 a[84:87],v[140:143],v[168:171],a[84:87]\n"
"ds_read_b128 v[4:7],v97 offset:16384\n"
"v_cvt_pk_bf16_f32 v117,v118,v119\n"
"v_mfma_f32_16x16x32_bf16 a[84:87],v[204:207],v[232:235],a[84:87]\n"
"ds_read_b128 v[12:15],v97 offset:17408\n"
"v_cvt_pk_bf16_f32 v118,v120,v121\n"
"v_mfma_f32_16x16x32_bf16 a[92:95],v[204:207],v[236:239],a[92:95]\n"
"ds_read_b128 v[20:23],v97 offset:18432\n"
"v_cvt_pk_bf16_f32 v119,v122,v123\n"
"v_mfma_f32_16x16x32_bf16 a[92:95],v[140:143],v[172:175],a[92:95]\n"
"ds_read_b128 v[28:31],v97 offset:19456\n"
"buffer_store_dwordx4 v[116:119],v112,s[24:27],s56 offen offset:1024 sc1\n"
"v_mfma_f32_16x16x32_bf16 a[100:103],v[140:143],v[176:179],a[100:103]\n"
"ds_read_b64_tr_b16 v[32:33],v98 offset:49152\n"
"v_accvgpr_read_b32 v116,a16\n"
"v_mfma_f32_16x16x32_bf16 a[100:103],v[204:207],v[240:243],a[100:103]\n"
"ds_read_b64_tr_b16 v[34:35],v98 offset:50176\n"
"v_accvgpr_read_b32 v117,a17\n"
"v_mfma_f32_16x16x32_bf16 a[108:111],v[204:207],v[244:247],a[108:111]\n"
"ds_read_b64_tr_b16 v[36:37],v98 offset:57344\n"
"v_accvgpr_read_b32 v118,a18\n"
"v_mfma_f32_16x16x32_bf16 a[108:111],v[140:143],v[180:183],a[108:111]\n"
"ds_read_b64_tr_b16 v[38:39],v98 offset:58368\n"
"v_accvgpr_read_b32 v119,a19\n"
"v_mfma_f32_16x16x32_bf16 a[116:119],v[140:143],v[184:187],a[116:119]\n"
"ds_read_b64_tr_b16 v[40:41],v99 offset:49152\n"
"v_accvgpr_read_b32 v120,a20\n"
"v_mfma_f32_16x16x32_bf16 a[116:119],v[204:207],v[248:251],a[116:119]\n"
"ds_read_b64_tr_b16 v[42:43],v99 offset:50176\n"
"v_accvgpr_read_b32 v121,a21\n"
"v_mfma_f32_16x16x32_bf16 a[124:127],v[204:207],v[252:255],a[124:127]\n"
"ds_read_b64_tr_b16 v[44:45],v99 offset:57344\n"
"v_accvgpr_read_b32 v122,a22\n"
"v_mfma_f32_16x16x32_bf16 a[124:127],v[140:143],v[188:191],a[124:127]\n"
"ds_read_b64_tr_b16 v[46:47],v99 offset:58368\n"
"v_accvgpr_read_b32 v123,a23\n"
"v_mfma_f32_16x16x32_bf16 a[128:131],v[144:147],v[160:163],a[128:131]\n"
"ds_read_b64_tr_b16 v[48:49],v100 offset:49152\n"
"v_cvt_pk_bf16_f32 v116,v116,v117\n"
"v_mfma_f32_16x16x32_bf16 a[128:131],v[208:211],v[224:227],a[128:131]\n"
"ds_read_b64_tr_b16 v[50:51],v100 offset:50176\n"
"v_cvt_pk_bf16_f32 v117,v118,v119\n"
"v_mfma_f32_16x16x32_bf16 a[136:139],v[208:211],v[228:231],a[136:139]\n"
"ds_read_b64_tr_b16 v[52:53],v100 offset:57344\n"
"v_cvt_pk_bf16_f32 v118,v120,v121\n"
"v_mfma_f32_16x16x32_bf16 a[136:139],v[144:147],v[164:167],a[136:139]\n"
"ds_read_b64_tr_b16 v[54:55],v100 offset:58368\n"
"v_cvt_pk_bf16_f32 v119,v122,v123\n"
"v_mfma_f32_16x16x32_bf16 a[144:147],v[144:147],v[168:171],a[144:147]\n"
"ds_read_b64_tr_b16 v[56:57],v101 offset:49152\n"
"buffer_store_dwordx4 v[116:119],v112,s[24:27],s56 offen offset:2048 sc1\n"
"v_mfma_f32_16x16x32_bf16 a[144:147],v[208:211],v[232:235],a[144:147]\n"
"ds_read_b64_tr_b16 v[58:59],v101 offset:50176\n"
"v_accvgpr_read_b32 v116,a24\n"
"v_mfma_f32_16x16x32_bf16 a[152:155],v[208:211],v[236:239],a[152:155]\n"
"ds_read_b64_tr_b16 v[60:61],v101 offset:57344\n"
"v_accvgpr_read_b32 v117,a25\n"
"v_mfma_f32_16x16x32_bf16 a[152:155],v[144:147],v[172:175],a[152:155]\n"
"ds_read_b64_tr_b16 v[62:63],v101 offset:58368\n"
"v_accvgpr_read_b32 v118,a26\n"
"v_mfma_f32_16x16x32_bf16 a[160:163],v[144:147],v[176:179],a[160:163]\n"
"ds_read_b64_tr_b16 v[64:65],v102 offset:49152\n"
"v_accvgpr_read_b32 v119,a27\n"
"v_mfma_f32_16x16x32_bf16 a[160:163],v[208:211],v[240:243],a[160:163]\n"
"ds_read_b64_tr_b16 v[66:67],v102 offset:50176\n"
"v_accvgpr_read_b32 v120,a28\n"
"v_mfma_f32_16x16x32_bf16 a[168:171],v[208:211],v[244:247],a[168:171]\n"
"ds_read_b64_tr_b16 v[68:69],v102 offset:57344\n"
"v_accvgpr_read_b32 v121,a29\n"
"v_mfma_f32_16x16x32_bf16 a[168:171],v[144:147],v[180:183],a[168:171]\n"
"ds_read_b64_tr_b16 v[70:71],v102 offset:58368\n"
"v_accvgpr_read_b32 v122,a30\n"
"v_mfma_f32_16x16x32_bf16 a[176:179],v[144:147],v[184:187],a[176:179]\n"
"ds_read_b64_tr_b16 v[72:73],v103 offset:49152\n"
"v_accvgpr_read_b32 v123,a31\n"
"v_mfma_f32_16x16x32_bf16 a[176:179],v[208:211],v[248:251],a[176:179]\n"
"ds_read_b64_tr_b16 v[74:75],v103 offset:50176\n"
"v_cvt_pk_bf16_f32 v116,v116,v117\n"
"v_mfma_f32_16x16x32_bf16 a[184:187],v[208:211],v[252:255],a[184:187]\n"
"ds_read_b64_tr_b16 v[76:77],v103 offset:57344\n"
"v_cvt_pk_bf16_f32 v117,v118,v119\n"
"v_mfma_f32_16x16x32_bf16 a[184:187],v[144:147],v[188:191],a[184:187]\n"
"ds_read_b64_tr_b16 v[78:79],v103 offset:58368\n"
"v_cvt_pk_bf16_f32 v118,v120,v121\n"
"v_mfma_f32_16x16x32_bf16 a[132:135],v[148:151],v[160:163],a[132:135]\n"
"ds_read_b64_tr_b16 v[80:81],v104 offset:49152\n"
"v_cvt_pk_bf16_f32 v119,v122,v123\n"
"v_mfma_f32_16x16x32_bf16 a[132:135],v[212:215],v[224:227],a[132:135]\n"
"ds_read_b64_tr_b16 v[82:83],v104 offset:50176\n"
"buffer_store_dwordx4 v[116:119],v112,s[24:27],s56 offen offset:3072 sc1\n"
"v_mfma_f32_16x16x32_bf16 a[140:143],v[212:215],v[228:231],a[140:143]\n"
"ds_read_b64_tr_b16 v[84:85],v104 offset:57344\n"
"v_mfma_f32_16x16x32_bf16 a[140:143],v[148:151],v[164:167],a[140:143]\n"
"ds_read_b64_tr_b16 v[86:87],v104 offset:58368\n"
"v_mfma_f32_16x16x32_bf16 a[148:151],v[148:151],v[168:171],a[148:151]\n"
"ds_read_b64_tr_b16 v[88:89],v105 offset:49152\n"
"v_mfma_f32_16x16x32_bf16 a[148:151],v[212:215],v[232:235],a[148:151]\n"
"ds_read_b64_tr_b16 v[90:91],v105 offset:50176\n"
"v_mfma_f32_16x16x32_bf16 a[156:159],v[212:215],v[236:239],a[156:159]\n"
"ds_read_b64_tr_b16 v[92:93],v105 offset:57344\n"
"v_mfma_f32_16x16x32_bf16 a[156:159],v[148:151],v[172:175],a[156:159]\n"
"ds_read_b64_tr_b16 v[94:95],v105 offset:58368\n"
"v_mfma_f32_16x16x32_bf16 a[164:167],v[148:151],v[176:179],a[164:167]\n"
"s_add_u32 m0,s85,106496\n"
"v_mfma_f32_16x16x32_bf16 a[164:167],v[212:215],v[240:243],a[164:167]\n"
"buffer_load_dwordx4 v110,s[28:31],s86 offen lds\n"
"v_mfma_f32_16x16x32_bf16 a[172:175],v[212:215],v[244:247],a[172:175]\n"
"v_mfma_f32_16x16x32_bf16 a[172:175],v[148:151],v[180:183],a[172:175]\n"
"s_add_u32 m0,s85,107520\n"
"v_mfma_f32_16x16x32_bf16 a[180:183],v[148:151],v[184:187],a[180:183]\n"
"buffer_load_dwordx4 v111,s[28:31],s86 offen lds\n"
"v_mfma_f32_16x16x32_bf16 a[180:183],v[212:215],v[248:251],a[180:183]\n"
"s_xor_b32 s86,s86,786432\n"
"v_mfma_f32_16x16x32_bf16 a[188:191],v[212:215],v[252:255],a[188:191]\n"
"s_add_u32 m0,s78,49152\n"
"v_mfma_f32_16x16x32_bf16 a[188:191],v[148:151],v[188:191],a[188:191]\n"
"buffer_load_dwordx4 v112,s[24:27],s76 offen offset:0 sc1 lds\n"
"v_mfma_f32_16x16x32_bf16 a[192:195],v[216:219],v[224:227],a[192:195]\n"
"v_mfma_f32_16x16x32_bf16 a[196:199],v[220:223],v[224:227],a[196:199]\n"
"v_mfma_f32_16x16x32_bf16 a[204:207],v[220:223],v[228:231],a[204:207]\n"
"buffer_load_dwordx4 v112,s[24:27],s76 offen offset:1024 sc1 lds\n"
"v_mfma_f32_16x16x32_bf16 a[200:203],v[216:219],v[228:231],a[200:203]\n"
"v_mfma_f32_16x16x32_bf16 a[208:211],v[216:219],v[232:235],a[208:211]\n"
"v_mfma_f32_16x16x32_bf16 a[212:215],v[220:223],v[232:235],a[212:215]\n"
"buffer_load_dwordx4 v112,s[24:27],s76 offen offset:2048 sc1 lds\n"
"v_mfma_f32_16x16x32_bf16 a[220:223],v[220:223],v[236:239],a[220:223]\n"
"v_mfma_f32_16x16x32_bf16 a[216:219],v[216:219],v[236:239],a[216:219]\n"
"buffer_load_dwordx4 v112,s[24:27],s76 offen offset:3072 sc1 lds\n"
"v_mfma_f32_16x16x32_bf16 a[224:227],v[216:219],v[240:243],a[224:227]\n"
"s_xor_b32 s76,s76,49152\n"
"v_mfma_f32_16x16x32_bf16 a[228:231],v[220:223],v[240:243],a[228:231]\n"
"v_mfma_f32_16x16x32_bf16 a[236:239],v[220:223],v[244:247],a[236:239]\n"
"v_mfma_f32_16x16x32_bf16 a[232:235],v[216:219],v[244:247],a[232:235]\n"
"v_mfma_f32_16x16x32_bf16 a[240:243],v[216:219],v[248:251],a[240:243]\n"
"v_mfma_f32_16x16x32_bf16 a[244:247],v[220:223],v[248:251],a[244:247]\n"
"v_mfma_f32_16x16x32_bf16 a[252:255],v[220:223],v[252:255],a[252:255]\n"
"v_mfma_f32_16x16x32_bf16 a[248:251],v[216:219],v[252:255],a[248:251]\n"
".Lfk_ph2:\n"
"s_waitcnt vmcnt(25) lgkmcnt(0)\n"
"s_barrier\n"
"v_mfma_f32_16x16x32_bf16 v[128:131],v[32:35],v[0:3],0\n"
"v_accvgpr_read_b32 v116,a32\n"
"v_accvgpr_read_b32 v117,a33\n"
"v_mfma_f32_16x16x32_bf16 v[128:131],v[36:39],v[4:7],v[128:131]\n"
"v_accvgpr_read_b32 v118,a34\n"
"v_accvgpr_read_b32 v119,a35\n"
"v_mfma_f32_16x16x32_bf16 v[160:163],v[36:39],v[12:15],0\n"
"v_accvgpr_read_b32 v120,a36\n"
"v_accvgpr_read_b32 v121,a37\n"
"v_mfma_f32_16x16x32_bf16 v[160:163],v[32:35],v[8:11],v[160:163]\n"
"s_add_u32 m0,s85,147456\n"
"v_accvgpr_read_b32 v122,a38\n"
"v_mfma_f32_16x16x32_bf16 v[192:195],v[32:35],v[16:19],0\n"
"buffer_load_dwordx4 v110,s[28:31],s86 offen lds\n"
"v_mfma_f32_16x16x32_bf16 v[192:195],v[36:39],v[20:23],v[192:195]\n"
"v_accvgpr_read_b32 v123,a39\n"
"v_cvt_pk_bf16_f32 v116,v116,v117\n"
"v_mfma_f32_16x16x32_bf16 v[224:227],v[36:39],v[28:31],0\n"
"v_cvt_pk_bf16_f32 v117,v118,v119\n"
"v_cvt_pk_bf16_f32 v118,v120,v121\n"
"v_mfma_f32_16x16x32_bf16 v[224:227],v[32:35],v[24:27],v[224:227]\n"
"v_cvt_pk_bf16_f32 v119,v122,v123\n"
"s_add_u32 s56,s56,4096\n"
"v_mfma_f32_16x16x32_bf16 v[228:231],v[40:43],v[24:27],0\n"
"ds_read_b64_tr_b16 v[32:33],v100 offset:32768\n"
"buffer_store_dwordx4 v[116:119],v112,s[24:27],s56 offen offset:0 sc1\n"
"v_mfma_f32_16x16x32_bf16 v[228:231],v[44:47],v[28:31],v[228:231]\n"
"ds_read_b64_tr_b16 v[34:35],v100 offset:33792\n"
"v_accvgpr_read_b32 v116,a40\n"
"v_mfma_f32_16x16x32_bf16 v[196:199],v[44:47],v[20:23],0\n"
"ds_read_b64_tr_b16 v[36:37],v100 offset:40960\n"
"v_accvgpr_read_b32 v117,a41\n"
"v_mfma_f32_16x16x32_bf16 v[196:199],v[40:43],v[16:19],v[196:199]\n"
"s_add_u32 m0,s85,148480\n"
"ds_read_b64_tr_b16 v[38:39],v100 offset:41984\n"
"v_mfma_f32_16x16x32_bf16 v[164:167],v[40:43],v[8:11],0\n"
"buffer_load_dwordx4 v111,s[28:31],s86 offen lds\n"
"v_mfma_f32_16x16x32_bf16 v[164:167],v[44:47],v[12:15],v[164:167]\n"
"s_add_u32 m0,s85,155648\n"
"s_xor_b32 s86,s86,262144\n"
"v_mfma_f32_16x16x32_bf16 v[132:135],v[44:47],v[4:7],0\n"
"buffer_load_dwordx4 v110,s[28:31],s86 offen lds\n"
"v_mfma_f32_16x16x32_bf16 v[132:135],v[40:43],v[0:3],v[132:135]\n"
"v_accvgpr_read_b32 v118,a42\n"
"v_accvgpr_read_b32 v119,a43\n"
"v_mfma_f32_16x16x32_bf16 v[136:139],v[48:51],v[0:3],0\n"
"ds_read_b64_tr_b16 v[40:41],v101 offset:32768\n"
"v_accvgpr_read_b32 v120,a44\n"
"v_mfma_f32_16x16x32_bf16 v[136:139],v[52:55],v[4:7],v[136:139]\n"
"ds_read_b64_tr_b16 v[42:43],v101 offset:33792\n"
"v_accvgpr_read_b32 v121,a45\n"
"v_mfma_f32_16x16x32_bf16 v[168:171],v[52:55],v[12:15],0\n"
"ds_read_b64_tr_b16 v[44:45],v101 offset:40960\n"
"v_accvgpr_read_b32 v122,a46\n"
"v_mfma_f32_16x16x32_bf16 v[168:171],v[48:51],v[8:11],v[168:171]\n"
"s_add_u32 m0,s85,156672\n"
"ds_read_b64_tr_b16 v[46:47],v101 offset:41984\n"
"v_mfma_f32_16x16x32_bf16 v[200:203],v[48:51],v[16:19],0\n"
"buffer_load_dwordx4 v111,s[28:31],s86 offen lds\n"
"v_mfma_f32_16x16x32_bf16 v[200:203],v[52:55],v[20:23],v[200:203]\n"
"s_xor_b32 s86,s86,3932160\n"
"v_accvgpr_read_b32 v123,a47\n"
"v_mfma_f32_16x16x32_bf16 v[232:235],v[52:55],v[28:31],0\n"
"v_cvt_pk_bf16_f32 v116,v116,v117\n"
"v_cvt_pk_bf16_f32 v117,v118,v119\n"
"v_mfma_f32_16x16x32_bf16 v[232:235],v[48:51],v[24:27],v[232:235]\n"
"v_cvt_pk_bf16_f32 v118,v120,v121\n"
"v_cvt_pk_bf16_f32 v119,v122,v123\n"
"v_mfma_f32_16x16x32_bf16 v[236:239],v[56:59],v[24:27],0\n"
"ds_read_b64_tr_b16 v[48:49],v102 offset:32768\n"
"buffer_store_dwordx4 v[116:119],v112,s[24:27],s56 offen offset:1024 sc1\n"
"v_mfma_f32_16x16x32_bf16 v[236:239],v[60:63],v[28:31],v[236:239]\n"
"ds_read_b64_tr_b16 v[50:51],v102 offset:33792\n"
"v_accvgpr_read_b32 v116,a48\n"
"v_mfma_f32_16x16x32_bf16 v[204:207],v[60:63],v[20:23],0\n"
"ds_read_b64_tr_b16 v[52:53],v102 offset:40960\n"
"v_accvgpr_read_b32 v117,a49\n"
"v_mfma_f32_16x16x32_bf16 v[204:207],v[56:59],v[16:19],v[204:207]\n"
"s_add_u32 m0,s78,65536\n"
"ds_read_b64_tr_b16 v[54:55],v102 offset:41984\n"
"v_mfma_f32_16x16x32_bf16 v[172:175],v[56:59],v[8:11],0\n"
"buffer_load_dwordx4 v112,s[24:27],s76 offen offset:0 sc1 lds\n"
"v_mfma_f32_16x16x32_bf16 v[172:175],v[60:63],v[12:15],v[172:175]\n"
"v_accvgpr_read_b32 v118,a50\n"
"v_mfma_f32_16x16x32_bf16 v[140:143],v[60:63],v[4:7],0\n"
"buffer_load_dwordx4 v112,s[24:27],s76 offen offset:1024 sc1 lds\n"
"v_mfma_f32_16x16x32_bf16 v[140:143],v[56:59],v[0:3],v[140:143]\n"
"s_waitcnt vmcnt(25)\n"
"v_mfma_f32_16x16x32_bf16 v[144:147],v[64:67],v[0:3],0\n"
"ds_read_b64_tr_b16 a[32:33],v98 offset:32768\n"
"ds_read_b64_tr_b16 v[56:57],v103 offset:32768\n"
"v_mfma_f32_16x16x32_bf16 v[144:147],v[68:71],v[4:7],v[144:147]\n"
"ds_read_b64_tr_b16 a[34:35],v98 offset:33792\n"
"ds_read_b64_tr_b16 v[58:59],v103 offset:33792\n"
"v_mfma_f32_16x16x32_bf16 v[176:179],v[68:71],v[12:15],0\n"
"ds_read_b64_tr_b16 a[36:37],v98 offset:40960\n"
"ds_read_b64_tr_b16 v[60:61],v103 offset:40960\n"
"v_mfma_f32_16x16x32_bf16 v[176:179],v[64:67],v[8:11],v[176:179]\n"
"ds_read_b64_tr_b16 a[38:39],v98 offset:41984\n"
"ds_read_b64_tr_b16 v[62:63],v103 offset:41984\n"
"v_mfma_f32_16x16x32_bf16 v[208:211],v[64:67],v[16:19],0\n"
"buffer_load_dwordx4 v112,s[24:27],s76 offen offset:2048 sc1 lds\n"
"v_mfma_f32_16x16x32_bf16 v[208:211],v[68:71],v[20:23],v[208:211]\n"
"ds_read_b128 a[0:3],v96 offset:0\n"
"v_accvgpr_read_b32 v119,a51\n"
"v_mfma_f32_16x16x32_bf16 v[240:243],v[68:71],v[28:31],0\n"
"v_accvgpr_read_b32 v120,a52\n"
"v_accvgpr_read_b32 v121,a53\n"
"v_mfma_f32_16x16x32_bf16 v[240:243],v[64:67],v[24:27],v[240:243]\n"
"ds_read_b128 a[8:11],v96 offset:1024\n"
"v_accvgpr_read_b32 v122,a54\n"
"v_mfma_f32_16x16x32_bf16 v[244:247],v[72:75],v[24:27],0\n"
"ds_read_b64_tr_b16 a[40:41],v99 offset:32768\n"
"ds_read_b64_tr_b16 v[64:65],v104 offset:32768\n"
"v_mfma_f32_16x16x32_bf16 v[244:247],v[76:79],v[28:31],v[244:247]\n"
"ds_read_b64_tr_b16 a[42:43],v99 offset:33792\n"
"ds_read_b64_tr_b16 v[66:67],v104 offset:33792\n"
"v_mfma_f32_16x16x32_bf16 v[212:215],v[76:79],v[20:23],0\n"
"ds_read_b64_tr_b16 a[44:45],v99 offset:40960\n"
"ds_read_b64_tr_b16 v[68:69],v104 offset:40960\n"
"v_mfma_f32_16x16x32_bf16 v[212:215],v[72:75],v[16:19],v[212:215]\n"
"ds_read_b64_tr_b16 a[46:47],v99 offset:41984\n"
"ds_read_b64_tr_b16 v[70:71],v104 offset:41984\n"
"v_mfma_f32_16x16x32_bf16 v[180:183],v[72:75],v[8:11],0\n"
"buffer_load_dwordx4 v112,s[24:27],s76 offen offset:3072 sc1 lds\n"
"v_mfma_f32_16x16x32_bf16 v[180:183],v[76:79],v[12:15],v[180:183]\n"
"s_add_u32 m0,s78,81920\n"
"s_xor_b32 s76,s76,16384\n"
"ds_read_b128 a[16:19],v96 offset:2048\n"
"v_mfma_f32_16x16x32_bf16 v[148:151],v[76:79],v[4:7],0\n"
"buffer_load_dwordx4 v112,s[24:27],s76 offen offset:0 sc1 lds\n"
"v_mfma_f32_16x16x32_bf16 v[148:151],v[72:75],v[0:3],v[148:151]\n"
"ds_read_b128 a[24:27],v96 offset:3072\n"
"v_accvgpr_read_b32 v123,a55\n"
"v_mfma_f32_16x16x32_bf16 v[152:155],v[80:83],v[0:3],0\n"
"ds_read_b64_tr_b16 v[72:73],v105 offset:32768\n"
"v_cvt_pk_bf16_f32 v116,v116,v117\n"
"v_mfma_f32_16x16x32_bf16 v[152:155],v[84:87],v[4:7],v[152:155]\n"
"ds_read_b64_tr_b16 v[74:75],v105 offset:33792\n"
"v_cvt_pk_bf16_f32 v117,v118,v119\n"
"v_mfma_f32_16x16x32_bf16 v[184:187],v[84:87],v[12:15],0\n"
"ds_read_b64_tr_b16 v[76:77],v105 offset:40960\n"
"v_cvt_pk_bf16_f32 v118,v120,v121\n"
"v_mfma_f32_16x16x32_bf16 v[184:187],v[80:83],v[8:11],v[184:187]\n"
"ds_read_b64_tr_b16 v[78:79],v105 offset:41984\n"
"v_cvt_pk_bf16_f32 v119,v122,v123\n"
"v_mfma_f32_16x16x32_bf16 v[216:219],v[80:83],v[16:19],0\n"
"ds_read_b128 a[4:7],v96 offset:16384\n"
"buffer_store_dwordx4 v[116:119],v112,s[24:27],s56 offen offset:2048 sc1\n"
"v_mfma_f32_16x16x32_bf16 v[216:219],v[84:87],v[20:23],v[216:219]\n"
"ds_read_b128 a[12:15],v96 offset:17408\n"
"v_accvgpr_read_b32 v116,a56\n"
"v_mfma_f32_16x16x32_bf16 v[248:251],v[84:87],v[28:31],0\n"
"ds_read_b128 a[20:23],v96 offset:18432\n"
"v_accvgpr_read_b32 v117,a57\n"
"v_mfma_f32_16x16x32_bf16 v[248:251],v[80:83],v[24:27],v[248:251]\n"
"ds_read_b128 a[28:31],v96 offset:19456\n"
"v_accvgpr_read_b32 v118,a58\n"
"v_mfma_f32_16x16x32_bf16 v[252:255],v[88:91],v[24:27],0\n"
"buffer_load_dwordx4 v112,s[24:27],s76 offen offset:1024 sc1 lds\n"
"v_mfma_f32_16x16x32_bf16 v[252:255],v[92:95],v[28:31],v[252:255]\n"
"v_accvgpr_read_b32 v119,a59\n"
"v_accvgpr_read_b32 v120,a60\n"
"v_mfma_f32_16x16x32_bf16 v[220:223],v[92:95],v[20:23],0\n"
"v_accvgpr_read_b32 v121,a61\n"
"v_accvgpr_read_b32 v122,a62\n"
"v_mfma_f32_16x16x32_bf16 v[220:223],v[88:91],v[16:19],v[220:223]\n"
"buffer_load_dwordx4 v112,s[24:27],s76 offen offset:2048 sc1 lds\n"
"v_mfma_f32_16x16x32_bf16 v[188:191],v[88:91],v[8:11],0\n"
"v_accvgpr_read_b32 v123,a63\n"
"v_cvt_pk_bf16_f32 v116,v116,v117\n"
"v_mfma_f32_16x16x32_bf16 v[188:191],v[92:95],v[12:15],v[188:191]\n"
"v_cvt_pk_bf16_f32 v117,v118,v119\n"
"v_cvt_pk_bf16_f32 v118,v120,v121\n"
"v_mfma_f32_16x16x32_bf16 v[156:159],v[92:95],v[4:7],0\n"
"buffer_load_dwordx4 v112,s[24:27],s76 offen offset:3072 sc1 lds\n"
"v_mfma_f32_16x16x32_bf16 v[156:159],v[88:91],v[0:3],v[156:159]\n"
"s_xor_b32 s76,s76,245760\n"
"v_cvt_pk_bf16_f32 v119,v122,v123\n"
"s_waitcnt vmcnt(19) lgkmcnt(0)\n"
"s_barrier\n"
"v_mfma_f32_16x16x32_bf16 v[128:131],a[32:35],a[0:3],v[128:131]\n"
"ds_read_b64_tr_b16 v[80:81],v98 offset:0\n"
"ds_read_b64_tr_b16 v[88:89],v99 offset:0\n"
"v_mfma_f32_16x16x32_bf16 v[128:131],a[36:39],a[4:7],v[128:131]\n"
"ds_read_b64_tr_b16 v[82:83],v98 offset:1024\n"
"ds_read_b64_tr_b16 v[90:91],v99 offset:1024\n"
"v_mfma_f32_16x16x32_bf16 v[160:163],a[36:39],a[12:15],v[160:163]\n"
"ds_read_b64_tr_b16 v[84:85],v98 offset:8192\n"
"ds_read_b64_tr_b16 v[92:93],v99 offset:8192\n"
"v_mfma_f32_16x16x32_bf16 v[160:163],a[32:35],a[8:11],v[160:163]\n"
"s_add_u32 m0,s85,131072\n"
"ds_read_b64_tr_b16 v[86:87],v98 offset:9216\n"
"ds_read_b64_tr_b16 v[94:95],v99 offset:9216\n"
"v_mfma_f32_16x16x32_bf16 v[192:195],a[32:35],a[16:19],v[192:195]\n"
"buffer_load_dwordx4 v110,s[28:31],s86 offen lds\n"
"buffer_store_dwordx4 v[116:119],v112,s[24:27],s56 offen offset:3072 sc1\n"
"v_mfma_f32_16x16x32_bf16 v[192:195],a[36:39],a[20:23],v[192:195]\n"
"s_nop 0\n"
"v_accvgpr_read_b32 v116,a64\n"
"v_mfma_f32_16x16x32_bf16 v[224:227],a[36:39],a[28:31],v[224:227]\n"
"v_accvgpr_read_b32 v117,a65\n"
"v_accvgpr_read_b32 v118,a66\n"
"v_mfma_f32_16x16x32_bf16 v[224:227],a[32:35],a[24:27],v[224:227]\n"
"v_accvgpr_read_b32 v119,a67\n"
"v_accvgpr_read_b32 v120,a68\n"
"v_mfma_f32_16x16x32_bf16 v[228:231],a[40:43],a[24:27],v[228:231]\n"
"ds_read_b64_tr_b16 a[32:33],v100 offset:0\n"
"v_accvgpr_read_b32 v121,a69\n"
"v_mfma_f32_16x16x32_bf16 v[228:231],a[44:47],a[28:31],v[228:231]\n"
"ds_read_b64_tr_b16 a[34:35],v100 offset:1024\n"
"v_accvgpr_read_b32 v122,a70\n"
"v_mfma_f32_16x16x32_bf16 v[196:199],a[44:47],a[20:23],v[196:199]\n"
"ds_read_b64_tr_b16 a[36:37],v100 offset:8192\n"
"v_accvgpr_read_b32 v123,a71\n"
"v_mfma_f32_16x16x32_bf16 v[196:199],a[40:43],a[16:19],v[196:199]\n"
"s_add_u32 m0,s85,132096\n"
"ds_read_b64_tr_b16 a[38:39],v100 offset:9216\n"
"v_mfma_f32_16x16x32_bf16 v[164:167],a[40:43],a[8:11],v[164:167]\n"
"buffer_load_dwordx4 v111,s[28:31],s86 offen lds\n"
"v_mfma_f32_16x16x32_bf16 v[164:167],a[44:47],a[12:15],v[164:167]\n"
"s_add_u32 m0,s85,139264\n"
"s_xor_b32 s86,s86,262144\n"
"v_mfma_f32_16x16x32_bf16 v[132:135],a[44:47],a[4:7],v[132:135]\n"
"buffer_load_dwordx4 v110,s[28:31],s86 offen lds\n"
"v_mfma_f32_16x16x32_bf16 v[132:135],a[40:43],a[0:3],v[132:135]\n"
"v_cvt_pk_bf16_f32 v116,v116,v117\n"
"v_cvt_pk_bf16_f32 v117,v118,v119\n"
"v_mfma_f32_16x16x32_bf16 v[136:139],v[32:35],a[0:3],v[136:139]\n"
"ds_read_b64_tr_b16 a[40:41],v101 offset:0\n"
"v_cvt_pk_bf16_f32 v118,v120,v121\n"
"v_mfma_f32_16x16x32_bf16 v[136:139],v[36:39],a[4:7],v[136:139]\n"
"ds_read_b64_tr_b16 a[42:43],v101 offset:1024\n"
"v_cvt_pk_bf16_f32 v119,v122,v123\n"
"v_mfma_f32_16x16x32_bf16 v[168:171],v[36:39],a[12:15],v[168:171]\n"
"ds_read_b64_tr_b16 a[44:45],v101 offset:8192\n"
"s_add_u32 s56,s56,126976\n"
"v_mfma_f32_16x16x32_bf16 v[168:171],v[32:35],a[8:11],v[168:171]\n"
"s_add_u32 m0,s85,140288\n"
"ds_read_b64_tr_b16 a[46:47],v101 offset:9216\n"
"v_mfma_f32_16x16x32_bf16 v[200:203],v[32:35],a[16:19],v[200:203]\n"
"buffer_load_dwordx4 v111,s[28:31],s86 offen lds\n"
"v_mfma_f32_16x16x32_bf16 v[200:203],v[36:39],a[20:23],v[200:203]\n"
"buffer_store_dwordx4 v[116:119],v112,s[24:27],s56 offen offset:0 sc1\n"
"v_mfma_f32_16x16x32_bf16 v[232:235],v[36:39],a[28:31],v[232:235]\n"
"s_xor_b32 s86,s86,786432\n"
"v_accvgpr_read_b32 v116,a72\n"
"v_accvgpr_read_b32 v117,a73\n"
"v_mfma_f32_16x16x32_bf16 v[232:235],v[32:35],a[24:27],v[232:235]\n"
"v_accvgpr_read_b32 v118,a74\n"
"v_accvgpr_read_b32 v119,a75\n"
"v_mfma_f32_16x16x32_bf16 v[236:239],v[40:43],a[24:27],v[236:239]\n"
"ds_read_b64_tr_b16 v[32:33],v102 offset:0\n"
"v_accvgpr_read_b32 v120,a76\n"
"v_mfma_f32_16x16x32_bf16 v[236:239],v[44:47],a[28:31],v[236:239]\n"
"ds_read_b64_tr_b16 v[34:35],v102 offset:1024\n"
"v_accvgpr_read_b32 v121,a77\n"
"v_mfma_f32_16x16x32_bf16 v[204:207],v[44:47],a[20:23],v[204:207]\n"
"ds_read_b64_tr_b16 v[36:37],v102 offset:8192\n"
"v_accvgpr_read_b32 v122,a78\n"
"v_mfma_f32_16x16x32_bf16 v[204:207],v[40:43],a[16:19],v[204:207]\n"
"s_add_u32 m0,s78,0\n"
"ds_read_b64_tr_b16 v[38:39],v102 offset:9216\n"
"v_mfma_f32_16x16x32_bf16 v[172:175],v[40:43],a[8:11],v[172:175]\n"
"buffer_load_dwordx4 v112,s[24:27],s76 offen offset:0 sc1 lds\n"
"v_mfma_f32_16x16x32_bf16 v[172:175],v[44:47],a[12:15],v[172:175]\n"
"v_accvgpr_read_b32 v123,a79\n"
"v_cvt_pk_bf16_f32 v116,v116,v117\n"
"v_mfma_f32_16x16x32_bf16 v[140:143],v[44:47],a[4:7],v[140:143]\n"
"buffer_load_dwordx4 v112,s[24:27],s76 offen offset:1024 sc1 lds\n"
"v_mfma_f32_16x16x32_bf16 v[140:143],v[40:43],a[0:3],v[140:143]\n"
"s_waitcnt vmcnt(23)\n"
"v_cvt_pk_bf16_f32 v117,v118,v119\n"
"v_cvt_pk_bf16_f32 v118,v120,v121\n"
"v_mfma_f32_16x16x32_bf16 v[144:147],v[48:51],a[0:3],v[144:147]\n"
"ds_read_b64_tr_b16 v[40:41],v103 offset:0\n"
"v_cvt_pk_bf16_f32 v119,v122,v123\n"
"v_mfma_f32_16x16x32_bf16 v[144:147],v[52:55],a[4:7],v[144:147]\n"
"ds_read_b64_tr_b16 v[42:43],v103 offset:1024\n"
"buffer_store_dwordx4 v[116:119],v112,s[24:27],s56 offen offset:1024 sc1\n"
"v_mfma_f32_16x16x32_bf16 v[176:179],v[52:55],a[12:15],v[176:179]\n"
"ds_read_b64_tr_b16 v[44:45],v103 offset:8192\n"
"v_accvgpr_read_b32 v116,a80\n"
"v_mfma_f32_16x16x32_bf16 v[176:179],v[48:51],a[8:11],v[176:179]\n"
"ds_read_b64_tr_b16 v[46:47],v103 offset:9216\n"
"v_accvgpr_read_b32 v117,a81\n"
"v_mfma_f32_16x16x32_bf16 v[208:211],v[48:51],a[16:19],v[208:211]\n"
"buffer_load_dwordx4 v112,s[24:27],s76 offen offset:2048 sc1 lds\n"
"v_mfma_f32_16x16x32_bf16 v[208:211],v[52:55],a[20:23],v[208:211]\n"
"ds_read_b128 v[0:3],v96 offset:32768\n"
"v_accvgpr_read_b32 v118,a82\n"
"v_mfma_f32_16x16x32_bf16 v[240:243],v[52:55],a[28:31],v[240:243]\n"
"v_accvgpr_read_b32 v119,a83\n"
"v_accvgpr_read_b32 v120,a84\n"
"v_mfma_f32_16x16x32_bf16 v[240:243],v[48:51],a[24:27],v[240:243]\n"
"ds_read_b128 v[8:11],v96 offset:33792\n"
"v_accvgpr_read_b32 v121,a85\n"
"v_mfma_f32_16x16x32_bf16 v[244:247],v[56:59],a[24:27],v[244:247]\n"
"ds_read_b64_tr_b16 v[48:49],v104 offset:0\n"
"v_accvgpr_read_b32 v122,a86\n"
"v_mfma_f32_16x16x32_bf16 v[244:247],v[60:63],a[28:31],v[244:247]\n"
"ds_read_b64_tr_b16 v[50:51],v104 offset:1024\n"
"v_accvgpr_read_b32 v123,a87\n"
"v_mfma_f32_16x16x32_bf16 v[212:215],v[60:63],a[20:23],v[212:215]\n"
"ds_read_b64_tr_b16 v[52:53],v104 offset:8192\n"
"v_cvt_pk_bf16_f32 v116,v116,v117\n"
"v_mfma_f32_16x16x32_bf16 v[212:215],v[56:59],a[16:19],v[212:215]\n"
"ds_read_b64_tr_b16 v[54:55],v104 offset:9216\n"
"v_cvt_pk_bf16_f32 v117,v118,v119\n"
"v_mfma_f32_16x16x32_bf16 v[180:183],v[56:59],a[8:11],v[180:183]\n"
"buffer_load_dwordx4 v112,s[24:27],s76 offen offset:3072 sc1 lds\n"
"v_mfma_f32_16x16x32_bf16 v[180:183],v[60:63],a[12:15],v[180:183]\n"
"s_add_u32 m0,s78,16384\n"
"s_xor_b32 s76,s76,16384\n"
"ds_read_b128 v[16:19],v96 offset:34816\n"
"v_mfma_f32_16x16x32_bf16 v[148:151],v[60:63],a[4:7],v[148:151]\n"
"buffer_load_dwordx4 v112,s[24:27],s76 offen offset:0 sc1 lds\n"
"v_mfma_f32_16x16x32_bf16 v[148:151],v[56:59],a[0:3],v[148:151]\n"
"ds_read_b128 v[24:27],v96 offset:35840\n"
"v_cvt_pk_bf16_f32 v118,v120,v121\n"
"v_mfma_f32_16x16x32_bf16 v[152:155],v[64:67],a[0:3],v[152:155]\n"
"ds_read_b64_tr_b16 v[56:57],v105 offset:0\n"
"v_cvt_pk_bf16_f32 v119,v122,v123\n"
"v_mfma_f32_16x16x32_bf16 v[152:155],v[68:71],a[4:7],v[152:155]\n"
"ds_read_b64_tr_b16 v[58:59],v105 offset:1024\n"
"buffer_store_dwordx4 v[116:119],v112,s[24:27],s56 offen offset:2048 sc1\n"
"v_mfma_f32_16x16x32_bf16 v[184:187],v[68:71],a[12:15],v[184:187]\n"
"ds_read_b64_tr_b16 v[60:61],v105 offset:8192\n"
"v_accvgpr_read_b32 v116,a88\n"
"v_mfma_f32_16x16x32_bf16 v[184:187],v[64:67],a[8:11],v[184:187]\n"
"ds_read_b64_tr_b16 v[62:63],v105 offset:9216\n"
"v_accvgpr_read_b32 v117,a89\n"
"v_mfma_f32_16x16x32_bf16 v[216:219],v[64:67],a[16:19],v[216:219]\n"
"ds_read_b128 v[4:7],v96 offset:49152\n"
"v_accvgpr_read_b32 v118,a90\n"
"v_mfma_f32_16x16x32_bf16 v[216:219],v[68:71],a[20:23],v[216:219]\n"
"ds_read_b128 v[12:15],v96 offset:50176\n"
"v_accvgpr_read_b32 v119,a91\n"
"v_mfma_f32_16x16x32_bf16 v[248:251],v[68:71],a[28:31],v[248:251]\n"
"ds_read_b128 v[20:23],v96 offset:51200\n"
"v_accvgpr_read_b32 v120,a92\n"
"v_mfma_f32_16x16x32_bf16 v[248:251],v[64:67],a[24:27],v[248:251]\n"
"ds_read_b128 v[28:31],v96 offset:52224\n"
"v_accvgpr_read_b32 v121,a93\n"
"v_mfma_f32_16x16x32_bf16 v[252:255],v[72:75],a[24:27],v[252:255]\n"
"buffer_load_dwordx4 v112,s[24:27],s76 offen offset:1024 sc1 lds\n"
"v_mfma_f32_16x16x32_bf16 v[252:255],v[76:79],a[28:31],v[252:255]\n"
"v_accvgpr_read_b32 v122,a94\n"
"v_accvgpr_read_b32 v123,a95\n"
"v_mfma_f32_16x16x32_bf16 v[220:223],v[76:79],a[20:23],v[220:223]\n"
"v_cvt_pk_bf16_f32 v116,v116,v117\n"
"v_cvt_pk_bf16_f32 v117,v118,v119\n"
"v_mfma_f32_16x16x32_bf16 v[220:223],v[72:75],a[16:19],v[220:223]\n"
"buffer_load_dwordx4 v112,s[24:27],s76 offen offset:2048 sc1 lds\n"
"v_mfma_f32_16x16x32_bf16 v[188:191],v[72:75],a[8:11],v[188:191]\n"
"v_cvt_pk_bf16_f32 v118,v120,v121\n"
"v_cvt_pk_bf16_f32 v119,v122,v123\n"
"v_mfma_f32_16x16x32_bf16 v[188:191],v[76:79],a[12:15],v[188:191]\n"
"buffer_store_dwordx4 v[116:119],v112,s[24:27],s56 offen offset:3072 sc1\n"
"v_mfma_f32_16x16x32_bf16 v[156:159],v[76:79],a[4:7],v[156:159]\n"
"buffer_load_dwordx4 v112,s[24:27],s76 offen offset:3072 sc1 lds\n"
"v_accvgpr_read_b32 v116,a96\n"
"v_mfma_f32_16x16x32_bf16 v[156:159],v[72:75],a[0:3],v[156:159]\n"
"s_xor_b32 s76,s76,49152\n"
"v_accvgpr_read_b32 v117,a97\n"
"s_waitcnt vmcnt(27) lgkmcnt(0)\n"
"s_barrier\n"
"v_mfma_f32_16x16x32_bf16 v[128:131],v[80:83],v[0:3],v[128:131]\n"
"ds_read_b64_tr_b16 v[64:65],v98 offset:49152\n"
"ds_read_b64_tr_b16 v[72:73],v99 offset:49152\n"
"v_mfma_f32_16x16x32_bf16 v[128:131],v[84:87],v[4:7],v[128:131]\n"
"ds_read_b64_tr_b16 v[66:67],v98 offset:50176\n"
"ds_read_b64_tr_b16 v[74:75],v99 offset:50176\n"
"v_mfma_f32_16x16x32_bf16 v[160:163],v[84:87],v[12:15],v[160:163]\n"
"ds_read_b64_tr_b16 v[68:69],v98 offset:57344\n"
"ds_read_b64_tr_b16 v[76:77],v99 offset:57344\n"
"v_mfma_f32_16x16x32_bf16 v[160:163],v[80:83],v[8:11],v[160:163]\n"
"s_add_u32 m0,s85,98304\n"
"ds_read_b64_tr_b16 v[70:71],v98 offset:58368\n"
"ds_read_b64_tr_b16 v[78:79],v99 offset:58368\n"
"v_mfma_f32_16x16x32_bf16 v[192:195],v[80:83],v[16:19],v[192:195]\n"
"buffer_load_dwordx4 v110,s[28:31],s86 offen lds\n"
"v_mfma_f32_16x16x32_bf16 v[192:195],v[84:87],v[20:23],v[192:195]\n"
"v_accvgpr_read_b32 v118,a98\n"
"v_accvgpr_read_b32 v119,a99\n"
"v_mfma_f32_16x16x32_bf16 v[224:227],v[84:87],v[28:31],v[224:227]\n"
"v_accvgpr_read_b32 v120,a100\n"
"v_accvgpr_read_b32 v121,a101\n"
"v_mfma_f32_16x16x32_bf16 v[224:227],v[80:83],v[24:27],v[224:227]\n"
"v_accvgpr_read_b32 v122,a102\n"
"v_accvgpr_read_b32 v123,a103\n"
"v_mfma_f32_16x16x32_bf16 v[228:231],v[88:91],v[24:27],v[228:231]\n"
"ds_read_b64_tr_b16 v[80:81],v100 offset:49152\n"
"v_cvt_pk_bf16_f32 v116,v116,v117\n"
"v_mfma_f32_16x16x32_bf16 v[228:231],v[92:95],v[28:31],v[228:231]\n"
"ds_read_b64_tr_b16 v[82:83],v100 offset:50176\n"
"v_cvt_pk_bf16_f32 v117,v118,v119\n"
"v_mfma_f32_16x16x32_bf16 v[196:199],v[92:95],v[20:23],v[196:199]\n"
"ds_read_b64_tr_b16 v[84:85],v100 offset:57344\n"
"v_cvt_pk_bf16_f32 v118,v120,v121\n"
"v_mfma_f32_16x16x32_bf16 v[196:199],v[88:91],v[16:19],v[196:199]\n"
"s_add_u32 m0,s85,99328\n"
"ds_read_b64_tr_b16 v[86:87],v100 offset:58368\n"
"v_mfma_f32_16x16x32_bf16 v[164:167],v[88:91],v[8:11],v[164:167]\n"
"buffer_load_dwordx4 v111,s[28:31],s86 offen lds\n"
"v_mfma_f32_16x16x32_bf16 v[164:167],v[92:95],v[12:15],v[164:167]\n"
"s_add_u32 m0,s85,106496\n"
"s_xor_b32 s86,s86,262144\n"
"v_mfma_f32_16x16x32_bf16 v[132:135],v[92:95],v[4:7],v[132:135]\n"
"buffer_load_dwordx4 v110,s[28:31],s86 offen lds\n"
"v_mfma_f32_16x16x32_bf16 v[132:135],v[88:91],v[0:3],v[132:135]\n"
"v_cvt_pk_bf16_f32 v119,v122,v123\n"
"s_add_u32 s56,s56,4096\n"
"v_mfma_f32_16x16x32_bf16 v[136:139],a[32:35],v[0:3],v[136:139]\n"
"ds_read_b64_tr_b16 v[88:89],v101 offset:49152\n"
"buffer_store_dwordx4 v[116:119],v112,s[24:27],s56 offen offset:0 sc1\n"
"v_mfma_f32_16x16x32_bf16 v[136:139],a[36:39],v[4:7],v[136:139]\n"
"ds_read_b64_tr_b16 v[90:91],v101 offset:50176\n"
"v_accvgpr_read_b32 v116,a104\n"
"v_mfma_f32_16x16x32_bf16 v[168:171],a[36:39],v[12:15],v[168:171]\n"
"ds_read_b64_tr_b16 v[92:93],v101 offset:57344\n"
"v_accvgpr_read_b32 v117,a105\n"
"v_mfma_f32_16x16x32_bf16 v[168:171],a[32:35],v[8:11],v[168:171]\n"
"s_add_u32 m0,s85,107520\n"
"ds_read_b64_tr_b16 v[94:95],v101 offset:58368\n"
"v_mfma_f32_16x16x32_bf16 v[200:203],a[32:35],v[16:19],v[200:203]\n"
"buffer_load_dwordx4 v111,s[28:31],s86 offen lds\n"
"v_mfma_f32_16x16x32_bf16 v[200:203],a[36:39],v[20:23],v[200:203]\n"
"s_xor_b32 s86,s86,1835008\n"
"v_accvgpr_read_b32 v118,a106\n"
"v_mfma_f32_16x16x32_bf16 v[232:235],a[36:39],v[28:31],v[232:235]\n"
"v_accvgpr_read_b32 v119,a107\n"
"v_accvgpr_read_b32 v120,a108\n"
"v_mfma_f32_16x16x32_bf16 v[232:235],a[32:35],v[24:27],v[232:235]\n"
"v_accvgpr_read_b32 v121,a109\n"
"v_accvgpr_read_b32 v122,a110\n"
"v_mfma_f32_16x16x32_bf16 v[236:239],a[40:43],v[24:27],v[236:239]\n"
"ds_read_b64_tr_b16 a[32:33],v102 offset:49152\n"
"v_accvgpr_read_b32 v123,a111\n"
"v_mfma_f32_16x16x32_bf16 v[236:239],a[44:47],v[28:31],v[236:239]\n"
"ds_read_b64_tr_b16 a[34:35],v102 offset:50176\n"
"v_cvt_pk_bf16_f32 v116,v116,v117\n"
"v_mfma_f32_16x16x32_bf16 v[204:207],a[44:47],v[20:23],v[204:207]\n"
"ds_read_b64_tr_b16 a[36:37],v102 offset:57344\n"
"v_cvt_pk_bf16_f32 v117,v118,v119\n"
"v_mfma_f32_16x16x32_bf16 v[204:207],a[40:43],v[16:19],v[204:207]\n"
"s_add_u32 m0,s78,32768\n"
"ds_read_b64_tr_b16 a[38:39],v102 offset:58368\n"
"v_mfma_f32_16x16x32_bf16 v[172:175],a[40:43],v[8:11],v[172:175]\n"
"buffer_load_dwordx4 v112,s[24:27],s76 offen offset:0 sc1 lds\n"
"v_mfma_f32_16x16x32_bf16 v[172:175],a[44:47],v[12:15],v[172:175]\n"
"v_cvt_pk_bf16_f32 v118,v120,v121\n"
"v_cvt_pk_bf16_f32 v119,v122,v123\n"
"v_mfma_f32_16x16x32_bf16 v[140:143],a[44:47],v[4:7],v[140:143]\n"
"buffer_load_dwordx4 v112,s[24:27],s76 offen offset:1024 sc1 lds\n"
"buffer_store_dwordx4 v[116:119],v112,s[24:27],s56 offen offset:1024 sc1\n"
"v_mfma_f32_16x16x32_bf16 v[140:143],a[40:43],v[0:3],v[140:143]\n"
"s_nop 0\n"
"v_accvgpr_read_b32 v116,a112\n"
"s_waitcnt vmcnt(25)\n"
"v_mfma_f32_16x16x32_bf16 v[144:147],v[32:35],v[0:3],v[144:147]\n"
"ds_read_b64_tr_b16 a[40:41],v103 offset:49152\n"
"v_accvgpr_read_b32 v117,a113\n"
"v_mfma_f32_16x16x32_bf16 v[144:147],v[36:39],v[4:7],v[144:147]\n"
"ds_read_b64_tr_b16 a[42:43],v103 offset:50176\n"
"v_accvgpr_read_b32 v118,a114\n"
"v_mfma_f32_16x16x32_bf16 v[176:179],v[36:39],v[12:15],v[176:179]\n"
"ds_read_b64_tr_b16 a[44:45],v103 offset:57344\n"
"v_accvgpr_read_b32 v119,a115\n"
"v_mfma_f32_16x16x32_bf16 v[176:179],v[32:35],v[8:11],v[176:179]\n"
"ds_read_b64_tr_b16 a[46:47],v103 offset:58368\n"
"v_accvgpr_read_b32 v120,a116\n"
"v_mfma_f32_16x16x32_bf16 v[208:211],v[32:35],v[16:19],v[208:211]\n"
"buffer_load_dwordx4 v112,s[24:27],s76 offen offset:2048 sc1 lds\n"
"v_mfma_f32_16x16x32_bf16 v[208:211],v[36:39],v[20:23],v[208:211]\n"
"ds_read_b128 a[0:3],v97 offset:0\n"
"v_accvgpr_read_b32 v121,a117\n"
"v_mfma_f32_16x16x32_bf16 v[240:243],v[36:39],v[28:31],v[240:243]\n"
"v_accvgpr_read_b32 v122,a118\n"
"v_accvgpr_read_b32 v123,a119\n"
"v_mfma_f32_16x16x32_bf16 v[240:243],v[32:35],v[24:27],v[240:243]\n"
"ds_read_b128 a[8:11],v97 offset:1024\n"
"v_cvt_pk_bf16_f32 v116,v116,v117\n"
"v_mfma_f32_16x16x32_bf16 v[244:247],v[40:43],v[24:27],v[244:247]\n"
"ds_read_b64_tr_b16 v[32:33],v104 offset:49152\n"
"v_cvt_pk_bf16_f32 v117,v118,v119\n"
"v_mfma_f32_16x16x32_bf16 v[244:247],v[44:47],v[28:31],v[244:247]\n"
"ds_read_b64_tr_b16 v[34:35],v104 offset:50176\n"
"v_cvt_pk_bf16_f32 v118,v120,v121\n"
"v_mfma_f32_16x16x32_bf16 v[212:215],v[44:47],v[20:23],v[212:215]\n"
"ds_read_b64_tr_b16 v[36:37],v104 offset:57344\n"
"v_cvt_pk_bf16_f32 v119,v122,v123\n"
"v_mfma_f32_16x16x32_bf16 v[212:215],v[40:43],v[16:19],v[212:215]\n"
"ds_read_b64_tr_b16 v[38:39],v104 offset:58368\n"
"buffer_store_dwordx4 v[116:119],v112,s[24:27],s56 offen offset:2048 sc1\n"
"v_mfma_f32_16x16x32_bf16 v[180:183],v[40:43],v[8:11],v[180:183]\n"
"buffer_load_dwordx4 v112,s[24:27],s76 offen offset:3072 sc1 lds\n"
"v_mfma_f32_16x16x32_bf16 v[180:183],v[44:47],v[12:15],v[180:183]\n"
"s_add_u32 m0,s78,49152\n"
"s_xor_b32 s76,s76,16384\n"
"ds_read_b128 a[16:19],v97 offset:2048\n"
"v_mfma_f32_16x16x32_bf16 v[148:151],v[44:47],v[4:7],v[148:151]\n"
"buffer_load_dwordx4 v112,s[24:27],s76 offen offset:0 sc1 lds\n"
"v_mfma_f32_16x16x32_bf16 v[148:151],v[40:43],v[0:3],v[148:151]\n"
"ds_read_b128 a[24:27],v97 offset:3072\n"
"v_accvgpr_read_b32 v116,a120\n"
"v_mfma_f32_16x16x32_bf16 v[152:155],v[48:51],v[0:3],v[152:155]\n"
"ds_read_b64_tr_b16 v[40:41],v105 offset:49152\n"
"v_accvgpr_read_b32 v117,a121\n"
"v_mfma_f32_16x16x32_bf16 v[152:155],v[52:55],v[4:7],v[152:155]\n"
"ds_read_b64_tr_b16 v[42:43],v105 offset:50176\n"
"v_accvgpr_read_b32 v118,a122\n"
"v_mfma_f32_16x16x32_bf16 v[184:187],v[52:55],v[12:15],v[184:187]\n"
"ds_read_b64_tr_b16 v[44:45],v105 offset:57344\n"
"v_accvgpr_read_b32 v119,a123\n"
"v_mfma_f32_16x16x32_bf16 v[184:187],v[48:51],v[8:11],v[184:187]\n"
"ds_read_b64_tr_b16 v[46:47],v105 offset:58368\n"
"v_accvgpr_read_b32 v120,a124\n"
"v_mfma_f32_16x16x32_bf16 v[216:219],v[48:51],v[16:19],v[216:219]\n"
"ds_read_b128 a[4:7],v97 offset:16384\n"
"v_accvgpr_read_b32 v121,a125\n"
"v_mfma_f32_16x16x32_bf16 v[216:219],v[52:55],v[20:23],v[216:219]\n"
"ds_read_b128 a[12:15],v97 offset:17408\n"
"v_accvgpr_read_b32 v122,a126\n"
"v_mfma_f32_16x16x32_bf16 v[248:251],v[52:55],v[28:31],v[248:251]\n"
"ds_read_b128 a[20:23],v97 offset:18432\n"
"v_accvgpr_read_b32 v123,a127\n"
"v_mfma_f32_16x16x32_bf16 v[248:251],v[48:51],v[24:27],v[248:251]\n"
"ds_read_b128 a[28:31],v97 offset:19456\n"
"v_cvt_pk_bf16_f32 v116,v116,v117\n"
"v_mfma_f32_16x16x32_bf16 v[252:255],v[56:59],v[24:27],v[252:255]\n"
"buffer_load_dwordx4 v112,s[24:27],s76 offen offset:1024 sc1 lds\n"
"v_mfma_f32_16x16x32_bf16 v[252:255],v[60:63],v[28:31],v[252:255]\n"
"v_cvt_pk_bf16_f32 v117,v118,v119\n"
"v_cvt_pk_bf16_f32 v118,v120,v121\n"
"v_cvt_pk_bf16_f32 v119,v122,v123\n"
"v_mfma_f32_16x16x32_bf16 v[220:223],v[60:63],v[20:23],v[220:223]\n"
"buffer_store_dwordx4 v[116:119],v112,s[24:27],s56 offen offset:3072 sc1\n"
"v_mfma_f32_16x16x32_bf16 v[220:223],v[56:59],v[16:19],v[220:223]\n"
"buffer_load_dwordx4 v112,s[24:27],s76 offen offset:2048 sc1 lds\n"
"v_mfma_f32_16x16x32_bf16 v[188:191],v[56:59],v[8:11],v[188:191]\n"
"v_accvgpr_read_b32 v116,a128\n"
"v_accvgpr_read_b32 v117,a129\n"
"v_mfma_f32_16x16x32_bf16 v[188:191],v[60:63],v[12:15],v[188:191]\n"
"v_accvgpr_read_b32 v118,a130\n"
"v_accvgpr_read_b32 v119,a131\n"
"v_mfma_f32_16x16x32_bf16 v[156:159],v[60:63],v[4:7],v[156:159]\n"
"buffer_load_dwordx4 v112,s[24:27],s76 offen offset:3072 sc1 lds\n"
"v_mfma_f32_16x16x32_bf16 v[156:159],v[56:59],v[0:3],v[156:159]\n"
"s_xor_b32 s76,s76,114688\n"
"v_accvgpr_read_b32 v120,a132\n"
"s_waitcnt vmcnt(28) lgkmcnt(0)\n"
"s_barrier\n"
"v_mfma_f32_16x16x32_bf16 v[128:131],v[64:67],a[0:3],v[128:131]\n"
"ds_read_b64_tr_b16 v[48:49],v98 offset:32768\n"
"ds_read_b64_tr_b16 v[56:57],v99 offset:32768\n"
"v_mfma_f32_16x16x32_bf16 v[128:131],v[68:71],a[4:7],v[128:131]\n"
"ds_read_b64_tr_b16 v[50:51],v98 offset:33792\n"
"ds_read_b64_tr_b16 v[58:59],v99 offset:33792\n"
"v_mfma_f32_16x16x32_bf16 v[160:163],v[68:71],a[12:15],v[160:163]\n"
"ds_read_b64_tr_b16 v[52:53],v98 offset:40960\n"
"ds_read_b64_tr_b16 v[60:61],v99 offset:40960\n"
"v_mfma_f32_16x16x32_bf16 v[160:163],v[64:67],a[8:11],v[160:163]\n"
"s_add_u32 m0,s85,147456\n"
"ds_read_b64_tr_b16 v[54:55],v98 offset:41984\n"
"ds_read_b64_tr_b16 v[62:63],v99 offset:41984\n"
"v_mfma_f32_16x16x32_bf16 v[192:195],v[64:67],a[16:19],v[192:195]\n"
"buffer_load_dwordx4 v110,s[28:31],s86 offen lds\n"
"v_mfma_f32_16x16x32_bf16 v[192:195],v[68:71],a[20:23],v[192:195]\n"
"v_accvgpr_read_b32 v121,a133\n"
"v_accvgpr_read_b32 v122,a134\n"
"v_mfma_f32_16x16x32_bf16 v[224:227],v[68:71],a[28:31],v[224:227]\n"
"v_accvgpr_read_b32 v123,a135\n"
"v_cvt_pk_bf16_f32 v116,v116,v117\n"
"v_mfma_f32_16x16x32_bf16 v[224:227],v[64:67],a[24:27],v[224:227]\n"
"v_cvt_pk_bf16_f32 v117,v118,v119\n"
"v_cvt_pk_bf16_f32 v118,v120,v121\n"
"v_mfma_f32_16x16x32_bf16 v[228:231],v[72:75],a[24:27],v[228:231]\n"
"ds_read_b64_tr_b16 v[64:65],v100 offset:32768\n"
"v_cvt_pk_bf16_f32 v119,v122,v123\n"
"v_mfma_f32_16x16x32_bf16 v[228:231],v[76:79],a[28:31],v[228:231]\n"
"ds_read_b64_tr_b16 v[66:67],v100 offset:33792\n"
"s_add_u32 s56,s56,126976\n"
"v_mfma_f32_16x16x32_bf16 v[196:199],v[76:79],a[20:23],v[196:199]\n"
"ds_read_b64_tr_b16 v[68:69],v100 offset:40960\n"
"buffer_store_dwordx4 v[116:119],v112,s[24:27],s56 offen offset:0 sc1\n"
"v_mfma_f32_16x16x32_bf16 v[196:199],v[72:75],a[16:19],v[196:199]\n"
"s_add_u32 m0,s85,148480\n"
"ds_read_b64_tr_b16 v[70:71],v100 offset:41984\n"
"v_mfma_f32_16x16x32_bf16 v[164:167],v[72:75],a[8:11],v[164:167]\n"
"buffer_load_dwordx4 v111,s[28:31],s86 offen lds\n"
"v_mfma_f32_16x16x32_bf16 v[164:167],v[76:79],a[12:15],v[164:167]\n"
"s_add_u32 m0,s85,155648\n"
"s_xor_b32 s86,s86,262144\n"
"v_mfma_f32_16x16x32_bf16 v[132:135],v[76:79],a[4:7],v[132:135]\n"
"buffer_load_dwordx4 v110,s[28:31],s86 offen lds\n"
"v_mfma_f32_16x16x32_bf16 v[132:135],v[72:75],a[0:3],v[132:135]\n"
"v_accvgpr_read_b32 v116,a136\n"
"v_accvgpr_read_b32 v117,a137\n"
"v_mfma_f32_16x16x32_bf16 v[136:139],v[80:83],a[0:3],v[136:139]\n"
"ds_read_b64_tr_b16 v[72:73],v101 offset:32768\n"
"v_accvgpr_read_b32 v118,a138\n"
"v_mfma_f32_16x16x32_bf16 v[136:139],v[84:87],a[4:7],v[136:139]\n"
"ds_read_b64_tr_b16 v[74:75],v101 offset:33792\n"
"v_accvgpr_read_b32 v119,a139\n"
"v_mfma_f32_16x16x32_bf16 v[168:171],v[84:87],a[12:15],v[168:171]\n"
"ds_read_b64_tr_b16 v[76:77],v101 offset:40960\n"
"v_accvgpr_read_b32 v120,a140\n"
"v_mfma_f32_16x16x32_bf16 v[168:171],v[80:83],a[8:11],v[168:171]\n"
"s_add_u32 m0,s85,156672\n"
"ds_read_b64_tr_b16 v[78:79],v101 offset:41984\n"
"v_mfma_f32_16x16x32_bf16 v[200:203],v[80:83],a[16:19],v[200:203]\n"
"buffer_load_dwordx4 v111,s[28:31],s86 offen lds\n"
"v_mfma_f32_16x16x32_bf16 v[200:203],v[84:87],a[20:23],v[200:203]\n"
"s_xor_b32 s86,s86,786432\n"
"v_accvgpr_read_b32 v121,a141\n"
"v_mfma_f32_16x16x32_bf16 v[232:235],v[84:87],a[28:31],v[232:235]\n"
"v_accvgpr_read_b32 v122,a142\n"
"v_accvgpr_read_b32 v123,a143\n"
"v_mfma_f32_16x16x32_bf16 v[232:235],v[80:83],a[24:27],v[232:235]\n"
"v_cvt_pk_bf16_f32 v116,v116,v117\n"
"v_cvt_pk_bf16_f32 v117,v118,v119\n"
"v_mfma_f32_16x16x32_bf16 v[236:239],v[88:91],a[24:27],v[236:239]\n"
"ds_read_b64_tr_b16 v[80:81],v102 offset:32768\n"
"v_cvt_pk_bf16_f32 v118,v120,v121\n"
"v_mfma_f32_16x16x32_bf16 v[236:239],v[92:95],a[28:31],v[236:239]\n"
"ds_read_b64_tr_b16 v[82:83],v102 offset:33792\n"
"v_cvt_pk_bf16_f32 v119,v122,v123\n"
"v_mfma_f32_16x16x32_bf16 v[204:207],v[92:95],a[20:23],v[204:207]\n"
"ds_read_b64_tr_b16 v[84:85],v102 offset:40960\n"
"buffer_store_dwordx4 v[116:119],v112,s[24:27],s56 offen offset:1024 sc1\n"
"v_mfma_f32_16x16x32_bf16 v[204:207],v[88:91],a[16:19],v[204:207]\n"
"s_add_u32 m0,s78,65536\n"
"ds_read_b64_tr_b16 v[86:87],v102 offset:41984\n"
"v_accvgpr_read_b32 v118,a146\n"
"v_mfma_f32_16x16x32_bf16 v[172:175],v[88:91],a[8:11],v[172:175]\n"
"buffer_load_dwordx4 v112,s[24:27],s76 offen offset:0 sc1 lds\n"
"v_mfma_f32_16x16x32_bf16 v[172:175],v[92:95],a[12:15],v[172:175]\n"
"v_accvgpr_read_b32 v116,a144\n"
"v_accvgpr_read_b32 v117,a145\n"
"v_mfma_f32_16x16x32_bf16 v[140:143],v[92:95],a[4:7],v[140:143]\n"
"buffer_load_dwordx4 v112,s[24:27],s76 offen offset:1024 sc1 lds\n"
"v_mfma_f32_16x16x32_bf16 v[140:143],v[88:91],a[0:3],v[140:143]\n"
"v_accvgpr_read_b32 v119,a147\n"
"s_waitcnt vmcnt(24)\n"
"v_mfma_f32_16x16x32_bf16 v[144:147],a[32:35],a[0:3],v[144:147]\n"
"ds_read_b64_tr_b16 v[88:89],v103 offset:32768\n"
"v_accvgpr_read_b32 v120,a148\n"
"v_mfma_f32_16x16x32_bf16 v[144:147],a[36:39],a[4:7],v[144:147]\n"
"ds_read_b64_tr_b16 v[90:91],v103 offset:33792\n"
"v_accvgpr_read_b32 v121,a149\n"
"v_mfma_f32_16x16x32_bf16 v[176:179],a[36:39],a[12:15],v[176:179]\n"
"ds_read_b64_tr_b16 v[92:93],v103 offset:40960\n"
"v_accvgpr_read_b32 v122,a150\n"
"v_mfma_f32_16x16x32_bf16 v[176:179],a[32:35],a[8:11],v[176:179]\n"
"ds_read_b64_tr_b16 v[94:95],v103 offset:41984\n"
"v_accvgpr_read_b32 v123,a151\n"
"v_mfma_f32_16x16x32_bf16 v[208:211],a[32:35],a[16:19],v[208:211]\n"
"buffer_load_dwordx4 v112,s[24:27],s76 offen offset:2048 sc1 lds\n"
"v_mfma_f32_16x16x32_bf16 v[208:211],a[36:39],a[20:23],v[208:211]\n"
"ds_read_b128 v[0:3],v96 offset:0\n"
"v_cvt_pk_bf16_f32 v116,v116,v117\n"
"v_mfma_f32_16x16x32_bf16 v[240:243],a[36:39],a[28:31],v[240:243]\n"
"v_cvt_pk_bf16_f32 v117,v118,v119\n"
"v_cvt_pk_bf16_f32 v118,v120,v121\n"
"v_mfma_f32_16x16x32_bf16 v[240:243],a[32:35],a[24:27],v[240:243]\n"
"ds_read_b128 v[8:11],v96 offset:1024\n"
"v_cvt_pk_bf16_f32 v119,v122,v123\n"
"v_mfma_f32_16x16x32_bf16 v[244:247],a[40:43],a[24:27],v[244:247]\n"
"ds_read_b64_tr_b16 a[32:33],v104 offset:32768\n"
"buffer_store_dwordx4 v[116:119],v112,s[24:27],s56 offen offset:2048 sc1\n"
"v_mfma_f32_16x16x32_bf16 v[244:247],a[44:47],a[28:31],v[244:247]\n"
"ds_read_b64_tr_b16 a[34:35],v104 offset:33792\n"
"v_accvgpr_read_b32 v116,a152\n"
"v_mfma_f32_16x16x32_bf16 v[212:215],a[44:47],a[20:23],v[212:215]\n"
"ds_read_b64_tr_b16 a[36:37],v104 offset:40960\n"
"v_accvgpr_read_b32 v117,a153\n"
"v_mfma_f32_16x16x32_bf16 v[212:215],a[40:43],a[16:19],v[212:215]\n"
"ds_read_b64_tr_b16 a[38:39],v104 offset:41984\n"
"v_accvgpr_read_b32 v118,a154\n"
"v_mfma_f32_16x16x32_bf16 v[180:183],a[40:43],a[8:11],v[180:183]\n"
"buffer_load_dwordx4 v112,s[24:27],s76 offen offset:3072 sc1 lds\n"
"v_mfma_f32_16x16x32_bf16 v[180:183],a[44:47],a[12:15],v[180:183]\n"
"s_add_u32 m0,s78,81920\n"
"s_xor_b32 s76,s76,16384\n"
"ds_read_b128 v[16:19],v96 offset:2048\n"
"v_mfma_f32_16x16x32_bf16 v[148:151],a[44:47],a[4:7],v[148:151]\n"
"buffer_load_dwordx4 v112,s[24:27],s76 offen offset:0 sc1 lds\n"
"v_mfma_f32_16x16x32_bf16 v[148:151],a[40:43],a[0:3],v[148:151]\n"
"ds_read_b128 v[24:27],v96 offset:3072\n"
"v_accvgpr_read_b32 v119,a155\n"
"v_mfma_f32_16x16x32_bf16 v[152:155],v[32:35],a[0:3],v[152:155]\n"
"ds_read_b64_tr_b16 a[40:41],v105 offset:32768\n"
"v_accvgpr_read_b32 v120,a156\n"
"v_mfma_f32_16x16x32_bf16 v[152:155],v[36:39],a[4:7],v[152:155]\n"
"ds_read_b64_tr_b16 a[42:43],v105 offset:33792\n"
"v_accvgpr_read_b32 v121,a157\n"
"v_mfma_f32_16x16x32_bf16 v[184:187],v[36:39],a[12:15],v[184:187]\n"
"ds_read_b64_tr_b16 a[44:45],v105 offset:40960\n"
"v_accvgpr_read_b32 v122,a158\n"
"v_mfma_f32_16x16x32_bf16 v[184:187],v[32:35],a[8:11],v[184:187]\n"
"ds_read_b64_tr_b16 a[46:47],v105 offset:41984\n"
"v_accvgpr_read_b32 v123,a159\n"
"v_mfma_f32_16x16x32_bf16 v[216:219],v[32:35],a[16:19],v[216:219]\n"
"ds_read_b128 v[4:7],v96 offset:16384\n"
"v_cvt_pk_bf16_f32 v116,v116,v117\n"
"v_mfma_f32_16x16x32_bf16 v[216:219],v[36:39],a[20:23],v[216:219]\n"
"ds_read_b128 v[12:15],v96 offset:17408\n"
"v_cvt_pk_bf16_f32 v117,v118,v119\n"
"v_mfma_f32_16x16x32_bf16 v[248:251],v[36:39],a[28:31],v[248:251]\n"
"ds_read_b128 v[20:23],v96 offset:18432\n"
"v_cvt_pk_bf16_f32 v118,v120,v121\n"
"v_mfma_f32_16x16x32_bf16 v[248:251],v[32:35],a[24:27],v[248:251]\n"
"ds_read_b128 v[28:31],v96 offset:19456\n"
"v_cvt_pk_bf16_f32 v119,v122,v123\n"
"v_mfma_f32_16x16x32_bf16 v[252:255],v[40:43],a[24:27],v[252:255]\n"
"buffer_load_dwordx4 v112,s[24:27],s76 offen offset:1024 sc1 lds\n"
"buffer_store_dwordx4 v[116:119],v112,s[24:27],s56 offen offset:3072 sc1\n"
"v_mfma_f32_16x16x32_bf16 v[252:255],v[44:47],a[28:31],v[252:255]\n"
"s_nop 0\n"
"v_accvgpr_read_b32 v116,a160\n"
"v_mfma_f32_16x16x32_bf16 v[220:223],v[44:47],a[20:23],v[220:223]\n"
"v_accvgpr_read_b32 v117,a161\n"
"v_accvgpr_read_b32 v118,a162\n"
"v_mfma_f32_16x16x32_bf16 v[220:223],v[40:43],a[16:19],v[220:223]\n"
"buffer_load_dwordx4 v112,s[24:27],s76 offen offset:2048 sc1 lds\n"
"v_mfma_f32_16x16x32_bf16 v[188:191],v[40:43],a[8:11],v[188:191]\n"
"v_accvgpr_read_b32 v119,a163\n"
"v_accvgpr_read_b32 v120,a164\n"
"v_mfma_f32_16x16x32_bf16 v[188:191],v[44:47],a[12:15],v[188:191]\n"
"v_accvgpr_read_b32 v121,a165\n"
"v_accvgpr_read_b32 v122,a166\n"
"v_mfma_f32_16x16x32_bf16 v[156:159],v[44:47],a[4:7],v[156:159]\n"
"buffer_load_dwordx4 v112,s[24:27],s76 offen offset:3072 sc1 lds\n"
"v_mfma_f32_16x16x32_bf16 v[156:159],v[40:43],a[0:3],v[156:159]\n"
"s_xor_b32 s76,s76,49152\n"
"v_accvgpr_read_b32 v123,a167\n"
"s_waitcnt vmcnt(27) lgkmcnt(0)\n"
"s_barrier\n"
"v_mfma_f32_16x16x32_bf16 v[128:131],v[48:51],v[0:3],v[128:131]\n"
"ds_read_b64_tr_b16 v[32:33],v98 offset:0\n"
"ds_read_b64_tr_b16 v[40:41],v99 offset:0\n"
"v_mfma_f32_16x16x32_bf16 v[128:131],v[52:55],v[4:7],v[128:131]\n"
"ds_read_b64_tr_b16 v[34:35],v98 offset:1024\n"
"ds_read_b64_tr_b16 v[42:43],v99 offset:1024\n"
"v_mfma_f32_16x16x32_bf16 v[160:163],v[52:55],v[12:15],v[160:163]\n"
"ds_read_b64_tr_b16 v[36:37],v98 offset:8192\n"
"ds_read_b64_tr_b16 v[44:45],v99 offset:8192\n"
"v_mfma_f32_16x16x32_bf16 v[160:163],v[48:51],v[8:11],v[160:163]\n"
"s_add_u32 m0,s85,131072\n"
"ds_read_b64_tr_b16 v[38:39],v98 offset:9216\n"
"ds_read_b64_tr_b16 v[46:47],v99 offset:9216\n"
"v_mfma_f32_16x16x32_bf16 v[192:195],v[48:51],v[16:19],v[192:195]\n"
"buffer_load_dwordx4 v110,s[28:31],s86 offen lds\n"
"v_mfma_f32_16x16x32_bf16 v[192:195],v[52:55],v[20:23],v[192:195]\n"
"v_cvt_pk_bf16_f32 v116,v116,v117\n"
"v_cvt_pk_bf16_f32 v117,v118,v119\n"
"v_mfma_f32_16x16x32_bf16 v[224:227],v[52:55],v[28:31],v[224:227]\n"
"s_add_u32 s56,s56,4096\n"
"v_cvt_pk_bf16_f32 v118,v120,v121\n"
"v_cvt_pk_bf16_f32 v119,v122,v123\n"
"v_mfma_f32_16x16x32_bf16 v[224:227],v[48:51],v[24:27],v[224:227]\n"
"buffer_store_dwordx4 v[116:119],v112,s[24:27],s56 offen offset:0 sc1\n"
"v_mfma_f32_16x16x32_bf16 v[228:231],v[56:59],v[24:27],v[228:231]\n"
"ds_read_b64_tr_b16 v[48:49],v100 offset:0\n"
"v_accvgpr_read_b32 v116,a168\n"
"v_mfma_f32_16x16x32_bf16 v[228:231],v[60:63],v[28:31],v[228:231]\n"
"ds_read_b64_tr_b16 v[50:51],v100 offset:1024\n"
"v_accvgpr_read_b32 v117,a169\n"
"v_mfma_f32_16x16x32_bf16 v[196:199],v[60:63],v[20:23],v[196:199]\n"
"ds_read_b64_tr_b16 v[52:53],v100 offset:8192\n"
"v_accvgpr_read_b32 v118,a170\n"
"v_mfma_f32_16x16x32_bf16 v[196:199],v[56:59],v[16:19],v[196:199]\n"
"s_add_u32 m0,s85,132096\n"
"ds_read_b64_tr_b16 v[54:55],v100 offset:9216\n"
"v_mfma_f32_16x16x32_bf16 v[164:167],v[56:59],v[8:11],v[164:167]\n"
"buffer_load_dwordx4 v111,s[28:31],s86 offen lds\n"
"v_mfma_f32_16x16x32_bf16 v[164:167],v[60:63],v[12:15],v[164:167]\n"
"s_add_u32 m0,s85,139264\n"
"s_xor_b32 s86,s86,262144\n"
"v_mfma_f32_16x16x32_bf16 v[132:135],v[60:63],v[4:7],v[132:135]\n"
"buffer_load_dwordx4 v110,s[28:31],s86 offen lds\n"
"v_mfma_f32_16x16x32_bf16 v[132:135],v[56:59],v[0:3],v[132:135]\n"
"v_accvgpr_read_b32 v119,a171\n"
"v_accvgpr_read_b32 v120,a172\n"
"v_mfma_f32_16x16x32_bf16 v[136:139],v[64:67],v[0:3],v[136:139]\n"
"ds_read_b64_tr_b16 v[56:57],v101 offset:0\n"
"v_accvgpr_read_b32 v121,a173\n"
"v_mfma_f32_16x16x32_bf16 v[136:139],v[68:71],v[4:7],v[136:139]\n"
"ds_read_b64_tr_b16 v[58:59],v101 offset:1024\n"
"v_accvgpr_read_b32 v122,a174\n"
"v_mfma_f32_16x16x32_bf16 v[168:171],v[68:71],v[12:15],v[168:171]\n"
"ds_read_b64_tr_b16 v[60:61],v101 offset:8192\n"
"v_accvgpr_read_b32 v123,a175\n"
"v_mfma_f32_16x16x32_bf16 v[168:171],v[64:67],v[8:11],v[168:171]\n"
"s_add_u32 m0,s85,140288\n"
"ds_read_b64_tr_b16 v[62:63],v101 offset:9216\n"
"v_mfma_f32_16x16x32_bf16 v[200:203],v[64:67],v[16:19],v[200:203]\n"
"buffer_load_dwordx4 v111,s[28:31],s86 offen lds\n"
"v_mfma_f32_16x16x32_bf16 v[200:203],v[68:71],v[20:23],v[200:203]\n"
"s_xor_b32 s86,s86,8126464\n"
"v_cvt_pk_bf16_f32 v116,v116,v117\n"
"v_cvt_pk_bf16_f32 v117,v118,v119\n"
"v_mfma_f32_16x16x32_bf16 v[232:235],v[68:71],v[28:31],v[232:235]\n"
"v_cvt_pk_bf16_f32 v118,v120,v121\n"
"v_cvt_pk_bf16_f32 v119,v122,v123\n"
"v_mfma_f32_16x16x32_bf16 v[232:235],v[64:67],v[24:27],v[232:235]\n"
"buffer_store_dwordx4 v[116:119],v112,s[24:27],s56 offen offset:1024 sc1\n"
"v_mfma_f32_16x16x32_bf16 v[236:239],v[72:75],v[24:27],v[236:239]\n"
"ds_read_b64_tr_b16 v[64:65],v102 offset:0\n"
"v_accvgpr_read_b32 v116,a176\n"
"v_mfma_f32_16x16x32_bf16 v[236:239],v[76:79],v[28:31],v[236:239]\n"
"ds_read_b64_tr_b16 v[66:67],v102 offset:1024\n"
"v_accvgpr_read_b32 v117,a177\n"
"v_mfma_f32_16x16x32_bf16 v[204:207],v[76:79],v[20:23],v[204:207]\n"
"ds_read_b64_tr_b16 v[68:69],v102 offset:8192\n"
"v_accvgpr_read_b32 v118,a178\n"
"v_mfma_f32_16x16x32_bf16 v[204:207],v[72:75],v[16:19],v[204:207]\n"
"s_add_u32 m0,s78,0\n"
"ds_read_b64_tr_b16 v[70:71],v102 offset:9216\n"
"v_accvgpr_read_b32 v121,a181\n"
"v_mfma_f32_16x16x32_bf16 v[172:175],v[72:75],v[8:11],v[172:175]\n"
"buffer_load_dwordx4 v112,s[24:27],s76 offen offset:0 sc1 lds\n"
"v_mfma_f32_16x16x32_bf16 v[172:175],v[76:79],v[12:15],v[172:175]\n"
"v_accvgpr_read_b32 v119,a179\n"
"v_accvgpr_read_b32 v120,a180\n"
"v_mfma_f32_16x16x32_bf16 v[140:143],v[76:79],v[4:7],v[140:143]\n"
"buffer_load_dwordx4 v112,s[24:27],s76 offen offset:1024 sc1 lds\n"
"v_mfma_f32_16x16x32_bf16 v[140:143],v[72:75],v[0:3],v[140:143]\n"
"v_accvgpr_read_b32 v122,a182\n"
"s_waitcnt vmcnt(24)\n"
"v_mfma_f32_16x16x32_bf16 v[144:147],v[80:83],v[0:3],v[144:147]\n"
"ds_read_b64_tr_b16 v[72:73],v103 offset:0\n"
"v_accvgpr_read_b32 v123,a183\n"
"v_mfma_f32_16x16x32_bf16 v[144:147],v[84:87],v[4:7],v[144:147]\n"
"ds_read_b64_tr_b16 v[74:75],v103 offset:1024\n"
"v_cvt_pk_bf16_f32 v116,v116,v117\n"
"v_mfma_f32_16x16x32_bf16 v[176:179],v[84:87],v[12:15],v[176:179]\n"
"ds_read_b64_tr_b16 v[76:77],v103 offset:8192\n"
"v_cvt_pk_bf16_f32 v117,v118,v119\n"
"v_mfma_f32_16x16x32_bf16 v[176:179],v[80:83],v[8:11],v[176:179]\n"
"ds_read_b64_tr_b16 v[78:79],v103 offset:9216\n"
"v_cvt_pk_bf16_f32 v118,v120,v121\n"
"v_mfma_f32_16x16x32_bf16 v[208:211],v[80:83],v[16:19],v[208:211]\n"
"buffer_load_dwordx4 v112,s[24:27],s76 offen offset:2048 sc1 lds\n"
"v_mfma_f32_16x16x32_bf16 v[208:211],v[84:87],v[20:23],v[208:211]\n"
"ds_read_b128 a[0:3],v96 offset:32768\n"
"v_cvt_pk_bf16_f32 v119,v122,v123\n"
"v_mfma_f32_16x16x32_bf16 v[240:243],v[84:87],v[28:31],v[240:243]\n"
"buffer_store_dwordx4 v[116:119],v112,s[24:27],s56 offen offset:2048 sc1\n"
"v_mfma_f32_16x16x32_bf16 v[240:243],v[80:83],v[24:27],v[240:243]\n"
"ds_read_b128 a[8:11],v96 offset:33792\n"
"v_accvgpr_read_b32 v116,a184\n"
"v_accvgpr_read_b32 v117,a185\n"
"v_mfma_f32_16x16x32_bf16 v[244:247],v[88:91],v[24:27],v[244:247]\n"
"ds_read_b64_tr_b16 v[80:81],v104 offset:0\n"
"v_accvgpr_read_b32 v118,a186\n"
"v_mfma_f32_16x16x32_bf16 v[244:247],v[92:95],v[28:31],v[244:247]\n"
"ds_read_b64_tr_b16 v[82:83],v104 offset:1024\n"
"v_accvgpr_read_b32 v119,a187\n"
"v_mfma_f32_16x16x32_bf16 v[212:215],v[92:95],v[20:23],v[212:215]\n"
"ds_read_b64_tr_b16 v[84:85],v104 offset:8192\n"
"v_accvgpr_read_b32 v120,a188\n"
"v_mfma_f32_16x16x32_bf16 v[212:215],v[88:91],v[16:19],v[212:215]\n"
"ds_read_b64_tr_b16 v[86:87],v104 offset:9216\n"
"v_accvgpr_read_b32 v121,a189\n"
"v_mfma_f32_16x16x32_bf16 v[180:183],v[88:91],v[8:11],v[180:183]\n"
"buffer_load_dwordx4 v112,s[24:27],s76 offen offset:3072 sc1 lds\n"
"v_mfma_f32_16x16x32_bf16 v[180:183],v[92:95],v[12:15],v[180:183]\n"
"s_add_u32 m0,s78,16384\n"
"s_xor_b32 s76,s76,16384\n"
"ds_read_b128 a[16:19],v96 offset:34816\n"
"v_mfma_f32_16x16x32_bf16 v[148:151],v[92:95],v[4:7],v[148:151]\n"
"buffer_load_dwordx4 v112,s[24:27],s76 offen offset:0 sc1 lds\n"
"v_mfma_f32_16x16x32_bf16 v[148:151],v[88:91],v[0:3],v[148:151]\n"
"ds_read_b128 a[24:27],v96 offset:35840\n"
"v_accvgpr_read_b32 v122,a190\n"
"v_mfma_f32_16x16x32_bf16 v[152:155],a[32:35],v[0:3],v[152:155]\n"
"ds_read_b64_tr_b16 v[88:89],v105 offset:0\n"
"v_accvgpr_read_b32 v123,a191\n"
"v_mfma_f32_16x16x32_bf16 v[152:155],a[36:39],v[4:7],v[152:155]\n"
"ds_read_b64_tr_b16 v[90:91],v105 offset:1024\n"
"v_cvt_pk_bf16_f32 v116,v116,v117\n"
"v_mfma_f32_16x16x32_bf16 v[184:187],a[36:39],v[12:15],v[184:187]\n"
"ds_read_b64_tr_b16 v[92:93],v105 offset:8192\n"
"v_cvt_pk_bf16_f32 v117,v118,v119\n"
"v_mfma_f32_16x16x32_bf16 v[184:187],a[32:35],v[8:11],v[184:187]\n"
"ds_read_b64_tr_b16 v[94:95],v105 offset:9216\n"
"v_cvt_pk_bf16_f32 v118,v120,v121\n"
"v_mfma_f32_16x16x32_bf16 v[216:219],a[32:35],v[16:19],v[216:219]\n"
"ds_read_b128 a[4:7],v96 offset:49152\n"
"v_cvt_pk_bf16_f32 v119,v122,v123\n"
"v_mfma_f32_16x16x32_bf16 v[216:219],a[36:39],v[20:23],v[216:219]\n"
"ds_read_b128 a[12:15],v96 offset:50176\n"
"buffer_store_dwordx4 v[116:119],v112,s[24:27],s56 offen offset:3072 sc1\n"
"v_mfma_f32_16x16x32_bf16 v[248:251],a[36:39],v[28:31],v[248:251]\n"
"ds_read_b128 a[20:23],v96 offset:51200\n"
"v_accvgpr_read_b32 v116,a248\n"
"v_mfma_f32_16x16x32_bf16 v[248:251],a[32:35],v[24:27],v[248:251]\n"
"ds_read_b128 a[28:31],v96 offset:52224\n"
"v_accvgpr_read_b32 v117,a249\n"
"v_mfma_f32_16x16x32_bf16 v[252:255],a[40:43],v[24:27],v[252:255]\n"
"buffer_load_dwordx4 v112,s[24:27],s76 offen offset:1024 sc1 lds\n"
"v_mfma_f32_16x16x32_bf16 v[252:255],a[44:47],v[28:31],v[252:255]\n"
"v_accvgpr_read_b32 v118,a250\n"
"v_accvgpr_read_b32 v119,a251\n"
"v_mfma_f32_16x16x32_bf16 v[220:223],a[44:47],v[20:23],v[220:223]\n"
"v_accvgpr_read_b32 v120,a252\n"
"v_accvgpr_read_b32 v121,a253\n"
"v_mfma_f32_16x16x32_bf16 v[220:223],a[40:43],v[16:19],v[220:223]\n"
"buffer_load_dwordx4 v112,s[24:27],s76 offen offset:2048 sc1 lds\n"
"v_mfma_f32_16x16x32_bf16 v[188:191],a[40:43],v[8:11],v[188:191]\n"
"v_accvgpr_read_b32 v122,a254\n"
"v_accvgpr_read_b32 v123,a255\n"
"v_mfma_f32_16x16x32_bf16 v[188:191],a[44:47],v[12:15],v[188:191]\n"
"v_cvt_pk_bf16_f32 v116,v116,v117\n"
"v_cvt_pk_bf16_f32 v117,v118,v119\n"
"v_mfma_f32_16x16x32_bf16 v[156:159],a[44:47],v[4:7],v[156:159]\n"
"buffer_load_dwordx4 v112,s[24:27],s76 offen offset:3072 sc1 lds\n"
"v_mfma_f32_16x16x32_bf16 v[156:159],a[40:43],v[0:3],v[156:159]\n"
"s_xor_b32 s76,s76,507904\n"
"v_cvt_pk_bf16_f32 v118,v120,v121\n"
"s_waitcnt vmcnt(27) lgkmcnt(0)\n"
"s_barrier\n"
"v_mfma_f32_16x16x32_bf16 v[128:131],v[32:35],a[0:3],v[128:131]\n"
"ds_read_b64_tr_b16 a[32:33],v98 offset:49152\n"
"ds_read_b64_tr_b16 a[40:41],v99 offset:49152\n"
"v_mfma_f32_16x16x32_bf16 v[128:131],v[36:39],a[4:7],v[128:131]\n"
"ds_read_b64_tr_b16 a[34:35],v98 offset:50176\n"
"ds_read_b64_tr_b16 a[42:43],v99 offset:50176\n"
"v_mfma_f32_16x16x32_bf16 v[160:163],v[36:39],a[12:15],v[160:163]\n"
"ds_read_b64_tr_b16 a[36:37],v98 offset:57344\n"
"ds_read_b64_tr_b16 a[44:45],v99 offset:57344\n"
"v_mfma_f32_16x16x32_bf16 v[160:163],v[32:35],a[8:11],v[160:163]\n"
"s_add_u32 m0,s85,98304\n"
"ds_read_b64_tr_b16 a[38:39],v98 offset:58368\n"
"ds_read_b64_tr_b16 a[46:47],v99 offset:58368\n"
"v_mfma_f32_16x16x32_bf16 v[192:195],v[32:35],a[16:19],v[192:195]\n"
"buffer_load_dwordx4 v110,s[28:31],s86 offen lds\n"
"v_mfma_f32_16x16x32_bf16 v[192:195],v[36:39],a[20:23],v[192:195]\n"
"v_cvt_pk_bf16_f32 v119,v122,v123\n"
"v_accvgpr_write_b32 a252,v116\n"
"v_mfma_f32_16x16x32_bf16 v[224:227],v[36:39],a[28:31],v[224:227]\n"
"v_accvgpr_write_b32 a253,v117\n"
"v_accvgpr_write_b32 a254,v118\n"
"v_mfma_f32_16x16x32_bf16 v[224:227],v[32:35],a[24:27],v[224:227]\n"
"v_accvgpr_write_b32 a255,v119\n"
"v_accvgpr_read_b32 v116,a240\n"
"v_mfma_f32_16x16x32_bf16 v[228:231],v[40:43],a[24:27],v[228:231]\n"
"ds_read_b64_tr_b16 v[32:33],v100 offset:49152\n"
"v_accvgpr_read_b32 v117,a241\n"
"v_mfma_f32_16x16x32_bf16 v[228:231],v[44:47],a[28:31],v[228:231]\n"
"ds_read_b64_tr_b16 v[34:35],v100 offset:50176\n"
"v_accvgpr_read_b32 v118,a242\n"
"v_mfma_f32_16x16x32_bf16 v[196:199],v[44:47],a[20:23],v[196:199]\n"
"ds_read_b64_tr_b16 v[36:37],v100 offset:57344\n"
"v_accvgpr_read_b32 v119,a243\n"
"v_mfma_f32_16x16x32_bf16 v[196:199],v[40:43],a[16:19],v[196:199]\n"
"s_add_u32 m0,s85,99328\n"
"ds_read_b64_tr_b16 v[38:39],v100 offset:58368\n"
"v_mfma_f32_16x16x32_bf16 v[164:167],v[40:43],a[8:11],v[164:167]\n"
"buffer_load_dwordx4 v111,s[28:31],s86 offen lds\n"
"v_mfma_f32_16x16x32_bf16 v[164:167],v[44:47],a[12:15],v[164:167]\n"
"s_add_u32 m0,s85,106496\n"
"s_xor_b32 s86,s86,262144\n"
"v_mfma_f32_16x16x32_bf16 v[132:135],v[44:47],a[4:7],v[132:135]\n"
"buffer_load_dwordx4 v110,s[28:31],s86 offen lds\n"
"v_mfma_f32_16x16x32_bf16 v[132:135],v[40:43],a[0:3],v[132:135]\n"
"v_accvgpr_read_b32 v120,a244\n"
"v_accvgpr_read_b32 v121,a245\n"
"v_mfma_f32_16x16x32_bf16 v[136:139],v[48:51],a[0:3],v[136:139]\n"
"ds_read_b64_tr_b16 v[40:41],v101 offset:49152\n"
"v_accvgpr_read_b32 v122,a246\n"
"v_mfma_f32_16x16x32_bf16 v[136:139],v[52:55],a[4:7],v[136:139]\n"
"ds_read_b64_tr_b16 v[42:43],v101 offset:50176\n"
"v_accvgpr_read_b32 v123,a247\n"
"v_mfma_f32_16x16x32_bf16 v[168:171],v[52:55],a[12:15],v[168:171]\n"
"ds_read_b64_tr_b16 v[44:45],v101 offset:57344\n"
"v_cvt_pk_bf16_f32 v116,v116,v117\n"
"v_mfma_f32_16x16x32_bf16 v[168:171],v[48:51],a[8:11],v[168:171]\n"
"s_add_u32 m0,s85,107520\n"
"ds_read_b64_tr_b16 v[46:47],v101 offset:58368\n"
"v_mfma_f32_16x16x32_bf16 v[200:203],v[48:51],a[16:19],v[200:203]\n"
"buffer_load_dwordx4 v111,s[28:31],s86 offen lds\n"
"v_mfma_f32_16x16x32_bf16 v[200:203],v[52:55],a[20:23],v[200:203]\n"
"s_xor_b32 s86,s86,786432\n"
"v_cvt_pk_bf16_f32 v117,v118,v119\n"
"v_mfma_f32_16x16x32_bf16 v[232:235],v[52:55],a[28:31],v[232:235]\n"
"v_cvt_pk_bf16_f32 v118,v120,v121\n"
"v_cvt_pk_bf16_f32 v119,v122,v123\n"
"v_mfma_f32_16x16x32_bf16 v[232:235],v[48:51],a[24:27],v[232:235]\n"
"v_accvgpr_write_b32 a248,v116\n"
"v_accvgpr_write_b32 a249,v117\n"
"v_mfma_f32_16x16x32_bf16 v[236:239],v[56:59],a[24:27],v[236:239]\n"
"ds_read_b64_tr_b16 v[48:49],v102 offset:49152\n"
"v_accvgpr_write_b32 a250,v118\n"
"v_mfma_f32_16x16x32_bf16 v[236:239],v[60:63],a[28:31],v[236:239]\n"
"ds_read_b64_tr_b16 v[50:51],v102 offset:50176\n"
"v_accvgpr_write_b32 a251,v119\n"
"v_mfma_f32_16x16x32_bf16 v[204:207],v[60:63],a[20:23],v[204:207]\n"
"ds_read_b64_tr_b16 v[52:53],v102 offset:57344\n"
"v_accvgpr_read_b32 v116,a232\n"
"v_mfma_f32_16x16x32_bf16 v[204:207],v[56:59],a[16:19],v[204:207]\n"
"s_add_u32 m0,s78,32768\n"
"ds_read_b64_tr_b16 v[54:55],v102 offset:58368\n"
"v_accvgpr_read_b32 v119,a235\n"
"v_mfma_f32_16x16x32_bf16 v[172:175],v[56:59],a[8:11],v[172:175]\n"
"buffer_load_dwordx4 v112,s[24:27],s76 offen offset:0 sc1 lds\n"
"v_mfma_f32_16x16x32_bf16 v[172:175],v[60:63],a[12:15],v[172:175]\n"
"v_accvgpr_read_b32 v117,a233\n"
"v_accvgpr_read_b32 v118,a234\n"
"v_mfma_f32_16x16x32_bf16 v[140:143],v[60:63],a[4:7],v[140:143]\n"
"buffer_load_dwordx4 v112,s[24:27],s76 offen offset:1024 sc1 lds\n"
"v_mfma_f32_16x16x32_bf16 v[140:143],v[56:59],a[0:3],v[140:143]\n"
"v_accvgpr_read_b32 v120,a236\n"
"s_waitcnt vmcnt(22)\n"
"v_mfma_f32_16x16x32_bf16 v[144:147],v[64:67],a[0:3],v[144:147]\n"
"ds_read_b64_tr_b16 v[56:57],v103 offset:49152\n"
"v_accvgpr_read_b32 v121,a237\n"
"v_mfma_f32_16x16x32_bf16 v[144:147],v[68:71],a[4:7],v[144:147]\n"
"ds_read_b64_tr_b16 v[58:59],v103 offset:50176\n"
"v_accvgpr_read_b32 v122,a238\n"
"v_mfma_f32_16x16x32_bf16 v[176:179],v[68:71],a[12:15],v[176:179]\n"
"ds_read_b64_tr_b16 v[60:61],v103 offset:57344\n"
"v_accvgpr_read_b32 v123,a239\n"
"v_mfma_f32_16x16x32_bf16 v[176:179],v[64:67],a[8:11],v[176:179]\n"
"ds_read_b64_tr_b16 v[62:63],v103 offset:58368\n"
"v_cvt_pk_bf16_f32 v116,v116,v117\n"
"v_mfma_f32_16x16x32_bf16 v[208:211],v[64:67],a[16:19],v[208:211]\n"
"buffer_load_dwordx4 v112,s[24:27],s76 offen offset:2048 sc1 lds\n"
"v_mfma_f32_16x16x32_bf16 v[208:211],v[68:71],a[20:23],v[208:211]\n"
"ds_read_b128 v[0:3],v97 offset:0\n"
"v_cvt_pk_bf16_f32 v117,v118,v119\n"
"v_mfma_f32_16x16x32_bf16 v[240:243],v[68:71],a[28:31],v[240:243]\n"
"v_cvt_pk_bf16_f32 v118,v120,v121\n"
"v_cvt_pk_bf16_f32 v119,v122,v123\n"
"v_mfma_f32_16x16x32_bf16 v[240:243],v[64:67],a[24:27],v[240:243]\n"
"ds_read_b128 v[8:11],v97 offset:1024\n"
"v_accvgpr_write_b32 a244,v116\n"
"v_mfma_f32_16x16x32_bf16 v[244:247],v[72:75],a[24:27],v[244:247]\n"
"ds_read_b64_tr_b16 v[64:65],v104 offset:49152\n"
"v_accvgpr_write_b32 a245,v117\n"
"v_mfma_f32_16x16x32_bf16 v[244:247],v[76:79],a[28:31],v[244:247]\n"
"ds_read_b64_tr_b16 v[66:67],v104 offset:50176\n"
"v_accvgpr_write_b32 a246,v118\n"
"v_mfma_f32_16x16x32_bf16 v[212:215],v[76:79],a[20:23],v[212:215]\n"
"ds_read_b64_tr_b16 v[68:69],v104 offset:57344\n"
"v_accvgpr_write_b32 a247,v119\n"
"v_mfma_f32_16x16x32_bf16 v[212:215],v[72:75],a[16:19],v[212:215]\n"
"ds_read_b64_tr_b16 v[70:71],v104 offset:58368\n"
"v_accvgpr_read_b32 v116,a224\n"
"v_mfma_f32_16x16x32_bf16 v[180:183],v[72:75],a[8:11],v[180:183]\n"
"buffer_load_dwordx4 v112,s[24:27],s76 offen offset:3072 sc1 lds\n"
"v_mfma_f32_16x16x32_bf16 v[180:183],v[76:79],a[12:15],v[180:183]\n"
"s_add_u32 m0,s78,49152\n"
"s_xor_b32 s76,s76,16384\n"
"ds_read_b128 v[16:19],v97 offset:2048\n"
"v_mfma_f32_16x16x32_bf16 v[148:151],v[76:79],a[4:7],v[148:151]\n"
"buffer_load_dwordx4 v112,s[24:27],s76 offen offset:0 sc1 lds\n"
"v_mfma_f32_16x16x32_bf16 v[148:151],v[72:75],a[0:3],v[148:151]\n"
"ds_read_b128 v[24:27],v97 offset:3072\n"
"v_accvgpr_read_b32 v117,a225\n"
"v_mfma_f32_16x16x32_bf16 v[152:155],v[80:83],a[0:3],v[152:155]\n"
"ds_read_b64_tr_b16 v[72:73],v105 offset:49152\n"
"v_accvgpr_read_b32 v118,a226\n"
"v_mfma_f32_16x16x32_bf16 v[152:155],v[84:87],a[4:7],v[152:155]\n"
"ds_read_b64_tr_b16 v[74:75],v105 offset:50176\n"
"v_accvgpr_read_b32 v119,a227\n"
"v_mfma_f32_16x16x32_bf16 v[184:187],v[84:87],a[12:15],v[184:187]\n"
"ds_read_b64_tr_b16 v[76:77],v105 offset:57344\n"
"v_accvgpr_read_b32 v120,a228\n"
"v_mfma_f32_16x16x32_bf16 v[184:187],v[80:83],a[8:11],v[184:187]\n"
"ds_read_b64_tr_b16 v[78:79],v105 offset:58368\n"
"v_accvgpr_read_b32 v121,a229\n"
"v_mfma_f32_16x16x32_bf16 v[216:219],v[80:83],a[16:19],v[216:219]\n"
"ds_read_b128 v[4:7],v97 offset:16384\n"
"v_accvgpr_read_b32 v122,a230\n"
"v_mfma_f32_16x16x32_bf16 v[216:219],v[84:87],a[20:23],v[216:219]\n"
"ds_read_b128 v[12:15],v97 offset:17408\n"
"v_accvgpr_read_b32 v123,a231\n"
"v_mfma_f32_16x16x32_bf16 v[248:251],v[84:87],a[28:31],v[248:251]\n"
"ds_read_b128 v[20:23],v97 offset:18432\n"
"v_cvt_pk_bf16_f32 v116,v116,v117\n"
"v_mfma_f32_16x16x32_bf16 v[248:251],v[80:83],a[24:27],v[248:251]\n"
"ds_read_b128 v[28:31],v97 offset:19456\n"
"v_cvt_pk_bf16_f32 v117,v118,v119\n"
"v_mfma_f32_16x16x32_bf16 v[252:255],v[88:91],a[24:27],v[252:255]\n"
"buffer_load_dwordx4 v112,s[24:27],s76 offen offset:1024 sc1 lds\n"
"v_mfma_f32_16x16x32_bf16 v[252:255],v[92:95],a[28:31],v[252:255]\n"
"v_cvt_pk_bf16_f32 v118,v120,v121\n"
"v_cvt_pk_bf16_f32 v119,v122,v123\n"
"v_mfma_f32_16x16x32_bf16 v[220:223],v[92:95],a[20:23],v[220:223]\n"
"v_accvgpr_write_b32 a240,v116\n"
"v_accvgpr_write_b32 a241,v117\n"
"v_mfma_f32_16x16x32_bf16 v[220:223],v[88:91],a[16:19],v[220:223]\n"
"buffer_load_dwordx4 v112,s[24:27],s76 offen offset:2048 sc1 lds\n"
"v_mfma_f32_16x16x32_bf16 v[188:191],v[88:91],a[8:11],v[188:191]\n"
"v_accvgpr_write_b32 a242,v118\n"
"v_accvgpr_write_b32 a243,v119\n"
"v_mfma_f32_16x16x32_bf16 v[188:191],v[92:95],a[12:15],v[188:191]\n"
"v_accvgpr_read_b32 v116,a216\n"
"v_accvgpr_read_b32 v117,a217\n"
"v_mfma_f32_16x16x32_bf16 v[156:159],v[92:95],a[4:7],v[156:159]\n"
"buffer_load_dwordx4 v112,s[24:27],s76 offen offset:3072 sc1 lds\n"
"v_mfma_f32_16x16x32_bf16 v[156:159],v[88:91],a[0:3],v[156:159]\n"
"s_xor_b32 s76,s76,49152\n"
"v_accvgpr_read_b32 v118,a218\n"
"s_waitcnt vmcnt(23) lgkmcnt(0)\n"
"s_barrier\n"
"v_mfma_f32_16x16x32_bf16 v[128:131],a[32:35],v[0:3],v[128:131]\n"
"ds_read_b64_tr_b16 v[80:81],v98 offset:32768\n"
"ds_read_b64_tr_b16 v[88:89],v99 offset:32768\n"
"v_mfma_f32_16x16x32_bf16 v[128:131],a[36:39],v[4:7],v[128:131]\n"
"ds_read_b64_tr_b16 v[82:83],v98 offset:33792\n"
"ds_read_b64_tr_b16 v[90:91],v99 offset:33792\n"
"v_mfma_f32_16x16x32_bf16 v[160:163],a[36:39],v[12:15],v[160:163]\n"
"ds_read_b64_tr_b16 v[84:85],v98 offset:40960\n"
"ds_read_b64_tr_b16 v[92:93],v99 offset:40960\n"
"v_mfma_f32_16x16x32_bf16 v[160:163],a[32:35],v[8:11],v[160:163]\n"
"s_add_u32 m0,s85,147456\n"
"ds_read_b64_tr_b16 v[86:87],v98 offset:41984\n"
"ds_read_b64_tr_b16 v[94:95],v99 offset:41984\n"
"v_mfma_f32_16x16x32_bf16 v[192:195],a[32:35],v[16:19],v[192:195]\n"
"buffer_load_dwordx4 v110,s[28:31],s86 offen lds\n"
"v_mfma_f32_16x16x32_bf16 v[192:195],a[36:39],v[20:23],v[192:195]\n"
"v_accvgpr_read_b32 v119,a219\n"
"v_accvgpr_read_b32 v120,a220\n"
"v_mfma_f32_16x16x32_bf16 v[224:227],a[36:39],v[28:31],v[224:227]\n"
"v_accvgpr_read_b32 v121,a221\n"
"v_accvgpr_read_b32 v122,a222\n"
"v_mfma_f32_16x16x32_bf16 v[224:227],a[32:35],v[24:27],v[224:227]\n"
"v_accvgpr_read_b32 v123,a223\n"
"v_cvt_pk_bf16_f32 v116,v116,v117\n"
"v_mfma_f32_16x16x32_bf16 v[228:231],a[40:43],v[24:27],v[228:231]\n"
"ds_read_b64_tr_b16 a[32:33],v100 offset:32768\n"
"v_cvt_pk_bf16_f32 v117,v118,v119\n"
"v_mfma_f32_16x16x32_bf16 v[228:231],a[44:47],v[28:31],v[228:231]\n"
"ds_read_b64_tr_b16 a[34:35],v100 offset:33792\n"
"v_cvt_pk_bf16_f32 v118,v120,v121\n"
"v_mfma_f32_16x16x32_bf16 v[196:199],a[44:47],v[20:23],v[196:199]\n"
"ds_read_b64_tr_b16 a[36:37],v100 offset:40960\n"
"v_cvt_pk_bf16_f32 v119,v122,v123\n"
"v_mfma_f32_16x16x32_bf16 v[196:199],a[40:43],v[16:19],v[196:199]\n"
"s_add_u32 m0,s85,148480\n"
"ds_read_b64_tr_b16 a[38:39],v100 offset:41984\n"
"v_mfma_f32_16x16x32_bf16 v[164:167],a[40:43],v[8:11],v[164:167]\n"
"buffer_load_dwordx4 v111,s[28:31],s86 offen lds\n"
"v_mfma_f32_16x16x32_bf16 v[164:167],a[44:47],v[12:15],v[164:167]\n"
"s_add_u32 m0,s85,155648\n"
"s_xor_b32 s86,s86,262144\n"
"v_mfma_f32_16x16x32_bf16 v[132:135],a[44:47],v[4:7],v[132:135]\n"
"buffer_load_dwordx4 v110,s[28:31],s86 offen lds\n"
"v_mfma_f32_16x16x32_bf16 v[132:135],a[40:43],v[0:3],v[132:135]\n"
"v_accvgpr_write_b32 a236,v116\n"
"v_accvgpr_write_b32 a237,v117\n"
"v_mfma_f32_16x16x32_bf16 v[136:139],v[32:35],v[0:3],v[136:139]\n"
"ds_read_b64_tr_b16 a[40:41],v101 offset:32768\n"
"v_accvgpr_write_b32 a238,v118\n"
"v_mfma_f32_16x16x32_bf16 v[136:139],v[36:39],v[4:7],v[136:139]\n"
"ds_read_b64_tr_b16 a[42:43],v101 offset:33792\n"
"v_accvgpr_write_b32 a239,v119\n"
"v_mfma_f32_16x16x32_bf16 v[168:171],v[36:39],v[12:15],v[168:171]\n"
"ds_read_b64_tr_b16 a[44:45],v101 offset:40960\n"
"v_accvgpr_read_b32 v116,a208\n"
"v_mfma_f32_16x16x32_bf16 v[168:171],v[32:35],v[8:11],v[168:171]\n"
"s_add_u32 m0,s85,156672\n"
"ds_read_b64_tr_b16 a[46:47],v101 offset:41984\n"
"v_mfma_f32_16x16x32_bf16 v[200:203],v[32:35],v[16:19],v[200:203]\n"
"buffer_load_dwordx4 v111,s[28:31],s86 offen lds\n"
"v_mfma_f32_16x16x32_bf16 v[200:203],v[36:39],v[20:23],v[200:203]\n"
"s_xor_b32 s86,s86,1835008\n"
"v_accvgpr_read_b32 v117,a209\n"
"v_mfma_f32_16x16x32_bf16 v[232:235],v[36:39],v[28:31],v[232:235]\n"
"v_accvgpr_read_b32 v118,a210\n"
"v_accvgpr_read_b32 v119,a211\n"
"v_mfma_f32_16x16x32_bf16 v[232:235],v[32:35],v[24:27],v[232:235]\n"
"v_accvgpr_read_b32 v120,a212\n"
"v_accvgpr_read_b32 v121,a213\n"
"v_mfma_f32_16x16x32_bf16 v[236:239],v[40:43],v[24:27],v[236:239]\n"
"ds_read_b64_tr_b16 v[32:33],v102 offset:32768\n"
"v_accvgpr_read_b32 v122,a214\n"
"v_mfma_f32_16x16x32_bf16 v[236:239],v[44:47],v[28:31],v[236:239]\n"
"ds_read_b64_tr_b16 v[34:35],v102 offset:33792\n"
"v_accvgpr_read_b32 v123,a215\n"
"v_mfma_f32_16x16x32_bf16 v[204:207],v[44:47],v[20:23],v[204:207]\n"
"ds_read_b64_tr_b16 v[36:37],v102 offset:40960\n"
"v_cvt_pk_bf16_f32 v116,v116,v117\n"
"v_mfma_f32_16x16x32_bf16 v[204:207],v[40:43],v[16:19],v[204:207]\n"
"s_add_u32 m0,s78,65536\n"
"ds_read_b64_tr_b16 v[38:39],v102 offset:41984\n"
"v_accvgpr_write_b32 a232,v116\n"
"v_mfma_f32_16x16x32_bf16 v[172:175],v[40:43],v[8:11],v[172:175]\n"
"buffer_load_dwordx4 v112,s[24:27],s76 offen offset:0 sc1 lds\n"
"v_mfma_f32_16x16x32_bf16 v[172:175],v[44:47],v[12:15],v[172:175]\n"
"v_cvt_pk_bf16_f32 v117,v118,v119\n"
"v_cvt_pk_bf16_f32 v118,v120,v121\n"
"v_mfma_f32_16x16x32_bf16 v[140:143],v[44:47],v[4:7],v[140:143]\n"
"buffer_load_dwordx4 v112,s[24:27],s76 offen offset:1024 sc1 lds\n"
"v_mfma_f32_16x16x32_bf16 v[140:143],v[40:43],v[0:3],v[140:143]\n"
"v_cvt_pk_bf16_f32 v119,v122,v123\n"
"s_waitcnt vmcnt(18)\n"
"v_mfma_f32_16x16x32_bf16 v[144:147],v[48:51],v[0:3],v[144:147]\n"
"ds_read_b64_tr_b16 v[40:41],v103 offset:32768\n"
"v_accvgpr_write_b32 a233,v117\n"
"v_lshrrev_b32 v106,8,v112\n"
"v_mfma_f32_16x16x32_bf16 v[144:147],v[52:55],v[4:7],v[144:147]\n"
"ds_read_b64_tr_b16 v[42:43],v103 offset:33792\n"
"v_accvgpr_write_b32 a234,v118\n"
"v_lshlrev_b32 v107,6,v106\n"
"v_mfma_f32_16x16x32_bf16 v[176:179],v[52:55],v[12:15],v[176:179]\n"
"ds_read_b64_tr_b16 v[44:45],v103 offset:40960\n"
"v_accvgpr_write_b32 a235,v119\n"
"v_and_b32 v108,0xf0,v112\n"
"v_mfma_f32_16x16x32_bf16 v[176:179],v[48:51],v[8:11],v[176:179]\n"
"ds_read_b64_tr_b16 v[46:47],v103 offset:41984\n"
"v_accvgpr_read_b32 v116,a200\n"
"v_xor_b32 v107,v107,v108\n"
"v_mfma_f32_16x16x32_bf16 v[208:211],v[48:51],v[16:19],v[208:211]\n"
"buffer_load_dwordx4 v112,s[24:27],s76 offen offset:2048 sc1 lds\n"
"v_lshl_add_u32 v106,v106,11,v107\n"
"v_mfma_f32_16x16x32_bf16 v[208:211],v[52:55],v[20:23],v[208:211]\n"
"ds_read_b128 a[0:3],v96 offset:0\n"
"v_accvgpr_read_b32 v117,a201\n"
"v_lshrrev_b32 v107,6,v112\n"
"v_mfma_f32_16x16x32_bf16 v[240:243],v[52:55],v[28:31],v[240:243]\n"
"v_accvgpr_read_b32 v118,a202\n"
"v_accvgpr_read_b32 v119,a203\n"
"v_and_b32 v108,3,v107\n"
"v_mfma_f32_16x16x32_bf16 v[240:243],v[48:51],v[24:27],v[240:243]\n"
"ds_read_b128 a[8:11],v96 offset:1024\n"
"v_accvgpr_read_b32 v120,a204\n"
"v_lshlrev_b32 v108,6,v108\n"
"v_mfma_f32_16x16x32_bf16 v[244:247],v[56:59],v[24:27],v[244:247]\n"
"ds_read_b64_tr_b16 v[48:49],v104 offset:32768\n"
"v_accvgpr_read_b32 v121,a205\n"
"v_mfma_f32_16x16x32_bf16 v[244:247],v[60:63],v[28:31],v[244:247]\n"
"s_lshl_b32 s82,s52,6\n"
"ds_read_b64_tr_b16 v[50:51],v104 offset:33792\n"
"v_accvgpr_read_b32 v122,a206\n"
"v_xor_b32 v108,s82,v108\n"
"v_mfma_f32_16x16x32_bf16 v[212:215],v[60:63],v[20:23],v[212:215]\n"
"ds_read_b64_tr_b16 v[52:53],v104 offset:40960\n"
"v_accvgpr_read_b32 v123,a207\n"
"v_lshl_add_u32 v107,v107,8,v108\n"
"v_mfma_f32_16x16x32_bf16 v[212:215],v[56:59],v[16:19],v[212:215]\n"
"ds_read_b64_tr_b16 v[54:55],v104 offset:41984\n"
"v_cvt_pk_bf16_f32 v116,v116,v117\n"
"v_and_b32 v108,0x30,v112\n"
"v_mfma_f32_16x16x32_bf16 v[180:183],v[56:59],v[8:11],v[180:183]\n"
"buffer_load_dwordx4 v112,s[24:27],s76 offen offset:3072 sc1 lds\n"
"v_add_u32 v107,v107,v108\n"
"v_mfma_f32_16x16x32_bf16 v[180:183],v[60:63],v[12:15],v[180:183]\n"
"s_add_u32 m0,s78,81920\n"
"s_xor_b32 s76,s76,16384\n"
"ds_read_b128 a[16:19],v96 offset:2048\n"
"v_mfma_f32_16x16x32_bf16 v[148:151],v[60:63],v[4:7],v[148:151]\n"
"buffer_load_dwordx4 v112,s[24:27],s76 offen offset:0 sc1 lds\n"
"v_add_u32 v107,114688,v107\n"
"v_mfma_f32_16x16x32_bf16 v[148:151],v[56:59],v[0:3],v[148:151]\n"
"ds_read_b128 a[24:27],v96 offset:3072\n"
"v_cvt_pk_bf16_f32 v117,v118,v119\n"
"v_mfma_f32_16x16x32_bf16 v[152:155],v[64:67],v[0:3],v[152:155]\n"
"ds_read_b64_tr_b16 v[56:57],v105 offset:32768\n"
"v_cvt_pk_bf16_f32 v118,v120,v121\n"
"v_mfma_f32_16x16x32_bf16 v[152:155],v[68:71],v[4:7],v[152:155]\n"
"ds_read_b64_tr_b16 v[58:59],v105 offset:33792\n"
"v_cvt_pk_bf16_f32 v119,v122,v123\n"
"v_mfma_f32_16x16x32_bf16 v[184:187],v[68:71],v[12:15],v[184:187]\n"
"ds_read_b64_tr_b16 v[60:61],v105 offset:40960\n"
"v_accvgpr_write_b32 a228,v116\n"
"v_mfma_f32_16x16x32_bf16 v[184:187],v[64:67],v[8:11],v[184:187]\n"
"ds_read_b64_tr_b16 v[62:63],v105 offset:41984\n"
"v_accvgpr_write_b32 a229,v117\n"
"v_mfma_f32_16x16x32_bf16 v[216:219],v[64:67],v[16:19],v[216:219]\n"
"ds_read_b128 a[4:7],v96 offset:16384\n"
"v_accvgpr_write_b32 a230,v118\n"
"v_mfma_f32_16x16x32_bf16 v[216:219],v[68:71],v[20:23],v[216:219]\n"
"ds_read_b128 a[12:15],v96 offset:17408\n"
"v_accvgpr_write_b32 a231,v119\n"
"v_mfma_f32_16x16x32_bf16 v[248:251],v[68:71],v[28:31],v[248:251]\n"
"ds_read_b128 a[20:23],v96 offset:18432\n"
"v_accvgpr_read_b32 v116,a192\n"
"v_mfma_f32_16x16x32_bf16 v[248:251],v[64:67],v[24:27],v[248:251]\n"
"ds_read_b128 a[28:31],v96 offset:19456\n"
"v_accvgpr_read_b32 v117,a193\n"
"v_mfma_f32_16x16x32_bf16 v[252:255],v[72:75],v[24:27],v[252:255]\n"
"buffer_load_dwordx4 v112,s[24:27],s76 offen offset:1024 sc1 lds\n"
"v_mfma_f32_16x16x32_bf16 v[252:255],v[76:79],v[28:31],v[252:255]\n"
"v_accvgpr_read_b32 v118,a194\n"
"v_accvgpr_read_b32 v119,a195\n"
"v_mfma_f32_16x16x32_bf16 v[220:223],v[76:79],v[20:23],v[220:223]\n"
"v_accvgpr_read_b32 v120,a196\n"
"v_accvgpr_read_b32 v121,a197\n"
"v_mfma_f32_16x16x32_bf16 v[220:223],v[72:75],v[16:19],v[220:223]\n"
"buffer_load_dwordx4 v112,s[24:27],s76 offen offset:2048 sc1 lds\n"
"v_mfma_f32_16x16x32_bf16 v[188:191],v[72:75],v[8:11],v[188:191]\n"
"v_accvgpr_read_b32 v122,a198\n"
"v_accvgpr_read_b32 v123,a199\n"
"v_mfma_f32_16x16x32_bf16 v[188:191],v[76:79],v[12:15],v[188:191]\n"
"v_cvt_pk_bf16_f32 v116,v116,v117\n"
"v_cvt_pk_bf16_f32 v117,v118,v119\n"
"v_mfma_f32_16x16x32_bf16 v[156:159],v[76:79],v[4:7],v[156:159]\n"
"buffer_load_dwordx4 v112,s[24:27],s76 offen offset:3072 sc1 lds\n"
"v_mfma_f32_16x16x32_bf16 v[156:159],v[72:75],v[0:3],v[156:159]\n"
"s_xor_b32 s76,s76,114688\n"
"v_cvt_pk_bf16_f32 v118,v120,v121\n"
"s_waitcnt vmcnt(20) lgkmcnt(0)\n"
"s_barrier\n"
"v_mfma_f32_16x16x32_bf16 v[128:131],v[80:83],a[0:3],v[128:131]\n"
"ds_read_b64_tr_b16 v[64:65],v98 offset:0\n"
"ds_read_b64_tr_b16 v[72:73],v99 offset:0\n"
"v_mfma_f32_16x16x32_bf16 v[128:131],v[84:87],a[4:7],v[128:131]\n"
"ds_read_b64_tr_b16 v[66:67],v98 offset:1024\n"
"ds_read_b64_tr_b16 v[74:75],v99 offset:1024\n"
"v_mfma_f32_16x16x32_bf16 v[160:163],v[84:87],a[12:15],v[160:163]\n"
"ds_read_b64_tr_b16 v[68:69],v98 offset:8192\n"
"ds_read_b64_tr_b16 v[76:77],v99 offset:8192\n"
"v_mfma_f32_16x16x32_bf16 v[160:163],v[80:83],a[8:11],v[160:163]\n"
"s_add_u32 m0,s85,131072\n"
"ds_read_b64_tr_b16 v[70:71],v98 offset:9216\n"
"ds_read_b64_tr_b16 v[78:79],v99 offset:9216\n"
"v_mfma_f32_16x16x32_bf16 v[192:195],v[80:83],a[16:19],v[192:195]\n"
"buffer_load_dwordx4 v110,s[28:31],s86 offen lds\n"
"v_mfma_f32_16x16x32_bf16 v[192:195],v[84:87],a[20:23],v[192:195]\n"
"s_add_u32 m0,s85,132096\n"
"v_cvt_pk_bf16_f32 v119,v122,v123\n"
"v_mfma_f32_16x16x32_bf16 v[224:227],v[84:87],a[28:31],v[224:227]\n"
"buffer_load_dwordx4 v111,s[28:31],s86 offen lds\n"
"v_mfma_f32_16x16x32_bf16 v[224:227],v[80:83],a[24:27],v[224:227]\n"
"s_xor_b32 s86,s86,262144\n"
"v_accvgpr_write_b32 a224,v116\n"
"v_mfma_f32_16x16x32_bf16 v[228:231],v[88:91],a[24:27],v[228:231]\n"
"ds_read_b64_tr_b16 v[80:81],v100 offset:0\n"
"v_accvgpr_write_b32 a225,v117\n"
"v_mfma_f32_16x16x32_bf16 v[228:231],v[92:95],a[28:31],v[228:231]\n"
"ds_read_b64_tr_b16 v[82:83],v100 offset:1024\n"
"v_accvgpr_write_b32 a226,v118\n"
"v_mfma_f32_16x16x32_bf16 v[196:199],v[92:95],a[20:23],v[196:199]\n"
"ds_read_b64_tr_b16 v[84:85],v100 offset:8192\n"
"v_accvgpr_write_b32 a227,v119\n"
"v_mfma_f32_16x16x32_bf16 v[196:199],v[88:91],a[16:19],v[196:199]\n"
"s_add_u32 m0,s85,139264\n"
"ds_read_b64_tr_b16 v[86:87],v100 offset:9216\n"
"v_mfma_f32_16x16x32_bf16 v[164:167],v[88:91],a[8:11],v[164:167]\n"
"buffer_load_dwordx4 v110,s[28:31],s86 offen lds\n"
"v_mfma_f32_16x16x32_bf16 v[164:167],v[92:95],a[12:15],v[164:167]\n"
"s_add_u32 m0,s85,140288\n"
"s_add_u32 s56,s56,126976\n"
"v_mfma_f32_16x16x32_bf16 v[132:135],v[92:95],a[4:7],v[132:135]\n"
"buffer_load_dwordx4 v111,s[28:31],s86 offen lds\n"
"v_mfma_f32_16x16x32_bf16 v[132:135],v[88:91],a[0:3],v[132:135]\n"
"s_xor_b32 s86,s86,786432\n"
"v_mfma_f32_16x16x32_bf16 v[136:139],a[32:35],a[0:3],v[136:139]\n"
"ds_read_b64_tr_b16 v[88:89],v101 offset:0\n"
"v_mfma_f32_16x16x32_bf16 v[136:139],a[36:39],a[4:7],v[136:139]\n"
"ds_read_b64_tr_b16 v[90:91],v101 offset:1024\n"
"v_mfma_f32_16x16x32_bf16 v[168:171],a[36:39],a[12:15],v[168:171]\n"
"ds_read_b64_tr_b16 v[92:93],v101 offset:8192\n"
"v_mfma_f32_16x16x32_bf16 v[168:171],a[32:35],a[8:11],v[168:171]\n"
"s_add_u32 m0,s78,0\n"
"ds_read_b64_tr_b16 v[94:95],v101 offset:9216\n"
"v_mfma_f32_16x16x32_bf16 v[200:203],a[32:35],a[16:19],v[200:203]\n"
"buffer_load_dwordx4 v112,s[24:27],s76 offen offset:0 sc1 lds\n"
"v_mfma_f32_16x16x32_bf16 v[200:203],a[36:39],a[20:23],v[200:203]\n"
"v_mfma_f32_16x16x32_bf16 v[232:235],a[36:39],a[28:31],v[232:235]\n"
"buffer_load_dwordx4 v112,s[24:27],s76 offen offset:1024 sc1 lds\n"
"v_mfma_f32_16x16x32_bf16 v[232:235],a[32:35],a[24:27],v[232:235]\n"
"v_mfma_f32_16x16x32_bf16 v[236:239],a[40:43],a[24:27],v[236:239]\n"
"ds_read_b64_tr_b16 a[32:33],v102 offset:0\n"
"v_mfma_f32_16x16x32_bf16 v[236:239],a[44:47],a[28:31],v[236:239]\n"
"ds_read_b64_tr_b16 a[34:35],v102 offset:1024\n"
"v_mfma_f32_16x16x32_bf16 v[204:207],a[44:47],a[20:23],v[204:207]\n"
"ds_read_b64_tr_b16 a[36:37],v102 offset:8192\n"
"v_mfma_f32_16x16x32_bf16 v[204:207],a[40:43],a[16:19],v[204:207]\n"
"ds_read_b64_tr_b16 a[38:39],v102 offset:9216\n"
"v_mfma_f32_16x16x32_bf16 v[172:175],a[40:43],a[8:11],v[172:175]\n"
"buffer_load_dwordx4 v112,s[24:27],s76 offen offset:2048 sc1 lds\n"
"v_mfma_f32_16x16x32_bf16 v[172:175],a[44:47],a[12:15],v[172:175]\n"
"v_mfma_f32_16x16x32_bf16 v[140:143],a[44:47],a[4:7],v[140:143]\n"
"buffer_load_dwordx4 v112,s[24:27],s76 offen offset:3072 sc1 lds\n"
"v_mfma_f32_16x16x32_bf16 v[140:143],a[40:43],a[0:3],v[140:143]\n"
"s_xor_b32 s76,s76,16384\n"
"s_waitcnt vmcnt(20)\n"
"v_mfma_f32_16x16x32_bf16 v[144:147],v[32:35],a[0:3],v[144:147]\n"
"ds_read_b64_tr_b16 a[40:41],v103 offset:0\n"
"v_mfma_f32_16x16x32_bf16 v[144:147],v[36:39],a[4:7],v[144:147]\n"
"ds_read_b64_tr_b16 a[42:43],v103 offset:1024\n"
"v_mfma_f32_16x16x32_bf16 v[176:179],v[36:39],a[12:15],v[176:179]\n"
"ds_read_b64_tr_b16 a[44:45],v103 offset:8192\n"
"v_mfma_f32_16x16x32_bf16 v[176:179],v[32:35],a[8:11],v[176:179]\n"
"s_add_u32 m0,s78,16384\n"
"ds_read_b64_tr_b16 a[46:47],v103 offset:9216\n"
"v_mfma_f32_16x16x32_bf16 v[208:211],v[32:35],a[16:19],v[208:211]\n"
"buffer_load_dwordx4 v112,s[24:27],s76 offen offset:0 sc1 lds\n"
"v_mfma_f32_16x16x32_bf16 v[208:211],v[36:39],a[20:23],v[208:211]\n"
"ds_read_b128 v[0:3],v96 offset:32768\n"
"v_mfma_f32_16x16x32_bf16 v[240:243],v[36:39],a[28:31],v[240:243]\n"
"buffer_load_dwordx4 v112,s[24:27],s76 offen offset:1024 sc1 lds\n"
"v_mfma_f32_16x16x32_bf16 v[240:243],v[32:35],a[24:27],v[240:243]\n"
"ds_read_b128 v[8:11],v96 offset:33792\n"
"v_mfma_f32_16x16x32_bf16 v[244:247],v[40:43],a[24:27],v[244:247]\n"
"ds_read_b64_tr_b16 v[32:33],v104 offset:0\n"
"v_mfma_f32_16x16x32_bf16 v[244:247],v[44:47],a[28:31],v[244:247]\n"
"ds_read_b64_tr_b16 v[34:35],v104 offset:1024\n"
"v_mfma_f32_16x16x32_bf16 v[212:215],v[44:47],a[20:23],v[212:215]\n"
"ds_read_b64_tr_b16 v[36:37],v104 offset:8192\n"
"v_mfma_f32_16x16x32_bf16 v[212:215],v[40:43],a[16:19],v[212:215]\n"
"ds_read_b64_tr_b16 v[38:39],v104 offset:9216\n"
"v_mfma_f32_16x16x32_bf16 v[180:183],v[40:43],a[8:11],v[180:183]\n"
"buffer_load_dwordx4 v112,s[24:27],s76 offen offset:2048 sc1 lds\n"
"v_mfma_f32_16x16x32_bf16 v[180:183],v[44:47],a[12:15],v[180:183]\n"
"ds_read_b128 v[16:19],v96 offset:34816\n"
"v_mfma_f32_16x16x32_bf16 v[148:151],v[44:47],a[4:7],v[148:151]\n"
"buffer_load_dwordx4 v112,s[24:27],s76 offen offset:3072 sc1 lds\n"
"v_mfma_f32_16x16x32_bf16 v[148:151],v[40:43],a[0:3],v[148:151]\n"
"s_xor_b32 s76,s76,49152\n"
"ds_read_b128 v[24:27],v96 offset:35840\n"
"v_mfma_f32_16x16x32_bf16 v[152:155],v[48:51],a[0:3],v[152:155]\n"
"ds_read_b64_tr_b16 v[40:41],v105 offset:0\n"
"v_mfma_f32_16x16x32_bf16 v[152:155],v[52:55],a[4:7],v[152:155]\n"
"ds_read_b64_tr_b16 v[42:43],v105 offset:1024\n"
"v_mfma_f32_16x16x32_bf16 v[184:187],v[52:55],a[12:15],v[184:187]\n"
"ds_read_b64_tr_b16 v[44:45],v105 offset:8192\n"
"v_mfma_f32_16x16x32_bf16 v[184:187],v[48:51],a[8:11],v[184:187]\n"
"ds_read_b64_tr_b16 v[46:47],v105 offset:9216\n"
"v_mfma_f32_16x16x32_bf16 v[216:219],v[48:51],a[16:19],v[216:219]\n"
"ds_read_b128 v[4:7],v96 offset:49152\n"
"v_mfma_f32_16x16x32_bf16 v[216:219],v[52:55],a[20:23],v[216:219]\n"
"ds_read_b128 v[12:15],v96 offset:50176\n"
"ds_read_b128 v[20:23],v96 offset:51200\n"
"v_mfma_f32_16x16x32_bf16 v[248:251],v[52:55],a[28:31],v[248:251]\n"
"buffer_store_dwordx2 v[124:125],off,s[24:27],s59 sc1\n"
"v_mfma_f32_16x16x32_bf16 v[248:251],v[48:51],a[24:27],v[248:251]\n"
"s_add_u32 m0,s78,114688\n"
"ds_read_b128 v[28:31],v96 offset:52224\n"
"v_mfma_f32_16x16x32_bf16 v[252:255],v[56:59],a[24:27],v[252:255]\n"
"buffer_load_dwordx4 v106,s[4:7],s13 offen offset:0 nt lds\n"
"v_mfma_f32_16x16x32_bf16 v[252:255],v[60:63],a[28:31],v[252:255]\n"
"s_add_u32 s13,s13,7168\n"
"v_mfma_f32_16x16x32_bf16 v[220:223],v[60:63],a[20:23],v[220:223]\n"
"buffer_load_dwordx4 v106,s[4:7],s13 offen offset:1024 nt lds\n"
"v_mfma_f32_16x16x32_bf16 v[220:223],v[56:59],a[16:19],v[220:223]\n"
"s_add_u32 s13,s13,7168\n"
"v_mfma_f32_16x16x32_bf16 v[188:191],v[56:59],a[8:11],v[188:191]\n"
"buffer_load_dwordx4 v106,s[4:7],s13 offen offset:2048 nt lds\n"
"v_mfma_f32_16x16x32_bf16 v[188:191],v[60:63],a[12:15],v[188:191]\n"
"s_add_u32 s13,s13,7168\n"
"v_mfma_f32_16x16x32_bf16 v[156:159],v[60:63],a[4:7],v[156:159]\n"
"buffer_load_dwordx4 v106,s[4:7],s13 offen offset:3072 nt lds\n"
"v_mfma_f32_16x16x32_bf16 v[156:159],v[56:59],a[0:3],v[156:159]\n"
"s_add_u32 s13,s13,109568\n"
"s_waitcnt vmcnt(25) lgkmcnt(0)\n"
"s_barrier\n"
"v_mfma_f32_16x16x32_bf16 v[128:131],v[64:67],v[0:3],v[128:131]\n"
"ds_read_b64_tr_b16 v[48:49],v98 offset:49152\n"
"ds_read_b64_tr_b16 v[56:57],v99 offset:49152\n"
"v_mfma_f32_16x16x32_bf16 v[128:131],v[68:71],v[4:7],v[128:131]\n"
"ds_read_b64_tr_b16 v[50:51],v98 offset:50176\n"
"ds_read_b64_tr_b16 v[58:59],v99 offset:50176\n"
"v_mfma_f32_16x16x32_bf16 v[160:163],v[68:71],v[12:15],v[160:163]\n"
"ds_read_b64_tr_b16 v[52:53],v98 offset:57344\n"
"ds_read_b64_tr_b16 v[60:61],v99 offset:57344\n"
"v_mfma_f32_16x16x32_bf16 v[160:163],v[64:67],v[8:11],v[160:163]\n"
"s_add_u32 m0,s85,98304\n"
"ds_read_b64_tr_b16 v[54:55],v98 offset:58368\n"
"ds_read_b64_tr_b16 v[62:63],v99 offset:58368\n"
"v_mfma_f32_16x16x32_bf16 v[192:195],v[64:67],v[16:19],v[192:195]\n"
"buffer_load_dwordx4 v110,s[28:31],s86 offen lds\n"
"v_mfma_f32_16x16x32_bf16 v[192:195],v[68:71],v[20:23],v[192:195]\n"
"v_mfma_f32_16x16x32_bf16 v[224:227],v[68:71],v[28:31],v[224:227]\n"
"v_mfma_f32_16x16x32_bf16 v[224:227],v[64:67],v[24:27],v[224:227]\n"
"v_mfma_f32_16x16x32_bf16 v[228:231],v[72:75],v[24:27],v[228:231]\n"
"ds_read_b64_tr_b16 v[64:65],v100 offset:49152\n"
"v_mfma_f32_16x16x32_bf16 v[228:231],v[76:79],v[28:31],v[228:231]\n"
"ds_read_b64_tr_b16 v[66:67],v100 offset:50176\n"
"v_mfma_f32_16x16x32_bf16 v[196:199],v[76:79],v[20:23],v[196:199]\n"
"ds_read_b64_tr_b16 v[68:69],v100 offset:57344\n"
"v_mfma_f32_16x16x32_bf16 v[196:199],v[72:75],v[16:19],v[196:199]\n"
"s_add_u32 m0,s85,99328\n"
"ds_read_b64_tr_b16 v[70:71],v100 offset:58368\n"
"v_mfma_f32_16x16x32_bf16 v[164:167],v[72:75],v[8:11],v[164:167]\n"
"buffer_load_dwordx4 v111,s[28:31],s86 offen lds\n"
"v_mfma_f32_16x16x32_bf16 v[164:167],v[76:79],v[12:15],v[164:167]\n"
"s_add_u32 m0,s85,106496\n"
"s_xor_b32 s86,s86,262144\n"
"v_mfma_f32_16x16x32_bf16 v[132:135],v[76:79],v[4:7],v[132:135]\n"
"buffer_load_dwordx4 v110,s[28:31],s86 offen lds\n"
"v_mfma_f32_16x16x32_bf16 v[132:135],v[72:75],v[0:3],v[132:135]\n"
"v_mfma_f32_16x16x32_bf16 v[136:139],v[80:83],v[0:3],v[136:139]\n"
"ds_read_b64_tr_b16 v[72:73],v101 offset:49152\n"
"v_mfma_f32_16x16x32_bf16 v[136:139],v[84:87],v[4:7],v[136:139]\n"
"ds_read_b64_tr_b16 v[74:75],v101 offset:50176\n"
"v_mfma_f32_16x16x32_bf16 v[168:171],v[84:87],v[12:15],v[168:171]\n"
"ds_read_b64_tr_b16 v[76:77],v101 offset:57344\n"
"v_mfma_f32_16x16x32_bf16 v[168:171],v[80:83],v[8:11],v[168:171]\n"
"s_add_u32 m0,s85,107520\n"
"ds_read_b64_tr_b16 v[78:79],v101 offset:58368\n"
"v_mfma_f32_16x16x32_bf16 v[200:203],v[80:83],v[16:19],v[200:203]\n"
"buffer_load_dwordx4 v111,s[28:31],s86 offen lds\n"
"v_mfma_f32_16x16x32_bf16 v[200:203],v[84:87],v[20:23],v[200:203]\n"
"s_xor_b32 s86,s86,3932160\n"
"v_mfma_f32_16x16x32_bf16 v[232:235],v[84:87],v[28:31],v[232:235]\n"
"v_mfma_f32_16x16x32_bf16 v[232:235],v[80:83],v[24:27],v[232:235]\n"
"v_mfma_f32_16x16x32_bf16 v[236:239],v[88:91],v[24:27],v[236:239]\n"
"ds_read_b64_tr_b16 v[80:81],v102 offset:49152\n"
"v_mfma_f32_16x16x32_bf16 v[236:239],v[92:95],v[28:31],v[236:239]\n"
"ds_read_b64_tr_b16 v[82:83],v102 offset:50176\n"
"v_mfma_f32_16x16x32_bf16 v[204:207],v[92:95],v[20:23],v[204:207]\n"
"ds_read_b64_tr_b16 v[84:85],v102 offset:57344\n"
"v_mfma_f32_16x16x32_bf16 v[204:207],v[88:91],v[16:19],v[204:207]\n"
"s_add_u32 m0,s78,32768\n"
"ds_read_b64_tr_b16 v[86:87],v102 offset:58368\n"
"v_mfma_f32_16x16x32_bf16 v[172:175],v[88:91],v[8:11],v[172:175]\n"
"buffer_load_dwordx4 v112,s[24:27],s76 offen offset:0 sc1 lds\n"
"v_mfma_f32_16x16x32_bf16 v[172:175],v[92:95],v[12:15],v[172:175]\n"
"v_mfma_f32_16x16x32_bf16 v[140:143],v[92:95],v[4:7],v[140:143]\n"
"buffer_load_dwordx4 v112,s[24:27],s76 offen offset:1024 sc1 lds\n"
"v_mfma_f32_16x16x32_bf16 v[140:143],v[88:91],v[0:3],v[140:143]\n"
"s_waitcnt vmcnt(23)\n"
"ds_read_b64_tr_b16 v[88:89],v103 offset:49152\n"
"ds_read_b64_tr_b16 v[90:91],v103 offset:50176\n"
"v_mfma_f32_16x16x32_bf16 v[144:147],a[32:35],v[0:3],v[144:147]\n"
"v_lshrrev_b32 v120,4,v112\n"
"v_and_b32 v121,15,v120\n"
"v_mfma_f32_16x16x32_bf16 v[144:147],a[36:39],v[4:7],v[144:147]\n"
"v_lshrrev_b32 v122,4,v120\n"
"v_lshlrev_b32 v122,2,v122\n"
"v_mfma_f32_16x16x32_bf16 v[176:179],a[36:39],v[12:15],v[176:179]\n"
"ds_read_b64_tr_b16 v[92:93],v103 offset:57344\n"
"v_sub_u32 v121,v121,v122\n"
"v_mfma_f32_16x16x32_bf16 v[176:179],a[32:35],v[8:11],v[176:179]\n"
"s_and_b32 s82,s52,1\n"
"ds_read_b64_tr_b16 v[94:95],v103 offset:58368\n"
"s_lshl_b32 s82,s82,4\n"
"v_add_u32 v121,s82,v121\n"
"v_mfma_f32_16x16x32_bf16 v[208:211],a[32:35],v[16:19],v[208:211]\n"
"buffer_load_dwordx4 v112,s[24:27],s76 offen offset:2048 sc1 lds\n"
"v_cvt_f32_i32 v121,v121\n"
"v_mfma_f32_16x16x32_bf16 v[208:211],a[36:39],v[20:23],v[208:211]\n"
"ds_read_b128 a[96:99],v97 offset:0\n"
"v_add_f32 v121,0.5,v121\n"
"v_mul_f32 v126,0x7149f2ca,v121\n"
"v_mfma_f32_16x16x32_bf16 v[240:243],a[36:39],v[28:31],v[240:243]\n"
"v_and_b32 v120,31,v120\n"
"v_mov_b32 v121,0x3f803f80\n"
"v_mfma_f32_16x16x32_bf16 v[240:243],a[32:35],v[24:27],v[240:243]\n"
"s_mov_b32 s93,0x3f800000\n"
"ds_read_b128 a[104:107],v97 offset:1024\n"
"v_accvgpr_write_b32 a144,v121\n"
"v_mfma_f32_16x16x32_bf16 v[244:247],a[40:43],v[24:27],v[244:247]\n"
"ds_read_b64_tr_b16 a[128:129],v104 offset:49152\n"
"v_accvgpr_write_b32 a146,v121\n"
"v_mfma_f32_16x16x32_bf16 v[244:247],a[44:47],v[28:31],v[244:247]\n"
"ds_read_b64_tr_b16 a[130:131],v104 offset:50176\n"
"v_accvgpr_write_b32 a147,v121\n"
"v_mfma_f32_16x16x32_bf16 v[212:215],a[44:47],v[20:23],v[212:215]\n"
"ds_read_b64_tr_b16 a[132:133],v104 offset:57344\n"
"v_lshlrev_b32 v120,3,v120\n"
"v_mfma_f32_16x16x32_bf16 v[212:215],a[40:43],v[16:19],v[212:215]\n"
"ds_read_b64_tr_b16 a[134:135],v104 offset:58368\n"
"v_accvgpr_write_b32 a145,v121\n"
"v_mfma_f32_16x16x32_bf16 v[180:183],a[40:43],v[8:11],v[180:183]\n"
"buffer_load_dwordx4 v112,s[24:27],s76 offen offset:3072 sc1 lds\n"
"v_mfma_f32_16x16x32_bf16 v[180:183],a[44:47],v[12:15],v[180:183]\n"
"s_add_u32 m0,s78,49152\n"
"s_xor_b32 s76,s76,16384\n"
"ds_read_b128 a[112:115],v97 offset:2048\n"
"v_mfma_f32_16x16x32_bf16 v[148:151],a[44:47],v[4:7],v[148:151]\n"
"buffer_load_dwordx4 v112,s[24:27],s76 offen offset:0 sc1 lds\n"
"v_mfma_f32_16x16x32_bf16 v[148:151],a[40:43],v[0:3],v[148:151]\n"
"ds_read_b128 a[120:123],v97 offset:3072\n"
"v_mfma_f32_16x16x32_bf16 v[152:155],v[32:35],v[0:3],v[152:155]\n"
"ds_read_b64_tr_b16 a[136:137],v105 offset:49152\n"
"v_mfma_f32_16x16x32_bf16 v[152:155],v[36:39],v[4:7],v[152:155]\n"
"ds_read_b64_tr_b16 a[138:139],v105 offset:50176\n"
"v_mfma_f32_16x16x32_bf16 v[184:187],v[36:39],v[12:15],v[184:187]\n"
"ds_read_b64_tr_b16 a[140:141],v105 offset:57344\n"
"v_mfma_f32_16x16x32_bf16 v[184:187],v[32:35],v[8:11],v[184:187]\n"
"ds_read_b64_tr_b16 a[142:143],v105 offset:58368\n"
"v_mfma_f32_16x16x32_bf16 v[216:219],v[32:35],v[16:19],v[216:219]\n"
"ds_read_b128 a[100:103],v97 offset:16384\n"
"v_mfma_f32_16x16x32_bf16 v[216:219],v[36:39],v[20:23],v[216:219]\n"
"ds_read_b128 a[108:111],v97 offset:17408\n"
"v_mfma_f32_16x16x32_bf16 v[248:251],v[36:39],v[28:31],v[248:251]\n"
"ds_read_b128 a[116:119],v97 offset:18432\n"
"v_mfma_f32_16x16x32_bf16 v[248:251],v[32:35],v[24:27],v[248:251]\n"
"ds_read_b128 a[124:127],v97 offset:19456\n"
"v_mfma_f32_16x16x32_bf16 v[252:255],v[40:43],v[24:27],v[252:255]\n"
"buffer_load_dwordx4 v112,s[24:27],s76 offen offset:1024 sc1 lds\n"
"v_mfma_f32_16x16x32_bf16 v[252:255],v[44:47],v[28:31],v[252:255]\n"
"v_mfma_f32_16x16x32_bf16 v[220:223],v[44:47],v[20:23],v[220:223]\n"
"v_mfma_f32_16x16x32_bf16 v[220:223],v[40:43],v[16:19],v[220:223]\n"
"buffer_load_dwordx4 v112,s[24:27],s76 offen offset:2048 sc1 lds\n"
"v_mfma_f32_16x16x32_bf16 v[188:191],v[40:43],v[8:11],v[188:191]\n"
"v_mfma_f32_16x16x32_bf16 v[188:191],v[44:47],v[12:15],v[188:191]\n"
"v_mfma_f32_16x16x32_bf16 v[156:159],v[44:47],v[4:7],v[156:159]\n"
"buffer_load_dwordx4 v112,s[24:27],s76 offen offset:3072 sc1 lds\n"
"v_mfma_f32_16x16x32_bf16 v[156:159],v[40:43],v[0:3],v[156:159]\n"
"s_xor_b32 s76,s76,245760\n"
"s_waitcnt vmcnt(12) lgkmcnt(0)\n"
"s_barrier\n"
"v_mfma_f32_16x16x32_bf16 v[128:131],v[48:51],a[96:99],v[128:131]\n"
"ds_read_b64_tr_b16 v[32:33],v98 offset:32768\n"
"ds_read_b64_tr_b16 v[40:41],v99 offset:32768\n"
"v_mfma_f32_16x16x32_bf16 v[128:131],v[52:55],a[100:103],v[128:131]\n"
"ds_read_b64_tr_b16 v[34:35],v98 offset:33792\n"
"ds_read_b64_tr_b16 v[42:43],v99 offset:33792\n"
"v_mfma_f32_16x16x32_bf16 v[160:163],v[52:55],a[108:111],v[160:163]\n"
"ds_read_b64_tr_b16 v[36:37],v98 offset:40960\n"
"ds_read_b64_tr_b16 v[44:45],v99 offset:40960\n"
"v_mfma_f32_16x16x32_bf16 v[160:163],v[48:51],a[104:107],v[160:163]\n"
"s_add_u32 m0,s85,147456\n"
"ds_read_b64_tr_b16 v[38:39],v98 offset:41984\n"
"ds_read_b64_tr_b16 v[46:47],v99 offset:41984\n"
"v_mfma_f32_16x16x32_bf16 v[192:195],v[48:51],a[112:115],v[192:195]\n"
"buffer_load_dwordx4 v110,s[28:31],s86 offen lds\n"
"ds_read_b64_tr_b16 a[160:161],v107 offset:0\n"
"v_mfma_f32_16x16x32_bf16 v[192:195],v[52:55],a[116:119],v[192:195]\n"
"ds_read_b64_tr_b16 a[162:163],v107 offset:4096\n"
"ds_read_b64_tr_b16 a[164:165],v107 offset:8\n"
"ds_read_b64_tr_b16 a[166:167],v107 offset:4104\n"
"v_mfma_f32_16x16x32_bf16 v[224:227],v[52:55],a[124:127],v[224:227]\n"
"ds_read_b64_tr_b16 a[168:169],v107 offset:8192\n"
"ds_read_b64_tr_b16 a[170:171],v107 offset:12288\n"
"v_mfma_f32_16x16x32_bf16 v[224:227],v[48:51],a[120:123],v[224:227]\n"
"ds_read_b64_tr_b16 a[172:173],v107 offset:8200\n"
"ds_read_b64_tr_b16 a[174:175],v107 offset:12296\n"
"v_mfma_f32_16x16x32_bf16 v[228:231],v[56:59],a[120:123],v[228:231]\n"
"ds_read_b64_tr_b16 v[48:49],v100 offset:32768\n"
"v_mfma_f32_16x16x32_bf16 v[228:231],v[60:63],a[124:127],v[228:231]\n"
"ds_read_b64_tr_b16 v[50:51],v100 offset:33792\n"
"v_mfma_f32_16x16x32_bf16 v[196:199],v[60:63],a[116:119],v[196:199]\n"
"ds_read_b64_tr_b16 v[52:53],v100 offset:40960\n"
"v_mfma_f32_16x16x32_bf16 v[196:199],v[56:59],a[112:115],v[196:199]\n"
"s_add_u32 m0,s85,148480\n"
"ds_read_b64_tr_b16 v[54:55],v100 offset:41984\n"
"v_mfma_f32_16x16x32_bf16 v[164:167],v[56:59],a[104:107],v[164:167]\n"
"buffer_load_dwordx4 v111,s[28:31],s86 offen lds\n"
"v_mfma_f32_16x16x32_bf16 v[164:167],v[60:63],a[108:111],v[164:167]\n"
"s_add_u32 m0,s85,155648\n"
"s_xor_b32 s86,s86,262144\n"
"v_mfma_f32_16x16x32_bf16 v[132:135],v[60:63],a[100:103],v[132:135]\n"
"buffer_load_dwordx4 v110,s[28:31],s86 offen lds\n"
"v_mfma_f32_16x16x32_bf16 v[132:135],v[56:59],a[96:99],v[132:135]\n"
"v_mfma_f32_16x16x32_bf16 v[136:139],v[64:67],a[96:99],v[136:139]\n"
"ds_read_b64_tr_b16 v[56:57],v101 offset:32768\n"
"v_mfma_f32_16x16x32_bf16 v[136:139],v[68:71],a[100:103],v[136:139]\n"
"ds_read_b64_tr_b16 v[58:59],v101 offset:33792\n"
"v_mfma_f32_16x16x32_bf16 v[168:171],v[68:71],a[108:111],v[168:171]\n"
"ds_read_b64_tr_b16 v[60:61],v101 offset:40960\n"
"v_mfma_f32_16x16x32_bf16 v[168:171],v[64:67],a[104:107],v[168:171]\n"
"s_add_u32 m0,s85,156672\n"
"ds_read_b64_tr_b16 v[62:63],v101 offset:41984\n"
"v_mfma_f32_16x16x32_bf16 v[200:203],v[64:67],a[112:115],v[200:203]\n"
"buffer_load_dwordx4 v111,s[28:31],s86 offen lds\n"
"v_mfma_f32_16x16x32_bf16 v[200:203],v[68:71],a[116:119],v[200:203]\n"
"s_xor_b32 s86,s86,786432\n"
"v_mfma_f32_16x16x32_bf16 v[232:235],v[68:71],a[124:127],v[232:235]\n"
"v_mfma_f32_16x16x32_bf16 v[232:235],v[64:67],a[120:123],v[232:235]\n"
"v_mfma_f32_16x16x32_bf16 v[236:239],v[72:75],a[120:123],v[236:239]\n"
"ds_read_b64_tr_b16 v[64:65],v102 offset:32768\n"
"v_mfma_f32_16x16x32_bf16 v[236:239],v[76:79],a[124:127],v[236:239]\n"
"ds_read_b64_tr_b16 v[66:67],v102 offset:33792\n"
"v_mfma_f32_16x16x32_bf16 v[204:207],v[76:79],a[116:119],v[204:207]\n"
"ds_read_b64_tr_b16 v[68:69],v102 offset:40960\n"
"v_mfma_f32_16x16x32_bf16 v[204:207],v[72:75],a[112:115],v[204:207]\n"
"s_add_u32 m0,s78,65536\n"
"ds_read_b64_tr_b16 v[70:71],v102 offset:41984\n"
"v_mfma_f32_16x16x32_bf16 v[172:175],v[72:75],a[104:107],v[172:175]\n"
"buffer_load_dwordx4 v112,s[24:27],s76 offen offset:0 sc1 lds\n"
"v_mfma_f32_16x16x32_bf16 v[172:175],v[76:79],a[108:111],v[172:175]\n"
"v_mfma_f32_16x16x32_bf16 v[140:143],v[76:79],a[100:103],v[140:143]\n"
"buffer_load_dwordx4 v112,s[24:27],s76 offen offset:1024 sc1 lds\n"
"v_mfma_f32_16x16x32_bf16 v[140:143],v[72:75],a[96:99],v[140:143]\n"
"v_mfma_f32_16x16x32_bf16 v[144:147],v[80:83],a[96:99],v[144:147]\n"
"ds_read_b64_tr_b16 v[72:73],v103 offset:32768\n"
"v_accvgpr_write_b32 a156,0\n"
"v_mfma_f32_16x16x32_bf16 v[144:147],v[84:87],a[100:103],v[144:147]\n"
"ds_read_b64_tr_b16 v[74:75],v103 offset:33792\n"
"v_accvgpr_write_b32 a157,0\n"
"v_mfma_f32_16x16x32_bf16 v[176:179],v[84:87],a[108:111],v[176:179]\n"
"ds_read_b64_tr_b16 v[76:77],v103 offset:40960\n"
"v_accvgpr_write_b32 a158,0\n"
"v_mfma_f32_16x16x32_bf16 v[176:179],v[80:83],a[104:107],v[176:179]\n"
"ds_read_b64_tr_b16 v[78:79],v103 offset:41984\n"
"v_accvgpr_write_b32 a159,0\n"
"v_mfma_f32_16x16x32_bf16 v[208:211],v[80:83],a[112:115],v[208:211]\n"
"buffer_load_dwordx4 v112,s[24:27],s76 offen offset:2048 sc1 lds\n"
"v_mfma_f32_16x16x32_bf16 v[208:211],v[84:87],a[116:119],v[208:211]\n"
"ds_read_b128 v[0:3],v96 offset:0\n"
"v_mfma_f32_16x16x32_bf16 v[240:243],v[84:87],a[124:127],v[240:243]\n"
"v_mfma_f32_16x16x32_bf16 v[240:243],v[80:83],a[120:123],v[240:243]\n"
"ds_read_b128 v[8:11],v96 offset:1024\n"
"v_mfma_f32_16x16x32_bf16 v[244:247],v[88:91],a[120:123],v[244:247]\n"
"ds_read_b64_tr_b16 v[80:81],v104 offset:32768\n"
"v_mfma_f32_16x16x32_bf16 v[244:247],v[92:95],a[124:127],v[244:247]\n"
"ds_read_b64_tr_b16 v[82:83],v104 offset:33792\n"
"v_mfma_f32_16x16x32_bf16 v[212:215],v[92:95],a[116:119],v[212:215]\n"
"ds_read_b64_tr_b16 v[84:85],v104 offset:40960\n"
"v_mfma_f32_16x16x32_bf16 v[212:215],v[88:91],a[112:115],v[212:215]\n"
"ds_read_b64_tr_b16 v[86:87],v104 offset:41984\n"
"v_mfma_f32_16x16x32_bf16 v[180:183],v[88:91],a[104:107],v[180:183]\n"
"buffer_load_dwordx4 v112,s[24:27],s76 offen offset:3072 sc1 lds\n"
"v_mfma_f32_16x16x32_bf16 v[180:183],v[92:95],a[108:111],v[180:183]\n"
"s_add_u32 m0,s78,81920\n"
"s_xor_b32 s76,s76,16384\n"
"ds_read_b128 v[16:19],v96 offset:2048\n"
"v_mfma_f32_16x16x32_bf16 v[148:151],v[92:95],a[100:103],v[148:151]\n"
"buffer_load_dwordx4 v112,s[24:27],s76 offen offset:0 sc1 lds\n"
"v_mfma_f32_16x16x32_bf16 v[148:151],v[88:91],a[96:99],v[148:151]\n"
"ds_read_b128 v[24:27],v96 offset:3072\n"
"v_mfma_f32_16x16x32_bf16 v[152:155],a[128:131],a[96:99],v[152:155]\n"
"ds_read_b64_tr_b16 v[88:89],v105 offset:32768\n"
"v_mfma_f32_16x16x32_bf16 v[152:155],a[132:135],a[100:103],v[152:155]\n"
"ds_read_b64_tr_b16 v[90:91],v105 offset:33792\n"
"v_mfma_f32_16x16x32_bf16 v[184:187],a[132:135],a[108:111],v[184:187]\n"
"ds_read_b64_tr_b16 v[92:93],v105 offset:40960\n"
"v_mfma_f32_16x16x32_bf16 v[184:187],a[128:131],a[104:107],v[184:187]\n"
"ds_read_b64_tr_b16 v[94:95],v105 offset:41984\n"
"v_mfma_f32_16x16x32_bf16 v[216:219],a[128:131],a[112:115],v[216:219]\n"
"ds_read_b128 v[4:7],v96 offset:16384\n"
"v_mfma_f32_16x16x32_bf16 v[216:219],a[132:135],a[116:119],v[216:219]\n"
"ds_read_b128 v[12:15],v96 offset:17408\n"
"v_mfma_f32_16x16x32_bf16 v[248:251],a[132:135],a[124:127],v[248:251]\n"
"ds_read_b128 v[20:23],v96 offset:18432\n"
"v_mfma_f32_16x16x32_bf16 v[248:251],a[128:131],a[120:123],v[248:251]\n"
"ds_read_b128 v[28:31],v96 offset:19456\n"
"v_mfma_f32_16x16x32_bf16 v[252:255],a[136:139],a[120:123],v[252:255]\n"
"buffer_load_dwordx4 v112,s[24:27],s76 offen offset:1024 sc1 lds\n"
"v_mfma_f32_16x16x32_bf16 v[252:255],a[140:143],a[124:127],v[252:255]\n"
"v_mfma_f32_16x16x32_bf16 v[220:223],a[140:143],a[116:119],v[220:223]\n"
"v_mfma_f32_16x16x32_bf16 v[220:223],a[136:139],a[112:115],v[220:223]\n"
"buffer_load_dwordx4 v112,s[24:27],s76 offen offset:2048 sc1 lds\n"
"v_mfma_f32_16x16x32_bf16 v[188:191],a[136:139],a[104:107],v[188:191]\n"
"v_mfma_f32_16x16x32_bf16 v[188:191],a[140:143],a[108:111],v[188:191]\n"
"v_mfma_f32_16x16x32_bf16 v[156:159],a[140:143],a[100:103],v[156:159]\n"
"buffer_load_dwordx4 v112,s[24:27],s76 offen offset:3072 sc1 lds\n"
"v_mfma_f32_16x16x32_bf16 v[156:159],a[136:139],a[96:99],v[156:159]\n"
"s_xor_b32 s76,s76,49152\n"
"s_waitcnt vmcnt(20) lgkmcnt(0)\n"
"s_barrier\n"
"v_mfma_f32_16x16x32_bf16 v[128:131],v[32:35],v[0:3],v[128:131]\n"
"ds_read_b64_tr_b16 a[128:129],v98 offset:0\n"
"ds_read_b64_tr_b16 a[136:137],v99 offset:0\n"
"v_mfma_f32_16x16x32_bf16 v[128:131],v[36:39],v[4:7],v[128:131]\n"
"ds_read_b64_tr_b16 a[130:131],v98 offset:1024\n"
"ds_read_b64_tr_b16 a[138:139],v99 offset:1024\n"
"v_mfma_f32_16x16x32_bf16 v[160:163],v[36:39],v[12:15],v[160:163]\n"
"ds_read_b64_tr_b16 a[132:133],v98 offset:8192\n"
"ds_read_b64_tr_b16 a[140:141],v99 offset:8192\n"
"v_mfma_f32_16x16x32_bf16 v[160:163],v[32:35],v[8:11],v[160:163]\n"
"s_add_u32 m0,s85,131072\n"
"ds_read_b64_tr_b16 a[134:135],v98 offset:9216\n"
"ds_read_b64_tr_b16 a[142:143],v99 offset:9216\n"
"v_mfma_f32_16x16x32_bf16 v[192:195],v[32:35],v[16:19],v[192:195]\n"
"buffer_load_dwordx4 v110,s[28:31],s86 offen lds\n"
"v_mfma_f32_16x16x32_bf16 v[192:195],v[36:39],v[20:23],v[192:195]\n"
"s_add_u32 m0,s85,132096\n"
"v_mfma_f32_16x16x32_bf16 v[224:227],v[36:39],v[28:31],v[224:227]\n"
"buffer_load_dwordx4 v111,s[28:31],s86 offen lds\n"
"v_mfma_f32_16x16x32_bf16 v[224:227],v[32:35],v[24:27],v[224:227]\n"
"s_xor_b32 s86,s86,262144\n"
"v_mfma_f32_16x16x32_bf16 v[228:231],v[40:43],v[24:27],v[228:231]\n"
"ds_read_b64_tr_b16 v[32:33],v100 offset:0\n"
"v_mfma_f32_16x16x32_bf16 v[228:231],v[44:47],v[28:31],v[228:231]\n"
"ds_read_b64_tr_b16 v[34:35],v100 offset:1024\n"
"v_mfma_f32_16x16x32_bf16 v[196:199],v[44:47],v[20:23],v[196:199]\n"
"ds_read_b64_tr_b16 v[36:37],v100 offset:8192\n"
"v_mfma_f32_16x16x32_bf16 v[196:199],v[40:43],v[16:19],v[196:199]\n"
"s_add_u32 m0,s85,139264\n"
"ds_read_b64_tr_b16 v[38:39],v100 offset:9216\n"
"v_mfma_f32_16x16x32_bf16 v[164:167],v[40:43],v[8:11],v[164:167]\n"
"buffer_load_dwordx4 v110,s[28:31],s86 offen lds\n"
"v_mfma_f32_16x16x32_bf16 v[164:167],v[44:47],v[12:15],v[164:167]\n"
"s_add_u32 m0,s85,140288\n"
"v_mfma_f32_16x16x32_bf16 v[132:135],v[44:47],v[4:7],v[132:135]\n"
"buffer_load_dwordx4 v111,s[28:31],s86 offen lds\n"
"v_mfma_f32_16x16x32_bf16 v[132:135],v[40:43],v[0:3],v[132:135]\n"
"s_xor_b32 s86,s86,1835008\n"
"v_mfma_f32_16x16x32_bf16 v[136:139],v[48:51],v[0:3],v[136:139]\n"
"ds_read_b64_tr_b16 v[40:41],v101 offset:0\n"
"v_mfma_f32_16x16x32_bf16 v[136:139],v[52:55],v[4:7],v[136:139]\n"
"ds_read_b64_tr_b16 v[42:43],v101 offset:1024\n"
"v_mfma_f32_16x16x32_bf16 v[168:171],v[52:55],v[12:15],v[168:171]\n"
"ds_read_b64_tr_b16 v[44:45],v101 offset:8192\n"
"v_mfma_f32_16x16x32_bf16 v[168:171],v[48:51],v[8:11],v[168:171]\n"
"s_add_u32 m0,s78,0\n"
"ds_read_b64_tr_b16 v[46:47],v101 offset:9216\n"
"v_mfma_f32_16x16x32_bf16 v[200:203],v[48:51],v[16:19],v[200:203]\n"
"buffer_load_dwordx4 v112,s[24:27],s76 offen offset:0 sc1 lds\n"
"v_mfma_f32_16x16x32_bf16 v[200:203],v[52:55],v[20:23],v[200:203]\n"
"v_mfma_f32_16x16x32_bf16 v[232:235],v[52:55],v[28:31],v[232:235]\n"
"buffer_load_dwordx4 v112,s[24:27],s76 offen offset:1024 sc1 lds\n"
"v_mfma_f32_16x16x32_bf16 v[232:235],v[48:51],v[24:27],v[232:235]\n"
"v_mfma_f32_16x16x32_bf16 v[236:239],v[56:59],v[24:27],v[236:239]\n"
"ds_read_b64_tr_b16 v[48:49],v102 offset:0\n"
"v_mfma_f32_16x16x32_bf16 v[236:239],v[60:63],v[28:31],v[236:239]\n"
"ds_read_b64_tr_b16 v[50:51],v102 offset:1024\n"
"v_mfma_f32_16x16x32_bf16 v[204:207],v[60:63],v[20:23],v[204:207]\n"
"ds_read_b64_tr_b16 v[52:53],v102 offset:8192\n"
"v_mfma_f32_16x16x32_bf16 v[204:207],v[56:59],v[16:19],v[204:207]\n"
"ds_read_b64_tr_b16 v[54:55],v102 offset:9216\n"
"v_mfma_f32_16x16x32_bf16 v[172:175],v[56:59],v[8:11],v[172:175]\n"
"buffer_load_dwordx4 v112,s[24:27],s76 offen offset:2048 sc1 lds\n"
"v_mfma_f32_16x16x32_bf16 v[172:175],v[60:63],v[12:15],v[172:175]\n"
"v_mfma_f32_16x16x32_bf16 v[140:143],v[60:63],v[4:7],v[140:143]\n"
"buffer_load_dwordx4 v112,s[24:27],s76 offen offset:3072 sc1 lds\n"
"v_mfma_f32_16x16x32_bf16 v[140:143],v[56:59],v[0:3],v[140:143]\n"
"s_xor_b32 s76,s76,16384\n"
"s_waitcnt vmcnt(20)\n"
"v_mfma_f32_16x16x32_bf16 v[144:147],v[64:67],v[0:3],v[144:147]\n"
"ds_read_b64_tr_b16 v[56:57],v103 offset:0\n"
"v_mfma_f32_16x16x32_bf16 v[144:147],v[68:71],v[4:7],v[144:147]\n"
"ds_read_b64_tr_b16 v[58:59],v103 offset:1024\n"
"v_mfma_f32_16x16x32_bf16 v[176:179],v[68:71],v[12:15],v[176:179]\n"
"ds_read_b64_tr_b16 v[60:61],v103 offset:8192\n"
"v_mfma_f32_16x16x32_bf16 v[176:179],v[64:67],v[8:11],v[176:179]\n"
"s_add_u32 m0,s78,16384\n"
"ds_read_b64_tr_b16 v[62:63],v103 offset:9216\n"
"v_mfma_f32_16x16x32_bf16 v[208:211],v[64:67],v[16:19],v[208:211]\n"
"buffer_load_dwordx4 v112,s[24:27],s76 offen offset:0 sc1 lds\n"
"v_mfma_f32_16x16x32_bf16 v[208:211],v[68:71],v[20:23],v[208:211]\n"
"ds_read_b128 a[96:99],v96 offset:32768\n"
"v_mfma_f32_16x16x32_bf16 v[240:243],v[68:71],v[28:31],v[240:243]\n"
"buffer_load_dwordx4 v112,s[24:27],s76 offen offset:1024 sc1 lds\n"
"v_mfma_f32_16x16x32_bf16 v[240:243],v[64:67],v[24:27],v[240:243]\n"
"ds_read_b128 a[104:107],v96 offset:33792\n"
"v_mfma_f32_16x16x32_bf16 v[244:247],v[72:75],v[24:27],v[244:247]\n"
"ds_read_b64_tr_b16 v[64:65],v104 offset:0\n"
"v_mfma_f32_16x16x32_bf16 v[244:247],v[76:79],v[28:31],v[244:247]\n"
"ds_read_b64_tr_b16 v[66:67],v104 offset:1024\n"
"v_mfma_f32_16x16x32_bf16 v[212:215],v[76:79],v[20:23],v[212:215]\n"
"ds_read_b64_tr_b16 v[68:69],v104 offset:8192\n"
"v_mfma_f32_16x16x32_bf16 v[212:215],v[72:75],v[16:19],v[212:215]\n"
"ds_read_b64_tr_b16 v[70:71],v104 offset:9216\n"
"v_mfma_f32_16x16x32_bf16 v[180:183],v[72:75],v[8:11],v[180:183]\n"
"buffer_load_dwordx4 v112,s[24:27],s76 offen offset:2048 sc1 lds\n"
"v_mfma_f32_16x16x32_bf16 v[180:183],v[76:79],v[12:15],v[180:183]\n"
"ds_read_b128 a[112:115],v96 offset:34816\n"
"v_mfma_f32_16x16x32_bf16 v[148:151],v[76:79],v[4:7],v[148:151]\n"
"buffer_load_dwordx4 v112,s[24:27],s76 offen offset:3072 sc1 lds\n"
"v_mfma_f32_16x16x32_bf16 v[148:151],v[72:75],v[0:3],v[148:151]\n"
"s_xor_b32 s76,s76,114688\n"
"ds_read_b128 a[120:123],v96 offset:35840\n"
"v_mfma_f32_16x16x32_bf16 v[152:155],v[80:83],v[0:3],v[152:155]\n"
"ds_read_b64_tr_b16 v[72:73],v105 offset:0\n"
"v_mfma_f32_16x16x32_bf16 v[152:155],v[84:87],v[4:7],v[152:155]\n"
"ds_read_b64_tr_b16 v[74:75],v105 offset:1024\n"
"v_mfma_f32_16x16x32_bf16 v[184:187],v[84:87],v[12:15],v[184:187]\n"
"ds_read_b64_tr_b16 v[76:77],v105 offset:8192\n"
"v_mfma_f32_16x16x32_bf16 v[184:187],v[80:83],v[8:11],v[184:187]\n"
"ds_read_b64_tr_b16 v[78:79],v105 offset:9216\n"
"v_mfma_f32_16x16x32_bf16 v[216:219],v[80:83],v[16:19],v[216:219]\n"
"ds_read_b128 a[100:103],v96 offset:49152\n"
"v_mfma_f32_16x16x32_bf16 v[216:219],v[84:87],v[20:23],v[216:219]\n"
"ds_read_b128 a[108:111],v96 offset:50176\n"
"ds_read_b128 a[116:119],v96 offset:51200\n"
"v_mfma_f32_16x16x32_bf16 v[248:251],v[84:87],v[28:31],v[248:251]\n"
"s_load_dwordx2 s[32:33],s[0:1],56\n"
"s_load_dwordx2 s[36:37],s[0:1],64\n"
"s_load_dwordx2 s[40:41],s[0:1],72\n"
"v_mfma_f32_16x16x32_bf16 v[248:251],v[80:83],v[24:27],v[248:251]\n"
"s_add_u32 m0,s78,114688\n"
"ds_read_b128 a[124:127],v96 offset:52224\n"
"v_mfma_f32_16x16x32_bf16 v[252:255],v[88:91],v[24:27],v[252:255]\n"
"buffer_load_dwordx4 v106,s[4:7],s13 offen offset:0 nt lds\n"
"v_mfma_f32_16x16x32_bf16 v[252:255],v[92:95],v[28:31],v[252:255]\n"
"s_add_u32 s13,s13,7168\n"
"v_mfma_f32_16x16x32_bf16 v[220:223],v[92:95],v[20:23],v[220:223]\n"
"buffer_load_dwordx4 v106,s[4:7],s13 offen offset:1024 nt lds\n"
"v_mfma_f32_16x16x32_bf16 v[220:223],v[88:91],v[16:19],v[220:223]\n"
"s_add_u32 s13,s13,7168\n"
"v_mfma_f32_16x16x32_bf16 v[188:191],v[88:91],v[8:11],v[188:191]\n"
"buffer_load_dwordx4 v106,s[4:7],s13 offen offset:2048 nt lds\n"
"v_mfma_f32_16x16x32_bf16 v[188:191],v[92:95],v[12:15],v[188:191]\n"
"s_add_u32 s13,s13,7168\n"
"v_mfma_f32_16x16x32_bf16 v[156:159],v[92:95],v[4:7],v[156:159]\n"
"buffer_load_dwordx4 v106,s[4:7],s13 offen offset:3072 nt lds\n"
"v_mfma_f32_16x16x32_bf16 v[156:159],v[88:91],v[0:3],v[156:159]\n"
"s_add_u32 s13,s13,109568\n"
"s_waitcnt vmcnt(24) lgkmcnt(0)\n"
"s_barrier\n"
"v_mfma_f32_16x16x32_bf16 v[128:131],a[128:131],a[96:99],v[128:131]\n"
"ds_read_b64_tr_b16 v[80:81],v98 offset:49152\n"
"ds_read_b64_tr_b16 v[88:89],v99 offset:49152\n"
"v_mfma_f32_16x16x32_bf16 v[128:131],a[132:135],a[100:103],v[128:131]\n"
"ds_read_b64_tr_b16 v[82:83],v98 offset:50176\n"
"ds_read_b64_tr_b16 v[90:91],v99 offset:50176\n"
"v_mfma_f32_16x16x32_bf16 v[160:163],a[132:135],a[108:111],v[160:163]\n"
"ds_read_b64_tr_b16 v[84:85],v98 offset:57344\n"
"ds_read_b64_tr_b16 v[92:93],v99 offset:57344\n"
"v_mfma_f32_16x16x32_bf16 v[160:163],a[128:131],a[104:107],v[160:163]\n"
"s_add_u32 m0,s85,98304\n"
"ds_read_b64_tr_b16 v[86:87],v98 offset:58368\n"
"ds_read_b64_tr_b16 v[94:95],v99 offset:58368\n"
"v_mfma_f32_16x16x32_bf16 v[192:195],a[128:131],a[112:115],v[192:195]\n"
"buffer_load_dwordx4 v110,s[28:31],s86 offen lds\n"
"v_mfma_f32_16x16x32_bf16 v[192:195],a[132:135],a[116:119],v[192:195]\n"
"v_accvgpr_write_b32 a154,v124\n"
"v_accvgpr_write_b32 a155,v125\n"
"v_mfma_f32_16x16x32_bf16 v[224:227],a[132:135],a[124:127],v[224:227]\n"
"v_accvgpr_write_b32 a152,1\n"
"v_lshrrev_b32 v124,2,v112\n"
"v_mfma_f32_16x16x32_bf16 v[224:227],a[128:131],a[120:123],v[224:227]\n"
"v_add_u32 v124,147456,v124\n"
"v_mov_b32 v125,0\n"
"v_mfma_f32_16x16x32_bf16 v[228:231],a[136:139],a[120:123],v[228:231]\n"
"ds_read_b64_tr_b16 a[128:129],v100 offset:49152\n"
"s_mov_b32 s101,0\n"
"v_mfma_f32_16x16x32_bf16 v[228:231],a[140:143],a[124:127],v[228:231]\n"
"ds_read_b64_tr_b16 a[130:131],v100 offset:50176\n"
"v_mfma_f32_16x16x32_bf16 v[196:199],a[140:143],a[116:119],v[196:199]\n"
"ds_read_b64_tr_b16 a[132:133],v100 offset:57344\n"
"v_mfma_f32_16x16x32_bf16 v[196:199],a[136:139],a[112:115],v[196:199]\n"
"s_add_u32 m0,s85,99328\n"
"ds_read_b64_tr_b16 a[134:135],v100 offset:58368\n"
"v_mfma_f32_16x16x32_bf16 v[164:167],a[136:139],a[104:107],v[164:167]\n"
"buffer_load_dwordx4 v111,s[28:31],s86 offen lds\n"
"v_mfma_f32_16x16x32_bf16 v[164:167],a[140:143],a[108:111],v[164:167]\n"
"s_add_u32 m0,s85,106496\n"
"s_xor_b32 s86,s86,262144\n"
"v_mfma_f32_16x16x32_bf16 v[132:135],a[140:143],a[100:103],v[132:135]\n"
"buffer_load_dwordx4 v110,s[28:31],s86 offen lds\n"
"v_mfma_f32_16x16x32_bf16 v[132:135],a[136:139],a[96:99],v[132:135]\n"
"v_mfma_f32_16x16x32_bf16 v[136:139],v[32:35],a[96:99],v[136:139]\n"
"ds_read_b64_tr_b16 a[136:137],v101 offset:49152\n"
"v_mfma_f32_16x16x32_bf16 v[136:139],v[36:39],a[100:103],v[136:139]\n"
"ds_read_b64_tr_b16 a[138:139],v101 offset:50176\n"
"v_mfma_f32_16x16x32_bf16 v[168:171],v[36:39],a[108:111],v[168:171]\n"
"ds_read_b64_tr_b16 a[140:141],v101 offset:57344\n"
"v_mfma_f32_16x16x32_bf16 v[168:171],v[32:35],a[104:107],v[168:171]\n"
"s_add_u32 m0,s85,107520\n"
"ds_read_b64_tr_b16 a[142:143],v101 offset:58368\n"
"v_mfma_f32_16x16x32_bf16 v[200:203],v[32:35],a[112:115],v[200:203]\n"
"buffer_load_dwordx4 v111,s[28:31],s86 offen lds\n"
"v_mfma_f32_16x16x32_bf16 v[200:203],v[36:39],a[116:119],v[200:203]\n"
"s_xor_b32 s86,s86,786432\n"
"v_mfma_f32_16x16x32_bf16 v[232:235],v[36:39],a[124:127],v[232:235]\n"
"v_mfma_f32_16x16x32_bf16 v[232:235],v[32:35],a[120:123],v[232:235]\n"
"v_mfma_f32_16x16x32_bf16 v[236:239],v[40:43],a[120:123],v[236:239]\n"
"ds_read_b64_tr_b16 v[32:33],v102 offset:49152\n"
"v_mfma_f32_16x16x32_bf16 v[236:239],v[44:47],a[124:127],v[236:239]\n"
"ds_read_b64_tr_b16 v[34:35],v102 offset:50176\n"
"v_mfma_f32_16x16x32_bf16 v[204:207],v[44:47],a[116:119],v[204:207]\n"
"ds_read_b64_tr_b16 v[36:37],v102 offset:57344\n"
"v_mfma_f32_16x16x32_bf16 v[204:207],v[40:43],a[112:115],v[204:207]\n"
"s_add_u32 m0,s78,32768\n"
"ds_read_b64_tr_b16 v[38:39],v102 offset:58368\n"
"buffer_load_dwordx4 v112,s[24:27],s76 offen offset:0 sc1 lds\n"
"v_mfma_f32_16x16x32_bf16 v[172:175],v[40:43],a[104:107],v[172:175]\n"
"buffer_load_dwordx2 v[118:119],v120,s[24:27],s63 offen sc1\n"
"v_mfma_f32_16x16x32_bf16 v[172:175],v[44:47],a[108:111],v[172:175]\n"
"v_mfma_f32_16x16x32_bf16 v[140:143],v[44:47],a[100:103],v[140:143]\n"
"buffer_load_dwordx4 v112,s[24:27],s76 offen offset:1024 sc1 lds\n"
"v_mfma_f32_16x16x32_bf16 v[140:143],v[40:43],a[96:99],v[140:143]\n"
"s_waitcnt vmcnt(23)\n"
"v_mfma_f32_16x16x32_bf16 v[144:147],v[48:51],a[96:99],v[144:147]\n"
"ds_read_b64_tr_b16 v[40:41],v103 offset:49152\n"
"s_and_b32 s82,s100,7\n"
"v_mfma_f32_16x16x32_bf16 v[144:147],v[52:55],a[100:103],v[144:147]\n"
"ds_read_b64_tr_b16 v[42:43],v103 offset:50176\n"
"s_mul_i32 s82,s82,28\n"
"v_mfma_f32_16x16x32_bf16 v[176:179],v[52:55],a[108:111],v[176:179]\n"
"ds_read_b64_tr_b16 v[44:45],v103 offset:57344\n"
"s_lshr_b32 s83,s100,3\n"
"v_mfma_f32_16x16x32_bf16 v[176:179],v[48:51],a[104:107],v[176:179]\n"
"ds_read_b64_tr_b16 v[46:47],v103 offset:58368\n"
"s_add_u32 s23,s82,s83\n"
"v_mfma_f32_16x16x32_bf16 v[208:211],v[48:51],a[112:115],v[208:211]\n"
"buffer_load_dwordx4 v112,s[24:27],s76 offen offset:2048 sc1 lds\n"
"v_mfma_f32_16x16x32_bf16 v[208:211],v[52:55],a[116:119],v[208:211]\n"
"s_cmp_lt_u32 s83,28\n"
"s_cselect_b32 s34,1827872,0\n"
"s_cselect_b32 s42,8192,0\n"
"s_cselect_b32 s23,s23,0\n"
"v_mfma_f32_16x16x32_bf16 v[240:243],v[52:55],a[124:127],v[240:243]\n"
"s_mov_b32 s35,0x20000\n"
"ds_read_b128 v[0:3],v97 offset:0\n"
"s_mov_b32 s43,0x20000\n"
"v_mfma_f32_16x16x32_bf16 v[240:243],v[48:51],a[120:123],v[240:243]\n"
"ds_read_b128 v[8:11],v97 offset:1024\n"
"s_lshl2_add_u32 s82,s83,s52\n"
"v_mfma_f32_16x16x32_bf16 v[244:247],v[56:59],a[120:123],v[244:247]\n"
"ds_read_b64_tr_b16 v[48:49],v104 offset:49152\n"
"s_mul_i32 s82,s82,57344\n"
"v_mfma_f32_16x16x32_bf16 v[244:247],v[60:63],a[124:127],v[244:247]\n"
"ds_read_b64_tr_b16 v[50:51],v104 offset:50176\n"
"s_and_b32 s83,s100,7\n"
"v_mfma_f32_16x16x32_bf16 v[212:215],v[60:63],a[116:119],v[212:215]\n"
"ds_read_b64_tr_b16 v[52:53],v104 offset:57344\n"
"s_mul_i32 s83,s83,896\n"
"v_mfma_f32_16x16x32_bf16 v[212:215],v[56:59],a[112:115],v[212:215]\n"
"ds_read_b64_tr_b16 v[54:55],v104 offset:58368\n"
"s_add_u32 s82,s82,s83\n"
"v_mfma_f32_16x16x32_bf16 v[180:183],v[56:59],a[104:107],v[180:183]\n"
"buffer_load_dwordx4 v112,s[24:27],s76 offen offset:3072 sc1 lds\n"
"v_mfma_f32_16x16x32_bf16 v[180:183],v[60:63],a[108:111],v[180:183]\n"
"s_add_u32 m0,s78,49152\n"
"s_xor_b32 s76,s76,16384\n"
"ds_read_b128 v[16:19],v97 offset:2048\n"
"v_mfma_f32_16x16x32_bf16 v[148:151],v[60:63],a[100:103],v[148:151]\n"
"buffer_load_dwordx4 v112,s[24:27],s76 offen offset:0 sc1 lds\n"
"v_mfma_f32_16x16x32_bf16 v[148:151],v[56:59],a[96:99],v[148:151]\n"
"s_add_u32 s44,s32,s82\n"
"ds_read_b128 v[24:27],v97 offset:3072\n"
"s_addc_u32 s45,s33,0\n"
"v_mfma_f32_16x16x32_bf16 v[152:155],v[64:67],a[96:99],v[152:155]\n"
"s_add_u32 s48,s36,s82\n"
"ds_read_b64_tr_b16 v[56:57],v105 offset:49152\n"
"s_addc_u32 s49,s37,0\n"
"v_mfma_f32_16x16x32_bf16 v[152:155],v[68:71],a[100:103],v[152:155]\n"
"ds_read_b64_tr_b16 v[58:59],v105 offset:50176\n"
"s_mov_b32 s46,51072\n"
"v_mfma_f32_16x16x32_bf16 v[184:187],v[68:71],a[108:111],v[184:187]\n"
"ds_read_b64_tr_b16 v[60:61],v105 offset:57344\n"
"s_mov_b32 s50,s46\n"
"v_mfma_f32_16x16x32_bf16 v[184:187],v[64:67],a[104:107],v[184:187]\n"
"ds_read_b64_tr_b16 v[62:63],v105 offset:58368\n"
"s_mov_b32 s47,0x20000\n"
"v_mfma_f32_16x16x32_bf16 v[216:219],v[64:67],a[112:115],v[216:219]\n"
"ds_read_b128 v[4:7],v97 offset:16384\n"
"s_mov_b32 s51,0x20000\n"
"v_mfma_f32_16x16x32_bf16 v[216:219],v[68:71],a[116:119],v[216:219]\n"
"ds_read_b128 v[12:15],v97 offset:17408\n"
"s_mul_i32 s82,s52,1835008\n"
"v_mfma_f32_16x16x32_bf16 v[248:251],v[68:71],a[124:127],v[248:251]\n"
"ds_read_b128 v[20:23],v97 offset:18432\n"
"s_lshl_b32 s83,s23,5\n"
"v_mfma_f32_16x16x32_bf16 v[248:251],v[64:67],a[120:123],v[248:251]\n"
"ds_read_b128 v[28:31],v97 offset:19456\n"
"s_add_u32 s82,s82,s83\n"
"v_mfma_f32_16x16x32_bf16 v[252:255],v[72:75],a[120:123],v[252:255]\n"
"buffer_load_dwordx4 v112,s[24:27],s76 offen offset:1024 sc1 lds\n"
"v_mfma_f32_16x16x32_bf16 v[252:255],v[76:79],a[124:127],v[252:255]\n"
"s_add_u32 s32,s32,s82\n"
"s_addc_u32 s33,s33,0\n"
"s_add_u32 s36,s36,s82\n"
"v_mfma_f32_16x16x32_bf16 v[220:223],v[76:79],a[116:119],v[220:223]\n"
"s_addc_u32 s37,s37,0\n"
"s_lshl2_add_u32 s82,s23,s52\n"
"s_lshl_b32 s82,s82,13\n"
"v_mfma_f32_16x16x32_bf16 v[220:223],v[72:75],a[112:115],v[220:223]\n"
"buffer_load_dwordx4 v112,s[24:27],s76 offen offset:2048 sc1 lds\n"
"v_mfma_f32_16x16x32_bf16 v[188:191],v[72:75],a[104:107],v[188:191]\n"
"s_add_u32 s40,s40,s82\n"
"s_addc_u32 s41,s41,0\n"
"v_bfe_u32 v121,v112,5,2\n"
"v_mfma_f32_16x16x32_bf16 v[188:191],v[76:79],a[108:111],v[188:191]\n"
"v_bfe_u32 v122,v112,7,2\n"
"v_lshl_add_u32 v121,v122,3,v121\n"
"v_mfma_f32_16x16x32_bf16 v[156:159],v[76:79],a[100:103],v[156:159]\n"
"buffer_load_dwordx4 v112,s[24:27],s76 offen offset:3072 sc1 lds\n"
"v_mfma_f32_16x16x32_bf16 v[156:159],v[72:75],a[96:99],v[156:159]\n"
"s_xor_b32 s76,s76,49152\n"
"v_bfe_u32 v122,v112,9,1\n"
"s_waitcnt vmcnt(13) lgkmcnt(0)\n"
"s_barrier\n"
"v_mfma_f32_16x16x32_bf16 v[128:131],v[80:83],v[0:3],v[128:131]\n"
"ds_read_b64_tr_b16 v[64:65],v98 offset:32768\n"
"ds_read_b64_tr_b16 v[72:73],v99 offset:32768\n"
"v_mfma_f32_16x16x32_bf16 v[128:131],v[84:87],v[4:7],v[128:131]\n"
"ds_read_b64_tr_b16 v[66:67],v98 offset:33792\n"
"ds_read_b64_tr_b16 v[74:75],v99 offset:33792\n"
"v_mfma_f32_16x16x32_bf16 v[160:163],v[84:87],v[12:15],v[160:163]\n"
"ds_read_b64_tr_b16 v[68:69],v98 offset:40960\n"
"ds_read_b64_tr_b16 v[76:77],v99 offset:40960\n"
"v_mfma_f32_16x16x32_bf16 v[160:163],v[80:83],v[8:11],v[160:163]\n"
"s_add_u32 m0,s85,147456\n"
"ds_read_b64_tr_b16 v[70:71],v98 offset:41984\n"
"ds_read_b64_tr_b16 v[78:79],v99 offset:41984\n"
"v_mfma_f32_16x16x32_bf16 v[192:195],v[80:83],v[16:19],v[192:195]\n"
"buffer_load_dwordx4 v110,s[28:31],s86 offen lds\n"
"ds_read_b64_tr_b16 a[176:177],v107 offset:0\n"
"v_mfma_f32_16x16x32_bf16 v[192:195],v[84:87],v[20:23],v[192:195]\n"
"s_add_u32 m0,s85,148480\n"
"ds_read_b64_tr_b16 a[178:179],v107 offset:4096\n"
"ds_read_b64_tr_b16 a[180:181],v107 offset:8\n"
"v_mfma_f32_16x16x32_bf16 v[224:227],v[84:87],v[28:31],v[224:227]\n"
"buffer_load_dwordx4 v111,s[28:31],s86 offen lds\n"
"ds_read_b64_tr_b16 a[182:183],v107 offset:4104\n"
"v_mfma_f32_16x16x32_bf16 v[224:227],v[80:83],v[24:27],v[224:227]\n"
"s_xor_b32 s86,s86,262144\n"
"ds_read_b64_tr_b16 a[184:185],v107 offset:8192\n"
"ds_read_b64_tr_b16 a[186:187],v107 offset:12288\n"
"v_mfma_f32_16x16x32_bf16 v[228:231],v[88:91],v[24:27],v[228:231]\n"
"ds_read_b64_tr_b16 v[80:81],v100 offset:32768\n"
"s_waitcnt vmcnt(9)\n"
"v_xor_b32 v116,s80,v118\n"
"v_mfma_f32_16x16x32_bf16 v[228:231],v[92:95],v[28:31],v[228:231]\n"
"ds_read_b64_tr_b16 v[82:83],v100 offset:33792\n"
"ds_read_b64_tr_b16 a[188:189],v107 offset:8200\n"
"v_xor_b32 v117,s81,v119\n"
"v_mfma_f32_16x16x32_bf16 v[196:199],v[92:95],v[20:23],v[196:199]\n"
"ds_read_b64_tr_b16 v[84:85],v100 offset:40960\n"
"ds_read_b64_tr_b16 a[190:191],v107 offset:12296\n"
"v_mfma_f32_16x16x32_bf16 v[196:199],v[88:91],v[16:19],v[196:199]\n"
"s_add_u32 m0,s85,155648\n"
"ds_read_b64_tr_b16 v[86:87],v100 offset:41984\n"
"v_or_b32 v116,v116,v117\n"
"v_mfma_f32_16x16x32_bf16 v[164:167],v[88:91],v[8:11],v[164:167]\n"
"buffer_load_dwordx4 v110,s[28:31],s86 offen lds\n"
"v_mfma_f32_16x16x32_bf16 v[164:167],v[92:95],v[12:15],v[164:167]\n"
"s_add_u32 m0,s85,156672\n"
"v_cmp_ne_u32 vcc,0,v116\n"
"v_mfma_f32_16x16x32_bf16 v[132:135],v[92:95],v[4:7],v[132:135]\n"
"buffer_load_dwordx4 v111,s[28:31],s86 offen lds\n"
"v_mfma_f32_16x16x32_bf16 v[132:135],v[88:91],v[0:3],v[132:135]\n"
"s_xor_b32 s86,s86,16515072\n"
"s_cbranch_vccnz .Lfk_kv_s\n"
".Lfk_kv_ok:\n"
"v_mfma_f32_16x16x32_bf16 v[136:139],a[128:131],v[0:3],v[136:139]\n"
"ds_read_b64_tr_b16 v[88:89],v101 offset:32768\n"
"v_lshl_add_u32 v121,v122,2,v121\n"
"v_mfma_f32_16x16x32_bf16 v[136:139],a[132:135],v[4:7],v[136:139]\n"
"ds_read_b64_tr_b16 v[90:91],v101 offset:33792\n"
"v_mul_u32_u24 v121,7168,v121\n"
"v_mfma_f32_16x16x32_bf16 v[168:171],a[132:135],v[12:15],v[168:171]\n"
"ds_read_b64_tr_b16 v[92:93],v101 offset:40960\n"
"v_and_b32 v122,16,v112\n"
"v_mfma_f32_16x16x32_bf16 v[168:171],a[128:131],v[8:11],v[168:171]\n"
"s_add_u32 m0,s85,81920\n"
"s_add_u32 s77,s73,16384\n"
"ds_read_b64_tr_b16 v[94:95],v101 offset:41984\n"
"v_mfma_f32_16x16x32_bf16 v[200:203],a[128:131],v[16:19],v[200:203]\n"
"buffer_load_dwordx4 v112,s[24:27],s77 offen offset:0 sc1 lds\n"
"v_mfma_f32_16x16x32_bf16 v[200:203],a[132:135],v[20:23],v[200:203]\n"
"v_add_u32 v115,v121,v122\n"
"s_mov_b32 s54,-229376\n"
"v_mfma_f32_16x16x32_bf16 v[232:235],a[132:135],v[28:31],v[232:235]\n"
"buffer_load_dwordx4 v112,s[24:27],s77 offen offset:1024 sc1 lds\n"
"v_mfma_f32_16x16x32_bf16 v[232:235],a[128:131],v[24:27],v[232:235]\n"
"s_lshl_b32 s55,s52,10\n"
"s_add_u32 s55,s55,131072\n"
"v_mfma_f32_16x16x32_bf16 v[236:239],a[136:139],v[24:27],v[236:239]\n"
"ds_read_b64_tr_b16 a[128:129],v102 offset:32768\n"
"s_mov_b32 s22,229376\n"
"v_mfma_f32_16x16x32_bf16 v[236:239],a[140:143],v[28:31],v[236:239]\n"
"ds_read_b64_tr_b16 a[130:131],v102 offset:33792\n"
"v_lshrrev_b32 v121,7,v112\n"
"v_mfma_f32_16x16x32_bf16 v[204:207],a[140:143],v[20:23],v[204:207]\n"
"ds_read_b64_tr_b16 a[132:133],v102 offset:40960\n"
"v_mul_u32_u24 v121,7168,v121\n"
"v_mfma_f32_16x16x32_bf16 v[204:207],a[136:139],v[16:19],v[204:207]\n"
"s_add_u32 m0,s85,65536\n"
"s_add_u32 s77,s73,65536\n"
"ds_read_b64_tr_b16 a[134:135],v102 offset:41984\n"
"v_mfma_f32_16x16x32_bf16 v[172:175],a[136:139],v[8:11],v[172:175]\n"
"buffer_load_dwordx4 v112,s[24:27],s77 offen offset:0 sc1 lds\n"
"v_mfma_f32_16x16x32_bf16 v[172:175],a[140:143],v[12:15],v[172:175]\n"
"v_lshlrev_b32 v123,3,v112\n"
"v_and_b32 v122,0x380,v123\n"
"v_mfma_f32_16x16x32_bf16 v[140:143],a[140:143],v[4:7],v[140:143]\n"
"buffer_load_dwordx4 v112,s[24:27],s77 offen offset:1024 sc1 lds\n"
"v_mfma_f32_16x16x32_bf16 v[140:143],a[136:139],v[0:3],v[140:143]\n"
"v_min_u32 v122,0x300,v122\n"
"v_add_u32 v122,v121,v122\n"
"v_mfma_f32_16x16x32_bf16 v[144:147],v[32:35],v[0:3],v[144:147]\n"
"ds_read_b64_tr_b16 a[136:137],v103 offset:32768\n"
"v_mfma_f32_16x16x32_bf16 v[144:147],v[36:39],v[4:7],v[144:147]\n"
"ds_read_b64_tr_b16 a[138:139],v103 offset:33792\n"
"v_mfma_f32_16x16x32_bf16 v[176:179],v[36:39],v[12:15],v[176:179]\n"
"ds_read_b64_tr_b16 a[140:141],v103 offset:40960\n"
"v_mfma_f32_16x16x32_bf16 v[176:179],v[32:35],v[8:11],v[176:179]\n"
"s_add_u32 m0,s85,73728\n"
"s_add_u32 s77,s73,24576\n"
"ds_read_b64_tr_b16 a[142:143],v103 offset:41984\n"
"v_mfma_f32_16x16x32_bf16 v[208:211],v[32:35],v[16:19],v[208:211]\n"
"buffer_load_dwordx4 v112,s[24:27],s77 offen offset:0 sc1 lds\n"
"v_mfma_f32_16x16x32_bf16 v[208:211],v[36:39],v[20:23],v[208:211]\n"
"ds_read_b128 a[96:99],v96 offset:0\n"
"v_mfma_f32_16x16x32_bf16 v[240:243],v[36:39],v[28:31],v[240:243]\n"
"buffer_load_dwordx4 v112,s[24:27],s77 offen offset:1024 sc1 lds\n"
"v_mfma_f32_16x16x32_bf16 v[240:243],v[32:35],v[24:27],v[240:243]\n"
"ds_read_b128 a[104:107],v96 offset:1024\n"
"v_mfma_f32_16x16x32_bf16 v[244:247],v[40:43],v[24:27],v[244:247]\n"
"ds_read_b64_tr_b16 v[32:33],v104 offset:32768\n"
"v_mfma_f32_16x16x32_bf16 v[244:247],v[44:47],v[28:31],v[244:247]\n"
"ds_read_b64_tr_b16 v[34:35],v104 offset:33792\n"
"v_mfma_f32_16x16x32_bf16 v[212:215],v[44:47],v[20:23],v[212:215]\n"
"ds_read_b64_tr_b16 v[36:37],v104 offset:40960\n"
"v_mfma_f32_16x16x32_bf16 v[212:215],v[40:43],v[16:19],v[212:215]\n"
"ds_read_b64_tr_b16 v[38:39],v104 offset:41984\n"
"s_mov_b32 s50,22400\n"
"v_mfma_f32_16x16x32_bf16 v[180:183],v[40:43],v[8:11],v[180:183]\n"
"buffer_load_dword a148,v122,s[48:51],0 offen\n"
"v_mfma_f32_16x16x32_bf16 v[180:183],v[44:47],v[12:15],v[180:183]\n"
"ds_read_b128 a[112:115],v96 offset:2048\n"
"v_mfma_f32_16x16x32_bf16 v[148:151],v[44:47],v[4:7],v[148:151]\n"
"v_mfma_f32_16x16x32_bf16 v[148:151],v[40:43],v[0:3],v[148:151]\n"
"ds_read_b128 a[120:123],v96 offset:3072\n"
"v_mfma_f32_16x16x32_bf16 v[152:155],v[48:51],v[0:3],v[152:155]\n"
"ds_read_b64_tr_b16 v[40:41],v105 offset:32768\n"
"v_mfma_f32_16x16x32_bf16 v[152:155],v[52:55],v[4:7],v[152:155]\n"
"ds_read_b64_tr_b16 v[42:43],v105 offset:33792\n"
"v_mfma_f32_16x16x32_bf16 v[184:187],v[52:55],v[12:15],v[184:187]\n"
"ds_read_b64_tr_b16 v[44:45],v105 offset:40960\n"
"v_mfma_f32_16x16x32_bf16 v[184:187],v[48:51],v[8:11],v[184:187]\n"
"ds_read_b64_tr_b16 v[46:47],v105 offset:41984\n"
"v_mfma_f32_16x16x32_bf16 v[216:219],v[48:51],v[16:19],v[216:219]\n"
"ds_read_b128 a[100:103],v96 offset:16384\n"
"v_mfma_f32_16x16x32_bf16 v[216:219],v[52:55],v[20:23],v[216:219]\n"
"ds_read_b128 a[108:111],v96 offset:17408\n"
"v_mfma_f32_16x16x32_bf16 v[248:251],v[52:55],v[28:31],v[248:251]\n"
"ds_read_b128 a[116:119],v96 offset:18432\n"
"v_mfma_f32_16x16x32_bf16 v[248:251],v[48:51],v[24:27],v[248:251]\n"
"ds_read_b128 a[124:127],v96 offset:19456\n"
"v_mfma_f32_16x16x32_bf16 v[252:255],v[56:59],v[24:27],v[252:255]\n"
"v_mfma_f32_16x16x32_bf16 v[252:255],v[60:63],v[28:31],v[252:255]\n"
"buffer_store_dwordx4 a[160:163],v112,s[24:27],s3 offen offset:0 sc1\n"
"v_mfma_f32_16x16x32_bf16 v[220:223],v[60:63],v[20:23],v[220:223]\n"
"v_mfma_f32_16x16x32_bf16 v[220:223],v[56:59],v[16:19],v[220:223]\n"
"v_mfma_f32_16x16x32_bf16 v[188:191],v[56:59],v[8:11],v[188:191]\n"
"v_mfma_f32_16x16x32_bf16 v[188:191],v[60:63],v[12:15],v[188:191]\n"
"buffer_store_dwordx4 a[164:167],v112,s[24:27],s3 offen offset:1024 sc1\n"
"v_mfma_f32_16x16x32_bf16 v[156:159],v[60:63],v[4:7],v[156:159]\n"
"v_mfma_f32_16x16x32_bf16 v[156:159],v[56:59],v[0:3],v[156:159]\n"
"s_waitcnt lgkmcnt(0)\n"
"s_barrier\n"
"v_mfma_f32_16x16x32_bf16 v[128:131],v[64:67],a[96:99],v[128:131]\n"
"ds_read_b64_tr_b16 v[48:49],v98 offset:0\n"
"ds_read_b64_tr_b16 v[56:57],v99 offset:0\n"
"v_mfma_f32_16x16x32_bf16 v[128:131],v[68:71],a[100:103],v[128:131]\n"
"ds_read_b64_tr_b16 v[50:51],v98 offset:1024\n"
"ds_read_b64_tr_b16 v[58:59],v99 offset:1024\n"
"v_mfma_f32_16x16x32_bf16 v[160:163],v[68:71],a[108:111],v[160:163]\n"
"ds_read_b64_tr_b16 v[52:53],v98 offset:8192\n"
"ds_read_b64_tr_b16 v[60:61],v99 offset:8192\n"
"v_mfma_f32_16x16x32_bf16 v[160:163],v[64:67],a[104:107],v[160:163]\n"
"s_add_u32 m0,s78,0\n"
"ds_read_b64_tr_b16 v[54:55],v98 offset:9216\n"
"ds_read_b64_tr_b16 v[62:63],v99 offset:9216\n"
"v_mfma_f32_16x16x32_bf16 v[192:195],v[64:67],a[112:115],v[192:195]\n"
"buffer_load_dwordx4 v112,s[24:27],s76 offen offset:0 sc1 lds\n"
"v_mfma_f32_16x16x32_bf16 v[192:195],v[68:71],a[116:119],v[192:195]\n"
"v_mfma_f32_16x16x32_bf16 v[224:227],v[68:71],a[124:127],v[224:227]\n"
"v_mfma_f32_16x16x32_bf16 v[224:227],v[64:67],a[120:123],v[224:227]\n"
"v_mfma_f32_16x16x32_bf16 v[228:231],v[72:75],a[120:123],v[228:231]\n"
"ds_read_b64_tr_b16 v[64:65],v100 offset:0\n"
"v_mfma_f32_16x16x32_bf16 v[228:231],v[76:79],a[124:127],v[228:231]\n"
"ds_read_b64_tr_b16 v[66:67],v100 offset:1024\n"
"v_mfma_f32_16x16x32_bf16 v[196:199],v[76:79],a[116:119],v[196:199]\n"
"ds_read_b64_tr_b16 v[68:69],v100 offset:8192\n"
"v_mfma_f32_16x16x32_bf16 v[196:199],v[72:75],a[112:115],v[196:199]\n"
"ds_read_b64_tr_b16 v[70:71],v100 offset:9216\n"
"v_mfma_f32_16x16x32_bf16 v[164:167],v[72:75],a[104:107],v[164:167]\n"
"buffer_load_dwordx4 v112,s[24:27],s76 offen offset:1024 sc1 lds\n"
"v_mfma_f32_16x16x32_bf16 v[164:167],v[76:79],a[108:111],v[164:167]\n"
"v_mfma_f32_16x16x32_bf16 v[132:135],v[76:79],a[100:103],v[132:135]\n"
"buffer_load_dwordx4 v112,s[24:27],s76 offen offset:2048 sc1 lds\n"
"v_mfma_f32_16x16x32_bf16 v[132:135],v[72:75],a[96:99],v[132:135]\n"
"v_mfma_f32_16x16x32_bf16 v[136:139],v[80:83],a[96:99],v[136:139]\n"
"ds_read_b64_tr_b16 v[72:73],v101 offset:0\n"
"v_mfma_f32_16x16x32_bf16 v[136:139],v[84:87],a[100:103],v[136:139]\n"
"ds_read_b64_tr_b16 v[74:75],v101 offset:1024\n"
"v_mfma_f32_16x16x32_bf16 v[168:171],v[84:87],a[108:111],v[168:171]\n"
"ds_read_b64_tr_b16 v[76:77],v101 offset:8192\n"
"v_mfma_f32_16x16x32_bf16 v[168:171],v[80:83],a[104:107],v[168:171]\n"
"ds_read_b64_tr_b16 v[78:79],v101 offset:9216\n"
"v_mfma_f32_16x16x32_bf16 v[200:203],v[80:83],a[112:115],v[200:203]\n"
"buffer_load_dwordx4 v112,s[24:27],s76 offen offset:3072 sc1 lds\n"
"v_mfma_f32_16x16x32_bf16 v[200:203],v[84:87],a[116:119],v[200:203]\n"
"s_xor_b32 s76,s76,16384\n"
"v_mfma_f32_16x16x32_bf16 v[232:235],v[84:87],a[124:127],v[232:235]\n"
"v_mfma_f32_16x16x32_bf16 v[232:235],v[80:83],a[120:123],v[232:235]\n"
"v_mfma_f32_16x16x32_bf16 v[236:239],v[88:91],a[120:123],v[236:239]\n"
"ds_read_b64_tr_b16 v[80:81],v102 offset:0\n"
"v_mfma_f32_16x16x32_bf16 v[236:239],v[92:95],a[124:127],v[236:239]\n"
"ds_read_b64_tr_b16 v[82:83],v102 offset:1024\n"
"v_mfma_f32_16x16x32_bf16 v[204:207],v[92:95],a[116:119],v[204:207]\n"
"ds_read_b64_tr_b16 v[84:85],v102 offset:8192\n"
"v_mfma_f32_16x16x32_bf16 v[204:207],v[88:91],a[112:115],v[204:207]\n"
"s_add_u32 m0,s78,16384\n"
"ds_read_b64_tr_b16 v[86:87],v102 offset:9216\n"
"v_mfma_f32_16x16x32_bf16 v[172:175],v[88:91],a[104:107],v[172:175]\n"
"buffer_load_dwordx4 v112,s[24:27],s76 offen offset:0 sc1 lds\n"
"v_mfma_f32_16x16x32_bf16 v[172:175],v[92:95],a[108:111],v[172:175]\n"
"v_mfma_f32_16x16x32_bf16 v[140:143],v[92:95],a[100:103],v[140:143]\n"
"buffer_load_dwordx4 v112,s[24:27],s76 offen offset:1024 sc1 lds\n"
"v_mfma_f32_16x16x32_bf16 v[140:143],v[88:91],a[96:99],v[140:143]\n"
".Lfk_q27:\n"
"s_waitcnt vmcnt(19)\n"
"v_mfma_f32_16x16x32_bf16 v[144:147],a[128:131],a[96:99],v[144:147]\n"
"ds_read_b64_tr_b16 v[88:89],v103 offset:0\n"
"v_mfma_f32_16x16x32_bf16 v[144:147],a[132:135],a[100:103],v[144:147]\n"
"ds_read_b64_tr_b16 v[90:91],v103 offset:1024\n"
"v_mfma_f32_16x16x32_bf16 v[176:179],a[132:135],a[108:111],v[176:179]\n"
"ds_read_b64_tr_b16 v[92:93],v103 offset:8192\n"
"v_mfma_f32_16x16x32_bf16 v[176:179],a[128:131],a[104:107],v[176:179]\n"
"ds_read_b64_tr_b16 v[94:95],v103 offset:9216\n"
"v_mfma_f32_16x16x32_bf16 v[208:211],a[128:131],a[112:115],v[208:211]\n"
"buffer_load_dwordx4 v112,s[24:27],s76 offen offset:2048 sc1 lds\n"
"v_mfma_f32_16x16x32_bf16 v[208:211],a[132:135],a[116:119],v[208:211]\n"
"ds_read_b128 v[0:3],v96 offset:32768\n"
"v_mfma_f32_16x16x32_bf16 v[240:243],a[132:135],a[124:127],v[240:243]\n"
"v_mfma_f32_16x16x32_bf16 v[240:243],a[128:131],a[120:123],v[240:243]\n"
"ds_read_b128 v[8:11],v96 offset:33792\n"
"v_mfma_f32_16x16x32_bf16 v[244:247],a[136:139],a[120:123],v[244:247]\n"
"ds_read_b64_tr_b16 a[128:129],v104 offset:0\n"
"v_mfma_f32_16x16x32_bf16 v[244:247],a[140:143],a[124:127],v[244:247]\n"
"ds_read_b64_tr_b16 a[130:131],v104 offset:1024\n"
"v_mfma_f32_16x16x32_bf16 v[212:215],a[140:143],a[116:119],v[212:215]\n"
"ds_read_b64_tr_b16 a[132:133],v104 offset:8192\n"
"v_mfma_f32_16x16x32_bf16 v[212:215],a[136:139],a[112:115],v[212:215]\n"
"ds_read_b64_tr_b16 a[134:135],v104 offset:9216\n"
"v_mfma_f32_16x16x32_bf16 v[180:183],a[136:139],a[104:107],v[180:183]\n"
"buffer_load_dwordx4 v112,s[24:27],s76 offen offset:3072 sc1 lds\n"
"v_mfma_f32_16x16x32_bf16 v[180:183],a[140:143],a[108:111],v[180:183]\n"
"s_add_u32 m0,s85,122880\n"
"s_add_u32 s77,s73,0\n"
"ds_read_b128 v[16:19],v96 offset:34816\n"
"v_mfma_f32_16x16x32_bf16 v[148:151],a[140:143],a[100:103],v[148:151]\n"
"buffer_load_dwordx4 v112,s[24:27],s77 offen offset:0 sc1 lds\n"
"v_mfma_f32_16x16x32_bf16 v[148:151],a[136:139],a[96:99],v[148:151]\n"
"s_xor_b32 s76,s76,1032192\n"
"ds_read_b128 v[24:27],v96 offset:35840\n"
"v_mfma_f32_16x16x32_bf16 v[152:155],v[32:35],a[96:99],v[152:155]\n"
"ds_read_b64_tr_b16 a[136:137],v105 offset:0\n"
"v_mfma_f32_16x16x32_bf16 v[152:155],v[36:39],a[100:103],v[152:155]\n"
"ds_read_b64_tr_b16 a[138:139],v105 offset:1024\n"
"v_mfma_f32_16x16x32_bf16 v[184:187],v[36:39],a[108:111],v[184:187]\n"
"ds_read_b64_tr_b16 a[140:141],v105 offset:8192\n"
"v_mfma_f32_16x16x32_bf16 v[184:187],v[32:35],a[104:107],v[184:187]\n"
"ds_read_b64_tr_b16 a[142:143],v105 offset:9216\n"
"v_mfma_f32_16x16x32_bf16 v[216:219],v[32:35],a[112:115],v[216:219]\n"
"ds_read_b128 v[4:7],v96 offset:49152\n"
"v_mfma_f32_16x16x32_bf16 v[216:219],v[36:39],a[116:119],v[216:219]\n"
"ds_read_b128 v[12:15],v96 offset:50176\n"
"v_mfma_f32_16x16x32_bf16 v[248:251],v[36:39],a[124:127],v[248:251]\n"
"ds_read_b128 v[20:23],v96 offset:51200\n"
"v_mfma_f32_16x16x32_bf16 v[248:251],v[32:35],a[120:123],v[248:251]\n"
"ds_read_b128 v[28:31],v96 offset:52224\n"
"v_mfma_f32_16x16x32_bf16 v[252:255],v[40:43],a[120:123],v[252:255]\n"
"buffer_load_dwordx4 v112,s[24:27],s77 offen offset:1024 sc1 lds\n"
"v_mfma_f32_16x16x32_bf16 v[252:255],v[44:47],a[124:127],v[252:255]\n"
"v_mfma_f32_16x16x32_bf16 v[220:223],v[44:47],a[116:119],v[220:223]\n"
"s_add_u32 m0,s85,114688\n"
"s_add_u32 s77,s73,32768\n"
"v_mfma_f32_16x16x32_bf16 v[220:223],v[40:43],a[112:115],v[220:223]\n"
"buffer_load_dwordx4 v112,s[24:27],s77 offen offset:0 sc1 lds\n"
"v_mfma_f32_16x16x32_bf16 v[188:191],v[40:43],a[104:107],v[188:191]\n"
"v_mfma_f32_16x16x32_bf16 v[188:191],v[44:47],a[108:111],v[188:191]\n"
"v_mfma_f32_16x16x32_bf16 v[156:159],v[44:47],a[100:103],v[156:159]\n"
"buffer_load_dwordx4 v112,s[24:27],s77 offen offset:1024 sc1 lds\n"
"v_mfma_f32_16x16x32_bf16 v[156:159],v[40:43],a[96:99],v[156:159]\n"
"s_waitcnt vmcnt(21) lgkmcnt(0)\n"
"s_barrier\n"
"v_mfma_f32_16x16x32_bf16 v[128:131],v[48:51],v[0:3],v[128:131]\n"
"ds_read_b64_tr_b16 v[32:33],v98 offset:49152\n"
"ds_read_b64_tr_b16 v[40:41],v99 offset:49152\n"
"v_mfma_f32_16x16x32_bf16 v[128:131],v[52:55],v[4:7],v[128:131]\n"
"ds_read_b64_tr_b16 v[34:35],v98 offset:50176\n"
"ds_read_b64_tr_b16 v[42:43],v99 offset:50176\n"
"v_mfma_f32_16x16x32_bf16 v[160:163],v[52:55],v[12:15],v[160:163]\n"
"s_add_u32 m0,s85,98304\n"
"ds_read_b64_tr_b16 v[36:37],v98 offset:57344\n"
"ds_read_b64_tr_b16 v[44:45],v99 offset:57344\n"
"v_mfma_f32_16x16x32_bf16 v[160:163],v[48:51],v[8:11],v[160:163]\n"
"s_add_u32 s77,s73,49152\n"
"ds_read_b64_tr_b16 v[38:39],v98 offset:58368\n"
"ds_read_b64_tr_b16 v[46:47],v99 offset:58368\n"
"v_mfma_f32_16x16x32_bf16 v[192:195],v[48:51],v[16:19],v[192:195]\n"
"buffer_load_dwordx4 v112,s[24:27],s77 offen offset:0 sc1 lds\n"
"v_mfma_f32_16x16x32_bf16 v[192:195],v[52:55],v[20:23],v[192:195]\n"
"v_mfma_f32_16x16x32_bf16 v[224:227],v[52:55],v[28:31],v[224:227]\n"
"buffer_load_dwordx4 v112,s[24:27],s77 offen offset:1024 sc1 lds\n"
"v_mfma_f32_16x16x32_bf16 v[224:227],v[48:51],v[24:27],v[224:227]\n"
"v_mfma_f32_16x16x32_bf16 v[228:231],v[56:59],v[24:27],v[228:231]\n"
"ds_read_b64_tr_b16 v[48:49],v100 offset:49152\n"
"v_mfma_f32_16x16x32_bf16 v[228:231],v[60:63],v[28:31],v[228:231]\n"
"ds_read_b64_tr_b16 v[50:51],v100 offset:50176\n"
"v_mfma_f32_16x16x32_bf16 v[196:199],v[60:63],v[20:23],v[196:199]\n"
"ds_read_b64_tr_b16 v[52:53],v100 offset:57344\n"
"v_mfma_f32_16x16x32_bf16 v[196:199],v[56:59],v[16:19],v[196:199]\n"
"s_add_u32 m0,s85,106496\n"
"s_add_u32 s77,s73,8192\n"
"ds_read_b64_tr_b16 v[54:55],v100 offset:58368\n"
"v_mfma_f32_16x16x32_bf16 v[164:167],v[56:59],v[8:11],v[164:167]\n"
"buffer_load_dwordx4 v112,s[24:27],s77 offen offset:0 sc1 lds\n"
"v_mfma_f32_16x16x32_bf16 v[164:167],v[60:63],v[12:15],v[164:167]\n"
"v_mfma_f32_16x16x32_bf16 v[132:135],v[60:63],v[4:7],v[132:135]\n"
"buffer_load_dwordx4 v112,s[24:27],s77 offen offset:1024 sc1 lds\n"
"v_mfma_f32_16x16x32_bf16 v[132:135],v[56:59],v[0:3],v[132:135]\n"
"v_mfma_f32_16x16x32_bf16 v[136:139],v[64:67],v[0:3],v[136:139]\n"
"ds_read_b64_tr_b16 v[56:57],v101 offset:49152\n"
"v_mfma_f32_16x16x32_bf16 v[136:139],v[68:71],v[4:7],v[136:139]\n"
"ds_read_b64_tr_b16 v[58:59],v101 offset:50176\n"
"v_mfma_f32_16x16x32_bf16 v[168:171],v[68:71],v[12:15],v[168:171]\n"
"ds_read_b64_tr_b16 v[60:61],v101 offset:57344\n"
"ds_read_b64_tr_b16 v[62:63],v101 offset:58368\n"
"v_mfma_f32_16x16x32_bf16 v[168:171],v[64:67],v[8:11],v[168:171]\n"
"s_add_u32 s48,s48,28672\n"
"s_addc_u32 s49,s49,0\n"
"v_mfma_f32_16x16x32_bf16 v[200:203],v[64:67],v[16:19],v[200:203]\n"
"buffer_load_dword a148,v122,s[48:51],0 offen\n"
"v_mfma_f32_16x16x32_bf16 v[200:203],v[68:71],v[20:23],v[200:203]\n"
"v_mfma_f32_16x16x32_bf16 v[232:235],v[68:71],v[28:31],v[232:235]\n"
"v_mfma_f32_16x16x32_bf16 v[232:235],v[64:67],v[24:27],v[232:235]\n"
"v_mfma_f32_16x16x32_bf16 v[236:239],v[72:75],v[24:27],v[236:239]\n"
"ds_read_b64_tr_b16 v[64:65],v102 offset:49152\n"
"v_mfma_f32_16x16x32_bf16 v[236:239],v[76:79],v[28:31],v[236:239]\n"
"ds_read_b64_tr_b16 v[66:67],v102 offset:50176\n"
"v_mfma_f32_16x16x32_bf16 v[204:207],v[76:79],v[20:23],v[204:207]\n"
"ds_read_b64_tr_b16 v[68:69],v102 offset:57344\n"
"v_mfma_f32_16x16x32_bf16 v[204:207],v[72:75],v[16:19],v[204:207]\n"
"ds_read_b64_tr_b16 v[70:71],v102 offset:58368\n"
"v_mfma_f32_16x16x32_bf16 v[172:175],v[72:75],v[8:11],v[172:175]\n"
"v_mfma_f32_16x16x32_bf16 v[172:175],v[76:79],v[12:15],v[172:175]\n"
"v_mfma_f32_16x16x32_bf16 v[140:143],v[76:79],v[4:7],v[140:143]\n"
"v_mfma_f32_16x16x32_bf16 v[140:143],v[72:75],v[0:3],v[140:143]\n"
"s_waitcnt vmcnt(9)\n"
"v_mfma_f32_16x16x32_bf16 v[144:147],v[80:83],v[0:3],v[144:147]\n"
"ds_read_b64_tr_b16 v[72:73],v103 offset:49152\n"
"s_mov_b32 s90,0\n"
"v_mfma_f32_16x16x32_bf16 v[144:147],v[84:87],v[4:7],v[144:147]\n"
"ds_read_b64_tr_b16 v[74:75],v103 offset:50176\n"
"s_mov_b32 s16,0xc21c0000\n"
"v_mfma_f32_16x16x32_bf16 v[176:179],v[84:87],v[12:15],v[176:179]\n"
"ds_read_b64_tr_b16 v[76:77],v103 offset:57344\n"
"s_mov_b32 s17,0x41a00000\n"
"v_mfma_f32_16x16x32_bf16 v[176:179],v[80:83],v[8:11],v[176:179]\n"
"ds_read_b64_tr_b16 v[78:79],v103 offset:58368\n"
"s_mov_b32 s89,0\n"
"v_mfma_f32_16x16x32_bf16 v[208:211],v[80:83],v[16:19],v[208:211]\n"
"s_xor_b32 s82,s78,8192\n"
"v_add_u32 v116,s82,v112\n"
"v_mfma_f32_16x16x32_bf16 v[208:211],v[84:87],v[20:23],v[208:211]\n"
"ds_read_b128 a[96:99],v96 offset:0\n"
"v_add_u32 v117,4096,v112\n"
"v_mfma_f32_16x16x32_bf16 v[240:243],v[84:87],v[28:31],v[240:243]\n"
"s_getpc_b64 s[20:21]\n"
".Lfk_evp:\n"
"s_add_u32 s20,s20,.Lfk_ev-.Lfk_evp\n"
"s_addc_u32 s21,s21,0\n"
"v_mfma_f32_16x16x32_bf16 v[240:243],v[80:83],v[24:27],v[240:243]\n"
"ds_read_b128 a[104:107],v96 offset:1024\n"
"v_mfma_f32_16x16x32_bf16 v[244:247],v[88:91],v[24:27],v[244:247]\n"
"ds_read_b64_tr_b16 v[80:81],v104 offset:49152\n"
"v_mfma_f32_16x16x32_bf16 v[244:247],v[92:95],v[28:31],v[244:247]\n"
"ds_read_b64_tr_b16 v[82:83],v104 offset:50176\n"
"v_mfma_f32_16x16x32_bf16 v[212:215],v[92:95],v[20:23],v[212:215]\n"
"ds_read_b64_tr_b16 v[84:85],v104 offset:57344\n"
"v_mfma_f32_16x16x32_bf16 v[212:215],v[88:91],v[16:19],v[212:215]\n"
"ds_read_b64_tr_b16 v[86:87],v104 offset:58368\n"
"v_mfma_f32_16x16x32_bf16 v[180:183],v[88:91],v[8:11],v[180:183]\n"
"buffer_store_dwordx4 a[156:159],v112,s[24:27],s74 offen offset:0 sc1\n"
"v_mfma_f32_16x16x32_bf16 v[180:183],v[92:95],v[12:15],v[180:183]\n"
"ds_read_b128 a[112:115],v96 offset:2048\n"
"v_mfma_f32_16x16x32_bf16 v[148:151],v[92:95],v[4:7],v[148:151]\n"
"v_mfma_f32_16x16x32_bf16 v[148:151],v[88:91],v[0:3],v[148:151]\n"
"ds_read_b128 a[120:123],v96 offset:3072\n"
"v_mfma_f32_16x16x32_bf16 v[152:155],a[128:131],v[0:3],v[152:155]\n"
"ds_read_b64_tr_b16 v[88:89],v105 offset:49152\n"
"v_mfma_f32_16x16x32_bf16 v[152:155],a[132:135],v[4:7],v[152:155]\n"
"ds_read_b64_tr_b16 v[90:91],v105 offset:50176\n"
"v_mfma_f32_16x16x32_bf16 v[184:187],a[132:135],v[12:15],v[184:187]\n"
"ds_read_b64_tr_b16 v[92:93],v105 offset:57344\n"
"v_mfma_f32_16x16x32_bf16 v[184:187],a[128:131],v[8:11],v[184:187]\n"
"ds_read_b64_tr_b16 v[94:95],v105 offset:58368\n"
"v_mfma_f32_16x16x32_bf16 v[216:219],a[128:131],v[16:19],v[216:219]\n"
"ds_read_b128 a[100:103],v96 offset:16384\n"
"v_mfma_f32_16x16x32_bf16 v[216:219],a[132:135],v[20:23],v[216:219]\n"
"ds_read_b128 a[108:111],v96 offset:17408\n"
"v_add_u32 v113,65536,v112\n"
"v_mfma_f32_16x16x32_bf16 v[248:251],a[132:135],v[28:31],v[248:251]\n"
"ds_read_b128 a[116:119],v96 offset:18432\n"
"v_add_u32 v114,65536,v112\n"
"v_mfma_f32_16x16x32_bf16 v[248:251],a[128:131],v[24:27],v[248:251]\n"
"ds_read_b128 a[124:127],v96 offset:19456\n"
"v_mfma_f32_16x16x32_bf16 v[252:255],a[136:139],v[24:27],v[252:255]\n"
"v_mfma_f32_16x16x32_bf16 v[252:255],a[140:143],v[28:31],v[252:255]\n"
"buffer_store_dwordx4 a[156:159],v112,s[24:27],s74 offen offset:1024 sc1\n"
"v_mfma_f32_16x16x32_bf16 v[220:223],a[140:143],v[20:23],v[220:223]\n"
"s_lshr_b32 s82,s74,1\n"
"s_add_u32 s82,s82,88080384\n"
"v_mfma_f32_16x16x32_bf16 v[220:223],a[136:139],v[16:19],v[220:223]\n"
"buffer_store_dwordx4 a[156:159],v112,s[24:27],s82 offen sc1\n"
"v_mfma_f32_16x16x32_bf16 v[188:191],a[136:139],v[8:11],v[188:191]\n"
"v_mfma_f32_16x16x32_bf16 v[188:191],a[140:143],v[12:15],v[188:191]\n"
"buffer_store_dword a156,off,s[24:27],s72 sc1\n"
"v_mfma_f32_16x16x32_bf16 v[156:159],a[140:143],v[4:7],v[156:159]\n"
"v_mfma_f32_16x16x32_bf16 v[156:159],a[136:139],v[0:3],v[156:159]\n"
"s_waitcnt vmcnt(11) lgkmcnt(0)\n"
"s_barrier\n"
"v_mfma_f32_16x16x32_bf16 v[224:227],v[32:35],a[120:123],v[224:227]\n"
"ds_read_b128 a[64:67],v113 offset:57344\n"
"v_mov_b32 v96,0xc1980000\n"
"v_mfma_f32_16x16x32_bf16 v[224:227],v[36:39],a[124:127],v[224:227]\n"
"ds_read_b128 a[68:71],v113 offset:58368\n"
"v_mov_b32 v97,0xc1980000\n"
"v_mfma_f32_16x16x32_bf16 v[228:231],v[44:47],a[124:127],v[228:231]\n"
"ds_read_b128 a[72:75],v113 offset:59392\n"
"v_mov_b32 v98,0xc1980000\n"
"v_mfma_f32_16x16x32_bf16 v[228:231],v[40:43],a[120:123],v[228:231]\n"
"ds_read_b128 a[76:79],v113 offset:60416\n"
"v_mov_b32 v99,0xc1980000\n"
"v_mfma_f32_16x16x32_bf16 v[232:235],v[48:51],a[120:123],v[232:235]\n"
"ds_read_b128 a[80:83],v113 offset:61440\n"
"v_mov_b32 v100,0xc1980000\n"
"v_mfma_f32_16x16x32_bf16 v[232:235],v[52:55],a[124:127],v[232:235]\n"
"ds_read_b128 a[84:87],v113 offset:62464\n"
"v_mov_b32 v101,0xc1980000\n"
"v_mfma_f32_16x16x32_bf16 v[236:239],v[60:63],a[124:127],v[236:239]\n"
"ds_read_b128 a[88:91],v113 offset:63488\n"
"v_mov_b32 v102,0xc1980000\n"
"v_mfma_f32_16x16x32_bf16 v[236:239],v[56:59],a[120:123],v[236:239]\n"
"ds_read_b128 a[92:95],v113 offset:64512\n"
"v_mov_b32 v103,0xc1980000\n"
"v_mfma_f32_16x16x32_bf16 v[240:243],v[64:67],a[120:123],v[240:243]\n"
"v_mov_b32 v104,0xc1980000\n"
"v_mov_b32 v105,0xc1980000\n"
"v_mfma_f32_16x16x32_bf16 v[240:243],v[68:71],a[124:127],v[240:243]\n"
"ds_write_b32 v124,v125\n"
"v_mfma_f32_16x16x32_bf16 v[244:247],v[76:79],a[124:127],v[244:247]\n"
"v_mov_b32 v107,0xc1980000\n"
"v_mov_b32 v108,0xc1980000\n"
"v_mfma_f32_16x16x32_bf16 v[244:247],v[72:75],a[120:123],v[244:247]\n"
"v_mov_b32 v109,0xc1980000\n"
"v_mov_b32 v110,0xc1980000\n"
"v_mfma_f32_16x16x32_bf16 v[248:251],v[80:83],a[120:123],v[248:251]\n"
"v_mov_b32 v106,0xc1980000\n"
"v_mov_b32 v111,0xc1980000\n"
"v_mfma_f32_16x16x32_bf16 v[248:251],v[84:87],a[124:127],v[248:251]\n"
"v_mfma_f32_16x16x32_bf16 v[252:255],v[92:95],a[124:127],v[252:255]\n"
"v_mfma_f32_16x16x32_bf16 v[252:255],v[88:91],a[120:123],v[252:255]\n"
"v_mfma_f32_16x16x32_bf16 v[128:131],v[32:35],a[96:99],v[128:131]\n"
"v_mfma_f32_16x16x32_bf16 v[128:131],v[36:39],a[100:103],v[128:131]\n"
"v_cvt_pk_bf16_f32 v232,v232,v233\n"
"v_cvt_pk_bf16_f32 v233,v234,v235\n"
"v_mfma_f32_16x16x32_bf16 v[132:135],v[44:47],a[100:103],v[132:135]\n"
"v_cvt_pk_bf16_f32 v224,v224,v225\n"
"v_cvt_pk_bf16_f32 v225,v226,v227\n"
"v_mfma_f32_16x16x32_bf16 v[132:135],v[40:43],a[96:99],v[132:135]\n"
"v_cvt_pk_bf16_f32 v226,v228,v229\n"
"v_cvt_pk_bf16_f32 v227,v230,v231\n"
"v_mfma_f32_16x16x32_bf16 v[136:139],v[48:51],a[96:99],v[136:139]\n"
"ds_write_b128 v116,v[224:227] offset:0\n"
"v_mfma_f32_16x16x32_bf16 v[136:139],v[52:55],a[100:103],v[136:139]\n"
"v_cvt_pk_bf16_f32 v234,v236,v237\n"
"v_cvt_pk_bf16_f32 v235,v238,v239\n"
"v_mfma_f32_16x16x32_bf16 v[140:143],v[60:63],a[100:103],v[140:143]\n"
"ds_write_b128 v116,v[232:235] offset:1024\n"
"v_mfma_f32_16x16x32_bf16 v[140:143],v[56:59],a[96:99],v[140:143]\n"
"v_cvt_pk_bf16_f32 v240,v240,v241\n"
"v_cvt_pk_bf16_f32 v241,v242,v243\n"
"v_mfma_f32_16x16x32_bf16 v[144:147],v[64:67],a[96:99],v[144:147]\n"
"v_cvt_pk_bf16_f32 v242,v244,v245\n"
"v_cvt_pk_bf16_f32 v243,v246,v247\n"
"v_mfma_f32_16x16x32_bf16 v[144:147],v[68:71],a[100:103],v[144:147]\n"
"ds_write_b128 v116,v[240:243] offset:2048\n"
"v_cvt_pk_bf16_f32 v248,v248,v249\n"
"v_mfma_f32_16x16x32_bf16 v[148:151],v[76:79],a[100:103],v[148:151]\n"
"v_cvt_pk_bf16_f32 v249,v250,v251\n"
"v_cvt_pk_bf16_f32 v250,v252,v253\n"
"v_cvt_pk_bf16_f32 v251,v254,v255\n"
"v_mfma_f32_16x16x32_bf16 v[148:151],v[72:75],a[96:99],v[148:151]\n"
"ds_write_b128 v116,v[248:251] offset:3072\n"
"v_mfma_f32_16x16x32_bf16 v[152:155],v[80:83],a[96:99],v[152:155]\n"
"ds_read_b128 a[48:51],v116 offset:0\n"
"ds_read_b128 a[52:55],v116 offset:1024\n"
"v_mfma_f32_16x16x32_bf16 v[152:155],v[84:87],a[100:103],v[152:155]\n"
"ds_read_b128 a[56:59],v116 offset:2048\n"
"ds_read_b128 a[60:63],v116 offset:3072\n"
"v_mfma_f32_16x16x32_bf16 v[156:159],v[92:95],a[100:103],v[156:159]\n"
"v_mfma_f32_16x16x32_bf16 v[156:159],v[88:91],a[96:99],v[156:159]\n"
"v_mfma_f32_16x16x32_bf16 v[160:163],v[32:35],a[104:107],v[160:163]\n"
"v_mfma_f32_16x16x32_bf16 v[160:163],v[36:39],a[108:111],v[160:163]\n"
"v_cvt_pk_bf16_f32 v136,v136,v137\n"
"v_cvt_pk_bf16_f32 v137,v138,v139\n"
"v_mfma_f32_16x16x32_bf16 v[164:167],v[44:47],a[108:111],v[164:167]\n"
"v_cvt_pk_bf16_f32 v128,v128,v129\n"
"v_cvt_pk_bf16_f32 v129,v130,v131\n"
"v_mfma_f32_16x16x32_bf16 v[164:167],v[40:43],a[104:107],v[164:167]\n"
"v_cvt_pk_bf16_f32 v130,v132,v133\n"
"v_cvt_pk_bf16_f32 v131,v134,v135\n"
"v_mfma_f32_16x16x32_bf16 v[168:171],v[48:51],a[104:107],v[168:171]\n"
"ds_write_b128 v116,v[128:131] offset:16384\n"
"v_mfma_f32_16x16x32_bf16 v[168:171],v[52:55],a[108:111],v[168:171]\n"
"v_cvt_pk_bf16_f32 v138,v140,v141\n"
"v_cvt_pk_bf16_f32 v139,v142,v143\n"
"v_mfma_f32_16x16x32_bf16 v[172:175],v[60:63],a[108:111],v[172:175]\n"
"ds_write_b128 v116,v[136:139] offset:17408\n"
"v_mfma_f32_16x16x32_bf16 v[172:175],v[56:59],a[104:107],v[172:175]\n"
"v_cvt_pk_bf16_f32 v144,v144,v145\n"
"v_cvt_pk_bf16_f32 v145,v146,v147\n"
"v_mfma_f32_16x16x32_bf16 v[176:179],v[64:67],a[104:107],v[176:179]\n"
"v_cvt_pk_bf16_f32 v146,v148,v149\n"
"v_cvt_pk_bf16_f32 v147,v150,v151\n"
"v_mfma_f32_16x16x32_bf16 v[176:179],v[68:71],a[108:111],v[176:179]\n"
"ds_write_b128 v116,v[144:147] offset:18432\n"
"v_cvt_pk_bf16_f32 v152,v152,v153\n"
"v_mfma_f32_16x16x32_bf16 v[180:183],v[76:79],a[108:111],v[180:183]\n"
"v_cvt_pk_bf16_f32 v153,v154,v155\n"
"v_cvt_pk_bf16_f32 v154,v156,v157\n"
"v_cvt_pk_bf16_f32 v155,v158,v159\n"
"v_mfma_f32_16x16x32_bf16 v[180:183],v[72:75],a[104:107],v[180:183]\n"
"ds_write_b128 v116,v[152:155] offset:19456\n"
"v_mfma_f32_16x16x32_bf16 v[184:187],v[80:83],a[104:107],v[184:187]\n"
"ds_read_b128 a[0:3],v116 offset:16384\n"
"ds_read_b128 a[4:7],v116 offset:17408\n"
"v_mfma_f32_16x16x32_bf16 v[184:187],v[84:87],a[108:111],v[184:187]\n"
"ds_read_b128 a[8:11],v116 offset:18432\n"
"ds_read_b128 a[12:15],v116 offset:19456\n"
"v_mfma_f32_16x16x32_bf16 v[188:191],v[92:95],a[108:111],v[188:191]\n"
"v_mfma_f32_16x16x32_bf16 v[188:191],v[88:91],a[104:107],v[188:191]\n"
"v_mfma_f32_16x16x32_bf16 v[192:195],v[32:35],a[112:115],v[192:195]\n"
"v_mfma_f32_16x16x32_bf16 v[192:195],v[36:39],a[116:119],v[192:195]\n"
"v_cvt_pk_bf16_f32 v168,v168,v169\n"
"v_cvt_pk_bf16_f32 v169,v170,v171\n"
"v_mfma_f32_16x16x32_bf16 v[196:199],v[44:47],a[116:119],v[196:199]\n"
"v_cvt_pk_bf16_f32 v160,v160,v161\n"
"v_cvt_pk_bf16_f32 v161,v162,v163\n"
"v_mfma_f32_16x16x32_bf16 v[196:199],v[40:43],a[112:115],v[196:199]\n"
"v_cvt_pk_bf16_f32 v162,v164,v165\n"
"v_cvt_pk_bf16_f32 v163,v166,v167\n"
"v_mfma_f32_16x16x32_bf16 v[200:203],v[48:51],a[112:115],v[200:203]\n"
"ds_write_b128 v116,v[160:163] offset:49152\n"
"v_mfma_f32_16x16x32_bf16 v[200:203],v[52:55],a[116:119],v[200:203]\n"
"v_cvt_pk_bf16_f32 v170,v172,v173\n"
"v_cvt_pk_bf16_f32 v171,v174,v175\n"
"v_mfma_f32_16x16x32_bf16 v[204:207],v[60:63],a[116:119],v[204:207]\n"
"ds_write_b128 v116,v[168:171] offset:50176\n"
"v_mfma_f32_16x16x32_bf16 v[204:207],v[56:59],a[112:115],v[204:207]\n"
"v_cvt_pk_bf16_f32 v176,v176,v177\n"
"v_cvt_pk_bf16_f32 v177,v178,v179\n"
"v_mfma_f32_16x16x32_bf16 v[208:211],v[64:67],a[112:115],v[208:211]\n"
"v_cvt_pk_bf16_f32 v178,v180,v181\n"
"v_cvt_pk_bf16_f32 v179,v182,v183\n"
"v_mfma_f32_16x16x32_bf16 v[208:211],v[68:71],a[116:119],v[208:211]\n"
"ds_write_b128 v116,v[176:179] offset:51200\n"
"v_cvt_pk_bf16_f32 v184,v184,v185\n"
"v_mfma_f32_16x16x32_bf16 v[212:215],v[76:79],a[116:119],v[212:215]\n"
"v_cvt_pk_bf16_f32 v185,v186,v187\n"
"v_cvt_pk_bf16_f32 v186,v188,v189\n"
"v_cvt_pk_bf16_f32 v187,v190,v191\n"
"v_mfma_f32_16x16x32_bf16 v[212:215],v[72:75],a[112:115],v[212:215]\n"
"ds_write_b128 v116,v[184:187] offset:52224\n"
"v_mfma_f32_16x16x32_bf16 v[216:219],v[80:83],a[112:115],v[216:219]\n"
"ds_read_b128 a[16:19],v116 offset:49152\n"
"ds_read_b128 a[20:23],v116 offset:50176\n"
"v_mfma_f32_16x16x32_bf16 v[216:219],v[84:87],a[116:119],v[216:219]\n"
"ds_read_b128 a[24:27],v116 offset:51200\n"
"ds_read_b128 a[28:31],v116 offset:52224\n"
"v_mfma_f32_16x16x32_bf16 v[220:223],v[92:95],a[116:119],v[220:223]\n"
"s_lshl2_add_u32 s82,s60,s52\n"
"s_lshr_b32 s82,s82,1\n"
"s_cmp_eq_u32 s82,1\n"
"v_mfma_f32_16x16x32_bf16 v[220:223],v[88:91],a[112:115],v[220:223]\n"
"s_cbranch_scc1 .Lfk_head0\n"
"s_waitcnt lgkmcnt(0)\n"
"s_barrier\n"
"v_mfma_f32_16x16x32_bf16 v[24:27],a[64:67],a[48:51],v[108:111]\n"
"s_andn2_b32 m0,s55,16384\n"
"s_add_u32 s54,s54,s22\n"
"v_cvt_pk_bf16_f32 v200,v200,v201\n"
"v_mfma_f32_16x16x32_bf16 v[24:27],a[68:71],a[52:55],v[24:27]\n"
"buffer_load_dwordx4 v115,s[32:35],s54 offen sc1 lds\n"
"v_mfma_f32_16x16x32_bf16 v[24:27],a[72:75],a[56:59],v[24:27]\n"
"v_cvt_pk_bf16_f32 v192,v192,v193\n"
"v_cvt_pk_bf16_f32 v193,v194,v195\n"
"v_mfma_f32_16x16x32_bf16 v[24:27],a[76:79],a[60:63],v[24:27]\n"
"v_cvt_pk_bf16_f32 v194,v196,v197\n"
"v_cvt_pk_bf16_f32 v195,v198,v199\n"
"v_mfma_f32_16x16x32_bf16 v[28:31],a[92:95],a[60:63],v[108:111]\n"
"ds_write_b128 v116,v[192:195] offset:32768\n"
"v_mfma_f32_16x16x32_bf16 v[28:31],a[88:91],a[56:59],v[28:31]\n"
"v_cvt_pk_bf16_f32 v201,v202,v203\n"
"v_cvt_pk_bf16_f32 v202,v204,v205\n"
"v_cvt_pk_bf16_f32 v203,v206,v207\n"
"v_mfma_f32_16x16x32_bf16 v[28:31],a[84:87],a[52:55],v[28:31]\n"
"ds_write_b128 v116,v[200:203] offset:33792\n"
"v_mfma_f32_16x16x32_bf16 v[28:31],a[80:83],a[48:51],v[28:31]\n"
"v_cvt_pk_bf16_f32 v208,v208,v209\n"
"v_cvt_pk_bf16_f32 v209,v210,v211\n"
"v_mfma_f32_16x16x32_bf16 v[4:7],a[80:83],a[0:3],v[96:99]\n"
"v_cvt_pk_bf16_f32 v210,v212,v213\n"
"v_cvt_pk_bf16_f32 v211,v214,v215\n"
"v_mfma_f32_16x16x32_bf16 v[4:7],a[84:87],a[4:7],v[4:7]\n"
"ds_read_b128 a[96:99],v113 offset:16384\n"
"ds_read_b128 a[100:103],v113 offset:17408\n"
"v_mfma_f32_16x16x32_bf16 v[4:7],a[88:91],a[8:11],v[4:7]\n"
"ds_read_b128 a[104:107],v113 offset:18432\n"
"ds_read_b128 a[108:111],v113 offset:19456\n"
"v_mfma_f32_16x16x32_bf16 v[4:7],a[92:95],a[12:15],v[4:7]\n"
"ds_read_b128 a[112:115],v113 offset:20480\n"
"ds_read_b128 a[116:119],v113 offset:21504\n"
"v_mfma_f32_16x16x32_bf16 v[0:3],a[76:79],a[12:15],v[96:99]\n"
"ds_read_b128 a[120:123],v113 offset:22528\n"
"ds_read_b128 a[124:127],v113 offset:23552\n"
"v_mfma_f32_16x16x32_bf16 v[0:3],a[72:75],a[8:11],v[0:3]\n"
"v_max3_f32 v118,v24,v25,v26\n"
"v_max3_f32 v118,v118,v27,v28\n"
"v_mfma_f32_16x16x32_bf16 v[0:3],a[68:71],a[4:7],v[0:3]\n"
"v_max3_f32 v118,v118,v29,v30\n"
"v_max_f32 v118,v118,v31\n"
"v_mov_b32 v120,v118\n"
"v_mfma_f32_16x16x32_bf16 v[0:3],a[64:67],a[0:3],v[0:3]\n"
"ds_write_b128 v116,v[208:211] offset:34816\n"
"v_mfma_f32_16x16x32_bf16 v[8:11],a[64:67],a[16:19],v[100:103]\n"
"v_cvt_pk_bf16_f32 v216,v216,v217\n"
"v_cvt_pk_bf16_f32 v217,v218,v219\n"
"v_mfma_f32_16x16x32_bf16 v[8:11],a[68:71],a[20:23],v[8:11]\n"
"v_cvt_pk_bf16_f32 v218,v220,v221\n"
"v_cvt_pk_bf16_f32 v219,v222,v223\n"
"v_mfma_f32_16x16x32_bf16 v[8:11],a[72:75],a[24:27],v[8:11]\n"
"ds_write_b128 v116,v[216:219] offset:35840\n"
"ds_read_b128 a[32:35],v116 offset:32768\n"
"v_mfma_f32_16x16x32_bf16 v[8:11],a[76:79],a[28:31],v[8:11]\n"
"v_max3_f32 v119,v0,v1,v2\n"
"v_max3_f32 v119,v119,v3,v4\n"
"v_mfma_f32_16x16x32_bf16 v[12:15],a[92:95],a[28:31],v[100:103]\n"
"v_max3_f32 v119,v119,v5,v6\n"
"v_max_f32 v119,v119,v7\n"
"v_mfma_f32_16x16x32_bf16 v[12:15],a[88:91],a[24:27],v[12:15]\n"
"v_min_f32 v120,v120,v119\n"
"v_max_f32 v118,v118,v119\n"
"v_mfma_f32_16x16x32_bf16 v[12:15],a[84:87],a[20:23],v[12:15]\n"
"ds_read_b128 a[36:39],v116 offset:33792\n"
"ds_read_b128 a[40:43],v116 offset:34816\n"
"v_mfma_f32_16x16x32_bf16 v[12:15],a[80:83],a[16:19],v[12:15]\n"
"ds_read_b128 a[44:47],v116 offset:35840\n"
"s_waitcnt lgkmcnt(0)\n"
"v_mfma_f32_16x16x32_bf16 v[20:23],a[80:83],a[32:35],v[104:107]\n"
"v_mfma_f32_16x16x32_bf16 v[20:23],a[84:87],a[36:39],v[20:23]\n"
"v_mfma_f32_16x16x32_bf16 v[20:23],a[88:91],a[40:43],v[20:23]\n"
"buffer_store_dwordx4 a[224:227],v112,s[24:27],s56 offen offset:0 sc1\n"
"v_mfma_f32_16x16x32_bf16 v[20:23],a[92:95],a[44:47],v[20:23]\n"
"v_max3_f32 v119,v8,v9,v10\n"
"v_mfma_f32_16x16x32_bf16 v[16:19],a[76:79],a[44:47],v[104:107]\n"
"v_max3_f32 v119,v119,v11,v12\n"
"v_max3_f32 v119,v119,v13,v14\n"
"v_mfma_f32_16x16x32_bf16 v[16:19],a[72:75],a[40:43],v[16:19]\n"
"v_max_f32 v119,v119,v15\n"
"v_min_f32 v120,v120,v119\n"
"v_mfma_f32_16x16x32_bf16 v[16:19],a[68:71],a[36:39],v[16:19]\n"
"v_max_f32 v118,v118,v119\n"
"v_mfma_f32_16x16x32_bf16 v[16:19],a[64:67],a[32:35],v[16:19]\n"
"s_nop 7\n"
"v_max3_f32 v119,v16,v17,v18\n"
"v_max3_f32 v119,v119,v19,v20\n"
"v_max3_f32 v119,v119,v21,v22\n"
"v_max_f32 v119,v119,v23\n"
"v_min_f32 v120,v120,v119\n"
"v_max_f32 v118,v118,v119\n"
"v_cmp_gt_f32 s[96:97],s16,v120\n"
"v_cmp_lt_f32 vcc,s93,v118\n"
"s_nop 0\n"
"s_or_b64 s[96:97],s[96:97],vcc\n"
"s_cbranch_scc1 .Lfk_cs_h3\n"
".Lfk_cr_h3:\n"
"s_cmp_ge_u32 s52,2\n"
"s_cbranch_scc1 .Lfk_g0\n"
"s_waitcnt vmcnt(11) lgkmcnt(0)\n"
"s_barrier\n"
"v_mfma_f32_16x16x32_bf16 v[32:35],a[96:99],a[0:3],v[96:99]\n"
"s_add_u32 m0,s55,4096\n"
"v_exp_f32 v0,v0\n"
"v_mfma_f32_16x16x32_bf16 v[32:35],a[100:103],a[4:7],v[32:35]\n"
"s_add_u32 s54,s54,s22\n"
"v_exp_f32 v1,v1\n"
"v_mfma_f32_16x16x32_bf16 v[32:35],a[104:107],a[8:11],v[32:35]\n"
"buffer_load_dwordx4 v115,s[32:35],s54 offen sc1 lds\n"
"v_exp_f32 v2,v2\n"
"v_cvt_pk_bf16_f32 v64,v0,v1\n"
"v_mfma_f32_16x16x32_bf16 v[32:35],a[108:111],a[12:15],v[32:35]\n"
"s_add_u32 m0,s67,16384\n"
"v_exp_f32 v3,v3\n"
"v_mfma_f32_16x16x32_bf16 v[36:39],a[124:127],a[12:15],v[96:99]\n"
"v_exp_f32 v4,v4\n"
"v_cvt_pk_bf16_f32 v65,v2,v3\n"
"v_exp_f32 v5,v5\n"
"v_mfma_f32_16x16x32_bf16 v[36:39],a[120:123],a[8:11],v[36:39]\n"
"buffer_load_dwordx4 v112,s[24:27],s64 offen offset:0 sc1 lds\n"
"v_mfma_f32_16x16x32_bf16 v[36:39],a[116:119],a[4:7],v[36:39]\n"
"v_exp_f32 v6,v6\n"
"v_cvt_pk_bf16_f32 v66,v4,v5\n"
"v_exp_f32 v7,v7\n"
"v_mfma_f32_16x16x32_bf16 v[36:39],a[112:115],a[0:3],v[36:39]\n"
"buffer_load_dwordx4 v112,s[24:27],s64 offen offset:1024 sc1 lds\n"
"v_mfma_f32_16x16x32_bf16 v[44:47],a[112:115],a[16:19],v[100:103]\n"
"v_exp_f32 v8,v8\n"
"v_cvt_pk_bf16_f32 v67,v6,v7\n"
"v_exp_f32 v9,v9\n"
"v_mfma_f32_16x16x32_bf16 v[44:47],a[116:119],a[20:23],v[44:47]\n"
"ds_read_b128 a[64:67],v113 offset:49152\n"
"ds_read_b128 a[68:71],v113 offset:50176\n"
"v_exp_f32 v10,v10\n"
"v_mfma_f32_16x16x32_bf16 v[44:47],a[120:123],a[24:27],v[44:47]\n"
"ds_read_b128 a[72:75],v113 offset:51200\n"
"ds_read_b128 a[76:79],v113 offset:52224\n"
"v_exp_f32 v11,v11\n"
"v_mfma_f32_16x16x32_bf16 v[44:47],a[124:127],a[28:31],v[44:47]\n"
"ds_read_b128 a[80:83],v113 offset:53248\n"
"ds_read_b128 a[84:87],v113 offset:54272\n"
"v_exp_f32 v12,v12\n"
"v_mfma_f32_16x16x32_bf16 v[40:43],a[108:111],a[28:31],v[100:103]\n"
"ds_read_b128 a[88:91],v113 offset:55296\n"
"ds_read_b128 a[92:95],v113 offset:56320\n"
"v_exp_f32 v13,v13\n"
"v_mfma_f32_16x16x32_bf16 v[40:43],a[104:107],a[24:27],v[40:43]\n"
"buffer_load_dwordx4 v112,s[24:27],s64 offen offset:2048 sc1 lds\n"
"v_exp_f32 v14,v14\n"
"v_mfma_f32_16x16x32_bf16 v[40:43],a[100:103],a[20:23],v[40:43]\n"
"buffer_load_dwordx4 v112,s[24:27],s64 offen offset:3072 sc1 lds\n"
"v_mfma_f32_16x16x32_bf16 v[40:43],a[96:99],a[16:19],v[40:43]\n"
"s_add_u32 m0,s67,20480\n"
"v_exp_f32 v15,v15\n"
"v_mfma_f32_16x16x32_bf16 v[48:51],a[96:99],a[32:35],v[104:107]\n"
"buffer_load_dwordx4 v117,s[24:27],s64 offen offset:0 sc1 lds\n"
"buffer_load_dwordx4 v117,s[24:27],s64 offen offset:1024 sc1 lds\n"
"v_exp_f32 v16,v16\n"
"v_exp_f32 v17,v17\n"
"v_mfma_f32_16x16x32_bf16 v[48:51],a[100:103],a[36:39],v[48:51]\n"
"buffer_load_dwordx4 v117,s[24:27],s64 offen offset:2048 sc1 lds\n"
"v_mfma_f32_16x16x32_bf16 v[48:51],a[104:107],a[40:43],v[48:51]\n"
"v_exp_f32 v18,v18\n"
"v_cvt_pk_bf16_f32 v68,v8,v9\n"
"v_cvt_pk_bf16_f32 v69,v10,v11\n"
"v_exp_f32 v19,v19\n"
"v_mfma_f32_16x16x32_bf16 v[48:51],a[108:111],a[44:47],v[48:51]\n"
"buffer_load_dwordx4 v117,s[24:27],s64 offen offset:3072 sc1 lds\n"
"v_mfma_f32_16x16x32_bf16 v[52:55],a[124:127],a[44:47],v[104:107]\n"
"s_add_u32 s64,s64,16384\n"
"v_exp_f32 v20,v20\n"
"v_cvt_pk_bf16_f32 v70,v12,v13\n"
"v_cvt_pk_bf16_f32 v71,v14,v15\n"
"v_mfma_f32_16x16x32_bf16 v[52:55],a[120:123],a[40:43],v[52:55]\n"
"v_exp_f32 v21,v21\n"
"v_cvt_pk_bf16_f32 v72,v16,v17\n"
"v_cvt_pk_bf16_f32 v73,v18,v19\n"
"v_mfma_f32_16x16x32_bf16 v[52:55],a[116:119],a[36:39],v[52:55]\n"
"v_exp_f32 v22,v22\n"
"v_cvt_pk_bf16_f32 v74,v20,v21\n"
"v_mfma_f32_16x16x32_bf16 v[52:55],a[112:115],a[32:35],v[52:55]\n"
"v_exp_f32 v23,v23\n"
"v_mfma_f32_16x16x32_bf16 v[60:63],a[112:115],a[48:51],v[108:111]\n"
"v_exp_f32 v24,v24\n"
"v_cvt_pk_bf16_f32 v75,v22,v23\n"
"v_mfma_f32_16x16x32_bf16 v[60:63],a[116:119],a[52:55],v[60:63]\n"
"v_exp_f32 v25,v25\n"
"v_mfma_f32_16x16x32_bf16 v[60:63],a[120:123],a[56:59],v[60:63]\n"
"v_exp_f32 v26,v26\n"
"v_exp_f32 v27,v27\n"
"v_mfma_f32_16x16x32_bf16 v[60:63],a[124:127],a[60:63],v[60:63]\n"
"buffer_store_dwordx4 a[228:231],v112,s[24:27],s56 offen offset:1024 sc1\n"
"v_mfma_f32_16x16x32_bf16 v[56:59],a[108:111],a[60:63],v[108:111]\n"
"v_exp_f32 v28,v28\n"
"v_cvt_pk_bf16_f32 v77,v26,v27\n"
"v_mfma_f32_16x16x32_bf16 v[56:59],a[104:107],a[56:59],v[56:59]\n"
"v_exp_f32 v29,v29\n"
"v_mfma_f32_16x16x32_bf16 v[56:59],a[100:103],a[52:55],v[56:59]\n"
"v_exp_f32 v30,v30\n"
"v_cvt_pk_bf16_f32 v78,v28,v29\n"
"v_mfma_f32_16x16x32_bf16 v[56:59],a[96:99],a[48:51],v[56:59]\n"
"s_waitcnt vmcnt(17) lgkmcnt(0)\n"
"v_exp_f32 v31,v31\n"
"v_cvt_pk_bf16_f32 v76,v24,v25\n"
"v_cvt_pk_bf16_f32 v79,v30,v31\n"
"s_barrier\n"
"v_mfma_f32_16x16x32_bf16 v[0:3],a[64:67],a[0:3],v[96:99]\n"
"s_add_u32 m0,s55,8192\n"
"v_exp_f32 v122,v32\n"
"v_mfma_f32_16x16x32_bf16 v[0:3],a[68:71],a[4:7],v[0:3]\n"
"s_add_u32 s54,s54,s22\n"
"v_exp_f32 v123,v33\n"
"v_mfma_f32_16x16x32_bf16 v[0:3],a[72:75],a[8:11],v[0:3]\n"
"ds_read_b128 a[96:99],v114 offset:40960\n"
"ds_read_b128 a[100:103],v114 offset:41984\n"
"v_mfma_f32_16x16x32_bf16 v[0:3],a[76:79],a[12:15],v[0:3]\n"
"v_exp_f32 v119,v34\n"
"v_mfma_f32_16x16x32_bf16 v[4:7],a[92:95],a[12:15],v[96:99]\n"
"ds_read_b128 a[104:107],v114 offset:43008\n"
"v_cvt_pk_bf16_f32 v80,v122,v123\n"
"v_mfma_f32_16x16x32_bf16 v[4:7],a[88:91],a[8:11],v[4:7]\n"
"v_exp_f32 v120,v35\n"
"v_mfma_f32_16x16x32_bf16 v[4:7],a[84:87],a[4:7],v[4:7]\n"
"ds_read_b128 a[108:111],v114 offset:44032\n"
"ds_read_b128 a[112:115],v114 offset:45056\n"
"v_mfma_f32_16x16x32_bf16 v[4:7],a[80:83],a[0:3],v[4:7]\n"
"v_exp_f32 v122,v36\n"
"v_mfma_f32_16x16x32_bf16 v[12:15],a[80:83],a[16:19],v[100:103]\n"
"ds_read_b128 a[116:119],v114 offset:46080\n"
"v_cvt_pk_bf16_f32 v81,v119,v120\n"
"v_mfma_f32_16x16x32_bf16 v[12:15],a[84:87],a[20:23],v[12:15]\n"
"v_exp_f32 v123,v37\n"
"v_mfma_f32_16x16x32_bf16 v[12:15],a[88:91],a[24:27],v[12:15]\n"
"ds_read_b128 a[120:123],v114 offset:47104\n"
"ds_read_b128 a[124:127],v114 offset:48128\n"
"v_mfma_f32_16x16x32_bf16 v[12:15],a[92:95],a[28:31],v[12:15]\n"
"v_exp_f32 v119,v38\n"
"v_cvt_pk_bf16_f32 v82,v122,v123\n"
"v_mfma_f32_16x16x32_bf16 v[8:11],a[76:79],a[28:31],v[100:103]\n"
"buffer_store_dwordx4 a[168:171],v112,s[24:27],s3 offen offset:2048 sc1\n"
"v_mfma_f32_16x16x32_bf16 v[8:11],a[72:75],a[24:27],v[8:11]\n"
"v_exp_f32 v120,v39\n"
"v_mfma_f32_16x16x32_bf16 v[8:11],a[68:71],a[20:23],v[8:11]\n"
"buffer_load_dwordx4 v115,s[32:35],s54 offen sc1 lds\n"
"v_mfma_f32_16x16x32_bf16 v[8:11],a[64:67],a[16:19],v[8:11]\n"
"s_add_u32 m0,s67,49152\n"
"v_exp_f32 v122,v40\n"
"v_mfma_f32_16x16x32_bf16 v[16:19],a[64:67],a[32:35],v[104:107]\n"
"v_cvt_pk_bf16_f32 v83,v119,v120\n"
"v_or3_b32 v118,v80,v81,v82\n"
"v_mfma_f32_16x16x32_bf16 v[16:19],a[68:71],a[36:39],v[16:19]\n"
"v_exp_f32 v123,v41\n"
"v_mfma_f32_16x16x32_bf16 v[16:19],a[72:75],a[40:43],v[16:19]\n"
"buffer_load_dwordx4 v112,s[24:27],s64 offen offset:0 sc1 lds\n"
"v_mfma_f32_16x16x32_bf16 v[16:19],a[76:79],a[44:47],v[16:19]\n"
"v_exp_f32 v119,v42\n"
"v_mfma_f32_16x16x32_bf16 v[20:23],a[92:95],a[44:47],v[104:107]\n"
"v_cvt_pk_bf16_f32 v84,v122,v123\n"
"v_or3_b32 v118,v118,v83,v84\n"
"v_mfma_f32_16x16x32_bf16 v[20:23],a[88:91],a[40:43],v[20:23]\n"
"v_exp_f32 v120,v43\n"
"v_mfma_f32_16x16x32_bf16 v[20:23],a[84:87],a[36:39],v[20:23]\n"
"buffer_load_dwordx4 v112,s[24:27],s64 offen offset:1024 sc1 lds\n"
"v_mfma_f32_16x16x32_bf16 v[20:23],a[80:83],a[32:35],v[20:23]\n"
"v_exp_f32 v122,v44\n"
"v_mfma_f32_16x16x32_bf16 v[28:31],a[80:83],a[48:51],v[108:111]\n"
"v_cvt_pk_bf16_f32 v85,v119,v120\n"
"v_mfma_f32_16x16x32_bf16 v[28:31],a[84:87],a[52:55],v[28:31]\n"
"v_exp_f32 v123,v45\n"
"v_mfma_f32_16x16x32_bf16 v[28:31],a[88:91],a[56:59],v[28:31]\n"
"buffer_load_dwordx4 v112,s[24:27],s64 offen offset:2048 sc1 lds\n"
"v_mfma_f32_16x16x32_bf16 v[28:31],a[92:95],a[60:63],v[28:31]\n"
"v_exp_f32 v119,v46\n"
"v_cvt_pk_bf16_f32 v86,v122,v123\n"
"v_mfma_f32_16x16x32_bf16 v[24:27],a[76:79],a[60:63],v[108:111]\n"
"buffer_store_dwordx4 a[172:175],v112,s[24:27],s3 offen offset:3072 sc1\n"
"v_mfma_f32_16x16x32_bf16 v[24:27],a[72:75],a[56:59],v[24:27]\n"
"v_exp_f32 v120,v47\n"
"v_mfma_f32_16x16x32_bf16 v[24:27],a[68:71],a[52:55],v[24:27]\n"
"buffer_load_dwordx4 v112,s[24:27],s64 offen offset:3072 sc1 lds\n"
"v_mfma_f32_16x16x32_bf16 v[24:27],a[64:67],a[48:51],v[24:27]\n"
"s_waitcnt lgkmcnt(0)\n"
"v_exp_f32 v122,v48\n"
"v_mfma_f32_16x16x32_bf16 v[128:131],a[96:99],v[64:67],0\n"
"v_exp_f32 v123,v49\n"
"v_mfma_f32_16x16x32_bf16 v[160:163],a[96:99],v[68:71],0\n"
"ds_read_b128 a[64:67],v113 offset:32768\n"
"v_cvt_pk_bf16_f32 v87,v119,v120\n"
"v_mfma_f32_16x16x32_bf16 v[192:195],a[96:99],v[72:75],0\n"
"s_add_u32 m0,s67,53248\n"
"v_exp_f32 v119,v50\n"
"v_mfma_f32_16x16x32_bf16 v[224:227],a[96:99],v[76:79],0\n"
"ds_read_b128 a[68:71],v113 offset:33792\n"
"v_cvt_pk_bf16_f32 v88,v122,v123\n"
"v_mfma_f32_16x16x32_bf16 a[140:143],a[144:147],v[76:79],0\n"
"v_exp_f32 v120,v51\n"
"v_mfma_f32_16x16x32_bf16 v[228:231],a[100:103],v[76:79],0\n"
"ds_read_b128 a[72:75],v113 offset:34816\n"
"ds_read_b128 a[76:79],v113 offset:35840\n"
"v_mfma_f32_16x16x32_bf16 v[132:135],a[100:103],v[64:67],0\n"
"v_exp_f32 v122,v52\n"
"v_mfma_f32_16x16x32_bf16 v[164:167],a[100:103],v[68:71],0\n"
"ds_read_b128 a[80:83],v113 offset:36864\n"
"v_cvt_pk_bf16_f32 v89,v119,v120\n"
"v_mfma_f32_16x16x32_bf16 v[196:199],a[100:103],v[72:75],0\n"
"v_exp_f32 v123,v53\n"
"v_mfma_f32_16x16x32_bf16 a[136:139],a[144:147],v[72:75],0\n"
"ds_read_b128 a[84:87],v113 offset:37888\n"
"ds_read_b128 a[88:91],v113 offset:38912\n"
"v_mfma_f32_16x16x32_bf16 v[200:203],a[104:107],v[72:75],0\n"
"v_exp_f32 v119,v54\n"
"v_mfma_f32_16x16x32_bf16 v[232:235],a[104:107],v[76:79],0\n"
"ds_read_b128 a[92:95],v113 offset:39936\n"
"v_cvt_pk_bf16_f32 v90,v122,v123\n"
"v_mfma_f32_16x16x32_bf16 v[136:139],a[104:107],v[64:67],0\n"
"v_exp_f32 v120,v55\n"
"v_mfma_f32_16x16x32_bf16 v[168:171],a[104:107],v[68:71],0\n"
"buffer_load_dwordx4 v117,s[24:27],s64 offen offset:0 sc1 lds\n"
"v_mfma_f32_16x16x32_bf16 a[132:135],a[144:147],v[68:71],0\n"
"v_exp_f32 v122,v56\n"
"v_mfma_f32_16x16x32_bf16 v[172:175],a[108:111],v[68:71],0\n"
"v_cvt_pk_bf16_f32 v91,v119,v120\n"
"s_sub_u32 s3,s3,16384\n"
"v_mfma_f32_16x16x32_bf16 v[204:207],a[108:111],v[72:75],0\n"
"v_exp_f32 v123,v57\n"
"v_mfma_f32_16x16x32_bf16 v[236:239],a[108:111],v[76:79],0\n"
"buffer_load_dwordx4 v117,s[24:27],s64 offen offset:1024 sc1 lds\n"
"v_mfma_f32_16x16x32_bf16 v[140:143],a[108:111],v[64:67],0\n"
"v_exp_f32 v119,v58\n"
"v_cvt_pk_bf16_f32 v92,v122,v123\n"
"v_mfma_f32_16x16x32_bf16 a[128:131],a[144:147],v[64:67],0\n"
"buffer_store_dwordx4 a[176:179],v112,s[24:27],s3 offen offset:0 sc1\n"
"v_mfma_f32_16x16x32_bf16 v[144:147],a[112:115],v[64:67],0\n"
"v_exp_f32 v120,v59\n"
"v_mfma_f32_16x16x32_bf16 v[176:179],a[112:115],v[68:71],0\n"
"buffer_load_dwordx4 v117,s[24:27],s64 offen offset:2048 sc1 lds\n"
"v_mfma_f32_16x16x32_bf16 v[208:211],a[112:115],v[72:75],0\n"
"v_exp_f32 v122,v60\n"
"v_mfma_f32_16x16x32_bf16 v[240:243],a[112:115],v[76:79],0\n"
"v_cvt_pk_bf16_f32 v93,v119,v120\n"
"v_or3_b32 v118,v118,v85,v86\n"
"v_mfma_f32_16x16x32_bf16 v[244:247],a[116:119],v[76:79],0\n"
"v_exp_f32 v123,v61\n"
"v_mfma_f32_16x16x32_bf16 v[148:151],a[116:119],v[64:67],0\n"
"buffer_load_dwordx4 v117,s[24:27],s64 offen offset:3072 sc1 lds\n"
"v_mfma_f32_16x16x32_bf16 v[180:183],a[116:119],v[68:71],0\n"
"s_add_u32 s64,s64,16384\n"
"v_exp_f32 v119,v62\n"
"v_cvt_pk_bf16_f32 v94,v122,v123\n"
"v_mfma_f32_16x16x32_bf16 v[212:215],a[116:119],v[72:75],0\n"
"buffer_store_dwordx4 a[232:235],v112,s[24:27],s56 offen offset:2048 sc1\n"
"v_mfma_f32_16x16x32_bf16 v[216:219],a[120:123],v[72:75],0\n"
"v_exp_f32 v120,v63\n"
"v_mfma_f32_16x16x32_bf16 v[248:251],a[120:123],v[76:79],0\n"
"v_cvt_pk_bf16_f32 v95,v119,v120\n"
"v_or3_b32 v118,v118,v87,v88\n"
"v_mfma_f32_16x16x32_bf16 v[152:155],a[120:123],v[64:67],0\n"
"v_or3_b32 v118,v118,v89,v90\n"
"v_or3_b32 v118,v118,v91,v92\n"
"v_mfma_f32_16x16x32_bf16 v[184:187],a[120:123],v[68:71],0\n"
"v_or3_b32 v118,v118,v93,v94\n"
"v_or_b32 v118,v118,v95\n"
"v_mfma_f32_16x16x32_bf16 v[188:191],a[124:127],v[68:71],0\n"
"v_and_b32 v118,0x40004000,v118\n"
"v_cmp_ne_u32 vcc,0,v118\n"
"v_mfma_f32_16x16x32_bf16 v[220:223],a[124:127],v[72:75],0\n"
"v_mfma_f32_16x16x32_bf16 v[252:255],a[124:127],v[76:79],0\n"
"v_mfma_f32_16x16x32_bf16 v[156:159],a[124:127],v[64:67],0\n"
"s_cbranch_vccnz .Lfk_cs_h1\n"
".Lfk_cr_h1:\n"
"s_waitcnt lgkmcnt(0)\n"
"s_barrier\n"
"v_mfma_f32_16x16x32_bf16 v[32:35],a[64:67],a[0:3],v[96:99]\n"
"s_add_u32 m0,s55,12288\n"
"v_exp_f32 v122,v0\n"
"v_mfma_f32_16x16x32_bf16 v[32:35],a[68:71],a[4:7],v[32:35]\n"
"s_add_u32 s54,s54,s22\n"
"v_exp_f32 v123,v1\n"
"v_mfma_f32_16x16x32_bf16 v[32:35],a[72:75],a[8:11],v[32:35]\n"
"ds_read_b128 a[96:99],v114 offset:8192\n"
"ds_read_b128 a[100:103],v114 offset:9216\n"
"v_mfma_f32_16x16x32_bf16 v[32:35],a[76:79],a[12:15],v[32:35]\n"
"v_exp_f32 v119,v2\n"
"v_mfma_f32_16x16x32_bf16 v[36:39],a[92:95],a[12:15],v[96:99]\n"
"ds_read_b128 a[104:107],v114 offset:10240\n"
"v_cvt_pk_bf16_f32 v64,v122,v123\n"
"v_mfma_f32_16x16x32_bf16 v[36:39],a[88:91],a[8:11],v[36:39]\n"
"v_exp_f32 v120,v3\n"
"v_mfma_f32_16x16x32_bf16 v[36:39],a[84:87],a[4:7],v[36:39]\n"
"ds_read_b128 a[108:111],v114 offset:11264\n"
"ds_read_b128 a[112:115],v114 offset:12288\n"
"v_mfma_f32_16x16x32_bf16 v[36:39],a[80:83],a[0:3],v[36:39]\n"
"v_exp_f32 v122,v4\n"
"v_mfma_f32_16x16x32_bf16 v[44:47],a[80:83],a[16:19],v[100:103]\n"
"ds_read_b128 a[116:119],v114 offset:13312\n"
"v_cvt_pk_bf16_f32 v65,v119,v120\n"
"v_mfma_f32_16x16x32_bf16 v[44:47],a[84:87],a[20:23],v[44:47]\n"
"v_exp_f32 v123,v5\n"
"v_mfma_f32_16x16x32_bf16 v[44:47],a[88:91],a[24:27],v[44:47]\n"
"ds_read_b128 a[120:123],v114 offset:14336\n"
"ds_read_b128 a[124:127],v114 offset:15360\n"
"v_mfma_f32_16x16x32_bf16 v[44:47],a[92:95],a[28:31],v[44:47]\n"
"v_exp_f32 v119,v6\n"
"v_cvt_pk_bf16_f32 v66,v122,v123\n"
"v_mfma_f32_16x16x32_bf16 v[40:43],a[76:79],a[28:31],v[100:103]\n"
"buffer_store_dwordx4 a[180:183],v112,s[24:27],s3 offen offset:1024 sc1\n"
"v_mfma_f32_16x16x32_bf16 v[40:43],a[72:75],a[24:27],v[40:43]\n"
"v_exp_f32 v120,v7\n"
"v_mfma_f32_16x16x32_bf16 v[40:43],a[68:71],a[20:23],v[40:43]\n"
"buffer_load_dwordx4 v115,s[32:35],s54 offen sc1 lds\n"
"v_mfma_f32_16x16x32_bf16 v[40:43],a[64:67],a[16:19],v[40:43]\n"
"s_add_u32 m0,s67,32768\n"
"v_exp_f32 v122,v8\n"
"v_mfma_f32_16x16x32_bf16 v[48:51],a[64:67],a[32:35],v[104:107]\n"
"v_cvt_pk_bf16_f32 v67,v119,v120\n"
"v_or3_b32 v118,v64,v65,v66\n"
"v_mfma_f32_16x16x32_bf16 v[48:51],a[68:71],a[36:39],v[48:51]\n"
"v_exp_f32 v123,v9\n"
"v_mfma_f32_16x16x32_bf16 v[48:51],a[72:75],a[40:43],v[48:51]\n"
"buffer_load_dwordx4 v112,s[24:27],s64 offen offset:0 sc1 lds\n"
"v_mfma_f32_16x16x32_bf16 v[48:51],a[76:79],a[44:47],v[48:51]\n"
"v_exp_f32 v119,v10\n"
"v_mfma_f32_16x16x32_bf16 v[52:55],a[92:95],a[44:47],v[104:107]\n"
"v_cvt_pk_bf16_f32 v68,v122,v123\n"
"v_or3_b32 v118,v118,v67,v68\n"
"v_mfma_f32_16x16x32_bf16 v[52:55],a[88:91],a[40:43],v[52:55]\n"
"v_exp_f32 v120,v11\n"
"v_mfma_f32_16x16x32_bf16 v[52:55],a[84:87],a[36:39],v[52:55]\n"
"buffer_load_dwordx4 v112,s[24:27],s64 offen offset:1024 sc1 lds\n"
"v_mfma_f32_16x16x32_bf16 v[52:55],a[80:83],a[32:35],v[52:55]\n"
"v_exp_f32 v122,v12\n"
"v_mfma_f32_16x16x32_bf16 v[60:63],a[80:83],a[48:51],v[108:111]\n"
"v_cvt_pk_bf16_f32 v69,v119,v120\n"
"v_mfma_f32_16x16x32_bf16 v[60:63],a[84:87],a[52:55],v[60:63]\n"
"v_exp_f32 v123,v13\n"
"v_mfma_f32_16x16x32_bf16 v[60:63],a[88:91],a[56:59],v[60:63]\n"
"buffer_load_dwordx4 v112,s[24:27],s64 offen offset:2048 sc1 lds\n"
"v_mfma_f32_16x16x32_bf16 v[60:63],a[92:95],a[60:63],v[60:63]\n"
"v_exp_f32 v119,v14\n"
"v_cvt_pk_bf16_f32 v70,v122,v123\n"
"v_mfma_f32_16x16x32_bf16 v[56:59],a[76:79],a[60:63],v[108:111]\n"
"buffer_store_dwordx4 a[184:187],v112,s[24:27],s3 offen offset:2048 sc1\n"
"v_mfma_f32_16x16x32_bf16 v[56:59],a[72:75],a[56:59],v[56:59]\n"
"v_exp_f32 v120,v15\n"
"v_mfma_f32_16x16x32_bf16 v[56:59],a[68:71],a[52:55],v[56:59]\n"
"buffer_load_dwordx4 v112,s[24:27],s64 offen offset:3072 sc1 lds\n"
"v_mfma_f32_16x16x32_bf16 v[56:59],a[64:67],a[48:51],v[56:59]\n"
"s_waitcnt lgkmcnt(0)\n"
"v_exp_f32 v122,v16\n"
"v_mfma_f32_16x16x32_bf16 v[128:131],a[96:99],v[80:83],v[128:131]\n"
"v_exp_f32 v123,v17\n"
"v_mfma_f32_16x16x32_bf16 v[160:163],a[96:99],v[84:87],v[160:163]\n"
"ds_read_b128 a[64:67],v113 offset:0\n"
"v_cvt_pk_bf16_f32 v71,v119,v120\n"
"v_mfma_f32_16x16x32_bf16 v[192:195],a[96:99],v[88:91],v[192:195]\n"
"s_add_u32 m0,s67,36864\n"
"v_exp_f32 v119,v18\n"
"v_mfma_f32_16x16x32_bf16 v[224:227],a[96:99],v[92:95],v[224:227]\n"
"ds_read_b128 a[68:71],v113 offset:1024\n"
"v_cvt_pk_bf16_f32 v72,v122,v123\n"
"v_mfma_f32_16x16x32_bf16 a[140:143],a[144:147],v[92:95],a[140:143]\n"
"v_exp_f32 v120,v19\n"
"v_mfma_f32_16x16x32_bf16 v[228:231],a[100:103],v[92:95],v[228:231]\n"
"ds_read_b128 a[72:75],v113 offset:2048\n"
"ds_read_b128 a[76:79],v113 offset:3072\n"
"v_mfma_f32_16x16x32_bf16 v[132:135],a[100:103],v[80:83],v[132:135]\n"
"v_exp_f32 v122,v20\n"
"v_mfma_f32_16x16x32_bf16 v[164:167],a[100:103],v[84:87],v[164:167]\n"
"ds_read_b128 a[80:83],v113 offset:4096\n"
"v_cvt_pk_bf16_f32 v73,v119,v120\n"
"v_mfma_f32_16x16x32_bf16 v[196:199],a[100:103],v[88:91],v[196:199]\n"
"v_exp_f32 v123,v21\n"
"v_mfma_f32_16x16x32_bf16 a[136:139],a[144:147],v[88:91],a[136:139]\n"
"ds_read_b128 a[84:87],v113 offset:5120\n"
"ds_read_b128 a[88:91],v113 offset:6144\n"
"v_mfma_f32_16x16x32_bf16 v[200:203],a[104:107],v[88:91],v[200:203]\n"
"v_exp_f32 v119,v22\n"
"v_mfma_f32_16x16x32_bf16 v[232:235],a[104:107],v[92:95],v[232:235]\n"
"ds_read_b128 a[92:95],v113 offset:7168\n"
"v_cvt_pk_bf16_f32 v74,v122,v123\n"
"v_mfma_f32_16x16x32_bf16 v[136:139],a[104:107],v[80:83],v[136:139]\n"
"v_exp_f32 v120,v23\n"
"v_mfma_f32_16x16x32_bf16 v[168:171],a[104:107],v[84:87],v[168:171]\n"
"buffer_load_dwordx4 v117,s[24:27],s64 offen offset:0 sc1 lds\n"
"v_mfma_f32_16x16x32_bf16 a[132:135],a[144:147],v[84:87],a[132:135]\n"
"v_exp_f32 v122,v24\n"
"v_cvt_pk_bf16_f32 v75,v119,v120\n"
"v_mfma_f32_16x16x32_bf16 v[172:175],a[108:111],v[84:87],v[172:175]\n"
"buffer_store_dwordx4 a[188:191],v112,s[24:27],s3 offen offset:3072 sc1\n"
"v_mfma_f32_16x16x32_bf16 v[204:207],a[108:111],v[88:91],v[204:207]\n"
"v_exp_f32 v123,v25\n"
"v_mfma_f32_16x16x32_bf16 v[236:239],a[108:111],v[92:95],v[236:239]\n"
"buffer_load_dwordx4 v117,s[24:27],s64 offen offset:1024 sc1 lds\n"
"v_mfma_f32_16x16x32_bf16 v[140:143],a[108:111],v[80:83],v[140:143]\n"
"v_exp_f32 v119,v26\n"
"v_mfma_f32_16x16x32_bf16 a[128:131],a[144:147],v[80:83],a[128:131]\n"
"v_cvt_pk_bf16_f32 v76,v122,v123\n"
"s_or_b32 s3,s3,0x3c000\n"
"v_mfma_f32_16x16x32_bf16 v[144:147],a[112:115],v[80:83],v[144:147]\n"
"v_exp_f32 v120,v27\n"
"v_mfma_f32_16x16x32_bf16 v[176:179],a[112:115],v[84:87],v[176:179]\n"
"buffer_load_dwordx4 v117,s[24:27],s64 offen offset:2048 sc1 lds\n"
"v_mfma_f32_16x16x32_bf16 v[208:211],a[112:115],v[88:91],v[208:211]\n"
"v_exp_f32 v122,v28\n"
"v_mfma_f32_16x16x32_bf16 v[240:243],a[112:115],v[92:95],v[240:243]\n"
"v_cvt_pk_bf16_f32 v77,v119,v120\n"
"v_or3_b32 v118,v118,v69,v70\n"
"v_mfma_f32_16x16x32_bf16 v[244:247],a[116:119],v[92:95],v[244:247]\n"
"v_exp_f32 v123,v29\n"
"v_mfma_f32_16x16x32_bf16 v[148:151],a[116:119],v[80:83],v[148:151]\n"
"buffer_load_dwordx4 v117,s[24:27],s64 offen offset:3072 sc1 lds\n"
"v_mfma_f32_16x16x32_bf16 v[180:183],a[116:119],v[84:87],v[180:183]\n"
"s_add_u32 s64,s64,16384\n"
"v_exp_f32 v119,v30\n"
"v_cvt_pk_bf16_f32 v78,v122,v123\n"
"v_mfma_f32_16x16x32_bf16 v[212:215],a[116:119],v[88:91],v[212:215]\n"
"buffer_store_dwordx4 a[236:239],v112,s[24:27],s56 offen offset:3072 sc1\n"
"v_mfma_f32_16x16x32_bf16 v[216:219],a[120:123],v[88:91],v[216:219]\n"
"v_exp_f32 v120,v31\n"
"v_mfma_f32_16x16x32_bf16 v[248:251],a[120:123],v[92:95],v[248:251]\n"
"v_cvt_pk_bf16_f32 v79,v119,v120\n"
"v_or3_b32 v118,v118,v71,v72\n"
"v_mfma_f32_16x16x32_bf16 v[152:155],a[120:123],v[80:83],v[152:155]\n"
"v_or3_b32 v118,v118,v73,v74\n"
"v_or3_b32 v118,v118,v75,v76\n"
"v_mfma_f32_16x16x32_bf16 v[184:187],a[120:123],v[84:87],v[184:187]\n"
"v_or3_b32 v118,v118,v77,v78\n"
"v_or_b32 v118,v118,v79\n"
"v_mfma_f32_16x16x32_bf16 v[188:191],a[124:127],v[84:87],v[188:191]\n"
"v_and_b32 v118,0x40004000,v118\n"
"v_cmp_ne_u32 vcc,0,v118\n"
"v_mfma_f32_16x16x32_bf16 v[220:223],a[124:127],v[88:91],v[220:223]\n"
"v_mfma_f32_16x16x32_bf16 v[252:255],a[124:127],v[92:95],v[252:255]\n"
"v_mfma_f32_16x16x32_bf16 v[156:159],a[124:127],v[80:83],v[156:159]\n"
"s_cbranch_vccnz .Lfk_cs_h2\n"
".Lfk_cr_h2:\n"
"s_waitcnt vmcnt(27) lgkmcnt(0)\n"
"s_barrier\n"
".Lfk_g1:\n"
"s_and_b32 s82,s78,4096\n"
"s_lshl1_add_u32 s82,s82,69632\n"
"s_cmp_lt_u32 s52,2\n"
"s_cselect_b32 s64,s64,s62\n"
"s_cselect_b32 s67,s67,s82\n"
"s_add_u32 s89,s89,1\n"
"s_cmp_ge_u32 s89,s60\n"
"s_cbranch_scc1 .Lfk_Fx\n"
".Lfk_F:\n"
"v_mfma_f32_16x16x32_bf16 v[0:3],a[64:67],a[0:3],v[96:99]\n"
"s_swappc_b64 s[94:95],s[20:21]\n"
"v_exp_f32 v122,v32\n"
"v_mfma_f32_16x16x32_bf16 v[0:3],a[68:71],a[4:7],v[0:3]\n"
"v_exp_f32 v123,v33\n"
"v_mfma_f32_16x16x32_bf16 v[0:3],a[72:75],a[8:11],v[0:3]\n"
"ds_read_b128 a[96:99],v114 offset:24576\n"
"ds_read_b128 a[100:103],v114 offset:25600\n"
"v_mfma_f32_16x16x32_bf16 v[0:3],a[76:79],a[12:15],v[0:3]\n"
"v_exp_f32 v119,v34\n"
"v_mfma_f32_16x16x32_bf16 v[4:7],a[92:95],a[12:15],v[96:99]\n"
"s_add_u32 s101,s101,4\n"
"ds_read_b128 a[104:107],v114 offset:26624\n"
"v_cvt_pk_bf16_f32 v80,v122,v123\n"
"v_mfma_f32_16x16x32_bf16 v[4:7],a[88:91],a[8:11],v[4:7]\n"
"v_exp_f32 v120,v35\n"
"v_mfma_f32_16x16x32_bf16 v[4:7],a[84:87],a[4:7],v[4:7]\n"
"ds_read_b128 a[108:111],v114 offset:27648\n"
"ds_read_b128 a[112:115],v114 offset:28672\n"
"v_mfma_f32_16x16x32_bf16 v[4:7],a[80:83],a[0:3],v[4:7]\n"
"v_exp_f32 v122,v36\n"
"v_mfma_f32_16x16x32_bf16 v[12:15],a[80:83],a[16:19],v[100:103]\n"
"ds_read_b128 a[116:119],v114 offset:29696\n"
"v_cvt_pk_bf16_f32 v81,v119,v120\n"
"v_mfma_f32_16x16x32_bf16 v[12:15],a[84:87],a[20:23],v[12:15]\n"
"v_exp_f32 v123,v37\n"
"v_mfma_f32_16x16x32_bf16 v[12:15],a[88:91],a[24:27],v[12:15]\n"
"ds_read_b128 a[120:123],v114 offset:30720\n"
"ds_read_b128 a[124:127],v114 offset:31744\n"
"v_mfma_f32_16x16x32_bf16 v[12:15],a[92:95],a[28:31],v[12:15]\n"
"s_add_u32 m0,s67,0\n"
"v_exp_f32 v119,v38\n"
"v_mfma_f32_16x16x32_bf16 v[8:11],a[76:79],a[28:31],v[100:103]\n"
"v_cvt_pk_bf16_f32 v82,v122,v123\n"
"v_or3_b32 v118,v80,v81,v82\n"
"v_mfma_f32_16x16x32_bf16 v[8:11],a[72:75],a[24:27],v[8:11]\n"
"v_exp_f32 v120,v39\n"
"v_mfma_f32_16x16x32_bf16 v[8:11],a[68:71],a[20:23],v[8:11]\n"
"buffer_load_dwordx4 v112,s[24:27],s64 offen offset:0 sc1 lds\n"
"v_mfma_f32_16x16x32_bf16 v[8:11],a[64:67],a[16:19],v[8:11]\n"
"v_exp_f32 v122,v40\n"
"v_mfma_f32_16x16x32_bf16 v[16:19],a[64:67],a[32:35],v[104:107]\n"
"v_cvt_pk_bf16_f32 v83,v119,v120\n"
"v_mfma_f32_16x16x32_bf16 v[16:19],a[68:71],a[36:39],v[16:19]\n"
"v_exp_f32 v123,v41\n"
"v_mfma_f32_16x16x32_bf16 v[16:19],a[72:75],a[40:43],v[16:19]\n"
"v_cvt_pk_bf16_f32 v84,v122,v123\n"
"v_or3_b32 v118,v118,v83,v84\n"
"v_mfma_f32_16x16x32_bf16 v[16:19],a[76:79],a[44:47],v[16:19]\n"
"v_exp_f32 v119,v42\n"
"v_mfma_f32_16x16x32_bf16 v[20:23],a[92:95],a[44:47],v[104:107]\n"
"v_mfma_f32_16x16x32_bf16 v[20:23],a[88:91],a[40:43],v[20:23]\n"
"v_exp_f32 v120,v43\n"
"v_mfma_f32_16x16x32_bf16 v[20:23],a[84:87],a[36:39],v[20:23]\n"
"buffer_load_dwordx4 v112,s[24:27],s64 offen offset:1024 sc1 lds\n"
"v_mfma_f32_16x16x32_bf16 v[20:23],a[80:83],a[32:35],v[20:23]\n"
"v_exp_f32 v122,v44\n"
"v_mfma_f32_16x16x32_bf16 v[28:31],a[80:83],a[48:51],v[108:111]\n"
"v_cvt_pk_bf16_f32 v85,v119,v120\n"
"v_mfma_f32_16x16x32_bf16 v[28:31],a[84:87],a[52:55],v[28:31]\n"
"v_exp_f32 v123,v45\n"
"v_mfma_f32_16x16x32_bf16 v[28:31],a[88:91],a[56:59],v[28:31]\n"
"v_cvt_pk_bf16_f32 v86,v122,v123\n"
"v_or3_b32 v118,v118,v85,v86\n"
"v_mfma_f32_16x16x32_bf16 v[28:31],a[92:95],a[60:63],v[28:31]\n"
"v_exp_f32 v119,v46\n"
"v_mfma_f32_16x16x32_bf16 v[24:27],a[76:79],a[60:63],v[108:111]\n"
"v_mfma_f32_16x16x32_bf16 v[24:27],a[72:75],a[56:59],v[24:27]\n"
"v_exp_f32 v120,v47\n"
"v_mfma_f32_16x16x32_bf16 v[24:27],a[68:71],a[52:55],v[24:27]\n"
"v_cvt_pk_bf16_f32 v87,v119,v120\n"
"v_mfma_f32_16x16x32_bf16 v[24:27],a[64:67],a[48:51],v[24:27]\n"
"s_waitcnt lgkmcnt(0)\n"
"v_exp_f32 v122,v48\n"
"v_mfma_f32_16x16x32_bf16 v[128:131],a[96:99],v[64:67],v[128:131]\n"
"v_exp_f32 v123,v49\n"
"v_mfma_f32_16x16x32_bf16 v[160:163],a[96:99],v[68:71],v[160:163]\n"
"ds_read_b128 a[64:67],v113 offset:16384\n"
"ds_read_b128 a[68:71],v113 offset:17408\n"
"v_mfma_f32_16x16x32_bf16 v[192:195],a[96:99],v[72:75],v[192:195]\n"
"v_exp_f32 v119,v50\n"
"v_mfma_f32_16x16x32_bf16 v[224:227],a[96:99],v[76:79],v[224:227]\n"
"ds_read_b128 a[72:75],v113 offset:18432\n"
"v_cvt_pk_bf16_f32 v88,v122,v123\n"
"v_mfma_f32_16x16x32_bf16 a[140:143],a[144:147],v[76:79],a[140:143]\n"
"v_exp_f32 v120,v51\n"
"v_mfma_f32_16x16x32_bf16 v[228:231],a[100:103],v[76:79],v[228:231]\n"
"ds_read_b128 a[76:79],v113 offset:19456\n"
"ds_read_b128 a[80:83],v113 offset:20480\n"
"v_mfma_f32_16x16x32_bf16 v[132:135],a[100:103],v[64:67],v[132:135]\n"
"v_exp_f32 v122,v52\n"
"v_mfma_f32_16x16x32_bf16 v[164:167],a[100:103],v[68:71],v[164:167]\n"
"ds_read_b128 a[84:87],v113 offset:21504\n"
"v_cvt_pk_bf16_f32 v89,v119,v120\n"
"v_mfma_f32_16x16x32_bf16 v[196:199],a[100:103],v[72:75],v[196:199]\n"
"v_exp_f32 v123,v53\n"
"v_mfma_f32_16x16x32_bf16 a[136:139],a[144:147],v[72:75],a[136:139]\n"
"ds_read_b128 a[88:91],v113 offset:22528\n"
"ds_read_b128 a[92:95],v113 offset:23552\n"
"v_mfma_f32_16x16x32_bf16 v[200:203],a[104:107],v[72:75],v[200:203]\n"
"v_exp_f32 v119,v54\n"
"v_mfma_f32_16x16x32_bf16 v[232:235],a[104:107],v[76:79],v[232:235]\n"
"v_cvt_pk_bf16_f32 v90,v122,v123\n"
"v_or3_b32 v118,v118,v87,v88\n"
"v_mfma_f32_16x16x32_bf16 v[136:139],a[104:107],v[64:67],v[136:139]\n"
"v_exp_f32 v120,v55\n"
"v_mfma_f32_16x16x32_bf16 v[168:171],a[104:107],v[68:71],v[168:171]\n"
"s_waitcnt vmcnt(9)\n"
"ds_add_u32 v124,a152\n"
"v_mfma_f32_16x16x32_bf16 a[132:135],a[144:147],v[68:71],a[132:135]\n"
"v_exp_f32 v122,v56\n"
"v_mfma_f32_16x16x32_bf16 v[172:175],a[108:111],v[68:71],v[172:175]\n"
"v_cvt_pk_bf16_f32 v91,v119,v120\n"
"v_or3_b32 v118,v118,v89,v90\n"
"v_mfma_f32_16x16x32_bf16 v[204:207],a[108:111],v[72:75],v[204:207]\n"
"v_exp_f32 v123,v57\n"
"v_mfma_f32_16x16x32_bf16 v[236:239],a[108:111],v[76:79],v[236:239]\n"
"buffer_load_dwordx4 v112,s[24:27],s64 offen offset:2048 sc1 lds\n"
"v_mfma_f32_16x16x32_bf16 v[140:143],a[108:111],v[64:67],v[140:143]\n"
"v_exp_f32 v119,v58\n"
"v_mfma_f32_16x16x32_bf16 a[128:131],a[144:147],v[64:67],a[128:131]\n"
"v_cvt_pk_bf16_f32 v92,v122,v123\n"
"v_or3_b32 v118,v118,v91,v92\n"
"v_mfma_f32_16x16x32_bf16 v[144:147],a[112:115],v[64:67],v[144:147]\n"
"v_exp_f32 v120,v59\n"
"v_mfma_f32_16x16x32_bf16 v[176:179],a[112:115],v[68:71],v[176:179]\n"
"buffer_load_dwordx4 v112,s[24:27],s64 offen offset:3072 sc1 lds\n"
"v_mfma_f32_16x16x32_bf16 v[208:211],a[112:115],v[72:75],v[208:211]\n"
"s_add_u32 s64,s64,16384\n"
"v_exp_f32 v122,v60\n"
"v_mfma_f32_16x16x32_bf16 v[240:243],a[112:115],v[76:79],v[240:243]\n"
"v_cvt_pk_bf16_f32 v93,v119,v120\n"
"s_andn2_b32 m0,s55,16384\n"
"v_mfma_f32_16x16x32_bf16 v[244:247],a[116:119],v[76:79],v[244:247]\n"
"s_add_u32 s54,s54,s22\n"
"v_exp_f32 v123,v61\n"
"v_mfma_f32_16x16x32_bf16 v[148:151],a[116:119],v[64:67],v[148:151]\n"
"buffer_load_dwordx4 v115,s[32:35],s54 offen sc1 lds\n"
"v_mfma_f32_16x16x32_bf16 v[180:183],a[116:119],v[68:71],v[180:183]\n"
"v_exp_f32 v119,v62\n"
"v_mfma_f32_16x16x32_bf16 v[212:215],a[116:119],v[72:75],v[212:215]\n"
"ds_read_b32 v125,v124\n"
"v_cvt_pk_bf16_f32 v94,v122,v123\n"
"v_mfma_f32_16x16x32_bf16 v[216:219],a[120:123],v[72:75],v[216:219]\n"
"v_exp_f32 v120,v63\n"
"v_mfma_f32_16x16x32_bf16 v[248:251],a[120:123],v[76:79],v[248:251]\n"
"buffer_store_dwordx4 a[240:243],v112,s[24:27],s56 offen offset:0 sc1\n"
"v_mfma_f32_16x16x32_bf16 v[152:155],a[120:123],v[64:67],v[152:155]\n"
"v_cvt_pk_bf16_f32 v95,v119,v120\n"
"v_or3_b32 v118,v118,v93,v94\n"
"v_mfma_f32_16x16x32_bf16 v[184:187],a[120:123],v[68:71],v[184:187]\n"
"v_mfma_f32_16x16x32_bf16 v[188:191],a[124:127],v[68:71],v[188:191]\n"
"s_waitcnt lgkmcnt(0)\n"
"v_subrev_u32 v125,s101,v125\n"
"v_or3_b32 v125,v118,v95,v125\n"
"v_mfma_f32_16x16x32_bf16 v[220:223],a[124:127],v[72:75],v[220:223]\n"
"v_and_b32 v125,0x40004000,v125\n"
"v_cmp_ne_u32 vcc,0,v125\n"
"v_mfma_f32_16x16x32_bf16 v[252:255],a[124:127],v[76:79],v[252:255]\n"
"v_mfma_f32_16x16x32_bf16 v[156:159],a[124:127],v[64:67],v[156:159]\n"
"s_cbranch_vccnz .Lfk_cs_f3\n"
".Lfk_cr_f3:\n"
"v_mfma_f32_16x16x32_bf16 v[32:35],a[64:67],a[0:3],v[96:99]\n"
"s_add_u32 s101,s101,4\n"
"v_exp_f32 v122,v0\n"
"v_mfma_f32_16x16x32_bf16 v[32:35],a[68:71],a[4:7],v[32:35]\n"
"s_add_u32 m0,s55,4096\n"
"v_exp_f32 v123,v1\n"
"v_mfma_f32_16x16x32_bf16 v[32:35],a[72:75],a[8:11],v[32:35]\n"
"ds_read_b128 a[96:99],v114 offset:57344\n"
"ds_read_b128 a[100:103],v114 offset:58368\n"
"v_mfma_f32_16x16x32_bf16 v[32:35],a[76:79],a[12:15],v[32:35]\n"
"s_add_u32 s54,s54,s22\n"
"v_exp_f32 v119,v2\n"
"v_mfma_f32_16x16x32_bf16 v[36:39],a[92:95],a[12:15],v[96:99]\n"
"ds_read_b128 a[104:107],v114 offset:59392\n"
"v_cvt_pk_bf16_f32 v64,v122,v123\n"
"v_mfma_f32_16x16x32_bf16 v[36:39],a[88:91],a[8:11],v[36:39]\n"
"v_exp_f32 v120,v3\n"
"v_mfma_f32_16x16x32_bf16 v[36:39],a[84:87],a[4:7],v[36:39]\n"
"ds_read_b128 a[108:111],v114 offset:60416\n"
"ds_read_b128 a[112:115],v114 offset:61440\n"
"v_mfma_f32_16x16x32_bf16 v[36:39],a[80:83],a[0:3],v[36:39]\n"
"v_exp_f32 v122,v4\n"
"v_mfma_f32_16x16x32_bf16 v[44:47],a[80:83],a[16:19],v[100:103]\n"
"ds_read_b128 a[116:119],v114 offset:62464\n"
"v_cvt_pk_bf16_f32 v65,v119,v120\n"
"v_mfma_f32_16x16x32_bf16 v[44:47],a[84:87],a[20:23],v[44:47]\n"
"v_exp_f32 v123,v5\n"
"v_mfma_f32_16x16x32_bf16 v[44:47],a[88:91],a[24:27],v[44:47]\n"
"ds_read_b128 a[120:123],v114 offset:63488\n"
"ds_read_b128 a[124:127],v114 offset:64512\n"
"v_mfma_f32_16x16x32_bf16 v[44:47],a[92:95],a[28:31],v[44:47]\n"
"v_exp_f32 v119,v6\n"
"v_mfma_f32_16x16x32_bf16 v[40:43],a[76:79],a[28:31],v[100:103]\n"
"v_cvt_pk_bf16_f32 v66,v122,v123\n"
"v_or3_b32 v118,v64,v65,v66\n"
"v_mfma_f32_16x16x32_bf16 v[40:43],a[72:75],a[24:27],v[40:43]\n"
"v_exp_f32 v120,v7\n"
"v_mfma_f32_16x16x32_bf16 v[40:43],a[68:71],a[20:23],v[40:43]\n"
"buffer_load_dwordx4 v115,s[32:35],s54 offen sc1 lds\n"
"v_mfma_f32_16x16x32_bf16 v[40:43],a[64:67],a[16:19],v[40:43]\n"
"s_add_u32 m0,s67,16384\n"
"v_exp_f32 v122,v8\n"
"v_mfma_f32_16x16x32_bf16 v[48:51],a[64:67],a[32:35],v[104:107]\n"
"v_cvt_pk_bf16_f32 v67,v119,v120\n"
"v_mfma_f32_16x16x32_bf16 v[48:51],a[68:71],a[36:39],v[48:51]\n"
"v_exp_f32 v123,v9\n"
"v_mfma_f32_16x16x32_bf16 v[48:51],a[72:75],a[40:43],v[48:51]\n"
"buffer_load_dwordx4 v112,s[24:27],s64 offen offset:0 sc1 lds\n"
"v_mfma_f32_16x16x32_bf16 v[48:51],a[76:79],a[44:47],v[48:51]\n"
"v_exp_f32 v119,v10\n"
"v_mfma_f32_16x16x32_bf16 v[52:55],a[92:95],a[44:47],v[104:107]\n"
"v_cvt_pk_bf16_f32 v68,v122,v123\n"
"v_or3_b32 v118,v118,v67,v68\n"
"v_mfma_f32_16x16x32_bf16 v[52:55],a[88:91],a[40:43],v[52:55]\n"
"v_exp_f32 v120,v11\n"
"v_mfma_f32_16x16x32_bf16 v[52:55],a[84:87],a[36:39],v[52:55]\n"
"buffer_load_dwordx4 v112,s[24:27],s64 offen offset:1024 sc1 lds\n"
"v_mfma_f32_16x16x32_bf16 v[52:55],a[80:83],a[32:35],v[52:55]\n"
"v_exp_f32 v122,v12\n"
"v_mfma_f32_16x16x32_bf16 v[60:63],a[80:83],a[48:51],v[108:111]\n"
"v_cvt_pk_bf16_f32 v69,v119,v120\n"
"v_mfma_f32_16x16x32_bf16 v[60:63],a[84:87],a[52:55],v[60:63]\n"
"v_exp_f32 v123,v13\n"
"v_mfma_f32_16x16x32_bf16 v[60:63],a[88:91],a[56:59],v[60:63]\n"
"v_cvt_pk_bf16_f32 v70,v122,v123\n"
"v_or3_b32 v118,v118,v69,v70\n"
"v_mfma_f32_16x16x32_bf16 v[60:63],a[92:95],a[60:63],v[60:63]\n"
"v_exp_f32 v119,v14\n"
"v_mfma_f32_16x16x32_bf16 v[56:59],a[76:79],a[60:63],v[108:111]\n"
"v_mfma_f32_16x16x32_bf16 v[56:59],a[72:75],a[56:59],v[56:59]\n"
"v_exp_f32 v120,v15\n"
"v_mfma_f32_16x16x32_bf16 v[56:59],a[68:71],a[52:55],v[56:59]\n"
"v_cvt_pk_bf16_f32 v71,v119,v120\n"
"v_mfma_f32_16x16x32_bf16 v[56:59],a[64:67],a[48:51],v[56:59]\n"
"s_waitcnt lgkmcnt(0)\n"
"v_exp_f32 v122,v16\n"
"v_mfma_f32_16x16x32_bf16 v[128:131],a[96:99],v[80:83],v[128:131]\n"
"v_exp_f32 v123,v17\n"
"v_mfma_f32_16x16x32_bf16 v[160:163],a[96:99],v[84:87],v[160:163]\n"
"ds_read_b128 a[64:67],v113 offset:49152\n"
"ds_read_b128 a[68:71],v113 offset:50176\n"
"v_mfma_f32_16x16x32_bf16 v[192:195],a[96:99],v[88:91],v[192:195]\n"
"v_exp_f32 v119,v18\n"
"v_mfma_f32_16x16x32_bf16 v[224:227],a[96:99],v[92:95],v[224:227]\n"
"ds_read_b128 a[72:75],v113 offset:51200\n"
"v_cvt_pk_bf16_f32 v72,v122,v123\n"
"v_mfma_f32_16x16x32_bf16 a[140:143],a[144:147],v[92:95],a[140:143]\n"
"v_exp_f32 v120,v19\n"
"v_mfma_f32_16x16x32_bf16 v[228:231],a[100:103],v[92:95],v[228:231]\n"
"ds_read_b128 a[76:79],v113 offset:52224\n"
"ds_read_b128 a[80:83],v113 offset:53248\n"
"v_mfma_f32_16x16x32_bf16 v[132:135],a[100:103],v[80:83],v[132:135]\n"
"v_exp_f32 v122,v20\n"
"v_mfma_f32_16x16x32_bf16 v[164:167],a[100:103],v[84:87],v[164:167]\n"
"ds_read_b128 a[84:87],v113 offset:54272\n"
"v_cvt_pk_bf16_f32 v73,v119,v120\n"
"v_mfma_f32_16x16x32_bf16 v[196:199],a[100:103],v[88:91],v[196:199]\n"
"v_exp_f32 v123,v21\n"
"v_mfma_f32_16x16x32_bf16 a[136:139],a[144:147],v[88:91],a[136:139]\n"
"ds_read_b128 a[88:91],v113 offset:55296\n"
"ds_read_b128 a[92:95],v113 offset:56320\n"
"v_mfma_f32_16x16x32_bf16 v[200:203],a[104:107],v[88:91],v[200:203]\n"
"v_exp_f32 v119,v22\n"
"v_mfma_f32_16x16x32_bf16 v[232:235],a[104:107],v[92:95],v[232:235]\n"
"v_cvt_pk_bf16_f32 v74,v122,v123\n"
"v_or3_b32 v118,v118,v71,v72\n"
"v_mfma_f32_16x16x32_bf16 v[136:139],a[104:107],v[80:83],v[136:139]\n"
"v_exp_f32 v120,v23\n"
"v_mfma_f32_16x16x32_bf16 v[168:171],a[104:107],v[84:87],v[168:171]\n"
"s_waitcnt vmcnt(10)\n"
"ds_add_u32 v124,a152\n"
"v_mfma_f32_16x16x32_bf16 a[132:135],a[144:147],v[84:87],a[132:135]\n"
"v_exp_f32 v122,v24\n"
"v_mfma_f32_16x16x32_bf16 v[172:175],a[108:111],v[84:87],v[172:175]\n"
"v_cvt_pk_bf16_f32 v75,v119,v120\n"
"v_or3_b32 v118,v118,v73,v74\n"
"v_mfma_f32_16x16x32_bf16 v[204:207],a[108:111],v[88:91],v[204:207]\n"
"v_exp_f32 v123,v25\n"
"v_mfma_f32_16x16x32_bf16 v[236:239],a[108:111],v[92:95],v[236:239]\n"
"buffer_load_dwordx4 v112,s[24:27],s64 offen offset:2048 sc1 lds\n"
"v_mfma_f32_16x16x32_bf16 v[140:143],a[108:111],v[80:83],v[140:143]\n"
"v_exp_f32 v119,v26\n"
"v_mfma_f32_16x16x32_bf16 a[128:131],a[144:147],v[80:83],a[128:131]\n"
"v_cvt_pk_bf16_f32 v76,v122,v123\n"
"v_or3_b32 v118,v118,v75,v76\n"
"v_mfma_f32_16x16x32_bf16 v[144:147],a[112:115],v[80:83],v[144:147]\n"
"v_exp_f32 v120,v27\n"
"v_mfma_f32_16x16x32_bf16 v[176:179],a[112:115],v[84:87],v[176:179]\n"
"buffer_load_dwordx4 v112,s[24:27],s64 offen offset:3072 sc1 lds\n"
"v_mfma_f32_16x16x32_bf16 v[208:211],a[112:115],v[88:91],v[208:211]\n"
"s_add_u32 s64,s64,16384\n"
"v_exp_f32 v122,v28\n"
"v_mfma_f32_16x16x32_bf16 v[240:243],a[112:115],v[92:95],v[240:243]\n"
"v_cvt_pk_bf16_f32 v77,v119,v120\n"
"v_mfma_f32_16x16x32_bf16 v[244:247],a[116:119],v[92:95],v[244:247]\n"
"v_exp_f32 v123,v29\n"
"v_mfma_f32_16x16x32_bf16 v[148:151],a[116:119],v[80:83],v[148:151]\n"
"v_cvt_pk_bf16_f32 v78,v122,v123\n"
"v_or3_b32 v118,v118,v77,v78\n"
"v_mfma_f32_16x16x32_bf16 v[180:183],a[116:119],v[84:87],v[180:183]\n"
"v_exp_f32 v119,v30\n"
"v_mfma_f32_16x16x32_bf16 v[212:215],a[116:119],v[88:91],v[212:215]\n"
"ds_read_b32 v125,v124\n"
"buffer_store_dwordx4 a[244:247],v112,s[24:27],s56 offen offset:1024 sc1\n"
"v_mfma_f32_16x16x32_bf16 v[216:219],a[120:123],v[88:91],v[216:219]\n"
"v_exp_f32 v120,v31\n"
"v_mfma_f32_16x16x32_bf16 v[248:251],a[120:123],v[92:95],v[248:251]\n"
"v_cvt_pk_bf16_f32 v79,v119,v120\n"
"v_mfma_f32_16x16x32_bf16 v[152:155],a[120:123],v[80:83],v[152:155]\n"
"v_mfma_f32_16x16x32_bf16 v[184:187],a[120:123],v[84:87],v[184:187]\n"
"v_mfma_f32_16x16x32_bf16 v[188:191],a[124:127],v[84:87],v[188:191]\n"
"s_waitcnt lgkmcnt(0)\n"
"v_subrev_u32 v125,s101,v125\n"
"v_or3_b32 v125,v118,v79,v125\n"
"v_mfma_f32_16x16x32_bf16 v[220:223],a[124:127],v[88:91],v[220:223]\n"
"v_and_b32 v125,0x40004000,v125\n"
"v_cmp_ne_u32 vcc,0,v125\n"
"v_mfma_f32_16x16x32_bf16 v[252:255],a[124:127],v[92:95],v[252:255]\n"
"v_mfma_f32_16x16x32_bf16 v[156:159],a[124:127],v[80:83],v[156:159]\n"
"s_cbranch_vccnz .Lfk_cs_f0\n"
".Lfk_cr_f0:\n"
"v_mfma_f32_16x16x32_bf16 v[0:3],a[64:67],a[0:3],v[96:99]\n"
"s_add_u32 s101,s101,4\n"
"v_exp_f32 v122,v32\n"
"v_mfma_f32_16x16x32_bf16 v[0:3],a[68:71],a[4:7],v[0:3]\n"
"s_add_u32 m0,s55,8192\n"
"v_exp_f32 v123,v33\n"
"v_mfma_f32_16x16x32_bf16 v[0:3],a[72:75],a[8:11],v[0:3]\n"
"ds_read_b128 a[96:99],v114 offset:40960\n"
"ds_read_b128 a[100:103],v114 offset:41984\n"
"v_mfma_f32_16x16x32_bf16 v[0:3],a[76:79],a[12:15],v[0:3]\n"
"s_add_u32 s54,s54,s22\n"
"v_exp_f32 v119,v34\n"
"v_mfma_f32_16x16x32_bf16 v[4:7],a[92:95],a[12:15],v[96:99]\n"
"ds_read_b128 a[104:107],v114 offset:43008\n"
"v_cvt_pk_bf16_f32 v80,v122,v123\n"
"v_mfma_f32_16x16x32_bf16 v[4:7],a[88:91],a[8:11],v[4:7]\n"
"v_exp_f32 v120,v35\n"
"v_mfma_f32_16x16x32_bf16 v[4:7],a[84:87],a[4:7],v[4:7]\n"
"ds_read_b128 a[108:111],v114 offset:44032\n"
"ds_read_b128 a[112:115],v114 offset:45056\n"
"v_mfma_f32_16x16x32_bf16 v[4:7],a[80:83],a[0:3],v[4:7]\n"
"v_exp_f32 v122,v36\n"
"v_mfma_f32_16x16x32_bf16 v[12:15],a[80:83],a[16:19],v[100:103]\n"
"ds_read_b128 a[116:119],v114 offset:46080\n"
"v_cvt_pk_bf16_f32 v81,v119,v120\n"
"v_mfma_f32_16x16x32_bf16 v[12:15],a[84:87],a[20:23],v[12:15]\n"
"v_exp_f32 v123,v37\n"
"v_mfma_f32_16x16x32_bf16 v[12:15],a[88:91],a[24:27],v[12:15]\n"
"ds_read_b128 a[120:123],v114 offset:47104\n"
"ds_read_b128 a[124:127],v114 offset:48128\n"
"v_mfma_f32_16x16x32_bf16 v[12:15],a[92:95],a[28:31],v[12:15]\n"
"v_exp_f32 v119,v38\n"
"v_mfma_f32_16x16x32_bf16 v[8:11],a[76:79],a[28:31],v[100:103]\n"
"v_cvt_pk_bf16_f32 v82,v122,v123\n"
"v_or3_b32 v118,v80,v81,v82\n"
"v_mfma_f32_16x16x32_bf16 v[8:11],a[72:75],a[24:27],v[8:11]\n"
"v_exp_f32 v120,v39\n"
"v_mfma_f32_16x16x32_bf16 v[8:11],a[68:71],a[20:23],v[8:11]\n"
"buffer_load_dwordx4 v115,s[32:35],s54 offen sc1 lds\n"
"v_mfma_f32_16x16x32_bf16 v[8:11],a[64:67],a[16:19],v[8:11]\n"
"s_add_u32 m0,s67,49152\n"
"v_exp_f32 v122,v40\n"
"v_mfma_f32_16x16x32_bf16 v[16:19],a[64:67],a[32:35],v[104:107]\n"
"v_cvt_pk_bf16_f32 v83,v119,v120\n"
"v_mfma_f32_16x16x32_bf16 v[16:19],a[68:71],a[36:39],v[16:19]\n"
"v_exp_f32 v123,v41\n"
"v_mfma_f32_16x16x32_bf16 v[16:19],a[72:75],a[40:43],v[16:19]\n"
"buffer_load_dwordx4 v112,s[24:27],s64 offen offset:0 sc1 lds\n"
"v_mfma_f32_16x16x32_bf16 v[16:19],a[76:79],a[44:47],v[16:19]\n"
"v_exp_f32 v119,v42\n"
"v_mfma_f32_16x16x32_bf16 v[20:23],a[92:95],a[44:47],v[104:107]\n"
"v_cvt_pk_bf16_f32 v84,v122,v123\n"
"v_or3_b32 v118,v118,v83,v84\n"
"v_mfma_f32_16x16x32_bf16 v[20:23],a[88:91],a[40:43],v[20:23]\n"
"v_exp_f32 v120,v43\n"
"v_mfma_f32_16x16x32_bf16 v[20:23],a[84:87],a[36:39],v[20:23]\n"
"buffer_load_dwordx4 v112,s[24:27],s64 offen offset:1024 sc1 lds\n"
"v_mfma_f32_16x16x32_bf16 v[20:23],a[80:83],a[32:35],v[20:23]\n"
"v_exp_f32 v122,v44\n"
"v_mfma_f32_16x16x32_bf16 v[28:31],a[80:83],a[48:51],v[108:111]\n"
"v_cvt_pk_bf16_f32 v85,v119,v120\n"
"v_mfma_f32_16x16x32_bf16 v[28:31],a[84:87],a[52:55],v[28:31]\n"
"v_exp_f32 v123,v45\n"
"v_mfma_f32_16x16x32_bf16 v[28:31],a[88:91],a[56:59],v[28:31]\n"
"v_cvt_pk_bf16_f32 v86,v122,v123\n"
"v_or3_b32 v118,v118,v85,v86\n"
"v_mfma_f32_16x16x32_bf16 v[28:31],a[92:95],a[60:63],v[28:31]\n"
"v_exp_f32 v119,v46\n"
"v_mfma_f32_16x16x32_bf16 v[24:27],a[76:79],a[60:63],v[108:111]\n"
"v_mfma_f32_16x16x32_bf16 v[24:27],a[72:75],a[56:59],v[24:27]\n"
"v_exp_f32 v120,v47\n"
"v_mfma_f32_16x16x32_bf16 v[24:27],a[68:71],a[52:55],v[24:27]\n"
"v_cvt_pk_bf16_f32 v87,v119,v120\n"
"v_mfma_f32_16x16x32_bf16 v[24:27],a[64:67],a[48:51],v[24:27]\n"
"s_waitcnt lgkmcnt(0)\n"
"v_exp_f32 v122,v48\n"
"v_mfma_f32_16x16x32_bf16 v[128:131],a[96:99],v[64:67],v[128:131]\n"
"v_exp_f32 v123,v49\n"
"v_mfma_f32_16x16x32_bf16 v[160:163],a[96:99],v[68:71],v[160:163]\n"
"ds_read_b128 a[64:67],v113 offset:32768\n"
"ds_read_b128 a[68:71],v113 offset:33792\n"
"v_mfma_f32_16x16x32_bf16 v[192:195],a[96:99],v[72:75],v[192:195]\n"
"v_exp_f32 v119,v50\n"
"v_mfma_f32_16x16x32_bf16 v[224:227],a[96:99],v[76:79],v[224:227]\n"
"ds_read_b128 a[72:75],v113 offset:34816\n"
"v_cvt_pk_bf16_f32 v88,v122,v123\n"
"v_mfma_f32_16x16x32_bf16 a[140:143],a[144:147],v[76:79],a[140:143]\n"
"v_exp_f32 v120,v51\n"
"v_mfma_f32_16x16x32_bf16 v[228:231],a[100:103],v[76:79],v[228:231]\n"
"ds_read_b128 a[76:79],v113 offset:35840\n"
"ds_read_b128 a[80:83],v113 offset:36864\n"
"v_mfma_f32_16x16x32_bf16 v[132:135],a[100:103],v[64:67],v[132:135]\n"
"v_exp_f32 v122,v52\n"
"v_mfma_f32_16x16x32_bf16 v[164:167],a[100:103],v[68:71],v[164:167]\n"
"ds_read_b128 a[84:87],v113 offset:37888\n"
"v_cvt_pk_bf16_f32 v89,v119,v120\n"
"v_mfma_f32_16x16x32_bf16 v[196:199],a[100:103],v[72:75],v[196:199]\n"
"v_exp_f32 v123,v53\n"
"v_mfma_f32_16x16x32_bf16 a[136:139],a[144:147],v[72:75],a[136:139]\n"
"ds_read_b128 a[88:91],v113 offset:38912\n"
"ds_read_b128 a[92:95],v113 offset:39936\n"
"v_mfma_f32_16x16x32_bf16 v[200:203],a[104:107],v[72:75],v[200:203]\n"
"v_exp_f32 v119,v54\n"
"v_mfma_f32_16x16x32_bf16 v[232:235],a[104:107],v[76:79],v[232:235]\n"
"v_cvt_pk_bf16_f32 v90,v122,v123\n"
"v_or3_b32 v118,v118,v87,v88\n"
"v_mfma_f32_16x16x32_bf16 v[136:139],a[104:107],v[64:67],v[136:139]\n"
"v_exp_f32 v120,v55\n"
"v_mfma_f32_16x16x32_bf16 v[168:171],a[104:107],v[68:71],v[168:171]\n"
"s_waitcnt vmcnt(10)\n"
"ds_add_u32 v124,a152\n"
"v_mfma_f32_16x16x32_bf16 a[132:135],a[144:147],v[68:71],a[132:135]\n"
"v_exp_f32 v122,v56\n"
"v_mfma_f32_16x16x32_bf16 v[172:175],a[108:111],v[68:71],v[172:175]\n"
"v_cvt_pk_bf16_f32 v91,v119,v120\n"
"v_or3_b32 v118,v118,v89,v90\n"
"v_mfma_f32_16x16x32_bf16 v[204:207],a[108:111],v[72:75],v[204:207]\n"
"v_exp_f32 v123,v57\n"
"v_mfma_f32_16x16x32_bf16 v[236:239],a[108:111],v[76:79],v[236:239]\n"
"buffer_load_dwordx4 v112,s[24:27],s64 offen offset:2048 sc1 lds\n"
"v_mfma_f32_16x16x32_bf16 v[140:143],a[108:111],v[64:67],v[140:143]\n"
"v_exp_f32 v119,v58\n"
"v_mfma_f32_16x16x32_bf16 a[128:131],a[144:147],v[64:67],a[128:131]\n"
"v_cvt_pk_bf16_f32 v92,v122,v123\n"
"v_or3_b32 v118,v118,v91,v92\n"
"v_mfma_f32_16x16x32_bf16 v[144:147],a[112:115],v[64:67],v[144:147]\n"
"v_exp_f32 v120,v59\n"
"v_mfma_f32_16x16x32_bf16 v[176:179],a[112:115],v[68:71],v[176:179]\n"
"buffer_load_dwordx4 v112,s[24:27],s64 offen offset:3072 sc1 lds\n"
"v_mfma_f32_16x16x32_bf16 v[208:211],a[112:115],v[72:75],v[208:211]\n"
"s_add_u32 s64,s64,16384\n"
"v_exp_f32 v122,v60\n"
"v_mfma_f32_16x16x32_bf16 v[240:243],a[112:115],v[76:79],v[240:243]\n"
"v_cvt_pk_bf16_f32 v93,v119,v120\n"
"v_mfma_f32_16x16x32_bf16 v[244:247],a[116:119],v[76:79],v[244:247]\n"
"v_exp_f32 v123,v61\n"
"v_mfma_f32_16x16x32_bf16 v[148:151],a[116:119],v[64:67],v[148:151]\n"
"v_cvt_pk_bf16_f32 v94,v122,v123\n"
"v_or3_b32 v118,v118,v93,v94\n"
"v_mfma_f32_16x16x32_bf16 v[180:183],a[116:119],v[68:71],v[180:183]\n"
"v_exp_f32 v119,v62\n"
"v_mfma_f32_16x16x32_bf16 v[212:215],a[116:119],v[72:75],v[212:215]\n"
"ds_read_b32 v125,v124\n"
"buffer_store_dwordx4 a[248:251],v112,s[24:27],s56 offen offset:2048 sc1\n"
"v_mfma_f32_16x16x32_bf16 v[216:219],a[120:123],v[72:75],v[216:219]\n"
"v_exp_f32 v120,v63\n"
"v_mfma_f32_16x16x32_bf16 v[248:251],a[120:123],v[76:79],v[248:251]\n"
"v_cvt_pk_bf16_f32 v95,v119,v120\n"
"v_mfma_f32_16x16x32_bf16 v[152:155],a[120:123],v[64:67],v[152:155]\n"
"v_mfma_f32_16x16x32_bf16 v[184:187],a[120:123],v[68:71],v[184:187]\n"
"v_mfma_f32_16x16x32_bf16 v[188:191],a[124:127],v[68:71],v[188:191]\n"
"s_waitcnt lgkmcnt(0)\n"
"v_subrev_u32 v125,s101,v125\n"
"v_or3_b32 v125,v118,v95,v125\n"
"v_mfma_f32_16x16x32_bf16 v[220:223],a[124:127],v[72:75],v[220:223]\n"
"v_and_b32 v125,0x40004000,v125\n"
"v_cmp_ne_u32 vcc,0,v125\n"
"v_mfma_f32_16x16x32_bf16 v[252:255],a[124:127],v[76:79],v[252:255]\n"
"v_mfma_f32_16x16x32_bf16 v[156:159],a[124:127],v[64:67],v[156:159]\n"
"s_cbranch_vccnz .Lfk_cs_f1\n"
".Lfk_cr_f1:\n"
"v_mfma_f32_16x16x32_bf16 v[32:35],a[64:67],a[0:3],v[96:99]\n"
"s_add_u32 s101,s101,4\n"
"v_exp_f32 v122,v0\n"
"v_mfma_f32_16x16x32_bf16 v[32:35],a[68:71],a[4:7],v[32:35]\n"
"s_add_u32 m0,s55,12288\n"
"v_exp_f32 v123,v1\n"
"v_mfma_f32_16x16x32_bf16 v[32:35],a[72:75],a[8:11],v[32:35]\n"
"ds_read_b128 a[96:99],v114 offset:8192\n"
"ds_read_b128 a[100:103],v114 offset:9216\n"
"v_mfma_f32_16x16x32_bf16 v[32:35],a[76:79],a[12:15],v[32:35]\n"
"s_add_u32 s54,s54,s22\n"
"v_exp_f32 v119,v2\n"
"v_mfma_f32_16x16x32_bf16 v[36:39],a[92:95],a[12:15],v[96:99]\n"
"ds_read_b128 a[104:107],v114 offset:10240\n"
"v_cvt_pk_bf16_f32 v64,v122,v123\n"
"v_mfma_f32_16x16x32_bf16 v[36:39],a[88:91],a[8:11],v[36:39]\n"
"v_exp_f32 v120,v3\n"
"v_mfma_f32_16x16x32_bf16 v[36:39],a[84:87],a[4:7],v[36:39]\n"
"ds_read_b128 a[108:111],v114 offset:11264\n"
"ds_read_b128 a[112:115],v114 offset:12288\n"
"v_mfma_f32_16x16x32_bf16 v[36:39],a[80:83],a[0:3],v[36:39]\n"
"v_exp_f32 v122,v4\n"
"v_mfma_f32_16x16x32_bf16 v[44:47],a[80:83],a[16:19],v[100:103]\n"
"ds_read_b128 a[116:119],v114 offset:13312\n"
"v_cvt_pk_bf16_f32 v65,v119,v120\n"
"v_mfma_f32_16x16x32_bf16 v[44:47],a[84:87],a[20:23],v[44:47]\n"
"v_exp_f32 v123,v5\n"
"v_mfma_f32_16x16x32_bf16 v[44:47],a[88:91],a[24:27],v[44:47]\n"
"ds_read_b128 a[120:123],v114 offset:14336\n"
"ds_read_b128 a[124:127],v114 offset:15360\n"
"v_mfma_f32_16x16x32_bf16 v[44:47],a[92:95],a[28:31],v[44:47]\n"
"v_exp_f32 v119,v6\n"
"v_mfma_f32_16x16x32_bf16 v[40:43],a[76:79],a[28:31],v[100:103]\n"
"v_cvt_pk_bf16_f32 v66,v122,v123\n"
"v_or3_b32 v118,v64,v65,v66\n"
"v_mfma_f32_16x16x32_bf16 v[40:43],a[72:75],a[24:27],v[40:43]\n"
"v_exp_f32 v120,v7\n"
"v_mfma_f32_16x16x32_bf16 v[40:43],a[68:71],a[20:23],v[40:43]\n"
"buffer_load_dwordx4 v115,s[32:35],s54 offen sc1 lds\n"
"v_mfma_f32_16x16x32_bf16 v[40:43],a[64:67],a[16:19],v[40:43]\n"
"s_add_u32 m0,s67,32768\n"
"v_exp_f32 v122,v8\n"
"v_mfma_f32_16x16x32_bf16 v[48:51],a[64:67],a[32:35],v[104:107]\n"
"v_cvt_pk_bf16_f32 v67,v119,v120\n"
"v_mfma_f32_16x16x32_bf16 v[48:51],a[68:71],a[36:39],v[48:51]\n"
"v_exp_f32 v123,v9\n"
"v_mfma_f32_16x16x32_bf16 v[48:51],a[72:75],a[40:43],v[48:51]\n"
"buffer_load_dwordx4 v112,s[24:27],s64 offen offset:0 sc1 lds\n"
"v_mfma_f32_16x16x32_bf16 v[48:51],a[76:79],a[44:47],v[48:51]\n"
"v_exp_f32 v119,v10\n"
"v_mfma_f32_16x16x32_bf16 v[52:55],a[92:95],a[44:47],v[104:107]\n"
"v_cvt_pk_bf16_f32 v68,v122,v123\n"
"v_or3_b32 v118,v118,v67,v68\n"
"v_mfma_f32_16x16x32_bf16 v[52:55],a[88:91],a[40:43],v[52:55]\n"
"v_exp_f32 v120,v11\n"
"v_mfma_f32_16x16x32_bf16 v[52:55],a[84:87],a[36:39],v[52:55]\n"
"buffer_load_dwordx4 v112,s[24:27],s64 offen offset:1024 sc1 lds\n"
"v_mfma_f32_16x16x32_bf16 v[52:55],a[80:83],a[32:35],v[52:55]\n"
"v_exp_f32 v122,v12\n"
"v_mfma_f32_16x16x32_bf16 v[60:63],a[80:83],a[48:51],v[108:111]\n"
"v_cvt_pk_bf16_f32 v69,v119,v120\n"
"v_mfma_f32_16x16x32_bf16 v[60:63],a[84:87],a[52:55],v[60:63]\n"
"v_exp_f32 v123,v13\n"
"v_mfma_f32_16x16x32_bf16 v[60:63],a[88:91],a[56:59],v[60:63]\n"
"v_cvt_pk_bf16_f32 v70,v122,v123\n"
"v_or3_b32 v118,v118,v69,v70\n"
"v_mfma_f32_16x16x32_bf16 v[60:63],a[92:95],a[60:63],v[60:63]\n"
"v_exp_f32 v119,v14\n"
"v_mfma_f32_16x16x32_bf16 v[56:59],a[76:79],a[60:63],v[108:111]\n"
"v_mfma_f32_16x16x32_bf16 v[56:59],a[72:75],a[56:59],v[56:59]\n"
"v_exp_f32 v120,v15\n"
"v_mfma_f32_16x16x32_bf16 v[56:59],a[68:71],a[52:55],v[56:59]\n"
"v_cvt_pk_bf16_f32 v71,v119,v120\n"
"v_mfma_f32_16x16x32_bf16 v[56:59],a[64:67],a[48:51],v[56:59]\n"
"s_waitcnt lgkmcnt(0)\n"
"v_exp_f32 v122,v16\n"
"v_mfma_f32_16x16x32_bf16 v[128:131],a[96:99],v[80:83],v[128:131]\n"
"v_exp_f32 v123,v17\n"
"v_mfma_f32_16x16x32_bf16 v[160:163],a[96:99],v[84:87],v[160:163]\n"
"ds_read_b128 a[64:67],v113 offset:0\n"
"ds_read_b128 a[68:71],v113 offset:1024\n"
"v_mfma_f32_16x16x32_bf16 v[192:195],a[96:99],v[88:91],v[192:195]\n"
"v_exp_f32 v119,v18\n"
"v_mfma_f32_16x16x32_bf16 v[224:227],a[96:99],v[92:95],v[224:227]\n"
"ds_read_b128 a[72:75],v113 offset:2048\n"
"v_cvt_pk_bf16_f32 v72,v122,v123\n"
"v_mfma_f32_16x16x32_bf16 a[140:143],a[144:147],v[92:95],a[140:143]\n"
"v_exp_f32 v120,v19\n"
"v_mfma_f32_16x16x32_bf16 v[228:231],a[100:103],v[92:95],v[228:231]\n"
"ds_read_b128 a[76:79],v113 offset:3072\n"
"ds_read_b128 a[80:83],v113 offset:4096\n"
"v_mfma_f32_16x16x32_bf16 v[132:135],a[100:103],v[80:83],v[132:135]\n"
"v_exp_f32 v122,v20\n"
"v_mfma_f32_16x16x32_bf16 v[164:167],a[100:103],v[84:87],v[164:167]\n"
"ds_read_b128 a[84:87],v113 offset:5120\n"
"v_cvt_pk_bf16_f32 v73,v119,v120\n"
"v_mfma_f32_16x16x32_bf16 v[196:199],a[100:103],v[88:91],v[196:199]\n"
"v_exp_f32 v123,v21\n"
"v_mfma_f32_16x16x32_bf16 a[136:139],a[144:147],v[88:91],a[136:139]\n"
"ds_read_b128 a[88:91],v113 offset:6144\n"
"ds_read_b128 a[92:95],v113 offset:7168\n"
"v_mfma_f32_16x16x32_bf16 v[200:203],a[104:107],v[88:91],v[200:203]\n"
"v_exp_f32 v119,v22\n"
"v_mfma_f32_16x16x32_bf16 v[232:235],a[104:107],v[92:95],v[232:235]\n"
"v_cvt_pk_bf16_f32 v74,v122,v123\n"
"v_or3_b32 v118,v118,v71,v72\n"
"v_mfma_f32_16x16x32_bf16 v[136:139],a[104:107],v[80:83],v[136:139]\n"
"v_exp_f32 v120,v23\n"
"v_mfma_f32_16x16x32_bf16 v[168:171],a[104:107],v[84:87],v[168:171]\n"
"s_waitcnt vmcnt(10)\n"
"ds_add_u32 v124,a152\n"
"v_mfma_f32_16x16x32_bf16 a[132:135],a[144:147],v[84:87],a[132:135]\n"
"v_exp_f32 v122,v24\n"
"v_mfma_f32_16x16x32_bf16 v[172:175],a[108:111],v[84:87],v[172:175]\n"
"v_cvt_pk_bf16_f32 v75,v119,v120\n"
"v_or3_b32 v118,v118,v73,v74\n"
"v_mfma_f32_16x16x32_bf16 v[204:207],a[108:111],v[88:91],v[204:207]\n"
"v_exp_f32 v123,v25\n"
"v_mfma_f32_16x16x32_bf16 v[236:239],a[108:111],v[92:95],v[236:239]\n"
"buffer_load_dwordx4 v112,s[24:27],s64 offen offset:2048 sc1 lds\n"
"v_mfma_f32_16x16x32_bf16 v[140:143],a[108:111],v[80:83],v[140:143]\n"
"v_exp_f32 v119,v26\n"
"v_mfma_f32_16x16x32_bf16 a[128:131],a[144:147],v[80:83],a[128:131]\n"
"v_cvt_pk_bf16_f32 v76,v122,v123\n"
"v_or3_b32 v118,v118,v75,v76\n"
"v_mfma_f32_16x16x32_bf16 v[144:147],a[112:115],v[80:83],v[144:147]\n"
"v_exp_f32 v120,v27\n"
"v_mfma_f32_16x16x32_bf16 v[176:179],a[112:115],v[84:87],v[176:179]\n"
"buffer_load_dwordx4 v112,s[24:27],s64 offen offset:3072 sc1 lds\n"
"v_mfma_f32_16x16x32_bf16 v[208:211],a[112:115],v[88:91],v[208:211]\n"
"s_add_u32 s64,s64,16384\n"
"v_exp_f32 v122,v28\n"
"v_mfma_f32_16x16x32_bf16 v[240:243],a[112:115],v[92:95],v[240:243]\n"
"v_cvt_pk_bf16_f32 v77,v119,v120\n"
"v_mfma_f32_16x16x32_bf16 v[244:247],a[116:119],v[92:95],v[244:247]\n"
"v_exp_f32 v123,v29\n"
"v_mfma_f32_16x16x32_bf16 v[148:151],a[116:119],v[80:83],v[148:151]\n"
"v_cvt_pk_bf16_f32 v78,v122,v123\n"
"v_or3_b32 v118,v118,v77,v78\n"
"v_mfma_f32_16x16x32_bf16 v[180:183],a[116:119],v[84:87],v[180:183]\n"
"v_exp_f32 v119,v30\n"
"v_mfma_f32_16x16x32_bf16 v[212:215],a[116:119],v[88:91],v[212:215]\n"
"ds_read_b32 v125,v124\n"
"buffer_store_dwordx4 a[252:255],v112,s[24:27],s56 offen offset:3072 sc1\n"
"v_mfma_f32_16x16x32_bf16 v[216:219],a[120:123],v[88:91],v[216:219]\n"
"s_add_u32 s89,s89,1\n"
"v_exp_f32 v120,v31\n"
"v_mfma_f32_16x16x32_bf16 v[248:251],a[120:123],v[92:95],v[248:251]\n"
"v_cvt_pk_bf16_f32 v79,v119,v120\n"
"v_mfma_f32_16x16x32_bf16 v[152:155],a[120:123],v[80:83],v[152:155]\n"
"v_mfma_f32_16x16x32_bf16 v[184:187],a[120:123],v[84:87],v[184:187]\n"
"v_mfma_f32_16x16x32_bf16 v[188:191],a[124:127],v[84:87],v[188:191]\n"
"s_waitcnt lgkmcnt(0)\n"
"v_subrev_u32 v125,s101,v125\n"
"v_or3_b32 v125,v118,v79,v125\n"
"v_mfma_f32_16x16x32_bf16 v[220:223],a[124:127],v[88:91],v[220:223]\n"
"v_and_b32 v125,0x40004000,v125\n"
"v_cmp_ne_u32 vcc,0,v125\n"
"v_mfma_f32_16x16x32_bf16 v[252:255],a[124:127],v[92:95],v[252:255]\n"
"v_mfma_f32_16x16x32_bf16 v[156:159],a[124:127],v[80:83],v[156:159]\n"
"s_cbranch_vccnz .Lfk_cs_f2\n"
".Lfk_cr_f2:\n"
"s_cmp_lt_u32 s89,s60\n"
"s_cbranch_scc1 .Lfk_F\n"
"s_branch .Lfk_Fx\n"
".Lfk_cs_f3:\n"
"s_call_b64 s[94:95],.Lfk_meet3\n"
"v_or_b32 v118,v118,v95\n"
"v_and_b32 v118,0x40004000,v118\n"
"v_cmp_ne_u32 vcc,0,v118\n"
"s_nop 0\n"
"s_cbranch_vccz .Lfk_cr_f3\n"
"s_mov_b32 s91,15\n"
"s_call_b64 s[94:95],.Lfk_rare3\n"
"s_branch .Lfk_cr_f3\n"
".Lfk_cs_f0:\n"
"s_call_b64 s[94:95],.Lfk_meet3\n"
"v_or_b32 v118,v118,v79\n"
"v_and_b32 v118,0x40004000,v118\n"
"v_cmp_ne_u32 vcc,0,v118\n"
"s_nop 0\n"
"s_cbranch_vccz .Lfk_cr_f0\n"
"s_mov_b32 s91,15\n"
"s_call_b64 s[94:95],.Lfk_rare2\n"
"s_branch .Lfk_cr_f0\n"
".Lfk_cs_f1:\n"
"s_call_b64 s[94:95],.Lfk_meet3\n"
"v_or_b32 v118,v118,v95\n"
"v_and_b32 v118,0x40004000,v118\n"
"v_cmp_ne_u32 vcc,0,v118\n"
"s_nop 0\n"
"s_cbranch_vccz .Lfk_cr_f1\n"
"s_mov_b32 s91,15\n"
"s_call_b64 s[94:95],.Lfk_rare3\n"
"s_branch .Lfk_cr_f1\n"
".Lfk_cs_f2:\n"
"s_call_b64 s[94:95],.Lfk_meet3\n"
"v_or_b32 v118,v118,v79\n"
"v_and_b32 v118,0x40004000,v118\n"
"v_cmp_ne_u32 vcc,0,v118\n"
"s_nop 0\n"
"s_cbranch_vccz .Lfk_cr_f2\n"
"s_mov_b32 s91,15\n"
"s_call_b64 s[94:95],.Lfk_rare2\n"
"s_branch .Lfk_cr_f2\n"
".Lfk_cs_g3:\n"
"s_call_b64 s[94:95],.Lfk_meet3\n"
"v_or_b32 v118,v118,v95\n"
"v_and_b32 v118,0x40004000,v118\n"
"v_cmp_ne_u32 vcc,0,v118\n"
"s_nop 0\n"
"s_cbranch_vccz .Lfk_cr_g3\n"
"s_mov_b32 s91,15\n"
"s_call_b64 s[94:95],.Lfk_rare3\n"
"s_branch .Lfk_cr_g3\n"
".Lfk_cs_g0:\n"
"s_call_b64 s[94:95],.Lfk_meet3\n"
"v_or_b32 v118,v118,v79\n"
"v_and_b32 v118,0x40004000,v118\n"
"v_cmp_ne_u32 vcc,0,v118\n"
"s_nop 0\n"
"s_cbranch_vccz .Lfk_cr_g0\n"
"s_mov_b32 s91,15\n"
"s_call_b64 s[94:95],.Lfk_rare2\n"
"s_branch .Lfk_cr_g0\n"
".Lfk_cs_g1:\n"
"s_call_b64 s[94:95],.Lfk_meet3\n"
"v_or_b32 v118,v118,v95\n"
"v_and_b32 v118,0x40004000,v118\n"
"v_cmp_ne_u32 vcc,0,v118\n"
"s_nop 0\n"
"s_cbranch_vccz .Lfk_cr_g1\n"
"s_mov_b32 s91,15\n"
"s_call_b64 s[94:95],.Lfk_rare3\n"
"s_branch .Lfk_cr_g1\n"
".Lfk_cs_g2:\n"
"s_call_b64 s[94:95],.Lfk_meet3\n"
"v_or_b32 v118,v118,v79\n"
"v_and_b32 v118,0x40004000,v118\n"
"v_cmp_ne_u32 vcc,0,v118\n"
"s_nop 0\n"
"s_cbranch_vccz .Lfk_cr_g2\n"
"s_mov_b32 s91,15\n"
"s_call_b64 s[94:95],.Lfk_rare2\n"
"s_branch .Lfk_cr_g2\n"
".Lfk_meet3:\n"
"s_sub_u32 s82,s101,0\n"
"s_mov_b32 s84,0x1000000\n"
".Lfk_meetl3:\n"
"ds_read_b32 v125,v124\n"
"s_sub_u32 s84,s84,1\n"
"s_cbranch_scc1 .Lfk_meetx3\n"
"s_waitcnt lgkmcnt(0)\n"
"v_cmp_gt_u32 vcc,s82,v125\n"
"s_nop 0\n"
"s_cbranch_vccnz .Lfk_meetl3\n"
".Lfk_meetx3:\n"
"s_setpc_b64 s[94:95]\n"
".Lfk_Fx:\n"
"s_mov_b32 s82,2146435072\n"
"buffer_load_dword a148,off,s[24:27],s82\n"
"buffer_load_dword a148,off,s[24:27],s82\n"
"buffer_load_dword a148,off,s[24:27],s82\n"
"buffer_load_dword a148,off,s[24:27],s82\n"
"s_cmp_ge_u32 s52,2\n"
"s_cbranch_scc1 .Lfk_winA\n"
".Lfk_H:\n"
"v_mfma_f32_16x16x32_bf16 v[0:3],a[64:67],a[0:3],v[96:99]\n"
"s_swappc_b64 s[94:95],s[20:21]\n"
"s_add_u32 s101,s101,4\n"
"v_exp_f32 v122,v32\n"
"v_mfma_f32_16x16x32_bf16 v[0:3],a[68:71],a[4:7],v[0:3]\n"
"s_cmp_eq_u32 s64,s65\n"
"v_exp_f32 v123,v33\n"
"v_mfma_f32_16x16x32_bf16 v[0:3],a[72:75],a[8:11],v[0:3]\n"
"ds_read_b128 a[96:99],v114 offset:24576\n"
"ds_read_b128 a[100:103],v114 offset:25600\n"
"v_mfma_f32_16x16x32_bf16 v[0:3],a[76:79],a[12:15],v[0:3]\n"
"s_cselect_b32 s64,s66,s64\n"
"v_exp_f32 v119,v34\n"
"v_mfma_f32_16x16x32_bf16 v[4:7],a[92:95],a[12:15],v[96:99]\n"
"ds_read_b128 a[104:107],v114 offset:26624\n"
"v_cvt_pk_bf16_f32 v80,v122,v123\n"
"v_mfma_f32_16x16x32_bf16 v[4:7],a[88:91],a[8:11],v[4:7]\n"
"v_exp_f32 v120,v35\n"
"v_mfma_f32_16x16x32_bf16 v[4:7],a[84:87],a[4:7],v[4:7]\n"
"ds_read_b128 a[108:111],v114 offset:27648\n"
"ds_read_b128 a[112:115],v114 offset:28672\n"
"v_mfma_f32_16x16x32_bf16 v[4:7],a[80:83],a[0:3],v[4:7]\n"
"v_exp_f32 v122,v36\n"
"v_mfma_f32_16x16x32_bf16 v[12:15],a[80:83],a[16:19],v[100:103]\n"
"ds_read_b128 a[116:119],v114 offset:29696\n"
"v_cvt_pk_bf16_f32 v81,v119,v120\n"
"v_mfma_f32_16x16x32_bf16 v[12:15],a[84:87],a[20:23],v[12:15]\n"
"v_exp_f32 v123,v37\n"
"v_mfma_f32_16x16x32_bf16 v[12:15],a[88:91],a[24:27],v[12:15]\n"
"ds_read_b128 a[120:123],v114 offset:30720\n"
"ds_read_b128 a[124:127],v114 offset:31744\n"
"v_mfma_f32_16x16x32_bf16 v[12:15],a[92:95],a[28:31],v[12:15]\n"
"s_add_u32 m0,s67,0\n"
"v_exp_f32 v119,v38\n"
"v_mfma_f32_16x16x32_bf16 v[8:11],a[76:79],a[28:31],v[100:103]\n"
"v_cvt_pk_bf16_f32 v82,v122,v123\n"
"v_or3_b32 v118,v80,v81,v82\n"
"v_mfma_f32_16x16x32_bf16 v[8:11],a[72:75],a[24:27],v[8:11]\n"
"v_exp_f32 v120,v39\n"
"v_mfma_f32_16x16x32_bf16 v[8:11],a[68:71],a[20:23],v[8:11]\n"
"buffer_load_dwordx4 v112,s[24:27],s64 offen offset:0 sc1 lds\n"
"v_mfma_f32_16x16x32_bf16 v[8:11],a[64:67],a[16:19],v[8:11]\n"
"v_exp_f32 v122,v40\n"
"v_mfma_f32_16x16x32_bf16 v[16:19],a[64:67],a[32:35],v[104:107]\n"
"v_cvt_pk_bf16_f32 v83,v119,v120\n"
"v_mfma_f32_16x16x32_bf16 v[16:19],a[68:71],a[36:39],v[16:19]\n"
"v_exp_f32 v123,v41\n"
"v_mfma_f32_16x16x32_bf16 v[16:19],a[72:75],a[40:43],v[16:19]\n"
"buffer_load_dwordx4 v112,s[24:27],s64 offen offset:1024 sc1 lds\n"
"v_mfma_f32_16x16x32_bf16 v[16:19],a[76:79],a[44:47],v[16:19]\n"
"v_exp_f32 v119,v42\n"
"v_mfma_f32_16x16x32_bf16 v[20:23],a[92:95],a[44:47],v[104:107]\n"
"v_cvt_pk_bf16_f32 v84,v122,v123\n"
"v_or3_b32 v118,v118,v83,v84\n"
"v_mfma_f32_16x16x32_bf16 v[20:23],a[88:91],a[40:43],v[20:23]\n"
"v_exp_f32 v120,v43\n"
"v_mfma_f32_16x16x32_bf16 v[20:23],a[84:87],a[36:39],v[20:23]\n"
"v_cvt_pk_bf16_f32 v85,v119,v120\n"
"v_mfma_f32_16x16x32_bf16 v[20:23],a[80:83],a[32:35],v[20:23]\n"
"v_exp_f32 v122,v44\n"
"v_mfma_f32_16x16x32_bf16 v[28:31],a[80:83],a[48:51],v[108:111]\n"
"buffer_load_dwordx4 v112,s[24:27],s64 offen offset:2048 sc1 lds\n"
"v_mfma_f32_16x16x32_bf16 v[28:31],a[84:87],a[52:55],v[28:31]\n"
"v_exp_f32 v123,v45\n"
"v_mfma_f32_16x16x32_bf16 v[28:31],a[88:91],a[56:59],v[28:31]\n"
"v_cvt_pk_bf16_f32 v86,v122,v123\n"
"v_or3_b32 v118,v118,v85,v86\n"
"v_mfma_f32_16x16x32_bf16 v[28:31],a[92:95],a[60:63],v[28:31]\n"
"v_exp_f32 v119,v46\n"
"v_mfma_f32_16x16x32_bf16 v[24:27],a[76:79],a[60:63],v[108:111]\n"
"v_mfma_f32_16x16x32_bf16 v[24:27],a[72:75],a[56:59],v[24:27]\n"
"v_exp_f32 v120,v47\n"
"v_mfma_f32_16x16x32_bf16 v[24:27],a[68:71],a[52:55],v[24:27]\n"
"buffer_load_dwordx4 v112,s[24:27],s64 offen offset:3072 sc1 lds\n"
"v_mfma_f32_16x16x32_bf16 v[24:27],a[64:67],a[48:51],v[24:27]\n"
"s_waitcnt lgkmcnt(0)\n"
"v_exp_f32 v122,v48\n"
"v_mfma_f32_16x16x32_bf16 v[128:131],a[96:99],v[64:67],v[128:131]\n"
"v_exp_f32 v123,v49\n"
"v_mfma_f32_16x16x32_bf16 v[160:163],a[96:99],v[68:71],v[160:163]\n"
"ds_read_b128 a[64:67],v113 offset:16384\n"
"v_cvt_pk_bf16_f32 v87,v119,v120\n"
"v_mfma_f32_16x16x32_bf16 v[192:195],a[96:99],v[72:75],v[192:195]\n"
"s_add_u32 m0,s67,4096\n"
"v_exp_f32 v119,v50\n"
"v_mfma_f32_16x16x32_bf16 v[224:227],a[96:99],v[76:79],v[224:227]\n"
"ds_read_b128 a[68:71],v113 offset:17408\n"
"v_cvt_pk_bf16_f32 v88,v122,v123\n"
"v_mfma_f32_16x16x32_bf16 a[140:143],a[144:147],v[76:79],a[140:143]\n"
"v_exp_f32 v120,v51\n"
"v_mfma_f32_16x16x32_bf16 v[228:231],a[100:103],v[76:79],v[228:231]\n"
"ds_read_b128 a[72:75],v113 offset:18432\n"
"ds_read_b128 a[76:79],v113 offset:19456\n"
"v_mfma_f32_16x16x32_bf16 v[132:135],a[100:103],v[64:67],v[132:135]\n"
"v_exp_f32 v122,v52\n"
"v_mfma_f32_16x16x32_bf16 v[164:167],a[100:103],v[68:71],v[164:167]\n"
"ds_read_b128 a[80:83],v113 offset:20480\n"
"v_cvt_pk_bf16_f32 v89,v119,v120\n"
"v_mfma_f32_16x16x32_bf16 v[196:199],a[100:103],v[72:75],v[196:199]\n"
"v_exp_f32 v123,v53\n"
"v_mfma_f32_16x16x32_bf16 a[136:139],a[144:147],v[72:75],a[136:139]\n"
"ds_read_b128 a[84:87],v113 offset:21504\n"
"ds_read_b128 a[88:91],v113 offset:22528\n"
"v_mfma_f32_16x16x32_bf16 v[200:203],a[104:107],v[72:75],v[200:203]\n"
"v_exp_f32 v119,v54\n"
"v_mfma_f32_16x16x32_bf16 v[232:235],a[104:107],v[76:79],v[232:235]\n"
"ds_read_b128 a[92:95],v113 offset:23552\n"
"v_cvt_pk_bf16_f32 v90,v122,v123\n"
"v_mfma_f32_16x16x32_bf16 v[136:139],a[104:107],v[64:67],v[136:139]\n"
"v_exp_f32 v120,v55\n"
"v_mfma_f32_16x16x32_bf16 v[168:171],a[104:107],v[68:71],v[168:171]\n"
"s_waitcnt vmcnt(15)\n"
"ds_add_u32 v124,a152\n"
"v_mfma_f32_16x16x32_bf16 a[132:135],a[144:147],v[68:71],a[132:135]\n"
"v_exp_f32 v122,v56\n"
"v_mfma_f32_16x16x32_bf16 v[172:175],a[108:111],v[68:71],v[172:175]\n"
"v_cvt_pk_bf16_f32 v91,v119,v120\n"
"v_or3_b32 v118,v118,v87,v88\n"
"v_mfma_f32_16x16x32_bf16 v[204:207],a[108:111],v[72:75],v[204:207]\n"
"v_exp_f32 v123,v57\n"
"v_mfma_f32_16x16x32_bf16 v[236:239],a[108:111],v[76:79],v[236:239]\n"
"buffer_load_dwordx4 v117,s[24:27],s64 offen offset:0 sc1 lds\n"
"v_mfma_f32_16x16x32_bf16 v[140:143],a[108:111],v[64:67],v[140:143]\n"
"v_exp_f32 v119,v58\n"
"v_mfma_f32_16x16x32_bf16 a[128:131],a[144:147],v[64:67],a[128:131]\n"
"v_cvt_pk_bf16_f32 v92,v122,v123\n"
"v_or3_b32 v118,v118,v89,v90\n"
"v_mfma_f32_16x16x32_bf16 v[144:147],a[112:115],v[64:67],v[144:147]\n"
"v_exp_f32 v120,v59\n"
"v_mfma_f32_16x16x32_bf16 v[176:179],a[112:115],v[68:71],v[176:179]\n"
"buffer_load_dwordx4 v117,s[24:27],s64 offen offset:1024 sc1 lds\n"
"v_mfma_f32_16x16x32_bf16 v[208:211],a[112:115],v[72:75],v[208:211]\n"
"v_exp_f32 v122,v60\n"
"v_mfma_f32_16x16x32_bf16 v[240:243],a[112:115],v[76:79],v[240:243]\n"
"v_cvt_pk_bf16_f32 v93,v119,v120\n"
"v_or3_b32 v118,v118,v91,v92\n"
"v_mfma_f32_16x16x32_bf16 v[244:247],a[116:119],v[76:79],v[244:247]\n"
"v_exp_f32 v123,v61\n"
"v_mfma_f32_16x16x32_bf16 v[148:151],a[116:119],v[64:67],v[148:151]\n"
"buffer_load_dwordx4 v117,s[24:27],s64 offen offset:2048 sc1 lds\n"
"v_mfma_f32_16x16x32_bf16 v[180:183],a[116:119],v[68:71],v[180:183]\n"
"v_exp_f32 v119,v62\n"
"v_mfma_f32_16x16x32_bf16 v[212:215],a[116:119],v[72:75],v[212:215]\n"
"ds_read_b32 v125,v124\n"
"v_cvt_pk_bf16_f32 v94,v122,v123\n"
"v_mfma_f32_16x16x32_bf16 v[216:219],a[120:123],v[72:75],v[216:219]\n"
"v_exp_f32 v120,v63\n"
"v_mfma_f32_16x16x32_bf16 v[248:251],a[120:123],v[76:79],v[248:251]\n"
"buffer_load_dwordx4 v117,s[24:27],s64 offen offset:3072 sc1 lds\n"
"v_mfma_f32_16x16x32_bf16 v[152:155],a[120:123],v[64:67],v[152:155]\n"
"s_add_u32 s64,s64,16384\n"
"s_andn2_b32 m0,s55,16384\n"
"v_mfma_f32_16x16x32_bf16 v[184:187],a[120:123],v[68:71],v[184:187]\n"
"s_add_u32 s54,s54,s22\n"
"v_cvt_pk_bf16_f32 v95,v119,v120\n"
"v_mfma_f32_16x16x32_bf16 v[188:191],a[124:127],v[68:71],v[188:191]\n"
"s_waitcnt lgkmcnt(0)\n"
"v_subrev_u32 v125,s101,v125\n"
"v_or3_b32 v118,v118,v93,v94\n"
"v_mfma_f32_16x16x32_bf16 v[220:223],a[124:127],v[72:75],v[220:223]\n"
"v_or3_b32 v125,v118,v95,v125\n"
"v_and_b32 v125,0x40004000,v125\n"
"v_mfma_f32_16x16x32_bf16 v[252:255],a[124:127],v[76:79],v[252:255]\n"
"buffer_load_dwordx4 v115,s[32:35],s54 offen sc1 lds\n"
"buffer_store_dwordx4 a[240:243],v112,s[24:27],s56 offen offset:0 sc1\n"
"v_cmp_ne_u32 vcc,0,v125\n"
"v_mfma_f32_16x16x32_bf16 v[156:159],a[124:127],v[64:67],v[156:159]\n"
"s_cbranch_vccnz .Lfk_cs_g3\n"
".Lfk_cr_g3:\n"
"v_mfma_f32_16x16x32_bf16 v[32:35],a[64:67],a[0:3],v[96:99]\n"
"s_add_u32 s101,s101,4\n"
"v_exp_f32 v122,v0\n"
"v_mfma_f32_16x16x32_bf16 v[32:35],a[68:71],a[4:7],v[32:35]\n"
"s_add_u32 m0,s55,4096\n"
"v_exp_f32 v123,v1\n"
"v_mfma_f32_16x16x32_bf16 v[32:35],a[72:75],a[8:11],v[32:35]\n"
"ds_read_b128 a[96:99],v114 offset:57344\n"
"ds_read_b128 a[100:103],v114 offset:58368\n"
"v_mfma_f32_16x16x32_bf16 v[32:35],a[76:79],a[12:15],v[32:35]\n"
"s_add_u32 s54,s54,s22\n"
"v_exp_f32 v119,v2\n"
"v_mfma_f32_16x16x32_bf16 v[36:39],a[92:95],a[12:15],v[96:99]\n"
"ds_read_b128 a[104:107],v114 offset:59392\n"
"v_cvt_pk_bf16_f32 v64,v122,v123\n"
"v_mfma_f32_16x16x32_bf16 v[36:39],a[88:91],a[8:11],v[36:39]\n"
"v_exp_f32 v120,v3\n"
"v_mfma_f32_16x16x32_bf16 v[36:39],a[84:87],a[4:7],v[36:39]\n"
"ds_read_b128 a[108:111],v114 offset:60416\n"
"ds_read_b128 a[112:115],v114 offset:61440\n"
"v_mfma_f32_16x16x32_bf16 v[36:39],a[80:83],a[0:3],v[36:39]\n"
"v_exp_f32 v122,v4\n"
"v_mfma_f32_16x16x32_bf16 v[44:47],a[80:83],a[16:19],v[100:103]\n"
"ds_read_b128 a[116:119],v114 offset:62464\n"
"v_cvt_pk_bf16_f32 v65,v119,v120\n"
"v_mfma_f32_16x16x32_bf16 v[44:47],a[84:87],a[20:23],v[44:47]\n"
"v_exp_f32 v123,v5\n"
"v_mfma_f32_16x16x32_bf16 v[44:47],a[88:91],a[24:27],v[44:47]\n"
"ds_read_b128 a[120:123],v114 offset:63488\n"
"ds_read_b128 a[124:127],v114 offset:64512\n"
"v_mfma_f32_16x16x32_bf16 v[44:47],a[92:95],a[28:31],v[44:47]\n"
"v_exp_f32 v119,v6\n"
"v_mfma_f32_16x16x32_bf16 v[40:43],a[76:79],a[28:31],v[100:103]\n"
"v_cvt_pk_bf16_f32 v66,v122,v123\n"
"v_or3_b32 v118,v64,v65,v66\n"
"v_mfma_f32_16x16x32_bf16 v[40:43],a[72:75],a[24:27],v[40:43]\n"
"v_exp_f32 v120,v7\n"
"v_mfma_f32_16x16x32_bf16 v[40:43],a[68:71],a[20:23],v[40:43]\n"
"buffer_load_dwordx4 v115,s[32:35],s54 offen sc1 lds\n"
"v_mfma_f32_16x16x32_bf16 v[40:43],a[64:67],a[16:19],v[40:43]\n"
"s_add_u32 m0,s67,16384\n"
"v_exp_f32 v122,v8\n"
"v_mfma_f32_16x16x32_bf16 v[48:51],a[64:67],a[32:35],v[104:107]\n"
"v_cvt_pk_bf16_f32 v67,v119,v120\n"
"v_mfma_f32_16x16x32_bf16 v[48:51],a[68:71],a[36:39],v[48:51]\n"
"v_exp_f32 v123,v9\n"
"v_mfma_f32_16x16x32_bf16 v[48:51],a[72:75],a[40:43],v[48:51]\n"
"buffer_load_dwordx4 v112,s[24:27],s64 offen offset:0 sc1 lds\n"
"v_mfma_f32_16x16x32_bf16 v[48:51],a[76:79],a[44:47],v[48:51]\n"
"v_exp_f32 v119,v10\n"
"v_mfma_f32_16x16x32_bf16 v[52:55],a[92:95],a[44:47],v[104:107]\n"
"v_cvt_pk_bf16_f32 v68,v122,v123\n"
"v_or3_b32 v118,v118,v67,v68\n"
"v_mfma_f32_16x16x32_bf16 v[52:55],a[88:91],a[40:43],v[52:55]\n"
"v_exp_f32 v120,v11\n"
"v_mfma_f32_16x16x32_bf16 v[52:55],a[84:87],a[36:39],v[52:55]\n"
"buffer_load_dwordx4 v112,s[24:27],s64 offen offset:1024 sc1 lds\n"
"v_mfma_f32_16x16x32_bf16 v[52:55],a[80:83],a[32:35],v[52:55]\n"
"v_exp_f32 v122,v12\n"
"v_mfma_f32_16x16x32_bf16 v[60:63],a[80:83],a[48:51],v[108:111]\n"
"v_cvt_pk_bf16_f32 v69,v119,v120\n"
"v_mfma_f32_16x16x32_bf16 v[60:63],a[84:87],a[52:55],v[60:63]\n"
"v_exp_f32 v123,v13\n"
"v_mfma_f32_16x16x32_bf16 v[60:63],a[88:91],a[56:59],v[60:63]\n"
"buffer_load_dwordx4 v112,s[24:27],s64 offen offset:2048 sc1 lds\n"
"v_mfma_f32_16x16x32_bf16 v[60:63],a[92:95],a[60:63],v[60:63]\n"
"v_exp_f32 v119,v14\n"
"v_mfma_f32_16x16x32_bf16 v[56:59],a[76:79],a[60:63],v[108:111]\n"
"v_cvt_pk_bf16_f32 v70,v122,v123\n"
"v_or3_b32 v118,v118,v69,v70\n"
"v_mfma_f32_16x16x32_bf16 v[56:59],a[72:75],a[56:59],v[56:59]\n"
"v_exp_f32 v120,v15\n"
"v_mfma_f32_16x16x32_bf16 v[56:59],a[68:71],a[52:55],v[56:59]\n"
"buffer_load_dwordx4 v112,s[24:27],s64 offen offset:3072 sc1 lds\n"
"v_mfma_f32_16x16x32_bf16 v[56:59],a[64:67],a[48:51],v[56:59]\n"
"s_waitcnt lgkmcnt(0)\n"
"v_exp_f32 v122,v16\n"
"v_mfma_f32_16x16x32_bf16 v[128:131],a[96:99],v[80:83],v[128:131]\n"
"v_exp_f32 v123,v17\n"
"v_mfma_f32_16x16x32_bf16 v[160:163],a[96:99],v[84:87],v[160:163]\n"
"ds_read_b128 a[64:67],v113 offset:49152\n"
"v_cvt_pk_bf16_f32 v71,v119,v120\n"
"v_mfma_f32_16x16x32_bf16 v[192:195],a[96:99],v[88:91],v[192:195]\n"
"s_add_u32 m0,s67,20480\n"
"v_exp_f32 v119,v18\n"
"v_mfma_f32_16x16x32_bf16 v[224:227],a[96:99],v[92:95],v[224:227]\n"
"ds_read_b128 a[68:71],v113 offset:50176\n"
"v_cvt_pk_bf16_f32 v72,v122,v123\n"
"v_mfma_f32_16x16x32_bf16 a[140:143],a[144:147],v[92:95],a[140:143]\n"
"v_exp_f32 v120,v19\n"
"v_mfma_f32_16x16x32_bf16 v[228:231],a[100:103],v[92:95],v[228:231]\n"
"ds_read_b128 a[72:75],v113 offset:51200\n"
"ds_read_b128 a[76:79],v113 offset:52224\n"
"v_mfma_f32_16x16x32_bf16 v[132:135],a[100:103],v[80:83],v[132:135]\n"
"v_exp_f32 v122,v20\n"
"v_mfma_f32_16x16x32_bf16 v[164:167],a[100:103],v[84:87],v[164:167]\n"
"ds_read_b128 a[80:83],v113 offset:53248\n"
"v_cvt_pk_bf16_f32 v73,v119,v120\n"
"v_mfma_f32_16x16x32_bf16 v[196:199],a[100:103],v[88:91],v[196:199]\n"
"v_exp_f32 v123,v21\n"
"v_mfma_f32_16x16x32_bf16 a[136:139],a[144:147],v[88:91],a[136:139]\n"
"ds_read_b128 a[84:87],v113 offset:54272\n"
"ds_read_b128 a[88:91],v113 offset:55296\n"
"v_mfma_f32_16x16x32_bf16 v[200:203],a[104:107],v[88:91],v[200:203]\n"
"v_exp_f32 v119,v22\n"
"v_mfma_f32_16x16x32_bf16 v[232:235],a[104:107],v[92:95],v[232:235]\n"
"ds_read_b128 a[92:95],v113 offset:56320\n"
"v_cvt_pk_bf16_f32 v74,v122,v123\n"
"v_mfma_f32_16x16x32_bf16 v[136:139],a[104:107],v[80:83],v[136:139]\n"
"v_exp_f32 v120,v23\n"
"v_mfma_f32_16x16x32_bf16 v[168:171],a[104:107],v[84:87],v[168:171]\n"
"s_waitcnt vmcnt(16)\n"
"ds_add_u32 v124,a152\n"
"v_mfma_f32_16x16x32_bf16 a[132:135],a[144:147],v[84:87],a[132:135]\n"
"v_exp_f32 v122,v24\n"
"v_mfma_f32_16x16x32_bf16 v[172:175],a[108:111],v[84:87],v[172:175]\n"
"v_cvt_pk_bf16_f32 v75,v119,v120\n"
"v_or3_b32 v118,v118,v71,v72\n"
"v_mfma_f32_16x16x32_bf16 v[204:207],a[108:111],v[88:91],v[204:207]\n"
"v_exp_f32 v123,v25\n"
"v_mfma_f32_16x16x32_bf16 v[236:239],a[108:111],v[92:95],v[236:239]\n"
"buffer_load_dwordx4 v117,s[24:27],s64 offen offset:0 sc1 lds\n"
"v_mfma_f32_16x16x32_bf16 v[140:143],a[108:111],v[80:83],v[140:143]\n"
"v_exp_f32 v119,v26\n"
"v_mfma_f32_16x16x32_bf16 a[128:131],a[144:147],v[80:83],a[128:131]\n"
"v_cvt_pk_bf16_f32 v76,v122,v123\n"
"v_or3_b32 v118,v118,v73,v74\n"
"v_mfma_f32_16x16x32_bf16 v[144:147],a[112:115],v[80:83],v[144:147]\n"
"v_exp_f32 v120,v27\n"
"v_mfma_f32_16x16x32_bf16 v[176:179],a[112:115],v[84:87],v[176:179]\n"
"buffer_load_dwordx4 v117,s[24:27],s64 offen offset:1024 sc1 lds\n"
"v_mfma_f32_16x16x32_bf16 v[208:211],a[112:115],v[88:91],v[208:211]\n"
"v_exp_f32 v122,v28\n"
"v_mfma_f32_16x16x32_bf16 v[240:243],a[112:115],v[92:95],v[240:243]\n"
"v_cvt_pk_bf16_f32 v77,v119,v120\n"
"v_or3_b32 v118,v118,v75,v76\n"
"v_mfma_f32_16x16x32_bf16 v[244:247],a[116:119],v[92:95],v[244:247]\n"
"v_exp_f32 v123,v29\n"
"v_mfma_f32_16x16x32_bf16 v[148:151],a[116:119],v[80:83],v[148:151]\n"
"buffer_load_dwordx4 v117,s[24:27],s64 offen offset:2048 sc1 lds\n"
"v_mfma_f32_16x16x32_bf16 v[180:183],a[116:119],v[84:87],v[180:183]\n"
"v_exp_f32 v119,v30\n"
"v_mfma_f32_16x16x32_bf16 v[212:215],a[116:119],v[88:91],v[212:215]\n"
"ds_read_b32 v125,v124\n"
"v_cvt_pk_bf16_f32 v78,v122,v123\n"
"v_mfma_f32_16x16x32_bf16 v[216:219],a[120:123],v[88:91],v[216:219]\n"
"v_exp_f32 v120,v31\n"
"v_mfma_f32_16x16x32_bf16 v[248:251],a[120:123],v[92:95],v[248:251]\n"
"buffer_load_dwordx4 v117,s[24:27],s64 offen offset:3072 sc1 lds\n"
"v_mfma_f32_16x16x32_bf16 v[152:155],a[120:123],v[80:83],v[152:155]\n"
"buffer_store_dwordx4 a[244:247],v112,s[24:27],s56 offen offset:1024 sc1\n"
"v_mfma_f32_16x16x32_bf16 v[184:187],a[120:123],v[84:87],v[184:187]\n"
"s_add_u32 s64,s64,16384\n"
"v_cvt_pk_bf16_f32 v79,v119,v120\n"
"v_or3_b32 v118,v118,v77,v78\n"
"v_mfma_f32_16x16x32_bf16 v[188:191],a[124:127],v[84:87],v[188:191]\n"
"s_waitcnt lgkmcnt(0)\n"
"v_subrev_u32 v125,s101,v125\n"
"v_or3_b32 v125,v118,v79,v125\n"
"v_mfma_f32_16x16x32_bf16 v[220:223],a[124:127],v[88:91],v[220:223]\n"
"v_and_b32 v125,0x40004000,v125\n"
"v_cmp_ne_u32 vcc,0,v125\n"
"v_mfma_f32_16x16x32_bf16 v[252:255],a[124:127],v[92:95],v[252:255]\n"
"v_mfma_f32_16x16x32_bf16 v[156:159],a[124:127],v[80:83],v[156:159]\n"
"s_cbranch_vccnz .Lfk_cs_g0\n"
".Lfk_cr_g0:\n"
"v_mfma_f32_16x16x32_bf16 v[0:3],a[64:67],a[0:3],v[96:99]\n"
"s_add_u32 s101,s101,4\n"
"v_exp_f32 v122,v32\n"
"v_mfma_f32_16x16x32_bf16 v[0:3],a[68:71],a[4:7],v[0:3]\n"
"s_add_u32 m0,s55,8192\n"
"v_exp_f32 v123,v33\n"
"v_mfma_f32_16x16x32_bf16 v[0:3],a[72:75],a[8:11],v[0:3]\n"
"ds_read_b128 a[96:99],v114 offset:40960\n"
"ds_read_b128 a[100:103],v114 offset:41984\n"
"v_mfma_f32_16x16x32_bf16 v[0:3],a[76:79],a[12:15],v[0:3]\n"
"s_add_u32 s54,s54,s22\n"
"v_exp_f32 v119,v34\n"
"v_mfma_f32_16x16x32_bf16 v[4:7],a[92:95],a[12:15],v[96:99]\n"
"ds_read_b128 a[104:107],v114 offset:43008\n"
"v_cvt_pk_bf16_f32 v80,v122,v123\n"
"v_mfma_f32_16x16x32_bf16 v[4:7],a[88:91],a[8:11],v[4:7]\n"
"v_exp_f32 v120,v35\n"
"v_mfma_f32_16x16x32_bf16 v[4:7],a[84:87],a[4:7],v[4:7]\n"
"ds_read_b128 a[108:111],v114 offset:44032\n"
"ds_read_b128 a[112:115],v114 offset:45056\n"
"v_mfma_f32_16x16x32_bf16 v[4:7],a[80:83],a[0:3],v[4:7]\n"
"v_exp_f32 v122,v36\n"
"v_mfma_f32_16x16x32_bf16 v[12:15],a[80:83],a[16:19],v[100:103]\n"
"ds_read_b128 a[116:119],v114 offset:46080\n"
"v_cvt_pk_bf16_f32 v81,v119,v120\n"
"v_mfma_f32_16x16x32_bf16 v[12:15],a[84:87],a[20:23],v[12:15]\n"
"v_exp_f32 v123,v37\n"
"v_mfma_f32_16x16x32_bf16 v[12:15],a[88:91],a[24:27],v[12:15]\n"
"ds_read_b128 a[120:123],v114 offset:47104\n"
"ds_read_b128 a[124:127],v114 offset:48128\n"
"v_mfma_f32_16x16x32_bf16 v[12:15],a[92:95],a[28:31],v[12:15]\n"
"v_exp_f32 v119,v38\n"
"v_mfma_f32_16x16x32_bf16 v[8:11],a[76:79],a[28:31],v[100:103]\n"
"v_cvt_pk_bf16_f32 v82,v122,v123\n"
"v_or3_b32 v118,v80,v81,v82\n"
"v_mfma_f32_16x16x32_bf16 v[8:11],a[72:75],a[24:27],v[8:11]\n"
"v_exp_f32 v120,v39\n"
"v_mfma_f32_16x16x32_bf16 v[8:11],a[68:71],a[20:23],v[8:11]\n"
"buffer_load_dwordx4 v115,s[32:35],s54 offen sc1 lds\n"
"v_mfma_f32_16x16x32_bf16 v[8:11],a[64:67],a[16:19],v[8:11]\n"
"s_add_u32 m0,s67,49152\n"
"v_exp_f32 v122,v40\n"
"v_mfma_f32_16x16x32_bf16 v[16:19],a[64:67],a[32:35],v[104:107]\n"
"v_cvt_pk_bf16_f32 v83,v119,v120\n"
"v_mfma_f32_16x16x32_bf16 v[16:19],a[68:71],a[36:39],v[16:19]\n"
"v_exp_f32 v123,v41\n"
"v_mfma_f32_16x16x32_bf16 v[16:19],a[72:75],a[40:43],v[16:19]\n"
"buffer_load_dwordx4 v112,s[24:27],s64 offen offset:0 sc1 lds\n"
"v_mfma_f32_16x16x32_bf16 v[16:19],a[76:79],a[44:47],v[16:19]\n"
"v_exp_f32 v119,v42\n"
"v_mfma_f32_16x16x32_bf16 v[20:23],a[92:95],a[44:47],v[104:107]\n"
"v_cvt_pk_bf16_f32 v84,v122,v123\n"
"v_or3_b32 v118,v118,v83,v84\n"
"v_mfma_f32_16x16x32_bf16 v[20:23],a[88:91],a[40:43],v[20:23]\n"
"v_exp_f32 v120,v43\n"
"v_mfma_f32_16x16x32_bf16 v[20:23],a[84:87],a[36:39],v[20:23]\n"
"buffer_load_dwordx4 v112,s[24:27],s64 offen offset:1024 sc1 lds\n"
"v_mfma_f32_16x16x32_bf16 v[20:23],a[80:83],a[32:35],v[20:23]\n"
"v_exp_f32 v122,v44\n"
"v_mfma_f32_16x16x32_bf16 v[28:31],a[80:83],a[48:51],v[108:111]\n"
"v_cvt_pk_bf16_f32 v85,v119,v120\n"
"v_mfma_f32_16x16x32_bf16 v[28:31],a[84:87],a[52:55],v[28:31]\n"
"v_exp_f32 v123,v45\n"
"v_mfma_f32_16x16x32_bf16 v[28:31],a[88:91],a[56:59],v[28:31]\n"
"buffer_load_dwordx4 v112,s[24:27],s64 offen offset:2048 sc1 lds\n"
"v_mfma_f32_16x16x32_bf16 v[28:31],a[92:95],a[60:63],v[28:31]\n"
"v_exp_f32 v119,v46\n"
"v_mfma_f32_16x16x32_bf16 v[24:27],a[76:79],a[60:63],v[108:111]\n"
"v_cvt_pk_bf16_f32 v86,v122,v123\n"
"v_or3_b32 v118,v118,v85,v86\n"
"v_mfma_f32_16x16x32_bf16 v[24:27],a[72:75],a[56:59],v[24:27]\n"
"v_exp_f32 v120,v47\n"
"v_mfma_f32_16x16x32_bf16 v[24:27],a[68:71],a[52:55],v[24:27]\n"
"buffer_load_dwordx4 v112,s[24:27],s64 offen offset:3072 sc1 lds\n"
"v_mfma_f32_16x16x32_bf16 v[24:27],a[64:67],a[48:51],v[24:27]\n"
"s_waitcnt lgkmcnt(0)\n"
"v_exp_f32 v122,v48\n"
"v_mfma_f32_16x16x32_bf16 v[128:131],a[96:99],v[64:67],v[128:131]\n"
"v_exp_f32 v123,v49\n"
"v_mfma_f32_16x16x32_bf16 v[160:163],a[96:99],v[68:71],v[160:163]\n"
"ds_read_b128 a[64:67],v113 offset:32768\n"
"v_cvt_pk_bf16_f32 v87,v119,v120\n"
"v_mfma_f32_16x16x32_bf16 v[192:195],a[96:99],v[72:75],v[192:195]\n"
"s_add_u32 m0,s67,53248\n"
"v_exp_f32 v119,v50\n"
"v_mfma_f32_16x16x32_bf16 v[224:227],a[96:99],v[76:79],v[224:227]\n"
"ds_read_b128 a[68:71],v113 offset:33792\n"
"v_cvt_pk_bf16_f32 v88,v122,v123\n"
"v_mfma_f32_16x16x32_bf16 a[140:143],a[144:147],v[76:79],a[140:143]\n"
"v_exp_f32 v120,v51\n"
"v_mfma_f32_16x16x32_bf16 v[228:231],a[100:103],v[76:79],v[228:231]\n"
"ds_read_b128 a[72:75],v113 offset:34816\n"
"ds_read_b128 a[76:79],v113 offset:35840\n"
"v_mfma_f32_16x16x32_bf16 v[132:135],a[100:103],v[64:67],v[132:135]\n"
"v_exp_f32 v122,v52\n"
"v_mfma_f32_16x16x32_bf16 v[164:167],a[100:103],v[68:71],v[164:167]\n"
"ds_read_b128 a[80:83],v113 offset:36864\n"
"v_cvt_pk_bf16_f32 v89,v119,v120\n"
"v_mfma_f32_16x16x32_bf16 v[196:199],a[100:103],v[72:75],v[196:199]\n"
"v_exp_f32 v123,v53\n"
"v_mfma_f32_16x16x32_bf16 a[136:139],a[144:147],v[72:75],a[136:139]\n"
"ds_read_b128 a[84:87],v113 offset:37888\n"
"ds_read_b128 a[88:91],v113 offset:38912\n"
"v_mfma_f32_16x16x32_bf16 v[200:203],a[104:107],v[72:75],v[200:203]\n"
"v_exp_f32 v119,v54\n"
"v_mfma_f32_16x16x32_bf16 v[232:235],a[104:107],v[76:79],v[232:235]\n"
"ds_read_b128 a[92:95],v113 offset:39936\n"
"v_cvt_pk_bf16_f32 v90,v122,v123\n"
"v_mfma_f32_16x16x32_bf16 v[136:139],a[104:107],v[64:67],v[136:139]\n"
"v_exp_f32 v120,v55\n"
"v_mfma_f32_16x16x32_bf16 v[168:171],a[104:107],v[68:71],v[168:171]\n"
"s_waitcnt vmcnt(16)\n"
"ds_add_u32 v124,a152\n"
"v_mfma_f32_16x16x32_bf16 a[132:135],a[144:147],v[68:71],a[132:135]\n"
"v_exp_f32 v122,v56\n"
"v_mfma_f32_16x16x32_bf16 v[172:175],a[108:111],v[68:71],v[172:175]\n"
"v_cvt_pk_bf16_f32 v91,v119,v120\n"
"v_or3_b32 v118,v118,v87,v88\n"
"v_mfma_f32_16x16x32_bf16 v[204:207],a[108:111],v[72:75],v[204:207]\n"
"v_exp_f32 v123,v57\n"
"v_mfma_f32_16x16x32_bf16 v[236:239],a[108:111],v[76:79],v[236:239]\n"
"buffer_load_dwordx4 v117,s[24:27],s64 offen offset:0 sc1 lds\n"
"v_mfma_f32_16x16x32_bf16 v[140:143],a[108:111],v[64:67],v[140:143]\n"
"v_exp_f32 v119,v58\n"
"v_mfma_f32_16x16x32_bf16 a[128:131],a[144:147],v[64:67],a[128:131]\n"
"v_cvt_pk_bf16_f32 v92,v122,v123\n"
"v_or3_b32 v118,v118,v89,v90\n"
"v_mfma_f32_16x16x32_bf16 v[144:147],a[112:115],v[64:67],v[144:147]\n"
"v_exp_f32 v120,v59\n"
"v_mfma_f32_16x16x32_bf16 v[176:179],a[112:115],v[68:71],v[176:179]\n"
"buffer_load_dwordx4 v117,s[24:27],s64 offen offset:1024 sc1 lds\n"
"v_mfma_f32_16x16x32_bf16 v[208:211],a[112:115],v[72:75],v[208:211]\n"
"v_exp_f32 v122,v60\n"
"v_mfma_f32_16x16x32_bf16 v[240:243],a[112:115],v[76:79],v[240:243]\n"
"v_cvt_pk_bf16_f32 v93,v119,v120\n"
"v_or3_b32 v118,v118,v91,v92\n"
"v_mfma_f32_16x16x32_bf16 v[244:247],a[116:119],v[76:79],v[244:247]\n"
"v_exp_f32 v123,v61\n"
"v_mfma_f32_16x16x32_bf16 v[148:151],a[116:119],v[64:67],v[148:151]\n"
"buffer_load_dwordx4 v117,s[24:27],s64 offen offset:2048 sc1 lds\n"
"v_mfma_f32_16x16x32_bf16 v[180:183],a[116:119],v[68:71],v[180:183]\n"
"v_exp_f32 v119,v62\n"
"v_mfma_f32_16x16x32_bf16 v[212:215],a[116:119],v[72:75],v[212:215]\n"
"ds_read_b32 v125,v124\n"
"v_cvt_pk_bf16_f32 v94,v122,v123\n"
"v_mfma_f32_16x16x32_bf16 v[216:219],a[120:123],v[72:75],v[216:219]\n"
"v_exp_f32 v120,v63\n"
"v_mfma_f32_16x16x32_bf16 v[248:251],a[120:123],v[76:79],v[248:251]\n"
"buffer_load_dwordx4 v117,s[24:27],s64 offen offset:3072 sc1 lds\n"
"v_mfma_f32_16x16x32_bf16 v[152:155],a[120:123],v[64:67],v[152:155]\n"
"buffer_store_dwordx4 a[248:251],v112,s[24:27],s56 offen offset:2048 sc1\n"
"v_mfma_f32_16x16x32_bf16 v[184:187],a[120:123],v[68:71],v[184:187]\n"
"s_add_u32 s64,s64,16384\n"
"v_cvt_pk_bf16_f32 v95,v119,v120\n"
"v_or3_b32 v118,v118,v93,v94\n"
"v_mfma_f32_16x16x32_bf16 v[188:191],a[124:127],v[68:71],v[188:191]\n"
"s_waitcnt lgkmcnt(0)\n"
"v_subrev_u32 v125,s101,v125\n"
"v_or3_b32 v125,v118,v95,v125\n"
"v_mfma_f32_16x16x32_bf16 v[220:223],a[124:127],v[72:75],v[220:223]\n"
"v_and_b32 v125,0x40004000,v125\n"
"v_cmp_ne_u32 vcc,0,v125\n"
"v_mfma_f32_16x16x32_bf16 v[252:255],a[124:127],v[76:79],v[252:255]\n"
"v_mfma_f32_16x16x32_bf16 v[156:159],a[124:127],v[64:67],v[156:159]\n"
"s_cbranch_vccnz .Lfk_cs_g1\n"
".Lfk_cr_g1:\n"
"v_mfma_f32_16x16x32_bf16 v[32:35],a[64:67],a[0:3],v[96:99]\n"
"s_add_u32 s101,s101,4\n"
"v_exp_f32 v122,v0\n"
"v_mfma_f32_16x16x32_bf16 v[32:35],a[68:71],a[4:7],v[32:35]\n"
"s_add_u32 m0,s55,12288\n"
"v_exp_f32 v123,v1\n"
"v_mfma_f32_16x16x32_bf16 v[32:35],a[72:75],a[8:11],v[32:35]\n"
"ds_read_b128 a[96:99],v114 offset:8192\n"
"ds_read_b128 a[100:103],v114 offset:9216\n"
"v_mfma_f32_16x16x32_bf16 v[32:35],a[76:79],a[12:15],v[32:35]\n"
"s_add_u32 s54,s54,s22\n"
"v_exp_f32 v119,v2\n"
"v_mfma_f32_16x16x32_bf16 v[36:39],a[92:95],a[12:15],v[96:99]\n"
"ds_read_b128 a[104:107],v114 offset:10240\n"
"v_cvt_pk_bf16_f32 v64,v122,v123\n"
"v_mfma_f32_16x16x32_bf16 v[36:39],a[88:91],a[8:11],v[36:39]\n"
"v_exp_f32 v120,v3\n"
"v_mfma_f32_16x16x32_bf16 v[36:39],a[84:87],a[4:7],v[36:39]\n"
"ds_read_b128 a[108:111],v114 offset:11264\n"
"ds_read_b128 a[112:115],v114 offset:12288\n"
"v_mfma_f32_16x16x32_bf16 v[36:39],a[80:83],a[0:3],v[36:39]\n"
"v_exp_f32 v122,v4\n"
"v_mfma_f32_16x16x32_bf16 v[44:47],a[80:83],a[16:19],v[100:103]\n"
"ds_read_b128 a[116:119],v114 offset:13312\n"
"v_cvt_pk_bf16_f32 v65,v119,v120\n"
"v_mfma_f32_16x16x32_bf16 v[44:47],a[84:87],a[20:23],v[44:47]\n"
"v_exp_f32 v123,v5\n"
"v_mfma_f32_16x16x32_bf16 v[44:47],a[88:91],a[24:27],v[44:47]\n"
"ds_read_b128 a[120:123],v114 offset:14336\n"
"ds_read_b128 a[124:127],v114 offset:15360\n"
"v_mfma_f32_16x16x32_bf16 v[44:47],a[92:95],a[28:31],v[44:47]\n"
"v_exp_f32 v119,v6\n"
"v_mfma_f32_16x16x32_bf16 v[40:43],a[76:79],a[28:31],v[100:103]\n"
"v_cvt_pk_bf16_f32 v66,v122,v123\n"
"v_or3_b32 v118,v64,v65,v66\n"
"v_mfma_f32_16x16x32_bf16 v[40:43],a[72:75],a[24:27],v[40:43]\n"
"v_exp_f32 v120,v7\n"
"v_mfma_f32_16x16x32_bf16 v[40:43],a[68:71],a[20:23],v[40:43]\n"
"buffer_load_dwordx4 v115,s[32:35],s54 offen sc1 lds\n"
"v_mfma_f32_16x16x32_bf16 v[40:43],a[64:67],a[16:19],v[40:43]\n"
"s_cmp_eq_u32 s64,s65\n"
"v_exp_f32 v122,v8\n"
"v_mfma_f32_16x16x32_bf16 v[48:51],a[64:67],a[32:35],v[104:107]\n"
"v_cvt_pk_bf16_f32 v67,v119,v120\n"
"s_cselect_b32 s64,s66,s64\n"
"v_mfma_f32_16x16x32_bf16 v[48:51],a[68:71],a[36:39],v[48:51]\n"
"s_add_u32 m0,s67,32768\n"
"v_exp_f32 v123,v9\n"
"v_mfma_f32_16x16x32_bf16 v[48:51],a[72:75],a[40:43],v[48:51]\n"
"buffer_load_dwordx4 v112,s[24:27],s64 offen offset:0 sc1 lds\n"
"v_mfma_f32_16x16x32_bf16 v[48:51],a[76:79],a[44:47],v[48:51]\n"
"v_exp_f32 v119,v10\n"
"v_mfma_f32_16x16x32_bf16 v[52:55],a[92:95],a[44:47],v[104:107]\n"
"v_cvt_pk_bf16_f32 v68,v122,v123\n"
"v_or3_b32 v118,v118,v67,v68\n"
"v_mfma_f32_16x16x32_bf16 v[52:55],a[88:91],a[40:43],v[52:55]\n"
"v_exp_f32 v120,v11\n"
"v_mfma_f32_16x16x32_bf16 v[52:55],a[84:87],a[36:39],v[52:55]\n"
"buffer_load_dwordx4 v112,s[24:27],s64 offen offset:1024 sc1 lds\n"
"v_mfma_f32_16x16x32_bf16 v[52:55],a[80:83],a[32:35],v[52:55]\n"
"v_exp_f32 v122,v12\n"
"v_mfma_f32_16x16x32_bf16 v[60:63],a[80:83],a[48:51],v[108:111]\n"
"v_cvt_pk_bf16_f32 v69,v119,v120\n"
"v_mfma_f32_16x16x32_bf16 v[60:63],a[84:87],a[52:55],v[60:63]\n"
"v_exp_f32 v123,v13\n"
"v_mfma_f32_16x16x32_bf16 v[60:63],a[88:91],a[56:59],v[60:63]\n"
"buffer_load_dwordx4 v112,s[24:27],s64 offen offset:2048 sc1 lds\n"
"v_mfma_f32_16x16x32_bf16 v[60:63],a[92:95],a[60:63],v[60:63]\n"
"v_exp_f32 v119,v14\n"
"v_mfma_f32_16x16x32_bf16 v[56:59],a[76:79],a[60:63],v[108:111]\n"
"v_cvt_pk_bf16_f32 v70,v122,v123\n"
"v_or3_b32 v118,v118,v69,v70\n"
"v_mfma_f32_16x16x32_bf16 v[56:59],a[72:75],a[56:59],v[56:59]\n"
"v_exp_f32 v120,v15\n"
"v_mfma_f32_16x16x32_bf16 v[56:59],a[68:71],a[52:55],v[56:59]\n"
"buffer_load_dwordx4 v112,s[24:27],s64 offen offset:3072 sc1 lds\n"
"v_mfma_f32_16x16x32_bf16 v[56:59],a[64:67],a[48:51],v[56:59]\n"
"s_waitcnt lgkmcnt(0)\n"
"v_exp_f32 v122,v16\n"
"v_mfma_f32_16x16x32_bf16 v[128:131],a[96:99],v[80:83],v[128:131]\n"
"v_exp_f32 v123,v17\n"
"v_mfma_f32_16x16x32_bf16 v[160:163],a[96:99],v[84:87],v[160:163]\n"
"ds_read_b128 a[64:67],v113 offset:0\n"
"v_cvt_pk_bf16_f32 v71,v119,v120\n"
"v_mfma_f32_16x16x32_bf16 v[192:195],a[96:99],v[88:91],v[192:195]\n"
"s_add_u32 m0,s67,36864\n"
"v_exp_f32 v119,v18\n"
"v_mfma_f32_16x16x32_bf16 v[224:227],a[96:99],v[92:95],v[224:227]\n"
"ds_read_b128 a[68:71],v113 offset:1024\n"
"v_cvt_pk_bf16_f32 v72,v122,v123\n"
"v_mfma_f32_16x16x32_bf16 a[140:143],a[144:147],v[92:95],a[140:143]\n"
"v_exp_f32 v120,v19\n"
"v_mfma_f32_16x16x32_bf16 v[228:231],a[100:103],v[92:95],v[228:231]\n"
"ds_read_b128 a[72:75],v113 offset:2048\n"
"ds_read_b128 a[76:79],v113 offset:3072\n"
"v_mfma_f32_16x16x32_bf16 v[132:135],a[100:103],v[80:83],v[132:135]\n"
"v_exp_f32 v122,v20\n"
"v_mfma_f32_16x16x32_bf16 v[164:167],a[100:103],v[84:87],v[164:167]\n"
"ds_read_b128 a[80:83],v113 offset:4096\n"
"v_cvt_pk_bf16_f32 v73,v119,v120\n"
"v_mfma_f32_16x16x32_bf16 v[196:199],a[100:103],v[88:91],v[196:199]\n"
"v_exp_f32 v123,v21\n"
"v_mfma_f32_16x16x32_bf16 a[136:139],a[144:147],v[88:91],a[136:139]\n"
"ds_read_b128 a[84:87],v113 offset:5120\n"
"ds_read_b128 a[88:91],v113 offset:6144\n"
"v_mfma_f32_16x16x32_bf16 v[200:203],a[104:107],v[88:91],v[200:203]\n"
"v_exp_f32 v119,v22\n"
"v_mfma_f32_16x16x32_bf16 v[232:235],a[104:107],v[92:95],v[232:235]\n"
"ds_read_b128 a[92:95],v113 offset:7168\n"
"v_cvt_pk_bf16_f32 v74,v122,v123\n"
"v_mfma_f32_16x16x32_bf16 v[136:139],a[104:107],v[80:83],v[136:139]\n"
"v_exp_f32 v120,v23\n"
"v_mfma_f32_16x16x32_bf16 v[168:171],a[104:107],v[84:87],v[168:171]\n"
"s_waitcnt vmcnt(16)\n"
"ds_add_u32 v124,a152\n"
"v_mfma_f32_16x16x32_bf16 a[132:135],a[144:147],v[84:87],a[132:135]\n"
"v_exp_f32 v122,v24\n"
"v_mfma_f32_16x16x32_bf16 v[172:175],a[108:111],v[84:87],v[172:175]\n"
"v_cvt_pk_bf16_f32 v75,v119,v120\n"
"v_or3_b32 v118,v118,v71,v72\n"
"v_mfma_f32_16x16x32_bf16 v[204:207],a[108:111],v[88:91],v[204:207]\n"
"v_exp_f32 v123,v25\n"
"v_mfma_f32_16x16x32_bf16 v[236:239],a[108:111],v[92:95],v[236:239]\n"
"buffer_load_dwordx4 v117,s[24:27],s64 offen offset:0 sc1 lds\n"
"v_mfma_f32_16x16x32_bf16 v[140:143],a[108:111],v[80:83],v[140:143]\n"
"v_exp_f32 v119,v26\n"
"v_mfma_f32_16x16x32_bf16 a[128:131],a[144:147],v[80:83],a[128:131]\n"
"v_cvt_pk_bf16_f32 v76,v122,v123\n"
"v_or3_b32 v118,v118,v73,v74\n"
"v_mfma_f32_16x16x32_bf16 v[144:147],a[112:115],v[80:83],v[144:147]\n"
"v_exp_f32 v120,v27\n"
"v_mfma_f32_16x16x32_bf16 v[176:179],a[112:115],v[84:87],v[176:179]\n"
"buffer_load_dwordx4 v117,s[24:27],s64 offen offset:1024 sc1 lds\n"
"v_mfma_f32_16x16x32_bf16 v[208:211],a[112:115],v[88:91],v[208:211]\n"
"v_exp_f32 v122,v28\n"
"v_mfma_f32_16x16x32_bf16 v[240:243],a[112:115],v[92:95],v[240:243]\n"
"v_cvt_pk_bf16_f32 v77,v119,v120\n"
"v_or3_b32 v118,v118,v75,v76\n"
"v_mfma_f32_16x16x32_bf16 v[244:247],a[116:119],v[92:95],v[244:247]\n"
"v_exp_f32 v123,v29\n"
"v_mfma_f32_16x16x32_bf16 v[148:151],a[116:119],v[80:83],v[148:151]\n"
"buffer_load_dwordx4 v117,s[24:27],s64 offen offset:2048 sc1 lds\n"
"v_mfma_f32_16x16x32_bf16 v[180:183],a[116:119],v[84:87],v[180:183]\n"
"v_exp_f32 v119,v30\n"
"v_mfma_f32_16x16x32_bf16 v[212:215],a[116:119],v[88:91],v[212:215]\n"
"ds_read_b32 v125,v124\n"
"v_cvt_pk_bf16_f32 v78,v122,v123\n"
"v_mfma_f32_16x16x32_bf16 v[216:219],a[120:123],v[88:91],v[216:219]\n"
"v_exp_f32 v120,v31\n"
"v_mfma_f32_16x16x32_bf16 v[248:251],a[120:123],v[92:95],v[248:251]\n"
"buffer_load_dwordx4 v117,s[24:27],s64 offen offset:3072 sc1 lds\n"
"v_mfma_f32_16x16x32_bf16 v[152:155],a[120:123],v[80:83],v[152:155]\n"
"buffer_store_dwordx4 a[252:255],v112,s[24:27],s56 offen offset:3072 sc1\n"
"v_mfma_f32_16x16x32_bf16 v[184:187],a[120:123],v[84:87],v[184:187]\n"
"s_add_u32 s64,s64,16384\n"
"s_add_u32 s89,s89,1\n"
"v_cvt_pk_bf16_f32 v79,v119,v120\n"
"v_mfma_f32_16x16x32_bf16 v[188:191],a[124:127],v[84:87],v[188:191]\n"
"s_waitcnt lgkmcnt(0)\n"
"v_subrev_u32 v125,s101,v125\n"
"v_or3_b32 v118,v118,v77,v78\n"
"v_mfma_f32_16x16x32_bf16 v[220:223],a[124:127],v[88:91],v[220:223]\n"
"v_or3_b32 v125,v118,v79,v125\n"
"v_and_b32 v125,0x40004000,v125\n"
"v_mfma_f32_16x16x32_bf16 v[252:255],a[124:127],v[92:95],v[252:255]\n"
"v_cmp_ne_u32 vcc,0,v125\n"
"v_mfma_f32_16x16x32_bf16 v[156:159],a[124:127],v[80:83],v[156:159]\n"
"s_cbranch_vccnz .Lfk_cs_g2\n"
".Lfk_cr_g2:\n"
".Lfk_Hx:\n"
"s_cmp_lt_u32 s89,8\n"
"s_cbranch_scc1 .Lfk_H\n"
"v_max3_f32 v118,v32,v33,v34\n"
"v_max3_f32 v119,v35,v36,v37\n"
"v_max3_f32 v119,v119,v38,v39\n"
"v_max3_f32 v118,v118,v40,v41\n"
"v_max3_f32 v119,v119,v42,v43\n"
"v_max3_f32 v118,v118,v44,v45\n"
"v_max3_f32 v119,v119,v46,v47\n"
"v_max3_f32 v118,v118,v48,v49\n"
"v_max3_f32 v119,v119,v50,v51\n"
"v_max3_f32 v118,v118,v52,v53\n"
"v_max3_f32 v119,v119,v54,v55\n"
"v_max3_f32 v118,v118,v56,v57\n"
"v_max3_f32 v119,v119,v58,v59\n"
"v_max3_f32 v118,v118,v60,v61\n"
"v_max3_f32 v119,v119,v62,v63\n"
"v_max_f32 v118,v118,v119\n"
"v_cmp_lt_f32 vcc,s93,v118\n"
"s_nop 0\n"
"s_cbranch_vccnz .Lfk_cs_x1\n"
".Lfk_cr_x1:\n"
"s_cmp_ge_u32 s52,2\n"
"s_cbranch_scc1 .Lfk_tailB\n"
".Lfk_tailA:\n"
"s_add_u32 s82,s66,28672\n"
"s_cmp_eq_u32 s52,0\n"
"s_cselect_b32 s67,77824,s67\n"
"s_cselect_b32 s64,s82,s64\n"
"s_waitcnt vmcnt(11) lgkmcnt(0)\n"
"s_barrier\n"
"v_mfma_f32_16x16x32_bf16 v[16:19],a[64:67],a[32:35],v[104:107]\n"
"ds_read_b128 a[96:99],v114 offset:24576\n"
"ds_read_b128 a[100:103],v114 offset:25600\n"
"ds_read_b128 a[104:107],v114 offset:26624\n"
"v_mfma_f32_16x16x32_bf16 v[16:19],a[68:71],a[36:39],v[16:19]\n"
"ds_read_b128 a[108:111],v114 offset:27648\n"
"ds_read_b128 a[112:115],v114 offset:28672\n"
"ds_read_b128 a[116:119],v114 offset:29696\n"
"v_mfma_f32_16x16x32_bf16 v[16:19],a[72:75],a[40:43],v[16:19]\n"
"ds_read_b128 a[120:123],v114 offset:30720\n"
"ds_read_b128 a[124:127],v114 offset:31744\n"
"v_add_f32 v8,0x80000000,v126\n"
"v_mfma_f32_16x16x32_bf16 v[16:19],a[76:79],a[44:47],v[16:19]\n"
"v_min_f32 v8,v8,v100\n"
"v_add_f32 v9,0xf149f2ca,v126\n"
"v_min_f32 v9,v9,v101\n"
"v_mfma_f32_16x16x32_bf16 v[20:23],a[92:95],a[44:47],v[104:107]\n"
"v_add_f32 v10,0xf1c9f2ca,v126\n"
"v_min_f32 v10,v10,v102\n"
"v_add_f32 v11,0xf2177617,v126\n"
"v_mfma_f32_16x16x32_bf16 v[20:23],a[88:91],a[40:43],v[20:23]\n"
"v_min_f32 v11,v11,v103\n"
"v_add_f32 v12,0xf349f2ca,v126\n"
"v_min_f32 v12,v12,v100\n"
"v_mfma_f32_16x16x32_bf16 v[20:23],a[84:87],a[36:39],v[20:23]\n"
"v_add_f32 v13,0xf35691f6,v126\n"
"v_min_f32 v13,v13,v101\n"
"v_add_f32 v14,0xf3633123,v126\n"
"v_mfma_f32_16x16x32_bf16 v[20:23],a[80:83],a[32:35],v[20:23]\n"
"v_min_f32 v14,v14,v102\n"
"v_add_f32 v15,0xf36fd050,v126\n"
"v_min_f32 v15,v15,v103\n"
"v_mfma_f32_16x16x32_bf16 v[28:31],a[80:83],a[48:51],v[108:111]\n"
"s_add_u32 m0,s67,0\n"
"v_exp_f32 v32,v32\n"
"v_mfma_f32_16x16x32_bf16 v[28:31],a[84:87],a[52:55],v[28:31]\n"
"buffer_load_dwordx4 v112,s[24:27],s64 offen offset:0 sc1 lds\n"
"buffer_load_dwordx4 v112,s[24:27],s64 offen offset:1024 sc1 lds\n"
"v_exp_f32 v33,v33\n"
"v_mfma_f32_16x16x32_bf16 v[28:31],a[88:91],a[56:59],v[28:31]\n"
"v_exp_f32 v34,v34\n"
"v_cvt_pk_bf16_f32 v80,v32,v33\n"
"v_mfma_f32_16x16x32_bf16 v[28:31],a[92:95],a[60:63],v[28:31]\n"
"v_exp_f32 v35,v35\n"
"v_max3_f32 v118,v16,v17,v18\n"
"v_exp_f32 v36,v36\n"
"v_mfma_f32_16x16x32_bf16 v[24:27],a[76:79],a[60:63],v[108:111]\n"
"buffer_load_dwordx4 v112,s[24:27],s64 offen offset:2048 sc1 lds\n"
"v_mfma_f32_16x16x32_bf16 v[24:27],a[72:75],a[56:59],v[24:27]\n"
"v_exp_f32 v37,v37\n"
"v_cvt_pk_bf16_f32 v81,v34,v35\n"
"v_mfma_f32_16x16x32_bf16 v[24:27],a[68:71],a[52:55],v[24:27]\n"
"v_exp_f32 v38,v38\n"
"v_cvt_pk_bf16_f32 v82,v36,v37\n"
"v_exp_f32 v39,v39\n"
"v_mfma_f32_16x16x32_bf16 v[24:27],a[64:67],a[48:51],v[24:27]\n"
"buffer_load_dwordx4 v112,s[24:27],s64 offen offset:3072 sc1 lds\n"
"v_mfma_f32_16x16x32_bf16 v[8:11],a[64:67],a[16:19],v[8:11]\n"
"s_add_u32 s64,s64,16384\n"
"v_exp_f32 v40,v40\n"
"v_accvgpr_read_b32 v119,a150\n"
"v_mfma_f32_16x16x32_bf16 v[8:11],a[68:71],a[20:23],v[8:11]\n"
"v_exp_f32 v41,v41\n"
"v_accvgpr_read_b32 v120,a151\n"
"v_mfma_f32_16x16x32_bf16 v[8:11],a[72:75],a[24:27],v[8:11]\n"
"v_exp_f32 v42,v42\n"
"v_xor_b32 v119,s80,v119\n"
"v_mfma_f32_16x16x32_bf16 v[8:11],a[76:79],a[28:31],v[8:11]\n"
"v_exp_f32 v43,v43\n"
"v_xor_b32 v120,s81,v120\n"
"v_mfma_f32_16x16x32_bf16 v[12:15],a[92:95],a[28:31],v[12:15]\n"
"v_exp_f32 v44,v44\n"
"v_or_b32 v119,v119,v120\n"
"v_mfma_f32_16x16x32_bf16 v[12:15],a[88:91],a[24:27],v[12:15]\n"
"s_add_u32 s18,s8,0\n"
"v_exp_f32 v45,v45\n"
"v_cmp_ne_u32 s[14:15],0,v119\n"
"v_mfma_f32_16x16x32_bf16 v[12:15],a[84:87],a[20:23],v[12:15]\n"
"s_cmp_lg_u32 s14,0\n"
"s_cselect_b32 s18,2146435072,s18\n"
"v_exp_f32 v46,v46\n"
"v_mfma_f32_16x16x32_bf16 v[12:15],a[80:83],a[16:19],v[12:15]\n"
"s_add_u32 m0,s30,0\n"
"ds_read_b128 a[64:67],v113 offset:16384\n"
"s_waitcnt lgkmcnt(1)\n"
"v_mfma_f32_16x16x32_bf16 v[128:131],a[96:99],v[64:67],v[128:131]\n"
"ds_read_b128 a[68:71],v113 offset:17408\n"
"ds_read_b128 a[72:75],v113 offset:18432\n"
"ds_read_b128 a[76:79],v113 offset:19456\n"
"v_mfma_f32_16x16x32_bf16 v[160:163],a[96:99],v[68:71],v[160:163]\n"
"ds_read_b128 a[80:83],v113 offset:20480\n"
"v_exp_f32 v47,v47\n"
"v_mfma_f32_16x16x32_bf16 v[192:195],a[96:99],v[72:75],v[192:195]\n"
"ds_read_b128 a[84:87],v113 offset:21504\n"
"ds_read_b128 a[88:91],v113 offset:22528\n"
"ds_read_b128 a[92:95],v113 offset:23552\n"
"v_exp_f32 v48,v48\n"
"v_mfma_f32_16x16x32_bf16 v[224:227],a[96:99],v[76:79],v[224:227]\n"
"buffer_load_dwordx4 v112,s[24:27],s18 offen offset:0 sc1 lds\n"
"v_exp_f32 v49,v49\n"
"v_mfma_f32_16x16x32_bf16 a[140:143],a[144:147],v[76:79],a[140:143]\n"
"buffer_load_dwordx4 v112,s[24:27],s18 offen offset:1024 sc1 lds\n"
"v_mfma_f32_16x16x32_bf16 v[228:231],a[100:103],v[76:79],v[228:231]\n"
"buffer_load_dwordx4 v112,s[24:27],s18 offen offset:2048 sc1 lds\n"
"buffer_load_dwordx4 v112,s[24:27],s18 offen offset:3072 sc1 lds\n"
"v_mfma_f32_16x16x32_bf16 v[132:135],a[100:103],v[64:67],v[132:135]\n"
"s_add_u32 m0,m0,4096\n"
"v_exp_f32 v50,v50\n"
"v_cvt_pk_bf16_f32 v83,v38,v39\n"
"v_mfma_f32_16x16x32_bf16 v[164:167],a[100:103],v[68:71],v[164:167]\n"
"buffer_load_dwordx4 v117,s[24:27],s18 offen offset:0 sc1 lds\n"
"v_cvt_pk_bf16_f32 v84,v40,v41\n"
"v_cvt_pk_bf16_f32 v85,v42,v43\n"
"v_mfma_f32_16x16x32_bf16 v[196:199],a[100:103],v[72:75],v[196:199]\n"
"v_exp_f32 v51,v51\n"
"v_cvt_pk_bf16_f32 v86,v44,v45\n"
"v_mfma_f32_16x16x32_bf16 a[136:139],a[144:147],v[72:75],a[136:139]\n"
"v_exp_f32 v52,v52\n"
"v_cvt_pk_bf16_f32 v87,v46,v47\n"
"v_mfma_f32_16x16x32_bf16 v[200:203],a[104:107],v[72:75],v[200:203]\n"
"buffer_load_dwordx4 v117,s[24:27],s18 offen offset:1024 sc1 lds\n"
"v_cvt_pk_bf16_f32 v88,v48,v49\n"
"v_cvt_pk_bf16_f32 v89,v50,v51\n"
"v_mfma_f32_16x16x32_bf16 v[232:235],a[104:107],v[76:79],v[232:235]\n"
"v_exp_f32 v53,v53\n"
"v_max3_f32 v118,v118,v19,v20\n"
"v_mfma_f32_16x16x32_bf16 v[136:139],a[104:107],v[64:67],v[136:139]\n"
"v_cvt_pk_bf16_f32 v90,v52,v53\n"
"v_max3_f32 v118,v118,v21,v22\n"
"v_max3_f32 v118,v118,v23,v28\n"
"v_exp_f32 v54,v54\n"
"v_mfma_f32_16x16x32_bf16 v[168:171],a[104:107],v[68:71],v[168:171]\n"
"buffer_load_dwordx4 v117,s[24:27],s18 offen offset:2048 sc1 lds\n"
"v_mfma_f32_16x16x32_bf16 a[132:135],a[144:147],v[68:71],a[132:135]\n"
"v_exp_f32 v55,v55\n"
"v_max3_f32 v118,v118,v29,v30\n"
"v_mfma_f32_16x16x32_bf16 v[172:175],a[108:111],v[68:71],v[172:175]\n"
"v_cvt_pk_bf16_f32 v91,v54,v55\n"
"v_max3_f32 v118,v118,v31,v24\n"
"v_max3_f32 v118,v118,v25,v26\n"
"v_exp_f32 v56,v56\n"
"v_mfma_f32_16x16x32_bf16 v[204:207],a[108:111],v[72:75],v[204:207]\n"
"buffer_load_dwordx4 v117,s[24:27],s18 offen offset:3072 sc1 lds\n"
"v_mfma_f32_16x16x32_bf16 v[236:239],a[108:111],v[76:79],v[236:239]\n"
"s_sub_u32 s82,s3,32768\n"
"v_max3_f32 v118,v118,v27,v8\n"
"v_max3_f32 v118,v118,v9,v10\n"
"v_mfma_f32_16x16x32_bf16 v[140:143],a[108:111],v[64:67],v[140:143]\n"
"v_exp_f32 v57,v57\n"
"v_max3_f32 v118,v118,v11,v12\n"
"v_exp_f32 v58,v58\n"
"v_mfma_f32_16x16x32_bf16 a[128:131],a[144:147],v[64:67],a[128:131]\n"
"buffer_load_dwordx4 a[0:3],v112,s[24:27],s82 offen offset:0 sc1\n"
"v_mfma_f32_16x16x32_bf16 v[144:147],a[112:115],v[64:67],v[144:147]\n"
"v_max3_f32 v118,v118,v13,v14\n"
"v_max_f32 v118,v118,v15\n"
"v_mfma_f32_16x16x32_bf16 v[176:179],a[112:115],v[68:71],v[176:179]\n"
"v_exp_f32 v59,v59\n"
"v_mfma_f32_16x16x32_bf16 v[208:211],a[112:115],v[72:75],v[208:211]\n"
"v_cvt_pk_bf16_f32 v92,v56,v57\n"
"v_cvt_pk_bf16_f32 v93,v58,v59\n"
"v_exp_f32 v60,v60\n"
"v_mfma_f32_16x16x32_bf16 v[240:243],a[112:115],v[76:79],v[240:243]\n"
"buffer_load_dwordx4 a[4:7],v112,s[24:27],s82 offen offset:1024 sc1\n"
"v_mfma_f32_16x16x32_bf16 v[244:247],a[116:119],v[76:79],v[244:247]\n"
"v_exp_f32 v61,v61\n"
"v_mfma_f32_16x16x32_bf16 v[148:151],a[116:119],v[64:67],v[148:151]\n"
"v_cmp_lt_f32 vcc,s93,v118\n"
"v_cvt_pk_bf16_f32 v94,v60,v61\n"
"v_mfma_f32_16x16x32_bf16 v[180:183],a[116:119],v[68:71],v[180:183]\n"
"buffer_load_dwordx4 a[8:11],v112,s[24:27],s82 offen offset:2048 sc1\n"
"v_mfma_f32_16x16x32_bf16 v[212:215],a[116:119],v[72:75],v[212:215]\n"
"v_exp_f32 v62,v62\n"
"v_mfma_f32_16x16x32_bf16 v[216:219],a[120:123],v[72:75],v[216:219]\n"
"v_exp_f32 v63,v63\n"
"v_mfma_f32_16x16x32_bf16 v[248:251],a[120:123],v[76:79],v[248:251]\n"
"v_cvt_pk_bf16_f32 v95,v62,v63\n"
"v_mfma_f32_16x16x32_bf16 v[152:155],a[120:123],v[64:67],v[152:155]\n"
"buffer_load_dwordx4 a[12:15],v112,s[24:27],s82 offen offset:3072 sc1\n"
"v_mfma_f32_16x16x32_bf16 v[184:187],a[120:123],v[68:71],v[184:187]\n"
"v_mfma_f32_16x16x32_bf16 v[188:191],a[124:127],v[68:71],v[188:191]\n"
"v_mfma_f32_16x16x32_bf16 v[220:223],a[124:127],v[72:75],v[220:223]\n"
"v_mfma_f32_16x16x32_bf16 v[252:255],a[124:127],v[76:79],v[252:255]\n"
"v_mfma_f32_16x16x32_bf16 v[156:159],a[124:127],v[64:67],v[156:159]\n"
"s_cbranch_vccnz .Lfk_cs_a3\n"
".Lfk_cr_a3:\n"
"s_waitcnt vmcnt(17) lgkmcnt(0)\n"
"s_barrier\n"
"v_mfma_f32_16x16x32_bf16 v[56:59],a[64:67],a[48:51],v[108:111]\n"
"ds_read_b128 a[96:99],v114 offset:57344\n"
"ds_read_b128 a[100:103],v114 offset:58368\n"
"ds_read_b128 a[104:107],v114 offset:59392\n"
"v_mfma_f32_16x16x32_bf16 v[56:59],a[68:71],a[52:55],v[56:59]\n"
"ds_read_b128 a[108:111],v114 offset:60416\n"
"ds_read_b128 a[112:115],v114 offset:61440\n"
"ds_read_b128 a[116:119],v114 offset:62464\n"
"v_mfma_f32_16x16x32_bf16 v[56:59],a[72:75],a[56:59],v[56:59]\n"
"ds_read_b128 a[120:123],v114 offset:63488\n"
"ds_read_b128 a[124:127],v114 offset:64512\n"
"v_add_f32 v48,0x80000000,v126\n"
"v_mfma_f32_16x16x32_bf16 v[56:59],a[76:79],a[60:63],v[56:59]\n"
"v_min_f32 v48,v48,v104\n"
"v_add_f32 v49,0xf149f2ca,v126\n"
"v_min_f32 v49,v49,v105\n"
"v_mfma_f32_16x16x32_bf16 v[60:63],a[92:95],a[60:63],v[108:111]\n"
"v_add_f32 v50,0xf1c9f2ca,v126\n"
"v_add_f32 v51,0xf2177617,v126\n"
"v_mfma_f32_16x16x32_bf16 v[60:63],a[88:91],a[56:59],v[60:63]\n"
"v_add_f32 v52,0xf349f2ca,v126\n"
"v_min_f32 v52,v52,v104\n"
"v_mfma_f32_16x16x32_bf16 v[60:63],a[84:87],a[52:55],v[60:63]\n"
"v_add_f32 v53,0xf35691f6,v126\n"
"v_min_f32 v53,v53,v105\n"
"v_add_f32 v54,0xf3633123,v126\n"
"v_mfma_f32_16x16x32_bf16 v[60:63],a[80:83],a[48:51],v[60:63]\n"
"v_min_f32 v54,v54,v106\n"
"v_add_f32 v55,0xf36fd050,v126\n"
"v_min_f32 v55,v55,v107\n"
"v_min_f32 v51,v51,v107\n"
"v_min_f32 v50,v50,v106\n"
"v_mfma_f32_16x16x32_bf16 v[52:55],a[80:83],a[32:35],v[52:55]\n"
"s_add_u32 m0,s67,16384\n"
"v_exp_f32 v8,v8\n"
"v_mfma_f32_16x16x32_bf16 v[52:55],a[84:87],a[36:39],v[52:55]\n"
"buffer_load_dwordx4 v112,s[24:27],s64 offen offset:0 sc1 lds\n"
"buffer_load_dwordx4 v112,s[24:27],s64 offen offset:1024 sc1 lds\n"
"v_exp_f32 v9,v9\n"
"v_mfma_f32_16x16x32_bf16 v[52:55],a[88:91],a[40:43],v[52:55]\n"
"v_exp_f32 v10,v10\n"
"v_cvt_pk_bf16_f32 v68,v8,v9\n"
"v_mfma_f32_16x16x32_bf16 v[52:55],a[92:95],a[44:47],v[52:55]\n"
"v_exp_f32 v11,v11\n"
"v_max3_f32 v118,v56,v57,v58\n"
"v_exp_f32 v12,v12\n"
"v_mfma_f32_16x16x32_bf16 v[48:51],a[76:79],a[44:47],v[48:51]\n"
"buffer_load_dwordx4 v112,s[24:27],s64 offen offset:2048 sc1 lds\n"
"v_mfma_f32_16x16x32_bf16 v[48:51],a[72:75],a[40:43],v[48:51]\n"
"v_exp_f32 v13,v13\n"
"v_cvt_pk_bf16_f32 v69,v10,v11\n"
"v_mfma_f32_16x16x32_bf16 v[48:51],a[68:71],a[36:39],v[48:51]\n"
"v_exp_f32 v14,v14\n"
"v_cvt_pk_bf16_f32 v70,v12,v13\n"
"v_exp_f32 v15,v15\n"
"v_mfma_f32_16x16x32_bf16 v[48:51],a[64:67],a[32:35],v[48:51]\n"
"ds_read_b128 a[64:67],v113 offset:49152\n"
"s_waitcnt lgkmcnt(1)\n"
"v_mfma_f32_16x16x32_bf16 v[128:131],a[96:99],v[80:83],v[128:131]\n"
"ds_read_b128 a[68:71],v113 offset:50176\n"
"v_exp_f32 v16,v16\n"
"v_mfma_f32_16x16x32_bf16 v[160:163],a[96:99],v[84:87],v[160:163]\n"
"ds_read_b128 a[72:75],v113 offset:51200\n"
"v_exp_f32 v17,v17\n"
"v_mfma_f32_16x16x32_bf16 v[192:195],a[96:99],v[88:91],v[192:195]\n"
"ds_read_b128 a[76:79],v113 offset:52224\n"
"ds_read_b128 a[80:83],v113 offset:53248\n"
"ds_read_b128 a[84:87],v113 offset:54272\n"
"v_mfma_f32_16x16x32_bf16 v[224:227],a[96:99],v[92:95],v[224:227]\n"
"ds_read_b128 a[88:91],v113 offset:55296\n"
"v_exp_f32 v18,v18\n"
"v_mfma_f32_16x16x32_bf16 a[140:143],a[144:147],v[92:95],a[140:143]\n"
"ds_read_b128 a[92:95],v113 offset:56320\n"
"buffer_load_dwordx4 v112,s[24:27],s64 offen offset:3072 sc1 lds\n"
"s_add_u32 s64,s64,16384\n"
"v_mfma_f32_16x16x32_bf16 v[228:231],a[100:103],v[92:95],v[228:231]\n"
"s_sub_u32 s82,s3,49152\n"
"v_exp_f32 v19,v19\n"
"v_mfma_f32_16x16x32_bf16 v[132:135],a[100:103],v[80:83],v[132:135]\n"
"buffer_load_dwordx4 a[16:19],v112,s[24:27],s82 offen offset:0 sc1\n"
"v_cvt_pk_bf16_f32 v71,v14,v15\n"
"v_cvt_pk_bf16_f32 v72,v16,v17\n"
"v_cvt_pk_bf16_f32 v73,v18,v19\n"
"v_exp_f32 v20,v20\n"
"v_mfma_f32_16x16x32_bf16 v[164:167],a[100:103],v[84:87],v[164:167]\n"
"buffer_load_dwordx4 a[20:23],v112,s[24:27],s82 offen offset:1024 sc1\n"
"v_mfma_f32_16x16x32_bf16 v[196:199],a[100:103],v[88:91],v[196:199]\n"
"v_exp_f32 v21,v21\n"
"v_max3_f32 v118,v118,v59,v60\n"
"v_mfma_f32_16x16x32_bf16 a[136:139],a[144:147],v[88:91],a[136:139]\n"
"v_cvt_pk_bf16_f32 v74,v20,v21\n"
"v_max3_f32 v118,v118,v61,v62\n"
"v_max3_f32 v118,v118,v63,v52\n"
"v_mfma_f32_16x16x32_bf16 v[200:203],a[104:107],v[88:91],v[200:203]\n"
"v_exp_f32 v22,v22\n"
"v_max3_f32 v118,v118,v53,v54\n"
"v_mfma_f32_16x16x32_bf16 v[232:235],a[104:107],v[92:95],v[232:235]\n"
"buffer_load_dwordx4 a[24:27],v112,s[24:27],s82 offen offset:2048 sc1\n"
"v_max3_f32 v118,v118,v55,v48\n"
"v_max3_f32 v118,v118,v49,v50\n"
"v_mfma_f32_16x16x32_bf16 v[136:139],a[104:107],v[80:83],v[136:139]\n"
"v_exp_f32 v23,v23\n"
"v_max_f32 v118,v118,v51\n"
"v_mfma_f32_16x16x32_bf16 v[168:171],a[104:107],v[84:87],v[168:171]\n"
"v_cvt_pk_bf16_f32 v75,v22,v23\n"
"v_cmp_lt_f32 vcc,s93,v118\n"
"v_mfma_f32_16x16x32_bf16 a[132:135],a[144:147],v[84:87],a[132:135]\n"
"buffer_load_dwordx4 a[28:31],v112,s[24:27],s82 offen offset:3072 sc1\n"
"v_mfma_f32_16x16x32_bf16 v[172:175],a[108:111],v[84:87],v[172:175]\n"
"v_exp_f32 v24,v24\n"
"v_mfma_f32_16x16x32_bf16 v[204:207],a[108:111],v[88:91],v[204:207]\n"
"s_sub_u32 s82,s3,65536\n"
"v_exp_f32 v25,v25\n"
"v_mfma_f32_16x16x32_bf16 v[236:239],a[108:111],v[92:95],v[236:239]\n"
"v_cvt_pk_bf16_f32 v76,v24,v25\n"
"v_mfma_f32_16x16x32_bf16 v[140:143],a[108:111],v[80:83],v[140:143]\n"
"buffer_load_dwordx4 v[32:35],v112,s[24:27],s82 offen offset:0 sc1\n"
"v_mfma_f32_16x16x32_bf16 a[128:131],a[144:147],v[80:83],a[128:131]\n"
"v_exp_f32 v26,v26\n"
"v_mfma_f32_16x16x32_bf16 v[144:147],a[112:115],v[80:83],v[144:147]\n"
"v_exp_f32 v27,v27\n"
"v_mfma_f32_16x16x32_bf16 v[176:179],a[112:115],v[84:87],v[176:179]\n"
"buffer_load_dwordx4 v[36:39],v112,s[24:27],s82 offen offset:1024 sc1\n"
"v_mfma_f32_16x16x32_bf16 v[208:211],a[112:115],v[88:91],v[208:211]\n"
"v_exp_f32 v28,v28\n"
"v_mfma_f32_16x16x32_bf16 v[240:243],a[112:115],v[92:95],v[240:243]\n"
"v_cvt_pk_bf16_f32 v77,v26,v27\n"
"v_mfma_f32_16x16x32_bf16 v[244:247],a[116:119],v[92:95],v[244:247]\n"
"v_exp_f32 v29,v29\n"
"v_mfma_f32_16x16x32_bf16 v[148:151],a[116:119],v[80:83],v[148:151]\n"
"buffer_load_dwordx4 v[40:43],v112,s[24:27],s82 offen offset:2048 sc1\n"
"v_mfma_f32_16x16x32_bf16 v[180:183],a[116:119],v[84:87],v[180:183]\n"
"v_exp_f32 v30,v30\n"
"v_mfma_f32_16x16x32_bf16 v[212:215],a[116:119],v[88:91],v[212:215]\n"
"v_cvt_pk_bf16_f32 v78,v28,v29\n"
"v_mfma_f32_16x16x32_bf16 v[216:219],a[120:123],v[88:91],v[216:219]\n"
"v_exp_f32 v31,v31\n"
"v_mfma_f32_16x16x32_bf16 v[248:251],a[120:123],v[92:95],v[248:251]\n"
"v_cvt_pk_bf16_f32 v79,v30,v31\n"
"v_mfma_f32_16x16x32_bf16 v[152:155],a[120:123],v[80:83],v[152:155]\n"
"buffer_load_dwordx4 v[44:47],v112,s[24:27],s82 offen offset:3072 sc1\n"
"v_mfma_f32_16x16x32_bf16 v[184:187],a[120:123],v[84:87],v[184:187]\n"
"v_mfma_f32_16x16x32_bf16 v[188:191],a[124:127],v[84:87],v[188:191]\n"
"v_mfma_f32_16x16x32_bf16 v[220:223],a[124:127],v[88:91],v[220:223]\n"
"v_mfma_f32_16x16x32_bf16 v[252:255],a[124:127],v[92:95],v[252:255]\n"
"v_mfma_f32_16x16x32_bf16 v[156:159],a[124:127],v[80:83],v[156:159]\n"
"s_cbranch_vccnz .Lfk_cs_a0\n"
".Lfk_cr_a0:\n"
"s_load_dwordx2 s[36:37],s[0:1],0\n"
"s_load_dwordx2 s[44:45],s[0:1],80\n"
"s_load_dwordx2 s[48:49],s[0:1],88\n"
"s_lshl_b32 s82,s78,2\n"
"s_add_u32 s82,s82,65536\n"
"v_add_u32 v113,s82,v112\n"
"s_lshr_b32 s82,s78,2\n"
"s_add_u32 s82,s82,122880\n"
"v_lshrrev_b32 v115,1,v112\n"
"v_add_u32 v116,s82,v115\n"
"s_waitcnt lgkmcnt(0)\n"
"s_barrier\n"
"v_add_f32 v24,0x80000000,v126\n"
"v_min_f32 v24,v24,v108\n"
"v_add_f32 v25,0xf149f2ca,v126\n"
"v_min_f32 v25,v25,v109\n"
"v_add_f32 v26,0xf1c9f2ca,v126\n"
"v_min_f32 v26,v26,v110\n"
"v_add_f32 v27,0xf2177617,v126\n"
"v_min_f32 v27,v27,v111\n"
"v_add_f32 v28,0xf349f2ca,v126\n"
"v_min_f32 v28,v28,v108\n"
"v_add_f32 v29,0xf35691f6,v126\n"
"v_min_f32 v29,v29,v109\n"
"v_add_f32 v30,0xf3633123,v126\n"
"v_min_f32 v30,v30,v110\n"
"v_add_f32 v31,0xf36fd050,v126\n"
"v_min_f32 v31,v31,v111\n"
"v_exp_f32 v48,v48\n"
"v_mfma_f32_16x16x32_bf16 v[24:27],a[64:67],a[48:51],v[24:27]\n"
"s_add_u32 s83,s18,2097152\n"
"ds_read_b128 a[96:99],v114 offset:40960\n"
"ds_read_b128 a[100:103],v114 offset:41984\n"
"v_exp_f32 v49,v49\n"
"v_mfma_f32_16x16x32_bf16 v[24:27],a[68:71],a[52:55],v[24:27]\n"
"s_add_u32 m0,s30,32768\n"
"ds_read_b128 a[104:107],v114 offset:43008\n"
"ds_read_b128 a[108:111],v114 offset:44032\n"
"v_mfma_f32_16x16x32_bf16 v[24:27],a[72:75],a[56:59],v[24:27]\n"
"ds_read_b128 a[112:115],v114 offset:45056\n"
"ds_read_b128 a[116:119],v114 offset:46080\n"
"ds_read_b128 a[120:123],v114 offset:47104\n"
"ds_read_b128 a[124:127],v114 offset:48128\n"
"v_exp_f32 v50,v50\n"
"v_mfma_f32_16x16x32_bf16 v[24:27],a[76:79],a[60:63],v[24:27]\n"
"buffer_load_dwordx4 v112,s[24:27],s83 offen offset:0 sc1 lds\n"
"v_cvt_pk_bf16_f32 v88,v48,v49\n"
"v_mfma_f32_16x16x32_bf16 v[28:31],a[92:95],a[60:63],v[28:31]\n"
"buffer_load_dwordx4 v112,s[24:27],s83 offen offset:1024 sc1 lds\n"
"v_accvgpr_read_b32 v97,a128\n"
"v_cvt_pk_bf16_f32 v128,v128,v129\n"
"v_cvt_pk_bf16_f32 v129,v130,v131\n"
"v_mfma_f32_16x16x32_bf16 v[28:31],a[88:91],a[56:59],v[28:31]\n"
"v_exp_f32 v51,v51\n"
"v_cvt_pk_bf16_f32 v130,v132,v133\n"
"v_cvt_pk_bf16_f32 v131,v134,v135\n"
"v_cvt_pk_bf16_f32 v89,v50,v51\n"
"v_mfma_f32_16x16x32_bf16 v[28:31],a[84:87],a[52:55],v[28:31]\n"
"ds_write_b128 v113,v[128:131] offset:0\n"
"v_cvt_pk_bf16_f32 v136,v136,v137\n"
"v_cvt_pk_bf16_f32 v137,v138,v139\n"
"v_exp_f32 v52,v52\n"
"v_mfma_f32_16x16x32_bf16 v[28:31],a[80:83],a[48:51],v[28:31]\n"
"buffer_load_dwordx4 v112,s[24:27],s83 offen offset:2048 sc1 lds\n"
"v_max3_f32 v118,v24,v25,v26\n"
"s_waitcnt lgkmcnt(1)\n"
"v_mfma_f32_16x16x32_bf16 v[160:163],a[96:99],v[68:71],v[160:163]\n"
"v_cvt_pk_bf16_f32 v138,v140,v141\n"
"v_cvt_pk_bf16_f32 v139,v142,v143\n"
"v_cvt_pk_bf16_f32 v144,v144,v145\n"
"v_mfma_f32_16x16x32_bf16 a[132:135],a[144:147],v[68:71],a[132:135]\n"
"ds_write_b128 v113,v[136:139] offset:1024\n"
"buffer_load_dwordx4 v112,s[24:27],s83 offen offset:3072 sc1 lds\n"
"v_exp_f32 v53,v53\n"
"v_cvt_pk_bf16_f32 v145,v146,v147\n"
"v_mfma_f32_16x16x32_bf16 v[164:167],a[100:103],v[68:71],v[164:167]\n"
"s_add_u32 m0,m0,4096\n"
"v_exp_f32 v54,v54\n"
"v_cvt_pk_bf16_f32 v90,v52,v53\n"
"v_cvt_pk_bf16_f32 v146,v148,v149\n"
"v_mfma_f32_16x16x32_bf16 v[168:171],a[104:107],v[68:71],v[168:171]\n"
"v_max3_f32 v118,v118,v27,v28\n"
"v_max3_f32 v118,v118,v29,v30\n"
"v_max_f32 v118,v118,v31\n"
"v_cmp_lt_f32 vcc,s93,v118\n"
"v_exp_f32 v55,v55\n"
"v_mfma_f32_16x16x32_bf16 v[172:175],a[108:111],v[68:71],v[172:175]\n"
"buffer_load_dwordx4 v117,s[24:27],s83 offen offset:0 sc1 lds\n"
"v_cvt_pk_bf16_f32 v147,v150,v151\n"
"v_cvt_pk_bf16_f32 v91,v54,v55\n"
"v_mfma_f32_16x16x32_bf16 v[176:179],a[112:115],v[68:71],v[176:179]\n"
"ds_write_b128 v113,v[144:147] offset:2048\n"
"v_cvt_pk_bf16_f32 v152,v152,v153\n"
"v_cvt_pk_bf16_f32 v153,v154,v155\n"
"v_exp_f32 v56,v56\n"
"v_mfma_f32_16x16x32_bf16 v[180:183],a[116:119],v[68:71],v[180:183]\n"
"buffer_load_dwordx4 v117,s[24:27],s83 offen offset:1024 sc1 lds\n"
"v_cvt_pk_bf16_f32 v154,v156,v157\n"
"v_cvt_pk_bf16_f32 v155,v158,v159\n"
"v_mfma_f32_16x16x32_bf16 v[184:187],a[120:123],v[68:71],v[184:187]\n"
"ds_write_b128 v113,v[152:155] offset:3072\n"
"ds_write_b64 v116,v[96:97] offset:0\n"
"v_mfma_f32_16x16x32_bf16 v[188:191],a[124:127],v[68:71],v[188:191]\n"
"v_exp_f32 v57,v57\n"
"v_mfma_f32_16x16x32_bf16 v[192:195],a[96:99],v[72:75],v[192:195]\n"
"buffer_load_dwordx4 v117,s[24:27],s83 offen offset:2048 sc1 lds\n"
"v_cvt_pk_bf16_f32 v92,v56,v57\n"
"v_mfma_f32_16x16x32_bf16 a[136:139],a[144:147],v[72:75],a[136:139]\n"
"v_exp_f32 v58,v58\n"
"v_exp_f32 v59,v59\n"
"v_mfma_f32_16x16x32_bf16 v[196:199],a[100:103],v[72:75],v[196:199]\n"
"buffer_load_dwordx4 v117,s[24:27],s83 offen offset:3072 sc1 lds\n"
"v_accvgpr_read_b32 v101,a132\n"
"v_mfma_f32_16x16x32_bf16 v[200:203],a[104:107],v[72:75],v[200:203]\n"
"s_sub_u32 s82,s3,81920\n"
"v_cvt_pk_bf16_f32 v93,v58,v59\n"
"v_cvt_pk_bf16_f32 v160,v160,v161\n"
"v_cvt_pk_bf16_f32 v161,v162,v163\n"
"v_mfma_f32_16x16x32_bf16 v[204:207],a[108:111],v[72:75],v[204:207]\n"
"v_exp_f32 v60,v60\n"
"v_cvt_pk_bf16_f32 v162,v164,v165\n"
"v_cvt_pk_bf16_f32 v163,v166,v167\n"
"v_mfma_f32_16x16x32_bf16 v[208:211],a[112:115],v[72:75],v[208:211]\n"
"buffer_load_dwordx4 a[32:35],v112,s[24:27],s82 offen offset:0 sc1\n"
"ds_write_b128 v113,v[160:163] offset:4096\n"
"v_cvt_pk_bf16_f32 v168,v168,v169\n"
"v_cvt_pk_bf16_f32 v169,v170,v171\n"
"v_mfma_f32_16x16x32_bf16 v[212:215],a[116:119],v[72:75],v[212:215]\n"
"v_exp_f32 v61,v61\n"
"v_cvt_pk_bf16_f32 v170,v172,v173\n"
"v_cvt_pk_bf16_f32 v171,v174,v175\n"
"v_cvt_pk_bf16_f32 v94,v60,v61\n"
"v_mfma_f32_16x16x32_bf16 v[216:219],a[120:123],v[72:75],v[216:219]\n"
"ds_write_b128 v113,v[168:171] offset:5120\n"
"v_cvt_pk_bf16_f32 v176,v176,v177\n"
"v_cvt_pk_bf16_f32 v177,v178,v179\n"
"v_exp_f32 v62,v62\n"
"v_mfma_f32_16x16x32_bf16 v[220:223],a[124:127],v[72:75],v[220:223]\n"
"buffer_load_dwordx4 a[36:39],v112,s[24:27],s82 offen offset:1024 sc1\n"
"v_cvt_pk_bf16_f32 v178,v180,v181\n"
"v_cvt_pk_bf16_f32 v179,v182,v183\n"
"v_mfma_f32_16x16x32_bf16 v[224:227],a[96:99],v[76:79],v[224:227]\n"
"ds_write_b128 v113,v[176:179] offset:6144\n"
"v_cvt_pk_bf16_f32 v184,v184,v185\n"
"v_cvt_pk_bf16_f32 v185,v186,v187\n"
"v_mfma_f32_16x16x32_bf16 a[140:143],a[144:147],v[76:79],a[140:143]\n"
"v_exp_f32 v63,v63\n"
"v_cvt_pk_bf16_f32 v186,v188,v189\n"
"v_cvt_pk_bf16_f32 v187,v190,v191\n"
"v_mfma_f32_16x16x32_bf16 v[228:231],a[100:103],v[76:79],v[228:231]\n"
"buffer_load_dwordx4 a[40:43],v112,s[24:27],s82 offen offset:2048 sc1\n"
"ds_write_b128 v113,v[184:187] offset:7168\n"
"v_mfma_f32_16x16x32_bf16 v[232:235],a[104:107],v[76:79],v[232:235]\n"
"ds_write_b64 v116,v[100:101] offset:512\n"
"v_cvt_pk_bf16_f32 v95,v62,v63\n"
"v_mfma_f32_16x16x32_bf16 v[236:239],a[108:111],v[76:79],v[236:239]\n"
"buffer_load_dwordx4 a[44:47],v112,s[24:27],s82 offen offset:3072 sc1\n"
"v_mfma_f32_16x16x32_bf16 v[240:243],a[112:115],v[76:79],v[240:243]\n"
"s_sub_u32 s82,s3,131072\n"
"buffer_load_dwordx4 v[128:131],v112,s[24:27],s82 offen offset:0 sc1\n"
"buffer_load_dwordx4 v[132:135],v112,s[24:27],s82 offen offset:1024 sc1\n"
"buffer_load_dwordx4 v[136:139],v112,s[24:27],s82 offen offset:2048 sc1\n"
"buffer_load_dwordx4 v[140:143],v112,s[24:27],s82 offen offset:3072 sc1\n"
"v_mfma_f32_16x16x32_bf16 v[244:247],a[116:119],v[76:79],v[244:247]\n"
"s_sub_u32 s82,s3,147456\n"
"buffer_load_dwordx4 v[144:147],v112,s[24:27],s82 offen offset:0 sc1\n"
"buffer_load_dwordx4 v[148:151],v112,s[24:27],s82 offen offset:1024 sc1\n"
"v_mfma_f32_16x16x32_bf16 v[248:251],a[120:123],v[76:79],v[248:251]\n"
"buffer_load_dwordx4 v[152:155],v112,s[24:27],s82 offen offset:2048 sc1\n"
"buffer_load_dwordx4 v[156:159],v112,s[24:27],s82 offen offset:3072 sc1\n"
"v_mfma_f32_16x16x32_bf16 v[252:255],a[124:127],v[76:79],v[252:255]\n"
"s_cbranch_vccnz .Lfk_cs_a1\n"
".Lfk_cr_a1:\n"
"s_waitcnt vmcnt(28) lgkmcnt(0)\n"
"s_barrier\n"
"ds_read_b128 a[96:99],v114 offset:8192\n"
"ds_read_b128 a[100:103],v114 offset:9216\n"
"ds_read_b128 a[104:107],v114 offset:10240\n"
"ds_read_b128 a[108:111],v114 offset:11264\n"
"ds_read_b128 a[112:115],v114 offset:12288\n"
"ds_read_b128 a[116:119],v114 offset:13312\n"
"ds_read_b128 a[120:123],v114 offset:14336\n"
"ds_read_b128 a[124:127],v114 offset:15360\n"
"v_xor_b32 v113,32768,v113\n"
"v_xor_b32 v116,2048,v116\n"
"ds_read_b64 v[98:99],v116 offset:0\n"
"ds_read_b128 a[64:67],v113 offset:0\n"
"ds_read_b128 a[68:71],v113 offset:1024\n"
"ds_read_b128 a[72:75],v113 offset:2048\n"
"ds_read_b128 a[76:79],v113 offset:3072\n"
"ds_read_b64 v[102:103],v116 offset:512\n"
"ds_read_b128 a[80:83],v113 offset:4096\n"
"ds_read_b128 a[84:87],v113 offset:5120\n"
"ds_read_b128 a[88:91],v113 offset:6144\n"
"ds_read_b128 a[92:95],v113 offset:7168\n"
"v_exp_f32 v24,v24\n"
"v_exp_f32 v25,v25\n"
"v_exp_f32 v26,v26\n"
"v_exp_f32 v27,v27\n"
"v_exp_f32 v28,v28\n"
"v_exp_f32 v29,v29\n"
"v_exp_f32 v30,v30\n"
"v_exp_f32 v31,v31\n"
"v_cvt_pk_bf16_f32 v76,v24,v25\n"
"v_cvt_pk_bf16_f32 v77,v26,v27\n"
"v_cvt_pk_bf16_f32 v78,v28,v29\n"
"v_cvt_pk_bf16_f32 v79,v30,v31\n"
"s_waitcnt lgkmcnt(10)\n"
"v_mfma_f32_16x16x32_bf16 a[136:139],a[144:147],v[88:91],a[136:139]\n"
"s_sub_u32 s82,s3,163840\n"
"v_lshrrev_b32 v70,4,v112\n"
"v_and_b32 v71,15,v70\n"
"v_mfma_f32_16x16x32_bf16 a[140:143],a[144:147],v[92:95],a[140:143]\n"
"buffer_load_dwordx4 v[0:3],v112,s[24:27],s82 offen offset:0 sc1\n"
"v_lshrrev_b32 v70,4,v70\n"
"v_lshlrev_b32 v70,2,v70\n"
"v_mfma_f32_16x16x32_bf16 v[192:195],a[96:99],v[88:91],v[192:195]\n"
"buffer_load_dwordx4 v[4:7],v112,s[24:27],s82 offen offset:1024 sc1\n"
"v_sub_u32 v70,v71,v70\n"
"v_mov_b32 v64,0\n"
"v_mfma_f32_16x16x32_bf16 v[224:227],a[96:99],v[92:95],v[224:227]\n"
"ds_read_b128 a[96:99],v114 offset:24576\n"
"buffer_load_dwordx4 v[8:11],v112,s[24:27],s82 offen offset:2048 sc1\n"
"v_mfma_f32_16x16x32_bf16 v[196:199],a[100:103],v[88:91],v[196:199]\n"
"buffer_load_dwordx4 v[12:15],v112,s[24:27],s82 offen offset:3072 sc1\n"
"v_mov_b32 v69,0\n"
"v_mfma_f32_16x16x32_bf16 v[228:231],a[100:103],v[92:95],v[228:231]\n"
"s_sub_u32 s82,s3,180224\n"
"ds_read_b128 a[100:103],v114 offset:25600\n"
"v_mov_b32 v71,0x3f80\n"
"v_mfma_f32_16x16x32_bf16 v[200:203],a[104:107],v[88:91],v[200:203]\n"
"buffer_load_dwordx4 v[160:163],v112,s[24:27],s82 offen offset:0 sc1\n"
"v_cmp_eq_u32 vcc,0,v70\n"
"v_mov_b32 v68,0\n"
"v_mov_b32 v65,0\n"
"v_cndmask_b32 v66,0,v71,vcc\n"
"v_cmp_eq_u32 vcc,1,v70\n"
"v_mfma_f32_16x16x32_bf16 v[232:235],a[104:107],v[92:95],v[232:235]\n"
"ds_read_b128 a[104:107],v114 offset:26624\n"
"buffer_load_dwordx4 v[164:167],v112,s[24:27],s82 offen offset:1024 sc1\n"
"v_mov_b32 v72,0x3f800000\n"
"v_cndmask_b32 v66,v66,v72,vcc\n"
"v_cmp_eq_u32 vcc,2,v70\n"
"v_mfma_f32_16x16x32_bf16 v[204:207],a[108:111],v[88:91],v[204:207]\n"
"buffer_load_dwordx4 v[168:171],v112,s[24:27],s82 offen offset:2048 sc1\n"
"v_cndmask_b32 v67,0,v71,vcc\n"
"v_cmp_eq_u32 vcc,3,v70\n"
"v_mfma_f32_16x16x32_bf16 v[236:239],a[108:111],v[92:95],v[236:239]\n"
"ds_read_b128 a[108:111],v114 offset:27648\n"
"buffer_load_dwordx4 v[172:175],v112,s[24:27],s82 offen offset:3072 sc1\n"
"v_mfma_f32_16x16x32_bf16 v[208:211],a[112:115],v[88:91],v[208:211]\n"
"v_cndmask_b32 v67,v67,v72,vcc\n"
"s_sub_u32 s82,s3,196608\n"
"v_mfma_f32_16x16x32_bf16 v[240:243],a[112:115],v[92:95],v[240:243]\n"
"ds_read_b128 a[112:115],v114 offset:28672\n"
"buffer_load_dwordx4 v[176:179],v112,s[24:27],s82 offen offset:0 sc1\n"
"v_mfma_f32_16x16x32_bf16 v[212:215],a[116:119],v[88:91],v[212:215]\n"
"buffer_load_dwordx4 v[180:183],v112,s[24:27],s82 offen offset:1024 sc1\n"
"v_mfma_f32_16x16x32_bf16 v[244:247],a[116:119],v[92:95],v[244:247]\n"
"ds_read_b128 a[116:119],v114 offset:29696\n"
"buffer_load_dwordx4 v[184:187],v112,s[24:27],s82 offen offset:2048 sc1\n"
"v_mfma_f32_16x16x32_bf16 v[216:219],a[120:123],v[88:91],v[216:219]\n"
"buffer_load_dwordx4 v[188:191],v112,s[24:27],s82 offen offset:3072 sc1\n"
"v_mfma_f32_16x16x32_bf16 v[248:251],a[120:123],v[92:95],v[248:251]\n"
"ds_read_b128 a[120:123],v114 offset:30720\n"
"v_mfma_f32_16x16x32_bf16 v[220:223],a[124:127],v[88:91],v[220:223]\n"
"v_mfma_f32_16x16x32_bf16 v[252:255],a[124:127],v[92:95],v[252:255]\n"
"ds_read_b128 a[124:127],v114 offset:31744\n"
"s_waitcnt lgkmcnt(13)\n"
"v_cmp_neq_f32 vcc,v98,v104\n"
"s_nop 0\n"
"s_cbranch_vccnz .Lfk_ms_a0\n"
".Lfk_mr_a0:\n"
"v_mfma_f32_16x16x32_bf16 v[192:195],v[66:69],a[64:67],v[192:195]\n"
"v_mfma_f32_16x16x32_bf16 v[196:199],v[64:67],a[64:67],v[196:199]\n"
"v_mfma_f32_16x16x32_bf16 v[200:203],v[66:69],a[68:71],v[200:203]\n"
"v_mfma_f32_16x16x32_bf16 v[204:207],v[64:67],a[68:71],v[204:207]\n"
"v_mfma_f32_16x16x32_bf16 v[208:211],v[66:69],a[72:75],v[208:211]\n"
"v_mfma_f32_16x16x32_bf16 v[212:215],v[64:67],a[72:75],v[212:215]\n"
"v_mfma_f32_16x16x32_bf16 v[216:219],v[66:69],a[76:79],v[216:219]\n"
"v_mfma_f32_16x16x32_bf16 v[220:223],v[64:67],a[76:79],v[220:223]\n"
"s_waitcnt lgkmcnt(0)\n"
"v_mfma_f32_16x16x32_bf16 a[140:143],a[144:147],v[76:79],a[140:143]\n"
"v_mfma_f32_16x16x32_bf16 v[224:227],a[96:99],v[76:79],v[224:227]\n"
"v_mfma_f32_16x16x32_bf16 v[228:231],a[100:103],v[76:79],v[228:231]\n"
"v_accvgpr_read_b32 v122,a136\n"
"v_add_f32 v122,v122,v99\n"
"v_mfma_f32_16x16x32_bf16 v[232:235],a[104:107],v[76:79],v[232:235]\n"
"v_rcp_f32 v122,v122\n"
"v_mfma_f32_16x16x32_bf16 v[236:239],a[108:111],v[76:79],v[236:239]\n"
"v_pk_mul_f32 v[192:193],v[192:193],v[122:123] op_sel_hi:[1,0]\n"
"v_pk_mul_f32 v[194:195],v[194:195],v[122:123] op_sel_hi:[1,0]\n"
"v_pk_mul_f32 v[196:197],v[196:197],v[122:123] op_sel_hi:[1,0]\n"
"v_pk_mul_f32 v[198:199],v[198:199],v[122:123] op_sel_hi:[1,0]\n"
"v_mfma_f32_16x16x32_bf16 v[240:243],a[112:115],v[76:79],v[240:243]\n"
"v_cvt_pk_bf16_f32 v192,v192,v193\n"
"v_cvt_pk_bf16_f32 v193,v194,v195\n"
"v_mfma_f32_16x16x32_bf16 v[244:247],a[116:119],v[76:79],v[244:247]\n"
"v_cvt_pk_bf16_f32 v194,v196,v197\n"
"v_cvt_pk_bf16_f32 v195,v198,v199\n"
"v_mfma_f32_16x16x32_bf16 v[248:251],a[120:123],v[76:79],v[248:251]\n"
"buffer_store_dwordx4 v[192:195],v112,s[24:27],s70 offen offset:0 sc1\n"
"v_pk_mul_f32 v[200:201],v[200:201],v[122:123] op_sel_hi:[1,0]\n"
"v_pk_mul_f32 v[202:203],v[202:203],v[122:123] op_sel_hi:[1,0]\n"
"v_pk_mul_f32 v[204:205],v[204:205],v[122:123] op_sel_hi:[1,0]\n"
"v_pk_mul_f32 v[206:207],v[206:207],v[122:123] op_sel_hi:[1,0]\n"
"v_mfma_f32_16x16x32_bf16 v[252:255],a[124:127],v[76:79],v[252:255]\n"
"v_cvt_pk_bf16_f32 v200,v200,v201\n"
"v_cvt_pk_bf16_f32 v201,v202,v203\n"
"v_cmp_neq_f32 vcc,v102,v108\n"
"s_nop 0\n"
"s_cbranch_vccnz .Lfk_ms_a1\n"
".Lfk_mr_a1:\n"
"v_mfma_f32_16x16x32_bf16 v[224:227],v[66:69],a[80:83],v[224:227]\n"
"v_cvt_pk_bf16_f32 v202,v204,v205\n"
"v_cvt_pk_bf16_f32 v203,v206,v207\n"
"v_mfma_f32_16x16x32_bf16 v[228:231],v[64:67],a[80:83],v[228:231]\n"
"buffer_store_dwordx4 v[200:203],v112,s[24:27],s70 offen offset:1024 sc1\n"
"v_pk_mul_f32 v[208:209],v[208:209],v[122:123] op_sel_hi:[1,0]\n"
"v_pk_mul_f32 v[210:211],v[210:211],v[122:123] op_sel_hi:[1,0]\n"
"v_pk_mul_f32 v[212:213],v[212:213],v[122:123] op_sel_hi:[1,0]\n"
"v_pk_mul_f32 v[214:215],v[214:215],v[122:123] op_sel_hi:[1,0]\n"
"v_mfma_f32_16x16x32_bf16 v[232:235],v[66:69],a[84:87],v[232:235]\n"
"v_cvt_pk_bf16_f32 v208,v208,v209\n"
"v_cvt_pk_bf16_f32 v209,v210,v211\n"
"v_mfma_f32_16x16x32_bf16 v[236:239],v[64:67],a[84:87],v[236:239]\n"
"v_cvt_pk_bf16_f32 v210,v212,v213\n"
"v_cvt_pk_bf16_f32 v211,v214,v215\n"
"v_mfma_f32_16x16x32_bf16 v[240:243],v[66:69],a[88:91],v[240:243]\n"
"buffer_store_dwordx4 v[208:211],v112,s[24:27],s70 offen offset:2048 sc1\n"
"v_pk_mul_f32 v[216:217],v[216:217],v[122:123] op_sel_hi:[1,0]\n"
"v_pk_mul_f32 v[218:219],v[218:219],v[122:123] op_sel_hi:[1,0]\n"
"v_pk_mul_f32 v[220:221],v[220:221],v[122:123] op_sel_hi:[1,0]\n"
"v_pk_mul_f32 v[222:223],v[222:223],v[122:123] op_sel_hi:[1,0]\n"
"v_mfma_f32_16x16x32_bf16 v[244:247],v[64:67],a[88:91],v[244:247]\n"
"v_cvt_pk_bf16_f32 v216,v216,v217\n"
"v_cvt_pk_bf16_f32 v217,v218,v219\n"
"v_mfma_f32_16x16x32_bf16 v[248:251],v[66:69],a[92:95],v[248:251]\n"
"v_cvt_pk_bf16_f32 v218,v220,v221\n"
"v_cvt_pk_bf16_f32 v219,v222,v223\n"
"v_mfma_f32_16x16x32_bf16 v[252:255],v[64:67],a[92:95],v[252:255]\n"
"buffer_store_dwordx4 v[216:219],v112,s[24:27],s70 offen offset:3072 sc1\n"
"s_add_u32 s70,s70,262144\n"
"s_branch .Lfk_p1\n"
".Lfk_look:\n"
"s_lshr_b64 s[28:29],s[14:15],s21\n"
"s_and_b32 s83,s28,s19\n"
"s_cbranch_scc0 .Lfk_lk1\n"
"s_mov_b32 s84,0x40000\n"
"s_waitcnt vmcnt(10)\n"
".Lfk_lk0:\n"
"v_xor_b32 v119,s80,v122\n"
"v_xor_b32 v120,s81,v123\n"
"v_or_b32 v119,v119,v120\n"
"v_cmp_ne_u32 s[14:15],0,v119\n"
"s_nop 0\n"
"s_lshr_b64 s[28:29],s[14:15],s21\n"
"s_and_b32 s83,s28,s19\n"
"s_cbranch_scc0 .Lfk_lk1\n"
"s_sleep 2\n"
"buffer_load_dwordx2 v[122:123],v115,s[24:27],s11 offen sc1\n"
"s_waitcnt vmcnt(0)\n"
"s_sub_u32 s84,s84,1\n"
"s_cbranch_scc0 .Lfk_lk0\n"
".Lfk_lk1:\n"
"s_andn2_b32 s28,s28,s19\n"
"s_lshl_b64 s[14:15],s[28:29],s21\n"
"s_lshl_b32 s83,s19,8\n"
"s_and_b32 s83,s28,s83\n"
"s_cbranch_scc0 .Lfk_lk2\n"
"buffer_load_dwordx2 v[122:123],v115,s[24:27],s11 offen sc1\n"
".Lfk_lk2:\n"
"s_nop 1\n"
"s_setpc_b64 s[94:95]\n"
".Lfk_meet4:\n"
"s_mov_b32 s84,0x1000000\n"
".Lfk_meetl4:\n"
"ds_read_b32 v125,v114\n"
"s_sub_u32 s84,s84,1\n"
"s_cbranch_scc1 .Lfk_meetx4\n"
"s_waitcnt lgkmcnt(0)\n"
"v_cmp_gt_u32 vcc,s16,v125\n"
"s_nop 0\n"
"s_cbranch_vccnz .Lfk_meetl4\n"
".Lfk_meetx4:\n"
"s_setpc_b64 s[94:95]\n"
".Lfk_es_lkone:\n"
"s_call_b64 s[94:95],.Lfk_look\n"
"s_branch .Lfk_er_lkone\n"
".Lfk_z0:\n"
"s_call_b64 s[94:95],.Lfk_meet4\n"
"v_xor_b32 v119,s80,v104\n"
"v_xor_b32 v120,s81,v105\n"
"v_or_b32 v119,v119,v120\n"
"v_xor_b32 v120,s80,v106\n"
"v_or_b32 v119,v119,v120\n"
"v_xor_b32 v120,s81,v107\n"
"v_or_b32 v119,v119,v120\n"
"v_xor_b32 v120,s80,v108\n"
"v_or_b32 v119,v119,v120\n"
"v_xor_b32 v120,s81,v109\n"
"v_or_b32 v119,v119,v120\n"
"v_xor_b32 v120,s80,v110\n"
"v_or_b32 v119,v119,v120\n"
"v_xor_b32 v120,s81,v111\n"
"v_or_b32 v119,v119,v120\n"
"v_cmp_ne_u32 vcc,0,v119\n"
"s_nop 0\n"
"s_cbranch_vccz .Lfk_z1\n"
"s_mov_b32 s84,0x40000\n"
"s_mov_b32 s83,148946944\n"
"buffer_load_dwordx4 v[104:107],v112,s[24:27],s83 offen offset:0 sc1\n"
"buffer_load_dwordx4 v[108:111],v112,s[24:27],s83 offen offset:1024 sc1\n"
"s_waitcnt vmcnt(0)\n"
".Lfk_w0:\n"
"v_xor_b32 v119,s80,v104\n"
"v_xor_b32 v120,s81,v105\n"
"v_or_b32 v119,v119,v120\n"
"v_xor_b32 v120,s80,v106\n"
"v_or_b32 v119,v119,v120\n"
"v_xor_b32 v120,s81,v107\n"
"v_or_b32 v119,v119,v120\n"
"v_xor_b32 v120,s80,v108\n"
"v_or_b32 v119,v119,v120\n"
"v_xor_b32 v120,s81,v109\n"
"v_or_b32 v119,v119,v120\n"
"v_xor_b32 v120,s80,v110\n"
"v_or_b32 v119,v119,v120\n"
"v_xor_b32 v120,s81,v111\n"
"v_or_b32 v119,v119,v120\n"
"v_cmp_ne_u32 vcc,0,v119\n"
"s_nop 0\n"
"s_cbranch_vccz .Lfk_w0_x\n"
"s_sleep 8\n"
"s_mov_b64 exec,vcc\n"
"s_mov_b32 s83,148946944\n"
"buffer_load_dwordx4 v[104:107],v112,s[24:27],s83 offen offset:0 sc1\n"
"buffer_load_dwordx4 v[108:111],v112,s[24:27],s83 offen offset:1024 sc1\n"
"s_mov_b64 exec,-1\n"
"s_sub_u32 s84,s84,1\n"
"s_waitcnt vmcnt(0)\n"
"s_cbranch_scc0 .Lfk_w0\n"
".Lfk_w0_x:\n"
"s_nop 1\n"
"s_branch .Lfk_z1\n"
".Lfk_es_lkonem:\n"
"s_call_b64 s[94:95],.Lfk_look\n"
"s_branch .Lfk_er_lkonem\n"
".Lfk_es_mone:\n"
"s_call_b64 s[94:95],.Lfk_meet4\n"
"s_branch .Lfk_er_mone\n"
".Lfk_es_lktwom:\n"
"s_call_b64 s[94:95],.Lfk_look\n"
"s_branch .Lfk_er_lktwom\n"
".Lfk_es_mtwo:\n"
"s_call_b64 s[94:95],.Lfk_meet4\n"
"s_branch .Lfk_er_mtwo\n"
".Lfk_es_lkloopm:\n"
"s_call_b64 s[94:95],.Lfk_look\n"
"s_branch .Lfk_er_lkloopm\n"
".Lfk_es_me3:\n"
"s_call_b64 s[94:95],.Lfk_meet4\n"
"s_branch .Lfk_er_me3\n"
".Lfk_es_me2:\n"
"s_call_b64 s[94:95],.Lfk_meet4\n"
"s_branch .Lfk_er_me2\n"
".Lfk_p1:\n"
"s_cmp_eq_u32 s18,2146435072\n"
"s_cbranch_scc1 .Lfk_p2\n"
".Lfk_p3:\n"
"s_lshr_b32 s22,s8,2\n"
"s_and_b32 s31,s8,0xfff1ffff\n"
"s_add_u32 s18,s18,16384\n"
"s_andn2_b32 s91,s55,16384\n"
"v_add_u32 v116,s91,v112\n"
"v_accvgpr_read_b32 v122,a140\n"
"v_add_f32 v122,v122,v103\n"
"v_rcp_f32 v122,v122\n"
"s_nop 0\n"
"v_pk_mul_f32 v[224:225],v[224:225],v[122:123] op_sel_hi:[1,0]\n"
"v_pk_mul_f32 v[226:227],v[226:227],v[122:123] op_sel_hi:[1,0]\n"
"v_pk_mul_f32 v[228:229],v[228:229],v[122:123] op_sel_hi:[1,0]\n"
"v_pk_mul_f32 v[230:231],v[230:231],v[122:123] op_sel_hi:[1,0]\n"
"v_pk_mul_f32 v[232:233],v[232:233],v[122:123] op_sel_hi:[1,0]\n"
"v_pk_mul_f32 v[234:235],v[234:235],v[122:123] op_sel_hi:[1,0]\n"
"v_pk_mul_f32 v[236:237],v[236:237],v[122:123] op_sel_hi:[1,0]\n"
"v_pk_mul_f32 v[238:239],v[238:239],v[122:123] op_sel_hi:[1,0]\n"
"v_pk_mul_f32 v[240:241],v[240:241],v[122:123] op_sel_hi:[1,0]\n"
"v_pk_mul_f32 v[242:243],v[242:243],v[122:123] op_sel_hi:[1,0]\n"
"v_pk_mul_f32 v[244:245],v[244:245],v[122:123] op_sel_hi:[1,0]\n"
"v_pk_mul_f32 v[246:247],v[246:247],v[122:123] op_sel_hi:[1,0]\n"
"v_pk_mul_f32 v[248:249],v[248:249],v[122:123] op_sel_hi:[1,0]\n"
"v_pk_mul_f32 v[250:251],v[250:251],v[122:123] op_sel_hi:[1,0]\n"
"v_pk_mul_f32 v[252:253],v[252:253],v[122:123] op_sel_hi:[1,0]\n"
"v_pk_mul_f32 v[254:255],v[254:255],v[122:123] op_sel_hi:[1,0]\n"
"s_waitcnt vmcnt(24) lgkmcnt(0)\n"
"s_barrier\n"
"ds_read_b128 v[192:195],v112 offset:0\n"
"ds_read_b128 v[196:199],v112 offset:1024\n"
"ds_read_b128 v[200:203],v112 offset:2048\n"
"ds_read_b128 v[204:207],v112 offset:3072\n"
"ds_read_b128 v[208:211],v112 offset:4096\n"
"ds_read_b128 v[212:215],v112 offset:5120\n"
"ds_read_b128 v[216:219],v112 offset:6144\n"
"ds_read_b128 v[220:223],v112 offset:7168\n"
"v_cvt_pk_bf16_f32 v224,v224,v225\n"
"v_cvt_pk_bf16_f32 v225,v226,v227\n"
"v_cvt_pk_bf16_f32 v226,v228,v229\n"
"v_cvt_pk_bf16_f32 v227,v230,v231\n"
"v_cvt_pk_bf16_f32 v232,v232,v233\n"
"v_cvt_pk_bf16_f32 v233,v234,v235\n"
"v_cvt_pk_bf16_f32 v234,v236,v237\n"
"v_cvt_pk_bf16_f32 v235,v238,v239\n"
"v_cvt_pk_bf16_f32 v240,v240,v241\n"
"v_cvt_pk_bf16_f32 v241,v242,v243\n"
"v_cvt_pk_bf16_f32 v242,v244,v245\n"
"v_cvt_pk_bf16_f32 v243,v246,v247\n"
"v_cvt_pk_bf16_f32 v248,v248,v249\n"
"v_cvt_pk_bf16_f32 v249,v250,v251\n"
"v_cvt_pk_bf16_f32 v250,v252,v253\n"
"v_cvt_pk_bf16_f32 v251,v254,v255\n"
"buffer_store_dwordx4 v[224:227],v112,s[24:27],s70 offen offset:0 sc1\n"
"buffer_store_dwordx4 v[232:235],v112,s[24:27],s70 offen offset:1024 sc1\n"
"buffer_store_dwordx4 v[240:243],v112,s[24:27],s70 offen offset:2048 sc1\n"
"buffer_store_dwordx4 v[248:251],v112,s[24:27],s70 offen offset:3072 sc1\n"
"s_waitcnt lgkmcnt(4)\n"
"v_mfma_f32_16x16x32_bf16 v[80:83],a[224:227],v[192:195],0\n"
"v_mfma_f32_16x16x32_bf16 v[80:83],a[232:235],v[196:199],v[80:83]\n"
"ds_read_b128 v[224:227],v112 offset:8192\n"
"ds_read_b128 v[228:231],v112 offset:9216\n"
"v_mfma_f32_16x16x32_bf16 v[80:83],a[240:243],v[200:203],v[80:83]\n"
"ds_read_b128 v[232:235],v112 offset:10240\n"
"ds_read_b128 v[236:239],v112 offset:11264\n"
"v_mfma_f32_16x16x32_bf16 v[80:83],a[248:251],v[204:207],v[80:83]\n"
"ds_read_b128 v[240:243],v112 offset:12288\n"
"ds_read_b128 v[244:247],v112 offset:13312\n"
"v_mfma_f32_16x16x32_bf16 v[84:87],a[252:255],v[204:207],0\n"
"ds_read_b128 v[248:251],v112 offset:14336\n"
"ds_read_b128 v[252:255],v112 offset:15360\n"
"v_mfma_f32_16x16x32_bf16 v[84:87],a[244:247],v[200:203],v[84:87]\n"
"ds_read_b128 a[68:71],v116 offset:4096\n"
"ds_read_b128 a[72:75],v116 offset:8192\n"
"v_mfma_f32_16x16x32_bf16 v[84:87],a[236:239],v[196:199],v[84:87]\n"
"ds_read_b128 a[76:79],v116 offset:12288\n"
"ds_read_b128 a[80:83],v116 offset:0\n"
"v_mfma_f32_16x16x32_bf16 v[84:87],a[228:231],v[192:195],v[84:87]\n"
"ds_read_b128 a[84:87],v116 offset:20480\n"
"ds_read_b128 a[88:91],v116 offset:24576\n"
"s_waitcnt lgkmcnt(14)\n"
"v_mfma_f32_16x16x32_bf16 v[84:87],a[4:7],v[208:211],v[84:87]\n"
"ds_read_b128 a[92:95],v116 offset:28672\n"
"v_mfma_f32_16x16x32_bf16 v[84:87],a[12:15],v[212:215],v[84:87]\n"
"ds_read_b128 v[204:207],v112 offset:19456\n"
"s_lshl_b32 s91,s78,3\n"
"v_mfma_f32_16x16x32_bf16 v[84:87],a[20:23],v[216:219],v[84:87]\n"
"ds_read_b128 v[200:203],v112 offset:18432\n"
"s_add_u32 s91,s91,131072\n"
"v_mfma_f32_16x16x32_bf16 v[84:87],a[28:31],v[220:223],v[84:87]\n"
"ds_read_b128 v[196:199],v112 offset:17408\n"
"v_lshrrev_b32 v101,2,v112\n"
"v_mfma_f32_16x16x32_bf16 v[80:83],a[24:27],v[220:223],v[80:83]\n"
"ds_read_b128 v[192:195],v112 offset:16384\n"
"v_add_u32 v101,s91,v101\n"
"v_mfma_f32_16x16x32_bf16 v[80:83],a[16:19],v[216:219],v[80:83]\n"
"ds_read_b128 v[220:223],v112 offset:23552\n"
"v_lshrrev_b32 v114,2,v112\n"
"v_mfma_f32_16x16x32_bf16 v[80:83],a[8:11],v[212:215],v[80:83]\n"
"ds_read_b128 v[216:219],v112 offset:22528\n"
"v_add_u32 v114,131328,v114\n"
"v_mfma_f32_16x16x32_bf16 v[80:83],a[0:3],v[208:211],v[80:83]\n"
"ds_read_b128 v[212:215],v112 offset:21504\n"
"s_waitcnt lgkmcnt(15)\n"
"v_mfma_f32_16x16x32_bf16 v[80:83],v[32:35],v[224:227],v[80:83]\n"
"ds_read_b128 v[208:211],v112 offset:20480\n"
"s_mov_b32 s83,148946944\n"
"v_mfma_f32_16x16x32_bf16 v[80:83],v[40:43],v[228:231],v[80:83]\n"
"buffer_load_dwordx4 v[104:107],v112,s[24:27],s83 offen offset:0 sc1\n"
"v_mov_b32 v116,0\n"
"v_mfma_f32_16x16x32_bf16 v[80:83],a[32:35],v[232:235],v[80:83]\n"
"ds_write_b32 v101,v116 offset:256\n"
"v_mov_b32 v116,1\n"
"v_mfma_f32_16x16x32_bf16 v[80:83],a[40:43],v[236:239],v[80:83]\n"
"buffer_load_dwordx4 v[108:111],v112,s[24:27],s83 offen offset:1024 sc1\n"
"v_mfma_f32_16x16x32_bf16 v[84:87],a[44:47],v[236:239],v[84:87]\n"
"s_mov_b32 s16,4\n"
"ds_read_b128 v[236:239],v112 offset:27648\n"
"v_add_u32 v113,65536,v112\n"
"v_mfma_f32_16x16x32_bf16 v[84:87],a[36:39],v[232:235],v[84:87]\n"
"ds_read_b128 v[232:235],v112 offset:26624\n"
"s_add_u32 s31,s31,8388608\n"
"v_mfma_f32_16x16x32_bf16 v[84:87],v[44:47],v[228:231],v[84:87]\n"
"ds_read_b128 v[228:231],v112 offset:25600\n"
"s_add_u32 s53,s22,786432\n"
"v_mfma_f32_16x16x32_bf16 v[84:87],v[36:39],v[224:227],v[84:87]\n"
"s_add_u32 m0,s30,65536\n"
"ds_read_b128 v[224:227],v112 offset:24576\n"
"s_waitcnt lgkmcnt(13)\n"
"v_mfma_f32_16x16x32_bf16 v[84:87],a[68:71],v[240:243],v[84:87]\n"
"buffer_load_dwordx4 v112,s[24:27],s18 offen offset:0 sc1 lds\n"
"v_mfma_f32_16x16x32_bf16 v[84:87],a[76:79],v[244:247],v[84:87]\n"
"buffer_load_dwordx4 v112,s[24:27],s18 offen offset:1024 sc1 lds\n"
"v_mfma_f32_16x16x32_bf16 v[84:87],a[84:87],v[248:251],v[84:87]\n"
"s_add_u32 s56,s22,1048576\n"
"s_add_u32 s58,s22,1310720\n"
"v_mfma_f32_16x16x32_bf16 v[84:87],a[92:95],v[252:255],v[84:87]\n"
"buffer_load_dwordx4 v112,s[24:27],s18 offen offset:2048 sc1 lds\n"
"v_mfma_f32_16x16x32_bf16 v[80:83],a[88:91],v[252:255],v[80:83]\n"
"ds_read_b128 v[252:255],v112 offset:31744\n"
"s_add_u32 s59,s22,1572864\n"
"v_mfma_f32_16x16x32_bf16 v[80:83],a[80:83],v[248:251],v[80:83]\n"
"ds_read_b128 v[248:251],v112 offset:30720\n"
"s_bfe_u32 s10,s3,0x20015\n"
"v_mfma_f32_16x16x32_bf16 v[80:83],a[72:75],v[244:247],v[80:83]\n"
"ds_read_b128 v[244:247],v112 offset:29696\n"
"s_mul_i32 s10,s10,4186112\n"
"v_mfma_f32_16x16x32_bf16 v[80:83],a[156:159],v[240:243],v[80:83]\n"
"ds_read_b128 v[240:243],v112 offset:28672\n"
"s_waitcnt lgkmcnt(13)\n"
"v_mfma_f32_16x16x32_bf16 v[64:67],a[224:227],v[192:195],0\n"
"buffer_load_dwordx4 v112,s[24:27],s18 offen offset:3072 sc1 lds\n"
"s_add_u32 m0,m0,4096\n"
"v_mfma_f32_16x16x32_bf16 v[64:67],a[232:235],v[196:199],v[64:67]\n"
"buffer_load_dwordx4 v117,s[24:27],s18 offen offset:0 sc1 lds\n"
"v_mfma_f32_16x16x32_bf16 v[64:67],a[240:243],v[200:203],v[64:67]\n"
"s_add_u32 s12,s12,s10\n"
"s_bfe_u32 s10,s3,0x30012\n"
"v_mfma_f32_16x16x32_bf16 v[64:67],a[248:251],v[204:207],v[64:67]\n"
"buffer_load_dwordx4 v117,s[24:27],s18 offen offset:1024 sc1 lds\n"
"v_mfma_f32_16x16x32_bf16 v[68:71],a[252:255],v[204:207],0\n"
"ds_read_b128 v[204:207],v112 offset:35840\n"
"s_lshl_b32 s10,s10,12\n"
"v_mfma_f32_16x16x32_bf16 v[68:71],a[244:247],v[200:203],v[68:71]\n"
"ds_read_b128 v[200:203],v112 offset:34816\n"
"s_add_u32 s12,s12,s10\n"
"v_mfma_f32_16x16x32_bf16 v[68:71],a[236:239],v[196:199],v[68:71]\n"
"ds_read_b128 v[196:199],v112 offset:33792\n"
"s_bfe_u32 s10,s3,0x50012\n"
"v_mfma_f32_16x16x32_bf16 v[68:71],a[228:231],v[192:195],v[68:71]\n"
"ds_read_b128 v[192:195],v112 offset:32768\n"
"s_waitcnt lgkmcnt(13)\n"
"s_waitcnt vmcnt(8) lgkmcnt(12)\n"
"s_barrier\n"
"v_mfma_f32_16x16x32_bf16 v[68:71],a[4:7],v[208:211],v[68:71]\n"
"buffer_store_dwordx2 a[154:155],off,s[24:27],s71 sc1\n"
"buffer_load_dwordx4 v117,s[24:27],s18 offen offset:2048 sc1 lds\n"
"v_mfma_f32_16x16x32_bf16 v[68:71],a[12:15],v[212:215],v[68:71]\n"
"buffer_load_dwordx4 v117,s[24:27],s18 offen offset:3072 sc1 lds\n"
"v_mfma_f32_16x16x32_bf16 v[68:71],a[20:23],v[216:219],v[68:71]\n"
"s_sub_u32 s82,s3,212992\n"
"s_lshl_b32 s10,s10,8\n"
"v_mfma_f32_16x16x32_bf16 v[68:71],a[28:31],v[220:223],v[68:71]\n"
"buffer_load_dwordx4 a[48:51],v112,s[24:27],s82 offen offset:0 sc1\n"
"v_mfma_f32_16x16x32_bf16 v[64:67],a[24:27],v[220:223],v[64:67]\n"
"s_add_u32 s10,s10,149061632\n"
"ds_read_b128 v[220:223],v112 offset:39936\n"
"s_mov_b32 s14,0\n"
"v_mfma_f32_16x16x32_bf16 v[64:67],a[16:19],v[216:219],v[64:67]\n"
"ds_read_b128 v[216:219],v112 offset:38912\n"
"v_not_b32 v122,s80\n"
"v_mfma_f32_16x16x32_bf16 v[64:67],a[8:11],v[212:215],v[64:67]\n"
"s_cmp_eq_u32 s52,0\n"
"ds_read_b128 v[212:215],v112 offset:37888\n"
"s_cselect_b64 s[14:15],s[14:15],0\n"
"v_mfma_f32_16x16x32_bf16 v[64:67],a[0:3],v[208:211],v[64:67]\n"
"s_cselect_b32 s10,s10,2146435072\n"
"ds_read_b128 v[208:211],v112 offset:36864\n"
"s_waitcnt lgkmcnt(12)\n"
"v_mfma_f32_16x16x32_bf16 v[64:67],v[32:35],v[224:227],v[64:67]\n"
"buffer_load_dwordx4 a[52:55],v112,s[24:27],s82 offen offset:1024 sc1\n"
"s_cmp_lg_u32 s15,0\n"
"s_cselect_b32 s91,s11,2146435072\n"
"v_mfma_f32_16x16x32_bf16 v[64:67],v[40:43],v[228:231],v[64:67]\n"
"buffer_load_dwordx4 a[56:59],v112,s[24:27],s82 offen offset:2048 sc1\n"
"buffer_load_dwordx2 v[122:123],v115,s[24:27],s91 offen sc1\n"
"v_mfma_f32_16x16x32_bf16 v[64:67],a[32:35],v[232:235],v[64:67]\n"
"s_mov_b32 s89,0\n"
"s_mov_b32 s20,32768\n"
"v_mfma_f32_16x16x32_bf16 v[64:67],a[40:43],v[236:239],v[64:67]\n"
"buffer_load_dwordx4 a[60:63],v112,s[24:27],s82 offen offset:3072 sc1\n"
"v_mfma_f32_16x16x32_bf16 v[68:71],a[44:47],v[236:239],v[68:71]\n"
"s_mov_b32 s19,0xffff\n"
"ds_read_b128 v[236:239],v112 offset:44032\n"
"s_mov_b32 s21,32\n"
"v_mfma_f32_16x16x32_bf16 v[68:71],a[36:39],v[232:235],v[68:71]\n"
"ds_read_b128 v[232:235],v112 offset:43008\n"
"v_mfma_f32_16x16x32_bf16 v[68:71],v[44:47],v[228:231],v[68:71]\n"
"ds_read_b128 v[228:231],v112 offset:41984\n"
"v_mfma_f32_16x16x32_bf16 v[68:71],v[36:39],v[224:227],v[68:71]\n"
"s_sub_u32 s82,s3,229376\n"
"ds_read_b128 v[224:227],v112 offset:40960\n"
"s_waitcnt lgkmcnt(12)\n"
"v_mfma_f32_16x16x32_bf16 v[68:71],a[68:71],v[240:243],v[68:71]\n"
"buffer_load_dwordx4 v[48:51],v112,s[24:27],s82 offen offset:0 sc1\n"
"v_mfma_f32_16x16x32_bf16 v[68:71],a[76:79],v[244:247],v[68:71]\n"
"buffer_load_dwordx4 v[52:55],v112,s[24:27],s82 offen offset:1024 sc1\n"
"v_mfma_f32_16x16x32_bf16 v[68:71],a[84:87],v[248:251],v[68:71]\n"
"buffer_load_dwordx4 v[56:59],v112,s[24:27],s82 offen offset:2048 sc1\n"
"v_mfma_f32_16x16x32_bf16 v[68:71],a[92:95],v[252:255],v[68:71]\n"
"s_waitcnt vmcnt(17)\n"
"v_mfma_f32_16x16x32_bf16 v[64:67],a[88:91],v[252:255],v[64:67]\n"
"ds_read_b128 v[252:255],v112 offset:48128\n"
"v_xor_b32 v119,s80,v104\n"
"v_mfma_f32_16x16x32_bf16 v[64:67],a[80:83],v[248:251],v[64:67]\n"
"ds_read_b128 v[248:251],v112 offset:47104\n"
"v_xor_b32 v120,s81,v105\n"
"v_mfma_f32_16x16x32_bf16 v[64:67],a[72:75],v[244:247],v[64:67]\n"
"ds_read_b128 v[244:247],v112 offset:46080\n"
"v_or_b32 v119,v119,v120\n"
"v_mfma_f32_16x16x32_bf16 v[64:67],a[156:159],v[240:243],v[64:67]\n"
"ds_read_b128 v[240:243],v112 offset:45056\n"
"s_waitcnt lgkmcnt(12)\n"
"v_mfma_f32_16x16x32_bf16 v[72:75],a[224:227],v[192:195],0\n"
"buffer_load_dwordx4 v[60:63],v112,s[24:27],s82 offen offset:3072 sc1\n"
"s_sub_u32 s82,s3,245760\n"
"v_mfma_f32_16x16x32_bf16 v[72:75],a[232:235],v[196:199],v[72:75]\n"
"buffer_load_dwordx4 v[16:19],v112,s[24:27],s82 offen offset:0 sc1\n"
"v_xor_b32 v120,s80,v106\n"
"v_mfma_f32_16x16x32_bf16 v[72:75],a[240:243],v[200:203],v[72:75]\n"
"v_or_b32 v119,v119,v120\n"
"v_xor_b32 v120,s81,v107\n"
"v_mfma_f32_16x16x32_bf16 v[72:75],a[248:251],v[204:207],v[72:75]\n"
"buffer_load_dwordx4 v[20:23],v112,s[24:27],s82 offen offset:1024 sc1\n"
"v_or_b32 v119,v119,v120\n"
"v_mfma_f32_16x16x32_bf16 v[76:79],a[252:255],v[204:207],0\n"
"ds_read_b128 v[204:207],v112 offset:52224\n"
"v_xor_b32 v120,s80,v108\n"
"v_mfma_f32_16x16x32_bf16 v[76:79],a[244:247],v[200:203],v[76:79]\n"
"ds_read_b128 v[200:203],v112 offset:51200\n"
"v_or_b32 v119,v119,v120\n"
"v_mfma_f32_16x16x32_bf16 v[76:79],a[236:239],v[196:199],v[76:79]\n"
"ds_read_b128 v[196:199],v112 offset:50176\n"
"v_xor_b32 v120,s81,v109\n"
"v_mfma_f32_16x16x32_bf16 v[76:79],a[228:231],v[192:195],v[76:79]\n"
"ds_read_b128 v[192:195],v112 offset:49152\n"
"s_waitcnt lgkmcnt(12)\n"
"v_mfma_f32_16x16x32_bf16 v[76:79],a[4:7],v[208:211],v[76:79]\n"
"buffer_load_dwordx4 v[24:27],v112,s[24:27],s82 offen offset:2048 sc1\n"
"v_or_b32 v119,v119,v120\n"
"v_mfma_f32_16x16x32_bf16 v[76:79],a[12:15],v[212:215],v[76:79]\n"
"buffer_load_dwordx4 v[28:31],v112,s[24:27],s82 offen offset:3072 sc1\n"
"v_mfma_f32_16x16x32_bf16 v[76:79],a[20:23],v[216:219],v[76:79]\n"
"s_add_u32 s18,s18,2097152\n"
"v_xor_b32 v120,s80,v110\n"
"s_add_u32 m0,s30,98304\n"
"v_mfma_f32_16x16x32_bf16 v[76:79],a[28:31],v[220:223],v[76:79]\n"
"buffer_load_dwordx4 v112,s[24:27],s18 offen offset:0 sc1 lds\n"
"v_mfma_f32_16x16x32_bf16 v[72:75],a[24:27],v[220:223],v[72:75]\n"
"ds_read_b128 v[220:223],v112 offset:56320\n"
"v_or_b32 v119,v119,v120\n"
"v_mfma_f32_16x16x32_bf16 v[72:75],a[16:19],v[216:219],v[72:75]\n"
"ds_read_b128 v[216:219],v112 offset:55296\n"
"v_xor_b32 v120,s81,v111\n"
"v_mfma_f32_16x16x32_bf16 v[72:75],a[8:11],v[212:215],v[72:75]\n"
"ds_read_b128 v[212:215],v112 offset:54272\n"
"v_or_b32 v119,v119,v120\n"
"v_mfma_f32_16x16x32_bf16 v[72:75],a[0:3],v[208:211],v[72:75]\n"
"ds_read_b128 v[208:211],v112 offset:53248\n"
"s_waitcnt lgkmcnt(12)\n"
"v_mfma_f32_16x16x32_bf16 v[72:75],v[32:35],v[224:227],v[72:75]\n"
"buffer_load_dwordx4 v112,s[24:27],s18 offen offset:1024 sc1 lds\n"
"v_mfma_f32_16x16x32_bf16 v[72:75],v[40:43],v[228:231],v[72:75]\n"
"buffer_load_dwordx4 v112,s[24:27],s18 offen offset:2048 sc1 lds\n"
"v_mfma_f32_16x16x32_bf16 v[72:75],a[32:35],v[232:235],v[72:75]\n"
"v_cmp_ne_u32 vcc,0,v119\n"
"v_mfma_f32_16x16x32_bf16 v[72:75],a[40:43],v[236:239],v[72:75]\n"
"buffer_load_dwordx4 v112,s[24:27],s18 offen offset:3072 sc1 lds\n"
"v_mfma_f32_16x16x32_bf16 v[76:79],a[44:47],v[236:239],v[76:79]\n"
"ds_read_b128 v[236:239],v112 offset:60416\n"
"v_mfma_f32_16x16x32_bf16 v[76:79],a[36:39],v[232:235],v[76:79]\n"
"ds_read_b128 v[232:235],v112 offset:59392\n"
"v_xor_b32 v119,s80,v122\n"
"v_mfma_f32_16x16x32_bf16 v[76:79],v[44:47],v[228:231],v[76:79]\n"
"ds_read_b128 v[228:231],v112 offset:58368\n"
"v_xor_b32 v120,s81,v123\n"
"v_mfma_f32_16x16x32_bf16 v[76:79],v[36:39],v[224:227],v[76:79]\n"
"s_add_u32 m0,m0,4096\n"
"ds_read_b128 v[224:227],v112 offset:57344\n"
"s_waitcnt lgkmcnt(12)\n"
"v_mfma_f32_16x16x32_bf16 v[76:79],a[68:71],v[240:243],v[76:79]\n"
"buffer_load_dwordx4 v117,s[24:27],s18 offen offset:0 sc1 lds\n"
"v_or_b32 v119,v119,v120\n"
"v_mfma_f32_16x16x32_bf16 v[76:79],a[76:79],v[244:247],v[76:79]\n"
"buffer_load_dwordx4 v117,s[24:27],s18 offen offset:1024 sc1 lds\n"
"v_cmp_ne_u32 s[28:29],0,v119\n"
"v_mfma_f32_16x16x32_bf16 v[76:79],a[84:87],v[248:251],v[76:79]\n"
"s_and_b64 s[14:15],s[14:15],s[28:29]\n"
"s_cselect_b32 s91,s11,2146435072\n"
"buffer_load_dwordx2 v[122:123],v115,s[24:27],s91 offen sc1\n"
"v_mfma_f32_16x16x32_bf16 v[76:79],a[92:95],v[252:255],v[76:79]\n"
"buffer_load_dwordx4 v117,s[24:27],s18 offen offset:2048 sc1 lds\n"
"v_mfma_f32_16x16x32_bf16 v[72:75],a[88:91],v[252:255],v[72:75]\n"
"ds_read_b128 v[252:255],v112 offset:64512\n"
"v_mfma_f32_16x16x32_bf16 v[72:75],a[80:83],v[248:251],v[72:75]\n"
"ds_read_b128 v[248:251],v112 offset:63488\n"
"v_mfma_f32_16x16x32_bf16 v[72:75],a[72:75],v[244:247],v[72:75]\n"
"ds_read_b128 v[244:247],v112 offset:62464\n"
"v_mfma_f32_16x16x32_bf16 v[72:75],a[156:159],v[240:243],v[72:75]\n"
"s_and_b32 s83,s15,0xffff\n"
"ds_read_b128 v[240:243],v112 offset:61440\n"
"s_waitcnt lgkmcnt(12)\n"
"v_mfma_f32_16x16x32_bf16 v[88:91],a[224:227],v[192:195],0\n"
"buffer_load_dwordx4 v117,s[24:27],s18 offen offset:3072 sc1 lds\n"
"v_mfma_f32_16x16x32_bf16 v[88:91],a[232:235],v[196:199],v[88:91]\n"
"s_cbranch_scc1 .Lfk_es_lkone\n"
".Lfk_er_lkone:\n"
"v_mfma_f32_16x16x32_bf16 v[88:91],a[240:243],v[200:203],v[88:91]\n"
"s_waitcnt vmcnt(22)\n"
"ds_add_u32 v114,v116\n"
"v_mfma_f32_16x16x32_bf16 v[88:91],a[248:251],v[204:207],v[88:91]\n"
"v_and_b32 v118,0xf0,v112\n"
"v_lshrrev_b32 v120,8,v112\n"
"v_lshlrev_b32 v120,4,v120\n"
"v_lshl_add_u32 v118,v118,2,v120\n"
"v_mfma_f32_16x16x32_bf16 v[92:95],a[252:255],v[204:207],0\n"
"s_lshr_b32 s82,s78,20\n"
"v_xor_b32 v118,s82,v118\n"
"v_mov_b32 v100,1\n"
"v_mfma_f32_16x16x32_bf16 v[92:95],a[244:247],v[200:203],v[92:95]\n"
"s_mov_b32 s77,0\n"
"v_mov_b32 v102,131076\n"
"s_and_b32 s91,s52,1\n"
"v_mfma_f32_16x16x32_bf16 v[92:95],a[236:239],v[196:199],v[92:95]\n"
"s_lshl_b32 s91,s91,7\n"
"v_xor_b32 v96,s91,v112\n"
"v_xor_b32 v97,32,v96\n"
"v_mfma_f32_16x16x32_bf16 v[92:95],a[228:231],v[192:195],v[92:95]\n"
"v_xor_b32 v99,96,v96\n"
"s_waitcnt lgkmcnt(9)\n"
"v_mfma_f32_16x16x32_bf16 v[92:95],a[4:7],v[208:211],v[92:95]\n"
"v_xor_b32 v98,64,v96\n"
"v_add_u32 v118,s12,v118\n"
"v_mfma_f32_16x16x32_bf16 v[92:95],a[12:15],v[212:215],v[92:95]\n"
"ds_read_b32 v125,v114\n"
"v_mfma_f32_16x16x32_bf16 v[92:95],a[20:23],v[216:219],v[92:95]\n"
"v_mfma_f32_16x16x32_bf16 v[92:95],a[28:31],v[220:223],v[92:95]\n"
"v_mfma_f32_16x16x32_bf16 v[88:91],a[24:27],v[220:223],v[88:91]\n"
"v_mfma_f32_16x16x32_bf16 v[88:91],a[16:19],v[216:219],v[88:91]\n"
"s_waitcnt lgkmcnt(0)\n"
"v_cmp_gt_u32 s[28:29],s16,v125\n"
"v_mfma_f32_16x16x32_bf16 v[88:91],a[8:11],v[212:215],v[88:91]\n"
"s_add_u32 s18,s8,4194304\n"
"s_add_u32 s8,s8,5242880\n"
"s_or_b64 vcc,vcc,s[28:29]\n"
"v_mfma_f32_16x16x32_bf16 v[88:91],a[0:3],v[208:211],v[88:91]\n"
"s_cbranch_vccnz .Lfk_z0\n"
".Lfk_z1:\n"
"ds_read_b128 v[192:195],v113 offset:0\n"
"v_mfma_f32_16x16x32_bf16 v[88:91],v[32:35],v[224:227],v[88:91]\n"
"s_mov_b32 s89,1\n"
"s_mov_b32 s19,0xff\n"
"s_lshl_b64 exec,1,s89\n"
"buffer_atomic_add v100,off,s[24:27],s10 sc0\n"
"s_mov_b64 exec,-1\n"
"v_mfma_f32_16x16x32_bf16 v[88:91],v[40:43],v[228:231],v[88:91]\n"
"ds_read_b128 v[196:199],v113 offset:1024\n"
"s_add_u32 m0,s30,0\n"
"v_mfma_f32_16x16x32_bf16 v[88:91],a[32:35],v[232:235],v[88:91]\n"
"ds_read_b128 v[200:203],v113 offset:2048\n"
"buffer_load_dwordx4 v112,s[24:27],s18 offen offset:0 sc1 lds\n"
"v_mfma_f32_16x16x32_bf16 v[88:91],a[40:43],v[236:239],v[88:91]\n"
"s_add_u32 s89,s89,1\n"
"ds_read_b128 v[204:207],v113 offset:3072\n"
"s_add_u32 s21,s21,16\n"
"v_mfma_f32_16x16x32_bf16 v[92:95],a[44:47],v[236:239],v[92:95]\n"
"s_mov_b32 s38,4194304\n"
"ds_read_b128 v[208:211],v113 offset:16384\n"
"s_mov_b32 s39,0x20000\n"
"v_mfma_f32_16x16x32_bf16 v[92:95],a[36:39],v[232:235],v[92:95]\n"
"s_add_u32 s16,s16,4\n"
"ds_read_b128 v[212:215],v113 offset:17408\n"
"s_mov_b32 s46,2048\n"
"v_mfma_f32_16x16x32_bf16 v[92:95],v[44:47],v[228:231],v[92:95]\n"
"ds_read_b128 v[216:219],v113 offset:18432\n"
"buffer_load_dwordx4 v112,s[24:27],s18 offen offset:1024 sc1 lds\n"
"v_mfma_f32_16x16x32_bf16 v[92:95],v[36:39],v[224:227],v[92:95]\n"
"s_mov_b32 s47,0x20000\n"
"ds_read_b128 v[220:223],v113 offset:19456\n"
"s_mov_b32 s50,4194304\n"
"v_mfma_f32_16x16x32_bf16 v[92:95],a[68:71],v[240:243],v[92:95]\n"
"s_mov_b32 s51,0x20000\n"
"ds_read_b128 v[236:239],v113 offset:7168\n"
"s_and_b32 s60,s100,7\n"
"v_mfma_f32_16x16x32_bf16 v[92:95],a[76:79],v[244:247],v[92:95]\n"
"s_lshl_b32 s60,s60,5\n"
"ds_read_b128 v[232:235],v113 offset:6144\n"
"s_lshr_b32 s73,s100,3\n"
"v_mfma_f32_16x16x32_bf16 v[92:95],a[84:87],v[248:251],v[92:95]\n"
"ds_read_b128 v[228:231],v113 offset:5120\n"
"buffer_load_dwordx4 v112,s[24:27],s18 offen offset:2048 sc1 lds\n"
"v_mfma_f32_16x16x32_bf16 v[92:95],a[92:95],v[252:255],v[92:95]\n"
"s_add_u32 s73,s73,s60\n"
"ds_read_b128 v[224:227],v113 offset:4096\n"
"s_and_b32 s60,s100,7\n"
"v_mfma_f32_16x16x32_bf16 v[88:91],a[88:91],v[252:255],v[88:91]\n"
"s_bfe_u32 s61,s100,0x10003\n"
"ds_read_b128 v[252:255],v113 offset:23552\n"
"s_lshl1_add_u32 s60,s60,s61\n"
"v_mfma_f32_16x16x32_bf16 v[88:91],a[80:83],v[248:251],v[88:91]\n"
"s_and_b32 s61,s100,0xf0\n"
"ds_read_b128 v[248:251],v113 offset:22528\n"
"s_add_u32 s60,s60,s61\n"
"v_mfma_f32_16x16x32_bf16 v[88:91],a[72:75],v[244:247],v[88:91]\n"
"s_lshl_b32 s61,s52,8\n"
"ds_read_b128 v[244:247],v113 offset:21504\n"
"s_add_u32 s60,s60,s61\n"
"v_mfma_f32_16x16x32_bf16 v[88:91],a[156:159],v[240:243],v[88:91]\n"
"ds_read_b128 v[240:243],v113 offset:20480\n"
"s_waitcnt lgkmcnt(12)\n"
"v_mfma_f32_16x16x32_bf16 v[80:83],v[128:131],v[192:195],v[80:83]\n"
"buffer_load_dwordx4 v112,s[24:27],s18 offen offset:3072 sc1 lds\n"
"v_mfma_f32_16x16x32_bf16 v[80:83],v[136:139],v[196:199],v[80:83]\n"
"s_lshl_b32 s61,s60,11\n"
"s_add_u32 s68,s61,2097152\n"
"v_mfma_f32_16x16x32_bf16 v[80:83],v[144:147],v[200:203],v[80:83]\n"
"s_add_u32 m0,m0,4096\n"
"v_mfma_f32_16x16x32_bf16 v[80:83],v[152:155],v[204:207],v[80:83]\n"
"buffer_load_dwordx4 v117,s[24:27],s18 offen offset:0 sc1 lds\n"
"v_mfma_f32_16x16x32_bf16 v[84:87],v[156:159],v[204:207],v[84:87]\n"
"ds_read_b128 v[204:207],v113 offset:11264\n"
"s_add_u32 s20,s20,32768\n"
"v_mfma_f32_16x16x32_bf16 v[84:87],v[148:151],v[200:203],v[84:87]\n"
"ds_read_b128 v[200:203],v113 offset:10240\n"
"s_and_b32 s20,s20,0x1ffff\n"
"v_mfma_f32_16x16x32_bf16 v[84:87],v[140:143],v[196:199],v[84:87]\n"
"ds_read_b128 v[196:199],v113 offset:9216\n"
"v_mfma_f32_16x16x32_bf16 v[84:87],v[132:135],v[192:195],v[84:87]\n"
"ds_read_b128 v[192:195],v113 offset:8192\n"
"s_waitcnt lgkmcnt(12)\n"
"v_mfma_f32_16x16x32_bf16 v[68:71],v[132:135],v[208:211],v[68:71]\n"
"buffer_load_dwordx4 v117,s[24:27],s18 offen offset:1024 sc1 lds\n"
"v_mfma_f32_16x16x32_bf16 v[68:71],v[140:143],v[212:215],v[68:71]\n"
"v_mfma_f32_16x16x32_bf16 v[68:71],v[148:151],v[216:219],v[68:71]\n"
"v_mfma_f32_16x16x32_bf16 v[68:71],v[156:159],v[220:223],v[68:71]\n"
"buffer_load_dwordx4 v117,s[24:27],s18 offen offset:2048 sc1 lds\n"
"v_mfma_f32_16x16x32_bf16 v[64:67],v[152:155],v[220:223],v[64:67]\n"
"ds_read_b128 v[220:223],v113 offset:27648\n"
"v_xor_b32 v119,s80,v122\n"
"v_mfma_f32_16x16x32_bf16 v[64:67],v[144:147],v[216:219],v[64:67]\n"
"ds_read_b128 v[216:219],v113 offset:26624\n"
"v_xor_b32 v120,s81,v123\n"
"v_mfma_f32_16x16x32_bf16 v[64:67],v[136:139],v[212:215],v[64:67]\n"
"ds_read_b128 v[212:215],v113 offset:25600\n"
"v_or_b32 v119,v119,v120\n"
"v_mfma_f32_16x16x32_bf16 v[64:67],v[128:131],v[208:211],v[64:67]\n"
"ds_read_b128 v[208:211],v113 offset:24576\n"
"s_waitcnt lgkmcnt(12)\n"
"v_mfma_f32_16x16x32_bf16 v[80:83],v[0:3],v[224:227],v[80:83]\n"
"buffer_load_dwordx4 v117,s[24:27],s18 offen offset:3072 sc1 lds\n"
"v_cmp_ne_u32 s[28:29],0,v119\n"
"v_mfma_f32_16x16x32_bf16 v[80:83],v[8:11],v[228:231],v[80:83]\n"
"s_and_b64 s[14:15],s[14:15],s[28:29]\n"
"s_cselect_b32 s91,s11,2146435072\n"
"s_add_u32 m0,s30,32768\n"
"v_mfma_f32_16x16x32_bf16 v[80:83],v[160:163],v[232:235],v[80:83]\n"
"buffer_load_dwordx2 v[122:123],v115,s[24:27],s91 offen sc1\n"
"v_mfma_f32_16x16x32_bf16 v[80:83],v[168:171],v[236:239],v[80:83]\n"
"buffer_load_dwordx4 v112,s[24:27],s8 offen offset:0 sc1 lds\n"
"v_mfma_f32_16x16x32_bf16 v[84:87],v[172:175],v[236:239],v[84:87]\n"
"ds_read_b128 v[236:239],v113 offset:15360\n"
"v_mfma_f32_16x16x32_bf16 v[84:87],v[164:167],v[232:235],v[84:87]\n"
"ds_read_b128 v[232:235],v113 offset:14336\n"
"v_mfma_f32_16x16x32_bf16 v[84:87],v[12:15],v[228:231],v[84:87]\n"
"ds_read_b128 v[228:231],v113 offset:13312\n"
"v_mfma_f32_16x16x32_bf16 v[84:87],v[4:7],v[224:227],v[84:87]\n"
"s_and_b32 s83,s15,0xff0000\n"
"ds_read_b128 v[224:227],v113 offset:12288\n"
"s_waitcnt lgkmcnt(12)\n"
"v_mfma_f32_16x16x32_bf16 v[68:71],v[4:7],v[240:243],v[68:71]\n"
"buffer_load_dwordx4 v112,s[24:27],s8 offen offset:1024 sc1 lds\n"
"v_mfma_f32_16x16x32_bf16 v[68:71],v[12:15],v[244:247],v[68:71]\n"
"s_cbranch_scc1 .Lfk_es_lkonem\n"
".Lfk_er_lkonem:\n"
"v_mfma_f32_16x16x32_bf16 v[68:71],v[164:167],v[248:251],v[68:71]\n"
"v_mfma_f32_16x16x32_bf16 v[68:71],v[172:175],v[252:255],v[68:71]\n"
"buffer_load_dwordx4 v112,s[24:27],s8 offen offset:2048 sc1 lds\n"
"v_mfma_f32_16x16x32_bf16 v[64:67],v[168:171],v[252:255],v[64:67]\n"
"ds_read_b128 v[252:255],v113 offset:31744\n"
"v_mfma_f32_16x16x32_bf16 v[64:67],v[160:163],v[248:251],v[64:67]\n"
"ds_read_b128 v[248:251],v113 offset:30720\n"
"v_mfma_f32_16x16x32_bf16 v[64:67],v[8:11],v[244:247],v[64:67]\n"
"ds_read_b128 v[244:247],v113 offset:29696\n"
"v_mfma_f32_16x16x32_bf16 v[64:67],v[0:3],v[240:243],v[64:67]\n"
"ds_read_b128 v[240:243],v113 offset:28672\n"
"s_waitcnt lgkmcnt(12)\n"
"v_mfma_f32_16x16x32_bf16 v[80:83],v[176:179],v[192:195],v[80:83]\n"
"s_waitcnt vmcnt(13)\n"
"ds_add_u32 v114,v116\n"
"v_mfma_f32_16x16x32_bf16 v[80:83],v[184:187],v[196:199],v[80:83]\n"
"buffer_load_dwordx4 v112,s[24:27],s8 offen offset:3072 sc1 lds\n"
"v_mfma_f32_16x16x32_bf16 v[80:83],a[48:51],v[200:203],v[80:83]\n"
"s_add_u32 m0,m0,4096\n"
"v_mfma_f32_16x16x32_bf16 v[80:83],a[56:59],v[204:207],v[80:83]\n"
"s_add_u32 s83,s20,32768\n"
"s_and_b32 s83,s83,0x1ffff\n"
"v_add_u32 v113,s83,v112\n"
"v_mfma_f32_16x16x32_bf16 v[84:87],a[60:63],v[204:207],v[84:87]\n"
"buffer_load_dwordx4 v117,s[24:27],s8 offen offset:0 sc1 lds\n"
"v_mfma_f32_16x16x32_bf16 v[84:87],a[52:55],v[200:203],v[84:87]\n"
"s_cmp_eq_u32 s52,0\n"
"v_not_b32 v104,s80\n"
"s_cselect_b32 s9,s9,2146435072\n"
"v_mfma_f32_16x16x32_bf16 v[84:87],v[188:191],v[196:199],v[84:87]\n"
"buffer_load_dwordx2 v[104:105],v115,s[24:27],s9 offen sc1\n"
"v_mfma_f32_16x16x32_bf16 v[84:87],v[180:183],v[192:195],v[84:87]\n"
"buffer_load_dwordx4 v117,s[24:27],s8 offen offset:1024 sc1 lds\n"
"s_waitcnt lgkmcnt(9)\n"
"v_mfma_f32_16x16x32_bf16 v[68:71],v[180:183],v[208:211],v[68:71]\n"
"v_mfma_f32_16x16x32_bf16 v[68:71],v[188:191],v[212:215],v[68:71]\n"
"ds_read_b32 v125,v114\n"
"v_mfma_f32_16x16x32_bf16 v[68:71],a[52:55],v[216:219],v[68:71]\n"
"buffer_load_dwordx4 v117,s[24:27],s8 offen offset:2048 sc1 lds\n"
"v_mfma_f32_16x16x32_bf16 v[68:71],a[60:63],v[220:223],v[68:71]\n"
"v_mfma_f32_16x16x32_bf16 v[64:67],a[56:59],v[220:223],v[64:67]\n"
"v_mfma_f32_16x16x32_bf16 v[64:67],a[48:51],v[216:219],v[64:67]\n"
"s_waitcnt lgkmcnt(0)\n"
"v_cmp_gt_u32 vcc,s16,v125\n"
"v_mfma_f32_16x16x32_bf16 v[64:67],v[184:187],v[212:215],v[64:67]\n"
"buffer_load_dwordx4 v117,s[24:27],s8 offen offset:3072 sc1 lds\n"
"v_mfma_f32_16x16x32_bf16 v[64:67],v[176:179],v[208:211],v[64:67]\n"
"s_lshl_b64 exec,1,s89\n"
"buffer_atomic_add v100,off,s[24:27],s10 sc0\n"
"s_mov_b64 exec,-1\n"
"s_add_u32 s8,s8,1048576\n"
"s_cbranch_vccnz .Lfk_es_mone\n"
".Lfk_er_mone:\n"
"v_mfma_f32_16x16x32_bf16 v[80:83],v[48:51],v[224:227],v[80:83]\n"
"ds_read_b128 v[192:195],v113 offset:0\n"
"v_mfma_f32_16x16x32_bf16 v[80:83],v[56:59],v[228:231],v[80:83]\n"
"ds_read_b128 v[196:199],v113 offset:1024\n"
"ds_read_b128 v[200:203],v113 offset:2048\n"
"v_mfma_f32_16x16x32_bf16 v[80:83],v[16:19],v[232:235],v[80:83]\n"
"buffer_load_dwordx4 a[96:99],v112,s[36:39],s61 offen offset:0\n"
"v_mfma_f32_16x16x32_bf16 v[80:83],v[24:27],v[236:239],v[80:83]\n"
"s_add_u32 s89,s89,1\n"
"ds_read_b128 v[204:207],v113 offset:3072\n"
"s_add_u32 s21,s21,8\n"
"v_mfma_f32_16x16x32_bf16 v[84:87],v[28:31],v[236:239],v[84:87]\n"
"ds_read_b128 v[208:211],v113 offset:4096\n"
"ds_read_b128 v[212:215],v113 offset:5120\n"
"v_mfma_f32_16x16x32_bf16 v[84:87],v[20:23],v[232:235],v[84:87]\n"
"buffer_load_dwordx4 a[100:103],v112,s[36:39],s61 offen offset:1024\n"
"v_mfma_f32_16x16x32_bf16 v[84:87],v[60:63],v[228:231],v[84:87]\n"
"s_add_u32 s16,s16,4\n"
"ds_read_b128 v[216:219],v113 offset:6144\n"
"v_mfma_f32_16x16x32_bf16 v[84:87],v[52:55],v[224:227],v[84:87]\n"
"ds_read_b128 v[220:223],v113 offset:7168\n"
"ds_read_b128 v[236:239],v113 offset:11264\n"
"v_mfma_f32_16x16x32_bf16 v[68:71],v[52:55],v[240:243],v[68:71]\n"
"buffer_load_dwordx4 a[104:107],v112,s[36:39],s68 offen offset:0\n"
"v_mfma_f32_16x16x32_bf16 v[68:71],v[60:63],v[244:247],v[68:71]\n"
"ds_read_b128 v[232:235],v113 offset:10240\n"
"v_cvt_pk_bf16_f32 v80,v80,v81\n"
"v_mfma_f32_16x16x32_bf16 v[68:71],v[20:23],v[248:251],v[68:71]\n"
"ds_read_b128 v[228:231],v113 offset:9216\n"
"ds_read_b128 v[224:227],v113 offset:8192\n"
"v_cvt_pk_bf16_f32 v81,v82,v83\n"
"v_mfma_f32_16x16x32_bf16 v[68:71],v[28:31],v[252:255],v[68:71]\n"
"buffer_load_dwordx4 a[108:111],v112,s[36:39],s68 offen offset:1024\n"
"v_mfma_f32_16x16x32_bf16 v[64:67],v[24:27],v[252:255],v[64:67]\n"
"ds_read_b128 v[252:255],v113 offset:15360\n"
"v_cvt_pk_bf16_f32 v82,v84,v85\n"
"v_cvt_pk_bf16_f32 v83,v86,v87\n"
"v_mfma_f32_16x16x32_bf16 v[64:67],v[16:19],v[248:251],v[64:67]\n"
"ds_read_b128 v[248:251],v113 offset:14336\n"
"v_max_u32 v80,1,v80\n"
"v_max_u32 v81,1,v81\n"
"v_mfma_f32_16x16x32_bf16 v[64:67],v[56:59],v[244:247],v[64:67]\n"
"ds_read_b128 v[244:247],v113 offset:13312\n"
"v_max_u32 v82,1,v82\n"
"v_max_u32 v83,1,v83\n"
"v_mfma_f32_16x16x32_bf16 v[64:67],v[48:51],v[240:243],v[64:67]\n"
"ds_read_b128 v[240:243],v113 offset:12288\n"
"buffer_store_dwordx4 v[80:83],v118,s[24:27],s22 offen sc1\n"
"s_waitcnt lgkmcnt(12)\n"
"v_mfma_f32_16x16x32_bf16 v[72:75],v[128:131],v[192:195],v[72:75]\n"
"s_add_u32 m0,s30,s20\n"
"s_add_u32 s22,s22,262144\n"
"v_mfma_f32_16x16x32_bf16 v[72:75],v[136:139],v[196:199],v[72:75]\n"
"buffer_load_dwordx4 v112,s[24:27],s8 offen offset:0 sc1 lds\n"
"v_mfma_f32_16x16x32_bf16 v[72:75],v[144:147],v[200:203],v[72:75]\n"
"v_mfma_f32_16x16x32_bf16 v[72:75],v[152:155],v[204:207],v[72:75]\n"
"v_cvt_pk_bf16_f32 v64,v64,v65\n"
"v_mfma_f32_16x16x32_bf16 v[76:79],v[156:159],v[204:207],v[76:79]\n"
"ds_read_b128 v[204:207],v113 offset:19456\n"
"s_add_u32 s20,s20,32768\n"
"v_mfma_f32_16x16x32_bf16 v[76:79],v[148:151],v[200:203],v[76:79]\n"
"ds_read_b128 v[200:203],v113 offset:18432\n"
"s_and_b32 s20,s20,0x1ffff\n"
"v_mfma_f32_16x16x32_bf16 v[76:79],v[140:143],v[196:199],v[76:79]\n"
"ds_read_b128 v[196:199],v113 offset:17408\n"
"v_cvt_pk_bf16_f32 v65,v66,v67\n"
"v_mfma_f32_16x16x32_bf16 v[76:79],v[132:135],v[192:195],v[76:79]\n"
"ds_read_b128 v[192:195],v113 offset:16384\n"
"v_cvt_pk_bf16_f32 v66,v68,v69\n"
"s_waitcnt lgkmcnt(12)\n"
"v_mfma_f32_16x16x32_bf16 v[76:79],v[4:7],v[208:211],v[76:79]\n"
"buffer_load_dwordx4 v112,s[24:27],s8 offen offset:1024 sc1 lds\n"
"v_cvt_pk_bf16_f32 v67,v70,v71\n"
"v_mfma_f32_16x16x32_bf16 v[76:79],v[12:15],v[212:215],v[76:79]\n"
"v_max_u32 v64,1,v64\n"
"v_max_u32 v65,1,v65\n"
"v_max_u32 v66,1,v66\n"
"v_max_u32 v67,1,v67\n"
"v_mfma_f32_16x16x32_bf16 v[76:79],v[164:167],v[216:219],v[76:79]\n"
"buffer_store_dwordx4 v[64:67],v118,s[24:27],s22 offen sc1\n"
"v_mfma_f32_16x16x32_bf16 v[76:79],v[172:175],v[220:223],v[76:79]\n"
"buffer_load_dwordx4 v112,s[24:27],s8 offen offset:2048 sc1 lds\n"
"v_mfma_f32_16x16x32_bf16 v[72:75],v[168:171],v[220:223],v[72:75]\n"
"s_add_u32 s22,s22,262144\n"
"ds_read_b128 v[220:223],v113 offset:23552\n"
"v_mfma_f32_16x16x32_bf16 v[72:75],v[160:163],v[216:219],v[72:75]\n"
"ds_read_b128 v[216:219],v113 offset:22528\n"
"v_mfma_f32_16x16x32_bf16 v[72:75],v[8:11],v[212:215],v[72:75]\n"
"ds_read_b128 v[212:215],v113 offset:21504\n"
"v_mfma_f32_16x16x32_bf16 v[72:75],v[0:3],v[208:211],v[72:75]\n"
"ds_read_b128 v[208:211],v113 offset:20480\n"
"s_waitcnt lgkmcnt(12)\n"
"v_mfma_f32_16x16x32_bf16 v[72:75],v[176:179],v[224:227],v[72:75]\n"
"buffer_load_dwordx4 v112,s[24:27],s8 offen offset:3072 sc1 lds\n"
"v_mfma_f32_16x16x32_bf16 v[72:75],v[184:187],v[228:231],v[72:75]\n"
"v_xor_b32 v119,s80,v122\n"
"v_xor_b32 v120,s81,v123\n"
"v_mfma_f32_16x16x32_bf16 v[72:75],a[48:51],v[232:235],v[72:75]\n"
"v_or_b32 v119,v119,v120\n"
"v_cmp_ne_u32 s[28:29],0,v119\n"
"v_mfma_f32_16x16x32_bf16 v[72:75],a[56:59],v[236:239],v[72:75]\n"
"s_and_b64 s[14:15],s[14:15],s[28:29]\n"
"v_mfma_f32_16x16x32_bf16 v[76:79],a[60:63],v[236:239],v[76:79]\n"
"ds_read_b128 v[236:239],v113 offset:27648\n"
"v_mfma_f32_16x16x32_bf16 v[76:79],a[52:55],v[232:235],v[76:79]\n"
"ds_read_b128 v[232:235],v113 offset:26624\n"
"v_mfma_f32_16x16x32_bf16 v[76:79],v[188:191],v[228:231],v[76:79]\n"
"ds_read_b128 v[228:231],v113 offset:25600\n"
"v_mfma_f32_16x16x32_bf16 v[76:79],v[180:183],v[224:227],v[76:79]\n"
"s_add_u32 m0,m0,4096\n"
"ds_read_b128 v[224:227],v113 offset:24576\n"
"s_waitcnt lgkmcnt(12)\n"
"v_mfma_f32_16x16x32_bf16 v[76:79],v[52:55],v[240:243],v[76:79]\n"
"buffer_load_dwordx4 v117,s[24:27],s8 offen offset:0 sc1 lds\n"
"v_mfma_f32_16x16x32_bf16 v[76:79],v[60:63],v[244:247],v[76:79]\n"
"s_and_b32 s83,s15,0xff000000\n"
"s_cbranch_scc1 .Lfk_es_lktwom\n"
".Lfk_er_lktwom:\n"
"v_mfma_f32_16x16x32_bf16 v[76:79],v[20:23],v[248:251],v[76:79]\n"
"v_xor_b32 v119,s80,v104\n"
"v_xor_b32 v120,s81,v105\n"
"v_or_b32 v119,v119,v120\n"
"v_mfma_f32_16x16x32_bf16 v[76:79],v[28:31],v[252:255],v[76:79]\n"
"buffer_load_dwordx4 v117,s[24:27],s8 offen offset:1024 sc1 lds\n"
"v_cmp_ne_u32 s[28:29],0,v119\n"
"v_mfma_f32_16x16x32_bf16 v[72:75],v[24:27],v[252:255],v[72:75]\n"
"s_cmp_eq_u32 s52,0\n"
"ds_read_b128 v[252:255],v113 offset:31744\n"
"s_cselect_b64 s[14:15],-1,0\n"
"v_mfma_f32_16x16x32_bf16 v[72:75],v[16:19],v[248:251],v[72:75]\n"
"s_mov_b32 s11,s9\n"
"ds_read_b128 v[248:251],v113 offset:30720\n"
"s_waitcnt vmcnt(22)\n"
"v_not_b32 v122,s80\n"
"v_mfma_f32_16x16x32_bf16 v[72:75],v[56:59],v[244:247],v[72:75]\n"
"s_and_b64 s[14:15],s[14:15],s[28:29]\n"
"s_cselect_b32 s91,s11,2146435072\n"
"ds_read_b128 v[244:247],v113 offset:29696\n"
"buffer_load_dwordx2 v[122:123],v115,s[24:27],s91 offen sc1\n"
"v_mfma_f32_16x16x32_bf16 v[72:75],v[48:51],v[240:243],v[72:75]\n"
"ds_read_b128 v[240:243],v113 offset:28672\n"
"s_waitcnt lgkmcnt(12)\n"
"v_mfma_f32_16x16x32_bf16 v[88:91],v[128:131],v[192:195],v[88:91]\n"
"ds_write_b32 v101,v100\n"
"v_mfma_f32_16x16x32_bf16 v[88:91],v[136:139],v[196:199],v[88:91]\n"
"ds_add_u32 v114,v116\n"
"v_mfma_f32_16x16x32_bf16 v[88:91],v[144:147],v[200:203],v[88:91]\n"
"buffer_load_dwordx4 v117,s[24:27],s8 offen offset:2048 sc1 lds\n"
"v_mfma_f32_16x16x32_bf16 v[88:91],v[152:155],v[204:207],v[88:91]\n"
"s_add_u32 s83,s20,32768\n"
"s_and_b32 s83,s83,0x1ffff\n"
"v_add_u32 v113,s83,v112\n"
"v_mfma_f32_16x16x32_bf16 v[92:95],v[156:159],v[204:207],v[92:95]\n"
"v_cvt_pk_bf16_f32 v72,v72,v73\n"
"v_cvt_pk_bf16_f32 v73,v74,v75\n"
"v_cvt_pk_bf16_f32 v74,v76,v77\n"
"v_mfma_f32_16x16x32_bf16 v[92:95],v[148:151],v[200:203],v[92:95]\n"
"v_cvt_pk_bf16_f32 v75,v78,v79\n"
"v_max_u32 v72,1,v72\n"
"v_max_u32 v73,1,v73\n"
"v_mfma_f32_16x16x32_bf16 v[92:95],v[140:143],v[196:199],v[92:95]\n"
"v_max_u32 v74,1,v74\n"
"v_max_u32 v75,1,v75\n"
"v_mfma_f32_16x16x32_bf16 v[92:95],v[132:135],v[192:195],v[92:95]\n"
"buffer_store_dwordx4 v[72:75],v118,s[24:27],s22 offen sc1\n"
"s_waitcnt lgkmcnt(10)\n"
"v_mfma_f32_16x16x32_bf16 v[92:95],v[4:7],v[208:211],v[92:95]\n"
"buffer_load_dwordx4 v117,s[24:27],s8 offen offset:3072 sc1 lds\n"
"v_mfma_f32_16x16x32_bf16 v[92:95],v[12:15],v[212:215],v[92:95]\n"
"s_add_u32 s22,s22,262144\n"
"ds_read_b32 v125,v114\n"
"s_add_u32 s77,s77,4096\n"
"s_add_u32 m0,s30,135168\n"
"v_mfma_f32_16x16x32_bf16 v[92:95],v[164:167],v[216:219],v[92:95]\n"
"buffer_load_dwordx4 v98,s[40:43],s77 offen offset:0 nt lds\n"
"v_mfma_f32_16x16x32_bf16 v[92:95],v[172:175],v[220:223],v[92:95]\n"
"buffer_load_dwordx4 v98,s[40:43],s77 offen offset:1024 nt lds\n"
"v_mfma_f32_16x16x32_bf16 v[88:91],v[168:171],v[220:223],v[88:91]\n"
"s_lshl_b64 exec,1,s89\n"
"buffer_atomic_add v100,off,s[24:27],s10 sc0\n"
"s_mov_b64 exec,-1\n"
"v_mfma_f32_16x16x32_bf16 v[88:91],v[160:163],v[216:219],v[88:91]\n"
"s_waitcnt lgkmcnt(0)\n"
"buffer_load_dwordx4 v99,s[40:43],s77 offen offset:2048 nt lds\n"
"v_cmp_gt_u32 vcc,s16,v125\n"
"v_mfma_f32_16x16x32_bf16 v[88:91],v[8:11],v[212:215],v[88:91]\n"
"buffer_load_dwordx4 v99,s[40:43],s77 offen offset:3072 nt lds\n"
"v_mfma_f32_16x16x32_bf16 v[88:91],v[0:3],v[208:211],v[88:91]\n"
"s_add_u32 s8,s8,1048576\n"
"s_cbranch_vccnz .Lfk_es_mtwo\n"
".Lfk_er_mtwo:\n"
".Lfk_l4:\n"
"v_mfma_f32_16x16x32_bf16 v[88:91],v[176:179],v[224:227],v[88:91]\n"
"ds_read_b128 v[192:195],v113 offset:0\n"
"v_mfma_f32_16x16x32_bf16 v[88:91],v[184:187],v[228:231],v[88:91]\n"
"ds_read_b128 v[196:199],v113 offset:1024\n"
"v_mfma_f32_16x16x32_bf16 v[88:91],a[48:51],v[232:235],v[88:91]\n"
"ds_read_b128 v[200:203],v113 offset:2048\n"
"s_add_u32 s89,s89,1\n"
"v_mfma_f32_16x16x32_bf16 v[88:91],a[56:59],v[236:239],v[88:91]\n"
"ds_read_b128 v[204:207],v113 offset:3072\n"
"v_mfma_f32_16x16x32_bf16 v[92:95],a[60:63],v[236:239],v[92:95]\n"
"ds_read_b128 v[208:211],v113 offset:4096\n"
"v_mfma_f32_16x16x32_bf16 v[92:95],a[52:55],v[232:235],v[92:95]\n"
"ds_read_b128 v[212:215],v113 offset:5120\n"
"s_add_u32 s16,s16,4\n"
"v_mfma_f32_16x16x32_bf16 v[92:95],v[188:191],v[228:231],v[92:95]\n"
"ds_read_b128 v[216:219],v113 offset:6144\n"
"v_mfma_f32_16x16x32_bf16 v[92:95],v[180:183],v[224:227],v[92:95]\n"
"ds_read_b128 v[220:223],v113 offset:7168\n"
"v_mfma_f32_16x16x32_bf16 v[92:95],v[52:55],v[240:243],v[92:95]\n"
"ds_read_b128 v[236:239],v113 offset:11264\n"
"v_mfma_f32_16x16x32_bf16 v[92:95],v[60:63],v[244:247],v[92:95]\n"
"ds_read_b128 v[232:235],v113 offset:10240\n"
"v_mfma_f32_16x16x32_bf16 v[92:95],v[20:23],v[248:251],v[92:95]\n"
"ds_read_b128 v[228:231],v113 offset:9216\n"
"v_mfma_f32_16x16x32_bf16 v[92:95],v[28:31],v[252:255],v[92:95]\n"
"ds_read_b128 v[224:227],v113 offset:8192\n"
"v_mfma_f32_16x16x32_bf16 v[88:91],v[24:27],v[252:255],v[88:91]\n"
"ds_read_b128 v[252:255],v113 offset:15360\n"
"v_mfma_f32_16x16x32_bf16 v[88:91],v[16:19],v[248:251],v[88:91]\n"
"ds_read_b128 v[248:251],v113 offset:14336\n"
"v_mfma_f32_16x16x32_bf16 v[88:91],v[56:59],v[244:247],v[88:91]\n"
"ds_read_b128 v[244:247],v113 offset:13312\n"
"v_mfma_f32_16x16x32_bf16 v[88:91],v[48:51],v[240:243],v[88:91]\n"
"ds_read_b128 v[240:243],v113 offset:12288\n"
"s_waitcnt lgkmcnt(12)\n"
"v_mfma_f32_16x16x32_bf16 v[80:83],a[224:227],v[192:195],0\n"
"s_add_u32 m0,s30,s20\n"
"ds_read_b32 v103,v102\n"
"v_mfma_f32_16x16x32_bf16 v[80:83],a[232:235],v[196:199],v[80:83]\n"
"buffer_load_dwordx4 v112,s[24:27],s8 offen offset:0 sc1 lds\n"
"v_mfma_f32_16x16x32_bf16 v[80:83],a[240:243],v[200:203],v[80:83]\n"
"v_add_u32 v102,4,v102\n"
"v_and_b32 v102,0x200ff,v102\n"
"v_mfma_f32_16x16x32_bf16 v[80:83],a[248:251],v[204:207],v[80:83]\n"
"v_cvt_pk_bf16_f32 v88,v88,v89\n"
"v_cvt_pk_bf16_f32 v89,v90,v91\n"
"v_mfma_f32_16x16x32_bf16 v[84:87],a[252:255],v[204:207],0\n"
"ds_read_b128 v[204:207],v113 offset:19456\n"
"s_add_u32 s20,s20,32768\n"
"v_mfma_f32_16x16x32_bf16 v[84:87],a[244:247],v[200:203],v[84:87]\n"
"ds_read_b128 v[200:203],v113 offset:18432\n"
"s_and_b32 s20,s20,0x1ffff\n"
"v_mfma_f32_16x16x32_bf16 v[84:87],a[236:239],v[196:199],v[84:87]\n"
"ds_read_b128 v[196:199],v113 offset:17408\n"
"v_cvt_pk_bf16_f32 v90,v92,v93\n"
"v_mfma_f32_16x16x32_bf16 v[84:87],a[228:231],v[192:195],v[84:87]\n"
"ds_read_b128 v[192:195],v113 offset:16384\n"
"s_waitcnt lgkmcnt(13)\n"
"v_mfma_f32_16x16x32_bf16 v[84:87],a[4:7],v[208:211],v[84:87]\n"
"buffer_load_dwordx4 v112,s[24:27],s8 offen offset:1024 sc1 lds\n"
"v_mfma_f32_16x16x32_bf16 v[84:87],a[12:15],v[212:215],v[84:87]\n"
"v_cvt_pk_bf16_f32 v91,v94,v95\n"
"v_max_u32 v88,1,v88\n"
"v_mfma_f32_16x16x32_bf16 v[84:87],a[20:23],v[216:219],v[84:87]\n"
"v_max_u32 v89,1,v89\n"
"v_max_u32 v90,1,v90\n"
"v_mfma_f32_16x16x32_bf16 v[84:87],a[28:31],v[220:223],v[84:87]\n"
"buffer_load_dwordx4 v112,s[24:27],s8 offen offset:2048 sc1 lds\n"
"v_mfma_f32_16x16x32_bf16 v[80:83],a[24:27],v[220:223],v[80:83]\n"
"ds_read_b128 v[220:223],v113 offset:23552\n"
"v_max_u32 v91,1,v91\n"
"v_mfma_f32_16x16x32_bf16 v[80:83],a[16:19],v[216:219],v[80:83]\n"
"ds_read_b128 v[216:219],v113 offset:22528\n"
"buffer_store_dwordx4 v[88:91],v118,s[24:27],s53 offen sc1\n"
"v_mfma_f32_16x16x32_bf16 v[80:83],a[8:11],v[212:215],v[80:83]\n"
"ds_read_b128 v[212:215],v113 offset:21504\n"
"v_mfma_f32_16x16x32_bf16 v[80:83],a[0:3],v[208:211],v[80:83]\n"
"ds_read_b128 v[208:211],v113 offset:20480\n"
"s_waitcnt lgkmcnt(13)\n"
"v_mfma_f32_16x16x32_bf16 v[80:83],v[32:35],v[224:227],v[80:83]\n"
"v_mfma_f32_16x16x32_bf16 v[80:83],v[40:43],v[228:231],v[80:83]\n"
"buffer_load_dwordx4 v112,s[24:27],s8 offen offset:3072 sc1 lds\n"
"v_mfma_f32_16x16x32_bf16 v[80:83],a[32:35],v[232:235],v[80:83]\n"
"s_waitcnt lgkmcnt(8)\n"
"v_readfirstlane_b32 s22,v103\n"
"v_mfma_f32_16x16x32_bf16 v[80:83],a[40:43],v[236:239],v[80:83]\n"
"v_mfma_f32_16x16x32_bf16 v[84:87],a[44:47],v[236:239],v[84:87]\n"
"ds_read_b128 v[236:239],v113 offset:27648\n"
"s_and_b32 s21,s22,0x38\n"
"v_mfma_f32_16x16x32_bf16 v[84:87],a[36:39],v[232:235],v[84:87]\n"
"ds_read_b128 v[232:235],v113 offset:26624\n"
"v_mfma_f32_16x16x32_bf16 v[84:87],v[44:47],v[228:231],v[84:87]\n"
"ds_read_b128 v[228:231],v113 offset:25600\n"
"v_mfma_f32_16x16x32_bf16 v[84:87],v[36:39],v[224:227],v[84:87]\n"
"ds_read_b128 v[224:227],v113 offset:24576\n"
"s_add_u32 m0,m0,4096\n"
"v_mfma_f32_16x16x32_bf16 v[84:87],a[68:71],v[240:243],v[84:87]\n"
"buffer_load_dwordx4 v117,s[24:27],s8 offen offset:0 sc1 lds\n"
"v_mfma_f32_16x16x32_bf16 v[84:87],a[76:79],v[244:247],v[84:87]\n"
"s_cmp_lg_u64 s[14:15],0\n"
"s_cbranch_scc1 .Lfk_es_lkloopm\n"
".Lfk_er_lkloopm:\n"
"v_mfma_f32_16x16x32_bf16 v[84:87],a[84:87],v[248:251],v[84:87]\n"
"v_mfma_f32_16x16x32_bf16 v[84:87],a[92:95],v[252:255],v[84:87]\n"
"buffer_load_dwordx4 v117,s[24:27],s8 offen offset:1024 sc1 lds\n"
"v_mfma_f32_16x16x32_bf16 v[80:83],a[88:91],v[252:255],v[80:83]\n"
"ds_read_b128 v[252:255],v113 offset:31744\n"
"s_mov_b32 s53,s56\n"
"v_mfma_f32_16x16x32_bf16 v[80:83],a[80:83],v[248:251],v[80:83]\n"
"ds_read_b128 v[248:251],v113 offset:30720\n"
"s_mov_b32 s56,s58\n"
"v_mfma_f32_16x16x32_bf16 v[80:83],a[72:75],v[244:247],v[80:83]\n"
"ds_read_b128 v[244:247],v113 offset:29696\n"
"s_mov_b32 s58,s59\n"
"v_mfma_f32_16x16x32_bf16 v[80:83],a[156:159],v[240:243],v[80:83]\n"
"ds_read_b128 v[240:243],v113 offset:28672\n"
"s_waitcnt lgkmcnt(12)\n"
"v_mfma_f32_16x16x32_bf16 v[88:91],v[128:131],v[192:195],v[80:83]\n"
"s_waitcnt vmcnt(17)\n"
"ds_write_b32 v101,v100\n"
"v_mfma_f32_16x16x32_bf16 v[88:91],v[136:139],v[196:199],v[88:91]\n"
"ds_add_u32 v114,v116\n"
"v_mfma_f32_16x16x32_bf16 v[88:91],v[144:147],v[200:203],v[88:91]\n"
"buffer_load_dwordx4 v117,s[24:27],s8 offen offset:2048 sc1 lds\n"
"v_mfma_f32_16x16x32_bf16 v[88:91],v[152:155],v[204:207],v[88:91]\n"
"s_add_u32 s83,s20,32768\n"
"s_and_b32 s83,s83,0x1ffff\n"
"v_add_u32 v113,s83,v112\n"
"v_mfma_f32_16x16x32_bf16 v[92:95],v[156:159],v[204:207],v[84:87]\n"
"s_cmp_lt_u32 s22,64\n"
"s_cselect_b32 s17,s16,-1\n"
"v_mfma_f32_16x16x32_bf16 v[92:95],v[148:151],v[200:203],v[92:95]\n"
"v_mfma_f32_16x16x32_bf16 v[92:95],v[140:143],v[196:199],v[92:95]\n"
"v_mfma_f32_16x16x32_bf16 v[92:95],v[132:135],v[192:195],v[92:95]\n"
"s_waitcnt lgkmcnt(10)\n"
"v_mfma_f32_16x16x32_bf16 v[92:95],v[4:7],v[208:211],v[92:95]\n"
"buffer_load_dwordx4 v117,s[24:27],s8 offen offset:3072 sc1 lds\n"
"v_mfma_f32_16x16x32_bf16 v[92:95],v[12:15],v[212:215],v[92:95]\n"
"s_lshr_b32 s59,s8,2\n"
"ds_read_b32 v125,v114\n"
"s_lshl_b32 s8,s22,17\n"
"v_mfma_f32_16x16x32_bf16 v[92:95],v[164:167],v[216:219],v[92:95]\n"
"s_add_u32 s8,s8,s31\n"
"v_mfma_f32_16x16x32_bf16 v[92:95],v[172:175],v[220:223],v[92:95]\n"
"v_mfma_f32_16x16x32_bf16 v[88:91],v[168:171],v[220:223],v[88:91]\n"
"s_lshl_b64 exec,1,s89\n"
"buffer_atomic_add v100,off,s[24:27],s10 sc0\n"
"s_mov_b64 exec,-1\n"
"v_mfma_f32_16x16x32_bf16 v[88:91],v[160:163],v[216:219],v[88:91]\n"
"s_waitcnt lgkmcnt(0)\n"
"v_cmp_gt_u32 vcc,s17,v125\n"
"v_mfma_f32_16x16x32_bf16 v[88:91],v[8:11],v[212:215],v[88:91]\n"
"v_mfma_f32_16x16x32_bf16 v[88:91],v[0:3],v[208:211],v[88:91]\n"
"s_cbranch_vccz .Lfk_l4\n"
"s_call_b64 s[94:95],.Lfk_meet4\n"
"s_cmp_lt_u32 s22,64\n"
"s_cbranch_scc1 .Lfk_l4\n"
"ds_read_b128 v[192:195],v113 offset:0\n"
"v_mfma_f32_16x16x32_bf16 v[88:91],v[176:179],v[224:227],v[88:91]\n"
"s_getpc_b64 s[96:97]\n"
".Lfk_rt2:\n"
"s_add_u32 s96,s96,.Lfk_tail-.Lfk_rt2\n"
"s_addc_u32 s97,s97,0\n"
"s_mov_b32 s98,.Lfk_cold-.Lfk_tail\n"
"s_mov_b32 s99,0x20000\n"
"s_lshr_b32 s82,s100,3\n"
"s_lshl2_add_u32 s82,s82,s52\n"
"v_lshrrev_b32 v121,2,v112\n"
"s_lshl_b32 s82,s82,8\n"
"s_lshr_b32 s62,s60,4\n"
"v_mfma_f32_16x16x32_bf16 v[88:91],v[184:187],v[228:231],v[88:91]\n"
"buffer_load_dword v120,v121,s[96:99],s82 offen\n"
"ds_read_b128 v[196:199],v113 offset:1024\n"
"s_lshl_b32 s62,s62,15\n"
"v_mfma_f32_16x16x32_bf16 v[88:91],a[48:51],v[232:235],v[88:91]\n"
"s_and_b32 s91,s60,15\n"
"ds_read_b128 v[200:203],v113 offset:2048\n"
"v_lshlrev_b32 v102,2,v112\n"
"v_mfma_f32_16x16x32_bf16 v[88:91],a[56:59],v[236:239],v[88:91]\n"
"s_lshl_b32 s91,s91,6\n"
"ds_read_b128 v[204:207],v113 offset:3072\n"
"v_and_b32 v102,0x0,v102\n"
"v_xor_b32 v102,s91,v102\n"
"v_mfma_f32_16x16x32_bf16 v[92:95],a[60:63],v[236:239],v[92:95]\n"
"s_add_u32 s62,s62,71303168\n"
"ds_read_b128 v[208:211],v113 offset:4096\n"
"s_cmp_eq_u32 s42,0\n"
"v_mfma_f32_16x16x32_bf16 v[92:95],a[52:55],v[232:235],v[92:95]\n"
"s_cselect_b32 s72,0x3ff80,s23\n"
"ds_read_b128 v[212:215],v113 offset:5120\n"
"s_add_u32 s16,s16,4\n"
"v_mfma_f32_16x16x32_bf16 v[92:95],v[188:191],v[228:231],v[92:95]\n"
"s_add_u32 s91,s61,2097152\n"
"ds_read_b128 v[216:219],v113 offset:6144\n"
"v_lshrrev_b32 v126,6,v112\n"
"v_and_or_b32 v103,v112,48,v102\n"
"v_mfma_f32_16x16x32_bf16 v[92:95],v[180:183],v[224:227],v[92:95]\n"
"v_lshl_add_u32 v126,v126,10,v103\n"
"v_add_u32 v117,16384,v126\n"
"v_mfma_f32_16x16x32_bf16 v[92:95],v[52:55],v[240:243],v[92:95]\n"
"ds_read_b128 v[220:223],v113 offset:7168\n"
"ds_read_b128 v[236:239],v113 offset:11264\n"
"v_mfma_f32_16x16x32_bf16 v[92:95],v[60:63],v[244:247],v[92:95]\n"
"ds_read_b128 v[232:235],v113 offset:10240\n"
"v_mfma_f32_16x16x32_bf16 v[92:95],v[20:23],v[248:251],v[92:95]\n"
"ds_read_b128 v[228:231],v113 offset:9216\n"
"v_mfma_f32_16x16x32_bf16 v[92:95],v[28:31],v[252:255],v[92:95]\n"
"ds_read_b128 v[224:227],v113 offset:8192\n"
"v_mfma_f32_16x16x32_bf16 v[88:91],v[24:27],v[252:255],v[88:91]\n"
"ds_read_b128 v[252:255],v113 offset:15360\n"
"v_mfma_f32_16x16x32_bf16 v[88:91],v[16:19],v[248:251],v[88:91]\n"
"ds_read_b128 v[248:251],v113 offset:14336\n"
"v_mfma_f32_16x16x32_bf16 v[88:91],v[56:59],v[244:247],v[88:91]\n"
"ds_read_b128 v[244:247],v113 offset:13312\n"
"v_mfma_f32_16x16x32_bf16 v[88:91],v[48:51],v[240:243],v[88:91]\n"
"ds_read_b128 v[240:243],v113 offset:12288\n"
"s_waitcnt lgkmcnt(12)\n"
"v_mfma_f32_16x16x32_bf16 v[80:83],a[224:227],v[192:195],0\n"
"buffer_load_dwordx4 v[72:75],v112,s[44:47],0 offen offset:0\n"
"v_mfma_f32_16x16x32_bf16 v[80:83],a[232:235],v[196:199],v[80:83]\n"
"buffer_load_dwordx4 v[76:79],v112,s[44:47],0 offen offset:1024\n"
"v_mfma_f32_16x16x32_bf16 v[80:83],a[240:243],v[200:203],v[80:83]\n"
"s_nop 0\n"
"v_cvt_pk_bf16_f32 v88,v88,v89\n"
"v_mfma_f32_16x16x32_bf16 v[80:83],a[248:251],v[204:207],v[80:83]\n"
"v_cvt_pk_bf16_f32 v89,v90,v91\n"
"v_mfma_f32_16x16x32_bf16 v[84:87],a[252:255],v[204:207],0\n"
"ds_read_b128 v[204:207],v113 offset:19456\n"
"s_add_u32 s20,s20,32768\n"
"v_mfma_f32_16x16x32_bf16 v[84:87],a[244:247],v[200:203],v[84:87]\n"
"ds_read_b128 v[200:203],v113 offset:18432\n"
"s_and_b32 s20,s20,0x1ffff\n"
"v_mfma_f32_16x16x32_bf16 v[84:87],a[236:239],v[196:199],v[84:87]\n"
"ds_read_b128 v[196:199],v113 offset:17408\n"
"v_cvt_pk_bf16_f32 v90,v92,v93\n"
"v_mfma_f32_16x16x32_bf16 v[84:87],a[228:231],v[192:195],v[84:87]\n"
"s_mov_b32 s91,s62\n"
"ds_read_b128 v[192:195],v113 offset:16384\n"
"s_waitcnt lgkmcnt(12)\n"
"v_mfma_f32_16x16x32_bf16 v[84:87],a[4:7],v[208:211],v[84:87]\n"
"v_cvt_pk_bf16_f32 v91,v94,v95\n"
"v_max_u32 v88,1,v88\n"
"v_mfma_f32_16x16x32_bf16 v[84:87],a[12:15],v[212:215],v[84:87]\n"
"v_max_u32 v89,1,v89\n"
"v_max_u32 v90,1,v90\n"
"v_max_u32 v91,1,v91\n"
"v_mfma_f32_16x16x32_bf16 v[84:87],a[20:23],v[216:219],v[84:87]\n"
"buffer_load_dwordx4 a[112:115],v126,s[24:27],s91 offen sc1\n"
"v_mfma_f32_16x16x32_bf16 v[84:87],a[28:31],v[220:223],v[84:87]\n"
"buffer_store_dwordx4 v[88:91],v118,s[24:27],s53 offen sc1\n"
"v_mfma_f32_16x16x32_bf16 v[80:83],a[24:27],v[220:223],v[80:83]\n"
"ds_read_b128 v[220:223],v113 offset:23552\n"
"buffer_load_dwordx4 a[116:119],v117,s[24:27],s91 offen sc1\n"
"v_mfma_f32_16x16x32_bf16 v[80:83],a[16:19],v[216:219],v[80:83]\n"
"ds_read_b128 v[216:219],v113 offset:22528\n"
"v_mfma_f32_16x16x32_bf16 v[80:83],a[8:11],v[212:215],v[80:83]\n"
"ds_read_b128 v[212:215],v113 offset:21504\n"
"v_mfma_f32_16x16x32_bf16 v[80:83],a[0:3],v[208:211],v[80:83]\n"
"s_add_u32 s91,s91,4194304\n"
"ds_read_b128 v[208:211],v113 offset:20480\n"
"s_waitcnt lgkmcnt(12)\n"
"v_mfma_f32_16x16x32_bf16 v[80:83],v[32:35],v[224:227],v[80:83]\n"
"v_mfma_f32_16x16x32_bf16 v[80:83],v[40:43],v[228:231],v[80:83]\n"
"v_mfma_f32_16x16x32_bf16 v[80:83],a[32:35],v[232:235],v[80:83]\n"
"buffer_load_dwordx4 a[120:123],v126,s[24:27],s91 offen sc1\n"
"v_mfma_f32_16x16x32_bf16 v[80:83],a[40:43],v[236:239],v[80:83]\n"
"v_mfma_f32_16x16x32_bf16 v[84:87],a[44:47],v[236:239],v[84:87]\n"
"ds_read_b128 v[236:239],v113 offset:27648\n"
"v_mfma_f32_16x16x32_bf16 v[84:87],a[36:39],v[232:235],v[84:87]\n"
"ds_read_b128 v[232:235],v113 offset:26624\n"
"buffer_load_dwordx4 a[124:127],v117,s[24:27],s91 offen sc1\n"
"v_mfma_f32_16x16x32_bf16 v[84:87],v[44:47],v[228:231],v[84:87]\n"
"ds_read_b128 v[228:231],v113 offset:25600\n"
"v_mfma_f32_16x16x32_bf16 v[84:87],v[36:39],v[224:227],v[84:87]\n"
"ds_read_b128 v[224:227],v113 offset:24576\n"
"s_waitcnt lgkmcnt(12)\n"
"v_mfma_f32_16x16x32_bf16 v[84:87],a[68:71],v[240:243],v[84:87]\n"
"s_add_u32 s91,s91,4194304\n"
"v_mfma_f32_16x16x32_bf16 v[84:87],a[76:79],v[244:247],v[84:87]\n"
"v_mfma_f32_16x16x32_bf16 v[84:87],a[84:87],v[248:251],v[84:87]\n"
"v_mfma_f32_16x16x32_bf16 v[84:87],a[92:95],v[252:255],v[84:87]\n"
"buffer_load_dwordx4 a[128:131],v126,s[24:27],s91 offen sc1\n"
"v_mfma_f32_16x16x32_bf16 v[80:83],a[88:91],v[252:255],v[80:83]\n"
"ds_read_b128 v[252:255],v113 offset:31744\n"
"s_mov_b32 s53,s56\n"
"v_mfma_f32_16x16x32_bf16 v[80:83],a[80:83],v[248:251],v[80:83]\n"
"ds_read_b128 v[248:251],v113 offset:30720\n"
"s_mov_b32 s56,s58\n"
"v_mfma_f32_16x16x32_bf16 v[80:83],a[72:75],v[244:247],v[80:83]\n"
"s_mov_b32 s58,s59\n"
"ds_read_b128 v[244:247],v113 offset:29696\n"
"buffer_load_dwordx4 a[132:135],v117,s[24:27],s91 offen sc1\n"
"v_mfma_f32_16x16x32_bf16 v[80:83],a[156:159],v[240:243],v[80:83]\n"
"ds_read_b128 v[240:243],v113 offset:28672\n"
"s_waitcnt lgkmcnt(12)\n"
"v_mfma_f32_16x16x32_bf16 v[88:91],v[128:131],v[192:195],v[80:83]\n"
"s_waitcnt vmcnt(21)\n"
"ds_add_u32 v114,v116\n"
"v_mfma_f32_16x16x32_bf16 v[88:91],v[136:139],v[196:199],v[88:91]\n"
"s_add_u32 s91,s91,4194304\n"
"v_mfma_f32_16x16x32_bf16 v[88:91],v[144:147],v[200:203],v[88:91]\n"
"v_mfma_f32_16x16x32_bf16 v[88:91],v[152:155],v[204:207],v[88:91]\n"
"s_add_u32 s83,s20,32768\n"
"s_and_b32 s83,s83,0x1ffff\n"
"v_add_u32 v113,s83,v112\n"
"v_mfma_f32_16x16x32_bf16 v[92:95],v[156:159],v[204:207],v[84:87]\n"
"buffer_load_dwordx4 a[136:139],v126,s[24:27],s91 offen sc1\n"
"v_mfma_f32_16x16x32_bf16 v[92:95],v[148:151],v[200:203],v[92:95]\n"
"v_mfma_f32_16x16x32_bf16 v[92:95],v[140:143],v[196:199],v[92:95]\n"
"buffer_load_dwordx4 a[140:143],v117,s[24:27],s91 offen sc1\n"
"v_mfma_f32_16x16x32_bf16 v[92:95],v[132:135],v[192:195],v[92:95]\n"
"s_waitcnt lgkmcnt(9)\n"
"v_mfma_f32_16x16x32_bf16 v[92:95],v[4:7],v[208:211],v[92:95]\n"
"v_mfma_f32_16x16x32_bf16 v[92:95],v[12:15],v[212:215],v[92:95]\n"
"ds_read_b32 v125,v114\n"
"v_mfma_f32_16x16x32_bf16 v[92:95],v[164:167],v[216:219],v[92:95]\n"
"v_mfma_f32_16x16x32_bf16 v[92:95],v[172:175],v[220:223],v[92:95]\n"
"v_mfma_f32_16x16x32_bf16 v[88:91],v[168:171],v[220:223],v[88:91]\n"
"v_mfma_f32_16x16x32_bf16 v[88:91],v[160:163],v[216:219],v[88:91]\n"
"s_waitcnt lgkmcnt(0)\n"
"v_cmp_gt_u32 vcc,s16,v125\n"
"v_mfma_f32_16x16x32_bf16 v[88:91],v[8:11],v[212:215],v[88:91]\n"
"v_mfma_f32_16x16x32_bf16 v[88:91],v[0:3],v[208:211],v[88:91]\n"
"s_cbranch_vccnz .Lfk_es_me3\n"
".Lfk_er_me3:\n"
"v_mfma_f32_16x16x32_bf16 v[88:91],v[176:179],v[224:227],v[88:91]\n"
"ds_read_b128 v[192:195],v113 offset:0\n"
"v_lshrrev_b32 v101,4,v112\n"
"v_mfma_f32_16x16x32_bf16 v[88:91],v[184:187],v[228:231],v[88:91]\n"
"ds_read_b128 v[196:199],v113 offset:1024\n"
"v_and_b32 v102,15,v101\n"
"v_mfma_f32_16x16x32_bf16 v[88:91],a[48:51],v[232:235],v[88:91]\n"
"ds_read_b128 v[200:203],v113 offset:2048\n"
"v_lshrrev_b32 v101,4,v101\n"
"v_mfma_f32_16x16x32_bf16 v[88:91],a[56:59],v[236:239],v[88:91]\n"
"ds_read_b128 v[204:207],v113 offset:3072\n"
"v_lshlrev_b32 v101,2,v101\n"
"v_mfma_f32_16x16x32_bf16 v[92:95],a[60:63],v[236:239],v[92:95]\n"
"ds_read_b128 v[208:211],v113 offset:4096\n"
"v_sub_u32 v101,v102,v101\n"
"v_mfma_f32_16x16x32_bf16 v[92:95],a[52:55],v[232:235],v[92:95]\n"
"ds_read_b128 v[212:215],v113 offset:5120\n"
"s_add_u32 s16,s16,4\n"
"v_mfma_f32_16x16x32_bf16 v[92:95],v[188:191],v[228:231],v[92:95]\n"
"ds_read_b128 v[216:219],v113 offset:6144\n"
"v_mov_b32 v64,0\n"
"v_mfma_f32_16x16x32_bf16 v[92:95],v[180:183],v[224:227],v[92:95]\n"
"ds_read_b128 v[220:223],v113 offset:7168\n"
"v_mov_b32 v65,0\n"
"v_mfma_f32_16x16x32_bf16 v[92:95],v[52:55],v[240:243],v[92:95]\n"
"ds_read_b128 v[236:239],v113 offset:11264\n"
"v_mov_b32 v68,0\n"
"v_mfma_f32_16x16x32_bf16 v[92:95],v[60:63],v[244:247],v[92:95]\n"
"ds_read_b128 v[232:235],v113 offset:10240\n"
"v_mov_b32 v69,0\n"
"v_mfma_f32_16x16x32_bf16 v[92:95],v[20:23],v[248:251],v[92:95]\n"
"ds_read_b128 v[228:231],v113 offset:9216\n"
"v_mov_b32 v102,0x3f80\n"
"v_mfma_f32_16x16x32_bf16 v[92:95],v[28:31],v[252:255],v[92:95]\n"
"ds_read_b128 v[224:227],v113 offset:8192\n"
"v_mov_b32 v103,0x3f800000\n"
"v_cmp_eq_u32 vcc,0,v101\n"
"v_mfma_f32_16x16x32_bf16 v[88:91],v[24:27],v[252:255],v[88:91]\n"
"ds_read_b128 v[252:255],v113 offset:15360\n"
"v_cndmask_b32 v66,0,v102,vcc\n"
"v_cmp_eq_u32 vcc,1,v101\n"
"v_mfma_f32_16x16x32_bf16 v[88:91],v[16:19],v[248:251],v[88:91]\n"
"ds_read_b128 v[248:251],v113 offset:14336\n"
"v_cndmask_b32 v66,v66,v103,vcc\n"
"v_cmp_eq_u32 vcc,2,v101\n"
"v_mfma_f32_16x16x32_bf16 v[88:91],v[56:59],v[244:247],v[88:91]\n"
"ds_read_b128 v[244:247],v113 offset:13312\n"
"v_cndmask_b32 v67,0,v102,vcc\n"
"v_mfma_f32_16x16x32_bf16 v[88:91],v[48:51],v[240:243],v[88:91]\n"
"ds_read_b128 v[240:243],v113 offset:12288\n"
"s_waitcnt lgkmcnt(12)\n"
"v_cmp_eq_u32 vcc,3,v101\n"
"v_mfma_f32_16x16x32_bf16 v[80:83],a[224:227],v[192:195],0\n"
"s_nop 0\n"
"v_cndmask_b32 v67,v67,v103,vcc\n"
"v_mfma_f32_16x16x32_bf16 v[80:83],a[232:235],v[196:199],v[80:83]\n"
"v_mfma_f32_16x16x32_bf16 v[80:83],a[240:243],v[200:203],v[80:83]\n"
"v_mfma_f32_16x16x32_bf16 v[80:83],a[248:251],v[204:207],v[80:83]\n"
"v_cvt_pk_bf16_f32 v88,v88,v89\n"
"v_cvt_pk_bf16_f32 v89,v90,v91\n"
"v_mfma_f32_16x16x32_bf16 v[84:87],a[252:255],v[204:207],0\n"
"ds_read_b128 v[204:207],v113 offset:19456\n"
"s_add_u32 s20,s20,32768\n"
"v_mfma_f32_16x16x32_bf16 v[84:87],a[244:247],v[200:203],v[84:87]\n"
"ds_read_b128 v[200:203],v113 offset:18432\n"
"s_and_b32 s20,s20,0x1ffff\n"
"v_mfma_f32_16x16x32_bf16 v[84:87],a[236:239],v[196:199],v[84:87]\n"
"ds_read_b128 v[196:199],v113 offset:17408\n"
"v_cvt_pk_bf16_f32 v90,v92,v93\n"
"v_mfma_f32_16x16x32_bf16 v[84:87],a[228:231],v[192:195],v[84:87]\n"
"ds_read_b128 v[192:195],v113 offset:16384\n"
"s_waitcnt lgkmcnt(12)\n"
"v_mfma_f32_16x16x32_bf16 v[84:87],a[4:7],v[208:211],v[84:87]\n"
"v_cvt_pk_bf16_f32 v91,v94,v95\n"
"v_max_u32 v88,1,v88\n"
"v_mfma_f32_16x16x32_bf16 v[84:87],a[12:15],v[212:215],v[84:87]\n"
"v_max_u32 v89,1,v89\n"
"v_max_u32 v90,1,v90\n"
"v_max_u32 v91,1,v91\n"
"v_mfma_f32_16x16x32_bf16 v[84:87],a[20:23],v[216:219],v[84:87]\n"
"buffer_store_dwordx4 v[88:91],v118,s[24:27],s53 offen sc1\n"
"v_mfma_f32_16x16x32_bf16 v[84:87],a[28:31],v[220:223],v[84:87]\n"
"v_mfma_f32_16x16x32_bf16 v[80:83],a[24:27],v[220:223],v[80:83]\n"
"ds_read_b128 v[220:223],v113 offset:23552\n"
"v_mfma_f32_16x16x32_bf16 v[80:83],a[16:19],v[216:219],v[80:83]\n"
"ds_read_b128 v[216:219],v113 offset:22528\n"
"v_mfma_f32_16x16x32_bf16 v[80:83],a[8:11],v[212:215],v[80:83]\n"
"ds_read_b128 v[212:215],v113 offset:21504\n"
"v_mfma_f32_16x16x32_bf16 v[80:83],a[0:3],v[208:211],v[80:83]\n"
"ds_read_b128 v[208:211],v113 offset:20480\n"
"s_waitcnt lgkmcnt(12)\n"
"v_mfma_f32_16x16x32_bf16 v[80:83],v[32:35],v[224:227],v[80:83]\n"
"v_mfma_f32_16x16x32_bf16 v[80:83],v[40:43],v[228:231],v[80:83]\n"
"v_mfma_f32_16x16x32_bf16 v[80:83],a[32:35],v[232:235],v[80:83]\n"
"v_mfma_f32_16x16x32_bf16 v[80:83],a[40:43],v[236:239],v[80:83]\n"
"v_mfma_f32_16x16x32_bf16 v[84:87],a[44:47],v[236:239],v[84:87]\n"
"ds_read_b128 v[236:239],v113 offset:27648\n"
"v_mfma_f32_16x16x32_bf16 v[84:87],a[36:39],v[232:235],v[84:87]\n"
"ds_read_b128 v[232:235],v113 offset:26624\n"
"v_mfma_f32_16x16x32_bf16 v[84:87],v[44:47],v[228:231],v[84:87]\n"
"ds_read_b128 v[228:231],v113 offset:25600\n"
"v_mfma_f32_16x16x32_bf16 v[84:87],v[36:39],v[224:227],v[84:87]\n"
"ds_read_b128 v[224:227],v113 offset:24576\n"
"s_waitcnt lgkmcnt(12)\n"
"v_mfma_f32_16x16x32_bf16 v[84:87],a[68:71],v[240:243],v[84:87]\n"
"v_mfma_f32_16x16x32_bf16 v[84:87],a[76:79],v[244:247],v[84:87]\n"
"v_mfma_f32_16x16x32_bf16 v[84:87],a[84:87],v[248:251],v[84:87]\n"
"v_mfma_f32_16x16x32_bf16 v[84:87],a[92:95],v[252:255],v[84:87]\n"
"v_mfma_f32_16x16x32_bf16 v[80:83],a[88:91],v[252:255],v[80:83]\n"
"ds_read_b128 v[252:255],v113 offset:31744\n"
"s_mov_b32 s53,s56\n"
"v_mfma_f32_16x16x32_bf16 v[80:83],a[80:83],v[248:251],v[80:83]\n"
"ds_read_b128 v[248:251],v113 offset:30720\n"
"s_mov_b32 s56,s58\n"
"v_mfma_f32_16x16x32_bf16 v[80:83],a[72:75],v[244:247],v[80:83]\n"
"ds_read_b128 v[244:247],v113 offset:29696\n"
"s_mov_b32 s58,s59\n"
"v_mfma_f32_16x16x32_bf16 v[80:83],a[156:159],v[240:243],v[80:83]\n"
"ds_read_b128 v[240:243],v113 offset:28672\n"
"s_waitcnt lgkmcnt(12)\n"
"v_mfma_f32_16x16x32_bf16 v[88:91],v[128:131],v[192:195],v[80:83]\n"
"s_waitcnt vmcnt(14)\n"
"ds_add_u32 v114,v116\n"
"v_mfma_f32_16x16x32_bf16 v[88:91],v[136:139],v[196:199],v[88:91]\n"
"v_mfma_f32_16x16x32_bf16 v[88:91],v[144:147],v[200:203],v[88:91]\n"
"v_mfma_f32_16x16x32_bf16 v[88:91],v[152:155],v[204:207],v[88:91]\n"
"s_add_u32 s83,s20,32768\n"
"s_and_b32 s83,s83,0x1ffff\n"
"v_add_u32 v113,s83,v112\n"
"v_mfma_f32_16x16x32_bf16 v[92:95],v[156:159],v[204:207],v[84:87]\n"
"v_mfma_f32_16x16x32_bf16 v[92:95],v[148:151],v[200:203],v[92:95]\n"
"v_mfma_f32_16x16x32_bf16 v[92:95],v[140:143],v[196:199],v[92:95]\n"
"v_mfma_f32_16x16x32_bf16 v[92:95],v[132:135],v[192:195],v[92:95]\n"
"s_waitcnt lgkmcnt(9)\n"
"v_mfma_f32_16x16x32_bf16 v[92:95],v[4:7],v[208:211],v[92:95]\n"
"v_mfma_f32_16x16x32_bf16 v[92:95],v[12:15],v[212:215],v[92:95]\n"
"ds_read_b32 v125,v114\n"
"v_mfma_f32_16x16x32_bf16 v[92:95],v[164:167],v[216:219],v[92:95]\n"
"v_mfma_f32_16x16x32_bf16 v[92:95],v[172:175],v[220:223],v[92:95]\n"
"v_mfma_f32_16x16x32_bf16 v[88:91],v[168:171],v[220:223],v[88:91]\n"
"v_mfma_f32_16x16x32_bf16 v[88:91],v[160:163],v[216:219],v[88:91]\n"
"s_waitcnt lgkmcnt(0)\n"
"v_cmp_gt_u32 vcc,s16,v125\n"
"v_mfma_f32_16x16x32_bf16 v[88:91],v[8:11],v[212:215],v[88:91]\n"
"v_mfma_f32_16x16x32_bf16 v[88:91],v[0:3],v[208:211],v[88:91]\n"
"s_cbranch_vccnz .Lfk_es_me2\n"
".Lfk_er_me2:\n"
"ds_read_b128 v[192:195],v113 offset:0\n"
"v_mfma_f32_16x16x32_bf16 v[88:91],v[176:179],v[224:227],v[88:91]\n"
"v_lshlrev_b32 v104,6,v112\n"
"v_and_b32 v105,0x8000,v104\n"
"v_mfma_f32_16x16x32_bf16 v[88:91],v[184:187],v[228:231],v[88:91]\n"
"ds_read_b128 v[196:199],v113 offset:1024\n"
"v_and_b32 v104,0x7c00,v104\n"
"v_lshl_add_u32 v104,v105,7,v104\n"
"v_mfma_f32_16x16x32_bf16 v[88:91],a[48:51],v[232:235],v[88:91]\n"
"s_and_b32 s82,s60,15\n"
"ds_read_b128 v[200:203],v113 offset:2048\n"
"s_lshl_b32 s82,s82,6\n"
"v_mfma_f32_16x16x32_bf16 v[88:91],a[56:59],v[236:239],v[88:91]\n"
"ds_read_b128 v[204:207],v113 offset:3072\n"
"v_add_u32 v104,s82,v104\n"
"v_mfma_f32_16x16x32_bf16 v[92:95],a[60:63],v[236:239],v[92:95]\n"
"s_and_b32 s82,s60,1\n"
"ds_read_b128 v[208:211],v113 offset:4096\n"
"s_lshl_b32 s82,s82,23\n"
"v_mfma_f32_16x16x32_bf16 v[92:95],a[52:55],v[232:235],v[92:95]\n"
"s_add_u32 s82,s82,s62\n"
"s_add_u32 s82,s82,2097152\n"
"s_cmp_lt_u32 s52,2\n"
"v_mfma_f32_16x16x32_bf16 v[92:95],v[188:191],v[228:231],v[92:95]\n"
"s_cselect_b32 s82,s82,2146435072\n"
"ds_read_b128 v[212:215],v113 offset:5120\n"
"ds_read_b128 v[216:219],v113 offset:6144\n"
"v_mfma_f32_16x16x32_bf16 v[92:95],v[180:183],v[224:227],v[92:95]\n"
"ds_read_b128 v[220:223],v113 offset:7168\n"
"v_mfma_f32_16x16x32_bf16 v[92:95],v[52:55],v[240:243],v[92:95]\n"
"ds_read_b128 v[236:239],v113 offset:11264\n"
"v_mfma_f32_16x16x32_bf16 v[92:95],v[60:63],v[244:247],v[92:95]\n"
"ds_read_b128 v[232:235],v113 offset:10240\n"
"v_mfma_f32_16x16x32_bf16 v[92:95],v[20:23],v[248:251],v[92:95]\n"
"ds_read_b128 v[228:231],v113 offset:9216\n"
"v_mfma_f32_16x16x32_bf16 v[92:95],v[28:31],v[252:255],v[92:95]\n"
"ds_read_b128 v[224:227],v113 offset:8192\n"
"v_mfma_f32_16x16x32_bf16 v[88:91],v[24:27],v[252:255],v[88:91]\n"
"ds_read_b128 v[252:255],v113 offset:15360\n"
"v_mfma_f32_16x16x32_bf16 v[88:91],v[16:19],v[248:251],v[88:91]\n"
"ds_read_b128 v[248:251],v113 offset:14336\n"
"v_mfma_f32_16x16x32_bf16 v[88:91],v[56:59],v[244:247],v[88:91]\n"
"ds_read_b128 v[244:247],v113 offset:13312\n"
"v_mfma_f32_16x16x32_bf16 v[88:91],v[48:51],v[240:243],v[88:91]\n"
"ds_read_b128 v[240:243],v113 offset:12288\n"
"s_waitcnt lgkmcnt(12)\n"
"v_mfma_f32_16x16x32_bf16 v[80:83],a[224:227],v[192:195],0\n"
"buffer_load_dword a148,v104,s[24:27],s82 offen sc1\n"
"v_mfma_f32_16x16x32_bf16 v[80:83],a[232:235],v[196:199],v[80:83]\n"
"v_mfma_f32_16x16x32_bf16 v[80:83],a[240:243],v[200:203],v[80:83]\n"
"v_mfma_f32_16x16x32_bf16 v[80:83],a[248:251],v[204:207],v[80:83]\n"
"s_nop 0\n"
"v_cvt_pk_bf16_f32 v88,v88,v89\n"
"v_cvt_pk_bf16_f32 v89,v90,v91\n"
"v_mfma_f32_16x16x32_bf16 v[84:87],a[252:255],v[204:207],0\n"
"ds_read_b128 v[204:207],v113 offset:19456\n"
"v_cvt_pk_bf16_f32 v90,v92,v93\n"
"v_mfma_f32_16x16x32_bf16 v[84:87],a[244:247],v[200:203],v[84:87]\n"
"ds_read_b128 v[200:203],v113 offset:18432\n"
"v_cvt_pk_bf16_f32 v91,v94,v95\n"
"v_mfma_f32_16x16x32_bf16 v[84:87],a[236:239],v[196:199],v[84:87]\n"
"ds_read_b128 v[196:199],v113 offset:17408\n"
"v_max_u32 v88,1,v88\n"
"v_mfma_f32_16x16x32_bf16 v[84:87],a[228:231],v[192:195],v[84:87]\n"
"ds_read_b128 v[192:195],v113 offset:16384\n"
"s_waitcnt lgkmcnt(12)\n"
"v_mfma_f32_16x16x32_bf16 v[84:87],a[4:7],v[208:211],v[84:87]\n"
"v_max_u32 v89,1,v89\n"
"v_max_u32 v90,1,v90\n"
"v_max_u32 v91,1,v91\n"
"v_mfma_f32_16x16x32_bf16 v[84:87],a[12:15],v[212:215],v[84:87]\n"
"buffer_store_dwordx4 v[88:91],v118,s[24:27],s53 offen sc1\n"
"v_mfma_f32_16x16x32_bf16 v[84:87],a[20:23],v[216:219],v[84:87]\n"
"v_mfma_f32_16x16x32_bf16 v[84:87],a[28:31],v[220:223],v[84:87]\n"
"v_mfma_f32_16x16x32_bf16 v[80:83],a[24:27],v[220:223],v[80:83]\n"
"ds_read_b128 v[220:223],v113 offset:23552\n"
"v_mfma_f32_16x16x32_bf16 v[80:83],a[16:19],v[216:219],v[80:83]\n"
"ds_read_b128 v[216:219],v113 offset:22528\n"
"v_mfma_f32_16x16x32_bf16 v[80:83],a[8:11],v[212:215],v[80:83]\n"
"ds_read_b128 v[212:215],v113 offset:21504\n"
"v_mfma_f32_16x16x32_bf16 v[80:83],a[0:3],v[208:211],v[80:83]\n"
"ds_read_b128 v[208:211],v113 offset:20480\n"
"s_waitcnt lgkmcnt(12)\n"
"v_mfma_f32_16x16x32_bf16 v[80:83],v[32:35],v[224:227],v[80:83]\n"
"v_mfma_f32_16x16x32_bf16 v[80:83],v[40:43],v[228:231],v[80:83]\n"
"v_mfma_f32_16x16x32_bf16 v[80:83],a[32:35],v[232:235],v[80:83]\n"
"v_mfma_f32_16x16x32_bf16 v[80:83],a[40:43],v[236:239],v[80:83]\n"
"v_mfma_f32_16x16x32_bf16 v[84:87],a[44:47],v[236:239],v[84:87]\n"
"ds_read_b128 v[236:239],v113 offset:27648\n"
"v_mfma_f32_16x16x32_bf16 v[84:87],a[36:39],v[232:235],v[84:87]\n"
"ds_read_b128 v[232:235],v113 offset:26624\n"
"v_mfma_f32_16x16x32_bf16 v[84:87],v[44:47],v[228:231],v[84:87]\n"
"ds_read_b128 v[228:231],v113 offset:25600\n"
"v_mfma_f32_16x16x32_bf16 v[84:87],v[36:39],v[224:227],v[84:87]\n"
"ds_read_b128 v[224:227],v113 offset:24576\n"
"s_waitcnt lgkmcnt(12)\n"
"v_mfma_f32_16x16x32_bf16 v[84:87],a[68:71],v[240:243],v[84:87]\n"
"v_mfma_f32_16x16x32_bf16 v[84:87],a[76:79],v[244:247],v[84:87]\n"
"v_mfma_f32_16x16x32_bf16 v[84:87],a[84:87],v[248:251],v[84:87]\n"
"v_mfma_f32_16x16x32_bf16 v[84:87],a[92:95],v[252:255],v[84:87]\n"
"v_mfma_f32_16x16x32_bf16 v[80:83],a[88:91],v[252:255],v[80:83]\n"
"ds_read_b128 v[252:255],v113 offset:31744\n"
"s_mov_b32 s53,s56\n"
"v_mfma_f32_16x16x32_bf16 v[80:83],a[80:83],v[248:251],v[80:83]\n"
"ds_read_b128 v[248:251],v113 offset:30720\n"
"s_mov_b32 s56,s58\n"
"v_mfma_f32_16x16x32_bf16 v[80:83],a[72:75],v[244:247],v[80:83]\n"
"ds_read_b128 v[244:247],v113 offset:29696\n"
"s_mov_b32 s58,s59\n"
"v_mfma_f32_16x16x32_bf16 v[80:83],a[156:159],v[240:243],v[80:83]\n"
"ds_read_b128 v[240:243],v113 offset:28672\n"
"s_waitcnt lgkmcnt(12)\n"
"v_mfma_f32_16x16x32_bf16 v[88:91],v[128:131],v[192:195],v[80:83]\n"
"v_mfma_f32_16x16x32_bf16 v[88:91],v[136:139],v[196:199],v[88:91]\n"
"v_mfma_f32_16x16x32_bf16 v[88:91],v[144:147],v[200:203],v[88:91]\n"
"v_mfma_f32_16x16x32_bf16 v[88:91],v[152:155],v[204:207],v[88:91]\n"
"v_mfma_f32_16x16x32_bf16 v[92:95],v[156:159],v[204:207],v[84:87]\n"
"v_mfma_f32_16x16x32_bf16 v[92:95],v[148:151],v[200:203],v[92:95]\n"
"v_mfma_f32_16x16x32_bf16 v[92:95],v[140:143],v[196:199],v[92:95]\n"
"v_mfma_f32_16x16x32_bf16 v[92:95],v[132:135],v[192:195],v[92:95]\n"
"s_waitcnt lgkmcnt(8)\n"
"v_mfma_f32_16x16x32_bf16 v[92:95],v[4:7],v[208:211],v[92:95]\n"
"v_mfma_f32_16x16x32_bf16 v[92:95],v[12:15],v[212:215],v[92:95]\n"
"v_mfma_f32_16x16x32_bf16 v[92:95],v[164:167],v[216:219],v[92:95]\n"
"v_mfma_f32_16x16x32_bf16 v[92:95],v[172:175],v[220:223],v[92:95]\n"
"v_mfma_f32_16x16x32_bf16 v[88:91],v[168:171],v[220:223],v[88:91]\n"
"s_mov_b32 s91,s62\n"
"v_mfma_f32_16x16x32_bf16 v[88:91],v[160:163],v[216:219],v[88:91]\n"
"v_mfma_f32_16x16x32_bf16 v[88:91],v[8:11],v[212:215],v[88:91]\n"
"v_mfma_f32_16x16x32_bf16 v[88:91],v[0:3],v[208:211],v[88:91]\n"
"s_waitcnt lgkmcnt(0)\n"
"s_waitcnt vmcnt(3)\n"
"v_mfma_f32_16x16x32_bf16 v[88:91],v[176:179],v[224:227],v[88:91]\n"
"v_accvgpr_read_b32 v128,a112\n"
"v_accvgpr_read_b32 v129,a113\n"
"v_mfma_f32_16x16x32_bf16 v[88:91],v[184:187],v[228:231],v[88:91]\n"
"v_accvgpr_read_b32 v130,a114\n"
"v_accvgpr_read_b32 v131,a115\n"
"v_mfma_f32_16x16x32_bf16 v[88:91],a[48:51],v[232:235],v[88:91]\n"
"v_accvgpr_read_b32 v132,a116\n"
"v_accvgpr_read_b32 v133,a117\n"
"v_mfma_f32_16x16x32_bf16 v[88:91],a[56:59],v[236:239],v[88:91]\n"
"v_accvgpr_read_b32 v134,a118\n"
"v_accvgpr_read_b32 v135,a119\n"
"v_mfma_f32_16x16x32_bf16 v[92:95],a[60:63],v[236:239],v[92:95]\n"
"v_accvgpr_read_b32 v136,a120\n"
"v_accvgpr_read_b32 v137,a121\n"
"v_mfma_f32_16x16x32_bf16 v[92:95],a[52:55],v[232:235],v[92:95]\n"
"v_accvgpr_read_b32 v138,a122\n"
"v_accvgpr_read_b32 v139,a123\n"
"v_mfma_f32_16x16x32_bf16 v[92:95],v[188:191],v[228:231],v[92:95]\n"
"v_accvgpr_read_b32 v140,a124\n"
"v_accvgpr_read_b32 v141,a125\n"
"v_mfma_f32_16x16x32_bf16 v[92:95],v[180:183],v[224:227],v[92:95]\n"
"v_accvgpr_read_b32 v142,a126\n"
"v_accvgpr_read_b32 v143,a127\n"
"v_mfma_f32_16x16x32_bf16 v[92:95],v[52:55],v[240:243],v[92:95]\n"
"v_accvgpr_read_b32 v144,a128\n"
"v_accvgpr_read_b32 v145,a129\n"
"v_mfma_f32_16x16x32_bf16 v[92:95],v[60:63],v[244:247],v[92:95]\n"
"v_accvgpr_read_b32 v146,a130\n"
"v_accvgpr_read_b32 v147,a131\n"
"v_mfma_f32_16x16x32_bf16 v[92:95],v[20:23],v[248:251],v[92:95]\n"
"v_accvgpr_read_b32 v148,a132\n"
"v_accvgpr_read_b32 v149,a133\n"
"v_mfma_f32_16x16x32_bf16 v[92:95],v[28:31],v[252:255],v[92:95]\n"
"v_accvgpr_read_b32 v150,a134\n"
"v_accvgpr_read_b32 v151,a135\n"
"v_mfma_f32_16x16x32_bf16 v[88:91],v[24:27],v[252:255],v[88:91]\n"
"v_accvgpr_read_b32 v152,a136\n"
"v_accvgpr_read_b32 v153,a137\n"
"v_mfma_f32_16x16x32_bf16 v[88:91],v[16:19],v[248:251],v[88:91]\n"
"v_accvgpr_read_b32 v154,a138\n"
"v_accvgpr_read_b32 v155,a139\n"
"v_mfma_f32_16x16x32_bf16 v[88:91],v[56:59],v[244:247],v[88:91]\n"
"v_accvgpr_read_b32 v156,a140\n"
"v_accvgpr_read_b32 v157,a141\n"
"v_mfma_f32_16x16x32_bf16 v[88:91],v[48:51],v[240:243],v[88:91]\n"
"v_accvgpr_read_b32 v158,a142\n"
"v_accvgpr_read_b32 v159,a143\n"
".Lfk_tail:\n"
"v_lshlrev_b32 v224,16,v72\n"
"v_and_b32 v225,0xffff0000,v72\n"
"v_lshlrev_b32 v226,16,v73\n"
"v_and_b32 v227,0xffff0000,v73\n"
"v_lshlrev_b32 v228,16,v74\n"
"s_nop 0\n"
"v_cvt_pk_bf16_f32 v88,v88,v89\n"
"v_cvt_pk_bf16_f32 v89,v90,v91\n"
"v_cvt_pk_bf16_f32 v90,v92,v93\n"
"v_cvt_pk_bf16_f32 v91,v94,v95\n"
"v_max_u32 v88,1,v88\n"
"v_max_u32 v89,1,v89\n"
"v_max_u32 v90,1,v90\n"
"v_max_u32 v91,1,v91\n"
"v_mfma_f32_16x16x32_bf16 v[0:3],v[66:69],a[96:99],0\n"
"buffer_store_dwordx4 v[88:91],v118,s[24:27],s53 offen sc1\n"
"v_mfma_f32_16x16x32_bf16 v[4:7],v[64:67],a[96:99],0\n"
"s_mov_b32 s101,s22\n"
"v_and_b32 v231,0xffff0000,v75\n"
"v_lshlrev_b32 v232,16,v76\n"
"v_mfma_f32_16x16x32_bf16 v[8:11],v[66:69],a[100:103],0\n"
"v_and_b32 v229,0xffff0000,v74\n"
"v_lshlrev_b32 v230,16,v75\n"
"v_mfma_f32_16x16x32_bf16 v[12:15],v[64:67],a[100:103],0\n"
"s_mov_b32 s67,0x3a800000\n"
"v_and_b32 v233,0xffff0000,v76\n"
"v_lshlrev_b32 v234,16,v77\n"
"v_and_b32 v235,0xffff0000,v77\n"
"v_lshlrev_b32 v236,16,v78\n"
"v_and_b32 v237,0xffff0000,v78\n"
"v_lshlrev_b32 v238,16,v79\n"
"v_and_b32 v239,0xffff0000,v79\n"
"v_pk_add_f32 v[70:71],v[224:225],v[226:227]\n"
"v_pk_add_f32 v[70:71],v[70:71],v[228:229]\n"
"v_pk_add_f32 v[70:71],v[70:71],v[230:231]\n"
"v_pk_add_f32 v[70:71],v[70:71],v[232:233]\n"
"v_pk_add_f32 v[70:71],v[70:71],v[234:235]\n"
"v_pk_add_f32 v[70:71],v[70:71],v[236:237]\n"
"v_pk_add_f32 v[70:71],v[70:71],v[238:239]\n"
"v_add_f32 v70,v70,v71\n"
"s_nop 1\n"
"v_add_f32_dpp v70,v70,v70 quad_perm:[1,0,3,2] row_mask:0xf bank_mask:0xf\n"
"s_nop 1\n"
"v_add_f32_dpp v70,v70,v70 quad_perm:[2,3,0,1] row_mask:0xf bank_mask:0xf\n"
"s_nop 1\n"
"v_add_f32_dpp v70,v70,v70 row_half_mirror row_mask:0xf bank_mask:0xf\n"
"s_nop 1\n"
"v_add_f32_dpp v70,v70,v70 row_mirror row_mask:0xf bank_mask:0xf\n"
"v_mov_b32 v71,v70\n"
"s_nop 1\n"
"v_permlane16_swap_b32 v71,v70\n"
"v_add_f32 v70,v70,v71\n"
"v_mov_b32 v71,v70\n"
"s_nop 1\n"
"v_permlane32_swap_b32 v71,v70\n"
"v_add_f32 v70,v70,v71\n"
"v_min3_u32 v36,v128,v129,v130\n"
"v_min3_u32 v37,v131,v132,v133\n"
"v_min3_u32 v36,v36,v134,v135\n"
"v_min3_u32 v37,v37,v136,v137\n"
"v_min3_u32 v36,v36,v138,v139\n"
"v_min3_u32 v37,v37,v140,v141\n"
"v_min3_u32 v36,v36,v142,v143\n"
"v_min3_u32 v37,v37,v144,v145\n"
"v_min3_u32 v36,v36,v146,v147\n"
"v_min3_u32 v37,v37,v148,v149\n"
"v_min3_u32 v36,v36,v150,v151\n"
"v_min3_u32 v37,v37,v152,v153\n"
"v_min3_u32 v36,v36,v154,v155\n"
"v_min3_u32 v37,v37,v156,v157\n"
"v_min3_u32 v36,v36,v158,v159\n"
"v_min_u32 v36,v36,v37\n"
"v_cmp_eq_u32 vcc,0,v36\n"
"s_nop 0\n"
"s_cbranch_vccz .Lfk_t0\n"
"s_mov_b32 s84,0x40000\n"
"s_mov_b32 s91,s62\n"
"buffer_load_dwordx4 v[128:131],v126,s[24:27],s91 offen sc1\n"
"buffer_load_dwordx4 v[132:135],v117,s[24:27],s91 offen sc1\n"
"s_add_u32 s91,s91,4194304\n"
"buffer_load_dwordx4 v[136:139],v126,s[24:27],s91 offen sc1\n"
"buffer_load_dwordx4 v[140:143],v117,s[24:27],s91 offen sc1\n"
"s_add_u32 s91,s91,4194304\n"
"buffer_load_dwordx4 v[144:147],v126,s[24:27],s91 offen sc1\n"
"buffer_load_dwordx4 v[148:151],v117,s[24:27],s91 offen sc1\n"
"s_add_u32 s91,s91,4194304\n"
"buffer_load_dwordx4 v[152:155],v126,s[24:27],s91 offen sc1\n"
"buffer_load_dwordx4 v[156:159],v117,s[24:27],s91 offen sc1\n"
"s_waitcnt vmcnt(0)\n"
".Lfk_wa:\n"
"v_min3_u32 v36,v128,v129,v130\n"
"v_min3_u32 v37,v131,v132,v133\n"
"v_min3_u32 v36,v36,v134,v135\n"
"v_min3_u32 v37,v37,v136,v137\n"
"v_min3_u32 v36,v36,v138,v139\n"
"v_min3_u32 v37,v37,v140,v141\n"
"v_min3_u32 v36,v36,v142,v143\n"
"v_min3_u32 v37,v37,v144,v145\n"
"v_min3_u32 v36,v36,v146,v147\n"
"v_min3_u32 v37,v37,v148,v149\n"
"v_min3_u32 v36,v36,v150,v151\n"
"v_min3_u32 v37,v37,v152,v153\n"
"v_min3_u32 v36,v36,v154,v155\n"
"v_min3_u32 v37,v37,v156,v157\n"
"v_min3_u32 v36,v36,v158,v159\n"
"v_min_u32 v36,v36,v37\n"
"v_cmp_eq_u32 vcc,0,v36\n"
"s_nop 0\n"
"s_cbranch_vccz .Lfk_wa_x\n"
"s_sleep 2\n"
"s_mov_b64 exec,vcc\n"
"s_mov_b32 s91,s62\n"
"buffer_load_dwordx4 v[128:131],v126,s[24:27],s91 offen sc1\n"
"buffer_load_dwordx4 v[132:135],v117,s[24:27],s91 offen sc1\n"
"s_add_u32 s91,s91,4194304\n"
"buffer_load_dwordx4 v[136:139],v126,s[24:27],s91 offen sc1\n"
"buffer_load_dwordx4 v[140:143],v117,s[24:27],s91 offen sc1\n"
"s_add_u32 s91,s91,4194304\n"
"buffer_load_dwordx4 v[144:147],v126,s[24:27],s91 offen sc1\n"
"buffer_load_dwordx4 v[148:151],v117,s[24:27],s91 offen sc1\n"
"s_add_u32 s91,s91,4194304\n"
"buffer_load_dwordx4 v[152:155],v126,s[24:27],s91 offen sc1\n"
"buffer_load_dwordx4 v[156:159],v117,s[24:27],s91 offen sc1\n"
"s_mov_b64 exec,-1\n"
"s_sub_u32 s84,s84,1\n"
"s_waitcnt vmcnt(0)\n"
"s_cbranch_scc0 .Lfk_wa\n"
".Lfk_wa_x:\n"
".Lfk_t0:\n"
"s_getpc_b64 s[94:95]\n"
".Lfk_rm0:\n"
"s_add_u32 s94,s94,.Lfk_rm1-.Lfk_rm0\n"
"v_mfma_f32_16x16x32_bf16 v[0:3],v[66:69],v[128:131],v[0:3]\n"
"s_addc_u32 s95,s95,0\n"
"v_mfma_f32_16x16x32_bf16 v[4:7],v[64:67],v[128:131],v[4:7]\n"
"v_mfma_f32_16x16x32_bf16 v[8:11],v[66:69],v[132:135],v[8:11]\n"
"v_mfma_f32_16x16x32_bf16 v[12:15],v[64:67],v[132:135],v[12:15]\n"
"v_mfma_f32_16x16x32_bf16 v[0:3],v[66:69],v[136:139],v[0:3]\n"
"v_mfma_f32_16x16x32_bf16 v[4:7],v[64:67],v[136:139],v[4:7]\n"
"v_mfma_f32_16x16x32_bf16 v[8:11],v[66:69],v[140:143],v[8:11]\n"
"v_mfma_f32_16x16x32_bf16 v[12:15],v[64:67],v[140:143],v[12:15]\n"
"v_mfma_f32_16x16x32_bf16 v[0:3],v[66:69],v[144:147],v[0:3]\n"
"v_mfma_f32_16x16x32_bf16 v[4:7],v[64:67],v[144:147],v[4:7]\n"
"v_mfma_f32_16x16x32_bf16 v[8:11],v[66:69],v[148:151],v[8:11]\n"
"v_mfma_f32_16x16x32_bf16 v[12:15],v[64:67],v[148:151],v[12:15]\n"
"v_mfma_f32_16x16x32_bf16 v[0:3],v[66:69],v[152:155],v[0:3]\n"
"v_mfma_f32_16x16x32_bf16 v[4:7],v[64:67],v[152:155],v[4:7]\n"
"v_mfma_f32_16x16x32_bf16 v[8:11],v[66:69],v[156:159],v[8:11]\n"
"v_mfma_f32_16x16x32_bf16 v[12:15],v[64:67],v[156:159],v[12:15]\n"
"s_nop 7\n"
".Lfk_rm:\n"
"v_readfirstlane_b32 s64,v0\n"
"s_nop 1\n"
"v_mov_b32 v38,s64\n"
"v_pk_add_f32 v[16:17],v[0:1],v[38:39] op_sel_hi:[1,0] neg_lo:[0,1] neg_hi:[0,1]\n"
"v_pk_add_f32 v[18:19],v[2:3],v[38:39] op_sel_hi:[1,0] neg_lo:[0,1] neg_hi:[0,1]\n"
"v_pk_add_f32 v[20:21],v[4:5],v[38:39] op_sel_hi:[1,0] neg_lo:[0,1] neg_hi:[0,1]\n"
"v_pk_add_f32 v[22:23],v[6:7],v[38:39] op_sel_hi:[1,0] neg_lo:[0,1] neg_hi:[0,1]\n"
"v_pk_add_f32 v[24:25],v[8:9],v[38:39] op_sel_hi:[1,0] neg_lo:[0,1] neg_hi:[0,1]\n"
"v_pk_add_f32 v[26:27],v[10:11],v[38:39] op_sel_hi:[1,0] neg_lo:[0,1] neg_hi:[0,1]\n"
"v_pk_add_f32 v[28:29],v[12:13],v[38:39] op_sel_hi:[1,0] neg_lo:[0,1] neg_hi:[0,1]\n"
"v_pk_add_f32 v[30:31],v[14:15],v[38:39] op_sel_hi:[1,0] neg_lo:[0,1] neg_hi:[0,1]\n"
"v_pk_add_f32 v[32:33],v[16:17],v[18:19]\n"
"v_pk_mul_f32 v[34:35],v[16:17],v[16:17]\n"
"v_pk_mul_f32 v[36:37],v[16:17],v[224:225]\n"
"v_pk_fma_f32 v[34:35],v[18:19],v[18:19],v[34:35]\n"
"v_pk_fma_f32 v[36:37],v[18:19],v[226:227],v[36:37]\n"
"v_pk_add_f32 v[32:33],v[32:33],v[20:21]\n"
"v_pk_fma_f32 v[34:35],v[20:21],v[20:21],v[34:35]\n"
"v_pk_fma_f32 v[36:37],v[20:21],v[228:229],v[36:37]\n"
"v_pk_add_f32 v[32:33],v[32:33],v[22:23]\n"
"v_pk_fma_f32 v[34:35],v[22:23],v[22:23],v[34:35]\n"
"v_pk_fma_f32 v[36:37],v[22:23],v[230:231],v[36:37]\n"
"v_pk_add_f32 v[32:33],v[32:33],v[24:25]\n"
"v_pk_fma_f32 v[34:35],v[24:25],v[24:25],v[34:35]\n"
"v_pk_fma_f32 v[36:37],v[24:25],v[232:233],v[36:37]\n"
"v_pk_add_f32 v[32:33],v[32:33],v[26:27]\n"
"v_pk_fma_f32 v[34:35],v[26:27],v[26:27],v[34:35]\n"
"v_pk_fma_f32 v[36:37],v[26:27],v[234:235],v[36:37]\n"
"v_pk_add_f32 v[32:33],v[32:33],v[28:29]\n"
"v_pk_fma_f32 v[34:35],v[28:29],v[28:29],v[34:35]\n"
"v_pk_fma_f32 v[36:37],v[28:29],v[236:237],v[36:37]\n"
"v_pk_add_f32 v[32:33],v[32:33],v[30:31]\n"
"v_pk_fma_f32 v[34:35],v[30:31],v[30:31],v[34:35]\n"
"v_pk_fma_f32 v[36:37],v[30:31],v[238:239],v[36:37]\n"
"v_add_f32 v32,v32,v33\n"
"v_add_f32 v33,v34,v35\n"
"v_add_f32 v34,v36,v37\n"
"v_add_f32_dpp v32,v32,v32 quad_perm:[1,0,3,2] row_mask:0xf bank_mask:0xf\n"
"v_add_f32_dpp v33,v33,v33 quad_perm:[1,0,3,2] row_mask:0xf bank_mask:0xf\n"
"v_add_f32_dpp v34,v34,v34 quad_perm:[1,0,3,2] row_mask:0xf bank_mask:0xf\n"
"v_add_f32_dpp v32,v32,v32 quad_perm:[2,3,0,1] row_mask:0xf bank_mask:0xf\n"
"v_add_f32_dpp v33,v33,v33 quad_perm:[2,3,0,1] row_mask:0xf bank_mask:0xf\n"
"v_add_f32_dpp v34,v34,v34 quad_perm:[2,3,0,1] row_mask:0xf bank_mask:0xf\n"
"v_add_f32_dpp v32,v32,v32 row_half_mirror row_mask:0xf bank_mask:0xf\n"
"v_add_f32_dpp v33,v33,v33 row_half_mirror row_mask:0xf bank_mask:0xf\n"
"v_add_f32_dpp v34,v34,v34 row_half_mirror row_mask:0xf bank_mask:0xf\n"
"v_add_f32_dpp v32,v32,v32 row_mirror row_mask:0xf bank_mask:0xf\n"
"v_add_f32_dpp v33,v33,v33 row_mirror row_mask:0xf bank_mask:0xf\n"
"v_add_f32_dpp v34,v34,v34 row_mirror row_mask:0xf bank_mask:0xf\n"
"v_add_f32_dpp v32,v32,v32 row_bcast:15 row_mask:0xa bank_mask:0xf\n"
"v_add_f32_dpp v33,v33,v33 row_bcast:15 row_mask:0xa bank_mask:0xf\n"
"v_add_f32_dpp v34,v34,v34 row_bcast:15 row_mask:0xa bank_mask:0xf\n"
"v_add_f32_dpp v32,v32,v32 row_bcast:31 row_mask:0xc bank_mask:0xf\n"
"v_add_f32_dpp v33,v33,v33 row_bcast:31 row_mask:0xc bank_mask:0xf\n"
"v_add_f32_dpp v34,v34,v34 row_bcast:31 row_mask:0xc bank_mask:0xf\n"
"v_mul_f32 v32,s67,v32\n"
"v_add_f32 v35,v32,v38\n"
"v_mul_f32 v32,v32,v32\n"
"v_cvt_pk_bf16_f32 v35,v35,v35\n"
"v_lshlrev_b32 v35,16,v35\n"
"v_fma_f32 v33,v33,s67,-v32\n"
"v_sub_f32 v36,v38,v35\n"
"v_max_f32 v33,0,v33\n"
"v_fmac_f32 v34,v36,v70\n"
"v_cvt_pk_bf16_f32 v33,v33,v33\n"
"v_lshlrev_b32 v33,16,v33\n"
"v_add_f32 v33,0x3727c5ac,v33\n"
"v_cvt_pk_bf16_f32 v33,v33,v33\n"
"v_lshlrev_b32 v33,16,v33\n"
"v_sqrt_f32 v33,v33\n"
"s_nop 0\n"
"v_cvt_pk_bf16_f32 v33,v33,v33\n"
"v_lshlrev_b32 v33,16,v33\n"
"v_rcp_f32 v36,v33\n"
"v_lshrrev_b32 v33,16,v33\n"
"v_mul_f32 v37,v34,v36\n"
"v_or_b32 v33,v33,v35\n"
"v_cvt_pk_bf16_f32 v37,v37,v37\n"
"v_ashrrev_i32 v36,31,v37\n"
"v_or_b32 v36,0x80000000,v36\n"
"v_xor_b32 v37,v37,v36\n"
"v_and_b32 v37,0xffff0000,v37\n"
"v_readlane_b32 s64,v33,63\n"
"v_readlane_b32 s63,v37,63\n"
"s_nop 1\n"
"s_setpc_b64 s[94:95]\n"
".Lfk_rm1:\n"
"s_add_u32 s91,s62,2097152\n"
"buffer_load_dwordx4 v[160:163],v126,s[24:27],s91 offen sc1\n"
"buffer_load_dwordx4 v[164:167],v117,s[24:27],s91 offen sc1\n"
"s_add_u32 s91,s91,4194304\n"
"buffer_load_dwordx4 v[168:171],v126,s[24:27],s91 offen sc1\n"
"buffer_load_dwordx4 v[172:175],v117,s[24:27],s91 offen sc1\n"
"s_add_u32 s91,s91,4194304\n"
"buffer_load_dwordx4 v[176:179],v126,s[24:27],s91 offen sc1\n"
"buffer_load_dwordx4 v[180:183],v117,s[24:27],s91 offen sc1\n"
"s_add_u32 s91,s91,4194304\n"
"buffer_load_dwordx4 v[184:187],v126,s[24:27],s91 offen sc1\n"
"buffer_load_dwordx4 v[188:191],v117,s[24:27],s91 offen sc1\n"
"s_sub_u32 s65,0xffff,s60\n"
"s_or_b32 s65,s65,s63\n"
"s_mov_b32 s66,s64\n"
"v_lshlrev_b32 v34,16,s64\n"
"v_mov_b32 v35,s64\n"
"v_rcp_f32 v34,v34\n"
"v_and_b32 v35,0xffff0000,v35\n"
"v_cvt_pk_bf16_f32 v240,v0,v1\n"
"v_cvt_pk_bf16_f32 v241,v2,v3\n"
"v_cvt_pk_bf16_f32 v242,v4,v5\n"
"v_cvt_pk_bf16_f32 v243,v6,v7\n"
"v_cvt_pk_bf16_f32 v244,v8,v9\n"
"v_cvt_pk_bf16_f32 v245,v10,v11\n"
"v_cvt_pk_bf16_f32 v246,v12,v13\n"
"v_cvt_pk_bf16_f32 v247,v14,v15\n"
"v_lshlrev_b32 v0,16,v240\n"
"v_and_b32 v1,0xffff0000,v240\n"
"v_lshlrev_b32 v2,16,v241\n"
"v_and_b32 v3,0xffff0000,v241\n"
"v_lshlrev_b32 v4,16,v242\n"
"v_and_b32 v5,0xffff0000,v242\n"
"v_lshlrev_b32 v6,16,v243\n"
"v_and_b32 v7,0xffff0000,v243\n"
"v_lshlrev_b32 v8,16,v244\n"
"v_and_b32 v9,0xffff0000,v244\n"
"v_lshlrev_b32 v10,16,v245\n"
"v_and_b32 v11,0xffff0000,v245\n"
"v_lshlrev_b32 v12,16,v246\n"
"v_and_b32 v13,0xffff0000,v246\n"
"v_lshlrev_b32 v14,16,v247\n"
"v_and_b32 v15,0xffff0000,v247\n"
"v_pk_add_f32 v[16:17],v[0:1],v[34:35] op_sel:[0,1] neg_lo:[0,1] neg_hi:[0,1]\n"
"v_pk_add_f32 v[18:19],v[2:3],v[34:35] op_sel:[0,1] neg_lo:[0,1] neg_hi:[0,1]\n"
"v_pk_add_f32 v[20:21],v[4:5],v[34:35] op_sel:[0,1] neg_lo:[0,1] neg_hi:[0,1]\n"
"v_pk_add_f32 v[22:23],v[6:7],v[34:35] op_sel:[0,1] neg_lo:[0,1] neg_hi:[0,1]\n"
"v_pk_add_f32 v[24:25],v[8:9],v[34:35] op_sel:[0,1] neg_lo:[0,1] neg_hi:[0,1]\n"
"v_pk_add_f32 v[26:27],v[10:11],v[34:35] op_sel:[0,1] neg_lo:[0,1] neg_hi:[0,1]\n"
"v_pk_add_f32 v[28:29],v[12:13],v[34:35] op_sel:[0,1] neg_lo:[0,1] neg_hi:[0,1]\n"
"v_pk_add_f32 v[30:31],v[14:15],v[34:35] op_sel:[0,1] neg_lo:[0,1] neg_hi:[0,1]\n"
"v_cvt_pk_bf16_f32 v240,v16,v17\n"
"v_cvt_pk_bf16_f32 v241,v18,v19\n"
"v_cvt_pk_bf16_f32 v242,v20,v21\n"
"v_cvt_pk_bf16_f32 v243,v22,v23\n"
"v_cvt_pk_bf16_f32 v244,v24,v25\n"
"v_cvt_pk_bf16_f32 v245,v26,v27\n"
"v_cvt_pk_bf16_f32 v246,v28,v29\n"
"v_cvt_pk_bf16_f32 v247,v30,v31\n"
"v_lshlrev_b32 v16,16,v240\n"
"v_and_b32 v17,0xffff0000,v240\n"
"v_lshlrev_b32 v18,16,v241\n"
"v_and_b32 v19,0xffff0000,v241\n"
"v_lshlrev_b32 v20,16,v242\n"
"v_and_b32 v21,0xffff0000,v242\n"
"v_lshlrev_b32 v22,16,v243\n"
"v_and_b32 v23,0xffff0000,v243\n"
"v_lshlrev_b32 v24,16,v244\n"
"v_and_b32 v25,0xffff0000,v244\n"
"v_lshlrev_b32 v26,16,v245\n"
"v_and_b32 v27,0xffff0000,v245\n"
"v_lshlrev_b32 v28,16,v246\n"
"v_and_b32 v29,0xffff0000,v246\n"
"v_lshlrev_b32 v30,16,v247\n"
"v_and_b32 v31,0xffff0000,v247\n"
"v_pk_mul_f32 v[16:17],v[16:17],v[34:35] op_sel_hi:[1,0]\n"
"v_pk_mul_f32 v[18:19],v[18:19],v[34:35] op_sel_hi:[1,0]\n"
"v_pk_mul_f32 v[20:21],v[20:21],v[34:35] op_sel_hi:[1,0]\n"
"v_pk_mul_f32 v[22:23],v[22:23],v[34:35] op_sel_hi:[1,0]\n"
"v_pk_mul_f32 v[24:25],v[24:25],v[34:35] op_sel_hi:[1,0]\n"
"v_pk_mul_f32 v[26:27],v[26:27],v[34:35] op_sel_hi:[1,0]\n"
"v_pk_mul_f32 v[28:29],v[28:29],v[34:35] op_sel_hi:[1,0]\n"
"v_pk_mul_f32 v[30:31],v[30:31],v[34:35] op_sel_hi:[1,0]\n"
"v_cvt_pk_bf16_f32 v240,v16,v17\n"
"v_cvt_pk_bf16_f32 v241,v18,v19\n"
"v_cvt_pk_bf16_f32 v242,v20,v21\n"
"v_cvt_pk_bf16_f32 v243,v22,v23\n"
"v_cvt_pk_bf16_f32 v244,v24,v25\n"
"v_cvt_pk_bf16_f32 v245,v26,v27\n"
"v_cvt_pk_bf16_f32 v246,v28,v29\n"
"v_cvt_pk_bf16_f32 v247,v30,v31\n"
"v_max_u32 v216,1,v240\n"
"v_max_u32 v217,1,v241\n"
"v_max_u32 v218,1,v242\n"
"v_max_u32 v219,1,v243\n"
"v_max_u32 v220,1,v244\n"
"v_max_u32 v221,1,v245\n"
"v_max_u32 v222,1,v246\n"
"v_max_u32 v223,1,v247\n"
"v_lshlrev_b32 v16,16,v240\n"
"v_and_b32 v17,0xffff0000,v240\n"
"v_lshlrev_b32 v18,16,v241\n"
"v_and_b32 v19,0xffff0000,v241\n"
"v_lshlrev_b32 v20,16,v242\n"
"v_and_b32 v21,0xffff0000,v242\n"
"v_lshlrev_b32 v22,16,v243\n"
"v_and_b32 v23,0xffff0000,v243\n"
"v_lshlrev_b32 v24,16,v244\n"
"v_and_b32 v25,0xffff0000,v244\n"
"v_lshlrev_b32 v26,16,v245\n"
"v_and_b32 v27,0xffff0000,v245\n"
"v_lshlrev_b32 v28,16,v246\n"
"v_and_b32 v29,0xffff0000,v246\n"
"v_lshlrev_b32 v30,16,v247\n"
"v_and_b32 v31,0xffff0000,v247\n"
"v_pk_add_f32 v[16:17],v[16:17],v[0:1]\n"
"v_pk_add_f32 v[18:19],v[18:19],v[2:3]\n"
"v_pk_add_f32 v[20:21],v[20:21],v[4:5]\n"
"v_pk_add_f32 v[22:23],v[22:23],v[6:7]\n"
"v_pk_add_f32 v[24:25],v[24:25],v[8:9]\n"
"v_pk_add_f32 v[26:27],v[26:27],v[10:11]\n"
"v_pk_add_f32 v[28:29],v[28:29],v[12:13]\n"
"v_pk_add_f32 v[30:31],v[30:31],v[14:15]\n"
"v_cvt_pk_bf16_f32 v240,v16,v17\n"
"v_cvt_pk_bf16_f32 v241,v18,v19\n"
"v_cvt_pk_bf16_f32 v242,v20,v21\n"
"v_cvt_pk_bf16_f32 v243,v22,v23\n"
"v_cvt_pk_bf16_f32 v244,v24,v25\n"
"v_cvt_pk_bf16_f32 v245,v26,v27\n"
"v_cvt_pk_bf16_f32 v246,v28,v29\n"
"v_cvt_pk_bf16_f32 v247,v30,v31\n"
"buffer_store_dwordx4 v[240:243],v112,s[48:51],s61 offen offset:0 sc1\n"
"buffer_store_dwordx4 v[244:247],v112,s[48:51],s61 offen offset:1024 sc1\n"
"s_barrier\n"
"s_lshl_b32 s82,s52,13\n"
"s_add_u32 s82,s82,0\n"
"v_bfe_u32 v41,v112,5,2\n"
"v_lshlrev_b32 v41,13,v41\n"
"v_bfe_u32 v42,v112,4,1\n"
"v_lshl_add_u32 v41,v42,3,v41\n"
"s_lshl_b32 s83,s100,4\n"
"v_mfma_f32_16x16x32_bf16 v[200:203],v[66:69],a[104:107],0\n"
"s_add_u32 s83,s83,149946368\n"
"v_mov_b32 v45,s66\n"
"v_add_u32 v41,0,v41\n"
"v_mfma_f32_16x16x32_bf16 v[204:207],v[64:67],a[104:107],0\n"
"v_mov_b32 v40,s82\n"
"v_mov_b32 v51,s80\n"
"v_mfma_f32_16x16x32_bf16 v[208:211],v[66:69],a[108:111],0\n"
"v_mov_b32 v44,s65\n"
"v_lshlrev_b32 v43,8,v112\n"
"v_mfma_f32_16x16x32_bf16 v[212:215],v[64:67],a[108:111],0\n"
"s_mov_b32 s84,0x40000\n"
"v_mov_b32 v49,s80\n"
"s_waitcnt vmcnt(2)\n"
".Lfk_w1:\n"
"v_mfma_f32_16x16x32_bf16 v[0:3],v[66:69],v[160:163],v[200:203]\n"
"v_min3_u32 v36,v160,v161,v162\n"
"v_min3_u32 v37,v163,v164,v165\n"
"v_mfma_f32_16x16x32_bf16 v[4:7],v[64:67],v[160:163],v[204:207]\n"
"v_min3_u32 v36,v36,v166,v167\n"
"v_min3_u32 v37,v37,v168,v169\n"
"v_mfma_f32_16x16x32_bf16 v[8:11],v[66:69],v[164:167],v[208:211]\n"
"v_min3_u32 v36,v36,v170,v171\n"
"v_min3_u32 v37,v37,v172,v173\n"
"v_mfma_f32_16x16x32_bf16 v[12:15],v[64:67],v[164:167],v[212:215]\n"
"v_min3_u32 v36,v36,v174,v175\n"
"v_min3_u32 v37,v37,v176,v177\n"
"v_mfma_f32_16x16x32_bf16 v[0:3],v[66:69],v[168:171],v[0:3]\n"
"v_min3_u32 v36,v36,v178,v179\n"
"v_min3_u32 v37,v37,v180,v181\n"
"v_mfma_f32_16x16x32_bf16 v[4:7],v[64:67],v[168:171],v[4:7]\n"
"v_min3_u32 v36,v36,v182,v183\n"
"v_min3_u32 v37,v37,v184,v185\n"
"v_mfma_f32_16x16x32_bf16 v[8:11],v[66:69],v[172:175],v[8:11]\n"
"v_min3_u32 v36,v36,v186,v187\n"
"v_min3_u32 v37,v37,v188,v189\n"
"v_mfma_f32_16x16x32_bf16 v[12:15],v[64:67],v[172:175],v[12:15]\n"
"v_min3_u32 v36,v36,v190,v191\n"
"v_min_u32 v36,v36,v37\n"
"v_cmp_eq_u32 vcc,0,v36\n"
"v_mfma_f32_16x16x32_bf16 v[0:3],v[66:69],v[176:179],v[0:3]\n"
"s_cbranch_vccz .Lfk_w1_x\n"
"s_mov_b64 exec,vcc\n"
"s_add_u32 s91,s62,2097152\n"
"buffer_load_dwordx4 v[160:163],v126,s[24:27],s91 offen sc1\n"
"buffer_load_dwordx4 v[164:167],v117,s[24:27],s91 offen sc1\n"
"s_add_u32 s91,s91,4194304\n"
"buffer_load_dwordx4 v[168:171],v126,s[24:27],s91 offen sc1\n"
"buffer_load_dwordx4 v[172:175],v117,s[24:27],s91 offen sc1\n"
"s_add_u32 s91,s91,4194304\n"
"buffer_load_dwordx4 v[176:179],v126,s[24:27],s91 offen sc1\n"
"buffer_load_dwordx4 v[180:183],v117,s[24:27],s91 offen sc1\n"
"s_add_u32 s91,s91,4194304\n"
"buffer_load_dwordx4 v[184:187],v126,s[24:27],s91 offen sc1\n"
"buffer_load_dwordx4 v[188:191],v117,s[24:27],s91 offen sc1\n"
"s_mov_b64 exec,-1\n"
"s_sub_u32 s84,s84,1\n"
"s_waitcnt vmcnt(0)\n"
"s_cbranch_scc0 .Lfk_w1\n"
".Lfk_w1_x:\n"
"v_mfma_f32_16x16x32_bf16 v[4:7],v[64:67],v[176:179],v[4:7]\n"
"v_mfma_f32_16x16x32_bf16 v[8:11],v[66:69],v[180:183],v[8:11]\n"
"v_mfma_f32_16x16x32_bf16 v[12:15],v[64:67],v[180:183],v[12:15]\n"
"v_mfma_f32_16x16x32_bf16 v[0:3],v[66:69],v[184:187],v[0:3]\n"
"v_mfma_f32_16x16x32_bf16 v[4:7],v[64:67],v[184:187],v[4:7]\n"
"v_mfma_f32_16x16x32_bf16 v[8:11],v[66:69],v[188:191],v[8:11]\n"
"v_mfma_f32_16x16x32_bf16 v[12:15],v[64:67],v[188:191],v[12:15]\n"
"s_nop 6\n"
"s_call_b64 s[94:95],.Lfk_rm\n"
"s_sub_u32 s82,64511,s60\n"
"s_or_b32 s63,s63,s82\n"
"v_mov_b32 v46,s63\n"
"v_mov_b32 v47,s64\n"
"v_mov_b32 v39,0\n"
"ds_write_b128 v40,v[44:47]\n"
"s_waitcnt vmcnt(0) lgkmcnt(0)\n"
"s_barrier\n"
"ds_read_b64 v[52:53],v41\n"
"s_waitcnt lgkmcnt(0)\n"
"v_mov_b32 v48,v52\n"
"s_nop 1\n"
"v_min_u32_dpp v54,v48,v48 quad_perm:[1,0,3,2] row_mask:0xf bank_mask:0xf\n"
"v_max_u32_dpp v48,v48,v48 quad_perm:[1,0,3,2] row_mask:0xf bank_mask:0xf\n"
"v_max_u32_dpp v39,v39,v39 quad_perm:[1,0,3,2] row_mask:0xf bank_mask:0xf\n"
"v_max_u32 v39,v39,v54\n"
"v_min_u32_dpp v54,v48,v48 quad_perm:[2,3,0,1] row_mask:0xf bank_mask:0xf\n"
"v_max_u32_dpp v48,v48,v48 quad_perm:[2,3,0,1] row_mask:0xf bank_mask:0xf\n"
"v_max_u32_dpp v39,v39,v39 quad_perm:[2,3,0,1] row_mask:0xf bank_mask:0xf\n"
"v_max_u32 v39,v39,v54\n"
"v_min_u32_dpp v54,v48,v48 row_half_mirror row_mask:0xf bank_mask:0xf\n"
"v_max_u32_dpp v48,v48,v48 row_half_mirror row_mask:0xf bank_mask:0xf\n"
"v_max_u32_dpp v39,v39,v39 row_half_mirror row_mask:0xf bank_mask:0xf\n"
"v_max_u32 v39,v39,v54\n"
"v_xor_b32 v50,0x800,v39\n"
"s_cmp_lg_u32 s52,0\n"
"s_cbranch_scc1 .Lfk_t1\n"
"s_mov_b64 exec,0xff\n"
"buffer_store_dwordx4 v[48:51],v43,s[24:27],s83 offen sc1\n"
"s_mov_b64 exec,-1\n"
"s_add_u32 s83,s83,0x40000\n"
"v_cmp_eq_u32 vcc,v48,v52\n"
"s_nop 0\n"
"s_ff1_i32_b64 s82,vcc\n"
"v_readlane_b32 s90,v53,s82\n"
"s_nop 1\n"
"v_mov_b32 v44,s90\n"
"v_mov_b32 v45,s80\n"
"v_cmp_eq_u32 vcc,v39,v52\n"
"s_nop 0\n"
"s_ff1_i32_b64 s82,vcc\n"
"v_readlane_b32 s90,v53,s82\n"
"s_nop 1\n"
"v_mov_b32 v46,s90\n"
"v_mov_b32 v47,s80\n"
"s_mov_b64 exec,0x100\n"
"buffer_store_dwordx4 v[44:47],v43,s[24:27],s83 offen sc1\n"
"s_mov_b64 exec,-1\n"
".Lfk_t1:\n"
"v_readfirstlane_b32 s89,v39\n"
"s_add_u32 m0,s30,131072\n"
"s_nop 0\n"
"buffer_load_dwordx4 v96,s[40:43],0 offen offset:0 nt lds\n"
"buffer_load_dwordx4 v96,s[40:43],0 offen offset:1024 nt lds\n"
"buffer_load_dwordx4 v97,s[40:43],0 offen offset:2048 nt lds\n"
"buffer_load_dwordx4 v97,s[40:43],0 offen offset:3072 nt lds\n"
"v_lshlrev_b32 v34,16,s64\n"
"v_mov_b32 v35,s64\n"
"v_rcp_f32 v34,v34\n"
"v_and_b32 v35,0xffff0000,v35\n"
"v_cvt_pk_bf16_f32 v248,v0,v1\n"
"v_cvt_pk_bf16_f32 v249,v2,v3\n"
"v_cvt_pk_bf16_f32 v250,v4,v5\n"
"v_cvt_pk_bf16_f32 v251,v6,v7\n"
"v_cvt_pk_bf16_f32 v252,v8,v9\n"
"v_cvt_pk_bf16_f32 v253,v10,v11\n"
"v_cvt_pk_bf16_f32 v254,v12,v13\n"
"v_cvt_pk_bf16_f32 v255,v14,v15\n"
"v_lshlrev_b32 v0,16,v248\n"
"v_and_b32 v1,0xffff0000,v248\n"
"v_lshlrev_b32 v2,16,v249\n"
"v_and_b32 v3,0xffff0000,v249\n"
"v_lshlrev_b32 v4,16,v250\n"
"v_and_b32 v5,0xffff0000,v250\n"
"v_lshlrev_b32 v6,16,v251\n"
"v_and_b32 v7,0xffff0000,v251\n"
"v_lshlrev_b32 v8,16,v252\n"
"v_and_b32 v9,0xffff0000,v252\n"
"v_lshlrev_b32 v10,16,v253\n"
"v_and_b32 v11,0xffff0000,v253\n"
"v_lshlrev_b32 v12,16,v254\n"
"v_and_b32 v13,0xffff0000,v254\n"
"v_lshlrev_b32 v14,16,v255\n"
"v_and_b32 v15,0xffff0000,v255\n"
"v_pk_add_f32 v[16:17],v[0:1],v[34:35] op_sel:[0,1] neg_lo:[0,1] neg_hi:[0,1]\n"
"v_pk_add_f32 v[18:19],v[2:3],v[34:35] op_sel:[0,1] neg_lo:[0,1] neg_hi:[0,1]\n"
"v_pk_add_f32 v[20:21],v[4:5],v[34:35] op_sel:[0,1] neg_lo:[0,1] neg_hi:[0,1]\n"
"v_pk_add_f32 v[22:23],v[6:7],v[34:35] op_sel:[0,1] neg_lo:[0,1] neg_hi:[0,1]\n"
"v_pk_add_f32 v[24:25],v[8:9],v[34:35] op_sel:[0,1] neg_lo:[0,1] neg_hi:[0,1]\n"
"v_pk_add_f32 v[26:27],v[10:11],v[34:35] op_sel:[0,1] neg_lo:[0,1] neg_hi:[0,1]\n"
"v_pk_add_f32 v[28:29],v[12:13],v[34:35] op_sel:[0,1] neg_lo:[0,1] neg_hi:[0,1]\n"
"v_pk_add_f32 v[30:31],v[14:15],v[34:35] op_sel:[0,1] neg_lo:[0,1] neg_hi:[0,1]\n"
"v_cvt_pk_bf16_f32 v248,v16,v17\n"
"v_cvt_pk_bf16_f32 v249,v18,v19\n"
"v_cvt_pk_bf16_f32 v250,v20,v21\n"
"v_cvt_pk_bf16_f32 v251,v22,v23\n"
"v_cvt_pk_bf16_f32 v252,v24,v25\n"
"v_cvt_pk_bf16_f32 v253,v26,v27\n"
"v_cvt_pk_bf16_f32 v254,v28,v29\n"
"v_cvt_pk_bf16_f32 v255,v30,v31\n"
"v_lshlrev_b32 v16,16,v248\n"
"v_and_b32 v17,0xffff0000,v248\n"
"v_lshlrev_b32 v18,16,v249\n"
"v_and_b32 v19,0xffff0000,v249\n"
"v_lshlrev_b32 v20,16,v250\n"
"v_and_b32 v21,0xffff0000,v250\n"
"v_lshlrev_b32 v22,16,v251\n"
"v_and_b32 v23,0xffff0000,v251\n"
"v_lshlrev_b32 v24,16,v252\n"
"v_and_b32 v25,0xffff0000,v252\n"
"v_lshlrev_b32 v26,16,v253\n"
"v_and_b32 v27,0xffff0000,v253\n"
"v_lshlrev_b32 v28,16,v254\n"
"v_and_b32 v29,0xffff0000,v254\n"
"v_lshlrev_b32 v30,16,v255\n"
"v_and_b32 v31,0xffff0000,v255\n"
"v_pk_mul_f32 v[16:17],v[16:17],v[34:35] op_sel_hi:[1,0]\n"
"v_pk_mul_f32 v[18:19],v[18:19],v[34:35] op_sel_hi:[1,0]\n"
"v_pk_mul_f32 v[20:21],v[20:21],v[34:35] op_sel_hi:[1,0]\n"
"v_pk_mul_f32 v[22:23],v[22:23],v[34:35] op_sel_hi:[1,0]\n"
"v_pk_mul_f32 v[24:25],v[24:25],v[34:35] op_sel_hi:[1,0]\n"
"v_pk_mul_f32 v[26:27],v[26:27],v[34:35] op_sel_hi:[1,0]\n"
"v_pk_mul_f32 v[28:29],v[28:29],v[34:35] op_sel_hi:[1,0]\n"
"v_pk_mul_f32 v[30:31],v[30:31],v[34:35] op_sel_hi:[1,0]\n"
"v_cvt_pk_bf16_f32 v248,v16,v17\n"
"v_cvt_pk_bf16_f32 v249,v18,v19\n"
"v_cvt_pk_bf16_f32 v250,v20,v21\n"
"v_cvt_pk_bf16_f32 v251,v22,v23\n"
"v_cvt_pk_bf16_f32 v252,v24,v25\n"
"v_cvt_pk_bf16_f32 v253,v26,v27\n"
"v_cvt_pk_bf16_f32 v254,v28,v29\n"
"v_cvt_pk_bf16_f32 v255,v30,v31\n"
"v_max_u32 v208,1,v248\n"
"v_max_u32 v209,1,v249\n"
"v_max_u32 v210,1,v250\n"
"v_max_u32 v211,1,v251\n"
"v_max_u32 v212,1,v252\n"
"v_max_u32 v213,1,v253\n"
"v_max_u32 v214,1,v254\n"
"v_max_u32 v215,1,v255\n"
"s_and_b32 s82,s60,255\n"
"s_lshl_b32 s82,s82,12\n"
"s_cmp_eq_u32 s63,s89\n"
"s_cselect_b32 s83,2048,0\n"
"s_add_u32 s82,s82,s83\n"
"s_add_u32 s82,s82,174063616\n"
"s_cmp_ge_u32 s63,s89\n"
"s_cselect_b32 s82,s82,2146435072\n"
"buffer_store_dwordx4 v[208:211],v112,s[24:27],s82 offen offset:0 sc1\n"
"buffer_store_dwordx4 v[212:215],v112,s[24:27],s82 offen offset:1024 sc1\n"
"s_and_b32 s82,s60,255\n"
"s_lshl_b32 s82,s82,12\n"
"s_cmp_eq_u32 s65,s89\n"
"s_cselect_b32 s83,2048,0\n"
"s_add_u32 s82,s82,s83\n"
"s_add_u32 s82,s82,174063616\n"
"s_cmp_ge_u32 s65,s89\n"
"s_cselect_b32 s82,s82,2146435072\n"
"buffer_store_dwordx4 v[216:219],v112,s[24:27],s82 offen offset:0 sc1\n"
"buffer_store_dwordx4 v[220:223],v112,s[24:27],s82 offen offset:1024 sc1\n"
"v_lshlrev_b32 v16,16,v248\n"
"v_and_b32 v17,0xffff0000,v248\n"
"v_lshlrev_b32 v18,16,v249\n"
"v_and_b32 v19,0xffff0000,v249\n"
"v_lshlrev_b32 v20,16,v250\n"
"v_and_b32 v21,0xffff0000,v250\n"
"v_lshlrev_b32 v22,16,v251\n"
"v_and_b32 v23,0xffff0000,v251\n"
"v_lshlrev_b32 v24,16,v252\n"
"v_and_b32 v25,0xffff0000,v252\n"
"v_lshlrev_b32 v26,16,v253\n"
"v_and_b32 v27,0xffff0000,v253\n"
"v_lshlrev_b32 v28,16,v254\n"
"v_and_b32 v29,0xffff0000,v254\n"
"v_lshlrev_b32 v30,16,v255\n"
"v_and_b32 v31,0xffff0000,v255\n"
"v_pk_add_f32 v[16:17],v[16:17],v[0:1]\n"
"v_pk_add_f32 v[18:19],v[18:19],v[2:3]\n"
"v_pk_add_f32 v[20:21],v[20:21],v[4:5]\n"
"v_pk_add_f32 v[22:23],v[22:23],v[6:7]\n"
"v_pk_add_f32 v[24:25],v[24:25],v[8:9]\n"
"v_pk_add_f32 v[26:27],v[26:27],v[10:11]\n"
"v_pk_add_f32 v[28:29],v[28:29],v[12:13]\n"
"v_pk_add_f32 v[30:31],v[30:31],v[14:15]\n"
"v_cvt_pk_bf16_f32 v248,v16,v17\n"
"v_cvt_pk_bf16_f32 v249,v18,v19\n"
"v_cvt_pk_bf16_f32 v250,v20,v21\n"
"v_cvt_pk_bf16_f32 v251,v22,v23\n"
"v_cvt_pk_bf16_f32 v252,v24,v25\n"
"v_cvt_pk_bf16_f32 v253,v26,v27\n"
"v_cvt_pk_bf16_f32 v254,v28,v29\n"
"v_cvt_pk_bf16_f32 v255,v30,v31\n"
"s_add_u32 s68,s61,2097152\n"
"s_cmp_lt_u32 s63,s89\n"
"s_cselect_b32 s83,s68,2146435072\n"
"s_cselect_b32 s68,2146435072,s68\n"
"buffer_store_dwordx4 v[248:251],v112,s[48:51],s83 offen offset:0 sc1\n"
"buffer_store_dwordx4 v[252:255],v112,s[48:51],s83 offen offset:1024 sc1\n"
"v_lshrrev_b32 v40,6,v112\n"
"v_and_b32 v41,7,v40\n"
"v_lshlrev_b32 v41,5,v41\n"
"v_bfe_u32 v42,v112,4,2\n"
"v_lshlrev_b32 v42,3,v42\n"
"v_lshl_add_u32 v40,v40,11,v42\n"
"s_lshl_b32 s82,s52,9\n"
"v_add_u32 v40,s82,v40\n"
"v_add_u32 v40,131072,v40\n"
"v_xor_b32 v42,0,v41\n"
"v_add_u32 v192,v40,v42\n"
"v_xor_b32 v42,32,v41\n"
"v_add_u32 v193,v40,v42\n"
"v_xor_b32 v42,64,v41\n"
"v_add_u32 v194,v40,v42\n"
"v_xor_b32 v42,96,v41\n"
"v_add_u32 v195,v40,v42\n"
"v_xor_b32 v42,128,v41\n"
"v_add_u32 v196,v40,v42\n"
"v_xor_b32 v42,160,v41\n"
"v_add_u32 v197,v40,v42\n"
"v_xor_b32 v42,192,v41\n"
"v_add_u32 v198,v40,v42\n"
"v_xor_b32 v42,224,v41\n"
"v_add_u32 v199,v40,v42\n"
"v_bfe_u32 v56,v112,4,1\n"
"v_lshrrev_b32 v57,1,v112\n"
"v_and_b32 v57,0x1f0,v57\n"
"s_lshl_b32 s83,s52,13\n"
"s_add_u32 s83,s83,1024\n"
"v_lshl_add_u32 v58,v56,9,s83\n"
"v_lshrrev_b32 v59,8,v112\n"
"v_lshl_add_u32 v59,v59,4,s82\n"
"v_add_u32 v59,174063616,v59\n"
"v_add_u32 v58,v58,v57\n"
"v_add_u32 v57,s82,v57\n"
"v_and_b32 v55,0x7c0,v57\n"
"v_and_b32 v54,0x30,v57\n"
"v_lshl_add_u32 v55,v55,4,v54\n"
"v_lshrrev_b32 v54,2,v55\n"
"v_and_b32 v54,0x0,v54\n"
"v_or_b32 v55,v55,v54\n"
"v_add_u32 v60,s83,v112\n"
"v_add_u32 v61,1024,v112\n"
"v_bfe_u32 v62,v112,5,3\n"
"v_cmp_eq_u32 s[8:9],0,v62\n"
"v_cmp_eq_u32 s[10:11],1,v62\n"
"v_cmp_eq_u32 s[12:13],2,v62\n"
"v_cmp_eq_u32 s[14:15],3,v62\n"
"v_cmp_eq_u32 s[16:17],4,v62\n"
"v_cmp_eq_u32 s[18:19],5,v62\n"
"v_cmp_eq_u32 s[20:21],6,v62\n"
"v_cmp_eq_u32 s[22:23],7,v62\n"
"v_lshlrev_b32 v62,6,v62\n"
"v_lshl_add_u32 v62,v56,12,v62\n"
"v_lshrrev_b32 v63,8,v112\n"
"v_lshl_add_u32 v62,v63,4,v62\n"
"s_lshl_b32 s69,s72,13\n"
"s_lshl_b32 s82,s52,10\n"
"s_add_u32 s69,s69,s82\n"
"s_add_u32 s69,s69,171966464\n"
"v_lshrrev_b32 v63,5,v112\n"
"v_min_u32 v63,27,v63\n"
"v_lshlrev_b32 v63,13,v63\n"
"v_lshl_add_u32 v63,v56,4,v63\n"
"s_mul_i32 s70,s52,458752\n"
"s_lshl_b32 s82,s73,5\n"
"s_add_u32 s70,s70,s82\n"
"s_add_u32 s70,s70,171966464\n"
"s_add_u32 s71,s70,229376\n"
"s_and_b32 s83,s100,7\n"
"s_lshl_b32 s83,s83,12\n"
"s_add_u32 s83,s83,149946368\n"
"v_add_u32 v20,0,v112\n"
"v_lshlrev_b32 v20,0,v20\n"
"v_add_u32 v21,1024,v112\n"
"v_lshlrev_b32 v21,0,v21\n"
"v_add_u32 v22,2048,v112\n"
"v_lshlrev_b32 v22,0,v22\n"
"v_add_u32 v23,3072,v112\n"
"v_lshlrev_b32 v23,0,v23\n"
"s_sleep 20\n"
".Lfk_nn:\n"
"s_mov_b32 s84,0x40000\n"
"buffer_load_dwordx4 v[0:3],v20,s[24:27],s83 offen sc1\n"
"buffer_load_dwordx4 v[4:7],v21,s[24:27],s83 offen sc1\n"
"buffer_load_dwordx4 v[8:11],v22,s[24:27],s83 offen sc1\n"
"buffer_load_dwordx4 v[12:15],v23,s[24:27],s83 offen sc1\n"
"s_waitcnt vmcnt(0)\n"
".Lfk_wc:\n"
"v_xor_b32 v24,s80,v1\n"
"v_xor_b32 v25,s80,v3\n"
"v_xor_b32 v26,s80,v5\n"
"v_xor_b32 v27,s80,v7\n"
"v_xor_b32 v28,s80,v9\n"
"v_xor_b32 v29,s80,v11\n"
"v_xor_b32 v30,s80,v13\n"
"v_xor_b32 v31,s80,v15\n"
"v_or3_b32 v16,v24,v25,v26\n"
"v_or3_b32 v17,v27,v28,v29\n"
"v_or3_b32 v16,v16,v30,v31\n"
"v_or_b32 v16,v16,v17\n"
"v_cmp_ne_u32 vcc,0,v16\n"
"s_nop 0\n"
"s_cbranch_vccz .Lfk_wc_x\n"
"s_sleep 2\n"
"s_mov_b64 exec,vcc\n"
"buffer_load_dwordx4 v[0:3],v20,s[24:27],s83 offen sc1\n"
"buffer_load_dwordx4 v[4:7],v21,s[24:27],s83 offen sc1\n"
"buffer_load_dwordx4 v[8:11],v22,s[24:27],s83 offen sc1\n"
"buffer_load_dwordx4 v[12:15],v23,s[24:27],s83 offen sc1\n"
"s_mov_b64 exec,-1\n"
"s_sub_u32 s84,s84,1\n"
"s_waitcnt vmcnt(0)\n"
"s_cbranch_scc0 .Lfk_wc\n"
".Lfk_wc_x:\n"
"v_min_u32 v16,v0,v4\n"
"v_max_u32 v0,v0,v4\n"
"v_max3_u32 v2,v2,v6,v16\n"
"v_min_u32 v16,v8,v12\n"
"v_max_u32 v8,v8,v12\n"
"v_max3_u32 v10,v10,v14,v16\n"
"v_min_u32 v16,v0,v8\n"
"v_max_u32 v0,v0,v8\n"
"v_max3_u32 v2,v2,v10,v16\n"
"v_cmp_eq_u32 vcc,0,v56\n"
"v_min_u32_dpp v16,v0,v0 quad_perm:[1,0,3,2] row_mask:0xf bank_mask:0xf\n"
"v_max_u32_dpp v0,v0,v0 quad_perm:[1,0,3,2] row_mask:0xf bank_mask:0xf\n"
"v_max_u32_dpp v2,v2,v2 quad_perm:[1,0,3,2] row_mask:0xf bank_mask:0xf\n"
"v_max_u32 v2,v2,v16\n"
"v_min_u32_dpp v16,v0,v0 quad_perm:[2,3,0,1] row_mask:0xf bank_mask:0xf\n"
"v_max_u32_dpp v0,v0,v0 quad_perm:[2,3,0,1] row_mask:0xf bank_mask:0xf\n"
"v_max_u32_dpp v2,v2,v2 quad_perm:[2,3,0,1] row_mask:0xf bank_mask:0xf\n"
"v_max_u32 v2,v2,v16\n"
"v_min_u32_dpp v16,v0,v0 row_half_mirror row_mask:0xf bank_mask:0xf\n"
"v_max_u32_dpp v0,v0,v0 row_half_mirror row_mask:0xf bank_mask:0xf\n"
"v_max_u32_dpp v2,v2,v2 row_half_mirror row_mask:0xf bank_mask:0xf\n"
"v_max_u32 v2,v2,v16\n"
"v_min_u32_dpp v16,v0,v0 row_mirror row_mask:0xf bank_mask:0xf\n"
"v_max_u32_dpp v0,v0,v0 row_mirror row_mask:0xf bank_mask:0xf\n"
"v_max_u32_dpp v2,v2,v2 row_mirror row_mask:0xf bank_mask:0xf\n"
"v_max_u32 v2,v2,v16\n"
"v_min_u32_dpp v16,v0,v0 row_bcast:15 row_mask:0xa bank_mask:0xf\n"
"v_max_u32_dpp v0,v0,v0 row_bcast:15 row_mask:0xa bank_mask:0xf\n"
"v_max_u32_dpp v2,v2,v2 row_bcast:15 row_mask:0xa bank_mask:0xf\n"
"v_max_u32 v2,v2,v16\n"
"v_min_u32_dpp v16,v0,v0 row_bcast:31 row_mask:0xc bank_mask:0xf\n"
"v_max_u32_dpp v0,v0,v0 row_bcast:31 row_mask:0xc bank_mask:0xf\n"
"v_max_u32_dpp v2,v2,v2 row_bcast:31 row_mask:0xc bank_mask:0xf\n"
"v_max_u32 v2,v2,v16\n"
"v_readlane_b32 s90,v0,63\n"
"v_readlane_b32 s92,v2,63\n"
"s_nop 0\n"
"v_mov_b32 v4,s90\n"
"v_mov_b32 v5,s92\n"
"v_cndmask_b32 v6,v5,v4,vcc\n"
"v_not_b32 v7,v6\n"
"v_bfe_u32 v9,v7,0,8\n"
"v_and_b32 v11,0x800,v7\n"
"v_lshl_add_u32 v11,v9,12,v11\n"
"v_add_u32 v11,v11,v59\n"
"buffer_load_dwordx4 v[128:131],v11,s[24:27],0 offen offset:0 sc1\n"
"buffer_load_dwordx4 v[132:135],v11,s[24:27],0 offen offset:64 sc1\n"
"buffer_load_dwordx4 v[136:139],v11,s[24:27],0 offen offset:128 sc1\n"
"buffer_load_dwordx4 v[140:143],v11,s[24:27],0 offen offset:192 sc1\n"
"buffer_load_dwordx4 v[144:147],v11,s[24:27],0 offen offset:256 sc1\n"
"buffer_load_dwordx4 v[148:151],v11,s[24:27],0 offen offset:320 sc1\n"
"buffer_load_dwordx4 v[152:155],v11,s[24:27],0 offen offset:384 sc1\n"
"buffer_load_dwordx4 v[156:159],v11,s[24:27],0 offen offset:448 sc1\n"
"v_and_b32 v7,0x7ff,v7\n"
"v_lshl_add_u32 v8,v7,11,v57\n"
"v_and_b32 v9,15,v7\n"
"v_lshlrev_b32 v9,6,v9\n"
"v_xor_b32 v9,v9,v55\n"
"v_and_b32 v10,0x7f0,v7\n"
"v_lshl_add_u32 v10,v10,11,v9\n"
"s_mov_b32 s82,71303168\n"
"buffer_load_dwordx4 a[0:3],v10,s[24:27],s82 offen sc1\n"
"s_add_u32 s82,s82,4194304\n"
"buffer_load_dwordx4 a[4:7],v10,s[24:27],s82 offen sc1\n"
"s_add_u32 s82,s82,4194304\n"
"buffer_load_dwordx4 a[8:11],v10,s[24:27],s82 offen sc1\n"
"s_add_u32 s82,s82,4194304\n"
"buffer_load_dwordx4 a[12:15],v10,s[24:27],s82 offen sc1\n"
"buffer_load_dwordx4 a[16:19],v8,s[36:39],0 offen\n"
"v_bfe_u32 v9,v7,0,8\n"
"v_bfe_u32 v12,v9,1,3\n"
"v_and_b32 v13,1,v9\n"
"v_and_b32 v9,0xf0,v9\n"
"v_lshl_add_u32 v9,v13,3,v9\n"
"v_add_u32 v9,v12,v9\n"
"v_lshlrev_b32 v9,4,v9\n"
"s_mov_b32 s82,150241280\n"
"buffer_load_dwordx4 v[16:19],v9,s[24:27],s82 offen sc1\n"
"s_barrier\n"
"ds_read_b64_tr_b16 a[224:225],v192 offset:0\n"
"ds_read_b64_tr_b16 a[240:241],v192 offset:256\n"
"ds_read_b64_tr_b16 a[226:227],v193 offset:0\n"
"ds_read_b64_tr_b16 a[242:243],v193 offset:256\n"
"ds_read_b64_tr_b16 a[228:229],v194 offset:0\n"
"ds_read_b64_tr_b16 a[244:245],v194 offset:256\n"
"ds_read_b64_tr_b16 a[230:231],v195 offset:0\n"
"ds_read_b64_tr_b16 a[246:247],v195 offset:256\n"
"ds_read_b64_tr_b16 a[232:233],v196 offset:0\n"
"ds_read_b64_tr_b16 a[248:249],v196 offset:256\n"
"ds_read_b64_tr_b16 a[234:235],v197 offset:0\n"
"ds_read_b64_tr_b16 a[250:251],v197 offset:256\n"
"ds_read_b64_tr_b16 a[236:237],v198 offset:0\n"
"ds_read_b64_tr_b16 a[252:253],v198 offset:256\n"
"ds_read_b64_tr_b16 a[238:239],v199 offset:0\n"
"ds_read_b64_tr_b16 a[254:255],v199 offset:256\n"
"s_mov_b32 s84,0x40000\n"
"s_waitcnt vmcnt(6)\n"
".Lfk_wh:\n"
"v_mfma_f32_16x16x32_bf16 v[40:43],a[160:163],v[128:131],0\n"
"v_min3_u32 v36,v128,v129,v130\n"
"v_min3_u32 v37,v131,v132,v133\n"
"v_mfma_f32_16x16x32_bf16 v[40:43],a[164:167],v[132:135],v[40:43]\n"
"v_min3_u32 v36,v36,v134,v135\n"
"v_min3_u32 v37,v37,v136,v137\n"
"v_mfma_f32_16x16x32_bf16 v[40:43],a[168:171],v[136:139],v[40:43]\n"
"v_min3_u32 v36,v36,v138,v139\n"
"v_min3_u32 v37,v37,v140,v141\n"
"v_mfma_f32_16x16x32_bf16 v[40:43],a[172:175],v[140:143],v[40:43]\n"
"v_min3_u32 v36,v36,v142,v143\n"
"v_min3_u32 v37,v37,v144,v145\n"
"v_mfma_f32_16x16x32_bf16 v[40:43],a[176:179],v[144:147],v[40:43]\n"
"v_min3_u32 v36,v36,v146,v147\n"
"v_min3_u32 v37,v37,v148,v149\n"
"v_mfma_f32_16x16x32_bf16 v[40:43],a[180:183],v[148:151],v[40:43]\n"
"v_min3_u32 v36,v36,v150,v151\n"
"v_min3_u32 v37,v37,v152,v153\n"
"v_mfma_f32_16x16x32_bf16 v[40:43],a[184:187],v[152:155],v[40:43]\n"
"v_min3_u32 v36,v36,v154,v155\n"
"v_min3_u32 v37,v37,v156,v157\n"
"v_mfma_f32_16x16x32_bf16 v[40:43],a[188:191],v[156:159],v[40:43]\n"
"v_min3_u32 v36,v36,v158,v159\n"
"v_min_u32 v36,v36,v37\n"
"v_cmp_eq_u32 vcc,0,v36\n"
"v_mfma_f32_16x16x32_bf16 v[44:47],a[192:195],v[128:131],0\n"
"s_cbranch_vccz .Lfk_wh_x\n"
"s_sleep 1\n"
"s_mov_b64 exec,vcc\n"
"buffer_load_dwordx4 v[128:131],v11,s[24:27],0 offen offset:0 sc1\n"
"buffer_load_dwordx4 v[132:135],v11,s[24:27],0 offen offset:64 sc1\n"
"buffer_load_dwordx4 v[136:139],v11,s[24:27],0 offen offset:128 sc1\n"
"buffer_load_dwordx4 v[140:143],v11,s[24:27],0 offen offset:192 sc1\n"
"buffer_load_dwordx4 v[144:147],v11,s[24:27],0 offen offset:256 sc1\n"
"buffer_load_dwordx4 v[148:151],v11,s[24:27],0 offen offset:320 sc1\n"
"buffer_load_dwordx4 v[152:155],v11,s[24:27],0 offen offset:384 sc1\n"
"buffer_load_dwordx4 v[156:159],v11,s[24:27],0 offen offset:448 sc1\n"
"s_mov_b64 exec,-1\n"
"s_sub_u32 s84,s84,1\n"
"s_waitcnt vmcnt(0)\n"
"s_cbranch_scc0 .Lfk_wh\n"
".Lfk_wh_x:\n"
"v_mfma_f32_16x16x32_bf16 v[44:47],a[196:199],v[132:135],v[44:47]\n"
"v_mfma_f32_16x16x32_bf16 v[44:47],a[200:203],v[136:139],v[44:47]\n"
"v_mfma_f32_16x16x32_bf16 v[44:47],a[204:207],v[140:143],v[44:47]\n"
"ds_write_b128 v60,v[40:43] offset:1024\n"
"v_mfma_f32_16x16x32_bf16 v[44:47],a[208:211],v[144:147],v[44:47]\n"
"v_mfma_f32_16x16x32_bf16 v[44:47],a[212:215],v[148:151],v[44:47]\n"
"v_mfma_f32_16x16x32_bf16 v[44:47],a[216:219],v[152:155],v[44:47]\n"
"v_mfma_f32_16x16x32_bf16 v[44:47],a[220:223],v[156:159],v[44:47]\n"
"s_nop 7\n"
"ds_write_b128 v60,v[44:47] offset:2048\n"
"s_waitcnt lgkmcnt(0)\n"
"s_barrier\n"
"ds_read_b128 v[128:131],v61 offset:1024\n"
"ds_read_b128 v[144:147],v61 offset:2048\n"
"ds_read_b128 v[132:135],v61 offset:9216\n"
"ds_read_b128 v[148:151],v61 offset:10240\n"
"ds_read_b128 v[136:139],v61 offset:17408\n"
"ds_read_b128 v[152:155],v61 offset:18432\n"
"ds_read_b128 v[140:143],v61 offset:25600\n"
"ds_read_b128 v[156:159],v61 offset:26624\n"
"s_waitcnt lgkmcnt(0)\n"
"v_pk_add_f32 v[128:129],v[128:129],v[132:133]\n"
"v_pk_add_f32 v[136:137],v[136:137],v[140:141]\n"
"v_pk_add_f32 v[130:131],v[130:131],v[134:135]\n"
"v_pk_add_f32 v[138:139],v[138:139],v[142:143]\n"
"v_pk_add_f32 v[128:129],v[128:129],v[136:137]\n"
"v_pk_add_f32 v[130:131],v[130:131],v[138:139]\n"
"v_pk_add_f32 v[144:145],v[144:145],v[148:149]\n"
"v_pk_add_f32 v[152:153],v[152:153],v[156:157]\n"
"v_pk_add_f32 v[146:147],v[146:147],v[150:151]\n"
"v_pk_add_f32 v[154:155],v[154:155],v[158:159]\n"
"v_pk_add_f32 v[144:145],v[144:145],v[152:153]\n"
"v_pk_add_f32 v[146:147],v[146:147],v[154:155]\n"
"v_mul_f32 v132,0xbfb8aa3b,v128\n"
"v_mul_f32 v133,0xbfb8aa3b,v129\n"
"v_mul_f32 v134,0xbfb8aa3b,v130\n"
"v_mul_f32 v135,0xbfb8aa3b,v131\n"
"v_exp_f32 v132,v132\n"
"v_exp_f32 v133,v133\n"
"v_exp_f32 v134,v134\n"
"v_exp_f32 v135,v135\n"
"v_add_f32 v132,1.0,v132\n"
"v_add_f32 v133,1.0,v133\n"
"v_add_f32 v134,1.0,v134\n"
"v_add_f32 v135,1.0,v135\n"
"v_rcp_f32 v132,v132\n"
"v_rcp_f32 v133,v133\n"
"v_rcp_f32 v134,v134\n"
"v_rcp_f32 v135,v135\n"
"v_pk_mul_f32 v[128:129],v[128:129],v[132:133]\n"
"v_pk_mul_f32 v[130:131],v[130:131],v[134:135]\n"
"v_pk_mul_f32 v[128:129],v[128:129],v[144:145]\n"
"v_pk_mul_f32 v[130:131],v[130:131],v[146:147]\n"
"v_cvt_pk_bf16_f32 v132,v128,v129\n"
"v_cvt_pk_bf16_f32 v133,v130,v131\n"
"v_cndmask_b32 v136,0,v132,s[8:9]\n"
"v_cndmask_b32 v137,0,v133,s[8:9]\n"
"v_cndmask_b32 v138,0,v132,s[10:11]\n"
"v_cndmask_b32 v139,0,v133,s[10:11]\n"
"v_cndmask_b32 v141,0,v133,s[12:13]\n"
"v_cndmask_b32 v143,0,v133,s[14:15]\n"
"v_mfma_f32_16x16x32_bf16 v[40:43],a[224:227],v[136:139],0\n"
"v_cndmask_b32 v142,0,v132,s[14:15]\n"
"v_cndmask_b32 v140,0,v132,s[12:13]\n"
"v_mfma_f32_16x16x32_bf16 v[44:47],a[240:243],v[136:139],0\n"
"v_cndmask_b32 v145,0,v133,s[16:17]\n"
"v_cndmask_b32 v147,0,v133,s[18:19]\n"
"v_mfma_f32_16x16x32_bf16 v[40:43],a[228:231],v[140:143],v[40:43]\n"
"v_cndmask_b32 v146,0,v132,s[18:19]\n"
"v_cndmask_b32 v144,0,v132,s[16:17]\n"
"v_mfma_f32_16x16x32_bf16 v[44:47],a[244:247],v[140:143],v[44:47]\n"
"v_cndmask_b32 v149,0,v133,s[20:21]\n"
"v_cndmask_b32 v151,0,v133,s[22:23]\n"
"v_mfma_f32_16x16x32_bf16 v[40:43],a[232:235],v[144:147],v[40:43]\n"
"v_cndmask_b32 v150,0,v132,s[22:23]\n"
"v_cndmask_b32 v148,0,v132,s[20:21]\n"
"v_mfma_f32_16x16x32_bf16 v[44:47],a[248:251],v[144:147],v[44:47]\n"
"s_nop 0\n"
"v_mfma_f32_16x16x32_bf16 v[40:43],a[236:239],v[148:151],v[40:43]\n"
"v_mfma_f32_16x16x32_bf16 v[44:47],a[252:255],v[148:151],v[44:47]\n"
"s_or_b32 s82,s90,0x800\n"
"s_cmp_eq_u32 s63,s82\n"
"s_nop 4\n"
"v_or_b32 v40,1,v40\n"
"v_or_b32 v41,1,v41\n"
"v_or_b32 v42,1,v42\n"
"v_or_b32 v43,1,v43\n"
"v_or_b32 v44,1,v44\n"
"v_or_b32 v45,1,v45\n"
"v_or_b32 v46,1,v46\n"
"v_or_b32 v47,1,v47\n"
"buffer_store_dwordx4 v[40:43],v62,s[24:27],s69 offen offset:0 sc1\n"
"buffer_store_dwordx4 v[44:47],v62,s[24:27],s69 offen offset:512 sc1\n"
"s_cselect_b32 s68,2146435072,s68\n"
"s_or_b32 s82,s92,0x800\n"
"s_cmp_eq_u32 s63,s82\n"
"s_cselect_b32 s68,2146435072,s68\n"
"buffer_store_dwordx4 v[248:251],v112,s[48:51],s68 offen offset:0 sc1\n"
"buffer_store_dwordx4 v[252:255],v112,s[48:51],s68 offen offset:1024 sc1\n"
"s_cmp_eq_u32 s60,0\n"
"s_cselect_b32 s82,s75,2146435072\n"
"buffer_store_dwordx2 a[154:155],off,s[24:27],s82 sc1\n"
"s_mov_b32 s84,0x40000\n"
"s_waitcnt vmcnt(5)\n"
".Lfk_ws:\n"
"v_xor_b32 v20,s80,v17\n"
"v_xor_b32 v21,s80,v19\n"
"v_or_b32 v20,v20,v21\n"
"v_cmp_ne_u32 vcc,0,v20\n"
"s_nop 0\n"
"s_cbranch_vccz .Lfk_ws_x\n"
"s_mov_b64 exec,vcc\n"
"s_mov_b32 s82,150241280\n"
"buffer_load_dwordx4 v[16:19],v9,s[24:27],s82 offen sc1\n"
"s_mov_b64 exec,-1\n"
"s_sub_u32 s84,s84,1\n"
"s_waitcnt vmcnt(0)\n"
"s_cbranch_scc0 .Lfk_ws\n"
".Lfk_ws_x:\n"
"v_mfma_f32_16x16x32_bf16 v[24:27],v[66:69],a[0:3],0\n"
"v_mfma_f32_16x16x32_bf16 v[28:31],v[64:67],a[0:3],0\n"
"v_mfma_f32_16x16x32_bf16 v[24:27],v[66:69],a[4:7],v[24:27]\n"
"v_mfma_f32_16x16x32_bf16 v[28:31],v[64:67],a[4:7],v[28:31]\n"
"v_mfma_f32_16x16x32_bf16 v[24:27],v[66:69],a[8:11],v[24:27]\n"
"v_mfma_f32_16x16x32_bf16 v[28:31],v[64:67],a[8:11],v[28:31]\n"
"v_mfma_f32_16x16x32_bf16 v[24:27],v[66:69],a[12:15],v[24:27]\n"
"v_mfma_f32_16x16x32_bf16 v[28:31],v[64:67],a[12:15],v[28:31]\n"
"v_mfma_f32_16x16x32_bf16 v[24:27],v[66:69],a[16:19],v[24:27]\n"
"v_mfma_f32_16x16x32_bf16 v[28:31],v[64:67],a[16:19],v[28:31]\n"
"v_and_b32 v12,0x800,v6\n"
"v_cmp_ne_u32 vcc,0,v12\n"
"s_nop 1\n"
"v_cndmask_b32 v10,v18,v16,vcc\n"
"v_lshlrev_b32 v11,16,v10\n"
"v_rcp_f32 v11,v11\n"
"v_and_b32 v10,0xffff0000,v10\n"
"v_cvt_pk_bf16_f32 v20,v24,v25\n"
"v_cvt_pk_bf16_f32 v21,v26,v27\n"
"v_cvt_pk_bf16_f32 v22,v28,v29\n"
"v_cvt_pk_bf16_f32 v23,v30,v31\n"
"v_lshlrev_b32 v24,16,v20\n"
"v_and_b32 v25,0xffff0000,v20\n"
"v_lshlrev_b32 v26,16,v21\n"
"v_and_b32 v27,0xffff0000,v21\n"
"v_lshlrev_b32 v28,16,v22\n"
"v_and_b32 v29,0xffff0000,v22\n"
"v_lshlrev_b32 v30,16,v23\n"
"v_and_b32 v31,0xffff0000,v23\n"
"v_pk_add_f32 v[32:33],v[24:25],v[10:11] op_sel_hi:[1,0] neg_lo:[0,1] neg_hi:[0,1]\n"
"v_pk_add_f32 v[34:35],v[26:27],v[10:11] op_sel_hi:[1,0] neg_lo:[0,1] neg_hi:[0,1]\n"
"v_pk_add_f32 v[36:37],v[28:29],v[10:11] op_sel_hi:[1,0] neg_lo:[0,1] neg_hi:[0,1]\n"
"v_pk_add_f32 v[38:39],v[30:31],v[10:11] op_sel_hi:[1,0] neg_lo:[0,1] neg_hi:[0,1]\n"
"v_cvt_pk_bf16_f32 v20,v32,v33\n"
"v_cvt_pk_bf16_f32 v21,v34,v35\n"
"v_cvt_pk_bf16_f32 v22,v36,v37\n"
"v_cvt_pk_bf16_f32 v23,v38,v39\n"
"v_lshlrev_b32 v32,16,v20\n"
"v_and_b32 v33,0xffff0000,v20\n"
"v_lshlrev_b32 v34,16,v21\n"
"v_and_b32 v35,0xffff0000,v21\n"
"v_lshlrev_b32 v36,16,v22\n"
"v_and_b32 v37,0xffff0000,v22\n"
"v_lshlrev_b32 v38,16,v23\n"
"v_and_b32 v39,0xffff0000,v23\n"
"v_pk_mul_f32 v[32:33],v[32:33],v[10:11] op_sel:[0,1]\n"
"v_pk_mul_f32 v[34:35],v[34:35],v[10:11] op_sel:[0,1]\n"
"v_pk_mul_f32 v[36:37],v[36:37],v[10:11] op_sel:[0,1]\n"
"v_pk_mul_f32 v[38:39],v[38:39],v[10:11] op_sel:[0,1]\n"
"v_cvt_pk_bf16_f32 v20,v32,v33\n"
"v_cvt_pk_bf16_f32 v21,v34,v35\n"
"v_cvt_pk_bf16_f32 v22,v36,v37\n"
"v_cvt_pk_bf16_f32 v23,v38,v39\n"
"ds_write_b128 v58,v[20:23]\n"
"ds_write_b128 v60,v[24:27] offset:3072\n"
"ds_write_b128 v60,v[28:31] offset:4096\n"
"s_bfe_u32 s82,s73,0x20005\n"
"s_lshl_b32 s82,s82,13\n"
"s_and_b32 s83,s73,31\n"
"s_lshr_b32 s91,s73,7\n"
"s_lshl_b32 s89,s91,9\n"
"s_lshl4_add_u32 s89,s83,s89\n"
"s_add_u32 s89,s89,s82\n"
"s_add_u32 s89,s89,1024\n"
"s_lshl1_add_u32 s83,s83,s91\n"
"s_lshl4_add_u32 s82,s83,s82\n"
"s_add_u32 s82,s82,4096\n"
"v_bfe_u32 v0,v112,4,3\n"
"v_lshl_add_u32 v1,v0,1,s89\n"
"v_and_b32 v3,3,v0\n"
"v_lshl_add_u32 v3,v3,2,s82\n"
"v_lshrrev_b32 v4,2,v0\n"
"v_lshl_add_u32 v3,v4,10,v3\n"
"s_cmp_eq_u32 s91,0\n"
"s_cselect_b32 s82,s90,s92\n"
"s_not_b32 s83,s82\n"
"s_and_b32 s89,s83,0x7ff\n"
"s_lshl_b32 s89,s89,11\n"
"s_and_b32 s91,s73,127\n"
"s_lshl4_add_u32 s89,s91,s89\n"
"s_ashr_i32 s83,s83,31\n"
"s_or_b32 s83,s83,0x80000000\n"
"s_xor_b32 s82,s82,s83\n"
"s_and_b32 s82,s82,0xffff0000\n"
"v_mov_b32 v5,0xbfb8aa3b\n"
"v_mul_f32 v5,s82,v5\n"
"v_exp_f32 v5,v5\n"
"s_nop 0\n"
"v_add_f32 v5,1.0,v5\n"
"v_rcp_f32 v5,v5\n"
"s_nop 0\n"
"v_cvt_pk_bf16_f32 v5,v5,v5\n"
"v_lshlrev_b32 v5,16,v5\n"
"s_lshl_b32 s82,s52,7\n"
"s_add_u32 s82,s82,64\n"
"v_lshrrev_b32 v6,3,v112\n"
"v_and_b32 v6,0x60,v6\n"
"v_and_b32 v7,16,v112\n"
"v_add3_u32 v6,v6,v7,s82\n"
"v_lshrrev_b32 v7,2,v112\n"
"v_add_u32 v7,64,v7\n"
"v_lshlrev_b32 v8,1,v0\n"
"s_sleep 1\n"
"s_mov_b32 s84,0x40000\n"
"buffer_load_dwordx4 v[16:19],v63,s[24:27],s70 offen sc1\n"
"buffer_load_dwordx4 v[20:23],v63,s[24:27],s71 offen sc1\n"
"s_waitcnt vmcnt(0)\n"
".Lfk_wy:\n"
"v_min3_u32 v24,v16,v17,v18\n"
"v_min3_u32 v25,v19,v20,v21\n"
"v_min3_u32 v24,v24,v22,v23\n"
"v_min_u32 v24,v24,v25\n"
"v_cmp_eq_u32 vcc,0,v24\n"
"s_nop 0\n"
"s_cbranch_vccz .Lfk_wy_x\n"
"s_sleep 1\n"
"s_mov_b64 exec,vcc\n"
"buffer_load_dwordx4 v[16:19],v63,s[24:27],s70 offen sc1\n"
"buffer_load_dwordx4 v[20:23],v63,s[24:27],s71 offen sc1\n"
"s_mov_b64 exec,-1\n"
"s_sub_u32 s84,s84,1\n"
"s_waitcnt vmcnt(0)\n"
"s_cbranch_scc0 .Lfk_wy\n"
".Lfk_wy_x:\n"
"s_mov_b32 vcc_lo,-1\n"
"s_mov_b32 vcc_hi,0x00ffffff\n"
"v_add_f32 v16,v16,v20\n"
"v_add_f32 v17,v17,v21\n"
"v_add_f32 v18,v18,v22\n"
"v_add_f32 v19,v19,v23\n"
"v_cndmask_b32 v16,0,v16,vcc\n"
"v_cndmask_b32 v17,0,v17,vcc\n"
"v_cndmask_b32 v18,0,v18,vcc\n"
"v_cndmask_b32 v19,0,v19,vcc\n"
"v_add_f32_dpp v16,v16,v16 quad_perm:[2,3,0,1] row_mask:0xf bank_mask:0xf\n"
"v_add_f32_dpp v17,v17,v17 quad_perm:[2,3,0,1] row_mask:0xf bank_mask:0xf\n"
"v_add_f32_dpp v18,v18,v18 quad_perm:[2,3,0,1] row_mask:0xf bank_mask:0xf\n"
"v_add_f32_dpp v19,v19,v19 quad_perm:[2,3,0,1] row_mask:0xf bank_mask:0xf\n"
"v_add_f32_dpp v16,v16,v16 row_ror:4 row_mask:0xf bank_mask:0xf\n"
"v_add_f32_dpp v17,v17,v17 row_ror:4 row_mask:0xf bank_mask:0xf\n"
"v_add_f32_dpp v18,v18,v18 row_ror:4 row_mask:0xf bank_mask:0xf\n"
"v_add_f32_dpp v19,v19,v19 row_ror:4 row_mask:0xf bank_mask:0xf\n"
"v_add_f32_dpp v16,v16,v16 row_ror:8 row_mask:0xf bank_mask:0xf\n"
"v_add_f32_dpp v17,v17,v17 row_ror:8 row_mask:0xf bank_mask:0xf\n"
"v_add_f32_dpp v18,v18,v18 row_ror:8 row_mask:0xf bank_mask:0xf\n"
"v_add_f32_dpp v19,v19,v19 row_ror:8 row_mask:0xf bank_mask:0xf\n"
"s_mov_b32 exec_lo,0x30003\n"
"s_mov_b32 exec_hi,0x30003\n"
"ds_write_b128 v6,v[16:19]\n"
"s_mov_b64 exec,-1\n"
"s_waitcnt lgkmcnt(0)\n"
"s_barrier\n"
"s_cmp_lg_u32 s52,0\n"
"s_cbranch_scc1 .Lfk_t2\n"
"ds_read2_b32 v[16:17],v7 offset0:0 offset1:64\n"
"ds_read_u16 v2,v1\n"
"ds_read_b32 v3,v3\n"
"s_waitcnt lgkmcnt(0)\n"
"v_lshlrev_b32 v2,16,v2\n"
"v_add_f32 v16,v16,v17\n"
"s_nop 1\n"
"v_add_f32_dpp v16,v16,v16 row_ror:8 row_mask:0xf bank_mask:0xf\n"
"v_mov_b32 v17,v16\n"
"s_nop 1\n"
"v_permlane16_swap_b32 v17,v16\n"
"v_add_f32 v16,v16,v17\n"
"v_mov_b32 v17,v16\n"
"s_nop 1\n"
"v_permlane32_swap_b32 v17,v16\n"
"v_add_f32 v16,v16,v17\n"
"v_cvt_pk_bf16_f32 v16,v16,v16\n"
"v_lshlrev_b32 v16,16,v16\n"
"v_mul_f32 v16,v16,v5\n"
"v_cvt_pk_bf16_f32 v16,v16,v16\n"
"v_lshlrev_b32 v16,16,v16\n"
"v_add_f32 v16,v16,v2\n"
"v_cvt_pk_bf16_f32 v16,v16,v16\n"
"v_lshlrev_b32 v16,16,v16\n"
"v_add_f32 v16,v16,v3\n"
"s_nop 1\n"
"v_mov_b32_dpp v17,v16 quad_perm:[1,0,3,2] row_mask:0xf bank_mask:0xf\n"
"v_cvt_pk_bf16_f32 v16,v16,v17\n"
"s_mov_b64 exec,0x55\n"
"buffer_store_dword v16,v8,s[48:51],s89 offen sc1\n"
"s_mov_b64 exec,-1\n"
".Lfk_t2:\n"
"s_branch .Lfk_end\n"
".Lfk_g0:\n"
"s_waitcnt vmcnt(11) lgkmcnt(0)\n"
"s_barrier\n"
"v_mfma_f32_16x16x32_bf16 v[32:35],a[96:99],a[0:3],v[96:99]\n"
"s_add_u32 m0,s55,4096\n"
"v_exp_f32 v0,v0\n"
"v_mfma_f32_16x16x32_bf16 v[32:35],a[100:103],a[4:7],v[32:35]\n"
"s_add_u32 s54,s54,s22\n"
"v_exp_f32 v1,v1\n"
"v_mfma_f32_16x16x32_bf16 v[32:35],a[104:107],a[8:11],v[32:35]\n"
"buffer_load_dwordx4 v115,s[32:35],s54 offen sc1 lds\n"
"v_exp_f32 v2,v2\n"
"v_cvt_pk_bf16_f32 v64,v0,v1\n"
"v_mfma_f32_16x16x32_bf16 v[32:35],a[108:111],a[12:15],v[32:35]\n"
"v_exp_f32 v3,v3\n"
"v_mfma_f32_16x16x32_bf16 v[36:39],a[124:127],a[12:15],v[96:99]\n"
"v_exp_f32 v4,v4\n"
"v_cvt_pk_bf16_f32 v65,v2,v3\n"
"v_mfma_f32_16x16x32_bf16 v[36:39],a[120:123],a[8:11],v[36:39]\n"
"v_exp_f32 v5,v5\n"
"v_mfma_f32_16x16x32_bf16 v[36:39],a[116:119],a[4:7],v[36:39]\n"
"v_exp_f32 v6,v6\n"
"v_cvt_pk_bf16_f32 v66,v4,v5\n"
"v_mfma_f32_16x16x32_bf16 v[36:39],a[112:115],a[0:3],v[36:39]\n"
"v_exp_f32 v7,v7\n"
"v_mfma_f32_16x16x32_bf16 v[44:47],a[112:115],a[16:19],v[100:103]\n"
"v_exp_f32 v8,v8\n"
"v_cvt_pk_bf16_f32 v67,v6,v7\n"
"v_exp_f32 v9,v9\n"
"v_mfma_f32_16x16x32_bf16 v[44:47],a[116:119],a[20:23],v[44:47]\n"
"ds_read_b128 a[64:67],v113 offset:49152\n"
"ds_read_b128 a[68:71],v113 offset:50176\n"
"v_exp_f32 v10,v10\n"
"v_mfma_f32_16x16x32_bf16 v[44:47],a[120:123],a[24:27],v[44:47]\n"
"ds_read_b128 a[72:75],v113 offset:51200\n"
"ds_read_b128 a[76:79],v113 offset:52224\n"
"v_exp_f32 v11,v11\n"
"v_mfma_f32_16x16x32_bf16 v[44:47],a[124:127],a[28:31],v[44:47]\n"
"ds_read_b128 a[80:83],v113 offset:53248\n"
"ds_read_b128 a[84:87],v113 offset:54272\n"
"v_exp_f32 v12,v12\n"
"v_mfma_f32_16x16x32_bf16 v[40:43],a[108:111],a[28:31],v[100:103]\n"
"ds_read_b128 a[88:91],v113 offset:55296\n"
"ds_read_b128 a[92:95],v113 offset:56320\n"
"v_mfma_f32_16x16x32_bf16 v[40:43],a[104:107],a[24:27],v[40:43]\n"
"v_exp_f32 v13,v13\n"
"v_cvt_pk_bf16_f32 v68,v8,v9\n"
"v_cvt_pk_bf16_f32 v69,v10,v11\n"
"v_mfma_f32_16x16x32_bf16 v[40:43],a[100:103],a[20:23],v[40:43]\n"
"v_exp_f32 v14,v14\n"
"v_cvt_pk_bf16_f32 v70,v12,v13\n"
"v_mfma_f32_16x16x32_bf16 v[40:43],a[96:99],a[16:19],v[40:43]\n"
"v_exp_f32 v15,v15\n"
"v_mfma_f32_16x16x32_bf16 v[48:51],a[96:99],a[32:35],v[104:107]\n"
"v_exp_f32 v16,v16\n"
"v_cvt_pk_bf16_f32 v71,v14,v15\n"
"v_mfma_f32_16x16x32_bf16 v[48:51],a[100:103],a[36:39],v[48:51]\n"
"v_exp_f32 v17,v17\n"
"v_mfma_f32_16x16x32_bf16 v[48:51],a[104:107],a[40:43],v[48:51]\n"
"v_exp_f32 v18,v18\n"
"v_cvt_pk_bf16_f32 v72,v16,v17\n"
"v_mfma_f32_16x16x32_bf16 v[48:51],a[108:111],a[44:47],v[48:51]\n"
"v_exp_f32 v19,v19\n"
"v_mfma_f32_16x16x32_bf16 v[52:55],a[124:127],a[44:47],v[104:107]\n"
"v_exp_f32 v20,v20\n"
"v_cvt_pk_bf16_f32 v73,v18,v19\n"
"v_mfma_f32_16x16x32_bf16 v[52:55],a[120:123],a[40:43],v[52:55]\n"
"v_exp_f32 v21,v21\n"
"v_mfma_f32_16x16x32_bf16 v[52:55],a[116:119],a[36:39],v[52:55]\n"
"v_exp_f32 v22,v22\n"
"v_cvt_pk_bf16_f32 v74,v20,v21\n"
"v_mfma_f32_16x16x32_bf16 v[52:55],a[112:115],a[32:35],v[52:55]\n"
"v_exp_f32 v23,v23\n"
"v_mfma_f32_16x16x32_bf16 v[60:63],a[112:115],a[48:51],v[108:111]\n"
"v_exp_f32 v24,v24\n"
"v_cvt_pk_bf16_f32 v75,v22,v23\n"
"v_mfma_f32_16x16x32_bf16 v[60:63],a[116:119],a[52:55],v[60:63]\n"
"v_exp_f32 v25,v25\n"
"v_mfma_f32_16x16x32_bf16 v[60:63],a[120:123],a[56:59],v[60:63]\n"
"v_exp_f32 v26,v26\n"
"v_exp_f32 v27,v27\n"
"v_mfma_f32_16x16x32_bf16 v[60:63],a[124:127],a[60:63],v[60:63]\n"
"buffer_store_dwordx4 a[228:231],v112,s[24:27],s56 offen offset:1024 sc1\n"
"v_mfma_f32_16x16x32_bf16 v[56:59],a[108:111],a[60:63],v[108:111]\n"
"v_exp_f32 v28,v28\n"
"v_cvt_pk_bf16_f32 v77,v26,v27\n"
"v_mfma_f32_16x16x32_bf16 v[56:59],a[104:107],a[56:59],v[56:59]\n"
"v_exp_f32 v29,v29\n"
"v_mfma_f32_16x16x32_bf16 v[56:59],a[100:103],a[52:55],v[56:59]\n"
"v_exp_f32 v30,v30\n"
"v_cvt_pk_bf16_f32 v78,v28,v29\n"
"v_mfma_f32_16x16x32_bf16 v[56:59],a[96:99],a[48:51],v[56:59]\n"
"s_waitcnt vmcnt(9) lgkmcnt(0)\n"
"v_exp_f32 v31,v31\n"
"v_cvt_pk_bf16_f32 v76,v24,v25\n"
"v_cvt_pk_bf16_f32 v79,v30,v31\n"
"s_barrier\n"
"v_mfma_f32_16x16x32_bf16 v[0:3],a[64:67],a[0:3],v[96:99]\n"
"s_add_u32 m0,s55,8192\n"
"v_exp_f32 v122,v32\n"
"v_mfma_f32_16x16x32_bf16 v[0:3],a[68:71],a[4:7],v[0:3]\n"
"s_add_u32 s54,s54,s22\n"
"v_exp_f32 v123,v33\n"
"v_mfma_f32_16x16x32_bf16 v[0:3],a[72:75],a[8:11],v[0:3]\n"
"ds_read_b128 a[96:99],v114 offset:40960\n"
"ds_read_b128 a[100:103],v114 offset:41984\n"
"v_mfma_f32_16x16x32_bf16 v[0:3],a[76:79],a[12:15],v[0:3]\n"
"v_exp_f32 v119,v34\n"
"v_mfma_f32_16x16x32_bf16 v[4:7],a[92:95],a[12:15],v[96:99]\n"
"ds_read_b128 a[104:107],v114 offset:43008\n"
"v_cvt_pk_bf16_f32 v80,v122,v123\n"
"v_mfma_f32_16x16x32_bf16 v[4:7],a[88:91],a[8:11],v[4:7]\n"
"v_exp_f32 v120,v35\n"
"v_mfma_f32_16x16x32_bf16 v[4:7],a[84:87],a[4:7],v[4:7]\n"
"ds_read_b128 a[108:111],v114 offset:44032\n"
"ds_read_b128 a[112:115],v114 offset:45056\n"
"v_mfma_f32_16x16x32_bf16 v[4:7],a[80:83],a[0:3],v[4:7]\n"
"v_exp_f32 v122,v36\n"
"v_mfma_f32_16x16x32_bf16 v[12:15],a[80:83],a[16:19],v[100:103]\n"
"ds_read_b128 a[116:119],v114 offset:46080\n"
"v_cvt_pk_bf16_f32 v81,v119,v120\n"
"v_mfma_f32_16x16x32_bf16 v[12:15],a[84:87],a[20:23],v[12:15]\n"
"v_exp_f32 v123,v37\n"
"v_mfma_f32_16x16x32_bf16 v[12:15],a[88:91],a[24:27],v[12:15]\n"
"ds_read_b128 a[120:123],v114 offset:47104\n"
"ds_read_b128 a[124:127],v114 offset:48128\n"
"v_mfma_f32_16x16x32_bf16 v[12:15],a[92:95],a[28:31],v[12:15]\n"
"v_exp_f32 v119,v38\n"
"v_cvt_pk_bf16_f32 v82,v122,v123\n"
"v_mfma_f32_16x16x32_bf16 v[8:11],a[76:79],a[28:31],v[100:103]\n"
"buffer_store_dwordx4 a[168:171],v112,s[24:27],s3 offen offset:2048 sc1\n"
"v_mfma_f32_16x16x32_bf16 v[8:11],a[72:75],a[24:27],v[8:11]\n"
"v_exp_f32 v120,v39\n"
"v_mfma_f32_16x16x32_bf16 v[8:11],a[68:71],a[20:23],v[8:11]\n"
"buffer_load_dwordx4 v115,s[32:35],s54 offen sc1 lds\n"
"v_mfma_f32_16x16x32_bf16 v[8:11],a[64:67],a[16:19],v[8:11]\n"
"v_exp_f32 v122,v40\n"
"v_mfma_f32_16x16x32_bf16 v[16:19],a[64:67],a[32:35],v[104:107]\n"
"v_cvt_pk_bf16_f32 v83,v119,v120\n"
"v_or3_b32 v118,v80,v81,v82\n"
"v_mfma_f32_16x16x32_bf16 v[16:19],a[68:71],a[36:39],v[16:19]\n"
"v_exp_f32 v123,v41\n"
"v_mfma_f32_16x16x32_bf16 v[16:19],a[72:75],a[40:43],v[16:19]\n"
"v_cvt_pk_bf16_f32 v84,v122,v123\n"
"v_or3_b32 v118,v118,v83,v84\n"
"v_mfma_f32_16x16x32_bf16 v[16:19],a[76:79],a[44:47],v[16:19]\n"
"v_exp_f32 v119,v42\n"
"v_mfma_f32_16x16x32_bf16 v[20:23],a[92:95],a[44:47],v[104:107]\n"
"v_mfma_f32_16x16x32_bf16 v[20:23],a[88:91],a[40:43],v[20:23]\n"
"v_exp_f32 v120,v43\n"
"v_mfma_f32_16x16x32_bf16 v[20:23],a[84:87],a[36:39],v[20:23]\n"
"v_cvt_pk_bf16_f32 v85,v119,v120\n"
"v_mfma_f32_16x16x32_bf16 v[20:23],a[80:83],a[32:35],v[20:23]\n"
"v_exp_f32 v122,v44\n"
"v_mfma_f32_16x16x32_bf16 v[28:31],a[80:83],a[48:51],v[108:111]\n"
"buffer_store_dwordx4 a[172:175],v112,s[24:27],s3 offen offset:3072 sc1\n"
"v_mfma_f32_16x16x32_bf16 v[28:31],a[84:87],a[52:55],v[28:31]\n"
"v_exp_f32 v123,v45\n"
"v_mfma_f32_16x16x32_bf16 v[28:31],a[88:91],a[56:59],v[28:31]\n"
"s_sub_u32 s3,s3,16384\n"
"v_mfma_f32_16x16x32_bf16 v[28:31],a[92:95],a[60:63],v[28:31]\n"
"v_exp_f32 v119,v46\n"
"v_mfma_f32_16x16x32_bf16 v[24:27],a[76:79],a[60:63],v[108:111]\n"
"v_cvt_pk_bf16_f32 v86,v122,v123\n"
"v_or3_b32 v118,v118,v85,v86\n"
"v_mfma_f32_16x16x32_bf16 v[24:27],a[72:75],a[56:59],v[24:27]\n"
"v_exp_f32 v120,v47\n"
"v_mfma_f32_16x16x32_bf16 v[24:27],a[68:71],a[52:55],v[24:27]\n"
"v_cvt_pk_bf16_f32 v87,v119,v120\n"
"v_mfma_f32_16x16x32_bf16 v[24:27],a[64:67],a[48:51],v[24:27]\n"
"s_waitcnt lgkmcnt(0)\n"
"v_exp_f32 v122,v48\n"
"v_mfma_f32_16x16x32_bf16 v[128:131],a[96:99],v[64:67],0\n"
"v_exp_f32 v123,v49\n"
"v_mfma_f32_16x16x32_bf16 v[160:163],a[96:99],v[68:71],0\n"
"ds_read_b128 a[64:67],v113 offset:32768\n"
"ds_read_b128 a[68:71],v113 offset:33792\n"
"v_mfma_f32_16x16x32_bf16 v[192:195],a[96:99],v[72:75],0\n"
"v_exp_f32 v119,v50\n"
"v_mfma_f32_16x16x32_bf16 v[224:227],a[96:99],v[76:79],0\n"
"ds_read_b128 a[72:75],v113 offset:34816\n"
"v_cvt_pk_bf16_f32 v88,v122,v123\n"
"v_mfma_f32_16x16x32_bf16 a[140:143],a[144:147],v[76:79],0\n"
"v_exp_f32 v120,v51\n"
"v_mfma_f32_16x16x32_bf16 v[228:231],a[100:103],v[76:79],0\n"
"ds_read_b128 a[76:79],v113 offset:35840\n"
"ds_read_b128 a[80:83],v113 offset:36864\n"
"v_mfma_f32_16x16x32_bf16 v[132:135],a[100:103],v[64:67],0\n"
"v_exp_f32 v122,v52\n"
"v_mfma_f32_16x16x32_bf16 v[164:167],a[100:103],v[68:71],0\n"
"ds_read_b128 a[84:87],v113 offset:37888\n"
"v_cvt_pk_bf16_f32 v89,v119,v120\n"
"v_mfma_f32_16x16x32_bf16 v[196:199],a[100:103],v[72:75],0\n"
"v_exp_f32 v123,v53\n"
"v_mfma_f32_16x16x32_bf16 a[136:139],a[144:147],v[72:75],0\n"
"ds_read_b128 a[88:91],v113 offset:38912\n"
"ds_read_b128 a[92:95],v113 offset:39936\n"
"v_mfma_f32_16x16x32_bf16 v[200:203],a[104:107],v[72:75],0\n"
"v_exp_f32 v119,v54\n"
"v_cvt_pk_bf16_f32 v90,v122,v123\n"
"v_mfma_f32_16x16x32_bf16 v[232:235],a[104:107],v[76:79],0\n"
"buffer_store_dwordx4 a[176:179],v112,s[24:27],s3 offen offset:0 sc1\n"
"v_mfma_f32_16x16x32_bf16 v[136:139],a[104:107],v[64:67],0\n"
"v_exp_f32 v120,v55\n"
"v_mfma_f32_16x16x32_bf16 v[168:171],a[104:107],v[68:71],0\n"
"v_cvt_pk_bf16_f32 v91,v119,v120\n"
"v_or3_b32 v118,v118,v87,v88\n"
"v_mfma_f32_16x16x32_bf16 a[132:135],a[144:147],v[68:71],0\n"
"v_exp_f32 v122,v56\n"
"v_mfma_f32_16x16x32_bf16 v[172:175],a[108:111],v[68:71],0\n"
"v_or3_b32 v118,v118,v89,v90\n"
"v_mfma_f32_16x16x32_bf16 v[204:207],a[108:111],v[72:75],0\n"
"v_exp_f32 v123,v57\n"
"v_mfma_f32_16x16x32_bf16 v[236:239],a[108:111],v[76:79],0\n"
"v_cvt_pk_bf16_f32 v92,v122,v123\n"
"v_or3_b32 v118,v118,v91,v92\n"
"v_mfma_f32_16x16x32_bf16 v[140:143],a[108:111],v[64:67],0\n"
"v_exp_f32 v119,v58\n"
"v_mfma_f32_16x16x32_bf16 a[128:131],a[144:147],v[64:67],0\n"
"v_mfma_f32_16x16x32_bf16 v[144:147],a[112:115],v[64:67],0\n"
"v_exp_f32 v120,v59\n"
"v_mfma_f32_16x16x32_bf16 v[176:179],a[112:115],v[68:71],0\n"
"v_cvt_pk_bf16_f32 v93,v119,v120\n"
"v_mfma_f32_16x16x32_bf16 v[208:211],a[112:115],v[72:75],0\n"
"v_exp_f32 v122,v60\n"
"v_mfma_f32_16x16x32_bf16 v[240:243],a[112:115],v[76:79],0\n"
"v_mfma_f32_16x16x32_bf16 v[244:247],a[116:119],v[76:79],0\n"
"v_exp_f32 v123,v61\n"
"v_mfma_f32_16x16x32_bf16 v[148:151],a[116:119],v[64:67],0\n"
"v_cvt_pk_bf16_f32 v94,v122,v123\n"
"v_or3_b32 v118,v118,v93,v94\n"
"v_mfma_f32_16x16x32_bf16 v[180:183],a[116:119],v[68:71],0\n"
"v_exp_f32 v119,v62\n"
"v_mfma_f32_16x16x32_bf16 v[212:215],a[116:119],v[72:75],0\n"
"buffer_store_dwordx4 a[232:235],v112,s[24:27],s56 offen offset:2048 sc1\n"
"v_mfma_f32_16x16x32_bf16 v[216:219],a[120:123],v[72:75],0\n"
"v_exp_f32 v120,v63\n"
"v_mfma_f32_16x16x32_bf16 v[248:251],a[120:123],v[76:79],0\n"
"v_cvt_pk_bf16_f32 v95,v119,v120\n"
"v_or_b32 v118,v118,v95\n"
"v_mfma_f32_16x16x32_bf16 v[152:155],a[120:123],v[64:67],0\n"
"v_and_b32 v118,0x40004000,v118\n"
"v_cmp_ne_u32 vcc,0,v118\n"
"v_mfma_f32_16x16x32_bf16 v[184:187],a[120:123],v[68:71],0\n"
"v_mfma_f32_16x16x32_bf16 v[188:191],a[124:127],v[68:71],0\n"
"v_mfma_f32_16x16x32_bf16 v[220:223],a[124:127],v[72:75],0\n"
"v_mfma_f32_16x16x32_bf16 v[252:255],a[124:127],v[76:79],0\n"
"v_mfma_f32_16x16x32_bf16 v[156:159],a[124:127],v[64:67],0\n"
"s_cbranch_vccnz .Lfk_cs_k1\n"
".Lfk_cr_k1:\n"
"s_waitcnt lgkmcnt(0)\n"
"s_barrier\n"
"v_mfma_f32_16x16x32_bf16 v[32:35],a[64:67],a[0:3],v[96:99]\n"
"s_add_u32 m0,s55,12288\n"
"v_exp_f32 v122,v0\n"
"v_mfma_f32_16x16x32_bf16 v[32:35],a[68:71],a[4:7],v[32:35]\n"
"s_add_u32 s54,s54,s22\n"
"v_exp_f32 v123,v1\n"
"v_mfma_f32_16x16x32_bf16 v[32:35],a[72:75],a[8:11],v[32:35]\n"
"ds_read_b128 a[96:99],v114 offset:8192\n"
"ds_read_b128 a[100:103],v114 offset:9216\n"
"v_mfma_f32_16x16x32_bf16 v[32:35],a[76:79],a[12:15],v[32:35]\n"
"v_exp_f32 v119,v2\n"
"v_mfma_f32_16x16x32_bf16 v[36:39],a[92:95],a[12:15],v[96:99]\n"
"ds_read_b128 a[104:107],v114 offset:10240\n"
"v_cvt_pk_bf16_f32 v64,v122,v123\n"
"v_mfma_f32_16x16x32_bf16 v[36:39],a[88:91],a[8:11],v[36:39]\n"
"v_exp_f32 v120,v3\n"
"v_mfma_f32_16x16x32_bf16 v[36:39],a[84:87],a[4:7],v[36:39]\n"
"ds_read_b128 a[108:111],v114 offset:11264\n"
"ds_read_b128 a[112:115],v114 offset:12288\n"
"v_mfma_f32_16x16x32_bf16 v[36:39],a[80:83],a[0:3],v[36:39]\n"
"v_exp_f32 v122,v4\n"
"v_mfma_f32_16x16x32_bf16 v[44:47],a[80:83],a[16:19],v[100:103]\n"
"ds_read_b128 a[116:119],v114 offset:13312\n"
"v_cvt_pk_bf16_f32 v65,v119,v120\n"
"v_mfma_f32_16x16x32_bf16 v[44:47],a[84:87],a[20:23],v[44:47]\n"
"v_exp_f32 v123,v5\n"
"v_mfma_f32_16x16x32_bf16 v[44:47],a[88:91],a[24:27],v[44:47]\n"
"ds_read_b128 a[120:123],v114 offset:14336\n"
"ds_read_b128 a[124:127],v114 offset:15360\n"
"v_mfma_f32_16x16x32_bf16 v[44:47],a[92:95],a[28:31],v[44:47]\n"
"v_exp_f32 v119,v6\n"
"v_cvt_pk_bf16_f32 v66,v122,v123\n"
"v_mfma_f32_16x16x32_bf16 v[40:43],a[76:79],a[28:31],v[100:103]\n"
"buffer_store_dwordx4 a[180:183],v112,s[24:27],s3 offen offset:1024 sc1\n"
"v_mfma_f32_16x16x32_bf16 v[40:43],a[72:75],a[24:27],v[40:43]\n"
"v_exp_f32 v120,v7\n"
"v_mfma_f32_16x16x32_bf16 v[40:43],a[68:71],a[20:23],v[40:43]\n"
"buffer_load_dwordx4 v115,s[32:35],s54 offen sc1 lds\n"
"v_mfma_f32_16x16x32_bf16 v[40:43],a[64:67],a[16:19],v[40:43]\n"
"v_exp_f32 v122,v8\n"
"v_mfma_f32_16x16x32_bf16 v[48:51],a[64:67],a[32:35],v[104:107]\n"
"v_cvt_pk_bf16_f32 v67,v119,v120\n"
"v_or3_b32 v118,v64,v65,v66\n"
"v_mfma_f32_16x16x32_bf16 v[48:51],a[68:71],a[36:39],v[48:51]\n"
"v_exp_f32 v123,v9\n"
"v_mfma_f32_16x16x32_bf16 v[48:51],a[72:75],a[40:43],v[48:51]\n"
"v_cvt_pk_bf16_f32 v68,v122,v123\n"
"v_or3_b32 v118,v118,v67,v68\n"
"v_mfma_f32_16x16x32_bf16 v[48:51],a[76:79],a[44:47],v[48:51]\n"
"v_exp_f32 v119,v10\n"
"v_mfma_f32_16x16x32_bf16 v[52:55],a[92:95],a[44:47],v[104:107]\n"
"v_mfma_f32_16x16x32_bf16 v[52:55],a[88:91],a[40:43],v[52:55]\n"
"v_exp_f32 v120,v11\n"
"v_mfma_f32_16x16x32_bf16 v[52:55],a[84:87],a[36:39],v[52:55]\n"
"v_cvt_pk_bf16_f32 v69,v119,v120\n"
"v_mfma_f32_16x16x32_bf16 v[52:55],a[80:83],a[32:35],v[52:55]\n"
"v_exp_f32 v122,v12\n"
"v_mfma_f32_16x16x32_bf16 v[60:63],a[80:83],a[48:51],v[108:111]\n"
"buffer_store_dwordx4 a[184:187],v112,s[24:27],s3 offen offset:2048 sc1\n"
"v_mfma_f32_16x16x32_bf16 v[60:63],a[84:87],a[52:55],v[60:63]\n"
"v_exp_f32 v123,v13\n"
"v_mfma_f32_16x16x32_bf16 v[60:63],a[88:91],a[56:59],v[60:63]\n"
"v_cvt_pk_bf16_f32 v70,v122,v123\n"
"v_mfma_f32_16x16x32_bf16 v[60:63],a[92:95],a[60:63],v[60:63]\n"
"v_exp_f32 v119,v14\n"
"v_mfma_f32_16x16x32_bf16 v[56:59],a[76:79],a[60:63],v[108:111]\n"
"v_or3_b32 v118,v118,v69,v70\n"
"v_mfma_f32_16x16x32_bf16 v[56:59],a[72:75],a[56:59],v[56:59]\n"
"v_exp_f32 v120,v15\n"
"v_mfma_f32_16x16x32_bf16 v[56:59],a[68:71],a[52:55],v[56:59]\n"
"v_cvt_pk_bf16_f32 v71,v119,v120\n"
"v_mfma_f32_16x16x32_bf16 v[56:59],a[64:67],a[48:51],v[56:59]\n"
"s_waitcnt lgkmcnt(0)\n"
"v_exp_f32 v122,v16\n"
"v_mfma_f32_16x16x32_bf16 v[128:131],a[96:99],v[80:83],v[128:131]\n"
"v_exp_f32 v123,v17\n"
"v_mfma_f32_16x16x32_bf16 v[160:163],a[96:99],v[84:87],v[160:163]\n"
"ds_read_b128 a[64:67],v113 offset:0\n"
"ds_read_b128 a[68:71],v113 offset:1024\n"
"v_mfma_f32_16x16x32_bf16 v[192:195],a[96:99],v[88:91],v[192:195]\n"
"v_exp_f32 v119,v18\n"
"v_mfma_f32_16x16x32_bf16 v[224:227],a[96:99],v[92:95],v[224:227]\n"
"ds_read_b128 a[72:75],v113 offset:2048\n"
"v_cvt_pk_bf16_f32 v72,v122,v123\n"
"v_mfma_f32_16x16x32_bf16 a[140:143],a[144:147],v[92:95],a[140:143]\n"
"v_exp_f32 v120,v19\n"
"v_mfma_f32_16x16x32_bf16 v[228:231],a[100:103],v[92:95],v[228:231]\n"
"ds_read_b128 a[76:79],v113 offset:3072\n"
"ds_read_b128 a[80:83],v113 offset:4096\n"
"v_mfma_f32_16x16x32_bf16 v[132:135],a[100:103],v[80:83],v[132:135]\n"
"v_exp_f32 v122,v20\n"
"v_mfma_f32_16x16x32_bf16 v[164:167],a[100:103],v[84:87],v[164:167]\n"
"ds_read_b128 a[84:87],v113 offset:5120\n"
"v_cvt_pk_bf16_f32 v73,v119,v120\n"
"v_mfma_f32_16x16x32_bf16 v[196:199],a[100:103],v[88:91],v[196:199]\n"
"v_exp_f32 v123,v21\n"
"v_mfma_f32_16x16x32_bf16 a[136:139],a[144:147],v[88:91],a[136:139]\n"
"ds_read_b128 a[88:91],v113 offset:6144\n"
"ds_read_b128 a[92:95],v113 offset:7168\n"
"v_mfma_f32_16x16x32_bf16 v[200:203],a[104:107],v[88:91],v[200:203]\n"
"v_exp_f32 v119,v22\n"
"v_mfma_f32_16x16x32_bf16 v[232:235],a[104:107],v[92:95],v[232:235]\n"
"buffer_store_dwordx4 a[188:191],v112,s[24:27],s3 offen offset:3072 sc1\n"
"v_mfma_f32_16x16x32_bf16 v[136:139],a[104:107],v[80:83],v[136:139]\n"
"v_exp_f32 v120,v23\n"
"v_mfma_f32_16x16x32_bf16 v[168:171],a[104:107],v[84:87],v[168:171]\n"
"s_or_b32 s3,s3,0x3c000\n"
"v_cvt_pk_bf16_f32 v74,v122,v123\n"
"v_cvt_pk_bf16_f32 v75,v119,v120\n"
"v_mfma_f32_16x16x32_bf16 a[132:135],a[144:147],v[84:87],a[132:135]\n"
"v_exp_f32 v122,v24\n"
"v_mfma_f32_16x16x32_bf16 v[172:175],a[108:111],v[84:87],v[172:175]\n"
"v_or3_b32 v118,v118,v71,v72\n"
"v_or3_b32 v118,v118,v73,v74\n"
"v_mfma_f32_16x16x32_bf16 v[204:207],a[108:111],v[88:91],v[204:207]\n"
"v_exp_f32 v123,v25\n"
"v_mfma_f32_16x16x32_bf16 v[236:239],a[108:111],v[92:95],v[236:239]\n"
"v_cvt_pk_bf16_f32 v76,v122,v123\n"
"v_or3_b32 v118,v118,v75,v76\n"
"v_mfma_f32_16x16x32_bf16 v[140:143],a[108:111],v[80:83],v[140:143]\n"
"v_exp_f32 v119,v26\n"
"v_mfma_f32_16x16x32_bf16 a[128:131],a[144:147],v[80:83],a[128:131]\n"
"v_mfma_f32_16x16x32_bf16 v[144:147],a[112:115],v[80:83],v[144:147]\n"
"v_exp_f32 v120,v27\n"
"v_mfma_f32_16x16x32_bf16 v[176:179],a[112:115],v[84:87],v[176:179]\n"
"v_cvt_pk_bf16_f32 v77,v119,v120\n"
"v_mfma_f32_16x16x32_bf16 v[208:211],a[112:115],v[88:91],v[208:211]\n"
"v_exp_f32 v122,v28\n"
"v_mfma_f32_16x16x32_bf16 v[240:243],a[112:115],v[92:95],v[240:243]\n"
"v_mfma_f32_16x16x32_bf16 v[244:247],a[116:119],v[92:95],v[244:247]\n"
"v_exp_f32 v123,v29\n"
"v_mfma_f32_16x16x32_bf16 v[148:151],a[116:119],v[80:83],v[148:151]\n"
"v_cvt_pk_bf16_f32 v78,v122,v123\n"
"v_or3_b32 v118,v118,v77,v78\n"
"v_mfma_f32_16x16x32_bf16 v[180:183],a[116:119],v[84:87],v[180:183]\n"
"v_exp_f32 v119,v30\n"
"v_mfma_f32_16x16x32_bf16 v[212:215],a[116:119],v[88:91],v[212:215]\n"
"buffer_store_dwordx4 a[236:239],v112,s[24:27],s56 offen offset:3072 sc1\n"
"v_mfma_f32_16x16x32_bf16 v[216:219],a[120:123],v[88:91],v[216:219]\n"
"v_exp_f32 v120,v31\n"
"v_mfma_f32_16x16x32_bf16 v[248:251],a[120:123],v[92:95],v[248:251]\n"
"v_cvt_pk_bf16_f32 v79,v119,v120\n"
"v_or_b32 v118,v118,v79\n"
"v_mfma_f32_16x16x32_bf16 v[152:155],a[120:123],v[80:83],v[152:155]\n"
"v_and_b32 v118,0x40004000,v118\n"
"v_cmp_ne_u32 vcc,0,v118\n"
"v_mfma_f32_16x16x32_bf16 v[184:187],a[120:123],v[84:87],v[184:187]\n"
"v_mfma_f32_16x16x32_bf16 v[188:191],a[124:127],v[84:87],v[188:191]\n"
"v_mfma_f32_16x16x32_bf16 v[220:223],a[124:127],v[88:91],v[220:223]\n"
"v_mfma_f32_16x16x32_bf16 v[252:255],a[124:127],v[92:95],v[252:255]\n"
"v_mfma_f32_16x16x32_bf16 v[156:159],a[124:127],v[80:83],v[156:159]\n"
"s_cbranch_vccnz .Lfk_cs_k2\n"
".Lfk_cr_k2:\n"
"s_waitcnt vmcnt(13) lgkmcnt(0)\n"
"s_barrier\n"
"s_branch .Lfk_g1\n"
".Lfk_winA:\n"
"v_max3_f32 v118,v32,v33,v34\n"
"v_max3_f32 v119,v35,v36,v37\n"
"v_max3_f32 v119,v119,v38,v39\n"
"v_max3_f32 v118,v118,v40,v41\n"
"v_max3_f32 v119,v119,v42,v43\n"
"v_max3_f32 v118,v118,v44,v45\n"
"v_max3_f32 v119,v119,v46,v47\n"
"v_max3_f32 v118,v118,v48,v49\n"
"v_max3_f32 v119,v119,v50,v51\n"
"v_max3_f32 v118,v118,v52,v53\n"
"v_max3_f32 v119,v119,v54,v55\n"
"v_max3_f32 v118,v118,v56,v57\n"
"v_max3_f32 v119,v119,v58,v59\n"
"v_max3_f32 v118,v118,v60,v61\n"
"v_max3_f32 v119,v119,v62,v63\n"
"v_max_f32 v118,v118,v119\n"
"v_cmp_lt_f32 vcc,s93,v118\n"
"s_nop 0\n"
"s_cbranch_vccnz .Lfk_cs_x2\n"
".Lfk_cr_x2:\n"
"s_mov_b32 s64,s61\n"
"s_cmp_eq_u32 s52,3\n"
"s_cselect_b32 s67,172032,0\n"
"v_mfma_f32_16x16x32_bf16 v[8:11],a[64:67],a[16:19],v[100:103]\n"
"s_swappc_b64 s[94:95],s[20:21]\n"
"ds_read_b128 a[96:99],v114 offset:24576\n"
"ds_read_b128 a[100:103],v114 offset:25600\n"
"v_mfma_f32_16x16x32_bf16 v[8:11],a[68:71],a[20:23],v[8:11]\n"
"ds_read_b128 a[104:107],v114 offset:26624\n"
"ds_read_b128 a[108:111],v114 offset:27648\n"
"v_mfma_f32_16x16x32_bf16 v[8:11],a[72:75],a[24:27],v[8:11]\n"
"ds_read_b128 a[112:115],v114 offset:28672\n"
"ds_read_b128 a[116:119],v114 offset:29696\n"
"v_mfma_f32_16x16x32_bf16 v[8:11],a[76:79],a[28:31],v[8:11]\n"
"ds_read_b128 a[120:123],v114 offset:30720\n"
"ds_read_b128 a[124:127],v114 offset:31744\n"
"v_mfma_f32_16x16x32_bf16 v[12:15],a[92:95],a[28:31],v[100:103]\n"
"v_add_f32 v0,0x80000000,v126\n"
"v_min_f32 v0,v0,v96\n"
"v_mfma_f32_16x16x32_bf16 v[12:15],a[88:91],a[24:27],v[12:15]\n"
"v_add_f32 v1,0xf149f2ca,v126\n"
"v_min_f32 v1,v1,v97\n"
"v_mfma_f32_16x16x32_bf16 v[12:15],a[84:87],a[20:23],v[12:15]\n"
"v_add_f32 v2,0xf1c9f2ca,v126\n"
"v_min_f32 v2,v2,v98\n"
"v_mfma_f32_16x16x32_bf16 v[12:15],a[80:83],a[16:19],v[12:15]\n"
"v_add_f32 v3,0xf2177617,v126\n"
"v_min_f32 v3,v3,v99\n"
"v_mfma_f32_16x16x32_bf16 v[20:23],a[80:83],a[32:35],v[104:107]\n"
"v_add_f32 v4,0xf349f2ca,v126\n"
"v_min_f32 v4,v4,v96\n"
"v_mfma_f32_16x16x32_bf16 v[20:23],a[84:87],a[36:39],v[20:23]\n"
"v_add_f32 v5,0xf35691f6,v126\n"
"v_min_f32 v5,v5,v97\n"
"v_mfma_f32_16x16x32_bf16 v[20:23],a[88:91],a[40:43],v[20:23]\n"
"v_add_f32 v6,0xf3633123,v126\n"
"v_min_f32 v6,v6,v98\n"
"v_mfma_f32_16x16x32_bf16 v[20:23],a[92:95],a[44:47],v[20:23]\n"
"v_add_f32 v7,0xf36fd050,v126\n"
"v_min_f32 v7,v7,v99\n"
"v_mfma_f32_16x16x32_bf16 v[16:19],a[76:79],a[44:47],v[104:107]\n"
"s_add_u32 s101,s101,4\n"
"v_exp_f32 v32,v32\n"
"v_mfma_f32_16x16x32_bf16 v[16:19],a[72:75],a[40:43],v[16:19]\n"
"s_add_u32 m0,s67,0\n"
"v_exp_f32 v33,v33\n"
"v_mfma_f32_16x16x32_bf16 v[16:19],a[68:71],a[36:39],v[16:19]\n"
"v_exp_f32 v34,v34\n"
"v_mfma_f32_16x16x32_bf16 v[16:19],a[64:67],a[32:35],v[16:19]\n"
"v_exp_f32 v35,v35\n"
"v_mfma_f32_16x16x32_bf16 v[24:27],a[64:67],a[48:51],v[108:111]\n"
"v_exp_f32 v36,v36\n"
"v_mfma_f32_16x16x32_bf16 v[24:27],a[68:71],a[52:55],v[24:27]\n"
"v_exp_f32 v37,v37\n"
"v_mfma_f32_16x16x32_bf16 v[24:27],a[72:75],a[56:59],v[24:27]\n"
"v_exp_f32 v38,v38\n"
"v_mfma_f32_16x16x32_bf16 v[24:27],a[76:79],a[60:63],v[24:27]\n"
"v_exp_f32 v39,v39\n"
"v_mfma_f32_16x16x32_bf16 v[28:31],a[92:95],a[60:63],v[108:111]\n"
"v_exp_f32 v40,v40\n"
"v_mfma_f32_16x16x32_bf16 v[28:31],a[88:91],a[56:59],v[28:31]\n"
"v_exp_f32 v41,v41\n"
"v_mfma_f32_16x16x32_bf16 v[28:31],a[84:87],a[52:55],v[28:31]\n"
"v_exp_f32 v42,v42\n"
"v_mfma_f32_16x16x32_bf16 v[28:31],a[80:83],a[48:51],v[28:31]\n"
"v_exp_f32 v43,v43\n"
"v_mfma_f32_16x16x32_bf16 v[4:7],a[80:83],a[0:3],v[4:7]\n"
"v_exp_f32 v44,v44\n"
"v_mfma_f32_16x16x32_bf16 v[4:7],a[84:87],a[4:7],v[4:7]\n"
"v_exp_f32 v45,v45\n"
"v_mfma_f32_16x16x32_bf16 v[4:7],a[88:91],a[8:11],v[4:7]\n"
"buffer_load_dwordx4 v112,s[24:27],s64 offen offset:0 sc1 lds\n"
"v_mfma_f32_16x16x32_bf16 v[4:7],a[92:95],a[12:15],v[4:7]\n"
"v_exp_f32 v46,v46\n"
"v_mfma_f32_16x16x32_bf16 v[0:3],a[76:79],a[12:15],v[0:3]\n"
"buffer_load_dwordx4 v112,s[24:27],s64 offen offset:1024 sc1 lds\n"
"v_mfma_f32_16x16x32_bf16 v[0:3],a[72:75],a[8:11],v[0:3]\n"
"v_exp_f32 v47,v47\n"
"v_mfma_f32_16x16x32_bf16 v[0:3],a[68:71],a[4:7],v[0:3]\n"
"buffer_load_dwordx4 v112,s[24:27],s64 offen offset:2048 sc1 lds\n"
"v_mfma_f32_16x16x32_bf16 v[0:3],a[64:67],a[0:3],v[0:3]\n"
"s_waitcnt lgkmcnt(0)\n"
"v_exp_f32 v48,v48\n"
"v_mfma_f32_16x16x32_bf16 v[128:131],a[96:99],v[64:67],v[128:131]\n"
"v_exp_f32 v49,v49\n"
"v_mfma_f32_16x16x32_bf16 v[160:163],a[96:99],v[68:71],v[160:163]\n"
"ds_read_b128 a[64:67],v113 offset:16384\n"
"ds_read_b128 a[68:71],v113 offset:17408\n"
"v_mfma_f32_16x16x32_bf16 v[192:195],a[96:99],v[72:75],v[192:195]\n"
"v_exp_f32 v50,v50\n"
"v_mfma_f32_16x16x32_bf16 v[224:227],a[96:99],v[76:79],v[224:227]\n"
"ds_read_b128 a[72:75],v113 offset:18432\n"
"ds_read_b128 a[76:79],v113 offset:19456\n"
"v_mfma_f32_16x16x32_bf16 a[140:143],a[144:147],v[76:79],a[140:143]\n"
"v_exp_f32 v51,v51\n"
"v_mfma_f32_16x16x32_bf16 v[228:231],a[100:103],v[76:79],v[228:231]\n"
"ds_read_b128 a[80:83],v113 offset:20480\n"
"ds_read_b128 a[84:87],v113 offset:21504\n"
"v_mfma_f32_16x16x32_bf16 v[132:135],a[100:103],v[64:67],v[132:135]\n"
"v_exp_f32 v52,v52\n"
"v_mfma_f32_16x16x32_bf16 v[164:167],a[100:103],v[68:71],v[164:167]\n"
"ds_read_b128 a[88:91],v113 offset:22528\n"
"ds_read_b128 a[92:95],v113 offset:23552\n"
"v_mfma_f32_16x16x32_bf16 v[196:199],a[100:103],v[72:75],v[196:199]\n"
"v_exp_f32 v53,v53\n"
"v_mfma_f32_16x16x32_bf16 a[136:139],a[144:147],v[72:75],a[136:139]\n"
"s_waitcnt vmcnt(14)\n"
"ds_add_u32 v124,a152\n"
"v_mfma_f32_16x16x32_bf16 v[200:203],a[104:107],v[72:75],v[200:203]\n"
"v_exp_f32 v54,v54\n"
"v_mfma_f32_16x16x32_bf16 v[232:235],a[104:107],v[76:79],v[232:235]\n"
"buffer_load_dwordx4 v112,s[24:27],s64 offen offset:3072 sc1 lds\n"
"v_mfma_f32_16x16x32_bf16 v[136:139],a[104:107],v[64:67],v[136:139]\n"
"s_add_u32 m0,s67,4096\n"
"v_exp_f32 v55,v55\n"
"v_mfma_f32_16x16x32_bf16 v[168:171],a[104:107],v[68:71],v[168:171]\n"
"buffer_load_dwordx4 v117,s[24:27],s64 offen offset:0 sc1 lds\n"
"v_mfma_f32_16x16x32_bf16 a[132:135],a[144:147],v[68:71],a[132:135]\n"
"v_exp_f32 v56,v56\n"
"v_mfma_f32_16x16x32_bf16 v[172:175],a[108:111],v[68:71],v[172:175]\n"
"buffer_load_dwordx4 v117,s[24:27],s64 offen offset:1024 sc1 lds\n"
"v_mfma_f32_16x16x32_bf16 v[204:207],a[108:111],v[72:75],v[204:207]\n"
"v_exp_f32 v57,v57\n"
"v_mfma_f32_16x16x32_bf16 v[236:239],a[108:111],v[76:79],v[236:239]\n"
"buffer_load_dwordx4 v117,s[24:27],s64 offen offset:2048 sc1 lds\n"
"v_mfma_f32_16x16x32_bf16 v[140:143],a[108:111],v[64:67],v[140:143]\n"
"v_exp_f32 v58,v58\n"
"v_mfma_f32_16x16x32_bf16 a[128:131],a[144:147],v[64:67],a[128:131]\n"
"v_cvt_pk_bf16_f32 v80,v32,v33\n"
"v_cvt_pk_bf16_f32 v81,v34,v35\n"
"v_mfma_f32_16x16x32_bf16 v[144:147],a[112:115],v[64:67],v[144:147]\n"
"v_exp_f32 v59,v59\n"
"v_mfma_f32_16x16x32_bf16 v[176:179],a[112:115],v[68:71],v[176:179]\n"
"v_cvt_pk_bf16_f32 v82,v36,v37\n"
"v_cvt_pk_bf16_f32 v83,v38,v39\n"
"v_mfma_f32_16x16x32_bf16 v[208:211],a[112:115],v[72:75],v[208:211]\n"
"v_exp_f32 v60,v60\n"
"v_mfma_f32_16x16x32_bf16 v[240:243],a[112:115],v[76:79],v[240:243]\n"
"buffer_load_dwordx4 v117,s[24:27],s64 offen offset:3072 sc1 lds\n"
"v_mfma_f32_16x16x32_bf16 v[244:247],a[116:119],v[76:79],v[244:247]\n"
"s_add_u32 s64,s64,16384\n"
"v_exp_f32 v61,v61\n"
"v_mfma_f32_16x16x32_bf16 v[148:151],a[116:119],v[64:67],v[148:151]\n"
"s_andn2_b32 m0,s55,16384\n"
"s_add_u32 s54,s54,s22\n"
"v_cvt_pk_bf16_f32 v85,v42,v43\n"
"v_mfma_f32_16x16x32_bf16 v[180:183],a[116:119],v[68:71],v[180:183]\n"
"v_exp_f32 v62,v62\n"
"v_mfma_f32_16x16x32_bf16 v[212:215],a[116:119],v[72:75],v[212:215]\n"
"ds_read_b32 v125,v124\n"
"v_cvt_pk_bf16_f32 v84,v40,v41\n"
"v_mfma_f32_16x16x32_bf16 v[216:219],a[120:123],v[72:75],v[216:219]\n"
"v_exp_f32 v63,v63\n"
"v_mfma_f32_16x16x32_bf16 v[248:251],a[120:123],v[76:79],v[248:251]\n"
"buffer_load_dwordx4 v115,s[32:35],s54 offen sc1 lds\n"
"v_mfma_f32_16x16x32_bf16 v[152:155],a[120:123],v[64:67],v[152:155]\n"
"buffer_store_dwordx4 a[240:243],v112,s[24:27],s56 offen offset:0 sc1\n"
"v_mfma_f32_16x16x32_bf16 v[184:187],a[120:123],v[68:71],v[184:187]\n"
"v_cvt_pk_bf16_f32 v86,v44,v45\n"
"v_cvt_pk_bf16_f32 v87,v46,v47\n"
"v_mfma_f32_16x16x32_bf16 v[188:191],a[124:127],v[68:71],v[188:191]\n"
"s_waitcnt lgkmcnt(0)\n"
"v_cvt_pk_bf16_f32 v88,v48,v49\n"
"v_cvt_pk_bf16_f32 v89,v50,v51\n"
"v_mfma_f32_16x16x32_bf16 v[220:223],a[124:127],v[72:75],v[220:223]\n"
"v_cvt_pk_bf16_f32 v90,v52,v53\n"
"v_cvt_pk_bf16_f32 v91,v54,v55\n"
"v_mfma_f32_16x16x32_bf16 v[252:255],a[124:127],v[76:79],v[252:255]\n"
"v_cvt_pk_bf16_f32 v92,v56,v57\n"
"v_cvt_pk_bf16_f32 v93,v58,v59\n"
"v_mfma_f32_16x16x32_bf16 v[156:159],a[124:127],v[64:67],v[156:159]\n"
"v_cvt_pk_bf16_f32 v94,v60,v61\n"
"v_cvt_pk_bf16_f32 v95,v62,v63\n"
"v_max3_f32 v118,v8,v9,v10\n"
"v_max3_f32 v118,v118,v11,v12\n"
"v_max3_f32 v118,v118,v13,v14\n"
"v_max3_f32 v118,v118,v15,v20\n"
"v_max3_f32 v118,v118,v21,v22\n"
"v_max3_f32 v118,v118,v23,v16\n"
"v_max3_f32 v118,v118,v17,v18\n"
"v_max3_f32 v118,v118,v19,v24\n"
"v_max3_f32 v118,v118,v25,v26\n"
"v_max3_f32 v118,v118,v27,v28\n"
"v_max3_f32 v118,v118,v29,v30\n"
"v_max3_f32 v118,v118,v31,v4\n"
"v_max3_f32 v118,v118,v5,v6\n"
"v_max3_f32 v118,v118,v7,v0\n"
"v_max3_f32 v118,v118,v1,v2\n"
"v_max_f32 v118,v118,v3\n"
"v_cmp_lt_f32 vcc,s93,v118\n"
"s_nop 0\n"
"s_cbranch_vccnz .Lfk_cs_u3\n"
".Lfk_cr_u3:\n"
"v_cmp_gt_u32 vcc,s101,v125\n"
"s_nop 0\n"
"s_cbranch_vccnz .Lfk_cs_mu3\n"
".Lfk_cr_mu3:\n"
"v_mfma_f32_16x16x32_bf16 v[48:51],a[64:67],a[32:35],v[104:107]\n"
"ds_read_b128 a[96:99],v114 offset:57344\n"
"ds_read_b128 a[100:103],v114 offset:58368\n"
"v_mfma_f32_16x16x32_bf16 v[48:51],a[68:71],a[36:39],v[48:51]\n"
"ds_read_b128 a[104:107],v114 offset:59392\n"
"ds_read_b128 a[108:111],v114 offset:60416\n"
"v_mfma_f32_16x16x32_bf16 v[48:51],a[72:75],a[40:43],v[48:51]\n"
"ds_read_b128 a[112:115],v114 offset:61440\n"
"ds_read_b128 a[116:119],v114 offset:62464\n"
"v_mfma_f32_16x16x32_bf16 v[48:51],a[76:79],a[44:47],v[48:51]\n"
"ds_read_b128 a[120:123],v114 offset:63488\n"
"ds_read_b128 a[124:127],v114 offset:64512\n"
"v_mfma_f32_16x16x32_bf16 v[52:55],a[92:95],a[44:47],v[104:107]\n"
"ds_read_b128 a[0:3],v116 offset:40960\n"
"ds_read_b128 a[4:7],v116 offset:41984\n"
"v_mfma_f32_16x16x32_bf16 v[52:55],a[88:91],a[40:43],v[52:55]\n"
"ds_read_b128 a[8:11],v116 offset:43008\n"
"ds_read_b128 a[12:15],v116 offset:44032\n"
"v_mfma_f32_16x16x32_bf16 v[52:55],a[84:87],a[36:39],v[52:55]\n"
"v_add_f32 v40,0x80000000,v126\n"
"v_min_f32 v40,v40,v100\n"
"v_mfma_f32_16x16x32_bf16 v[52:55],a[80:83],a[32:35],v[52:55]\n"
"v_add_f32 v41,0xf149f2ca,v126\n"
"v_min_f32 v41,v41,v101\n"
"v_mfma_f32_16x16x32_bf16 v[60:63],a[80:83],a[48:51],v[108:111]\n"
"v_add_f32 v42,0xf1c9f2ca,v126\n"
"v_min_f32 v42,v42,v102\n"
"v_mfma_f32_16x16x32_bf16 v[60:63],a[84:87],a[52:55],v[60:63]\n"
"v_add_f32 v43,0xf2177617,v126\n"
"v_min_f32 v43,v43,v103\n"
"v_mfma_f32_16x16x32_bf16 v[60:63],a[88:91],a[56:59],v[60:63]\n"
"v_add_f32 v44,0xf349f2ca,v126\n"
"v_min_f32 v44,v44,v100\n"
"v_mfma_f32_16x16x32_bf16 v[60:63],a[92:95],a[60:63],v[60:63]\n"
"v_add_f32 v45,0xf35691f6,v126\n"
"v_min_f32 v45,v45,v101\n"
"v_mfma_f32_16x16x32_bf16 v[56:59],a[76:79],a[60:63],v[108:111]\n"
"v_add_f32 v46,0xf3633123,v126\n"
"v_min_f32 v46,v46,v102\n"
"v_mfma_f32_16x16x32_bf16 v[56:59],a[72:75],a[56:59],v[56:59]\n"
"v_add_f32 v47,0xf36fd050,v126\n"
"v_min_f32 v47,v47,v103\n"
"v_mfma_f32_16x16x32_bf16 v[56:59],a[68:71],a[52:55],v[56:59]\n"
"s_add_u32 s101,s101,4\n"
"v_exp_f32 v0,v0\n"
"v_mfma_f32_16x16x32_bf16 v[56:59],a[64:67],a[48:51],v[56:59]\n"
"s_add_u32 m0,s55,4096\n"
"v_exp_f32 v1,v1\n"
"v_mfma_f32_16x16x32_bf16 v[40:43],a[64:67],a[16:19],v[40:43]\n"
"s_add_u32 s54,s54,s22\n"
"v_exp_f32 v2,v2\n"
"v_mfma_f32_16x16x32_bf16 v[40:43],a[68:71],a[20:23],v[40:43]\n"
"v_exp_f32 v3,v3\n"
"v_mfma_f32_16x16x32_bf16 v[40:43],a[72:75],a[24:27],v[40:43]\n"
"v_exp_f32 v4,v4\n"
"v_mfma_f32_16x16x32_bf16 v[40:43],a[76:79],a[28:31],v[40:43]\n"
"v_exp_f32 v5,v5\n"
"v_mfma_f32_16x16x32_bf16 v[44:47],a[92:95],a[28:31],v[44:47]\n"
"v_exp_f32 v6,v6\n"
"v_mfma_f32_16x16x32_bf16 v[44:47],a[88:91],a[24:27],v[44:47]\n"
"v_exp_f32 v7,v7\n"
"v_mfma_f32_16x16x32_bf16 v[44:47],a[84:87],a[20:23],v[44:47]\n"
"v_exp_f32 v8,v8\n"
"v_mfma_f32_16x16x32_bf16 v[44:47],a[80:83],a[16:19],v[44:47]\n"
"s_waitcnt lgkmcnt(4)\n"
"v_exp_f32 v9,v9\n"
"v_mfma_f32_16x16x32_bf16 v[128:131],a[96:99],v[80:83],v[128:131]\n"
"v_exp_f32 v10,v10\n"
"v_mfma_f32_16x16x32_bf16 v[160:163],a[96:99],v[84:87],v[160:163]\n"
"v_exp_f32 v11,v11\n"
"v_mfma_f32_16x16x32_bf16 v[192:195],a[96:99],v[88:91],v[192:195]\n"
"v_exp_f32 v12,v12\n"
"v_mfma_f32_16x16x32_bf16 v[224:227],a[96:99],v[92:95],v[224:227]\n"
"v_exp_f32 v13,v13\n"
"v_mfma_f32_16x16x32_bf16 a[140:143],a[144:147],v[92:95],a[140:143]\n"
"v_exp_f32 v14,v14\n"
"v_mfma_f32_16x16x32_bf16 v[228:231],a[100:103],v[92:95],v[228:231]\n"
"v_exp_f32 v15,v15\n"
"v_mfma_f32_16x16x32_bf16 v[132:135],a[100:103],v[80:83],v[132:135]\n"
"v_exp_f32 v16,v16\n"
"v_mfma_f32_16x16x32_bf16 v[164:167],a[100:103],v[84:87],v[164:167]\n"
"v_exp_f32 v17,v17\n"
"v_mfma_f32_16x16x32_bf16 v[196:199],a[100:103],v[88:91],v[196:199]\n"
"v_exp_f32 v18,v18\n"
"v_mfma_f32_16x16x32_bf16 a[136:139],a[144:147],v[88:91],a[136:139]\n"
"v_exp_f32 v19,v19\n"
"v_mfma_f32_16x16x32_bf16 v[200:203],a[104:107],v[88:91],v[200:203]\n"
"v_exp_f32 v20,v20\n"
"v_mfma_f32_16x16x32_bf16 v[232:235],a[104:107],v[92:95],v[232:235]\n"
"v_exp_f32 v21,v21\n"
"v_mfma_f32_16x16x32_bf16 v[136:139],a[104:107],v[80:83],v[136:139]\n"
"ds_read_b128 a[64:67],v113 offset:49152\n"
"ds_read_b128 a[68:71],v113 offset:50176\n"
"v_mfma_f32_16x16x32_bf16 v[168:171],a[104:107],v[84:87],v[168:171]\n"
"v_exp_f32 v22,v22\n"
"v_mfma_f32_16x16x32_bf16 a[132:135],a[144:147],v[84:87],a[132:135]\n"
"v_exp_f32 v23,v23\n"
"v_mfma_f32_16x16x32_bf16 v[172:175],a[108:111],v[84:87],v[172:175]\n"
"ds_read_b128 a[72:75],v113 offset:51200\n"
"ds_read_b128 a[76:79],v113 offset:52224\n"
"v_mfma_f32_16x16x32_bf16 v[204:207],a[108:111],v[88:91],v[204:207]\n"
"v_exp_f32 v24,v24\n"
"v_mfma_f32_16x16x32_bf16 v[236:239],a[108:111],v[92:95],v[236:239]\n"
"ds_read_b128 a[80:83],v113 offset:53248\n"
"ds_read_b128 a[84:87],v113 offset:54272\n"
"v_mfma_f32_16x16x32_bf16 v[140:143],a[108:111],v[80:83],v[140:143]\n"
"v_exp_f32 v25,v25\n"
"v_mfma_f32_16x16x32_bf16 a[128:131],a[144:147],v[80:83],a[128:131]\n"
"v_exp_f32 v26,v26\n"
"v_mfma_f32_16x16x32_bf16 v[144:147],a[112:115],v[80:83],v[144:147]\n"
"ds_read_b128 a[88:91],v113 offset:55296\n"
"ds_read_b128 a[92:95],v113 offset:56320\n"
"v_mfma_f32_16x16x32_bf16 v[176:179],a[112:115],v[84:87],v[176:179]\n"
"v_exp_f32 v27,v27\n"
"v_mfma_f32_16x16x32_bf16 v[208:211],a[112:115],v[88:91],v[208:211]\n"
"s_waitcnt vmcnt(11)\n"
"ds_add_u32 v124,a152\n"
"v_mfma_f32_16x16x32_bf16 v[240:243],a[112:115],v[92:95],v[240:243]\n"
"v_exp_f32 v28,v28\n"
"v_mfma_f32_16x16x32_bf16 v[244:247],a[116:119],v[92:95],v[244:247]\n"
"v_exp_f32 v29,v29\n"
"v_mfma_f32_16x16x32_bf16 v[148:151],a[116:119],v[80:83],v[148:151]\n"
"buffer_load_dwordx4 v115,s[32:35],s54 offen sc1 lds\n"
"v_mfma_f32_16x16x32_bf16 v[180:183],a[116:119],v[84:87],v[180:183]\n"
"s_add_u32 m0,s67,16384\n"
"v_exp_f32 v30,v30\n"
"v_mfma_f32_16x16x32_bf16 v[212:215],a[116:119],v[88:91],v[212:215]\n"
"ds_read_b32 v125,v124\n"
"v_cvt_pk_bf16_f32 v64,v0,v1\n"
"v_mfma_f32_16x16x32_bf16 v[216:219],a[120:123],v[88:91],v[216:219]\n"
"v_exp_f32 v31,v31\n"
"v_mfma_f32_16x16x32_bf16 v[248:251],a[120:123],v[92:95],v[248:251]\n"
"buffer_load_dwordx4 v112,s[24:27],s64 offen offset:0 sc1 lds\n"
"v_mfma_f32_16x16x32_bf16 v[152:155],a[120:123],v[80:83],v[152:155]\n"
"buffer_load_dwordx4 v112,s[24:27],s64 offen offset:1024 sc1 lds\n"
"v_mfma_f32_16x16x32_bf16 v[184:187],a[120:123],v[84:87],v[184:187]\n"
"buffer_load_dwordx4 v112,s[24:27],s64 offen offset:2048 sc1 lds\n"
"v_mfma_f32_16x16x32_bf16 v[188:191],a[124:127],v[84:87],v[188:191]\n"
"s_waitcnt lgkmcnt(0)\n"
"buffer_load_dwordx4 v112,s[24:27],s64 offen offset:3072 sc1 lds\n"
"v_mfma_f32_16x16x32_bf16 v[220:223],a[124:127],v[88:91],v[220:223]\n"
"s_add_u32 m0,s67,20480\n"
"v_cvt_pk_bf16_f32 v65,v2,v3\n"
"v_cvt_pk_bf16_f32 v66,v4,v5\n"
"v_mfma_f32_16x16x32_bf16 v[252:255],a[124:127],v[92:95],v[252:255]\n"
"buffer_load_dwordx4 v117,s[24:27],s64 offen offset:0 sc1 lds\n"
"v_mfma_f32_16x16x32_bf16 v[156:159],a[124:127],v[80:83],v[156:159]\n"
"buffer_load_dwordx4 v117,s[24:27],s64 offen offset:1024 sc1 lds\n"
"buffer_load_dwordx4 v117,s[24:27],s64 offen offset:2048 sc1 lds\n"
"buffer_load_dwordx4 v117,s[24:27],s64 offen offset:3072 sc1 lds\n"
"buffer_store_dwordx4 a[244:247],v112,s[24:27],s56 offen offset:1024 sc1\n"
"s_add_u32 s64,s64,16384\n"
"v_cvt_pk_bf16_f32 v67,v6,v7\n"
"v_cvt_pk_bf16_f32 v68,v8,v9\n"
"v_cvt_pk_bf16_f32 v69,v10,v11\n"
"v_cvt_pk_bf16_f32 v70,v12,v13\n"
"v_cvt_pk_bf16_f32 v71,v14,v15\n"
"v_cvt_pk_bf16_f32 v72,v16,v17\n"
"v_cvt_pk_bf16_f32 v73,v18,v19\n"
"v_cvt_pk_bf16_f32 v74,v20,v21\n"
"v_cvt_pk_bf16_f32 v75,v22,v23\n"
"v_cvt_pk_bf16_f32 v76,v24,v25\n"
"v_cvt_pk_bf16_f32 v77,v26,v27\n"
"v_cvt_pk_bf16_f32 v78,v28,v29\n"
"v_cvt_pk_bf16_f32 v79,v30,v31\n"
"v_max3_f32 v118,v48,v49,v50\n"
"v_max3_f32 v118,v118,v51,v52\n"
"v_max3_f32 v118,v118,v53,v54\n"
"v_max3_f32 v118,v118,v55,v60\n"
"v_max3_f32 v118,v118,v61,v62\n"
"v_max3_f32 v118,v118,v63,v56\n"
"v_max3_f32 v118,v118,v57,v58\n"
"v_max3_f32 v118,v118,v59,v40\n"
"v_max3_f32 v118,v118,v41,v42\n"
"v_max3_f32 v118,v118,v43,v44\n"
"v_max3_f32 v118,v118,v45,v46\n"
"v_max_f32 v118,v118,v47\n"
"v_cmp_lt_f32 vcc,s93,v118\n"
"s_nop 0\n"
"s_cbranch_vccnz .Lfk_cs_u0\n"
".Lfk_cr_u0:\n"
"v_cmp_gt_u32 vcc,s101,v125\n"
"s_nop 0\n"
"s_cbranch_vccnz .Lfk_cs_mu0\n"
".Lfk_cr_mu0:\n"
"v_mfma_f32_16x16x32_bf16 v[24:27],a[64:67],a[48:51],v[108:111]\n"
"ds_read_b128 a[96:99],v114 offset:40960\n"
"ds_read_b128 a[100:103],v114 offset:41984\n"
"v_mfma_f32_16x16x32_bf16 v[24:27],a[68:71],a[52:55],v[24:27]\n"
"ds_read_b128 a[104:107],v114 offset:43008\n"
"ds_read_b128 a[108:111],v114 offset:44032\n"
"v_mfma_f32_16x16x32_bf16 v[24:27],a[72:75],a[56:59],v[24:27]\n"
"ds_read_b128 a[112:115],v114 offset:45056\n"
"ds_read_b128 a[116:119],v114 offset:46080\n"
"v_mfma_f32_16x16x32_bf16 v[24:27],a[76:79],a[60:63],v[24:27]\n"
"ds_read_b128 a[120:123],v114 offset:47104\n"
"ds_read_b128 a[124:127],v114 offset:48128\n"
"v_mfma_f32_16x16x32_bf16 v[28:31],a[92:95],a[60:63],v[108:111]\n"
"ds_read_b128 a[16:19],v116 offset:8192\n"
"ds_read_b128 a[20:23],v116 offset:9216\n"
"v_mfma_f32_16x16x32_bf16 v[28:31],a[88:91],a[56:59],v[28:31]\n"
"ds_read_b128 a[24:27],v116 offset:10240\n"
"ds_read_b128 a[28:31],v116 offset:11264\n"
"v_mfma_f32_16x16x32_bf16 v[28:31],a[84:87],a[52:55],v[28:31]\n"
"v_add_f32 v16,0x80000000,v126\n"
"v_min_f32 v16,v16,v104\n"
"v_mfma_f32_16x16x32_bf16 v[28:31],a[80:83],a[48:51],v[28:31]\n"
"v_add_f32 v17,0xf149f2ca,v126\n"
"v_add_f32 v18,0xf1c9f2ca,v126\n"
"v_add_f32 v19,0xf2177617,v126\n"
"v_min_f32 v19,v19,v107\n"
"v_add_f32 v20,0xf349f2ca,v126\n"
"v_min_f32 v20,v20,v104\n"
"v_add_f32 v21,0xf35691f6,v126\n"
"v_min_f32 v21,v21,v105\n"
"v_add_f32 v22,0xf3633123,v126\n"
"v_min_f32 v22,v22,v106\n"
"v_add_f32 v23,0xf36fd050,v126\n"
"v_min_f32 v23,v23,v107\n"
"v_min_f32 v18,v18,v106\n"
"v_min_f32 v17,v17,v105\n"
"v_mfma_f32_16x16x32_bf16 v[20:23],a[80:83],a[32:35],v[20:23]\n"
"s_add_u32 s101,s101,4\n"
"v_exp_f32 v40,v40\n"
"v_mfma_f32_16x16x32_bf16 v[20:23],a[84:87],a[36:39],v[20:23]\n"
"s_add_u32 m0,s55,8192\n"
"v_exp_f32 v41,v41\n"
"v_mfma_f32_16x16x32_bf16 v[20:23],a[88:91],a[40:43],v[20:23]\n"
"s_add_u32 s54,s54,s22\n"
"v_exp_f32 v42,v42\n"
"v_mfma_f32_16x16x32_bf16 v[20:23],a[92:95],a[44:47],v[20:23]\n"
"v_exp_f32 v43,v43\n"
"v_mfma_f32_16x16x32_bf16 v[16:19],a[76:79],a[44:47],v[16:19]\n"
"v_exp_f32 v44,v44\n"
"v_mfma_f32_16x16x32_bf16 v[16:19],a[72:75],a[40:43],v[16:19]\n"
"v_exp_f32 v45,v45\n"
"v_mfma_f32_16x16x32_bf16 v[16:19],a[68:71],a[36:39],v[16:19]\n"
"v_exp_f32 v46,v46\n"
"v_mfma_f32_16x16x32_bf16 v[16:19],a[64:67],a[32:35],v[16:19]\n"
"s_waitcnt lgkmcnt(4)\n"
"v_exp_f32 v47,v47\n"
"v_mfma_f32_16x16x32_bf16 v[128:131],a[96:99],v[64:67],v[128:131]\n"
"v_exp_f32 v48,v48\n"
"v_mfma_f32_16x16x32_bf16 v[160:163],a[96:99],v[68:71],v[160:163]\n"
"v_exp_f32 v49,v49\n"
"v_mfma_f32_16x16x32_bf16 v[192:195],a[96:99],v[72:75],v[192:195]\n"
"ds_read_b128 a[64:67],v113 offset:32768\n"
"ds_read_b128 a[68:71],v113 offset:33792\n"
"v_mfma_f32_16x16x32_bf16 v[224:227],a[96:99],v[76:79],v[224:227]\n"
"v_exp_f32 v50,v50\n"
"v_mfma_f32_16x16x32_bf16 a[140:143],a[144:147],v[76:79],a[140:143]\n"
"ds_read_b128 a[72:75],v113 offset:34816\n"
"ds_read_b128 a[76:79],v113 offset:35840\n"
"v_mfma_f32_16x16x32_bf16 v[228:231],a[100:103],v[76:79],v[228:231]\n"
"v_exp_f32 v51,v51\n"
"v_mfma_f32_16x16x32_bf16 v[132:135],a[100:103],v[64:67],v[132:135]\n"
"ds_read_b128 a[80:83],v113 offset:36864\n"
"ds_read_b128 a[84:87],v113 offset:37888\n"
"v_mfma_f32_16x16x32_bf16 v[164:167],a[100:103],v[68:71],v[164:167]\n"
"v_exp_f32 v52,v52\n"
"v_mfma_f32_16x16x32_bf16 v[196:199],a[100:103],v[72:75],v[196:199]\n"
"v_exp_f32 v53,v53\n"
"v_mfma_f32_16x16x32_bf16 a[136:139],a[144:147],v[72:75],a[136:139]\n"
"ds_read_b128 a[88:91],v113 offset:38912\n"
"ds_read_b128 a[92:95],v113 offset:39936\n"
"v_mfma_f32_16x16x32_bf16 v[200:203],a[104:107],v[72:75],v[200:203]\n"
"v_exp_f32 v54,v54\n"
"v_mfma_f32_16x16x32_bf16 v[232:235],a[104:107],v[76:79],v[232:235]\n"
"s_waitcnt vmcnt(11)\n"
"ds_add_u32 v124,a152\n"
"v_mfma_f32_16x16x32_bf16 v[136:139],a[104:107],v[64:67],v[136:139]\n"
"v_exp_f32 v55,v55\n"
"v_mfma_f32_16x16x32_bf16 v[168:171],a[104:107],v[68:71],v[168:171]\n"
"buffer_load_dwordx4 v115,s[32:35],s54 offen sc1 lds\n"
"v_mfma_f32_16x16x32_bf16 a[132:135],a[144:147],v[68:71],a[132:135]\n"
"s_add_u32 m0,s67,49152\n"
"v_exp_f32 v56,v56\n"
"v_mfma_f32_16x16x32_bf16 v[172:175],a[108:111],v[68:71],v[172:175]\n"
"buffer_load_dwordx4 v112,s[24:27],s64 offen offset:0 sc1 lds\n"
"v_mfma_f32_16x16x32_bf16 v[204:207],a[108:111],v[72:75],v[204:207]\n"
"v_exp_f32 v57,v57\n"
"v_mfma_f32_16x16x32_bf16 v[236:239],a[108:111],v[76:79],v[236:239]\n"
"buffer_load_dwordx4 v112,s[24:27],s64 offen offset:1024 sc1 lds\n"
"v_mfma_f32_16x16x32_bf16 v[140:143],a[108:111],v[64:67],v[140:143]\n"
"v_exp_f32 v58,v58\n"
"v_mfma_f32_16x16x32_bf16 a[128:131],a[144:147],v[64:67],a[128:131]\n"
"buffer_load_dwordx4 v112,s[24:27],s64 offen offset:2048 sc1 lds\n"
"v_mfma_f32_16x16x32_bf16 v[144:147],a[112:115],v[64:67],v[144:147]\n"
"v_exp_f32 v59,v59\n"
"v_mfma_f32_16x16x32_bf16 v[176:179],a[112:115],v[68:71],v[176:179]\n"
"buffer_load_dwordx4 v112,s[24:27],s64 offen offset:3072 sc1 lds\n"
"v_mfma_f32_16x16x32_bf16 v[208:211],a[112:115],v[72:75],v[208:211]\n"
"s_add_u32 m0,s67,53248\n"
"v_exp_f32 v60,v60\n"
"v_mfma_f32_16x16x32_bf16 v[240:243],a[112:115],v[76:79],v[240:243]\n"
"buffer_load_dwordx4 v117,s[24:27],s64 offen offset:0 sc1 lds\n"
"v_mfma_f32_16x16x32_bf16 v[244:247],a[116:119],v[76:79],v[244:247]\n"
"v_exp_f32 v61,v61\n"
"v_mfma_f32_16x16x32_bf16 v[148:151],a[116:119],v[64:67],v[148:151]\n"
"buffer_load_dwordx4 v117,s[24:27],s64 offen offset:1024 sc1 lds\n"
"v_mfma_f32_16x16x32_bf16 v[180:183],a[116:119],v[68:71],v[180:183]\n"
"v_exp_f32 v62,v62\n"
"v_mfma_f32_16x16x32_bf16 v[212:215],a[116:119],v[72:75],v[212:215]\n"
"ds_read_b32 v125,v124\n"
"v_cvt_pk_bf16_f32 v84,v40,v41\n"
"v_mfma_f32_16x16x32_bf16 v[216:219],a[120:123],v[72:75],v[216:219]\n"
"v_exp_f32 v63,v63\n"
"v_mfma_f32_16x16x32_bf16 v[248:251],a[120:123],v[76:79],v[248:251]\n"
"buffer_load_dwordx4 v117,s[24:27],s64 offen offset:2048 sc1 lds\n"
"v_mfma_f32_16x16x32_bf16 v[152:155],a[120:123],v[64:67],v[152:155]\n"
"buffer_load_dwordx4 v117,s[24:27],s64 offen offset:3072 sc1 lds\n"
"v_mfma_f32_16x16x32_bf16 v[184:187],a[120:123],v[68:71],v[184:187]\n"
"s_add_u32 s64,s64,16384\n"
"buffer_store_dwordx4 a[248:251],v112,s[24:27],s56 offen offset:2048 sc1\n"
"v_mfma_f32_16x16x32_bf16 v[188:191],a[124:127],v[68:71],v[188:191]\n"
"s_waitcnt lgkmcnt(0)\n"
"s_and_b32 s67,s67,8192\n"
"v_cvt_pk_bf16_f32 v85,v42,v43\n"
"v_mfma_f32_16x16x32_bf16 v[220:223],a[124:127],v[72:75],v[220:223]\n"
"v_cvt_pk_bf16_f32 v86,v44,v45\n"
"v_cvt_pk_bf16_f32 v87,v46,v47\n"
"v_mfma_f32_16x16x32_bf16 v[252:255],a[124:127],v[76:79],v[252:255]\n"
"v_cvt_pk_bf16_f32 v88,v48,v49\n"
"v_cvt_pk_bf16_f32 v89,v50,v51\n"
"v_mfma_f32_16x16x32_bf16 v[156:159],a[124:127],v[64:67],v[156:159]\n"
"v_cvt_pk_bf16_f32 v90,v52,v53\n"
"v_cvt_pk_bf16_f32 v91,v54,v55\n"
"v_cvt_pk_bf16_f32 v92,v56,v57\n"
"v_cvt_pk_bf16_f32 v93,v58,v59\n"
"v_cvt_pk_bf16_f32 v94,v60,v61\n"
"v_cvt_pk_bf16_f32 v95,v62,v63\n"
"v_max3_f32 v118,v24,v25,v26\n"
"v_max3_f32 v118,v118,v27,v28\n"
"v_max3_f32 v118,v118,v29,v30\n"
"v_max3_f32 v118,v118,v31,v20\n"
"v_max3_f32 v118,v118,v21,v22\n"
"v_max3_f32 v118,v118,v23,v16\n"
"v_max3_f32 v118,v118,v17,v18\n"
"v_max_f32 v118,v118,v19\n"
"v_cmp_lt_f32 vcc,s93,v118\n"
"s_nop 0\n"
"s_cbranch_vccnz .Lfk_cs_u1\n"
".Lfk_cr_u1:\n"
"v_cmp_gt_u32 vcc,s101,v125\n"
"s_nop 0\n"
"s_cbranch_vccnz .Lfk_cs_mu1\n"
".Lfk_cr_mu1:\n"
"v_mov_b32 v113,v112\n"
"v_add_f32 v56,0x80000000,v126\n"
"v_min_f32 v56,v56,v108\n"
"v_add_f32 v57,0xf149f2ca,v126\n"
"v_min_f32 v57,v57,v109\n"
"v_add_f32 v58,0xf1c9f2ca,v126\n"
"v_min_f32 v58,v58,v110\n"
"v_add_f32 v59,0xf2177617,v126\n"
"v_min_f32 v59,v59,v111\n"
"v_add_f32 v60,0xf349f2ca,v126\n"
"v_min_f32 v60,v60,v108\n"
"v_add_f32 v61,0xf35691f6,v126\n"
"v_min_f32 v61,v61,v109\n"
"v_add_f32 v62,0xf3633123,v126\n"
"v_min_f32 v62,v62,v110\n"
"v_add_f32 v63,0xf36fd050,v126\n"
"v_min_f32 v63,v63,v111\n"
"v_exp_f32 v16,v16\n"
"v_mfma_f32_16x16x32_bf16 v[56:59],a[64:67],a[48:51],v[56:59]\n"
"s_add_u32 s101,s101,4\n"
"ds_read_b128 a[96:99],v114 offset:8192\n"
"ds_read_b128 a[100:103],v114 offset:9216\n"
"ds_read_b128 a[104:107],v114 offset:10240\n"
"v_exp_f32 v17,v17\n"
"v_mfma_f32_16x16x32_bf16 v[56:59],a[68:71],a[52:55],v[56:59]\n"
"s_add_u32 m0,s55,12288\n"
"ds_read_b128 a[108:111],v114 offset:11264\n"
"ds_read_b128 a[112:115],v114 offset:12288\n"
"ds_read_b128 a[116:119],v114 offset:13312\n"
"v_mfma_f32_16x16x32_bf16 v[56:59],a[72:75],a[56:59],v[56:59]\n"
"ds_read_b128 a[120:123],v114 offset:14336\n"
"ds_read_b128 a[124:127],v114 offset:15360\n"
"ds_read_b128 a[32:35],v116 offset:24576\n"
"ds_read_b128 a[36:39],v116 offset:25600\n"
"ds_read_b128 a[40:43],v116 offset:26624\n"
"v_mfma_f32_16x16x32_bf16 v[56:59],a[76:79],a[60:63],v[56:59]\n"
"s_add_u32 s54,s54,s22\n"
"ds_read_b128 a[44:47],v116 offset:27648\n"
"v_exp_f32 v18,v18\n"
"v_mfma_f32_16x16x32_bf16 v[60:63],a[92:95],a[60:63],v[60:63]\n"
"buffer_load_dwordx4 v115,s[32:35],s54 offen sc1 lds\n"
"v_cvt_pk_bf16_f32 v72,v16,v17\n"
"v_mfma_f32_16x16x32_bf16 v[60:63],a[88:91],a[56:59],v[60:63]\n"
"s_add_u32 m0,s67,32768\n"
"v_exp_f32 v19,v19\n"
"v_mfma_f32_16x16x32_bf16 v[60:63],a[84:87],a[52:55],v[60:63]\n"
"buffer_load_dwordx4 v112,s[24:27],s64 offen offset:0 sc1 lds\n"
"v_cvt_pk_bf16_f32 v73,v18,v19\n"
"v_exp_f32 v20,v20\n"
"v_mfma_f32_16x16x32_bf16 v[60:63],a[80:83],a[48:51],v[60:63]\n"
"ds_read_b128 a[64:67],v113 offset:0\n"
"ds_read_b128 a[68:71],v113 offset:1024\n"
"ds_read_b128 a[72:75],v113 offset:2048\n"
"s_waitcnt lgkmcnt(7)\n"
"v_mfma_f32_16x16x32_bf16 v[160:163],a[96:99],v[84:87],v[160:163]\n"
"ds_read_b128 a[76:79],v113 offset:3072\n"
"ds_read_b128 a[80:83],v113 offset:4096\n"
"ds_read_b128 a[84:87],v113 offset:5120\n"
"ds_read_b128 a[88:91],v113 offset:6144\n"
"ds_read_b128 a[92:95],v113 offset:7168\n"
"v_exp_f32 v21,v21\n"
"v_mfma_f32_16x16x32_bf16 a[132:135],a[144:147],v[84:87],a[132:135]\n"
"buffer_load_dwordx4 v112,s[24:27],s64 offen offset:1024 sc1 lds\n"
"v_max3_f32 v118,v56,v57,v58\n"
"v_mfma_f32_16x16x32_bf16 v[164:167],a[100:103],v[84:87],v[164:167]\n"
"v_exp_f32 v22,v22\n"
"v_cvt_pk_bf16_f32 v74,v20,v21\n"
"ds_read_b128 a[48:51],v116 offset:57344\n"
"ds_read_b128 a[52:55],v116 offset:58368\n"
"v_mfma_f32_16x16x32_bf16 v[168:171],a[104:107],v[84:87],v[168:171]\n"
"v_max3_f32 v118,v118,v59,v60\n"
"v_max3_f32 v118,v118,v61,v62\n"
"v_max_f32 v118,v118,v63\n"
"v_exp_f32 v23,v23\n"
"v_mfma_f32_16x16x32_bf16 v[172:175],a[108:111],v[84:87],v[172:175]\n"
"ds_read_b128 a[56:59],v116 offset:59392\n"
"buffer_load_dwordx4 v112,s[24:27],s64 offen offset:2048 sc1 lds\n"
"ds_read_b128 a[60:63],v116 offset:60416\n"
"v_mfma_f32_16x16x32_bf16 v[176:179],a[112:115],v[84:87],v[176:179]\n"
"s_waitcnt vmcnt(15)\n"
"v_cvt_pk_bf16_f32 v75,v22,v23\n"
"v_accvgpr_read_b32 v122,a128\n"
"v_rcp_f32 v122,v122\n"
"v_mfma_f32_16x16x32_bf16 v[180:183],a[116:119],v[84:87],v[180:183]\n"
"ds_add_u32 v124,a152\n"
"v_cmp_lt_f32 vcc,s93,v118\n"
"v_exp_f32 v24,v24\n"
"v_pk_mul_f32 v[128:129],v[128:129],v[122:123] op_sel_hi:[1,0]\n"
"v_pk_mul_f32 v[130:131],v[130:131],v[122:123] op_sel_hi:[1,0]\n"
"v_pk_mul_f32 v[132:133],v[132:133],v[122:123] op_sel_hi:[1,0]\n"
"v_pk_mul_f32 v[134:135],v[134:135],v[122:123] op_sel_hi:[1,0]\n"
"v_mfma_f32_16x16x32_bf16 v[184:187],a[120:123],v[84:87],v[184:187]\n"
"buffer_load_dwordx4 v112,s[24:27],s64 offen offset:3072 sc1 lds\n"
"v_cvt_pk_bf16_f32 v128,v128,v129\n"
"v_cvt_pk_bf16_f32 v129,v130,v131\n"
"v_mfma_f32_16x16x32_bf16 v[188:191],a[124:127],v[84:87],v[188:191]\n"
"s_add_u32 m0,s67,36864\n"
"v_cvt_pk_bf16_f32 v130,v132,v133\n"
"v_cvt_pk_bf16_f32 v131,v134,v135\n"
"v_exp_f32 v25,v25\n"
"v_mfma_f32_16x16x32_bf16 v[192:195],a[96:99],v[88:91],v[192:195]\n"
"buffer_store_dwordx4 v[128:131],v112,s[24:27],s70 offen offset:0 sc1\n"
"v_cvt_pk_bf16_f32 v76,v24,v25\n"
"v_pk_mul_f32 v[136:137],v[136:137],v[122:123] op_sel_hi:[1,0]\n"
"v_pk_mul_f32 v[138:139],v[138:139],v[122:123] op_sel_hi:[1,0]\n"
"v_pk_mul_f32 v[140:141],v[140:141],v[122:123] op_sel_hi:[1,0]\n"
"v_pk_mul_f32 v[142:143],v[142:143],v[122:123] op_sel_hi:[1,0]\n"
"v_exp_f32 v26,v26\n"
"v_mfma_f32_16x16x32_bf16 a[136:139],a[144:147],v[88:91],a[136:139]\n"
"buffer_load_dwordx4 v117,s[24:27],s64 offen offset:0 sc1 lds\n"
"v_cvt_pk_bf16_f32 v136,v136,v137\n"
"v_mfma_f32_16x16x32_bf16 v[196:199],a[100:103],v[88:91],v[196:199]\n"
"v_exp_f32 v27,v27\n"
"v_cvt_pk_bf16_f32 v137,v138,v139\n"
"v_cvt_pk_bf16_f32 v138,v140,v141\n"
"v_cvt_pk_bf16_f32 v139,v142,v143\n"
"v_cvt_pk_bf16_f32 v77,v26,v27\n"
"v_mfma_f32_16x16x32_bf16 v[200:203],a[104:107],v[88:91],v[200:203]\n"
"buffer_store_dwordx4 v[136:139],v112,s[24:27],s70 offen offset:1024 sc1\n"
"v_pk_mul_f32 v[144:145],v[144:145],v[122:123] op_sel_hi:[1,0]\n"
"v_pk_mul_f32 v[146:147],v[146:147],v[122:123] op_sel_hi:[1,0]\n"
"v_pk_mul_f32 v[148:149],v[148:149],v[122:123] op_sel_hi:[1,0]\n"
"v_pk_mul_f32 v[150:151],v[150:151],v[122:123] op_sel_hi:[1,0]\n"
"v_mfma_f32_16x16x32_bf16 v[204:207],a[108:111],v[88:91],v[204:207]\n"
"buffer_load_dwordx4 v117,s[24:27],s64 offen offset:1024 sc1 lds\n"
"v_exp_f32 v28,v28\n"
"v_mfma_f32_16x16x32_bf16 v[208:211],a[112:115],v[88:91],v[208:211]\n"
"v_cvt_pk_bf16_f32 v144,v144,v145\n"
"v_cvt_pk_bf16_f32 v145,v146,v147\n"
"v_cvt_pk_bf16_f32 v146,v148,v149\n"
"v_cvt_pk_bf16_f32 v147,v150,v151\n"
"v_mfma_f32_16x16x32_bf16 v[212:215],a[116:119],v[88:91],v[212:215]\n"
"buffer_store_dwordx4 v[144:147],v112,s[24:27],s70 offen offset:2048 sc1\n"
"v_pk_mul_f32 v[152:153],v[152:153],v[122:123] op_sel_hi:[1,0]\n"
"v_pk_mul_f32 v[154:155],v[154:155],v[122:123] op_sel_hi:[1,0]\n"
"v_pk_mul_f32 v[156:157],v[156:157],v[122:123] op_sel_hi:[1,0]\n"
"v_mfma_f32_16x16x32_bf16 v[216:219],a[120:123],v[88:91],v[216:219]\n"
"buffer_load_dwordx4 v117,s[24:27],s64 offen offset:2048 sc1 lds\n"
"v_exp_f32 v29,v29\n"
"v_cvt_pk_bf16_f32 v152,v152,v153\n"
"v_cvt_pk_bf16_f32 v78,v28,v29\n"
"v_pk_mul_f32 v[158:159],v[158:159],v[122:123] op_sel_hi:[1,0]\n"
"v_mfma_f32_16x16x32_bf16 v[220:223],a[124:127],v[88:91],v[220:223]\n"
"v_cvt_pk_bf16_f32 v153,v154,v155\n"
"v_cvt_pk_bf16_f32 v154,v156,v157\n"
"v_cvt_pk_bf16_f32 v155,v158,v159\n"
"v_mfma_f32_16x16x32_bf16 v[224:227],a[96:99],v[92:95],v[224:227]\n"
"ds_read_b32 v125,v124\n"
"buffer_store_dwordx4 v[152:155],v112,s[24:27],s70 offen offset:3072 sc1\n"
"buffer_load_dwordx4 v117,s[24:27],s64 offen offset:3072 sc1 lds\n"
"v_accvgpr_read_b32 v122,a132\n"
"v_mfma_f32_16x16x32_bf16 a[140:143],a[144:147],v[92:95],a[140:143]\n"
"s_add_u32 s70,s70,262144\n"
"v_exp_f32 v31,v31\n"
"v_mfma_f32_16x16x32_bf16 v[228:231],a[100:103],v[92:95],v[228:231]\n"
"s_add_u32 s64,s64,16384\n"
"v_exp_f32 v30,v30\n"
"v_rcp_f32 v122,v122\n"
"v_cvt_pk_bf16_f32 v79,v30,v31\n"
"v_pk_mul_f32 v[160:161],v[160:161],v[122:123] op_sel_hi:[1,0]\n"
"v_pk_mul_f32 v[162:163],v[162:163],v[122:123] op_sel_hi:[1,0]\n"
"v_pk_mul_f32 v[164:165],v[164:165],v[122:123] op_sel_hi:[1,0]\n"
"v_pk_mul_f32 v[166:167],v[166:167],v[122:123] op_sel_hi:[1,0]\n"
"v_mfma_f32_16x16x32_bf16 v[232:235],a[104:107],v[92:95],v[232:235]\n"
"buffer_store_dwordx4 a[252:255],v112,s[24:27],s56 offen offset:3072 sc1\n"
"v_cvt_pk_bf16_f32 v160,v160,v161\n"
"v_cvt_pk_bf16_f32 v161,v162,v163\n"
"v_cvt_pk_bf16_f32 v162,v164,v165\n"
"v_cvt_pk_bf16_f32 v163,v166,v167\n"
"v_mfma_f32_16x16x32_bf16 v[236:239],a[108:111],v[92:95],v[236:239]\n"
"buffer_store_dwordx4 v[160:163],v112,s[24:27],s70 offen offset:0 sc1\n"
"v_pk_mul_f32 v[168:169],v[168:169],v[122:123] op_sel_hi:[1,0]\n"
"v_pk_mul_f32 v[170:171],v[170:171],v[122:123] op_sel_hi:[1,0]\n"
"v_pk_mul_f32 v[172:173],v[172:173],v[122:123] op_sel_hi:[1,0]\n"
"v_pk_mul_f32 v[174:175],v[174:175],v[122:123] op_sel_hi:[1,0]\n"
"v_mfma_f32_16x16x32_bf16 v[240:243],a[112:115],v[92:95],v[240:243]\n"
"s_waitcnt lgkmcnt(0)\n"
"v_cvt_pk_bf16_f32 v168,v168,v169\n"
"v_cvt_pk_bf16_f32 v169,v170,v171\n"
"v_cvt_pk_bf16_f32 v170,v172,v173\n"
"v_cvt_pk_bf16_f32 v171,v174,v175\n"
"v_mfma_f32_16x16x32_bf16 v[244:247],a[116:119],v[92:95],v[244:247]\n"
"buffer_store_dwordx4 v[168:171],v112,s[24:27],s70 offen offset:1024 sc1\n"
"v_pk_mul_f32 v[176:177],v[176:177],v[122:123] op_sel_hi:[1,0]\n"
"v_pk_mul_f32 v[178:179],v[178:179],v[122:123] op_sel_hi:[1,0]\n"
"v_pk_mul_f32 v[180:181],v[180:181],v[122:123] op_sel_hi:[1,0]\n"
"v_pk_mul_f32 v[182:183],v[182:183],v[122:123] op_sel_hi:[1,0]\n"
"v_mfma_f32_16x16x32_bf16 v[248:251],a[120:123],v[92:95],v[248:251]\n"
"v_cvt_pk_bf16_f32 v176,v176,v177\n"
"v_cvt_pk_bf16_f32 v177,v178,v179\n"
"v_cvt_pk_bf16_f32 v178,v180,v181\n"
"v_cvt_pk_bf16_f32 v179,v182,v183\n"
"v_mfma_f32_16x16x32_bf16 v[252:255],a[124:127],v[92:95],v[252:255]\n"
"buffer_store_dwordx4 v[176:179],v112,s[24:27],s70 offen offset:2048 sc1\n"
"v_pk_mul_f32 v[184:185],v[184:185],v[122:123] op_sel_hi:[1,0]\n"
"v_pk_mul_f32 v[186:187],v[186:187],v[122:123] op_sel_hi:[1,0]\n"
"v_pk_mul_f32 v[188:189],v[188:189],v[122:123] op_sel_hi:[1,0]\n"
"v_pk_mul_f32 v[190:191],v[190:191],v[122:123] op_sel_hi:[1,0]\n"
"v_cvt_pk_bf16_f32 v184,v184,v185\n"
"v_cvt_pk_bf16_f32 v185,v186,v187\n"
"v_cvt_pk_bf16_f32 v186,v188,v189\n"
"v_cvt_pk_bf16_f32 v187,v190,v191\n"
"buffer_store_dwordx4 v[184:187],v112,s[24:27],s70 offen offset:3072 sc1\n"
"s_add_u32 s70,s70,262144\n"
"s_cbranch_vccnz .Lfk_cs_u2\n"
".Lfk_cr_u2:\n"
"v_cmp_gt_u32 vcc,s101,v125\n"
"s_nop 0\n"
"s_cbranch_vccnz .Lfk_cs_mu2\n"
".Lfk_cr_mu2:\n"
"s_add_u32 s89,s89,1\n"
"s_cmp_eq_u32 s89,8\n"
"s_cbranch_scc1 .Lfk_tailB7\n"
".Lfk_winB:\n"
"v_mfma_f32_16x16x32_bf16 v[0:3],a[64:67],a[0:3],v[96:99]\n"
"s_swappc_b64 s[94:95],s[20:21]\n"
"ds_read_b128 a[96:99],v114 offset:24576\n"
"v_mfma_f32_16x16x32_bf16 v[0:3],a[68:71],a[4:7],v[0:3]\n"
"s_add_u32 s101,s101,4\n"
"ds_read_b128 a[100:103],v114 offset:25600\n"
"ds_read_b128 a[104:107],v114 offset:26624\n"
"ds_read_b128 a[108:111],v114 offset:27648\n"
"v_mfma_f32_16x16x32_bf16 v[0:3],a[72:75],a[8:11],v[0:3]\n"
"ds_read_b128 a[112:115],v114 offset:28672\n"
"ds_read_b128 a[116:119],v114 offset:29696\n"
"v_mfma_f32_16x16x32_bf16 v[0:3],a[76:79],a[12:15],v[0:3]\n"
"ds_read_b128 a[120:123],v114 offset:30720\n"
"ds_read_b128 a[124:127],v114 offset:31744\n"
"v_mfma_f32_16x16x32_bf16 v[4:7],a[92:95],a[12:15],v[96:99]\n"
"v_exp_f32 v56,v56\n"
"v_mfma_f32_16x16x32_bf16 v[4:7],a[88:91],a[8:11],v[4:7]\n"
"v_exp_f32 v57,v57\n"
"v_mfma_f32_16x16x32_bf16 v[4:7],a[84:87],a[4:7],v[4:7]\n"
"v_cvt_pk_bf16_f32 v92,v56,v57\n"
"v_mfma_f32_16x16x32_bf16 v[4:7],a[80:83],a[0:3],v[4:7]\n"
"s_add_u32 m0,s67,0\n"
"s_waitcnt lgkmcnt(0)\n"
"v_mfma_f32_16x16x32_bf16 v[192:195],a[96:99],v[72:75],v[192:195]\n"
"buffer_load_dwordx4 v112,s[24:27],s64 offen offset:0 sc1 lds\n"
"v_mfma_f32_16x16x32_bf16 a[136:139],a[144:147],v[72:75],a[136:139]\n"
"buffer_load_dwordx4 v112,s[24:27],s64 offen offset:1024 sc1 lds\n"
"v_mfma_f32_16x16x32_bf16 v[196:199],a[100:103],v[72:75],v[196:199]\n"
"v_exp_f32 v58,v58\n"
"v_mfma_f32_16x16x32_bf16 v[200:203],a[104:107],v[72:75],v[200:203]\n"
"v_max3_f32 v118,v0,v1,v2\n"
"v_mfma_f32_16x16x32_bf16 v[204:207],a[108:111],v[72:75],v[204:207]\n"
"v_max3_f32 v118,v118,v3,v4\n"
"v_max3_f32 v118,v118,v5,v6\n"
"v_mfma_f32_16x16x32_bf16 v[208:211],a[112:115],v[72:75],v[208:211]\n"
"v_max_f32 v118,v118,v7\n"
"v_mov_b32 v120,v118\n"
"v_mfma_f32_16x16x32_bf16 v[212:215],a[116:119],v[72:75],v[212:215]\n"
"v_mfma_f32_16x16x32_bf16 v[216:219],a[120:123],v[72:75],v[216:219]\n"
"v_mfma_f32_16x16x32_bf16 v[220:223],a[124:127],v[72:75],v[220:223]\n"
"v_exp_f32 v59,v59\n"
"v_mfma_f32_16x16x32_bf16 v[224:227],a[96:99],v[76:79],v[224:227]\n"
"v_cvt_pk_bf16_f32 v93,v58,v59\n"
"v_accvgpr_read_b32 v122,a136\n"
"v_mfma_f32_16x16x32_bf16 a[140:143],a[144:147],v[76:79],a[140:143]\n"
"buffer_load_dwordx4 v112,s[24:27],s64 offen offset:2048 sc1 lds\n"
"v_mfma_f32_16x16x32_bf16 v[228:231],a[100:103],v[76:79],v[228:231]\n"
"v_rcp_f32 v122,v122\n"
"v_mfma_f32_16x16x32_bf16 v[232:235],a[104:107],v[76:79],v[232:235]\n"
"buffer_load_dwordx4 v112,s[24:27],s64 offen offset:3072 sc1 lds\n"
"v_pk_mul_f32 v[192:193],v[192:193],v[122:123] op_sel_hi:[1,0]\n"
"v_pk_mul_f32 v[194:195],v[194:195],v[122:123] op_sel_hi:[1,0]\n"
"v_mfma_f32_16x16x32_bf16 v[236:239],a[108:111],v[76:79],v[236:239]\n"
"v_cvt_pk_bf16_f32 v192,v192,v193\n"
"v_exp_f32 v60,v60\n"
"v_cvt_pk_bf16_f32 v193,v194,v195\n"
"v_pk_mul_f32 v[196:197],v[196:197],v[122:123] op_sel_hi:[1,0]\n"
"v_pk_mul_f32 v[198:199],v[198:199],v[122:123] op_sel_hi:[1,0]\n"
"v_mfma_f32_16x16x32_bf16 v[240:243],a[112:115],v[76:79],v[240:243]\n"
"s_add_u32 m0,s67,4096\n"
"v_cvt_pk_bf16_f32 v194,v196,v197\n"
"v_cvt_pk_bf16_f32 v195,v198,v199\n"
"v_mfma_f32_16x16x32_bf16 v[244:247],a[116:119],v[76:79],v[244:247]\n"
"v_mfma_f32_16x16x32_bf16 v[248:251],a[120:123],v[76:79],v[248:251]\n"
"buffer_store_dwordx4 v[192:195],v112,s[24:27],s70 offen offset:0 sc1\n"
"v_pk_mul_f32 v[200:201],v[200:201],v[122:123] op_sel_hi:[1,0]\n"
"v_mfma_f32_16x16x32_bf16 v[252:255],a[124:127],v[76:79],v[252:255]\n"
"buffer_load_dwordx4 v117,s[24:27],s64 offen offset:0 sc1 lds\n"
"v_cvt_pk_bf16_f32 v200,v200,v201\n"
"v_exp_f32 v61,v61\n"
"v_pk_mul_f32 v[202:203],v[202:203],v[122:123] op_sel_hi:[1,0]\n"
"v_pk_mul_f32 v[204:205],v[204:205],v[122:123] op_sel_hi:[1,0]\n"
"v_pk_mul_f32 v[206:207],v[206:207],v[122:123] op_sel_hi:[1,0]\n"
"v_mfma_f32_16x16x32_bf16 v[12:15],a[80:83],a[16:19],v[100:103]\n"
"v_cvt_pk_bf16_f32 v201,v202,v203\n"
"v_cvt_pk_bf16_f32 v202,v204,v205\n"
"v_mfma_f32_16x16x32_bf16 v[12:15],a[84:87],a[20:23],v[12:15]\n"
"v_cvt_pk_bf16_f32 v203,v206,v207\n"
"v_cvt_pk_bf16_f32 v94,v60,v61\n"
"v_mfma_f32_16x16x32_bf16 v[12:15],a[88:91],a[24:27],v[12:15]\n"
"buffer_load_dwordx4 v117,s[24:27],s64 offen offset:1024 sc1 lds\n"
"v_mfma_f32_16x16x32_bf16 v[12:15],a[92:95],a[28:31],v[12:15]\n"
"buffer_store_dwordx4 v[200:203],v112,s[24:27],s70 offen offset:1024 sc1\n"
"v_pk_mul_f32 v[208:209],v[208:209],v[122:123] op_sel_hi:[1,0]\n"
"v_pk_mul_f32 v[210:211],v[210:211],v[122:123] op_sel_hi:[1,0]\n"
"v_mfma_f32_16x16x32_bf16 v[8:11],a[76:79],a[28:31],v[100:103]\n"
"buffer_load_dwordx4 v117,s[24:27],s64 offen offset:2048 sc1 lds\n"
"v_cvt_pk_bf16_f32 v208,v208,v209\n"
"v_cvt_pk_bf16_f32 v209,v210,v211\n"
"v_pk_mul_f32 v[212:213],v[212:213],v[122:123] op_sel_hi:[1,0]\n"
"v_pk_mul_f32 v[214:215],v[214:215],v[122:123] op_sel_hi:[1,0]\n"
"v_mfma_f32_16x16x32_bf16 v[8:11],a[72:75],a[24:27],v[8:11]\n"
"v_cvt_pk_bf16_f32 v210,v212,v213\n"
"v_cvt_pk_bf16_f32 v211,v214,v215\n"
"v_mfma_f32_16x16x32_bf16 v[8:11],a[68:71],a[20:23],v[8:11]\n"
"v_exp_f32 v62,v62\n"
"v_mfma_f32_16x16x32_bf16 v[8:11],a[64:67],a[16:19],v[8:11]\n"
"buffer_store_dwordx4 v[208:211],v112,s[24:27],s70 offen offset:2048 sc1\n"
"v_pk_mul_f32 v[216:217],v[216:217],v[122:123] op_sel_hi:[1,0]\n"
"v_mfma_f32_16x16x32_bf16 v[16:19],a[64:67],a[32:35],v[104:107]\n"
"v_cvt_pk_bf16_f32 v216,v216,v217\n"
"buffer_load_dwordx4 v117,s[24:27],s64 offen offset:3072 sc1 lds\n"
"v_pk_mul_f32 v[218:219],v[218:219],v[122:123] op_sel_hi:[1,0]\n"
"v_pk_mul_f32 v[220:221],v[220:221],v[122:123] op_sel_hi:[1,0]\n"
"v_pk_mul_f32 v[222:223],v[222:223],v[122:123] op_sel_hi:[1,0]\n"
"v_mfma_f32_16x16x32_bf16 v[16:19],a[68:71],a[36:39],v[16:19]\n"
"v_cvt_pk_bf16_f32 v217,v218,v219\n"
"v_cvt_pk_bf16_f32 v218,v220,v221\n"
"v_mfma_f32_16x16x32_bf16 v[16:19],a[72:75],a[40:43],v[16:19]\n"
"s_add_u32 s64,s64,16384\n"
"v_exp_f32 v63,v63\n"
"v_mfma_f32_16x16x32_bf16 v[16:19],a[76:79],a[44:47],v[16:19]\n"
"s_andn2_b32 m0,s55,16384\n"
"s_add_u32 s54,s54,s22\n"
"v_max3_f32 v119,v8,v9,v10\n"
"v_mfma_f32_16x16x32_bf16 v[20:23],a[92:95],a[44:47],v[104:107]\n"
"v_max3_f32 v119,v119,v11,v12\n"
"v_cvt_pk_bf16_f32 v95,v62,v63\n"
"v_mfma_f32_16x16x32_bf16 v[20:23],a[88:91],a[40:43],v[20:23]\n"
"buffer_load_dwordx4 v115,s[32:35],s54 offen sc1 lds\n"
"v_mfma_f32_16x16x32_bf16 v[20:23],a[84:87],a[36:39],v[20:23]\n"
"v_max3_f32 v119,v119,v13,v14\n"
"v_max_f32 v119,v119,v15\n"
"v_mfma_f32_16x16x32_bf16 v[20:23],a[80:83],a[32:35],v[20:23]\n"
"v_cvt_pk_bf16_f32 v219,v222,v223\n"
"v_mfma_f32_16x16x32_bf16 v[28:31],a[80:83],a[48:51],v[108:111]\n"
"buffer_store_dwordx4 v[216:219],v112,s[24:27],s70 offen offset:3072 sc1\n"
"buffer_store_dwordx4 a[240:243],v112,s[24:27],s56 offen offset:0 sc1\n"
"v_mfma_f32_16x16x32_bf16 v[28:31],a[84:87],a[52:55],v[28:31]\n"
"s_add_u32 s70,s70,262144\n"
"v_max_f32 v118,v118,v119\n"
"v_min_f32 v120,v120,v119\n"
"v_mfma_f32_16x16x32_bf16 v[28:31],a[88:91],a[56:59],v[28:31]\n"
"v_max3_f32 v119,v16,v17,v18\n"
"v_mfma_f32_16x16x32_bf16 v[28:31],a[92:95],a[60:63],v[28:31]\n"
"v_max3_f32 v119,v119,v19,v20\n"
"v_max3_f32 v119,v119,v21,v22\n"
"v_mfma_f32_16x16x32_bf16 v[24:27],a[76:79],a[60:63],v[108:111]\n"
"v_max_f32 v119,v119,v23\n"
"v_max_f32 v118,v118,v119\n"
"v_mfma_f32_16x16x32_bf16 v[24:27],a[72:75],a[56:59],v[24:27]\n"
"v_min_f32 v120,v120,v119\n"
"v_mfma_f32_16x16x32_bf16 v[24:27],a[68:71],a[52:55],v[24:27]\n"
"v_mfma_f32_16x16x32_bf16 v[24:27],a[64:67],a[48:51],v[24:27]\n"
"ds_read_b128 a[64:67],v113 offset:16384\n"
"ds_read_b128 a[68:71],v113 offset:17408\n"
"ds_read_b128 a[72:75],v113 offset:18432\n"
"ds_read_b128 a[76:79],v113 offset:19456\n"
"ds_read_b128 a[80:83],v113 offset:20480\n"
"ds_read_b128 a[84:87],v113 offset:21504\n"
"ds_read_b128 a[88:91],v113 offset:22528\n"
"ds_read_b128 a[92:95],v113 offset:23552\n"
"v_max3_f32 v119,v24,v25,v26\n"
"v_max3_f32 v119,v119,v27,v28\n"
"v_max3_f32 v119,v119,v29,v30\n"
"v_max_f32 v119,v119,v31\n"
"v_min_f32 v120,v120,v119\n"
"v_max_f32 v118,v118,v119\n"
"v_cmp_gt_f32 s[96:97],s16,v120\n"
"v_cmp_lt_f32 vcc,s93,v118\n"
"s_waitcnt vmcnt(25)\n"
"ds_add_u32 v124,a152\n"
"ds_read_b32 v125,v124\n"
"s_waitcnt lgkmcnt(0)\n"
"s_or_b64 s[96:97],s[96:97],vcc\n"
"s_cbranch_scc1 .Lfk_cs_v3\n"
".Lfk_cr_v3:\n"
"v_cmp_gt_u32 vcc,s101,v125\n"
"s_nop 0\n"
"s_cbranch_vccnz .Lfk_cs_mv3\n"
".Lfk_cr_mv3:\n"
"v_exp_f32 v0,v0\n"
"v_mfma_f32_16x16x32_bf16 v[32:35],a[64:67],a[0:3],v[96:99]\n"
"s_add_u32 s101,s101,4\n"
"ds_read_b128 a[96:99],v114 offset:57344\n"
"ds_read_b128 a[100:103],v114 offset:58368\n"
"v_exp_f32 v1,v1\n"
"v_mfma_f32_16x16x32_bf16 v[32:35],a[68:71],a[4:7],v[32:35]\n"
"s_add_u32 m0,s55,4096\n"
"ds_read_b128 a[104:107],v114 offset:59392\n"
"ds_read_b128 a[108:111],v114 offset:60416\n"
"v_exp_f32 v2,v2\n"
"v_mfma_f32_16x16x32_bf16 v[32:35],a[72:75],a[8:11],v[32:35]\n"
"s_add_u32 s54,s54,s22\n"
"ds_read_b128 a[112:115],v114 offset:61440\n"
"ds_read_b128 a[116:119],v114 offset:62464\n"
"v_exp_f32 v3,v3\n"
"v_mfma_f32_16x16x32_bf16 v[32:35],a[76:79],a[12:15],v[32:35]\n"
"ds_read_b128 a[120:123],v114 offset:63488\n"
"ds_read_b128 a[124:127],v114 offset:64512\n"
"v_exp_f32 v4,v4\n"
"v_mfma_f32_16x16x32_bf16 v[36:39],a[92:95],a[12:15],v[96:99]\n"
"buffer_load_dwordx4 v115,s[32:35],s54 offen sc1 lds\n"
"v_mfma_f32_16x16x32_bf16 v[36:39],a[88:91],a[8:11],v[36:39]\n"
"s_add_u32 m0,s67,16384\n"
"v_exp_f32 v5,v5\n"
"v_cvt_pk_bf16_f32 v64,v0,v1\n"
"v_cvt_pk_bf16_f32 v65,v2,v3\n"
"v_mfma_f32_16x16x32_bf16 v[36:39],a[84:87],a[4:7],v[36:39]\n"
"v_exp_f32 v6,v6\n"
"v_exp_f32 v7,v7\n"
"v_mfma_f32_16x16x32_bf16 v[36:39],a[80:83],a[0:3],v[36:39]\n"
"buffer_load_dwordx4 v112,s[24:27],s64 offen offset:0 sc1 lds\n"
"s_waitcnt lgkmcnt(0)\n"
"v_mfma_f32_16x16x32_bf16 v[224:227],a[96:99],v[92:95],v[224:227]\n"
"v_exp_f32 v8,v8\n"
"v_cvt_pk_bf16_f32 v67,v6,v7\n"
"v_mfma_f32_16x16x32_bf16 a[140:143],a[144:147],v[92:95],a[140:143]\n"
"v_exp_f32 v9,v9\n"
"v_exp_f32 v10,v10\n"
"v_mfma_f32_16x16x32_bf16 v[228:231],a[100:103],v[92:95],v[228:231]\n"
"buffer_load_dwordx4 v112,s[24:27],s64 offen offset:1024 sc1 lds\n"
"v_mfma_f32_16x16x32_bf16 v[232:235],a[104:107],v[92:95],v[232:235]\n"
"v_cvt_pk_bf16_f32 v66,v4,v5\n"
"v_cvt_pk_bf16_f32 v68,v8,v9\n"
"v_mfma_f32_16x16x32_bf16 v[236:239],a[108:111],v[92:95],v[236:239]\n"
"v_exp_f32 v11,v11\n"
"v_exp_f32 v12,v12\n"
"v_mfma_f32_16x16x32_bf16 v[240:243],a[112:115],v[92:95],v[240:243]\n"
"buffer_load_dwordx4 v112,s[24:27],s64 offen offset:2048 sc1 lds\n"
"v_mfma_f32_16x16x32_bf16 v[244:247],a[116:119],v[92:95],v[244:247]\n"
"v_exp_f32 v13,v13\n"
"v_cvt_pk_bf16_f32 v69,v10,v11\n"
"v_mfma_f32_16x16x32_bf16 v[248:251],a[120:123],v[92:95],v[248:251]\n"
"v_exp_f32 v14,v14\n"
"v_cvt_pk_bf16_f32 v70,v12,v13\n"
"v_exp_f32 v15,v15\n"
"v_mfma_f32_16x16x32_bf16 v[252:255],a[124:127],v[92:95],v[252:255]\n"
"buffer_load_dwordx4 v112,s[24:27],s64 offen offset:3072 sc1 lds\n"
"v_mfma_f32_16x16x32_bf16 v[44:47],a[80:83],a[16:19],v[100:103]\n"
"s_add_u32 m0,s67,20480\n"
"v_exp_f32 v16,v16\n"
"v_cvt_pk_bf16_f32 v71,v14,v15\n"
"v_mfma_f32_16x16x32_bf16 v[44:47],a[84:87],a[20:23],v[44:47]\n"
"v_exp_f32 v17,v17\n"
"v_exp_f32 v18,v18\n"
"v_mfma_f32_16x16x32_bf16 v[44:47],a[88:91],a[24:27],v[44:47]\n"
"buffer_load_dwordx4 v117,s[24:27],s64 offen offset:0 sc1 lds\n"
"v_mfma_f32_16x16x32_bf16 v[44:47],a[92:95],a[28:31],v[44:47]\n"
"v_exp_f32 v19,v19\n"
"v_accvgpr_read_b32 v122,a140\n"
"v_mfma_f32_16x16x32_bf16 v[40:43],a[76:79],a[28:31],v[100:103]\n"
"v_cvt_pk_bf16_f32 v73,v18,v19\n"
"v_rcp_f32 v122,v122\n"
"v_mfma_f32_16x16x32_bf16 v[40:43],a[72:75],a[24:27],v[40:43]\n"
"buffer_load_dwordx4 v117,s[24:27],s64 offen offset:1024 sc1 lds\n"
"v_mfma_f32_16x16x32_bf16 v[40:43],a[68:71],a[20:23],v[40:43]\n"
"v_exp_f32 v21,v21\n"
"v_exp_f32 v20,v20\n"
"v_cvt_pk_bf16_f32 v72,v16,v17\n"
"v_pk_mul_f32 v[224:225],v[224:225],v[122:123] op_sel_hi:[1,0]\n"
"v_pk_mul_f32 v[226:227],v[226:227],v[122:123] op_sel_hi:[1,0]\n"
"v_pk_mul_f32 v[228:229],v[228:229],v[122:123] op_sel_hi:[1,0]\n"
"v_pk_mul_f32 v[230:231],v[230:231],v[122:123] op_sel_hi:[1,0]\n"
"v_mfma_f32_16x16x32_bf16 v[40:43],a[64:67],a[16:19],v[40:43]\n"
"v_cvt_pk_bf16_f32 v74,v20,v21\n"
"v_exp_f32 v22,v22\n"
"v_cvt_pk_bf16_f32 v224,v224,v225\n"
"v_exp_f32 v23,v23\n"
"v_mfma_f32_16x16x32_bf16 v[48:51],a[64:67],a[32:35],v[104:107]\n"
"buffer_load_dwordx4 v117,s[24:27],s64 offen offset:2048 sc1 lds\n"
"v_mfma_f32_16x16x32_bf16 v[48:51],a[68:71],a[36:39],v[48:51]\n"
"v_exp_f32 v24,v24\n"
"v_cvt_pk_bf16_f32 v225,v226,v227\n"
"v_mfma_f32_16x16x32_bf16 v[48:51],a[72:75],a[40:43],v[48:51]\n"
"v_exp_f32 v25,v25\n"
"v_cvt_pk_bf16_f32 v226,v228,v229\n"
"v_cvt_pk_bf16_f32 v227,v230,v231\n"
"v_mfma_f32_16x16x32_bf16 v[48:51],a[76:79],a[44:47],v[48:51]\n"
"buffer_load_dwordx4 v117,s[24:27],s64 offen offset:3072 sc1 lds\n"
"v_mfma_f32_16x16x32_bf16 v[52:55],a[92:95],a[44:47],v[104:107]\n"
"buffer_store_dwordx4 v[224:227],v112,s[24:27],s70 offen offset:0 sc1\n"
"v_exp_f32 v27,v27\n"
"v_mfma_f32_16x16x32_bf16 v[52:55],a[88:91],a[40:43],v[52:55]\n"
"s_add_u32 s64,s64,16384\n"
"v_exp_f32 v26,v26\n"
"v_exp_f32 v28,v28\n"
"v_cvt_pk_bf16_f32 v77,v26,v27\n"
"v_pk_mul_f32 v[232:233],v[232:233],v[122:123] op_sel_hi:[1,0]\n"
"v_mfma_f32_16x16x32_bf16 v[52:55],a[84:87],a[36:39],v[52:55]\n"
"v_cvt_pk_bf16_f32 v75,v22,v23\n"
"v_cvt_pk_bf16_f32 v76,v24,v25\n"
"v_exp_f32 v29,v29\n"
"v_exp_f32 v31,v31\n"
"v_pk_mul_f32 v[234:235],v[234:235],v[122:123] op_sel_hi:[1,0]\n"
"v_pk_mul_f32 v[236:237],v[236:237],v[122:123] op_sel_hi:[1,0]\n"
"v_pk_mul_f32 v[238:239],v[238:239],v[122:123] op_sel_hi:[1,0]\n"
"v_mfma_f32_16x16x32_bf16 v[52:55],a[80:83],a[32:35],v[52:55]\n"
"v_exp_f32 v30,v30\n"
"v_cvt_pk_bf16_f32 v78,v28,v29\n"
"v_mfma_f32_16x16x32_bf16 v[60:63],a[80:83],a[48:51],v[108:111]\n"
"v_cvt_pk_bf16_f32 v232,v232,v233\n"
"v_cvt_pk_bf16_f32 v233,v234,v235\n"
"v_mfma_f32_16x16x32_bf16 v[60:63],a[84:87],a[52:55],v[60:63]\n"
"buffer_store_dwordx4 a[244:247],v112,s[24:27],s56 offen offset:1024 sc1\n"
"v_cvt_pk_bf16_f32 v234,v236,v237\n"
"v_cvt_pk_bf16_f32 v235,v238,v239\n"
"v_mfma_f32_16x16x32_bf16 v[60:63],a[88:91],a[56:59],v[60:63]\n"
"buffer_store_dwordx4 v[232:235],v112,s[24:27],s70 offen offset:1024 sc1\n"
"v_pk_mul_f32 v[240:241],v[240:241],v[122:123] op_sel_hi:[1,0]\n"
"v_pk_mul_f32 v[242:243],v[242:243],v[122:123] op_sel_hi:[1,0]\n"
"v_pk_mul_f32 v[244:245],v[244:245],v[122:123] op_sel_hi:[1,0]\n"
"v_pk_mul_f32 v[246:247],v[246:247],v[122:123] op_sel_hi:[1,0]\n"
"v_mfma_f32_16x16x32_bf16 v[60:63],a[92:95],a[60:63],v[60:63]\n"
"v_cvt_pk_bf16_f32 v240,v240,v241\n"
"v_cvt_pk_bf16_f32 v241,v242,v243\n"
"v_cvt_pk_bf16_f32 v242,v244,v245\n"
"v_cvt_pk_bf16_f32 v243,v246,v247\n"
"v_mfma_f32_16x16x32_bf16 v[56:59],a[76:79],a[60:63],v[108:111]\n"
"buffer_store_dwordx4 v[240:243],v112,s[24:27],s70 offen offset:2048 sc1\n"
"v_pk_mul_f32 v[248:249],v[248:249],v[122:123] op_sel_hi:[1,0]\n"
"v_pk_mul_f32 v[250:251],v[250:251],v[122:123] op_sel_hi:[1,0]\n"
"v_mfma_f32_16x16x32_bf16 v[56:59],a[72:75],a[56:59],v[56:59]\n"
"v_cvt_pk_bf16_f32 v79,v30,v31\n"
"v_cvt_pk_bf16_f32 v248,v248,v249\n"
"v_cvt_pk_bf16_f32 v249,v250,v251\n"
"v_pk_mul_f32 v[252:253],v[252:253],v[122:123] op_sel_hi:[1,0]\n"
"v_pk_mul_f32 v[254:255],v[254:255],v[122:123] op_sel_hi:[1,0]\n"
"v_mfma_f32_16x16x32_bf16 v[56:59],a[68:71],a[52:55],v[56:59]\n"
"v_cvt_pk_bf16_f32 v250,v252,v253\n"
"v_cvt_pk_bf16_f32 v251,v254,v255\n"
"v_mfma_f32_16x16x32_bf16 v[56:59],a[64:67],a[48:51],v[56:59]\n"
"buffer_store_dwordx4 v[248:251],v112,s[24:27],s70 offen offset:3072 sc1\n"
"s_add_u32 s70,s70,262144\n"
"ds_read_b128 a[64:67],v113 offset:49152\n"
"ds_read_b128 a[68:71],v113 offset:50176\n"
"ds_read_b128 a[72:75],v113 offset:51200\n"
"ds_read_b128 a[76:79],v113 offset:52224\n"
"ds_read_b128 a[80:83],v113 offset:53248\n"
"ds_read_b128 a[84:87],v113 offset:54272\n"
"ds_read_b128 a[88:91],v113 offset:55296\n"
"ds_read_b128 a[92:95],v113 offset:56320\n"
"v_mov_b32 v114,v112\n"
"s_mov_b32 s70,s68\n"
"s_waitcnt vmcnt(25)\n"
"ds_add_u32 v124,a152\n"
"ds_read_b32 v125,v124\n"
"s_waitcnt lgkmcnt(0)\n"
"v_cmp_gt_u32 vcc,s101,v125\n"
"s_nop 0\n"
"s_cbranch_vccnz .Lfk_cs_mv0\n"
".Lfk_cr_mv0:\n"
"v_mfma_f32_16x16x32_bf16 v[0:3],a[64:67],a[0:3],v[96:99]\n"
"s_add_u32 s101,s101,4\n"
"v_exp_f32 v122,v32\n"
"v_mfma_f32_16x16x32_bf16 v[0:3],a[68:71],a[4:7],v[0:3]\n"
"s_add_u32 m0,s55,8192\n"
"v_exp_f32 v123,v33\n"
"v_mfma_f32_16x16x32_bf16 v[0:3],a[72:75],a[8:11],v[0:3]\n"
"ds_read_b128 a[96:99],v114 offset:40960\n"
"ds_read_b128 a[100:103],v114 offset:41984\n"
"v_mfma_f32_16x16x32_bf16 v[0:3],a[76:79],a[12:15],v[0:3]\n"
"s_add_u32 s54,s54,s22\n"
"v_exp_f32 v119,v34\n"
"v_mfma_f32_16x16x32_bf16 v[4:7],a[92:95],a[12:15],v[96:99]\n"
"ds_read_b128 a[104:107],v114 offset:43008\n"
"v_cvt_pk_bf16_f32 v80,v122,v123\n"
"v_mfma_f32_16x16x32_bf16 v[4:7],a[88:91],a[8:11],v[4:7]\n"
"v_exp_f32 v120,v35\n"
"v_mfma_f32_16x16x32_bf16 v[4:7],a[84:87],a[4:7],v[4:7]\n"
"ds_read_b128 a[108:111],v114 offset:44032\n"
"ds_read_b128 a[112:115],v114 offset:45056\n"
"v_mfma_f32_16x16x32_bf16 v[4:7],a[80:83],a[0:3],v[4:7]\n"
"v_exp_f32 v122,v36\n"
"v_mfma_f32_16x16x32_bf16 v[12:15],a[80:83],a[16:19],v[100:103]\n"
"ds_read_b128 a[116:119],v114 offset:46080\n"
"v_cvt_pk_bf16_f32 v81,v119,v120\n"
"v_mfma_f32_16x16x32_bf16 v[12:15],a[84:87],a[20:23],v[12:15]\n"
"v_exp_f32 v123,v37\n"
"v_mfma_f32_16x16x32_bf16 v[12:15],a[88:91],a[24:27],v[12:15]\n"
"ds_read_b128 a[120:123],v114 offset:47104\n"
"ds_read_b128 a[124:127],v114 offset:48128\n"
"v_mfma_f32_16x16x32_bf16 v[12:15],a[92:95],a[28:31],v[12:15]\n"
"v_exp_f32 v119,v38\n"
"v_mfma_f32_16x16x32_bf16 v[8:11],a[76:79],a[28:31],v[100:103]\n"
"v_cvt_pk_bf16_f32 v82,v122,v123\n"
"v_or3_b32 v118,v80,v81,v82\n"
"v_mfma_f32_16x16x32_bf16 v[8:11],a[72:75],a[24:27],v[8:11]\n"
"v_exp_f32 v120,v39\n"
"v_mfma_f32_16x16x32_bf16 v[8:11],a[68:71],a[20:23],v[8:11]\n"
"buffer_load_dwordx4 v115,s[32:35],s54 offen sc1 lds\n"
"v_mfma_f32_16x16x32_bf16 v[8:11],a[64:67],a[16:19],v[8:11]\n"
"s_add_u32 m0,s67,49152\n"
"v_exp_f32 v122,v40\n"
"v_mfma_f32_16x16x32_bf16 v[16:19],a[64:67],a[32:35],v[104:107]\n"
"v_cvt_pk_bf16_f32 v83,v119,v120\n"
"v_mfma_f32_16x16x32_bf16 v[16:19],a[68:71],a[36:39],v[16:19]\n"
"v_exp_f32 v123,v41\n"
"v_mfma_f32_16x16x32_bf16 v[16:19],a[72:75],a[40:43],v[16:19]\n"
"buffer_load_dwordx4 v112,s[24:27],s64 offen offset:0 sc1 lds\n"
"v_mfma_f32_16x16x32_bf16 v[16:19],a[76:79],a[44:47],v[16:19]\n"
"v_exp_f32 v119,v42\n"
"v_mfma_f32_16x16x32_bf16 v[20:23],a[92:95],a[44:47],v[104:107]\n"
"v_cvt_pk_bf16_f32 v84,v122,v123\n"
"v_or3_b32 v118,v118,v83,v84\n"
"v_mfma_f32_16x16x32_bf16 v[20:23],a[88:91],a[40:43],v[20:23]\n"
"v_exp_f32 v120,v43\n"
"v_mfma_f32_16x16x32_bf16 v[20:23],a[84:87],a[36:39],v[20:23]\n"
"buffer_load_dwordx4 v112,s[24:27],s64 offen offset:1024 sc1 lds\n"
"v_mfma_f32_16x16x32_bf16 v[20:23],a[80:83],a[32:35],v[20:23]\n"
"v_exp_f32 v122,v44\n"
"v_mfma_f32_16x16x32_bf16 v[28:31],a[80:83],a[48:51],v[108:111]\n"
"v_cvt_pk_bf16_f32 v85,v119,v120\n"
"v_mfma_f32_16x16x32_bf16 v[28:31],a[84:87],a[52:55],v[28:31]\n"
"v_exp_f32 v123,v45\n"
"v_mfma_f32_16x16x32_bf16 v[28:31],a[88:91],a[56:59],v[28:31]\n"
"buffer_load_dwordx4 v112,s[24:27],s64 offen offset:2048 sc1 lds\n"
"v_mfma_f32_16x16x32_bf16 v[28:31],a[92:95],a[60:63],v[28:31]\n"
"v_exp_f32 v119,v46\n"
"v_mfma_f32_16x16x32_bf16 v[24:27],a[76:79],a[60:63],v[108:111]\n"
"v_cvt_pk_bf16_f32 v86,v122,v123\n"
"v_or3_b32 v118,v118,v85,v86\n"
"v_mfma_f32_16x16x32_bf16 v[24:27],a[72:75],a[56:59],v[24:27]\n"
"v_exp_f32 v120,v47\n"
"v_mfma_f32_16x16x32_bf16 v[24:27],a[68:71],a[52:55],v[24:27]\n"
"buffer_load_dwordx4 v112,s[24:27],s64 offen offset:3072 sc1 lds\n"
"v_mfma_f32_16x16x32_bf16 v[24:27],a[64:67],a[48:51],v[24:27]\n"
"s_waitcnt lgkmcnt(0)\n"
"v_exp_f32 v122,v48\n"
"v_mfma_f32_16x16x32_bf16 v[128:131],a[96:99],v[64:67],0\n"
"v_exp_f32 v123,v49\n"
"v_mfma_f32_16x16x32_bf16 v[160:163],a[96:99],v[68:71],0\n"
"ds_read_b128 a[64:67],v113 offset:32768\n"
"v_cvt_pk_bf16_f32 v87,v119,v120\n"
"v_mfma_f32_16x16x32_bf16 v[192:195],a[96:99],v[72:75],0\n"
"s_add_u32 m0,s67,53248\n"
"v_exp_f32 v119,v50\n"
"v_mfma_f32_16x16x32_bf16 v[224:227],a[96:99],v[76:79],0\n"
"ds_read_b128 a[68:71],v113 offset:33792\n"
"v_cvt_pk_bf16_f32 v88,v122,v123\n"
"v_mfma_f32_16x16x32_bf16 a[140:143],a[144:147],v[76:79],0\n"
"v_exp_f32 v120,v51\n"
"v_mfma_f32_16x16x32_bf16 v[228:231],a[100:103],v[76:79],0\n"
"ds_read_b128 a[72:75],v113 offset:34816\n"
"ds_read_b128 a[76:79],v113 offset:35840\n"
"v_mfma_f32_16x16x32_bf16 v[132:135],a[100:103],v[64:67],0\n"
"v_exp_f32 v122,v52\n"
"v_mfma_f32_16x16x32_bf16 v[164:167],a[100:103],v[68:71],0\n"
"ds_read_b128 a[80:83],v113 offset:36864\n"
"v_cvt_pk_bf16_f32 v89,v119,v120\n"
"v_mfma_f32_16x16x32_bf16 v[196:199],a[100:103],v[72:75],0\n"
"v_exp_f32 v123,v53\n"
"v_mfma_f32_16x16x32_bf16 a[136:139],a[144:147],v[72:75],0\n"
"ds_read_b128 a[84:87],v113 offset:37888\n"
"ds_read_b128 a[88:91],v113 offset:38912\n"
"v_mfma_f32_16x16x32_bf16 v[200:203],a[104:107],v[72:75],0\n"
"v_exp_f32 v119,v54\n"
"v_mfma_f32_16x16x32_bf16 v[232:235],a[104:107],v[76:79],0\n"
"ds_read_b128 a[92:95],v113 offset:39936\n"
"v_cvt_pk_bf16_f32 v90,v122,v123\n"
"v_mfma_f32_16x16x32_bf16 v[136:139],a[104:107],v[64:67],0\n"
"v_exp_f32 v120,v55\n"
"v_mfma_f32_16x16x32_bf16 v[168:171],a[104:107],v[68:71],0\n"
"s_waitcnt vmcnt(16)\n"
"ds_add_u32 v124,a152\n"
"v_mfma_f32_16x16x32_bf16 a[132:135],a[144:147],v[68:71],0\n"
"v_exp_f32 v122,v56\n"
"v_mfma_f32_16x16x32_bf16 v[172:175],a[108:111],v[68:71],0\n"
"v_cvt_pk_bf16_f32 v91,v119,v120\n"
"v_or3_b32 v118,v118,v87,v88\n"
"v_mfma_f32_16x16x32_bf16 v[204:207],a[108:111],v[72:75],0\n"
"v_exp_f32 v123,v57\n"
"v_mfma_f32_16x16x32_bf16 v[236:239],a[108:111],v[76:79],0\n"
"buffer_load_dwordx4 v117,s[24:27],s64 offen offset:0 sc1 lds\n"
"v_mfma_f32_16x16x32_bf16 v[140:143],a[108:111],v[64:67],0\n"
"v_exp_f32 v119,v58\n"
"v_mfma_f32_16x16x32_bf16 a[128:131],a[144:147],v[64:67],0\n"
"v_cvt_pk_bf16_f32 v92,v122,v123\n"
"v_or3_b32 v118,v118,v89,v90\n"
"v_mfma_f32_16x16x32_bf16 v[144:147],a[112:115],v[64:67],0\n"
"v_exp_f32 v120,v59\n"
"v_mfma_f32_16x16x32_bf16 v[176:179],a[112:115],v[68:71],0\n"
"buffer_load_dwordx4 v117,s[24:27],s64 offen offset:1024 sc1 lds\n"
"v_mfma_f32_16x16x32_bf16 v[208:211],a[112:115],v[72:75],0\n"
"v_exp_f32 v122,v60\n"
"v_mfma_f32_16x16x32_bf16 v[240:243],a[112:115],v[76:79],0\n"
"v_cvt_pk_bf16_f32 v93,v119,v120\n"
"v_or3_b32 v118,v118,v91,v92\n"
"v_mfma_f32_16x16x32_bf16 v[244:247],a[116:119],v[76:79],0\n"
"v_exp_f32 v123,v61\n"
"v_mfma_f32_16x16x32_bf16 v[148:151],a[116:119],v[64:67],0\n"
"buffer_load_dwordx4 v117,s[24:27],s64 offen offset:2048 sc1 lds\n"
"v_mfma_f32_16x16x32_bf16 v[180:183],a[116:119],v[68:71],0\n"
"v_exp_f32 v119,v62\n"
"v_mfma_f32_16x16x32_bf16 v[212:215],a[116:119],v[72:75],0\n"
"ds_read_b32 v125,v124\n"
"v_cvt_pk_bf16_f32 v94,v122,v123\n"
"v_mfma_f32_16x16x32_bf16 v[216:219],a[120:123],v[72:75],0\n"
"v_exp_f32 v120,v63\n"
"v_mfma_f32_16x16x32_bf16 v[248:251],a[120:123],v[76:79],0\n"
"buffer_load_dwordx4 v117,s[24:27],s64 offen offset:3072 sc1 lds\n"
"v_mfma_f32_16x16x32_bf16 v[152:155],a[120:123],v[64:67],0\n"
"buffer_store_dwordx4 a[248:251],v112,s[24:27],s56 offen offset:2048 sc1\n"
"v_mfma_f32_16x16x32_bf16 v[184:187],a[120:123],v[68:71],0\n"
"s_add_u32 s64,s64,16384\n"
"v_cvt_pk_bf16_f32 v95,v119,v120\n"
"v_or3_b32 v118,v118,v93,v94\n"
"v_mfma_f32_16x16x32_bf16 v[188:191],a[124:127],v[68:71],0\n"
"s_waitcnt lgkmcnt(0)\n"
"v_subrev_u32 v125,s101,v125\n"
"v_or3_b32 v125,v118,v95,v125\n"
"v_mfma_f32_16x16x32_bf16 v[220:223],a[124:127],v[72:75],0\n"
"v_and_b32 v125,0x40004000,v125\n"
"v_cmp_ne_u32 vcc,0,v125\n"
"v_mfma_f32_16x16x32_bf16 v[252:255],a[124:127],v[76:79],0\n"
"v_mfma_f32_16x16x32_bf16 v[156:159],a[124:127],v[64:67],0\n"
"s_cbranch_vccnz .Lfk_cs_v1\n"
".Lfk_cr_v1:\n"
"v_mfma_f32_16x16x32_bf16 v[32:35],a[64:67],a[0:3],v[96:99]\n"
"s_add_u32 s101,s101,4\n"
"v_exp_f32 v122,v0\n"
"v_mfma_f32_16x16x32_bf16 v[32:35],a[68:71],a[4:7],v[32:35]\n"
"s_add_u32 m0,s55,12288\n"
"v_exp_f32 v123,v1\n"
"v_mfma_f32_16x16x32_bf16 v[32:35],a[72:75],a[8:11],v[32:35]\n"
"ds_read_b128 a[96:99],v114 offset:8192\n"
"ds_read_b128 a[100:103],v114 offset:9216\n"
"v_mfma_f32_16x16x32_bf16 v[32:35],a[76:79],a[12:15],v[32:35]\n"
"s_add_u32 s54,s54,s22\n"
"v_exp_f32 v119,v2\n"
"v_mfma_f32_16x16x32_bf16 v[36:39],a[92:95],a[12:15],v[96:99]\n"
"ds_read_b128 a[104:107],v114 offset:10240\n"
"v_cvt_pk_bf16_f32 v64,v122,v123\n"
"v_mfma_f32_16x16x32_bf16 v[36:39],a[88:91],a[8:11],v[36:39]\n"
"v_exp_f32 v120,v3\n"
"v_mfma_f32_16x16x32_bf16 v[36:39],a[84:87],a[4:7],v[36:39]\n"
"ds_read_b128 a[108:111],v114 offset:11264\n"
"ds_read_b128 a[112:115],v114 offset:12288\n"
"v_mfma_f32_16x16x32_bf16 v[36:39],a[80:83],a[0:3],v[36:39]\n"
"v_exp_f32 v122,v4\n"
"v_mfma_f32_16x16x32_bf16 v[44:47],a[80:83],a[16:19],v[100:103]\n"
"ds_read_b128 a[116:119],v114 offset:13312\n"
"v_cvt_pk_bf16_f32 v65,v119,v120\n"
"v_mfma_f32_16x16x32_bf16 v[44:47],a[84:87],a[20:23],v[44:47]\n"
"v_exp_f32 v123,v5\n"
"v_mfma_f32_16x16x32_bf16 v[44:47],a[88:91],a[24:27],v[44:47]\n"
"ds_read_b128 a[120:123],v114 offset:14336\n"
"ds_read_b128 a[124:127],v114 offset:15360\n"
"v_mfma_f32_16x16x32_bf16 v[44:47],a[92:95],a[28:31],v[44:47]\n"
"v_exp_f32 v119,v6\n"
"v_mfma_f32_16x16x32_bf16 v[40:43],a[76:79],a[28:31],v[100:103]\n"
"v_cvt_pk_bf16_f32 v66,v122,v123\n"
"v_or3_b32 v118,v64,v65,v66\n"
"v_mfma_f32_16x16x32_bf16 v[40:43],a[72:75],a[24:27],v[40:43]\n"
"v_exp_f32 v120,v7\n"
"v_mfma_f32_16x16x32_bf16 v[40:43],a[68:71],a[20:23],v[40:43]\n"
"buffer_load_dwordx4 v115,s[32:35],s54 offen sc1 lds\n"
"v_mfma_f32_16x16x32_bf16 v[40:43],a[64:67],a[16:19],v[40:43]\n"
"s_add_u32 m0,s67,32768\n"
"v_exp_f32 v122,v8\n"
"v_mfma_f32_16x16x32_bf16 v[48:51],a[64:67],a[32:35],v[104:107]\n"
"v_cvt_pk_bf16_f32 v67,v119,v120\n"
"v_mfma_f32_16x16x32_bf16 v[48:51],a[68:71],a[36:39],v[48:51]\n"
"v_exp_f32 v123,v9\n"
"v_mfma_f32_16x16x32_bf16 v[48:51],a[72:75],a[40:43],v[48:51]\n"
"buffer_load_dwordx4 v112,s[24:27],s64 offen offset:0 sc1 lds\n"
"v_mfma_f32_16x16x32_bf16 v[48:51],a[76:79],a[44:47],v[48:51]\n"
"v_exp_f32 v119,v10\n"
"v_mfma_f32_16x16x32_bf16 v[52:55],a[92:95],a[44:47],v[104:107]\n"
"v_cvt_pk_bf16_f32 v68,v122,v123\n"
"v_or3_b32 v118,v118,v67,v68\n"
"v_mfma_f32_16x16x32_bf16 v[52:55],a[88:91],a[40:43],v[52:55]\n"
"v_exp_f32 v120,v11\n"
"v_mfma_f32_16x16x32_bf16 v[52:55],a[84:87],a[36:39],v[52:55]\n"
"buffer_load_dwordx4 v112,s[24:27],s64 offen offset:1024 sc1 lds\n"
"v_mfma_f32_16x16x32_bf16 v[52:55],a[80:83],a[32:35],v[52:55]\n"
"v_exp_f32 v122,v12\n"
"v_mfma_f32_16x16x32_bf16 v[60:63],a[80:83],a[48:51],v[108:111]\n"
"v_cvt_pk_bf16_f32 v69,v119,v120\n"
"v_mfma_f32_16x16x32_bf16 v[60:63],a[84:87],a[52:55],v[60:63]\n"
"v_exp_f32 v123,v13\n"
"v_mfma_f32_16x16x32_bf16 v[60:63],a[88:91],a[56:59],v[60:63]\n"
"buffer_load_dwordx4 v112,s[24:27],s64 offen offset:2048 sc1 lds\n"
"v_mfma_f32_16x16x32_bf16 v[60:63],a[92:95],a[60:63],v[60:63]\n"
"v_exp_f32 v119,v14\n"
"v_mfma_f32_16x16x32_bf16 v[56:59],a[76:79],a[60:63],v[108:111]\n"
"v_cvt_pk_bf16_f32 v70,v122,v123\n"
"v_or3_b32 v118,v118,v69,v70\n"
"v_mfma_f32_16x16x32_bf16 v[56:59],a[72:75],a[56:59],v[56:59]\n"
"v_exp_f32 v120,v15\n"
"v_mfma_f32_16x16x32_bf16 v[56:59],a[68:71],a[52:55],v[56:59]\n"
"buffer_load_dwordx4 v112,s[24:27],s64 offen offset:3072 sc1 lds\n"
"v_mfma_f32_16x16x32_bf16 v[56:59],a[64:67],a[48:51],v[56:59]\n"
"s_waitcnt lgkmcnt(0)\n"
"v_exp_f32 v122,v16\n"
"v_mfma_f32_16x16x32_bf16 v[128:131],a[96:99],v[80:83],v[128:131]\n"
"v_exp_f32 v123,v17\n"
"v_mfma_f32_16x16x32_bf16 v[160:163],a[96:99],v[84:87],v[160:163]\n"
"ds_read_b128 a[64:67],v113 offset:0\n"
"v_cvt_pk_bf16_f32 v71,v119,v120\n"
"v_mfma_f32_16x16x32_bf16 v[192:195],a[96:99],v[88:91],v[192:195]\n"
"s_add_u32 m0,s67,36864\n"
"v_exp_f32 v119,v18\n"
"v_mfma_f32_16x16x32_bf16 v[224:227],a[96:99],v[92:95],v[224:227]\n"
"ds_read_b128 a[68:71],v113 offset:1024\n"
"v_cvt_pk_bf16_f32 v72,v122,v123\n"
"v_mfma_f32_16x16x32_bf16 a[140:143],a[144:147],v[92:95],a[140:143]\n"
"v_exp_f32 v120,v19\n"
"v_mfma_f32_16x16x32_bf16 v[228:231],a[100:103],v[92:95],v[228:231]\n"
"ds_read_b128 a[72:75],v113 offset:2048\n"
"ds_read_b128 a[76:79],v113 offset:3072\n"
"v_mfma_f32_16x16x32_bf16 v[132:135],a[100:103],v[80:83],v[132:135]\n"
"v_exp_f32 v122,v20\n"
"v_mfma_f32_16x16x32_bf16 v[164:167],a[100:103],v[84:87],v[164:167]\n"
"ds_read_b128 a[80:83],v113 offset:4096\n"
"v_cvt_pk_bf16_f32 v73,v119,v120\n"
"v_mfma_f32_16x16x32_bf16 v[196:199],a[100:103],v[88:91],v[196:199]\n"
"v_exp_f32 v123,v21\n"
"v_mfma_f32_16x16x32_bf16 a[136:139],a[144:147],v[88:91],a[136:139]\n"
"ds_read_b128 a[84:87],v113 offset:5120\n"
"ds_read_b128 a[88:91],v113 offset:6144\n"
"v_mfma_f32_16x16x32_bf16 v[200:203],a[104:107],v[88:91],v[200:203]\n"
"v_exp_f32 v119,v22\n"
"v_mfma_f32_16x16x32_bf16 v[232:235],a[104:107],v[92:95],v[232:235]\n"
"ds_read_b128 a[92:95],v113 offset:7168\n"
"v_cvt_pk_bf16_f32 v74,v122,v123\n"
"v_mfma_f32_16x16x32_bf16 v[136:139],a[104:107],v[80:83],v[136:139]\n"
"v_exp_f32 v120,v23\n"
"v_mfma_f32_16x16x32_bf16 v[168:171],a[104:107],v[84:87],v[168:171]\n"
"s_waitcnt vmcnt(16)\n"
"ds_add_u32 v124,a152\n"
"v_mfma_f32_16x16x32_bf16 a[132:135],a[144:147],v[84:87],a[132:135]\n"
"v_exp_f32 v122,v24\n"
"v_mfma_f32_16x16x32_bf16 v[172:175],a[108:111],v[84:87],v[172:175]\n"
"v_cvt_pk_bf16_f32 v75,v119,v120\n"
"v_or3_b32 v118,v118,v71,v72\n"
"v_mfma_f32_16x16x32_bf16 v[204:207],a[108:111],v[88:91],v[204:207]\n"
"v_exp_f32 v123,v25\n"
"v_mfma_f32_16x16x32_bf16 v[236:239],a[108:111],v[92:95],v[236:239]\n"
"buffer_load_dwordx4 v117,s[24:27],s64 offen offset:0 sc1 lds\n"
"v_mfma_f32_16x16x32_bf16 v[140:143],a[108:111],v[80:83],v[140:143]\n"
"v_exp_f32 v119,v26\n"
"v_mfma_f32_16x16x32_bf16 a[128:131],a[144:147],v[80:83],a[128:131]\n"
"v_cvt_pk_bf16_f32 v76,v122,v123\n"
"v_or3_b32 v118,v118,v73,v74\n"
"v_mfma_f32_16x16x32_bf16 v[144:147],a[112:115],v[80:83],v[144:147]\n"
"v_exp_f32 v120,v27\n"
"v_mfma_f32_16x16x32_bf16 v[176:179],a[112:115],v[84:87],v[176:179]\n"
"buffer_load_dwordx4 v117,s[24:27],s64 offen offset:1024 sc1 lds\n"
"v_mfma_f32_16x16x32_bf16 v[208:211],a[112:115],v[88:91],v[208:211]\n"
"v_exp_f32 v122,v28\n"
"v_mfma_f32_16x16x32_bf16 v[240:243],a[112:115],v[92:95],v[240:243]\n"
"v_cvt_pk_bf16_f32 v77,v119,v120\n"
"v_or3_b32 v118,v118,v75,v76\n"
"v_mfma_f32_16x16x32_bf16 v[244:247],a[116:119],v[92:95],v[244:247]\n"
"v_exp_f32 v123,v29\n"
"v_mfma_f32_16x16x32_bf16 v[148:151],a[116:119],v[80:83],v[148:151]\n"
"buffer_load_dwordx4 v117,s[24:27],s64 offen offset:2048 sc1 lds\n"
"v_mfma_f32_16x16x32_bf16 v[180:183],a[116:119],v[84:87],v[180:183]\n"
"v_exp_f32 v119,v30\n"
"v_mfma_f32_16x16x32_bf16 v[212:215],a[116:119],v[88:91],v[212:215]\n"
"ds_read_b32 v125,v124\n"
"v_cvt_pk_bf16_f32 v78,v122,v123\n"
"v_mfma_f32_16x16x32_bf16 v[216:219],a[120:123],v[88:91],v[216:219]\n"
"v_exp_f32 v120,v31\n"
"v_mfma_f32_16x16x32_bf16 v[248:251],a[120:123],v[92:95],v[248:251]\n"
"buffer_load_dwordx4 v117,s[24:27],s64 offen offset:3072 sc1 lds\n"
"v_mfma_f32_16x16x32_bf16 v[152:155],a[120:123],v[80:83],v[152:155]\n"
"buffer_store_dwordx4 a[252:255],v112,s[24:27],s56 offen offset:3072 sc1\n"
"v_mfma_f32_16x16x32_bf16 v[184:187],a[120:123],v[84:87],v[184:187]\n"
"s_add_u32 s64,s64,16384\n"
"s_add_u32 s89,s89,1\n"
"v_cvt_pk_bf16_f32 v79,v119,v120\n"
"v_mfma_f32_16x16x32_bf16 v[188:191],a[124:127],v[84:87],v[188:191]\n"
"s_waitcnt lgkmcnt(0)\n"
"v_subrev_u32 v125,s101,v125\n"
"v_or3_b32 v118,v118,v77,v78\n"
"v_mfma_f32_16x16x32_bf16 v[220:223],a[124:127],v[88:91],v[220:223]\n"
"v_or3_b32 v125,v118,v79,v125\n"
"v_and_b32 v125,0x40004000,v125\n"
"v_mfma_f32_16x16x32_bf16 v[252:255],a[124:127],v[92:95],v[252:255]\n"
"v_cmp_ne_u32 vcc,0,v125\n"
"v_mfma_f32_16x16x32_bf16 v[156:159],a[124:127],v[80:83],v[156:159]\n"
"s_cbranch_vccnz .Lfk_cs_v2\n"
".Lfk_cr_v2:\n"
"s_waitcnt vmcnt(20)\n"
"buffer_store_dwordx2 a[154:155],off,s[24:27],s69 sc1\n"
"s_branch .Lfk_Hx\n"
".Lfk_tailB:\n"
"s_waitcnt vmcnt(11) lgkmcnt(0)\n"
"s_barrier\n"
"v_mfma_f32_16x16x32_bf16 v[0:3],a[64:67],a[0:3],v[96:99]\n"
"ds_read_b128 a[96:99],v114 offset:24576\n"
"ds_read_b128 a[100:103],v114 offset:25600\n"
"ds_read_b128 a[104:107],v114 offset:26624\n"
"v_mfma_f32_16x16x32_bf16 v[0:3],a[68:71],a[4:7],v[0:3]\n"
"ds_read_b128 a[108:111],v114 offset:27648\n"
"ds_read_b128 a[112:115],v114 offset:28672\n"
"ds_read_b128 a[116:119],v114 offset:29696\n"
"v_mfma_f32_16x16x32_bf16 v[0:3],a[72:75],a[8:11],v[0:3]\n"
"ds_read_b128 a[120:123],v114 offset:30720\n"
"ds_read_b128 a[124:127],v114 offset:31744\n"
"v_add_f32 v16,0x80000000,v126\n"
"v_mfma_f32_16x16x32_bf16 v[0:3],a[76:79],a[12:15],v[0:3]\n"
"v_min_f32 v16,v16,v104\n"
"v_add_f32 v17,0xf149f2ca,v126\n"
"v_min_f32 v17,v17,v105\n"
"v_mfma_f32_16x16x32_bf16 v[4:7],a[92:95],a[12:15],v[96:99]\n"
"v_add_f32 v18,0xf1c9f2ca,v126\n"
"v_min_f32 v18,v18,v106\n"
"v_add_f32 v19,0xf2177617,v126\n"
"v_mfma_f32_16x16x32_bf16 v[4:7],a[88:91],a[8:11],v[4:7]\n"
"v_min_f32 v19,v19,v107\n"
"v_add_f32 v20,0xf349f2ca,v126\n"
"v_min_f32 v20,v20,v104\n"
"v_mfma_f32_16x16x32_bf16 v[4:7],a[84:87],a[4:7],v[4:7]\n"
"v_add_f32 v21,0xf35691f6,v126\n"
"v_min_f32 v21,v21,v105\n"
"v_add_f32 v22,0xf3633123,v126\n"
"v_mfma_f32_16x16x32_bf16 v[4:7],a[80:83],a[0:3],v[4:7]\n"
"v_min_f32 v22,v22,v106\n"
"v_add_f32 v23,0xf36fd050,v126\n"
"v_min_f32 v23,v23,v107\n"
"v_mfma_f32_16x16x32_bf16 v[12:15],a[80:83],a[16:19],v[100:103]\n"
"v_exp_f32 v32,v32\n"
"v_max3_f32 v118,v0,v1,v2\n"
"v_mfma_f32_16x16x32_bf16 v[12:15],a[84:87],a[20:23],v[12:15]\n"
"v_exp_f32 v33,v33\n"
"v_mfma_f32_16x16x32_bf16 v[12:15],a[88:91],a[24:27],v[12:15]\n"
"v_exp_f32 v34,v34\n"
"v_cvt_pk_bf16_f32 v80,v32,v33\n"
"v_mfma_f32_16x16x32_bf16 v[12:15],a[92:95],a[28:31],v[12:15]\n"
"v_exp_f32 v35,v35\n"
"v_max3_f32 v118,v118,v3,v4\n"
"v_mfma_f32_16x16x32_bf16 v[8:11],a[76:79],a[28:31],v[100:103]\n"
"v_exp_f32 v36,v36\n"
"v_cvt_pk_bf16_f32 v81,v34,v35\n"
"v_mfma_f32_16x16x32_bf16 v[8:11],a[72:75],a[24:27],v[8:11]\n"
"v_exp_f32 v37,v37\n"
"v_max3_f32 v118,v118,v5,v6\n"
"v_mfma_f32_16x16x32_bf16 v[8:11],a[68:71],a[20:23],v[8:11]\n"
"v_exp_f32 v38,v38\n"
"v_mfma_f32_16x16x32_bf16 v[8:11],a[64:67],a[16:19],v[8:11]\n"
"v_exp_f32 v39,v39\n"
"v_max3_f32 v118,v118,v7,v12\n"
"v_mfma_f32_16x16x32_bf16 v[24:27],a[64:67],a[48:51],v[108:111]\n"
"v_exp_f32 v40,v40\n"
"v_mfma_f32_16x16x32_bf16 v[24:27],a[68:71],a[52:55],v[24:27]\n"
"v_exp_f32 v41,v41\n"
"v_max3_f32 v118,v118,v13,v14\n"
"v_mfma_f32_16x16x32_bf16 v[24:27],a[72:75],a[56:59],v[24:27]\n"
"v_cvt_pk_bf16_f32 v82,v36,v37\n"
"v_cvt_pk_bf16_f32 v84,v40,v41\n"
"v_mfma_f32_16x16x32_bf16 v[24:27],a[76:79],a[60:63],v[24:27]\n"
"v_exp_f32 v42,v42\n"
"v_max3_f32 v118,v118,v15,v8\n"
"v_mfma_f32_16x16x32_bf16 v[28:31],a[92:95],a[60:63],v[108:111]\n"
"v_cvt_pk_bf16_f32 v83,v38,v39\n"
"v_max3_f32 v118,v118,v9,v10\n"
"v_mfma_f32_16x16x32_bf16 v[28:31],a[88:91],a[56:59],v[28:31]\n"
"v_exp_f32 v43,v43\n"
"v_mfma_f32_16x16x32_bf16 v[28:31],a[84:87],a[52:55],v[28:31]\n"
"v_cvt_pk_bf16_f32 v85,v42,v43\n"
"v_mfma_f32_16x16x32_bf16 v[28:31],a[80:83],a[48:51],v[28:31]\n"
"v_exp_f32 v44,v44\n"
"v_mfma_f32_16x16x32_bf16 v[20:23],a[80:83],a[32:35],v[20:23]\n"
"v_max3_f32 v118,v118,v11,v24\n"
"v_max3_f32 v118,v118,v25,v26\n"
"v_mfma_f32_16x16x32_bf16 v[20:23],a[84:87],a[36:39],v[20:23]\n"
"v_exp_f32 v45,v45\n"
"v_mfma_f32_16x16x32_bf16 v[20:23],a[88:91],a[40:43],v[20:23]\n"
"v_cvt_pk_bf16_f32 v86,v44,v45\n"
"v_mfma_f32_16x16x32_bf16 v[20:23],a[92:95],a[44:47],v[20:23]\n"
"v_exp_f32 v46,v46\n"
"v_mfma_f32_16x16x32_bf16 v[16:19],a[76:79],a[44:47],v[16:19]\n"
"v_max3_f32 v118,v118,v27,v28\n"
"v_max3_f32 v118,v118,v29,v30\n"
"v_mfma_f32_16x16x32_bf16 v[16:19],a[72:75],a[40:43],v[16:19]\n"
"v_exp_f32 v47,v47\n"
"v_mfma_f32_16x16x32_bf16 v[16:19],a[68:71],a[36:39],v[16:19]\n"
"v_cvt_pk_bf16_f32 v87,v46,v47\n"
"v_exp_f32 v48,v48\n"
"v_mfma_f32_16x16x32_bf16 v[16:19],a[64:67],a[32:35],v[16:19]\n"
"v_max3_f32 v118,v118,v31,v20\n"
"s_waitcnt lgkmcnt(0)\n"
"v_mfma_f32_16x16x32_bf16 v[128:131],a[96:99],v[64:67],v[128:131]\n"
"v_exp_f32 v49,v49\n"
"v_max3_f32 v118,v118,v21,v22\n"
"v_cvt_pk_bf16_f32 v88,v48,v49\n"
"v_mfma_f32_16x16x32_bf16 v[160:163],a[96:99],v[68:71],v[160:163]\n"
"ds_read_b128 a[64:67],v114 offset:57344\n"
"ds_read_b128 a[68:71],v114 offset:58368\n"
"v_mfma_f32_16x16x32_bf16 v[192:195],a[96:99],v[72:75],v[192:195]\n"
"ds_read_b128 a[72:75],v114 offset:59392\n"
"v_exp_f32 v50,v50\n"
"v_mfma_f32_16x16x32_bf16 v[224:227],a[96:99],v[76:79],v[224:227]\n"
"v_max3_f32 v118,v118,v23,v16\n"
"v_max3_f32 v118,v118,v17,v18\n"
"v_max_f32 v118,v118,v19\n"
"v_mfma_f32_16x16x32_bf16 a[140:143],a[144:147],v[76:79],a[140:143]\n"
"v_exp_f32 v51,v51\n"
"v_cmp_lt_f32 vcc,s93,v118\n"
"v_cvt_pk_bf16_f32 v89,v50,v51\n"
"v_mfma_f32_16x16x32_bf16 v[228:231],a[100:103],v[76:79],v[228:231]\n"
"ds_read_b128 a[76:79],v114 offset:60416\n"
"ds_read_b128 a[80:83],v114 offset:61440\n"
"v_mfma_f32_16x16x32_bf16 v[132:135],a[100:103],v[64:67],v[132:135]\n"
"ds_read_b128 a[84:87],v114 offset:62464\n"
"v_exp_f32 v52,v52\n"
"v_mfma_f32_16x16x32_bf16 v[164:167],a[100:103],v[68:71],v[164:167]\n"
"s_sub_u32 s82,s3,32768\n"
"ds_read_b128 a[88:91],v114 offset:63488\n"
"ds_read_b128 a[92:95],v114 offset:64512\n"
"v_mfma_f32_16x16x32_bf16 v[196:199],a[100:103],v[72:75],v[196:199]\n"
"buffer_load_dwordx4 a[0:3],v112,s[24:27],s82 offen offset:0 sc1\n"
"v_exp_f32 v53,v53\n"
"v_mfma_f32_16x16x32_bf16 a[136:139],a[144:147],v[72:75],a[136:139]\n"
"buffer_load_dwordx4 a[4:7],v112,s[24:27],s82 offen offset:1024 sc1\n"
"buffer_load_dwordx4 a[8:11],v112,s[24:27],s82 offen offset:2048 sc1\n"
"v_cvt_pk_bf16_f32 v90,v52,v53\n"
"v_exp_f32 v54,v54\n"
"v_mfma_f32_16x16x32_bf16 v[200:203],a[104:107],v[72:75],v[200:203]\n"
"buffer_load_dwordx4 a[12:15],v112,s[24:27],s82 offen offset:3072 sc1\n"
"v_mfma_f32_16x16x32_bf16 v[232:235],a[104:107],v[76:79],v[232:235]\n"
"s_sub_u32 s82,s3,49152\n"
"buffer_load_dwordx4 a[16:19],v112,s[24:27],s82 offen offset:0 sc1\n"
"buffer_load_dwordx4 a[20:23],v112,s[24:27],s82 offen offset:1024 sc1\n"
"v_exp_f32 v55,v55\n"
"v_mfma_f32_16x16x32_bf16 v[136:139],a[104:107],v[64:67],v[136:139]\n"
"buffer_load_dwordx4 a[24:27],v112,s[24:27],s82 offen offset:2048 sc1\n"
"v_mfma_f32_16x16x32_bf16 v[168:171],a[104:107],v[68:71],v[168:171]\n"
"buffer_load_dwordx4 a[28:31],v112,s[24:27],s82 offen offset:3072 sc1\n"
"v_mfma_f32_16x16x32_bf16 a[132:135],a[144:147],v[68:71],a[132:135]\n"
"v_exp_f32 v56,v56\n"
"v_mfma_f32_16x16x32_bf16 v[172:175],a[108:111],v[68:71],v[172:175]\n"
"v_cvt_pk_bf16_f32 v91,v54,v55\n"
"v_mfma_f32_16x16x32_bf16 v[204:207],a[108:111],v[72:75],v[204:207]\n"
"v_exp_f32 v57,v57\n"
"v_mfma_f32_16x16x32_bf16 v[236:239],a[108:111],v[76:79],v[236:239]\n"
"v_cvt_pk_bf16_f32 v92,v56,v57\n"
"v_mfma_f32_16x16x32_bf16 v[140:143],a[108:111],v[64:67],v[140:143]\n"
"v_exp_f32 v58,v58\n"
"v_mfma_f32_16x16x32_bf16 a[128:131],a[144:147],v[64:67],a[128:131]\n"
"v_mfma_f32_16x16x32_bf16 v[144:147],a[112:115],v[64:67],v[144:147]\n"
"v_exp_f32 v59,v59\n"
"v_mfma_f32_16x16x32_bf16 v[176:179],a[112:115],v[68:71],v[176:179]\n"
"v_cvt_pk_bf16_f32 v93,v58,v59\n"
"v_mfma_f32_16x16x32_bf16 v[208:211],a[112:115],v[72:75],v[208:211]\n"
"v_exp_f32 v60,v60\n"
"v_mfma_f32_16x16x32_bf16 v[240:243],a[112:115],v[76:79],v[240:243]\n"
"v_mfma_f32_16x16x32_bf16 v[244:247],a[116:119],v[76:79],v[244:247]\n"
"v_exp_f32 v61,v61\n"
"v_mfma_f32_16x16x32_bf16 v[148:151],a[116:119],v[64:67],v[148:151]\n"
"v_cvt_pk_bf16_f32 v94,v60,v61\n"
"v_mfma_f32_16x16x32_bf16 v[180:183],a[116:119],v[68:71],v[180:183]\n"
"v_exp_f32 v62,v62\n"
"v_mfma_f32_16x16x32_bf16 v[212:215],a[116:119],v[72:75],v[212:215]\n"
"v_mfma_f32_16x16x32_bf16 v[216:219],a[120:123],v[72:75],v[216:219]\n"
"v_exp_f32 v63,v63\n"
"v_mfma_f32_16x16x32_bf16 v[248:251],a[120:123],v[76:79],v[248:251]\n"
"v_cvt_pk_bf16_f32 v95,v62,v63\n"
"v_mfma_f32_16x16x32_bf16 v[152:155],a[120:123],v[64:67],v[152:155]\n"
"v_mfma_f32_16x16x32_bf16 v[184:187],a[120:123],v[68:71],v[184:187]\n"
"v_mfma_f32_16x16x32_bf16 v[188:191],a[124:127],v[68:71],v[188:191]\n"
"v_mfma_f32_16x16x32_bf16 v[220:223],a[124:127],v[72:75],v[220:223]\n"
"v_mfma_f32_16x16x32_bf16 v[252:255],a[124:127],v[76:79],v[252:255]\n"
"v_mfma_f32_16x16x32_bf16 v[156:159],a[124:127],v[64:67],v[156:159]\n"
"s_cbranch_vccnz .Lfk_cs_b3\n"
".Lfk_cr_b3:\n"
"s_waitcnt vmcnt(9) lgkmcnt(0)\n"
"s_barrier\n"
"v_mfma_f32_16x16x32_bf16 v[128:131],a[64:67],v[80:83],v[128:131]\n"
"v_exp_f32 v0,v0\n"
"v_mfma_f32_16x16x32_bf16 v[160:163],a[64:67],v[84:87],v[160:163]\n"
"v_exp_f32 v1,v1\n"
"v_mfma_f32_16x16x32_bf16 v[192:195],a[64:67],v[88:91],v[192:195]\n"
"v_exp_f32 v2,v2\n"
"v_cvt_pk_bf16_f32 v64,v0,v1\n"
"v_mfma_f32_16x16x32_bf16 v[224:227],a[64:67],v[92:95],v[224:227]\n"
"v_exp_f32 v3,v3\n"
"v_mfma_f32_16x16x32_bf16 a[140:143],a[144:147],v[92:95],a[140:143]\n"
"v_exp_f32 v4,v4\n"
"v_cvt_pk_bf16_f32 v65,v2,v3\n"
"v_mfma_f32_16x16x32_bf16 v[228:231],a[68:71],v[92:95],v[228:231]\n"
"v_exp_f32 v5,v5\n"
"v_mfma_f32_16x16x32_bf16 v[132:135],a[68:71],v[80:83],v[132:135]\n"
"v_exp_f32 v6,v6\n"
"v_cvt_pk_bf16_f32 v66,v4,v5\n"
"v_exp_f32 v7,v7\n"
"v_mfma_f32_16x16x32_bf16 v[164:167],a[68:71],v[84:87],v[164:167]\n"
"ds_read_b128 a[96:99],v114 offset:40960\n"
"ds_read_b128 a[100:103],v114 offset:41984\n"
"v_exp_f32 v8,v8\n"
"v_mfma_f32_16x16x32_bf16 v[196:199],a[68:71],v[88:91],v[196:199]\n"
"ds_read_b128 a[104:107],v114 offset:43008\n"
"ds_read_b128 a[108:111],v114 offset:44032\n"
"v_exp_f32 v9,v9\n"
"v_mfma_f32_16x16x32_bf16 a[136:139],a[144:147],v[88:91],a[136:139]\n"
"ds_read_b128 a[112:115],v114 offset:45056\n"
"ds_read_b128 a[116:119],v114 offset:46080\n"
"v_exp_f32 v10,v10\n"
"v_mfma_f32_16x16x32_bf16 v[200:203],a[72:75],v[88:91],v[200:203]\n"
"ds_read_b128 a[120:123],v114 offset:47104\n"
"ds_read_b128 a[124:127],v114 offset:48128\n"
"v_mfma_f32_16x16x32_bf16 v[232:235],a[72:75],v[92:95],v[232:235]\n"
"v_exp_f32 v11,v11\n"
"v_accvgpr_read_b32 v119,a150\n"
"v_accvgpr_read_b32 v120,a151\n"
"v_mfma_f32_16x16x32_bf16 v[136:139],a[72:75],v[80:83],v[136:139]\n"
"v_exp_f32 v12,v12\n"
"v_xor_b32 v119,s80,v119\n"
"v_xor_b32 v120,s81,v120\n"
"v_mfma_f32_16x16x32_bf16 v[168:171],a[72:75],v[84:87],v[168:171]\n"
"v_exp_f32 v13,v13\n"
"v_or_b32 v119,v119,v120\n"
"v_cmp_ne_u32 s[14:15],0,v119\n"
"v_mfma_f32_16x16x32_bf16 a[132:135],a[144:147],v[84:87],a[132:135]\n"
"s_add_u32 s18,s8,1032192\n"
"v_exp_f32 v14,v14\n"
"s_cmp_lg_u32 s14,0\n"
"s_cselect_b32 s18,2146435072,s18\n"
"v_mfma_f32_16x16x32_bf16 v[172:175],a[76:79],v[84:87],v[172:175]\n"
"s_add_u32 m0,s30,0\n"
"v_exp_f32 v15,v15\n"
"buffer_load_dwordx4 v112,s[24:27],s18 offen offset:0 sc1 lds\n"
"buffer_load_dwordx4 v112,s[24:27],s18 offen offset:1024 sc1 lds\n"
"v_exp_f32 v16,v16\n"
"v_mfma_f32_16x16x32_bf16 v[204:207],a[76:79],v[88:91],v[204:207]\n"
"buffer_load_dwordx4 v112,s[24:27],s18 offen offset:2048 sc1 lds\n"
"buffer_load_dwordx4 v112,s[24:27],s18 offen offset:3072 sc1 lds\n"
"v_mfma_f32_16x16x32_bf16 v[236:239],a[76:79],v[92:95],v[236:239]\n"
"s_add_u32 m0,m0,4096\n"
"v_exp_f32 v17,v17\n"
"v_cvt_pk_bf16_f32 v67,v6,v7\n"
"v_mfma_f32_16x16x32_bf16 v[140:143],a[76:79],v[80:83],v[140:143]\n"
"buffer_load_dwordx4 v117,s[24:27],s18 offen offset:0 sc1 lds\n"
"v_exp_f32 v18,v18\n"
"v_cvt_pk_bf16_f32 v68,v8,v9\n"
"v_cvt_pk_bf16_f32 v69,v10,v11\n"
"v_exp_f32 v19,v19\n"
"v_mfma_f32_16x16x32_bf16 a[128:131],a[144:147],v[80:83],a[128:131]\n"
"buffer_load_dwordx4 v117,s[24:27],s18 offen offset:1024 sc1 lds\n"
"v_cvt_pk_bf16_f32 v70,v12,v13\n"
"v_mfma_f32_16x16x32_bf16 v[144:147],a[80:83],v[80:83],v[144:147]\n"
"v_exp_f32 v20,v20\n"
"v_cvt_pk_bf16_f32 v71,v14,v15\n"
"v_cvt_pk_bf16_f32 v72,v16,v17\n"
"v_exp_f32 v21,v21\n"
"v_mfma_f32_16x16x32_bf16 v[176:179],a[80:83],v[84:87],v[176:179]\n"
"buffer_load_dwordx4 v117,s[24:27],s18 offen offset:2048 sc1 lds\n"
"v_cvt_pk_bf16_f32 v73,v18,v19\n"
"v_mfma_f32_16x16x32_bf16 v[208:211],a[80:83],v[88:91],v[208:211]\n"
"v_exp_f32 v22,v22\n"
"v_cvt_pk_bf16_f32 v74,v20,v21\n"
"v_exp_f32 v23,v23\n"
"v_mfma_f32_16x16x32_bf16 v[240:243],a[80:83],v[92:95],v[240:243]\n"
"buffer_load_dwordx4 v117,s[24:27],s18 offen offset:3072 sc1 lds\n"
"v_mfma_f32_16x16x32_bf16 v[244:247],a[84:87],v[92:95],v[244:247]\n"
"s_sub_u32 s82,s3,65536\n"
"v_exp_f32 v24,v24\n"
"v_cvt_pk_bf16_f32 v75,v22,v23\n"
"v_exp_f32 v25,v25\n"
"v_mfma_f32_16x16x32_bf16 v[148:151],a[84:87],v[80:83],v[148:151]\n"
"buffer_load_dwordx4 v[32:35],v112,s[24:27],s82 offen offset:0 sc1\n"
"v_mfma_f32_16x16x32_bf16 v[180:183],a[84:87],v[84:87],v[180:183]\n"
"v_exp_f32 v26,v26\n"
"v_cvt_pk_bf16_f32 v76,v24,v25\n"
"v_exp_f32 v27,v27\n"
"v_mfma_f32_16x16x32_bf16 v[212:215],a[84:87],v[88:91],v[212:215]\n"
"buffer_load_dwordx4 v[36:39],v112,s[24:27],s82 offen offset:1024 sc1\n"
"v_mfma_f32_16x16x32_bf16 v[216:219],a[88:91],v[88:91],v[216:219]\n"
"v_exp_f32 v28,v28\n"
"v_exp_f32 v29,v29\n"
"v_mfma_f32_16x16x32_bf16 v[248:251],a[88:91],v[92:95],v[248:251]\n"
"buffer_load_dwordx4 v[40:43],v112,s[24:27],s82 offen offset:2048 sc1\n"
"v_mfma_f32_16x16x32_bf16 v[152:155],a[88:91],v[80:83],v[152:155]\n"
"v_exp_f32 v30,v30\n"
"v_mfma_f32_16x16x32_bf16 v[184:187],a[88:91],v[84:87],v[184:187]\n"
"v_exp_f32 v31,v31\n"
"v_mfma_f32_16x16x32_bf16 v[188:191],a[92:95],v[84:87],v[188:191]\n"
"buffer_load_dwordx4 v[44:47],v112,s[24:27],s82 offen offset:3072 sc1\n"
"v_mfma_f32_16x16x32_bf16 v[220:223],a[92:95],v[88:91],v[220:223]\n"
"v_cvt_pk_bf16_f32 v78,v28,v29\n"
"v_cvt_pk_bf16_f32 v77,v26,v27\n"
"v_mfma_f32_16x16x32_bf16 v[252:255],a[92:95],v[92:95],v[252:255]\n"
"v_cvt_pk_bf16_f32 v79,v30,v31\n"
"v_mfma_f32_16x16x32_bf16 v[156:159],a[92:95],v[80:83],v[156:159]\n"
"s_load_dwordx2 s[36:37],s[0:1],0\n"
"s_load_dwordx2 s[44:45],s[0:1],80\n"
"s_load_dwordx2 s[48:49],s[0:1],88\n"
"s_lshl_b32 s82,s78,2\n"
"s_add_u32 s82,s82,65536\n"
"v_add_u32 v113,s82,v112\n"
"s_lshr_b32 s82,s78,2\n"
"s_add_u32 s82,s82,122880\n"
"v_lshrrev_b32 v115,1,v112\n"
"v_add_u32 v116,s82,v115\n"
"s_waitcnt lgkmcnt(0)\n"
"s_barrier\n"
"v_mfma_f32_16x16x32_bf16 v[128:131],a[96:99],v[64:67],v[128:131]\n"
"s_add_u32 s83,s18,2097152\n"
"s_add_u32 m0,s30,32768\n"
"v_mfma_f32_16x16x32_bf16 a[128:131],a[144:147],v[64:67],a[128:131]\n"
"buffer_load_dwordx4 v112,s[24:27],s83 offen offset:0 sc1 lds\n"
"v_mfma_f32_16x16x32_bf16 v[132:135],a[100:103],v[64:67],v[132:135]\n"
"buffer_load_dwordx4 v112,s[24:27],s83 offen offset:1024 sc1 lds\n"
"v_mfma_f32_16x16x32_bf16 v[136:139],a[104:107],v[64:67],v[136:139]\n"
"buffer_load_dwordx4 v112,s[24:27],s83 offen offset:2048 sc1 lds\n"
"v_mfma_f32_16x16x32_bf16 v[140:143],a[108:111],v[64:67],v[140:143]\n"
"v_mfma_f32_16x16x32_bf16 v[144:147],a[112:115],v[64:67],v[144:147]\n"
"buffer_load_dwordx4 v112,s[24:27],s83 offen offset:3072 sc1 lds\n"
"v_mfma_f32_16x16x32_bf16 v[148:151],a[116:119],v[64:67],v[148:151]\n"
"s_add_u32 m0,m0,4096\n"
"v_mfma_f32_16x16x32_bf16 v[152:155],a[120:123],v[64:67],v[152:155]\n"
"v_mfma_f32_16x16x32_bf16 v[156:159],a[124:127],v[64:67],v[156:159]\n"
"buffer_load_dwordx4 v117,s[24:27],s83 offen offset:0 sc1 lds\n"
"v_mfma_f32_16x16x32_bf16 v[160:163],a[96:99],v[68:71],v[160:163]\n"
"v_accvgpr_read_b32 v97,a128\n"
"v_cvt_pk_bf16_f32 v136,v136,v137\n"
"v_mfma_f32_16x16x32_bf16 a[132:135],a[144:147],v[68:71],a[132:135]\n"
"buffer_load_dwordx4 v117,s[24:27],s83 offen offset:1024 sc1 lds\n"
"v_mfma_f32_16x16x32_bf16 v[164:167],a[100:103],v[68:71],v[164:167]\n"
"v_cvt_pk_bf16_f32 v128,v128,v129\n"
"v_cvt_pk_bf16_f32 v129,v130,v131\n"
"v_mfma_f32_16x16x32_bf16 v[168:171],a[104:107],v[68:71],v[168:171]\n"
"buffer_load_dwordx4 v117,s[24:27],s83 offen offset:2048 sc1 lds\n"
"v_cvt_pk_bf16_f32 v130,v132,v133\n"
"v_cvt_pk_bf16_f32 v131,v134,v135\n"
"v_mfma_f32_16x16x32_bf16 v[172:175],a[108:111],v[68:71],v[172:175]\n"
"ds_write_b128 v113,v[128:131] offset:0\n"
"v_cvt_pk_bf16_f32 v137,v138,v139\n"
"v_mfma_f32_16x16x32_bf16 v[176:179],a[112:115],v[68:71],v[176:179]\n"
"buffer_load_dwordx4 v117,s[24:27],s83 offen offset:3072 sc1 lds\n"
"v_cvt_pk_bf16_f32 v138,v140,v141\n"
"v_cvt_pk_bf16_f32 v139,v142,v143\n"
"v_mfma_f32_16x16x32_bf16 v[180:183],a[116:119],v[68:71],v[180:183]\n"
"s_sub_u32 s82,s3,81920\n"
"ds_write_b128 v113,v[136:139] offset:1024\n"
"v_cvt_pk_bf16_f32 v144,v144,v145\n"
"v_mfma_f32_16x16x32_bf16 v[184:187],a[120:123],v[68:71],v[184:187]\n"
"buffer_load_dwordx4 a[32:35],v112,s[24:27],s82 offen offset:0 sc1\n"
"v_cvt_pk_bf16_f32 v145,v146,v147\n"
"v_cvt_pk_bf16_f32 v146,v148,v149\n"
"v_cvt_pk_bf16_f32 v147,v150,v151\n"
"v_mfma_f32_16x16x32_bf16 v[188:191],a[124:127],v[68:71],v[188:191]\n"
"ds_write_b128 v113,v[144:147] offset:2048\n"
"v_cvt_pk_bf16_f32 v152,v152,v153\n"
"v_mfma_f32_16x16x32_bf16 v[192:195],a[96:99],v[72:75],v[192:195]\n"
"buffer_load_dwordx4 a[36:39],v112,s[24:27],s82 offen offset:1024 sc1\n"
"v_cvt_pk_bf16_f32 v153,v154,v155\n"
"v_cvt_pk_bf16_f32 v154,v156,v157\n"
"v_cvt_pk_bf16_f32 v155,v158,v159\n"
"v_mfma_f32_16x16x32_bf16 a[136:139],a[144:147],v[72:75],a[136:139]\n"
"ds_write_b128 v113,v[152:155] offset:3072\n"
"ds_write_b64 v116,v[96:97] offset:0\n"
"v_mfma_f32_16x16x32_bf16 v[196:199],a[100:103],v[72:75],v[196:199]\n"
"buffer_load_dwordx4 a[40:43],v112,s[24:27],s82 offen offset:2048 sc1\n"
"v_accvgpr_read_b32 v101,a132\n"
"v_cvt_pk_bf16_f32 v160,v160,v161\n"
"v_mfma_f32_16x16x32_bf16 v[200:203],a[104:107],v[72:75],v[200:203]\n"
"v_cvt_pk_bf16_f32 v161,v162,v163\n"
"v_cvt_pk_bf16_f32 v162,v164,v165\n"
"v_cvt_pk_bf16_f32 v163,v166,v167\n"
"v_mfma_f32_16x16x32_bf16 v[204:207],a[108:111],v[72:75],v[204:207]\n"
"ds_write_b128 v113,v[160:163] offset:4096\n"
"v_cvt_pk_bf16_f32 v168,v168,v169\n"
"v_cvt_pk_bf16_f32 v169,v170,v171\n"
"v_mfma_f32_16x16x32_bf16 v[208:211],a[112:115],v[72:75],v[208:211]\n"
"buffer_load_dwordx4 a[44:47],v112,s[24:27],s82 offen offset:3072 sc1\n"
"v_cvt_pk_bf16_f32 v170,v172,v173\n"
"v_cvt_pk_bf16_f32 v171,v174,v175\n"
"v_mfma_f32_16x16x32_bf16 v[212:215],a[116:119],v[72:75],v[212:215]\n"
"ds_write_b128 v113,v[168:171] offset:5120\n"
"v_cvt_pk_bf16_f32 v176,v176,v177\n"
"v_cvt_pk_bf16_f32 v177,v178,v179\n"
"v_mfma_f32_16x16x32_bf16 v[216:219],a[120:123],v[72:75],v[216:219]\n"
"v_cvt_pk_bf16_f32 v178,v180,v181\n"
"v_cvt_pk_bf16_f32 v179,v182,v183\n"
"v_mfma_f32_16x16x32_bf16 v[220:223],a[124:127],v[72:75],v[220:223]\n"
"ds_write_b128 v113,v[176:179] offset:6144\n"
"v_cvt_pk_bf16_f32 v184,v184,v185\n"
"v_cvt_pk_bf16_f32 v185,v186,v187\n"
"v_cvt_pk_bf16_f32 v186,v188,v189\n"
"v_cvt_pk_bf16_f32 v187,v190,v191\n"
"v_mfma_f32_16x16x32_bf16 v[224:227],a[96:99],v[76:79],v[224:227]\n"
"ds_write_b128 v113,v[184:187] offset:7168\n"
"v_mfma_f32_16x16x32_bf16 a[140:143],a[144:147],v[76:79],a[140:143]\n"
"s_sub_u32 s82,s3,131072\n"
"ds_write_b64 v116,v[100:101] offset:512\n"
"buffer_load_dwordx4 v[128:131],v112,s[24:27],s82 offen offset:0 sc1\n"
"buffer_load_dwordx4 v[132:135],v112,s[24:27],s82 offen offset:1024 sc1\n"
"v_mfma_f32_16x16x32_bf16 v[228:231],a[100:103],v[76:79],v[228:231]\n"
"buffer_load_dwordx4 v[136:139],v112,s[24:27],s82 offen offset:2048 sc1\n"
"buffer_load_dwordx4 v[140:143],v112,s[24:27],s82 offen offset:3072 sc1\n"
"s_sub_u32 s82,s3,147456\n"
"v_mfma_f32_16x16x32_bf16 v[232:235],a[104:107],v[76:79],v[232:235]\n"
"buffer_load_dwordx4 v[144:147],v112,s[24:27],s82 offen offset:0 sc1\n"
"buffer_load_dwordx4 v[148:151],v112,s[24:27],s82 offen offset:1024 sc1\n"
"buffer_load_dwordx4 v[152:155],v112,s[24:27],s82 offen offset:2048 sc1\n"
"v_mfma_f32_16x16x32_bf16 v[236:239],a[108:111],v[76:79],v[236:239]\n"
"buffer_load_dwordx4 v[156:159],v112,s[24:27],s82 offen offset:3072 sc1\n"
"v_mfma_f32_16x16x32_bf16 v[240:243],a[112:115],v[76:79],v[240:243]\n"
"v_mfma_f32_16x16x32_bf16 v[244:247],a[116:119],v[76:79],v[244:247]\n"
"v_mfma_f32_16x16x32_bf16 v[248:251],a[120:123],v[76:79],v[248:251]\n"
"v_mfma_f32_16x16x32_bf16 v[252:255],a[124:127],v[76:79],v[252:255]\n"
".Lfk_endB:\n"
"v_lshrrev_b32 v70,4,v112\n"
"v_and_b32 v71,15,v70\n"
"v_lshrrev_b32 v70,4,v70\n"
"v_lshlrev_b32 v70,2,v70\n"
"v_sub_u32 v70,v71,v70\n"
"v_mov_b32 v71,0x3f80\n"
"v_cmp_eq_u32 vcc,0,v70\n"
"v_mov_b32 v68,0\n"
"v_mov_b32 v64,0\n"
"v_cndmask_b32 v66,0,v71,vcc\n"
"v_cmp_eq_u32 vcc,1,v70\n"
"v_mov_b32 v65,0\n"
"v_mov_b32 v72,0x3f800000\n"
"v_cndmask_b32 v66,v66,v72,vcc\n"
"v_cmp_eq_u32 vcc,2,v70\n"
"v_mov_b32 v69,0\n"
"s_nop 0\n"
"v_cndmask_b32 v67,0,v71,vcc\n"
"v_cmp_eq_u32 vcc,3,v70\n"
"s_nop 1\n"
"v_cndmask_b32 v67,v67,v72,vcc\n"
"s_sub_u32 s82,s3,163840\n"
"buffer_load_dwordx4 v[0:3],v112,s[24:27],s82 offen offset:0 sc1\n"
"buffer_load_dwordx4 v[4:7],v112,s[24:27],s82 offen offset:1024 sc1\n"
"buffer_load_dwordx4 v[8:11],v112,s[24:27],s82 offen offset:2048 sc1\n"
"buffer_load_dwordx4 v[12:15],v112,s[24:27],s82 offen offset:3072 sc1\n"
"s_sub_u32 s82,s3,180224\n"
"buffer_load_dwordx4 v[160:163],v112,s[24:27],s82 offen offset:0 sc1\n"
"buffer_load_dwordx4 v[164:167],v112,s[24:27],s82 offen offset:1024 sc1\n"
"buffer_load_dwordx4 v[168:171],v112,s[24:27],s82 offen offset:2048 sc1\n"
"buffer_load_dwordx4 v[172:175],v112,s[24:27],s82 offen offset:3072 sc1\n"
"s_sub_u32 s82,s3,196608\n"
"buffer_load_dwordx4 v[176:179],v112,s[24:27],s82 offen offset:0 sc1\n"
"buffer_load_dwordx4 v[180:183],v112,s[24:27],s82 offen offset:1024 sc1\n"
"buffer_load_dwordx4 v[184:187],v112,s[24:27],s82 offen offset:2048 sc1\n"
"buffer_load_dwordx4 v[188:191],v112,s[24:27],s82 offen offset:3072 sc1\n"
"s_waitcnt lgkmcnt(0)\n"
"s_barrier\n"
"v_xor_b32 v113,32768,v113\n"
"v_xor_b32 v116,2048,v116\n"
"ds_read_b64 v[98:99],v116 offset:0\n"
"ds_read_b128 a[64:67],v113 offset:0\n"
"ds_read_b128 a[68:71],v113 offset:1024\n"
"ds_read_b128 a[72:75],v113 offset:2048\n"
"ds_read_b128 a[76:79],v113 offset:3072\n"
"ds_read_b64 v[102:103],v116 offset:512\n"
"ds_read_b128 a[80:83],v113 offset:4096\n"
"ds_read_b128 a[84:87],v113 offset:5120\n"
"ds_read_b128 a[88:91],v113 offset:6144\n"
"ds_read_b128 a[92:95],v113 offset:7168\n"
"s_waitcnt lgkmcnt(5)\n"
"v_cmp_neq_f32 vcc,v98,v104\n"
"s_nop 0\n"
"s_cbranch_vccnz .Lfk_ms_b0\n"
".Lfk_mr_b0:\n"
"v_mfma_f32_16x16x32_bf16 v[192:195],v[66:69],a[64:67],v[192:195]\n"
"v_mfma_f32_16x16x32_bf16 v[196:199],v[64:67],a[64:67],v[196:199]\n"
"v_mfma_f32_16x16x32_bf16 v[200:203],v[66:69],a[68:71],v[200:203]\n"
"v_mfma_f32_16x16x32_bf16 v[204:207],v[64:67],a[68:71],v[204:207]\n"
"v_mfma_f32_16x16x32_bf16 v[208:211],v[66:69],a[72:75],v[208:211]\n"
"v_mfma_f32_16x16x32_bf16 v[212:215],v[64:67],a[72:75],v[212:215]\n"
"v_mfma_f32_16x16x32_bf16 v[216:219],v[66:69],a[76:79],v[216:219]\n"
"v_mfma_f32_16x16x32_bf16 v[220:223],v[64:67],a[76:79],v[220:223]\n"
"s_waitcnt lgkmcnt(0)\n"
"v_cmp_neq_f32 vcc,v102,v108\n"
"s_nop 0\n"
"s_cbranch_vccnz .Lfk_ms_b1\n"
".Lfk_mr_b1:\n"
"v_mfma_f32_16x16x32_bf16 v[224:227],v[66:69],a[80:83],v[224:227]\n"
"v_mfma_f32_16x16x32_bf16 v[228:231],v[64:67],a[80:83],v[228:231]\n"
"v_accvgpr_read_b32 v122,a136\n"
"v_add_f32 v122,v122,v99\n"
"v_mfma_f32_16x16x32_bf16 v[232:235],v[66:69],a[84:87],v[232:235]\n"
"v_rcp_f32 v122,v122\n"
"v_mfma_f32_16x16x32_bf16 v[236:239],v[64:67],a[84:87],v[236:239]\n"
"v_pk_mul_f32 v[192:193],v[192:193],v[122:123] op_sel_hi:[1,0]\n"
"v_pk_mul_f32 v[194:195],v[194:195],v[122:123] op_sel_hi:[1,0]\n"
"v_pk_mul_f32 v[196:197],v[196:197],v[122:123] op_sel_hi:[1,0]\n"
"v_pk_mul_f32 v[198:199],v[198:199],v[122:123] op_sel_hi:[1,0]\n"
"v_mfma_f32_16x16x32_bf16 v[240:243],v[66:69],a[88:91],v[240:243]\n"
"v_cvt_pk_bf16_f32 v192,v192,v193\n"
"v_cvt_pk_bf16_f32 v193,v194,v195\n"
"v_mfma_f32_16x16x32_bf16 v[244:247],v[64:67],a[88:91],v[244:247]\n"
"v_cvt_pk_bf16_f32 v194,v196,v197\n"
"v_cvt_pk_bf16_f32 v195,v198,v199\n"
"v_mfma_f32_16x16x32_bf16 v[248:251],v[66:69],a[92:95],v[248:251]\n"
"buffer_store_dwordx4 v[192:195],v112,s[24:27],s70 offen offset:0 sc1\n"
"v_pk_mul_f32 v[200:201],v[200:201],v[122:123] op_sel_hi:[1,0]\n"
"v_pk_mul_f32 v[202:203],v[202:203],v[122:123] op_sel_hi:[1,0]\n"
"v_pk_mul_f32 v[204:205],v[204:205],v[122:123] op_sel_hi:[1,0]\n"
"v_pk_mul_f32 v[206:207],v[206:207],v[122:123] op_sel_hi:[1,0]\n"
"v_mfma_f32_16x16x32_bf16 v[252:255],v[64:67],a[92:95],v[252:255]\n"
"v_cvt_pk_bf16_f32 v200,v200,v201\n"
"v_cvt_pk_bf16_f32 v201,v202,v203\n"
"v_cvt_pk_bf16_f32 v202,v204,v205\n"
"v_cvt_pk_bf16_f32 v203,v206,v207\n"
"v_pk_mul_f32 v[208:209],v[208:209],v[122:123] op_sel_hi:[1,0]\n"
"v_pk_mul_f32 v[210:211],v[210:211],v[122:123] op_sel_hi:[1,0]\n"
"v_pk_mul_f32 v[212:213],v[212:213],v[122:123] op_sel_hi:[1,0]\n"
"v_pk_mul_f32 v[214:215],v[214:215],v[122:123] op_sel_hi:[1,0]\n"
"v_cvt_pk_bf16_f32 v208,v208,v209\n"
"v_cvt_pk_bf16_f32 v209,v210,v211\n"
"v_cvt_pk_bf16_f32 v210,v212,v213\n"
"v_cvt_pk_bf16_f32 v211,v214,v215\n"
"buffer_store_dwordx4 v[200:203],v112,s[24:27],s70 offen offset:1024 sc1\n"
"buffer_store_dwordx4 v[208:211],v112,s[24:27],s70 offen offset:2048 sc1\n"
"v_pk_mul_f32 v[216:217],v[216:217],v[122:123] op_sel_hi:[1,0]\n"
"v_pk_mul_f32 v[218:219],v[218:219],v[122:123] op_sel_hi:[1,0]\n"
"v_pk_mul_f32 v[220:221],v[220:221],v[122:123] op_sel_hi:[1,0]\n"
"v_pk_mul_f32 v[222:223],v[222:223],v[122:123] op_sel_hi:[1,0]\n"
"v_cvt_pk_bf16_f32 v216,v216,v217\n"
"v_cvt_pk_bf16_f32 v217,v218,v219\n"
"v_cvt_pk_bf16_f32 v218,v220,v221\n"
"v_cvt_pk_bf16_f32 v219,v222,v223\n"
"buffer_store_dwordx4 v[216:219],v112,s[24:27],s70 offen offset:3072 sc1\n"
"s_add_u32 s70,s70,262144\n"
"s_branch .Lfk_p1\n"
".Lfk_tailB7:\n"
"s_waitcnt vmcnt(11) lgkmcnt(0)\n"
"s_barrier\n"
"v_mfma_f32_16x16x32_bf16 v[0:3],a[64:67],a[0:3],v[96:99]\n"
"ds_read_b128 a[96:99],v114 offset:24576\n"
"ds_read_b128 a[100:103],v114 offset:25600\n"
"ds_read_b128 a[104:107],v114 offset:26624\n"
"v_mfma_f32_16x16x32_bf16 v[0:3],a[68:71],a[4:7],v[0:3]\n"
"ds_read_b128 a[108:111],v114 offset:27648\n"
"ds_read_b128 a[112:115],v114 offset:28672\n"
"ds_read_b128 a[116:119],v114 offset:29696\n"
"v_mfma_f32_16x16x32_bf16 v[0:3],a[72:75],a[8:11],v[0:3]\n"
"ds_read_b128 a[120:123],v114 offset:30720\n"
"ds_read_b128 a[124:127],v114 offset:31744\n"
"v_add_f32 v16,0x80000000,v126\n"
"v_mfma_f32_16x16x32_bf16 v[0:3],a[76:79],a[12:15],v[0:3]\n"
"v_min_f32 v16,v16,v104\n"
"v_add_f32 v17,0xf149f2ca,v126\n"
"v_min_f32 v17,v17,v105\n"
"v_mfma_f32_16x16x32_bf16 v[4:7],a[92:95],a[12:15],v[96:99]\n"
"v_add_f32 v18,0xf1c9f2ca,v126\n"
"v_min_f32 v18,v18,v106\n"
"v_add_f32 v19,0xf2177617,v126\n"
"v_mfma_f32_16x16x32_bf16 v[4:7],a[88:91],a[8:11],v[4:7]\n"
"v_min_f32 v19,v19,v107\n"
"v_add_f32 v20,0xf349f2ca,v126\n"
"v_mfma_f32_16x16x32_bf16 v[4:7],a[84:87],a[4:7],v[4:7]\n"
"v_add_f32 v21,0xf35691f6,v126\n"
"v_min_f32 v21,v21,v105\n"
"v_add_f32 v22,0xf3633123,v126\n"
"v_mfma_f32_16x16x32_bf16 v[4:7],a[80:83],a[0:3],v[4:7]\n"
"s_waitcnt lgkmcnt(0)\n"
"v_min_f32 v22,v22,v106\n"
"v_add_f32 v23,0xf36fd050,v126\n"
"v_mfma_f32_16x16x32_bf16 v[192:195],a[96:99],v[72:75],v[192:195]\n"
"v_exp_f32 v56,v56\n"
"v_mfma_f32_16x16x32_bf16 a[136:139],a[144:147],v[72:75],a[136:139]\n"
"v_exp_f32 v57,v57\n"
"v_mfma_f32_16x16x32_bf16 v[196:199],a[100:103],v[72:75],v[196:199]\n"
"v_min_f32 v20,v20,v104\n"
"v_cvt_pk_bf16_f32 v92,v56,v57\n"
"v_mfma_f32_16x16x32_bf16 v[200:203],a[104:107],v[72:75],v[200:203]\n"
"v_max3_f32 v118,v0,v1,v2\n"
"v_max3_f32 v118,v118,v3,v4\n"
"v_mfma_f32_16x16x32_bf16 v[204:207],a[108:111],v[72:75],v[204:207]\n"
"v_max3_f32 v118,v118,v5,v6\n"
"v_max_f32 v118,v118,v7\n"
"v_mfma_f32_16x16x32_bf16 v[208:211],a[112:115],v[72:75],v[208:211]\n"
"v_min_f32 v23,v23,v107\n"
"v_mov_b32 v120,v118\n"
"v_mfma_f32_16x16x32_bf16 v[212:215],a[116:119],v[72:75],v[212:215]\n"
"v_exp_f32 v58,v58\n"
"v_mfma_f32_16x16x32_bf16 v[216:219],a[120:123],v[72:75],v[216:219]\n"
"v_mfma_f32_16x16x32_bf16 v[220:223],a[124:127],v[72:75],v[220:223]\n"
"v_exp_f32 v59,v59\n"
"v_mfma_f32_16x16x32_bf16 v[224:227],a[96:99],v[76:79],v[224:227]\n"
"v_cvt_pk_bf16_f32 v93,v58,v59\n"
"v_mfma_f32_16x16x32_bf16 a[140:143],a[144:147],v[76:79],a[140:143]\n"
"v_accvgpr_read_b32 v122,a136\n"
"v_mfma_f32_16x16x32_bf16 v[228:231],a[100:103],v[76:79],v[228:231]\n"
"v_rcp_f32 v122,v122\n"
"v_mfma_f32_16x16x32_bf16 v[232:235],a[104:107],v[76:79],v[232:235]\n"
"v_pk_mul_f32 v[192:193],v[192:193],v[122:123] op_sel_hi:[1,0]\n"
"v_pk_mul_f32 v[194:195],v[194:195],v[122:123] op_sel_hi:[1,0]\n"
"v_pk_mul_f32 v[196:197],v[196:197],v[122:123] op_sel_hi:[1,0]\n"
"v_pk_mul_f32 v[198:199],v[198:199],v[122:123] op_sel_hi:[1,0]\n"
"v_mfma_f32_16x16x32_bf16 v[236:239],a[108:111],v[76:79],v[236:239]\n"
"v_cvt_pk_bf16_f32 v192,v192,v193\n"
"v_cvt_pk_bf16_f32 v193,v194,v195\n"
"v_mfma_f32_16x16x32_bf16 v[240:243],a[112:115],v[76:79],v[240:243]\n"
"v_cvt_pk_bf16_f32 v194,v196,v197\n"
"v_cvt_pk_bf16_f32 v195,v198,v199\n"
"v_mfma_f32_16x16x32_bf16 v[244:247],a[116:119],v[76:79],v[244:247]\n"
"buffer_store_dwordx4 v[192:195],v112,s[24:27],s70 offen offset:0 sc1\n"
"v_pk_mul_f32 v[200:201],v[200:201],v[122:123] op_sel_hi:[1,0]\n"
"v_pk_mul_f32 v[202:203],v[202:203],v[122:123] op_sel_hi:[1,0]\n"
"v_mfma_f32_16x16x32_bf16 v[248:251],a[120:123],v[76:79],v[248:251]\n"
"v_exp_f32 v60,v60\n"
"v_cvt_pk_bf16_f32 v200,v200,v201\n"
"v_cvt_pk_bf16_f32 v201,v202,v203\n"
"v_pk_mul_f32 v[204:205],v[204:205],v[122:123] op_sel_hi:[1,0]\n"
"v_pk_mul_f32 v[206:207],v[206:207],v[122:123] op_sel_hi:[1,0]\n"
"v_mfma_f32_16x16x32_bf16 v[252:255],a[124:127],v[76:79],v[252:255]\n"
"v_cvt_pk_bf16_f32 v202,v204,v205\n"
"v_cvt_pk_bf16_f32 v203,v206,v207\n"
"v_mfma_f32_16x16x32_bf16 v[12:15],a[80:83],a[16:19],v[100:103]\n"
"buffer_store_dwordx4 v[200:203],v112,s[24:27],s70 offen offset:1024 sc1\n"
"v_pk_mul_f32 v[208:209],v[208:209],v[122:123] op_sel_hi:[1,0]\n"
"v_pk_mul_f32 v[210:211],v[210:211],v[122:123] op_sel_hi:[1,0]\n"
"v_pk_mul_f32 v[212:213],v[212:213],v[122:123] op_sel_hi:[1,0]\n"
"v_pk_mul_f32 v[214:215],v[214:215],v[122:123] op_sel_hi:[1,0]\n"
"v_mfma_f32_16x16x32_bf16 v[12:15],a[84:87],a[20:23],v[12:15]\n"
"v_exp_f32 v61,v61\n"
"v_mfma_f32_16x16x32_bf16 v[12:15],a[88:91],a[24:27],v[12:15]\n"
"v_cvt_pk_bf16_f32 v94,v60,v61\n"
"v_cvt_pk_bf16_f32 v208,v208,v209\n"
"v_mfma_f32_16x16x32_bf16 v[12:15],a[92:95],a[28:31],v[12:15]\n"
"v_cvt_pk_bf16_f32 v209,v210,v211\n"
"v_cvt_pk_bf16_f32 v210,v212,v213\n"
"v_cvt_pk_bf16_f32 v211,v214,v215\n"
"v_mfma_f32_16x16x32_bf16 v[8:11],a[76:79],a[28:31],v[100:103]\n"
"buffer_store_dwordx4 v[208:211],v112,s[24:27],s70 offen offset:2048 sc1\n"
"v_pk_mul_f32 v[216:217],v[216:217],v[122:123] op_sel_hi:[1,0]\n"
"v_pk_mul_f32 v[218:219],v[218:219],v[122:123] op_sel_hi:[1,0]\n"
"v_mfma_f32_16x16x32_bf16 v[8:11],a[72:75],a[24:27],v[8:11]\n"
"v_cvt_pk_bf16_f32 v216,v216,v217\n"
"v_cvt_pk_bf16_f32 v217,v218,v219\n"
"v_pk_mul_f32 v[220:221],v[220:221],v[122:123] op_sel_hi:[1,0]\n"
"v_pk_mul_f32 v[222:223],v[222:223],v[122:123] op_sel_hi:[1,0]\n"
"v_mfma_f32_16x16x32_bf16 v[8:11],a[68:71],a[20:23],v[8:11]\n"
"v_cvt_pk_bf16_f32 v218,v220,v221\n"
"v_cvt_pk_bf16_f32 v219,v222,v223\n"
"v_mfma_f32_16x16x32_bf16 v[8:11],a[64:67],a[16:19],v[8:11]\n"
"buffer_store_dwordx4 v[216:219],v112,s[24:27],s70 offen offset:3072 sc1\n"
"v_mfma_f32_16x16x32_bf16 v[24:27],a[64:67],a[48:51],v[108:111]\n"
"s_add_u32 s70,s70,262144\n"
"v_exp_f32 v62,v62\n"
"v_mfma_f32_16x16x32_bf16 v[24:27],a[68:71],a[52:55],v[24:27]\n"
"v_exp_f32 v63,v63\n"
"v_mfma_f32_16x16x32_bf16 v[24:27],a[72:75],a[56:59],v[24:27]\n"
"s_nop 0\n"
"v_max3_f32 v119,v8,v9,v10\n"
"v_mfma_f32_16x16x32_bf16 v[24:27],a[76:79],a[60:63],v[24:27]\n"
"v_max3_f32 v119,v119,v11,v12\n"
"v_max3_f32 v119,v119,v13,v14\n"
"v_mfma_f32_16x16x32_bf16 v[28:31],a[92:95],a[60:63],v[108:111]\n"
"v_max_f32 v119,v119,v15\n"
"v_mfma_f32_16x16x32_bf16 v[28:31],a[88:91],a[56:59],v[28:31]\n"
"v_min_f32 v120,v120,v119\n"
"v_max_f32 v118,v118,v119\n"
"v_mfma_f32_16x16x32_bf16 v[28:31],a[84:87],a[52:55],v[28:31]\n"
"v_mfma_f32_16x16x32_bf16 v[28:31],a[80:83],a[48:51],v[28:31]\n"
"v_cvt_pk_bf16_f32 v95,v62,v63\n"
"v_mfma_f32_16x16x32_bf16 v[20:23],a[80:83],a[32:35],v[20:23]\n"
"v_mfma_f32_16x16x32_bf16 v[20:23],a[84:87],a[36:39],v[20:23]\n"
"v_mfma_f32_16x16x32_bf16 v[20:23],a[88:91],a[40:43],v[20:23]\n"
"v_max3_f32 v119,v24,v25,v26\n"
"v_mfma_f32_16x16x32_bf16 v[20:23],a[92:95],a[44:47],v[20:23]\n"
"s_nop 1\n"
"v_max3_f32 v119,v119,v27,v28\n"
"v_mfma_f32_16x16x32_bf16 v[16:19],a[76:79],a[44:47],v[16:19]\n"
"v_max3_f32 v119,v119,v29,v30\n"
"v_max_f32 v119,v119,v31\n"
"v_mfma_f32_16x16x32_bf16 v[16:19],a[72:75],a[40:43],v[16:19]\n"
"v_mfma_f32_16x16x32_bf16 v[16:19],a[68:71],a[36:39],v[16:19]\n"
"v_mfma_f32_16x16x32_bf16 v[16:19],a[64:67],a[32:35],v[16:19]\n"
"ds_read_b128 a[64:67],v114 offset:57344\n"
"ds_read_b128 a[68:71],v114 offset:58368\n"
"ds_read_b128 a[72:75],v114 offset:59392\n"
"v_max_f32 v118,v118,v119\n"
"v_min_f32 v120,v120,v119\n"
"v_cmp_gt_f32 s[96:97],s16,v120\n"
"s_nop 1\n"
"v_max3_f32 v119,v16,v17,v18\n"
"v_max3_f32 v119,v119,v19,v20\n"
"v_max3_f32 v119,v119,v21,v22\n"
"v_max_f32 v119,v119,v23\n"
"v_cmp_gt_f32 s[28:29],s16,v119\n"
"v_max_f32 v118,v118,v119\n"
"v_cmp_lt_f32 vcc,s93,v118\n"
"ds_read_b128 a[76:79],v114 offset:60416\n"
"ds_read_b128 a[80:83],v114 offset:61440\n"
"ds_read_b128 a[84:87],v114 offset:62464\n"
"ds_read_b128 a[88:91],v114 offset:63488\n"
"ds_read_b128 a[92:95],v114 offset:64512\n"
"v_mov_b32 v114,v112\n"
"s_sub_u32 s82,s3,32768\n"
"buffer_load_dwordx4 a[0:3],v112,s[24:27],s82 offen offset:0 sc1\n"
"buffer_load_dwordx4 a[4:7],v112,s[24:27],s82 offen offset:1024 sc1\n"
"buffer_load_dwordx4 a[8:11],v112,s[24:27],s82 offen offset:2048 sc1\n"
"buffer_load_dwordx4 a[12:15],v112,s[24:27],s82 offen offset:3072 sc1\n"
"s_sub_u32 s82,s3,49152\n"
"buffer_load_dwordx4 a[16:19],v112,s[24:27],s82 offen offset:0 sc1\n"
"buffer_load_dwordx4 a[20:23],v112,s[24:27],s82 offen offset:1024 sc1\n"
"buffer_load_dwordx4 a[24:27],v112,s[24:27],s82 offen offset:2048 sc1\n"
"buffer_load_dwordx4 a[28:31],v112,s[24:27],s82 offen offset:3072 sc1\n"
"s_and_b32 s28,s28,0xffff\n"
"s_or_b32 s96,s96,s28\n"
"s_or_b64 s[96:97],s[96:97],vcc\n"
"s_cbranch_scc1 .Lfk_cs_c3\n"
".Lfk_cr_c3:\n"
"s_waitcnt vmcnt(13) lgkmcnt(0)\n"
"s_barrier\n"
"v_mfma_f32_16x16x32_bf16 v[224:227],a[64:67],v[92:95],v[224:227]\n"
"ds_read_b128 a[96:99],v114 offset:40960\n"
"v_exp_f32 v0,v0\n"
"v_mfma_f32_16x16x32_bf16 a[140:143],a[144:147],v[92:95],a[140:143]\n"
"ds_read_b128 a[100:103],v114 offset:41984\n"
"v_exp_f32 v1,v1\n"
"v_mfma_f32_16x16x32_bf16 v[228:231],a[68:71],v[92:95],v[228:231]\n"
"ds_read_b128 a[104:107],v114 offset:43008\n"
"v_exp_f32 v2,v2\n"
"v_mfma_f32_16x16x32_bf16 v[232:235],a[72:75],v[92:95],v[232:235]\n"
"ds_read_b128 a[108:111],v114 offset:44032\n"
"v_exp_f32 v3,v3\n"
"v_mfma_f32_16x16x32_bf16 v[236:239],a[76:79],v[92:95],v[236:239]\n"
"ds_read_b128 a[112:115],v114 offset:45056\n"
"v_exp_f32 v4,v4\n"
"v_mfma_f32_16x16x32_bf16 v[240:243],a[80:83],v[92:95],v[240:243]\n"
"ds_read_b128 a[116:119],v114 offset:46080\n"
"v_exp_f32 v5,v5\n"
"v_mfma_f32_16x16x32_bf16 v[244:247],a[84:87],v[92:95],v[244:247]\n"
"ds_read_b128 a[120:123],v114 offset:47104\n"
"v_exp_f32 v6,v6\n"
"v_mfma_f32_16x16x32_bf16 v[248:251],a[88:91],v[92:95],v[248:251]\n"
"v_exp_f32 v7,v7\n"
"v_mfma_f32_16x16x32_bf16 v[252:255],a[92:95],v[92:95],v[252:255]\n"
"v_exp_f32 v8,v8\n"
"v_accvgpr_read_b32 v119,a150\n"
"v_accvgpr_read_b32 v120,a151\n"
"v_xor_b32 v119,s80,v119\n"
"v_xor_b32 v120,s81,v120\n"
"v_or_b32 v119,v119,v120\n"
"v_cmp_ne_u32 s[14:15],0,v119\n"
"s_add_u32 s18,s8,1032192\n"
"s_cmp_lg_u32 s14,0\n"
"s_cselect_b32 s18,2146435072,s18\n"
"s_add_u32 m0,s30,0\n"
"ds_read_b128 a[124:127],v114 offset:48128\n"
"buffer_load_dwordx4 v112,s[24:27],s18 offen offset:0 sc1 lds\n"
"buffer_load_dwordx4 v112,s[24:27],s18 offen offset:1024 sc1 lds\n"
"buffer_load_dwordx4 v112,s[24:27],s18 offen offset:2048 sc1 lds\n"
"buffer_load_dwordx4 v112,s[24:27],s18 offen offset:3072 sc1 lds\n"
"s_add_u32 m0,m0,4096\n"
"s_nop 0\n"
"buffer_load_dwordx4 v117,s[24:27],s18 offen offset:0 sc1 lds\n"
"buffer_load_dwordx4 v117,s[24:27],s18 offen offset:1024 sc1 lds\n"
"buffer_load_dwordx4 v117,s[24:27],s18 offen offset:2048 sc1 lds\n"
"buffer_load_dwordx4 v117,s[24:27],s18 offen offset:3072 sc1 lds\n"
"s_sub_u32 s82,s3,65536\n"
"buffer_load_dwordx4 v[32:35],v112,s[24:27],s82 offen offset:0 sc1\n"
"buffer_load_dwordx4 v[36:39],v112,s[24:27],s82 offen offset:1024 sc1\n"
"buffer_load_dwordx4 v[40:43],v112,s[24:27],s82 offen offset:2048 sc1\n"
"buffer_load_dwordx4 v[44:47],v112,s[24:27],s82 offen offset:3072 sc1\n"
"v_exp_f32 v9,v9\n"
"v_cvt_pk_bf16_f32 v64,v0,v1\n"
"v_exp_f32 v10,v10\n"
"v_cvt_pk_bf16_f32 v65,v2,v3\n"
"v_exp_f32 v11,v11\n"
"v_cvt_pk_bf16_f32 v66,v4,v5\n"
"v_exp_f32 v12,v12\n"
"v_cvt_pk_bf16_f32 v67,v6,v7\n"
"v_exp_f32 v13,v13\n"
"v_cvt_pk_bf16_f32 v68,v8,v9\n"
"v_exp_f32 v14,v14\n"
"v_cvt_pk_bf16_f32 v69,v10,v11\n"
"v_exp_f32 v15,v15\n"
"v_cvt_pk_bf16_f32 v70,v12,v13\n"
"v_exp_f32 v16,v16\n"
"v_cvt_pk_bf16_f32 v71,v14,v15\n"
"v_exp_f32 v17,v17\n"
"v_exp_f32 v18,v18\n"
"v_cvt_pk_bf16_f32 v72,v16,v17\n"
"v_exp_f32 v19,v19\n"
"v_exp_f32 v20,v20\n"
"v_cvt_pk_bf16_f32 v73,v18,v19\n"
"v_exp_f32 v21,v21\n"
"v_exp_f32 v22,v22\n"
"v_cvt_pk_bf16_f32 v74,v20,v21\n"
"v_exp_f32 v23,v23\n"
"v_exp_f32 v24,v24\n"
"v_cvt_pk_bf16_f32 v75,v22,v23\n"
"v_exp_f32 v25,v25\n"
"v_exp_f32 v26,v26\n"
"v_cvt_pk_bf16_f32 v76,v24,v25\n"
"v_exp_f32 v27,v27\n"
"v_exp_f32 v28,v28\n"
"v_cvt_pk_bf16_f32 v77,v26,v27\n"
"v_exp_f32 v29,v29\n"
"v_exp_f32 v30,v30\n"
"v_cvt_pk_bf16_f32 v78,v28,v29\n"
"v_exp_f32 v31,v31\n"
"s_nop 0\n"
"v_cvt_pk_bf16_f32 v79,v30,v31\n"
"v_accvgpr_read_b32 v122,a140\n"
"v_rcp_f32 v122,v122\n"
"s_nop 0\n"
"v_pk_mul_f32 v[224:225],v[224:225],v[122:123] op_sel_hi:[1,0]\n"
"v_pk_mul_f32 v[226:227],v[226:227],v[122:123] op_sel_hi:[1,0]\n"
"v_pk_mul_f32 v[228:229],v[228:229],v[122:123] op_sel_hi:[1,0]\n"
"v_pk_mul_f32 v[230:231],v[230:231],v[122:123] op_sel_hi:[1,0]\n"
"v_cvt_pk_bf16_f32 v224,v224,v225\n"
"v_cvt_pk_bf16_f32 v225,v226,v227\n"
"v_cvt_pk_bf16_f32 v226,v228,v229\n"
"v_cvt_pk_bf16_f32 v227,v230,v231\n"
"buffer_store_dwordx4 v[224:227],v112,s[24:27],s70 offen offset:0 sc1\n"
"v_pk_mul_f32 v[232:233],v[232:233],v[122:123] op_sel_hi:[1,0]\n"
"v_pk_mul_f32 v[234:235],v[234:235],v[122:123] op_sel_hi:[1,0]\n"
"v_pk_mul_f32 v[236:237],v[236:237],v[122:123] op_sel_hi:[1,0]\n"
"v_pk_mul_f32 v[238:239],v[238:239],v[122:123] op_sel_hi:[1,0]\n"
"v_cvt_pk_bf16_f32 v232,v232,v233\n"
"v_cvt_pk_bf16_f32 v233,v234,v235\n"
"v_cvt_pk_bf16_f32 v234,v236,v237\n"
"v_cvt_pk_bf16_f32 v235,v238,v239\n"
"buffer_store_dwordx4 v[232:235],v112,s[24:27],s70 offen offset:1024 sc1\n"
"v_pk_mul_f32 v[240:241],v[240:241],v[122:123] op_sel_hi:[1,0]\n"
"v_pk_mul_f32 v[242:243],v[242:243],v[122:123] op_sel_hi:[1,0]\n"
"v_pk_mul_f32 v[244:245],v[244:245],v[122:123] op_sel_hi:[1,0]\n"
"v_pk_mul_f32 v[246:247],v[246:247],v[122:123] op_sel_hi:[1,0]\n"
"v_cvt_pk_bf16_f32 v240,v240,v241\n"
"v_cvt_pk_bf16_f32 v241,v242,v243\n"
"v_cvt_pk_bf16_f32 v242,v244,v245\n"
"v_cvt_pk_bf16_f32 v243,v246,v247\n"
"buffer_store_dwordx4 v[240:243],v112,s[24:27],s70 offen offset:2048 sc1\n"
"v_pk_mul_f32 v[248:249],v[248:249],v[122:123] op_sel_hi:[1,0]\n"
"v_pk_mul_f32 v[250:251],v[250:251],v[122:123] op_sel_hi:[1,0]\n"
"v_pk_mul_f32 v[252:253],v[252:253],v[122:123] op_sel_hi:[1,0]\n"
"v_pk_mul_f32 v[254:255],v[254:255],v[122:123] op_sel_hi:[1,0]\n"
"v_cvt_pk_bf16_f32 v248,v248,v249\n"
"v_cvt_pk_bf16_f32 v249,v250,v251\n"
"v_cvt_pk_bf16_f32 v250,v252,v253\n"
"v_cvt_pk_bf16_f32 v251,v254,v255\n"
"buffer_store_dwordx4 v[248:251],v112,s[24:27],s70 offen offset:3072 sc1\n"
"s_add_u32 s70,s70,262144\n"
"s_mov_b32 s70,s68\n"
"s_load_dwordx2 s[36:37],s[0:1],0\n"
"s_load_dwordx2 s[44:45],s[0:1],80\n"
"s_load_dwordx2 s[48:49],s[0:1],88\n"
"s_lshl_b32 s82,s78,2\n"
"s_add_u32 s82,s82,65536\n"
"v_add_u32 v113,s82,v112\n"
"s_lshr_b32 s82,s78,2\n"
"s_add_u32 s82,s82,122880\n"
"v_lshrrev_b32 v115,1,v112\n"
"v_add_u32 v116,s82,v115\n"
"s_waitcnt lgkmcnt(0)\n"
"s_barrier\n"
"v_mfma_f32_16x16x32_bf16 v[128:131],a[96:99],v[64:67],0\n"
"s_add_u32 s83,s18,2097152\n"
"s_add_u32 m0,s30,32768\n"
"v_mfma_f32_16x16x32_bf16 a[128:131],a[144:147],v[64:67],0\n"
"buffer_load_dwordx4 v112,s[24:27],s83 offen offset:0 sc1 lds\n"
"v_mfma_f32_16x16x32_bf16 v[132:135],a[100:103],v[64:67],0\n"
"buffer_load_dwordx4 v112,s[24:27],s83 offen offset:1024 sc1 lds\n"
"v_mfma_f32_16x16x32_bf16 v[136:139],a[104:107],v[64:67],0\n"
"buffer_load_dwordx4 v112,s[24:27],s83 offen offset:2048 sc1 lds\n"
"v_mfma_f32_16x16x32_bf16 v[140:143],a[108:111],v[64:67],0\n"
"v_mfma_f32_16x16x32_bf16 v[144:147],a[112:115],v[64:67],0\n"
"buffer_load_dwordx4 v112,s[24:27],s83 offen offset:3072 sc1 lds\n"
"v_mfma_f32_16x16x32_bf16 v[148:151],a[116:119],v[64:67],0\n"
"s_add_u32 m0,m0,4096\n"
"v_mfma_f32_16x16x32_bf16 v[152:155],a[120:123],v[64:67],0\n"
"v_mfma_f32_16x16x32_bf16 v[156:159],a[124:127],v[64:67],0\n"
"buffer_load_dwordx4 v117,s[24:27],s83 offen offset:0 sc1 lds\n"
"v_mfma_f32_16x16x32_bf16 v[160:163],a[96:99],v[68:71],0\n"
"v_accvgpr_read_b32 v97,a128\n"
"v_cvt_pk_bf16_f32 v136,v136,v137\n"
"v_mfma_f32_16x16x32_bf16 a[132:135],a[144:147],v[68:71],0\n"
"buffer_load_dwordx4 v117,s[24:27],s83 offen offset:1024 sc1 lds\n"
"v_mfma_f32_16x16x32_bf16 v[164:167],a[100:103],v[68:71],0\n"
"v_cvt_pk_bf16_f32 v128,v128,v129\n"
"v_cvt_pk_bf16_f32 v129,v130,v131\n"
"v_mfma_f32_16x16x32_bf16 v[168:171],a[104:107],v[68:71],0\n"
"buffer_load_dwordx4 v117,s[24:27],s83 offen offset:2048 sc1 lds\n"
"v_cvt_pk_bf16_f32 v130,v132,v133\n"
"v_cvt_pk_bf16_f32 v131,v134,v135\n"
"v_mfma_f32_16x16x32_bf16 v[172:175],a[108:111],v[68:71],0\n"
"ds_write_b128 v113,v[128:131] offset:0\n"
"v_cvt_pk_bf16_f32 v137,v138,v139\n"
"v_mfma_f32_16x16x32_bf16 v[176:179],a[112:115],v[68:71],0\n"
"buffer_load_dwordx4 v117,s[24:27],s83 offen offset:3072 sc1 lds\n"
"v_cvt_pk_bf16_f32 v138,v140,v141\n"
"v_cvt_pk_bf16_f32 v139,v142,v143\n"
"v_mfma_f32_16x16x32_bf16 v[180:183],a[116:119],v[68:71],0\n"
"s_sub_u32 s82,s3,81920\n"
"ds_write_b128 v113,v[136:139] offset:1024\n"
"v_cvt_pk_bf16_f32 v144,v144,v145\n"
"v_mfma_f32_16x16x32_bf16 v[184:187],a[120:123],v[68:71],0\n"
"buffer_load_dwordx4 a[32:35],v112,s[24:27],s82 offen offset:0 sc1\n"
"v_cvt_pk_bf16_f32 v145,v146,v147\n"
"v_cvt_pk_bf16_f32 v146,v148,v149\n"
"v_cvt_pk_bf16_f32 v147,v150,v151\n"
"v_mfma_f32_16x16x32_bf16 v[188:191],a[124:127],v[68:71],0\n"
"ds_write_b128 v113,v[144:147] offset:2048\n"
"v_cvt_pk_bf16_f32 v152,v152,v153\n"
"v_mfma_f32_16x16x32_bf16 v[192:195],a[96:99],v[72:75],0\n"
"buffer_load_dwordx4 a[36:39],v112,s[24:27],s82 offen offset:1024 sc1\n"
"v_cvt_pk_bf16_f32 v153,v154,v155\n"
"v_cvt_pk_bf16_f32 v154,v156,v157\n"
"v_cvt_pk_bf16_f32 v155,v158,v159\n"
"v_mfma_f32_16x16x32_bf16 a[136:139],a[144:147],v[72:75],0\n"
"ds_write_b128 v113,v[152:155] offset:3072\n"
"ds_write_b64 v116,v[96:97] offset:0\n"
"v_mfma_f32_16x16x32_bf16 v[196:199],a[100:103],v[72:75],0\n"
"buffer_load_dwordx4 a[40:43],v112,s[24:27],s82 offen offset:2048 sc1\n"
"v_accvgpr_read_b32 v101,a132\n"
"v_cvt_pk_bf16_f32 v160,v160,v161\n"
"v_mfma_f32_16x16x32_bf16 v[200:203],a[104:107],v[72:75],0\n"
"v_cvt_pk_bf16_f32 v161,v162,v163\n"
"v_cvt_pk_bf16_f32 v162,v164,v165\n"
"v_cvt_pk_bf16_f32 v163,v166,v167\n"
"v_mfma_f32_16x16x32_bf16 v[204:207],a[108:111],v[72:75],0\n"
"ds_write_b128 v113,v[160:163] offset:4096\n"
"v_cvt_pk_bf16_f32 v168,v168,v169\n"
"v_cvt_pk_bf16_f32 v169,v170,v171\n"
"v_mfma_f32_16x16x32_bf16 v[208:211],a[112:115],v[72:75],0\n"
"buffer_load_dwordx4 a[44:47],v112,s[24:27],s82 offen offset:3072 sc1\n"
"v_cvt_pk_bf16_f32 v170,v172,v173\n"
"v_cvt_pk_bf16_f32 v171,v174,v175\n"
"v_mfma_f32_16x16x32_bf16 v[212:215],a[116:119],v[72:75],0\n"
"ds_write_b128 v113,v[168:171] offset:5120\n"
"v_cvt_pk_bf16_f32 v176,v176,v177\n"
"v_cvt_pk_bf16_f32 v177,v178,v179\n"
"v_mfma_f32_16x16x32_bf16 v[216:219],a[120:123],v[72:75],0\n"
"v_cvt_pk_bf16_f32 v178,v180,v181\n"
"v_cvt_pk_bf16_f32 v179,v182,v183\n"
"v_mfma_f32_16x16x32_bf16 v[220:223],a[124:127],v[72:75],0\n"
"ds_write_b128 v113,v[176:179] offset:6144\n"
"v_cvt_pk_bf16_f32 v184,v184,v185\n"
"v_cvt_pk_bf16_f32 v185,v186,v187\n"
"v_cvt_pk_bf16_f32 v186,v188,v189\n"
"v_cvt_pk_bf16_f32 v187,v190,v191\n"
"v_mfma_f32_16x16x32_bf16 v[224:227],a[96:99],v[76:79],0\n"
"ds_write_b128 v113,v[184:187] offset:7168\n"
"v_mfma_f32_16x16x32_bf16 a[140:143],a[144:147],v[76:79],0\n"
"s_sub_u32 s82,s3,131072\n"
"ds_write_b64 v116,v[100:101] offset:512\n"
"buffer_load_dwordx4 v[128:131],v112,s[24:27],s82 offen offset:0 sc1\n"
"buffer_load_dwordx4 v[132:135],v112,s[24:27],s82 offen offset:1024 sc1\n"
"v_mfma_f32_16x16x32_bf16 v[228:231],a[100:103],v[76:79],0\n"
"buffer_load_dwordx4 v[136:139],v112,s[24:27],s82 offen offset:2048 sc1\n"
"buffer_load_dwordx4 v[140:143],v112,s[24:27],s82 offen offset:3072 sc1\n"
"s_sub_u32 s82,s3,147456\n"
"v_mfma_f32_16x16x32_bf16 v[232:235],a[104:107],v[76:79],0\n"
"buffer_load_dwordx4 v[144:147],v112,s[24:27],s82 offen offset:0 sc1\n"
"buffer_load_dwordx4 v[148:151],v112,s[24:27],s82 offen offset:1024 sc1\n"
"buffer_load_dwordx4 v[152:155],v112,s[24:27],s82 offen offset:2048 sc1\n"
"v_mfma_f32_16x16x32_bf16 v[236:239],a[108:111],v[76:79],0\n"
"buffer_load_dwordx4 v[156:159],v112,s[24:27],s82 offen offset:3072 sc1\n"
"v_mfma_f32_16x16x32_bf16 v[240:243],a[112:115],v[76:79],0\n"
"v_mfma_f32_16x16x32_bf16 v[244:247],a[116:119],v[76:79],0\n"
"v_mfma_f32_16x16x32_bf16 v[248:251],a[120:123],v[76:79],0\n"
"v_mfma_f32_16x16x32_bf16 v[252:255],a[124:127],v[76:79],0\n"
"s_waitcnt vmcnt(20)\n"
"buffer_store_dwordx2 a[154:155],off,s[24:27],s69 sc1\n"
"s_branch .Lfk_endB\n"
".Lfk_head0:\n"
"s_mov_b32 s64,s61\n"
"s_cmp_eq_u32 s52,3\n"
"s_cselect_b32 s67,172032,0\n"
"s_waitcnt lgkmcnt(0)\n"
"s_barrier\n"
"v_mfma_f32_16x16x32_bf16 v[24:27],a[64:67],a[48:51],v[108:111]\n"
"v_add_f32 v0,0x80000000,v126\n"
"v_min_f32 v0,v0,v96\n"
"v_mfma_f32_16x16x32_bf16 v[24:27],a[68:71],a[52:55],v[24:27]\n"
"v_add_f32 v1,0xf149f2ca,v126\n"
"v_min_f32 v1,v1,v97\n"
"v_mfma_f32_16x16x32_bf16 v[24:27],a[72:75],a[56:59],v[24:27]\n"
"v_add_f32 v2,0xf1c9f2ca,v126\n"
"v_min_f32 v2,v2,v98\n"
"v_mfma_f32_16x16x32_bf16 v[24:27],a[76:79],a[60:63],v[24:27]\n"
"v_add_f32 v3,0xf2177617,v126\n"
"v_min_f32 v3,v3,v99\n"
"v_mfma_f32_16x16x32_bf16 v[28:31],a[92:95],a[60:63],v[108:111]\n"
"v_add_f32 v4,0xf349f2ca,v126\n"
"v_min_f32 v4,v4,v96\n"
"v_mfma_f32_16x16x32_bf16 v[28:31],a[88:91],a[56:59],v[28:31]\n"
"v_add_f32 v5,0xf35691f6,v126\n"
"v_min_f32 v5,v5,v97\n"
"v_mfma_f32_16x16x32_bf16 v[28:31],a[84:87],a[52:55],v[28:31]\n"
"v_add_f32 v6,0xf3633123,v126\n"
"v_min_f32 v6,v6,v98\n"
"v_mfma_f32_16x16x32_bf16 v[28:31],a[80:83],a[48:51],v[28:31]\n"
"v_add_f32 v7,0xf36fd050,v126\n"
"v_min_f32 v7,v7,v99\n"
"s_nop 1\n"
"v_mfma_f32_16x16x32_bf16 v[4:7],a[80:83],a[0:3],v[4:7]\n"
"s_andn2_b32 m0,s55,16384\n"
"s_add_u32 s54,s54,s22\n"
"v_mfma_f32_16x16x32_bf16 v[4:7],a[84:87],a[4:7],v[4:7]\n"
"ds_read_b128 a[96:99],v113 offset:16384\n"
"ds_read_b128 a[100:103],v113 offset:17408\n"
"v_mfma_f32_16x16x32_bf16 v[4:7],a[88:91],a[8:11],v[4:7]\n"
"ds_read_b128 a[104:107],v113 offset:18432\n"
"ds_read_b128 a[108:111],v113 offset:19456\n"
"v_mfma_f32_16x16x32_bf16 v[4:7],a[92:95],a[12:15],v[4:7]\n"
"ds_read_b128 a[112:115],v113 offset:20480\n"
"ds_read_b128 a[116:119],v113 offset:21504\n"
"v_mfma_f32_16x16x32_bf16 v[0:3],a[76:79],a[12:15],v[0:3]\n"
"ds_read_b128 a[120:123],v113 offset:22528\n"
"ds_read_b128 a[124:127],v113 offset:23552\n"
"v_mfma_f32_16x16x32_bf16 v[0:3],a[72:75],a[8:11],v[0:3]\n"
"buffer_load_dwordx4 v115,s[32:35],s54 offen sc1 lds\n"
"v_mfma_f32_16x16x32_bf16 v[0:3],a[68:71],a[4:7],v[0:3]\n"
"s_add_u32 m0,s67,0\n"
"v_max3_f32 v118,v24,v25,v26\n"
"v_mfma_f32_16x16x32_bf16 v[0:3],a[64:67],a[0:3],v[0:3]\n"
"buffer_load_dwordx4 v112,s[24:27],s64 offen offset:0 sc1 lds\n"
"v_mfma_f32_16x16x32_bf16 v[8:11],a[64:67],a[16:19],v[100:103]\n"
"buffer_load_dwordx4 v112,s[24:27],s64 offen offset:1024 sc1 lds\n"
"v_mfma_f32_16x16x32_bf16 v[8:11],a[68:71],a[20:23],v[8:11]\n"
"buffer_load_dwordx4 v112,s[24:27],s64 offen offset:2048 sc1 lds\n"
"v_mfma_f32_16x16x32_bf16 v[8:11],a[72:75],a[24:27],v[8:11]\n"
"buffer_load_dwordx4 v112,s[24:27],s64 offen offset:3072 sc1 lds\n"
"v_mfma_f32_16x16x32_bf16 v[8:11],a[76:79],a[28:31],v[8:11]\n"
"s_add_u32 m0,s67,4096\n"
"v_max3_f32 v118,v118,v27,v28\n"
"v_cvt_pk_bf16_f32 v192,v192,v193\n"
"v_mfma_f32_16x16x32_bf16 v[12:15],a[92:95],a[28:31],v[100:103]\n"
"buffer_load_dwordx4 v117,s[24:27],s64 offen offset:0 sc1 lds\n"
"v_mfma_f32_16x16x32_bf16 v[12:15],a[88:91],a[24:27],v[12:15]\n"
"buffer_load_dwordx4 v117,s[24:27],s64 offen offset:1024 sc1 lds\n"
"v_mfma_f32_16x16x32_bf16 v[12:15],a[84:87],a[20:23],v[12:15]\n"
"buffer_load_dwordx4 v117,s[24:27],s64 offen offset:2048 sc1 lds\n"
"v_mfma_f32_16x16x32_bf16 v[12:15],a[80:83],a[16:19],v[12:15]\n"
"buffer_load_dwordx4 v117,s[24:27],s64 offen offset:3072 sc1 lds\n"
"v_cvt_pk_bf16_f32 v193,v194,v195\n"
"v_cvt_pk_bf16_f32 v194,v196,v197\n"
"v_cvt_pk_bf16_f32 v195,v198,v199\n"
"v_cvt_pk_bf16_f32 v200,v200,v201\n"
"v_cvt_pk_bf16_f32 v201,v202,v203\n"
"v_cvt_pk_bf16_f32 v202,v204,v205\n"
"v_cvt_pk_bf16_f32 v203,v206,v207\n"
"ds_write_b128 v116,v[192:195] offset:32768\n"
"ds_write_b128 v116,v[200:203] offset:33792\n"
"v_cvt_pk_bf16_f32 v208,v208,v209\n"
"v_cvt_pk_bf16_f32 v209,v210,v211\n"
"v_cvt_pk_bf16_f32 v210,v212,v213\n"
"v_cvt_pk_bf16_f32 v211,v214,v215\n"
"v_cvt_pk_bf16_f32 v216,v216,v217\n"
"v_cvt_pk_bf16_f32 v217,v218,v219\n"
"v_cvt_pk_bf16_f32 v218,v220,v221\n"
"v_cvt_pk_bf16_f32 v219,v222,v223\n"
"ds_write_b128 v116,v[208:211] offset:34816\n"
"ds_write_b128 v116,v[216:219] offset:35840\n"
"ds_read_b128 a[32:35],v116 offset:32768\n"
"ds_read_b128 a[36:39],v116 offset:33792\n"
"ds_read_b128 a[40:43],v116 offset:34816\n"
"ds_read_b128 a[44:47],v116 offset:35840\n"
"s_waitcnt lgkmcnt(0)\n"
"v_mfma_f32_16x16x32_bf16 v[20:23],a[80:83],a[32:35],v[104:107]\n"
"s_add_u32 s64,s64,16384\n"
"v_max3_f32 v118,v118,v29,v30\n"
"v_mfma_f32_16x16x32_bf16 v[20:23],a[84:87],a[36:39],v[20:23]\n"
"v_max_f32 v118,v118,v31\n"
"v_mov_b32 v120,v118\n"
"v_mfma_f32_16x16x32_bf16 v[20:23],a[88:91],a[40:43],v[20:23]\n"
"v_max3_f32 v119,v0,v1,v2\n"
"v_max3_f32 v119,v119,v3,v4\n"
"v_mfma_f32_16x16x32_bf16 v[20:23],a[92:95],a[44:47],v[20:23]\n"
"buffer_store_dwordx4 a[224:227],v112,s[24:27],s56 offen offset:0 sc1\n"
"v_max3_f32 v119,v119,v5,v6\n"
"v_mfma_f32_16x16x32_bf16 v[16:19],a[76:79],a[44:47],v[104:107]\n"
"v_max_f32 v119,v119,v7\n"
"v_cmp_gt_f32 s[28:29],s16,v119\n"
"v_mfma_f32_16x16x32_bf16 v[16:19],a[72:75],a[40:43],v[16:19]\n"
"v_max_f32 v118,v118,v119\n"
"v_max3_f32 v119,v8,v9,v10\n"
"v_mfma_f32_16x16x32_bf16 v[16:19],a[68:71],a[36:39],v[16:19]\n"
"v_max3_f32 v119,v119,v11,v12\n"
"v_max3_f32 v119,v119,v13,v14\n"
"v_mfma_f32_16x16x32_bf16 v[16:19],a[64:67],a[32:35],v[16:19]\n"
"s_and_b32 s28,s28,0xffff\n"
"v_max_f32 v119,v119,v15\n"
"v_min_f32 v120,v120,v119\n"
"v_max_f32 v118,v118,v119\n"
"s_nop 3\n"
"v_max3_f32 v119,v16,v17,v18\n"
"v_max3_f32 v119,v119,v19,v20\n"
"v_max3_f32 v119,v119,v21,v22\n"
"v_max_f32 v119,v119,v23\n"
"v_min_f32 v120,v120,v119\n"
"v_max_f32 v118,v118,v119\n"
"v_cmp_gt_f32 s[96:97],s16,v120\n"
"v_cmp_lt_f32 vcc,s93,v118\n"
"s_or_b32 s96,s96,s28\n"
"s_or_b64 s[96:97],s[96:97],vcc\n"
"s_cbranch_scc1 .Lfk_cs_z3\n"
".Lfk_cr_z3:\n"
"s_waitcnt vmcnt(19) lgkmcnt(0)\n"
"s_barrier\n"
"v_mfma_f32_16x16x32_bf16 v[48:51],a[96:99],a[32:35],v[104:107]\n"
"v_add_f32 v40,0x80000000,v126\n"
"v_min_f32 v40,v40,v100\n"
"v_add_f32 v41,0xf149f2ca,v126\n"
"v_min_f32 v41,v41,v101\n"
"v_mfma_f32_16x16x32_bf16 v[48:51],a[100:103],a[36:39],v[48:51]\n"
"v_add_f32 v42,0xf1c9f2ca,v126\n"
"v_min_f32 v42,v42,v102\n"
"v_add_f32 v43,0xf2177617,v126\n"
"v_min_f32 v43,v43,v103\n"
"v_mfma_f32_16x16x32_bf16 v[48:51],a[104:107],a[40:43],v[48:51]\n"
"v_add_f32 v44,0xf349f2ca,v126\n"
"v_min_f32 v44,v44,v100\n"
"v_add_f32 v45,0xf35691f6,v126\n"
"v_min_f32 v45,v45,v101\n"
"v_mfma_f32_16x16x32_bf16 v[48:51],a[108:111],a[44:47],v[48:51]\n"
"v_add_f32 v46,0xf3633123,v126\n"
"v_min_f32 v46,v46,v102\n"
"v_add_f32 v47,0xf36fd050,v126\n"
"v_min_f32 v47,v47,v103\n"
"v_mfma_f32_16x16x32_bf16 v[52:55],a[124:127],a[44:47],v[104:107]\n"
"s_add_u32 m0,s55,4096\n"
"v_exp_f32 v0,v0\n"
"s_add_u32 s54,s54,s22\n"
"ds_read_b128 a[0:3],v116 offset:40960\n"
"v_exp_f32 v1,v1\n"
"v_mfma_f32_16x16x32_bf16 v[52:55],a[120:123],a[40:43],v[52:55]\n"
"buffer_load_dwordx4 v115,s[32:35],s54 offen sc1 lds\n"
"v_mfma_f32_16x16x32_bf16 v[52:55],a[116:119],a[36:39],v[52:55]\n"
"s_add_u32 m0,s67,16384\n"
"v_exp_f32 v2,v2\n"
"v_mfma_f32_16x16x32_bf16 v[52:55],a[112:115],a[32:35],v[52:55]\n"
"buffer_load_dwordx4 v112,s[24:27],s64 offen offset:0 sc1 lds\n"
"buffer_load_dwordx4 v112,s[24:27],s64 offen offset:1024 sc1 lds\n"
"v_exp_f32 v3,v3\n"
"v_exp_f32 v4,v4\n"
"v_mfma_f32_16x16x32_bf16 v[60:63],a[112:115],a[48:51],v[108:111]\n"
"buffer_load_dwordx4 v112,s[24:27],s64 offen offset:2048 sc1 lds\n"
"v_exp_f32 v5,v5\n"
"v_mfma_f32_16x16x32_bf16 v[60:63],a[116:119],a[52:55],v[60:63]\n"
"ds_read_b128 a[64:67],v113 offset:49152\n"
"ds_read_b128 a[68:71],v113 offset:50176\n"
"v_exp_f32 v6,v6\n"
"v_mfma_f32_16x16x32_bf16 v[60:63],a[120:123],a[56:59],v[60:63]\n"
"ds_read_b128 a[72:75],v113 offset:51200\n"
"ds_read_b128 a[76:79],v113 offset:52224\n"
"v_exp_f32 v7,v7\n"
"v_mfma_f32_16x16x32_bf16 v[60:63],a[124:127],a[60:63],v[60:63]\n"
"ds_read_b128 a[80:83],v113 offset:53248\n"
"ds_read_b128 a[84:87],v113 offset:54272\n"
"v_exp_f32 v8,v8\n"
"v_mfma_f32_16x16x32_bf16 v[56:59],a[108:111],a[60:63],v[108:111]\n"
"ds_read_b128 a[88:91],v113 offset:55296\n"
"ds_read_b128 a[92:95],v113 offset:56320\n"
"v_exp_f32 v9,v9\n"
"v_mfma_f32_16x16x32_bf16 v[56:59],a[104:107],a[56:59],v[56:59]\n"
"buffer_load_dwordx4 v112,s[24:27],s64 offen offset:3072 sc1 lds\n"
"v_mfma_f32_16x16x32_bf16 v[56:59],a[100:103],a[52:55],v[56:59]\n"
"s_add_u32 m0,s67,20480\n"
"v_exp_f32 v10,v10\n"
"v_mfma_f32_16x16x32_bf16 v[56:59],a[96:99],a[48:51],v[56:59]\n"
"buffer_load_dwordx4 v117,s[24:27],s64 offen offset:0 sc1 lds\n"
"buffer_load_dwordx4 v117,s[24:27],s64 offen offset:1024 sc1 lds\n"
"v_exp_f32 v11,v11\n"
"v_exp_f32 v12,v12\n"
"v_mfma_f32_16x16x32_bf16 v[40:43],a[96:99],a[16:19],v[40:43]\n"
"buffer_load_dwordx4 v117,s[24:27],s64 offen offset:2048 sc1 lds\n"
"v_exp_f32 v13,v13\n"
"v_mfma_f32_16x16x32_bf16 v[40:43],a[100:103],a[20:23],v[40:43]\n"
"buffer_store_dwordx4 a[168:171],v112,s[24:27],s3 offen offset:2048 sc1\n"
"v_cvt_pk_bf16_f32 v64,v0,v1\n"
"v_exp_f32 v14,v14\n"
"v_mfma_f32_16x16x32_bf16 v[40:43],a[104:107],a[24:27],v[40:43]\n"
"buffer_load_dwordx4 v117,s[24:27],s64 offen offset:3072 sc1 lds\n"
"v_mfma_f32_16x16x32_bf16 v[40:43],a[108:111],a[28:31],v[40:43]\n"
"s_add_u32 s64,s64,16384\n"
"v_exp_f32 v15,v15\n"
"v_cvt_pk_bf16_f32 v65,v2,v3\n"
"v_cvt_pk_bf16_f32 v66,v4,v5\n"
"v_exp_f32 v16,v16\n"
"v_mfma_f32_16x16x32_bf16 v[44:47],a[124:127],a[28:31],v[44:47]\n"
"buffer_store_dwordx4 a[228:231],v112,s[24:27],s56 offen offset:1024 sc1\n"
"v_cvt_pk_bf16_f32 v67,v6,v7\n"
"v_mfma_f32_16x16x32_bf16 v[44:47],a[120:123],a[24:27],v[44:47]\n"
"v_exp_f32 v17,v17\n"
"v_cvt_pk_bf16_f32 v68,v8,v9\n"
"v_cvt_pk_bf16_f32 v69,v10,v11\n"
"v_mfma_f32_16x16x32_bf16 v[44:47],a[116:119],a[20:23],v[44:47]\n"
"v_exp_f32 v18,v18\n"
"v_cvt_pk_bf16_f32 v70,v12,v13\n"
"v_cvt_pk_bf16_f32 v71,v14,v15\n"
"v_mfma_f32_16x16x32_bf16 v[44:47],a[112:115],a[16:19],v[44:47]\n"
"v_exp_f32 v19,v19\n"
"v_cvt_pk_bf16_f32 v72,v16,v17\n"
"v_max3_f32 v118,v48,v49,v50\n"
"v_exp_f32 v20,v20\n"
"v_cvt_pk_bf16_f32 v73,v18,v19\n"
"v_exp_f32 v21,v21\n"
"ds_read_b128 a[4:7],v116 offset:41984\n"
"ds_read_b128 a[8:11],v116 offset:43008\n"
"ds_read_b128 a[12:15],v116 offset:44032\n"
"v_exp_f32 v22,v22\n"
"v_exp_f32 v23,v23\n"
"v_exp_f32 v24,v24\n"
"v_cvt_pk_bf16_f32 v75,v22,v23\n"
"v_exp_f32 v25,v25\n"
"v_exp_f32 v26,v26\n"
"v_cvt_pk_bf16_f32 v76,v24,v25\n"
"v_exp_f32 v27,v27\n"
"v_exp_f32 v28,v28\n"
"v_cvt_pk_bf16_f32 v77,v26,v27\n"
"v_exp_f32 v29,v29\n"
"v_exp_f32 v30,v30\n"
"v_cvt_pk_bf16_f32 v78,v28,v29\n"
"v_exp_f32 v31,v31\n"
"v_cvt_pk_bf16_f32 v74,v20,v21\n"
"v_cvt_pk_bf16_f32 v79,v30,v31\n"
"v_max3_f32 v118,v118,v51,v52\n"
"v_max3_f32 v118,v118,v53,v54\n"
"v_max3_f32 v118,v118,v55,v60\n"
"v_max3_f32 v118,v118,v61,v62\n"
"v_max3_f32 v118,v118,v63,v56\n"
"v_max3_f32 v118,v118,v57,v58\n"
"v_max3_f32 v118,v118,v59,v40\n"
"v_max3_f32 v118,v118,v41,v42\n"
"v_max3_f32 v118,v118,v43,v44\n"
"v_max3_f32 v118,v118,v45,v46\n"
"v_max_f32 v118,v118,v47\n"
"v_cmp_lt_f32 vcc,s93,v118\n"
"s_nop 0\n"
"s_cbranch_vccnz .Lfk_cs_z0\n"
".Lfk_cr_z0:\n"
"s_waitcnt vmcnt(26) lgkmcnt(0)\n"
"s_barrier\n"
"v_mfma_f32_16x16x32_bf16 v[24:27],a[64:67],a[48:51],v[108:111]\n"
"ds_read_b128 a[96:99],v114 offset:40960\n"
"ds_read_b128 a[100:103],v114 offset:41984\n"
"v_mfma_f32_16x16x32_bf16 v[24:27],a[68:71],a[52:55],v[24:27]\n"
"ds_read_b128 a[104:107],v114 offset:43008\n"
"ds_read_b128 a[108:111],v114 offset:44032\n"
"v_mfma_f32_16x16x32_bf16 v[24:27],a[72:75],a[56:59],v[24:27]\n"
"ds_read_b128 a[112:115],v114 offset:45056\n"
"ds_read_b128 a[116:119],v114 offset:46080\n"
"v_mfma_f32_16x16x32_bf16 v[24:27],a[76:79],a[60:63],v[24:27]\n"
"ds_read_b128 a[120:123],v114 offset:47104\n"
"ds_read_b128 a[124:127],v114 offset:48128\n"
"v_mfma_f32_16x16x32_bf16 v[28:31],a[92:95],a[60:63],v[108:111]\n"
"v_add_f32 v16,0x80000000,v126\n"
"v_min_f32 v16,v16,v104\n"
"v_mfma_f32_16x16x32_bf16 v[28:31],a[88:91],a[56:59],v[28:31]\n"
"v_add_f32 v17,0xf149f2ca,v126\n"
"v_min_f32 v17,v17,v105\n"
"v_mfma_f32_16x16x32_bf16 v[28:31],a[84:87],a[52:55],v[28:31]\n"
"v_add_f32 v18,0xf1c9f2ca,v126\n"
"v_min_f32 v18,v18,v106\n"
"v_mfma_f32_16x16x32_bf16 v[28:31],a[80:83],a[48:51],v[28:31]\n"
"v_add_f32 v20,0xf349f2ca,v126\n"
"v_min_f32 v20,v20,v104\n"
"v_add_f32 v21,0xf35691f6,v126\n"
"v_min_f32 v21,v21,v105\n"
"v_add_f32 v22,0xf3633123,v126\n"
"v_min_f32 v22,v22,v106\n"
"v_add_f32 v23,0xf36fd050,v126\n"
"v_min_f32 v23,v23,v107\n"
"v_add_f32 v19,0xf2177617,v126\n"
"v_min_f32 v19,v19,v107\n"
"v_mfma_f32_16x16x32_bf16 v[20:23],a[80:83],a[32:35],v[20:23]\n"
"s_add_u32 m0,s55,8192\n"
"v_exp_f32 v40,v40\n"
"v_mfma_f32_16x16x32_bf16 v[20:23],a[84:87],a[36:39],v[20:23]\n"
"s_add_u32 s54,s54,s22\n"
"v_exp_f32 v41,v41\n"
"v_mfma_f32_16x16x32_bf16 v[20:23],a[88:91],a[40:43],v[20:23]\n"
"v_exp_f32 v42,v42\n"
"v_mfma_f32_16x16x32_bf16 v[20:23],a[92:95],a[44:47],v[20:23]\n"
"v_exp_f32 v43,v43\n"
"v_mfma_f32_16x16x32_bf16 v[16:19],a[76:79],a[44:47],v[16:19]\n"
"v_exp_f32 v44,v44\n"
"v_mfma_f32_16x16x32_bf16 v[16:19],a[72:75],a[40:43],v[16:19]\n"
"v_exp_f32 v45,v45\n"
"v_mfma_f32_16x16x32_bf16 v[16:19],a[68:71],a[36:39],v[16:19]\n"
"v_exp_f32 v46,v46\n"
"v_mfma_f32_16x16x32_bf16 v[16:19],a[64:67],a[32:35],v[16:19]\n"
"s_waitcnt lgkmcnt(0)\n"
"v_exp_f32 v47,v47\n"
"v_mfma_f32_16x16x32_bf16 v[128:131],a[96:99],v[64:67],0\n"
"v_exp_f32 v48,v48\n"
"v_mfma_f32_16x16x32_bf16 v[160:163],a[96:99],v[68:71],0\n"
"v_exp_f32 v49,v49\n"
"v_mfma_f32_16x16x32_bf16 v[192:195],a[96:99],v[72:75],0\n"
"ds_read_b128 a[64:67],v113 offset:32768\n"
"ds_read_b128 a[68:71],v113 offset:33792\n"
"v_mfma_f32_16x16x32_bf16 v[224:227],a[96:99],v[76:79],0\n"
"v_exp_f32 v50,v50\n"
"v_mfma_f32_16x16x32_bf16 a[140:143],a[144:147],v[76:79],0\n"
"ds_read_b128 a[72:75],v113 offset:34816\n"
"ds_read_b128 a[76:79],v113 offset:35840\n"
"v_mfma_f32_16x16x32_bf16 v[228:231],a[100:103],v[76:79],0\n"
"v_exp_f32 v51,v51\n"
"v_mfma_f32_16x16x32_bf16 v[132:135],a[100:103],v[64:67],0\n"
"ds_read_b128 a[80:83],v113 offset:36864\n"
"ds_read_b128 a[84:87],v113 offset:37888\n"
"v_mfma_f32_16x16x32_bf16 v[164:167],a[100:103],v[68:71],0\n"
"v_exp_f32 v52,v52\n"
"v_mfma_f32_16x16x32_bf16 v[196:199],a[100:103],v[72:75],0\n"
"v_exp_f32 v53,v53\n"
"v_mfma_f32_16x16x32_bf16 a[136:139],a[144:147],v[72:75],0\n"
"ds_read_b128 a[88:91],v113 offset:38912\n"
"ds_read_b128 a[92:95],v113 offset:39936\n"
"v_mfma_f32_16x16x32_bf16 v[200:203],a[104:107],v[72:75],0\n"
"v_exp_f32 v54,v54\n"
"v_mfma_f32_16x16x32_bf16 v[232:235],a[104:107],v[76:79],0\n"
"buffer_load_dwordx4 v115,s[32:35],s54 offen sc1 lds\n"
"v_mfma_f32_16x16x32_bf16 v[136:139],a[104:107],v[64:67],0\n"
"s_add_u32 m0,s67,49152\n"
"v_exp_f32 v55,v55\n"
"v_mfma_f32_16x16x32_bf16 v[168:171],a[104:107],v[68:71],0\n"
"buffer_load_dwordx4 v112,s[24:27],s64 offen offset:0 sc1 lds\n"
"v_mfma_f32_16x16x32_bf16 a[132:135],a[144:147],v[68:71],0\n"
"v_exp_f32 v56,v56\n"
"v_mfma_f32_16x16x32_bf16 v[172:175],a[108:111],v[68:71],0\n"
"buffer_load_dwordx4 v112,s[24:27],s64 offen offset:1024 sc1 lds\n"
"v_mfma_f32_16x16x32_bf16 v[204:207],a[108:111],v[72:75],0\n"
"v_exp_f32 v57,v57\n"
"v_mfma_f32_16x16x32_bf16 v[236:239],a[108:111],v[76:79],0\n"
"buffer_load_dwordx4 v112,s[24:27],s64 offen offset:2048 sc1 lds\n"
"v_mfma_f32_16x16x32_bf16 v[140:143],a[108:111],v[64:67],0\n"
"v_exp_f32 v58,v58\n"
"v_mfma_f32_16x16x32_bf16 a[128:131],a[144:147],v[64:67],0\n"
"buffer_load_dwordx4 v112,s[24:27],s64 offen offset:3072 sc1 lds\n"
"v_mfma_f32_16x16x32_bf16 v[144:147],a[112:115],v[64:67],0\n"
"s_add_u32 m0,s67,53248\n"
"v_exp_f32 v59,v59\n"
"v_mfma_f32_16x16x32_bf16 v[176:179],a[112:115],v[68:71],0\n"
"buffer_load_dwordx4 v117,s[24:27],s64 offen offset:0 sc1 lds\n"
"v_mfma_f32_16x16x32_bf16 v[208:211],a[112:115],v[72:75],0\n"
"v_exp_f32 v60,v60\n"
"v_mfma_f32_16x16x32_bf16 v[240:243],a[112:115],v[76:79],0\n"
"buffer_load_dwordx4 v117,s[24:27],s64 offen offset:1024 sc1 lds\n"
"v_mfma_f32_16x16x32_bf16 v[244:247],a[116:119],v[76:79],0\n"
"v_exp_f32 v61,v61\n"
"v_mfma_f32_16x16x32_bf16 v[148:151],a[116:119],v[64:67],0\n"
"buffer_load_dwordx4 v117,s[24:27],s64 offen offset:2048 sc1 lds\n"
"v_mfma_f32_16x16x32_bf16 v[180:183],a[116:119],v[68:71],0\n"
"v_exp_f32 v62,v62\n"
"v_mfma_f32_16x16x32_bf16 v[212:215],a[116:119],v[72:75],0\n"
"buffer_load_dwordx4 v117,s[24:27],s64 offen offset:3072 sc1 lds\n"
"v_mfma_f32_16x16x32_bf16 v[216:219],a[120:123],v[72:75],0\n"
"s_add_u32 s64,s64,16384\n"
"v_exp_f32 v63,v63\n"
"v_mfma_f32_16x16x32_bf16 v[248:251],a[120:123],v[76:79],0\n"
"buffer_store_dwordx4 a[232:235],v112,s[24:27],s56 offen offset:2048 sc1\n"
"s_and_b32 s67,s67,8192\n"
"v_mfma_f32_16x16x32_bf16 v[152:155],a[120:123],v[64:67],0\n"
"buffer_store_dwordx4 a[172:175],v112,s[24:27],s3 offen offset:3072 sc1\n"
"s_sub_u32 s3,s3,16384\n"
"v_mfma_f32_16x16x32_bf16 v[184:187],a[120:123],v[68:71],0\n"
"buffer_store_dwordx4 a[176:179],v112,s[24:27],s3 offen offset:0 sc1\n"
"v_cvt_pk_bf16_f32 v84,v40,v41\n"
"v_mfma_f32_16x16x32_bf16 v[188:191],a[124:127],v[68:71],0\n"
"v_cvt_pk_bf16_f32 v85,v42,v43\n"
"v_cvt_pk_bf16_f32 v86,v44,v45\n"
"v_mfma_f32_16x16x32_bf16 v[220:223],a[124:127],v[72:75],0\n"
"v_cvt_pk_bf16_f32 v87,v46,v47\n"
"v_cvt_pk_bf16_f32 v88,v48,v49\n"
"v_mfma_f32_16x16x32_bf16 v[252:255],a[124:127],v[76:79],0\n"
"v_cvt_pk_bf16_f32 v89,v50,v51\n"
"v_cvt_pk_bf16_f32 v90,v52,v53\n"
"v_mfma_f32_16x16x32_bf16 v[156:159],a[124:127],v[64:67],0\n"
"v_cvt_pk_bf16_f32 v91,v54,v55\n"
"v_cvt_pk_bf16_f32 v92,v56,v57\n"
"v_cvt_pk_bf16_f32 v93,v58,v59\n"
"ds_read_b128 a[16:19],v116 offset:8192\n"
"ds_read_b128 a[20:23],v116 offset:9216\n"
"ds_read_b128 a[24:27],v116 offset:10240\n"
"ds_read_b128 a[28:31],v116 offset:11264\n"
"v_cvt_pk_bf16_f32 v94,v60,v61\n"
"v_cvt_pk_bf16_f32 v95,v62,v63\n"
"v_max3_f32 v118,v24,v25,v26\n"
"v_max3_f32 v118,v118,v27,v28\n"
"v_max3_f32 v118,v118,v29,v30\n"
"v_max3_f32 v118,v118,v31,v20\n"
"v_max3_f32 v118,v118,v21,v22\n"
"v_max3_f32 v118,v118,v23,v16\n"
"v_max3_f32 v118,v118,v17,v18\n"
"v_max_f32 v118,v118,v19\n"
"v_cmp_lt_f32 vcc,s93,v118\n"
"s_nop 0\n"
"s_cbranch_vccnz .Lfk_cs_z1\n"
".Lfk_cr_z1:\n"
"v_mov_b32 v113,v112\n"
"s_waitcnt vmcnt(21) lgkmcnt(0)\n"
"s_barrier\n"
"v_add_f32 v56,0x80000000,v126\n"
"v_min_f32 v56,v56,v108\n"
"v_add_f32 v57,0xf149f2ca,v126\n"
"v_min_f32 v57,v57,v109\n"
"v_add_f32 v58,0xf1c9f2ca,v126\n"
"v_min_f32 v58,v58,v110\n"
"v_add_f32 v59,0xf2177617,v126\n"
"v_min_f32 v59,v59,v111\n"
"v_add_f32 v60,0xf349f2ca,v126\n"
"v_min_f32 v60,v60,v108\n"
"v_add_f32 v61,0xf35691f6,v126\n"
"v_min_f32 v61,v61,v109\n"
"v_add_f32 v62,0xf3633123,v126\n"
"v_min_f32 v62,v62,v110\n"
"v_add_f32 v63,0xf36fd050,v126\n"
"v_min_f32 v63,v63,v111\n"
"v_exp_f32 v16,v16\n"
"v_mfma_f32_16x16x32_bf16 v[56:59],a[64:67],a[48:51],v[56:59]\n"
"s_add_u32 m0,s55,12288\n"
"ds_read_b128 a[96:99],v114 offset:8192\n"
"ds_read_b128 a[100:103],v114 offset:9216\n"
"ds_read_b128 a[104:107],v114 offset:10240\n"
"v_exp_f32 v17,v17\n"
"v_mfma_f32_16x16x32_bf16 v[56:59],a[68:71],a[52:55],v[56:59]\n"
"s_add_u32 s54,s54,s22\n"
"ds_read_b128 a[108:111],v114 offset:11264\n"
"ds_read_b128 a[112:115],v114 offset:12288\n"
"ds_read_b128 a[116:119],v114 offset:13312\n"
"v_mfma_f32_16x16x32_bf16 v[56:59],a[72:75],a[56:59],v[56:59]\n"
"ds_read_b128 a[120:123],v114 offset:14336\n"
"ds_read_b128 a[124:127],v114 offset:15360\n"
"buffer_load_dwordx4 v115,s[32:35],s54 offen sc1 lds\n"
"v_cvt_pk_bf16_f32 v72,v16,v17\n"
"v_exp_f32 v18,v18\n"
"v_mfma_f32_16x16x32_bf16 v[56:59],a[76:79],a[60:63],v[56:59]\n"
"ds_read_b128 a[32:35],v116 offset:24576\n"
"ds_read_b128 a[36:39],v116 offset:25600\n"
"ds_read_b128 a[40:43],v116 offset:26624\n"
"v_mfma_f32_16x16x32_bf16 v[60:63],a[92:95],a[60:63],v[60:63]\n"
"buffer_store_dwordx4 a[180:183],v112,s[24:27],s3 offen offset:1024 sc1\n"
"ds_read_b128 a[44:47],v116 offset:27648\n"
"v_mfma_f32_16x16x32_bf16 v[60:63],a[88:91],a[56:59],v[60:63]\n"
"s_add_u32 m0,s67,32768\n"
"v_exp_f32 v19,v19\n"
"v_mfma_f32_16x16x32_bf16 v[60:63],a[84:87],a[52:55],v[60:63]\n"
"buffer_load_dwordx4 v112,s[24:27],s64 offen offset:0 sc1 lds\n"
"v_cvt_pk_bf16_f32 v73,v18,v19\n"
"v_exp_f32 v20,v20\n"
"v_mfma_f32_16x16x32_bf16 v[60:63],a[80:83],a[48:51],v[60:63]\n"
"ds_read_b128 a[64:67],v113 offset:0\n"
"ds_read_b128 a[68:71],v113 offset:1024\n"
"ds_read_b128 a[72:75],v113 offset:2048\n"
"s_waitcnt lgkmcnt(7)\n"
"v_mfma_f32_16x16x32_bf16 v[160:163],a[96:99],v[84:87],v[160:163]\n"
"ds_read_b128 a[76:79],v113 offset:3072\n"
"ds_read_b128 a[80:83],v113 offset:4096\n"
"ds_read_b128 a[84:87],v113 offset:5120\n"
"ds_read_b128 a[88:91],v113 offset:6144\n"
"ds_read_b128 a[92:95],v113 offset:7168\n"
"v_exp_f32 v21,v21\n"
"v_mfma_f32_16x16x32_bf16 a[132:135],a[144:147],v[84:87],a[132:135]\n"
"buffer_load_dwordx4 v112,s[24:27],s64 offen offset:1024 sc1 lds\n"
"v_max3_f32 v118,v56,v57,v58\n"
"v_mfma_f32_16x16x32_bf16 v[164:167],a[100:103],v[84:87],v[164:167]\n"
"v_exp_f32 v22,v22\n"
"v_cvt_pk_bf16_f32 v74,v20,v21\n"
"ds_read_b128 a[48:51],v116 offset:57344\n"
"ds_read_b128 a[52:55],v116 offset:58368\n"
"v_mfma_f32_16x16x32_bf16 v[168:171],a[104:107],v[84:87],v[168:171]\n"
"v_max3_f32 v118,v118,v59,v60\n"
"v_max3_f32 v118,v118,v61,v62\n"
"v_max_f32 v118,v118,v63\n"
"v_exp_f32 v23,v23\n"
"v_mfma_f32_16x16x32_bf16 v[172:175],a[108:111],v[84:87],v[172:175]\n"
"ds_read_b128 a[56:59],v116 offset:59392\n"
"buffer_load_dwordx4 v112,s[24:27],s64 offen offset:2048 sc1 lds\n"
"ds_read_b128 a[60:63],v116 offset:60416\n"
"v_mfma_f32_16x16x32_bf16 v[176:179],a[112:115],v[84:87],v[176:179]\n"
"v_cvt_pk_bf16_f32 v75,v22,v23\n"
"v_accvgpr_read_b32 v122,a128\n"
"v_rcp_f32 v122,v122\n"
"v_cmp_lt_f32 vcc,s93,v118\n"
"v_pk_mul_f32 v[128:129],v[128:129],v[122:123] op_sel_hi:[1,0]\n"
"v_mfma_f32_16x16x32_bf16 v[180:183],a[116:119],v[84:87],v[180:183]\n"
"buffer_store_dwordx4 a[184:187],v112,s[24:27],s3 offen offset:2048 sc1\n"
"v_exp_f32 v24,v24\n"
"v_pk_mul_f32 v[130:131],v[130:131],v[122:123] op_sel_hi:[1,0]\n"
"v_pk_mul_f32 v[132:133],v[132:133],v[122:123] op_sel_hi:[1,0]\n"
"v_pk_mul_f32 v[134:135],v[134:135],v[122:123] op_sel_hi:[1,0]\n"
"v_mfma_f32_16x16x32_bf16 v[184:187],a[120:123],v[84:87],v[184:187]\n"
"buffer_load_dwordx4 v112,s[24:27],s64 offen offset:3072 sc1 lds\n"
"v_cvt_pk_bf16_f32 v128,v128,v129\n"
"v_cvt_pk_bf16_f32 v129,v130,v131\n"
"v_cvt_pk_bf16_f32 v130,v132,v133\n"
"v_cvt_pk_bf16_f32 v131,v134,v135\n"
"v_mfma_f32_16x16x32_bf16 v[188:191],a[124:127],v[84:87],v[188:191]\n"
"buffer_store_dwordx4 v[128:131],v112,s[24:27],s70 offen offset:0 sc1\n"
"v_pk_mul_f32 v[136:137],v[136:137],v[122:123] op_sel_hi:[1,0]\n"
"v_mfma_f32_16x16x32_bf16 v[192:195],a[96:99],v[88:91],v[192:195]\n"
"s_add_u32 m0,s67,36864\n"
"v_exp_f32 v25,v25\n"
"v_exp_f32 v27,v27\n"
"v_cvt_pk_bf16_f32 v76,v24,v25\n"
"v_cvt_pk_bf16_f32 v136,v136,v137\n"
"v_pk_mul_f32 v[138:139],v[138:139],v[122:123] op_sel_hi:[1,0]\n"
"v_pk_mul_f32 v[140:141],v[140:141],v[122:123] op_sel_hi:[1,0]\n"
"v_pk_mul_f32 v[142:143],v[142:143],v[122:123] op_sel_hi:[1,0]\n"
"v_mfma_f32_16x16x32_bf16 a[136:139],a[144:147],v[88:91],a[136:139]\n"
"buffer_load_dwordx4 v117,s[24:27],s64 offen offset:0 sc1 lds\n"
"v_cvt_pk_bf16_f32 v137,v138,v139\n"
"v_mfma_f32_16x16x32_bf16 v[196:199],a[100:103],v[88:91],v[196:199]\n"
"v_cvt_pk_bf16_f32 v138,v140,v141\n"
"v_cvt_pk_bf16_f32 v139,v142,v143\n"
"v_exp_f32 v26,v26\n"
"v_mfma_f32_16x16x32_bf16 v[200:203],a[104:107],v[88:91],v[200:203]\n"
"buffer_store_dwordx4 v[136:139],v112,s[24:27],s70 offen offset:1024 sc1\n"
"v_cvt_pk_bf16_f32 v77,v26,v27\n"
"v_pk_mul_f32 v[144:145],v[144:145],v[122:123] op_sel_hi:[1,0]\n"
"v_pk_mul_f32 v[146:147],v[146:147],v[122:123] op_sel_hi:[1,0]\n"
"v_pk_mul_f32 v[148:149],v[148:149],v[122:123] op_sel_hi:[1,0]\n"
"v_pk_mul_f32 v[150:151],v[150:151],v[122:123] op_sel_hi:[1,0]\n"
"v_exp_f32 v28,v28\n"
"v_mfma_f32_16x16x32_bf16 v[204:207],a[108:111],v[88:91],v[204:207]\n"
"buffer_load_dwordx4 v117,s[24:27],s64 offen offset:1024 sc1 lds\n"
"buffer_store_dwordx4 a[188:191],v112,s[24:27],s3 offen offset:3072 sc1\n"
"v_mfma_f32_16x16x32_bf16 v[208:211],a[112:115],v[88:91],v[208:211]\n"
"s_or_b32 s3,s3,0x3c000\n"
"v_cvt_pk_bf16_f32 v144,v144,v145\n"
"v_cvt_pk_bf16_f32 v145,v146,v147\n"
"v_cvt_pk_bf16_f32 v146,v148,v149\n"
"v_cvt_pk_bf16_f32 v147,v150,v151\n"
"v_mfma_f32_16x16x32_bf16 v[212:215],a[116:119],v[88:91],v[212:215]\n"
"buffer_store_dwordx4 v[144:147],v112,s[24:27],s70 offen offset:2048 sc1\n"
"v_pk_mul_f32 v[152:153],v[152:153],v[122:123] op_sel_hi:[1,0]\n"
"v_pk_mul_f32 v[154:155],v[154:155],v[122:123] op_sel_hi:[1,0]\n"
"v_mfma_f32_16x16x32_bf16 v[216:219],a[120:123],v[88:91],v[216:219]\n"
"buffer_load_dwordx4 v117,s[24:27],s64 offen offset:2048 sc1 lds\n"
"v_exp_f32 v29,v29\n"
"v_exp_f32 v31,v31\n"
"v_cvt_pk_bf16_f32 v78,v28,v29\n"
"v_pk_mul_f32 v[156:157],v[156:157],v[122:123] op_sel_hi:[1,0]\n"
"v_pk_mul_f32 v[158:159],v[158:159],v[122:123] op_sel_hi:[1,0]\n"
"v_mfma_f32_16x16x32_bf16 v[220:223],a[124:127],v[88:91],v[220:223]\n"
"v_cvt_pk_bf16_f32 v152,v152,v153\n"
"v_cvt_pk_bf16_f32 v153,v154,v155\n"
"v_cvt_pk_bf16_f32 v154,v156,v157\n"
"v_cvt_pk_bf16_f32 v155,v158,v159\n"
"v_mfma_f32_16x16x32_bf16 v[224:227],a[96:99],v[92:95],v[224:227]\n"
"buffer_store_dwordx4 v[152:155],v112,s[24:27],s70 offen offset:3072 sc1\n"
"v_accvgpr_read_b32 v122,a132\n"
"v_mfma_f32_16x16x32_bf16 a[140:143],a[144:147],v[92:95],a[140:143]\n"
"s_add_u32 s70,s70,262144\n"
"buffer_load_dwordx4 v117,s[24:27],s64 offen offset:3072 sc1 lds\n"
"v_mfma_f32_16x16x32_bf16 v[228:231],a[100:103],v[92:95],v[228:231]\n"
"s_add_u32 s64,s64,16384\n"
"v_exp_f32 v30,v30\n"
"v_rcp_f32 v122,v122\n"
"v_cvt_pk_bf16_f32 v79,v30,v31\n"
"v_pk_mul_f32 v[160:161],v[160:161],v[122:123] op_sel_hi:[1,0]\n"
"v_pk_mul_f32 v[162:163],v[162:163],v[122:123] op_sel_hi:[1,0]\n"
"v_pk_mul_f32 v[164:165],v[164:165],v[122:123] op_sel_hi:[1,0]\n"
"v_pk_mul_f32 v[166:167],v[166:167],v[122:123] op_sel_hi:[1,0]\n"
"v_mfma_f32_16x16x32_bf16 v[232:235],a[104:107],v[92:95],v[232:235]\n"
"buffer_store_dwordx4 a[236:239],v112,s[24:27],s56 offen offset:3072 sc1\n"
"v_cvt_pk_bf16_f32 v160,v160,v161\n"
"v_cvt_pk_bf16_f32 v161,v162,v163\n"
"v_cvt_pk_bf16_f32 v162,v164,v165\n"
"v_cvt_pk_bf16_f32 v163,v166,v167\n"
"v_mfma_f32_16x16x32_bf16 v[236:239],a[108:111],v[92:95],v[236:239]\n"
"buffer_store_dwordx4 v[160:163],v112,s[24:27],s70 offen offset:0 sc1\n"
"v_pk_mul_f32 v[168:169],v[168:169],v[122:123] op_sel_hi:[1,0]\n"
"v_pk_mul_f32 v[170:171],v[170:171],v[122:123] op_sel_hi:[1,0]\n"
"v_pk_mul_f32 v[172:173],v[172:173],v[122:123] op_sel_hi:[1,0]\n"
"v_pk_mul_f32 v[174:175],v[174:175],v[122:123] op_sel_hi:[1,0]\n"
"v_mfma_f32_16x16x32_bf16 v[240:243],a[112:115],v[92:95],v[240:243]\n"
"v_cvt_pk_bf16_f32 v168,v168,v169\n"
"v_cvt_pk_bf16_f32 v169,v170,v171\n"
"v_cvt_pk_bf16_f32 v170,v172,v173\n"
"v_cvt_pk_bf16_f32 v171,v174,v175\n"
"v_mfma_f32_16x16x32_bf16 v[244:247],a[116:119],v[92:95],v[244:247]\n"
"buffer_store_dwordx4 v[168:171],v112,s[24:27],s70 offen offset:1024 sc1\n"
"v_pk_mul_f32 v[176:177],v[176:177],v[122:123] op_sel_hi:[1,0]\n"
"v_pk_mul_f32 v[178:179],v[178:179],v[122:123] op_sel_hi:[1,0]\n"
"v_pk_mul_f32 v[180:181],v[180:181],v[122:123] op_sel_hi:[1,0]\n"
"v_pk_mul_f32 v[182:183],v[182:183],v[122:123] op_sel_hi:[1,0]\n"
"v_mfma_f32_16x16x32_bf16 v[248:251],a[120:123],v[92:95],v[248:251]\n"
"v_cvt_pk_bf16_f32 v176,v176,v177\n"
"v_cvt_pk_bf16_f32 v177,v178,v179\n"
"v_cvt_pk_bf16_f32 v178,v180,v181\n"
"v_cvt_pk_bf16_f32 v179,v182,v183\n"
"v_mfma_f32_16x16x32_bf16 v[252:255],a[124:127],v[92:95],v[252:255]\n"
"buffer_store_dwordx4 v[176:179],v112,s[24:27],s70 offen offset:2048 sc1\n"
"v_pk_mul_f32 v[184:185],v[184:185],v[122:123] op_sel_hi:[1,0]\n"
"v_pk_mul_f32 v[186:187],v[186:187],v[122:123] op_sel_hi:[1,0]\n"
"v_pk_mul_f32 v[188:189],v[188:189],v[122:123] op_sel_hi:[1,0]\n"
"v_pk_mul_f32 v[190:191],v[190:191],v[122:123] op_sel_hi:[1,0]\n"
"v_cvt_pk_bf16_f32 v184,v184,v185\n"
"v_cvt_pk_bf16_f32 v185,v186,v187\n"
"v_cvt_pk_bf16_f32 v186,v188,v189\n"
"v_cvt_pk_bf16_f32 v187,v190,v191\n"
"buffer_store_dwordx4 v[184:187],v112,s[24:27],s70 offen offset:3072 sc1\n"
"s_add_u32 s70,s70,262144\n"
"s_cbranch_vccnz .Lfk_cs_z2\n"
".Lfk_cr_z2:\n"
"s_waitcnt vmcnt(34) lgkmcnt(0)\n"
"s_barrier\n"
"s_add_u32 s89,s89,1\n"
"s_branch .Lfk_winB\n"
".Lfk_ev:\n"
"s_xor_b32 s55,s55,16384\n"
"s_andn2_b32 s91,s55,16384\n"
"v_lshrrev_b32 v121,1,v112\n"
"v_add_u32 v121,s91,v121\n"
"ds_read_b64_tr_b16 a[160:161],v121 offset:0\n"
"ds_read_b64_tr_b16 a[162:163],v121 offset:512\n"
"s_add_u32 s56,s56,4096\n"
"s_swappc_b64 s[20:21],s[94:95]\n"
"s_xor_b32 s55,s55,16384\n"
"s_andn2_b32 s91,s55,16384\n"
"v_lshrrev_b32 v121,1,v112\n"
"v_add_u32 v121,s91,v121\n"
"ds_read_b64_tr_b16 a[164:165],v121 offset:4096\n"
"ds_read_b64_tr_b16 a[166:167],v121 offset:4608\n"
"ds_read_b64_tr_b16 a[168:169],v121 offset:8192\n"
"ds_read_b64_tr_b16 a[170:171],v121 offset:8704\n"
"ds_read_b64_tr_b16 a[172:173],v121 offset:12288\n"
"ds_read_b64_tr_b16 a[174:175],v121 offset:12800\n"
"ds_read_b64_tr_b16 a[176:177],v121 offset:0\n"
"ds_read_b64_tr_b16 a[178:179],v121 offset:512\n"
"buffer_store_dwordx2 a[154:155],off,s[24:27],s2 sc1\n"
"s_and_b32 s2,s2,0xfffffc00\n"
"s_mov_b32 s56,s10\n"
"v_accvgpr_write_b32 a240,0\n"
"v_accvgpr_write_b32 a241,0\n"
"v_accvgpr_write_b32 a242,0\n"
"v_accvgpr_write_b32 a243,0\n"
"v_accvgpr_write_b32 a244,0\n"
"v_accvgpr_write_b32 a245,0\n"
"v_accvgpr_write_b32 a246,0\n"
"v_accvgpr_write_b32 a247,0\n"
"v_accvgpr_write_b32 a248,0\n"
"v_accvgpr_write_b32 a249,0\n"
"v_accvgpr_write_b32 a250,0\n"
"v_accvgpr_write_b32 a251,0\n"
"v_accvgpr_write_b32 a252,0\n"
"v_accvgpr_write_b32 a253,0\n"
"v_accvgpr_write_b32 a254,0\n"
"v_accvgpr_write_b32 a255,0\n"
"s_mov_b64 s[32:33],s[36:37]\n"
"s_mov_b32 s54,-229376\n"
"s_swappc_b64 s[20:21],s[94:95]\n"
"s_xor_b32 s55,s55,16384\n"
"s_add_u32 s56,s56,131072\n"
"s_andn2_b32 s91,s55,16384\n"
"v_lshrrev_b32 v121,1,v112\n"
"v_add_u32 v121,s91,v121\n"
"ds_read_b64_tr_b16 a[180:181],v121 offset:20480\n"
"ds_read_b64_tr_b16 a[182:183],v121 offset:20992\n"
"ds_read_b64_tr_b16 a[184:185],v121 offset:24576\n"
"ds_read_b64_tr_b16 a[186:187],v121 offset:25088\n"
"ds_read_b64_tr_b16 a[188:189],v121 offset:28672\n"
"ds_read_b64_tr_b16 a[190:191],v121 offset:29184\n"
"ds_read_b64_tr_b16 a[192:193],v121 offset:0\n"
"ds_read_b64_tr_b16 a[194:195],v121 offset:512\n"
"buffer_load_dwordx2 a[150:151],v112,s[24:27],s2 offen sc1\n"
"s_add_u32 s82,s59,8192\n"
"buffer_store_dwordx2 a[154:155],off,s[24:27],s82 sc1\n"
"s_swappc_b64 s[20:21],s[94:95]\n"
"s_xor_b32 s55,s55,16384\n"
"s_add_u32 s56,s56,131072\n"
"s_andn2_b32 s91,s55,16384\n"
"v_lshrrev_b32 v121,1,v112\n"
"v_add_u32 v121,s91,v121\n"
"ds_read_b64_tr_b16 a[196:197],v121 offset:4096\n"
"ds_read_b64_tr_b16 a[198:199],v121 offset:4608\n"
"ds_read_b64_tr_b16 a[200:201],v121 offset:8192\n"
"ds_read_b64_tr_b16 a[202:203],v121 offset:8704\n"
"ds_read_b64_tr_b16 a[204:205],v121 offset:12288\n"
"ds_read_b64_tr_b16 a[206:207],v121 offset:12800\n"
"ds_read_b64_tr_b16 a[208:209],v121 offset:0\n"
"ds_read_b64_tr_b16 a[210:211],v121 offset:512\n"
"s_mov_b32 s84,0x40000\n"
".Lfk_w_chk:\n"
"v_accvgpr_read_b32 v119,a150\n"
"v_accvgpr_read_b32 v120,a151\n"
"v_xor_b32 v119,s80,v119\n"
"v_xor_b32 v120,s81,v120\n"
"v_or_b32 v119,v119,v120\n"
"v_cmp_ne_u32 vcc,0,v119\n"
"s_nop 0\n"
"s_cbranch_vccz .Lfk_w_ok\n"
"s_sub_u32 s84,s84,1\n"
"s_cbranch_scc1 .Lfk_w_ok\n"
"s_sleep 4\n"
"buffer_load_dwordx2 a[150:151],v112,s[24:27],s2 offen sc1\n"
"s_waitcnt vmcnt(0)\n"
"s_branch .Lfk_w_chk\n"
".Lfk_w_ok:\n"
"v_lshrrev_b32 v121,1,v112\n"
"v_and_b32 v121,0xf8,v121\n"
"s_add_u32 s82,s63,8192\n"
"buffer_load_dwordx2 a[150:151],v121,s[24:27],s82 offen sc1\n"
"s_mov_b64 s[32:33],s[24:25]\n"
"s_mov_b32 s34,s26\n"
"s_mov_b32 s22,1024\n"
"v_mov_b32 v115,v112\n"
"s_sub_u32 s54,s3,1024\n"
"s_swappc_b64 s[20:21],s[94:95]\n"
"s_xor_b32 s55,s55,16384\n"
"s_add_u32 s56,s56,131072\n"
"s_andn2_b32 s91,s55,16384\n"
"v_lshrrev_b32 v121,1,v112\n"
"v_add_u32 v121,s91,v121\n"
"ds_read_b64_tr_b16 a[212:213],v121 offset:20480\n"
"ds_read_b64_tr_b16 a[214:215],v121 offset:20992\n"
"ds_read_b64_tr_b16 a[216:217],v121 offset:24576\n"
"ds_read_b64_tr_b16 a[218:219],v121 offset:25088\n"
"ds_read_b64_tr_b16 a[220:221],v121 offset:28672\n"
"ds_read_b64_tr_b16 a[222:223],v121 offset:29184\n"
"v_add_u32 v121,s91,v112\n"
"ds_read_b128 a[224:227],v121 offset:0\n"
"s_sub_u32 s54,s3,17408\n"
"s_mov_b32 s84,0x40000\n"
".Lfk_k_chk:\n"
"v_accvgpr_read_b32 v119,a150\n"
"v_accvgpr_read_b32 v120,a151\n"
"v_xor_b32 v119,s80,v119\n"
"v_xor_b32 v120,s81,v120\n"
"v_or_b32 v119,v119,v120\n"
"v_cmp_ne_u32 vcc,0,v119\n"
"s_nop 0\n"
"s_cbranch_vccz .Lfk_k_ok\n"
"s_sub_u32 s84,s84,1\n"
"s_cbranch_scc1 .Lfk_k_ok\n"
"s_sleep 4\n"
"v_lshrrev_b32 v121,1,v112\n"
"v_and_b32 v121,0xf8,v121\n"
"s_add_u32 s82,s63,8192\n"
"buffer_load_dwordx2 a[150:151],v121,s[24:27],s82 offen sc1\n"
"s_waitcnt vmcnt(0)\n"
"s_branch .Lfk_k_chk\n"
".Lfk_k_ok:\n"
"s_swappc_b64 s[20:21],s[94:95]\n"
"s_xor_b32 s55,s55,16384\n"
"s_andn2_b32 s91,s55,16384\n"
"v_add_u32 v121,s91,v112\n"
"ds_read_b128 a[228:231],v121 offset:4096\n"
"ds_read_b128 a[232:235],v121 offset:8192\n"
"ds_read_b128 a[236:239],v121 offset:12288\n"
"ds_read_b128 a[240:243],v121 offset:0\n"
"s_sub_u32 s54,s3,99328\n"
"s_mov_b32 s56,2146435072\n"
"s_getpc_b64 s[96:97]\n"
".Lfk_rt1:\n"
"s_sub_u32 s96,s96,.Lfk_rt1-.Lfk_tailA\n"
"s_subb_u32 s97,s97,0\n"
"s_mov_b32 s98,.Lfk_cold-.Lfk_tailA\n"
"s_mov_b32 s99,0x20000\n"
"s_lshr_b32 s82,s100,3\n"
"s_lshl2_add_u32 s82,s82,s52\n"
"s_lshl_b32 s82,s82,9\n"
"v_lshrrev_b32 v121,1,v112\n"
"buffer_load_dword a148,v121,s[96:99],s82 offen\n"
"s_swappc_b64 s[20:21],s[94:95]\n"
"s_xor_b32 s55,s55,16384\n"
"s_andn2_b32 s91,s55,16384\n"
"v_add_u32 v121,s91,v112\n"
"ds_read_b128 a[244:247],v121 offset:20480\n"
"ds_read_b128 a[248:251],v121 offset:24576\n"
"ds_read_b128 a[252:255],v121 offset:28672\n"
"ds_read_b128 a[156:159],v121 offset:0\n"
"s_sub_u32 s54,s3,115712\n"
"s_lshl_b32 s82,s100,3\n"
"s_add_u32 s82,s82,148946944\n"
"s_cmp_eq_u32 s52,0\n"
"s_cselect_b32 s82,s82,2146435072\n"
"buffer_store_dwordx2 a[154:155],off,s[24:27],s82 sc1\n"
"s_lshr_b32 s82,s3,9\n"
"s_and_b32 s82,s82,0x3000\n"
"s_add_u32 s11,s11,s82\n"
"s_add_u32 s9,s9,s82\n"
"s_lshl3_add_u32 s8,s82,s8\n"
"s_lshr_b32 s82,s3,18\n"
"s_sub_u32 s82,s82,s60\n"
"s_and_b32 s82,s82,7\n"
"s_lshl_b32 s82,s82,17\n"
"s_add_u32 s8,s8,s82\n"
"v_lshrrev_b32 v120,1,v112\n"
"buffer_load_dwordx2 a[150:151],v120,s[24:27],s11 offen sc1\n"
"s_lshl_b32 s30,s78,1\n"
"s_swappc_b64 s[20:21],s[94:95]\n"
"s_nop 1\n"
".Lfk_xn0_s:\n"
"s_mov_b32 s84,0x40000\n"
"buffer_load_dwordx4 v[74:77],v112,s[24:27],s58 offen sc1\n"
"s_waitcnt vmcnt(0)\n"
".Lfk_xn0:\n"
"v_xor_b32 v116,s80,v74\n"
"v_xor_b32 v117,s81,v75\n"
"v_or_b32 v116,v116,v117\n"
"v_xor_b32 v117,s80,v76\n"
"v_or_b32 v116,v116,v117\n"
"v_xor_b32 v117,s81,v77\n"
"v_or_b32 v116,v116,v117\n"
"v_cmp_ne_u32 vcc,0,v116\n"
"s_nop 0\n"
"s_cbranch_vccz .Lfk_xn0_x\n"
"s_sleep 4\n"
"s_mov_b64 exec,vcc\n"
"buffer_load_dwordx4 v[74:77],v112,s[24:27],s58 offen sc1\n"
"s_mov_b64 exec,-1\n"
"s_sub_u32 s84,s84,1\n"
"s_waitcnt vmcnt(0)\n"
"s_cbranch_scc0 .Lfk_xn0\n"
".Lfk_xn0_x:\n"
"s_nop 1\n"
"s_branch .Lfk_xn0_ok\n"
".Lfk_xn1_s:\n"
"s_mov_b32 s84,0x40000\n"
"buffer_load_dwordx4 v[74:77],v112,s[24:27],s58 offen sc1\n"
"s_waitcnt vmcnt(0)\n"
".Lfk_xn1:\n"
"v_xor_b32 v116,s80,v74\n"
"v_xor_b32 v117,s81,v75\n"
"v_or_b32 v116,v116,v117\n"
"v_xor_b32 v117,s80,v76\n"
"v_or_b32 v116,v116,v117\n"
"v_xor_b32 v117,s81,v77\n"
"v_or_b32 v116,v116,v117\n"
"v_cmp_ne_u32 vcc,0,v116\n"
"s_nop 0\n"
"s_cbranch_vccz .Lfk_xn1_x\n"
"s_sleep 4\n"
"s_mov_b64 exec,vcc\n"
"buffer_load_dwordx4 v[74:77],v112,s[24:27],s58 offen sc1\n"
"s_mov_b64 exec,-1\n"
"s_sub_u32 s84,s84,1\n"
"s_waitcnt vmcnt(0)\n"
"s_cbranch_scc0 .Lfk_xn1\n"
".Lfk_xn1_x:\n"
"s_nop 1\n"
"s_branch .Lfk_xn1_ok\n"
".Lfk_kv_s:\n"
"s_mov_b32 s84,0x40000\n"
"buffer_load_dwordx2 v[118:119],v120,s[24:27],s63 offen sc1\n"
"s_waitcnt vmcnt(0)\n"
".Lfk_kv:\n"
"v_xor_b32 v116,s80,v118\n"
"v_xor_b32 v117,s81,v119\n"
"v_or_b32 v116,v116,v117\n"
"v_cmp_ne_u32 vcc,0,v116\n"
"s_nop 0\n"
"s_cbranch_vccz .Lfk_kv_x\n"
"s_sleep 4\n"
"s_mov_b64 exec,vcc\n"
"buffer_load_dwordx2 v[118:119],v120,s[24:27],s63 offen sc1\n"
"s_mov_b64 exec,-1\n"
"s_sub_u32 s84,s84,1\n"
"s_waitcnt vmcnt(0)\n"
"s_cbranch_scc0 .Lfk_kv\n"
".Lfk_kv_x:\n"
"s_nop 1\n"
"s_branch .Lfk_kv_ok\n"
".Lfk_cs_h3:\n"
"s_mov_b32 s91,15\n"
"s_mov_b32 s90,15\n"
"s_call_b64 s[94:95],.Lfk_rare0\n"
"s_branch .Lfk_cr_h3\n"
".Lfk_cs_h1:\n"
"s_mov_b32 s91,15\n"
"s_call_b64 s[94:95],.Lfk_rare3\n"
"s_branch .Lfk_cr_h1\n"
".Lfk_cs_h2:\n"
"s_mov_b32 s91,15\n"
"s_call_b64 s[94:95],.Lfk_rare2\n"
"s_branch .Lfk_cr_h2\n"
".Lfk_cs_x1:\n"
"s_mov_b32 s91,15\n"
"s_call_b64 s[94:95],.Lfk_rare1\n"
"s_branch .Lfk_cr_x1\n"
".Lfk_cs_a3:\n"
"s_mov_b32 s91,14\n"
"s_call_b64 s[94:95],.Lfk_rare0\n"
"s_branch .Lfk_cr_a3\n"
".Lfk_cs_a0:\n"
"s_mov_b32 s91,12\n"
"s_call_b64 s[94:95],.Lfk_rare1\n"
"s_branch .Lfk_cr_a0\n"
".Lfk_cs_a1:\n"
"s_mov_b32 s91,8\n"
"s_call_b64 s[94:95],.Lfk_rare0\n"
"s_branch .Lfk_cr_a1\n"
".Lfk_ms_a0:\n"
"s_call_b64 s[94:95],.Lfk_mslow0\n"
"s_branch .Lfk_mr_a0\n"
".Lfk_ms_a1:\n"
"s_call_b64 s[94:95],.Lfk_mslow1\n"
"s_branch .Lfk_mr_a1\n"
".Lfk_p2:\n"
"s_nop 7\n"
"s_mov_b32 s84,0x40000\n"
"buffer_load_dwordx2 v[122:123],v115,s[24:27],s11 offen sc1\n"
"s_waitcnt vmcnt(0)\n"
".Lfk_pj:\n"
"v_xor_b32 v119,s80,v122\n"
"v_xor_b32 v120,s81,v123\n"
"v_or_b32 v119,v119,v120\n"
"v_cmp_ne_u32 s[14:15],0,v119\n"
"s_nop 0\n"
"s_mov_b32 vcc_lo,s14\n"
"s_mov_b32 vcc_hi,0\n"
"s_cbranch_vccz .Lfk_pj_x\n"
"s_sleep 4\n"
"s_mov_b64 exec,vcc\n"
"buffer_load_dwordx2 v[122:123],v115,s[24:27],s11 offen sc1\n"
"s_mov_b64 exec,-1\n"
"s_sub_u32 s84,s84,1\n"
"s_waitcnt vmcnt(0)\n"
"s_cbranch_scc0 .Lfk_pj\n"
".Lfk_pj_x:\n"
"s_cmp_ge_u32 s52,2\n"
"s_cselect_b32 s18,1032192,0\n"
"s_add_u32 s18,s18,s8\n"
"s_add_u32 m0,s30,0\n"
"s_nop 0\n"
"buffer_load_dwordx4 v112,s[24:27],s18 offen offset:0 sc1 lds\n"
"buffer_load_dwordx4 v112,s[24:27],s18 offen offset:1024 sc1 lds\n"
"buffer_load_dwordx4 v112,s[24:27],s18 offen offset:2048 sc1 lds\n"
"buffer_load_dwordx4 v112,s[24:27],s18 offen offset:3072 sc1 lds\n"
"s_add_u32 m0,m0,4096\n"
"s_nop 0\n"
"buffer_load_dwordx4 v117,s[24:27],s18 offen offset:0 sc1 lds\n"
"buffer_load_dwordx4 v117,s[24:27],s18 offen offset:1024 sc1 lds\n"
"buffer_load_dwordx4 v117,s[24:27],s18 offen offset:2048 sc1 lds\n"
"buffer_load_dwordx4 v117,s[24:27],s18 offen offset:3072 sc1 lds\n"
"s_add_u32 s83,s18,2097152\n"
"s_add_u32 m0,s30,32768\n"
"s_nop 0\n"
"buffer_load_dwordx4 v112,s[24:27],s83 offen offset:0 sc1 lds\n"
"buffer_load_dwordx4 v112,s[24:27],s83 offen offset:1024 sc1 lds\n"
"buffer_load_dwordx4 v112,s[24:27],s83 offen offset:2048 sc1 lds\n"
"buffer_load_dwordx4 v112,s[24:27],s83 offen offset:3072 sc1 lds\n"
"s_add_u32 m0,m0,4096\n"
"s_nop 0\n"
"buffer_load_dwordx4 v117,s[24:27],s83 offen offset:0 sc1 lds\n"
"buffer_load_dwordx4 v117,s[24:27],s83 offen offset:1024 sc1 lds\n"
"buffer_load_dwordx4 v117,s[24:27],s83 offen offset:2048 sc1 lds\n"
"buffer_load_dwordx4 v117,s[24:27],s83 offen offset:3072 sc1 lds\n"
"s_waitcnt vmcnt(0)\n"
"s_nop 1\n"
"s_branch .Lfk_p3\n"
".Lfk_cs_k1:\n"
"s_mov_b32 s91,15\n"
"s_call_b64 s[94:95],.Lfk_rare3\n"
"s_branch .Lfk_cr_k1\n"
".Lfk_cs_k2:\n"
"s_mov_b32 s91,15\n"
"s_call_b64 s[94:95],.Lfk_rare2\n"
"s_branch .Lfk_cr_k2\n"
".Lfk_cs_x2:\n"
"s_mov_b32 s91,15\n"
"s_call_b64 s[94:95],.Lfk_rare1\n"
"s_branch .Lfk_cr_x2\n"
".Lfk_cs_u3:\n"
"s_mov_b32 s91,15\n"
"s_call_b64 s[94:95],.Lfk_rare0\n"
"s_branch .Lfk_cr_u3\n"
".Lfk_cs_mu3:\n"
"s_call_b64 s[94:95],.Lfk_meet3\n"
"s_branch .Lfk_cr_mu3\n"
".Lfk_cs_u0:\n"
"s_mov_b32 s91,14\n"
"s_call_b64 s[94:95],.Lfk_rare1\n"
"s_branch .Lfk_cr_u0\n"
".Lfk_cs_mu0:\n"
"s_call_b64 s[94:95],.Lfk_meet3\n"
"s_branch .Lfk_cr_mu0\n"
".Lfk_cs_u1:\n"
"s_mov_b32 s91,12\n"
"s_call_b64 s[94:95],.Lfk_rare0\n"
"s_branch .Lfk_cr_u1\n"
".Lfk_cs_mu1:\n"
"s_call_b64 s[94:95],.Lfk_meet3\n"
"s_branch .Lfk_cr_mu1\n"
".Lfk_cs_u2:\n"
"s_mov_b32 s91,8\n"
"s_call_b64 s[94:95],.Lfk_rare1\n"
"s_branch .Lfk_cr_u2\n"
".Lfk_cs_mu2:\n"
"s_call_b64 s[94:95],.Lfk_meet3\n"
"s_branch .Lfk_cr_mu2\n"
".Lfk_cs_v3:\n"
"s_mov_b32 s91,15\n"
"s_mov_b32 s90,15\n"
"s_call_b64 s[94:95],.Lfk_rare0\n"
"s_branch .Lfk_cr_v3\n"
".Lfk_cs_mv3:\n"
"s_call_b64 s[94:95],.Lfk_meet3\n"
"s_branch .Lfk_cr_mv3\n"
".Lfk_cs_mv0:\n"
"s_call_b64 s[94:95],.Lfk_meet3\n"
"s_branch .Lfk_cr_mv0\n"
".Lfk_cs_v1:\n"
"s_call_b64 s[94:95],.Lfk_meet3\n"
"v_or_b32 v118,v118,v95\n"
"v_and_b32 v118,0x40004000,v118\n"
"v_cmp_ne_u32 vcc,0,v118\n"
"s_nop 0\n"
"s_cbranch_vccz .Lfk_cr_v1\n"
"s_mov_b32 s91,15\n"
"s_call_b64 s[94:95],.Lfk_rare3\n"
"s_branch .Lfk_cr_v1\n"
".Lfk_cs_v2:\n"
"s_call_b64 s[94:95],.Lfk_meet3\n"
"v_or_b32 v118,v118,v79\n"
"v_and_b32 v118,0x40004000,v118\n"
"v_cmp_ne_u32 vcc,0,v118\n"
"s_nop 0\n"
"s_cbranch_vccz .Lfk_cr_v2\n"
"s_mov_b32 s91,15\n"
"s_call_b64 s[94:95],.Lfk_rare2\n"
"s_branch .Lfk_cr_v2\n"
".Lfk_cs_b3:\n"
"s_mov_b32 s91,15\n"
"s_call_b64 s[94:95],.Lfk_rare0\n"
"s_branch .Lfk_cr_b3\n"
".Lfk_ms_b0:\n"
"s_call_b64 s[94:95],.Lfk_mslow0\n"
"s_branch .Lfk_mr_b0\n"
".Lfk_ms_b1:\n"
"s_call_b64 s[94:95],.Lfk_mslow1\n"
"s_branch .Lfk_mr_b1\n"
".Lfk_cs_c3:\n"
"s_mov_b32 s91,15\n"
"s_mov_b32 s90,15\n"
"s_call_b64 s[94:95],.Lfk_rare0\n"
"s_branch .Lfk_cr_c3\n"
".Lfk_cs_z3:\n"
"s_mov_b32 s91,15\n"
"s_mov_b32 s90,15\n"
"s_call_b64 s[94:95],.Lfk_rare0\n"
"s_branch .Lfk_cr_z3\n"
".Lfk_cs_z0:\n"
"s_mov_b32 s91,14\n"
"s_call_b64 s[94:95],.Lfk_rare1\n"
"s_branch .Lfk_cr_z0\n"
".Lfk_cs_z1:\n"
"s_mov_b32 s91,12\n"
"s_call_b64 s[94:95],.Lfk_rare0\n"
"s_branch .Lfk_cr_z1\n"
".Lfk_cs_z2:\n"
"s_mov_b32 s91,8\n"
"s_call_b64 s[94:95],.Lfk_rare1\n"
"s_branch .Lfk_cr_z2\n"
".Lfk_cold:\n"
".Lfk_rare0:\n"
"s_nop 7\n"
"s_bitcmp1_b32 s91,0\n"
"s_cbranch_scc0 .Lfk_rare0_0\n"
"v_max3_f32 v119,v0,v1,v2\n"
"v_max3_f32 v120,v3,v4,v5\n"
"v_max3_f32 v119,v119,v6,v7\n"
"v_max_f32 v119,v119,v120\n"
"v_mov_b32 v120,v119\n"
"s_nop 1\n"
"v_permlane16_swap_b32 v120,v119\n"
"v_max_f32 v119,v119,v120\n"
"v_mov_b32 v120,v119\n"
"s_nop 1\n"
"v_permlane32_swap_b32 v120,v119\n"
"v_max_f32 v119,v119,v120\n"
"s_bitcmp1_b32 s90,0\n"
"s_cselect_b32 s82,0xff800000,0\n"
"v_add_f32 v119,0x41980000,v119\n"
"v_max_f32 v119,s82,v119\n"
"v_cmp_lt_f32 vcc,s17,|v119|\n"
"s_nop 0\n"
"s_cbranch_vccz .Lfk_rare0_0\n"
"v_ceil_f32 v119,v119\n"
"v_sub_f32 v0,v0,v119\n"
"v_sub_f32 v1,v1,v119\n"
"v_sub_f32 v2,v2,v119\n"
"v_sub_f32 v3,v3,v119\n"
"v_sub_f32 v4,v4,v119\n"
"v_sub_f32 v5,v5,v119\n"
"v_sub_f32 v6,v6,v119\n"
"v_sub_f32 v7,v7,v119\n"
"v_sub_f32 v96,v96,v119\n"
"v_sub_f32 v97,v97,v119\n"
"v_sub_f32 v98,v98,v119\n"
"v_sub_f32 v99,v99,v119\n"
"s_cbranch_scc1 .Lfk_rare0_0\n"
"v_exp_f32 v119,-v119\n"
"s_nop 0\n"
"v_mul_f32 v128,v128,v119\n"
"v_mul_f32 v129,v129,v119\n"
"v_mul_f32 v130,v130,v119\n"
"v_mul_f32 v131,v131,v119\n"
"v_mul_f32 v132,v132,v119\n"
"v_mul_f32 v133,v133,v119\n"
"v_mul_f32 v134,v134,v119\n"
"v_mul_f32 v135,v135,v119\n"
"v_mul_f32 v136,v136,v119\n"
"v_mul_f32 v137,v137,v119\n"
"v_mul_f32 v138,v138,v119\n"
"v_mul_f32 v139,v139,v119\n"
"v_mul_f32 v140,v140,v119\n"
"v_mul_f32 v141,v141,v119\n"
"v_mul_f32 v142,v142,v119\n"
"v_mul_f32 v143,v143,v119\n"
"v_mul_f32 v144,v144,v119\n"
"v_mul_f32 v145,v145,v119\n"
"v_mul_f32 v146,v146,v119\n"
"v_mul_f32 v147,v147,v119\n"
"v_mul_f32 v148,v148,v119\n"
"v_mul_f32 v149,v149,v119\n"
"v_mul_f32 v150,v150,v119\n"
"v_mul_f32 v151,v151,v119\n"
"v_mul_f32 v152,v152,v119\n"
"v_mul_f32 v153,v153,v119\n"
"v_mul_f32 v154,v154,v119\n"
"v_mul_f32 v155,v155,v119\n"
"v_mul_f32 v156,v156,v119\n"
"v_mul_f32 v157,v157,v119\n"
"v_mul_f32 v158,v158,v119\n"
"v_mul_f32 v159,v159,v119\n"
"v_accvgpr_read_b32 v120,a128\n"
"v_mul_f32 v120,v120,v119\n"
"v_accvgpr_write_b32 a128,v120\n"
"v_lshlrev_b32 v120,16,v80\n"
"v_and_b32 v121,0xffff0000,v80\n"
"v_mul_f32 v120,v120,v119\n"
"v_mul_f32 v121,v121,v119\n"
"v_cvt_pk_bf16_f32 v80,v120,v121\n"
"v_accvgpr_read_b32 v120,a129\n"
"v_mul_f32 v120,v120,v119\n"
"v_accvgpr_write_b32 a129,v120\n"
"v_lshlrev_b32 v120,16,v81\n"
"v_and_b32 v121,0xffff0000,v81\n"
"v_mul_f32 v120,v120,v119\n"
"v_mul_f32 v121,v121,v119\n"
"v_cvt_pk_bf16_f32 v81,v120,v121\n"
"v_accvgpr_read_b32 v120,a130\n"
"v_mul_f32 v120,v120,v119\n"
"v_accvgpr_write_b32 a130,v120\n"
"v_lshlrev_b32 v120,16,v82\n"
"v_and_b32 v121,0xffff0000,v82\n"
"v_mul_f32 v120,v120,v119\n"
"v_mul_f32 v121,v121,v119\n"
"v_cvt_pk_bf16_f32 v82,v120,v121\n"
"v_accvgpr_read_b32 v120,a131\n"
"v_mul_f32 v120,v120,v119\n"
"v_accvgpr_write_b32 a131,v120\n"
"v_lshlrev_b32 v120,16,v83\n"
"v_and_b32 v121,0xffff0000,v83\n"
"v_mul_f32 v120,v120,v119\n"
"v_mul_f32 v121,v121,v119\n"
"v_cvt_pk_bf16_f32 v83,v120,v121\n"
".Lfk_rare0_0:\n"
"s_bitcmp1_b32 s91,1\n"
"s_cbranch_scc0 .Lfk_rare0_1\n"
"v_max3_f32 v119,v8,v9,v10\n"
"v_max3_f32 v120,v11,v12,v13\n"
"v_max3_f32 v119,v119,v14,v15\n"
"v_max_f32 v119,v119,v120\n"
"v_mov_b32 v120,v119\n"
"s_nop 1\n"
"v_permlane16_swap_b32 v120,v119\n"
"v_max_f32 v119,v119,v120\n"
"v_mov_b32 v120,v119\n"
"s_nop 1\n"
"v_permlane32_swap_b32 v120,v119\n"
"v_max_f32 v119,v119,v120\n"
"s_bitcmp1_b32 s90,1\n"
"s_cselect_b32 s82,0xff800000,0\n"
"v_add_f32 v119,0x41980000,v119\n"
"v_max_f32 v119,s82,v119\n"
"v_cmp_lt_f32 vcc,s17,|v119|\n"
"s_nop 0\n"
"s_cbranch_vccz .Lfk_rare0_1\n"
"v_ceil_f32 v119,v119\n"
"v_sub_f32 v8,v8,v119\n"
"v_sub_f32 v9,v9,v119\n"
"v_sub_f32 v10,v10,v119\n"
"v_sub_f32 v11,v11,v119\n"
"v_sub_f32 v12,v12,v119\n"
"v_sub_f32 v13,v13,v119\n"
"v_sub_f32 v14,v14,v119\n"
"v_sub_f32 v15,v15,v119\n"
"v_sub_f32 v100,v100,v119\n"
"v_sub_f32 v101,v101,v119\n"
"v_sub_f32 v102,v102,v119\n"
"v_sub_f32 v103,v103,v119\n"
"s_cbranch_scc1 .Lfk_rare0_1\n"
"v_exp_f32 v119,-v119\n"
"s_nop 0\n"
"v_mul_f32 v160,v160,v119\n"
"v_mul_f32 v161,v161,v119\n"
"v_mul_f32 v162,v162,v119\n"
"v_mul_f32 v163,v163,v119\n"
"v_mul_f32 v164,v164,v119\n"
"v_mul_f32 v165,v165,v119\n"
"v_mul_f32 v166,v166,v119\n"
"v_mul_f32 v167,v167,v119\n"
"v_mul_f32 v168,v168,v119\n"
"v_mul_f32 v169,v169,v119\n"
"v_mul_f32 v170,v170,v119\n"
"v_mul_f32 v171,v171,v119\n"
"v_mul_f32 v172,v172,v119\n"
"v_mul_f32 v173,v173,v119\n"
"v_mul_f32 v174,v174,v119\n"
"v_mul_f32 v175,v175,v119\n"
"v_mul_f32 v176,v176,v119\n"
"v_mul_f32 v177,v177,v119\n"
"v_mul_f32 v178,v178,v119\n"
"v_mul_f32 v179,v179,v119\n"
"v_mul_f32 v180,v180,v119\n"
"v_mul_f32 v181,v181,v119\n"
"v_mul_f32 v182,v182,v119\n"
"v_mul_f32 v183,v183,v119\n"
"v_mul_f32 v184,v184,v119\n"
"v_mul_f32 v185,v185,v119\n"
"v_mul_f32 v186,v186,v119\n"
"v_mul_f32 v187,v187,v119\n"
"v_mul_f32 v188,v188,v119\n"
"v_mul_f32 v189,v189,v119\n"
"v_mul_f32 v190,v190,v119\n"
"v_mul_f32 v191,v191,v119\n"
"v_accvgpr_read_b32 v120,a132\n"
"v_mul_f32 v120,v120,v119\n"
"v_accvgpr_write_b32 a132,v120\n"
"v_lshlrev_b32 v120,16,v84\n"
"v_and_b32 v121,0xffff0000,v84\n"
"v_mul_f32 v120,v120,v119\n"
"v_mul_f32 v121,v121,v119\n"
"v_cvt_pk_bf16_f32 v84,v120,v121\n"
"v_accvgpr_read_b32 v120,a133\n"
"v_mul_f32 v120,v120,v119\n"
"v_accvgpr_write_b32 a133,v120\n"
"v_lshlrev_b32 v120,16,v85\n"
"v_and_b32 v121,0xffff0000,v85\n"
"v_mul_f32 v120,v120,v119\n"
"v_mul_f32 v121,v121,v119\n"
"v_cvt_pk_bf16_f32 v85,v120,v121\n"
"v_accvgpr_read_b32 v120,a134\n"
"v_mul_f32 v120,v120,v119\n"
"v_accvgpr_write_b32 a134,v120\n"
"v_lshlrev_b32 v120,16,v86\n"
"v_and_b32 v121,0xffff0000,v86\n"
"v_mul_f32 v120,v120,v119\n"
"v_mul_f32 v121,v121,v119\n"
"v_cvt_pk_bf16_f32 v86,v120,v121\n"
"v_accvgpr_read_b32 v120,a135\n"
"v_mul_f32 v120,v120,v119\n"
"v_accvgpr_write_b32 a135,v120\n"
"v_lshlrev_b32 v120,16,v87\n"
"v_and_b32 v121,0xffff0000,v87\n"
"v_mul_f32 v120,v120,v119\n"
"v_mul_f32 v121,v121,v119\n"
"v_cvt_pk_bf16_f32 v87,v120,v121\n"
".Lfk_rare0_1:\n"
"s_bitcmp1_b32 s91,2\n"
"s_cbranch_scc0 .Lfk_rare0_2\n"
"v_max3_f32 v119,v16,v17,v18\n"
"v_max3_f32 v120,v19,v20,v21\n"
"v_max3_f32 v119,v119,v22,v23\n"
"v_max_f32 v119,v119,v120\n"
"v_mov_b32 v120,v119\n"
"s_nop 1\n"
"v_permlane16_swap_b32 v120,v119\n"
"v_max_f32 v119,v119,v120\n"
"v_mov_b32 v120,v119\n"
"s_nop 1\n"
"v_permlane32_swap_b32 v120,v119\n"
"v_max_f32 v119,v119,v120\n"
"s_bitcmp1_b32 s90,2\n"
"s_cselect_b32 s82,0xff800000,0\n"
"v_add_f32 v119,0x41980000,v119\n"
"v_max_f32 v119,s82,v119\n"
"v_cmp_lt_f32 vcc,s17,|v119|\n"
"s_nop 0\n"
"s_cbranch_vccz .Lfk_rare0_2\n"
"v_ceil_f32 v119,v119\n"
"v_sub_f32 v16,v16,v119\n"
"v_sub_f32 v17,v17,v119\n"
"v_sub_f32 v18,v18,v119\n"
"v_sub_f32 v19,v19,v119\n"
"v_sub_f32 v20,v20,v119\n"
"v_sub_f32 v21,v21,v119\n"
"v_sub_f32 v22,v22,v119\n"
"v_sub_f32 v23,v23,v119\n"
"v_sub_f32 v104,v104,v119\n"
"v_sub_f32 v105,v105,v119\n"
"v_sub_f32 v106,v106,v119\n"
"v_sub_f32 v107,v107,v119\n"
"s_cbranch_scc1 .Lfk_rare0_2\n"
"v_exp_f32 v119,-v119\n"
"s_nop 0\n"
"v_mul_f32 v192,v192,v119\n"
"v_mul_f32 v193,v193,v119\n"
"v_mul_f32 v194,v194,v119\n"
"v_mul_f32 v195,v195,v119\n"
"v_mul_f32 v196,v196,v119\n"
"v_mul_f32 v197,v197,v119\n"
"v_mul_f32 v198,v198,v119\n"
"v_mul_f32 v199,v199,v119\n"
"v_mul_f32 v200,v200,v119\n"
"v_mul_f32 v201,v201,v119\n"
"v_mul_f32 v202,v202,v119\n"
"v_mul_f32 v203,v203,v119\n"
"v_mul_f32 v204,v204,v119\n"
"v_mul_f32 v205,v205,v119\n"
"v_mul_f32 v206,v206,v119\n"
"v_mul_f32 v207,v207,v119\n"
"v_mul_f32 v208,v208,v119\n"
"v_mul_f32 v209,v209,v119\n"
"v_mul_f32 v210,v210,v119\n"
"v_mul_f32 v211,v211,v119\n"
"v_mul_f32 v212,v212,v119\n"
"v_mul_f32 v213,v213,v119\n"
"v_mul_f32 v214,v214,v119\n"
"v_mul_f32 v215,v215,v119\n"
"v_mul_f32 v216,v216,v119\n"
"v_mul_f32 v217,v217,v119\n"
"v_mul_f32 v218,v218,v119\n"
"v_mul_f32 v219,v219,v119\n"
"v_mul_f32 v220,v220,v119\n"
"v_mul_f32 v221,v221,v119\n"
"v_mul_f32 v222,v222,v119\n"
"v_mul_f32 v223,v223,v119\n"
"v_accvgpr_read_b32 v120,a136\n"
"v_mul_f32 v120,v120,v119\n"
"v_accvgpr_write_b32 a136,v120\n"
"v_lshlrev_b32 v120,16,v88\n"
"v_and_b32 v121,0xffff0000,v88\n"
"v_mul_f32 v120,v120,v119\n"
"v_mul_f32 v121,v121,v119\n"
"v_cvt_pk_bf16_f32 v88,v120,v121\n"
"v_accvgpr_read_b32 v120,a137\n"
"v_mul_f32 v120,v120,v119\n"
"v_accvgpr_write_b32 a137,v120\n"
"v_lshlrev_b32 v120,16,v89\n"
"v_and_b32 v121,0xffff0000,v89\n"
"v_mul_f32 v120,v120,v119\n"
"v_mul_f32 v121,v121,v119\n"
"v_cvt_pk_bf16_f32 v89,v120,v121\n"
"v_accvgpr_read_b32 v120,a138\n"
"v_mul_f32 v120,v120,v119\n"
"v_accvgpr_write_b32 a138,v120\n"
"v_lshlrev_b32 v120,16,v90\n"
"v_and_b32 v121,0xffff0000,v90\n"
"v_mul_f32 v120,v120,v119\n"
"v_mul_f32 v121,v121,v119\n"
"v_cvt_pk_bf16_f32 v90,v120,v121\n"
"v_accvgpr_read_b32 v120,a139\n"
"v_mul_f32 v120,v120,v119\n"
"v_accvgpr_write_b32 a139,v120\n"
"v_lshlrev_b32 v120,16,v91\n"
"v_and_b32 v121,0xffff0000,v91\n"
"v_mul_f32 v120,v120,v119\n"
"v_mul_f32 v121,v121,v119\n"
"v_cvt_pk_bf16_f32 v91,v120,v121\n"
".Lfk_rare0_2:\n"
"s_bitcmp1_b32 s91,3\n"
"s_cbranch_scc0 .Lfk_rare0_3\n"
"v_max3_f32 v119,v24,v25,v26\n"
"v_max3_f32 v120,v27,v28,v29\n"
"v_max3_f32 v119,v119,v30,v31\n"
"v_max_f32 v119,v119,v120\n"
"v_mov_b32 v120,v119\n"
"s_nop 1\n"
"v_permlane16_swap_b32 v120,v119\n"
"v_max_f32 v119,v119,v120\n"
"v_mov_b32 v120,v119\n"
"s_nop 1\n"
"v_permlane32_swap_b32 v120,v119\n"
"v_max_f32 v119,v119,v120\n"
"s_bitcmp1_b32 s90,3\n"
"s_cselect_b32 s82,0xff800000,0\n"
"v_add_f32 v119,0x41980000,v119\n"
"v_max_f32 v119,s82,v119\n"
"v_cmp_lt_f32 vcc,s17,|v119|\n"
"s_nop 0\n"
"s_cbranch_vccz .Lfk_rare0_3\n"
"v_ceil_f32 v119,v119\n"
"v_sub_f32 v24,v24,v119\n"
"v_sub_f32 v25,v25,v119\n"
"v_sub_f32 v26,v26,v119\n"
"v_sub_f32 v27,v27,v119\n"
"v_sub_f32 v28,v28,v119\n"
"v_sub_f32 v29,v29,v119\n"
"v_sub_f32 v30,v30,v119\n"
"v_sub_f32 v31,v31,v119\n"
"v_sub_f32 v108,v108,v119\n"
"v_sub_f32 v109,v109,v119\n"
"v_sub_f32 v110,v110,v119\n"
"v_sub_f32 v111,v111,v119\n"
"s_cbranch_scc1 .Lfk_rare0_3\n"
"v_exp_f32 v119,-v119\n"
"s_nop 0\n"
"v_mul_f32 v224,v224,v119\n"
"v_mul_f32 v225,v225,v119\n"
"v_mul_f32 v226,v226,v119\n"
"v_mul_f32 v227,v227,v119\n"
"v_mul_f32 v228,v228,v119\n"
"v_mul_f32 v229,v229,v119\n"
"v_mul_f32 v230,v230,v119\n"
"v_mul_f32 v231,v231,v119\n"
"v_mul_f32 v232,v232,v119\n"
"v_mul_f32 v233,v233,v119\n"
"v_mul_f32 v234,v234,v119\n"
"v_mul_f32 v235,v235,v119\n"
"v_mul_f32 v236,v236,v119\n"
"v_mul_f32 v237,v237,v119\n"
"v_mul_f32 v238,v238,v119\n"
"v_mul_f32 v239,v239,v119\n"
"v_mul_f32 v240,v240,v119\n"
"v_mul_f32 v241,v241,v119\n"
"v_mul_f32 v242,v242,v119\n"
"v_mul_f32 v243,v243,v119\n"
"v_mul_f32 v244,v244,v119\n"
"v_mul_f32 v245,v245,v119\n"
"v_mul_f32 v246,v246,v119\n"
"v_mul_f32 v247,v247,v119\n"
"v_mul_f32 v248,v248,v119\n"
"v_mul_f32 v249,v249,v119\n"
"v_mul_f32 v250,v250,v119\n"
"v_mul_f32 v251,v251,v119\n"
"v_mul_f32 v252,v252,v119\n"
"v_mul_f32 v253,v253,v119\n"
"v_mul_f32 v254,v254,v119\n"
"v_mul_f32 v255,v255,v119\n"
"v_accvgpr_read_b32 v120,a140\n"
"v_mul_f32 v120,v120,v119\n"
"v_accvgpr_write_b32 a140,v120\n"
"v_lshlrev_b32 v120,16,v92\n"
"v_and_b32 v121,0xffff0000,v92\n"
"v_mul_f32 v120,v120,v119\n"
"v_mul_f32 v121,v121,v119\n"
"v_cvt_pk_bf16_f32 v92,v120,v121\n"
"v_accvgpr_read_b32 v120,a141\n"
"v_mul_f32 v120,v120,v119\n"
"v_accvgpr_write_b32 a141,v120\n"
"v_lshlrev_b32 v120,16,v93\n"
"v_and_b32 v121,0xffff0000,v93\n"
"v_mul_f32 v120,v120,v119\n"
"v_mul_f32 v121,v121,v119\n"
"v_cvt_pk_bf16_f32 v93,v120,v121\n"
"v_accvgpr_read_b32 v120,a142\n"
"v_mul_f32 v120,v120,v119\n"
"v_accvgpr_write_b32 a142,v120\n"
"v_lshlrev_b32 v120,16,v94\n"
"v_and_b32 v121,0xffff0000,v94\n"
"v_mul_f32 v120,v120,v119\n"
"v_mul_f32 v121,v121,v119\n"
"v_cvt_pk_bf16_f32 v94,v120,v121\n"
"v_accvgpr_read_b32 v120,a143\n"
"v_mul_f32 v120,v120,v119\n"
"v_accvgpr_write_b32 a143,v120\n"
"v_lshlrev_b32 v120,16,v95\n"
"v_and_b32 v121,0xffff0000,v95\n"
"v_mul_f32 v120,v120,v119\n"
"v_mul_f32 v121,v121,v119\n"
"v_cvt_pk_bf16_f32 v95,v120,v121\n"
".Lfk_rare0_3:\n"
"s_mov_b32 s90,0\n"
"s_nop 1\n"
"s_setpc_b64 s[94:95]\n"
".Lfk_rare2:\n"
"s_nop 7\n"
"v_max3_f32 v119,v0,v1,v2\n"
"v_max3_f32 v120,v3,v4,v5\n"
"v_max3_f32 v119,v119,v6,v7\n"
"v_max_f32 v119,v119,v120\n"
"v_mov_b32 v120,v119\n"
"s_nop 1\n"
"v_permlane16_swap_b32 v120,v119\n"
"v_max_f32 v119,v119,v120\n"
"v_mov_b32 v120,v119\n"
"s_nop 1\n"
"v_permlane32_swap_b32 v120,v119\n"
"v_max_f32 v119,v119,v120\n"
"s_bitcmp1_b32 s90,0\n"
"s_cselect_b32 s82,0xff800000,0\n"
"v_add_f32 v119,0x41980000,v119\n"
"v_max_f32 v119,s82,v119\n"
"v_cmp_lt_f32 vcc,s17,|v119|\n"
"s_nop 0\n"
"s_cbranch_vccz .Lfk_rare2_0\n"
"v_ceil_f32 v119,v119\n"
"v_sub_f32 v0,v0,v119\n"
"v_sub_f32 v1,v1,v119\n"
"v_sub_f32 v2,v2,v119\n"
"v_sub_f32 v3,v3,v119\n"
"v_sub_f32 v4,v4,v119\n"
"v_sub_f32 v5,v5,v119\n"
"v_sub_f32 v6,v6,v119\n"
"v_sub_f32 v7,v7,v119\n"
"v_sub_f32 v96,v96,v119\n"
"v_sub_f32 v97,v97,v119\n"
"v_sub_f32 v98,v98,v119\n"
"v_sub_f32 v99,v99,v119\n"
"v_sub_f32 v32,v32,v119\n"
"v_sub_f32 v33,v33,v119\n"
"v_sub_f32 v34,v34,v119\n"
"v_sub_f32 v35,v35,v119\n"
"v_sub_f32 v36,v36,v119\n"
"v_sub_f32 v37,v37,v119\n"
"v_sub_f32 v38,v38,v119\n"
"v_sub_f32 v39,v39,v119\n"
"v_exp_f32 v0,v0\n"
"v_exp_f32 v1,v1\n"
"v_exp_f32 v2,v2\n"
"v_exp_f32 v3,v3\n"
"v_exp_f32 v4,v4\n"
"v_exp_f32 v5,v5\n"
"v_exp_f32 v6,v6\n"
"v_exp_f32 v7,v7\n"
"v_cvt_pk_bf16_f32 v64,v0,v1\n"
"v_cvt_pk_bf16_f32 v65,v2,v3\n"
"v_cvt_pk_bf16_f32 v66,v4,v5\n"
"v_cvt_pk_bf16_f32 v67,v6,v7\n"
"s_cbranch_scc1 .Lfk_rare2_0\n"
"v_exp_f32 v119,-v119\n"
"s_nop 0\n"
"v_mul_f32 v128,v128,v119\n"
"v_mul_f32 v129,v129,v119\n"
"v_mul_f32 v130,v130,v119\n"
"v_mul_f32 v131,v131,v119\n"
"v_mul_f32 v132,v132,v119\n"
"v_mul_f32 v133,v133,v119\n"
"v_mul_f32 v134,v134,v119\n"
"v_mul_f32 v135,v135,v119\n"
"v_mul_f32 v136,v136,v119\n"
"v_mul_f32 v137,v137,v119\n"
"v_mul_f32 v138,v138,v119\n"
"v_mul_f32 v139,v139,v119\n"
"v_mul_f32 v140,v140,v119\n"
"v_mul_f32 v141,v141,v119\n"
"v_mul_f32 v142,v142,v119\n"
"v_mul_f32 v143,v143,v119\n"
"v_mul_f32 v144,v144,v119\n"
"v_mul_f32 v145,v145,v119\n"
"v_mul_f32 v146,v146,v119\n"
"v_mul_f32 v147,v147,v119\n"
"v_mul_f32 v148,v148,v119\n"
"v_mul_f32 v149,v149,v119\n"
"v_mul_f32 v150,v150,v119\n"
"v_mul_f32 v151,v151,v119\n"
"v_mul_f32 v152,v152,v119\n"
"v_mul_f32 v153,v153,v119\n"
"v_mul_f32 v154,v154,v119\n"
"v_mul_f32 v155,v155,v119\n"
"v_mul_f32 v156,v156,v119\n"
"v_mul_f32 v157,v157,v119\n"
"v_mul_f32 v158,v158,v119\n"
"v_mul_f32 v159,v159,v119\n"
"v_accvgpr_read_b32 v120,a128\n"
"v_mul_f32 v120,v120,v119\n"
"v_accvgpr_write_b32 a128,v120\n"
"v_accvgpr_read_b32 v120,a129\n"
"v_mul_f32 v120,v120,v119\n"
"v_accvgpr_write_b32 a129,v120\n"
"v_accvgpr_read_b32 v120,a130\n"
"v_mul_f32 v120,v120,v119\n"
"v_accvgpr_write_b32 a130,v120\n"
"v_accvgpr_read_b32 v120,a131\n"
"v_mul_f32 v120,v120,v119\n"
"v_accvgpr_write_b32 a131,v120\n"
".Lfk_rare2_0:\n"
"v_max3_f32 v119,v8,v9,v10\n"
"v_max3_f32 v120,v11,v12,v13\n"
"v_max3_f32 v119,v119,v14,v15\n"
"v_max_f32 v119,v119,v120\n"
"v_mov_b32 v120,v119\n"
"s_nop 1\n"
"v_permlane16_swap_b32 v120,v119\n"
"v_max_f32 v119,v119,v120\n"
"v_mov_b32 v120,v119\n"
"s_nop 1\n"
"v_permlane32_swap_b32 v120,v119\n"
"v_max_f32 v119,v119,v120\n"
"s_bitcmp1_b32 s90,1\n"
"s_cselect_b32 s82,0xff800000,0\n"
"v_add_f32 v119,0x41980000,v119\n"
"v_max_f32 v119,s82,v119\n"
"v_cmp_lt_f32 vcc,s17,|v119|\n"
"s_nop 0\n"
"s_cbranch_vccz .Lfk_rare2_1\n"
"v_ceil_f32 v119,v119\n"
"v_sub_f32 v8,v8,v119\n"
"v_sub_f32 v9,v9,v119\n"
"v_sub_f32 v10,v10,v119\n"
"v_sub_f32 v11,v11,v119\n"
"v_sub_f32 v12,v12,v119\n"
"v_sub_f32 v13,v13,v119\n"
"v_sub_f32 v14,v14,v119\n"
"v_sub_f32 v15,v15,v119\n"
"v_sub_f32 v100,v100,v119\n"
"v_sub_f32 v101,v101,v119\n"
"v_sub_f32 v102,v102,v119\n"
"v_sub_f32 v103,v103,v119\n"
"v_sub_f32 v40,v40,v119\n"
"v_sub_f32 v41,v41,v119\n"
"v_sub_f32 v42,v42,v119\n"
"v_sub_f32 v43,v43,v119\n"
"v_sub_f32 v44,v44,v119\n"
"v_sub_f32 v45,v45,v119\n"
"v_sub_f32 v46,v46,v119\n"
"v_sub_f32 v47,v47,v119\n"
"v_exp_f32 v8,v8\n"
"v_exp_f32 v9,v9\n"
"v_exp_f32 v10,v10\n"
"v_exp_f32 v11,v11\n"
"v_exp_f32 v12,v12\n"
"v_exp_f32 v13,v13\n"
"v_exp_f32 v14,v14\n"
"v_exp_f32 v15,v15\n"
"v_cvt_pk_bf16_f32 v68,v8,v9\n"
"v_cvt_pk_bf16_f32 v69,v10,v11\n"
"v_cvt_pk_bf16_f32 v70,v12,v13\n"
"v_cvt_pk_bf16_f32 v71,v14,v15\n"
"s_cbranch_scc1 .Lfk_rare2_1\n"
"v_exp_f32 v119,-v119\n"
"s_nop 0\n"
"v_mul_f32 v160,v160,v119\n"
"v_mul_f32 v161,v161,v119\n"
"v_mul_f32 v162,v162,v119\n"
"v_mul_f32 v163,v163,v119\n"
"v_mul_f32 v164,v164,v119\n"
"v_mul_f32 v165,v165,v119\n"
"v_mul_f32 v166,v166,v119\n"
"v_mul_f32 v167,v167,v119\n"
"v_mul_f32 v168,v168,v119\n"
"v_mul_f32 v169,v169,v119\n"
"v_mul_f32 v170,v170,v119\n"
"v_mul_f32 v171,v171,v119\n"
"v_mul_f32 v172,v172,v119\n"
"v_mul_f32 v173,v173,v119\n"
"v_mul_f32 v174,v174,v119\n"
"v_mul_f32 v175,v175,v119\n"
"v_mul_f32 v176,v176,v119\n"
"v_mul_f32 v177,v177,v119\n"
"v_mul_f32 v178,v178,v119\n"
"v_mul_f32 v179,v179,v119\n"
"v_mul_f32 v180,v180,v119\n"
"v_mul_f32 v181,v181,v119\n"
"v_mul_f32 v182,v182,v119\n"
"v_mul_f32 v183,v183,v119\n"
"v_mul_f32 v184,v184,v119\n"
"v_mul_f32 v185,v185,v119\n"
"v_mul_f32 v186,v186,v119\n"
"v_mul_f32 v187,v187,v119\n"
"v_mul_f32 v188,v188,v119\n"
"v_mul_f32 v189,v189,v119\n"
"v_mul_f32 v190,v190,v119\n"
"v_mul_f32 v191,v191,v119\n"
"v_accvgpr_read_b32 v120,a132\n"
"v_mul_f32 v120,v120,v119\n"
"v_accvgpr_write_b32 a132,v120\n"
"v_accvgpr_read_b32 v120,a133\n"
"v_mul_f32 v120,v120,v119\n"
"v_accvgpr_write_b32 a133,v120\n"
"v_accvgpr_read_b32 v120,a134\n"
"v_mul_f32 v120,v120,v119\n"
"v_accvgpr_write_b32 a134,v120\n"
"v_accvgpr_read_b32 v120,a135\n"
"v_mul_f32 v120,v120,v119\n"
"v_accvgpr_write_b32 a135,v120\n"
".Lfk_rare2_1:\n"
"v_max3_f32 v119,v16,v17,v18\n"
"v_max3_f32 v120,v19,v20,v21\n"
"v_max3_f32 v119,v119,v22,v23\n"
"v_max_f32 v119,v119,v120\n"
"v_mov_b32 v120,v119\n"
"s_nop 1\n"
"v_permlane16_swap_b32 v120,v119\n"
"v_max_f32 v119,v119,v120\n"
"v_mov_b32 v120,v119\n"
"s_nop 1\n"
"v_permlane32_swap_b32 v120,v119\n"
"v_max_f32 v119,v119,v120\n"
"s_bitcmp1_b32 s90,2\n"
"s_cselect_b32 s82,0xff800000,0\n"
"v_add_f32 v119,0x41980000,v119\n"
"v_max_f32 v119,s82,v119\n"
"v_cmp_lt_f32 vcc,s17,|v119|\n"
"s_nop 0\n"
"s_cbranch_vccz .Lfk_rare2_2\n"
"v_ceil_f32 v119,v119\n"
"v_sub_f32 v16,v16,v119\n"
"v_sub_f32 v17,v17,v119\n"
"v_sub_f32 v18,v18,v119\n"
"v_sub_f32 v19,v19,v119\n"
"v_sub_f32 v20,v20,v119\n"
"v_sub_f32 v21,v21,v119\n"
"v_sub_f32 v22,v22,v119\n"
"v_sub_f32 v23,v23,v119\n"
"v_sub_f32 v104,v104,v119\n"
"v_sub_f32 v105,v105,v119\n"
"v_sub_f32 v106,v106,v119\n"
"v_sub_f32 v107,v107,v119\n"
"v_sub_f32 v48,v48,v119\n"
"v_sub_f32 v49,v49,v119\n"
"v_sub_f32 v50,v50,v119\n"
"v_sub_f32 v51,v51,v119\n"
"v_sub_f32 v52,v52,v119\n"
"v_sub_f32 v53,v53,v119\n"
"v_sub_f32 v54,v54,v119\n"
"v_sub_f32 v55,v55,v119\n"
"v_exp_f32 v16,v16\n"
"v_exp_f32 v17,v17\n"
"v_exp_f32 v18,v18\n"
"v_exp_f32 v19,v19\n"
"v_exp_f32 v20,v20\n"
"v_exp_f32 v21,v21\n"
"v_exp_f32 v22,v22\n"
"v_exp_f32 v23,v23\n"
"v_cvt_pk_bf16_f32 v72,v16,v17\n"
"v_cvt_pk_bf16_f32 v73,v18,v19\n"
"v_cvt_pk_bf16_f32 v74,v20,v21\n"
"v_cvt_pk_bf16_f32 v75,v22,v23\n"
"s_cbranch_scc1 .Lfk_rare2_2\n"
"v_exp_f32 v119,-v119\n"
"s_nop 0\n"
"v_mul_f32 v192,v192,v119\n"
"v_mul_f32 v193,v193,v119\n"
"v_mul_f32 v194,v194,v119\n"
"v_mul_f32 v195,v195,v119\n"
"v_mul_f32 v196,v196,v119\n"
"v_mul_f32 v197,v197,v119\n"
"v_mul_f32 v198,v198,v119\n"
"v_mul_f32 v199,v199,v119\n"
"v_mul_f32 v200,v200,v119\n"
"v_mul_f32 v201,v201,v119\n"
"v_mul_f32 v202,v202,v119\n"
"v_mul_f32 v203,v203,v119\n"
"v_mul_f32 v204,v204,v119\n"
"v_mul_f32 v205,v205,v119\n"
"v_mul_f32 v206,v206,v119\n"
"v_mul_f32 v207,v207,v119\n"
"v_mul_f32 v208,v208,v119\n"
"v_mul_f32 v209,v209,v119\n"
"v_mul_f32 v210,v210,v119\n"
"v_mul_f32 v211,v211,v119\n"
"v_mul_f32 v212,v212,v119\n"
"v_mul_f32 v213,v213,v119\n"
"v_mul_f32 v214,v214,v119\n"
"v_mul_f32 v215,v215,v119\n"
"v_mul_f32 v216,v216,v119\n"
"v_mul_f32 v217,v217,v119\n"
"v_mul_f32 v218,v218,v119\n"
"v_mul_f32 v219,v219,v119\n"
"v_mul_f32 v220,v220,v119\n"
"v_mul_f32 v221,v221,v119\n"
"v_mul_f32 v222,v222,v119\n"
"v_mul_f32 v223,v223,v119\n"
"v_accvgpr_read_b32 v120,a136\n"
"v_mul_f32 v120,v120,v119\n"
"v_accvgpr_write_b32 a136,v120\n"
"v_accvgpr_read_b32 v120,a137\n"
"v_mul_f32 v120,v120,v119\n"
"v_accvgpr_write_b32 a137,v120\n"
"v_accvgpr_read_b32 v120,a138\n"
"v_mul_f32 v120,v120,v119\n"
"v_accvgpr_write_b32 a138,v120\n"
"v_accvgpr_read_b32 v120,a139\n"
"v_mul_f32 v120,v120,v119\n"
"v_accvgpr_write_b32 a139,v120\n"
".Lfk_rare2_2:\n"
"v_max3_f32 v119,v24,v25,v26\n"
"v_max3_f32 v120,v27,v28,v29\n"
"v_max3_f32 v119,v119,v30,v31\n"
"v_max_f32 v119,v119,v120\n"
"v_mov_b32 v120,v119\n"
"s_nop 1\n"
"v_permlane16_swap_b32 v120,v119\n"
"v_max_f32 v119,v119,v120\n"
"v_mov_b32 v120,v119\n"
"s_nop 1\n"
"v_permlane32_swap_b32 v120,v119\n"
"v_max_f32 v119,v119,v120\n"
"s_bitcmp1_b32 s90,3\n"
"s_cselect_b32 s82,0xff800000,0\n"
"v_add_f32 v119,0x41980000,v119\n"
"v_max_f32 v119,s82,v119\n"
"v_cmp_lt_f32 vcc,s17,|v119|\n"
"s_nop 0\n"
"s_cbranch_vccz .Lfk_rare2_3\n"
"v_ceil_f32 v119,v119\n"
"v_sub_f32 v24,v24,v119\n"
"v_sub_f32 v25,v25,v119\n"
"v_sub_f32 v26,v26,v119\n"
"v_sub_f32 v27,v27,v119\n"
"v_sub_f32 v28,v28,v119\n"
"v_sub_f32 v29,v29,v119\n"
"v_sub_f32 v30,v30,v119\n"
"v_sub_f32 v31,v31,v119\n"
"v_sub_f32 v108,v108,v119\n"
"v_sub_f32 v109,v109,v119\n"
"v_sub_f32 v110,v110,v119\n"
"v_sub_f32 v111,v111,v119\n"
"v_sub_f32 v56,v56,v119\n"
"v_sub_f32 v57,v57,v119\n"
"v_sub_f32 v58,v58,v119\n"
"v_sub_f32 v59,v59,v119\n"
"v_sub_f32 v60,v60,v119\n"
"v_sub_f32 v61,v61,v119\n"
"v_sub_f32 v62,v62,v119\n"
"v_sub_f32 v63,v63,v119\n"
"v_exp_f32 v24,v24\n"
"v_exp_f32 v25,v25\n"
"v_exp_f32 v26,v26\n"
"v_exp_f32 v27,v27\n"
"v_exp_f32 v28,v28\n"
"v_exp_f32 v29,v29\n"
"v_exp_f32 v30,v30\n"
"v_exp_f32 v31,v31\n"
"v_cvt_pk_bf16_f32 v76,v24,v25\n"
"v_cvt_pk_bf16_f32 v77,v26,v27\n"
"v_cvt_pk_bf16_f32 v78,v28,v29\n"
"v_cvt_pk_bf16_f32 v79,v30,v31\n"
"s_cbranch_scc1 .Lfk_rare2_3\n"
"v_exp_f32 v119,-v119\n"
"s_nop 0\n"
"v_mul_f32 v224,v224,v119\n"
"v_mul_f32 v225,v225,v119\n"
"v_mul_f32 v226,v226,v119\n"
"v_mul_f32 v227,v227,v119\n"
"v_mul_f32 v228,v228,v119\n"
"v_mul_f32 v229,v229,v119\n"
"v_mul_f32 v230,v230,v119\n"
"v_mul_f32 v231,v231,v119\n"
"v_mul_f32 v232,v232,v119\n"
"v_mul_f32 v233,v233,v119\n"
"v_mul_f32 v234,v234,v119\n"
"v_mul_f32 v235,v235,v119\n"
"v_mul_f32 v236,v236,v119\n"
"v_mul_f32 v237,v237,v119\n"
"v_mul_f32 v238,v238,v119\n"
"v_mul_f32 v239,v239,v119\n"
"v_mul_f32 v240,v240,v119\n"
"v_mul_f32 v241,v241,v119\n"
"v_mul_f32 v242,v242,v119\n"
"v_mul_f32 v243,v243,v119\n"
"v_mul_f32 v244,v244,v119\n"
"v_mul_f32 v245,v245,v119\n"
"v_mul_f32 v246,v246,v119\n"
"v_mul_f32 v247,v247,v119\n"
"v_mul_f32 v248,v248,v119\n"
"v_mul_f32 v249,v249,v119\n"
"v_mul_f32 v250,v250,v119\n"
"v_mul_f32 v251,v251,v119\n"
"v_mul_f32 v252,v252,v119\n"
"v_mul_f32 v253,v253,v119\n"
"v_mul_f32 v254,v254,v119\n"
"v_mul_f32 v255,v255,v119\n"
"v_accvgpr_read_b32 v120,a140\n"
"v_mul_f32 v120,v120,v119\n"
"v_accvgpr_write_b32 a140,v120\n"
"v_accvgpr_read_b32 v120,a141\n"
"v_mul_f32 v120,v120,v119\n"
"v_accvgpr_write_b32 a141,v120\n"
"v_accvgpr_read_b32 v120,a142\n"
"v_mul_f32 v120,v120,v119\n"
"v_accvgpr_write_b32 a142,v120\n"
"v_accvgpr_read_b32 v120,a143\n"
"v_mul_f32 v120,v120,v119\n"
"v_accvgpr_write_b32 a143,v120\n"
".Lfk_rare2_3:\n"
"s_mov_b32 s90,0\n"
"s_nop 1\n"
"s_setpc_b64 s[94:95]\n"
".Lfk_mslow0:\n"
"s_nop 7\n"
"v_min_f32 v119,v98,v104\n"
"v_sub_f32 v120,v119,v104\n"
"v_sub_f32 v119,v119,v98\n"
"v_exp_f32 v120,v120\n"
"v_exp_f32 v119,v119\n"
"v_mul_f32 v192,v192,v120\n"
"v_mul_f32 v193,v193,v120\n"
"v_mul_f32 v194,v194,v120\n"
"v_mul_f32 v195,v195,v120\n"
"v_mul_f32 v196,v196,v120\n"
"v_mul_f32 v197,v197,v120\n"
"v_mul_f32 v198,v198,v120\n"
"v_mul_f32 v199,v199,v120\n"
"v_mul_f32 v200,v200,v120\n"
"v_mul_f32 v201,v201,v120\n"
"v_mul_f32 v202,v202,v120\n"
"v_mul_f32 v203,v203,v120\n"
"v_mul_f32 v204,v204,v120\n"
"v_mul_f32 v205,v205,v120\n"
"v_mul_f32 v206,v206,v120\n"
"v_mul_f32 v207,v207,v120\n"
"v_mul_f32 v208,v208,v120\n"
"v_mul_f32 v209,v209,v120\n"
"v_mul_f32 v210,v210,v120\n"
"v_mul_f32 v211,v211,v120\n"
"v_mul_f32 v212,v212,v120\n"
"v_mul_f32 v213,v213,v120\n"
"v_mul_f32 v214,v214,v120\n"
"v_mul_f32 v215,v215,v120\n"
"v_mul_f32 v216,v216,v120\n"
"v_mul_f32 v217,v217,v120\n"
"v_mul_f32 v218,v218,v120\n"
"v_mul_f32 v219,v219,v120\n"
"v_mul_f32 v220,v220,v120\n"
"v_mul_f32 v221,v221,v120\n"
"v_mul_f32 v222,v222,v120\n"
"v_mul_f32 v223,v223,v120\n"
"v_accvgpr_read_b32 v121,a136\n"
"v_mul_f32 v121,v121,v120\n"
"v_accvgpr_write_b32 a136,v121\n"
"v_mul_f32 v99,v99,v119\n"
"v_accvgpr_read_b32 v121,a64\n"
"v_lshlrev_b32 v120,16,v121\n"
"v_and_b32 v121,0xffff0000,v121\n"
"v_mul_f32 v120,v120,v119\n"
"v_mul_f32 v121,v121,v119\n"
"v_cvt_pk_bf16_f32 v121,v120,v121\n"
"v_accvgpr_write_b32 a64,v121\n"
"v_accvgpr_read_b32 v121,a65\n"
"v_lshlrev_b32 v120,16,v121\n"
"v_and_b32 v121,0xffff0000,v121\n"
"v_mul_f32 v120,v120,v119\n"
"v_mul_f32 v121,v121,v119\n"
"v_cvt_pk_bf16_f32 v121,v120,v121\n"
"v_accvgpr_write_b32 a65,v121\n"
"v_accvgpr_read_b32 v121,a66\n"
"v_lshlrev_b32 v120,16,v121\n"
"v_and_b32 v121,0xffff0000,v121\n"
"v_mul_f32 v120,v120,v119\n"
"v_mul_f32 v121,v121,v119\n"
"v_cvt_pk_bf16_f32 v121,v120,v121\n"
"v_accvgpr_write_b32 a66,v121\n"
"v_accvgpr_read_b32 v121,a67\n"
"v_lshlrev_b32 v120,16,v121\n"
"v_and_b32 v121,0xffff0000,v121\n"
"v_mul_f32 v120,v120,v119\n"
"v_mul_f32 v121,v121,v119\n"
"v_cvt_pk_bf16_f32 v121,v120,v121\n"
"v_accvgpr_write_b32 a67,v121\n"
"v_accvgpr_read_b32 v121,a68\n"
"v_lshlrev_b32 v120,16,v121\n"
"v_and_b32 v121,0xffff0000,v121\n"
"v_mul_f32 v120,v120,v119\n"
"v_mul_f32 v121,v121,v119\n"
"v_cvt_pk_bf16_f32 v121,v120,v121\n"
"v_accvgpr_write_b32 a68,v121\n"
"v_accvgpr_read_b32 v121,a69\n"
"v_lshlrev_b32 v120,16,v121\n"
"v_and_b32 v121,0xffff0000,v121\n"
"v_mul_f32 v120,v120,v119\n"
"v_mul_f32 v121,v121,v119\n"
"v_cvt_pk_bf16_f32 v121,v120,v121\n"
"v_accvgpr_write_b32 a69,v121\n"
"v_accvgpr_read_b32 v121,a70\n"
"v_lshlrev_b32 v120,16,v121\n"
"v_and_b32 v121,0xffff0000,v121\n"
"v_mul_f32 v120,v120,v119\n"
"v_mul_f32 v121,v121,v119\n"
"v_cvt_pk_bf16_f32 v121,v120,v121\n"
"v_accvgpr_write_b32 a70,v121\n"
"v_accvgpr_read_b32 v121,a71\n"
"v_lshlrev_b32 v120,16,v121\n"
"v_and_b32 v121,0xffff0000,v121\n"
"v_mul_f32 v120,v120,v119\n"
"v_mul_f32 v121,v121,v119\n"
"v_cvt_pk_bf16_f32 v121,v120,v121\n"
"v_accvgpr_write_b32 a71,v121\n"
"v_accvgpr_read_b32 v121,a72\n"
"v_lshlrev_b32 v120,16,v121\n"
"v_and_b32 v121,0xffff0000,v121\n"
"v_mul_f32 v120,v120,v119\n"
"v_mul_f32 v121,v121,v119\n"
"v_cvt_pk_bf16_f32 v121,v120,v121\n"
"v_accvgpr_write_b32 a72,v121\n"
"v_accvgpr_read_b32 v121,a73\n"
"v_lshlrev_b32 v120,16,v121\n"
"v_and_b32 v121,0xffff0000,v121\n"
"v_mul_f32 v120,v120,v119\n"
"v_mul_f32 v121,v121,v119\n"
"v_cvt_pk_bf16_f32 v121,v120,v121\n"
"v_accvgpr_write_b32 a73,v121\n"
"v_accvgpr_read_b32 v121,a74\n"
"v_lshlrev_b32 v120,16,v121\n"
"v_and_b32 v121,0xffff0000,v121\n"
"v_mul_f32 v120,v120,v119\n"
"v_mul_f32 v121,v121,v119\n"
"v_cvt_pk_bf16_f32 v121,v120,v121\n"
"v_accvgpr_write_b32 a74,v121\n"
"v_accvgpr_read_b32 v121,a75\n"
"v_lshlrev_b32 v120,16,v121\n"
"v_and_b32 v121,0xffff0000,v121\n"
"v_mul_f32 v120,v120,v119\n"
"v_mul_f32 v121,v121,v119\n"
"v_cvt_pk_bf16_f32 v121,v120,v121\n"
"v_accvgpr_write_b32 a75,v121\n"
"v_accvgpr_read_b32 v121,a76\n"
"v_lshlrev_b32 v120,16,v121\n"
"v_and_b32 v121,0xffff0000,v121\n"
"v_mul_f32 v120,v120,v119\n"
"v_mul_f32 v121,v121,v119\n"
"v_cvt_pk_bf16_f32 v121,v120,v121\n"
"v_accvgpr_write_b32 a76,v121\n"
"v_accvgpr_read_b32 v121,a77\n"
"v_lshlrev_b32 v120,16,v121\n"
"v_and_b32 v121,0xffff0000,v121\n"
"v_mul_f32 v120,v120,v119\n"
"v_mul_f32 v121,v121,v119\n"
"v_cvt_pk_bf16_f32 v121,v120,v121\n"
"v_accvgpr_write_b32 a77,v121\n"
"v_accvgpr_read_b32 v121,a78\n"
"v_lshlrev_b32 v120,16,v121\n"
"v_and_b32 v121,0xffff0000,v121\n"
"v_mul_f32 v120,v120,v119\n"
"v_mul_f32 v121,v121,v119\n"
"v_cvt_pk_bf16_f32 v121,v120,v121\n"
"v_accvgpr_write_b32 a78,v121\n"
"v_accvgpr_read_b32 v121,a79\n"
"v_lshlrev_b32 v120,16,v121\n"
"v_and_b32 v121,0xffff0000,v121\n"
"v_mul_f32 v120,v120,v119\n"
"v_mul_f32 v121,v121,v119\n"
"v_cvt_pk_bf16_f32 v121,v120,v121\n"
"v_accvgpr_write_b32 a79,v121\n"
"s_nop 1\n"
"s_setpc_b64 s[94:95]\n"
".Lfk_rare1:\n"
"s_nop 7\n"
"s_bitcmp1_b32 s91,0\n"
"s_cbranch_scc0 .Lfk_rare1_0\n"
"v_max3_f32 v119,v32,v33,v34\n"
"v_max3_f32 v120,v35,v36,v37\n"
"v_max3_f32 v119,v119,v38,v39\n"
"v_max_f32 v119,v119,v120\n"
"v_mov_b32 v120,v119\n"
"s_nop 1\n"
"v_permlane16_swap_b32 v120,v119\n"
"v_max_f32 v119,v119,v120\n"
"v_mov_b32 v120,v119\n"
"s_nop 1\n"
"v_permlane32_swap_b32 v120,v119\n"
"v_max_f32 v119,v119,v120\n"
"s_bitcmp1_b32 s90,0\n"
"s_cselect_b32 s82,0xff800000,0\n"
"v_add_f32 v119,0x41980000,v119\n"
"v_max_f32 v119,s82,v119\n"
"v_cmp_lt_f32 vcc,s17,|v119|\n"
"s_nop 0\n"
"s_cbranch_vccz .Lfk_rare1_0\n"
"v_ceil_f32 v119,v119\n"
"v_sub_f32 v32,v32,v119\n"
"v_sub_f32 v33,v33,v119\n"
"v_sub_f32 v34,v34,v119\n"
"v_sub_f32 v35,v35,v119\n"
"v_sub_f32 v36,v36,v119\n"
"v_sub_f32 v37,v37,v119\n"
"v_sub_f32 v38,v38,v119\n"
"v_sub_f32 v39,v39,v119\n"
"v_sub_f32 v96,v96,v119\n"
"v_sub_f32 v97,v97,v119\n"
"v_sub_f32 v98,v98,v119\n"
"v_sub_f32 v99,v99,v119\n"
"s_cbranch_scc1 .Lfk_rare1_0\n"
"v_exp_f32 v119,-v119\n"
"s_nop 0\n"
"v_mul_f32 v128,v128,v119\n"
"v_mul_f32 v129,v129,v119\n"
"v_mul_f32 v130,v130,v119\n"
"v_mul_f32 v131,v131,v119\n"
"v_mul_f32 v132,v132,v119\n"
"v_mul_f32 v133,v133,v119\n"
"v_mul_f32 v134,v134,v119\n"
"v_mul_f32 v135,v135,v119\n"
"v_mul_f32 v136,v136,v119\n"
"v_mul_f32 v137,v137,v119\n"
"v_mul_f32 v138,v138,v119\n"
"v_mul_f32 v139,v139,v119\n"
"v_mul_f32 v140,v140,v119\n"
"v_mul_f32 v141,v141,v119\n"
"v_mul_f32 v142,v142,v119\n"
"v_mul_f32 v143,v143,v119\n"
"v_mul_f32 v144,v144,v119\n"
"v_mul_f32 v145,v145,v119\n"
"v_mul_f32 v146,v146,v119\n"
"v_mul_f32 v147,v147,v119\n"
"v_mul_f32 v148,v148,v119\n"
"v_mul_f32 v149,v149,v119\n"
"v_mul_f32 v150,v150,v119\n"
"v_mul_f32 v151,v151,v119\n"
"v_mul_f32 v152,v152,v119\n"
"v_mul_f32 v153,v153,v119\n"
"v_mul_f32 v154,v154,v119\n"
"v_mul_f32 v155,v155,v119\n"
"v_mul_f32 v156,v156,v119\n"
"v_mul_f32 v157,v157,v119\n"
"v_mul_f32 v158,v158,v119\n"
"v_mul_f32 v159,v159,v119\n"
"v_accvgpr_read_b32 v120,a128\n"
"v_mul_f32 v120,v120,v119\n"
"v_accvgpr_write_b32 a128,v120\n"
"v_lshlrev_b32 v120,16,v64\n"
"v_and_b32 v121,0xffff0000,v64\n"
"v_mul_f32 v120,v120,v119\n"
"v_mul_f32 v121,v121,v119\n"
"v_cvt_pk_bf16_f32 v64,v120,v121\n"
"v_accvgpr_read_b32 v120,a129\n"
"v_mul_f32 v120,v120,v119\n"
"v_accvgpr_write_b32 a129,v120\n"
"v_lshlrev_b32 v120,16,v65\n"
"v_and_b32 v121,0xffff0000,v65\n"
"v_mul_f32 v120,v120,v119\n"
"v_mul_f32 v121,v121,v119\n"
"v_cvt_pk_bf16_f32 v65,v120,v121\n"
"v_accvgpr_read_b32 v120,a130\n"
"v_mul_f32 v120,v120,v119\n"
"v_accvgpr_write_b32 a130,v120\n"
"v_lshlrev_b32 v120,16,v66\n"
"v_and_b32 v121,0xffff0000,v66\n"
"v_mul_f32 v120,v120,v119\n"
"v_mul_f32 v121,v121,v119\n"
"v_cvt_pk_bf16_f32 v66,v120,v121\n"
"v_accvgpr_read_b32 v120,a131\n"
"v_mul_f32 v120,v120,v119\n"
"v_accvgpr_write_b32 a131,v120\n"
"v_lshlrev_b32 v120,16,v67\n"
"v_and_b32 v121,0xffff0000,v67\n"
"v_mul_f32 v120,v120,v119\n"
"v_mul_f32 v121,v121,v119\n"
"v_cvt_pk_bf16_f32 v67,v120,v121\n"
".Lfk_rare1_0:\n"
"s_bitcmp1_b32 s91,1\n"
"s_cbranch_scc0 .Lfk_rare1_1\n"
"v_max3_f32 v119,v40,v41,v42\n"
"v_max3_f32 v120,v43,v44,v45\n"
"v_max3_f32 v119,v119,v46,v47\n"
"v_max_f32 v119,v119,v120\n"
"v_mov_b32 v120,v119\n"
"s_nop 1\n"
"v_permlane16_swap_b32 v120,v119\n"
"v_max_f32 v119,v119,v120\n"
"v_mov_b32 v120,v119\n"
"s_nop 1\n"
"v_permlane32_swap_b32 v120,v119\n"
"v_max_f32 v119,v119,v120\n"
"s_bitcmp1_b32 s90,1\n"
"s_cselect_b32 s82,0xff800000,0\n"
"v_add_f32 v119,0x41980000,v119\n"
"v_max_f32 v119,s82,v119\n"
"v_cmp_lt_f32 vcc,s17,|v119|\n"
"s_nop 0\n"
"s_cbranch_vccz .Lfk_rare1_1\n"
"v_ceil_f32 v119,v119\n"
"v_sub_f32 v40,v40,v119\n"
"v_sub_f32 v41,v41,v119\n"
"v_sub_f32 v42,v42,v119\n"
"v_sub_f32 v43,v43,v119\n"
"v_sub_f32 v44,v44,v119\n"
"v_sub_f32 v45,v45,v119\n"
"v_sub_f32 v46,v46,v119\n"
"v_sub_f32 v47,v47,v119\n"
"v_sub_f32 v100,v100,v119\n"
"v_sub_f32 v101,v101,v119\n"
"v_sub_f32 v102,v102,v119\n"
"v_sub_f32 v103,v103,v119\n"
"s_cbranch_scc1 .Lfk_rare1_1\n"
"v_exp_f32 v119,-v119\n"
"s_nop 0\n"
"v_mul_f32 v160,v160,v119\n"
"v_mul_f32 v161,v161,v119\n"
"v_mul_f32 v162,v162,v119\n"
"v_mul_f32 v163,v163,v119\n"
"v_mul_f32 v164,v164,v119\n"
"v_mul_f32 v165,v165,v119\n"
"v_mul_f32 v166,v166,v119\n"
"v_mul_f32 v167,v167,v119\n"
"v_mul_f32 v168,v168,v119\n"
"v_mul_f32 v169,v169,v119\n"
"v_mul_f32 v170,v170,v119\n"
"v_mul_f32 v171,v171,v119\n"
"v_mul_f32 v172,v172,v119\n"
"v_mul_f32 v173,v173,v119\n"
"v_mul_f32 v174,v174,v119\n"
"v_mul_f32 v175,v175,v119\n"
"v_mul_f32 v176,v176,v119\n"
"v_mul_f32 v177,v177,v119\n"
"v_mul_f32 v178,v178,v119\n"
"v_mul_f32 v179,v179,v119\n"
"v_mul_f32 v180,v180,v119\n"
"v_mul_f32 v181,v181,v119\n"
"v_mul_f32 v182,v182,v119\n"
"v_mul_f32 v183,v183,v119\n"
"v_mul_f32 v184,v184,v119\n"
"v_mul_f32 v185,v185,v119\n"
"v_mul_f32 v186,v186,v119\n"
"v_mul_f32 v187,v187,v119\n"
"v_mul_f32 v188,v188,v119\n"
"v_mul_f32 v189,v189,v119\n"
"v_mul_f32 v190,v190,v119\n"
"v_mul_f32 v191,v191,v119\n"
"v_accvgpr_read_b32 v120,a132\n"
"v_mul_f32 v120,v120,v119\n"
"v_accvgpr_write_b32 a132,v120\n"
"v_lshlrev_b32 v120,16,v68\n"
"v_and_b32 v121,0xffff0000,v68\n"
"v_mul_f32 v120,v120,v119\n"
"v_mul_f32 v121,v121,v119\n"
"v_cvt_pk_bf16_f32 v68,v120,v121\n"
"v_accvgpr_read_b32 v120,a133\n"
"v_mul_f32 v120,v120,v119\n"
"v_accvgpr_write_b32 a133,v120\n"
"v_lshlrev_b32 v120,16,v69\n"
"v_and_b32 v121,0xffff0000,v69\n"
"v_mul_f32 v120,v120,v119\n"
"v_mul_f32 v121,v121,v119\n"
"v_cvt_pk_bf16_f32 v69,v120,v121\n"
"v_accvgpr_read_b32 v120,a134\n"
"v_mul_f32 v120,v120,v119\n"
"v_accvgpr_write_b32 a134,v120\n"
"v_lshlrev_b32 v120,16,v70\n"
"v_and_b32 v121,0xffff0000,v70\n"
"v_mul_f32 v120,v120,v119\n"
"v_mul_f32 v121,v121,v119\n"
"v_cvt_pk_bf16_f32 v70,v120,v121\n"
"v_accvgpr_read_b32 v120,a135\n"
"v_mul_f32 v120,v120,v119\n"
"v_accvgpr_write_b32 a135,v120\n"
"v_lshlrev_b32 v120,16,v71\n"
"v_and_b32 v121,0xffff0000,v71\n"
"v_mul_f32 v120,v120,v119\n"
"v_mul_f32 v121,v121,v119\n"
"v_cvt_pk_bf16_f32 v71,v120,v121\n"
".Lfk_rare1_1:\n"
"s_bitcmp1_b32 s91,2\n"
"s_cbranch_scc0 .Lfk_rare1_2\n"
"v_max3_f32 v119,v48,v49,v50\n"
"v_max3_f32 v120,v51,v52,v53\n"
"v_max3_f32 v119,v119,v54,v55\n"
"v_max_f32 v119,v119,v120\n"
"v_mov_b32 v120,v119\n"
"s_nop 1\n"
"v_permlane16_swap_b32 v120,v119\n"
"v_max_f32 v119,v119,v120\n"
"v_mov_b32 v120,v119\n"
"s_nop 1\n"
"v_permlane32_swap_b32 v120,v119\n"
"v_max_f32 v119,v119,v120\n"
"s_bitcmp1_b32 s90,2\n"
"s_cselect_b32 s82,0xff800000,0\n"
"v_add_f32 v119,0x41980000,v119\n"
"v_max_f32 v119,s82,v119\n"
"v_cmp_lt_f32 vcc,s17,|v119|\n"
"s_nop 0\n"
"s_cbranch_vccz .Lfk_rare1_2\n"
"v_ceil_f32 v119,v119\n"
"v_sub_f32 v48,v48,v119\n"
"v_sub_f32 v49,v49,v119\n"
"v_sub_f32 v50,v50,v119\n"
"v_sub_f32 v51,v51,v119\n"
"v_sub_f32 v52,v52,v119\n"
"v_sub_f32 v53,v53,v119\n"
"v_sub_f32 v54,v54,v119\n"
"v_sub_f32 v55,v55,v119\n"
"v_sub_f32 v104,v104,v119\n"
"v_sub_f32 v105,v105,v119\n"
"v_sub_f32 v106,v106,v119\n"
"v_sub_f32 v107,v107,v119\n"
"s_cbranch_scc1 .Lfk_rare1_2\n"
"v_exp_f32 v119,-v119\n"
"s_nop 0\n"
"v_mul_f32 v192,v192,v119\n"
"v_mul_f32 v193,v193,v119\n"
"v_mul_f32 v194,v194,v119\n"
"v_mul_f32 v195,v195,v119\n"
"v_mul_f32 v196,v196,v119\n"
"v_mul_f32 v197,v197,v119\n"
"v_mul_f32 v198,v198,v119\n"
"v_mul_f32 v199,v199,v119\n"
"v_mul_f32 v200,v200,v119\n"
"v_mul_f32 v201,v201,v119\n"
"v_mul_f32 v202,v202,v119\n"
"v_mul_f32 v203,v203,v119\n"
"v_mul_f32 v204,v204,v119\n"
"v_mul_f32 v205,v205,v119\n"
"v_mul_f32 v206,v206,v119\n"
"v_mul_f32 v207,v207,v119\n"
"v_mul_f32 v208,v208,v119\n"
"v_mul_f32 v209,v209,v119\n"
"v_mul_f32 v210,v210,v119\n"
"v_mul_f32 v211,v211,v119\n"
"v_mul_f32 v212,v212,v119\n"
"v_mul_f32 v213,v213,v119\n"
"v_mul_f32 v214,v214,v119\n"
"v_mul_f32 v215,v215,v119\n"
"v_mul_f32 v216,v216,v119\n"
"v_mul_f32 v217,v217,v119\n"
"v_mul_f32 v218,v218,v119\n"
"v_mul_f32 v219,v219,v119\n"
"v_mul_f32 v220,v220,v119\n"
"v_mul_f32 v221,v221,v119\n"
"v_mul_f32 v222,v222,v119\n"
"v_mul_f32 v223,v223,v119\n"
"v_accvgpr_read_b32 v120,a136\n"
"v_mul_f32 v120,v120,v119\n"
"v_accvgpr_write_b32 a136,v120\n"
"v_lshlrev_b32 v120,16,v72\n"
"v_and_b32 v121,0xffff0000,v72\n"
"v_mul_f32 v120,v120,v119\n"
"v_mul_f32 v121,v121,v119\n"
"v_cvt_pk_bf16_f32 v72,v120,v121\n"
"v_accvgpr_read_b32 v120,a137\n"
"v_mul_f32 v120,v120,v119\n"
"v_accvgpr_write_b32 a137,v120\n"
"v_lshlrev_b32 v120,16,v73\n"
"v_and_b32 v121,0xffff0000,v73\n"
"v_mul_f32 v120,v120,v119\n"
"v_mul_f32 v121,v121,v119\n"
"v_cvt_pk_bf16_f32 v73,v120,v121\n"
"v_accvgpr_read_b32 v120,a138\n"
"v_mul_f32 v120,v120,v119\n"
"v_accvgpr_write_b32 a138,v120\n"
"v_lshlrev_b32 v120,16,v74\n"
"v_and_b32 v121,0xffff0000,v74\n"
"v_mul_f32 v120,v120,v119\n"
"v_mul_f32 v121,v121,v119\n"
"v_cvt_pk_bf16_f32 v74,v120,v121\n"
"v_accvgpr_read_b32 v120,a139\n"
"v_mul_f32 v120,v120,v119\n"
"v_accvgpr_write_b32 a139,v120\n"
"v_lshlrev_b32 v120,16,v75\n"
"v_and_b32 v121,0xffff0000,v75\n"
"v_mul_f32 v120,v120,v119\n"
"v_mul_f32 v121,v121,v119\n"
"v_cvt_pk_bf16_f32 v75,v120,v121\n"
".Lfk_rare1_2:\n"
"s_bitcmp1_b32 s91,3\n"
"s_cbranch_scc0 .Lfk_rare1_3\n"
"v_max3_f32 v119,v56,v57,v58\n"
"v_max3_f32 v120,v59,v60,v61\n"
"v_max3_f32 v119,v119,v62,v63\n"
"v_max_f32 v119,v119,v120\n"
"v_mov_b32 v120,v119\n"
"s_nop 1\n"
"v_permlane16_swap_b32 v120,v119\n"
"v_max_f32 v119,v119,v120\n"
"v_mov_b32 v120,v119\n"
"s_nop 1\n"
"v_permlane32_swap_b32 v120,v119\n"
"v_max_f32 v119,v119,v120\n"
"s_bitcmp1_b32 s90,3\n"
"s_cselect_b32 s82,0xff800000,0\n"
"v_add_f32 v119,0x41980000,v119\n"
"v_max_f32 v119,s82,v119\n"
"v_cmp_lt_f32 vcc,s17,|v119|\n"
"s_nop 0\n"
"s_cbranch_vccz .Lfk_rare1_3\n"
"v_ceil_f32 v119,v119\n"
"v_sub_f32 v56,v56,v119\n"
"v_sub_f32 v57,v57,v119\n"
"v_sub_f32 v58,v58,v119\n"
"v_sub_f32 v59,v59,v119\n"
"v_sub_f32 v60,v60,v119\n"
"v_sub_f32 v61,v61,v119\n"
"v_sub_f32 v62,v62,v119\n"
"v_sub_f32 v63,v63,v119\n"
"v_sub_f32 v108,v108,v119\n"
"v_sub_f32 v109,v109,v119\n"
"v_sub_f32 v110,v110,v119\n"
"v_sub_f32 v111,v111,v119\n"
"s_cbranch_scc1 .Lfk_rare1_3\n"
"v_exp_f32 v119,-v119\n"
"s_nop 0\n"
"v_mul_f32 v224,v224,v119\n"
"v_mul_f32 v225,v225,v119\n"
"v_mul_f32 v226,v226,v119\n"
"v_mul_f32 v227,v227,v119\n"
"v_mul_f32 v228,v228,v119\n"
"v_mul_f32 v229,v229,v119\n"
"v_mul_f32 v230,v230,v119\n"
"v_mul_f32 v231,v231,v119\n"
"v_mul_f32 v232,v232,v119\n"
"v_mul_f32 v233,v233,v119\n"
"v_mul_f32 v234,v234,v119\n"
"v_mul_f32 v235,v235,v119\n"
"v_mul_f32 v236,v236,v119\n"
"v_mul_f32 v237,v237,v119\n"
"v_mul_f32 v238,v238,v119\n"
"v_mul_f32 v239,v239,v119\n"
"v_mul_f32 v240,v240,v119\n"
"v_mul_f32 v241,v241,v119\n"
"v_mul_f32 v242,v242,v119\n"
"v_mul_f32 v243,v243,v119\n"
"v_mul_f32 v244,v244,v119\n"
"v_mul_f32 v245,v245,v119\n"
"v_mul_f32 v246,v246,v119\n"
"v_mul_f32 v247,v247,v119\n"
"v_mul_f32 v248,v248,v119\n"
"v_mul_f32 v249,v249,v119\n"
"v_mul_f32 v250,v250,v119\n"
"v_mul_f32 v251,v251,v119\n"
"v_mul_f32 v252,v252,v119\n"
"v_mul_f32 v253,v253,v119\n"
"v_mul_f32 v254,v254,v119\n"
"v_mul_f32 v255,v255,v119\n"
"v_accvgpr_read_b32 v120,a140\n"
"v_mul_f32 v120,v120,v119\n"
"v_accvgpr_write_b32 a140,v120\n"
"v_lshlrev_b32 v120,16,v76\n"
"v_and_b32 v121,0xffff0000,v76\n"
"v_mul_f32 v120,v120,v119\n"
"v_mul_f32 v121,v121,v119\n"
"v_cvt_pk_bf16_f32 v76,v120,v121\n"
"v_accvgpr_read_b32 v120,a141\n"
"v_mul_f32 v120,v120,v119\n"
"v_accvgpr_write_b32 a141,v120\n"
"v_lshlrev_b32 v120,16,v77\n"
"v_and_b32 v121,0xffff0000,v77\n"
"v_mul_f32 v120,v120,v119\n"
"v_mul_f32 v121,v121,v119\n"
"v_cvt_pk_bf16_f32 v77,v120,v121\n"
"v_accvgpr_read_b32 v120,a142\n"
"v_mul_f32 v120,v120,v119\n"
"v_accvgpr_write_b32 a142,v120\n"
"v_lshlrev_b32 v120,16,v78\n"
"v_and_b32 v121,0xffff0000,v78\n"
"v_mul_f32 v120,v120,v119\n"
"v_mul_f32 v121,v121,v119\n"
"v_cvt_pk_bf16_f32 v78,v120,v121\n"
"v_accvgpr_read_b32 v120,a143\n"
"v_mul_f32 v120,v120,v119\n"
"v_accvgpr_write_b32 a143,v120\n"
"v_lshlrev_b32 v120,16,v79\n"
"v_and_b32 v121,0xffff0000,v79\n"
"v_mul_f32 v120,v120,v119\n"
"v_mul_f32 v121,v121,v119\n"
"v_cvt_pk_bf16_f32 v79,v120,v121\n"
".Lfk_rare1_3:\n"
"s_mov_b32 s90,0\n"
"s_nop 1\n"
"s_setpc_b64 s[94:95]\n"
".Lfk_rare3:\n"
"s_nop 7\n"
"v_max3_f32 v119,v32,v33,v34\n"
"v_max3_f32 v120,v35,v36,v37\n"
"v_max3_f32 v119,v119,v38,v39\n"
"v_max_f32 v119,v119,v120\n"
"v_mov_b32 v120,v119\n"
"s_nop 1\n"
"v_permlane16_swap_b32 v120,v119\n"
"v_max_f32 v119,v119,v120\n"
"v_mov_b32 v120,v119\n"
"s_nop 1\n"
"v_permlane32_swap_b32 v120,v119\n"
"v_max_f32 v119,v119,v120\n"
"s_bitcmp1_b32 s90,0\n"
"s_cselect_b32 s82,0xff800000,0\n"
"v_add_f32 v119,0x41980000,v119\n"
"v_max_f32 v119,s82,v119\n"
"v_cmp_lt_f32 vcc,s17,|v119|\n"
"s_nop 0\n"
"s_cbranch_vccz .Lfk_rare3_0\n"
"v_ceil_f32 v119,v119\n"
"v_sub_f32 v32,v32,v119\n"
"v_sub_f32 v33,v33,v119\n"
"v_sub_f32 v34,v34,v119\n"
"v_sub_f32 v35,v35,v119\n"
"v_sub_f32 v36,v36,v119\n"
"v_sub_f32 v37,v37,v119\n"
"v_sub_f32 v38,v38,v119\n"
"v_sub_f32 v39,v39,v119\n"
"v_sub_f32 v96,v96,v119\n"
"v_sub_f32 v97,v97,v119\n"
"v_sub_f32 v98,v98,v119\n"
"v_sub_f32 v99,v99,v119\n"
"v_sub_f32 v0,v0,v119\n"
"v_sub_f32 v1,v1,v119\n"
"v_sub_f32 v2,v2,v119\n"
"v_sub_f32 v3,v3,v119\n"
"v_sub_f32 v4,v4,v119\n"
"v_sub_f32 v5,v5,v119\n"
"v_sub_f32 v6,v6,v119\n"
"v_sub_f32 v7,v7,v119\n"
"v_exp_f32 v32,v32\n"
"v_exp_f32 v33,v33\n"
"v_exp_f32 v34,v34\n"
"v_exp_f32 v35,v35\n"
"v_exp_f32 v36,v36\n"
"v_exp_f32 v37,v37\n"
"v_exp_f32 v38,v38\n"
"v_exp_f32 v39,v39\n"
"v_cvt_pk_bf16_f32 v80,v32,v33\n"
"v_cvt_pk_bf16_f32 v81,v34,v35\n"
"v_cvt_pk_bf16_f32 v82,v36,v37\n"
"v_cvt_pk_bf16_f32 v83,v38,v39\n"
"s_cbranch_scc1 .Lfk_rare3_0\n"
"v_exp_f32 v119,-v119\n"
"s_nop 0\n"
"v_mul_f32 v128,v128,v119\n"
"v_mul_f32 v129,v129,v119\n"
"v_mul_f32 v130,v130,v119\n"
"v_mul_f32 v131,v131,v119\n"
"v_mul_f32 v132,v132,v119\n"
"v_mul_f32 v133,v133,v119\n"
"v_mul_f32 v134,v134,v119\n"
"v_mul_f32 v135,v135,v119\n"
"v_mul_f32 v136,v136,v119\n"
"v_mul_f32 v137,v137,v119\n"
"v_mul_f32 v138,v138,v119\n"
"v_mul_f32 v139,v139,v119\n"
"v_mul_f32 v140,v140,v119\n"
"v_mul_f32 v141,v141,v119\n"
"v_mul_f32 v142,v142,v119\n"
"v_mul_f32 v143,v143,v119\n"
"v_mul_f32 v144,v144,v119\n"
"v_mul_f32 v145,v145,v119\n"
"v_mul_f32 v146,v146,v119\n"
"v_mul_f32 v147,v147,v119\n"
"v_mul_f32 v148,v148,v119\n"
"v_mul_f32 v149,v149,v119\n"
"v_mul_f32 v150,v150,v119\n"
"v_mul_f32 v151,v151,v119\n"
"v_mul_f32 v152,v152,v119\n"
"v_mul_f32 v153,v153,v119\n"
"v_mul_f32 v154,v154,v119\n"
"v_mul_f32 v155,v155,v119\n"
"v_mul_f32 v156,v156,v119\n"
"v_mul_f32 v157,v157,v119\n"
"v_mul_f32 v158,v158,v119\n"
"v_mul_f32 v159,v159,v119\n"
"v_accvgpr_read_b32 v120,a128\n"
"v_mul_f32 v120,v120,v119\n"
"v_accvgpr_write_b32 a128,v120\n"
"v_accvgpr_read_b32 v120,a129\n"
"v_mul_f32 v120,v120,v119\n"
"v_accvgpr_write_b32 a129,v120\n"
"v_accvgpr_read_b32 v120,a130\n"
"v_mul_f32 v120,v120,v119\n"
"v_accvgpr_write_b32 a130,v120\n"
"v_accvgpr_read_b32 v120,a131\n"
"v_mul_f32 v120,v120,v119\n"
"v_accvgpr_write_b32 a131,v120\n"
".Lfk_rare3_0:\n"
"v_max3_f32 v119,v40,v41,v42\n"
"v_max3_f32 v120,v43,v44,v45\n"
"v_max3_f32 v119,v119,v46,v47\n"
"v_max_f32 v119,v119,v120\n"
"v_mov_b32 v120,v119\n"
"s_nop 1\n"
"v_permlane16_swap_b32 v120,v119\n"
"v_max_f32 v119,v119,v120\n"
"v_mov_b32 v120,v119\n"
"s_nop 1\n"
"v_permlane32_swap_b32 v120,v119\n"
"v_max_f32 v119,v119,v120\n"
"s_bitcmp1_b32 s90,1\n"
"s_cselect_b32 s82,0xff800000,0\n"
"v_add_f32 v119,0x41980000,v119\n"
"v_max_f32 v119,s82,v119\n"
"v_cmp_lt_f32 vcc,s17,|v119|\n"
"s_nop 0\n"
"s_cbranch_vccz .Lfk_rare3_1\n"
"v_ceil_f32 v119,v119\n"
"v_sub_f32 v40,v40,v119\n"
"v_sub_f32 v41,v41,v119\n"
"v_sub_f32 v42,v42,v119\n"
"v_sub_f32 v43,v43,v119\n"
"v_sub_f32 v44,v44,v119\n"
"v_sub_f32 v45,v45,v119\n"
"v_sub_f32 v46,v46,v119\n"
"v_sub_f32 v47,v47,v119\n"
"v_sub_f32 v100,v100,v119\n"
"v_sub_f32 v101,v101,v119\n"
"v_sub_f32 v102,v102,v119\n"
"v_sub_f32 v103,v103,v119\n"
"v_sub_f32 v8,v8,v119\n"
"v_sub_f32 v9,v9,v119\n"
"v_sub_f32 v10,v10,v119\n"
"v_sub_f32 v11,v11,v119\n"
"v_sub_f32 v12,v12,v119\n"
"v_sub_f32 v13,v13,v119\n"
"v_sub_f32 v14,v14,v119\n"
"v_sub_f32 v15,v15,v119\n"
"v_exp_f32 v40,v40\n"
"v_exp_f32 v41,v41\n"
"v_exp_f32 v42,v42\n"
"v_exp_f32 v43,v43\n"
"v_exp_f32 v44,v44\n"
"v_exp_f32 v45,v45\n"
"v_exp_f32 v46,v46\n"
"v_exp_f32 v47,v47\n"
"v_cvt_pk_bf16_f32 v84,v40,v41\n"
"v_cvt_pk_bf16_f32 v85,v42,v43\n"
"v_cvt_pk_bf16_f32 v86,v44,v45\n"
"v_cvt_pk_bf16_f32 v87,v46,v47\n"
"s_cbranch_scc1 .Lfk_rare3_1\n"
"v_exp_f32 v119,-v119\n"
"s_nop 0\n"
"v_mul_f32 v160,v160,v119\n"
"v_mul_f32 v161,v161,v119\n"
"v_mul_f32 v162,v162,v119\n"
"v_mul_f32 v163,v163,v119\n"
"v_mul_f32 v164,v164,v119\n"
"v_mul_f32 v165,v165,v119\n"
"v_mul_f32 v166,v166,v119\n"
"v_mul_f32 v167,v167,v119\n"
"v_mul_f32 v168,v168,v119\n"
"v_mul_f32 v169,v169,v119\n"
"v_mul_f32 v170,v170,v119\n"
"v_mul_f32 v171,v171,v119\n"
"v_mul_f32 v172,v172,v119\n"
"v_mul_f32 v173,v173,v119\n"
"v_mul_f32 v174,v174,v119\n"
"v_mul_f32 v175,v175,v119\n"
"v_mul_f32 v176,v176,v119\n"
"v_mul_f32 v177,v177,v119\n"
"v_mul_f32 v178,v178,v119\n"
"v_mul_f32 v179,v179,v119\n"
"v_mul_f32 v180,v180,v119\n"
"v_mul_f32 v181,v181,v119\n"
"v_mul_f32 v182,v182,v119\n"
"v_mul_f32 v183,v183,v119\n"
"v_mul_f32 v184,v184,v119\n"
"v_mul_f32 v185,v185,v119\n"
"v_mul_f32 v186,v186,v119\n"
"v_mul_f32 v187,v187,v119\n"
"v_mul_f32 v188,v188,v119\n"
"v_mul_f32 v189,v189,v119\n"
"v_mul_f32 v190,v190,v119\n"
"v_mul_f32 v191,v191,v119\n"
"v_accvgpr_read_b32 v120,a132\n"
"v_mul_f32 v120,v120,v119\n"
"v_accvgpr_write_b32 a132,v120\n"
"v_accvgpr_read_b32 v120,a133\n"
"v_mul_f32 v120,v120,v119\n"
"v_accvgpr_write_b32 a133,v120\n"
"v_accvgpr_read_b32 v120,a134\n"
"v_mul_f32 v120,v120,v119\n"
"v_accvgpr_write_b32 a134,v120\n"
"v_accvgpr_read_b32 v120,a135\n"
"v_mul_f32 v120,v120,v119\n"
"v_accvgpr_write_b32 a135,v120\n"
".Lfk_rare3_1:\n"
"v_max3_f32 v119,v48,v49,v50\n"
"v_max3_f32 v120,v51,v52,v53\n"
"v_max3_f32 v119,v119,v54,v55\n"
"v_max_f32 v119,v119,v120\n"
"v_mov_b32 v120,v119\n"
"s_nop 1\n"
"v_permlane16_swap_b32 v120,v119\n"
"v_max_f32 v119,v119,v120\n"
"v_mov_b32 v120,v119\n"
"s_nop 1\n"
"v_permlane32_swap_b32 v120,v119\n"
"v_max_f32 v119,v119,v120\n"
"s_bitcmp1_b32 s90,2\n"
"s_cselect_b32 s82,0xff800000,0\n"
"v_add_f32 v119,0x41980000,v119\n"
"v_max_f32 v119,s82,v119\n"
"v_cmp_lt_f32 vcc,s17,|v119|\n"
"s_nop 0\n"
"s_cbranch_vccz .Lfk_rare3_2\n"
"v_ceil_f32 v119,v119\n"
"v_sub_f32 v48,v48,v119\n"
"v_sub_f32 v49,v49,v119\n"
"v_sub_f32 v50,v50,v119\n"
"v_sub_f32 v51,v51,v119\n"
"v_sub_f32 v52,v52,v119\n"
"v_sub_f32 v53,v53,v119\n"
"v_sub_f32 v54,v54,v119\n"
"v_sub_f32 v55,v55,v119\n"
"v_sub_f32 v104,v104,v119\n"
"v_sub_f32 v105,v105,v119\n"
"v_sub_f32 v106,v106,v119\n"
"v_sub_f32 v107,v107,v119\n"
"v_sub_f32 v16,v16,v119\n"
"v_sub_f32 v17,v17,v119\n"
"v_sub_f32 v18,v18,v119\n"
"v_sub_f32 v19,v19,v119\n"
"v_sub_f32 v20,v20,v119\n"
"v_sub_f32 v21,v21,v119\n"
"v_sub_f32 v22,v22,v119\n"
"v_sub_f32 v23,v23,v119\n"
"v_exp_f32 v48,v48\n"
"v_exp_f32 v49,v49\n"
"v_exp_f32 v50,v50\n"
"v_exp_f32 v51,v51\n"
"v_exp_f32 v52,v52\n"
"v_exp_f32 v53,v53\n"
"v_exp_f32 v54,v54\n"
"v_exp_f32 v55,v55\n"
"v_cvt_pk_bf16_f32 v88,v48,v49\n"
"v_cvt_pk_bf16_f32 v89,v50,v51\n"
"v_cvt_pk_bf16_f32 v90,v52,v53\n"
"v_cvt_pk_bf16_f32 v91,v54,v55\n"
"s_cbranch_scc1 .Lfk_rare3_2\n"
"v_exp_f32 v119,-v119\n"
"s_nop 0\n"
"v_mul_f32 v192,v192,v119\n"
"v_mul_f32 v193,v193,v119\n"
"v_mul_f32 v194,v194,v119\n"
"v_mul_f32 v195,v195,v119\n"
"v_mul_f32 v196,v196,v119\n"
"v_mul_f32 v197,v197,v119\n"
"v_mul_f32 v198,v198,v119\n"
"v_mul_f32 v199,v199,v119\n"
"v_mul_f32 v200,v200,v119\n"
"v_mul_f32 v201,v201,v119\n"
"v_mul_f32 v202,v202,v119\n"
"v_mul_f32 v203,v203,v119\n"
"v_mul_f32 v204,v204,v119\n"
"v_mul_f32 v205,v205,v119\n"
"v_mul_f32 v206,v206,v119\n"
"v_mul_f32 v207,v207,v119\n"
"v_mul_f32 v208,v208,v119\n"
"v_mul_f32 v209,v209,v119\n"
"v_mul_f32 v210,v210,v119\n"
"v_mul_f32 v211,v211,v119\n"
"v_mul_f32 v212,v212,v119\n"
"v_mul_f32 v213,v213,v119\n"
"v_mul_f32 v214,v214,v119\n"
"v_mul_f32 v215,v215,v119\n"
"v_mul_f32 v216,v216,v119\n"
"v_mul_f32 v217,v217,v119\n"
"v_mul_f32 v218,v218,v119\n"
"v_mul_f32 v219,v219,v119\n"
"v_mul_f32 v220,v220,v119\n"
"v_mul_f32 v221,v221,v119\n"
"v_mul_f32 v222,v222,v119\n"
"v_mul_f32 v223,v223,v119\n"
"v_accvgpr_read_b32 v120,a136\n"
"v_mul_f32 v120,v120,v119\n"
"v_accvgpr_write_b32 a136,v120\n"
"v_accvgpr_read_b32 v120,a137\n"
"v_mul_f32 v120,v120,v119\n"
"v_accvgpr_write_b32 a137,v120\n"
"v_accvgpr_read_b32 v120,a138\n"
"v_mul_f32 v120,v120,v119\n"
"v_accvgpr_write_b32 a138,v120\n"
"v_accvgpr_read_b32 v120,a139\n"
"v_mul_f32 v120,v120,v119\n"
"v_accvgpr_write_b32 a139,v120\n"
".Lfk_rare3_2:\n"
"v_max3_f32 v119,v56,v57,v58\n"
"v_max3_f32 v120,v59,v60,v61\n"
"v_max3_f32 v119,v119,v62,v63\n"
"v_max_f32 v119,v119,v120\n"
"v_mov_b32 v120,v119\n"
"s_nop 1\n"
"v_permlane16_swap_b32 v120,v119\n"
"v_max_f32 v119,v119,v120\n"
"v_mov_b32 v120,v119\n"
"s_nop 1\n"
"v_permlane32_swap_b32 v120,v119\n"
"v_max_f32 v119,v119,v120\n"
"s_bitcmp1_b32 s90,3\n"
"s_cselect_b32 s82,0xff800000,0\n"
"v_add_f32 v119,0x41980000,v119\n"
"v_max_f32 v119,s82,v119\n"
"v_cmp_lt_f32 vcc,s17,|v119|\n"
"s_nop 0\n"
"s_cbranch_vccz .Lfk_rare3_3\n"
"v_ceil_f32 v119,v119\n"
"v_sub_f32 v56,v56,v119\n"
"v_sub_f32 v57,v57,v119\n"
"v_sub_f32 v58,v58,v119\n"
"v_sub_f32 v59,v59,v119\n"
"v_sub_f32 v60,v60,v119\n"
"v_sub_f32 v61,v61,v119\n"
"v_sub_f32 v62,v62,v119\n"
"v_sub_f32 v63,v63,v119\n"
"v_sub_f32 v108,v108,v119\n"
"v_sub_f32 v109,v109,v119\n"
"v_sub_f32 v110,v110,v119\n"
"v_sub_f32 v111,v111,v119\n"
"v_sub_f32 v24,v24,v119\n"
"v_sub_f32 v25,v25,v119\n"
"v_sub_f32 v26,v26,v119\n"
"v_sub_f32 v27,v27,v119\n"
"v_sub_f32 v28,v28,v119\n"
"v_sub_f32 v29,v29,v119\n"
"v_sub_f32 v30,v30,v119\n"
"v_sub_f32 v31,v31,v119\n"
"v_exp_f32 v56,v56\n"
"v_exp_f32 v57,v57\n"
"v_exp_f32 v58,v58\n"
"v_exp_f32 v59,v59\n"
"v_exp_f32 v60,v60\n"
"v_exp_f32 v61,v61\n"
"v_exp_f32 v62,v62\n"
"v_exp_f32 v63,v63\n"
"v_cvt_pk_bf16_f32 v92,v56,v57\n"
"v_cvt_pk_bf16_f32 v93,v58,v59\n"
"v_cvt_pk_bf16_f32 v94,v60,v61\n"
"v_cvt_pk_bf16_f32 v95,v62,v63\n"
"s_cbranch_scc1 .Lfk_rare3_3\n"
"v_exp_f32 v119,-v119\n"
"s_nop 0\n"
"v_mul_f32 v224,v224,v119\n"
"v_mul_f32 v225,v225,v119\n"
"v_mul_f32 v226,v226,v119\n"
"v_mul_f32 v227,v227,v119\n"
"v_mul_f32 v228,v228,v119\n"
"v_mul_f32 v229,v229,v119\n"
"v_mul_f32 v230,v230,v119\n"
"v_mul_f32 v231,v231,v119\n"
"v_mul_f32 v232,v232,v119\n"
"v_mul_f32 v233,v233,v119\n"
"v_mul_f32 v234,v234,v119\n"
"v_mul_f32 v235,v235,v119\n"
"v_mul_f32 v236,v236,v119\n"
"v_mul_f32 v237,v237,v119\n"
"v_mul_f32 v238,v238,v119\n"
"v_mul_f32 v239,v239,v119\n"
"v_mul_f32 v240,v240,v119\n"
"v_mul_f32 v241,v241,v119\n"
"v_mul_f32 v242,v242,v119\n"
"v_mul_f32 v243,v243,v119\n"
"v_mul_f32 v244,v244,v119\n"
"v_mul_f32 v245,v245,v119\n"
"v_mul_f32 v246,v246,v119\n"
"v_mul_f32 v247,v247,v119\n"
"v_mul_f32 v248,v248,v119\n"
"v_mul_f32 v249,v249,v119\n"
"v_mul_f32 v250,v250,v119\n"
"v_mul_f32 v251,v251,v119\n"
"v_mul_f32 v252,v252,v119\n"
"v_mul_f32 v253,v253,v119\n"
"v_mul_f32 v254,v254,v119\n"
"v_mul_f32 v255,v255,v119\n"
"v_accvgpr_read_b32 v120,a140\n"
"v_mul_f32 v120,v120,v119\n"
"v_accvgpr_write_b32 a140,v120\n"
"v_accvgpr_read_b32 v120,a141\n"
"v_mul_f32 v120,v120,v119\n"
"v_accvgpr_write_b32 a141,v120\n"
"v_accvgpr_read_b32 v120,a142\n"
"v_mul_f32 v120,v120,v119\n"
"v_accvgpr_write_b32 a142,v120\n"
"v_accvgpr_read_b32 v120,a143\n"
"v_mul_f32 v120,v120,v119\n"
"v_accvgpr_write_b32 a143,v120\n"
".Lfk_rare3_3:\n"
"s_mov_b32 s90,0\n"
"s_nop 1\n"
"s_setpc_b64 s[94:95]\n"
".Lfk_mslow1:\n"
"s_nop 7\n"
"v_min_f32 v119,v102,v108\n"
"v_sub_f32 v120,v119,v108\n"
"v_sub_f32 v119,v119,v102\n"
"v_exp_f32 v120,v120\n"
"v_exp_f32 v119,v119\n"
"v_mul_f32 v224,v224,v120\n"
"v_mul_f32 v225,v225,v120\n"
"v_mul_f32 v226,v226,v120\n"
"v_mul_f32 v227,v227,v120\n"
"v_mul_f32 v228,v228,v120\n"
"v_mul_f32 v229,v229,v120\n"
"v_mul_f32 v230,v230,v120\n"
"v_mul_f32 v231,v231,v120\n"
"v_mul_f32 v232,v232,v120\n"
"v_mul_f32 v233,v233,v120\n"
"v_mul_f32 v234,v234,v120\n"
"v_mul_f32 v235,v235,v120\n"
"v_mul_f32 v236,v236,v120\n"
"v_mul_f32 v237,v237,v120\n"
"v_mul_f32 v238,v238,v120\n"
"v_mul_f32 v239,v239,v120\n"
"v_mul_f32 v240,v240,v120\n"
"v_mul_f32 v241,v241,v120\n"
"v_mul_f32 v242,v242,v120\n"
"v_mul_f32 v243,v243,v120\n"
"v_mul_f32 v244,v244,v120\n"
"v_mul_f32 v245,v245,v120\n"
"v_mul_f32 v246,v246,v120\n"
"v_mul_f32 v247,v247,v120\n"
"v_mul_f32 v248,v248,v120\n"
"v_mul_f32 v249,v249,v120\n"
"v_mul_f32 v250,v250,v120\n"
"v_mul_f32 v251,v251,v120\n"
"v_mul_f32 v252,v252,v120\n"
"v_mul_f32 v253,v253,v120\n"
"v_mul_f32 v254,v254,v120\n"
"v_mul_f32 v255,v255,v120\n"
"v_accvgpr_read_b32 v121,a140\n"
"v_mul_f32 v121,v121,v120\n"
"v_accvgpr_write_b32 a140,v121\n"
"v_mul_f32 v103,v103,v119\n"
"v_accvgpr_read_b32 v121,a80\n"
"v_lshlrev_b32 v120,16,v121\n"
"v_and_b32 v121,0xffff0000,v121\n"
"v_mul_f32 v120,v120,v119\n"
"v_mul_f32 v121,v121,v119\n"
"v_cvt_pk_bf16_f32 v121,v120,v121\n"
"v_accvgpr_write_b32 a80,v121\n"
"v_accvgpr_read_b32 v121,a81\n"
"v_lshlrev_b32 v120,16,v121\n"
"v_and_b32 v121,0xffff0000,v121\n"
"v_mul_f32 v120,v120,v119\n"
"v_mul_f32 v121,v121,v119\n"
"v_cvt_pk_bf16_f32 v121,v120,v121\n"
"v_accvgpr_write_b32 a81,v121\n"
"v_accvgpr_read_b32 v121,a82\n"
"v_lshlrev_b32 v120,16,v121\n"
"v_and_b32 v121,0xffff0000,v121\n"
"v_mul_f32 v120,v120,v119\n"
"v_mul_f32 v121,v121,v119\n"
"v_cvt_pk_bf16_f32 v121,v120,v121\n"
"v_accvgpr_write_b32 a82,v121\n"
"v_accvgpr_read_b32 v121,a83\n"
"v_lshlrev_b32 v120,16,v121\n"
"v_and_b32 v121,0xffff0000,v121\n"
"v_mul_f32 v120,v120,v119\n"
"v_mul_f32 v121,v121,v119\n"
"v_cvt_pk_bf16_f32 v121,v120,v121\n"
"v_accvgpr_write_b32 a83,v121\n"
"v_accvgpr_read_b32 v121,a84\n"
"v_lshlrev_b32 v120,16,v121\n"
"v_and_b32 v121,0xffff0000,v121\n"
"v_mul_f32 v120,v120,v119\n"
"v_mul_f32 v121,v121,v119\n"
"v_cvt_pk_bf16_f32 v121,v120,v121\n"
"v_accvgpr_write_b32 a84,v121\n"
"v_accvgpr_read_b32 v121,a85\n"
"v_lshlrev_b32 v120,16,v121\n"
"v_and_b32 v121,0xffff0000,v121\n"
"v_mul_f32 v120,v120,v119\n"
"v_mul_f32 v121,v121,v119\n"
"v_cvt_pk_bf16_f32 v121,v120,v121\n"
"v_accvgpr_write_b32 a85,v121\n"
"v_accvgpr_read_b32 v121,a86\n"
"v_lshlrev_b32 v120,16,v121\n"
"v_and_b32 v121,0xffff0000,v121\n"
"v_mul_f32 v120,v120,v119\n"
"v_mul_f32 v121,v121,v119\n"
"v_cvt_pk_bf16_f32 v121,v120,v121\n"
"v_accvgpr_write_b32 a86,v121\n"
"v_accvgpr_read_b32 v121,a87\n"
"v_lshlrev_b32 v120,16,v121\n"
"v_and_b32 v121,0xffff0000,v121\n"
"v_mul_f32 v120,v120,v119\n"
"v_mul_f32 v121,v121,v119\n"
"v_cvt_pk_bf16_f32 v121,v120,v121\n"
"v_accvgpr_write_b32 a87,v121\n"
"v_accvgpr_read_b32 v121,a88\n"
"v_lshlrev_b32 v120,16,v121\n"
"v_and_b32 v121,0xffff0000,v121\n"
"v_mul_f32 v120,v120,v119\n"
"v_mul_f32 v121,v121,v119\n"
"v_cvt_pk_bf16_f32 v121,v120,v121\n"
"v_accvgpr_write_b32 a88,v121\n"
"v_accvgpr_read_b32 v121,a89\n"
"v_lshlrev_b32 v120,16,v121\n"
"v_and_b32 v121,0xffff0000,v121\n"
"v_mul_f32 v120,v120,v119\n"
"v_mul_f32 v121,v121,v119\n"
"v_cvt_pk_bf16_f32 v121,v120,v121\n"
"v_accvgpr_write_b32 a89,v121\n"
"v_accvgpr_read_b32 v121,a90\n"
"v_lshlrev_b32 v120,16,v121\n"
"v_and_b32 v121,0xffff0000,v121\n"
"v_mul_f32 v120,v120,v119\n"
"v_mul_f32 v121,v121,v119\n"
"v_cvt_pk_bf16_f32 v121,v120,v121\n"
"v_accvgpr_write_b32 a90,v121\n"
"v_accvgpr_read_b32 v121,a91\n"
"v_lshlrev_b32 v120,16,v121\n"
"v_and_b32 v121,0xffff0000,v121\n"
"v_mul_f32 v120,v120,v119\n"
"v_mul_f32 v121,v121,v119\n"
"v_cvt_pk_bf16_f32 v121,v120,v121\n"
"v_accvgpr_write_b32 a91,v121\n"
"v_accvgpr_read_b32 v121,a92\n"
"v_lshlrev_b32 v120,16,v121\n"
"v_and_b32 v121,0xffff0000,v121\n"
"v_mul_f32 v120,v120,v119\n"
"v_mul_f32 v121,v121,v119\n"
"v_cvt_pk_bf16_f32 v121,v120,v121\n"
"v_accvgpr_write_b32 a92,v121\n"
"v_accvgpr_read_b32 v121,a93\n"
"v_lshlrev_b32 v120,16,v121\n"
"v_and_b32 v121,0xffff0000,v121\n"
"v_mul_f32 v120,v120,v119\n"
"v_mul_f32 v121,v121,v119\n"
"v_cvt_pk_bf16_f32 v121,v120,v121\n"
"v_accvgpr_write_b32 a93,v121\n"
"v_accvgpr_read_b32 v121,a94\n"
"v_lshlrev_b32 v120,16,v121\n"
"v_and_b32 v121,0xffff0000,v121\n"
"v_mul_f32 v120,v120,v119\n"
"v_mul_f32 v121,v121,v119\n"
"v_cvt_pk_bf16_f32 v121,v120,v121\n"
"v_accvgpr_write_b32 a94,v121\n"
"v_accvgpr_read_b32 v121,a95\n"
"v_lshlrev_b32 v120,16,v121\n"
"v_and_b32 v121,0xffff0000,v121\n"
"v_mul_f32 v120,v120,v119\n"
"v_mul_f32 v121,v121,v119\n"
"v_cvt_pk_bf16_f32 v121,v120,v121\n"
"v_accvgpr_write_b32 a95,v121\n"
"s_nop 1\n"
"s_setpc_b64 s[94:95]\n"
".Lfk_end:\n"
        : "+{v0}"(tid), "+{s2}"(bid)
        : "{s[0:1]}"(__builtin_amdgcn_kernarg_segment_ptr())
        : "v1","v2","v3","v4","v5","v6","v7","v8","v9","v10","v11","v12","v13","v14","v15","v16","v17","v18","v19","v20","v21","v22","v23","v24","v25","v26","v27","v28","v29","v30","v31","v32","v33","v34","v35","v36","v37","v38","v39","v40","v41","v42","v43","v44","v45","v46","v47","v48","v49","v50","v51","v52","v53","v54","v55","v56","v57","v58","v59","v60","v61","v62","v63","v64","v65","v66","v67","v68","v69","v70","v71","v72","v73","v74","v75","v76","v77","v78","v79","v80","v81","v82","v83","v84","v85","v86","v87","v88","v89","v90","v91","v92","v93","v94","v95","v96","v97","v98","v99","v100","v101","v102","v103","v104","v105","v106","v107","v108","v109","v110","v111","v112","v113","v114","v115","v116","v117","v118","v119","v120","v121","v122","v123","v124","v125","v126","v127","v128","v129","v130","v131","v132","v133","v134","v135","v136","v137","v138","v139","v140","v141","v142","v143","v144","v145","v146","v147","v148","v149","v150","v151","v152","v153","v154","v155","v156","v157","v158","v159","v160","v161","v162","v163","v164","v165","v166","v167","v168","v169","v170","v171","v172","v173","v174","v175","v176","v177","v178","v179","v180","v181","v182","v183","v184","v185","v186","v187","v188","v189","v190","v191","v192","v193","v194","v195","v196","v197","v198","v199","v200","v201","v202","v203","v204","v205","v206","v207","v208","v209","v210","v211","v212","v213","v214","v215","v216","v217","v218","v219","v220","v221","v222","v223","v224","v225","v226","v227","v228","v229","v230","v231","v232","v233","v234","v235","v236","v237","v238","v239","v240","v241","v242","v243","v244","v245","v246","v247","v248","v249","v250","v251","v252","v253","v254","v255",
          "a0","a1","a2","a3","a4","a5","a6","a7","a8","a9","a10","a11","a12","a13","a14","a15","a16","a17","a18","a19","a20","a21","a22","a23","a24","a25","a26","a27","a28","a29","a30","a31","a32","a33","a34","a35","a36","a37","a38","a39","a40","a41","a42","a43","a44","a45","a46","a47","a48","a49","a50","a51","a52","a53","a54","a55","a56","a57","a58","a59","a60","a61","a62","a63","a64","a65","a66","a67","a68","a69","a70","a71","a72","a73","a74","a75","a76","a77","a78","a79","a80","a81","a82","a83","a84","a85","a86","a87","a88","a89","a90","a91","a92","a93","a94","a95","a96","a97","a98","a99","a100","a101","a102","a103","a104","a105","a106","a107","a108","a109","a110","a111","a112","a113","a114","a115","a116","a117","a118","a119","a120","a121","a122","a123","a124","a125","a126","a127","a128","a129","a130","a131","a132","a133","a134","a135","a136","a137","a138","a139","a140","a141","a142","a143","a144","a145","a146","a147","a148","a149","a150","a151","a152","a153","a154","a155","a156","a157","a158","a159","a160","a161","a162","a163","a164","a165","a166","a167","a168","a169","a170","a171","a172","a173","a174","a175","a176","a177","a178","a179","a180","a181","a182","a183","a184","a185","a186","a187","a188","a189","a190","a191","a192","a193","a194","a195","a196","a197","a198","a199","a200","a201","a202","a203","a204","a205","a206","a207","a208","a209","a210","a211","a212","a213","a214","a215","a216","a217","a218","a219","a220","a221","a222","a223","a224","a225","a226","a227","a228","a229","a230","a231","a232","a233","a234","a235","a236","a237","a238","a239","a240","a241","a242","a243","a244","a245","a246","a247","a248","a249","a250","a251","a252","a253","a254","a255",
          "s3","s4","s5","s6","s7","s8","s9","s10","s11","s12","s13","s14","s15","s16","s17","s18","s19","s20","s21","s22","s23","s24","s25","s26","s27","s28","s29","s30","s31","s32","s33","s34","s35","s36","s37","s38","s39","s40","s41","s42","s43","s44","s45","s46","s47","s48","s49","s50","s51","s52","s53","s54","s55","s56","s57","s58","s59","s60","s61","s62","s63","s64","s65","s66","s67","s68","s69","s70","s71","s72","s73","s74","s75","s76","s77","s78","s79","s80","s81","s82","s83","s84","s85","s86","s87","s88","s89","s90","s91","s92","s93","s94","s95","s96","s97","s98","s99","s100","s101","vcc","scc","m0","memory");
#endif
}

extern "C" void kernel_launch(void* const* d_in, const int* in_sizes, int n_in, void* d_out, int out_size, void* d_ws, size_t ws_size,
                              hipStream_t stream) {
    (void)hipFuncSetAttribute((const void*)fused_kernel, hipFuncAttributeMaxDynamicSharedMemorySize, 163840);
    fused_kernel<<<256, 256, 163840, stream>>>((const char*)d_in[0], (const char*)d_in[1], (const char*)d_in[2], (const char*)d_in[3], (const char*)d_in[4], (char*)d_ws,
                                               (const char*)d_in[5], (const char*)d_in[8], (const char*)d_in[9], (const char*)d_in[10], (const char*)d_in[7], (char*)d_out);
}
```

```cpp
#include <hip/hip_runtime.h>

extern "C" __global__ __launch_bounds__(256, 1) void fused_kernel(const char* __restrict__ x, const char* __restrict__ ckv, const char* __restrict__ wq,
                                                       const char* __restrict__ wuk, const char* __restrict__ wuv, char* ws,
                                                       const char* __restrict__ pwo, const char* __restrict__ pwg, const char* __restrict__ pwu, const char* __restrict__ pwd,
                                                       const char* __restrict__ pwr, char* pout) {
    unsigned tid = threadIdx.x, bid = blockIdx.x;
#if defined(__HIP_DEVICE_COMPILE__)
    asm volatile(
"s_load_dwordx2 s[16:17],s[0:1],8\n"
"s_load_dword s3,s[0:1],72\n"
"s_and_b32 s82,s2,7\n"
"s_lshl_b32 s82,s82,2\n"
"s_lshr_b32 s82,0x13576420,s82\n"
"s_and_b32 s82,s82,7\n"
"s_andn2_b32 s2,s2,7\n"
"s_or_b32 s2,s2,s82\n"
"s_getpc_b64 s[8:9]\n"
".Lfk_pc:\n"
"s_lshr_b32 s82,s2,3\n"
"s_lshl_b32 s82,s82,12\n"
"v_lshlrev_b32 v1,4,v0\n"
"s_mov_b32 s10,.Lfk_cold-.Lfk_pc\n"
"s_mov_b32 s11,0x20000\n"
"buffer_load_dword v115,v1,s[8:11],s82 offen\n"
"v_readfirstlane_b32 s52,v0\n"
"s_load_dwordx2 s[32:33],s[0:1],0\n"
"s_load_dwordx2 s[24:25],s[0:1],40\n"
"s_load_dwordx2 s[44:45],s[0:1],8\n"
"s_load_dwordx2 s[28:29],s[0:1],16\n"
"s_load_dwordx2 s[40:41],s[0:1],16\n"
"s_load_dwordx2 s[4:5],s[0:1],48\n"
"s_and_b32 s82,s2,8\n"
"s_add_u32 s82,s82,24\n"
"s_load_dwordx2 s[20:21],s[0:1],s82\n"
"s_lshr_b32 s52,s52,6\n"
"s_lshr_b32 s83,s2,7\n"
"s_lshl_b32 s83,s83,3\n"
"s_add_u32 s83,s83,24\n"
"s_load_dwordx2 s[36:37],s[0:1],s83\n"
"s_mov_b32 s100,s2\n"
"s_lshl_b32 s92,s2,2\n"
"s_add_u32 s92,s92,s52\n"
"v_and_b32 v0,63,v0\n"
"v_lshlrev_b32 v112,4,v0\n"
"s_and_b32 s88,s2,7\n"
"s_lshr_b32 s89,s2,3\n"
"s_lshl_b32 s93,s89,2\n"
"s_add_u32 s93,s93,s52\n"
"v_lshrrev_b32 v2,3,v0\n"
"v_xor_b32 v3,v2,v0\n"
"v_and_b32 v3,7,v3\n"
"v_lshlrev_b32 v3,4,v3\n"
"s_lshl_b32 s82,s52,9\n"
"v_add_u32 v2,s82,v2\n"
"v_lshl_add_u32 v106,v2,10,v3\n"
"v_add_u32 v107,7168,v106\n"
"v_add_u32 v108,14336,v106\n"
"v_add_u32 v109,21504,v106\n"
"v_lshrrev_b32 v4,4,v0\n"
"v_and_b32 v5,15,v0\n"
"v_lshlrev_b32 v6,1,v4\n"
"v_xor_b32 v5,v5,v6\n"
"v_lshlrev_b32 v5,4,v5\n"
"v_xor_b32 v6,0x80,v5\n"
"s_and_b32 s82,s52,1\n"
"s_lshl_b32 s82,s82,17\n"
"s_lshr_b32 s83,s52,1\n"
"s_lshl_b32 s83,s83,8\n"
"s_add_u32 s82,s82,s83\n"
"v_lshl_add_u32 v7,v4,13,s82\n"
"v_add_u32 v110,v7,v5\n"
"v_add_u32 v111,31744,v110\n"
"v_add_u32 v113,v7,v6\n"
"v_add_u32 v113,63488,v113\n"
"v_add_u32 v114,31744,v113\n"
"s_lshl_b32 s78,s52,12\n"
"s_lshl_b32 s79,s52,13\n"
"s_lshl_b32 s85,s52,11\n"
"s_lshr_b32 s82,s2,5\n"
"s_lshl_b32 s82,s82,15\n"
"s_lshl_b32 s83,s88,10\n"
"s_bfe_u32 s97,s2,0x10004\n"
"s_lshl_b32 s97,s97,9\n"
"s_add_u32 s83,s83,s97\n"
"v_lshrrev_b32 v60,3,v0\n"
"v_lshlrev_b32 v60,19,v60\n"
"v_and_b32 v61,7,v0\n"
"v_lshl_add_u32 v60,v61,7,v60\n"
"s_and_b32 s12,s93,63\n"
"s_lshl3_add_u32 s12,s12,s88\n"
"s_lshl_b32 s12,s12,10\n"
"s_add_u32 s38,s12,1049600\n"
"s_mov_b32 s39,0x20000\n"
"s_and_b32 s14,s52,1\n"
"s_mul_i32 s13,s14,507904\n"
"v_subrev_u32 v62,s13,v106\n"
"v_subrev_u32 v63,s13,v107\n"
"s_mul_i32 s14,s14,6144\n"
"s_sub_u32 s13,s79,s14\n"
"s_waitcnt lgkmcnt(0)\n"
"buffer_load_dword v115,v60,s[36:39],s12 offen\n"
"s_lshl_b32 s87,s92,11\n"
"s_add_u32 s44,s44,s87\n"
"s_addc_u32 s45,s45,0\n"
"s_mov_b32 s46,2048\n"
"s_mov_b32 s47,0x20000\n"
"v_lshlrev_b32 v1,5,v0\n"
"buffer_load_dword v115,v1,s[44:47],0 offen\n"
"s_add_u32 s16,s16,s82\n"
"s_addc_u32 s17,s17,0\n"
"s_sub_u32 s18,2097152,s82\n"
"s_mov_b32 s19,0x20000\n"
"s_add_u32 s20,s20,s83\n"
"s_addc_u32 s21,s21,0\n"
"s_sub_u32 s22,4194304,s83\n"
"s_mov_b32 s23,0x20000\n"
"s_mov_b32 s77,0\n"
"s_add_u32 m0,s78,147456\n"
"s_nop 0\n"
"buffer_load_dwordx4 v110,s[20:23],s77 offen offset:0 lds\n"
"buffer_load_dwordx4 v111,s[20:23],s77 offen offset:1024 lds\n"
"buffer_load_dwordx4 v113,s[20:23],s77 offen offset:2048 lds\n"
"buffer_load_dwordx4 v114,s[20:23],s77 offen offset:3072 lds\n"
"s_mov_b32 s76,786432\n"
"s_add_u32 m0,s13,12288\n"
"s_nop 0\n"
"buffer_load_dwordx4 v62,s[16:19],s76 offen offset:0 lds\n"
"buffer_load_dwordx4 v63,s[16:19],s76 offen offset:1024 lds\n"
"s_mov_b32 s77,262144\n"
"s_add_u32 m0,s78,131072\n"
"s_nop 0\n"
"buffer_load_dwordx4 v110,s[20:23],s77 offen offset:0 lds\n"
"buffer_load_dwordx4 v111,s[20:23],s77 offen offset:1024 lds\n"
"buffer_load_dwordx4 v113,s[20:23],s77 offen offset:2048 lds\n"
"buffer_load_dwordx4 v114,s[20:23],s77 offen offset:3072 lds\n"
"s_mov_b32 s76,0\n"
"s_add_u32 m0,s13,0\n"
"s_nop 0\n"
"buffer_load_dwordx4 v62,s[16:19],s76 offen offset:0 lds\n"
"buffer_load_dwordx4 v63,s[16:19],s76 offen offset:1024 lds\n"
"s_mov_b32 s76,262144\n"
"s_add_u32 m0,s13,4096\n"
"s_nop 0\n"
"buffer_load_dwordx4 v62,s[16:19],s76 offen offset:0 lds\n"
"buffer_load_dwordx4 v63,s[16:19],s76 offen offset:1024 lds\n"
"s_mov_b32 s76,524288\n"
"s_add_u32 m0,s13,8192\n"
"s_nop 0\n"
"buffer_load_dwordx4 v62,s[16:19],s76 offen offset:0 lds\n"
"buffer_load_dwordx4 v63,s[16:19],s76 offen offset:1024 lds\n"
"s_lshl_b32 s82,s92,12\n"
"s_add_u32 s32,s32,s82\n"
"s_addc_u32 s33,s33,0\n"
"s_mov_b32 s34,4096\n"
"s_mov_b32 s35,0x20000\n"
"s_mov_b32 s26,176160768\n"
"s_mov_b32 s27,0x20000\n"
"s_mov_b32 s75,148897792\n"
"s_lshr_b32 s54,s2,5\n"
"s_lshl_b32 s54,s54,19\n"
"s_and_b32 s82,s92,0x47\n"
"s_bfe_u32 s83,s92,0x10003\n"
"s_lshl_b32 s83,s83,5\n"
"s_or_b32 s82,s82,s83\n"
"s_bfe_u32 s83,s92,0x20004\n"
"s_lshl3_add_u32 s82,s83,s82\n"
"s_lshl_b32 s82,s82,7\n"
"s_add_u32 s54,s54,s82\n"
"v_lshrrev_b32 v1,3,v0\n"
"v_bfe_u32 v2,v0,2,1\n"
"v_and_b32 v3,3,v0\n"
"v_lshlrev_b32 v3,4,v3\n"
"v_lshl_add_u32 v70,v1,6,v3\n"
"v_lshl_add_u32 v70,v2,11,v70\n"
"s_bfe_u32 s82,s54,0x10009\n"
"s_lshl_b32 s82,s82,5\n"
"v_xor_b32 v3,s82,v3\n"
"v_lshl_add_u32 v71,v2,6,v3\n"
"v_lshl_add_u32 v71,v1,14,v71\n"
"buffer_load_dwordx2 v[78:79],off,s[24:27],s75 sc1\n"
"s_lshl_b32 s82,s52,16\n"
"v_lshl_add_u32 v7,v4,13,s82\n"
"s_bitcmp1_b32 s52,0\n"
"s_cselect_b32 s83,0x80,0\n"
"v_xor_b32 v5,s83,v5\n"
"v_add_u32 v72,v7,v5\n"
"v_add_u32 v73,32768,v72\n"
"s_mov_b32 s86,0\n"
"v_and_b32 v1,15,v0\n"
"v_lshrrev_b32 v2,4,v0\n"
"v_lshlrev_b32 v3,6,v1\n"
"v_lshl_add_u32 v3,v2,4,v3\n"
"v_lshrrev_b32 v4,4,v3\n"
"v_and_b32 v4,32,v4\n"
"v_xor_b32 v97,v3,v4\n"
"s_lshr_b32 s82,s52,1\n"
"s_lshl_b32 s82,s82,14\n"
"v_and_b32 v3,7,v1\n"
"v_xor_b32 v3,v3,v2\n"
"v_lshlrev_b32 v3,4,v3\n"
"v_lshl_add_u32 v3,v1,7,v3\n"
"v_add_u32 v80,s82,v3\n"
"v_xor_b32 v81,64,v80\n"
"v_add_u32 v82,65536,v80\n"
"v_add_u32 v83,65536,v81\n"
"v_bfe_u32 v3,v0,2,2\n"
"v_lshl_add_u32 v4,v2,3,v3\n"
"v_lshlrev_b32 v4,8,v4\n"
"v_and_b32 v5,1,v2\n"
"v_lshl_add_u32 v5,v5,2,v3\n"
"v_lshlrev_b32 v5,5,v5\n"
"v_and_b32 v6,3,v0\n"
"v_lshl_add_u32 v4,v6,3,v4\n"
"s_and_b32 s82,s52,1\n"
"s_lshl_b32 s82,s82,13\n"
"s_add_u32 s82,s82,98304\n"
"v_add_u32 v4,s82,v4\n"
"v_xor_b32 v6,0,v5\n"
"v_add_u32 v98,v4,v6\n"
"v_xor_b32 v6,32,v5\n"
"v_add_u32 v99,v4,v6\n"
"v_xor_b32 v6,64,v5\n"
"v_add_u32 v100,v4,v6\n"
"v_xor_b32 v6,96,v5\n"
"v_add_u32 v101,v4,v6\n"
"v_xor_b32 v6,128,v5\n"
"v_add_u32 v102,v4,v6\n"
"v_xor_b32 v6,160,v5\n"
"v_add_u32 v103,v4,v6\n"
"v_xor_b32 v6,192,v5\n"
"v_add_u32 v104,v4,v6\n"
"v_xor_b32 v6,224,v5\n"
"v_add_u32 v105,v4,v6\n"
"s_and_b32 s53,s89,1\n"
"s_lshl_b32 s83,s88,10\n"
"s_and_b32 s82,s93,63\n"
"s_add_u32 s82,s82,128\n"
"s_lshl_b32 s82,s82,13\n"
"s_add_u32 s82,s82,s83\n"
"s_add_u32 s36,s36,s82\n"
"s_addc_u32 s37,s37,0\n"
"s_mov_b32 s38,2622464\n"
"s_mov_b32 s39,0x20000\n"
"s_lshl_b32 s82,s93,13\n"
"s_add_u32 s82,s82,s83\n"
"s_add_u32 s40,s40,s82\n"
"s_addc_u32 s41,s41,0\n"
"s_mov_b32 s42,7341056\n"
"s_mov_b32 s43,0x20000\n"
"s_cmp_lg_u32 s53,0\n"
"s_cbranch_scc1 .Lfk_v\n"
"s_waitcnt vmcnt(11)\n"
"s_barrier\n"
"ds_read_b128 v[152:155],v80 offset:12288\n"
"ds_read_b128 v[156:159],v80 offset:14336\n"
"ds_read_b64_tr_b16 v[160:161],v98 offset:49152\n"
"ds_read_b64_tr_b16 v[162:163],v98 offset:50176\n"
"ds_read_b64_tr_b16 v[164:165],v99 offset:49152\n"
"ds_read_b64_tr_b16 v[166:167],v99 offset:50176\n"
"ds_read_b64_tr_b16 v[168:169],v100 offset:49152\n"
"ds_read_b64_tr_b16 v[170:171],v100 offset:50176\n"
"ds_read_b64_tr_b16 v[172:173],v101 offset:49152\n"
"ds_read_b64_tr_b16 v[174:175],v101 offset:50176\n"
"ds_read_b64_tr_b16 v[176:177],v102 offset:49152\n"
"ds_read_b64_tr_b16 v[178:179],v102 offset:50176\n"
"ds_read_b64_tr_b16 v[180:181],v103 offset:49152\n"
"ds_read_b64_tr_b16 v[182:183],v103 offset:50176\n"
"ds_read_b64_tr_b16 v[184:185],v104 offset:49152\n"
"ds_read_b64_tr_b16 v[186:187],v104 offset:50176\n"
"ds_read_b64_tr_b16 v[188:189],v105 offset:49152\n"
"ds_read_b64_tr_b16 v[190:191],v105 offset:50176\n"
"s_waitcnt lgkmcnt(12)\n"
"v_mfma_f32_16x16x32_bf16 a[192:195],v[160:163],v[152:155],0\n"
"s_and_b32 s91,s89,7\n"
"s_mov_b32 s76,128\n"
"s_add_u32 m0,s79,32768\n"
"v_mfma_f32_16x16x32_bf16 a[224:227],v[160:163],v[156:159],0\n"
"buffer_load_dwordx4 v106,s[16:19],s76 offen offset:0 lds\n"
"v_mfma_f32_16x16x32_bf16 a[228:231],v[164:167],v[156:159],0\n"
"s_lshr_b32 s97,s89,3\n"
"s_lshl2_add_u32 s90,s88,s97\n"
"v_mfma_f32_16x16x32_bf16 a[196:199],v[164:167],v[152:155],0\n"
"buffer_load_dwordx4 v107,s[16:19],s76 offen offset:1024 lds\n"
"s_waitcnt lgkmcnt(8)\n"
"v_mfma_f32_16x16x32_bf16 a[200:203],v[168:171],v[152:155],0\n"
"s_lshl_b32 vcc_lo,s90,8\n"
"s_add_u32 s28,s28,vcc_lo\n"
"s_addc_u32 s29,s29,0\n"
"v_mfma_f32_16x16x32_bf16 a[232:235],v[168:171],v[156:159],0\n"
"buffer_load_dwordx4 v108,s[16:19],s76 offen offset:2048 lds\n"
"v_mfma_f32_16x16x32_bf16 a[236:239],v[172:175],v[156:159],0\n"
"s_sub_u32 s30,8388608,vcc_lo\n"
"s_mov_b32 s31,0x20000\n"
"buffer_load_dwordx4 v109,s[16:19],s76 offen offset:3072 lds\n"
"v_mfma_f32_16x16x32_bf16 a[204:207],v[172:175],v[152:155],0\n"
"s_waitcnt vmcnt(9)\n"
"s_barrier\n"
"s_waitcnt lgkmcnt(4)\n"
"v_mfma_f32_16x16x32_bf16 a[208:211],v[176:179],v[152:155],0\n"
"ds_read_b128 v[128:131],v80 offset:0\n"
"ds_read_b128 v[192:195],v81 offset:0\n"
"v_mfma_f32_16x16x32_bf16 a[240:243],v[176:179],v[156:159],0\n"
"ds_read_b64_tr_b16 v[224:225],v98 offset:32768\n"
"ds_read_b64_tr_b16 v[226:227],v98 offset:33792\n"
"v_mfma_f32_16x16x32_bf16 a[244:247],v[180:183],v[156:159],0\n"
"ds_read_b64_tr_b16 v[228:229],v99 offset:32768\n"
"ds_read_b64_tr_b16 v[230:231],v99 offset:33792\n"
"v_mfma_f32_16x16x32_bf16 a[212:215],v[180:183],v[152:155],0\n"
"ds_read_b64_tr_b16 v[232:233],v100 offset:32768\n"
"ds_read_b64_tr_b16 v[234:235],v100 offset:33792\n"
"s_waitcnt lgkmcnt(8)\n"
"v_mfma_f32_16x16x32_bf16 a[216:219],v[184:187],v[152:155],0\n"
"ds_read_b64_tr_b16 v[236:237],v101 offset:32768\n"
"ds_read_b64_tr_b16 v[238:239],v101 offset:33792\n"
"v_mfma_f32_16x16x32_bf16 a[248:251],v[184:187],v[156:159],0\n"
"ds_read_b64_tr_b16 v[240:241],v102 offset:32768\n"
"ds_read_b64_tr_b16 v[242:243],v102 offset:33792\n"
"v_mfma_f32_16x16x32_bf16 a[252:255],v[188:191],v[156:159],0\n"
"ds_read_b64_tr_b16 v[244:245],v103 offset:32768\n"
"ds_read_b64_tr_b16 v[246:247],v103 offset:33792\n"
"v_mfma_f32_16x16x32_bf16 a[220:223],v[188:191],v[152:155],0\n"
"ds_read_b64_tr_b16 v[248:249],v104 offset:32768\n"
"ds_read_b64_tr_b16 v[250:251],v104 offset:33792\n"
"s_waitcnt lgkmcnt(10)\n"
"v_mfma_f32_16x16x32_bf16 a[0:3],v[160:163],v[128:131],0\n"
"ds_read_b64_tr_b16 v[252:253],v105 offset:32768\n"
"ds_read_b64_tr_b16 v[254:255],v105 offset:33792\n"
"v_mfma_f32_16x16x32_bf16 a[0:3],v[224:227],v[192:195],a[0:3]\n"
"ds_read_b128 v[132:135],v80 offset:2048\n"
"ds_read_b128 v[196:199],v81 offset:2048\n"
"v_mfma_f32_16x16x32_bf16 a[4:7],v[228:231],v[192:195],0\n"
"ds_read_b128 v[216:219],v81 offset:12288\n"
"ds_read_b128 v[220:223],v81 offset:14336\n"
"v_mfma_f32_16x16x32_bf16 a[4:7],v[164:167],v[128:131],a[4:7]\n"
"s_waitcnt lgkmcnt(12)\n"
"s_mov_b32 s76,262272\n"
"s_add_u32 m0,s79,36864\n"
"v_mfma_f32_16x16x32_bf16 a[8:11],v[168:171],v[128:131],0\n"
"buffer_load_dwordx4 v106,s[16:19],s76 offen offset:0 lds\n"
"v_mfma_f32_16x16x32_bf16 a[8:11],v[232:235],v[192:195],a[8:11]\n"
"s_mov_b32 s60,s91\n"
"s_sub_u32 s94,15,s91\n"
"v_mfma_f32_16x16x32_bf16 a[12:15],v[236:239],v[192:195],0\n"
"buffer_load_dwordx4 v107,s[16:19],s76 offen offset:1024 lds\n"
"v_mfma_f32_16x16x32_bf16 a[12:15],v[172:175],v[128:131],a[12:15]\n"
"s_cmp_lt_u32 s52,2\n"
"s_cselect_b32 s95,s94,s91\n"
"s_waitcnt lgkmcnt(8)\n"
"v_mfma_f32_16x16x32_bf16 a[16:19],v[176:179],v[128:131],0\n"
"buffer_load_dwordx4 v108,s[16:19],s76 offen offset:2048 lds\n"
"v_mfma_f32_16x16x32_bf16 a[16:19],v[240:243],v[192:195],a[16:19]\n"
"s_lshr_b32 vcc_lo,s95,1\n"
"s_lshl_b32 s55,vcc_lo,19\n"
"v_mfma_f32_16x16x32_bf16 a[20:23],v[244:247],v[192:195],0\n"
"buffer_load_dwordx4 v109,s[16:19],s76 offen offset:3072 lds\n"
"v_mfma_f32_16x16x32_bf16 a[20:23],v[180:183],v[128:131],a[20:23]\n"
"s_waitcnt lgkmcnt(4)\n"
"s_mov_b32 s77,524288\n"
"s_add_u32 m0,s78,98304\n"
"v_mfma_f32_16x16x32_bf16 a[24:27],v[184:187],v[128:131],0\n"
"buffer_load_dwordx4 v110,s[20:23],s77 offen offset:0 lds\n"
"v_mfma_f32_16x16x32_bf16 a[24:27],v[248:251],v[192:195],a[24:27]\n"
"s_lshl_b32 s58,vcc_lo,10\n"
"s_add_u32 s58,s58,148898816\n"
"v_mfma_f32_16x16x32_bf16 a[28:31],v[252:255],v[192:195],0\n"
"buffer_load_dwordx4 v111,s[20:23],s77 offen offset:1024 lds\n"
"v_mfma_f32_16x16x32_bf16 a[28:31],v[188:191],v[128:131],a[28:31]\n"
"s_and_b32 vcc_lo,s95,1\n"
"s_waitcnt lgkmcnt(2)\n"
"v_mfma_f32_16x16x32_bf16 a[32:35],v[160:163],v[132:135],0\n"
"buffer_load_dwordx4 v113,s[20:23],s77 offen offset:2048 lds\n"
"v_mfma_f32_16x16x32_bf16 a[32:35],v[224:227],v[196:199],a[32:35]\n"
"s_lshl_b32 vcc_lo,vcc_lo,13\n"
"s_and_b32 vcc_hi,s78,4096\n"
"v_mfma_f32_16x16x32_bf16 a[36:39],v[228:231],v[196:199],0\n"
"buffer_load_dwordx4 v114,s[20:23],s77 offen offset:3072 lds\n"
"v_mfma_f32_16x16x32_bf16 a[36:39],v[164:167],v[132:135],a[36:39]\n"
"s_waitcnt vmcnt(13)\n"
"s_barrier\n"
"v_mfma_f32_16x16x32_bf16 a[40:43],v[168:171],v[132:135],0\n"
"ds_read_b128 v[136:139],v80 offset:4096\n"
"ds_read_b128 v[200:203],v81 offset:4096\n"
"v_mfma_f32_16x16x32_bf16 a[40:43],v[232:235],v[196:199],a[40:43]\n"
"ds_read_b128 v[140:143],v80 offset:6144\n"
"ds_read_b128 v[204:207],v81 offset:6144\n"
"v_mfma_f32_16x16x32_bf16 a[44:47],v[236:239],v[196:199],0\n"
"ds_read_b128 v[144:147],v80 offset:8192\n"
"ds_read_b128 v[208:211],v81 offset:8192\n"
"v_mfma_f32_16x16x32_bf16 a[44:47],v[172:175],v[132:135],a[44:47]\n"
"ds_read_b128 v[148:151],v80 offset:10240\n"
"ds_read_b128 v[212:215],v81 offset:10240\n"
"v_mfma_f32_16x16x32_bf16 a[48:51],v[176:179],v[132:135],0\n"
"s_add_u32 s55,s55,vcc_lo\n"
"s_mov_b32 s77,786432\n"
"s_add_u32 m0,s78,114688\n"
"v_mfma_f32_16x16x32_bf16 a[48:51],v[240:243],v[196:199],a[48:51]\n"
"buffer_load_dwordx4 v110,s[20:23],s77 offen offset:0 lds\n"
"v_mfma_f32_16x16x32_bf16 a[52:55],v[244:247],v[196:199],0\n"
"s_add_u32 s55,s55,vcc_hi\n"
"s_and_b32 vcc_lo,s89,2\n"
"s_lshl2_add_u32 vcc_lo,s88,vcc_lo\n"
"v_mfma_f32_16x16x32_bf16 a[52:55],v[180:183],v[132:135],a[52:55]\n"
"buffer_load_dwordx4 v111,s[20:23],s77 offen offset:1024 lds\n"
"v_mfma_f32_16x16x32_bf16 a[56:59],v[184:187],v[132:135],0\n"
"s_and_b32 vcc_hi,s52,1\n"
"buffer_load_dwordx4 v113,s[20:23],s77 offen offset:2048 lds\n"
"v_mfma_f32_16x16x32_bf16 a[56:59],v[248:251],v[196:199],a[56:59]\n"
"s_waitcnt lgkmcnt(0)\n"
"s_barrier\n"
"v_mfma_f32_16x16x32_bf16 a[60:63],v[252:255],v[196:199],0\n"
"s_add_u32 vcc_lo,vcc_lo,vcc_hi\n"
"s_lshr_b32 vcc_hi,s89,2\n"
"v_mfma_f32_16x16x32_bf16 a[60:63],v[188:191],v[132:135],a[60:63]\n"
"s_lshr_b32 s84,s52,1\n"
"s_lshl1_add_u32 vcc_hi,vcc_hi,s84\n"
"v_mfma_f32_16x16x32_bf16 a[64:67],v[160:163],v[136:139],0\n"
"buffer_load_dwordx4 v114,s[20:23],s77 offen offset:3072 lds\n"
"v_mfma_f32_16x16x32_bf16 a[64:67],v[224:227],v[200:203],a[64:67]\n"
"s_mul_i32 s56,vcc_lo,1179648\n"
"s_lshl_b32 s84,s84,19\n"
"v_mfma_f32_16x16x32_bf16 a[68:71],v[228:231],v[200:203],0\n"
"s_add_u32 s56,s56,s84\n"
"s_mov_b32 s76,256\n"
"s_add_u32 m0,s79,65536\n"
"v_mfma_f32_16x16x32_bf16 a[68:71],v[164:167],v[136:139],a[68:71]\n"
"buffer_load_dwordx4 v106,s[16:19],s76 offen offset:0 lds\n"
"v_mfma_f32_16x16x32_bf16 a[72:75],v[168:171],v[136:139],0\n"
"s_lshr_b32 s84,s89,2\n"
"s_lshl_b32 s84,s84,14\n"
"v_mfma_f32_16x16x32_bf16 a[72:75],v[232:235],v[200:203],a[72:75]\n"
"s_add_u32 s56,s56,s84\n"
"s_lshl_b32 s84,s53,13\n"
"v_mfma_f32_16x16x32_bf16 a[76:79],v[236:239],v[200:203],0\n"
"buffer_load_dwordx4 v107,s[16:19],s76 offen offset:1024 lds\n"
"v_mfma_f32_16x16x32_bf16 a[76:79],v[172:175],v[136:139],a[76:79]\n"
"s_add_u32 s56,s56,s84\n"
"s_add_u32 s56,s56,4194304\n"
"v_mfma_f32_16x16x32_bf16 a[80:83],v[176:179],v[136:139],0\n"
"s_lshl4_add_u32 vcc_hi,s53,vcc_hi\n"
"s_lshl_b32 vcc_lo,vcc_lo,8\n"
"v_mfma_f32_16x16x32_bf16 a[80:83],v[240:243],v[200:203],a[80:83]\n"
"buffer_load_dwordx4 v108,s[16:19],s76 offen offset:2048 lds\n"
"v_mfma_f32_16x16x32_bf16 a[84:87],v[244:247],v[200:203],0\n"
"s_lshl3_add_u32 s59,vcc_hi,vcc_lo\n"
"s_add_u32 s59,s59,148914176\n"
"v_mfma_f32_16x16x32_bf16 a[84:87],v[180:183],v[136:139],a[84:87]\n"
"s_lshl_b32 s57,s92,3\n"
"s_add_u32 s57,s57,148898816\n"
"v_mfma_f32_16x16x32_bf16 a[88:91],v[184:187],v[136:139],0\n"
"buffer_load_dwordx4 v109,s[16:19],s76 offen offset:3072 lds\n"
"v_mfma_f32_16x16x32_bf16 a[88:91],v[248:251],v[200:203],a[88:91]\n"
"s_lshl_b32 s63,s90,8\n"
"s_add_u32 s63,s63,148914176\n"
"v_mfma_f32_16x16x32_bf16 a[92:95],v[252:255],v[200:203],0\n"
"s_lshl_b32 s74,s92,11\n"
"s_add_u32 s74,s74,171966464\n"
"v_mfma_f32_16x16x32_bf16 a[92:95],v[188:191],v[136:139],a[92:95]\n"
"s_mul_i32 s96,s90,1179648\n"
"s_add_u32 s96,s96,4194304\n"
"v_mfma_f32_16x16x32_bf16 a[96:99],v[160:163],v[140:143],0\n"
"s_add_u32 s73,s96,s85\n"
"s_mov_b32 s76,262400\n"
"s_add_u32 m0,s79,69632\n"
"v_mfma_f32_16x16x32_bf16 a[96:99],v[224:227],v[204:207],a[96:99]\n"
"buffer_load_dwordx4 v106,s[16:19],s76 offen offset:0 lds\n"
"v_mfma_f32_16x16x32_bf16 a[100:103],v[228:231],v[204:207],0\n"
"s_and_b32 vcc_hi,s78,4096\n"
"s_lshl_b32 vcc_hi,vcc_hi,1\n"
"v_mfma_f32_16x16x32_bf16 a[100:103],v[164:167],v[140:143],a[100:103]\n"
"s_add_u32 vcc_lo,s96,vcc_hi\n"
"s_add_u32 s65,vcc_lo,524288\n"
"v_mfma_f32_16x16x32_bf16 a[104:107],v[168:171],v[140:143],0\n"
"buffer_load_dwordx4 v107,s[16:19],s76 offen offset:1024 lds\n"
"v_mfma_f32_16x16x32_bf16 a[104:107],v[232:235],v[204:207],a[104:107]\n"
"s_lshl_b32 s66,s91,16\n"
"s_sub_u32 s66,vcc_lo,s66\n"
"v_mfma_f32_16x16x32_bf16 a[108:111],v[236:239],v[204:207],0\n"
"s_add_u32 s66,s66,999424\n"
"s_add_u32 s67,vcc_hi,65536\n"
"v_mfma_f32_16x16x32_bf16 a[108:111],v[172:175],v[140:143],a[108:111]\n"
"buffer_load_dwordx4 v108,s[16:19],s76 offen offset:2048 lds\n"
"v_mfma_f32_16x16x32_bf16 a[112:115],v[176:179],v[140:143],0\n"
"s_mul_i32 vcc_lo,vcc_hi,5\n"
"s_sub_u32 vcc_lo,s96,vcc_lo\n"
"v_mfma_f32_16x16x32_bf16 a[112:115],v[240:243],v[204:207],a[112:115]\n"
"s_add_u32 s64,vcc_lo,81920\n"
"s_add_u32 s62,vcc_lo,135168\n"
"s_add_u32 s61,vcc_lo,524288\n"
"v_mfma_f32_16x16x32_bf16 a[116:119],v[244:247],v[204:207],0\n"
"buffer_load_dwordx4 v109,s[16:19],s76 offen offset:3072 lds\n"
"v_mfma_f32_16x16x32_bf16 a[116:119],v[180:183],v[140:143],a[116:119]\n"
"s_cmp_lt_u32 s52,2\n"
"s_cselect_b32 s64,s64,2146435072\n"
"s_cselect_b32 s67,s67,163840\n"
"s_cselect_b32 s65,s65,1\n"
"s_cselect_b32 vcc_lo,4,0\n"
"v_mfma_f32_16x16x32_bf16 a[120:123],v[184:187],v[140:143],0\n"
"s_and_b32 vcc_hi,s52,1\n"
"s_mov_b32 s77,1048576\n"
"s_add_u32 m0,s78,147456\n"
"v_mfma_f32_16x16x32_bf16 a[120:123],v[248:251],v[204:207],a[120:123]\n"
"buffer_load_dwordx4 v110,s[20:23],s77 offen offset:0 lds\n"
"v_mfma_f32_16x16x32_bf16 a[124:127],v[252:255],v[204:207],0\n"
"s_add_u32 vcc_lo,vcc_lo,vcc_hi\n"
"s_lshl_b32 vcc_hi,s90,12\n"
"v_mfma_f32_16x16x32_bf16 a[124:127],v[188:191],v[140:143],a[124:127]\n"
"s_add_u32 vcc_hi,vcc_hi,54525952\n"
"s_lshl3_add_u32 s70,s95,vcc_lo\n"
"v_mfma_f32_16x16x32_bf16 a[128:131],v[160:163],v[144:147],0\n"
"buffer_load_dwordx4 v111,s[20:23],s77 offen offset:1024 lds\n"
"v_mfma_f32_16x16x32_bf16 a[128:131],v[224:227],v[208:211],a[128:131]\n"
"s_lshl_b32 s70,s70,17\n"
"s_add_u32 s70,s70,vcc_hi\n"
"v_mfma_f32_16x16x32_bf16 a[132:135],v[228:231],v[208:211],0\n"
"s_lshl3_add_u32 s68,s94,vcc_lo\n"
"s_lshl_b32 s68,s68,17\n"
"v_mfma_f32_16x16x32_bf16 a[132:135],v[164:167],v[144:147],a[132:135]\n"
"buffer_load_dwordx4 v113,s[20:23],s77 offen offset:2048 lds\n"
"v_mfma_f32_16x16x32_bf16 a[136:139],v[168:171],v[144:147],0\n"
"s_add_u32 s68,s68,vcc_hi\n"
"s_lshr_b32 vcc_lo,s90,3\n"
"v_mfma_f32_16x16x32_bf16 a[136:139],v[232:235],v[208:211],a[136:139]\n"
"s_lshl2_add_u32 vcc_lo,vcc_lo,s52\n"
"s_lshl_b32 vcc_lo,vcc_lo,7\n"
"v_mfma_f32_16x16x32_bf16 a[140:143],v[236:239],v[208:211],0\n"
"buffer_load_dwordx4 v114,s[20:23],s77 offen offset:3072 lds\n"
"v_mfma_f32_16x16x32_bf16 a[140:143],v[172:175],v[144:147],a[140:143]\n"
"s_and_b32 vcc_hi,s90,7\n"
"s_add_u32 vcc_lo,vcc_lo,vcc_hi\n"
"v_mfma_f32_16x16x32_bf16 a[144:147],v[176:179],v[144:147],0\n"
"s_lshl3_add_u32 s69,s91,vcc_lo\n"
"s_mov_b32 s77,1310720\n"
"s_add_u32 m0,s78,131072\n"
"v_mfma_f32_16x16x32_bf16 a[144:147],v[240:243],v[208:211],a[144:147]\n"
"buffer_load_dwordx4 v110,s[20:23],s77 offen offset:0 lds\n"
"v_mfma_f32_16x16x32_bf16 a[148:151],v[244:247],v[208:211],0\n"
"s_lshl_b32 s69,s69,3\n"
"s_add_u32 s69,s69,148930560\n"
"v_mfma_f32_16x16x32_bf16 a[148:151],v[180:183],v[144:147],a[148:151]\n"
"s_lshl3_add_u32 s71,s94,vcc_lo\n"
"s_lshl_b32 s71,s71,3\n"
"v_mfma_f32_16x16x32_bf16 a[152:155],v[184:187],v[144:147],0\n"
"buffer_load_dwordx4 v111,s[20:23],s77 offen offset:1024 lds\n"
"v_mfma_f32_16x16x32_bf16 a[152:155],v[248:251],v[208:211],a[152:155]\n"
"s_add_u32 s71,s71,148930560\n"
"s_mov_b32 s6,8388608\n"
"v_mfma_f32_16x16x32_bf16 a[156:159],v[252:255],v[208:211],0\n"
"s_mov_b32 s7,0x20000\n"
"s_lshr_b32 s94,s88,1\n"
"v_mfma_f32_16x16x32_bf16 a[156:159],v[188:191],v[144:147],a[156:159]\n"
"buffer_load_dwordx4 v113,s[20:23],s77 offen offset:2048 lds\n"
"v_mfma_f32_16x16x32_bf16 a[160:163],v[160:163],v[148:151],0\n"
"s_and_b32 vcc_lo,s91,1\n"
"s_lshl1_add_u32 s95,s97,vcc_lo\n"
"v_mfma_f32_16x16x32_bf16 a[160:163],v[224:227],v[212:215],a[160:163]\n"
"s_and_b32 vcc_lo,s88,1\n"
"s_lshr_b32 vcc_hi,s91,1\n"
"v_mfma_f32_16x16x32_bf16 a[164:167],v[228:231],v[212:215],0\n"
"buffer_load_dwordx4 v114,s[20:23],s77 offen offset:3072 lds\n"
"v_mfma_f32_16x16x32_bf16 a[164:167],v[164:167],v[148:151],a[164:167]\n"
"s_lshl2_add_u32 s96,vcc_lo,vcc_hi\n"
"s_mov_b32 s76,384\n"
"s_add_u32 m0,s79,0\n"
"v_mfma_f32_16x16x32_bf16 a[168:171],v[168:171],v[148:151],0\n"
"buffer_load_dwordx4 v106,s[16:19],s76 offen offset:0 lds\n"
"v_mfma_f32_16x16x32_bf16 a[168:171],v[232:235],v[212:215],a[168:171]\n"
"s_lshr_b32 vcc_lo,s88,2\n"
"s_lshl2_add_u32 s96,vcc_lo,vcc_hi\n"
"v_mfma_f32_16x16x32_bf16 a[172:175],v[236:239],v[212:215],0\n"
"buffer_load_dwordx4 v107,s[16:19],s76 offen offset:1024 lds\n"
"v_mfma_f32_16x16x32_bf16 a[172:175],v[172:175],v[148:151],a[172:175]\n"
"s_sub_u32 vcc_lo,7,s88\n"
"s_min_u32 s94,vcc_lo,s88\n"
"v_mfma_f32_16x16x32_bf16 a[176:179],v[176:179],v[148:151],0\n"
"buffer_load_dwordx4 v108,s[16:19],s76 offen offset:2048 lds\n"
"v_mfma_f32_16x16x32_bf16 a[176:179],v[240:243],v[212:215],a[176:179]\n"
"s_lshr_b32 s94,s88,1\n"
"s_xor_b32 s94,s94,2\n"
"v_mfma_f32_16x16x32_bf16 a[180:183],v[244:247],v[212:215],0\n"
"buffer_load_dwordx4 v109,s[16:19],s76 offen offset:3072 lds\n"
"v_mfma_f32_16x16x32_bf16 a[180:183],v[180:183],v[148:151],a[180:183]\n"
"s_lshl_b32 s96,s88,2\n"
"s_mov_b32 s76,262528\n"
"s_add_u32 m0,s79,4096\n"
"v_mfma_f32_16x16x32_bf16 a[184:187],v[184:187],v[148:151],0\n"
"buffer_load_dwordx4 v106,s[16:19],s76 offen offset:0 lds\n"
"v_mfma_f32_16x16x32_bf16 a[184:187],v[248:251],v[212:215],a[184:187]\n"
"buffer_load_dwordx4 v107,s[16:19],s76 offen offset:1024 lds\n"
"v_mfma_f32_16x16x32_bf16 a[188:191],v[252:255],v[212:215],0\n"
"buffer_load_dwordx4 v108,s[16:19],s76 offen offset:2048 lds\n"
"buffer_load_dwordx4 v109,s[16:19],s76 offen offset:3072 lds\n"
"v_mfma_f32_16x16x32_bf16 a[188:191],v[188:191],v[148:151],a[188:191]\n"
"s_waitcnt vmcnt(24)\n"
"s_barrier\n"
"v_mfma_f32_16x16x32_bf16 a[192:195],v[224:227],v[216:219],a[192:195]\n"
"ds_read_b128 v[152:155],v80 offset:45056\n"
"s_and_b32 s96,s96,4\n"
"v_mfma_f32_16x16x32_bf16 a[224:227],v[224:227],v[220:223],a[224:227]\n"
"ds_read_b128 v[156:159],v80 offset:47104\n"
"buffer_load_dwordx4 v[8:11],v70,s[32:35],0 offen offset:0\n"
"v_mfma_f32_16x16x32_bf16 a[228:231],v[228:231],v[220:223],a[228:231]\n"
"ds_read_b64_tr_b16 v[160:161],v98 offset:0\n"
"buffer_load_dwordx4 v[12:15],v70,s[32:35],0 offen offset:512\n"
"v_mfma_f32_16x16x32_bf16 a[196:199],v[228:231],v[216:219],a[196:199]\n"
"ds_read_b64_tr_b16 v[162:163],v98 offset:1024\n"
"buffer_load_dwordx4 v[16:19],v70,s[32:35],0 offen offset:1024\n"
"v_mfma_f32_16x16x32_bf16 a[200:203],v[232:235],v[216:219],a[200:203]\n"
"ds_read_b64_tr_b16 v[164:165],v99 offset:0\n"
"buffer_load_dwordx4 v[20:23],v70,s[32:35],0 offen offset:1536\n"
"v_mfma_f32_16x16x32_bf16 a[232:235],v[232:235],v[220:223],a[232:235]\n"
"ds_read_b64_tr_b16 v[166:167],v99 offset:1024\n"
"s_mov_b32 s87,524288\n"
"v_mfma_f32_16x16x32_bf16 a[236:239],v[236:239],v[220:223],a[236:239]\n"
"ds_read_b64_tr_b16 v[168:169],v100 offset:0\n"
"buffer_load_dword v115,v112,s[36:39],s87 offen\n"
"v_mfma_f32_16x16x32_bf16 a[204:207],v[236:239],v[216:219],a[204:207]\n"
"ds_read_b64_tr_b16 v[170:171],v100 offset:1024\n"
"s_mov_b32 s87,1048576\n"
"v_mfma_f32_16x16x32_bf16 a[208:211],v[240:243],v[216:219],a[208:211]\n"
"ds_read_b64_tr_b16 v[172:173],v101 offset:0\n"
"buffer_load_dword v115,v112,s[36:39],s87 offen\n"
"v_mfma_f32_16x16x32_bf16 a[240:243],v[240:243],v[220:223],a[240:243]\n"
"ds_read_b64_tr_b16 v[174:175],v101 offset:1024\n"
"s_mov_b32 s87,1572864\n"
"v_mfma_f32_16x16x32_bf16 a[244:247],v[244:247],v[220:223],a[244:247]\n"
"ds_read_b64_tr_b16 v[176:177],v102 offset:0\n"
"buffer_load_dword v115,v112,s[36:39],s87 offen\n"
"v_mfma_f32_16x16x32_bf16 a[212:215],v[244:247],v[216:219],a[212:215]\n"
"ds_read_b64_tr_b16 v[178:179],v102 offset:1024\n"
"s_add_u32 s96,s96,s97\n"
"v_mfma_f32_16x16x32_bf16 a[216:219],v[248:251],v[216:219],a[216:219]\n"
"ds_read_b64_tr_b16 v[180:181],v103 offset:0\n"
"s_add_u32 s95,s91,s96\n"
"v_mfma_f32_16x16x32_bf16 a[248:251],v[248:251],v[220:223],a[248:251]\n"
"ds_read_b64_tr_b16 v[182:183],v103 offset:1024\n"
"s_and_b32 s95,s95,7\n"
"v_mfma_f32_16x16x32_bf16 a[252:255],v[252:255],v[220:223],a[252:255]\n"
"ds_read_b64_tr_b16 v[184:185],v104 offset:0\n"
"s_lshl3_add_u32 vcc_lo,s94,s96\n"
"v_mfma_f32_16x16x32_bf16 a[220:223],v[252:255],v[216:219],a[220:223]\n"
"s_lshl3_add_u32 vcc_lo,vcc_lo,s52\n"
"ds_read_b64_tr_b16 v[186:187],v104 offset:1024\n"
"s_waitcnt lgkmcnt(10)\n"
"v_mfma_f32_16x16x32_bf16 a[192:195],v[160:163],v[152:155],a[192:195]\n"
"ds_read_b64_tr_b16 v[188:189],v105 offset:0\n"
"s_lshl_b32 s13,vcc_lo,15\n"
"v_mfma_f32_16x16x32_bf16 a[224:227],v[160:163],v[156:159],a[224:227]\n"
"ds_read_b64_tr_b16 v[190:191],v105 offset:1024\n"
"s_lshl_b32 vcc_lo,s95,8\n"
"v_mfma_f32_16x16x32_bf16 a[228:231],v[164:167],v[156:159],a[228:231]\n"
"ds_read_b128 v[128:131],v80 offset:32768\n"
"s_add_u32 s13,s13,vcc_lo\n"
"v_mfma_f32_16x16x32_bf16 a[196:199],v[164:167],v[152:155],a[196:199]\n"
"ds_read_b128 v[192:195],v81 offset:32768\n"
"s_waitcnt lgkmcnt(10)\n"
"v_mfma_f32_16x16x32_bf16 a[200:203],v[168:171],v[152:155],a[200:203]\n"
"ds_read_b64_tr_b16 v[224:225],v98 offset:16384\n"
"s_lshl3_add_u32 vcc_hi,s94,s95\n"
"v_mfma_f32_16x16x32_bf16 a[232:235],v[168:171],v[156:159],a[232:235]\n"
"ds_read_b64_tr_b16 v[226:227],v98 offset:17408\n"
"s_lshl_b32 s72,vcc_hi,8\n"
"v_mfma_f32_16x16x32_bf16 a[236:239],v[172:175],v[156:159],a[236:239]\n"
"ds_read_b64_tr_b16 v[228:229],v99 offset:16384\n"
"s_add_u32 s72,s72,149061632\n"
"v_mfma_f32_16x16x32_bf16 a[204:207],v[172:175],v[152:155],a[204:207]\n"
"ds_read_b64_tr_b16 v[230:231],v99 offset:17408\n"
"s_waitcnt lgkmcnt(10)\n"
"v_mfma_f32_16x16x32_bf16 a[208:211],v[176:179],v[152:155],a[208:211]\n"
"s_or_b32 vcc_lo,s96,s52\n"
"ds_read_b64_tr_b16 v[232:233],v100 offset:16384\n"
"s_cselect_b32 s72,2146435072,s72\n"
"v_mfma_f32_16x16x32_bf16 a[240:243],v[176:179],v[156:159],a[240:243]\n"
"ds_read_b64_tr_b16 v[234:235],v100 offset:17408\n"
"s_lshl_b32 s3,vcc_hi,18\n"
"v_mfma_f32_16x16x32_bf16 a[244:247],v[180:183],v[156:159],a[244:247]\n"
"ds_read_b64_tr_b16 v[236:237],v101 offset:16384\n"
"s_lshl_b32 vcc_lo,s96,15\n"
"v_mfma_f32_16x16x32_bf16 a[212:215],v[180:183],v[152:155],a[212:215]\n"
"ds_read_b64_tr_b16 v[238:239],v101 offset:17408\n"
"s_waitcnt lgkmcnt(10)\n"
"v_mfma_f32_16x16x32_bf16 a[216:219],v[184:187],v[152:155],a[216:219]\n"
"ds_read_b64_tr_b16 v[240:241],v102 offset:16384\n"
"s_sub_u32 s3,s3,vcc_lo\n"
"v_mfma_f32_16x16x32_bf16 a[248:251],v[184:187],v[156:159],a[248:251]\n"
"ds_read_b64_tr_b16 v[242:243],v102 offset:17408\n"
"s_add_u32 s3,s3,s78\n"
"v_mfma_f32_16x16x32_bf16 a[252:255],v[188:191],v[156:159],a[252:255]\n"
"ds_read_b64_tr_b16 v[244:245],v103 offset:16384\n"
"s_add_u32 s3,s3,100909056\n"
"v_mfma_f32_16x16x32_bf16 a[220:223],v[188:191],v[152:155],a[220:223]\n"
"ds_read_b64_tr_b16 v[246:247],v103 offset:17408\n"
"s_waitcnt lgkmcnt(8)\n"
"v_mfma_f32_16x16x32_bf16 a[0:3],v[160:163],v[128:131],a[0:3]\n"
"ds_read_b64_tr_b16 v[248:249],v104 offset:16384\n"
"s_lshl3_add_u32 vcc_hi,vcc_hi,s96\n"
"v_mfma_f32_16x16x32_bf16 a[0:3],v[224:227],v[192:195],a[0:3]\n"
"ds_read_b64_tr_b16 v[250:251],v104 offset:17408\n"
"s_lshl2_add_u32 vcc_hi,vcc_hi,s52\n"
"v_mfma_f32_16x16x32_bf16 a[4:7],v[228:231],v[192:195],a[4:7]\n"
"ds_read_b64_tr_b16 v[252:253],v105 offset:16384\n"
"s_lshl_b32 s2,vcc_hi,4\n"
"v_mfma_f32_16x16x32_bf16 a[4:7],v[164:167],v[128:131],a[4:7]\n"
"ds_read_b64_tr_b16 v[254:255],v105 offset:17408\n"
"s_waitcnt lgkmcnt(8)\n"
"v_mfma_f32_16x16x32_bf16 a[8:11],v[168:171],v[128:131],a[8:11]\n"
"ds_read_b128 v[132:135],v80 offset:34816\n"
"s_add_u32 s2,s2,149028864\n"
"v_mfma_f32_16x16x32_bf16 a[8:11],v[232:235],v[192:195],a[8:11]\n"
"ds_read_b128 v[196:199],v81 offset:34816\n"
"s_lshl2_add_u32 vcc_lo,s95,s52\n"
"v_mfma_f32_16x16x32_bf16 a[12:15],v[236:239],v[192:195],a[12:15]\n"
"ds_read_b128 v[136:139],v80 offset:36864\n"
"s_lshl_b32 vcc_lo,vcc_lo,10\n"
"v_mfma_f32_16x16x32_bf16 a[12:15],v[172:175],v[128:131],a[12:15]\n"
"ds_read_b128 v[200:203],v81 offset:36864\n"
"s_waitcnt lgkmcnt(8)\n"
"v_mfma_f32_16x16x32_bf16 a[16:19],v[176:179],v[128:131],a[16:19]\n"
"ds_read_b128 v[140:143],v80 offset:38912\n"
"s_lshl_b32 vcc_hi,s94,22\n"
"v_mfma_f32_16x16x32_bf16 a[16:19],v[240:243],v[192:195],a[16:19]\n"
"ds_read_b128 v[204:207],v81 offset:38912\n"
"s_add_u32 s12,vcc_lo,vcc_hi\n"
"v_mfma_f32_16x16x32_bf16 a[20:23],v[244:247],v[192:195],a[20:23]\n"
"ds_read_b128 v[144:147],v80 offset:40960\n"
"s_lshl_b32 vcc_lo,s96,19\n"
"v_mfma_f32_16x16x32_bf16 a[20:23],v[180:183],v[128:131],a[20:23]\n"
"ds_read_b128 v[208:211],v81 offset:40960\n"
"s_waitcnt lgkmcnt(8)\n"
"v_mfma_f32_16x16x32_bf16 a[24:27],v[184:187],v[128:131],a[24:27]\n"
"ds_read_b128 v[148:151],v80 offset:43008\n"
"s_add_u32 s10,s12,vcc_lo\n"
"v_mfma_f32_16x16x32_bf16 a[24:27],v[248:251],v[192:195],a[24:27]\n"
"ds_read_b128 v[212:215],v81 offset:43008\n"
"s_add_u32 s10,s10,71303168\n"
"v_mfma_f32_16x16x32_bf16 a[28:31],v[252:255],v[192:195],a[28:31]\n"
"ds_read_b128 v[216:219],v81 offset:45056\n"
"s_mul_i32 vcc_lo,s52,31744\n"
"v_mfma_f32_16x16x32_bf16 a[28:31],v[188:191],v[128:131],a[28:31]\n"
"ds_read_b128 v[220:223],v81 offset:47104\n"
"s_waitcnt lgkmcnt(10)\n"
"v_mfma_f32_16x16x32_bf16 a[32:35],v[160:163],v[132:135],a[32:35]\n"
"s_add_u32 s10,s10,vcc_lo\n"
"s_lshl_b32 vcc_lo,s52,10\n"
"v_mfma_f32_16x16x32_bf16 a[32:35],v[224:227],v[196:199],a[32:35]\n"
"s_sub_u32 s12,57671680,vcc_lo\n"
"s_add_u32 s8,s79,54525952\n"
"v_mfma_f32_16x16x32_bf16 a[36:39],v[228:231],v[196:199],a[36:39]\n"
"s_and_b32 vcc_lo,s96,1\n"
"s_lshl_b32 s11,vcc_lo,10\n"
"v_mfma_f32_16x16x32_bf16 a[36:39],v[164:167],v[132:135],a[36:39]\n"
"s_add_u32 s11,s11,148932608\n"
"s_mov_b32 s9,148931072\n"
"v_mfma_f32_16x16x32_bf16 a[40:43],v[168:171],v[132:135],a[40:43]\n"
"s_waitcnt lgkmcnt(0)\n"
"s_barrier\n"
"v_mfma_f32_16x16x32_bf16 a[40:43],v[232:235],v[196:199],a[40:43]\n"
"s_cmp_eq_u32 s52,0\n"
"s_cselect_b32 s71,s71,2146435072\n"
"s_mov_b32 s77,1572864\n"
"s_add_u32 m0,s78,98304\n"
"v_mfma_f32_16x16x32_bf16 a[44:47],v[236:239],v[196:199],a[44:47]\n"
"buffer_load_dwordx4 v110,s[20:23],s77 offen offset:0 lds\n"
"v_mfma_f32_16x16x32_bf16 a[44:47],v[172:175],v[132:135],a[44:47]\n"
"v_mfma_f32_16x16x32_bf16 a[48:51],v[176:179],v[132:135],a[48:51]\n"
"v_mfma_f32_16x16x32_bf16 a[48:51],v[240:243],v[196:199],a[48:51]\n"
"v_mfma_f32_16x16x32_bf16 a[52:55],v[244:247],v[196:199],a[52:55]\n"
"buffer_load_dwordx4 v111,s[20:23],s77 offen offset:1024 lds\n"
"v_mfma_f32_16x16x32_bf16 a[52:55],v[180:183],v[132:135],a[52:55]\n"
"v_mfma_f32_16x16x32_bf16 a[56:59],v[184:187],v[132:135],a[56:59]\n"
"v_mfma_f32_16x16x32_bf16 a[56:59],v[248:251],v[196:199],a[56:59]\n"
"v_mfma_f32_16x16x32_bf16 a[60:63],v[252:255],v[196:199],a[60:63]\n"
"buffer_load_dwordx4 v113,s[20:23],s77 offen offset:2048 lds\n"
"v_mfma_f32_16x16x32_bf16 a[60:63],v[188:191],v[132:135],a[60:63]\n"
"v_mfma_f32_16x16x32_bf16 a[64:67],v[160:163],v[136:139],a[64:67]\n"
"v_mfma_f32_16x16x32_bf16 a[64:67],v[224:227],v[200:203],a[64:67]\n"
"v_mfma_f32_16x16x32_bf16 a[68:71],v[228:231],v[200:203],a[68:71]\n"
"buffer_load_dwordx4 v114,s[20:23],s77 offen offset:3072 lds\n"
"v_mfma_f32_16x16x32_bf16 a[68:71],v[164:167],v[136:139],a[68:71]\n"
"v_mfma_f32_16x16x32_bf16 a[72:75],v[168:171],v[136:139],a[72:75]\n"
"v_mfma_f32_16x16x32_bf16 a[72:75],v[232:235],v[200:203],a[72:75]\n"
"s_mov_b32 s77,1835008\n"
"s_add_u32 m0,s78,114688\n"
"v_mfma_f32_16x16x32_bf16 a[76:79],v[236:239],v[200:203],a[76:79]\n"
"buffer_load_dwordx4 v110,s[20:23],s77 offen offset:0 lds\n"
"v_mfma_f32_16x16x32_bf16 a[76:79],v[172:175],v[136:139],a[76:79]\n"
"v_mfma_f32_16x16x32_bf16 a[80:83],v[176:179],v[136:139],a[80:83]\n"
"v_mfma_f32_16x16x32_bf16 a[80:83],v[240:243],v[200:203],a[80:83]\n"
"v_mfma_f32_16x16x32_bf16 a[84:87],v[244:247],v[200:203],a[84:87]\n"
"buffer_load_dwordx4 v111,s[20:23],s77 offen offset:1024 lds\n"
"v_mfma_f32_16x16x32_bf16 a[84:87],v[180:183],v[136:139],a[84:87]\n"
"v_mfma_f32_16x16x32_bf16 a[88:91],v[184:187],v[136:139],a[88:91]\n"
"v_mfma_f32_16x16x32_bf16 a[88:91],v[248:251],v[200:203],a[88:91]\n"
"v_mfma_f32_16x16x32_bf16 a[92:95],v[252:255],v[200:203],a[92:95]\n"
"buffer_load_dwordx4 v113,s[20:23],s77 offen offset:2048 lds\n"
"v_mfma_f32_16x16x32_bf16 a[92:95],v[188:191],v[136:139],a[92:95]\n"
"s_waitcnt vmcnt(10)\n"
"v_mfma_f32_16x16x32_bf16 a[96:99],v[160:163],v[140:143],a[96:99]\n"
"v_lshlrev_b32 v24,16,v8\n"
"v_and_b32 v25,0xffff0000,v8\n"
"v_mfma_f32_16x16x32_bf16 a[96:99],v[224:227],v[204:207],a[96:99]\n"
"v_lshlrev_b32 v26,16,v9\n"
"v_and_b32 v27,0xffff0000,v9\n"
"v_mfma_f32_16x16x32_bf16 a[100:103],v[228:231],v[204:207],a[100:103]\n"
"buffer_load_dwordx4 v114,s[20:23],s77 offen offset:3072 lds\n"
"v_mfma_f32_16x16x32_bf16 a[100:103],v[164:167],v[140:143],a[100:103]\n"
"v_lshlrev_b32 v28,16,v10\n"
"v_and_b32 v29,0xffff0000,v10\n"
"v_mfma_f32_16x16x32_bf16 a[104:107],v[168:171],v[140:143],a[104:107]\n"
"v_lshlrev_b32 v30,16,v11\n"
"v_and_b32 v31,0xffff0000,v11\n"
"v_mfma_f32_16x16x32_bf16 a[104:107],v[232:235],v[204:207],a[104:107]\n"
"v_lshlrev_b32 v32,16,v12\n"
"v_and_b32 v33,0xffff0000,v12\n"
"v_mfma_f32_16x16x32_bf16 a[108:111],v[236:239],v[204:207],a[108:111]\n"
"s_mov_b32 s76,512\n"
"v_lshlrev_b32 v34,16,v13\n"
"s_add_u32 m0,s79,32768\n"
"v_mfma_f32_16x16x32_bf16 a[108:111],v[172:175],v[140:143],a[108:111]\n"
"buffer_load_dwordx4 v106,s[16:19],s76 offen offset:0 lds\n"
"v_mfma_f32_16x16x32_bf16 a[112:115],v[176:179],v[140:143],a[112:115]\n"
"v_and_b32 v35,0xffff0000,v13\n"
"v_lshlrev_b32 v36,16,v14\n"
"v_mfma_f32_16x16x32_bf16 a[112:115],v[240:243],v[204:207],a[112:115]\n"
"v_and_b32 v37,0xffff0000,v14\n"
"v_lshlrev_b32 v38,16,v15\n"
"v_mfma_f32_16x16x32_bf16 a[116:119],v[244:247],v[204:207],a[116:119]\n"
"v_and_b32 v39,0xffff0000,v15\n"
"v_lshlrev_b32 v40,16,v16\n"
"v_mfma_f32_16x16x32_bf16 a[116:119],v[180:183],v[140:143],a[116:119]\n"
"v_and_b32 v41,0xffff0000,v16\n"
"v_lshlrev_b32 v42,16,v17\n"
"v_mfma_f32_16x16x32_bf16 a[120:123],v[184:187],v[140:143],a[120:123]\n"
"buffer_load_dwordx4 v107,s[16:19],s76 offen offset:1024 lds\n"
"v_mfma_f32_16x16x32_bf16 a[120:123],v[248:251],v[204:207],a[120:123]\n"
"v_and_b32 v43,0xffff0000,v17\n"
"v_lshlrev_b32 v44,16,v18\n"
"v_mfma_f32_16x16x32_bf16 a[124:127],v[252:255],v[204:207],a[124:127]\n"
"v_and_b32 v45,0xffff0000,v18\n"
"v_lshlrev_b32 v46,16,v19\n"
"v_mfma_f32_16x16x32_bf16 a[124:127],v[188:191],v[140:143],a[124:127]\n"
"v_and_b32 v47,0xffff0000,v19\n"
"v_lshlrev_b32 v48,16,v20\n"
"v_mfma_f32_16x16x32_bf16 a[128:131],v[160:163],v[144:147],a[128:131]\n"
"v_and_b32 v49,0xffff0000,v20\n"
"v_lshlrev_b32 v50,16,v21\n"
"v_mfma_f32_16x16x32_bf16 a[128:131],v[224:227],v[208:211],a[128:131]\n"
"buffer_load_dwordx4 v108,s[16:19],s76 offen offset:2048 lds\n"
"v_mfma_f32_16x16x32_bf16 a[132:135],v[228:231],v[208:211],a[132:135]\n"
"v_and_b32 v51,0xffff0000,v21\n"
"v_lshlrev_b32 v52,16,v22\n"
"v_mfma_f32_16x16x32_bf16 a[132:135],v[164:167],v[144:147],a[132:135]\n"
"v_and_b32 v53,0xffff0000,v22\n"
"v_lshlrev_b32 v54,16,v23\n"
"v_mfma_f32_16x16x32_bf16 a[136:139],v[168:171],v[144:147],a[136:139]\n"
"v_and_b32 v55,0xffff0000,v23\n"
"v_add_f32 v56,v24,v25\n"
"v_mfma_f32_16x16x32_bf16 a[136:139],v[232:235],v[208:211],a[136:139]\n"
"v_add_f32 v57,v26,v27\n"
"v_add_f32 v56,v56,v28\n"
"v_mfma_f32_16x16x32_bf16 a[140:143],v[236:239],v[208:211],a[140:143]\n"
"buffer_load_dwordx4 v109,s[16:19],s76 offen offset:3072 lds\n"
"v_mfma_f32_16x16x32_bf16 a[140:143],v[172:175],v[144:147],a[140:143]\n"
"v_add_f32 v57,v57,v29\n"
"v_add_f32 v56,v56,v30\n"
"v_mfma_f32_16x16x32_bf16 a[144:147],v[176:179],v[144:147],a[144:147]\n"
"v_add_f32 v57,v57,v31\n"
"v_add_f32 v56,v56,v32\n"
"v_mfma_f32_16x16x32_bf16 a[144:147],v[240:243],v[208:211],a[144:147]\n"
"v_readfirstlane_b32 s82,v78\n"
"v_readfirstlane_b32 s83,v79\n"
"v_mfma_f32_16x16x32_bf16 a[148:151],v[244:247],v[208:211],a[148:151]\n"
"s_mov_b32 s76,262656\n"
"v_add_f32 v57,v57,v33\n"
"s_add_u32 m0,s79,36864\n"
"v_mfma_f32_16x16x32_bf16 a[148:151],v[180:183],v[144:147],a[148:151]\n"
"buffer_load_dwordx4 v106,s[16:19],s76 offen offset:0 lds\n"
"s_mul_i32 s80,s82,0x9e3779b1\n"
"v_mfma_f32_16x16x32_bf16 a[152:155],v[184:187],v[144:147],a[152:155]\n"
"s_add_u32 s80,s80,0x7f4a7c15\n"
"v_add_f32 v56,v56,v34\n"
"v_add_f32 v57,v57,v35\n"
"v_mfma_f32_16x16x32_bf16 a[152:155],v[248:251],v[208:211],a[152:155]\n"
"s_add_u32 s81,s83,s80\n"
"s_add_u32 s81,s81,1\n"
"v_mfma_f32_16x16x32_bf16 a[156:159],v[252:255],v[208:211],a[156:159]\n"
"v_mov_b32 v124,s80\n"
"v_mov_b32 v125,s81\n"
"v_mfma_f32_16x16x32_bf16 a[156:159],v[188:191],v[144:147],a[156:159]\n"
"v_add_f32 v56,v56,v36\n"
"v_add_f32 v57,v57,v37\n"
"v_mfma_f32_16x16x32_bf16 a[160:163],v[160:163],v[148:151],a[160:163]\n"
"buffer_load_dwordx4 v107,s[16:19],s76 offen offset:1024 lds\n"
"v_mfma_f32_16x16x32_bf16 a[160:163],v[224:227],v[212:215],a[160:163]\n"
"v_add_f32 v56,v56,v38\n"
"v_add_f32 v57,v57,v39\n"
"v_mfma_f32_16x16x32_bf16 a[164:167],v[228:231],v[212:215],a[164:167]\n"
"v_add_f32 v56,v56,v40\n"
"v_add_f32 v57,v57,v41\n"
"v_mfma_f32_16x16x32_bf16 a[164:167],v[164:167],v[148:151],a[164:167]\n"
"v_add_f32 v56,v56,v42\n"
"v_add_f32 v57,v57,v43\n"
"v_mfma_f32_16x16x32_bf16 a[168:171],v[168:171],v[148:151],a[168:171]\n"
"v_add_f32 v56,v56,v44\n"
"v_add_f32 v57,v57,v45\n"
"v_mfma_f32_16x16x32_bf16 a[168:171],v[232:235],v[212:215],a[168:171]\n"
"buffer_load_dwordx4 v108,s[16:19],s76 offen offset:2048 lds\n"
"v_mfma_f32_16x16x32_bf16 a[172:175],v[236:239],v[212:215],a[172:175]\n"
"v_add_f32 v56,v56,v46\n"
"v_add_f32 v57,v57,v47\n"
"v_mfma_f32_16x16x32_bf16 a[172:175],v[172:175],v[148:151],a[172:175]\n"
"v_add_f32 v56,v56,v48\n"
"v_add_f32 v57,v57,v49\n"
"v_mfma_f32_16x16x32_bf16 a[176:179],v[176:179],v[148:151],a[176:179]\n"
"v_add_f32 v56,v56,v50\n"
"v_add_f32 v57,v57,v51\n"
"v_mfma_f32_16x16x32_bf16 a[176:179],v[240:243],v[212:215],a[176:179]\n"
"v_add_f32 v56,v56,v52\n"
"v_add_f32 v57,v57,v53\n"
"v_mfma_f32_16x16x32_bf16 a[180:183],v[244:247],v[212:215],a[180:183]\n"
"buffer_load_dwordx4 v109,s[16:19],s76 offen offset:3072 lds\n"
"v_mfma_f32_16x16x32_bf16 a[180:183],v[180:183],v[148:151],a[180:183]\n"
"s_mov_b32 s87,2097152\n"
"v_add_f32 v56,v56,v54\n"
"v_add_f32 v57,v57,v55\n"
"v_mfma_f32_16x16x32_bf16 a[184:187],v[184:187],v[148:151],a[184:187]\n"
"buffer_load_dword v115,v112,s[36:39],s87 offen\n"
"v_mfma_f32_16x16x32_bf16 a[184:187],v[248:251],v[212:215],a[184:187]\n"
"s_mov_b32 s87,2621440\n"
"v_add_f32 v56,v56,v57\n"
"v_mfma_f32_16x16x32_bf16 a[188:191],v[252:255],v[212:215],a[188:191]\n"
"buffer_load_dword v115,v112,s[36:39],s87 offen\n"
"v_add_f32_dpp v56,v56,v56 quad_perm:[1,0,3,2] row_mask:0xf bank_mask:0xf\n"
"v_mfma_f32_16x16x32_bf16 a[188:191],v[188:191],v[148:151],a[188:191]\n"
"s_barrier\n"
"v_mfma_f32_16x16x32_bf16 a[192:195],v[224:227],v[216:219],a[192:195]\n"
"ds_read_b128 v[152:155],v82 offset:12288\n"
"v_mfma_f32_16x16x32_bf16 a[224:227],v[224:227],v[220:223],a[224:227]\n"
"ds_read_b128 v[156:159],v82 offset:14336\n"
"v_add_f32_dpp v56,v56,v56 quad_perm:[2,3,0,1] row_mask:0xf bank_mask:0xf\n"
"v_mfma_f32_16x16x32_bf16 a[228:231],v[228:231],v[220:223],a[228:231]\n"
"ds_read_b64_tr_b16 v[160:161],v98 offset:49152\n"
"v_mfma_f32_16x16x32_bf16 a[196:199],v[228:231],v[216:219],a[196:199]\n"
"ds_read_b64_tr_b16 v[162:163],v98 offset:50176\n"
"v_add_f32_dpp v56,v56,v56 row_ror:8 row_mask:0xf bank_mask:0xf\n"
"v_mfma_f32_16x16x32_bf16 a[200:203],v[232:235],v[216:219],a[200:203]\n"
"ds_read_b64_tr_b16 v[164:165],v99 offset:49152\n"
"v_mov_b32 v57,v56\n"
"v_mfma_f32_16x16x32_bf16 a[232:235],v[232:235],v[220:223],a[232:235]\n"
"ds_read_b64_tr_b16 v[166:167],v99 offset:50176\n"
"v_mfma_f32_16x16x32_bf16 a[236:239],v[236:239],v[220:223],a[236:239]\n"
"ds_read_b64_tr_b16 v[168:169],v100 offset:49152\n"
"v_permlane16_swap_b32 v57,v56\n"
"v_mfma_f32_16x16x32_bf16 a[204:207],v[236:239],v[216:219],a[204:207]\n"
"ds_read_b64_tr_b16 v[170:171],v100 offset:50176\n"
"v_add_f32 v56,v56,v57\n"
"v_mfma_f32_16x16x32_bf16 a[208:211],v[240:243],v[216:219],a[208:211]\n"
"ds_read_b64_tr_b16 v[172:173],v101 offset:49152\n"
"v_mov_b32 v57,v56\n"
"v_mfma_f32_16x16x32_bf16 a[240:243],v[240:243],v[220:223],a[240:243]\n"
"ds_read_b64_tr_b16 v[174:175],v101 offset:50176\n"
"v_mfma_f32_16x16x32_bf16 a[244:247],v[244:247],v[220:223],a[244:247]\n"
"ds_read_b64_tr_b16 v[176:177],v102 offset:49152\n"
"v_permlane32_swap_b32 v57,v56\n"
"v_mfma_f32_16x16x32_bf16 a[212:215],v[244:247],v[216:219],a[212:215]\n"
"ds_read_b64_tr_b16 v[178:179],v102 offset:50176\n"
"v_add_f32 v56,v56,v57\n"
"v_mfma_f32_16x16x32_bf16 a[216:219],v[248:251],v[216:219],a[216:219]\n"
"ds_read_b64_tr_b16 v[180:181],v103 offset:49152\n"
"v_mul_f32 v58,0x3a800000,v56\n"
"v_mfma_f32_16x16x32_bf16 a[248:251],v[248:251],v[220:223],a[248:251]\n"
"ds_read_b64_tr_b16 v[182:183],v103 offset:50176\n"
"v_sub_f32 v62,v24,v58\n"
"v_mfma_f32_16x16x32_bf16 a[252:255],v[252:255],v[220:223],a[252:255]\n"
"ds_read_b64_tr_b16 v[184:185],v104 offset:49152\n"
"v_mul_f32 v59,v62,v62\n"
"v_mfma_f32_16x16x32_bf16 a[220:223],v[252:255],v[216:219],a[220:223]\n"
"ds_read_b64_tr_b16 v[186:187],v104 offset:50176\n"
"v_sub_f32 v63,v25,v58\n"
"s_waitcnt lgkmcnt(10)\n"
"v_mfma_f32_16x16x32_bf16 a[192:195],v[160:163],v[152:155],a[192:195]\n"
"ds_read_b64_tr_b16 v[188:189],v105 offset:49152\n"
"v_mul_f32 v60,v63,v63\n"
"v_mfma_f32_16x16x32_bf16 a[224:227],v[160:163],v[156:159],a[224:227]\n"
"ds_read_b64_tr_b16 v[190:191],v105 offset:50176\n"
"v_sub_f32 v64,v26,v58\n"
"v_mfma_f32_16x16x32_bf16 a[228:231],v[164:167],v[156:159],a[228:231]\n"
"ds_read_b128 v[128:131],v82 offset:0\n"
"v_fmac_f32 v59,v64,v64\n"
"v_mfma_f32_16x16x32_bf16 a[196:199],v[164:167],v[152:155],a[196:199]\n"
"ds_read_b128 v[192:195],v83 offset:0\n"
"s_waitcnt lgkmcnt(10)\n"
"v_mfma_f32_16x16x32_bf16 a[200:203],v[168:171],v[152:155],a[200:203]\n"
"ds_read_b64_tr_b16 v[224:225],v98 offset:32768\n"
"v_sub_f32 v65,v27,v58\n"
"v_mfma_f32_16x16x32_bf16 a[232:235],v[168:171],v[156:159],a[232:235]\n"
"ds_read_b64_tr_b16 v[226:227],v98 offset:33792\n"
"v_fmac_f32 v60,v65,v65\n"
"v_mfma_f32_16x16x32_bf16 a[236:239],v[172:175],v[156:159],a[236:239]\n"
"ds_read_b64_tr_b16 v[228:229],v99 offset:32768\n"
"v_sub_f32 v62,v28,v58\n"
"v_mfma_f32_16x16x32_bf16 a[204:207],v[172:175],v[152:155],a[204:207]\n"
"ds_read_b64_tr_b16 v[230:231],v99 offset:33792\n"
"s_waitcnt lgkmcnt(10)\n"
"v_mfma_f32_16x16x32_bf16 a[208:211],v[176:179],v[152:155],a[208:211]\n"
"ds_read_b64_tr_b16 v[232:233],v100 offset:32768\n"
"v_fmac_f32 v59,v62,v62\n"
"v_mfma_f32_16x16x32_bf16 a[240:243],v[176:179],v[156:159],a[240:243]\n"
"ds_read_b64_tr_b16 v[234:235],v100 offset:33792\n"
"v_sub_f32 v63,v29,v58\n"
"v_mfma_f32_16x16x32_bf16 a[244:247],v[180:183],v[156:159],a[244:247]\n"
"ds_read_b64_tr_b16 v[236:237],v101 offset:32768\n"
"v_fmac_f32 v60,v63,v63\n"
"v_mfma_f32_16x16x32_bf16 a[212:215],v[180:183],v[152:155],a[212:215]\n"
"ds_read_b64_tr_b16 v[238:239],v101 offset:33792\n"
"s_waitcnt lgkmcnt(10)\n"
"v_mfma_f32_16x16x32_bf16 a[216:219],v[184:187],v[152:155],a[216:219]\n"
"ds_read_b64_tr_b16 v[240:241],v102 offset:32768\n"
"v_sub_f32 v64,v30,v58\n"
"v_mfma_f32_16x16x32_bf16 a[248:251],v[184:187],v[156:159],a[248:251]\n"
"ds_read_b64_tr_b16 v[242:243],v102 offset:33792\n"
"v_fmac_f32 v59,v64,v64\n"
"v_mfma_f32_16x16x32_bf16 a[252:255],v[188:191],v[156:159],a[252:255]\n"
"ds_read_b64_tr_b16 v[244:245],v103 offset:32768\n"
"v_sub_f32 v65,v31,v58\n"
"v_mfma_f32_16x16x32_bf16 a[220:223],v[188:191],v[152:155],a[220:223]\n"
"ds_read_b64_tr_b16 v[246:247],v103 offset:33792\n"
"s_waitcnt lgkmcnt(8)\n"
"v_mfma_f32_16x16x32_bf16 a[0:3],v[160:163],v[128:131],a[0:3]\n"
"ds_read_b64_tr_b16 v[248:249],v104 offset:32768\n"
"v_fmac_f32 v60,v65,v65\n"
"v_mfma_f32_16x16x32_bf16 a[0:3],v[224:227],v[192:195],a[0:3]\n"
"ds_read_b64_tr_b16 v[250:251],v104 offset:33792\n"
"v_sub_f32 v62,v32,v58\n"
"v_mfma_f32_16x16x32_bf16 a[4:7],v[228:231],v[192:195],a[4:7]\n"
"ds_read_b64_tr_b16 v[252:253],v105 offset:32768\n"
"v_fmac_f32 v59,v62,v62\n"
"v_mfma_f32_16x16x32_bf16 a[4:7],v[164:167],v[128:131],a[4:7]\n"
"ds_read_b64_tr_b16 v[254:255],v105 offset:33792\n"
"s_waitcnt lgkmcnt(8)\n"
"v_mfma_f32_16x16x32_bf16 a[8:11],v[168:171],v[128:131],a[8:11]\n"
"ds_read_b128 v[132:135],v82 offset:2048\n"
"v_sub_f32 v63,v33,v58\n"
"v_mfma_f32_16x16x32_bf16 a[8:11],v[232:235],v[192:195],a[8:11]\n"
"ds_read_b128 v[196:199],v83 offset:2048\n"
"v_fmac_f32 v60,v63,v63\n"
"v_mfma_f32_16x16x32_bf16 a[12:15],v[236:239],v[192:195],a[12:15]\n"
"ds_read_b128 v[136:139],v82 offset:4096\n"
"v_sub_f32 v64,v34,v58\n"
"v_mfma_f32_16x16x32_bf16 a[12:15],v[172:175],v[128:131],a[12:15]\n"
"ds_read_b128 v[200:203],v83 offset:4096\n"
"s_waitcnt lgkmcnt(8)\n"
"v_mfma_f32_16x16x32_bf16 a[16:19],v[176:179],v[128:131],a[16:19]\n"
"ds_read_b128 v[140:143],v82 offset:6144\n"
"v_fmac_f32 v59,v64,v64\n"
"v_mfma_f32_16x16x32_bf16 a[16:19],v[240:243],v[192:195],a[16:19]\n"
"ds_read_b128 v[204:207],v83 offset:6144\n"
"v_sub_f32 v65,v35,v58\n"
"v_mfma_f32_16x16x32_bf16 a[20:23],v[244:247],v[192:195],a[20:23]\n"
"ds_read_b128 v[144:147],v82 offset:8192\n"
"v_fmac_f32 v60,v65,v65\n"
"v_mfma_f32_16x16x32_bf16 a[20:23],v[180:183],v[128:131],a[20:23]\n"
"ds_read_b128 v[208:211],v83 offset:8192\n"
"s_waitcnt lgkmcnt(8)\n"
"v_mfma_f32_16x16x32_bf16 a[24:27],v[184:187],v[128:131],a[24:27]\n"
"ds_read_b128 v[148:151],v82 offset:10240\n"
"v_sub_f32 v62,v36,v58\n"
"v_mfma_f32_16x16x32_bf16 a[24:27],v[248:251],v[192:195],a[24:27]\n"
"ds_read_b128 v[212:215],v83 offset:10240\n"
"v_fmac_f32 v59,v62,v62\n"
"v_mfma_f32_16x16x32_bf16 a[28:31],v[252:255],v[192:195],a[28:31]\n"
"ds_read_b128 v[216:219],v83 offset:12288\n"
"v_sub_f32 v63,v37,v58\n"
"v_mfma_f32_16x16x32_bf16 a[28:31],v[188:191],v[128:131],a[28:31]\n"
"ds_read_b128 v[220:223],v83 offset:14336\n"
"s_waitcnt lgkmcnt(10)\n"
"v_mfma_f32_16x16x32_bf16 a[32:35],v[160:163],v[132:135],a[32:35]\n"
"v_fmac_f32 v60,v63,v63\n"
"v_sub_f32 v64,v38,v58\n"
"v_mfma_f32_16x16x32_bf16 a[32:35],v[224:227],v[196:199],a[32:35]\n"
"v_fmac_f32 v59,v64,v64\n"
"v_sub_f32 v65,v39,v58\n"
"v_mfma_f32_16x16x32_bf16 a[36:39],v[228:231],v[196:199],a[36:39]\n"
"v_fmac_f32 v60,v65,v65\n"
"v_sub_f32 v62,v40,v58\n"
"v_mfma_f32_16x16x32_bf16 a[36:39],v[164:167],v[132:135],a[36:39]\n"
"v_fmac_f32 v59,v62,v62\n"
"v_sub_f32 v63,v41,v58\n"
"v_mfma_f32_16x16x32_bf16 a[40:43],v[168:171],v[132:135],a[40:43]\n"
"s_waitcnt lgkmcnt(0)\n"
"s_barrier\n"
"v_mfma_f32_16x16x32_bf16 a[40:43],v[232:235],v[196:199],a[40:43]\n"
"s_mov_b32 s77,2097152\n"
"v_fmac_f32 v60,v63,v63\n"
"s_add_u32 m0,s78,147456\n"
"v_mfma_f32_16x16x32_bf16 a[44:47],v[236:239],v[196:199],a[44:47]\n"
"buffer_load_dwordx4 v110,s[20:23],s77 offen offset:0 lds\n"
"v_mfma_f32_16x16x32_bf16 a[44:47],v[172:175],v[132:135],a[44:47]\n"
"v_sub_f32 v64,v42,v58\n"
"v_fmac_f32 v59,v64,v64\n"
"v_mfma_f32_16x16x32_bf16 a[48:51],v[176:179],v[132:135],a[48:51]\n"
"v_sub_f32 v65,v43,v58\n"
"v_fmac_f32 v60,v65,v65\n"
"v_mfma_f32_16x16x32_bf16 a[48:51],v[240:243],v[196:199],a[48:51]\n"
"v_sub_f32 v62,v44,v58\n"
"v_fmac_f32 v59,v62,v62\n"
"v_mfma_f32_16x16x32_bf16 a[52:55],v[244:247],v[196:199],a[52:55]\n"
"buffer_load_dwordx4 v111,s[20:23],s77 offen offset:1024 lds\n"
"v_mfma_f32_16x16x32_bf16 a[52:55],v[180:183],v[132:135],a[52:55]\n"
"v_sub_f32 v63,v45,v58\n"
"v_fmac_f32 v60,v63,v63\n"
"v_mfma_f32_16x16x32_bf16 a[56:59],v[184:187],v[132:135],a[56:59]\n"
"v_sub_f32 v64,v46,v58\n"
"v_fmac_f32 v59,v64,v64\n"
"v_mfma_f32_16x16x32_bf16 a[56:59],v[248:251],v[196:199],a[56:59]\n"
"v_sub_f32 v65,v47,v58\n"
"v_fmac_f32 v60,v65,v65\n"
"v_mfma_f32_16x16x32_bf16 a[60:63],v[252:255],v[196:199],a[60:63]\n"
"buffer_load_dwordx4 v113,s[20:23],s77 offen offset:2048 lds\n"
"v_mfma_f32_16x16x32_bf16 a[60:63],v[188:191],v[132:135],a[60:63]\n"
"v_sub_f32 v62,v48,v58\n"
"v_fmac_f32 v59,v62,v62\n"
"v_mfma_f32_16x16x32_bf16 a[64:67],v[160:163],v[136:139],a[64:67]\n"
"v_sub_f32 v63,v49,v58\n"
"v_fmac_f32 v60,v63,v63\n"
"v_mfma_f32_16x16x32_bf16 a[64:67],v[224:227],v[200:203],a[64:67]\n"
"v_sub_f32 v64,v50,v58\n"
"v_fmac_f32 v59,v64,v64\n"
"v_mfma_f32_16x16x32_bf16 a[68:71],v[228:231],v[200:203],a[68:71]\n"
"buffer_load_dwordx4 v114,s[20:23],s77 offen offset:3072 lds\n"
"v_mfma_f32_16x16x32_bf16 a[68:71],v[164:167],v[136:139],a[68:71]\n"
"v_sub_f32 v65,v51,v58\n"
"v_fmac_f32 v60,v65,v65\n"
"v_mfma_f32_16x16x32_bf16 a[72:75],v[168:171],v[136:139],a[72:75]\n"
"v_sub_f32 v62,v52,v58\n"
"v_fmac_f32 v59,v62,v62\n"
"v_mfma_f32_16x16x32_bf16 a[72:75],v[232:235],v[200:203],a[72:75]\n"
"s_mov_b32 s77,2359296\n"
"v_sub_f32 v63,v53,v58\n"
"s_add_u32 m0,s78,131072\n"
"v_mfma_f32_16x16x32_bf16 a[76:79],v[236:239],v[200:203],a[76:79]\n"
"buffer_load_dwordx4 v110,s[20:23],s77 offen offset:0 lds\n"
"v_mfma_f32_16x16x32_bf16 a[76:79],v[172:175],v[136:139],a[76:79]\n"
"v_fmac_f32 v60,v63,v63\n"
"v_sub_f32 v64,v54,v58\n"
"v_mfma_f32_16x16x32_bf16 a[80:83],v[176:179],v[136:139],a[80:83]\n"
"v_fmac_f32 v59,v64,v64\n"
"v_sub_f32 v65,v55,v58\n"
"v_mfma_f32_16x16x32_bf16 a[80:83],v[240:243],v[200:203],a[80:83]\n"
"v_fmac_f32 v60,v65,v65\n"
"v_add_f32 v59,v59,v60\n"
"v_mfma_f32_16x16x32_bf16 a[84:87],v[244:247],v[200:203],a[84:87]\n"
"buffer_load_dwordx4 v111,s[20:23],s77 offen offset:1024 lds\n"
"v_mfma_f32_16x16x32_bf16 a[84:87],v[180:183],v[136:139],a[84:87]\n"
"v_mfma_f32_16x16x32_bf16 a[88:91],v[184:187],v[136:139],a[88:91]\n"
"v_add_f32_dpp v59,v59,v59 quad_perm:[1,0,3,2] row_mask:0xf bank_mask:0xf\n"
"v_mfma_f32_16x16x32_bf16 a[88:91],v[248:251],v[200:203],a[88:91]\n"
"s_nop 0\n"
"v_add_f32_dpp v59,v59,v59 quad_perm:[2,3,0,1] row_mask:0xf bank_mask:0xf\n"
"v_mfma_f32_16x16x32_bf16 a[92:95],v[252:255],v[200:203],a[92:95]\n"
"buffer_load_dwordx4 v113,s[20:23],s77 offen offset:2048 lds\n"
"v_mfma_f32_16x16x32_bf16 a[92:95],v[188:191],v[136:139],a[92:95]\n"
"v_add_f32_dpp v59,v59,v59 row_ror:8 row_mask:0xf bank_mask:0xf\n"
"v_mov_b32 v57,v59\n"
"v_mfma_f32_16x16x32_bf16 a[96:99],v[160:163],v[140:143],a[96:99]\n"
"v_mfma_f32_16x16x32_bf16 a[96:99],v[224:227],v[204:207],a[96:99]\n"
"v_permlane16_swap_b32 v57,v59\n"
"v_add_f32 v59,v59,v57\n"
"v_mfma_f32_16x16x32_bf16 a[100:103],v[228:231],v[204:207],a[100:103]\n"
"buffer_load_dwordx4 v114,s[20:23],s77 offen offset:3072 lds\n"
"v_mfma_f32_16x16x32_bf16 a[100:103],v[164:167],v[140:143],a[100:103]\n"
"v_mov_b32 v57,v59\n"
"v_mfma_f32_16x16x32_bf16 a[104:107],v[168:171],v[140:143],a[104:107]\n"
"s_nop 0\n"
"v_permlane32_swap_b32 v57,v59\n"
"v_add_f32 v59,v59,v57\n"
"v_mfma_f32_16x16x32_bf16 a[104:107],v[232:235],v[204:207],a[104:107]\n"
"v_mul_f32 v59,0x3a800000,v59\n"
"v_cvt_pk_bf16_f32 v59,v59,v59\n"
"v_mfma_f32_16x16x32_bf16 a[108:111],v[236:239],v[204:207],a[108:111]\n"
"s_mov_b32 s76,640\n"
"v_lshlrev_b32 v59,16,v59\n"
"s_add_u32 m0,s79,65536\n"
"v_mfma_f32_16x16x32_bf16 a[108:111],v[172:175],v[140:143],a[108:111]\n"
"buffer_load_dwordx4 v106,s[16:19],s76 offen offset:0 lds\n"
"v_mfma_f32_16x16x32_bf16 a[112:115],v[176:179],v[140:143],a[112:115]\n"
"v_add_f32 v59,0x3727c5ac,v59\n"
"v_cvt_pk_bf16_f32 v59,v59,v59\n"
"v_mfma_f32_16x16x32_bf16 a[112:115],v[240:243],v[204:207],a[112:115]\n"
"v_lshlrev_b32 v59,16,v59\n"
"v_sqrt_f32 v59,v59\n"
"v_mfma_f32_16x16x32_bf16 a[116:119],v[244:247],v[204:207],a[116:119]\n"
"v_cvt_pk_bf16_f32 v58,v58,v58\n"
"v_lshlrev_b32 v58,16,v58\n"
"v_mfma_f32_16x16x32_bf16 a[116:119],v[180:183],v[140:143],a[116:119]\n"
"v_cvt_pk_bf16_f32 v59,v59,v59\n"
"v_lshlrev_b32 v59,16,v59\n"
"v_mfma_f32_16x16x32_bf16 a[120:123],v[184:187],v[140:143],a[120:123]\n"
"buffer_load_dwordx4 v107,s[16:19],s76 offen offset:1024 lds\n"
"v_mfma_f32_16x16x32_bf16 a[120:123],v[248:251],v[204:207],a[120:123]\n"
"v_rcp_f32 v66,v59\n"
"v_mfma_f32_16x16x32_bf16 a[124:127],v[252:255],v[204:207],a[124:127]\n"
"v_mul_f32 v66,0x3e0293ee,v66\n"
"v_mul_f32 v58,v58,v66\n"
"v_mfma_f32_16x16x32_bf16 a[124:127],v[188:191],v[140:143],a[124:127]\n"
"v_fma_f32 v24,v24,v66,-v58\n"
"v_fma_f32 v25,v25,v66,-v58\n"
"v_mfma_f32_16x16x32_bf16 a[128:131],v[160:163],v[144:147],a[128:131]\n"
"v_cvt_pk_bf16_f32 v8,v24,v25\n"
"v_fma_f32 v26,v26,v66,-v58\n"
"v_mfma_f32_16x16x32_bf16 a[128:131],v[224:227],v[208:211],a[128:131]\n"
"buffer_load_dwordx4 v108,s[16:19],s76 offen offset:2048 lds\n"
"v_mfma_f32_16x16x32_bf16 a[132:135],v[228:231],v[208:211],a[132:135]\n"
"v_fma_f32 v27,v27,v66,-v58\n"
"v_cvt_pk_bf16_f32 v9,v26,v27\n"
"v_mfma_f32_16x16x32_bf16 a[132:135],v[164:167],v[144:147],a[132:135]\n"
"v_fma_f32 v28,v28,v66,-v58\n"
"v_fma_f32 v29,v29,v66,-v58\n"
"v_mfma_f32_16x16x32_bf16 a[136:139],v[168:171],v[144:147],a[136:139]\n"
"v_cvt_pk_bf16_f32 v10,v28,v29\n"
"v_fma_f32 v30,v30,v66,-v58\n"
"v_mfma_f32_16x16x32_bf16 a[136:139],v[232:235],v[208:211],a[136:139]\n"
"v_fma_f32 v31,v31,v66,-v58\n"
"v_cvt_pk_bf16_f32 v11,v30,v31\n"
"v_mfma_f32_16x16x32_bf16 a[140:143],v[236:239],v[208:211],a[140:143]\n"
"buffer_load_dwordx4 v109,s[16:19],s76 offen offset:3072 lds\n"
"v_mfma_f32_16x16x32_bf16 a[140:143],v[172:175],v[144:147],a[140:143]\n"
"buffer_store_dwordx4 v[8:11],v71,s[24:27],s54 offen sc1\n"
"v_mfma_f32_16x16x32_bf16 a[144:147],v[176:179],v[144:147],a[144:147]\n"
"s_add_u32 s54,s54,131072\n"
"v_fma_f32 v32,v32,v66,-v58\n"
"v_fma_f32 v33,v33,v66,-v58\n"
"v_mfma_f32_16x16x32_bf16 a[144:147],v[240:243],v[208:211],a[144:147]\n"
"v_cvt_pk_bf16_f32 v12,v32,v33\n"
"v_fma_f32 v34,v34,v66,-v58\n"
"v_mfma_f32_16x16x32_bf16 a[148:151],v[244:247],v[208:211],a[148:151]\n"
"s_mov_b32 s76,262784\n"
"v_fma_f32 v35,v35,v66,-v58\n"
"s_add_u32 m0,s79,69632\n"
"v_mfma_f32_16x16x32_bf16 a[148:151],v[180:183],v[144:147],a[148:151]\n"
"buffer_load_dwordx4 v106,s[16:19],s76 offen offset:0 lds\n"
"v_mfma_f32_16x16x32_bf16 a[152:155],v[184:187],v[144:147],a[152:155]\n"
"v_cvt_pk_bf16_f32 v13,v34,v35\n"
"v_fma_f32 v36,v36,v66,-v58\n"
"v_mfma_f32_16x16x32_bf16 a[152:155],v[248:251],v[208:211],a[152:155]\n"
"v_fma_f32 v37,v37,v66,-v58\n"
"v_cvt_pk_bf16_f32 v14,v36,v37\n"
"v_mfma_f32_16x16x32_bf16 a[156:159],v[252:255],v[208:211],a[156:159]\n"
"v_fma_f32 v38,v38,v66,-v58\n"
"v_fma_f32 v39,v39,v66,-v58\n"
"v_cvt_pk_bf16_f32 v15,v38,v39\n"
"v_mfma_f32_16x16x32_bf16 a[156:159],v[188:191],v[144:147],a[156:159]\n"
"buffer_store_dwordx4 v[12:15],v71,s[24:27],s54 offen sc1\n"
"v_mfma_f32_16x16x32_bf16 a[160:163],v[160:163],v[148:151],a[160:163]\n"
"buffer_load_dwordx4 v107,s[16:19],s76 offen offset:1024 lds\n"
"v_mfma_f32_16x16x32_bf16 a[160:163],v[224:227],v[212:215],a[160:163]\n"
"s_add_u32 s54,s54,131072\n"
"v_fma_f32 v40,v40,v66,-v58\n"
"v_mfma_f32_16x16x32_bf16 a[164:167],v[228:231],v[212:215],a[164:167]\n"
"v_fma_f32 v41,v41,v66,-v58\n"
"v_cvt_pk_bf16_f32 v16,v40,v41\n"
"v_mfma_f32_16x16x32_bf16 a[164:167],v[164:167],v[148:151],a[164:167]\n"
"v_fma_f32 v42,v42,v66,-v58\n"
"v_fma_f32 v43,v43,v66,-v58\n"
"v_mfma_f32_16x16x32_bf16 a[168:171],v[168:171],v[148:151],a[168:171]\n"
"v_cvt_pk_bf16_f32 v17,v42,v43\n"
"v_fma_f32 v44,v44,v66,-v58\n"
"v_mfma_f32_16x16x32_bf16 a[168:171],v[232:235],v[212:215],a[168:171]\n"
"buffer_load_dwordx4 v108,s[16:19],s76 offen offset:2048 lds\n"
"v_mfma_f32_16x16x32_bf16 a[172:175],v[236:239],v[212:215],a[172:175]\n"
"v_fma_f32 v45,v45,v66,-v58\n"
"v_cvt_pk_bf16_f32 v18,v44,v45\n"
"v_mfma_f32_16x16x32_bf16 a[172:175],v[172:175],v[148:151],a[172:175]\n"
"v_fma_f32 v46,v46,v66,-v58\n"
"v_fma_f32 v47,v47,v66,-v58\n"
"v_cvt_pk_bf16_f32 v19,v46,v47\n"
"v_mfma_f32_16x16x32_bf16 a[176:179],v[176:179],v[148:151],a[176:179]\n"
"buffer_store_dwordx4 v[16:19],v71,s[24:27],s54 offen sc1\n"
"v_mfma_f32_16x16x32_bf16 a[176:179],v[240:243],v[212:215],a[176:179]\n"
"s_add_u32 s54,s54,131072\n"
"v_fma_f32 v48,v48,v66,-v58\n"
"v_mfma_f32_16x16x32_bf16 a[180:183],v[244:247],v[212:215],a[180:183]\n"
"buffer_load_dwordx4 v109,s[16:19],s76 offen offset:3072 lds\n"
"v_mfma_f32_16x16x32_bf16 a[180:183],v[180:183],v[148:151],a[180:183]\n"
"s_mov_b32 s87,0\n"
"v_fma_f32 v49,v49,v66,-v58\n"
"v_cvt_pk_bf16_f32 v20,v48,v49\n"
"v_mfma_f32_16x16x32_bf16 a[184:187],v[184:187],v[148:151],a[184:187]\n"
"buffer_load_dword v115,v112,s[40:43],s87 offen\n"
"v_mfma_f32_16x16x32_bf16 a[184:187],v[248:251],v[212:215],a[184:187]\n"
"s_mov_b32 s87,1048576\n"
"v_fma_f32 v50,v50,v66,-v58\n"
"v_fma_f32 v51,v51,v66,-v58\n"
"v_mfma_f32_16x16x32_bf16 a[188:191],v[252:255],v[212:215],a[188:191]\n"
"buffer_load_dword v115,v112,s[40:43],s87 offen\n"
"v_mfma_f32_16x16x32_bf16 a[188:191],v[188:191],v[148:151],a[188:191]\n"
"s_mov_b32 s87,2097152\n"
"s_waitcnt vmcnt(31)\n"
"s_barrier\n"
"v_mfma_f32_16x16x32_bf16 a[192:195],v[224:227],v[216:219],a[192:195]\n"
"ds_read_b128 v[152:155],v80 offset:12288\n"
"buffer_load_dword v115,v112,s[40:43],s87 offen\n"
"v_mfma_f32_16x16x32_bf16 a[224:227],v[224:227],v[220:223],a[224:227]\n"
"ds_read_b128 v[156:159],v80 offset:14336\n"
"v_cvt_pk_bf16_f32 v21,v50,v51\n"
"v_mfma_f32_16x16x32_bf16 a[228:231],v[228:231],v[220:223],a[228:231]\n"
"ds_read_b64_tr_b16 v[160:161],v98 offset:0\n"
"v_fma_f32 v52,v52,v66,-v58\n"
"v_mfma_f32_16x16x32_bf16 a[196:199],v[228:231],v[216:219],a[196:199]\n"
"ds_read_b64_tr_b16 v[162:163],v98 offset:1024\n"
"v_fma_f32 v53,v53,v66,-v58\n"
"v_mfma_f32_16x16x32_bf16 a[200:203],v[232:235],v[216:219],a[200:203]\n"
"ds_read_b64_tr_b16 v[164:165],v99 offset:0\n"
"v_cvt_pk_bf16_f32 v22,v52,v53\n"
"v_mfma_f32_16x16x32_bf16 a[232:235],v[232:235],v[220:223],a[232:235]\n"
"ds_read_b64_tr_b16 v[166:167],v99 offset:1024\n"
"v_fma_f32 v54,v54,v66,-v58\n"
"v_mfma_f32_16x16x32_bf16 a[236:239],v[236:239],v[220:223],a[236:239]\n"
"ds_read_b64_tr_b16 v[168:169],v100 offset:0\n"
"v_fma_f32 v55,v55,v66,-v58\n"
"v_mfma_f32_16x16x32_bf16 a[204:207],v[236:239],v[216:219],a[204:207]\n"
"ds_read_b64_tr_b16 v[170:171],v100 offset:1024\n"
"v_cvt_pk_bf16_f32 v23,v54,v55\n"
"v_mfma_f32_16x16x32_bf16 a[208:211],v[240:243],v[216:219],a[208:211]\n"
"ds_read_b64_tr_b16 v[172:173],v101 offset:0\n"
"buffer_store_dwordx4 v[20:23],v71,s[24:27],s54 offen sc1\n"
"v_mfma_f32_16x16x32_bf16 a[240:243],v[240:243],v[220:223],a[240:243]\n"
"ds_read_b64_tr_b16 v[174:175],v101 offset:1024\n"
"v_mfma_f32_16x16x32_bf16 a[244:247],v[244:247],v[220:223],a[244:247]\n"
"ds_read_b64_tr_b16 v[176:177],v102 offset:0\n"
"v_mfma_f32_16x16x32_bf16 a[212:215],v[244:247],v[216:219],a[212:215]\n"
"ds_read_b64_tr_b16 v[178:179],v102 offset:1024\n"
"v_mfma_f32_16x16x32_bf16 a[216:219],v[248:251],v[216:219],a[216:219]\n"
"ds_read_b64_tr_b16 v[180:181],v103 offset:0\n"
"v_mfma_f32_16x16x32_bf16 a[248:251],v[248:251],v[220:223],a[248:251]\n"
"ds_read_b64_tr_b16 v[182:183],v103 offset:1024\n"
"v_mfma_f32_16x16x32_bf16 a[252:255],v[252:255],v[220:223],a[252:255]\n"
"ds_read_b64_tr_b16 v[184:185],v104 offset:0\n"
"v_mfma_f32_16x16x32_bf16 a[220:223],v[252:255],v[216:219],a[220:223]\n"
"ds_read_b64_tr_b16 v[186:187],v104 offset:1024\n"
"s_waitcnt lgkmcnt(10)\n"
"v_mfma_f32_16x16x32_bf16 a[192:195],v[160:163],v[152:155],a[192:195]\n"
"ds_read_b64_tr_b16 v[188:189],v105 offset:0\n"
"v_mfma_f32_16x16x32_bf16 a[224:227],v[160:163],v[156:159],a[224:227]\n"
"ds_read_b64_tr_b16 v[190:191],v105 offset:1024\n"
"v_mfma_f32_16x16x32_bf16 a[228:231],v[164:167],v[156:159],a[228:231]\n"
"ds_read_b128 v[128:131],v80 offset:0\n"
"v_mfma_f32_16x16x32_bf16 a[196:199],v[164:167],v[152:155],a[196:199]\n"
"ds_read_b128 v[192:195],v81 offset:0\n"
"s_waitcnt lgkmcnt(10)\n"
"v_mfma_f32_16x16x32_bf16 a[200:203],v[168:171],v[152:155],a[200:203]\n"
"ds_read_b64_tr_b16 v[224:225],v98 offset:16384\n"
"v_mfma_f32_16x16x32_bf16 a[232:235],v[168:171],v[156:159],a[232:235]\n"
"ds_read_b64_tr_b16 v[226:227],v98 offset:17408\n"
"v_mfma_f32_16x16x32_bf16 a[236:239],v[172:175],v[156:159],a[236:239]\n"
"ds_read_b64_tr_b16 v[228:229],v99 offset:16384\n"
"v_mfma_f32_16x16x32_bf16 a[204:207],v[172:175],v[152:155],a[204:207]\n"
"ds_read_b64_tr_b16 v[230:231],v99 offset:17408\n"
"s_waitcnt lgkmcnt(10)\n"
"v_mfma_f32_16x16x32_bf16 a[208:211],v[176:179],v[152:155],a[208:211]\n"
"ds_read_b64_tr_b16 v[232:233],v100 offset:16384\n"
"v_mfma_f32_16x16x32_bf16 a[240:243],v[176:179],v[156:159],a[240:243]\n"
"ds_read_b64_tr_b16 v[234:235],v100 offset:17408\n"
"v_mfma_f32_16x16x32_bf16 a[244:247],v[180:183],v[156:159],a[244:247]\n"
"ds_read_b64_tr_b16 v[236:237],v101 offset:16384\n"
"v_mfma_f32_16x16x32_bf16 a[212:215],v[180:183],v[152:155],a[212:215]\n"
"ds_read_b64_tr_b16 v[238:239],v101 offset:17408\n"
"s_waitcnt lgkmcnt(10)\n"
"v_mfma_f32_16x16x32_bf16 a[216:219],v[184:187],v[152:155],a[216:219]\n"
"ds_read_b64_tr_b16 v[240:241],v102 offset:16384\n"
"v_mfma_f32_16x16x32_bf16 a[248:251],v[184:187],v[156:159],a[248:251]\n"
"ds_read_b64_tr_b16 v[242:243],v102 offset:17408\n"
"v_mfma_f32_16x16x32_bf16 a[252:255],v[188:191],v[156:159],a[252:255]\n"
"ds_read_b64_tr_b16 v[244:245],v103 offset:16384\n"
"v_mfma_f32_16x16x32_bf16 a[220:223],v[188:191],v[152:155],a[220:223]\n"
"ds_read_b64_tr_b16 v[246:247],v103 offset:17408\n"
"s_waitcnt lgkmcnt(8)\n"
"v_mfma_f32_16x16x32_bf16 a[0:3],v[160:163],v[128:131],a[0:3]\n"
"ds_read_b64_tr_b16 v[248:249],v104 offset:16384\n"
"v_mfma_f32_16x16x32_bf16 a[0:3],v[224:227],v[192:195],a[0:3]\n"
"ds_read_b64_tr_b16 v[250:251],v104 offset:17408\n"
"v_mfma_f32_16x16x32_bf16 a[4:7],v[228:231],v[192:195],a[4:7]\n"
"ds_read_b64_tr_b16 v[252:253],v105 offset:16384\n"
"v_mfma_f32_16x16x32_bf16 a[4:7],v[164:167],v[128:131],a[4:7]\n"
"ds_read_b64_tr_b16 v[254:255],v105 offset:17408\n"
"s_waitcnt lgkmcnt(8)\n"
"v_mfma_f32_16x16x32_bf16 a[8:11],v[168:171],v[128:131],a[8:11]\n"
"ds_read_b128 v[132:135],v80 offset:2048\n"
"v_mfma_f32_16x16x32_bf16 a[8:11],v[232:235],v[192:195],a[8:11]\n"
"ds_read_b128 v[196:199],v81 offset:2048\n"
"v_mfma_f32_16x16x32_bf16 a[12:15],v[236:239],v[192:195],a[12:15]\n"
"ds_read_b128 v[136:139],v80 offset:4096\n"
"v_mfma_f32_16x16x32_bf16 a[12:15],v[172:175],v[128:131],a[12:15]\n"
"ds_read_b128 v[200:203],v81 offset:4096\n"
"s_waitcnt lgkmcnt(8)\n"
"v_mfma_f32_16x16x32_bf16 a[16:19],v[176:179],v[128:131],a[16:19]\n"
"ds_read_b128 v[140:143],v80 offset:6144\n"
"v_mfma_f32_16x16x32_bf16 a[16:19],v[240:243],v[192:195],a[16:19]\n"
"ds_read_b128 v[204:207],v81 offset:6144\n"
"v_mfma_f32_16x16x32_bf16 a[20:23],v[244:247],v[192:195],a[20:23]\n"
"ds_read_b128 v[144:147],v80 offset:8192\n"
"v_mfma_f32_16x16x32_bf16 a[20:23],v[180:183],v[128:131],a[20:23]\n"
"ds_read_b128 v[208:211],v81 offset:8192\n"
"s_waitcnt lgkmcnt(8)\n"
"v_mfma_f32_16x16x32_bf16 a[24:27],v[184:187],v[128:131],a[24:27]\n"
"ds_read_b128 v[148:151],v80 offset:10240\n"
"v_mfma_f32_16x16x32_bf16 a[24:27],v[248:251],v[192:195],a[24:27]\n"
"ds_read_b128 v[212:215],v81 offset:10240\n"
"v_mfma_f32_16x16x32_bf16 a[28:31],v[252:255],v[192:195],a[28:31]\n"
"ds_read_b128 v[216:219],v81 offset:12288\n"
"v_mfma_f32_16x16x32_bf16 a[28:31],v[188:191],v[128:131],a[28:31]\n"
"ds_read_b128 v[220:223],v81 offset:14336\n"
"s_waitcnt lgkmcnt(10)\n"
"v_mfma_f32_16x16x32_bf16 a[32:35],v[160:163],v[132:135],a[32:35]\n"
"v_mfma_f32_16x16x32_bf16 a[32:35],v[224:227],v[196:199],a[32:35]\n"
"v_mfma_f32_16x16x32_bf16 a[36:39],v[228:231],v[196:199],a[36:39]\n"
"v_mfma_f32_16x16x32_bf16 a[36:39],v[164:167],v[132:135],a[36:39]\n"
"v_mfma_f32_16x16x32_bf16 a[40:43],v[168:171],v[132:135],a[40:43]\n"
"s_waitcnt lgkmcnt(0)\n"
"s_barrier\n"
"v_mfma_f32_16x16x32_bf16 a[40:43],v[232:235],v[196:199],a[40:43]\n"
"s_mov_b32 s77,2621440\n"
"s_add_u32 m0,s78,98304\n"
"v_mfma_f32_16x16x32_bf16 a[44:47],v[236:239],v[196:199],a[44:47]\n"
"buffer_load_dwordx4 v110,s[20:23],s77 offen offset:0 lds\n"
"v_mfma_f32_16x16x32_bf16 a[44:47],v[172:175],v[132:135],a[44:47]\n"
"v_mfma_f32_16x16x32_bf16 a[48:51],v[176:179],v[132:135],a[48:51]\n"
"v_mfma_f32_16x16x32_bf16 a[48:51],v[240:243],v[196:199],a[48:51]\n"
"v_mfma_f32_16x16x32_bf16 a[52:55],v[244:247],v[196:199],a[52:55]\n"
"buffer_load_dwordx4 v111,s[20:23],s77 offen offset:1024 lds\n"
"v_mfma_f32_16x16x32_bf16 a[52:55],v[180:183],v[132:135],a[52:55]\n"
"v_mfma_f32_16x16x32_bf16 a[56:59],v[184:187],v[132:135],a[56:59]\n"
"v_mfma_f32_16x16x32_bf16 a[56:59],v[248:251],v[196:199],a[56:59]\n"
"v_mfma_f32_16x16x32_bf16 a[60:63],v[252:255],v[196:199],a[60:63]\n"
"buffer_load_dwordx4 v113,s[20:23],s77 offen offset:2048 lds\n"
"v_mfma_f32_16x16x32_bf16 a[60:63],v[188:191],v[132:135],a[60:63]\n"
"v_mfma_f32_16x16x32_bf16 a[64:67],v[160:163],v[136:139],a[64:67]\n"
"v_mfma_f32_16x16x32_bf16 a[64:67],v[224:227],v[200:203],a[64:67]\n"
"v_mfma_f32_16x16x32_bf16 a[68:71],v[228:231],v[200:203],a[68:71]\n"
"buffer_load_dwordx4 v114,s[20:23],s77 offen offset:3072 lds\n"
"v_mfma_f32_16x16x32_bf16 a[68:71],v[164:167],v[136:139],a[68:71]\n"
"v_mfma_f32_16x16x32_bf16 a[72:75],v[168:171],v[136:139],a[72:75]\n"
"v_mfma_f32_16x16x32_bf16 a[72:75],v[232:235],v[200:203],a[72:75]\n"
"s_mov_b32 s77,2883584\n"
"s_add_u32 m0,s78,114688\n"
"v_mfma_f32_16x16x32_bf16 a[76:79],v[236:239],v[200:203],a[76:79]\n"
"buffer_load_dwordx4 v110,s[20:23],s77 offen offset:0 lds\n"
"v_mfma_f32_16x16x32_bf16 a[76:79],v[172:175],v[136:139],a[76:79]\n"
"v_mfma_f32_16x16x32_bf16 a[80:83],v[176:179],v[136:139],a[80:83]\n"
"v_mfma_f32_16x16x32_bf16 a[80:83],v[240:243],v[200:203],a[80:83]\n"
"v_mfma_f32_16x16x32_bf16 a[84:87],v[244:247],v[200:203],a[84:87]\n"
"buffer_load_dwordx4 v111,s[20:23],s77 offen offset:1024 lds\n"
"v_mfma_f32_16x16x32_bf16 a[84:87],v[180:183],v[136:139],a[84:87]\n"
"v_mfma_f32_16x16x32_bf16 a[88:91],v[184:187],v[136:139],a[88:91]\n"
"v_mfma_f32_16x16x32_bf16 a[88:91],v[248:251],v[200:203],a[88:91]\n"
"v_mfma_f32_16x16x32_bf16 a[92:95],v[252:255],v[200:203],a[92:95]\n"
"buffer_load_dwordx4 v113,s[20:23],s77 offen offset:2048 lds\n"
"v_mfma_f32_16x16x32_bf16 a[92:95],v[188:191],v[136:139],a[92:95]\n"
"v_mfma_f32_16x16x32_bf16 a[96:99],v[160:163],v[140:143],a[96:99]\n"
"v_mfma_f32_16x16x32_bf16 a[96:99],v[224:227],v[204:207],a[96:99]\n"
"v_mfma_f32_16x16x32_bf16 a[100:103],v[228:231],v[204:207],a[100:103]\n"
"buffer_load_dwordx4 v114,s[20:23],s77 offen offset:3072 lds\n"
"v_mfma_f32_16x16x32_bf16 a[100:103],v[164:167],v[140:143],a[100:103]\n"
"v_mfma_f32_16x16x32_bf16 a[104:107],v[168:171],v[140:143],a[104:107]\n"
"v_mfma_f32_16x16x32_bf16 a[104:107],v[232:235],v[204:207],a[104:107]\n"
"v_mfma_f32_16x16x32_bf16 a[108:111],v[236:239],v[204:207],a[108:111]\n"
"s_mov_b32 s76,768\n"
"s_add_u32 m0,s79,0\n"
"v_mfma_f32_16x16x32_bf16 a[108:111],v[172:175],v[140:143],a[108:111]\n"
"buffer_load_dwordx4 v106,s[16:19],s76 offen offset:0 lds\n"
"v_mfma_f32_16x16x32_bf16 a[112:115],v[176:179],v[140:143],a[112:115]\n"
"v_mfma_f32_16x16x32_bf16 a[112:115],v[240:243],v[204:207],a[112:115]\n"
"v_mfma_f32_16x16x32_bf16 a[116:119],v[244:247],v[204:207],a[116:119]\n"
"v_mfma_f32_16x16x32_bf16 a[116:119],v[180:183],v[140:143],a[116:119]\n"
"v_mfma_f32_16x16x32_bf16 a[120:123],v[184:187],v[140:143],a[120:123]\n"
"buffer_load_dwordx4 v107,s[16:19],s76 offen offset:1024 lds\n"
"v_mfma_f32_16x16x32_bf16 a[120:123],v[248:251],v[204:207],a[120:123]\n"
"v_mfma_f32_16x16x32_bf16 a[124:127],v[252:255],v[204:207],a[124:127]\n"
"v_mfma_f32_16x16x32_bf16 a[124:127],v[188:191],v[140:143],a[124:127]\n"
"v_mfma_f32_16x16x32_bf16 a[128:131],v[160:163],v[144:147],a[128:131]\n"
"v_mfma_f32_16x16x32_bf16 a[128:131],v[224:227],v[208:211],a[128:131]\n"
"buffer_load_dwordx4 v108,s[16:19],s76 offen offset:2048 lds\n"
"v_mfma_f32_16x16x32_bf16 a[132:135],v[228:231],v[208:211],a[132:135]\n"
"v_mfma_f32_16x16x32_bf16 a[132:135],v[164:167],v[144:147],a[132:135]\n"
"v_mfma_f32_16x16x32_bf16 a[136:139],v[168:171],v[144:147],a[136:139]\n"
"v_mfma_f32_16x16x32_bf16 a[136:139],v[232:235],v[208:211],a[136:139]\n"
"v_mfma_f32_16x16x32_bf16 a[140:143],v[236:239],v[208:211],a[140:143]\n"
"buffer_load_dwordx4 v109,s[16:19],s76 offen offset:3072 lds\n"
"v_mfma_f32_16x16x32_bf16 a[140:143],v[172:175],v[144:147],a[140:143]\n"
"v_mfma_f32_16x16x32_bf16 a[144:147],v[176:179],v[144:147],a[144:147]\n"
"v_mfma_f32_16x16x32_bf16 a[144:147],v[240:243],v[208:211],a[144:147]\n"
"v_mfma_f32_16x16x32_bf16 a[148:151],v[244:247],v[208:211],a[148:151]\n"
"s_mov_b32 s76,262912\n"
"s_add_u32 m0,s79,4096\n"
"v_mfma_f32_16x16x32_bf16 a[148:151],v[180:183],v[144:147],a[148:151]\n"
"buffer_load_dwordx4 v106,s[16:19],s76 offen offset:0 lds\n"
"v_mfma_f32_16x16x32_bf16 a[152:155],v[184:187],v[144:147],a[152:155]\n"
"v_mfma_f32_16x16x32_bf16 a[152:155],v[248:251],v[208:211],a[152:155]\n"
"v_mfma_f32_16x16x32_bf16 a[156:159],v[252:255],v[208:211],a[156:159]\n"
"v_mfma_f32_16x16x32_bf16 a[156:159],v[188:191],v[144:147],a[156:159]\n"
"v_mfma_f32_16x16x32_bf16 a[160:163],v[160:163],v[148:151],a[160:163]\n"
"buffer_load_dwordx4 v107,s[16:19],s76 offen offset:1024 lds\n"
"v_mfma_f32_16x16x32_bf16 a[160:163],v[224:227],v[212:215],a[160:163]\n"
"v_mfma_f32_16x16x32_bf16 a[164:167],v[228:231],v[212:215],a[164:167]\n"
"v_mfma_f32_16x16x32_bf16 a[164:167],v[164:167],v[148:151],a[164:167]\n"
"v_mfma_f32_16x16x32_bf16 a[168:171],v[168:171],v[148:151],a[168:171]\n"
"v_mfma_f32_16x16x32_bf16 a[168:171],v[232:235],v[212:215],a[168:171]\n"
"buffer_load_dwordx4 v108,s[16:19],s76 offen offset:2048 lds\n"
"v_mfma_f32_16x16x32_bf16 a[172:175],v[236:239],v[212:215],a[172:175]\n"
"v_mfma_f32_16x16x32_bf16 a[172:175],v[172:175],v[148:151],a[172:175]\n"
"v_mfma_f32_16x16x32_bf16 a[176:179],v[176:179],v[148:151],a[176:179]\n"
"v_mfma_f32_16x16x32_bf16 a[176:179],v[240:243],v[212:215],a[176:179]\n"
"v_mfma_f32_16x16x32_bf16 a[180:183],v[244:247],v[212:215],a[180:183]\n"
"buffer_load_dwordx4 v109,s[16:19],s76 offen offset:3072 lds\n"
"v_mfma_f32_16x16x32_bf16 a[180:183],v[180:183],v[148:151],a[180:183]\n"
"s_mov_b32 s87,3145728\n"
"v_mfma_f32_16x16x32_bf16 a[184:187],v[184:187],v[148:151],a[184:187]\n"
"buffer_load_dword v115,v112,s[40:43],s87 offen\n"
"v_mfma_f32_16x16x32_bf16 a[184:187],v[248:251],v[212:215],a[184:187]\n"
"s_mov_b32 s87,4194304\n"
"v_mfma_f32_16x16x32_bf16 a[188:191],v[252:255],v[212:215],a[188:191]\n"
"buffer_load_dword v115,v112,s[40:43],s87 offen\n"
"v_mfma_f32_16x16x32_bf16 a[188:191],v[188:191],v[148:151],a[188:191]\n"
"s_mov_b32 s87,5242880\n"
"s_waitcnt vmcnt(33)\n"
"s_barrier\n"
"v_mfma_f32_16x16x32_bf16 a[192:195],v[224:227],v[216:219],a[192:195]\n"
"ds_read_b128 v[152:155],v80 offset:45056\n"
"buffer_load_dword v115,v112,s[40:43],s87 offen\n"
"v_mfma_f32_16x16x32_bf16 a[224:227],v[224:227],v[220:223],a[224:227]\n"
"ds_read_b128 v[156:159],v80 offset:47104\n"
"v_mfma_f32_16x16x32_bf16 a[228:231],v[228:231],v[220:223],a[228:231]\n"
"ds_read_b64_tr_b16 v[160:161],v98 offset:49152\n"
"v_mfma_f32_16x16x32_bf16 a[196:199],v[228:231],v[216:219],a[196:199]\n"
"ds_read_b64_tr_b16 v[162:163],v98 offset:50176\n"
"v_mfma_f32_16x16x32_bf16 a[200:203],v[232:235],v[216:219],a[200:203]\n"
"ds_read_b64_tr_b16 v[164:165],v99 offset:49152\n"
"v_mfma_f32_16x16x32_bf16 a[232:235],v[232:235],v[220:223],a[232:235]\n"
"ds_read_b64_tr_b16 v[166:167],v99 offset:50176\n"
"v_mfma_f32_16x16x32_bf16 a[236:239],v[236:239],v[220:223],a[236:239]\n"
"ds_read_b64_tr_b16 v[168:169],v100 offset:49152\n"
"v_mfma_f32_16x16x32_bf16 a[204:207],v[236:239],v[216:219],a[204:207]\n"
"ds_read_b64_tr_b16 v[170:171],v100 offset:50176\n"
"v_mfma_f32_16x16x32_bf16 a[208:211],v[240:243],v[216:219],a[208:211]\n"
"ds_read_b64_tr_b16 v[172:173],v101 offset:49152\n"
"v_mfma_f32_16x16x32_bf16 a[240:243],v[240:243],v[220:223],a[240:243]\n"
"ds_read_b64_tr_b16 v[174:175],v101 offset:50176\n"
"v_mfma_f32_16x16x32_bf16 a[244:247],v[244:247],v[220:223],a[244:247]\n"
"ds_read_b64_tr_b16 v[176:177],v102 offset:49152\n"
"v_mfma_f32_16x16x32_bf16 a[212:215],v[244:247],v[216:219],a[212:215]\n"
"ds_read_b64_tr_b16 v[178:179],v102 offset:50176\n"
"v_mfma_f32_16x16x32_bf16 a[216:219],v[248:251],v[216:219],a[216:219]\n"
"ds_read_b64_tr_b16 v[180:181],v103 offset:49152\n"
"v_mfma_f32_16x16x32_bf16 a[248:251],v[248:251],v[220:223],a[248:251]\n"
"ds_read_b64_tr_b16 v[182:183],v103 offset:50176\n"
"v_mfma_f32_16x16x32_bf16 a[252:255],v[252:255],v[220:223],a[252:255]\n"
"ds_read_b64_tr_b16 v[184:185],v104 offset:49152\n"
"ds_read_b64_tr_b16 v[186:187],v104 offset:50176\n"
"v_mfma_f32_16x16x32_bf16 a[220:223],v[252:255],v[216:219],a[220:223]\n"
"s_waitcnt vmcnt(19)\n"
"s_waitcnt lgkmcnt(10)\n"
"v_mfma_f32_16x16x32_bf16 a[192:195],v[160:163],v[152:155],a[192:195]\n"
"ds_read_b64_tr_b16 v[188:189],v105 offset:49152\n"
"ds_read_b64_tr_b16 v[190:191],v105 offset:50176\n"
"v_mfma_f32_16x16x32_bf16 a[224:227],v[160:163],v[156:159],a[224:227]\n"
"buffer_store_dwordx2 v[124:125],off,s[24:27],s57 sc1\n"
"v_mfma_f32_16x16x32_bf16 a[228:231],v[164:167],v[156:159],a[228:231]\n"
"ds_read_b128 v[128:131],v80 offset:32768\n"
"v_mfma_f32_16x16x32_bf16 a[196:199],v[164:167],v[152:155],a[196:199]\n"
"ds_read_b128 v[192:195],v81 offset:32768\n"
"s_waitcnt lgkmcnt(10)\n"
"v_mfma_f32_16x16x32_bf16 a[200:203],v[168:171],v[152:155],a[200:203]\n"
"ds_read_b64_tr_b16 v[224:225],v98 offset:32768\n"
"v_mfma_f32_16x16x32_bf16 a[232:235],v[168:171],v[156:159],a[232:235]\n"
"ds_read_b64_tr_b16 v[226:227],v98 offset:33792\n"
"v_mfma_f32_16x16x32_bf16 a[236:239],v[172:175],v[156:159],a[236:239]\n"
"ds_read_b64_tr_b16 v[228:229],v99 offset:32768\n"
"v_mfma_f32_16x16x32_bf16 a[204:207],v[172:175],v[152:155],a[204:207]\n"
"ds_read_b64_tr_b16 v[230:231],v99 offset:33792\n"
"s_waitcnt lgkmcnt(10)\n"
"v_mfma_f32_16x16x32_bf16 a[208:211],v[176:179],v[152:155],a[208:211]\n"
"ds_read_b64_tr_b16 v[232:233],v100 offset:32768\n"
"v_mfma_f32_16x16x32_bf16 a[240:243],v[176:179],v[156:159],a[240:243]\n"
"ds_read_b64_tr_b16 v[234:235],v100 offset:33792\n"
"v_mfma_f32_16x16x32_bf16 a[244:247],v[180:183],v[156:159],a[244:247]\n"
"ds_read_b64_tr_b16 v[236:237],v101 offset:32768\n"
"v_mfma_f32_16x16x32_bf16 a[212:215],v[180:183],v[152:155],a[212:215]\n"
"ds_read_b64_tr_b16 v[238:239],v101 offset:33792\n"
"s_waitcnt lgkmcnt(10)\n"
"v_mfma_f32_16x16x32_bf16 a[216:219],v[184:187],v[152:155],a[216:219]\n"
"ds_read_b64_tr_b16 v[240:241],v102 offset:32768\n"
"v_mfma_f32_16x16x32_bf16 a[248:251],v[184:187],v[156:159],a[248:251]\n"
"ds_read_b64_tr_b16 v[242:243],v102 offset:33792\n"
"v_mfma_f32_16x16x32_bf16 a[252:255],v[188:191],v[156:159],a[252:255]\n"
"ds_read_b64_tr_b16 v[244:245],v103 offset:32768\n"
"v_mfma_f32_16x16x32_bf16 a[220:223],v[188:191],v[152:155],a[220:223]\n"
"ds_read_b64_tr_b16 v[246:247],v103 offset:33792\n"
"s_waitcnt lgkmcnt(8)\n"
"v_mfma_f32_16x16x32_bf16 a[0:3],v[160:163],v[128:131],a[0:3]\n"
"ds_read_b64_tr_b16 v[248:249],v104 offset:32768\n"
"v_mfma_f32_16x16x32_bf16 a[0:3],v[224:227],v[192:195],a[0:3]\n"
"ds_read_b64_tr_b16 v[250:251],v104 offset:33792\n"
"v_mfma_f32_16x16x32_bf16 a[4:7],v[228:231],v[192:195],a[4:7]\n"
"ds_read_b64_tr_b16 v[252:253],v105 offset:32768\n"
"v_mfma_f32_16x16x32_bf16 a[4:7],v[164:167],v[128:131],a[4:7]\n"
"ds_read_b64_tr_b16 v[254:255],v105 offset:33792\n"
"s_waitcnt lgkmcnt(8)\n"
"v_mfma_f32_16x16x32_bf16 a[8:11],v[168:171],v[128:131],a[8:11]\n"
"ds_read_b128 v[132:135],v80 offset:34816\n"
"v_mfma_f32_16x16x32_bf16 a[8:11],v[232:235],v[192:195],a[8:11]\n"
"ds_read_b128 v[196:199],v81 offset:34816\n"
"v_mfma_f32_16x16x32_bf16 a[12:15],v[236:239],v[192:195],a[12:15]\n"
"ds_read_b128 v[136:139],v80 offset:36864\n"
"v_mfma_f32_16x16x32_bf16 a[12:15],v[172:175],v[128:131],a[12:15]\n"
"ds_read_b128 v[200:203],v81 offset:36864\n"
"s_waitcnt lgkmcnt(8)\n"
"v_mfma_f32_16x16x32_bf16 a[16:19],v[176:179],v[128:131],a[16:19]\n"
"ds_read_b128 v[140:143],v80 offset:38912\n"
"v_mfma_f32_16x16x32_bf16 a[16:19],v[240:243],v[192:195],a[16:19]\n"
"ds_read_b128 v[204:207],v81 offset:38912\n"
"v_mfma_f32_16x16x32_bf16 a[20:23],v[244:247],v[192:195],a[20:23]\n"
"ds_read_b128 v[144:147],v80 offset:40960\n"
"v_mfma_f32_16x16x32_bf16 a[20:23],v[180:183],v[128:131],a[20:23]\n"
"ds_read_b128 v[208:211],v81 offset:40960\n"
"s_waitcnt lgkmcnt(8)\n"
"v_mfma_f32_16x16x32_bf16 a[24:27],v[184:187],v[128:131],a[24:27]\n"
"ds_read_b128 v[148:151],v80 offset:43008\n"
"v_mfma_f32_16x16x32_bf16 a[24:27],v[248:251],v[192:195],a[24:27]\n"
"ds_read_b128 v[212:215],v81 offset:43008\n"
"v_mfma_f32_16x16x32_bf16 a[28:31],v[252:255],v[192:195],a[28:31]\n"
"ds_read_b128 v[216:219],v81 offset:45056\n"
"v_mfma_f32_16x16x32_bf16 a[28:31],v[188:191],v[128:131],a[28:31]\n"
"ds_read_b128 v[220:223],v81 offset:47104\n"
"s_waitcnt lgkmcnt(10)\n"
"v_mfma_f32_16x16x32_bf16 a[32:35],v[160:163],v[132:135],a[32:35]\n"
"v_mfma_f32_16x16x32_bf16 a[32:35],v[224:227],v[196:199],a[32:35]\n"
"v_mfma_f32_16x16x32_bf16 a[36:39],v[228:231],v[196:199],a[36:39]\n"
"v_mfma_f32_16x16x32_bf16 a[36:39],v[164:167],v[132:135],a[36:39]\n"
"v_mfma_f32_16x16x32_bf16 a[40:43],v[168:171],v[132:135],a[40:43]\n"
"s_waitcnt lgkmcnt(0)\n"
"s_barrier\n"
"v_mfma_f32_16x16x32_bf16 a[40:43],v[232:235],v[196:199],a[40:43]\n"
"s_mov_b32 s77,3145728\n"
"s_add_u32 m0,s78,147456\n"
"v_mfma_f32_16x16x32_bf16 a[44:47],v[236:239],v[196:199],a[44:47]\n"
"buffer_load_dwordx4 v110,s[20:23],s77 offen offset:0 lds\n"
"v_mfma_f32_16x16x32_bf16 a[44:47],v[172:175],v[132:135],a[44:47]\n"
"v_mfma_f32_16x16x32_bf16 a[48:51],v[176:179],v[132:135],a[48:51]\n"
"v_mfma_f32_16x16x32_bf16 a[48:51],v[240:243],v[196:199],a[48:51]\n"
"v_mfma_f32_16x16x32_bf16 a[52:55],v[244:247],v[196:199],a[52:55]\n"
"buffer_load_dwordx4 v111,s[20:23],s77 offen offset:1024 lds\n"
"v_mfma_f32_16x16x32_bf16 a[52:55],v[180:183],v[132:135],a[52:55]\n"
"v_mfma_f32_16x16x32_bf16 a[56:59],v[184:187],v[132:135],a[56:59]\n"
"v_mfma_f32_16x16x32_bf16 a[56:59],v[248:251],v[196:199],a[56:59]\n"
"v_mfma_f32_16x16x32_bf16 a[60:63],v[252:255],v[196:199],a[60:63]\n"
"buffer_load_dwordx4 v113,s[20:23],s77 offen offset:2048 lds\n"
"v_mfma_f32_16x16x32_bf16 a[60:63],v[188:191],v[132:135],a[60:63]\n"
"v_mfma_f32_16x16x32_bf16 a[64:67],v[160:163],v[136:139],a[64:67]\n"
"v_mfma_f32_16x16x32_bf16 a[64:67],v[224:227],v[200:203],a[64:67]\n"
"v_mfma_f32_16x16x32_bf16 a[68:71],v[228:231],v[200:203],a[68:71]\n"
"buffer_load_dwordx4 v114,s[20:23],s77 offen offset:3072 lds\n"
"v_mfma_f32_16x16x32_bf16 a[68:71],v[164:167],v[136:139],a[68:71]\n"
"v_mfma_f32_16x16x32_bf16 a[72:75],v[168:171],v[136:139],a[72:75]\n"
"v_mfma_f32_16x16x32_bf16 a[72:75],v[232:235],v[200:203],a[72:75]\n"
"s_mov_b32 s77,3407872\n"
"s_add_u32 m0,s78,131072\n"
"v_mfma_f32_16x16x32_bf16 a[76:79],v[236:239],v[200:203],a[76:79]\n"
"buffer_load_dwordx4 v110,s[20:23],s77 offen offset:0 lds\n"
"v_mfma_f32_16x16x32_bf16 a[76:79],v[172:175],v[136:139],a[76:79]\n"
"v_mfma_f32_16x16x32_bf16 a[80:83],v[176:179],v[136:139],a[80:83]\n"
"v_mfma_f32_16x16x32_bf16 a[80:83],v[240:243],v[200:203],a[80:83]\n"
"v_mfma_f32_16x16x32_bf16 a[84:87],v[244:247],v[200:203],a[84:87]\n"
"buffer_load_dwordx4 v111,s[20:23],s77 offen offset:1024 lds\n"
"v_mfma_f32_16x16x32_bf16 a[84:87],v[180:183],v[136:139],a[84:87]\n"
"v_mfma_f32_16x16x32_bf16 a[88:91],v[184:187],v[136:139],a[88:91]\n"
"v_mfma_f32_16x16x32_bf16 a[88:91],v[248:251],v[200:203],a[88:91]\n"
"v_mfma_f32_16x16x32_bf16 a[92:95],v[252:255],v[200:203],a[92:95]\n"
"buffer_load_dwordx4 v113,s[20:23],s77 offen offset:2048 lds\n"
"v_mfma_f32_16x16x32_bf16 a[92:95],v[188:191],v[136:139],a[92:95]\n"
"v_mfma_f32_16x16x32_bf16 a[96:99],v[160:163],v[140:143],a[96:99]\n"
"v_mfma_f32_16x16x32_bf16 a[96:99],v[224:227],v[204:207],a[96:99]\n"
"v_mfma_f32_16x16x32_bf16 a[100:103],v[228:231],v[204:207],a[100:103]\n"
"buffer_load_dwordx4 v114,s[20:23],s77 offen offset:3072 lds\n"
"v_mfma_f32_16x16x32_bf16 a[100:103],v[164:167],v[140:143],a[100:103]\n"
"v_mfma_f32_16x16x32_bf16 a[104:107],v[168:171],v[140:143],a[104:107]\n"
"v_mfma_f32_16x16x32_bf16 a[104:107],v[232:235],v[204:207],a[104:107]\n"
"v_mfma_f32_16x16x32_bf16 a[108:111],v[236:239],v[204:207],a[108:111]\n"
"s_mov_b32 s76,896\n"
"s_add_u32 m0,s79,32768\n"
"v_mfma_f32_16x16x32_bf16 a[108:111],v[172:175],v[140:143],a[108:111]\n"
"buffer_load_dwordx4 v106,s[16:19],s76 offen offset:0 lds\n"
"v_mfma_f32_16x16x32_bf16 a[112:115],v[176:179],v[140:143],a[112:115]\n"
"v_mfma_f32_16x16x32_bf16 a[112:115],v[240:243],v[204:207],a[112:115]\n"
"v_mfma_f32_16x16x32_bf16 a[116:119],v[244:247],v[204:207],a[116:119]\n"
"v_mfma_f32_16x16x32_bf16 a[116:119],v[180:183],v[140:143],a[116:119]\n"
"v_mfma_f32_16x16x32_bf16 a[120:123],v[184:187],v[140:143],a[120:123]\n"
"buffer_load_dwordx4 v107,s[16:19],s76 offen offset:1024 lds\n"
"v_mfma_f32_16x16x32_bf16 a[120:123],v[248:251],v[204:207],a[120:123]\n"
"v_mfma_f32_16x16x32_bf16 a[124:127],v[252:255],v[204:207],a[124:127]\n"
"v_mfma_f32_16x16x32_bf16 a[124:127],v[188:191],v[140:143],a[124:127]\n"
"v_mfma_f32_16x16x32_bf16 a[128:131],v[160:163],v[144:147],a[128:131]\n"
"v_mfma_f32_16x16x32_bf16 a[128:131],v[224:227],v[208:211],a[128:131]\n"
"buffer_load_dwordx4 v108,s[16:19],s76 offen offset:2048 lds\n"
"v_mfma_f32_16x16x32_bf16 a[132:135],v[228:231],v[208:211],a[132:135]\n"
"v_mfma_f32_16x16x32_bf16 a[132:135],v[164:167],v[144:147],a[132:135]\n"
"v_mfma_f32_16x16x32_bf16 a[136:139],v[168:171],v[144:147],a[136:139]\n"
"v_mfma_f32_16x16x32_bf16 a[136:139],v[232:235],v[208:211],a[136:139]\n"
"v_mfma_f32_16x16x32_bf16 a[140:143],v[236:239],v[208:211],a[140:143]\n"
"buffer_load_dwordx4 v109,s[16:19],s76 offen offset:3072 lds\n"
"v_mfma_f32_16x16x32_bf16 a[140:143],v[172:175],v[144:147],a[140:143]\n"
"v_mfma_f32_16x16x32_bf16 a[144:147],v[176:179],v[144:147],a[144:147]\n"
"v_mfma_f32_16x16x32_bf16 a[144:147],v[240:243],v[208:211],a[144:147]\n"
"v_mfma_f32_16x16x32_bf16 a[148:151],v[244:247],v[208:211],a[148:151]\n"
"s_mov_b32 s76,263040\n"
"s_add_u32 m0,s79,36864\n"
"v_mfma_f32_16x16x32_bf16 a[148:151],v[180:183],v[144:147],a[148:151]\n"
"buffer_load_dwordx4 v106,s[16:19],s76 offen offset:0 lds\n"
"v_mfma_f32_16x16x32_bf16 a[152:155],v[184:187],v[144:147],a[152:155]\n"
"v_mfma_f32_16x16x32_bf16 a[152:155],v[248:251],v[208:211],a[152:155]\n"
"v_mfma_f32_16x16x32_bf16 a[156:159],v[252:255],v[208:211],a[156:159]\n"
"v_mfma_f32_16x16x32_bf16 a[156:159],v[188:191],v[144:147],a[156:159]\n"
"v_mfma_f32_16x16x32_bf16 a[160:163],v[160:163],v[148:151],a[160:163]\n"
"buffer_load_dwordx4 v107,s[16:19],s76 offen offset:1024 lds\n"
"v_mfma_f32_16x16x32_bf16 a[160:163],v[224:227],v[212:215],a[160:163]\n"
"v_mfma_f32_16x16x32_bf16 a[164:167],v[228:231],v[212:215],a[164:167]\n"
"v_mfma_f32_16x16x32_bf16 a[164:167],v[164:167],v[148:151],a[164:167]\n"
"v_mfma_f32_16x16x32_bf16 a[168:171],v[168:171],v[148:151],a[168:171]\n"
"v_mfma_f32_16x16x32_bf16 a[168:171],v[232:235],v[212:215],a[168:171]\n"
"buffer_load_dwordx4 v108,s[16:19],s76 offen offset:2048 lds\n"
"v_mfma_f32_16x16x32_bf16 a[172:175],v[236:239],v[212:215],a[172:175]\n"
"v_mfma_f32_16x16x32_bf16 a[172:175],v[172:175],v[148:151],a[172:175]\n"
"v_mfma_f32_16x16x32_bf16 a[176:179],v[176:179],v[148:151],a[176:179]\n"
"v_mfma_f32_16x16x32_bf16 a[176:179],v[240:243],v[212:215],a[176:179]\n"
"v_mfma_f32_16x16x32_bf16 a[180:183],v[244:247],v[212:215],a[180:183]\n"
"buffer_load_dwordx4 v109,s[16:19],s76 offen offset:3072 lds\n"
"v_mfma_f32_16x16x32_bf16 a[180:183],v[180:183],v[148:151],a[180:183]\n"
"s_mov_b32 s87,6291456\n"
"v_mfma_f32_16x16x32_bf16 a[184:187],v[184:187],v[148:151],a[184:187]\n"
"buffer_load_dword v115,v112,s[40:43],s87 offen\n"
"v_mfma_f32_16x16x32_bf16 a[184:187],v[248:251],v[212:215],a[184:187]\n"
"s_mov_b32 s87,7340032\n"
"v_mfma_f32_16x16x32_bf16 a[188:191],v[252:255],v[212:215],a[188:191]\n"
"buffer_load_dword v115,v112,s[40:43],s87 offen\n"
"v_mfma_f32_16x16x32_bf16 a[188:191],v[188:191],v[148:151],a[188:191]\n"
"s_waitcnt vmcnt(30)\n"
"s_barrier\n"
"v_mfma_f32_16x16x32_bf16 a[192:195],v[224:227],v[216:219],a[192:195]\n"
"ds_read_b128 v[152:155],v82 offset:12288\n"
"v_mfma_f32_16x16x32_bf16 a[224:227],v[224:227],v[220:223],a[224:227]\n"
"ds_read_b128 v[156:159],v82 offset:14336\n"
"v_mfma_f32_16x16x32_bf16 a[228:231],v[228:231],v[220:223],a[228:231]\n"
"ds_read_b64_tr_b16 v[160:161],v98 offset:0\n"
"v_mfma_f32_16x16x32_bf16 a[196:199],v[228:231],v[216:219],a[196:199]\n"
"ds_read_b64_tr_b16 v[162:163],v98 offset:1024\n"
"v_mfma_f32_16x16x32_bf16 a[200:203],v[232:235],v[216:219],a[200:203]\n"
"ds_read_b64_tr_b16 v[164:165],v99 offset:0\n"
"v_mfma_f32_16x16x32_bf16 a[232:235],v[232:235],v[220:223],a[232:235]\n"
"ds_read_b64_tr_b16 v[166:167],v99 offset:1024\n"
"v_mfma_f32_16x16x32_bf16 a[236:239],v[236:239],v[220:223],a[236:239]\n"
"ds_read_b64_tr_b16 v[168:169],v100 offset:0\n"
"v_mfma_f32_16x16x32_bf16 a[204:207],v[236:239],v[216:219],a[204:207]\n"
"ds_read_b64_tr_b16 v[170:171],v100 offset:1024\n"
"v_mfma_f32_16x16x32_bf16 a[208:211],v[240:243],v[216:219],a[208:211]\n"
"ds_read_b64_tr_b16 v[172:173],v101 offset:0\n"
"v_mfma_f32_16x16x32_bf16 a[240:243],v[240:243],v[220:223],a[240:243]\n"
"ds_read_b64_tr_b16 v[174:175],v101 offset:1024\n"
"v_mfma_f32_16x16x32_bf16 a[244:247],v[244:247],v[220:223],a[244:247]\n"
"ds_read_b64_tr_b16 v[176:177],v102 offset:0\n"
"v_mfma_f32_16x16x32_bf16 a[212:215],v[244:247],v[216:219],a[212:215]\n"
"ds_read_b64_tr_b16 v[178:179],v102 offset:1024\n"
"v_mfma_f32_16x16x32_bf16 a[216:219],v[248:251],v[216:219],a[216:219]\n"
"ds_read_b64_tr_b16 v[180:181],v103 offset:0\n"
"v_mfma_f32_16x16x32_bf16 a[248:251],v[248:251],v[220:223],a[248:251]\n"
"ds_read_b64_tr_b16 v[182:183],v103 offset:1024\n"
"v_mfma_f32_16x16x32_bf16 a[252:255],v[252:255],v[220:223],a[252:255]\n"
"ds_read_b64_tr_b16 v[184:185],v104 offset:0\n"
"v_mfma_f32_16x16x32_bf16 a[220:223],v[252:255],v[216:219],a[220:223]\n"
"ds_read_b64_tr_b16 v[186:187],v104 offset:1024\n"
"s_waitcnt lgkmcnt(10)\n"
"v_mfma_f32_16x16x32_bf16 a[192:195],v[160:163],v[152:155],a[192:195]\n"
"ds_read_b64_tr_b16 v[188:189],v105 offset:0\n"
"v_mfma_f32_16x16x32_bf16 a[224:227],v[160:163],v[156:159],a[224:227]\n"
"ds_read_b64_tr_b16 v[190:191],v105 offset:1024\n"
"v_mfma_f32_16x16x32_bf16 a[228:231],v[164:167],v[156:159],a[228:231]\n"
"ds_read_b128 v[128:131],v82 offset:0\n"
"v_mfma_f32_16x16x32_bf16 a[196:199],v[164:167],v[152:155],a[196:199]\n"
"ds_read_b128 v[192:195],v83 offset:0\n"
"s_waitcnt lgkmcnt(10)\n"
"v_mfma_f32_16x16x32_bf16 a[200:203],v[168:171],v[152:155],a[200:203]\n"
"ds_read_b64_tr_b16 v[224:225],v98 offset:16384\n"
"v_mfma_f32_16x16x32_bf16 a[232:235],v[168:171],v[156:159],a[232:235]\n"
"ds_read_b64_tr_b16 v[226:227],v98 offset:17408\n"
"v_mfma_f32_16x16x32_bf16 a[236:239],v[172:175],v[156:159],a[236:239]\n"
"ds_read_b64_tr_b16 v[228:229],v99 offset:16384\n"
"v_mfma_f32_16x16x32_bf16 a[204:207],v[172:175],v[152:155],a[204:207]\n"
"ds_read_b64_tr_b16 v[230:231],v99 offset:17408\n"
"s_waitcnt lgkmcnt(10)\n"
"v_mfma_f32_16x16x32_bf16 a[208:211],v[176:179],v[152:155],a[208:211]\n"
"ds_read_b64_tr_b16 v[232:233],v100 offset:16384\n"
"v_mfma_f32_16x16x32_bf16 a[240:243],v[176:179],v[156:159],a[240:243]\n"
"ds_read_b64_tr_b16 v[234:235],v100 offset:17408\n"
"v_mfma_f32_16x16x32_bf16 a[244:247],v[180:183],v[156:159],a[244:247]\n"
"ds_read_b64_tr_b16 v[236:237],v101 offset:16384\n"
"v_mfma_f32_16x16x32_bf16 a[212:215],v[180:183],v[152:155],a[212:215]\n"
"ds_read_b64_tr_b16 v[238:239],v101 offset:17408\n"
"s_waitcnt lgkmcnt(10)\n"
"v_mfma_f32_16x16x32_bf16 a[216:219],v[184:187],v[152:155],a[216:219]\n"
"ds_read_b64_tr_b16 v[240:241],v102 offset:16384\n"
"v_mfma_f32_16x16x32_bf16 a[248:251],v[184:187],v[156:159],a[248:251]\n"
"ds_read_b64_tr_b16 v[242:243],v102 offset:17408\n"
"v_mfma_f32_16x16x32_bf16 a[252:255],v[188:191],v[156:159],a[252:255]\n"
"ds_read_b64_tr_b16 v[244:245],v103 offset:16384\n"
"v_mfma_f32_16x16x32_bf16 a[220:223],v[188:191],v[152:155],a[220:223]\n"
"ds_read_b64_tr_b16 v[246:247],v103 offset:17408\n"
"s_waitcnt lgkmcnt(8)\n"
"v_mfma_f32_16x16x32_bf16 a[0:3],v[160:163],v[128:131],a[0:3]\n"
"ds_read_b64_tr_b16 v[248:249],v104 offset:16384\n"
"v_mfma_f32_16x16x32_bf16 a[0:3],v[224:227],v[192:195],a[0:3]\n"
"ds_read_b64_tr_b16 v[250:251],v104 offset:17408\n"
"v_mfma_f32_16x16x32_bf16 a[4:7],v[228:231],v[192:195],a[4:7]\n"
"ds_read_b64_tr_b16 v[252:253],v105 offset:16384\n"
"v_mfma_f32_16x16x32_bf16 a[4:7],v[164:167],v[128:131],a[4:7]\n"
"ds_read_b64_tr_b16 v[254:255],v105 offset:17408\n"
"s_waitcnt lgkmcnt(8)\n"
"v_mfma_f32_16x16x32_bf16 a[8:11],v[168:171],v[128:131],a[8:11]\n"
"ds_read_b128 v[132:135],v82 offset:2048\n"
"v_mfma_f32_16x16x32_bf16 a[8:11],v[232:235],v[192:195],a[8:11]\n"
"ds_read_b128 v[196:199],v83 offset:2048\n"
"v_mfma_f32_16x16x32_bf16 a[12:15],v[236:239],v[192:195],a[12:15]\n"
"ds_read_b128 v[136:139],v82 offset:4096\n"
"v_mfma_f32_16x16x32_bf16 a[12:15],v[172:175],v[128:131],a[12:15]\n"
"ds_read_b128 v[200:203],v83 offset:4096\n"
"s_waitcnt lgkmcnt(8)\n"
"v_mfma_f32_16x16x32_bf16 a[16:19],v[176:179],v[128:131],a[16:19]\n"
"ds_read_b128 v[140:143],v82 offset:6144\n"
"v_mfma_f32_16x16x32_bf16 a[16:19],v[240:243],v[192:195],a[16:19]\n"
"ds_read_b128 v[204:207],v83 offset:6144\n"
"v_mfma_f32_16x16x32_bf16 a[20:23],v[244:247],v[192:195],a[20:23]\n"
"ds_read_b128 v[144:147],v82 offset:8192\n"
"v_mfma_f32_16x16x32_bf16 a[20:23],v[180:183],v[128:131],a[20:23]\n"
"ds_read_b128 v[208:211],v83 offset:8192\n"
"s_waitcnt lgkmcnt(8)\n"
"v_mfma_f32_16x16x32_bf16 a[24:27],v[184:187],v[128:131],a[24:27]\n"
"ds_read_b128 v[148:151],v82 offset:10240\n"
"v_mfma_f32_16x16x32_bf16 a[24:27],v[248:251],v[192:195],a[24:27]\n"
"ds_read_b128 v[212:215],v83 offset:10240\n"
"v_mfma_f32_16x16x32_bf16 a[28:31],v[252:255],v[192:195],a[28:31]\n"
"ds_read_b128 v[216:219],v83 offset:12288\n"
"v_mfma_f32_16x16x32_bf16 a[28:31],v[188:191],v[128:131],a[28:31]\n"
"ds_read_b128 v[220:223],v83 offset:14336\n"
"s_waitcnt lgkmcnt(10)\n"
"v_mfma_f32_16x16x32_bf16 a[32:35],v[160:163],v[132:135],a[32:35]\n"
"v_mfma_f32_16x16x32_bf16 a[32:35],v[224:227],v[196:199],a[32:35]\n"
"buffer_load_dwordx4 v[74:77],v112,s[24:27],s58 offen sc1\n"
"v_mfma_f32_16x16x32_bf16 a[36:39],v[228:231],v[196:199],a[36:39]\n"
"v_mfma_f32_16x16x32_bf16 a[36:39],v[164:167],v[132:135],a[36:39]\n"
"v_mfma_f32_16x16x32_bf16 a[40:43],v[168:171],v[132:135],a[40:43]\n"
"s_waitcnt lgkmcnt(0)\n"
"s_barrier\n"
"v_mfma_f32_16x16x32_bf16 a[40:43],v[232:235],v[196:199],a[40:43]\n"
"s_mov_b32 s77,3670016\n"
"s_add_u32 m0,s78,98304\n"
"v_mfma_f32_16x16x32_bf16 a[44:47],v[236:239],v[196:199],a[44:47]\n"
"buffer_load_dwordx4 v110,s[20:23],s77 offen offset:0 lds\n"
"v_mfma_f32_16x16x32_bf16 a[44:47],v[172:175],v[132:135],a[44:47]\n"
"v_mfma_f32_16x16x32_bf16 a[48:51],v[176:179],v[132:135],a[48:51]\n"
"s_load_dwordx2 s[48:49],s[0:1],56\n"
"v_mfma_f32_16x16x32_bf16 a[48:51],v[240:243],v[196:199],a[48:51]\n"
"v_mfma_f32_16x16x32_bf16 a[52:55],v[244:247],v[196:199],a[52:55]\n"
"buffer_load_dwordx4 v111,s[20:23],s77 offen offset:1024 lds\n"
"v_mfma_f32_16x16x32_bf16 a[52:55],v[180:183],v[132:135],a[52:55]\n"
"v_mfma_f32_16x16x32_bf16 a[56:59],v[184:187],v[132:135],a[56:59]\n"
"v_mfma_f32_16x16x32_bf16 a[56:59],v[248:251],v[196:199],a[56:59]\n"
"v_mfma_f32_16x16x32_bf16 a[60:63],v[252:255],v[196:199],a[60:63]\n"
"buffer_load_dwordx4 v113,s[20:23],s77 offen offset:2048 lds\n"
"v_mfma_f32_16x16x32_bf16 a[60:63],v[188:191],v[132:135],a[60:63]\n"
"v_mfma_f32_16x16x32_bf16 a[64:67],v[160:163],v[136:139],a[64:67]\n"
"v_mfma_f32_16x16x32_bf16 a[64:67],v[224:227],v[200:203],a[64:67]\n"
"v_mfma_f32_16x16x32_bf16 a[68:71],v[228:231],v[200:203],a[68:71]\n"
"buffer_load_dwordx4 v114,s[20:23],s77 offen offset:3072 lds\n"
"v_mfma_f32_16x16x32_bf16 a[68:71],v[164:167],v[136:139],a[68:71]\n"
"v_mfma_f32_16x16x32_bf16 a[72:75],v[168:171],v[136:139],a[72:75]\n"
"v_mfma_f32_16x16x32_bf16 a[72:75],v[232:235],v[200:203],a[72:75]\n"
"s_mov_b32 s77,3932160\n"
"s_add_u32 m0,s78,114688\n"
"v_mfma_f32_16x16x32_bf16 a[76:79],v[236:239],v[200:203],a[76:79]\n"
"buffer_load_dwordx4 v110,s[20:23],s77 offen offset:0 lds\n"
"v_mfma_f32_16x16x32_bf16 a[76:79],v[172:175],v[136:139],a[76:79]\n"
"v_mfma_f32_16x16x32_bf16 a[80:83],v[176:179],v[136:139],a[80:83]\n"
"v_mfma_f32_16x16x32_bf16 a[80:83],v[240:243],v[200:203],a[80:83]\n"
"v_mfma_f32_16x16x32_bf16 a[84:87],v[244:247],v[200:203],a[84:87]\n"
"buffer_load_dwordx4 v111,s[20:23],s77 offen offset:1024 lds\n"
"v_mfma_f32_16x16x32_bf16 a[84:87],v[180:183],v[136:139],a[84:87]\n"
"v_mfma_f32_16x16x32_bf16 a[88:91],v[184:187],v[136:139],a[88:91]\n"
"v_mfma_f32_16x16x32_bf16 a[88:91],v[248:251],v[200:203],a[88:91]\n"
"v_mfma_f32_16x16x32_bf16 a[92:95],v[252:255],v[200:203],a[92:95]\n"
"buffer_load_dwordx4 v113,s[20:23],s77 offen offset:2048 lds\n"
"v_mfma_f32_16x16x32_bf16 a[92:95],v[188:191],v[136:139],a[92:95]\n"
"s_mov_b32 s76,s55\n"
"s_waitcnt vmcnt(7)\n"
"v_mfma_f32_16x16x32_bf16 a[96:99],v[160:163],v[140:143],a[96:99]\n"
"v_xor_b32 v116,s80,v74\n"
"v_xor_b32 v117,s81,v75\n"
"v_mfma_f32_16x16x32_bf16 a[96:99],v[224:227],v[204:207],a[96:99]\n"
"v_or_b32 v116,v116,v117\n"
"v_xor_b32 v117,s80,v76\n"
"v_mfma_f32_16x16x32_bf16 a[100:103],v[228:231],v[204:207],a[100:103]\n"
"buffer_load_dwordx4 v114,s[20:23],s77 offen offset:3072 lds\n"
"v_mfma_f32_16x16x32_bf16 a[100:103],v[164:167],v[140:143],a[100:103]\n"
"v_or_b32 v116,v116,v117\n"
"v_xor_b32 v117,s81,v77\n"
"v_mfma_f32_16x16x32_bf16 a[104:107],v[168:171],v[140:143],a[104:107]\n"
"v_or_b32 v116,v116,v117\n"
"v_cmp_ne_u32 vcc,0,v116\n"
"v_mfma_f32_16x16x32_bf16 a[104:107],v[232:235],v[204:207],a[104:107]\n"
"s_cbranch_vccnz .Lfk_xn0_s\n"
".Lfk_xn0_ok:\n"
"v_mfma_f32_16x16x32_bf16 a[108:111],v[236:239],v[204:207],a[108:111]\n"
"s_add_u32 m0,s78,65536\n"
"v_mfma_f32_16x16x32_bf16 a[108:111],v[172:175],v[140:143],a[108:111]\n"
"buffer_load_dwordx4 v112,s[24:27],s76 offen offset:0 sc1 lds\n"
"v_mfma_f32_16x16x32_bf16 a[112:115],v[176:179],v[140:143],a[112:115]\n"
"s_waitcnt lgkmcnt(0)\n"
"s_lshl2_add_u32 s88,s100,s52\n"
"v_mfma_f32_16x16x32_bf16 a[112:115],v[240:243],v[204:207],a[112:115]\n"
"s_mul_i32 s88,s88,7168\n"
"s_add_u32 s88,s88,0\n"
"v_mfma_f32_16x16x32_bf16 a[116:119],v[244:247],v[204:207],a[116:119]\n"
"s_add_u32 s48,s48,s88\n"
"s_addc_u32 s49,s49,0\n"
"s_mov_b32 s50,3584\n"
"v_mfma_f32_16x16x32_bf16 a[116:119],v[180:183],v[140:143],a[116:119]\n"
"s_mov_b32 s51,0x20000\n"
"v_lshlrev_b32 v8,3,v112\n"
"v_mfma_f32_16x16x32_bf16 a[120:123],v[184:187],v[140:143],a[120:123]\n"
"buffer_load_dwordx4 v112,s[24:27],s76 offen offset:1024 sc1 lds\n"
"v_mfma_f32_16x16x32_bf16 a[120:123],v[248:251],v[204:207],a[120:123]\n"
"v_mfma_f32_16x16x32_bf16 a[124:127],v[252:255],v[204:207],a[124:127]\n"
"v_mfma_f32_16x16x32_bf16 a[124:127],v[188:191],v[140:143],a[124:127]\n"
"v_mfma_f32_16x16x32_bf16 a[128:131],v[160:163],v[144:147],a[128:131]\n"
"v_mfma_f32_16x16x32_bf16 a[128:131],v[224:227],v[208:211],a[128:131]\n"
"buffer_load_dwordx4 v112,s[24:27],s76 offen offset:2048 sc1 lds\n"
"v_mfma_f32_16x16x32_bf16 a[132:135],v[228:231],v[208:211],a[132:135]\n"
"v_mfma_f32_16x16x32_bf16 a[132:135],v[164:167],v[144:147],a[132:135]\n"
"v_mfma_f32_16x16x32_bf16 a[136:139],v[168:171],v[144:147],a[136:139]\n"
"v_mfma_f32_16x16x32_bf16 a[136:139],v[232:235],v[208:211],a[136:139]\n"
"buffer_load_dwordx4 v112,s[24:27],s76 offen offset:3072 sc1 lds\n"
"v_mfma_f32_16x16x32_bf16 a[140:143],v[236:239],v[208:211],a[140:143]\n"
"s_xor_b32 s76,s76,16384\n"
"v_mfma_f32_16x16x32_bf16 a[140:143],v[172:175],v[144:147],a[140:143]\n"
"v_mfma_f32_16x16x32_bf16 a[144:147],v[176:179],v[144:147],a[144:147]\n"
"v_mfma_f32_16x16x32_bf16 a[144:147],v[240:243],v[208:211],a[144:147]\n"
"v_mfma_f32_16x16x32_bf16 a[148:151],v[244:247],v[208:211],a[148:151]\n"
"s_add_u32 m0,s78,81920\n"
"v_mfma_f32_16x16x32_bf16 a[148:151],v[180:183],v[144:147],a[148:151]\n"
"buffer_load_dwordx4 v112,s[24:27],s76 offen offset:0 sc1 lds\n"
"v_mfma_f32_16x16x32_bf16 a[152:155],v[184:187],v[144:147],a[152:155]\n"
"v_mfma_f32_16x16x32_bf16 a[152:155],v[248:251],v[208:211],a[152:155]\n"
"v_mfma_f32_16x16x32_bf16 a[156:159],v[252:255],v[208:211],a[156:159]\n"
"v_mfma_f32_16x16x32_bf16 a[156:159],v[188:191],v[144:147],a[156:159]\n"
"v_mfma_f32_16x16x32_bf16 a[160:163],v[160:163],v[148:151],a[160:163]\n"
"buffer_load_dwordx4 v112,s[24:27],s76 offen offset:1024 sc1 lds\n"
"v_mfma_f32_16x16x32_bf16 a[160:163],v[224:227],v[212:215],a[160:163]\n"
"v_mfma_f32_16x16x32_bf16 a[164:167],v[228:231],v[212:215],a[164:167]\n"
"v_mfma_f32_16x16x32_bf16 a[164:167],v[164:167],v[148:151],a[164:167]\n"
"v_mfma_f32_16x16x32_bf16 a[168:171],v[168:171],v[148:151],a[168:171]\n"
"v_mfma_f32_16x16x32_bf16 a[168:171],v[232:235],v[212:215],a[168:171]\n"
"buffer_load_dwordx4 v112,s[24:27],s76 offen offset:2048 sc1 lds\n"
"v_mfma_f32_16x16x32_bf16 a[172:175],v[236:239],v[212:215],a[172:175]\n"
"v_mfma_f32_16x16x32_bf16 a[172:175],v[172:175],v[148:151],a[172:175]\n"
"v_mfma_f32_16x16x32_bf16 a[176:179],v[176:179],v[148:151],a[176:179]\n"
"v_mfma_f32_16x16x32_bf16 a[176:179],v[240:243],v[212:215],a[176:179]\n"
"buffer_load_dwordx4 v112,s[24:27],s76 offen offset:3072 sc1 lds\n"
"v_mfma_f32_16x16x32_bf16 a[180:183],v[244:247],v[212:215],a[180:183]\n"
"s_xor_b32 s76,s76,49152\n"
"v_mfma_f32_16x16x32_bf16 a[180:183],v[180:183],v[148:151],a[180:183]\n"
"v_mfma_f32_16x16x32_bf16 a[184:187],v[184:187],v[148:151],a[184:187]\n"
"buffer_load_dword v115,v8,s[48:51],0 offen\n"
"v_mfma_f32_16x16x32_bf16 a[184:187],v[248:251],v[212:215],a[184:187]\n"
"v_mfma_f32_16x16x32_bf16 a[188:191],v[252:255],v[212:215],a[188:191]\n"
"v_mfma_f32_16x16x32_bf16 a[188:191],v[188:191],v[148:151],a[188:191]\n"
"s_barrier\n"
"v_mfma_f32_16x16x32_bf16 a[192:195],v[224:227],v[216:219],a[192:195]\n"
"ds_read_b128 v[152:155],v80 offset:12288\n"
"v_mfma_f32_16x16x32_bf16 a[224:227],v[224:227],v[220:223],a[224:227]\n"
"ds_read_b128 v[156:159],v80 offset:14336\n"
"v_mfma_f32_16x16x32_bf16 a[228:231],v[228:231],v[220:223],a[228:231]\n"
"ds_read_b64_tr_b16 v[160:161],v98 offset:49152\n"
"v_mfma_f32_16x16x32_bf16 a[196:199],v[228:231],v[216:219],a[196:199]\n"
"ds_read_b64_tr_b16 v[162:163],v98 offset:50176\n"
"v_mfma_f32_16x16x32_bf16 a[200:203],v[232:235],v[216:219],a[200:203]\n"
"ds_read_b64_tr_b16 v[164:165],v99 offset:49152\n"
"v_mfma_f32_16x16x32_bf16 a[232:235],v[232:235],v[220:223],a[232:235]\n"
"ds_read_b64_tr_b16 v[166:167],v99 offset:50176\n"
"v_mfma_f32_16x16x32_bf16 a[236:239],v[236:239],v[220:223],a[236:239]\n"
"ds_read_b64_tr_b16 v[168:169],v100 offset:49152\n"
"v_mfma_f32_16x16x32_bf16 a[204:207],v[236:239],v[216:219],a[204:207]\n"
"ds_read_b64_tr_b16 v[170:171],v100 offset:50176\n"
"v_mfma_f32_16x16x32_bf16 a[208:211],v[240:243],v[216:219],a[208:211]\n"
"ds_read_b64_tr_b16 v[172:173],v101 offset:49152\n"
"v_mfma_f32_16x16x32_bf16 a[240:243],v[240:243],v[220:223],a[240:243]\n"
"ds_read_b64_tr_b16 v[174:175],v101 offset:50176\n"
"v_mfma_f32_16x16x32_bf16 a[244:247],v[244:247],v[220:223],a[244:247]\n"
"ds_read_b64_tr_b16 v[176:177],v102 offset:49152\n"
"v_mfma_f32_16x16x32_bf16 a[212:215],v[244:247],v[216:219],a[212:215]\n"
"ds_read_b64_tr_b16 v[178:179],v102 offset:50176\n"
"v_mfma_f32_16x16x32_bf16 a[216:219],v[248:251],v[216:219],a[216:219]\n"
"ds_read_b64_tr_b16 v[180:181],v103 offset:49152\n"
"v_mfma_f32_16x16x32_bf16 a[248:251],v[248:251],v[220:223],a[248:251]\n"
"ds_read_b64_tr_b16 v[182:183],v103 offset:50176\n"
"ds_read_b64_tr_b16 v[184:185],v104 offset:49152\n"
"v_mfma_f32_16x16x32_bf16 a[252:255],v[252:255],v[220:223],a[252:255]\n"
"v_mov_b32 v110,v72\n"
"v_mov_b32 v111,v73\n"
"v_mfma_f32_16x16x32_bf16 a[220:223],v[252:255],v[216:219],a[220:223]\n"
"ds_read_b64_tr_b16 v[186:187],v104 offset:50176\n"
"s_waitcnt lgkmcnt(10)\n"
"v_mfma_f32_16x16x32_bf16 a[192:195],v[160:163],v[152:155],a[192:195]\n"
"ds_read_b64_tr_b16 v[188:189],v105 offset:49152\n"
"v_mfma_f32_16x16x32_bf16 a[224:227],v[160:163],v[156:159],a[224:227]\n"
"ds_read_b64_tr_b16 v[190:191],v105 offset:50176\n"
"v_mfma_f32_16x16x32_bf16 a[228:231],v[164:167],v[156:159],a[228:231]\n"
"ds_read_b128 v[128:131],v80 offset:0\n"
"v_mfma_f32_16x16x32_bf16 a[196:199],v[164:167],v[152:155],a[196:199]\n"
"ds_read_b128 v[192:195],v81 offset:0\n"
"s_waitcnt lgkmcnt(10)\n"
"v_mfma_f32_16x16x32_bf16 a[200:203],v[168:171],v[152:155],a[200:203]\n"
"ds_read_b64_tr_b16 v[224:225],v98 offset:32768\n"
"v_mfma_f32_16x16x32_bf16 a[232:235],v[168:171],v[156:159],a[232:235]\n"
"ds_read_b64_tr_b16 v[226:227],v98 offset:33792\n"
"v_mfma_f32_16x16x32_bf16 a[236:239],v[172:175],v[156:159],a[236:239]\n"
"ds_read_b64_tr_b16 v[228:229],v99 offset:32768\n"
"v_mfma_f32_16x16x32_bf16 a[204:207],v[172:175],v[152:155],a[204:207]\n"
"ds_read_b64_tr_b16 v[230:231],v99 offset:33792\n"
"s_waitcnt lgkmcnt(10)\n"
"v_mfma_f32_16x16x32_bf16 a[208:211],v[176:179],v[152:155],a[208:211]\n"
"ds_read_b64_tr_b16 v[232:233],v100 offset:32768\n"
"v_mfma_f32_16x16x32_bf16 a[240:243],v[176:179],v[156:159],a[240:243]\n"
"ds_read_b64_tr_b16 v[234:235],v100 offset:33792\n"
"v_mfma_f32_16x16x32_bf16 a[244:247],v[180:183],v[156:159],a[244:247]\n"
"ds_read_b64_tr_b16 v[236:237],v101 offset:32768\n"
"v_mfma_f32_16x16x32_bf16 a[212:215],v[180:183],v[152:155],a[212:215]\n"
"ds_read_b64_tr_b16 v[238:239],v101 offset:33792\n"
"s_waitcnt lgkmcnt(10)\n"
"v_mfma_f32_16x16x32_bf16 a[216:219],v[184:187],v[152:155],a[216:219]\n"
"ds_read_b64_tr_b16 v[240:241],v102 offset:32768\n"
"v_mfma_f32_16x16x32_bf16 a[248:251],v[184:187],v[156:159],a[248:251]\n"
"ds_read_b64_tr_b16 v[242:243],v102 offset:33792\n"
"v_mfma_f32_16x16x32_bf16 a[252:255],v[188:191],v[156:159],a[252:255]\n"
"ds_read_b64_tr_b16 v[244:245],v103 offset:32768\n"
"v_mfma_f32_16x16x32_bf16 a[220:223],v[188:191],v[152:155],a[220:223]\n"
"ds_read_b64_tr_b16 v[246:247],v103 offset:33792\n"
"s_waitcnt lgkmcnt(8)\n"
"v_mfma_f32_16x16x32_bf16 a[0:3],v[160:163],v[128:131],a[0:3]\n"
"ds_read_b64_tr_b16 v[248:249],v104 offset:32768\n"
"v_mfma_f32_16x16x32_bf16 a[0:3],v[224:227],v[192:195],a[0:3]\n"
"ds_read_b64_tr_b16 v[250:251],v104 offset:33792\n"
"v_mfma_f32_16x16x32_bf16 a[4:7],v[228:231],v[192:195],a[4:7]\n"
"ds_read_b64_tr_b16 v[252:253],v105 offset:32768\n"
"v_mfma_f32_16x16x32_bf16 a[4:7],v[164:167],v[128:131],a[4:7]\n"
"ds_read_b64_tr_b16 v[254:255],v105 offset:33792\n"
"s_waitcnt lgkmcnt(8)\n"
"v_mfma_f32_16x16x32_bf16 a[8:11],v[168:171],v[128:131],a[8:11]\n"
"ds_read_b128 v[132:135],v80 offset:2048\n"
"v_mfma_f32_16x16x32_bf16 a[8:11],v[232:235],v[192:195],a[8:11]\n"
"ds_read_b128 v[196:199],v81 offset:2048\n"
"v_mfma_f32_16x16x32_bf16 a[12:15],v[236:239],v[192:195],a[12:15]\n"
"ds_read_b128 v[136:139],v80 offset:4096\n"
"v_mfma_f32_16x16x32_bf16 a[12:15],v[172:175],v[128:131],a[12:15]\n"
"ds_read_b128 v[200:203],v81 offset:4096\n"
"s_waitcnt lgkmcnt(8)\n"
"v_mfma_f32_16x16x32_bf16 a[16:19],v[176:179],v[128:131],a[16:19]\n"
"ds_read_b128 v[140:143],v80 offset:6144\n"
"v_mfma_f32_16x16x32_bf16 a[16:19],v[240:243],v[192:195],a[16:19]\n"
"ds_read_b128 v[204:207],v81 offset:6144\n"
"v_mfma_f32_16x16x32_bf16 a[20:23],v[244:247],v[192:195],a[20:23]\n"
"ds_read_b128 v[144:147],v80 offset:8192\n"
"v_mfma_f32_16x16x32_bf16 a[20:23],v[180:183],v[128:131],a[20:23]\n"
"ds_read_b128 v[208:211],v81 offset:8192\n"
"s_waitcnt lgkmcnt(8)\n"
"v_mfma_f32_16x16x32_bf16 a[24:27],v[184:187],v[128:131],a[24:27]\n"
"ds_read_b128 v[148:151],v80 offset:10240\n"
"v_mfma_f32_16x16x32_bf16 a[24:27],v[248:251],v[192:195],a[24:27]\n"
"ds_read_b128 v[212:215],v81 offset:10240\n"
"v_mfma_f32_16x16x32_bf16 a[28:31],v[252:255],v[192:195],a[28:31]\n"
"ds_read_b128 v[216:219],v81 offset:12288\n"
"v_mfma_f32_16x16x32_bf16 a[28:31],v[188:191],v[128:131],a[28:31]\n"
"ds_read_b128 v[220:223],v81 offset:14336\n"
"s_waitcnt lgkmcnt(10)\n"
"v_mfma_f32_16x16x32_bf16 a[32:35],v[160:163],v[132:135],a[32:35]\n"
"v_mfma_f32_16x16x32_bf16 a[32:35],v[224:227],v[196:199],a[32:35]\n"
"v_mfma_f32_16x16x32_bf16 a[36:39],v[228:231],v[196:199],a[36:39]\n"
"v_mfma_f32_16x16x32_bf16 a[36:39],v[164:167],v[132:135],a[36:39]\n"
"v_mfma_f32_16x16x32_bf16 a[40:43],v[168:171],v[132:135],a[40:43]\n"
"s_waitcnt lgkmcnt(0)\n"
"s_barrier\n"
"v_mfma_f32_16x16x32_bf16 a[40:43],v[232:235],v[196:199],a[40:43]\n"
"s_add_u32 m0,s85,147456\n"
"v_mfma_f32_16x16x32_bf16 a[44:47],v[236:239],v[196:199],a[44:47]\n"
"buffer_load_dwordx4 v110,s[28:31],s86 offen lds\n"
"v_mfma_f32_16x16x32_bf16 a[44:47],v[172:175],v[132:135],a[44:47]\n"
"v_mfma_f32_16x16x32_bf16 a[48:51],v[176:179],v[132:135],a[48:51]\n"
"s_load_dwordx2 s[48:49],s[0:1],56\n"
"v_mfma_f32_16x16x32_bf16 a[48:51],v[240:243],v[196:199],a[48:51]\n"
"s_add_u32 m0,s85,148480\n"
"v_mfma_f32_16x16x32_bf16 a[52:55],v[244:247],v[196:199],a[52:55]\n"
"buffer_load_dwordx4 v111,s[28:31],s86 offen lds\n"
"v_mfma_f32_16x16x32_bf16 a[52:55],v[180:183],v[132:135],a[52:55]\n"
"s_xor_b32 s86,s86,262144\n"
"v_mfma_f32_16x16x32_bf16 a[56:59],v[184:187],v[132:135],a[56:59]\n"
"v_mfma_f32_16x16x32_bf16 a[56:59],v[248:251],v[196:199],a[56:59]\n"
"s_add_u32 m0,s85,155648\n"
"v_mfma_f32_16x16x32_bf16 a[60:63],v[252:255],v[196:199],a[60:63]\n"
"buffer_load_dwordx4 v110,s[28:31],s86 offen lds\n"
"v_mfma_f32_16x16x32_bf16 a[60:63],v[188:191],v[132:135],a[60:63]\n"
"v_mfma_f32_16x16x32_bf16 a[64:67],v[160:163],v[136:139],a[64:67]\n"
"v_mfma_f32_16x16x32_bf16 a[64:67],v[224:227],v[200:203],a[64:67]\n"
"s_add_u32 m0,s85,156672\n"
"v_mfma_f32_16x16x32_bf16 a[68:71],v[228:231],v[200:203],a[68:71]\n"
"buffer_load_dwordx4 v111,s[28:31],s86 offen lds\n"
"v_mfma_f32_16x16x32_bf16 a[68:71],v[164:167],v[136:139],a[68:71]\n"
"s_xor_b32 s86,s86,786432\n"
"v_mfma_f32_16x16x32_bf16 a[72:75],v[168:171],v[136:139],a[72:75]\n"
"v_mfma_f32_16x16x32_bf16 a[72:75],v[232:235],v[200:203],a[72:75]\n"
"s_add_u32 m0,s85,131072\n"
"v_mfma_f32_16x16x32_bf16 a[76:79],v[236:239],v[200:203],a[76:79]\n"
"buffer_load_dwordx4 v110,s[28:31],s86 offen lds\n"
"v_mfma_f32_16x16x32_bf16 a[76:79],v[172:175],v[136:139],a[76:79]\n"
"v_mfma_f32_16x16x32_bf16 a[80:83],v[176:179],v[136:139],a[80:83]\n"
"v_mfma_f32_16x16x32_bf16 a[80:83],v[240:243],v[200:203],a[80:83]\n"
"s_add_u32 m0,s85,132096\n"
"v_mfma_f32_16x16x32_bf16 a[84:87],v[244:247],v[200:203],a[84:87]\n"
"buffer_load_dwordx4 v111,s[28:31],s86 offen lds\n"
"v_mfma_f32_16x16x32_bf16 a[84:87],v[180:183],v[136:139],a[84:87]\n"
"s_xor_b32 s86,s86,262144\n"
"v_mfma_f32_16x16x32_bf16 a[88:91],v[184:187],v[136:139],a[88:91]\n"
"v_mfma_f32_16x16x32_bf16 a[88:91],v[248:251],v[200:203],a[88:91]\n"
"v_mfma_f32_16x16x32_bf16 a[92:95],v[252:255],v[200:203],a[92:95]\n"
"v_mfma_f32_16x16x32_bf16 a[92:95],v[188:191],v[136:139],a[92:95]\n"
"v_mfma_f32_16x16x32_bf16 a[96:99],v[160:163],v[140:143],a[96:99]\n"
"v_mfma_f32_16x16x32_bf16 a[96:99],v[224:227],v[204:207],a[96:99]\n"
"s_add_u32 m0,s85,139264\n"
"v_mfma_f32_16x16x32_bf16 a[100:103],v[228:231],v[204:207],a[100:103]\n"
"buffer_load_dwordx4 v110,s[28:31],s86 offen lds\n"
"v_mfma_f32_16x16x32_bf16 a[100:103],v[164:167],v[140:143],a[100:103]\n"
"v_mfma_f32_16x16x32_bf16 a[104:107],v[168:171],v[140:143],a[104:107]\n"
"v_mfma_f32_16x16x32_bf16 a[104:107],v[232:235],v[204:207],a[104:107]\n"
"s_add_u32 m0,s85,140288\n"
"v_mfma_f32_16x16x32_bf16 a[108:111],v[236:239],v[204:207],a[108:111]\n"
"buffer_load_dwordx4 v111,s[28:31],s86 offen lds\n"
"v_mfma_f32_16x16x32_bf16 a[108:111],v[172:175],v[140:143],a[108:111]\n"
"s_xor_b32 s86,s86,1835008\n"
"v_mfma_f32_16x16x32_bf16 a[112:115],v[176:179],v[140:143],a[112:115]\n"
"s_waitcnt lgkmcnt(0)\n"
"s_lshl2_add_u32 s88,s100,s52\n"
"v_mfma_f32_16x16x32_bf16 a[112:115],v[240:243],v[204:207],a[112:115]\n"
"s_mul_i32 s88,s88,7168\n"
"s_add_u32 m0,s78,0\n"
"v_mfma_f32_16x16x32_bf16 a[116:119],v[244:247],v[204:207],a[116:119]\n"
"buffer_load_dwordx4 v112,s[24:27],s76 offen offset:0 sc1 lds\n"
"v_mfma_f32_16x16x32_bf16 a[116:119],v[180:183],v[140:143],a[116:119]\n"
"s_add_u32 s88,s88,3584\n"
"s_add_u32 s48,s48,s88\n"
"s_addc_u32 s49,s49,0\n"
"v_mfma_f32_16x16x32_bf16 a[120:123],v[184:187],v[140:143],a[120:123]\n"
"s_mov_b32 s50,3584\n"
"s_mov_b32 s51,0x20000\n"
"v_mfma_f32_16x16x32_bf16 a[120:123],v[248:251],v[204:207],a[120:123]\n"
"v_lshlrev_b32 v8,3,v112\n"
"v_mfma_f32_16x16x32_bf16 a[124:127],v[252:255],v[204:207],a[124:127]\n"
"buffer_load_dwordx4 v112,s[24:27],s76 offen offset:1024 sc1 lds\n"
"v_mfma_f32_16x16x32_bf16 a[124:127],v[188:191],v[140:143],a[124:127]\n"
"v_mfma_f32_16x16x32_bf16 a[128:131],v[160:163],v[144:147],a[128:131]\n"
"v_mfma_f32_16x16x32_bf16 a[128:131],v[224:227],v[208:211],a[128:131]\n"
"v_mfma_f32_16x16x32_bf16 a[132:135],v[228:231],v[208:211],a[132:135]\n"
"buffer_load_dwordx4 v112,s[24:27],s76 offen offset:2048 sc1 lds\n"
"v_mfma_f32_16x16x32_bf16 a[132:135],v[164:167],v[144:147],a[132:135]\n"
"v_mfma_f32_16x16x32_bf16 a[136:139],v[168:171],v[144:147],a[136:139]\n"
"v_mfma_f32_16x16x32_bf16 a[136:139],v[232:235],v[208:211],a[136:139]\n"
"buffer_load_dwordx4 v112,s[24:27],s76 offen offset:3072 sc1 lds\n"
"v_mfma_f32_16x16x32_bf16 a[140:143],v[236:239],v[208:211],a[140:143]\n"
"s_xor_b32 s76,s76,16384\n"
"v_mfma_f32_16x16x32_bf16 a[140:143],v[172:175],v[144:147],a[140:143]\n"
"v_mfma_f32_16x16x32_bf16 a[144:147],v[176:179],v[144:147],a[144:147]\n"
"v_mfma_f32_16x16x32_bf16 a[144:147],v[240:243],v[208:211],a[144:147]\n"
"s_add_u32 m0,s78,16384\n"
"v_mfma_f32_16x16x32_bf16 a[148:151],v[244:247],v[208:211],a[148:151]\n"
"buffer_load_dwordx4 v112,s[24:27],s76 offen offset:0 sc1 lds\n"
"v_mfma_f32_16x16x32_bf16 a[148:151],v[180:183],v[144:147],a[148:151]\n"
"v_mfma_f32_16x16x32_bf16 a[152:155],v[184:187],v[144:147],a[152:155]\n"
"v_mfma_f32_16x16x32_bf16 a[152:155],v[248:251],v[208:211],a[152:155]\n"
"v_mfma_f32_16x16x32_bf16 a[156:159],v[252:255],v[208:211],a[156:159]\n"
"buffer_load_dwordx4 v112,s[24:27],s76 offen offset:1024 sc1 lds\n"
"v_mfma_f32_16x16x32_bf16 a[156:159],v[188:191],v[144:147],a[156:159]\n"
"v_mfma_f32_16x16x32_bf16 a[160:163],v[160:163],v[148:151],a[160:163]\n"
"v_mfma_f32_16x16x32_bf16 a[160:163],v[224:227],v[212:215],a[160:163]\n"
"v_mfma_f32_16x16x32_bf16 a[164:167],v[228:231],v[212:215],a[164:167]\n"
"buffer_load_dwordx4 v112,s[24:27],s76 offen offset:2048 sc1 lds\n"
"v_mfma_f32_16x16x32_bf16 a[164:167],v[164:167],v[148:151],a[164:167]\n"
"v_mfma_f32_16x16x32_bf16 a[168:171],v[168:171],v[148:151],a[168:171]\n"
"v_mfma_f32_16x16x32_bf16 a[168:171],v[232:235],v[212:215],a[168:171]\n"
"buffer_load_dwordx4 v112,s[24:27],s76 offen offset:3072 sc1 lds\n"
"v_mfma_f32_16x16x32_bf16 a[172:175],v[236:239],v[212:215],a[172:175]\n"
"s_xor_b32 s76,s76,114688\n"
"v_mfma_f32_16x16x32_bf16 a[172:175],v[172:175],v[148:151],a[172:175]\n"
"v_mfma_f32_16x16x32_bf16 a[176:179],v[176:179],v[148:151],a[176:179]\n"
"buffer_load_dword v115,v8,s[48:51],0 offen\n"
"v_mfma_f32_16x16x32_bf16 a[176:179],v[240:243],v[212:215],a[176:179]\n"
"v_mfma_f32_16x16x32_bf16 a[180:183],v[244:247],v[212:215],a[180:183]\n"
"v_mfma_f32_16x16x32_bf16 a[180:183],v[180:183],v[148:151],a[180:183]\n"
"v_mfma_f32_16x16x32_bf16 a[184:187],v[184:187],v[148:151],a[184:187]\n"
"v_mfma_f32_16x16x32_bf16 a[184:187],v[248:251],v[212:215],a[184:187]\n"
"v_mfma_f32_16x16x32_bf16 a[188:191],v[252:255],v[212:215],a[188:191]\n"
"v_mfma_f32_16x16x32_bf16 a[188:191],v[188:191],v[148:151],a[188:191]\n"
"s_waitcnt vmcnt(26)\n"
"s_barrier\n"
"v_mfma_f32_16x16x32_bf16 a[192:195],v[224:227],v[216:219],a[192:195]\n"
"ds_read_b128 v[152:155],v80 offset:45056\n"
"v_mfma_f32_16x16x32_bf16 a[224:227],v[224:227],v[220:223],a[224:227]\n"
"ds_read_b128 v[156:159],v80 offset:47104\n"
"v_mfma_f32_16x16x32_bf16 a[228:231],v[228:231],v[220:223],a[228:231]\n"
"ds_read_b64_tr_b16 v[160:161],v98 offset:0\n"
"v_mfma_f32_16x16x32_bf16 a[196:199],v[228:231],v[216:219],a[196:199]\n"
"ds_read_b64_tr_b16 v[162:163],v98 offset:1024\n"
"v_mfma_f32_16x16x32_bf16 a[200:203],v[232:235],v[216:219],a[200:203]\n"
"ds_read_b64_tr_b16 v[164:165],v99 offset:0\n"
"v_mfma_f32_16x16x32_bf16 a[232:235],v[232:235],v[220:223],a[232:235]\n"
"ds_read_b64_tr_b16 v[166:167],v99 offset:1024\n"
"v_mfma_f32_16x16x32_bf16 a[236:239],v[236:239],v[220:223],a[236:239]\n"
"ds_read_b64_tr_b16 v[168:169],v100 offset:0\n"
"v_mfma_f32_16x16x32_bf16 a[204:207],v[236:239],v[216:219],a[204:207]\n"
"ds_read_b64_tr_b16 v[170:171],v100 offset:1024\n"
"v_mfma_f32_16x16x32_bf16 a[208:211],v[240:243],v[216:219],a[208:211]\n"
"ds_read_b64_tr_b16 v[172:173],v101 offset:0\n"
"v_mfma_f32_16x16x32_bf16 a[240:243],v[240:243],v[220:223],a[240:243]\n"
"ds_read_b64_tr_b16 v[174:175],v101 offset:1024\n"
"v_mfma_f32_16x16x32_bf16 a[244:247],v[244:247],v[220:223],a[244:247]\n"
"ds_read_b64_tr_b16 v[176:177],v102 offset:0\n"
"v_mfma_f32_16x16x32_bf16 a[212:215],v[244:247],v[216:219],a[212:215]\n"
"ds_read_b64_tr_b16 v[178:179],v102 offset:1024\n"
"v_mfma_f32_16x16x32_bf16 a[216:219],v[248:251],v[216:219],a[216:219]\n"
"ds_read_b64_tr_b16 v[180:181],v103 offset:0\n"
"v_mfma_f32_16x16x32_bf16 a[248:251],v[248:251],v[220:223],a[248:251]\n"
"ds_read_b64_tr_b16 v[182:183],v103 offset:1024\n"
"v_mfma_f32_16x16x32_bf16 a[252:255],v[252:255],v[220:223],a[252:255]\n"
"ds_read_b64_tr_b16 v[184:185],v104 offset:0\n"
"v_mfma_f32_16x16x32_bf16 a[220:223],v[252:255],v[216:219],a[220:223]\n"
"ds_read_b64_tr_b16 v[186:187],v104 offset:1024\n"
"s_waitcnt lgkmcnt(10)\n"
"v_mfma_f32_16x16x32_bf16 a[192:195],v[160:163],v[152:155],a[192:195]\n"
"ds_read_b64_tr_b16 v[188:189],v105 offset:0\n"
"v_mfma_f32_16x16x32_bf16 a[224:227],v[160:163],v[156:159],a[224:227]\n"
"ds_read_b64_tr_b16 v[190:191],v105 offset:1024\n"
"v_mfma_f32_16x16x32_bf16 a[228:231],v[164:167],v[156:159],a[228:231]\n"
"ds_read_b128 v[128:131],v80 offset:32768\n"
"v_mfma_f32_16x16x32_bf16 a[196:199],v[164:167],v[152:155],a[196:199]\n"
"ds_read_b128 v[192:195],v81 offset:32768\n"
"s_waitcnt lgkmcnt(10)\n"
"v_mfma_f32_16x16x32_bf16 a[200:203],v[168:171],v[152:155],a[200:203]\n"
"ds_read_b64_tr_b16 v[224:225],v98 offset:16384\n"
"v_mfma_f32_16x16x32_bf16 a[232:235],v[168:171],v[156:159],a[232:235]\n"
"ds_read_b64_tr_b16 v[226:227],v98 offset:17408\n"
"v_mfma_f32_16x16x32_bf16 a[236:239],v[172:175],v[156:159],a[236:239]\n"
"ds_read_b64_tr_b16 v[228:229],v99 offset:16384\n"
"v_mfma_f32_16x16x32_bf16 a[204:207],v[172:175],v[152:155],a[204:207]\n"
"ds_read_b64_tr_b16 v[230:231],v99 offset:17408\n"
"s_waitcnt lgkmcnt(10)\n"
"v_mfma_f32_16x16x32_bf16 a[208:211],v[176:179],v[152:155],a[208:211]\n"
"ds_read_b64_tr_b16 v[232:233],v100 offset:16384\n"
"v_mfma_f32_16x16x32_bf16 a[240:243],v[176:179],v[156:159],a[240:243]\n"
"ds_read_b64_tr_b16 v[234:235],v100 offset:17408\n"
"v_mfma_f32_16x16x32_bf16 a[244:247],v[180:183],v[156:159],a[244:247]\n"
"ds_read_b64_tr_b16 v[236:237],v101 offset:16384\n"
"v_mfma_f32_16x16x32_bf16 a[212:215],v[180:183],v[152:155],a[212:215]\n"
"ds_read_b64_tr_b16 v[238:239],v101 offset:17408\n"
"s_waitcnt lgkmcnt(10)\n"
"v_mfma_f32_16x16x32_bf16 a[216:219],v[184:187],v[152:155],a[216:219]\n"
"ds_read_b64_tr_b16 v[240:241],v102 offset:16384\n"
"v_mfma_f32_16x16x32_bf16 a[248:251],v[184:187],v[156:159],a[248:251]\n"
"ds_read_b64_tr_b16 v[242:243],v102 offset:17408\n"
"v_mfma_f32_16x16x32_bf16 a[252:255],v[188:191],v[156:159],a[252:255]\n"
"ds_read_b64_tr_b16 v[244:245],v103 offset:16384\n"
"v_mfma_f32_16x16x32_bf16 a[220:223],v[188:191],v[152:155],a[220:223]\n"
"ds_read_b64_tr_b16 v[246:247],v103 offset:17408\n"
"s_waitcnt lgkmcnt(8)\n"
"v_mfma_f32_16x16x32_bf16 a[0:3],v[160:163],v[128:131],a[0:3]\n"
"ds_read_b64_tr_b16 v[248:249],v104 offset:16384\n"
"v_mfma_f32_16x16x32_bf16 a[0:3],v[224:227],v[192:195],a[0:3]\n"
"ds_read_b64_tr_b16 v[250:251],v104 offset:17408\n"
"v_mfma_f32_16x16x32_bf16 a[4:7],v[228:231],v[192:195],a[4:7]\n"
"ds_read_b64_tr_b16 v[252:253],v105 offset:16384\n"
"v_mfma_f32_16x16x32_bf16 a[4:7],v[164:167],v[128:131],a[4:7]\n"
"ds_read_b64_tr_b16 v[254:255],v105 offset:17408\n"
"s_waitcnt lgkmcnt(8)\n"
"v_mfma_f32_16x16x32_bf16 a[8:11],v[168:171],v[128:131],a[8:11]\n"
"ds_read_b128 v[132:135],v80 offset:34816\n"
"v_mfma_f32_16x16x32_bf16 a[8:11],v[232:235],v[192:195],a[8:11]\n"
"ds_read_b128 v[196:199],v81 offset:34816\n"
"v_mfma_f32_16x16x32_bf16 a[12:15],v[236:239],v[192:195],a[12:15]\n"
"ds_read_b128 v[136:139],v80 offset:36864\n"
"v_mfma_f32_16x16x32_bf16 a[12:15],v[172:175],v[128:131],a[12:15]\n"
"ds_read_b128 v[200:203],v81 offset:36864\n"
"s_waitcnt lgkmcnt(8)\n"
"v_mfma_f32_16x16x32_bf16 a[16:19],v[176:179],v[128:131],a[16:19]\n"
"s_and_b32 s82,s52,1\n"
"ds_read_b128 v[140:143],v80 offset:38912\n"
"s_lshl_b32 s82,s82,13\n"
"v_mfma_f32_16x16x32_bf16 a[16:19],v[240:243],v[192:195],a[16:19]\n"
"ds_read_b128 v[204:207],v81 offset:38912\n"
"v_mfma_f32_16x16x32_bf16 a[20:23],v[244:247],v[192:195],a[20:23]\n"
"ds_read_b128 v[144:147],v80 offset:40960\n"
"v_mfma_f32_16x16x32_bf16 a[20:23],v[180:183],v[128:131],a[20:23]\n"
"ds_read_b128 v[208:211],v81 offset:40960\n"
"s_waitcnt lgkmcnt(8)\n"
"v_mfma_f32_16x16x32_bf16 a[24:27],v[184:187],v[128:131],a[24:27]\n"
"ds_read_b128 v[148:151],v80 offset:43008\n"
"v_mfma_f32_16x16x32_bf16 a[24:27],v[248:251],v[192:195],a[24:27]\n"
"ds_read_b128 v[212:215],v81 offset:43008\n"
"v_subrev_u32 v98,s82,v98\n"
"v_mfma_f32_16x16x32_bf16 a[28:31],v[252:255],v[192:195],a[28:31]\n"
"ds_read_b128 v[216:219],v81 offset:45056\n"
"v_add_u32 v96,s78,v97\n"
"v_mfma_f32_16x16x32_bf16 a[28:31],v[188:191],v[128:131],a[28:31]\n"
"ds_read_b128 v[220:223],v81 offset:47104\n"
"s_waitcnt lgkmcnt(10)\n"
"v_mfma_f32_16x16x32_bf16 a[32:35],v[160:163],v[132:135],a[32:35]\n"
"v_add_u32 v97,65536,v96\n"
"v_mfma_f32_16x16x32_bf16 a[32:35],v[224:227],v[196:199],a[32:35]\n"
"v_subrev_u32 v99,s82,v99\n"
"v_mfma_f32_16x16x32_bf16 a[36:39],v[228:231],v[196:199],a[36:39]\n"
"v_mfma_f32_16x16x32_bf16 a[36:39],v[164:167],v[132:135],a[36:39]\n"
"v_mfma_f32_16x16x32_bf16 a[40:43],v[168:171],v[132:135],a[40:43]\n"
"s_waitcnt lgkmcnt(0)\n"
"s_barrier\n"
"v_mfma_f32_16x16x32_bf16 a[40:43],v[232:235],v[196:199],a[40:43]\n"
"v_subrev_u32 v100,s82,v100\n"
"s_add_u32 m0,s85,98304\n"
"v_mfma_f32_16x16x32_bf16 a[44:47],v[236:239],v[196:199],a[44:47]\n"
"buffer_load_dwordx4 v110,s[28:31],s86 offen lds\n"
"v_mfma_f32_16x16x32_bf16 a[44:47],v[172:175],v[132:135],a[44:47]\n"
"v_mfma_f32_16x16x32_bf16 a[48:51],v[176:179],v[132:135],a[48:51]\n"
"v_mfma_f32_16x16x32_bf16 a[48:51],v[240:243],v[196:199],a[48:51]\n"
"v_subrev_u32 v101,s82,v101\n"
"s_add_u32 m0,s85,99328\n"
"v_mfma_f32_16x16x32_bf16 a[52:55],v[244:247],v[196:199],a[52:55]\n"
"buffer_load_dwordx4 v111,s[28:31],s86 offen lds\n"
"v_mfma_f32_16x16x32_bf16 a[52:55],v[180:183],v[132:135],a[52:55]\n"
"s_xor_b32 s86,s86,262144\n"
"v_mfma_f32_16x16x32_bf16 a[56:59],v[184:187],v[132:135],a[56:59]\n"
"v_mfma_f32_16x16x32_bf16 a[56:59],v[248:251],v[196:199],a[56:59]\n"
"v_subrev_u32 v102,s82,v102\n"
"s_add_u32 m0,s78,32768\n"
"v_mfma_f32_16x16x32_bf16 a[60:63],v[252:255],v[196:199],a[60:63]\n"
"buffer_load_dwordx4 v112,s[24:27],s76 offen offset:0 sc1 lds\n"
"v_mfma_f32_16x16x32_bf16 a[60:63],v[188:191],v[132:135],a[60:63]\n"
"v_mfma_f32_16x16x32_bf16 a[64:67],v[160:163],v[136:139],a[64:67]\n"
"v_mfma_f32_16x16x32_bf16 a[64:67],v[224:227],v[200:203],a[64:67]\n"
"v_subrev_u32 v103,s82,v103\n"
"v_mfma_f32_16x16x32_bf16 a[68:71],v[228:231],v[200:203],a[68:71]\n"
"buffer_load_dwordx4 v112,s[24:27],s76 offen offset:1024 sc1 lds\n"
"v_mfma_f32_16x16x32_bf16 a[68:71],v[164:167],v[136:139],a[68:71]\n"
"v_mfma_f32_16x16x32_bf16 a[72:75],v[168:171],v[136:139],a[72:75]\n"
"v_accvgpr_read_b32 v116,a0\n"
"v_accvgpr_read_b32 v117,a1\n"
"v_mfma_f32_16x16x32_bf16 a[72:75],v[232:235],v[200:203],a[72:75]\n"
"v_subrev_u32 v104,s82,v104\n"
"v_accvgpr_read_b32 v118,a2\n"
"v_mfma_f32_16x16x32_bf16 a[76:79],v[236:239],v[200:203],a[76:79]\n"
"buffer_load_dwordx4 v112,s[24:27],s76 offen offset:2048 sc1 lds\n"
"v_mfma_f32_16x16x32_bf16 a[76:79],v[172:175],v[136:139],a[76:79]\n"
"v_accvgpr_read_b32 v119,a3\n"
"v_accvgpr_read_b32 v120,a4\n"
"v_mfma_f32_16x16x32_bf16 a[80:83],v[176:179],v[136:139],a[80:83]\n"
"v_accvgpr_read_b32 v121,a5\n"
"v_accvgpr_read_b32 v122,a6\n"
"v_mfma_f32_16x16x32_bf16 a[80:83],v[240:243],v[200:203],a[80:83]\n"
"v_subrev_u32 v105,s82,v105\n"
"v_accvgpr_read_b32 v123,a7\n"
"v_mfma_f32_16x16x32_bf16 a[84:87],v[244:247],v[200:203],a[84:87]\n"
"buffer_load_dwordx4 v112,s[24:27],s76 offen offset:3072 sc1 lds\n"
"v_mfma_f32_16x16x32_bf16 a[84:87],v[180:183],v[136:139],a[84:87]\n"
"s_xor_b32 s76,s76,16384\n"
"v_cvt_pk_bf16_f32 v116,v116,v117\n"
"v_cvt_pk_bf16_f32 v117,v118,v119\n"
"v_mfma_f32_16x16x32_bf16 a[88:91],v[184:187],v[136:139],a[88:91]\n"
"v_cvt_pk_bf16_f32 v118,v120,v121\n"
"v_cvt_pk_bf16_f32 v119,v122,v123\n"
"v_mfma_f32_16x16x32_bf16 a[88:91],v[248:251],v[200:203],a[88:91]\n"
"buffer_store_dwordx4 v[116:119],v112,s[24:27],s56 offen offset:0 sc1\n"
"v_mfma_f32_16x16x32_bf16 a[92:95],v[252:255],v[200:203],a[92:95]\n"
"s_nop 0\n"
"v_accvgpr_read_b32 v116,a8\n"
"v_accvgpr_read_b32 v117,a9\n"
"v_accvgpr_read_b32 v118,a10\n"
"v_mfma_f32_16x16x32_bf16 a[92:95],v[188:191],v[136:139],a[92:95]\n"
"s_waitcnt vmcnt(20)\n"
"v_accvgpr_read_b32 v119,a11\n"
"v_accvgpr_read_b32 v120,a12\n"
"s_barrier\n"
"v_mfma_f32_16x16x32_bf16 a[96:99],v[160:163],v[140:143],a[96:99]\n"
"ds_read_b128 v[0:3],v97 offset:0\n"
"v_accvgpr_read_b32 v121,a13\n"
"v_mfma_f32_16x16x32_bf16 a[96:99],v[224:227],v[204:207],a[96:99]\n"
"ds_read_b128 v[8:11],v97 offset:1024\n"
"v_accvgpr_read_b32 v122,a14\n"
"v_mfma_f32_16x16x32_bf16 a[100:103],v[228:231],v[204:207],a[100:103]\n"
"ds_read_b128 v[16:19],v97 offset:2048\n"
"v_accvgpr_read_b32 v123,a15\n"
"v_mfma_f32_16x16x32_bf16 a[100:103],v[164:167],v[140:143],a[100:103]\n"
"ds_read_b128 v[24:27],v97 offset:3072\n"
"v_cvt_pk_bf16_f32 v116,v116,v117\n"
"v_mfma_f32_16x16x32_bf16 a[104:107],v[168:171],v[140:143],a[104:107]\n"
"ds_read_b128 v[4:7],v97 offset:16384\n"
"v_cvt_pk_bf16_f32 v117,v118,v119\n"
"v_mfma_f32_16x16x32_bf16 a[104:107],v[232:235],v[204:207],a[104:107]\n"
"ds_read_b128 v[12:15],v97 offset:17408\n"
"v_cvt_pk_bf16_f32 v118,v120,v121\n"
"v_mfma_f32_16x16x32_bf16 a[108:111],v[236:239],v[204:207],a[108:111]\n"
"ds_read_b128 v[20:23],v97 offset:18432\n"
"v_cvt_pk_bf16_f32 v119,v122,v123\n"
"v_mfma_f32_16x16x32_bf16 a[108:111],v[172:175],v[140:143],a[108:111]\n"
"ds_read_b128 v[28:31],v97 offset:19456\n"
"buffer_store_dwordx4 v[116:119],v112,s[24:27],s56 offen offset:1024 sc1\n"
"v_mfma_f32_16x16x32_bf16 a[112:115],v[176:179],v[140:143],a[112:115]\n"
"ds_read_b64_tr_b16 v[32:33],v98 offset:49152\n"
"v_accvgpr_read_b32 v116,a16\n"
"v_mfma_f32_16x16x32_bf16 a[112:115],v[240:243],v[204:207],a[112:115]\n"
"ds_read_b64_tr_b16 v[34:35],v98 offset:50176\n"
"v_accvgpr_read_b32 v117,a17\n"
"v_mfma_f32_16x16x32_bf16 a[116:119],v[244:247],v[204:207],a[116:119]\n"
"ds_read_b64_tr_b16 v[36:37],v98 offset:57344\n"
"v_accvgpr_read_b32 v118,a18\n"
"v_mfma_f32_16x16x32_bf16 a[116:119],v[180:183],v[140:143],a[116:119]\n"
"ds_read_b64_tr_b16 v[38:39],v98 offset:58368\n"
"v_accvgpr_read_b32 v119,a19\n"
"v_mfma_f32_16x16x32_bf16 a[120:123],v[184:187],v[140:143],a[120:123]\n"
"ds_read_b64_tr_b16 v[40:41],v99 offset:49152\n"
"v_accvgpr_read_b32 v120,a20\n"
"v_mfma_f32_16x16x32_bf16 a[120:123],v[248:251],v[204:207],a[120:123]\n"
"ds_read_b64_tr_b16 v[42:43],v99 offset:50176\n"
"v_accvgpr_read_b32 v121,a21\n"
"v_mfma_f32_16x16x32_bf16 a[124:127],v[252:255],v[204:207],a[124:127]\n"
"ds_read_b64_tr_b16 v[44:45],v99 offset:57344\n"
"v_accvgpr_read_b32 v122,a22\n"
"v_mfma_f32_16x16x32_bf16 a[124:127],v[188:191],v[140:143],a[124:127]\n"
"ds_read_b64_tr_b16 v[46:47],v99 offset:58368\n"
"v_accvgpr_read_b32 v123,a23\n"
"v_mfma_f32_16x16x32_bf16 a[128:131],v[160:163],v[144:147],a[128:131]\n"
"ds_read_b64_tr_b16 v[48:49],v100 offset:49152\n"
"v_cvt_pk_bf16_f32 v116,v116,v117\n"
"v_mfma_f32_16x16x32_bf16 a[128:131],v[224:227],v[208:211],a[128:131]\n"
"ds_read_b64_tr_b16 v[50:51],v100 offset:50176\n"
"v_cvt_pk_bf16_f32 v117,v118,v119\n"
"v_mfma_f32_16x16x32_bf16 a[132:135],v[228:231],v[208:211],a[132:135]\n"
"ds_read_b64_tr_b16 v[52:53],v100 offset:57344\n"
"v_cvt_pk_bf16_f32 v118,v120,v121\n"
"v_mfma_f32_16x16x32_bf16 a[132:135],v[164:167],v[144:147],a[132:135]\n"
"ds_read_b64_tr_b16 v[54:55],v100 offset:58368\n"
"v_cvt_pk_bf16_f32 v119,v122,v123\n"
"v_mfma_f32_16x16x32_bf16 a[136:139],v[168:171],v[144:147],a[136:139]\n"
"ds_read_b64_tr_b16 v[56:57],v101 offset:49152\n"
"buffer_store_dwordx4 v[116:119],v112,s[24:27],s56 offen offset:2048 sc1\n"
"v_mfma_f32_16x16x32_bf16 a[136:139],v[232:235],v[208:211],a[136:139]\n"
"ds_read_b64_tr_b16 v[58:59],v101 offset:50176\n"
"v_accvgpr_read_b32 v116,a24\n"
"v_mfma_f32_16x16x32_bf16 a[140:143],v[236:239],v[208:211],a[140:143]\n"
"ds_read_b64_tr_b16 v[60:61],v101 offset:57344\n"
"v_accvgpr_read_b32 v117,a25\n"
"v_mfma_f32_16x16x32_bf16 a[140:143],v[172:175],v[144:147],a[140:143]\n"
"ds_read_b64_tr_b16 v[62:63],v101 offset:58368\n"
"v_accvgpr_read_b32 v118,a26\n"
"v_mfma_f32_16x16x32_bf16 a[144:147],v[176:179],v[144:147],a[144:147]\n"
"ds_read_b64_tr_b16 v[64:65],v102 offset:49152\n"
"v_accvgpr_read_b32 v119,a27\n"
"v_mfma_f32_16x16x32_bf16 a[144:147],v[240:243],v[208:211],a[144:147]\n"
"ds_read_b64_tr_b16 v[66:67],v102 offset:50176\n"
"v_accvgpr_read_b32 v120,a28\n"
"v_mfma_f32_16x16x32_bf16 a[148:151],v[244:247],v[208:211],a[148:151]\n"
"ds_read_b64_tr_b16 v[68:69],v102 offset:57344\n"
"v_accvgpr_read_b32 v121,a29\n"
"v_mfma_f32_16x16x32_bf16 a[148:151],v[180:183],v[144:147],a[148:151]\n"
"ds_read_b64_tr_b16 v[70:71],v102 offset:58368\n"
"v_accvgpr_read_b32 v122,a30\n"
"v_mfma_f32_16x16x32_bf16 a[152:155],v[184:187],v[144:147],a[152:155]\n"
"ds_read_b64_tr_b16 v[72:73],v103 offset:49152\n"
"v_accvgpr_read_b32 v123,a31\n"
"v_mfma_f32_16x16x32_bf16 a[152:155],v[248:251],v[208:211],a[152:155]\n"
"ds_read_b64_tr_b16 v[74:75],v103 offset:50176\n"
"v_cvt_pk_bf16_f32 v116,v116,v117\n"
"v_mfma_f32_16x16x32_bf16 a[156:159],v[252:255],v[208:211],a[156:159]\n"
"ds_read_b64_tr_b16 v[76:77],v103 offset:57344\n"
"v_cvt_pk_bf16_f32 v117,v118,v119\n"
"v_mfma_f32_16x16x32_bf16 a[156:159],v[188:191],v[144:147],a[156:159]\n"
"ds_read_b64_tr_b16 v[78:79],v103 offset:58368\n"
"v_cvt_pk_bf16_f32 v118,v120,v121\n"
"v_mfma_f32_16x16x32_bf16 a[160:163],v[160:163],v[148:151],a[160:163]\n"
"ds_read_b64_tr_b16 v[80:81],v104 offset:49152\n"
"v_cvt_pk_bf16_f32 v119,v122,v123\n"
"v_mfma_f32_16x16x32_bf16 a[160:163],v[224:227],v[212:215],a[160:163]\n"
"ds_read_b64_tr_b16 v[82:83],v104 offset:50176\n"
"buffer_store_dwordx4 v[116:119],v112,s[24:27],s56 offen offset:3072 sc1\n"
"v_mfma_f32_16x16x32_bf16 a[164:167],v[228:231],v[212:215],a[164:167]\n"
"ds_read_b64_tr_b16 v[84:85],v104 offset:57344\n"
"v_mfma_f32_16x16x32_bf16 a[164:167],v[164:167],v[148:151],a[164:167]\n"
"ds_read_b64_tr_b16 v[86:87],v104 offset:58368\n"
"v_mfma_f32_16x16x32_bf16 a[168:171],v[168:171],v[148:151],a[168:171]\n"
"ds_read_b64_tr_b16 v[88:89],v105 offset:49152\n"
"v_mfma_f32_16x16x32_bf16 a[168:171],v[232:235],v[212:215],a[168:171]\n"
"ds_read_b64_tr_b16 v[90:91],v105 offset:50176\n"
"v_mfma_f32_16x16x32_bf16 a[172:175],v[236:239],v[212:215],a[172:175]\n"
"ds_read_b64_tr_b16 v[92:93],v105 offset:57344\n"
"v_mfma_f32_16x16x32_bf16 a[172:175],v[172:175],v[148:151],a[172:175]\n"
"ds_read_b64_tr_b16 v[94:95],v105 offset:58368\n"
"v_mfma_f32_16x16x32_bf16 a[176:179],v[176:179],v[148:151],a[176:179]\n"
"s_add_u32 m0,s85,106496\n"
"v_mfma_f32_16x16x32_bf16 a[176:179],v[240:243],v[212:215],a[176:179]\n"
"buffer_load_dwordx4 v110,s[28:31],s86 offen lds\n"
"v_mfma_f32_16x16x32_bf16 a[180:183],v[244:247],v[212:215],a[180:183]\n"
"v_mfma_f32_16x16x32_bf16 a[180:183],v[180:183],v[148:151],a[180:183]\n"
"s_add_u32 m0,s85,107520\n"
"v_mfma_f32_16x16x32_bf16 a[184:187],v[184:187],v[148:151],a[184:187]\n"
"buffer_load_dwordx4 v111,s[28:31],s86 offen lds\n"
"v_mfma_f32_16x16x32_bf16 a[184:187],v[248:251],v[212:215],a[184:187]\n"
"s_xor_b32 s86,s86,786432\n"
"v_mfma_f32_16x16x32_bf16 a[188:191],v[252:255],v[212:215],a[188:191]\n"
"s_add_u32 m0,s78,49152\n"
"v_mfma_f32_16x16x32_bf16 a[188:191],v[188:191],v[148:151],a[188:191]\n"
"buffer_load_dwordx4 v112,s[24:27],s76 offen offset:0 sc1 lds\n"
"v_mfma_f32_16x16x32_bf16 a[192:195],v[224:227],v[216:219],a[192:195]\n"
"v_mfma_f32_16x16x32_bf16 a[224:227],v[224:227],v[220:223],a[224:227]\n"
"v_mfma_f32_16x16x32_bf16 a[228:231],v[228:231],v[220:223],a[228:231]\n"
"buffer_load_dwordx4 v112,s[24:27],s76 offen offset:1024 sc1 lds\n"
"v_mfma_f32_16x16x32_bf16 a[196:199],v[228:231],v[216:219],a[196:199]\n"
"v_mfma_f32_16x16x32_bf16 a[200:203],v[232:235],v[216:219],a[200:203]\n"
"v_mfma_f32_16x16x32_bf16 a[232:235],v[232:235],v[220:223],a[232:235]\n"
"buffer_load_dwordx4 v112,s[24:27],s76 offen offset:2048 sc1 lds\n"
"v_mfma_f32_16x16x32_bf16 a[236:239],v[236:239],v[220:223],a[236:239]\n"
"v_mfma_f32_16x16x32_bf16 a[204:207],v[236:239],v[216:219],a[204:207]\n"
"buffer_load_dwordx4 v112,s[24:27],s76 offen offset:3072 sc1 lds\n"
"v_mfma_f32_16x16x32_bf16 a[208:211],v[240:243],v[216:219],a[208:211]\n"
"s_xor_b32 s76,s76,49152\n"
"v_mfma_f32_16x16x32_bf16 a[240:243],v[240:243],v[220:223],a[240:243]\n"
"v_mfma_f32_16x16x32_bf16 a[244:247],v[244:247],v[220:223],a[244:247]\n"
"v_mfma_f32_16x16x32_bf16 a[212:215],v[244:247],v[216:219],a[212:215]\n"
"v_mfma_f32_16x16x32_bf16 a[216:219],v[248:251],v[216:219],a[216:219]\n"
"v_mfma_f32_16x16x32_bf16 a[248:251],v[248:251],v[220:223],a[248:251]\n"
"v_mfma_f32_16x16x32_bf16 a[252:255],v[252:255],v[220:223],a[252:255]\n"
"v_mfma_f32_16x16x32_bf16 a[220:223],v[252:255],v[216:219],a[220:223]\n"
"s_branch .Lfk_ph2\n"
".Lfk_v:\n"
"s_waitcnt vmcnt(11)\n"
"s_barrier\n"
"ds_read_b128 v[152:155],v80 offset:12288\n"
"ds_read_b128 v[156:159],v80 offset:14336\n"
"ds_read_b64_tr_b16 v[160:161],v98 offset:49152\n"
"ds_read_b64_tr_b16 v[162:163],v98 offset:50176\n"
"ds_read_b64_tr_b16 v[164:165],v99 offset:49152\n"
"ds_read_b64_tr_b16 v[166:167],v99 offset:50176\n"
"ds_read_b64_tr_b16 v[168:169],v100 offset:49152\n"
"ds_read_b64_tr_b16 v[170:171],v100 offset:50176\n"
"ds_read_b64_tr_b16 v[172:173],v101 offset:49152\n"
"ds_read_b64_tr_b16 v[174:175],v101 offset:50176\n"
"ds_read_b64_tr_b16 v[176:177],v102 offset:49152\n"
"ds_read_b64_tr_b16 v[178:179],v102 offset:50176\n"
"ds_read_b64_tr_b16 v[180:181],v103 offset:49152\n"
"ds_read_b64_tr_b16 v[182:183],v103 offset:50176\n"
"ds_read_b64_tr_b16 v[184:185],v104 offset:49152\n"
"ds_read_b64_tr_b16 v[186:187],v104 offset:50176\n"
"ds_read_b64_tr_b16 v[188:189],v105 offset:49152\n"
"ds_read_b64_tr_b16 v[190:191],v105 offset:50176\n"
"s_waitcnt lgkmcnt(12)\n"
"v_mfma_f32_16x16x32_bf16 a[192:195],v[152:155],v[160:163],0\n"
"s_and_b32 s91,s89,7\n"
"s_mov_b32 s76,128\n"
"s_add_u32 m0,s79,32768\n"
"v_mfma_f32_16x16x32_bf16 a[196:199],v[156:159],v[160:163],0\n"
"buffer_load_dwordx4 v106,s[16:19],s76 offen offset:0 lds\n"
"v_mfma_f32_16x16x32_bf16 a[204:207],v[156:159],v[164:167],0\n"
"s_lshr_b32 s97,s89,3\n"
"s_lshl2_add_u32 s90,s88,s97\n"
"v_mfma_f32_16x16x32_bf16 a[200:203],v[152:155],v[164:167],0\n"
"buffer_load_dwordx4 v107,s[16:19],s76 offen offset:1024 lds\n"
"s_waitcnt lgkmcnt(8)\n"
"v_mfma_f32_16x16x32_bf16 a[208:211],v[152:155],v[168:171],0\n"
"s_lshl_b32 vcc_lo,s90,8\n"
"s_add_u32 s28,s28,vcc_lo\n"
"s_addc_u32 s29,s29,0\n"
"v_mfma_f32_16x16x32_bf16 a[212:215],v[156:159],v[168:171],0\n"
"buffer_load_dwordx4 v108,s[16:19],s76 offen offset:2048 lds\n"
"v_mfma_f32_16x16x32_bf16 a[220:223],v[156:159],v[172:175],0\n"
"s_sub_u32 s30,8388608,vcc_lo\n"
"s_mov_b32 s31,0x20000\n"
"buffer_load_dwordx4 v109,s[16:19],s76 offen offset:3072 lds\n"
"v_mfma_f32_16x16x32_bf16 a[216:219],v[152:155],v[172:175],0\n"
"s_waitcnt vmcnt(9)\n"
"s_barrier\n"
"s_waitcnt lgkmcnt(4)\n"
"v_mfma_f32_16x16x32_bf16 a[224:227],v[152:155],v[176:179],0\n"
"ds_read_b128 v[128:131],v80 offset:0\n"
"ds_read_b128 v[192:195],v81 offset:0\n"
"v_mfma_f32_16x16x32_bf16 a[228:231],v[156:159],v[176:179],0\n"
"ds_read_b64_tr_b16 v[224:225],v98 offset:32768\n"
"ds_read_b64_tr_b16 v[226:227],v98 offset:33792\n"
"v_mfma_f32_16x16x32_bf16 a[236:239],v[156:159],v[180:183],0\n"
"ds_read_b64_tr_b16 v[228:229],v99 offset:32768\n"
"ds_read_b64_tr_b16 v[230:231],v99 offset:33792\n"
"v_mfma_f32_16x16x32_bf16 a[232:235],v[152:155],v[180:183],0\n"
"ds_read_b64_tr_b16 v[232:233],v100 offset:32768\n"
"ds_read_b64_tr_b16 v[234:235],v100 offset:33792\n"
"s_waitcnt lgkmcnt(8)\n"
"v_mfma_f32_16x16x32_bf16 a[240:243],v[152:155],v[184:187],0\n"
"ds_read_b64_tr_b16 v[236:237],v101 offset:32768\n"
"ds_read_b64_tr_b16 v[238:239],v101 offset:33792\n"
"v_mfma_f32_16x16x32_bf16 a[244:247],v[156:159],v[184:187],0\n"
"ds_read_b64_tr_b16 v[240:241],v102 offset:32768\n"
"ds_read_b64_tr_b16 v[242:243],v102 offset:33792\n"
"v_mfma_f32_16x16x32_bf16 a[252:255],v[156:159],v[188:191],0\n"
"ds_read_b64_tr_b16 v[244:245],v103 offset:32768\n"
"ds_read_b64_tr_b16 v[246:247],v103 offset:33792\n"
"v_mfma_f32_16x16x32_bf16 a[248:251],v[152:155],v[188:191],0\n"
"ds_read_b64_tr_b16 v[248:249],v104 offset:32768\n"
"ds_read_b64_tr_b16 v[250:251],v104 offset:33792\n"
"s_waitcnt lgkmcnt(10)\n"
"v_mfma_f32_16x16x32_bf16 a[0:3],v[128:131],v[160:163],0\n"
"ds_read_b64_tr_b16 v[252:253],v105 offset:32768\n"
"ds_read_b64_tr_b16 v[254:255],v105 offset:33792\n"
"v_mfma_f32_16x16x32_bf16 a[0:3],v[192:195],v[224:227],a[0:3]\n"
"ds_read_b128 v[132:135],v80 offset:2048\n"
"ds_read_b128 v[196:199],v81 offset:2048\n"
"v_mfma_f32_16x16x32_bf16 a[8:11],v[192:195],v[228:231],0\n"
"ds_read_b128 v[216:219],v81 offset:12288\n"
"ds_read_b128 v[220:223],v81 offset:14336\n"
"v_mfma_f32_16x16x32_bf16 a[8:11],v[128:131],v[164:167],a[8:11]\n"
"s_waitcnt lgkmcnt(12)\n"
"s_mov_b32 s76,262272\n"
"s_add_u32 m0,s79,36864\n"
"v_mfma_f32_16x16x32_bf16 a[16:19],v[128:131],v[168:171],0\n"
"buffer_load_dwordx4 v106,s[16:19],s76 offen offset:0 lds\n"
"v_mfma_f32_16x16x32_bf16 a[16:19],v[192:195],v[232:235],a[16:19]\n"
"s_mov_b32 s60,s91\n"
"s_sub_u32 s94,15,s91\n"
"v_mfma_f32_16x16x32_bf16 a[24:27],v[192:195],v[236:239],0\n"
"buffer_load_dwordx4 v107,s[16:19],s76 offen offset:1024 lds\n"
"v_mfma_f32_16x16x32_bf16 a[24:27],v[128:131],v[172:175],a[24:27]\n"
"s_cmp_lt_u32 s52,2\n"
"s_cselect_b32 s95,s94,s91\n"
"s_waitcnt lgkmcnt(8)\n"
"v_mfma_f32_16x16x32_bf16 a[32:35],v[128:131],v[176:179],0\n"
"buffer_load_dwordx4 v108,s[16:19],s76 offen offset:2048 lds\n"
"v_mfma_f32_16x16x32_bf16 a[32:35],v[192:195],v[240:243],a[32:35]\n"
"s_lshr_b32 vcc_lo,s95,1\n"
"s_lshl_b32 s55,vcc_lo,19\n"
"v_mfma_f32_16x16x32_bf16 a[40:43],v[192:195],v[244:247],0\n"
"buffer_load_dwordx4 v109,s[16:19],s76 offen offset:3072 lds\n"
"v_mfma_f32_16x16x32_bf16 a[40:43],v[128:131],v[180:183],a[40:43]\n"
"s_waitcnt lgkmcnt(4)\n"
"s_mov_b32 s77,524288\n"
"s_add_u32 m0,s78,98304\n"
"v_mfma_f32_16x16x32_bf16 a[48:51],v[128:131],v[184:187],0\n"
"buffer_load_dwordx4 v110,s[20:23],s77 offen offset:0 lds\n"
"v_mfma_f32_16x16x32_bf16 a[48:51],v[192:195],v[248:251],a[48:51]\n"
"s_lshl_b32 s58,vcc_lo,10\n"
"s_add_u32 s58,s58,148898816\n"
"v_mfma_f32_16x16x32_bf16 a[56:59],v[192:195],v[252:255],0\n"
"buffer_load_dwordx4 v111,s[20:23],s77 offen offset:1024 lds\n"
"v_mfma_f32_16x16x32_bf16 a[56:59],v[128:131],v[188:191],a[56:59]\n"
"s_and_b32 vcc_lo,s95,1\n"
"s_waitcnt lgkmcnt(2)\n"
"v_mfma_f32_16x16x32_bf16 a[4:7],v[132:135],v[160:163],0\n"
"buffer_load_dwordx4 v113,s[20:23],s77 offen offset:2048 lds\n"
"v_mfma_f32_16x16x32_bf16 a[4:7],v[196:199],v[224:227],a[4:7]\n"
"s_lshl_b32 vcc_lo,vcc_lo,13\n"
"s_and_b32 vcc_hi,s78,4096\n"
"v_mfma_f32_16x16x32_bf16 a[12:15],v[196:199],v[228:231],0\n"
"buffer_load_dwordx4 v114,s[20:23],s77 offen offset:3072 lds\n"
"v_mfma_f32_16x16x32_bf16 a[12:15],v[132:135],v[164:167],a[12:15]\n"
"s_waitcnt vmcnt(13)\n"
"s_barrier\n"
"v_mfma_f32_16x16x32_bf16 a[20:23],v[132:135],v[168:171],0\n"
"ds_read_b128 v[136:139],v80 offset:4096\n"
"ds_read_b128 v[200:203],v81 offset:4096\n"
"v_mfma_f32_16x16x32_bf16 a[20:23],v[196:199],v[232:235],a[20:23]\n"
"ds_read_b128 v[140:143],v80 offset:6144\n"
"ds_read_b128 v[204:207],v81 offset:6144\n"
"v_mfma_f32_16x16x32_bf16 a[28:31],v[196:199],v[236:239],0\n"
"ds_read_b128 v[144:147],v80 offset:8192\n"
"ds_read_b128 v[208:211],v81 offset:8192\n"
"v_mfma_f32_16x16x32_bf16 a[28:31],v[132:135],v[172:175],a[28:31]\n"
"ds_read_b128 v[148:151],v80 offset:10240\n"
"ds_read_b128 v[212:215],v81 offset:10240\n"
"v_mfma_f32_16x16x32_bf16 a[36:39],v[132:135],v[176:179],0\n"
"s_add_u32 s55,s55,vcc_lo\n"
"s_mov_b32 s77,786432\n"
"s_add_u32 m0,s78,114688\n"
"v_mfma_f32_16x16x32_bf16 a[36:39],v[196:199],v[240:243],a[36:39]\n"
"buffer_load_dwordx4 v110,s[20:23],s77 offen offset:0 lds\n"
"v_mfma_f32_16x16x32_bf16 a[44:47],v[196:199],v[244:247],0\n"
"s_add_u32 s55,s55,vcc_hi\n"
"s_and_b32 vcc_lo,s89,2\n"
"s_lshl2_add_u32 vcc_lo,s88,vcc_lo\n"
"v_mfma_f32_16x16x32_bf16 a[44:47],v[132:135],v[180:183],a[44:47]\n"
"buffer_load_dwordx4 v111,s[20:23],s77 offen offset:1024 lds\n"
"v_mfma_f32_16x16x32_bf16 a[52:55],v[132:135],v[184:187],0\n"
"s_and_b32 vcc_hi,s52,1\n"
"buffer_load_dwordx4 v113,s[20:23],s77 offen offset:2048 lds\n"
"v_mfma_f32_16x16x32_bf16 a[52:55],v[196:199],v[248:251],a[52:55]\n"
"s_waitcnt lgkmcnt(0)\n"
"s_barrier\n"
"v_mfma_f32_16x16x32_bf16 a[60:63],v[196:199],v[252:255],0\n"
"s_add_u32 vcc_lo,vcc_lo,vcc_hi\n"
"s_lshr_b32 vcc_hi,s89,2\n"
"v_mfma_f32_16x16x32_bf16 a[60:63],v[132:135],v[188:191],a[60:63]\n"
"s_lshr_b32 s84,s52,1\n"
"s_lshl1_add_u32 vcc_hi,vcc_hi,s84\n"
"v_mfma_f32_16x16x32_bf16 a[64:67],v[136:139],v[160:163],0\n"
"buffer_load_dwordx4 v114,s[20:23],s77 offen offset:3072 lds\n"
"v_mfma_f32_16x16x32_bf16 a[64:67],v[200:203],v[224:227],a[64:67]\n"
"s_mul_i32 s56,vcc_lo,1179648\n"
"s_lshl_b32 s84,s84,19\n"
"v_mfma_f32_16x16x32_bf16 a[72:75],v[200:203],v[228:231],0\n"
"s_add_u32 s56,s56,s84\n"
"s_mov_b32 s76,256\n"
"s_add_u32 m0,s79,65536\n"
"v_mfma_f32_16x16x32_bf16 a[72:75],v[136:139],v[164:167],a[72:75]\n"
"buffer_load_dwordx4 v106,s[16:19],s76 offen offset:0 lds\n"
"v_mfma_f32_16x16x32_bf16 a[80:83],v[136:139],v[168:171],0\n"
"s_lshr_b32 s84,s89,2\n"
"s_lshl_b32 s84,s84,14\n"
"v_mfma_f32_16x16x32_bf16 a[80:83],v[200:203],v[232:235],a[80:83]\n"
"s_add_u32 s56,s56,s84\n"
"s_lshl_b32 s84,s53,13\n"
"v_mfma_f32_16x16x32_bf16 a[88:91],v[200:203],v[236:239],0\n"
"buffer_load_dwordx4 v107,s[16:19],s76 offen offset:1024 lds\n"
"v_mfma_f32_16x16x32_bf16 a[88:91],v[136:139],v[172:175],a[88:91]\n"
"s_add_u32 s56,s56,s84\n"
"s_add_u32 s56,s56,4194304\n"
"v_mfma_f32_16x16x32_bf16 a[96:99],v[136:139],v[176:179],0\n"
"s_lshl4_add_u32 vcc_hi,s53,vcc_hi\n"
"s_lshl_b32 vcc_lo,vcc_lo,8\n"
"v_mfma_f32_16x16x32_bf16 a[96:99],v[200:203],v[240:243],a[96:99]\n"
"buffer_load_dwordx4 v108,s[16:19],s76 offen offset:2048 lds\n"
"v_mfma_f32_16x16x32_bf16 a[104:107],v[200:203],v[244:247],0\n"
"s_lshl3_add_u32 s59,vcc_hi,vcc_lo\n"
"s_add_u32 s59,s59,148914176\n"
"v_mfma_f32_16x16x32_bf16 a[104:107],v[136:139],v[180:183],a[104:107]\n"
"s_lshl_b32 s57,s92,3\n"
"s_add_u32 s57,s57,148898816\n"
"v_mfma_f32_16x16x32_bf16 a[112:115],v[136:139],v[184:187],0\n"
"buffer_load_dwordx4 v109,s[16:19],s76 offen offset:3072 lds\n"
"v_mfma_f32_16x16x32_bf16 a[112:115],v[200:203],v[248:251],a[112:115]\n"
"s_lshl_b32 s63,s90,8\n"
"s_add_u32 s63,s63,148914176\n"
"v_mfma_f32_16x16x32_bf16 a[120:123],v[200:203],v[252:255],0\n"
"s_lshl_b32 s74,s92,11\n"
"s_add_u32 s74,s74,171966464\n"
"v_mfma_f32_16x16x32_bf16 a[120:123],v[136:139],v[188:191],a[120:123]\n"
"s_mul_i32 s96,s90,1179648\n"
"s_add_u32 s96,s96,4194304\n"
"v_mfma_f32_16x16x32_bf16 a[68:71],v[140:143],v[160:163],0\n"
"s_add_u32 s73,s96,s85\n"
"s_mov_b32 s76,262400\n"
"s_add_u32 m0,s79,69632\n"
"v_mfma_f32_16x16x32_bf16 a[68:71],v[204:207],v[224:227],a[68:71]\n"
"buffer_load_dwordx4 v106,s[16:19],s76 offen offset:0 lds\n"
"v_mfma_f32_16x16x32_bf16 a[76:79],v[204:207],v[228:231],0\n"
"s_and_b32 vcc_hi,s78,4096\n"
"s_lshl_b32 vcc_hi,vcc_hi,1\n"
"v_mfma_f32_16x16x32_bf16 a[76:79],v[140:143],v[164:167],a[76:79]\n"
"s_add_u32 vcc_lo,s96,vcc_hi\n"
"s_add_u32 s65,vcc_lo,524288\n"
"v_mfma_f32_16x16x32_bf16 a[84:87],v[140:143],v[168:171],0\n"
"buffer_load_dwordx4 v107,s[16:19],s76 offen offset:1024 lds\n"
"v_mfma_f32_16x16x32_bf16 a[84:87],v[204:207],v[232:235],a[84:87]\n"
"s_lshl_b32 s66,s91,16\n"
"s_sub_u32 s66,vcc_lo,s66\n"
"v_mfma_f32_16x16x32_bf16 a[92:95],v[204:207],v[236:239],0\n"
"s_add_u32 s66,s66,999424\n"
"s_add_u32 s67,vcc_hi,65536\n"
"v_mfma_f32_16x16x32_bf16 a[92:95],v[140:143],v[172:175],a[92:95]\n"
"buffer_load_dwordx4 v108,s[16:19],s76 offen offset:2048 lds\n"
"v_mfma_f32_16x16x32_bf16 a[100:103],v[140:143],v[176:179],0\n"
"s_mul_i32 vcc_lo,vcc_hi,5\n"
"s_sub_u32 vcc_lo,s96,vcc_lo\n"
"v_mfma_f32_16x16x32_bf16 a[100:103],v[204:207],v[240:243],a[100:103]\n"
"s_add_u32 s64,vcc_lo,81920\n"
"s_add_u32 s62,vcc_lo,135168\n"
"s_add_u32 s61,vcc_lo,524288\n"
"v_mfma_f32_16x16x32_bf16 a[108:111],v[204:207],v[244:247],0\n"
"buffer_load_dwordx4 v109,s[16:19],s76 offen offset:3072 lds\n"
"v_mfma_f32_16x16x32_bf16 a[108:111],v[140:143],v[180:183],a[108:111]\n"
"s_cmp_lt_u32 s52,2\n"
"s_cselect_b32 s64,s64,2146435072\n"
"s_cselect_b32 s67,s67,163840\n"
"s_cselect_b32 s65,s65,1\n"
"s_cselect_b32 vcc_lo,4,0\n"
"v_mfma_f32_16x16x32_bf16 a[116:119],v[140:143],v[184:187],0\n"
"s_and_b32 vcc_hi,s52,1\n"
"s_mov_b32 s77,1048576\n"
"s_add_u32 m0,s78,147456\n"
"v_mfma_f32_16x16x32_bf16 a[116:119],v[204:207],v[248:251],a[116:119]\n"
"buffer_load_dwordx4 v110,s[20:23],s77 offen offset:0 lds\n"
"v_mfma_f32_16x16x32_bf16 a[124:127],v[204:207],v[252:255],0\n"
"s_add_u32 vcc_lo,vcc_lo,vcc_hi\n"
"s_lshl_b32 vcc_hi,s90,12\n"
"v_mfma_f32_16x16x32_bf16 a[124:127],v[140:143],v[188:191],a[124:127]\n"
"s_add_u32 vcc_hi,vcc_hi,54525952\n"
"s_lshl3_add_u32 s70,s95,vcc_lo\n"
"v_mfma_f32_16x16x32_bf16 a[128:131],v[144:147],v[160:163],0\n"
"buffer_load_dwordx4 v111,s[20:23],s77 offen offset:1024 lds\n"
"v_mfma_f32_16x16x32_bf16 a[128:131],v[208:211],v[224:227],a[128:131]\n"
"s_lshl_b32 s70,s70,17\n"
"s_add_u32 s70,s70,vcc_hi\n"
"v_mfma_f32_16x16x32_bf16 a[136:139],v[208:211],v[228:231],0\n"
"s_lshl3_add_u32 s68,s94,vcc_lo\n"
"s_lshl_b32 s68,s68,17\n"
"v_mfma_f32_16x16x32_bf16 a[136:139],v[144:147],v[164:167],a[136:139]\n"
"buffer_load_dwordx4 v113,s[20:23],s77 offen offset:2048 lds\n"
"v_mfma_f32_16x16x32_bf16 a[144:147],v[144:147],v[168:171],0\n"
"s_add_u32 s68,s68,vcc_hi\n"
"s_lshr_b32 vcc_lo,s90,3\n"
"v_mfma_f32_16x16x32_bf16 a[144:147],v[208:211],v[232:235],a[144:147]\n"
"s_lshl2_add_u32 vcc_lo,vcc_lo,s52\n"
"s_lshl_b32 vcc_lo,vcc_lo,7\n"
"v_mfma_f32_16x16x32_bf16 a[152:155],v[208:211],v[236:239],0\n"
"buffer_load_dwordx4 v114,s[20:23],s77 offen offset:3072 lds\n"
"v_mfma_f32_16x16x32_bf16 a[152:155],v[144:147],v[172:175],a[152:155]\n"
"s_and_b32 vcc_hi,s90,7\n"
"s_add_u32 vcc_lo,vcc_lo,vcc_hi\n"
"v_mfma_f32_16x16x32_bf16 a[160:163],v[144:147],v[176:179],0\n"
"s_lshl3_add_u32 s69,s91,vcc_lo\n"
"s_mov_b32 s77,1310720\n"
"s_add_u32 m0,s78,131072\n"
"v_mfma_f32_16x16x32_bf16 a[160:163],v[208:211],v[240:243],a[160:163]\n"
"buffer_load_dwordx4 v110,s[20:23],s77 offen offset:0 lds\n"
"v_mfma_f32_16x16x32_bf16 a[168:171],v[208:211],v[244:247],0\n"
"s_lshl_b32 s69,s69,3\n"
"s_add_u32 s69,s69,148930560\n"
"v_mfma_f32_16x16x32_bf16 a[168:171],v[144:147],v[180:183],a[168:171]\n"
"s_lshl3_add_u32 s71,s94,vcc_lo\n"
"s_lshl_b32 s71,s71,3\n"
"v_mfma_f32_16x16x32_bf16 a[176:179],v[144:147],v[184:187],0\n"
"buffer_load_dwordx4 v111,s[20:23],s77 offen offset:1024 lds\n"
"v_mfma_f32_16x16x32_bf16 a[176:179],v[208:211],v[248:251],a[176:179]\n"
"s_add_u32 s71,s71,148930560\n"
"s_mov_b32 s6,8388608\n"
"v_mfma_f32_16x16x32_bf16 a[184:187],v[208:211],v[252:255],0\n"
"s_mov_b32 s7,0x20000\n"
"s_lshr_b32 s94,s88,1\n"
"v_mfma_f32_16x16x32_bf16 a[184:187],v[144:147],v[188:191],a[184:187]\n"
"buffer_load_dwordx4 v113,s[20:23],s77 offen offset:2048 lds\n"
"v_mfma_f32_16x16x32_bf16 a[132:135],v[148:151],v[160:163],0\n"
"s_and_b32 vcc_lo,s91,1\n"
"s_lshl1_add_u32 s95,s97,vcc_lo\n"
"v_mfma_f32_16x16x32_bf16 a[132:135],v[212:215],v[224:227],a[132:135]\n"
"s_and_b32 vcc_lo,s88,1\n"
"s_lshr_b32 vcc_hi,s91,1\n"
"v_mfma_f32_16x16x32_bf16 a[140:143],v[212:215],v[228:231],0\n"
"buffer_load_dwordx4 v114,s[20:23],s77 offen offset:3072 lds\n"
"v_mfma_f32_16x16x32_bf16 a[140:143],v[148:151],v[164:167],a[140:143]\n"
"s_lshl2_add_u32 s96,vcc_lo,vcc_hi\n"
"s_mov_b32 s76,384\n"
"s_add_u32 m0,s79,0\n"
"v_mfma_f32_16x16x32_bf16 a[148:151],v[148:151],v[168:171],0\n"
"buffer_load_dwordx4 v106,s[16:19],s76 offen offset:0 lds\n"
"v_mfma_f32_16x16x32_bf16 a[148:151],v[212:215],v[232:235],a[148:151]\n"
"s_lshr_b32 vcc_lo,s88,2\n"
"s_lshl2_add_u32 s96,vcc_lo,vcc_hi\n"
"v_mfma_f32_16x16x32_bf16 a[156:159],v[212:215],v[236:239],0\n"
"buffer_load_dwordx4 v107,s[16:19],s76 offen offset:1024 lds\n"
"v_mfma_f32_16x16x32_bf16 a[156:159],v[148:151],v[172:175],a[156:159]\n"
"s_sub_u32 vcc_lo,7,s88\n"
"s_min_u32 s94,vcc_lo,s88\n"
"v_mfma_f32_16x16x32_bf16 a[164:167],v[148:151],v[176:179],0\n"
"buffer_load_dwordx4 v108,s[16:19],s76 offen offset:2048 lds\n"
"v_mfma_f32_16x16x32_bf16 a[164:167],v[212:215],v[240:243],a[164:167]\n"
"s_lshr_b32 s94,s88,1\n"
"s_xor_b32 s94,s94,2\n"
"v_mfma_f32_16x16x32_bf16 a[172:175],v[212:215],v[244:247],0\n"
"buffer_load_dwordx4 v109,s[16:19],s76 offen offset:3072 lds\n"
"v_mfma_f32_16x16x32_bf16 a[172:175],v[148:151],v[180:183],a[172:175]\n"
"s_lshl_b32 s96,s88,2\n"
"s_mov_b32 s76,262528\n"
"s_add_u32 m0,s79,4096\n"
"v_mfma_f32_16x16x32_bf16 a[180:183],v[148:151],v[184:187],0\n"
"buffer_load_dwordx4 v106,s[16:19],s76 offen offset:0 lds\n"
"v_mfma_f32_16x16x32_bf16 a[180:183],v[212:215],v[248:251],a[180:183]\n"
"buffer_load_dwordx4 v107,s[16:19],s76 offen offset:1024 lds\n"
"v_mfma_f32_16x16x32_bf16 a[188:191],v[212:215],v[252:255],0\n"
"buffer_load_dwordx4 v108,s[16:19],s76 offen offset:2048 lds\n"
"buffer_load_dwordx4 v109,s[16:19],s76 offen offset:3072 lds\n"
"v_mfma_f32_16x16x32_bf16 a[188:191],v[148:151],v[188:191],a[188:191]\n"
"s_waitcnt vmcnt(24)\n"
"s_barrier\n"
"v_mfma_f32_16x16x32_bf16 a[192:195],v[216:219],v[224:227],a[192:195]\n"
"ds_read_b128 v[152:155],v80 offset:45056\n"
"s_and_b32 s96,s96,4\n"
"v_mfma_f32_16x16x32_bf16 a[196:199],v[220:223],v[224:227],a[196:199]\n"
"ds_read_b128 v[156:159],v80 offset:47104\n"
"buffer_load_dwordx4 v[8:11],v70,s[32:35],0 offen offset:0\n"
"v_mfma_f32_16x16x32_bf16 a[204:207],v[220:223],v[228:231],a[204:207]\n"
"ds_read_b64_tr_b16 v[160:161],v98 offset:0\n"
"buffer_load_dwordx4 v[12:15],v70,s[32:35],0 offen offset:512\n"
"v_mfma_f32_16x16x32_bf16 a[200:203],v[216:219],v[228:231],a[200:203]\n"
"ds_read_b64_tr_b16 v[162:163],v98 offset:1024\n"
"buffer_load_dwordx4 v[16:19],v70,s[32:35],0 offen offset:1024\n"
"v_mfma_f32_16x16x32_bf16 a[208:211],v[216:219],v[232:235],a[208:211]\n"
"ds_read_b64_tr_b16 v[164:165],v99 offset:0\n"
"buffer_load_dwordx4 v[20:23],v70,s[32:35],0 offen offset:1536\n"
"v_mfma_f32_16x16x32_bf16 a[212:215],v[220:223],v[232:235],a[212:215]\n"
"ds_read_b64_tr_b16 v[166:167],v99 offset:1024\n"
"s_mov_b32 s87,524288\n"
"v_mfma_f32_16x16x32_bf16 a[220:223],v[220:223],v[236:239],a[220:223]\n"
"ds_read_b64_tr_b16 v[168:169],v100 offset:0\n"
"buffer_load_dword v115,v112,s[36:39],s87 offen\n"
"v_mfma_f32_16x16x32_bf16 a[216:219],v[216:219],v[236:239],a[216:219]\n"
"ds_read_b64_tr_b16 v[170:171],v100 offset:1024\n"
"s_mov_b32 s87,1048576\n"
"v_mfma_f32_16x16x32_bf16 a[224:227],v[216:219],v[240:243],a[224:227]\n"
"ds_read_b64_tr_b16 v[172:173],v101 offset:0\n"
"buffer_load_dword v115,v112,s[36:39],s87 offen\n"
"v_mfma_f32_16x16x32_bf16 a[228:231],v[220:223],v[240:243],a[228:231]\n"
"ds_read_b64_tr_b16 v[174:175],v101 offset:1024\n"
"s_mov_b32 s87,1572864\n"
"v_mfma_f32_16x16x32_bf16 a[236:239],v[220:223],v[244:247],a[236:239]\n"
"ds_read_b64_tr_b16 v[176:177],v102 offset:0\n"
"buffer_load_dword v115,v112,s[36:39],s87 offen\n"
"v_mfma_f32_16x16x32_bf16 a[232:235],v[216:219],v[244:247],a[232:235]\n"
"ds_read_b64_tr_b16 v[178:179],v102 offset:1024\n"
"s_add_u32 s96,s96,s97\n"
"v_mfma_f32_16x16x32_bf16 a[240:243],v[216:219],v[248:251],a[240:243]\n"
"ds_read_b64_tr_b16 v[180:181],v103 offset:0\n"
"s_add_u32 s95,s91,s96\n"
"v_mfma_f32_16x16x32_bf16 a[244:247],v[220:223],v[248:251],a[244:247]\n"
"ds_read_b64_tr_b16 v[182:183],v103 offset:1024\n"
"s_and_b32 s95,s95,7\n"
"v_mfma_f32_16x16x32_bf16 a[252:255],v[220:223],v[252:255],a[252:255]\n"
"ds_read_b64_tr_b16 v[184:185],v104 offset:0\n"
"s_lshl3_add_u32 vcc_lo,s94,s96\n"
"v_mfma_f32_16x16x32_bf16 a[248:251],v[216:219],v[252:255],a[248:251]\n"
"s_lshl3_add_u32 vcc_lo,vcc_lo,s52\n"
"ds_read_b64_tr_b16 v[186:187],v104 offset:1024\n"
"s_waitcnt lgkmcnt(10)\n"
"v_mfma_f32_16x16x32_bf16 a[192:195],v[152:155],v[160:163],a[192:195]\n"
"ds_read_b64_tr_b16 v[188:189],v105 offset:0\n"
"s_lshl_b32 s13,vcc_lo,15\n"
"v_mfma_f32_16x16x32_bf16 a[196:199],v[156:159],v[160:163],a[196:199]\n"
"ds_read_b64_tr_b16 v[190:191],v105 offset:1024\n"
"s_lshl_b32 vcc_lo,s95,8\n"
"v_mfma_f32_16x16x32_bf16 a[204:207],v[156:159],v[164:167],a[204:207]\n"
"ds_read_b128 v[128:131],v80 offset:32768\n"
"s_add_u32 s13,s13,vcc_lo\n"
"v_mfma_f32_16x16x32_bf16 a[200:203],v[152:155],v[164:167],a[200:203]\n"
"ds_read_b128 v[192:195],v81 offset:32768\n"
"s_waitcnt lgkmcnt(10)\n"
"v_mfma_f32_16x16x32_bf16 a[208:211],v[152:155],v[168:171],a[208:211]\n"
"ds_read_b64_tr_b16 v[224:225],v98 offset:16384\n"
"s_lshl3_add_u32 vcc_hi,s94,s95\n"
"v_mfma_f32_16x16x32_bf16 a[212:215],v[156:159],v[168:171],a[212:215]\n"
"ds_read_b64_tr_b16 v[226:227],v98 offset:17408\n"
"s_lshl_b32 s72,vcc_hi,8\n"
"v_mfma_f32_16x16x32_bf16 a[220:223],v[156:159],v[172:175],a[220:223]\n"
"ds_read_b64_tr_b16 v[228:229],v99 offset:16384\n"
"s_add_u32 s72,s72,149061632\n"
"v_mfma_f32_16x16x32_bf16 a[216:219],v[152:155],v[172:175],a[216:219]\n"
"ds_read_b64_tr_b16 v[230:231],v99 offset:17408\n"
"s_waitcnt lgkmcnt(10)\n"
"v_mfma_f32_16x16x32_bf16 a[224:227],v[152:155],v[176:179],a[224:227]\n"
"s_or_b32 vcc_lo,s96,s52\n"
"ds_read_b64_tr_b16 v[232:233],v100 offset:16384\n"
"s_cselect_b32 s72,2146435072,s72\n"
"v_mfma_f32_16x16x32_bf16 a[228:231],v[156:159],v[176:179],a[228:231]\n"
"ds_read_b64_tr_b16 v[234:235],v100 offset:17408\n"
"s_lshl_b32 s3,vcc_hi,18\n"
"v_mfma_f32_16x16x32_bf16 a[236:239],v[156:159],v[180:183],a[236:239]\n"
"ds_read_b64_tr_b16 v[236:237],v101 offset:16384\n"
"s_lshl_b32 vcc_lo,s96,15\n"
"v_mfma_f32_16x16x32_bf16 a[232:235],v[152:155],v[180:183],a[232:235]\n"
"ds_read_b64_tr_b16 v[238:239],v101 offset:17408\n"
"s_waitcnt lgkmcnt(10)\n"
"v_mfma_f32_16x16x32_bf16 a[240:243],v[152:155],v[184:187],a[240:243]\n"
"ds_read_b64_tr_b16 v[240:241],v102 offset:16384\n"
"s_sub_u32 s3,s3,vcc_lo\n"
"v_mfma_f32_16x16x32_bf16 a[244:247],v[156:159],v[184:187],a[244:247]\n"
"ds_read_b64_tr_b16 v[242:243],v102 offset:17408\n"
"s_add_u32 s3,s3,s78\n"
"v_mfma_f32_16x16x32_bf16 a[252:255],v[156:159],v[188:191],a[252:255]\n"
"ds_read_b64_tr_b16 v[244:245],v103 offset:16384\n"
"s_add_u32 s3,s3,100909056\n"
"v_mfma_f32_16x16x32_bf16 a[248:251],v[152:155],v[188:191],a[248:251]\n"
"ds_read_b64_tr_b16 v[246:247],v103 offset:17408\n"
"s_waitcnt lgkmcnt(8)\n"
"v_mfma_f32_16x16x32_bf16 a[0:3],v[128:131],v[160:163],a[0:3]\n"
"ds_read_b64_tr_b16 v[248:249],v104 offset:16384\n"
"s_lshl3_add_u32 vcc_hi,vcc_hi,s96\n"
"v_mfma_f32_16x16x32_bf16 a[0:3],v[192:195],v[224:227],a[0:3]\n"
"ds_read_b64_tr_b16 v[250:251],v104 offset:17408\n"
"s_lshl2_add_u32 vcc_hi,vcc_hi,s52\n"
"v_mfma_f32_16x16x32_bf16 a[8:11],v[192:195],v[228:231],a[8:11]\n"
"ds_read_b64_tr_b16 v[252:253],v105 offset:16384\n"
"s_lshl_b32 s2,vcc_hi,4\n"
"v_mfma_f32_16x16x32_bf16 a[8:11],v[128:131],v[164:167],a[8:11]\n"
"ds_read_b64_tr_b16 v[254:255],v105 offset:17408\n"
"s_waitcnt lgkmcnt(8)\n"
"v_mfma_f32_16x16x32_bf16 a[16:19],v[128:131],v[168:171],a[16:19]\n"
"ds_read_b128 v[132:135],v80 offset:34816\n"
"s_add_u32 s2,s2,149028864\n"
"v_mfma_f32_16x16x32_bf16 a[16:19],v[192:195],v[232:235],a[16:19]\n"
"ds_read_b128 v[196:199],v81 offset:34816\n"
"s_lshl2_add_u32 vcc_lo,s95,s52\n"
"v_mfma_f32_16x16x32_bf16 a[24:27],v[192:195],v[236:239],a[24:27]\n"
"ds_read_b128 v[136:139],v80 offset:36864\n"
"s_lshl_b32 vcc_lo,vcc_lo,10\n"
"v_mfma_f32_16x16x32_bf16 a[24:27],v[128:131],v[172:175],a[24:27]\n"
"ds_read_b128 v[200:203],v81 offset:36864\n"
"s_waitcnt lgkmcnt(8)\n"
"v_mfma_f32_16x16x32_bf16 a[32:35],v[128:131],v[176:179],a[32:35]\n"
"ds_read_b128 v[140:143],v80 offset:38912\n"
"s_lshl_b32 vcc_hi,s94,22\n"
"v_mfma_f32_16x16x32_bf16 a[32:35],v[192:195],v[240:243],a[32:35]\n"
"ds_read_b128 v[204:207],v81 offset:38912\n"
"s_add_u32 s12,vcc_lo,vcc_hi\n"
"v_mfma_f32_16x16x32_bf16 a[40:43],v[192:195],v[244:247],a[40:43]\n"
"ds_read_b128 v[144:147],v80 offset:40960\n"
"s_lshl_b32 vcc_lo,s96,19\n"
"v_mfma_f32_16x16x32_bf16 a[40:43],v[128:131],v[180:183],a[40:43]\n"
"ds_read_b128 v[208:211],v81 offset:40960\n"
"s_waitcnt lgkmcnt(8)\n"
"v_mfma_f32_16x16x32_bf16 a[48:51],v[128:131],v[184:187],a[48:51]\n"
"ds_read_b128 v[148:151],v80 offset:43008\n"
"s_add_u32 s10,s12,vcc_lo\n"
"v_mfma_f32_16x16x32_bf16 a[48:51],v[192:195],v[248:251],a[48:51]\n"
"ds_read_b128 v[212:215],v81 offset:43008\n"
"s_add_u32 s10,s10,71303168\n"
"v_mfma_f32_16x16x32_bf16 a[56:59],v[192:195],v[252:255],a[56:59]\n"
"ds_read_b128 v[216:219],v81 offset:45056\n"
"s_mul_i32 vcc_lo,s52,31744\n"
"v_mfma_f32_16x16x32_bf16 a[56:59],v[128:131],v[188:191],a[56:59]\n"
"ds_read_b128 v[220:223],v81 offset:47104\n"
"s_waitcnt lgkmcnt(10)\n"
"v_mfma_f32_16x16x32_bf16 a[4:7],v[132:135],v[160:163],a[4:7]\n"
"s_add_u32 s10,s10,vcc_lo\n"
"s_lshl_b32 vcc_lo,s52,10\n"
"v_mfma_f32_16x16x32_bf16 a[4:7],v[196:199],v[224:227],a[4:7]\n"
"s_sub_u32 s12,57671680,vcc_lo\n"
"s_add_u32 s8,s79,54525952\n"
"v_mfma_f32_16x16x32_bf16 a[12:15],v[196:199],v[228:231],a[12:15]\n"
"s_and_b32 vcc_lo,s96,1\n"
"s_lshl_b32 s11,vcc_lo,10\n"
"v_mfma_f32_16x16x32_bf16 a[12:15],v[132:135],v[164:167],a[12:15]\n"
"s_add_u32 s11,s11,148932608\n"
"s_mov_b32 s9,148931072\n"
"v_mfma_f32_16x16x32_bf16 a[20:23],v[132:135],v[168:171],a[20:23]\n"
"s_waitcnt lgkmcnt(0)\n"
"s_barrier\n"
"v_mfma_f32_16x16x32_bf16 a[20:23],v[196:199],v[232:235],a[20:23]\n"
"s_cmp_eq_u32 s52,0\n"
"s_cselect_b32 s71,s71,2146435072\n"
"s_mov_b32 s77,1572864\n"
"s_add_u32 m0,s78,98304\n"
"v_mfma_f32_16x16x32_bf16 a[28:31],v[196:199],v[236:239],a[28:31]\n"
"buffer_load_dwordx4 v110,s[20:23],s77 offen offset:0 lds\n"
"v_mfma_f32_16x16x32_bf16 a[28:31],v[132:135],v[172:175],a[28:31]\n"
"v_mfma_f32_16x16x32_bf16 a[36:39],v[132:135],v[176:179],a[36:39]\n"
"v_mfma_f32_16x16x32_bf16 a[36:39],v[196:199],v[240:243],a[36:39]\n"
"v_mfma_f32_16x16x32_bf16 a[44:47],v[196:199],v[244:247],a[44:47]\n"
"buffer_load_dwordx4 v111,s[20:23],s77 offen offset:1024 lds\n"
"v_mfma_f32_16x16x32_bf16 a[44:47],v[132:135],v[180:183],a[44:47]\n"
"v_mfma_f32_16x16x32_bf16 a[52:55],v[132:135],v[184:187],a[52:55]\n"
"v_mfma_f32_16x16x32_bf16 a[52:55],v[196:199],v[248:251],a[52:55]\n"
"v_mfma_f32_16x16x32_bf16 a[60:63],v[196:199],v[252:255],a[60:63]\n"
"buffer_load_dwordx4 v113,s[20:23],s77 offen offset:2048 lds\n"
"v_mfma_f32_16x16x32_bf16 a[60:63],v[132:135],v[188:191],a[60:63]\n"
"v_mfma_f32_16x16x32_bf16 a[64:67],v[136:139],v[160:163],a[64:67]\n"
"v_mfma_f32_16x16x32_bf16 a[64:67],v[200:203],v[224:227],a[64:67]\n"
"v_mfma_f32_16x16x32_bf16 a[72:75],v[200:203],v[228:231],a[72:75]\n"
"buffer_load_dwordx4 v114,s[20:23],s77 offen offset:3072 lds\n"
"v_mfma_f32_16x16x32_bf16 a[72:75],v[136:139],v[164:167],a[72:75]\n"
"v_mfma_f32_16x16x32_bf16 a[80:83],v[136:139],v[168:171],a[80:83]\n"
"v_mfma_f32_16x16x32_bf16 a[80:83],v[200:203],v[232:235],a[80:83]\n"
"s_mov_b32 s77,1835008\n"
"s_add_u32 m0,s78,114688\n"
"v_mfma_f32_16x16x32_bf16 a[88:91],v[200:203],v[236:239],a[88:91]\n"
"buffer_load_dwordx4 v110,s[20:23],s77 offen offset:0 lds\n"
"v_mfma_f32_16x16x32_bf16 a[88:91],v[136:139],v[172:175],a[88:91]\n"
"v_mfma_f32_16x16x32_bf16 a[96:99],v[136:139],v[176:179],a[96:99]\n"
"v_mfma_f32_16x16x32_bf16 a[96:99],v[200:203],v[240:243],a[96:99]\n"
"v_mfma_f32_16x16x32_bf16 a[104:107],v[200:203],v[244:247],a[104:107]\n"
"buffer_load_dwordx4 v111,s[20:23],s77 offen offset:1024 lds\n"
"v_mfma_f32_16x16x32_bf16 a[104:107],v[136:139],v[180:183],a[104:107]\n"
"v_mfma_f32_16x16x32_bf16 a[112:115],v[136:139],v[184:187],a[112:115]\n"
"v_mfma_f32_16x16x32_bf16 a[112:115],v[200:203],v[248:251],a[112:115]\n"
"v_mfma_f32_16x16x32_bf16 a[120:123],v[200:203],v[252:255],a[120:123]\n"
"buffer_load_dwordx4 v113,s[20:23],s77 offen offset:2048 lds\n"
"v_mfma_f32_16x16x32_bf16 a[120:123],v[136:139],v[188:191],a[120:123]\n"
"s_waitcnt vmcnt(10)\n"
"v_mfma_f32_16x16x32_bf16 a[68:71],v[140:143],v[160:163],a[68:71]\n"
"v_lshlrev_b32 v24,16,v8\n"
"v_and_b32 v25,0xffff0000,v8\n"
"v_mfma_f32_16x16x32_bf16 a[68:71],v[204:207],v[224:227],a[68:71]\n"
"v_lshlrev_b32 v26,16,v9\n"
"v_and_b32 v27,0xffff0000,v9\n"
"v_mfma_f32_16x16x32_bf16 a[76:79],v[204:207],v[228:231],a[76:79]\n"
"buffer_load_dwordx4 v114,s[20:23],s77 offen offset:3072 lds\n"
"v_mfma_f32_16x16x32_bf16 a[76:79],v[140:143],v[164:167],a[76:79]\n"
"v_lshlrev_b32 v28,16,v10\n"
"v_and_b32 v29,0xffff0000,v10\n"
"v_mfma_f32_16x16x32_bf16 a[84:87],v[140:143],v[168:171],a[84:87]\n"
"v_lshlrev_b32 v30,16,v11\n"
"v_and_b32 v31,0xffff0000,v11\n"
"v_mfma_f32_16x16x32_bf16 a[84:87],v[204:207],v[232:235],a[84:87]\n"
"v_lshlrev_b32 v32,16,v12\n"
"v_and_b32 v33,0xffff0000,v12\n"
"v_mfma_f32_16x16x32_bf16 a[92:95],v[204:207],v[236:239],a[92:95]\n"
"s_mov_b32 s76,512\n"
"v_lshlrev_b32 v34,16,v13\n"
"s_add_u32 m0,s79,32768\n"
"v_mfma_f32_16x16x32_bf16 a[92:95],v[140:143],v[172:175],a[92:95]\n"
"buffer_load_dwordx4 v106,s[16:19],s76 offen offset:0 lds\n"
"v_mfma_f32_16x16x32_bf16 a[100:103],v[140:143],v[176:179],a[100:103]\n"
"v_and_b32 v35,0xffff0000,v13\n"
"v_lshlrev_b32 v36,16,v14\n"
"v_mfma_f32_16x16x32_bf16 a[100:103],v[204:207],v[240:243],a[100:103]\n"
"v_and_b32 v37,0xffff0000,v14\n"
"v_lshlrev_b32 v38,16,v15\n"
"v_mfma_f32_16x16x32_bf16 a[108:111],v[204:207],v[244:247],a[108:111]\n"
"v_and_b32 v39,0xffff0000,v15\n"
"v_lshlrev_b32 v40,16,v16\n"
"v_mfma_f32_16x16x32_bf16 a[108:111],v[140:143],v[180:183],a[108:111]\n"
"v_and_b32 v41,0xffff0000,v16\n"
"v_lshlrev_b32 v42,16,v17\n"
"v_mfma_f32_16x16x32_bf16 a[116:119],v[140:143],v[184:187],a[116:119]\n"
"buffer_load_dwordx4 v107,s[16:19],s76 offen offset:1024 lds\n"
"v_mfma_f32_16x16x32_bf16 a[116:119],v[204:207],v[248:251],a[116:119]\n"
"v_and_b32 v43,0xffff0000,v17\n"
"v_lshlrev_b32 v44,16,v18\n"
"v_mfma_f32_16x16x32_bf16 a[124:127],v[204:207],v[252:255],a[124:127]\n"
"v_and_b32 v45,0xffff0000,v18\n"
"v_lshlrev_b32 v46,16,v19\n"
"v_mfma_f32_16x16x32_bf16 a[124:127],v[140:143],v[188:191],a[124:127]\n"
"v_and_b32 v47,0xffff0000,v19\n"
"v_lshlrev_b32 v48,16,v20\n"
"v_mfma_f32_16x16x32_bf16 a[128:131],v[144:147],v[160:163],a[128:131]\n"
"v_and_b32 v49,0xffff0000,v20\n"
"v_lshlrev_b32 v50,16,v21\n"
"v_mfma_f32_16x16x32_bf16 a[128:131],v[208:211],v[224:227],a[128:131]\n"
"buffer_load_dwordx4 v108,s[16:19],s76 offen offset:2048 lds\n"
"v_mfma_f32_16x16x32_bf16 a[136:139],v[208:211],v[228:231],a[136:139]\n"
"v_and_b32 v51,0xffff0000,v21\n"
"v_lshlrev_b32 v52,16,v22\n"
"v_mfma_f32_16x16x32_bf16 a[136:139],v[144:147],v[164:167],a[136:139]\n"
"v_and_b32 v53,0xffff0000,v22\n"
"v_lshlrev_b32 v54,16,v23\n"
"v_mfma_f32_16x16x32_bf16 a[144:147],v[144:147],v[168:171],a[144:147]\n"
"v_and_b32 v55,0xffff0000,v23\n"
"v_add_f32 v56,v24,v25\n"
"v_mfma_f32_16x16x32_bf16 a[144:147],v[208:211],v[232:235],a[144:147]\n"
"v_add_f32 v57,v26,v27\n"
"v_add_f32 v56,v56,v28\n"
"v_mfma_f32_16x16x32_bf16 a[152:155],v[208:211],v[236:239],a[152:155]\n"
"buffer_load_dwordx4 v109,s[16:19],s76 offen offset:3072 lds\n"
"v_mfma_f32_16x16x32_bf16 a[152:155],v[144:147],v[172:175],a[152:155]\n"
"v_add_f32 v57,v57,v29\n"
"v_add_f32 v56,v56,v30\n"
"v_mfma_f32_16x16x32_bf16 a[160:163],v[144:147],v[176:179],a[160:163]\n"
"v_add_f32 v57,v57,v31\n"
"v_add_f32 v56,v56,v32\n"
"v_mfma_f32_16x16x32_bf16 a[160:163],v[208:211],v[240:243],a[160:163]\n"
"v_readfirstlane_b32 s82,v78\n"
"v_readfirstlane_b32 s83,v79\n"
"v_mfma_f32_16x16x32_bf16 a[168:171],v[208:211],v[244:247],a[168:171]\n"
"s_mov_b32 s76,262656\n"
"v_add_f32 v57,v57,v33\n"
"s_add_u32 m0,s79,36864\n"
"v_mfma_f32_16x16x32_bf16 a[168:171],v[144:147],v[180:183],a[168:171]\n"
"buffer_load_dwordx4 v106,s[16:19],s76 offen offset:0 lds\n"
"s_mul_i32 s80,s82,0x9e3779b1\n"
"v_mfma_f32_16x16x32_bf16 a[176:179],v[144:147],v[184:187],a[176:179]\n"
"s_add_u32 s80,s80,0x7f4a7c15\n"
"v_add_f32 v56,v56,v34\n"
"v_add_f32 v57,v57,v35\n"
"v_mfma_f32_16x16x32_bf16 a[176:179],v[208:211],v[248:251],a[176:179]\n"
"s_add_u32 s81,s83,s80\n"
"s_add_u32 s81,s81,1\n"
"v_mfma_f32_16x16x32_bf16 a[184:187],v[208:211],v[252:255],a[184:187]\n"
"v_mov_b32 v124,s80\n"
"v_mov_b32 v125,s81\n"
"v_mfma_f32_16x16x32_bf16 a[184:187],v[144:147],v[188:191],a[184:187]\n"
"v_add_f32 v56,v56,v36\n"
"v_add_f32 v57,v57,v37\n"
"v_mfma_f32_16x16x32_bf16 a[132:135],v[148:151],v[160:163],a[132:135]\n"
"buffer_load_dwordx4 v107,s[16:19],s76 offen offset:1024 lds\n"
"v_mfma_f32_16x16x32_bf16 a[132:135],v[212:215],v[224:227],a[132:135]\n"
"v_add_f32 v56,v56,v38\n"
"v_add_f32 v57,v57,v39\n"
"v_mfma_f32_16x16x32_bf16 a[140:143],v[212:215],v[228:231],a[140:143]\n"
"v_add_f32 v56,v56,v40\n"
"v_add_f32 v57,v57,v41\n"
"v_mfma_f32_16x16x32_bf16 a[140:143],v[148:151],v[164:167],a[140:143]\n"
"v_add_f32 v56,v56,v42\n"
"v_add_f32 v57,v57,v43\n"
"v_mfma_f32_16x16x32_bf16 a[148:151],v[148:151],v[168:171],a[148:151]\n"
"v_add_f32 v56,v56,v44\n"
"v_add_f32 v57,v57,v45\n"
"v_mfma_f32_16x16x32_bf16 a[148:151],v[212:215],v[232:235],a[148:151]\n"
"buffer_load_dwordx4 v108,s[16:19],s76 offen offset:2048 lds\n"
"v_mfma_f32_16x16x32_bf16 a[156:159],v[212:215],v[236:239],a[156:159]\n"
"v_add_f32 v56,v56,v46\n"
"v_add_f32 v57,v57,v47\n"
"v_mfma_f32_16x16x32_bf16 a[156:159],v[148:151],v[172:175],a[156:159]\n"
"v_add_f32 v56,v56,v48\n"
"v_add_f32 v57,v57,v49\n"
"v_mfma_f32_16x16x32_bf16 a[164:167],v[148:151],v[176:179],a[164:167]\n"
"v_add_f32 v56,v56,v50\n"
"v_add_f32 v57,v57,v51\n"
"v_mfma_f32_16x16x32_bf16 a[164:167],v[212:215],v[240:243],a[164:167]\n"
"v_add_f32 v56,v56,v52\n"
"v_add_f32 v57,v57,v53\n"
"v_mfma_f32_16x16x32_bf16 a[172:175],v[212:215],v[244:247],a[172:175]\n"
"buffer_load_dwordx4 v109,s[16:19],s76 offen offset:3072 lds\n"
"v_mfma_f32_16x16x32_bf16 a[172:175],v[148:151],v[180:183],a[172:175]\n"
"s_mov_b32 s87,2097152\n"
"v_add_f32 v56,v56,v54\n"
"v_add_f32 v57,v57,v55\n"
"v_mfma_f32_16x16x32_bf16 a[180:183],v[148:151],v[184:187],a[180:183]\n"
"buffer_load_dword v115,v112,s[36:39],s87 offen\n"
"v_mfma_f32_16x16x32_bf16 a[180:183],v[212:215],v[248:251],a[180:183]\n"
"s_mov_b32 s87,2621440\n"
"v_add_f32 v56,v56,v57\n"
"v_mfma_f32_16x16x32_bf16 a[188:191],v[212:215],v[252:255],a[188:191]\n"
"buffer_load_dword v115,v112,s[36:39],s87 offen\n"
"v_add_f32_dpp v56,v56,v56 quad_perm:[1,0,3,2] row_mask:0xf bank_mask:0xf\n"
"v_mfma_f32_16x16x32_bf16 a[188:191],v[148:151],v[188:191],a[188:191]\n"
"s_barrier\n"
"v_mfma_f32_16x16x32_bf16 a[192:195],v[216:219],v[224:227],a[192:195]\n"
"ds_read_b128 v[152:155],v82 offset:12288\n"
"v_mfma_f32_16x16x32_bf16 a[196:199],v[220:223],v[224:227],a[196:199]\n"
"ds_read_b128 v[156:159],v82 offset:14336\n"
"v_add_f32_dpp v56,v56,v56 quad_perm:[2,3,0,1] row_mask:0xf bank_mask:0xf\n"
"v_mfma_f32_16x16x32_bf16 a[204:207],v[220:223],v[228:231],a[204:207]\n"
"ds_read_b64_tr_b16 v[160:161],v98 offset:49152\n"
"v_mfma_f32_16x16x32_bf16 a[200:203],v[216:219],v[228:231],a[200:203]\n"
"ds_read_b64_tr_b16 v[162:163],v98 offset:50176\n"
"v_add_f32_dpp v56,v56,v56 row_ror:8 row_mask:0xf bank_mask:0xf\n"
"v_mfma_f32_16x16x32_bf16 a[208:211],v[216:219],v[232:235],a[208:211]\n"
"ds_read_b64_tr_b16 v[164:165],v99 offset:49152\n"
"v_mov_b32 v57,v56\n"
"v_mfma_f32_16x16x32_bf16 a[212:215],v[220:223],v[232:235],a[212:215]\n"
"ds_read_b64_tr_b16 v[166:167],v99 offset:50176\n"
"v_mfma_f32_16x16x32_bf16 a[220:223],v[220:223],v[236:239],a[220:223]\n"
"ds_read_b64_tr_b16 v[168:169],v100 offset:49152\n"
"v_permlane16_swap_b32 v57,v56\n"
"v_mfma_f32_16x16x32_bf16 a[216:219],v[216:219],v[236:239],a[216:219]\n"
"ds_read_b64_tr_b16 v[170:171],v100 offset:50176\n"
"v_add_f32 v56,v56,v57\n"
"v_mfma_f32_16x16x32_bf16 a[224:227],v[216:219],v[240:243],a[224:227]\n"
"ds_read_b64_tr_b16 v[172:173],v101 offset:49152\n"
"v_mov_b32 v57,v56\n"
"v_mfma_f32_16x16x32_bf16 a[228:231],v[220:223],v[240:243],a[228:231]\n"
"ds_read_b64_tr_b16 v[174:175],v101 offset:50176\n"
"v_mfma_f32_16x16x32_bf16 a[236:239],v[220:223],v[244:247],a[236:239]\n"
"ds_read_b64_tr_b16 v[176:177],v102 offset:49152\n"
"v_permlane32_swap_b32 v57,v56\n"
"v_mfma_f32_16x16x32_bf16 a[232:235],v[216:219],v[244:247],a[232:235]\n"
"ds_read_b64_tr_b16 v[178:179],v102 offset:50176\n"
"v_add_f32 v56,v56,v57\n"
"v_mfma_f32_16x16x32_bf16 a[240:243],v[216:219],v[248:251],a[240:243]\n"
"ds_read_b64_tr_b16 v[180:181],v103 offset:49152\n"
"v_mul_f32 v58,0x3a800000,v56\n"
"v_mfma_f32_16x16x32_bf16 a[244:247],v[220:223],v[248:251],a[244:247]\n"
"ds_read_b64_tr_b16 v[182:183],v103 offset:50176\n"
"v_sub_f32 v62,v24,v58\n"
"v_mfma_f32_16x16x32_bf16 a[252:255],v[220:223],v[252:255],a[252:255]\n"
"ds_read_b64_tr_b16 v[184:185],v104 offset:49152\n"
"v_mul_f32 v59,v62,v62\n"
"v_mfma_f32_16x16x32_bf16 a[248:251],v[216:219],v[252:255],a[248:251]\n"
"ds_read_b64_tr_b16 v[186:187],v104 offset:50176\n"
"v_sub_f32 v63,v25,v58\n"
"s_waitcnt lgkmcnt(10)\n"
"v_mfma_f32_16x16x32_bf16 a[192:195],v[152:155],v[160:163],a[192:195]\n"
"ds_read_b64_tr_b16 v[188:189],v105 offset:49152\n"
"v_mul_f32 v60,v63,v63\n"
"v_mfma_f32_16x16x32_bf16 a[196:199],v[156:159],v[160:163],a[196:199]\n"
"ds_read_b64_tr_b16 v[190:191],v105 offset:50176\n"
"v_sub_f32 v64,v26,v58\n"
"v_mfma_f32_16x16x32_bf16 a[204:207],v[156:159],v[164:167],a[204:207]\n"
"ds_read_b128 v[128:131],v82 offset:0\n"
"v_fmac_f32 v59,v64,v64\n"
"v_mfma_f32_16x16x32_bf16 a[200:203],v[152:155],v[164:167],a[200:203]\n"
"ds_read_b128 v[192:195],v83 offset:0\n"
"s_waitcnt lgkmcnt(10)\n"
"v_mfma_f32_16x16x32_bf16 a[208:211],v[152:155],v[168:171],a[208:211]\n"
"ds_read_b64_tr_b16 v[224:225],v98 offset:32768\n"
"v_sub_f32 v65,v27,v58\n"
"v_mfma_f32_16x16x32_bf16 a[212:215],v[156:159],v[168:171],a[212:215]\n"
"ds_read_b64_tr_b16 v[226:227],v98 offset:33792\n"
"v_fmac_f32 v60,v65,v65\n"
"v_mfma_f32_16x16x32_bf16 a[220:223],v[156:159],v[172:175],a[220:223]\n"
"ds_read_b64_tr_b16 v[228:229],v99 offset:32768\n"
"v_sub_f32 v62,v28,v58\n"
"v_mfma_f32_16x16x32_bf16 a[216:219],v[152:155],v[172:175],a[216:219]\n"
"ds_read_b64_tr_b16 v[230:231],v99 offset:33792\n"
"s_waitcnt lgkmcnt(10)\n"
"v_mfma_f32_16x16x32_bf16 a[224:227],v[152:155],v[176:179],a[224:227]\n"
"ds_read_b64_tr_b16 v[232:233],v100 offset:32768\n"
"v_fmac_f32 v59,v62,v62\n"
"v_mfma_f32_16x16x32_bf16 a[228:231],v[156:159],v[176:179],a[228:231]\n"
"ds_read_b64_tr_b16 v[234:235],v100 offset:33792\n"
"v_sub_f32 v63,v29,v58\n"
"v_mfma_f32_16x16x32_bf16 a[236:239],v[156:159],v[180:183],a[236:239]\n"
"ds_read_b64_tr_b16 v[236:237],v101 offset:32768\n"
"v_fmac_f32 v60,v63,v63\n"
"v_mfma_f32_16x16x32_bf16 a[232:235],v[152:155],v[180:183],a[232:235]\n"
"ds_read_b64_tr_b16 v[238:239],v101 offset:33792\n"
"s_waitcnt lgkmcnt(10)\n"
"v_mfma_f32_16x16x32_bf16 a[240:243],v[152:155],v[184:187],a[240:243]\n"
"ds_read_b64_tr_b16 v[240:241],v102 offset:32768\n"
"v_sub_f32 v64,v30,v58\n"
"v_mfma_f32_16x16x32_bf16 a[244:247],v[156:159],v[184:187],a[244:247]\n"
"ds_read_b64_tr_b16 v[242:243],v102 offset:33792\n"
"v_fmac_f32 v59,v64,v64\n"
"v_mfma_f32_16x16x32_bf16 a[252:255],v[156:159],v[188:191],a[252:255]\n"
"ds_read_b64_tr_b16 v[244:245],v103 offset:32768\n"
"v_sub_f32 v65,v31,v58\n"
"v_mfma_f32_16x16x32_bf16 a[248:251],v[152:155],v[188:191],a[248:251]\n"
"ds_read_b64_tr_b16 v[246:247],v103 offset:33792\n"
"s_waitcnt lgkmcnt(8)\n"
"v_mfma_f32_16x16x32_bf16 a[0:3],v[128:131],v[160:163],a[0:3]\n"
"ds_read_b64_tr_b16 v[248:249],v104 offset:32768\n"
"v_fmac_f32 v60,v65,v65\n"
"v_mfma_f32_16x16x32_bf16 a[0:3],v[192:195],v[224:227],a[0:3]\n"
"ds_read_b64_tr_b16 v[250:251],v104 offset:33792\n"
"v_sub_f32 v62,v32,v58\n"
"v_mfma_f32_16x16x32_bf16 a[8:11],v[192:195],v[228:231],a[8:11]\n"
"ds_read_b64_tr_b16 v[252:253],v105 offset:32768\n"
"v_fmac_f32 v59,v62,v62\n"
"v_mfma_f32_16x16x32_bf16 a[8:11],v[128:131],v[164:167],a[8:11]\n"
"ds_read_b64_tr_b16 v[254:255],v105 offset:33792\n"
"s_waitcnt lgkmcnt(8)\n"
"v_mfma_f32_16x16x32_bf16 a[16:19],v[128:131],v[168:171],a[16:19]\n"
"ds_read_b128 v[132:135],v82 offset:2048\n"
"v_sub_f32 v63,v33,v58\n"
"v_mfma_f32_16x16x32_bf16 a[16:19],v[192:195],v[232:235],a[16:19]\n"
"ds_read_b128 v[196:199],v83 offset:2048\n"
"v_fmac_f32 v60,v63,v63\n"
"v_mfma_f32_16x16x32_bf16 a[24:27],v[192:195],v[236:239],a[24:27]\n"
"ds_read_b128 v[136:139],v82 offset:4096\n"
"v_sub_f32 v64,v34,v58\n"
"v_mfma_f32_16x16x32_bf16 a[24:27],v[128:131],v[172:175],a[24:27]\n"
"ds_read_b128 v[200:203],v83 offset:4096\n"
"s_waitcnt lgkmcnt(8)\n"
"v_mfma_f32_16x16x32_bf16 a[32:35],v[128:131],v[176:179],a[32:35]\n"
"ds_read_b128 v[140:143],v82 offset:6144\n"
"v_fmac_f32 v59,v64,v64\n"
"v_mfma_f32_16x16x32_bf16 a[32:35],v[192:195],v[240:243],a[32:35]\n"
"ds_read_b128 v[204:207],v83 offset:6144\n"
"v_sub_f32 v65,v35,v58\n"
"v_mfma_f32_16x16x32_bf16 a[40:43],v[192:195],v[244:247],a[40:43]\n"
"ds_read_b128 v[144:147],v82 offset:8192\n"
"v_fmac_f32 v60,v65,v65\n"
"v_mfma_f32_16x16x32_bf16 a[40:43],v[128:131],v[180:183],a[40:43]\n"
"ds_read_b128 v[208:211],v83 offset:8192\n"
"s_waitcnt lgkmcnt(8)\n"
"v_mfma_f32_16x16x32_bf16 a[48:51],v[128:131],v[184:187],a[48:51]\n"
"ds_read_b128 v[148:151],v82 offset:10240\n"
"v_sub_f32 v62,v36,v58\n"
"v_mfma_f32_16x16x32_bf16 a[48:51],v[192:195],v[248:251],a[48:51]\n"
"ds_read_b128 v[212:215],v83 offset:10240\n"
"v_fmac_f32 v59,v62,v62\n"
"v_mfma_f32_16x16x32_bf16 a[56:59],v[192:195],v[252:255],a[56:59]\n"
"ds_read_b128 v[216:219],v83 offset:12288\n"
"v_sub_f32 v63,v37,v58\n"
"v_mfma_f32_16x16x32_bf16 a[56:59],v[128:131],v[188:191],a[56:59]\n"
"ds_read_b128 v[220:223],v83 offset:14336\n"
"s_waitcnt lgkmcnt(10)\n"
"v_mfma_f32_16x16x32_bf16 a[4:7],v[132:135],v[160:163],a[4:7]\n"
"v_fmac_f32 v60,v63,v63\n"
"v_sub_f32 v64,v38,v58\n"
"v_mfma_f32_16x16x32_bf16 a[4:7],v[196:199],v[224:227],a[4:7]\n"
"v_fmac_f32 v59,v64,v64\n"
"v_sub_f32 v65,v39,v58\n"
"v_mfma_f32_16x16x32_bf16 a[12:15],v[196:199],v[228:231],a[12:15]\n"
"v_fmac_f32 v60,v65,v65\n"
"v_sub_f32 v62,v40,v58\n"
"v_mfma_f32_16x16x32_bf16 a[12:15],v[132:135],v[164:167],a[12:15]\n"
"v_fmac_f32 v59,v62,v62\n"
"v_sub_f32 v63,v41,v58\n"
"v_mfma_f32_16x16x32_bf16 a[20:23],v[132:135],v[168:171],a[20:23]\n"
"s_waitcnt lgkmcnt(0)\n"
"s_barrier\n"
"v_mfma_f32_16x16x32_bf16 a[20:23],v[196:199],v[232:235],a[20:23]\n"
"s_mov_b32 s77,2097152\n"
"v_fmac_f32 v60,v63,v63\n"
"s_add_u32 m0,s78,147456\n"
"v_mfma_f32_16x16x32_bf16 a[28:31],v[196:199],v[236:239],a[28:31]\n"
"buffer_load_dwordx4 v110,s[20:23],s77 offen offset:0 lds\n"
"v_mfma_f32_16x16x32_bf16 a[28:31],v[132:135],v[172:175],a[28:31]\n"
"v_sub_f32 v64,v42,v58\n"
"v_fmac_f32 v59,v64,v64\n"
"v_mfma_f32_16x16x32_bf16 a[36:39],v[132:135],v[176:179],a[36:39]\n"
"v_sub_f32 v65,v43,v58\n"
"v_fmac_f32 v60,v65,v65\n"
"v_mfma_f32_16x16x32_bf16 a[36:39],v[196:199],v[240:243],a[36:39]\n"
"v_sub_f32 v62,v44,v58\n"
"v_fmac_f32 v59,v62,v62\n"
"v_mfma_f32_16x16x32_bf16 a[44:47],v[196:199],v[244:247],a[44:47]\n"
"buffer_load_dwordx4 v111,s[20:23],s77 offen offset:1024 lds\n"
"v_mfma_f32_16x16x32_bf16 a[44:47],v[132:135],v[180:183],a[44:47]\n"
"v_sub_f32 v63,v45,v58\n"
"v_fmac_f32 v60,v63,v63\n"
"v_mfma_f32_16x16x32_bf16 a[52:55],v[132:135],v[184:187],a[52:55]\n"
"v_sub_f32 v64,v46,v58\n"
"v_fmac_f32 v59,v64,v64\n"
"v_mfma_f32_16x16x32_bf16 a[52:55],v[196:199],v[248:251],a[52:55]\n"
"v_sub_f32 v65,v47,v58\n"
"v_fmac_f32 v60,v65,v65\n"
"v_mfma_f32_16x16x32_bf16 a[60:63],v[196:199],v[252:255],a[60:63]\n"
"buffer_load_dwordx4 v113,s[20:23],s77 offen offset:2048 lds\n"
"v_mfma_f32_16x16x32_bf16 a[60:63],v[132:135],v[188:191],a[60:63]\n"
"v_sub_f32 v62,v48,v58\n"
"v_fmac_f32 v59,v62,v62\n"
"v_mfma_f32_16x16x32_bf16 a[64:67],v[136:139],v[160:163],a[64:67]\n"
"v_sub_f32 v63,v49,v58\n"
"v_fmac_f32 v60,v63,v63\n"
"v_mfma_f32_16x16x32_bf16 a[64:67],v[200:203],v[224:227],a[64:67]\n"
"v_sub_f32 v64,v50,v58\n"
"v_fmac_f32 v59,v64,v64\n"
"v_mfma_f32_16x16x32_bf16 a[72:75],v[200:203],v[228:231],a[72:75]\n"
"buffer_load_dwordx4 v114,s[20:23],s77 offen offset:3072 lds\n"
"v_mfma_f32_16x16x32_bf16 a[72:75],v[136:139],v[164:167],a[72:75]\n"
"v_sub_f32 v65,v51,v58\n"
"v_fmac_f32 v60,v65,v65\n"
"v_mfma_f32_16x16x32_bf16 a[80:83],v[136:139],v[168:171],a[80:83]\n"
"v_sub_f32 v62,v52,v58\n"
"v_fmac_f32 v59,v62,v62\n"
"v_mfma_f32_16x16x32_bf16 a[80:83],v[200:203],v[232:235],a[80:83]\n"
"s_mov_b32 s77,2359296\n"
"v_sub_f32 v63,v53,v58\n"
"s_add_u32 m0,s78,131072\n"
"v_mfma_f32_16x16x32_bf16 a[88:91],v[200:203],v[236:239],a[88:91]\n"
"buffer_load_dwordx4 v110,s[20:23],s77 offen offset:0 lds\n"
"v_mfma_f32_16x16x32_bf16 a[88:91],v[136:139],v[172:175],a[88:91]\n"
"v_fmac_f32 v60,v63,v63\n"
"v_sub_f32 v64,v54,v58\n"
"v_mfma_f32_16x16x32_bf16 a[96:99],v[136:139],v[176:179],a[96:99]\n"
"v_fmac_f32 v59,v64,v64\n"
"v_sub_f32 v65,v55,v58\n"
"v_mfma_f32_16x16x32_bf16 a[96:99],v[200:203],v[240:243],a[96:99]\n"
"v_fmac_f32 v60,v65,v65\n"
"v_add_f32 v59,v59,v60\n"
"v_mfma_f32_16x16x32_bf16 a[104:107],v[200:203],v[244:247],a[104:107]\n"
"buffer_load_dwordx4 v111,s[20:23],s77 offen offset:1024 lds\n"
"v_mfma_f32_16x16x32_bf16 a[104:107],v[136:139],v[180:183],a[104:107]\n"
"v_mfma_f32_16x16x32_bf16 a[112:115],v[136:139],v[184:187],a[112:115]\n"
"v_add_f32_dpp v59,v59,v59 quad_perm:[1,0,3,2] row_mask:0xf bank_mask:0xf\n"
"v_mfma_f32_16x16x32_bf16 a[112:115],v[200:203],v[248:251],a[112:115]\n"
"s_nop 0\n"
"v_add_f32_dpp v59,v59,v59 quad_perm:[2,3,0,1] row_mask:0xf bank_mask:0xf\n"
"v_mfma_f32_16x16x32_bf16 a[120:123],v[200:203],v[252:255],a[120:123]\n"
"buffer_load_dwordx4 v113,s[20:23],s77 offen offset:2048 lds\n"
"v_mfma_f32_16x16x32_bf16 a[120:123],v[136:139],v[188:191],a[120:123]\n"
"v_add_f32_dpp v59,v59,v59 row_ror:8 row_mask:0xf bank_mask:0xf\n"
"v_mov_b32 v57,v59\n"
"v_mfma_f32_16x16x32_bf16 a[68:71],v[140:143],v[160:163],a[68:71]\n"
"v_mfma_f32_16x16x32_bf16 a[68:71],v[204:207],v[224:227],a[68:71]\n"
"v_permlane16_swap_b32 v57,v59\n"
"v_add_f32 v59,v59,v57\n"
"v_mfma_f32_16x16x32_bf16 a[76:79],v[204:207],v[228:231],a[76:79]\n"
"buffer_load_dwordx4 v114,s[20:23],s77 offen offset:3072 lds\n"
"v_mfma_f32_16x16x32_bf16 a[76:79],v[140:143],v[164:167],a[76:79]\n"
"v_mov_b32 v57,v59\n"
"v_mfma_f32_16x16x32_bf16 a[84:87],v[140:143],v[168:171],a[84:87]\n"
"s_nop 0\n"
"v_permlane32_swap_b32 v57,v59\n"
"v_add_f32 v59,v59,v57\n"
"v_mfma_f32_16x16x32_bf16 a[84:87],v[204:207],v[232:235],a[84:87]\n"
"v_mul_f32 v59,0x3a800000,v59\n"
"v_cvt_pk_bf16_f32 v59,v59,v59\n"
"v_mfma_f32_16x16x32_bf16 a[92:95],v[204:207],v[236:239],a[92:95]\n"
"s_mov_b32 s76,640\n"
"v_lshlrev_b32 v59,16,v59\n"
"s_add_u32 m0,s79,65536\n"
"v_mfma_f32_16x16x32_bf16 a[92:95],v[140:143],v[172:175],a[92:95]\n"
"buffer_load_dwordx4 v106,s[16:19],s76 offen offset:0 lds\n"
"v_mfma_f32_16x16x32_bf16 a[100:103],v[140:143],v[176:179],a[100:103]\n"
"v_add_f32 v59,0x3727c5ac,v59\n"
"v_cvt_pk_bf16_f32 v59,v59,v59\n"
"v_mfma_f32_16x16x32_bf16 a[100:103],v[204:207],v[240:243],a[100:103]\n"
"v_lshlrev_b32 v59,16,v59\n"
"v_sqrt_f32 v59,v59\n"
"v_mfma_f32_16x16x32_bf16 a[108:111],v[204:207],v[244:247],a[108:111]\n"
"v_cvt_pk_bf16_f32 v58,v58,v58\n"
"v_lshlrev_b32 v58,16,v58\n"
"v_mfma_f32_16x16x32_bf16 a[108:111],v[140:143],v[180:183],a[108:111]\n"
"v_cvt_pk_bf16_f32 v59,v59,v59\n"
"v_lshlrev_b32 v59,16,v59\n"
"v_mfma_f32_16x16x32_bf16 a[116:119],v[140:143],v[184:187],a[116:119]\n"
"buffer_load_dwordx4 v107,s[16:19],s76 offen offset:1024 lds\n"
"v_mfma_f32_16x16x32_bf16 a[116:119],v[204:207],v[248:251],a[116:119]\n"
"v_rcp_f32 v66,v59\n"
"v_mfma_f32_16x16x32_bf16 a[124:127],v[204:207],v[252:255],a[124:127]\n"
"v_mul_f32 v66,0x3e0293ee,v66\n"
"v_mul_f32 v58,v58,v66\n"
"v_mfma_f32_16x16x32_bf16 a[124:127],v[140:143],v[188:191],a[124:127]\n"
"v_fma_f32 v24,v24,v66,-v58\n"
"v_fma_f32 v25,v25,v66,-v58\n"
"v_mfma_f32_16x16x32_bf16 a[128:131],v[144:147],v[160:163],a[128:131]\n"
"v_cvt_pk_bf16_f32 v8,v24,v25\n"
"v_fma_f32 v26,v26,v66,-v58\n"
"v_mfma_f32_16x16x32_bf16 a[128:131],v[208:211],v[224:227],a[128:131]\n"
"buffer_load_dwordx4 v108,s[16:19],s76 offen offset:2048 lds\n"
"v_mfma_f32_16x16x32_bf16 a[136:139],v[208:211],v[228:231],a[136:139]\n"
"v_fma_f32 v27,v27,v66,-v58\n"
"v_cvt_pk_bf16_f32 v9,v26,v27\n"
"v_mfma_f32_16x16x32_bf16 a[136:139],v[144:147],v[164:167],a[136:139]\n"
"v_fma_f32 v28,v28,v66,-v58\n"
"v_fma_f32 v29,v29,v66,-v58\n"
"v_mfma_f32_16x16x32_bf16 a[144:147],v[144:147],v[168:171],a[144:147]\n"
"v_cvt_pk_bf16_f32 v10,v28,v29\n"
"v_fma_f32 v30,v30,v66,-v58\n"
"v_mfma_f32_16x16x32_bf16 a[144:147],v[208:211],v[232:235],a[144:147]\n"
"v_fma_f32 v31,v31,v66,-v58\n"
"v_cvt_pk_bf16_f32 v11,v30,v31\n"
"v_mfma_f32_16x16x32_bf16 a[152:155],v[208:211],v[236:239],a[152:155]\n"
"buffer_load_dwordx4 v109,s[16:19],s76 offen offset:3072 lds\n"
"v_mfma_f32_16x16x32_bf16 a[152:155],v[144:147],v[172:175],a[152:155]\n"
"buffer_store_dwordx4 v[8:11],v71,s[24:27],s54 offen sc1\n"
"v_mfma_f32_16x16x32_bf16 a[160:163],v[144:147],v[176:179],a[160:163]\n"
"s_add_u32 s54,s54,131072\n"
"v_fma_f32 v32,v32,v66,-v58\n"
"v_fma_f32 v33,v33,v66,-v58\n"
"v_mfma_f32_16x16x32_bf16 a[160:163],v[208:211],v[240:243],a[160:163]\n"
"v_cvt_pk_bf16_f32 v12,v32,v33\n"
"v_fma_f32 v34,v34,v66,-v58\n"
"v_mfma_f32_16x16x32_bf16 a[168:171],v[208:211],v[244:247],a[168:171]\n"
"s_mov_b32 s76,262784\n"
"v_fma_f32 v35,v35,v66,-v58\n"
"s_add_u32 m0,s79,69632\n"
"v_mfma_f32_16x16x32_bf16 a[168:171],v[144:147],v[180:183],a[168:171]\n"
"buffer_load_dwordx4 v106,s[16:19],s76 offen offset:0 lds\n"
"v_mfma_f32_16x16x32_bf16 a[176:179],v[144:147],v[184:187],a[176:179]\n"
"v_cvt_pk_bf16_f32 v13,v34,v35\n"
"v_fma_f32 v36,v36,v66,-v58\n"
"v_mfma_f32_16x16x32_bf16 a[176:179],v[208:211],v[248:251],a[176:179]\n"
"v_fma_f32 v37,v37,v66,-v58\n"
"v_cvt_pk_bf16_f32 v14,v36,v37\n"
"v_mfma_f32_16x16x32_bf16 a[184:187],v[208:211],v[252:255],a[184:187]\n"
"v_fma_f32 v38,v38,v66,-v58\n"
"v_fma_f32 v39,v39,v66,-v58\n"
"v_cvt_pk_bf16_f32 v15,v38,v39\n"
"v_mfma_f32_16x16x32_bf16 a[184:187],v[144:147],v[188:191],a[184:187]\n"
"buffer_store_dwordx4 v[12:15],v71,s[24:27],s54 offen sc1\n"
"v_mfma_f32_16x16x32_bf16 a[132:135],v[148:151],v[160:163],a[132:135]\n"
"buffer_load_dwordx4 v107,s[16:19],s76 offen offset:1024 lds\n"
"v_mfma_f32_16x16x32_bf16 a[132:135],v[212:215],v[224:227],a[132:135]\n"
"s_add_u32 s54,s54,131072\n"
"v_fma_f32 v40,v40,v66,-v58\n"
"v_mfma_f32_16x16x32_bf16 a[140:143],v[212:215],v[228:231],a[140:143]\n"
"v_fma_f32 v41,v41,v66,-v58\n"
"v_cvt_pk_bf16_f32 v16,v40,v41\n"
"v_mfma_f32_16x16x32_bf16 a[140:143],v[148:151],v[164:167],a[140:143]\n"
"v_fma_f32 v42,v42,v66,-v58\n"
"v_fma_f32 v43,v43,v66,-v58\n"
"v_mfma_f32_16x16x32_bf16 a[148:151],v[148:151],v[168:171],a[148:151]\n"
"v_cvt_pk_bf16_f32 v17,v42,v43\n"
"v_fma_f32 v44,v44,v66,-v58\n"
"v_mfma_f32_16x16x32_bf16 a[148:151],v[212:215],v[232:235],a[148:151]\n"
"buffer_load_dwordx4 v108,s[16:19],s76 offen offset:2048 lds\n"
"v_mfma_f32_16x16x32_bf16 a[156:159],v[212:215],v[236:239],a[156:159]\n"
"v_fma_f32 v45,v45,v66,-v58\n"
"v_cvt_pk_bf16_f32 v18,v44,v45\n"
"v_mfma_f32_16x16x32_bf16 a[156:159],v[148:151],v[172:175],a[156:159]\n"
"v_fma_f32 v46,v46,v66,-v58\n"
"v_fma_f32 v47,v47,v66,-v58\n"
"v_cvt_pk_bf16_f32 v19,v46,v47\n"
"v_mfma_f32_16x16x32_bf16 a[164:167],v[148:151],v[176:179],a[164:167]\n"
"buffer_store_dwordx4 v[16:19],v71,s[24:27],s54 offen sc1\n"
"v_mfma_f32_16x16x32_bf16 a[164:167],v[212:215],v[240:243],a[164:167]\n"
"s_add_u32 s54,s54,131072\n"
"v_fma_f32 v48,v48,v66,-v58\n"
"v_mfma_f32_16x16x32_bf16 a[172:175],v[212:215],v[244:247],a[172:175]\n"
"buffer_load_dwordx4 v109,s[16:19],s76 offen offset:3072 lds\n"
"v_mfma_f32_16x16x32_bf16 a[172:175],v[148:151],v[180:183],a[172:175]\n"
"s_mov_b32 s87,0\n"
"v_fma_f32 v49,v49,v66,-v58\n"
"v_cvt_pk_bf16_f32 v20,v48,v49\n"
"v_mfma_f32_16x16x32_bf16 a[180:183],v[148:151],v[184:187],a[180:183]\n"
"buffer_load_dword v115,v112,s[40:43],s87 offen\n"
"v_mfma_f32_16x16x32_bf16 a[180:183],v[212:215],v[248:251],a[180:183]\n"
"s_mov_b32 s87,1048576\n"
"v_fma_f32 v50,v50,v66,-v58\n"
"v_fma_f32 v51,v51,v66,-v58\n"
"v_mfma_f32_16x16x32_bf16 a[188:191],v[212:215],v[252:255],a[188:191]\n"
"buffer_load_dword v115,v112,s[40:43],s87 offen\n"
"v_mfma_f32_16x16x32_bf16 a[188:191],v[148:151],v[188:191],a[188:191]\n"
"s_mov_b32 s87,2097152\n"
"s_waitcnt vmcnt(31)\n"
"s_barrier\n"
"v_mfma_f32_16x16x32_bf16 a[192:195],v[216:219],v[224:227],a[192:195]\n"
"ds_read_b128 v[152:155],v80 offset:12288\n"
"buffer_load_dword v115,v112,s[40:43],s87 offen\n"
"v_mfma_f32_16x16x32_bf16 a[196:199],v[220:223],v[224:227],a[196:199]\n"
"ds_read_b128 v[156:159],v80 offset:14336\n"
"v_cvt_pk_bf16_f32 v21,v50,v51\n"
"v_mfma_f32_16x16x32_bf16 a[204:207],v[220:223],v[228:231],a[204:207]\n"
"ds_read_b64_tr_b16 v[160:161],v98 offset:0\n"
"v_fma_f32 v52,v52,v66,-v58\n"
"v_mfma_f32_16x16x32_bf16 a[200:203],v[216:219],v[228:231],a[200:203]\n"
"ds_read_b64_tr_b16 v[162:163],v98 offset:1024\n"
"v_fma_f32 v53,v53,v66,-v58\n"
"v_mfma_f32_16x16x32_bf16 a[208:211],v[216:219],v[232:235],a[208:211]\n"
"ds_read_b64_tr_b16 v[164:165],v99 offset:0\n"
"v_cvt_pk_bf16_f32 v22,v52,v53\n"
"v_mfma_f32_16x16x32_bf16 a[212:215],v[220:223],v[232:235],a[212:215]\n"
"ds_read_b64_tr_b16 v[166:167],v99 offset:1024\n"
"v_fma_f32 v54,v54,v66,-v58\n"
"v_mfma_f32_16x16x32_bf16 a[220:223],v[220:223],v[236:239],a[220:223]\n"
"ds_read_b64_tr_b16 v[168:169],v100 offset:0\n"
"v_fma_f32 v55,v55,v66,-v58\n"
"v_mfma_f32_16x16x32_bf16 a[216:219],v[216:219],v[236:239],a[216:219]\n"
"ds_read_b64_tr_b16 v[170:171],v100 offset:1024\n"
"v_cvt_pk_bf16_f32 v23,v54,v55\n"
"v_mfma_f32_16x16x32_bf16 a[224:227],v[216:219],v[240:243],a[224:227]\n"
"ds_read_b64_tr_b16 v[172:173],v101 offset:0\n"
"buffer_store_dwordx4 v[20:23],v71,s[24:27],s54 offen sc1\n"
"v_mfma_f32_16x16x32_bf16 a[228:231],v[220:223],v[240:243],a[228:231]\n"
"ds_read_b64_tr_b16 v[174:175],v101 offset:1024\n"
"v_mfma_f32_16x16x32_bf16 a[236:239],v[220:223],v[244:247],a[236:239]\n"
"ds_read_b64_tr_b16 v[176:177],v102 offset:0\n"
"v_mfma_f32_16x16x32_bf16 a[232:235],v[216:219],v[244:247],a[232:235]\n"
"ds_read_b64_tr_b16 v[178:179],v102 offset:1024\n"
"v_mfma_f32_16x16x32_bf16 a[240:243],v[216:219],v[248:251],a[240:243]\n"
"ds_read_b64_tr_b16 v[180:181],v103 offset:0\n"
"v_mfma_f32_16x16x32_bf16 a[244:247],v[220:223],v[248:251],a[244:247]\n"
"ds_read_b64_tr_b16 v[182:183],v103 offset:1024\n"
"v_mfma_f32_16x16x32_bf16 a[252:255],v[220:223],v[252:255],a[252:255]\n"
"ds_read_b64_tr_b16 v[184:185],v104 offset:0\n"
"v_mfma_f32_16x16x32_bf16 a[248:251],v[216:219],v[252:255],a[248:251]\n"
"ds_read_b64_tr_b16 v[186:187],v104 offset:1024\n"
"s_waitcnt lgkmcnt(10)\n"
"v_mfma_f32_16x16x32_bf16 a[192:195],v[152:155],v[160:163],a[192:195]\n"
"ds_read_b64_tr_b16 v[188:189],v105 offset:0\n"
"v_mfma_f32_16x16x32_bf16 a[196:199],v[156:159],v[160:163],a[196:199]\n"
"ds_read_b64_tr_b16 v[190:191],v105 offset:1024\n"
"v_mfma_f32_16x16x32_bf16 a[204:207],v[156:159],v[164:167],a[204:207]\n"
"ds_read_b128 v[128:131],v80 offset:0\n"
"v_mfma_f32_16x16x32_bf16 a[200:203],v[152:155],v[164:167],a[200:203]\n"
"ds_read_b128 v[192:195],v81 offset:0\n"
"s_waitcnt lgkmcnt(10)\n"
"v_mfma_f32_16x16x32_bf16 a[208:211],v[152:155],v[168:171],a[208:211]\n"
"ds_read_b64_tr_b16 v[224:225],v98 offset:16384\n"
"v_mfma_f32_16x16x32_bf16 a[212:215],v[156:159],v[168:171],a[212:215]\n"
"ds_read_b64_tr_b16 v[226:227],v98 offset:17408\n"
"v_mfma_f32_16x16x32_bf16 a[220:223],v[156:159],v[172:175],a[220:223]\n"
"ds_read_b64_tr_b16 v[228:229],v99 offset:16384\n"
"v_mfma_f32_16x16x32_bf16 a[216:219],v[152:155],v[172:175],a[216:219]\n"
"ds_read_b64_tr_b16 v[230:231],v99 offset:17408\n"
"s_waitcnt lgkmcnt(10)\n"
"v_mfma_f32_16x16x32_bf16 a[224:227],v[152:155],v[176:179],a[224:227]\n"
"ds_read_b64_tr_b16 v[232:233],v100 offset:16384\n"
"v_mfma_f32_16x16x32_bf16 a[228:231],v[156:159],v[176:179],a[228:231]\n"
"ds_read_b64_tr_b16 v[234:235],v100 offset:17408\n"
"v_mfma_f32_16x16x32_bf16 a[236:239],v[156:159],v[180:183],a[236:239]\n"
"ds_read_b64_tr_b16 v[236:237],v101 offset:16384\n"
"v_mfma_f32_16x16x32_bf16 a[232:235],v[152:155],v[180:183],a[232:235]\n"
"ds_read_b64_tr_b16 v[238:239],v101 offset:17408\n"
"s_waitcnt lgkmcnt(10)\n"
"v_mfma_f32_16x16x32_bf16 a[240:243],v[152:155],v[184:187],a[240:243]\n"
"ds_read_b64_tr_b16 v[240:241],v102 offset:16384\n"
"v_mfma_f32_16x16x32_bf16 a[244:247],v[156:159],v[184:187],a[244:247]\n"
"ds_read_b64_tr_b16 v[242:243],v102 offset:17408\n"
"v_mfma_f32_16x16x32_bf16 a[252:255],v[156:159],v[188:191],a[252:255]\n"
"ds_read_b64_tr_b16 v[244:245],v103 offset:16384\n"
"v_mfma_f32_16x16x32_bf16 a[248:251],v[152:155],v[188:191],a[248:251]\n"
"ds_read_b64_tr_b16 v[246:247],v103 offset:17408\n"
"s_waitcnt lgkmcnt(8)\n"
"v_mfma_f32_16x16x32_bf16 a[0:3],v[128:131],v[160:163],a[0:3]\n"
"ds_read_b64_tr_b16 v[248:249],v104 offset:16384\n"
"v_mfma_f32_16x16x32_bf16 a[0:3],v[192:195],v[224:227],a[0:3]\n"
"ds_read_b64_tr_b16 v[250:251],v104 offset:17408\n"
"v_mfma_f32_16x16x32_bf16 a[8:11],v[192:195],v[228:231],a[8:11]\n"
"ds_read_b64_tr_b16 v[252:253],v105 offset:16384\n"
"v_mfma_f32_16x16x32_bf16 a[8:11],v[128:131],v[164:167],a[8:11]\n"
"ds_read_b64_tr_b16 v[254:255],v105 offset:17408\n"
"s_waitcnt lgkmcnt(8)\n"
"v_mfma_f32_16x16x32_bf16 a[16:19],v[128:131],v[168:171],a[16:19]\n"
"ds_read_b128 v[132:135],v80 offset:2048\n"
"v_mfma_f32_16x16x32_bf16 a[16:19],v[192:195],v[232:235],a[16:19]\n"
"ds_read_b128 v[196:199],v81 offset:2048\n"
"v_mfma_f32_16x16x32_bf16 a[24:27],v[192:195],v[236:239],a[24:27]\n"
"ds_read_b128 v[136:139],v80 offset:4096\n"
"v_mfma_f32_16x16x32_bf16 a[24:27],v[128:131],v[172:175],a[24:27]\n"
"ds_read_b128 v[200:203],v81 offset:4096\n"
"s_waitcnt lgkmcnt(8)\n"
"v_mfma_f32_16x16x32_bf16 a[32:35],v[128:131],v[176:179],a[32:35]\n"
"ds_read_b128 v[140:143],v80 offset:6144\n"
"v_mfma_f32_16x16x32_bf16 a[32:35],v[192:195],v[240:243],a[32:35]\n"
"ds_read_b128 v[204:207],v81 offset:6144\n"
"v_mfma_f32_16x16x32_bf16 a[40:43],v[192:195],v[244:247],a[40:43]\n"
"ds_read_b128 v[144:147],v80 offset:8192\n"
"v_mfma_f32_16x16x32_bf16 a[40:43],v[128:131],v[180:183],a[40:43]\n"
"ds_read_b128 v[208:211],v81 offset:8192\n"
"s_waitcnt lgkmcnt(8)\n"
"v_mfma_f32_16x16x32_bf16 a[48:51],v[128:131],v[184:187],a[48:51]\n"
"ds_read_b128 v[148:151],v80 offset:10240\n"
"v_mfma_f32_16x16x32_bf16 a[48:51],v[192:195],v[248:251],a[48:51]\n"
"ds_read_b128 v[212:215],v81 offset:10240\n"
"v_mfma_f32_16x16x32_bf16 a[56:59],v[192:195],v[252:255],a[56:59]\n"
"ds_read_b128 v[216:219],v81 offset:12288\n"
"v_mfma_f32_16x16x32_bf16 a[56:59],v[128:131],v[188:191],a[56:59]\n"
"ds_read_b128 v[220:223],v81 offset:14336\n"
"s_waitcnt lgkmcnt(10)\n"
"v_mfma_f32_16x16x32_bf16 a[4:7],v[132:135],v[160:163],a[4:7]\n"
"v_mfma_f32_16x16x32_bf16 a[4:7],v[196:199],v[224:227],a[4:7]\n"
"v_mfma_f32_16x16x32_bf16 a[12:15],v[196:199],v[228:231],a[12:15]\n"
"v_mfma_f32_16x16x32_bf16 a[12:15],v[132:135],v[164:167],a[12:15]\n"
"v_mfma_f32_16x16x32_bf16 a[20:23],v[132:135],v[168:171],a[20:23]\n"
"s_waitcnt lgkmcnt(0)\n"
"s_barrier\n"
"v_mfma_f32_16x16x32_bf16 a[20:23],v[196:199],v[232:235],a[20:23]\n"
"s_mov_b32 s77,2621440\n"
"s_add_u32 m0,s78,98304\n"
"v_mfma_f32_16x16x32_bf16 a[28:31],v[196:199],v[236:239],a[28:31]\n"
"buffer_load_dwordx4 v110,s[20:23],s77 offen offset:0 lds\n"
"v_mfma_f32_16x16x32_bf16 a[28:31],v[132:135],v[172:175],a[28:31]\n"
"v_mfma_f32_16x16x32_bf16 a[36:39],v[132:135],v[176:179],a[36:39]\n"
"v_mfma_f32_16x16x32_bf16 a[36:39],v[196:199],v[240:243],a[36:39]\n"
"v_mfma_f32_16x16x32_bf16 a[44:47],v[196:199],v[244:247],a[44:47]\n"
"buffer_load_dwordx4 v111,s[20:23],s77 offen offset:1024 lds\n"
"v_mfma_f32_16x16x32_bf16 a[44:47],v[132:135],v[180:183],a[44:47]\n"
"v_mfma_f32_16x16x32_bf16 a[52:55],v[132:135],v[184:187],a[52:55]\n"
"v_mfma_f32_16x16x32_bf16 a[52:55],v[196:199],v[248:251],a[52:55]\n"
"v_mfma_f32_16x16x32_bf16 a[60:63],v[196:199],v[252:255],a[60:63]\n"
"buffer_load_dwordx4 v113,s[20:23],s77 offen offset:2048 lds\n"
"v_mfma_f32_16x16x32_bf16 a[60:63],v[132:135],v[188:191],a[60:63]\n"
"v_mfma_f32_16x16x32_bf16 a[64:67],v[136:139],v[160:163],a[64:67]\n"
"v_mfma_f32_16x16x32_bf16 a[64:67],v[200:203],v[224:227],a[64:67]\n"
"v_mfma_f32_16x16x32_bf16 a[72:75],v[200:203],v[228:231],a[72:75]\n"
"buffer_load_dwordx4 v114,s[20:23],s77 offen offset:3072 lds\n"
"v_mfma_f32_16x16x32_bf16 a[72:75],v[136:139],v[164:167],a[72:75]\n"
"v_mfma_f32_16x16x32_bf16 a[80:83],v[136:139],v[168:171],a[80:83]\n"
"v_mfma_f32_16x16x32_bf16 a[80:83],v[200:203],v[232:235],a[80:83]\n"
"s_mov_b32 s77,2883584\n"
"s_add_u32 m0,s78,114688\n"
"v_mfma_f32_16x16x32_bf16 a[88:91],v[200:203],v[236:239],a[88:91]\n"
"buffer_load_dwordx4 v110,s[20:23],s77 offen offset:0 lds\n"
"v_mfma_f32_16x16x32_bf16 a[88:91],v[136:139],v[172:175],a[88:91]\n"
"v_mfma_f32_16x16x32_bf16 a[96:99],v[136:139],v[176:179],a[96:99]\n"
"v_mfma_f32_16x16x32_bf16 a[96:99],v[200:203],v[240:243],a[96:99]\n"
"v_mfma_f32_16x16x32_bf16 a[104:107],v[200:203],v[244:247],a[104:107]\n"
"buffer_load_dwordx4 v111,s[20:23],s77 offen offset:1024 lds\n"
"v_mfma_f32_16x16x32_bf16 a[104:107],v[136:139],v[180:183],a[104:107]\n"
"v_mfma_f32_16x16x32_bf16 a[112:115],v[136:139],v[184:187],a[112:115]\n"
"v_mfma_f32_16x16x32_bf16 a[112:115],v[200:203],v[248:251],a[112:115]\n"
"v_mfma_f32_16x16x32_bf16 a[120:123],v[200:203],v[252:255],a[120:123]\n"
"buffer_load_dwordx4 v113,s[20:23],s77 offen offset:2048 lds\n"
"v_mfma_f32_16x16x32_bf16 a[120:123],v[136:139],v[188:191],a[120:123]\n"
"v_mfma_f32_16x16x32_bf16 a[68:71],v[140:143],v[160:163],a[68:71]\n"
"v_mfma_f32_16x16x32_bf16 a[68:71],v[204:207],v[224:227],a[68:71]\n"
"v_mfma_f32_16x16x32_bf16 a[76:79],v[204:207],v[228:231],a[76:79]\n"
"buffer_load_dwordx4 v114,s[20:23],s77 offen offset:3072 lds\n"
"v_mfma_f32_16x16x32_bf16 a[76:79],v[140:143],v[164:167],a[76:79]\n"
"v_mfma_f32_16x16x32_bf16 a[84:87],v[140:143],v[168:171],a[84:87]\n"
"v_mfma_f32_16x16x32_bf16 a[84:87],v[204:207],v[232:235],a[84:87]\n"
"v_mfma_f32_16x16x32_bf16 a[92:95],v[204:207],v[236:239],a[92:95]\n"
"s_mov_b32 s76,768\n"
"s_add_u32 m0,s79,0\n"
"v_mfma_f32_16x16x32_bf16 a[92:95],v[140:143],v[172:175],a[92:95]\n"
"buffer_load_dwordx4 v106,s[16:19],s76 offen offset:0 lds\n"
"v_mfma_f32_16x16x32_bf16 a[100:103],v[140:143],v[176:179],a[100:103]\n"
"v_mfma_f32_16x16x32_bf16 a[100:103],v[204:207],v[240:243],a[100:103]\n"
"v_mfma_f32_16x16x32_bf16 a[108:111],v[204:207],v[244:247],a[108:111]\n"
"v_mfma_f32_16x16x32_bf16 a[108:111],v[140:143],v[180:183],a[108:111]\n"
"v_mfma_f32_16x16x32_bf16 a[116:119],v[140:143],v[184:187],a[116:119]\n"
"buffer_load_dwordx4 v107,s[16:19],s76 offen offset:1024 lds\n"
"v_mfma_f32_16x16x32_bf16 a[116:119],v[204:207],v[248:251],a[116:119]\n"
"v_mfma_f32_16x16x32_bf16 a[124:127],v[204:207],v[252:255],a[124:127]\n"
"v_mfma_f32_16x16x32_bf16 a[124:127],v[140:143],v[188:191],a[124:127]\n"
"v_mfma_f32_16x16x32_bf16 a[128:131],v[144:147],v[160:163],a[128:131]\n"
"v_mfma_f32_16x16x32_bf16 a[128:131],v[208:211],v[224:227],a[128:131]\n"
"buffer_load_dwordx4 v108,s[16:19],s76 offen offset:2048 lds\n"
"v_mfma_f32_16x16x32_bf16 a[136:139],v[208:211],v[228:231],a[136:139]\n"
"v_mfma_f32_16x16x32_bf16 a[136:139],v[144:147],v[164:167],a[136:139]\n"
"v_mfma_f32_16x16x32_bf16 a[144:147],v[144:147],v[168:171],a[144:147]\n"
"v_mfma_f32_16x16x32_bf16 a[144:147],v[208:211],v[232:235],a[144:147]\n"
"v_mfma_f32_16x16x32_bf16 a[152:155],v[208:211],v[236:239],a[152:155]\n"
"buffer_load_dwordx4 v109,s[16:19],s76 offen offset:3072 lds\n"
"v_mfma_f32_16x16x32_bf16 a[152:155],v[144:147],v[172:175],a[152:155]\n"
"v_mfma_f32_16x16x32_bf16 a[160:163],v[144:147],v[176:179],a[160:163]\n"
"v_mfma_f32_16x16x32_bf16 a[160:163],v[208:211],v[240:243],a[160:163]\n"
"v_mfma_f32_16x16x32_bf16 a[168:171],v[208:211],v[244:247],a[168:171]\n"
"s_mov_b32 s76,262912\n"
"s_add_u32 m0,s79,4096\n"
"v_mfma_f32_16x16x32_bf16 a[168:171],v[144:147],v[180:183],a[168:171]\n"
"buffer_load_dwordx4 v106,s[16:19],s76 offen offset:0 lds\n"
"v_mfma_f32_16x16x32_bf16 a[176:179],v[144:147],v[184:187],a[176:179]\n"
"v_mfma_f32_16x16x32_bf16 a[176:179],v[208:211],v[248:251],a[176:179]\n"
"v_mfma_f32_16x16x32_bf16 a[184:187],v[208:211],v[252:255],a[184:187]\n"
"v_mfma_f32_16x16x32_bf16 a[184:187],v[144:147],v[188:191],a[184:187]\n"
"v_mfma_f32_16x16x32_bf16 a[132:135],v[148:151],v[160:163],a[132:135]\n"
"buffer_load_dwordx4 v107,s[16:19],s76 offen offset:1024 lds\n"
"v_mfma_f32_16x16x32_bf16 a[132:135],v[212:215],v[224:227],a[132:135]\n"
"v_mfma_f32_16x16x32_bf16 a[140:143],v[212:215],v[228:231],a[140:143]\n"
"v_mfma_f32_16x16x32_bf16 a[140:143],v[148:151],v[164:167],a[140:143]\n"
"v_mfma_f32_16x16x32_bf16 a[148:151],v[148:151],v[168:171],a[148:151]\n"
"v_mfma_f32_16x16x32_bf16 a[148:151],v[212:215],v[232:235],a[148:151]\n"
"buffer_load_dwordx4 v108,s[16:19],s76 offen offset:2048 lds\n"
"v_mfma_f32_16x16x32_bf16 a[156:159],v[212:215],v[236:239],a[156:159]\n"
"v_mfma_f32_16x16x32_bf16 a[156:159],v[148:151],v[172:175],a[156:159]\n"
"v_mfma_f32_16x16x32_bf16 a[164:167],v[148:151],v[176:179],a[164:167]\n"
"v_mfma_f32_16x16x32_bf16 a[164:167],v[212:215],v[240:243],a[164:167]\n"
"v_mfma_f32_16x16x32_bf16 a[172:175],v[212:215],v[244:247],a[172:175]\n"
"buffer_load_dwordx4 v109,s[16:19],s76 offen offset:3072 lds\n"
"v_mfma_f32_16x16x32_bf16 a[172:175],v[148:151],v[180:183],a[172:175]\n"
"s_mov_b32 s87,3145728\n"
"v_mfma_f32_16x16x32_bf16 a[180:183],v[148:151],v[184:187],a[180:183]\n"
"buffer_load_dword v115,v112,s[40:43],s87 offen\n"
"v_mfma_f32_16x16x32_bf16 a[180:183],v[212:215],v[248:251],a[180:183]\n"
"s_mov_b32 s87,4194304\n"
"v_mfma_f32_16x16x32_bf16 a[188:191],v[212:215],v[252:255],a[188:191]\n"
"buffer_load_dword v115,v112,s[40:43],s87 offen\n"
"v_mfma_f32_16x16x32_bf16 a[188:191],v[148:151],v[188:191],a[188:191]\n"
"s_mov_b32 s87,5242880\n"
"s_waitcnt vmcnt(33)\n"
"s_barrier\n"
"v_mfma_f32_16x16x32_bf16 a[192:195],v[216:219],v[224:227],a[192:195]\n"
"ds_read_b128 v[152:155],v80 offset:45056\n"
"buffer_load_dword v115,v112,s[40:43],s87 offen\n"
"v_mfma_f32_16x16x32_bf16 a[196:199],v[220:223],v[224:227],a[196:199]\n"
"ds_read_b128 v[156:159],v80 offset:47104\n"
"v_mfma_f32_16x16x32_bf16 a[204:207],v[220:223],v[228:231],a[204:207]\n"
"ds_read_b64_tr_b16 v[160:161],v98 offset:49152\n"
"v_mfma_f32_16x16x32_bf16 a[200:203],v[216:219],v[228:231],a[200:203]\n"
"ds_read_b64_tr_b16 v[162:163],v98 offset:50176\n"
"v_mfma_f32_16x16x32_bf16 a[208:211],v[216:219],v[232:235],a[208:211]\n"
"ds_read_b64_tr_b16 v[164:165],v99 offset:49152\n"
"v_mfma_f32_16x16x32_bf16 a[212:215],v[220:223],v[232:235],a[212:215]\n"
"ds_read_b64_tr_b16 v[166:167],v99 offset:50176\n"
"v_mfma_f32_16x16x32_bf16 a[220:223],v[220:223],v[236:239],a[220:223]\n"
"ds_read_b64_tr_b16 v[168:169],v100 offset:49152\n"
"v_mfma_f32_16x16x32_bf16 a[216:219],v[216:219],v[236:239],a[216:219]\n"
"ds_read_b64_tr_b16 v[170:171],v100 offset:50176\n"
"v_mfma_f32_16x16x32_bf16 a[224:227],v[216:219],v[240:243],a[224:227]\n"
"ds_read_b64_tr_b16 v[172:173],v101 offset:49152\n"
"v_mfma_f32_16x16x32_bf16 a[228:231],v[220:223],v[240:243],a[228:231]\n"
"ds_read_b64_tr_b16 v[174:175],v101 offset:50176\n"
"v_mfma_f32_16x16x32_bf16 a[236:239],v[220:223],v[244:247],a[236:239]\n"
"ds_read_b64_tr_b16 v[176:177],v102 offset:49152\n"
"v_mfma_f32_16x16x32_bf16 a[232:235],v[216:219],v[244:247],a[232:235]\n"
"ds_read_b64_tr_b16 v[178:179],v102 offset:50176\n"
"v_mfma_f32_16x16x32_bf16 a[240:243],v[216:219],v[248:251],a[240:243]\n"
"ds_read_b64_tr_b16 v[180:181],v103 offset:49152\n"
"v_mfma_f32_16x16x32_bf16 a[244:247],v[220:223],v[248:251],a[244:247]\n"
"ds_read_b64_tr_b16 v[182:183],v103 offset:50176\n"
"v_mfma_f32_16x16x32_bf16 a[252:255],v[220:223],v[252:255],a[252:255]\n"
"ds_read_b64_tr_b16 v[184:185],v104 offset:49152\n"
"ds_read_b64_tr_b16 v[186:187],v104 offset:50176\n"
"v_mfma_f32_16x16x32_bf16 a[248:251],v[216:219],v[252:255],a[248:251]\n"
"s_waitcnt vmcnt(19)\n"
"s_waitcnt lgkmcnt(10)\n"
"v_mfma_f32_16x16x32_bf16 a[192:195],v[152:155],v[160:163],a[192:195]\n"
"ds_read_b64_tr_b16 v[188:189],v105 offset:49152\n"
"ds_read_b64_tr_b16 v[190:191],v105 offset:50176\n"
"v_mfma_f32_16x16x32_bf16 a[196:199],v[156:159],v[160:163],a[196:199]\n"
"buffer_store_dwordx2 v[124:125],off,s[24:27],s57 sc1\n"
"v_mfma_f32_16x16x32_bf16 a[204:207],v[156:159],v[164:167],a[204:207]\n"
"ds_read_b128 v[128:131],v80 offset:32768\n"
"v_mfma_f32_16x16x32_bf16 a[200:203],v[152:155],v[164:167],a[200:203]\n"
"ds_read_b128 v[192:195],v81 offset:32768\n"
"s_waitcnt lgkmcnt(10)\n"
"v_mfma_f32_16x16x32_bf16 a[208:211],v[152:155],v[168:171],a[208:211]\n"
"ds_read_b64_tr_b16 v[224:225],v98 offset:32768\n"
"v_mfma_f32_16x16x32_bf16 a[212:215],v[156:159],v[168:171],a[212:215]\n"
"ds_read_b64_tr_b16 v[226:227],v98 offset:33792\n"
"v_mfma_f32_16x16x32_bf16 a[220:223],v[156:159],v[172:175],a[220:223]\n"
"ds_read_b64_tr_b16 v[228:229],v99 offset:32768\n"
"v_mfma_f32_16x16x32_bf16 a[216:219],v[152:155],v[172:175],a[216:219]\n"
"ds_read_b64_tr_b16 v[230:231],v99 offset:33792\n"
"s_waitcnt lgkmcnt(10)\n"
"v_mfma_f32_16x16x32_bf16 a[224:227],v[152:155],v[176:179],a[224:227]\n"
"ds_read_b64_tr_b16 v[232:233],v100 offset:32768\n"
"v_mfma_f32_16x16x32_bf16 a[228:231],v[156:159],v[176:179],a[228:231]\n"
"ds_read_b64_tr_b16 v[234:235],v100 offset:33792\n"
"v_mfma_f32_16x16x32_bf16 a[236:239],v[156:159],v[180:183],a[236:239]\n"
"ds_read_b64_tr_b16 v[236:237],v101 offset:32768\n"
"v_mfma_f32_16x16x32_bf16 a[232:235],v[152:155],v[180:183],a[232:235]\n"
"ds_read_b64_tr_b16 v[238:239],v101 offset:33792\n"
"s_waitcnt lgkmcnt(10)\n"
"v_mfma_f32_16x16x32_bf16 a[240:243],v[152:155],v[184:187],a[240:243]\n"
"ds_read_b64_tr_b16 v[240:241],v102 offset:32768\n"
"v_mfma_f32_16x16x32_bf16 a[244:247],v[156:159],v[184:187],a[244:247]\n"
"ds_read_b64_tr_b16 v[242:243],v102 offset:33792\n"
"v_mfma_f32_16x16x32_bf16 a[252:255],v[156:159],v[188:191],a[252:255]\n"
"ds_read_b64_tr_b16 v[244:245],v103 offset:32768\n"
"v_mfma_f32_16x16x32_bf16 a[248:251],v[152:155],v[188:191],a[248:251]\n"
"ds_read_b64_tr_b16 v[246:247],v103 offset:33792\n"
"s_waitcnt lgkmcnt(8)\n"
"v_mfma_f32_16x16x32_bf16 a[0:3],v[128:131],v[160:163],a[0:3]\n"
"ds_read_b64_tr_b16 v[248:249],v104 offset:32768\n"
"v_mfma_f32_16x16x32_bf16 a[0:3],v[192:195],v[224:227],a[0:3]\n"
"ds_read_b64_tr_b16 v[250:251],v104 offset:33792\n"
"v_mfma_f32_16x16x32_bf16 a[8:11],v[192:195],v[228:231],a[8:11]\n"
"ds_read_b64_tr_b16 v[252:253],v105 offset:32768\n"
"v_mfma_f32_16x16x32_bf16 a[8:11],v[128:131],v[164:167],a[8:11]\n"
"ds_read_b64_tr_b16 v[254:255],v105 offset:33792\n"
"s_waitcnt lgkmcnt(8)\n"
"v_mfma_f32_16x16x32_bf16 a[16:19],v[128:131],v[168:171],a[16:19]\n"
"ds_read_b128 v[132:135],v80 offset:34816\n"
"v_mfma_f32_16x16x32_bf16 a[16:19],v[192:195],v[232:235],a[16:19]\n"
"ds_read_b128 v[196:199],v81 offset:34816\n"
"v_mfma_f32_16x16x32_bf16 a[24:27],v[192:195],v[236:239],a[24:27]\n"
"ds_read_b128 v[136:139],v80 offset:36864\n"
"v_mfma_f32_16x16x32_bf16 a[24:27],v[128:131],v[172:175],a[24:27]\n"
"ds_read_b128 v[200:203],v81 offset:36864\n"
"s_waitcnt lgkmcnt(8)\n"
"v_mfma_f32_16x16x32_bf16 a[32:35],v[128:131],v[176:179],a[32:35]\n"
"ds_read_b128 v[140:143],v80 offset:38912\n"
"v_mfma_f32_16x16x32_bf16 a[32:35],v[192:195],v[240:243],a[32:35]\n"
"ds_read_b128 v[204:207],v81 offset:38912\n"
"v_mfma_f32_16x16x32_bf16 a[40:43],v[192:195],v[244:247],a[40:43]\n"
"ds_read_b128 v[144:147],v80 offset:40960\n"
"v_mfma_f32_16x16x32_bf16 a[40:43],v[128:131],v[180:183],a[40:43]\n"
"ds_read_b128 v[208:211],v81 offset:40960\n"
"s_waitcnt lgkmcnt(8)\n"
"v_mfma_f32_16x16x32_bf16 a[48:51],v[128:131],v[184:187],a[48:51]\n"
"ds_read_b128 v[148:151],v80 offset:43008\n"
"v_mfma_f32_16x16x32_bf16 a[48:51],v[192:195],v[248:251],a[48:51]\n"
"ds_read_b128 v[212:215],v81 offset:43008\n"
"v_mfma_f32_16x16x32_bf16 a[56:59],v[192:195],v[252:255],a[56:59]\n"
"ds_read_b128 v[216:219],v81 offset:45056\n"
"v_mfma_f32_16x16x32_bf16 a[56:59],v[128:131],v[188:191],a[56:59]\n"
"ds_read_b128 v[220:223],v81 offset:47104\n"
"s_waitcnt lgkmcnt(10)\n"
"v_mfma_f32_16x16x32_bf16 a[4:7],v[132:135],v[160:163],a[4:7]\n"
"v_mfma_f32_16x16x32_bf16 a[4:7],v[196:199],v[224:227],a[4:7]\n"
"v_mfma_f32_16x16x32_bf16 a[12:15],v[196:199],v[228:231],a[12:15]\n"
"v_mfma_f32_16x16x32_bf16 a[12:15],v[132:135],v[164:167],a[12:15]\n"
"v_mfma_f32_16x16x32_bf16 a[20:23],v[132:135],v[168:171],a[20:23]\n"
"s_waitcnt lgkmcnt(0)\n"
"s_barrier\n"
"v_mfma_f32_16x16x32_bf16 a[20:23],v[196:199],v[232:235],a[20:23]\n"
"s_mov_b32 s77,3145728\n"
"s_add_u32 m0,s78,147456\n"
"v_mfma_f32_16x16x32_bf16 a[28:31],v[196:199],v[236:239],a[28:31]\n"
"buffer_load_dwordx4 v110,s[20:23],s77 offen offset:0 lds\n"
"v_mfma_f32_16x16x32_bf16 a[28:31],v[132:135],v[172:175],a[28:31]\n"
"v_mfma_f32_16x16x32_bf16 a[36:39],v[132:135],v[176:179],a[36:39]\n"
"v_mfma_f32_16x16x32_bf16 a[36:39],v[196:199],v[240:243],a[36:39]\n"
"v_mfma_f32_16x16x32_bf16 a[44:47],v[196:199],v[244:247],a[44:47]\n"
"buffer_load_dwordx4 v111,s[20:23],s77 offen offset:1024 lds\n"
"v_mfma_f32_16x16x32_bf16 a[44:47],v[132:135],v[180:183],a[44:47]\n"
"v_mfma_f32_16x16x32_bf16 a[52:55],v[132:135],v[184:187],a[52:55]\n"
"v_mfma_f32_16x16x32_bf16 a[52:55],v[196:199],v[248:251],a[52:55]\n"
"v_mfma_f32_16x16x32_bf16 a[60:63],v[196:199],v[252:255],a[60:63]\n"
"buffer_load_dwordx4 v113,s[20:23],s77 offen offset:2048 lds\n"
"v_mfma_f32_16x16x32_bf16 a[60:63],v[132:135],v[188:191],a[60:63]\n"
"v_mfma_f32_16x16x32_bf16 a[64:67],v[136:139],v[160:163],a[64:67]\n"
"v_mfma_f32_16x16x32_bf16 a[64:67],v[200:203],v[224:227],a[64:67]\n"
"v_mfma_f32_16x16x32_bf16 a[72:75],v[200:203],v[228:231],a[72:75]\n"
"buffer_load_dwordx4 v114,s[20:23],s77 offen offset:3072 lds\n"
"v_mfma_f32_16x16x32_bf16 a[72:75],v[136:139],v[164:167],a[72:75]\n"
"v_mfma_f32_16x16x32_bf16 a[80:83],v[136:139],v[168:171],a[80:83]\n"
"v_mfma_f32_16x16x32_bf16 a[80:83],v[200:203],v[232:235],a[80:83]\n"
"s_mov_b32 s77,3407872\n"
"s_add_u32 m0,s78,131072\n"
"v_mfma_f32_16x16x32_bf16 a[88:91],v[200:203],v[236:239],a[88:91]\n"
"buffer_load_dwordx4 v110,s[20:23],s77 offen offset:0 lds\n"
"v_mfma_f32_16x16x32_bf16 a[88:91],v[136:139],v[172:175],a[88:91]\n"
"v_mfma_f32_16x16x32_bf16 a[96:99],v[136:139],v[176:179],a[96:99]\n"
"v_mfma_f32_16x16x32_bf16 a[96:99],v[200:203],v[240:243],a[96:99]\n"
"v_mfma_f32_16x16x32_bf16 a[104:107],v[200:203],v[244:247],a[104:107]\n"
"buffer_load_dwordx4 v111,s[20:23],s77 offen offset:1024 lds\n"
"v_mfma_f32_16x16x32_bf16 a[104:107],v[136:139],v[180:183],a[104:107]\n"
"v_mfma_f32_16x16x32_bf16 a[112:115],v[136:139],v[184:187],a[112:115]\n"
"v_mfma_f32_16x16x32_bf16 a[112:115],v[200:203],v[248:251],a[112:115]\n"
"v_mfma_f32_16x16x32_bf16 a[120:123],v[200:203],v[252:255],a[120:123]\n"
"buffer_load_dwordx4 v113,s[20:23],s77 offen offset:2048 lds\n"
"v_mfma_f32_16x16x32_bf16 a[120:123],v[136:139],v[188:191],a[120:123]\n"
"v_mfma_f32_16x16x32_bf16 a[68:71],v[140:143],v[160:163],a[68:71]\n"
"v_mfma_f32_16x16x32_bf16 a[68:71],v[204:207],v[224:227],a[68:71]\n"
"v_mfma_f32_16x16x32_bf16 a[76:79],v[204:207],v[228:231],a[76:79]\n"
"buffer_load_dwordx4 v114,s[20:23],s77 offen offset:3072 lds\n"
"v_mfma_f32_16x16x32_bf16 a[76:79],v[140:143],v[164:167],a[76:79]\n"
"v_mfma_f32_16x16x32_bf16 a[84:87],v[140:143],v[168:171],a[84:87]\n"
"v_mfma_f32_16x16x32_bf16 a[84:87],v[204:207],v[232:235],a[84:87]\n"
"v_mfma_f32_16x16x32_bf16 a[92:95],v[204:207],v[236:239],a[92:95]\n"
"s_mov_b32 s76,896\n"
"s_add_u32 m0,s79,32768\n"
"v_mfma_f32_16x16x32_bf16 a[92:95],v[140:143],v[172:175],a[92:95]\n"
"buffer_load_dwordx4 v106,s[16:19],s76 offen offset:0 lds\n"
"v_mfma_f32_16x16x32_bf16 a[100:103],v[140:143],v[176:179],a[100:103]\n"
"v_mfma_f32_16x16x32_bf16 a[100:103],v[204:207],v[240:243],a[100:103]\n"
"v_mfma_f32_16x16x32_bf16 a[108:111],v[204:207],v[244:247],a[108:111]\n"
"v_mfma_f32_16x16x32_bf16 a[108:111],v[140:143],v[180:183],a[108:111]\n"
"v_mfma_f32_16x16x32_bf16 a[116:119],v[140:143],v[184:187],a[116:119]\n"
"buffer_load_dwordx4 v107,s[16:19],s76 offen offset:1024 lds\n"
"v_mfma_f32_16x16x32_bf16 a[116:119],v[204:207],v[248:251],a[116:119]\n"
"v_mfma_f32_16x16x32_bf16 a[124:127],v[204:207],v[252:255],a[124:127]\n"
"v_mfma_f32_16x16x32_bf16 a[124:127],v[140:143],v[188:191],a[124:127]\n"
"v_mfma_f32_16x16x32_bf16 a[128:131],v[144:147],v[160:163],a[128:131]\n"
"v_mfma_f32_16x16x32_bf16 a[128:131],v[208:211],v[224:227],a[128:131]\n"
"buffer_load_dwordx4 v108,s[16:19],s76 offen offset:2048 lds\n"
"v_mfma_f32_16x16x32_bf16 a[136:139],v[208:211],v[228:231],a[136:139]\n"
"v_mfma_f32_16x16x32_bf16 a[136:139],v[144:147],v[164:167],a[136:139]\n"
"v_mfma_f32_16x16x32_bf16 a[144:147],v[144:147],v[168:171],a[144:147]\n"
"v_mfma_f32_16x16x32_bf16 a[144:147],v[208:211],v[232:235],a[144:147]\n"
"v_mfma_f32_16x16x32_bf16 a[152:155],v[208:211],v[236:239],a[152:155]\n"
"buffer_load_dwordx4 v109,s[16:19],s76 offen offset:3072 lds\n"
"v_mfma_f32_16x16x32_bf16 a[152:155],v[144:147],v[172:175],a[152:155]\n"
"v_mfma_f32_16x16x32_bf16 a[160:163],v[144:147],v[176:179],a[160:163]\n"
"v_mfma_f32_16x16x32_bf16 a[160:163],v[208:211],v[240:243],a[160:163]\n"
"v_mfma_f32_16x16x32_bf16 a[168:171],v[208:211],v[244:247],a[168:171]\n"
"s_mov_b32 s76,263040\n"
"s_add_u32 m0,s79,36864\n"
"v_mfma_f32_16x16x32_bf16 a[168:171],v[144:147],v[180:183],a[168:171]\n"
"buffer_load_dwordx4 v106,s[16:19],s76 offen offset:0 lds\n"
"v_mfma_f32_16x16x32_bf16 a[176:179],v[144:147],v[184:187],a[176:179]\n"
"v_mfma_f32_16x16x32_bf16 a[176:179],v[208:211],v[248:251],a[176:179]\n"
"v_mfma_f32_16x16x32_bf16 a[184:187],v[208:211],v[252:255],a[184:187]\n"
"v_mfma_f32_16x16x32_bf16 a[184:187],v[144:147],v[188:191],a[184:187]\n"
"v_mfma_f32_16x16x32_bf16 a[132:135],v[148:151],v[160:163],a[132:135]\n"
"buffer_load_dwordx4 v107,s[16:19],s76 offen offset:1024 lds\n"
"v_mfma_f32_16x16x32_bf16 a[132:135],v[212:215],v[224:227],a[132:135]\n"
"v_mfma_f32_16x16x32_bf16 a[140:143],v[212:215],v[228:231],a[140:143]\n"
"v_mfma_f32_16x16x32_bf16 a[140:143],v[148:151],v[164:167],a[140:143]\n"
"v_mfma_f32_16x16x32_bf16 a[148:151],v[148:151],v[168:171],a[148:151]\n"
"v_mfma_f32_16x16x32_bf16 a[148:151],v[212:215],v[232:235],a[148:151]\n"
"buffer_load_dwordx4 v108,s[16:19],s76 offen offset:2048 lds\n"
"v_mfma_f32_16x16x32_bf16 a[156:159],v[212:215],v[236:239],a[156:159]\n"
"v_mfma_f32_16x16x32_bf16 a[156:159],v[148:151],v[172:175],a[156:159]\n"
"v_mfma_f32_16x16x32_bf16 a[164:167],v[148:151],v[176:179],a[164:167]\n"
"v_mfma_f32_16x16x32_bf16 a[164:167],v[212:215],v[240:243],a[164:167]\n"
"v_mfma_f32_16x16x32_bf16 a[172:175],v[212:215],v[244:247],a[172:175]\n"
"buffer_load_dwordx4 v109,s[16:19],s76 offen offset:3072 lds\n"
"v_mfma_f32_16x16x32_bf16 a[172:175],v[148:151],v[180:183],a[172:175]\n"
"s_mov_b32 s87,6291456\n"
"v_mfma_f32_16x16x32_bf16 a[180:183],v[148:151],v[184:187],a[180:183]\n"
"buffer_load_dword v115,v112,s[40:43],s87 offen\n"
"v_mfma_f32_16x16x32_bf16 a[180:183],v[212:215],v[248:251],a[180:183]\n"
"s_mov_b32 s87,7340032\n"
"v_mfma_f32_16x16x32_bf16 a[188:191],v[212:215],v[252:255],a[188:191]\n"
"buffer_load_dword v115,v112,s[40:43],s87 offen\n"
"v_mfma_f32_16x16x32_bf16 a[188:191],v[148:151],v[188:191],a[188:191]\n"
"s_waitcnt vmcnt(30)\n"
"s_barrier\n"
"v_mfma_f32_16x16x32_bf16 a[192:195],v[216:219],v[224:227],a[192:195]\n"
"ds_read_b128 v[152:155],v82 offset:12288\n"
"v_mfma_f32_16x16x32_bf16 a[196:199],v[220:223],v[224:227],a[196:199]\n"
"ds_read_b128 v[156:159],v82 offset:14336\n"
"v_mfma_f32_16x16x32_bf16 a[204:207],v[220:223],v[228:231],a[204:207]\n"
"ds_read_b64_tr_b16 v[160:161],v98 offset:0\n"
"v_mfma_f32_16x16x32_bf16 a[200:203],v[216:219],v[228:231],a[200:203]\n"
"ds_read_b64_tr_b16 v[162:163],v98 offset:1024\n"
"v_mfma_f32_16x16x32_bf16 a[208:211],v[216:219],v[232:235],a[208:211]\n"
"ds_read_b64_tr_b16 v[164:165],v99 offset:0\n"
"v_mfma_f32_16x16x32_bf16 a[212:215],v[220:223],v[232:235],a[212:215]\n"
"ds_read_b64_tr_b16 v[166:167],v99 offset:1024\n"
"v_mfma_f32_16x16x32_bf16 a[220:223],v[220:223],v[236:239],a[220:223]\n"
"ds_read_b64_tr_b16 v[168:169],v100 offset:0\n"
"v_mfma_f32_16x16x32_bf16 a[216:219],v[216:219],v[236:239],a[216:219]\n"
"ds_read_b64_tr_b16 v[170:171],v100 offset:1024\n"
"v_mfma_f32_16x16x32_bf16 a[224:227],v[216:219],v[240:243],a[224:227]\n"
"ds_read_b64_tr_b16 v[172:173],v101 offset:0\n"
"v_mfma_f32_16x16x32_bf16 a[228:231],v[220:223],v[240:243],a[228:231]\n"
"ds_read_b64_tr_b16 v[174:175],v101 offset:1024\n"
"v_mfma_f32_16x16x32_bf16 a[236:239],v[220:223],v[244:247],a[236:239]\n"
"ds_read_b64_tr_b16 v[176:177],v102 offset:0\n"
"v_mfma_f32_16x16x32_bf16 a[232:235],v[216:219],v[244:247],a[232:235]\n"
"ds_read_b64_tr_b16 v[178:179],v102 offset:1024\n"
"v_mfma_f32_16x16x32_bf16 a[240:243],v[216:219],v[248:251],a[240:243]\n"
"ds_read_b64_tr_b16 v[180:181],v103 offset:0\n"
"v_mfma_f32_16x16x32_bf16 a[244:247],v[220:223],v[248:251],a[244:247]\n"
"ds_read_b64_tr_b16 v[182:183],v103 offset:1024\n"
"v_mfma_f32_16x16x32_bf16 a[252:255],v[220:223],v[252:255],a[252:255]\n"
"ds_read_b64_tr_b16 v[184:185],v104 offset:0\n"
"v_mfma_f32_16x16x32_bf16 a[248:251],v[216:219],v[252:255],a[248:251]\n"
"ds_read_b64_tr_b16 v[186:187],v104 offset:1024\n"
"s_waitcnt lgkmcnt(10)\n"
"v_mfma_f32_16x16x32_bf16 a[192:195],v[152:155],v[160:163],a[192:195]\n"
"ds_read_b64_tr_b16 v[188:189],v105 offset:0\n"
"v_mfma_f32_16x16x32_bf16 a[196:199],v[156:159],v[160:163],a[196:199]\n"
"ds_read_b64_tr_b16 v[190:191],v105 offset:1024\n"
"v_mfma_f32_16x16x32_bf16 a[204:207],v[156:159],v[164:167],a[204:207]\n"
"ds_read_b128 v[128:131],v82 offset:0\n"
"v_mfma_f32_16x16x32_bf16 a[200:203],v[152:155],v[164:167],a[200:203]\n"
"ds_read_b128 v[192:195],v83 offset:0\n"
"s_waitcnt lgkmcnt(10)\n"
"v_mfma_f32_16x16x32_bf16 a[208:211],v[152:155],v[168:171],a[208:211]\n"
"ds_read_b64_tr_b16 v[224:225],v98 offset:16384\n"
"v_mfma_f32_16x16x32_bf16 a[212:215],v[156:159],v[168:171],a[212:215]\n"
"ds_read_b64_tr_b16 v[226:227],v98 offset:17408\n"
"v_mfma_f32_16x16x32_bf16 a[220:223],v[156:159],v[172:175],a[220:223]\n"
"ds_read_b64_tr_b16 v[228:229],v99 offset:16384\n"
"v_mfma_f32_16x16x32_bf16 a[216:219],v[152:155],v[172:175],a[216:219]\n"
"ds_read_b64_tr_b16 v[230:231],v99 offset:17408\n"
"s_waitcnt lgkmcnt(10)\n"
"v_mfma_f32_16x16x32_bf16 a[224:227],v[152:155],v[176:179],a[224:227]\n"
"ds_read_b64_tr_b16 v[232:233],v100 offset:16384\n"
"v_mfma_f32_16x16x32_bf16 a[228:231],v[156:159],v[176:179],a[228:231]\n"
"ds_read_b64_tr_b16 v[234:235],v100 offset:17408\n"
"v_mfma_f32_16x16x32_bf16 a[236:239],v[156:159],v[180:183],a[236:239]\n"
"ds_read_b64_tr_b16 v[236:237],v101 offset:16384\n"
"v_mfma_f32_16x16x32_bf16 a[232:235],v[152:155],v[180:183],a[232:235]\n"
"ds_read_b64_tr_b16 v[238:239],v101 offset:17408\n"
"s_waitcnt lgkmcnt(10)\n"
"v_mfma_f32_16x16x32_bf16 a[240:243],v[152:155],v[184:187],a[240:243]\n"
"ds_read_b64_tr_b16 v[240:241],v102 offset:16384\n"
"v_mfma_f32_16x16x32_bf16 a[244:247],v[156:159],v[184:187],a[244:247]\n"
"ds_read_b64_tr_b16 v[242:243],v102 offset:17408\n"
"v_mfma_f32_16x16x32_bf16 a[252:255],v[156:159],v[188:191],a[252:255]\n"
"ds_read_b64_tr_b16 v[244:245],v103 offset:16384\n"
"v_mfma_f32_16x16x32_bf16 a[248:251],v[152:155],v[188:191],a[248:251]\n"
"ds_read_b64_tr_b16 v[246:247],v103 offset:17408\n"
"s_waitcnt lgkmcnt(8)\n"
"v_mfma_f32_16x16x32_bf16 a[0:3],v[128:131],v[160:163],a[0:3]\n"
"ds_read_b64_tr_b16 v[248:249],v104 offset:16384\n"
"v_mfma_f32_16x16x32_bf16 a[0:3],v[192:195],v[224:227],a[0:3]\n"
"ds_read_b64_tr_b16 v[250:251],v104 offset:17408\n"
"v_mfma_f32_16x16x32_bf16 a[8:11],v[192:195],v[228:231],a[8:11]\n"
"ds_read_b64_tr_b16 v[252:253],v105 offset:16384\n"
"v_mfma_f32_16x16x32_bf16 a[8:11],v[128:131],v[164:167],a[8:11]\n"
"ds_read_b64_tr_b16 v[254:255],v105 offset:17408\n"
"s_waitcnt lgkmcnt(8)\n"
"v_mfma_f32_16x16x32_bf16 a[16:19],v[128:131],v[168:171],a[16:19]\n"
"ds_read_b128 v[132:135],v82 offset:2048\n"
"v_mfma_f32_16x16x32_bf16 a[16:19],v[192:195],v[232:235],a[16:19]\n"
"ds_read_b128 v[196:199],v83 offset:2048\n"
"v_mfma_f32_16x16x32_bf16 a[24:27],v[192:195],v[236:239],a[24:27]\n"
"ds_read_b128 v[136:139],v82 offset:4096\n"
"v_mfma_f32_16x16x32_bf16 a[24:27],v[128:131],v[172:175],a[24:27]\n"
"ds_read_b128 v[200:203],v83 offset:4096\n"
"s_waitcnt lgkmcnt(8)\n"
"v_mfma_f32_16x16x32_bf16 a[32:35],v[128:131],v[176:179],a[32:35]\n"
"ds_read_b128 v[140:143],v82 offset:6144\n"
"v_mfma_f32_16x16x32_bf16 a[32:35],v[192:195],v[240:243],a[32:35]\n"
"ds_read_b128 v[204:207],v83 offset:6144\n"
"v_mfma_f32_16x16x32_bf16 a[40:43],v[192:195],v[244:247],a[40:43]\n"
"ds_read_b128 v[144:147],v82 offset:8192\n"
"v_mfma_f32_16x16x32_bf16 a[40:43],v[128:131],v[180:183],a[40:43]\n"
"ds_read_b128 v[208:211],v83 offset:8192\n"
"s_waitcnt lgkmcnt(8)\n"
"v_mfma_f32_16x16x32_bf16 a[48:51],v[128:131],v[184:187],a[48:51]\n"
"ds_read_b128 v[148:151],v82 offset:10240\n"
"v_mfma_f32_16x16x32_bf16 a[48:51],v[192:195],v[248:251],a[48:51]\n"
"ds_read_b128 v[212:215],v83 offset:10240\n"
"v_mfma_f32_16x16x32_bf16 a[56:59],v[192:195],v[252:255],a[56:59]\n"
"ds_read_b128 v[216:219],v83 offset:12288\n"
"v_mfma_f32_16x16x32_bf16 a[56:59],v[128:131],v[188:191],a[56:59]\n"
"ds_read_b128 v[220:223],v83 offset:14336\n"
"s_waitcnt lgkmcnt(10)\n"
"v_mfma_f32_16x16x32_bf16 a[4:7],v[132:135],v[160:163],a[4:7]\n"
"v_mfma_f32_16x16x32_bf16 a[4:7],v[196:199],v[224:227],a[4:7]\n"
"buffer_load_dwordx4 v[74:77],v112,s[24:27],s58 offen sc1\n"
"v_mfma_f32_16x16x32_bf16 a[12:15],v[196:199],v[228:231],a[12:15]\n"
"v_mfma_f32_16x16x32_bf16 a[12:15],v[132:135],v[164:167],a[12:15]\n"
"v_mfma_f32_16x16x32_bf16 a[20:23],v[132:135],v[168:171],a[20:23]\n"
"s_waitcnt lgkmcnt(0)\n"
"s_barrier\n"
"v_mfma_f32_16x16x32_bf16 a[20:23],v[196:199],v[232:235],a[20:23]\n"
"s_mov_b32 s77,3670016\n"
"s_add_u32 m0,s78,98304\n"
"v_mfma_f32_16x16x32_bf16 a[28:31],v[196:199],v[236:239],a[28:31]\n"
"buffer_load_dwordx4 v110,s[20:23],s77 offen offset:0 lds\n"
"v_mfma_f32_16x16x32_bf16 a[28:31],v[132:135],v[172:175],a[28:31]\n"
"v_mfma_f32_16x16x32_bf16 a[36:39],v[132:135],v[176:179],a[36:39]\n"
"s_load_dwordx2 s[48:49],s[0:1],56\n"
"v_mfma_f32_16x16x32_bf16 a[36:39],v[196:199],v[240:243],a[36:39]\n"
"v_mfma_f32_16x16x32_bf16 a[44:47],v[196:199],v[244:247],a[44:47]\n"
"buffer_load_dwordx4 v111,s[20:23],s77 offen offset:1024 lds\n"
"v_mfma_f32_16x16x32_bf16 a[44:47],v[132:135],v[180:183],a[44:47]\n"
"v_mfma_f32_16x16x32_bf16 a[52:55],v[132:135],v[184:187],a[52:55]\n"
"v_mfma_f32_16x16x32_bf16 a[52:55],v[196:199],v[248:251],a[52:55]\n"
"v_mfma_f32_16x16x32_bf16 a[60:63],v[196:199],v[252:255],a[60:63]\n"
"buffer_load_dwordx4 v113,s[20:23],s77 offen offset:2048 lds\n"
"v_mfma_f32_16x16x32_bf16 a[60:63],v[132:135],v[188:191],a[60:63]\n"
"v_mfma_f32_16x16x32_bf16 a[64:67],v[136:139],v[160:163],a[64:67]\n"
"v_mfma_f32_16x16x32_bf16 a[64:67],v[200:203],v[224:227],a[64:67]\n"
"v_mfma_f32_16x16x32_bf16 a[72:75],v[200:203],v[228:231],a[72:75]\n"
"buffer_load_dwordx4 v114,s[20:23],s77 offen offset:3072 lds\n"
"v_mfma_f32_16x16x32_bf16 a[72:75],v[136:139],v[164:167],a[72:75]\n"
"v_mfma_f32_16x16x32_bf16 a[80:83],v[136:139],v[168:171],a[80:83]\n"
"v_mfma_f32_16x16x32_bf16 a[80:83],v[200:203],v[232:235],a[80:83]\n"
"s_mov_b32 s77,3932160\n"
"s_add_u32 m0,s78,114688\n"
"v_mfma_f32_16x16x32_bf16 a[88:91],v[200:203],v[236:239],a[88:91]\n"
"buffer_load_dwordx4 v110,s[20:23],s77 offen offset:0 lds\n"
"v_mfma_f32_16x16x32_bf16 a[88:91],v[136:139],v[172:175],a[88:91]\n"
"v_mfma_f32_16x16x32_bf16 a[96:99],v[136:139],v[176:179],a[96:99]\n"
"v_mfma_f32_16x16x32_bf16 a[96:99],v[200:203],v[240:243],a[96:99]\n"
"v_mfma_f32_16x16x32_bf16 a[104:107],v[200:203],v[244:247],a[104:107]\n"
"buffer_load_dwordx4 v111,s[20:23],s77 offen offset:1024 lds\n"
"v_mfma_f32_16x16x32_bf16 a[104:107],v[136:139],v[180:183],a[104:107]\n"
"v_mfma_f32_16x16x32_bf16 a[112:115],v[136:139],v[184:187],a[112:115]\n"
"v_mfma_f32_16x16x32_bf16 a[112:115],v[200:203],v[248:251],a[112:115]\n"
"v_mfma_f32_16x16x32_bf16 a[120:123],v[200:203],v[252:255],a[120:123]\n"
"buffer_load_dwordx4 v113,s[20:23],s77 offen offset:2048 lds\n"
"v_mfma_f32_16x16x32_bf16 a[120:123],v[136:139],v[188:191],a[120:123]\n"
"s_mov_b32 s76,s55\n"
"s_waitcnt vmcnt(7)\n"
"v_mfma_f32_16x16x32_bf16 a[68:71],v[140:143],v[160:163],a[68:71]\n"
"v_xor_b32 v116,s80,v74\n"
"v_xor_b32 v117,s81,v75\n"
"v_mfma_f32_16x16x32_bf16 a[68:71],v[204:207],v[224:227],a[68:71]\n"
"v_or_b32 v116,v116,v117\n"
"v_xor_b32 v117,s80,v76\n"
"v_mfma_f32_16x16x32_bf16 a[76:79],v[204:207],v[228:231],a[76:79]\n"
"buffer_load_dwordx4 v114,s[20:23],s77 offen offset:3072 lds\n"
"v_mfma_f32_16x16x32_bf16 a[76:79],v[140:143],v[164:167],a[76:79]\n"
"v_or_b32 v116,v116,v117\n"
"v_xor_b32 v117,s81,v77\n"
"v_mfma_f32_16x16x32_bf16 a[84:87],v[140:143],v[168:171],a[84:87]\n"
"v_or_b32 v116,v116,v117\n"
"v_cmp_ne_u32 vcc,0,v116\n"
"v_mfma_f32_16x16x32_bf16 a[84:87],v[204:207],v[232:235],a[84:87]\n"
"s_cbranch_vccnz .Lfk_xn1_s\n"
".Lfk_xn1_ok:\n"
"v_mfma_f32_16x16x32_bf16 a[92:95],v[204:207],v[236:239],a[92:95]\n"
"s_add_u32 m0,s78,65536\n"
"v_mfma_f32_16x16x32_bf16 a[92:95],v[140:143],v[172:175],a[92:95]\n"
"buffer_load_dwordx4 v112,s[24:27],s76 offen offset:0 sc1 lds\n"
"v_mfma_f32_16x16x32_bf16 a[100:103],v[140:143],v[176:179],a[100:103]\n"
"s_waitcnt lgkmcnt(0)\n"
"s_lshl2_add_u32 s88,s100,s52\n"
"v_mfma_f32_16x16x32_bf16 a[100:103],v[204:207],v[240:243],a[100:103]\n"
"s_mul_i32 s88,s88,7168\n"
"s_add_u32 s88,s88,0\n"
"v_mfma_f32_16x16x32_bf16 a[108:111],v[204:207],v[244:247],a[108:111]\n"
"s_add_u32 s48,s48,s88\n"
"s_addc_u32 s49,s49,0\n"
"s_mov_b32 s50,3584\n"
"v_mfma_f32_16x16x32_bf16 a[108:111],v[140:143],v[180:183],a[108:111]\n"
"s_mov_b32 s51,0x20000\n"
"v_lshlrev_b32 v8,3,v112\n"
"v_mfma_f32_16x16x32_bf16 a[116:119],v[140:143],v[184:187],a[116:119]\n"
"buffer_load_dwordx4 v112,s[24:27],s76 offen offset:1024 sc1 lds\n"
"v_mfma_f32_16x16x32_bf16 a[116:119],v[204:207],v[248:251],a[116:119]\n"
"v_mfma_f32_16x16x32_bf16 a[124:127],v[204:207],v[252:255],a[124:127]\n"
"v_mfma_f32_16x16x32_bf16 a[124:127],v[140:143],v[188:191],a[124:127]\n"
"v_mfma_f32_16x16x32_bf16 a[128:131],v[144:147],v[160:163],a[128:131]\n"
"v_mfma_f32_16x16x32_bf16 a[128:131],v[208:211],v[224:227],a[128:131]\n"
"buffer_load_dwordx4 v112,s[24:27],s76 offen offset:2048 sc1 lds\n"
"v_mfma_f32_16x16x32_bf16 a[136:139],v[208:211],v[228:231],a[136:139]\n"
"v_mfma_f32_16x16x32_bf16 a[136:139],v[144:147],v[164:167],a[136:139]\n"
"v_mfma_f32_16x16x32_bf16 a[144:147],v[144:147],v[168:171],a[144:147]\n"
"v_mfma_f32_16x16x32_bf16 a[144:147],v[208:211],v[232:235],a[144:147]\n"
"buffer_load_dwordx4 v112,s[24:27],s76 offen offset:3072 sc1 lds\n"
"v_mfma_f32_16x16x32_bf16 a[152:155],v[208:211],v[236:239],a[152:155]\n"
"s_xor_b32 s76,s76,16384\n"
"v_mfma_f32_16x16x32_bf16 a[152:155],v[144:147],v[172:175],a[152:155]\n"
"v_mfma_f32_16x16x32_bf16 a[160:163],v[144:147],v[176:179],a[160:163]\n"
"v_mfma_f32_16x16x32_bf16 a[160:163],v[208:211],v[240:243],a[160:163]\n"
"v_mfma_f32_16x16x32_bf16 a[168:171],v[208:211],v[244:247],a[168:171]\n"
"s_add_u32 m0,s78,81920\n"
"v_mfma_f32_16x16x32_bf16 a[168:171],v[144:147],v[180:183],a[168:171]\n"
"buffer_load_dwordx4 v112,s[24:27],s76 offen offset:0 sc1 lds\n"
"v_mfma_f32_16x16x32_bf16 a[176:179],v[144:147],v[184:187],a[176:179]\n"
"v_mfma_f32_16x16x32_bf16 a[176:179],v[208:211],v[248:251],a[176:179]\n"
"v_mfma_f32_16x16x32_bf16 a[184:187],v[208:211],v[252:255],a[184:187]\n"
"v_mfma_f32_16x16x32_bf16 a[184:187],v[144:147],v[188:191],a[184:187]\n"
"v_mfma_f32_16x16x32_bf16 a[132:135],v[148:151],v[160:163],a[132:135]\n"
"buffer_load_dwordx4 v112,s[24:27],s76 offen offset:1024 sc1 lds\n"
"v_mfma_f32_16x16x32_bf16 a[132:135],v[212:215],v[224:227],a[132:135]\n"
"v_mfma_f32_16x16x32_bf16 a[140:143],v[212:215],v[228:231],a[140:143]\n"
"v_mfma_f32_16x16x32_bf16 a[140:143],v[148:151],v[164:167],a[140:143]\n"
"v_mfma_f32_16x16x32_bf16 a[148:151],v[148:151],v[168:171],a[148:151]\n"
"v_mfma_f32_16x16x32_bf16 a[148:151],v[212:215],v[232:235],a[148:151]\n"
"buffer_load_dwordx4 v112,s[24:27],s76 offen offset:2048 sc1 lds\n"
"v_mfma_f32_16x16x32_bf16 a[156:159],v[212:215],v[236:239],a[156:159]\n"
"v_mfma_f32_16x16x32_bf16 a[156:159],v[148:151],v[172:175],a[156:159]\n"
"v_mfma_f32_16x16x32_bf16 a[164:167],v[148:151],v[176:179],a[164:167]\n"
"v_mfma_f32_16x16x32_bf16 a[164:167],v[212:215],v[240:243],a[164:167]\n"
"buffer_load_dwordx4 v112,s[24:27],s76 offen offset:3072 sc1 lds\n"
"v_mfma_f32_16x16x32_bf16 a[172:175],v[212:215],v[244:247],a[172:175]\n"
"s_xor_b32 s76,s76,49152\n"
"v_mfma_f32_16x16x32_bf16 a[172:175],v[148:151],v[180:183],a[172:175]\n"
"v_mfma_f32_16x16x32_bf16 a[180:183],v[148:151],v[184:187],a[180:183]\n"
"buffer_load_dword v115,v8,s[48:51],0 offen\n"
"v_mfma_f32_16x16x32_bf16 a[180:183],v[212:215],v[248:251],a[180:183]\n"
"v_mfma_f32_16x16x32_bf16 a[188:191],v[212:215],v[252:255],a[188:191]\n"
"v_mfma_f32_16x16x32_bf16 a[188:191],v[148:151],v[188:191],a[188:191]\n"
"s_barrier\n"
"v_mfma_f32_16x16x32_bf16 a[192:195],v[216:219],v[224:227],a[192:195]\n"
"ds_read_b128 v[152:155],v80 offset:12288\n"
"v_mfma_f32_16x16x32_bf16 a[196:199],v[220:223],v[224:227],a[196:199]\n"
"ds_read_b128 v[156:159],v80 offset:14336\n"
"v_mfma_f32_16x16x32_bf16 a[204:207],v[220:223],v[228:231],a[204:207]\n"
"ds_read_b64_tr_b16 v[160:161],v98 offset:49152\n"
"v_mfma_f32_16x16x32_bf16 a[200:203],v[216:219],v[228:231],a[200:203]\n"
"ds_read_b64_tr_b16 v[162:163],v98 offset:50176\n"
"v_mfma_f32_16x16x32_bf16 a[208:211],v[216:219],v[232:235],a[208:211]\n"
"ds_read_b64_tr_b16 v[164:165],v99 offset:49152\n"
"v_mfma_f32_16x16x32_bf16 a[212:215],v[220:223],v[232:235],a[212:215]\n"
"ds_read_b64_tr_b16 v[166:167],v99 offset:50176\n"
"v_mfma_f32_16x16x32_bf16 a[220:223],v[220:223],v[236:239],a[220:223]\n"
"ds_read_b64_tr_b16 v[168:169],v100 offset:49152\n"
"v_mfma_f32_16x16x32_bf16 a[216:219],v[216:219],v[236:239],a[216:219]\n"
"ds_read_b64_tr_b16 v[170:171],v100 offset:50176\n"
"v_mfma_f32_16x16x32_bf16 a[224:227],v[216:219],v[240:243],a[224:227]\n"
"ds_read_b64_tr_b16 v[172:173],v101 offset:49152\n"
"v_mfma_f32_16x16x32_bf16 a[228:231],v[220:223],v[240:243],a[228:231]\n"
"ds_read_b64_tr_b16 v[174:175],v101 offset:50176\n"
"v_mfma_f32_16x16x32_bf16 a[236:239],v[220:223],v[244:247],a[236:239]\n"
"ds_read_b64_tr_b16 v[176:177],v102 offset:49152\n"
"v_mfma_f32_16x16x32_bf16 a[232:235],v[216:219],v[244:247],a[232:235]\n"
"ds_read_b64_tr_b16 v[178:179],v102 offset:50176\n"
"v_mfma_f32_16x16x32_bf16 a[240:243],v[216:219],v[248:251],a[240:243]\n"
"ds_read_b64_tr_b16 v[180:181],v103 offset:49152\n"
"v_mfma_f32_16x16x32_bf16 a[244:247],v[220:223],v[248:251],a[244:247]\n"
"ds_read_b64_tr_b16 v[182:183],v103 offset:50176\n"
"ds_read_b64_tr_b16 v[184:185],v104 offset:49152\n"
"v_mfma_f32_16x16x32_bf16 a[252:255],v[220:223],v[252:255],a[252:255]\n"
"v_mov_b32 v110,v72\n"
"v_mov_b32 v111,v73\n"
"v_mfma_f32_16x16x32_bf16 a[248:251],v[216:219],v[252:255],a[248:251]\n"
"ds_read_b64_tr_b16 v[186:187],v104 offset:50176\n"
"s_waitcnt lgkmcnt(10)\n"
"v_mfma_f32_16x16x32_bf16 a[192:195],v[152:155],v[160:163],a[192:195]\n"
"ds_read_b64_tr_b16 v[188:189],v105 offset:49152\n"
"v_mfma_f32_16x16x32_bf16 a[196:199],v[156:159],v[160:163],a[196:199]\n"
"ds_read_b64_tr_b16 v[190:191],v105 offset:50176\n"
"v_mfma_f32_16x16x32_bf16 a[204:207],v[156:159],v[164:167],a[204:207]\n"
"ds_read_b128 v[128:131],v80 offset:0\n"
"v_mfma_f32_16x16x32_bf16 a[200:203],v[152:155],v[164:167],a[200:203]\n"
"ds_read_b128 v[192:195],v81 offset:0\n"
"s_waitcnt lgkmcnt(10)\n"
"v_mfma_f32_16x16x32_bf16 a[208:211],v[152:155],v[168:171],a[208:211]\n"
"ds_read_b64_tr_b16 v[224:225],v98 offset:32768\n"
"v_mfma_f32_16x16x32_bf16 a[212:215],v[156:159],v[168:171],a[212:215]\n"
"ds_read_b64_tr_b16 v[226:227],v98 offset:33792\n"
"v_mfma_f32_16x16x32_bf16 a[220:223],v[156:159],v[172:175],a[220:223]\n"
"ds_read_b64_tr_b16 v[228:229],v99 offset:32768\n"
"v_mfma_f32_16x16x32_bf16 a[216:219],v[152:155],v[172:175],a[216:219]\n"
"ds_read_b64_tr_b16 v[230:231],v99 offset:33792\n"
"s_waitcnt lgkmcnt(10)\n"
"v_mfma_f32_16x16x32_bf16 a[224:227],v[152:155],v[176:179],a[224:227]\n"
"ds_read_b64_tr_b16 v[232:233],v100 offset:32768\n"
"v_mfma_f32_16x16x32_bf16 a[228:231],v[156:159],v[176:179],a[228:231]\n"
"ds_read_b64_tr_b16 v[234:235],v100 offset:33792\n"
"v_mfma_f32_16x16x32_bf16 a[236:239],v[156:159],v[180:183],a[236:239]\n"
"ds_read_b64_tr_b16 v[236:237],v101 offset:32768\n"
"v_mfma_f32_16x16x32_bf16 a[232:235],v[152:155],v[180:183],a[232:235]\n"
"ds_read_b64_tr_b16 v[238:239],v101 offset:33792\n"
"s_waitcnt lgkmcnt(10)\n"
"v_mfma_f32_16x16x32_bf16 a[240:243],v[152:155],v[184:187],a[240:243]\n"
"ds_read_b64_tr_b16 v[240:241],v102 offset:32768\n"
"v_mfma_f32_16x16x32_bf16 a[244:247],v[156:159],v[184:187],a[244:247]\n"
"ds_read_b64_tr_b16 v[242:243],v102 offset:33792\n"
"v_mfma_f32_16x16x32_bf16 a[252:255],v[156:159],v[188:191],a[252:255]\n"
"ds_read_b64_tr_b16 v[244:245],v103 offset:32768\n"
"v_mfma_f32_16x16x32_bf16 a[248:251],v[152:155],v[188:191],a[248:251]\n"
"ds_read_b64_tr_b16 v[246:247],v103 offset:33792\n"
"s_waitcnt lgkmcnt(8)\n"
"v_mfma_f32_16x16x32_bf16 a[0:3],v[128:131],v[160:163],a[0:3]\n"
"ds_read_b64_tr_b16 v[248:249],v104 offset:32768\n"
"v_mfma_f32_16x16x32_bf16 a[0:3],v[192:195],v[224:227],a[0:3]\n"
"ds_read_b64_tr_b16 v[250:251],v104 offset:33792\n"
"v_mfma_f32_16x16x32_bf16 a[8:11],v[192:195],v[228:231],a[8:11]\n"
"ds_read_b64_tr_b16 v[252:253],v105 offset:32768\n"
"v_mfma_f32_16x16x32_bf16 a[8:11],v[128:131],v[164:167],a[8:11]\n"
"ds_read_b64_tr_b16 v[254:255],v105 offset:33792\n"
"s_waitcnt lgkmcnt(8)\n"
"v_mfma_f32_16x16x32_bf16 a[16:19],v[128:131],v[168:171],a[16:19]\n"
"ds_read_b128 v[132:135],v80 offset:2048\n"
"v_mfma_f32_16x16x32_bf16 a[16:19],v[192:195],v[232:235],a[16:19]\n"
"ds_read_b128 v[196:199],v81 offset:2048\n"
"v_mfma_f32_16x16x32_bf16 a[24:27],v[192:195],v[236:239],a[24:27]\n"
"ds_read_b128 v[136:139],v80 offset:4096\n"
"v_mfma_f32_16x16x32_bf16 a[24:27],v[128:131],v[172:175],a[24:27]\n"
"ds_read_b128 v[200:203],v81 offset:4096\n"
"s_waitcnt lgkmcnt(8)\n"
"v_mfma_f32_16x16x32_bf16 a[32:35],v[128:131],v[176:179],a[32:35]\n"
"ds_read_b128 v[140:143],v80 offset:6144\n"
"v_mfma_f32_16x16x32_bf16 a[32:35],v[192:195],v[240:243],a[32:35]\n"
"ds_read_b128 v[204:207],v81 offset:6144\n"
"v_mfma_f32_16x16x32_bf16 a[40:43],v[192:195],v[244:247],a[40:43]\n"
"ds_read_b128 v[144:147],v80 offset:8192\n"
"v_mfma_f32_16x16x32_bf16 a[40:43],v[128:131],v[180:183],a[40:43]\n"
"ds_read_b128 v[208:211],v81 offset:8192\n"
"s_waitcnt lgkmcnt(8)\n"
"v_mfma_f32_16x16x32_bf16 a[48:51],v[128:131],v[184:187],a[48:51]\n"
"ds_read_b128 v[148:151],v80 offset:10240\n"
"v_mfma_f32_16x16x32_bf16 a[48:51],v[192:195],v[248:251],a[48:51]\n"
"ds_read_b128 v[212:215],v81 offset:10240\n"
"v_mfma_f32_16x16x32_bf16 a[56:59],v[192:195],v[252:255],a[56:59]\n"
"ds_read_b128 v[216:219],v81 offset:12288\n"
"v_mfma_f32_16x16x32_bf16 a[56:59],v[128:131],v[188:191],a[56:59]\n"
"ds_read_b128 v[220:223],v81 offset:14336\n"
"s_waitcnt lgkmcnt(10)\n"
"v_mfma_f32_16x16x32_bf16 a[4:7],v[132:135],v[160:163],a[4:7]\n"
"v_mfma_f32_16x16x32_bf16 a[4:7],v[196:199],v[224:227],a[4:7]\n"
"v_mfma_f32_16x16x32_bf16 a[12:15],v[196:199],v[228:231],a[12:15]\n"
"v_mfma_f32_16x16x32_bf16 a[12:15],v[132:135],v[164:167],a[12:15]\n"
"v_mfma_f32_16x16x32_bf16 a[20:23],v[132:135],v[168:171],a[20:23]\n"
"s_waitcnt lgkmcnt(0)\n"
"s_barrier\n"
"v_mfma_f32_16x16x32_bf16 a[20:23],v[196:199],v[232:235],a[20:23]\n"
"s_add_u32 m0,s85,147456\n"
"v_mfma_f32_16x16x32_bf16 a[28:31],v[196:199],v[236:239],a[28:31]\n"
"buffer_load_dwordx4 v110,s[28:31],s86 offen lds\n"
"v_mfma_f32_16x16x32_bf16 a[28:31],v[132:135],v[172:175],a[28:31]\n"
"v_mfma_f32_16x16x32_bf16 a[36:39],v[132:135],v[176:179],a[36:39]\n"
"s_load_dwordx2 s[48:49],s[0:1],56\n"
"v_mfma_f32_16x16x32_bf16 a[36:39],v[196:199],v[240:243],a[36:39]\n"
"s_add_u32 m0,s85,148480\n"
"v_mfma_f32_16x16x32_bf16 a[44:47],v[196:199],v[244:247],a[44:47]\n"
"buffer_load_dwordx4 v111,s[28:31],s86 offen lds\n"
"v_mfma_f32_16x16x32_bf16 a[44:47],v[132:135],v[180:183],a[44:47]\n"
"s_xor_b32 s86,s86,262144\n"
"v_mfma_f32_16x16x32_bf16 a[52:55],v[132:135],v[184:187],a[52:55]\n"
"v_mfma_f32_16x16x32_bf16 a[52:55],v[196:199],v[248:251],a[52:55]\n"
"s_add_u32 m0,s85,155648\n"
"v_mfma_f32_16x16x32_bf16 a[60:63],v[196:199],v[252:255],a[60:63]\n"
"buffer_load_dwordx4 v110,s[28:31],s86 offen lds\n"
"v_mfma_f32_16x16x32_bf16 a[60:63],v[132:135],v[188:191],a[60:63]\n"
"v_mfma_f32_16x16x32_bf16 a[64:67],v[136:139],v[160:163],a[64:67]\n"
"v_mfma_f32_16x16x32_bf16 a[64:67],v[200:203],v[224:227],a[64:67]\n"
"s_add_u32 m0,s85,156672\n"
"v_mfma_f32_16x16x32_bf16 a[72:75],v[200:203],v[228:231],a[72:75]\n"
"buffer_load_dwordx4 v111,s[28:31],s86 offen lds\n"
"v_mfma_f32_16x16x32_bf16 a[72:75],v[136:139],v[164:167],a[72:75]\n"
"s_xor_b32 s86,s86,786432\n"
"v_mfma_f32_16x16x32_bf16 a[80:83],v[136:139],v[168:171],a[80:83]\n"
"v_mfma_f32_16x16x32_bf16 a[80:83],v[200:203],v[232:235],a[80:83]\n"
"s_add_u32 m0,s85,131072\n"
"v_mfma_f32_16x16x32_bf16 a[88:91],v[200:203],v[236:239],a[88:91]\n"
"buffer_load_dwordx4 v110,s[28:31],s86 offen lds\n"
"v_mfma_f32_16x16x32_bf16 a[88:91],v[136:139],v[172:175],a[88:91]\n"
"v_mfma_f32_16x16x32_bf16 a[96:99],v[136:139],v[176:179],a[96:99]\n"
"v_mfma_f32_16x16x32_bf16 a[96:99],v[200:203],v[240:243],a[96:99]\n"
"s_add_u32 m0,s85,132096\n"
"v_mfma_f32_16x16x32_bf16 a[104:107],v[200:203],v[244:247],a[104:107]\n"
"buffer_load_dwordx4 v111,s[28:31],s86 offen lds\n"
"v_mfma_f32_16x16x32_bf16 a[104:107],v[136:139],v[180:183],a[104:107]\n"
"s_xor_b32 s86,s86,262144\n"
"v_mfma_f32_16x16x32_bf16 a[112:115],v[136:139],v[184:187],a[112:115]\n"
"v_mfma_f32_16x16x32_bf16 a[112:115],v[200:203],v[248:251],a[112:115]\n"
"v_mfma_f32_16x16x32_bf16 a[120:123],v[200:203],v[252:255],a[120:123]\n"
"v_mfma_f32_16x16x32_bf16 a[120:123],v[136:139],v[188:191],a[120:123]\n"
"v_mfma_f32_16x16x32_bf16 a[68:71],v[140:143],v[160:163],a[68:71]\n"
"v_mfma_f32_16x16x32_bf16 a[68:71],v[204:207],v[224:227],a[68:71]\n"
"s_add_u32 m0,s85,139264\n"
"v_mfma_f32_16x16x32_bf16 a[76:79],v[204:207],v[228:231],a[76:79]\n"
"buffer_load_dwordx4 v110,s[28:31],s86 offen lds\n"
"v_mfma_f32_16x16x32_bf16 a[76:79],v[140:143],v[164:167],a[76:79]\n"
"v_mfma_f32_16x16x32_bf16 a[84:87],v[140:143],v[168:171],a[84:87]\n"
"v_mfma_f32_16x16x32_bf16 a[84:87],v[204:207],v[232:235],a[84:87]\n"
"s_add_u32 m0,s85,140288\n"
"v_mfma_f32_16x16x32_bf16 a[92:95],v[204:207],v[236:239],a[92:95]\n"
"buffer_load_dwordx4 v111,s[28:31],s86 offen lds\n"
"v_mfma_f32_16x16x32_bf16 a[92:95],v[140:143],v[172:175],a[92:95]\n"
"s_xor_b32 s86,s86,1835008\n"
"v_mfma_f32_16x16x32_bf16 a[100:103],v[140:143],v[176:179],a[100:103]\n"
"s_waitcnt lgkmcnt(0)\n"
"s_lshl2_add_u32 s88,s100,s52\n"
"v_mfma_f32_16x16x32_bf16 a[100:103],v[204:207],v[240:243],a[100:103]\n"
"s_mul_i32 s88,s88,7168\n"
"s_add_u32 m0,s78,0\n"
"v_mfma_f32_16x16x32_bf16 a[108:111],v[204:207],v[244:247],a[108:111]\n"
"buffer_load_dwordx4 v112,s[24:27],s76 offen offset:0 sc1 lds\n"
"v_mfma_f32_16x16x32_bf16 a[108:111],v[140:143],v[180:183],a[108:111]\n"
"s_add_u32 s88,s88,3584\n"
"s_add_u32 s48,s48,s88\n"
"s_addc_u32 s49,s49,0\n"
"v_mfma_f32_16x16x32_bf16 a[116:119],v[140:143],v[184:187],a[116:119]\n"
"s_mov_b32 s50,3584\n"
"s_mov_b32 s51,0x20000\n"
"v_mfma_f32_16x16x32_bf16 a[116:119],v[204:207],v[248:251],a[116:119]\n"
"v_lshlrev_b32 v8,3,v112\n"
"v_mfma_f32_16x16x32_bf16 a[124:127],v[204:207],v[252:255],a[124:127]\n"
"buffer_load_dwordx4 v112,s[24:27],s76 offen offset:1024 sc1 lds\n"
"v_mfma_f32_16x16x32_bf16 a[124:127],v[140:143],v[188:191],a[124:127]\n"
"v_mfma_f32_16x16x32_bf16 a[128:131],v[144:147],v[160:163],a[128:131]\n"
"v_mfma_f32_16x16x32_bf16 a[128:131],v[208:211],v[224:227],a[128:131]\n"
"v_mfma_f32_16x16x32_bf16 a[136:139],v[208:211],v[228:231],a[136:139]\n"
"buffer_load_dwordx4 v112,s[24:27],s76 offen offset:2048 sc1 lds\n"
"v_mfma_f32_16x16x32_bf16 a[136:139],v[144:147],v[164:167],a[136:139]\n"
"v_mfma_f32_16x16x32_bf16 a[144:147],v[144:147],v[168:171],a[144:147]\n"
"v_mfma_f32_16x16x32_bf16 a[144:147],v[208:211],v[232:235],a[144:147]\n"
"buffer_load_dwordx4 v112,s[24:27],s76 offen offset:3072 sc1 lds\n"
"v_mfma_f32_16x16x32_bf16 a[152:155],v[208:211],v[236:239],a[152:155]\n"
"s_xor_b32 s76,s76,16384\n"
"v_mfma_f32_16x16x32_bf16 a[152:155],v[144:147],v[172:175],a[152:155]\n"
"v_mfma_f32_16x16x32_bf16 a[160:163],v[144:147],v[176:179],a[160:163]\n"
"v_mfma_f32_16x16x32_bf16 a[160:163],v[208:211],v[240:243],a[160:163]\n"
"s_add_u32 m0,s78,16384\n"
"v_mfma_f32_16x16x32_bf16 a[168:171],v[208:211],v[244:247],a[168:171]\n"
"buffer_load_dwordx4 v112,s[24:27],s76 offen offset:0 sc1 lds\n"
"v_mfma_f32_16x16x32_bf16 a[168:171],v[144:147],v[180:183],a[168:171]\n"
"v_mfma_f32_16x16x32_bf16 a[176:179],v[144:147],v[184:187],a[176:179]\n"
"v_mfma_f32_16x16x32_bf16 a[176:179],v[208:211],v[248:251],a[176:179]\n"
"v_mfma_f32_16x16x32_bf16 a[184:187],v[208:211],v[252:255],a[184:187]\n"
"buffer_load_dwordx4 v112,s[24:27],s76 offen offset:1024 sc1 lds\n"
"v_mfma_f32_16x16x32_bf16 a[184:187],v[144:147],v[188:191],a[184:187]\n"
"v_mfma_f32_16x16x32_bf16 a[132:135],v[148:151],v[160:163],a[132:135]\n"
"v_mfma_f32_16x16x32_bf16 a[132:135],v[212:215],v[224:227],a[132:135]\n"
"v_mfma_f32_16x16x32_bf16 a[140:143],v[212:215],v[228:231],a[140:143]\n"
"buffer_load_dwordx4 v112,s[24:27],s76 offen offset:2048 sc1 lds\n"
"v_mfma_f32_16x16x32_bf16 a[140:143],v[148:151],v[164:167],a[140:143]\n"
"v_mfma_f32_16x16x32_bf16 a[148:151],v[148:151],v[168:171],a[148:151]\n"
"v_mfma_f32_16x16x32_bf16 a[148:151],v[212:215],v[232:235],a[148:151]\n"
"buffer_load_dwordx4 v112,s[24:27],s76 offen offset:3072 sc1 lds\n"
"v_mfma_f32_16x16x32_bf16 a[156:159],v[212:215],v[236:239],a[156:159]\n"
"s_xor_b32 s76,s76,114688\n"
"v_mfma_f32_16x16x32_bf16 a[156:159],v[148:151],v[172:175],a[156:159]\n"
"v_mfma_f32_16x16x32_bf16 a[164:167],v[148:151],v[176:179],a[164:167]\n"
"buffer_load_dword v115,v8,s[48:51],0 offen\n"
"v_mfma_f32_16x16x32_bf16 a[164:167],v[212:215],v[240:243],a[164:167]\n"
"v_mfma_f32_16x16x32_bf16 a[172:175],v[212:215],v[244:247],a[172:175]\n"
"v_mfma_f32_16x16x32_bf16 a[172:175],v[148:151],v[180:183],a[172:175]\n"
"v_mfma_f32_16x16x32_bf16 a[180:183],v[148:151],v[184:187],a[180:183]\n"
"v_mfma_f32_16x16x32_bf16 a[180:183],v[212:215],v[248:251],a[180:183]\n"
"v_mfma_f32_16x16x32_bf16 a[188:191],v[212:215],v[252:255],a[188:191]\n"
"v_mfma_f32_16x16x32_bf16 a[188:191],v[148:151],v[188:191],a[188:191]\n"
"s_waitcnt vmcnt(26)\n"
"s_barrier\n"
"v_mfma_f32_16x16x32_bf16 a[192:195],v[216:219],v[224:227],a[192:195]\n"
"ds_read_b128 v[152:155],v80 offset:45056\n"
"v_mfma_f32_16x16x32_bf16 a[196:199],v[220:223],v[224:227],a[196:199]\n"
"ds_read_b128 v[156:159],v80 offset:47104\n"
"v_mfma_f32_16x16x32_bf16 a[204:207],v[220:223],v[228:231],a[204:207]\n"
"ds_read_b64_tr_b16 v[160:161],v98 offset:0\n"
"v_mfma_f32_16x16x32_bf16 a[200:203],v[216:219],v[228:231],a[200:203]\n"
"ds_read_b64_tr_b16 v[162:163],v98 offset:1024\n"
"v_mfma_f32_16x16x32_bf16 a[208:211],v[216:219],v[232:235],a[208:211]\n"
"ds_read_b64_tr_b16 v[164:165],v99 offset:0\n"
"v_mfma_f32_16x16x32_bf16 a[212:215],v[220:223],v[232:235],a[212:215]\n"
"ds_read_b64_tr_b16 v[166:167],v99 offset:1024\n"
"v_mfma_f32_16x16x32_bf16 a[220:223],v[220:223],v[236:239],a[220:223]\n"
"ds_read_b64_tr_b16 v[168:169],v100 offset:0\n"
"v_mfma_f32_16x16x32_bf16 a[216:219],v[216:219],v[236:239],a[216:219]\n"
"ds_read_b64_tr_b16 v[170:171],v100 offset:1024\n"
"v_mfma_f32_16x16x32_bf16 a[224:227],v[216:219],v[240:243],a[224:227]\n"
"ds_read_b64_tr_b16 v[172:173],v101 offset:0\n"
"v_mfma_f32_16x16x32_bf16 a[228:231],v[220:223],v[240:243],a[228:231]\n"
"ds_read_b64_tr_b16 v[174:175],v101 offset:1024\n"
"v_mfma_f32_16x16x32_bf16 a[236:239],v[220:223],v[244:247],a[236:239]\n"
"ds_read_b64_tr_b16 v[176:177],v102 offset:0\n"
"v_mfma_f32_16x16x32_bf16 a[232:235],v[216:219],v[244:247],a[232:235]\n"
"ds_read_b64_tr_b16 v[178:179],v102 offset:1024\n"
"v_mfma_f32_16x16x32_bf16 a[240:243],v[216:219],v[248:251],a[240:243]\n"
"ds_read_b64_tr_b16 v[180:181],v103 offset:0\n"
"v_mfma_f32_16x16x32_bf16 a[244:247],v[220:223],v[248:251],a[244:247]\n"
"ds_read_b64_tr_b16 v[182:183],v103 offset:1024\n"
"v_mfma_f32_16x16x32_bf16 a[252:255],v[220:223],v[252:255],a[252:255]\n"
"ds_read_b64_tr_b16 v[184:185],v104 offset:0\n"
"v_mfma_f32_16x16x32_bf16 a[248:251],v[216:219],v[252:255],a[248:251]\n"
"ds_read_b64_tr_b16 v[186:187],v104 offset:1024\n"
"s_waitcnt lgkmcnt(10)\n"
"v_mfma_f32_16x16x32_bf16 a[192:195],v[152:155],v[160:163],a[192:195]\n"
"ds_read_b64_tr_b16 v[188:189],v105 offset:0\n"
"v_mfma_f32_16x16x32_bf16 a[196:199],v[156:159],v[160:163],a[196:199]\n"
"ds_read_b64_tr_b16 v[190:191],v105 offset:1024\n"
"v_mfma_f32_16x16x32_bf16 a[204:207],v[156:159],v[164:167],a[204:207]\n"
"ds_read_b128 v[128:131],v80 offset:32768\n"
"v_mfma_f32_16x16x32_bf16 a[200:203],v[152:155],v[164:167],a[200:203]\n"
"ds_read_b128 v[192:195],v81 offset:32768\n"
"s_waitcnt lgkmcnt(10)\n"
"v_mfma_f32_16x16x32_bf16 a[208:211],v[152:155],v[168:171],a[208:211]\n"
"ds_read_b64_tr_b16 v[224:225],v98 offset:16384\n"
"v_mfma_f32_16x16x32_bf16 a[212:215],v[156:159],v[168:171],a[212:215]\n"
"ds_read_b64_tr_b16 v[226:227],v98 offset:17408\n"
"v_mfma_f32_16x16x32_bf16 a[220:223],v[156:159],v[172:175],a[220:223]\n"
"ds_read_b64_tr_b16 v[228:229],v99 offset:16384\n"
"v_mfma_f32_16x16x32_bf16 a[216:219],v[152:155],v[172:175],a[216:219]\n"
"ds_read_b64_tr_b16 v[230:231],v99 offset:17408\n"
"s_waitcnt lgkmcnt(10)\n"
"v_mfma_f32_16x16x32_bf16 a[224:227],v[152:155],v[176:179],a[224:227]\n"
"ds_read_b64_tr_b16 v[232:233],v100 offset:16384\n"
"v_mfma_f32_16x16x32_bf16 a[228:231],v[156:159],v[176:179],a[228:231]\n"
"ds_read_b64_tr_b16 v[234:235],v100 offset:17408\n"
"v_mfma_f32_16x16x32_bf16 a[236:239],v[156:159],v[180:183],a[236:239]\n"
"ds_read_b64_tr_b16 v[236:237],v101 offset:16384\n"
"v_mfma_f32_16x16x32_bf16 a[232:235],v[152:155],v[180:183],a[232:235]\n"
"ds_read_b64_tr_b16 v[238:239],v101 offset:17408\n"
"s_waitcnt lgkmcnt(10)\n"
"v_mfma_f32_16x16x32_bf16 a[240:243],v[152:155],v[184:187],a[240:243]\n"
"ds_read_b64_tr_b16 v[240:241],v102 offset:16384\n"
"v_mfma_f32_16x16x32_bf16 a[244:247],v[156:159],v[184:187],a[244:247]\n"
"ds_read_b64_tr_b16 v[242:243],v102 offset:17408\n"
"v_mfma_f32_16x16x32_bf16 a[252:255],v[156:159],v[188:191],a[252:255]\n"
"ds_read_b64_tr_b16 v[244:245],v103 offset:16384\n"
"v_mfma_f32_16x16x32_bf16 a[248:251],v[152:155],v[188:191],a[248:251]\n"
"ds_read_b64_tr_b16 v[246:247],v103 offset:17408\n"
"s_waitcnt lgkmcnt(8)\n"
"v_mfma_f32_16x16x32_bf16 a[0:3],v[128:131],v[160:163],a[0:3]\n"
"ds_read_b64_tr_b16 v[248:249],v104 offset:16384\n"
"v_mfma_f32_16x16x32_bf16 a[0:3],v[192:195],v[224:227],a[0:3]\n"
"ds_read_b64_tr_b16 v[250:251],v104 offset:17408\n"
"v_mfma_f32_16x16x32_bf16 a[8:11],v[192:195],v[228:231],a[8:11]\n"
"ds_read_b64_tr_b16 v[252:253],v105 offset:16384\n"
"v_mfma_f32_16x16x32_bf16 a[8:11],v[128:131],v[164:167],a[8:11]\n"
"ds_read_b64_tr_b16 v[254:255],v105 offset:17408\n"
"s_waitcnt lgkmcnt(8)\n"
"v_mfma_f32_16x16x32_bf16 a[16:19],v[128:131],v[168:171],a[16:19]\n"
"ds_read_b128 v[132:135],v80 offset:34816\n"
"v_mfma_f32_16x16x32_bf16 a[16:19],v[192:195],v[232:235],a[16:19]\n"
"ds_read_b128 v[196:199],v81 offset:34816\n"
"v_mfma_f32_16x16x32_bf16 a[24:27],v[192:195],v[236:239],a[24:27]\n"
"ds_read_b128 v[136:139],v80 offset:36864\n"
"v_mfma_f32_16x16x32_bf16 a[24:27],v[128:131],v[172:175],a[24:27]\n"
"ds_read_b128 v[200:203],v81 offset:36864\n"
"s_waitcnt lgkmcnt(8)\n"
"v_mfma_f32_16x16x32_bf16 a[32:35],v[128:131],v[176:179],a[32:35]\n"
"s_and_b32 s82,s52,1\n"
"ds_read_b128 v[140:143],v80 offset:38912\n"
"s_lshl_b32 s82,s82,13\n"
"v_mfma_f32_16x16x32_bf16 a[32:35],v[192:195],v[240:243],a[32:35]\n"
"ds_read_b128 v[204:207],v81 offset:38912\n"
"v_mfma_f32_16x16x32_bf16 a[40:43],v[192:195],v[244:247],a[40:43]\n"
"ds_read_b128 v[144:147],v80 offset:40960\n"
"v_mfma_f32_16x16x32_bf16 a[40:43],v[128:131],v[180:183],a[40:43]\n"
"ds_read_b128 v[208:211],v81 offset:40960\n"
"s_waitcnt lgkmcnt(8)\n"
"v_mfma_f32_16x16x32_bf16 a[48:51],v[128:131],v[184:187],a[48:51]\n"
"ds_read_b128 v[148:151],v80 offset:43008\n"
"v_mfma_f32_16x16x32_bf16 a[48:51],v[192:195],v[248:251],a[48:51]\n"
"ds_read_b128 v[212:215],v81 offset:43008\n"
"v_subrev_u32 v98,s82,v98\n"
"v_mfma_f32_16x16x32_bf16 a[56:59],v[192:195],v[252:255],a[56:59]\n"
"ds_read_b128 v[216:219],v81 offset:45056\n"
"v_add_u32 v96,s78,v97\n"
"v_mfma_f32_16x16x32_bf16 a[56:59],v[128:131],v[188:191],a[56:59]\n"
"ds_read_b128 v[220:223],v81 offset:47104\n"
"s_waitcnt lgkmcnt(10)\n"
"v_mfma_f32_16x16x32_bf16 a[4:7],v[132:135],v[160:163],a[4:7]\n"
"v_add_u32 v97,65536,v96\n"
"v_mfma_f32_16x16x32_bf16 a[4:7],v[196:199],v[224:227],a[4:7]\n"
"v_subrev_u32 v99,s82,v99\n"
"v_mfma_f32_16x16x32_bf16 a[12:15],v[196:199],v[228:231],a[12:15]\n"
"v_mfma_f32_16x16x32_bf16 a[12:15],v[132:135],v[164:167],a[12:15]\n"
"v_mfma_f32_16x16x32_bf16 a[20:23],v[132:135],v[168:171],a[20:23]\n"
"s_waitcnt lgkmcnt(0)\n"
"s_barrier\n"
"v_mfma_f32_16x16x32_bf16 a[20:23],v[196:199],v[232:235],a[20:23]\n"
"v_subrev_u32 v100,s82,v100\n"
"s_add_u32 m0,s85,98304\n"
"v_mfma_f32_16x16x32_bf16 a[28:31],v[196:199],v[236:239],a[28:31]\n"
"buffer_load_dwordx4 v110,s[28:31],s86 offen lds\n"
"v_mfma_f32_16x16x32_bf16 a[28:31],v[132:135],v[172:175],a[28:31]\n"
"v_mfma_f32_16x16x32_bf16 a[36:39],v[132:135],v[176:179],a[36:39]\n"
"v_mfma_f32_16x16x32_bf16 a[36:39],v[196:199],v[240:243],a[36:39]\n"
"v_subrev_u32 v101,s82,v101\n"
"s_add_u32 m0,s85,99328\n"
"v_mfma_f32_16x16x32_bf16 a[44:47],v[196:199],v[244:247],a[44:47]\n"
"buffer_load_dwordx4 v111,s[28:31],s86 offen lds\n"
"v_mfma_f32_16x16x32_bf16 a[44:47],v[132:135],v[180:183],a[44:47]\n"
"s_xor_b32 s86,s86,262144\n"
"v_mfma_f32_16x16x32_bf16 a[52:55],v[132:135],v[184:187],a[52:55]\n"
"v_mfma_f32_16x16x32_bf16 a[52:55],v[196:199],v[248:251],a[52:55]\n"
"v_subrev_u32 v102,s82,v102\n"
"s_add_u32 m0,s78,32768\n"
"v_mfma_f32_16x16x32_bf16 a[60:63],v[196:199],v[252:255],a[60:63]\n"
"buffer_load_dwordx4 v112,s[24:27],s76 offen offset:0 sc1 lds\n"
"v_mfma_f32_16x16x32_bf16 a[60:63],v[132:135],v[188:191],a[60:63]\n"
"v_mfma_f32_16x16x32_bf16 a[64:67],v[136:139],v[160:163],a[64:67]\n"
"v_mfma_f32_16x16x32_bf16 a[64:67],v[200:203],v[224:227],a[64:67]\n"
"v_subrev_u32 v103,s82,v103\n"
"v_mfma_f32_16x16x32_bf16 a[72:75],v[200:203],v[228:231],a[72:75]\n"
"buffer_load_dwordx4 v112,s[24:27],s76 offen offset:1024 sc1 lds\n"
"v_mfma_f32_16x16x32_bf16 a[72:75],v[136:139],v[164:167],a[72:75]\n"
"v_mfma_f32_16x16x32_bf16 a[80:83],v[136:139],v[168:171],a[80:83]\n"
"v_accvgpr_read_b32 v116,a0\n"
"v_accvgpr_read_b32 v117,a1\n"
"v_mfma_f32_16x16x32_bf16 a[80:83],v[200:203],v[232:235],a[80:83]\n"
"v_subrev_u32 v104,s82,v104\n"
"v_accvgpr_read_b32 v118,a2\n"
"v_mfma_f32_16x16x32_bf16 a[88:91],v[200:203],v[236:239],a[88:91]\n"
"buffer_load_dwordx4 v112,s[24:27],s76 offen offset:2048 sc1 lds\n"
"v_mfma_f32_16x16x32_bf16 a[88:91],v[136:139],v[172:175],a[88:91]\n"
"v_accvgpr_read_b32 v119,a3\n"
"v_accvgpr_read_b32 v120,a4\n"
"v_mfma_f32_16x16x32_bf16 a[96:99],v[136:139],v[176:179],a[96:99]\n"
"v_accvgpr_read_b32 v121,a5\n"
"v_accvgpr_read_b32 v122,a6\n"
"v_mfma_f32_16x16x32_bf16 a[96:99],v[200:203],v[240:243],a[96:99]\n"
"v_subrev_u32 v105,s82,v105\n"
"v_accvgpr_read_b32 v123,a7\n"
"v_mfma_f32_16x16x32_bf16 a[104:107],v[200:203],v[244:247],a[104:107]\n"
"buffer_load_dwordx4 v112,s[24:27],s76 offen offset:3072 sc1 lds\n"
"v_mfma_f32_16x16x32_bf16 a[104:107],v[136:139],v[180:183],a[104:107]\n"
"s_xor_b32 s76,s76,16384\n"
"v_cvt_pk_bf16_f32 v116,v116,v117\n"
"v_cvt_pk_bf16_f32 v117,v118,v119\n"
"v_mfma_f32_16x16x32_bf16 a[112:115],v[136:139],v[184:187],a[112:115]\n"
"v_cvt_pk_bf16_f32 v118,v120,v121\n"
"v_cvt_pk_bf16_f32 v119,v122,v123\n"
"v_mfma_f32_16x16x32_bf16 a[112:115],v[200:203],v[248:251],a[112:115]\n"
"buffer_store_dwordx4 v[116:119],v112,s[24:27],s56 offen offset:0 sc1\n"
"v_mfma_f32_16x16x32_bf16 a[120:123],v[200:203],v[252:255],a[120:123]\n"
"s_nop 0\n"
"v_accvgpr_read_b32 v116,a8\n"
"v_accvgpr_read_b32 v117,a9\n"
"v_accvgpr_read_b32 v118,a10\n"
"v_mfma_f32_16x16x32_bf16 a[120:123],v[136:139],v[188:191],a[120:123]\n"
"s_waitcnt vmcnt(20)\n"
"v_accvgpr_read_b32 v119,a11\n"
"v_accvgpr_read_b32 v120,a12\n"
"s_barrier\n"
"v_mfma_f32_16x16x32_bf16 a[68:71],v[140:143],v[160:163],a[68:71]\n"
"ds_read_b128 v[0:3],v97 offset:0\n"
"v_accvgpr_read_b32 v121,a13\n"
"v_mfma_f32_16x16x32_bf16 a[68:71],v[204:207],v[224:227],a[68:71]\n"
"ds_read_b128 v[8:11],v97 offset:1024\n"
"v_accvgpr_read_b32 v122,a14\n"
"v_mfma_f32_16x16x32_bf16 a[76:79],v[204:207],v[228:231],a[76:79]\n"
"ds_read_b128 v[16:19],v97 offset:2048\n"
"v_accvgpr_read_b32 v123,a15\n"
"v_mfma_f32_16x16x32_bf16 a[76:79],v[140:143],v[164:167],a[76:79]\n"
"ds_read_b128 v[24:27],v97 offset:3072\n"
"v_cvt_pk_bf16_f32 v116,v116,v117\n"
"v_mfma_f32_16x16x32_bf16 a[84:87],v[140:143],v[168:171],a[84:87]\n"
"ds_read_b128 v[4:7],v97 offset:16384\n"
"v_cvt_pk_bf16_f32 v117,v118,v119\n"
"v_mfma_f32_16x16x32_bf16 a[84:87],v[204:207],v[232:235],a[84:87]\n"
"ds_read_b128 v[12:15],v97 offset:17408\n"
"v_cvt_pk_bf16_f32 v118,v120,v121\n"
"v_mfma_f32_16x16x32_bf16 a[92:95],v[204:207],v[236:239],a[92:95]\n"
"ds_read_b128 v[20:23],v97 offset:18432\n"
"v_cvt_pk_bf16_f32 v119,v122,v123\n"
"v_mfma_f32_16x16x32_bf16 a[92:95],v[140:143],v[172:175],a[92:95]\n"
"ds_read_b128 v[28:31],v97 offset:19456\n"
"buffer_store_dwordx4 v[116:119],v112,s[24:27],s56 offen offset:1024 sc1\n"
"v_mfma_f32_16x16x32_bf16 a[100:103],v[140:143],v[176:179],a[100:103]\n"
"ds_read_b64_tr_b16 v[32:33],v98 offset:49152\n"
"v_accvgpr_read_b32 v116,a16\n"
"v_mfma_f32_16x16x32_bf16 a[100:103],v[204:207],v[240:243],a[100:103]\n"
"ds_read_b64_tr_b16 v[34:35],v98 offset:50176\n"
"v_accvgpr_read_b32 v117,a17\n"
"v_mfma_f32_16x16x32_bf16 a[108:111],v[204:207],v[244:247],a[108:111]\n"
"ds_read_b64_tr_b16 v[36:37],v98 offset:57344\n"
"v_accvgpr_read_b32 v118,a18\n"
"v_mfma_f32_16x16x32_bf16 a[108:111],v[140:143],v[180:183],a[108:111]\n"
"ds_read_b64_tr_b16 v[38:39],v98 offset:58368\n"
"v_accvgpr_read_b32 v119,a19\n"
"v_mfma_f32_16x16x32_bf16 a[116:119],v[140:143],v[184:187],a[116:119]\n"
"ds_read_b64_tr_b16 v[40:41],v99 offset:49152\n"
"v_accvgpr_read_b32 v120,a20\n"
"v_mfma_f32_16x16x32_bf16 a[116:119],v[204:207],v[248:251],a[116:119]\n"
"ds_read_b64_tr_b16 v[42:43],v99 offset:50176\n"
"v_accvgpr_read_b32 v121,a21\n"
"v_mfma_f32_16x16x32_bf16 a[124:127],v[204:207],v[252:255],a[124:127]\n"
"ds_read_b64_tr_b16 v[44:45],v99 offset:57344\n"
"v_accvgpr_read_b32 v122,a22\n"
"v_mfma_f32_16x16x32_bf16 a[124:127],v[140:143],v[188:191],a[124:127]\n"
"ds_read_b64_tr_b16 v[46:47],v99 offset:58368\n"
"v_accvgpr_read_b32 v123,a23\n"
"v_mfma_f32_16x16x32_bf16 a[128:131],v[144:147],v[160:163],a[128:131]\n"
"ds_read_b64_tr_b16 v[48:49],v100 offset:49152\n"
"v_cvt_pk_bf16_f32 v116,v116,v117\n"
"v_mfma_f32_16x16x32_bf16 a[128:131],v[208:211],v[224:227],a[128:131]\n"
"ds_read_b64_tr_b16 v[50:51],v100 offset:50176\n"
"v_cvt_pk_bf16_f32 v117,v118,v119\n"
"v_mfma_f32_16x16x32_bf16 a[136:139],v[208:211],v[228:231],a[136:139]\n"
"ds_read_b64_tr_b16 v[52:53],v100 offset:57344\n"
"v_cvt_pk_bf16_f32 v118,v120,v121\n"
"v_mfma_f32_16x16x32_bf16 a[136:139],v[144:147],v[164:167],a[136:139]\n"
"ds_read_b64_tr_b16 v[54:55],v100 offset:58368\n"
"v_cvt_pk_bf16_f32 v119,v122,v123\n"
"v_mfma_f32_16x16x32_bf16 a[144:147],v[144:147],v[168:171],a[144:147]\n"
"ds_read_b64_tr_b16 v[56:57],v101 offset:49152\n"
"buffer_store_dwordx4 v[116:119],v112,s[24:27],s56 offen offset:2048 sc1\n"
"v_mfma_f32_16x16x32_bf16 a[144:147],v[208:211],v[232:235],a[144:147]\n"
"ds_read_b64_tr_b16 v[58:59],v101 offset:50176\n"
"v_accvgpr_read_b32 v116,a24\n"
"v_mfma_f32_16x16x32_bf16 a[152:155],v[208:211],v[236:239],a[152:155]\n"
"ds_read_b64_tr_b16 v[60:61],v101 offset:57344\n"
"v_accvgpr_read_b32 v117,a25\n"
"v_mfma_f32_16x16x32_bf16 a[152:155],v[144:147],v[172:175],a[152:155]\n"
"ds_read_b64_tr_b16 v[62:63],v101 offset:58368\n"
"v_accvgpr_read_b32 v118,a26\n"
"v_mfma_f32_16x16x32_bf16 a[160:163],v[144:147],v[176:179],a[160:163]\n"
"ds_read_b64_tr_b16 v[64:65],v102 offset:49152\n"
"v_accvgpr_read_b32 v119,a27\n"
"v_mfma_f32_16x16x32_bf16 a[160:163],v[208:211],v[240:243],a[160:163]\n"
"ds_read_b64_tr_b16 v[66:67],v102 offset:50176\n"
"v_accvgpr_read_b32 v120,a28\n"
"v_mfma_f32_16x16x32_bf16 a[168:171],v[208:211],v[244:247],a[168:171]\n"
"ds_read_b64_tr_b16 v[68:69],v102 offset:57344\n"
"v_accvgpr_read_b32 v121,a29\n"
"v_mfma_f32_16x16x32_bf16 a[168:171],v[144:147],v[180:183],a[168:171]\n"
"ds_read_b64_tr_b16 v[70:71],v102 offset:58368\n"
"v_accvgpr_read_b32 v122,a30\n"
"v_mfma_f32_16x16x32_bf16 a[176:179],v[144:147],v[184:187],a[176:179]\n"
"ds_read_b64_tr_b16 v[72:73],v103 offset:49152\n"
"v_accvgpr_read_b32 v123,a31\n"
"v_mfma_f32_16x16x32_bf16 a[176:179],v[208:211],v[248:251],a[176:179]\n"
"ds_read_b64_tr_b16 v[74:75],v103 offset:50176\n"
"v_cvt_pk_bf16_f32 v116,v116,v117\n"
"v_mfma_f32_16x16x32_bf16 a[184:187],v[208:211],v[252:255],a[184:187]\n"
"ds_read_b64_tr_b16 v[76:77],v103 offset:57344\n"
"v_cvt_pk_bf16_f32 v117,v118,v119\n"
"v_mfma_f32_16x16x32_bf16 a[184:187],v[144:147],v[188:191],a[184:187]\n"
"ds_read_b64_tr_b16 v[78:79],v103 offset:58368\n"
"v_cvt_pk_bf16_f32 v118,v120,v121\n"
"v_mfma_f32_16x16x32_bf16 a[132:135],v[148:151],v[160:163],a[132:135]\n"
"ds_read_b64_tr_b16 v[80:81],v104 offset:49152\n"
"v_cvt_pk_bf16_f32 v119,v122,v123\n"
"v_mfma_f32_16x16x32_bf16 a[132:135],v[212:215],v[224:227],a[132:135]\n"
"ds_read_b64_tr_b16 v[82:83],v104 offset:50176\n"
"buffer_store_dwordx4 v[116:119],v112,s[24:27],s56 offen offset:3072 sc1\n"
"v_mfma_f32_16x16x32_bf16 a[140:143],v[212:215],v[228:231],a[140:143]\n"
"ds_read_b64_tr_b16 v[84:85],v104 offset:57344\n"
"v_mfma_f32_16x16x32_bf16 a[140:143],v[148:151],v[164:167],a[140:143]\n"
"ds_read_b64_tr_b16 v[86:87],v104 offset:58368\n"
"v_mfma_f32_16x16x32_bf16 a[148:151],v[148:151],v[168:171],a[148:151]\n"
"ds_read_b64_tr_b16 v[88:89],v105 offset:49152\n"
"v_mfma_f32_16x16x32_bf16 a[148:151],v[212:215],v[232:235],a[148:151]\n"
"ds_read_b64_tr_b16 v[90:91],v105 offset:50176\n"
"v_mfma_f32_16x16x32_bf16 a[156:159],v[212:215],v[236:239],a[156:159]\n"
"ds_read_b64_tr_b16 v[92:93],v105 offset:57344\n"
"v_mfma_f32_16x16x32_bf16 a[156:159],v[148:151],v[172:175],a[156:159]\n"
"ds_read_b64_tr_b16 v[94:95],v105 offset:58368\n"
"v_mfma_f32_16x16x32_bf16 a[164:167],v[148:151],v[176:179],a[164:167]\n"
"s_add_u32 m0,s85,106496\n"
"v_mfma_f32_16x16x32_bf16 a[164:167],v[212:215],v[240:243],a[164:167]\n"
"buffer_load_dwordx4 v110,s[28:31],s86 offen lds\n"
"v_mfma_f32_16x16x32_bf16 a[172:175],v[212:215],v[244:247],a[172:175]\n"
"v_mfma_f32_16x16x32_bf16 a[172:175],v[148:151],v[180:183],a[172:175]\n"
"s_add_u32 m0,s85,107520\n"
"v_mfma_f32_16x16x32_bf16 a[180:183],v[148:151],v[184:187],a[180:183]\n"
"buffer_load_dwordx4 v111,s[28:31],s86 offen lds\n"
"v_mfma_f32_16x16x32_bf16 a[180:183],v[212:215],v[248:251],a[180:183]\n"
"s_xor_b32 s86,s86,786432\n"
"v_mfma_f32_16x16x32_bf16 a[188:191],v[212:215],v[252:255],a[188:191]\n"
"s_add_u32 m0,s78,49152\n"
"v_mfma_f32_16x16x32_bf16 a[188:191],v[148:151],v[188:191],a[188:191]\n"
"buffer_load_dwordx4 v112,s[24:27],s76 offen offset:0 sc1 lds\n"
"v_mfma_f32_16x16x32_bf16 a[192:195],v[216:219],v[224:227],a[192:195]\n"
"v_mfma_f32_16x16x32_bf16 a[196:199],v[220:223],v[224:227],a[196:199]\n"
"v_mfma_f32_16x16x32_bf16 a[204:207],v[220:223],v[228:231],a[204:207]\n"
"buffer_load_dwordx4 v112,s[24:27],s76 offen offset:1024 sc1 lds\n"
"v_mfma_f32_16x16x32_bf16 a[200:203],v[216:219],v[228:231],a[200:203]\n"
"v_mfma_f32_16x16x32_bf16 a[208:211],v[216:219],v[232:235],a[208:211]\n"
"v_mfma_f32_16x16x32_bf16 a[212:215],v[220:223],v[232:235],a[212:215]\n"
"buffer_load_dwordx4 v112,s[24:27],s76 offen offset:2048 sc1 lds\n"
"v_mfma_f32_16x16x32_bf16 a[220:223],v[220:223],v[236:239],a[220:223]\n"
"v_mfma_f32_16x16x32_bf16 a[216:219],v[216:219],v[236:239],a[216:219]\n"
"buffer_load_dwordx4 v112,s[24:27],s76 offen offset:3072 sc1 lds\n"
"v_mfma_f32_16x16x32_bf16 a[224:227],v[216:219],v[240:243],a[224:227]\n"
"s_xor_b32 s76,s76,49152\n"
"v_mfma_f32_16x16x32_bf16 a[228:231],v[220:223],v[240:243],a[228:231]\n"
"v_mfma_f32_16x16x32_bf16 a[236:239],v[220:223],v[244:247],a[236:239]\n"
"v_mfma_f32_16x16x32_bf16 a[232:235],v[216:219],v[244:247],a[232:235]\n"
"v_mfma_f32_16x16x32_bf16 a[240:243],v[216:219],v[248:251],a[240:243]\n"
"v_mfma_f32_16x16x32_bf16 a[244:247],v[220:223],v[248:251],a[244:247]\n"
"v_mfma_f32_16x16x32_bf16 a[252:255],v[220:223],v[252:255],a[252:255]\n"
"v_mfma_f32_16x16x32_bf16 a[248:251],v[216:219],v[252:255],a[248:251]\n"
".Lfk_ph2:\n"
"s_waitcnt vmcnt(25) lgkmcnt(0)\n"
"s_barrier\n"
"v_mfma_f32_16x16x32_bf16 v[128:131],v[32:35],v[0:3],0\n"
"v_accvgpr_read_b32 v116,a32\n"
"v_accvgpr_read_b32 v117,a33\n"
"v_mfma_f32_16x16x32_bf16 v[128:131],v[36:39],v[4:7],v[128:131]\n"
"v_accvgpr_read_b32 v118,a34\n"
"v_accvgpr_read_b32 v119,a35\n"
"v_mfma_f32_16x16x32_bf16 v[160:163],v[36:39],v[12:15],0\n"
"v_accvgpr_read_b32 v120,a36\n"
"v_accvgpr_read_b32 v121,a37\n"
"v_mfma_f32_16x16x32_bf16 v[160:163],v[32:35],v[8:11],v[160:163]\n"
"s_add_u32 m0,s85,147456\n"
"v_accvgpr_read_b32 v122,a38\n"
"v_mfma_f32_16x16x32_bf16 v[192:195],v[32:35],v[16:19],0\n"
"buffer_load_dwordx4 v110,s[28:31],s86 offen lds\n"
"v_mfma_f32_16x16x32_bf16 v[192:195],v[36:39],v[20:23],v[192:195]\n"
"v_accvgpr_read_b32 v123,a39\n"
"v_cvt_pk_bf16_f32 v116,v116,v117\n"
"v_mfma_f32_16x16x32_bf16 v[224:227],v[36:39],v[28:31],0\n"
"v_cvt_pk_bf16_f32 v117,v118,v119\n"
"v_cvt_pk_bf16_f32 v118,v120,v121\n"
"v_mfma_f32_16x16x32_bf16 v[224:227],v[32:35],v[24:27],v[224:227]\n"
"v_cvt_pk_bf16_f32 v119,v122,v123\n"
"s_add_u32 s56,s56,4096\n"
"v_mfma_f32_16x16x32_bf16 v[228:231],v[40:43],v[24:27],0\n"
"ds_read_b64_tr_b16 v[32:33],v100 offset:32768\n"
"buffer_store_dwordx4 v[116:119],v112,s[24:27],s56 offen offset:0 sc1\n"
"v_mfma_f32_16x16x32_bf16 v[228:231],v[44:47],v[28:31],v[228:231]\n"
"ds_read_b64_tr_b16 v[34:35],v100 offset:33792\n"
"v_accvgpr_read_b32 v116,a40\n"
"v_mfma_f32_16x16x32_bf16 v[196:199],v[44:47],v[20:23],0\n"
"ds_read_b64_tr_b16 v[36:37],v100 offset:40960\n"
"v_accvgpr_read_b32 v117,a41\n"
"v_mfma_f32_16x16x32_bf16 v[196:199],v[40:43],v[16:19],v[196:199]\n"
"s_add_u32 m0,s85,148480\n"
"ds_read_b64_tr_b16 v[38:39],v100 offset:41984\n"
"v_mfma_f32_16x16x32_bf16 v[164:167],v[40:43],v[8:11],0\n"
"buffer_load_dwordx4 v111,s[28:31],s86 offen lds\n"
"v_mfma_f32_16x16x32_bf16 v[164:167],v[44:47],v[12:15],v[164:167]\n"
"s_add_u32 m0,s85,155648\n"
"s_xor_b32 s86,s86,262144\n"
"v_mfma_f32_16x16x32_bf16 v[132:135],v[44:47],v[4:7],0\n"
"buffer_load_dwordx4 v110,s[28:31],s86 offen lds\n"
"v_mfma_f32_16x16x32_bf16 v[132:135],v[40:43],v[0:3],v[132:135]\n"
"v_accvgpr_read_b32 v118,a42\n"
"v_accvgpr_read_b32 v119,a43\n"
"v_mfma_f32_16x16x32_bf16 v[136:139],v[48:51],v[0:3],0\n"
"ds_read_b64_tr_b16 v[40:41],v101 offset:32768\n"
"v_accvgpr_read_b32 v120,a44\n"
"v_mfma_f32_16x16x32_bf16 v[136:139],v[52:55],v[4:7],v[136:139]\n"
"ds_read_b64_tr_b16 v[42:43],v101 offset:33792\n"
"v_accvgpr_read_b32 v121,a45\n"
"v_mfma_f32_16x16x32_bf16 v[168:171],v[52:55],v[12:15],0\n"
"ds_read_b64_tr_b16 v[44:45],v101 offset:40960\n"
"v_accvgpr_read_b32 v122,a46\n"
"v_mfma_f32_16x16x32_bf16 v[168:171],v[48:51],v[8:11],v[168:171]\n"
"s_add_u32 m0,s85,156672\n"
"ds_read_b64_tr_b16 v[46:47],v101 offset:41984\n"
"v_mfma_f32_16x16x32_bf16 v[200:203],v[48:51],v[16:19],0\n"
"buffer_load_dwordx4 v111,s[28:31],s86 offen lds\n"
"v_mfma_f32_16x16x32_bf16 v[200:203],v[52:55],v[20:23],v[200:203]\n"
"s_xor_b32 s86,s86,3932160\n"
"v_accvgpr_read_b32 v123,a47\n"
"v_mfma_f32_16x16x32_bf16 v[232:235],v[52:55],v[28:31],0\n"
"v_cvt_pk_bf16_f32 v116,v116,v117\n"
"v_cvt_pk_bf16_f32 v117,v118,v119\n"
"v_mfma_f32_16x16x32_bf16 v[232:235],v[48:51],v[24:27],v[232:235]\n"
"v_cvt_pk_bf16_f32 v118,v120,v121\n"
"v_cvt_pk_bf16_f32 v119,v122,v123\n"
"v_mfma_f32_16x16x32_bf16 v[236:239],v[56:59],v[24:27],0\n"
"ds_read_b64_tr_b16 v[48:49],v102 offset:32768\n"
"buffer_store_dwordx4 v[116:119],v112,s[24:27],s56 offen offset:1024 sc1\n"
"v_mfma_f32_16x16x32_bf16 v[236:239],v[60:63],v[28:31],v[236:239]\n"
"ds_read_b64_tr_b16 v[50:51],v102 offset:33792\n"
"v_accvgpr_read_b32 v116,a48\n"
"v_mfma_f32_16x16x32_bf16 v[204:207],v[60:63],v[20:23],0\n"
"ds_read_b64_tr_b16 v[52:53],v102 offset:40960\n"
"v_accvgpr_read_b32 v117,a49\n"
"v_mfma_f32_16x16x32_bf16 v[204:207],v[56:59],v[16:19],v[204:207]\n"
"s_add_u32 m0,s78,65536\n"
"ds_read_b64_tr_b16 v[54:55],v102 offset:41984\n"
"v_mfma_f32_16x16x32_bf16 v[172:175],v[56:59],v[8:11],0\n"
"buffer_load_dwordx4 v112,s[24:27],s76 offen offset:0 sc1 lds\n"
"v_mfma_f32_16x16x32_bf16 v[172:175],v[60:63],v[12:15],v[172:175]\n"
"v_accvgpr_read_b32 v118,a50\n"
"v_mfma_f32_16x16x32_bf16 v[140:143],v[60:63],v[4:7],0\n"
"buffer_load_dwordx4 v112,s[24:27],s76 offen offset:1024 sc1 lds\n"
"v_mfma_f32_16x16x32_bf16 v[140:143],v[56:59],v[0:3],v[140:143]\n"
"s_waitcnt vmcnt(25)\n"
"v_mfma_f32_16x16x32_bf16 v[144:147],v[64:67],v[0:3],0\n"
"ds_read_b64_tr_b16 a[32:33],v98 offset:32768\n"
"ds_read_b64_tr_b16 v[56:57],v103 offset:32768\n"
"v_mfma_f32_16x16x32_bf16 v[144:147],v[68:71],v[4:7],v[144:147]\n"
"ds_read_b64_tr_b16 a[34:35],v98 offset:33792\n"
"ds_read_b64_tr_b16 v[58:59],v103 offset:33792\n"
"v_mfma_f32_16x16x32_bf16 v[176:179],v[68:71],v[12:15],0\n"
"ds_read_b64_tr_b16 a[36:37],v98 offset:40960\n"
"ds_read_b64_tr_b16 v[60:61],v103 offset:40960\n"
"v_mfma_f32_16x16x32_bf16 v[176:179],v[64:67],v[8:11],v[176:179]\n"
"ds_read_b64_tr_b16 a[38:39],v98 offset:41984\n"
"ds_read_b64_tr_b16 v[62:63],v103 offset:41984\n"
"v_mfma_f32_16x16x32_bf16 v[208:211],v[64:67],v[16:19],0\n"
"buffer_load_dwordx4 v112,s[24:27],s76 offen offset:2048 sc1 lds\n"
"v_mfma_f32_16x16x32_bf16 v[208:211],v[68:71],v[20:23],v[208:211]\n"
"ds_read_b128 a[0:3],v96 offset:0\n"
"v_accvgpr_read_b32 v119,a51\n"
"v_mfma_f32_16x16x32_bf16 v[240:243],v[68:71],v[28:31],0\n"
"v_accvgpr_read_b32 v120,a52\n"
"v_accvgpr_read_b32 v121,a53\n"
"v_mfma_f32_16x16x32_bf16 v[240:243],v[64:67],v[24:27],v[240:243]\n"
"ds_read_b128 a[8:11],v96 offset:1024\n"
"v_accvgpr_read_b32 v122,a54\n"
"v_mfma_f32_16x16x32_bf16 v[244:247],v[72:75],v[24:27],0\n"
"ds_read_b64_tr_b16 a[40:41],v99 offset:32768\n"
"ds_read_b64_tr_b16 v[64:65],v104 offset:32768\n"
"v_mfma_f32_16x16x32_bf16 v[244:247],v[76:79],v[28:31],v[244:247]\n"
"ds_read_b64_tr_b16 a[42:43],v99 offset:33792\n"
"ds_read_b64_tr_b16 v[66:67],v104 offset:33792\n"
"v_mfma_f32_16x16x32_bf16 v[212:215],v[76:79],v[20:23],0\n"
"ds_read_b64_tr_b16 a[44:45],v99 offset:40960\n"
"ds_read_b64_tr_b16 v[68:69],v104 offset:40960\n"
"v_mfma_f32_16x16x32_bf16 v[212:215],v[72:75],v[16:19],v[212:215]\n"
"ds_read_b64_tr_b16 a[46:47],v99 offset:41984\n"
"ds_read_b64_tr_b16 v[70:71],v104 offset:41984\n"
"v_mfma_f32_16x16x32_bf16 v[180:183],v[72:75],v[8:11],0\n"
"buffer_load_dwordx4 v112,s[24:27],s76 offen offset:3072 sc1 lds\n"
"v_mfma_f32_16x16x32_bf16 v[180:183],v[76:79],v[12:15],v[180:183]\n"
"s_add_u32 m0,s78,81920\n"
"s_xor_b32 s76,s76,16384\n"
"ds_read_b128 a[16:19],v96 offset:2048\n"
"v_mfma_f32_16x16x32_bf16 v[148:151],v[76:79],v[4:7],0\n"
"buffer_load_dwordx4 v112,s[24:27],s76 offen offset:0 sc1 lds\n"
"v_mfma_f32_16x16x32_bf16 v[148:151],v[72:75],v[0:3],v[148:151]\n"
"ds_read_b128 a[24:27],v96 offset:3072\n"
"v_accvgpr_read_b32 v123,a55\n"
"v_mfma_f32_16x16x32_bf16 v[152:155],v[80:83],v[0:3],0\n"
"ds_read_b64_tr_b16 v[72:73],v105 offset:32768\n"
"v_cvt_pk_bf16_f32 v116,v116,v117\n"
"v_mfma_f32_16x16x32_bf16 v[152:155],v[84:87],v[4:7],v[152:155]\n"
"ds_read_b64_tr_b16 v[74:75],v105 offset:33792\n"
"v_cvt_pk_bf16_f32 v117,v118,v119\n"
"v_mfma_f32_16x16x32_bf16 v[184:187],v[84:87],v[12:15],0\n"
"ds_read_b64_tr_b16 v[76:77],v105 offset:40960\n"
"v_cvt_pk_bf16_f32 v118,v120,v121\n"
"v_mfma_f32_16x16x32_bf16 v[184:187],v[80:83],v[8:11],v[184:187]\n"
"ds_read_b64_tr_b16 v[78:79],v105 offset:41984\n"
"v_cvt_pk_bf16_f32 v119,v122,v123\n"
"v_mfma_f32_16x16x32_bf16 v[216:219],v[80:83],v[16:19],0\n"
"ds_read_b128 a[4:7],v96 offset:16384\n"
"buffer_store_dwordx4 v[116:119],v112,s[24:27],s56 offen offset:2048 sc1\n"
"v_mfma_f32_16x16x32_bf16 v[216:219],v[84:87],v[20:23],v[216:219]\n"
"ds_read_b128 a[12:15],v96 offset:17408\n"
"v_accvgpr_read_b32 v116,a56\n"
"v_mfma_f32_16x16x32_bf16 v[248:251],v[84:87],v[28:31],0\n"
"ds_read_b128 a[20:23],v96 offset:18432\n"
"v_accvgpr_read_b32 v117,a57\n"
"v_mfma_f32_16x16x32_bf16 v[248:251],v[80:83],v[24:27],v[248:251]\n"
"ds_read_b128 a[28:31],v96 offset:19456\n"
"v_accvgpr_read_b32 v118,a58\n"
"v_mfma_f32_16x16x32_bf16 v[252:255],v[88:91],v[24:27],0\n"
"buffer_load_dwordx4 v112,s[24:27],s76 offen offset:1024 sc1 lds\n"
"v_mfma_f32_16x16x32_bf16 v[252:255],v[92:95],v[28:31],v[252:255]\n"
"v_accvgpr_read_b32 v119,a59\n"
"v_accvgpr_read_b32 v120,a60\n"
"v_mfma_f32_16x16x32_bf16 v[220:223],v[92:95],v[20:23],0\n"
"v_accvgpr_read_b32 v121,a61\n"
"v_accvgpr_read_b32 v122,a62\n"
"v_mfma_f32_16x16x32_bf16 v[220:223],v[88:91],v[16:19],v[220:223]\n"
"buffer_load_dwordx4 v112,s[24:27],s76 offen offset:2048 sc1 lds\n"
"v_mfma_f32_16x16x32_bf16 v[188:191],v[88:91],v[8:11],0\n"
"v_accvgpr_read_b32 v123,a63\n"
"v_cvt_pk_bf16_f32 v116,v116,v117\n"
"v_mfma_f32_16x16x32_bf16 v[188:191],v[92:95],v[12:15],v[188:191]\n"
"v_cvt_pk_bf16_f32 v117,v118,v119\n"
"v_cvt_pk_bf16_f32 v118,v120,v121\n"
"v_mfma_f32_16x16x32_bf16 v[156:159],v[92:95],v[4:7],0\n"
"buffer_load_dwordx4 v112,s[24:27],s76 offen offset:3072 sc1 lds\n"
"v_mfma_f32_16x16x32_bf16 v[156:159],v[88:91],v[0:3],v[156:159]\n"
"s_xor_b32 s76,s76,245760\n"
"v_cvt_pk_bf16_f32 v119,v122,v123\n"
"s_waitcnt vmcnt(19) lgkmcnt(0)\n"
"s_barrier\n"
"v_mfma_f32_16x16x32_bf16 v[128:131],a[32:35],a[0:3],v[128:131]\n"
"ds_read_b64_tr_b16 v[80:81],v98 offset:0\n"
"ds_read_b64_tr_b16 v[88:89],v99 offset:0\n"
"v_mfma_f32_16x16x32_bf16 v[128:131],a[36:39],a[4:7],v[128:131]\n"
"ds_read_b64_tr_b16 v[82:83],v98 offset:1024\n"
"ds_read_b64_tr_b16 v[90:91],v99 offset:1024\n"
"v_mfma_f32_16x16x32_bf16 v[160:163],a[36:39],a[12:15],v[160:163]\n"
"ds_read_b64_tr_b16 v[84:85],v98 offset:8192\n"
"ds_read_b64_tr_b16 v[92:93],v99 offset:8192\n"
"v_mfma_f32_16x16x32_bf16 v[160:163],a[32:35],a[8:11],v[160:163]\n"
"s_add_u32 m0,s85,131072\n"
"ds_read_b64_tr_b16 v[86:87],v98 offset:9216\n"
"ds_read_b64_tr_b16 v[94:95],v99 offset:9216\n"
"v_mfma_f32_16x16x32_bf16 v[192:195],a[32:35],a[16:19],v[192:195]\n"
"buffer_load_dwordx4 v110,s[28:31],s86 offen lds\n"
"buffer_store_dwordx4 v[116:119],v112,s[24:27],s56 offen offset:3072 sc1\n"
"v_mfma_f32_16x16x32_bf16 v[192:195],a[36:39],a[20:23],v[192:195]\n"
"s_nop 0\n"
"v_accvgpr_read_b32 v116,a64\n"
"v_mfma_f32_16x16x32_bf16 v[224:227],a[36:39],a[28:31],v[224:227]\n"
"v_accvgpr_read_b32 v117,a65\n"
"v_accvgpr_read_b32 v118,a66\n"
"v_mfma_f32_16x16x32_bf16 v[224:227],a[32:35],a[24:27],v[224:227]\n"
"v_accvgpr_read_b32 v119,a67\n"
"v_accvgpr_read_b32 v120,a68\n"
"v_mfma_f32_16x16x32_bf16 v[228:231],a[40:43],a[24:27],v[228:231]\n"
"ds_read_b64_tr_b16 a[32:33],v100 offset:0\n"
"v_accvgpr_read_b32 v121,a69\n"
"v_mfma_f32_16x16x32_bf16 v[228:231],a[44:47],a[28:31],v[228:231]\n"
"ds_read_b64_tr_b16 a[34:35],v100 offset:1024\n"
"v_accvgpr_read_b32 v122,a70\n"
"v_mfma_f32_16x16x32_bf16 v[196:199],a[44:47],a[20:23],v[196:199]\n"
"ds_read_b64_tr_b16 a[36:37],v100 offset:8192\n"
"v_accvgpr_read_b32 v123,a71\n"
"v_mfma_f32_16x16x32_bf16 v[196:199],a[40:43],a[16:19],v[196:199]\n"
"s_add_u32 m0,s85,132096\n"
"ds_read_b64_tr_b16 a[38:39],v100 offset:9216\n"
"v_mfma_f32_16x16x32_bf16 v[164:167],a[40:43],a[8:11],v[164:167]\n"
"buffer_load_dwordx4 v111,s[28:31],s86 offen lds\n"
"v_mfma_f32_16x16x32_bf16 v[164:167],a[44:47],a[12:15],v[164:167]\n"
"s_add_u32 m0,s85,139264\n"
"s_xor_b32 s86,s86,262144\n"
"v_mfma_f32_16x16x32_bf16 v[132:135],a[44:47],a[4:7],v[132:135]\n"
"buffer_load_dwordx4 v110,s[28:31],s86 offen lds\n"
"v_mfma_f32_16x16x32_bf16 v[132:135],a[40:43],a[0:3],v[132:135]\n"
"v_cvt_pk_bf16_f32 v116,v116,v117\n"
"v_cvt_pk_bf16_f32 v117,v118,v119\n"
"v_mfma_f32_16x16x32_bf16 v[136:139],v[32:35],a[0:3],v[136:139]\n"
"ds_read_b64_tr_b16 a[40:41],v101 offset:0\n"
"v_cvt_pk_bf16_f32 v118,v120,v121\n"
"v_mfma_f32_16x16x32_bf16 v[136:139],v[36:39],a[4:7],v[136:139]\n"
"ds_read_b64_tr_b16 a[42:43],v101 offset:1024\n"
"v_cvt_pk_bf16_f32 v119,v122,v123\n"
"v_mfma_f32_16x16x32_bf16 v[168:171],v[36:39],a[12:15],v[168:171]\n"
"ds_read_b64_tr_b16 a[44:45],v101 offset:8192\n"
"s_add_u32 s56,s56,126976\n"
"v_mfma_f32_16x16x32_bf16 v[168:171],v[32:35],a[8:11],v[168:171]\n"
"s_add_u32 m0,s85,140288\n"
"ds_read_b64_tr_b16 a[46:47],v101 offset:9216\n"
"v_mfma_f32_16x16x32_bf16 v[200:203],v[32:35],a[16:19],v[200:203]\n"
"buffer_load_dwordx4 v111,s[28:31],s86 offen lds\n"
"v_mfma_f32_16x16x32_bf16 v[200:203],v[36:39],a[20:23],v[200:203]\n"
"buffer_store_dwordx4 v[116:119],v112,s[24:27],s56 offen offset:0 sc1\n"
"v_mfma_f32_16x16x32_bf16 v[232:235],v[36:39],a[28:31],v[232:235]\n"
"s_xor_b32 s86,s86,786432\n"
"v_accvgpr_read_b32 v116,a72\n"
"v_accvgpr_read_b32 v117,a73\n"
"v_mfma_f32_16x16x32_bf16 v[232:235],v[32:35],a[24:27],v[232:235]\n"
"v_accvgpr_read_b32 v118,a74\n"
"v_accvgpr_read_b32 v119,a75\n"
"v_mfma_f32_16x16x32_bf16 v[236:239],v[40:43],a[24:27],v[236:239]\n"
"ds_read_b64_tr_b16 v[32:33],v102 offset:0\n"
"v_accvgpr_read_b32 v120,a76\n"
"v_mfma_f32_16x16x32_bf16 v[236:239],v[44:47],a[28:31],v[236:239]\n"
"ds_read_b64_tr_b16 v[34:35],v102 offset:1024\n"
"v_accvgpr_read_b32 v121,a77\n"
"v_mfma_f32_16x16x32_bf16 v[204:207],v[44:47],a[20:23],v[204:207]\n"
"ds_read_b64_tr_b16 v[36:37],v102 offset:8192\n"
"v_accvgpr_read_b32 v122,a78\n"
"v_mfma_f32_16x16x32_bf16 v[204:207],v[40:43],a[16:19],v[204:207]\n"
"s_add_u32 m0,s78,0\n"
"ds_read_b64_tr_b16 v[38:39],v102 offset:9216\n"
"v_mfma_f32_16x16x32_bf16 v[172:175],v[40:43],a[8:11],v[172:175]\n"
"buffer_load_dwordx4 v112,s[24:27],s76 offen offset:0 sc1 lds\n"
"v_mfma_f32_16x16x32_bf16 v[172:175],v[44:47],a[12:15],v[172:175]\n"
"v_accvgpr_read_b32 v123,a79\n"
"v_cvt_pk_bf16_f32 v116,v116,v117\n"
"v_mfma_f32_16x16x32_bf16 v[140:143],v[44:47],a[4:7],v[140:143]\n"
"buffer_load_dwordx4 v112,s[24:27],s76 offen offset:1024 sc1 lds\n"
"v_mfma_f32_16x16x32_bf16 v[140:143],v[40:43],a[0:3],v[140:143]\n"
"s_waitcnt vmcnt(23)\n"
"v_cvt_pk_bf16_f32 v117,v118,v119\n"
"v_cvt_pk_bf16_f32 v118,v120,v121\n"
"v_mfma_f32_16x16x32_bf16 v[144:147],v[48:51],a[0:3],v[144:147]\n"
"ds_read_b64_tr_b16 v[40:41],v103 offset:0\n"
"v_cvt_pk_bf16_f32 v119,v122,v123\n"
"v_mfma_f32_16x16x32_bf16 v[144:147],v[52:55],a[4:7],v[144:147]\n"
"ds_read_b64_tr_b16 v[42:43],v103 offset:1024\n"
"buffer_store_dwordx4 v[116:119],v112,s[24:27],s56 offen offset:1024 sc1\n"
"v_mfma_f32_16x16x32_bf16 v[176:179],v[52:55],a[12:15],v[176:179]\n"
"ds_read_b64_tr_b16 v[44:45],v103 offset:8192\n"
"v_accvgpr_read_b32 v116,a80\n"
"v_mfma_f32_16x16x32_bf16 v[176:179],v[48:51],a[8:11],v[176:179]\n"
"ds_read_b64_tr_b16 v[46:47],v103 offset:9216\n"
"v_accvgpr_read_b32 v117,a81\n"
"v_mfma_f32_16x16x32_bf16 v[208:211],v[48:51],a[16:19],v[208:211]\n"
"buffer_load_dwordx4 v112,s[24:27],s76 offen offset:2048 sc1 lds\n"
"v_mfma_f32_16x16x32_bf16 v[208:211],v[52:55],a[20:23],v[208:211]\n"
"ds_read_b128 v[0:3],v96 offset:32768\n"
"v_accvgpr_read_b32 v118,a82\n"
"v_mfma_f32_16x16x32_bf16 v[240:243],v[52:55],a[28:31],v[240:243]\n"
"v_accvgpr_read_b32 v119,a83\n"
"v_accvgpr_read_b32 v120,a84\n"
"v_mfma_f32_16x16x32_bf16 v[240:243],v[48:51],a[24:27],v[240:243]\n"
"ds_read_b128 v[8:11],v96 offset:33792\n"
"v_accvgpr_read_b32 v121,a85\n"
"v_mfma_f32_16x16x32_bf16 v[244:247],v[56:59],a[24:27],v[244:247]\n"
"ds_read_b64_tr_b16 v[48:49],v104 offset:0\n"
"v_accvgpr_read_b32 v122,a86\n"
"v_mfma_f32_16x16x32_bf16 v[244:247],v[60:63],a[28:31],v[244:247]\n"
"ds_read_b64_tr_b16 v[50:51],v104 offset:1024\n"
"v_accvgpr_read_b32 v123,a87\n"
"v_mfma_f32_16x16x32_bf16 v[212:215],v[60:63],a[20:23],v[212:215]\n"
"ds_read_b64_tr_b16 v[52:53],v104 offset:8192\n"
"v_cvt_pk_bf16_f32 v116,v116,v117\n"
"v_mfma_f32_16x16x32_bf16 v[212:215],v[56:59],a[16:19],v[212:215]\n"
"ds_read_b64_tr_b16 v[54:55],v104 offset:9216\n"
"v_cvt_pk_bf16_f32 v117,v118,v119\n"
"v_mfma_f32_16x16x32_bf16 v[180:183],v[56:59],a[8:11],v[180:183]\n"
"buffer_load_dwordx4 v112,s[24:27],s76 offen offset:3072 sc1 lds\n"
"v_mfma_f32_16x16x32_bf16 v[180:183],v[60:63],a[12:15],v[180:183]\n"
"s_add_u32 m0,s78,16384\n"
"s_xor_b32 s76,s76,16384\n"
"ds_read_b128 v[16:19],v96 offset:34816\n"
"v_mfma_f32_16x16x32_bf16 v[148:151],v[60:63],a[4:7],v[148:151]\n"
"buffer_load_dwordx4 v112,s[24:27],s76 offen offset:0 sc1 lds\n"
"v_mfma_f32_16x16x32_bf16 v[148:151],v[56:59],a[0:3],v[148:151]\n"
"ds_read_b128 v[24:27],v96 offset:35840\n"
"v_cvt_pk_bf16_f32 v118,v120,v121\n"
"v_mfma_f32_16x16x32_bf16 v[152:155],v[64:67],a[0:3],v[152:155]\n"
"ds_read_b64_tr_b16 v[56:57],v105 offset:0\n"
"v_cvt_pk_bf16_f32 v119,v122,v123\n"
"v_mfma_f32_16x16x32_bf16 v[152:155],v[68:71],a[4:7],v[152:155]\n"
"ds_read_b64_tr_b16 v[58:59],v105 offset:1024\n"
"buffer_store_dwordx4 v[116:119],v112,s[24:27],s56 offen offset:2048 sc1\n"
"v_mfma_f32_16x16x32_bf16 v[184:187],v[68:71],a[12:15],v[184:187]\n"
"ds_read_b64_tr_b16 v[60:61],v105 offset:8192\n"
"v_accvgpr_read_b32 v116,a88\n"
"v_mfma_f32_16x16x32_bf16 v[184:187],v[64:67],a[8:11],v[184:187]\n"
"ds_read_b64_tr_b16 v[62:63],v105 offset:9216\n"
"v_accvgpr_read_b32 v117,a89\n"
"v_mfma_f32_16x16x32_bf16 v[216:219],v[64:67],a[16:19],v[216:219]\n"
"ds_read_b128 v[4:7],v96 offset:49152\n"
"v_accvgpr_read_b32 v118,a90\n"
"v_mfma_f32_16x16x32_bf16 v[216:219],v[68:71],a[20:23],v[216:219]\n"
"ds_read_b128 v[12:15],v96 offset:50176\n"
"v_accvgpr_read_b32 v119,a91\n"
"v_mfma_f32_16x16x32_bf16 v[248:251],v[68:71],a[28:31],v[248:251]\n"
"ds_read_b128 v[20:23],v96 offset:51200\n"
"v_accvgpr_read_b32 v120,a92\n"
"v_mfma_f32_16x16x32_bf16 v[248:251],v[64:67],a[24:27],v[248:251]\n"
"ds_read_b128 v[28:31],v96 offset:52224\n"
"v_accvgpr_read_b32 v121,a93\n"
"v_mfma_f32_16x16x32_bf16 v[252:255],v[72:75],a[24:27],v[252:255]\n"
"buffer_load_dwordx4 v112,s[24:27],s76 offen offset:1024 sc1 lds\n"
"v_mfma_f32_16x16x32_bf16 v[252:255],v[76:79],a[28:31],v[252:255]\n"
"v_accvgpr_read_b32 v122,a94\n"
"v_accvgpr_read_b32 v123,a95\n"
"v_mfma_f32_16x16x32_bf16 v[220:223],v[76:79],a[20:23],v[220:223]\n"
"v_cvt_pk_bf16_f32 v116,v116,v117\n"
"v_cvt_pk_bf16_f32 v117,v118,v119\n"
"v_mfma_f32_16x16x32_bf16 v[220:223],v[72:75],a[16:19],v[220:223]\n"
"buffer_load_dwordx4 v112,s[24:27],s76 offen offset:2048 sc1 lds\n"
"v_mfma_f32_16x16x32_bf16 v[188:191],v[72:75],a[8:11],v[188:191]\n"
"v_cvt_pk_bf16_f32 v118,v120,v121\n"
"v_cvt_pk_bf16_f32 v119,v122,v123\n"
"v_mfma_f32_16x16x32_bf16 v[188:191],v[76:79],a[12:15],v[188:191]\n"
"buffer_store_dwordx4 v[116:119],v112,s[24:27],s56 offen offset:3072 sc1\n"
"v_mfma_f32_16x16x32_bf16 v[156:159],v[76:79],a[4:7],v[156:159]\n"
"buffer_load_dwordx4 v112,s[24:27],s76 offen offset:3072 sc1 lds\n"
"v_accvgpr_read_b32 v116,a96\n"
"v_mfma_f32_16x16x32_bf16 v[156:159],v[72:75],a[0:3],v[156:159]\n"
"s_xor_b32 s76,s76,49152\n"
"v_accvgpr_read_b32 v117,a97\n"
"s_waitcnt vmcnt(27) lgkmcnt(0)\n"
"s_barrier\n"
"v_mfma_f32_16x16x32_bf16 v[128:131],v[80:83],v[0:3],v[128:131]\n"
"ds_read_b64_tr_b16 v[64:65],v98 offset:49152\n"
"ds_read_b64_tr_b16 v[72:73],v99 offset:49152\n"
"v_mfma_f32_16x16x32_bf16 v[128:131],v[84:87],v[4:7],v[128:131]\n"
"ds_read_b64_tr_b16 v[66:67],v98 offset:50176\n"
"ds_read_b64_tr_b16 v[74:75],v99 offset:50176\n"
"v_mfma_f32_16x16x32_bf16 v[160:163],v[84:87],v[12:15],v[160:163]\n"
"ds_read_b64_tr_b16 v[68:69],v98 offset:57344\n"
"ds_read_b64_tr_b16 v[76:77],v99 offset:57344\n"
"v_mfma_f32_16x16x32_bf16 v[160:163],v[80:83],v[8:11],v[160:163]\n"
"s_add_u32 m0,s85,98304\n"
"ds_read_b64_tr_b16 v[70:71],v98 offset:58368\n"
"ds_read_b64_tr_b16 v[78:79],v99 offset:58368\n"
"v_mfma_f32_16x16x32_bf16 v[192:195],v[80:83],v[16:19],v[192:195]\n"
"buffer_load_dwordx4 v110,s[28:31],s86 offen lds\n"
"v_mfma_f32_16x16x32_bf16 v[192:195],v[84:87],v[20:23],v[192:195]\n"
"v_accvgpr_read_b32 v118,a98\n"
"v_accvgpr_read_b32 v119,a99\n"
"v_mfma_f32_16x16x32_bf16 v[224:227],v[84:87],v[28:31],v[224:227]\n"
"v_accvgpr_read_b32 v120,a100\n"
"v_accvgpr_read_b32 v121,a101\n"
"v_mfma_f32_16x16x32_bf16 v[224:227],v[80:83],v[24:27],v[224:227]\n"
"v_accvgpr_read_b32 v122,a102\n"
"v_accvgpr_read_b32 v123,a103\n"
"v_mfma_f32_16x16x32_bf16 v[228:231],v[88:91],v[24:27],v[228:231]\n"
"ds_read_b64_tr_b16 v[80:81],v100 offset:49152\n"
"v_cvt_pk_bf16_f32 v116,v116,v117\n"
"v_mfma_f32_16x16x32_bf16 v[228:231],v[92:95],v[28:31],v[228:231]\n"
"ds_read_b64_tr_b16 v[82:83],v100 offset:50176\n"
"v_cvt_pk_bf16_f32 v117,v118,v119\n"
"v_mfma_f32_16x16x32_bf16 v[196:199],v[92:95],v[20:23],v[196:199]\n"
"ds_read_b64_tr_b16 v[84:85],v100 offset:57344\n"
"v_cvt_pk_bf16_f32 v118,v120,v121\n"
"v_mfma_f32_16x16x32_bf16 v[196:199],v[88:91],v[16:19],v[196:199]\n"
"s_add_u32 m0,s85,99328\n"
"ds_read_b64_tr_b16 v[86:87],v100 offset:58368\n"
"v_mfma_f32_16x16x32_bf16 v[164:167],v[88:91],v[8:11],v[164:167]\n"
"buffer_load_dwordx4 v111,s[28:31],s86 offen lds\n"
"v_mfma_f32_16x16x32_bf16 v[164:167],v[92:95],v[12:15],v[164:167]\n"
"s_add_u32 m0,s85,106496\n"
"s_xor_b32 s86,s86,262144\n"
"v_mfma_f32_16x16x32_bf16 v[132:135],v[92:95],v[4:7],v[132:135]\n"
"buffer_load_dwordx4 v110,s[28:31],s86 offen lds\n"
"v_mfma_f32_16x16x32_bf16 v[132:135],v[88:91],v[0:3],v[132:135]\n"
"v_cvt_pk_bf16_f32 v119,v122,v123\n"
"s_add_u32 s56,s56,4096\n"
"v_mfma_f32_16x16x32_bf16 v[136:139],a[32:35],v[0:3],v[136:139]\n"
"ds_read_b64_tr_b16 v[88:89],v101 offset:49152\n"
"buffer_store_dwordx4 v[116:119],v112,s[24:27],s56 offen offset:0 sc1\n"
"v_mfma_f32_16x16x32_bf16 v[136:139],a[36:39],v[4:7],v[136:139]\n"
"ds_read_b64_tr_b16 v[90:91],v101 offset:50176\n"
"v_accvgpr_read_b32 v116,a104\n"
"v_mfma_f32_16x16x32_bf16 v[168:171],a[36:39],v[12:15],v[168:171]\n"
"ds_read_b64_tr_b16 v[92:93],v101 offset:57344\n"
"v_accvgpr_read_b32 v117,a105\n"
"v_mfma_f32_16x16x32_bf16 v[168:171],a[32:35],v[8:11],v[168:171]\n"
"s_add_u32 m0,s85,107520\n"
"ds_read_b64_tr_b16 v[94:95],v101 offset:58368\n"
"v_mfma_f32_16x16x32_bf16 v[200:203],a[32:35],v[16:19],v[200:203]\n"
"buffer_load_dwordx4 v111,s[28:31],s86 offen lds\n"
"v_mfma_f32_16x16x32_bf16 v[200:203],a[36:39],v[20:23],v[200:203]\n"
"s_xor_b32 s86,s86,1835008\n"
"v_accvgpr_read_b32 v118,a106\n"
"v_mfma_f32_16x16x32_bf16 v[232:235],a[36:39],v[28:31],v[232:235]\n"
"v_accvgpr_read_b32 v119,a107\n"
"v_accvgpr_read_b32 v120,a108\n"
"v_mfma_f32_16x16x32_bf16 v[232:235],a[32:35],v[24:27],v[232:235]\n"
"v_accvgpr_read_b32 v121,a109\n"
"v_accvgpr_read_b32 v122,a110\n"
"v_mfma_f32_16x16x32_bf16 v[236:239],a[40:43],v[24:27],v[236:239]\n"
"ds_read_b64_tr_b16 a[32:33],v102 offset:49152\n"
"v_accvgpr_read_b32 v123,a111\n"
"v_mfma_f32_16x16x32_bf16 v[236:239],a[44:47],v[28:31],v[236:239]\n"
"ds_read_b64_tr_b16 a[34:35],v102 offset:50176\n"
"v_cvt_pk_bf16_f32 v116,v116,v117\n"
"v_mfma_f32_16x16x32_bf16 v[204:207],a[44:47],v[20:23],v[204:207]\n"
"ds_read_b64_tr_b16 a[36:37],v102 offset:57344\n"
"v_cvt_pk_bf16_f32 v117,v118,v119\n"
"v_mfma_f32_16x16x32_bf16 v[204:207],a[40:43],v[16:19],v[204:207]\n"
"s_add_u32 m0,s78,32768\n"
"ds_read_b64_tr_b16 a[38:39],v102 offset:58368\n"
"v_mfma_f32_16x16x32_bf16 v[172:175],a[40:43],v[8:11],v[172:175]\n"
"buffer_load_dwordx4 v112,s[24:27],s76 offen offset:0 sc1 lds\n"
"v_mfma_f32_16x16x32_bf16 v[172:175],a[44:47],v[12:15],v[172:175]\n"
"v_cvt_pk_bf16_f32 v118,v120,v121\n"
"v_cvt_pk_bf16_f32 v119,v122,v123\n"
"v_mfma_f32_16x16x32_bf16 v[140:143],a[44:47],v[4:7],v[140:143]\n"
"buffer_load_dwordx4 v112,s[24:27],s76 offen offset:1024 sc1 lds\n"
"buffer_store_dwordx4 v[116:119],v112,s[24:27],s56 offen offset:1024 sc1\n"
"v_mfma_f32_16x16x32_bf16 v[140:143],a[40:43],v[0:3],v[140:143]\n"
"s_nop 0\n"
"v_accvgpr_read_b32 v116,a112\n"
"s_waitcnt vmcnt(25)\n"
"v_mfma_f32_16x16x32_bf16 v[144:147],v[32:35],v[0:3],v[144:147]\n"
"ds_read_b64_tr_b16 a[40:41],v103 offset:49152\n"
"v_accvgpr_read_b32 v117,a113\n"
"v_mfma_f32_16x16x32_bf16 v[144:147],v[36:39],v[4:7],v[144:147]\n"
"ds_read_b64_tr_b16 a[42:43],v103 offset:50176\n"
"v_accvgpr_read_b32 v118,a114\n"
"v_mfma_f32_16x16x32_bf16 v[176:179],v[36:39],v[12:15],v[176:179]\n"
"ds_read_b64_tr_b16 a[44:45],v103 offset:57344\n"
"v_accvgpr_read_b32 v119,a115\n"
"v_mfma_f32_16x16x32_bf16 v[176:179],v[32:35],v[8:11],v[176:179]\n"
"ds_read_b64_tr_b16 a[46:47],v103 offset:58368\n"
"v_accvgpr_read_b32 v120,a116\n"
"v_mfma_f32_16x16x32_bf16 v[208:211],v[32:35],v[16:19],v[208:211]\n"
"buffer_load_dwordx4 v112,s[24:27],s76 offen offset:2048 sc1 lds\n"
"v_mfma_f32_16x16x32_bf16 v[208:211],v[36:39],v[20:23],v[208:211]\n"
"ds_read_b128 a[0:3],v97 offset:0\n"
"v_accvgpr_read_b32 v121,a117\n"
"v_mfma_f32_16x16x32_bf16 v[240:243],v[36:39],v[28:31],v[240:243]\n"
"v_accvgpr_read_b32 v122,a118\n"
"v_accvgpr_read_b32 v123,a119\n"
"v_mfma_f32_16x16x32_bf16 v[240:243],v[32:35],v[24:27],v[240:243]\n"
"ds_read_b128 a[8:11],v97 offset:1024\n"
"v_cvt_pk_bf16_f32 v116,v116,v117\n"
"v_mfma_f32_16x16x32_bf16 v[244:247],v[40:43],v[24:27],v[244:247]\n"
"ds_read_b64_tr_b16 v[32:33],v104 offset:49152\n"
"v_cvt_pk_bf16_f32 v117,v118,v119\n"
"v_mfma_f32_16x16x32_bf16 v[244:247],v[44:47],v[28:31],v[244:247]\n"
"ds_read_b64_tr_b16 v[34:35],v104 offset:50176\n"
"v_cvt_pk_bf16_f32 v118,v120,v121\n"
"v_mfma_f32_16x16x32_bf16 v[212:215],v[44:47],v[20:23],v[212:215]\n"
"ds_read_b64_tr_b16 v[36:37],v104 offset:57344\n"
"v_cvt_pk_bf16_f32 v119,v122,v123\n"
"v_mfma_f32_16x16x32_bf16 v[212:215],v[40:43],v[16:19],v[212:215]\n"
"ds_read_b64_tr_b16 v[38:39],v104 offset:58368\n"
"buffer_store_dwordx4 v[116:119],v112,s[24:27],s56 offen offset:2048 sc1\n"
"v_mfma_f32_16x16x32_bf16 v[180:183],v[40:43],v[8:11],v[180:183]\n"
"buffer_load_dwordx4 v112,s[24:27],s76 offen offset:3072 sc1 lds\n"
"v_mfma_f32_16x16x32_bf16 v[180:183],v[44:47],v[12:15],v[180:183]\n"
"s_add_u32 m0,s78,49152\n"
"s_xor_b32 s76,s76,16384\n"
"ds_read_b128 a[16:19],v97 offset:2048\n"
"v_mfma_f32_16x16x32_bf16 v[148:151],v[44:47],v[4:7],v[148:151]\n"
"buffer_load_dwordx4 v112,s[24:27],s76 offen offset:0 sc1 lds\n"
"v_mfma_f32_16x16x32_bf16 v[148:151],v[40:43],v[0:3],v[148:151]\n"
"ds_read_b128 a[24:27],v97 offset:3072\n"
"v_accvgpr_read_b32 v116,a120\n"
"v_mfma_f32_16x16x32_bf16 v[152:155],v[48:51],v[0:3],v[152:155]\n"
"ds_read_b64_tr_b16 v[40:41],v105 offset:49152\n"
"v_accvgpr_read_b32 v117,a121\n"
"v_mfma_f32_16x16x32_bf16 v[152:155],v[52:55],v[4:7],v[152:155]\n"
"ds_read_b64_tr_b16 v[42:43],v105 offset:50176\n"
"v_accvgpr_read_b32 v118,a122\n"
"v_mfma_f32_16x16x32_bf16 v[184:187],v[52:55],v[12:15],v[184:187]\n"
"ds_read_b64_tr_b16 v[44:45],v105 offset:57344\n"
"v_accvgpr_read_b32 v119,a123\n"
"v_mfma_f32_16x16x32_bf16 v[184:187],v[48:51],v[8:11],v[184:187]\n"
"ds_read_b64_tr_b16 v[46:47],v105 offset:58368\n"
"v_accvgpr_read_b32 v120,a124\n"
"v_mfma_f32_16x16x32_bf16 v[216:219],v[48:51],v[16:19],v[216:219]\n"
"ds_read_b128 a[4:7],v97 offset:16384\n"
"v_accvgpr_read_b32 v121,a125\n"
"v_mfma_f32_16x16x32_bf16 v[216:219],v[52:55],v[20:23],v[216:219]\n"
"ds_read_b128 a[12:15],v97 offset:17408\n"
"v_accvgpr_read_b32 v122,a126\n"
"v_mfma_f32_16x16x32_bf16 v[248:251],v[52:55],v[28:31],v[248:251]\n"
"ds_read_b128 a[20:23],v97 offset:18432\n"
"v_accvgpr_read_b32 v123,a127\n"
"v_mfma_f32_16x16x32_bf16 v[248:251],v[48:51],v[24:27],v[248:251]\n"
"ds_read_b128 a[28:31],v97 offset:19456\n"
"v_cvt_pk_bf16_f32 v116,v116,v117\n"
"v_mfma_f32_16x16x32_bf16 v[252:255],v[56:59],v[24:27],v[252:255]\n"
"buffer_load_dwordx4 v112,s[24:27],s76 offen offset:1024 sc1 lds\n"
"v_mfma_f32_16x16x32_bf16 v[252:255],v[60:63],v[28:31],v[252:255]\n"
"v_cvt_pk_bf16_f32 v117,v118,v119\n"
"v_cvt_pk_bf16_f32 v118,v120,v121\n"
"v_cvt_pk_bf16_f32 v119,v122,v123\n"
"v_mfma_f32_16x16x32_bf16 v[220:223],v[60:63],v[20:23],v[220:223]\n"
"buffer_store_dwordx4 v[116:119],v112,s[24:27],s56 offen offset:3072 sc1\n"
"v_mfma_f32_16x16x32_bf16 v[220:223],v[56:59],v[16:19],v[220:223]\n"
"buffer_load_dwordx4 v112,s[24:27],s76 offen offset:2048 sc1 lds\n"
"v_mfma_f32_16x16x32_bf16 v[188:191],v[56:59],v[8:11],v[188:191]\n"
"v_accvgpr_read_b32 v116,a128\n"
"v_accvgpr_read_b32 v117,a129\n"
"v_mfma_f32_16x16x32_bf16 v[188:191],v[60:63],v[12:15],v[188:191]\n"
"v_accvgpr_read_b32 v118,a130\n"
"v_accvgpr_read_b32 v119,a131\n"
"v_mfma_f32_16x16x32_bf16 v[156:159],v[60:63],v[4:7],v[156:159]\n"
"buffer_load_dwordx4 v112,s[24:27],s76 offen offset:3072 sc1 lds\n"
"v_mfma_f32_16x16x32_bf16 v[156:159],v[56:59],v[0:3],v[156:159]\n"
"s_xor_b32 s76,s76,114688\n"
"v_accvgpr_read_b32 v120,a132\n"
"s_waitcnt vmcnt(28) lgkmcnt(0)\n"
"s_barrier\n"
"v_mfma_f32_16x16x32_bf16 v[128:131],v[64:67],a[0:3],v[128:131]\n"
"ds_read_b64_tr_b16 v[48:49],v98 offset:32768\n"
"ds_read_b64_tr_b16 v[56:57],v99 offset:32768\n"
"v_mfma_f32_16x16x32_bf16 v[128:131],v[68:71],a[4:7],v[128:131]\n"
"ds_read_b64_tr_b16 v[50:51],v98 offset:33792\n"
"ds_read_b64_tr_b16 v[58:59],v99 offset:33792\n"
"v_mfma_f32_16x16x32_bf16 v[160:163],v[68:71],a[12:15],v[160:163]\n"
"ds_read_b64_tr_b16 v[52:53],v98 offset:40960\n"
"ds_read_b64_tr_b16 v[60:61],v99 offset:40960\n"
"v_mfma_f32_16x16x32_bf16 v[160:163],v[64:67],a[8:11],v[160:163]\n"
"s_add_u32 m0,s85,147456\n"
"ds_read_b64_tr_b16 v[54:55],v98 offset:41984\n"
"ds_read_b64_tr_b16 v[62:63],v99 offset:41984\n"
"v_mfma_f32_16x16x32_bf16 v[192:195],v[64:67],a[16:19],v[192:195]\n"
"buffer_load_dwordx4 v110,s[28:31],s86 offen lds\n"
"v_mfma_f32_16x16x32_bf16 v[192:195],v[68:71],a[20:23],v[192:195]\n"
"v_accvgpr_read_b32 v121,a133\n"
"v_accvgpr_read_b32 v122,a134\n"
"v_mfma_f32_16x16x32_bf16 v[224:227],v[68:71],a[28:31],v[224:227]\n"
"v_accvgpr_read_b32 v123,a135\n"
"v_cvt_pk_bf16_f32 v116,v116,v117\n"
"v_mfma_f32_16x16x32_bf16 v[224:227],v[64:67],a[24:27],v[224:227]\n"
"v_cvt_pk_bf16_f32 v117,v118,v119\n"
"v_cvt_pk_bf16_f32 v118,v120,v121\n"
"v_mfma_f32_16x16x32_bf16 v[228:231],v[72:75],a[24:27],v[228:231]\n"
"ds_read_b64_tr_b16 v[64:65],v100 offset:32768\n"
"v_cvt_pk_bf16_f32 v119,v122,v123\n"
"v_mfma_f32_16x16x32_bf16 v[228:231],v[76:79],a[28:31],v[228:231]\n"
"ds_read_b64_tr_b16 v[66:67],v100 offset:33792\n"
"s_add_u32 s56,s56,126976\n"
"v_mfma_f32_16x16x32_bf16 v[196:199],v[76:79],a[20:23],v[196:199]\n"
"ds_read_b64_tr_b16 v[68:69],v100 offset:40960\n"
"buffer_store_dwordx4 v[116:119],v112,s[24:27],s56 offen offset:0 sc1\n"
"v_mfma_f32_16x16x32_bf16 v[196:199],v[72:75],a[16:19],v[196:199]\n"
"s_add_u32 m0,s85,148480\n"
"ds_read_b64_tr_b16 v[70:71],v100 offset:41984\n"
"v_mfma_f32_16x16x32_bf16 v[164:167],v[72:75],a[8:11],v[164:167]\n"
"buffer_load_dwordx4 v111,s[28:31],s86 offen lds\n"
"v_mfma_f32_16x16x32_bf16 v[164:167],v[76:79],a[12:15],v[164:167]\n"
"s_add_u32 m0,s85,155648\n"
"s_xor_b32 s86,s86,262144\n"
"v_mfma_f32_16x16x32_bf16 v[132:135],v[76:79],a[4:7],v[132:135]\n"
"buffer_load_dwordx4 v110,s[28:31],s86 offen lds\n"
"v_mfma_f32_16x16x32_bf16 v[132:135],v[72:75],a[0:3],v[132:135]\n"
"v_accvgpr_read_b32 v116,a136\n"
"v_accvgpr_read_b32 v117,a137\n"
"v_mfma_f32_16x16x32_bf16 v[136:139],v[80:83],a[0:3],v[136:139]\n"
"ds_read_b64_tr_b16 v[72:73],v101 offset:32768\n"
"v_accvgpr_read_b32 v118,a138\n"
"v_mfma_f32_16x16x32_bf16 v[136:139],v[84:87],a[4:7],v[136:139]\n"
"ds_read_b64_tr_b16 v[74:75],v101 offset:33792\n"
"v_accvgpr_read_b32 v119,a139\n"
"v_mfma_f32_16x16x32_bf16 v[168:171],v[84:87],a[12:15],v[168:171]\n"
"ds_read_b64_tr_b16 v[76:77],v101 offset:40960\n"
"v_accvgpr_read_b32 v120,a140\n"
"v_mfma_f32_16x16x32_bf16 v[168:171],v[80:83],a[8:11],v[168:171]\n"
"s_add_u32 m0,s85,156672\n"
"ds_read_b64_tr_b16 v[78:79],v101 offset:41984\n"
"v_mfma_f32_16x16x32_bf16 v[200:203],v[80:83],a[16:19],v[200:203]\n"
"buffer_load_dwordx4 v111,s[28:31],s86 offen lds\n"
"v_mfma_f32_16x16x32_bf16 v[200:203],v[84:87],a[20:23],v[200:203]\n"
"s_xor_b32 s86,s86,786432\n"
"v_accvgpr_read_b32 v121,a141\n"
"v_mfma_f32_16x16x32_bf16 v[232:235],v[84:87],a[28:31],v[232:235]\n"
"v_accvgpr_read_b32 v122,a142\n"
"v_accvgpr_read_b32 v123,a143\n"
"v_mfma_f32_16x16x32_bf16 v[232:235],v[80:83],a[24:27],v[232:235]\n"
"v_cvt_pk_bf16_f32 v116,v116,v117\n"
"v_cvt_pk_bf16_f32 v117,v118,v119\n"
"v_mfma_f32_16x16x32_bf16 v[236:239],v[88:91],a[24:27],v[236:239]\n"
"ds_read_b64_tr_b16 v[80:81],v102 offset:32768\n"
"v_cvt_pk_bf16_f32 v118,v120,v121\n"
"v_mfma_f32_16x16x32_bf16 v[236:239],v[92:95],a[28:31],v[236:239]\n"
"ds_read_b64_tr_b16 v[82:83],v102 offset:33792\n"
"v_cvt_pk_bf16_f32 v119,v122,v123\n"
"v_mfma_f32_16x16x32_bf16 v[204:207],v[92:95],a[20:23],v[204:207]\n"
"ds_read_b64_tr_b16 v[84:85],v102 offset:40960\n"
"buffer_store_dwordx4 v[116:119],v112,s[24:27],s56 offen offset:1024 sc1\n"
"v_mfma_f32_16x16x32_bf16 v[204:207],v[88:91],a[16:19],v[204:207]\n"
"s_add_u32 m0,s78,65536\n"
"ds_read_b64_tr_b16 v[86:87],v102 offset:41984\n"
"v_accvgpr_read_b32 v118,a146\n"
"v_mfma_f32_16x16x32_bf16 v[172:175],v[88:91],a[8:11],v[172:175]\n"
"buffer_load_dwordx4 v112,s[24:27],s76 offen offset:0 sc1 lds\n"
"v_mfma_f32_16x16x32_bf16 v[172:175],v[92:95],a[12:15],v[172:175]\n"
"v_accvgpr_read_b32 v116,a144\n"
"v_accvgpr_read_b32 v117,a145\n"
"v_mfma_f32_16x16x32_bf16 v[140:143],v[92:95],a[4:7],v[140:143]\n"
"buffer_load_dwordx4 v112,s[24:27],s76 offen offset:1024 sc1 lds\n"
"v_mfma_f32_16x16x32_bf16 v[140:143],v[88:91],a[0:3],v[140:143]\n"
"v_accvgpr_read_b32 v119,a147\n"
"s_waitcnt vmcnt(24)\n"
"v_mfma_f32_16x16x32_bf16 v[144:147],a[32:35],a[0:3],v[144:147]\n"
"ds_read_b64_tr_b16 v[88:89],v103 offset:32768\n"
"v_accvgpr_read_b32 v120,a148\n"
"v_mfma_f32_16x16x32_bf16 v[144:147],a[36:39],a[4:7],v[144:147]\n"
"ds_read_b64_tr_b16 v[90:91],v103 offset:33792\n"
"v_accvgpr_read_b32 v121,a149\n"
"v_mfma_f32_16x16x32_bf16 v[176:179],a[36:39],a[12:15],v[176:179]\n"
"ds_read_b64_tr_b16 v[92:93],v103 offset:40960\n"
"v_accvgpr_read_b32 v122,a150\n"
"v_mfma_f32_16x16x32_bf16 v[176:179],a[32:35],a[8:11],v[176:179]\n"
"ds_read_b64_tr_b16 v[94:95],v103 offset:41984\n"
"v_accvgpr_read_b32 v123,a151\n"
"v_mfma_f32_16x16x32_bf16 v[208:211],a[32:35],a[16:19],v[208:211]\n"
"buffer_load_dwordx4 v112,s[24:27],s76 offen offset:2048 sc1 lds\n"
"v_mfma_f32_16x16x32_bf16 v[208:211],a[36:39],a[20:23],v[208:211]\n"
"ds_read_b128 v[0:3],v96 offset:0\n"
"v_cvt_pk_bf16_f32 v116,v116,v117\n"
"v_mfma_f32_16x16x32_bf16 v[240:243],a[36:39],a[28:31],v[240:243]\n"
"v_cvt_pk_bf16_f32 v117,v118,v119\n"
"v_cvt_pk_bf16_f32 v118,v120,v121\n"
"v_mfma_f32_16x16x32_bf16 v[240:243],a[32:35],a[24:27],v[240:243]\n"
"ds_read_b128 v[8:11],v96 offset:1024\n"
"v_cvt_pk_bf16_f32 v119,v122,v123\n"
"v_mfma_f32_16x16x32_bf16 v[244:247],a[40:43],a[24:27],v[244:247]\n"
"ds_read_b64_tr_b16 a[32:33],v104 offset:32768\n"
"buffer_store_dwordx4 v[116:119],v112,s[24:27],s56 offen offset:2048 sc1\n"
"v_mfma_f32_16x16x32_bf16 v[244:247],a[44:47],a[28:31],v[244:247]\n"
"ds_read_b64_tr_b16 a[34:35],v104 offset:33792\n"
"v_accvgpr_read_b32 v116,a152\n"
"v_mfma_f32_16x16x32_bf16 v[212:215],a[44:47],a[20:23],v[212:215]\n"
"ds_read_b64_tr_b16 a[36:37],v104 offset:40960\n"
"v_accvgpr_read_b32 v117,a153\n"
"v_mfma_f32_16x16x32_bf16 v[212:215],a[40:43],a[16:19],v[212:215]\n"
"ds_read_b64_tr_b16 a[38:39],v104 offset:41984\n"
"v_accvgpr_read_b32 v118,a154\n"
"v_mfma_f32_16x16x32_bf16 v[180:183],a[40:43],a[8:11],v[180:183]\n"
"buffer_load_dwordx4 v112,s[24:27],s76 offen offset:3072 sc1 lds\n"
"v_mfma_f32_16x16x32_bf16 v[180:183],a[44:47],a[12:15],v[180:183]\n"
"s_add_u32 m0,s78,81920\n"
"s_xor_b32 s76,s76,16384\n"
"ds_read_b128 v[16:19],v96 offset:2048\n"
"v_mfma_f32_16x16x32_bf16 v[148:151],a[44:47],a[4:7],v[148:151]\n"
"buffer_load_dwordx4 v112,s[24:27],s76 offen offset:0 sc1 lds\n"
"v_mfma_f32_16x16x32_bf16 v[148:151],a[40:43],a[0:3],v[148:151]\n"
"ds_read_b128 v[24:27],v96 offset:3072\n"
"v_accvgpr_read_b32 v119,a155\n"
"v_mfma_f32_16x16x32_bf16 v[152:155],v[32:35],a[0:3],v[152:155]\n"
"ds_read_b64_tr_b16 a[40:41],v105 offset:32768\n"
"v_accvgpr_read_b32 v120,a156\n"
"v_mfma_f32_16x16x32_bf16 v[152:155],v[36:39],a[4:7],v[152:155]\n"
"ds_read_b64_tr_b16 a[42:43],v105 offset:33792\n"
"v_accvgpr_read_b32 v121,a157\n"
"v_mfma_f32_16x16x32_bf16 v[184:187],v[36:39],a[12:15],v[184:187]\n"
"ds_read_b64_tr_b16 a[44:45],v105 offset:40960\n"
"v_accvgpr_read_b32 v122,a158\n"
"v_mfma_f32_16x16x32_bf16 v[184:187],v[32:35],a[8:11],v[184:187]\n"
"ds_read_b64_tr_b16 a[46:47],v105 offset:41984\n"
"v_accvgpr_read_b32 v123,a159\n"
"v_mfma_f32_16x16x32_bf16 v[216:219],v[32:35],a[16:19],v[216:219]\n"
"ds_read_b128 v[4:7],v96 offset:16384\n"
"v_cvt_pk_bf16_f32 v116,v116,v117\n"
"v_mfma_f32_16x16x32_bf16 v[216:219],v[36:39],a[20:23],v[216:219]\n"
"ds_read_b128 v[12:15],v96 offset:17408\n"
"v_cvt_pk_bf16_f32 v117,v118,v119\n"
"v_mfma_f32_16x16x32_bf16 v[248:251],v[36:39],a[28:31],v[248:251]\n"
"ds_read_b128 v[20:23],v96 offset:18432\n"
"v_cvt_pk_bf16_f32 v118,v120,v121\n"
"v_mfma_f32_16x16x32_bf16 v[248:251],v[32:35],a[24:27],v[248:251]\n"
"ds_read_b128 v[28:31],v96 offset:19456\n"
"v_cvt_pk_bf16_f32 v119,v122,v123\n"
"v_mfma_f32_16x16x32_bf16 v[252:255],v[40:43],a[24:27],v[252:255]\n"
"buffer_load_dwordx4 v112,s[24:27],s76 offen offset:1024 sc1 lds\n"
"buffer_store_dwordx4 v[116:119],v112,s[24:27],s56 offen offset:3072 sc1\n"
"v_mfma_f32_16x16x32_bf16 v[252:255],v[44:47],a[28:31],v[252:255]\n"
"s_nop 0\n"
"v_accvgpr_read_b32 v116,a160\n"
"v_mfma_f32_16x16x32_bf16 v[220:223],v[44:47],a[20:23],v[220:223]\n"
"v_accvgpr_read_b32 v117,a161\n"
"v_accvgpr_read_b32 v118,a162\n"
"v_mfma_f32_16x16x32_bf16 v[220:223],v[40:43],a[16:19],v[220:223]\n"
"buffer_load_dwordx4 v112,s[24:27],s76 offen offset:2048 sc1 lds\n"
"v_mfma_f32_16x16x32_bf16 v[188:191],v[40:43],a[8:11],v[188:191]\n"
"v_accvgpr_read_b32 v119,a163\n"
"v_accvgpr_read_b32 v120,a164\n"
"v_mfma_f32_16x16x32_bf16 v[188:191],v[44:47],a[12:15],v[188:191]\n"
"v_accvgpr_read_b32 v121,a165\n"
"v_accvgpr_read_b32 v122,a166\n"
"v_mfma_f32_16x16x32_bf16 v[156:159],v[44:47],a[4:7],v[156:159]\n"
"buffer_load_dwordx4 v112,s[24:27],s76 offen offset:3072 sc1 lds\n"
"v_mfma_f32_16x16x32_bf16 v[156:159],v[40:43],a[0:3],v[156:159]\n"
"s_xor_b32 s76,s76,49152\n"
"v_accvgpr_read_b32 v123,a167\n"
"s_waitcnt vmcnt(27) lgkmcnt(0)\n"
"s_barrier\n"
"v_mfma_f32_16x16x32_bf16 v[128:131],v[48:51],v[0:3],v[128:131]\n"
"ds_read_b64_tr_b16 v[32:33],v98 offset:0\n"
"ds_read_b64_tr_b16 v[40:41],v99 offset:0\n"
"v_mfma_f32_16x16x32_bf16 v[128:131],v[52:55],v[4:7],v[128:131]\n"
"ds_read_b64_tr_b16 v[34:35],v98 offset:1024\n"
"ds_read_b64_tr_b16 v[42:43],v99 offset:1024\n"
"v_mfma_f32_16x16x32_bf16 v[160:163],v[52:55],v[12:15],v[160:163]\n"
"ds_read_b64_tr_b16 v[36:37],v98 offset:8192\n"
"ds_read_b64_tr_b16 v[44:45],v99 offset:8192\n"
"v_mfma_f32_16x16x32_bf16 v[160:163],v[48:51],v[8:11],v[160:163]\n"
"s_add_u32 m0,s85,131072\n"
"ds_read_b64_tr_b16 v[38:39],v98 offset:9216\n"
"ds_read_b64_tr_b16 v[46:47],v99 offset:9216\n"
"v_mfma_f32_16x16x32_bf16 v[192:195],v[48:51],v[16:19],v[192:195]\n"
"buffer_load_dwordx4 v110,s[28:31],s86 offen lds\n"
"v_mfma_f32_16x16x32_bf16 v[192:195],v[52:55],v[20:23],v[192:195]\n"
"v_cvt_pk_bf16_f32 v116,v116,v117\n"
"v_cvt_pk_bf16_f32 v117,v118,v119\n"
"v_mfma_f32_16x16x32_bf16 v[224:227],v[52:55],v[28:31],v[224:227]\n"
"s_add_u32 s56,s56,4096\n"
"v_cvt_pk_bf16_f32 v118,v120,v121\n"
"v_cvt_pk_bf16_f32 v119,v122,v123\n"
"v_mfma_f32_16x16x32_bf16 v[224:227],v[48:51],v[24:27],v[224:227]\n"
"buffer_store_dwordx4 v[116:119],v112,s[24:27],s56 offen offset:0 sc1\n"
"v_mfma_f32_16x16x32_bf16 v[228:231],v[56:59],v[24:27],v[228:231]\n"
"ds_read_b64_tr_b16 v[48:49],v100 offset:0\n"
"v_accvgpr_read_b32 v116,a168\n"
"v_mfma_f32_16x16x32_bf16 v[228:231],v[60:63],v[28:31],v[228:231]\n"
"ds_read_b64_tr_b16 v[50:51],v100 offset:1024\n"
"v_accvgpr_read_b32 v117,a169\n"
"v_mfma_f32_16x16x32_bf16 v[196:199],v[60:63],v[20:23],v[196:199]\n"
"ds_read_b64_tr_b16 v[52:53],v100 offset:8192\n"
"v_accvgpr_read_b32 v118,a170\n"
"v_mfma_f32_16x16x32_bf16 v[196:199],v[56:59],v[16:19],v[196:199]\n"
"s_add_u32 m0,s85,132096\n"
"ds_read_b64_tr_b16 v[54:55],v100 offset:9216\n"
"v_mfma_f32_16x16x32_bf16 v[164:167],v[56:59],v[8:11],v[164:167]\n"
"buffer_load_dwordx4 v111,s[28:31],s86 offen lds\n"
"v_mfma_f32_16x16x32_bf16 v[164:167],v[60:63],v[12:15],v[164:167]\n"
"s_add_u32 m0,s85,139264\n"
"s_xor_b32 s86,s86,262144\n"
"v_mfma_f32_16x16x32_bf16 v[132:135],v[60:63],v[4:7],v[132:135]\n"
"buffer_load_dwordx4 v110,s[28:31],s86 offen lds\n"
"v_mfma_f32_16x16x32_bf16 v[132:135],v[56:59],v[0:3],v[132:135]\n"
"v_accvgpr_read_b32 v119,a171\n"
"v_accvgpr_read_b32 v120,a172\n"
"v_mfma_f32_16x16x32_bf16 v[136:139],v[64:67],v[0:3],v[136:139]\n"
"ds_read_b64_tr_b16 v[56:57],v101 offset:0\n"
"v_accvgpr_read_b32 v121,a173\n"
"v_mfma_f32_16x16x32_bf16 v[136:139],v[68:71],v[4:7],v[136:139]\n"
"ds_read_b64_tr_b16 v[58:59],v101 offset:1024\n"
"v_accvgpr_read_b32 v122,a174\n"
"v_mfma_f32_16x16x32_bf16 v[168:171],v[68:71],v[12:15],v[168:171]\n"
"ds_read_b64_tr_b16 v[60:61],v101 offset:8192\n"
"v_accvgpr_read_b32 v123,a175\n"
"v_mfma_f32_16x16x32_bf16 v[168:171],v[64:67],v[8:11],v[168:171]\n"
"s_add_u32 m0,s85,140288\n"
"ds_read_b64_tr_b16 v[62:63],v101 offset:9216\n"
"v_mfma_f32_16x16x32_bf16 v[200:203],v[64:67],v[16:19],v[200:203]\n"
"buffer_load_dwordx4 v111,s[28:31],s86 offen lds\n"
"v_mfma_f32_16x16x32_bf16 v[200:203],v[68:71],v[20:23],v[200:203]\n"
"s_xor_b32 s86,s86,8126464\n"
"v_cvt_pk_bf16_f32 v116,v116,v117\n"
"v_cvt_pk_bf16_f32 v117,v118,v119\n"
"v_mfma_f32_16x16x32_bf16 v[232:235],v[68:71],v[28:31],v[232:235]\n"
"v_cvt_pk_bf16_f32 v118,v120,v121\n"
"v_cvt_pk_bf16_f32 v119,v122,v123\n"
"v_mfma_f32_16x16x32_bf16 v[232:235],v[64:67],v[24:27],v[232:235]\n"
"buffer_store_dwordx4 v[116:119],v112,s[24:27],s56 offen offset:1024 sc1\n"
"v_mfma_f32_16x16x32_bf16 v[236:239],v[72:75],v[24:27],v[236:239]\n"
"ds_read_b64_tr_b16 v[64:65],v102 offset:0\n"
"v_accvgpr_read_b32 v116,a176\n"
"v_mfma_f32_16x16x32_bf16 v[236:239],v[76:79],v[28:31],v[236:239]\n"
"ds_read_b64_tr_b16 v[66:67],v102 offset:1024\n"
"v_accvgpr_read_b32 v117,a177\n"
"v_mfma_f32_16x16x32_bf16 v[204:207],v[76:79],v[20:23],v[204:207]\n"
"ds_read_b64_tr_b16 v[68:69],v102 offset:8192\n"
"v_accvgpr_read_b32 v118,a178\n"
"v_mfma_f32_16x16x32_bf16 v[204:207],v[72:75],v[16:19],v[204:207]\n"
"s_add_u32 m0,s78,0\n"
"ds_read_b64_tr_b16 v[70:71],v102 offset:9216\n"
"v_accvgpr_read_b32 v121,a181\n"
"v_mfma_f32_16x16x32_bf16 v[172:175],v[72:75],v[8:11],v[172:175]\n"
"buffer_load_dwordx4 v112,s[24:27],s76 offen offset:0 sc1 lds\n"
"v_mfma_f32_16x16x32_bf16 v[172:175],v[76:79],v[12:15],v[172:175]\n"
"v_accvgpr_read_b32 v119,a179\n"
"v_accvgpr_read_b32 v120,a180\n"
"v_mfma_f32_16x16x32_bf16 v[140:143],v[76:79],v[4:7],v[140:143]\n"
"buffer_load_dwordx4 v112,s[24:27],s76 offen offset:1024 sc1 lds\n"
"v_mfma_f32_16x16x32_bf16 v[140:143],v[72:75],v[0:3],v[140:143]\n"
"v_accvgpr_read_b32 v122,a182\n"
"s_waitcnt vmcnt(24)\n"
"v_mfma_f32_16x16x32_bf16 v[144:147],v[80:83],v[0:3],v[144:147]\n"
"ds_read_b64_tr_b16 v[72:73],v103 offset:0\n"
"v_accvgpr_read_b32 v123,a183\n"
"v_mfma_f32_16x16x32_bf16 v[144:147],v[84:87],v[4:7],v[144:147]\n"
"ds_read_b64_tr_b16 v[74:75],v103 offset:1024\n"
"v_cvt_pk_bf16_f32 v116,v116,v117\n"
"v_mfma_f32_16x16x32_bf16 v[176:179],v[84:87],v[12:15],v[176:179]\n"
"ds_read_b64_tr_b16 v[76:77],v103 offset:8192\n"
"v_cvt_pk_bf16_f32 v117,v118,v119\n"
"v_mfma_f32_16x16x32_bf16 v[176:179],v[80:83],v[8:11],v[176:179]\n"
"ds_read_b64_tr_b16 v[78:79],v103 offset:9216\n"
"v_cvt_pk_bf16_f32 v118,v120,v121\n"
"v_mfma_f32_16x16x32_bf16 v[208:211],v[80:83],v[16:19],v[208:211]\n"
"buffer_load_dwordx4 v112,s[24:27],s76 offen offset:2048 sc1 lds\n"
"v_mfma_f32_16x16x32_bf16 v[208:211],v[84:87],v[20:23],v[208:211]\n"
"ds_read_b128 a[0:3],v96 offset:32768\n"
"v_cvt_pk_bf16_f32 v119,v122,v123\n"
"v_mfma_f32_16x16x32_bf16 v[240:243],v[84:87],v[28:31],v[240:243]\n"
"buffer_store_dwordx4 v[116:119],v112,s[24:27],s56 offen offset:2048 sc1\n"
"v_mfma_f32_16x16x32_bf16 v[240:243],v[80:83],v[24:27],v[240:243]\n"
"ds_read_b128 a[8:11],v96 offset:33792\n"
"v_accvgpr_read_b32 v116,a184\n"
"v_accvgpr_read_b32 v117,a185\n"
"v_mfma_f32_16x16x32_bf16 v[244:247],v[88:91],v[24:27],v[244:247]\n"
"ds_read_b64_tr_b16 v[80:81],v104 offset:0\n"
"v_accvgpr_read_b32 v118,a186\n"
"v_mfma_f32_16x16x32_bf16 v[244:247],v[92:95],v[28:31],v[244:247]\n"
"ds_read_b64_tr_b16 v[82:83],v104 offset:1024\n"
"v_accvgpr_read_b32 v119,a187\n"
"v_mfma_f32_16x16x32_bf16 v[212:215],v[92:95],v[20:23],v[212:215]\n"
"ds_read_b64_tr_b16 v[84:85],v104 offset:8192\n"
"v_accvgpr_read_b32 v120,a188\n"
"v_mfma_f32_16x16x32_bf16 v[212:215],v[88:91],v[16:19],v[212:215]\n"
"ds_read_b64_tr_b16 v[86:87],v104 offset:9216\n"
"v_accvgpr_read_b32 v121,a189\n"
"v_mfma_f32_16x16x32_bf16 v[180:183],v[88:91],v[8:11],v[180:183]\n"
"buffer_load_dwordx4 v112,s[24:27],s76 offen offset:3072 sc1 lds\n"
"v_mfma_f32_16x16x32_bf16 v[180:183],v[92:95],v[12:15],v[180:183]\n"
"s_add_u32 m0,s78,16384\n"
"s_xor_b32 s76,s76,16384\n"
"ds_read_b128 a[16:19],v96 offset:34816\n"
"v_mfma_f32_16x16x32_bf16 v[148:151],v[92:95],v[4:7],v[148:151]\n"
"buffer_load_dwordx4 v112,s[24:27],s76 offen offset:0 sc1 lds\n"
"v_mfma_f32_16x16x32_bf16 v[148:151],v[88:91],v[0:3],v[148:151]\n"
"ds_read_b128 a[24:27],v96 offset:35840\n"
"v_accvgpr_read_b32 v122,a190\n"
"v_mfma_f32_16x16x32_bf16 v[152:155],a[32:35],v[0:3],v[152:155]\n"
"ds_read_b64_tr_b16 v[88:89],v105 offset:0\n"
"v_accvgpr_read_b32 v123,a191\n"
"v_mfma_f32_16x16x32_bf16 v[152:155],a[36:39],v[4:7],v[152:155]\n"
"ds_read_b64_tr_b16 v[90:91],v105 offset:1024\n"
"v_cvt_pk_bf16_f32 v116,v116,v117\n"
"v_mfma_f32_16x16x32_bf16 v[184:187],a[36:39],v[12:15],v[184:187]\n"
"ds_read_b64_tr_b16 v[92:93],v105 offset:8192\n"
"v_cvt_pk_bf16_f32 v117,v118,v119\n"
"v_mfma_f32_16x16x32_bf16 v[184:187],a[32:35],v[8:11],v[184:187]\n"
"ds_read_b64_tr_b16 v[94:95],v105 offset:9216\n"
"v_cvt_pk_bf16_f32 v118,v120,v121\n"
"v_mfma_f32_16x16x32_bf16 v[216:219],a[32:35],v[16:19],v[216:219]\n"
"ds_read_b128 a[4:7],v96 offset:49152\n"
"v_cvt_pk_bf16_f32 v119,v122,v123\n"
"v_mfma_f32_16x16x32_bf16 v[216:219],a[36:39],v[20:23],v[216:219]\n"
"ds_read_b128 a[12:15],v96 offset:50176\n"
"buffer_store_dwordx4 v[116:119],v112,s[24:27],s56 offen offset:3072 sc1\n"
"v_mfma_f32_16x16x32_bf16 v[248:251],a[36:39],v[28:31],v[248:251]\n"
"ds_read_b128 a[20:23],v96 offset:51200\n"
"v_accvgpr_read_b32 v116,a248\n"
"v_mfma_f32_16x16x32_bf16 v[248:251],a[32:35],v[24:27],v[248:251]\n"
"ds_read_b128 a[28:31],v96 offset:52224\n"
"v_accvgpr_read_b32 v117,a249\n"
"v_mfma_f32_16x16x32_bf16 v[252:255],a[40:43],v[24:27],v[252:255]\n"
"buffer_load_dwordx4 v112,s[24:27],s76 offen offset:1024 sc1 lds\n"
"v_mfma_f32_16x16x32_bf16 v[252:255],a[44:47],v[28:31],v[252:255]\n"
"v_accvgpr_read_b32 v118,a250\n"
"v_accvgpr_read_b32 v119,a251\n"
"v_mfma_f32_16x16x32_bf16 v[220:223],a[44:47],v[20:23],v[220:223]\n"
"v_accvgpr_read_b32 v120,a252\n"
"v_accvgpr_read_b32 v121,a253\n"
"v_mfma_f32_16x16x32_bf16 v[220:223],a[40:43],v[16:19],v[220:223]\n"
"buffer_load_dwordx4 v112,s[24:27],s76 offen offset:2048 sc1 lds\n"
"v_mfma_f32_16x16x32_bf16 v[188:191],a[40:43],v[8:11],v[188:191]\n"
"v_accvgpr_read_b32 v122,a254\n"
"v_accvgpr_read_b32 v123,a255\n"
"v_mfma_f32_16x16x32_bf16 v[188:191],a[44:47],v[12:15],v[188:191]\n"
"v_cvt_pk_bf16_f32 v116,v116,v117\n"
"v_cvt_pk_bf16_f32 v117,v118,v119\n"
"v_mfma_f32_16x16x32_bf16 v[156:159],a[44:47],v[4:7],v[156:159]\n"
"buffer_load_dwordx4 v112,s[24:27],s76 offen offset:3072 sc1 lds\n"
"v_mfma_f32_16x16x32_bf16 v[156:159],a[40:43],v[0:3],v[156:159]\n"
"s_xor_b32 s76,s76,507904\n"
"v_cvt_pk_bf16_f32 v118,v120,v121\n"
"s_waitcnt vmcnt(27) lgkmcnt(0)\n"
"s_barrier\n"
"v_mfma_f32_16x16x32_bf16 v[128:131],v[32:35],a[0:3],v[128:131]\n"
"ds_read_b64_tr_b16 a[32:33],v98 offset:49152\n"
"ds_read_b64_tr_b16 a[40:41],v99 offset:49152\n"
"v_mfma_f32_16x16x32_bf16 v[128:131],v[36:39],a[4:7],v[128:131]\n"
"ds_read_b64_tr_b16 a[34:35],v98 offset:50176\n"
"ds_read_b64_tr_b16 a[42:43],v99 offset:50176\n"
"v_mfma_f32_16x16x32_bf16 v[160:163],v[36:39],a[12:15],v[160:163]\n"
"ds_read_b64_tr_b16 a[36:37],v98 offset:57344\n"
"ds_read_b64_tr_b16 a[44:45],v99 offset:57344\n"
"v_mfma_f32_16x16x32_bf16 v[160:163],v[32:35],a[8:11],v[160:163]\n"
"s_add_u32 m0,s85,98304\n"
"ds_read_b64_tr_b16 a[38:39],v98 offset:58368\n"
"ds_read_b64_tr_b16 a[46:47],v99 offset:58368\n"
"v_mfma_f32_16x16x32_bf16 v[192:195],v[32:35],a[16:19],v[192:195]\n"
"buffer_load_dwordx4 v110,s[28:31],s86 offen lds\n"
"v_mfma_f32_16x16x32_bf16 v[192:195],v[36:39],a[20:23],v[192:195]\n"
"v_cvt_pk_bf16_f32 v119,v122,v123\n"
"v_accvgpr_write_b32 a252,v116\n"
"v_mfma_f32_16x16x32_bf16 v[224:227],v[36:39],a[28:31],v[224:227]\n"
"v_accvgpr_write_b32 a253,v117\n"
"v_accvgpr_write_b32 a254,v118\n"
"v_mfma_f32_16x16x32_bf16 v[224:227],v[32:35],a[24:27],v[224:227]\n"
"v_accvgpr_write_b32 a255,v119\n"
"v_accvgpr_read_b32 v116,a240\n"
"v_mfma_f32_16x16x32_bf16 v[228:231],v[40:43],a[24:27],v[228:231]\n"
"ds_read_b64_tr_b16 v[32:33],v100 offset:49152\n"
"v_accvgpr_read_b32 v117,a241\n"
"v_mfma_f32_16x16x32_bf16 v[228:231],v[44:47],a[28:31],v[228:231]\n"
"ds_read_b64_tr_b16 v[34:35],v100 offset:50176\n"
"v_accvgpr_read_b32 v118,a242\n"
"v_mfma_f32_16x16x32_bf16 v[196:199],v[44:47],a[20:23],v[196:199]\n"
"ds_read_b64_tr_b16 v[36:37],v100 offset:57344\n"
"v_accvgpr_read_b32 v119,a243\n"
"v_mfma_f32_16x16x32_bf16 v[196:199],v[40:43],a[16:19],v[196:199]\n"
"s_add_u32 m0,s85,99328\n"
"ds_read_b64_tr_b16 v[38:39],v100 offset:58368\n"
"v_mfma_f32_16x16x32_bf16 v[164:167],v[40:43],a[8:11],v[164:167]\n"
"buffer_load_dwordx4 v111,s[28:31],s86 offen lds\n"
"v_mfma_f32_16x16x32_bf16 v[164:167],v[44:47],a[12:15],v[164:167]\n"
"s_add_u32 m0,s85,106496\n"
"s_xor_b32 s86,s86,262144\n"
"v_mfma_f32_16x16x32_bf16 v[132:135],v[44:47],a[4:7],v[132:135]\n"
"buffer_load_dwordx4 v110,s[28:31],s86 offen lds\n"
"v_mfma_f32_16x16x32_bf16 v[132:135],v[40:43],a[0:3],v[132:135]\n"
"v_accvgpr_read_b32 v120,a244\n"
"v_accvgpr_read_b32 v121,a245\n"
"v_mfma_f32_16x16x32_bf16 v[136:139],v[48:51],a[0:3],v[136:139]\n"
"ds_read_b64_tr_b16 v[40:41],v101 offset:49152\n"
"v_accvgpr_read_b32 v122,a246\n"
"v_mfma_f32_16x16x32_bf16 v[136:139],v[52:55],a[4:7],v[136:139]\n"
"ds_read_b64_tr_b16 v[42:43],v101 offset:50176\n"
"v_accvgpr_read_b32 v123,a247\n"
"v_mfma_f32_16x16x32_bf16 v[168:171],v[52:55],a[12:15],v[168:171]\n"
"ds_read_b64_tr_b16 v[44:45],v101 offset:57344\n"
"v_cvt_pk_bf16_f32 v116,v116,v117\n"
"v_mfma_f32_16x16x32_bf16 v[168:171],v[48:51],a[8:11],v[168:171]\n"
"s_add_u32 m0,s85,107520\n"
"ds_read_b64_tr_b16 v[46:47],v101 offset:58368\n"
"v_mfma_f32_16x16x32_bf16 v[200:203],v[48:51],a[16:19],v[200:203]\n"
"buffer_load_dwordx4 v111,s[28:31],s86 offen lds\n"
"v_mfma_f32_16x16x32_bf16 v[200:203],v[52:55],a[20:23],v[200:203]\n"
"s_xor_b32 s86,s86,786432\n"
"v_cvt_pk_bf16_f32 v117,v118,v119\n"
"v_mfma_f32_16x16x32_bf16 v[232:235],v[52:55],a[28:31],v[232:235]\n"
"v_cvt_pk_bf16_f32 v118,v120,v121\n"
"v_cvt_pk_bf16_f32 v119,v122,v123\n"
"v_mfma_f32_16x16x32_bf16 v[232:235],v[48:51],a[24:27],v[232:235]\n"
"v_accvgpr_write_b32 a248,v116\n"
"v_accvgpr_write_b32 a249,v117\n"
"v_mfma_f32_16x16x32_bf16 v[236:239],v[56:59],a[24:27],v[236:239]\n"
"ds_read_b64_tr_b16 v[48:49],v102 offset:49152\n"
"v_accvgpr_write_b32 a250,v118\n"
"v_mfma_f32_16x16x32_bf16 v[236:239],v[60:63],a[28:31],v[236:239]\n"
"ds_read_b64_tr_b16 v[50:51],v102 offset:50176\n"
"v_accvgpr_write_b32 a251,v119\n"
"v_mfma_f32_16x16x32_bf16 v[204:207],v[60:63],a[20:23],v[204:207]\n"
"ds_read_b64_tr_b16 v[52:53],v102 offset:57344\n"
"v_accvgpr_read_b32 v116,a232\n"
"v_mfma_f32_16x16x32_bf16 v[204:207],v[56:59],a[16:19],v[204:207]\n"
"s_add_u32 m0,s78,32768\n"
"ds_read_b64_tr_b16 v[54:55],v102 offset:58368\n"
"v_accvgpr_read_b32 v119,a235\n"
"v_mfma_f32_16x16x32_bf16 v[172:175],v[56:59],a[8:11],v[172:175]\n"
"buffer_load_dwordx4 v112,s[24:27],s76 offen offset:0 sc1 lds\n"
"v_mfma_f32_16x16x32_bf16 v[172:175],v[60:63],a[12:15],v[172:175]\n"
"v_accvgpr_read_b32 v117,a233\n"
"v_accvgpr_read_b32 v118,a234\n"
"v_mfma_f32_16x16x32_bf16 v[140:143],v[60:63],a[4:7],v[140:143]\n"
"buffer_load_dwordx4 v112,s[24:27],s76 offen offset:1024 sc1 lds\n"
"v_mfma_f32_16x16x32_bf16 v[140:143],v[56:59],a[0:3],v[140:143]\n"
"v_accvgpr_read_b32 v120,a236\n"
"s_waitcnt vmcnt(22)\n"
"v_mfma_f32_16x16x32_bf16 v[144:147],v[64:67],a[0:3],v[144:147]\n"
"ds_read_b64_tr_b16 v[56:57],v103 offset:49152\n"
"v_accvgpr_read_b32 v121,a237\n"
"v_mfma_f32_16x16x32_bf16 v[144:147],v[68:71],a[4:7],v[144:147]\n"
"ds_read_b64_tr_b16 v[58:59],v103 offset:50176\n"
"v_accvgpr_read_b32 v122,a238\n"
"v_mfma_f32_16x16x32_bf16 v[176:179],v[68:71],a[12:15],v[176:179]\n"
"ds_read_b64_tr_b16 v[60:61],v103 offset:57344\n"
"v_accvgpr_read_b32 v123,a239\n"
"v_mfma_f32_16x16x32_bf16 v[176:179],v[64:67],a[8:11],v[176:179]\n"
"ds_read_b64_tr_b16 v[62:63],v103 offset:58368\n"
"v_cvt_pk_bf16_f32 v116,v116,v117\n"
"v_mfma_f32_16x16x32_bf16 v[208:211],v[64:67],a[16:19],v[208:211]\n"
"buffer_load_dwordx4 v112,s[24:27],s76 offen offset:2048 sc1 lds\n"
"v_mfma_f32_16x16x32_bf16 v[208:211],v[68:71],a[20:23],v[208:211]\n"
"ds_read_b128 v[0:3],v97 offset:0\n"
"v_cvt_pk_bf16_f32 v117,v118,v119\n"
"v_mfma_f32_16x16x32_bf16 v[240:243],v[68:71],a[28:31],v[240:243]\n"
"v_cvt_pk_bf16_f32 v118,v120,v121\n"
"v_cvt_pk_bf16_f32 v119,v122,v123\n"
"v_mfma_f32_16x16x32_bf16 v[240:243],v[64:67],a[24:27],v[240:243]\n"
"ds_read_b128 v[8:11],v97 offset:1024\n"
"v_accvgpr_write_b32 a244,v116\n"
"v_mfma_f32_16x16x32_bf16 v[244:247],v[72:75],a[24:27],v[244:247]\n"
"ds_read_b64_tr_b16 v[64:65],v104 offset:49152\n"
"v_accvgpr_write_b32 a245,v117\n"
"v_mfma_f32_16x16x32_bf16 v[244:247],v[76:79],a[28:31],v[244:247]\n"
"ds_read_b64_tr_b16 v[66:67],v104 offset:50176\n"
"v_accvgpr_write_b32 a246,v118\n"
"v_mfma_f32_16x16x32_bf16 v[212:215],v[76:79],a[20:23],v[212:215]\n"
"ds_read_b64_tr_b16 v[68:69],v104 offset:57344\n"
"v_accvgpr_write_b32 a247,v119\n"
"v_mfma_f32_16x16x32_bf16 v[212:215],v[72:75],a[16:19],v[212:215]\n"
"ds_read_b64_tr_b16 v[70:71],v104 offset:58368\n"
"v_accvgpr_read_b32 v116,a224\n"
"v_mfma_f32_16x16x32_bf16 v[180:183],v[72:75],a[8:11],v[180:183]\n"
"buffer_load_dwordx4 v112,s[24:27],s76 offen offset:3072 sc1 lds\n"
"v_mfma_f32_16x16x32_bf16 v[180:183],v[76:79],a[12:15],v[180:183]\n"
"s_add_u32 m0,s78,49152\n"
"s_xor_b32 s76,s76,16384\n"
"ds_read_b128 v[16:19],v97 offset:2048\n"
"v_mfma_f32_16x16x32_bf16 v[148:151],v[76:79],a[4:7],v[148:151]\n"
"buffer_load_dwordx4 v112,s[24:27],s76 offen offset:0 sc1 lds\n"
"v_mfma_f32_16x16x32_bf16 v[148:151],v[72:75],a[0:3],v[148:151]\n"
"ds_read_b128 v[24:27],v97 offset:3072\n"
"v_accvgpr_read_b32 v117,a225\n"
"v_mfma_f32_16x16x32_bf16 v[152:155],v[80:83],a[0:3],v[152:155]\n"
"ds_read_b64_tr_b16 v[72:73],v105 offset:49152\n"
"v_accvgpr_read_b32 v118,a226\n"
"v_mfma_f32_16x16x32_bf16 v[152:155],v[84:87],a[4:7],v[152:155]\n"
"ds_read_b64_tr_b16 v[74:75],v105 offset:50176\n"
"v_accvgpr_read_b32 v119,a227\n"
"v_mfma_f32_16x16x32_bf16 v[184:187],v[84:87],a[12:15],v[184:187]\n"
"ds_read_b64_tr_b16 v[76:77],v105 offset:57344\n"
"v_accvgpr_read_b32 v120,a228\n"
"v_mfma_f32_16x16x32_bf16 v[184:187],v[80:83],a[8:11],v[184:187]\n"
"ds_read_b64_tr_b16 v[78:79],v105 offset:58368\n"
"v_accvgpr_read_b32 v121,a229\n"
"v_mfma_f32_16x16x32_bf16 v[216:219],v[80:83],a[16:19],v[216:219]\n"
"ds_read_b128 v[4:7],v97 offset:16384\n"
"v_accvgpr_read_b32 v122,a230\n"
"v_mfma_f32_16x16x32_bf16 v[216:219],v[84:87],a[20:23],v[216:219]\n"
"ds_read_b128 v[12:15],v97 offset:17408\n"
"v_accvgpr_read_b32 v123,a231\n"
"v_mfma_f32_16x16x32_bf16 v[248:251],v[84:87],a[28:31],v[248:251]\n"
"ds_read_b128 v[20:23],v97 offset:18432\n"
"v_cvt_pk_bf16_f32 v116,v116,v117\n"
"v_mfma_f32_16x16x32_bf16 v[248:251],v[80:83],a[24:27],v[248:251]\n"
"ds_read_b128 v[28:31],v97 offset:19456\n"
"v_cvt_pk_bf16_f32 v117,v118,v119\n"
"v_mfma_f32_16x16x32_bf16 v[252:255],v[88:91],a[24:27],v[252:255]\n"
"buffer_load_dwordx4 v112,s[24:27],s76 offen offset:1024 sc1 lds\n"
"v_mfma_f32_16x16x32_bf16 v[252:255],v[92:95],a[28:31],v[252:255]\n"
"v_cvt_pk_bf16_f32 v118,v120,v121\n"
"v_cvt_pk_bf16_f32 v119,v122,v123\n"
"v_mfma_f32_16x16x32_bf16 v[220:223],v[92:95],a[20:23],v[220:223]\n"
"v_accvgpr_write_b32 a240,v116\n"
"v_accvgpr_write_b32 a241,v117\n"
"v_mfma_f32_16x16x32_bf16 v[220:223],v[88:91],a[16:19],v[220:223]\n"
"buffer_load_dwordx4 v112,s[24:27],s76 offen offset:2048 sc1 lds\n"
"v_mfma_f32_16x16x32_bf16 v[188:191],v[88:91],a[8:11],v[188:191]\n"
"v_accvgpr_write_b32 a242,v118\n"
"v_accvgpr_write_b32 a243,v119\n"
"v_mfma_f32_16x16x32_bf16 v[188:191],v[92:95],a[12:15],v[188:191]\n"
"v_accvgpr_read_b32 v116,a216\n"
"v_accvgpr_read_b32 v117,a217\n"
"v_mfma_f32_16x16x32_bf16 v[156:159],v[92:95],a[4:7],v[156:159]\n"
"buffer_load_dwordx4 v112,s[24:27],s76 offen offset:3072 sc1 lds\n"
"v_mfma_f32_16x16x32_bf16 v[156:159],v[88:91],a[0:3],v[156:159]\n"
"s_xor_b32 s76,s76,49152\n"
"v_accvgpr_read_b32 v118,a218\n"
"s_waitcnt vmcnt(23) lgkmcnt(0)\n"
"s_barrier\n"
"v_mfma_f32_16x16x32_bf16 v[128:131],a[32:35],v[0:3],v[128:131]\n"
"ds_read_b64_tr_b16 v[80:81],v98 offset:32768\n"
"ds_read_b64_tr_b16 v[88:89],v99 offset:32768\n"
"v_mfma_f32_16x16x32_bf16 v[128:131],a[36:39],v[4:7],v[128:131]\n"
"ds_read_b64_tr_b16 v[82:83],v98 offset:33792\n"
"ds_read_b64_tr_b16 v[90:91],v99 offset:33792\n"
"v_mfma_f32_16x16x32_bf16 v[160:163],a[36:39],v[12:15],v[160:163]\n"
"ds_read_b64_tr_b16 v[84:85],v98 offset:40960\n"
"ds_read_b64_tr_b16 v[92:93],v99 offset:40960\n"
"v_mfma_f32_16x16x32_bf16 v[160:163],a[32:35],v[8:11],v[160:163]\n"
"s_add_u32 m0,s85,147456\n"
"ds_read_b64_tr_b16 v[86:87],v98 offset:41984\n"
"ds_read_b64_tr_b16 v[94:95],v99 offset:41984\n"
"v_mfma_f32_16x16x32_bf16 v[192:195],a[32:35],v[16:19],v[192:195]\n"
"buffer_load_dwordx4 v110,s[28:31],s86 offen lds\n"
"v_mfma_f32_16x16x32_bf16 v[192:195],a[36:39],v[20:23],v[192:195]\n"
"v_accvgpr_read_b32 v119,a219\n"
"v_accvgpr_read_b32 v120,a220\n"
"v_mfma_f32_16x16x32_bf16 v[224:227],a[36:39],v[28:31],v[224:227]\n"
"v_accvgpr_read_b32 v121,a221\n"
"v_accvgpr_read_b32 v122,a222\n"
"v_mfma_f32_16x16x32_bf16 v[224:227],a[32:35],v[24:27],v[224:227]\n"
"v_accvgpr_read_b32 v123,a223\n"
"v_cvt_pk_bf16_f32 v116,v116,v117\n"
"v_mfma_f32_16x16x32_bf16 v[228:231],a[40:43],v[24:27],v[228:231]\n"
"ds_read_b64_tr_b16 a[32:33],v100 offset:32768\n"
"v_cvt_pk_bf16_f32 v117,v118,v119\n"
"v_mfma_f32_16x16x32_bf16 v[228:231],a[44:47],v[28:31],v[228:231]\n"
"ds_read_b64_tr_b16 a[34:35],v100 offset:33792\n"
"v_cvt_pk_bf16_f32 v118,v120,v121\n"
"v_mfma_f32_16x16x32_bf16 v[196:199],a[44:47],v[20:23],v[196:199]\n"
"ds_read_b64_tr_b16 a[36:37],v100 offset:40960\n"
"v_cvt_pk_bf16_f32 v119,v122,v123\n"
"v_mfma_f32_16x16x32_bf16 v[196:199],a[40:43],v[16:19],v[196:199]\n"
"s_add_u32 m0,s85,148480\n"
"ds_read_b64_tr_b16 a[38:39],v100 offset:41984\n"
"v_mfma_f32_16x16x32_bf16 v[164:167],a[40:43],v[8:11],v[164:167]\n"
"buffer_load_dwordx4 v111,s[28:31],s86 offen lds\n"
"v_mfma_f32_16x16x32_bf16 v[164:167],a[44:47],v[12:15],v[164:167]\n"
"s_add_u32 m0,s85,155648\n"
"s_xor_b32 s86,s86,262144\n"
"v_mfma_f32_16x16x32_bf16 v[132:135],a[44:47],v[4:7],v[132:135]\n"
"buffer_load_dwordx4 v110,s[28:31],s86 offen lds\n"
"v_mfma_f32_16x16x32_bf16 v[132:135],a[40:43],v[0:3],v[132:135]\n"
"v_accvgpr_write_b32 a236,v116\n"
"v_accvgpr_write_b32 a237,v117\n"
"v_mfma_f32_16x16x32_bf16 v[136:139],v[32:35],v[0:3],v[136:139]\n"
"ds_read_b64_tr_b16 a[40:41],v101 offset:32768\n"
"v_accvgpr_write_b32 a238,v118\n"
"v_mfma_f32_16x16x32_bf16 v[136:139],v[36:39],v[4:7],v[136:139]\n"
"ds_read_b64_tr_b16 a[42:43],v101 offset:33792\n"
"v_accvgpr_write_b32 a239,v119\n"
"v_mfma_f32_16x16x32_bf16 v[168:171],v[36:39],v[12:15],v[168:171]\n"
"ds_read_b64_tr_b16 a[44:45],v101 offset:40960\n"
"v_accvgpr_read_b32 v116,a208\n"
"v_mfma_f32_16x16x32_bf16 v[168:171],v[32:35],v[8:11],v[168:171]\n"
"s_add_u32 m0,s85,156672\n"
"ds_read_b64_tr_b16 a[46:47],v101 offset:41984\n"
"v_mfma_f32_16x16x32_bf16 v[200:203],v[32:35],v[16:19],v[200:203]\n"
"buffer_load_dwordx4 v111,s[28:31],s86 offen lds\n"
"v_mfma_f32_16x16x32_bf16 v[200:203],v[36:39],v[20:23],v[200:203]\n"
"s_xor_b32 s86,s86,1835008\n"
"v_accvgpr_read_b32 v117,a209\n"
"v_mfma_f32_16x16x32_bf16 v[232:235],v[36:39],v[28:31],v[232:235]\n"
"v_accvgpr_read_b32 v118,a210\n"
"v_accvgpr_read_b32 v119,a211\n"
"v_mfma_f32_16x16x32_bf16 v[232:235],v[32:35],v[24:27],v[232:235]\n"
"v_accvgpr_read_b32 v120,a212\n"
"v_accvgpr_read_b32 v121,a213\n"
"v_mfma_f32_16x16x32_bf16 v[236:239],v[40:43],v[24:27],v[236:239]\n"
"ds_read_b64_tr_b16 v[32:33],v102 offset:32768\n"
"v_accvgpr_read_b32 v122,a214\n"
"v_mfma_f32_16x16x32_bf16 v[236:239],v[44:47],v[28:31],v[236:239]\n"
"ds_read_b64_tr_b16 v[34:35],v102 offset:33792\n"
"v_accvgpr_read_b32 v123,a215\n"
"v_mfma_f32_16x16x32_bf16 v[204:207],v[44:47],v[20:23],v[204:207]\n"
"ds_read_b64_tr_b16 v[36:37],v102 offset:40960\n"
"v_cvt_pk_bf16_f32 v116,v116,v117\n"
"v_mfma_f32_16x16x32_bf16 v[204:207],v[40:43],v[16:19],v[204:207]\n"
"s_add_u32 m0,s78,65536\n"
"ds_read_b64_tr_b16 v[38:39],v102 offset:41984\n"
"v_accvgpr_write_b32 a232,v116\n"
"v_mfma_f32_16x16x32_bf16 v[172:175],v[40:43],v[8:11],v[172:175]\n"
"buffer_load_dwordx4 v112,s[24:27],s76 offen offset:0 sc1 lds\n"
"v_mfma_f32_16x16x32_bf16 v[172:175],v[44:47],v[12:15],v[172:175]\n"
"v_cvt_pk_bf16_f32 v117,v118,v119\n"
"v_cvt_pk_bf16_f32 v118,v120,v121\n"
"v_mfma_f32_16x16x32_bf16 v[140:143],v[44:47],v[4:7],v[140:143]\n"
"buffer_load_dwordx4 v112,s[24:27],s76 offen offset:1024 sc1 lds\n"
"v_mfma_f32_16x16x32_bf16 v[140:143],v[40:43],v[0:3],v[140:143]\n"
"v_cvt_pk_bf16_f32 v119,v122,v123\n"
"s_waitcnt vmcnt(18)\n"
"v_mfma_f32_16x16x32_bf16 v[144:147],v[48:51],v[0:3],v[144:147]\n"
"ds_read_b64_tr_b16 v[40:41],v103 offset:32768\n"
"v_accvgpr_write_b32 a233,v117\n"
"v_lshrrev_b32 v106,8,v112\n"
"v_mfma_f32_16x16x32_bf16 v[144:147],v[52:55],v[4:7],v[144:147]\n"
"ds_read_b64_tr_b16 v[42:43],v103 offset:33792\n"
"v_accvgpr_write_b32 a234,v118\n"
"v_lshlrev_b32 v107,6,v106\n"
"v_mfma_f32_16x16x32_bf16 v[176:179],v[52:55],v[12:15],v[176:179]\n"
"ds_read_b64_tr_b16 v[44:45],v103 offset:40960\n"
"v_accvgpr_write_b32 a235,v119\n"
"v_and_b32 v108,0xf0,v112\n"
"v_mfma_f32_16x16x32_bf16 v[176:179],v[48:51],v[8:11],v[176:179]\n"
"ds_read_b64_tr_b16 v[46:47],v103 offset:41984\n"
"v_accvgpr_read_b32 v116,a200\n"
"v_xor_b32 v107,v107,v108\n"
"v_mfma_f32_16x16x32_bf16 v[208:211],v[48:51],v[16:19],v[208:211]\n"
"buffer_load_dwordx4 v112,s[24:27],s76 offen offset:2048 sc1 lds\n"
"v_lshl_add_u32 v106,v106,11,v107\n"
"v_mfma_f32_16x16x32_bf16 v[208:211],v[52:55],v[20:23],v[208:211]\n"
"ds_read_b128 a[0:3],v96 offset:0\n"
"v_accvgpr_read_b32 v117,a201\n"
"v_lshrrev_b32 v107,6,v112\n"
"v_mfma_f32_16x16x32_bf16 v[240:243],v[52:55],v[28:31],v[240:243]\n"
"v_accvgpr_read_b32 v118,a202\n"
"v_accvgpr_read_b32 v119,a203\n"
"v_and_b32 v108,3,v107\n"
"v_mfma_f32_16x16x32_bf16 v[240:243],v[48:51],v[24:27],v[240:243]\n"
"ds_read_b128 a[8:11],v96 offset:1024\n"
"v_accvgpr_read_b32 v120,a204\n"
"v_lshlrev_b32 v108,6,v108\n"
"v_mfma_f32_16x16x32_bf16 v[244:247],v[56:59],v[24:27],v[244:247]\n"
"ds_read_b64_tr_b16 v[48:49],v104 offset:32768\n"
"v_accvgpr_read_b32 v121,a205\n"
"v_mfma_f32_16x16x32_bf16 v[244:247],v[60:63],v[28:31],v[244:247]\n"
"s_lshl_b32 s82,s52,6\n"
"ds_read_b64_tr_b16 v[50:51],v104 offset:33792\n"
"v_accvgpr_read_b32 v122,a206\n"
"v_xor_b32 v108,s82,v108\n"
"v_mfma_f32_16x16x32_bf16 v[212:215],v[60:63],v[20:23],v[212:215]\n"
"ds_read_b64_tr_b16 v[52:53],v104 offset:40960\n"
"v_accvgpr_read_b32 v123,a207\n"
"v_lshl_add_u32 v107,v107,8,v108\n"
"v_mfma_f32_16x16x32_bf16 v[212:215],v[56:59],v[16:19],v[212:215]\n"
"ds_read_b64_tr_b16 v[54:55],v104 offset:41984\n"
"v_cvt_pk_bf16_f32 v116,v116,v117\n"
"v_and_b32 v108,0x30,v112\n"
"v_mfma_f32_16x16x32_bf16 v[180:183],v[56:59],v[8:11],v[180:183]\n"
"buffer_load_dwordx4 v112,s[24:27],s76 offen offset:3072 sc1 lds\n"
"v_add_u32 v107,v107,v108\n"
"v_mfma_f32_16x16x32_bf16 v[180:183],v[60:63],v[12:15],v[180:183]\n"
"s_add_u32 m0,s78,81920\n"
"s_xor_b32 s76,s76,16384\n"
"ds_read_b128 a[16:19],v96 offset:2048\n"
"v_mfma_f32_16x16x32_bf16 v[148:151],v[60:63],v[4:7],v[148:151]\n"
"buffer_load_dwordx4 v112,s[24:27],s76 offen offset:0 sc1 lds\n"
"v_add_u32 v107,114688,v107\n"
"v_mfma_f32_16x16x32_bf16 v[148:151],v[56:59],v[0:3],v[148:151]\n"
"ds_read_b128 a[24:27],v96 offset:3072\n"
"v_cvt_pk_bf16_f32 v117,v118,v119\n"
"v_mfma_f32_16x16x32_bf16 v[152:155],v[64:67],v[0:3],v[152:155]\n"
"ds_read_b64_tr_b16 v[56:57],v105 offset:32768\n"
"v_cvt_pk_bf16_f32 v118,v120,v121\n"
"v_mfma_f32_16x16x32_bf16 v[152:155],v[68:71],v[4:7],v[152:155]\n"
"ds_read_b64_tr_b16 v[58:59],v105 offset:33792\n"
"v_cvt_pk_bf16_f32 v119,v122,v123\n"
"v_mfma_f32_16x16x32_bf16 v[184:187],v[68:71],v[12:15],v[184:187]\n"
"ds_read_b64_tr_b16 v[60:61],v105 offset:40960\n"
"v_accvgpr_write_b32 a228,v116\n"
"v_mfma_f32_16x16x32_bf16 v[184:187],v[64:67],v[8:11],v[184:187]\n"
"ds_read_b64_tr_b16 v[62:63],v105 offset:41984\n"
"v_accvgpr_write_b32 a229,v117\n"
"v_mfma_f32_16x16x32_bf16 v[216:219],v[64:67],v[16:19],v[216:219]\n"
"ds_read_b128 a[4:7],v96 offset:16384\n"
"v_accvgpr_write_b32 a230,v118\n"
"v_mfma_f32_16x16x32_bf16 v[216:219],v[68:71],v[20:23],v[216:219]\n"
"ds_read_b128 a[12:15],v96 offset:17408\n"
"v_accvgpr_write_b32 a231,v119\n"
"v_mfma_f32_16x16x32_bf16 v[248:251],v[68:71],v[28:31],v[248:251]\n"
"ds_read_b128 a[20:23],v96 offset:18432\n"
"v_accvgpr_read_b32 v116,a192\n"
"v_mfma_f32_16x16x32_bf16 v[248:251],v[64:67],v[24:27],v[248:251]\n"
"ds_read_b128 a[28:31],v96 offset:19456\n"
"v_accvgpr_read_b32 v117,a193\n"
"v_mfma_f32_16x16x32_bf16 v[252:255],v[72:75],v[24:27],v[252:255]\n"
"buffer_load_dwordx4 v112,s[24:27],s76 offen offset:1024 sc1 lds\n"
"v_mfma_f32_16x16x32_bf16 v[252:255],v[76:79],v[28:31],v[252:255]\n"
"v_accvgpr_read_b32 v118,a194\n"
"v_accvgpr_read_b32 v119,a195\n"
"v_mfma_f32_16x16x32_bf16 v[220:223],v[76:79],v[20:23],v[220:223]\n"
"v_accvgpr_read_b32 v120,a196\n"
"v_accvgpr_read_b32 v121,a197\n"
"v_mfma_f32_16x16x32_bf16 v[220:223],v[72:75],v[16:19],v[220:223]\n"
"buffer_load_dwordx4 v112,s[24:27],s76 offen offset:2048 sc1 lds\n"
"v_mfma_f32_16x16x32_bf16 v[188:191],v[72:75],v[8:11],v[188:191]\n"
"v_accvgpr_read_b32 v122,a198\n"
"v_accvgpr_read_b32 v123,a199\n"
"v_mfma_f32_16x16x32_bf16 v[188:191],v[76:79],v[12:15],v[188:191]\n"
"v_cvt_pk_bf16_f32 v116,v116,v117\n"
"v_cvt_pk_bf16_f32 v117,v118,v119\n"
"v_mfma_f32_16x16x32_bf16 v[156:159],v[76:79],v[4:7],v[156:159]\n"
"buffer_load_dwordx4 v112,s[24:27],s76 offen offset:3072 sc1 lds\n"
"v_mfma_f32_16x16x32_bf16 v[156:159],v[72:75],v[0:3],v[156:159]\n"
"s_xor_b32 s76,s76,114688\n"
"v_cvt_pk_bf16_f32 v118,v120,v121\n"
"s_waitcnt vmcnt(20) lgkmcnt(0)\n"
"s_barrier\n"
"v_mfma_f32_16x16x32_bf16 v[128:131],v[80:83],a[0:3],v[128:131]\n"
"ds_read_b64_tr_b16 v[64:65],v98 offset:0\n"
"ds_read_b64_tr_b16 v[72:73],v99 offset:0\n"
"v_mfma_f32_16x16x32_bf16 v[128:131],v[84:87],a[4:7],v[128:131]\n"
"ds_read_b64_tr_b16 v[66:67],v98 offset:1024\n"
"ds_read_b64_tr_b16 v[74:75],v99 offset:1024\n"
"v_mfma_f32_16x16x32_bf16 v[160:163],v[84:87],a[12:15],v[160:163]\n"
"ds_read_b64_tr_b16 v[68:69],v98 offset:8192\n"
"ds_read_b64_tr_b16 v[76:77],v99 offset:8192\n"
"v_mfma_f32_16x16x32_bf16 v[160:163],v[80:83],a[8:11],v[160:163]\n"
"s_add_u32 m0,s85,131072\n"
"ds_read_b64_tr_b16 v[70:71],v98 offset:9216\n"
"ds_read_b64_tr_b16 v[78:79],v99 offset:9216\n"
"v_mfma_f32_16x16x32_bf16 v[192:195],v[80:83],a[16:19],v[192:195]\n"
"buffer_load_dwordx4 v110,s[28:31],s86 offen lds\n"
"v_mfma_f32_16x16x32_bf16 v[192:195],v[84:87],a[20:23],v[192:195]\n"
"s_add_u32 m0,s85,132096\n"
"v_cvt_pk_bf16_f32 v119,v122,v123\n"
"v_mfma_f32_16x16x32_bf16 v[224:227],v[84:87],a[28:31],v[224:227]\n"
"buffer_load_dwordx4 v111,s[28:31],s86 offen lds\n"
"v_mfma_f32_16x16x32_bf16 v[224:227],v[80:83],a[24:27],v[224:227]\n"
"s_xor_b32 s86,s86,262144\n"
"v_accvgpr_write_b32 a224,v116\n"
"v_mfma_f32_16x16x32_bf16 v[228:231],v[88:91],a[24:27],v[228:231]\n"
"ds_read_b64_tr_b16 v[80:81],v100 offset:0\n"
"v_accvgpr_write_b32 a225,v117\n"
"v_mfma_f32_16x16x32_bf16 v[228:231],v[92:95],a[28:31],v[228:231]\n"
"ds_read_b64_tr_b16 v[82:83],v100 offset:1024\n"
"v_accvgpr_write_b32 a226,v118\n"
"v_mfma_f32_16x16x32_bf16 v[196:199],v[92:95],a[20:23],v[196:199]\n"
"ds_read_b64_tr_b16 v[84:85],v100 offset:8192\n"
"v_accvgpr_write_b32 a227,v119\n"
"v_mfma_f32_16x16x32_bf16 v[196:199],v[88:91],a[16:19],v[196:199]\n"
"s_add_u32 m0,s85,139264\n"
"ds_read_b64_tr_b16 v[86:87],v100 offset:9216\n"
"v_mfma_f32_16x16x32_bf16 v[164:167],v[88:91],a[8:11],v[164:167]\n"
"buffer_load_dwordx4 v110,s[28:31],s86 offen lds\n"
"v_mfma_f32_16x16x32_bf16 v[164:167],v[92:95],a[12:15],v[164:167]\n"
"s_add_u32 m0,s85,140288\n"
"s_add_u32 s56,s56,126976\n"
"v_mfma_f32_16x16x32_bf16 v[132:135],v[92:95],a[4:7],v[132:135]\n"
"buffer_load_dwordx4 v111,s[28:31],s86 offen lds\n"
"v_mfma_f32_16x16x32_bf16 v[132:135],v[88:91],a[0:3],v[132:135]\n"
"s_xor_b32 s86,s86,786432\n"
"v_mfma_f32_16x16x32_bf16 v[136:139],a[32:35],a[0:3],v[136:139]\n"
"ds_read_b64_tr_b16 v[88:89],v101 offset:0\n"
"v_mfma_f32_16x16x32_bf16 v[136:139],a[36:39],a[4:7],v[136:139]\n"
"ds_read_b64_tr_b16 v[90:91],v101 offset:1024\n"
"v_mfma_f32_16x16x32_bf16 v[168:171],a[36:39],a[12:15],v[168:171]\n"
"ds_read_b64_tr_b16 v[92:93],v101 offset:8192\n"
"v_mfma_f32_16x16x32_bf16 v[168:171],a[32:35],a[8:11],v[168:171]\n"
"s_add_u32 m0,s78,0\n"
"ds_read_b64_tr_b16 v[94:95],v101 offset:9216\n"
"v_mfma_f32_16x16x32_bf16 v[200:203],a[32:35],a[16:19],v[200:203]\n"
"buffer_load_dwordx4 v112,s[24:27],s76 offen offset:0 sc1 lds\n"
"v_mfma_f32_16x16x32_bf16 v[200:203],a[36:39],a[20:23],v[200:203]\n"
"v_mfma_f32_16x16x32_bf16 v[232:235],a[36:39],a[28:31],v[232:235]\n"
"buffer_load_dwordx4 v112,s[24:27],s76 offen offset:1024 sc1 lds\n"
"v_mfma_f32_16x16x32_bf16 v[232:235],a[32:35],a[24:27],v[232:235]\n"
"v_mfma_f32_16x16x32_bf16 v[236:239],a[40:43],a[24:27],v[236:239]\n"
"ds_read_b64_tr_b16 a[32:33],v102 offset:0\n"
"v_mfma_f32_16x16x32_bf16 v[236:239],a[44:47],a[28:31],v[236:239]\n"
"ds_read_b64_tr_b16 a[34:35],v102 offset:1024\n"
"v_mfma_f32_16x16x32_bf16 v[204:207],a[44:47],a[20:23],v[204:207]\n"
"ds_read_b64_tr_b16 a[36:37],v102 offset:8192\n"
"v_mfma_f32_16x16x32_bf16 v[204:207],a[40:43],a[16:19],v[204:207]\n"
"ds_read_b64_tr_b16 a[38:39],v102 offset:9216\n"
"v_mfma_f32_16x16x32_bf16 v[172:175],a[40:43],a[8:11],v[172:175]\n"
"buffer_load_dwordx4 v112,s[24:27],s76 offen offset:2048 sc1 lds\n"
"v_mfma_f32_16x16x32_bf16 v[172:175],a[44:47],a[12:15],v[172:175]\n"
"v_mfma_f32_16x16x32_bf16 v[140:143],a[44:47],a[4:7],v[140:143]\n"
"buffer_load_dwordx4 v112,s[24:27],s76 offen offset:3072 sc1 lds\n"
"v_mfma_f32_16x16x32_bf16 v[140:143],a[40:43],a[0:3],v[140:143]\n"
"s_xor_b32 s76,s76,16384\n"
"s_waitcnt vmcnt(20)\n"
"v_mfma_f32_16x16x32_bf16 v[144:147],v[32:35],a[0:3],v[144:147]\n"
"ds_read_b64_tr_b16 a[40:41],v103 offset:0\n"
"v_mfma_f32_16x16x32_bf16 v[144:147],v[36:39],a[4:7],v[144:147]\n"
"ds_read_b64_tr_b16 a[42:43],v103 offset:1024\n"
"v_mfma_f32_16x16x32_bf16 v[176:179],v[36:39],a[12:15],v[176:179]\n"
"ds_read_b64_tr_b16 a[44:45],v103 offset:8192\n"
"v_mfma_f32_16x16x32_bf16 v[176:179],v[32:35],a[8:11],v[176:179]\n"
"s_add_u32 m0,s78,16384\n"
"ds_read_b64_tr_b16 a[46:47],v103 offset:9216\n"
"v_mfma_f32_16x16x32_bf16 v[208:211],v[32:35],a[16:19],v[208:211]\n"
"buffer_load_dwordx4 v112,s[24:27],s76 offen offset:0 sc1 lds\n"
"v_mfma_f32_16x16x32_bf16 v[208:211],v[36:39],a[20:23],v[208:211]\n"
"ds_read_b128 v[0:3],v96 offset:32768\n"
"v_mfma_f32_16x16x32_bf16 v[240:243],v[36:39],a[28:31],v[240:243]\n"
"buffer_load_dwordx4 v112,s[24:27],s76 offen offset:1024 sc1 lds\n"
"v_mfma_f32_16x16x32_bf16 v[240:243],v[32:35],a[24:27],v[240:243]\n"
"ds_read_b128 v[8:11],v96 offset:33792\n"
"v_mfma_f32_16x16x32_bf16 v[244:247],v[40:43],a[24:27],v[244:247]\n"
"ds_read_b64_tr_b16 v[32:33],v104 offset:0\n"
"v_mfma_f32_16x16x32_bf16 v[244:247],v[44:47],a[28:31],v[244:247]\n"
"ds_read_b64_tr_b16 v[34:35],v104 offset:1024\n"
"v_mfma_f32_16x16x32_bf16 v[212:215],v[44:47],a[20:23],v[212:215]\n"
"ds_read_b64_tr_b16 v[36:37],v104 offset:8192\n"
"v_mfma_f32_16x16x32_bf16 v[212:215],v[40:43],a[16:19],v[212:215]\n"
"ds_read_b64_tr_b16 v[38:39],v104 offset:9216\n"
"v_mfma_f32_16x16x32_bf16 v[180:183],v[40:43],a[8:11],v[180:183]\n"
"buffer_load_dwordx4 v112,s[24:27],s76 offen offset:2048 sc1 lds\n"
"v_mfma_f32_16x16x32_bf16 v[180:183],v[44:47],a[12:15],v[180:183]\n"
"ds_read_b128 v[16:19],v96 offset:34816\n"
"v_mfma_f32_16x16x32_bf16 v[148:151],v[44:47],a[4:7],v[148:151]\n"
"buffer_load_dwordx4 v112,s[24:27],s76 offen offset:3072 sc1 lds\n"
"v_mfma_f32_16x16x32_bf16 v[148:151],v[40:43],a[0:3],v[148:151]\n"
"s_xor_b32 s76,s76,49152\n"
"ds_read_b128 v[24:27],v96 offset:35840\n"
"v_mfma_f32_16x16x32_bf16 v[152:155],v[48:51],a[0:3],v[152:155]\n"
"ds_read_b64_tr_b16 v[40:41],v105 offset:0\n"
"v_mfma_f32_16x16x32_bf16 v[152:155],v[52:55],a[4:7],v[152:155]\n"
"ds_read_b64_tr_b16 v[42:43],v105 offset:1024\n"
"v_mfma_f32_16x16x32_bf16 v[184:187],v[52:55],a[12:15],v[184:187]\n"
"ds_read_b64_tr_b16 v[44:45],v105 offset:8192\n"
"v_mfma_f32_16x16x32_bf16 v[184:187],v[48:51],a[8:11],v[184:187]\n"
"ds_read_b64_tr_b16 v[46:47],v105 offset:9216\n"
"v_mfma_f32_16x16x32_bf16 v[216:219],v[48:51],a[16:19],v[216:219]\n"
"ds_read_b128 v[4:7],v96 offset:49152\n"
"v_mfma_f32_16x16x32_bf16 v[216:219],v[52:55],a[20:23],v[216:219]\n"
"ds_read_b128 v[12:15],v96 offset:50176\n"
"ds_read_b128 v[20:23],v96 offset:51200\n"
"v_mfma_f32_16x16x32_bf16 v[248:251],v[52:55],a[28:31],v[248:251]\n"
"buffer_store_dwordx2 v[124:125],off,s[24:27],s59 sc1\n"
"v_mfma_f32_16x16x32_bf16 v[248:251],v[48:51],a[24:27],v[248:251]\n"
"s_add_u32 m0,s78,114688\n"
"ds_read_b128 v[28:31],v96 offset:52224\n"
"v_mfma_f32_16x16x32_bf16 v[252:255],v[56:59],a[24:27],v[252:255]\n"
"buffer_load_dwordx4 v106,s[4:7],s13 offen offset:0 nt lds\n"
"v_mfma_f32_16x16x32_bf16 v[252:255],v[60:63],a[28:31],v[252:255]\n"
"s_add_u32 s13,s13,7168\n"
"v_mfma_f32_16x16x32_bf16 v[220:223],v[60:63],a[20:23],v[220:223]\n"
"buffer_load_dwordx4 v106,s[4:7],s13 offen offset:1024 nt lds\n"
"v_mfma_f32_16x16x32_bf16 v[220:223],v[56:59],a[16:19],v[220:223]\n"
"s_add_u32 s13,s13,7168\n"
"v_mfma_f32_16x16x32_bf16 v[188:191],v[56:59],a[8:11],v[188:191]\n"
"buffer_load_dwordx4 v106,s[4:7],s13 offen offset:2048 nt lds\n"
"v_mfma_f32_16x16x32_bf16 v[188:191],v[60:63],a[12:15],v[188:191]\n"
"s_add_u32 s13,s13,7168\n"
"v_mfma_f32_16x16x32_bf16 v[156:159],v[60:63],a[4:7],v[156:159]\n"
"buffer_load_dwordx4 v106,s[4:7],s13 offen offset:3072 nt lds\n"
"v_mfma_f32_16x16x32_bf16 v[156:159],v[56:59],a[0:3],v[156:159]\n"
"s_add_u32 s13,s13,109568\n"
"s_waitcnt vmcnt(25) lgkmcnt(0)\n"
"s_barrier\n"
"v_mfma_f32_16x16x32_bf16 v[128:131],v[64:67],v[0:3],v[128:131]\n"
"ds_read_b64_tr_b16 v[48:49],v98 offset:49152\n"
"ds_read_b64_tr_b16 v[56:57],v99 offset:49152\n"
"v_mfma_f32_16x16x32_bf16 v[128:131],v[68:71],v[4:7],v[128:131]\n"
"ds_read_b64_tr_b16 v[50:51],v98 offset:50176\n"
"ds_read_b64_tr_b16 v[58:59],v99 offset:50176\n"
"v_mfma_f32_16x16x32_bf16 v[160:163],v[68:71],v[12:15],v[160:163]\n"
"ds_read_b64_tr_b16 v[52:53],v98 offset:57344\n"
"ds_read_b64_tr_b16 v[60:61],v99 offset:57344\n"
"v_mfma_f32_16x16x32_bf16 v[160:163],v[64:67],v[8:11],v[160:163]\n"
"s_add_u32 m0,s85,98304\n"
"ds_read_b64_tr_b16 v[54:55],v98 offset:58368\n"
"ds_read_b64_tr_b16 v[62:63],v99 offset:58368\n"
"v_mfma_f32_16x16x32_bf16 v[192:195],v[64:67],v[16:19],v[192:195]\n"
"buffer_load_dwordx4 v110,s[28:31],s86 offen lds\n"
"v_mfma_f32_16x16x32_bf16 v[192:195],v[68:71],v[20:23],v[192:195]\n"
"v_mfma_f32_16x16x32_bf16 v[224:227],v[68:71],v[28:31],v[224:227]\n"
"v_mfma_f32_16x16x32_bf16 v[224:227],v[64:67],v[24:27],v[224:227]\n"
"v_mfma_f32_16x16x32_bf16 v[228:231],v[72:75],v[24:27],v[228:231]\n"
"ds_read_b64_tr_b16 v[64:65],v100 offset:49152\n"
"v_mfma_f32_16x16x32_bf16 v[228:231],v[76:79],v[28:31],v[228:231]\n"
"ds_read_b64_tr_b16 v[66:67],v100 offset:50176\n"
"v_mfma_f32_16x16x32_bf16 v[196:199],v[76:79],v[20:23],v[196:199]\n"
"ds_read_b64_tr_b16 v[68:69],v100 offset:57344\n"
"v_mfma_f32_16x16x32_bf16 v[196:199],v[72:75],v[16:19],v[196:199]\n"
"s_add_u32 m0,s85,99328\n"
"ds_read_b64_tr_b16 v[70:71],v100 offset:58368\n"
"v_mfma_f32_16x16x32_bf16 v[164:167],v[72:75],v[8:11],v[164:167]\n"
"buffer_load_dwordx4 v111,s[28:31],s86 offen lds\n"
"v_mfma_f32_16x16x32_bf16 v[164:167],v[76:79],v[12:15],v[164:167]\n"
"s_add_u32 m0,s85,106496\n"
"s_xor_b32 s86,s86,262144\n"
"v_mfma_f32_16x16x32_bf16 v[132:135],v[76:79],v[4:7],v[132:135]\n"
"buffer_load_dwordx4 v110,s[28:31],s86 offen lds\n"
"v_mfma_f32_16x16x32_bf16 v[132:135],v[72:75],v[0:3],v[132:135]\n"
"v_mfma_f32_16x16x32_bf16 v[136:139],v[80:83],v[0:3],v[136:139]\n"
"ds_read_b64_tr_b16 v[72:73],v101 offset:49152\n"
"v_mfma_f32_16x16x32_bf16 v[136:139],v[84:87],v[4:7],v[136:139]\n"
"ds_read_b64_tr_b16 v[74:75],v101 offset:50176\n"
"v_mfma_f32_16x16x32_bf16 v[168:171],v[84:87],v[12:15],v[168:171]\n"
"ds_read_b64_tr_b16 v[76:77],v101 offset:57344\n"
"v_mfma_f32_16x16x32_bf16 v[168:171],v[80:83],v[8:11],v[168:171]\n"
"s_add_u32 m0,s85,107520\n"
"ds_read_b64_tr_b16 v[78:79],v101 offset:58368\n"
"v_mfma_f32_16x16x32_bf16 v[200:203],v[80:83],v[16:19],v[200:203]\n"
"buffer_load_dwordx4 v111,s[28:31],s86 offen lds\n"
"v_mfma_f32_16x16x32_bf16 v[200:203],v[84:87],v[20:23],v[200:203]\n"
"s_xor_b32 s86,s86,3932160\n"
"v_mfma_f32_16x16x32_bf16 v[232:235],v[84:87],v[28:31],v[232:235]\n"
"v_mfma_f32_16x16x32_bf16 v[232:235],v[80:83],v[24:27],v[232:235]\n"
"v_mfma_f32_16x16x32_bf16 v[236:239],v[88:91],v[24:27],v[236:239]\n"
"ds_read_b64_tr_b16 v[80:81],v102 offset:49152\n"
"v_mfma_f32_16x16x32_bf16 v[236:239],v[92:95],v[28:31],v[236:239]\n"
"ds_read_b64_tr_b16 v[82:83],v102 offset:50176\n"
"v_mfma_f32_16x16x32_bf16 v[204:207],v[92:95],v[20:23],v[204:207]\n"
"ds_read_b64_tr_b16 v[84:85],v102 offset:57344\n"
"v_mfma_f32_16x16x32_bf16 v[204:207],v[88:91],v[16:19],v[204:207]\n"
"s_add_u32 m0,s78,32768\n"
"ds_read_b64_tr_b16 v[86:87],v102 offset:58368\n"
"v_mfma_f32_16x16x32_bf16 v[172:175],v[88:91],v[8:11],v[172:175]\n"
"buffer_load_dwordx4 v112,s[24:27],s76 offen offset:0 sc1 lds\n"
"v_mfma_f32_16x16x32_bf16 v[172:175],v[92:95],v[12:15],v[172:175]\n"
"v_mfma_f32_16x16x32_bf16 v[140:143],v[92:95],v[4:7],v[140:143]\n"
"buffer_load_dwordx4 v112,s[24:27],s76 offen offset:1024 sc1 lds\n"
"v_mfma_f32_16x16x32_bf16 v[140:143],v[88:91],v[0:3],v[140:143]\n"
"s_waitcnt vmcnt(23)\n"
"ds_read_b64_tr_b16 v[88:89],v103 offset:49152\n"
"ds_read_b64_tr_b16 v[90:91],v103 offset:50176\n"
"v_mfma_f32_16x16x32_bf16 v[144:147],a[32:35],v[0:3],v[144:147]\n"
"v_lshrrev_b32 v120,4,v112\n"
"v_and_b32 v121,15,v120\n"
"v_mfma_f32_16x16x32_bf16 v[144:147],a[36:39],v[4:7],v[144:147]\n"
"v_lshrrev_b32 v122,4,v120\n"
"v_lshlrev_b32 v122,2,v122\n"
"v_mfma_f32_16x16x32_bf16 v[176:179],a[36:39],v[12:15],v[176:179]\n"
"ds_read_b64_tr_b16 v[92:93],v103 offset:57344\n"
"v_sub_u32 v121,v121,v122\n"
"v_mfma_f32_16x16x32_bf16 v[176:179],a[32:35],v[8:11],v[176:179]\n"
"s_and_b32 s82,s52,1\n"
"ds_read_b64_tr_b16 v[94:95],v103 offset:58368\n"
"s_lshl_b32 s82,s82,4\n"
"v_add_u32 v121,s82,v121\n"
"v_mfma_f32_16x16x32_bf16 v[208:211],a[32:35],v[16:19],v[208:211]\n"
"buffer_load_dwordx4 v112,s[24:27],s76 offen offset:2048 sc1 lds\n"
"v_cvt_f32_i32 v121,v121\n"
"v_mfma_f32_16x16x32_bf16 v[208:211],a[36:39],v[20:23],v[208:211]\n"
"ds_read_b128 a[96:99],v97 offset:0\n"
"v_add_f32 v121,0.5,v121\n"
"v_mul_f32 v126,0x7149f2ca,v121\n"
"v_mfma_f32_16x16x32_bf16 v[240:243],a[36:39],v[28:31],v[240:243]\n"
"v_and_b32 v120,31,v120\n"
"v_mov_b32 v121,0x3f803f80\n"
"v_mfma_f32_16x16x32_bf16 v[240:243],a[32:35],v[24:27],v[240:243]\n"
"s_mov_b32 s93,0x3f800000\n"
"ds_read_b128 a[104:107],v97 offset:1024\n"
"v_accvgpr_write_b32 a144,v121\n"
"v_mfma_f32_16x16x32_bf16 v[244:247],a[40:43],v[24:27],v[244:247]\n"
"ds_read_b64_tr_b16 a[128:129],v104 offset:49152\n"
"v_accvgpr_write_b32 a146,v121\n"
"v_mfma_f32_16x16x32_bf16 v[244:247],a[44:47],v[28:31],v[244:247]\n"
"ds_read_b64_tr_b16 a[130:131],v104 offset:50176\n"
"v_accvgpr_write_b32 a147,v121\n"
"v_mfma_f32_16x16x32_bf16 v[212:215],a[44:47],v[20:23],v[212:215]\n"
"ds_read_b64_tr_b16 a[132:133],v104 offset:57344\n"
"v_lshlrev_b32 v120,3,v120\n"
"v_mfma_f32_16x16x32_bf16 v[212:215],a[40:43],v[16:19],v[212:215]\n"
"ds_read_b64_tr_b16 a[134:135],v104 offset:58368\n"
"v_accvgpr_write_b32 a145,v121\n"
"v_mfma_f32_16x16x32_bf16 v[180:183],a[40:43],v[8:11],v[180:183]\n"
"buffer_load_dwordx4 v112,s[24:27],s76 offen offset:3072 sc1 lds\n"
"v_mfma_f32_16x16x32_bf16 v[180:183],a[44:47],v[12:15],v[180:183]\n"
"s_add_u32 m0,s78,49152\n"
"s_xor_b32 s76,s76,16384\n"
"ds_read_b128 a[112:115],v97 offset:2048\n"
"v_mfma_f32_16x16x32_bf16 v[148:151],a[44:47],v[4:7],v[148:151]\n"
"buffer_load_dwordx4 v112,s[24:27],s76 offen offset:0 sc1 lds\n"
"v_mfma_f32_16x16x32_bf16 v[148:151],a[40:43],v[0:3],v[148:151]\n"
"ds_read_b128 a[120:123],v97 offset:3072\n"
"v_mfma_f32_16x16x32_bf16 v[152:155],v[32:35],v[0:3],v[152:155]\n"
"ds_read_b64_tr_b16 a[136:137],v105 offset:49152\n"
"v_mfma_f32_16x16x32_bf16 v[152:155],v[36:39],v[4:7],v[152:155]\n"
"ds_read_b64_tr_b16 a[138:139],v105 offset:50176\n"
"v_mfma_f32_16x16x32_bf16 v[184:187],v[36:39],v[12:15],v[184:187]\n"
"ds_read_b64_tr_b16 a[140:141],v105 offset:57344\n"
"v_mfma_f32_16x16x32_bf16 v[184:187],v[32:35],v[8:11],v[184:187]\n"
"ds_read_b64_tr_b16 a[142:143],v105 offset:58368\n"
"v_mfma_f32_16x16x32_bf16 v[216:219],v[32:35],v[16:19],v[216:219]\n"
"ds_read_b128 a[100:103],v97 offset:16384\n"
"v_mfma_f32_16x16x32_bf16 v[216:219],v[36:39],v[20:23],v[216:219]\n"
"ds_read_b128 a[108:111],v97 offset:17408\n"
"v_mfma_f32_16x16x32_bf16 v[248:251],v[36:39],v[28:31],v[248:251]\n"
"ds_read_b128 a[116:119],v97 offset:18432\n"
"v_mfma_f32_16x16x32_bf16 v[248:251],v[32:35],v[24:27],v[248:251]\n"
"ds_read_b128 a[124:127],v97 offset:19456\n"
"v_mfma_f32_16x16x32_bf16 v[252:255],v[40:43],v[24:27],v[252:255]\n"
"buffer_load_dwordx4 v112,s[24:27],s76 offen offset:1024 sc1 lds\n"
"v_mfma_f32_16x16x32_bf16 v[252:255],v[44:47],v[28:31],v[252:255]\n"
"v_mfma_f32_16x16x32_bf16 v[220:223],v[44:47],v[20:23],v[220:223]\n"
"v_mfma_f32_16x16x32_bf16 v[220:223],v[40:43],v[16:19],v[220:223]\n"
"buffer_load_dwordx4 v112,s[24:27],s76 offen offset:2048 sc1 lds\n"
"v_mfma_f32_16x16x32_bf16 v[188:191],v[40:43],v[8:11],v[188:191]\n"
"v_mfma_f32_16x16x32_bf16 v[188:191],v[44:47],v[12:15],v[188:191]\n"
"v_mfma_f32_16x16x32_bf16 v[156:159],v[44:47],v[4:7],v[156:159]\n"
"buffer_load_dwordx4 v112,s[24:27],s76 offen offset:3072 sc1 lds\n"
"v_mfma_f32_16x16x32_bf16 v[156:159],v[40:43],v[0:3],v[156:159]\n"
"s_xor_b32 s76,s76,245760\n"
"s_waitcnt vmcnt(12) lgkmcnt(0)\n"
"s_barrier\n"
"v_mfma_f32_16x16x32_bf16 v[128:131],v[48:51],a[96:99],v[128:131]\n"
"ds_read_b64_tr_b16 v[32:33],v98 offset:32768\n"
"ds_read_b64_tr_b16 v[40:41],v99 offset:32768\n"
"v_mfma_f32_16x16x32_bf16 v[128:131],v[52:55],a[100:103],v[128:131]\n"
"ds_read_b64_tr_b16 v[34:35],v98 offset:33792\n"
"ds_read_b64_tr_b16 v[42:43],v99 offset:33792\n"
"v_mfma_f32_16x16x32_bf16 v[160:163],v[52:55],a[108:111],v[160:163]\n"
"ds_read_b64_tr_b16 v[36:37],v98 offset:40960\n"
"ds_read_b64_tr_b16 v[44:45],v99 offset:40960\n"
"v_mfma_f32_16x16x32_bf16 v[160:163],v[48:51],a[104:107],v[160:163]\n"
"s_add_u32 m0,s85,147456\n"
"ds_read_b64_tr_b16 v[38:39],v98 offset:41984\n"
"ds_read_b64_tr_b16 v[46:47],v99 offset:41984\n"
"v_mfma_f32_16x16x32_bf16 v[192:195],v[48:51],a[112:115],v[192:195]\n"
"buffer_load_dwordx4 v110,s[28:31],s86 offen lds\n"
"ds_read_b64_tr_b16 a[160:161],v107 offset:0\n"
"v_mfma_f32_16x16x32_bf16 v[192:195],v[52:55],a[116:119],v[192:195]\n"
"ds_read_b64_tr_b16 a[162:163],v107 offset:4096\n"
"ds_read_b64_tr_b16 a[164:165],v107 offset:8\n"
"ds_read_b64_tr_b16 a[166:167],v107 offset:4104\n"
"v_mfma_f32_16x16x32_bf16 v[224:227],v[52:55],a[124:127],v[224:227]\n"
"ds_read_b64_tr_b16 a[168:169],v107 offset:8192\n"
"ds_read_b64_tr_b16 a[170:171],v107 offset:12288\n"
"v_mfma_f32_16x16x32_bf16 v[224:227],v[48:51],a[120:123],v[224:227]\n"
"ds_read_b64_tr_b16 a[172:173],v107 offset:8200\n"
"ds_read_b64_tr_b16 a[174:175],v107 offset:12296\n"
"v_mfma_f32_16x16x32_bf16 v[228:231],v[56:59],a[120:123],v[228:231]\n"
"ds_read_b64_tr_b16 v[48:49],v100 offset:32768\n"
"v_mfma_f32_16x16x32_bf16 v[228:231],v[60:63],a[124:127],v[228:231]\n"
"ds_read_b64_tr_b16 v[50:51],v100 offset:33792\n"
"v_mfma_f32_16x16x32_bf16 v[196:199],v[60:63],a[116:119],v[196:199]\n"
"ds_read_b64_tr_b16 v[52:53],v100 offset:40960\n"
"v_mfma_f32_16x16x32_bf16 v[196:199],v[56:59],a[112:115],v[196:199]\n"
"s_add_u32 m0,s85,148480\n"
"ds_read_b64_tr_b16 v[54:55],v100 offset:41984\n"
"v_mfma_f32_16x16x32_bf16 v[164:167],v[56:59],a[104:107],v[164:167]\n"
"buffer_load_dwordx4 v111,s[28:31],s86 offen lds\n"
"v_mfma_f32_16x16x32_bf16 v[164:167],v[60:63],a[108:111],v[164:167]\n"
"s_add_u32 m0,s85,155648\n"
"s_xor_b32 s86,s86,262144\n"
"v_mfma_f32_16x16x32_bf16 v[132:135],v[60:63],a[100:103],v[132:135]\n"
"buffer_load_dwordx4 v110,s[28:31],s86 offen lds\n"
"v_mfma_f32_16x16x32_bf16 v[132:135],v[56:59],a[96:99],v[132:135]\n"
"v_mfma_f32_16x16x32_bf16 v[136:139],v[64:67],a[96:99],v[136:139]\n"
"ds_read_b64_tr_b16 v[56:57],v101 offset:32768\n"
"v_mfma_f32_16x16x32_bf16 v[136:139],v[68:71],a[100:103],v[136:139]\n"
"ds_read_b64_tr_b16 v[58:59],v101 offset:33792\n"
"v_mfma_f32_16x16x32_bf16 v[168:171],v[68:71],a[108:111],v[168:171]\n"
"ds_read_b64_tr_b16 v[60:61],v101 offset:40960\n"
"v_mfma_f32_16x16x32_bf16 v[168:171],v[64:67],a[104:107],v[168:171]\n"
"s_add_u32 m0,s85,156672\n"
"ds_read_b64_tr_b16 v[62:63],v101 offset:41984\n"
"v_mfma_f32_16x16x32_bf16 v[200:203],v[64:67],a[112:115],v[200:203]\n"
"buffer_load_dwordx4 v111,s[28:31],s86 offen lds\n"
"v_mfma_f32_16x16x32_bf16 v[200:203],v[68:71],a[116:119],v[200:203]\n"
"s_xor_b32 s86,s86,786432\n"
"v_mfma_f32_16x16x32_bf16 v[232:235],v[68:71],a[124:127],v[232:235]\n"
"v_mfma_f32_16x16x32_bf16 v[232:235],v[64:67],a[120:123],v[232:235]\n"
"v_mfma_f32_16x16x32_bf16 v[236:239],v[72:75],a[120:123],v[236:239]\n"
"ds_read_b64_tr_b16 v[64:65],v102 offset:32768\n"
"v_mfma_f32_16x16x32_bf16 v[236:239],v[76:79],a[124:127],v[236:239]\n"
"ds_read_b64_tr_b16 v[66:67],v102 offset:33792\n"
"v_mfma_f32_16x16x32_bf16 v[204:207],v[76:79],a[116:119],v[204:207]\n"
"ds_read_b64_tr_b16 v[68:69],v102 offset:40960\n"
"v_mfma_f32_16x16x32_bf16 v[204:207],v[72:75],a[112:115],v[204:207]\n"
"s_add_u32 m0,s78,65536\n"
"ds_read_b64_tr_b16 v[70:71],v102 offset:41984\n"
"v_mfma_f32_16x16x32_bf16 v[172:175],v[72:75],a[104:107],v[172:175]\n"
"buffer_load_dwordx4 v112,s[24:27],s76 offen offset:0 sc1 lds\n"
"v_mfma_f32_16x16x32_bf16 v[172:175],v[76:79],a[108:111],v[172:175]\n"
"v_mfma_f32_16x16x32_bf16 v[140:143],v[76:79],a[100:103],v[140:143]\n"
"buffer_load_dwordx4 v112,s[24:27],s76 offen offset:1024 sc1 lds\n"
"v_mfma_f32_16x16x32_bf16 v[140:143],v[72:75],a[96:99],v[140:143]\n"
"v_mfma_f32_16x16x32_bf16 v[144:147],v[80:83],a[96:99],v[144:147]\n"
"ds_read_b64_tr_b16 v[72:73],v103 offset:32768\n"
"v_accvgpr_write_b32 a156,0\n"
"v_mfma_f32_16x16x32_bf16 v[144:147],v[84:87],a[100:103],v[144:147]\n"
"ds_read_b64_tr_b16 v[74:75],v103 offset:33792\n"
"v_accvgpr_write_b32 a157,0\n"
"v_mfma_f32_16x16x32_bf16 v[176:179],v[84:87],a[108:111],v[176:179]\n"
"ds_read_b64_tr_b16 v[76:77],v103 offset:40960\n"
"v_accvgpr_write_b32 a158,0\n"
"v_mfma_f32_16x16x32_bf16 v[176:179],v[80:83],a[104:107],v[176:179]\n"
"ds_read_b64_tr_b16 v[78:79],v103 offset:41984\n"
"v_accvgpr_write_b32 a159,0\n"
"v_mfma_f32_16x16x32_bf16 v[208:211],v[80:83],a[112:115],v[208:211]\n"
"buffer_load_dwordx4 v112,s[24:27],s76 offen offset:2048 sc1 lds\n"
"v_mfma_f32_16x16x32_bf16 v[208:211],v[84:87],a[116:119],v[208:211]\n"
"ds_read_b128 v[0:3],v96 offset:0\n"
"v_mfma_f32_16x16x32_bf16 v[240:243],v[84:87],a[124:127],v[240:243]\n"
"v_mfma_f32_16x16x32_bf16 v[240:243],v[80:83],a[120:123],v[240:243]\n"
"ds_read_b128 v[8:11],v96 offset:1024\n"
"v_mfma_f32_16x16x32_bf16 v[244:247],v[88:91],a[120:123],v[244:247]\n"
"ds_read_b64_tr_b16 v[80:81],v104 offset:32768\n"
"v_mfma_f32_16x16x32_bf16 v[244:247],v[92:95],a[124:127],v[244:247]\n"
"ds_read_b64_tr_b16 v[82:83],v104 offset:33792\n"
"v_mfma_f32_16x16x32_bf16 v[212:215],v[92:95],a[116:119],v[212:215]\n"
"ds_read_b64_tr_b16 v[84:85],v104 offset:40960\n"
"v_mfma_f32_16x16x32_bf16 v[212:215],v[88:91],a[112:115],v[212:215]\n"
"ds_read_b64_tr_b16 v[86:87],v104 offset:41984\n"
"v_mfma_f32_16x16x32_bf16 v[180:183],v[88:91],a[104:107],v[180:183]\n"
"buffer_load_dwordx4 v112,s[24:27],s76 offen offset:3072 sc1 lds\n"
"v_mfma_f32_16x16x32_bf16 v[180:183],v[92:95],a[108:111],v[180:183]\n"
"s_add_u32 m0,s78,81920\n"
"s_xor_b32 s76,s76,16384\n"
"ds_read_b128 v[16:19],v96 offset:2048\n"
"v_mfma_f32_16x16x32_bf16 v[148:151],v[92:95],a[100:103],v[148:151]\n"
"buffer_load_dwordx4 v112,s[24:27],s76 offen offset:0 sc1 lds\n"
"v_mfma_f32_16x16x32_bf16 v[148:151],v[88:91],a[96:99],v[148:151]\n"
"ds_read_b128 v[24:27],v96 offset:3072\n"
"v_mfma_f32_16x16x32_bf16 v[152:155],a[128:131],a[96:99],v[152:155]\n"
"ds_read_b64_tr_b16 v[88:89],v105 offset:32768\n"
"v_mfma_f32_16x16x32_bf16 v[152:155],a[132:135],a[100:103],v[152:155]\n"
"ds_read_b64_tr_b16 v[90:91],v105 offset:33792\n"
"v_mfma_f32_16x16x32_bf16 v[184:187],a[132:135],a[108:111],v[184:187]\n"
"ds_read_b64_tr_b16 v[92:93],v105 offset:40960\n"
"v_mfma_f32_16x16x32_bf16 v[184:187],a[128:131],a[104:107],v[184:187]\n"
"ds_read_b64_tr_b16 v[94:95],v105 offset:41984\n"
"v_mfma_f32_16x16x32_bf16 v[216:219],a[128:131],a[112:115],v[216:219]\n"
"ds_read_b128 v[4:7],v96 offset:16384\n"
"v_mfma_f32_16x16x32_bf16 v[216:219],a[132:135],a[116:119],v[216:219]\n"
"ds_read_b128 v[12:15],v96 offset:17408\n"
"v_mfma_f32_16x16x32_bf16 v[248:251],a[132:135],a[124:127],v[248:251]\n"
"ds_read_b128 v[20:23],v96 offset:18432\n"
"v_mfma_f32_16x16x32_bf16 v[248:251],a[128:131],a[120:123],v[248:251]\n"
"ds_read_b128 v[28:31],v96 offset:19456\n"
"v_mfma_f32_16x16x32_bf16 v[252:255],a[136:139],a[120:123],v[252:255]\n"
"buffer_load_dwordx4 v112,s[24:27],s76 offen offset:1024 sc1 lds\n"
"v_mfma_f32_16x16x32_bf16 v[252:255],a[140:143],a[124:127],v[252:255]\n"
"v_mfma_f32_16x16x32_bf16 v[220:223],a[140:143],a[116:119],v[220:223]\n"
"v_mfma_f32_16x16x32_bf16 v[220:223],a[136:139],a[112:115],v[220:223]\n"
"buffer_load_dwordx4 v112,s[24:27],s76 offen offset:2048 sc1 lds\n"
"v_mfma_f32_16x16x32_bf16 v[188:191],a[136:139],a[104:107],v[188:191]\n"
"v_mfma_f32_16x16x32_bf16 v[188:191],a[140:143],a[108:111],v[188:191]\n"
"v_mfma_f32_16x16x32_bf16 v[156:159],a[140:143],a[100:103],v[156:159]\n"
"buffer_load_dwordx4 v112,s[24:27],s76 offen offset:3072 sc1 lds\n"
"v_mfma_f32_16x16x32_bf16 v[156:159],a[136:139],a[96:99],v[156:159]\n"
"s_xor_b32 s76,s76,49152\n"
"s_waitcnt vmcnt(20) lgkmcnt(0)\n"
"s_barrier\n"
"v_mfma_f32_16x16x32_bf16 v[128:131],v[32:35],v[0:3],v[128:131]\n"
"ds_read_b64_tr_b16 a[128:129],v98 offset:0\n"
"ds_read_b64_tr_b16 a[136:137],v99 offset:0\n"
"v_mfma_f32_16x16x32_bf16 v[128:131],v[36:39],v[4:7],v[128:131]\n"
"ds_read_b64_tr_b16 a[130:131],v98 offset:1024\n"
"ds_read_b64_tr_b16 a[138:139],v99 offset:1024\n"
"v_mfma_f32_16x16x32_bf16 v[160:163],v[36:39],v[12:15],v[160:163]\n"
"ds_read_b64_tr_b16 a[132:133],v98 offset:8192\n"
"ds_read_b64_tr_b16 a[140:141],v99 offset:8192\n"
"v_mfma_f32_16x16x32_bf16 v[160:163],v[32:35],v[8:11],v[160:163]\n"
"s_add_u32 m0,s85,131072\n"
"ds_read_b64_tr_b16 a[134:135],v98 offset:9216\n"
"ds_read_b64_tr_b16 a[142:143],v99 offset:9216\n"
"v_mfma_f32_16x16x32_bf16 v[192:195],v[32:35],v[16:19],v[192:195]\n"
"buffer_load_dwordx4 v110,s[28:31],s86 offen lds\n"
"v_mfma_f32_16x16x32_bf16 v[192:195],v[36:39],v[20:23],v[192:195]\n"
"s_add_u32 m0,s85,132096\n"
"v_mfma_f32_16x16x32_bf16 v[224:227],v[36:39],v[28:31],v[224:227]\n"
"buffer_load_dwordx4 v111,s[28:31],s86 offen lds\n"
"v_mfma_f32_16x16x32_bf16 v[224:227],v[32:35],v[24:27],v[224:227]\n"
"s_xor_b32 s86,s86,262144\n"
"v_mfma_f32_16x16x32_bf16 v[228:231],v[40:43],v[24:27],v[228:231]\n"
"ds_read_b64_tr_b16 v[32:33],v100 offset:0\n"
"v_mfma_f32_16x16x32_bf16 v[228:231],v[44:47],v[28:31],v[228:231]\n"
"ds_read_b64_tr_b16 v[34:35],v100 offset:1024\n"
"v_mfma_f32_16x16x32_bf16 v[196:199],v[44:47],v[20:23],v[196:199]\n"
"ds_read_b64_tr_b16 v[36:37],v100 offset:8192\n"
"v_mfma_f32_16x16x32_bf16 v[196:199],v[40:43],v[16:19],v[196:199]\n"
"s_add_u32 m0,s85,139264\n"
"ds_read_b64_tr_b16 v[38:39],v100 offset:9216\n"
"v_mfma_f32_16x16x32_bf16 v[164:167],v[40:43],v[8:11],v[164:167]\n"
"buffer_load_dwordx4 v110,s[28:31],s86 offen lds\n"
"v_mfma_f32_16x16x32_bf16 v[164:167],v[44:47],v[12:15],v[164:167]\n"
"s_add_u32 m0,s85,140288\n"
"v_mfma_f32_16x16x32_bf16 v[132:135],v[44:47],v[4:7],v[132:135]\n"
"buffer_load_dwordx4 v111,s[28:31],s86 offen lds\n"
"v_mfma_f32_16x16x32_bf16 v[132:135],v[40:43],v[0:3],v[132:135]\n"
"s_xor_b32 s86,s86,1835008\n"
"v_mfma_f32_16x16x32_bf16 v[136:139],v[48:51],v[0:3],v[136:139]\n"
"ds_read_b64_tr_b16 v[40:41],v101 offset:0\n"
"v_mfma_f32_16x16x32_bf16 v[136:139],v[52:55],v[4:7],v[136:139]\n"
"ds_read_b64_tr_b16 v[42:43],v101 offset:1024\n"
"v_mfma_f32_16x16x32_bf16 v[168:171],v[52:55],v[12:15],v[168:171]\n"
"ds_read_b64_tr_b16 v[44:45],v101 offset:8192\n"
"v_mfma_f32_16x16x32_bf16 v[168:171],v[48:51],v[8:11],v[168:171]\n"
"s_add_u32 m0,s78,0\n"
"ds_read_b64_tr_b16 v[46:47],v101 offset:9216\n"
"v_mfma_f32_16x16x32_bf16 v[200:203],v[48:51],v[16:19],v[200:203]\n"
"buffer_load_dwordx4 v112,s[24:27],s76 offen offset:0 sc1 lds\n"
"v_mfma_f32_16x16x32_bf16 v[200:203],v[52:55],v[20:23],v[200:203]\n"
"v_mfma_f32_16x16x32_bf16 v[232:235],v[52:55],v[28:31],v[232:235]\n"
"buffer_load_dwordx4 v112,s[24:27],s76 offen offset:1024 sc1 lds\n"
"v_mfma_f32_16x16x32_bf16 v[232:235],v[48:51],v[24:27],v[232:235]\n"
"v_mfma_f32_16x16x32_bf16 v[236:239],v[56:59],v[24:27],v[236:239]\n"
"ds_read_b64_tr_b16 v[48:49],v102 offset:0\n"
"v_mfma_f32_16x16x32_bf16 v[236:239],v[60:63],v[28:31],v[236:239]\n"
"ds_read_b64_tr_b16 v[50:51],v102 offset:1024\n"
"v_mfma_f32_16x16x32_bf16 v[204:207],v[60:63],v[20:23],v[204:207]\n"
"ds_read_b64_tr_b16 v[52:53],v102 offset:8192\n"
"v_mfma_f32_16x16x32_bf16 v[204:207],v[56:59],v[16:19],v[204:207]\n"
"ds_read_b64_tr_b16 v[54:55],v102 offset:9216\n"
"v_mfma_f32_16x16x32_bf16 v[172:175],v[56:59],v[8:11],v[172:175]\n"
"buffer_load_dwordx4 v112,s[24:27],s76 offen offset:2048 sc1 lds\n"
"v_mfma_f32_16x16x32_bf16 v[172:175],v[60:63],v[12:15],v[172:175]\n"
"v_mfma_f32_16x16x32_bf16 v[140:143],v[60:63],v[4:7],v[140:143]\n"
"buffer_load_dwordx4 v112,s[24:27],s76 offen offset:3072 sc1 lds\n"
"v_mfma_f32_16x16x32_bf16 v[140:143],v[56:59],v[0:3],v[140:143]\n"
"s_xor_b32 s76,s76,16384\n"
"s_waitcnt vmcnt(20)\n"
"v_mfma_f32_16x16x32_bf16 v[144:147],v[64:67],v[0:3],v[144:147]\n"
"ds_read_b64_tr_b16 v[56:57],v103 offset:0\n"
"v_mfma_f32_16x16x32_bf16 v[144:147],v[68:71],v[4:7],v[144:147]\n"
"ds_read_b64_tr_b16 v[58:59],v103 offset:1024\n"
"v_mfma_f32_16x16x32_bf16 v[176:179],v[68:71],v[12:15],v[176:179]\n"
"ds_read_b64_tr_b16 v[60:61],v103 offset:8192\n"
"v_mfma_f32_16x16x32_bf16 v[176:179],v[64:67],v[8:11],v[176:179]\n"
"s_add_u32 m0,s78,16384\n"
"ds_read_b64_tr_b16 v[62:63],v103 offset:9216\n"
"v_mfma_f32_16x16x32_bf16 v[208:211],v[64:67],v[16:19],v[208:211]\n"
"buffer_load_dwordx4 v112,s[24:27],s76 offen offset:0 sc1 lds\n"
"v_mfma_f32_16x16x32_bf16 v[208:211],v[68:71],v[20:23],v[208:211]\n"
"ds_read_b128 a[96:99],v96 offset:32768\n"
"v_mfma_f32_16x16x32_bf16 v[240:243],v[68:71],v[28:31],v[240:243]\n"
"buffer_load_dwordx4 v112,s[24:27],s76 offen offset:1024 sc1 lds\n"
"v_mfma_f32_16x16x32_bf16 v[240:243],v[64:67],v[24:27],v[240:243]\n"
"ds_read_b128 a[104:107],v96 offset:33792\n"
"v_mfma_f32_16x16x32_bf16 v[244:247],v[72:75],v[24:27],v[244:247]\n"
"ds_read_b64_tr_b16 v[64:65],v104 offset:0\n"
"v_mfma_f32_16x16x32_bf16 v[244:247],v[76:79],v[28:31],v[244:247]\n"
"ds_read_b64_tr_b16 v[66:67],v104 offset:1024\n"
"v_mfma_f32_16x16x32_bf16 v[212:215],v[76:79],v[20:23],v[212:215]\n"
"ds_read_b64_tr_b16 v[68:69],v104 offset:8192\n"
"v_mfma_f32_16x16x32_bf16 v[212:215],v[72:75],v[16:19],v[212:215]\n"
"ds_read_b64_tr_b16 v[70:71],v104 offset:9216\n"
"v_mfma_f32_16x16x32_bf16 v[180:183],v[72:75],v[8:11],v[180:183]\n"
"buffer_load_dwordx4 v112,s[24:27],s76 offen offset:2048 sc1 lds\n"
"v_mfma_f32_16x16x32_bf16 v[180:183],v[76:79],v[12:15],v[180:183]\n"
"ds_read_b128 a[112:115],v96 offset:34816\n"
"v_mfma_f32_16x16x32_bf16 v[148:151],v[76:79],v[4:7],v[148:151]\n"
"buffer_load_dwordx4 v112,s[24:27],s76 offen offset:3072 sc1 lds\n"
"v_mfma_f32_16x16x32_bf16 v[148:151],v[72:75],v[0:3],v[148:151]\n"
"s_xor_b32 s76,s76,114688\n"
"ds_read_b128 a[120:123],v96 offset:35840\n"
"v_mfma_f32_16x16x32_bf16 v[152:155],v[80:83],v[0:3],v[152:155]\n"
"ds_read_b64_tr_b16 v[72:73],v105 offset:0\n"
"v_mfma_f32_16x16x32_bf16 v[152:155],v[84:87],v[4:7],v[152:155]\n"
"ds_read_b64_tr_b16 v[74:75],v105 offset:1024\n"
"v_mfma_f32_16x16x32_bf16 v[184:187],v[84:87],v[12:15],v[184:187]\n"
"ds_read_b64_tr_b16 v[76:77],v105 offset:8192\n"
"v_mfma_f32_16x16x32_bf16 v[184:187],v[80:83],v[8:11],v[184:187]\n"
"ds_read_b64_tr_b16 v[78:79],v105 offset:9216\n"
"v_mfma_f32_16x16x32_bf16 v[216:219],v[80:83],v[16:19],v[216:219]\n"
"ds_read_b128 a[100:103],v96 offset:49152\n"
"v_mfma_f32_16x16x32_bf16 v[216:219],v[84:87],v[20:23],v[216:219]\n"
"ds_read_b128 a[108:111],v96 offset:50176\n"
"ds_read_b128 a[116:119],v96 offset:51200\n"
"v_mfma_f32_16x16x32_bf16 v[248:251],v[84:87],v[28:31],v[248:251]\n"
"s_load_dwordx2 s[32:33],s[0:1],56\n"
"s_load_dwordx2 s[36:37],s[0:1],64\n"
"s_load_dwordx2 s[40:41],s[0:1],72\n"
"v_mfma_f32_16x16x32_bf16 v[248:251],v[80:83],v[24:27],v[248:251]\n"
"s_add_u32 m0,s78,114688\n"
"ds_read_b128 a[124:127],v96 offset:52224\n"
"v_mfma_f32_16x16x32_bf16 v[252:255],v[88:91],v[24:27],v[252:255]\n"
"buffer_load_dwordx4 v106,s[4:7],s13 offen offset:0 nt lds\n"
"v_mfma_f32_16x16x32_bf16 v[252:255],v[92:95],v[28:31],v[252:255]\n"
"s_add_u32 s13,s13,7168\n"
"v_mfma_f32_16x16x32_bf16 v[220:223],v[92:95],v[20:23],v[220:223]\n"
"buffer_load_dwordx4 v106,s[4:7],s13 offen offset:1024 nt lds\n"
"v_mfma_f32_16x16x32_bf16 v[220:223],v[88:91],v[16:19],v[220:223]\n"
"s_add_u32 s13,s13,7168\n"
"v_mfma_f32_16x16x32_bf16 v[188:191],v[88:91],v[8:11],v[188:191]\n"
"buffer_load_dwordx4 v106,s[4:7],s13 offen offset:2048 nt lds\n"
"v_mfma_f32_16x16x32_bf16 v[188:191],v[92:95],v[12:15],v[188:191]\n"
"s_add_u32 s13,s13,7168\n"
"v_mfma_f32_16x16x32_bf16 v[156:159],v[92:95],v[4:7],v[156:159]\n"
"buffer_load_dwordx4 v106,s[4:7],s13 offen offset:3072 nt lds\n"
"v_mfma_f32_16x16x32_bf16 v[156:159],v[88:91],v[0:3],v[156:159]\n"
"s_add_u32 s13,s13,109568\n"
"s_waitcnt vmcnt(24) lgkmcnt(0)\n"
"s_barrier\n"
"v_mfma_f32_16x16x32_bf16 v[128:131],a[128:131],a[96:99],v[128:131]\n"
"ds_read_b64_tr_b16 v[80:81],v98 offset:49152\n"
"ds_read_b64_tr_b16 v[88:89],v99 offset:49152\n"
"v_mfma_f32_16x16x32_bf16 v[128:131],a[132:135],a[100:103],v[128:131]\n"
"ds_read_b64_tr_b16 v[82:83],v98 offset:50176\n"
"ds_read_b64_tr_b16 v[90:91],v99 offset:50176\n"
"v_mfma_f32_16x16x32_bf16 v[160:163],a[132:135],a[108:111],v[160:163]\n"
"ds_read_b64_tr_b16 v[84:85],v98 offset:57344\n"
"ds_read_b64_tr_b16 v[92:93],v99 offset:57344\n"
"v_mfma_f32_16x16x32_bf16 v[160:163],a[128:131],a[104:107],v[160:163]\n"
"s_add_u32 m0,s85,98304\n"
"ds_read_b64_tr_b16 v[86:87],v98 offset:58368\n"
"ds_read_b64_tr_b16 v[94:95],v99 offset:58368\n"
"v_mfma_f32_16x16x32_bf16 v[192:195],a[128:131],a[112:115],v[192:195]\n"
"buffer_load_dwordx4 v110,s[28:31],s86 offen lds\n"
"v_mfma_f32_16x16x32_bf16 v[192:195],a[132:135],a[116:119],v[192:195]\n"
"v_accvgpr_write_b32 a154,v124\n"
"v_accvgpr_write_b32 a155,v125\n"
"v_mfma_f32_16x16x32_bf16 v[224:227],a[132:135],a[124:127],v[224:227]\n"
"v_accvgpr_write_b32 a152,1\n"
"v_lshrrev_b32 v124,2,v112\n"
"v_mfma_f32_16x16x32_bf16 v[224:227],a[128:131],a[120:123],v[224:227]\n"
"v_add_u32 v124,147456,v124\n"
"v_mov_b32 v125,0\n"
"v_mfma_f32_16x16x32_bf16 v[228:231],a[136:139],a[120:123],v[228:231]\n"
"ds_read_b64_tr_b16 a[128:129],v100 offset:49152\n"
"s_mov_b32 s101,0\n"
"v_mfma_f32_16x16x32_bf16 v[228:231],a[140:143],a[124:127],v[228:231]\n"
"ds_read_b64_tr_b16 a[130:131],v100 offset:50176\n"
"v_mfma_f32_16x16x32_bf16 v[196:199],a[140:143],a[116:119],v[196:199]\n"
"ds_read_b64_tr_b16 a[132:133],v100 offset:57344\n"
"v_mfma_f32_16x16x32_bf16 v[196:199],a[136:139],a[112:115],v[196:199]\n"
"s_add_u32 m0,s85,99328\n"
"ds_read_b64_tr_b16 a[134:135],v100 offset:58368\n"
"v_mfma_f32_16x16x32_bf16 v[164:167],a[136:139],a[104:107],v[164:167]\n"
"buffer_load_dwordx4 v111,s[28:31],s86 offen lds\n"
"v_mfma_f32_16x16x32_bf16 v[164:167],a[140:143],a[108:111],v[164:167]\n"
"s_add_u32 m0,s85,106496\n"
"s_xor_b32 s86,s86,262144\n"
"v_mfma_f32_16x16x32_bf16 v[132:135],a[140:143],a[100:103],v[132:135]\n"
"buffer_load_dwordx4 v110,s[28:31],s86 offen lds\n"
"v_mfma_f32_16x16x32_bf16 v[132:135],a[136:139],a[96:99],v[132:135]\n"
"v_mfma_f32_16x16x32_bf16 v[136:139],v[32:35],a[96:99],v[136:139]\n"
"ds_read_b64_tr_b16 a[136:137],v101 offset:49152\n"
"v_mfma_f32_16x16x32_bf16 v[136:139],v[36:39],a[100:103],v[136:139]\n"
"ds_read_b64_tr_b16 a[138:139],v101 offset:50176\n"
"v_mfma_f32_16x16x32_bf16 v[168:171],v[36:39],a[108:111],v[168:171]\n"
"ds_read_b64_tr_b16 a[140:141],v101 offset:57344\n"
"v_mfma_f32_16x16x32_bf16 v[168:171],v[32:35],a[104:107],v[168:171]\n"
"s_add_u32 m0,s85,107520\n"
"ds_read_b64_tr_b16 a[142:143],v101 offset:58368\n"
"v_mfma_f32_16x16x32_bf16 v[200:203],v[32:35],a[112:115],v[200:203]\n"
"buffer_load_dwordx4 v111,s[28:31],s86 offen lds\n"
"v_mfma_f32_16x16x32_bf16 v[200:203],v[36:39],a[116:119],v[200:203]\n"
"s_xor_b32 s86,s86,786432\n"
"v_mfma_f32_16x16x32_bf16 v[232:235],v[36:39],a[124:127],v[232:235]\n"
"v_mfma_f32_16x16x32_bf16 v[232:235],v[32:35],a[120:123],v[232:235]\n"
"v_mfma_f32_16x16x32_bf16 v[236:239],v[40:43],a[120:123],v[236:239]\n"
"ds_read_b64_tr_b16 v[32:33],v102 offset:49152\n"
"v_mfma_f32_16x16x32_bf16 v[236:239],v[44:47],a[124:127],v[236:239]\n"
"ds_read_b64_tr_b16 v[34:35],v102 offset:50176\n"
"v_mfma_f32_16x16x32_bf16 v[204:207],v[44:47],a[116:119],v[204:207]\n"
"ds_read_b64_tr_b16 v[36:37],v102 offset:57344\n"
"v_mfma_f32_16x16x32_bf16 v[204:207],v[40:43],a[112:115],v[204:207]\n"
"s_add_u32 m0,s78,32768\n"
"ds_read_b64_tr_b16 v[38:39],v102 offset:58368\n"
"buffer_load_dwordx4 v112,s[24:27],s76 offen offset:0 sc1 lds\n"
"v_mfma_f32_16x16x32_bf16 v[172:175],v[40:43],a[104:107],v[172:175]\n"
"buffer_load_dwordx2 v[118:119],v120,s[24:27],s63 offen sc1\n"
"v_mfma_f32_16x16x32_bf16 v[172:175],v[44:47],a[108:111],v[172:175]\n"
"v_mfma_f32_16x16x32_bf16 v[140:143],v[44:47],a[100:103],v[140:143]\n"
"buffer_load_dwordx4 v112,s[24:27],s76 offen offset:1024 sc1 lds\n"
"v_mfma_f32_16x16x32_bf16 v[140:143],v[40:43],a[96:99],v[140:143]\n"
"s_waitcnt vmcnt(23)\n"
"v_mfma_f32_16x16x32_bf16 v[144:147],v[48:51],a[96:99],v[144:147]\n"
"ds_read_b64_tr_b16 v[40:41],v103 offset:49152\n"
"s_and_b32 s82,s100,7\n"
"v_mfma_f32_16x16x32_bf16 v[144:147],v[52:55],a[100:103],v[144:147]\n"
"ds_read_b64_tr_b16 v[42:43],v103 offset:50176\n"
"s_mul_i32 s82,s82,28\n"
"v_mfma_f32_16x16x32_bf16 v[176:179],v[52:55],a[108:111],v[176:179]\n"
"ds_read_b64_tr_b16 v[44:45],v103 offset:57344\n"
"s_lshr_b32 s83,s100,3\n"
"v_mfma_f32_16x16x32_bf16 v[176:179],v[48:51],a[104:107],v[176:179]\n"
"ds_read_b64_tr_b16 v[46:47],v103 offset:58368\n"
"s_add_u32 s23,s82,s83\n"
"v_mfma_f32_16x16x32_bf16 v[208:211],v[48:51],a[112:115],v[208:211]\n"
"buffer_load_dwordx4 v112,s[24:27],s76 offen offset:2048 sc1 lds\n"
"v_mfma_f32_16x16x32_bf16 v[208:211],v[52:55],a[116:119],v[208:211]\n"
"s_cmp_lt_u32 s83,28\n"
"s_cselect_b32 s34,1827872,0\n"
"s_cselect_b32 s42,8192,0\n"
"s_cselect_b32 s23,s23,0\n"
"v_mfma_f32_16x16x32_bf16 v[240:243],v[52:55],a[124:127],v[240:243]\n"
"s_mov_b32 s35,0x20000\n"
"ds_read_b128 v[0:3],v97 offset:0\n"
"s_mov_b32 s43,0x20000\n"
"v_mfma_f32_16x16x32_bf16 v[240:243],v[48:51],a[120:123],v[240:243]\n"
"ds_read_b128 v[8:11],v97 offset:1024\n"
"s_lshl2_add_u32 s82,s83,s52\n"
"v_mfma_f32_16x16x32_bf16 v[244:247],v[56:59],a[120:123],v[244:247]\n"
"ds_read_b64_tr_b16 v[48:49],v104 offset:49152\n"
"s_mul_i32 s82,s82,57344\n"
"v_mfma_f32_16x16x32_bf16 v[244:247],v[60:63],a[124:127],v[244:247]\n"
"ds_read_b64_tr_b16 v[50:51],v104 offset:50176\n"
"s_and_b32 s83,s100,7\n"
"v_mfma_f32_16x16x32_bf16 v[212:215],v[60:63],a[116:119],v[212:215]\n"
"ds_read_b64_tr_b16 v[52:53],v104 offset:57344\n"
"s_mul_i32 s83,s83,896\n"
"v_mfma_f32_16x16x32_bf16 v[212:215],v[56:59],a[112:115],v[212:215]\n"
"ds_read_b64_tr_b16 v[54:55],v104 offset:58368\n"
"s_add_u32 s82,s82,s83\n"
"v_mfma_f32_16x16x32_bf16 v[180:183],v[56:59],a[104:107],v[180:183]\n"
"buffer_load_dwordx4 v112,s[24:27],s76 offen offset:3072 sc1 lds\n"
"v_mfma_f32_16x16x32_bf16 v[180:183],v[60:63],a[108:111],v[180:183]\n"
"s_add_u32 m0,s78,49152\n"
"s_xor_b32 s76,s76,16384\n"
"ds_read_b128 v[16:19],v97 offset:2048\n"
"v_mfma_f32_16x16x32_bf16 v[148:151],v[60:63],a[100:103],v[148:151]\n"
"buffer_load_dwordx4 v112,s[24:27],s76 offen offset:0 sc1 lds\n"
"v_mfma_f32_16x16x32_bf16 v[148:151],v[56:59],a[96:99],v[148:151]\n"
"s_add_u32 s44,s32,s82\n"
"ds_read_b128 v[24:27],v97 offset:3072\n"
"s_addc_u32 s45,s33,0\n"
"v_mfma_f32_16x16x32_bf16 v[152:155],v[64:67],a[96:99],v[152:155]\n"
"s_add_u32 s48,s36,s82\n"
"ds_read_b64_tr_b16 v[56:57],v105 offset:49152\n"
"s_addc_u32 s49,s37,0\n"
"v_mfma_f32_16x16x32_bf16 v[152:155],v[68:71],a[100:103],v[152:155]\n"
"ds_read_b64_tr_b16 v[58:59],v105 offset:50176\n"
"s_mov_b32 s46,51072\n"
"v_mfma_f32_16x16x32_bf16 v[184:187],v[68:71],a[108:111],v[184:187]\n"
"ds_read_b64_tr_b16 v[60:61],v105 offset:57344\n"
"s_mov_b32 s50,s46\n"
"v_mfma_f32_16x16x32_bf16 v[184:187],v[64:67],a[104:107],v[184:187]\n"
"ds_read_b64_tr_b16 v[62:63],v105 offset:58368\n"
"s_mov_b32 s47,0x20000\n"
"v_mfma_f32_16x16x32_bf16 v[216:219],v[64:67],a[112:115],v[216:219]\n"
"ds_read_b128 v[4:7],v97 offset:16384\n"
"s_mov_b32 s51,0x20000\n"
"v_mfma_f32_16x16x32_bf16 v[216:219],v[68:71],a[116:119],v[216:219]\n"
"ds_read_b128 v[12:15],v97 offset:17408\n"
"s_mul_i32 s82,s52,1835008\n"
"v_mfma_f32_16x16x32_bf16 v[248:251],v[68:71],a[124:127],v[248:251]\n"
"ds_read_b128 v[20:23],v97 offset:18432\n"
"s_lshl_b32 s83,s23,5\n"
"v_mfma_f32_16x16x32_bf16 v[248:251],v[64:67],a[120:123],v[248:251]\n"
"ds_read_b128 v[28:31],v97 offset:19456\n"
"s_add_u32 s82,s82,s83\n"
"v_mfma_f32_16x16x32_bf16 v[252:255],v[72:75],a[120:123],v[252:255]\n"
"buffer_load_dwordx4 v112,s[24:27],s76 offen offset:1024 sc1 lds\n"
"v_mfma_f32_16x16x32_bf16 v[252:255],v[76:79],a[124:127],v[252:255]\n"
"s_add_u32 s32,s32,s82\n"
"s_addc_u32 s33,s33,0\n"
"s_add_u32 s36,s36,s82\n"
"v_mfma_f32_16x16x32_bf16 v[220:223],v[76:79],a[116:119],v[220:223]\n"
"s_addc_u32 s37,s37,0\n"
"s_lshl2_add_u32 s82,s23,s52\n"
"s_lshl_b32 s82,s82,13\n"
"v_mfma_f32_16x16x32_bf16 v[220:223],v[72:75],a[112:115],v[220:223]\n"
"buffer_load_dwordx4 v112,s[24:27],s76 offen offset:2048 sc1 lds\n"
"v_mfma_f32_16x16x32_bf16 v[188:191],v[72:75],a[104:107],v[188:191]\n"
"s_add_u32 s40,s40,s82\n"
"s_addc_u32 s41,s41,0\n"
"v_bfe_u32 v121,v112,5,2\n"
"v_mfma_f32_16x16x32_bf16 v[188:191],v[76:79],a[108:111],v[188:191]\n"
"v_bfe_u32 v122,v112,7,2\n"
"v_lshl_add_u32 v121,v122,3,v121\n"
"v_mfma_f32_16x16x32_bf16 v[156:159],v[76:79],a[100:103],v[156:159]\n"
"buffer_load_dwordx4 v112,s[24:27],s76 offen offset:3072 sc1 lds\n"
"v_mfma_f32_16x16x32_bf16 v[156:159],v[72:75],a[96:99],v[156:159]\n"
"s_xor_b32 s76,s76,49152\n"
"v_bfe_u32 v122,v112,9,1\n"
"s_waitcnt vmcnt(13) lgkmcnt(0)\n"
"s_barrier\n"
"v_mfma_f32_16x16x32_bf16 v[128:131],v[80:83],v[0:3],v[128:131]\n"
"ds_read_b64_tr_b16 v[64:65],v98 offset:32768\n"
"ds_read_b64_tr_b16 v[72:73],v99 offset:32768\n"
"v_mfma_f32_16x16x32_bf16 v[128:131],v[84:87],v[4:7],v[128:131]\n"
"ds_read_b64_tr_b16 v[66:67],v98 offset:33792\n"
"ds_read_b64_tr_b16 v[74:75],v99 offset:33792\n"
"v_mfma_f32_16x16x32_bf16 v[160:163],v[84:87],v[12:15],v[160:163]\n"
"ds_read_b64_tr_b16 v[68:69],v98 offset:40960\n"
"ds_read_b64_tr_b16 v[76:77],v99 offset:40960\n"
"v_mfma_f32_16x16x32_bf16 v[160:163],v[80:83],v[8:11],v[160:163]\n"
"s_add_u32 m0,s85,147456\n"
"ds_read_b64_tr_b16 v[70:71],v98 offset:41984\n"
"ds_read_b64_tr_b16 v[78:79],v99 offset:41984\n"
"v_mfma_f32_16x16x32_bf16 v[192:195],v[80:83],v[16:19],v[192:195]\n"
"buffer_load_dwordx4 v110,s[28:31],s86 offen lds\n"
"ds_read_b64_tr_b16 a[176:177],v107 offset:0\n"
"v_mfma_f32_16x16x32_bf16 v[192:195],v[84:87],v[20:23],v[192:195]\n"
"s_add_u32 m0,s85,148480\n"
"ds_read_b64_tr_b16 a[178:179],v107 offset:4096\n"
"ds_read_b64_tr_b16 a[180:181],v107 offset:8\n"
"v_mfma_f32_16x16x32_bf16 v[224:227],v[84:87],v[28:31],v[224:227]\n"
"buffer_load_dwordx4 v111,s[28:31],s86 offen lds\n"
"ds_read_b64_tr_b16 a[182:183],v107 offset:4104\n"
"v_mfma_f32_16x16x32_bf16 v[224:227],v[80:83],v[24:27],v[224:227]\n"
"s_xor_b32 s86,s86,262144\n"
"ds_read_b64_tr_b16 a[184:185],v107 offset:8192\n"
"ds_read_b64_tr_b16 a[186:187],v107 offset:12288\n"
"v_mfma_f32_16x16x32_bf16 v[228:231],v[88:91],v[24:27],v[228:231]\n"
"ds_read_b64_tr_b16 v[80:81],v100 offset:32768\n"
"s_waitcnt vmcnt(9)\n"
"v_xor_b32 v116,s80,v118\n"
"v_mfma_f32_16x16x32_bf16 v[228:231],v[92:95],v[28:31],v[228:231]\n"
"ds_read_b64_tr_b16 v[82:83],v100 offset:33792\n"
"ds_read_b64_tr_b16 a[188:189],v107 offset:8200\n"
"v_xor_b32 v117,s81,v119\n"
"v_mfma_f32_16x16x32_bf16 v[196:199],v[92:95],v[20:23],v[196:199]\n"
"ds_read_b64_tr_b16 v[84:85],v100 offset:40960\n"
"ds_read_b64_tr_b16 a[190:191],v107 offset:12296\n"
"v_mfma_f32_16x16x32_bf16 v[196:199],v[88:91],v[16:19],v[196:199]\n"
"s_add_u32 m0,s85,155648\n"
"ds_read_b64_tr_b16 v[86:87],v100 offset:41984\n"
"v_or_b32 v116,v116,v117\n"
"v_mfma_f32_16x16x32_bf16 v[164:167],v[88:91],v[8:11],v[164:167]\n"
"buffer_load_dwordx4 v110,s[28:31],s86 offen lds\n"
"v_mfma_f32_16x16x32_bf16 v[164:167],v[92:95],v[12:15],v[164:167]\n"
"s_add_u32 m0,s85,156672\n"
"v_cmp_ne_u32 vcc,0,v116\n"
"v_mfma_f32_16x16x32_bf16 v[132:135],v[92:95],v[4:7],v[132:135]\n"
"buffer_load_dwordx4 v111,s[28:31],s86 offen lds\n"
"v_mfma_f32_16x16x32_bf16 v[132:135],v[88:91],v[0:3],v[132:135]\n"
"s_xor_b32 s86,s86,16515072\n"
"s_cbranch_vccnz .Lfk_kv_s\n"
".Lfk_kv_ok:\n"
"v_mfma_f32_16x16x32_bf16 v[136:139],a[128:131],v[0:3],v[136:139]\n"
"ds_read_b64_tr_b16 v[88:89],v101 offset:32768\n"
"v_lshl_add_u32 v121,v122,2,v121\n"
"v_mfma_f32_16x16x32_bf16 v[136:139],a[132:135],v[4:7],v[136:139]\n"
"ds_read_b64_tr_b16 v[90:91],v101 offset:33792\n"
"v_mul_u32_u24 v121,7168,v121\n"
"v_mfma_f32_16x16x32_bf16 v[168:171],a[132:135],v[12:15],v[168:171]\n"
"ds_read_b64_tr_b16 v[92:93],v101 offset:40960\n"
"v_and_b32 v122,16,v112\n"
"v_mfma_f32_16x16x32_bf16 v[168:171],a[128:131],v[8:11],v[168:171]\n"
"s_add_u32 m0,s85,81920\n"
"s_add_u32 s77,s73,16384\n"
"ds_read_b64_tr_b16 v[94:95],v101 offset:41984\n"
"v_mfma_f32_16x16x32_bf16 v[200:203],a[128:131],v[16:19],v[200:203]\n"
"buffer_load_dwordx4 v112,s[24:27],s77 offen offset:0 sc1 lds\n"
"v_mfma_f32_16x16x32_bf16 v[200:203],a[132:135],v[20:23],v[200:203]\n"
"v_add_u32 v115,v121,v122\n"
"s_mov_b32 s54,-229376\n"
"v_mfma_f32_16x16x32_bf16 v[232:235],a[132:135],v[28:31],v[232:235]\n"
"buffer_load_dwordx4 v112,s[24:27],s77 offen offset:1024 sc1 lds\n"
"v_mfma_f32_16x16x32_bf16 v[232:235],a[128:131],v[24:27],v[232:235]\n"
"s_lshl_b32 s55,s52,10\n"
"s_add_u32 s55,s55,131072\n"
"v_mfma_f32_16x16x32_bf16 v[236:239],a[136:139],v[24:27],v[236:239]\n"
"ds_read_b64_tr_b16 a[128:129],v102 offset:32768\n"
"s_mov_b32 s22,229376\n"
"v_mfma_f32_16x16x32_bf16 v[236:239],a[140:143],v[28:31],v[236:239]\n"
"ds_read_b64_tr_b16 a[130:131],v102 offset:33792\n"
"v_lshrrev_b32 v121,7,v112\n"
"v_mfma_f32_16x16x32_bf16 v[204:207],a[140:143],v[20:23],v[204:207]\n"
"ds_read_b64_tr_b16 a[132:133],v102 offset:40960\n"
"v_mul_u32_u24 v121,7168,v121\n"
"v_mfma_f32_16x16x32_bf16 v[204:207],a[136:139],v[16:19],v[204:207]\n"
"s_add_u32 m0,s85,65536\n"
"s_add_u32 s77,s73,65536\n"
"ds_read_b64_tr_b16 a[134:135],v102 offset:41984\n"
"v_mfma_f32_16x16x32_bf16 v[172:175],a[136:139],v[8:11],v[172:175]\n"
"buffer_load_dwordx4 v112,s[24:27],s77 offen offset:0 sc1 lds\n"
"v_mfma_f32_16x16x32_bf16 v[172:175],a[140:143],v[12:15],v[172:175]\n"
"v_lshlrev_b32 v123,3,v112\n"
"v_and_b32 v122,0x380,v123\n"
"v_mfma_f32_16x16x32_bf16 v[140:143],a[140:143],v[4:7],v[140:143]\n"
"buffer_load_dwordx4 v112,s[24:27],s77 offen offset:1024 sc1 lds\n"
"v_mfma_f32_16x16x32_bf16 v[140:143],a[136:139],v[0:3],v[140:143]\n"
"v_min_u32 v122,0x300,v122\n"
"v_add_u32 v122,v121,v122\n"
"v_mfma_f32_16x16x32_bf16 v[144:147],v[32:35],v[0:3],v[144:147]\n"
"ds_read_b64_tr_b16 a[136:137],v103 offset:32768\n"
"v_mfma_f32_16x16x32_bf16 v[144:147],v[36:39],v[4:7],v[144:147]\n"
"ds_read_b64_tr_b16 a[138:139],v103 offset:33792\n"
"v_mfma_f32_16x16x32_bf16 v[176:179],v[36:39],v[12:15],v[176:179]\n"
"ds_read_b64_tr_b16 a[140:141],v103 offset:40960\n"
"v_mfma_f32_16x16x32_bf16 v[176:179],v[32:35],v[8:11],v[176:179]\n"
"s_add_u32 m0,s85,73728\n"
"s_add_u32 s77,s73,24576\n"
"ds_read_b64_tr_b16 a[142:143],v103 offset:41984\n"
"v_mfma_f32_16x16x32_bf16 v[208:211],v[32:35],v[16:19],v[208:211]\n"
"buffer_load_dwordx4 v112,s[24:27],s77 offen offset:0 sc1 lds\n"
"v_mfma_f32_16x16x32_bf16 v[208:211],v[36:39],v[20:23],v[208:211]\n"
"ds_read_b128 a[96:99],v96 offset:0\n"
"v_mfma_f32_16x16x32_bf16 v[240:243],v[36:39],v[28:31],v[240:243]\n"
"buffer_load_dwordx4 v112,s[24:27],s77 offen offset:1024 sc1 lds\n"
"v_mfma_f32_16x16x32_bf16 v[240:243],v[32:35],v[24:27],v[240:243]\n"
"ds_read_b128 a[104:107],v96 offset:1024\n"
"v_mfma_f32_16x16x32_bf16 v[244:247],v[40:43],v[24:27],v[244:247]\n"
"ds_read_b64_tr_b16 v[32:33],v104 offset:32768\n"
"v_mfma_f32_16x16x32_bf16 v[244:247],v[44:47],v[28:31],v[244:247]\n"
"ds_read_b64_tr_b16 v[34:35],v104 offset:33792\n"
"v_mfma_f32_16x16x32_bf16 v[212:215],v[44:47],v[20:23],v[212:215]\n"
"ds_read_b64_tr_b16 v[36:37],v104 offset:40960\n"
"v_mfma_f32_16x16x32_bf16 v[212:215],v[40:43],v[16:19],v[212:215]\n"
"ds_read_b64_tr_b16 v[38:39],v104 offset:41984\n"
"s_mov_b32 s50,22400\n"
"v_mfma_f32_16x16x32_bf16 v[180:183],v[40:43],v[8:11],v[180:183]\n"
"buffer_load_dword a148,v122,s[48:51],0 offen\n"
"v_mfma_f32_16x16x32_bf16 v[180:183],v[44:47],v[12:15],v[180:183]\n"
"ds_read_b128 a[112:115],v96 offset:2048\n"
"v_mfma_f32_16x16x32_bf16 v[148:151],v[44:47],v[4:7],v[148:151]\n"
"v_mfma_f32_16x16x32_bf16 v[148:151],v[40:43],v[0:3],v[148:151]\n"
"ds_read_b128 a[120:123],v96 offset:3072\n"
"v_mfma_f32_16x16x32_bf16 v[152:155],v[48:51],v[0:3],v[152:155]\n"
"ds_read_b64_tr_b16 v[40:41],v105 offset:32768\n"
"v_mfma_f32_16x16x32_bf16 v[152:155],v[52:55],v[4:7],v[152:155]\n"
"ds_read_b64_tr_b16 v[42:43],v105 offset:33792\n"
"v_mfma_f32_16x16x32_bf16 v[184:187],v[52:55],v[12:15],v[184:187]\n"
"ds_read_b64_tr_b16 v[44:45],v105 offset:40960\n"
"v_mfma_f32_16x16x32_bf16 v[184:187],v[48:51],v[8:11],v[184:187]\n"
"ds_read_b64_tr_b16 v[46:47],v105 offset:41984\n"
"v_mfma_f32_16x16x32_bf16 v[216:219],v[48:51],v[16:19],v[216:219]\n"
"ds_read_b128 a[100:103],v96 offset:16384\n"
"v_mfma_f32_16x16x32_bf16 v[216:219],v[52:55],v[20:23],v[216:219]\n"
"ds_read_b128 a[108:111],v96 offset:17408\n"
"v_mfma_f32_16x16x32_bf16 v[248:251],v[52:55],v[28:31],v[248:251]\n"
"ds_read_b128 a[116:119],v96 offset:18432\n"
"v_mfma_f32_16x16x32_bf16 v[248:251],v[48:51],v[24:27],v[248:251]\n"
"ds_read_b128 a[124:127],v96 offset:19456\n"
"v_mfma_f32_16x16x32_bf16 v[252:255],v[56:59],v[24:27],v[252:255]\n"
"v_mfma_f32_16x16x32_bf16 v[252:255],v[60:63],v[28:31],v[252:255]\n"
"buffer_store_dwordx4 a[160:163],v112,s[24:27],s3 offen offset:0 sc1\n"
"v_mfma_f32_16x16x32_bf16 v[220:223],v[60:63],v[20:23],v[220:223]\n"
"v_mfma_f32_16x16x32_bf16 v[220:223],v[56:59],v[16:19],v[220:223]\n"
"v_mfma_f32_16x16x32_bf16 v[188:191],v[56:59],v[8:11],v[188:191]\n"
"v_mfma_f32_16x16x32_bf16 v[188:191],v[60:63],v[12:15],v[188:191]\n"
"buffer_store_dwordx4 a[164:167],v112,s[24:27],s3 offen offset:1024 sc1\n"
"v_mfma_f32_16x16x32_bf16 v[156:159],v[60:63],v[4:7],v[156:159]\n"
"v_mfma_f32_16x16x32_bf16 v[156:159],v[56:59],v[0:3],v[156:159]\n"
"s_waitcnt lgkmcnt(0)\n"
"s_barrier\n"
"v_mfma_f32_16x16x32_bf16 v[128:131],v[64:67],a[96:99],v[128:131]\n"
"ds_read_b64_tr_b16 v[48:49],v98 offset:0\n"
"ds_read_b64_tr_b16 v[56:57],v99 offset:0\n"
"v_mfma_f32_16x16x32_bf16 v[128:131],v[68:71],a[100:103],v[128:131]\n"
"ds_read_b64_tr_b16 v[50:51],v98 offset:1024\n"
"ds_read_b64_tr_b16 v[58:59],v99 offset:1024\n"
"v_mfma_f32_16x16x32_bf16 v[160:163],v[68:71],a[108:111],v[160:163]\n"
"ds_read_b64_tr_b16 v[52:53],v98 offset:8192\n"
"ds_read_b64_tr_b16 v[60:61],v99 offset:8192\n"
"v_mfma_f32_16x16x32_bf16 v[160:163],v[64:67],a[104:107],v[160:163]\n"
"s_add_u32 m0,s78,0\n"
"ds_read_b64_tr_b16 v[54:55],v98 offset:9216\n"
"ds_read_b64_tr_b16 v[62:63],v99 offset:9216\n"
"v_mfma_f32_16x16x32_bf16 v[192:195],v[64:67],a[112:115],v[192:195]\n"
"buffer_load_dwordx4 v112,s[24:27],s76 offen offset:0 sc1 lds\n"
"v_mfma_f32_16x16x32_bf16 v[192:195],v[68:71],a[116:119],v[192:195]\n"
"v_mfma_f32_16x16x32_bf16 v[224:227],v[68:71],a[124:127],v[224:227]\n"
"v_mfma_f32_16x16x32_bf16 v[224:227],v[64:67],a[120:123],v[224:227]\n"
"v_mfma_f32_16x16x32_bf16 v[228:231],v[72:75],a[120:123],v[228:231]\n"
"ds_read_b64_tr_b16 v[64:65],v100 offset:0\n"
"v_mfma_f32_16x16x32_bf16 v[228:231],v[76:79],a[124:127],v[228:231]\n"
"ds_read_b64_tr_b16 v[66:67],v100 offset:1024\n"
"v_mfma_f32_16x16x32_bf16 v[196:199],v[76:79],a[116:119],v[196:199]\n"
"ds_read_b64_tr_b16 v[68:69],v100 offset:8192\n"
"v_mfma_f32_16x16x32_bf16 v[196:199],v[72:75],a[112:115],v[196:199]\n"
"ds_read_b64_tr_b16 v[70:71],v100 offset:9216\n"
"v_mfma_f32_16x16x32_bf16 v[164:167],v[72:75],a[104:107],v[164:167]\n"
"buffer_load_dwordx4 v112,s[24:27],s76 offen offset:1024 sc1 lds\n"
"v_mfma_f32_16x16x32_bf16 v[164:167],v[76:79],a[108:111],v[164:167]\n"
"v_mfma_f32_16x16x32_bf16 v[132:135],v[76:79],a[100:103],v[132:135]\n"
"buffer_load_dwordx4 v112,s[24:27],s76 offen offset:2048 sc1 lds\n"
"v_mfma_f32_16x16x32_bf16 v[132:135],v[72:75],a[96:99],v[132:135]\n"
"v_mfma_f32_16x16x32_bf16 v[136:139],v[80:83],a[96:99],v[136:139]\n"
"ds_read_b64_tr_b16 v[72:73],v101 offset:0\n"
"v_mfma_f32_16x16x32_bf16 v[136:139],v[84:87],a[100:103],v[136:139]\n"
"ds_read_b64_tr_b16 v[74:75],v101 offset:1024\n"
"v_mfma_f32_16x16x32_bf16 v[168:171],v[84:87],a[108:111],v[168:171]\n"
"ds_read_b64_tr_b16 v[76:77],v101 offset:8192\n"
"v_mfma_f32_16x16x32_bf16 v[168:171],v[80:83],a[104:107],v[168:171]\n"
"ds_read_b64_tr_b16 v[78:79],v101 offset:9216\n"
"v_mfma_f32_16x16x32_bf16 v[200:203],v[80:83],a[112:115],v[200:203]\n"
"buffer_load_dwordx4 v112,s[24:27],s76 offen offset:3072 sc1 lds\n"
"v_mfma_f32_16x16x32_bf16 v[200:203],v[84:87],a[116:119],v[200:203]\n"
"s_xor_b32 s76,s76,16384\n"
"v_mfma_f32_16x16x32_bf16 v[232:235],v[84:87],a[124:127],v[232:235]\n"
"v_mfma_f32_16x16x32_bf16 v[232:235],v[80:83],a[120:123],v[232:235]\n"
"v_mfma_f32_16x16x32_bf16 v[236:239],v[88:91],a[120:123],v[236:239]\n"
"ds_read_b64_tr_b16 v[80:81],v102 offset:0\n"
"v_mfma_f32_16x16x32_bf16 v[236:239],v[92:95],a[124:127],v[236:239]\n"
"ds_read_b64_tr_b16 v[82:83],v102 offset:1024\n"
"v_mfma_f32_16x16x32_bf16 v[204:207],v[92:95],a[116:119],v[204:207]\n"
"ds_read_b64_tr_b16 v[84:85],v102 offset:8192\n"
"v_mfma_f32_16x16x32_bf16 v[204:207],v[88:91],a[112:115],v[204:207]\n"
"s_add_u32 m0,s78,16384\n"
"ds_read_b64_tr_b16 v[86:87],v102 offset:9216\n"
"v_mfma_f32_16x16x32_bf16 v[172:175],v[88:91],a[104:107],v[172:175]\n"
"buffer_load_dwordx4 v112,s[24:27],s76 offen offset:0 sc1 lds\n"
"v_mfma_f32_16x16x32_bf16 v[172:175],v[92:95],a[108:111],v[172:175]\n"
"v_mfma_f32_16x16x32_bf16 v[140:143],v[92:95],a[100:103],v[140:143]\n"
"buffer_load_dwordx4 v112,s[24:27],s76 offen offset:1024 sc1 lds\n"
"v_mfma_f32_16x16x32_bf16 v[140:143],v[88:91],a[96:99],v[140:143]\n"
".Lfk_q27:\n"
"s_waitcnt vmcnt(19)\n"
"v_mfma_f32_16x16x32_bf16 v[144:147],a[128:131],a[96:99],v[144:147]\n"
"ds_read_b64_tr_b16 v[88:89],v103 offset:0\n"
"v_mfma_f32_16x16x32_bf16 v[144:147],a[132:135],a[100:103],v[144:147]\n"
"ds_read_b64_tr_b16 v[90:91],v103 offset:1024\n"
"v_mfma_f32_16x16x32_bf16 v[176:179],a[132:135],a[108:111],v[176:179]\n"
"ds_read_b64_tr_b16 v[92:93],v103 offset:8192\n"
"v_mfma_f32_16x16x32_bf16 v[176:179],a[128:131],a[104:107],v[176:179]\n"
"ds_read_b64_tr_b16 v[94:95],v103 offset:9216\n"
"v_mfma_f32_16x16x32_bf16 v[208:211],a[128:131],a[112:115],v[208:211]\n"
"buffer_load_dwordx4 v112,s[24:27],s76 offen offset:2048 sc1 lds\n"
"v_mfma_f32_16x16x32_bf16 v[208:211],a[132:135],a[116:119],v[208:211]\n"
"ds_read_b128 v[0:3],v96 offset:32768\n"
"v_mfma_f32_16x16x32_bf16 v[240:243],a[132:135],a[124:127],v[240:243]\n"
"v_mfma_f32_16x16x32_bf16 v[240:243],a[128:131],a[120:123],v[240:243]\n"
"ds_read_b128 v[8:11],v96 offset:33792\n"
"v_mfma_f32_16x16x32_bf16 v[244:247],a[136:139],a[120:123],v[244:247]\n"
"ds_read_b64_tr_b16 a[128:129],v104 offset:0\n"
"v_mfma_f32_16x16x32_bf16 v[244:247],a[140:143],a[124:127],v[244:247]\n"
"ds_read_b64_tr_b16 a[130:131],v104 offset:1024\n"
"v_mfma_f32_16x16x32_bf16 v[212:215],a[140:143],a[116:119],v[212:215]\n"
"ds_read_b64_tr_b16 a[132:133],v104 offset:8192\n"
"v_mfma_f32_16x16x32_bf16 v[212:215],a[136:139],a[112:115],v[212:215]\n"
"ds_read_b64_tr_b16 a[134:135],v104 offset:9216\n"
"v_mfma_f32_16x16x32_bf16 v[180:183],a[136:139],a[104:107],v[180:183]\n"
"buffer_load_dwordx4 v112,s[24:27],s76 offen offset:3072 sc1 lds\n"
"v_mfma_f32_16x16x32_bf16 v[180:183],a[140:143],a[108:111],v[180:183]\n"
"s_add_u32 m0,s85,122880\n"
"s_add_u32 s77,s73,0\n"
"ds_read_b128 v[16:19],v96 offset:34816\n"
"v_mfma_f32_16x16x32_bf16 v[148:151],a[140:143],a[100:103],v[148:151]\n"
"buffer_load_dwordx4 v112,s[24:27],s77 offen offset:0 sc1 lds\n"
"v_mfma_f32_16x16x32_bf16 v[148:151],a[136:139],a[96:99],v[148:151]\n"
"s_xor_b32 s76,s76,1032192\n"
"ds_read_b128 v[24:27],v96 offset:35840\n"
"v_mfma_f32_16x16x32_bf16 v[152:155],v[32:35],a[96:99],v[152:155]\n"
"ds_read_b64_tr_b16 a[136:137],v105 offset:0\n"
"v_mfma_f32_16x16x32_bf16 v[152:155],v[36:39],a[100:103],v[152:155]\n"
"ds_read_b64_tr_b16 a[138:139],v105 offset:1024\n"
"v_mfma_f32_16x16x32_bf16 v[184:187],v[36:39],a[108:111],v[184:187]\n"
"ds_read_b64_tr_b16 a[140:141],v105 offset:8192\n"
"v_mfma_f32_16x16x32_bf16 v[184:187],v[32:35],a[104:107],v[184:187]\n"
"ds_read_b64_tr_b16 a[142:143],v105 offset:9216\n"
"v_mfma_f32_16x16x32_bf16 v[216:219],v[32:35],a[112:115],v[216:219]\n"
"ds_read_b128 v[4:7],v96 offset:49152\n"
"v_mfma_f32_16x16x32_bf16 v[216:219],v[36:39],a[116:119],v[216:219]\n"
"ds_read_b128 v[12:15],v96 offset:50176\n"
"v_mfma_f32_16x16x32_bf16 v[248:251],v[36:39],a[124:127],v[248:251]\n"
"ds_read_b128 v[20:23],v96 offset:51200\n"
"v_mfma_f32_16x16x32_bf16 v[248:251],v[32:35],a[120:123],v[248:251]\n"
"ds_read_b128 v[28:31],v96 offset:52224\n"
"v_mfma_f32_16x16x32_bf16 v[252:255],v[40:43],a[120:123],v[252:255]\n"
"buffer_load_dwordx4 v112,s[24:27],s77 offen offset:1024 sc1 lds\n"
"v_mfma_f32_16x16x32_bf16 v[252:255],v[44:47],a[124:127],v[252:255]\n"
"v_mfma_f32_16x16x32_bf16 v[220:223],v[44:47],a[116:119],v[220:223]\n"
"s_add_u32 m0,s85,114688\n"
"s_add_u32 s77,s73,32768\n"
"v_mfma_f32_16x16x32_bf16 v[220:223],v[40:43],a[112:115],v[220:223]\n"
"buffer_load_dwordx4 v112,s[24:27],s77 offen offset:0 sc1 lds\n"
"v_mfma_f32_16x16x32_bf16 v[188:191],v[40:43],a[104:107],v[188:191]\n"
"v_mfma_f32_16x16x32_bf16 v[188:191],v[44:47],a[108:111],v[188:191]\n"
"v_mfma_f32_16x16x32_bf16 v[156:159],v[44:47],a[100:103],v[156:159]\n"
"buffer_load_dwordx4 v112,s[24:27],s77 offen offset:1024 sc1 lds\n"
"v_mfma_f32_16x16x32_bf16 v[156:159],v[40:43],a[96:99],v[156:159]\n"
"s_waitcnt vmcnt(21) lgkmcnt(0)\n"
"s_barrier\n"
"v_mfma_f32_16x16x32_bf16 v[128:131],v[48:51],v[0:3],v[128:131]\n"
"ds_read_b64_tr_b16 v[32:33],v98 offset:49152\n"
"ds_read_b64_tr_b16 v[40:41],v99 offset:49152\n"
"v_mfma_f32_16x16x32_bf16 v[128:131],v[52:55],v[4:7],v[128:131]\n"
"ds_read_b64_tr_b16 v[34:35],v98 offset:50176\n"
"ds_read_b64_tr_b16 v[42:43],v99 offset:50176\n"
"v_mfma_f32_16x16x32_bf16 v[160:163],v[52:55],v[12:15],v[160:163]\n"
"s_add_u32 m0,s85,98304\n"
"ds_read_b64_tr_b16 v[36:37],v98 offset:57344\n"
"ds_read_b64_tr_b16 v[44:45],v99 offset:57344\n"
"v_mfma_f32_16x16x32_bf16 v[160:163],v[48:51],v[8:11],v[160:163]\n"
"s_add_u32 s77,s73,49152\n"
"ds_read_b64_tr_b16 v[38:39],v98 offset:58368\n"
"ds_read_b64_tr_b16 v[46:47],v99 offset:58368\n"
"v_mfma_f32_16x16x32_bf16 v[192:195],v[48:51],v[16:19],v[192:195]\n"
"buffer_load_dwordx4 v112,s[24:27],s77 offen offset:0 sc1 lds\n"
"v_mfma_f32_16x16x32_bf16 v[192:195],v[52:55],v[20:23],v[192:195]\n"
"v_mfma_f32_16x16x32_bf16 v[224:227],v[52:55],v[28:31],v[224:227]\n"
"buffer_load_dwordx4 v112,s[24:27],s77 offen offset:1024 sc1 lds\n"
"v_mfma_f32_16x16x32_bf16 v[224:227],v[48:51],v[24:27],v[224:227]\n"
"v_mfma_f32_16x16x32_bf16 v[228:231],v[56:59],v[24:27],v[228:231]\n"
"ds_read_b64_tr_b16 v[48:49],v100 offset:49152\n"
"v_mfma_f32_16x16x32_bf16 v[228:231],v[60:63],v[28:31],v[228:231]\n"
"ds_read_b64_tr_b16 v[50:51],v100 offset:50176\n"
"v_mfma_f32_16x16x32_bf16 v[196:199],v[60:63],v[20:23],v[196:199]\n"
"ds_read_b64_tr_b16 v[52:53],v100 offset:57344\n"
"v_mfma_f32_16x16x32_bf16 v[196:199],v[56:59],v[16:19],v[196:199]\n"
"s_add_u32 m0,s85,106496\n"
"s_add_u32 s77,s73,8192\n"
"ds_read_b64_tr_b16 v[54:55],v100 offset:58368\n"
"v_mfma_f32_16x16x32_bf16 v[164:167],v[56:59],v[8:11],v[164:167]\n"
"buffer_load_dwordx4 v112,s[24:27],s77 offen offset:0 sc1 lds\n"
"v_mfma_f32_16x16x32_bf16 v[164:167],v[60:63],v[12:15],v[164:167]\n"
"v_mfma_f32_16x16x32_bf16 v[132:135],v[60:63],v[4:7],v[132:135]\n"
"buffer_load_dwordx4 v112,s[24:27],s77 offen offset:1024 sc1 lds\n"
"v_mfma_f32_16x16x32_bf16 v[132:135],v[56:59],v[0:3],v[132:135]\n"
"v_mfma_f32_16x16x32_bf16 v[136:139],v[64:67],v[0:3],v[136:139]\n"
"ds_read_b64_tr_b16 v[56:57],v101 offset:49152\n"
"v_mfma_f32_16x16x32_bf16 v[136:139],v[68:71],v[4:7],v[136:139]\n"
"ds_read_b64_tr_b16 v[58:59],v101 offset:50176\n"
"v_mfma_f32_16x16x32_bf16 v[168:171],v[68:71],v[12:15],v[168:171]\n"
"ds_read_b64_tr_b16 v[60:61],v101 offset:57344\n"
"ds_read_b64_tr_b16 v[62:63],v101 offset:58368\n"
"v_mfma_f32_16x16x32_bf16 v[168:171],v[64:67],v[8:11],v[168:171]\n"
"s_add_u32 s48,s48,28672\n"
"s_addc_u32 s49,s49,0\n"
"v_mfma_f32_16x16x32_bf16 v[200:203],v[64:67],v[16:19],v[200:203]\n"
"buffer_load_dword a148,v122,s[48:51],0 offen\n"
"v_mfma_f32_16x16x32_bf16 v[200:203],v[68:71],v[20:23],v[200:203]\n"
"v_mfma_f32_16x16x32_bf16 v[232:235],v[68:71],v[28:31],v[232:235]\n"
"v_mfma_f32_16x16x32_bf16 v[232:235],v[64:67],v[24:27],v[232:235]\n"
"v_mfma_f32_16x16x32_bf16 v[236:239],v[72:75],v[24:27],v[236:239]\n"
"ds_read_b64_tr_b16 v[64:65],v102 offset:49152\n"
"v_mfma_f32_16x16x32_bf16 v[236:239],v[76:79],v[28:31],v[236:239]\n"
"ds_read_b64_tr_b16 v[66:67],v102 offset:50176\n"
"v_mfma_f32_16x16x32_bf16 v[204:207],v[76:79],v[20:23],v[204:207]\n"
"ds_read_b64_tr_b16 v[68:69],v102 offset:57344\n"
"v_mfma_f32_16x16x32_bf16 v[204:207],v[72:75],v[16:19],v[204:207]\n"
"ds_read_b64_tr_b16 v[70:71],v102 offset:58368\n"
"v_mfma_f32_16x16x32_bf16 v[172:175],v[72:75],v[8:11],v[172:175]\n"
"v_mfma_f32_16x16x32_bf16 v[172:175],v[76:79],v[12:15],v[172:175]\n"
"v_mfma_f32_16x16x32_bf16 v[140:143],v[76:79],v[4:7],v[140:143]\n"
"v_mfma_f32_16x16x32_bf16 v[140:143],v[72:75],v[0:3],v[140:143]\n"
"s_waitcnt vmcnt(9)\n"
"v_mfma_f32_16x16x32_bf16 v[144:147],v[80:83],v[0:3],v[144:147]\n"
"ds_read_b64_tr_b16 v[72:73],v103 offset:49152\n"
"s_mov_b32 s90,0\n"
"v_mfma_f32_16x16x32_bf16 v[144:147],v[84:87],v[4:7],v[144:147]\n"
"ds_read_b64_tr_b16 v[74:75],v103 offset:50176\n"
"s_mov_b32 s16,0xc21c0000\n"
"v_mfma_f32_16x16x32_bf16 v[176:179],v[84:87],v[12:15],v[176:179]\n"
"ds_read_b64_tr_b16 v[76:77],v103 offset:57344\n"
"s_mov_b32 s17,0x41a00000\n"
"v_mfma_f32_16x16x32_bf16 v[176:179],v[80:83],v[8:11],v[176:179]\n"
"ds_read_b64_tr_b16 v[78:79],v103 offset:58368\n"
"s_mov_b32 s89,0\n"
"v_mfma_f32_16x16x32_bf16 v[208:211],v[80:83],v[16:19],v[208:211]\n"
"s_xor_b32 s82,s78,8192\n"
"v_add_u32 v116,s82,v112\n"
"v_mfma_f32_16x16x32_bf16 v[208:211],v[84:87],v[20:23],v[208:211]\n"
"ds_read_b128 a[96:99],v96 offset:0\n"
"v_add_u32 v117,4096,v112\n"
"v_mfma_f32_16x16x32_bf16 v[240:243],v[84:87],v[28:31],v[240:243]\n"
"s_getpc_b64 s[20:21]\n"
".Lfk_evp:\n"
"s_add_u32 s20,s20,.Lfk_ev-.Lfk_evp\n"
"s_addc_u32 s21,s21,0\n"
"v_mfma_f32_16x16x32_bf16 v[240:243],v[80:83],v[24:27],v[240:243]\n"
"ds_read_b128 a[104:107],v96 offset:1024\n"
"v_mfma_f32_16x16x32_bf16 v[244:247],v[88:91],v[24:27],v[244:247]\n"
"ds_read_b64_tr_b16 v[80:81],v104 offset:49152\n"
"v_mfma_f32_16x16x32_bf16 v[244:247],v[92:95],v[28:31],v[244:247]\n"
"ds_read_b64_tr_b16 v[82:83],v104 offset:50176\n"
"v_mfma_f32_16x16x32_bf16 v[212:215],v[92:95],v[20:23],v[212:215]\n"
"ds_read_b64_tr_b16 v[84:85],v104 offset:57344\n"
"v_mfma_f32_16x16x32_bf16 v[212:215],v[88:91],v[16:19],v[212:215]\n"
"ds_read_b64_tr_b16 v[86:87],v104 offset:58368\n"
"v_mfma_f32_16x16x32_bf16 v[180:183],v[88:91],v[8:11],v[180:183]\n"
"buffer_store_dwordx4 a[156:159],v112,s[24:27],s74 offen offset:0 sc1\n"
"v_mfma_f32_16x16x32_bf16 v[180:183],v[92:95],v[12:15],v[180:183]\n"
"ds_read_b128 a[112:115],v96 offset:2048\n"
"v_mfma_f32_16x16x32_bf16 v[148:151],v[92:95],v[4:7],v[148:151]\n"
"v_mfma_f32_16x16x32_bf16 v[148:151],v[88:91],v[0:3],v[148:151]\n"
"ds_read_b128 a[120:123],v96 offset:3072\n"
"v_mfma_f32_16x16x32_bf16 v[152:155],a[128:131],v[0:3],v[152:155]\n"
"ds_read_b64_tr_b16 v[88:89],v105 offset:49152\n"
"v_mfma_f32_16x16x32_bf16 v[152:155],a[132:135],v[4:7],v[152:155]\n"
"ds_read_b64_tr_b16 v[90:91],v105 offset:50176\n"
"v_mfma_f32_16x16x32_bf16 v[184:187],a[132:135],v[12:15],v[184:187]\n"
"ds_read_b64_tr_b16 v[92:93],v105 offset:57344\n"
"v_mfma_f32_16x16x32_bf16 v[184:187],a[128:131],v[8:11],v[184:187]\n"
"ds_read_b64_tr_b16 v[94:95],v105 offset:58368\n"
"v_mfma_f32_16x16x32_bf16 v[216:219],a[128:131],v[16:19],v[216:219]\n"
"ds_read_b128 a[100:103],v96 offset:16384\n"
"v_mfma_f32_16x16x32_bf16 v[216:219],a[132:135],v[20:23],v[216:219]\n"
"ds_read_b128 a[108:111],v96 offset:17408\n"
"v_add_u32 v113,65536,v112\n"
"v_mfma_f32_16x16x32_bf16 v[248:251],a[132:135],v[28:31],v[248:251]\n"
"ds_read_b128 a[116:119],v96 offset:18432\n"
"v_add_u32 v114,65536,v112\n"
"v_mfma_f32_16x16x32_bf16 v[248:251],a[128:131],v[24:27],v[248:251]\n"
"ds_read_b128 a[124:127],v96 offset:19456\n"
"v_mfma_f32_16x16x32_bf16 v[252:255],a[136:139],v[24:27],v[252:255]\n"
"v_mfma_f32_16x16x32_bf16 v[252:255],a[140:143],v[28:31],v[252:255]\n"
"buffer_store_dwordx4 a[156:159],v112,s[24:27],s74 offen offset:1024 sc1\n"
"v_mfma_f32_16x16x32_bf16 v[220:223],a[140:143],v[20:23],v[220:223]\n"
"s_lshr_b32 s82,s74,1\n"
"s_add_u32 s82,s82,88080384\n"
"v_mfma_f32_16x16x32_bf16 v[220:223],a[136:139],v[16:19],v[220:223]\n"
"buffer_store_dwordx4 a[156:159],v112,s[24:27],s82 offen sc1\n"
"v_mfma_f32_16x16x32_bf16 v[188:191],a[136:139],v[8:11],v[188:191]\n"
"v_mfma_f32_16x16x32_bf16 v[188:191],a[140:143],v[12:15],v[188:191]\n"
"buffer_store_dword a156,off,s[24:27],s72 sc1\n"
"v_mfma_f32_16x16x32_bf16 v[156:159],a[140:143],v[4:7],v[156:159]\n"
"v_mfma_f32_16x16x32_bf16 v[156:159],a[136:139],v[0:3],v[156:159]\n"
"s_waitcnt vmcnt(11) lgkmcnt(0)\n"
"s_barrier\n"
"v_mfma_f32_16x16x32_bf16 v[224:227],v[32:35],a[120:123],v[224:227]\n"
"ds_read_b128 a[64:67],v113 offset:57344\n"
"v_mov_b32 v96,0xc1980000\n"
"v_mfma_f32_16x16x32_bf16 v[224:227],v[36:39],a[124:127],v[224:227]\n"
"ds_read_b128 a[68:71],v113 offset:58368\n"
"v_mov_b32 v97,0xc1980000\n"
"v_mfma_f32_16x16x32_bf16 v[228:231],v[44:47],a[124:127],v[228:231]\n"
"ds_read_b128 a[72:75],v113 offset:59392\n"
"v_mov_b32 v98,0xc1980000\n"
"v_mfma_f32_16x16x32_bf16 v[228:231],v[40:43],a[120:123],v[228:231]\n"
"ds_read_b128 a[76:79],v113 offset:60416\n"
"v_mov_b32 v99,0xc1980000\n"
"v_mfma_f32_16x16x32_bf16 v[232:235],v[48:51],a[120:123],v[232:235]\n"
"ds_read_b128 a[80:83],v113 offset:61440\n"
"v_mov_b32 v100,0xc1980000\n"
"v_mfma_f32_16x16x32_bf16 v[232:235],v[52:55],a[124:127],v[232:235]\n"
"ds_read_b128 a[84:87],v113 offset:62464\n"
"v_mov_b32 v101,0xc1980000\n"
"v_mfma_f32_16x16x32_bf16 v[236:239],v[60:63],a[124:127],v[236:239]\n"
"ds_read_b128 a[88:91],v113 offset:63488\n"
"v_mov_b32 v102,0xc1980000\n"
"v_mfma_f32_16x16x32_bf16 v[236:239],v[56:59],a[120:123],v[236:239]\n"
"ds_read_b128 a[92:95],v113 offset:64512\n"
"v_mov_b32 v103,0xc1980000\n"
"v_mfma_f32_16x16x32_bf16 v[240:243],v[64:67],a[120:123],v[240:243]\n"
"v_mov_b32 v104,0xc1980000\n"
"v_mov_b32 v105,0xc1980000\n"
"v_mfma_f32_16x16x32_bf16 v[240:243],v[68:71],a[124:127],v[240:243]\n"
"ds_write_b32 v124,v125\n"
"v_mfma_f32_16x16x32_bf16 v[244:247],v[76:79],a[124:127],v[244:247]\n"
"v_mov_b32 v107,0xc1980000\n"
"v_mov_b32 v108,0xc1980000\n"
"v_mfma_f32_16x16x32_bf16 v[244:247],v[72:75],a[120:123],v[244:247]\n"
"v_mov_b32 v109,0xc1980000\n"
"v_mov_b32 v110,0xc1980000\n"
"v_mfma_f32_16x16x32_bf16 v[248:251],v[80:83],a[120:123],v[248:251]\n"
"v_mov_b32 v106,0xc1980000\n"
"v_mov_b32 v111,0xc1980000\n"
"v_mfma_f32_16x16x32_bf16 v[248:251],v[84:87],a[124:127],v[248:251]\n"
"v_mfma_f32_16x16x32_bf16 v[252:255],v[92:95],a[124:127],v[252:255]\n"
"v_mfma_f32_16x16x32_bf16 v[252:255],v[88:91],a[120:123],v[252:255]\n"
"v_mfma_f32_16x16x32_bf16 v[128:131],v[32:35],a[96:99],v[128:131]\n"
"v_mfma_f32_16x16x32_bf16 v[128:131],v[36:39],a[100:103],v[128:131]\n"
"v_cvt_pk_bf16_f32 v232,v232,v233\n"
"v_cvt_pk_bf16_f32 v233,v234,v235\n"
"v_mfma_f32_16x16x32_bf16 v[132:135],v[44:47],a[100:103],v[132:135]\n"
"v_cvt_pk_bf16_f32 v224,v224,v225\n"
"v_cvt_pk_bf16_f32 v225,v226,v227\n"
"v_mfma_f32_16x16x32_bf16 v[132:135],v[40:43],a[96:99],v[132:135]\n"
"v_cvt_pk_bf16_f32 v226,v228,v229\n"
"v_cvt_pk_bf16_f32 v227,v230,v231\n"
"v_mfma_f32_16x16x32_bf16 v[136:139],v[48:51],a[96:99],v[136:139]\n"
"ds_write_b128 v116,v[224:227] offset:0\n"
"v_mfma_f32_16x16x32_bf16 v[136:139],v[52:55],a[100:103],v[136:139]\n"
"v_cvt_pk_bf16_f32 v234,v236,v237\n"
"v_cvt_pk_bf16_f32 v235,v238,v239\n"
"v_mfma_f32_16x16x32_bf16 v[140:143],v[60:63],a[100:103],v[140:143]\n"
"ds_write_b128 v116,v[232:235] offset:1024\n"
"v_mfma_f32_16x16x32_bf16 v[140:143],v[56:59],a[96:99],v[140:143]\n"
"v_cvt_pk_bf16_f32 v240,v240,v241\n"
"v_cvt_pk_bf16_f32 v241,v242,v243\n"
"v_mfma_f32_16x16x32_bf16 v[144:147],v[64:67],a[96:99],v[144:147]\n"
"v_cvt_pk_bf16_f32 v242,v244,v245\n"
"v_cvt_pk_bf16_f32 v243,v246,v247\n"
"v_mfma_f32_16x16x32_bf16 v[144:147],v[68:71],a[100:103],v[144:147]\n"
"ds_write_b128 v116,v[240:243] offset:2048\n"
"v_cvt_pk_bf16_f32 v248,v248,v249\n"
"v_mfma_f32_16x16x32_bf16 v[148:151],v[76:79],a[100:103],v[148:151]\n"
"v_cvt_pk_bf16_f32 v249,v250,v251\n"
"v_cvt_pk_bf16_f32 v250,v252,v253\n"
"v_cvt_pk_bf16_f32 v251,v254,v255\n"
"v_mfma_f32_16x16x32_bf16 v[148:151],v[72:75],a[96:99],v[148:151]\n"
"ds_write_b128 v116,v[248:251] offset:3072\n"
"v_mfma_f32_16x16x32_bf16 v[152:155],v[80:83],a[96:99],v[152:155]\n"
"ds_read_b128 a[48:51],v116 offset:0\n"
"ds_read_b128 a[52:55],v116 offset:1024\n"
"v_mfma_f32_16x16x32_bf16 v[152:155],v[84:87],a[100:103],v[152:155]\n"
"ds_read_b128 a[56:59],v116 offset:2048\n"
"ds_read_b128 a[60:63],v116 offset:3072\n"
"v_mfma_f32_16x16x32_bf16 v[156:159],v[92:95],a[100:103],v[156:159]\n"
"v_mfma_f32_16x16x32_bf16 v[156:159],v[88:91],a[96:99],v[156:159]\n"
"v_mfma_f32_16x16x32_bf16 v[160:163],v[32:35],a[104:107],v[160:163]\n"
"v_mfma_f32_16x16x32_bf16 v[160:163],v[36:39],a[108:111],v[160:163]\n"
"v_cvt_pk_bf16_f32 v136,v136,v137\n"
"v_cvt_pk_bf16_f32 v137,v138,v139\n"
"v_mfma_f32_16x16x32_bf16 v[164:167],v[44:47],a[108:111],v[164:167]\n"
"v_cvt_pk_bf16_f32 v128,v128,v129\n"
"v_cvt_pk_bf16_f32 v129,v130,v131\n"
"v_mfma_f32_16x16x32_bf16 v[164:167],v[40:43],a[104:107],v[164:167]\n"
"v_cvt_pk_bf16_f32 v130,v132,v133\n"
"v_cvt_pk_bf16_f32 v131,v134,v135\n"
"v_mfma_f32_16x16x32_bf16 v[168:171],v[48:51],a[104:107],v[168:171]\n"
"ds_write_b128 v116,v[128:131] offset:16384\n"
"v_mfma_f32_16x16x32_bf16 v[168:171],v[52:55],a[108:111],v[168:171]\n"
"v_cvt_pk_bf16_f32 v138,v140,v141\n"
"v_cvt_pk_bf16_f32 v139,v142,v143\n"
"v_mfma_f32_16x16x32_bf16 v[172:175],v[60:63],a[108:111],v[172:175]\n"
"ds_write_b128 v116,v[136:139] offset:17408\n"
"v_mfma_f32_16x16x32_bf16 v[172:175],v[56:59],a[104:107],v[172:175]\n"
"v_cvt_pk_bf16_f32 v144,v144,v145\n"
"v_cvt_pk_bf16_f32 v145,v146,v147\n"
"v_mfma_f32_16x16x32_bf16 v[176:179],v[64:67],a[104:107],v[176:179]\n"
"v_cvt_pk_bf16_f32 v146,v148,v149\n"
"v_cvt_pk_bf16_f32 v147,v150,v151\n"
"v_mfma_f32_16x16x32_bf16 v[176:179],v[68:71],a[108:111],v[176:179]\n"
"ds_write_b128 v116,v[144:147] offset:18432\n"
"v_cvt_pk_bf16_f32 v152,v152,v153\n"
"v_mfma_f32_16x16x32_bf16 v[180:183],v[76:79],a[108:111],v[180:183]\n"
"v_cvt_pk_bf16_f32 v153,v154,v155\n"
"v_cvt_pk_bf16_f32 v154,v156,v157\n"
"v_cvt_pk_bf16_f32 v155,v158,v159\n"
"v_mfma_f32_16x16x32_bf16 v[180:183],v[72:75],a[104:107],v[180:183]\n"
"ds_write_b128 v116,v[152:155] offset:19456\n"
"v_mfma_f32_16x16x32_bf16 v[184:187],v[80:83],a[104:107],v[184:187]\n"
"ds_read_b128 a[0:3],v116 offset:16384\n"
"ds_read_b128 a[4:7],v116 offset:17408\n"
"v_mfma_f32_16x16x32_bf16 v[184:187],v[84:87],a[108:111],v[184:187]\n"
"ds_read_b128 a[8:11],v116 offset:18432\n"
"ds_read_b128 a[12:15],v116 offset:19456\n"
"v_mfma_f32_16x16x32_bf16 v[188:191],v[92:95],a[108:111],v[188:191]\n"
"v_mfma_f32_16x16x32_bf16 v[188:191],v[88:91],a[104:107],v[188:191]\n"
"v_mfma_f32_16x16x32_bf16 v[192:195],v[32:35],a[112:115],v[192:195]\n"
"v_mfma_f32_16x16x32_bf16 v[192:195],v[36:39],a[116:119],v[192:195]\n"
"v_cvt_pk_bf16_f32 v168,v168,v169\n"
"v_cvt_pk_bf16_f32 v169,v170,v171\n"
"v_mfma_f32_16x16x32_bf16 v[196:199],v[44:47],a[116:119],v[196:199]\n"
"v_cvt_pk_bf16_f32 v160,v160,v161\n"
"v_cvt_pk_bf16_f32 v161,v162,v163\n"
"v_mfma_f32_16x16x32_bf16 v[196:199],v[40:43],a[112:115],v[196:199]\n"
"v_cvt_pk_bf16_f32 v162,v164,v165\n"
"v_cvt_pk_bf16_f32 v163,v166,v167\n"
"v_mfma_f32_16x16x32_bf16 v[200:203],v[48:51],a[112:115],v[200:203]\n"
"ds_write_b128 v116,v[160:163] offset:49152\n"
"v_mfma_f32_16x16x32_bf16 v[200:203],v[52:55],a[116:119],v[200:203]\n"
"v_cvt_pk_bf16_f32 v170,v172,v173\n"
"v_cvt_pk_bf16_f32 v171,v174,v175\n"
"v_mfma_f32_16x16x32_bf16 v[204:207],v[60:63],a[116:119],v[204:207]\n"
"ds_write_b128 v116,v[168:171] offset:50176\n"
"v_mfma_f32_16x16x32_bf16 v[204:207],v[56:59],a[112:115],v[204:207]\n"
"v_cvt_pk_bf16_f32 v176,v176,v177\n"
"v_cvt_pk_bf16_f32 v177,v178,v179\n"
"v_mfma_f32_16x16x32_bf16 v[208:211],v[64:67],a[112:115],v[208:211]\n"
"v_cvt_pk_bf16_f32 v178,v180,v181\n"
"v_cvt_pk_bf16_f32 v179,v182,v183\n"
"v_mfma_f32_16x16x32_bf16 v[208:211],v[68:71],a[116:119],v[208:211]\n"
"ds_write_b128 v116,v[176:179] offset:51200\n"
"v_cvt_pk_bf16_f32 v184,v184,v185\n"
"v_mfma_f32_16x16x32_bf16 v[212:215],v[76:79],a[116:119],v[212:215]\n"
"v_cvt_pk_bf16_f32 v185,v186,v187\n"
"v_cvt_pk_bf16_f32 v186,v188,v189\n"
"v_cvt_pk_bf16_f32 v187,v190,v191\n"
"v_mfma_f32_16x16x32_bf16 v[212:215],v[72:75],a[112:115],v[212:215]\n"
"ds_write_b128 v116,v[184:187] offset:52224\n"
"v_mfma_f32_16x16x32_bf16 v[216:219],v[80:83],a[112:115],v[216:219]\n"
"ds_read_b128 a[16:19],v116 offset:49152\n"
"ds_read_b128 a[20:23],v116 offset:50176\n"
"v_mfma_f32_16x16x32_bf16 v[216:219],v[84:87],a[116:119],v[216:219]\n"
"ds_read_b128 a[24:27],v116 offset:51200\n"
"ds_read_b128 a[28:31],v116 offset:52224\n"
"v_mfma_f32_16x16x32_bf16 v[220:223],v[92:95],a[116:119],v[220:223]\n"
"s_lshl2_add_u32 s82,s60,s52\n"
"s_lshr_b32 s82,s82,1\n"
"s_cmp_eq_u32 s82,1\n"
"v_mfma_f32_16x16x32_bf16 v[220:223],v[88:91],a[112:115],v[220:223]\n"
"s_cbranch_scc1 .Lfk_head0\n"
"s_waitcnt lgkmcnt(0)\n"
"s_barrier\n"
"v_mfma_f32_16x16x32_bf16 v[24:27],a[64:67],a[48:51],v[108:111]\n"
"s_andn2_b32 m0,s55,16384\n"
"s_add_u32 s54,s54,s22\n"
"v_cvt_pk_bf16_f32 v200,v200,v201\n"
"v_mfma_f32_16x16x32_bf16 v[24:27],a[68:71],a[52:55],v[24:27]\n"
"buffer_load_dwordx4 v115,s[32:35],s54 offen sc1 lds\n"
"v_mfma_f32_16x16x32_bf16 v[24:27],a[72:75],a[56:59],v[24:27]\n"
"v_cvt_pk_bf16_f32 v192,v192,v193\n"
"v_cvt_pk_bf16_f32 v193,v194,v195\n"
"v_mfma_f32_16x16x32_bf16 v[24:27],a[76:79],a[60:63],v[24:27]\n"
"v_cvt_pk_bf16_f32 v194,v196,v197\n"
"v_cvt_pk_bf16_f32 v195,v198,v199\n"
"v_mfma_f32_16x16x32_bf16 v[28:31],a[92:95],a[60:63],v[108:111]\n"
"ds_write_b128 v116,v[192:195] offset:32768\n"
"v_mfma_f32_16x16x32_bf16 v[28:31],a[88:91],a[56:59],v[28:31]\n"
"v_cvt_pk_bf16_f32 v201,v202,v203\n"
"v_cvt_pk_bf16_f32 v202,v204,v205\n"
"v_cvt_pk_bf16_f32 v203,v206,v207\n"
"v_mfma_f32_16x16x32_bf16 v[28:31],a[84:87],a[52:55],v[28:31]\n"
"ds_write_b128 v116,v[200:203] offset:33792\n"
"v_mfma_f32_16x16x32_bf16 v[28:31],a[80:83],a[48:51],v[28:31]\n"
"v_cvt_pk_bf16_f32 v208,v208,v209\n"
"v_cvt_pk_bf16_f32 v209,v210,v211\n"
"v_mfma_f32_16x16x32_bf16 v[4:7],a[80:83],a[0:3],v[96:99]\n"
"v_cvt_pk_bf16_f32 v210,v212,v213\n"
"v_cvt_pk_bf16_f32 v211,v214,v215\n"
"v_mfma_f32_16x16x32_bf16 v[4:7],a[84:87],a[4:7],v[4:7]\n"
"ds_read_b128 a[96:99],v113 offset:16384\n"
"ds_read_b128 a[100:103],v113 offset:17408\n"
"v_mfma_f32_16x16x32_bf16 v[4:7],a[88:91],a[8:11],v[4:7]\n"
"ds_read_b128 a[104:107],v113 offset:18432\n"
"ds_read_b128 a[108:111],v113 offset:19456\n"
"v_mfma_f32_16x16x32_bf16 v[4:7],a[92:95],a[12:15],v[4:7]\n"
"ds_read_b128 a[112:115],v113 offset:20480\n"
"ds_read_b128 a[116:119],v113 offset:21504\n"
"v_mfma_f32_16x16x32_bf16 v[0:3],a[76:79],a[12:15],v[96:99]\n"
"ds_read_b128 a[120:123],v113 offset:22528\n"
"ds_read_b128 a[124:127],v113 offset:23552\n"
"v_mfma_f32_16x16x32_bf16 v[0:3],a[72:75],a[8:11],v[0:3]\n"
"v_max3_f32 v118,v24,v25,v26\n"
"v_max3_f32 v118,v118,v27,v28\n"
"v_mfma_f32_16x16x32_bf16 v[0:3],a[68:71],a[4:7],v[0:3]\n"
"v_max3_f32 v118,v118,v29,v30\n"
"v_max_f32 v118,v118,v31\n"
"v_mov_b32 v120,v118\n"
"v_mfma_f32_16x16x32_bf16 v[0:3],a[64:67],a[0:3],v[0:3]\n"
"ds_write_b128 v116,v[208:211] offset:34816\n"
"v_mfma_f32_16x16x32_bf16 v[8:11],a[64:67],a[16:19],v[100:103]\n"
"v_cvt_pk_bf16_f32 v216,v216,v217\n"
"v_cvt_pk_bf16_f32 v217,v218,v219\n"
"v_mfma_f32_16x16x32_bf16 v[8:11],a[68:71],a[20:23],v[8:11]\n"
"v_cvt_pk_bf16_f32 v218,v220,v221\n"
"v_cvt_pk_bf16_f32 v219,v222,v223\n"
"v_mfma_f32_16x16x32_bf16 v[8:11],a[72:75],a[24:27],v[8:11]\n"
"ds_write_b128 v116,v[216:219] offset:35840\n"
"ds_read_b128 a[32:35],v116 offset:32768\n"
"v_mfma_f32_16x16x32_bf16 v[8:11],a[76:79],a[28:31],v[8:11]\n"
"v_max3_f32 v119,v0,v1,v2\n"
"v_max3_f32 v119,v119,v3,v4\n"
"v_mfma_f32_16x16x32_bf16 v[12:15],a[92:95],a[28:31],v[100:103]\n"
"v_max3_f32 v119,v119,v5,v6\n"
"v_max_f32 v119,v119,v7\n"
"v_mfma_f32_16x16x32_bf16 v[12:15],a[88:91],a[24:27],v[12:15]\n"
"v_min_f32 v120,v120,v119\n"
"v_max_f32 v118,v118,v119\n"
"v_mfma_f32_16x16x32_bf16 v[12:15],a[84:87],a[20:23],v[12:15]\n"
"ds_read_b128 a[36:39],v116 offset:33792\n"
"ds_read_b128 a[40:43],v116 offset:34816\n"
"v_mfma_f32_16x16x32_bf16 v[12:15],a[80:83],a[16:19],v[12:15]\n"
"ds_read_b128 a[44:47],v116 offset:35840\n"
"s_waitcnt lgkmcnt(0)\n"
"v_mfma_f32_16x16x32_bf16 v[20:23],a[80:83],a[32:35],v[104:107]\n"
"v_mfma_f32_16x16x32_bf16 v[20:23],a[84:87],a[36:39],v[20:23]\n"
"v_mfma_f32_16x16x32_bf16 v[20:23],a[88:91],a[40:43],v[20:23]\n"
"buffer_store_dwordx4 a[224:227],v112,s[24:27],s56 offen offset:0 sc1\n"
"v_mfma_f32_16x16x32_bf16 v[20:23],a[92:95],a[44:47],v[20:23]\n"
"v_max3_f32 v119,v8,v9,v10\n"
"v_mfma_f32_16x16x32_bf16 v[16:19],a[76:79],a[44:47],v[104:107]\n"
"v_max3_f32 v119,v119,v11,v12\n"
"v_max3_f32 v119,v119,v13,v14\n"
"v_mfma_f32_16x16x32_bf16 v[16:19],a[72:75],a[40:43],v[16:19]\n"
"v_max_f32 v119,v119,v15\n"
"v_min_f32 v120,v120,v119\n"
"v_mfma_f32_16x16x32_bf16 v[16:19],a[68:71],a[36:39],v[16:19]\n"
"v_max_f32 v118,v118,v119\n"
"v_mfma_f32_16x16x32_bf16 v[16:19],a[64:67],a[32:35],v[16:19]\n"
"s_nop 7\n"
"v_max3_f32 v119,v16,v17,v18\n"
"v_max3_f32 v119,v119,v19,v20\n"
"v_max3_f32 v119,v119,v21,v22\n"
"v_max_f32 v119,v119,v23\n"
"v_min_f32 v120,v120,v119\n"
"v_max_f32 v118,v118,v119\n"
"v_cmp_gt_f32 s[96:97],s16,v120\n"
"v_cmp_lt_f32 vcc,s93,v118\n"
"s_nop 0\n"
"s_or_b64 s[96:97],s[96:97],vcc\n"
"s_cbranch_scc1 .Lfk_cs_h3\n"
".Lfk_cr_h3:\n"
"s_cmp_ge_u32 s52,2\n"
"s_cbranch_scc1 .Lfk_g0\n"
"s_waitcnt vmcnt(11) lgkmcnt(0)\n"
"s_barrier\n"
"v_mfma_f32_16x16x32_bf16 v[32:35],a[96:99],a[0:3],v[96:99]\n"
"s_add_u32 m0,s55,4096\n"
"v_exp_f32 v0,v0\n"
"v_mfma_f32_16x16x32_bf16 v[32:35],a[100:103],a[4:7],v[32:35]\n"
"s_add_u32 s54,s54,s22\n"
"v_exp_f32 v1,v1\n"
"v_mfma_f32_16x16x32_bf16 v[32:35],a[104:107],a[8:11],v[32:35]\n"
"buffer_load_dwordx4 v115,s[32:35],s54 offen sc1 lds\n"
"v_exp_f32 v2,v2\n"
"v_cvt_pk_bf16_f32 v64,v0,v1\n"
"v_mfma_f32_16x16x32_bf16 v[32:35],a[108:111],a[12:15],v[32:35]\n"
"s_add_u32 m0,s67,16384\n"
"v_exp_f32 v3,v3\n"
"v_mfma_f32_16x16x32_bf16 v[36:39],a[124:127],a[12:15],v[96:99]\n"
"v_exp_f32 v4,v4\n"
"v_cvt_pk_bf16_f32 v65,v2,v3\n"
"v_exp_f32 v5,v5\n"
"v_mfma_f32_16x16x32_bf16 v[36:39],a[120:123],a[8:11],v[36:39]\n"
"buffer_load_dwordx4 v112,s[24:27],s64 offen offset:0 sc1 lds\n"
"v_mfma_f32_16x16x32_bf16 v[36:39],a[116:119],a[4:7],v[36:39]\n"
"v_exp_f32 v6,v6\n"
"v_cvt_pk_bf16_f32 v66,v4,v5\n"
"v_exp_f32 v7,v7\n"
"v_mfma_f32_16x16x32_bf16 v[36:39],a[112:115],a[0:3],v[36:39]\n"
"buffer_load_dwordx4 v112,s[24:27],s64 offen offset:1024 sc1 lds\n"
"v_mfma_f32_16x16x32_bf16 v[44:47],a[112:115],a[16:19],v[100:103]\n"
"v_exp_f32 v8,v8\n"
"v_cvt_pk_bf16_f32 v67,v6,v7\n"
"v_exp_f32 v9,v9\n"
"v_mfma_f32_16x16x32_bf16 v[44:47],a[116:119],a[20:23],v[44:47]\n"
"ds_read_b128 a[64:67],v113 offset:49152\n"
"ds_read_b128 a[68:71],v113 offset:50176\n"
"v_exp_f32 v10,v10\n"
"v_mfma_f32_16x16x32_bf16 v[44:47],a[120:123],a[24:27],v[44:47]\n"
"ds_read_b128 a[72:75],v113 offset:51200\n"
"ds_read_b128 a[76:79],v113 offset:52224\n"
"v_exp_f32 v11,v11\n"
"v_mfma_f32_16x16x32_bf16 v[44:47],a[124:127],a[28:31],v[44:47]\n"
"ds_read_b128 a[80:83],v113 offset:53248\n"
"ds_read_b128 a[84:87],v113 offset:54272\n"
"v_exp_f32 v12,v12\n"
"v_mfma_f32_16x16x32_bf16 v[40:43],a[108:111],a[28:31],v[100:103]\n"
"ds_read_b128 a[88:91],v113 offset:55296\n"
"ds_read_b128 a[92:95],v113 offset:56320\n"
"v_exp_f32 v13,v13\n"
"v_mfma_f32_16x16x32_bf16 v[40:43],a[104:107],a[24:27],v[40:43]\n"
"buffer_load_dwordx4 v112,s[24:27],s64 offen offset:2048 sc1 lds\n"
"v_exp_f32 v14,v14\n"
"v_mfma_f32_16x16x32_bf16 v[40:43],a[100:103],a[20:23],v[40:43]\n"
"buffer_load_dwordx4 v112,s[24:27],s64 offen offset:3072 sc1 lds\n"
"v_mfma_f32_16x16x32_bf16 v[40:43],a[96:99],a[16:19],v[40:43]\n"
"s_add_u32 m0,s67,20480\n"
"v_exp_f32 v15,v15\n"
"v_mfma_f32_16x16x32_bf16 v[48:51],a[96:99],a[32:35],v[104:107]\n"
"buffer_load_dwordx4 v117,s[24:27],s64 offen offset:0 sc1 lds\n"
"buffer_load_dwordx4 v117,s[24:27],s64 offen offset:1024 sc1 lds\n"
"v_exp_f32 v16,v16\n"
"v_exp_f32 v17,v17\n"
"v_mfma_f32_16x16x32_bf16 v[48:51],a[100:103],a[36:39],v[48:51]\n"
"buffer_load_dwordx4 v117,s[24:27],s64 offen offset:2048 sc1 lds\n"
"v_mfma_f32_16x16x32_bf16 v[48:51],a[104:107],a[40:43],v[48:51]\n"
"v_exp_f32 v18,v18\n"
"v_cvt_pk_bf16_f32 v68,v8,v9\n"
"v_cvt_pk_bf16_f32 v69,v10,v11\n"
"v_exp_f32 v19,v19\n"
"v_mfma_f32_16x16x32_bf16 v[48:51],a[108:111],a[44:47],v[48:51]\n"
"buffer_load_dwordx4 v117,s[24:27],s64 offen offset:3072 sc1 lds\n"
"v_mfma_f32_16x16x32_bf16 v[52:55],a[124:127],a[44:47],v[104:107]\n"
"s_add_u32 s64,s64,16384\n"
"v_exp_f32 v20,v20\n"
"v_cvt_pk_bf16_f32 v70,v12,v13\n"
"v_cvt_pk_bf16_f32 v71,v14,v15\n"
"v_mfma_f32_16x16x32_bf16 v[52:55],a[120:123],a[40:43],v[52:55]\n"
"v_exp_f32 v21,v21\n"
"v_cvt_pk_bf16_f32 v72,v16,v17\n"
"v_cvt_pk_bf16_f32 v73,v18,v19\n"
"v_mfma_f32_16x16x32_bf16 v[52:55],a[116:119],a[36:39],v[52:55]\n"
"v_exp_f32 v22,v22\n"
"v_cvt_pk_bf16_f32 v74,v20,v21\n"
"v_mfma_f32_16x16x32_bf16 v[52:55],a[112:115],a[32:35],v[52:55]\n"
"v_exp_f32 v23,v23\n"
"v_mfma_f32_16x16x32_bf16 v[60:63],a[112:115],a[48:51],v[108:111]\n"
"v_exp_f32 v24,v24\n"
"v_cvt_pk_bf16_f32 v75,v22,v23\n"
"v_mfma_f32_16x16x32_bf16 v[60:63],a[116:119],a[52:55],v[60:63]\n"
"v_exp_f32 v25,v25\n"
"v_mfma_f32_16x16x32_bf16 v[60:63],a[120:123],a[56:59],v[60:63]\n"
"v_exp_f32 v26,v26\n"
"v_exp_f32 v27,v27\n"
"v_mfma_f32_16x16x32_bf16 v[60:63],a[124:127],a[60:63],v[60:63]\n"
"buffer_store_dwordx4 a[228:231],v112,s[24:27],s56 offen offset:1024 sc1\n"
"v_mfma_f32_16x16x32_bf16 v[56:59],a[108:111],a[60:63],v[108:111]\n"
"v_exp_f32 v28,v28\n"
"v_cvt_pk_bf16_f32 v77,v26,v27\n"
"v_mfma_f32_16x16x32_bf16 v[56:59],a[104:107],a[56:59],v[56:59]\n"
"v_exp_f32 v29,v29\n"
"v_mfma_f32_16x16x32_bf16 v[56:59],a[100:103],a[52:55],v[56:59]\n"
"v_exp_f32 v30,v30\n"
"v_cvt_pk_bf16_f32 v78,v28,v29\n"
"v_mfma_f32_16x16x32_bf16 v[56:59],a[96:99],a[48:51],v[56:59]\n"
"s_waitcnt vmcnt(17) lgkmcnt(0)\n"
"v_exp_f32 v31,v31\n"
"v_cvt_pk_bf16_f32 v76,v24,v25\n"
"v_cvt_pk_bf16_f32 v79,v30,v31\n"
"s_barrier\n"
"v_mfma_f32_16x16x32_bf16 v[0:3],a[64:67],a[0:3],v[96:99]\n"
"s_add_u32 m0,s55,8192\n"
"v_exp_f32 v122,v32\n"
"v_mfma_f32_16x16x32_bf16 v[0:3],a[68:71],a[4:7],v[0:3]\n"
"s_add_u32 s54,s54,s22\n"
"v_exp_f32 v123,v33\n"
"v_mfma_f32_16x16x32_bf16 v[0:3],a[72:75],a[8:11],v[0:3]\n"
"ds_read_b128 a[96:99],v114 offset:40960\n"
"ds_read_b128 a[100:103],v114 offset:41984\n"
"v_mfma_f32_16x16x32_bf16 v[0:3],a[76:79],a[12:15],v[0:3]\n"
"v_exp_f32 v119,v34\n"
"v_mfma_f32_16x16x32_bf16 v[4:7],a[92:95],a[12:15],v[96:99]\n"
"ds_read_b128 a[104:107],v114 offset:43008\n"
"v_cvt_pk_bf16_f32 v80,v122,v123\n"
"v_mfma_f32_16x16x32_bf16 v[4:7],a[88:91],a[8:11],v[4:7]\n"
"v_exp_f32 v120,v35\n"
"v_mfma_f32_16x16x32_bf16 v[4:7],a[84:87],a[4:7],v[4:7]\n"
"ds_read_b128 a[108:111],v114 offset:44032\n"
"ds_read_b128 a[112:115],v114 offset:45056\n"
"v_mfma_f32_16x16x32_bf16 v[4:7],a[80:83],a[0:3],v[4:7]\n"
"v_exp_f32 v122,v36\n"
"v_mfma_f32_16x16x32_bf16 v[12:15],a[80:83],a[16:19],v[100:103]\n"
"ds_read_b128 a[116:119],v114 offset:46080\n"
"v_cvt_pk_bf16_f32 v81,v119,v120\n"
"v_mfma_f32_16x16x32_bf16 v[12:15],a[84:87],a[20:23],v[12:15]\n"
"v_exp_f32 v123,v37\n"
"v_mfma_f32_16x16x32_bf16 v[12:15],a[88:91],a[24:27],v[12:15]\n"
"ds_read_b128 a[120:123],v114 offset:47104\n"
"ds_read_b128 a[124:127],v114 offset:48128\n"
"v_mfma_f32_16x16x32_bf16 v[12:15],a[92:95],a[28:31],v[12:15]\n"
"v_exp_f32 v119,v38\n"
"v_cvt_pk_bf16_f32 v82,v122,v123\n"
"v_mfma_f32_16x16x32_bf16 v[8:11],a[76:79],a[28:31],v[100:103]\n"
"buffer_store_dwordx4 a[168:171],v112,s[24:27],s3 offen offset:2048 sc1\n"
"v_mfma_f32_16x16x32_bf16 v[8:11],a[72:75],a[24:27],v[8:11]\n"
"v_exp_f32 v120,v39\n"
"v_mfma_f32_16x16x32_bf16 v[8:11],a[68:71],a[20:23],v[8:11]\n"
"buffer_load_dwordx4 v115,s[32:35],s54 offen sc1 lds\n"
"v_mfma_f32_16x16x32_bf16 v[8:11],a[64:67],a[16:19],v[8:11]\n"
"s_add_u32 m0,s67,49152\n"
"v_exp_f32 v122,v40\n"
"v_mfma_f32_16x16x32_bf16 v[16:19],a[64:67],a[32:35],v[104:107]\n"
"v_cvt_pk_bf16_f32 v83,v119,v120\n"
"v_or3_b32 v118,v80,v81,v82\n"
"v_mfma_f32_16x16x32_bf16 v[16:19],a[68:71],a[36:39],v[16:19]\n"
"v_exp_f32 v123,v41\n"
"v_mfma_f32_16x16x32_bf16 v[16:19],a[72:75],a[40:43],v[16:19]\n"
"buffer_load_dwordx4 v112,s[24:27],s64 offen offset:0 sc1 lds\n"
"v_mfma_f32_16x16x32_bf16 v[16:19],a[76:79],a[44:47],v[16:19]\n"
"v_exp_f32 v119,v42\n"
"v_mfma_f32_16x16x32_bf16 v[20:23],a[92:95],a[44:47],v[104:107]\n"
"v_cvt_pk_bf16_f32 v84,v122,v123\n"
"v_or3_b32 v118,v118,v83,v84\n"
"v_mfma_f32_16x16x32_bf16 v[20:23],a[88:91],a[40:43],v[20:23]\n"
"v_exp_f32 v120,v43\n"
"v_mfma_f32_16x16x32_bf16 v[20:23],a[84:87],a[36:39],v[20:23]\n"
"buffer_load_dwordx4 v112,s[24:27],s64 offen offset:1024 sc1 lds\n"
"v_mfma_f32_16x16x32_bf16 v[20:23],a[80:83],a[32:35],v[20:23]\n"
"v_exp_f32 v122,v44\n"
"v_mfma_f32_16x16x32_bf16 v[28:31],a[80:83],a[48:51],v[108:111]\n"
"v_cvt_pk_bf16_f32 v85,v119,v120\n"
"v_mfma_f32_16x16x32_bf16 v[28:31],a[84:87],a[52:55],v[28:31]\n"
"v_exp_f32 v123,v45\n"
"v_mfma_f32_16x16x32_bf16 v[28:31],a[88:91],a[56:59],v[28:31]\n"
"buffer_load_dwordx4 v112,s[24:27],s64 offen offset:2048 sc1 lds\n"
"v_mfma_f32_16x16x32_bf16 v[28:31],a[92:95],a[60:63],v[28:31]\n"
"v_exp_f32 v119,v46\n"
"v_cvt_pk_bf16_f32 v86,v122,v123\n"
"v_mfma_f32_16x16x32_bf16 v[24:27],a[76:79],a[60:63],v[108:111]\n"
"buffer_store_dwordx4 a[172:175],v112,s[24:27],s3 offen offset:3072 sc1\n"
"v_mfma_f32_16x16x32_bf16 v[24:27],a[72:75],a[56:59],v[24:27]\n"
"v_exp_f32 v120,v47\n"
"v_mfma_f32_16x16x32_bf16 v[24:27],a[68:71],a[52:55],v[24:27]\n"
"buffer_load_dwordx4 v112,s[24:27],s64 offen offset:3072 sc1 lds\n"
"v_mfma_f32_16x16x32_bf16 v[24:27],a[64:67],a[48:51],v[24:27]\n"
"s_waitcnt lgkmcnt(0)\n"
"v_exp_f32 v122,v48\n"
"v_mfma_f32_16x16x32_bf16 v[128:131],a[96:99],v[64:67],0\n"
"v_exp_f32 v123,v49\n"
"v_mfma_f32_16x16x32_bf16 v[160:163],a[96:99],v[68:71],0\n"
"ds_read_b128 a[64:67],v113 offset:32768\n"
"v_cvt_pk_bf16_f32 v87,v119,v120\n"
"v_mfma_f32_16x16x32_bf16 v[192:195],a[96:99],v[72:75],0\n"
"s_add_u32 m0,s67,53248\n"
"v_exp_f32 v119,v50\n"
"v_mfma_f32_16x16x32_bf16 v[224:227],a[96:99],v[76:79],0\n"
"ds_read_b128 a[68:71],v113 offset:33792\n"
"v_cvt_pk_bf16_f32 v88,v122,v123\n"
"v_mfma_f32_16x16x32_bf16 a[140:143],a[144:147],v[76:79],0\n"
"v_exp_f32 v120,v51\n"
"v_mfma_f32_16x16x32_bf16 v[228:231],a[100:103],v[76:79],0\n"
"ds_read_b128 a[72:75],v113 offset:34816\n"
"ds_read_b128 a[76:79],v113 offset:35840\n"
"v_mfma_f32_16x16x32_bf16 v[132:135],a[100:103],v[64:67],0\n"
"v_exp_f32 v122,v52\n"
"v_mfma_f32_16x16x32_bf16 v[164:167],a[100:103],v[68:71],0\n"
"ds_read_b128 a[80:83],v113 offset:36864\n"
"v_cvt_pk_bf16_f32 v89,v119,v120\n"
"v_mfma_f32_16x16x32_bf16 v[196:199],a[100:103],v[72:75],0\n"
"v_exp_f32 v123,v53\n"
"v_mfma_f32_16x16x32_bf16 a[136:139],a[144:147],v[72:75],0\n"
"ds_read_b128 a[84:87],v113 offset:37888\n"
"ds_read_b128 a[88:91],v113 offset:38912\n"
"v_mfma_f32_16x16x32_bf16 v[200:203],a[104:107],v[72:75],0\n"
"v_exp_f32 v119,v54\n"
"v_mfma_f32_16x16x32_bf16 v[232:235],a[104:107],v[76:79],0\n"
"ds_read_b128 a[92:95],v113 offset:39936\n"
"v_cvt_pk_bf16_f32 v90,v122,v123\n"
"v_mfma_f32_16x16x32_bf16 v[136:139],a[104:107],v[64:67],0\n"
"v_exp_f32 v120,v55\n"
"v_mfma_f32_16x16x32_bf16 v[168:171],a[104:107],v[68:71],0\n"
"buffer_load_dwordx4 v117,s[24:27],s64 offen offset:0 sc1 lds\n"
"v_mfma_f32_16x16x32_bf16 a[132:135],a[144:147],v[68:71],0\n"
"v_exp_f32 v122,v56\n"
"v_mfma_f32_16x16x32_bf16 v[172:175],a[108:111],v[68:71],0\n"
"v_cvt_pk_bf16_f32 v91,v119,v120\n"
"s_sub_u32 s3,s3,16384\n"
"v_mfma_f32_16x16x32_bf16 v[204:207],a[108:111],v[72:75],0\n"
"v_exp_f32 v123,v57\n"
"v_mfma_f32_16x16x32_bf16 v[236:239],a[108:111],v[76:79],0\n"
"buffer_load_dwordx4 v117,s[24:27],s64 offen offset:1024 sc1 lds\n"
"v_mfma_f32_16x16x32_bf16 v[140:143],a[108:111],v[64:67],0\n"
"v_exp_f32 v119,v58\n"
"v_cvt_pk_bf16_f32 v92,v122,v123\n"
"v_mfma_f32_16x16x32_bf16 a[128:131],a[144:147],v[64:67],0\n"
"buffer_store_dwordx4 a[176:179],v112,s[24:27],s3 offen offset:0 sc1\n"
"v_mfma_f32_16x16x32_bf16 v[144:147],a[112:115],v[64:67],0\n"
"v_exp_f32 v120,v59\n"
"v_mfma_f32_16x16x32_bf16 v[176:179],a[112:115],v[68:71],0\n"
"buffer_load_dwordx4 v117,s[24:27],s64 offen offset:2048 sc1 lds\n"
"v_mfma_f32_16x16x32_bf16 v[208:211],a[112:115],v[72:75],0\n"
"v_exp_f32 v122,v60\n"
"v_mfma_f32_16x16x32_bf16 v[240:243],a[112:115],v[76:79],0\n"
"v_cvt_pk_bf16_f32 v93,v119,v120\n"
"v_or3_b32 v118,v118,v85,v86\n"
"v_mfma_f32_16x16x32_bf16 v[244:247],a[116:119],v[76:79],0\n"
"v_exp_f32 v123,v61\n"
"v_mfma_f32_16x16x32_bf16 v[148:151],a[116:119],v[64:67],0\n"
"buffer_load_dwordx4 v117,s[24:27],s64 offen offset:3072 sc1 lds\n"
"v_mfma_f32_16x16x32_bf16 v[180:183],a[116:119],v[68:71],0\n"
"s_add_u32 s64,s64,16384\n"
"v_exp_f32 v119,v62\n"
"v_cvt_pk_bf16_f32 v94,v122,v123\n"
"v_mfma_f32_16x16x32_bf16 v[212:215],a[116:119],v[72:75],0\n"
"buffer_store_dwordx4 a[232:235],v112,s[24:27],s56 offen offset:2048 sc1\n"
"v_mfma_f32_16x16x32_bf16 v[216:219],a[120:123],v[72:75],0\n"
"v_exp_f32 v120,v63\n"
"v_mfma_f32_16x16x32_bf16 v[248:251],a[120:123],v[76:79],0\n"
"v_cvt_pk_bf16_f32 v95,v119,v120\n"
"v_or3_b32 v118,v118,v87,v88\n"
"v_mfma_f32_16x16x32_bf16 v[152:155],a[120:123],v[64:67],0\n"
"v_or3_b32 v118,v118,v89,v90\n"
"v_or3_b32 v118,v118,v91,v92\n"
"v_mfma_f32_16x16x32_bf16 v[184:187],a[120:123],v[68:71],0\n"
"v_or3_b32 v118,v118,v93,v94\n"
"v_or_b32 v118,v118,v95\n"
"v_mfma_f32_16x16x32_bf16 v[188:191],a[124:127],v[68:71],0\n"
"v_and_b32 v118,0x40004000,v118\n"
"v_cmp_ne_u32 vcc,0,v118\n"
"v_mfma_f32_16x16x32_bf16 v[220:223],a[124:127],v[72:75],0\n"
"v_mfma_f32_16x16x32_bf16 v[252:255],a[124:127],v[76:79],0\n"
"v_mfma_f32_16x16x32_bf16 v[156:159],a[124:127],v[64:67],0\n"
"s_cbranch_vccnz .Lfk_cs_h1\n"
".Lfk_cr_h1:\n"
"s_waitcnt lgkmcnt(0)\n"
"s_barrier\n"
"v_mfma_f32_16x16x32_bf16 v[32:35],a[64:67],a[0:3],v[96:99]\n"
"s_add_u32 m0,s55,12288\n"
"v_exp_f32 v122,v0\n"
"v_mfma_f32_16x16x32_bf16 v[32:35],a[68:71],a[4:7],v[32:35]\n"
"s_add_u32 s54,s54,s22\n"
"v_exp_f32 v123,v1\n"
"v_mfma_f32_16x16x32_bf16 v[32:35],a[72:75],a[8:11],v[32:35]\n"
"ds_read_b128 a[96:99],v114 offset:8192\n"
"ds_read_b128 a[100:103],v114 offset:9216\n"
"v_mfma_f32_16x16x32_bf16 v[32:35],a[76:79],a[12:15],v[32:35]\n"
"v_exp_f32 v119,v2\n"
"v_mfma_f32_16x16x32_bf16 v[36:39],a[92:95],a[12:15],v[96:99]\n"
"ds_read_b128 a[104:107],v114 offset:10240\n"
"v_cvt_pk_bf16_f32 v64,v122,v123\n"
"v_mfma_f32_16x16x32_bf16 v[36:39],a[88:91],a[8:11],v[36:39]\n"
"v_exp_f32 v120,v3\n"
"v_mfma_f32_16x16x32_bf16 v[36:39],a[84:87],a[4:7],v[36:39]\n"
"ds_read_b128 a[108:111],v114 offset:11264\n"
"ds_read_b128 a[112:115],v114 offset:12288\n"
"v_mfma_f32_16x16x32_bf16 v[36:39],a[80:83],a[0:3],v[36:39]\n"
"v_exp_f32 v122,v4\n"
"v_mfma_f32_16x16x32_bf16 v[44:47],a[80:83],a[16:19],v[100:103]\n"
"ds_read_b128 a[116:119],v114 offset:13312\n"
"v_cvt_pk_bf16_f32 v65,v119,v120\n"
"v_mfma_f32_16x16x32_bf16 v[44:47],a[84:87],a[20:23],v[44:47]\n"
"v_exp_f32 v123,v5\n"
"v_mfma_f32_16x16x32_bf16 v[44:47],a[88:91],a[24:27],v[44:47]\n"
"ds_read_b128 a[120:123],v114 offset:14336\n"
"ds_read_b128 a[124:127],v114 offset:15360\n"
"v_mfma_f32_16x16x32_bf16 v[44:47],a[92:95],a[28:31],v[44:47]\n"
"v_exp_f32 v119,v6\n"
"v_cvt_pk_bf16_f32 v66,v122,v123\n"
"v_mfma_f32_16x16x32_bf16 v[40:43],a[76:79],a[28:31],v[100:103]\n"
"buffer_store_dwordx4 a[180:183],v112,s[24:27],s3 offen offset:1024 sc1\n"
"v_mfma_f32_16x16x32_bf16 v[40:43],a[72:75],a[24:27],v[40:43]\n"
"v_exp_f32 v120,v7\n"
"v_mfma_f32_16x16x32_bf16 v[40:43],a[68:71],a[20:23],v[40:43]\n"
"buffer_load_dwordx4 v115,s[32:35],s54 offen sc1 lds\n"
"v_mfma_f32_16x16x32_bf16 v[40:43],a[64:67],a[16:19],v[40:43]\n"
"s_add_u32 m0,s67,32768\n"
"v_exp_f32 v122,v8\n"
"v_mfma_f32_16x16x32_bf16 v[48:51],a[64:67],a[32:35],v[104:107]\n"
"v_cvt_pk_bf16_f32 v67,v119,v120\n"
"v_or3_b32 v118,v64,v65,v66\n"
"v_mfma_f32_16x16x32_bf16 v[48:51],a[68:71],a[36:39],v[48:51]\n"
"v_exp_f32 v123,v9\n"
"v_mfma_f32_16x16x32_bf16 v[48:51],a[72:75],a[40:43],v[48:51]\n"
"buffer_load_dwordx4 v112,s[24:27],s64 offen offset:0 sc1 lds\n"
"v_mfma_f32_16x16x32_bf16 v[48:51],a[76:79],a[44:47],v[48:51]\n"
"v_exp_f32 v119,v10\n"
"v_mfma_f32_16x16x32_bf16 v[52:55],a[92:95],a[44:47],v[104:107]\n"
"v_cvt_pk_bf16_f32 v68,v122,v123\n"
"v_or3_b32 v118,v118,v67,v68\n"
"v_mfma_f32_16x16x32_bf16 v[52:55],a[88:91],a[40:43],v[52:55]\n"
"v_exp_f32 v120,v11\n"
"v_mfma_f32_16x16x32_bf16 v[52:55],a[84:87],a[36:39],v[52:55]\n"
"buffer_load_dwordx4 v112,s[24:27],s64 offen offset:1024 sc1 lds\n"
"v_mfma_f32_16x16x32_bf16 v[52:55],a[80:83],a[32:35],v[52:55]\n"
"v_exp_f32 v122,v12\n"
"v_mfma_f32_16x16x32_bf16 v[60:63],a[80:83],a[48:51],v[108:111]\n"
"v_cvt_pk_bf16_f32 v69,v119,v120\n"
"v_mfma_f32_16x16x32_bf16 v[60:63],a[84:87],a[52:55],v[60:63]\n"
"v_exp_f32 v123,v13\n"
"v_mfma_f32_16x16x32_bf16 v[60:63],a[88:91],a[56:59],v[60:63]\n"
"buffer_load_dwordx4 v112,s[24:27],s64 offen offset:2048 sc1 lds\n"
"v_mfma_f32_16x16x32_bf16 v[60:63],a[92:95],a[60:63],v[60:63]\n"
"v_exp_f32 v119,v14\n"
"v_cvt_pk_bf16_f32 v70,v122,v123\n"
"v_mfma_f32_16x16x32_bf16 v[56:59],a[76:79],a[60:63],v[108:111]\n"
"buffer_store_dwordx4 a[184:187],v112,s[24:27],s3 offen offset:2048 sc1\n"
"v_mfma_f32_16x16x32_bf16 v[56:59],a[72:75],a[56:59],v[56:59]\n"
"v_exp_f32 v120,v15\n"
"v_mfma_f32_16x16x32_bf16 v[56:59],a[68:71],a[52:55],v[56:59]\n"
"buffer_load_dwordx4 v112,s[24:27],s64 offen offset:3072 sc1 lds\n"
"v_mfma_f32_16x16x32_bf16 v[56:59],a[64:67],a[48:51],v[56:59]\n"
"s_waitcnt lgkmcnt(0)\n"
"v_exp_f32 v122,v16\n"
"v_mfma_f32_16x16x32_bf16 v[128:131],a[96:99],v[80:83],v[128:131]\n"
"v_exp_f32 v123,v17\n"
"v_mfma_f32_16x16x32_bf16 v[160:163],a[96:99],v[84:87],v[160:163]\n"
"ds_read_b128 a[64:67],v113 offset:0\n"
"v_cvt_pk_bf16_f32 v71,v119,v120\n"
"v_mfma_f32_16x16x32_bf16 v[192:195],a[96:99],v[88:91],v[192:195]\n"
"s_add_u32 m0,s67,36864\n"
"v_exp_f32 v119,v18\n"
"v_mfma_f32_16x16x32_bf16 v[224:227],a[96:99],v[92:95],v[224:227]\n"
"ds_read_b128 a[68:71],v113 offset:1024\n"
"v_cvt_pk_bf16_f32 v72,v122,v123\n"
"v_mfma_f32_16x16x32_bf16 a[140:143],a[144:147],v[92:95],a[140:143]\n"
"v_exp_f32 v120,v19\n"
"v_mfma_f32_16x16x32_bf16 v[228:231],a[100:103],v[92:95],v[228:231]\n"
"ds_read_b128 a[72:75],v113 offset:2048\n"
"ds_read_b128 a[76:79],v113 offset:3072\n"
"v_mfma_f32_16x16x32_bf16 v[132:135],a[100:103],v[80:83],v[132:135]\n"
"v_exp_f32 v122,v20\n"
"v_mfma_f32_16x16x32_bf16 v[164:167],a[100:103],v[84:87],v[164:167]\n"
"ds_read_b128 a[80:83],v113 offset:4096\n"
"v_cvt_pk_bf16_f32 v73,v119,v120\n"
"v_mfma_f32_16x16x32_bf16 v[196:199],a[100:103],v[88:91],v[196:199]\n"
"v_exp_f32 v123,v21\n"
"v_mfma_f32_16x16x32_bf16 a[136:139],a[144:147],v[88:91],a[136:139]\n"
"ds_read_b128 a[84:87],v113 offset:5120\n"
"ds_read_b128 a[88:91],v113 offset:6144\n"
"v_mfma_f32_16x16x32_bf16 v[200:203],a[104:107],v[88:91],v[200:203]\n"
"v_exp_f32 v119,v22\n"
"v_mfma_f32_16x16x32_bf16 v[232:235],a[104:107],v[92:95],v[232:235]\n"
"ds_read_b128 a[92:95],v113 offset:7168\n"
"v_cvt_pk_bf16_f32 v74,v122,v123\n"
"v_mfma_f32_16x16x32_bf16 v[136:139],a[104:107],v[80:83],v[136:139]\n"
"v_exp_f32 v120,v23\n"
"v_mfma_f32_16x16x32_bf16 v[168:171],a[104:107],v[84:87],v[168:171]\n"
"buffer_load_dwordx4 v117,s[24:27],s64 offen offset:0 sc1 lds\n"
"v_mfma_f32_16x16x32_bf16 a[132:135],a[144:147],v[84:87],a[132:135]\n"
"v_exp_f32 v122,v24\n"
"v_cvt_pk_bf16_f32 v75,v119,v120\n"
"v_mfma_f32_16x16x32_bf16 v[172:175],a[108:111],v[84:87],v[172:175]\n"
"buffer_store_dwordx4 a[188:191],v112,s[24:27],s3 offen offset:3072 sc1\n"
"v_mfma_f32_16x16x32_bf16 v[204:207],a[108:111],v[88:91],v[204:207]\n"
"v_exp_f32 v123,v25\n"
"v_mfma_f32_16x16x32_bf16 v[236:239],a[108:111],v[92:95],v[236:239]\n"
"buffer_load_dwordx4 v117,s[24:27],s64 offen offset:1024 sc1 lds\n"
"v_mfma_f32_16x16x32_bf16 v[140:143],a[108:111],v[80:83],v[140:143]\n"
"v_exp_f32 v119,v26\n"
"v_mfma_f32_16x16x32_bf16 a[128:131],a[144:147],v[80:83],a[128:131]\n"
"v_cvt_pk_bf16_f32 v76,v122,v123\n"
"s_or_b32 s3,s3,0x3c000\n"
"v_mfma_f32_16x16x32_bf16 v[144:147],a[112:115],v[80:83],v[144:147]\n"
"v_exp_f32 v120,v27\n"
"v_mfma_f32_16x16x32_bf16 v[176:179],a[112:115],v[84:87],v[176:179]\n"
"buffer_load_dwordx4 v117,s[24:27],s64 offen offset:2048 sc1 lds\n"
"v_mfma_f32_16x16x32_bf16 v[208:211],a[112:115],v[88:91],v[208:211]\n"
"v_exp_f32 v122,v28\n"
"v_mfma_f32_16x16x32_bf16 v[240:243],a[112:115],v[92:95],v[240:243]\n"
"v_cvt_pk_bf16_f32 v77,v119,v120\n"
"v_or3_b32 v118,v118,v69,v70\n"
"v_mfma_f32_16x16x32_bf16 v[244:247],a[116:119],v[92:95],v[244:247]\n"
"v_exp_f32 v123,v29\n"
"v_mfma_f32_16x16x32_bf16 v[148:151],a[116:119],v[80:83],v[148:151]\n"
"buffer_load_dwordx4 v117,s[24:27],s64 offen offset:3072 sc1 lds\n"
"v_mfma_f32_16x16x32_bf16 v[180:183],a[116:119],v[84:87],v[180:183]\n"
"s_add_u32 s64,s64,16384\n"
"v_exp_f32 v119,v30\n"
"v_cvt_pk_bf16_f32 v78,v122,v123\n"
"v_mfma_f32_16x16x32_bf16 v[212:215],a[116:119],v[88:91],v[212:215]\n"
"buffer_store_dwordx4 a[236:239],v112,s[24:27],s56 offen offset:3072 sc1\n"
"v_mfma_f32_16x16x32_bf16 v[216:219],a[120:123],v[88:91],v[216:219]\n"
"v_exp_f32 v120,v31\n"
"v_mfma_f32_16x16x32_bf16 v[248:251],a[120:123],v[92:95],v[248:251]\n"
"v_cvt_pk_bf16_f32 v79,v119,v120\n"
"v_or3_b32 v118,v118,v71,v72\n"
"v_mfma_f32_16x16x32_bf16 v[152:155],a[120:123],v[80:83],v[152:155]\n"
"v_or3_b32 v118,v118,v73,v74\n"
"v_or3_b32 v118,v118,v75,v76\n"
"v_mfma_f32_16x16x32_bf16 v[184:187],a[120:123],v[84:87],v[184:187]\n"
"v_or3_b32 v118,v118,v77,v78\n"
"v_or_b32 v118,v118,v79\n"
"v_mfma_f32_16x16x32_bf16 v[188:191],a[124:127],v[84:87],v[188:191]\n"
"v_and_b32 v118,0x40004000,v118\n"
"v_cmp_ne_u32 vcc,0,v118\n"
"v_mfma_f32_16x16x32_bf16 v[220:223],a[124:127],v[88:91],v[220:223]\n"
"v_mfma_f32_16x16x32_bf16 v[252:255],a[124:127],v[92:95],v[252:255]\n"
"v_mfma_f32_16x16x32_bf16 v[156:159],a[124:127],v[80:83],v[156:159]\n"
"s_cbranch_vccnz .Lfk_cs_h2\n"
".Lfk_cr_h2:\n"
"s_waitcnt vmcnt(27) lgkmcnt(0)\n"
"s_barrier\n"
".Lfk_g1:\n"
"s_and_b32 s82,s78,4096\n"
"s_lshl1_add_u32 s82,s82,69632\n"
"s_cmp_lt_u32 s52,2\n"
"s_cselect_b32 s64,s64,s62\n"
"s_cselect_b32 s67,s67,s82\n"
"s_add_u32 s89,s89,1\n"
"s_cmp_ge_u32 s89,s60\n"
"s_cbranch_scc1 .Lfk_Fx\n"
".Lfk_F:\n"
"v_mfma_f32_16x16x32_bf16 v[0:3],a[64:67],a[0:3],v[96:99]\n"
"s_swappc_b64 s[94:95],s[20:21]\n"
"v_exp_f32 v122,v32\n"
"v_mfma_f32_16x16x32_bf16 v[0:3],a[68:71],a[4:7],v[0:3]\n"
"v_exp_f32 v123,v33\n"
"v_mfma_f32_16x16x32_bf16 v[0:3],a[72:75],a[8:11],v[0:3]\n"
"ds_read_b128 a[96:99],v114 offset:24576\n"
"ds_read_b128 a[100:103],v114 offset:25600\n"
"v_mfma_f32_16x16x32_bf16 v[0:3],a[76:79],a[12:15],v[0:3]\n"
"v_exp_f32 v119,v34\n"
"v_mfma_f32_16x16x32_bf16 v[4:7],a[92:95],a[12:15],v[96:99]\n"
"s_add_u32 s101,s101,4\n"
"ds_read_b128 a[104:107],v114 offset:26624\n"
"v_cvt_pk_bf16_f32 v80,v122,v123\n"
"v_mfma_f32_16x16x32_bf16 v[4:7],a[88:91],a[8:11],v[4:7]\n"
"v_exp_f32 v120,v35\n"
"v_mfma_f32_16x16x32_bf16 v[4:7],a[84:87],a[4:7],v[4:7]\n"
"ds_read_b128 a[108:111],v114 offset:27648\n"
"ds_read_b128 a[112:115],v114 offset:28672\n"
"v_mfma_f32_16x16x32_bf16 v[4:7],a[80:83],a[0:3],v[4:7]\n"
"v_exp_f32 v122,v36\n"
"v_mfma_f32_16x16x32_bf16 v[12:15],a[80:83],a[16:19],v[100:103]\n"
"ds_read_b128 a[116:119],v114 offset:29696\n"
"v_cvt_pk_bf16_f32 v81,v119,v120\n"
"v_mfma_f32_16x16x32_bf16 v[12:15],a[84:87],a[20:23],v[12:15]\n"
"v_exp_f32 v123,v37\n"
"v_mfma_f32_16x16x32_bf16 v[12:15],a[88:91],a[24:27],v[12:15]\n"
"ds_read_b128 a[120:123],v114 offset:30720\n"
"ds_read_b128 a[124:127],v114 offset:31744\n"
"v_mfma_f32_16x16x32_bf16 v[12:15],a[92:95],a[28:31],v[12:15]\n"
"s_add_u32 m0,s67,0\n"
"v_exp_f32 v119,v38\n"
"v_mfma_f32_16x16x32_bf16 v[8:11],a[76:79],a[28:31],v[100:103]\n"
"v_cvt_pk_bf16_f32 v82,v122,v123\n"
"v_or3_b32 v118,v80,v81,v82\n"
"v_mfma_f32_16x16x32_bf16 v[8:11],a[72:75],a[24:27],v[8:11]\n"
"v_exp_f32 v120,v39\n"
"v_mfma_f32_16x16x32_bf16 v[8:11],a[68:71],a[20:23],v[8:11]\n"
"buffer_load_dwordx4 v112,s[24:27],s64 offen offset:0 sc1 lds\n"
"v_mfma_f32_16x16x32_bf16 v[8:11],a[64:67],a[16:19],v[8:11]\n"
"v_exp_f32 v122,v40\n"
"v_mfma_f32_16x16x32_bf16 v[16:19],a[64:67],a[32:35],v[104:107]\n"
"v_cvt_pk_bf16_f32 v83,v119,v120\n"
"v_mfma_f32_16x16x32_bf16 v[16:19],a[68:71],a[36:39],v[16:19]\n"
"v_exp_f32 v123,v41\n"
"v_mfma_f32_16x16x32_bf16 v[16:19],a[72:75],a[40:43],v[16:19]\n"
"v_cvt_pk_bf16_f32 v84,v122,v123\n"
"v_or3_b32 v118,v118,v83,v84\n"
"v_mfma_f32_16x16x32_bf16 v[16:19],a[76:79],a[44:47],v[16:19]\n"
"v_exp_f32 v119,v42\n"
"v_mfma_f32_16x16x32_bf16 v[20:23],a[92:95],a[44:47],v[104:107]\n"
"v_mfma_f32_16x16x32_bf16 v[20:23],a[88:91],a[40:43],v[20:23]\n"
"v_exp_f32 v120,v43\n"
"v_mfma_f32_16x16x32_bf16 v[20:23],a[84:87],a[36:39],v[20:23]\n"
"buffer_load_dwordx4 v112,s[24:27],s64 offen offset:1024 sc1 lds\n"
"v_mfma_f32_16x16x32_bf16 v[20:23],a[80:83],a[32:35],v[20:23]\n"
"v_exp_f32 v122,v44\n"
"v_mfma_f32_16x16x32_bf16 v[28:31],a[80:83],a[48:51],v[108:111]\n"
"v_cvt_pk_bf16_f32 v85,v119,v120\n"
"v_mfma_f32_16x16x32_bf16 v[28:31],a[84:87],a[52:55],v[28:31]\n"
"v_exp_f32 v123,v45\n"
"v_mfma_f32_16x16x32_bf16 v[28:31],a[88:91],a[56:59],v[28:31]\n"
"v_cvt_pk_bf16_f32 v86,v122,v123\n"
"v_or3_b32 v118,v118,v85,v86\n"
"v_mfma_f32_16x16x32_bf16 v[28:31],a[92:95],a[60:63],v[28:31]\n"
"v_exp_f32 v119,v46\n"
"v_mfma_f32_16x16x32_bf16 v[24:27],a[76:79],a[60:63],v[108:111]\n"
"v_mfma_f32_16x16x32_bf16 v[24:27],a[72:75],a[56:59],v[24:27]\n"
"v_exp_f32 v120,v47\n"
"v_mfma_f32_16x16x32_bf16 v[24:27],a[68:71],a[52:55],v[24:27]\n"
"v_cvt_pk_bf16_f32 v87,v119,v120\n"
"v_mfma_f32_16x16x32_bf16 v[24:27],a[64:67],a[48:51],v[24:27]\n"
"s_waitcnt lgkmcnt(0)\n"
"v_exp_f32 v122,v48\n"
"v_mfma_f32_16x16x32_bf16 v[128:131],a[96:99],v[64:67],v[128:131]\n"
"v_exp_f32 v123,v49\n"
"v_mfma_f32_16x16x32_bf16 v[160:163],a[96:99],v[68:71],v[160:163]\n"
"ds_read_b128 a[64:67],v113 offset:16384\n"
"ds_read_b128 a[68:71],v113 offset:17408\n"
"v_mfma_f32_16x16x32_bf16 v[192:195],a[96:99],v[72:75],v[192:195]\n"
"v_exp_f32 v119,v50\n"
"v_mfma_f32_16x16x32_bf16 v[224:227],a[96:99],v[76:79],v[224:227]\n"
"ds_read_b128 a[72:75],v113 offset:18432\n"
"v_cvt_pk_bf16_f32 v88,v122,v123\n"
"v_mfma_f32_16x16x32_bf16 a[140:143],a[144:147],v[76:79],a[140:143]\n"
"v_exp_f32 v120,v51\n"
"v_mfma_f32_16x16x32_bf16 v[228:231],a[100:103],v[76:79],v[228:231]\n"
"ds_read_b128 a[76:79],v113 offset:19456\n"
"ds_read_b128 a[80:83],v113 offset:20480\n"
"v_mfma_f32_16x16x32_bf16 v[132:135],a[100:103],v[64:67],v[132:135]\n"
"v_exp_f32 v122,v52\n"
"v_mfma_f32_16x16x32_bf16 v[164:167],a[100:103],v[68:71],v[164:167]\n"
"ds_read_b128 a[84:87],v113 offset:21504\n"
"v_cvt_pk_bf16_f32 v89,v119,v120\n"
"v_mfma_f32_16x16x32_bf16 v[196:199],a[100:103],v[72:75],v[196:199]\n"
"v_exp_f32 v123,v53\n"
"v_mfma_f32_16x16x32_bf16 a[136:139],a[144:147],v[72:75],a[136:139]\n"
"ds_read_b128 a[88:91],v113 offset:22528\n"
"ds_read_b128 a[92:95],v113 offset:23552\n"
"v_mfma_f32_16x16x32_bf16 v[200:203],a[104:107],v[72:75],v[200:203]\n"
"v_exp_f32 v119,v54\n"
"v_mfma_f32_16x16x32_bf16 v[232:235],a[104:107],v[76:79],v[232:235]\n"
"v_cvt_pk_bf16_f32 v90,v122,v123\n"
"v_or3_b32 v118,v118,v87,v88\n"
"v_mfma_f32_16x16x32_bf16 v[136:139],a[104:107],v[64:67],v[136:139]\n"
"v_exp_f32 v120,v55\n"
"v_mfma_f32_16x16x32_bf16 v[168:171],a[104:107],v[68:71],v[168:171]\n"
"s_waitcnt vmcnt(9)\n"
"ds_add_u32 v124,a152\n"
"v_mfma_f32_16x16x32_bf16 a[132:135],a[144:147],v[68:71],a[132:135]\n"
"v_exp_f32 v122,v56\n"
"v_mfma_f32_16x16x32_bf16 v[172:175],a[108:111],v[68:71],v[172:175]\n"
"v_cvt_pk_bf16_f32 v91,v119,v120\n"
"v_or3_b32 v118,v118,v89,v90\n"
"v_mfma_f32_16x16x32_bf16 v[204:207],a[108:111],v[72:75],v[204:207]\n"
"v_exp_f32 v123,v57\n"
"v_mfma_f32_16x16x32_bf16 v[236:239],a[108:111],v[76:79],v[236:239]\n"
"buffer_load_dwordx4 v112,s[24:27],s64 offen offset:2048 sc1 lds\n"
"v_mfma_f32_16x16x32_bf16 v[140:143],a[108:111],v[64:67],v[140:143]\n"
"v_exp_f32 v119,v58\n"
"v_mfma_f32_16x16x32_bf16 a[128:131],a[144:147],v[64:67],a[128:131]\n"
"v_cvt_pk_bf16_f32 v92,v122,v123\n"
"v_or3_b32 v118,v118,v91,v92\n"
"v_mfma_f32_16x16x32_bf16 v[144:147],a[112:115],v[64:67],v[144:147]\n"
"v_exp_f32 v120,v59\n"
"v_mfma_f32_16x16x32_bf16 v[176:179],a[112:115],v[68:71],v[176:179]\n"
"buffer_load_dwordx4 v112,s[24:27],s64 offen offset:3072 sc1 lds\n"
"v_mfma_f32_16x16x32_bf16 v[208:211],a[112:115],v[72:75],v[208:211]\n"
"s_add_u32 s64,s64,16384\n"
"v_exp_f32 v122,v60\n"
"v_mfma_f32_16x16x32_bf16 v[240:243],a[112:115],v[76:79],v[240:243]\n"
"v_cvt_pk_bf16_f32 v93,v119,v120\n"
"s_andn2_b32 m0,s55,16384\n"
"v_mfma_f32_16x16x32_bf16 v[244:247],a[116:119],v[76:79],v[244:247]\n"
"s_add_u32 s54,s54,s22\n"
"v_exp_f32 v123,v61\n"
"v_mfma_f32_16x16x32_bf16 v[148:151],a[116:119],v[64:67],v[148:151]\n"
"buffer_load_dwordx4 v115,s[32:35],s54 offen sc1 lds\n"
"v_mfma_f32_16x16x32_bf16 v[180:183],a[116:119],v[68:71],v[180:183]\n"
"v_exp_f32 v119,v62\n"
"v_mfma_f32_16x16x32_bf16 v[212:215],a[116:119],v[72:75],v[212:215]\n"
"ds_read_b32 v125,v124\n"
"v_cvt_pk_bf16_f32 v94,v122,v123\n"
"v_mfma_f32_16x16x32_bf16 v[216:219],a[120:123],v[72:75],v[216:219]\n"
"v_exp_f32 v120,v63\n"
"v_mfma_f32_16x16x32_bf16 v[248:251],a[120:123],v[76:79],v[248:251]\n"
"buffer_store_dwordx4 a[240:243],v112,s[24:27],s56 offen offset:0 sc1\n"
"v_mfma_f32_16x16x32_bf16 v[152:155],a[120:123],v[64:67],v[152:155]\n"
"v_cvt_pk_bf16_f32 v95,v119,v120\n"
"v_or3_b32 v118,v118,v93,v94\n"
"v_mfma_f32_16x16x32_bf16 v[184:187],a[120:123],v[68:71],v[184:187]\n"
"v_mfma_f32_16x16x32_bf16 v[188:191],a[124:127],v[68:71],v[188:191]\n"
"s_waitcnt lgkmcnt(0)\n"
"v_subrev_u32 v125,s101,v125\n"
"v_or3_b32 v125,v118,v95,v125\n"
"v_mfma_f32_16x16x32_bf16 v[220:223],a[124:127],v[72:75],v[220:223]\n"
"v_and_b32 v125,0x40004000,v125\n"
"v_cmp_ne_u32 vcc,0,v125\n"
"v_mfma_f32_16x16x32_bf16 v[252:255],a[124:127],v[76:79],v[252:255]\n"
"v_mfma_f32_16x16x32_bf16 v[156:159],a[124:127],v[64:67],v[156:159]\n"
"s_cbranch_vccnz .Lfk_cs_f3\n"
".Lfk_cr_f3:\n"
"v_mfma_f32_16x16x32_bf16 v[32:35],a[64:67],a[0:3],v[96:99]\n"
"s_add_u32 s101,s101,4\n"
"v_exp_f32 v122,v0\n"
"v_mfma_f32_16x16x32_bf16 v[32:35],a[68:71],a[4:7],v[32:35]\n"
"s_add_u32 m0,s55,4096\n"
"v_exp_f32 v123,v1\n"
"v_mfma_f32_16x16x32_bf16 v[32:35],a[72:75],a[8:11],v[32:35]\n"
"ds_read_b128 a[96:99],v114 offset:57344\n"
"ds_read_b128 a[100:103],v114 offset:58368\n"
"v_mfma_f32_16x16x32_bf16 v[32:35],a[76:79],a[12:15],v[32:35]\n"
"s_add_u32 s54,s54,s22\n"
"v_exp_f32 v119,v2\n"
"v_mfma_f32_16x16x32_bf16 v[36:39],a[92:95],a[12:15],v[96:99]\n"
"ds_read_b128 a[104:107],v114 offset:59392\n"
"v_cvt_pk_bf16_f32 v64,v122,v123\n"
"v_mfma_f32_16x16x32_bf16 v[36:39],a[88:91],a[8:11],v[36:39]\n"
"v_exp_f32 v120,v3\n"
"v_mfma_f32_16x16x32_bf16 v[36:39],a[84:87],a[4:7],v[36:39]\n"
"ds_read_b128 a[108:111],v114 offset:60416\n"
"ds_read_b128 a[112:115],v114 offset:61440\n"
"v_mfma_f32_16x16x32_bf16 v[36:39],a[80:83],a[0:3],v[36:39]\n"
"v_exp_f32 v122,v4\n"
"v_mfma_f32_16x16x32_bf16 v[44:47],a[80:83],a[16:19],v[100:103]\n"
"ds_read_b128 a[116:119],v114 offset:62464\n"
"v_cvt_pk_bf16_f32 v65,v119,v120\n"
"v_mfma_f32_16x16x32_bf16 v[44:47],a[84:87],a[20:23],v[44:47]\n"
"v_exp_f32 v123,v5\n"
"v_mfma_f32_16x16x32_bf16 v[44:47],a[88:91],a[24:27],v[44:47]\n"
"ds_read_b128 a[120:123],v114 offset:63488\n"
"ds_read_b128 a[124:127],v114 offset:64512\n"
"v_mfma_f32_16x16x32_bf16 v[44:47],a[92:95],a[28:31],v[44:47]\n"
"v_exp_f32 v119,v6\n"
"v_mfma_f32_16x16x32_bf16 v[40:43],a[76:79],a[28:31],v[100:103]\n"
"v_cvt_pk_bf16_f32 v66,v122,v123\n"
"v_or3_b32 v118,v64,v65,v66\n"
"v_mfma_f32_16x16x32_bf16 v[40:43],a[72:75],a[24:27],v[40:43]\n"
"v_exp_f32 v120,v7\n"
"v_mfma_f32_16x16x32_bf16 v[40:43],a[68:71],a[20:23],v[40:43]\n"
"buffer_load_dwordx4 v115,s[32:35],s54 offen sc1 lds\n"
"v_mfma_f32_16x16x32_bf16 v[40:43],a[64:67],a[16:19],v[40:43]\n"
"s_add_u32 m0,s67,16384\n"
"v_exp_f32 v122,v8\n"
"v_mfma_f32_16x16x32_bf16 v[48:51],a[64:67],a[32:35],v[104:107]\n"
"v_cvt_pk_bf16_f32 v67,v119,v120\n"
"v_mfma_f32_16x16x32_bf16 v[48:51],a[68:71],a[36:39],v[48:51]\n"
"v_exp_f32 v123,v9\n"
"v_mfma_f32_16x16x32_bf16 v[48:51],a[72:75],a[40:43],v[48:51]\n"
"buffer_load_dwordx4 v112,s[24:27],s64 offen offset:0 sc1 lds\n"
"v_mfma_f32_16x16x32_bf16 v[48:51],a[76:79],a[44:47],v[48:51]\n"
"v_exp_f32 v119,v10\n"
"v_mfma_f32_16x16x32_bf16 v[52:55],a[92:95],a[44:47],v[104:107]\n"
"v_cvt_pk_bf16_f32 v68,v122,v123\n"
"v_or3_b32 v118,v118,v67,v68\n"
"v_mfma_f32_16x16x32_bf16 v[52:55],a[88:91],a[40:43],v[52:55]\n"
"v_exp_f32 v120,v11\n"
"v_mfma_f32_16x16x32_bf16 v[52:55],a[84:87],a[36:39],v[52:55]\n"
"buffer_load_dwordx4 v112,s[24:27],s64 offen offset:1024 sc1 lds\n"
"v_mfma_f32_16x16x32_bf16 v[52:55],a[80:83],a[32:35],v[52:55]\n"
"v_exp_f32 v122,v12\n"
"v_mfma_f32_16x16x32_bf16 v[60:63],a[80:83],a[48:51],v[108:111]\n"
"v_cvt_pk_bf16_f32 v69,v119,v120\n"
"v_mfma_f32_16x16x32_bf16 v[60:63],a[84:87],a[52:55],v[60:63]\n"
"v_exp_f32 v123,v13\n"
"v_mfma_f32_16x16x32_bf16 v[60:63],a[88:91],a[56:59],v[60:63]\n"
"v_cvt_pk_bf16_f32 v70,v122,v123\n"
"v_or3_b32 v118,v118,v69,v70\n"
"v_mfma_f32_16x16x32_bf16 v[60:63],a[92:95],a[60:63],v[60:63]\n"
"v_exp_f32 v119,v14\n"
"v_mfma_f32_16x16x32_bf16 v[56:59],a[76:79],a[60:63],v[108:111]\n"
"v_mfma_f32_16x16x32_bf16 v[56:59],a[72:75],a[56:59],v[56:59]\n"
"v_exp_f32 v120,v15\n"
"v_mfma_f32_16x16x32_bf16 v[56:59],a[68:71],a[52:55],v[56:59]\n"
"v_cvt_pk_bf16_f32 v71,v119,v120\n"
"v_mfma_f32_16x16x32_bf16 v[56:59],a[64:67],a[48:51],v[56:59]\n"
"s_waitcnt lgkmcnt(0)\n"
"v_exp_f32 v122,v16\n"
"v_mfma_f32_16x16x32_bf16 v[128:131],a[96:99],v[80:83],v[128:131]\n"
"v_exp_f32 v123,v17\n"
"v_mfma_f32_16x16x32_bf16 v[160:163],a[96:99],v[84:87],v[160:163]\n"
"ds_read_b128 a[64:67],v113 offset:49152\n"
"ds_read_b128 a[68:71],v113 offset:50176\n"
"v_mfma_f32_16x16x32_bf16 v[192:195],a[96:99],v[88:91],v[192:195]\n"
"v_exp_f32 v119,v18\n"
"v_mfma_f32_16x16x32_bf16 v[224:227],a[96:99],v[92:95],v[224:227]\n"
"ds_read_b128 a[72:75],v113 offset:51200\n"
"v_cvt_pk_bf16_f32 v72,v122,v123\n"
"v_mfma_f32_16x16x32_bf16 a[140:143],a[144:147],v[92:95],a[140:143]\n"
"v_exp_f32 v120,v19\n"
"v_mfma_f32_16x16x32_bf16 v[228:231],a[100:103],v[92:95],v[228:231]\n"
"ds_read_b128 a[76:79],v113 offset:52224\n"
"ds_read_b128 a[80:83],v113 offset:53248\n"
"v_mfma_f32_16x16x32_bf16 v[132:135],a[100:103],v[80:83],v[132:135]\n"
"v_exp_f32 v122,v20\n"
"v_mfma_f32_16x16x32_bf16 v[164:167],a[100:103],v[84:87],v[164:167]\n"
"ds_read_b128 a[84:87],v113 offset:54272\n"
"v_cvt_pk_bf16_f32 v73,v119,v120\n"
"v_mfma_f32_16x16x32_bf16 v[196:199],a[100:103],v[88:91],v[196:199]\n"
"v_exp_f32 v123,v21\n"
"v_mfma_f32_16x16x32_bf16 a[136:139],a[144:147],v[88:91],a[136:139]\n"
"ds_read_b128 a[88:91],v113 offset:55296\n"
"ds_read_b128 a[92:95],v113 offset:56320\n"
"v_mfma_f32_16x16x32_bf16 v[200:203],a[104:107],v[88:91],v[200:203]\n"
"v_exp_f32 v119,v22\n"
"v_mfma_f32_16x16x32_bf16 v[232:235],a[104:107],v[92:95],v[232:235]\n"
"v_cvt_pk_bf16_f32 v74,v122,v123\n"
"v_or3_b32 v118,v118,v71,v72\n"
"v_mfma_f32_16x16x32_bf16 v[136:139],a[104:107],v[80:83],v[136:139]\n"
"v_exp_f32 v120,v23\n"
"v_mfma_f32_16x16x32_bf16 v[168:171],a[104:107],v[84:87],v[168:171]\n"
"s_waitcnt vmcnt(10)\n"
"ds_add_u32 v124,a152\n"
"v_mfma_f32_16x16x32_bf16 a[132:135],a[144:147],v[84:87],a[132:135]\n"
"v_exp_f32 v122,v24\n"
"v_mfma_f32_16x16x32_bf16 v[172:175],a[108:111],v[84:87],v[172:175]\n"
"v_cvt_pk_bf16_f32 v75,v119,v120\n"
"v_or3_b32 v118,v118,v73,v74\n"
"v_mfma_f32_16x16x32_bf16 v[204:207],a[108:111],v[88:91],v[204:207]\n"
"v_exp_f32 v123,v25\n"
"v_mfma_f32_16x16x32_bf16 v[236:239],a[108:111],v[92:95],v[236:239]\n"
"buffer_load_dwordx4 v112,s[24:27],s64 offen offset:2048 sc1 lds\n"
"v_mfma_f32_16x16x32_bf16 v[140:143],a[108:111],v[80:83],v[140:143]\n"
"v_exp_f32 v119,v26\n"
"v_mfma_f32_16x16x32_bf16 a[128:131],a[144:147],v[80:83],a[128:131]\n"
"v_cvt_pk_bf16_f32 v76,v122,v123\n"
"v_or3_b32 v118,v118,v75,v76\n"
"v_mfma_f32_16x16x32_bf16 v[144:147],a[112:115],v[80:83],v[144:147]\n"
"v_exp_f32 v120,v27\n"
"v_mfma_f32_16x16x32_bf16 v[176:179],a[112:115],v[84:87],v[176:179]\n"
"buffer_load_dwordx4 v112,s[24:27],s64 offen offset:3072 sc1 lds\n"
"v_mfma_f32_16x16x32_bf16 v[208:211],a[112:115],v[88:91],v[208:211]\n"
"s_add_u32 s64,s64,16384\n"
"v_exp_f32 v122,v28\n"
"v_mfma_f32_16x16x32_bf16 v[240:243],a[112:115],v[92:95],v[240:243]\n"
"v_cvt_pk_bf16_f32 v77,v119,v120\n"
"v_mfma_f32_16x16x32_bf16 v[244:247],a[116:119],v[92:95],v[244:247]\n"
"v_exp_f32 v123,v29\n"
"v_mfma_f32_16x16x32_bf16 v[148:151],a[116:119],v[80:83],v[148:151]\n"
"v_cvt_pk_bf16_f32 v78,v122,v123\n"
"v_or3_b32 v118,v118,v77,v78\n"
"v_mfma_f32_16x16x32_bf16 v[180:183],a[116:119],v[84:87],v[180:183]\n"
"v_exp_f32 v119,v30\n"
"v_mfma_f32_16x16x32_bf16 v[212:215],a[116:119],v[88:91],v[212:215]\n"
"ds_read_b32 v125,v124\n"
"buffer_store_dwordx4 a[244:247],v112,s[24:27],s56 offen offset:1024 sc1\n"
"v_mfma_f32_16x16x32_bf16 v[216:219],a[120:123],v[88:91],v[216:219]\n"
"v_exp_f32 v120,v31\n"
"v_mfma_f32_16x16x32_bf16 v[248:251],a[120:123],v[92:95],v[248:251]\n"
"v_cvt_pk_bf16_f32 v79,v119,v120\n"
"v_mfma_f32_16x16x32_bf16 v[152:155],a[120:123],v[80:83],v[152:155]\n"
"v_mfma_f32_16x16x32_bf16 v[184:187],a[120:123],v[84:87],v[184:187]\n"
"v_mfma_f32_16x16x32_bf16 v[188:191],a[124:127],v[84:87],v[188:191]\n"
"s_waitcnt lgkmcnt(0)\n"
"v_subrev_u32 v125,s101,v125\n"
"v_or3_b32 v125,v118,v79,v125\n"
"v_mfma_f32_16x16x32_bf16 v[220:223],a[124:127],v[88:91],v[220:223]\n"
"v_and_b32 v125,0x40004000,v125\n"
"v_cmp_ne_u32 vcc,0,v125\n"
"v_mfma_f32_16x16x32_bf16 v[252:255],a[124:127],v[92:95],v[252:255]\n"
"v_mfma_f32_16x16x32_bf16 v[156:159],a[124:127],v[80:83],v[156:159]\n"
"s_cbranch_vccnz .Lfk_cs_f0\n"
".Lfk_cr_f0:\n"
"v_mfma_f32_16x16x32_bf16 v[0:3],a[64:67],a[0:3],v[96:99]\n"
"s_add_u32 s101,s101,4\n"
"v_exp_f32 v122,v32\n"
"v_mfma_f32_16x16x32_bf16 v[0:3],a[68:71],a[4:7],v[0:3]\n"
"s_add_u32 m0,s55,8192\n"
"v_exp_f32 v123,v33\n"
"v_mfma_f32_16x16x32_bf16 v[0:3],a[72:75],a[8:11],v[0:3]\n"
"ds_read_b128 a[96:99],v114 offset:40960\n"
"ds_read_b128 a[100:103],v114 offset:41984\n"
"v_mfma_f32_16x16x32_bf16 v[0:3],a[76:79],a[12:15],v[0:3]\n"
"s_add_u32 s54,s54,s22\n"
"v_exp_f32 v119,v34\n"
"v_mfma_f32_16x16x32_bf16 v[4:7],a[92:95],a[12:15],v[96:99]\n"
"ds_read_b128 a[104:107],v114 offset:43008\n"
"v_cvt_pk_bf16_f32 v80,v122,v123\n"
"v_mfma_f32_16x16x32_bf16 v[4:7],a[88:91],a[8:11],v[4:7]\n"
"v_exp_f32 v120,v35\n"
"v_mfma_f32_16x16x32_bf16 v[4:7],a[84:87],a[4:7],v[4:7]\n"
"ds_read_b128 a[108:111],v114 offset:44032\n"
"ds_read_b128 a[112:115],v114 offset:45056\n"
"v_mfma_f32_16x16x32_bf16 v[4:7],a[80:83],a[0:3],v[4:7]\n"
"v_exp_f32 v122,v36\n"
"v_mfma_f32_16x16x32_bf16 v[12:15],a[80:83],a[16:19],v[100:103]\n"
"ds_read_b128 a[116:119],v114 offset:46080\n"
"v_cvt_pk_bf16_f32 v81,v119,v120\n"
"v_mfma_f32_16x16x32_bf16 v[12:15],a[84:87],a[20:23],v[12:15]\n"
"v_exp_f32 v123,v37\n"
"v_mfma_f32_16x16x32_bf16 v[12:15],a[88:91],a[24:27],v[12:15]\n"
"ds_read_b128 a[120:123],v114 offset:47104\n"
"ds_read_b128 a[124:127],v114 offset:48128\n"
"v_mfma_f32_16x16x32_bf16 v[12:15],a[92:95],a[28:31],v[12:15]\n"
"v_exp_f32 v119,v38\n"
"v_mfma_f32_16x16x32_bf16 v[8:11],a[76:79],a[28:31],v[100:103]\n"
"v_cvt_pk_bf16_f32 v82,v122,v123\n"
"v_or3_b32 v118,v80,v81,v82\n"
"v_mfma_f32_16x16x32_bf16 v[8:11],a[72:75],a[24:27],v[8:11]\n"
"v_exp_f32 v120,v39\n"
"v_mfma_f32_16x16x32_bf16 v[8:11],a[68:71],a[20:23],v[8:11]\n"
"buffer_load_dwordx4 v115,s[32:35],s54 offen sc1 lds\n"
"v_mfma_f32_16x16x32_bf16 v[8:11],a[64:67],a[16:19],v[8:11]\n"
"s_add_u32 m0,s67,49152\n"
"v_exp_f32 v122,v40\n"
"v_mfma_f32_16x16x32_bf16 v[16:19],a[64:67],a[32:35],v[104:107]\n"
"v_cvt_pk_bf16_f32 v83,v119,v120\n"
"v_mfma_f32_16x16x32_bf16 v[16:19],a[68:71],a[36:39],v[16:19]\n"
"v_exp_f32 v123,v41\n"
"v_mfma_f32_16x16x32_bf16 v[16:19],a[72:75],a[40:43],v[16:19]\n"
"buffer_load_dwordx4 v112,s[24:27],s64 offen offset:0 sc1 lds\n"
"v_mfma_f32_16x16x32_bf16 v[16:19],a[76:79],a[44:47],v[16:19]\n"
"v_exp_f32 v119,v42\n"
"v_mfma_f32_16x16x32_bf16 v[20:23],a[92:95],a[44:47],v[104:107]\n"
"v_cvt_pk_bf16_f32 v84,v122,v123\n"
"v_or3_b32 v118,v118,v83,v84\n"
"v_mfma_f32_16x16x32_bf16 v[20:23],a[88:91],a[40:43],v[20:23]\n"
"v_exp_f32 v120,v43\n"
"v_mfma_f32_16x16x32_bf16 v[20:23],a[84:87],a[36:39],v[20:23]\n"
"buffer_load_dwordx4 v112,s[24:27],s64 offen offset:1024 sc1 lds\n"
"v_mfma_f32_16x16x32_bf16 v[20:23],a[80:83],a[32:35],v[20:23]\n"
"v_exp_f32 v122,v44\n"
"v_mfma_f32_16x16x32_bf16 v[28:31],a[80:83],a[48:51],v[108:111]\n"
"v_cvt_pk_bf16_f32 v85,v119,v120\n"
"v_mfma_f32_16x16x32_bf16 v[28:31],a[84:87],a[52:55],v[28:31]\n"
"v_exp_f32 v123,v45\n"
"v_mfma_f32_16x16x32_bf16 v[28:31],a[88:91],a[56:59],v[28:31]\n"
"v_cvt_pk_bf16_f32 v86,v122,v123\n"
"v_or3_b32 v118,v118,v85,v86\n"
"v_mfma_f32_16x16x32_bf16 v[28:31],a[92:95],a[60:63],v[28:31]\n"
"v_exp_f32 v119,v46\n"
"v_mfma_f32_16x16x32_bf16 v[24:27],a[76:79],a[60:63],v[108:111]\n"
"v_mfma_f32_16x16x32_bf16 v[24:27],a[72:75],a[56:59],v[24:27]\n"
"v_exp_f32 v120,v47\n"
"v_mfma_f32_16x16x32_bf16 v[24:27],a[68:71],a[52:55],v[24:27]\n"
"v_cvt_pk_bf16_f32 v87,v119,v120\n"
"v_mfma_f32_16x16x32_bf16 v[24:27],a[64:67],a[48:51],v[24:27]\n"
"s_waitcnt lgkmcnt(0)\n"
"v_exp_f32 v122,v48\n"
"v_mfma_f32_16x16x32_bf16 v[128:131],a[96:99],v[64:67],v[128:131]\n"
"v_exp_f32 v123,v49\n"
"v_mfma_f32_16x16x32_bf16 v[160:163],a[96:99],v[68:71],v[160:163]\n"
"ds_read_b128 a[64:67],v113 offset:32768\n"
"ds_read_b128 a[68:71],v113 offset:33792\n"
"v_mfma_f32_16x16x32_bf16 v[192:195],a[96:99],v[72:75],v[192:195]\n"
"v_exp_f32 v119,v50\n"
"v_mfma_f32_16x16x32_bf16 v[224:227],a[96:99],v[76:79],v[224:227]\n"
"ds_read_b128 a[72:75],v113 offset:34816\n"
"v_cvt_pk_bf16_f32 v88,v122,v123\n"
"v_mfma_f32_16x16x32_bf16 a[140:143],a[144:147],v[76:79],a[140:143]\n"
"v_exp_f32 v120,v51\n"
"v_mfma_f32_16x16x32_bf16 v[228:231],a[100:103],v[76:79],v[228:231]\n"
"ds_read_b128 a[76:79],v113 offset:35840\n"
"ds_read_b128 a[80:83],v113 offset:36864\n"
"v_mfma_f32_16x16x32_bf16 v[132:135],a[100:103],v[64:67],v[132:135]\n"
"v_exp_f32 v122,v52\n"
"v_mfma_f32_16x16x32_bf16 v[164:167],a[100:103],v[68:71],v[164:167]\n"
"ds_read_b128 a[84:87],v113 offset:37888\n"
"v_cvt_pk_bf16_f32 v89,v119,v120\n"
"v_mfma_f32_16x16x32_bf16 v[196:199],a[100:103],v[72:75],v[196:199]\n"
"v_exp_f32 v123,v53\n"
"v_mfma_f32_16x16x32_bf16 a[136:139],a[144:147],v[72:75],a[136:139]\n"
"ds_read_b128 a[88:91],v113 offset:38912\n"
"ds_read_b128 a[92:95],v113 offset:39936\n"
"v_mfma_f32_16x16x32_bf16 v[200:203],a[104:107],v[72:75],v[200:203]\n"
"v_exp_f32 v119,v54\n"
"v_mfma_f32_16x16x32_bf16 v[232:235],a[104:107],v[76:79],v[232:235]\n"
"v_cvt_pk_bf16_f32 v90,v122,v123\n"
"v_or3_b32 v118,v118,v87,v88\n"
"v_mfma_f32_16x16x32_bf16 v[136:139],a[104:107],v[64:67],v[136:139]\n"
"v_exp_f32 v120,v55\n"
"v_mfma_f32_16x16x32_bf16 v[168:171],a[104:107],v[68:71],v[168:171]\n"
"s_waitcnt vmcnt(10)\n"
"ds_add_u32 v124,a152\n"
"v_mfma_f32_16x16x32_bf16 a[132:135],a[144:147],v[68:71],a[132:135]\n"
"v_exp_f32 v122,v56\n"
"v_mfma_f32_16x16x32_bf16 v[172:175],a[108:111],v[68:71],v[172:175]\n"
"v_cvt_pk_bf16_f32 v91,v119,v120\n"
"v_or3_b32 v118,v118,v89,v90\n"
"v_mfma_f32_16x16x32_bf16 v[204:207],a[108:111],v[72:75],v[204:207]\n"
"v_exp_f32 v123,v57\n"
"v_mfma_f32_16x16x32_bf16 v[236:239],a[108:111],v[76:79],v[236:239]\n"
"buffer_load_dwordx4 v112,s[24:27],s64 offen offset:2048 sc1 lds\n"
"v_mfma_f32_16x16x32_bf16 v[140:143],a[108:111],v[64:67],v[140:143]\n"
"v_exp_f32 v119,v58\n"
"v_mfma_f32_16x16x32_bf16 a[128:131],a[144:147],v[64:67],a[128:131]\n"
"v_cvt_pk_bf16_f32 v92,v122,v123\n"
"v_or3_b32 v118,v118,v91,v92\n"
"v_mfma_f32_16x16x32_bf16 v[144:147],a[112:115],v[64:67],v[144:147]\n"
"v_exp_f32 v120,v59\n"
"v_mfma_f32_16x16x32_bf16 v[176:179],a[112:115],v[68:71],v[176:179]\n"
"buffer_load_dwordx4 v112,s[24:27],s64 offen offset:3072 sc1 lds\n"
"v_mfma_f32_16x16x32_bf16 v[208:211],a[112:115],v[72:75],v[208:211]\n"
"s_add_u32 s64,s64,16384\n"
"v_exp_f32 v122,v60\n"
"v_mfma_f32_16x16x32_bf16 v[240:243],a[112:115],v[76:79],v[240:243]\n"
"v_cvt_pk_bf16_f32 v93,v119,v120\n"
"v_mfma_f32_16x16x32_bf16 v[244:247],a[116:119],v[76:79],v[244:247]\n"
"v_exp_f32 v123,v61\n"
"v_mfma_f32_16x16x32_bf16 v[148:151],a[116:119],v[64:67],v[148:151]\n"
"v_cvt_pk_bf16_f32 v94,v122,v123\n"
"v_or3_b32 v118,v118,v93,v94\n"
"v_mfma_f32_16x16x32_bf16 v[180:183],a[116:119],v[68:71],v[180:183]\n"
"v_exp_f32 v119,v62\n"
"v_mfma_f32_16x16x32_bf16 v[212:215],a[116:119],v[72:75],v[212:215]\n"
"ds_read_b32 v125,v124\n"
"buffer_store_dwordx4 a[248:251],v112,s[24:27],s56 offen offset:2048 sc1\n"
"v_mfma_f32_16x16x32_bf16 v[216:219],a[120:123],v[72:75],v[216:219]\n"
"v_exp_f32 v120,v63\n"
"v_mfma_f32_16x16x32_bf16 v[248:251],a[120:123],v[76:79],v[248:251]\n"
"v_cvt_pk_bf16_f32 v95,v119,v120\n"
"v_mfma_f32_16x16x32_bf16 v[152:155],a[120:123],v[64:67],v[152:155]\n"
"v_mfma_f32_16x16x32_bf16 v[184:187],a[120:123],v[68:71],v[184:187]\n"
"v_mfma_f32_16x16x32_bf16 v[188:191],a[124:127],v[68:71],v[188:191]\n"
"s_waitcnt lgkmcnt(0)\n"
"v_subrev_u32 v125,s101,v125\n"
"v_or3_b32 v125,v118,v95,v125\n"
"v_mfma_f32_16x16x32_bf16 v[220:223],a[124:127],v[72:75],v[220:223]\n"
"v_and_b32 v125,0x40004000,v125\n"
"v_cmp_ne_u32 vcc,0,v125\n"
"v_mfma_f32_16x16x32_bf16 v[252:255],a[124:127],v[76:79],v[252:255]\n"
"v_mfma_f32_16x16x32_bf16 v[156:159],a[124:127],v[64:67],v[156:159]\n"
"s_cbranch_vccnz .Lfk_cs_f1\n"
".Lfk_cr_f1:\n"
"v_mfma_f32_16x16x32_bf16 v[32:35],a[64:67],a[0:3],v[96:99]\n"
"s_add_u32 s101,s101,4\n"
"v_exp_f32 v122,v0\n"
"v_mfma_f32_16x16x32_bf16 v[32:35],a[68:71],a[4:7],v[32:35]\n"
"s_add_u32 m0,s55,12288\n"
"v_exp_f32 v123,v1\n"
"v_mfma_f32_16x16x32_bf16 v[32:35],a[72:75],a[8:11],v[32:35]\n"
"ds_read_b128 a[96:99],v114 offset:8192\n"
"ds_read_b128 a[100:103],v114 offset:9216\n"
"v_mfma_f32_16x16x32_bf16 v[32:35],a[76:79],a[12:15],v[32:35]\n"
"s_add_u32 s54,s54,s22\n"
"v_exp_f32 v119,v2\n"
"v_mfma_f32_16x16x32_bf16 v[36:39],a[92:95],a[12:15],v[96:99]\n"
"ds_read_b128 a[104:107],v114 offset:10240\n"
"v_cvt_pk_bf16_f32 v64,v122,v123\n"
"v_mfma_f32_16x16x32_bf16 v[36:39],a[88:91],a[8:11],v[36:39]\n"
"v_exp_f32 v120,v3\n"
"v_mfma_f32_16x16x32_bf16 v[36:39],a[84:87],a[4:7],v[36:39]\n"
"ds_read_b128 a[108:111],v114 offset:11264\n"
"ds_read_b128 a[112:115],v114 offset:12288\n"
"v_mfma_f32_16x16x32_bf16 v[36:39],a[80:83],a[0:3],v[36:39]\n"
"v_exp_f32 v122,v4\n"
"v_mfma_f32_16x16x32_bf16 v[44:47],a[80:83],a[16:19],v[100:103]\n"
"ds_read_b128 a[116:119],v114 offset:13312\n"
"v_cvt_pk_bf16_f32 v65,v119,v120\n"
"v_mfma_f32_16x16x32_bf16 v[44:47],a[84:87],a[20:23],v[44:47]\n"
"v_exp_f32 v123,v5\n"
"v_mfma_f32_16x16x32_bf16 v[44:47],a[88:91],a[24:27],v[44:47]\n"
"ds_read_b128 a[120:123],v114 offset:14336\n"
"ds_read_b128 a[124:127],v114 offset:15360\n"
"v_mfma_f32_16x16x32_bf16 v[44:47],a[92:95],a[28:31],v[44:47]\n"
"v_exp_f32 v119,v6\n"
"v_mfma_f32_16x16x32_bf16 v[40:43],a[76:79],a[28:31],v[100:103]\n"
"v_cvt_pk_bf16_f32 v66,v122,v123\n"
"v_or3_b32 v118,v64,v65,v66\n"
"v_mfma_f32_16x16x32_bf16 v[40:43],a[72:75],a[24:27],v[40:43]\n"
"v_exp_f32 v120,v7\n"
"v_mfma_f32_16x16x32_bf16 v[40:43],a[68:71],a[20:23],v[40:43]\n"
"buffer_load_dwordx4 v115,s[32:35],s54 offen sc1 lds\n"
"v_mfma_f32_16x16x32_bf16 v[40:43],a[64:67],a[16:19],v[40:43]\n"
"s_add_u32 m0,s67,32768\n"
"v_exp_f32 v122,v8\n"
"v_mfma_f32_16x16x32_bf16 v[48:51],a[64:67],a[32:35],v[104:107]\n"
"v_cvt_pk_bf16_f32 v67,v119,v120\n"
"v_mfma_f32_16x16x32_bf16 v[48:51],a[68:71],a[36:39],v[48:51]\n"
"v_exp_f32 v123,v9\n"
"v_mfma_f32_16x16x32_bf16 v[48:51],a[72:75],a[40:43],v[48:51]\n"
"buffer_load_dwordx4 v112,s[24:27],s64 offen offset:0 sc1 lds\n"
"v_mfma_f32_16x16x32_bf16 v[48:51],a[76:79],a[44:47],v[48:51]\n"
"v_exp_f32 v119,v10\n"
"v_mfma_f32_16x16x32_bf16 v[52:55],a[92:95],a[44:47],v[104:107]\n"
"v_cvt_pk_bf16_f32 v68,v122,v123\n"
"v_or3_b32 v118,v118,v67,v68\n"
"v_mfma_f32_16x16x32_bf16 v[52:55],a[88:91],a[40:43],v[52:55]\n"
"v_exp_f32 v120,v11\n"
"v_mfma_f32_16x16x32_bf16 v[52:55],a[84:87],a[36:39],v[52:55]\n"
"buffer_load_dwordx4 v112,s[24:27],s64 offen offset:1024 sc1 lds\n"
"v_mfma_f32_16x16x32_bf16 v[52:55],a[80:83],a[32:35],v[52:55]\n"
"v_exp_f32 v122,v12\n"
"v_mfma_f32_16x16x32_bf16 v[60:63],a[80:83],a[48:51],v[108:111]\n"
"v_cvt_pk_bf16_f32 v69,v119,v120\n"
"v_mfma_f32_16x16x32_bf16 v[60:63],a[84:87],a[52:55],v[60:63]\n"
"v_exp_f32 v123,v13\n"
"v_mfma_f32_16x16x32_bf16 v[60:63],a[88:91],a[56:59],v[60:63]\n"
"v_cvt_pk_bf16_f32 v70,v122,v123\n"
"v_or3_b32 v118,v118,v69,v70\n"
"v_mfma_f32_16x16x32_bf16 v[60:63],a[92:95],a[60:63],v[60:63]\n"
"v_exp_f32 v119,v14\n"
"v_mfma_f32_16x16x32_bf16 v[56:59],a[76:79],a[60:63],v[108:111]\n"
"v_mfma_f32_16x16x32_bf16 v[56:59],a[72:75],a[56:59],v[56:59]\n"
"v_exp_f32 v120,v15\n"
"v_mfma_f32_16x16x32_bf16 v[56:59],a[68:71],a[52:55],v[56:59]\n"
"v_cvt_pk_bf16_f32 v71,v119,v120\n"
"v_mfma_f32_16x16x32_bf16 v[56:59],a[64:67],a[48:51],v[56:59]\n"
"s_waitcnt lgkmcnt(0)\n"
"v_exp_f32 v122,v16\n"
"v_mfma_f32_16x16x32_bf16 v[128:131],a[96:99],v[80:83],v[128:131]\n"
"v_exp_f32 v123,v17\n"
"v_mfma_f32_16x16x32_bf16 v[160:163],a[96:99],v[84:87],v[160:163]\n"
"ds_read_b128 a[64:67],v113 offset:0\n"
"ds_read_b128 a[68:71],v113 offset:1024\n"
"v_mfma_f32_16x16x32_bf16 v[192:195],a[96:99],v[88:91],v[192:195]\n"
"v_exp_f32 v119,v18\n"
"v_mfma_f32_16x16x32_bf16 v[224:227],a[96:99],v[92:95],v[224:227]\n"
"ds_read_b128 a[72:75],v113 offset:2048\n"
"v_cvt_pk_bf16_f32 v72,v122,v123\n"
"v_mfma_f32_16x16x32_bf16 a[140:143],a[144:147],v[92:95],a[140:143]\n"
"v_exp_f32 v120,v19\n"
"v_mfma_f32_16x16x32_bf16 v[228:231],a[100:103],v[92:95],v[228:231]\n"
"ds_read_b128 a[76:79],v113 offset:3072\n"
"ds_read_b128 a[80:83],v113 offset:4096\n"
"v_mfma_f32_16x16x32_bf16 v[132:135],a[100:103],v[80:83],v[132:135]\n"
"v_exp_f32 v122,v20\n"
"v_mfma_f32_16x16x32_bf16 v[164:167],a[100:103],v[84:87],v[164:167]\n"
"ds_read_b128 a[84:87],v113 offset:5120\n"
"v_cvt_pk_bf16_f32 v73,v119,v120\n"
"v_mfma_f32_16x16x32_bf16 v[196:199],a[100:103],v[88:91],v[196:199]\n"
"v_exp_f32 v123,v21\n"
"v_mfma_f32_16x16x32_bf16 a[136:139],a[144:147],v[88:91],a[136:139]\n"
"ds_read_b128 a[88:91],v113 offset:6144\n"
"ds_read_b128 a[92:95],v113 offset:7168\n"
"v_mfma_f32_16x16x32_bf16 v[200:203],a[104:107],v[88:91],v[200:203]\n"
"v_exp_f32 v119,v22\n"
"v_mfma_f32_16x16x32_bf16 v[232:235],a[104:107],v[92:95],v[232:235]\n"
"v_cvt_pk_bf16_f32 v74,v122,v123\n"
"v_or3_b32 v118,v118,v71,v72\n"
"v_mfma_f32_16x16x32_bf16 v[136:139],a[104:107],v[80:83],v[136:139]\n"
"v_exp_f32 v120,v23\n"
"v_mfma_f32_16x16x32_bf16 v[168:171],a[104:107],v[84:87],v[168:171]\n"
"s_waitcnt vmcnt(10)\n"
"ds_add_u32 v124,a152\n"
"v_mfma_f32_16x16x32_bf16 a[132:135],a[144:147],v[84:87],a[132:135]\n"
"v_exp_f32 v122,v24\n"
"v_mfma_f32_16x16x32_bf16 v[172:175],a[108:111],v[84:87],v[172:175]\n"
"v_cvt_pk_bf16_f32 v75,v119,v120\n"
"v_or3_b32 v118,v118,v73,v74\n"
"v_mfma_f32_16x16x32_bf16 v[204:207],a[108:111],v[88:91],v[204:207]\n"
"v_exp_f32 v123,v25\n"
"v_mfma_f32_16x16x32_bf16 v[236:239],a[108:111],v[92:95],v[236:239]\n"
"buffer_load_dwordx4 v112,s[24:27],s64 offen offset:2048 sc1 lds\n"
"v_mfma_f32_16x16x32_bf16 v[140:143],a[108:111],v[80:83],v[140:143]\n"
"v_exp_f32 v119,v26\n"
"v_mfma_f32_16x16x32_bf16 a[128:131],a[144:147],v[80:83],a[128:131]\n"
"v_cvt_pk_bf16_f32 v76,v122,v123\n"
"v_or3_b32 v118,v118,v75,v76\n"
"v_mfma_f32_16x16x32_bf16 v[144:147],a[112:115],v[80:83],v[144:147]\n"
"v_exp_f32 v120,v27\n"
"v_mfma_f32_16x16x32_bf16 v[176:179],a[112:115],v[84:87],v[176:179]\n"
"buffer_load_dwordx4 v112,s[24:27],s64 offen offset:3072 sc1 lds\n"
"v_mfma_f32_16x16x32_bf16 v[208:211],a[112:115],v[88:91],v[208:211]\n"
"s_add_u32 s64,s64,16384\n"
"v_exp_f32 v122,v28\n"
"v_mfma_f32_16x16x32_bf16 v[240:243],a[112:115],v[92:95],v[240:243]\n"
"v_cvt_pk_bf16_f32 v77,v119,v120\n"
"v_mfma_f32_16x16x32_bf16 v[244:247],a[116:119],v[92:95],v[244:247]\n"
"v_exp_f32 v123,v29\n"
"v_mfma_f32_16x16x32_bf16 v[148:151],a[116:119],v[80:83],v[148:151]\n"
"v_cvt_pk_bf16_f32 v78,v122,v123\n"
"v_or3_b32 v118,v118,v77,v78\n"
"v_mfma_f32_16x16x32_bf16 v[180:183],a[116:119],v[84:87],v[180:183]\n"
"v_exp_f32 v119,v30\n"
"v_mfma_f32_16x16x32_bf16 v[212:215],a[116:119],v[88:91],v[212:215]\n"
"ds_read_b32 v125,v124\n"
"buffer_store_dwordx4 a[252:255],v112,s[24:27],s56 offen offset:3072 sc1\n"
"v_mfma_f32_16x16x32_bf16 v[216:219],a[120:123],v[88:91],v[216:219]\n"
"s_add_u32 s89,s89,1\n"
"v_exp_f32 v120,v31\n"
"v_mfma_f32_16x16x32_bf16 v[248:251],a[120:123],v[92:95],v[248:251]\n"
"v_cvt_pk_bf16_f32 v79,v119,v120\n"
"v_mfma_f32_16x16x32_bf16 v[152:155],a[120:123],v[80:83],v[152:155]\n"
"v_mfma_f32_16x16x32_bf16 v[184:187],a[120:123],v[84:87],v[184:187]\n"
"v_mfma_f32_16x16x32_bf16 v[188:191],a[124:127],v[84:87],v[188:191]\n"
"s_waitcnt lgkmcnt(0)\n"
"v_subrev_u32 v125,s101,v125\n"
"v_or3_b32 v125,v118,v79,v125\n"
"v_mfma_f32_16x16x32_bf16 v[220:223],a[124:127],v[88:91],v[220:223]\n"
"v_and_b32 v125,0x40004000,v125\n"
"v_cmp_ne_u32 vcc,0,v125\n"
"v_mfma_f32_16x16x32_bf16 v[252:255],a[124:127],v[92:95],v[252:255]\n"
"v_mfma_f32_16x16x32_bf16 v[156:159],a[124:127],v[80:83],v[156:159]\n"
"s_cbranch_vccnz .Lfk_cs_f2\n"
".Lfk_cr_f2:\n"
"s_cmp_lt_u32 s89,s60\n"
"s_cbranch_scc1 .Lfk_F\n"
"s_branch .Lfk_Fx\n"
".Lfk_cs_f3:\n"
"s_call_b64 s[94:95],.Lfk_meet3\n"
"v_or_b32 v118,v118,v95\n"
"v_and_b32 v118,0x40004000,v118\n"
"v_cmp_ne_u32 vcc,0,v118\n"
"s_nop 0\n"
"s_cbranch_vccz .Lfk_cr_f3\n"
"s_mov_b32 s91,15\n"
"s_call_b64 s[94:95],.Lfk_rare3\n"
"s_branch .Lfk_cr_f3\n"
".Lfk_cs_f0:\n"
"s_call_b64 s[94:95],.Lfk_meet3\n"
"v_or_b32 v118,v118,v79\n"
"v_and_b32 v118,0x40004000,v118\n"
"v_cmp_ne_u32 vcc,0,v118\n"
"s_nop 0\n"
"s_cbranch_vccz .Lfk_cr_f0\n"
"s_mov_b32 s91,15\n"
"s_call_b64 s[94:95],.Lfk_rare2\n"
"s_branch .Lfk_cr_f0\n"
".Lfk_cs_f1:\n"
"s_call_b64 s[94:95],.Lfk_meet3\n"
"v_or_b32 v118,v118,v95\n"
"v_and_b32 v118,0x40004000,v118\n"
"v_cmp_ne_u32 vcc,0,v118\n"
"s_nop 0\n"
"s_cbranch_vccz .Lfk_cr_f1\n"
"s_mov_b32 s91,15\n"
"s_call_b64 s[94:95],.Lfk_rare3\n"
"s_branch .Lfk_cr_f1\n"
".Lfk_cs_f2:\n"
"s_call_b64 s[94:95],.Lfk_meet3\n"
"v_or_b32 v118,v118,v79\n"
"v_and_b32 v118,0x40004000,v118\n"
"v_cmp_ne_u32 vcc,0,v118\n"
"s_nop 0\n"
"s_cbranch_vccz .Lfk_cr_f2\n"
"s_mov_b32 s91,15\n"
"s_call_b64 s[94:95],.Lfk_rare2\n"
"s_branch .Lfk_cr_f2\n"
".Lfk_cs_g3:\n"
"s_call_b64 s[94:95],.Lfk_meet3\n"
"v_or_b32 v118,v118,v95\n"
"v_and_b32 v118,0x40004000,v118\n"
"v_cmp_ne_u32 vcc,0,v118\n"
"s_nop 0\n"
"s_cbranch_vccz .Lfk_cr_g3\n"
"s_mov_b32 s91,15\n"
"s_call_b64 s[94:95],.Lfk_rare3\n"
"s_branch .Lfk_cr_g3\n"
".Lfk_cs_g0:\n"
"s_call_b64 s[94:95],.Lfk_meet3\n"
"v_or_b32 v118,v118,v79\n"
"v_and_b32 v118,0x40004000,v118\n"
"v_cmp_ne_u32 vcc,0,v118\n"
"s_nop 0\n"
"s_cbranch_vccz .Lfk_cr_g0\n"
"s_mov_b32 s91,15\n"
"s_call_b64 s[94:95],.Lfk_rare2\n"
"s_branch .Lfk_cr_g0\n"
".Lfk_cs_g1:\n"
"s_call_b64 s[94:95],.Lfk_meet3\n"
"v_or_b32 v118,v118,v95\n"
"v_and_b32 v118,0x40004000,v118\n"
"v_cmp_ne_u32 vcc,0,v118\n"
"s_nop 0\n"
"s_cbranch_vccz .Lfk_cr_g1\n"
"s_mov_b32 s91,15\n"
"s_call_b64 s[94:95],.Lfk_rare3\n"
"s_branch .Lfk_cr_g1\n"
".Lfk_cs_g2:\n"
"s_call_b64 s[94:95],.Lfk_meet3\n"
"v_or_b32 v118,v118,v79\n"
"v_and_b32 v118,0x40004000,v118\n"
"v_cmp_ne_u32 vcc,0,v118\n"
"s_nop 0\n"
"s_cbranch_vccz .Lfk_cr_g2\n"
"s_mov_b32 s91,15\n"
"s_call_b64 s[94:95],.Lfk_rare2\n"
"s_branch .Lfk_cr_g2\n"
".Lfk_meet3:\n"
"s_sub_u32 s82,s101,0\n"
"s_mov_b32 s84,0x1000000\n"
".Lfk_meetl3:\n"
"ds_read_b32 v125,v124\n"
"s_sub_u32 s84,s84,1\n"
"s_cbranch_scc1 .Lfk_meetx3\n"
"s_waitcnt lgkmcnt(0)\n"
"v_cmp_gt_u32 vcc,s82,v125\n"
"s_nop 0\n"
"s_cbranch_vccnz .Lfk_meetl3\n"
".Lfk_meetx3:\n"
"s_setpc_b64 s[94:95]\n"
".Lfk_Fx:\n"
"s_mov_b32 s82,2146435072\n"
"buffer_load_dword a148,off,s[24:27],s82\n"
"buffer_load_dword a148,off,s[24:27],s82\n"
"buffer_load_dword a148,off,s[24:27],s82\n"
"buffer_load_dword a148,off,s[24:27],s82\n"
"s_cmp_ge_u32 s52,2\n"
"s_cbranch_scc1 .Lfk_winA\n"
".Lfk_H:\n"
"v_mfma_f32_16x16x32_bf16 v[0:3],a[64:67],a[0:3],v[96:99]\n"
"s_swappc_b64 s[94:95],s[20:21]\n"
"s_add_u32 s101,s101,4\n"
"v_exp_f32 v122,v32\n"
"v_mfma_f32_16x16x32_bf16 v[0:3],a[68:71],a[4:7],v[0:3]\n"
"s_cmp_eq_u32 s64,s65\n"
"v_exp_f32 v123,v33\n"
"v_mfma_f32_16x16x32_bf16 v[0:3],a[72:75],a[8:11],v[0:3]\n"
"ds_read_b128 a[96:99],v114 offset:24576\n"
"ds_read_b128 a[100:103],v114 offset:25600\n"
"v_mfma_f32_16x16x32_bf16 v[0:3],a[76:79],a[12:15],v[0:3]\n"
"s_cselect_b32 s64,s66,s64\n"
"v_exp_f32 v119,v34\n"
"v_mfma_f32_16x16x32_bf16 v[4:7],a[92:95],a[12:15],v[96:99]\n"
"ds_read_b128 a[104:107],v114 offset:26624\n"
"v_cvt_pk_bf16_f32 v80,v122,v123\n"
"v_mfma_f32_16x16x32_bf16 v[4:7],a[88:91],a[8:11],v[4:7]\n"
"v_exp_f32 v120,v35\n"
"v_mfma_f32_16x16x32_bf16 v[4:7],a[84:87],a[4:7],v[4:7]\n"
"ds_read_b128 a[108:111],v114 offset:27648\n"
"ds_read_b128 a[112:115],v114 offset:28672\n"
"v_mfma_f32_16x16x32_bf16 v[4:7],a[80:83],a[0:3],v[4:7]\n"
"v_exp_f32 v122,v36\n"
"v_mfma_f32_16x16x32_bf16 v[12:15],a[80:83],a[16:19],v[100:103]\n"
"ds_read_b128 a[116:119],v114 offset:29696\n"
"v_cvt_pk_bf16_f32 v81,v119,v120\n"
"v_mfma_f32_16x16x32_bf16 v[12:15],a[84:87],a[20:23],v[12:15]\n"
"v_exp_f32 v123,v37\n"
"v_mfma_f32_16x16x32_bf16 v[12:15],a[88:91],a[24:27],v[12:15]\n"
"ds_read_b128 a[120:123],v114 offset:30720\n"
"ds_read_b128 a[124:127],v114 offset:31744\n"
"v_mfma_f32_16x16x32_bf16 v[12:15],a[92:95],a[28:31],v[12:15]\n"
"s_add_u32 m0,s67,0\n"
"v_exp_f32 v119,v38\n"
"v_mfma_f32_16x16x32_bf16 v[8:11],a[76:79],a[28:31],v[100:103]\n"
"v_cvt_pk_bf16_f32 v82,v122,v123\n"
"v_or3_b32 v118,v80,v81,v82\n"
"v_mfma_f32_16x16x32_bf16 v[8:11],a[72:75],a[24:27],v[8:11]\n"
"v_exp_f32 v120,v39\n"
"v_mfma_f32_16x16x32_bf16 v[8:11],a[68:71],a[20:23],v[8:11]\n"
"buffer_load_dwordx4 v112,s[24:27],s64 offen offset:0 sc1 lds\n"
"v_mfma_f32_16x16x32_bf16 v[8:11],a[64:67],a[16:19],v[8:11]\n"
"v_exp_f32 v122,v40\n"
"v_mfma_f32_16x16x32_bf16 v[16:19],a[64:67],a[32:35],v[104:107]\n"
"v_cvt_pk_bf16_f32 v83,v119,v120\n"
"v_mfma_f32_16x16x32_bf16 v[16:19],a[68:71],a[36:39],v[16:19]\n"
"v_exp_f32 v123,v41\n"
"v_mfma_f32_16x16x32_bf16 v[16:19],a[72:75],a[40:43],v[16:19]\n"
"buffer_load_dwordx4 v112,s[24:27],s64 offen offset:1024 sc1 lds\n"
"v_mfma_f32_16x16x32_bf16 v[16:19],a[76:79],a[44:47],v[16:19]\n"
"v_exp_f32 v119,v42\n"
"v_mfma_f32_16x16x32_bf16 v[20:23],a[92:95],a[44:47],v[104:107]\n"
"v_cvt_pk_bf16_f32 v84,v122,v123\n"
"v_or3_b32 v118,v118,v83,v84\n"
"v_mfma_f32_16x16x32_bf16 v[20:23],a[88:91],a[40:43],v[20:23]\n"
"v_exp_f32 v120,v43\n"
"v_mfma_f32_16x16x32_bf16 v[20:23],a[84:87],a[36:39],v[20:23]\n"
"v_cvt_pk_bf16_f32 v85,v119,v120\n"
"v_mfma_f32_16x16x32_bf16 v[20:23],a[80:83],a[32:35],v[20:23]\n"
"v_exp_f32 v122,v44\n"
"v_mfma_f32_16x16x32_bf16 v[28:31],a[80:83],a[48:51],v[108:111]\n"
"buffer_load_dwordx4 v112,s[24:27],s64 offen offset:2048 sc1 lds\n"
"v_mfma_f32_16x16x32_bf16 v[28:31],a[84:87],a[52:55],v[28:31]\n"
"v_exp_f32 v123,v45\n"
"v_mfma_f32_16x16x32_bf16 v[28:31],a[88:91],a[56:59],v[28:31]\n"
"v_cvt_pk_bf16_f32 v86,v122,v123\n"
"v_or3_b32 v118,v118,v85,v86\n"
"v_mfma_f32_16x16x32_bf16 v[28:31],a[92:95],a[60:63],v[28:31]\n"
"v_exp_f32 v119,v46\n"
"v_mfma_f32_16x16x32_bf16 v[24:27],a[76:79],a[60:63],v[108:111]\n"
"v_mfma_f32_16x16x32_bf16 v[24:27],a[72:75],a[56:59],v[24:27]\n"
"v_exp_f32 v120,v47\n"
"v_mfma_f32_16x16x32_bf16 v[24:27],a[68:71],a[52:55],v[24:27]\n"
"buffer_load_dwordx4 v112,s[24:27],s64 offen offset:3072 sc1 lds\n"
"v_mfma_f32_16x16x32_bf16 v[24:27],a[64:67],a[48:51],v[24:27]\n"
"s_waitcnt lgkmcnt(0)\n"
"v_exp_f32 v122,v48\n"
"v_mfma_f32_16x16x32_bf16 v[128:131],a[96:99],v[64:67],v[128:131]\n"
"v_exp_f32 v123,v49\n"
"v_mfma_f32_16x16x32_bf16 v[160:163],a[96:99],v[68:71],v[160:163]\n"
"ds_read_b128 a[64:67],v113 offset:16384\n"
"v_cvt_pk_bf16_f32 v87,v119,v120\n"
"v_mfma_f32_16x16x32_bf16 v[192:195],a[96:99],v[72:75],v[192:195]\n"
"s_add_u32 m0,s67,4096\n"
"v_exp_f32 v119,v50\n"
"v_mfma_f32_16x16x32_bf16 v[224:227],a[96:99],v[76:79],v[224:227]\n"
"ds_read_b128 a[68:71],v113 offset:17408\n"
"v_cvt_pk_bf16_f32 v88,v122,v123\n"
"v_mfma_f32_16x16x32_bf16 a[140:143],a[144:147],v[76:79],a[140:143]\n"
"v_exp_f32 v120,v51\n"
"v_mfma_f32_16x16x32_bf16 v[228:231],a[100:103],v[76:79],v[228:231]\n"
"ds_read_b128 a[72:75],v113 offset:18432\n"
"ds_read_b128 a[76:79],v113 offset:19456\n"
"v_mfma_f32_16x16x32_bf16 v[132:135],a[100:103],v[64:67],v[132:135]\n"
"v_exp_f32 v122,v52\n"
"v_mfma_f32_16x16x32_bf16 v[164:167],a[100:103],v[68:71],v[164:167]\n"
"ds_read_b128 a[80:83],v113 offset:20480\n"
"v_cvt_pk_bf16_f32 v89,v119,v120\n"
"v_mfma_f32_16x16x32_bf16 v[196:199],a[100:103],v[72:75],v[196:199]\n"
"v_exp_f32 v123,v53\n"
"v_mfma_f32_16x16x32_bf16 a[136:139],a[144:147],v[72:75],a[136:139]\n"
"ds_read_b128 a[84:87],v113 offset:21504\n"
"ds_read_b128 a[88:91],v113 offset:22528\n"
"v_mfma_f32_16x16x32_bf16 v[200:203],a[104:107],v[72:75],v[200:203]\n"
"v_exp_f32 v119,v54\n"
"v_mfma_f32_16x16x32_bf16 v[232:235],a[104:107],v[76:79],v[232:235]\n"
"ds_read_b128 a[92:95],v113 offset:23552\n"
"v_cvt_pk_bf16_f32 v90,v122,v123\n"
"v_mfma_f32_16x16x32_bf16 v[136:139],a[104:107],v[64:67],v[136:139]\n"
"v_exp_f32 v120,v55\n"
"v_mfma_f32_16x16x32_bf16 v[168:171],a[104:107],v[68:71],v[168:171]\n"
"s_waitcnt vmcnt(15)\n"
"ds_add_u32 v124,a152\n"
"v_mfma_f32_16x16x32_bf16 a[132:135],a[144:147],v[68:71],a[132:135]\n"
"v_exp_f32 v122,v56\n"
"v_mfma_f32_16x16x32_bf16 v[172:175],a[108:111],v[68:71],v[172:175]\n"
"v_cvt_pk_bf16_f32 v91,v119,v120\n"
"v_or3_b32 v118,v118,v87,v88\n"
"v_mfma_f32_16x16x32_bf16 v[204:207],a[108:111],v[72:75],v[204:207]\n"
"v_exp_f32 v123,v57\n"
"v_mfma_f32_16x16x32_bf16 v[236:239],a[108:111],v[76:79],v[236:239]\n"
"buffer_load_dwordx4 v117,s[24:27],s64 offen offset:0 sc1 lds\n"
"v_mfma_f32_16x16x32_bf16 v[140:143],a[108:111],v[64:67],v[140:143]\n"
"v_exp_f32 v119,v58\n"
"v_mfma_f32_16x16x32_bf16 a[128:131],a[144:147],v[64:67],a[128:131]\n"
"v_cvt_pk_bf16_f32 v92,v122,v123\n"
"v_or3_b32 v118,v118,v89,v90\n"
"v_mfma_f32_16x16x32_bf16 v[144:147],a[112:115],v[64:67],v[144:147]\n"
"v_exp_f32 v120,v59\n"
"v_mfma_f32_16x16x32_bf16 v[176:179],a[112:115],v[68:71],v[176:179]\n"
"buffer_load_dwordx4 v117,s[24:27],s64 offen offset:1024 sc1 lds\n"
"v_mfma_f32_16x16x32_bf16 v[208:211],a[112:115],v[72:75],v[208:211]\n"
"v_exp_f32 v122,v60\n"
"v_mfma_f32_16x16x32_bf16 v[240:243],a[112:115],v[76:79],v[240:243]\n"
"v_cvt_pk_bf16_f32 v93,v119,v120\n"
"v_or3_b32 v118,v118,v91,v92\n"
"v_mfma_f32_16x16x32_bf16 v[244:247],a[116:119],v[76:79],v[244:247]\n"
"v_exp_f32 v123,v61\n"
"v_mfma_f32_16x16x32_bf16 v[148:151],a[116:119],v[64:67],v[148:151]\n"
"buffer_load_dwordx4 v117,s[24:27],s64 offen offset:2048 sc1 lds\n"
"v_mfma_f32_16x16x32_bf16 v[180:183],a[116:119],v[68:71],v[180:183]\n"
"v_exp_f32 v119,v62\n"
"v_mfma_f32_16x16x32_bf16 v[212:215],a[116:119],v[72:75],v[212:215]\n"
"ds_read_b32 v125,v124\n"
"v_cvt_pk_bf16_f32 v94,v122,v123\n"
"v_mfma_f32_16x16x32_bf16 v[216:219],a[120:123],v[72:75],v[216:219]\n"
"v_exp_f32 v120,v63\n"
"v_mfma_f32_16x16x32_bf16 v[248:251],a[120:123],v[76:79],v[248:251]\n"
"buffer_load_dwordx4 v117,s[24:27],s64 offen offset:3072 sc1 lds\n"
"v_mfma_f32_16x16x32_bf16 v[152:155],a[120:123],v[64:67],v[152:155]\n"
"s_add_u32 s64,s64,16384\n"
"s_andn2_b32 m0,s55,16384\n"
"v_mfma_f32_16x16x32_bf16 v[184:187],a[120:123],v[68:71],v[184:187]\n"
"s_add_u32 s54,s54,s22\n"
"v_cvt_pk_bf16_f32 v95,v119,v120\n"
"v_mfma_f32_16x16x32_bf16 v[188:191],a[124:127],v[68:71],v[188:191]\n"
"s_waitcnt lgkmcnt(0)\n"
"v_subrev_u32 v125,s101,v125\n"
"v_or3_b32 v118,v118,v93,v94\n"
"v_mfma_f32_16x16x32_bf16 v[220:223],a[124:127],v[72:75],v[220:223]\n"
"v_or3_b32 v125,v118,v95,v125\n"
"v_and_b32 v125,0x40004000,v125\n"
"v_mfma_f32_16x16x32_bf16 v[252:255],a[124:127],v[76:79],v[252:255]\n"
"buffer_load_dwordx4 v115,s[32:35],s54 offen sc1 lds\n"
"buffer_store_dwordx4 a[240:243],v112,s[24:27],s56 offen offset:0 sc1\n"
"v_cmp_ne_u32 vcc,0,v125\n"
"v_mfma_f32_16x16x32_bf16 v[156:159],a[124:127],v[64:67],v[156:159]\n"
"s_cbranch_vccnz .Lfk_cs_g3\n"
".Lfk_cr_g3:\n"
"v_mfma_f32_16x16x32_bf16 v[32:35],a[64:67],a[0:3],v[96:99]\n"
"s_add_u32 s101,s101,4\n"
"v_exp_f32 v122,v0\n"
"v_mfma_f32_16x16x32_bf16 v[32:35],a[68:71],a[4:7],v[32:35]\n"
"s_add_u32 m0,s55,4096\n"
"v_exp_f32 v123,v1\n"
"v_mfma_f32_16x16x32_bf16 v[32:35],a[72:75],a[8:11],v[32:35]\n"
"ds_read_b128 a[96:99],v114 offset:57344\n"
"ds_read_b128 a[100:103],v114 offset:58368\n"
"v_mfma_f32_16x16x32_bf16 v[32:35],a[76:79],a[12:15],v[32:35]\n"
"s_add_u32 s54,s54,s22\n"
"v_exp_f32 v119,v2\n"
"v_mfma_f32_16x16x32_bf16 v[36:39],a[92:95],a[12:15],v[96:99]\n"
"ds_read_b128 a[104:107],v114 offset:59392\n"
"v_cvt_pk_bf16_f32 v64,v122,v123\n"
"v_mfma_f32_16x16x32_bf16 v[36:39],a[88:91],a[8:11],v[36:39]\n"
"v_exp_f32 v120,v3\n"
"v_mfma_f32_16x16x32_bf16 v[36:39],a[84:87],a[4:7],v[36:39]\n"
"ds_read_b128 a[108:111],v114 offset:60416\n"
"ds_read_b128 a[112:115],v114 offset:61440\n"
"v_mfma_f32_16x16x32_bf16 v[36:39],a[80:83],a[0:3],v[36:39]\n"
"v_exp_f32 v122,v4\n"
"v_mfma_f32_16x16x32_bf16 v[44:47],a[80:83],a[16:19],v[100:103]\n"
"ds_read_b128 a[116:119],v114 offset:62464\n"
"v_cvt_pk_bf16_f32 v65,v119,v120\n"
"v_mfma_f32_16x16x32_bf16 v[44:47],a[84:87],a[20:23],v[44:47]\n"
"v_exp_f32 v123,v5\n"
"v_mfma_f32_16x16x32_bf16 v[44:47],a[88:91],a[24:27],v[44:47]\n"
"ds_read_b128 a[120:123],v114 offset:63488\n"
"ds_read_b128 a[124:127],v114 offset:64512\n"
"v_mfma_f32_16x16x32_bf16 v[44:47],a[92:95],a[28:31],v[44:47]\n"
"v_exp_f32 v119,v6\n"
"v_mfma_f32_16x16x32_bf16 v[40:43],a[76:79],a[28:31],v[100:103]\n"
"v_cvt_pk_bf16_f32 v66,v122,v123\n"
"v_or3_b32 v118,v64,v65,v66\n"
"v_mfma_f32_16x16x32_bf16 v[40:43],a[72:75],a[24:27],v[40:43]\n"
"v_exp_f32 v120,v7\n"
"v_mfma_f32_16x16x32_bf16 v[40:43],a[68:71],a[20:23],v[40:43]\n"
"buffer_load_dwordx4 v115,s[32:35],s54 offen sc1 lds\n"
"v_mfma_f32_16x16x32_bf16 v[40:43],a[64:67],a[16:19],v[40:43]\n"
"s_add_u32 m0,s67,16384\n"
"v_exp_f32 v122,v8\n"
"v_mfma_f32_16x16x32_bf16 v[48:51],a[64:67],a[32:35],v[104:107]\n"
"v_cvt_pk_bf16_f32 v67,v119,v120\n"
"v_mfma_f32_16x16x32_bf16 v[48:51],a[68:71],a[36:39],v[48:51]\n"
"v_exp_f32 v123,v9\n"
"v_mfma_f32_16x16x32_bf16 v[48:51],a[72:75],a[40:43],v[48:51]\n"
"buffer_load_dwordx4 v112,s[24:27],s64 offen offset:0 sc1 lds\n"
"v_mfma_f32_16x16x32_bf16 v[48:51],a[76:79],a[44:47],v[48:51]\n"
"v_exp_f32 v119,v10\n"
"v_mfma_f32_16x16x32_bf16 v[52:55],a[92:95],a[44:47],v[104:107]\n"
"v_cvt_pk_bf16_f32 v68,v122,v123\n"
"v_or3_b32 v118,v118,v67,v68\n"
"v_mfma_f32_16x16x32_bf16 v[52:55],a[88:91],a[40:43],v[52:55]\n"
"v_exp_f32 v120,v11\n"
"v_mfma_f32_16x16x32_bf16 v[52:55],a[84:87],a[36:39],v[52:55]\n"
"buffer_load_dwordx4 v112,s[24:27],s64 offen offset:1024 sc1 lds\n"
"v_mfma_f32_16x16x32_bf16 v[52:55],a[80:83],a[32:35],v[52:55]\n"
"v_exp_f32 v122,v12\n"
"v_mfma_f32_16x16x32_bf16 v[60:63],a[80:83],a[48:51],v[108:111]\n"
"v_cvt_pk_bf16_f32 v69,v119,v120\n"
"v_mfma_f32_16x16x32_bf16 v[60:63],a[84:87],a[52:55],v[60:63]\n"
"v_exp_f32 v123,v13\n"
"v_mfma_f32_16x16x32_bf16 v[60:63],a[88:91],a[56:59],v[60:63]\n"
"buffer_load_dwordx4 v112,s[24:27],s64 offen offset:2048 sc1 lds\n"
"v_mfma_f32_16x16x32_bf16 v[60:63],a[92:95],a[60:63],v[60:63]\n"
"v_exp_f32 v119,v14\n"
"v_mfma_f32_16x16x32_bf16 v[56:59],a[76:79],a[60:63],v[108:111]\n"
"v_cvt_pk_bf16_f32 v70,v122,v123\n"
"v_or3_b32 v118,v118,v69,v70\n"
"v_mfma_f32_16x16x32_bf16 v[56:59],a[72:75],a[56:59],v[56:59]\n"
"v_exp_f32 v120,v15\n"
"v_mfma_f32_16x16x32_bf16 v[56:59],a[68:71],a[52:55],v[56:59]\n"
"buffer_load_dwordx4 v112,s[24:27],s64 offen offset:3072 sc1 lds\n"
"v_mfma_f32_16x16x32_bf16 v[56:59],a[64:67],a[48:51],v[56:59]\n"
"s_waitcnt lgkmcnt(0)\n"
"v_exp_f32 v122,v16\n"
"v_mfma_f32_16x16x32_bf16 v[128:131],a[96:99],v[80:83],v[128:131]\n"
"v_exp_f32 v123,v17\n"
"v_mfma_f32_16x16x32_bf16 v[160:163],a[96:99],v[84:87],v[160:163]\n"
"ds_read_b128 a[64:67],v113 offset:49152\n"
"v_cvt_pk_bf16_f32 v71,v119,v120\n"
"v_mfma_f32_16x16x32_bf16 v[192:195],a[96:99],v[88:91],v[192:195]\n"
"s_add_u32 m0,s67,20480\n"
"v_exp_f32 v119,v18\n"
"v_mfma_f32_16x16x32_bf16 v[224:227],a[96:99],v[92:95],v[224:227]\n"
"ds_read_b128 a[68:71],v113 offset:50176\n"
"v_cvt_pk_bf16_f32 v72,v122,v123\n"
"v_mfma_f32_16x16x32_bf16 a[140:143],a[144:147],v[92:95],a[140:143]\n"
"v_exp_f32 v120,v19\n"
"v_mfma_f32_16x16x32_bf16 v[228:231],a[100:103],v[92:95],v[228:231]\n"
"ds_read_b128 a[72:75],v113 offset:51200\n"
"ds_read_b128 a[76:79],v113 offset:52224\n"
"v_mfma_f32_16x16x32_bf16 v[132:135],a[100:103],v[80:83],v[132:135]\n"
"v_exp_f32 v122,v20\n"
"v_mfma_f32_16x16x32_bf16 v[164:167],a[100:103],v[84:87],v[164:167]\n"
"ds_read_b128 a[80:83],v113 offset:53248\n"
"v_cvt_pk_bf16_f32 v73,v119,v120\n"
"v_mfma_f32_16x16x32_bf16 v[196:199],a[100:103],v[88:91],v[196:199]\n"
"v_exp_f32 v123,v21\n"
"v_mfma_f32_16x16x32_bf16 a[136:139],a[144:147],v[88:91],a[136:139]\n"
"ds_read_b128 a[84:87],v113 offset:54272\n"
"ds_read_b128 a[88:91],v113 offset:55296\n"
"v_mfma_f32_16x16x32_bf16 v[200:203],a[104:107],v[88:91],v[200:203]\n"
"v_exp_f32 v119,v22\n"
"v_mfma_f32_16x16x32_bf16 v[232:235],a[104:107],v[92:95],v[232:235]\n"
"ds_read_b128 a[92:95],v113 offset:56320\n"
"v_cvt_pk_bf16_f32 v74,v122,v123\n"
"v_mfma_f32_16x16x32_bf16 v[136:139],a[104:107],v[80:83],v[136:139]\n"
"v_exp_f32 v120,v23\n"
"v_mfma_f32_16x16x32_bf16 v[168:171],a[104:107],v[84:87],v[168:171]\n"
"s_waitcnt vmcnt(16)\n"
"ds_add_u32 v124,a152\n"
"v_mfma_f32_16x16x32_bf16 a[132:135],a[144:147],v[84:87],a[132:135]\n"
"v_exp_f32 v122,v24\n"
"v_mfma_f32_16x16x32_bf16 v[172:175],a[108:111],v[84:87],v[172:175]\n"
"v_cvt_pk_bf16_f32 v75,v119,v120\n"
"v_or3_b32 v118,v118,v71,v72\n"
"v_mfma_f32_16x16x32_bf16 v[204:207],a[108:111],v[88:91],v[204:207]\n"
"v_exp_f32 v123,v25\n"
"v_mfma_f32_16x16x32_bf16 v[236:239],a[108:111],v[92:95],v[236:239]\n"
"buffer_load_dwordx4 v117,s[24:27],s64 offen offset:0 sc1 lds\n"
"v_mfma_f32_16x16x32_bf16 v[140:143],a[108:111],v[80:83],v[140:143]\n"
"v_exp_f32 v119,v26\n"
"v_mfma_f32_16x16x32_bf16 a[128:131],a[144:147],v[80:83],a[128:131]\n"
"v_cvt_pk_bf16_f32 v76,v122,v123\n"
"v_or3_b32 v118,v118,v73,v74\n"
"v_mfma_f32_16x16x32_bf16 v[144:147],a[112:115],v[80:83],v[144:147]\n"
"v_exp_f32 v120,v27\n"
"v_mfma_f32_16x16x32_bf16 v[176:179],a[112:115],v[84:87],v[176:179]\n"
"buffer_load_dwordx4 v117,s[24:27],s64 offen offset:1024 sc1 lds\n"
"v_mfma_f32_16x16x32_bf16 v[208:211],a[112:115],v[88:91],v[208:211]\n"
"v_exp_f32 v122,v28\n"
"v_mfma_f32_16x16x32_bf16 v[240:243],a[112:115],v[92:95],v[240:243]\n"
"v_cvt_pk_bf16_f32 v77,v119,v120\n"
"v_or3_b32 v118,v118,v75,v76\n"
"v_mfma_f32_16x16x32_bf16 v[244:247],a[116:119],v[92:95],v[244:247]\n"
"v_exp_f32 v123,v29\n"
"v_mfma_f32_16x16x32_bf16 v[148:151],a[116:119],v[80:83],v[148:151]\n"
"buffer_load_dwordx4 v117,s[24:27],s64 offen offset:2048 sc1 lds\n"
"v_mfma_f32_16x16x32_bf16 v[180:183],a[116:119],v[84:87],v[180:183]\n"
"v_exp_f32 v119,v30\n"
"v_mfma_f32_16x16x32_bf16 v[212:215],a[116:119],v[88:91],v[212:215]\n"
"ds_read_b32 v125,v124\n"
"v_cvt_pk_bf16_f32 v78,v122,v123\n"
"v_mfma_f32_16x16x32_bf16 v[216:219],a[120:123],v[88:91],v[216:219]\n"
"v_exp_f32 v120,v31\n"
"v_mfma_f32_16x16x32_bf16 v[248:251],a[120:123],v[92:95],v[248:251]\n"
"buffer_load_dwordx4 v117,s[24:27],s64 offen offset:3072 sc1 lds\n"
"v_mfma_f32_16x16x32_bf16 v[152:155],a[120:123],v[80:83],v[152:155]\n"
"buffer_store_dwordx4 a[244:247],v112,s[24:27],s56 offen offset:1024 sc1\n"
"v_mfma_f32_16x16x32_bf16 v[184:187],a[120:123],v[84:87],v[184:187]\n"
"s_add_u32 s64,s64,16384\n"
"v_cvt_pk_bf16_f32 v79,v119,v120\n"
"v_or3_b32 v118,v118,v77,v78\n"
"v_mfma_f32_16x16x32_bf16 v[188:191],a[124:127],v[84:87],v[188:191]\n"
"s_waitcnt lgkmcnt(0)\n"
"v_subrev_u32 v125,s101,v125\n"
"v_or3_b32 v125,v118,v79,v125\n"
"v_mfma_f32_16x16x32_bf16 v[220:223],a[124:127],v[88:91],v[220:223]\n"
"v_and_b32 v125,0x40004000,v125\n"
"v_cmp_ne_u32 vcc,0,v125\n"
"v_mfma_f32_16x16x32_bf16 v[252:255],a[124:127],v[92:95],v[252:255]\n"
"v_mfma_f32_16x16x32_bf16 v[156:159],a[124:127],v[80:83],v[156:159]\n"
"s_cbranch_vccnz .Lfk_cs_g0\n"
".Lfk_cr_g0:\n"
"v_mfma_f32_16x16x32_bf16 v[0:3],a[64:67],a[0:3],v[96:99]\n"
"s_add_u32 s101,s101,4\n"
"v_exp_f32 v122,v32\n"
"v_mfma_f32_16x16x32_bf16 v[0:3],a[68:71],a[4:7],v[0:3]\n"
"s_add_u32 m0,s55,8192\n"
"v_exp_f32 v123,v33\n"
"v_mfma_f32_16x16x32_bf16 v[0:3],a[72:75],a[8:11],v[0:3]\n"
"ds_read_b128 a[96:99],v114 offset:40960\n"
"ds_read_b128 a[100:103],v114 offset:41984\n"
"v_mfma_f32_16x16x32_bf16 v[0:3],a[76:79],a[12:15],v[0:3]\n"
"s_add_u32 s54,s54,s22\n"
"v_exp_f32 v119,v34\n"
"v_mfma_f32_16x16x32_bf16 v[4:7],a[92:95],a[12:15],v[96:99]\n"
"ds_read_b128 a[104:107],v114 offset:43008\n"
"v_cvt_pk_bf16_f32 v80,v122,v123\n"
"v_mfma_f32_16x16x32_bf16 v[4:7],a[88:91],a[8:11],v[4:7]\n"
"v_exp_f32 v120,v35\n"
"v_mfma_f32_16x16x32_bf16 v[4:7],a[84:87],a[4:7],v[4:7]\n"
"ds_read_b128 a[108:111],v114 offset:44032\n"
"ds_read_b128 a[112:115],v114 offset:45056\n"
"v_mfma_f32_16x16x32_bf16 v[4:7],a[80:83],a[0:3],v[4:7]\n"
"v_exp_f32 v122,v36\n"
"v_mfma_f32_16x16x32_bf16 v[12:15],a[80:83],a[16:19],v[100:103]\n"
"ds_read_b128 a[116:119],v114 offset:46080\n"
"v_cvt_pk_bf16_f32 v81,v119,v120\n"
"v_mfma_f32_16x16x32_bf16 v[12:15],a[84:87],a[20:23],v[12:15]\n"
"v_exp_f32 v123,v37\n"
"v_mfma_f32_16x16x32_bf16 v[12:15],a[88:91],a[24:27],v[12:15]\n"
"ds_read_b128 a[120:123],v114 offset:47104\n"
"ds_read_b128 a[124:127],v114 offset:48128\n"
"v_mfma_f32_16x16x32_bf16 v[12:15],a[92:95],a[28:31],v[12:15]\n"
"v_exp_f32 v119,v38\n"
"v_mfma_f32_16x16x32_bf16 v[8:11],a[76:79],a[28:31],v[100:103]\n"
"v_cvt_pk_bf16_f32 v82,v122,v123\n"
"v_or3_b32 v118,v80,v81,v82\n"
"v_mfma_f32_16x16x32_bf16 v[8:11],a[72:75],a[24:27],v[8:11]\n"
"v_exp_f32 v120,v39\n"
"v_mfma_f32_16x16x32_bf16 v[8:11],a[68:71],a[20:23],v[8:11]\n"
"buffer_load_dwordx4 v115,s[32:35],s54 offen sc1 lds\n"
"v_mfma_f32_16x16x32_bf16 v[8:11],a[64:67],a[16:19],v[8:11]\n"
"s_add_u32 m0,s67,49152\n"
"v_exp_f32 v122,v40\n"
"v_mfma_f32_16x16x32_bf16 v[16:19],a[64:67],a[32:35],v[104:107]\n"
"v_cvt_pk_bf16_f32 v83,v119,v120\n"
"v_mfma_f32_16x16x32_bf16 v[16:19],a[68:71],a[36:39],v[16:19]\n"
"v_exp_f32 v123,v41\n"
"v_mfma_f32_16x16x32_bf16 v[16:19],a[72:75],a[40:43],v[16:19]\n"
"buffer_load_dwordx4 v112,s[24:27],s64 offen offset:0 sc1 lds\n"
"v_mfma_f32_16x16x32_bf16 v[16:19],a[76:79],a[44:47],v[16:19]\n"
"v_exp_f32 v119,v42\n"
"v_mfma_f32_16x16x32_bf16 v[20:23],a[92:95],a[44:47],v[104:107]\n"
"v_cvt_pk_bf16_f32 v84,v122,v123\n"
"v_or3_b32 v118,v118,v83,v84\n"
"v_mfma_f32_16x16x32_bf16 v[20:23],a[88:91],a[40:43],v[20:23]\n"
"v_exp_f32 v120,v43\n"
"v_mfma_f32_16x16x32_bf16 v[20:23],a[84:87],a[36:39],v[20:23]\n"
"buffer_load_dwordx4 v112,s[24:27],s64 offen offset:1024 sc1 lds\n"
"v_mfma_f32_16x16x32_bf16 v[20:23],a[80:83],a[32:35],v[20:23]\n"
"v_exp_f32 v122,v44\n"
"v_mfma_f32_16x16x32_bf16 v[28:31],a[80:83],a[48:51],v[108:111]\n"
"v_cvt_pk_bf16_f32 v85,v119,v120\n"
"v_mfma_f32_16x16x32_bf16 v[28:31],a[84:87],a[52:55],v[28:31]\n"
"v_exp_f32 v123,v45\n"
"v_mfma_f32_16x16x32_bf16 v[28:31],a[88:91],a[56:59],v[28:31]\n"
"buffer_load_dwordx4 v112,s[24:27],s64 offen offset:2048 sc1 lds\n"
"v_mfma_f32_16x16x32_bf16 v[28:31],a[92:95],a[60:63],v[28:31]\n"
"v_exp_f32 v119,v46\n"
"v_mfma_f32_16x16x32_bf16 v[24:27],a[76:79],a[60:63],v[108:111]\n"
"v_cvt_pk_bf16_f32 v86,v122,v123\n"
"v_or3_b32 v118,v118,v85,v86\n"
"v_mfma_f32_16x16x32_bf16 v[24:27],a[72:75],a[56:59],v[24:27]\n"
"v_exp_f32 v120,v47\n"
"v_mfma_f32_16x16x32_bf16 v[24:27],a[68:71],a[52:55],v[24:27]\n"
"buffer_load_dwordx4 v112,s[24:27],s64 offen offset:3072 sc1 lds\n"
"v_mfma_f32_16x16x32_bf16 v[24:27],a[64:67],a[48:51],v[24:27]\n"
"s_waitcnt lgkmcnt(0)\n"
"v_exp_f32 v122,v48\n"
"v_mfma_f32_16x16x32_bf16 v[128:131],a[96:99],v[64:67],v[128:131]\n"
"v_exp_f32 v123,v49\n"
"v_mfma_f32_16x16x32_bf16 v[160:163],a[96:99],v[68:71],v[160:163]\n"
"ds_read_b128 a[64:67],v113 offset:32768\n"
"v_cvt_pk_bf16_f32 v87,v119,v120\n"
"v_mfma_f32_16x16x32_bf16 v[192:195],a[96:99],v[72:75],v[192:195]\n"
"s_add_u32 m0,s67,53248\n"
"v_exp_f32 v119,v50\n"
"v_mfma_f32_16x16x32_bf16 v[224:227],a[96:99],v[76:79],v[224:227]\n"
"ds_read_b128 a[68:71],v113 offset:33792\n"
"v_cvt_pk_bf16_f32 v88,v122,v123\n"
"v_mfma_f32_16x16x32_bf16 a[140:143],a[144:147],v[76:79],a[140:143]\n"
"v_exp_f32 v120,v51\n"
"v_mfma_f32_16x16x32_bf16 v[228:231],a[100:103],v[76:79],v[228:231]\n"
"ds_read_b128 a[72:75],v113 offset:34816\n"
"ds_read_b128 a[76:79],v113 offset:35840\n"
"v_mfma_f32_16x16x32_bf16 v[132:135],a[100:103],v[64:67],v[132:135]\n"
"v_exp_f32 v122,v52\n"
"v_mfma_f32_16x16x32_bf16 v[164:167],a[100:103],v[68:71],v[164:167]\n"
"ds_read_b128 a[80:83],v113 offset:36864\n"
"v_cvt_pk_bf16_f32 v89,v119,v120\n"
"v_mfma_f32_16x16x32_bf16 v[196:199],a[100:103],v[72:75],v[196:199]\n"
"v_exp_f32 v123,v53\n"
"v_mfma_f32_16x16x32_bf16 a[136:139],a[144:147],v[72:75],a[136:139]\n"
"ds_read_b128 a[84:87],v113 offset:37888\n"
"ds_read_b128 a[88:91],v113 offset:38912\n"
"v_mfma_f32_16x16x32_bf16 v[200:203],a[104:107],v[72:75],v[200:203]\n"
"v_exp_f32 v119,v54\n"
"v_mfma_f32_16x16x32_bf16 v[232:235],a[104:107],v[76:79],v[232:235]\n"
"ds_read_b128 a[92:95],v113 offset:39936\n"
"v_cvt_pk_bf16_f32 v90,v122,v123\n"
"v_mfma_f32_16x16x32_bf16 v[136:139],a[104:107],v[64:67],v[136:139]\n"
"v_exp_f32 v120,v55\n"
"v_mfma_f32_16x16x32_bf16 v[168:171],a[104:107],v[68:71],v[168:171]\n"
"s_waitcnt vmcnt(16)\n"
"ds_add_u32 v124,a152\n"
"v_mfma_f32_16x16x32_bf16 a[132:135],a[144:147],v[68:71],a[132:135]\n"
"v_exp_f32 v122,v56\n"
"v_mfma_f32_16x16x32_bf16 v[172:175],a[108:111],v[68:71],v[172:175]\n"
"v_cvt_pk_bf16_f32 v91,v119,v120\n"
"v_or3_b32 v118,v118,v87,v88\n"
"v_mfma_f32_16x16x32_bf16 v[204:207],a[108:111],v[72:75],v[204:207]\n"
"v_exp_f32 v123,v57\n"
"v_mfma_f32_16x16x32_bf16 v[236:239],a[108:111],v[76:79],v[236:239]\n"
"buffer_load_dwordx4 v117,s[24:27],s64 offen offset:0 sc1 lds\n"
"v_mfma_f32_16x16x32_bf16 v[140:143],a[108:111],v[64:67],v[140:143]\n"
"v_exp_f32 v119,v58\n"
"v_mfma_f32_16x16x32_bf16 a[128:131],a[144:147],v[64:67],a[128:131]\n"
"v_cvt_pk_bf16_f32 v92,v122,v123\n"
"v_or3_b32 v118,v118,v89,v90\n"
"v_mfma_f32_16x16x32_bf16 v[144:147],a[112:115],v[64:67],v[144:147]\n"
"v_exp_f32 v120,v59\n"
"v_mfma_f32_16x16x32_bf16 v[176:179],a[112:115],v[68:71],v[176:179]\n"
"buffer_load_dwordx4 v117,s[24:27],s64 offen offset:1024 sc1 lds\n"
"v_mfma_f32_16x16x32_bf16 v[208:211],a[112:115],v[72:75],v[208:211]\n"
"v_exp_f32 v122,v60\n"
"v_mfma_f32_16x16x32_bf16 v[240:243],a[112:115],v[76:79],v[240:243]\n"
"v_cvt_pk_bf16_f32 v93,v119,v120\n"
"v_or3_b32 v118,v118,v91,v92\n"
"v_mfma_f32_16x16x32_bf16 v[244:247],a[116:119],v[76:79],v[244:247]\n"
"v_exp_f32 v123,v61\n"
"v_mfma_f32_16x16x32_bf16 v[148:151],a[116:119],v[64:67],v[148:151]\n"
"buffer_load_dwordx4 v117,s[24:27],s64 offen offset:2048 sc1 lds\n"
"v_mfma_f32_16x16x32_bf16 v[180:183],a[116:119],v[68:71],v[180:183]\n"
"v_exp_f32 v119,v62\n"
"v_mfma_f32_16x16x32_bf16 v[212:215],a[116:119],v[72:75],v[212:215]\n"
"ds_read_b32 v125,v124\n"
"v_cvt_pk_bf16_f32 v94,v122,v123\n"
"v_mfma_f32_16x16x32_bf16 v[216:219],a[120:123],v[72:75],v[216:219]\n"
"v_exp_f32 v120,v63\n"
"v_mfma_f32_16x16x32_bf16 v[248:251],a[120:123],v[76:79],v[248:251]\n"
"buffer_load_dwordx4 v117,s[24:27],s64 offen offset:3072 sc1 lds\n"
"v_mfma_f32_16x16x32_bf16 v[152:155],a[120:123],v[64:67],v[152:155]\n"
"buffer_store_dwordx4 a[248:251],v112,s[24:27],s56 offen offset:2048 sc1\n"
"v_mfma_f32_16x16x32_bf16 v[184:187],a[120:123],v[68:71],v[184:187]\n"
"s_add_u32 s64,s64,16384\n"
"v_cvt_pk_bf16_f32 v95,v119,v120\n"
"v_or3_b32 v118,v118,v93,v94\n"
"v_mfma_f32_16x16x32_bf16 v[188:191],a[124:127],v[68:71],v[188:191]\n"
"s_waitcnt lgkmcnt(0)\n"
"v_subrev_u32 v125,s101,v125\n"
"v_or3_b32 v125,v118,v95,v125\n"
"v_mfma_f32_16x16x32_bf16 v[220:223],a[124:127],v[72:75],v[220:223]\n"
"v_and_b32 v125,0x40004000,v125\n"
"v_cmp_ne_u32 vcc,0,v125\n"
"v_mfma_f32_16x16x32_bf16 v[252:255],a[124:127],v[76:79],v[252:255]\n"
"v_mfma_f32_16x16x32_bf16 v[156:159],a[124:127],v[64:67],v[156:159]\n"
"s_cbranch_vccnz .Lfk_cs_g1\n"
".Lfk_cr_g1:\n"
"v_mfma_f32_16x16x32_bf16 v[32:35],a[64:67],a[0:3],v[96:99]\n"
"s_add_u32 s101,s101,4\n"
"v_exp_f32 v122,v0\n"
"v_mfma_f32_16x16x32_bf16 v[32:35],a[68:71],a[4:7],v[32:35]\n"
"s_add_u32 m0,s55,12288\n"
"v_exp_f32 v123,v1\n"
"v_mfma_f32_16x16x32_bf16 v[32:35],a[72:75],a[8:11],v[32:35]\n"
"ds_read_b128 a[96:99],v114 offset:8192\n"
"ds_read_b128 a[100:103],v114 offset:9216\n"
"v_mfma_f32_16x16x32_bf16 v[32:35],a[76:79],a[12:15],v[32:35]\n"
"s_add_u32 s54,s54,s22\n"
"v_exp_f32 v119,v2\n"
"v_mfma_f32_16x16x32_bf16 v[36:39],a[92:95],a[12:15],v[96:99]\n"
"ds_read_b128 a[104:107],v114 offset:10240\n"
"v_cvt_pk_bf16_f32 v64,v122,v123\n"
"v_mfma_f32_16x16x32_bf16 v[36:39],a[88:91],a[8:11],v[36:39]\n"
"v_exp_f32 v120,v3\n"
"v_mfma_f32_16x16x32_bf16 v[36:39],a[84:87],a[4:7],v[36:39]\n"
"ds_read_b128 a[108:111],v114 offset:11264\n"
"ds_read_b128 a[112:115],v114 offset:12288\n"
"v_mfma_f32_16x16x32_bf16 v[36:39],a[80:83],a[0:3],v[36:39]\n"
"v_exp_f32 v122,v4\n"
"v_mfma_f32_16x16x32_bf16 v[44:47],a[80:83],a[16:19],v[100:103]\n"
"ds_read_b128 a[116:119],v114 offset:13312\n"
"v_cvt_pk_bf16_f32 v65,v119,v120\n"
"v_mfma_f32_16x16x32_bf16 v[44:47],a[84:87],a[20:23],v[44:47]\n"
"v_exp_f32 v123,v5\n"
"v_mfma_f32_16x16x32_bf16 v[44:47],a[88:91],a[24:27],v[44:47]\n"
"ds_read_b128 a[120:123],v114 offset:14336\n"
"ds_read_b128 a[124:127],v114 offset:15360\n"
"v_mfma_f32_16x16x32_bf16 v[44:47],a[92:95],a[28:31],v[44:47]\n"
"v_exp_f32 v119,v6\n"
"v_mfma_f32_16x16x32_bf16 v[40:43],a[76:79],a[28:31],v[100:103]\n"
"v_cvt_pk_bf16_f32 v66,v122,v123\n"
"v_or3_b32 v118,v64,v65,v66\n"
"v_mfma_f32_16x16x32_bf16 v[40:43],a[72:75],a[24:27],v[40:43]\n"
"v_exp_f32 v120,v7\n"
"v_mfma_f32_16x16x32_bf16 v[40:43],a[68:71],a[20:23],v[40:43]\n"
"buffer_load_dwordx4 v115,s[32:35],s54 offen sc1 lds\n"
"v_mfma_f32_16x16x32_bf16 v[40:43],a[64:67],a[16:19],v[40:43]\n"
"s_cmp_eq_u32 s64,s65\n"
"v_exp_f32 v122,v8\n"
"v_mfma_f32_16x16x32_bf16 v[48:51],a[64:67],a[32:35],v[104:107]\n"
"v_cvt_pk_bf16_f32 v67,v119,v120\n"
"s_cselect_b32 s64,s66,s64\n"
"v_mfma_f32_16x16x32_bf16 v[48:51],a[68:71],a[36:39],v[48:51]\n"
"s_add_u32 m0,s67,32768\n"
"v_exp_f32 v123,v9\n"
"v_mfma_f32_16x16x32_bf16 v[48:51],a[72:75],a[40:43],v[48:51]\n"
"buffer_load_dwordx4 v112,s[24:27],s64 offen offset:0 sc1 lds\n"
"v_mfma_f32_16x16x32_bf16 v[48:51],a[76:79],a[44:47],v[48:51]\n"
"v_exp_f32 v119,v10\n"
"v_mfma_f32_16x16x32_bf16 v[52:55],a[92:95],a[44:47],v[104:107]\n"
"v_cvt_pk_bf16_f32 v68,v122,v123\n"
"v_or3_b32 v118,v118,v67,v68\n"
"v_mfma_f32_16x16x32_bf16 v[52:55],a[88:91],a[40:43],v[52:55]\n"
"v_exp_f32 v120,v11\n"
"v_mfma_f32_16x16x32_bf16 v[52:55],a[84:87],a[36:39],v[52:55]\n"
"buffer_load_dwordx4 v112,s[24:27],s64 offen offset:1024 sc1 lds\n"
"v_mfma_f32_16x16x32_bf16 v[52:55],a[80:83],a[32:35],v[52:55]\n"
"v_exp_f32 v122,v12\n"
"v_mfma_f32_16x16x32_bf16 v[60:63],a[80:83],a[48:51],v[108:111]\n"
"v_cvt_pk_bf16_f32 v69,v119,v120\n"
"v_mfma_f32_16x16x32_bf16 v[60:63],a[84:87],a[52:55],v[60:63]\n"
"v_exp_f32 v123,v13\n"
"v_mfma_f32_16x16x32_bf16 v[60:63],a[88:91],a[56:59],v[60:63]\n"
"buffer_load_dwordx4 v112,s[24:27],s64 offen offset:2048 sc1 lds\n"
"v_mfma_f32_16x16x32_bf16 v[60:63],a[92:95],a[60:63],v[60:63]\n"
"v_exp_f32 v119,v14\n"
"v_mfma_f32_16x16x32_bf16 v[56:59],a[76:79],a[60:63],v[108:111]\n"
"v_cvt_pk_bf16_f32 v70,v122,v123\n"
"v_or3_b32 v118,v118,v69,v70\n"
"v_mfma_f32_16x16x32_bf16 v[56:59],a[72:75],a[56:59],v[56:59]\n"
"v_exp_f32 v120,v15\n"
"v_mfma_f32_16x16x32_bf16 v[56:59],a[68:71],a[52:55],v[56:59]\n"
"buffer_load_dwordx4 v112,s[24:27],s64 offen offset:3072 sc1 lds\n"
"v_mfma_f32_16x16x32_bf16 v[56:59],a[64:67],a[48:51],v[56:59]\n"
"s_waitcnt lgkmcnt(0)\n"
"v_exp_f32 v122,v16\n"
"v_mfma_f32_16x16x32_bf16 v[128:131],a[96:99],v[80:83],v[128:131]\n"
"v_exp_f32 v123,v17\n"
"v_mfma_f32_16x16x32_bf16 v[160:163],a[96:99],v[84:87],v[160:163]\n"
"ds_read_b128 a[64:67],v113 offset:0\n"
"v_cvt_pk_bf16_f32 v71,v119,v120\n"
"v_mfma_f32_16x16x32_bf16 v[192:195],a[96:99],v[88:91],v[192:195]\n"
"s_add_u32 m0,s67,36864\n"
"v_exp_f32 v119,v18\n"
"v_mfma_f32_16x16x32_bf16 v[224:227],a[96:99],v[92:95],v[224:227]\n"
"ds_read_b128 a[68:71],v113 offset:1024\n"
"v_cvt_pk_bf16_f32 v72,v122,v123\n"
"v_mfma_f32_16x16x32_bf16 a[140:143],a[144:147],v[92:95],a[140:143]\n"
"v_exp_f32 v120,v19\n"
"v_mfma_f32_16x16x32_bf16 v[228:231],a[100:103],v[92:95],v[228:231]\n"
"ds_read_b128 a[72:75],v113 offset:2048\n"
"ds_read_b128 a[76:79],v113 offset:3072\n"
"v_mfma_f32_16x16x32_bf16 v[132:135],a[100:103],v[80:83],v[132:135]\n"
"v_exp_f32 v122,v20\n"
"v_mfma_f32_16x16x32_bf16 v[164:167],a[100:103],v[84:87],v[164:167]\n"
"ds_read_b128 a[80:83],v113 offset:4096\n"
"v_cvt_pk_bf16_f32 v73,v119,v120\n"
"v_mfma_f32_16x16x32_bf16 v[196:199],a[100:103],v[88:91],v[196:199]\n"
"v_exp_f32 v123,v21\n"
"v_mfma_f32_16x16x32_bf16 a[136:139],a[144:147],v[88:91],a[136:139]\n"
"ds_read_b128 a[84:87],v113 offset:5120\n"
"ds_read_b128 a[88:91],v113 offset:6144\n"
"v_mfma_f32_16x16x32_bf16 v[200:203],a[104:107],v[88:91],v[200:203]\n"
"v_exp_f32 v119,v22\n"
"v_mfma_f32_16x16x32_bf16 v[232:235],a[104:107],v[92:95],v[232:235]\n"
"ds_read_b128 a[92:95],v113 offset:7168\n"
"v_cvt_pk_bf16_f32 v74,v122,v123\n"
"v_mfma_f32_16x16x32_bf16 v[136:139],a[104:107],v[80:83],v[136:139]\n"
"v_exp_f32 v120,v23\n"
"v_mfma_f32_16x16x32_bf16 v[168:171],a[104:107],v[84:87],v[168:171]\n"
"s_waitcnt vmcnt(16)\n"
"ds_add_u32 v124,a152\n"
"v_mfma_f32_16x16x32_bf16 a[132:135],a[144:147],v[84:87],a[132:135]\n"
"v_exp_f32 v122,v24\n"
"v_mfma_f32_16x16x32_bf16 v[172:175],a[108:111],v[84:87],v[172:175]\n"
"v_cvt_pk_bf16_f32 v75,v119,v120\n"
"v_or3_b32 v118,v118,v71,v72\n"
"v_mfma_f32_16x16x32_bf16 v[204:207],a[108:111],v[88:91],v[204:207]\n"
"v_exp_f32 v123,v25\n"
"v_mfma_f32_16x16x32_bf16 v[236:239],a[108:111],v[92:95],v[236:239]\n"
"buffer_load_dwordx4 v117,s[24:27],s64 offen offset:0 sc1 lds\n"
"v_mfma_f32_16x16x32_bf16 v[140:143],a[108:111],v[80:83],v[140:143]\n"
"v_exp_f32 v119,v26\n"
"v_mfma_f32_16x16x32_bf16 a[128:131],a[144:147],v[80:83],a[128:131]\n"
"v_cvt_pk_bf16_f32 v76,v122,v123\n"
"v_or3_b32 v118,v118,v73,v74\n"
"v_mfma_f32_16x16x32_bf16 v[144:147],a[112:115],v[80:83],v[144:147]\n"
"v_exp_f32 v120,v27\n"
"v_mfma_f32_16x16x32_bf16 v[176:179],a[112:115],v[84:87],v[176:179]\n"
"buffer_load_dwordx4 v117,s[24:27],s64 offen offset:1024 sc1 lds\n"
"v_mfma_f32_16x16x32_bf16 v[208:211],a[112:115],v[88:91],v[208:211]\n"
"v_exp_f32 v122,v28\n"
"v_mfma_f32_16x16x32_bf16 v[240:243],a[112:115],v[92:95],v[240:243]\n"
"v_cvt_pk_bf16_f32 v77,v119,v120\n"
"v_or3_b32 v118,v118,v75,v76\n"
"v_mfma_f32_16x16x32_bf16 v[244:247],a[116:119],v[92:95],v[244:247]\n"
"v_exp_f32 v123,v29\n"
"v_mfma_f32_16x16x32_bf16 v[148:151],a[116:119],v[80:83],v[148:151]\n"
"buffer_load_dwordx4 v117,s[24:27],s64 offen offset:2048 sc1 lds\n"
"v_mfma_f32_16x16x32_bf16 v[180:183],a[116:119],v[84:87],v[180:183]\n"
"v_exp_f32 v119,v30\n"
"v_mfma_f32_16x16x32_bf16 v[212:215],a[116:119],v[88:91],v[212:215]\n"
"ds_read_b32 v125,v124\n"
"v_cvt_pk_bf16_f32 v78,v122,v123\n"
"v_mfma_f32_16x16x32_bf16 v[216:219],a[120:123],v[88:91],v[216:219]\n"
"v_exp_f32 v120,v31\n"
"v_mfma_f32_16x16x32_bf16 v[248:251],a[120:123],v[92:95],v[248:251]\n"
"buffer_load_dwordx4 v117,s[24:27],s64 offen offset:3072 sc1 lds\n"
"v_mfma_f32_16x16x32_bf16 v[152:155],a[120:123],v[80:83],v[152:155]\n"
"buffer_store_dwordx4 a[252:255],v112,s[24:27],s56 offen offset:3072 sc1\n"
"v_mfma_f32_16x16x32_bf16 v[184:187],a[120:123],v[84:87],v[184:187]\n"
"s_add_u32 s64,s64,16384\n"
"s_add_u32 s89,s89,1\n"
"v_cvt_pk_bf16_f32 v79,v119,v120\n"
"v_mfma_f32_16x16x32_bf16 v[188:191],a[124:127],v[84:87],v[188:191]\n"
"s_waitcnt lgkmcnt(0)\n"
"v_subrev_u32 v125,s101,v125\n"
"v_or3_b32 v118,v118,v77,v78\n"
"v_mfma_f32_16x16x32_bf16 v[220:223],a[124:127],v[88:91],v[220:223]\n"
"v_or3_b32 v125,v118,v79,v125\n"
"v_and_b32 v125,0x40004000,v125\n"
"v_mfma_f32_16x16x32_bf16 v[252:255],a[124:127],v[92:95],v[252:255]\n"
"v_cmp_ne_u32 vcc,0,v125\n"
"v_mfma_f32_16x16x32_bf16 v[156:159],a[124:127],v[80:83],v[156:159]\n"
"s_cbranch_vccnz .Lfk_cs_g2\n"
".Lfk_cr_g2:\n"
".Lfk_Hx:\n"
"s_cmp_lt_u32 s89,8\n"
"s_cbranch_scc1 .Lfk_H\n"
"v_max3_f32 v118,v32,v33,v34\n"
"v_max3_f32 v119,v35,v36,v37\n"
"v_max3_f32 v119,v119,v38,v39\n"
"v_max3_f32 v118,v118,v40,v41\n"
"v_max3_f32 v119,v119,v42,v43\n"
"v_max3_f32 v118,v118,v44,v45\n"
"v_max3_f32 v119,v119,v46,v47\n"
"v_max3_f32 v118,v118,v48,v49\n"
"v_max3_f32 v119,v119,v50,v51\n"
"v_max3_f32 v118,v118,v52,v53\n"
"v_max3_f32 v119,v119,v54,v55\n"
"v_max3_f32 v118,v118,v56,v57\n"
"v_max3_f32 v119,v119,v58,v59\n"
"v_max3_f32 v118,v118,v60,v61\n"
"v_max3_f32 v119,v119,v62,v63\n"
"v_max_f32 v118,v118,v119\n"
"v_cmp_lt_f32 vcc,s93,v118\n"
"s_nop 0\n"
"s_cbranch_vccnz .Lfk_cs_x1\n"
".Lfk_cr_x1:\n"
"s_cmp_ge_u32 s52,2\n"
"s_cbranch_scc1 .Lfk_tailB\n"
".Lfk_tailA:\n"
"s_add_u32 s82,s66,28672\n"
"s_cmp_eq_u32 s52,0\n"
"s_cselect_b32 s67,77824,s67\n"
"s_cselect_b32 s64,s82,s64\n"
"s_waitcnt vmcnt(11) lgkmcnt(0)\n"
"s_barrier\n"
"v_mfma_f32_16x16x32_bf16 v[16:19],a[64:67],a[32:35],v[104:107]\n"
"ds_read_b128 a[96:99],v114 offset:24576\n"
"ds_read_b128 a[100:103],v114 offset:25600\n"
"ds_read_b128 a[104:107],v114 offset:26624\n"
"v_mfma_f32_16x16x32_bf16 v[16:19],a[68:71],a[36:39],v[16:19]\n"
"ds_read_b128 a[108:111],v114 offset:27648\n"
"ds_read_b128 a[112:115],v114 offset:28672\n"
"ds_read_b128 a[116:119],v114 offset:29696\n"
"v_mfma_f32_16x16x32_bf16 v[16:19],a[72:75],a[40:43],v[16:19]\n"
"ds_read_b128 a[120:123],v114 offset:30720\n"
"ds_read_b128 a[124:127],v114 offset:31744\n"
"v_add_f32 v8,0x80000000,v126\n"
"v_mfma_f32_16x16x32_bf16 v[16:19],a[76:79],a[44:47],v[16:19]\n"
"v_min_f32 v8,v8,v100\n"
"v_add_f32 v9,0xf149f2ca,v126\n"
"v_min_f32 v9,v9,v101\n"
"v_mfma_f32_16x16x32_bf16 v[20:23],a[92:95],a[44:47],v[104:107]\n"
"v_add_f32 v10,0xf1c9f2ca,v126\n"
"v_min_f32 v10,v10,v102\n"
"v_add_f32 v11,0xf2177617,v126\n"
"v_mfma_f32_16x16x32_bf16 v[20:23],a[88:91],a[40:43],v[20:23]\n"
"v_min_f32 v11,v11,v103\n"
"v_add_f32 v12,0xf349f2ca,v126\n"
"v_min_f32 v12,v12,v100\n"
"v_mfma_f32_16x16x32_bf16 v[20:23],a[84:87],a[36:39],v[20:23]\n"
"v_add_f32 v13,0xf35691f6,v126\n"
"v_min_f32 v13,v13,v101\n"
"v_add_f32 v14,0xf3633123,v126\n"
"v_mfma_f32_16x16x32_bf16 v[20:23],a[80:83],a[32:35],v[20:23]\n"
"v_min_f32 v14,v14,v102\n"
"v_add_f32 v15,0xf36fd050,v126\n"
"v_min_f32 v15,v15,v103\n"
"v_mfma_f32_16x16x32_bf16 v[28:31],a[80:83],a[48:51],v[108:111]\n"
"s_add_u32 m0,s67,0\n"
"v_exp_f32 v32,v32\n"
"v_mfma_f32_16x16x32_bf16 v[28:31],a[84:87],a[52:55],v[28:31]\n"
"buffer_load_dwordx4 v112,s[24:27],s64 offen offset:0 sc1 lds\n"
"buffer_load_dwordx4 v112,s[24:27],s64 offen offset:1024 sc1 lds\n"
"v_exp_f32 v33,v33\n"
"v_mfma_f32_16x16x32_bf16 v[28:31],a[88:91],a[56:59],v[28:31]\n"
"v_exp_f32 v34,v34\n"
"v_cvt_pk_bf16_f32 v80,v32,v33\n"
"v_mfma_f32_16x16x32_bf16 v[28:31],a[92:95],a[60:63],v[28:31]\n"
"v_exp_f32 v35,v35\n"
"v_max3_f32 v118,v16,v17,v18\n"
"v_exp_f32 v36,v36\n"
"v_mfma_f32_16x16x32_bf16 v[24:27],a[76:79],a[60:63],v[108:111]\n"
"buffer_load_dwordx4 v112,s[24:27],s64 offen offset:2048 sc1 lds\n"
"v_mfma_f32_16x16x32_bf16 v[24:27],a[72:75],a[56:59],v[24:27]\n"
"v_exp_f32 v37,v37\n"
"v_cvt_pk_bf16_f32 v81,v34,v35\n"
"v_mfma_f32_16x16x32_bf16 v[24:27],a[68:71],a[52:55],v[24:27]\n"
"v_exp_f32 v38,v38\n"
"v_cvt_pk_bf16_f32 v82,v36,v37\n"
"v_exp_f32 v39,v39\n"
"v_mfma_f32_16x16x32_bf16 v[24:27],a[64:67],a[48:51],v[24:27]\n"
"buffer_load_dwordx4 v112,s[24:27],s64 offen offset:3072 sc1 lds\n"
"v_mfma_f32_16x16x32_bf16 v[8:11],a[64:67],a[16:19],v[8:11]\n"
"s_add_u32 s64,s64,16384\n"
"v_exp_f32 v40,v40\n"
"v_accvgpr_read_b32 v119,a150\n"
"v_mfma_f32_16x16x32_bf16 v[8:11],a[68:71],a[20:23],v[8:11]\n"
"v_exp_f32 v41,v41\n"
"v_accvgpr_read_b32 v120,a151\n"
"v_mfma_f32_16x16x32_bf16 v[8:11],a[72:75],a[24:27],v[8:11]\n"
"v_exp_f32 v42,v42\n"
"v_xor_b32 v119,s80,v119\n"
"v_mfma_f32_16x16x32_bf16 v[8:11],a[76:79],a[28:31],v[8:11]\n"
"v_exp_f32 v43,v43\n"
"v_xor_b32 v120,s81,v120\n"
"v_mfma_f32_16x16x32_bf16 v[12:15],a[92:95],a[28:31],v[12:15]\n"
"v_exp_f32 v44,v44\n"
"v_or_b32 v119,v119,v120\n"
"v_mfma_f32_16x16x32_bf16 v[12:15],a[88:91],a[24:27],v[12:15]\n"
"s_add_u32 s18,s8,0\n"
"v_exp_f32 v45,v45\n"
"v_cmp_ne_u32 s[14:15],0,v119\n"
"v_mfma_f32_16x16x32_bf16 v[12:15],a[84:87],a[20:23],v[12:15]\n"
"s_cmp_lg_u32 s14,0\n"
"s_cselect_b32 s18,2146435072,s18\n"
"v_exp_f32 v46,v46\n"
"v_mfma_f32_16x16x32_bf16 v[12:15],a[80:83],a[16:19],v[12:15]\n"
"s_add_u32 m0,s30,0\n"
"ds_read_b128 a[64:67],v113 offset:16384\n"
"s_waitcnt lgkmcnt(1)\n"
"v_mfma_f32_16x16x32_bf16 v[128:131],a[96:99],v[64:67],v[128:131]\n"
"ds_read_b128 a[68:71],v113 offset:17408\n"
"ds_read_b128 a[72:75],v113 offset:18432\n"
"ds_read_b128 a[76:79],v113 offset:19456\n"
"v_mfma_f32_16x16x32_bf16 v[160:163],a[96:99],v[68:71],v[160:163]\n"
"ds_read_b128 a[80:83],v113 offset:20480\n"
"v_exp_f32 v47,v47\n"
"v_mfma_f32_16x16x32_bf16 v[192:195],a[96:99],v[72:75],v[192:195]\n"
"ds_read_b128 a[84:87],v113 offset:21504\n"
"ds_read_b128 a[88:91],v113 offset:22528\n"
"ds_read_b128 a[92:95],v113 offset:23552\n"
"v_exp_f32 v48,v48\n"
"v_mfma_f32_16x16x32_bf16 v[224:227],a[96:99],v[76:79],v[224:227]\n"
"buffer_load_dwordx4 v112,s[24:27],s18 offen offset:0 sc1 lds\n"
"v_exp_f32 v49,v49\n"
"v_mfma_f32_16x16x32_bf16 a[140:143],a[144:147],v[76:79],a[140:143]\n"
"buffer_load_dwordx4 v112,s[24:27],s18 offen offset:1024 sc1 lds\n"
"v_mfma_f32_16x16x32_bf16 v[228:231],a[100:103],v[76:79],v[228:231]\n"
"buffer_load_dwordx4 v112,s[24:27],s18 offen offset:2048 sc1 lds\n"
"buffer_load_dwordx4 v112,s[24:27],s18 offen offset:3072 sc1 lds\n"
"v_mfma_f32_16x16x32_bf16 v[132:135],a[100:103],v[64:67],v[132:135]\n"
"s_add_u32 m0,m0,4096\n"
"v_exp_f32 v50,v50\n"
"v_cvt_pk_bf16_f32 v83,v38,v39\n"
"v_mfma_f32_16x16x32_bf16 v[164:167],a[100:103],v[68:71],v[164:167]\n"
"buffer_load_dwordx4 v117,s[24:27],s18 offen offset:0 sc1 lds\n"
"v_cvt_pk_bf16_f32 v84,v40,v41\n"
"v_cvt_pk_bf16_f32 v85,v42,v43\n"
"v_mfma_f32_16x16x32_bf16 v[196:199],a[100:103],v[72:75],v[196:199]\n"
"v_exp_f32 v51,v51\n"
"v_cvt_pk_bf16_f32 v86,v44,v45\n"
"v_mfma_f32_16x16x32_bf16 a[136:139],a[144:147],v[72:75],a[136:139]\n"
"v_exp_f32 v52,v52\n"
"v_cvt_pk_bf16_f32 v87,v46,v47\n"
"v_mfma_f32_16x16x32_bf16 v[200:203],a[104:107],v[72:75],v[200:203]\n"
"buffer_load_dwordx4 v117,s[24:27],s18 offen offset:1024 sc1 lds\n"
"v_cvt_pk_bf16_f32 v88,v48,v49\n"
"v_cvt_pk_bf16_f32 v89,v50,v51\n"
"v_mfma_f32_16x16x32_bf16 v[232:235],a[104:107],v[76:79],v[232:235]\n"
"v_exp_f32 v53,v53\n"
"v_max3_f32 v118,v118,v19,v20\n"
"v_mfma_f32_16x16x32_bf16 v[136:139],a[104:107],v[64:67],v[136:139]\n"
"v_cvt_pk_bf16_f32 v90,v52,v53\n"
"v_max3_f32 v118,v118,v21,v22\n"
"v_max3_f32 v118,v118,v23,v28\n"
"v_exp_f32 v54,v54\n"
"v_mfma_f32_16x16x32_bf16 v[168:171],a[104:107],v[68:71],v[168:171]\n"
"buffer_load_dwordx4 v117,s[24:27],s18 offen offset:2048 sc1 lds\n"
"v_mfma_f32_16x16x32_bf16 a[132:135],a[144:147],v[68:71],a[132:135]\n"
"v_exp_f32 v55,v55\n"
"v_max3_f32 v118,v118,v29,v30\n"
"v_mfma_f32_16x16x32_bf16 v[172:175],a[108:111],v[68:71],v[172:175]\n"
"v_cvt_pk_bf16_f32 v91,v54,v55\n"
"v_max3_f32 v118,v118,v31,v24\n"
"v_max3_f32 v118,v118,v25,v26\n"
"v_exp_f32 v56,v56\n"
"v_mfma_f32_16x16x32_bf16 v[204:207],a[108:111],v[72:75],v[204:207]\n"
"buffer_load_dwordx4 v117,s[24:27],s18 offen offset:3072 sc1 lds\n"
"v_mfma_f32_16x16x32_bf16 v[236:239],a[108:111],v[76:79],v[236:239]\n"
"s_sub_u32 s82,s3,32768\n"
"v_max3_f32 v118,v118,v27,v8\n"
"v_max3_f32 v118,v118,v9,v10\n"
"v_mfma_f32_16x16x32_bf16 v[140:143],a[108:111],v[64:67],v[140:143]\n"
"v_exp_f32 v57,v57\n"
"v_max3_f32 v118,v118,v11,v12\n"
"v_exp_f32 v58,v58\n"
"v_mfma_f32_16x16x32_bf16 a[128:131],a[144:147],v[64:67],a[128:131]\n"
"buffer_load_dwordx4 a[0:3],v112,s[24:27],s82 offen offset:0 sc1\n"
"v_mfma_f32_16x16x32_bf16 v[144:147],a[112:115],v[64:67],v[144:147]\n"
"v_max3_f32 v118,v118,v13,v14\n"
"v_max_f32 v118,v118,v15\n"
"v_mfma_f32_16x16x32_bf16 v[176:179],a[112:115],v[68:71],v[176:179]\n"
"v_exp_f32 v59,v59\n"
"v_mfma_f32_16x16x32_bf16 v[208:211],a[112:115],v[72:75],v[208:211]\n"
"v_cvt_pk_bf16_f32 v92,v56,v57\n"
"v_cvt_pk_bf16_f32 v93,v58,v59\n"
"v_exp_f32 v60,v60\n"
"v_mfma_f32_16x16x32_bf16 v[240:243],a[112:115],v[76:79],v[240:243]\n"
"buffer_load_dwordx4 a[4:7],v112,s[24:27],s82 offen offset:1024 sc1\n"
"v_mfma_f32_16x16x32_bf16 v[244:247],a[116:119],v[76:79],v[244:247]\n"
"v_exp_f32 v61,v61\n"
"v_mfma_f32_16x16x32_bf16 v[148:151],a[116:119],v[64:67],v[148:151]\n"
"v_cmp_lt_f32 vcc,s93,v118\n"
"v_cvt_pk_bf16_f32 v94,v60,v61\n"
"v_mfma_f32_16x16x32_bf16 v[180:183],a[116:119],v[68:71],v[180:183]\n"
"buffer_load_dwordx4 a[8:11],v112,s[24:27],s82 offen offset:2048 sc1\n"
"v_mfma_f32_16x16x32_bf16 v[212:215],a[116:119],v[72:75],v[212:215]\n"
"v_exp_f32 v62,v62\n"
"v_mfma_f32_16x16x32_bf16 v[216:219],a[120:123],v[72:75],v[216:219]\n"
"v_exp_f32 v63,v63\n"
"v_mfma_f32_16x16x32_bf16 v[248:251],a[120:123],v[76:79],v[248:251]\n"
"v_cvt_pk_bf16_f32 v95,v62,v63\n"
"v_mfma_f32_16x16x32_bf16 v[152:155],a[120:123],v[64:67],v[152:155]\n"
"buffer_load_dwordx4 a[12:15],v112,s[24:27],s82 offen offset:3072 sc1\n"
"v_mfma_f32_16x16x32_bf16 v[184:187],a[120:123],v[68:71],v[184:187]\n"
"v_mfma_f32_16x16x32_bf16 v[188:191],a[124:127],v[68:71],v[188:191]\n"
"v_mfma_f32_16x16x32_bf16 v[220:223],a[124:127],v[72:75],v[220:223]\n"
"v_mfma_f32_16x16x32_bf16 v[252:255],a[124:127],v[76:79],v[252:255]\n"
"v_mfma_f32_16x16x32_bf16 v[156:159],a[124:127],v[64:67],v[156:159]\n"
"s_cbranch_vccnz .Lfk_cs_a3\n"
".Lfk_cr_a3:\n"
"s_waitcnt vmcnt(17) lgkmcnt(0)\n"
"s_barrier\n"
"v_mfma_f32_16x16x32_bf16 v[56:59],a[64:67],a[48:51],v[108:111]\n"
"ds_read_b128 a[96:99],v114 offset:57344\n"
"ds_read_b128 a[100:103],v114 offset:58368\n"
"ds_read_b128 a[104:107],v114 offset:59392\n"
"v_mfma_f32_16x16x32_bf16 v[56:59],a[68:71],a[52:55],v[56:59]\n"
"ds_read_b128 a[108:111],v114 offset:60416\n"
"ds_read_b128 a[112:115],v114 offset:61440\n"
"ds_read_b128 a[116:119],v114 offset:62464\n"
"v_mfma_f32_16x16x32_bf16 v[56:59],a[72:75],a[56:59],v[56:59]\n"
"ds_read_b128 a[120:123],v114 offset:63488\n"
"ds_read_b128 a[124:127],v114 offset:64512\n"
"v_add_f32 v48,0x80000000,v126\n"
"v_mfma_f32_16x16x32_bf16 v[56:59],a[76:79],a[60:63],v[56:59]\n"
"v_min_f32 v48,v48,v104\n"
"v_add_f32 v49,0xf149f2ca,v126\n"
"v_min_f32 v49,v49,v105\n"
"v_mfma_f32_16x16x32_bf16 v[60:63],a[92:95],a[60:63],v[108:111]\n"
"v_add_f32 v50,0xf1c9f2ca,v126\n"
"v_add_f32 v51,0xf2177617,v126\n"
"v_mfma_f32_16x16x32_bf16 v[60:63],a[88:91],a[56:59],v[60:63]\n"
"v_add_f32 v52,0xf349f2ca,v126\n"
"v_min_f32 v52,v52,v104\n"
"v_mfma_f32_16x16x32_bf16 v[60:63],a[84:87],a[52:55],v[60:63]\n"
"v_add_f32 v53,0xf35691f6,v126\n"
"v_min_f32 v53,v53,v105\n"
"v_add_f32 v54,0xf3633123,v126\n"
"v_mfma_f32_16x16x32_bf16 v[60:63],a[80:83],a[48:51],v[60:63]\n"
"v_min_f32 v54,v54,v106\n"
"v_add_f32 v55,0xf36fd050,v126\n"
"v_min_f32 v55,v55,v107\n"
"v_min_f32 v51,v51,v107\n"
"v_min_f32 v50,v50,v106\n"
"v_mfma_f32_16x16x32_bf16 v[52:55],a[80:83],a[32:35],v[52:55]\n"
"s_add_u32 m0,s67,16384\n"
"v_exp_f32 v8,v8\n"
"v_mfma_f32_16x16x32_bf16 v[52:55],a[84:87],a[36:39],v[52:55]\n"
"buffer_load_dwordx4 v112,s[24:27],s64 offen offset:0 sc1 lds\n"
"buffer_load_dwordx4 v112,s[24:27],s64 offen offset:1024 sc1 lds\n"
"v_exp_f32 v9,v9\n"
"v_mfma_f32_16x16x32_bf16 v[52:55],a[88:91],a[40:43],v[52:55]\n"
"v_exp_f32 v10,v10\n"
"v_cvt_pk_bf16_f32 v68,v8,v9\n"
"v_mfma_f32_16x16x32_bf16 v[52:55],a[92:95],a[44:47],v[52:55]\n"
"v_exp_f32 v11,v11\n"
"v_max3_f32 v118,v56,v57,v58\n"
"v_exp_f32 v12,v12\n"
"v_mfma_f32_16x16x32_bf16 v[48:51],a[76:79],a[44:47],v[48:51]\n"
"buffer_load_dwordx4 v112,s[24:27],s64 offen offset:2048 sc1 lds\n"
"v_mfma_f32_16x16x32_bf16 v[48:51],a[72:75],a[40:43],v[48:51]\n"
"v_exp_f32 v13,v13\n"
"v_cvt_pk_bf16_f32 v69,v10,v11\n"
"v_mfma_f32_16x16x32_bf16 v[48:51],a[68:71],a[36:39],v[48:51]\n"
"v_exp_f32 v14,v14\n"
"v_cvt_pk_bf16_f32 v70,v12,v13\n"
"v_exp_f32 v15,v15\n"
"v_mfma_f32_16x16x32_bf16 v[48:51],a[64:67],a[32:35],v[48:51]\n"
"ds_read_b128 a[64:67],v113 offset:49152\n"
"s_waitcnt lgkmcnt(1)\n"
"v_mfma_f32_16x16x32_bf16 v[128:131],a[96:99],v[80:83],v[128:131]\n"
"ds_read_b128 a[68:71],v113 offset:50176\n"
"v_exp_f32 v16,v16\n"
"v_mfma_f32_16x16x32_bf16 v[160:163],a[96:99],v[84:87],v[160:163]\n"
"ds_read_b128 a[72:75],v113 offset:51200\n"
"v_exp_f32 v17,v17\n"
"v_mfma_f32_16x16x32_bf16 v[192:195],a[96:99],v[88:91],v[192:195]\n"
"ds_read_b128 a[76:79],v113 offset:52224\n"
"ds_read_b128 a[80:83],v113 offset:53248\n"
"ds_read_b128 a[84:87],v113 offset:54272\n"
"v_mfma_f32_16x16x32_bf16 v[224:227],a[96:99],v[92:95],v[224:227]\n"
"ds_read_b128 a[88:91],v113 offset:55296\n"
"v_exp_f32 v18,v18\n"
"v_mfma_f32_16x16x32_bf16 a[140:143],a[144:147],v[92:95],a[140:143]\n"
"ds_read_b128 a[92:95],v113 offset:56320\n"
"buffer_load_dwordx4 v112,s[24:27],s64 offen offset:3072 sc1 lds\n"
"s_add_u32 s64,s64,16384\n"
"v_mfma_f32_16x16x32_bf16 v[228:231],a[100:103],v[92:95],v[228:231]\n"
"s_sub_u32 s82,s3,49152\n"
"v_exp_f32 v19,v19\n"
"v_mfma_f32_16x16x32_bf16 v[132:135],a[100:103],v[80:83],v[132:135]\n"
"buffer_load_dwordx4 a[16:19],v112,s[24:27],s82 offen offset:0 sc1\n"
"v_cvt_pk_bf16_f32 v71,v14,v15\n"
"v_cvt_pk_bf16_f32 v72,v16,v17\n"
"v_cvt_pk_bf16_f32 v73,v18,v19\n"
"v_exp_f32 v20,v20\n"
"v_mfma_f32_16x16x32_bf16 v[164:167],a[100:103],v[84:87],v[164:167]\n"
"buffer_load_dwordx4 a[20:23],v112,s[24:27],s82 offen offset:1024 sc1\n"
"v_mfma_f32_16x16x32_bf16 v[196:199],a[100:103],v[88:91],v[196:199]\n"
"v_exp_f32 v21,v21\n"
"v_max3_f32 v118,v118,v59,v60\n"
"v_mfma_f32_16x16x32_bf16 a[136:139],a[144:147],v[88:91],a[136:139]\n"
"v_cvt_pk_bf16_f32 v74,v20,v21\n"
"v_max3_f32 v118,v118,v61,v62\n"
"v_max3_f32 v118,v118,v63,v52\n"
"v_mfma_f32_16x16x32_bf16 v[200:203],a[104:107],v[88:91],v[200:203]\n"
"v_exp_f32 v22,v22\n"
"v_max3_f32 v118,v118,v53,v54\n"
"v_mfma_f32_16x16x32_bf16 v[232:235],a[104:107],v[92:95],v[232:235]\n"
"buffer_load_dwordx4 a[24:27],v112,s[24:27],s82 offen offset:2048 sc1\n"
"v_max3_f32 v118,v118,v55,v48\n"
"v_max3_f32 v118,v118,v49,v50\n"
"v_mfma_f32_16x16x32_bf16 v[136:139],a[104:107],v[80:83],v[136:139]\n"
"v_exp_f32 v23,v23\n"
"v_max_f32 v118,v118,v51\n"
"v_mfma_f32_16x16x32_bf16 v[168:171],a[104:107],v[84:87],v[168:171]\n"
"v_cvt_pk_bf16_f32 v75,v22,v23\n"
"v_cmp_lt_f32 vcc,s93,v118\n"
"v_mfma_f32_16x16x32_bf16 a[132:135],a[144:147],v[84:87],a[132:135]\n"
"buffer_load_dwordx4 a[28:31],v112,s[24:27],s82 offen offset:3072 sc1\n"
"v_mfma_f32_16x16x32_bf16 v[172:175],a[108:111],v[84:87],v[172:175]\n"
"v_exp_f32 v24,v24\n"
"v_mfma_f32_16x16x32_bf16 v[204:207],a[108:111],v[88:91],v[204:207]\n"
"s_sub_u32 s82,s3,65536\n"
"v_exp_f32 v25,v25\n"
"v_mfma_f32_16x16x32_bf16 v[236:239],a[108:111],v[92:95],v[236:239]\n"
"v_cvt_pk_bf16_f32 v76,v24,v25\n"
"v_mfma_f32_16x16x32_bf16 v[140:143],a[108:111],v[80:83],v[140:143]\n"
"buffer_load_dwordx4 v[32:35],v112,s[24:27],s82 offen offset:0 sc1\n"
"v_mfma_f32_16x16x32_bf16 a[128:131],a[144:147],v[80:83],a[128:131]\n"
"v_exp_f32 v26,v26\n"
"v_mfma_f32_16x16x32_bf16 v[144:147],a[112:115],v[80:83],v[144:147]\n"
"v_exp_f32 v27,v27\n"
"v_mfma_f32_16x16x32_bf16 v[176:179],a[112:115],v[84:87],v[176:179]\n"
"buffer_load_dwordx4 v[36:39],v112,s[24:27],s82 offen offset:1024 sc1\n"
"v_mfma_f32_16x16x32_bf16 v[208:211],a[112:115],v[88:91],v[208:211]\n"
"v_exp_f32 v28,v28\n"
"v_mfma_f32_16x16x32_bf16 v[240:243],a[112:115],v[92:95],v[240:243]\n"
"v_cvt_pk_bf16_f32 v77,v26,v27\n"
"v_mfma_f32_16x16x32_bf16 v[244:247],a[116:119],v[92:95],v[244:247]\n"
"v_exp_f32 v29,v29\n"
"v_mfma_f32_16x16x32_bf16 v[148:151],a[116:119],v[80:83],v[148:151]\n"
"buffer_load_dwordx4 v[40:43],v112,s[24:27],s82 offen offset:2048 sc1\n"
"v_mfma_f32_16x16x32_bf16 v[180:183],a[116:119],v[84:87],v[180:183]\n"
"v_exp_f32 v30,v30\n"
"v_mfma_f32_16x16x32_bf16 v[212:215],a[116:119],v[88:91],v[212:215]\n"
"v_cvt_pk_bf16_f32 v78,v28,v29\n"
"v_mfma_f32_16x16x32_bf16 v[216:219],a[120:123],v[88:91],v[216:219]\n"
"v_exp_f32 v31,v31\n"
"v_mfma_f32_16x16x32_bf16 v[248:251],a[120:123],v[92:95],v[248:251]\n"
"v_cvt_pk_bf16_f32 v79,v30,v31\n"
"v_mfma_f32_16x16x32_bf16 v[152:155],a[120:123],v[80:83],v[152:155]\n"
"buffer_load_dwordx4 v[44:47],v112,s[24:27],s82 offen offset:3072 sc1\n"
"v_mfma_f32_16x16x32_bf16 v[184:187],a[120:123],v[84:87],v[184:187]\n"
"v_mfma_f32_16x16x32_bf16 v[188:191],a[124:127],v[84:87],v[188:191]\n"
"v_mfma_f32_16x16x32_bf16 v[220:223],a[124:127],v[88:91],v[220:223]\n"
"v_mfma_f32_16x16x32_bf16 v[252:255],a[124:127],v[92:95],v[252:255]\n"
"v_mfma_f32_16x16x32_bf16 v[156:159],a[124:127],v[80:83],v[156:159]\n"
"s_cbranch_vccnz .Lfk_cs_a0\n"
".Lfk_cr_a0:\n"
"s_load_dwordx2 s[36:37],s[0:1],0\n"
"s_load_dwordx2 s[44:45],s[0:1],80\n"
"s_load_dwordx2 s[48:49],s[0:1],88\n"
"s_lshl_b32 s82,s78,2\n"
"s_add_u32 s82,s82,65536\n"
"v_add_u32 v113,s82,v112\n"
"s_lshr_b32 s82,s78,2\n"
"s_add_u32 s82,s82,122880\n"
"v_lshrrev_b32 v115,1,v112\n"
"v_add_u32 v116,s82,v115\n"
"s_waitcnt lgkmcnt(0)\n"
"s_barrier\n"
"v_add_f32 v24,0x80000000,v126\n"
"v_min_f32 v24,v24,v108\n"
"v_add_f32 v25,0xf149f2ca,v126\n"
"v_min_f32 v25,v25,v109\n"
"v_add_f32 v26,0xf1c9f2ca,v126\n"
"v_min_f32 v26,v26,v110\n"
"v_add_f32 v27,0xf2177617,v126\n"
"v_min_f32 v27,v27,v111\n"
"v_add_f32 v28,0xf349f2ca,v126\n"
"v_min_f32 v28,v28,v108\n"
"v_add_f32 v29,0xf35691f6,v126\n"
"v_min_f32 v29,v29,v109\n"
"v_add_f32 v30,0xf3633123,v126\n"
"v_min_f32 v30,v30,v110\n"
"v_add_f32 v31,0xf36fd050,v126\n"
"v_min_f32 v31,v31,v111\n"
"v_exp_f32 v48,v48\n"
"v_mfma_f32_16x16x32_bf16 v[24:27],a[64:67],a[48:51],v[24:27]\n"
"s_add_u32 s83,s18,2097152\n"
"ds_read_b128 a[96:99],v114 offset:40960\n"
"ds_read_b128 a[100:103],v114 offset:41984\n"
"v_exp_f32 v49,v49\n"
"v_mfma_f32_16x16x32_bf16 v[24:27],a[68:71],a[52:55],v[24:27]\n"
"s_add_u32 m0,s30,32768\n"
"ds_read_b128 a[104:107],v114 offset:43008\n"
"ds_read_b128 a[108:111],v114 offset:44032\n"
"v_mfma_f32_16x16x32_bf16 v[24:27],a[72:75],a[56:59],v[24:27]\n"
"ds_read_b128 a[112:115],v114 offset:45056\n"
"ds_read_b128 a[116:119],v114 offset:46080\n"
"ds_read_b128 a[120:123],v114 offset:47104\n"
"ds_read_b128 a[124:127],v114 offset:48128\n"
"v_exp_f32 v50,v50\n"
"v_mfma_f32_16x16x32_bf16 v[24:27],a[76:79],a[60:63],v[24:27]\n"
"buffer_load_dwordx4 v112,s[24:27],s83 offen offset:0 sc1 lds\n"
"v_cvt_pk_bf16_f32 v88,v48,v49\n"
"v_mfma_f32_16x16x32_bf16 v[28:31],a[92:95],a[60:63],v[28:31]\n"
"buffer_load_dwordx4 v112,s[24:27],s83 offen offset:1024 sc1 lds\n"
"v_accvgpr_read_b32 v97,a128\n"
"v_cvt_pk_bf16_f32 v128,v128,v129\n"
"v_cvt_pk_bf16_f32 v129,v130,v131\n"
"v_mfma_f32_16x16x32_bf16 v[28:31],a[88:91],a[56:59],v[28:31]\n"
"v_exp_f32 v51,v51\n"
"v_cvt_pk_bf16_f32 v130,v132,v133\n"
"v_cvt_pk_bf16_f32 v131,v134,v135\n"
"v_cvt_pk_bf16_f32 v89,v50,v51\n"
"v_mfma_f32_16x16x32_bf16 v[28:31],a[84:87],a[52:55],v[28:31]\n"
"ds_write_b128 v113,v[128:131] offset:0\n"
"v_cvt_pk_bf16_f32 v136,v136,v137\n"
"v_cvt_pk_bf16_f32 v137,v138,v139\n"
"v_exp_f32 v52,v52\n"
"v_mfma_f32_16x16x32_bf16 v[28:31],a[80:83],a[48:51],v[28:31]\n"
"buffer_load_dwordx4 v112,s[24:27],s83 offen offset:2048 sc1 lds\n"
"v_max3_f32 v118,v24,v25,v26\n"
"s_waitcnt lgkmcnt(1)\n"
"v_mfma_f32_16x16x32_bf16 v[160:163],a[96:99],v[68:71],v[160:163]\n"
"v_cvt_pk_bf16_f32 v138,v140,v141\n"
"v_cvt_pk_bf16_f32 v139,v142,v143\n"
"v_cvt_pk_bf16_f32 v144,v144,v145\n"
"v_mfma_f32_16x16x32_bf16 a[132:135],a[144:147],v[68:71],a[132:135]\n"
"ds_write_b128 v113,v[136:139] offset:1024\n"
"buffer_load_dwordx4 v112,s[24:27],s83 offen offset:3072 sc1 lds\n"
"v_exp_f32 v53,v53\n"
"v_cvt_pk_bf16_f32 v145,v146,v147\n"
"v_mfma_f32_16x16x32_bf16 v[164:167],a[100:103],v[68:71],v[164:167]\n"
"s_add_u32 m0,m0,4096\n"
"v_exp_f32 v54,v54\n"
"v_cvt_pk_bf16_f32 v90,v52,v53\n"
"v_cvt_pk_bf16_f32 v146,v148,v149\n"
"v_mfma_f32_16x16x32_bf16 v[168:171],a[104:107],v[68:71],v[168:171]\n"
"v_max3_f32 v118,v118,v27,v28\n"
"v_max3_f32 v118,v118,v29,v30\n"
"v_max_f32 v118,v118,v31\n"
"v_cmp_lt_f32 vcc,s93,v118\n"
"v_exp_f32 v55,v55\n"
"v_mfma_f32_16x16x32_bf16 v[172:175],a[108:111],v[68:71],v[172:175]\n"
"buffer_load_dwordx4 v117,s[24:27],s83 offen offset:0 sc1 lds\n"
"v_cvt_pk_bf16_f32 v147,v150,v151\n"
"v_cvt_pk_bf16_f32 v91,v54,v55\n"
"v_mfma_f32_16x16x32_bf16 v[176:179],a[112:115],v[68:71],v[176:179]\n"
"ds_write_b128 v113,v[144:147] offset:2048\n"
"v_cvt_pk_bf16_f32 v152,v152,v153\n"
"v_cvt_pk_bf16_f32 v153,v154,v155\n"
"v_exp_f32 v56,v56\n"
"v_mfma_f32_16x16x32_bf16 v[180:183],a[116:119],v[68:71],v[180:183]\n"
"buffer_load_dwordx4 v117,s[24:27],s83 offen offset:1024 sc1 lds\n"
"v_cvt_pk_bf16_f32 v154,v156,v157\n"
"v_cvt_pk_bf16_f32 v155,v158,v159\n"
"v_mfma_f32_16x16x32_bf16 v[184:187],a[120:123],v[68:71],v[184:187]\n"
"ds_write_b128 v113,v[152:155] offset:3072\n"
"ds_write_b64 v116,v[96:97] offset:0\n"
"v_mfma_f32_16x16x32_bf16 v[188:191],a[124:127],v[68:71],v[188:191]\n"
"v_exp_f32 v57,v57\n"
"v_mfma_f32_16x16x32_bf16 v[192:195],a[96:99],v[72:75],v[192:195]\n"
"buffer_load_dwordx4 v117,s[24:27],s83 offen offset:2048 sc1 lds\n"
"v_cvt_pk_bf16_f32 v92,v56,v57\n"
"v_mfma_f32_16x16x32_bf16 a[136:139],a[144:147],v[72:75],a[136:139]\n"
"v_exp_f32 v58,v58\n"
"v_exp_f32 v59,v59\n"
"v_mfma_f32_16x16x32_bf16 v[196:199],a[100:103],v[72:75],v[196:199]\n"
"buffer_load_dwordx4 v117,s[24:27],s83 offen offset:3072 sc1 lds\n"
"v_accvgpr_read_b32 v101,a132\n"
"v_mfma_f32_16x16x32_bf16 v[200:203],a[104:107],v[72:75],v[200:203]\n"
"s_sub_u32 s82,s3,81920\n"
"v_cvt_pk_bf16_f32 v93,v58,v59\n"
"v_cvt_pk_bf16_f32 v160,v160,v161\n"
"v_cvt_pk_bf16_f32 v161,v162,v163\n"
"v_mfma_f32_16x16x32_bf16 v[204:207],a[108:111],v[72:75],v[204:207]\n"
"v_exp_f32 v60,v60\n"
"v_cvt_pk_bf16_f32 v162,v164,v165\n"
"v_cvt_pk_bf16_f32 v163,v166,v167\n"
"v_mfma_f32_16x16x32_bf16 v[208:211],a[112:115],v[72:75],v[208:211]\n"
"buffer_load_dwordx4 a[32:35],v112,s[24:27],s82 offen offset:0 sc1\n"
"ds_write_b128 v113,v[160:163] offset:4096\n"
"v_cvt_pk_bf16_f32 v168,v168,v169\n"
"v_cvt_pk_bf16_f32 v169,v170,v171\n"
"v_mfma_f32_16x16x32_bf16 v[212:215],a[116:119],v[72:75],v[212:215]\n"
"v_exp_f32 v61,v61\n"
"v_cvt_pk_bf16_f32 v170,v172,v173\n"
"v_cvt_pk_bf16_f32 v171,v174,v175\n"
"v_cvt_pk_bf16_f32 v94,v60,v61\n"
"v_mfma_f32_16x16x32_bf16 v[216:219],a[120:123],v[72:75],v[216:219]\n"
"ds_write_b128 v113,v[168:171] offset:5120\n"
"v_cvt_pk_bf16_f32 v176,v176,v177\n"
"v_cvt_pk_bf16_f32 v177,v178,v179\n"
"v_exp_f32 v62,v62\n"
"v_mfma_f32_16x16x32_bf16 v[220:223],a[124:127],v[72:75],v[220:223]\n"
"buffer_load_dwordx4 a[36:39],v112,s[24:27],s82 offen offset:1024 sc1\n"
"v_cvt_pk_bf16_f32 v178,v180,v181\n"
"v_cvt_pk_bf16_f32 v179,v182,v183\n"
"v_mfma_f32_16x16x32_bf16 v[224:227],a[96:99],v[76:79],v[224:227]\n"
"ds_write_b128 v113,v[176:179] offset:6144\n"
"v_cvt_pk_bf16_f32 v184,v184,v185\n"
"v_cvt_pk_bf16_f32 v185,v186,v187\n"
"v_mfma_f32_16x16x32_bf16 a[140:143],a[144:147],v[76:79],a[140:143]\n"
"v_exp_f32 v63,v63\n"
"v_cvt_pk_bf16_f32 v186,v188,v189\n"
"v_cvt_pk_bf16_f32 v187,v190,v191\n"
"v_mfma_f32_16x16x32_bf16 v[228:231],a[100:103],v[76:79],v[228:231]\n"
"buffer_load_dwordx4 a[40:43],v112,s[24:27],s82 offen offset:2048 sc1\n"
"ds_write_b128 v113,v[184:187] offset:7168\n"
"v_mfma_f32_16x16x32_bf16 v[232:235],a[104:107],v[76:79],v[232:235]\n"
"ds_write_b64 v116,v[100:101] offset:512\n"
"v_cvt_pk_bf16_f32 v95,v62,v63\n"
"v_mfma_f32_16x16x32_bf16 v[236:239],a[108:111],v[76:79],v[236:239]\n"
"buffer_load_dwordx4 a[44:47],v112,s[24:27],s82 offen offset:3072 sc1\n"
"v_mfma_f32_16x16x32_bf16 v[240:243],a[112:115],v[76:79],v[240:243]\n"
"s_sub_u32 s82,s3,131072\n"
"buffer_load_dwordx4 v[128:131],v112,s[24:27],s82 offen offset:0 sc1\n"
"buffer_load_dwordx4 v[132:135],v112,s[24:27],s82 offen offset:1024 sc1\n"
"buffer_load_dwordx4 v[136:139],v112,s[24:27],s82 offen offset:2048 sc1\n"
"buffer_load_dwordx4 v[140:143],v112,s[24:27],s82 offen offset:3072 sc1\n"
"v_mfma_f32_16x16x32_bf16 v[244:247],a[116:119],v[76:79],v[244:247]\n"
"s_sub_u32 s82,s3,147456\n"
"buffer_load_dwordx4 v[144:147],v112,s[24:27],s82 offen offset:0 sc1\n"
"buffer_load_dwordx4 v[148:151],v112,s[24:27],s82 offen offset:1024 sc1\n"
"v_mfma_f32_16x16x32_bf16 v[248:251],a[120:123],v[76:79],v[248:251]\n"
"buffer_load_dwordx4 v[152:155],v112,s[24:27],s82 offen offset:2048 sc1\n"
"buffer_load_dwordx4 v[156:159],v112,s[24:27],s82 offen offset:3072 sc1\n"
"v_mfma_f32_16x16x32_bf16 v[252:255],a[124:127],v[76:79],v[252:255]\n"
"s_cbranch_vccnz .Lfk_cs_a1\n"
".Lfk_cr_a1:\n"
"s_waitcnt vmcnt(28) lgkmcnt(0)\n"
"s_barrier\n"
"ds_read_b128 a[96:99],v114 offset:8192\n"
"ds_read_b128 a[100:103],v114 offset:9216\n"
"ds_read_b128 a[104:107],v114 offset:10240\n"
"ds_read_b128 a[108:111],v114 offset:11264\n"
"ds_read_b128 a[112:115],v114 offset:12288\n"
"ds_read_b128 a[116:119],v114 offset:13312\n"
"ds_read_b128 a[120:123],v114 offset:14336\n"
"ds_read_b128 a[124:127],v114 offset:15360\n"
"v_xor_b32 v113,32768,v113\n"
"v_xor_b32 v116,2048,v116\n"
"ds_read_b64 v[98:99],v116 offset:0\n"
"ds_read_b128 a[64:67],v113 offset:0\n"
"ds_read_b128 a[68:71],v113 offset:1024\n"
"ds_read_b128 a[72:75],v113 offset:2048\n"
"ds_read_b128 a[76:79],v113 offset:3072\n"
"ds_read_b64 v[102:103],v116 offset:512\n"
"ds_read_b128 a[80:83],v113 offset:4096\n"
"ds_read_b128 a[84:87],v113 offset:5120\n"
"ds_read_b128 a[88:91],v113 offset:6144\n"
"ds_read_b128 a[92:95],v113 offset:7168\n"
"v_exp_f32 v24,v24\n"
"v_exp_f32 v25,v25\n"
"v_exp_f32 v26,v26\n"
"v_exp_f32 v27,v27\n"
"v_exp_f32 v28,v28\n"
"v_exp_f32 v29,v29\n"
"v_exp_f32 v30,v30\n"
"v_exp_f32 v31,v31\n"
"v_cvt_pk_bf16_f32 v76,v24,v25\n"
"v_cvt_pk_bf16_f32 v77,v26,v27\n"
"v_cvt_pk_bf16_f32 v78,v28,v29\n"
"v_cvt_pk_bf16_f32 v79,v30,v31\n"
"s_waitcnt lgkmcnt(10)\n"
"v_mfma_f32_16x16x32_bf16 a[136:139],a[144:147],v[88:91],a[136:139]\n"
"s_sub_u32 s82,s3,163840\n"
"v_lshrrev_b32 v70,4,v112\n"
"v_and_b32 v71,15,v70\n"
"v_mfma_f32_16x16x32_bf16 a[140:143],a[144:147],v[92:95],a[140:143]\n"
"buffer_load_dwordx4 v[0:3],v112,s[24:27],s82 offen offset:0 sc1\n"
"v_lshrrev_b32 v70,4,v70\n"
"v_lshlrev_b32 v70,2,v70\n"
"v_mfma_f32_16x16x32_bf16 v[192:195],a[96:99],v[88:91],v[192:195]\n"
"buffer_load_dwordx4 v[4:7],v112,s[24:27],s82 offen offset:1024 sc1\n"
"v_sub_u32 v70,v71,v70\n"
"v_mov_b32 v64,0\n"
"v_mfma_f32_16x16x32_bf16 v[224:227],a[96:99],v[92:95],v[224:227]\n"
"ds_read_b128 a[96:99],v114 offset:24576\n"
"buffer_load_dwordx4 v[8:11],v112,s[24:27],s82 offen offset:2048 sc1\n"
"v_mfma_f32_16x16x32_bf16 v[196:199],a[100:103],v[88:91],v[196:199]\n"
"buffer_load_dwordx4 v[12:15],v112,s[24:27],s82 offen offset:3072 sc1\n"
"v_mov_b32 v69,0\n"
"v_mfma_f32_16x16x32_bf16 v[228:231],a[100:103],v[92:95],v[228:231]\n"
"s_sub_u32 s82,s3,180224\n"
"ds_read_b128 a[100:103],v114 offset:25600\n"
"v_mov_b32 v71,0x3f80\n"
"v_mfma_f32_16x16x32_bf16 v[200:203],a[104:107],v[88:91],v[200:203]\n"
"buffer_load_dwordx4 v[160:163],v112,s[24:27],s82 offen offset:0 sc1\n"
"v_cmp_eq_u32 vcc,0,v70\n"
"v_mov_b32 v68,0\n"
"v_mov_b32 v65,0\n"
"v_cndmask_b32 v66,0,v71,vcc\n"
"v_cmp_eq_u32 vcc,1,v70\n"
"v_mfma_f32_16x16x32_bf16 v[232:235],a[104:107],v[92:95],v[232:235]\n"
"ds_read_b128 a[104:107],v114 offset:26624\n"
"buffer_load_dwordx4 v[164:167],v112,s[24:27],s82 offen offset:1024 sc1\n"
"v_mov_b32 v72,0x3f800000\n"
"v_cndmask_b32 v66,v66,v72,vcc\n"
"v_cmp_eq_u32 vcc,2,v70\n"
"v_mfma_f32_16x16x32_bf16 v[204:207],a[108:111],v[88:91],v[204:207]\n"
"buffer_load_dwordx4 v[168:171],v112,s[24:27],s82 offen offset:2048 sc1\n"
"v_cndmask_b32 v67,0,v71,vcc\n"
"v_cmp_eq_u32 vcc,3,v70\n"
"v_mfma_f32_16x16x32_bf16 v[236:239],a[108:111],v[92:95],v[236:239]\n"
"ds_read_b128 a[108:111],v114 offset:27648\n"
"buffer_load_dwordx4 v[172:175],v112,s[24:27],s82 offen offset:3072 sc1\n"
"v_mfma_f32_16x16x32_bf16 v[208:211],a[112:115],v[88:91],v[208:211]\n"
"v_cndmask_b32 v67,v67,v72,vcc\n"
"s_sub_u32 s82,s3,196608\n"
"v_mfma_f32_16x16x32_bf16 v[240:243],a[112:115],v[92:95],v[240:243]\n"
"ds_read_b128 a[112:115],v114 offset:28672\n"
"buffer_load_dwordx4 v[176:179],v112,s[24:27],s82 offen offset:0 sc1\n"
"v_mfma_f32_16x16x32_bf16 v[212:215],a[116:119],v[88:91],v[212:215]\n"
"buffer_load_dwordx4 v[180:183],v112,s[24:27],s82 offen offset:1024 sc1\n"
"v_mfma_f32_16x16x32_bf16 v[244:247],a[116:119],v[92:95],v[244:247]\n"
"ds_read_b128 a[116:119],v114 offset:29696\n"
"buffer_load_dwordx4 v[184:187],v112,s[24:27],s82 offen offset:2048 sc1\n"
"v_mfma_f32_16x16x32_bf16 v[216:219],a[120:123],v[88:91],v[216:219]\n"
"buffer_load_dwordx4 v[188:191],v112,s[24:27],s82 offen offset:3072 sc1\n"
"v_mfma_f32_16x16x32_bf16 v[248:251],a[120:123],v[92:95],v[248:251]\n"
"ds_read_b128 a[120:123],v114 offset:30720\n"
"v_mfma_f32_16x16x32_bf16 v[220:223],a[124:127],v[88:91],v[220:223]\n"
"v_mfma_f32_16x16x32_bf16 v[252:255],a[124:127],v[92:95],v[252:255]\n"
"ds_read_b128 a[124:127],v114 offset:31744\n"
"s_waitcnt lgkmcnt(13)\n"
"v_cmp_neq_f32 vcc,v98,v104\n"
"s_nop 0\n"
"s_cbranch_vccnz .Lfk_ms_a0\n"
".Lfk_mr_a0:\n"
"v_mfma_f32_16x16x32_bf16 v[192:195],v[66:69],a[64:67],v[192:195]\n"
"v_mfma_f32_16x16x32_bf16 v[196:199],v[64:67],a[64:67],v[196:199]\n"
"v_mfma_f32_16x16x32_bf16 v[200:203],v[66:69],a[68:71],v[200:203]\n"
"v_mfma_f32_16x16x32_bf16 v[204:207],v[64:67],a[68:71],v[204:207]\n"
"v_mfma_f32_16x16x32_bf16 v[208:211],v[66:69],a[72:75],v[208:211]\n"
"v_mfma_f32_16x16x32_bf16 v[212:215],v[64:67],a[72:75],v[212:215]\n"
"v_mfma_f32_16x16x32_bf16 v[216:219],v[66:69],a[76:79],v[216:219]\n"
"v_mfma_f32_16x16x32_bf16 v[220:223],v[64:67],a[76:79],v[220:223]\n"
"s_waitcnt lgkmcnt(0)\n"
"v_mfma_f32_16x16x32_bf16 a[140:143],a[144:147],v[76:79],a[140:143]\n"
"v_mfma_f32_16x16x32_bf16 v[224:227],a[96:99],v[76:79],v[224:227]\n"
"v_mfma_f32_16x16x32_bf16 v[228:231],a[100:103],v[76:79],v[228:231]\n"
"v_accvgpr_read_b32 v122,a136\n"
"v_add_f32 v122,v122,v99\n"
"v_mfma_f32_16x16x32_bf16 v[232:235],a[104:107],v[76:79],v[232:235]\n"
"v_rcp_f32 v122,v122\n"
"v_mfma_f32_16x16x32_bf16 v[236:239],a[108:111],v[76:79],v[236:239]\n"
"v_pk_mul_f32 v[192:193],v[192:193],v[122:123] op_sel_hi:[1,0]\n"
"v_pk_mul_f32 v[194:195],v[194:195],v[122:123] op_sel_hi:[1,0]\n"
"v_pk_mul_f32 v[196:197],v[196:197],v[122:123] op_sel_hi:[1,0]\n"
"v_pk_mul_f32 v[198:199],v[198:199],v[122:123] op_sel_hi:[1,0]\n"
"v_mfma_f32_16x16x32_bf16 v[240:243],a[112:115],v[76:79],v[240:243]\n"
"v_cvt_pk_bf16_f32 v192,v192,v193\n"
"v_cvt_pk_bf16_f32 v193,v194,v195\n"
"v_mfma_f32_16x16x32_bf16 v[244:247],a[116:119],v[76:79],v[244:247]\n"
"v_cvt_pk_bf16_f32 v194,v196,v197\n"
"v_cvt_pk_bf16_f32 v195,v198,v199\n"
"v_mfma_f32_16x16x32_bf16 v[248:251],a[120:123],v[76:79],v[248:251]\n"
"buffer_store_dwordx4 v[192:195],v112,s[24:27],s70 offen offset:0 sc1\n"
"v_pk_mul_f32 v[200:201],v[200:201],v[122:123] op_sel_hi:[1,0]\n"
"v_pk_mul_f32 v[202:203],v[202:203],v[122:123] op_sel_hi:[1,0]\n"
"v_pk_mul_f32 v[204:205],v[204:205],v[122:123] op_sel_hi:[1,0]\n"
"v_pk_mul_f32 v[206:207],v[206:207],v[122:123] op_sel_hi:[1,0]\n"
"v_mfma_f32_16x16x32_bf16 v[252:255],a[124:127],v[76:79],v[252:255]\n"
"v_cvt_pk_bf16_f32 v200,v200,v201\n"
"v_cvt_pk_bf16_f32 v201,v202,v203\n"
"v_cmp_neq_f32 vcc,v102,v108\n"
"s_nop 0\n"
"s_cbranch_vccnz .Lfk_ms_a1\n"
".Lfk_mr_a1:\n"
"v_mfma_f32_16x16x32_bf16 v[224:227],v[66:69],a[80:83],v[224:227]\n"
"v_cvt_pk_bf16_f32 v202,v204,v205\n"
"v_cvt_pk_bf16_f32 v203,v206,v207\n"
"v_mfma_f32_16x16x32_bf16 v[228:231],v[64:67],a[80:83],v[228:231]\n"
"buffer_store_dwordx4 v[200:203],v112,s[24:27],s70 offen offset:1024 sc1\n"
"v_pk_mul_f32 v[208:209],v[208:209],v[122:123] op_sel_hi:[1,0]\n"
"v_pk_mul_f32 v[210:211],v[210:211],v[122:123] op_sel_hi:[1,0]\n"
"v_pk_mul_f32 v[212:213],v[212:213],v[122:123] op_sel_hi:[1,0]\n"
"v_pk_mul_f32 v[214:215],v[214:215],v[122:123] op_sel_hi:[1,0]\n"
"v_mfma_f32_16x16x32_bf16 v[232:235],v[66:69],a[84:87],v[232:235]\n"
"v_cvt_pk_bf16_f32 v208,v208,v209\n"
"v_cvt_pk_bf16_f32 v209,v210,v211\n"
"v_mfma_f32_16x16x32_bf16 v[236:239],v[64:67],a[84:87],v[236:239]\n"
"v_cvt_pk_bf16_f32 v210,v212,v213\n"
"v_cvt_pk_bf16_f32 v211,v214,v215\n"
"v_mfma_f32_16x16x32_bf16 v[240:243],v[66:69],a[88:91],v[240:243]\n"
"buffer_store_dwordx4 v[208:211],v112,s[24:27],s70 offen offset:2048 sc1\n"
"v_pk_mul_f32 v[216:217],v[216:217],v[122:123] op_sel_hi:[1,0]\n"
"v_pk_mul_f32 v[218:219],v[218:219],v[122:123] op_sel_hi:[1,0]\n"
"v_pk_mul_f32 v[220:221],v[220:221],v[122:123] op_sel_hi:[1,0]\n"
"v_pk_mul_f32 v[222:223],v[222:223],v[122:123] op_sel_hi:[1,0]\n"
"v_mfma_f32_16x16x32_bf16 v[244:247],v[64:67],a[88:91],v[244:247]\n"
"v_cvt_pk_bf16_f32 v216,v216,v217\n"
"v_cvt_pk_bf16_f32 v217,v218,v219\n"
"v_mfma_f32_16x16x32_bf16 v[248:251],v[66:69],a[92:95],v[248:251]\n"
"v_cvt_pk_bf16_f32 v218,v220,v221\n"
"v_cvt_pk_bf16_f32 v219,v222,v223\n"
"v_mfma_f32_16x16x32_bf16 v[252:255],v[64:67],a[92:95],v[252:255]\n"
"buffer_store_dwordx4 v[216:219],v112,s[24:27],s70 offen offset:3072 sc1\n"
"s_add_u32 s70,s70,262144\n"
"s_branch .Lfk_p1\n"
".Lfk_look:\n"
"s_mov_b32 s84,0x40000\n"
"s_waitcnt vmcnt(10)\n"
".Lfk_lk0:\n"
"v_xor_b32 v119,s80,v122\n"
"v_xor_b32 v120,s81,v123\n"
"v_or_b32 v119,v119,v120\n"
"v_cmp_ne_u32 s[28:29],0,v119\n"
"s_nop 0\n"
"s_and_b64 s[14:15],s[14:15],s[28:29]\n"
"s_cbranch_scc0 .Lfk_lk2\n"
"buffer_load_dwordx2 v[122:123],v115,s[24:27],s11 offen sc1\n"
"s_lshr_b64 s[28:29],s[14:15],s21\n"
"s_and_b32 s83,s28,s19\n"
"s_cbranch_scc0 .Lfk_lk2\n"
"s_waitcnt vmcnt(0)\n"
"s_sub_u32 s84,s84,1\n"
"s_cbranch_scc0 .Lfk_lk0\n"
".Lfk_lk2:\n"
"s_nop 1\n"
"s_setpc_b64 s[94:95]\n"
".Lfk_meet4:\n"
"s_mov_b32 s84,0x1000000\n"
".Lfk_meetl4:\n"
"ds_read_b32 v125,v114\n"
"s_sub_u32 s84,s84,1\n"
"s_cbranch_scc1 .Lfk_meetx4\n"
"s_waitcnt lgkmcnt(0)\n"
"v_cmp_gt_u32 vcc,s16,v125\n"
"s_nop 0\n"
"s_cbranch_vccnz .Lfk_meetl4\n"
".Lfk_meetx4:\n"
"s_setpc_b64 s[94:95]\n"
".Lfk_es_lkone:\n"
"s_call_b64 s[94:95],.Lfk_look\n"
"s_branch .Lfk_er_lkone\n"
".Lfk_z0:\n"
"s_call_b64 s[94:95],.Lfk_meet4\n"
"v_xor_b32 v119,s80,v104\n"
"v_xor_b32 v120,s81,v105\n"
"v_or_b32 v119,v119,v120\n"
"v_xor_b32 v120,s80,v106\n"
"v_or_b32 v119,v119,v120\n"
"v_xor_b32 v120,s81,v107\n"
"v_or_b32 v119,v119,v120\n"
"v_xor_b32 v120,s80,v108\n"
"v_or_b32 v119,v119,v120\n"
"v_xor_b32 v120,s81,v109\n"
"v_or_b32 v119,v119,v120\n"
"v_xor_b32 v120,s80,v110\n"
"v_or_b32 v119,v119,v120\n"
"v_xor_b32 v120,s81,v111\n"
"v_or_b32 v119,v119,v120\n"
"v_cmp_ne_u32 vcc,0,v119\n"
"s_nop 0\n"
"s_cbranch_vccz .Lfk_z1\n"
"s_mov_b32 s84,0x40000\n"
"s_mov_b32 s83,148946944\n"
"buffer_load_dwordx4 v[104:107],v112,s[24:27],s83 offen offset:0 sc1\n"
"buffer_load_dwordx4 v[108:111],v112,s[24:27],s83 offen offset:1024 sc1\n"
"s_waitcnt vmcnt(0)\n"
".Lfk_w0:\n"
"v_xor_b32 v119,s80,v104\n"
"v_xor_b32 v120,s81,v105\n"
"v_or_b32 v119,v119,v120\n"
"v_xor_b32 v120,s80,v106\n"
"v_or_b32 v119,v119,v120\n"
"v_xor_b32 v120,s81,v107\n"
"v_or_b32 v119,v119,v120\n"
"v_xor_b32 v120,s80,v108\n"
"v_or_b32 v119,v119,v120\n"
"v_xor_b32 v120,s81,v109\n"
"v_or_b32 v119,v119,v120\n"
"v_xor_b32 v120,s80,v110\n"
"v_or_b32 v119,v119,v120\n"
"v_xor_b32 v120,s81,v111\n"
"v_or_b32 v119,v119,v120\n"
"v_cmp_ne_u32 vcc,0,v119\n"
"s_nop 0\n"
"s_cbranch_vccz .Lfk_w0_x\n"
"s_sleep 8\n"
"s_mov_b64 exec,vcc\n"
"s_mov_b32 s83,148946944\n"
"buffer_load_dwordx4 v[104:107],v112,s[24:27],s83 offen offset:0 sc1\n"
"buffer_load_dwordx4 v[108:111],v112,s[24:27],s83 offen offset:1024 sc1\n"
"s_mov_b64 exec,-1\n"
"s_sub_u32 s84,s84,1\n"
"s_waitcnt vmcnt(0)\n"
"s_cbranch_scc0 .Lfk_w0\n"
".Lfk_w0_x:\n"
"s_nop 1\n"
"s_branch .Lfk_z1\n"
".Lfk_es_lkonem:\n"
"s_call_b64 s[94:95],.Lfk_look\n"
"s_branch .Lfk_er_lkonem\n"
".Lfk_es_mone:\n"
"s_call_b64 s[94:95],.Lfk_meet4\n"
"s_branch .Lfk_er_mone\n"
".Lfk_es_lktwom:\n"
"s_call_b64 s[94:95],.Lfk_look\n"
"s_branch .Lfk_er_lktwom\n"
".Lfk_es_mtwo:\n"
"s_call_b64 s[94:95],.Lfk_meet4\n"
"s_branch .Lfk_er_mtwo\n"
".Lfk_es_lkloopm:\n"
"s_call_b64 s[94:95],.Lfk_look\n"
"s_branch .Lfk_er_lkloopm\n"
".Lfk_es_me3:\n"
"s_call_b64 s[94:95],.Lfk_meet4\n"
"s_branch .Lfk_er_me3\n"
".Lfk_es_me2:\n"
"s_call_b64 s[94:95],.Lfk_meet4\n"
"s_branch .Lfk_er_me2\n"
".Lfk_p1:\n"
"s_cmp_eq_u32 s18,2146435072\n"
"s_cbranch_scc1 .Lfk_p2\n"
".Lfk_p3:\n"
"s_lshr_b32 s22,s8,2\n"
"s_and_b32 s31,s8,0xfff1ffff\n"
"s_add_u32 s18,s18,16384\n"
"s_andn2_b32 s91,s55,16384\n"
"v_add_u32 v116,s91,v112\n"
"v_accvgpr_read_b32 v122,a140\n"
"v_add_f32 v122,v122,v103\n"
"v_rcp_f32 v122,v122\n"
"s_nop 0\n"
"v_pk_mul_f32 v[224:225],v[224:225],v[122:123] op_sel_hi:[1,0]\n"
"v_pk_mul_f32 v[226:227],v[226:227],v[122:123] op_sel_hi:[1,0]\n"
"v_pk_mul_f32 v[228:229],v[228:229],v[122:123] op_sel_hi:[1,0]\n"
"v_pk_mul_f32 v[230:231],v[230:231],v[122:123] op_sel_hi:[1,0]\n"
"v_pk_mul_f32 v[232:233],v[232:233],v[122:123] op_sel_hi:[1,0]\n"
"v_pk_mul_f32 v[234:235],v[234:235],v[122:123] op_sel_hi:[1,0]\n"
"v_pk_mul_f32 v[236:237],v[236:237],v[122:123] op_sel_hi:[1,0]\n"
"v_pk_mul_f32 v[238:239],v[238:239],v[122:123] op_sel_hi:[1,0]\n"
"v_pk_mul_f32 v[240:241],v[240:241],v[122:123] op_sel_hi:[1,0]\n"
"v_pk_mul_f32 v[242:243],v[242:243],v[122:123] op_sel_hi:[1,0]\n"
"v_pk_mul_f32 v[244:245],v[244:245],v[122:123] op_sel_hi:[1,0]\n"
"v_pk_mul_f32 v[246:247],v[246:247],v[122:123] op_sel_hi:[1,0]\n"
"v_pk_mul_f32 v[248:249],v[248:249],v[122:123] op_sel_hi:[1,0]\n"
"v_pk_mul_f32 v[250:251],v[250:251],v[122:123] op_sel_hi:[1,0]\n"
"v_pk_mul_f32 v[252:253],v[252:253],v[122:123] op_sel_hi:[1,0]\n"
"v_pk_mul_f32 v[254:255],v[254:255],v[122:123] op_sel_hi:[1,0]\n"
"s_waitcnt vmcnt(24) lgkmcnt(0)\n"
"s_barrier\n"
"ds_read_b128 v[192:195],v112 offset:0\n"
"ds_read_b128 v[196:199],v112 offset:1024\n"
"ds_read_b128 v[200:203],v112 offset:2048\n"
"ds_read_b128 v[204:207],v112 offset:3072\n"
"ds_read_b128 v[208:211],v112 offset:4096\n"
"ds_read_b128 v[212:215],v112 offset:5120\n"
"ds_read_b128 v[216:219],v112 offset:6144\n"
"ds_read_b128 v[220:223],v112 offset:7168\n"
"v_cvt_pk_bf16_f32 v224,v224,v225\n"
"v_cvt_pk_bf16_f32 v225,v226,v227\n"
"v_cvt_pk_bf16_f32 v226,v228,v229\n"
"v_cvt_pk_bf16_f32 v227,v230,v231\n"
"v_cvt_pk_bf16_f32 v232,v232,v233\n"
"v_cvt_pk_bf16_f32 v233,v234,v235\n"
"v_cvt_pk_bf16_f32 v234,v236,v237\n"
"v_cvt_pk_bf16_f32 v235,v238,v239\n"
"v_cvt_pk_bf16_f32 v240,v240,v241\n"
"v_cvt_pk_bf16_f32 v241,v242,v243\n"
"v_cvt_pk_bf16_f32 v242,v244,v245\n"
"v_cvt_pk_bf16_f32 v243,v246,v247\n"
"v_cvt_pk_bf16_f32 v248,v248,v249\n"
"v_cvt_pk_bf16_f32 v249,v250,v251\n"
"v_cvt_pk_bf16_f32 v250,v252,v253\n"
"v_cvt_pk_bf16_f32 v251,v254,v255\n"
"buffer_store_dwordx4 v[224:227],v112,s[24:27],s70 offen offset:0 sc1\n"
"buffer_store_dwordx4 v[232:235],v112,s[24:27],s70 offen offset:1024 sc1\n"
"buffer_store_dwordx4 v[240:243],v112,s[24:27],s70 offen offset:2048 sc1\n"
"buffer_store_dwordx4 v[248:251],v112,s[24:27],s70 offen offset:3072 sc1\n"
"s_waitcnt lgkmcnt(4)\n"
"v_mfma_f32_16x16x32_bf16 v[80:83],a[224:227],v[192:195],0\n"
"v_mfma_f32_16x16x32_bf16 v[80:83],a[232:235],v[196:199],v[80:83]\n"
"ds_read_b128 v[224:227],v112 offset:8192\n"
"ds_read_b128 v[228:231],v112 offset:9216\n"
"v_mfma_f32_16x16x32_bf16 v[80:83],a[240:243],v[200:203],v[80:83]\n"
"ds_read_b128 v[232:235],v112 offset:10240\n"
"ds_read_b128 v[236:239],v112 offset:11264\n"
"v_mfma_f32_16x16x32_bf16 v[80:83],a[248:251],v[204:207],v[80:83]\n"
"ds_read_b128 v[240:243],v112 offset:12288\n"
"ds_read_b128 v[244:247],v112 offset:13312\n"
"v_mfma_f32_16x16x32_bf16 v[84:87],a[252:255],v[204:207],0\n"
"ds_read_b128 v[248:251],v112 offset:14336\n"
"ds_read_b128 v[252:255],v112 offset:15360\n"
"v_mfma_f32_16x16x32_bf16 v[84:87],a[244:247],v[200:203],v[84:87]\n"
"ds_read_b128 a[68:71],v116 offset:4096\n"
"ds_read_b128 a[72:75],v116 offset:8192\n"
"v_mfma_f32_16x16x32_bf16 v[84:87],a[236:239],v[196:199],v[84:87]\n"
"ds_read_b128 a[76:79],v116 offset:12288\n"
"ds_read_b128 a[80:83],v116 offset:0\n"
"v_mfma_f32_16x16x32_bf16 v[84:87],a[228:231],v[192:195],v[84:87]\n"
"ds_read_b128 a[84:87],v116 offset:20480\n"
"ds_read_b128 a[88:91],v116 offset:24576\n"
"s_waitcnt lgkmcnt(14)\n"
"v_mfma_f32_16x16x32_bf16 v[84:87],a[4:7],v[208:211],v[84:87]\n"
"ds_read_b128 a[92:95],v116 offset:28672\n"
"v_mfma_f32_16x16x32_bf16 v[84:87],a[12:15],v[212:215],v[84:87]\n"
"ds_read_b128 v[204:207],v112 offset:19456\n"
"s_lshl_b32 s91,s78,3\n"
"v_mfma_f32_16x16x32_bf16 v[84:87],a[20:23],v[216:219],v[84:87]\n"
"ds_read_b128 v[200:203],v112 offset:18432\n"
"s_add_u32 s91,s91,131072\n"
"v_mfma_f32_16x16x32_bf16 v[84:87],a[28:31],v[220:223],v[84:87]\n"
"ds_read_b128 v[196:199],v112 offset:17408\n"
"v_lshrrev_b32 v101,2,v112\n"
"v_mfma_f32_16x16x32_bf16 v[80:83],a[24:27],v[220:223],v[80:83]\n"
"ds_read_b128 v[192:195],v112 offset:16384\n"
"v_add_u32 v101,s91,v101\n"
"v_mfma_f32_16x16x32_bf16 v[80:83],a[16:19],v[216:219],v[80:83]\n"
"ds_read_b128 v[220:223],v112 offset:23552\n"
"v_lshrrev_b32 v114,2,v112\n"
"v_mfma_f32_16x16x32_bf16 v[80:83],a[8:11],v[212:215],v[80:83]\n"
"ds_read_b128 v[216:219],v112 offset:22528\n"
"v_add_u32 v114,131328,v114\n"
"v_mfma_f32_16x16x32_bf16 v[80:83],a[0:3],v[208:211],v[80:83]\n"
"ds_read_b128 v[212:215],v112 offset:21504\n"
"s_waitcnt lgkmcnt(15)\n"
"v_mfma_f32_16x16x32_bf16 v[80:83],v[32:35],v[224:227],v[80:83]\n"
"ds_read_b128 v[208:211],v112 offset:20480\n"
"s_mov_b32 s83,148946944\n"
"v_mfma_f32_16x16x32_bf16 v[80:83],v[40:43],v[228:231],v[80:83]\n"
"buffer_load_dwordx4 v[104:107],v112,s[24:27],s83 offen offset:0 sc1\n"
"v_mov_b32 v116,0\n"
"v_mfma_f32_16x16x32_bf16 v[80:83],a[32:35],v[232:235],v[80:83]\n"
"ds_write_b32 v101,v116 offset:256\n"
"v_mov_b32 v116,1\n"
"v_mfma_f32_16x16x32_bf16 v[80:83],a[40:43],v[236:239],v[80:83]\n"
"buffer_load_dwordx4 v[108:111],v112,s[24:27],s83 offen offset:1024 sc1\n"
"v_mfma_f32_16x16x32_bf16 v[84:87],a[44:47],v[236:239],v[84:87]\n"
"s_mov_b32 s16,4\n"
"ds_read_b128 v[236:239],v112 offset:27648\n"
"v_add_u32 v113,65536,v112\n"
"v_mfma_f32_16x16x32_bf16 v[84:87],a[36:39],v[232:235],v[84:87]\n"
"ds_read_b128 v[232:235],v112 offset:26624\n"
"s_add_u32 s31,s31,8388608\n"
"v_mfma_f32_16x16x32_bf16 v[84:87],v[44:47],v[228:231],v[84:87]\n"
"ds_read_b128 v[228:231],v112 offset:25600\n"
"s_add_u32 s53,s22,786432\n"
"v_mfma_f32_16x16x32_bf16 v[84:87],v[36:39],v[224:227],v[84:87]\n"
"s_add_u32 m0,s30,65536\n"
"ds_read_b128 v[224:227],v112 offset:24576\n"
"s_waitcnt lgkmcnt(13)\n"
"v_mfma_f32_16x16x32_bf16 v[84:87],a[68:71],v[240:243],v[84:87]\n"
"buffer_load_dwordx4 v112,s[24:27],s18 offen offset:0 sc1 lds\n"
"v_mfma_f32_16x16x32_bf16 v[84:87],a[76:79],v[244:247],v[84:87]\n"
"buffer_load_dwordx4 v112,s[24:27],s18 offen offset:1024 sc1 lds\n"
"v_mfma_f32_16x16x32_bf16 v[84:87],a[84:87],v[248:251],v[84:87]\n"
"s_add_u32 s56,s22,1048576\n"
"s_add_u32 s58,s22,1310720\n"
"v_mfma_f32_16x16x32_bf16 v[84:87],a[92:95],v[252:255],v[84:87]\n"
"buffer_load_dwordx4 v112,s[24:27],s18 offen offset:2048 sc1 lds\n"
"v_mfma_f32_16x16x32_bf16 v[80:83],a[88:91],v[252:255],v[80:83]\n"
"ds_read_b128 v[252:255],v112 offset:31744\n"
"s_add_u32 s59,s22,1572864\n"
"v_mfma_f32_16x16x32_bf16 v[80:83],a[80:83],v[248:251],v[80:83]\n"
"ds_read_b128 v[248:251],v112 offset:30720\n"
"s_bfe_u32 s10,s3,0x20015\n"
"v_mfma_f32_16x16x32_bf16 v[80:83],a[72:75],v[244:247],v[80:83]\n"
"ds_read_b128 v[244:247],v112 offset:29696\n"
"s_mul_i32 s10,s10,4186112\n"
"v_mfma_f32_16x16x32_bf16 v[80:83],a[156:159],v[240:243],v[80:83]\n"
"ds_read_b128 v[240:243],v112 offset:28672\n"
"s_waitcnt lgkmcnt(13)\n"
"v_mfma_f32_16x16x32_bf16 v[64:67],a[224:227],v[192:195],0\n"
"buffer_load_dwordx4 v112,s[24:27],s18 offen offset:3072 sc1 lds\n"
"s_add_u32 m0,m0,4096\n"
"v_mfma_f32_16x16x32_bf16 v[64:67],a[232:235],v[196:199],v[64:67]\n"
"buffer_load_dwordx4 v117,s[24:27],s18 offen offset:0 sc1 lds\n"
"v_mfma_f32_16x16x32_bf16 v[64:67],a[240:243],v[200:203],v[64:67]\n"
"s_add_u32 s12,s12,s10\n"
"s_bfe_u32 s10,s3,0x30012\n"
"v_mfma_f32_16x16x32_bf16 v[64:67],a[248:251],v[204:207],v[64:67]\n"
"buffer_load_dwordx4 v117,s[24:27],s18 offen offset:1024 sc1 lds\n"
"v_mfma_f32_16x16x32_bf16 v[68:71],a[252:255],v[204:207],0\n"
"ds_read_b128 v[204:207],v112 offset:35840\n"
"s_lshl_b32 s10,s10,12\n"
"v_mfma_f32_16x16x32_bf16 v[68:71],a[244:247],v[200:203],v[68:71]\n"
"ds_read_b128 v[200:203],v112 offset:34816\n"
"s_add_u32 s12,s12,s10\n"
"v_mfma_f32_16x16x32_bf16 v[68:71],a[236:239],v[196:199],v[68:71]\n"
"ds_read_b128 v[196:199],v112 offset:33792\n"
"s_bfe_u32 s10,s3,0x50012\n"
"v_mfma_f32_16x16x32_bf16 v[68:71],a[228:231],v[192:195],v[68:71]\n"
"ds_read_b128 v[192:195],v112 offset:32768\n"
"s_waitcnt lgkmcnt(13)\n"
"s_waitcnt vmcnt(8) lgkmcnt(12)\n"
"s_barrier\n"
"v_mfma_f32_16x16x32_bf16 v[68:71],a[4:7],v[208:211],v[68:71]\n"
"buffer_store_dwordx2 a[154:155],off,s[24:27],s71 sc1\n"
"buffer_load_dwordx4 v117,s[24:27],s18 offen offset:2048 sc1 lds\n"
"v_mfma_f32_16x16x32_bf16 v[68:71],a[12:15],v[212:215],v[68:71]\n"
"buffer_load_dwordx4 v117,s[24:27],s18 offen offset:3072 sc1 lds\n"
"v_mfma_f32_16x16x32_bf16 v[68:71],a[20:23],v[216:219],v[68:71]\n"
"s_sub_u32 s82,s3,212992\n"
"s_lshl_b32 s10,s10,8\n"
"v_mfma_f32_16x16x32_bf16 v[68:71],a[28:31],v[220:223],v[68:71]\n"
"buffer_load_dwordx4 a[48:51],v112,s[24:27],s82 offen offset:0 sc1\n"
"v_mfma_f32_16x16x32_bf16 v[64:67],a[24:27],v[220:223],v[64:67]\n"
"s_add_u32 s10,s10,149061632\n"
"ds_read_b128 v[220:223],v112 offset:39936\n"
"s_mov_b32 s14,0\n"
"v_mfma_f32_16x16x32_bf16 v[64:67],a[16:19],v[216:219],v[64:67]\n"
"ds_read_b128 v[216:219],v112 offset:38912\n"
"v_not_b32 v122,s80\n"
"v_mfma_f32_16x16x32_bf16 v[64:67],a[8:11],v[212:215],v[64:67]\n"
"s_cmp_eq_u32 s52,0\n"
"ds_read_b128 v[212:215],v112 offset:37888\n"
"s_cselect_b64 s[14:15],s[14:15],0\n"
"v_mfma_f32_16x16x32_bf16 v[64:67],a[0:3],v[208:211],v[64:67]\n"
"s_cselect_b32 s10,s10,2146435072\n"
"ds_read_b128 v[208:211],v112 offset:36864\n"
"s_waitcnt lgkmcnt(12)\n"
"v_mfma_f32_16x16x32_bf16 v[64:67],v[32:35],v[224:227],v[64:67]\n"
"buffer_load_dwordx4 a[52:55],v112,s[24:27],s82 offen offset:1024 sc1\n"
"s_cmp_lg_u32 s15,0\n"
"s_cselect_b32 s91,s11,2146435072\n"
"v_mfma_f32_16x16x32_bf16 v[64:67],v[40:43],v[228:231],v[64:67]\n"
"buffer_load_dwordx4 a[56:59],v112,s[24:27],s82 offen offset:2048 sc1\n"
"buffer_load_dwordx2 v[122:123],v115,s[24:27],s91 offen sc1\n"
"v_mfma_f32_16x16x32_bf16 v[64:67],a[32:35],v[232:235],v[64:67]\n"
"s_mov_b32 s89,0\n"
"s_mov_b32 s20,32768\n"
"v_mfma_f32_16x16x32_bf16 v[64:67],a[40:43],v[236:239],v[64:67]\n"
"buffer_load_dwordx4 a[60:63],v112,s[24:27],s82 offen offset:3072 sc1\n"
"v_mfma_f32_16x16x32_bf16 v[68:71],a[44:47],v[236:239],v[68:71]\n"
"s_mov_b32 s19,0xffff\n"
"ds_read_b128 v[236:239],v112 offset:44032\n"
"s_mov_b32 s21,32\n"
"v_mfma_f32_16x16x32_bf16 v[68:71],a[36:39],v[232:235],v[68:71]\n"
"ds_read_b128 v[232:235],v112 offset:43008\n"
"v_mfma_f32_16x16x32_bf16 v[68:71],v[44:47],v[228:231],v[68:71]\n"
"ds_read_b128 v[228:231],v112 offset:41984\n"
"v_mfma_f32_16x16x32_bf16 v[68:71],v[36:39],v[224:227],v[68:71]\n"
"s_sub_u32 s82,s3,229376\n"
"ds_read_b128 v[224:227],v112 offset:40960\n"
"s_waitcnt lgkmcnt(12)\n"
"v_mfma_f32_16x16x32_bf16 v[68:71],a[68:71],v[240:243],v[68:71]\n"
"buffer_load_dwordx4 v[48:51],v112,s[24:27],s82 offen offset:0 sc1\n"
"v_mfma_f32_16x16x32_bf16 v[68:71],a[76:79],v[244:247],v[68:71]\n"
"buffer_load_dwordx4 v[52:55],v112,s[24:27],s82 offen offset:1024 sc1\n"
"v_mfma_f32_16x16x32_bf16 v[68:71],a[84:87],v[248:251],v[68:71]\n"
"buffer_load_dwordx4 v[56:59],v112,s[24:27],s82 offen offset:2048 sc1\n"
"v_mfma_f32_16x16x32_bf16 v[68:71],a[92:95],v[252:255],v[68:71]\n"
"s_waitcnt vmcnt(17)\n"
"v_mfma_f32_16x16x32_bf16 v[64:67],a[88:91],v[252:255],v[64:67]\n"
"ds_read_b128 v[252:255],v112 offset:48128\n"
"v_xor_b32 v119,s80,v104\n"
"v_mfma_f32_16x16x32_bf16 v[64:67],a[80:83],v[248:251],v[64:67]\n"
"ds_read_b128 v[248:251],v112 offset:47104\n"
"v_xor_b32 v120,s81,v105\n"
"v_mfma_f32_16x16x32_bf16 v[64:67],a[72:75],v[244:247],v[64:67]\n"
"ds_read_b128 v[244:247],v112 offset:46080\n"
"v_or_b32 v119,v119,v120\n"
"v_mfma_f32_16x16x32_bf16 v[64:67],a[156:159],v[240:243],v[64:67]\n"
"ds_read_b128 v[240:243],v112 offset:45056\n"
"s_waitcnt lgkmcnt(12)\n"
"v_mfma_f32_16x16x32_bf16 v[72:75],a[224:227],v[192:195],0\n"
"buffer_load_dwordx4 v[60:63],v112,s[24:27],s82 offen offset:3072 sc1\n"
"s_sub_u32 s82,s3,245760\n"
"v_mfma_f32_16x16x32_bf16 v[72:75],a[232:235],v[196:199],v[72:75]\n"
"buffer_load_dwordx4 v[16:19],v112,s[24:27],s82 offen offset:0 sc1\n"
"v_xor_b32 v120,s80,v106\n"
"v_mfma_f32_16x16x32_bf16 v[72:75],a[240:243],v[200:203],v[72:75]\n"
"v_or_b32 v119,v119,v120\n"
"v_xor_b32 v120,s81,v107\n"
"v_mfma_f32_16x16x32_bf16 v[72:75],a[248:251],v[204:207],v[72:75]\n"
"buffer_load_dwordx4 v[20:23],v112,s[24:27],s82 offen offset:1024 sc1\n"
"v_or_b32 v119,v119,v120\n"
"v_mfma_f32_16x16x32_bf16 v[76:79],a[252:255],v[204:207],0\n"
"ds_read_b128 v[204:207],v112 offset:52224\n"
"v_xor_b32 v120,s80,v108\n"
"v_mfma_f32_16x16x32_bf16 v[76:79],a[244:247],v[200:203],v[76:79]\n"
"ds_read_b128 v[200:203],v112 offset:51200\n"
"v_or_b32 v119,v119,v120\n"
"v_mfma_f32_16x16x32_bf16 v[76:79],a[236:239],v[196:199],v[76:79]\n"
"ds_read_b128 v[196:199],v112 offset:50176\n"
"v_xor_b32 v120,s81,v109\n"
"v_mfma_f32_16x16x32_bf16 v[76:79],a[228:231],v[192:195],v[76:79]\n"
"ds_read_b128 v[192:195],v112 offset:49152\n"
"s_waitcnt lgkmcnt(12)\n"
"v_mfma_f32_16x16x32_bf16 v[76:79],a[4:7],v[208:211],v[76:79]\n"
"buffer_load_dwordx4 v[24:27],v112,s[24:27],s82 offen offset:2048 sc1\n"
"v_or_b32 v119,v119,v120\n"
"v_mfma_f32_16x16x32_bf16 v[76:79],a[12:15],v[212:215],v[76:79]\n"
"buffer_load_dwordx4 v[28:31],v112,s[24:27],s82 offen offset:3072 sc1\n"
"v_mfma_f32_16x16x32_bf16 v[76:79],a[20:23],v[216:219],v[76:79]\n"
"s_add_u32 s18,s18,2097152\n"
"v_xor_b32 v120,s80,v110\n"
"s_add_u32 m0,s30,98304\n"
"v_mfma_f32_16x16x32_bf16 v[76:79],a[28:31],v[220:223],v[76:79]\n"
"buffer_load_dwordx4 v112,s[24:27],s18 offen offset:0 sc1 lds\n"
"v_mfma_f32_16x16x32_bf16 v[72:75],a[24:27],v[220:223],v[72:75]\n"
"ds_read_b128 v[220:223],v112 offset:56320\n"
"v_or_b32 v119,v119,v120\n"
"v_mfma_f32_16x16x32_bf16 v[72:75],a[16:19],v[216:219],v[72:75]\n"
"ds_read_b128 v[216:219],v112 offset:55296\n"
"v_xor_b32 v120,s81,v111\n"
"v_mfma_f32_16x16x32_bf16 v[72:75],a[8:11],v[212:215],v[72:75]\n"
"ds_read_b128 v[212:215],v112 offset:54272\n"
"v_or_b32 v119,v119,v120\n"
"v_mfma_f32_16x16x32_bf16 v[72:75],a[0:3],v[208:211],v[72:75]\n"
"ds_read_b128 v[208:211],v112 offset:53248\n"
"s_waitcnt lgkmcnt(12)\n"
"v_mfma_f32_16x16x32_bf16 v[72:75],v[32:35],v[224:227],v[72:75]\n"
"buffer_load_dwordx4 v112,s[24:27],s18 offen offset:1024 sc1 lds\n"
"v_mfma_f32_16x16x32_bf16 v[72:75],v[40:43],v[228:231],v[72:75]\n"
"buffer_load_dwordx4 v112,s[24:27],s18 offen offset:2048 sc1 lds\n"
"v_mfma_f32_16x16x32_bf16 v[72:75],a[32:35],v[232:235],v[72:75]\n"
"v_cmp_ne_u32 vcc,0,v119\n"
"v_mfma_f32_16x16x32_bf16 v[72:75],a[40:43],v[236:239],v[72:75]\n"
"buffer_load_dwordx4 v112,s[24:27],s18 offen offset:3072 sc1 lds\n"
"v_mfma_f32_16x16x32_bf16 v[76:79],a[44:47],v[236:239],v[76:79]\n"
"ds_read_b128 v[236:239],v112 offset:60416\n"
"v_mfma_f32_16x16x32_bf16 v[76:79],a[36:39],v[232:235],v[76:79]\n"
"ds_read_b128 v[232:235],v112 offset:59392\n"
"v_xor_b32 v119,s80,v122\n"
"v_mfma_f32_16x16x32_bf16 v[76:79],v[44:47],v[228:231],v[76:79]\n"
"ds_read_b128 v[228:231],v112 offset:58368\n"
"v_xor_b32 v120,s81,v123\n"
"v_mfma_f32_16x16x32_bf16 v[76:79],v[36:39],v[224:227],v[76:79]\n"
"s_add_u32 m0,m0,4096\n"
"ds_read_b128 v[224:227],v112 offset:57344\n"
"s_waitcnt lgkmcnt(12)\n"
"v_mfma_f32_16x16x32_bf16 v[76:79],a[68:71],v[240:243],v[76:79]\n"
"buffer_load_dwordx4 v117,s[24:27],s18 offen offset:0 sc1 lds\n"
"v_or_b32 v119,v119,v120\n"
"v_mfma_f32_16x16x32_bf16 v[76:79],a[76:79],v[244:247],v[76:79]\n"
"buffer_load_dwordx4 v117,s[24:27],s18 offen offset:1024 sc1 lds\n"
"v_cmp_ne_u32 s[28:29],0,v119\n"
"v_mfma_f32_16x16x32_bf16 v[76:79],a[84:87],v[248:251],v[76:79]\n"
"s_and_b64 s[14:15],s[14:15],s[28:29]\n"
"s_cselect_b32 s91,s11,2146435072\n"
"buffer_load_dwordx2 v[122:123],v115,s[24:27],s91 offen sc1\n"
"v_mfma_f32_16x16x32_bf16 v[76:79],a[92:95],v[252:255],v[76:79]\n"
"buffer_load_dwordx4 v117,s[24:27],s18 offen offset:2048 sc1 lds\n"
"v_mfma_f32_16x16x32_bf16 v[72:75],a[88:91],v[252:255],v[72:75]\n"
"ds_read_b128 v[252:255],v112 offset:64512\n"
"v_mfma_f32_16x16x32_bf16 v[72:75],a[80:83],v[248:251],v[72:75]\n"
"ds_read_b128 v[248:251],v112 offset:63488\n"
"v_mfma_f32_16x16x32_bf16 v[72:75],a[72:75],v[244:247],v[72:75]\n"
"ds_read_b128 v[244:247],v112 offset:62464\n"
"v_mfma_f32_16x16x32_bf16 v[72:75],a[156:159],v[240:243],v[72:75]\n"
"s_and_b32 s83,s15,0xffff\n"
"ds_read_b128 v[240:243],v112 offset:61440\n"
"s_waitcnt lgkmcnt(12)\n"
"v_mfma_f32_16x16x32_bf16 v[88:91],a[224:227],v[192:195],0\n"
"buffer_load_dwordx4 v117,s[24:27],s18 offen offset:3072 sc1 lds\n"
"v_mfma_f32_16x16x32_bf16 v[88:91],a[232:235],v[196:199],v[88:91]\n"
"s_cbranch_scc1 .Lfk_es_lkone\n"
".Lfk_er_lkone:\n"
"v_mfma_f32_16x16x32_bf16 v[88:91],a[240:243],v[200:203],v[88:91]\n"
"s_waitcnt vmcnt(22)\n"
"ds_add_u32 v114,v116\n"
"v_mfma_f32_16x16x32_bf16 v[88:91],a[248:251],v[204:207],v[88:91]\n"
"v_and_b32 v118,0xf0,v112\n"
"v_lshrrev_b32 v120,8,v112\n"
"v_lshlrev_b32 v120,4,v120\n"
"v_lshl_add_u32 v118,v118,2,v120\n"
"v_mfma_f32_16x16x32_bf16 v[92:95],a[252:255],v[204:207],0\n"
"s_lshr_b32 s82,s78,20\n"
"v_xor_b32 v118,s82,v118\n"
"v_mov_b32 v100,1\n"
"v_mfma_f32_16x16x32_bf16 v[92:95],a[244:247],v[200:203],v[92:95]\n"
"s_mov_b32 s77,0\n"
"v_mov_b32 v102,131076\n"
"s_and_b32 s91,s52,1\n"
"v_mfma_f32_16x16x32_bf16 v[92:95],a[236:239],v[196:199],v[92:95]\n"
"s_lshl_b32 s91,s91,7\n"
"v_xor_b32 v96,s91,v112\n"
"v_xor_b32 v97,32,v96\n"
"v_mfma_f32_16x16x32_bf16 v[92:95],a[228:231],v[192:195],v[92:95]\n"
"v_xor_b32 v99,96,v96\n"
"s_waitcnt lgkmcnt(9)\n"
"v_mfma_f32_16x16x32_bf16 v[92:95],a[4:7],v[208:211],v[92:95]\n"
"v_xor_b32 v98,64,v96\n"
"v_add_u32 v118,s12,v118\n"
"v_mfma_f32_16x16x32_bf16 v[92:95],a[12:15],v[212:215],v[92:95]\n"
"ds_read_b32 v125,v114\n"
"v_mfma_f32_16x16x32_bf16 v[92:95],a[20:23],v[216:219],v[92:95]\n"
"v_mfma_f32_16x16x32_bf16 v[92:95],a[28:31],v[220:223],v[92:95]\n"
"v_mfma_f32_16x16x32_bf16 v[88:91],a[24:27],v[220:223],v[88:91]\n"
"v_mfma_f32_16x16x32_bf16 v[88:91],a[16:19],v[216:219],v[88:91]\n"
"s_waitcnt lgkmcnt(0)\n"
"v_cmp_gt_u32 s[28:29],s16,v125\n"
"v_mfma_f32_16x16x32_bf16 v[88:91],a[8:11],v[212:215],v[88:91]\n"
"s_add_u32 s18,s8,4194304\n"
"s_add_u32 s8,s8,5242880\n"
"s_or_b64 vcc,vcc,s[28:29]\n"
"v_mfma_f32_16x16x32_bf16 v[88:91],a[0:3],v[208:211],v[88:91]\n"
"s_cbranch_vccnz .Lfk_z0\n"
".Lfk_z1:\n"
"ds_read_b128 v[192:195],v113 offset:0\n"
"v_mfma_f32_16x16x32_bf16 v[88:91],v[32:35],v[224:227],v[88:91]\n"
"s_mov_b32 s89,1\n"
"s_mov_b32 s19,0xff\n"
"s_lshl_b64 exec,1,s89\n"
"buffer_atomic_add v100,off,s[24:27],s10 sc0\n"
"s_mov_b64 exec,-1\n"
"v_mfma_f32_16x16x32_bf16 v[88:91],v[40:43],v[228:231],v[88:91]\n"
"ds_read_b128 v[196:199],v113 offset:1024\n"
"s_add_u32 m0,s30,0\n"
"v_mfma_f32_16x16x32_bf16 v[88:91],a[32:35],v[232:235],v[88:91]\n"
"ds_read_b128 v[200:203],v113 offset:2048\n"
"buffer_load_dwordx4 v112,s[24:27],s18 offen offset:0 sc1 lds\n"
"v_mfma_f32_16x16x32_bf16 v[88:91],a[40:43],v[236:239],v[88:91]\n"
"s_add_u32 s89,s89,1\n"
"ds_read_b128 v[204:207],v113 offset:3072\n"
"s_add_u32 s21,s21,16\n"
"v_mfma_f32_16x16x32_bf16 v[92:95],a[44:47],v[236:239],v[92:95]\n"
"s_mov_b32 s38,4194304\n"
"ds_read_b128 v[208:211],v113 offset:16384\n"
"s_mov_b32 s39,0x20000\n"
"v_mfma_f32_16x16x32_bf16 v[92:95],a[36:39],v[232:235],v[92:95]\n"
"s_add_u32 s16,s16,4\n"
"ds_read_b128 v[212:215],v113 offset:17408\n"
"s_mov_b32 s46,2048\n"
"v_mfma_f32_16x16x32_bf16 v[92:95],v[44:47],v[228:231],v[92:95]\n"
"ds_read_b128 v[216:219],v113 offset:18432\n"
"buffer_load_dwordx4 v112,s[24:27],s18 offen offset:1024 sc1 lds\n"
"v_mfma_f32_16x16x32_bf16 v[92:95],v[36:39],v[224:227],v[92:95]\n"
"s_mov_b32 s47,0x20000\n"
"ds_read_b128 v[220:223],v113 offset:19456\n"
"s_mov_b32 s50,4194304\n"
"v_mfma_f32_16x16x32_bf16 v[92:95],a[68:71],v[240:243],v[92:95]\n"
"s_mov_b32 s51,0x20000\n"
"ds_read_b128 v[236:239],v113 offset:7168\n"
"s_and_b32 s60,s100,7\n"
"v_mfma_f32_16x16x32_bf16 v[92:95],a[76:79],v[244:247],v[92:95]\n"
"s_lshl_b32 s60,s60,5\n"
"ds_read_b128 v[232:235],v113 offset:6144\n"
"s_lshr_b32 s73,s100,3\n"
"v_mfma_f32_16x16x32_bf16 v[92:95],a[84:87],v[248:251],v[92:95]\n"
"ds_read_b128 v[228:231],v113 offset:5120\n"
"buffer_load_dwordx4 v112,s[24:27],s18 offen offset:2048 sc1 lds\n"
"v_mfma_f32_16x16x32_bf16 v[92:95],a[92:95],v[252:255],v[92:95]\n"
"s_add_u32 s73,s73,s60\n"
"ds_read_b128 v[224:227],v113 offset:4096\n"
"s_and_b32 s60,s100,7\n"
"v_mfma_f32_16x16x32_bf16 v[88:91],a[88:91],v[252:255],v[88:91]\n"
"s_bfe_u32 s61,s100,0x10003\n"
"ds_read_b128 v[252:255],v113 offset:23552\n"
"s_lshl1_add_u32 s60,s60,s61\n"
"v_mfma_f32_16x16x32_bf16 v[88:91],a[80:83],v[248:251],v[88:91]\n"
"s_and_b32 s61,s100,0xf0\n"
"ds_read_b128 v[248:251],v113 offset:22528\n"
"s_add_u32 s60,s60,s61\n"
"v_mfma_f32_16x16x32_bf16 v[88:91],a[72:75],v[244:247],v[88:91]\n"
"s_lshl_b32 s61,s52,8\n"
"ds_read_b128 v[244:247],v113 offset:21504\n"
"s_add_u32 s60,s60,s61\n"
"v_mfma_f32_16x16x32_bf16 v[88:91],a[156:159],v[240:243],v[88:91]\n"
"ds_read_b128 v[240:243],v113 offset:20480\n"
"s_waitcnt lgkmcnt(12)\n"
"v_mfma_f32_16x16x32_bf16 v[80:83],v[128:131],v[192:195],v[80:83]\n"
"buffer_load_dwordx4 v112,s[24:27],s18 offen offset:3072 sc1 lds\n"
"v_mfma_f32_16x16x32_bf16 v[80:83],v[136:139],v[196:199],v[80:83]\n"
"s_lshl_b32 s61,s60,11\n"
"s_add_u32 s68,s61,2097152\n"
"v_mfma_f32_16x16x32_bf16 v[80:83],v[144:147],v[200:203],v[80:83]\n"
"s_add_u32 m0,m0,4096\n"
"v_mfma_f32_16x16x32_bf16 v[80:83],v[152:155],v[204:207],v[80:83]\n"
"buffer_load_dwordx4 v117,s[24:27],s18 offen offset:0 sc1 lds\n"
"v_mfma_f32_16x16x32_bf16 v[84:87],v[156:159],v[204:207],v[84:87]\n"
"ds_read_b128 v[204:207],v113 offset:11264\n"
"s_add_u32 s20,s20,32768\n"
"v_mfma_f32_16x16x32_bf16 v[84:87],v[148:151],v[200:203],v[84:87]\n"
"ds_read_b128 v[200:203],v113 offset:10240\n"
"s_and_b32 s20,s20,0x1ffff\n"
"v_mfma_f32_16x16x32_bf16 v[84:87],v[140:143],v[196:199],v[84:87]\n"
"ds_read_b128 v[196:199],v113 offset:9216\n"
"v_mfma_f32_16x16x32_bf16 v[84:87],v[132:135],v[192:195],v[84:87]\n"
"ds_read_b128 v[192:195],v113 offset:8192\n"
"s_waitcnt lgkmcnt(12)\n"
"v_mfma_f32_16x16x32_bf16 v[68:71],v[132:135],v[208:211],v[68:71]\n"
"buffer_load_dwordx4 v117,s[24:27],s18 offen offset:1024 sc1 lds\n"
"v_mfma_f32_16x16x32_bf16 v[68:71],v[140:143],v[212:215],v[68:71]\n"
"v_mfma_f32_16x16x32_bf16 v[68:71],v[148:151],v[216:219],v[68:71]\n"
"v_mfma_f32_16x16x32_bf16 v[68:71],v[156:159],v[220:223],v[68:71]\n"
"buffer_load_dwordx4 v117,s[24:27],s18 offen offset:2048 sc1 lds\n"
"v_mfma_f32_16x16x32_bf16 v[64:67],v[152:155],v[220:223],v[64:67]\n"
"ds_read_b128 v[220:223],v113 offset:27648\n"
"v_xor_b32 v119,s80,v122\n"
"v_mfma_f32_16x16x32_bf16 v[64:67],v[144:147],v[216:219],v[64:67]\n"
"ds_read_b128 v[216:219],v113 offset:26624\n"
"v_xor_b32 v120,s81,v123\n"
"v_mfma_f32_16x16x32_bf16 v[64:67],v[136:139],v[212:215],v[64:67]\n"
"ds_read_b128 v[212:215],v113 offset:25600\n"
"v_or_b32 v119,v119,v120\n"
"v_mfma_f32_16x16x32_bf16 v[64:67],v[128:131],v[208:211],v[64:67]\n"
"ds_read_b128 v[208:211],v113 offset:24576\n"
"s_waitcnt lgkmcnt(12)\n"
"v_mfma_f32_16x16x32_bf16 v[80:83],v[0:3],v[224:227],v[80:83]\n"
"buffer_load_dwordx4 v117,s[24:27],s18 offen offset:3072 sc1 lds\n"
"v_cmp_ne_u32 s[28:29],0,v119\n"
"v_mfma_f32_16x16x32_bf16 v[80:83],v[8:11],v[228:231],v[80:83]\n"
"s_and_b64 s[14:15],s[14:15],s[28:29]\n"
"s_cselect_b32 s91,s11,2146435072\n"
"s_add_u32 m0,s30,32768\n"
"v_mfma_f32_16x16x32_bf16 v[80:83],v[160:163],v[232:235],v[80:83]\n"
"buffer_load_dwordx2 v[122:123],v115,s[24:27],s91 offen sc1\n"
"v_mfma_f32_16x16x32_bf16 v[80:83],v[168:171],v[236:239],v[80:83]\n"
"buffer_load_dwordx4 v112,s[24:27],s8 offen offset:0 sc1 lds\n"
"v_mfma_f32_16x16x32_bf16 v[84:87],v[172:175],v[236:239],v[84:87]\n"
"ds_read_b128 v[236:239],v113 offset:15360\n"
"v_mfma_f32_16x16x32_bf16 v[84:87],v[164:167],v[232:235],v[84:87]\n"
"ds_read_b128 v[232:235],v113 offset:14336\n"
"v_mfma_f32_16x16x32_bf16 v[84:87],v[12:15],v[228:231],v[84:87]\n"
"ds_read_b128 v[228:231],v113 offset:13312\n"
"v_mfma_f32_16x16x32_bf16 v[84:87],v[4:7],v[224:227],v[84:87]\n"
"s_and_b32 s83,s15,0xff0000\n"
"ds_read_b128 v[224:227],v113 offset:12288\n"
"s_waitcnt lgkmcnt(12)\n"
"v_mfma_f32_16x16x32_bf16 v[68:71],v[4:7],v[240:243],v[68:71]\n"
"buffer_load_dwordx4 v112,s[24:27],s8 offen offset:1024 sc1 lds\n"
"v_mfma_f32_16x16x32_bf16 v[68:71],v[12:15],v[244:247],v[68:71]\n"
"s_cbranch_scc1 .Lfk_es_lkonem\n"
".Lfk_er_lkonem:\n"
"v_mfma_f32_16x16x32_bf16 v[68:71],v[164:167],v[248:251],v[68:71]\n"
"v_mfma_f32_16x16x32_bf16 v[68:71],v[172:175],v[252:255],v[68:71]\n"
"buffer_load_dwordx4 v112,s[24:27],s8 offen offset:2048 sc1 lds\n"
"v_mfma_f32_16x16x32_bf16 v[64:67],v[168:171],v[252:255],v[64:67]\n"
"ds_read_b128 v[252:255],v113 offset:31744\n"
"v_mfma_f32_16x16x32_bf16 v[64:67],v[160:163],v[248:251],v[64:67]\n"
"ds_read_b128 v[248:251],v113 offset:30720\n"
"v_mfma_f32_16x16x32_bf16 v[64:67],v[8:11],v[244:247],v[64:67]\n"
"ds_read_b128 v[244:247],v113 offset:29696\n"
"v_mfma_f32_16x16x32_bf16 v[64:67],v[0:3],v[240:243],v[64:67]\n"
"ds_read_b128 v[240:243],v113 offset:28672\n"
"s_waitcnt lgkmcnt(12)\n"
"v_mfma_f32_16x16x32_bf16 v[80:83],v[176:179],v[192:195],v[80:83]\n"
"s_waitcnt vmcnt(13)\n"
"ds_add_u32 v114,v116\n"
"v_mfma_f32_16x16x32_bf16 v[80:83],v[184:187],v[196:199],v[80:83]\n"
"buffer_load_dwordx4 v112,s[24:27],s8 offen offset:3072 sc1 lds\n"
"v_mfma_f32_16x16x32_bf16 v[80:83],a[48:51],v[200:203],v[80:83]\n"
"s_add_u32 m0,m0,4096\n"
"v_mfma_f32_16x16x32_bf16 v[80:83],a[56:59],v[204:207],v[80:83]\n"
"s_add_u32 s83,s20,32768\n"
"s_and_b32 s83,s83,0x1ffff\n"
"v_add_u32 v113,s83,v112\n"
"v_mfma_f32_16x16x32_bf16 v[84:87],a[60:63],v[204:207],v[84:87]\n"
"buffer_load_dwordx4 v117,s[24:27],s8 offen offset:0 sc1 lds\n"
"v_mfma_f32_16x16x32_bf16 v[84:87],a[52:55],v[200:203],v[84:87]\n"
"s_cmp_eq_u32 s52,0\n"
"v_not_b32 v104,s80\n"
"s_cselect_b32 s9,s9,2146435072\n"
"v_mfma_f32_16x16x32_bf16 v[84:87],v[188:191],v[196:199],v[84:87]\n"
"buffer_load_dwordx2 v[104:105],v115,s[24:27],s9 offen sc1\n"
"v_mfma_f32_16x16x32_bf16 v[84:87],v[180:183],v[192:195],v[84:87]\n"
"buffer_load_dwordx4 v117,s[24:27],s8 offen offset:1024 sc1 lds\n"
"s_waitcnt lgkmcnt(9)\n"
"v_mfma_f32_16x16x32_bf16 v[68:71],v[180:183],v[208:211],v[68:71]\n"
"v_mfma_f32_16x16x32_bf16 v[68:71],v[188:191],v[212:215],v[68:71]\n"
"ds_read_b32 v125,v114\n"
"v_mfma_f32_16x16x32_bf16 v[68:71],a[52:55],v[216:219],v[68:71]\n"
"buffer_load_dwordx4 v117,s[24:27],s8 offen offset:2048 sc1 lds\n"
"v_mfma_f32_16x16x32_bf16 v[68:71],a[60:63],v[220:223],v[68:71]\n"
"v_mfma_f32_16x16x32_bf16 v[64:67],a[56:59],v[220:223],v[64:67]\n"
"v_mfma_f32_16x16x32_bf16 v[64:67],a[48:51],v[216:219],v[64:67]\n"
"s_waitcnt lgkmcnt(0)\n"
"v_cmp_gt_u32 vcc,s16,v125\n"
"v_mfma_f32_16x16x32_bf16 v[64:67],v[184:187],v[212:215],v[64:67]\n"
"buffer_load_dwordx4 v117,s[24:27],s8 offen offset:3072 sc1 lds\n"
"v_mfma_f32_16x16x32_bf16 v[64:67],v[176:179],v[208:211],v[64:67]\n"
"s_lshl_b64 exec,1,s89\n"
"buffer_atomic_add v100,off,s[24:27],s10 sc0\n"
"s_mov_b64 exec,-1\n"
"s_add_u32 s8,s8,1048576\n"
"s_cbranch_vccnz .Lfk_es_mone\n"
".Lfk_er_mone:\n"
"v_mfma_f32_16x16x32_bf16 v[80:83],v[48:51],v[224:227],v[80:83]\n"
"ds_read_b128 v[192:195],v113 offset:0\n"
"v_mfma_f32_16x16x32_bf16 v[80:83],v[56:59],v[228:231],v[80:83]\n"
"ds_read_b128 v[196:199],v113 offset:1024\n"
"ds_read_b128 v[200:203],v113 offset:2048\n"
"v_mfma_f32_16x16x32_bf16 v[80:83],v[16:19],v[232:235],v[80:83]\n"
"buffer_load_dwordx4 a[96:99],v112,s[36:39],s61 offen offset:0\n"
"v_mfma_f32_16x16x32_bf16 v[80:83],v[24:27],v[236:239],v[80:83]\n"
"s_add_u32 s89,s89,1\n"
"ds_read_b128 v[204:207],v113 offset:3072\n"
"s_add_u32 s21,s21,8\n"
"v_mfma_f32_16x16x32_bf16 v[84:87],v[28:31],v[236:239],v[84:87]\n"
"ds_read_b128 v[208:211],v113 offset:4096\n"
"ds_read_b128 v[212:215],v113 offset:5120\n"
"v_mfma_f32_16x16x32_bf16 v[84:87],v[20:23],v[232:235],v[84:87]\n"
"buffer_load_dwordx4 a[100:103],v112,s[36:39],s61 offen offset:1024\n"
"v_mfma_f32_16x16x32_bf16 v[84:87],v[60:63],v[228:231],v[84:87]\n"
"s_add_u32 s16,s16,4\n"
"ds_read_b128 v[216:219],v113 offset:6144\n"
"v_mfma_f32_16x16x32_bf16 v[84:87],v[52:55],v[224:227],v[84:87]\n"
"ds_read_b128 v[220:223],v113 offset:7168\n"
"ds_read_b128 v[236:239],v113 offset:11264\n"
"v_mfma_f32_16x16x32_bf16 v[68:71],v[52:55],v[240:243],v[68:71]\n"
"buffer_load_dwordx4 a[104:107],v112,s[36:39],s68 offen offset:0\n"
"v_mfma_f32_16x16x32_bf16 v[68:71],v[60:63],v[244:247],v[68:71]\n"
"ds_read_b128 v[232:235],v113 offset:10240\n"
"v_cvt_pk_bf16_f32 v80,v80,v81\n"
"v_mfma_f32_16x16x32_bf16 v[68:71],v[20:23],v[248:251],v[68:71]\n"
"ds_read_b128 v[228:231],v113 offset:9216\n"
"ds_read_b128 v[224:227],v113 offset:8192\n"
"v_cvt_pk_bf16_f32 v81,v82,v83\n"
"v_mfma_f32_16x16x32_bf16 v[68:71],v[28:31],v[252:255],v[68:71]\n"
"buffer_load_dwordx4 a[108:111],v112,s[36:39],s68 offen offset:1024\n"
"v_mfma_f32_16x16x32_bf16 v[64:67],v[24:27],v[252:255],v[64:67]\n"
"ds_read_b128 v[252:255],v113 offset:15360\n"
"v_cvt_pk_bf16_f32 v82,v84,v85\n"
"v_cvt_pk_bf16_f32 v83,v86,v87\n"
"v_mfma_f32_16x16x32_bf16 v[64:67],v[16:19],v[248:251],v[64:67]\n"
"ds_read_b128 v[248:251],v113 offset:14336\n"
"v_max_u32 v80,1,v80\n"
"v_max_u32 v81,1,v81\n"
"v_mfma_f32_16x16x32_bf16 v[64:67],v[56:59],v[244:247],v[64:67]\n"
"ds_read_b128 v[244:247],v113 offset:13312\n"
"v_max_u32 v82,1,v82\n"
"v_max_u32 v83,1,v83\n"
"v_mfma_f32_16x16x32_bf16 v[64:67],v[48:51],v[240:243],v[64:67]\n"
"ds_read_b128 v[240:243],v113 offset:12288\n"
"buffer_store_dwordx4 v[80:83],v118,s[24:27],s22 offen sc1\n"
"s_waitcnt lgkmcnt(12)\n"
"v_mfma_f32_16x16x32_bf16 v[72:75],v[128:131],v[192:195],v[72:75]\n"
"s_add_u32 m0,s30,s20\n"
"s_add_u32 s22,s22,262144\n"
"v_mfma_f32_16x16x32_bf16 v[72:75],v[136:139],v[196:199],v[72:75]\n"
"buffer_load_dwordx4 v112,s[24:27],s8 offen offset:0 sc1 lds\n"
"v_mfma_f32_16x16x32_bf16 v[72:75],v[144:147],v[200:203],v[72:75]\n"
"v_mfma_f32_16x16x32_bf16 v[72:75],v[152:155],v[204:207],v[72:75]\n"
"v_cvt_pk_bf16_f32 v64,v64,v65\n"
"v_mfma_f32_16x16x32_bf16 v[76:79],v[156:159],v[204:207],v[76:79]\n"
"ds_read_b128 v[204:207],v113 offset:19456\n"
"s_add_u32 s20,s20,32768\n"
"v_mfma_f32_16x16x32_bf16 v[76:79],v[148:151],v[200:203],v[76:79]\n"
"ds_read_b128 v[200:203],v113 offset:18432\n"
"s_and_b32 s20,s20,0x1ffff\n"
"v_mfma_f32_16x16x32_bf16 v[76:79],v[140:143],v[196:199],v[76:79]\n"
"ds_read_b128 v[196:199],v113 offset:17408\n"
"v_cvt_pk_bf16_f32 v65,v66,v67\n"
"v_mfma_f32_16x16x32_bf16 v[76:79],v[132:135],v[192:195],v[76:79]\n"
"ds_read_b128 v[192:195],v113 offset:16384\n"
"v_cvt_pk_bf16_f32 v66,v68,v69\n"
"s_waitcnt lgkmcnt(12)\n"
"v_mfma_f32_16x16x32_bf16 v[76:79],v[4:7],v[208:211],v[76:79]\n"
"buffer_load_dwordx4 v112,s[24:27],s8 offen offset:1024 sc1 lds\n"
"v_cvt_pk_bf16_f32 v67,v70,v71\n"
"v_mfma_f32_16x16x32_bf16 v[76:79],v[12:15],v[212:215],v[76:79]\n"
"v_max_u32 v64,1,v64\n"
"v_max_u32 v65,1,v65\n"
"v_max_u32 v66,1,v66\n"
"v_max_u32 v67,1,v67\n"
"v_mfma_f32_16x16x32_bf16 v[76:79],v[164:167],v[216:219],v[76:79]\n"
"buffer_store_dwordx4 v[64:67],v118,s[24:27],s22 offen sc1\n"
"v_mfma_f32_16x16x32_bf16 v[76:79],v[172:175],v[220:223],v[76:79]\n"
"buffer_load_dwordx4 v112,s[24:27],s8 offen offset:2048 sc1 lds\n"
"v_mfma_f32_16x16x32_bf16 v[72:75],v[168:171],v[220:223],v[72:75]\n"
"s_add_u32 s22,s22,262144\n"
"ds_read_b128 v[220:223],v113 offset:23552\n"
"v_mfma_f32_16x16x32_bf16 v[72:75],v[160:163],v[216:219],v[72:75]\n"
"ds_read_b128 v[216:219],v113 offset:22528\n"
"v_mfma_f32_16x16x32_bf16 v[72:75],v[8:11],v[212:215],v[72:75]\n"
"ds_read_b128 v[212:215],v113 offset:21504\n"
"v_mfma_f32_16x16x32_bf16 v[72:75],v[0:3],v[208:211],v[72:75]\n"
"ds_read_b128 v[208:211],v113 offset:20480\n"
"s_waitcnt lgkmcnt(12)\n"
"v_mfma_f32_16x16x32_bf16 v[72:75],v[176:179],v[224:227],v[72:75]\n"
"buffer_load_dwordx4 v112,s[24:27],s8 offen offset:3072 sc1 lds\n"
"v_mfma_f32_16x16x32_bf16 v[72:75],v[184:187],v[228:231],v[72:75]\n"
"v_xor_b32 v119,s80,v122\n"
"v_xor_b32 v120,s81,v123\n"
"v_mfma_f32_16x16x32_bf16 v[72:75],a[48:51],v[232:235],v[72:75]\n"
"v_or_b32 v119,v119,v120\n"
"v_cmp_ne_u32 s[28:29],0,v119\n"
"v_mfma_f32_16x16x32_bf16 v[72:75],a[56:59],v[236:239],v[72:75]\n"
"s_and_b64 s[14:15],s[14:15],s[28:29]\n"
"v_mfma_f32_16x16x32_bf16 v[76:79],a[60:63],v[236:239],v[76:79]\n"
"ds_read_b128 v[236:239],v113 offset:27648\n"
"v_mfma_f32_16x16x32_bf16 v[76:79],a[52:55],v[232:235],v[76:79]\n"
"ds_read_b128 v[232:235],v113 offset:26624\n"
"v_mfma_f32_16x16x32_bf16 v[76:79],v[188:191],v[228:231],v[76:79]\n"
"ds_read_b128 v[228:231],v113 offset:25600\n"
"v_mfma_f32_16x16x32_bf16 v[76:79],v[180:183],v[224:227],v[76:79]\n"
"s_add_u32 m0,m0,4096\n"
"ds_read_b128 v[224:227],v113 offset:24576\n"
"s_waitcnt lgkmcnt(12)\n"
"v_mfma_f32_16x16x32_bf16 v[76:79],v[52:55],v[240:243],v[76:79]\n"
"buffer_load_dwordx4 v117,s[24:27],s8 offen offset:0 sc1 lds\n"
"v_mfma_f32_16x16x32_bf16 v[76:79],v[60:63],v[244:247],v[76:79]\n"
"s_and_b32 s83,s15,0xff000000\n"
"s_cbranch_scc1 .Lfk_es_lktwom\n"
".Lfk_er_lktwom:\n"
"v_mfma_f32_16x16x32_bf16 v[76:79],v[20:23],v[248:251],v[76:79]\n"
"v_xor_b32 v119,s80,v104\n"
"v_xor_b32 v120,s81,v105\n"
"v_or_b32 v119,v119,v120\n"
"v_mfma_f32_16x16x32_bf16 v[76:79],v[28:31],v[252:255],v[76:79]\n"
"buffer_load_dwordx4 v117,s[24:27],s8 offen offset:1024 sc1 lds\n"
"v_cmp_ne_u32 s[28:29],0,v119\n"
"v_mfma_f32_16x16x32_bf16 v[72:75],v[24:27],v[252:255],v[72:75]\n"
"s_cmp_eq_u32 s52,0\n"
"ds_read_b128 v[252:255],v113 offset:31744\n"
"s_cselect_b64 s[14:15],-1,0\n"
"v_mfma_f32_16x16x32_bf16 v[72:75],v[16:19],v[248:251],v[72:75]\n"
"s_mov_b32 s11,s9\n"
"ds_read_b128 v[248:251],v113 offset:30720\n"
"s_waitcnt vmcnt(22)\n"
"v_not_b32 v122,s80\n"
"v_mfma_f32_16x16x32_bf16 v[72:75],v[56:59],v[244:247],v[72:75]\n"
"s_and_b64 s[14:15],s[14:15],s[28:29]\n"
"s_cselect_b32 s91,s11,2146435072\n"
"ds_read_b128 v[244:247],v113 offset:29696\n"
"buffer_load_dwordx2 v[122:123],v115,s[24:27],s91 offen sc1\n"
"v_mfma_f32_16x16x32_bf16 v[72:75],v[48:51],v[240:243],v[72:75]\n"
"ds_read_b128 v[240:243],v113 offset:28672\n"
"s_waitcnt lgkmcnt(12)\n"
"v_mfma_f32_16x16x32_bf16 v[88:91],v[128:131],v[192:195],v[88:91]\n"
"ds_write_b32 v101,v100\n"
"v_mfma_f32_16x16x32_bf16 v[88:91],v[136:139],v[196:199],v[88:91]\n"
"ds_add_u32 v114,v116\n"
"v_mfma_f32_16x16x32_bf16 v[88:91],v[144:147],v[200:203],v[88:91]\n"
"buffer_load_dwordx4 v117,s[24:27],s8 offen offset:2048 sc1 lds\n"
"v_mfma_f32_16x16x32_bf16 v[88:91],v[152:155],v[204:207],v[88:91]\n"
"s_add_u32 s83,s20,32768\n"
"s_and_b32 s83,s83,0x1ffff\n"
"v_add_u32 v113,s83,v112\n"
"v_mfma_f32_16x16x32_bf16 v[92:95],v[156:159],v[204:207],v[92:95]\n"
"v_cvt_pk_bf16_f32 v72,v72,v73\n"
"v_cvt_pk_bf16_f32 v73,v74,v75\n"
"v_cvt_pk_bf16_f32 v74,v76,v77\n"
"v_mfma_f32_16x16x32_bf16 v[92:95],v[148:151],v[200:203],v[92:95]\n"
"v_cvt_pk_bf16_f32 v75,v78,v79\n"
"v_max_u32 v72,1,v72\n"
"v_max_u32 v73,1,v73\n"
"v_mfma_f32_16x16x32_bf16 v[92:95],v[140:143],v[196:199],v[92:95]\n"
"v_max_u32 v74,1,v74\n"
"v_max_u32 v75,1,v75\n"
"v_mfma_f32_16x16x32_bf16 v[92:95],v[132:135],v[192:195],v[92:95]\n"
"buffer_store_dwordx4 v[72:75],v118,s[24:27],s22 offen sc1\n"
"s_waitcnt lgkmcnt(10)\n"
"v_mfma_f32_16x16x32_bf16 v[92:95],v[4:7],v[208:211],v[92:95]\n"
"buffer_load_dwordx4 v117,s[24:27],s8 offen offset:3072 sc1 lds\n"
"v_mfma_f32_16x16x32_bf16 v[92:95],v[12:15],v[212:215],v[92:95]\n"
"s_add_u32 s22,s22,262144\n"
"ds_read_b32 v125,v114\n"
"s_add_u32 s77,s77,4096\n"
"s_add_u32 m0,s30,135168\n"
"v_mfma_f32_16x16x32_bf16 v[92:95],v[164:167],v[216:219],v[92:95]\n"
"buffer_load_dwordx4 v98,s[40:43],s77 offen offset:0 nt lds\n"
"v_mfma_f32_16x16x32_bf16 v[92:95],v[172:175],v[220:223],v[92:95]\n"
"buffer_load_dwordx4 v98,s[40:43],s77 offen offset:1024 nt lds\n"
"v_mfma_f32_16x16x32_bf16 v[88:91],v[168:171],v[220:223],v[88:91]\n"
"s_lshl_b64 exec,1,s89\n"
"buffer_atomic_add v100,off,s[24:27],s10 sc0\n"
"s_mov_b64 exec,-1\n"
"v_mfma_f32_16x16x32_bf16 v[88:91],v[160:163],v[216:219],v[88:91]\n"
"s_waitcnt lgkmcnt(0)\n"
"buffer_load_dwordx4 v99,s[40:43],s77 offen offset:2048 nt lds\n"
"v_cmp_gt_u32 vcc,s16,v125\n"
"v_mfma_f32_16x16x32_bf16 v[88:91],v[8:11],v[212:215],v[88:91]\n"
"buffer_load_dwordx4 v99,s[40:43],s77 offen offset:3072 nt lds\n"
"v_mfma_f32_16x16x32_bf16 v[88:91],v[0:3],v[208:211],v[88:91]\n"
"s_add_u32 s8,s8,1048576\n"
"s_cbranch_vccnz .Lfk_es_mtwo\n"
".Lfk_er_mtwo:\n"
".Lfk_l4:\n"
"v_mfma_f32_16x16x32_bf16 v[88:91],v[176:179],v[224:227],v[88:91]\n"
"ds_read_b128 v[192:195],v113 offset:0\n"
"v_mfma_f32_16x16x32_bf16 v[88:91],v[184:187],v[228:231],v[88:91]\n"
"ds_read_b128 v[196:199],v113 offset:1024\n"
"v_mfma_f32_16x16x32_bf16 v[88:91],a[48:51],v[232:235],v[88:91]\n"
"ds_read_b128 v[200:203],v113 offset:2048\n"
"s_add_u32 s89,s89,1\n"
"v_mfma_f32_16x16x32_bf16 v[88:91],a[56:59],v[236:239],v[88:91]\n"
"ds_read_b128 v[204:207],v113 offset:3072\n"
"v_mfma_f32_16x16x32_bf16 v[92:95],a[60:63],v[236:239],v[92:95]\n"
"ds_read_b128 v[208:211],v113 offset:4096\n"
"v_mfma_f32_16x16x32_bf16 v[92:95],a[52:55],v[232:235],v[92:95]\n"
"ds_read_b128 v[212:215],v113 offset:5120\n"
"s_add_u32 s16,s16,4\n"
"v_mfma_f32_16x16x32_bf16 v[92:95],v[188:191],v[228:231],v[92:95]\n"
"ds_read_b128 v[216:219],v113 offset:6144\n"
"v_mfma_f32_16x16x32_bf16 v[92:95],v[180:183],v[224:227],v[92:95]\n"
"ds_read_b128 v[220:223],v113 offset:7168\n"
"v_mfma_f32_16x16x32_bf16 v[92:95],v[52:55],v[240:243],v[92:95]\n"
"ds_read_b128 v[236:239],v113 offset:11264\n"
"v_mfma_f32_16x16x32_bf16 v[92:95],v[60:63],v[244:247],v[92:95]\n"
"ds_read_b128 v[232:235],v113 offset:10240\n"
"v_mfma_f32_16x16x32_bf16 v[92:95],v[20:23],v[248:251],v[92:95]\n"
"ds_read_b128 v[228:231],v113 offset:9216\n"
"v_mfma_f32_16x16x32_bf16 v[92:95],v[28:31],v[252:255],v[92:95]\n"
"ds_read_b128 v[224:227],v113 offset:8192\n"
"v_mfma_f32_16x16x32_bf16 v[88:91],v[24:27],v[252:255],v[88:91]\n"
"ds_read_b128 v[252:255],v113 offset:15360\n"
"v_mfma_f32_16x16x32_bf16 v[88:91],v[16:19],v[248:251],v[88:91]\n"
"ds_read_b128 v[248:251],v113 offset:14336\n"
"v_mfma_f32_16x16x32_bf16 v[88:91],v[56:59],v[244:247],v[88:91]\n"
"ds_read_b128 v[244:247],v113 offset:13312\n"
"v_mfma_f32_16x16x32_bf16 v[88:91],v[48:51],v[240:243],v[88:91]\n"
"ds_read_b128 v[240:243],v113 offset:12288\n"
"s_waitcnt lgkmcnt(12)\n"
"v_mfma_f32_16x16x32_bf16 v[80:83],a[224:227],v[192:195],0\n"
"s_add_u32 m0,s30,s20\n"
"ds_read_b32 v103,v102\n"
"v_mfma_f32_16x16x32_bf16 v[80:83],a[232:235],v[196:199],v[80:83]\n"
"buffer_load_dwordx4 v112,s[24:27],s8 offen offset:0 sc1 lds\n"
"v_mfma_f32_16x16x32_bf16 v[80:83],a[240:243],v[200:203],v[80:83]\n"
"v_add_u32 v102,4,v102\n"
"v_and_b32 v102,0x200ff,v102\n"
"v_mfma_f32_16x16x32_bf16 v[80:83],a[248:251],v[204:207],v[80:83]\n"
"v_cvt_pk_bf16_f32 v88,v88,v89\n"
"v_cvt_pk_bf16_f32 v89,v90,v91\n"
"v_mfma_f32_16x16x32_bf16 v[84:87],a[252:255],v[204:207],0\n"
"ds_read_b128 v[204:207],v113 offset:19456\n"
"s_add_u32 s20,s20,32768\n"
"v_mfma_f32_16x16x32_bf16 v[84:87],a[244:247],v[200:203],v[84:87]\n"
"ds_read_b128 v[200:203],v113 offset:18432\n"
"s_and_b32 s20,s20,0x1ffff\n"
"v_mfma_f32_16x16x32_bf16 v[84:87],a[236:239],v[196:199],v[84:87]\n"
"ds_read_b128 v[196:199],v113 offset:17408\n"
"v_cvt_pk_bf16_f32 v90,v92,v93\n"
"v_mfma_f32_16x16x32_bf16 v[84:87],a[228:231],v[192:195],v[84:87]\n"
"ds_read_b128 v[192:195],v113 offset:16384\n"
"s_waitcnt lgkmcnt(13)\n"
"v_mfma_f32_16x16x32_bf16 v[84:87],a[4:7],v[208:211],v[84:87]\n"
"buffer_load_dwordx4 v112,s[24:27],s8 offen offset:1024 sc1 lds\n"
"v_mfma_f32_16x16x32_bf16 v[84:87],a[12:15],v[212:215],v[84:87]\n"
"v_cvt_pk_bf16_f32 v91,v94,v95\n"
"v_max_u32 v88,1,v88\n"
"v_mfma_f32_16x16x32_bf16 v[84:87],a[20:23],v[216:219],v[84:87]\n"
"v_max_u32 v89,1,v89\n"
"v_max_u32 v90,1,v90\n"
"v_mfma_f32_16x16x32_bf16 v[84:87],a[28:31],v[220:223],v[84:87]\n"
"buffer_load_dwordx4 v112,s[24:27],s8 offen offset:2048 sc1 lds\n"
"v_mfma_f32_16x16x32_bf16 v[80:83],a[24:27],v[220:223],v[80:83]\n"
"ds_read_b128 v[220:223],v113 offset:23552\n"
"v_max_u32 v91,1,v91\n"
"v_mfma_f32_16x16x32_bf16 v[80:83],a[16:19],v[216:219],v[80:83]\n"
"ds_read_b128 v[216:219],v113 offset:22528\n"
"buffer_store_dwordx4 v[88:91],v118,s[24:27],s53 offen sc1\n"
"v_mfma_f32_16x16x32_bf16 v[80:83],a[8:11],v[212:215],v[80:83]\n"
"ds_read_b128 v[212:215],v113 offset:21504\n"
"v_mfma_f32_16x16x32_bf16 v[80:83],a[0:3],v[208:211],v[80:83]\n"
"ds_read_b128 v[208:211],v113 offset:20480\n"
"s_waitcnt lgkmcnt(13)\n"
"v_mfma_f32_16x16x32_bf16 v[80:83],v[32:35],v[224:227],v[80:83]\n"
"v_mfma_f32_16x16x32_bf16 v[80:83],v[40:43],v[228:231],v[80:83]\n"
"buffer_load_dwordx4 v112,s[24:27],s8 offen offset:3072 sc1 lds\n"
"v_mfma_f32_16x16x32_bf16 v[80:83],a[32:35],v[232:235],v[80:83]\n"
"s_waitcnt lgkmcnt(8)\n"
"v_readfirstlane_b32 s22,v103\n"
"v_mfma_f32_16x16x32_bf16 v[80:83],a[40:43],v[236:239],v[80:83]\n"
"v_mfma_f32_16x16x32_bf16 v[84:87],a[44:47],v[236:239],v[84:87]\n"
"ds_read_b128 v[236:239],v113 offset:27648\n"
"s_and_b32 s21,s22,0x38\n"
"v_mfma_f32_16x16x32_bf16 v[84:87],a[36:39],v[232:235],v[84:87]\n"
"ds_read_b128 v[232:235],v113 offset:26624\n"
"v_mfma_f32_16x16x32_bf16 v[84:87],v[44:47],v[228:231],v[84:87]\n"
"ds_read_b128 v[228:231],v113 offset:25600\n"
"v_mfma_f32_16x16x32_bf16 v[84:87],v[36:39],v[224:227],v[84:87]\n"
"ds_read_b128 v[224:227],v113 offset:24576\n"
"s_add_u32 m0,m0,4096\n"
"v_mfma_f32_16x16x32_bf16 v[84:87],a[68:71],v[240:243],v[84:87]\n"
"buffer_load_dwordx4 v117,s[24:27],s8 offen offset:0 sc1 lds\n"
"v_mfma_f32_16x16x32_bf16 v[84:87],a[76:79],v[244:247],v[84:87]\n"
"s_cmp_lg_u64 s[14:15],0\n"
"s_cbranch_scc1 .Lfk_es_lkloopm\n"
".Lfk_er_lkloopm:\n"
"v_mfma_f32_16x16x32_bf16 v[84:87],a[84:87],v[248:251],v[84:87]\n"
"v_mfma_f32_16x16x32_bf16 v[84:87],a[92:95],v[252:255],v[84:87]\n"
"buffer_load_dwordx4 v117,s[24:27],s8 offen offset:1024 sc1 lds\n"
"v_mfma_f32_16x16x32_bf16 v[80:83],a[88:91],v[252:255],v[80:83]\n"
"ds_read_b128 v[252:255],v113 offset:31744\n"
"s_mov_b32 s53,s56\n"
"v_mfma_f32_16x16x32_bf16 v[80:83],a[80:83],v[248:251],v[80:83]\n"
"ds_read_b128 v[248:251],v113 offset:30720\n"
"s_mov_b32 s56,s58\n"
"v_mfma_f32_16x16x32_bf16 v[80:83],a[72:75],v[244:247],v[80:83]\n"
"ds_read_b128 v[244:247],v113 offset:29696\n"
"s_mov_b32 s58,s59\n"
"v_mfma_f32_16x16x32_bf16 v[80:83],a[156:159],v[240:243],v[80:83]\n"
"ds_read_b128 v[240:243],v113 offset:28672\n"
"s_waitcnt lgkmcnt(12)\n"
"v_mfma_f32_16x16x32_bf16 v[88:91],v[128:131],v[192:195],v[80:83]\n"
"s_waitcnt vmcnt(17)\n"
"ds_write_b32 v101,v100\n"
"v_mfma_f32_16x16x32_bf16 v[88:91],v[136:139],v[196:199],v[88:91]\n"
"ds_add_u32 v114,v116\n"
"v_mfma_f32_16x16x32_bf16 v[88:91],v[144:147],v[200:203],v[88:91]\n"
"buffer_load_dwordx4 v117,s[24:27],s8 offen offset:2048 sc1 lds\n"
"v_mfma_f32_16x16x32_bf16 v[88:91],v[152:155],v[204:207],v[88:91]\n"
"s_add_u32 s83,s20,32768\n"
"s_and_b32 s83,s83,0x1ffff\n"
"v_add_u32 v113,s83,v112\n"
"v_mfma_f32_16x16x32_bf16 v[92:95],v[156:159],v[204:207],v[84:87]\n"
"s_cmp_lt_u32 s22,64\n"
"s_cselect_b32 s17,s16,-1\n"
"v_mfma_f32_16x16x32_bf16 v[92:95],v[148:151],v[200:203],v[92:95]\n"
"v_mfma_f32_16x16x32_bf16 v[92:95],v[140:143],v[196:199],v[92:95]\n"
"v_mfma_f32_16x16x32_bf16 v[92:95],v[132:135],v[192:195],v[92:95]\n"
"s_waitcnt lgkmcnt(10)\n"
"v_mfma_f32_16x16x32_bf16 v[92:95],v[4:7],v[208:211],v[92:95]\n"
"buffer_load_dwordx4 v117,s[24:27],s8 offen offset:3072 sc1 lds\n"
"v_mfma_f32_16x16x32_bf16 v[92:95],v[12:15],v[212:215],v[92:95]\n"
"s_lshr_b32 s59,s8,2\n"
"ds_read_b32 v125,v114\n"
"s_lshl_b32 s8,s22,17\n"
"v_mfma_f32_16x16x32_bf16 v[92:95],v[164:167],v[216:219],v[92:95]\n"
"s_add_u32 s8,s8,s31\n"
"v_mfma_f32_16x16x32_bf16 v[92:95],v[172:175],v[220:223],v[92:95]\n"
"v_mfma_f32_16x16x32_bf16 v[88:91],v[168:171],v[220:223],v[88:91]\n"
"s_lshl_b64 exec,1,s89\n"
"buffer_atomic_add v100,off,s[24:27],s10 sc0\n"
"s_mov_b64 exec,-1\n"
"v_mfma_f32_16x16x32_bf16 v[88:91],v[160:163],v[216:219],v[88:91]\n"
"s_waitcnt lgkmcnt(0)\n"
"v_cmp_gt_u32 vcc,s17,v125\n"
"v_mfma_f32_16x16x32_bf16 v[88:91],v[8:11],v[212:215],v[88:91]\n"
"v_mfma_f32_16x16x32_bf16 v[88:91],v[0:3],v[208:211],v[88:91]\n"
"s_cbranch_vccz .Lfk_l4\n"
"s_call_b64 s[94:95],.Lfk_meet4\n"
"s_cmp_lt_u32 s22,64\n"
"s_cbranch_scc1 .Lfk_l4\n"
"ds_read_b128 v[192:195],v113 offset:0\n"
"v_mfma_f32_16x16x32_bf16 v[88:91],v[176:179],v[224:227],v[88:91]\n"
"s_getpc_b64 s[96:97]\n"
".Lfk_rt2:\n"
"s_add_u32 s96,s96,.Lfk_tail-.Lfk_rt2\n"
"s_addc_u32 s97,s97,0\n"
"s_mov_b32 s98,.Lfk_cold-.Lfk_tail\n"
"s_mov_b32 s99,0x20000\n"
"s_lshr_b32 s82,s100,3\n"
"s_lshl2_add_u32 s82,s82,s52\n"
"v_lshrrev_b32 v121,2,v112\n"
"s_lshl_b32 s82,s82,8\n"
"s_lshr_b32 s62,s60,4\n"
"v_mfma_f32_16x16x32_bf16 v[88:91],v[184:187],v[228:231],v[88:91]\n"
"buffer_load_dword v120,v121,s[96:99],s82 offen\n"
"ds_read_b128 v[196:199],v113 offset:1024\n"
"s_lshl_b32 s62,s62,15\n"
"v_mfma_f32_16x16x32_bf16 v[88:91],a[48:51],v[232:235],v[88:91]\n"
"s_and_b32 s91,s60,15\n"
"ds_read_b128 v[200:203],v113 offset:2048\n"
"v_lshlrev_b32 v102,2,v112\n"
"v_mfma_f32_16x16x32_bf16 v[88:91],a[56:59],v[236:239],v[88:91]\n"
"s_lshl_b32 s91,s91,6\n"
"ds_read_b128 v[204:207],v113 offset:3072\n"
"v_and_b32 v102,0x0,v102\n"
"v_xor_b32 v102,s91,v102\n"
"v_mfma_f32_16x16x32_bf16 v[92:95],a[60:63],v[236:239],v[92:95]\n"
"s_add_u32 s62,s62,71303168\n"
"ds_read_b128 v[208:211],v113 offset:4096\n"
"s_cmp_eq_u32 s42,0\n"
"v_mfma_f32_16x16x32_bf16 v[92:95],a[52:55],v[232:235],v[92:95]\n"
"s_cselect_b32 s72,0x3ff80,s23\n"
"ds_read_b128 v[212:215],v113 offset:5120\n"
"s_add_u32 s16,s16,4\n"
"v_mfma_f32_16x16x32_bf16 v[92:95],v[188:191],v[228:231],v[92:95]\n"
"s_add_u32 s91,s61,2097152\n"
"ds_read_b128 v[216:219],v113 offset:6144\n"
"v_lshrrev_b32 v126,6,v112\n"
"v_and_or_b32 v103,v112,48,v102\n"
"v_mfma_f32_16x16x32_bf16 v[92:95],v[180:183],v[224:227],v[92:95]\n"
"v_lshl_add_u32 v126,v126,10,v103\n"
"v_add_u32 v117,16384,v126\n"
"v_mfma_f32_16x16x32_bf16 v[92:95],v[52:55],v[240:243],v[92:95]\n"
"ds_read_b128 v[220:223],v113 offset:7168\n"
"ds_read_b128 v[236:239],v113 offset:11264\n"
"v_mfma_f32_16x16x32_bf16 v[92:95],v[60:63],v[244:247],v[92:95]\n"
"ds_read_b128 v[232:235],v113 offset:10240\n"
"v_mfma_f32_16x16x32_bf16 v[92:95],v[20:23],v[248:251],v[92:95]\n"
"ds_read_b128 v[228:231],v113 offset:9216\n"
"v_mfma_f32_16x16x32_bf16 v[92:95],v[28:31],v[252:255],v[92:95]\n"
"ds_read_b128 v[224:227],v113 offset:8192\n"
"v_mfma_f32_16x16x32_bf16 v[88:91],v[24:27],v[252:255],v[88:91]\n"
"ds_read_b128 v[252:255],v113 offset:15360\n"
"v_mfma_f32_16x16x32_bf16 v[88:91],v[16:19],v[248:251],v[88:91]\n"
"ds_read_b128 v[248:251],v113 offset:14336\n"
"v_mfma_f32_16x16x32_bf16 v[88:91],v[56:59],v[244:247],v[88:91]\n"
"ds_read_b128 v[244:247],v113 offset:13312\n"
"v_mfma_f32_16x16x32_bf16 v[88:91],v[48:51],v[240:243],v[88:91]\n"
"ds_read_b128 v[240:243],v113 offset:12288\n"
"s_waitcnt lgkmcnt(12)\n"
"v_mfma_f32_16x16x32_bf16 v[80:83],a[224:227],v[192:195],0\n"
"buffer_load_dwordx4 v[72:75],v112,s[44:47],0 offen offset:0\n"
"v_mfma_f32_16x16x32_bf16 v[80:83],a[232:235],v[196:199],v[80:83]\n"
"buffer_load_dwordx4 v[76:79],v112,s[44:47],0 offen offset:1024\n"
"v_mfma_f32_16x16x32_bf16 v[80:83],a[240:243],v[200:203],v[80:83]\n"
"s_nop 0\n"
"v_cvt_pk_bf16_f32 v88,v88,v89\n"
"v_mfma_f32_16x16x32_bf16 v[80:83],a[248:251],v[204:207],v[80:83]\n"
"v_cvt_pk_bf16_f32 v89,v90,v91\n"
"v_mfma_f32_16x16x32_bf16 v[84:87],a[252:255],v[204:207],0\n"
"ds_read_b128 v[204:207],v113 offset:19456\n"
"s_add_u32 s20,s20,32768\n"
"v_mfma_f32_16x16x32_bf16 v[84:87],a[244:247],v[200:203],v[84:87]\n"
"ds_read_b128 v[200:203],v113 offset:18432\n"
"s_and_b32 s20,s20,0x1ffff\n"
"v_mfma_f32_16x16x32_bf16 v[84:87],a[236:239],v[196:199],v[84:87]\n"
"ds_read_b128 v[196:199],v113 offset:17408\n"
"v_cvt_pk_bf16_f32 v90,v92,v93\n"
"v_mfma_f32_16x16x32_bf16 v[84:87],a[228:231],v[192:195],v[84:87]\n"
"s_mov_b32 s91,s62\n"
"ds_read_b128 v[192:195],v113 offset:16384\n"
"s_waitcnt lgkmcnt(12)\n"
"v_mfma_f32_16x16x32_bf16 v[84:87],a[4:7],v[208:211],v[84:87]\n"
"v_cvt_pk_bf16_f32 v91,v94,v95\n"
"v_max_u32 v88,1,v88\n"
"v_mfma_f32_16x16x32_bf16 v[84:87],a[12:15],v[212:215],v[84:87]\n"
"v_max_u32 v89,1,v89\n"
"v_max_u32 v90,1,v90\n"
"v_max_u32 v91,1,v91\n"
"v_mfma_f32_16x16x32_bf16 v[84:87],a[20:23],v[216:219],v[84:87]\n"
"buffer_load_dwordx4 a[112:115],v126,s[24:27],s91 offen sc1\n"
"v_mfma_f32_16x16x32_bf16 v[84:87],a[28:31],v[220:223],v[84:87]\n"
"buffer_store_dwordx4 v[88:91],v118,s[24:27],s53 offen sc1\n"
"v_mfma_f32_16x16x32_bf16 v[80:83],a[24:27],v[220:223],v[80:83]\n"
"ds_read_b128 v[220:223],v113 offset:23552\n"
"buffer_load_dwordx4 a[116:119],v117,s[24:27],s91 offen sc1\n"
"v_mfma_f32_16x16x32_bf16 v[80:83],a[16:19],v[216:219],v[80:83]\n"
"ds_read_b128 v[216:219],v113 offset:22528\n"
"v_mfma_f32_16x16x32_bf16 v[80:83],a[8:11],v[212:215],v[80:83]\n"
"ds_read_b128 v[212:215],v113 offset:21504\n"
"v_mfma_f32_16x16x32_bf16 v[80:83],a[0:3],v[208:211],v[80:83]\n"
"s_add_u32 s91,s91,4194304\n"
"ds_read_b128 v[208:211],v113 offset:20480\n"
"s_waitcnt lgkmcnt(12)\n"
"v_mfma_f32_16x16x32_bf16 v[80:83],v[32:35],v[224:227],v[80:83]\n"
"v_mfma_f32_16x16x32_bf16 v[80:83],v[40:43],v[228:231],v[80:83]\n"
"v_mfma_f32_16x16x32_bf16 v[80:83],a[32:35],v[232:235],v[80:83]\n"
"buffer_load_dwordx4 a[120:123],v126,s[24:27],s91 offen sc1\n"
"v_mfma_f32_16x16x32_bf16 v[80:83],a[40:43],v[236:239],v[80:83]\n"
"v_mfma_f32_16x16x32_bf16 v[84:87],a[44:47],v[236:239],v[84:87]\n"
"ds_read_b128 v[236:239],v113 offset:27648\n"
"v_mfma_f32_16x16x32_bf16 v[84:87],a[36:39],v[232:235],v[84:87]\n"
"ds_read_b128 v[232:235],v113 offset:26624\n"
"buffer_load_dwordx4 a[124:127],v117,s[24:27],s91 offen sc1\n"
"v_mfma_f32_16x16x32_bf16 v[84:87],v[44:47],v[228:231],v[84:87]\n"
"ds_read_b128 v[228:231],v113 offset:25600\n"
"v_mfma_f32_16x16x32_bf16 v[84:87],v[36:39],v[224:227],v[84:87]\n"
"ds_read_b128 v[224:227],v113 offset:24576\n"
"s_waitcnt lgkmcnt(12)\n"
"v_mfma_f32_16x16x32_bf16 v[84:87],a[68:71],v[240:243],v[84:87]\n"
"s_add_u32 s91,s91,4194304\n"
"v_mfma_f32_16x16x32_bf16 v[84:87],a[76:79],v[244:247],v[84:87]\n"
"v_mfma_f32_16x16x32_bf16 v[84:87],a[84:87],v[248:251],v[84:87]\n"
"v_mfma_f32_16x16x32_bf16 v[84:87],a[92:95],v[252:255],v[84:87]\n"
"buffer_load_dwordx4 a[128:131],v126,s[24:27],s91 offen sc1\n"
"v_mfma_f32_16x16x32_bf16 v[80:83],a[88:91],v[252:255],v[80:83]\n"
"ds_read_b128 v[252:255],v113 offset:31744\n"
"s_mov_b32 s53,s56\n"
"v_mfma_f32_16x16x32_bf16 v[80:83],a[80:83],v[248:251],v[80:83]\n"
"ds_read_b128 v[248:251],v113 offset:30720\n"
"s_mov_b32 s56,s58\n"
"v_mfma_f32_16x16x32_bf16 v[80:83],a[72:75],v[244:247],v[80:83]\n"
"s_mov_b32 s58,s59\n"
"ds_read_b128 v[244:247],v113 offset:29696\n"
"buffer_load_dwordx4 a[132:135],v117,s[24:27],s91 offen sc1\n"
"v_mfma_f32_16x16x32_bf16 v[80:83],a[156:159],v[240:243],v[80:83]\n"
"ds_read_b128 v[240:243],v113 offset:28672\n"
"s_waitcnt lgkmcnt(12)\n"
"v_mfma_f32_16x16x32_bf16 v[88:91],v[128:131],v[192:195],v[80:83]\n"
"s_waitcnt vmcnt(21)\n"
"ds_add_u32 v114,v116\n"
"v_mfma_f32_16x16x32_bf16 v[88:91],v[136:139],v[196:199],v[88:91]\n"
"s_add_u32 s91,s91,4194304\n"
"v_mfma_f32_16x16x32_bf16 v[88:91],v[144:147],v[200:203],v[88:91]\n"
"v_mfma_f32_16x16x32_bf16 v[88:91],v[152:155],v[204:207],v[88:91]\n"
"s_add_u32 s83,s20,32768\n"
"s_and_b32 s83,s83,0x1ffff\n"
"v_add_u32 v113,s83,v112\n"
"v_mfma_f32_16x16x32_bf16 v[92:95],v[156:159],v[204:207],v[84:87]\n"
"buffer_load_dwordx4 a[136:139],v126,s[24:27],s91 offen sc1\n"
"v_mfma_f32_16x16x32_bf16 v[92:95],v[148:151],v[200:203],v[92:95]\n"
"v_mfma_f32_16x16x32_bf16 v[92:95],v[140:143],v[196:199],v[92:95]\n"
"buffer_load_dwordx4 a[140:143],v117,s[24:27],s91 offen sc1\n"
"v_mfma_f32_16x16x32_bf16 v[92:95],v[132:135],v[192:195],v[92:95]\n"
"s_waitcnt lgkmcnt(9)\n"
"v_mfma_f32_16x16x32_bf16 v[92:95],v[4:7],v[208:211],v[92:95]\n"
"v_mfma_f32_16x16x32_bf16 v[92:95],v[12:15],v[212:215],v[92:95]\n"
"ds_read_b32 v125,v114\n"
"v_mfma_f32_16x16x32_bf16 v[92:95],v[164:167],v[216:219],v[92:95]\n"
"v_mfma_f32_16x16x32_bf16 v[92:95],v[172:175],v[220:223],v[92:95]\n"
"v_mfma_f32_16x16x32_bf16 v[88:91],v[168:171],v[220:223],v[88:91]\n"
"v_mfma_f32_16x16x32_bf16 v[88:91],v[160:163],v[216:219],v[88:91]\n"
"s_waitcnt lgkmcnt(0)\n"
"v_cmp_gt_u32 vcc,s16,v125\n"
"v_mfma_f32_16x16x32_bf16 v[88:91],v[8:11],v[212:215],v[88:91]\n"
"v_mfma_f32_16x16x32_bf16 v[88:91],v[0:3],v[208:211],v[88:91]\n"
"s_cbranch_vccnz .Lfk_es_me3\n"
".Lfk_er_me3:\n"
"v_mfma_f32_16x16x32_bf16 v[88:91],v[176:179],v[224:227],v[88:91]\n"
"ds_read_b128 v[192:195],v113 offset:0\n"
"v_lshrrev_b32 v101,4,v112\n"
"v_mfma_f32_16x16x32_bf16 v[88:91],v[184:187],v[228:231],v[88:91]\n"
"ds_read_b128 v[196:199],v113 offset:1024\n"
"v_and_b32 v102,15,v101\n"
"v_mfma_f32_16x16x32_bf16 v[88:91],a[48:51],v[232:235],v[88:91]\n"
"ds_read_b128 v[200:203],v113 offset:2048\n"
"v_lshrrev_b32 v101,4,v101\n"
"v_mfma_f32_16x16x32_bf16 v[88:91],a[56:59],v[236:239],v[88:91]\n"
"ds_read_b128 v[204:207],v113 offset:3072\n"
"v_lshlrev_b32 v101,2,v101\n"
"v_mfma_f32_16x16x32_bf16 v[92:95],a[60:63],v[236:239],v[92:95]\n"
"ds_read_b128 v[208:211],v113 offset:4096\n"
"v_sub_u32 v101,v102,v101\n"
"v_mfma_f32_16x16x32_bf16 v[92:95],a[52:55],v[232:235],v[92:95]\n"
"ds_read_b128 v[212:215],v113 offset:5120\n"
"s_add_u32 s16,s16,4\n"
"v_mfma_f32_16x16x32_bf16 v[92:95],v[188:191],v[228:231],v[92:95]\n"
"ds_read_b128 v[216:219],v113 offset:6144\n"
"v_mov_b32 v64,0\n"
"v_mfma_f32_16x16x32_bf16 v[92:95],v[180:183],v[224:227],v[92:95]\n"
"ds_read_b128 v[220:223],v113 offset:7168\n"
"v_mov_b32 v65,0\n"
"v_mfma_f32_16x16x32_bf16 v[92:95],v[52:55],v[240:243],v[92:95]\n"
"ds_read_b128 v[236:239],v113 offset:11264\n"
"v_mov_b32 v68,0\n"
"v_mfma_f32_16x16x32_bf16 v[92:95],v[60:63],v[244:247],v[92:95]\n"
"ds_read_b128 v[232:235],v113 offset:10240\n"
"v_mov_b32 v69,0\n"
"v_mfma_f32_16x16x32_bf16 v[92:95],v[20:23],v[248:251],v[92:95]\n"
"ds_read_b128 v[228:231],v113 offset:9216\n"
"v_mov_b32 v102,0x3f80\n"
"v_mfma_f32_16x16x32_bf16 v[92:95],v[28:31],v[252:255],v[92:95]\n"
"ds_read_b128 v[224:227],v113 offset:8192\n"
"v_mov_b32 v103,0x3f800000\n"
"v_cmp_eq_u32 vcc,0,v101\n"
"v_mfma_f32_16x16x32_bf16 v[88:91],v[24:27],v[252:255],v[88:91]\n"
"ds_read_b128 v[252:255],v113 offset:15360\n"
"v_cndmask_b32 v66,0,v102,vcc\n"
"v_cmp_eq_u32 vcc,1,v101\n"
"v_mfma_f32_16x16x32_bf16 v[88:91],v[16:19],v[248:251],v[88:91]\n"
"ds_read_b128 v[248:251],v113 offset:14336\n"
"v_cndmask_b32 v66,v66,v103,vcc\n"
"v_cmp_eq_u32 vcc,2,v101\n"
"v_mfma_f32_16x16x32_bf16 v[88:91],v[56:59],v[244:247],v[88:91]\n"
"ds_read_b128 v[244:247],v113 offset:13312\n"
"v_cndmask_b32 v67,0,v102,vcc\n"
"v_mfma_f32_16x16x32_bf16 v[88:91],v[48:51],v[240:243],v[88:91]\n"
"ds_read_b128 v[240:243],v113 offset:12288\n"
"s_waitcnt lgkmcnt(12)\n"
"v_cmp_eq_u32 vcc,3,v101\n"
"v_mfma_f32_16x16x32_bf16 v[80:83],a[224:227],v[192:195],0\n"
"s_nop 0\n"
"v_cndmask_b32 v67,v67,v103,vcc\n"
"v_mfma_f32_16x16x32_bf16 v[80:83],a[232:235],v[196:199],v[80:83]\n"
"v_mfma_f32_16x16x32_bf16 v[80:83],a[240:243],v[200:203],v[80:83]\n"
"v_mfma_f32_16x16x32_bf16 v[80:83],a[248:251],v[204:207],v[80:83]\n"
"v_cvt_pk_bf16_f32 v88,v88,v89\n"
"v_cvt_pk_bf16_f32 v89,v90,v91\n"
"v_mfma_f32_16x16x32_bf16 v[84:87],a[252:255],v[204:207],0\n"
"ds_read_b128 v[204:207],v113 offset:19456\n"
"s_add_u32 s20,s20,32768\n"
"v_mfma_f32_16x16x32_bf16 v[84:87],a[244:247],v[200:203],v[84:87]\n"
"ds_read_b128 v[200:203],v113 offset:18432\n"
"s_and_b32 s20,s20,0x1ffff\n"
"v_mfma_f32_16x16x32_bf16 v[84:87],a[236:239],v[196:199],v[84:87]\n"
"ds_read_b128 v[196:199],v113 offset:17408\n"
"v_cvt_pk_bf16_f32 v90,v92,v93\n"
"v_mfma_f32_16x16x32_bf16 v[84:87],a[228:231],v[192:195],v[84:87]\n"
"ds_read_b128 v[192:195],v113 offset:16384\n"
"s_waitcnt lgkmcnt(12)\n"
"v_mfma_f32_16x16x32_bf16 v[84:87],a[4:7],v[208:211],v[84:87]\n"
"v_cvt_pk_bf16_f32 v91,v94,v95\n"
"v_max_u32 v88,1,v88\n"
"v_mfma_f32_16x16x32_bf16 v[84:87],a[12:15],v[212:215],v[84:87]\n"
"v_max_u32 v89,1,v89\n"
"v_max_u32 v90,1,v90\n"
"v_max_u32 v91,1,v91\n"
"v_mfma_f32_16x16x32_bf16 v[84:87],a[20:23],v[216:219],v[84:87]\n"
"buffer_store_dwordx4 v[88:91],v118,s[24:27],s53 offen sc1\n"
"v_mfma_f32_16x16x32_bf16 v[84:87],a[28:31],v[220:223],v[84:87]\n"
"v_mfma_f32_16x16x32_bf16 v[80:83],a[24:27],v[220:223],v[80:83]\n"
"ds_read_b128 v[220:223],v113 offset:23552\n"
"v_mfma_f32_16x16x32_bf16 v[80:83],a[16:19],v[216:219],v[80:83]\n"
"ds_read_b128 v[216:219],v113 offset:22528\n"
"v_mfma_f32_16x16x32_bf16 v[80:83],a[8:11],v[212:215],v[80:83]\n"
"ds_read_b128 v[212:215],v113 offset:21504\n"
"v_mfma_f32_16x16x32_bf16 v[80:83],a[0:3],v[208:211],v[80:83]\n"
"ds_read_b128 v[208:211],v113 offset:20480\n"
"s_waitcnt lgkmcnt(12)\n"
"v_mfma_f32_16x16x32_bf16 v[80:83],v[32:35],v[224:227],v[80:83]\n"
"v_mfma_f32_16x16x32_bf16 v[80:83],v[40:43],v[228:231],v[80:83]\n"
"v_mfma_f32_16x16x32_bf16 v[80:83],a[32:35],v[232:235],v[80:83]\n"
"v_mfma_f32_16x16x32_bf16 v[80:83],a[40:43],v[236:239],v[80:83]\n"
"v_mfma_f32_16x16x32_bf16 v[84:87],a[44:47],v[236:239],v[84:87]\n"
"ds_read_b128 v[236:239],v113 offset:27648\n"
"v_mfma_f32_16x16x32_bf16 v[84:87],a[36:39],v[232:235],v[84:87]\n"
"ds_read_b128 v[232:235],v113 offset:26624\n"
"v_mfma_f32_16x16x32_bf16 v[84:87],v[44:47],v[228:231],v[84:87]\n"
"ds_read_b128 v[228:231],v113 offset:25600\n"
"v_mfma_f32_16x16x32_bf16 v[84:87],v[36:39],v[224:227],v[84:87]\n"
"ds_read_b128 v[224:227],v113 offset:24576\n"
"s_waitcnt lgkmcnt(12)\n"
"v_mfma_f32_16x16x32_bf16 v[84:87],a[68:71],v[240:243],v[84:87]\n"
"v_mfma_f32_16x16x32_bf16 v[84:87],a[76:79],v[244:247],v[84:87]\n"
"v_mfma_f32_16x16x32_bf16 v[84:87],a[84:87],v[248:251],v[84:87]\n"
"v_mfma_f32_16x16x32_bf16 v[84:87],a[92:95],v[252:255],v[84:87]\n"
"v_mfma_f32_16x16x32_bf16 v[80:83],a[88:91],v[252:255],v[80:83]\n"
"ds_read_b128 v[252:255],v113 offset:31744\n"
"s_mov_b32 s53,s56\n"
"v_mfma_f32_16x16x32_bf16 v[80:83],a[80:83],v[248:251],v[80:83]\n"
"ds_read_b128 v[248:251],v113 offset:30720\n"
"s_mov_b32 s56,s58\n"
"v_mfma_f32_16x16x32_bf16 v[80:83],a[72:75],v[244:247],v[80:83]\n"
"ds_read_b128 v[244:247],v113 offset:29696\n"
"s_mov_b32 s58,s59\n"
"v_mfma_f32_16x16x32_bf16 v[80:83],a[156:159],v[240:243],v[80:83]\n"
"ds_read_b128 v[240:243],v113 offset:28672\n"
"s_waitcnt lgkmcnt(12)\n"
"v_mfma_f32_16x16x32_bf16 v[88:91],v[128:131],v[192:195],v[80:83]\n"
"s_waitcnt vmcnt(14)\n"
"ds_add_u32 v114,v116\n"
"v_mfma_f32_16x16x32_bf16 v[88:91],v[136:139],v[196:199],v[88:91]\n"
"v_mfma_f32_16x16x32_bf16 v[88:91],v[144:147],v[200:203],v[88:91]\n"
"v_mfma_f32_16x16x32_bf16 v[88:91],v[152:155],v[204:207],v[88:91]\n"
"s_add_u32 s83,s20,32768\n"
"s_and_b32 s83,s83,0x1ffff\n"
"v_add_u32 v113,s83,v112\n"
"v_mfma_f32_16x16x32_bf16 v[92:95],v[156:159],v[204:207],v[84:87]\n"
"v_mfma_f32_16x16x32_bf16 v[92:95],v[148:151],v[200:203],v[92:95]\n"
"v_mfma_f32_16x16x32_bf16 v[92:95],v[140:143],v[196:199],v[92:95]\n"
"v_mfma_f32_16x16x32_bf16 v[92:95],v[132:135],v[192:195],v[92:95]\n"
"s_waitcnt lgkmcnt(9)\n"
"v_mfma_f32_16x16x32_bf16 v[92:95],v[4:7],v[208:211],v[92:95]\n"
"v_mfma_f32_16x16x32_bf16 v[92:95],v[12:15],v[212:215],v[92:95]\n"
"ds_read_b32 v125,v114\n"
"v_mfma_f32_16x16x32_bf16 v[92:95],v[164:167],v[216:219],v[92:95]\n"
"v_mfma_f32_16x16x32_bf16 v[92:95],v[172:175],v[220:223],v[92:95]\n"
"v_mfma_f32_16x16x32_bf16 v[88:91],v[168:171],v[220:223],v[88:91]\n"
"v_mfma_f32_16x16x32_bf16 v[88:91],v[160:163],v[216:219],v[88:91]\n"
"s_waitcnt lgkmcnt(0)\n"
"v_cmp_gt_u32 vcc,s16,v125\n"
"v_mfma_f32_16x16x32_bf16 v[88:91],v[8:11],v[212:215],v[88:91]\n"
"v_mfma_f32_16x16x32_bf16 v[88:91],v[0:3],v[208:211],v[88:91]\n"
"s_cbranch_vccnz .Lfk_es_me2\n"
".Lfk_er_me2:\n"
"ds_read_b128 v[192:195],v113 offset:0\n"
"v_mfma_f32_16x16x32_bf16 v[88:91],v[176:179],v[224:227],v[88:91]\n"
"v_lshlrev_b32 v104,6,v112\n"
"v_and_b32 v105,0x8000,v104\n"
"v_mfma_f32_16x16x32_bf16 v[88:91],v[184:187],v[228:231],v[88:91]\n"
"ds_read_b128 v[196:199],v113 offset:1024\n"
"v_and_b32 v104,0x7c00,v104\n"
"v_lshl_add_u32 v104,v105,7,v104\n"
"v_mfma_f32_16x16x32_bf16 v[88:91],a[48:51],v[232:235],v[88:91]\n"
"s_and_b32 s82,s60,15\n"
"ds_read_b128 v[200:203],v113 offset:2048\n"
"s_lshl_b32 s82,s82,6\n"
"v_mfma_f32_16x16x32_bf16 v[88:91],a[56:59],v[236:239],v[88:91]\n"
"ds_read_b128 v[204:207],v113 offset:3072\n"
"v_add_u32 v104,s82,v104\n"
"v_mfma_f32_16x16x32_bf16 v[92:95],a[60:63],v[236:239],v[92:95]\n"
"s_and_b32 s82,s60,1\n"
"ds_read_b128 v[208:211],v113 offset:4096\n"
"s_lshl_b32 s82,s82,23\n"
"v_mfma_f32_16x16x32_bf16 v[92:95],a[52:55],v[232:235],v[92:95]\n"
"s_add_u32 s82,s82,s62\n"
"s_add_u32 s82,s82,2097152\n"
"s_cmp_lt_u32 s52,2\n"
"v_mfma_f32_16x16x32_bf16 v[92:95],v[188:191],v[228:231],v[92:95]\n"
"s_cselect_b32 s82,s82,2146435072\n"
"ds_read_b128 v[212:215],v113 offset:5120\n"
"ds_read_b128 v[216:219],v113 offset:6144\n"
"v_mfma_f32_16x16x32_bf16 v[92:95],v[180:183],v[224:227],v[92:95]\n"
"ds_read_b128 v[220:223],v113 offset:7168\n"
"v_mfma_f32_16x16x32_bf16 v[92:95],v[52:55],v[240:243],v[92:95]\n"
"ds_read_b128 v[236:239],v113 offset:11264\n"
"v_mfma_f32_16x16x32_bf16 v[92:95],v[60:63],v[244:247],v[92:95]\n"
"ds_read_b128 v[232:235],v113 offset:10240\n"
"v_mfma_f32_16x16x32_bf16 v[92:95],v[20:23],v[248:251],v[92:95]\n"
"ds_read_b128 v[228:231],v113 offset:9216\n"
"v_mfma_f32_16x16x32_bf16 v[92:95],v[28:31],v[252:255],v[92:95]\n"
"ds_read_b128 v[224:227],v113 offset:8192\n"
"v_mfma_f32_16x16x32_bf16 v[88:91],v[24:27],v[252:255],v[88:91]\n"
"ds_read_b128 v[252:255],v113 offset:15360\n"
"v_mfma_f32_16x16x32_bf16 v[88:91],v[16:19],v[248:251],v[88:91]\n"
"ds_read_b128 v[248:251],v113 offset:14336\n"
"v_mfma_f32_16x16x32_bf16 v[88:91],v[56:59],v[244:247],v[88:91]\n"
"ds_read_b128 v[244:247],v113 offset:13312\n"
"v_mfma_f32_16x16x32_bf16 v[88:91],v[48:51],v[240:243],v[88:91]\n"
"ds_read_b128 v[240:243],v113 offset:12288\n"
"s_waitcnt lgkmcnt(12)\n"
"v_mfma_f32_16x16x32_bf16 v[80:83],a[224:227],v[192:195],0\n"
"buffer_load_dword a148,v104,s[24:27],s82 offen sc1\n"
"v_mfma_f32_16x16x32_bf16 v[80:83],a[232:235],v[196:199],v[80:83]\n"
"v_mfma_f32_16x16x32_bf16 v[80:83],a[240:243],v[200:203],v[80:83]\n"
"v_mfma_f32_16x16x32_bf16 v[80:83],a[248:251],v[204:207],v[80:83]\n"
"s_nop 0\n"
"v_cvt_pk_bf16_f32 v88,v88,v89\n"
"v_cvt_pk_bf16_f32 v89,v90,v91\n"
"v_mfma_f32_16x16x32_bf16 v[84:87],a[252:255],v[204:207],0\n"
"ds_read_b128 v[204:207],v113 offset:19456\n"
"v_cvt_pk_bf16_f32 v90,v92,v93\n"
"v_mfma_f32_16x16x32_bf16 v[84:87],a[244:247],v[200:203],v[84:87]\n"
"ds_read_b128 v[200:203],v113 offset:18432\n"
"v_cvt_pk_bf16_f32 v91,v94,v95\n"
"v_mfma_f32_16x16x32_bf16 v[84:87],a[236:239],v[196:199],v[84:87]\n"
"ds_read_b128 v[196:199],v113 offset:17408\n"
"v_max_u32 v88,1,v88\n"
"v_mfma_f32_16x16x32_bf16 v[84:87],a[228:231],v[192:195],v[84:87]\n"
"ds_read_b128 v[192:195],v113 offset:16384\n"
"s_waitcnt lgkmcnt(12)\n"
"v_mfma_f32_16x16x32_bf16 v[84:87],a[4:7],v[208:211],v[84:87]\n"
"v_max_u32 v89,1,v89\n"
"v_max_u32 v90,1,v90\n"
"v_max_u32 v91,1,v91\n"
"v_mfma_f32_16x16x32_bf16 v[84:87],a[12:15],v[212:215],v[84:87]\n"
"buffer_store_dwordx4 v[88:91],v118,s[24:27],s53 offen sc1\n"
"v_mfma_f32_16x16x32_bf16 v[84:87],a[20:23],v[216:219],v[84:87]\n"
"v_mfma_f32_16x16x32_bf16 v[84:87],a[28:31],v[220:223],v[84:87]\n"
"v_mfma_f32_16x16x32_bf16 v[80:83],a[24:27],v[220:223],v[80:83]\n"
"ds_read_b128 v[220:223],v113 offset:23552\n"
"v_mfma_f32_16x16x32_bf16 v[80:83],a[16:19],v[216:219],v[80:83]\n"
"ds_read_b128 v[216:219],v113 offset:22528\n"
"v_mfma_f32_16x16x32_bf16 v[80:83],a[8:11],v[212:215],v[80:83]\n"
"ds_read_b128 v[212:215],v113 offset:21504\n"
"v_mfma_f32_16x16x32_bf16 v[80:83],a[0:3],v[208:211],v[80:83]\n"
"ds_read_b128 v[208:211],v113 offset:20480\n"
"s_waitcnt lgkmcnt(12)\n"
"v_mfma_f32_16x16x32_bf16 v[80:83],v[32:35],v[224:227],v[80:83]\n"
"v_mfma_f32_16x16x32_bf16 v[80:83],v[40:43],v[228:231],v[80:83]\n"
"v_mfma_f32_16x16x32_bf16 v[80:83],a[32:35],v[232:235],v[80:83]\n"
"v_mfma_f32_16x16x32_bf16 v[80:83],a[40:43],v[236:239],v[80:83]\n"
"v_mfma_f32_16x16x32_bf16 v[84:87],a[44:47],v[236:239],v[84:87]\n"
"ds_read_b128 v[236:239],v113 offset:27648\n"
"v_mfma_f32_16x16x32_bf16 v[84:87],a[36:39],v[232:235],v[84:87]\n"
"ds_read_b128 v[232:235],v113 offset:26624\n"
"v_mfma_f32_16x16x32_bf16 v[84:87],v[44:47],v[228:231],v[84:87]\n"
"ds_read_b128 v[228:231],v113 offset:25600\n"
"v_mfma_f32_16x16x32_bf16 v[84:87],v[36:39],v[224:227],v[84:87]\n"
"ds_read_b128 v[224:227],v113 offset:24576\n"
"s_waitcnt lgkmcnt(12)\n"
"v_mfma_f32_16x16x32_bf16 v[84:87],a[68:71],v[240:243],v[84:87]\n"
"v_mfma_f32_16x16x32_bf16 v[84:87],a[76:79],v[244:247],v[84:87]\n"
"v_mfma_f32_16x16x32_bf16 v[84:87],a[84:87],v[248:251],v[84:87]\n"
"v_mfma_f32_16x16x32_bf16 v[84:87],a[92:95],v[252:255],v[84:87]\n"
"v_mfma_f32_16x16x32_bf16 v[80:83],a[88:91],v[252:255],v[80:83]\n"
"ds_read_b128 v[252:255],v113 offset:31744\n"
"s_mov_b32 s53,s56\n"
"v_mfma_f32_16x16x32_bf16 v[80:83],a[80:83],v[248:251],v[80:83]\n"
"ds_read_b128 v[248:251],v113 offset:30720\n"
"s_mov_b32 s56,s58\n"
"v_mfma_f32_16x16x32_bf16 v[80:83],a[72:75],v[244:247],v[80:83]\n"
"ds_read_b128 v[244:247],v113 offset:29696\n"
"s_mov_b32 s58,s59\n"
"v_mfma_f32_16x16x32_bf16 v[80:83],a[156:159],v[240:243],v[80:83]\n"
"ds_read_b128 v[240:243],v113 offset:28672\n"
"s_waitcnt lgkmcnt(12)\n"
"v_mfma_f32_16x16x32_bf16 v[88:91],v[128:131],v[192:195],v[80:83]\n"
"v_mfma_f32_16x16x32_bf16 v[88:91],v[136:139],v[196:199],v[88:91]\n"
"v_mfma_f32_16x16x32_bf16 v[88:91],v[144:147],v[200:203],v[88:91]\n"
"v_mfma_f32_16x16x32_bf16 v[88:91],v[152:155],v[204:207],v[88:91]\n"
"v_mfma_f32_16x16x32_bf16 v[92:95],v[156:159],v[204:207],v[84:87]\n"
"v_mfma_f32_16x16x32_bf16 v[92:95],v[148:151],v[200:203],v[92:95]\n"
"v_mfma_f32_16x16x32_bf16 v[92:95],v[140:143],v[196:199],v[92:95]\n"
"v_mfma_f32_16x16x32_bf16 v[92:95],v[132:135],v[192:195],v[92:95]\n"
"s_waitcnt lgkmcnt(8)\n"
"v_mfma_f32_16x16x32_bf16 v[92:95],v[4:7],v[208:211],v[92:95]\n"
"v_mfma_f32_16x16x32_bf16 v[92:95],v[12:15],v[212:215],v[92:95]\n"
"v_mfma_f32_16x16x32_bf16 v[92:95],v[164:167],v[216:219],v[92:95]\n"
"v_mfma_f32_16x16x32_bf16 v[92:95],v[172:175],v[220:223],v[92:95]\n"
"v_mfma_f32_16x16x32_bf16 v[88:91],v[168:171],v[220:223],v[88:91]\n"
"s_mov_b32 s91,s62\n"
"v_mfma_f32_16x16x32_bf16 v[88:91],v[160:163],v[216:219],v[88:91]\n"
"v_mfma_f32_16x16x32_bf16 v[88:91],v[8:11],v[212:215],v[88:91]\n"
"v_mfma_f32_16x16x32_bf16 v[88:91],v[0:3],v[208:211],v[88:91]\n"
"s_waitcnt lgkmcnt(0)\n"
"s_waitcnt vmcnt(3)\n"
"v_mfma_f32_16x16x32_bf16 v[88:91],v[176:179],v[224:227],v[88:91]\n"
"v_accvgpr_read_b32 v128,a112\n"
"v_accvgpr_read_b32 v129,a113\n"
"v_mfma_f32_16x16x32_bf16 v[88:91],v[184:187],v[228:231],v[88:91]\n"
"v_accvgpr_read_b32 v130,a114\n"
"v_accvgpr_read_b32 v131,a115\n"
"v_mfma_f32_16x16x32_bf16 v[88:91],a[48:51],v[232:235],v[88:91]\n"
"v_accvgpr_read_b32 v132,a116\n"
"v_accvgpr_read_b32 v133,a117\n"
"v_mfma_f32_16x16x32_bf16 v[88:91],a[56:59],v[236:239],v[88:91]\n"
"v_accvgpr_read_b32 v134,a118\n"
"v_accvgpr_read_b32 v135,a119\n"
"v_mfma_f32_16x16x32_bf16 v[92:95],a[60:63],v[236:239],v[92:95]\n"
"v_accvgpr_read_b32 v136,a120\n"
"v_accvgpr_read_b32 v137,a121\n"
"v_mfma_f32_16x16x32_bf16 v[92:95],a[52:55],v[232:235],v[92:95]\n"
"v_accvgpr_read_b32 v138,a122\n"
"v_accvgpr_read_b32 v139,a123\n"
"v_mfma_f32_16x16x32_bf16 v[92:95],v[188:191],v[228:231],v[92:95]\n"
"v_accvgpr_read_b32 v140,a124\n"
"v_accvgpr_read_b32 v141,a125\n"
"v_mfma_f32_16x16x32_bf16 v[92:95],v[180:183],v[224:227],v[92:95]\n"
"v_accvgpr_read_b32 v142,a126\n"
"v_accvgpr_read_b32 v143,a127\n"
"v_mfma_f32_16x16x32_bf16 v[92:95],v[52:55],v[240:243],v[92:95]\n"
"v_accvgpr_read_b32 v144,a128\n"
"v_accvgpr_read_b32 v145,a129\n"
"v_mfma_f32_16x16x32_bf16 v[92:95],v[60:63],v[244:247],v[92:95]\n"
"v_accvgpr_read_b32 v146,a130\n"
"v_accvgpr_read_b32 v147,a131\n"
"v_mfma_f32_16x16x32_bf16 v[92:95],v[20:23],v[248:251],v[92:95]\n"
"v_accvgpr_read_b32 v148,a132\n"
"v_accvgpr_read_b32 v149,a133\n"
"v_mfma_f32_16x16x32_bf16 v[92:95],v[28:31],v[252:255],v[92:95]\n"
"v_accvgpr_read_b32 v150,a134\n"
"v_accvgpr_read_b32 v151,a135\n"
"v_mfma_f32_16x16x32_bf16 v[88:91],v[24:27],v[252:255],v[88:91]\n"
"v_accvgpr_read_b32 v152,a136\n"
"v_accvgpr_read_b32 v153,a137\n"
"v_mfma_f32_16x16x32_bf16 v[88:91],v[16:19],v[248:251],v[88:91]\n"
"v_accvgpr_read_b32 v154,a138\n"
"v_accvgpr_read_b32 v155,a139\n"
"v_mfma_f32_16x16x32_bf16 v[88:91],v[56:59],v[244:247],v[88:91]\n"
"v_accvgpr_read_b32 v156,a140\n"
"v_accvgpr_read_b32 v157,a141\n"
"v_mfma_f32_16x16x32_bf16 v[88:91],v[48:51],v[240:243],v[88:91]\n"
"v_accvgpr_read_b32 v158,a142\n"
"v_accvgpr_read_b32 v159,a143\n"
".Lfk_tail:\n"
"v_lshlrev_b32 v224,16,v72\n"
"v_and_b32 v225,0xffff0000,v72\n"
"v_lshlrev_b32 v226,16,v73\n"
"v_and_b32 v227,0xffff0000,v73\n"
"v_lshlrev_b32 v228,16,v74\n"
"s_nop 0\n"
"v_cvt_pk_bf16_f32 v88,v88,v89\n"
"v_cvt_pk_bf16_f32 v89,v90,v91\n"
"v_cvt_pk_bf16_f32 v90,v92,v93\n"
"v_cvt_pk_bf16_f32 v91,v94,v95\n"
"v_max_u32 v88,1,v88\n"
"v_max_u32 v89,1,v89\n"
"v_max_u32 v90,1,v90\n"
"v_max_u32 v91,1,v91\n"
"v_mfma_f32_16x16x32_bf16 v[0:3],v[66:69],a[96:99],0\n"
"buffer_store_dwordx4 v[88:91],v118,s[24:27],s53 offen sc1\n"
"v_mfma_f32_16x16x32_bf16 v[4:7],v[64:67],a[96:99],0\n"
"s_mov_b32 s101,s22\n"
"v_and_b32 v231,0xffff0000,v75\n"
"v_lshlrev_b32 v232,16,v76\n"
"v_mfma_f32_16x16x32_bf16 v[8:11],v[66:69],a[100:103],0\n"
"v_and_b32 v229,0xffff0000,v74\n"
"v_lshlrev_b32 v230,16,v75\n"
"v_mfma_f32_16x16x32_bf16 v[12:15],v[64:67],a[100:103],0\n"
"s_mov_b32 s67,0x3a800000\n"
"v_and_b32 v233,0xffff0000,v76\n"
"v_lshlrev_b32 v234,16,v77\n"
"v_and_b32 v235,0xffff0000,v77\n"
"v_lshlrev_b32 v236,16,v78\n"
"v_and_b32 v237,0xffff0000,v78\n"
"v_lshlrev_b32 v238,16,v79\n"
"v_and_b32 v239,0xffff0000,v79\n"
"v_pk_add_f32 v[70:71],v[224:225],v[226:227]\n"
"v_pk_add_f32 v[70:71],v[70:71],v[228:229]\n"
"v_pk_add_f32 v[70:71],v[70:71],v[230:231]\n"
"v_pk_add_f32 v[70:71],v[70:71],v[232:233]\n"
"v_pk_add_f32 v[70:71],v[70:71],v[234:235]\n"
"v_pk_add_f32 v[70:71],v[70:71],v[236:237]\n"
"v_pk_add_f32 v[70:71],v[70:71],v[238:239]\n"
"v_add_f32 v70,v70,v71\n"
"s_nop 1\n"
"v_add_f32_dpp v70,v70,v70 quad_perm:[1,0,3,2] row_mask:0xf bank_mask:0xf\n"
"s_nop 1\n"
"v_add_f32_dpp v70,v70,v70 quad_perm:[2,3,0,1] row_mask:0xf bank_mask:0xf\n"
"s_nop 1\n"
"v_add_f32_dpp v70,v70,v70 row_half_mirror row_mask:0xf bank_mask:0xf\n"
"s_nop 1\n"
"v_add_f32_dpp v70,v70,v70 row_mirror row_mask:0xf bank_mask:0xf\n"
"v_mov_b32 v71,v70\n"
"s_nop 1\n"
"v_permlane16_swap_b32 v71,v70\n"
"v_add_f32 v70,v70,v71\n"
"v_mov_b32 v71,v70\n"
"s_nop 1\n"
"v_permlane32_swap_b32 v71,v70\n"
"v_add_f32 v70,v70,v71\n"
"v_min3_u32 v36,v128,v129,v130\n"
"v_min3_u32 v37,v131,v132,v133\n"
"v_min3_u32 v36,v36,v134,v135\n"
"v_min3_u32 v37,v37,v136,v137\n"
"v_min3_u32 v36,v36,v138,v139\n"
"v_min3_u32 v37,v37,v140,v141\n"
"v_min3_u32 v36,v36,v142,v143\n"
"v_min3_u32 v37,v37,v144,v145\n"
"v_min3_u32 v36,v36,v146,v147\n"
"v_min3_u32 v37,v37,v148,v149\n"
"v_min3_u32 v36,v36,v150,v151\n"
"v_min3_u32 v37,v37,v152,v153\n"
"v_min3_u32 v36,v36,v154,v155\n"
"v_min3_u32 v37,v37,v156,v157\n"
"v_min3_u32 v36,v36,v158,v159\n"
"v_min_u32 v36,v36,v37\n"
"v_cmp_eq_u32 vcc,0,v36\n"
"s_nop 0\n"
"s_cbranch_vccz .Lfk_t0\n"
"s_mov_b32 s84,0x40000\n"
"s_mov_b32 s91,s62\n"
"buffer_load_dwordx4 v[128:131],v126,s[24:27],s91 offen sc1\n"
"buffer_load_dwordx4 v[132:135],v117,s[24:27],s91 offen sc1\n"
"s_add_u32 s91,s91,4194304\n"
"buffer_load_dwordx4 v[136:139],v126,s[24:27],s91 offen sc1\n"
"buffer_load_dwordx4 v[140:143],v117,s[24:27],s91 offen sc1\n"
"s_add_u32 s91,s91,4194304\n"
"buffer_load_dwordx4 v[144:147],v126,s[24:27],s91 offen sc1\n"
"buffer_load_dwordx4 v[148:151],v117,s[24:27],s91 offen sc1\n"
"s_add_u32 s91,s91,4194304\n"
"buffer_load_dwordx4 v[152:155],v126,s[24:27],s91 offen sc1\n"
"buffer_load_dwordx4 v[156:159],v117,s[24:27],s91 offen sc1\n"
"s_waitcnt vmcnt(0)\n"
".Lfk_wa:\n"
"v_min3_u32 v36,v128,v129,v130\n"
"v_min3_u32 v37,v131,v132,v133\n"
"v_min3_u32 v36,v36,v134,v135\n"
"v_min3_u32 v37,v37,v136,v137\n"
"v_min3_u32 v36,v36,v138,v139\n"
"v_min3_u32 v37,v37,v140,v141\n"
"v_min3_u32 v36,v36,v142,v143\n"
"v_min3_u32 v37,v37,v144,v145\n"
"v_min3_u32 v36,v36,v146,v147\n"
"v_min3_u32 v37,v37,v148,v149\n"
"v_min3_u32 v36,v36,v150,v151\n"
"v_min3_u32 v37,v37,v152,v153\n"
"v_min3_u32 v36,v36,v154,v155\n"
"v_min3_u32 v37,v37,v156,v157\n"
"v_min3_u32 v36,v36,v158,v159\n"
"v_min_u32 v36,v36,v37\n"
"v_cmp_eq_u32 vcc,0,v36\n"
"s_nop 0\n"
"s_cbranch_vccz .Lfk_wa_x\n"
"s_sleep 2\n"
"s_mov_b64 exec,vcc\n"
"s_mov_b32 s91,s62\n"
"buffer_load_dwordx4 v[128:131],v126,s[24:27],s91 offen sc1\n"
"buffer_load_dwordx4 v[132:135],v117,s[24:27],s91 offen sc1\n"
"s_add_u32 s91,s91,4194304\n"
"buffer_load_dwordx4 v[136:139],v126,s[24:27],s91 offen sc1\n"
"buffer_load_dwordx4 v[140:143],v117,s[24:27],s91 offen sc1\n"
"s_add_u32 s91,s91,4194304\n"
"buffer_load_dwordx4 v[144:147],v126,s[24:27],s91 offen sc1\n"
"buffer_load_dwordx4 v[148:151],v117,s[24:27],s91 offen sc1\n"
"s_add_u32 s91,s91,4194304\n"
"buffer_load_dwordx4 v[152:155],v126,s[24:27],s91 offen sc1\n"
"buffer_load_dwordx4 v[156:159],v117,s[24:27],s91 offen sc1\n"
"s_mov_b64 exec,-1\n"
"s_sub_u32 s84,s84,1\n"
"s_waitcnt vmcnt(0)\n"
"s_cbranch_scc0 .Lfk_wa\n"
".Lfk_wa_x:\n"
".Lfk_t0:\n"
"s_getpc_b64 s[94:95]\n"
".Lfk_rm0:\n"
"s_add_u32 s94,s94,.Lfk_rm1-.Lfk_rm0\n"
"v_mfma_f32_16x16x32_bf16 v[0:3],v[66:69],v[128:131],v[0:3]\n"
"s_addc_u32 s95,s95,0\n"
"v_mfma_f32_16x16x32_bf16 v[4:7],v[64:67],v[128:131],v[4:7]\n"
"v_mfma_f32_16x16x32_bf16 v[8:11],v[66:69],v[132:135],v[8:11]\n"
"v_mfma_f32_16x16x32_bf16 v[12:15],v[64:67],v[132:135],v[12:15]\n"
"v_mfma_f32_16x16x32_bf16 v[0:3],v[66:69],v[136:139],v[0:3]\n"
"v_mfma_f32_16x16x32_bf16 v[4:7],v[64:67],v[136:139],v[4:7]\n"
"v_mfma_f32_16x16x32_bf16 v[8:11],v[66:69],v[140:143],v[8:11]\n"
"v_mfma_f32_16x16x32_bf16 v[12:15],v[64:67],v[140:143],v[12:15]\n"
"v_mfma_f32_16x16x32_bf16 v[0:3],v[66:69],v[144:147],v[0:3]\n"
"v_mfma_f32_16x16x32_bf16 v[4:7],v[64:67],v[144:147],v[4:7]\n"
"v_mfma_f32_16x16x32_bf16 v[8:11],v[66:69],v[148:151],v[8:11]\n"
"v_mfma_f32_16x16x32_bf16 v[12:15],v[64:67],v[148:151],v[12:15]\n"
"v_mfma_f32_16x16x32_bf16 v[0:3],v[66:69],v[152:155],v[0:3]\n"
"v_mfma_f32_16x16x32_bf16 v[4:7],v[64:67],v[152:155],v[4:7]\n"
"v_mfma_f32_16x16x32_bf16 v[8:11],v[66:69],v[156:159],v[8:11]\n"
"v_mfma_f32_16x16x32_bf16 v[12:15],v[64:67],v[156:159],v[12:15]\n"
"s_nop 7\n"
".Lfk_rm:\n"
"v_readfirstlane_b32 s64,v0\n"
"s_nop 1\n"
"v_mov_b32 v38,s64\n"
"v_pk_add_f32 v[16:17],v[0:1],v[38:39] op_sel_hi:[1,0] neg_lo:[0,1] neg_hi:[0,1]\n"
"v_pk_add_f32 v[18:19],v[2:3],v[38:39] op_sel_hi:[1,0] neg_lo:[0,1] neg_hi:[0,1]\n"
"v_pk_add_f32 v[20:21],v[4:5],v[38:39] op_sel_hi:[1,0] neg_lo:[0,1] neg_hi:[0,1]\n"
"v_pk_add_f32 v[22:23],v[6:7],v[38:39] op_sel_hi:[1,0] neg_lo:[0,1] neg_hi:[0,1]\n"
"v_pk_add_f32 v[24:25],v[8:9],v[38:39] op_sel_hi:[1,0] neg_lo:[0,1] neg_hi:[0,1]\n"
"v_pk_add_f32 v[26:27],v[10:11],v[38:39] op_sel_hi:[1,0] neg_lo:[0,1] neg_hi:[0,1]\n"
"v_pk_add_f32 v[28:29],v[12:13],v[38:39] op_sel_hi:[1,0] neg_lo:[0,1] neg_hi:[0,1]\n"
"v_pk_add_f32 v[30:31],v[14:15],v[38:39] op_sel_hi:[1,0] neg_lo:[0,1] neg_hi:[0,1]\n"
"v_pk_add_f32 v[32:33],v[16:17],v[18:19]\n"
"v_pk_mul_f32 v[34:35],v[16:17],v[16:17]\n"
"v_pk_mul_f32 v[36:37],v[16:17],v[224:225]\n"
"v_pk_fma_f32 v[34:35],v[18:19],v[18:19],v[34:35]\n"
"v_pk_fma_f32 v[36:37],v[18:19],v[226:227],v[36:37]\n"
"v_pk_add_f32 v[32:33],v[32:33],v[20:21]\n"
"v_pk_fma_f32 v[34:35],v[20:21],v[20:21],v[34:35]\n"
"v_pk_fma_f32 v[36:37],v[20:21],v[228:229],v[36:37]\n"
"v_pk_add_f32 v[32:33],v[32:33],v[22:23]\n"
"v_pk_fma_f32 v[34:35],v[22:23],v[22:23],v[34:35]\n"
"v_pk_fma_f32 v[36:37],v[22:23],v[230:231],v[36:37]\n"
"v_pk_add_f32 v[32:33],v[32:33],v[24:25]\n"
"v_pk_fma_f32 v[34:35],v[24:25],v[24:25],v[34:35]\n"
"v_pk_fma_f32 v[36:37],v[24:25],v[232:233],v[36:37]\n"
"v_pk_add_f32 v[32:33],v[32:33],v[26:27]\n"
"v_pk_fma_f32 v[34:35],v[26:27],v[26:27],v[34:35]\n"
"v_pk_fma_f32 v[36:37],v[26:27],v[234:235],v[36:37]\n"
"v_pk_add_f32 v[32:33],v[32:33],v[28:29]\n"
"v_pk_fma_f32 v[34:35],v[28:29],v[28:29],v[34:35]\n"
"v_pk_fma_f32 v[36:37],v[28:29],v[236:237],v[36:37]\n"
"v_pk_add_f32 v[32:33],v[32:33],v[30:31]\n"
"v_pk_fma_f32 v[34:35],v[30:31],v[30:31],v[34:35]\n"
"v_pk_fma_f32 v[36:37],v[30:31],v[238:239],v[36:37]\n"
"v_add_f32 v32,v32,v33\n"
"v_add_f32 v33,v34,v35\n"
"v_add_f32 v34,v36,v37\n"
"v_add_f32_dpp v32,v32,v32 quad_perm:[1,0,3,2] row_mask:0xf bank_mask:0xf\n"
"v_add_f32_dpp v33,v33,v33 quad_perm:[1,0,3,2] row_mask:0xf bank_mask:0xf\n"
"v_add_f32_dpp v34,v34,v34 quad_perm:[1,0,3,2] row_mask:0xf bank_mask:0xf\n"
"v_add_f32_dpp v32,v32,v32 quad_perm:[2,3,0,1] row_mask:0xf bank_mask:0xf\n"
"v_add_f32_dpp v33,v33,v33 quad_perm:[2,3,0,1] row_mask:0xf bank_mask:0xf\n"
"v_add_f32_dpp v34,v34,v34 quad_perm:[2,3,0,1] row_mask:0xf bank_mask:0xf\n"
"v_add_f32_dpp v32,v32,v32 row_half_mirror row_mask:0xf bank_mask:0xf\n"
"v_add_f32_dpp v33,v33,v33 row_half_mirror row_mask:0xf bank_mask:0xf\n"
"v_add_f32_dpp v34,v34,v34 row_half_mirror row_mask:0xf bank_mask:0xf\n"
"v_add_f32_dpp v32,v32,v32 row_mirror row_mask:0xf bank_mask:0xf\n"
"v_add_f32_dpp v33,v33,v33 row_mirror row_mask:0xf bank_mask:0xf\n"
"v_add_f32_dpp v34,v34,v34 row_mirror row_mask:0xf bank_mask:0xf\n"
"v_add_f32_dpp v32,v32,v32 row_bcast:15 row_mask:0xa bank_mask:0xf\n"
"v_add_f32_dpp v33,v33,v33 row_bcast:15 row_mask:0xa bank_mask:0xf\n"
"v_add_f32_dpp v34,v34,v34 row_bcast:15 row_mask:0xa bank_mask:0xf\n"
"v_add_f32_dpp v32,v32,v32 row_bcast:31 row_mask:0xc bank_mask:0xf\n"
"v_add_f32_dpp v33,v33,v33 row_bcast:31 row_mask:0xc bank_mask:0xf\n"
"v_add_f32_dpp v34,v34,v34 row_bcast:31 row_mask:0xc bank_mask:0xf\n"
"v_mul_f32 v32,s67,v32\n"
"v_add_f32 v35,v32,v38\n"
"v_mul_f32 v32,v32,v32\n"
"v_cvt_pk_bf16_f32 v35,v35,v35\n"
"v_lshlrev_b32 v35,16,v35\n"
"v_fma_f32 v33,v33,s67,-v32\n"
"v_sub_f32 v36,v38,v35\n"
"v_max_f32 v33,0,v33\n"
"v_fmac_f32 v34,v36,v70\n"
"v_cvt_pk_bf16_f32 v33,v33,v33\n"
"v_lshlrev_b32 v33,16,v33\n"
"v_add_f32 v33,0x3727c5ac,v33\n"
"v_cvt_pk_bf16_f32 v33,v33,v33\n"
"v_lshlrev_b32 v33,16,v33\n"
"v_sqrt_f32 v33,v33\n"
"s_nop 0\n"
"v_cvt_pk_bf16_f32 v33,v33,v33\n"
"v_lshlrev_b32 v33,16,v33\n"
"v_rcp_f32 v36,v33\n"
"v_lshrrev_b32 v33,16,v33\n"
"v_mul_f32 v37,v34,v36\n"
"v_or_b32 v33,v33,v35\n"
"v_cvt_pk_bf16_f32 v37,v37,v37\n"
"v_ashrrev_i32 v36,31,v37\n"
"v_or_b32 v36,0x80000000,v36\n"
"v_xor_b32 v37,v37,v36\n"
"v_and_b32 v37,0xffff0000,v37\n"
"v_readlane_b32 s64,v33,63\n"
"v_readlane_b32 s63,v37,63\n"
"s_nop 1\n"
"s_setpc_b64 s[94:95]\n"
".Lfk_rm1:\n"
"s_add_u32 s91,s62,2097152\n"
"buffer_load_dwordx4 v[160:163],v126,s[24:27],s91 offen sc1\n"
"buffer_load_dwordx4 v[164:167],v117,s[24:27],s91 offen sc1\n"
"s_add_u32 s91,s91,4194304\n"
"buffer_load_dwordx4 v[168:171],v126,s[24:27],s91 offen sc1\n"
"buffer_load_dwordx4 v[172:175],v117,s[24:27],s91 offen sc1\n"
"s_add_u32 s91,s91,4194304\n"
"buffer_load_dwordx4 v[176:179],v126,s[24:27],s91 offen sc1\n"
"buffer_load_dwordx4 v[180:183],v117,s[24:27],s91 offen sc1\n"
"s_add_u32 s91,s91,4194304\n"
"buffer_load_dwordx4 v[184:187],v126,s[24:27],s91 offen sc1\n"
"buffer_load_dwordx4 v[188:191],v117,s[24:27],s91 offen sc1\n"
"s_sub_u32 s65,0xffff,s60\n"
"s_or_b32 s65,s65,s63\n"
"s_mov_b32 s66,s64\n"
"v_lshlrev_b32 v34,16,s64\n"
"v_mov_b32 v35,s64\n"
"v_rcp_f32 v34,v34\n"
"v_and_b32 v35,0xffff0000,v35\n"
"v_cvt_pk_bf16_f32 v240,v0,v1\n"
"v_cvt_pk_bf16_f32 v241,v2,v3\n"
"v_cvt_pk_bf16_f32 v242,v4,v5\n"
"v_cvt_pk_bf16_f32 v243,v6,v7\n"
"v_cvt_pk_bf16_f32 v244,v8,v9\n"
"v_cvt_pk_bf16_f32 v245,v10,v11\n"
"v_cvt_pk_bf16_f32 v246,v12,v13\n"
"v_cvt_pk_bf16_f32 v247,v14,v15\n"
"v_lshlrev_b32 v0,16,v240\n"
"v_and_b32 v1,0xffff0000,v240\n"
"v_lshlrev_b32 v2,16,v241\n"
"v_and_b32 v3,0xffff0000,v241\n"
"v_lshlrev_b32 v4,16,v242\n"
"v_and_b32 v5,0xffff0000,v242\n"
"v_lshlrev_b32 v6,16,v243\n"
"v_and_b32 v7,0xffff0000,v243\n"
"v_lshlrev_b32 v8,16,v244\n"
"v_and_b32 v9,0xffff0000,v244\n"
"v_lshlrev_b32 v10,16,v245\n"
"v_and_b32 v11,0xffff0000,v245\n"
"v_lshlrev_b32 v12,16,v246\n"
"v_and_b32 v13,0xffff0000,v246\n"
"v_lshlrev_b32 v14,16,v247\n"
"v_and_b32 v15,0xffff0000,v247\n"
"v_pk_add_f32 v[16:17],v[0:1],v[34:35] op_sel:[0,1] neg_lo:[0,1] neg_hi:[0,1]\n"
"v_pk_add_f32 v[18:19],v[2:3],v[34:35] op_sel:[0,1] neg_lo:[0,1] neg_hi:[0,1]\n"
"v_pk_add_f32 v[20:21],v[4:5],v[34:35] op_sel:[0,1] neg_lo:[0,1] neg_hi:[0,1]\n"
"v_pk_add_f32 v[22:23],v[6:7],v[34:35] op_sel:[0,1] neg_lo:[0,1] neg_hi:[0,1]\n"
"v_pk_add_f32 v[24:25],v[8:9],v[34:35] op_sel:[0,1] neg_lo:[0,1] neg_hi:[0,1]\n"
"v_pk_add_f32 v[26:27],v[10:11],v[34:35] op_sel:[0,1] neg_lo:[0,1] neg_hi:[0,1]\n"
"v_pk_add_f32 v[28:29],v[12:13],v[34:35] op_sel:[0,1] neg_lo:[0,1] neg_hi:[0,1]\n"
"v_pk_add_f32 v[30:31],v[14:15],v[34:35] op_sel:[0,1] neg_lo:[0,1] neg_hi:[0,1]\n"
"v_cvt_pk_bf16_f32 v240,v16,v17\n"
"v_cvt_pk_bf16_f32 v241,v18,v19\n"
"v_cvt_pk_bf16_f32 v242,v20,v21\n"
"v_cvt_pk_bf16_f32 v243,v22,v23\n"
"v_cvt_pk_bf16_f32 v244,v24,v25\n"
"v_cvt_pk_bf16_f32 v245,v26,v27\n"
"v_cvt_pk_bf16_f32 v246,v28,v29\n"
"v_cvt_pk_bf16_f32 v247,v30,v31\n"
"v_lshlrev_b32 v16,16,v240\n"
"v_and_b32 v17,0xffff0000,v240\n"
"v_lshlrev_b32 v18,16,v241\n"
"v_and_b32 v19,0xffff0000,v241\n"
"v_lshlrev_b32 v20,16,v242\n"
"v_and_b32 v21,0xffff0000,v242\n"
"v_lshlrev_b32 v22,16,v243\n"
"v_and_b32 v23,0xffff0000,v243\n"
"v_lshlrev_b32 v24,16,v244\n"
"v_and_b32 v25,0xffff0000,v244\n"
"v_lshlrev_b32 v26,16,v245\n"
"v_and_b32 v27,0xffff0000,v245\n"
"v_lshlrev_b32 v28,16,v246\n"
"v_and_b32 v29,0xffff0000,v246\n"
"v_lshlrev_b32 v30,16,v247\n"
"v_and_b32 v31,0xffff0000,v247\n"
"v_pk_mul_f32 v[16:17],v[16:17],v[34:35] op_sel_hi:[1,0]\n"
"v_pk_mul_f32 v[18:19],v[18:19],v[34:35] op_sel_hi:[1,0]\n"
"v_pk_mul_f32 v[20:21],v[20:21],v[34:35] op_sel_hi:[1,0]\n"
"v_pk_mul_f32 v[22:23],v[22:23],v[34:35] op_sel_hi:[1,0]\n"
"v_pk_mul_f32 v[24:25],v[24:25],v[34:35] op_sel_hi:[1,0]\n"
"v_pk_mul_f32 v[26:27],v[26:27],v[34:35] op_sel_hi:[1,0]\n"
"v_pk_mul_f32 v[28:29],v[28:29],v[34:35] op_sel_hi:[1,0]\n"
"v_pk_mul_f32 v[30:31],v[30:31],v[34:35] op_sel_hi:[1,0]\n"
"v_cvt_pk_bf16_f32 v240,v16,v17\n"
"v_cvt_pk_bf16_f32 v241,v18,v19\n"
"v_cvt_pk_bf16_f32 v242,v20,v21\n"
"v_cvt_pk_bf16_f32 v243,v22,v23\n"
"v_cvt_pk_bf16_f32 v244,v24,v25\n"
"v_cvt_pk_bf16_f32 v245,v26,v27\n"
"v_cvt_pk_bf16_f32 v246,v28,v29\n"
"v_cvt_pk_bf16_f32 v247,v30,v31\n"
"v_max_u32 v216,1,v240\n"
"v_max_u32 v217,1,v241\n"
"v_max_u32 v218,1,v242\n"
"v_max_u32 v219,1,v243\n"
"v_max_u32 v220,1,v244\n"
"v_max_u32 v221,1,v245\n"
"v_max_u32 v222,1,v246\n"
"v_max_u32 v223,1,v247\n"
"v_lshlrev_b32 v16,16,v240\n"
"v_and_b32 v17,0xffff0000,v240\n"
"v_lshlrev_b32 v18,16,v241\n"
"v_and_b32 v19,0xffff0000,v241\n"
"v_lshlrev_b32 v20,16,v242\n"
"v_and_b32 v21,0xffff0000,v242\n"
"v_lshlrev_b32 v22,16,v243\n"
"v_and_b32 v23,0xffff0000,v243\n"
"v_lshlrev_b32 v24,16,v244\n"
"v_and_b32 v25,0xffff0000,v244\n"
"v_lshlrev_b32 v26,16,v245\n"
"v_and_b32 v27,0xffff0000,v245\n"
"v_lshlrev_b32 v28,16,v246\n"
"v_and_b32 v29,0xffff0000,v246\n"
"v_lshlrev_b32 v30,16,v247\n"
"v_and_b32 v31,0xffff0000,v247\n"
"v_pk_add_f32 v[16:17],v[16:17],v[0:1]\n"
"v_pk_add_f32 v[18:19],v[18:19],v[2:3]\n"
"v_pk_add_f32 v[20:21],v[20:21],v[4:5]\n"
"v_pk_add_f32 v[22:23],v[22:23],v[6:7]\n"
"v_pk_add_f32 v[24:25],v[24:25],v[8:9]\n"
"v_pk_add_f32 v[26:27],v[26:27],v[10:11]\n"
"v_pk_add_f32 v[28:29],v[28:29],v[12:13]\n"
"v_pk_add_f32 v[30:31],v[30:31],v[14:15]\n"
"v_cvt_pk_bf16_f32 v240,v16,v17\n"
"v_cvt_pk_bf16_f32 v241,v18,v19\n"
"v_cvt_pk_bf16_f32 v242,v20,v21\n"
"v_cvt_pk_bf16_f32 v243,v22,v23\n"
"v_cvt_pk_bf16_f32 v244,v24,v25\n"
"v_cvt_pk_bf16_f32 v245,v26,v27\n"
"v_cvt_pk_bf16_f32 v246,v28,v29\n"
"v_cvt_pk_bf16_f32 v247,v30,v31\n"
"buffer_store_dwordx4 v[240:243],v112,s[48:51],s61 offen offset:0 sc1\n"
"buffer_store_dwordx4 v[244:247],v112,s[48:51],s61 offen offset:1024 sc1\n"
"s_barrier\n"
"s_lshl_b32 s82,s52,13\n"
"s_add_u32 s82,s82,0\n"
"v_bfe_u32 v41,v112,5,2\n"
"v_lshlrev_b32 v41,13,v41\n"
"v_bfe_u32 v42,v112,4,1\n"
"v_lshl_add_u32 v41,v42,3,v41\n"
"s_lshl_b32 s83,s100,4\n"
"v_mfma_f32_16x16x32_bf16 v[200:203],v[66:69],a[104:107],0\n"
"s_add_u32 s83,s83,149946368\n"
"v_mov_b32 v45,s66\n"
"v_add_u32 v41,0,v41\n"
"v_mfma_f32_16x16x32_bf16 v[204:207],v[64:67],a[104:107],0\n"
"v_mov_b32 v40,s82\n"
"v_mov_b32 v51,s80\n"
"v_mfma_f32_16x16x32_bf16 v[208:211],v[66:69],a[108:111],0\n"
"v_mov_b32 v44,s65\n"
"v_lshlrev_b32 v43,8,v112\n"
"v_mfma_f32_16x16x32_bf16 v[212:215],v[64:67],a[108:111],0\n"
"s_mov_b32 s84,0x40000\n"
"v_mov_b32 v49,s80\n"
"s_waitcnt vmcnt(2)\n"
".Lfk_w1:\n"
"v_mfma_f32_16x16x32_bf16 v[0:3],v[66:69],v[160:163],v[200:203]\n"
"v_min3_u32 v36,v160,v161,v162\n"
"v_min3_u32 v37,v163,v164,v165\n"
"v_mfma_f32_16x16x32_bf16 v[4:7],v[64:67],v[160:163],v[204:207]\n"
"v_min3_u32 v36,v36,v166,v167\n"
"v_min3_u32 v37,v37,v168,v169\n"
"v_mfma_f32_16x16x32_bf16 v[8:11],v[66:69],v[164:167],v[208:211]\n"
"v_min3_u32 v36,v36,v170,v171\n"
"v_min3_u32 v37,v37,v172,v173\n"
"v_mfma_f32_16x16x32_bf16 v[12:15],v[64:67],v[164:167],v[212:215]\n"
"v_min3_u32 v36,v36,v174,v175\n"
"v_min3_u32 v37,v37,v176,v177\n"
"v_mfma_f32_16x16x32_bf16 v[0:3],v[66:69],v[168:171],v[0:3]\n"
"v_min3_u32 v36,v36,v178,v179\n"
"v_min3_u32 v37,v37,v180,v181\n"
"v_mfma_f32_16x16x32_bf16 v[4:7],v[64:67],v[168:171],v[4:7]\n"
"v_min3_u32 v36,v36,v182,v183\n"
"v_min3_u32 v37,v37,v184,v185\n"
"v_mfma_f32_16x16x32_bf16 v[8:11],v[66:69],v[172:175],v[8:11]\n"
"v_min3_u32 v36,v36,v186,v187\n"
"v_min3_u32 v37,v37,v188,v189\n"
"v_mfma_f32_16x16x32_bf16 v[12:15],v[64:67],v[172:175],v[12:15]\n"
"v_min3_u32 v36,v36,v190,v191\n"
"v_min_u32 v36,v36,v37\n"
"v_cmp_eq_u32 vcc,0,v36\n"
"v_mfma_f32_16x16x32_bf16 v[0:3],v[66:69],v[176:179],v[0:3]\n"
"s_cbranch_vccz .Lfk_w1_x\n"
"s_mov_b64 exec,vcc\n"
"s_add_u32 s91,s62,2097152\n"
"buffer_load_dwordx4 v[160:163],v126,s[24:27],s91 offen sc1\n"
"buffer_load_dwordx4 v[164:167],v117,s[24:27],s91 offen sc1\n"
"s_add_u32 s91,s91,4194304\n"
"buffer_load_dwordx4 v[168:171],v126,s[24:27],s91 offen sc1\n"
"buffer_load_dwordx4 v[172:175],v117,s[24:27],s91 offen sc1\n"
"s_add_u32 s91,s91,4194304\n"
"buffer_load_dwordx4 v[176:179],v126,s[24:27],s91 offen sc1\n"
"buffer_load_dwordx4 v[180:183],v117,s[24:27],s91 offen sc1\n"
"s_add_u32 s91,s91,4194304\n"
"buffer_load_dwordx4 v[184:187],v126,s[24:27],s91 offen sc1\n"
"buffer_load_dwordx4 v[188:191],v117,s[24:27],s91 offen sc1\n"
"s_mov_b64 exec,-1\n"
"s_sub_u32 s84,s84,1\n"
"s_waitcnt vmcnt(0)\n"
"s_cbranch_scc0 .Lfk_w1\n"
".Lfk_w1_x:\n"
"v_mfma_f32_16x16x32_bf16 v[4:7],v[64:67],v[176:179],v[4:7]\n"
"v_mfma_f32_16x16x32_bf16 v[8:11],v[66:69],v[180:183],v[8:11]\n"
"v_mfma_f32_16x16x32_bf16 v[12:15],v[64:67],v[180:183],v[12:15]\n"
"v_mfma_f32_16x16x32_bf16 v[0:3],v[66:69],v[184:187],v[0:3]\n"
"v_mfma_f32_16x16x32_bf16 v[4:7],v[64:67],v[184:187],v[4:7]\n"
"v_mfma_f32_16x16x32_bf16 v[8:11],v[66:69],v[188:191],v[8:11]\n"
"v_mfma_f32_16x16x32_bf16 v[12:15],v[64:67],v[188:191],v[12:15]\n"
"s_nop 6\n"
"s_call_b64 s[94:95],.Lfk_rm\n"
"s_sub_u32 s82,64511,s60\n"
"s_or_b32 s63,s63,s82\n"
"v_mov_b32 v46,s63\n"
"v_mov_b32 v47,s64\n"
"v_mov_b32 v39,0\n"
"ds_write_b128 v40,v[44:47]\n"
"s_waitcnt vmcnt(0) lgkmcnt(0)\n"
"s_barrier\n"
"ds_read_b64 v[52:53],v41\n"
"s_waitcnt lgkmcnt(0)\n"
"v_mov_b32 v48,v52\n"
"s_nop 1\n"
"v_min_u32_dpp v54,v48,v48 quad_perm:[1,0,3,2] row_mask:0xf bank_mask:0xf\n"
"v_max_u32_dpp v48,v48,v48 quad_perm:[1,0,3,2] row_mask:0xf bank_mask:0xf\n"
"v_max_u32_dpp v39,v39,v39 quad_perm:[1,0,3,2] row_mask:0xf bank_mask:0xf\n"
"v_max_u32 v39,v39,v54\n"
"v_min_u32_dpp v54,v48,v48 quad_perm:[2,3,0,1] row_mask:0xf bank_mask:0xf\n"
"v_max_u32_dpp v48,v48,v48 quad_perm:[2,3,0,1] row_mask:0xf bank_mask:0xf\n"
"v_max_u32_dpp v39,v39,v39 quad_perm:[2,3,0,1] row_mask:0xf bank_mask:0xf\n"
"v_max_u32 v39,v39,v54\n"
"v_min_u32_dpp v54,v48,v48 row_half_mirror row_mask:0xf bank_mask:0xf\n"
"v_max_u32_dpp v48,v48,v48 row_half_mirror row_mask:0xf bank_mask:0xf\n"
"v_max_u32_dpp v39,v39,v39 row_half_mirror row_mask:0xf bank_mask:0xf\n"
"v_max_u32 v39,v39,v54\n"
"v_xor_b32 v50,0x800,v39\n"
"s_cmp_lg_u32 s52,0\n"
"s_cbranch_scc1 .Lfk_t1\n"
"s_mov_b64 exec,0xff\n"
"buffer_store_dwordx4 v[48:51],v43,s[24:27],s83 offen sc1\n"
"s_mov_b64 exec,-1\n"
"s_add_u32 s83,s83,0x40000\n"
"v_cmp_eq_u32 vcc,v48,v52\n"
"s_nop 0\n"
"s_ff1_i32_b64 s82,vcc\n"
"v_readlane_b32 s90,v53,s82\n"
"s_nop 1\n"
"v_mov_b32 v44,s90\n"
"v_mov_b32 v45,s80\n"
"v_cmp_eq_u32 vcc,v39,v52\n"
"s_nop 0\n"
"s_ff1_i32_b64 s82,vcc\n"
"v_readlane_b32 s90,v53,s82\n"
"s_nop 1\n"
"v_mov_b32 v46,s90\n"
"v_mov_b32 v47,s80\n"
"s_mov_b64 exec,0x100\n"
"buffer_store_dwordx4 v[44:47],v43,s[24:27],s83 offen sc1\n"
"s_mov_b64 exec,-1\n"
".Lfk_t1:\n"
"v_readfirstlane_b32 s89,v39\n"
"s_add_u32 m0,s30,131072\n"
"s_nop 0\n"
"buffer_load_dwordx4 v96,s[40:43],0 offen offset:0 nt lds\n"
"buffer_load_dwordx4 v96,s[40:43],0 offen offset:1024 nt lds\n"
"buffer_load_dwordx4 v97,s[40:43],0 offen offset:2048 nt lds\n"
"buffer_load_dwordx4 v97,s[40:43],0 offen offset:3072 nt lds\n"
"v_lshlrev_b32 v34,16,s64\n"
"v_mov_b32 v35,s64\n"
"v_rcp_f32 v34,v34\n"
"v_and_b32 v35,0xffff0000,v35\n"
"v_cvt_pk_bf16_f32 v248,v0,v1\n"
"v_cvt_pk_bf16_f32 v249,v2,v3\n"
"v_cvt_pk_bf16_f32 v250,v4,v5\n"
"v_cvt_pk_bf16_f32 v251,v6,v7\n"
"v_cvt_pk_bf16_f32 v252,v8,v9\n"
"v_cvt_pk_bf16_f32 v253,v10,v11\n"
"v_cvt_pk_bf16_f32 v254,v12,v13\n"
"v_cvt_pk_bf16_f32 v255,v14,v15\n"
"v_lshlrev_b32 v0,16,v248\n"
"v_and_b32 v1,0xffff0000,v248\n"
"v_lshlrev_b32 v2,16,v249\n"
"v_and_b32 v3,0xffff0000,v249\n"
"v_lshlrev_b32 v4,16,v250\n"
"v_and_b32 v5,0xffff0000,v250\n"
"v_lshlrev_b32 v6,16,v251\n"
"v_and_b32 v7,0xffff0000,v251\n"
"v_lshlrev_b32 v8,16,v252\n"
"v_and_b32 v9,0xffff0000,v252\n"
"v_lshlrev_b32 v10,16,v253\n"
"v_and_b32 v11,0xffff0000,v253\n"
"v_lshlrev_b32 v12,16,v254\n"
"v_and_b32 v13,0xffff0000,v254\n"
"v_lshlrev_b32 v14,16,v255\n"
"v_and_b32 v15,0xffff0000,v255\n"
"v_pk_add_f32 v[16:17],v[0:1],v[34:35] op_sel:[0,1] neg_lo:[0,1] neg_hi:[0,1]\n"
"v_pk_add_f32 v[18:19],v[2:3],v[34:35] op_sel:[0,1] neg_lo:[0,1] neg_hi:[0,1]\n"
"v_pk_add_f32 v[20:21],v[4:5],v[34:35] op_sel:[0,1] neg_lo:[0,1] neg_hi:[0,1]\n"
"v_pk_add_f32 v[22:23],v[6:7],v[34:35] op_sel:[0,1] neg_lo:[0,1] neg_hi:[0,1]\n"
"v_pk_add_f32 v[24:25],v[8:9],v[34:35] op_sel:[0,1] neg_lo:[0,1] neg_hi:[0,1]\n"
"v_pk_add_f32 v[26:27],v[10:11],v[34:35] op_sel:[0,1] neg_lo:[0,1] neg_hi:[0,1]\n"
"v_pk_add_f32 v[28:29],v[12:13],v[34:35] op_sel:[0,1] neg_lo:[0,1] neg_hi:[0,1]\n"
"v_pk_add_f32 v[30:31],v[14:15],v[34:35] op_sel:[0,1] neg_lo:[0,1] neg_hi:[0,1]\n"
"v_cvt_pk_bf16_f32 v248,v16,v17\n"
"v_cvt_pk_bf16_f32 v249,v18,v19\n"
"v_cvt_pk_bf16_f32 v250,v20,v21\n"
"v_cvt_pk_bf16_f32 v251,v22,v23\n"
"v_cvt_pk_bf16_f32 v252,v24,v25\n"
"v_cvt_pk_bf16_f32 v253,v26,v27\n"
"v_cvt_pk_bf16_f32 v254,v28,v29\n"
"v_cvt_pk_bf16_f32 v255,v30,v31\n"
"v_lshlrev_b32 v16,16,v248\n"
"v_and_b32 v17,0xffff0000,v248\n"
"v_lshlrev_b32 v18,16,v249\n"
"v_and_b32 v19,0xffff0000,v249\n"
"v_lshlrev_b32 v20,16,v250\n"
"v_and_b32 v21,0xffff0000,v250\n"
"v_lshlrev_b32 v22,16,v251\n"
"v_and_b32 v23,0xffff0000,v251\n"
"v_lshlrev_b32 v24,16,v252\n"
"v_and_b32 v25,0xffff0000,v252\n"
"v_lshlrev_b32 v26,16,v253\n"
"v_and_b32 v27,0xffff0000,v253\n"
"v_lshlrev_b32 v28,16,v254\n"
"v_and_b32 v29,0xffff0000,v254\n"
"v_lshlrev_b32 v30,16,v255\n"
"v_and_b32 v31,0xffff0000,v255\n"
"v_pk_mul_f32 v[16:17],v[16:17],v[34:35] op_sel_hi:[1,0]\n"
"v_pk_mul_f32 v[18:19],v[18:19],v[34:35] op_sel_hi:[1,0]\n"
"v_pk_mul_f32 v[20:21],v[20:21],v[34:35] op_sel_hi:[1,0]\n"
"v_pk_mul_f32 v[22:23],v[22:23],v[34:35] op_sel_hi:[1,0]\n"
"v_pk_mul_f32 v[24:25],v[24:25],v[34:35] op_sel_hi:[1,0]\n"
"v_pk_mul_f32 v[26:27],v[26:27],v[34:35] op_sel_hi:[1,0]\n"
"v_pk_mul_f32 v[28:29],v[28:29],v[34:35] op_sel_hi:[1,0]\n"
"v_pk_mul_f32 v[30:31],v[30:31],v[34:35] op_sel_hi:[1,0]\n"
"v_cvt_pk_bf16_f32 v248,v16,v17\n"
"v_cvt_pk_bf16_f32 v249,v18,v19\n"
"v_cvt_pk_bf16_f32 v250,v20,v21\n"
"v_cvt_pk_bf16_f32 v251,v22,v23\n"
"v_cvt_pk_bf16_f32 v252,v24,v25\n"
"v_cvt_pk_bf16_f32 v253,v26,v27\n"
"v_cvt_pk_bf16_f32 v254,v28,v29\n"
"v_cvt_pk_bf16_f32 v255,v30,v31\n"
"v_max_u32 v208,1,v248\n"
"v_max_u32 v209,1,v249\n"
"v_max_u32 v210,1,v250\n"
"v_max_u32 v211,1,v251\n"
"v_max_u32 v212,1,v252\n"
"v_max_u32 v213,1,v253\n"
"v_max_u32 v214,1,v254\n"
"v_max_u32 v215,1,v255\n"
"s_and_b32 s82,s60,255\n"
"s_lshl_b32 s82,s82,12\n"
"s_cmp_eq_u32 s63,s89\n"
"s_cselect_b32 s83,2048,0\n"
"s_add_u32 s82,s82,s83\n"
"s_add_u32 s82,s82,174063616\n"
"s_cmp_ge_u32 s63,s89\n"
"s_cselect_b32 s82,s82,2146435072\n"
"buffer_store_dwordx4 v[208:211],v112,s[24:27],s82 offen offset:0 sc1\n"
"buffer_store_dwordx4 v[212:215],v112,s[24:27],s82 offen offset:1024 sc1\n"
"s_and_b32 s82,s60,255\n"
"s_lshl_b32 s82,s82,12\n"
"s_cmp_eq_u32 s65,s89\n"
"s_cselect_b32 s83,2048,0\n"
"s_add_u32 s82,s82,s83\n"
"s_add_u32 s82,s82,174063616\n"
"s_cmp_ge_u32 s65,s89\n"
"s_cselect_b32 s82,s82,2146435072\n"
"buffer_store_dwordx4 v[216:219],v112,s[24:27],s82 offen offset:0 sc1\n"
"buffer_store_dwordx4 v[220:223],v112,s[24:27],s82 offen offset:1024 sc1\n"
"v_lshlrev_b32 v16,16,v248\n"
"v_and_b32 v17,0xffff0000,v248\n"
"v_lshlrev_b32 v18,16,v249\n"
"v_and_b32 v19,0xffff0000,v249\n"
"v_lshlrev_b32 v20,16,v250\n"
"v_and_b32 v21,0xffff0000,v250\n"
"v_lshlrev_b32 v22,16,v251\n"
"v_and_b32 v23,0xffff0000,v251\n"
"v_lshlrev_b32 v24,16,v252\n"
"v_and_b32 v25,0xffff0000,v252\n"
"v_lshlrev_b32 v26,16,v253\n"
"v_and_b32 v27,0xffff0000,v253\n"
"v_lshlrev_b32 v28,16,v254\n"
"v_and_b32 v29,0xffff0000,v254\n"
"v_lshlrev_b32 v30,16,v255\n"
"v_and_b32 v31,0xffff0000,v255\n"
"v_pk_add_f32 v[16:17],v[16:17],v[0:1]\n"
"v_pk_add_f32 v[18:19],v[18:19],v[2:3]\n"
"v_pk_add_f32 v[20:21],v[20:21],v[4:5]\n"
"v_pk_add_f32 v[22:23],v[22:23],v[6:7]\n"
"v_pk_add_f32 v[24:25],v[24:25],v[8:9]\n"
"v_pk_add_f32 v[26:27],v[26:27],v[10:11]\n"
"v_pk_add_f32 v[28:29],v[28:29],v[12:13]\n"
"v_pk_add_f32 v[30:31],v[30:31],v[14:15]\n"
"v_cvt_pk_bf16_f32 v248,v16,v17\n"
"v_cvt_pk_bf16_f32 v249,v18,v19\n"
"v_cvt_pk_bf16_f32 v250,v20,v21\n"
"v_cvt_pk_bf16_f32 v251,v22,v23\n"
"v_cvt_pk_bf16_f32 v252,v24,v25\n"
"v_cvt_pk_bf16_f32 v253,v26,v27\n"
"v_cvt_pk_bf16_f32 v254,v28,v29\n"
"v_cvt_pk_bf16_f32 v255,v30,v31\n"
"s_add_u32 s68,s61,2097152\n"
"s_cmp_lt_u32 s63,s89\n"
"s_cselect_b32 s83,s68,2146435072\n"
"s_cselect_b32 s68,2146435072,s68\n"
"buffer_store_dwordx4 v[248:251],v112,s[48:51],s83 offen offset:0 sc1\n"
"buffer_store_dwordx4 v[252:255],v112,s[48:51],s83 offen offset:1024 sc1\n"
"v_lshrrev_b32 v40,6,v112\n"
"v_and_b32 v41,7,v40\n"
"v_lshlrev_b32 v41,5,v41\n"
"v_bfe_u32 v42,v112,4,2\n"
"v_lshlrev_b32 v42,3,v42\n"
"v_lshl_add_u32 v40,v40,11,v42\n"
"s_lshl_b32 s82,s52,9\n"
"v_add_u32 v40,s82,v40\n"
"v_add_u32 v40,131072,v40\n"
"v_xor_b32 v42,0,v41\n"
"v_add_u32 v192,v40,v42\n"
"v_xor_b32 v42,32,v41\n"
"v_add_u32 v193,v40,v42\n"
"v_xor_b32 v42,64,v41\n"
"v_add_u32 v194,v40,v42\n"
"v_xor_b32 v42,96,v41\n"
"v_add_u32 v195,v40,v42\n"
"v_xor_b32 v42,128,v41\n"
"v_add_u32 v196,v40,v42\n"
"v_xor_b32 v42,160,v41\n"
"v_add_u32 v197,v40,v42\n"
"v_xor_b32 v42,192,v41\n"
"v_add_u32 v198,v40,v42\n"
"v_xor_b32 v42,224,v41\n"
"v_add_u32 v199,v40,v42\n"
"v_bfe_u32 v56,v112,4,1\n"
"v_lshrrev_b32 v57,1,v112\n"
"v_and_b32 v57,0x1f0,v57\n"
"s_lshl_b32 s83,s52,13\n"
"s_add_u32 s83,s83,1024\n"
"v_lshl_add_u32 v58,v56,9,s83\n"
"v_lshrrev_b32 v59,8,v112\n"
"v_lshl_add_u32 v59,v59,4,s82\n"
"v_add_u32 v59,174063616,v59\n"
"v_add_u32 v58,v58,v57\n"
"v_add_u32 v57,s82,v57\n"
"v_and_b32 v55,0x7c0,v57\n"
"v_and_b32 v54,0x30,v57\n"
"v_lshl_add_u32 v55,v55,4,v54\n"
"v_lshrrev_b32 v54,2,v55\n"
"v_and_b32 v54,0x0,v54\n"
"v_or_b32 v55,v55,v54\n"
"v_add_u32 v60,s83,v112\n"
"v_add_u32 v61,1024,v112\n"
"v_bfe_u32 v62,v112,5,3\n"
"v_cmp_eq_u32 s[8:9],0,v62\n"
"v_cmp_eq_u32 s[10:11],1,v62\n"
"v_cmp_eq_u32 s[12:13],2,v62\n"
"v_cmp_eq_u32 s[14:15],3,v62\n"
"v_cmp_eq_u32 s[16:17],4,v62\n"
"v_cmp_eq_u32 s[18:19],5,v62\n"
"v_cmp_eq_u32 s[20:21],6,v62\n"
"v_cmp_eq_u32 s[22:23],7,v62\n"
"v_lshlrev_b32 v62,6,v62\n"
"v_lshl_add_u32 v62,v56,12,v62\n"
"v_lshrrev_b32 v63,8,v112\n"
"v_lshl_add_u32 v62,v63,4,v62\n"
"s_lshl_b32 s69,s72,13\n"
"s_lshl_b32 s82,s52,10\n"
"s_add_u32 s69,s69,s82\n"
"s_add_u32 s69,s69,171966464\n"
"v_lshrrev_b32 v63,5,v112\n"
"v_min_u32 v63,27,v63\n"
"v_lshlrev_b32 v63,13,v63\n"
"v_lshl_add_u32 v63,v56,4,v63\n"
"s_mul_i32 s70,s52,458752\n"
"s_lshl_b32 s82,s73,5\n"
"s_add_u32 s70,s70,s82\n"
"s_add_u32 s70,s70,171966464\n"
"s_add_u32 s71,s70,229376\n"
"s_and_b32 s83,s100,7\n"
"s_lshl_b32 s83,s83,12\n"
"s_add_u32 s83,s83,149946368\n"
"v_add_u32 v20,0,v112\n"
"v_lshlrev_b32 v20,0,v20\n"
"v_add_u32 v21,1024,v112\n"
"v_lshlrev_b32 v21,0,v21\n"
"v_add_u32 v22,2048,v112\n"
"v_lshlrev_b32 v22,0,v22\n"
"v_add_u32 v23,3072,v112\n"
"v_lshlrev_b32 v23,0,v23\n"
"s_sleep 20\n"
".Lfk_nn:\n"
"s_mov_b32 s84,0x40000\n"
"buffer_load_dwordx4 v[0:3],v20,s[24:27],s83 offen sc1\n"
"buffer_load_dwordx4 v[4:7],v21,s[24:27],s83 offen sc1\n"
"buffer_load_dwordx4 v[8:11],v22,s[24:27],s83 offen sc1\n"
"buffer_load_dwordx4 v[12:15],v23,s[24:27],s83 offen sc1\n"
"s_waitcnt vmcnt(0)\n"
".Lfk_wc:\n"
"v_xor_b32 v24,s80,v1\n"
"v_xor_b32 v25,s80,v3\n"
"v_xor_b32 v26,s80,v5\n"
"v_xor_b32 v27,s80,v7\n"
"v_xor_b32 v28,s80,v9\n"
"v_xor_b32 v29,s80,v11\n"
"v_xor_b32 v30,s80,v13\n"
"v_xor_b32 v31,s80,v15\n"
"v_or3_b32 v16,v24,v25,v26\n"
"v_or3_b32 v17,v27,v28,v29\n"
"v_or3_b32 v16,v16,v30,v31\n"
"v_or_b32 v16,v16,v17\n"
"v_cmp_ne_u32 vcc,0,v16\n"
"s_nop 0\n"
"s_cbranch_vccz .Lfk_wc_x\n"
"s_sleep 2\n"
"s_mov_b64 exec,vcc\n"
"buffer_load_dwordx4 v[0:3],v20,s[24:27],s83 offen sc1\n"
"buffer_load_dwordx4 v[4:7],v21,s[24:27],s83 offen sc1\n"
"buffer_load_dwordx4 v[8:11],v22,s[24:27],s83 offen sc1\n"
"buffer_load_dwordx4 v[12:15],v23,s[24:27],s83 offen sc1\n"
"s_mov_b64 exec,-1\n"
"s_sub_u32 s84,s84,1\n"
"s_waitcnt vmcnt(0)\n"
"s_cbranch_scc0 .Lfk_wc\n"
".Lfk_wc_x:\n"
"v_min_u32 v16,v0,v4\n"
"v_max_u32 v0,v0,v4\n"
"v_max3_u32 v2,v2,v6,v16\n"
"v_min_u32 v16,v8,v12\n"
"v_max_u32 v8,v8,v12\n"
"v_max3_u32 v10,v10,v14,v16\n"
"v_min_u32 v16,v0,v8\n"
"v_max_u32 v0,v0,v8\n"
"v_max3_u32 v2,v2,v10,v16\n"
"v_cmp_eq_u32 vcc,0,v56\n"
"v_min_u32_dpp v16,v0,v0 quad_perm:[1,0,3,2] row_mask:0xf bank_mask:0xf\n"
"v_max_u32_dpp v0,v0,v0 quad_perm:[1,0,3,2] row_mask:0xf bank_mask:0xf\n"
"v_max_u32_dpp v2,v2,v2 quad_perm:[1,0,3,2] row_mask:0xf bank_mask:0xf\n"
"v_max_u32 v2,v2,v16\n"
"v_min_u32_dpp v16,v0,v0 quad_perm:[2,3,0,1] row_mask:0xf bank_mask:0xf\n"
"v_max_u32_dpp v0,v0,v0 quad_perm:[2,3,0,1] row_mask:0xf bank_mask:0xf\n"
"v_max_u32_dpp v2,v2,v2 quad_perm:[2,3,0,1] row_mask:0xf bank_mask:0xf\n"
"v_max_u32 v2,v2,v16\n"
"v_min_u32_dpp v16,v0,v0 row_half_mirror row_mask:0xf bank_mask:0xf\n"
"v_max_u32_dpp v0,v0,v0 row_half_mirror row_mask:0xf bank_mask:0xf\n"
"v_max_u32_dpp v2,v2,v2 row_half_mirror row_mask:0xf bank_mask:0xf\n"
"v_max_u32 v2,v2,v16\n"
"v_min_u32_dpp v16,v0,v0 row_mirror row_mask:0xf bank_mask:0xf\n"
"v_max_u32_dpp v0,v0,v0 row_mirror row_mask:0xf bank_mask:0xf\n"
"v_max_u32_dpp v2,v2,v2 row_mirror row_mask:0xf bank_mask:0xf\n"
"v_max_u32 v2,v2,v16\n"
"v_min_u32_dpp v16,v0,v0 row_bcast:15 row_mask:0xa bank_mask:0xf\n"
"v_max_u32_dpp v0,v0,v0 row_bcast:15 row_mask:0xa bank_mask:0xf\n"
"v_max_u32_dpp v2,v2,v2 row_bcast:15 row_mask:0xa bank_mask:0xf\n"
"v_max_u32 v2,v2,v16\n"
"v_min_u32_dpp v16,v0,v0 row_bcast:31 row_mask:0xc bank_mask:0xf\n"
"v_max_u32_dpp v0,v0,v0 row_bcast:31 row_mask:0xc bank_mask:0xf\n"
"v_max_u32_dpp v2,v2,v2 row_bcast:31 row_mask:0xc bank_mask:0xf\n"
"v_max_u32 v2,v2,v16\n"
"v_readlane_b32 s90,v0,63\n"
"v_readlane_b32 s92,v2,63\n"
"s_nop 0\n"
"v_mov_b32 v4,s90\n"
"v_mov_b32 v5,s92\n"
"v_cndmask_b32 v6,v5,v4,vcc\n"
"v_not_b32 v7,v6\n"
"v_bfe_u32 v9,v7,0,8\n"
"v_and_b32 v11,0x800,v7\n"
"v_lshl_add_u32 v11,v9,12,v11\n"
"v_add_u32 v11,v11,v59\n"
"buffer_load_dwordx4 v[128:131],v11,s[24:27],0 offen offset:0 sc1\n"
"buffer_load_dwordx4 v[132:135],v11,s[24:27],0 offen offset:64 sc1\n"
"buffer_load_dwordx4 v[136:139],v11,s[24:27],0 offen offset:128 sc1\n"
"buffer_load_dwordx4 v[140:143],v11,s[24:27],0 offen offset:192 sc1\n"
"buffer_load_dwordx4 v[144:147],v11,s[24:27],0 offen offset:256 sc1\n"
"buffer_load_dwordx4 v[148:151],v11,s[24:27],0 offen offset:320 sc1\n"
"buffer_load_dwordx4 v[152:155],v11,s[24:27],0 offen offset:384 sc1\n"
"buffer_load_dwordx4 v[156:159],v11,s[24:27],0 offen offset:448 sc1\n"
"v_and_b32 v7,0x7ff,v7\n"
"v_lshl_add_u32 v8,v7,11,v57\n"
"v_and_b32 v9,15,v7\n"
"v_lshlrev_b32 v9,6,v9\n"
"v_xor_b32 v9,v9,v55\n"
"v_and_b32 v10,0x7f0,v7\n"
"v_lshl_add_u32 v10,v10,11,v9\n"
"s_mov_b32 s82,71303168\n"
"buffer_load_dwordx4 a[0:3],v10,s[24:27],s82 offen sc1\n"
"s_add_u32 s82,s82,4194304\n"
"buffer_load_dwordx4 a[4:7],v10,s[24:27],s82 offen sc1\n"
"s_add_u32 s82,s82,4194304\n"
"buffer_load_dwordx4 a[8:11],v10,s[24:27],s82 offen sc1\n"
"s_add_u32 s82,s82,4194304\n"
"buffer_load_dwordx4 a[12:15],v10,s[24:27],s82 offen sc1\n"
"buffer_load_dwordx4 a[16:19],v8,s[36:39],0 offen\n"
"v_bfe_u32 v9,v7,0,8\n"
"v_bfe_u32 v12,v9,1,3\n"
"v_and_b32 v13,1,v9\n"
"v_and_b32 v9,0xf0,v9\n"
"v_lshl_add_u32 v9,v13,3,v9\n"
"v_add_u32 v9,v12,v9\n"
"v_lshlrev_b32 v9,4,v9\n"
"s_mov_b32 s82,150241280\n"
"buffer_load_dwordx4 v[16:19],v9,s[24:27],s82 offen sc1\n"
"s_barrier\n"
"ds_read_b64_tr_b16 a[224:225],v192 offset:0\n"
"ds_read_b64_tr_b16 a[240:241],v192 offset:256\n"
"ds_read_b64_tr_b16 a[226:227],v193 offset:0\n"
"ds_read_b64_tr_b16 a[242:243],v193 offset:256\n"
"ds_read_b64_tr_b16 a[228:229],v194 offset:0\n"
"ds_read_b64_tr_b16 a[244:245],v194 offset:256\n"
"ds_read_b64_tr_b16 a[230:231],v195 offset:0\n"
"ds_read_b64_tr_b16 a[246:247],v195 offset:256\n"
"ds_read_b64_tr_b16 a[232:233],v196 offset:0\n"
"ds_read_b64_tr_b16 a[248:249],v196 offset:256\n"
"ds_read_b64_tr_b16 a[234:235],v197 offset:0\n"
"ds_read_b64_tr_b16 a[250:251],v197 offset:256\n"
"ds_read_b64_tr_b16 a[236:237],v198 offset:0\n"
"ds_read_b64_tr_b16 a[252:253],v198 offset:256\n"
"ds_read_b64_tr_b16 a[238:239],v199 offset:0\n"
"ds_read_b64_tr_b16 a[254:255],v199 offset:256\n"
"s_mov_b32 s84,0x40000\n"
"s_waitcnt vmcnt(6)\n"
".Lfk_wh:\n"
"v_mfma_f32_16x16x32_bf16 v[40:43],a[160:163],v[128:131],0\n"
"v_min3_u32 v36,v128,v129,v130\n"
"v_min3_u32 v37,v131,v132,v133\n"
"v_mfma_f32_16x16x32_bf16 v[40:43],a[164:167],v[132:135],v[40:43]\n"
"v_min3_u32 v36,v36,v134,v135\n"
"v_min3_u32 v37,v37,v136,v137\n"
"v_mfma_f32_16x16x32_bf16 v[40:43],a[168:171],v[136:139],v[40:43]\n"
"v_min3_u32 v36,v36,v138,v139\n"
"v_min3_u32 v37,v37,v140,v141\n"
"v_mfma_f32_16x16x32_bf16 v[40:43],a[172:175],v[140:143],v[40:43]\n"
"v_min3_u32 v36,v36,v142,v143\n"
"v_min3_u32 v37,v37,v144,v145\n"
"v_mfma_f32_16x16x32_bf16 v[40:43],a[176:179],v[144:147],v[40:43]\n"
"v_min3_u32 v36,v36,v146,v147\n"
"v_min3_u32 v37,v37,v148,v149\n"
"v_mfma_f32_16x16x32_bf16 v[40:43],a[180:183],v[148:151],v[40:43]\n"
"v_min3_u32 v36,v36,v150,v151\n"
"v_min3_u32 v37,v37,v152,v153\n"
"v_mfma_f32_16x16x32_bf16 v[40:43],a[184:187],v[152:155],v[40:43]\n"
"v_min3_u32 v36,v36,v154,v155\n"
"v_min3_u32 v37,v37,v156,v157\n"
"v_mfma_f32_16x16x32_bf16 v[40:43],a[188:191],v[156:159],v[40:43]\n"
"v_min3_u32 v36,v36,v158,v159\n"
"v_min_u32 v36,v36,v37\n"
"v_cmp_eq_u32 vcc,0,v36\n"
"v_mfma_f32_16x16x32_bf16 v[44:47],a[192:195],v[128:131],0\n"
"s_cbranch_vccz .Lfk_wh_x\n"
"s_sleep 1\n"
"s_mov_b64 exec,vcc\n"
"buffer_load_dwordx4 v[128:131],v11,s[24:27],0 offen offset:0 sc1\n"
"buffer_load_dwordx4 v[132:135],v11,s[24:27],0 offen offset:64 sc1\n"
"buffer_load_dwordx4 v[136:139],v11,s[24:27],0 offen offset:128 sc1\n"
"buffer_load_dwordx4 v[140:143],v11,s[24:27],0 offen offset:192 sc1\n"
"buffer_load_dwordx4 v[144:147],v11,s[24:27],0 offen offset:256 sc1\n"
"buffer_load_dwordx4 v[148:151],v11,s[24:27],0 offen offset:320 sc1\n"
"buffer_load_dwordx4 v[152:155],v11,s[24:27],0 offen offset:384 sc1\n"
"buffer_load_dwordx4 v[156:159],v11,s[24:27],0 offen offset:448 sc1\n"
"s_mov_b64 exec,-1\n"
"s_sub_u32 s84,s84,1\n"
"s_waitcnt vmcnt(0)\n"
"s_cbranch_scc0 .Lfk_wh\n"
".Lfk_wh_x:\n"
"v_mfma_f32_16x16x32_bf16 v[44:47],a[196:199],v[132:135],v[44:47]\n"
"v_mfma_f32_16x16x32_bf16 v[44:47],a[200:203],v[136:139],v[44:47]\n"
"v_mfma_f32_16x16x32_bf16 v[44:47],a[204:207],v[140:143],v[44:47]\n"
"ds_write_b128 v60,v[40:43] offset:1024\n"
"v_mfma_f32_16x16x32_bf16 v[44:47],a[208:211],v[144:147],v[44:47]\n"
"v_mfma_f32_16x16x32_bf16 v[44:47],a[212:215],v[148:151],v[44:47]\n"
"v_mfma_f32_16x16x32_bf16 v[44:47],a[216:219],v[152:155],v[44:47]\n"
"v_mfma_f32_16x16x32_bf16 v[44:47],a[220:223],v[156:159],v[44:47]\n"
"s_nop 7\n"
"ds_write_b128 v60,v[44:47] offset:2048\n"
"s_waitcnt lgkmcnt(0)\n"
"s_barrier\n"
"ds_read_b128 v[128:131],v61 offset:1024\n"
"ds_read_b128 v[144:147],v61 offset:2048\n"
"ds_read_b128 v[132:135],v61 offset:9216\n"
"ds_read_b128 v[148:151],v61 offset:10240\n"
"ds_read_b128 v[136:139],v61 offset:17408\n"
"ds_read_b128 v[152:155],v61 offset:18432\n"
"ds_read_b128 v[140:143],v61 offset:25600\n"
"ds_read_b128 v[156:159],v61 offset:26624\n"
"s_waitcnt lgkmcnt(0)\n"
"v_pk_add_f32 v[128:129],v[128:129],v[132:133]\n"
"v_pk_add_f32 v[136:137],v[136:137],v[140:141]\n"
"v_pk_add_f32 v[130:131],v[130:131],v[134:135]\n"
"v_pk_add_f32 v[138:139],v[138:139],v[142:143]\n"
"v_pk_add_f32 v[128:129],v[128:129],v[136:137]\n"
"v_pk_add_f32 v[130:131],v[130:131],v[138:139]\n"
"v_pk_add_f32 v[144:145],v[144:145],v[148:149]\n"
"v_pk_add_f32 v[152:153],v[152:153],v[156:157]\n"
"v_pk_add_f32 v[146:147],v[146:147],v[150:151]\n"
"v_pk_add_f32 v[154:155],v[154:155],v[158:159]\n"
"v_pk_add_f32 v[144:145],v[144:145],v[152:153]\n"
"v_pk_add_f32 v[146:147],v[146:147],v[154:155]\n"
"v_mul_f32 v132,0xbfb8aa3b,v128\n"
"v_mul_f32 v133,0xbfb8aa3b,v129\n"
"v_mul_f32 v134,0xbfb8aa3b,v130\n"
"v_mul_f32 v135,0xbfb8aa3b,v131\n"
"v_exp_f32 v132,v132\n"
"v_exp_f32 v133,v133\n"
"v_exp_f32 v134,v134\n"
"v_exp_f32 v135,v135\n"
"v_add_f32 v132,1.0,v132\n"
"v_add_f32 v133,1.0,v133\n"
"v_add_f32 v134,1.0,v134\n"
"v_add_f32 v135,1.0,v135\n"
"v_rcp_f32 v132,v132\n"
"v_rcp_f32 v133,v133\n"
"v_rcp_f32 v134,v134\n"
"v_rcp_f32 v135,v135\n"
"v_pk_mul_f32 v[128:129],v[128:129],v[132:133]\n"
"v_pk_mul_f32 v[130:131],v[130:131],v[134:135]\n"
"v_pk_mul_f32 v[128:129],v[128:129],v[144:145]\n"
"v_pk_mul_f32 v[130:131],v[130:131],v[146:147]\n"
"v_cvt_pk_bf16_f32 v132,v128,v129\n"
"v_cvt_pk_bf16_f32 v133,v130,v131\n"
"v_cndmask_b32 v136,0,v132,s[8:9]\n"
"v_cndmask_b32 v137,0,v133,s[8:9]\n"
"v_cndmask_b32 v138,0,v132,s[10:11]\n"
"v_cndmask_b32 v139,0,v133,s[10:11]\n"
"v_cndmask_b32 v141,0,v133,s[12:13]\n"
"v_cndmask_b32 v143,0,v133,s[14:15]\n"
"v_mfma_f32_16x16x32_bf16 v[40:43],a[224:227],v[136:139],0\n"
"v_cndmask_b32 v142,0,v132,s[14:15]\n"
"v_cndmask_b32 v140,0,v132,s[12:13]\n"
"v_mfma_f32_16x16x32_bf16 v[44:47],a[240:243],v[136:139],0\n"
"v_cndmask_b32 v145,0,v133,s[16:17]\n"
"v_cndmask_b32 v147,0,v133,s[18:19]\n"
"v_mfma_f32_16x16x32_bf16 v[40:43],a[228:231],v[140:143],v[40:43]\n"
"v_cndmask_b32 v146,0,v132,s[18:19]\n"
"v_cndmask_b32 v144,0,v132,s[16:17]\n"
"v_mfma_f32_16x16x32_bf16 v[44:47],a[244:247],v[140:143],v[44:47]\n"
"v_cndmask_b32 v149,0,v133,s[20:21]\n"
"v_cndmask_b32 v151,0,v133,s[22:23]\n"
"v_mfma_f32_16x16x32_bf16 v[40:43],a[232:235],v[144:147],v[40:43]\n"
"v_cndmask_b32 v150,0,v132,s[22:23]\n"
"v_cndmask_b32 v148,0,v132,s[20:21]\n"
"v_mfma_f32_16x16x32_bf16 v[44:47],a[248:251],v[144:147],v[44:47]\n"
"s_nop 0\n"
"v_mfma_f32_16x16x32_bf16 v[40:43],a[236:239],v[148:151],v[40:43]\n"
"v_mfma_f32_16x16x32_bf16 v[44:47],a[252:255],v[148:151],v[44:47]\n"
"s_or_b32 s82,s90,0x800\n"
"s_cmp_eq_u32 s63,s82\n"
"s_nop 4\n"
"v_or_b32 v40,1,v40\n"
"v_or_b32 v41,1,v41\n"
"v_or_b32 v42,1,v42\n"
"v_or_b32 v43,1,v43\n"
"v_or_b32 v44,1,v44\n"
"v_or_b32 v45,1,v45\n"
"v_or_b32 v46,1,v46\n"
"v_or_b32 v47,1,v47\n"
"buffer_store_dwordx4 v[40:43],v62,s[24:27],s69 offen offset:0 sc1\n"
"buffer_store_dwordx4 v[44:47],v62,s[24:27],s69 offen offset:512 sc1\n"
"s_cselect_b32 s68,2146435072,s68\n"
"s_or_b32 s82,s92,0x800\n"
"s_cmp_eq_u32 s63,s82\n"
"s_cselect_b32 s68,2146435072,s68\n"
"buffer_store_dwordx4 v[248:251],v112,s[48:51],s68 offen offset:0 sc1\n"
"buffer_store_dwordx4 v[252:255],v112,s[48:51],s68 offen offset:1024 sc1\n"
"s_cmp_eq_u32 s60,0\n"
"s_cselect_b32 s82,s75,2146435072\n"
"buffer_store_dwordx2 a[154:155],off,s[24:27],s82 sc1\n"
"s_mov_b32 s84,0x40000\n"
"s_waitcnt vmcnt(5)\n"
".Lfk_ws:\n"
"v_xor_b32 v20,s80,v17\n"
"v_xor_b32 v21,s80,v19\n"
"v_or_b32 v20,v20,v21\n"
"v_cmp_ne_u32 vcc,0,v20\n"
"s_nop 0\n"
"s_cbranch_vccz .Lfk_ws_x\n"
"s_mov_b64 exec,vcc\n"
"s_mov_b32 s82,150241280\n"
"buffer_load_dwordx4 v[16:19],v9,s[24:27],s82 offen sc1\n"
"s_mov_b64 exec,-1\n"
"s_sub_u32 s84,s84,1\n"
"s_waitcnt vmcnt(0)\n"
"s_cbranch_scc0 .Lfk_ws\n"
".Lfk_ws_x:\n"
"v_mfma_f32_16x16x32_bf16 v[24:27],v[66:69],a[0:3],0\n"
"v_mfma_f32_16x16x32_bf16 v[28:31],v[64:67],a[0:3],0\n"
"v_mfma_f32_16x16x32_bf16 v[24:27],v[66:69],a[4:7],v[24:27]\n"
"v_mfma_f32_16x16x32_bf16 v[28:31],v[64:67],a[4:7],v[28:31]\n"
"v_mfma_f32_16x16x32_bf16 v[24:27],v[66:69],a[8:11],v[24:27]\n"
"v_mfma_f32_16x16x32_bf16 v[28:31],v[64:67],a[8:11],v[28:31]\n"
"v_mfma_f32_16x16x32_bf16 v[24:27],v[66:69],a[12:15],v[24:27]\n"
"v_mfma_f32_16x16x32_bf16 v[28:31],v[64:67],a[12:15],v[28:31]\n"
"v_mfma_f32_16x16x32_bf16 v[24:27],v[66:69],a[16:19],v[24:27]\n"
"v_mfma_f32_16x16x32_bf16 v[28:31],v[64:67],a[16:19],v[28:31]\n"
"v_and_b32 v12,0x800,v6\n"
"v_cmp_ne_u32 vcc,0,v12\n"
"s_nop 1\n"
"v_cndmask_b32 v10,v18,v16,vcc\n"
"v_lshlrev_b32 v11,16,v10\n"
"v_rcp_f32 v11,v11\n"
"v_and_b32 v10,0xffff0000,v10\n"
"v_cvt_pk_bf16_f32 v20,v24,v25\n"
"v_cvt_pk_bf16_f32 v21,v26,v27\n"
"v_cvt_pk_bf16_f32 v22,v28,v29\n"
"v_cvt_pk_bf16_f32 v23,v30,v31\n"
"v_lshlrev_b32 v24,16,v20\n"
"v_and_b32 v25,0xffff0000,v20\n"
"v_lshlrev_b32 v26,16,v21\n"
"v_and_b32 v27,0xffff0000,v21\n"
"v_lshlrev_b32 v28,16,v22\n"
"v_and_b32 v29,0xffff0000,v22\n"
"v_lshlrev_b32 v30,16,v23\n"
"v_and_b32 v31,0xffff0000,v23\n"
"v_pk_add_f32 v[32:33],v[24:25],v[10:11] op_sel_hi:[1,0] neg_lo:[0,1] neg_hi:[0,1]\n"
"v_pk_add_f32 v[34:35],v[26:27],v[10:11] op_sel_hi:[1,0] neg_lo:[0,1] neg_hi:[0,1]\n"
"v_pk_add_f32 v[36:37],v[28:29],v[10:11] op_sel_hi:[1,0] neg_lo:[0,1] neg_hi:[0,1]\n"
"v_pk_add_f32 v[38:39],v[30:31],v[10:11] op_sel_hi:[1,0] neg_lo:[0,1] neg_hi:[0,1]\n"
"v_cvt_pk_bf16_f32 v20,v32,v33\n"
"v_cvt_pk_bf16_f32 v21,v34,v35\n"
"v_cvt_pk_bf16_f32 v22,v36,v37\n"
"v_cvt_pk_bf16_f32 v23,v38,v39\n"
"v_lshlrev_b32 v32,16,v20\n"
"v_and_b32 v33,0xffff0000,v20\n"
"v_lshlrev_b32 v34,16,v21\n"
"v_and_b32 v35,0xffff0000,v21\n"
"v_lshlrev_b32 v36,16,v22\n"
"v_and_b32 v37,0xffff0000,v22\n"
"v_lshlrev_b32 v38,16,v23\n"
"v_and_b32 v39,0xffff0000,v23\n"
"v_pk_mul_f32 v[32:33],v[32:33],v[10:11] op_sel:[0,1]\n"
"v_pk_mul_f32 v[34:35],v[34:35],v[10:11] op_sel:[0,1]\n"
"v_pk_mul_f32 v[36:37],v[36:37],v[10:11] op_sel:[0,1]\n"
"v_pk_mul_f32 v[38:39],v[38:39],v[10:11] op_sel:[0,1]\n"
"v_cvt_pk_bf16_f32 v20,v32,v33\n"
"v_cvt_pk_bf16_f32 v21,v34,v35\n"
"v_cvt_pk_bf16_f32 v22,v36,v37\n"
"v_cvt_pk_bf16_f32 v23,v38,v39\n"
"ds_write_b128 v58,v[20:23]\n"
"ds_write_b128 v60,v[24:27] offset:3072\n"
"ds_write_b128 v60,v[28:31] offset:4096\n"
"s_bfe_u32 s82,s73,0x20005\n"
"s_lshl_b32 s82,s82,13\n"
"s_and_b32 s83,s73,31\n"
"s_lshr_b32 s91,s73,7\n"
"s_lshl_b32 s89,s91,9\n"
"s_lshl4_add_u32 s89,s83,s89\n"
"s_add_u32 s89,s89,s82\n"
"s_add_u32 s89,s89,1024\n"
"s_lshl1_add_u32 s83,s83,s91\n"
"s_lshl4_add_u32 s82,s83,s82\n"
"s_add_u32 s82,s82,4096\n"
"v_bfe_u32 v0,v112,4,3\n"
"v_lshl_add_u32 v1,v0,1,s89\n"
"v_and_b32 v3,3,v0\n"
"v_lshl_add_u32 v3,v3,2,s82\n"
"v_lshrrev_b32 v4,2,v0\n"
"v_lshl_add_u32 v3,v4,10,v3\n"
"s_cmp_eq_u32 s91,0\n"
"s_cselect_b32 s82,s90,s92\n"
"s_not_b32 s83,s82\n"
"s_and_b32 s89,s83,0x7ff\n"
"s_lshl_b32 s89,s89,11\n"
"s_and_b32 s91,s73,127\n"
"s_lshl4_add_u32 s89,s91,s89\n"
"s_ashr_i32 s83,s83,31\n"
"s_or_b32 s83,s83,0x80000000\n"
"s_xor_b32 s82,s82,s83\n"
"s_and_b32 s82,s82,0xffff0000\n"
"v_mov_b32 v5,0xbfb8aa3b\n"
"v_mul_f32 v5,s82,v5\n"
"v_exp_f32 v5,v5\n"
"s_nop 0\n"
"v_add_f32 v5,1.0,v5\n"
"v_rcp_f32 v5,v5\n"
"s_nop 0\n"
"v_cvt_pk_bf16_f32 v5,v5,v5\n"
"v_lshlrev_b32 v5,16,v5\n"
"s_lshl_b32 s82,s52,7\n"
"s_add_u32 s82,s82,64\n"
"v_lshrrev_b32 v6,3,v112\n"
"v_and_b32 v6,0x60,v6\n"
"v_and_b32 v7,16,v112\n"
"v_add3_u32 v6,v6,v7,s82\n"
"v_lshrrev_b32 v7,2,v112\n"
"v_add_u32 v7,64,v7\n"
"v_lshlrev_b32 v8,1,v0\n"
"s_sleep 1\n"
"s_mov_b32 s84,0x40000\n"
"buffer_load_dwordx4 v[16:19],v63,s[24:27],s70 offen sc1\n"
"buffer_load_dwordx4 v[20:23],v63,s[24:27],s71 offen sc1\n"
"s_waitcnt vmcnt(0)\n"
".Lfk_wy:\n"
"v_min3_u32 v24,v16,v17,v18\n"
"v_min3_u32 v25,v19,v20,v21\n"
"v_min3_u32 v24,v24,v22,v23\n"
"v_min_u32 v24,v24,v25\n"
"v_cmp_eq_u32 vcc,0,v24\n"
"s_nop 0\n"
"s_cbranch_vccz .Lfk_wy_x\n"
"s_sleep 1\n"
"s_mov_b64 exec,vcc\n"
"buffer_load_dwordx4 v[16:19],v63,s[24:27],s70 offen sc1\n"
"buffer_load_dwordx4 v[20:23],v63,s[24:27],s71 offen sc1\n"
"s_mov_b64 exec,-1\n"
"s_sub_u32 s84,s84,1\n"
"s_waitcnt vmcnt(0)\n"
"s_cbranch_scc0 .Lfk_wy\n"
".Lfk_wy_x:\n"
"s_mov_b32 vcc_lo,-1\n"
"s_mov_b32 vcc_hi,0x00ffffff\n"
"v_add_f32 v16,v16,v20\n"
"v_add_f32 v17,v17,v21\n"
"v_add_f32 v18,v18,v22\n"
"v_add_f32 v19,v19,v23\n"
"v_cndmask_b32 v16,0,v16,vcc\n"
"v_cndmask_b32 v17,0,v17,vcc\n"
"v_cndmask_b32 v18,0,v18,vcc\n"
"v_cndmask_b32 v19,0,v19,vcc\n"
"v_add_f32_dpp v16,v16,v16 quad_perm:[2,3,0,1] row_mask:0xf bank_mask:0xf\n"
"v_add_f32_dpp v17,v17,v17 quad_perm:[2,3,0,1] row_mask:0xf bank_mask:0xf\n"
"v_add_f32_dpp v18,v18,v18 quad_perm:[2,3,0,1] row_mask:0xf bank_mask:0xf\n"
"v_add_f32_dpp v19,v19,v19 quad_perm:[2,3,0,1] row_mask:0xf bank_mask:0xf\n"
"v_add_f32_dpp v16,v16,v16 row_ror:4 row_mask:0xf bank_mask:0xf\n"
"v_add_f32_dpp v17,v17,v17 row_ror:4 row_mask:0xf bank_mask:0xf\n"
"v_add_f32_dpp v18,v18,v18 row_ror:4 row_mask:0xf bank_mask:0xf\n"
"v_add_f32_dpp v19,v19,v19 row_ror:4 row_mask:0xf bank_mask:0xf\n"
"v_add_f32_dpp v16,v16,v16 row_ror:8 row_mask:0xf bank_mask:0xf\n"
"v_add_f32_dpp v17,v17,v17 row_ror:8 row_mask:0xf bank_mask:0xf\n"
"v_add_f32_dpp v18,v18,v18 row_ror:8 row_mask:0xf bank_mask:0xf\n"
"v_add_f32_dpp v19,v19,v19 row_ror:8 row_mask:0xf bank_mask:0xf\n"
"s_mov_b32 exec_lo,0x30003\n"
"s_mov_b32 exec_hi,0x30003\n"
"ds_write_b128 v6,v[16:19]\n"
"s_mov_b64 exec,-1\n"
"s_waitcnt lgkmcnt(0)\n"
"s_barrier\n"
"s_cmp_lg_u32 s52,0\n"
"s_cbranch_scc1 .Lfk_t2\n"
"ds_read2_b32 v[16:17],v7 offset0:0 offset1:64\n"
"ds_read_u16 v2,v1\n"
"ds_read_b32 v3,v3\n"
"s_waitcnt lgkmcnt(0)\n"
"v_lshlrev_b32 v2,16,v2\n"
"v_add_f32 v16,v16,v17\n"
"s_nop 1\n"
"v_add_f32_dpp v16,v16,v16 row_ror:8 row_mask:0xf bank_mask:0xf\n"
"v_mov_b32 v17,v16\n"
"s_nop 1\n"
"v_permlane16_swap_b32 v17,v16\n"
"v_add_f32 v16,v16,v17\n"
"v_mov_b32 v17,v16\n"
"s_nop 1\n"
"v_permlane32_swap_b32 v17,v16\n"
"v_add_f32 v16,v16,v17\n"
"v_cvt_pk_bf16_f32 v16,v16,v16\n"
"v_lshlrev_b32 v16,16,v16\n"
"v_mul_f32 v16,v16,v5\n"
"v_cvt_pk_bf16_f32 v16,v16,v16\n"
"v_lshlrev_b32 v16,16,v16\n"
"v_add_f32 v16,v16,v2\n"
"v_cvt_pk_bf16_f32 v16,v16,v16\n"
"v_lshlrev_b32 v16,16,v16\n"
"v_add_f32 v16,v16,v3\n"
"s_nop 1\n"
"v_mov_b32_dpp v17,v16 quad_perm:[1,0,3,2] row_mask:0xf bank_mask:0xf\n"
"v_cvt_pk_bf16_f32 v16,v16,v17\n"
"s_mov_b64 exec,0x55\n"
"buffer_store_dword v16,v8,s[48:51],s89 offen sc1\n"
"s_mov_b64 exec,-1\n"
".Lfk_t2:\n"
"s_branch .Lfk_end\n"
".Lfk_g0:\n"
"s_waitcnt vmcnt(11) lgkmcnt(0)\n"
"s_barrier\n"
"v_mfma_f32_16x16x32_bf16 v[32:35],a[96:99],a[0:3],v[96:99]\n"
"s_add_u32 m0,s55,4096\n"
"v_exp_f32 v0,v0\n"
"v_mfma_f32_16x16x32_bf16 v[32:35],a[100:103],a[4:7],v[32:35]\n"
"s_add_u32 s54,s54,s22\n"
"v_exp_f32 v1,v1\n"
"v_mfma_f32_16x16x32_bf16 v[32:35],a[104:107],a[8:11],v[32:35]\n"
"buffer_load_dwordx4 v115,s[32:35],s54 offen sc1 lds\n"
"v_exp_f32 v2,v2\n"
"v_cvt_pk_bf16_f32 v64,v0,v1\n"
"v_mfma_f32_16x16x32_bf16 v[32:35],a[108:111],a[12:15],v[32:35]\n"
"v_exp_f32 v3,v3\n"
"v_mfma_f32_16x16x32_bf16 v[36:39],a[124:127],a[12:15],v[96:99]\n"
"v_exp_f32 v4,v4\n"
"v_cvt_pk_bf16_f32 v65,v2,v3\n"
"v_mfma_f32_16x16x32_bf16 v[36:39],a[120:123],a[8:11],v[36:39]\n"
"v_exp_f32 v5,v5\n"
"v_mfma_f32_16x16x32_bf16 v[36:39],a[116:119],a[4:7],v[36:39]\n"
"v_exp_f32 v6,v6\n"
"v_cvt_pk_bf16_f32 v66,v4,v5\n"
"v_mfma_f32_16x16x32_bf16 v[36:39],a[112:115],a[0:3],v[36:39]\n"
"v_exp_f32 v7,v7\n"
"v_mfma_f32_16x16x32_bf16 v[44:47],a[112:115],a[16:19],v[100:103]\n"
"v_exp_f32 v8,v8\n"
"v_cvt_pk_bf16_f32 v67,v6,v7\n"
"v_exp_f32 v9,v9\n"
"v_mfma_f32_16x16x32_bf16 v[44:47],a[116:119],a[20:23],v[44:47]\n"
"ds_read_b128 a[64:67],v113 offset:49152\n"
"ds_read_b128 a[68:71],v113 offset:50176\n"
"v_exp_f32 v10,v10\n"
"v_mfma_f32_16x16x32_bf16 v[44:47],a[120:123],a[24:27],v[44:47]\n"
"ds_read_b128 a[72:75],v113 offset:51200\n"
"ds_read_b128 a[76:79],v113 offset:52224\n"
"v_exp_f32 v11,v11\n"
"v_mfma_f32_16x16x32_bf16 v[44:47],a[124:127],a[28:31],v[44:47]\n"
"ds_read_b128 a[80:83],v113 offset:53248\n"
"ds_read_b128 a[84:87],v113 offset:54272\n"
"v_exp_f32 v12,v12\n"
"v_mfma_f32_16x16x32_bf16 v[40:43],a[108:111],a[28:31],v[100:103]\n"
"ds_read_b128 a[88:91],v113 offset:55296\n"
"ds_read_b128 a[92:95],v113 offset:56320\n"
"v_mfma_f32_16x16x32_bf16 v[40:43],a[104:107],a[24:27],v[40:43]\n"
"v_exp_f32 v13,v13\n"
"v_cvt_pk_bf16_f32 v68,v8,v9\n"
"v_cvt_pk_bf16_f32 v69,v10,v11\n"
"v_mfma_f32_16x16x32_bf16 v[40:43],a[100:103],a[20:23],v[40:43]\n"
"v_exp_f32 v14,v14\n"
"v_cvt_pk_bf16_f32 v70,v12,v13\n"
"v_mfma_f32_16x16x32_bf16 v[40:43],a[96:99],a[16:19],v[40:43]\n"
"v_exp_f32 v15,v15\n"
"v_mfma_f32_16x16x32_bf16 v[48:51],a[96:99],a[32:35],v[104:107]\n"
"v_exp_f32 v16,v16\n"
"v_cvt_pk_bf16_f32 v71,v14,v15\n"
"v_mfma_f32_16x16x32_bf16 v[48:51],a[100:103],a[36:39],v[48:51]\n"
"v_exp_f32 v17,v17\n"
"v_mfma_f32_16x16x32_bf16 v[48:51],a[104:107],a[40:43],v[48:51]\n"
"v_exp_f32 v18,v18\n"
"v_cvt_pk_bf16_f32 v72,v16,v17\n"
"v_mfma_f32_16x16x32_bf16 v[48:51],a[108:111],a[44:47],v[48:51]\n"
"v_exp_f32 v19,v19\n"
"v_mfma_f32_16x16x32_bf16 v[52:55],a[124:127],a[44:47],v[104:107]\n"
"v_exp_f32 v20,v20\n"
"v_cvt_pk_bf16_f32 v73,v18,v19\n"
"v_mfma_f32_16x16x32_bf16 v[52:55],a[120:123],a[40:43],v[52:55]\n"
"v_exp_f32 v21,v21\n"
"v_mfma_f32_16x16x32_bf16 v[52:55],a[116:119],a[36:39],v[52:55]\n"
"v_exp_f32 v22,v22\n"
"v_cvt_pk_bf16_f32 v74,v20,v21\n"
"v_mfma_f32_16x16x32_bf16 v[52:55],a[112:115],a[32:35],v[52:55]\n"
"v_exp_f32 v23,v23\n"
"v_mfma_f32_16x16x32_bf16 v[60:63],a[112:115],a[48:51],v[108:111]\n"
"v_exp_f32 v24,v24\n"
"v_cvt_pk_bf16_f32 v75,v22,v23\n"
"v_mfma_f32_16x16x32_bf16 v[60:63],a[116:119],a[52:55],v[60:63]\n"
"v_exp_f32 v25,v25\n"
"v_mfma_f32_16x16x32_bf16 v[60:63],a[120:123],a[56:59],v[60:63]\n"
"v_exp_f32 v26,v26\n"
"v_exp_f32 v27,v27\n"
"v_mfma_f32_16x16x32_bf16 v[60:63],a[124:127],a[60:63],v[60:63]\n"
"buffer_store_dwordx4 a[228:231],v112,s[24:27],s56 offen offset:1024 sc1\n"
"v_mfma_f32_16x16x32_bf16 v[56:59],a[108:111],a[60:63],v[108:111]\n"
"v_exp_f32 v28,v28\n"
"v_cvt_pk_bf16_f32 v77,v26,v27\n"
"v_mfma_f32_16x16x32_bf16 v[56:59],a[104:107],a[56:59],v[56:59]\n"
"v_exp_f32 v29,v29\n"
"v_mfma_f32_16x16x32_bf16 v[56:59],a[100:103],a[52:55],v[56:59]\n"
"v_exp_f32 v30,v30\n"
"v_cvt_pk_bf16_f32 v78,v28,v29\n"
"v_mfma_f32_16x16x32_bf16 v[56:59],a[96:99],a[48:51],v[56:59]\n"
"s_waitcnt vmcnt(9) lgkmcnt(0)\n"
"v_exp_f32 v31,v31\n"
"v_cvt_pk_bf16_f32 v76,v24,v25\n"
"v_cvt_pk_bf16_f32 v79,v30,v31\n"
"s_barrier\n"
"v_mfma_f32_16x16x32_bf16 v[0:3],a[64:67],a[0:3],v[96:99]\n"
"s_add_u32 m0,s55,8192\n"
"v_exp_f32 v122,v32\n"
"v_mfma_f32_16x16x32_bf16 v[0:3],a[68:71],a[4:7],v[0:3]\n"
"s_add_u32 s54,s54,s22\n"
"v_exp_f32 v123,v33\n"
"v_mfma_f32_16x16x32_bf16 v[0:3],a[72:75],a[8:11],v[0:3]\n"
"ds_read_b128 a[96:99],v114 offset:40960\n"
"ds_read_b128 a[100:103],v114 offset:41984\n"
"v_mfma_f32_16x16x32_bf16 v[0:3],a[76:79],a[12:15],v[0:3]\n"
"v_exp_f32 v119,v34\n"
"v_mfma_f32_16x16x32_bf16 v[4:7],a[92:95],a[12:15],v[96:99]\n"
"ds_read_b128 a[104:107],v114 offset:43008\n"
"v_cvt_pk_bf16_f32 v80,v122,v123\n"
"v_mfma_f32_16x16x32_bf16 v[4:7],a[88:91],a[8:11],v[4:7]\n"
"v_exp_f32 v120,v35\n"
"v_mfma_f32_16x16x32_bf16 v[4:7],a[84:87],a[4:7],v[4:7]\n"
"ds_read_b128 a[108:111],v114 offset:44032\n"
"ds_read_b128 a[112:115],v114 offset:45056\n"
"v_mfma_f32_16x16x32_bf16 v[4:7],a[80:83],a[0:3],v[4:7]\n"
"v_exp_f32 v122,v36\n"
"v_mfma_f32_16x16x32_bf16 v[12:15],a[80:83],a[16:19],v[100:103]\n"
"ds_read_b128 a[116:119],v114 offset:46080\n"
"v_cvt_pk_bf16_f32 v81,v119,v120\n"
"v_mfma_f32_16x16x32_bf16 v[12:15],a[84:87],a[20:23],v[12:15]\n"
"v_exp_f32 v123,v37\n"
"v_mfma_f32_16x16x32_bf16 v[12:15],a[88:91],a[24:27],v[12:15]\n"
"ds_read_b128 a[120:123],v114 offset:47104\n"
"ds_read_b128 a[124:127],v114 offset:48128\n"
"v_mfma_f32_16x16x32_bf16 v[12:15],a[92:95],a[28:31],v[12:15]\n"
"v_exp_f32 v119,v38\n"
"v_cvt_pk_bf16_f32 v82,v122,v123\n"
"v_mfma_f32_16x16x32_bf16 v[8:11],a[76:79],a[28:31],v[100:103]\n"
"buffer_store_dwordx4 a[168:171],v112,s[24:27],s3 offen offset:2048 sc1\n"
"v_mfma_f32_16x16x32_bf16 v[8:11],a[72:75],a[24:27],v[8:11]\n"
"v_exp_f32 v120,v39\n"
"v_mfma_f32_16x16x32_bf16 v[8:11],a[68:71],a[20:23],v[8:11]\n"
"buffer_load_dwordx4 v115,s[32:35],s54 offen sc1 lds\n"
"v_mfma_f32_16x16x32_bf16 v[8:11],a[64:67],a[16:19],v[8:11]\n"
"v_exp_f32 v122,v40\n"
"v_mfma_f32_16x16x32_bf16 v[16:19],a[64:67],a[32:35],v[104:107]\n"
"v_cvt_pk_bf16_f32 v83,v119,v120\n"
"v_or3_b32 v118,v80,v81,v82\n"
"v_mfma_f32_16x16x32_bf16 v[16:19],a[68:71],a[36:39],v[16:19]\n"
"v_exp_f32 v123,v41\n"
"v_mfma_f32_16x16x32_bf16 v[16:19],a[72:75],a[40:43],v[16:19]\n"
"v_cvt_pk_bf16_f32 v84,v122,v123\n"
"v_or3_b32 v118,v118,v83,v84\n"
"v_mfma_f32_16x16x32_bf16 v[16:19],a[76:79],a[44:47],v[16:19]\n"
"v_exp_f32 v119,v42\n"
"v_mfma_f32_16x16x32_bf16 v[20:23],a[92:95],a[44:47],v[104:107]\n"
"v_mfma_f32_16x16x32_bf16 v[20:23],a[88:91],a[40:43],v[20:23]\n"
"v_exp_f32 v120,v43\n"
"v_mfma_f32_16x16x32_bf16 v[20:23],a[84:87],a[36:39],v[20:23]\n"
"v_cvt_pk_bf16_f32 v85,v119,v120\n"
"v_mfma_f32_16x16x32_bf16 v[20:23],a[80:83],a[32:35],v[20:23]\n"
"v_exp_f32 v122,v44\n"
"v_mfma_f32_16x16x32_bf16 v[28:31],a[80:83],a[48:51],v[108:111]\n"
"buffer_store_dwordx4 a[172:175],v112,s[24:27],s3 offen offset:3072 sc1\n"
"v_mfma_f32_16x16x32_bf16 v[28:31],a[84:87],a[52:55],v[28:31]\n"
"v_exp_f32 v123,v45\n"
"v_mfma_f32_16x16x32_bf16 v[28:31],a[88:91],a[56:59],v[28:31]\n"
"s_sub_u32 s3,s3,16384\n"
"v_mfma_f32_16x16x32_bf16 v[28:31],a[92:95],a[60:63],v[28:31]\n"
"v_exp_f32 v119,v46\n"
"v_mfma_f32_16x16x32_bf16 v[24:27],a[76:79],a[60:63],v[108:111]\n"
"v_cvt_pk_bf16_f32 v86,v122,v123\n"
"v_or3_b32 v118,v118,v85,v86\n"
"v_mfma_f32_16x16x32_bf16 v[24:27],a[72:75],a[56:59],v[24:27]\n"
"v_exp_f32 v120,v47\n"
"v_mfma_f32_16x16x32_bf16 v[24:27],a[68:71],a[52:55],v[24:27]\n"
"v_cvt_pk_bf16_f32 v87,v119,v120\n"
"v_mfma_f32_16x16x32_bf16 v[24:27],a[64:67],a[48:51],v[24:27]\n"
"s_waitcnt lgkmcnt(0)\n"
"v_exp_f32 v122,v48\n"
"v_mfma_f32_16x16x32_bf16 v[128:131],a[96:99],v[64:67],0\n"
"v_exp_f32 v123,v49\n"
"v_mfma_f32_16x16x32_bf16 v[160:163],a[96:99],v[68:71],0\n"
"ds_read_b128 a[64:67],v113 offset:32768\n"
"ds_read_b128 a[68:71],v113 offset:33792\n"
"v_mfma_f32_16x16x32_bf16 v[192:195],a[96:99],v[72:75],0\n"
"v_exp_f32 v119,v50\n"
"v_mfma_f32_16x16x32_bf16 v[224:227],a[96:99],v[76:79],0\n"
"ds_read_b128 a[72:75],v113 offset:34816\n"
"v_cvt_pk_bf16_f32 v88,v122,v123\n"
"v_mfma_f32_16x16x32_bf16 a[140:143],a[144:147],v[76:79],0\n"
"v_exp_f32 v120,v51\n"
"v_mfma_f32_16x16x32_bf16 v[228:231],a[100:103],v[76:79],0\n"
"ds_read_b128 a[76:79],v113 offset:35840\n"
"ds_read_b128 a[80:83],v113 offset:36864\n"
"v_mfma_f32_16x16x32_bf16 v[132:135],a[100:103],v[64:67],0\n"
"v_exp_f32 v122,v52\n"
"v_mfma_f32_16x16x32_bf16 v[164:167],a[100:103],v[68:71],0\n"
"ds_read_b128 a[84:87],v113 offset:37888\n"
"v_cvt_pk_bf16_f32 v89,v119,v120\n"
"v_mfma_f32_16x16x32_bf16 v[196:199],a[100:103],v[72:75],0\n"
"v_exp_f32 v123,v53\n"
"v_mfma_f32_16x16x32_bf16 a[136:139],a[144:147],v[72:75],0\n"
"ds_read_b128 a[88:91],v113 offset:38912\n"
"ds_read_b128 a[92:95],v113 offset:39936\n"
"v_mfma_f32_16x16x32_bf16 v[200:203],a[104:107],v[72:75],0\n"
"v_exp_f32 v119,v54\n"
"v_cvt_pk_bf16_f32 v90,v122,v123\n"
"v_mfma_f32_16x16x32_bf16 v[232:235],a[104:107],v[76:79],0\n"
"buffer_store_dwordx4 a[176:179],v112,s[24:27],s3 offen offset:0 sc1\n"
"v_mfma_f32_16x16x32_bf16 v[136:139],a[104:107],v[64:67],0\n"
"v_exp_f32 v120,v55\n"
"v_mfma_f32_16x16x32_bf16 v[168:171],a[104:107],v[68:71],0\n"
"v_cvt_pk_bf16_f32 v91,v119,v120\n"
"v_or3_b32 v118,v118,v87,v88\n"
"v_mfma_f32_16x16x32_bf16 a[132:135],a[144:147],v[68:71],0\n"
"v_exp_f32 v122,v56\n"
"v_mfma_f32_16x16x32_bf16 v[172:175],a[108:111],v[68:71],0\n"
"v_or3_b32 v118,v118,v89,v90\n"
"v_mfma_f32_16x16x32_bf16 v[204:207],a[108:111],v[72:75],0\n"
"v_exp_f32 v123,v57\n"
"v_mfma_f32_16x16x32_bf16 v[236:239],a[108:111],v[76:79],0\n"
"v_cvt_pk_bf16_f32 v92,v122,v123\n"
"v_or3_b32 v118,v118,v91,v92\n"
"v_mfma_f32_16x16x32_bf16 v[140:143],a[108:111],v[64:67],0\n"
"v_exp_f32 v119,v58\n"
"v_mfma_f32_16x16x32_bf16 a[128:131],a[144:147],v[64:67],0\n"
"v_mfma_f32_16x16x32_bf16 v[144:147],a[112:115],v[64:67],0\n"
"v_exp_f32 v120,v59\n"
"v_mfma_f32_16x16x32_bf16 v[176:179],a[112:115],v[68:71],0\n"
"v_cvt_pk_bf16_f32 v93,v119,v120\n"
"v_mfma_f32_16x16x32_bf16 v[208:211],a[112:115],v[72:75],0\n"
"v_exp_f32 v122,v60\n"
"v_mfma_f32_16x16x32_bf16 v[240:243],a[112:115],v[76:79],0\n"
"v_mfma_f32_16x16x32_bf16 v[244:247],a[116:119],v[76:79],0\n"
"v_exp_f32 v123,v61\n"
"v_mfma_f32_16x16x32_bf16 v[148:151],a[116:119],v[64:67],0\n"
"v_cvt_pk_bf16_f32 v94,v122,v123\n"
"v_or3_b32 v118,v118,v93,v94\n"
"v_mfma_f32_16x16x32_bf16 v[180:183],a[116:119],v[68:71],0\n"
"v_exp_f32 v119,v62\n"
"v_mfma_f32_16x16x32_bf16 v[212:215],a[116:119],v[72:75],0\n"
"buffer_store_dwordx4 a[232:235],v112,s[24:27],s56 offen offset:2048 sc1\n"
"v_mfma_f32_16x16x32_bf16 v[216:219],a[120:123],v[72:75],0\n"
"v_exp_f32 v120,v63\n"
"v_mfma_f32_16x16x32_bf16 v[248:251],a[120:123],v[76:79],0\n"
"v_cvt_pk_bf16_f32 v95,v119,v120\n"
"v_or_b32 v118,v118,v95\n"
"v_mfma_f32_16x16x32_bf16 v[152:155],a[120:123],v[64:67],0\n"
"v_and_b32 v118,0x40004000,v118\n"
"v_cmp_ne_u32 vcc,0,v118\n"
"v_mfma_f32_16x16x32_bf16 v[184:187],a[120:123],v[68:71],0\n"
"v_mfma_f32_16x16x32_bf16 v[188:191],a[124:127],v[68:71],0\n"
"v_mfma_f32_16x16x32_bf16 v[220:223],a[124:127],v[72:75],0\n"
"v_mfma_f32_16x16x32_bf16 v[252:255],a[124:127],v[76:79],0\n"
"v_mfma_f32_16x16x32_bf16 v[156:159],a[124:127],v[64:67],0\n"
"s_cbranch_vccnz .Lfk_cs_k1\n"
".Lfk_cr_k1:\n"
"s_waitcnt lgkmcnt(0)\n"
"s_barrier\n"
"v_mfma_f32_16x16x32_bf16 v[32:35],a[64:67],a[0:3],v[96:99]\n"
"s_add_u32 m0,s55,12288\n"
"v_exp_f32 v122,v0\n"
"v_mfma_f32_16x16x32_bf16 v[32:35],a[68:71],a[4:7],v[32:35]\n"
"s_add_u32 s54,s54,s22\n"
"v_exp_f32 v123,v1\n"
"v_mfma_f32_16x16x32_bf16 v[32:35],a[72:75],a[8:11],v[32:35]\n"
"ds_read_b128 a[96:99],v114 offset:8192\n"
"ds_read_b128 a[100:103],v114 offset:9216\n"
"v_mfma_f32_16x16x32_bf16 v[32:35],a[76:79],a[12:15],v[32:35]\n"
"v_exp_f32 v119,v2\n"
"v_mfma_f32_16x16x32_bf16 v[36:39],a[92:95],a[12:15],v[96:99]\n"
"ds_read_b128 a[104:107],v114 offset:10240\n"
"v_cvt_pk_bf16_f32 v64,v122,v123\n"
"v_mfma_f32_16x16x32_bf16 v[36:39],a[88:91],a[8:11],v[36:39]\n"
"v_exp_f32 v120,v3\n"
"v_mfma_f32_16x16x32_bf16 v[36:39],a[84:87],a[4:7],v[36:39]\n"
"ds_read_b128 a[108:111],v114 offset:11264\n"
"ds_read_b128 a[112:115],v114 offset:12288\n"
"v_mfma_f32_16x16x32_bf16 v[36:39],a[80:83],a[0:3],v[36:39]\n"
"v_exp_f32 v122,v4\n"
"v_mfma_f32_16x16x32_bf16 v[44:47],a[80:83],a[16:19],v[100:103]\n"
"ds_read_b128 a[116:119],v114 offset:13312\n"
"v_cvt_pk_bf16_f32 v65,v119,v120\n"
"v_mfma_f32_16x16x32_bf16 v[44:47],a[84:87],a[20:23],v[44:47]\n"
"v_exp_f32 v123,v5\n"
"v_mfma_f32_16x16x32_bf16 v[44:47],a[88:91],a[24:27],v[44:47]\n"
"ds_read_b128 a[120:123],v114 offset:14336\n"
"ds_read_b128 a[124:127],v114 offset:15360\n"
"v_mfma_f32_16x16x32_bf16 v[44:47],a[92:95],a[28:31],v[44:47]\n"
"v_exp_f32 v119,v6\n"
"v_cvt_pk_bf16_f32 v66,v122,v123\n"
"v_mfma_f32_16x16x32_bf16 v[40:43],a[76:79],a[28:31],v[100:103]\n"
"buffer_store_dwordx4 a[180:183],v112,s[24:27],s3 offen offset:1024 sc1\n"
"v_mfma_f32_16x16x32_bf16 v[40:43],a[72:75],a[24:27],v[40:43]\n"
"v_exp_f32 v120,v7\n"
"v_mfma_f32_16x16x32_bf16 v[40:43],a[68:71],a[20:23],v[40:43]\n"
"buffer_load_dwordx4 v115,s[32:35],s54 offen sc1 lds\n"
"v_mfma_f32_16x16x32_bf16 v[40:43],a[64:67],a[16:19],v[40:43]\n"
"v_exp_f32 v122,v8\n"
"v_mfma_f32_16x16x32_bf16 v[48:51],a[64:67],a[32:35],v[104:107]\n"
"v_cvt_pk_bf16_f32 v67,v119,v120\n"
"v_or3_b32 v118,v64,v65,v66\n"
"v_mfma_f32_16x16x32_bf16 v[48:51],a[68:71],a[36:39],v[48:51]\n"
"v_exp_f32 v123,v9\n"
"v_mfma_f32_16x16x32_bf16 v[48:51],a[72:75],a[40:43],v[48:51]\n"
"v_cvt_pk_bf16_f32 v68,v122,v123\n"
"v_or3_b32 v118,v118,v67,v68\n"
"v_mfma_f32_16x16x32_bf16 v[48:51],a[76:79],a[44:47],v[48:51]\n"
"v_exp_f32 v119,v10\n"
"v_mfma_f32_16x16x32_bf16 v[52:55],a[92:95],a[44:47],v[104:107]\n"
"v_mfma_f32_16x16x32_bf16 v[52:55],a[88:91],a[40:43],v[52:55]\n"
"v_exp_f32 v120,v11\n"
"v_mfma_f32_16x16x32_bf16 v[52:55],a[84:87],a[36:39],v[52:55]\n"
"v_cvt_pk_bf16_f32 v69,v119,v120\n"
"v_mfma_f32_16x16x32_bf16 v[52:55],a[80:83],a[32:35],v[52:55]\n"
"v_exp_f32 v122,v12\n"
"v_mfma_f32_16x16x32_bf16 v[60:63],a[80:83],a[48:51],v[108:111]\n"
"buffer_store_dwordx4 a[184:187],v112,s[24:27],s3 offen offset:2048 sc1\n"
"v_mfma_f32_16x16x32_bf16 v[60:63],a[84:87],a[52:55],v[60:63]\n"
"v_exp_f32 v123,v13\n"
"v_mfma_f32_16x16x32_bf16 v[60:63],a[88:91],a[56:59],v[60:63]\n"
"v_cvt_pk_bf16_f32 v70,v122,v123\n"
"v_mfma_f32_16x16x32_bf16 v[60:63],a[92:95],a[60:63],v[60:63]\n"
"v_exp_f32 v119,v14\n"
"v_mfma_f32_16x16x32_bf16 v[56:59],a[76:79],a[60:63],v[108:111]\n"
"v_or3_b32 v118,v118,v69,v70\n"
"v_mfma_f32_16x16x32_bf16 v[56:59],a[72:75],a[56:59],v[56:59]\n"
"v_exp_f32 v120,v15\n"
"v_mfma_f32_16x16x32_bf16 v[56:59],a[68:71],a[52:55],v[56:59]\n"
"v_cvt_pk_bf16_f32 v71,v119,v120\n"
"v_mfma_f32_16x16x32_bf16 v[56:59],a[64:67],a[48:51],v[56:59]\n"
"s_waitcnt lgkmcnt(0)\n"
"v_exp_f32 v122,v16\n"
"v_mfma_f32_16x16x32_bf16 v[128:131],a[96:99],v[80:83],v[128:131]\n"
"v_exp_f32 v123,v17\n"
"v_mfma_f32_16x16x32_bf16 v[160:163],a[96:99],v[84:87],v[160:163]\n"
"ds_read_b128 a[64:67],v113 offset:0\n"
"ds_read_b128 a[68:71],v113 offset:1024\n"
"v_mfma_f32_16x16x32_bf16 v[192:195],a[96:99],v[88:91],v[192:195]\n"
"v_exp_f32 v119,v18\n"
"v_mfma_f32_16x16x32_bf16 v[224:227],a[96:99],v[92:95],v[224:227]\n"
"ds_read_b128 a[72:75],v113 offset:2048\n"
"v_cvt_pk_bf16_f32 v72,v122,v123\n"
"v_mfma_f32_16x16x32_bf16 a[140:143],a[144:147],v[92:95],a[140:143]\n"
"v_exp_f32 v120,v19\n"
"v_mfma_f32_16x16x32_bf16 v[228:231],a[100:103],v[92:95],v[228:231]\n"
"ds_read_b128 a[76:79],v113 offset:3072\n"
"ds_read_b128 a[80:83],v113 offset:4096\n"
"v_mfma_f32_16x16x32_bf16 v[132:135],a[100:103],v[80:83],v[132:135]\n"
"v_exp_f32 v122,v20\n"
"v_mfma_f32_16x16x32_bf16 v[164:167],a[100:103],v[84:87],v[164:167]\n"
"ds_read_b128 a[84:87],v113 offset:5120\n"
"v_cvt_pk_bf16_f32 v73,v119,v120\n"
"v_mfma_f32_16x16x32_bf16 v[196:199],a[100:103],v[88:91],v[196:199]\n"
"v_exp_f32 v123,v21\n"
"v_mfma_f32_16x16x32_bf16 a[136:139],a[144:147],v[88:91],a[136:139]\n"
"ds_read_b128 a[88:91],v113 offset:6144\n"
"ds_read_b128 a[92:95],v113 offset:7168\n"
"v_mfma_f32_16x16x32_bf16 v[200:203],a[104:107],v[88:91],v[200:203]\n"
"v_exp_f32 v119,v22\n"
"v_mfma_f32_16x16x32_bf16 v[232:235],a[104:107],v[92:95],v[232:235]\n"
"buffer_store_dwordx4 a[188:191],v112,s[24:27],s3 offen offset:3072 sc1\n"
"v_mfma_f32_16x16x32_bf16 v[136:139],a[104:107],v[80:83],v[136:139]\n"
"v_exp_f32 v120,v23\n"
"v_mfma_f32_16x16x32_bf16 v[168:171],a[104:107],v[84:87],v[168:171]\n"
"s_or_b32 s3,s3,0x3c000\n"
"v_cvt_pk_bf16_f32 v74,v122,v123\n"
"v_cvt_pk_bf16_f32 v75,v119,v120\n"
"v_mfma_f32_16x16x32_bf16 a[132:135],a[144:147],v[84:87],a[132:135]\n"
"v_exp_f32 v122,v24\n"
"v_mfma_f32_16x16x32_bf16 v[172:175],a[108:111],v[84:87],v[172:175]\n"
"v_or3_b32 v118,v118,v71,v72\n"
"v_or3_b32 v118,v118,v73,v74\n"
"v_mfma_f32_16x16x32_bf16 v[204:207],a[108:111],v[88:91],v[204:207]\n"
"v_exp_f32 v123,v25\n"
"v_mfma_f32_16x16x32_bf16 v[236:239],a[108:111],v[92:95],v[236:239]\n"
"v_cvt_pk_bf16_f32 v76,v122,v123\n"
"v_or3_b32 v118,v118,v75,v76\n"
"v_mfma_f32_16x16x32_bf16 v[140:143],a[108:111],v[80:83],v[140:143]\n"
"v_exp_f32 v119,v26\n"
"v_mfma_f32_16x16x32_bf16 a[128:131],a[144:147],v[80:83],a[128:131]\n"
"v_mfma_f32_16x16x32_bf16 v[144:147],a[112:115],v[80:83],v[144:147]\n"
"v_exp_f32 v120,v27\n"
"v_mfma_f32_16x16x32_bf16 v[176:179],a[112:115],v[84:87],v[176:179]\n"
"v_cvt_pk_bf16_f32 v77,v119,v120\n"
"v_mfma_f32_16x16x32_bf16 v[208:211],a[112:115],v[88:91],v[208:211]\n"
"v_exp_f32 v122,v28\n"
"v_mfma_f32_16x16x32_bf16 v[240:243],a[112:115],v[92:95],v[240:243]\n"
"v_mfma_f32_16x16x32_bf16 v[244:247],a[116:119],v[92:95],v[244:247]\n"
"v_exp_f32 v123,v29\n"
"v_mfma_f32_16x16x32_bf16 v[148:151],a[116:119],v[80:83],v[148:151]\n"
"v_cvt_pk_bf16_f32 v78,v122,v123\n"
"v_or3_b32 v118,v118,v77,v78\n"
"v_mfma_f32_16x16x32_bf16 v[180:183],a[116:119],v[84:87],v[180:183]\n"
"v_exp_f32 v119,v30\n"
"v_mfma_f32_16x16x32_bf16 v[212:215],a[116:119],v[88:91],v[212:215]\n"
"buffer_store_dwordx4 a[236:239],v112,s[24:27],s56 offen offset:3072 sc1\n"
"v_mfma_f32_16x16x32_bf16 v[216:219],a[120:123],v[88:91],v[216:219]\n"
"v_exp_f32 v120,v31\n"
"v_mfma_f32_16x16x32_bf16 v[248:251],a[120:123],v[92:95],v[248:251]\n"
"v_cvt_pk_bf16_f32 v79,v119,v120\n"
"v_or_b32 v118,v118,v79\n"
"v_mfma_f32_16x16x32_bf16 v[152:155],a[120:123],v[80:83],v[152:155]\n"
"v_and_b32 v118,0x40004000,v118\n"
"v_cmp_ne_u32 vcc,0,v118\n"
"v_mfma_f32_16x16x32_bf16 v[184:187],a[120:123],v[84:87],v[184:187]\n"
"v_mfma_f32_16x16x32_bf16 v[188:191],a[124:127],v[84:87],v[188:191]\n"
"v_mfma_f32_16x16x32_bf16 v[220:223],a[124:127],v[88:91],v[220:223]\n"
"v_mfma_f32_16x16x32_bf16 v[252:255],a[124:127],v[92:95],v[252:255]\n"
"v_mfma_f32_16x16x32_bf16 v[156:159],a[124:127],v[80:83],v[156:159]\n"
"s_cbranch_vccnz .Lfk_cs_k2\n"
".Lfk_cr_k2:\n"
"s_waitcnt vmcnt(13) lgkmcnt(0)\n"
"s_barrier\n"
"s_branch .Lfk_g1\n"
".Lfk_winA:\n"
"v_max3_f32 v118,v32,v33,v34\n"
"v_max3_f32 v119,v35,v36,v37\n"
"v_max3_f32 v119,v119,v38,v39\n"
"v_max3_f32 v118,v118,v40,v41\n"
"v_max3_f32 v119,v119,v42,v43\n"
"v_max3_f32 v118,v118,v44,v45\n"
"v_max3_f32 v119,v119,v46,v47\n"
"v_max3_f32 v118,v118,v48,v49\n"
"v_max3_f32 v119,v119,v50,v51\n"
"v_max3_f32 v118,v118,v52,v53\n"
"v_max3_f32 v119,v119,v54,v55\n"
"v_max3_f32 v118,v118,v56,v57\n"
"v_max3_f32 v119,v119,v58,v59\n"
"v_max3_f32 v118,v118,v60,v61\n"
"v_max3_f32 v119,v119,v62,v63\n"
"v_max_f32 v118,v118,v119\n"
"v_cmp_lt_f32 vcc,s93,v118\n"
"s_nop 0\n"
"s_cbranch_vccnz .Lfk_cs_x2\n"
".Lfk_cr_x2:\n"
"s_mov_b32 s64,s61\n"
"s_cmp_eq_u32 s52,3\n"
"s_cselect_b32 s67,172032,0\n"
"v_mfma_f32_16x16x32_bf16 v[8:11],a[64:67],a[16:19],v[100:103]\n"
"s_swappc_b64 s[94:95],s[20:21]\n"
"ds_read_b128 a[96:99],v114 offset:24576\n"
"ds_read_b128 a[100:103],v114 offset:25600\n"
"v_mfma_f32_16x16x32_bf16 v[8:11],a[68:71],a[20:23],v[8:11]\n"
"ds_read_b128 a[104:107],v114 offset:26624\n"
"ds_read_b128 a[108:111],v114 offset:27648\n"
"v_mfma_f32_16x16x32_bf16 v[8:11],a[72:75],a[24:27],v[8:11]\n"
"ds_read_b128 a[112:115],v114 offset:28672\n"
"ds_read_b128 a[116:119],v114 offset:29696\n"
"v_mfma_f32_16x16x32_bf16 v[8:11],a[76:79],a[28:31],v[8:11]\n"
"ds_read_b128 a[120:123],v114 offset:30720\n"
"ds_read_b128 a[124:127],v114 offset:31744\n"
"v_mfma_f32_16x16x32_bf16 v[12:15],a[92:95],a[28:31],v[100:103]\n"
"v_add_f32 v0,0x80000000,v126\n"
"v_min_f32 v0,v0,v96\n"
"v_mfma_f32_16x16x32_bf16 v[12:15],a[88:91],a[24:27],v[12:15]\n"
"v_add_f32 v1,0xf149f2ca,v126\n"
"v_min_f32 v1,v1,v97\n"
"v_mfma_f32_16x16x32_bf16 v[12:15],a[84:87],a[20:23],v[12:15]\n"
"v_add_f32 v2,0xf1c9f2ca,v126\n"
"v_min_f32 v2,v2,v98\n"
"v_mfma_f32_16x16x32_bf16 v[12:15],a[80:83],a[16:19],v[12:15]\n"
"v_add_f32 v3,0xf2177617,v126\n"
"v_min_f32 v3,v3,v99\n"
"v_mfma_f32_16x16x32_bf16 v[20:23],a[80:83],a[32:35],v[104:107]\n"
"v_add_f32 v4,0xf349f2ca,v126\n"
"v_min_f32 v4,v4,v96\n"
"v_mfma_f32_16x16x32_bf16 v[20:23],a[84:87],a[36:39],v[20:23]\n"
"v_add_f32 v5,0xf35691f6,v126\n"
"v_min_f32 v5,v5,v97\n"
"v_mfma_f32_16x16x32_bf16 v[20:23],a[88:91],a[40:43],v[20:23]\n"
"v_add_f32 v6,0xf3633123,v126\n"
"v_min_f32 v6,v6,v98\n"
"v_mfma_f32_16x16x32_bf16 v[20:23],a[92:95],a[44:47],v[20:23]\n"
"v_add_f32 v7,0xf36fd050,v126\n"
"v_min_f32 v7,v7,v99\n"
"v_mfma_f32_16x16x32_bf16 v[16:19],a[76:79],a[44:47],v[104:107]\n"
"s_add_u32 s101,s101,4\n"
"v_exp_f32 v32,v32\n"
"v_mfma_f32_16x16x32_bf16 v[16:19],a[72:75],a[40:43],v[16:19]\n"
"s_add_u32 m0,s67,0\n"
"v_exp_f32 v33,v33\n"
"v_mfma_f32_16x16x32_bf16 v[16:19],a[68:71],a[36:39],v[16:19]\n"
"v_exp_f32 v34,v34\n"
"v_mfma_f32_16x16x32_bf16 v[16:19],a[64:67],a[32:35],v[16:19]\n"
"v_exp_f32 v35,v35\n"
"v_mfma_f32_16x16x32_bf16 v[24:27],a[64:67],a[48:51],v[108:111]\n"
"v_exp_f32 v36,v36\n"
"v_mfma_f32_16x16x32_bf16 v[24:27],a[68:71],a[52:55],v[24:27]\n"
"v_exp_f32 v37,v37\n"
"v_mfma_f32_16x16x32_bf16 v[24:27],a[72:75],a[56:59],v[24:27]\n"
"v_exp_f32 v38,v38\n"
"v_mfma_f32_16x16x32_bf16 v[24:27],a[76:79],a[60:63],v[24:27]\n"
"v_exp_f32 v39,v39\n"
"v_mfma_f32_16x16x32_bf16 v[28:31],a[92:95],a[60:63],v[108:111]\n"
"v_exp_f32 v40,v40\n"
"v_mfma_f32_16x16x32_bf16 v[28:31],a[88:91],a[56:59],v[28:31]\n"
"v_exp_f32 v41,v41\n"
"v_mfma_f32_16x16x32_bf16 v[28:31],a[84:87],a[52:55],v[28:31]\n"
"v_exp_f32 v42,v42\n"
"v_mfma_f32_16x16x32_bf16 v[28:31],a[80:83],a[48:51],v[28:31]\n"
"v_exp_f32 v43,v43\n"
"v_mfma_f32_16x16x32_bf16 v[4:7],a[80:83],a[0:3],v[4:7]\n"
"v_exp_f32 v44,v44\n"
"v_mfma_f32_16x16x32_bf16 v[4:7],a[84:87],a[4:7],v[4:7]\n"
"v_exp_f32 v45,v45\n"
"v_mfma_f32_16x16x32_bf16 v[4:7],a[88:91],a[8:11],v[4:7]\n"
"buffer_load_dwordx4 v112,s[24:27],s64 offen offset:0 sc1 lds\n"
"v_mfma_f32_16x16x32_bf16 v[4:7],a[92:95],a[12:15],v[4:7]\n"
"v_exp_f32 v46,v46\n"
"v_mfma_f32_16x16x32_bf16 v[0:3],a[76:79],a[12:15],v[0:3]\n"
"buffer_load_dwordx4 v112,s[24:27],s64 offen offset:1024 sc1 lds\n"
"v_mfma_f32_16x16x32_bf16 v[0:3],a[72:75],a[8:11],v[0:3]\n"
"v_exp_f32 v47,v47\n"
"v_mfma_f32_16x16x32_bf16 v[0:3],a[68:71],a[4:7],v[0:3]\n"
"buffer_load_dwordx4 v112,s[24:27],s64 offen offset:2048 sc1 lds\n"
"v_mfma_f32_16x16x32_bf16 v[0:3],a[64:67],a[0:3],v[0:3]\n"
"s_waitcnt lgkmcnt(0)\n"
"v_exp_f32 v48,v48\n"
"v_mfma_f32_16x16x32_bf16 v[128:131],a[96:99],v[64:67],v[128:131]\n"
"v_exp_f32 v49,v49\n"
"v_mfma_f32_16x16x32_bf16 v[160:163],a[96:99],v[68:71],v[160:163]\n"
"ds_read_b128 a[64:67],v113 offset:16384\n"
"ds_read_b128 a[68:71],v113 offset:17408\n"
"v_mfma_f32_16x16x32_bf16 v[192:195],a[96:99],v[72:75],v[192:195]\n"
"v_exp_f32 v50,v50\n"
"v_mfma_f32_16x16x32_bf16 v[224:227],a[96:99],v[76:79],v[224:227]\n"
"ds_read_b128 a[72:75],v113 offset:18432\n"
"ds_read_b128 a[76:79],v113 offset:19456\n"
"v_mfma_f32_16x16x32_bf16 a[140:143],a[144:147],v[76:79],a[140:143]\n"
"v_exp_f32 v51,v51\n"
"v_mfma_f32_16x16x32_bf16 v[228:231],a[100:103],v[76:79],v[228:231]\n"
"ds_read_b128 a[80:83],v113 offset:20480\n"
"ds_read_b128 a[84:87],v113 offset:21504\n"
"v_mfma_f32_16x16x32_bf16 v[132:135],a[100:103],v[64:67],v[132:135]\n"
"v_exp_f32 v52,v52\n"
"v_mfma_f32_16x16x32_bf16 v[164:167],a[100:103],v[68:71],v[164:167]\n"
"ds_read_b128 a[88:91],v113 offset:22528\n"
"ds_read_b128 a[92:95],v113 offset:23552\n"
"v_mfma_f32_16x16x32_bf16 v[196:199],a[100:103],v[72:75],v[196:199]\n"
"v_exp_f32 v53,v53\n"
"v_mfma_f32_16x16x32_bf16 a[136:139],a[144:147],v[72:75],a[136:139]\n"
"s_waitcnt vmcnt(14)\n"
"ds_add_u32 v124,a152\n"
"v_mfma_f32_16x16x32_bf16 v[200:203],a[104:107],v[72:75],v[200:203]\n"
"v_exp_f32 v54,v54\n"
"v_mfma_f32_16x16x32_bf16 v[232:235],a[104:107],v[76:79],v[232:235]\n"
"buffer_load_dwordx4 v112,s[24:27],s64 offen offset:3072 sc1 lds\n"
"v_mfma_f32_16x16x32_bf16 v[136:139],a[104:107],v[64:67],v[136:139]\n"
"s_add_u32 m0,s67,4096\n"
"v_exp_f32 v55,v55\n"
"v_mfma_f32_16x16x32_bf16 v[168:171],a[104:107],v[68:71],v[168:171]\n"
"buffer_load_dwordx4 v117,s[24:27],s64 offen offset:0 sc1 lds\n"
"v_mfma_f32_16x16x32_bf16 a[132:135],a[144:147],v[68:71],a[132:135]\n"
"v_exp_f32 v56,v56\n"
"v_mfma_f32_16x16x32_bf16 v[172:175],a[108:111],v[68:71],v[172:175]\n"
"buffer_load_dwordx4 v117,s[24:27],s64 offen offset:1024 sc1 lds\n"
"v_mfma_f32_16x16x32_bf16 v[204:207],a[108:111],v[72:75],v[204:207]\n"
"v_exp_f32 v57,v57\n"
"v_mfma_f32_16x16x32_bf16 v[236:239],a[108:111],v[76:79],v[236:239]\n"
"buffer_load_dwordx4 v117,s[24:27],s64 offen offset:2048 sc1 lds\n"
"v_mfma_f32_16x16x32_bf16 v[140:143],a[108:111],v[64:67],v[140:143]\n"
"v_exp_f32 v58,v58\n"
"v_mfma_f32_16x16x32_bf16 a[128:131],a[144:147],v[64:67],a[128:131]\n"
"v_cvt_pk_bf16_f32 v80,v32,v33\n"
"v_cvt_pk_bf16_f32 v81,v34,v35\n"
"v_mfma_f32_16x16x32_bf16 v[144:147],a[112:115],v[64:67],v[144:147]\n"
"v_exp_f32 v59,v59\n"
"v_mfma_f32_16x16x32_bf16 v[176:179],a[112:115],v[68:71],v[176:179]\n"
"v_cvt_pk_bf16_f32 v82,v36,v37\n"
"v_cvt_pk_bf16_f32 v83,v38,v39\n"
"v_mfma_f32_16x16x32_bf16 v[208:211],a[112:115],v[72:75],v[208:211]\n"
"v_exp_f32 v60,v60\n"
"v_mfma_f32_16x16x32_bf16 v[240:243],a[112:115],v[76:79],v[240:243]\n"
"buffer_load_dwordx4 v117,s[24:27],s64 offen offset:3072 sc1 lds\n"
"v_mfma_f32_16x16x32_bf16 v[244:247],a[116:119],v[76:79],v[244:247]\n"
"s_add_u32 s64,s64,16384\n"
"v_exp_f32 v61,v61\n"
"v_mfma_f32_16x16x32_bf16 v[148:151],a[116:119],v[64:67],v[148:151]\n"
"s_andn2_b32 m0,s55,16384\n"
"s_add_u32 s54,s54,s22\n"
"v_cvt_pk_bf16_f32 v85,v42,v43\n"
"v_mfma_f32_16x16x32_bf16 v[180:183],a[116:119],v[68:71],v[180:183]\n"
"v_exp_f32 v62,v62\n"
"v_mfma_f32_16x16x32_bf16 v[212:215],a[116:119],v[72:75],v[212:215]\n"
"ds_read_b32 v125,v124\n"
"v_cvt_pk_bf16_f32 v84,v40,v41\n"
"v_mfma_f32_16x16x32_bf16 v[216:219],a[120:123],v[72:75],v[216:219]\n"
"v_exp_f32 v63,v63\n"
"v_mfma_f32_16x16x32_bf16 v[248:251],a[120:123],v[76:79],v[248:251]\n"
"buffer_load_dwordx4 v115,s[32:35],s54 offen sc1 lds\n"
"v_mfma_f32_16x16x32_bf16 v[152:155],a[120:123],v[64:67],v[152:155]\n"
"buffer_store_dwordx4 a[240:243],v112,s[24:27],s56 offen offset:0 sc1\n"
"v_mfma_f32_16x16x32_bf16 v[184:187],a[120:123],v[68:71],v[184:187]\n"
"v_cvt_pk_bf16_f32 v86,v44,v45\n"
"v_cvt_pk_bf16_f32 v87,v46,v47\n"
"v_mfma_f32_16x16x32_bf16 v[188:191],a[124:127],v[68:71],v[188:191]\n"
"s_waitcnt lgkmcnt(0)\n"
"v_cvt_pk_bf16_f32 v88,v48,v49\n"
"v_cvt_pk_bf16_f32 v89,v50,v51\n"
"v_mfma_f32_16x16x32_bf16 v[220:223],a[124:127],v[72:75],v[220:223]\n"
"v_cvt_pk_bf16_f32 v90,v52,v53\n"
"v_cvt_pk_bf16_f32 v91,v54,v55\n"
"v_mfma_f32_16x16x32_bf16 v[252:255],a[124:127],v[76:79],v[252:255]\n"
"v_cvt_pk_bf16_f32 v92,v56,v57\n"
"v_cvt_pk_bf16_f32 v93,v58,v59\n"
"v_mfma_f32_16x16x32_bf16 v[156:159],a[124:127],v[64:67],v[156:159]\n"
"v_cvt_pk_bf16_f32 v94,v60,v61\n"
"v_cvt_pk_bf16_f32 v95,v62,v63\n"
"v_max3_f32 v118,v8,v9,v10\n"
"v_max3_f32 v118,v118,v11,v12\n"
"v_max3_f32 v118,v118,v13,v14\n"
"v_max3_f32 v118,v118,v15,v20\n"
"v_max3_f32 v118,v118,v21,v22\n"
"v_max3_f32 v118,v118,v23,v16\n"
"v_max3_f32 v118,v118,v17,v18\n"
"v_max3_f32 v118,v118,v19,v24\n"
"v_max3_f32 v118,v118,v25,v26\n"
"v_max3_f32 v118,v118,v27,v28\n"
"v_max3_f32 v118,v118,v29,v30\n"
"v_max3_f32 v118,v118,v31,v4\n"
"v_max3_f32 v118,v118,v5,v6\n"
"v_max3_f32 v118,v118,v7,v0\n"
"v_max3_f32 v118,v118,v1,v2\n"
"v_max_f32 v118,v118,v3\n"
"v_cmp_lt_f32 vcc,s93,v118\n"
"s_nop 0\n"
"s_cbranch_vccnz .Lfk_cs_u3\n"
".Lfk_cr_u3:\n"
"v_cmp_gt_u32 vcc,s101,v125\n"
"s_nop 0\n"
"s_cbranch_vccnz .Lfk_cs_mu3\n"
".Lfk_cr_mu3:\n"
"v_mfma_f32_16x16x32_bf16 v[48:51],a[64:67],a[32:35],v[104:107]\n"
"ds_read_b128 a[96:99],v114 offset:57344\n"
"ds_read_b128 a[100:103],v114 offset:58368\n"
"v_mfma_f32_16x16x32_bf16 v[48:51],a[68:71],a[36:39],v[48:51]\n"
"ds_read_b128 a[104:107],v114 offset:59392\n"
"ds_read_b128 a[108:111],v114 offset:60416\n"
"v_mfma_f32_16x16x32_bf16 v[48:51],a[72:75],a[40:43],v[48:51]\n"
"ds_read_b128 a[112:115],v114 offset:61440\n"
"ds_read_b128 a[116:119],v114 offset:62464\n"
"v_mfma_f32_16x16x32_bf16 v[48:51],a[76:79],a[44:47],v[48:51]\n"
"ds_read_b128 a[120:123],v114 offset:63488\n"
"ds_read_b128 a[124:127],v114 offset:64512\n"
"v_mfma_f32_16x16x32_bf16 v[52:55],a[92:95],a[44:47],v[104:107]\n"
"ds_read_b128 a[0:3],v116 offset:40960\n"
"ds_read_b128 a[4:7],v116 offset:41984\n"
"v_mfma_f32_16x16x32_bf16 v[52:55],a[88:91],a[40:43],v[52:55]\n"
"ds_read_b128 a[8:11],v116 offset:43008\n"
"ds_read_b128 a[12:15],v116 offset:44032\n"
"v_mfma_f32_16x16x32_bf16 v[52:55],a[84:87],a[36:39],v[52:55]\n"
"v_add_f32 v40,0x80000000,v126\n"
"v_min_f32 v40,v40,v100\n"
"v_mfma_f32_16x16x32_bf16 v[52:55],a[80:83],a[32:35],v[52:55]\n"
"v_add_f32 v41,0xf149f2ca,v126\n"
"v_min_f32 v41,v41,v101\n"
"v_mfma_f32_16x16x32_bf16 v[60:63],a[80:83],a[48:51],v[108:111]\n"
"v_add_f32 v42,0xf1c9f2ca,v126\n"
"v_min_f32 v42,v42,v102\n"
"v_mfma_f32_16x16x32_bf16 v[60:63],a[84:87],a[52:55],v[60:63]\n"
"v_add_f32 v43,0xf2177617,v126\n"
"v_min_f32 v43,v43,v103\n"
"v_mfma_f32_16x16x32_bf16 v[60:63],a[88:91],a[56:59],v[60:63]\n"
"v_add_f32 v44,0xf349f2ca,v126\n"
"v_min_f32 v44,v44,v100\n"
"v_mfma_f32_16x16x32_bf16 v[60:63],a[92:95],a[60:63],v[60:63]\n"
"v_add_f32 v45,0xf35691f6,v126\n"
"v_min_f32 v45,v45,v101\n"
"v_mfma_f32_16x16x32_bf16 v[56:59],a[76:79],a[60:63],v[108:111]\n"
"v_add_f32 v46,0xf3633123,v126\n"
"v_min_f32 v46,v46,v102\n"
"v_mfma_f32_16x16x32_bf16 v[56:59],a[72:75],a[56:59],v[56:59]\n"
"v_add_f32 v47,0xf36fd050,v126\n"
"v_min_f32 v47,v47,v103\n"
"v_mfma_f32_16x16x32_bf16 v[56:59],a[68:71],a[52:55],v[56:59]\n"
"s_add_u32 s101,s101,4\n"
"v_exp_f32 v0,v0\n"
"v_mfma_f32_16x16x32_bf16 v[56:59],a[64:67],a[48:51],v[56:59]\n"
"s_add_u32 m0,s55,4096\n"
"v_exp_f32 v1,v1\n"
"v_mfma_f32_16x16x32_bf16 v[40:43],a[64:67],a[16:19],v[40:43]\n"
"s_add_u32 s54,s54,s22\n"
"v_exp_f32 v2,v2\n"
"v_mfma_f32_16x16x32_bf16 v[40:43],a[68:71],a[20:23],v[40:43]\n"
"v_exp_f32 v3,v3\n"
"v_mfma_f32_16x16x32_bf16 v[40:43],a[72:75],a[24:27],v[40:43]\n"
"v_exp_f32 v4,v4\n"
"v_mfma_f32_16x16x32_bf16 v[40:43],a[76:79],a[28:31],v[40:43]\n"
"v_exp_f32 v5,v5\n"
"v_mfma_f32_16x16x32_bf16 v[44:47],a[92:95],a[28:31],v[44:47]\n"
"v_exp_f32 v6,v6\n"
"v_mfma_f32_16x16x32_bf16 v[44:47],a[88:91],a[24:27],v[44:47]\n"
"v_exp_f32 v7,v7\n"
"v_mfma_f32_16x16x32_bf16 v[44:47],a[84:87],a[20:23],v[44:47]\n"
"v_exp_f32 v8,v8\n"
"v_mfma_f32_16x16x32_bf16 v[44:47],a[80:83],a[16:19],v[44:47]\n"
"s_waitcnt lgkmcnt(4)\n"
"v_exp_f32 v9,v9\n"
"v_mfma_f32_16x16x32_bf16 v[128:131],a[96:99],v[80:83],v[128:131]\n"
"v_exp_f32 v10,v10\n"
"v_mfma_f32_16x16x32_bf16 v[160:163],a[96:99],v[84:87],v[160:163]\n"
"v_exp_f32 v11,v11\n"
"v_mfma_f32_16x16x32_bf16 v[192:195],a[96:99],v[88:91],v[192:195]\n"
"v_exp_f32 v12,v12\n"
"v_mfma_f32_16x16x32_bf16 v[224:227],a[96:99],v[92:95],v[224:227]\n"
"v_exp_f32 v13,v13\n"
"v_mfma_f32_16x16x32_bf16 a[140:143],a[144:147],v[92:95],a[140:143]\n"
"v_exp_f32 v14,v14\n"
"v_mfma_f32_16x16x32_bf16 v[228:231],a[100:103],v[92:95],v[228:231]\n"
"v_exp_f32 v15,v15\n"
"v_mfma_f32_16x16x32_bf16 v[132:135],a[100:103],v[80:83],v[132:135]\n"
"v_exp_f32 v16,v16\n"
"v_mfma_f32_16x16x32_bf16 v[164:167],a[100:103],v[84:87],v[164:167]\n"
"v_exp_f32 v17,v17\n"
"v_mfma_f32_16x16x32_bf16 v[196:199],a[100:103],v[88:91],v[196:199]\n"
"v_exp_f32 v18,v18\n"
"v_mfma_f32_16x16x32_bf16 a[136:139],a[144:147],v[88:91],a[136:139]\n"
"v_exp_f32 v19,v19\n"
"v_mfma_f32_16x16x32_bf16 v[200:203],a[104:107],v[88:91],v[200:203]\n"
"v_exp_f32 v20,v20\n"
"v_mfma_f32_16x16x32_bf16 v[232:235],a[104:107],v[92:95],v[232:235]\n"
"v_exp_f32 v21,v21\n"
"v_mfma_f32_16x16x32_bf16 v[136:139],a[104:107],v[80:83],v[136:139]\n"
"ds_read_b128 a[64:67],v113 offset:49152\n"
"ds_read_b128 a[68:71],v113 offset:50176\n"
"v_mfma_f32_16x16x32_bf16 v[168:171],a[104:107],v[84:87],v[168:171]\n"
"v_exp_f32 v22,v22\n"
"v_mfma_f32_16x16x32_bf16 a[132:135],a[144:147],v[84:87],a[132:135]\n"
"v_exp_f32 v23,v23\n"
"v_mfma_f32_16x16x32_bf16 v[172:175],a[108:111],v[84:87],v[172:175]\n"
"ds_read_b128 a[72:75],v113 offset:51200\n"
"ds_read_b128 a[76:79],v113 offset:52224\n"
"v_mfma_f32_16x16x32_bf16 v[204:207],a[108:111],v[88:91],v[204:207]\n"
"v_exp_f32 v24,v24\n"
"v_mfma_f32_16x16x32_bf16 v[236:239],a[108:111],v[92:95],v[236:239]\n"
"ds_read_b128 a[80:83],v113 offset:53248\n"
"ds_read_b128 a[84:87],v113 offset:54272\n"
"v_mfma_f32_16x16x32_bf16 v[140:143],a[108:111],v[80:83],v[140:143]\n"
"v_exp_f32 v25,v25\n"
"v_mfma_f32_16x16x32_bf16 a[128:131],a[144:147],v[80:83],a[128:131]\n"
"v_exp_f32 v26,v26\n"
"v_mfma_f32_16x16x32_bf16 v[144:147],a[112:115],v[80:83],v[144:147]\n"
"ds_read_b128 a[88:91],v113 offset:55296\n"
"ds_read_b128 a[92:95],v113 offset:56320\n"
"v_mfma_f32_16x16x32_bf16 v[176:179],a[112:115],v[84:87],v[176:179]\n"
"v_exp_f32 v27,v27\n"
"v_mfma_f32_16x16x32_bf16 v[208:211],a[112:115],v[88:91],v[208:211]\n"
"s_waitcnt vmcnt(11)\n"
"ds_add_u32 v124,a152\n"
"v_mfma_f32_16x16x32_bf16 v[240:243],a[112:115],v[92:95],v[240:243]\n"
"v_exp_f32 v28,v28\n"
"v_mfma_f32_16x16x32_bf16 v[244:247],a[116:119],v[92:95],v[244:247]\n"
"v_exp_f32 v29,v29\n"
"v_mfma_f32_16x16x32_bf16 v[148:151],a[116:119],v[80:83],v[148:151]\n"
"buffer_load_dwordx4 v115,s[32:35],s54 offen sc1 lds\n"
"v_mfma_f32_16x16x32_bf16 v[180:183],a[116:119],v[84:87],v[180:183]\n"
"s_add_u32 m0,s67,16384\n"
"v_exp_f32 v30,v30\n"
"v_mfma_f32_16x16x32_bf16 v[212:215],a[116:119],v[88:91],v[212:215]\n"
"ds_read_b32 v125,v124\n"
"v_cvt_pk_bf16_f32 v64,v0,v1\n"
"v_mfma_f32_16x16x32_bf16 v[216:219],a[120:123],v[88:91],v[216:219]\n"
"v_exp_f32 v31,v31\n"
"v_mfma_f32_16x16x32_bf16 v[248:251],a[120:123],v[92:95],v[248:251]\n"
"buffer_load_dwordx4 v112,s[24:27],s64 offen offset:0 sc1 lds\n"
"v_mfma_f32_16x16x32_bf16 v[152:155],a[120:123],v[80:83],v[152:155]\n"
"buffer_load_dwordx4 v112,s[24:27],s64 offen offset:1024 sc1 lds\n"
"v_mfma_f32_16x16x32_bf16 v[184:187],a[120:123],v[84:87],v[184:187]\n"
"buffer_load_dwordx4 v112,s[24:27],s64 offen offset:2048 sc1 lds\n"
"v_mfma_f32_16x16x32_bf16 v[188:191],a[124:127],v[84:87],v[188:191]\n"
"s_waitcnt lgkmcnt(0)\n"
"buffer_load_dwordx4 v112,s[24:27],s64 offen offset:3072 sc1 lds\n"
"v_mfma_f32_16x16x32_bf16 v[220:223],a[124:127],v[88:91],v[220:223]\n"
"s_add_u32 m0,s67,20480\n"
"v_cvt_pk_bf16_f32 v65,v2,v3\n"
"v_cvt_pk_bf16_f32 v66,v4,v5\n"
"v_mfma_f32_16x16x32_bf16 v[252:255],a[124:127],v[92:95],v[252:255]\n"
"buffer_load_dwordx4 v117,s[24:27],s64 offen offset:0 sc1 lds\n"
"v_mfma_f32_16x16x32_bf16 v[156:159],a[124:127],v[80:83],v[156:159]\n"
"buffer_load_dwordx4 v117,s[24:27],s64 offen offset:1024 sc1 lds\n"
"buffer_load_dwordx4 v117,s[24:27],s64 offen offset:2048 sc1 lds\n"
"buffer_load_dwordx4 v117,s[24:27],s64 offen offset:3072 sc1 lds\n"
"buffer_store_dwordx4 a[244:247],v112,s[24:27],s56 offen offset:1024 sc1\n"
"s_add_u32 s64,s64,16384\n"
"v_cvt_pk_bf16_f32 v67,v6,v7\n"
"v_cvt_pk_bf16_f32 v68,v8,v9\n"
"v_cvt_pk_bf16_f32 v69,v10,v11\n"
"v_cvt_pk_bf16_f32 v70,v12,v13\n"
"v_cvt_pk_bf16_f32 v71,v14,v15\n"
"v_cvt_pk_bf16_f32 v72,v16,v17\n"
"v_cvt_pk_bf16_f32 v73,v18,v19\n"
"v_cvt_pk_bf16_f32 v74,v20,v21\n"
"v_cvt_pk_bf16_f32 v75,v22,v23\n"
"v_cvt_pk_bf16_f32 v76,v24,v25\n"
"v_cvt_pk_bf16_f32 v77,v26,v27\n"
"v_cvt_pk_bf16_f32 v78,v28,v29\n"
"v_cvt_pk_bf16_f32 v79,v30,v31\n"
"v_max3_f32 v118,v48,v49,v50\n"
"v_max3_f32 v118,v118,v51,v52\n"
"v_max3_f32 v118,v118,v53,v54\n"
"v_max3_f32 v118,v118,v55,v60\n"
"v_max3_f32 v118,v118,v61,v62\n"
"v_max3_f32 v118,v118,v63,v56\n"
"v_max3_f32 v118,v118,v57,v58\n"
"v_max3_f32 v118,v118,v59,v40\n"
"v_max3_f32 v118,v118,v41,v42\n"
"v_max3_f32 v118,v118,v43,v44\n"
"v_max3_f32 v118,v118,v45,v46\n"
"v_max_f32 v118,v118,v47\n"
"v_cmp_lt_f32 vcc,s93,v118\n"
"s_nop 0\n"
"s_cbranch_vccnz .Lfk_cs_u0\n"
".Lfk_cr_u0:\n"
"v_cmp_gt_u32 vcc,s101,v125\n"
"s_nop 0\n"
"s_cbranch_vccnz .Lfk_cs_mu0\n"
".Lfk_cr_mu0:\n"
"v_mfma_f32_16x16x32_bf16 v[24:27],a[64:67],a[48:51],v[108:111]\n"
"ds_read_b128 a[96:99],v114 offset:40960\n"
"ds_read_b128 a[100:103],v114 offset:41984\n"
"v_mfma_f32_16x16x32_bf16 v[24:27],a[68:71],a[52:55],v[24:27]\n"
"ds_read_b128 a[104:107],v114 offset:43008\n"
"ds_read_b128 a[108:111],v114 offset:44032\n"
"v_mfma_f32_16x16x32_bf16 v[24:27],a[72:75],a[56:59],v[24:27]\n"
"ds_read_b128 a[112:115],v114 offset:45056\n"
"ds_read_b128 a[116:119],v114 offset:46080\n"
"v_mfma_f32_16x16x32_bf16 v[24:27],a[76:79],a[60:63],v[24:27]\n"
"ds_read_b128 a[120:123],v114 offset:47104\n"
"ds_read_b128 a[124:127],v114 offset:48128\n"
"v_mfma_f32_16x16x32_bf16 v[28:31],a[92:95],a[60:63],v[108:111]\n"
"ds_read_b128 a[16:19],v116 offset:8192\n"
"ds_read_b128 a[20:23],v116 offset:9216\n"
"v_mfma_f32_16x16x32_bf16 v[28:31],a[88:91],a[56:59],v[28:31]\n"
"ds_read_b128 a[24:27],v116 offset:10240\n"
"ds_read_b128 a[28:31],v116 offset:11264\n"
"v_mfma_f32_16x16x32_bf16 v[28:31],a[84:87],a[52:55],v[28:31]\n"
"v_add_f32 v16,0x80000000,v126\n"
"v_min_f32 v16,v16,v104\n"
"v_mfma_f32_16x16x32_bf16 v[28:31],a[80:83],a[48:51],v[28:31]\n"
"v_add_f32 v17,0xf149f2ca,v126\n"
"v_add_f32 v18,0xf1c9f2ca,v126\n"
"v_add_f32 v19,0xf2177617,v126\n"
"v_min_f32 v19,v19,v107\n"
"v_add_f32 v20,0xf349f2ca,v126\n"
"v_min_f32 v20,v20,v104\n"
"v_add_f32 v21,0xf35691f6,v126\n"
"v_min_f32 v21,v21,v105\n"
"v_add_f32 v22,0xf3633123,v126\n"
"v_min_f32 v22,v22,v106\n"
"v_add_f32 v23,0xf36fd050,v126\n"
"v_min_f32 v23,v23,v107\n"
"v_min_f32 v18,v18,v106\n"
"v_min_f32 v17,v17,v105\n"
"v_mfma_f32_16x16x32_bf16 v[20:23],a[80:83],a[32:35],v[20:23]\n"
"s_add_u32 s101,s101,4\n"
"v_exp_f32 v40,v40\n"
"v_mfma_f32_16x16x32_bf16 v[20:23],a[84:87],a[36:39],v[20:23]\n"
"s_add_u32 m0,s55,8192\n"
"v_exp_f32 v41,v41\n"
"v_mfma_f32_16x16x32_bf16 v[20:23],a[88:91],a[40:43],v[20:23]\n"
"s_add_u32 s54,s54,s22\n"
"v_exp_f32 v42,v42\n"
"v_mfma_f32_16x16x32_bf16 v[20:23],a[92:95],a[44:47],v[20:23]\n"
"v_exp_f32 v43,v43\n"
"v_mfma_f32_16x16x32_bf16 v[16:19],a[76:79],a[44:47],v[16:19]\n"
"v_exp_f32 v44,v44\n"
"v_mfma_f32_16x16x32_bf16 v[16:19],a[72:75],a[40:43],v[16:19]\n"
"v_exp_f32 v45,v45\n"
"v_mfma_f32_16x16x32_bf16 v[16:19],a[68:71],a[36:39],v[16:19]\n"
"v_exp_f32 v46,v46\n"
"v_mfma_f32_16x16x32_bf16 v[16:19],a[64:67],a[32:35],v[16:19]\n"
"s_waitcnt lgkmcnt(4)\n"
"v_exp_f32 v47,v47\n"
"v_mfma_f32_16x16x32_bf16 v[128:131],a[96:99],v[64:67],v[128:131]\n"
"v_exp_f32 v48,v48\n"
"v_mfma_f32_16x16x32_bf16 v[160:163],a[96:99],v[68:71],v[160:163]\n"
"v_exp_f32 v49,v49\n"
"v_mfma_f32_16x16x32_bf16 v[192:195],a[96:99],v[72:75],v[192:195]\n"
"ds_read_b128 a[64:67],v113 offset:32768\n"
"ds_read_b128 a[68:71],v113 offset:33792\n"
"v_mfma_f32_16x16x32_bf16 v[224:227],a[96:99],v[76:79],v[224:227]\n"
"v_exp_f32 v50,v50\n"
"v_mfma_f32_16x16x32_bf16 a[140:143],a[144:147],v[76:79],a[140:143]\n"
"ds_read_b128 a[72:75],v113 offset:34816\n"
"ds_read_b128 a[76:79],v113 offset:35840\n"
"v_mfma_f32_16x16x32_bf16 v[228:231],a[100:103],v[76:79],v[228:231]\n"
"v_exp_f32 v51,v51\n"
"v_mfma_f32_16x16x32_bf16 v[132:135],a[100:103],v[64:67],v[132:135]\n"
"ds_read_b128 a[80:83],v113 offset:36864\n"
"ds_read_b128 a[84:87],v113 offset:37888\n"
"v_mfma_f32_16x16x32_bf16 v[164:167],a[100:103],v[68:71],v[164:167]\n"
"v_exp_f32 v52,v52\n"
"v_mfma_f32_16x16x32_bf16 v[196:199],a[100:103],v[72:75],v[196:199]\n"
"v_exp_f32 v53,v53\n"
"v_mfma_f32_16x16x32_bf16 a[136:139],a[144:147],v[72:75],a[136:139]\n"
"ds_read_b128 a[88:91],v113 offset:38912\n"
"ds_read_b128 a[92:95],v113 offset:39936\n"
"v_mfma_f32_16x16x32_bf16 v[200:203],a[104:107],v[72:75],v[200:203]\n"
"v_exp_f32 v54,v54\n"
"v_mfma_f32_16x16x32_bf16 v[232:235],a[104:107],v[76:79],v[232:235]\n"
"s_waitcnt vmcnt(11)\n"
"ds_add_u32 v124,a152\n"
"v_mfma_f32_16x16x32_bf16 v[136:139],a[104:107],v[64:67],v[136:139]\n"
"v_exp_f32 v55,v55\n"
"v_mfma_f32_16x16x32_bf16 v[168:171],a[104:107],v[68:71],v[168:171]\n"
"buffer_load_dwordx4 v115,s[32:35],s54 offen sc1 lds\n"
"v_mfma_f32_16x16x32_bf16 a[132:135],a[144:147],v[68:71],a[132:135]\n"
"s_add_u32 m0,s67,49152\n"
"v_exp_f32 v56,v56\n"
"v_mfma_f32_16x16x32_bf16 v[172:175],a[108:111],v[68:71],v[172:175]\n"
"buffer_load_dwordx4 v112,s[24:27],s64 offen offset:0 sc1 lds\n"
"v_mfma_f32_16x16x32_bf16 v[204:207],a[108:111],v[72:75],v[204:207]\n"
"v_exp_f32 v57,v57\n"
"v_mfma_f32_16x16x32_bf16 v[236:239],a[108:111],v[76:79],v[236:239]\n"
"buffer_load_dwordx4 v112,s[24:27],s64 offen offset:1024 sc1 lds\n"
"v_mfma_f32_16x16x32_bf16 v[140:143],a[108:111],v[64:67],v[140:143]\n"
"v_exp_f32 v58,v58\n"
"v_mfma_f32_16x16x32_bf16 a[128:131],a[144:147],v[64:67],a[128:131]\n"
"buffer_load_dwordx4 v112,s[24:27],s64 offen offset:2048 sc1 lds\n"
"v_mfma_f32_16x16x32_bf16 v[144:147],a[112:115],v[64:67],v[144:147]\n"
"v_exp_f32 v59,v59\n"
"v_mfma_f32_16x16x32_bf16 v[176:179],a[112:115],v[68:71],v[176:179]\n"
"buffer_load_dwordx4 v112,s[24:27],s64 offen offset:3072 sc1 lds\n"
"v_mfma_f32_16x16x32_bf16 v[208:211],a[112:115],v[72:75],v[208:211]\n"
"s_add_u32 m0,s67,53248\n"
"v_exp_f32 v60,v60\n"
"v_mfma_f32_16x16x32_bf16 v[240:243],a[112:115],v[76:79],v[240:243]\n"
"buffer_load_dwordx4 v117,s[24:27],s64 offen offset:0 sc1 lds\n"
"v_mfma_f32_16x16x32_bf16 v[244:247],a[116:119],v[76:79],v[244:247]\n"
"v_exp_f32 v61,v61\n"
"v_mfma_f32_16x16x32_bf16 v[148:151],a[116:119],v[64:67],v[148:151]\n"
"buffer_load_dwordx4 v117,s[24:27],s64 offen offset:1024 sc1 lds\n"
"v_mfma_f32_16x16x32_bf16 v[180:183],a[116:119],v[68:71],v[180:183]\n"
"v_exp_f32 v62,v62\n"
"v_mfma_f32_16x16x32_bf16 v[212:215],a[116:119],v[72:75],v[212:215]\n"
"ds_read_b32 v125,v124\n"
"v_cvt_pk_bf16_f32 v84,v40,v41\n"
"v_mfma_f32_16x16x32_bf16 v[216:219],a[120:123],v[72:75],v[216:219]\n"
"v_exp_f32 v63,v63\n"
"v_mfma_f32_16x16x32_bf16 v[248:251],a[120:123],v[76:79],v[248:251]\n"
"buffer_load_dwordx4 v117,s[24:27],s64 offen offset:2048 sc1 lds\n"
"v_mfma_f32_16x16x32_bf16 v[152:155],a[120:123],v[64:67],v[152:155]\n"
"buffer_load_dwordx4 v117,s[24:27],s64 offen offset:3072 sc1 lds\n"
"v_mfma_f32_16x16x32_bf16 v[184:187],a[120:123],v[68:71],v[184:187]\n"
"s_add_u32 s64,s64,16384\n"
"buffer_store_dwordx4 a[248:251],v112,s[24:27],s56 offen offset:2048 sc1\n"
"v_mfma_f32_16x16x32_bf16 v[188:191],a[124:127],v[68:71],v[188:191]\n"
"s_waitcnt lgkmcnt(0)\n"
"s_and_b32 s67,s67,8192\n"
"v_cvt_pk_bf16_f32 v85,v42,v43\n"
"v_mfma_f32_16x16x32_bf16 v[220:223],a[124:127],v[72:75],v[220:223]\n"
"v_cvt_pk_bf16_f32 v86,v44,v45\n"
"v_cvt_pk_bf16_f32 v87,v46,v47\n"
"v_mfma_f32_16x16x32_bf16 v[252:255],a[124:127],v[76:79],v[252:255]\n"
"v_cvt_pk_bf16_f32 v88,v48,v49\n"
"v_cvt_pk_bf16_f32 v89,v50,v51\n"
"v_mfma_f32_16x16x32_bf16 v[156:159],a[124:127],v[64:67],v[156:159]\n"
"v_cvt_pk_bf16_f32 v90,v52,v53\n"
"v_cvt_pk_bf16_f32 v91,v54,v55\n"
"v_cvt_pk_bf16_f32 v92,v56,v57\n"
"v_cvt_pk_bf16_f32 v93,v58,v59\n"
"v_cvt_pk_bf16_f32 v94,v60,v61\n"
"v_cvt_pk_bf16_f32 v95,v62,v63\n"
"v_max3_f32 v118,v24,v25,v26\n"
"v_max3_f32 v118,v118,v27,v28\n"
"v_max3_f32 v118,v118,v29,v30\n"
"v_max3_f32 v118,v118,v31,v20\n"
"v_max3_f32 v118,v118,v21,v22\n"
"v_max3_f32 v118,v118,v23,v16\n"
"v_max3_f32 v118,v118,v17,v18\n"
"v_max_f32 v118,v118,v19\n"
"v_cmp_lt_f32 vcc,s93,v118\n"
"s_nop 0\n"
"s_cbranch_vccnz .Lfk_cs_u1\n"
".Lfk_cr_u1:\n"
"v_cmp_gt_u32 vcc,s101,v125\n"
"s_nop 0\n"
"s_cbranch_vccnz .Lfk_cs_mu1\n"
".Lfk_cr_mu1:\n"
"v_mov_b32 v113,v112\n"
"v_add_f32 v56,0x80000000,v126\n"
"v_min_f32 v56,v56,v108\n"
"v_add_f32 v57,0xf149f2ca,v126\n"
"v_min_f32 v57,v57,v109\n"
"v_add_f32 v58,0xf1c9f2ca,v126\n"
"v_min_f32 v58,v58,v110\n"
"v_add_f32 v59,0xf2177617,v126\n"
"v_min_f32 v59,v59,v111\n"
"v_add_f32 v60,0xf349f2ca,v126\n"
"v_min_f32 v60,v60,v108\n"
"v_add_f32 v61,0xf35691f6,v126\n"
"v_min_f32 v61,v61,v109\n"
"v_add_f32 v62,0xf3633123,v126\n"
"v_min_f32 v62,v62,v110\n"
"v_add_f32 v63,0xf36fd050,v126\n"
"v_min_f32 v63,v63,v111\n"
"v_exp_f32 v16,v16\n"
"v_mfma_f32_16x16x32_bf16 v[56:59],a[64:67],a[48:51],v[56:59]\n"
"s_add_u32 s101,s101,4\n"
"ds_read_b128 a[96:99],v114 offset:8192\n"
"ds_read_b128 a[100:103],v114 offset:9216\n"
"ds_read_b128 a[104:107],v114 offset:10240\n"
"v_exp_f32 v17,v17\n"
"v_mfma_f32_16x16x32_bf16 v[56:59],a[68:71],a[52:55],v[56:59]\n"
"s_add_u32 m0,s55,12288\n"
"ds_read_b128 a[108:111],v114 offset:11264\n"
"ds_read_b128 a[112:115],v114 offset:12288\n"
"ds_read_b128 a[116:119],v114 offset:13312\n"
"v_mfma_f32_16x16x32_bf16 v[56:59],a[72:75],a[56:59],v[56:59]\n"
"ds_read_b128 a[120:123],v114 offset:14336\n"
"ds_read_b128 a[124:127],v114 offset:15360\n"
"ds_read_b128 a[32:35],v116 offset:24576\n"
"ds_read_b128 a[36:39],v116 offset:25600\n"
"ds_read_b128 a[40:43],v116 offset:26624\n"
"v_mfma_f32_16x16x32_bf16 v[56:59],a[76:79],a[60:63],v[56:59]\n"
"s_add_u32 s54,s54,s22\n"
"ds_read_b128 a[44:47],v116 offset:27648\n"
"v_exp_f32 v18,v18\n"
"v_mfma_f32_16x16x32_bf16 v[60:63],a[92:95],a[60:63],v[60:63]\n"
"buffer_load_dwordx4 v115,s[32:35],s54 offen sc1 lds\n"
"v_cvt_pk_bf16_f32 v72,v16,v17\n"
"v_mfma_f32_16x16x32_bf16 v[60:63],a[88:91],a[56:59],v[60:63]\n"
"s_add_u32 m0,s67,32768\n"
"v_exp_f32 v19,v19\n"
"v_mfma_f32_16x16x32_bf16 v[60:63],a[84:87],a[52:55],v[60:63]\n"
"buffer_load_dwordx4 v112,s[24:27],s64 offen offset:0 sc1 lds\n"
"v_cvt_pk_bf16_f32 v73,v18,v19\n"
"v_exp_f32 v20,v20\n"
"v_mfma_f32_16x16x32_bf16 v[60:63],a[80:83],a[48:51],v[60:63]\n"
"ds_read_b128 a[64:67],v113 offset:0\n"
"ds_read_b128 a[68:71],v113 offset:1024\n"
"ds_read_b128 a[72:75],v113 offset:2048\n"
"s_waitcnt lgkmcnt(7)\n"
"v_mfma_f32_16x16x32_bf16 v[160:163],a[96:99],v[84:87],v[160:163]\n"
"ds_read_b128 a[76:79],v113 offset:3072\n"
"ds_read_b128 a[80:83],v113 offset:4096\n"
"ds_read_b128 a[84:87],v113 offset:5120\n"
"ds_read_b128 a[88:91],v113 offset:6144\n"
"ds_read_b128 a[92:95],v113 offset:7168\n"
"v_exp_f32 v21,v21\n"
"v_mfma_f32_16x16x32_bf16 a[132:135],a[144:147],v[84:87],a[132:135]\n"
"buffer_load_dwordx4 v112,s[24:27],s64 offen offset:1024 sc1 lds\n"
"v_max3_f32 v118,v56,v57,v58\n"
"v_mfma_f32_16x16x32_bf16 v[164:167],a[100:103],v[84:87],v[164:167]\n"
"v_exp_f32 v22,v22\n"
"v_cvt_pk_bf16_f32 v74,v20,v21\n"
"ds_read_b128 a[48:51],v116 offset:57344\n"
"ds_read_b128 a[52:55],v116 offset:58368\n"
"v_mfma_f32_16x16x32_bf16 v[168:171],a[104:107],v[84:87],v[168:171]\n"
"v_max3_f32 v118,v118,v59,v60\n"
"v_max3_f32 v118,v118,v61,v62\n"
"v_max_f32 v118,v118,v63\n"
"v_exp_f32 v23,v23\n"
"v_mfma_f32_16x16x32_bf16 v[172:175],a[108:111],v[84:87],v[172:175]\n"
"ds_read_b128 a[56:59],v116 offset:59392\n"
"buffer_load_dwordx4 v112,s[24:27],s64 offen offset:2048 sc1 lds\n"
"ds_read_b128 a[60:63],v116 offset:60416\n"
"v_mfma_f32_16x16x32_bf16 v[176:179],a[112:115],v[84:87],v[176:179]\n"
"s_waitcnt vmcnt(15)\n"
"v_cvt_pk_bf16_f32 v75,v22,v23\n"
"v_accvgpr_read_b32 v122,a128\n"
"v_rcp_f32 v122,v122\n"
"v_mfma_f32_16x16x32_bf16 v[180:183],a[116:119],v[84:87],v[180:183]\n"
"ds_add_u32 v124,a152\n"
"v_cmp_lt_f32 vcc,s93,v118\n"
"v_exp_f32 v24,v24\n"
"v_pk_mul_f32 v[128:129],v[128:129],v[122:123] op_sel_hi:[1,0]\n"
"v_pk_mul_f32 v[130:131],v[130:131],v[122:123] op_sel_hi:[1,0]\n"
"v_pk_mul_f32 v[132:133],v[132:133],v[122:123] op_sel_hi:[1,0]\n"
"v_pk_mul_f32 v[134:135],v[134:135],v[122:123] op_sel_hi:[1,0]\n"
"v_mfma_f32_16x16x32_bf16 v[184:187],a[120:123],v[84:87],v[184:187]\n"
"buffer_load_dwordx4 v112,s[24:27],s64 offen offset:3072 sc1 lds\n"
"v_cvt_pk_bf16_f32 v128,v128,v129\n"
"v_cvt_pk_bf16_f32 v129,v130,v131\n"
"v_mfma_f32_16x16x32_bf16 v[188:191],a[124:127],v[84:87],v[188:191]\n"
"s_add_u32 m0,s67,36864\n"
"v_cvt_pk_bf16_f32 v130,v132,v133\n"
"v_cvt_pk_bf16_f32 v131,v134,v135\n"
"v_exp_f32 v25,v25\n"
"v_mfma_f32_16x16x32_bf16 v[192:195],a[96:99],v[88:91],v[192:195]\n"
"buffer_store_dwordx4 v[128:131],v112,s[24:27],s70 offen offset:0 sc1\n"
"v_cvt_pk_bf16_f32 v76,v24,v25\n"
"v_pk_mul_f32 v[136:137],v[136:137],v[122:123] op_sel_hi:[1,0]\n"
"v_pk_mul_f32 v[138:139],v[138:139],v[122:123] op_sel_hi:[1,0]\n"
"v_pk_mul_f32 v[140:141],v[140:141],v[122:123] op_sel_hi:[1,0]\n"
"v_pk_mul_f32 v[142:143],v[142:143],v[122:123] op_sel_hi:[1,0]\n"
"v_exp_f32 v26,v26\n"
"v_mfma_f32_16x16x32_bf16 a[136:139],a[144:147],v[88:91],a[136:139]\n"
"buffer_load_dwordx4 v117,s[24:27],s64 offen offset:0 sc1 lds\n"
"v_cvt_pk_bf16_f32 v136,v136,v137\n"
"v_mfma_f32_16x16x32_bf16 v[196:199],a[100:103],v[88:91],v[196:199]\n"
"v_exp_f32 v27,v27\n"
"v_cvt_pk_bf16_f32 v137,v138,v139\n"
"v_cvt_pk_bf16_f32 v138,v140,v141\n"
"v_cvt_pk_bf16_f32 v139,v142,v143\n"
"v_cvt_pk_bf16_f32 v77,v26,v27\n"
"v_mfma_f32_16x16x32_bf16 v[200:203],a[104:107],v[88:91],v[200:203]\n"
"buffer_store_dwordx4 v[136:139],v112,s[24:27],s70 offen offset:1024 sc1\n"
"v_pk_mul_f32 v[144:145],v[144:145],v[122:123] op_sel_hi:[1,0]\n"
"v_pk_mul_f32 v[146:147],v[146:147],v[122:123] op_sel_hi:[1,0]\n"
"v_pk_mul_f32 v[148:149],v[148:149],v[122:123] op_sel_hi:[1,0]\n"
"v_pk_mul_f32 v[150:151],v[150:151],v[122:123] op_sel_hi:[1,0]\n"
"v_mfma_f32_16x16x32_bf16 v[204:207],a[108:111],v[88:91],v[204:207]\n"
"buffer_load_dwordx4 v117,s[24:27],s64 offen offset:1024 sc1 lds\n"
"v_exp_f32 v28,v28\n"
"v_mfma_f32_16x16x32_bf16 v[208:211],a[112:115],v[88:91],v[208:211]\n"
"v_cvt_pk_bf16_f32 v144,v144,v145\n"
"v_cvt_pk_bf16_f32 v145,v146,v147\n"
"v_cvt_pk_bf16_f32 v146,v148,v149\n"
"v_cvt_pk_bf16_f32 v147,v150,v151\n"
"v_mfma_f32_16x16x32_bf16 v[212:215],a[116:119],v[88:91],v[212:215]\n"
"buffer_store_dwordx4 v[144:147],v112,s[24:27],s70 offen offset:2048 sc1\n"
"v_pk_mul_f32 v[152:153],v[152:153],v[122:123] op_sel_hi:[1,0]\n"
"v_pk_mul_f32 v[154:155],v[154:155],v[122:123] op_sel_hi:[1,0]\n"
"v_pk_mul_f32 v[156:157],v[156:157],v[122:123] op_sel_hi:[1,0]\n"
"v_mfma_f32_16x16x32_bf16 v[216:219],a[120:123],v[88:91],v[216:219]\n"
"buffer_load_dwordx4 v117,s[24:27],s64 offen offset:2048 sc1 lds\n"
"v_exp_f32 v29,v29\n"
"v_cvt_pk_bf16_f32 v152,v152,v153\n"
"v_cvt_pk_bf16_f32 v78,v28,v29\n"
"v_pk_mul_f32 v[158:159],v[158:159],v[122:123] op_sel_hi:[1,0]\n"
"v_mfma_f32_16x16x32_bf16 v[220:223],a[124:127],v[88:91],v[220:223]\n"
"v_cvt_pk_bf16_f32 v153,v154,v155\n"
"v_cvt_pk_bf16_f32 v154,v156,v157\n"
"v_cvt_pk_bf16_f32 v155,v158,v159\n"
"v_mfma_f32_16x16x32_bf16 v[224:227],a[96:99],v[92:95],v[224:227]\n"
"ds_read_b32 v125,v124\n"
"buffer_store_dwordx4 v[152:155],v112,s[24:27],s70 offen offset:3072 sc1\n"
"buffer_load_dwordx4 v117,s[24:27],s64 offen offset:3072 sc1 lds\n"
"v_accvgpr_read_b32 v122,a132\n"
"v_mfma_f32_16x16x32_bf16 a[140:143],a[144:147],v[92:95],a[140:143]\n"
"s_add_u32 s70,s70,262144\n"
"v_exp_f32 v31,v31\n"
"v_mfma_f32_16x16x32_bf16 v[228:231],a[100:103],v[92:95],v[228:231]\n"
"s_add_u32 s64,s64,16384\n"
"v_exp_f32 v30,v30\n"
"v_rcp_f32 v122,v122\n"
"v_cvt_pk_bf16_f32 v79,v30,v31\n"
"v_pk_mul_f32 v[160:161],v[160:161],v[122:123] op_sel_hi:[1,0]\n"
"v_pk_mul_f32 v[162:163],v[162:163],v[122:123] op_sel_hi:[1,0]\n"
"v_pk_mul_f32 v[164:165],v[164:165],v[122:123] op_sel_hi:[1,0]\n"
"v_pk_mul_f32 v[166:167],v[166:167],v[122:123] op_sel_hi:[1,0]\n"
"v_mfma_f32_16x16x32_bf16 v[232:235],a[104:107],v[92:95],v[232:235]\n"
"buffer_store_dwordx4 a[252:255],v112,s[24:27],s56 offen offset:3072 sc1\n"
"v_cvt_pk_bf16_f32 v160,v160,v161\n"
"v_cvt_pk_bf16_f32 v161,v162,v163\n"
"v_cvt_pk_bf16_f32 v162,v164,v165\n"
"v_cvt_pk_bf16_f32 v163,v166,v167\n"
"v_mfma_f32_16x16x32_bf16 v[236:239],a[108:111],v[92:95],v[236:239]\n"
"buffer_store_dwordx4 v[160:163],v112,s[24:27],s70 offen offset:0 sc1\n"
"v_pk_mul_f32 v[168:169],v[168:169],v[122:123] op_sel_hi:[1,0]\n"
"v_pk_mul_f32 v[170:171],v[170:171],v[122:123] op_sel_hi:[1,0]\n"
"v_pk_mul_f32 v[172:173],v[172:173],v[122:123] op_sel_hi:[1,0]\n"
"v_pk_mul_f32 v[174:175],v[174:175],v[122:123] op_sel_hi:[1,0]\n"
"v_mfma_f32_16x16x32_bf16 v[240:243],a[112:115],v[92:95],v[240:243]\n"
"s_waitcnt lgkmcnt(0)\n"
"v_cvt_pk_bf16_f32 v168,v168,v169\n"
"v_cvt_pk_bf16_f32 v169,v170,v171\n"
"v_cvt_pk_bf16_f32 v170,v172,v173\n"
"v_cvt_pk_bf16_f32 v171,v174,v175\n"
"v_mfma_f32_16x16x32_bf16 v[244:247],a[116:119],v[92:95],v[244:247]\n"
"buffer_store_dwordx4 v[168:171],v112,s[24:27],s70 offen offset:1024 sc1\n"
"v_pk_mul_f32 v[176:177],v[176:177],v[122:123] op_sel_hi:[1,0]\n"
"v_pk_mul_f32 v[178:179],v[178:179],v[122:123] op_sel_hi:[1,0]\n"
"v_pk_mul_f32 v[180:181],v[180:181],v[122:123] op_sel_hi:[1,0]\n"
"v_pk_mul_f32 v[182:183],v[182:183],v[122:123] op_sel_hi:[1,0]\n"
"v_mfma_f32_16x16x32_bf16 v[248:251],a[120:123],v[92:95],v[248:251]\n"
"v_cvt_pk_bf16_f32 v176,v176,v177\n"
"v_cvt_pk_bf16_f32 v177,v178,v179\n"
"v_cvt_pk_bf16_f32 v178,v180,v181\n"
"v_cvt_pk_bf16_f32 v179,v182,v183\n"
"v_mfma_f32_16x16x32_bf16 v[252:255],a[124:127],v[92:95],v[252:255]\n"
"buffer_store_dwordx4 v[176:179],v112,s[24:27],s70 offen offset:2048 sc1\n"
"v_pk_mul_f32 v[184:185],v[184:185],v[122:123] op_sel_hi:[1,0]\n"
"v_pk_mul_f32 v[186:187],v[186:187],v[122:123] op_sel_hi:[1,0]\n"
"v_pk_mul_f32 v[188:189],v[188:189],v[122:123] op_sel_hi:[1,0]\n"
"v_pk_mul_f32 v[190:191],v[190:191],v[122:123] op_sel_hi:[1,0]\n"
"v_cvt_pk_bf16_f32 v184,v184,v185\n"
"v_cvt_pk_bf16_f32 v185,v186,v187\n"
"v_cvt_pk_bf16_f32 v186,v188,v189\n"
"v_cvt_pk_bf16_f32 v187,v190,v191\n"
"buffer_store_dwordx4 v[184:187],v112,s[24:27],s70 offen offset:3072 sc1\n"
"s_add_u32 s70,s70,262144\n"
"s_cbranch_vccnz .Lfk_cs_u2\n"
".Lfk_cr_u2:\n"
"v_cmp_gt_u32 vcc,s101,v125\n"
"s_nop 0\n"
"s_cbranch_vccnz .Lfk_cs_mu2\n"
".Lfk_cr_mu2:\n"
"s_add_u32 s89,s89,1\n"
"s_cmp_eq_u32 s89,8\n"
"s_cbranch_scc1 .Lfk_tailB7\n"
".Lfk_winB:\n"
"v_mfma_f32_16x16x32_bf16 v[0:3],a[64:67],a[0:3],v[96:99]\n"
"s_swappc_b64 s[94:95],s[20:21]\n"
"ds_read_b128 a[96:99],v114 offset:24576\n"
"v_mfma_f32_16x16x32_bf16 v[0:3],a[68:71],a[4:7],v[0:3]\n"
"s_add_u32 s101,s101,4\n"
"ds_read_b128 a[100:103],v114 offset:25600\n"
"ds_read_b128 a[104:107],v114 offset:26624\n"
"ds_read_b128 a[108:111],v114 offset:27648\n"
"v_mfma_f32_16x16x32_bf16 v[0:3],a[72:75],a[8:11],v[0:3]\n"
"ds_read_b128 a[112:115],v114 offset:28672\n"
"ds_read_b128 a[116:119],v114 offset:29696\n"
"v_mfma_f32_16x16x32_bf16 v[0:3],a[76:79],a[12:15],v[0:3]\n"
"ds_read_b128 a[120:123],v114 offset:30720\n"
"ds_read_b128 a[124:127],v114 offset:31744\n"
"v_mfma_f32_16x16x32_bf16 v[4:7],a[92:95],a[12:15],v[96:99]\n"
"v_exp_f32 v56,v56\n"
"v_mfma_f32_16x16x32_bf16 v[4:7],a[88:91],a[8:11],v[4:7]\n"
"v_exp_f32 v57,v57\n"
"v_mfma_f32_16x16x32_bf16 v[4:7],a[84:87],a[4:7],v[4:7]\n"
"v_cvt_pk_bf16_f32 v92,v56,v57\n"
"v_mfma_f32_16x16x32_bf16 v[4:7],a[80:83],a[0:3],v[4:7]\n"
"s_add_u32 m0,s67,0\n"
"s_waitcnt lgkmcnt(0)\n"
"v_mfma_f32_16x16x32_bf16 v[192:195],a[96:99],v[72:75],v[192:195]\n"
"buffer_load_dwordx4 v112,s[24:27],s64 offen offset:0 sc1 lds\n"
"v_mfma_f32_16x16x32_bf16 a[136:139],a[144:147],v[72:75],a[136:139]\n"
"buffer_load_dwordx4 v112,s[24:27],s64 offen offset:1024 sc1 lds\n"
"v_mfma_f32_16x16x32_bf16 v[196:199],a[100:103],v[72:75],v[196:199]\n"
"v_exp_f32 v58,v58\n"
"v_mfma_f32_16x16x32_bf16 v[200:203],a[104:107],v[72:75],v[200:203]\n"
"v_max3_f32 v118,v0,v1,v2\n"
"v_mfma_f32_16x16x32_bf16 v[204:207],a[108:111],v[72:75],v[204:207]\n"
"v_max3_f32 v118,v118,v3,v4\n"
"v_max3_f32 v118,v118,v5,v6\n"
"v_mfma_f32_16x16x32_bf16 v[208:211],a[112:115],v[72:75],v[208:211]\n"
"v_max_f32 v118,v118,v7\n"
"v_mov_b32 v120,v118\n"
"v_mfma_f32_16x16x32_bf16 v[212:215],a[116:119],v[72:75],v[212:215]\n"
"v_mfma_f32_16x16x32_bf16 v[216:219],a[120:123],v[72:75],v[216:219]\n"
"v_mfma_f32_16x16x32_bf16 v[220:223],a[124:127],v[72:75],v[220:223]\n"
"v_exp_f32 v59,v59\n"
"v_mfma_f32_16x16x32_bf16 v[224:227],a[96:99],v[76:79],v[224:227]\n"
"v_cvt_pk_bf16_f32 v93,v58,v59\n"
"v_accvgpr_read_b32 v122,a136\n"
"v_mfma_f32_16x16x32_bf16 a[140:143],a[144:147],v[76:79],a[140:143]\n"
"buffer_load_dwordx4 v112,s[24:27],s64 offen offset:2048 sc1 lds\n"
"v_mfma_f32_16x16x32_bf16 v[228:231],a[100:103],v[76:79],v[228:231]\n"
"v_rcp_f32 v122,v122\n"
"v_mfma_f32_16x16x32_bf16 v[232:235],a[104:107],v[76:79],v[232:235]\n"
"buffer_load_dwordx4 v112,s[24:27],s64 offen offset:3072 sc1 lds\n"
"v_pk_mul_f32 v[192:193],v[192:193],v[122:123] op_sel_hi:[1,0]\n"
"v_pk_mul_f32 v[194:195],v[194:195],v[122:123] op_sel_hi:[1,0]\n"
"v_mfma_f32_16x16x32_bf16 v[236:239],a[108:111],v[76:79],v[236:239]\n"
"v_cvt_pk_bf16_f32 v192,v192,v193\n"
"v_exp_f32 v60,v60\n"
"v_cvt_pk_bf16_f32 v193,v194,v195\n"
"v_pk_mul_f32 v[196:197],v[196:197],v[122:123] op_sel_hi:[1,0]\n"
"v_pk_mul_f32 v[198:199],v[198:199],v[122:123] op_sel_hi:[1,0]\n"
"v_mfma_f32_16x16x32_bf16 v[240:243],a[112:115],v[76:79],v[240:243]\n"
"s_add_u32 m0,s67,4096\n"
"v_cvt_pk_bf16_f32 v194,v196,v197\n"
"v_cvt_pk_bf16_f32 v195,v198,v199\n"
"v_mfma_f32_16x16x32_bf16 v[244:247],a[116:119],v[76:79],v[244:247]\n"
"v_mfma_f32_16x16x32_bf16 v[248:251],a[120:123],v[76:79],v[248:251]\n"
"buffer_store_dwordx4 v[192:195],v112,s[24:27],s70 offen offset:0 sc1\n"
"v_pk_mul_f32 v[200:201],v[200:201],v[122:123] op_sel_hi:[1,0]\n"
"v_mfma_f32_16x16x32_bf16 v[252:255],a[124:127],v[76:79],v[252:255]\n"
"buffer_load_dwordx4 v117,s[24:27],s64 offen offset:0 sc1 lds\n"
"v_cvt_pk_bf16_f32 v200,v200,v201\n"
"v_exp_f32 v61,v61\n"
"v_pk_mul_f32 v[202:203],v[202:203],v[122:123] op_sel_hi:[1,0]\n"
"v_pk_mul_f32 v[204:205],v[204:205],v[122:123] op_sel_hi:[1,0]\n"
"v_pk_mul_f32 v[206:207],v[206:207],v[122:123] op_sel_hi:[1,0]\n"
"v_mfma_f32_16x16x32_bf16 v[12:15],a[80:83],a[16:19],v[100:103]\n"
"v_cvt_pk_bf16_f32 v201,v202,v203\n"
"v_cvt_pk_bf16_f32 v202,v204,v205\n"
"v_mfma_f32_16x16x32_bf16 v[12:15],a[84:87],a[20:23],v[12:15]\n"
"v_cvt_pk_bf16_f32 v203,v206,v207\n"
"v_cvt_pk_bf16_f32 v94,v60,v61\n"
"v_mfma_f32_16x16x32_bf16 v[12:15],a[88:91],a[24:27],v[12:15]\n"
"buffer_load_dwordx4 v117,s[24:27],s64 offen offset:1024 sc1 lds\n"
"v_mfma_f32_16x16x32_bf16 v[12:15],a[92:95],a[28:31],v[12:15]\n"
"buffer_store_dwordx4 v[200:203],v112,s[24:27],s70 offen offset:1024 sc1\n"
"v_pk_mul_f32 v[208:209],v[208:209],v[122:123] op_sel_hi:[1,0]\n"
"v_pk_mul_f32 v[210:211],v[210:211],v[122:123] op_sel_hi:[1,0]\n"
"v_mfma_f32_16x16x32_bf16 v[8:11],a[76:79],a[28:31],v[100:103]\n"
"buffer_load_dwordx4 v117,s[24:27],s64 offen offset:2048 sc1 lds\n"
"v_cvt_pk_bf16_f32 v208,v208,v209\n"
"v_cvt_pk_bf16_f32 v209,v210,v211\n"
"v_pk_mul_f32 v[212:213],v[212:213],v[122:123] op_sel_hi:[1,0]\n"
"v_pk_mul_f32 v[214:215],v[214:215],v[122:123] op_sel_hi:[1,0]\n"
"v_mfma_f32_16x16x32_bf16 v[8:11],a[72:75],a[24:27],v[8:11]\n"
"v_cvt_pk_bf16_f32 v210,v212,v213\n"
"v_cvt_pk_bf16_f32 v211,v214,v215\n"
"v_mfma_f32_16x16x32_bf16 v[8:11],a[68:71],a[20:23],v[8:11]\n"
"v_exp_f32 v62,v62\n"
"v_mfma_f32_16x16x32_bf16 v[8:11],a[64:67],a[16:19],v[8:11]\n"
"buffer_store_dwordx4 v[208:211],v112,s[24:27],s70 offen offset:2048 sc1\n"
"v_pk_mul_f32 v[216:217],v[216:217],v[122:123] op_sel_hi:[1,0]\n"
"v_mfma_f32_16x16x32_bf16 v[16:19],a[64:67],a[32:35],v[104:107]\n"
"v_cvt_pk_bf16_f32 v216,v216,v217\n"
"buffer_load_dwordx4 v117,s[24:27],s64 offen offset:3072 sc1 lds\n"
"v_pk_mul_f32 v[218:219],v[218:219],v[122:123] op_sel_hi:[1,0]\n"
"v_pk_mul_f32 v[220:221],v[220:221],v[122:123] op_sel_hi:[1,0]\n"
"v_pk_mul_f32 v[222:223],v[222:223],v[122:123] op_sel_hi:[1,0]\n"
"v_mfma_f32_16x16x32_bf16 v[16:19],a[68:71],a[36:39],v[16:19]\n"
"v_cvt_pk_bf16_f32 v217,v218,v219\n"
"v_cvt_pk_bf16_f32 v218,v220,v221\n"
"v_mfma_f32_16x16x32_bf16 v[16:19],a[72:75],a[40:43],v[16:19]\n"
"s_add_u32 s64,s64,16384\n"
"v_exp_f32 v63,v63\n"
"v_mfma_f32_16x16x32_bf16 v[16:19],a[76:79],a[44:47],v[16:19]\n"
"s_andn2_b32 m0,s55,16384\n"
"s_add_u32 s54,s54,s22\n"
"v_max3_f32 v119,v8,v9,v10\n"
"v_mfma_f32_16x16x32_bf16 v[20:23],a[92:95],a[44:47],v[104:107]\n"
"v_max3_f32 v119,v119,v11,v12\n"
"v_cvt_pk_bf16_f32 v95,v62,v63\n"
"v_mfma_f32_16x16x32_bf16 v[20:23],a[88:91],a[40:43],v[20:23]\n"
"buffer_load_dwordx4 v115,s[32:35],s54 offen sc1 lds\n"
"v_mfma_f32_16x16x32_bf16 v[20:23],a[84:87],a[36:39],v[20:23]\n"
"v_max3_f32 v119,v119,v13,v14\n"
"v_max_f32 v119,v119,v15\n"
"v_mfma_f32_16x16x32_bf16 v[20:23],a[80:83],a[32:35],v[20:23]\n"
"v_cvt_pk_bf16_f32 v219,v222,v223\n"
"v_mfma_f32_16x16x32_bf16 v[28:31],a[80:83],a[48:51],v[108:111]\n"
"buffer_store_dwordx4 v[216:219],v112,s[24:27],s70 offen offset:3072 sc1\n"
"buffer_store_dwordx4 a[240:243],v112,s[24:27],s56 offen offset:0 sc1\n"
"v_mfma_f32_16x16x32_bf16 v[28:31],a[84:87],a[52:55],v[28:31]\n"
"s_add_u32 s70,s70,262144\n"
"v_max_f32 v118,v118,v119\n"
"v_min_f32 v120,v120,v119\n"
"v_mfma_f32_16x16x32_bf16 v[28:31],a[88:91],a[56:59],v[28:31]\n"
"v_max3_f32 v119,v16,v17,v18\n"
"v_mfma_f32_16x16x32_bf16 v[28:31],a[92:95],a[60:63],v[28:31]\n"
"v_max3_f32 v119,v119,v19,v20\n"
"v_max3_f32 v119,v119,v21,v22\n"
"v_mfma_f32_16x16x32_bf16 v[24:27],a[76:79],a[60:63],v[108:111]\n"
"v_max_f32 v119,v119,v23\n"
"v_max_f32 v118,v118,v119\n"
"v_mfma_f32_16x16x32_bf16 v[24:27],a[72:75],a[56:59],v[24:27]\n"
"v_min_f32 v120,v120,v119\n"
"v_mfma_f32_16x16x32_bf16 v[24:27],a[68:71],a[52:55],v[24:27]\n"
"v_mfma_f32_16x16x32_bf16 v[24:27],a[64:67],a[48:51],v[24:27]\n"
"ds_read_b128 a[64:67],v113 offset:16384\n"
"ds_read_b128 a[68:71],v113 offset:17408\n"
"ds_read_b128 a[72:75],v113 offset:18432\n"
"ds_read_b128 a[76:79],v113 offset:19456\n"
"ds_read_b128 a[80:83],v113 offset:20480\n"
"ds_read_b128 a[84:87],v113 offset:21504\n"
"ds_read_b128 a[88:91],v113 offset:22528\n"
"ds_read_b128 a[92:95],v113 offset:23552\n"
"v_max3_f32 v119,v24,v25,v26\n"
"v_max3_f32 v119,v119,v27,v28\n"
"v_max3_f32 v119,v119,v29,v30\n"
"v_max_f32 v119,v119,v31\n"
"v_min_f32 v120,v120,v119\n"
"v_max_f32 v118,v118,v119\n"
"v_cmp_gt_f32 s[96:97],s16,v120\n"
"v_cmp_lt_f32 vcc,s93,v118\n"
"s_waitcnt vmcnt(25)\n"
"ds_add_u32 v124,a152\n"
"ds_read_b32 v125,v124\n"
"s_waitcnt lgkmcnt(0)\n"
"s_or_b64 s[96:97],s[96:97],vcc\n"
"s_cbranch_scc1 .Lfk_cs_v3\n"
".Lfk_cr_v3:\n"
"v_cmp_gt_u32 vcc,s101,v125\n"
"s_nop 0\n"
"s_cbranch_vccnz .Lfk_cs_mv3\n"
".Lfk_cr_mv3:\n"
"v_exp_f32 v0,v0\n"
"v_mfma_f32_16x16x32_bf16 v[32:35],a[64:67],a[0:3],v[96:99]\n"
"s_add_u32 s101,s101,4\n"
"ds_read_b128 a[96:99],v114 offset:57344\n"
"ds_read_b128 a[100:103],v114 offset:58368\n"
"v_exp_f32 v1,v1\n"
"v_mfma_f32_16x16x32_bf16 v[32:35],a[68:71],a[4:7],v[32:35]\n"
"s_add_u32 m0,s55,4096\n"
"ds_read_b128 a[104:107],v114 offset:59392\n"
"ds_read_b128 a[108:111],v114 offset:60416\n"
"v_exp_f32 v2,v2\n"
"v_mfma_f32_16x16x32_bf16 v[32:35],a[72:75],a[8:11],v[32:35]\n"
"s_add_u32 s54,s54,s22\n"
"ds_read_b128 a[112:115],v114 offset:61440\n"
"ds_read_b128 a[116:119],v114 offset:62464\n"
"v_exp_f32 v3,v3\n"
"v_mfma_f32_16x16x32_bf16 v[32:35],a[76:79],a[12:15],v[32:35]\n"
"ds_read_b128 a[120:123],v114 offset:63488\n"
"ds_read_b128 a[124:127],v114 offset:64512\n"
"v_exp_f32 v4,v4\n"
"v_mfma_f32_16x16x32_bf16 v[36:39],a[92:95],a[12:15],v[96:99]\n"
"buffer_load_dwordx4 v115,s[32:35],s54 offen sc1 lds\n"
"v_mfma_f32_16x16x32_bf16 v[36:39],a[88:91],a[8:11],v[36:39]\n"
"s_add_u32 m0,s67,16384\n"
"v_exp_f32 v5,v5\n"
"v_cvt_pk_bf16_f32 v64,v0,v1\n"
"v_cvt_pk_bf16_f32 v65,v2,v3\n"
"v_mfma_f32_16x16x32_bf16 v[36:39],a[84:87],a[4:7],v[36:39]\n"
"v_exp_f32 v6,v6\n"
"v_exp_f32 v7,v7\n"
"v_mfma_f32_16x16x32_bf16 v[36:39],a[80:83],a[0:3],v[36:39]\n"
"buffer_load_dwordx4 v112,s[24:27],s64 offen offset:0 sc1 lds\n"
"s_waitcnt lgkmcnt(0)\n"
"v_mfma_f32_16x16x32_bf16 v[224:227],a[96:99],v[92:95],v[224:227]\n"
"v_exp_f32 v8,v8\n"
"v_cvt_pk_bf16_f32 v67,v6,v7\n"
"v_mfma_f32_16x16x32_bf16 a[140:143],a[144:147],v[92:95],a[140:143]\n"
"v_exp_f32 v9,v9\n"
"v_exp_f32 v10,v10\n"
"v_mfma_f32_16x16x32_bf16 v[228:231],a[100:103],v[92:95],v[228:231]\n"
"buffer_load_dwordx4 v112,s[24:27],s64 offen offset:1024 sc1 lds\n"
"v_mfma_f32_16x16x32_bf16 v[232:235],a[104:107],v[92:95],v[232:235]\n"
"v_cvt_pk_bf16_f32 v66,v4,v5\n"
"v_cvt_pk_bf16_f32 v68,v8,v9\n"
"v_mfma_f32_16x16x32_bf16 v[236:239],a[108:111],v[92:95],v[236:239]\n"
"v_exp_f32 v11,v11\n"
"v_exp_f32 v12,v12\n"
"v_mfma_f32_16x16x32_bf16 v[240:243],a[112:115],v[92:95],v[240:243]\n"
"buffer_load_dwordx4 v112,s[24:27],s64 offen offset:2048 sc1 lds\n"
"v_mfma_f32_16x16x32_bf16 v[244:247],a[116:119],v[92:95],v[244:247]\n"
"v_exp_f32 v13,v13\n"
"v_cvt_pk_bf16_f32 v69,v10,v11\n"
"v_mfma_f32_16x16x32_bf16 v[248:251],a[120:123],v[92:95],v[248:251]\n"
"v_exp_f32 v14,v14\n"
"v_cvt_pk_bf16_f32 v70,v12,v13\n"
"v_exp_f32 v15,v15\n"
"v_mfma_f32_16x16x32_bf16 v[252:255],a[124:127],v[92:95],v[252:255]\n"
"buffer_load_dwordx4 v112,s[24:27],s64 offen offset:3072 sc1 lds\n"
"v_mfma_f32_16x16x32_bf16 v[44:47],a[80:83],a[16:19],v[100:103]\n"
"s_add_u32 m0,s67,20480\n"
"v_exp_f32 v16,v16\n"
"v_cvt_pk_bf16_f32 v71,v14,v15\n"
"v_mfma_f32_16x16x32_bf16 v[44:47],a[84:87],a[20:23],v[44:47]\n"
"v_exp_f32 v17,v17\n"
"v_exp_f32 v18,v18\n"
"v_mfma_f32_16x16x32_bf16 v[44:47],a[88:91],a[24:27],v[44:47]\n"
"buffer_load_dwordx4 v117,s[24:27],s64 offen offset:0 sc1 lds\n"
"v_mfma_f32_16x16x32_bf16 v[44:47],a[92:95],a[28:31],v[44:47]\n"
"v_exp_f32 v19,v19\n"
"v_accvgpr_read_b32 v122,a140\n"
"v_mfma_f32_16x16x32_bf16 v[40:43],a[76:79],a[28:31],v[100:103]\n"
"v_cvt_pk_bf16_f32 v73,v18,v19\n"
"v_rcp_f32 v122,v122\n"
"v_mfma_f32_16x16x32_bf16 v[40:43],a[72:75],a[24:27],v[40:43]\n"
"buffer_load_dwordx4 v117,s[24:27],s64 offen offset:1024 sc1 lds\n"
"v_mfma_f32_16x16x32_bf16 v[40:43],a[68:71],a[20:23],v[40:43]\n"
"v_exp_f32 v21,v21\n"
"v_exp_f32 v20,v20\n"
"v_cvt_pk_bf16_f32 v72,v16,v17\n"
"v_pk_mul_f32 v[224:225],v[224:225],v[122:123] op_sel_hi:[1,0]\n"
"v_pk_mul_f32 v[226:227],v[226:227],v[122:123] op_sel_hi:[1,0]\n"
"v_pk_mul_f32 v[228:229],v[228:229],v[122:123] op_sel_hi:[1,0]\n"
"v_pk_mul_f32 v[230:231],v[230:231],v[122:123] op_sel_hi:[1,0]\n"
"v_mfma_f32_16x16x32_bf16 v[40:43],a[64:67],a[16:19],v[40:43]\n"
"v_cvt_pk_bf16_f32 v74,v20,v21\n"
"v_exp_f32 v22,v22\n"
"v_cvt_pk_bf16_f32 v224,v224,v225\n"
"v_exp_f32 v23,v23\n"
"v_mfma_f32_16x16x32_bf16 v[48:51],a[64:67],a[32:35],v[104:107]\n"
"buffer_load_dwordx4 v117,s[24:27],s64 offen offset:2048 sc1 lds\n"
"v_mfma_f32_16x16x32_bf16 v[48:51],a[68:71],a[36:39],v[48:51]\n"
"v_exp_f32 v24,v24\n"
"v_cvt_pk_bf16_f32 v225,v226,v227\n"
"v_mfma_f32_16x16x32_bf16 v[48:51],a[72:75],a[40:43],v[48:51]\n"
"v_exp_f32 v25,v25\n"
"v_cvt_pk_bf16_f32 v226,v228,v229\n"
"v_cvt_pk_bf16_f32 v227,v230,v231\n"
"v_mfma_f32_16x16x32_bf16 v[48:51],a[76:79],a[44:47],v[48:51]\n"
"buffer_load_dwordx4 v117,s[24:27],s64 offen offset:3072 sc1 lds\n"
"v_mfma_f32_16x16x32_bf16 v[52:55],a[92:95],a[44:47],v[104:107]\n"
"buffer_store_dwordx4 v[224:227],v112,s[24:27],s70 offen offset:0 sc1\n"
"v_exp_f32 v27,v27\n"
"v_mfma_f32_16x16x32_bf16 v[52:55],a[88:91],a[40:43],v[52:55]\n"
"s_add_u32 s64,s64,16384\n"
"v_exp_f32 v26,v26\n"
"v_exp_f32 v28,v28\n"
"v_cvt_pk_bf16_f32 v77,v26,v27\n"
"v_pk_mul_f32 v[232:233],v[232:233],v[122:123] op_sel_hi:[1,0]\n"
"v_mfma_f32_16x16x32_bf16 v[52:55],a[84:87],a[36:39],v[52:55]\n"
"v_cvt_pk_bf16_f32 v75,v22,v23\n"
"v_cvt_pk_bf16_f32 v76,v24,v25\n"
"v_exp_f32 v29,v29\n"
"v_exp_f32 v31,v31\n"
"v_pk_mul_f32 v[234:235],v[234:235],v[122:123] op_sel_hi:[1,0]\n"
"v_pk_mul_f32 v[236:237],v[236:237],v[122:123] op_sel_hi:[1,0]\n"
"v_pk_mul_f32 v[238:239],v[238:239],v[122:123] op_sel_hi:[1,0]\n"
"v_mfma_f32_16x16x32_bf16 v[52:55],a[80:83],a[32:35],v[52:55]\n"
"v_exp_f32 v30,v30\n"
"v_cvt_pk_bf16_f32 v78,v28,v29\n"
"v_mfma_f32_16x16x32_bf16 v[60:63],a[80:83],a[48:51],v[108:111]\n"
"v_cvt_pk_bf16_f32 v232,v232,v233\n"
"v_cvt_pk_bf16_f32 v233,v234,v235\n"
"v_mfma_f32_16x16x32_bf16 v[60:63],a[84:87],a[52:55],v[60:63]\n"
"buffer_store_dwordx4 a[244:247],v112,s[24:27],s56 offen offset:1024 sc1\n"
"v_cvt_pk_bf16_f32 v234,v236,v237\n"
"v_cvt_pk_bf16_f32 v235,v238,v239\n"
"v_mfma_f32_16x16x32_bf16 v[60:63],a[88:91],a[56:59],v[60:63]\n"
"buffer_store_dwordx4 v[232:235],v112,s[24:27],s70 offen offset:1024 sc1\n"
"v_pk_mul_f32 v[240:241],v[240:241],v[122:123] op_sel_hi:[1,0]\n"
"v_pk_mul_f32 v[242:243],v[242:243],v[122:123] op_sel_hi:[1,0]\n"
"v_pk_mul_f32 v[244:245],v[244:245],v[122:123] op_sel_hi:[1,0]\n"
"v_pk_mul_f32 v[246:247],v[246:247],v[122:123] op_sel_hi:[1,0]\n"
"v_mfma_f32_16x16x32_bf16 v[60:63],a[92:95],a[60:63],v[60:63]\n"
"v_cvt_pk_bf16_f32 v240,v240,v241\n"
"v_cvt_pk_bf16_f32 v241,v242,v243\n"
"v_cvt_pk_bf16_f32 v242,v244,v245\n"
"v_cvt_pk_bf16_f32 v243,v246,v247\n"
"v_mfma_f32_16x16x32_bf16 v[56:59],a[76:79],a[60:63],v[108:111]\n"
"buffer_store_dwordx4 v[240:243],v112,s[24:27],s70 offen offset:2048 sc1\n"
"v_pk_mul_f32 v[248:249],v[248:249],v[122:123] op_sel_hi:[1,0]\n"
"v_pk_mul_f32 v[250:251],v[250:251],v[122:123] op_sel_hi:[1,0]\n"
"v_mfma_f32_16x16x32_bf16 v[56:59],a[72:75],a[56:59],v[56:59]\n"
"v_cvt_pk_bf16_f32 v79,v30,v31\n"
"v_cvt_pk_bf16_f32 v248,v248,v249\n"
"v_cvt_pk_bf16_f32 v249,v250,v251\n"
"v_pk_mul_f32 v[252:253],v[252:253],v[122:123] op_sel_hi:[1,0]\n"
"v_pk_mul_f32 v[254:255],v[254:255],v[122:123] op_sel_hi:[1,0]\n"
"v_mfma_f32_16x16x32_bf16 v[56:59],a[68:71],a[52:55],v[56:59]\n"
"v_cvt_pk_bf16_f32 v250,v252,v253\n"
"v_cvt_pk_bf16_f32 v251,v254,v255\n"
"v_mfma_f32_16x16x32_bf16 v[56:59],a[64:67],a[48:51],v[56:59]\n"
"buffer_store_dwordx4 v[248:251],v112,s[24:27],s70 offen offset:3072 sc1\n"
"s_add_u32 s70,s70,262144\n"
"ds_read_b128 a[64:67],v113 offset:49152\n"
"ds_read_b128 a[68:71],v113 offset:50176\n"
"ds_read_b128 a[72:75],v113 offset:51200\n"
"ds_read_b128 a[76:79],v113 offset:52224\n"
"ds_read_b128 a[80:83],v113 offset:53248\n"
"ds_read_b128 a[84:87],v113 offset:54272\n"
"ds_read_b128 a[88:91],v113 offset:55296\n"
"ds_read_b128 a[92:95],v113 offset:56320\n"
"v_mov_b32 v114,v112\n"
"s_mov_b32 s70,s68\n"
"s_waitcnt vmcnt(25)\n"
"ds_add_u32 v124,a152\n"
"ds_read_b32 v125,v124\n"
"s_waitcnt lgkmcnt(0)\n"
"v_cmp_gt_u32 vcc,s101,v125\n"
"s_nop 0\n"
"s_cbranch_vccnz .Lfk_cs_mv0\n"
".Lfk_cr_mv0:\n"
"v_mfma_f32_16x16x32_bf16 v[0:3],a[64:67],a[0:3],v[96:99]\n"
"s_add_u32 s101,s101,4\n"
"v_exp_f32 v122,v32\n"
"v_mfma_f32_16x16x32_bf16 v[0:3],a[68:71],a[4:7],v[0:3]\n"
"s_add_u32 m0,s55,8192\n"
"v_exp_f32 v123,v33\n"
"v_mfma_f32_16x16x32_bf16 v[0:3],a[72:75],a[8:11],v[0:3]\n"
"ds_read_b128 a[96:99],v114 offset:40960\n"
"ds_read_b128 a[100:103],v114 offset:41984\n"
"v_mfma_f32_16x16x32_bf16 v[0:3],a[76:79],a[12:15],v[0:3]\n"
"s_add_u32 s54,s54,s22\n"
"v_exp_f32 v119,v34\n"
"v_mfma_f32_16x16x32_bf16 v[4:7],a[92:95],a[12:15],v[96:99]\n"
"ds_read_b128 a[104:107],v114 offset:43008\n"
"v_cvt_pk_bf16_f32 v80,v122,v123\n"
"v_mfma_f32_16x16x32_bf16 v[4:7],a[88:91],a[8:11],v[4:7]\n"
"v_exp_f32 v120,v35\n"
"v_mfma_f32_16x16x32_bf16 v[4:7],a[84:87],a[4:7],v[4:7]\n"
"ds_read_b128 a[108:111],v114 offset:44032\n"
"ds_read_b128 a[112:115],v114 offset:45056\n"
"v_mfma_f32_16x16x32_bf16 v[4:7],a[80:83],a[0:3],v[4:7]\n"
"v_exp_f32 v122,v36\n"
"v_mfma_f32_16x16x32_bf16 v[12:15],a[80:83],a[16:19],v[100:103]\n"
"ds_read_b128 a[116:119],v114 offset:46080\n"
"v_cvt_pk_bf16_f32 v81,v119,v120\n"
"v_mfma_f32_16x16x32_bf16 v[12:15],a[84:87],a[20:23],v[12:15]\n"
"v_exp_f32 v123,v37\n"
"v_mfma_f32_16x16x32_bf16 v[12:15],a[88:91],a[24:27],v[12:15]\n"
"ds_read_b128 a[120:123],v114 offset:47104\n"
"ds_read_b128 a[124:127],v114 offset:48128\n"
"v_mfma_f32_16x16x32_bf16 v[12:15],a[92:95],a[28:31],v[12:15]\n"
"v_exp_f32 v119,v38\n"
"v_mfma_f32_16x16x32_bf16 v[8:11],a[76:79],a[28:31],v[100:103]\n"
"v_cvt_pk_bf16_f32 v82,v122,v123\n"
"v_or3_b32 v118,v80,v81,v82\n"
"v_mfma_f32_16x16x32_bf16 v[8:11],a[72:75],a[24:27],v[8:11]\n"
"v_exp_f32 v120,v39\n"
"v_mfma_f32_16x16x32_bf16 v[8:11],a[68:71],a[20:23],v[8:11]\n"
"buffer_load_dwordx4 v115,s[32:35],s54 offen sc1 lds\n"
"v_mfma_f32_16x16x32_bf16 v[8:11],a[64:67],a[16:19],v[8:11]\n"
"s_add_u32 m0,s67,49152\n"
"v_exp_f32 v122,v40\n"
"v_mfma_f32_16x16x32_bf16 v[16:19],a[64:67],a[32:35],v[104:107]\n"
"v_cvt_pk_bf16_f32 v83,v119,v120\n"
"v_mfma_f32_16x16x32_bf16 v[16:19],a[68:71],a[36:39],v[16:19]\n"
"v_exp_f32 v123,v41\n"
"v_mfma_f32_16x16x32_bf16 v[16:19],a[72:75],a[40:43],v[16:19]\n"
"buffer_load_dwordx4 v112,s[24:27],s64 offen offset:0 sc1 lds\n"
"v_mfma_f32_16x16x32_bf16 v[16:19],a[76:79],a[44:47],v[16:19]\n"
"v_exp_f32 v119,v42\n"
"v_mfma_f32_16x16x32_bf16 v[20:23],a[92:95],a[44:47],v[104:107]\n"
"v_cvt_pk_bf16_f32 v84,v122,v123\n"
"v_or3_b32 v118,v118,v83,v84\n"
"v_mfma_f32_16x16x32_bf16 v[20:23],a[88:91],a[40:43],v[20:23]\n"
"v_exp_f32 v120,v43\n"
"v_mfma_f32_16x16x32_bf16 v[20:23],a[84:87],a[36:39],v[20:23]\n"
"buffer_load_dwordx4 v112,s[24:27],s64 offen offset:1024 sc1 lds\n"
"v_mfma_f32_16x16x32_bf16 v[20:23],a[80:83],a[32:35],v[20:23]\n"
"v_exp_f32 v122,v44\n"
"v_mfma_f32_16x16x32_bf16 v[28:31],a[80:83],a[48:51],v[108:111]\n"
"v_cvt_pk_bf16_f32 v85,v119,v120\n"
"v_mfma_f32_16x16x32_bf16 v[28:31],a[84:87],a[52:55],v[28:31]\n"
"v_exp_f32 v123,v45\n"
"v_mfma_f32_16x16x32_bf16 v[28:31],a[88:91],a[56:59],v[28:31]\n"
"buffer_load_dwordx4 v112,s[24:27],s64 offen offset:2048 sc1 lds\n"
"v_mfma_f32_16x16x32_bf16 v[28:31],a[92:95],a[60:63],v[28:31]\n"
"v_exp_f32 v119,v46\n"
"v_mfma_f32_16x16x32_bf16 v[24:27],a[76:79],a[60:63],v[108:111]\n"
"v_cvt_pk_bf16_f32 v86,v122,v123\n"
"v_or3_b32 v118,v118,v85,v86\n"
"v_mfma_f32_16x16x32_bf16 v[24:27],a[72:75],a[56:59],v[24:27]\n"
"v_exp_f32 v120,v47\n"
"v_mfma_f32_16x16x32_bf16 v[24:27],a[68:71],a[52:55],v[24:27]\n"
"buffer_load_dwordx4 v112,s[24:27],s64 offen offset:3072 sc1 lds\n"
"v_mfma_f32_16x16x32_bf16 v[24:27],a[64:67],a[48:51],v[24:27]\n"
"s_waitcnt lgkmcnt(0)\n"
"v_exp_f32 v122,v48\n"
"v_mfma_f32_16x16x32_bf16 v[128:131],a[96:99],v[64:67],0\n"
"v_exp_f32 v123,v49\n"
"v_mfma_f32_16x16x32_bf16 v[160:163],a[96:99],v[68:71],0\n"
"ds_read_b128 a[64:67],v113 offset:32768\n"
"v_cvt_pk_bf16_f32 v87,v119,v120\n"
"v_mfma_f32_16x16x32_bf16 v[192:195],a[96:99],v[72:75],0\n"
"s_add_u32 m0,s67,53248\n"
"v_exp_f32 v119,v50\n"
"v_mfma_f32_16x16x32_bf16 v[224:227],a[96:99],v[76:79],0\n"
"ds_read_b128 a[68:71],v113 offset:33792\n"
"v_cvt_pk_bf16_f32 v88,v122,v123\n"
"v_mfma_f32_16x16x32_bf16 a[140:143],a[144:147],v[76:79],0\n"
"v_exp_f32 v120,v51\n"
"v_mfma_f32_16x16x32_bf16 v[228:231],a[100:103],v[76:79],0\n"
"ds_read_b128 a[72:75],v113 offset:34816\n"
"ds_read_b128 a[76:79],v113 offset:35840\n"
"v_mfma_f32_16x16x32_bf16 v[132:135],a[100:103],v[64:67],0\n"
"v_exp_f32 v122,v52\n"
"v_mfma_f32_16x16x32_bf16 v[164:167],a[100:103],v[68:71],0\n"
"ds_read_b128 a[80:83],v113 offset:36864\n"
"v_cvt_pk_bf16_f32 v89,v119,v120\n"
"v_mfma_f32_16x16x32_bf16 v[196:199],a[100:103],v[72:75],0\n"
"v_exp_f32 v123,v53\n"
"v_mfma_f32_16x16x32_bf16 a[136:139],a[144:147],v[72:75],0\n"
"ds_read_b128 a[84:87],v113 offset:37888\n"
"ds_read_b128 a[88:91],v113 offset:38912\n"
"v_mfma_f32_16x16x32_bf16 v[200:203],a[104:107],v[72:75],0\n"
"v_exp_f32 v119,v54\n"
"v_mfma_f32_16x16x32_bf16 v[232:235],a[104:107],v[76:79],0\n"
"ds_read_b128 a[92:95],v113 offset:39936\n"
"v_cvt_pk_bf16_f32 v90,v122,v123\n"
"v_mfma_f32_16x16x32_bf16 v[136:139],a[104:107],v[64:67],0\n"
"v_exp_f32 v120,v55\n"
"v_mfma_f32_16x16x32_bf16 v[168:171],a[104:107],v[68:71],0\n"
"s_waitcnt vmcnt(16)\n"
"ds_add_u32 v124,a152\n"
"v_mfma_f32_16x16x32_bf16 a[132:135],a[144:147],v[68:71],0\n"
"v_exp_f32 v122,v56\n"
"v_mfma_f32_16x16x32_bf16 v[172:175],a[108:111],v[68:71],0\n"
"v_cvt_pk_bf16_f32 v91,v119,v120\n"
"v_or3_b32 v118,v118,v87,v88\n"
"v_mfma_f32_16x16x32_bf16 v[204:207],a[108:111],v[72:75],0\n"
"v_exp_f32 v123,v57\n"
"v_mfma_f32_16x16x32_bf16 v[236:239],a[108:111],v[76:79],0\n"
"buffer_load_dwordx4 v117,s[24:27],s64 offen offset:0 sc1 lds\n"
"v_mfma_f32_16x16x32_bf16 v[140:143],a[108:111],v[64:67],0\n"
"v_exp_f32 v119,v58\n"
"v_mfma_f32_16x16x32_bf16 a[128:131],a[144:147],v[64:67],0\n"
"v_cvt_pk_bf16_f32 v92,v122,v123\n"
"v_or3_b32 v118,v118,v89,v90\n"
"v_mfma_f32_16x16x32_bf16 v[144:147],a[112:115],v[64:67],0\n"
"v_exp_f32 v120,v59\n"
"v_mfma_f32_16x16x32_bf16 v[176:179],a[112:115],v[68:71],0\n"
"buffer_load_dwordx4 v117,s[24:27],s64 offen offset:1024 sc1 lds\n"
"v_mfma_f32_16x16x32_bf16 v[208:211],a[112:115],v[72:75],0\n"
"v_exp_f32 v122,v60\n"
"v_mfma_f32_16x16x32_bf16 v[240:243],a[112:115],v[76:79],0\n"
"v_cvt_pk_bf16_f32 v93,v119,v120\n"
"v_or3_b32 v118,v118,v91,v92\n"
"v_mfma_f32_16x16x32_bf16 v[244:247],a[116:119],v[76:79],0\n"
"v_exp_f32 v123,v61\n"
"v_mfma_f32_16x16x32_bf16 v[148:151],a[116:119],v[64:67],0\n"
"buffer_load_dwordx4 v117,s[24:27],s64 offen offset:2048 sc1 lds\n"
"v_mfma_f32_16x16x32_bf16 v[180:183],a[116:119],v[68:71],0\n"
"v_exp_f32 v119,v62\n"
"v_mfma_f32_16x16x32_bf16 v[212:215],a[116:119],v[72:75],0\n"
"ds_read_b32 v125,v124\n"
"v_cvt_pk_bf16_f32 v94,v122,v123\n"
"v_mfma_f32_16x16x32_bf16 v[216:219],a[120:123],v[72:75],0\n"
"v_exp_f32 v120,v63\n"
"v_mfma_f32_16x16x32_bf16 v[248:251],a[120:123],v[76:79],0\n"
"buffer_load_dwordx4 v117,s[24:27],s64 offen offset:3072 sc1 lds\n"
"v_mfma_f32_16x16x32_bf16 v[152:155],a[120:123],v[64:67],0\n"
"buffer_store_dwordx4 a[248:251],v112,s[24:27],s56 offen offset:2048 sc1\n"
"v_mfma_f32_16x16x32_bf16 v[184:187],a[120:123],v[68:71],0\n"
"s_add_u32 s64,s64,16384\n"
"v_cvt_pk_bf16_f32 v95,v119,v120\n"
"v_or3_b32 v118,v118,v93,v94\n"
"v_mfma_f32_16x16x32_bf16 v[188:191],a[124:127],v[68:71],0\n"
"s_waitcnt lgkmcnt(0)\n"
"v_subrev_u32 v125,s101,v125\n"
"v_or3_b32 v125,v118,v95,v125\n"
"v_mfma_f32_16x16x32_bf16 v[220:223],a[124:127],v[72:75],0\n"
"v_and_b32 v125,0x40004000,v125\n"
"v_cmp_ne_u32 vcc,0,v125\n"
"v_mfma_f32_16x16x32_bf16 v[252:255],a[124:127],v[76:79],0\n"
"v_mfma_f32_16x16x32_bf16 v[156:159],a[124:127],v[64:67],0\n"
"s_cbranch_vccnz .Lfk_cs_v1\n"
".Lfk_cr_v1:\n"
"v_mfma_f32_16x16x32_bf16 v[32:35],a[64:67],a[0:3],v[96:99]\n"
"s_add_u32 s101,s101,4\n"
"v_exp_f32 v122,v0\n"
"v_mfma_f32_16x16x32_bf16 v[32:35],a[68:71],a[4:7],v[32:35]\n"
"s_add_u32 m0,s55,12288\n"
"v_exp_f32 v123,v1\n"
"v_mfma_f32_16x16x32_bf16 v[32:35],a[72:75],a[8:11],v[32:35]\n"
"ds_read_b128 a[96:99],v114 offset:8192\n"
"ds_read_b128 a[100:103],v114 offset:9216\n"
"v_mfma_f32_16x16x32_bf16 v[32:35],a[76:79],a[12:15],v[32:35]\n"
"s_add_u32 s54,s54,s22\n"
"v_exp_f32 v119,v2\n"
"v_mfma_f32_16x16x32_bf16 v[36:39],a[92:95],a[12:15],v[96:99]\n"
"ds_read_b128 a[104:107],v114 offset:10240\n"
"v_cvt_pk_bf16_f32 v64,v122,v123\n"
"v_mfma_f32_16x16x32_bf16 v[36:39],a[88:91],a[8:11],v[36:39]\n"
"v_exp_f32 v120,v3\n"
"v_mfma_f32_16x16x32_bf16 v[36:39],a[84:87],a[4:7],v[36:39]\n"
"ds_read_b128 a[108:111],v114 offset:11264\n"
"ds_read_b128 a[112:115],v114 offset:12288\n"
"v_mfma_f32_16x16x32_bf16 v[36:39],a[80:83],a[0:3],v[36:39]\n"
"v_exp_f32 v122,v4\n"
"v_mfma_f32_16x16x32_bf16 v[44:47],a[80:83],a[16:19],v[100:103]\n"
"ds_read_b128 a[116:119],v114 offset:13312\n"
"v_cvt_pk_bf16_f32 v65,v119,v120\n"
"v_mfma_f32_16x16x32_bf16 v[44:47],a[84:87],a[20:23],v[44:47]\n"
"v_exp_f32 v123,v5\n"
"v_mfma_f32_16x16x32_bf16 v[44:47],a[88:91],a[24:27],v[44:47]\n"
"ds_read_b128 a[120:123],v114 offset:14336\n"
"ds_read_b128 a[124:127],v114 offset:15360\n"
"v_mfma_f32_16x16x32_bf16 v[44:47],a[92:95],a[28:31],v[44:47]\n"
"v_exp_f32 v119,v6\n"
"v_mfma_f32_16x16x32_bf16 v[40:43],a[76:79],a[28:31],v[100:103]\n"
"v_cvt_pk_bf16_f32 v66,v122,v123\n"
"v_or3_b32 v118,v64,v65,v66\n"
"v_mfma_f32_16x16x32_bf16 v[40:43],a[72:75],a[24:27],v[40:43]\n"
"v_exp_f32 v120,v7\n"
"v_mfma_f32_16x16x32_bf16 v[40:43],a[68:71],a[20:23],v[40:43]\n"
"buffer_load_dwordx4 v115,s[32:35],s54 offen sc1 lds\n"
"v_mfma_f32_16x16x32_bf16 v[40:43],a[64:67],a[16:19],v[40:43]\n"
"s_add_u32 m0,s67,32768\n"
"v_exp_f32 v122,v8\n"
"v_mfma_f32_16x16x32_bf16 v[48:51],a[64:67],a[32:35],v[104:107]\n"
"v_cvt_pk_bf16_f32 v67,v119,v120\n"
"v_mfma_f32_16x16x32_bf16 v[48:51],a[68:71],a[36:39],v[48:51]\n"
"v_exp_f32 v123,v9\n"
"v_mfma_f32_16x16x32_bf16 v[48:51],a[72:75],a[40:43],v[48:51]\n"
"buffer_load_dwordx4 v112,s[24:27],s64 offen offset:0 sc1 lds\n"
"v_mfma_f32_16x16x32_bf16 v[48:51],a[76:79],a[44:47],v[48:51]\n"
"v_exp_f32 v119,v10\n"
"v_mfma_f32_16x16x32_bf16 v[52:55],a[92:95],a[44:47],v[104:107]\n"
"v_cvt_pk_bf16_f32 v68,v122,v123\n"
"v_or3_b32 v118,v118,v67,v68\n"
"v_mfma_f32_16x16x32_bf16 v[52:55],a[88:91],a[40:43],v[52:55]\n"
"v_exp_f32 v120,v11\n"
"v_mfma_f32_16x16x32_bf16 v[52:55],a[84:87],a[36:39],v[52:55]\n"
"buffer_load_dwordx4 v112,s[24:27],s64 offen offset:1024 sc1 lds\n"
"v_mfma_f32_16x16x32_bf16 v[52:55],a[80:83],a[32:35],v[52:55]\n"
"v_exp_f32 v122,v12\n"
"v_mfma_f32_16x16x32_bf16 v[60:63],a[80:83],a[48:51],v[108:111]\n"
"v_cvt_pk_bf16_f32 v69,v119,v120\n"
"v_mfma_f32_16x16x32_bf16 v[60:63],a[84:87],a[52:55],v[60:63]\n"
"v_exp_f32 v123,v13\n"
"v_mfma_f32_16x16x32_bf16 v[60:63],a[88:91],a[56:59],v[60:63]\n"
"buffer_load_dwordx4 v112,s[24:27],s64 offen offset:2048 sc1 lds\n"
"v_mfma_f32_16x16x32_bf16 v[60:63],a[92:95],a[60:63],v[60:63]\n"
"v_exp_f32 v119,v14\n"
"v_mfma_f32_16x16x32_bf16 v[56:59],a[76:79],a[60:63],v[108:111]\n"
"v_cvt_pk_bf16_f32 v70,v122,v123\n"
"v_or3_b32 v118,v118,v69,v70\n"
"v_mfma_f32_16x16x32_bf16 v[56:59],a[72:75],a[56:59],v[56:59]\n"
"v_exp_f32 v120,v15\n"
"v_mfma_f32_16x16x32_bf16 v[56:59],a[68:71],a[52:55],v[56:59]\n"
"buffer_load_dwordx4 v112,s[24:27],s64 offen offset:3072 sc1 lds\n"
"v_mfma_f32_16x16x32_bf16 v[56:59],a[64:67],a[48:51],v[56:59]\n"
"s_waitcnt lgkmcnt(0)\n"
"v_exp_f32 v122,v16\n"
"v_mfma_f32_16x16x32_bf16 v[128:131],a[96:99],v[80:83],v[128:131]\n"
"v_exp_f32 v123,v17\n"
"v_mfma_f32_16x16x32_bf16 v[160:163],a[96:99],v[84:87],v[160:163]\n"
"ds_read_b128 a[64:67],v113 offset:0\n"
"v_cvt_pk_bf16_f32 v71,v119,v120\n"
"v_mfma_f32_16x16x32_bf16 v[192:195],a[96:99],v[88:91],v[192:195]\n"
"s_add_u32 m0,s67,36864\n"
"v_exp_f32 v119,v18\n"
"v_mfma_f32_16x16x32_bf16 v[224:227],a[96:99],v[92:95],v[224:227]\n"
"ds_read_b128 a[68:71],v113 offset:1024\n"
"v_cvt_pk_bf16_f32 v72,v122,v123\n"
"v_mfma_f32_16x16x32_bf16 a[140:143],a[144:147],v[92:95],a[140:143]\n"
"v_exp_f32 v120,v19\n"
"v_mfma_f32_16x16x32_bf16 v[228:231],a[100:103],v[92:95],v[228:231]\n"
"ds_read_b128 a[72:75],v113 offset:2048\n"
"ds_read_b128 a[76:79],v113 offset:3072\n"
"v_mfma_f32_16x16x32_bf16 v[132:135],a[100:103],v[80:83],v[132:135]\n"
"v_exp_f32 v122,v20\n"
"v_mfma_f32_16x16x32_bf16 v[164:167],a[100:103],v[84:87],v[164:167]\n"
"ds_read_b128 a[80:83],v113 offset:4096\n"
"v_cvt_pk_bf16_f32 v73,v119,v120\n"
"v_mfma_f32_16x16x32_bf16 v[196:199],a[100:103],v[88:91],v[196:199]\n"
"v_exp_f32 v123,v21\n"
"v_mfma_f32_16x16x32_bf16 a[136:139],a[144:147],v[88:91],a[136:139]\n"
"ds_read_b128 a[84:87],v113 offset:5120\n"
"ds_read_b128 a[88:91],v113 offset:6144\n"
"v_mfma_f32_16x16x32_bf16 v[200:203],a[104:107],v[88:91],v[200:203]\n"
"v_exp_f32 v119,v22\n"
"v_mfma_f32_16x16x32_bf16 v[232:235],a[104:107],v[92:95],v[232:235]\n"
"ds_read_b128 a[92:95],v113 offset:7168\n"
"v_cvt_pk_bf16_f32 v74,v122,v123\n"
"v_mfma_f32_16x16x32_bf16 v[136:139],a[104:107],v[80:83],v[136:139]\n"
"v_exp_f32 v120,v23\n"
"v_mfma_f32_16x16x32_bf16 v[168:171],a[104:107],v[84:87],v[168:171]\n"
"s_waitcnt vmcnt(16)\n"
"ds_add_u32 v124,a152\n"
"v_mfma_f32_16x16x32_bf16 a[132:135],a[144:147],v[84:87],a[132:135]\n"
"v_exp_f32 v122,v24\n"
"v_mfma_f32_16x16x32_bf16 v[172:175],a[108:111],v[84:87],v[172:175]\n"
"v_cvt_pk_bf16_f32 v75,v119,v120\n"
"v_or3_b32 v118,v118,v71,v72\n"
"v_mfma_f32_16x16x32_bf16 v[204:207],a[108:111],v[88:91],v[204:207]\n"
"v_exp_f32 v123,v25\n"
"v_mfma_f32_16x16x32_bf16 v[236:239],a[108:111],v[92:95],v[236:239]\n"
"buffer_load_dwordx4 v117,s[24:27],s64 offen offset:0 sc1 lds\n"
"v_mfma_f32_16x16x32_bf16 v[140:143],a[108:111],v[80:83],v[140:143]\n"
"v_exp_f32 v119,v26\n"
"v_mfma_f32_16x16x32_bf16 a[128:131],a[144:147],v[80:83],a[128:131]\n"
"v_cvt_pk_bf16_f32 v76,v122,v123\n"
"v_or3_b32 v118,v118,v73,v74\n"
"v_mfma_f32_16x16x32_bf16 v[144:147],a[112:115],v[80:83],v[144:147]\n"
"v_exp_f32 v120,v27\n"
"v_mfma_f32_16x16x32_bf16 v[176:179],a[112:115],v[84:87],v[176:179]\n"
"buffer_load_dwordx4 v117,s[24:27],s64 offen offset:1024 sc1 lds\n"
"v_mfma_f32_16x16x32_bf16 v[208:211],a[112:115],v[88:91],v[208:211]\n"
"v_exp_f32 v122,v28\n"
"v_mfma_f32_16x16x32_bf16 v[240:243],a[112:115],v[92:95],v[240:243]\n"
"v_cvt_pk_bf16_f32 v77,v119,v120\n"
"v_or3_b32 v118,v118,v75,v76\n"
"v_mfma_f32_16x16x32_bf16 v[244:247],a[116:119],v[92:95],v[244:247]\n"
"v_exp_f32 v123,v29\n"
"v_mfma_f32_16x16x32_bf16 v[148:151],a[116:119],v[80:83],v[148:151]\n"
"buffer_load_dwordx4 v117,s[24:27],s64 offen offset:2048 sc1 lds\n"
"v_mfma_f32_16x16x32_bf16 v[180:183],a[116:119],v[84:87],v[180:183]\n"
"v_exp_f32 v119,v30\n"
"v_mfma_f32_16x16x32_bf16 v[212:215],a[116:119],v[88:91],v[212:215]\n"
"ds_read_b32 v125,v124\n"
"v_cvt_pk_bf16_f32 v78,v122,v123\n"
"v_mfma_f32_16x16x32_bf16 v[216:219],a[120:123],v[88:91],v[216:219]\n"
"v_exp_f32 v120,v31\n"
"v_mfma_f32_16x16x32_bf16 v[248:251],a[120:123],v[92:95],v[248:251]\n"
"buffer_load_dwordx4 v117,s[24:27],s64 offen offset:3072 sc1 lds\n"
"v_mfma_f32_16x16x32_bf16 v[152:155],a[120:123],v[80:83],v[152:155]\n"
"buffer_store_dwordx4 a[252:255],v112,s[24:27],s56 offen offset:3072 sc1\n"
"v_mfma_f32_16x16x32_bf16 v[184:187],a[120:123],v[84:87],v[184:187]\n"
"s_add_u32 s64,s64,16384\n"
"s_add_u32 s89,s89,1\n"
"v_cvt_pk_bf16_f32 v79,v119,v120\n"
"v_mfma_f32_16x16x32_bf16 v[188:191],a[124:127],v[84:87],v[188:191]\n"
"s_waitcnt lgkmcnt(0)\n"
"v_subrev_u32 v125,s101,v125\n"
"v_or3_b32 v118,v118,v77,v78\n"
"v_mfma_f32_16x16x32_bf16 v[220:223],a[124:127],v[88:91],v[220:223]\n"
"v_or3_b32 v125,v118,v79,v125\n"
"v_and_b32 v125,0x40004000,v125\n"
"v_mfma_f32_16x16x32_bf16 v[252:255],a[124:127],v[92:95],v[252:255]\n"
"v_cmp_ne_u32 vcc,0,v125\n"
"v_mfma_f32_16x16x32_bf16 v[156:159],a[124:127],v[80:83],v[156:159]\n"
"s_cbranch_vccnz .Lfk_cs_v2\n"
".Lfk_cr_v2:\n"
"s_waitcnt vmcnt(20)\n"
"buffer_store_dwordx2 a[154:155],off,s[24:27],s69 sc1\n"
"s_branch .Lfk_Hx\n"
".Lfk_tailB:\n"
"s_waitcnt vmcnt(11) lgkmcnt(0)\n"
"s_barrier\n"
"v_mfma_f32_16x16x32_bf16 v[0:3],a[64:67],a[0:3],v[96:99]\n"
"ds_read_b128 a[96:99],v114 offset:24576\n"
"ds_read_b128 a[100:103],v114 offset:25600\n"
"ds_read_b128 a[104:107],v114 offset:26624\n"
"v_mfma_f32_16x16x32_bf16 v[0:3],a[68:71],a[4:7],v[0:3]\n"
"ds_read_b128 a[108:111],v114 offset:27648\n"
"ds_read_b128 a[112:115],v114 offset:28672\n"
"ds_read_b128 a[116:119],v114 offset:29696\n"
"v_mfma_f32_16x16x32_bf16 v[0:3],a[72:75],a[8:11],v[0:3]\n"
"ds_read_b128 a[120:123],v114 offset:30720\n"
"ds_read_b128 a[124:127],v114 offset:31744\n"
"v_add_f32 v16,0x80000000,v126\n"
"v_mfma_f32_16x16x32_bf16 v[0:3],a[76:79],a[12:15],v[0:3]\n"
"v_min_f32 v16,v16,v104\n"
"v_add_f32 v17,0xf149f2ca,v126\n"
"v_min_f32 v17,v17,v105\n"
"v_mfma_f32_16x16x32_bf16 v[4:7],a[92:95],a[12:15],v[96:99]\n"
"v_add_f32 v18,0xf1c9f2ca,v126\n"
"v_min_f32 v18,v18,v106\n"
"v_add_f32 v19,0xf2177617,v126\n"
"v_mfma_f32_16x16x32_bf16 v[4:7],a[88:91],a[8:11],v[4:7]\n"
"v_min_f32 v19,v19,v107\n"
"v_add_f32 v20,0xf349f2ca,v126\n"
"v_min_f32 v20,v20,v104\n"
"v_mfma_f32_16x16x32_bf16 v[4:7],a[84:87],a[4:7],v[4:7]\n"
"v_add_f32 v21,0xf35691f6,v126\n"
"v_min_f32 v21,v21,v105\n"
"v_add_f32 v22,0xf3633123,v126\n"
"v_mfma_f32_16x16x32_bf16 v[4:7],a[80:83],a[0:3],v[4:7]\n"
"v_min_f32 v22,v22,v106\n"
"v_add_f32 v23,0xf36fd050,v126\n"
"v_min_f32 v23,v23,v107\n"
"v_mfma_f32_16x16x32_bf16 v[12:15],a[80:83],a[16:19],v[100:103]\n"
"v_exp_f32 v32,v32\n"
"v_max3_f32 v118,v0,v1,v2\n"
"v_mfma_f32_16x16x32_bf16 v[12:15],a[84:87],a[20:23],v[12:15]\n"
"v_exp_f32 v33,v33\n"
"v_mfma_f32_16x16x32_bf16 v[12:15],a[88:91],a[24:27],v[12:15]\n"
"v_exp_f32 v34,v34\n"
"v_cvt_pk_bf16_f32 v80,v32,v33\n"
"v_mfma_f32_16x16x32_bf16 v[12:15],a[92:95],a[28:31],v[12:15]\n"
"v_exp_f32 v35,v35\n"
"v_max3_f32 v118,v118,v3,v4\n"
"v_mfma_f32_16x16x32_bf16 v[8:11],a[76:79],a[28:31],v[100:103]\n"
"v_exp_f32 v36,v36\n"
"v_cvt_pk_bf16_f32 v81,v34,v35\n"
"v_mfma_f32_16x16x32_bf16 v[8:11],a[72:75],a[24:27],v[8:11]\n"
"v_exp_f32 v37,v37\n"
"v_max3_f32 v118,v118,v5,v6\n"
"v_mfma_f32_16x16x32_bf16 v[8:11],a[68:71],a[20:23],v[8:11]\n"
"v_exp_f32 v38,v38\n"
"v_mfma_f32_16x16x32_bf16 v[8:11],a[64:67],a[16:19],v[8:11]\n"
"v_exp_f32 v39,v39\n"
"v_max3_f32 v118,v118,v7,v12\n"
"v_mfma_f32_16x16x32_bf16 v[24:27],a[64:67],a[48:51],v[108:111]\n"
"v_exp_f32 v40,v40\n"
"v_mfma_f32_16x16x32_bf16 v[24:27],a[68:71],a[52:55],v[24:27]\n"
"v_exp_f32 v41,v41\n"
"v_max3_f32 v118,v118,v13,v14\n"
"v_mfma_f32_16x16x32_bf16 v[24:27],a[72:75],a[56:59],v[24:27]\n"
"v_cvt_pk_bf16_f32 v82,v36,v37\n"
"v_cvt_pk_bf16_f32 v84,v40,v41\n"
"v_mfma_f32_16x16x32_bf16 v[24:27],a[76:79],a[60:63],v[24:27]\n"
"v_exp_f32 v42,v42\n"
"v_max3_f32 v118,v118,v15,v8\n"
"v_mfma_f32_16x16x32_bf16 v[28:31],a[92:95],a[60:63],v[108:111]\n"
"v_cvt_pk_bf16_f32 v83,v38,v39\n"
"v_max3_f32 v118,v118,v9,v10\n"
"v_mfma_f32_16x16x32_bf16 v[28:31],a[88:91],a[56:59],v[28:31]\n"
"v_exp_f32 v43,v43\n"
"v_mfma_f32_16x16x32_bf16 v[28:31],a[84:87],a[52:55],v[28:31]\n"
"v_cvt_pk_bf16_f32 v85,v42,v43\n"
"v_mfma_f32_16x16x32_bf16 v[28:31],a[80:83],a[48:51],v[28:31]\n"
"v_exp_f32 v44,v44\n"
"v_mfma_f32_16x16x32_bf16 v[20:23],a[80:83],a[32:35],v[20:23]\n"
"v_max3_f32 v118,v118,v11,v24\n"
"v_max3_f32 v118,v118,v25,v26\n"
"v_mfma_f32_16x16x32_bf16 v[20:23],a[84:87],a[36:39],v[20:23]\n"
"v_exp_f32 v45,v45\n"
"v_mfma_f32_16x16x32_bf16 v[20:23],a[88:91],a[40:43],v[20:23]\n"
"v_cvt_pk_bf16_f32 v86,v44,v45\n"
"v_mfma_f32_16x16x32_bf16 v[20:23],a[92:95],a[44:47],v[20:23]\n"
"v_exp_f32 v46,v46\n"
"v_mfma_f32_16x16x32_bf16 v[16:19],a[76:79],a[44:47],v[16:19]\n"
"v_max3_f32 v118,v118,v27,v28\n"
"v_max3_f32 v118,v118,v29,v30\n"
"v_mfma_f32_16x16x32_bf16 v[16:19],a[72:75],a[40:43],v[16:19]\n"
"v_exp_f32 v47,v47\n"
"v_mfma_f32_16x16x32_bf16 v[16:19],a[68:71],a[36:39],v[16:19]\n"
"v_cvt_pk_bf16_f32 v87,v46,v47\n"
"v_exp_f32 v48,v48\n"
"v_mfma_f32_16x16x32_bf16 v[16:19],a[64:67],a[32:35],v[16:19]\n"
"v_max3_f32 v118,v118,v31,v20\n"
"s_waitcnt lgkmcnt(0)\n"
"v_mfma_f32_16x16x32_bf16 v[128:131],a[96:99],v[64:67],v[128:131]\n"
"v_exp_f32 v49,v49\n"
"v_max3_f32 v118,v118,v21,v22\n"
"v_cvt_pk_bf16_f32 v88,v48,v49\n"
"v_mfma_f32_16x16x32_bf16 v[160:163],a[96:99],v[68:71],v[160:163]\n"
"ds_read_b128 a[64:67],v114 offset:57344\n"
"ds_read_b128 a[68:71],v114 offset:58368\n"
"v_mfma_f32_16x16x32_bf16 v[192:195],a[96:99],v[72:75],v[192:195]\n"
"ds_read_b128 a[72:75],v114 offset:59392\n"
"v_exp_f32 v50,v50\n"
"v_mfma_f32_16x16x32_bf16 v[224:227],a[96:99],v[76:79],v[224:227]\n"
"v_max3_f32 v118,v118,v23,v16\n"
"v_max3_f32 v118,v118,v17,v18\n"
"v_max_f32 v118,v118,v19\n"
"v_mfma_f32_16x16x32_bf16 a[140:143],a[144:147],v[76:79],a[140:143]\n"
"v_exp_f32 v51,v51\n"
"v_cmp_lt_f32 vcc,s93,v118\n"
"v_cvt_pk_bf16_f32 v89,v50,v51\n"
"v_mfma_f32_16x16x32_bf16 v[228:231],a[100:103],v[76:79],v[228:231]\n"
"ds_read_b128 a[76:79],v114 offset:60416\n"
"ds_read_b128 a[80:83],v114 offset:61440\n"
"v_mfma_f32_16x16x32_bf16 v[132:135],a[100:103],v[64:67],v[132:135]\n"
"ds_read_b128 a[84:87],v114 offset:62464\n"
"v_exp_f32 v52,v52\n"
"v_mfma_f32_16x16x32_bf16 v[164:167],a[100:103],v[68:71],v[164:167]\n"
"s_sub_u32 s82,s3,32768\n"
"ds_read_b128 a[88:91],v114 offset:63488\n"
"ds_read_b128 a[92:95],v114 offset:64512\n"
"v_mfma_f32_16x16x32_bf16 v[196:199],a[100:103],v[72:75],v[196:199]\n"
"buffer_load_dwordx4 a[0:3],v112,s[24:27],s82 offen offset:0 sc1\n"
"v_exp_f32 v53,v53\n"
"v_mfma_f32_16x16x32_bf16 a[136:139],a[144:147],v[72:75],a[136:139]\n"
"buffer_load_dwordx4 a[4:7],v112,s[24:27],s82 offen offset:1024 sc1\n"
"buffer_load_dwordx4 a[8:11],v112,s[24:27],s82 offen offset:2048 sc1\n"
"v_cvt_pk_bf16_f32 v90,v52,v53\n"
"v_exp_f32 v54,v54\n"
"v_mfma_f32_16x16x32_bf16 v[200:203],a[104:107],v[72:75],v[200:203]\n"
"buffer_load_dwordx4 a[12:15],v112,s[24:27],s82 offen offset:3072 sc1\n"
"v_mfma_f32_16x16x32_bf16 v[232:235],a[104:107],v[76:79],v[232:235]\n"
"s_sub_u32 s82,s3,49152\n"
"buffer_load_dwordx4 a[16:19],v112,s[24:27],s82 offen offset:0 sc1\n"
"buffer_load_dwordx4 a[20:23],v112,s[24:27],s82 offen offset:1024 sc1\n"
"v_exp_f32 v55,v55\n"
"v_mfma_f32_16x16x32_bf16 v[136:139],a[104:107],v[64:67],v[136:139]\n"
"buffer_load_dwordx4 a[24:27],v112,s[24:27],s82 offen offset:2048 sc1\n"
"v_mfma_f32_16x16x32_bf16 v[168:171],a[104:107],v[68:71],v[168:171]\n"
"buffer_load_dwordx4 a[28:31],v112,s[24:27],s82 offen offset:3072 sc1\n"
"v_mfma_f32_16x16x32_bf16 a[132:135],a[144:147],v[68:71],a[132:135]\n"
"v_exp_f32 v56,v56\n"
"v_mfma_f32_16x16x32_bf16 v[172:175],a[108:111],v[68:71],v[172:175]\n"
"v_cvt_pk_bf16_f32 v91,v54,v55\n"
"v_mfma_f32_16x16x32_bf16 v[204:207],a[108:111],v[72:75],v[204:207]\n"
"v_exp_f32 v57,v57\n"
"v_mfma_f32_16x16x32_bf16 v[236:239],a[108:111],v[76:79],v[236:239]\n"
"v_cvt_pk_bf16_f32 v92,v56,v57\n"
"v_mfma_f32_16x16x32_bf16 v[140:143],a[108:111],v[64:67],v[140:143]\n"
"v_exp_f32 v58,v58\n"
"v_mfma_f32_16x16x32_bf16 a[128:131],a[144:147],v[64:67],a[128:131]\n"
"v_mfma_f32_16x16x32_bf16 v[144:147],a[112:115],v[64:67],v[144:147]\n"
"v_exp_f32 v59,v59\n"
"v_mfma_f32_16x16x32_bf16 v[176:179],a[112:115],v[68:71],v[176:179]\n"
"v_cvt_pk_bf16_f32 v93,v58,v59\n"
"v_mfma_f32_16x16x32_bf16 v[208:211],a[112:115],v[72:75],v[208:211]\n"
"v_exp_f32 v60,v60\n"
"v_mfma_f32_16x16x32_bf16 v[240:243],a[112:115],v[76:79],v[240:243]\n"
"v_mfma_f32_16x16x32_bf16 v[244:247],a[116:119],v[76:79],v[244:247]\n"
"v_exp_f32 v61,v61\n"
"v_mfma_f32_16x16x32_bf16 v[148:151],a[116:119],v[64:67],v[148:151]\n"
"v_cvt_pk_bf16_f32 v94,v60,v61\n"
"v_mfma_f32_16x16x32_bf16 v[180:183],a[116:119],v[68:71],v[180:183]\n"
"v_exp_f32 v62,v62\n"
"v_mfma_f32_16x16x32_bf16 v[212:215],a[116:119],v[72:75],v[212:215]\n"
"v_mfma_f32_16x16x32_bf16 v[216:219],a[120:123],v[72:75],v[216:219]\n"
"v_exp_f32 v63,v63\n"
"v_mfma_f32_16x16x32_bf16 v[248:251],a[120:123],v[76:79],v[248:251]\n"
"v_cvt_pk_bf16_f32 v95,v62,v63\n"
"v_mfma_f32_16x16x32_bf16 v[152:155],a[120:123],v[64:67],v[152:155]\n"
"v_mfma_f32_16x16x32_bf16 v[184:187],a[120:123],v[68:71],v[184:187]\n"
"v_mfma_f32_16x16x32_bf16 v[188:191],a[124:127],v[68:71],v[188:191]\n"
"v_mfma_f32_16x16x32_bf16 v[220:223],a[124:127],v[72:75],v[220:223]\n"
"v_mfma_f32_16x16x32_bf16 v[252:255],a[124:127],v[76:79],v[252:255]\n"
"v_mfma_f32_16x16x32_bf16 v[156:159],a[124:127],v[64:67],v[156:159]\n"
"s_cbranch_vccnz .Lfk_cs_b3\n"
".Lfk_cr_b3:\n"
"s_waitcnt vmcnt(9) lgkmcnt(0)\n"
"s_barrier\n"
"v_mfma_f32_16x16x32_bf16 v[128:131],a[64:67],v[80:83],v[128:131]\n"
"v_exp_f32 v0,v0\n"
"v_mfma_f32_16x16x32_bf16 v[160:163],a[64:67],v[84:87],v[160:163]\n"
"v_exp_f32 v1,v1\n"
"v_mfma_f32_16x16x32_bf16 v[192:195],a[64:67],v[88:91],v[192:195]\n"
"v_exp_f32 v2,v2\n"
"v_cvt_pk_bf16_f32 v64,v0,v1\n"
"v_mfma_f32_16x16x32_bf16 v[224:227],a[64:67],v[92:95],v[224:227]\n"
"v_exp_f32 v3,v3\n"
"v_mfma_f32_16x16x32_bf16 a[140:143],a[144:147],v[92:95],a[140:143]\n"
"v_exp_f32 v4,v4\n"
"v_cvt_pk_bf16_f32 v65,v2,v3\n"
"v_mfma_f32_16x16x32_bf16 v[228:231],a[68:71],v[92:95],v[228:231]\n"
"v_exp_f32 v5,v5\n"
"v_mfma_f32_16x16x32_bf16 v[132:135],a[68:71],v[80:83],v[132:135]\n"
"v_exp_f32 v6,v6\n"
"v_cvt_pk_bf16_f32 v66,v4,v5\n"
"v_exp_f32 v7,v7\n"
"v_mfma_f32_16x16x32_bf16 v[164:167],a[68:71],v[84:87],v[164:167]\n"
"ds_read_b128 a[96:99],v114 offset:40960\n"
"ds_read_b128 a[100:103],v114 offset:41984\n"
"v_exp_f32 v8,v8\n"
"v_mfma_f32_16x16x32_bf16 v[196:199],a[68:71],v[88:91],v[196:199]\n"
"ds_read_b128 a[104:107],v114 offset:43008\n"
"ds_read_b128 a[108:111],v114 offset:44032\n"
"v_exp_f32 v9,v9\n"
"v_mfma_f32_16x16x32_bf16 a[136:139],a[144:147],v[88:91],a[136:139]\n"
"ds_read_b128 a[112:115],v114 offset:45056\n"
"ds_read_b128 a[116:119],v114 offset:46080\n"
"v_exp_f32 v10,v10\n"
"v_mfma_f32_16x16x32_bf16 v[200:203],a[72:75],v[88:91],v[200:203]\n"
"ds_read_b128 a[120:123],v114 offset:47104\n"
"ds_read_b128 a[124:127],v114 offset:48128\n"
"v_mfma_f32_16x16x32_bf16 v[232:235],a[72:75],v[92:95],v[232:235]\n"
"v_exp_f32 v11,v11\n"
"v_accvgpr_read_b32 v119,a150\n"
"v_accvgpr_read_b32 v120,a151\n"
"v_mfma_f32_16x16x32_bf16 v[136:139],a[72:75],v[80:83],v[136:139]\n"
"v_exp_f32 v12,v12\n"
"v_xor_b32 v119,s80,v119\n"
"v_xor_b32 v120,s81,v120\n"
"v_mfma_f32_16x16x32_bf16 v[168:171],a[72:75],v[84:87],v[168:171]\n"
"v_exp_f32 v13,v13\n"
"v_or_b32 v119,v119,v120\n"
"v_cmp_ne_u32 s[14:15],0,v119\n"
"v_mfma_f32_16x16x32_bf16 a[132:135],a[144:147],v[84:87],a[132:135]\n"
"s_add_u32 s18,s8,1032192\n"
"v_exp_f32 v14,v14\n"
"s_cmp_lg_u32 s14,0\n"
"s_cselect_b32 s18,2146435072,s18\n"
"v_mfma_f32_16x16x32_bf16 v[172:175],a[76:79],v[84:87],v[172:175]\n"
"s_add_u32 m0,s30,0\n"
"v_exp_f32 v15,v15\n"
"buffer_load_dwordx4 v112,s[24:27],s18 offen offset:0 sc1 lds\n"
"buffer_load_dwordx4 v112,s[24:27],s18 offen offset:1024 sc1 lds\n"
"v_exp_f32 v16,v16\n"
"v_mfma_f32_16x16x32_bf16 v[204:207],a[76:79],v[88:91],v[204:207]\n"
"buffer_load_dwordx4 v112,s[24:27],s18 offen offset:2048 sc1 lds\n"
"buffer_load_dwordx4 v112,s[24:27],s18 offen offset:3072 sc1 lds\n"
"v_mfma_f32_16x16x32_bf16 v[236:239],a[76:79],v[92:95],v[236:239]\n"
"s_add_u32 m0,m0,4096\n"
"v_exp_f32 v17,v17\n"
"v_cvt_pk_bf16_f32 v67,v6,v7\n"
"v_mfma_f32_16x16x32_bf16 v[140:143],a[76:79],v[80:83],v[140:143]\n"
"buffer_load_dwordx4 v117,s[24:27],s18 offen offset:0 sc1 lds\n"
"v_exp_f32 v18,v18\n"
"v_cvt_pk_bf16_f32 v68,v8,v9\n"
"v_cvt_pk_bf16_f32 v69,v10,v11\n"
"v_exp_f32 v19,v19\n"
"v_mfma_f32_16x16x32_bf16 a[128:131],a[144:147],v[80:83],a[128:131]\n"
"buffer_load_dwordx4 v117,s[24:27],s18 offen offset:1024 sc1 lds\n"
"v_cvt_pk_bf16_f32 v70,v12,v13\n"
"v_mfma_f32_16x16x32_bf16 v[144:147],a[80:83],v[80:83],v[144:147]\n"
"v_exp_f32 v20,v20\n"
"v_cvt_pk_bf16_f32 v71,v14,v15\n"
"v_cvt_pk_bf16_f32 v72,v16,v17\n"
"v_exp_f32 v21,v21\n"
"v_mfma_f32_16x16x32_bf16 v[176:179],a[80:83],v[84:87],v[176:179]\n"
"buffer_load_dwordx4 v117,s[24:27],s18 offen offset:2048 sc1 lds\n"
"v_cvt_pk_bf16_f32 v73,v18,v19\n"
"v_mfma_f32_16x16x32_bf16 v[208:211],a[80:83],v[88:91],v[208:211]\n"
"v_exp_f32 v22,v22\n"
"v_cvt_pk_bf16_f32 v74,v20,v21\n"
"v_exp_f32 v23,v23\n"
"v_mfma_f32_16x16x32_bf16 v[240:243],a[80:83],v[92:95],v[240:243]\n"
"buffer_load_dwordx4 v117,s[24:27],s18 offen offset:3072 sc1 lds\n"
"v_mfma_f32_16x16x32_bf16 v[244:247],a[84:87],v[92:95],v[244:247]\n"
"s_sub_u32 s82,s3,65536\n"
"v_exp_f32 v24,v24\n"
"v_cvt_pk_bf16_f32 v75,v22,v23\n"
"v_exp_f32 v25,v25\n"
"v_mfma_f32_16x16x32_bf16 v[148:151],a[84:87],v[80:83],v[148:151]\n"
"buffer_load_dwordx4 v[32:35],v112,s[24:27],s82 offen offset:0 sc1\n"
"v_mfma_f32_16x16x32_bf16 v[180:183],a[84:87],v[84:87],v[180:183]\n"
"v_exp_f32 v26,v26\n"
"v_cvt_pk_bf16_f32 v76,v24,v25\n"
"v_exp_f32 v27,v27\n"
"v_mfma_f32_16x16x32_bf16 v[212:215],a[84:87],v[88:91],v[212:215]\n"
"buffer_load_dwordx4 v[36:39],v112,s[24:27],s82 offen offset:1024 sc1\n"
"v_mfma_f32_16x16x32_bf16 v[216:219],a[88:91],v[88:91],v[216:219]\n"
"v_exp_f32 v28,v28\n"
"v_exp_f32 v29,v29\n"
"v_mfma_f32_16x16x32_bf16 v[248:251],a[88:91],v[92:95],v[248:251]\n"
"buffer_load_dwordx4 v[40:43],v112,s[24:27],s82 offen offset:2048 sc1\n"
"v_mfma_f32_16x16x32_bf16 v[152:155],a[88:91],v[80:83],v[152:155]\n"
"v_exp_f32 v30,v30\n"
"v_mfma_f32_16x16x32_bf16 v[184:187],a[88:91],v[84:87],v[184:187]\n"
"v_exp_f32 v31,v31\n"
"v_mfma_f32_16x16x32_bf16 v[188:191],a[92:95],v[84:87],v[188:191]\n"
"buffer_load_dwordx4 v[44:47],v112,s[24:27],s82 offen offset:3072 sc1\n"
"v_mfma_f32_16x16x32_bf16 v[220:223],a[92:95],v[88:91],v[220:223]\n"
"v_cvt_pk_bf16_f32 v78,v28,v29\n"
"v_cvt_pk_bf16_f32 v77,v26,v27\n"
"v_mfma_f32_16x16x32_bf16 v[252:255],a[92:95],v[92:95],v[252:255]\n"
"v_cvt_pk_bf16_f32 v79,v30,v31\n"
"v_mfma_f32_16x16x32_bf16 v[156:159],a[92:95],v[80:83],v[156:159]\n"
"s_load_dwordx2 s[36:37],s[0:1],0\n"
"s_load_dwordx2 s[44:45],s[0:1],80\n"
"s_load_dwordx2 s[48:49],s[0:1],88\n"
"s_lshl_b32 s82,s78,2\n"
"s_add_u32 s82,s82,65536\n"
"v_add_u32 v113,s82,v112\n"
"s_lshr_b32 s82,s78,2\n"
"s_add_u32 s82,s82,122880\n"
"v_lshrrev_b32 v115,1,v112\n"
"v_add_u32 v116,s82,v115\n"
"s_waitcnt lgkmcnt(0)\n"
"s_barrier\n"
"v_mfma_f32_16x16x32_bf16 v[128:131],a[96:99],v[64:67],v[128:131]\n"
"s_add_u32 s83,s18,2097152\n"
"s_add_u32 m0,s30,32768\n"
"v_mfma_f32_16x16x32_bf16 a[128:131],a[144:147],v[64:67],a[128:131]\n"
"buffer_load_dwordx4 v112,s[24:27],s83 offen offset:0 sc1 lds\n"
"v_mfma_f32_16x16x32_bf16 v[132:135],a[100:103],v[64:67],v[132:135]\n"
"buffer_load_dwordx4 v112,s[24:27],s83 offen offset:1024 sc1 lds\n"
"v_mfma_f32_16x16x32_bf16 v[136:139],a[104:107],v[64:67],v[136:139]\n"
"buffer_load_dwordx4 v112,s[24:27],s83 offen offset:2048 sc1 lds\n"
"v_mfma_f32_16x16x32_bf16 v[140:143],a[108:111],v[64:67],v[140:143]\n"
"v_mfma_f32_16x16x32_bf16 v[144:147],a[112:115],v[64:67],v[144:147]\n"
"buffer_load_dwordx4 v112,s[24:27],s83 offen offset:3072 sc1 lds\n"
"v_mfma_f32_16x16x32_bf16 v[148:151],a[116:119],v[64:67],v[148:151]\n"
"s_add_u32 m0,m0,4096\n"
"v_mfma_f32_16x16x32_bf16 v[152:155],a[120:123],v[64:67],v[152:155]\n"
"v_mfma_f32_16x16x32_bf16 v[156:159],a[124:127],v[64:67],v[156:159]\n"
"buffer_load_dwordx4 v117,s[24:27],s83 offen offset:0 sc1 lds\n"
"v_mfma_f32_16x16x32_bf16 v[160:163],a[96:99],v[68:71],v[160:163]\n"
"v_accvgpr_read_b32 v97,a128\n"
"v_cvt_pk_bf16_f32 v136,v136,v137\n"
"v_mfma_f32_16x16x32_bf16 a[132:135],a[144:147],v[68:71],a[132:135]\n"
"buffer_load_dwordx4 v117,s[24:27],s83 offen offset:1024 sc1 lds\n"
"v_mfma_f32_16x16x32_bf16 v[164:167],a[100:103],v[68:71],v[164:167]\n"
"v_cvt_pk_bf16_f32 v128,v128,v129\n"
"v_cvt_pk_bf16_f32 v129,v130,v131\n"
"v_mfma_f32_16x16x32_bf16 v[168:171],a[104:107],v[68:71],v[168:171]\n"
"buffer_load_dwordx4 v117,s[24:27],s83 offen offset:2048 sc1 lds\n"
"v_cvt_pk_bf16_f32 v130,v132,v133\n"
"v_cvt_pk_bf16_f32 v131,v134,v135\n"
"v_mfma_f32_16x16x32_bf16 v[172:175],a[108:111],v[68:71],v[172:175]\n"
"ds_write_b128 v113,v[128:131] offset:0\n"
"v_cvt_pk_bf16_f32 v137,v138,v139\n"
"v_mfma_f32_16x16x32_bf16 v[176:179],a[112:115],v[68:71],v[176:179]\n"
"buffer_load_dwordx4 v117,s[24:27],s83 offen offset:3072 sc1 lds\n"
"v_cvt_pk_bf16_f32 v138,v140,v141\n"
"v_cvt_pk_bf16_f32 v139,v142,v143\n"
"v_mfma_f32_16x16x32_bf16 v[180:183],a[116:119],v[68:71],v[180:183]\n"
"s_sub_u32 s82,s3,81920\n"
"ds_write_b128 v113,v[136:139] offset:1024\n"
"v_cvt_pk_bf16_f32 v144,v144,v145\n"
"v_mfma_f32_16x16x32_bf16 v[184:187],a[120:123],v[68:71],v[184:187]\n"
"buffer_load_dwordx4 a[32:35],v112,s[24:27],s82 offen offset:0 sc1\n"
"v_cvt_pk_bf16_f32 v145,v146,v147\n"
"v_cvt_pk_bf16_f32 v146,v148,v149\n"
"v_cvt_pk_bf16_f32 v147,v150,v151\n"
"v_mfma_f32_16x16x32_bf16 v[188:191],a[124:127],v[68:71],v[188:191]\n"
"ds_write_b128 v113,v[144:147] offset:2048\n"
"v_cvt_pk_bf16_f32 v152,v152,v153\n"
"v_mfma_f32_16x16x32_bf16 v[192:195],a[96:99],v[72:75],v[192:195]\n"
"buffer_load_dwordx4 a[36:39],v112,s[24:27],s82 offen offset:1024 sc1\n"
"v_cvt_pk_bf16_f32 v153,v154,v155\n"
"v_cvt_pk_bf16_f32 v154,v156,v157\n"
"v_cvt_pk_bf16_f32 v155,v158,v159\n"
"v_mfma_f32_16x16x32_bf16 a[136:139],a[144:147],v[72:75],a[136:139]\n"
"ds_write_b128 v113,v[152:155] offset:3072\n"
"ds_write_b64 v116,v[96:97] offset:0\n"
"v_mfma_f32_16x16x32_bf16 v[196:199],a[100:103],v[72:75],v[196:199]\n"
"buffer_load_dwordx4 a[40:43],v112,s[24:27],s82 offen offset:2048 sc1\n"
"v_accvgpr_read_b32 v101,a132\n"
"v_cvt_pk_bf16_f32 v160,v160,v161\n"
"v_mfma_f32_16x16x32_bf16 v[200:203],a[104:107],v[72:75],v[200:203]\n"
"v_cvt_pk_bf16_f32 v161,v162,v163\n"
"v_cvt_pk_bf16_f32 v162,v164,v165\n"
"v_cvt_pk_bf16_f32 v163,v166,v167\n"
"v_mfma_f32_16x16x32_bf16 v[204:207],a[108:111],v[72:75],v[204:207]\n"
"ds_write_b128 v113,v[160:163] offset:4096\n"
"v_cvt_pk_bf16_f32 v168,v168,v169\n"
"v_cvt_pk_bf16_f32 v169,v170,v171\n"
"v_mfma_f32_16x16x32_bf16 v[208:211],a[112:115],v[72:75],v[208:211]\n"
"buffer_load_dwordx4 a[44:47],v112,s[24:27],s82 offen offset:3072 sc1\n"
"v_cvt_pk_bf16_f32 v170,v172,v173\n"
"v_cvt_pk_bf16_f32 v171,v174,v175\n"
"v_mfma_f32_16x16x32_bf16 v[212:215],a[116:119],v[72:75],v[212:215]\n"
"ds_write_b128 v113,v[168:171] offset:5120\n"
"v_cvt_pk_bf16_f32 v176,v176,v177\n"
"v_cvt_pk_bf16_f32 v177,v178,v179\n"
"v_mfma_f32_16x16x32_bf16 v[216:219],a[120:123],v[72:75],v[216:219]\n"
"v_cvt_pk_bf16_f32 v178,v180,v181\n"
"v_cvt_pk_bf16_f32 v179,v182,v183\n"
"v_mfma_f32_16x16x32_bf16 v[220:223],a[124:127],v[72:75],v[220:223]\n"
"ds_write_b128 v113,v[176:179] offset:6144\n"
"v_cvt_pk_bf16_f32 v184,v184,v185\n"
"v_cvt_pk_bf16_f32 v185,v186,v187\n"
"v_cvt_pk_bf16_f32 v186,v188,v189\n"
"v_cvt_pk_bf16_f32 v187,v190,v191\n"
"v_mfma_f32_16x16x32_bf16 v[224:227],a[96:99],v[76:79],v[224:227]\n"
"ds_write_b128 v113,v[184:187] offset:7168\n"
"v_mfma_f32_16x16x32_bf16 a[140:143],a[144:147],v[76:79],a[140:143]\n"
"s_sub_u32 s82,s3,131072\n"
"ds_write_b64 v116,v[100:101] offset:512\n"
"buffer_load_dwordx4 v[128:131],v112,s[24:27],s82 offen offset:0 sc1\n"
"buffer_load_dwordx4 v[132:135],v112,s[24:27],s82 offen offset:1024 sc1\n"
"v_mfma_f32_16x16x32_bf16 v[228:231],a[100:103],v[76:79],v[228:231]\n"
"buffer_load_dwordx4 v[136:139],v112,s[24:27],s82 offen offset:2048 sc1\n"
"buffer_load_dwordx4 v[140:143],v112,s[24:27],s82 offen offset:3072 sc1\n"
"s_sub_u32 s82,s3,147456\n"
"v_mfma_f32_16x16x32_bf16 v[232:235],a[104:107],v[76:79],v[232:235]\n"
"buffer_load_dwordx4 v[144:147],v112,s[24:27],s82 offen offset:0 sc1\n"
"buffer_load_dwordx4 v[148:151],v112,s[24:27],s82 offen offset:1024 sc1\n"
"buffer_load_dwordx4 v[152:155],v112,s[24:27],s82 offen offset:2048 sc1\n"
"v_mfma_f32_16x16x32_bf16 v[236:239],a[108:111],v[76:79],v[236:239]\n"
"buffer_load_dwordx4 v[156:159],v112,s[24:27],s82 offen offset:3072 sc1\n"
"v_mfma_f32_16x16x32_bf16 v[240:243],a[112:115],v[76:79],v[240:243]\n"
"v_mfma_f32_16x16x32_bf16 v[244:247],a[116:119],v[76:79],v[244:247]\n"
"v_mfma_f32_16x16x32_bf16 v[248:251],a[120:123],v[76:79],v[248:251]\n"
"v_mfma_f32_16x16x32_bf16 v[252:255],a[124:127],v[76:79],v[252:255]\n"
".Lfk_endB:\n"
"v_lshrrev_b32 v70,4,v112\n"
"v_and_b32 v71,15,v70\n"
"v_lshrrev_b32 v70,4,v70\n"
"v_lshlrev_b32 v70,2,v70\n"
"v_sub_u32 v70,v71,v70\n"
"v_mov_b32 v71,0x3f80\n"
"v_cmp_eq_u32 vcc,0,v70\n"
"v_mov_b32 v68,0\n"
"v_mov_b32 v64,0\n"
"v_cndmask_b32 v66,0,v71,vcc\n"
"v_cmp_eq_u32 vcc,1,v70\n"
"v_mov_b32 v65,0\n"
"v_mov_b32 v72,0x3f800000\n"
"v_cndmask_b32 v66,v66,v72,vcc\n"
"v_cmp_eq_u32 vcc,2,v70\n"
"v_mov_b32 v69,0\n"
"s_nop 0\n"
"v_cndmask_b32 v67,0,v71,vcc\n"
"v_cmp_eq_u32 vcc,3,v70\n"
"s_nop 1\n"
"v_cndmask_b32 v67,v67,v72,vcc\n"
"s_sub_u32 s82,s3,163840\n"
"buffer_load_dwordx4 v[0:3],v112,s[24:27],s82 offen offset:0 sc1\n"
"buffer_load_dwordx4 v[4:7],v112,s[24:27],s82 offen offset:1024 sc1\n"
"buffer_load_dwordx4 v[8:11],v112,s[24:27],s82 offen offset:2048 sc1\n"
"buffer_load_dwordx4 v[12:15],v112,s[24:27],s82 offen offset:3072 sc1\n"
"s_sub_u32 s82,s3,180224\n"
"buffer_load_dwordx4 v[160:163],v112,s[24:27],s82 offen offset:0 sc1\n"
"buffer_load_dwordx4 v[164:167],v112,s[24:27],s82 offen offset:1024 sc1\n"
"buffer_load_dwordx4 v[168:171],v112,s[24:27],s82 offen offset:2048 sc1\n"
"buffer_load_dwordx4 v[172:175],v112,s[24:27],s82 offen offset:3072 sc1\n"
"s_sub_u32 s82,s3,196608\n"
"buffer_load_dwordx4 v[176:179],v112,s[24:27],s82 offen offset:0 sc1\n"
"buffer_load_dwordx4 v[180:183],v112,s[24:27],s82 offen offset:1024 sc1\n"
"buffer_load_dwordx4 v[184:187],v112,s[24:27],s82 offen offset:2048 sc1\n"
"buffer_load_dwordx4 v[188:191],v112,s[24:27],s82 offen offset:3072 sc1\n"
"s_waitcnt lgkmcnt(0)\n"
"s_barrier\n"
"v_xor_b32 v113,32768,v113\n"
"v_xor_b32 v116,2048,v116\n"
"ds_read_b64 v[98:99],v116 offset:0\n"
"ds_read_b128 a[64:67],v113 offset:0\n"
"ds_read_b128 a[68:71],v113 offset:1024\n"
"ds_read_b128 a[72:75],v113 offset:2048\n"
"ds_read_b128 a[76:79],v113 offset:3072\n"
"ds_read_b64 v[102:103],v116 offset:512\n"
"ds_read_b128 a[80:83],v113 offset:4096\n"
"ds_read_b128 a[84:87],v113 offset:5120\n"
"ds_read_b128 a[88:91],v113 offset:6144\n"
"ds_read_b128 a[92:95],v113 offset:7168\n"
"s_waitcnt lgkmcnt(5)\n"
"v_cmp_neq_f32 vcc,v98,v104\n"
"s_nop 0\n"
"s_cbranch_vccnz .Lfk_ms_b0\n"
".Lfk_mr_b0:\n"
"v_mfma_f32_16x16x32_bf16 v[192:195],v[66:69],a[64:67],v[192:195]\n"
"v_mfma_f32_16x16x32_bf16 v[196:199],v[64:67],a[64:67],v[196:199]\n"
"v_mfma_f32_16x16x32_bf16 v[200:203],v[66:69],a[68:71],v[200:203]\n"
"v_mfma_f32_16x16x32_bf16 v[204:207],v[64:67],a[68:71],v[204:207]\n"
"v_mfma_f32_16x16x32_bf16 v[208:211],v[66:69],a[72:75],v[208:211]\n"
"v_mfma_f32_16x16x32_bf16 v[212:215],v[64:67],a[72:75],v[212:215]\n"
"v_mfma_f32_16x16x32_bf16 v[216:219],v[66:69],a[76:79],v[216:219]\n"
"v_mfma_f32_16x16x32_bf16 v[220:223],v[64:67],a[76:79],v[220:223]\n"
"s_waitcnt lgkmcnt(0)\n"
"v_cmp_neq_f32 vcc,v102,v108\n"
"s_nop 0\n"
"s_cbranch_vccnz .Lfk_ms_b1\n"
".Lfk_mr_b1:\n"
"v_mfma_f32_16x16x32_bf16 v[224:227],v[66:69],a[80:83],v[224:227]\n"
"v_mfma_f32_16x16x32_bf16 v[228:231],v[64:67],a[80:83],v[228:231]\n"
"v_accvgpr_read_b32 v122,a136\n"
"v_add_f32 v122,v122,v99\n"
"v_mfma_f32_16x16x32_bf16 v[232:235],v[66:69],a[84:87],v[232:235]\n"
"v_rcp_f32 v122,v122\n"
"v_mfma_f32_16x16x32_bf16 v[236:239],v[64:67],a[84:87],v[236:239]\n"
"v_pk_mul_f32 v[192:193],v[192:193],v[122:123] op_sel_hi:[1,0]\n"
"v_pk_mul_f32 v[194:195],v[194:195],v[122:123] op_sel_hi:[1,0]\n"
"v_pk_mul_f32 v[196:197],v[196:197],v[122:123] op_sel_hi:[1,0]\n"
"v_pk_mul_f32 v[198:199],v[198:199],v[122:123] op_sel_hi:[1,0]\n"
"v_mfma_f32_16x16x32_bf16 v[240:243],v[66:69],a[88:91],v[240:243]\n"
"v_cvt_pk_bf16_f32 v192,v192,v193\n"
"v_cvt_pk_bf16_f32 v193,v194,v195\n"
"v_mfma_f32_16x16x32_bf16 v[244:247],v[64:67],a[88:91],v[244:247]\n"
"v_cvt_pk_bf16_f32 v194,v196,v197\n"
"v_cvt_pk_bf16_f32 v195,v198,v199\n"
"v_mfma_f32_16x16x32_bf16 v[248:251],v[66:69],a[92:95],v[248:251]\n"
"buffer_store_dwordx4 v[192:195],v112,s[24:27],s70 offen offset:0 sc1\n"
"v_pk_mul_f32 v[200:201],v[200:201],v[122:123] op_sel_hi:[1,0]\n"
"v_pk_mul_f32 v[202:203],v[202:203],v[122:123] op_sel_hi:[1,0]\n"
"v_pk_mul_f32 v[204:205],v[204:205],v[122:123] op_sel_hi:[1,0]\n"
"v_pk_mul_f32 v[206:207],v[206:207],v[122:123] op_sel_hi:[1,0]\n"
"v_mfma_f32_16x16x32_bf16 v[252:255],v[64:67],a[92:95],v[252:255]\n"
"v_cvt_pk_bf16_f32 v200,v200,v201\n"
"v_cvt_pk_bf16_f32 v201,v202,v203\n"
"v_cvt_pk_bf16_f32 v202,v204,v205\n"
"v_cvt_pk_bf16_f32 v203,v206,v207\n"
"v_pk_mul_f32 v[208:209],v[208:209],v[122:123] op_sel_hi:[1,0]\n"
"v_pk_mul_f32 v[210:211],v[210:211],v[122:123] op_sel_hi:[1,0]\n"
"v_pk_mul_f32 v[212:213],v[212:213],v[122:123] op_sel_hi:[1,0]\n"
"v_pk_mul_f32 v[214:215],v[214:215],v[122:123] op_sel_hi:[1,0]\n"
"v_cvt_pk_bf16_f32 v208,v208,v209\n"
"v_cvt_pk_bf16_f32 v209,v210,v211\n"
"v_cvt_pk_bf16_f32 v210,v212,v213\n"
"v_cvt_pk_bf16_f32 v211,v214,v215\n"
"buffer_store_dwordx4 v[200:203],v112,s[24:27],s70 offen offset:1024 sc1\n"
"buffer_store_dwordx4 v[208:211],v112,s[24:27],s70 offen offset:2048 sc1\n"
"v_pk_mul_f32 v[216:217],v[216:217],v[122:123] op_sel_hi:[1,0]\n"
"v_pk_mul_f32 v[218:219],v[218:219],v[122:123] op_sel_hi:[1,0]\n"
"v_pk_mul_f32 v[220:221],v[220:221],v[122:123] op_sel_hi:[1,0]\n"
"v_pk_mul_f32 v[222:223],v[222:223],v[122:123] op_sel_hi:[1,0]\n"
"v_cvt_pk_bf16_f32 v216,v216,v217\n"
"v_cvt_pk_bf16_f32 v217,v218,v219\n"
"v_cvt_pk_bf16_f32 v218,v220,v221\n"
"v_cvt_pk_bf16_f32 v219,v222,v223\n"
"buffer_store_dwordx4 v[216:219],v112,s[24:27],s70 offen offset:3072 sc1\n"
"s_add_u32 s70,s70,262144\n"
"s_branch .Lfk_p1\n"
".Lfk_tailB7:\n"
"s_waitcnt vmcnt(11) lgkmcnt(0)\n"
"s_barrier\n"
"v_mfma_f32_16x16x32_bf16 v[0:3],a[64:67],a[0:3],v[96:99]\n"
"ds_read_b128 a[96:99],v114 offset:24576\n"
"ds_read_b128 a[100:103],v114 offset:25600\n"
"ds_read_b128 a[104:107],v114 offset:26624\n"
"v_mfma_f32_16x16x32_bf16 v[0:3],a[68:71],a[4:7],v[0:3]\n"
"ds_read_b128 a[108:111],v114 offset:27648\n"
"ds_read_b128 a[112:115],v114 offset:28672\n"
"ds_read_b128 a[116:119],v114 offset:29696\n"
"v_mfma_f32_16x16x32_bf16 v[0:3],a[72:75],a[8:11],v[0:3]\n"
"ds_read_b128 a[120:123],v114 offset:30720\n"
"ds_read_b128 a[124:127],v114 offset:31744\n"
"v_add_f32 v16,0x80000000,v126\n"
"v_mfma_f32_16x16x32_bf16 v[0:3],a[76:79],a[12:15],v[0:3]\n"
"v_min_f32 v16,v16,v104\n"
"v_add_f32 v17,0xf149f2ca,v126\n"
"v_min_f32 v17,v17,v105\n"
"v_mfma_f32_16x16x32_bf16 v[4:7],a[92:95],a[12:15],v[96:99]\n"
"v_add_f32 v18,0xf1c9f2ca,v126\n"
"v_min_f32 v18,v18,v106\n"
"v_add_f32 v19,0xf2177617,v126\n"
"v_mfma_f32_16x16x32_bf16 v[4:7],a[88:91],a[8:11],v[4:7]\n"
"v_min_f32 v19,v19,v107\n"
"v_add_f32 v20,0xf349f2ca,v126\n"
"v_mfma_f32_16x16x32_bf16 v[4:7],a[84:87],a[4:7],v[4:7]\n"
"v_add_f32 v21,0xf35691f6,v126\n"
"v_min_f32 v21,v21,v105\n"
"v_add_f32 v22,0xf3633123,v126\n"
"v_mfma_f32_16x16x32_bf16 v[4:7],a[80:83],a[0:3],v[4:7]\n"
"s_waitcnt lgkmcnt(0)\n"
"v_min_f32 v22,v22,v106\n"
"v_add_f32 v23,0xf36fd050,v126\n"
"v_mfma_f32_16x16x32_bf16 v[192:195],a[96:99],v[72:75],v[192:195]\n"
"v_exp_f32 v56,v56\n"
"v_mfma_f32_16x16x32_bf16 a[136:139],a[144:147],v[72:75],a[136:139]\n"
"v_exp_f32 v57,v57\n"
"v_mfma_f32_16x16x32_bf16 v[196:199],a[100:103],v[72:75],v[196:199]\n"
"v_min_f32 v20,v20,v104\n"
"v_cvt_pk_bf16_f32 v92,v56,v57\n"
"v_mfma_f32_16x16x32_bf16 v[200:203],a[104:107],v[72:75],v[200:203]\n"
"v_max3_f32 v118,v0,v1,v2\n"
"v_max3_f32 v118,v118,v3,v4\n"
"v_mfma_f32_16x16x32_bf16 v[204:207],a[108:111],v[72:75],v[204:207]\n"
"v_max3_f32 v118,v118,v5,v6\n"
"v_max_f32 v118,v118,v7\n"
"v_mfma_f32_16x16x32_bf16 v[208:211],a[112:115],v[72:75],v[208:211]\n"
"v_min_f32 v23,v23,v107\n"
"v_mov_b32 v120,v118\n"
"v_mfma_f32_16x16x32_bf16 v[212:215],a[116:119],v[72:75],v[212:215]\n"
"v_exp_f32 v58,v58\n"
"v_mfma_f32_16x16x32_bf16 v[216:219],a[120:123],v[72:75],v[216:219]\n"
"v_mfma_f32_16x16x32_bf16 v[220:223],a[124:127],v[72:75],v[220:223]\n"
"v_exp_f32 v59,v59\n"
"v_mfma_f32_16x16x32_bf16 v[224:227],a[96:99],v[76:79],v[224:227]\n"
"v_cvt_pk_bf16_f32 v93,v58,v59\n"
"v_mfma_f32_16x16x32_bf16 a[140:143],a[144:147],v[76:79],a[140:143]\n"
"v_accvgpr_read_b32 v122,a136\n"
"v_mfma_f32_16x16x32_bf16 v[228:231],a[100:103],v[76:79],v[228:231]\n"
"v_rcp_f32 v122,v122\n"
"v_mfma_f32_16x16x32_bf16 v[232:235],a[104:107],v[76:79],v[232:235]\n"
"v_pk_mul_f32 v[192:193],v[192:193],v[122:123] op_sel_hi:[1,0]\n"
"v_pk_mul_f32 v[194:195],v[194:195],v[122:123] op_sel_hi:[1,0]\n"
"v_pk_mul_f32 v[196:197],v[196:197],v[122:123] op_sel_hi:[1,0]\n"
"v_pk_mul_f32 v[198:199],v[198:199],v[122:123] op_sel_hi:[1,0]\n"
"v_mfma_f32_16x16x32_bf16 v[236:239],a[108:111],v[76:79],v[236:239]\n"
"v_cvt_pk_bf16_f32 v192,v192,v193\n"
"v_cvt_pk_bf16_f32 v193,v194,v195\n"
"v_mfma_f32_16x16x32_bf16 v[240:243],a[112:115],v[76:79],v[240:243]\n"
"v_cvt_pk_bf16_f32 v194,v196,v197\n"
"v_cvt_pk_bf16_f32 v195,v198,v199\n"
"v_mfma_f32_16x16x32_bf16 v[244:247],a[116:119],v[76:79],v[244:247]\n"
"buffer_store_dwordx4 v[192:195],v112,s[24:27],s70 offen offset:0 sc1\n"
"v_pk_mul_f32 v[200:201],v[200:201],v[122:123] op_sel_hi:[1,0]\n"
"v_pk_mul_f32 v[202:203],v[202:203],v[122:123] op_sel_hi:[1,0]\n"
"v_mfma_f32_16x16x32_bf16 v[248:251],a[120:123],v[76:79],v[248:251]\n"
"v_exp_f32 v60,v60\n"
"v_cvt_pk_bf16_f32 v200,v200,v201\n"
"v_cvt_pk_bf16_f32 v201,v202,v203\n"
"v_pk_mul_f32 v[204:205],v[204:205],v[122:123] op_sel_hi:[1,0]\n"
"v_pk_mul_f32 v[206:207],v[206:207],v[122:123] op_sel_hi:[1,0]\n"
"v_mfma_f32_16x16x32_bf16 v[252:255],a[124:127],v[76:79],v[252:255]\n"
"v_cvt_pk_bf16_f32 v202,v204,v205\n"
"v_cvt_pk_bf16_f32 v203,v206,v207\n"
"v_mfma_f32_16x16x32_bf16 v[12:15],a[80:83],a[16:19],v[100:103]\n"
"buffer_store_dwordx4 v[200:203],v112,s[24:27],s70 offen offset:1024 sc1\n"
"v_pk_mul_f32 v[208:209],v[208:209],v[122:123] op_sel_hi:[1,0]\n"
"v_pk_mul_f32 v[210:211],v[210:211],v[122:123] op_sel_hi:[1,0]\n"
"v_pk_mul_f32 v[212:213],v[212:213],v[122:123] op_sel_hi:[1,0]\n"
"v_pk_mul_f32 v[214:215],v[214:215],v[122:123] op_sel_hi:[1,0]\n"
"v_mfma_f32_16x16x32_bf16 v[12:15],a[84:87],a[20:23],v[12:15]\n"
"v_exp_f32 v61,v61\n"
"v_mfma_f32_16x16x32_bf16 v[12:15],a[88:91],a[24:27],v[12:15]\n"
"v_cvt_pk_bf16_f32 v94,v60,v61\n"
"v_cvt_pk_bf16_f32 v208,v208,v209\n"
"v_mfma_f32_16x16x32_bf16 v[12:15],a[92:95],a[28:31],v[12:15]\n"
"v_cvt_pk_bf16_f32 v209,v210,v211\n"
"v_cvt_pk_bf16_f32 v210,v212,v213\n"
"v_cvt_pk_bf16_f32 v211,v214,v215\n"
"v_mfma_f32_16x16x32_bf16 v[8:11],a[76:79],a[28:31],v[100:103]\n"
"buffer_store_dwordx4 v[208:211],v112,s[24:27],s70 offen offset:2048 sc1\n"
"v_pk_mul_f32 v[216:217],v[216:217],v[122:123] op_sel_hi:[1,0]\n"
"v_pk_mul_f32 v[218:219],v[218:219],v[122:123] op_sel_hi:[1,0]\n"
"v_mfma_f32_16x16x32_bf16 v[8:11],a[72:75],a[24:27],v[8:11]\n"
"v_cvt_pk_bf16_f32 v216,v216,v217\n"
"v_cvt_pk_bf16_f32 v217,v218,v219\n"
"v_pk_mul_f32 v[220:221],v[220:221],v[122:123] op_sel_hi:[1,0]\n"
"v_pk_mul_f32 v[222:223],v[222:223],v[122:123] op_sel_hi:[1,0]\n"
"v_mfma_f32_16x16x32_bf16 v[8:11],a[68:71],a[20:23],v[8:11]\n"
"v_cvt_pk_bf16_f32 v218,v220,v221\n"
"v_cvt_pk_bf16_f32 v219,v222,v223\n"
"v_mfma_f32_16x16x32_bf16 v[8:11],a[64:67],a[16:19],v[8:11]\n"
"buffer_store_dwordx4 v[216:219],v112,s[24:27],s70 offen offset:3072 sc1\n"
"v_mfma_f32_16x16x32_bf16 v[24:27],a[64:67],a[48:51],v[108:111]\n"
"s_add_u32 s70,s70,262144\n"
"v_exp_f32 v62,v62\n"
"v_mfma_f32_16x16x32_bf16 v[24:27],a[68:71],a[52:55],v[24:27]\n"
"v_exp_f32 v63,v63\n"
"v_mfma_f32_16x16x32_bf16 v[24:27],a[72:75],a[56:59],v[24:27]\n"
"s_nop 0\n"
"v_max3_f32 v119,v8,v9,v10\n"
"v_mfma_f32_16x16x32_bf16 v[24:27],a[76:79],a[60:63],v[24:27]\n"
"v_max3_f32 v119,v119,v11,v12\n"
"v_max3_f32 v119,v119,v13,v14\n"
"v_mfma_f32_16x16x32_bf16 v[28:31],a[92:95],a[60:63],v[108:111]\n"
"v_max_f32 v119,v119,v15\n"
"v_mfma_f32_16x16x32_bf16 v[28:31],a[88:91],a[56:59],v[28:31]\n"
"v_min_f32 v120,v120,v119\n"
"v_max_f32 v118,v118,v119\n"
"v_mfma_f32_16x16x32_bf16 v[28:31],a[84:87],a[52:55],v[28:31]\n"
"v_mfma_f32_16x16x32_bf16 v[28:31],a[80:83],a[48:51],v[28:31]\n"
"v_cvt_pk_bf16_f32 v95,v62,v63\n"
"v_mfma_f32_16x16x32_bf16 v[20:23],a[80:83],a[32:35],v[20:23]\n"
"v_mfma_f32_16x16x32_bf16 v[20:23],a[84:87],a[36:39],v[20:23]\n"
"v_mfma_f32_16x16x32_bf16 v[20:23],a[88:91],a[40:43],v[20:23]\n"
"v_max3_f32 v119,v24,v25,v26\n"
"v_mfma_f32_16x16x32_bf16 v[20:23],a[92:95],a[44:47],v[20:23]\n"
"s_nop 1\n"
"v_max3_f32 v119,v119,v27,v28\n"
"v_mfma_f32_16x16x32_bf16 v[16:19],a[76:79],a[44:47],v[16:19]\n"
"v_max3_f32 v119,v119,v29,v30\n"
"v_max_f32 v119,v119,v31\n"
"v_mfma_f32_16x16x32_bf16 v[16:19],a[72:75],a[40:43],v[16:19]\n"
"v_mfma_f32_16x16x32_bf16 v[16:19],a[68:71],a[36:39],v[16:19]\n"
"v_mfma_f32_16x16x32_bf16 v[16:19],a[64:67],a[32:35],v[16:19]\n"
"ds_read_b128 a[64:67],v114 offset:57344\n"
"ds_read_b128 a[68:71],v114 offset:58368\n"
"ds_read_b128 a[72:75],v114 offset:59392\n"
"v_max_f32 v118,v118,v119\n"
"v_min_f32 v120,v120,v119\n"
"v_cmp_gt_f32 s[96:97],s16,v120\n"
"s_nop 1\n"
"v_max3_f32 v119,v16,v17,v18\n"
"v_max3_f32 v119,v119,v19,v20\n"
"v_max3_f32 v119,v119,v21,v22\n"
"v_max_f32 v119,v119,v23\n"
"v_cmp_gt_f32 s[28:29],s16,v119\n"
"v_max_f32 v118,v118,v119\n"
"v_cmp_lt_f32 vcc,s93,v118\n"
"ds_read_b128 a[76:79],v114 offset:60416\n"
"ds_read_b128 a[80:83],v114 offset:61440\n"
"ds_read_b128 a[84:87],v114 offset:62464\n"
"ds_read_b128 a[88:91],v114 offset:63488\n"
"ds_read_b128 a[92:95],v114 offset:64512\n"
"v_mov_b32 v114,v112\n"
"s_sub_u32 s82,s3,32768\n"
"buffer_load_dwordx4 a[0:3],v112,s[24:27],s82 offen offset:0 sc1\n"
"buffer_load_dwordx4 a[4:7],v112,s[24:27],s82 offen offset:1024 sc1\n"
"buffer_load_dwordx4 a[8:11],v112,s[24:27],s82 offen offset:2048 sc1\n"
"buffer_load_dwordx4 a[12:15],v112,s[24:27],s82 offen offset:3072 sc1\n"
"s_sub_u32 s82,s3,49152\n"
"buffer_load_dwordx4 a[16:19],v112,s[24:27],s82 offen offset:0 sc1\n"
"buffer_load_dwordx4 a[20:23],v112,s[24:27],s82 offen offset:1024 sc1\n"
"buffer_load_dwordx4 a[24:27],v112,s[24:27],s82 offen offset:2048 sc1\n"
"buffer_load_dwordx4 a[28:31],v112,s[24:27],s82 offen offset:3072 sc1\n"
"s_and_b32 s28,s28,0xffff\n"
"s_or_b32 s96,s96,s28\n"
"s_or_b64 s[96:97],s[96:97],vcc\n"
"s_cbranch_scc1 .Lfk_cs_c3\n"
".Lfk_cr_c3:\n"
"s_waitcnt vmcnt(13) lgkmcnt(0)\n"
"s_barrier\n"
"v_mfma_f32_16x16x32_bf16 v[224:227],a[64:67],v[92:95],v[224:227]\n"
"ds_read_b128 a[96:99],v114 offset:40960\n"
"v_exp_f32 v0,v0\n"
"v_mfma_f32_16x16x32_bf16 a[140:143],a[144:147],v[92:95],a[140:143]\n"
"ds_read_b128 a[100:103],v114 offset:41984\n"
"v_exp_f32 v1,v1\n"
"v_mfma_f32_16x16x32_bf16 v[228:231],a[68:71],v[92:95],v[228:231]\n"
"ds_read_b128 a[104:107],v114 offset:43008\n"
"v_exp_f32 v2,v2\n"
"v_mfma_f32_16x16x32_bf16 v[232:235],a[72:75],v[92:95],v[232:235]\n"
"ds_read_b128 a[108:111],v114 offset:44032\n"
"v_exp_f32 v3,v3\n"
"v_mfma_f32_16x16x32_bf16 v[236:239],a[76:79],v[92:95],v[236:239]\n"
"ds_read_b128 a[112:115],v114 offset:45056\n"
"v_exp_f32 v4,v4\n"
"v_mfma_f32_16x16x32_bf16 v[240:243],a[80:83],v[92:95],v[240:243]\n"
"ds_read_b128 a[116:119],v114 offset:46080\n"
"v_exp_f32 v5,v5\n"
"v_mfma_f32_16x16x32_bf16 v[244:247],a[84:87],v[92:95],v[244:247]\n"
"ds_read_b128 a[120:123],v114 offset:47104\n"
"v_exp_f32 v6,v6\n"
"v_mfma_f32_16x16x32_bf16 v[248:251],a[88:91],v[92:95],v[248:251]\n"
"v_exp_f32 v7,v7\n"
"v_mfma_f32_16x16x32_bf16 v[252:255],a[92:95],v[92:95],v[252:255]\n"
"v_exp_f32 v8,v8\n"
"v_accvgpr_read_b32 v119,a150\n"
"v_accvgpr_read_b32 v120,a151\n"
"v_xor_b32 v119,s80,v119\n"
"v_xor_b32 v120,s81,v120\n"
"v_or_b32 v119,v119,v120\n"
"v_cmp_ne_u32 s[14:15],0,v119\n"
"s_add_u32 s18,s8,1032192\n"
"s_cmp_lg_u32 s14,0\n"
"s_cselect_b32 s18,2146435072,s18\n"
"s_add_u32 m0,s30,0\n"
"ds_read_b128 a[124:127],v114 offset:48128\n"
"buffer_load_dwordx4 v112,s[24:27],s18 offen offset:0 sc1 lds\n"
"buffer_load_dwordx4 v112,s[24:27],s18 offen offset:1024 sc1 lds\n"
"buffer_load_dwordx4 v112,s[24:27],s18 offen offset:2048 sc1 lds\n"
"buffer_load_dwordx4 v112,s[24:27],s18 offen offset:3072 sc1 lds\n"
"s_add_u32 m0,m0,4096\n"
"s_nop 0\n"
"buffer_load_dwordx4 v117,s[24:27],s18 offen offset:0 sc1 lds\n"
"buffer_load_dwordx4 v117,s[24:27],s18 offen offset:1024 sc1 lds\n"
"buffer_load_dwordx4 v117,s[24:27],s18 offen offset:2048 sc1 lds\n"
"buffer_load_dwordx4 v117,s[24:27],s18 offen offset:3072 sc1 lds\n"
"s_sub_u32 s82,s3,65536\n"
"buffer_load_dwordx4 v[32:35],v112,s[24:27],s82 offen offset:0 sc1\n"
"buffer_load_dwordx4 v[36:39],v112,s[24:27],s82 offen offset:1024 sc1\n"
"buffer_load_dwordx4 v[40:43],v112,s[24:27],s82 offen offset:2048 sc1\n"
"buffer_load_dwordx4 v[44:47],v112,s[24:27],s82 offen offset:3072 sc1\n"
"v_exp_f32 v9,v9\n"
"v_cvt_pk_bf16_f32 v64,v0,v1\n"
"v_exp_f32 v10,v10\n"
"v_cvt_pk_bf16_f32 v65,v2,v3\n"
"v_exp_f32 v11,v11\n"
"v_cvt_pk_bf16_f32 v66,v4,v5\n"
"v_exp_f32 v12,v12\n"
"v_cvt_pk_bf16_f32 v67,v6,v7\n"
"v_exp_f32 v13,v13\n"
"v_cvt_pk_bf16_f32 v68,v8,v9\n"
"v_exp_f32 v14,v14\n"
"v_cvt_pk_bf16_f32 v69,v10,v11\n"
"v_exp_f32 v15,v15\n"
"v_cvt_pk_bf16_f32 v70,v12,v13\n"
"v_exp_f32 v16,v16\n"
"v_cvt_pk_bf16_f32 v71,v14,v15\n"
"v_exp_f32 v17,v17\n"
"v_exp_f32 v18,v18\n"
"v_cvt_pk_bf16_f32 v72,v16,v17\n"
"v_exp_f32 v19,v19\n"
"v_exp_f32 v20,v20\n"
"v_cvt_pk_bf16_f32 v73,v18,v19\n"
"v_exp_f32 v21,v21\n"
"v_exp_f32 v22,v22\n"
"v_cvt_pk_bf16_f32 v74,v20,v21\n"
"v_exp_f32 v23,v23\n"
"v_exp_f32 v24,v24\n"
"v_cvt_pk_bf16_f32 v75,v22,v23\n"
"v_exp_f32 v25,v25\n"
"v_exp_f32 v26,v26\n"
"v_cvt_pk_bf16_f32 v76,v24,v25\n"
"v_exp_f32 v27,v27\n"
"v_exp_f32 v28,v28\n"
"v_cvt_pk_bf16_f32 v77,v26,v27\n"
"v_exp_f32 v29,v29\n"
"v_exp_f32 v30,v30\n"
"v_cvt_pk_bf16_f32 v78,v28,v29\n"
"v_exp_f32 v31,v31\n"
"s_nop 0\n"
"v_cvt_pk_bf16_f32 v79,v30,v31\n"
"v_accvgpr_read_b32 v122,a140\n"
"v_rcp_f32 v122,v122\n"
"s_nop 0\n"
"v_pk_mul_f32 v[224:225],v[224:225],v[122:123] op_sel_hi:[1,0]\n"
"v_pk_mul_f32 v[226:227],v[226:227],v[122:123] op_sel_hi:[1,0]\n"
"v_pk_mul_f32 v[228:229],v[228:229],v[122:123] op_sel_hi:[1,0]\n"
"v_pk_mul_f32 v[230:231],v[230:231],v[122:123] op_sel_hi:[1,0]\n"
"v_cvt_pk_bf16_f32 v224,v224,v225\n"
"v_cvt_pk_bf16_f32 v225,v226,v227\n"
"v_cvt_pk_bf16_f32 v226,v228,v229\n"
"v_cvt_pk_bf16_f32 v227,v230,v231\n"
"buffer_store_dwordx4 v[224:227],v112,s[24:27],s70 offen offset:0 sc1\n"
"v_pk_mul_f32 v[232:233],v[232:233],v[122:123] op_sel_hi:[1,0]\n"
"v_pk_mul_f32 v[234:235],v[234:235],v[122:123] op_sel_hi:[1,0]\n"
"v_pk_mul_f32 v[236:237],v[236:237],v[122:123] op_sel_hi:[1,0]\n"
"v_pk_mul_f32 v[238:239],v[238:239],v[122:123] op_sel_hi:[1,0]\n"
"v_cvt_pk_bf16_f32 v232,v232,v233\n"
"v_cvt_pk_bf16_f32 v233,v234,v235\n"
"v_cvt_pk_bf16_f32 v234,v236,v237\n"
"v_cvt_pk_bf16_f32 v235,v238,v239\n"
"buffer_store_dwordx4 v[232:235],v112,s[24:27],s70 offen offset:1024 sc1\n"
"v_pk_mul_f32 v[240:241],v[240:241],v[122:123] op_sel_hi:[1,0]\n"
"v_pk_mul_f32 v[242:243],v[242:243],v[122:123] op_sel_hi:[1,0]\n"
"v_pk_mul_f32 v[244:245],v[244:245],v[122:123] op_sel_hi:[1,0]\n"
"v_pk_mul_f32 v[246:247],v[246:247],v[122:123] op_sel_hi:[1,0]\n"
"v_cvt_pk_bf16_f32 v240,v240,v241\n"
"v_cvt_pk_bf16_f32 v241,v242,v243\n"
"v_cvt_pk_bf16_f32 v242,v244,v245\n"
"v_cvt_pk_bf16_f32 v243,v246,v247\n"
"buffer_store_dwordx4 v[240:243],v112,s[24:27],s70 offen offset:2048 sc1\n"
"v_pk_mul_f32 v[248:249],v[248:249],v[122:123] op_sel_hi:[1,0]\n"
"v_pk_mul_f32 v[250:251],v[250:251],v[122:123] op_sel_hi:[1,0]\n"
"v_pk_mul_f32 v[252:253],v[252:253],v[122:123] op_sel_hi:[1,0]\n"
"v_pk_mul_f32 v[254:255],v[254:255],v[122:123] op_sel_hi:[1,0]\n"
"v_cvt_pk_bf16_f32 v248,v248,v249\n"
"v_cvt_pk_bf16_f32 v249,v250,v251\n"
"v_cvt_pk_bf16_f32 v250,v252,v253\n"
"v_cvt_pk_bf16_f32 v251,v254,v255\n"
"buffer_store_dwordx4 v[248:251],v112,s[24:27],s70 offen offset:3072 sc1\n"
"s_add_u32 s70,s70,262144\n"
"s_mov_b32 s70,s68\n"
"s_load_dwordx2 s[36:37],s[0:1],0\n"
"s_load_dwordx2 s[44:45],s[0:1],80\n"
"s_load_dwordx2 s[48:49],s[0:1],88\n"
"s_lshl_b32 s82,s78,2\n"
"s_add_u32 s82,s82,65536\n"
"v_add_u32 v113,s82,v112\n"
"s_lshr_b32 s82,s78,2\n"
"s_add_u32 s82,s82,122880\n"
"v_lshrrev_b32 v115,1,v112\n"
"v_add_u32 v116,s82,v115\n"
"s_waitcnt lgkmcnt(0)\n"
"s_barrier\n"
"v_mfma_f32_16x16x32_bf16 v[128:131],a[96:99],v[64:67],0\n"
"s_add_u32 s83,s18,2097152\n"
"s_add_u32 m0,s30,32768\n"
"v_mfma_f32_16x16x32_bf16 a[128:131],a[144:147],v[64:67],0\n"
"buffer_load_dwordx4 v112,s[24:27],s83 offen offset:0 sc1 lds\n"
"v_mfma_f32_16x16x32_bf16 v[132:135],a[100:103],v[64:67],0\n"
"buffer_load_dwordx4 v112,s[24:27],s83 offen offset:1024 sc1 lds\n"
"v_mfma_f32_16x16x32_bf16 v[136:139],a[104:107],v[64:67],0\n"
"buffer_load_dwordx4 v112,s[24:27],s83 offen offset:2048 sc1 lds\n"
"v_mfma_f32_16x16x32_bf16 v[140:143],a[108:111],v[64:67],0\n"
"v_mfma_f32_16x16x32_bf16 v[144:147],a[112:115],v[64:67],0\n"
"buffer_load_dwordx4 v112,s[24:27],s83 offen offset:3072 sc1 lds\n"
"v_mfma_f32_16x16x32_bf16 v[148:151],a[116:119],v[64:67],0\n"
"s_add_u32 m0,m0,4096\n"
"v_mfma_f32_16x16x32_bf16 v[152:155],a[120:123],v[64:67],0\n"
"v_mfma_f32_16x16x32_bf16 v[156:159],a[124:127],v[64:67],0\n"
"buffer_load_dwordx4 v117,s[24:27],s83 offen offset:0 sc1 lds\n"
"v_mfma_f32_16x16x32_bf16 v[160:163],a[96:99],v[68:71],0\n"
"v_accvgpr_read_b32 v97,a128\n"
"v_cvt_pk_bf16_f32 v136,v136,v137\n"
"v_mfma_f32_16x16x32_bf16 a[132:135],a[144:147],v[68:71],0\n"
"buffer_load_dwordx4 v117,s[24:27],s83 offen offset:1024 sc1 lds\n"
"v_mfma_f32_16x16x32_bf16 v[164:167],a[100:103],v[68:71],0\n"
"v_cvt_pk_bf16_f32 v128,v128,v129\n"
"v_cvt_pk_bf16_f32 v129,v130,v131\n"
"v_mfma_f32_16x16x32_bf16 v[168:171],a[104:107],v[68:71],0\n"
"buffer_load_dwordx4 v117,s[24:27],s83 offen offset:2048 sc1 lds\n"
"v_cvt_pk_bf16_f32 v130,v132,v133\n"
"v_cvt_pk_bf16_f32 v131,v134,v135\n"
"v_mfma_f32_16x16x32_bf16 v[172:175],a[108:111],v[68:71],0\n"
"ds_write_b128 v113,v[128:131] offset:0\n"
"v_cvt_pk_bf16_f32 v137,v138,v139\n"
"v_mfma_f32_16x16x32_bf16 v[176:179],a[112:115],v[68:71],0\n"
"buffer_load_dwordx4 v117,s[24:27],s83 offen offset:3072 sc1 lds\n"
"v_cvt_pk_bf16_f32 v138,v140,v141\n"
"v_cvt_pk_bf16_f32 v139,v142,v143\n"
"v_mfma_f32_16x16x32_bf16 v[180:183],a[116:119],v[68:71],0\n"
"s_sub_u32 s82,s3,81920\n"
"ds_write_b128 v113,v[136:139] offset:1024\n"
"v_cvt_pk_bf16_f32 v144,v144,v145\n"
"v_mfma_f32_16x16x32_bf16 v[184:187],a[120:123],v[68:71],0\n"
"buffer_load_dwordx4 a[32:35],v112,s[24:27],s82 offen offset:0 sc1\n"
"v_cvt_pk_bf16_f32 v145,v146,v147\n"
"v_cvt_pk_bf16_f32 v146,v148,v149\n"
"v_cvt_pk_bf16_f32 v147,v150,v151\n"
"v_mfma_f32_16x16x32_bf16 v[188:191],a[124:127],v[68:71],0\n"
"ds_write_b128 v113,v[144:147] offset:2048\n"
"v_cvt_pk_bf16_f32 v152,v152,v153\n"
"v_mfma_f32_16x16x32_bf16 v[192:195],a[96:99],v[72:75],0\n"
"buffer_load_dwordx4 a[36:39],v112,s[24:27],s82 offen offset:1024 sc1\n"
"v_cvt_pk_bf16_f32 v153,v154,v155\n"
"v_cvt_pk_bf16_f32 v154,v156,v157\n"
"v_cvt_pk_bf16_f32 v155,v158,v159\n"
"v_mfma_f32_16x16x32_bf16 a[136:139],a[144:147],v[72:75],0\n"
"ds_write_b128 v113,v[152:155] offset:3072\n"
"ds_write_b64 v116,v[96:97] offset:0\n"
"v_mfma_f32_16x16x32_bf16 v[196:199],a[100:103],v[72:75],0\n"
"buffer_load_dwordx4 a[40:43],v112,s[24:27],s82 offen offset:2048 sc1\n"
"v_accvgpr_read_b32 v101,a132\n"
"v_cvt_pk_bf16_f32 v160,v160,v161\n"
"v_mfma_f32_16x16x32_bf16 v[200:203],a[104:107],v[72:75],0\n"
"v_cvt_pk_bf16_f32 v161,v162,v163\n"
"v_cvt_pk_bf16_f32 v162,v164,v165\n"
"v_cvt_pk_bf16_f32 v163,v166,v167\n"
"v_mfma_f32_16x16x32_bf16 v[204:207],a[108:111],v[72:75],0\n"
"ds_write_b128 v113,v[160:163] offset:4096\n"
"v_cvt_pk_bf16_f32 v168,v168,v169\n"
"v_cvt_pk_bf16_f32 v169,v170,v171\n"
"v_mfma_f32_16x16x32_bf16 v[208:211],a[112:115],v[72:75],0\n"
"buffer_load_dwordx4 a[44:47],v112,s[24:27],s82 offen offset:3072 sc1\n"
"v_cvt_pk_bf16_f32 v170,v172,v173\n"
"v_cvt_pk_bf16_f32 v171,v174,v175\n"
"v_mfma_f32_16x16x32_bf16 v[212:215],a[116:119],v[72:75],0\n"
"ds_write_b128 v113,v[168:171] offset:5120\n"
"v_cvt_pk_bf16_f32 v176,v176,v177\n"
"v_cvt_pk_bf16_f32 v177,v178,v179\n"
"v_mfma_f32_16x16x32_bf16 v[216:219],a[120:123],v[72:75],0\n"
"v_cvt_pk_bf16_f32 v178,v180,v181\n"
"v_cvt_pk_bf16_f32 v179,v182,v183\n"
"v_mfma_f32_16x16x32_bf16 v[220:223],a[124:127],v[72:75],0\n"
"ds_write_b128 v113,v[176:179] offset:6144\n"
"v_cvt_pk_bf16_f32 v184,v184,v185\n"
"v_cvt_pk_bf16_f32 v185,v186,v187\n"
"v_cvt_pk_bf16_f32 v186,v188,v189\n"
"v_cvt_pk_bf16_f32 v187,v190,v191\n"
"v_mfma_f32_16x16x32_bf16 v[224:227],a[96:99],v[76:79],0\n"
"ds_write_b128 v113,v[184:187] offset:7168\n"
"v_mfma_f32_16x16x32_bf16 a[140:143],a[144:147],v[76:79],0\n"
"s_sub_u32 s82,s3,131072\n"
"ds_write_b64 v116,v[100:101] offset:512\n"
"buffer_load_dwordx4 v[128:131],v112,s[24:27],s82 offen offset:0 sc1\n"
"buffer_load_dwordx4 v[132:135],v112,s[24:27],s82 offen offset:1024 sc1\n"
"v_mfma_f32_16x16x32_bf16 v[228:231],a[100:103],v[76:79],0\n"
"buffer_load_dwordx4 v[136:139],v112,s[24:27],s82 offen offset:2048 sc1\n"
"buffer_load_dwordx4 v[140:143],v112,s[24:27],s82 offen offset:3072 sc1\n"
"s_sub_u32 s82,s3,147456\n"
"v_mfma_f32_16x16x32_bf16 v[232:235],a[104:107],v[76:79],0\n"
"buffer_load_dwordx4 v[144:147],v112,s[24:27],s82 offen offset:0 sc1\n"
"buffer_load_dwordx4 v[148:151],v112,s[24:27],s82 offen offset:1024 sc1\n"
"buffer_load_dwordx4 v[152:155],v112,s[24:27],s82 offen offset:2048 sc1\n"
"v_mfma_f32_16x16x32_bf16 v[236:239],a[108:111],v[76:79],0\n"
"buffer_load_dwordx4 v[156:159],v112,s[24:27],s82 offen offset:3072 sc1\n"
"v_mfma_f32_16x16x32_bf16 v[240:243],a[112:115],v[76:79],0\n"
"v_mfma_f32_16x16x32_bf16 v[244:247],a[116:119],v[76:79],0\n"
"v_mfma_f32_16x16x32_bf16 v[248:251],a[120:123],v[76:79],0\n"
"v_mfma_f32_16x16x32_bf16 v[252:255],a[124:127],v[76:79],0\n"
"s_waitcnt vmcnt(20)\n"
"buffer_store_dwordx2 a[154:155],off,s[24:27],s69 sc1\n"
"s_branch .Lfk_endB\n"
".Lfk_head0:\n"
"s_mov_b32 s64,s61\n"
"s_cmp_eq_u32 s52,3\n"
"s_cselect_b32 s67,172032,0\n"
"s_waitcnt lgkmcnt(0)\n"
"s_barrier\n"
"v_mfma_f32_16x16x32_bf16 v[24:27],a[64:67],a[48:51],v[108:111]\n"
"v_add_f32 v0,0x80000000,v126\n"
"v_min_f32 v0,v0,v96\n"
"v_mfma_f32_16x16x32_bf16 v[24:27],a[68:71],a[52:55],v[24:27]\n"
"v_add_f32 v1,0xf149f2ca,v126\n"
"v_min_f32 v1,v1,v97\n"
"v_mfma_f32_16x16x32_bf16 v[24:27],a[72:75],a[56:59],v[24:27]\n"
"v_add_f32 v2,0xf1c9f2ca,v126\n"
"v_min_f32 v2,v2,v98\n"
"v_mfma_f32_16x16x32_bf16 v[24:27],a[76:79],a[60:63],v[24:27]\n"
"v_add_f32 v3,0xf2177617,v126\n"
"v_min_f32 v3,v3,v99\n"
"v_mfma_f32_16x16x32_bf16 v[28:31],a[92:95],a[60:63],v[108:111]\n"
"v_add_f32 v4,0xf349f2ca,v126\n"
"v_min_f32 v4,v4,v96\n"
"v_mfma_f32_16x16x32_bf16 v[28:31],a[88:91],a[56:59],v[28:31]\n"
"v_add_f32 v5,0xf35691f6,v126\n"
"v_min_f32 v5,v5,v97\n"
"v_mfma_f32_16x16x32_bf16 v[28:31],a[84:87],a[52:55],v[28:31]\n"
"v_add_f32 v6,0xf3633123,v126\n"
"v_min_f32 v6,v6,v98\n"
"v_mfma_f32_16x16x32_bf16 v[28:31],a[80:83],a[48:51],v[28:31]\n"
"v_add_f32 v7,0xf36fd050,v126\n"
"v_min_f32 v7,v7,v99\n"
"s_nop 1\n"
"v_mfma_f32_16x16x32_bf16 v[4:7],a[80:83],a[0:3],v[4:7]\n"
"s_andn2_b32 m0,s55,16384\n"
"s_add_u32 s54,s54,s22\n"
"v_mfma_f32_16x16x32_bf16 v[4:7],a[84:87],a[4:7],v[4:7]\n"
"ds_read_b128 a[96:99],v113 offset:16384\n"
"ds_read_b128 a[100:103],v113 offset:17408\n"
"v_mfma_f32_16x16x32_bf16 v[4:7],a[88:91],a[8:11],v[4:7]\n"
"ds_read_b128 a[104:107],v113 offset:18432\n"
"ds_read_b128 a[108:111],v113 offset:19456\n"
"v_mfma_f32_16x16x32_bf16 v[4:7],a[92:95],a[12:15],v[4:7]\n"
"ds_read_b128 a[112:115],v113 offset:20480\n"
"ds_read_b128 a[116:119],v113 offset:21504\n"
"v_mfma_f32_16x16x32_bf16 v[0:3],a[76:79],a[12:15],v[0:3]\n"
"ds_read_b128 a[120:123],v113 offset:22528\n"
"ds_read_b128 a[124:127],v113 offset:23552\n"
"v_mfma_f32_16x16x32_bf16 v[0:3],a[72:75],a[8:11],v[0:3]\n"
"buffer_load_dwordx4 v115,s[32:35],s54 offen sc1 lds\n"
"v_mfma_f32_16x16x32_bf16 v[0:3],a[68:71],a[4:7],v[0:3]\n"
"s_add_u32 m0,s67,0\n"
"v_max3_f32 v118,v24,v25,v26\n"
"v_mfma_f32_16x16x32_bf16 v[0:3],a[64:67],a[0:3],v[0:3]\n"
"buffer_load_dwordx4 v112,s[24:27],s64 offen offset:0 sc1 lds\n"
"v_mfma_f32_16x16x32_bf16 v[8:11],a[64:67],a[16:19],v[100:103]\n"
"buffer_load_dwordx4 v112,s[24:27],s64 offen offset:1024 sc1 lds\n"
"v_mfma_f32_16x16x32_bf16 v[8:11],a[68:71],a[20:23],v[8:11]\n"
"buffer_load_dwordx4 v112,s[24:27],s64 offen offset:2048 sc1 lds\n"
"v_mfma_f32_16x16x32_bf16 v[8:11],a[72:75],a[24:27],v[8:11]\n"
"buffer_load_dwordx4 v112,s[24:27],s64 offen offset:3072 sc1 lds\n"
"v_mfma_f32_16x16x32_bf16 v[8:11],a[76:79],a[28:31],v[8:11]\n"
"s_add_u32 m0,s67,4096\n"
"v_max3_f32 v118,v118,v27,v28\n"
"v_cvt_pk_bf16_f32 v192,v192,v193\n"
"v_mfma_f32_16x16x32_bf16 v[12:15],a[92:95],a[28:31],v[100:103]\n"
"buffer_load_dwordx4 v117,s[24:27],s64 offen offset:0 sc1 lds\n"
"v_mfma_f32_16x16x32_bf16 v[12:15],a[88:91],a[24:27],v[12:15]\n"
"buffer_load_dwordx4 v117,s[24:27],s64 offen offset:1024 sc1 lds\n"
"v_mfma_f32_16x16x32_bf16 v[12:15],a[84:87],a[20:23],v[12:15]\n"
"buffer_load_dwordx4 v117,s[24:27],s64 offen offset:2048 sc1 lds\n"
"v_mfma_f32_16x16x32_bf16 v[12:15],a[80:83],a[16:19],v[12:15]\n"
"buffer_load_dwordx4 v117,s[24:27],s64 offen offset:3072 sc1 lds\n"
"v_cvt_pk_bf16_f32 v193,v194,v195\n"
"v_cvt_pk_bf16_f32 v194,v196,v197\n"
"v_cvt_pk_bf16_f32 v195,v198,v199\n"
"v_cvt_pk_bf16_f32 v200,v200,v201\n"
"v_cvt_pk_bf16_f32 v201,v202,v203\n"
"v_cvt_pk_bf16_f32 v202,v204,v205\n"
"v_cvt_pk_bf16_f32 v203,v206,v207\n"
"ds_write_b128 v116,v[192:195] offset:32768\n"
"ds_write_b128 v116,v[200:203] offset:33792\n"
"v_cvt_pk_bf16_f32 v208,v208,v209\n"
"v_cvt_pk_bf16_f32 v209,v210,v211\n"
"v_cvt_pk_bf16_f32 v210,v212,v213\n"
"v_cvt_pk_bf16_f32 v211,v214,v215\n"
"v_cvt_pk_bf16_f32 v216,v216,v217\n"
"v_cvt_pk_bf16_f32 v217,v218,v219\n"
"v_cvt_pk_bf16_f32 v218,v220,v221\n"
"v_cvt_pk_bf16_f32 v219,v222,v223\n"
"ds_write_b128 v116,v[208:211] offset:34816\n"
"ds_write_b128 v116,v[216:219] offset:35840\n"
"ds_read_b128 a[32:35],v116 offset:32768\n"
"ds_read_b128 a[36:39],v116 offset:33792\n"
"ds_read_b128 a[40:43],v116 offset:34816\n"
"ds_read_b128 a[44:47],v116 offset:35840\n"
"s_waitcnt lgkmcnt(0)\n"
"v_mfma_f32_16x16x32_bf16 v[20:23],a[80:83],a[32:35],v[104:107]\n"
"s_add_u32 s64,s64,16384\n"
"v_max3_f32 v118,v118,v29,v30\n"
"v_mfma_f32_16x16x32_bf16 v[20:23],a[84:87],a[36:39],v[20:23]\n"
"v_max_f32 v118,v118,v31\n"
"v_mov_b32 v120,v118\n"
"v_mfma_f32_16x16x32_bf16 v[20:23],a[88:91],a[40:43],v[20:23]\n"
"v_max3_f32 v119,v0,v1,v2\n"
"v_max3_f32 v119,v119,v3,v4\n"
"v_mfma_f32_16x16x32_bf16 v[20:23],a[92:95],a[44:47],v[20:23]\n"
"buffer_store_dwordx4 a[224:227],v112,s[24:27],s56 offen offset:0 sc1\n"
"v_max3_f32 v119,v119,v5,v6\n"
"v_mfma_f32_16x16x32_bf16 v[16:19],a[76:79],a[44:47],v[104:107]\n"
"v_max_f32 v119,v119,v7\n"
"v_cmp_gt_f32 s[28:29],s16,v119\n"
"v_mfma_f32_16x16x32_bf16 v[16:19],a[72:75],a[40:43],v[16:19]\n"
"v_max_f32 v118,v118,v119\n"
"v_max3_f32 v119,v8,v9,v10\n"
"v_mfma_f32_16x16x32_bf16 v[16:19],a[68:71],a[36:39],v[16:19]\n"
"v_max3_f32 v119,v119,v11,v12\n"
"v_max3_f32 v119,v119,v13,v14\n"
"v_mfma_f32_16x16x32_bf16 v[16:19],a[64:67],a[32:35],v[16:19]\n"
"s_and_b32 s28,s28,0xffff\n"
"v_max_f32 v119,v119,v15\n"
"v_min_f32 v120,v120,v119\n"
"v_max_f32 v118,v118,v119\n"
"s_nop 3\n"
"v_max3_f32 v119,v16,v17,v18\n"
"v_max3_f32 v119,v119,v19,v20\n"
"v_max3_f32 v119,v119,v21,v22\n"
"v_max_f32 v119,v119,v23\n"
"v_min_f32 v120,v120,v119\n"
"v_max_f32 v118,v118,v119\n"
"v_cmp_gt_f32 s[96:97],s16,v120\n"
"v_cmp_lt_f32 vcc,s93,v118\n"
"s_or_b32 s96,s96,s28\n"
"s_or_b64 s[96:97],s[96:97],vcc\n"
"s_cbranch_scc1 .Lfk_cs_z3\n"
".Lfk_cr_z3:\n"
"s_waitcnt vmcnt(19) lgkmcnt(0)\n"
"s_barrier\n"
"v_mfma_f32_16x16x32_bf16 v[48:51],a[96:99],a[32:35],v[104:107]\n"
"v_add_f32 v40,0x80000000,v126\n"
"v_min_f32 v40,v40,v100\n"
"v_add_f32 v41,0xf149f2ca,v126\n"
"v_min_f32 v41,v41,v101\n"
"v_mfma_f32_16x16x32_bf16 v[48:51],a[100:103],a[36:39],v[48:51]\n"
"v_add_f32 v42,0xf1c9f2ca,v126\n"
"v_min_f32 v42,v42,v102\n"
"v_add_f32 v43,0xf2177617,v126\n"
"v_min_f32 v43,v43,v103\n"
"v_mfma_f32_16x16x32_bf16 v[48:51],a[104:107],a[40:43],v[48:51]\n"
"v_add_f32 v44,0xf349f2ca,v126\n"
"v_min_f32 v44,v44,v100\n"
"v_add_f32 v45,0xf35691f6,v126\n"
"v_min_f32 v45,v45,v101\n"
"v_mfma_f32_16x16x32_bf16 v[48:51],a[108:111],a[44:47],v[48:51]\n"
"v_add_f32 v46,0xf3633123,v126\n"
"v_min_f32 v46,v46,v102\n"
"v_add_f32 v47,0xf36fd050,v126\n"
"v_min_f32 v47,v47,v103\n"
"v_mfma_f32_16x16x32_bf16 v[52:55],a[124:127],a[44:47],v[104:107]\n"
"s_add_u32 m0,s55,4096\n"
"v_exp_f32 v0,v0\n"
"s_add_u32 s54,s54,s22\n"
"ds_read_b128 a[0:3],v116 offset:40960\n"
"v_exp_f32 v1,v1\n"
"v_mfma_f32_16x16x32_bf16 v[52:55],a[120:123],a[40:43],v[52:55]\n"
"buffer_load_dwordx4 v115,s[32:35],s54 offen sc1 lds\n"
"v_mfma_f32_16x16x32_bf16 v[52:55],a[116:119],a[36:39],v[52:55]\n"
"s_add_u32 m0,s67,16384\n"
"v_exp_f32 v2,v2\n"
"v_mfma_f32_16x16x32_bf16 v[52:55],a[112:115],a[32:35],v[52:55]\n"
"buffer_load_dwordx4 v112,s[24:27],s64 offen offset:0 sc1 lds\n"
"buffer_load_dwordx4 v112,s[24:27],s64 offen offset:1024 sc1 lds\n"
"v_exp_f32 v3,v3\n"
"v_exp_f32 v4,v4\n"
"v_mfma_f32_16x16x32_bf16 v[60:63],a[112:115],a[48:51],v[108:111]\n"
"buffer_load_dwordx4 v112,s[24:27],s64 offen offset:2048 sc1 lds\n"
"v_exp_f32 v5,v5\n"
"v_mfma_f32_16x16x32_bf16 v[60:63],a[116:119],a[52:55],v[60:63]\n"
"ds_read_b128 a[64:67],v113 offset:49152\n"
"ds_read_b128 a[68:71],v113 offset:50176\n"
"v_exp_f32 v6,v6\n"
"v_mfma_f32_16x16x32_bf16 v[60:63],a[120:123],a[56:59],v[60:63]\n"
"ds_read_b128 a[72:75],v113 offset:51200\n"
"ds_read_b128 a[76:79],v113 offset:52224\n"
"v_exp_f32 v7,v7\n"
"v_mfma_f32_16x16x32_bf16 v[60:63],a[124:127],a[60:63],v[60:63]\n"
"ds_read_b128 a[80:83],v113 offset:53248\n"
"ds_read_b128 a[84:87],v113 offset:54272\n"
"v_exp_f32 v8,v8\n"
"v_mfma_f32_16x16x32_bf16 v[56:59],a[108:111],a[60:63],v[108:111]\n"
"ds_read_b128 a[88:91],v113 offset:55296\n"
"ds_read_b128 a[92:95],v113 offset:56320\n"
"v_exp_f32 v9,v9\n"
"v_mfma_f32_16x16x32_bf16 v[56:59],a[104:107],a[56:59],v[56:59]\n"
"buffer_load_dwordx4 v112,s[24:27],s64 offen offset:3072 sc1 lds\n"
"v_mfma_f32_16x16x32_bf16 v[56:59],a[100:103],a[52:55],v[56:59]\n"
"s_add_u32 m0,s67,20480\n"
"v_exp_f32 v10,v10\n"
"v_mfma_f32_16x16x32_bf16 v[56:59],a[96:99],a[48:51],v[56:59]\n"
"buffer_load_dwordx4 v117,s[24:27],s64 offen offset:0 sc1 lds\n"
"buffer_load_dwordx4 v117,s[24:27],s64 offen offset:1024 sc1 lds\n"
"v_exp_f32 v11,v11\n"
"v_exp_f32 v12,v12\n"
"v_mfma_f32_16x16x32_bf16 v[40:43],a[96:99],a[16:19],v[40:43]\n"
"buffer_load_dwordx4 v117,s[24:27],s64 offen offset:2048 sc1 lds\n"
"v_exp_f32 v13,v13\n"
"v_mfma_f32_16x16x32_bf16 v[40:43],a[100:103],a[20:23],v[40:43]\n"
"buffer_store_dwordx4 a[168:171],v112,s[24:27],s3 offen offset:2048 sc1\n"
"v_cvt_pk_bf16_f32 v64,v0,v1\n"
"v_exp_f32 v14,v14\n"
"v_mfma_f32_16x16x32_bf16 v[40:43],a[104:107],a[24:27],v[40:43]\n"
"buffer_load_dwordx4 v117,s[24:27],s64 offen offset:3072 sc1 lds\n"
"v_mfma_f32_16x16x32_bf16 v[40:43],a[108:111],a[28:31],v[40:43]\n"
"s_add_u32 s64,s64,16384\n"
"v_exp_f32 v15,v15\n"
"v_cvt_pk_bf16_f32 v65,v2,v3\n"
"v_cvt_pk_bf16_f32 v66,v4,v5\n"
"v_exp_f32 v16,v16\n"
"v_mfma_f32_16x16x32_bf16 v[44:47],a[124:127],a[28:31],v[44:47]\n"
"buffer_store_dwordx4 a[228:231],v112,s[24:27],s56 offen offset:1024 sc1\n"
"v_cvt_pk_bf16_f32 v67,v6,v7\n"
"v_mfma_f32_16x16x32_bf16 v[44:47],a[120:123],a[24:27],v[44:47]\n"
"v_exp_f32 v17,v17\n"
"v_cvt_pk_bf16_f32 v68,v8,v9\n"
"v_cvt_pk_bf16_f32 v69,v10,v11\n"
"v_mfma_f32_16x16x32_bf16 v[44:47],a[116:119],a[20:23],v[44:47]\n"
"v_exp_f32 v18,v18\n"
"v_cvt_pk_bf16_f32 v70,v12,v13\n"
"v_cvt_pk_bf16_f32 v71,v14,v15\n"
"v_mfma_f32_16x16x32_bf16 v[44:47],a[112:115],a[16:19],v[44:47]\n"
"v_exp_f32 v19,v19\n"
"v_cvt_pk_bf16_f32 v72,v16,v17\n"
"v_max3_f32 v118,v48,v49,v50\n"
"v_exp_f32 v20,v20\n"
"v_cvt_pk_bf16_f32 v73,v18,v19\n"
"v_exp_f32 v21,v21\n"
"ds_read_b128 a[4:7],v116 offset:41984\n"
"ds_read_b128 a[8:11],v116 offset:43008\n"
"ds_read_b128 a[12:15],v116 offset:44032\n"
"v_exp_f32 v22,v22\n"
"v_exp_f32 v23,v23\n"
"v_exp_f32 v24,v24\n"
"v_cvt_pk_bf16_f32 v75,v22,v23\n"
"v_exp_f32 v25,v25\n"
"v_exp_f32 v26,v26\n"
"v_cvt_pk_bf16_f32 v76,v24,v25\n"
"v_exp_f32 v27,v27\n"
"v_exp_f32 v28,v28\n"
"v_cvt_pk_bf16_f32 v77,v26,v27\n"
"v_exp_f32 v29,v29\n"
"v_exp_f32 v30,v30\n"
"v_cvt_pk_bf16_f32 v78,v28,v29\n"
"v_exp_f32 v31,v31\n"
"v_cvt_pk_bf16_f32 v74,v20,v21\n"
"v_cvt_pk_bf16_f32 v79,v30,v31\n"
"v_max3_f32 v118,v118,v51,v52\n"
"v_max3_f32 v118,v118,v53,v54\n"
"v_max3_f32 v118,v118,v55,v60\n"
"v_max3_f32 v118,v118,v61,v62\n"
"v_max3_f32 v118,v118,v63,v56\n"
"v_max3_f32 v118,v118,v57,v58\n"
"v_max3_f32 v118,v118,v59,v40\n"
"v_max3_f32 v118,v118,v41,v42\n"
"v_max3_f32 v118,v118,v43,v44\n"
"v_max3_f32 v118,v118,v45,v46\n"
"v_max_f32 v118,v118,v47\n"
"v_cmp_lt_f32 vcc,s93,v118\n"
"s_nop 0\n"
"s_cbranch_vccnz .Lfk_cs_z0\n"
".Lfk_cr_z0:\n"
"s_waitcnt vmcnt(26) lgkmcnt(0)\n"
"s_barrier\n"
"v_mfma_f32_16x16x32_bf16 v[24:27],a[64:67],a[48:51],v[108:111]\n"
"ds_read_b128 a[96:99],v114 offset:40960\n"
"ds_read_b128 a[100:103],v114 offset:41984\n"
"v_mfma_f32_16x16x32_bf16 v[24:27],a[68:71],a[52:55],v[24:27]\n"
"ds_read_b128 a[104:107],v114 offset:43008\n"
"ds_read_b128 a[108:111],v114 offset:44032\n"
"v_mfma_f32_16x16x32_bf16 v[24:27],a[72:75],a[56:59],v[24:27]\n"
"ds_read_b128 a[112:115],v114 offset:45056\n"
"ds_read_b128 a[116:119],v114 offset:46080\n"
"v_mfma_f32_16x16x32_bf16 v[24:27],a[76:79],a[60:63],v[24:27]\n"
"ds_read_b128 a[120:123],v114 offset:47104\n"
"ds_read_b128 a[124:127],v114 offset:48128\n"
"v_mfma_f32_16x16x32_bf16 v[28:31],a[92:95],a[60:63],v[108:111]\n"
"v_add_f32 v16,0x80000000,v126\n"
"v_min_f32 v16,v16,v104\n"
"v_mfma_f32_16x16x32_bf16 v[28:31],a[88:91],a[56:59],v[28:31]\n"
"v_add_f32 v17,0xf149f2ca,v126\n"
"v_min_f32 v17,v17,v105\n"
"v_mfma_f32_16x16x32_bf16 v[28:31],a[84:87],a[52:55],v[28:31]\n"
"v_add_f32 v18,0xf1c9f2ca,v126\n"
"v_min_f32 v18,v18,v106\n"
"v_mfma_f32_16x16x32_bf16 v[28:31],a[80:83],a[48:51],v[28:31]\n"
"v_add_f32 v20,0xf349f2ca,v126\n"
"v_min_f32 v20,v20,v104\n"
"v_add_f32 v21,0xf35691f6,v126\n"
"v_min_f32 v21,v21,v105\n"
"v_add_f32 v22,0xf3633123,v126\n"
"v_min_f32 v22,v22,v106\n"
"v_add_f32 v23,0xf36fd050,v126\n"
"v_min_f32 v23,v23,v107\n"
"v_add_f32 v19,0xf2177617,v126\n"
"v_min_f32 v19,v19,v107\n"
"v_mfma_f32_16x16x32_bf16 v[20:23],a[80:83],a[32:35],v[20:23]\n"
"s_add_u32 m0,s55,8192\n"
"v_exp_f32 v40,v40\n"
"v_mfma_f32_16x16x32_bf16 v[20:23],a[84:87],a[36:39],v[20:23]\n"
"s_add_u32 s54,s54,s22\n"
"v_exp_f32 v41,v41\n"
"v_mfma_f32_16x16x32_bf16 v[20:23],a[88:91],a[40:43],v[20:23]\n"
"v_exp_f32 v42,v42\n"
"v_mfma_f32_16x16x32_bf16 v[20:23],a[92:95],a[44:47],v[20:23]\n"
"v_exp_f32 v43,v43\n"
"v_mfma_f32_16x16x32_bf16 v[16:19],a[76:79],a[44:47],v[16:19]\n"
"v_exp_f32 v44,v44\n"
"v_mfma_f32_16x16x32_bf16 v[16:19],a[72:75],a[40:43],v[16:19]\n"
"v_exp_f32 v45,v45\n"
"v_mfma_f32_16x16x32_bf16 v[16:19],a[68:71],a[36:39],v[16:19]\n"
"v_exp_f32 v46,v46\n"
"v_mfma_f32_16x16x32_bf16 v[16:19],a[64:67],a[32:35],v[16:19]\n"
"s_waitcnt lgkmcnt(0)\n"
"v_exp_f32 v47,v47\n"
"v_mfma_f32_16x16x32_bf16 v[128:131],a[96:99],v[64:67],0\n"
"v_exp_f32 v48,v48\n"
"v_mfma_f32_16x16x32_bf16 v[160:163],a[96:99],v[68:71],0\n"
"v_exp_f32 v49,v49\n"
"v_mfma_f32_16x16x32_bf16 v[192:195],a[96:99],v[72:75],0\n"
"ds_read_b128 a[64:67],v113 offset:32768\n"
"ds_read_b128 a[68:71],v113 offset:33792\n"
"v_mfma_f32_16x16x32_bf16 v[224:227],a[96:99],v[76:79],0\n"
"v_exp_f32 v50,v50\n"
"v_mfma_f32_16x16x32_bf16 a[140:143],a[144:147],v[76:79],0\n"
"ds_read_b128 a[72:75],v113 offset:34816\n"
"ds_read_b128 a[76:79],v113 offset:35840\n"
"v_mfma_f32_16x16x32_bf16 v[228:231],a[100:103],v[76:79],0\n"
"v_exp_f32 v51,v51\n"
"v_mfma_f32_16x16x32_bf16 v[132:135],a[100:103],v[64:67],0\n"
"ds_read_b128 a[80:83],v113 offset:36864\n"
"ds_read_b128 a[84:87],v113 offset:37888\n"
"v_mfma_f32_16x16x32_bf16 v[164:167],a[100:103],v[68:71],0\n"
"v_exp_f32 v52,v52\n"
"v_mfma_f32_16x16x32_bf16 v[196:199],a[100:103],v[72:75],0\n"
"v_exp_f32 v53,v53\n"
"v_mfma_f32_16x16x32_bf16 a[136:139],a[144:147],v[72:75],0\n"
"ds_read_b128 a[88:91],v113 offset:38912\n"
"ds_read_b128 a[92:95],v113 offset:39936\n"
"v_mfma_f32_16x16x32_bf16 v[200:203],a[104:107],v[72:75],0\n"
"v_exp_f32 v54,v54\n"
"v_mfma_f32_16x16x32_bf16 v[232:235],a[104:107],v[76:79],0\n"
"buffer_load_dwordx4 v115,s[32:35],s54 offen sc1 lds\n"
"v_mfma_f32_16x16x32_bf16 v[136:139],a[104:107],v[64:67],0\n"
"s_add_u32 m0,s67,49152\n"
"v_exp_f32 v55,v55\n"
"v_mfma_f32_16x16x32_bf16 v[168:171],a[104:107],v[68:71],0\n"
"buffer_load_dwordx4 v112,s[24:27],s64 offen offset:0 sc1 lds\n"
"v_mfma_f32_16x16x32_bf16 a[132:135],a[144:147],v[68:71],0\n"
"v_exp_f32 v56,v56\n"
"v_mfma_f32_16x16x32_bf16 v[172:175],a[108:111],v[68:71],0\n"
"buffer_load_dwordx4 v112,s[24:27],s64 offen offset:1024 sc1 lds\n"
"v_mfma_f32_16x16x32_bf16 v[204:207],a[108:111],v[72:75],0\n"
"v_exp_f32 v57,v57\n"
"v_mfma_f32_16x16x32_bf16 v[236:239],a[108:111],v[76:79],0\n"
"buffer_load_dwordx4 v112,s[24:27],s64 offen offset:2048 sc1 lds\n"
"v_mfma_f32_16x16x32_bf16 v[140:143],a[108:111],v[64:67],0\n"
"v_exp_f32 v58,v58\n"
"v_mfma_f32_16x16x32_bf16 a[128:131],a[144:147],v[64:67],0\n"
"buffer_load_dwordx4 v112,s[24:27],s64 offen offset:3072 sc1 lds\n"
"v_mfma_f32_16x16x32_bf16 v[144:147],a[112:115],v[64:67],0\n"
"s_add_u32 m0,s67,53248\n"
"v_exp_f32 v59,v59\n"
"v_mfma_f32_16x16x32_bf16 v[176:179],a[112:115],v[68:71],0\n"
"buffer_load_dwordx4 v117,s[24:27],s64 offen offset:0 sc1 lds\n"
"v_mfma_f32_16x16x32_bf16 v[208:211],a[112:115],v[72:75],0\n"
"v_exp_f32 v60,v60\n"
"v_mfma_f32_16x16x32_bf16 v[240:243],a[112:115],v[76:79],0\n"
"buffer_load_dwordx4 v117,s[24:27],s64 offen offset:1024 sc1 lds\n"
"v_mfma_f32_16x16x32_bf16 v[244:247],a[116:119],v[76:79],0\n"
"v_exp_f32 v61,v61\n"
"v_mfma_f32_16x16x32_bf16 v[148:151],a[116:119],v[64:67],0\n"
"buffer_load_dwordx4 v117,s[24:27],s64 offen offset:2048 sc1 lds\n"
"v_mfma_f32_16x16x32_bf16 v[180:183],a[116:119],v[68:71],0\n"
"v_exp_f32 v62,v62\n"
"v_mfma_f32_16x16x32_bf16 v[212:215],a[116:119],v[72:75],0\n"
"buffer_load_dwordx4 v117,s[24:27],s64 offen offset:3072 sc1 lds\n"
"v_mfma_f32_16x16x32_bf16 v[216:219],a[120:123],v[72:75],0\n"
"s_add_u32 s64,s64,16384\n"
"v_exp_f32 v63,v63\n"
"v_mfma_f32_16x16x32_bf16 v[248:251],a[120:123],v[76:79],0\n"
"buffer_store_dwordx4 a[232:235],v112,s[24:27],s56 offen offset:2048 sc1\n"
"s_and_b32 s67,s67,8192\n"
"v_mfma_f32_16x16x32_bf16 v[152:155],a[120:123],v[64:67],0\n"
"buffer_store_dwordx4 a[172:175],v112,s[24:27],s3 offen offset:3072 sc1\n"
"s_sub_u32 s3,s3,16384\n"
"v_mfma_f32_16x16x32_bf16 v[184:187],a[120:123],v[68:71],0\n"
"buffer_store_dwordx4 a[176:179],v112,s[24:27],s3 offen offset:0 sc1\n"
"v_cvt_pk_bf16_f32 v84,v40,v41\n"
"v_mfma_f32_16x16x32_bf16 v[188:191],a[124:127],v[68:71],0\n"
"v_cvt_pk_bf16_f32 v85,v42,v43\n"
"v_cvt_pk_bf16_f32 v86,v44,v45\n"
"v_mfma_f32_16x16x32_bf16 v[220:223],a[124:127],v[72:75],0\n"
"v_cvt_pk_bf16_f32 v87,v46,v47\n"
"v_cvt_pk_bf16_f32 v88,v48,v49\n"
"v_mfma_f32_16x16x32_bf16 v[252:255],a[124:127],v[76:79],0\n"
"v_cvt_pk_bf16_f32 v89,v50,v51\n"
"v_cvt_pk_bf16_f32 v90,v52,v53\n"
"v_mfma_f32_16x16x32_bf16 v[156:159],a[124:127],v[64:67],0\n"
"v_cvt_pk_bf16_f32 v91,v54,v55\n"
"v_cvt_pk_bf16_f32 v92,v56,v57\n"
"v_cvt_pk_bf16_f32 v93,v58,v59\n"
"ds_read_b128 a[16:19],v116 offset:8192\n"
"ds_read_b128 a[20:23],v116 offset:9216\n"
"ds_read_b128 a[24:27],v116 offset:10240\n"
"ds_read_b128 a[28:31],v116 offset:11264\n"
"v_cvt_pk_bf16_f32 v94,v60,v61\n"
"v_cvt_pk_bf16_f32 v95,v62,v63\n"
"v_max3_f32 v118,v24,v25,v26\n"
"v_max3_f32 v118,v118,v27,v28\n"
"v_max3_f32 v118,v118,v29,v30\n"
"v_max3_f32 v118,v118,v31,v20\n"
"v_max3_f32 v118,v118,v21,v22\n"
"v_max3_f32 v118,v118,v23,v16\n"
"v_max3_f32 v118,v118,v17,v18\n"
"v_max_f32 v118,v118,v19\n"
"v_cmp_lt_f32 vcc,s93,v118\n"
"s_nop 0\n"
"s_cbranch_vccnz .Lfk_cs_z1\n"
".Lfk_cr_z1:\n"
"v_mov_b32 v113,v112\n"
"s_waitcnt vmcnt(21) lgkmcnt(0)\n"
"s_barrier\n"
"v_add_f32 v56,0x80000000,v126\n"
"v_min_f32 v56,v56,v108\n"
"v_add_f32 v57,0xf149f2ca,v126\n"
"v_min_f32 v57,v57,v109\n"
"v_add_f32 v58,0xf1c9f2ca,v126\n"
"v_min_f32 v58,v58,v110\n"
"v_add_f32 v59,0xf2177617,v126\n"
"v_min_f32 v59,v59,v111\n"
"v_add_f32 v60,0xf349f2ca,v126\n"
"v_min_f32 v60,v60,v108\n"
"v_add_f32 v61,0xf35691f6,v126\n"
"v_min_f32 v61,v61,v109\n"
"v_add_f32 v62,0xf3633123,v126\n"
"v_min_f32 v62,v62,v110\n"
"v_add_f32 v63,0xf36fd050,v126\n"
"v_min_f32 v63,v63,v111\n"
"v_exp_f32 v16,v16\n"
"v_mfma_f32_16x16x32_bf16 v[56:59],a[64:67],a[48:51],v[56:59]\n"
"s_add_u32 m0,s55,12288\n"
"ds_read_b128 a[96:99],v114 offset:8192\n"
"ds_read_b128 a[100:103],v114 offset:9216\n"
"ds_read_b128 a[104:107],v114 offset:10240\n"
"v_exp_f32 v17,v17\n"
"v_mfma_f32_16x16x32_bf16 v[56:59],a[68:71],a[52:55],v[56:59]\n"
"s_add_u32 s54,s54,s22\n"
"ds_read_b128 a[108:111],v114 offset:11264\n"
"ds_read_b128 a[112:115],v114 offset:12288\n"
"ds_read_b128 a[116:119],v114 offset:13312\n"
"v_mfma_f32_16x16x32_bf16 v[56:59],a[72:75],a[56:59],v[56:59]\n"
"ds_read_b128 a[120:123],v114 offset:14336\n"
"ds_read_b128 a[124:127],v114 offset:15360\n"
"buffer_load_dwordx4 v115,s[32:35],s54 offen sc1 lds\n"
"v_cvt_pk_bf16_f32 v72,v16,v17\n"
"v_exp_f32 v18,v18\n"
"v_mfma_f32_16x16x32_bf16 v[56:59],a[76:79],a[60:63],v[56:59]\n"
"ds_read_b128 a[32:35],v116 offset:24576\n"
"ds_read_b128 a[36:39],v116 offset:25600\n"
"ds_read_b128 a[40:43],v116 offset:26624\n"
"v_mfma_f32_16x16x32_bf16 v[60:63],a[92:95],a[60:63],v[60:63]\n"
"buffer_store_dwordx4 a[180:183],v112,s[24:27],s3 offen offset:1024 sc1\n"
"ds_read_b128 a[44:47],v116 offset:27648\n"
"v_mfma_f32_16x16x32_bf16 v[60:63],a[88:91],a[56:59],v[60:63]\n"
"s_add_u32 m0,s67,32768\n"
"v_exp_f32 v19,v19\n"
"v_mfma_f32_16x16x32_bf16 v[60:63],a[84:87],a[52:55],v[60:63]\n"
"buffer_load_dwordx4 v112,s[24:27],s64 offen offset:0 sc1 lds\n"
"v_cvt_pk_bf16_f32 v73,v18,v19\n"
"v_exp_f32 v20,v20\n"
"v_mfma_f32_16x16x32_bf16 v[60:63],a[80:83],a[48:51],v[60:63]\n"
"ds_read_b128 a[64:67],v113 offset:0\n"
"ds_read_b128 a[68:71],v113 offset:1024\n"
"ds_read_b128 a[72:75],v113 offset:2048\n"
"s_waitcnt lgkmcnt(7)\n"
"v_mfma_f32_16x16x32_bf16 v[160:163],a[96:99],v[84:87],v[160:163]\n"
"ds_read_b128 a[76:79],v113 offset:3072\n"
"ds_read_b128 a[80:83],v113 offset:4096\n"
"ds_read_b128 a[84:87],v113 offset:5120\n"
"ds_read_b128 a[88:91],v113 offset:6144\n"
"ds_read_b128 a[92:95],v113 offset:7168\n"
"v_exp_f32 v21,v21\n"
"v_mfma_f32_16x16x32_bf16 a[132:135],a[144:147],v[84:87],a[132:135]\n"
"buffer_load_dwordx4 v112,s[24:27],s64 offen offset:1024 sc1 lds\n"
"v_max3_f32 v118,v56,v57,v58\n"
"v_mfma_f32_16x16x32_bf16 v[164:167],a[100:103],v[84:87],v[164:167]\n"
"v_exp_f32 v22,v22\n"
"v_cvt_pk_bf16_f32 v74,v20,v21\n"
"ds_read_b128 a[48:51],v116 offset:57344\n"
"ds_read_b128 a[52:55],v116 offset:58368\n"
"v_mfma_f32_16x16x32_bf16 v[168:171],a[104:107],v[84:87],v[168:171]\n"
"v_max3_f32 v118,v118,v59,v60\n"
"v_max3_f32 v118,v118,v61,v62\n"
"v_max_f32 v118,v118,v63\n"
"v_exp_f32 v23,v23\n"
"v_mfma_f32_16x16x32_bf16 v[172:175],a[108:111],v[84:87],v[172:175]\n"
"ds_read_b128 a[56:59],v116 offset:59392\n"
"buffer_load_dwordx4 v112,s[24:27],s64 offen offset:2048 sc1 lds\n"
"ds_read_b128 a[60:63],v116 offset:60416\n"
"v_mfma_f32_16x16x32_bf16 v[176:179],a[112:115],v[84:87],v[176:179]\n"
"v_cvt_pk_bf16_f32 v75,v22,v23\n"
"v_accvgpr_read_b32 v122,a128\n"
"v_rcp_f32 v122,v122\n"
"v_cmp_lt_f32 vcc,s93,v118\n"
"v_pk_mul_f32 v[128:129],v[128:129],v[122:123] op_sel_hi:[1,0]\n"
"v_mfma_f32_16x16x32_bf16 v[180:183],a[116:119],v[84:87],v[180:183]\n"
"buffer_store_dwordx4 a[184:187],v112,s[24:27],s3 offen offset:2048 sc1\n"
"v_exp_f32 v24,v24\n"
"v_pk_mul_f32 v[130:131],v[130:131],v[122:123] op_sel_hi:[1,0]\n"
"v_pk_mul_f32 v[132:133],v[132:133],v[122:123] op_sel_hi:[1,0]\n"
"v_pk_mul_f32 v[134:135],v[134:135],v[122:123] op_sel_hi:[1,0]\n"
"v_mfma_f32_16x16x32_bf16 v[184:187],a[120:123],v[84:87],v[184:187]\n"
"buffer_load_dwordx4 v112,s[24:27],s64 offen offset:3072 sc1 lds\n"
"v_cvt_pk_bf16_f32 v128,v128,v129\n"
"v_cvt_pk_bf16_f32 v129,v130,v131\n"
"v_cvt_pk_bf16_f32 v130,v132,v133\n"
"v_cvt_pk_bf16_f32 v131,v134,v135\n"
"v_mfma_f32_16x16x32_bf16 v[188:191],a[124:127],v[84:87],v[188:191]\n"
"buffer_store_dwordx4 v[128:131],v112,s[24:27],s70 offen offset:0 sc1\n"
"v_pk_mul_f32 v[136:137],v[136:137],v[122:123] op_sel_hi:[1,0]\n"
"v_mfma_f32_16x16x32_bf16 v[192:195],a[96:99],v[88:91],v[192:195]\n"
"s_add_u32 m0,s67,36864\n"
"v_exp_f32 v25,v25\n"
"v_exp_f32 v27,v27\n"
"v_cvt_pk_bf16_f32 v76,v24,v25\n"
"v_cvt_pk_bf16_f32 v136,v136,v137\n"
"v_pk_mul_f32 v[138:139],v[138:139],v[122:123] op_sel_hi:[1,0]\n"
"v_pk_mul_f32 v[140:141],v[140:141],v[122:123] op_sel_hi:[1,0]\n"
"v_pk_mul_f32 v[142:143],v[142:143],v[122:123] op_sel_hi:[1,0]\n"
"v_mfma_f32_16x16x32_bf16 a[136:139],a[144:147],v[88:91],a[136:139]\n"
"buffer_load_dwordx4 v117,s[24:27],s64 offen offset:0 sc1 lds\n"
"v_cvt_pk_bf16_f32 v137,v138,v139\n"
"v_mfma_f32_16x16x32_bf16 v[196:199],a[100:103],v[88:91],v[196:199]\n"
"v_cvt_pk_bf16_f32 v138,v140,v141\n"
"v_cvt_pk_bf16_f32 v139,v142,v143\n"
"v_exp_f32 v26,v26\n"
"v_mfma_f32_16x16x32_bf16 v[200:203],a[104:107],v[88:91],v[200:203]\n"
"buffer_store_dwordx4 v[136:139],v112,s[24:27],s70 offen offset:1024 sc1\n"
"v_cvt_pk_bf16_f32 v77,v26,v27\n"
"v_pk_mul_f32 v[144:145],v[144:145],v[122:123] op_sel_hi:[1,0]\n"
"v_pk_mul_f32 v[146:147],v[146:147],v[122:123] op_sel_hi:[1,0]\n"
"v_pk_mul_f32 v[148:149],v[148:149],v[122:123] op_sel_hi:[1,0]\n"
"v_pk_mul_f32 v[150:151],v[150:151],v[122:123] op_sel_hi:[1,0]\n"
"v_exp_f32 v28,v28\n"
"v_mfma_f32_16x16x32_bf16 v[204:207],a[108:111],v[88:91],v[204:207]\n"
"buffer_load_dwordx4 v117,s[24:27],s64 offen offset:1024 sc1 lds\n"
"buffer_store_dwordx4 a[188:191],v112,s[24:27],s3 offen offset:3072 sc1\n"
"v_mfma_f32_16x16x32_bf16 v[208:211],a[112:115],v[88:91],v[208:211]\n"
"s_or_b32 s3,s3,0x3c000\n"
"v_cvt_pk_bf16_f32 v144,v144,v145\n"
"v_cvt_pk_bf16_f32 v145,v146,v147\n"
"v_cvt_pk_bf16_f32 v146,v148,v149\n"
"v_cvt_pk_bf16_f32 v147,v150,v151\n"
"v_mfma_f32_16x16x32_bf16 v[212:215],a[116:119],v[88:91],v[212:215]\n"
"buffer_store_dwordx4 v[144:147],v112,s[24:27],s70 offen offset:2048 sc1\n"
"v_pk_mul_f32 v[152:153],v[152:153],v[122:123] op_sel_hi:[1,0]\n"
"v_pk_mul_f32 v[154:155],v[154:155],v[122:123] op_sel_hi:[1,0]\n"
"v_mfma_f32_16x16x32_bf16 v[216:219],a[120:123],v[88:91],v[216:219]\n"
"buffer_load_dwordx4 v117,s[24:27],s64 offen offset:2048 sc1 lds\n"
"v_exp_f32 v29,v29\n"
"v_exp_f32 v31,v31\n"
"v_cvt_pk_bf16_f32 v78,v28,v29\n"
"v_pk_mul_f32 v[156:157],v[156:157],v[122:123] op_sel_hi:[1,0]\n"
"v_pk_mul_f32 v[158:159],v[158:159],v[122:123] op_sel_hi:[1,0]\n"
"v_mfma_f32_16x16x32_bf16 v[220:223],a[124:127],v[88:91],v[220:223]\n"
"v_cvt_pk_bf16_f32 v152,v152,v153\n"
"v_cvt_pk_bf16_f32 v153,v154,v155\n"
"v_cvt_pk_bf16_f32 v154,v156,v157\n"
"v_cvt_pk_bf16_f32 v155,v158,v159\n"
"v_mfma_f32_16x16x32_bf16 v[224:227],a[96:99],v[92:95],v[224:227]\n"
"buffer_store_dwordx4 v[152:155],v112,s[24:27],s70 offen offset:3072 sc1\n"
"v_accvgpr_read_b32 v122,a132\n"
"v_mfma_f32_16x16x32_bf16 a[140:143],a[144:147],v[92:95],a[140:143]\n"
"s_add_u32 s70,s70,262144\n"
"buffer_load_dwordx4 v117,s[24:27],s64 offen offset:3072 sc1 lds\n"
"v_mfma_f32_16x16x32_bf16 v[228:231],a[100:103],v[92:95],v[228:231]\n"
"s_add_u32 s64,s64,16384\n"
"v_exp_f32 v30,v30\n"
"v_rcp_f32 v122,v122\n"
"v_cvt_pk_bf16_f32 v79,v30,v31\n"
"v_pk_mul_f32 v[160:161],v[160:161],v[122:123] op_sel_hi:[1,0]\n"
"v_pk_mul_f32 v[162:163],v[162:163],v[122:123] op_sel_hi:[1,0]\n"
"v_pk_mul_f32 v[164:165],v[164:165],v[122:123] op_sel_hi:[1,0]\n"
"v_pk_mul_f32 v[166:167],v[166:167],v[122:123] op_sel_hi:[1,0]\n"
"v_mfma_f32_16x16x32_bf16 v[232:235],a[104:107],v[92:95],v[232:235]\n"
"buffer_store_dwordx4 a[236:239],v112,s[24:27],s56 offen offset:3072 sc1\n"
"v_cvt_pk_bf16_f32 v160,v160,v161\n"
"v_cvt_pk_bf16_f32 v161,v162,v163\n"
"v_cvt_pk_bf16_f32 v162,v164,v165\n"
"v_cvt_pk_bf16_f32 v163,v166,v167\n"
"v_mfma_f32_16x16x32_bf16 v[236:239],a[108:111],v[92:95],v[236:239]\n"
"buffer_store_dwordx4 v[160:163],v112,s[24:27],s70 offen offset:0 sc1\n"
"v_pk_mul_f32 v[168:169],v[168:169],v[122:123] op_sel_hi:[1,0]\n"
"v_pk_mul_f32 v[170:171],v[170:171],v[122:123] op_sel_hi:[1,0]\n"
"v_pk_mul_f32 v[172:173],v[172:173],v[122:123] op_sel_hi:[1,0]\n"
"v_pk_mul_f32 v[174:175],v[174:175],v[122:123] op_sel_hi:[1,0]\n"
"v_mfma_f32_16x16x32_bf16 v[240:243],a[112:115],v[92:95],v[240:243]\n"
"v_cvt_pk_bf16_f32 v168,v168,v169\n"
"v_cvt_pk_bf16_f32 v169,v170,v171\n"
"v_cvt_pk_bf16_f32 v170,v172,v173\n"
"v_cvt_pk_bf16_f32 v171,v174,v175\n"
"v_mfma_f32_16x16x32_bf16 v[244:247],a[116:119],v[92:95],v[244:247]\n"
"buffer_store_dwordx4 v[168:171],v112,s[24:27],s70 offen offset:1024 sc1\n"
"v_pk_mul_f32 v[176:177],v[176:177],v[122:123] op_sel_hi:[1,0]\n"
"v_pk_mul_f32 v[178:179],v[178:179],v[122:123] op_sel_hi:[1,0]\n"
"v_pk_mul_f32 v[180:181],v[180:181],v[122:123] op_sel_hi:[1,0]\n"
"v_pk_mul_f32 v[182:183],v[182:183],v[122:123] op_sel_hi:[1,0]\n"
"v_mfma_f32_16x16x32_bf16 v[248:251],a[120:123],v[92:95],v[248:251]\n"
"v_cvt_pk_bf16_f32 v176,v176,v177\n"
"v_cvt_pk_bf16_f32 v177,v178,v179\n"
"v_cvt_pk_bf16_f32 v178,v180,v181\n"
"v_cvt_pk_bf16_f32 v179,v182,v183\n"
"v_mfma_f32_16x16x32_bf16 v[252:255],a[124:127],v[92:95],v[252:255]\n"
"buffer_store_dwordx4 v[176:179],v112,s[24:27],s70 offen offset:2048 sc1\n"
"v_pk_mul_f32 v[184:185],v[184:185],v[122:123] op_sel_hi:[1,0]\n"
"v_pk_mul_f32 v[186:187],v[186:187],v[122:123] op_sel_hi:[1,0]\n"
"v_pk_mul_f32 v[188:189],v[188:189],v[122:123] op_sel_hi:[1,0]\n"
"v_pk_mul_f32 v[190:191],v[190:191],v[122:123] op_sel_hi:[1,0]\n"
"v_cvt_pk_bf16_f32 v184,v184,v185\n"
"v_cvt_pk_bf16_f32 v185,v186,v187\n"
"v_cvt_pk_bf16_f32 v186,v188,v189\n"
"v_cvt_pk_bf16_f32 v187,v190,v191\n"
"buffer_store_dwordx4 v[184:187],v112,s[24:27],s70 offen offset:3072 sc1\n"
"s_add_u32 s70,s70,262144\n"
"s_cbranch_vccnz .Lfk_cs_z2\n"
".Lfk_cr_z2:\n"
"s_waitcnt vmcnt(34) lgkmcnt(0)\n"
"s_barrier\n"
"s_add_u32 s89,s89,1\n"
"s_branch .Lfk_winB\n"
".Lfk_ev:\n"
"s_xor_b32 s55,s55,16384\n"
"s_andn2_b32 s91,s55,16384\n"
"v_lshrrev_b32 v121,1,v112\n"
"v_add_u32 v121,s91,v121\n"
"ds_read_b64_tr_b16 a[160:161],v121 offset:0\n"
"ds_read_b64_tr_b16 a[162:163],v121 offset:512\n"
"s_add_u32 s56,s56,4096\n"
"s_swappc_b64 s[20:21],s[94:95]\n"
"s_xor_b32 s55,s55,16384\n"
"s_andn2_b32 s91,s55,16384\n"
"v_lshrrev_b32 v121,1,v112\n"
"v_add_u32 v121,s91,v121\n"
"ds_read_b64_tr_b16 a[164:165],v121 offset:4096\n"
"ds_read_b64_tr_b16 a[166:167],v121 offset:4608\n"
"ds_read_b64_tr_b16 a[168:169],v121 offset:8192\n"
"ds_read_b64_tr_b16 a[170:171],v121 offset:8704\n"
"ds_read_b64_tr_b16 a[172:173],v121 offset:12288\n"
"ds_read_b64_tr_b16 a[174:175],v121 offset:12800\n"
"ds_read_b64_tr_b16 a[176:177],v121 offset:0\n"
"ds_read_b64_tr_b16 a[178:179],v121 offset:512\n"
"buffer_store_dwordx2 a[154:155],off,s[24:27],s2 sc1\n"
"s_and_b32 s2,s2,0xfffffc00\n"
"s_mov_b32 s56,s10\n"
"v_accvgpr_write_b32 a240,0\n"
"v_accvgpr_write_b32 a241,0\n"
"v_accvgpr_write_b32 a242,0\n"
"v_accvgpr_write_b32 a243,0\n"
"v_accvgpr_write_b32 a244,0\n"
"v_accvgpr_write_b32 a245,0\n"
"v_accvgpr_write_b32 a246,0\n"
"v_accvgpr_write_b32 a247,0\n"
"v_accvgpr_write_b32 a248,0\n"
"v_accvgpr_write_b32 a249,0\n"
"v_accvgpr_write_b32 a250,0\n"
"v_accvgpr_write_b32 a251,0\n"
"v_accvgpr_write_b32 a252,0\n"
"v_accvgpr_write_b32 a253,0\n"
"v_accvgpr_write_b32 a254,0\n"
"v_accvgpr_write_b32 a255,0\n"
"s_mov_b64 s[32:33],s[36:37]\n"
"s_mov_b32 s54,-229376\n"
"s_swappc_b64 s[20:21],s[94:95]\n"
"s_xor_b32 s55,s55,16384\n"
"s_add_u32 s56,s56,131072\n"
"s_andn2_b32 s91,s55,16384\n"
"v_lshrrev_b32 v121,1,v112\n"
"v_add_u32 v121,s91,v121\n"
"ds_read_b64_tr_b16 a[180:181],v121 offset:20480\n"
"ds_read_b64_tr_b16 a[182:183],v121 offset:20992\n"
"ds_read_b64_tr_b16 a[184:185],v121 offset:24576\n"
"ds_read_b64_tr_b16 a[186:187],v121 offset:25088\n"
"ds_read_b64_tr_b16 a[188:189],v121 offset:28672\n"
"ds_read_b64_tr_b16 a[190:191],v121 offset:29184\n"
"ds_read_b64_tr_b16 a[192:193],v121 offset:0\n"
"ds_read_b64_tr_b16 a[194:195],v121 offset:512\n"
"buffer_load_dwordx2 a[150:151],v112,s[24:27],s2 offen sc1\n"
"s_add_u32 s82,s59,8192\n"
"buffer_store_dwordx2 a[154:155],off,s[24:27],s82 sc1\n"
"s_swappc_b64 s[20:21],s[94:95]\n"
"s_xor_b32 s55,s55,16384\n"
"s_add_u32 s56,s56,131072\n"
"s_andn2_b32 s91,s55,16384\n"
"v_lshrrev_b32 v121,1,v112\n"
"v_add_u32 v121,s91,v121\n"
"ds_read_b64_tr_b16 a[196:197],v121 offset:4096\n"
"ds_read_b64_tr_b16 a[198:199],v121 offset:4608\n"
"ds_read_b64_tr_b16 a[200:201],v121 offset:8192\n"
"ds_read_b64_tr_b16 a[202:203],v121 offset:8704\n"
"ds_read_b64_tr_b16 a[204:205],v121 offset:12288\n"
"ds_read_b64_tr_b16 a[206:207],v121 offset:12800\n"
"ds_read_b64_tr_b16 a[208:209],v121 offset:0\n"
"ds_read_b64_tr_b16 a[210:211],v121 offset:512\n"
"s_mov_b32 s84,0x40000\n"
".Lfk_w_chk:\n"
"v_accvgpr_read_b32 v119,a150\n"
"v_accvgpr_read_b32 v120,a151\n"
"v_xor_b32 v119,s80,v119\n"
"v_xor_b32 v120,s81,v120\n"
"v_or_b32 v119,v119,v120\n"
"v_cmp_ne_u32 vcc,0,v119\n"
"s_nop 0\n"
"s_cbranch_vccz .Lfk_w_ok\n"
"s_sub_u32 s84,s84,1\n"
"s_cbranch_scc1 .Lfk_w_ok\n"
"s_sleep 4\n"
"buffer_load_dwordx2 a[150:151],v112,s[24:27],s2 offen sc1\n"
"s_waitcnt vmcnt(0)\n"
"s_branch .Lfk_w_chk\n"
".Lfk_w_ok:\n"
"v_lshrrev_b32 v121,1,v112\n"
"v_and_b32 v121,0xf8,v121\n"
"s_add_u32 s82,s63,8192\n"
"buffer_load_dwordx2 a[150:151],v121,s[24:27],s82 offen sc1\n"
"s_mov_b64 s[32:33],s[24:25]\n"
"s_mov_b32 s34,s26\n"
"s_mov_b32 s22,1024\n"
"v_mov_b32 v115,v112\n"
"s_sub_u32 s54,s3,1024\n"
"s_swappc_b64 s[20:21],s[94:95]\n"
"s_xor_b32 s55,s55,16384\n"
"s_add_u32 s56,s56,131072\n"
"s_andn2_b32 s91,s55,16384\n"
"v_lshrrev_b32 v121,1,v112\n"
"v_add_u32 v121,s91,v121\n"
"ds_read_b64_tr_b16 a[212:213],v121 offset:20480\n"
"ds_read_b64_tr_b16 a[214:215],v121 offset:20992\n"
"ds_read_b64_tr_b16 a[216:217],v121 offset:24576\n"
"ds_read_b64_tr_b16 a[218:219],v121 offset:25088\n"
"ds_read_b64_tr_b16 a[220:221],v121 offset:28672\n"
"ds_read_b64_tr_b16 a[222:223],v121 offset:29184\n"
"v_add_u32 v121,s91,v112\n"
"ds_read_b128 a[224:227],v121 offset:0\n"
"s_sub_u32 s54,s3,17408\n"
"s_mov_b32 s84,0x40000\n"
".Lfk_k_chk:\n"
"v_accvgpr_read_b32 v119,a150\n"
"v_accvgpr_read_b32 v120,a151\n"
"v_xor_b32 v119,s80,v119\n"
"v_xor_b32 v120,s81,v120\n"
"v_or_b32 v119,v119,v120\n"
"v_cmp_ne_u32 vcc,0,v119\n"
"s_nop 0\n"
"s_cbranch_vccz .Lfk_k_ok\n"
"s_sub_u32 s84,s84,1\n"
"s_cbranch_scc1 .Lfk_k_ok\n"
"s_sleep 4\n"
"v_lshrrev_b32 v121,1,v112\n"
"v_and_b32 v121,0xf8,v121\n"
"s_add_u32 s82,s63,8192\n"
"buffer_load_dwordx2 a[150:151],v121,s[24:27],s82 offen sc1\n"
"s_waitcnt vmcnt(0)\n"
"s_branch .Lfk_k_chk\n"
".Lfk_k_ok:\n"
"s_swappc_b64 s[20:21],s[94:95]\n"
"s_xor_b32 s55,s55,16384\n"
"s_andn2_b32 s91,s55,16384\n"
"v_add_u32 v121,s91,v112\n"
"ds_read_b128 a[228:231],v121 offset:4096\n"
"ds_read_b128 a[232:235],v121 offset:8192\n"
"ds_read_b128 a[236:239],v121 offset:12288\n"
"ds_read_b128 a[240:243],v121 offset:0\n"
"s_sub_u32 s54,s3,99328\n"
"s_mov_b32 s56,2146435072\n"
"s_getpc_b64 s[96:97]\n"
".Lfk_rt1:\n"
"s_sub_u32 s96,s96,.Lfk_rt1-.Lfk_tailA\n"
"s_subb_u32 s97,s97,0\n"
"s_mov_b32 s98,.Lfk_cold-.Lfk_tailA\n"
"s_mov_b32 s99,0x20000\n"
"s_lshr_b32 s82,s100,3\n"
"s_lshl2_add_u32 s82,s82,s52\n"
"s_lshl_b32 s82,s82,9\n"
"v_lshrrev_b32 v121,1,v112\n"
"buffer_load_dword a148,v121,s[96:99],s82 offen\n"
"s_swappc_b64 s[20:21],s[94:95]\n"
"s_xor_b32 s55,s55,16384\n"
"s_andn2_b32 s91,s55,16384\n"
"v_add_u32 v121,s91,v112\n"
"ds_read_b128 a[244:247],v121 offset:20480\n"
"ds_read_b128 a[248:251],v121 offset:24576\n"
"ds_read_b128 a[252:255],v121 offset:28672\n"
"ds_read_b128 a[156:159],v121 offset:0\n"
"s_sub_u32 s54,s3,115712\n"
"s_lshl_b32 s82,s100,3\n"
"s_add_u32 s82,s82,148946944\n"
"s_cmp_eq_u32 s52,0\n"
"s_cselect_b32 s82,s82,2146435072\n"
"buffer_store_dwordx2 a[154:155],off,s[24:27],s82 sc1\n"
"s_lshr_b32 s82,s3,9\n"
"s_and_b32 s82,s82,0x3000\n"
"s_add_u32 s11,s11,s82\n"
"s_add_u32 s9,s9,s82\n"
"s_lshl3_add_u32 s8,s82,s8\n"
"s_lshr_b32 s82,s3,18\n"
"s_sub_u32 s82,s82,s60\n"
"s_and_b32 s82,s82,7\n"
"s_lshl_b32 s82,s82,17\n"
"s_add_u32 s8,s8,s82\n"
"v_lshrrev_b32 v120,1,v112\n"
"buffer_load_dwordx2 a[150:151],v120,s[24:27],s11 offen sc1\n"
"s_lshl_b32 s30,s78,1\n"
"s_swappc_b64 s[20:21],s[94:95]\n"
"s_nop 1\n"
".Lfk_xn0_s:\n"
"s_mov_b32 s84,0x40000\n"
"buffer_load_dwordx4 v[74:77],v112,s[24:27],s58 offen sc1\n"
"s_waitcnt vmcnt(0)\n"
".Lfk_xn0:\n"
"v_xor_b32 v116,s80,v74\n"
"v_xor_b32 v117,s81,v75\n"
"v_or_b32 v116,v116,v117\n"
"v_xor_b32 v117,s80,v76\n"
"v_or_b32 v116,v116,v117\n"
"v_xor_b32 v117,s81,v77\n"
"v_or_b32 v116,v116,v117\n"
"v_cmp_ne_u32 vcc,0,v116\n"
"s_nop 0\n"
"s_cbranch_vccz .Lfk_xn0_x\n"
"s_sleep 4\n"
"s_mov_b64 exec,vcc\n"
"buffer_load_dwordx4 v[74:77],v112,s[24:27],s58 offen sc1\n"
"s_mov_b64 exec,-1\n"
"s_sub_u32 s84,s84,1\n"
"s_waitcnt vmcnt(0)\n"
"s_cbranch_scc0 .Lfk_xn0\n"
".Lfk_xn0_x:\n"
"s_nop 1\n"
"s_branch .Lfk_xn0_ok\n"
".Lfk_xn1_s:\n"
"s_mov_b32 s84,0x40000\n"
"buffer_load_dwordx4 v[74:77],v112,s[24:27],s58 offen sc1\n"
"s_waitcnt vmcnt(0)\n"
".Lfk_xn1:\n"
"v_xor_b32 v116,s80,v74\n"
"v_xor_b32 v117,s81,v75\n"
"v_or_b32 v116,v116,v117\n"
"v_xor_b32 v117,s80,v76\n"
"v_or_b32 v116,v116,v117\n"
"v_xor_b32 v117,s81,v77\n"
"v_or_b32 v116,v116,v117\n"
"v_cmp_ne_u32 vcc,0,v116\n"
"s_nop 0\n"
"s_cbranch_vccz .Lfk_xn1_x\n"
"s_sleep 4\n"
"s_mov_b64 exec,vcc\n"
"buffer_load_dwordx4 v[74:77],v112,s[24:27],s58 offen sc1\n"
"s_mov_b64 exec,-1\n"
"s_sub_u32 s84,s84,1\n"
"s_waitcnt vmcnt(0)\n"
"s_cbranch_scc0 .Lfk_xn1\n"
".Lfk_xn1_x:\n"
"s_nop 1\n"
"s_branch .Lfk_xn1_ok\n"
".Lfk_kv_s:\n"
"s_mov_b32 s84,0x40000\n"
"buffer_load_dwordx2 v[118:119],v120,s[24:27],s63 offen sc1\n"
"s_waitcnt vmcnt(0)\n"
".Lfk_kv:\n"
"v_xor_b32 v116,s80,v118\n"
"v_xor_b32 v117,s81,v119\n"
"v_or_b32 v116,v116,v117\n"
"v_cmp_ne_u32 vcc,0,v116\n"
"s_nop 0\n"
"s_cbranch_vccz .Lfk_kv_x\n"
"s_sleep 4\n"
"s_mov_b64 exec,vcc\n"
"buffer_load_dwordx2 v[118:119],v120,s[24:27],s63 offen sc1\n"
"s_mov_b64 exec,-1\n"
"s_sub_u32 s84,s84,1\n"
"s_waitcnt vmcnt(0)\n"
"s_cbranch_scc0 .Lfk_kv\n"
".Lfk_kv_x:\n"
"s_nop 1\n"
"s_branch .Lfk_kv_ok\n"
".Lfk_cs_h3:\n"
"s_mov_b32 s91,15\n"
"s_mov_b32 s90,15\n"
"s_call_b64 s[94:95],.Lfk_rare0\n"
"s_branch .Lfk_cr_h3\n"
".Lfk_cs_h1:\n"
"s_mov_b32 s91,15\n"
"s_call_b64 s[94:95],.Lfk_rare3\n"
"s_branch .Lfk_cr_h1\n"
".Lfk_cs_h2:\n"
"s_mov_b32 s91,15\n"
"s_call_b64 s[94:95],.Lfk_rare2\n"
"s_branch .Lfk_cr_h2\n"
".Lfk_cs_x1:\n"
"s_mov_b32 s91,15\n"
"s_call_b64 s[94:95],.Lfk_rare1\n"
"s_branch .Lfk_cr_x1\n"
".Lfk_cs_a3:\n"
"s_mov_b32 s91,14\n"
"s_call_b64 s[94:95],.Lfk_rare0\n"
"s_branch .Lfk_cr_a3\n"
".Lfk_cs_a0:\n"
"s_mov_b32 s91,12\n"
"s_call_b64 s[94:95],.Lfk_rare1\n"
"s_branch .Lfk_cr_a0\n"
".Lfk_cs_a1:\n"
"s_mov_b32 s91,8\n"
"s_call_b64 s[94:95],.Lfk_rare0\n"
"s_branch .Lfk_cr_a1\n"
".Lfk_ms_a0:\n"
"s_call_b64 s[94:95],.Lfk_mslow0\n"
"s_branch .Lfk_mr_a0\n"
".Lfk_ms_a1:\n"
"s_call_b64 s[94:95],.Lfk_mslow1\n"
"s_branch .Lfk_mr_a1\n"
".Lfk_p2:\n"
"s_nop 7\n"
"s_mov_b32 s84,0x40000\n"
"buffer_load_dwordx2 v[122:123],v115,s[24:27],s11 offen sc1\n"
"s_waitcnt vmcnt(0)\n"
".Lfk_pj:\n"
"v_xor_b32 v119,s80,v122\n"
"v_xor_b32 v120,s81,v123\n"
"v_or_b32 v119,v119,v120\n"
"v_cmp_ne_u32 s[14:15],0,v119\n"
"s_nop 0\n"
"s_mov_b32 vcc_lo,s14\n"
"s_mov_b32 vcc_hi,0\n"
"s_cbranch_vccz .Lfk_pj_x\n"
"s_sleep 4\n"
"s_mov_b64 exec,vcc\n"
"buffer_load_dwordx2 v[122:123],v115,s[24:27],s11 offen sc1\n"
"s_mov_b64 exec,-1\n"
"s_sub_u32 s84,s84,1\n"
"s_waitcnt vmcnt(0)\n"
"s_cbranch_scc0 .Lfk_pj\n"
".Lfk_pj_x:\n"
"s_cmp_ge_u32 s52,2\n"
"s_cselect_b32 s18,1032192,0\n"
"s_add_u32 s18,s18,s8\n"
"s_add_u32 m0,s30,0\n"
"s_nop 0\n"
"buffer_load_dwordx4 v112,s[24:27],s18 offen offset:0 sc1 lds\n"
"buffer_load_dwordx4 v112,s[24:27],s18 offen offset:1024 sc1 lds\n"
"buffer_load_dwordx4 v112,s[24:27],s18 offen offset:2048 sc1 lds\n"
"buffer_load_dwordx4 v112,s[24:27],s18 offen offset:3072 sc1 lds\n"
"s_add_u32 m0,m0,4096\n"
"s_nop 0\n"
"buffer_load_dwordx4 v117,s[24:27],s18 offen offset:0 sc1 lds\n"
"buffer_load_dwordx4 v117,s[24:27],s18 offen offset:1024 sc1 lds\n"
"buffer_load_dwordx4 v117,s[24:27],s18 offen offset:2048 sc1 lds\n"
"buffer_load_dwordx4 v117,s[24:27],s18 offen offset:3072 sc1 lds\n"
"s_add_u32 s83,s18,2097152\n"
"s_add_u32 m0,s30,32768\n"
"s_nop 0\n"
"buffer_load_dwordx4 v112,s[24:27],s83 offen offset:0 sc1 lds\n"
"buffer_load_dwordx4 v112,s[24:27],s83 offen offset:1024 sc1 lds\n"
"buffer_load_dwordx4 v112,s[24:27],s83 offen offset:2048 sc1 lds\n"
"buffer_load_dwordx4 v112,s[24:27],s83 offen offset:3072 sc1 lds\n"
"s_add_u32 m0,m0,4096\n"
"s_nop 0\n"
"buffer_load_dwordx4 v117,s[24:27],s83 offen offset:0 sc1 lds\n"
"buffer_load_dwordx4 v117,s[24:27],s83 offen offset:1024 sc1 lds\n"
"buffer_load_dwordx4 v117,s[24:27],s83 offen offset:2048 sc1 lds\n"
"buffer_load_dwordx4 v117,s[24:27],s83 offen offset:3072 sc1 lds\n"
"s_waitcnt vmcnt(0)\n"
"s_nop 1\n"
"s_branch .Lfk_p3\n"
".Lfk_cs_k1:\n"
"s_mov_b32 s91,15\n"
"s_call_b64 s[94:95],.Lfk_rare3\n"
"s_branch .Lfk_cr_k1\n"
".Lfk_cs_k2:\n"
"s_mov_b32 s91,15\n"
"s_call_b64 s[94:95],.Lfk_rare2\n"
"s_branch .Lfk_cr_k2\n"
".Lfk_cs_x2:\n"
"s_mov_b32 s91,15\n"
"s_call_b64 s[94:95],.Lfk_rare1\n"
"s_branch .Lfk_cr_x2\n"
".Lfk_cs_u3:\n"
"s_mov_b32 s91,15\n"
"s_call_b64 s[94:95],.Lfk_rare0\n"
"s_branch .Lfk_cr_u3\n"
".Lfk_cs_mu3:\n"
"s_call_b64 s[94:95],.Lfk_meet3\n"
"s_branch .Lfk_cr_mu3\n"
".Lfk_cs_u0:\n"
"s_mov_b32 s91,14\n"
"s_call_b64 s[94:95],.Lfk_rare1\n"
"s_branch .Lfk_cr_u0\n"
".Lfk_cs_mu0:\n"
"s_call_b64 s[94:95],.Lfk_meet3\n"
"s_branch .Lfk_cr_mu0\n"
".Lfk_cs_u1:\n"
"s_mov_b32 s91,12\n"
"s_call_b64 s[94:95],.Lfk_rare0\n"
"s_branch .Lfk_cr_u1\n"
".Lfk_cs_mu1:\n"
"s_call_b64 s[94:95],.Lfk_meet3\n"
"s_branch .Lfk_cr_mu1\n"
".Lfk_cs_u2:\n"
"s_mov_b32 s91,8\n"
"s_call_b64 s[94:95],.Lfk_rare1\n"
"s_branch .Lfk_cr_u2\n"
".Lfk_cs_mu2:\n"
"s_call_b64 s[94:95],.Lfk_meet3\n"
"s_branch .Lfk_cr_mu2\n"
".Lfk_cs_v3:\n"
"s_mov_b32 s91,15\n"
"s_mov_b32 s90,15\n"
"s_call_b64 s[94:95],.Lfk_rare0\n"
"s_branch .Lfk_cr_v3\n"
".Lfk_cs_mv3:\n"
"s_call_b64 s[94:95],.Lfk_meet3\n"
"s_branch .Lfk_cr_mv3\n"
".Lfk_cs_mv0:\n"
"s_call_b64 s[94:95],.Lfk_meet3\n"
"s_branch .Lfk_cr_mv0\n"
".Lfk_cs_v1:\n"
"s_call_b64 s[94:95],.Lfk_meet3\n"
"v_or_b32 v118,v118,v95\n"
"v_and_b32 v118,0x40004000,v118\n"
"v_cmp_ne_u32 vcc,0,v118\n"
"s_nop 0\n"
"s_cbranch_vccz .Lfk_cr_v1\n"
"s_mov_b32 s91,15\n"
"s_call_b64 s[94:95],.Lfk_rare3\n"
"s_branch .Lfk_cr_v1\n"
".Lfk_cs_v2:\n"
"s_call_b64 s[94:95],.Lfk_meet3\n"
"v_or_b32 v118,v118,v79\n"
"v_and_b32 v118,0x40004000,v118\n"
"v_cmp_ne_u32 vcc,0,v118\n"
"s_nop 0\n"
"s_cbranch_vccz .Lfk_cr_v2\n"
"s_mov_b32 s91,15\n"
"s_call_b64 s[94:95],.Lfk_rare2\n"
"s_branch .Lfk_cr_v2\n"
".Lfk_cs_b3:\n"
"s_mov_b32 s91,15\n"
"s_call_b64 s[94:95],.Lfk_rare0\n"
"s_branch .Lfk_cr_b3\n"
".Lfk_ms_b0:\n"
"s_call_b64 s[94:95],.Lfk_mslow0\n"
"s_branch .Lfk_mr_b0\n"
".Lfk_ms_b1:\n"
"s_call_b64 s[94:95],.Lfk_mslow1\n"
"s_branch .Lfk_mr_b1\n"
".Lfk_cs_c3:\n"
"s_mov_b32 s91,15\n"
"s_mov_b32 s90,15\n"
"s_call_b64 s[94:95],.Lfk_rare0\n"
"s_branch .Lfk_cr_c3\n"
".Lfk_cs_z3:\n"
"s_mov_b32 s91,15\n"
"s_mov_b32 s90,15\n"
"s_call_b64 s[94:95],.Lfk_rare0\n"
"s_branch .Lfk_cr_z3\n"
".Lfk_cs_z0:\n"
"s_mov_b32 s91,14\n"
"s_call_b64 s[94:95],.Lfk_rare1\n"
"s_branch .Lfk_cr_z0\n"
".Lfk_cs_z1:\n"
"s_mov_b32 s91,12\n"
"s_call_b64 s[94:95],.Lfk_rare0\n"
"s_branch .Lfk_cr_z1\n"
".Lfk_cs_z2:\n"
"s_mov_b32 s91,8\n"
"s_call_b64 s[94:95],.Lfk_rare1\n"
"s_branch .Lfk_cr_z2\n"
".Lfk_cold:\n"
".Lfk_rare0:\n"
"s_nop 7\n"
"s_bitcmp1_b32 s91,0\n"
"s_cbranch_scc0 .Lfk_rare0_0\n"
"v_max3_f32 v119,v0,v1,v2\n"
"v_max3_f32 v120,v3,v4,v5\n"
"v_max3_f32 v119,v119,v6,v7\n"
"v_max_f32 v119,v119,v120\n"
"v_mov_b32 v120,v119\n"
"s_nop 1\n"
"v_permlane16_swap_b32 v120,v119\n"
"v_max_f32 v119,v119,v120\n"
"v_mov_b32 v120,v119\n"
"s_nop 1\n"
"v_permlane32_swap_b32 v120,v119\n"
"v_max_f32 v119,v119,v120\n"
"s_bitcmp1_b32 s90,0\n"
"s_cselect_b32 s82,0xff800000,0\n"
"v_add_f32 v119,0x41980000,v119\n"
"v_max_f32 v119,s82,v119\n"
"v_cmp_lt_f32 vcc,s17,|v119|\n"
"s_nop 0\n"
"s_cbranch_vccz .Lfk_rare0_0\n"
"v_ceil_f32 v119,v119\n"
"v_sub_f32 v0,v0,v119\n"
"v_sub_f32 v1,v1,v119\n"
"v_sub_f32 v2,v2,v119\n"
"v_sub_f32 v3,v3,v119\n"
"v_sub_f32 v4,v4,v119\n"
"v_sub_f32 v5,v5,v119\n"
"v_sub_f32 v6,v6,v119\n"
"v_sub_f32 v7,v7,v119\n"
"v_sub_f32 v96,v96,v119\n"
"v_sub_f32 v97,v97,v119\n"
"v_sub_f32 v98,v98,v119\n"
"v_sub_f32 v99,v99,v119\n"
"s_cbranch_scc1 .Lfk_rare0_0\n"
"v_exp_f32 v119,-v119\n"
"s_nop 0\n"
"v_mul_f32 v128,v128,v119\n"
"v_mul_f32 v129,v129,v119\n"
"v_mul_f32 v130,v130,v119\n"
"v_mul_f32 v131,v131,v119\n"
"v_mul_f32 v132,v132,v119\n"
"v_mul_f32 v133,v133,v119\n"
"v_mul_f32 v134,v134,v119\n"
"v_mul_f32 v135,v135,v119\n"
"v_mul_f32 v136,v136,v119\n"
"v_mul_f32 v137,v137,v119\n"
"v_mul_f32 v138,v138,v119\n"
"v_mul_f32 v139,v139,v119\n"
"v_mul_f32 v140,v140,v119\n"
"v_mul_f32 v141,v141,v119\n"
"v_mul_f32 v142,v142,v119\n"
"v_mul_f32 v143,v143,v119\n"
"v_mul_f32 v144,v144,v119\n"
"v_mul_f32 v145,v145,v119\n"
"v_mul_f32 v146,v146,v119\n"
"v_mul_f32 v147,v147,v119\n"
"v_mul_f32 v148,v148,v119\n"
"v_mul_f32 v149,v149,v119\n"
"v_mul_f32 v150,v150,v119\n"
"v_mul_f32 v151,v151,v119\n"
"v_mul_f32 v152,v152,v119\n"
"v_mul_f32 v153,v153,v119\n"
"v_mul_f32 v154,v154,v119\n"
"v_mul_f32 v155,v155,v119\n"
"v_mul_f32 v156,v156,v119\n"
"v_mul_f32 v157,v157,v119\n"
"v_mul_f32 v158,v158,v119\n"
"v_mul_f32 v159,v159,v119\n"
"v_accvgpr_read_b32 v120,a128\n"
"v_mul_f32 v120,v120,v119\n"
"v_accvgpr_write_b32 a128,v120\n"
"v_lshlrev_b32 v120,16,v80\n"
"v_and_b32 v121,0xffff0000,v80\n"
"v_mul_f32 v120,v120,v119\n"
"v_mul_f32 v121,v121,v119\n"
"v_cvt_pk_bf16_f32 v80,v120,v121\n"
"v_accvgpr_read_b32 v120,a129\n"
"v_mul_f32 v120,v120,v119\n"
"v_accvgpr_write_b32 a129,v120\n"
"v_lshlrev_b32 v120,16,v81\n"
"v_and_b32 v121,0xffff0000,v81\n"
"v_mul_f32 v120,v120,v119\n"
"v_mul_f32 v121,v121,v119\n"
"v_cvt_pk_bf16_f32 v81,v120,v121\n"
"v_accvgpr_read_b32 v120,a130\n"
"v_mul_f32 v120,v120,v119\n"
"v_accvgpr_write_b32 a130,v120\n"
"v_lshlrev_b32 v120,16,v82\n"
"v_and_b32 v121,0xffff0000,v82\n"
"v_mul_f32 v120,v120,v119\n"
"v_mul_f32 v121,v121,v119\n"
"v_cvt_pk_bf16_f32 v82,v120,v121\n"
"v_accvgpr_read_b32 v120,a131\n"
"v_mul_f32 v120,v120,v119\n"
"v_accvgpr_write_b32 a131,v120\n"
"v_lshlrev_b32 v120,16,v83\n"
"v_and_b32 v121,0xffff0000,v83\n"
"v_mul_f32 v120,v120,v119\n"
"v_mul_f32 v121,v121,v119\n"
"v_cvt_pk_bf16_f32 v83,v120,v121\n"
".Lfk_rare0_0:\n"
"s_bitcmp1_b32 s91,1\n"
"s_cbranch_scc0 .Lfk_rare0_1\n"
"v_max3_f32 v119,v8,v9,v10\n"
"v_max3_f32 v120,v11,v12,v13\n"
"v_max3_f32 v119,v119,v14,v15\n"
"v_max_f32 v119,v119,v120\n"
"v_mov_b32 v120,v119\n"
"s_nop 1\n"
"v_permlane16_swap_b32 v120,v119\n"
"v_max_f32 v119,v119,v120\n"
"v_mov_b32 v120,v119\n"
"s_nop 1\n"
"v_permlane32_swap_b32 v120,v119\n"
"v_max_f32 v119,v119,v120\n"
"s_bitcmp1_b32 s90,1\n"
"s_cselect_b32 s82,0xff800000,0\n"
"v_add_f32 v119,0x41980000,v119\n"
"v_max_f32 v119,s82,v119\n"
"v_cmp_lt_f32 vcc,s17,|v119|\n"
"s_nop 0\n"
"s_cbranch_vccz .Lfk_rare0_1\n"
"v_ceil_f32 v119,v119\n"
"v_sub_f32 v8,v8,v119\n"
"v_sub_f32 v9,v9,v119\n"
"v_sub_f32 v10,v10,v119\n"
"v_sub_f32 v11,v11,v119\n"
"v_sub_f32 v12,v12,v119\n"
"v_sub_f32 v13,v13,v119\n"
"v_sub_f32 v14,v14,v119\n"
"v_sub_f32 v15,v15,v119\n"
"v_sub_f32 v100,v100,v119\n"
"v_sub_f32 v101,v101,v119\n"
"v_sub_f32 v102,v102,v119\n"
"v_sub_f32 v103,v103,v119\n"
"s_cbranch_scc1 .Lfk_rare0_1\n"
"v_exp_f32 v119,-v119\n"
"s_nop 0\n"
"v_mul_f32 v160,v160,v119\n"
"v_mul_f32 v161,v161,v119\n"
"v_mul_f32 v162,v162,v119\n"
"v_mul_f32 v163,v163,v119\n"
"v_mul_f32 v164,v164,v119\n"
"v_mul_f32 v165,v165,v119\n"
"v_mul_f32 v166,v166,v119\n"
"v_mul_f32 v167,v167,v119\n"
"v_mul_f32 v168,v168,v119\n"
"v_mul_f32 v169,v169,v119\n"
"v_mul_f32 v170,v170,v119\n"
"v_mul_f32 v171,v171,v119\n"
"v_mul_f32 v172,v172,v119\n"
"v_mul_f32 v173,v173,v119\n"
"v_mul_f32 v174,v174,v119\n"
"v_mul_f32 v175,v175,v119\n"
"v_mul_f32 v176,v176,v119\n"
"v_mul_f32 v177,v177,v119\n"
"v_mul_f32 v178,v178,v119\n"
"v_mul_f32 v179,v179,v119\n"
"v_mul_f32 v180,v180,v119\n"
"v_mul_f32 v181,v181,v119\n"
"v_mul_f32 v182,v182,v119\n"
"v_mul_f32 v183,v183,v119\n"
"v_mul_f32 v184,v184,v119\n"
"v_mul_f32 v185,v185,v119\n"
"v_mul_f32 v186,v186,v119\n"
"v_mul_f32 v187,v187,v119\n"
"v_mul_f32 v188,v188,v119\n"
"v_mul_f32 v189,v189,v119\n"
"v_mul_f32 v190,v190,v119\n"
"v_mul_f32 v191,v191,v119\n"
"v_accvgpr_read_b32 v120,a132\n"
"v_mul_f32 v120,v120,v119\n"
"v_accvgpr_write_b32 a132,v120\n"
"v_lshlrev_b32 v120,16,v84\n"
"v_and_b32 v121,0xffff0000,v84\n"
"v_mul_f32 v120,v120,v119\n"
"v_mul_f32 v121,v121,v119\n"
"v_cvt_pk_bf16_f32 v84,v120,v121\n"
"v_accvgpr_read_b32 v120,a133\n"
"v_mul_f32 v120,v120,v119\n"
"v_accvgpr_write_b32 a133,v120\n"
"v_lshlrev_b32 v120,16,v85\n"
"v_and_b32 v121,0xffff0000,v85\n"
"v_mul_f32 v120,v120,v119\n"
"v_mul_f32 v121,v121,v119\n"
"v_cvt_pk_bf16_f32 v85,v120,v121\n"
"v_accvgpr_read_b32 v120,a134\n"
"v_mul_f32 v120,v120,v119\n"
"v_accvgpr_write_b32 a134,v120\n"
"v_lshlrev_b32 v120,16,v86\n"
"v_and_b32 v121,0xffff0000,v86\n"
"v_mul_f32 v120,v120,v119\n"
"v_mul_f32 v121,v121,v119\n"
"v_cvt_pk_bf16_f32 v86,v120,v121\n"
"v_accvgpr_read_b32 v120,a135\n"
"v_mul_f32 v120,v120,v119\n"
"v_accvgpr_write_b32 a135,v120\n"
"v_lshlrev_b32 v120,16,v87\n"
"v_and_b32 v121,0xffff0000,v87\n"
"v_mul_f32 v120,v120,v119\n"
"v_mul_f32 v121,v121,v119\n"
"v_cvt_pk_bf16_f32 v87,v120,v121\n"
".Lfk_rare0_1:\n"
"s_bitcmp1_b32 s91,2\n"
"s_cbranch_scc0 .Lfk_rare0_2\n"
"v_max3_f32 v119,v16,v17,v18\n"
"v_max3_f32 v120,v19,v20,v21\n"
"v_max3_f32 v119,v119,v22,v23\n"
"v_max_f32 v119,v119,v120\n"
"v_mov_b32 v120,v119\n"
"s_nop 1\n"
"v_permlane16_swap_b32 v120,v119\n"
"v_max_f32 v119,v119,v120\n"
"v_mov_b32 v120,v119\n"
"s_nop 1\n"
"v_permlane32_swap_b32 v120,v119\n"
"v_max_f32 v119,v119,v120\n"
"s_bitcmp1_b32 s90,2\n"
"s_cselect_b32 s82,0xff800000,0\n"
"v_add_f32 v119,0x41980000,v119\n"
"v_max_f32 v119,s82,v119\n"
"v_cmp_lt_f32 vcc,s17,|v119|\n"
"s_nop 0\n"
"s_cbranch_vccz .Lfk_rare0_2\n"
"v_ceil_f32 v119,v119\n"
"v_sub_f32 v16,v16,v119\n"
"v_sub_f32 v17,v17,v119\n"
"v_sub_f32 v18,v18,v119\n"
"v_sub_f32 v19,v19,v119\n"
"v_sub_f32 v20,v20,v119\n"
"v_sub_f32 v21,v21,v119\n"
"v_sub_f32 v22,v22,v119\n"
"v_sub_f32 v23,v23,v119\n"
"v_sub_f32 v104,v104,v119\n"
"v_sub_f32 v105,v105,v119\n"
"v_sub_f32 v106,v106,v119\n"
"v_sub_f32 v107,v107,v119\n"
"s_cbranch_scc1 .Lfk_rare0_2\n"
"v_exp_f32 v119,-v119\n"
"s_nop 0\n"
"v_mul_f32 v192,v192,v119\n"
"v_mul_f32 v193,v193,v119\n"
"v_mul_f32 v194,v194,v119\n"
"v_mul_f32 v195,v195,v119\n"
"v_mul_f32 v196,v196,v119\n"
"v_mul_f32 v197,v197,v119\n"
"v_mul_f32 v198,v198,v119\n"
"v_mul_f32 v199,v199,v119\n"
"v_mul_f32 v200,v200,v119\n"
"v_mul_f32 v201,v201,v119\n"
"v_mul_f32 v202,v202,v119\n"
"v_mul_f32 v203,v203,v119\n"
"v_mul_f32 v204,v204,v119\n"
"v_mul_f32 v205,v205,v119\n"
"v_mul_f32 v206,v206,v119\n"
"v_mul_f32 v207,v207,v119\n"
"v_mul_f32 v208,v208,v119\n"
"v_mul_f32 v209,v209,v119\n"
"v_mul_f32 v210,v210,v119\n"
"v_mul_f32 v211,v211,v119\n"
"v_mul_f32 v212,v212,v119\n"
"v_mul_f32 v213,v213,v119\n"
"v_mul_f32 v214,v214,v119\n"
"v_mul_f32 v215,v215,v119\n"
"v_mul_f32 v216,v216,v119\n"
"v_mul_f32 v217,v217,v119\n"
"v_mul_f32 v218,v218,v119\n"
"v_mul_f32 v219,v219,v119\n"
"v_mul_f32 v220,v220,v119\n"
"v_mul_f32 v221,v221,v119\n"
"v_mul_f32 v222,v222,v119\n"
"v_mul_f32 v223,v223,v119\n"
"v_accvgpr_read_b32 v120,a136\n"
"v_mul_f32 v120,v120,v119\n"
"v_accvgpr_write_b32 a136,v120\n"
"v_lshlrev_b32 v120,16,v88\n"
"v_and_b32 v121,0xffff0000,v88\n"
"v_mul_f32 v120,v120,v119\n"
"v_mul_f32 v121,v121,v119\n"
"v_cvt_pk_bf16_f32 v88,v120,v121\n"
"v_accvgpr_read_b32 v120,a137\n"
"v_mul_f32 v120,v120,v119\n"
"v_accvgpr_write_b32 a137,v120\n"
"v_lshlrev_b32 v120,16,v89\n"
"v_and_b32 v121,0xffff0000,v89\n"
"v_mul_f32 v120,v120,v119\n"
"v_mul_f32 v121,v121,v119\n"
"v_cvt_pk_bf16_f32 v89,v120,v121\n"
"v_accvgpr_read_b32 v120,a138\n"
"v_mul_f32 v120,v120,v119\n"
"v_accvgpr_write_b32 a138,v120\n"
"v_lshlrev_b32 v120,16,v90\n"
"v_and_b32 v121,0xffff0000,v90\n"
"v_mul_f32 v120,v120,v119\n"
"v_mul_f32 v121,v121,v119\n"
"v_cvt_pk_bf16_f32 v90,v120,v121\n"
"v_accvgpr_read_b32 v120,a139\n"
"v_mul_f32 v120,v120,v119\n"
"v_accvgpr_write_b32 a139,v120\n"
"v_lshlrev_b32 v120,16,v91\n"
"v_and_b32 v121,0xffff0000,v91\n"
"v_mul_f32 v120,v120,v119\n"
"v_mul_f32 v121,v121,v119\n"
"v_cvt_pk_bf16_f32 v91,v120,v121\n"
".Lfk_rare0_2:\n"
"s_bitcmp1_b32 s91,3\n"
"s_cbranch_scc0 .Lfk_rare0_3\n"
"v_max3_f32 v119,v24,v25,v26\n"
"v_max3_f32 v120,v27,v28,v29\n"
"v_max3_f32 v119,v119,v30,v31\n"
"v_max_f32 v119,v119,v120\n"
"v_mov_b32 v120,v119\n"
"s_nop 1\n"
"v_permlane16_swap_b32 v120,v119\n"
"v_max_f32 v119,v119,v120\n"
"v_mov_b32 v120,v119\n"
"s_nop 1\n"
"v_permlane32_swap_b32 v120,v119\n"
"v_max_f32 v119,v119,v120\n"
"s_bitcmp1_b32 s90,3\n"
"s_cselect_b32 s82,0xff800000,0\n"
"v_add_f32 v119,0x41980000,v119\n"
"v_max_f32 v119,s82,v119\n"
"v_cmp_lt_f32 vcc,s17,|v119|\n"
"s_nop 0\n"
"s_cbranch_vccz .Lfk_rare0_3\n"
"v_ceil_f32 v119,v119\n"
"v_sub_f32 v24,v24,v119\n"
"v_sub_f32 v25,v25,v119\n"
"v_sub_f32 v26,v26,v119\n"
"v_sub_f32 v27,v27,v119\n"
"v_sub_f32 v28,v28,v119\n"
"v_sub_f32 v29,v29,v119\n"
"v_sub_f32 v30,v30,v119\n"
"v_sub_f32 v31,v31,v119\n"
"v_sub_f32 v108,v108,v119\n"
"v_sub_f32 v109,v109,v119\n"
"v_sub_f32 v110,v110,v119\n"
"v_sub_f32 v111,v111,v119\n"
"s_cbranch_scc1 .Lfk_rare0_3\n"
"v_exp_f32 v119,-v119\n"
"s_nop 0\n"
"v_mul_f32 v224,v224,v119\n"
"v_mul_f32 v225,v225,v119\n"
"v_mul_f32 v226,v226,v119\n"
"v_mul_f32 v227,v227,v119\n"
"v_mul_f32 v228,v228,v119\n"
"v_mul_f32 v229,v229,v119\n"
"v_mul_f32 v230,v230,v119\n"
"v_mul_f32 v231,v231,v119\n"
"v_mul_f32 v232,v232,v119\n"
"v_mul_f32 v233,v233,v119\n"
"v_mul_f32 v234,v234,v119\n"
"v_mul_f32 v235,v235,v119\n"
"v_mul_f32 v236,v236,v119\n"
"v_mul_f32 v237,v237,v119\n"
"v_mul_f32 v238,v238,v119\n"
"v_mul_f32 v239,v239,v119\n"
"v_mul_f32 v240,v240,v119\n"
"v_mul_f32 v241,v241,v119\n"
"v_mul_f32 v242,v242,v119\n"
"v_mul_f32 v243,v243,v119\n"
"v_mul_f32 v244,v244,v119\n"
"v_mul_f32 v245,v245,v119\n"
"v_mul_f32 v246,v246,v119\n"
"v_mul_f32 v247,v247,v119\n"
"v_mul_f32 v248,v248,v119\n"
"v_mul_f32 v249,v249,v119\n"
"v_mul_f32 v250,v250,v119\n"
"v_mul_f32 v251,v251,v119\n"
"v_mul_f32 v252,v252,v119\n"
"v_mul_f32 v253,v253,v119\n"
"v_mul_f32 v254,v254,v119\n"
"v_mul_f32 v255,v255,v119\n"
"v_accvgpr_read_b32 v120,a140\n"
"v_mul_f32 v120,v120,v119\n"
"v_accvgpr_write_b32 a140,v120\n"
"v_lshlrev_b32 v120,16,v92\n"
"v_and_b32 v121,0xffff0000,v92\n"
"v_mul_f32 v120,v120,v119\n"
"v_mul_f32 v121,v121,v119\n"
"v_cvt_pk_bf16_f32 v92,v120,v121\n"
"v_accvgpr_read_b32 v120,a141\n"
"v_mul_f32 v120,v120,v119\n"
"v_accvgpr_write_b32 a141,v120\n"
"v_lshlrev_b32 v120,16,v93\n"
"v_and_b32 v121,0xffff0000,v93\n"
"v_mul_f32 v120,v120,v119\n"
"v_mul_f32 v121,v121,v119\n"
"v_cvt_pk_bf16_f32 v93,v120,v121\n"
"v_accvgpr_read_b32 v120,a142\n"
"v_mul_f32 v120,v120,v119\n"
"v_accvgpr_write_b32 a142,v120\n"
"v_lshlrev_b32 v120,16,v94\n"
"v_and_b32 v121,0xffff0000,v94\n"
"v_mul_f32 v120,v120,v119\n"
"v_mul_f32 v121,v121,v119\n"
"v_cvt_pk_bf16_f32 v94,v120,v121\n"
"v_accvgpr_read_b32 v120,a143\n"
"v_mul_f32 v120,v120,v119\n"
"v_accvgpr_write_b32 a143,v120\n"
"v_lshlrev_b32 v120,16,v95\n"
"v_and_b32 v121,0xffff0000,v95\n"
"v_mul_f32 v120,v120,v119\n"
"v_mul_f32 v121,v121,v119\n"
"v_cvt_pk_bf16_f32 v95,v120,v121\n"
".Lfk_rare0_3:\n"
"s_mov_b32 s90,0\n"
"s_nop 1\n"
"s_setpc_b64 s[94:95]\n"
".Lfk_rare2:\n"
"s_nop 7\n"
"v_max3_f32 v119,v0,v1,v2\n"
"v_max3_f32 v120,v3,v4,v5\n"
"v_max3_f32 v119,v119,v6,v7\n"
"v_max_f32 v119,v119,v120\n"
"v_mov_b32 v120,v119\n"
"s_nop 1\n"
"v_permlane16_swap_b32 v120,v119\n"
"v_max_f32 v119,v119,v120\n"
"v_mov_b32 v120,v119\n"
"s_nop 1\n"
"v_permlane32_swap_b32 v120,v119\n"
"v_max_f32 v119,v119,v120\n"
"s_bitcmp1_b32 s90,0\n"
"s_cselect_b32 s82,0xff800000,0\n"
"v_add_f32 v119,0x41980000,v119\n"
"v_max_f32 v119,s82,v119\n"
"v_cmp_lt_f32 vcc,s17,|v119|\n"
"s_nop 0\n"
"s_cbranch_vccz .Lfk_rare2_0\n"
"v_ceil_f32 v119,v119\n"
"v_sub_f32 v0,v0,v119\n"
"v_sub_f32 v1,v1,v119\n"
"v_sub_f32 v2,v2,v119\n"
"v_sub_f32 v3,v3,v119\n"
"v_sub_f32 v4,v4,v119\n"
"v_sub_f32 v5,v5,v119\n"
"v_sub_f32 v6,v6,v119\n"
"v_sub_f32 v7,v7,v119\n"
"v_sub_f32 v96,v96,v119\n"
"v_sub_f32 v97,v97,v119\n"
"v_sub_f32 v98,v98,v119\n"
"v_sub_f32 v99,v99,v119\n"
"v_sub_f32 v32,v32,v119\n"
"v_sub_f32 v33,v33,v119\n"
"v_sub_f32 v34,v34,v119\n"
"v_sub_f32 v35,v35,v119\n"
"v_sub_f32 v36,v36,v119\n"
"v_sub_f32 v37,v37,v119\n"
"v_sub_f32 v38,v38,v119\n"
"v_sub_f32 v39,v39,v119\n"
"v_exp_f32 v0,v0\n"
"v_exp_f32 v1,v1\n"
"v_exp_f32 v2,v2\n"
"v_exp_f32 v3,v3\n"
"v_exp_f32 v4,v4\n"
"v_exp_f32 v5,v5\n"
"v_exp_f32 v6,v6\n"
"v_exp_f32 v7,v7\n"
"v_cvt_pk_bf16_f32 v64,v0,v1\n"
"v_cvt_pk_bf16_f32 v65,v2,v3\n"
"v_cvt_pk_bf16_f32 v66,v4,v5\n"
"v_cvt_pk_bf16_f32 v67,v6,v7\n"
"s_cbranch_scc1 .Lfk_rare2_0\n"
"v_exp_f32 v119,-v119\n"
"s_nop 0\n"
"v_mul_f32 v128,v128,v119\n"
"v_mul_f32 v129,v129,v119\n"
"v_mul_f32 v130,v130,v119\n"
"v_mul_f32 v131,v131,v119\n"
"v_mul_f32 v132,v132,v119\n"
"v_mul_f32 v133,v133,v119\n"
"v_mul_f32 v134,v134,v119\n"
"v_mul_f32 v135,v135,v119\n"
"v_mul_f32 v136,v136,v119\n"
"v_mul_f32 v137,v137,v119\n"
"v_mul_f32 v138,v138,v119\n"
"v_mul_f32 v139,v139,v119\n"
"v_mul_f32 v140,v140,v119\n"
"v_mul_f32 v141,v141,v119\n"
"v_mul_f32 v142,v142,v119\n"
"v_mul_f32 v143,v143,v119\n"
"v_mul_f32 v144,v144,v119\n"
"v_mul_f32 v145,v145,v119\n"
"v_mul_f32 v146,v146,v119\n"
"v_mul_f32 v147,v147,v119\n"
"v_mul_f32 v148,v148,v119\n"
"v_mul_f32 v149,v149,v119\n"
"v_mul_f32 v150,v150,v119\n"
"v_mul_f32 v151,v151,v119\n"
"v_mul_f32 v152,v152,v119\n"
"v_mul_f32 v153,v153,v119\n"
"v_mul_f32 v154,v154,v119\n"
"v_mul_f32 v155,v155,v119\n"
"v_mul_f32 v156,v156,v119\n"
"v_mul_f32 v157,v157,v119\n"
"v_mul_f32 v158,v158,v119\n"
"v_mul_f32 v159,v159,v119\n"
"v_accvgpr_read_b32 v120,a128\n"
"v_mul_f32 v120,v120,v119\n"
"v_accvgpr_write_b32 a128,v120\n"
"v_accvgpr_read_b32 v120,a129\n"
"v_mul_f32 v120,v120,v119\n"
"v_accvgpr_write_b32 a129,v120\n"
"v_accvgpr_read_b32 v120,a130\n"
"v_mul_f32 v120,v120,v119\n"
"v_accvgpr_write_b32 a130,v120\n"
"v_accvgpr_read_b32 v120,a131\n"
"v_mul_f32 v120,v120,v119\n"
"v_accvgpr_write_b32 a131,v120\n"
".Lfk_rare2_0:\n"
"v_max3_f32 v119,v8,v9,v10\n"
"v_max3_f32 v120,v11,v12,v13\n"
"v_max3_f32 v119,v119,v14,v15\n"
"v_max_f32 v119,v119,v120\n"
"v_mov_b32 v120,v119\n"
"s_nop 1\n"
"v_permlane16_swap_b32 v120,v119\n"
"v_max_f32 v119,v119,v120\n"
"v_mov_b32 v120,v119\n"
"s_nop 1\n"
"v_permlane32_swap_b32 v120,v119\n"
"v_max_f32 v119,v119,v120\n"
"s_bitcmp1_b32 s90,1\n"
"s_cselect_b32 s82,0xff800000,0\n"
"v_add_f32 v119,0x41980000,v119\n"
"v_max_f32 v119,s82,v119\n"
"v_cmp_lt_f32 vcc,s17,|v119|\n"
"s_nop 0\n"
"s_cbranch_vccz .Lfk_rare2_1\n"
"v_ceil_f32 v119,v119\n"
"v_sub_f32 v8,v8,v119\n"
"v_sub_f32 v9,v9,v119\n"
"v_sub_f32 v10,v10,v119\n"
"v_sub_f32 v11,v11,v119\n"
"v_sub_f32 v12,v12,v119\n"
"v_sub_f32 v13,v13,v119\n"
"v_sub_f32 v14,v14,v119\n"
"v_sub_f32 v15,v15,v119\n"
"v_sub_f32 v100,v100,v119\n"
"v_sub_f32 v101,v101,v119\n"
"v_sub_f32 v102,v102,v119\n"
"v_sub_f32 v103,v103,v119\n"
"v_sub_f32 v40,v40,v119\n"
"v_sub_f32 v41,v41,v119\n"
"v_sub_f32 v42,v42,v119\n"
"v_sub_f32 v43,v43,v119\n"
"v_sub_f32 v44,v44,v119\n"
"v_sub_f32 v45,v45,v119\n"
"v_sub_f32 v46,v46,v119\n"
"v_sub_f32 v47,v47,v119\n"
"v_exp_f32 v8,v8\n"
"v_exp_f32 v9,v9\n"
"v_exp_f32 v10,v10\n"
"v_exp_f32 v11,v11\n"
"v_exp_f32 v12,v12\n"
"v_exp_f32 v13,v13\n"
"v_exp_f32 v14,v14\n"
"v_exp_f32 v15,v15\n"
"v_cvt_pk_bf16_f32 v68,v8,v9\n"
"v_cvt_pk_bf16_f32 v69,v10,v11\n"
"v_cvt_pk_bf16_f32 v70,v12,v13\n"
"v_cvt_pk_bf16_f32 v71,v14,v15\n"
"s_cbranch_scc1 .Lfk_rare2_1\n"
"v_exp_f32 v119,-v119\n"
"s_nop 0\n"
"v_mul_f32 v160,v160,v119\n"
"v_mul_f32 v161,v161,v119\n"
"v_mul_f32 v162,v162,v119\n"
"v_mul_f32 v163,v163,v119\n"
"v_mul_f32 v164,v164,v119\n"
"v_mul_f32 v165,v165,v119\n"
"v_mul_f32 v166,v166,v119\n"
"v_mul_f32 v167,v167,v119\n"
"v_mul_f32 v168,v168,v119\n"
"v_mul_f32 v169,v169,v119\n"
"v_mul_f32 v170,v170,v119\n"
"v_mul_f32 v171,v171,v119\n"
"v_mul_f32 v172,v172,v119\n"
"v_mul_f32 v173,v173,v119\n"
"v_mul_f32 v174,v174,v119\n"
"v_mul_f32 v175,v175,v119\n"
"v_mul_f32 v176,v176,v119\n"
"v_mul_f32 v177,v177,v119\n"
"v_mul_f32 v178,v178,v119\n"
"v_mul_f32 v179,v179,v119\n"
"v_mul_f32 v180,v180,v119\n"
"v_mul_f32 v181,v181,v119\n"
"v_mul_f32 v182,v182,v119\n"
"v_mul_f32 v183,v183,v119\n"
"v_mul_f32 v184,v184,v119\n"
"v_mul_f32 v185,v185,v119\n"
"v_mul_f32 v186,v186,v119\n"
"v_mul_f32 v187,v187,v119\n"
"v_mul_f32 v188,v188,v119\n"
"v_mul_f32 v189,v189,v119\n"
"v_mul_f32 v190,v190,v119\n"
"v_mul_f32 v191,v191,v119\n"
"v_accvgpr_read_b32 v120,a132\n"
"v_mul_f32 v120,v120,v119\n"
"v_accvgpr_write_b32 a132,v120\n"
"v_accvgpr_read_b32 v120,a133\n"
"v_mul_f32 v120,v120,v119\n"
"v_accvgpr_write_b32 a133,v120\n"
"v_accvgpr_read_b32 v120,a134\n"
"v_mul_f32 v120,v120,v119\n"
"v_accvgpr_write_b32 a134,v120\n"
"v_accvgpr_read_b32 v120,a135\n"
"v_mul_f32 v120,v120,v119\n"
"v_accvgpr_write_b32 a135,v120\n"
".Lfk_rare2_1:\n"
"v_max3_f32 v119,v16,v17,v18\n"
"v_max3_f32 v120,v19,v20,v21\n"
"v_max3_f32 v119,v119,v22,v23\n"
"v_max_f32 v119,v119,v120\n"
"v_mov_b32 v120,v119\n"
"s_nop 1\n"
"v_permlane16_swap_b32 v120,v119\n"
"v_max_f32 v119,v119,v120\n"
"v_mov_b32 v120,v119\n"
"s_nop 1\n"
"v_permlane32_swap_b32 v120,v119\n"
"v_max_f32 v119,v119,v120\n"
"s_bitcmp1_b32 s90,2\n"
"s_cselect_b32 s82,0xff800000,0\n"
"v_add_f32 v119,0x41980000,v119\n"
"v_max_f32 v119,s82,v119\n"
"v_cmp_lt_f32 vcc,s17,|v119|\n"
"s_nop 0\n"
"s_cbranch_vccz .Lfk_rare2_2\n"
"v_ceil_f32 v119,v119\n"
"v_sub_f32 v16,v16,v119\n"
"v_sub_f32 v17,v17,v119\n"
"v_sub_f32 v18,v18,v119\n"
"v_sub_f32 v19,v19,v119\n"
"v_sub_f32 v20,v20,v119\n"
"v_sub_f32 v21,v21,v119\n"
"v_sub_f32 v22,v22,v119\n"
"v_sub_f32 v23,v23,v119\n"
"v_sub_f32 v104,v104,v119\n"
"v_sub_f32 v105,v105,v119\n"
"v_sub_f32 v106,v106,v119\n"
"v_sub_f32 v107,v107,v119\n"
"v_sub_f32 v48,v48,v119\n"
"v_sub_f32 v49,v49,v119\n"
"v_sub_f32 v50,v50,v119\n"
"v_sub_f32 v51,v51,v119\n"
"v_sub_f32 v52,v52,v119\n"
"v_sub_f32 v53,v53,v119\n"
"v_sub_f32 v54,v54,v119\n"
"v_sub_f32 v55,v55,v119\n"
"v_exp_f32 v16,v16\n"
"v_exp_f32 v17,v17\n"
"v_exp_f32 v18,v18\n"
"v_exp_f32 v19,v19\n"
"v_exp_f32 v20,v20\n"
"v_exp_f32 v21,v21\n"
"v_exp_f32 v22,v22\n"
"v_exp_f32 v23,v23\n"
"v_cvt_pk_bf16_f32 v72,v16,v17\n"
"v_cvt_pk_bf16_f32 v73,v18,v19\n"
"v_cvt_pk_bf16_f32 v74,v20,v21\n"
"v_cvt_pk_bf16_f32 v75,v22,v23\n"
"s_cbranch_scc1 .Lfk_rare2_2\n"
"v_exp_f32 v119,-v119\n"
"s_nop 0\n"
"v_mul_f32 v192,v192,v119\n"
"v_mul_f32 v193,v193,v119\n"
"v_mul_f32 v194,v194,v119\n"
"v_mul_f32 v195,v195,v119\n"
"v_mul_f32 v196,v196,v119\n"
"v_mul_f32 v197,v197,v119\n"
"v_mul_f32 v198,v198,v119\n"
"v_mul_f32 v199,v199,v119\n"
"v_mul_f32 v200,v200,v119\n"
"v_mul_f32 v201,v201,v119\n"
"v_mul_f32 v202,v202,v119\n"
"v_mul_f32 v203,v203,v119\n"
"v_mul_f32 v204,v204,v119\n"
"v_mul_f32 v205,v205,v119\n"
"v_mul_f32 v206,v206,v119\n"
"v_mul_f32 v207,v207,v119\n"
"v_mul_f32 v208,v208,v119\n"
"v_mul_f32 v209,v209,v119\n"
"v_mul_f32 v210,v210,v119\n"
"v_mul_f32 v211,v211,v119\n"
"v_mul_f32 v212,v212,v119\n"
"v_mul_f32 v213,v213,v119\n"
"v_mul_f32 v214,v214,v119\n"
"v_mul_f32 v215,v215,v119\n"
"v_mul_f32 v216,v216,v119\n"
"v_mul_f32 v217,v217,v119\n"
"v_mul_f32 v218,v218,v119\n"
"v_mul_f32 v219,v219,v119\n"
"v_mul_f32 v220,v220,v119\n"
"v_mul_f32 v221,v221,v119\n"
"v_mul_f32 v222,v222,v119\n"
"v_mul_f32 v223,v223,v119\n"
"v_accvgpr_read_b32 v120,a136\n"
"v_mul_f32 v120,v120,v119\n"
"v_accvgpr_write_b32 a136,v120\n"
"v_accvgpr_read_b32 v120,a137\n"
"v_mul_f32 v120,v120,v119\n"
"v_accvgpr_write_b32 a137,v120\n"
"v_accvgpr_read_b32 v120,a138\n"
"v_mul_f32 v120,v120,v119\n"
"v_accvgpr_write_b32 a138,v120\n"
"v_accvgpr_read_b32 v120,a139\n"
"v_mul_f32 v120,v120,v119\n"
"v_accvgpr_write_b32 a139,v120\n"
".Lfk_rare2_2:\n"
"v_max3_f32 v119,v24,v25,v26\n"
"v_max3_f32 v120,v27,v28,v29\n"
"v_max3_f32 v119,v119,v30,v31\n"
"v_max_f32 v119,v119,v120\n"
"v_mov_b32 v120,v119\n"
"s_nop 1\n"
"v_permlane16_swap_b32 v120,v119\n"
"v_max_f32 v119,v119,v120\n"
"v_mov_b32 v120,v119\n"
"s_nop 1\n"
"v_permlane32_swap_b32 v120,v119\n"
"v_max_f32 v119,v119,v120\n"
"s_bitcmp1_b32 s90,3\n"
"s_cselect_b32 s82,0xff800000,0\n"
"v_add_f32 v119,0x41980000,v119\n"
"v_max_f32 v119,s82,v119\n"
"v_cmp_lt_f32 vcc,s17,|v119|\n"
"s_nop 0\n"
"s_cbranch_vccz .Lfk_rare2_3\n"
"v_ceil_f32 v119,v119\n"
"v_sub_f32 v24,v24,v119\n"
"v_sub_f32 v25,v25,v119\n"
"v_sub_f32 v26,v26,v119\n"
"v_sub_f32 v27,v27,v119\n"
"v_sub_f32 v28,v28,v119\n"
"v_sub_f32 v29,v29,v119\n"
"v_sub_f32 v30,v30,v119\n"
"v_sub_f32 v31,v31,v119\n"
"v_sub_f32 v108,v108,v119\n"
"v_sub_f32 v109,v109,v119\n"
"v_sub_f32 v110,v110,v119\n"
"v_sub_f32 v111,v111,v119\n"
"v_sub_f32 v56,v56,v119\n"
"v_sub_f32 v57,v57,v119\n"
"v_sub_f32 v58,v58,v119\n"
"v_sub_f32 v59,v59,v119\n"
"v_sub_f32 v60,v60,v119\n"
"v_sub_f32 v61,v61,v119\n"
"v_sub_f32 v62,v62,v119\n"
"v_sub_f32 v63,v63,v119\n"
"v_exp_f32 v24,v24\n"
"v_exp_f32 v25,v25\n"
"v_exp_f32 v26,v26\n"
"v_exp_f32 v27,v27\n"
"v_exp_f32 v28,v28\n"
"v_exp_f32 v29,v29\n"
"v_exp_f32 v30,v30\n"
"v_exp_f32 v31,v31\n"
"v_cvt_pk_bf16_f32 v76,v24,v25\n"
"v_cvt_pk_bf16_f32 v77,v26,v27\n"
"v_cvt_pk_bf16_f32 v78,v28,v29\n"
"v_cvt_pk_bf16_f32 v79,v30,v31\n"
"s_cbranch_scc1 .Lfk_rare2_3\n"
"v_exp_f32 v119,-v119\n"
"s_nop 0\n"
"v_mul_f32 v224,v224,v119\n"
"v_mul_f32 v225,v225,v119\n"
"v_mul_f32 v226,v226,v119\n"
"v_mul_f32 v227,v227,v119\n"
"v_mul_f32 v228,v228,v119\n"
"v_mul_f32 v229,v229,v119\n"
"v_mul_f32 v230,v230,v119\n"
"v_mul_f32 v231,v231,v119\n"
"v_mul_f32 v232,v232,v119\n"
"v_mul_f32 v233,v233,v119\n"
"v_mul_f32 v234,v234,v119\n"
"v_mul_f32 v235,v235,v119\n"
"v_mul_f32 v236,v236,v119\n"
"v_mul_f32 v237,v237,v119\n"
"v_mul_f32 v238,v238,v119\n"
"v_mul_f32 v239,v239,v119\n"
"v_mul_f32 v240,v240,v119\n"
"v_mul_f32 v241,v241,v119\n"
"v_mul_f32 v242,v242,v119\n"
"v_mul_f32 v243,v243,v119\n"
"v_mul_f32 v244,v244,v119\n"
"v_mul_f32 v245,v245,v119\n"
"v_mul_f32 v246,v246,v119\n"
"v_mul_f32 v247,v247,v119\n"
"v_mul_f32 v248,v248,v119\n"
"v_mul_f32 v249,v249,v119\n"
"v_mul_f32 v250,v250,v119\n"
"v_mul_f32 v251,v251,v119\n"
"v_mul_f32 v252,v252,v119\n"
"v_mul_f32 v253,v253,v119\n"
"v_mul_f32 v254,v254,v119\n"
"v_mul_f32 v255,v255,v119\n"
"v_accvgpr_read_b32 v120,a140\n"
"v_mul_f32 v120,v120,v119\n"
"v_accvgpr_write_b32 a140,v120\n"
"v_accvgpr_read_b32 v120,a141\n"
"v_mul_f32 v120,v120,v119\n"
"v_accvgpr_write_b32 a141,v120\n"
"v_accvgpr_read_b32 v120,a142\n"
"v_mul_f32 v120,v120,v119\n"
"v_accvgpr_write_b32 a142,v120\n"
"v_accvgpr_read_b32 v120,a143\n"
"v_mul_f32 v120,v120,v119\n"
"v_accvgpr_write_b32 a143,v120\n"
".Lfk_rare2_3:\n"
"s_mov_b32 s90,0\n"
"s_nop 1\n"
"s_setpc_b64 s[94:95]\n"
".Lfk_mslow0:\n"
"s_nop 7\n"
"v_min_f32 v119,v98,v104\n"
"v_sub_f32 v120,v119,v104\n"
"v_sub_f32 v119,v119,v98\n"
"v_exp_f32 v120,v120\n"
"v_exp_f32 v119,v119\n"
"v_mul_f32 v192,v192,v120\n"
"v_mul_f32 v193,v193,v120\n"
"v_mul_f32 v194,v194,v120\n"
"v_mul_f32 v195,v195,v120\n"
"v_mul_f32 v196,v196,v120\n"
"v_mul_f32 v197,v197,v120\n"
"v_mul_f32 v198,v198,v120\n"
"v_mul_f32 v199,v199,v120\n"
"v_mul_f32 v200,v200,v120\n"
"v_mul_f32 v201,v201,v120\n"
"v_mul_f32 v202,v202,v120\n"
"v_mul_f32 v203,v203,v120\n"
"v_mul_f32 v204,v204,v120\n"
"v_mul_f32 v205,v205,v120\n"
"v_mul_f32 v206,v206,v120\n"
"v_mul_f32 v207,v207,v120\n"
"v_mul_f32 v208,v208,v120\n"
"v_mul_f32 v209,v209,v120\n"
"v_mul_f32 v210,v210,v120\n"
"v_mul_f32 v211,v211,v120\n"
"v_mul_f32 v212,v212,v120\n"
"v_mul_f32 v213,v213,v120\n"
"v_mul_f32 v214,v214,v120\n"
"v_mul_f32 v215,v215,v120\n"
"v_mul_f32 v216,v216,v120\n"
"v_mul_f32 v217,v217,v120\n"
"v_mul_f32 v218,v218,v120\n"
"v_mul_f32 v219,v219,v120\n"
"v_mul_f32 v220,v220,v120\n"
"v_mul_f32 v221,v221,v120\n"
"v_mul_f32 v222,v222,v120\n"
"v_mul_f32 v223,v223,v120\n"
"v_accvgpr_read_b32 v121,a136\n"
"v_mul_f32 v121,v121,v120\n"
"v_accvgpr_write_b32 a136,v121\n"
"v_mul_f32 v99,v99,v119\n"
"v_accvgpr_read_b32 v121,a64\n"
"v_lshlrev_b32 v120,16,v121\n"
"v_and_b32 v121,0xffff0000,v121\n"
"v_mul_f32 v120,v120,v119\n"
"v_mul_f32 v121,v121,v119\n"
"v_cvt_pk_bf16_f32 v121,v120,v121\n"
"v_accvgpr_write_b32 a64,v121\n"
"v_accvgpr_read_b32 v121,a65\n"
"v_lshlrev_b32 v120,16,v121\n"
"v_and_b32 v121,0xffff0000,v121\n"
"v_mul_f32 v120,v120,v119\n"
"v_mul_f32 v121,v121,v119\n"
"v_cvt_pk_bf16_f32 v121,v120,v121\n"
"v_accvgpr_write_b32 a65,v121\n"
"v_accvgpr_read_b32 v121,a66\n"
"v_lshlrev_b32 v120,16,v121\n"
"v_and_b32 v121,0xffff0000,v121\n"
"v_mul_f32 v120,v120,v119\n"
"v_mul_f32 v121,v121,v119\n"
"v_cvt_pk_bf16_f32 v121,v120,v121\n"
"v_accvgpr_write_b32 a66,v121\n"
"v_accvgpr_read_b32 v121,a67\n"
"v_lshlrev_b32 v120,16,v121\n"
"v_and_b32 v121,0xffff0000,v121\n"
"v_mul_f32 v120,v120,v119\n"
"v_mul_f32 v121,v121,v119\n"
"v_cvt_pk_bf16_f32 v121,v120,v121\n"
"v_accvgpr_write_b32 a67,v121\n"
"v_accvgpr_read_b32 v121,a68\n"
"v_lshlrev_b32 v120,16,v121\n"
"v_and_b32 v121,0xffff0000,v121\n"
"v_mul_f32 v120,v120,v119\n"
"v_mul_f32 v121,v121,v119\n"
"v_cvt_pk_bf16_f32 v121,v120,v121\n"
"v_accvgpr_write_b32 a68,v121\n"
"v_accvgpr_read_b32 v121,a69\n"
"v_lshlrev_b32 v120,16,v121\n"
"v_and_b32 v121,0xffff0000,v121\n"
"v_mul_f32 v120,v120,v119\n"
"v_mul_f32 v121,v121,v119\n"
"v_cvt_pk_bf16_f32 v121,v120,v121\n"
"v_accvgpr_write_b32 a69,v121\n"
"v_accvgpr_read_b32 v121,a70\n"
"v_lshlrev_b32 v120,16,v121\n"
"v_and_b32 v121,0xffff0000,v121\n"
"v_mul_f32 v120,v120,v119\n"
"v_mul_f32 v121,v121,v119\n"
"v_cvt_pk_bf16_f32 v121,v120,v121\n"
"v_accvgpr_write_b32 a70,v121\n"
"v_accvgpr_read_b32 v121,a71\n"
"v_lshlrev_b32 v120,16,v121\n"
"v_and_b32 v121,0xffff0000,v121\n"
"v_mul_f32 v120,v120,v119\n"
"v_mul_f32 v121,v121,v119\n"
"v_cvt_pk_bf16_f32 v121,v120,v121\n"
"v_accvgpr_write_b32 a71,v121\n"
"v_accvgpr_read_b32 v121,a72\n"
"v_lshlrev_b32 v120,16,v121\n"
"v_and_b32 v121,0xffff0000,v121\n"
"v_mul_f32 v120,v120,v119\n"
"v_mul_f32 v121,v121,v119\n"
"v_cvt_pk_bf16_f32 v121,v120,v121\n"
"v_accvgpr_write_b32 a72,v121\n"
"v_accvgpr_read_b32 v121,a73\n"
"v_lshlrev_b32 v120,16,v121\n"
"v_and_b32 v121,0xffff0000,v121\n"
"v_mul_f32 v120,v120,v119\n"
"v_mul_f32 v121,v121,v119\n"
"v_cvt_pk_bf16_f32 v121,v120,v121\n"
"v_accvgpr_write_b32 a73,v121\n"
"v_accvgpr_read_b32 v121,a74\n"
"v_lshlrev_b32 v120,16,v121\n"
"v_and_b32 v121,0xffff0000,v121\n"
"v_mul_f32 v120,v120,v119\n"
"v_mul_f32 v121,v121,v119\n"
"v_cvt_pk_bf16_f32 v121,v120,v121\n"
"v_accvgpr_write_b32 a74,v121\n"
"v_accvgpr_read_b32 v121,a75\n"
"v_lshlrev_b32 v120,16,v121\n"
"v_and_b32 v121,0xffff0000,v121\n"
"v_mul_f32 v120,v120,v119\n"
"v_mul_f32 v121,v121,v119\n"
"v_cvt_pk_bf16_f32 v121,v120,v121\n"
"v_accvgpr_write_b32 a75,v121\n"
"v_accvgpr_read_b32 v121,a76\n"
"v_lshlrev_b32 v120,16,v121\n"
"v_and_b32 v121,0xffff0000,v121\n"
"v_mul_f32 v120,v120,v119\n"
"v_mul_f32 v121,v121,v119\n"
"v_cvt_pk_bf16_f32 v121,v120,v121\n"
"v_accvgpr_write_b32 a76,v121\n"
"v_accvgpr_read_b32 v121,a77\n"
"v_lshlrev_b32 v120,16,v121\n"
"v_and_b32 v121,0xffff0000,v121\n"
"v_mul_f32 v120,v120,v119\n"
"v_mul_f32 v121,v121,v119\n"
"v_cvt_pk_bf16_f32 v121,v120,v121\n"
"v_accvgpr_write_b32 a77,v121\n"
"v_accvgpr_read_b32 v121,a78\n"
"v_lshlrev_b32 v120,16,v121\n"
"v_and_b32 v121,0xffff0000,v121\n"
"v_mul_f32 v120,v120,v119\n"
"v_mul_f32 v121,v121,v119\n"
"v_cvt_pk_bf16_f32 v121,v120,v121\n"
"v_accvgpr_write_b32 a78,v121\n"
"v_accvgpr_read_b32 v121,a79\n"
"v_lshlrev_b32 v120,16,v121\n"
"v_and_b32 v121,0xffff0000,v121\n"
"v_mul_f32 v120,v120,v119\n"
"v_mul_f32 v121,v121,v119\n"
"v_cvt_pk_bf16_f32 v121,v120,v121\n"
"v_accvgpr_write_b32 a79,v121\n"
"s_nop 1\n"
"s_setpc_b64 s[94:95]\n"
".Lfk_rare1:\n"
"s_nop 7\n"
"s_bitcmp1_b32 s91,0\n"
"s_cbranch_scc0 .Lfk_rare1_0\n"
"v_max3_f32 v119,v32,v33,v34\n"
"v_max3_f32 v120,v35,v36,v37\n"
"v_max3_f32 v119,v119,v38,v39\n"
"v_max_f32 v119,v119,v120\n"
"v_mov_b32 v120,v119\n"
"s_nop 1\n"
"v_permlane16_swap_b32 v120,v119\n"
"v_max_f32 v119,v119,v120\n"
"v_mov_b32 v120,v119\n"
"s_nop 1\n"
"v_permlane32_swap_b32 v120,v119\n"
"v_max_f32 v119,v119,v120\n"
"s_bitcmp1_b32 s90,0\n"
"s_cselect_b32 s82,0xff800000,0\n"
"v_add_f32 v119,0x41980000,v119\n"
"v_max_f32 v119,s82,v119\n"
"v_cmp_lt_f32 vcc,s17,|v119|\n"
"s_nop 0\n"
"s_cbranch_vccz .Lfk_rare1_0\n"
"v_ceil_f32 v119,v119\n"
"v_sub_f32 v32,v32,v119\n"
"v_sub_f32 v33,v33,v119\n"
"v_sub_f32 v34,v34,v119\n"
"v_sub_f32 v35,v35,v119\n"
"v_sub_f32 v36,v36,v119\n"
"v_sub_f32 v37,v37,v119\n"
"v_sub_f32 v38,v38,v119\n"
"v_sub_f32 v39,v39,v119\n"
"v_sub_f32 v96,v96,v119\n"
"v_sub_f32 v97,v97,v119\n"
"v_sub_f32 v98,v98,v119\n"
"v_sub_f32 v99,v99,v119\n"
"s_cbranch_scc1 .Lfk_rare1_0\n"
"v_exp_f32 v119,-v119\n"
"s_nop 0\n"
"v_mul_f32 v128,v128,v119\n"
"v_mul_f32 v129,v129,v119\n"
"v_mul_f32 v130,v130,v119\n"
"v_mul_f32 v131,v131,v119\n"
"v_mul_f32 v132,v132,v119\n"
"v_mul_f32 v133,v133,v119\n"
"v_mul_f32 v134,v134,v119\n"
"v_mul_f32 v135,v135,v119\n"
"v_mul_f32 v136,v136,v119\n"
"v_mul_f32 v137,v137,v119\n"
"v_mul_f32 v138,v138,v119\n"
"v_mul_f32 v139,v139,v119\n"
"v_mul_f32 v140,v140,v119\n"
"v_mul_f32 v141,v141,v119\n"
"v_mul_f32 v142,v142,v119\n"
"v_mul_f32 v143,v143,v119\n"
"v_mul_f32 v144,v144,v119\n"
"v_mul_f32 v145,v145,v119\n"
"v_mul_f32 v146,v146,v119\n"
"v_mul_f32 v147,v147,v119\n"
"v_mul_f32 v148,v148,v119\n"
"v_mul_f32 v149,v149,v119\n"
"v_mul_f32 v150,v150,v119\n"
"v_mul_f32 v151,v151,v119\n"
"v_mul_f32 v152,v152,v119\n"
"v_mul_f32 v153,v153,v119\n"
"v_mul_f32 v154,v154,v119\n"
"v_mul_f32 v155,v155,v119\n"
"v_mul_f32 v156,v156,v119\n"
"v_mul_f32 v157,v157,v119\n"
"v_mul_f32 v158,v158,v119\n"
"v_mul_f32 v159,v159,v119\n"
"v_accvgpr_read_b32 v120,a128\n"
"v_mul_f32 v120,v120,v119\n"
"v_accvgpr_write_b32 a128,v120\n"
"v_lshlrev_b32 v120,16,v64\n"
"v_and_b32 v121,0xffff0000,v64\n"
"v_mul_f32 v120,v120,v119\n"
"v_mul_f32 v121,v121,v119\n"
"v_cvt_pk_bf16_f32 v64,v120,v121\n"
"v_accvgpr_read_b32 v120,a129\n"
"v_mul_f32 v120,v120,v119\n"
"v_accvgpr_write_b32 a129,v120\n"
"v_lshlrev_b32 v120,16,v65\n"
"v_and_b32 v121,0xffff0000,v65\n"
"v_mul_f32 v120,v120,v119\n"
"v_mul_f32 v121,v121,v119\n"
"v_cvt_pk_bf16_f32 v65,v120,v121\n"
"v_accvgpr_read_b32 v120,a130\n"
"v_mul_f32 v120,v120,v119\n"
"v_accvgpr_write_b32 a130,v120\n"
"v_lshlrev_b32 v120,16,v66\n"
"v_and_b32 v121,0xffff0000,v66\n"
"v_mul_f32 v120,v120,v119\n"
"v_mul_f32 v121,v121,v119\n"
"v_cvt_pk_bf16_f32 v66,v120,v121\n"
"v_accvgpr_read_b32 v120,a131\n"
"v_mul_f32 v120,v120,v119\n"
"v_accvgpr_write_b32 a131,v120\n"
"v_lshlrev_b32 v120,16,v67\n"
"v_and_b32 v121,0xffff0000,v67\n"
"v_mul_f32 v120,v120,v119\n"
"v_mul_f32 v121,v121,v119\n"
"v_cvt_pk_bf16_f32 v67,v120,v121\n"
".Lfk_rare1_0:\n"
"s_bitcmp1_b32 s91,1\n"
"s_cbranch_scc0 .Lfk_rare1_1\n"
"v_max3_f32 v119,v40,v41,v42\n"
"v_max3_f32 v120,v43,v44,v45\n"
"v_max3_f32 v119,v119,v46,v47\n"
"v_max_f32 v119,v119,v120\n"
"v_mov_b32 v120,v119\n"
"s_nop 1\n"
"v_permlane16_swap_b32 v120,v119\n"
"v_max_f32 v119,v119,v120\n"
"v_mov_b32 v120,v119\n"
"s_nop 1\n"
"v_permlane32_swap_b32 v120,v119\n"
"v_max_f32 v119,v119,v120\n"
"s_bitcmp1_b32 s90,1\n"
"s_cselect_b32 s82,0xff800000,0\n"
"v_add_f32 v119,0x41980000,v119\n"
"v_max_f32 v119,s82,v119\n"
"v_cmp_lt_f32 vcc,s17,|v119|\n"
"s_nop 0\n"
"s_cbranch_vccz .Lfk_rare1_1\n"
"v_ceil_f32 v119,v119\n"
"v_sub_f32 v40,v40,v119\n"
"v_sub_f32 v41,v41,v119\n"
"v_sub_f32 v42,v42,v119\n"
"v_sub_f32 v43,v43,v119\n"
"v_sub_f32 v44,v44,v119\n"
"v_sub_f32 v45,v45,v119\n"
"v_sub_f32 v46,v46,v119\n"
"v_sub_f32 v47,v47,v119\n"
"v_sub_f32 v100,v100,v119\n"
"v_sub_f32 v101,v101,v119\n"
"v_sub_f32 v102,v102,v119\n"
"v_sub_f32 v103,v103,v119\n"
"s_cbranch_scc1 .Lfk_rare1_1\n"
"v_exp_f32 v119,-v119\n"
"s_nop 0\n"
"v_mul_f32 v160,v160,v119\n"
"v_mul_f32 v161,v161,v119\n"
"v_mul_f32 v162,v162,v119\n"
"v_mul_f32 v163,v163,v119\n"
"v_mul_f32 v164,v164,v119\n"
"v_mul_f32 v165,v165,v119\n"
"v_mul_f32 v166,v166,v119\n"
"v_mul_f32 v167,v167,v119\n"
"v_mul_f32 v168,v168,v119\n"
"v_mul_f32 v169,v169,v119\n"
"v_mul_f32 v170,v170,v119\n"
"v_mul_f32 v171,v171,v119\n"
"v_mul_f32 v172,v172,v119\n"
"v_mul_f32 v173,v173,v119\n"
"v_mul_f32 v174,v174,v119\n"
"v_mul_f32 v175,v175,v119\n"
"v_mul_f32 v176,v176,v119\n"
"v_mul_f32 v177,v177,v119\n"
"v_mul_f32 v178,v178,v119\n"
"v_mul_f32 v179,v179,v119\n"
"v_mul_f32 v180,v180,v119\n"
"v_mul_f32 v181,v181,v119\n"
"v_mul_f32 v182,v182,v119\n"
"v_mul_f32 v183,v183,v119\n"
"v_mul_f32 v184,v184,v119\n"
"v_mul_f32 v185,v185,v119\n"
"v_mul_f32 v186,v186,v119\n"
"v_mul_f32 v187,v187,v119\n"
"v_mul_f32 v188,v188,v119\n"
"v_mul_f32 v189,v189,v119\n"
"v_mul_f32 v190,v190,v119\n"
"v_mul_f32 v191,v191,v119\n"
"v_accvgpr_read_b32 v120,a132\n"
"v_mul_f32 v120,v120,v119\n"
"v_accvgpr_write_b32 a132,v120\n"
"v_lshlrev_b32 v120,16,v68\n"
"v_and_b32 v121,0xffff0000,v68\n"
"v_mul_f32 v120,v120,v119\n"
"v_mul_f32 v121,v121,v119\n"
"v_cvt_pk_bf16_f32 v68,v120,v121\n"
"v_accvgpr_read_b32 v120,a133\n"
"v_mul_f32 v120,v120,v119\n"
"v_accvgpr_write_b32 a133,v120\n"
"v_lshlrev_b32 v120,16,v69\n"
"v_and_b32 v121,0xffff0000,v69\n"
"v_mul_f32 v120,v120,v119\n"
"v_mul_f32 v121,v121,v119\n"
"v_cvt_pk_bf16_f32 v69,v120,v121\n"
"v_accvgpr_read_b32 v120,a134\n"
"v_mul_f32 v120,v120,v119\n"
"v_accvgpr_write_b32 a134,v120\n"
"v_lshlrev_b32 v120,16,v70\n"
"v_and_b32 v121,0xffff0000,v70\n"
"v_mul_f32 v120,v120,v119\n"
"v_mul_f32 v121,v121,v119\n"
"v_cvt_pk_bf16_f32 v70,v120,v121\n"
"v_accvgpr_read_b32 v120,a135\n"
"v_mul_f32 v120,v120,v119\n"
"v_accvgpr_write_b32 a135,v120\n"
"v_lshlrev_b32 v120,16,v71\n"
"v_and_b32 v121,0xffff0000,v71\n"
"v_mul_f32 v120,v120,v119\n"
"v_mul_f32 v121,v121,v119\n"
"v_cvt_pk_bf16_f32 v71,v120,v121\n"
".Lfk_rare1_1:\n"
"s_bitcmp1_b32 s91,2\n"
"s_cbranch_scc0 .Lfk_rare1_2\n"
"v_max3_f32 v119,v48,v49,v50\n"
"v_max3_f32 v120,v51,v52,v53\n"
"v_max3_f32 v119,v119,v54,v55\n"
"v_max_f32 v119,v119,v120\n"
"v_mov_b32 v120,v119\n"
"s_nop 1\n"
"v_permlane16_swap_b32 v120,v119\n"
"v_max_f32 v119,v119,v120\n"
"v_mov_b32 v120,v119\n"
"s_nop 1\n"
"v_permlane32_swap_b32 v120,v119\n"
"v_max_f32 v119,v119,v120\n"
"s_bitcmp1_b32 s90,2\n"
"s_cselect_b32 s82,0xff800000,0\n"
"v_add_f32 v119,0x41980000,v119\n"
"v_max_f32 v119,s82,v119\n"
"v_cmp_lt_f32 vcc,s17,|v119|\n"
"s_nop 0\n"
"s_cbranch_vccz .Lfk_rare1_2\n"
"v_ceil_f32 v119,v119\n"
"v_sub_f32 v48,v48,v119\n"
"v_sub_f32 v49,v49,v119\n"
"v_sub_f32 v50,v50,v119\n"
"v_sub_f32 v51,v51,v119\n"
"v_sub_f32 v52,v52,v119\n"
"v_sub_f32 v53,v53,v119\n"
"v_sub_f32 v54,v54,v119\n"
"v_sub_f32 v55,v55,v119\n"
"v_sub_f32 v104,v104,v119\n"
"v_sub_f32 v105,v105,v119\n"
"v_sub_f32 v106,v106,v119\n"
"v_sub_f32 v107,v107,v119\n"
"s_cbranch_scc1 .Lfk_rare1_2\n"
"v_exp_f32 v119,-v119\n"
"s_nop 0\n"
"v_mul_f32 v192,v192,v119\n"
"v_mul_f32 v193,v193,v119\n"
"v_mul_f32 v194,v194,v119\n"
"v_mul_f32 v195,v195,v119\n"
"v_mul_f32 v196,v196,v119\n"
"v_mul_f32 v197,v197,v119\n"
"v_mul_f32 v198,v198,v119\n"
"v_mul_f32 v199,v199,v119\n"
"v_mul_f32 v200,v200,v119\n"
"v_mul_f32 v201,v201,v119\n"
"v_mul_f32 v202,v202,v119\n"
"v_mul_f32 v203,v203,v119\n"
"v_mul_f32 v204,v204,v119\n"
"v_mul_f32 v205,v205,v119\n"
"v_mul_f32 v206,v206,v119\n"
"v_mul_f32 v207,v207,v119\n"
"v_mul_f32 v208,v208,v119\n"
"v_mul_f32 v209,v209,v119\n"
"v_mul_f32 v210,v210,v119\n"
"v_mul_f32 v211,v211,v119\n"
"v_mul_f32 v212,v212,v119\n"
"v_mul_f32 v213,v213,v119\n"
"v_mul_f32 v214,v214,v119\n"
"v_mul_f32 v215,v215,v119\n"
"v_mul_f32 v216,v216,v119\n"
"v_mul_f32 v217,v217,v119\n"
"v_mul_f32 v218,v218,v119\n"
"v_mul_f32 v219,v219,v119\n"
"v_mul_f32 v220,v220,v119\n"
"v_mul_f32 v221,v221,v119\n"
"v_mul_f32 v222,v222,v119\n"
"v_mul_f32 v223,v223,v119\n"
"v_accvgpr_read_b32 v120,a136\n"
"v_mul_f32 v120,v120,v119\n"
"v_accvgpr_write_b32 a136,v120\n"
"v_lshlrev_b32 v120,16,v72\n"
"v_and_b32 v121,0xffff0000,v72\n"
"v_mul_f32 v120,v120,v119\n"
"v_mul_f32 v121,v121,v119\n"
"v_cvt_pk_bf16_f32 v72,v120,v121\n"
"v_accvgpr_read_b32 v120,a137\n"
"v_mul_f32 v120,v120,v119\n"
"v_accvgpr_write_b32 a137,v120\n"
"v_lshlrev_b32 v120,16,v73\n"
"v_and_b32 v121,0xffff0000,v73\n"
"v_mul_f32 v120,v120,v119\n"
"v_mul_f32 v121,v121,v119\n"
"v_cvt_pk_bf16_f32 v73,v120,v121\n"
"v_accvgpr_read_b32 v120,a138\n"
"v_mul_f32 v120,v120,v119\n"
"v_accvgpr_write_b32 a138,v120\n"
"v_lshlrev_b32 v120,16,v74\n"
"v_and_b32 v121,0xffff0000,v74\n"
"v_mul_f32 v120,v120,v119\n"
"v_mul_f32 v121,v121,v119\n"
"v_cvt_pk_bf16_f32 v74,v120,v121\n"
"v_accvgpr_read_b32 v120,a139\n"
"v_mul_f32 v120,v120,v119\n"
"v_accvgpr_write_b32 a139,v120\n"
"v_lshlrev_b32 v120,16,v75\n"
"v_and_b32 v121,0xffff0000,v75\n"
"v_mul_f32 v120,v120,v119\n"
"v_mul_f32 v121,v121,v119\n"
"v_cvt_pk_bf16_f32 v75,v120,v121\n"
".Lfk_rare1_2:\n"
"s_bitcmp1_b32 s91,3\n"
"s_cbranch_scc0 .Lfk_rare1_3\n"
"v_max3_f32 v119,v56,v57,v58\n"
"v_max3_f32 v120,v59,v60,v61\n"
"v_max3_f32 v119,v119,v62,v63\n"
"v_max_f32 v119,v119,v120\n"
"v_mov_b32 v120,v119\n"
"s_nop 1\n"
"v_permlane16_swap_b32 v120,v119\n"
"v_max_f32 v119,v119,v120\n"
"v_mov_b32 v120,v119\n"
"s_nop 1\n"
"v_permlane32_swap_b32 v120,v119\n"
"v_max_f32 v119,v119,v120\n"
"s_bitcmp1_b32 s90,3\n"
"s_cselect_b32 s82,0xff800000,0\n"
"v_add_f32 v119,0x41980000,v119\n"
"v_max_f32 v119,s82,v119\n"
"v_cmp_lt_f32 vcc,s17,|v119|\n"
"s_nop 0\n"
"s_cbranch_vccz .Lfk_rare1_3\n"
"v_ceil_f32 v119,v119\n"
"v_sub_f32 v56,v56,v119\n"
"v_sub_f32 v57,v57,v119\n"
"v_sub_f32 v58,v58,v119\n"
"v_sub_f32 v59,v59,v119\n"
"v_sub_f32 v60,v60,v119\n"
"v_sub_f32 v61,v61,v119\n"
"v_sub_f32 v62,v62,v119\n"
"v_sub_f32 v63,v63,v119\n"
"v_sub_f32 v108,v108,v119\n"
"v_sub_f32 v109,v109,v119\n"
"v_sub_f32 v110,v110,v119\n"
"v_sub_f32 v111,v111,v119\n"
"s_cbranch_scc1 .Lfk_rare1_3\n"
"v_exp_f32 v119,-v119\n"
"s_nop 0\n"
"v_mul_f32 v224,v224,v119\n"
"v_mul_f32 v225,v225,v119\n"
"v_mul_f32 v226,v226,v119\n"
"v_mul_f32 v227,v227,v119\n"
"v_mul_f32 v228,v228,v119\n"
"v_mul_f32 v229,v229,v119\n"
"v_mul_f32 v230,v230,v119\n"
"v_mul_f32 v231,v231,v119\n"
"v_mul_f32 v232,v232,v119\n"
"v_mul_f32 v233,v233,v119\n"
"v_mul_f32 v234,v234,v119\n"
"v_mul_f32 v235,v235,v119\n"
"v_mul_f32 v236,v236,v119\n"
"v_mul_f32 v237,v237,v119\n"
"v_mul_f32 v238,v238,v119\n"
"v_mul_f32 v239,v239,v119\n"
"v_mul_f32 v240,v240,v119\n"
"v_mul_f32 v241,v241,v119\n"
"v_mul_f32 v242,v242,v119\n"
"v_mul_f32 v243,v243,v119\n"
"v_mul_f32 v244,v244,v119\n"
"v_mul_f32 v245,v245,v119\n"
"v_mul_f32 v246,v246,v119\n"
"v_mul_f32 v247,v247,v119\n"
"v_mul_f32 v248,v248,v119\n"
"v_mul_f32 v249,v249,v119\n"
"v_mul_f32 v250,v250,v119\n"
"v_mul_f32 v251,v251,v119\n"
"v_mul_f32 v252,v252,v119\n"
"v_mul_f32 v253,v253,v119\n"
"v_mul_f32 v254,v254,v119\n"
"v_mul_f32 v255,v255,v119\n"
"v_accvgpr_read_b32 v120,a140\n"
"v_mul_f32 v120,v120,v119\n"
"v_accvgpr_write_b32 a140,v120\n"
"v_lshlrev_b32 v120,16,v76\n"
"v_and_b32 v121,0xffff0000,v76\n"
"v_mul_f32 v120,v120,v119\n"
"v_mul_f32 v121,v121,v119\n"
"v_cvt_pk_bf16_f32 v76,v120,v121\n"
"v_accvgpr_read_b32 v120,a141\n"
"v_mul_f32 v120,v120,v119\n"
"v_accvgpr_write_b32 a141,v120\n"
"v_lshlrev_b32 v120,16,v77\n"
"v_and_b32 v121,0xffff0000,v77\n"
"v_mul_f32 v120,v120,v119\n"
"v_mul_f32 v121,v121,v119\n"
"v_cvt_pk_bf16_f32 v77,v120,v121\n"
"v_accvgpr_read_b32 v120,a142\n"
"v_mul_f32 v120,v120,v119\n"
"v_accvgpr_write_b32 a142,v120\n"
"v_lshlrev_b32 v120,16,v78\n"
"v_and_b32 v121,0xffff0000,v78\n"
"v_mul_f32 v120,v120,v119\n"
"v_mul_f32 v121,v121,v119\n"
"v_cvt_pk_bf16_f32 v78,v120,v121\n"
"v_accvgpr_read_b32 v120,a143\n"
"v_mul_f32 v120,v120,v119\n"
"v_accvgpr_write_b32 a143,v120\n"
"v_lshlrev_b32 v120,16,v79\n"
"v_and_b32 v121,0xffff0000,v79\n"
"v_mul_f32 v120,v120,v119\n"
"v_mul_f32 v121,v121,v119\n"
"v_cvt_pk_bf16_f32 v79,v120,v121\n"
".Lfk_rare1_3:\n"
"s_mov_b32 s90,0\n"
"s_nop 1\n"
"s_setpc_b64 s[94:95]\n"
".Lfk_rare3:\n"
"s_nop 7\n"
"v_max3_f32 v119,v32,v33,v34\n"
"v_max3_f32 v120,v35,v36,v37\n"
"v_max3_f32 v119,v119,v38,v39\n"
"v_max_f32 v119,v119,v120\n"
"v_mov_b32 v120,v119\n"
"s_nop 1\n"
"v_permlane16_swap_b32 v120,v119\n"
"v_max_f32 v119,v119,v120\n"
"v_mov_b32 v120,v119\n"
"s_nop 1\n"
"v_permlane32_swap_b32 v120,v119\n"
"v_max_f32 v119,v119,v120\n"
"s_bitcmp1_b32 s90,0\n"
"s_cselect_b32 s82,0xff800000,0\n"
"v_add_f32 v119,0x41980000,v119\n"
"v_max_f32 v119,s82,v119\n"
"v_cmp_lt_f32 vcc,s17,|v119|\n"
"s_nop 0\n"
"s_cbranch_vccz .Lfk_rare3_0\n"
"v_ceil_f32 v119,v119\n"
"v_sub_f32 v32,v32,v119\n"
"v_sub_f32 v33,v33,v119\n"
"v_sub_f32 v34,v34,v119\n"
"v_sub_f32 v35,v35,v119\n"
"v_sub_f32 v36,v36,v119\n"
"v_sub_f32 v37,v37,v119\n"
"v_sub_f32 v38,v38,v119\n"
"v_sub_f32 v39,v39,v119\n"
"v_sub_f32 v96,v96,v119\n"
"v_sub_f32 v97,v97,v119\n"
"v_sub_f32 v98,v98,v119\n"
"v_sub_f32 v99,v99,v119\n"
"v_sub_f32 v0,v0,v119\n"
"v_sub_f32 v1,v1,v119\n"
"v_sub_f32 v2,v2,v119\n"
"v_sub_f32 v3,v3,v119\n"
"v_sub_f32 v4,v4,v119\n"
"v_sub_f32 v5,v5,v119\n"
"v_sub_f32 v6,v6,v119\n"
"v_sub_f32 v7,v7,v119\n"
"v_exp_f32 v32,v32\n"
"v_exp_f32 v33,v33\n"
"v_exp_f32 v34,v34\n"
"v_exp_f32 v35,v35\n"
"v_exp_f32 v36,v36\n"
"v_exp_f32 v37,v37\n"
"v_exp_f32 v38,v38\n"
"v_exp_f32 v39,v39\n"
"v_cvt_pk_bf16_f32 v80,v32,v33\n"
"v_cvt_pk_bf16_f32 v81,v34,v35\n"
"v_cvt_pk_bf16_f32 v82,v36,v37\n"
"v_cvt_pk_bf16_f32 v83,v38,v39\n"
"s_cbranch_scc1 .Lfk_rare3_0\n"
"v_exp_f32 v119,-v119\n"
"s_nop 0\n"
"v_mul_f32 v128,v128,v119\n"
"v_mul_f32 v129,v129,v119\n"
"v_mul_f32 v130,v130,v119\n"
"v_mul_f32 v131,v131,v119\n"
"v_mul_f32 v132,v132,v119\n"
"v_mul_f32 v133,v133,v119\n"
"v_mul_f32 v134,v134,v119\n"
"v_mul_f32 v135,v135,v119\n"
"v_mul_f32 v136,v136,v119\n"
"v_mul_f32 v137,v137,v119\n"
"v_mul_f32 v138,v138,v119\n"
"v_mul_f32 v139,v139,v119\n"
"v_mul_f32 v140,v140,v119\n"
"v_mul_f32 v141,v141,v119\n"
"v_mul_f32 v142,v142,v119\n"
"v_mul_f32 v143,v143,v119\n"
"v_mul_f32 v144,v144,v119\n"
"v_mul_f32 v145,v145,v119\n"
"v_mul_f32 v146,v146,v119\n"
"v_mul_f32 v147,v147,v119\n"
"v_mul_f32 v148,v148,v119\n"
"v_mul_f32 v149,v149,v119\n"
"v_mul_f32 v150,v150,v119\n"
"v_mul_f32 v151,v151,v119\n"
"v_mul_f32 v152,v152,v119\n"
"v_mul_f32 v153,v153,v119\n"
"v_mul_f32 v154,v154,v119\n"
"v_mul_f32 v155,v155,v119\n"
"v_mul_f32 v156,v156,v119\n"
"v_mul_f32 v157,v157,v119\n"
"v_mul_f32 v158,v158,v119\n"
"v_mul_f32 v159,v159,v119\n"
"v_accvgpr_read_b32 v120,a128\n"
"v_mul_f32 v120,v120,v119\n"
"v_accvgpr_write_b32 a128,v120\n"
"v_accvgpr_read_b32 v120,a129\n"
"v_mul_f32 v120,v120,v119\n"
"v_accvgpr_write_b32 a129,v120\n"
"v_accvgpr_read_b32 v120,a130\n"
"v_mul_f32 v120,v120,v119\n"
"v_accvgpr_write_b32 a130,v120\n"
"v_accvgpr_read_b32 v120,a131\n"
"v_mul_f32 v120,v120,v119\n"
"v_accvgpr_write_b32 a131,v120\n"
".Lfk_rare3_0:\n"
"v_max3_f32 v119,v40,v41,v42\n"
"v_max3_f32 v120,v43,v44,v45\n"
"v_max3_f32 v119,v119,v46,v47\n"
"v_max_f32 v119,v119,v120\n"
"v_mov_b32 v120,v119\n"
"s_nop 1\n"
"v_permlane16_swap_b32 v120,v119\n"
"v_max_f32 v119,v119,v120\n"
"v_mov_b32 v120,v119\n"
"s_nop 1\n"
"v_permlane32_swap_b32 v120,v119\n"
"v_max_f32 v119,v119,v120\n"
"s_bitcmp1_b32 s90,1\n"
"s_cselect_b32 s82,0xff800000,0\n"
"v_add_f32 v119,0x41980000,v119\n"
"v_max_f32 v119,s82,v119\n"
"v_cmp_lt_f32 vcc,s17,|v119|\n"
"s_nop 0\n"
"s_cbranch_vccz .Lfk_rare3_1\n"
"v_ceil_f32 v119,v119\n"
"v_sub_f32 v40,v40,v119\n"
"v_sub_f32 v41,v41,v119\n"
"v_sub_f32 v42,v42,v119\n"
"v_sub_f32 v43,v43,v119\n"
"v_sub_f32 v44,v44,v119\n"
"v_sub_f32 v45,v45,v119\n"
"v_sub_f32 v46,v46,v119\n"
"v_sub_f32 v47,v47,v119\n"
"v_sub_f32 v100,v100,v119\n"
"v_sub_f32 v101,v101,v119\n"
"v_sub_f32 v102,v102,v119\n"
"v_sub_f32 v103,v103,v119\n"
"v_sub_f32 v8,v8,v119\n"
"v_sub_f32 v9,v9,v119\n"
"v_sub_f32 v10,v10,v119\n"
"v_sub_f32 v11,v11,v119\n"
"v_sub_f32 v12,v12,v119\n"
"v_sub_f32 v13,v13,v119\n"
"v_sub_f32 v14,v14,v119\n"
"v_sub_f32 v15,v15,v119\n"
"v_exp_f32 v40,v40\n"
"v_exp_f32 v41,v41\n"
"v_exp_f32 v42,v42\n"
"v_exp_f32 v43,v43\n"
"v_exp_f32 v44,v44\n"
"v_exp_f32 v45,v45\n"
"v_exp_f32 v46,v46\n"
"v_exp_f32 v47,v47\n"
"v_cvt_pk_bf16_f32 v84,v40,v41\n"
"v_cvt_pk_bf16_f32 v85,v42,v43\n"
"v_cvt_pk_bf16_f32 v86,v44,v45\n"
"v_cvt_pk_bf16_f32 v87,v46,v47\n"
"s_cbranch_scc1 .Lfk_rare3_1\n"
"v_exp_f32 v119,-v119\n"
"s_nop 0\n"
"v_mul_f32 v160,v160,v119\n"
"v_mul_f32 v161,v161,v119\n"
"v_mul_f32 v162,v162,v119\n"
"v_mul_f32 v163,v163,v119\n"
"v_mul_f32 v164,v164,v119\n"
"v_mul_f32 v165,v165,v119\n"
"v_mul_f32 v166,v166,v119\n"
"v_mul_f32 v167,v167,v119\n"
"v_mul_f32 v168,v168,v119\n"
"v_mul_f32 v169,v169,v119\n"
"v_mul_f32 v170,v170,v119\n"
"v_mul_f32 v171,v171,v119\n"
"v_mul_f32 v172,v172,v119\n"
"v_mul_f32 v173,v173,v119\n"
"v_mul_f32 v174,v174,v119\n"
"v_mul_f32 v175,v175,v119\n"
"v_mul_f32 v176,v176,v119\n"
"v_mul_f32 v177,v177,v119\n"
"v_mul_f32 v178,v178,v119\n"
"v_mul_f32 v179,v179,v119\n"
"v_mul_f32 v180,v180,v119\n"
"v_mul_f32 v181,v181,v119\n"
"v_mul_f32 v182,v182,v119\n"
"v_mul_f32 v183,v183,v119\n"
"v_mul_f32 v184,v184,v119\n"
"v_mul_f32 v185,v185,v119\n"
"v_mul_f32 v186,v186,v119\n"
"v_mul_f32 v187,v187,v119\n"
"v_mul_f32 v188,v188,v119\n"
"v_mul_f32 v189,v189,v119\n"
"v_mul_f32 v190,v190,v119\n"
"v_mul_f32 v191,v191,v119\n"
"v_accvgpr_read_b32 v120,a132\n"
"v_mul_f32 v120,v120,v119\n"
"v_accvgpr_write_b32 a132,v120\n"
"v_accvgpr_read_b32 v120,a133\n"
"v_mul_f32 v120,v120,v119\n"
"v_accvgpr_write_b32 a133,v120\n"
"v_accvgpr_read_b32 v120,a134\n"
"v_mul_f32 v120,v120,v119\n"
"v_accvgpr_write_b32 a134,v120\n"
"v_accvgpr_read_b32 v120,a135\n"
"v_mul_f32 v120,v120,v119\n"
"v_accvgpr_write_b32 a135,v120\n"
".Lfk_rare3_1:\n"
"v_max3_f32 v119,v48,v49,v50\n"
"v_max3_f32 v120,v51,v52,v53\n"
"v_max3_f32 v119,v119,v54,v55\n"
"v_max_f32 v119,v119,v120\n"
"v_mov_b32 v120,v119\n"
"s_nop 1\n"
"v_permlane16_swap_b32 v120,v119\n"
"v_max_f32 v119,v119,v120\n"
"v_mov_b32 v120,v119\n"
"s_nop 1\n"
"v_permlane32_swap_b32 v120,v119\n"
"v_max_f32 v119,v119,v120\n"
"s_bitcmp1_b32 s90,2\n"
"s_cselect_b32 s82,0xff800000,0\n"
"v_add_f32 v119,0x41980000,v119\n"
"v_max_f32 v119,s82,v119\n"
"v_cmp_lt_f32 vcc,s17,|v119|\n"
"s_nop 0\n"
"s_cbranch_vccz .Lfk_rare3_2\n"
"v_ceil_f32 v119,v119\n"
"v_sub_f32 v48,v48,v119\n"
"v_sub_f32 v49,v49,v119\n"
"v_sub_f32 v50,v50,v119\n"
"v_sub_f32 v51,v51,v119\n"
"v_sub_f32 v52,v52,v119\n"
"v_sub_f32 v53,v53,v119\n"
"v_sub_f32 v54,v54,v119\n"
"v_sub_f32 v55,v55,v119\n"
"v_sub_f32 v104,v104,v119\n"
"v_sub_f32 v105,v105,v119\n"
"v_sub_f32 v106,v106,v119\n"
"v_sub_f32 v107,v107,v119\n"
"v_sub_f32 v16,v16,v119\n"
"v_sub_f32 v17,v17,v119\n"
"v_sub_f32 v18,v18,v119\n"
"v_sub_f32 v19,v19,v119\n"
"v_sub_f32 v20,v20,v119\n"
"v_sub_f32 v21,v21,v119\n"
"v_sub_f32 v22,v22,v119\n"
"v_sub_f32 v23,v23,v119\n"
"v_exp_f32 v48,v48\n"
"v_exp_f32 v49,v49\n"
"v_exp_f32 v50,v50\n"
"v_exp_f32 v51,v51\n"
"v_exp_f32 v52,v52\n"
"v_exp_f32 v53,v53\n"
"v_exp_f32 v54,v54\n"
"v_exp_f32 v55,v55\n"
"v_cvt_pk_bf16_f32 v88,v48,v49\n"
"v_cvt_pk_bf16_f32 v89,v50,v51\n"
"v_cvt_pk_bf16_f32 v90,v52,v53\n"
"v_cvt_pk_bf16_f32 v91,v54,v55\n"
"s_cbranch_scc1 .Lfk_rare3_2\n"
"v_exp_f32 v119,-v119\n"
"s_nop 0\n"
"v_mul_f32 v192,v192,v119\n"
"v_mul_f32 v193,v193,v119\n"
"v_mul_f32 v194,v194,v119\n"
"v_mul_f32 v195,v195,v119\n"
"v_mul_f32 v196,v196,v119\n"
"v_mul_f32 v197,v197,v119\n"
"v_mul_f32 v198,v198,v119\n"
"v_mul_f32 v199,v199,v119\n"
"v_mul_f32 v200,v200,v119\n"
"v_mul_f32 v201,v201,v119\n"
"v_mul_f32 v202,v202,v119\n"
"v_mul_f32 v203,v203,v119\n"
"v_mul_f32 v204,v204,v119\n"
"v_mul_f32 v205,v205,v119\n"
"v_mul_f32 v206,v206,v119\n"
"v_mul_f32 v207,v207,v119\n"
"v_mul_f32 v208,v208,v119\n"
"v_mul_f32 v209,v209,v119\n"
"v_mul_f32 v210,v210,v119\n"
"v_mul_f32 v211,v211,v119\n"
"v_mul_f32 v212,v212,v119\n"
"v_mul_f32 v213,v213,v119\n"
"v_mul_f32 v214,v214,v119\n"
"v_mul_f32 v215,v215,v119\n"
"v_mul_f32 v216,v216,v119\n"
"v_mul_f32 v217,v217,v119\n"
"v_mul_f32 v218,v218,v119\n"
"v_mul_f32 v219,v219,v119\n"
"v_mul_f32 v220,v220,v119\n"
"v_mul_f32 v221,v221,v119\n"
"v_mul_f32 v222,v222,v119\n"
"v_mul_f32 v223,v223,v119\n"
"v_accvgpr_read_b32 v120,a136\n"
"v_mul_f32 v120,v120,v119\n"
"v_accvgpr_write_b32 a136,v120\n"
"v_accvgpr_read_b32 v120,a137\n"
"v_mul_f32 v120,v120,v119\n"
"v_accvgpr_write_b32 a137,v120\n"
"v_accvgpr_read_b32 v120,a138\n"
"v_mul_f32 v120,v120,v119\n"
"v_accvgpr_write_b32 a138,v120\n"
"v_accvgpr_read_b32 v120,a139\n"
"v_mul_f32 v120,v120,v119\n"
"v_accvgpr_write_b32 a139,v120\n"
".Lfk_rare3_2:\n"
"v_max3_f32 v119,v56,v57,v58\n"
"v_max3_f32 v120,v59,v60,v61\n"
"v_max3_f32 v119,v119,v62,v63\n"
"v_max_f32 v119,v119,v120\n"
"v_mov_b32 v120,v119\n"
"s_nop 1\n"
"v_permlane16_swap_b32 v120,v119\n"
"v_max_f32 v119,v119,v120\n"
"v_mov_b32 v120,v119\n"
"s_nop 1\n"
"v_permlane32_swap_b32 v120,v119\n"
"v_max_f32 v119,v119,v120\n"
"s_bitcmp1_b32 s90,3\n"
"s_cselect_b32 s82,0xff800000,0\n"
"v_add_f32 v119,0x41980000,v119\n"
"v_max_f32 v119,s82,v119\n"
"v_cmp_lt_f32 vcc,s17,|v119|\n"
"s_nop 0\n"
"s_cbranch_vccz .Lfk_rare3_3\n"
"v_ceil_f32 v119,v119\n"
"v_sub_f32 v56,v56,v119\n"
"v_sub_f32 v57,v57,v119\n"
"v_sub_f32 v58,v58,v119\n"
"v_sub_f32 v59,v59,v119\n"
"v_sub_f32 v60,v60,v119\n"
"v_sub_f32 v61,v61,v119\n"
"v_sub_f32 v62,v62,v119\n"
"v_sub_f32 v63,v63,v119\n"
"v_sub_f32 v108,v108,v119\n"
"v_sub_f32 v109,v109,v119\n"
"v_sub_f32 v110,v110,v119\n"
"v_sub_f32 v111,v111,v119\n"
"v_sub_f32 v24,v24,v119\n"
"v_sub_f32 v25,v25,v119\n"
"v_sub_f32 v26,v26,v119\n"
"v_sub_f32 v27,v27,v119\n"
"v_sub_f32 v28,v28,v119\n"
"v_sub_f32 v29,v29,v119\n"
"v_sub_f32 v30,v30,v119\n"
"v_sub_f32 v31,v31,v119\n"
"v_exp_f32 v56,v56\n"
"v_exp_f32 v57,v57\n"
"v_exp_f32 v58,v58\n"
"v_exp_f32 v59,v59\n"
"v_exp_f32 v60,v60\n"
"v_exp_f32 v61,v61\n"
"v_exp_f32 v62,v62\n"
"v_exp_f32 v63,v63\n"
"v_cvt_pk_bf16_f32 v92,v56,v57\n"
"v_cvt_pk_bf16_f32 v93,v58,v59\n"
"v_cvt_pk_bf16_f32 v94,v60,v61\n"
"v_cvt_pk_bf16_f32 v95,v62,v63\n"
"s_cbranch_scc1 .Lfk_rare3_3\n"
"v_exp_f32 v119,-v119\n"
"s_nop 0\n"
"v_mul_f32 v224,v224,v119\n"
"v_mul_f32 v225,v225,v119\n"
"v_mul_f32 v226,v226,v119\n"
"v_mul_f32 v227,v227,v119\n"
"v_mul_f32 v228,v228,v119\n"
"v_mul_f32 v229,v229,v119\n"
"v_mul_f32 v230,v230,v119\n"
"v_mul_f32 v231,v231,v119\n"
"v_mul_f32 v232,v232,v119\n"
"v_mul_f32 v233,v233,v119\n"
"v_mul_f32 v234,v234,v119\n"
"v_mul_f32 v235,v235,v119\n"
"v_mul_f32 v236,v236,v119\n"
"v_mul_f32 v237,v237,v119\n"
"v_mul_f32 v238,v238,v119\n"
"v_mul_f32 v239,v239,v119\n"
"v_mul_f32 v240,v240,v119\n"
"v_mul_f32 v241,v241,v119\n"
"v_mul_f32 v242,v242,v119\n"
"v_mul_f32 v243,v243,v119\n"
"v_mul_f32 v244,v244,v119\n"
"v_mul_f32 v245,v245,v119\n"
"v_mul_f32 v246,v246,v119\n"
"v_mul_f32 v247,v247,v119\n"
"v_mul_f32 v248,v248,v119\n"
"v_mul_f32 v249,v249,v119\n"
"v_mul_f32 v250,v250,v119\n"
"v_mul_f32 v251,v251,v119\n"
"v_mul_f32 v252,v252,v119\n"
"v_mul_f32 v253,v253,v119\n"
"v_mul_f32 v254,v254,v119\n"
"v_mul_f32 v255,v255,v119\n"
"v_accvgpr_read_b32 v120,a140\n"
"v_mul_f32 v120,v120,v119\n"
"v_accvgpr_write_b32 a140,v120\n"
"v_accvgpr_read_b32 v120,a141\n"
"v_mul_f32 v120,v120,v119\n"
"v_accvgpr_write_b32 a141,v120\n"
"v_accvgpr_read_b32 v120,a142\n"
"v_mul_f32 v120,v120,v119\n"
"v_accvgpr_write_b32 a142,v120\n"
"v_accvgpr_read_b32 v120,a143\n"
"v_mul_f32 v120,v120,v119\n"
"v_accvgpr_write_b32 a143,v120\n"
".Lfk_rare3_3:\n"
"s_mov_b32 s90,0\n"
"s_nop 1\n"
"s_setpc_b64 s[94:95]\n"
".Lfk_mslow1:\n"
"s_nop 7\n"
"v_min_f32 v119,v102,v108\n"
"v_sub_f32 v120,v119,v108\n"
"v_sub_f32 v119,v119,v102\n"
"v_exp_f32 v120,v120\n"
"v_exp_f32 v119,v119\n"
"v_mul_f32 v224,v224,v120\n"
"v_mul_f32 v225,v225,v120\n"
"v_mul_f32 v226,v226,v120\n"
"v_mul_f32 v227,v227,v120\n"
"v_mul_f32 v228,v228,v120\n"
"v_mul_f32 v229,v229,v120\n"
"v_mul_f32 v230,v230,v120\n"
"v_mul_f32 v231,v231,v120\n"
"v_mul_f32 v232,v232,v120\n"
"v_mul_f32 v233,v233,v120\n"
"v_mul_f32 v234,v234,v120\n"
"v_mul_f32 v235,v235,v120\n"
"v_mul_f32 v236,v236,v120\n"
"v_mul_f32 v237,v237,v120\n"
"v_mul_f32 v238,v238,v120\n"
"v_mul_f32 v239,v239,v120\n"
"v_mul_f32 v240,v240,v120\n"
"v_mul_f32 v241,v241,v120\n"
"v_mul_f32 v242,v242,v120\n"
"v_mul_f32 v243,v243,v120\n"
"v_mul_f32 v244,v244,v120\n"
"v_mul_f32 v245,v245,v120\n"
"v_mul_f32 v246,v246,v120\n"
"v_mul_f32 v247,v247,v120\n"
"v_mul_f32 v248,v248,v120\n"
"v_mul_f32 v249,v249,v120\n"
"v_mul_f32 v250,v250,v120\n"
"v_mul_f32 v251,v251,v120\n"
"v_mul_f32 v252,v252,v120\n"
"v_mul_f32 v253,v253,v120\n"
"v_mul_f32 v254,v254,v120\n"
"v_mul_f32 v255,v255,v120\n"
"v_accvgpr_read_b32 v121,a140\n"
"v_mul_f32 v121,v121,v120\n"
"v_accvgpr_write_b32 a140,v121\n"
"v_mul_f32 v103,v103,v119\n"
"v_accvgpr_read_b32 v121,a80\n"
"v_lshlrev_b32 v120,16,v121\n"
"v_and_b32 v121,0xffff0000,v121\n"
"v_mul_f32 v120,v120,v119\n"
"v_mul_f32 v121,v121,v119\n"
"v_cvt_pk_bf16_f32 v121,v120,v121\n"
"v_accvgpr_write_b32 a80,v121\n"
"v_accvgpr_read_b32 v121,a81\n"
"v_lshlrev_b32 v120,16,v121\n"
"v_and_b32 v121,0xffff0000,v121\n"
"v_mul_f32 v120,v120,v119\n"
"v_mul_f32 v121,v121,v119\n"
"v_cvt_pk_bf16_f32 v121,v120,v121\n"
"v_accvgpr_write_b32 a81,v121\n"
"v_accvgpr_read_b32 v121,a82\n"
"v_lshlrev_b32 v120,16,v121\n"
"v_and_b32 v121,0xffff0000,v121\n"
"v_mul_f32 v120,v120,v119\n"
"v_mul_f32 v121,v121,v119\n"
"v_cvt_pk_bf16_f32 v121,v120,v121\n"
"v_accvgpr_write_b32 a82,v121\n"
"v_accvgpr_read_b32 v121,a83\n"
"v_lshlrev_b32 v120,16,v121\n"
"v_and_b32 v121,0xffff0000,v121\n"
"v_mul_f32 v120,v120,v119\n"
"v_mul_f32 v121,v121,v119\n"
"v_cvt_pk_bf16_f32 v121,v120,v121\n"
"v_accvgpr_write_b32 a83,v121\n"
"v_accvgpr_read_b32 v121,a84\n"
"v_lshlrev_b32 v120,16,v121\n"
"v_and_b32 v121,0xffff0000,v121\n"
"v_mul_f32 v120,v120,v119\n"
"v_mul_f32 v121,v121,v119\n"
"v_cvt_pk_bf16_f32 v121,v120,v121\n"
"v_accvgpr_write_b32 a84,v121\n"
"v_accvgpr_read_b32 v121,a85\n"
"v_lshlrev_b32 v120,16,v121\n"
"v_and_b32 v121,0xffff0000,v121\n"
"v_mul_f32 v120,v120,v119\n"
"v_mul_f32 v121,v121,v119\n"
"v_cvt_pk_bf16_f32 v121,v120,v121\n"
"v_accvgpr_write_b32 a85,v121\n"
"v_accvgpr_read_b32 v121,a86\n"
"v_lshlrev_b32 v120,16,v121\n"
"v_and_b32 v121,0xffff0000,v121\n"
"v_mul_f32 v120,v120,v119\n"
"v_mul_f32 v121,v121,v119\n"
"v_cvt_pk_bf16_f32 v121,v120,v121\n"
"v_accvgpr_write_b32 a86,v121\n"
"v_accvgpr_read_b32 v121,a87\n"
"v_lshlrev_b32 v120,16,v121\n"
"v_and_b32 v121,0xffff0000,v121\n"
"v_mul_f32 v120,v120,v119\n"
"v_mul_f32 v121,v121,v119\n"
"v_cvt_pk_bf16_f32 v121,v120,v121\n"
"v_accvgpr_write_b32 a87,v121\n"
"v_accvgpr_read_b32 v121,a88\n"
"v_lshlrev_b32 v120,16,v121\n"
"v_and_b32 v121,0xffff0000,v121\n"
"v_mul_f32 v120,v120,v119\n"
"v_mul_f32 v121,v121,v119\n"
"v_cvt_pk_bf16_f32 v121,v120,v121\n"
"v_accvgpr_write_b32 a88,v121\n"
"v_accvgpr_read_b32 v121,a89\n"
"v_lshlrev_b32 v120,16,v121\n"
"v_and_b32 v121,0xffff0000,v121\n"
"v_mul_f32 v120,v120,v119\n"
"v_mul_f32 v121,v121,v119\n"
"v_cvt_pk_bf16_f32 v121,v120,v121\n"
"v_accvgpr_write_b32 a89,v121\n"
"v_accvgpr_read_b32 v121,a90\n"
"v_lshlrev_b32 v120,16,v121\n"
"v_and_b32 v121,0xffff0000,v121\n"
"v_mul_f32 v120,v120,v119\n"
"v_mul_f32 v121,v121,v119\n"
"v_cvt_pk_bf16_f32 v121,v120,v121\n"
"v_accvgpr_write_b32 a90,v121\n"
"v_accvgpr_read_b32 v121,a91\n"
"v_lshlrev_b32 v120,16,v121\n"
"v_and_b32 v121,0xffff0000,v121\n"
"v_mul_f32 v120,v120,v119\n"
"v_mul_f32 v121,v121,v119\n"
"v_cvt_pk_bf16_f32 v121,v120,v121\n"
"v_accvgpr_write_b32 a91,v121\n"
"v_accvgpr_read_b32 v121,a92\n"
"v_lshlrev_b32 v120,16,v121\n"
"v_and_b32 v121,0xffff0000,v121\n"
"v_mul_f32 v120,v120,v119\n"
"v_mul_f32 v121,v121,v119\n"
"v_cvt_pk_bf16_f32 v121,v120,v121\n"
"v_accvgpr_write_b32 a92,v121\n"
"v_accvgpr_read_b32 v121,a93\n"
"v_lshlrev_b32 v120,16,v121\n"
"v_and_b32 v121,0xffff0000,v121\n"
"v_mul_f32 v120,v120,v119\n"
"v_mul_f32 v121,v121,v119\n"
"v_cvt_pk_bf16_f32 v121,v120,v121\n"
"v_accvgpr_write_b32 a93,v121\n"
"v_accvgpr_read_b32 v121,a94\n"
"v_lshlrev_b32 v120,16,v121\n"
"v_and_b32 v121,0xffff0000,v121\n"
"v_mul_f32 v120,v120,v119\n"
"v_mul_f32 v121,v121,v119\n"
"v_cvt_pk_bf16_f32 v121,v120,v121\n"
"v_accvgpr_write_b32 a94,v121\n"
"v_accvgpr_read_b32 v121,a95\n"
"v_lshlrev_b32 v120,16,v121\n"
"v_and_b32 v121,0xffff0000,v121\n"
"v_mul_f32 v120,v120,v119\n"
"v_mul_f32 v121,v121,v119\n"
"v_cvt_pk_bf16_f32 v121,v120,v121\n"
"v_accvgpr_write_b32 a95,v121\n"
"s_nop 1\n"
"s_setpc_b64 s[94:95]\n"
".Lfk_end:\n"
        : "+{v0}"(tid), "+{s2}"(bid)
        : "{s[0:1]}"(__builtin_amdgcn_kernarg_segment_ptr())
        : "v1","v2","v3","v4","v5","v6","v7","v8","v9","v10","v11","v12","v13","v14","v15","v16","v17","v18","v19","v20","v21","v22","v23","v24","v25","v26","v27","v28","v29","v30","v31","v32","v33","v34","v35","v36","v37","v38","v39","v40","v41","v42","v43","v44","v45","v46","v47","v48","v49","v50","v51","v52","v53","v54","v55","v56","v57","v58","v59","v60","v61","v62","v63","v64","v65","v66","v67","v68","v69","v70","v71","v72","v73","v74","v75","v76","v77","v78","v79","v80","v81","v82","v83","v84","v85","v86","v87","v88","v89","v90","v91","v92","v93","v94","v95","v96","v97","v98","v99","v100","v101","v102","v103","v104","v105","v106","v107","v108","v109","v110","v111","v112","v113","v114","v115","v116","v117","v118","v119","v120","v121","v122","v123","v124","v125","v126","v127","v128","v129","v130","v131","v132","v133","v134","v135","v136","v137","v138","v139","v140","v141","v142","v143","v144","v145","v146","v147","v148","v149","v150","v151","v152","v153","v154","v155","v156","v157","v158","v159","v160","v161","v162","v163","v164","v165","v166","v167","v168","v169","v170","v171","v172","v173","v174","v175","v176","v177","v178","v179","v180","v181","v182","v183","v184","v185","v186","v187","v188","v189","v190","v191","v192","v193","v194","v195","v196","v197","v198","v199","v200","v201","v202","v203","v204","v205","v206","v207","v208","v209","v210","v211","v212","v213","v214","v215","v216","v217","v218","v219","v220","v221","v222","v223","v224","v225","v226","v227","v228","v229","v230","v231","v232","v233","v234","v235","v236","v237","v238","v239","v240","v241","v242","v243","v244","v245","v246","v247","v248","v249","v250","v251","v252","v253","v254","v255",
          "a0","a1","a2","a3","a4","a5","a6","a7","a8","a9","a10","a11","a12","a13","a14","a15","a16","a17","a18","a19","a20","a21","a22","a23","a24","a25","a26","a27","a28","a29","a30","a31","a32","a33","a34","a35","a36","a37","a38","a39","a40","a41","a42","a43","a44","a45","a46","a47","a48","a49","a50","a51","a52","a53","a54","a55","a56","a57","a58","a59","a60","a61","a62","a63","a64","a65","a66","a67","a68","a69","a70","a71","a72","a73","a74","a75","a76","a77","a78","a79","a80","a81","a82","a83","a84","a85","a86","a87","a88","a89","a90","a91","a92","a93","a94","a95","a96","a97","a98","a99","a100","a101","a102","a103","a104","a105","a106","a107","a108","a109","a110","a111","a112","a113","a114","a115","a116","a117","a118","a119","a120","a121","a122","a123","a124","a125","a126","a127","a128","a129","a130","a131","a132","a133","a134","a135","a136","a137","a138","a139","a140","a141","a142","a143","a144","a145","a146","a147","a148","a149","a150","a151","a152","a153","a154","a155","a156","a157","a158","a159","a160","a161","a162","a163","a164","a165","a166","a167","a168","a169","a170","a171","a172","a173","a174","a175","a176","a177","a178","a179","a180","a181","a182","a183","a184","a185","a186","a187","a188","a189","a190","a191","a192","a193","a194","a195","a196","a197","a198","a199","a200","a201","a202","a203","a204","a205","a206","a207","a208","a209","a210","a211","a212","a213","a214","a215","a216","a217","a218","a219","a220","a221","a222","a223","a224","a225","a226","a227","a228","a229","a230","a231","a232","a233","a234","a235","a236","a237","a238","a239","a240","a241","a242","a243","a244","a245","a246","a247","a248","a249","a250","a251","a252","a253","a254","a255",
          "s3","s4","s5","s6","s7","s8","s9","s10","s11","s12","s13","s14","s15","s16","s17","s18","s19","s20","s21","s22","s23","s24","s25","s26","s27","s28","s29","s30","s31","s32","s33","s34","s35","s36","s37","s38","s39","s40","s41","s42","s43","s44","s45","s46","s47","s48","s49","s50","s51","s52","s53","s54","s55","s56","s57","s58","s59","s60","s61","s62","s63","s64","s65","s66","s67","s68","s69","s70","s71","s72","s73","s74","s75","s76","s77","s78","s79","s80","s81","s82","s83","s84","s85","s86","s87","s88","s89","s90","s91","s92","s93","s94","s95","s96","s97","s98","s99","s100","s101","vcc","scc","m0","memory");
#endif
}

extern "C" void kernel_launch(void* const* d_in, const int* in_sizes, int n_in, void* d_out, int out_size, void* d_ws, size_t ws_size,
                              hipStream_t stream) {
    (void)hipFuncSetAttribute((const void*)fused_kernel, hipFuncAttributeMaxDynamicSharedMemorySize, 163840);
    fused_kernel<<<256, 256, 163840, stream>>>((const char*)d_in[0], (const char*)d_in[1], (const char*)d_in[2], (const char*)d_in[3], (const char*)d_in[4], (char*)d_ws,
                                               (const char*)d_in[5], (const char*)d_in[8], (const char*)d_in[9], (const char*)d_in[10], (const char*)d_in[7], (char*)d_out);
}
```
